```python
import torch

def _softmax(x, axis=-1):
    return torch.softmax(x, dim=axis)
def _gelu(x):
    return torch.nn.functional.gelu(x, approximate="tanh")
def _silu(x):
    return torch.nn.functional.silu(x)
def _sigmoid(x):
    return torch.sigmoid(x)
def _layernorm(x, axis=-1, eps=1e-5):
    m = x.mean(dim=axis, keepdim=True)
    v = x.var(dim=axis, keepdim=True, unbiased=False)
    return (x - m) / torch.sqrt(v + eps)
def _scatter_add(tgt, idx, src, axis=0):
    return tgt.clone().index_add_(axis, idx, src)
def _segment_sum(src, seg, n):
    out = torch.zeros((n,) + src.shape[1:], dtype=src.dtype)
    return out.index_add_(0, seg, src)
def _pad(x, pw):
    flat = tuple(v for pair in reversed(pw) for v in pair)
    return torch.nn.functional.pad(x, flat)

kernel_name = 'sequential_preLN__compressed_kv_cla__moe_arch_mod__prompt'

def setup_inputs():

    _w = (lambda a: (lambda t: t.to(torch.bfloat16) if t.is_floating_point() else t)(torch.as_tensor(a)))
    def _A_setup():
        B, H, Sq, Sk, D, M = 1, 32, 2048, 2048, 128, 1024
        Dc = 4 * D
        return {
            "x": _w((lambda *s: torch.randn(*s, dtype=torch.bfloat16) * 0.1)(B, Sq, M)),
            "c_kv": _w((lambda *s: torch.randn(*s, dtype=torch.bfloat16) * 1.6)(B, Sk, Dc)),
            "w_q": _w((lambda *s: torch.randn(*s, dtype=torch.bfloat16) * 0.8)(M, H, D) * (M ** -0.5)),
            "w_uk": _w((lambda *s: torch.randn(*s, dtype=torch.bfloat16) * 0.8)(Dc, H, D) * (Dc ** -0.5)),
            "w_uv": _w((lambda *s: torch.randn(*s, dtype=torch.bfloat16) * 1.6)(Dc, H, D) * (Dc ** -0.5)),
            "w_o": _w((lambda *s: torch.randn(*s, dtype=torch.bfloat16) * 1.6)(H, D, M) * ((H * D) ** -0.5)),
        }
    _A_in = _A_setup()

    def _B_setup():
        B, S, D, F, C = 1, 2048, 1024, 3584, 2
        return {
            "x": _w((lambda *s: torch.randn(*s, dtype=torch.bfloat16) * 0.1)(B, S, D)),
            "w_r": _w((lambda *s: torch.randn(*s, dtype=torch.bfloat16) * 0.2)(D)),
            "w_gate": _w((lambda *s: torch.randn(*s, dtype=torch.bfloat16) * 0.2)(D, F)),
            "w_up": _w((lambda *s: torch.randn(*s, dtype=torch.bfloat16) * 0.2)(D, F)),
            "w_down": _w((lambda *s: torch.randn(*s, dtype=torch.bfloat16) * 0.2)(F, D)),
        }
    _B_in = _B_setup()

    return {
        "A_x": _A_in["x"],
        "A_c_kv": _A_in["c_kv"],
        "A_w_q": _A_in["w_q"],
        "A_w_uk": _A_in["w_uk"],
        "A_w_uv": _A_in["w_uv"],
        "A_w_o": _A_in["w_o"],
        "B_x": _B_in["x"],
        "B_w_r": _B_in["w_r"],
        "B_w_gate": _B_in["w_gate"],
        "B_w_up": _B_in["w_up"],
        "B_w_down": _B_in["w_down"],
    }

def reference(A_x, A_c_kv, A_w_q, A_w_uk, A_w_uv, A_w_o, B_x, B_w_r, B_w_gate, B_w_up, B_w_down):
    def _A_ref(x, c_kv, w_q, w_uk, w_uv, w_o):
        B, Sq, M = x.shape[0], x.shape[1], x.shape[2]
        Sk = c_kv.shape[1]
        H, D = w_q.shape[1], w_q.shape[2]
        q = torch.einsum("bqm,mhd->bhqd", x, w_q)
        k = torch.einsum("bkc,chd->bhkd", c_kv, w_uk)
        v = torch.einsum("bkc,chd->bhkd", c_kv, w_uv)
        scores = torch.einsum("bhqd,bhkd->bhqk", q, k) * (D ** -0.5)
        off = Sk - Sq
        qi = torch.arange(Sq)[:, None]
        kj = torch.arange(Sk)[None, :]
        cmask = kj <= (qi + off)
        scores = torch.where(cmask[None, None], scores, -1e9)
        p = _softmax(scores, axis=-1)
        ctx = torch.einsum("bhqk,bhkd->bhqd", p, v)
        return torch.einsum("bhqd,hdm->bqm", ctx, w_o)

    def _B_ref(x, w_r, w_gate, w_up, w_down):
        B, S, D = x.shape
        C = 2
        r = x @ w_r
        idx = torch.argsort(-r, axis=-1)[:, :C]
        idx_e = (lambda x, n, axis: torch.repeat_interleave(x, n, dim=axis))(idx.reshape(B, C, 1), D, axis=2)
        x_sel = (lambda x, i, axis: torch.take_along_dim(x, i, dim=axis))(x, idx_e, axis=1)
        g = _silu(x_sel @ w_gate)
        u = x_sel @ w_up
        y = (g * u) @ w_down
        r_sel = _sigmoid((lambda x, i, axis: torch.take_along_dim(x, i, dim=axis))(r, idx, axis=1))
        y = y * r_sel.reshape(B, C, 1)
        b_off = (torch.arange(B) * S).reshape(B, 1)
        flat_idx = (idx + b_off).reshape(B * C)
        zero = (x * 0.0).reshape(B * S, D)
        upd = _scatter_add(zero, flat_idx, y.reshape(B * C, D), axis=0)
        return x + upd.reshape(B, S, D)

    x = A_x
    h = x + _A_ref(_layernorm(x, axis=-1), A_c_kv, A_w_q, A_w_uk, A_w_uv, A_w_o)
    h2 = h + _B_ref(_layernorm(h, axis=-1), B_w_r, B_w_gate, B_w_up, B_w_down)
    return h2
```

```cpp
#include <hip/hip_runtime.h>
#include <hip/hip_bf16.h>
#include <stdint.h>
#pragma clang diagnostic ignored "-Winline-asm"
#pragma clang diagnostic ignored "-Woverlength-strings"

typedef __hip_bfloat16 bf16;
#define DBGP ((unsigned*)nullptr)
constexpr int LDS_BYTES = 163840;
extern "C" __global__ __attribute__((aligned(4096))) __launch_bounds__(256, 1) void fa_kernel(const bf16* __restrict__ px, const bf16* __restrict__ ckv, const bf16* __restrict__ wq,
                                                    const bf16* __restrict__ wuk, const bf16* __restrict__ wuv, char* pws,
                                                    const bf16* __restrict__ pwo, const bf16* __restrict__ pwg, const bf16* __restrict__ pwu,
                                                    const bf16* __restrict__ pwd, const bf16* __restrict__ pwr, bf16* __restrict__ pout) {
    asm volatile(R"ASM(
; ---- roles: head = 4 xcd + (wg >> 6), j = (wg >> 3) & 7; odd j: block j >> 1 (then helps with block 7 - (j >> 1)), even j: block 7 - (j >> 1). First operands, addresses
.La5_LSTART:
s_load_dwordx16 s[16:31], s[0:1], 0
s_load_dwordx8 s[32:39], s[0:1], 64
.p2align 6
v_mbcnt_lo_u32_b32 v64, -1, 0
v_mbcnt_hi_u32_b32 v64, -1, v64
s_getpc_b64 s[92:93]
.La5_LPC:
s_sub_u32 s48, s92, .La5_LPC-.La5_LSTART
s_subb_u32 s49, s93, 0
s_and_b32 s49, s49, 0xffff
s_mov_b32 s50, .La5_LCODEEND-.La5_LSTART
s_mov_b32 s51, 0x20000
s_lshr_b32 s97, s2, 3
s_lshl2_add_u32 s87, s97, s3
v_lshlrev_b32 v165, 7, v64
s_lshl_b32 s87, s87, 13
buffer_load_dword v254, v165, s[48:51], s87 offen
s_and_b32 s92, s2, 7
s_lshr_b32 s93, s2, 6
s_lshl2_add_u32 s72, s92, s93
s_bfe_u32 s73, s2, 0x30003
s_lshr_b32 s74, s73, 1
s_xor_b32 s92, s74, 7
s_and_b32 s75, s73, 1
s_cselect_b32 s76, s74, s92
s_lshr_b32 s77, s3, 1
s_and_b32 s78, s3, 1
v_and_b32 v65, 15, v64
v_lshrrev_b32 v66, 4, v64
v_lshlrev_b32 v255, 4, v64
v_lshlrev_b32 v67, 1, v66
v_xor_b32 v67, v67, v65
v_lshlrev_b32 v67, 4, v67
v_lshl_add_u32 v67, v66, 13, v67
s_lshl_b32 s94, s78, 17
v_add_u32 v232, s94, v67
v_add_u32 v233, 32768, v232
v_add_u32 v234, 65536, v232
v_add_u32 v235, 98304, v232
v_and_b32 v67, 7, v64
v_xor_b32 v68, v67, v66
v_lshlrev_b32 v68, 4, v68
v_lshrrev_b32 v69, 3, v64
v_lshl_add_u32 v68, v69, 10, v68
s_lshl2_add_u32 s94, s3, s93
s_lshl_b32 s82, s94, 11
s_lshl_b32 s94, s94, 14
v_add_u32 v224, s94, v68
v_xor_b32 v225, 0x2040, v224
v_xor_b32 v226, 16384, v224
v_xor_b32 v227, 16384, v225
v_xor_b32 v228, 32768, v224
v_xor_b32 v229, 32768, v225
v_xor_b32 v230, 49152, v224
v_xor_b32 v231, 49152, v225
s_mul_i32 s94, s77, 8448
s_mul_i32 s95, s78, 4224
s_add_u32 s83, s94, s95
s_add_u32 s83, s83, 96256
v_lshlrev_b32 v167, 7, v67
v_lshl_add_u32 v167, v69, 13, v167
v_lshrrev_b32 v164, 5, v64
v_lshl_add_u32 v164, v164, 28, v167
v_lshl_add_u32 v165, v66, 28, v165
v_lshlrev_b32 v166, 10, v65
v_lshl_add_u32 v166, v66, 7, v166
v_lshrrev_b32 v68, 4, v64
v_lshl_add_u32 v166, v68, 28, v166
s_lshl1_add_u32 s97, s97, s78
s_lshl_b32 s97, s97, 15
s_lshl_b32 s96, s93, 8
s_sub_u32 s87, s87, s96
s_sub_u32 s97, s97, s96
s_add_u32 s86, s97, 0
s_lshl2_add_u32 s101, s72, s3
s_lshl_b32 s101, s101, 11
s_lshl2_add_u32 s100, s93, s3
s_lshl_b32 s100, s100, 14
v_lshlrev_b32 v118, 11, v67
s_lshl_b32 s93, s3, 9
v_lshl_add_u32 v119, v69, 4, v118
v_add_u32 v119, s93, v119
s_lshl_b32 s97, s2, 14
s_lshl_b32 s94, s72, 8
s_lshl_b32 s95, s76, 18
s_sub_u32 s46, 4194304, s94
s_mov_b32 s47, 0x20000
s_mov_b32 s42, 262144
s_mov_b32 s43, 0x20000
s_mov_b32 s62, 16384
s_mov_b32 s63, 0x20000
s_mov_b32 s79, 0
s_mov_b32 s80, 0
s_mov_b32 s81, 262144
s_cmp_eq_u32 s77, 0
s_waitcnt lgkmcnt(0)
s_cselect_b64 s[92:93], s[22:23], s[24:25]
s_add_u32 s44, s92, s94
s_addc_u32 s45, s93, 0
s_and_b32 s45, s45, 0xffff
s_add_u32 s40, s18, s95
s_addc_u32 s41, s19, 0
s_and_b32 s41, s41, 0xffff
s_add_u32 s60, s16, s97
s_addc_u32 s61, s17, 0
s_and_b32 s61, s61, 0xffff
buffer_load_dword v254, v164, s[44:47], s86 offen
s_add_u32 s86, s86, 2097152
buffer_load_dword v254, v165, s[40:43], s101 offen
buffer_load_dword v254, v166, s[40:43], s100 offen
s_add_u32 m0, s83, 0
s_nop 0
buffer_load_dwordx4 v232, s[44:47], s80 offen lds
s_add_u32 m0, s83, 1024
s_nop 0
buffer_load_dwordx4 v233, s[44:47], s80 offen lds
s_add_u32 m0, s83, 2176
s_nop 0
buffer_load_dwordx4 v234, s[44:47], s80 offen lds
s_add_u32 m0, s83, 3200
s_nop 0
buffer_load_dwordx4 v235, s[44:47], s80 offen lds
s_add_u32 s80, s80, s81
v_lshrrev_b32 v67, 1, v65
v_xor_b32 v67, v67, v66
v_lshlrev_b32 v67, 4, v67
v_lshl_add_u32 v67, v65, 7, v67
s_lshl_b32 s92, s77, 12
v_add_u32 v236, s92, v67
v_xor_b32 v237, 64, v236
v_bfe_u32 v120, v64, 2, 2
v_add_u32 v67, 1, v66
v_lshrrev_b32 v67, 1, v67
v_lshlrev_b32 v67, 7, v67
v_lshl_add_u32 v67, v66, 11, v67
v_lshl_add_u32 v67, v120, 8, v67
v_and_b32 v68, 3, v64
v_lshl_add_u32 v67, v68, 3, v67
s_mul_i32 s92, s78, 8448
v_lshl_add_u32 v67, v120, 5, v67
v_add_u32 v67, s92, v67
v_add_u32 v240, 96256, v67
v_xor_b32 v241, 32, v240
v_xor_b32 v242, 64, v240
v_xor_b32 v243, 96, v240
s_add_u32 s64, s26, 148897792
s_addc_u32 s65, s27, 0
s_and_b32 s65, s65, 0xffff
s_mov_b32 s66, 655360
s_mov_b32 s67, 0x20000
s_mov_b32 s88, 4096
s_lshr_b32 s92, s2, 1
s_lshl_b32 s92, s92, 15
s_and_b32 s93, s2, 1
s_lshl_b32 s93, s93, 7
s_add_u32 s92, s92, s93
s_lshl_b32 s93, s3, 13
s_add_u32 s92, s92, s93
s_add_u32 s56, s26, s92
s_addc_u32 s57, s27, 0
s_and_b32 s57, s57, 0xffff
s_mov_b32 s58, 8192
s_mov_b32 s59, 0x20000
v_and_b32 v71, 0x380, v255
v_and_b32 v127, 0x70, v255
v_lshl_add_u32 v71, v71, 1, v127
s_lshl_b32 s92, s3, 3
v_lshlrev_b32 v126, 1, v127
v_or_b32 v126, 65536, v126
v_add_u32 v127, s92, v126
s_add_u32 s52, s26, 171966464
s_addc_u32 s53, s27, 0
s_and_b32 s53, s53, 0xffff
s_mov_b32 s54, 950272
s_mov_b32 s55, 0x20000
s_lshl2_add_u32 s95, s2, s3
s_lshl_b32 s91, s95, 3
s_lshl_b32 s95, s95, 10
s_lshl_b32 s89, s76, 10
v_lshl_add_u32 v167, v69, 28, v167
s_mov_b32 s96, s87
s_mov_b32 s84, 33792
s_mov_b32 s85, 0
s_add_u32 s97, s83, 67584
s_mov_b32 s90, s83
s_lshl_b32 s92, s72, 8
s_sub_u32 s93, 8388608, s92
s_add_u32 s68, s20, s92
s_addc_u32 s69, s21, 0
s_and_b32 s69, s69, 0xffff
s_mov_b32 s70, s93
s_mov_b32 s71, 0x20000
s_mov_b64 exec, 0
s_cmp_eq_u32 s3, 0
s_cbranch_scc1 .La5_LW1_0
s_cmp_eq_u32 s3, 1
s_cbranch_scc1 .La5_LW1_1
s_cmp_eq_u32 s3, 2
s_cbranch_scc1 .La5_LW1_2
s_branch .La5_LW1_3
.La5_LW1_R0:
s_mov_b64 exec, -1
s_xor_b32 m0, s82, 0
s_nop 0
buffer_load_dwordx4 v224, s[40:43], s79 offen lds
s_xor_b32 m0, s82, 1024
s_nop 0
buffer_load_dwordx4 v225, s[40:43], s79 offen lds
s_xor_b32 m0, s82, 2048
s_nop 0
buffer_load_dwordx4 v226, s[40:43], s79 offen lds
s_xor_b32 m0, s82, 3072
s_nop 0
buffer_load_dwordx4 v227, s[40:43], s79 offen lds
s_xor_b32 m0, s82, 4096
s_nop 0
buffer_load_dwordx4 v228, s[40:43], s79 offen lds
s_xor_b32 m0, s82, 5120
s_nop 0
buffer_load_dwordx4 v229, s[40:43], s79 offen lds
s_xor_b32 m0, s82, 6144
s_nop 0
buffer_load_dwordx4 v230, s[40:43], s79 offen lds
s_xor_b32 m0, s82, 7168
s_nop 0
buffer_load_dwordx4 v231, s[40:43], s79 offen lds
s_add_u32 s79, s79, 128
s_add_u32 m0, s83, 16896
s_nop 0
buffer_load_dwordx4 v232, s[44:47], s80 offen lds
s_add_u32 m0, s83, 17920
s_nop 0
buffer_load_dwordx4 v233, s[44:47], s80 offen lds
s_add_u32 m0, s83, 19072
s_nop 0
buffer_load_dwordx4 v234, s[44:47], s80 offen lds
s_add_u32 m0, s83, 20096
s_nop 0
buffer_load_dwordx4 v235, s[44:47], s80 offen lds
s_add_u32 s80, s80, s81
buffer_load_dwordx2 v[252:253], off, s[64:67], 0 sc1
s_waitcnt vmcnt(5)
s_barrier
ds_read_b128 v[128:131], v236 offset:24576
ds_read_b64_tr_b16 v[192:193], v240 offset:0
ds_read_b64_tr_b16 v[194:195], v240 offset:1024
ds_read_b64_tr_b16 v[196:197], v241 offset:0
ds_read_b64_tr_b16 v[198:199], v241 offset:1024
ds_read_b64_tr_b16 v[200:201], v242 offset:0
ds_read_b64_tr_b16 v[202:203], v242 offset:1024
ds_read_b64_tr_b16 v[204:205], v243 offset:0
ds_read_b64_tr_b16 v[206:207], v243 offset:1024
ds_read_b64_tr_b16 v[208:209], v240 offset:128
ds_read_b64_tr_b16 v[210:211], v240 offset:1152
ds_read_b64_tr_b16 v[212:213], v241 offset:128
ds_read_b64_tr_b16 v[214:215], v241 offset:1152
ds_read_b64_tr_b16 v[216:217], v242 offset:128
ds_read_b64_tr_b16 v[218:219], v242 offset:1152
ds_read_b64_tr_b16 v[220:221], v243 offset:128
ds_read_b64_tr_b16 v[222:223], v243 offset:1152
ds_read_b128 v[132:135], v236 offset:26624
ds_read_b128 v[136:139], v236 offset:16384
ds_read_b128 v[140:143], v236 offset:18432
ds_read_b128 v[144:147], v236 offset:8192
ds_read_b128 v[148:151], v236 offset:10240
ds_read_b128 v[152:155], v236 offset:0
ds_read_b128 v[156:159], v236 offset:2048
s_waitcnt lgkmcnt(0)
s_cmp_eq_u32 s78, 1
s_cbranch_scc1 .La5_LV_START
; ---- K tile (waves 0, 2): 256 keys x 128 columns, K = 512; LayerNorm of 8 rows of x in the gaps (softmax scale folded in)
.La5_LK_START:
s_waitcnt vmcnt(1)
s_barrier
v_mfma_f32_16x16x32_bf16 a[224:227], v[192:195], v[128:131], 0
ds_read_b128 v[0:3], v237 offset:24576
s_add_u32 m0, s83, 33792
v_mfma_f32_16x16x32_bf16 a[228:231], v[196:199], v[128:131], 0
buffer_load_dwordx4 v232, s[44:47], s80 offen lds
v_mfma_f32_16x16x32_bf16 a[232:235], v[200:203], v[128:131], 0
ds_read_b64_tr_b16 v[32:33], v240 offset:16896
v_mfma_f32_16x16x32_bf16 a[236:239], v[204:207], v[128:131], 0
v_mfma_f32_16x16x32_bf16 a[208:211], v[208:211], v[128:131], 0
ds_read_b64_tr_b16 v[34:35], v240 offset:17920
s_add_u32 m0, s83, 34816
v_mfma_f32_16x16x32_bf16 a[212:215], v[212:215], v[128:131], 0
buffer_load_dwordx4 v233, s[44:47], s80 offen lds
v_mfma_f32_16x16x32_bf16 a[128:131], v[216:219], v[128:131], 0
ds_read_b64_tr_b16 v[36:37], v241 offset:16896
v_mfma_f32_16x16x32_bf16 a[132:135], v[220:223], v[128:131], 0
v_mfma_f32_16x16x32_bf16 a[216:219], v[192:195], v[132:135], 0
ds_read_b64_tr_b16 v[38:39], v241 offset:17920
s_add_u32 m0, s83, 35968
v_mfma_f32_16x16x32_bf16 a[220:223], v[196:199], v[132:135], 0
buffer_load_dwordx4 v234, s[44:47], s80 offen lds
v_mfma_f32_16x16x32_bf16 a[136:139], v[200:203], v[132:135], 0
ds_read_b64_tr_b16 v[40:41], v242 offset:16896
v_mfma_f32_16x16x32_bf16 a[140:143], v[204:207], v[132:135], 0
v_mfma_f32_16x16x32_bf16 a[192:195], v[208:211], v[132:135], 0
ds_read_b64_tr_b16 v[42:43], v242 offset:17920
s_add_u32 m0, s83, 36992
v_mfma_f32_16x16x32_bf16 a[196:199], v[212:215], v[132:135], 0
buffer_load_dwordx4 v235, s[44:47], s80 offen lds
v_mfma_f32_16x16x32_bf16 a[144:147], v[216:219], v[132:135], 0
ds_read_b64_tr_b16 v[44:45], v243 offset:16896
v_mfma_f32_16x16x32_bf16 a[148:151], v[220:223], v[132:135], 0
s_add_u32 s80, s80, s81
v_mfma_f32_16x16x32_bf16 a[200:203], v[192:195], v[136:139], 0
ds_read_b64_tr_b16 v[46:47], v243 offset:17920
s_xor_b32 m0, s82, 32768
v_mfma_f32_16x16x32_bf16 a[204:207], v[196:199], v[136:139], 0
buffer_load_dwordx4 v224, s[40:43], s79 offen lds
v_mfma_f32_16x16x32_bf16 a[152:155], v[200:203], v[136:139], 0
ds_read_b64_tr_b16 v[48:49], v240 offset:17024
v_mfma_f32_16x16x32_bf16 a[156:159], v[204:207], v[136:139], 0
v_mfma_f32_16x16x32_bf16 a[96:99], v[208:211], v[136:139], 0
ds_read_b64_tr_b16 v[50:51], v240 offset:18048
s_xor_b32 m0, s82, 33792
v_mfma_f32_16x16x32_bf16 a[100:103], v[212:215], v[136:139], 0
buffer_load_dwordx4 v225, s[40:43], s79 offen lds
v_mfma_f32_16x16x32_bf16 a[160:163], v[216:219], v[136:139], 0
ds_read_b64_tr_b16 v[52:53], v241 offset:17024
v_mfma_f32_16x16x32_bf16 a[164:167], v[220:223], v[136:139], 0
v_mfma_f32_16x16x32_bf16 a[104:107], v[192:195], v[140:143], 0
ds_read_b64_tr_b16 v[54:55], v241 offset:18048
s_xor_b32 m0, s82, 34816
v_mfma_f32_16x16x32_bf16 a[108:111], v[196:199], v[140:143], 0
buffer_load_dwordx4 v226, s[40:43], s79 offen lds
v_mfma_f32_16x16x32_bf16 a[168:171], v[200:203], v[140:143], 0
ds_read_b64_tr_b16 v[56:57], v242 offset:17024
v_mfma_f32_16x16x32_bf16 a[172:175], v[204:207], v[140:143], 0
v_mfma_f32_16x16x32_bf16 a[112:115], v[208:211], v[140:143], 0
ds_read_b64_tr_b16 v[58:59], v242 offset:18048
s_xor_b32 m0, s82, 35840
v_mfma_f32_16x16x32_bf16 a[116:119], v[212:215], v[140:143], 0
buffer_load_dwordx4 v227, s[40:43], s79 offen lds
v_mfma_f32_16x16x32_bf16 a[176:179], v[216:219], v[140:143], 0
ds_read_b64_tr_b16 v[60:61], v243 offset:17024
v_mfma_f32_16x16x32_bf16 a[180:183], v[220:223], v[140:143], 0
v_mfma_f32_16x16x32_bf16 a[120:123], v[192:195], v[144:147], 0
ds_read_b64_tr_b16 v[62:63], v243 offset:18048
s_xor_b32 m0, s82, 36864
v_mfma_f32_16x16x32_bf16 a[124:127], v[196:199], v[144:147], 0
buffer_load_dwordx4 v228, s[40:43], s79 offen lds
v_mfma_f32_16x16x32_bf16 a[184:187], v[200:203], v[144:147], 0
ds_read_b128 v[4:7], v237 offset:26624
v_mfma_f32_16x16x32_bf16 a[188:191], v[204:207], v[144:147], 0
v_mfma_f32_16x16x32_bf16 a[64:67], v[208:211], v[144:147], 0
ds_read_b128 v[8:11], v237 offset:16384
s_xor_b32 m0, s82, 37888
v_mfma_f32_16x16x32_bf16 a[68:71], v[212:215], v[144:147], 0
buffer_load_dwordx4 v229, s[40:43], s79 offen lds
v_mfma_f32_16x16x32_bf16 a[0:3], v[216:219], v[144:147], 0
ds_read_b128 v[12:15], v237 offset:18432
v_mfma_f32_16x16x32_bf16 a[4:7], v[220:223], v[144:147], 0
v_mfma_f32_16x16x32_bf16 a[72:75], v[192:195], v[148:151], 0
ds_read_b128 v[16:19], v237 offset:8192
s_xor_b32 m0, s82, 38912
v_mfma_f32_16x16x32_bf16 a[76:79], v[196:199], v[148:151], 0
buffer_load_dwordx4 v230, s[40:43], s79 offen lds
v_mfma_f32_16x16x32_bf16 a[8:11], v[200:203], v[148:151], 0
ds_read_b128 v[20:23], v237 offset:10240
v_mfma_f32_16x16x32_bf16 a[12:15], v[204:207], v[148:151], 0
v_mfma_f32_16x16x32_bf16 a[80:83], v[208:211], v[148:151], 0
ds_read_b128 v[24:27], v237 offset:0
s_xor_b32 m0, s82, 39936
v_mfma_f32_16x16x32_bf16 a[84:87], v[212:215], v[148:151], 0
buffer_load_dwordx4 v231, s[40:43], s79 offen lds
v_mfma_f32_16x16x32_bf16 a[16:19], v[216:219], v[148:151], 0
ds_read_b128 v[28:31], v237 offset:2048
v_mfma_f32_16x16x32_bf16 a[20:23], v[220:223], v[148:151], 0
s_add_u32 s79, s79, 128
v_mfma_f32_16x16x32_bf16 a[88:91], v[192:195], v[152:155], 0
v_mfma_f32_16x16x32_bf16 a[92:95], v[196:199], v[152:155], 0
buffer_load_dword v70, v118, s[60:63], 0 offen
v_mfma_f32_16x16x32_bf16 a[24:27], v[200:203], v[152:155], 0
v_mfma_f32_16x16x32_bf16 a[28:31], v[204:207], v[152:155], 0
v_mfma_f32_16x16x32_bf16 a[240:243], v[208:211], v[152:155], 0
buffer_load_dwordx4 v[72:75], v119, s[60:63], 0 offen offset:0
v_mfma_f32_16x16x32_bf16 a[244:247], v[212:215], v[152:155], 0
v_mfma_f32_16x16x32_bf16 a[32:35], v[216:219], v[152:155], 0
v_mfma_f32_16x16x32_bf16 a[36:39], v[220:223], v[152:155], 0
buffer_load_dwordx4 v[76:79], v119, s[60:63], 0 offen offset:128
v_mfma_f32_16x16x32_bf16 a[248:251], v[192:195], v[156:159], 0
v_mfma_f32_16x16x32_bf16 a[252:255], v[196:199], v[156:159], 0
v_mfma_f32_16x16x32_bf16 a[40:43], v[200:203], v[156:159], 0
buffer_load_dwordx4 v[80:83], v119, s[60:63], 0 offen offset:256
v_mfma_f32_16x16x32_bf16 a[44:47], v[204:207], v[156:159], 0
v_mfma_f32_16x16x32_bf16 a[48:51], v[208:211], v[156:159], 0
v_mfma_f32_16x16x32_bf16 a[52:55], v[212:215], v[156:159], 0
buffer_load_dwordx4 v[84:87], v119, s[60:63], 0 offen offset:384
v_mfma_f32_16x16x32_bf16 a[56:59], v[216:219], v[156:159], 0
v_mfma_f32_16x16x32_bf16 a[60:63], v[220:223], v[156:159], 0
s_mov_b64 exec, 0
s_cmp_eq_u32 s3, 0
s_cbranch_scc1 .La5_LW2_0
s_branch .La5_LW2_2
.La5_LW2_R0:
s_mov_b64 exec, -1
s_waitcnt lgkmcnt(0)
s_waitcnt vmcnt(5)
s_barrier
v_mfma_f32_16x16x32_bf16 a[224:227], v[32:35], v[0:3], a[224:227]
ds_read_b128 v[128:131], v236 offset:57344
s_add_u32 m0, s83, 50688
v_mfma_f32_16x16x32_bf16 a[228:231], v[36:39], v[0:3], a[228:231]
buffer_load_dwordx4 v232, s[44:47], s80 offen lds
v_mfma_f32_16x16x32_bf16 a[232:235], v[40:43], v[0:3], a[232:235]
ds_read_b64_tr_b16 v[192:193], v240 offset:33792
v_mfma_f32_16x16x32_bf16 a[236:239], v[44:47], v[0:3], a[236:239]
v_mfma_f32_16x16x32_bf16 a[208:211], v[48:51], v[0:3], a[208:211]
ds_read_b64_tr_b16 v[194:195], v240 offset:34816
s_add_u32 m0, s83, 51712
v_mfma_f32_16x16x32_bf16 a[212:215], v[52:55], v[0:3], a[212:215]
buffer_load_dwordx4 v233, s[44:47], s80 offen lds
v_mfma_f32_16x16x32_bf16 a[128:131], v[56:59], v[0:3], a[128:131]
ds_read_b64_tr_b16 v[196:197], v241 offset:33792
v_mfma_f32_16x16x32_bf16 a[132:135], v[60:63], v[0:3], a[132:135]
v_mfma_f32_16x16x32_bf16 a[216:219], v[32:35], v[4:7], a[216:219]
ds_read_b64_tr_b16 v[198:199], v241 offset:34816
s_add_u32 m0, s83, 52864
v_mfma_f32_16x16x32_bf16 a[220:223], v[36:39], v[4:7], a[220:223]
buffer_load_dwordx4 v234, s[44:47], s80 offen lds
v_mfma_f32_16x16x32_bf16 a[136:139], v[40:43], v[4:7], a[136:139]
ds_read_b64_tr_b16 v[200:201], v242 offset:33792
v_mfma_f32_16x16x32_bf16 a[140:143], v[44:47], v[4:7], a[140:143]
v_mfma_f32_16x16x32_bf16 a[192:195], v[48:51], v[4:7], a[192:195]
ds_read_b64_tr_b16 v[202:203], v242 offset:34816
s_add_u32 m0, s83, 53888
v_mfma_f32_16x16x32_bf16 a[196:199], v[52:55], v[4:7], a[196:199]
buffer_load_dwordx4 v235, s[44:47], s80 offen lds
v_mfma_f32_16x16x32_bf16 a[144:147], v[56:59], v[4:7], a[144:147]
ds_read_b64_tr_b16 v[204:205], v243 offset:33792
v_mfma_f32_16x16x32_bf16 a[148:151], v[60:63], v[4:7], a[148:151]
s_add_u32 s80, s80, s81
v_mfma_f32_16x16x32_bf16 a[200:203], v[32:35], v[8:11], a[200:203]
ds_read_b64_tr_b16 v[206:207], v243 offset:34816
s_xor_b32 m0, s82, 0
v_mfma_f32_16x16x32_bf16 a[204:207], v[36:39], v[8:11], a[204:207]
buffer_load_dwordx4 v224, s[40:43], s79 offen lds
v_mfma_f32_16x16x32_bf16 a[152:155], v[40:43], v[8:11], a[152:155]
ds_read_b64_tr_b16 v[208:209], v240 offset:33920
v_mfma_f32_16x16x32_bf16 a[156:159], v[44:47], v[8:11], a[156:159]
v_mfma_f32_16x16x32_bf16 a[96:99], v[48:51], v[8:11], a[96:99]
ds_read_b64_tr_b16 v[210:211], v240 offset:34944
s_xor_b32 m0, s82, 1024
v_mfma_f32_16x16x32_bf16 a[100:103], v[52:55], v[8:11], a[100:103]
buffer_load_dwordx4 v225, s[40:43], s79 offen lds
v_mfma_f32_16x16x32_bf16 a[160:163], v[56:59], v[8:11], a[160:163]
ds_read_b64_tr_b16 v[212:213], v241 offset:33920
v_mfma_f32_16x16x32_bf16 a[164:167], v[60:63], v[8:11], a[164:167]
v_mfma_f32_16x16x32_bf16 a[104:107], v[32:35], v[12:15], a[104:107]
ds_read_b64_tr_b16 v[214:215], v241 offset:34944
s_xor_b32 m0, s82, 2048
v_mfma_f32_16x16x32_bf16 a[108:111], v[36:39], v[12:15], a[108:111]
buffer_load_dwordx4 v226, s[40:43], s79 offen lds
v_mfma_f32_16x16x32_bf16 a[168:171], v[40:43], v[12:15], a[168:171]
ds_read_b64_tr_b16 v[216:217], v242 offset:33920
v_mfma_f32_16x16x32_bf16 a[172:175], v[44:47], v[12:15], a[172:175]
v_mfma_f32_16x16x32_bf16 a[112:115], v[48:51], v[12:15], a[112:115]
ds_read_b64_tr_b16 v[218:219], v242 offset:34944
s_xor_b32 m0, s82, 3072
v_mfma_f32_16x16x32_bf16 a[116:119], v[52:55], v[12:15], a[116:119]
buffer_load_dwordx4 v227, s[40:43], s79 offen lds
v_mfma_f32_16x16x32_bf16 a[176:179], v[56:59], v[12:15], a[176:179]
ds_read_b64_tr_b16 v[220:221], v243 offset:33920
v_mfma_f32_16x16x32_bf16 a[180:183], v[60:63], v[12:15], a[180:183]
v_mfma_f32_16x16x32_bf16 a[120:123], v[32:35], v[16:19], a[120:123]
ds_read_b64_tr_b16 v[222:223], v243 offset:34944
s_xor_b32 m0, s82, 4096
v_mfma_f32_16x16x32_bf16 a[124:127], v[36:39], v[16:19], a[124:127]
buffer_load_dwordx4 v228, s[40:43], s79 offen lds
v_mfma_f32_16x16x32_bf16 a[184:187], v[40:43], v[16:19], a[184:187]
ds_read_b128 v[132:135], v236 offset:59392
v_mfma_f32_16x16x32_bf16 a[188:191], v[44:47], v[16:19], a[188:191]
v_mfma_f32_16x16x32_bf16 a[64:67], v[48:51], v[16:19], a[64:67]
ds_read_b128 v[136:139], v236 offset:49152
s_xor_b32 m0, s82, 5120
v_mfma_f32_16x16x32_bf16 a[68:71], v[52:55], v[16:19], a[68:71]
buffer_load_dwordx4 v229, s[40:43], s79 offen lds
v_mfma_f32_16x16x32_bf16 a[0:3], v[56:59], v[16:19], a[0:3]
ds_read_b128 v[140:143], v236 offset:51200
v_mfma_f32_16x16x32_bf16 a[4:7], v[60:63], v[16:19], a[4:7]
v_mfma_f32_16x16x32_bf16 a[72:75], v[32:35], v[20:23], a[72:75]
ds_read_b128 v[144:147], v236 offset:40960
s_xor_b32 m0, s82, 6144
v_mfma_f32_16x16x32_bf16 a[76:79], v[36:39], v[20:23], a[76:79]
buffer_load_dwordx4 v230, s[40:43], s79 offen lds
v_mfma_f32_16x16x32_bf16 a[8:11], v[40:43], v[20:23], a[8:11]
ds_read_b128 v[148:151], v236 offset:43008
v_mfma_f32_16x16x32_bf16 a[12:15], v[44:47], v[20:23], a[12:15]
v_mfma_f32_16x16x32_bf16 a[80:83], v[48:51], v[20:23], a[80:83]
ds_read_b128 v[152:155], v236 offset:32768
s_xor_b32 m0, s82, 7168
v_mfma_f32_16x16x32_bf16 a[84:87], v[52:55], v[20:23], a[84:87]
buffer_load_dwordx4 v231, s[40:43], s79 offen lds
v_mfma_f32_16x16x32_bf16 a[16:19], v[56:59], v[20:23], a[16:19]
ds_read_b128 v[156:159], v236 offset:34816
v_mfma_f32_16x16x32_bf16 a[20:23], v[60:63], v[20:23], a[20:23]
s_add_u32 s79, s79, 128
v_mfma_f32_16x16x32_bf16 a[88:91], v[32:35], v[24:27], a[88:91]
s_add_u32 m0, s83, 0
v_mfma_f32_16x16x32_bf16 a[92:95], v[36:39], v[24:27], a[92:95]
buffer_load_dwordx4 v232, s[44:47], s80 offen lds
v_mfma_f32_16x16x32_bf16 a[24:27], v[40:43], v[24:27], a[24:27]
v_mfma_f32_16x16x32_bf16 a[28:31], v[44:47], v[24:27], a[28:31]
v_mfma_f32_16x16x32_bf16 a[240:243], v[48:51], v[24:27], a[240:243]
s_add_u32 m0, s83, 1024
v_mfma_f32_16x16x32_bf16 a[244:247], v[52:55], v[24:27], a[244:247]
buffer_load_dwordx4 v233, s[44:47], s80 offen lds
v_mfma_f32_16x16x32_bf16 a[32:35], v[56:59], v[24:27], a[32:35]
v_mfma_f32_16x16x32_bf16 a[36:39], v[60:63], v[24:27], a[36:39]
v_mfma_f32_16x16x32_bf16 a[248:251], v[32:35], v[28:31], a[248:251]
s_add_u32 m0, s83, 2176
v_mfma_f32_16x16x32_bf16 a[252:255], v[36:39], v[28:31], a[252:255]
buffer_load_dwordx4 v234, s[44:47], s80 offen lds
v_mfma_f32_16x16x32_bf16 a[40:43], v[40:43], v[28:31], a[40:43]
v_mfma_f32_16x16x32_bf16 a[44:47], v[44:47], v[28:31], a[44:47]
v_mfma_f32_16x16x32_bf16 a[48:51], v[48:51], v[28:31], a[48:51]
s_add_u32 m0, s83, 3200
v_mfma_f32_16x16x32_bf16 a[52:55], v[52:55], v[28:31], a[52:55]
buffer_load_dwordx4 v235, s[44:47], s80 offen lds
v_mfma_f32_16x16x32_bf16 a[56:59], v[56:59], v[28:31], a[56:59]
v_mfma_f32_16x16x32_bf16 a[60:63], v[60:63], v[28:31], a[60:63]
s_add_u32 s80, s80, s81
s_waitcnt lgkmcnt(0)
s_waitcnt vmcnt(12)
s_barrier
v_mfma_f32_16x16x32_bf16 a[224:227], v[192:195], v[128:131], a[224:227]
ds_read_b128 v[0:3], v237 offset:57344
s_add_u32 m0, s83, 16896
v_mfma_f32_16x16x32_bf16 a[228:231], v[196:199], v[128:131], a[228:231]
buffer_load_dwordx4 v232, s[44:47], s80 offen lds
v_mfma_f32_16x16x32_bf16 a[232:235], v[200:203], v[128:131], a[232:235]
ds_read_b64_tr_b16 v[32:33], v240 offset:50688
s_nop 0
v_mfma_f32_16x16x32_bf16 a[236:239], v[204:207], v[128:131], a[236:239]
v_lshlrev_b32 v70, 16, v70
v_lshlrev_b32 v88, 16, v72
v_mfma_f32_16x16x32_bf16 a[208:211], v[208:211], v[128:131], a[208:211]
ds_read_b64_tr_b16 v[34:35], v240 offset:51712
v_and_b32 v89, 0xffff0000, v72
s_add_u32 m0, s83, 17920
v_mfma_f32_16x16x32_bf16 a[212:215], v[212:215], v[128:131], a[212:215]
buffer_load_dwordx4 v233, s[44:47], s80 offen lds
v_mfma_f32_16x16x32_bf16 a[128:131], v[216:219], v[128:131], a[128:131]
ds_read_b64_tr_b16 v[36:37], v241 offset:50688
v_sub_f32 v88, v88, v70
v_mfma_f32_16x16x32_bf16 a[132:135], v[220:223], v[128:131], a[132:135]
v_sub_f32 v89, v89, v70
v_add_f32 v120, v88, v89
v_mfma_f32_16x16x32_bf16 a[216:219], v[192:195], v[132:135], a[216:219]
ds_read_b64_tr_b16 v[38:39], v241 offset:51712
v_mul_f32 v122, v88, v88
s_add_u32 m0, s83, 19072
v_mfma_f32_16x16x32_bf16 a[220:223], v[196:199], v[132:135], a[220:223]
buffer_load_dwordx4 v234, s[44:47], s80 offen lds
v_mfma_f32_16x16x32_bf16 a[136:139], v[200:203], v[132:135], a[136:139]
ds_read_b64_tr_b16 v[40:41], v242 offset:50688
v_mul_f32 v123, v89, v89
v_mfma_f32_16x16x32_bf16 a[140:143], v[204:207], v[132:135], a[140:143]
v_lshlrev_b32 v90, 16, v73
v_and_b32 v91, 0xffff0000, v73
v_mfma_f32_16x16x32_bf16 a[192:195], v[208:211], v[132:135], a[192:195]
ds_read_b64_tr_b16 v[42:43], v242 offset:51712
v_sub_f32 v90, v90, v70
s_add_u32 m0, s83, 20096
v_mfma_f32_16x16x32_bf16 a[196:199], v[212:215], v[132:135], a[196:199]
buffer_load_dwordx4 v235, s[44:47], s80 offen lds
v_mfma_f32_16x16x32_bf16 a[144:147], v[216:219], v[132:135], a[144:147]
ds_read_b64_tr_b16 v[44:45], v243 offset:50688
v_sub_f32 v91, v91, v70
v_mfma_f32_16x16x32_bf16 a[148:151], v[220:223], v[132:135], a[148:151]
s_add_u32 s80, s80, s81
v_add_f32 v124, v90, v91
v_mfma_f32_16x16x32_bf16 a[200:203], v[192:195], v[136:139], a[200:203]
ds_read_b64_tr_b16 v[46:47], v243 offset:51712
v_fmac_f32 v122, v90, v90
s_add_u32 m0, s83, 33792
v_mfma_f32_16x16x32_bf16 a[204:207], v[196:199], v[136:139], a[204:207]
buffer_load_dwordx4 v232, s[44:47], s80 offen lds
v_mfma_f32_16x16x32_bf16 a[152:155], v[200:203], v[136:139], a[152:155]
ds_read_b64_tr_b16 v[48:49], v240 offset:50816
v_add_f32 v120, v120, v124
v_mfma_f32_16x16x32_bf16 a[156:159], v[204:207], v[136:139], a[156:159]
v_fmac_f32 v123, v91, v91
v_lshlrev_b32 v92, 16, v74
v_mfma_f32_16x16x32_bf16 a[96:99], v[208:211], v[136:139], a[96:99]
ds_read_b64_tr_b16 v[50:51], v240 offset:51840
v_and_b32 v93, 0xffff0000, v74
s_add_u32 m0, s83, 34816
v_mfma_f32_16x16x32_bf16 a[100:103], v[212:215], v[136:139], a[100:103]
buffer_load_dwordx4 v233, s[44:47], s80 offen lds
v_mfma_f32_16x16x32_bf16 a[160:163], v[216:219], v[136:139], a[160:163]
ds_read_b64_tr_b16 v[52:53], v241 offset:50816
v_sub_f32 v92, v92, v70
v_mfma_f32_16x16x32_bf16 a[164:167], v[220:223], v[136:139], a[164:167]
v_sub_f32 v93, v93, v70
.La5_LW1_0:
s_cbranch_execz .La5_LW1_4
v_add_f32 v124, v92, v93
v_mfma_f32_16x16x32_bf16 a[104:107], v[192:195], v[140:143], a[104:107]
ds_read_b64_tr_b16 v[54:55], v241 offset:51840
v_fmac_f32 v122, v92, v92
s_add_u32 m0, s83, 35968
v_mfma_f32_16x16x32_bf16 a[108:111], v[196:199], v[140:143], a[108:111]
buffer_load_dwordx4 v234, s[44:47], s80 offen lds
v_mfma_f32_16x16x32_bf16 a[168:171], v[200:203], v[140:143], a[168:171]
ds_read_b64_tr_b16 v[56:57], v242 offset:50816
v_add_f32 v120, v120, v124
v_mfma_f32_16x16x32_bf16 a[172:175], v[204:207], v[140:143], a[172:175]
v_fmac_f32 v123, v93, v93
v_lshlrev_b32 v94, 16, v75
v_mfma_f32_16x16x32_bf16 a[112:115], v[208:211], v[140:143], a[112:115]
ds_read_b64_tr_b16 v[58:59], v242 offset:51840
v_and_b32 v95, 0xffff0000, v75
s_add_u32 m0, s83, 36992
v_mfma_f32_16x16x32_bf16 a[116:119], v[212:215], v[140:143], a[116:119]
buffer_load_dwordx4 v235, s[44:47], s80 offen lds
v_mfma_f32_16x16x32_bf16 a[176:179], v[216:219], v[140:143], a[176:179]
ds_read_b64_tr_b16 v[60:61], v243 offset:50816
v_sub_f32 v94, v94, v70
v_mfma_f32_16x16x32_bf16 a[180:183], v[220:223], v[140:143], a[180:183]
s_add_u32 s80, s80, s81
v_sub_f32 v95, v95, v70
v_mfma_f32_16x16x32_bf16 a[120:123], v[192:195], v[144:147], a[120:123]
ds_read_b64_tr_b16 v[62:63], v243 offset:51840
v_add_f32 v124, v94, v95
v_mfma_f32_16x16x32_bf16 a[124:127], v[196:199], v[144:147], a[124:127]
buffer_load_dword v254, v164, s[44:47], s86 offen
v_fmac_f32 v122, v94, v94
v_mfma_f32_16x16x32_bf16 a[184:187], v[200:203], v[144:147], a[184:187]
ds_read_b128 v[4:7], v237 offset:59392
v_add_f32 v120, v120, v124
v_mfma_f32_16x16x32_bf16 a[188:191], v[204:207], v[144:147], a[188:191]
s_xor_b32 s82, s82, 32768
s_add_u32 s83, s83, 50688
v_mfma_f32_16x16x32_bf16 a[64:67], v[208:211], v[144:147], a[64:67]
ds_read_b128 v[8:11], v237 offset:49152
s_add_u32 s86, s86, 2097152
v_mfma_f32_16x16x32_bf16 a[68:71], v[212:215], v[144:147], a[68:71]
v_fmac_f32 v123, v95, v95
v_lshlrev_b32 v96, 16, v76
v_mfma_f32_16x16x32_bf16 a[0:3], v[216:219], v[144:147], a[0:3]
ds_read_b128 v[12:15], v237 offset:51200
v_and_b32 v97, 0xffff0000, v76
v_mfma_f32_16x16x32_bf16 a[4:7], v[220:223], v[144:147], a[4:7]
v_sub_f32 v96, v96, v70
v_sub_f32 v97, v97, v70
v_mfma_f32_16x16x32_bf16 a[72:75], v[192:195], v[148:151], a[72:75]
ds_read_b128 v[16:19], v237 offset:40960
v_add_f32 v124, v96, v97
v_mfma_f32_16x16x32_bf16 a[76:79], v[196:199], v[148:151], a[76:79]
v_fmac_f32 v122, v96, v96
v_add_f32 v120, v120, v124
v_mfma_f32_16x16x32_bf16 a[8:11], v[200:203], v[148:151], a[8:11]
ds_read_b128 v[20:23], v237 offset:43008
v_fmac_f32 v123, v97, v97
v_mfma_f32_16x16x32_bf16 a[12:15], v[204:207], v[148:151], a[12:15]
v_lshlrev_b32 v98, 16, v77
v_and_b32 v99, 0xffff0000, v77
v_mfma_f32_16x16x32_bf16 a[80:83], v[208:211], v[148:151], a[80:83]
ds_read_b128 v[24:27], v237 offset:32768
v_sub_f32 v98, v98, v70
v_mfma_f32_16x16x32_bf16 a[84:87], v[212:215], v[148:151], a[84:87]
v_sub_f32 v99, v99, v70
v_add_f32 v124, v98, v99
v_mfma_f32_16x16x32_bf16 a[16:19], v[216:219], v[148:151], a[16:19]
ds_read_b128 v[28:31], v237 offset:34816
v_fmac_f32 v122, v98, v98
v_mfma_f32_16x16x32_bf16 a[20:23], v[220:223], v[148:151], a[20:23]
v_add_f32 v120, v120, v124
v_fmac_f32 v123, v99, v99
v_mfma_f32_16x16x32_bf16 a[88:91], v[192:195], v[152:155], a[88:91]
v_lshlrev_b32 v100, 16, v78
v_and_b32 v101, 0xffff0000, v78
v_mfma_f32_16x16x32_bf16 a[92:95], v[196:199], v[152:155], a[92:95]
v_sub_f32 v100, v100, v70
v_sub_f32 v101, v101, v70
v_mfma_f32_16x16x32_bf16 a[24:27], v[200:203], v[152:155], a[24:27]
v_add_f32 v124, v100, v101
v_fmac_f32 v122, v100, v100
v_mfma_f32_16x16x32_bf16 a[28:31], v[204:207], v[152:155], a[28:31]
v_add_f32 v120, v120, v124
v_fmac_f32 v123, v101, v101
v_mfma_f32_16x16x32_bf16 a[240:243], v[208:211], v[152:155], a[240:243]
v_lshlrev_b32 v102, 16, v79
v_and_b32 v103, 0xffff0000, v79
v_mfma_f32_16x16x32_bf16 a[244:247], v[212:215], v[152:155], a[244:247]
v_sub_f32 v102, v102, v70
v_sub_f32 v103, v103, v70
v_mfma_f32_16x16x32_bf16 a[32:35], v[216:219], v[152:155], a[32:35]
v_add_f32 v124, v102, v103
v_fmac_f32 v122, v102, v102
v_mfma_f32_16x16x32_bf16 a[36:39], v[220:223], v[152:155], a[36:39]
v_add_f32 v120, v120, v124
v_fmac_f32 v123, v103, v103
v_mfma_f32_16x16x32_bf16 a[248:251], v[192:195], v[156:159], a[248:251]
v_lshlrev_b32 v104, 16, v80
v_and_b32 v105, 0xffff0000, v80
v_mfma_f32_16x16x32_bf16 a[252:255], v[196:199], v[156:159], a[252:255]
v_sub_f32 v104, v104, v70
v_sub_f32 v105, v105, v70
v_mfma_f32_16x16x32_bf16 a[40:43], v[200:203], v[156:159], a[40:43]
v_add_f32 v124, v104, v105
v_fmac_f32 v122, v104, v104
v_mfma_f32_16x16x32_bf16 a[44:47], v[204:207], v[156:159], a[44:47]
v_add_f32 v120, v120, v124
v_fmac_f32 v123, v105, v105
v_mfma_f32_16x16x32_bf16 a[48:51], v[208:211], v[156:159], a[48:51]
v_lshlrev_b32 v106, 16, v81
v_and_b32 v107, 0xffff0000, v81
v_mfma_f32_16x16x32_bf16 a[52:55], v[212:215], v[156:159], a[52:55]
v_sub_f32 v106, v106, v70
v_sub_f32 v107, v107, v70
v_mfma_f32_16x16x32_bf16 a[56:59], v[216:219], v[156:159], a[56:59]
v_add_f32 v124, v106, v107
v_fmac_f32 v122, v106, v106
v_mfma_f32_16x16x32_bf16 a[60:63], v[220:223], v[156:159], a[60:63]
v_add_f32 v120, v120, v124
v_fmac_f32 v123, v107, v107
s_waitcnt lgkmcnt(0)
s_waitcnt vmcnt(9)
s_barrier
v_mfma_f32_16x16x32_bf16 a[224:227], v[32:35], v[0:3], a[224:227]
ds_read_b128 v[128:131], v236 offset:24576
s_xor_b32 m0, s82, 0
v_mfma_f32_16x16x32_bf16 a[228:231], v[36:39], v[0:3], a[228:231]
buffer_load_dwordx4 v224, s[40:43], s79 offen lds
v_mfma_f32_16x16x32_bf16 a[232:235], v[40:43], v[0:3], a[232:235]
ds_read_b64_tr_b16 v[192:193], v240 offset:0
v_lshlrev_b32 v108, 16, v82
v_mfma_f32_16x16x32_bf16 a[236:239], v[44:47], v[0:3], a[236:239]
v_and_b32 v109, 0xffff0000, v82
v_sub_f32 v108, v108, v70
v_mfma_f32_16x16x32_bf16 a[208:211], v[48:51], v[0:3], a[208:211]
ds_read_b64_tr_b16 v[194:195], v240 offset:1024
v_sub_f32 v109, v109, v70
v_mfma_f32_16x16x32_bf16 a[212:215], v[52:55], v[0:3], a[212:215]
v_add_f32 v124, v108, v109
v_fmac_f32 v122, v108, v108
v_mfma_f32_16x16x32_bf16 a[128:131], v[56:59], v[0:3], a[128:131]
ds_read_b64_tr_b16 v[196:197], v241 offset:0
v_add_f32 v120, v120, v124
v_mfma_f32_16x16x32_bf16 a[132:135], v[60:63], v[0:3], a[132:135]
v_fmac_f32 v123, v109, v109
v_lshlrev_b32 v110, 16, v83
v_mfma_f32_16x16x32_bf16 a[216:219], v[32:35], v[4:7], a[216:219]
ds_read_b64_tr_b16 v[198:199], v241 offset:1024
v_and_b32 v111, 0xffff0000, v83
s_xor_b32 m0, s82, 1024
v_mfma_f32_16x16x32_bf16 a[220:223], v[36:39], v[4:7], a[220:223]
buffer_load_dwordx4 v225, s[40:43], s79 offen lds
v_mfma_f32_16x16x32_bf16 a[136:139], v[40:43], v[4:7], a[136:139]
ds_read_b64_tr_b16 v[200:201], v242 offset:0
v_sub_f32 v110, v110, v70
v_mfma_f32_16x16x32_bf16 a[140:143], v[44:47], v[4:7], a[140:143]
v_sub_f32 v111, v111, v70
v_add_f32 v124, v110, v111
v_mfma_f32_16x16x32_bf16 a[192:195], v[48:51], v[4:7], a[192:195]
ds_read_b64_tr_b16 v[202:203], v242 offset:1024
v_fmac_f32 v122, v110, v110
v_mfma_f32_16x16x32_bf16 a[196:199], v[52:55], v[4:7], a[196:199]
v_add_f32 v120, v120, v124
v_fmac_f32 v123, v111, v111
v_mfma_f32_16x16x32_bf16 a[144:147], v[56:59], v[4:7], a[144:147]
ds_read_b64_tr_b16 v[204:205], v243 offset:0
v_lshlrev_b32 v112, 16, v84
v_mfma_f32_16x16x32_bf16 a[148:151], v[60:63], v[4:7], a[148:151]
v_and_b32 v113, 0xffff0000, v84
v_sub_f32 v112, v112, v70
v_mfma_f32_16x16x32_bf16 a[200:203], v[32:35], v[8:11], a[200:203]
ds_read_b64_tr_b16 v[206:207], v243 offset:1024
v_sub_f32 v113, v113, v70
s_xor_b32 m0, s82, 2048
v_mfma_f32_16x16x32_bf16 a[204:207], v[36:39], v[8:11], a[204:207]
buffer_load_dwordx4 v226, s[40:43], s79 offen lds
v_mfma_f32_16x16x32_bf16 a[152:155], v[40:43], v[8:11], a[152:155]
ds_read_b64_tr_b16 v[208:209], v240 offset:128
v_add_f32 v124, v112, v113
v_mfma_f32_16x16x32_bf16 a[156:159], v[44:47], v[8:11], a[156:159]
v_fmac_f32 v122, v112, v112
v_add_f32 v120, v120, v124
v_mfma_f32_16x16x32_bf16 a[96:99], v[48:51], v[8:11], a[96:99]
ds_read_b64_tr_b16 v[210:211], v240 offset:1152
v_fmac_f32 v123, v113, v113
s_add_u32 m0, s83, 0
v_mfma_f32_16x16x32_bf16 a[100:103], v[52:55], v[8:11], a[100:103]
buffer_load_dwordx4 v232, s[44:47], s80 offen lds
v_mfma_f32_16x16x32_bf16 a[160:163], v[56:59], v[8:11], a[160:163]
ds_read_b64_tr_b16 v[212:213], v241 offset:128
v_lshlrev_b32 v114, 16, v85
v_mfma_f32_16x16x32_bf16 a[164:167], v[60:63], v[8:11], a[164:167]
v_and_b32 v115, 0xffff0000, v85
v_sub_f32 v114, v114, v70
v_mfma_f32_16x16x32_bf16 a[104:107], v[32:35], v[12:15], a[104:107]
ds_read_b64_tr_b16 v[214:215], v241 offset:1152
v_sub_f32 v115, v115, v70
s_xor_b32 m0, s82, 3072
v_mfma_f32_16x16x32_bf16 a[108:111], v[36:39], v[12:15], a[108:111]
buffer_load_dwordx4 v227, s[40:43], s79 offen lds
v_mfma_f32_16x16x32_bf16 a[168:171], v[40:43], v[12:15], a[168:171]
ds_read_b64_tr_b16 v[216:217], v242 offset:128
v_add_f32 v124, v114, v115
v_mfma_f32_16x16x32_bf16 a[172:175], v[44:47], v[12:15], a[172:175]
v_fmac_f32 v122, v114, v114
v_add_f32 v120, v120, v124
v_mfma_f32_16x16x32_bf16 a[112:115], v[48:51], v[12:15], a[112:115]
ds_read_b64_tr_b16 v[218:219], v242 offset:1152
v_fmac_f32 v123, v115, v115
s_add_u32 m0, s83, 1024
v_mfma_f32_16x16x32_bf16 a[116:119], v[52:55], v[12:15], a[116:119]
buffer_load_dwordx4 v233, s[44:47], s80 offen lds
v_mfma_f32_16x16x32_bf16 a[176:179], v[56:59], v[12:15], a[176:179]
ds_read_b64_tr_b16 v[220:221], v243 offset:128
v_lshlrev_b32 v116, 16, v86
v_mfma_f32_16x16x32_bf16 a[180:183], v[60:63], v[12:15], a[180:183]
v_and_b32 v117, 0xffff0000, v86
v_sub_f32 v116, v116, v70
v_mfma_f32_16x16x32_bf16 a[120:123], v[32:35], v[16:19], a[120:123]
ds_read_b64_tr_b16 v[222:223], v243 offset:1152
v_sub_f32 v117, v117, v70
s_xor_b32 m0, s82, 4096
v_mfma_f32_16x16x32_bf16 a[124:127], v[36:39], v[16:19], a[124:127]
buffer_load_dwordx4 v228, s[40:43], s79 offen lds
v_mfma_f32_16x16x32_bf16 a[184:187], v[40:43], v[16:19], a[184:187]
ds_read_b128 v[132:135], v236 offset:26624
v_add_f32 v124, v116, v117
v_mfma_f32_16x16x32_bf16 a[188:191], v[44:47], v[16:19], a[188:191]
v_fmac_f32 v122, v116, v116
v_add_f32 v120, v120, v124
v_mfma_f32_16x16x32_bf16 a[64:67], v[48:51], v[16:19], a[64:67]
ds_read_b128 v[136:139], v236 offset:16384
v_fmac_f32 v123, v117, v117
s_add_u32 m0, s83, 2176
v_mfma_f32_16x16x32_bf16 a[68:71], v[52:55], v[16:19], a[68:71]
buffer_load_dwordx4 v234, s[44:47], s80 offen lds
v_mfma_f32_16x16x32_bf16 a[0:3], v[56:59], v[16:19], a[0:3]
ds_read_b128 v[140:143], v236 offset:18432
v_lshlrev_b32 v118, 16, v87
v_mfma_f32_16x16x32_bf16 a[4:7], v[60:63], v[16:19], a[4:7]
v_and_b32 v119, 0xffff0000, v87
v_sub_f32 v118, v118, v70
v_mfma_f32_16x16x32_bf16 a[72:75], v[32:35], v[20:23], a[72:75]
ds_read_b128 v[144:147], v236 offset:8192
v_sub_f32 v119, v119, v70
s_xor_b32 m0, s82, 5120
v_mfma_f32_16x16x32_bf16 a[76:79], v[36:39], v[20:23], a[76:79]
buffer_load_dwordx4 v229, s[40:43], s79 offen lds
v_mfma_f32_16x16x32_bf16 a[8:11], v[40:43], v[20:23], a[8:11]
ds_read_b128 v[148:151], v236 offset:10240
v_add_f32 v124, v118, v119
v_mfma_f32_16x16x32_bf16 a[12:15], v[44:47], v[20:23], a[12:15]
v_fmac_f32 v122, v118, v118
v_add_f32 v120, v120, v124
v_mfma_f32_16x16x32_bf16 a[80:83], v[48:51], v[20:23], a[80:83]
ds_read_b128 v[152:155], v236 offset:0
v_fmac_f32 v123, v119, v119
s_add_u32 m0, s83, 3200
v_mfma_f32_16x16x32_bf16 a[84:87], v[52:55], v[20:23], a[84:87]
buffer_load_dwordx4 v235, s[44:47], s80 offen lds
v_mfma_f32_16x16x32_bf16 a[16:19], v[56:59], v[20:23], a[16:19]
ds_read_b128 v[156:159], v236 offset:2048
v_add_f32 v121, v122, v123
v_mfma_f32_16x16x32_bf16 a[20:23], v[60:63], v[20:23], a[20:23]
s_add_u32 s80, s80, s81
s_add_u32 s83, s83, 16896
v_mfma_f32_16x16x32_bf16 a[88:91], v[32:35], v[24:27], a[88:91]
v_add_f32_dpp v120, v120, v120 row_ror:8 row_mask:0xf bank_mask:0xf
s_xor_b32 m0, s82, 6144
v_mfma_f32_16x16x32_bf16 a[92:95], v[36:39], v[24:27], a[92:95]
buffer_load_dwordx4 v230, s[40:43], s79 offen lds
s_cmp_eq_u32 s83, s97
s_cselect_b32 s83, s90, s83
v_mfma_f32_16x16x32_bf16 a[24:27], v[40:43], v[24:27], a[24:27]
v_add_f32_dpp v121, v121, v121 row_ror:8 row_mask:0xf bank_mask:0xf
v_mov_b32 v124, v120
v_mfma_f32_16x16x32_bf16 a[28:31], v[44:47], v[24:27], a[28:31]
v_mov_b32 v125, v121
v_mfma_f32_16x16x32_bf16 a[240:243], v[48:51], v[24:27], a[240:243]
v_mfma_f32_16x16x32_bf16 a[244:247], v[52:55], v[24:27], a[244:247]
v_permlane16_swap_b32 v124, v120
v_permlane16_swap_b32 v125, v121
v_mfma_f32_16x16x32_bf16 a[32:35], v[56:59], v[24:27], a[32:35]
v_add_f32 v120, v120, v124
v_add_f32 v121, v121, v125
v_mfma_f32_16x16x32_bf16 a[36:39], v[60:63], v[24:27], a[36:39]
v_mov_b32 v124, v120
v_mov_b32 v125, v121
v_mfma_f32_16x16x32_bf16 a[248:251], v[32:35], v[28:31], a[248:251]
s_xor_b32 m0, s82, 7168
v_mfma_f32_16x16x32_bf16 a[252:255], v[36:39], v[28:31], a[252:255]
buffer_load_dwordx4 v231, s[40:43], s79 offen lds
v_mfma_f32_16x16x32_bf16 a[40:43], v[40:43], v[28:31], a[40:43]
v_permlane32_swap_b32 v124, v120
v_permlane32_swap_b32 v125, v121
v_mfma_f32_16x16x32_bf16 a[44:47], v[44:47], v[28:31], a[44:47]
s_add_u32 s79, s79, 128
s_xor_b32 s82, s82, 32768
v_mfma_f32_16x16x32_bf16 a[48:51], v[48:51], v[28:31], a[48:51]
v_add_f32 v120, v120, v124
v_add_f32 v121, v121, v125
v_mfma_f32_16x16x32_bf16 a[52:55], v[52:55], v[28:31], a[52:55]
ds_write_b64 v127, v[120:121]
s_nop 0
v_mfma_f32_16x16x32_bf16 a[56:59], v[56:59], v[28:31], a[56:59]
v_readfirstlane_b32 s92, v252
v_readfirstlane_b32 s93, v253
v_mfma_f32_16x16x32_bf16 a[60:63], v[60:63], v[28:31], a[60:63]
s_add_u32 s92, s92, 1
s_addc_u32 s93, s93, 0
s_waitcnt lgkmcnt(0)
s_waitcnt vmcnt(17)
s_barrier
v_mfma_f32_16x16x32_bf16 a[224:227], v[192:195], v[128:131], a[224:227]
ds_read_b128 v[0:3], v237 offset:24576
ds_read_b128 v[72:75], v126
v_mfma_f32_16x16x32_bf16 a[228:231], v[196:199], v[128:131], a[228:231]
ds_read_b128 v[76:79], v126 offset:16
v_mfma_f32_16x16x32_bf16 a[232:235], v[200:203], v[128:131], a[232:235]
ds_read_b64_tr_b16 v[32:33], v240 offset:16896
v_mfma_f32_16x16x32_bf16 a[236:239], v[204:207], v[128:131], a[236:239]
v_mfma_f32_16x16x32_bf16 a[208:211], v[208:211], v[128:131], a[208:211]
ds_read_b64_tr_b16 v[34:35], v240 offset:17920
v_mov_b32 v252, s92
v_mfma_f32_16x16x32_bf16 a[212:215], v[212:215], v[128:131], a[212:215]
v_mov_b32 v253, s93
v_mov_b32 v160, 0
v_mfma_f32_16x16x32_bf16 a[128:131], v[216:219], v[128:131], a[128:131]
ds_read_b64_tr_b16 v[36:37], v241 offset:16896
v_mov_b32 v161, 0
v_mfma_f32_16x16x32_bf16 a[132:135], v[220:223], v[128:131], a[132:135]
v_mov_b32 v162, 0
v_mov_b32 v163, 0
v_mfma_f32_16x16x32_bf16 a[216:219], v[192:195], v[132:135], a[216:219]
ds_read_b64_tr_b16 v[38:39], v241 offset:17920
buffer_store_dwordx4 v[160:163], v255, s[52:55], s95 offen sc1
v_mfma_f32_16x16x32_bf16 a[220:223], v[196:199], v[132:135], a[220:223]
s_waitcnt lgkmcnt(4)
v_add_f32 v72, v72, v74
v_mfma_f32_16x16x32_bf16 a[136:139], v[200:203], v[132:135], a[136:139]
ds_read_b64_tr_b16 v[40:41], v242 offset:16896
v_add_f32 v73, v73, v75
v_mfma_f32_16x16x32_bf16 a[140:143], v[204:207], v[132:135], a[140:143]
v_add_f32 v76, v76, v78
v_add_f32 v77, v77, v79
v_mfma_f32_16x16x32_bf16 a[192:195], v[208:211], v[132:135], a[192:195]
ds_read_b64_tr_b16 v[42:43], v242 offset:17920
v_add_f32 v72, v72, v76
v_mfma_f32_16x16x32_bf16 a[196:199], v[212:215], v[132:135], a[196:199]
v_add_f32 v73, v73, v77
v_mul_f32 v72, 0x3a800000, v72
v_mfma_f32_16x16x32_bf16 a[144:147], v[216:219], v[132:135], a[144:147]
ds_read_b64_tr_b16 v[44:45], v243 offset:16896
v_mul_f32 v73, 0x3a800000, v73
v_mfma_f32_16x16x32_bf16 a[148:151], v[220:223], v[132:135], a[148:151]
v_add_f32 v124, v70, v72
v_fma_f32 v125, -v72, v72, v73
v_mfma_f32_16x16x32_bf16 a[200:203], v[192:195], v[136:139], a[200:203]
ds_read_b64_tr_b16 v[46:47], v243 offset:17920
v_cvt_pk_bf16_f32 v124, v124, v124
v_mfma_f32_16x16x32_bf16 a[204:207], v[196:199], v[136:139], a[204:207]
v_lshlrev_b32 v124, 16, v124
v_max_f32 v125, 0, v125
v_mfma_f32_16x16x32_bf16 a[152:155], v[200:203], v[136:139], a[152:155]
ds_read_b64_tr_b16 v[48:49], v240 offset:17024
v_cvt_pk_bf16_f32 v125, v125, v125
v_mfma_f32_16x16x32_bf16 a[156:159], v[204:207], v[136:139], a[156:159]
v_lshlrev_b32 v125, 16, v125
v_add_f32 v125, 0x3727c5ac, v125
v_mfma_f32_16x16x32_bf16 a[96:99], v[208:211], v[136:139], a[96:99]
ds_read_b64_tr_b16 v[50:51], v240 offset:18048
v_cvt_pk_bf16_f32 v125, v125, v125
v_mfma_f32_16x16x32_bf16 a[100:103], v[212:215], v[136:139], a[100:103]
v_lshlrev_b32 v125, 16, v125
v_mfma_f32_16x16x32_bf16 a[160:163], v[216:219], v[136:139], a[160:163]
ds_read_b64_tr_b16 v[52:53], v241 offset:17024
v_mfma_f32_16x16x32_bf16 a[164:167], v[220:223], v[136:139], a[164:167]
v_sqrt_f32 v125, v125
v_mfma_f32_16x16x32_bf16 a[104:107], v[192:195], v[140:143], a[104:107]
ds_read_b64_tr_b16 v[54:55], v241 offset:18048
v_sub_f32 v124, v70, v124
v_mfma_f32_16x16x32_bf16 a[108:111], v[196:199], v[140:143], a[108:111]
v_cvt_pk_bf16_f32 v125, v125, v125
v_lshlrev_b32 v125, 16, v125
v_mfma_f32_16x16x32_bf16 a[168:171], v[200:203], v[140:143], a[168:171]
ds_read_b64_tr_b16 v[56:57], v242 offset:17024
v_mfma_f32_16x16x32_bf16 a[172:175], v[204:207], v[140:143], a[172:175]
v_rcp_f32 v125, v125
v_mfma_f32_16x16x32_bf16 a[112:115], v[208:211], v[140:143], a[112:115]
ds_read_b64_tr_b16 v[58:59], v242 offset:18048
v_mul_f32 v125, 0x3e0293ee, v125
v_mfma_f32_16x16x32_bf16 a[116:119], v[212:215], v[140:143], a[116:119]
v_mul_f32 v124, v124, v125
v_fma_f32 v88, v88, v125, v124
v_mfma_f32_16x16x32_bf16 a[176:179], v[216:219], v[140:143], a[176:179]
ds_read_b64_tr_b16 v[60:61], v243 offset:17024
v_fma_f32 v89, v89, v125, v124
v_mfma_f32_16x16x32_bf16 a[180:183], v[220:223], v[140:143], a[180:183]
v_cvt_pk_bf16_f32 v72, v88, v89
v_fma_f32 v90, v90, v125, v124
v_mfma_f32_16x16x32_bf16 a[120:123], v[192:195], v[144:147], a[120:123]
ds_read_b64_tr_b16 v[62:63], v243 offset:18048
v_fma_f32 v91, v91, v125, v124
s_add_u32 m0, s83, 0
v_mfma_f32_16x16x32_bf16 a[124:127], v[196:199], v[144:147], a[124:127]
buffer_load_dwordx4 v232, s[44:47], s80 offen lds
v_mfma_f32_16x16x32_bf16 a[184:187], v[200:203], v[144:147], a[184:187]
ds_read_b128 v[4:7], v237 offset:26624
v_cvt_pk_bf16_f32 v73, v90, v91
v_mfma_f32_16x16x32_bf16 a[188:191], v[204:207], v[144:147], a[188:191]
v_fma_f32 v92, v92, v125, v124
v_fma_f32 v93, v93, v125, v124
v_mfma_f32_16x16x32_bf16 a[64:67], v[208:211], v[144:147], a[64:67]
ds_read_b128 v[8:11], v237 offset:16384
v_cvt_pk_bf16_f32 v74, v92, v93
s_add_u32 m0, s83, 1024
v_mfma_f32_16x16x32_bf16 a[68:71], v[212:215], v[144:147], a[68:71]
buffer_load_dwordx4 v233, s[44:47], s80 offen lds
v_mfma_f32_16x16x32_bf16 a[0:3], v[216:219], v[144:147], a[0:3]
ds_read_b128 v[12:15], v237 offset:18432
v_fma_f32 v94, v94, v125, v124
v_mfma_f32_16x16x32_bf16 a[4:7], v[220:223], v[144:147], a[4:7]
v_fma_f32 v95, v95, v125, v124
v_cvt_pk_bf16_f32 v75, v94, v95
v_mfma_f32_16x16x32_bf16 a[72:75], v[192:195], v[148:151], a[72:75]
ds_read_b128 v[16:19], v237 offset:8192
buffer_store_dwordx4 v[72:75], v71, s[56:59], 0 offen offset:0 sc1
v_fma_f32 v96, v96, v125, v124
s_add_u32 m0, s83, 2176
v_mfma_f32_16x16x32_bf16 a[76:79], v[196:199], v[148:151], a[76:79]
buffer_load_dwordx4 v234, s[44:47], s80 offen lds
v_fma_f32 v97, v97, v125, v124
v_mfma_f32_16x16x32_bf16 a[8:11], v[200:203], v[148:151], a[8:11]
ds_read_b128 v[20:23], v237 offset:10240
v_cvt_pk_bf16_f32 v76, v96, v97
v_fma_f32 v98, v98, v125, v124
v_mfma_f32_16x16x32_bf16 a[12:15], v[204:207], v[148:151], a[12:15]
v_fma_f32 v99, v99, v125, v124
v_cvt_pk_bf16_f32 v77, v98, v99
v_fma_f32 v100, v100, v125, v124
v_mfma_f32_16x16x32_bf16 a[80:83], v[208:211], v[148:151], a[80:83]
ds_read_b128 v[24:27], v237 offset:0
v_fma_f32 v101, v101, v125, v124
v_cvt_pk_bf16_f32 v78, v100, v101
s_add_u32 m0, s83, 3200
v_mfma_f32_16x16x32_bf16 a[84:87], v[212:215], v[148:151], a[84:87]
buffer_load_dwordx4 v235, s[44:47], s80 offen lds
v_fma_f32 v102, v102, v125, v124
v_mfma_f32_16x16x32_bf16 a[16:19], v[216:219], v[148:151], a[16:19]
ds_read_b128 v[28:31], v237 offset:2048
v_fma_f32 v103, v103, v125, v124
v_cvt_pk_bf16_f32 v79, v102, v103
v_mfma_f32_16x16x32_bf16 a[20:23], v[220:223], v[148:151], a[20:23]
s_add_u32 s80, s80, s81
s_add_u32 s83, s83, 16896
buffer_store_dwordx4 v[76:79], v71, s[56:59], 0 offen offset:2048 sc1
v_mfma_f32_16x16x32_bf16 a[88:91], v[192:195], v[152:155], a[88:91]
v_fma_f32 v104, v104, v125, v124
v_fma_f32 v105, v105, v125, v124
v_cvt_pk_bf16_f32 v80, v104, v105
v_mfma_f32_16x16x32_bf16 a[92:95], v[196:199], v[152:155], a[92:95]
s_cmp_eq_u32 s83, s97
s_cselect_b32 s83, s90, s83
v_fma_f32 v106, v106, v125, v124
v_mfma_f32_16x16x32_bf16 a[24:27], v[200:203], v[152:155], a[24:27]
v_fma_f32 v107, v107, v125, v124
v_cvt_pk_bf16_f32 v81, v106, v107
v_fma_f32 v108, v108, v125, v124
v_mfma_f32_16x16x32_bf16 a[28:31], v[204:207], v[152:155], a[28:31]
v_fma_f32 v109, v109, v125, v124
v_cvt_pk_bf16_f32 v82, v108, v109
v_fma_f32 v110, v110, v125, v124
v_mfma_f32_16x16x32_bf16 a[240:243], v[208:211], v[152:155], a[240:243]
v_fma_f32 v111, v111, v125, v124
v_cvt_pk_bf16_f32 v83, v110, v111
buffer_store_dwordx4 v[80:83], v71, s[56:59], s88 offen offset:0 sc1
v_mfma_f32_16x16x32_bf16 a[244:247], v[212:215], v[152:155], a[244:247]
v_fma_f32 v112, v112, v125, v124
v_fma_f32 v113, v113, v125, v124
v_cvt_pk_bf16_f32 v84, v112, v113
v_mfma_f32_16x16x32_bf16 a[32:35], v[216:219], v[152:155], a[32:35]
v_fma_f32 v114, v114, v125, v124
v_fma_f32 v115, v115, v125, v124
v_cvt_pk_bf16_f32 v85, v114, v115
v_mfma_f32_16x16x32_bf16 a[36:39], v[220:223], v[152:155], a[36:39]
v_fma_f32 v116, v116, v125, v124
v_fma_f32 v117, v117, v125, v124
v_cvt_pk_bf16_f32 v86, v116, v117
v_mfma_f32_16x16x32_bf16 a[248:251], v[192:195], v[156:159], a[248:251]
v_fma_f32 v118, v118, v125, v124
v_fma_f32 v119, v119, v125, v124
v_cvt_pk_bf16_f32 v87, v118, v119
v_mfma_f32_16x16x32_bf16 a[252:255], v[196:199], v[156:159], a[252:255]
buffer_store_dwordx4 v[84:87], v71, s[56:59], s88 offen offset:2048 sc1
v_mfma_f32_16x16x32_bf16 a[40:43], v[200:203], v[156:159], a[40:43]
v_add_u32 v240, s84, v240
v_add_u32 v241, s84, v241
v_mfma_f32_16x16x32_bf16 a[44:47], v[204:207], v[156:159], a[44:47]
v_add_u32 v242, s84, v242
v_add_u32 v243, s84, v243
v_mfma_f32_16x16x32_bf16 a[48:51], v[208:211], v[156:159], a[48:51]
v_xor_b32 v236, 0x8000, v236
v_xor_b32 v237, 0x8000, v237
v_mfma_f32_16x16x32_bf16 a[52:55], v[212:215], v[156:159], a[52:55]
s_sub_u32 s84, 0, s84
v_mfma_f32_16x16x32_bf16 a[56:59], v[216:219], v[156:159], a[56:59]
v_mfma_f32_16x16x32_bf16 a[60:63], v[220:223], v[156:159], a[60:63]
s_waitcnt lgkmcnt(0)
.p2align 6
.La5_LK_LOOP:
s_waitcnt vmcnt(9)
s_barrier
v_mfma_f32_16x16x32_bf16 a[224:227], v[32:35], v[0:3], a[224:227]
ds_read_b128 v[128:131], v236 offset:24576
s_xor_b32 m0, s82, 0
v_mfma_f32_16x16x32_bf16 a[228:231], v[36:39], v[0:3], a[228:231]
buffer_load_dwordx4 v224, s[40:43], s79 offen lds
v_mfma_f32_16x16x32_bf16 a[232:235], v[40:43], v[0:3], a[232:235]
ds_read_b64_tr_b16 v[192:193], v240 offset:0
v_mfma_f32_16x16x32_bf16 a[236:239], v[44:47], v[0:3], a[236:239]
v_mfma_f32_16x16x32_bf16 a[208:211], v[48:51], v[0:3], a[208:211]
ds_read_b64_tr_b16 v[194:195], v240 offset:1024
s_xor_b32 m0, s82, 1024
v_mfma_f32_16x16x32_bf16 a[212:215], v[52:55], v[0:3], a[212:215]
buffer_load_dwordx4 v225, s[40:43], s79 offen lds
v_mfma_f32_16x16x32_bf16 a[128:131], v[56:59], v[0:3], a[128:131]
ds_read_b64_tr_b16 v[196:197], v241 offset:0
v_mfma_f32_16x16x32_bf16 a[132:135], v[60:63], v[0:3], a[132:135]
v_mfma_f32_16x16x32_bf16 a[216:219], v[32:35], v[4:7], a[216:219]
ds_read_b64_tr_b16 v[198:199], v241 offset:1024
s_xor_b32 m0, s82, 2048
v_mfma_f32_16x16x32_bf16 a[220:223], v[36:39], v[4:7], a[220:223]
buffer_load_dwordx4 v226, s[40:43], s79 offen lds
v_mfma_f32_16x16x32_bf16 a[136:139], v[40:43], v[4:7], a[136:139]
ds_read_b64_tr_b16 v[200:201], v242 offset:0
v_mfma_f32_16x16x32_bf16 a[140:143], v[44:47], v[4:7], a[140:143]
v_mfma_f32_16x16x32_bf16 a[192:195], v[48:51], v[4:7], a[192:195]
ds_read_b64_tr_b16 v[202:203], v242 offset:1024
s_xor_b32 m0, s82, 3072
v_mfma_f32_16x16x32_bf16 a[196:199], v[52:55], v[4:7], a[196:199]
buffer_load_dwordx4 v227, s[40:43], s79 offen lds
v_mfma_f32_16x16x32_bf16 a[144:147], v[56:59], v[4:7], a[144:147]
ds_read_b64_tr_b16 v[204:205], v243 offset:0
v_mfma_f32_16x16x32_bf16 a[148:151], v[60:63], v[4:7], a[148:151]
v_mfma_f32_16x16x32_bf16 a[200:203], v[32:35], v[8:11], a[200:203]
ds_read_b64_tr_b16 v[206:207], v243 offset:1024
s_xor_b32 m0, s82, 4096
v_mfma_f32_16x16x32_bf16 a[204:207], v[36:39], v[8:11], a[204:207]
buffer_load_dwordx4 v228, s[40:43], s79 offen lds
v_mfma_f32_16x16x32_bf16 a[152:155], v[40:43], v[8:11], a[152:155]
ds_read_b64_tr_b16 v[208:209], v240 offset:128
v_mfma_f32_16x16x32_bf16 a[156:159], v[44:47], v[8:11], a[156:159]
v_mfma_f32_16x16x32_bf16 a[96:99], v[48:51], v[8:11], a[96:99]
ds_read_b64_tr_b16 v[210:211], v240 offset:1152
s_xor_b32 m0, s82, 5120
v_mfma_f32_16x16x32_bf16 a[100:103], v[52:55], v[8:11], a[100:103]
buffer_load_dwordx4 v229, s[40:43], s79 offen lds
v_mfma_f32_16x16x32_bf16 a[160:163], v[56:59], v[8:11], a[160:163]
ds_read_b64_tr_b16 v[212:213], v241 offset:128
v_mfma_f32_16x16x32_bf16 a[164:167], v[60:63], v[8:11], a[164:167]
v_mfma_f32_16x16x32_bf16 a[104:107], v[32:35], v[12:15], a[104:107]
ds_read_b64_tr_b16 v[214:215], v241 offset:1152
s_xor_b32 m0, s82, 6144
v_mfma_f32_16x16x32_bf16 a[108:111], v[36:39], v[12:15], a[108:111]
buffer_load_dwordx4 v230, s[40:43], s79 offen lds
v_mfma_f32_16x16x32_bf16 a[168:171], v[40:43], v[12:15], a[168:171]
ds_read_b64_tr_b16 v[216:217], v242 offset:128
v_mfma_f32_16x16x32_bf16 a[172:175], v[44:47], v[12:15], a[172:175]
v_mfma_f32_16x16x32_bf16 a[112:115], v[48:51], v[12:15], a[112:115]
ds_read_b64_tr_b16 v[218:219], v242 offset:1152
s_xor_b32 m0, s82, 7168
v_mfma_f32_16x16x32_bf16 a[116:119], v[52:55], v[12:15], a[116:119]
buffer_load_dwordx4 v231, s[40:43], s79 offen lds
v_mfma_f32_16x16x32_bf16 a[176:179], v[56:59], v[12:15], a[176:179]
ds_read_b64_tr_b16 v[220:221], v243 offset:128
v_mfma_f32_16x16x32_bf16 a[180:183], v[60:63], v[12:15], a[180:183]
s_add_u32 s79, s79, 128
s_xor_b32 s82, s82, 32768
v_mfma_f32_16x16x32_bf16 a[120:123], v[32:35], v[16:19], a[120:123]
ds_read_b64_tr_b16 v[222:223], v243 offset:1152
s_add_u32 m0, s83, 0
v_mfma_f32_16x16x32_bf16 a[124:127], v[36:39], v[16:19], a[124:127]
buffer_load_dwordx4 v232, s[44:47], s80 offen lds
v_mfma_f32_16x16x32_bf16 a[184:187], v[40:43], v[16:19], a[184:187]
ds_read_b128 v[132:135], v236 offset:26624
v_mfma_f32_16x16x32_bf16 a[188:191], v[44:47], v[16:19], a[188:191]
v_mfma_f32_16x16x32_bf16 a[64:67], v[48:51], v[16:19], a[64:67]
ds_read_b128 v[136:139], v236 offset:16384
s_add_u32 m0, s83, 1024
v_mfma_f32_16x16x32_bf16 a[68:71], v[52:55], v[16:19], a[68:71]
buffer_load_dwordx4 v233, s[44:47], s80 offen lds
v_mfma_f32_16x16x32_bf16 a[0:3], v[56:59], v[16:19], a[0:3]
.La5_LW1_1:
s_cbranch_execz .La5_LW1_5
ds_read_b128 v[140:143], v236 offset:18432
v_mfma_f32_16x16x32_bf16 a[4:7], v[60:63], v[16:19], a[4:7]
v_mfma_f32_16x16x32_bf16 a[72:75], v[32:35], v[20:23], a[72:75]
ds_read_b128 v[144:147], v236 offset:8192
s_add_u32 m0, s83, 2176
v_mfma_f32_16x16x32_bf16 a[76:79], v[36:39], v[20:23], a[76:79]
buffer_load_dwordx4 v234, s[44:47], s80 offen lds
v_mfma_f32_16x16x32_bf16 a[8:11], v[40:43], v[20:23], a[8:11]
ds_read_b128 v[148:151], v236 offset:10240
v_mfma_f32_16x16x32_bf16 a[12:15], v[44:47], v[20:23], a[12:15]
v_mfma_f32_16x16x32_bf16 a[80:83], v[48:51], v[20:23], a[80:83]
ds_read_b128 v[152:155], v236 offset:0
s_add_u32 m0, s83, 3200
v_mfma_f32_16x16x32_bf16 a[84:87], v[52:55], v[20:23], a[84:87]
buffer_load_dwordx4 v235, s[44:47], s80 offen lds
v_mfma_f32_16x16x32_bf16 a[16:19], v[56:59], v[20:23], a[16:19]
ds_read_b128 v[156:159], v236 offset:2048
v_mfma_f32_16x16x32_bf16 a[20:23], v[60:63], v[20:23], a[20:23]
s_add_u32 s80, s80, s81
s_add_u32 s83, s83, 16896
v_mfma_f32_16x16x32_bf16 a[88:91], v[32:35], v[24:27], a[88:91]
v_mfma_f32_16x16x32_bf16 a[92:95], v[36:39], v[24:27], a[92:95]
s_cmp_eq_u32 s83, s97
s_cselect_b32 s83, s90, s83
v_mfma_f32_16x16x32_bf16 a[24:27], v[40:43], v[24:27], a[24:27]
v_mfma_f32_16x16x32_bf16 a[28:31], v[44:47], v[24:27], a[28:31]
buffer_load_dword v254, v167, s[68:71], s96 offen
s_add_u32 s96, s96, 1048576
v_mfma_f32_16x16x32_bf16 a[240:243], v[48:51], v[24:27], a[240:243]
v_mfma_f32_16x16x32_bf16 a[244:247], v[52:55], v[24:27], a[244:247]
v_mfma_f32_16x16x32_bf16 a[32:35], v[56:59], v[24:27], a[32:35]
v_mfma_f32_16x16x32_bf16 a[36:39], v[60:63], v[24:27], a[36:39]
buffer_load_dword v254, v167, s[68:71], s96 offen
s_add_u32 s96, s96, 1048576
v_mfma_f32_16x16x32_bf16 a[248:251], v[32:35], v[28:31], a[248:251]
s_lshl_b32 s92, s77, 18
s_cmp_eq_u32 s85, 3
v_mfma_f32_16x16x32_bf16 a[252:255], v[36:39], v[28:31], a[252:255]
s_cselect_b32 s44, s68, s44
s_cselect_b32 s45, s69, s45
v_mfma_f32_16x16x32_bf16 a[40:43], v[40:43], v[28:31], a[40:43]
s_cselect_b32 s46, s70, s46
s_cselect_b32 s81, 524288, s81
v_mfma_f32_16x16x32_bf16 a[44:47], v[44:47], v[28:31], a[44:47]
s_cselect_b32 s42, 0, s42
s_cselect_b32 s80, s92, s80
v_mfma_f32_16x16x32_bf16 a[48:51], v[48:51], v[28:31], a[48:51]
v_mfma_f32_16x16x32_bf16 a[52:55], v[52:55], v[28:31], a[52:55]
v_mfma_f32_16x16x32_bf16 a[56:59], v[56:59], v[28:31], a[56:59]
v_mfma_f32_16x16x32_bf16 a[60:63], v[60:63], v[28:31], a[60:63]
s_waitcnt lgkmcnt(0)
s_waitcnt vmcnt(22)
s_barrier
v_mfma_f32_16x16x32_bf16 a[224:227], v[192:195], v[128:131], a[224:227]
ds_read_b128 v[0:3], v237 offset:24576
v_mfma_f32_16x16x32_bf16 a[228:231], v[196:199], v[128:131], a[228:231]
s_waitcnt vmcnt(14)
buffer_store_dwordx2 v[252:253], off, s[64:67], s91 offset:1024 sc1
v_mfma_f32_16x16x32_bf16 a[232:235], v[200:203], v[128:131], a[232:235]
ds_read_b64_tr_b16 v[32:33], v240 offset:16896
v_mfma_f32_16x16x32_bf16 a[236:239], v[204:207], v[128:131], a[236:239]
s_cmp_eq_u32 s85, 4
s_cselect_b32 s92, s89, 1073741824
v_mfma_f32_16x16x32_bf16 a[208:211], v[208:211], v[128:131], a[208:211]
ds_read_b64_tr_b16 v[34:35], v240 offset:17920
v_mfma_f32_16x16x32_bf16 a[212:215], v[212:215], v[128:131], a[212:215]
buffer_load_dwordx4 v[72:75], v255, s[64:67], s92 offen offset:1024 sc1
v_mfma_f32_16x16x32_bf16 a[128:131], v[216:219], v[128:131], a[128:131]
ds_read_b64_tr_b16 v[36:37], v241 offset:16896
v_mfma_f32_16x16x32_bf16 a[132:135], v[220:223], v[128:131], a[132:135]
s_mov_b32 s91, 1073741824
v_mfma_f32_16x16x32_bf16 a[216:219], v[192:195], v[132:135], a[216:219]
ds_read_b64_tr_b16 v[38:39], v241 offset:17920
v_mfma_f32_16x16x32_bf16 a[220:223], v[196:199], v[132:135], a[220:223]
v_mfma_f32_16x16x32_bf16 a[136:139], v[200:203], v[132:135], a[136:139]
ds_read_b64_tr_b16 v[40:41], v242 offset:16896
s_lshl_b32 s92, s76, 19
v_mfma_f32_16x16x32_bf16 a[140:143], v[204:207], v[132:135], a[140:143]
s_add_u32 s52, s26, s92
s_addc_u32 s53, s27, 0
v_mfma_f32_16x16x32_bf16 a[192:195], v[208:211], v[132:135], a[192:195]
ds_read_b64_tr_b16 v[42:43], v242 offset:17920
s_and_b32 s53, s53, 0xffff
v_mfma_f32_16x16x32_bf16 a[196:199], v[212:215], v[132:135], a[196:199]
s_mov_b32 s54, 524288
s_mov_b32 s55, 0x20000
v_mfma_f32_16x16x32_bf16 a[144:147], v[216:219], v[132:135], a[144:147]
ds_read_b64_tr_b16 v[44:45], v243 offset:16896
s_lshl_b32 s92, s3, 16
v_mfma_f32_16x16x32_bf16 a[148:151], v[220:223], v[132:135], a[148:151]
s_sub_u32 s93, 458752, s92
v_add_u32 v244, s92, v255
v_mfma_f32_16x16x32_bf16 a[200:203], v[192:195], v[136:139], a[200:203]
ds_read_b64_tr_b16 v[46:47], v243 offset:17920
v_add_u32 v246, s93, v255
v_mfma_f32_16x16x32_bf16 a[204:207], v[196:199], v[136:139], a[204:207]
v_add_u32 v245, 0x8000, v244
v_add_u32 v247, 0x8000, v246
v_mfma_f32_16x16x32_bf16 a[152:155], v[200:203], v[136:139], a[152:155]
ds_read_b64_tr_b16 v[48:49], v240 offset:17024
s_lshl_b32 s94, s72, 19
v_mfma_f32_16x16x32_bf16 a[156:159], v[204:207], v[136:139], a[156:159]
s_lshl_b32 s93, s76, 16
s_add_u32 s92, s94, s93
v_mfma_f32_16x16x32_bf16 a[96:99], v[208:211], v[136:139], a[96:99]
ds_read_b64_tr_b16 v[50:51], v240 offset:18048
s_lshl_b32 s93, s77, 13
v_mfma_f32_16x16x32_bf16 a[100:103], v[212:215], v[136:139], a[100:103]
s_add_u32 s92, s92, s93
s_add_u32 s92, s92, 20971520
v_mfma_f32_16x16x32_bf16 a[160:163], v[216:219], v[136:139], a[160:163]
ds_read_b64_tr_b16 v[52:53], v241 offset:17024
v_mfma_f32_16x16x32_bf16 a[164:167], v[220:223], v[136:139], a[164:167]
s_add_u32 s48, s26, s92
s_addc_u32 s49, s27, 0
v_mfma_f32_16x16x32_bf16 a[104:107], v[192:195], v[140:143], a[104:107]
ds_read_b64_tr_b16 v[54:55], v241 offset:18048
s_and_b32 s49, s49, 0xffff
v_mfma_f32_16x16x32_bf16 a[108:111], v[196:199], v[140:143], a[108:111]
s_mov_b32 s50, 57344
s_mov_b32 s51, 0x20000
v_mfma_f32_16x16x32_bf16 a[168:171], v[200:203], v[140:143], a[168:171]
ds_read_b64_tr_b16 v[56:57], v242 offset:17024
s_lshl_b32 s93, s77, 24
v_mfma_f32_16x16x32_bf16 a[172:175], v[204:207], v[140:143], a[172:175]
s_add_u32 s92, s94, s93
s_add_u32 s92, s92, 20971520
v_mfma_f32_16x16x32_bf16 a[112:115], v[208:211], v[140:143], a[112:115]
ds_read_b64_tr_b16 v[58:59], v242 offset:18048
v_mfma_f32_16x16x32_bf16 a[116:119], v[212:215], v[140:143], a[116:119]
s_add_u32 s60, s26, s92
s_addc_u32 s61, s27, 0
v_mfma_f32_16x16x32_bf16 a[176:179], v[216:219], v[140:143], a[176:179]
ds_read_b64_tr_b16 v[60:61], v243 offset:17024
s_and_b32 s61, s61, 0xffff
v_mfma_f32_16x16x32_bf16 a[180:183], v[220:223], v[140:143], a[180:183]
s_mov_b32 s62, 524288
s_mov_b32 s63, 0x20000
v_mfma_f32_16x16x32_bf16 a[120:123], v[192:195], v[144:147], a[120:123]
ds_read_b64_tr_b16 v[62:63], v243 offset:18048
s_lshl_b32 s92, s77, 14
s_add_u32 m0, s83, 0
v_mfma_f32_16x16x32_bf16 a[124:127], v[196:199], v[144:147], a[124:127]
buffer_load_dwordx4 v232, s[44:47], s80 offen lds
v_mfma_f32_16x16x32_bf16 a[184:187], v[200:203], v[144:147], a[184:187]
ds_read_b128 v[4:7], v237 offset:26624
s_sub_u32 s95, 16384, s92
v_mfma_f32_16x16x32_bf16 a[188:191], v[204:207], v[144:147], a[188:191]
v_add_u32 v238, s95, v255
v_mfma_f32_16x16x32_bf16 a[64:67], v[208:211], v[144:147], a[64:67]
ds_read_b128 v[8:11], v237 offset:16384
s_add_u32 m0, s83, 1024
v_mfma_f32_16x16x32_bf16 a[68:71], v[212:215], v[144:147], a[68:71]
buffer_load_dwordx4 v233, s[44:47], s80 offen lds
v_mfma_f32_16x16x32_bf16 a[0:3], v[216:219], v[144:147], a[0:3]
ds_read_b128 v[12:15], v237 offset:18432
v_mfma_f32_16x16x32_bf16 a[4:7], v[220:223], v[144:147], a[4:7]
v_mfma_f32_16x16x32_bf16 a[72:75], v[192:195], v[148:151], a[72:75]
ds_read_b128 v[16:19], v237 offset:8192
s_add_u32 m0, s83, 2176
v_mfma_f32_16x16x32_bf16 a[76:79], v[196:199], v[148:151], a[76:79]
buffer_load_dwordx4 v234, s[44:47], s80 offen lds
v_mfma_f32_16x16x32_bf16 a[8:11], v[200:203], v[148:151], a[8:11]
ds_read_b128 v[20:23], v237 offset:10240
v_mfma_f32_16x16x32_bf16 a[12:15], v[204:207], v[148:151], a[12:15]
v_mfma_f32_16x16x32_bf16 a[80:83], v[208:211], v[148:151], a[80:83]
ds_read_b128 v[24:27], v237 offset:0
s_add_u32 m0, s83, 3200
v_mfma_f32_16x16x32_bf16 a[84:87], v[212:215], v[148:151], a[84:87]
buffer_load_dwordx4 v235, s[44:47], s80 offen lds
v_mfma_f32_16x16x32_bf16 a[16:19], v[216:219], v[148:151], a[16:19]
ds_read_b128 v[28:31], v237 offset:2048
v_mfma_f32_16x16x32_bf16 a[20:23], v[220:223], v[148:151], a[20:23]
s_add_u32 s80, s80, s81
s_add_u32 s83, s83, 16896
v_mfma_f32_16x16x32_bf16 a[88:91], v[192:195], v[152:155], a[88:91]
v_mfma_f32_16x16x32_bf16 a[92:95], v[196:199], v[152:155], a[92:95]
s_cmp_eq_u32 s83, s97
s_cselect_b32 s83, s90, s83
v_mfma_f32_16x16x32_bf16 a[24:27], v[200:203], v[152:155], a[24:27]
v_mfma_f32_16x16x32_bf16 a[28:31], v[204:207], v[152:155], a[28:31]
v_mfma_f32_16x16x32_bf16 a[240:243], v[208:211], v[152:155], a[240:243]
v_mfma_f32_16x16x32_bf16 a[244:247], v[212:215], v[152:155], a[244:247]
v_mfma_f32_16x16x32_bf16 a[32:35], v[216:219], v[152:155], a[32:35]
v_mfma_f32_16x16x32_bf16 a[36:39], v[220:223], v[152:155], a[36:39]
v_mfma_f32_16x16x32_bf16 a[248:251], v[192:195], v[156:159], a[248:251]
v_mfma_f32_16x16x32_bf16 a[252:255], v[196:199], v[156:159], a[252:255]
v_add_u32 v240, s84, v240
v_add_u32 v241, s84, v241
v_mfma_f32_16x16x32_bf16 a[40:43], v[200:203], v[156:159], a[40:43]
v_add_u32 v242, s84, v242
v_add_u32_e64 v243, s84, v243
v_mfma_f32_16x16x32_bf16 a[44:47], v[204:207], v[156:159], a[44:47]
v_xor_b32 v236, 0x8000, v236
v_xor_b32 v237, 0x8000, v237
v_mfma_f32_16x16x32_bf16 a[48:51], v[208:211], v[156:159], a[48:51]
s_sub_u32 s84, 0, s84
s_add_u32 s85, s85, 1
v_mfma_f32_16x16x32_bf16 a[52:55], v[212:215], v[156:159], a[52:55]
s_cmp_lg_u32 s85, 5
v_mfma_f32_16x16x32_bf16 a[56:59], v[216:219], v[156:159], a[56:59]
v_mfma_f32_16x16x32_bf16 a[60:63], v[220:223], v[156:159], a[60:63]
s_waitcnt lgkmcnt(0)
s_cbranch_scc1 .La5_LK_LOOP
s_waitcnt vmcnt(12)
s_barrier
v_mfma_f32_16x16x32_bf16 a[224:227], v[32:35], v[0:3], a[224:227]
v_mfma_f32_16x16x32_bf16 a[228:231], v[36:39], v[0:3], a[228:231]
s_waitcnt vmcnt(4)
v_cmp_eq_u64 vcc, v[72:73], v[252:253]
v_mfma_f32_16x16x32_bf16 a[232:235], v[40:43], v[0:3], a[232:235]
v_cmp_eq_u64 s[96:97], v[74:75], v[252:253]
s_mov_b32 s91, 0
v_mfma_f32_16x16x32_bf16 a[236:239], v[44:47], v[0:3], a[236:239]
s_and_b64 vcc, vcc, s[96:97]
s_cmp_eq_u64 vcc, -1
s_cbranch_scc0 .La5_LK_XSPIN
.La5_LK_XOK:
v_mfma_f32_16x16x32_bf16 a[208:211], v[48:51], v[0:3], a[208:211]
s_mov_b32 s89, 49152
v_mfma_f32_16x16x32_bf16 a[212:215], v[52:55], v[0:3], a[212:215]
buffer_load_dwordx4 v[128:131], v244, s[52:55], s91 offen offset:0 sc1
v_mfma_f32_16x16x32_bf16 a[128:131], v[56:59], v[0:3], a[128:131]
v_mfma_f32_16x16x32_bf16 a[132:135], v[60:63], v[0:3], a[132:135]
v_mfma_f32_16x16x32_bf16 a[216:219], v[32:35], v[4:7], a[216:219]
ds_read_b64_tr_b16 v[192:193], v240 offset:0
v_mfma_f32_16x16x32_bf16 a[220:223], v[36:39], v[4:7], a[220:223]
buffer_load_dwordx4 v[132:135], v245, s[52:55], s91 offen offset:0 sc1
v_mfma_f32_16x16x32_bf16 a[136:139], v[40:43], v[4:7], a[136:139]
ds_read_b64_tr_b16 v[194:195], v240 offset:1024
v_mfma_f32_16x16x32_bf16 a[140:143], v[44:47], v[4:7], a[140:143]
v_mfma_f32_16x16x32_bf16 a[192:195], v[48:51], v[4:7], a[192:195]
ds_read_b64_tr_b16 v[196:197], v241 offset:0
v_mfma_f32_16x16x32_bf16 a[196:199], v[52:55], v[4:7], a[196:199]
buffer_load_dwordx4 v[136:139], v246, s[52:55], s91 offen offset:0 sc1
v_mfma_f32_16x16x32_bf16 a[144:147], v[56:59], v[4:7], a[144:147]
ds_read_b64_tr_b16 v[198:199], v241 offset:1024
v_accvgpr_read_b32 v224, a224
v_mfma_f32_16x16x32_bf16 a[148:151], v[60:63], v[4:7], a[148:151]
v_accvgpr_read_b32 v225, a225
v_accvgpr_read_b32 v226, a226
v_mfma_f32_16x16x32_bf16 a[200:203], v[32:35], v[8:11], a[200:203]
ds_read_b64_tr_b16 v[200:201], v242 offset:0
v_accvgpr_read_b32 v227, a227
v_mfma_f32_16x16x32_bf16 a[204:207], v[36:39], v[8:11], a[204:207]
buffer_load_dwordx4 v[140:143], v247, s[52:55], s91 offen offset:0 sc1
v_accvgpr_read_b32 v228, a228
v_mfma_f32_16x16x32_bf16 a[152:155], v[40:43], v[8:11], a[152:155]
ds_read_b64_tr_b16 v[202:203], v242 offset:1024
v_accvgpr_read_b32 v229, a229
v_mfma_f32_16x16x32_bf16 a[156:159], v[44:47], v[8:11], a[156:159]
v_accvgpr_read_b32 v230, a230
v_accvgpr_read_b32 v231, a231
v_mfma_f32_16x16x32_bf16 a[96:99], v[48:51], v[8:11], a[96:99]
ds_read_b64_tr_b16 v[204:205], v243 offset:0
v_cvt_pk_bf16_f32 v248, v224, v225
v_mfma_f32_16x16x32_bf16 a[100:103], v[52:55], v[8:11], a[100:103]
buffer_load_dwordx4 v[144:147], v244, s[52:55], s91 offen offset:1024 sc1
v_cvt_pk_bf16_f32 v249, v226, v227
v_mfma_f32_16x16x32_bf16 a[160:163], v[56:59], v[8:11], a[160:163]
ds_read_b64_tr_b16 v[206:207], v243 offset:1024
v_cvt_pk_bf16_f32 v250, v228, v229
v_mfma_f32_16x16x32_bf16 a[164:167], v[60:63], v[8:11], a[164:167]
v_cvt_pk_bf16_f32 v251, v230, v231
buffer_store_dwordx4 v[248:251], v255, s[48:51], s89 offen sc1
v_mfma_f32_16x16x32_bf16 a[104:107], v[32:35], v[12:15], a[104:107]
ds_read_b64_tr_b16 v[208:209], v240 offset:128
s_add_u32 s89, s89, 1024
v_mfma_f32_16x16x32_bf16 a[108:111], v[36:39], v[12:15], a[108:111]
buffer_load_dwordx4 v[148:151], v245, s[52:55], s91 offen offset:1024 sc1
ds_write_b128 v238, v[248:251] offset:0
v_mfma_f32_16x16x32_bf16 a[168:171], v[40:43], v[12:15], a[168:171]
ds_read_b64_tr_b16 v[210:211], v240 offset:1152
v_mfma_f32_16x16x32_bf16 a[172:175], v[44:47], v[12:15], a[172:175]
v_mfma_f32_16x16x32_bf16 a[112:115], v[48:51], v[12:15], a[112:115]
ds_read_b64_tr_b16 v[212:213], v241 offset:128
v_mfma_f32_16x16x32_bf16 a[116:119], v[52:55], v[12:15], a[116:119]
buffer_load_dwordx4 v[152:155], v246, s[52:55], s91 offen offset:1024 sc1
v_mfma_f32_16x16x32_bf16 a[176:179], v[56:59], v[12:15], a[176:179]
ds_read_b64_tr_b16 v[214:215], v241 offset:1152
v_accvgpr_read_b32 v224, a232
v_mfma_f32_16x16x32_bf16 a[180:183], v[60:63], v[12:15], a[180:183]
v_accvgpr_read_b32 v225, a233
v_accvgpr_read_b32 v226, a234
v_mfma_f32_16x16x32_bf16 a[120:123], v[32:35], v[16:19], a[120:123]
ds_read_b64_tr_b16 v[216:217], v242 offset:128
v_accvgpr_read_b32 v227, a235
s_add_u32 m0, s83, 0
v_mfma_f32_16x16x32_bf16 a[124:127], v[36:39], v[16:19], a[124:127]
buffer_load_dwordx4 v232, s[44:47], s80 offen lds
v_mfma_f32_16x16x32_bf16 a[184:187], v[40:43], v[16:19], a[184:187]
ds_read_b64_tr_b16 v[218:219], v242 offset:1152
v_accvgpr_read_b32 v228, a236
v_mfma_f32_16x16x32_bf16 a[188:191], v[44:47], v[16:19], a[188:191]
buffer_load_dwordx4 v[156:159], v247, s[52:55], s91 offen offset:1024 sc1
v_accvgpr_read_b32 v229, a237
v_mfma_f32_16x16x32_bf16 a[64:67], v[48:51], v[16:19], a[64:67]
ds_read_b64_tr_b16 v[220:221], v243 offset:128
v_accvgpr_read_b32 v230, a238
s_add_u32 m0, s83, 1024
v_mfma_f32_16x16x32_bf16 a[68:71], v[52:55], v[16:19], a[68:71]
buffer_load_dwordx4 v233, s[44:47], s80 offen lds
v_mfma_f32_16x16x32_bf16 a[0:3], v[56:59], v[16:19], a[0:3]
ds_read_b64_tr_b16 v[222:223], v243 offset:1152
v_accvgpr_read_b32 v231, a239
v_mfma_f32_16x16x32_bf16 a[4:7], v[60:63], v[16:19], a[4:7]
v_cvt_pk_bf16_f32 v248, v224, v225
v_cvt_pk_bf16_f32 v249, v226, v227
v_mfma_f32_16x16x32_bf16 a[72:75], v[32:35], v[20:23], a[72:75]
v_cvt_pk_bf16_f32 v250, v228, v229
v_cvt_pk_bf16_f32 v251, v230, v231
s_add_u32 m0, s83, 2176
v_mfma_f32_16x16x32_bf16 a[76:79], v[36:39], v[20:23], a[76:79]
buffer_load_dwordx4 v234, s[44:47], s80 offen lds
v_mfma_f32_16x16x32_bf16 a[8:11], v[40:43], v[20:23], a[8:11]
buffer_store_dwordx4 v[248:251], v255, s[48:51], s89 offen sc1
s_add_u32 s89, s89, 1024
v_mfma_f32_16x16x32_bf16 a[12:15], v[44:47], v[20:23], a[12:15]
buffer_load_dwordx4 v[160:163], v244, s[52:55], s91 offen offset:2048 sc1
ds_write_b128 v238, v[248:251] offset:1024
v_mfma_f32_16x16x32_bf16 a[80:83], v[48:51], v[20:23], a[80:83]
s_add_u32 m0, s83, 3200
v_mfma_f32_16x16x32_bf16 a[84:87], v[52:55], v[20:23], a[84:87]
buffer_load_dwordx4 v235, s[44:47], s80 offen lds
v_mfma_f32_16x16x32_bf16 a[16:19], v[56:59], v[20:23], a[16:19]
v_accvgpr_read_b32 v224, a208
v_accvgpr_read_b32 v225, a209
v_mfma_f32_16x16x32_bf16 a[20:23], v[60:63], v[20:23], a[20:23]
s_add_u32 s80, s80, s81
s_add_u32 s83, s83, 16896
v_mfma_f32_16x16x32_bf16 a[88:91], v[32:35], v[24:27], a[88:91]
v_accvgpr_read_b32 v226, a210
v_accvgpr_read_b32 v227, a211
v_mfma_f32_16x16x32_bf16 a[92:95], v[36:39], v[24:27], a[92:95]
s_mov_b32 s83, s90
v_accvgpr_read_b32 v228, a212
v_mfma_f32_16x16x32_bf16 a[24:27], v[40:43], v[24:27], a[24:27]
v_accvgpr_read_b32 v229, a213
v_accvgpr_read_b32 v230, a214
v_mfma_f32_16x16x32_bf16 a[28:31], v[44:47], v[24:27], a[28:31]
buffer_load_dwordx4 v[164:167], v245, s[52:55], s91 offen offset:2048 sc1
v_accvgpr_read_b32 v231, a215
v_mfma_f32_16x16x32_bf16 a[240:243], v[48:51], v[24:27], a[240:243]
v_cvt_pk_bf16_f32 v248, v224, v225
v_cvt_pk_bf16_f32 v249, v226, v227
v_mfma_f32_16x16x32_bf16 a[244:247], v[52:55], v[24:27], a[244:247]
v_cvt_pk_bf16_f32 v250, v228, v229
v_cvt_pk_bf16_f32 v251, v230, v231
v_mfma_f32_16x16x32_bf16 a[32:35], v[56:59], v[24:27], a[32:35]
buffer_store_dwordx4 v[248:251], v255, s[48:51], s89 offen sc1
s_add_u32 s89, s89, 1024
v_mfma_f32_16x16x32_bf16 a[36:39], v[60:63], v[24:27], a[36:39]
buffer_load_dwordx4 v[168:171], v246, s[52:55], s91 offen offset:2048 sc1
ds_write_b128 v238, v[248:251] offset:2048
v_mfma_f32_16x16x32_bf16 a[248:251], v[32:35], v[28:31], a[248:251]
v_mfma_f32_16x16x32_bf16 a[252:255], v[36:39], v[28:31], a[252:255]
v_mfma_f32_16x16x32_bf16 a[40:43], v[40:43], v[28:31], a[40:43]
v_mfma_f32_16x16x32_bf16 a[44:47], v[44:47], v[28:31], a[44:47]
buffer_load_dwordx4 v[172:175], v247, s[52:55], s91 offen offset:2048 sc1
v_mfma_f32_16x16x32_bf16 a[48:51], v[48:51], v[28:31], a[48:51]
v_mfma_f32_16x16x32_bf16 a[52:55], v[52:55], v[28:31], a[52:55]
v_mfma_f32_16x16x32_bf16 a[56:59], v[56:59], v[28:31], a[56:59]
v_mfma_f32_16x16x32_bf16 a[60:63], v[60:63], v[28:31], a[60:63]
s_branch .La5_LQ_START
; ---- V tile (waves 1, 3): the same with the operands exchanged (V comes out transposed)
.La5_LV_START:
.La5_LW1_2:
s_cbranch_execz .La5_LW1_6
s_waitcnt vmcnt(1)
s_barrier
v_mfma_f32_16x16x32_bf16 a[224:227], v[128:131], v[192:195], 0
ds_read_b128 v[0:3], v237 offset:24576
s_add_u32 m0, s83, 33792
v_mfma_f32_16x16x32_bf16 a[232:235], v[128:131], v[196:199], 0
buffer_load_dwordx4 v232, s[44:47], s80 offen lds
v_mfma_f32_16x16x32_bf16 a[208:211], v[128:131], v[200:203], 0
ds_read_b64_tr_b16 v[32:33], v240 offset:16896
v_mfma_f32_16x16x32_bf16 a[128:131], v[128:131], v[204:207], 0
v_mfma_f32_16x16x32_bf16 a[216:219], v[128:131], v[208:211], 0
ds_read_b64_tr_b16 v[34:35], v240 offset:17920
s_add_u32 m0, s83, 34816
v_mfma_f32_16x16x32_bf16 a[136:139], v[128:131], v[212:215], 0
buffer_load_dwordx4 v233, s[44:47], s80 offen lds
v_mfma_f32_16x16x32_bf16 a[192:195], v[128:131], v[216:219], 0
ds_read_b64_tr_b16 v[36:37], v241 offset:16896
v_mfma_f32_16x16x32_bf16 a[144:147], v[128:131], v[220:223], 0
v_mfma_f32_16x16x32_bf16 a[228:231], v[132:135], v[192:195], 0
ds_read_b64_tr_b16 v[38:39], v241 offset:17920
s_add_u32 m0, s83, 35968
v_mfma_f32_16x16x32_bf16 a[236:239], v[132:135], v[196:199], 0
buffer_load_dwordx4 v234, s[44:47], s80 offen lds
v_mfma_f32_16x16x32_bf16 a[212:215], v[132:135], v[200:203], 0
ds_read_b64_tr_b16 v[40:41], v242 offset:16896
v_mfma_f32_16x16x32_bf16 a[132:135], v[132:135], v[204:207], 0
v_mfma_f32_16x16x32_bf16 a[220:223], v[132:135], v[208:211], 0
ds_read_b64_tr_b16 v[42:43], v242 offset:17920
s_add_u32 m0, s83, 36992
v_mfma_f32_16x16x32_bf16 a[140:143], v[132:135], v[212:215], 0
buffer_load_dwordx4 v235, s[44:47], s80 offen lds
v_mfma_f32_16x16x32_bf16 a[196:199], v[132:135], v[216:219], 0
ds_read_b64_tr_b16 v[44:45], v243 offset:16896
v_mfma_f32_16x16x32_bf16 a[148:151], v[132:135], v[220:223], 0
s_add_u32 s80, s80, s81
v_mfma_f32_16x16x32_bf16 a[200:203], v[136:139], v[192:195], 0
ds_read_b64_tr_b16 v[46:47], v243 offset:17920
s_xor_b32 m0, s82, 32768
v_mfma_f32_16x16x32_bf16 a[152:155], v[136:139], v[196:199], 0
buffer_load_dwordx4 v224, s[40:43], s79 offen lds
v_mfma_f32_16x16x32_bf16 a[96:99], v[136:139], v[200:203], 0
ds_read_b64_tr_b16 v[48:49], v240 offset:17024
v_mfma_f32_16x16x32_bf16 a[160:163], v[136:139], v[204:207], 0
v_mfma_f32_16x16x32_bf16 a[104:107], v[136:139], v[208:211], 0
ds_read_b64_tr_b16 v[50:51], v240 offset:18048
s_xor_b32 m0, s82, 33792
v_mfma_f32_16x16x32_bf16 a[168:171], v[136:139], v[212:215], 0
buffer_load_dwordx4 v225, s[40:43], s79 offen lds
v_mfma_f32_16x16x32_bf16 a[112:115], v[136:139], v[216:219], 0
ds_read_b64_tr_b16 v[52:53], v241 offset:17024
v_mfma_f32_16x16x32_bf16 a[176:179], v[136:139], v[220:223], 0
v_mfma_f32_16x16x32_bf16 a[204:207], v[140:143], v[192:195], 0
ds_read_b64_tr_b16 v[54:55], v241 offset:18048
s_xor_b32 m0, s82, 34816
v_mfma_f32_16x16x32_bf16 a[156:159], v[140:143], v[196:199], 0
buffer_load_dwordx4 v226, s[40:43], s79 offen lds
v_mfma_f32_16x16x32_bf16 a[100:103], v[140:143], v[200:203], 0
ds_read_b64_tr_b16 v[56:57], v242 offset:17024
v_mfma_f32_16x16x32_bf16 a[164:167], v[140:143], v[204:207], 0
v_mfma_f32_16x16x32_bf16 a[108:111], v[140:143], v[208:211], 0
ds_read_b64_tr_b16 v[58:59], v242 offset:18048
s_xor_b32 m0, s82, 35840
v_mfma_f32_16x16x32_bf16 a[172:175], v[140:143], v[212:215], 0
buffer_load_dwordx4 v227, s[40:43], s79 offen lds
v_mfma_f32_16x16x32_bf16 a[116:119], v[140:143], v[216:219], 0
ds_read_b64_tr_b16 v[60:61], v243 offset:17024
v_mfma_f32_16x16x32_bf16 a[180:183], v[140:143], v[220:223], 0
v_mfma_f32_16x16x32_bf16 a[120:123], v[144:147], v[192:195], 0
ds_read_b64_tr_b16 v[62:63], v243 offset:18048
s_xor_b32 m0, s82, 36864
v_mfma_f32_16x16x32_bf16 a[184:187], v[144:147], v[196:199], 0
buffer_load_dwordx4 v228, s[40:43], s79 offen lds
v_mfma_f32_16x16x32_bf16 a[64:67], v[144:147], v[200:203], 0
ds_read_b128 v[4:7], v237 offset:26624
v_mfma_f32_16x16x32_bf16 a[0:3], v[144:147], v[204:207], 0
v_mfma_f32_16x16x32_bf16 a[72:75], v[144:147], v[208:211], 0
ds_read_b128 v[8:11], v237 offset:16384
s_xor_b32 m0, s82, 37888
v_mfma_f32_16x16x32_bf16 a[8:11], v[144:147], v[212:215], 0
buffer_load_dwordx4 v229, s[40:43], s79 offen lds
v_mfma_f32_16x16x32_bf16 a[80:83], v[144:147], v[216:219], 0
ds_read_b128 v[12:15], v237 offset:18432
v_mfma_f32_16x16x32_bf16 a[16:19], v[144:147], v[220:223], 0
v_mfma_f32_16x16x32_bf16 a[124:127], v[148:151], v[192:195], 0
ds_read_b128 v[16:19], v237 offset:8192
s_xor_b32 m0, s82, 38912
v_mfma_f32_16x16x32_bf16 a[188:191], v[148:151], v[196:199], 0
buffer_load_dwordx4 v230, s[40:43], s79 offen lds
v_mfma_f32_16x16x32_bf16 a[68:71], v[148:151], v[200:203], 0
ds_read_b128 v[20:23], v237 offset:10240
v_mfma_f32_16x16x32_bf16 a[4:7], v[148:151], v[204:207], 0
v_mfma_f32_16x16x32_bf16 a[76:79], v[148:151], v[208:211], 0
ds_read_b128 v[24:27], v237 offset:0
s_xor_b32 m0, s82, 39936
v_mfma_f32_16x16x32_bf16 a[12:15], v[148:151], v[212:215], 0
buffer_load_dwordx4 v231, s[40:43], s79 offen lds
v_mfma_f32_16x16x32_bf16 a[84:87], v[148:151], v[216:219], 0
ds_read_b128 v[28:31], v237 offset:2048
v_mfma_f32_16x16x32_bf16 a[20:23], v[148:151], v[220:223], 0
s_add_u32 s79, s79, 128
v_mfma_f32_16x16x32_bf16 a[88:91], v[152:155], v[192:195], 0
v_mfma_f32_16x16x32_bf16 a[24:27], v[152:155], v[196:199], 0
buffer_load_dword v70, v118, s[60:63], 0 offen
v_mfma_f32_16x16x32_bf16 a[240:243], v[152:155], v[200:203], 0
v_mfma_f32_16x16x32_bf16 a[32:35], v[152:155], v[204:207], 0
v_mfma_f32_16x16x32_bf16 a[248:251], v[152:155], v[208:211], 0
buffer_load_dwordx4 v[72:75], v119, s[60:63], 0 offen offset:0
v_mfma_f32_16x16x32_bf16 a[40:43], v[152:155], v[212:215], 0
v_mfma_f32_16x16x32_bf16 a[48:51], v[152:155], v[216:219], 0
v_mfma_f32_16x16x32_bf16 a[56:59], v[152:155], v[220:223], 0
buffer_load_dwordx4 v[76:79], v119, s[60:63], 0 offen offset:128
v_mfma_f32_16x16x32_bf16 a[92:95], v[156:159], v[192:195], 0
v_mfma_f32_16x16x32_bf16 a[28:31], v[156:159], v[196:199], 0
v_mfma_f32_16x16x32_bf16 a[244:247], v[156:159], v[200:203], 0
buffer_load_dwordx4 v[80:83], v119, s[60:63], 0 offen offset:256
v_mfma_f32_16x16x32_bf16 a[36:39], v[156:159], v[204:207], 0
v_mfma_f32_16x16x32_bf16 a[252:255], v[156:159], v[208:211], 0
v_mfma_f32_16x16x32_bf16 a[44:47], v[156:159], v[212:215], 0
buffer_load_dwordx4 v[84:87], v119, s[60:63], 0 offen offset:384
v_mfma_f32_16x16x32_bf16 a[52:55], v[156:159], v[216:219], 0
v_mfma_f32_16x16x32_bf16 a[60:63], v[156:159], v[220:223], 0
s_mov_b64 exec, 0
s_cmp_eq_u32 s3, 1
s_cbranch_scc1 .La5_LW2_1
s_branch .La5_LW2_3
.La5_LW2_R1:
s_mov_b64 exec, -1
s_waitcnt lgkmcnt(0)
s_waitcnt vmcnt(5)
s_barrier
v_mfma_f32_16x16x32_bf16 a[224:227], v[0:3], v[32:35], a[224:227]
ds_read_b128 v[128:131], v236 offset:57344
s_add_u32 m0, s83, 50688
v_mfma_f32_16x16x32_bf16 a[232:235], v[0:3], v[36:39], a[232:235]
buffer_load_dwordx4 v232, s[44:47], s80 offen lds
v_mfma_f32_16x16x32_bf16 a[208:211], v[0:3], v[40:43], a[208:211]
ds_read_b64_tr_b16 v[192:193], v240 offset:33792
v_mfma_f32_16x16x32_bf16 a[128:131], v[0:3], v[44:47], a[128:131]
v_mfma_f32_16x16x32_bf16 a[216:219], v[0:3], v[48:51], a[216:219]
ds_read_b64_tr_b16 v[194:195], v240 offset:34816
s_add_u32 m0, s83, 51712
v_mfma_f32_16x16x32_bf16 a[136:139], v[0:3], v[52:55], a[136:139]
buffer_load_dwordx4 v233, s[44:47], s80 offen lds
v_mfma_f32_16x16x32_bf16 a[192:195], v[0:3], v[56:59], a[192:195]
ds_read_b64_tr_b16 v[196:197], v241 offset:33792
v_mfma_f32_16x16x32_bf16 a[144:147], v[0:3], v[60:63], a[144:147]
v_mfma_f32_16x16x32_bf16 a[228:231], v[4:7], v[32:35], a[228:231]
ds_read_b64_tr_b16 v[198:199], v241 offset:34816
s_add_u32 m0, s83, 52864
v_mfma_f32_16x16x32_bf16 a[236:239], v[4:7], v[36:39], a[236:239]
buffer_load_dwordx4 v234, s[44:47], s80 offen lds
v_mfma_f32_16x16x32_bf16 a[212:215], v[4:7], v[40:43], a[212:215]
ds_read_b64_tr_b16 v[200:201], v242 offset:33792
v_mfma_f32_16x16x32_bf16 a[132:135], v[4:7], v[44:47], a[132:135]
v_mfma_f32_16x16x32_bf16 a[220:223], v[4:7], v[48:51], a[220:223]
ds_read_b64_tr_b16 v[202:203], v242 offset:34816
s_add_u32 m0, s83, 53888
v_mfma_f32_16x16x32_bf16 a[140:143], v[4:7], v[52:55], a[140:143]
buffer_load_dwordx4 v235, s[44:47], s80 offen lds
v_mfma_f32_16x16x32_bf16 a[196:199], v[4:7], v[56:59], a[196:199]
ds_read_b64_tr_b16 v[204:205], v243 offset:33792
v_mfma_f32_16x16x32_bf16 a[148:151], v[4:7], v[60:63], a[148:151]
s_add_u32 s80, s80, s81
v_mfma_f32_16x16x32_bf16 a[200:203], v[8:11], v[32:35], a[200:203]
ds_read_b64_tr_b16 v[206:207], v243 offset:34816
s_xor_b32 m0, s82, 0
v_mfma_f32_16x16x32_bf16 a[152:155], v[8:11], v[36:39], a[152:155]
buffer_load_dwordx4 v224, s[40:43], s79 offen lds
v_mfma_f32_16x16x32_bf16 a[96:99], v[8:11], v[40:43], a[96:99]
ds_read_b64_tr_b16 v[208:209], v240 offset:33920
v_mfma_f32_16x16x32_bf16 a[160:163], v[8:11], v[44:47], a[160:163]
v_mfma_f32_16x16x32_bf16 a[104:107], v[8:11], v[48:51], a[104:107]
ds_read_b64_tr_b16 v[210:211], v240 offset:34944
s_xor_b32 m0, s82, 1024
v_mfma_f32_16x16x32_bf16 a[168:171], v[8:11], v[52:55], a[168:171]
buffer_load_dwordx4 v225, s[40:43], s79 offen lds
v_mfma_f32_16x16x32_bf16 a[112:115], v[8:11], v[56:59], a[112:115]
ds_read_b64_tr_b16 v[212:213], v241 offset:33920
v_mfma_f32_16x16x32_bf16 a[176:179], v[8:11], v[60:63], a[176:179]
v_mfma_f32_16x16x32_bf16 a[204:207], v[12:15], v[32:35], a[204:207]
ds_read_b64_tr_b16 v[214:215], v241 offset:34944
s_xor_b32 m0, s82, 2048
v_mfma_f32_16x16x32_bf16 a[156:159], v[12:15], v[36:39], a[156:159]
buffer_load_dwordx4 v226, s[40:43], s79 offen lds
v_mfma_f32_16x16x32_bf16 a[100:103], v[12:15], v[40:43], a[100:103]
ds_read_b64_tr_b16 v[216:217], v242 offset:33920
.La5_LW1_3:
s_cbranch_execz .La5_LW1_7
v_mfma_f32_16x16x32_bf16 a[164:167], v[12:15], v[44:47], a[164:167]
v_mfma_f32_16x16x32_bf16 a[108:111], v[12:15], v[48:51], a[108:111]
ds_read_b64_tr_b16 v[218:219], v242 offset:34944
s_xor_b32 m0, s82, 3072
v_mfma_f32_16x16x32_bf16 a[172:175], v[12:15], v[52:55], a[172:175]
buffer_load_dwordx4 v227, s[40:43], s79 offen lds
v_mfma_f32_16x16x32_bf16 a[116:119], v[12:15], v[56:59], a[116:119]
ds_read_b64_tr_b16 v[220:221], v243 offset:33920
v_mfma_f32_16x16x32_bf16 a[180:183], v[12:15], v[60:63], a[180:183]
v_mfma_f32_16x16x32_bf16 a[120:123], v[16:19], v[32:35], a[120:123]
ds_read_b64_tr_b16 v[222:223], v243 offset:34944
s_xor_b32 m0, s82, 4096
v_mfma_f32_16x16x32_bf16 a[184:187], v[16:19], v[36:39], a[184:187]
buffer_load_dwordx4 v228, s[40:43], s79 offen lds
v_mfma_f32_16x16x32_bf16 a[64:67], v[16:19], v[40:43], a[64:67]
ds_read_b128 v[132:135], v236 offset:59392
v_mfma_f32_16x16x32_bf16 a[0:3], v[16:19], v[44:47], a[0:3]
v_mfma_f32_16x16x32_bf16 a[72:75], v[16:19], v[48:51], a[72:75]
ds_read_b128 v[136:139], v236 offset:49152
s_xor_b32 m0, s82, 5120
v_mfma_f32_16x16x32_bf16 a[8:11], v[16:19], v[52:55], a[8:11]
buffer_load_dwordx4 v229, s[40:43], s79 offen lds
v_mfma_f32_16x16x32_bf16 a[80:83], v[16:19], v[56:59], a[80:83]
ds_read_b128 v[140:143], v236 offset:51200
v_mfma_f32_16x16x32_bf16 a[16:19], v[16:19], v[60:63], a[16:19]
v_mfma_f32_16x16x32_bf16 a[124:127], v[20:23], v[32:35], a[124:127]
ds_read_b128 v[144:147], v236 offset:40960
s_xor_b32 m0, s82, 6144
v_mfma_f32_16x16x32_bf16 a[188:191], v[20:23], v[36:39], a[188:191]
buffer_load_dwordx4 v230, s[40:43], s79 offen lds
v_mfma_f32_16x16x32_bf16 a[68:71], v[20:23], v[40:43], a[68:71]
ds_read_b128 v[148:151], v236 offset:43008
v_mfma_f32_16x16x32_bf16 a[4:7], v[20:23], v[44:47], a[4:7]
v_mfma_f32_16x16x32_bf16 a[76:79], v[20:23], v[48:51], a[76:79]
ds_read_b128 v[152:155], v236 offset:32768
s_xor_b32 m0, s82, 7168
v_mfma_f32_16x16x32_bf16 a[12:15], v[20:23], v[52:55], a[12:15]
buffer_load_dwordx4 v231, s[40:43], s79 offen lds
v_mfma_f32_16x16x32_bf16 a[84:87], v[20:23], v[56:59], a[84:87]
ds_read_b128 v[156:159], v236 offset:34816
v_mfma_f32_16x16x32_bf16 a[20:23], v[20:23], v[60:63], a[20:23]
s_add_u32 s79, s79, 128
v_mfma_f32_16x16x32_bf16 a[88:91], v[24:27], v[32:35], a[88:91]
s_add_u32 m0, s83, 0
v_mfma_f32_16x16x32_bf16 a[24:27], v[24:27], v[36:39], a[24:27]
buffer_load_dwordx4 v232, s[44:47], s80 offen lds
v_mfma_f32_16x16x32_bf16 a[240:243], v[24:27], v[40:43], a[240:243]
v_mfma_f32_16x16x32_bf16 a[32:35], v[24:27], v[44:47], a[32:35]
v_mfma_f32_16x16x32_bf16 a[248:251], v[24:27], v[48:51], a[248:251]
s_add_u32 m0, s83, 1024
v_mfma_f32_16x16x32_bf16 a[40:43], v[24:27], v[52:55], a[40:43]
buffer_load_dwordx4 v233, s[44:47], s80 offen lds
v_mfma_f32_16x16x32_bf16 a[48:51], v[24:27], v[56:59], a[48:51]
v_mfma_f32_16x16x32_bf16 a[56:59], v[24:27], v[60:63], a[56:59]
v_mfma_f32_16x16x32_bf16 a[92:95], v[28:31], v[32:35], a[92:95]
s_add_u32 m0, s83, 2176
v_mfma_f32_16x16x32_bf16 a[28:31], v[28:31], v[36:39], a[28:31]
buffer_load_dwordx4 v234, s[44:47], s80 offen lds
v_mfma_f32_16x16x32_bf16 a[244:247], v[28:31], v[40:43], a[244:247]
v_mfma_f32_16x16x32_bf16 a[36:39], v[28:31], v[44:47], a[36:39]
v_mfma_f32_16x16x32_bf16 a[252:255], v[28:31], v[48:51], a[252:255]
s_add_u32 m0, s83, 3200
v_mfma_f32_16x16x32_bf16 a[44:47], v[28:31], v[52:55], a[44:47]
buffer_load_dwordx4 v235, s[44:47], s80 offen lds
v_mfma_f32_16x16x32_bf16 a[52:55], v[28:31], v[56:59], a[52:55]
v_mfma_f32_16x16x32_bf16 a[60:63], v[28:31], v[60:63], a[60:63]
s_add_u32 s80, s80, s81
s_waitcnt lgkmcnt(0)
s_waitcnt vmcnt(12)
s_barrier
v_mfma_f32_16x16x32_bf16 a[224:227], v[128:131], v[192:195], a[224:227]
ds_read_b128 v[0:3], v237 offset:57344
s_add_u32 m0, s83, 16896
v_mfma_f32_16x16x32_bf16 a[232:235], v[128:131], v[196:199], a[232:235]
buffer_load_dwordx4 v232, s[44:47], s80 offen lds
v_mfma_f32_16x16x32_bf16 a[208:211], v[128:131], v[200:203], a[208:211]
ds_read_b64_tr_b16 v[32:33], v240 offset:50688
s_nop 0
v_mfma_f32_16x16x32_bf16 a[128:131], v[128:131], v[204:207], a[128:131]
v_lshlrev_b32 v70, 16, v70
v_lshlrev_b32 v88, 16, v72
v_mfma_f32_16x16x32_bf16 a[216:219], v[128:131], v[208:211], a[216:219]
ds_read_b64_tr_b16 v[34:35], v240 offset:51712
v_and_b32 v89, 0xffff0000, v72
s_add_u32 m0, s83, 17920
v_mfma_f32_16x16x32_bf16 a[136:139], v[128:131], v[212:215], a[136:139]
buffer_load_dwordx4 v233, s[44:47], s80 offen lds
v_mfma_f32_16x16x32_bf16 a[192:195], v[128:131], v[216:219], a[192:195]
ds_read_b64_tr_b16 v[36:37], v241 offset:50688
v_sub_f32 v88, v88, v70
v_mfma_f32_16x16x32_bf16 a[144:147], v[128:131], v[220:223], a[144:147]
v_sub_f32 v89, v89, v70
v_add_f32 v120, v88, v89
v_mfma_f32_16x16x32_bf16 a[228:231], v[132:135], v[192:195], a[228:231]
ds_read_b64_tr_b16 v[38:39], v241 offset:51712
v_mul_f32 v122, v88, v88
s_add_u32 m0, s83, 19072
v_mfma_f32_16x16x32_bf16 a[236:239], v[132:135], v[196:199], a[236:239]
buffer_load_dwordx4 v234, s[44:47], s80 offen lds
v_mfma_f32_16x16x32_bf16 a[212:215], v[132:135], v[200:203], a[212:215]
ds_read_b64_tr_b16 v[40:41], v242 offset:50688
v_mul_f32 v123, v89, v89
v_mfma_f32_16x16x32_bf16 a[132:135], v[132:135], v[204:207], a[132:135]
v_lshlrev_b32 v90, 16, v73
v_and_b32 v91, 0xffff0000, v73
v_mfma_f32_16x16x32_bf16 a[220:223], v[132:135], v[208:211], a[220:223]
ds_read_b64_tr_b16 v[42:43], v242 offset:51712
v_sub_f32 v90, v90, v70
s_add_u32 m0, s83, 20096
v_mfma_f32_16x16x32_bf16 a[140:143], v[132:135], v[212:215], a[140:143]
buffer_load_dwordx4 v235, s[44:47], s80 offen lds
v_mfma_f32_16x16x32_bf16 a[196:199], v[132:135], v[216:219], a[196:199]
ds_read_b64_tr_b16 v[44:45], v243 offset:50688
v_sub_f32 v91, v91, v70
v_mfma_f32_16x16x32_bf16 a[148:151], v[132:135], v[220:223], a[148:151]
s_add_u32 s80, s80, s81
v_add_f32 v124, v90, v91
v_mfma_f32_16x16x32_bf16 a[200:203], v[136:139], v[192:195], a[200:203]
ds_read_b64_tr_b16 v[46:47], v243 offset:51712
v_fmac_f32 v122, v90, v90
s_add_u32 m0, s83, 33792
v_mfma_f32_16x16x32_bf16 a[152:155], v[136:139], v[196:199], a[152:155]
buffer_load_dwordx4 v232, s[44:47], s80 offen lds
v_mfma_f32_16x16x32_bf16 a[96:99], v[136:139], v[200:203], a[96:99]
ds_read_b64_tr_b16 v[48:49], v240 offset:50816
v_add_f32 v120, v120, v124
v_mfma_f32_16x16x32_bf16 a[160:163], v[136:139], v[204:207], a[160:163]
v_fmac_f32 v123, v91, v91
v_lshlrev_b32 v92, 16, v74
v_mfma_f32_16x16x32_bf16 a[104:107], v[136:139], v[208:211], a[104:107]
ds_read_b64_tr_b16 v[50:51], v240 offset:51840
v_and_b32 v93, 0xffff0000, v74
s_add_u32 m0, s83, 34816
v_mfma_f32_16x16x32_bf16 a[168:171], v[136:139], v[212:215], a[168:171]
buffer_load_dwordx4 v233, s[44:47], s80 offen lds
v_mfma_f32_16x16x32_bf16 a[112:115], v[136:139], v[216:219], a[112:115]
ds_read_b64_tr_b16 v[52:53], v241 offset:50816
v_sub_f32 v92, v92, v70
v_mfma_f32_16x16x32_bf16 a[176:179], v[136:139], v[220:223], a[176:179]
v_sub_f32 v93, v93, v70
v_add_f32 v124, v92, v93
v_mfma_f32_16x16x32_bf16 a[204:207], v[140:143], v[192:195], a[204:207]
ds_read_b64_tr_b16 v[54:55], v241 offset:51840
v_fmac_f32 v122, v92, v92
s_add_u32 m0, s83, 35968
v_mfma_f32_16x16x32_bf16 a[156:159], v[140:143], v[196:199], a[156:159]
buffer_load_dwordx4 v234, s[44:47], s80 offen lds
v_mfma_f32_16x16x32_bf16 a[100:103], v[140:143], v[200:203], a[100:103]
ds_read_b64_tr_b16 v[56:57], v242 offset:50816
v_add_f32 v120, v120, v124
v_mfma_f32_16x16x32_bf16 a[164:167], v[140:143], v[204:207], a[164:167]
v_fmac_f32 v123, v93, v93
v_lshlrev_b32 v94, 16, v75
v_mfma_f32_16x16x32_bf16 a[108:111], v[140:143], v[208:211], a[108:111]
ds_read_b64_tr_b16 v[58:59], v242 offset:51840
v_and_b32 v95, 0xffff0000, v75
s_add_u32 m0, s83, 36992
v_mfma_f32_16x16x32_bf16 a[172:175], v[140:143], v[212:215], a[172:175]
buffer_load_dwordx4 v235, s[44:47], s80 offen lds
v_mfma_f32_16x16x32_bf16 a[116:119], v[140:143], v[216:219], a[116:119]
ds_read_b64_tr_b16 v[60:61], v243 offset:50816
v_sub_f32 v94, v94, v70
v_mfma_f32_16x16x32_bf16 a[180:183], v[140:143], v[220:223], a[180:183]
s_add_u32 s80, s80, s81
v_sub_f32 v95, v95, v70
v_mfma_f32_16x16x32_bf16 a[120:123], v[144:147], v[192:195], a[120:123]
ds_read_b64_tr_b16 v[62:63], v243 offset:51840
v_add_f32 v124, v94, v95
v_mfma_f32_16x16x32_bf16 a[184:187], v[144:147], v[196:199], a[184:187]
buffer_load_dword v254, v164, s[44:47], s86 offen
v_fmac_f32 v122, v94, v94
v_mfma_f32_16x16x32_bf16 a[64:67], v[144:147], v[200:203], a[64:67]
ds_read_b128 v[4:7], v237 offset:59392
v_add_f32 v120, v120, v124
v_mfma_f32_16x16x32_bf16 a[0:3], v[144:147], v[204:207], a[0:3]
s_xor_b32 s82, s82, 32768
s_add_u32 s83, s83, 50688
v_mfma_f32_16x16x32_bf16 a[72:75], v[144:147], v[208:211], a[72:75]
ds_read_b128 v[8:11], v237 offset:49152
s_add_u32 s86, s86, 2097152
v_mfma_f32_16x16x32_bf16 a[8:11], v[144:147], v[212:215], a[8:11]
v_fmac_f32 v123, v95, v95
v_lshlrev_b32 v96, 16, v76
v_mfma_f32_16x16x32_bf16 a[80:83], v[144:147], v[216:219], a[80:83]
ds_read_b128 v[12:15], v237 offset:51200
v_and_b32 v97, 0xffff0000, v76
v_mfma_f32_16x16x32_bf16 a[16:19], v[144:147], v[220:223], a[16:19]
v_sub_f32 v96, v96, v70
v_sub_f32 v97, v97, v70
v_mfma_f32_16x16x32_bf16 a[124:127], v[148:151], v[192:195], a[124:127]
ds_read_b128 v[16:19], v237 offset:40960
v_add_f32 v124, v96, v97
v_mfma_f32_16x16x32_bf16 a[188:191], v[148:151], v[196:199], a[188:191]
v_fmac_f32 v122, v96, v96
v_add_f32 v120, v120, v124
v_mfma_f32_16x16x32_bf16 a[68:71], v[148:151], v[200:203], a[68:71]
ds_read_b128 v[20:23], v237 offset:43008
v_fmac_f32 v123, v97, v97
v_mfma_f32_16x16x32_bf16 a[4:7], v[148:151], v[204:207], a[4:7]
v_lshlrev_b32 v98, 16, v77
v_and_b32 v99, 0xffff0000, v77
v_mfma_f32_16x16x32_bf16 a[76:79], v[148:151], v[208:211], a[76:79]
ds_read_b128 v[24:27], v237 offset:32768
v_sub_f32 v98, v98, v70
v_mfma_f32_16x16x32_bf16 a[12:15], v[148:151], v[212:215], a[12:15]
v_sub_f32 v99, v99, v70
v_add_f32 v124, v98, v99
v_mfma_f32_16x16x32_bf16 a[84:87], v[148:151], v[216:219], a[84:87]
ds_read_b128 v[28:31], v237 offset:34816
v_fmac_f32 v122, v98, v98
v_mfma_f32_16x16x32_bf16 a[20:23], v[148:151], v[220:223], a[20:23]
v_add_f32 v120, v120, v124
v_fmac_f32 v123, v99, v99
v_mfma_f32_16x16x32_bf16 a[88:91], v[152:155], v[192:195], a[88:91]
v_lshlrev_b32 v100, 16, v78
v_and_b32 v101, 0xffff0000, v78
v_mfma_f32_16x16x32_bf16 a[24:27], v[152:155], v[196:199], a[24:27]
v_sub_f32 v100, v100, v70
v_sub_f32 v101, v101, v70
v_mfma_f32_16x16x32_bf16 a[240:243], v[152:155], v[200:203], a[240:243]
v_add_f32 v124, v100, v101
v_fmac_f32 v122, v100, v100
v_mfma_f32_16x16x32_bf16 a[32:35], v[152:155], v[204:207], a[32:35]
v_add_f32 v120, v120, v124
v_fmac_f32 v123, v101, v101
v_mfma_f32_16x16x32_bf16 a[248:251], v[152:155], v[208:211], a[248:251]
v_lshlrev_b32 v102, 16, v79
v_and_b32 v103, 0xffff0000, v79
v_mfma_f32_16x16x32_bf16 a[40:43], v[152:155], v[212:215], a[40:43]
v_sub_f32 v102, v102, v70
v_sub_f32 v103, v103, v70
v_mfma_f32_16x16x32_bf16 a[48:51], v[152:155], v[216:219], a[48:51]
v_add_f32 v124, v102, v103
v_fmac_f32 v122, v102, v102
v_mfma_f32_16x16x32_bf16 a[56:59], v[152:155], v[220:223], a[56:59]
v_add_f32 v120, v120, v124
v_fmac_f32 v123, v103, v103
v_mfma_f32_16x16x32_bf16 a[92:95], v[156:159], v[192:195], a[92:95]
v_lshlrev_b32 v104, 16, v80
v_and_b32 v105, 0xffff0000, v80
v_mfma_f32_16x16x32_bf16 a[28:31], v[156:159], v[196:199], a[28:31]
v_sub_f32 v104, v104, v70
v_sub_f32 v105, v105, v70
v_mfma_f32_16x16x32_bf16 a[244:247], v[156:159], v[200:203], a[244:247]
v_add_f32 v124, v104, v105
v_fmac_f32 v122, v104, v104
v_mfma_f32_16x16x32_bf16 a[36:39], v[156:159], v[204:207], a[36:39]
v_add_f32 v120, v120, v124
v_fmac_f32 v123, v105, v105
v_mfma_f32_16x16x32_bf16 a[252:255], v[156:159], v[208:211], a[252:255]
v_lshlrev_b32 v106, 16, v81
v_and_b32 v107, 0xffff0000, v81
v_mfma_f32_16x16x32_bf16 a[44:47], v[156:159], v[212:215], a[44:47]
v_sub_f32 v106, v106, v70
v_sub_f32 v107, v107, v70
v_mfma_f32_16x16x32_bf16 a[52:55], v[156:159], v[216:219], a[52:55]
v_add_f32 v124, v106, v107
v_fmac_f32 v122, v106, v106
v_mfma_f32_16x16x32_bf16 a[60:63], v[156:159], v[220:223], a[60:63]
v_add_f32 v120, v120, v124
v_fmac_f32 v123, v107, v107
s_waitcnt lgkmcnt(0)
s_waitcnt vmcnt(9)
s_barrier
v_mfma_f32_16x16x32_bf16 a[224:227], v[0:3], v[32:35], a[224:227]
ds_read_b128 v[128:131], v236 offset:24576
s_xor_b32 m0, s82, 0
v_mfma_f32_16x16x32_bf16 a[232:235], v[0:3], v[36:39], a[232:235]
buffer_load_dwordx4 v224, s[40:43], s79 offen lds
v_mfma_f32_16x16x32_bf16 a[208:211], v[0:3], v[40:43], a[208:211]
ds_read_b64_tr_b16 v[192:193], v240 offset:0
v_lshlrev_b32 v108, 16, v82
v_mfma_f32_16x16x32_bf16 a[128:131], v[0:3], v[44:47], a[128:131]
v_and_b32 v109, 0xffff0000, v82
v_sub_f32 v108, v108, v70
v_mfma_f32_16x16x32_bf16 a[216:219], v[0:3], v[48:51], a[216:219]
ds_read_b64_tr_b16 v[194:195], v240 offset:1024
v_sub_f32 v109, v109, v70
v_mfma_f32_16x16x32_bf16 a[136:139], v[0:3], v[52:55], a[136:139]
v_add_f32 v124, v108, v109
v_fmac_f32 v122, v108, v108
v_mfma_f32_16x16x32_bf16 a[192:195], v[0:3], v[56:59], a[192:195]
ds_read_b64_tr_b16 v[196:197], v241 offset:0
v_add_f32 v120, v120, v124
v_mfma_f32_16x16x32_bf16 a[144:147], v[0:3], v[60:63], a[144:147]
v_fmac_f32 v123, v109, v109
v_lshlrev_b32 v110, 16, v83
v_mfma_f32_16x16x32_bf16 a[228:231], v[4:7], v[32:35], a[228:231]
ds_read_b64_tr_b16 v[198:199], v241 offset:1024
v_and_b32 v111, 0xffff0000, v83
s_xor_b32 m0, s82, 1024
v_mfma_f32_16x16x32_bf16 a[236:239], v[4:7], v[36:39], a[236:239]
buffer_load_dwordx4 v225, s[40:43], s79 offen lds
v_mfma_f32_16x16x32_bf16 a[212:215], v[4:7], v[40:43], a[212:215]
ds_read_b64_tr_b16 v[200:201], v242 offset:0
v_sub_f32 v110, v110, v70
v_mfma_f32_16x16x32_bf16 a[132:135], v[4:7], v[44:47], a[132:135]
v_sub_f32 v111, v111, v70
v_add_f32 v124, v110, v111
v_mfma_f32_16x16x32_bf16 a[220:223], v[4:7], v[48:51], a[220:223]
ds_read_b64_tr_b16 v[202:203], v242 offset:1024
v_fmac_f32 v122, v110, v110
v_mfma_f32_16x16x32_bf16 a[140:143], v[4:7], v[52:55], a[140:143]
v_add_f32 v120, v120, v124
v_fmac_f32 v123, v111, v111
v_mfma_f32_16x16x32_bf16 a[196:199], v[4:7], v[56:59], a[196:199]
ds_read_b64_tr_b16 v[204:205], v243 offset:0
v_lshlrev_b32 v112, 16, v84
v_mfma_f32_16x16x32_bf16 a[148:151], v[4:7], v[60:63], a[148:151]
v_and_b32 v113, 0xffff0000, v84
v_sub_f32 v112, v112, v70
v_mfma_f32_16x16x32_bf16 a[200:203], v[8:11], v[32:35], a[200:203]
ds_read_b64_tr_b16 v[206:207], v243 offset:1024
v_sub_f32 v113, v113, v70
s_xor_b32 m0, s82, 2048
v_mfma_f32_16x16x32_bf16 a[152:155], v[8:11], v[36:39], a[152:155]
buffer_load_dwordx4 v226, s[40:43], s79 offen lds
v_mfma_f32_16x16x32_bf16 a[96:99], v[8:11], v[40:43], a[96:99]
ds_read_b64_tr_b16 v[208:209], v240 offset:128
v_add_f32 v124, v112, v113
v_mfma_f32_16x16x32_bf16 a[160:163], v[8:11], v[44:47], a[160:163]
v_fmac_f32 v122, v112, v112
v_add_f32 v120, v120, v124
v_mfma_f32_16x16x32_bf16 a[104:107], v[8:11], v[48:51], a[104:107]
ds_read_b64_tr_b16 v[210:211], v240 offset:1152
v_fmac_f32 v123, v113, v113
s_add_u32 m0, s83, 0
v_mfma_f32_16x16x32_bf16 a[168:171], v[8:11], v[52:55], a[168:171]
buffer_load_dwordx4 v232, s[44:47], s80 offen lds
v_mfma_f32_16x16x32_bf16 a[112:115], v[8:11], v[56:59], a[112:115]
ds_read_b64_tr_b16 v[212:213], v241 offset:128
v_lshlrev_b32 v114, 16, v85
v_mfma_f32_16x16x32_bf16 a[176:179], v[8:11], v[60:63], a[176:179]
v_and_b32 v115, 0xffff0000, v85
v_sub_f32 v114, v114, v70
v_mfma_f32_16x16x32_bf16 a[204:207], v[12:15], v[32:35], a[204:207]
ds_read_b64_tr_b16 v[214:215], v241 offset:1152
v_sub_f32 v115, v115, v70
s_xor_b32 m0, s82, 3072
v_mfma_f32_16x16x32_bf16 a[156:159], v[12:15], v[36:39], a[156:159]
buffer_load_dwordx4 v227, s[40:43], s79 offen lds
v_mfma_f32_16x16x32_bf16 a[100:103], v[12:15], v[40:43], a[100:103]
ds_read_b64_tr_b16 v[216:217], v242 offset:128
v_add_f32 v124, v114, v115
v_mfma_f32_16x16x32_bf16 a[164:167], v[12:15], v[44:47], a[164:167]
v_fmac_f32 v122, v114, v114
v_add_f32 v120, v120, v124
v_mfma_f32_16x16x32_bf16 a[108:111], v[12:15], v[48:51], a[108:111]
ds_read_b64_tr_b16 v[218:219], v242 offset:1152
v_fmac_f32 v123, v115, v115
s_add_u32 m0, s83, 1024
v_mfma_f32_16x16x32_bf16 a[172:175], v[12:15], v[52:55], a[172:175]
buffer_load_dwordx4 v233, s[44:47], s80 offen lds
v_mfma_f32_16x16x32_bf16 a[116:119], v[12:15], v[56:59], a[116:119]
ds_read_b64_tr_b16 v[220:221], v243 offset:128
v_lshlrev_b32 v116, 16, v86
v_mfma_f32_16x16x32_bf16 a[180:183], v[12:15], v[60:63], a[180:183]
v_and_b32 v117, 0xffff0000, v86
v_sub_f32 v116, v116, v70
v_mfma_f32_16x16x32_bf16 a[120:123], v[16:19], v[32:35], a[120:123]
ds_read_b64_tr_b16 v[222:223], v243 offset:1152
v_sub_f32 v117, v117, v70
s_xor_b32 m0, s82, 4096
v_mfma_f32_16x16x32_bf16 a[184:187], v[16:19], v[36:39], a[184:187]
buffer_load_dwordx4 v228, s[40:43], s79 offen lds
v_mfma_f32_16x16x32_bf16 a[64:67], v[16:19], v[40:43], a[64:67]
ds_read_b128 v[132:135], v236 offset:26624
v_add_f32 v124, v116, v117
v_mfma_f32_16x16x32_bf16 a[0:3], v[16:19], v[44:47], a[0:3]
v_fmac_f32 v122, v116, v116
v_add_f32 v120, v120, v124
v_mfma_f32_16x16x32_bf16 a[72:75], v[16:19], v[48:51], a[72:75]
ds_read_b128 v[136:139], v236 offset:16384
v_fmac_f32 v123, v117, v117
s_add_u32 m0, s83, 2176
v_mfma_f32_16x16x32_bf16 a[8:11], v[16:19], v[52:55], a[8:11]
buffer_load_dwordx4 v234, s[44:47], s80 offen lds
v_mfma_f32_16x16x32_bf16 a[80:83], v[16:19], v[56:59], a[80:83]
ds_read_b128 v[140:143], v236 offset:18432
v_lshlrev_b32 v118, 16, v87
v_mfma_f32_16x16x32_bf16 a[16:19], v[16:19], v[60:63], a[16:19]
v_and_b32 v119, 0xffff0000, v87
v_sub_f32 v118, v118, v70
v_mfma_f32_16x16x32_bf16 a[124:127], v[20:23], v[32:35], a[124:127]
ds_read_b128 v[144:147], v236 offset:8192
v_sub_f32 v119, v119, v70
s_xor_b32 m0, s82, 5120
v_mfma_f32_16x16x32_bf16 a[188:191], v[20:23], v[36:39], a[188:191]
buffer_load_dwordx4 v229, s[40:43], s79 offen lds
v_mfma_f32_16x16x32_bf16 a[68:71], v[20:23], v[40:43], a[68:71]
ds_read_b128 v[148:151], v236 offset:10240
v_add_f32 v124, v118, v119
v_mfma_f32_16x16x32_bf16 a[4:7], v[20:23], v[44:47], a[4:7]
v_fmac_f32 v122, v118, v118
v_add_f32 v120, v120, v124
v_mfma_f32_16x16x32_bf16 a[76:79], v[20:23], v[48:51], a[76:79]
ds_read_b128 v[152:155], v236 offset:0
v_fmac_f32 v123, v119, v119
s_add_u32 m0, s83, 3200
v_mfma_f32_16x16x32_bf16 a[12:15], v[20:23], v[52:55], a[12:15]
buffer_load_dwordx4 v235, s[44:47], s80 offen lds
v_mfma_f32_16x16x32_bf16 a[84:87], v[20:23], v[56:59], a[84:87]
ds_read_b128 v[156:159], v236 offset:2048
v_add_f32 v121, v122, v123
v_mfma_f32_16x16x32_bf16 a[20:23], v[20:23], v[60:63], a[20:23]
s_add_u32 s80, s80, s81
s_add_u32 s83, s83, 16896
v_mfma_f32_16x16x32_bf16 a[88:91], v[24:27], v[32:35], a[88:91]
v_add_f32_dpp v120, v120, v120 row_ror:8 row_mask:0xf bank_mask:0xf
s_xor_b32 m0, s82, 6144
v_mfma_f32_16x16x32_bf16 a[24:27], v[24:27], v[36:39], a[24:27]
buffer_load_dwordx4 v230, s[40:43], s79 offen lds
s_cmp_eq_u32 s83, s97
s_cselect_b32 s83, s90, s83
v_mfma_f32_16x16x32_bf16 a[240:243], v[24:27], v[40:43], a[240:243]
v_add_f32_dpp v121, v121, v121 row_ror:8 row_mask:0xf bank_mask:0xf
v_mov_b32 v124, v120
v_mfma_f32_16x16x32_bf16 a[32:35], v[24:27], v[44:47], a[32:35]
v_mov_b32 v125, v121
v_mfma_f32_16x16x32_bf16 a[248:251], v[24:27], v[48:51], a[248:251]
v_mfma_f32_16x16x32_bf16 a[40:43], v[24:27], v[52:55], a[40:43]
v_permlane16_swap_b32 v124, v120
v_permlane16_swap_b32 v125, v121
v_mfma_f32_16x16x32_bf16 a[48:51], v[24:27], v[56:59], a[48:51]
v_add_f32 v120, v120, v124
v_add_f32 v121, v121, v125
v_mfma_f32_16x16x32_bf16 a[56:59], v[24:27], v[60:63], a[56:59]
v_mov_b32 v124, v120
v_mov_b32 v125, v121
v_mfma_f32_16x16x32_bf16 a[92:95], v[28:31], v[32:35], a[92:95]
s_xor_b32 m0, s82, 7168
v_mfma_f32_16x16x32_bf16 a[28:31], v[28:31], v[36:39], a[28:31]
buffer_load_dwordx4 v231, s[40:43], s79 offen lds
v_mfma_f32_16x16x32_bf16 a[244:247], v[28:31], v[40:43], a[244:247]
v_permlane32_swap_b32 v124, v120
v_permlane32_swap_b32 v125, v121
v_mfma_f32_16x16x32_bf16 a[36:39], v[28:31], v[44:47], a[36:39]
s_add_u32 s79, s79, 128
s_xor_b32 s82, s82, 32768
v_mfma_f32_16x16x32_bf16 a[252:255], v[28:31], v[48:51], a[252:255]
v_add_f32 v120, v120, v124
v_add_f32 v121, v121, v125
v_mfma_f32_16x16x32_bf16 a[44:47], v[28:31], v[52:55], a[44:47]
ds_write_b64 v127, v[120:121]
s_nop 0
v_mfma_f32_16x16x32_bf16 a[52:55], v[28:31], v[56:59], a[52:55]
v_readfirstlane_b32 s92, v252
v_readfirstlane_b32 s93, v253
v_mfma_f32_16x16x32_bf16 a[60:63], v[28:31], v[60:63], a[60:63]
s_add_u32 s92, s92, 1
s_addc_u32 s93, s93, 0
s_waitcnt lgkmcnt(0)
s_waitcnt vmcnt(17)
s_barrier
v_mfma_f32_16x16x32_bf16 a[224:227], v[128:131], v[192:195], a[224:227]
ds_read_b128 v[0:3], v237 offset:24576
ds_read_b128 v[72:75], v126
v_mfma_f32_16x16x32_bf16 a[232:235], v[128:131], v[196:199], a[232:235]
ds_read_b128 v[76:79], v126 offset:16
v_mfma_f32_16x16x32_bf16 a[208:211], v[128:131], v[200:203], a[208:211]
ds_read_b64_tr_b16 v[32:33], v240 offset:16896
v_mfma_f32_16x16x32_bf16 a[128:131], v[128:131], v[204:207], a[128:131]
v_mfma_f32_16x16x32_bf16 a[216:219], v[128:131], v[208:211], a[216:219]
ds_read_b64_tr_b16 v[34:35], v240 offset:17920
v_mov_b32 v252, s92
v_mfma_f32_16x16x32_bf16 a[136:139], v[128:131], v[212:215], a[136:139]
v_mov_b32 v253, s93
v_mov_b32 v160, 0
v_mfma_f32_16x16x32_bf16 a[192:195], v[128:131], v[216:219], a[192:195]
ds_read_b64_tr_b16 v[36:37], v241 offset:16896
v_mov_b32 v161, 0
v_mfma_f32_16x16x32_bf16 a[144:147], v[128:131], v[220:223], a[144:147]
v_mov_b32 v162, 0
v_mov_b32 v163, 0
v_mfma_f32_16x16x32_bf16 a[228:231], v[132:135], v[192:195], a[228:231]
ds_read_b64_tr_b16 v[38:39], v241 offset:17920
buffer_store_dwordx4 v[160:163], v255, s[52:55], s95 offen sc1
v_mfma_f32_16x16x32_bf16 a[236:239], v[132:135], v[196:199], a[236:239]
s_waitcnt lgkmcnt(4)
v_add_f32 v72, v72, v74
v_mfma_f32_16x16x32_bf16 a[212:215], v[132:135], v[200:203], a[212:215]
ds_read_b64_tr_b16 v[40:41], v242 offset:16896
v_add_f32 v73, v73, v75
v_mfma_f32_16x16x32_bf16 a[132:135], v[132:135], v[204:207], a[132:135]
v_add_f32 v76, v76, v78
v_add_f32 v77, v77, v79
v_mfma_f32_16x16x32_bf16 a[220:223], v[132:135], v[208:211], a[220:223]
ds_read_b64_tr_b16 v[42:43], v242 offset:17920
v_add_f32 v72, v72, v76
v_mfma_f32_16x16x32_bf16 a[140:143], v[132:135], v[212:215], a[140:143]
v_add_f32 v73, v73, v77
v_mul_f32 v72, 0x3a800000, v72
v_mfma_f32_16x16x32_bf16 a[196:199], v[132:135], v[216:219], a[196:199]
ds_read_b64_tr_b16 v[44:45], v243 offset:16896
v_mul_f32 v73, 0x3a800000, v73
v_mfma_f32_16x16x32_bf16 a[148:151], v[132:135], v[220:223], a[148:151]
v_add_f32 v124, v70, v72
v_fma_f32 v125, -v72, v72, v73
v_mfma_f32_16x16x32_bf16 a[200:203], v[136:139], v[192:195], a[200:203]
ds_read_b64_tr_b16 v[46:47], v243 offset:17920
v_cvt_pk_bf16_f32 v124, v124, v124
v_mfma_f32_16x16x32_bf16 a[152:155], v[136:139], v[196:199], a[152:155]
v_lshlrev_b32 v124, 16, v124
v_max_f32 v125, 0, v125
v_mfma_f32_16x16x32_bf16 a[96:99], v[136:139], v[200:203], a[96:99]
ds_read_b64_tr_b16 v[48:49], v240 offset:17024
v_cvt_pk_bf16_f32 v125, v125, v125
v_mfma_f32_16x16x32_bf16 a[160:163], v[136:139], v[204:207], a[160:163]
v_lshlrev_b32 v125, 16, v125
v_add_f32 v125, 0x3727c5ac, v125
v_mfma_f32_16x16x32_bf16 a[104:107], v[136:139], v[208:211], a[104:107]
ds_read_b64_tr_b16 v[50:51], v240 offset:18048
v_cvt_pk_bf16_f32 v125, v125, v125
v_mfma_f32_16x16x32_bf16 a[168:171], v[136:139], v[212:215], a[168:171]
v_lshlrev_b32 v125, 16, v125
v_mfma_f32_16x16x32_bf16 a[112:115], v[136:139], v[216:219], a[112:115]
ds_read_b64_tr_b16 v[52:53], v241 offset:17024
v_mfma_f32_16x16x32_bf16 a[176:179], v[136:139], v[220:223], a[176:179]
v_sqrt_f32 v125, v125
v_mfma_f32_16x16x32_bf16 a[204:207], v[140:143], v[192:195], a[204:207]
ds_read_b64_tr_b16 v[54:55], v241 offset:18048
v_sub_f32 v124, v70, v124
v_mfma_f32_16x16x32_bf16 a[156:159], v[140:143], v[196:199], a[156:159]
v_cvt_pk_bf16_f32 v125, v125, v125
v_lshlrev_b32 v125, 16, v125
v_mfma_f32_16x16x32_bf16 a[100:103], v[140:143], v[200:203], a[100:103]
ds_read_b64_tr_b16 v[56:57], v242 offset:17024
v_mfma_f32_16x16x32_bf16 a[164:167], v[140:143], v[204:207], a[164:167]
v_rcp_f32 v125, v125
v_mfma_f32_16x16x32_bf16 a[108:111], v[140:143], v[208:211], a[108:111]
ds_read_b64_tr_b16 v[58:59], v242 offset:18048
v_mul_f32 v125, 0x3e0293ee, v125
v_mfma_f32_16x16x32_bf16 a[172:175], v[140:143], v[212:215], a[172:175]
v_mul_f32 v124, v124, v125
v_fma_f32 v88, v88, v125, v124
v_mfma_f32_16x16x32_bf16 a[116:119], v[140:143], v[216:219], a[116:119]
ds_read_b64_tr_b16 v[60:61], v243 offset:17024
v_fma_f32 v89, v89, v125, v124
v_mfma_f32_16x16x32_bf16 a[180:183], v[140:143], v[220:223], a[180:183]
v_cvt_pk_bf16_f32 v72, v88, v89
v_fma_f32 v90, v90, v125, v124
v_mfma_f32_16x16x32_bf16 a[120:123], v[144:147], v[192:195], a[120:123]
ds_read_b64_tr_b16 v[62:63], v243 offset:18048
v_fma_f32 v91, v91, v125, v124
s_add_u32 m0, s83, 0
v_mfma_f32_16x16x32_bf16 a[184:187], v[144:147], v[196:199], a[184:187]
buffer_load_dwordx4 v232, s[44:47], s80 offen lds
v_mfma_f32_16x16x32_bf16 a[64:67], v[144:147], v[200:203], a[64:67]
ds_read_b128 v[4:7], v237 offset:26624
v_cvt_pk_bf16_f32 v73, v90, v91
v_mfma_f32_16x16x32_bf16 a[0:3], v[144:147], v[204:207], a[0:3]
v_fma_f32 v92, v92, v125, v124
v_fma_f32 v93, v93, v125, v124
v_mfma_f32_16x16x32_bf16 a[72:75], v[144:147], v[208:211], a[72:75]
ds_read_b128 v[8:11], v237 offset:16384
v_cvt_pk_bf16_f32 v74, v92, v93
s_add_u32 m0, s83, 1024
v_mfma_f32_16x16x32_bf16 a[8:11], v[144:147], v[212:215], a[8:11]
buffer_load_dwordx4 v233, s[44:47], s80 offen lds
v_mfma_f32_16x16x32_bf16 a[80:83], v[144:147], v[216:219], a[80:83]
ds_read_b128 v[12:15], v237 offset:18432
v_fma_f32 v94, v94, v125, v124
v_mfma_f32_16x16x32_bf16 a[16:19], v[144:147], v[220:223], a[16:19]
v_fma_f32 v95, v95, v125, v124
v_cvt_pk_bf16_f32 v75, v94, v95
v_mfma_f32_16x16x32_bf16 a[124:127], v[148:151], v[192:195], a[124:127]
ds_read_b128 v[16:19], v237 offset:8192
buffer_store_dwordx4 v[72:75], v71, s[56:59], 0 offen offset:0 sc1
v_fma_f32 v96, v96, v125, v124
s_add_u32 m0, s83, 2176
v_mfma_f32_16x16x32_bf16 a[188:191], v[148:151], v[196:199], a[188:191]
buffer_load_dwordx4 v234, s[44:47], s80 offen lds
v_fma_f32 v97, v97, v125, v124
v_mfma_f32_16x16x32_bf16 a[68:71], v[148:151], v[200:203], a[68:71]
ds_read_b128 v[20:23], v237 offset:10240
v_cvt_pk_bf16_f32 v76, v96, v97
v_fma_f32 v98, v98, v125, v124
v_mfma_f32_16x16x32_bf16 a[4:7], v[148:151], v[204:207], a[4:7]
v_fma_f32 v99, v99, v125, v124
v_cvt_pk_bf16_f32 v77, v98, v99
v_fma_f32 v100, v100, v125, v124
v_mfma_f32_16x16x32_bf16 a[76:79], v[148:151], v[208:211], a[76:79]
ds_read_b128 v[24:27], v237 offset:0
v_fma_f32 v101, v101, v125, v124
v_cvt_pk_bf16_f32 v78, v100, v101
s_add_u32 m0, s83, 3200
v_mfma_f32_16x16x32_bf16 a[12:15], v[148:151], v[212:215], a[12:15]
buffer_load_dwordx4 v235, s[44:47], s80 offen lds
v_fma_f32 v102, v102, v125, v124
v_mfma_f32_16x16x32_bf16 a[84:87], v[148:151], v[216:219], a[84:87]
ds_read_b128 v[28:31], v237 offset:2048
v_fma_f32 v103, v103, v125, v124
v_cvt_pk_bf16_f32 v79, v102, v103
v_mfma_f32_16x16x32_bf16 a[20:23], v[148:151], v[220:223], a[20:23]
s_add_u32 s80, s80, s81
s_add_u32 s83, s83, 16896
buffer_store_dwordx4 v[76:79], v71, s[56:59], 0 offen offset:2048 sc1
v_mfma_f32_16x16x32_bf16 a[88:91], v[152:155], v[192:195], a[88:91]
.La5_LW1_4:
s_cbranch_execz .La5_LW1_8
v_fma_f32 v104, v104, v125, v124
v_fma_f32 v105, v105, v125, v124
v_cvt_pk_bf16_f32 v80, v104, v105
v_mfma_f32_16x16x32_bf16 a[24:27], v[152:155], v[196:199], a[24:27]
s_cmp_eq_u32 s83, s97
s_cselect_b32 s83, s90, s83
v_fma_f32 v106, v106, v125, v124
v_mfma_f32_16x16x32_bf16 a[240:243], v[152:155], v[200:203], a[240:243]
v_fma_f32 v107, v107, v125, v124
v_cvt_pk_bf16_f32 v81, v106, v107
v_fma_f32 v108, v108, v125, v124
v_mfma_f32_16x16x32_bf16 a[32:35], v[152:155], v[204:207], a[32:35]
v_fma_f32 v109, v109, v125, v124
v_cvt_pk_bf16_f32 v82, v108, v109
v_fma_f32 v110, v110, v125, v124
v_mfma_f32_16x16x32_bf16 a[248:251], v[152:155], v[208:211], a[248:251]
v_fma_f32 v111, v111, v125, v124
v_cvt_pk_bf16_f32 v83, v110, v111
buffer_store_dwordx4 v[80:83], v71, s[56:59], s88 offen offset:0 sc1
v_mfma_f32_16x16x32_bf16 a[40:43], v[152:155], v[212:215], a[40:43]
v_fma_f32 v112, v112, v125, v124
v_fma_f32 v113, v113, v125, v124
v_cvt_pk_bf16_f32 v84, v112, v113
v_mfma_f32_16x16x32_bf16 a[48:51], v[152:155], v[216:219], a[48:51]
v_fma_f32 v114, v114, v125, v124
v_fma_f32 v115, v115, v125, v124
v_cvt_pk_bf16_f32 v85, v114, v115
v_mfma_f32_16x16x32_bf16 a[56:59], v[152:155], v[220:223], a[56:59]
v_fma_f32 v116, v116, v125, v124
v_fma_f32 v117, v117, v125, v124
v_cvt_pk_bf16_f32 v86, v116, v117
v_mfma_f32_16x16x32_bf16 a[92:95], v[156:159], v[192:195], a[92:95]
v_fma_f32 v118, v118, v125, v124
v_fma_f32 v119, v119, v125, v124
v_cvt_pk_bf16_f32 v87, v118, v119
v_mfma_f32_16x16x32_bf16 a[28:31], v[156:159], v[196:199], a[28:31]
buffer_store_dwordx4 v[84:87], v71, s[56:59], s88 offen offset:2048 sc1
v_mfma_f32_16x16x32_bf16 a[244:247], v[156:159], v[200:203], a[244:247]
v_add_u32 v240, s84, v240
v_add_u32 v241, s84, v241
v_mfma_f32_16x16x32_bf16 a[36:39], v[156:159], v[204:207], a[36:39]
v_add_u32 v242, s84, v242
v_add_u32 v243, s84, v243
v_mfma_f32_16x16x32_bf16 a[252:255], v[156:159], v[208:211], a[252:255]
v_xor_b32 v236, 0x8000, v236
v_xor_b32 v237, 0x8000, v237
v_mfma_f32_16x16x32_bf16 a[44:47], v[156:159], v[212:215], a[44:47]
s_sub_u32 s84, 0, s84
v_mfma_f32_16x16x32_bf16 a[52:55], v[156:159], v[216:219], a[52:55]
v_mfma_f32_16x16x32_bf16 a[60:63], v[156:159], v[220:223], a[60:63]
s_waitcnt lgkmcnt(0)
.p2align 6
.La5_LV_LOOP:
s_waitcnt vmcnt(9)
s_barrier
v_mfma_f32_16x16x32_bf16 a[224:227], v[0:3], v[32:35], a[224:227]
ds_read_b128 v[128:131], v236 offset:24576
s_xor_b32 m0, s82, 0
v_mfma_f32_16x16x32_bf16 a[232:235], v[0:3], v[36:39], a[232:235]
buffer_load_dwordx4 v224, s[40:43], s79 offen lds
v_mfma_f32_16x16x32_bf16 a[208:211], v[0:3], v[40:43], a[208:211]
ds_read_b64_tr_b16 v[192:193], v240 offset:0
v_mfma_f32_16x16x32_bf16 a[128:131], v[0:3], v[44:47], a[128:131]
v_mfma_f32_16x16x32_bf16 a[216:219], v[0:3], v[48:51], a[216:219]
ds_read_b64_tr_b16 v[194:195], v240 offset:1024
s_xor_b32 m0, s82, 1024
v_mfma_f32_16x16x32_bf16 a[136:139], v[0:3], v[52:55], a[136:139]
buffer_load_dwordx4 v225, s[40:43], s79 offen lds
v_mfma_f32_16x16x32_bf16 a[192:195], v[0:3], v[56:59], a[192:195]
ds_read_b64_tr_b16 v[196:197], v241 offset:0
v_mfma_f32_16x16x32_bf16 a[144:147], v[0:3], v[60:63], a[144:147]
v_mfma_f32_16x16x32_bf16 a[228:231], v[4:7], v[32:35], a[228:231]
ds_read_b64_tr_b16 v[198:199], v241 offset:1024
s_xor_b32 m0, s82, 2048
v_mfma_f32_16x16x32_bf16 a[236:239], v[4:7], v[36:39], a[236:239]
buffer_load_dwordx4 v226, s[40:43], s79 offen lds
v_mfma_f32_16x16x32_bf16 a[212:215], v[4:7], v[40:43], a[212:215]
ds_read_b64_tr_b16 v[200:201], v242 offset:0
v_mfma_f32_16x16x32_bf16 a[132:135], v[4:7], v[44:47], a[132:135]
v_mfma_f32_16x16x32_bf16 a[220:223], v[4:7], v[48:51], a[220:223]
ds_read_b64_tr_b16 v[202:203], v242 offset:1024
s_xor_b32 m0, s82, 3072
v_mfma_f32_16x16x32_bf16 a[140:143], v[4:7], v[52:55], a[140:143]
buffer_load_dwordx4 v227, s[40:43], s79 offen lds
v_mfma_f32_16x16x32_bf16 a[196:199], v[4:7], v[56:59], a[196:199]
ds_read_b64_tr_b16 v[204:205], v243 offset:0
v_mfma_f32_16x16x32_bf16 a[148:151], v[4:7], v[60:63], a[148:151]
v_mfma_f32_16x16x32_bf16 a[200:203], v[8:11], v[32:35], a[200:203]
ds_read_b64_tr_b16 v[206:207], v243 offset:1024
s_xor_b32 m0, s82, 4096
v_mfma_f32_16x16x32_bf16 a[152:155], v[8:11], v[36:39], a[152:155]
buffer_load_dwordx4 v228, s[40:43], s79 offen lds
v_mfma_f32_16x16x32_bf16 a[96:99], v[8:11], v[40:43], a[96:99]
ds_read_b64_tr_b16 v[208:209], v240 offset:128
v_mfma_f32_16x16x32_bf16 a[160:163], v[8:11], v[44:47], a[160:163]
v_mfma_f32_16x16x32_bf16 a[104:107], v[8:11], v[48:51], a[104:107]
ds_read_b64_tr_b16 v[210:211], v240 offset:1152
s_xor_b32 m0, s82, 5120
v_mfma_f32_16x16x32_bf16 a[168:171], v[8:11], v[52:55], a[168:171]
buffer_load_dwordx4 v229, s[40:43], s79 offen lds
v_mfma_f32_16x16x32_bf16 a[112:115], v[8:11], v[56:59], a[112:115]
ds_read_b64_tr_b16 v[212:213], v241 offset:128
v_mfma_f32_16x16x32_bf16 a[176:179], v[8:11], v[60:63], a[176:179]
v_mfma_f32_16x16x32_bf16 a[204:207], v[12:15], v[32:35], a[204:207]
ds_read_b64_tr_b16 v[214:215], v241 offset:1152
s_xor_b32 m0, s82, 6144
v_mfma_f32_16x16x32_bf16 a[156:159], v[12:15], v[36:39], a[156:159]
buffer_load_dwordx4 v230, s[40:43], s79 offen lds
v_mfma_f32_16x16x32_bf16 a[100:103], v[12:15], v[40:43], a[100:103]
ds_read_b64_tr_b16 v[216:217], v242 offset:128
v_mfma_f32_16x16x32_bf16 a[164:167], v[12:15], v[44:47], a[164:167]
v_mfma_f32_16x16x32_bf16 a[108:111], v[12:15], v[48:51], a[108:111]
ds_read_b64_tr_b16 v[218:219], v242 offset:1152
s_xor_b32 m0, s82, 7168
v_mfma_f32_16x16x32_bf16 a[172:175], v[12:15], v[52:55], a[172:175]
buffer_load_dwordx4 v231, s[40:43], s79 offen lds
v_mfma_f32_16x16x32_bf16 a[116:119], v[12:15], v[56:59], a[116:119]
ds_read_b64_tr_b16 v[220:221], v243 offset:128
v_mfma_f32_16x16x32_bf16 a[180:183], v[12:15], v[60:63], a[180:183]
s_add_u32 s79, s79, 128
s_xor_b32 s82, s82, 32768
v_mfma_f32_16x16x32_bf16 a[120:123], v[16:19], v[32:35], a[120:123]
ds_read_b64_tr_b16 v[222:223], v243 offset:1152
s_add_u32 m0, s83, 0
v_mfma_f32_16x16x32_bf16 a[184:187], v[16:19], v[36:39], a[184:187]
buffer_load_dwordx4 v232, s[44:47], s80 offen lds
v_mfma_f32_16x16x32_bf16 a[64:67], v[16:19], v[40:43], a[64:67]
ds_read_b128 v[132:135], v236 offset:26624
v_mfma_f32_16x16x32_bf16 a[0:3], v[16:19], v[44:47], a[0:3]
v_mfma_f32_16x16x32_bf16 a[72:75], v[16:19], v[48:51], a[72:75]
ds_read_b128 v[136:139], v236 offset:16384
s_add_u32 m0, s83, 1024
v_mfma_f32_16x16x32_bf16 a[8:11], v[16:19], v[52:55], a[8:11]
buffer_load_dwordx4 v233, s[44:47], s80 offen lds
v_mfma_f32_16x16x32_bf16 a[80:83], v[16:19], v[56:59], a[80:83]
ds_read_b128 v[140:143], v236 offset:18432
v_mfma_f32_16x16x32_bf16 a[16:19], v[16:19], v[60:63], a[16:19]
v_mfma_f32_16x16x32_bf16 a[124:127], v[20:23], v[32:35], a[124:127]
ds_read_b128 v[144:147], v236 offset:8192
s_add_u32 m0, s83, 2176
v_mfma_f32_16x16x32_bf16 a[188:191], v[20:23], v[36:39], a[188:191]
buffer_load_dwordx4 v234, s[44:47], s80 offen lds
v_mfma_f32_16x16x32_bf16 a[68:71], v[20:23], v[40:43], a[68:71]
ds_read_b128 v[148:151], v236 offset:10240
v_mfma_f32_16x16x32_bf16 a[4:7], v[20:23], v[44:47], a[4:7]
v_mfma_f32_16x16x32_bf16 a[76:79], v[20:23], v[48:51], a[76:79]
ds_read_b128 v[152:155], v236 offset:0
s_add_u32 m0, s83, 3200
v_mfma_f32_16x16x32_bf16 a[12:15], v[20:23], v[52:55], a[12:15]
buffer_load_dwordx4 v235, s[44:47], s80 offen lds
v_mfma_f32_16x16x32_bf16 a[84:87], v[20:23], v[56:59], a[84:87]
ds_read_b128 v[156:159], v236 offset:2048
v_mfma_f32_16x16x32_bf16 a[20:23], v[20:23], v[60:63], a[20:23]
s_add_u32 s80, s80, s81
s_add_u32 s83, s83, 16896
v_mfma_f32_16x16x32_bf16 a[88:91], v[24:27], v[32:35], a[88:91]
v_mfma_f32_16x16x32_bf16 a[24:27], v[24:27], v[36:39], a[24:27]
s_cmp_eq_u32 s83, s97
s_cselect_b32 s83, s90, s83
v_mfma_f32_16x16x32_bf16 a[240:243], v[24:27], v[40:43], a[240:243]
v_mfma_f32_16x16x32_bf16 a[32:35], v[24:27], v[44:47], a[32:35]
buffer_load_dword v254, v167, s[68:71], s96 offen
s_add_u32 s96, s96, 1048576
v_mfma_f32_16x16x32_bf16 a[248:251], v[24:27], v[48:51], a[248:251]
v_mfma_f32_16x16x32_bf16 a[40:43], v[24:27], v[52:55], a[40:43]
v_mfma_f32_16x16x32_bf16 a[48:51], v[24:27], v[56:59], a[48:51]
v_mfma_f32_16x16x32_bf16 a[56:59], v[24:27], v[60:63], a[56:59]
buffer_load_dword v254, v167, s[68:71], s96 offen
s_add_u32 s96, s96, 1048576
v_mfma_f32_16x16x32_bf16 a[92:95], v[28:31], v[32:35], a[92:95]
s_lshl_b32 s92, s77, 18
s_cmp_eq_u32 s85, 3
v_mfma_f32_16x16x32_bf16 a[28:31], v[28:31], v[36:39], a[28:31]
s_cselect_b32 s44, s68, s44
s_cselect_b32 s45, s69, s45
v_mfma_f32_16x16x32_bf16 a[244:247], v[28:31], v[40:43], a[244:247]
s_cselect_b32 s46, s70, s46
s_cselect_b32 s81, 524288, s81
v_mfma_f32_16x16x32_bf16 a[36:39], v[28:31], v[44:47], a[36:39]
s_cselect_b32 s42, 0, s42
s_cselect_b32 s80, s92, s80
v_mfma_f32_16x16x32_bf16 a[252:255], v[28:31], v[48:51], a[252:255]
v_mfma_f32_16x16x32_bf16 a[44:47], v[28:31], v[52:55], a[44:47]
v_mfma_f32_16x16x32_bf16 a[52:55], v[28:31], v[56:59], a[52:55]
v_mfma_f32_16x16x32_bf16 a[60:63], v[28:31], v[60:63], a[60:63]
s_waitcnt lgkmcnt(0)
s_waitcnt vmcnt(22)
s_barrier
v_mfma_f32_16x16x32_bf16 a[224:227], v[128:131], v[192:195], a[224:227]
ds_read_b128 v[0:3], v237 offset:24576
v_mfma_f32_16x16x32_bf16 a[232:235], v[128:131], v[196:199], a[232:235]
s_waitcnt vmcnt(14)
buffer_store_dwordx2 v[252:253], off, s[64:67], s91 offset:1024 sc1
v_mfma_f32_16x16x32_bf16 a[208:211], v[128:131], v[200:203], a[208:211]
ds_read_b64_tr_b16 v[32:33], v240 offset:16896
v_mfma_f32_16x16x32_bf16 a[128:131], v[128:131], v[204:207], a[128:131]
s_cmp_eq_u32 s85, 4
s_cselect_b32 s92, s89, 1073741824
v_mfma_f32_16x16x32_bf16 a[216:219], v[128:131], v[208:211], a[216:219]
ds_read_b64_tr_b16 v[34:35], v240 offset:17920
v_mfma_f32_16x16x32_bf16 a[136:139], v[128:131], v[212:215], a[136:139]
buffer_load_dwordx4 v[72:75], v255, s[64:67], s92 offen offset:1024 sc1
v_mfma_f32_16x16x32_bf16 a[192:195], v[128:131], v[216:219], a[192:195]
ds_read_b64_tr_b16 v[36:37], v241 offset:16896
v_mfma_f32_16x16x32_bf16 a[144:147], v[128:131], v[220:223], a[144:147]
s_mov_b32 s91, 1073741824
v_mfma_f32_16x16x32_bf16 a[228:231], v[132:135], v[192:195], a[228:231]
ds_read_b64_tr_b16 v[38:39], v241 offset:17920
v_mfma_f32_16x16x32_bf16 a[236:239], v[132:135], v[196:199], a[236:239]
v_mfma_f32_16x16x32_bf16 a[212:215], v[132:135], v[200:203], a[212:215]
ds_read_b64_tr_b16 v[40:41], v242 offset:16896
s_lshl_b32 s92, s76, 19
v_mfma_f32_16x16x32_bf16 a[132:135], v[132:135], v[204:207], a[132:135]
s_add_u32 s52, s26, s92
s_addc_u32 s53, s27, 0
v_mfma_f32_16x16x32_bf16 a[220:223], v[132:135], v[208:211], a[220:223]
ds_read_b64_tr_b16 v[42:43], v242 offset:17920
s_and_b32 s53, s53, 0xffff
v_mfma_f32_16x16x32_bf16 a[140:143], v[132:135], v[212:215], a[140:143]
s_mov_b32 s54, 524288
s_mov_b32 s55, 0x20000
v_mfma_f32_16x16x32_bf16 a[196:199], v[132:135], v[216:219], a[196:199]
ds_read_b64_tr_b16 v[44:45], v243 offset:16896
s_lshl_b32 s92, s3, 16
v_mfma_f32_16x16x32_bf16 a[148:151], v[132:135], v[220:223], a[148:151]
s_sub_u32 s93, 458752, s92
v_add_u32 v244, s92, v255
v_mfma_f32_16x16x32_bf16 a[200:203], v[136:139], v[192:195], a[200:203]
ds_read_b64_tr_b16 v[46:47], v243 offset:17920
v_add_u32 v246, s93, v255
v_mfma_f32_16x16x32_bf16 a[152:155], v[136:139], v[196:199], a[152:155]
v_add_u32 v245, 0x8000, v244
v_add_u32 v247, 0x8000, v246
v_mfma_f32_16x16x32_bf16 a[96:99], v[136:139], v[200:203], a[96:99]
ds_read_b64_tr_b16 v[48:49], v240 offset:17024
s_lshl_b32 s94, s72, 19
v_mfma_f32_16x16x32_bf16 a[160:163], v[136:139], v[204:207], a[160:163]
s_lshl_b32 s93, s76, 16
s_add_u32 s92, s94, s93
v_mfma_f32_16x16x32_bf16 a[104:107], v[136:139], v[208:211], a[104:107]
ds_read_b64_tr_b16 v[50:51], v240 offset:18048
s_lshl_b32 s93, s77, 13
v_mfma_f32_16x16x32_bf16 a[168:171], v[136:139], v[212:215], a[168:171]
s_add_u32 s92, s92, s93
s_add_u32 s92, s92, 37748736
v_mfma_f32_16x16x32_bf16 a[112:115], v[136:139], v[216:219], a[112:115]
ds_read_b64_tr_b16 v[52:53], v241 offset:17024
v_mfma_f32_16x16x32_bf16 a[176:179], v[136:139], v[220:223], a[176:179]
s_add_u32 s48, s26, s92
s_addc_u32 s49, s27, 0
v_mfma_f32_16x16x32_bf16 a[204:207], v[140:143], v[192:195], a[204:207]
ds_read_b64_tr_b16 v[54:55], v241 offset:18048
s_and_b32 s49, s49, 0xffff
v_mfma_f32_16x16x32_bf16 a[156:159], v[140:143], v[196:199], a[156:159]
s_mov_b32 s50, 57344
s_mov_b32 s51, 0x20000
v_mfma_f32_16x16x32_bf16 a[100:103], v[140:143], v[200:203], a[100:103]
ds_read_b64_tr_b16 v[56:57], v242 offset:17024
s_lshl_b32 s93, s77, 24
v_mfma_f32_16x16x32_bf16 a[164:167], v[140:143], v[204:207], a[164:167]
s_add_u32 s92, s94, s93
s_add_u32 s92, s92, 20975616
v_mfma_f32_16x16x32_bf16 a[108:111], v[140:143], v[208:211], a[108:111]
ds_read_b64_tr_b16 v[58:59], v242 offset:18048
v_mfma_f32_16x16x32_bf16 a[172:175], v[140:143], v[212:215], a[172:175]
s_add_u32 s60, s26, s92
s_addc_u32 s61, s27, 0
v_mfma_f32_16x16x32_bf16 a[116:119], v[140:143], v[216:219], a[116:119]
ds_read_b64_tr_b16 v[60:61], v243 offset:17024
s_and_b32 s61, s61, 0xffff
v_mfma_f32_16x16x32_bf16 a[180:183], v[140:143], v[220:223], a[180:183]
s_mov_b32 s62, 520192
s_mov_b32 s63, 0x20000
v_mfma_f32_16x16x32_bf16 a[120:123], v[144:147], v[192:195], a[120:123]
ds_read_b64_tr_b16 v[62:63], v243 offset:18048
s_lshl_b32 s92, s77, 14
s_add_u32 m0, s83, 0
v_mfma_f32_16x16x32_bf16 a[184:187], v[144:147], v[196:199], a[184:187]
buffer_load_dwordx4 v232, s[44:47], s80 offen lds
v_mfma_f32_16x16x32_bf16 a[64:67], v[144:147], v[200:203], a[64:67]
ds_read_b128 v[4:7], v237 offset:26624
s_sub_u32 s95, 24576, s92
v_mfma_f32_16x16x32_bf16 a[0:3], v[144:147], v[204:207], a[0:3]
v_add_u32 v238, s95, v255
v_mfma_f32_16x16x32_bf16 a[72:75], v[144:147], v[208:211], a[72:75]
ds_read_b128 v[8:11], v237 offset:16384
s_add_u32 m0, s83, 1024
v_mfma_f32_16x16x32_bf16 a[8:11], v[144:147], v[212:215], a[8:11]
buffer_load_dwordx4 v233, s[44:47], s80 offen lds
v_mfma_f32_16x16x32_bf16 a[80:83], v[144:147], v[216:219], a[80:83]
ds_read_b128 v[12:15], v237 offset:18432
v_mfma_f32_16x16x32_bf16 a[16:19], v[144:147], v[220:223], a[16:19]
v_mfma_f32_16x16x32_bf16 a[124:127], v[148:151], v[192:195], a[124:127]
ds_read_b128 v[16:19], v237 offset:8192
s_add_u32 m0, s83, 2176
v_mfma_f32_16x16x32_bf16 a[188:191], v[148:151], v[196:199], a[188:191]
buffer_load_dwordx4 v234, s[44:47], s80 offen lds
v_mfma_f32_16x16x32_bf16 a[68:71], v[148:151], v[200:203], a[68:71]
ds_read_b128 v[20:23], v237 offset:10240
v_mfma_f32_16x16x32_bf16 a[4:7], v[148:151], v[204:207], a[4:7]
v_mfma_f32_16x16x32_bf16 a[76:79], v[148:151], v[208:211], a[76:79]
ds_read_b128 v[24:27], v237 offset:0
s_add_u32 m0, s83, 3200
v_mfma_f32_16x16x32_bf16 a[12:15], v[148:151], v[212:215], a[12:15]
buffer_load_dwordx4 v235, s[44:47], s80 offen lds
v_mfma_f32_16x16x32_bf16 a[84:87], v[148:151], v[216:219], a[84:87]
ds_read_b128 v[28:31], v237 offset:2048
v_mfma_f32_16x16x32_bf16 a[20:23], v[148:151], v[220:223], a[20:23]
s_add_u32 s80, s80, s81
s_add_u32 s83, s83, 16896
v_mfma_f32_16x16x32_bf16 a[88:91], v[152:155], v[192:195], a[88:91]
v_mfma_f32_16x16x32_bf16 a[24:27], v[152:155], v[196:199], a[24:27]
s_cmp_eq_u32 s83, s97
s_cselect_b32 s83, s90, s83
v_mfma_f32_16x16x32_bf16 a[240:243], v[152:155], v[200:203], a[240:243]
v_mfma_f32_16x16x32_bf16 a[32:35], v[152:155], v[204:207], a[32:35]
v_mfma_f32_16x16x32_bf16 a[248:251], v[152:155], v[208:211], a[248:251]
v_mfma_f32_16x16x32_bf16 a[40:43], v[152:155], v[212:215], a[40:43]
v_mfma_f32_16x16x32_bf16 a[48:51], v[152:155], v[216:219], a[48:51]
v_mfma_f32_16x16x32_bf16 a[56:59], v[152:155], v[220:223], a[56:59]
v_mfma_f32_16x16x32_bf16 a[92:95], v[156:159], v[192:195], a[92:95]
v_mfma_f32_16x16x32_bf16 a[28:31], v[156:159], v[196:199], a[28:31]
v_add_u32 v240, s84, v240
v_add_u32 v241, s84, v241
v_mfma_f32_16x16x32_bf16 a[244:247], v[156:159], v[200:203], a[244:247]
v_add_u32_e64 v242, s84, v242
v_add_u32_e64 v243, s84, v243
v_mfma_f32_16x16x32_bf16 a[36:39], v[156:159], v[204:207], a[36:39]
v_xor_b32 v236, 0x8000, v236
v_xor_b32 v237, 0x8000, v237
v_mfma_f32_16x16x32_bf16 a[252:255], v[156:159], v[208:211], a[252:255]
s_sub_u32 s84, 0, s84
s_add_u32 s85, s85, 1
v_mfma_f32_16x16x32_bf16 a[44:47], v[156:159], v[212:215], a[44:47]
s_cmp_lg_u32 s85, 5
v_mfma_f32_16x16x32_bf16 a[52:55], v[156:159], v[216:219], a[52:55]
v_mfma_f32_16x16x32_bf16 a[60:63], v[156:159], v[220:223], a[60:63]
s_waitcnt lgkmcnt(0)
s_cbranch_scc1 .La5_LV_LOOP
s_waitcnt vmcnt(12)
s_barrier
v_mfma_f32_16x16x32_bf16 a[224:227], v[0:3], v[32:35], a[224:227]
v_mfma_f32_16x16x32_bf16 a[232:235], v[0:3], v[36:39], a[232:235]
s_waitcnt vmcnt(4)
v_cmp_eq_u64 vcc, v[72:73], v[252:253]
v_mfma_f32_16x16x32_bf16 a[208:211], v[0:3], v[40:43], a[208:211]
v_cmp_eq_u64 s[96:97], v[74:75], v[252:253]
s_mov_b32 s91, 0
v_mfma_f32_16x16x32_bf16 a[128:131], v[0:3], v[44:47], a[128:131]
s_and_b64 vcc, vcc, s[96:97]
s_cmp_eq_u64 vcc, -1
s_cbranch_scc0 .La5_LV_XSPIN
.La5_LV_XOK:
v_mfma_f32_16x16x32_bf16 a[216:219], v[0:3], v[48:51], a[216:219]
s_mov_b32 s89, 49152
v_subrev_u32 v240, 8448, v240
v_mfma_f32_16x16x32_bf16 a[136:139], v[0:3], v[52:55], a[136:139]
v_subrev_u32 v241, 8448, v241
v_subrev_u32 v242, 8448, v242
buffer_load_dwordx4 v[128:131], v244, s[52:55], s91 offen offset:0 sc1
v_mfma_f32_16x16x32_bf16 a[192:195], v[0:3], v[56:59], a[192:195]
v_subrev_u32 v243, 8448, v243
v_mfma_f32_16x16x32_bf16 a[144:147], v[0:3], v[60:63], a[144:147]
v_mfma_f32_16x16x32_bf16 a[228:231], v[4:7], v[32:35], a[228:231]
ds_read_b64_tr_b16 v[192:193], v240 offset:0
v_mfma_f32_16x16x32_bf16 a[236:239], v[4:7], v[36:39], a[236:239]
buffer_load_dwordx4 v[132:135], v245, s[52:55], s91 offen offset:0 sc1
v_mfma_f32_16x16x32_bf16 a[212:215], v[4:7], v[40:43], a[212:215]
ds_read_b64_tr_b16 v[194:195], v240 offset:1024
v_mfma_f32_16x16x32_bf16 a[132:135], v[4:7], v[44:47], a[132:135]
v_mfma_f32_16x16x32_bf16 a[220:223], v[4:7], v[48:51], a[220:223]
ds_read_b64_tr_b16 v[196:197], v241 offset:0
v_mfma_f32_16x16x32_bf16 a[140:143], v[4:7], v[52:55], a[140:143]
buffer_load_dwordx4 v[136:139], v246, s[52:55], s91 offen offset:0 sc1
v_mfma_f32_16x16x32_bf16 a[196:199], v[4:7], v[56:59], a[196:199]
ds_read_b64_tr_b16 v[198:199], v241 offset:1024
v_accvgpr_read_b32 v224, a224
v_mfma_f32_16x16x32_bf16 a[148:151], v[4:7], v[60:63], a[148:151]
v_accvgpr_read_b32 v225, a225
v_accvgpr_read_b32 v226, a226
v_mfma_f32_16x16x32_bf16 a[200:203], v[8:11], v[32:35], a[200:203]
ds_read_b64_tr_b16 v[200:201], v242 offset:0
v_accvgpr_read_b32 v227, a227
v_mfma_f32_16x16x32_bf16 a[152:155], v[8:11], v[36:39], a[152:155]
buffer_load_dwordx4 v[140:143], v247, s[52:55], s91 offen offset:0 sc1
v_accvgpr_read_b32 v228, a228
v_mfma_f32_16x16x32_bf16 a[96:99], v[8:11], v[40:43], a[96:99]
ds_read_b64_tr_b16 v[202:203], v242 offset:1024
v_accvgpr_read_b32 v229, a229
v_mfma_f32_16x16x32_bf16 a[160:163], v[8:11], v[44:47], a[160:163]
v_accvgpr_read_b32 v230, a230
v_accvgpr_read_b32 v231, a231
v_mfma_f32_16x16x32_bf16 a[104:107], v[8:11], v[48:51], a[104:107]
ds_read_b64_tr_b16 v[204:205], v243 offset:0
v_cvt_pk_bf16_f32 v248, v224, v225
v_mfma_f32_16x16x32_bf16 a[168:171], v[8:11], v[52:55], a[168:171]
buffer_load_dwordx4 v[144:147], v244, s[52:55], s91 offen offset:1024 sc1
v_cvt_pk_bf16_f32 v249, v226, v227
v_mfma_f32_16x16x32_bf16 a[112:115], v[8:11], v[56:59], a[112:115]
ds_read_b64_tr_b16 v[206:207], v243 offset:1024
v_cvt_pk_bf16_f32 v250, v228, v229
v_mfma_f32_16x16x32_bf16 a[176:179], v[8:11], v[60:63], a[176:179]
v_cvt_pk_bf16_f32 v251, v230, v231
buffer_store_dwordx4 v[248:251], v255, s[48:51], s89 offen sc1
v_mfma_f32_16x16x32_bf16 a[204:207], v[12:15], v[32:35], a[204:207]
ds_read_b64_tr_b16 v[208:209], v240 offset:128
s_add_u32 s89, s89, 1024
v_mfma_f32_16x16x32_bf16 a[156:159], v[12:15], v[36:39], a[156:159]
buffer_load_dwordx4 v[148:151], v245, s[52:55], s91 offen offset:1024 sc1
ds_write_b128 v238, v[248:251] offset:0
v_mfma_f32_16x16x32_bf16 a[100:103], v[12:15], v[40:43], a[100:103]
ds_read_b64_tr_b16 v[210:211], v240 offset:1152
v_mfma_f32_16x16x32_bf16 a[164:167], v[12:15], v[44:47], a[164:167]
v_mfma_f32_16x16x32_bf16 a[108:111], v[12:15], v[48:51], a[108:111]
ds_read_b64_tr_b16 v[212:213], v241 offset:128
v_mfma_f32_16x16x32_bf16 a[172:175], v[12:15], v[52:55], a[172:175]
buffer_load_dwordx4 v[152:155], v246, s[52:55], s91 offen offset:1024 sc1
v_mfma_f32_16x16x32_bf16 a[116:119], v[12:15], v[56:59], a[116:119]
ds_read_b64_tr_b16 v[214:215], v241 offset:1152
v_accvgpr_read_b32 v224, a232
v_mfma_f32_16x16x32_bf16 a[180:183], v[12:15], v[60:63], a[180:183]
v_accvgpr_read_b32 v225, a233
v_accvgpr_read_b32 v226, a234
v_mfma_f32_16x16x32_bf16 a[120:123], v[16:19], v[32:35], a[120:123]
ds_read_b64_tr_b16 v[216:217], v242 offset:128
v_accvgpr_read_b32 v227, a235
s_add_u32 m0, s83, 0
v_mfma_f32_16x16x32_bf16 a[184:187], v[16:19], v[36:39], a[184:187]
buffer_load_dwordx4 v232, s[44:47], s80 offen lds
v_mfma_f32_16x16x32_bf16 a[64:67], v[16:19], v[40:43], a[64:67]
ds_read_b64_tr_b16 v[218:219], v242 offset:1152
v_accvgpr_read_b32 v228, a236
v_mfma_f32_16x16x32_bf16 a[0:3], v[16:19], v[44:47], a[0:3]
buffer_load_dwordx4 v[156:159], v247, s[52:55], s91 offen offset:1024 sc1
v_accvgpr_read_b32 v229, a237
v_mfma_f32_16x16x32_bf16 a[72:75], v[16:19], v[48:51], a[72:75]
ds_read_b64_tr_b16 v[220:221], v243 offset:128
v_accvgpr_read_b32 v230, a238
s_add_u32 m0, s83, 1024
v_mfma_f32_16x16x32_bf16 a[8:11], v[16:19], v[52:55], a[8:11]
buffer_load_dwordx4 v233, s[44:47], s80 offen lds
v_mfma_f32_16x16x32_bf16 a[80:83], v[16:19], v[56:59], a[80:83]
ds_read_b64_tr_b16 v[222:223], v243 offset:1152
v_accvgpr_read_b32 v231, a239
v_mfma_f32_16x16x32_bf16 a[16:19], v[16:19], v[60:63], a[16:19]
v_cvt_pk_bf16_f32 v248, v224, v225
v_cvt_pk_bf16_f32 v249, v226, v227
v_mfma_f32_16x16x32_bf16 a[124:127], v[20:23], v[32:35], a[124:127]
v_cvt_pk_bf16_f32 v250, v228, v229
v_cvt_pk_bf16_f32 v251, v230, v231
s_add_u32 m0, s83, 2176
v_mfma_f32_16x16x32_bf16 a[188:191], v[20:23], v[36:39], a[188:191]
buffer_load_dwordx4 v234, s[44:47], s80 offen lds
v_mfma_f32_16x16x32_bf16 a[68:71], v[20:23], v[40:43], a[68:71]
buffer_store_dwordx4 v[248:251], v255, s[48:51], s89 offen sc1
s_add_u32 s89, s89, 1024
v_mfma_f32_16x16x32_bf16 a[4:7], v[20:23], v[44:47], a[4:7]
buffer_load_dwordx4 v[160:163], v244, s[52:55], s91 offen offset:2048 sc1
ds_write_b128 v238, v[248:251] offset:1024
v_mfma_f32_16x16x32_bf16 a[76:79], v[20:23], v[48:51], a[76:79]
s_add_u32 m0, s83, 3200
v_mfma_f32_16x16x32_bf16 a[12:15], v[20:23], v[52:55], a[12:15]
buffer_load_dwordx4 v235, s[44:47], s80 offen lds
v_mfma_f32_16x16x32_bf16 a[84:87], v[20:23], v[56:59], a[84:87]
v_accvgpr_read_b32 v224, a208
v_accvgpr_read_b32 v225, a209
v_mfma_f32_16x16x32_bf16 a[20:23], v[20:23], v[60:63], a[20:23]
s_add_u32 s80, s80, s81
s_add_u32 s83, s83, 16896
v_mfma_f32_16x16x32_bf16 a[88:91], v[24:27], v[32:35], a[88:91]
v_accvgpr_read_b32 v226, a210
v_accvgpr_read_b32 v227, a211
v_mfma_f32_16x16x32_bf16 a[24:27], v[24:27], v[36:39], a[24:27]
s_mov_b32 s83, s90
v_accvgpr_read_b32 v228, a212
v_mfma_f32_16x16x32_bf16 a[240:243], v[24:27], v[40:43], a[240:243]
v_accvgpr_read_b32 v229, a213
v_accvgpr_read_b32 v230, a214
v_mfma_f32_16x16x32_bf16 a[32:35], v[24:27], v[44:47], a[32:35]
buffer_load_dwordx4 v[164:167], v245, s[52:55], s91 offen offset:2048 sc1
v_accvgpr_read_b32 v231, a215
v_mfma_f32_16x16x32_bf16 a[248:251], v[24:27], v[48:51], a[248:251]
v_cvt_pk_bf16_f32 v248, v224, v225
v_cvt_pk_bf16_f32 v249, v226, v227
v_mfma_f32_16x16x32_bf16 a[40:43], v[24:27], v[52:55], a[40:43]
v_cvt_pk_bf16_f32 v250, v228, v229
v_cvt_pk_bf16_f32 v251, v230, v231
v_mfma_f32_16x16x32_bf16 a[48:51], v[24:27], v[56:59], a[48:51]
buffer_store_dwordx4 v[248:251], v255, s[48:51], s89 offen sc1
s_add_u32 s89, s89, 1024
v_mfma_f32_16x16x32_bf16 a[56:59], v[24:27], v[60:63], a[56:59]
buffer_load_dwordx4 v[168:171], v246, s[52:55], s91 offen offset:2048 sc1
ds_write_b128 v238, v[248:251] offset:2048
v_mfma_f32_16x16x32_bf16 a[92:95], v[28:31], v[32:35], a[92:95]
v_mfma_f32_16x16x32_bf16 a[28:31], v[28:31], v[36:39], a[28:31]
v_mfma_f32_16x16x32_bf16 a[244:247], v[28:31], v[40:43], a[244:247]
v_mfma_f32_16x16x32_bf16 a[36:39], v[28:31], v[44:47], a[36:39]
buffer_load_dwordx4 v[172:175], v247, s[52:55], s91 offen offset:2048 sc1
v_mfma_f32_16x16x32_bf16 a[252:255], v[28:31], v[48:51], a[252:255]
v_mfma_f32_16x16x32_bf16 a[44:47], v[28:31], v[52:55], a[44:47]
v_mfma_f32_16x16x32_bf16 a[52:55], v[28:31], v[56:59], a[52:55]
v_mfma_f32_16x16x32_bf16 a[60:63], v[28:31], v[60:63], a[60:63]
; ---- Q tile: 256 rows x 128 columns, K = 1024, while K / V leave the accumulators (memory + .La5_LDS ring); behind it the first step on the diagonal
.La5_LQ_START:
.La5_LW1_5:
s_cbranch_execz .La5_LW1_9
s_waitcnt vmcnt(15) lgkmcnt(6)
v_mfma_f32_16x16x32_bf16 v[0:3], v[192:195], v[128:131], 0
v_accvgpr_read_b32 v224, a128
v_accvgpr_read_b32 v225, a129
v_mfma_f32_16x16x32_bf16 v[32:35], v[192:195], v[132:135], 0
buffer_load_dwordx4 v[176:179], v244, s[52:55], s91 offen offset:3072 sc1
v_mfma_f32_16x16x32_bf16 v[64:67], v[192:195], v[136:139], 0
v_accvgpr_read_b32 v226, a130
v_accvgpr_read_b32 v227, a131
v_mfma_f32_16x16x32_bf16 v[96:99], v[192:195], v[140:143], 0
ds_read_b64_tr_b16 v[192:193], v240 offset:8448
ds_read_b64_tr_b16 v[194:195], v240 offset:9472
v_mfma_f32_16x16x32_bf16 v[4:7], v[196:199], v[128:131], 0
v_accvgpr_read_b32 v228, a132
v_accvgpr_read_b32 v229, a133
v_mfma_f32_16x16x32_bf16 v[36:39], v[196:199], v[132:135], 0
buffer_load_dwordx4 v[180:183], v245, s[52:55], s91 offen offset:3072 sc1
v_mfma_f32_16x16x32_bf16 v[68:71], v[196:199], v[136:139], 0
s_waitcnt lgkmcnt(4)
v_accvgpr_read_b32 v230, a134
v_mfma_f32_16x16x32_bf16 v[100:103], v[196:199], v[140:143], 0
ds_read_b64_tr_b16 v[196:197], v241 offset:8448
ds_read_b64_tr_b16 v[198:199], v241 offset:9472
v_mfma_f32_16x16x32_bf16 v[8:11], v[200:203], v[128:131], 0
v_accvgpr_read_b32 v231, a135
v_cvt_pk_bf16_f32 v248, v224, v225
v_mfma_f32_16x16x32_bf16 v[40:43], v[200:203], v[132:135], 0
buffer_load_dwordx4 v[184:187], v246, s[52:55], s91 offen offset:3072 sc1
v_mfma_f32_16x16x32_bf16 v[72:75], v[200:203], v[136:139], 0
v_cvt_pk_bf16_f32 v249, v226, v227
v_cvt_pk_bf16_f32 v250, v228, v229
v_mfma_f32_16x16x32_bf16 v[104:107], v[200:203], v[140:143], 0
ds_read_b64_tr_b16 v[200:201], v242 offset:8448
ds_read_b64_tr_b16 v[202:203], v242 offset:9472
v_mfma_f32_16x16x32_bf16 v[12:15], v[204:207], v[128:131], 0
v_cvt_pk_bf16_f32 v251, v230, v231
s_mov_b32 s85, s95
v_mfma_f32_16x16x32_bf16 v[44:47], v[204:207], v[132:135], 0
buffer_load_dwordx4 v[188:191], v247, s[52:55], s91 offen offset:3072 sc1
v_mfma_f32_16x16x32_bf16 v[76:79], v[204:207], v[136:139], 0
s_add_u32 s91, s91, 4096
v_mfma_f32_16x16x32_bf16 v[108:111], v[204:207], v[140:143], 0
ds_read_b64_tr_b16 v[204:205], v243 offset:8448
ds_read_b64_tr_b16 v[206:207], v243 offset:9472
v_mfma_f32_16x16x32_bf16 v[16:19], v[208:211], v[128:131], 0
v_mfma_f32_16x16x32_bf16 v[48:51], v[208:211], v[132:135], 0
v_mfma_f32_16x16x32_bf16 v[80:83], v[208:211], v[136:139], 0
v_mfma_f32_16x16x32_bf16 v[112:115], v[208:211], v[140:143], 0
.La5_LW1_6:
s_cbranch_execz .La5_LW1_10
ds_read_b64_tr_b16 v[208:209], v240 offset:8576
ds_read_b64_tr_b16 v[210:211], v240 offset:9600
v_mfma_f32_16x16x32_bf16 v[20:23], v[212:215], v[128:131], 0
v_mfma_f32_16x16x32_bf16 v[52:55], v[212:215], v[132:135], 0
v_mfma_f32_16x16x32_bf16 v[84:87], v[212:215], v[136:139], 0
v_mfma_f32_16x16x32_bf16 v[116:119], v[212:215], v[140:143], 0
ds_read_b64_tr_b16 v[212:213], v241 offset:8576
ds_read_b64_tr_b16 v[214:215], v241 offset:9600
v_mfma_f32_16x16x32_bf16 v[24:27], v[216:219], v[128:131], 0
v_mfma_f32_16x16x32_bf16 v[56:59], v[216:219], v[132:135], 0
v_mfma_f32_16x16x32_bf16 v[88:91], v[216:219], v[136:139], 0
v_mfma_f32_16x16x32_bf16 v[120:123], v[216:219], v[140:143], 0
ds_read_b64_tr_b16 v[216:217], v242 offset:8576
ds_read_b64_tr_b16 v[218:219], v242 offset:9600
v_mfma_f32_16x16x32_bf16 v[28:31], v[220:223], v[128:131], 0
ds_write_b128 v238, v[248:251] offset:3072
v_mfma_f32_16x16x32_bf16 v[60:63], v[220:223], v[132:135], 0
buffer_store_dwordx4 v[248:251], v255, s[48:51], s89 offen offset:0 sc1
v_mfma_f32_16x16x32_bf16 v[92:95], v[220:223], v[136:139], 0
s_waitcnt vmcnt(14) lgkmcnt(3)
v_mfma_f32_16x16x32_bf16 v[124:127], v[220:223], v[140:143], 0
ds_read_b64_tr_b16 v[220:221], v243 offset:8576
ds_read_b64_tr_b16 v[222:223], v243 offset:9600
v_mfma_f32_16x16x32_bf16 v[0:3], v[192:195], v[144:147], v[0:3]
v_accvgpr_read_b32 v224, a216
v_accvgpr_read_b32 v225, a217
v_mfma_f32_16x16x32_bf16 v[32:35], v[192:195], v[148:151], v[32:35]
buffer_load_dwordx4 v[128:131], v244, s[52:55], s91 offen offset:0 sc1
v_mfma_f32_16x16x32_bf16 v[64:67], v[192:195], v[152:155], v[64:67]
v_accvgpr_read_b32 v226, a218
v_accvgpr_read_b32 v227, a219
v_mfma_f32_16x16x32_bf16 v[96:99], v[192:195], v[156:159], v[96:99]
v_accvgpr_read_b32 v228, a220
v_accvgpr_read_b32 v229, a221
v_mfma_f32_16x16x32_bf16 v[4:7], v[196:199], v[144:147], v[4:7]
s_waitcnt lgkmcnt(0)
s_barrier
v_mfma_f32_16x16x32_bf16 v[36:39], v[196:199], v[148:151], v[36:39]
buffer_load_dwordx4 v[132:135], v245, s[52:55], s91 offen offset:0 sc1
v_mfma_f32_16x16x32_bf16 v[68:71], v[196:199], v[152:155], v[68:71]
ds_read_b64_tr_b16 v[192:193], v240 offset:16896
ds_read_b64_tr_b16 v[194:195], v240 offset:17920
v_mfma_f32_16x16x32_bf16 v[100:103], v[196:199], v[156:159], v[100:103]
ds_read_b64_tr_b16 v[196:197], v241 offset:16896
ds_read_b64_tr_b16 v[198:199], v241 offset:17920
v_mfma_f32_16x16x32_bf16 v[8:11], v[200:203], v[144:147], v[8:11]
v_accvgpr_read_b32 v230, a222
v_accvgpr_read_b32 v231, a223
v_mfma_f32_16x16x32_bf16 v[40:43], v[200:203], v[148:151], v[40:43]
buffer_load_dwordx4 v[136:139], v246, s[52:55], s91 offen offset:0 sc1
v_mfma_f32_16x16x32_bf16 v[72:75], v[200:203], v[152:155], v[72:75]
v_cvt_pk_bf16_f32 v248, v224, v225
v_cvt_pk_bf16_f32 v249, v226, v227
v_mfma_f32_16x16x32_bf16 v[104:107], v[200:203], v[156:159], v[104:107]
ds_read_b64_tr_b16 v[200:201], v242 offset:16896
ds_read_b64_tr_b16 v[202:203], v242 offset:17920
v_mfma_f32_16x16x32_bf16 v[12:15], v[204:207], v[144:147], v[12:15]
s_add_u32 m0, s83, 0
v_cvt_pk_bf16_f32 v250, v228, v229
v_mfma_f32_16x16x32_bf16 v[44:47], v[204:207], v[148:151], v[44:47]
buffer_load_dwordx4 v232, s[44:47], s80 offen lds
v_mfma_f32_16x16x32_bf16 v[76:79], v[204:207], v[152:155], v[76:79]
v_cvt_pk_bf16_f32 v251, v230, v231
s_mov_b32 s89, 53248
v_mfma_f32_16x16x32_bf16 v[108:111], v[204:207], v[156:159], v[108:111]
ds_read_b64_tr_b16 v[204:205], v243 offset:16896
ds_read_b64_tr_b16 v[206:207], v243 offset:17920
v_mfma_f32_16x16x32_bf16 v[16:19], v[208:211], v[144:147], v[16:19]
s_and_b32 s97, s2, 7
s_mul_i32 s95, s97, 7
v_mfma_f32_16x16x32_bf16 v[48:51], v[208:211], v[148:151], v[48:51]
buffer_load_dwordx4 v[140:143], v247, s[52:55], s91 offen offset:0 sc1
v_mfma_f32_16x16x32_bf16 v[80:83], v[208:211], v[152:155], v[80:83]
s_lshr_b32 s96, s30, 7
s_add_u32 s95, s95, s96
v_mfma_f32_16x16x32_bf16 v[112:115], v[208:211], v[156:159], v[112:115]
ds_read_b64_tr_b16 v[208:209], v240 offset:17024
ds_read_b64_tr_b16 v[210:211], v240 offset:18048
v_mfma_f32_16x16x32_bf16 v[20:23], v[212:215], v[144:147], v[20:23]
s_add_u32 m0, s83, 1024
s_sub_u32 s95, 3, s95
v_mfma_f32_16x16x32_bf16 v[52:55], v[212:215], v[148:151], v[52:55]
buffer_load_dwordx4 v233, s[44:47], s80 offen lds
v_mfma_f32_16x16x32_bf16 v[84:87], v[212:215], v[152:155], v[84:87]
s_and_b32 s95, s95, 7
s_min_u32 s95, s95, 6
v_mfma_f32_16x16x32_bf16 v[116:119], v[212:215], v[156:159], v[116:119]
ds_read_b64_tr_b16 v[212:213], v241 offset:17024
ds_read_b64_tr_b16 v[214:215], v241 offset:18048
v_mfma_f32_16x16x32_bf16 v[24:27], v[216:219], v[144:147], v[24:27]
s_lshl_b32 s95, s95, 1
s_lshr_b32 s92, s2, 6
v_mfma_f32_16x16x32_bf16 v[56:59], v[216:219], v[148:151], v[56:59]
s_mul_i32 s93, s92, 3
s_add_u32 s93, s93, s74
v_mfma_f32_16x16x32_bf16 v[88:91], v[216:219], v[152:155], v[88:91]
s_cmp_ge_u32 s93, s95
s_cselect_b32 s96, 2, 0
v_mfma_f32_16x16x32_bf16 v[120:123], v[216:219], v[156:159], v[120:123]
ds_read_b64_tr_b16 v[216:217], v242 offset:17024
ds_read_b64_tr_b16 v[218:219], v242 offset:18048
v_mfma_f32_16x16x32_bf16 v[28:31], v[220:223], v[144:147], v[28:31]
ds_write_b128 v238, v[248:251] offset:4096
v_mfma_f32_16x16x32_bf16 v[60:63], v[220:223], v[148:151], v[60:63]
buffer_store_dwordx4 v[248:251], v255, s[48:51], s89 offen offset:0 sc1
v_mfma_f32_16x16x32_bf16 v[92:95], v[220:223], v[152:155], v[92:95]
s_waitcnt vmcnt(12) lgkmcnt(3)
s_add_u32 s93, s93, s96
v_mfma_f32_16x16x32_bf16 v[124:127], v[220:223], v[156:159], v[124:127]
ds_read_b64_tr_b16 v[220:221], v243 offset:17024
ds_read_b64_tr_b16 v[222:223], v243 offset:18048
v_mfma_f32_16x16x32_bf16 v[0:3], v[192:195], v[160:163], v[0:3]
v_accvgpr_read_b32 v224, a136
v_accvgpr_read_b32 v225, a137
v_mfma_f32_16x16x32_bf16 v[32:35], v[192:195], v[164:167], v[32:35]
buffer_load_dwordx4 v[144:147], v244, s[52:55], s91 offen offset:1024 sc1
v_mfma_f32_16x16x32_bf16 v[64:67], v[192:195], v[168:171], v[64:67]
v_accvgpr_read_b32 v226, a138
v_accvgpr_read_b32 v227, a139
v_mfma_f32_16x16x32_bf16 v[96:99], v[192:195], v[172:175], v[96:99]
ds_read_b64_tr_b16 v[192:193], v240 offset:25344
ds_read_b64_tr_b16 v[194:195], v240 offset:26368
v_mfma_f32_16x16x32_bf16 v[4:7], v[196:199], v[160:163], v[4:7]
v_accvgpr_read_b32 v228, a140
v_accvgpr_read_b32 v229, a141
v_mfma_f32_16x16x32_bf16 v[36:39], v[196:199], v[164:167], v[36:39]
buffer_load_dwordx4 v[148:151], v245, s[52:55], s91 offen offset:1024 sc1
v_mfma_f32_16x16x32_bf16 v[68:71], v[196:199], v[168:171], v[68:71]
s_waitcnt lgkmcnt(2)
v_accvgpr_read_b32 v230, a142
v_mfma_f32_16x16x32_bf16 v[100:103], v[196:199], v[172:175], v[100:103]
ds_read_b64_tr_b16 v[196:197], v241 offset:25344
ds_read_b64_tr_b16 v[198:199], v241 offset:26368
v_mfma_f32_16x16x32_bf16 v[8:11], v[200:203], v[160:163], v[8:11]
s_add_u32 m0, s83, 2176
v_accvgpr_read_b32 v231, a143
v_mfma_f32_16x16x32_bf16 v[40:43], v[200:203], v[164:167], v[40:43]
buffer_load_dwordx4 v234, s[44:47], s80 offen lds
v_mfma_f32_16x16x32_bf16 v[72:75], v[200:203], v[168:171], v[72:75]
v_cvt_pk_bf16_f32 v248, v224, v225
v_cvt_pk_bf16_f32 v249, v226, v227
v_mfma_f32_16x16x32_bf16 v[104:107], v[200:203], v[172:175], v[104:107]
ds_read_b64_tr_b16 v[200:201], v242 offset:25344
ds_read_b64_tr_b16 v[202:203], v242 offset:26368
v_mfma_f32_16x16x32_bf16 v[12:15], v[204:207], v[160:163], v[12:15]
v_cvt_pk_bf16_f32 v250, v228, v229
v_cvt_pk_bf16_f32 v251, v230, v231
v_mfma_f32_16x16x32_bf16 v[44:47], v[204:207], v[164:167], v[44:47]
buffer_load_dwordx4 v[152:155], v246, s[52:55], s91 offen offset:1024 sc1
v_mfma_f32_16x16x32_bf16 v[76:79], v[204:207], v[168:171], v[76:79]
s_add_u32 s95, s95, s92
v_mfma_f32_16x16x32_bf16 v[108:111], v[204:207], v[172:175], v[108:111]
ds_read_b64_tr_b16 v[204:205], v243 offset:25344
ds_read_b64_tr_b16 v[206:207], v243 offset:26368
v_mfma_f32_16x16x32_bf16 v[16:19], v[208:211], v[160:163], v[16:19]
s_add_u32 m0, s83, 3200
v_mfma_f32_16x16x32_bf16 v[48:51], v[208:211], v[164:167], v[48:51]
buffer_load_dwordx4 v235, s[44:47], s80 offen lds
v_mfma_f32_16x16x32_bf16 v[80:83], v[208:211], v[168:171], v[80:83]
s_add_u32 s80, s80, 524288
v_mfma_f32_16x16x32_bf16 v[112:115], v[208:211], v[172:175], v[112:115]
ds_read_b64_tr_b16 v[208:209], v240 offset:25472
ds_read_b64_tr_b16 v[210:211], v240 offset:26496
v_mfma_f32_16x16x32_bf16 v[20:23], v[212:215], v[160:163], v[20:23]
s_cmp_lt_u32 s92, 2
s_cselect_b32 s94, s75, 0
v_mfma_f32_16x16x32_bf16 v[52:55], v[212:215], v[164:167], v[52:55]
buffer_load_dwordx4 v[156:159], v247, s[52:55], s91 offen offset:1024 sc1
v_mfma_f32_16x16x32_bf16 v[84:87], v[212:215], v[168:171], v[84:87]
s_cmp_eq_u32 s74, 3
s_cselect_b32 s92, s95, s93
s_cselect_b32 s94, s94, s75
v_mfma_f32_16x16x32_bf16 v[116:119], v[212:215], v[172:175], v[116:119]
ds_read_b64_tr_b16 v[212:213], v241 offset:25472
ds_read_b64_tr_b16 v[214:215], v241 offset:26496
v_mfma_f32_16x16x32_bf16 v[24:27], v[216:219], v[160:163], v[24:27]
s_mul_i32 s93, s97, 14
s_add_u32 s92, s92, s93
v_mfma_f32_16x16x32_bf16 v[56:59], v[216:219], v[164:167], v[56:59]
s_mul_i32 s96, s92, s94
s_mov_b32 s100, s96
v_mfma_f32_16x16x32_bf16 v[88:91], v[216:219], v[168:171], v[88:91]
s_lshr_b32 s92, s2, 3
s_and_b32 s93, s2, 7
v_mfma_f32_16x16x32_bf16 v[120:123], v[216:219], v[172:175], v[120:123]
ds_read_b64_tr_b16 v[216:217], v242 offset:25472
ds_read_b64_tr_b16 v[218:219], v242 offset:26496
v_mfma_f32_16x16x32_bf16 v[28:31], v[220:223], v[160:163], v[28:31]
ds_write_b128 v238, v[248:251] offset:5120
v_mfma_f32_16x16x32_bf16 v[60:63], v[220:223], v[164:167], v[60:63]
buffer_store_dwordx4 v[248:251], v255, s[48:51], s89 offen offset:1024 sc1
v_mfma_f32_16x16x32_bf16 v[92:95], v[220:223], v[168:171], v[92:95]
s_waitcnt vmcnt(15) lgkmcnt(3)
s_lshl_b32 s95, s92, 16
v_mfma_f32_16x16x32_bf16 v[124:127], v[220:223], v[172:175], v[124:127]
ds_read_b64_tr_b16 v[220:221], v243 offset:25472
ds_read_b64_tr_b16 v[222:223], v243 offset:26496
v_mfma_f32_16x16x32_bf16 v[0:3], v[192:195], v[176:179], v[0:3]
v_accvgpr_read_b32 v224, a192
v_accvgpr_read_b32 v225, a193
v_mfma_f32_16x16x32_bf16 v[32:35], v[192:195], v[180:183], v[32:35]
buffer_load_dwordx4 v[160:163], v244, s[52:55], s91 offen offset:2048 sc1
v_mfma_f32_16x16x32_bf16 v[64:67], v[192:195], v[184:187], v[64:67]
v_accvgpr_read_b32 v226, a194
v_accvgpr_read_b32 v227, a195
v_mfma_f32_16x16x32_bf16 v[96:99], v[192:195], v[188:191], v[96:99]
v_accvgpr_read_b32 v228, a196
v_accvgpr_read_b32 v229, a197
v_mfma_f32_16x16x32_bf16 v[4:7], v[196:199], v[176:179], v[4:7]
s_waitcnt lgkmcnt(0)
s_barrier
v_mfma_f32_16x16x32_bf16 v[36:39], v[196:199], v[180:183], v[36:39]
buffer_load_dwordx4 v[164:167], v245, s[52:55], s91 offen offset:2048 sc1
v_mfma_f32_16x16x32_bf16 v[68:71], v[196:199], v[184:187], v[68:71]
ds_read_b64_tr_b16 v[192:193], v240 offset:33792
ds_read_b64_tr_b16 v[194:195], v240 offset:34816
v_mfma_f32_16x16x32_bf16 v[100:103], v[196:199], v[188:191], v[100:103]
ds_read_b64_tr_b16 v[196:197], v241 offset:33792
ds_read_b64_tr_b16 v[198:199], v241 offset:34816
v_mfma_f32_16x16x32_bf16 v[8:11], v[200:203], v[176:179], v[8:11]
v_accvgpr_read_b32 v230, a198
v_accvgpr_read_b32 v231, a199
v_mfma_f32_16x16x32_bf16 v[40:43], v[200:203], v[180:183], v[40:43]
buffer_load_dwordx4 v[168:171], v246, s[52:55], s91 offen offset:2048 sc1
v_mfma_f32_16x16x32_bf16 v[72:75], v[200:203], v[184:187], v[72:75]
v_cvt_pk_bf16_f32 v248, v224, v225
v_cvt_pk_bf16_f32 v249, v226, v227
v_mfma_f32_16x16x32_bf16 v[104:107], v[200:203], v[188:191], v[104:107]
ds_read_b64_tr_b16 v[200:201], v242 offset:33792
ds_read_b64_tr_b16 v[202:203], v242 offset:34816
v_mfma_f32_16x16x32_bf16 v[12:15], v[204:207], v[176:179], v[12:15]
s_add_u32 m0, s83, 16896
v_cvt_pk_bf16_f32 v250, v228, v229
v_mfma_f32_16x16x32_bf16 v[44:47], v[204:207], v[180:183], v[44:47]
buffer_load_dwordx4 v232, s[44:47], s80 offen lds
v_mfma_f32_16x16x32_bf16 v[76:79], v[204:207], v[184:187], v[76:79]
v_cvt_pk_bf16_f32 v251, v230, v231
s_lshr_b32 s97, s93, 1
v_mfma_f32_16x16x32_bf16 v[108:111], v[204:207], v[188:191], v[108:111]
ds_read_b64_tr_b16 v[204:205], v243 offset:33792
ds_read_b64_tr_b16 v[206:207], v243 offset:34816
v_mfma_f32_16x16x32_bf16 v[16:19], v[208:211], v[176:179], v[16:19]
s_lshl_b32 s97, s97, 21
s_add_u32 s95, s95, s97
v_mfma_f32_16x16x32_bf16 v[48:51], v[208:211], v[180:183], v[48:51]
buffer_load_dwordx4 v[172:175], v247, s[52:55], s91 offen offset:2048 sc1
v_mfma_f32_16x16x32_bf16 v[80:83], v[208:211], v[184:187], v[80:83]
s_lshr_b32 s97, s93, 8
s_xor_b32 s97, s97, s93
v_mfma_f32_16x16x32_bf16 v[112:115], v[208:211], v[188:191], v[112:115]
ds_read_b64_tr_b16 v[208:209], v240 offset:33920
ds_read_b64_tr_b16 v[210:211], v240 offset:34944
v_mfma_f32_16x16x32_bf16 v[20:23], v[212:215], v[176:179], v[20:23]
s_add_u32 m0, s83, 17920
s_and_b32 s97, s97, 1
v_mfma_f32_16x16x32_bf16 v[52:55], v[212:215], v[180:183], v[52:55]
buffer_load_dwordx4 v233, s[44:47], s80 offen lds
v_mfma_f32_16x16x32_bf16 v[84:87], v[212:215], v[184:187], v[84:87]
s_lshl_b32 s97, s97, 10
s_add_u32 s95, s95, s97
v_mfma_f32_16x16x32_bf16 v[116:119], v[212:215], v[188:191], v[116:119]
ds_read_b64_tr_b16 v[212:213], v241 offset:33920
ds_read_b64_tr_b16 v[214:215], v241 offset:34944
v_mfma_f32_16x16x32_bf16 v[24:27], v[216:219], v[176:179], v[24:27]
s_mul_i32 s97, s92, 229376
s_mul_i32 s38, s93, 896
v_mfma_f32_16x16x32_bf16 v[56:59], v[216:219], v[180:183], v[56:59]
s_add_u32 s97, s97, s38
v_mfma_f32_16x16x32_bf16 v[88:91], v[216:219], v[184:187], v[88:91]
s_cmp_eq_u32 s3, 0
s_cselect_b32 s95, s95, s97
s_cselect_b64 s[36:37], s[28:29], s[30:31]
s_cselect_b32 s21, 2, 7
v_mfma_f32_16x16x32_bf16 v[120:123], v[216:219], v[188:191], v[120:123]
ds_read_b64_tr_b16 v[216:217], v242 offset:33920
ds_read_b64_tr_b16 v[218:219], v242 offset:34944
v_mfma_f32_16x16x32_bf16 v[28:31], v[220:223], v[176:179], v[28:31]
ds_write_b128 v238, v[248:251] offset:6144
v_mfma_f32_16x16x32_bf16 v[60:63], v[220:223], v[180:183], v[60:63]
buffer_store_dwordx4 v[248:251], v255, s[48:51], s89 offen offset:2048 sc1
v_mfma_f32_16x16x32_bf16 v[92:95], v[220:223], v[184:187], v[92:95]
s_waitcnt vmcnt(16) lgkmcnt(3)
v_mfma_f32_16x16x32_bf16 v[124:127], v[220:223], v[188:191], v[124:127]
ds_read_b64_tr_b16 v[220:221], v243 offset:33920
ds_read_b64_tr_b16 v[222:223], v243 offset:34944
v_mfma_f32_16x16x32_bf16 v[0:3], v[192:195], v[128:131], v[0:3]
v_accvgpr_read_b32 v224, a144
v_accvgpr_read_b32 v225, a145
v_mfma_f32_16x16x32_bf16 v[32:35], v[192:195], v[132:135], v[32:35]
buffer_load_dwordx4 v[176:179], v244, s[52:55], s91 offen offset:3072 sc1
v_mfma_f32_16x16x32_bf16 v[64:67], v[192:195], v[136:139], v[64:67]
v_accvgpr_read_b32 v226, a146
v_accvgpr_read_b32 v227, a147
v_mfma_f32_16x16x32_bf16 v[96:99], v[192:195], v[140:143], v[96:99]
ds_read_b64_tr_b16 v[192:193], v240 offset:42240
ds_read_b64_tr_b16 v[194:195], v240 offset:43264
v_mfma_f32_16x16x32_bf16 v[4:7], v[196:199], v[128:131], v[4:7]
v_accvgpr_read_b32 v228, a148
v_accvgpr_read_b32 v229, a149
v_mfma_f32_16x16x32_bf16 v[36:39], v[196:199], v[132:135], v[36:39]
buffer_load_dwordx4 v[180:183], v245, s[52:55], s91 offen offset:3072 sc1
v_mfma_f32_16x16x32_bf16 v[68:71], v[196:199], v[136:139], v[68:71]
s_waitcnt lgkmcnt(2)
v_accvgpr_read_b32 v230, a150
v_mfma_f32_16x16x32_bf16 v[100:103], v[196:199], v[140:143], v[100:103]
ds_read_b64_tr_b16 v[196:197], v241 offset:42240
ds_read_b64_tr_b16 v[198:199], v241 offset:43264
v_mfma_f32_16x16x32_bf16 v[8:11], v[200:203], v[128:131], v[8:11]
s_add_u32 m0, s83, 19072
v_accvgpr_read_b32 v231, a151
v_mfma_f32_16x16x32_bf16 v[40:43], v[200:203], v[132:135], v[40:43]
buffer_load_dwordx4 v234, s[44:47], s80 offen lds
v_mfma_f32_16x16x32_bf16 v[72:75], v[200:203], v[136:139], v[72:75]
v_cvt_pk_bf16_f32 v248, v224, v225
v_cvt_pk_bf16_f32 v249, v226, v227
v_mfma_f32_16x16x32_bf16 v[104:107], v[200:203], v[140:143], v[104:107]
ds_read_b64_tr_b16 v[200:201], v242 offset:42240
ds_read_b64_tr_b16 v[202:203], v242 offset:43264
v_mfma_f32_16x16x32_bf16 v[12:15], v[204:207], v[128:131], v[12:15]
v_cvt_pk_bf16_f32 v250, v228, v229
v_cvt_pk_bf16_f32 v251, v230, v231
v_mfma_f32_16x16x32_bf16 v[44:47], v[204:207], v[132:135], v[44:47]
buffer_load_dwordx4 v[184:187], v246, s[52:55], s91 offen offset:3072 sc1
v_mfma_f32_16x16x32_bf16 v[76:79], v[204:207], v[136:139], v[76:79]
s_cmp_eq_u32 s3, 2
s_cselect_b64 s[36:37], s[32:33], s[36:37]
v_mfma_f32_16x16x32_bf16 v[108:111], v[204:207], v[140:143], v[108:111]
ds_read_b64_tr_b16 v[204:205], v243 offset:42240
ds_read_b64_tr_b16 v[206:207], v243 offset:43264
v_mfma_f32_16x16x32_bf16 v[16:19], v[208:211], v[128:131], v[16:19]
s_add_u32 m0, s83, 20096
s_lshl_b32 s93, s93, 5
v_mfma_f32_16x16x32_bf16 v[48:51], v[208:211], v[132:135], v[48:51]
buffer_load_dwordx4 v235, s[44:47], s80 offen lds
v_mfma_f32_16x16x32_bf16 v[80:83], v[208:211], v[136:139], v[80:83]
s_add_u32 s80, s80, 524288
s_add_u32 s92, s92, s93
v_mfma_f32_16x16x32_bf16 v[112:115], v[208:211], v[140:143], v[112:115]
ds_read_b64_tr_b16 v[208:209], v240 offset:42368
ds_read_b64_tr_b16 v[210:211], v240 offset:43392
v_mfma_f32_16x16x32_bf16 v[20:23], v[212:215], v[128:131], v[20:23]
s_mul_i32 s97, s92, 28672
v_mfma_f32_16x16x32_bf16 v[52:55], v[212:215], v[132:135], v[52:55]
buffer_load_dwordx4 v[188:191], v247, s[52:55], s91 offen offset:3072 sc1
v_mfma_f32_16x16x32_bf16 v[84:87], v[212:215], v[136:139], v[84:87]
s_add_u32 s91, s91, 4096
v_mfma_f32_16x16x32_bf16 v[116:119], v[212:215], v[140:143], v[116:119]
ds_read_b64_tr_b16 v[212:213], v241 offset:42368
ds_read_b64_tr_b16 v[214:215], v241 offset:43392
v_mfma_f32_16x16x32_bf16 v[24:27], v[216:219], v[128:131], v[24:27]
s_cmp_eq_u32 s3, 3
s_cselect_b32 s95, s97, s95
s_cselect_b64 s[36:37], s[34:35], s[36:37]
s_cselect_b32 s21, 1, s21
s_cselect_b32 s97, 27, 31
v_mfma_f32_16x16x32_bf16 v[56:59], v[216:219], v[132:135], v[56:59]
s_lshl_b32 s21, s21, 10
s_mul_i32 s97, s97, s21
v_mfma_f32_16x16x32_bf16 v[88:91], v[216:219], v[136:139], v[88:91]
s_add_u32 s97, s97, 896
v_mfma_f32_16x16x32_bf16 v[120:123], v[216:219], v[140:143], v[120:123]
ds_read_b64_tr_b16 v[216:217], v242 offset:42368
ds_read_b64_tr_b16 v[218:219], v242 offset:43392
v_mfma_f32_16x16x32_bf16 v[28:31], v[220:223], v[128:131], v[28:31]
ds_write_b128 v238, v[248:251] offset:7168
v_mfma_f32_16x16x32_bf16 v[60:63], v[220:223], v[132:135], v[60:63]
buffer_store_dwordx4 v[248:251], v255, s[48:51], s89 offen offset:3072 sc1
v_mfma_f32_16x16x32_bf16 v[92:95], v[220:223], v[136:139], v[92:95]
s_waitcnt vmcnt(15) lgkmcnt(3)
v_mfma_f32_16x16x32_bf16 v[124:127], v[220:223], v[140:143], v[124:127]
ds_read_b64_tr_b16 v[220:221], v243 offset:42368
ds_read_b64_tr_b16 v[222:223], v243 offset:43392
v_mfma_f32_16x16x32_bf16 v[0:3], v[192:195], v[144:147], v[0:3]
v_accvgpr_read_b32 v224, a200
v_accvgpr_read_b32 v225, a201
v_mfma_f32_16x16x32_bf16 v[32:35], v[192:195], v[148:151], v[32:35]
buffer_load_dwordx4 v[128:131], v244, s[52:55], s91 offen offset:0 sc1
v_mfma_f32_16x16x32_bf16 v[64:67], v[192:195], v[152:155], v[64:67]
v_accvgpr_read_b32 v226, a202
v_accvgpr_read_b32 v227, a203
v_mfma_f32_16x16x32_bf16 v[96:99], v[192:195], v[156:159], v[96:99]
v_accvgpr_read_b32 v228, a204
v_accvgpr_read_b32 v229, a205
v_mfma_f32_16x16x32_bf16 v[4:7], v[196:199], v[144:147], v[4:7]
s_waitcnt lgkmcnt(0)
s_barrier
v_mfma_f32_16x16x32_bf16 v[36:39], v[196:199], v[148:151], v[36:39]
buffer_load_dwordx4 v[132:135], v245, s[52:55], s91 offen offset:0 sc1
v_mfma_f32_16x16x32_bf16 v[68:71], v[196:199], v[152:155], v[68:71]
ds_read_b64_tr_b16 v[192:193], v240 offset:50688
ds_read_b64_tr_b16 v[194:195], v240 offset:51712
v_mfma_f32_16x16x32_bf16 v[100:103], v[196:199], v[156:159], v[100:103]
ds_read_b64_tr_b16 v[196:197], v241 offset:50688
ds_read_b64_tr_b16 v[198:199], v241 offset:51712
v_mfma_f32_16x16x32_bf16 v[8:11], v[200:203], v[144:147], v[8:11]
v_accvgpr_read_b32 v230, a206
v_accvgpr_read_b32 v231, a207
v_mfma_f32_16x16x32_bf16 v[40:43], v[200:203], v[148:151], v[40:43]
buffer_load_dwordx4 v[136:139], v246, s[52:55], s91 offen offset:0 sc1
v_mfma_f32_16x16x32_bf16 v[72:75], v[200:203], v[152:155], v[72:75]
v_cvt_pk_bf16_f32 v248, v224, v225
v_cvt_pk_bf16_f32 v249, v226, v227
v_mfma_f32_16x16x32_bf16 v[104:107], v[200:203], v[156:159], v[104:107]
ds_read_b64_tr_b16 v[200:201], v242 offset:50688
ds_read_b64_tr_b16 v[202:203], v242 offset:51712
v_mfma_f32_16x16x32_bf16 v[12:15], v[204:207], v[144:147], v[12:15]
s_add_u32 m0, s83, 33792
v_cvt_pk_bf16_f32 v250, v228, v229
v_mfma_f32_16x16x32_bf16 v[44:47], v[204:207], v[148:151], v[44:47]
buffer_load_dwordx4 v232, s[44:47], s80 offen lds
v_mfma_f32_16x16x32_bf16 v[76:79], v[204:207], v[152:155], v[76:79]
v_cvt_pk_bf16_f32 v251, v230, v231
s_mov_b32 s89, 32768
v_mfma_f32_16x16x32_bf16 v[108:111], v[204:207], v[156:159], v[108:111]
ds_read_b64_tr_b16 v[204:205], v243 offset:50688
ds_read_b64_tr_b16 v[206:207], v243 offset:51712
v_mfma_f32_16x16x32_bf16 v[16:19], v[208:211], v[144:147], v[16:19]
s_add_u32 s36, s36, s95
s_addc_u32 s37, s37, 0
v_mfma_f32_16x16x32_bf16 v[48:51], v[208:211], v[148:151], v[48:51]
buffer_load_dwordx4 v[140:143], v247, s[52:55], s91 offen offset:0 sc1
v_mfma_f32_16x16x32_bf16 v[80:83], v[208:211], v[152:155], v[80:83]
s_and_b32 s37, s37, 0xffff
s_mov_b32 s38, s97
v_mfma_f32_16x16x32_bf16 v[112:115], v[208:211], v[156:159], v[112:115]
ds_read_b64_tr_b16 v[208:209], v240 offset:50816
ds_read_b64_tr_b16 v[210:211], v240 offset:51840
v_mfma_f32_16x16x32_bf16 v[20:23], v[212:215], v[144:147], v[20:23]
s_add_u32 m0, s83, 34816
s_mov_b32 s39, 0x20000
v_mfma_f32_16x16x32_bf16 v[52:55], v[212:215], v[148:151], v[52:55]
buffer_load_dwordx4 v233, s[44:47], s80 offen lds
v_mfma_f32_16x16x32_bf16 v[84:87], v[212:215], v[152:155], v[84:87]
s_lshl_b32 s93, s96, 6
s_mul_i32 s95, s94, 7332928
v_mfma_f32_16x16x32_bf16 v[116:119], v[212:215], v[156:159], v[116:119]
ds_read_b64_tr_b16 v[212:213], v241 offset:50816
ds_read_b64_tr_b16 v[214:215], v241 offset:51840
v_mfma_f32_16x16x32_bf16 v[24:27], v[216:219], v[144:147], v[24:27]
s_cmp_eq_u32 s77, 0
s_cselect_b64 s[40:41], s[30:31], s[32:33]
v_mfma_f32_16x16x32_bf16 v[56:59], v[216:219], v[148:151], v[56:59]
s_add_u32 s40, s40, s93
s_addc_u32 s41, s41, 0
v_mfma_f32_16x16x32_bf16 v[88:91], v[216:219], v[152:155], v[88:91]
s_and_b32 s41, s41, 0xffff
s_mov_b32 s42, s95
v_mfma_f32_16x16x32_bf16 v[120:123], v[216:219], v[156:159], v[120:123]
ds_read_b64_tr_b16 v[216:217], v242 offset:50816
ds_read_b64_tr_b16 v[218:219], v242 offset:51840
v_mfma_f32_16x16x32_bf16 v[28:31], v[220:223], v[144:147], v[28:31]
ds_write_b128 v238, v[248:251] offset:32768
v_mfma_f32_16x16x32_bf16 v[60:63], v[220:223], v[148:151], v[60:63]
buffer_store_dwordx4 v[248:251], v255, s[48:51], s89 offen offset:0 sc1
v_mfma_f32_16x16x32_bf16 v[92:95], v[220:223], v[152:155], v[92:95]
s_waitcnt vmcnt(16) lgkmcnt(3)
s_mov_b32 s43, 0x20000
v_mfma_f32_16x16x32_bf16 v[124:127], v[220:223], v[156:159], v[124:127]
ds_read_b64_tr_b16 v[220:221], v243 offset:50816
ds_read_b64_tr_b16 v[222:223], v243 offset:51840
v_mfma_f32_16x16x32_bf16 v[0:3], v[192:195], v[160:163], v[0:3]
v_accvgpr_read_b32 v224, a152
v_accvgpr_read_b32 v225, a153
v_mfma_f32_16x16x32_bf16 v[32:35], v[192:195], v[164:167], v[32:35]
buffer_load_dwordx4 v[144:147], v244, s[52:55], s91 offen offset:1024 sc1
v_mfma_f32_16x16x32_bf16 v[64:67], v[192:195], v[168:171], v[64:67]
v_accvgpr_read_b32 v226, a154
v_accvgpr_read_b32 v227, a155
v_mfma_f32_16x16x32_bf16 v[96:99], v[192:195], v[172:175], v[96:99]
ds_read_b64_tr_b16 v[192:193], v240 offset:59136
ds_read_b64_tr_b16 v[194:195], v240 offset:60160
v_mfma_f32_16x16x32_bf16 v[4:7], v[196:199], v[160:163], v[4:7]
v_accvgpr_read_b32 v228, a156
v_accvgpr_read_b32 v229, a157
v_mfma_f32_16x16x32_bf16 v[36:39], v[196:199], v[164:167], v[36:39]
buffer_load_dwordx4 v[148:151], v245, s[52:55], s91 offen offset:1024 sc1
v_mfma_f32_16x16x32_bf16 v[68:71], v[196:199], v[168:171], v[68:71]
s_waitcnt lgkmcnt(2)
v_accvgpr_read_b32 v230, a158
v_mfma_f32_16x16x32_bf16 v[100:103], v[196:199], v[172:175], v[100:103]
ds_read_b64_tr_b16 v[196:197], v241 offset:59136
ds_read_b64_tr_b16 v[198:199], v241 offset:60160
v_mfma_f32_16x16x32_bf16 v[8:11], v[200:203], v[160:163], v[8:11]
s_add_u32 m0, s83, 35968
v_accvgpr_read_b32 v231, a159
v_mfma_f32_16x16x32_bf16 v[40:43], v[200:203], v[164:167], v[40:43]
buffer_load_dwordx4 v234, s[44:47], s80 offen lds
v_mfma_f32_16x16x32_bf16 v[72:75], v[200:203], v[168:171], v[72:75]
v_cvt_pk_bf16_f32 v248, v224, v225
v_cvt_pk_bf16_f32 v249, v226, v227
v_mfma_f32_16x16x32_bf16 v[104:107], v[200:203], v[172:175], v[104:107]
ds_read_b64_tr_b16 v[200:201], v242 offset:59136
ds_read_b64_tr_b16 v[202:203], v242 offset:60160
v_mfma_f32_16x16x32_bf16 v[12:15], v[204:207], v[160:163], v[12:15]
v_cvt_pk_bf16_f32 v250, v228, v229
v_cvt_pk_bf16_f32 v251, v230, v231
v_mfma_f32_16x16x32_bf16 v[44:47], v[204:207], v[164:167], v[44:47]
buffer_load_dwordx4 v[152:155], v246, s[52:55], s91 offen offset:1024 sc1
v_mfma_f32_16x16x32_bf16 v[76:79], v[204:207], v[168:171], v[76:79]
s_lshr_b32 s92, s72, 1
s_and_b32 s92, s92, 0
v_mfma_f32_16x16x32_bf16 v[108:111], v[204:207], v[172:175], v[108:111]
ds_read_b64_tr_b16 v[204:205], v243 offset:59136
ds_read_b64_tr_b16 v[206:207], v243 offset:60160
v_mfma_f32_16x16x32_bf16 v[16:19], v[208:211], v[160:163], v[16:19]
s_add_u32 m0, s83, 36992
s_xor_b32 s92, s92, s72
v_mfma_f32_16x16x32_bf16 v[48:51], v[208:211], v[164:167], v[48:51]
buffer_load_dwordx4 v235, s[44:47], s80 offen lds
v_mfma_f32_16x16x32_bf16 v[80:83], v[208:211], v[168:171], v[80:83]
s_add_u32 s80, s80, 524288
s_xor_b32 s92, s92, 1
v_mfma_f32_16x16x32_bf16 v[112:115], v[208:211], v[172:175], v[112:115]
ds_read_b64_tr_b16 v[208:209], v240 offset:59264
ds_read_b64_tr_b16 v[210:211], v240 offset:60288
v_mfma_f32_16x16x32_bf16 v[20:23], v[212:215], v[160:163], v[20:23]
s_and_b32 s101, s92, 7
s_lshl_b32 s92, s101, 8
v_mfma_f32_16x16x32_bf16 v[52:55], v[212:215], v[164:167], v[52:55]
buffer_load_dwordx4 v[156:159], v247, s[52:55], s91 offen offset:1024 sc1
v_mfma_f32_16x16x32_bf16 v[84:87], v[212:215], v[168:171], v[84:87]
s_sub_u32 s93, 8388608, s92
v_mfma_f32_16x16x32_bf16 v[116:119], v[212:215], v[172:175], v[116:119]
ds_read_b64_tr_b16 v[212:213], v241 offset:59264
ds_read_b64_tr_b16 v[214:215], v241 offset:60288
v_mfma_f32_16x16x32_bf16 v[24:27], v[216:219], v[160:163], v[24:27]
s_add_u32 s56, s28, s92
s_addc_u32 s57, s29, 0
v_mfma_f32_16x16x32_bf16 v[56:59], v[216:219], v[164:167], v[56:59]
s_and_b32 s57, s57, 0xffff
s_mov_b32 s58, s93
v_mfma_f32_16x16x32_bf16 v[88:91], v[216:219], v[168:171], v[88:91]
s_mov_b32 s59, 0x20000
s_lshl_b32 s93, s96, 16
v_mfma_f32_16x16x32_bf16 v[120:123], v[216:219], v[172:175], v[120:123]
ds_read_b64_tr_b16 v[216:217], v242 offset:59264
ds_read_b64_tr_b16 v[218:219], v242 offset:60288
v_mfma_f32_16x16x32_bf16 v[28:31], v[220:223], v[160:163], v[28:31]
ds_write_b128 v238, v[248:251] offset:33792
v_mfma_f32_16x16x32_bf16 v[60:63], v[220:223], v[164:167], v[60:63]
buffer_store_dwordx4 v[248:251], v255, s[48:51], s89 offen offset:1024 sc1
v_mfma_f32_16x16x32_bf16 v[92:95], v[220:223], v[168:171], v[92:95]
s_waitcnt vmcnt(15) lgkmcnt(3)
s_lshl_b32 s95, s94, 16
v_mfma_f32_16x16x32_bf16 v[124:127], v[220:223], v[172:175], v[124:127]
ds_read_b64_tr_b16 v[220:221], v243 offset:59264
ds_read_b64_tr_b16 v[222:223], v243 offset:60288
v_mfma_f32_16x16x32_bf16 v[0:3], v[192:195], v[176:179], v[0:3]
v_accvgpr_read_b32 v224, a96
v_accvgpr_read_b32 v225, a97
v_mfma_f32_16x16x32_bf16 v[32:35], v[192:195], v[180:183], v[32:35]
buffer_load_dwordx4 v[160:163], v244, s[52:55], s91 offen offset:2048 sc1
v_mfma_f32_16x16x32_bf16 v[64:67], v[192:195], v[184:187], v[64:67]
v_accvgpr_read_b32 v226, a98
v_accvgpr_read_b32 v227, a99
v_mfma_f32_16x16x32_bf16 v[96:99], v[192:195], v[188:191], v[96:99]
.La5_LW1_7:
s_cbranch_execz .La5_LW1_11
v_accvgpr_read_b32 v228, a100
v_accvgpr_read_b32 v229, a101
v_mfma_f32_16x16x32_bf16 v[4:7], v[196:199], v[176:179], v[4:7]
s_waitcnt lgkmcnt(0)
s_barrier
v_mfma_f32_16x16x32_bf16 v[36:39], v[196:199], v[180:183], v[36:39]
buffer_load_dwordx4 v[164:167], v245, s[52:55], s91 offen offset:2048 sc1
v_mfma_f32_16x16x32_bf16 v[68:71], v[196:199], v[184:187], v[68:71]
ds_read_b64_tr_b16 v[192:193], v240 offset:0
ds_read_b64_tr_b16 v[194:195], v240 offset:1024
v_mfma_f32_16x16x32_bf16 v[100:103], v[196:199], v[188:191], v[100:103]
ds_read_b64_tr_b16 v[196:197], v241 offset:0
ds_read_b64_tr_b16 v[198:199], v241 offset:1024
v_mfma_f32_16x16x32_bf16 v[8:11], v[200:203], v[176:179], v[8:11]
v_accvgpr_read_b32 v230, a102
v_accvgpr_read_b32 v231, a103
v_mfma_f32_16x16x32_bf16 v[40:43], v[200:203], v[180:183], v[40:43]
buffer_load_dwordx4 v[168:171], v246, s[52:55], s91 offen offset:2048 sc1
v_mfma_f32_16x16x32_bf16 v[72:75], v[200:203], v[184:187], v[72:75]
v_cvt_pk_bf16_f32 v248, v224, v225
v_cvt_pk_bf16_f32 v249, v226, v227
v_mfma_f32_16x16x32_bf16 v[104:107], v[200:203], v[188:191], v[104:107]
ds_read_b64_tr_b16 v[200:201], v242 offset:0
ds_read_b64_tr_b16 v[202:203], v242 offset:1024
v_mfma_f32_16x16x32_bf16 v[12:15], v[204:207], v[176:179], v[12:15]
s_add_u32 m0, s83, 50688
v_cvt_pk_bf16_f32 v250, v228, v229
v_mfma_f32_16x16x32_bf16 v[44:47], v[204:207], v[180:183], v[44:47]
buffer_load_dwordx4 v232, s[44:47], s80 offen lds
v_mfma_f32_16x16x32_bf16 v[76:79], v[204:207], v[184:187], v[76:79]
v_cvt_pk_bf16_f32 v251, v230, v231
v_mfma_f32_16x16x32_bf16 v[108:111], v[204:207], v[188:191], v[108:111]
ds_read_b64_tr_b16 v[204:205], v243 offset:0
ds_read_b64_tr_b16 v[206:207], v243 offset:1024
v_mfma_f32_16x16x32_bf16 v[16:19], v[208:211], v[176:179], v[16:19]
s_add_u32 s28, s34, s93
s_addc_u32 s29, s35, 0
v_mfma_f32_16x16x32_bf16 v[48:51], v[208:211], v[180:183], v[48:51]
buffer_load_dwordx4 v[172:175], v247, s[52:55], s91 offen offset:2048 sc1
v_mfma_f32_16x16x32_bf16 v[80:83], v[208:211], v[184:187], v[80:83]
s_and_b32 s29, s29, 0xffff
s_mov_b32 s30, s95
v_mfma_f32_16x16x32_bf16 v[112:115], v[208:211], v[188:191], v[112:115]
ds_read_b64_tr_b16 v[208:209], v240 offset:128
ds_read_b64_tr_b16 v[210:211], v240 offset:1152
v_mfma_f32_16x16x32_bf16 v[20:23], v[212:215], v[176:179], v[20:23]
s_add_u32 m0, s83, 51712
s_mov_b32 s31, 0x20000
v_mfma_f32_16x16x32_bf16 v[52:55], v[212:215], v[180:183], v[52:55]
buffer_load_dwordx4 v233, s[44:47], s80 offen lds
v_mfma_f32_16x16x32_bf16 v[84:87], v[212:215], v[184:187], v[84:87]
s_lshl_b32 s93, s101, 12
s_add_u32 s92, s93, 71303168
v_mfma_f32_16x16x32_bf16 v[116:119], v[212:215], v[188:191], v[116:119]
ds_read_b64_tr_b16 v[212:213], v241 offset:128
ds_read_b64_tr_b16 v[214:215], v241 offset:1152
v_mfma_f32_16x16x32_bf16 v[24:27], v[216:219], v[176:179], v[24:27]
s_sub_u32 s93, 25165824, s93
v_mfma_f32_16x16x32_bf16 v[56:59], v[216:219], v[180:183], v[56:59]
s_add_u32 s32, s26, s92
s_addc_u32 s33, s27, 0
v_mfma_f32_16x16x32_bf16 v[88:91], v[216:219], v[184:187], v[88:91]
s_and_b32 s33, s33, 0xffff
s_mov_b32 s34, s93
v_mfma_f32_16x16x32_bf16 v[120:123], v[216:219], v[188:191], v[120:123]
ds_read_b64_tr_b16 v[216:217], v242 offset:128
ds_read_b64_tr_b16 v[218:219], v242 offset:1152
v_mfma_f32_16x16x32_bf16 v[28:31], v[220:223], v[176:179], v[28:31]
ds_write_b128 v238, v[248:251] offset:34816
v_mfma_f32_16x16x32_bf16 v[60:63], v[220:223], v[180:183], v[60:63]
buffer_store_dwordx4 v[248:251], v255, s[48:51], s89 offen offset:2048 sc1
v_mfma_f32_16x16x32_bf16 v[92:95], v[220:223], v[184:187], v[92:95]
s_waitcnt vmcnt(16) lgkmcnt(3)
s_mov_b32 s35, 0x20000
v_mfma_f32_16x16x32_bf16 v[124:127], v[220:223], v[188:191], v[124:127]
ds_read_b64_tr_b16 v[220:221], v243 offset:128
ds_read_b64_tr_b16 v[222:223], v243 offset:1152
v_mfma_f32_16x16x32_bf16 v[0:3], v[192:195], v[128:131], v[0:3]
v_accvgpr_read_b32 v224, a160
v_accvgpr_read_b32 v225, a161
v_mfma_f32_16x16x32_bf16 v[32:35], v[192:195], v[132:135], v[32:35]
buffer_load_dwordx4 v[176:179], v244, s[52:55], s91 offen offset:3072 sc1
v_mfma_f32_16x16x32_bf16 v[64:67], v[192:195], v[136:139], v[64:67]
v_accvgpr_read_b32 v226, a162
v_accvgpr_read_b32 v227, a163
v_mfma_f32_16x16x32_bf16 v[96:99], v[192:195], v[140:143], v[96:99]
ds_read_b64_tr_b16 v[192:193], v240 offset:8448
ds_read_b64_tr_b16 v[194:195], v240 offset:9472
v_mfma_f32_16x16x32_bf16 v[4:7], v[196:199], v[128:131], v[4:7]
v_accvgpr_read_b32 v228, a164
v_accvgpr_read_b32 v229, a165
v_mfma_f32_16x16x32_bf16 v[36:39], v[196:199], v[132:135], v[36:39]
buffer_load_dwordx4 v[180:183], v245, s[52:55], s91 offen offset:3072 sc1
v_mfma_f32_16x16x32_bf16 v[68:71], v[196:199], v[136:139], v[68:71]
s_waitcnt lgkmcnt(2)
v_accvgpr_read_b32 v230, a166
v_mfma_f32_16x16x32_bf16 v[100:103], v[196:199], v[140:143], v[100:103]
ds_read_b64_tr_b16 v[196:197], v241 offset:8448
ds_read_b64_tr_b16 v[198:199], v241 offset:9472
v_mfma_f32_16x16x32_bf16 v[8:11], v[200:203], v[128:131], v[8:11]
s_add_u32 m0, s83, 52864
v_accvgpr_read_b32 v231, a167
v_mfma_f32_16x16x32_bf16 v[40:43], v[200:203], v[132:135], v[40:43]
buffer_load_dwordx4 v234, s[44:47], s80 offen lds
v_mfma_f32_16x16x32_bf16 v[72:75], v[200:203], v[136:139], v[72:75]
v_cvt_pk_bf16_f32 v248, v224, v225
v_cvt_pk_bf16_f32 v249, v226, v227
v_mfma_f32_16x16x32_bf16 v[104:107], v[200:203], v[140:143], v[104:107]
ds_read_b64_tr_b16 v[200:201], v242 offset:8448
ds_read_b64_tr_b16 v[202:203], v242 offset:9472
v_mfma_f32_16x16x32_bf16 v[12:15], v[204:207], v[128:131], v[12:15]
v_cvt_pk_bf16_f32 v250, v228, v229
v_cvt_pk_bf16_f32 v251, v230, v231
v_mfma_f32_16x16x32_bf16 v[44:47], v[204:207], v[132:135], v[44:47]
buffer_load_dwordx4 v[184:187], v246, s[52:55], s91 offen offset:3072 sc1
v_mfma_f32_16x16x32_bf16 v[76:79], v[204:207], v[136:139], v[76:79]
s_lshl2_add_u32 s92, s72, s74
s_lshl2_add_u32 s92, s92, s3
v_mfma_f32_16x16x32_bf16 v[108:111], v[204:207], v[140:143], v[108:111]
ds_read_b64_tr_b16 v[204:205], v243 offset:8448
ds_read_b64_tr_b16 v[206:207], v243 offset:9472
v_mfma_f32_16x16x32_bf16 v[16:19], v[208:211], v[128:131], v[16:19]
s_add_u32 m0, s83, 53888
s_lshl_b32 s88, s92, 8
v_mfma_f32_16x16x32_bf16 v[48:51], v[208:211], v[132:135], v[48:51]
buffer_load_dwordx4 v235, s[44:47], s80 offen lds
v_mfma_f32_16x16x32_bf16 v[80:83], v[208:211], v[136:139], v[80:83]
s_add_u32 s80, s80, 524288
s_mul_i32 s93, s92, 17408
v_mfma_f32_16x16x32_bf16 v[112:115], v[208:211], v[140:143], v[112:115]
ds_read_b64_tr_b16 v[208:209], v240 offset:8576
ds_read_b64_tr_b16 v[210:211], v240 offset:9600
v_mfma_f32_16x16x32_bf16 v[20:23], v[212:215], v[128:131], v[20:23]
s_lshl_b32 s92, s92, 14
s_add_u32 s92, s92, 4194304
v_mfma_f32_16x16x32_bf16 v[52:55], v[212:215], v[132:135], v[52:55]
buffer_load_dwordx4 v[188:191], v247, s[52:55], s91 offen offset:3072 sc1
v_mfma_f32_16x16x32_bf16 v[84:87], v[212:215], v[136:139], v[84:87]
s_add_u32 s91, s91, 4096
v_mfma_f32_16x16x32_bf16 v[116:119], v[212:215], v[140:143], v[116:119]
ds_read_b64_tr_b16 v[212:213], v241 offset:8576
ds_read_b64_tr_b16 v[214:215], v241 offset:9600
v_mfma_f32_16x16x32_bf16 v[24:27], v[216:219], v[128:131], v[24:27]
s_add_u32 s86, s26, s92
s_addc_u32 s87, s27, 0
v_mfma_f32_16x16x32_bf16 v[56:59], v[216:219], v[132:135], v[56:59]
s_and_b32 s87, s87, 0xffff
s_add_u32 s93, s93, 150994944
v_mfma_f32_16x16x32_bf16 v[88:91], v[216:219], v[136:139], v[88:91]
s_add_u32 s68, s26, s93
s_addc_u32 s69, s27, 0
v_mfma_f32_16x16x32_bf16 v[120:123], v[216:219], v[140:143], v[120:123]
ds_read_b64_tr_b16 v[216:217], v242 offset:8576
ds_read_b64_tr_b16 v[218:219], v242 offset:9600
v_mfma_f32_16x16x32_bf16 v[28:31], v[220:223], v[128:131], v[28:31]
ds_write_b128 v238, v[248:251] offset:35840
v_mfma_f32_16x16x32_bf16 v[60:63], v[220:223], v[132:135], v[60:63]
buffer_store_dwordx4 v[248:251], v255, s[48:51], s89 offen offset:3072 sc1
v_mfma_f32_16x16x32_bf16 v[92:95], v[220:223], v[136:139], v[92:95]
s_waitcnt vmcnt(15) lgkmcnt(3)
s_and_b32 s69, s69, 0xffff
v_mfma_f32_16x16x32_bf16 v[124:127], v[220:223], v[140:143], v[124:127]
ds_read_b64_tr_b16 v[220:221], v243 offset:8576
ds_read_b64_tr_b16 v[222:223], v243 offset:9600
v_mfma_f32_16x16x32_bf16 v[0:3], v[192:195], v[144:147], v[0:3]
v_accvgpr_read_b32 v224, a104
v_accvgpr_read_b32 v225, a105
v_mfma_f32_16x16x32_bf16 v[32:35], v[192:195], v[148:151], v[32:35]
buffer_load_dwordx4 v[128:131], v244, s[52:55], s91 offen offset:0 sc1
v_mfma_f32_16x16x32_bf16 v[64:67], v[192:195], v[152:155], v[64:67]
v_accvgpr_read_b32 v226, a106
v_accvgpr_read_b32 v227, a107
v_mfma_f32_16x16x32_bf16 v[96:99], v[192:195], v[156:159], v[96:99]
v_accvgpr_read_b32 v228, a108
v_accvgpr_read_b32 v229, a109
v_mfma_f32_16x16x32_bf16 v[4:7], v[196:199], v[144:147], v[4:7]
s_waitcnt lgkmcnt(0)
s_barrier
v_mfma_f32_16x16x32_bf16 v[36:39], v[196:199], v[148:151], v[36:39]
buffer_load_dwordx4 v[132:135], v245, s[52:55], s91 offen offset:0 sc1
v_mfma_f32_16x16x32_bf16 v[68:71], v[196:199], v[152:155], v[68:71]
ds_read_b64_tr_b16 v[192:193], v240 offset:16896
ds_read_b64_tr_b16 v[194:195], v240 offset:17920
v_mfma_f32_16x16x32_bf16 v[100:103], v[196:199], v[156:159], v[100:103]
ds_read_b64_tr_b16 v[196:197], v241 offset:16896
ds_read_b64_tr_b16 v[198:199], v241 offset:17920
v_mfma_f32_16x16x32_bf16 v[8:11], v[200:203], v[144:147], v[8:11]
v_accvgpr_read_b32 v230, a110
v_accvgpr_read_b32 v231, a111
v_mfma_f32_16x16x32_bf16 v[40:43], v[200:203], v[148:151], v[40:43]
buffer_load_dwordx4 v[136:139], v246, s[52:55], s91 offen offset:0 sc1
v_mfma_f32_16x16x32_bf16 v[72:75], v[200:203], v[152:155], v[72:75]
v_cvt_pk_bf16_f32 v248, v224, v225
v_cvt_pk_bf16_f32 v249, v226, v227
v_mfma_f32_16x16x32_bf16 v[104:107], v[200:203], v[156:159], v[104:107]
ds_read_b64_tr_b16 v[200:201], v242 offset:16896
ds_read_b64_tr_b16 v[202:203], v242 offset:17920
v_mfma_f32_16x16x32_bf16 v[12:15], v[204:207], v[144:147], v[12:15]
s_add_u32 m0, s83, 0
v_cvt_pk_bf16_f32 v250, v228, v229
v_mfma_f32_16x16x32_bf16 v[44:47], v[204:207], v[148:151], v[44:47]
buffer_load_dwordx4 v232, s[44:47], s80 offen lds
v_mfma_f32_16x16x32_bf16 v[76:79], v[204:207], v[152:155], v[76:79]
v_cvt_pk_bf16_f32 v251, v230, v231
s_mov_b32 s89, 36864
v_mfma_f32_16x16x32_bf16 v[108:111], v[204:207], v[156:159], v[108:111]
ds_read_b64_tr_b16 v[204:205], v243 offset:16896
ds_read_b64_tr_b16 v[206:207], v243 offset:17920
v_mfma_f32_16x16x32_bf16 v[16:19], v[208:211], v[144:147], v[16:19]
s_mov_b32 s70, 17408
s_lshl3_add_u32 s96, s74, 4
v_mfma_f32_16x16x32_bf16 v[48:51], v[208:211], v[148:151], v[48:51]
buffer_load_dwordx4 v[140:143], v247, s[52:55], s91 offen offset:0 sc1
v_mfma_f32_16x16x32_bf16 v[80:83], v[208:211], v[152:155], v[80:83]
s_mov_b32 s84, 0
s_sub_u32 s79, s96, 2
v_mfma_f32_16x16x32_bf16 v[112:115], v[208:211], v[156:159], v[112:115]
ds_read_b64_tr_b16 v[208:209], v240 offset:17024
ds_read_b64_tr_b16 v[210:211], v240 offset:18048
v_mfma_f32_16x16x32_bf16 v[20:23], v[212:215], v[144:147], v[20:23]
s_add_u32 m0, s83, 1024
s_max_u32 s79, s79, 3
v_mfma_f32_16x16x32_bf16 v[52:55], v[212:215], v[148:151], v[52:55]
buffer_load_dwordx4 v233, s[44:47], s80 offen lds
v_mfma_f32_16x16x32_bf16 v[84:87], v[212:215], v[152:155], v[84:87]
s_lshl_b32 s95, 9, s96
s_lshl_b32 s97, 3, s79
v_mfma_f32_16x16x32_bf16 v[116:119], v[212:215], v[156:159], v[116:119]
ds_read_b64_tr_b16 v[212:213], v241 offset:17024
ds_read_b64_tr_b16 v[214:215], v241 offset:18048
v_mfma_f32_16x16x32_bf16 v[24:27], v[216:219], v[144:147], v[24:27]
s_lshr_b32 s97, s97, 1
s_or_b32 s95, s95, s97
v_mfma_f32_16x16x32_bf16 v[56:59], v[216:219], v[148:151], v[56:59]
s_or_b32 s95, s95, 4194304
v_mfma_f32_16x16x32_bf16 v[88:91], v[216:219], v[152:155], v[88:91]
s_cmp_eq_u32 s74, 3
s_cselect_b32 s23, 3, 0
s_cselect_b32 s22, 0, s75
s_cselect_b32 s95, 4194304, s95
s_cselect_b32 s96, 22, s96
v_mfma_f32_16x16x32_bf16 v[120:123], v[216:219], v[156:159], v[120:123]
ds_read_b64_tr_b16 v[216:217], v242 offset:17024
ds_read_b64_tr_b16 v[218:219], v242 offset:18048
v_mfma_f32_16x16x32_bf16 v[28:31], v[220:223], v[144:147], v[28:31]
ds_write_b128 v238, v[248:251] offset:36864
v_mfma_f32_16x16x32_bf16 v[60:63], v[220:223], v[148:151], v[60:63]
buffer_store_dwordx4 v[248:251], v255, s[48:51], s89 offen offset:0 sc1
v_mfma_f32_16x16x32_bf16 v[92:95], v[220:223], v[152:155], v[92:95]
s_waitcnt vmcnt(16) lgkmcnt(3)
s_or_b32 s95, s95, 4
v_mfma_f32_16x16x32_bf16 v[124:127], v[220:223], v[156:159], v[124:127]
ds_read_b64_tr_b16 v[220:221], v243 offset:17024
ds_read_b64_tr_b16 v[222:223], v243 offset:18048
v_mfma_f32_16x16x32_bf16 v[0:3], v[192:195], v[160:163], v[0:3]
v_accvgpr_read_b32 v224, a168
v_accvgpr_read_b32 v225, a169
v_mfma_f32_16x16x32_bf16 v[32:35], v[192:195], v[164:167], v[32:35]
buffer_load_dwordx4 v[144:147], v244, s[52:55], s91 offen offset:1024 sc1
v_mfma_f32_16x16x32_bf16 v[64:67], v[192:195], v[168:171], v[64:67]
v_accvgpr_read_b32 v226, a170
v_accvgpr_read_b32 v227, a171
v_mfma_f32_16x16x32_bf16 v[96:99], v[192:195], v[172:175], v[96:99]
ds_read_b64_tr_b16 v[192:193], v240 offset:25344
ds_read_b64_tr_b16 v[194:195], v240 offset:26368
v_mfma_f32_16x16x32_bf16 v[4:7], v[196:199], v[160:163], v[4:7]
v_accvgpr_read_b32 v228, a172
v_accvgpr_read_b32 v229, a173
v_mfma_f32_16x16x32_bf16 v[36:39], v[196:199], v[164:167], v[36:39]
buffer_load_dwordx4 v[148:151], v245, s[52:55], s91 offen offset:1024 sc1
v_mfma_f32_16x16x32_bf16 v[68:71], v[196:199], v[168:171], v[68:71]
s_waitcnt lgkmcnt(2)
v_accvgpr_read_b32 v230, a174
v_mfma_f32_16x16x32_bf16 v[100:103], v[196:199], v[172:175], v[100:103]
ds_read_b64_tr_b16 v[196:197], v241 offset:25344
ds_read_b64_tr_b16 v[198:199], v241 offset:26368
v_mfma_f32_16x16x32_bf16 v[8:11], v[200:203], v[160:163], v[8:11]
s_add_u32 m0, s83, 2176
v_accvgpr_read_b32 v231, a175
v_mfma_f32_16x16x32_bf16 v[40:43], v[200:203], v[164:167], v[40:43]
buffer_load_dwordx4 v234, s[44:47], s80 offen lds
v_mfma_f32_16x16x32_bf16 v[72:75], v[200:203], v[168:171], v[72:75]
v_cvt_pk_bf16_f32 v248, v224, v225
v_cvt_pk_bf16_f32 v249, v226, v227
v_mfma_f32_16x16x32_bf16 v[104:107], v[200:203], v[172:175], v[104:107]
ds_read_b64_tr_b16 v[200:201], v242 offset:25344
ds_read_b64_tr_b16 v[202:203], v242 offset:26368
v_mfma_f32_16x16x32_bf16 v[12:15], v[204:207], v[160:163], v[12:15]
v_cvt_pk_bf16_f32 v250, v228, v229
v_cvt_pk_bf16_f32 v251, v230, v231
v_mfma_f32_16x16x32_bf16 v[44:47], v[204:207], v[164:167], v[44:47]
buffer_load_dwordx4 v[152:155], v246, s[52:55], s91 offen offset:1024 sc1
v_mfma_f32_16x16x32_bf16 v[76:79], v[204:207], v[168:171], v[76:79]
s_cmp_eq_u32 s75, 1
s_cselect_b32 s98, s95, 1073741828
s_cselect_b32 s99, 0, 0
s_cselect_b32 s81, 25, 33
s_cselect_b32 s82, s96, 30
v_mfma_f32_16x16x32_bf16 v[108:111], v[204:207], v[172:175], v[108:111]
ds_read_b64_tr_b16 v[204:205], v243 offset:25344
ds_read_b64_tr_b16 v[206:207], v243 offset:26368
v_mfma_f32_16x16x32_bf16 v[16:19], v[208:211], v[160:163], v[16:19]
s_add_u32 m0, s83, 3200
v_mfma_f32_16x16x32_bf16 v[48:51], v[208:211], v[164:167], v[48:51]
buffer_load_dwordx4 v235, s[44:47], s80 offen lds
v_mfma_f32_16x16x32_bf16 v[80:83], v[208:211], v[168:171], v[80:83]
s_add_u32 s80, s80, 524288
v_mfma_f32_16x16x32_bf16 v[112:115], v[208:211], v[172:175], v[112:115]
ds_read_b64_tr_b16 v[208:209], v240 offset:25472
ds_read_b64_tr_b16 v[210:211], v240 offset:26496
v_mfma_f32_16x16x32_bf16 v[20:23], v[212:215], v[160:163], v[20:23]
s_bitcmp1_b32 2, s73
s_cselect_b32 s95, 131072, 0
v_mfma_f32_16x16x32_bf16 v[52:55], v[212:215], v[164:167], v[52:55]
buffer_load_dwordx4 v[156:159], v247, s[52:55], s91 offen offset:1024 sc1
v_mfma_f32_16x16x32_bf16 v[84:87], v[212:215], v[168:171], v[84:87]
s_or_b32 s98, s98, s95
s_lshl_b32 s95, s76, 14
v_mfma_f32_16x16x32_bf16 v[116:119], v[212:215], v[172:175], v[116:119]
ds_read_b64_tr_b16 v[212:213], v241 offset:25472
ds_read_b64_tr_b16 v[214:215], v241 offset:26496
v_mfma_f32_16x16x32_bf16 v[24:27], v[216:219], v[160:163], v[24:27]
s_lshl3_add_u32 s95, s72, s95
s_lshl_b32 s96, s3, 9
v_mfma_f32_16x16x32_bf16 v[56:59], v[216:219], v[164:167], v[56:59]
s_add_u32 s95, s95, s96
s_add_u32 s6, s95, 524288
v_mfma_f32_16x16x32_bf16 v[88:91], v[216:219], v[168:171], v[88:91]
v_and_b32 v239, 0x70, v255
v_lshlrev_b32 v239, 7, v239
v_mfma_f32_16x16x32_bf16 v[120:123], v[216:219], v[172:175], v[120:123]
ds_read_b64_tr_b16 v[216:217], v242 offset:25472
ds_read_b64_tr_b16 v[218:219], v242 offset:26496
v_mfma_f32_16x16x32_bf16 v[28:31], v[220:223], v[160:163], v[28:31]
ds_write_b128 v238, v[248:251] offset:37888
v_mfma_f32_16x16x32_bf16 v[60:63], v[220:223], v[164:167], v[60:63]
buffer_store_dwordx4 v[248:251], v255, s[48:51], s89 offen offset:1024 sc1
v_mfma_f32_16x16x32_bf16 v[92:95], v[220:223], v[168:171], v[92:95]
s_waitcnt vmcnt(15) lgkmcnt(3)
v_accvgpr_write_b32 a231, v239
v_mfma_f32_16x16x32_bf16 v[124:127], v[220:223], v[172:175], v[124:127]
ds_read_b64_tr_b16 v[220:221], v243 offset:25472
ds_read_b64_tr_b16 v[222:223], v243 offset:26496
v_mfma_f32_16x16x32_bf16 v[0:3], v[192:195], v[176:179], v[0:3]
v_accvgpr_read_b32 v224, a112
v_accvgpr_read_b32 v225, a113
v_mfma_f32_16x16x32_bf16 v[32:35], v[192:195], v[180:183], v[32:35]
buffer_load_dwordx4 v[160:163], v244, s[52:55], s91 offen offset:2048 sc1
v_mfma_f32_16x16x32_bf16 v[64:67], v[192:195], v[184:187], v[64:67]
v_accvgpr_read_b32 v226, a114
v_accvgpr_read_b32 v227, a115
v_mfma_f32_16x16x32_bf16 v[96:99], v[192:195], v[188:191], v[96:99]
v_accvgpr_read_b32 v228, a116
v_accvgpr_read_b32 v229, a117
v_mfma_f32_16x16x32_bf16 v[4:7], v[196:199], v[176:179], v[4:7]
s_waitcnt lgkmcnt(0)
s_barrier
v_mfma_f32_16x16x32_bf16 v[36:39], v[196:199], v[180:183], v[36:39]
buffer_load_dwordx4 v[164:167], v245, s[52:55], s91 offen offset:2048 sc1
v_mfma_f32_16x16x32_bf16 v[68:71], v[196:199], v[184:187], v[68:71]
ds_read_b64_tr_b16 v[192:193], v240 offset:33792
ds_read_b64_tr_b16 v[194:195], v240 offset:34816
v_mfma_f32_16x16x32_bf16 v[100:103], v[196:199], v[188:191], v[100:103]
ds_read_b64_tr_b16 v[196:197], v241 offset:33792
ds_read_b64_tr_b16 v[198:199], v241 offset:34816
v_mfma_f32_16x16x32_bf16 v[8:11], v[200:203], v[176:179], v[8:11]
v_accvgpr_read_b32 v230, a118
v_accvgpr_read_b32 v231, a119
v_mfma_f32_16x16x32_bf16 v[40:43], v[200:203], v[180:183], v[40:43]
buffer_load_dwordx4 v[168:171], v246, s[52:55], s91 offen offset:2048 sc1
v_mfma_f32_16x16x32_bf16 v[72:75], v[200:203], v[184:187], v[72:75]
v_cvt_pk_bf16_f32 v248, v224, v225
v_cvt_pk_bf16_f32 v249, v226, v227
v_mfma_f32_16x16x32_bf16 v[104:107], v[200:203], v[188:191], v[104:107]
ds_read_b64_tr_b16 v[200:201], v242 offset:33792
ds_read_b64_tr_b16 v[202:203], v242 offset:34816
v_mfma_f32_16x16x32_bf16 v[12:15], v[204:207], v[176:179], v[12:15]
s_add_u32 m0, s83, 16896
v_cvt_pk_bf16_f32 v250, v228, v229
v_mfma_f32_16x16x32_bf16 v[44:47], v[204:207], v[180:183], v[44:47]
buffer_load_dwordx4 v232, s[44:47], s80 offen lds
v_mfma_f32_16x16x32_bf16 v[76:79], v[204:207], v[184:187], v[76:79]
v_cvt_pk_bf16_f32 v251, v230, v231
s_mov_b32 s95, 1
v_mfma_f32_16x16x32_bf16 v[108:111], v[204:207], v[188:191], v[108:111]
ds_read_b64_tr_b16 v[204:205], v243 offset:33792
ds_read_b64_tr_b16 v[206:207], v243 offset:34816
v_mfma_f32_16x16x32_bf16 v[16:19], v[208:211], v[176:179], v[16:19]
v_lshrrev_b32 v239, 7, v255
v_mul_u32_u24 v254, s21, v239
v_mfma_f32_16x16x32_bf16 v[48:51], v[208:211], v[180:183], v[48:51]
buffer_load_dwordx4 v[172:175], v247, s[52:55], s91 offen offset:2048 sc1
v_mfma_f32_16x16x32_bf16 v[80:83], v[208:211], v[184:187], v[80:83]
v_lshrrev_b32 v239, s95, v239
v_lshl_add_u32 v254, v239, 28, v254
v_mfma_f32_16x16x32_bf16 v[112:115], v[208:211], v[188:191], v[112:115]
ds_read_b64_tr_b16 v[208:209], v240 offset:33920
ds_read_b64_tr_b16 v[210:211], v240 offset:34944
v_mfma_f32_16x16x32_bf16 v[20:23], v[212:215], v[176:179], v[20:23]
s_add_u32 m0, s83, 17920
v_and_b32 v239, 0x70, v255
v_mfma_f32_16x16x32_bf16 v[52:55], v[212:215], v[180:183], v[52:55]
buffer_load_dwordx4 v233, s[44:47], s80 offen lds
v_mfma_f32_16x16x32_bf16 v[84:87], v[212:215], v[184:187], v[84:87]
v_lshl_add_u32 v254, v239, 3, v254
s_lshl_b32 s21, s21, s95
v_mfma_f32_16x16x32_bf16 v[116:119], v[212:215], v[188:191], v[116:119]
ds_read_b64_tr_b16 v[212:213], v241 offset:33920
ds_read_b64_tr_b16 v[214:215], v241 offset:34944
v_mfma_f32_16x16x32_bf16 v[24:27], v[216:219], v[176:179], v[24:27]
v_lshrrev_b32 v236, 8, v255
v_lshlrev_b32 v239, 5, v236
v_mfma_f32_16x16x32_bf16 v[56:59], v[216:219], v[180:183], v[56:59]
v_xor_b32 v239, v239, v255
v_and_b32 v239, 0xf0, v239
v_mfma_f32_16x16x32_bf16 v[88:91], v[216:219], v[184:187], v[88:91]
v_lshl_add_u32 v239, v236, 11, v239
s_lshl_b32 s92, s77, 16
v_mfma_f32_16x16x32_bf16 v[120:123], v[216:219], v[188:191], v[120:123]
ds_read_b64_tr_b16 v[216:217], v242 offset:33920
ds_read_b64_tr_b16 v[218:219], v242 offset:34944
v_mfma_f32_16x16x32_bf16 v[28:31], v[220:223], v[176:179], v[28:31]
ds_write_b128 v238, v[248:251] offset:38912
v_mfma_f32_16x16x32_bf16 v[60:63], v[220:223], v[180:183], v[60:63]
buffer_store_dwordx4 v[248:251], v255, s[48:51], s89 offen offset:2048 sc1
v_mfma_f32_16x16x32_bf16 v[92:95], v[220:223], v[184:187], v[92:95]
s_waitcnt vmcnt(16) lgkmcnt(3)
s_lshl_b32 s93, s78, 14
v_mfma_f32_16x16x32_bf16 v[124:127], v[220:223], v[188:191], v[124:127]
ds_read_b64_tr_b16 v[220:221], v243 offset:33920
ds_read_b64_tr_b16 v[222:223], v243 offset:34944
v_mfma_f32_16x16x32_bf16 v[0:3], v[192:195], v[128:131], v[0:3]
v_accvgpr_read_b32 v224, a176
v_accvgpr_read_b32 v225, a177
v_mfma_f32_16x16x32_bf16 v[32:35], v[192:195], v[132:135], v[32:35]
buffer_load_dwordx4 v[176:179], v244, s[52:55], s91 offen offset:3072 sc1
v_mfma_f32_16x16x32_bf16 v[64:67], v[192:195], v[136:139], v[64:67]
v_accvgpr_read_b32 v226, a178
v_accvgpr_read_b32 v227, a179
v_mfma_f32_16x16x32_bf16 v[96:99], v[192:195], v[140:143], v[96:99]
ds_read_b64_tr_b16 v[192:193], v240 offset:42240
ds_read_b64_tr_b16 v[194:195], v240 offset:43264
v_mfma_f32_16x16x32_bf16 v[4:7], v[196:199], v[128:131], v[4:7]
v_accvgpr_read_b32 v228, a180
v_accvgpr_read_b32 v229, a181
v_mfma_f32_16x16x32_bf16 v[36:39], v[196:199], v[132:135], v[36:39]
buffer_load_dwordx4 v[180:183], v245, s[52:55], s91 offen offset:3072 sc1
v_mfma_f32_16x16x32_bf16 v[68:71], v[196:199], v[136:139], v[68:71]
s_waitcnt lgkmcnt(2)
v_accvgpr_read_b32 v230, a182
v_mfma_f32_16x16x32_bf16 v[100:103], v[196:199], v[140:143], v[100:103]
ds_read_b64_tr_b16 v[196:197], v241 offset:42240
ds_read_b64_tr_b16 v[198:199], v241 offset:43264
v_mfma_f32_16x16x32_bf16 v[8:11], v[200:203], v[128:131], v[8:11]
s_add_u32 m0, s83, 19072
v_accvgpr_read_b32 v231, a183
v_mfma_f32_16x16x32_bf16 v[40:43], v[200:203], v[132:135], v[40:43]
buffer_load_dwordx4 v234, s[44:47], s80 offen lds
v_mfma_f32_16x16x32_bf16 v[72:75], v[200:203], v[136:139], v[72:75]
v_cvt_pk_bf16_f32 v248, v224, v225
v_cvt_pk_bf16_f32 v249, v226, v227
v_mfma_f32_16x16x32_bf16 v[104:107], v[200:203], v[140:143], v[104:107]
ds_read_b64_tr_b16 v[200:201], v242 offset:42240
ds_read_b64_tr_b16 v[202:203], v242 offset:43264
v_mfma_f32_16x16x32_bf16 v[12:15], v[204:207], v[128:131], v[12:15]
v_cvt_pk_bf16_f32 v250, v228, v229
v_cvt_pk_bf16_f32 v251, v230, v231
v_mfma_f32_16x16x32_bf16 v[44:47], v[204:207], v[132:135], v[44:47]
buffer_load_dwordx4 v[184:187], v246, s[52:55], s91 offen offset:3072 sc1
v_mfma_f32_16x16x32_bf16 v[76:79], v[204:207], v[136:139], v[76:79]
s_add_u32 s92, s92, s93
v_add_u32 v239, s92, v239
v_mfma_f32_16x16x32_bf16 v[108:111], v[204:207], v[140:143], v[108:111]
ds_read_b64_tr_b16 v[204:205], v243 offset:42240
ds_read_b64_tr_b16 v[206:207], v243 offset:43264
v_mfma_f32_16x16x32_bf16 v[16:19], v[208:211], v[128:131], v[16:19]
s_add_u32 m0, s83, 20096
v_accvgpr_write_b32 a228, v239
v_mfma_f32_16x16x32_bf16 v[48:51], v[208:211], v[132:135], v[48:51]
buffer_load_dwordx4 v235, s[44:47], s80 offen lds
v_mfma_f32_16x16x32_bf16 v[80:83], v[208:211], v[136:139], v[80:83]
s_add_u32 s80, s80, 524288
v_bfe_u32 v239, v255, 6, 2
v_mfma_f32_16x16x32_bf16 v[112:115], v[208:211], v[140:143], v[112:115]
ds_read_b64_tr_b16 v[208:209], v240 offset:42368
ds_read_b64_tr_b16 v[210:211], v240 offset:43392
v_mfma_f32_16x16x32_bf16 v[20:23], v[212:215], v[128:131], v[20:23]
v_lshlrev_b32 v239, 7, v239
v_lshl_add_u32 v239, v236, 5, v239
v_mfma_f32_16x16x32_bf16 v[52:55], v[212:215], v[132:135], v[52:55]
buffer_load_dwordx4 v[188:191], v247, s[52:55], s91 offen offset:3072 sc1
v_mfma_f32_16x16x32_bf16 v[84:87], v[212:215], v[136:139], v[84:87]
s_add_u32 s91, s91, 4096
s_lshl_b32 s92, s78, 9
v_mfma_f32_16x16x32_bf16 v[116:119], v[212:215], v[140:143], v[116:119]
ds_read_b64_tr_b16 v[212:213], v241 offset:42368
ds_read_b64_tr_b16 v[214:215], v241 offset:43392
v_mfma_f32_16x16x32_bf16 v[24:27], v[216:219], v[128:131], v[24:27]
v_add_u32 v239, s92, v239
v_mul_u32_u24 v239, 7168, v239
v_mfma_f32_16x16x32_bf16 v[56:59], v[216:219], v[132:135], v[56:59]
v_and_or_b32 v239, v255, 48, v239
v_accvgpr_write_b32 a229, v239
v_mfma_f32_16x16x32_bf16 v[88:91], v[216:219], v[136:139], v[88:91]
v_and_b32 v239, 0xf0, v255
v_lshl_add_u32 v239, v236, 14, v239
v_mfma_f32_16x16x32_bf16 v[120:123], v[216:219], v[140:143], v[120:123]
ds_read_b64_tr_b16 v[216:217], v242 offset:42368
ds_read_b64_tr_b16 v[218:219], v242 offset:43392
v_mfma_f32_16x16x32_bf16 v[28:31], v[220:223], v[128:131], v[28:31]
ds_write_b128 v238, v[248:251] offset:39936
v_mfma_f32_16x16x32_bf16 v[60:63], v[220:223], v[132:135], v[60:63]
buffer_store_dwordx4 v[248:251], v255, s[48:51], s89 offen offset:3072 sc1
v_mfma_f32_16x16x32_bf16 v[92:95], v[220:223], v[136:139], v[92:95]
s_waitcnt vmcnt(15) lgkmcnt(3)
s_lshl_b32 s92, s3, 9
v_mfma_f32_16x16x32_bf16 v[124:127], v[220:223], v[140:143], v[124:127]
ds_read_b64_tr_b16 v[220:221], v243 offset:42368
ds_read_b64_tr_b16 v[222:223], v243 offset:43392
v_mfma_f32_16x16x32_bf16 v[0:3], v[192:195], v[144:147], v[0:3]
v_accvgpr_read_b32 v224, a120
v_accvgpr_read_b32 v225, a121
v_mfma_f32_16x16x32_bf16 v[32:35], v[192:195], v[148:151], v[32:35]
buffer_load_dwordx4 v[128:131], v244, s[52:55], s91 offen offset:0 sc1
v_mfma_f32_16x16x32_bf16 v[64:67], v[192:195], v[152:155], v[64:67]
v_accvgpr_read_b32 v226, a122
v_accvgpr_read_b32 v227, a123
v_mfma_f32_16x16x32_bf16 v[96:99], v[192:195], v[156:159], v[96:99]
v_accvgpr_read_b32 v228, a124
v_accvgpr_read_b32 v229, a125
v_mfma_f32_16x16x32_bf16 v[4:7], v[196:199], v[144:147], v[4:7]
s_waitcnt lgkmcnt(0)
s_barrier
v_mfma_f32_16x16x32_bf16 v[36:39], v[196:199], v[148:151], v[36:39]
buffer_load_dwordx4 v[132:135], v245, s[52:55], s91 offen offset:0 sc1
v_mfma_f32_16x16x32_bf16 v[68:71], v[196:199], v[152:155], v[68:71]
ds_read_b64_tr_b16 v[192:193], v240 offset:50688
ds_read_b64_tr_b16 v[194:195], v240 offset:51712
v_mfma_f32_16x16x32_bf16 v[100:103], v[196:199], v[156:159], v[100:103]
ds_read_b64_tr_b16 v[196:197], v241 offset:50688
ds_read_b64_tr_b16 v[198:199], v241 offset:51712
v_mfma_f32_16x16x32_bf16 v[8:11], v[200:203], v[144:147], v[8:11]
v_accvgpr_read_b32 v230, a126
v_accvgpr_read_b32 v231, a127
v_mfma_f32_16x16x32_bf16 v[40:43], v[200:203], v[148:151], v[40:43]
buffer_load_dwordx4 v[136:139], v246, s[52:55], s91 offen offset:0 sc1
v_mfma_f32_16x16x32_bf16 v[72:75], v[200:203], v[152:155], v[72:75]
v_cvt_pk_bf16_f32 v248, v224, v225
.La5_LW1_8:
s_cbranch_execz .La5_LW1_R0
v_cvt_pk_bf16_f32 v249, v226, v227
v_mfma_f32_16x16x32_bf16 v[104:107], v[200:203], v[156:159], v[104:107]
ds_read_b64_tr_b16 v[200:201], v242 offset:50688
ds_read_b64_tr_b16 v[202:203], v242 offset:51712
v_mfma_f32_16x16x32_bf16 v[12:15], v[204:207], v[144:147], v[12:15]
s_add_u32 m0, s83, 33792
v_cvt_pk_bf16_f32 v250, v228, v229
v_mfma_f32_16x16x32_bf16 v[44:47], v[204:207], v[148:151], v[44:47]
buffer_load_dwordx4 v232, s[44:47], s80 offen lds
v_mfma_f32_16x16x32_bf16 v[76:79], v[204:207], v[152:155], v[76:79]
v_cvt_pk_bf16_f32 v251, v230, v231
s_mov_b32 s89, 16384
v_mfma_f32_16x16x32_bf16 v[108:111], v[204:207], v[156:159], v[108:111]
ds_read_b64_tr_b16 v[204:205], v243 offset:50688
ds_read_b64_tr_b16 v[206:207], v243 offset:51712
v_mfma_f32_16x16x32_bf16 v[16:19], v[208:211], v[144:147], v[16:19]
s_add_u32 s85, s85, 65536
v_add_u32 v238, s85, v255
v_mfma_f32_16x16x32_bf16 v[48:51], v[208:211], v[148:151], v[48:51]
buffer_load_dwordx4 v[140:143], v247, s[52:55], s91 offen offset:0 sc1
v_mfma_f32_16x16x32_bf16 v[80:83], v[208:211], v[152:155], v[80:83]
s_mov_b32 s85, 0
v_add_u32 v239, s92, v239
v_mfma_f32_16x16x32_bf16 v[112:115], v[208:211], v[156:159], v[112:115]
ds_read_b64_tr_b16 v[208:209], v240 offset:50816
ds_read_b64_tr_b16 v[210:211], v240 offset:51840
v_mfma_f32_16x16x32_bf16 v[20:23], v[212:215], v[144:147], v[20:23]
s_add_u32 m0, s83, 34816
v_accvgpr_write_b32 a230, v239
v_mfma_f32_16x16x32_bf16 v[52:55], v[212:215], v[148:151], v[52:55]
buffer_load_dwordx4 v233, s[44:47], s80 offen lds
v_mfma_f32_16x16x32_bf16 v[84:87], v[212:215], v[152:155], v[84:87]
v_add_u32 v239, 4294886912, v240
v_accvgpr_write_b32 a224, v239
v_mfma_f32_16x16x32_bf16 v[116:119], v[212:215], v[156:159], v[116:119]
ds_read_b64_tr_b16 v[212:213], v241 offset:50816
ds_read_b64_tr_b16 v[214:215], v241 offset:51840
v_mfma_f32_16x16x32_bf16 v[24:27], v[216:219], v[144:147], v[24:27]
v_bfe_u32 v239, v255, 4, 4
v_lshlrev_b32 v236, 2, v236
v_mfma_f32_16x16x32_bf16 v[56:59], v[216:219], v[148:151], v[56:59]
v_sub_u32 v239, v239, v236
v_subrev_u32 v236, 0, v239
v_mfma_f32_16x16x32_bf16 v[88:91], v[216:219], v[152:155], v[88:91]
v_ashrrev_i32 v236, 31, v236
v_and_b32 v236, 0xff800000, v236
v_mfma_f32_16x16x32_bf16 v[120:123], v[216:219], v[156:159], v[120:123]
ds_read_b64_tr_b16 v[216:217], v242 offset:50816
ds_read_b64_tr_b16 v[218:219], v242 offset:51840
v_mfma_f32_16x16x32_bf16 v[28:31], v[220:223], v[144:147], v[28:31]
ds_write_b128 v238, v[248:251] offset:0
v_mfma_f32_16x16x32_bf16 v[60:63], v[220:223], v[148:151], v[60:63]
buffer_store_dwordx4 v[248:251], v255, s[48:51], s89 offen offset:0 sc1
v_mfma_f32_16x16x32_bf16 v[92:95], v[220:223], v[152:155], v[92:95]
s_waitcnt vmcnt(16) lgkmcnt(3)
v_accvgpr_write_b32 a236, v236
v_mfma_f32_16x16x32_bf16 v[124:127], v[220:223], v[156:159], v[124:127]
ds_read_b64_tr_b16 v[220:221], v243 offset:50816
ds_read_b64_tr_b16 v[222:223], v243 offset:51840
v_mfma_f32_16x16x32_bf16 v[0:3], v[192:195], v[160:163], v[0:3]
v_accvgpr_read_b32 v224, a184
v_accvgpr_read_b32 v225, a185
v_mfma_f32_16x16x32_bf16 v[32:35], v[192:195], v[164:167], v[32:35]
buffer_load_dwordx4 v[144:147], v244, s[52:55], s91 offen offset:1024 sc1
v_mfma_f32_16x16x32_bf16 v[64:67], v[192:195], v[168:171], v[64:67]
v_accvgpr_read_b32 v226, a186
v_accvgpr_read_b32 v227, a187
v_mfma_f32_16x16x32_bf16 v[96:99], v[192:195], v[172:175], v[96:99]
ds_read_b64_tr_b16 v[192:193], v240 offset:59136
ds_read_b64_tr_b16 v[194:195], v240 offset:60160
v_mfma_f32_16x16x32_bf16 v[4:7], v[196:199], v[160:163], v[4:7]
v_accvgpr_read_b32 v228, a188
v_accvgpr_read_b32 v229, a189
v_mfma_f32_16x16x32_bf16 v[36:39], v[196:199], v[164:167], v[36:39]
buffer_load_dwordx4 v[148:151], v245, s[52:55], s91 offen offset:1024 sc1
v_mfma_f32_16x16x32_bf16 v[68:71], v[196:199], v[168:171], v[68:71]
s_waitcnt lgkmcnt(2)
v_accvgpr_read_b32 v230, a190
v_mfma_f32_16x16x32_bf16 v[100:103], v[196:199], v[172:175], v[100:103]
ds_read_b64_tr_b16 v[196:197], v241 offset:59136
ds_read_b64_tr_b16 v[198:199], v241 offset:60160
v_mfma_f32_16x16x32_bf16 v[8:11], v[200:203], v[160:163], v[8:11]
s_add_u32 m0, s83, 35968
v_accvgpr_read_b32 v231, a191
v_mfma_f32_16x16x32_bf16 v[40:43], v[200:203], v[164:167], v[40:43]
buffer_load_dwordx4 v234, s[44:47], s80 offen lds
v_mfma_f32_16x16x32_bf16 v[72:75], v[200:203], v[168:171], v[72:75]
v_cvt_pk_bf16_f32 v248, v224, v225
v_cvt_pk_bf16_f32 v249, v226, v227
v_mfma_f32_16x16x32_bf16 v[104:107], v[200:203], v[172:175], v[104:107]
ds_read_b64_tr_b16 v[200:201], v242 offset:59136
ds_read_b64_tr_b16 v[202:203], v242 offset:60160
v_mfma_f32_16x16x32_bf16 v[12:15], v[204:207], v[160:163], v[12:15]
v_cvt_pk_bf16_f32 v250, v228, v229
v_cvt_pk_bf16_f32 v251, v230, v231
v_mfma_f32_16x16x32_bf16 v[44:47], v[204:207], v[164:167], v[44:47]
buffer_load_dwordx4 v[152:155], v246, s[52:55], s91 offen offset:1024 sc1
v_mfma_f32_16x16x32_bf16 v[76:79], v[204:207], v[168:171], v[76:79]
v_subrev_u32 v236, 1, v239
v_ashrrev_i32 v236, 31, v236
v_mfma_f32_16x16x32_bf16 v[108:111], v[204:207], v[172:175], v[108:111]
ds_read_b64_tr_b16 v[204:205], v243 offset:59136
ds_read_b64_tr_b16 v[206:207], v243 offset:60160
v_mfma_f32_16x16x32_bf16 v[16:19], v[208:211], v[160:163], v[16:19]
s_add_u32 m0, s83, 36992
v_and_b32 v236, 0xff800000, v236
v_mfma_f32_16x16x32_bf16 v[48:51], v[208:211], v[164:167], v[48:51]
buffer_load_dwordx4 v235, s[44:47], s80 offen lds
v_mfma_f32_16x16x32_bf16 v[80:83], v[208:211], v[168:171], v[80:83]
s_add_u32 s80, s80, 524288
v_accvgpr_write_b32 a237, v236
v_mfma_f32_16x16x32_bf16 v[112:115], v[208:211], v[172:175], v[112:115]
ds_read_b64_tr_b16 v[208:209], v240 offset:59264
ds_read_b64_tr_b16 v[210:211], v240 offset:60288
v_mfma_f32_16x16x32_bf16 v[20:23], v[212:215], v[160:163], v[20:23]
v_subrev_u32 v236, 2, v239
v_ashrrev_i32 v236, 31, v236
v_mfma_f32_16x16x32_bf16 v[52:55], v[212:215], v[164:167], v[52:55]
buffer_load_dwordx4 v[156:159], v247, s[52:55], s91 offen offset:1024 sc1
v_mfma_f32_16x16x32_bf16 v[84:87], v[212:215], v[168:171], v[84:87]
v_and_b32 v236, 0xff800000, v236
v_accvgpr_write_b32 a238, v236
v_mfma_f32_16x16x32_bf16 v[116:119], v[212:215], v[172:175], v[116:119]
ds_read_b64_tr_b16 v[212:213], v241 offset:59264
ds_read_b64_tr_b16 v[214:215], v241 offset:60288
v_mfma_f32_16x16x32_bf16 v[24:27], v[216:219], v[160:163], v[24:27]
v_subrev_u32 v236, 3, v239
v_ashrrev_i32 v236, 31, v236
v_mfma_f32_16x16x32_bf16 v[56:59], v[216:219], v[164:167], v[56:59]
v_and_b32 v236, 0xff800000, v236
v_accvgpr_write_b32 a239, v236
v_mfma_f32_16x16x32_bf16 v[88:91], v[216:219], v[168:171], v[88:91]
v_lshlrev_b32 v236, 4, v239
s_mov_b32 s92, 0x3f80
v_mfma_f32_16x16x32_bf16 v[120:123], v[216:219], v[172:175], v[120:123]
ds_read_b64_tr_b16 v[216:217], v242 offset:59264
ds_read_b64_tr_b16 v[218:219], v242 offset:60288
v_mfma_f32_16x16x32_bf16 v[28:31], v[220:223], v[160:163], v[28:31]
ds_write_b128 v238, v[248:251] offset:1024
v_mfma_f32_16x16x32_bf16 v[60:63], v[220:223], v[164:167], v[60:63]
buffer_store_dwordx4 v[248:251], v255, s[48:51], s89 offen offset:1024 sc1
v_mfma_f32_16x16x32_bf16 v[92:95], v[220:223], v[168:171], v[92:95]
s_waitcnt vmcnt(15) lgkmcnt(3)
v_lshlrev_b32 v236, v236, s92
v_mfma_f32_16x16x32_bf16 v[124:127], v[220:223], v[172:175], v[124:127]
ds_read_b64_tr_b16 v[220:221], v243 offset:59264
ds_read_b64_tr_b16 v[222:223], v243 offset:60288
v_mfma_f32_16x16x32_bf16 v[0:3], v[192:195], v[176:179], v[0:3]
v_accvgpr_read_b32 v224, a64
v_accvgpr_read_b32 v225, a65
v_mfma_f32_16x16x32_bf16 v[32:35], v[192:195], v[180:183], v[32:35]
buffer_load_dwordx4 v[160:163], v244, s[52:55], s91 offen offset:2048 sc1
v_mfma_f32_16x16x32_bf16 v[64:67], v[192:195], v[184:187], v[64:67]
v_accvgpr_read_b32 v226, a66
v_accvgpr_read_b32 v227, a67
v_mfma_f32_16x16x32_bf16 v[96:99], v[192:195], v[188:191], v[96:99]
v_accvgpr_read_b32 v228, a68
v_accvgpr_read_b32 v229, a69
v_mfma_f32_16x16x32_bf16 v[4:7], v[196:199], v[176:179], v[4:7]
s_waitcnt lgkmcnt(0)
s_barrier
v_mfma_f32_16x16x32_bf16 v[36:39], v[196:199], v[180:183], v[36:39]
buffer_load_dwordx4 v[164:167], v245, s[52:55], s91 offen offset:2048 sc1
v_mfma_f32_16x16x32_bf16 v[68:71], v[196:199], v[184:187], v[68:71]
ds_read_b64_tr_b16 v[192:193], v240 offset:0
ds_read_b64_tr_b16 v[194:195], v240 offset:1024
v_mfma_f32_16x16x32_bf16 v[100:103], v[196:199], v[188:191], v[100:103]
ds_read_b64_tr_b16 v[196:197], v241 offset:0
ds_read_b64_tr_b16 v[198:199], v241 offset:1024
v_mfma_f32_16x16x32_bf16 v[8:11], v[200:203], v[176:179], v[8:11]
v_accvgpr_read_b32 v230, a70
v_accvgpr_read_b32 v231, a71
v_mfma_f32_16x16x32_bf16 v[40:43], v[200:203], v[180:183], v[40:43]
buffer_load_dwordx4 v[168:171], v246, s[52:55], s91 offen offset:2048 sc1
v_mfma_f32_16x16x32_bf16 v[72:75], v[200:203], v[184:187], v[72:75]
v_cvt_pk_bf16_f32 v248, v224, v225
v_cvt_pk_bf16_f32 v249, v226, v227
v_mfma_f32_16x16x32_bf16 v[104:107], v[200:203], v[188:191], v[104:107]
ds_read_b64_tr_b16 v[200:201], v242 offset:0
ds_read_b64_tr_b16 v[202:203], v242 offset:1024
v_mfma_f32_16x16x32_bf16 v[12:15], v[204:207], v[176:179], v[12:15]
s_add_u32 m0, s83, 50688
v_cvt_pk_bf16_f32 v250, v228, v229
v_mfma_f32_16x16x32_bf16 v[44:47], v[204:207], v[180:183], v[44:47]
buffer_load_dwordx4 v232, s[44:47], s80 offen lds
v_mfma_f32_16x16x32_bf16 v[76:79], v[204:207], v[184:187], v[76:79]
v_cvt_pk_bf16_f32 v251, v230, v231
v_cmp_gt_u32 vcc, 2, v239
v_mfma_f32_16x16x32_bf16 v[108:111], v[204:207], v[188:191], v[108:111]
ds_read_b64_tr_b16 v[204:205], v243 offset:0
ds_read_b64_tr_b16 v[206:207], v243 offset:1024
v_mfma_f32_16x16x32_bf16 v[16:19], v[208:211], v[176:179], v[16:19]
v_subrev_u32 v239, 2, v239
v_cmp_gt_u32 s[92:93], 2, v239
v_mfma_f32_16x16x32_bf16 v[48:51], v[208:211], v[180:183], v[48:51]
buffer_load_dwordx4 v[172:175], v247, s[52:55], s91 offen offset:2048 sc1
v_mfma_f32_16x16x32_bf16 v[80:83], v[208:211], v[184:187], v[80:83]
v_cndmask_b32 v239, 0, v236, vcc
v_accvgpr_write_b32 a212, 0
v_mfma_f32_16x16x32_bf16 v[112:115], v[208:211], v[188:191], v[112:115]
ds_read_b64_tr_b16 v[208:209], v240 offset:128
ds_read_b64_tr_b16 v[210:211], v240 offset:1152
v_mfma_f32_16x16x32_bf16 v[20:23], v[212:215], v[176:179], v[20:23]
s_add_u32 m0, s83, 51712
v_accvgpr_write_b32 a214, v239
v_mfma_f32_16x16x32_bf16 v[52:55], v[212:215], v[180:183], v[52:55]
buffer_load_dwordx4 v233, s[44:47], s80 offen lds
v_mfma_f32_16x16x32_bf16 v[84:87], v[212:215], v[184:187], v[84:87]
v_cndmask_b32 v239, 0, v236, s[92:93]
v_mov_b32 v236, 0x3f803f80
v_mfma_f32_16x16x32_bf16 v[116:119], v[212:215], v[188:191], v[116:119]
ds_read_b64_tr_b16 v[212:213], v241 offset:128
ds_read_b64_tr_b16 v[214:215], v241 offset:1152
v_mfma_f32_16x16x32_bf16 v[24:27], v[216:219], v[176:179], v[24:27]
v_accvgpr_write_b32 a215, v239
v_accvgpr_write_b32 a213, 0
v_mfma_f32_16x16x32_bf16 v[56:59], v[216:219], v[180:183], v[56:59]
v_accvgpr_write_b32 a216, 0
v_accvgpr_write_b32 a217, 0
v_mfma_f32_16x16x32_bf16 v[88:91], v[216:219], v[184:187], v[88:91]
v_lshrrev_b32 v239, 4, v255
v_xor_b32 v239, v239, v255
v_mfma_f32_16x16x32_bf16 v[120:123], v[216:219], v[188:191], v[120:123]
ds_read_b64_tr_b16 v[216:217], v242 offset:128
ds_read_b64_tr_b16 v[218:219], v242 offset:1152
v_mfma_f32_16x16x32_bf16 v[28:31], v[220:223], v[176:179], v[28:31]
ds_write_b128 v238, v[248:251] offset:2048
v_mfma_f32_16x16x32_bf16 v[60:63], v[220:223], v[180:183], v[60:63]
buffer_store_dwordx4 v[248:251], v255, s[48:51], s89 offen offset:2048 sc1
v_mfma_f32_16x16x32_bf16 v[92:95], v[220:223], v[184:187], v[92:95]
s_waitcnt vmcnt(16) lgkmcnt(3)
v_and_b32 v239, 32, v239
v_mfma_f32_16x16x32_bf16 v[124:127], v[220:223], v[188:191], v[124:127]
ds_read_b64_tr_b16 v[220:221], v243 offset:128
ds_read_b64_tr_b16 v[222:223], v243 offset:1152
v_mfma_f32_16x16x32_bf16 v[0:3], v[192:195], v[128:131], v[0:3]
v_accvgpr_read_b32 v224, a0
v_accvgpr_read_b32 v225, a1
v_mfma_f32_16x16x32_bf16 v[32:35], v[192:195], v[132:135], v[32:35]
buffer_load_dwordx4 v[176:179], v244, s[52:55], s91 offen offset:3072 sc1
v_mfma_f32_16x16x32_bf16 v[64:67], v[192:195], v[136:139], v[64:67]
v_accvgpr_read_b32 v226, a2
v_accvgpr_read_b32 v227, a3
v_mfma_f32_16x16x32_bf16 v[96:99], v[192:195], v[140:143], v[96:99]
ds_read_b64_tr_b16 v[192:193], v240 offset:8448
ds_read_b64_tr_b16 v[194:195], v240 offset:9472
v_mfma_f32_16x16x32_bf16 v[4:7], v[196:199], v[128:131], v[4:7]
v_accvgpr_read_b32 v228, a4
v_accvgpr_read_b32 v229, a5
v_mfma_f32_16x16x32_bf16 v[36:39], v[196:199], v[132:135], v[36:39]
buffer_load_dwordx4 v[180:183], v245, s[52:55], s91 offen offset:3072 sc1
v_mfma_f32_16x16x32_bf16 v[68:71], v[196:199], v[136:139], v[68:71]
s_waitcnt lgkmcnt(2)
v_accvgpr_read_b32 v230, a6
v_mfma_f32_16x16x32_bf16 v[100:103], v[196:199], v[140:143], v[100:103]
ds_read_b64_tr_b16 v[196:197], v241 offset:8448
ds_read_b64_tr_b16 v[198:199], v241 offset:9472
v_mfma_f32_16x16x32_bf16 v[8:11], v[200:203], v[128:131], v[8:11]
s_add_u32 m0, s83, 52864
v_accvgpr_read_b32 v231, a7
v_mfma_f32_16x16x32_bf16 v[40:43], v[200:203], v[132:135], v[40:43]
buffer_load_dwordx4 v234, s[44:47], s80 offen lds
v_mfma_f32_16x16x32_bf16 v[72:75], v[200:203], v[136:139], v[72:75]
v_cvt_pk_bf16_f32 v248, v224, v225
v_cvt_pk_bf16_f32 v249, v226, v227
v_mfma_f32_16x16x32_bf16 v[104:107], v[200:203], v[140:143], v[104:107]
ds_read_b64_tr_b16 v[200:201], v242 offset:8448
ds_read_b64_tr_b16 v[202:203], v242 offset:9472
v_mfma_f32_16x16x32_bf16 v[12:15], v[204:207], v[128:131], v[12:15]
v_cvt_pk_bf16_f32 v250, v228, v229
v_cvt_pk_bf16_f32 v251, v230, v231
v_mfma_f32_16x16x32_bf16 v[44:47], v[204:207], v[132:135], v[44:47]
buffer_load_dwordx4 v[184:187], v246, s[52:55], s91 offen offset:3072 sc1
v_mfma_f32_16x16x32_bf16 v[76:79], v[204:207], v[136:139], v[76:79]
v_cmp_eq_u32 vcc, 0, v239
s_mov_b32 s78, -1
v_mfma_f32_16x16x32_bf16 v[108:111], v[204:207], v[140:143], v[108:111]
ds_read_b64_tr_b16 v[204:205], v243 offset:8448
ds_read_b64_tr_b16 v[206:207], v243 offset:9472
v_mfma_f32_16x16x32_bf16 v[16:19], v[208:211], v[128:131], v[16:19]
s_add_u32 m0, s83, 53888
s_mul_i32 s70, s70, s22
v_mfma_f32_16x16x32_bf16 v[48:51], v[208:211], v[132:135], v[48:51]
buffer_load_dwordx4 v235, s[44:47], s80 offen lds
v_mfma_f32_16x16x32_bf16 v[80:83], v[208:211], v[136:139], v[80:83]
s_add_u32 s80, s80, 524288
v_cndmask_b32 v236, 0, v236, vcc
v_mfma_f32_16x16x32_bf16 v[112:115], v[208:211], v[140:143], v[112:115]
ds_read_b64_tr_b16 v[208:209], v240 offset:8576
ds_read_b64_tr_b16 v[210:211], v240 offset:9600
v_mfma_f32_16x16x32_bf16 v[20:23], v[212:215], v[128:131], v[20:23]
s_sub_u32 s92, 4294967295, s3
s_mul_i32 s90, s92, s21
v_mfma_f32_16x16x32_bf16 v[52:55], v[212:215], v[132:135], v[52:55]
buffer_load_dwordx4 v[188:191], v247, s[52:55], s91 offen offset:3072 sc1
v_mfma_f32_16x16x32_bf16 v[84:87], v[212:215], v[136:139], v[84:87]
s_add_u32 s91, s91, 4096
s_mov_b32 s17, 1073741824
v_mfma_f32_16x16x32_bf16 v[116:119], v[212:215], v[140:143], v[116:119]
ds_read_b64_tr_b16 v[212:213], v241 offset:8576
ds_read_b64_tr_b16 v[214:215], v241 offset:9600
v_mfma_f32_16x16x32_bf16 v[24:27], v[216:219], v[128:131], v[24:27]
s_lshl_b32 s92, s73, 2
s_lshr_b32 s92, 0x00010202, s92
v_mfma_f32_16x16x32_bf16 v[56:59], v[216:219], v[132:135], v[56:59]
s_and_b32 s92, s92, 3
s_lshl_b32 s16, s92, 16
v_mfma_f32_16x16x32_bf16 v[88:91], v[216:219], v[136:139], v[88:91]
s_mul_i32 s92, s75, 4294836224
s_add_u32 s92, s92, s88
v_mfma_f32_16x16x32_bf16 v[120:123], v[216:219], v[140:143], v[120:123]
ds_read_b64_tr_b16 v[216:217], v242 offset:8576
ds_read_b64_tr_b16 v[218:219], v242 offset:9600
v_mfma_f32_16x16x32_bf16 v[28:31], v[220:223], v[128:131], v[28:31]
ds_write_b128 v238, v[248:251] offset:3072
v_mfma_f32_16x16x32_bf16 v[60:63], v[220:223], v[132:135], v[60:63]
buffer_store_dwordx4 v[248:251], v255, s[48:51], s89 offen offset:3072 sc1
v_mfma_f32_16x16x32_bf16 v[92:95], v[220:223], v[136:139], v[92:95]
s_waitcnt vmcnt(15) lgkmcnt(3)
s_add_u32 s19, s92, 393216
v_mfma_f32_16x16x32_bf16 v[124:127], v[220:223], v[140:143], v[124:127]
ds_read_b64_tr_b16 v[220:221], v243 offset:8576
ds_read_b64_tr_b16 v[222:223], v243 offset:9600
v_mfma_f32_16x16x32_bf16 v[0:3], v[192:195], v[144:147], v[0:3]
v_accvgpr_read_b32 v224, a72
v_accvgpr_read_b32 v225, a73
v_mfma_f32_16x16x32_bf16 v[32:35], v[192:195], v[148:151], v[32:35]
buffer_load_dwordx4 v[128:131], v244, s[52:55], s91 offen offset:0 sc1
v_mfma_f32_16x16x32_bf16 v[64:67], v[192:195], v[152:155], v[64:67]
v_accvgpr_read_b32 v226, a74
v_accvgpr_read_b32 v227, a75
v_mfma_f32_16x16x32_bf16 v[96:99], v[192:195], v[156:159], v[96:99]
v_accvgpr_read_b32 v228, a76
v_accvgpr_read_b32 v229, a77
v_mfma_f32_16x16x32_bf16 v[4:7], v[196:199], v[144:147], v[4:7]
s_waitcnt lgkmcnt(0)
s_barrier
v_mfma_f32_16x16x32_bf16 v[36:39], v[196:199], v[148:151], v[36:39]
buffer_load_dwordx4 v[132:135], v245, s[52:55], s91 offen offset:0 sc1
v_mfma_f32_16x16x32_bf16 v[68:71], v[196:199], v[152:155], v[68:71]
ds_read_b64_tr_b16 v[192:193], v240 offset:16896
ds_read_b64_tr_b16 v[194:195], v240 offset:17920
v_mfma_f32_16x16x32_bf16 v[100:103], v[196:199], v[156:159], v[100:103]
ds_read_b64_tr_b16 v[196:197], v241 offset:16896
ds_read_b64_tr_b16 v[198:199], v241 offset:17920
v_mfma_f32_16x16x32_bf16 v[8:11], v[200:203], v[144:147], v[8:11]
v_accvgpr_read_b32 v230, a78
v_accvgpr_read_b32 v231, a79
v_mfma_f32_16x16x32_bf16 v[40:43], v[200:203], v[148:151], v[40:43]
buffer_load_dwordx4 v[136:139], v246, s[52:55], s91 offen offset:0 sc1
v_mfma_f32_16x16x32_bf16 v[72:75], v[200:203], v[152:155], v[72:75]
v_cvt_pk_bf16_f32 v248, v224, v225
v_cvt_pk_bf16_f32 v249, v226, v227
v_mfma_f32_16x16x32_bf16 v[104:107], v[200:203], v[156:159], v[104:107]
ds_read_b64_tr_b16 v[200:201], v242 offset:16896
ds_read_b64_tr_b16 v[202:203], v242 offset:17920
v_mfma_f32_16x16x32_bf16 v[12:15], v[204:207], v[144:147], v[12:15]
s_add_u32 m0, s83, 0
v_cvt_pk_bf16_f32 v250, v228, v229
v_mfma_f32_16x16x32_bf16 v[44:47], v[204:207], v[148:151], v[44:47]
buffer_load_dwordx4 v232, s[44:47], s80 offen lds
v_mfma_f32_16x16x32_bf16 v[76:79], v[204:207], v[152:155], v[76:79]
v_cvt_pk_bf16_f32 v251, v230, v231
s_mov_b32 s89, 20480
v_mfma_f32_16x16x32_bf16 v[108:111], v[204:207], v[156:159], v[108:111]
ds_read_b64_tr_b16 v[204:205], v243 offset:16896
ds_read_b64_tr_b16 v[206:207], v243 offset:17920
v_mfma_f32_16x16x32_bf16 v[16:19], v[208:211], v[144:147], v[16:19]
v_accvgpr_write_b32 a208, v236
v_accvgpr_write_b32 a209, v236
v_mfma_f32_16x16x32_bf16 v[48:51], v[208:211], v[148:151], v[48:51]
buffer_load_dwordx4 v[140:143], v247, s[52:55], s91 offen offset:0 sc1
v_mfma_f32_16x16x32_bf16 v[80:83], v[208:211], v[152:155], v[80:83]
v_accvgpr_write_b32 a210, v236
v_accvgpr_write_b32 a211, v236
v_mfma_f32_16x16x32_bf16 v[112:115], v[208:211], v[156:159], v[112:115]
ds_read_b64_tr_b16 v[208:209], v240 offset:17024
ds_read_b64_tr_b16 v[210:211], v240 offset:18048
v_mfma_f32_16x16x32_bf16 v[20:23], v[212:215], v[144:147], v[20:23]
s_add_u32 m0, s83, 1024
v_accvgpr_write_b32 a232, 0
v_mfma_f32_16x16x32_bf16 v[52:55], v[212:215], v[148:151], v[52:55]
buffer_load_dwordx4 v233, s[44:47], s80 offen lds
v_mfma_f32_16x16x32_bf16 v[84:87], v[212:215], v[152:155], v[84:87]
v_accvgpr_write_b32 a233, 0
v_accvgpr_write_b32 a234, 0
v_mfma_f32_16x16x32_bf16 v[116:119], v[212:215], v[156:159], v[116:119]
ds_read_b64_tr_b16 v[212:213], v241 offset:17024
ds_read_b64_tr_b16 v[214:215], v241 offset:18048
v_mfma_f32_16x16x32_bf16 v[24:27], v[216:219], v[144:147], v[24:27]
v_accvgpr_write_b32 a235, 0
s_lshl_b32 s20, s72, 8
v_mfma_f32_16x16x32_bf16 v[56:59], v[216:219], v[148:151], v[56:59]
s_add_u32 s20, s20, 16384
s_mov_b32 s4, 1985221408
v_mfma_f32_16x16x32_bf16 v[88:91], v[216:219], v[152:155], v[88:91]
s_cmp_ge_u32 s2, 64
s_cselect_b32 s4, 1985225488, s4
v_mfma_f32_16x16x32_bf16 v[120:123], v[216:219], v[156:159], v[120:123]
ds_read_b64_tr_b16 v[216:217], v242 offset:17024
ds_read_b64_tr_b16 v[218:219], v242 offset:18048
v_mfma_f32_16x16x32_bf16 v[28:31], v[220:223], v[144:147], v[28:31]
ds_write_b128 v238, v[248:251] offset:4096
v_mfma_f32_16x16x32_bf16 v[60:63], v[220:223], v[148:151], v[60:63]
buffer_store_dwordx4 v[248:251], v255, s[48:51], s89 offen offset:0 sc1
v_mfma_f32_16x16x32_bf16 v[92:95], v[220:223], v[152:155], v[92:95]
s_waitcnt vmcnt(16) lgkmcnt(3)
v_mfma_f32_16x16x32_bf16 v[124:127], v[220:223], v[156:159], v[124:127]
ds_read_b64_tr_b16 v[220:221], v243 offset:17024
ds_read_b64_tr_b16 v[222:223], v243 offset:18048
v_mfma_f32_16x16x32_bf16 v[0:3], v[192:195], v[160:163], v[0:3]
v_accvgpr_read_b32 v224, a8
v_accvgpr_read_b32 v225, a9
v_mfma_f32_16x16x32_bf16 v[32:35], v[192:195], v[164:167], v[32:35]
buffer_load_dwordx4 v[144:147], v244, s[52:55], s91 offen offset:1024 sc1
v_mfma_f32_16x16x32_bf16 v[64:67], v[192:195], v[168:171], v[64:67]
v_accvgpr_read_b32 v226, a10
v_accvgpr_read_b32 v227, a11
v_mfma_f32_16x16x32_bf16 v[96:99], v[192:195], v[172:175], v[96:99]
ds_read_b64_tr_b16 v[192:193], v240 offset:25344
ds_read_b64_tr_b16 v[194:195], v240 offset:26368
v_mfma_f32_16x16x32_bf16 v[4:7], v[196:199], v[160:163], v[4:7]
v_accvgpr_read_b32 v228, a12
v_accvgpr_read_b32 v229, a13
v_mfma_f32_16x16x32_bf16 v[36:39], v[196:199], v[164:167], v[36:39]
buffer_load_dwordx4 v[148:151], v245, s[52:55], s91 offen offset:1024 sc1
v_mfma_f32_16x16x32_bf16 v[68:71], v[196:199], v[168:171], v[68:71]
s_waitcnt lgkmcnt(2)
v_accvgpr_read_b32 v230, a14
v_mfma_f32_16x16x32_bf16 v[100:103], v[196:199], v[172:175], v[100:103]
ds_read_b64_tr_b16 v[196:197], v241 offset:25344
ds_read_b64_tr_b16 v[198:199], v241 offset:26368
v_mfma_f32_16x16x32_bf16 v[8:11], v[200:203], v[160:163], v[8:11]
s_add_u32 m0, s83, 2176
v_accvgpr_read_b32 v231, a15
v_mfma_f32_16x16x32_bf16 v[40:43], v[200:203], v[164:167], v[40:43]
buffer_load_dwordx4 v234, s[44:47], s80 offen lds
v_mfma_f32_16x16x32_bf16 v[72:75], v[200:203], v[168:171], v[72:75]
v_cvt_pk_bf16_f32 v248, v224, v225
v_cvt_pk_bf16_f32 v249, v226, v227
v_mfma_f32_16x16x32_bf16 v[104:107], v[200:203], v[172:175], v[104:107]
ds_read_b64_tr_b16 v[200:201], v242 offset:25344
ds_read_b64_tr_b16 v[202:203], v242 offset:26368
v_mfma_f32_16x16x32_bf16 v[12:15], v[204:207], v[160:163], v[12:15]
v_cvt_pk_bf16_f32 v250, v228, v229
v_cvt_pk_bf16_f32 v251, v230, v231
v_mfma_f32_16x16x32_bf16 v[44:47], v[204:207], v[164:167], v[44:47]
buffer_load_dwordx4 v[152:155], v246, s[52:55], s91 offen offset:1024 sc1
v_mfma_f32_16x16x32_bf16 v[76:79], v[204:207], v[168:171], v[76:79]
s_cmp_ge_u32 s2, 128
s_cselect_b32 s4, 1985229328, s4
v_mfma_f32_16x16x32_bf16 v[108:111], v[204:207], v[172:175], v[108:111]
ds_read_b64_tr_b16 v[204:205], v243 offset:25344
ds_read_b64_tr_b16 v[206:207], v243 offset:26368
v_mfma_f32_16x16x32_bf16 v[16:19], v[208:211], v[160:163], v[16:19]
s_add_u32 m0, s83, 3200
v_mfma_f32_16x16x32_bf16 v[48:51], v[208:211], v[164:167], v[48:51]
buffer_load_dwordx4 v235, s[44:47], s80 offen lds
v_mfma_f32_16x16x32_bf16 v[80:83], v[208:211], v[168:171], v[80:83]
s_add_u32 s80, s80, 524288
v_mfma_f32_16x16x32_bf16 v[112:115], v[208:211], v[172:175], v[112:115]
ds_read_b64_tr_b16 v[208:209], v240 offset:25472
ds_read_b64_tr_b16 v[210:211], v240 offset:26496
v_mfma_f32_16x16x32_bf16 v[20:23], v[212:215], v[160:163], v[20:23]
s_cmp_ge_u32 s2, 192
s_cselect_b32 s4, 1985225008, s4
v_mfma_f32_16x16x32_bf16 v[52:55], v[212:215], v[164:167], v[52:55]
buffer_load_dwordx4 v[156:159], v247, s[52:55], s91 offen offset:1024 sc1
v_mfma_f32_16x16x32_bf16 v[84:87], v[212:215], v[168:171], v[84:87]
v_bfe_u32 v236, v255, 4, 3
v_lshlrev_b32 v236, 2, v236
v_mfma_f32_16x16x32_bf16 v[116:119], v[212:215], v[172:175], v[116:119]
ds_read_b64_tr_b16 v[212:213], v241 offset:25472
ds_read_b64_tr_b16 v[214:215], v241 offset:26496
v_mfma_f32_16x16x32_bf16 v[24:27], v[216:219], v[160:163], v[24:27]
v_lshrrev_b32 v236, v236, s4
v_and_b32 v236, 15, v236
v_mfma_f32_16x16x32_bf16 v[56:59], v[216:219], v[164:167], v[56:59]
v_xor_b32 v236, s72, v236
v_bfe_u32 v239, v255, 7, 1
v_mfma_f32_16x16x32_bf16 v[88:91], v[216:219], v[168:171], v[88:91]
v_lshlrev_b32 v239, 5, v239
v_add_lshl_u32 v236, v236, v239, 3
v_mfma_f32_16x16x32_bf16 v[120:123], v[216:219], v[172:175], v[120:123]
ds_read_b64_tr_b16 v[216:217], v242 offset:25472
ds_read_b64_tr_b16 v[218:219], v242 offset:26496
v_mfma_f32_16x16x32_bf16 v[28:31], v[220:223], v[160:163], v[28:31]
ds_write_b128 v238, v[248:251] offset:5120
v_mfma_f32_16x16x32_bf16 v[60:63], v[220:223], v[164:167], v[60:63]
buffer_store_dwordx4 v[248:251], v255, s[48:51], s89 offen offset:1024 sc1
v_mfma_f32_16x16x32_bf16 v[92:95], v[220:223], v[168:171], v[92:95]
s_waitcnt vmcnt(15) lgkmcnt(3)
v_accvgpr_write_b32 a225, v236
v_mfma_f32_16x16x32_bf16 v[124:127], v[220:223], v[172:175], v[124:127]
ds_read_b64_tr_b16 v[220:221], v243 offset:25472
ds_read_b64_tr_b16 v[222:223], v243 offset:26496
v_mfma_f32_16x16x32_bf16 v[0:3], v[192:195], v[176:179], v[0:3]
v_accvgpr_read_b32 v224, a80
v_accvgpr_read_b32 v225, a81
v_mfma_f32_16x16x32_bf16 v[32:35], v[192:195], v[180:183], v[32:35]
buffer_load_dwordx4 v[160:163], v244, s[52:55], s91 offen offset:2048 sc1
v_mfma_f32_16x16x32_bf16 v[64:67], v[192:195], v[184:187], v[64:67]
v_accvgpr_read_b32 v226, a82
v_accvgpr_read_b32 v227, a83
v_mfma_f32_16x16x32_bf16 v[96:99], v[192:195], v[188:191], v[96:99]
v_accvgpr_read_b32 v228, a84
v_accvgpr_read_b32 v229, a85
v_mfma_f32_16x16x32_bf16 v[4:7], v[196:199], v[176:179], v[4:7]
s_waitcnt lgkmcnt(0)
s_barrier
v_mfma_f32_16x16x32_bf16 v[36:39], v[196:199], v[180:183], v[36:39]
buffer_load_dwordx4 v[164:167], v245, s[52:55], s91 offen offset:2048 sc1
v_mfma_f32_16x16x32_bf16 v[68:71], v[196:199], v[184:187], v[68:71]
ds_read_b64_tr_b16 v[192:193], v240 offset:33792
ds_read_b64_tr_b16 v[194:195], v240 offset:34816
v_mfma_f32_16x16x32_bf16 v[100:103], v[196:199], v[188:191], v[100:103]
ds_read_b64_tr_b16 v[196:197], v241 offset:33792
ds_read_b64_tr_b16 v[198:199], v241 offset:34816
v_mfma_f32_16x16x32_bf16 v[8:11], v[200:203], v[176:179], v[8:11]
v_accvgpr_read_b32 v230, a86
v_accvgpr_read_b32 v231, a87
v_mfma_f32_16x16x32_bf16 v[40:43], v[200:203], v[180:183], v[40:43]
buffer_load_dwordx4 v[168:171], v246, s[52:55], s91 offen offset:2048 sc1
v_mfma_f32_16x16x32_bf16 v[72:75], v[200:203], v[184:187], v[72:75]
v_cvt_pk_bf16_f32 v248, v224, v225
v_cvt_pk_bf16_f32 v249, v226, v227
v_mfma_f32_16x16x32_bf16 v[104:107], v[200:203], v[188:191], v[104:107]
ds_read_b64_tr_b16 v[200:201], v242 offset:33792
ds_read_b64_tr_b16 v[202:203], v242 offset:34816
v_mfma_f32_16x16x32_bf16 v[12:15], v[204:207], v[176:179], v[12:15]
s_add_u32 m0, s83, 16896
v_cvt_pk_bf16_f32 v250, v228, v229
v_mfma_f32_16x16x32_bf16 v[44:47], v[204:207], v[180:183], v[44:47]
buffer_load_dwordx4 v232, s[44:47], s80 offen lds
v_mfma_f32_16x16x32_bf16 v[76:79], v[204:207], v[184:187], v[76:79]
v_cvt_pk_bf16_f32 v251, v230, v231
v_accvgpr_write_b32 a12, v248
v_mfma_f32_16x16x32_bf16 v[108:111], v[204:207], v[188:191], v[108:111]
.La5_LW1_9:
s_cbranch_execz .La5_LW1_R0
ds_read_b64_tr_b16 v[204:205], v243 offset:33792
ds_read_b64_tr_b16 v[206:207], v243 offset:34816
v_mfma_f32_16x16x32_bf16 v[16:19], v[208:211], v[176:179], v[16:19]
v_accvgpr_write_b32 a13, v249
v_accvgpr_write_b32 a14, v250
v_mfma_f32_16x16x32_bf16 v[48:51], v[208:211], v[180:183], v[48:51]
buffer_load_dwordx4 v[172:175], v247, s[52:55], s91 offen offset:2048 sc1
v_mfma_f32_16x16x32_bf16 v[80:83], v[208:211], v[184:187], v[80:83]
v_accvgpr_write_b32 a15, v251
s_lshl3_add_u32 s5, s76, s101
v_mfma_f32_16x16x32_bf16 v[112:115], v[208:211], v[188:191], v[112:115]
ds_read_b64_tr_b16 v[208:209], v240 offset:33920
ds_read_b64_tr_b16 v[210:211], v240 offset:34944
v_mfma_f32_16x16x32_bf16 v[20:23], v[212:215], v[176:179], v[20:23]
s_add_u32 m0, s83, 17920
s_lshl2_add_u32 s5, s5, s3
v_mfma_f32_16x16x32_bf16 v[52:55], v[212:215], v[180:183], v[52:55]
buffer_load_dwordx4 v233, s[44:47], s80 offen lds
v_mfma_f32_16x16x32_bf16 v[84:87], v[212:215], v[184:187], v[84:87]
s_lshl_b32 s5, s5, 9
s_add_u32 s5, s5, 524288
v_mfma_f32_16x16x32_bf16 v[116:119], v[212:215], v[188:191], v[116:119]
ds_read_b64_tr_b16 v[212:213], v241 offset:33920
ds_read_b64_tr_b16 v[214:215], v241 offset:34944
v_mfma_f32_16x16x32_bf16 v[24:27], v[216:219], v[176:179], v[24:27]
v_mfma_f32_16x16x32_bf16 v[56:59], v[216:219], v[180:183], v[56:59]
v_mfma_f32_16x16x32_bf16 v[88:91], v[216:219], v[184:187], v[88:91]
v_mfma_f32_16x16x32_bf16 v[120:123], v[216:219], v[188:191], v[120:123]
ds_read_b64_tr_b16 v[216:217], v242 offset:33920
ds_read_b64_tr_b16 v[218:219], v242 offset:34944
v_mfma_f32_16x16x32_bf16 v[28:31], v[220:223], v[176:179], v[28:31]
v_mfma_f32_16x16x32_bf16 v[60:63], v[220:223], v[180:183], v[60:63]
buffer_store_dwordx4 v[248:251], v255, s[48:51], s89 offen offset:2048 sc1
v_mfma_f32_16x16x32_bf16 v[92:95], v[220:223], v[184:187], v[92:95]
s_waitcnt vmcnt(16) lgkmcnt(2)
v_mfma_f32_16x16x32_bf16 v[124:127], v[220:223], v[188:191], v[124:127]
ds_read_b64_tr_b16 v[220:221], v243 offset:33920
ds_read_b64_tr_b16 v[222:223], v243 offset:34944
v_mfma_f32_16x16x32_bf16 v[0:3], v[192:195], v[128:131], v[0:3]
v_accvgpr_read_b32 v224, a16
v_accvgpr_read_b32 v225, a17
v_mfma_f32_16x16x32_bf16 v[32:35], v[192:195], v[132:135], v[32:35]
buffer_load_dwordx4 v[176:179], v244, s[52:55], s91 offen offset:3072 sc1
v_mfma_f32_16x16x32_bf16 v[64:67], v[192:195], v[136:139], v[64:67]
v_accvgpr_read_b32 v226, a18
v_accvgpr_read_b32 v227, a19
v_mfma_f32_16x16x32_bf16 v[96:99], v[192:195], v[140:143], v[96:99]
ds_read_b64_tr_b16 v[192:193], v240 offset:42240
ds_read_b64_tr_b16 v[194:195], v240 offset:43264
v_mfma_f32_16x16x32_bf16 v[4:7], v[196:199], v[128:131], v[4:7]
v_accvgpr_read_b32 v228, a20
v_accvgpr_read_b32 v229, a21
v_mfma_f32_16x16x32_bf16 v[36:39], v[196:199], v[132:135], v[36:39]
buffer_load_dwordx4 v[180:183], v245, s[52:55], s91 offen offset:3072 sc1
v_mfma_f32_16x16x32_bf16 v[68:71], v[196:199], v[136:139], v[68:71]
s_waitcnt lgkmcnt(2)
v_accvgpr_read_b32 v230, a22
v_mfma_f32_16x16x32_bf16 v[100:103], v[196:199], v[140:143], v[100:103]
ds_read_b64_tr_b16 v[196:197], v241 offset:42240
ds_read_b64_tr_b16 v[198:199], v241 offset:43264
v_mfma_f32_16x16x32_bf16 v[8:11], v[200:203], v[128:131], v[8:11]
s_add_u32 m0, s83, 19072
v_accvgpr_read_b32 v231, a23
v_mfma_f32_16x16x32_bf16 v[40:43], v[200:203], v[132:135], v[40:43]
buffer_load_dwordx4 v234, s[44:47], s80 offen lds
v_mfma_f32_16x16x32_bf16 v[72:75], v[200:203], v[136:139], v[72:75]
v_cvt_pk_bf16_f32 v248, v224, v225
v_cvt_pk_bf16_f32 v249, v226, v227
v_mfma_f32_16x16x32_bf16 v[104:107], v[200:203], v[140:143], v[104:107]
ds_read_b64_tr_b16 v[200:201], v242 offset:42240
ds_read_b64_tr_b16 v[202:203], v242 offset:43264
v_mfma_f32_16x16x32_bf16 v[12:15], v[204:207], v[128:131], v[12:15]
v_cvt_pk_bf16_f32 v250, v228, v229
v_cvt_pk_bf16_f32 v251, v230, v231
v_mfma_f32_16x16x32_bf16 v[44:47], v[204:207], v[132:135], v[44:47]
buffer_load_dwordx4 v[184:187], v246, s[52:55], s91 offen offset:3072 sc1
v_mfma_f32_16x16x32_bf16 v[76:79], v[204:207], v[136:139], v[76:79]
v_accvgpr_write_b32 a16, v248
v_accvgpr_write_b32 a17, v249
v_mfma_f32_16x16x32_bf16 v[108:111], v[204:207], v[140:143], v[108:111]
ds_read_b64_tr_b16 v[204:205], v243 offset:42240
ds_read_b64_tr_b16 v[206:207], v243 offset:43264
v_mfma_f32_16x16x32_bf16 v[16:19], v[208:211], v[128:131], v[16:19]
s_add_u32 m0, s83, 20096
v_accvgpr_write_b32 a18, v250
v_mfma_f32_16x16x32_bf16 v[48:51], v[208:211], v[132:135], v[48:51]
buffer_load_dwordx4 v235, s[44:47], s80 offen lds
v_mfma_f32_16x16x32_bf16 v[80:83], v[208:211], v[136:139], v[80:83]
s_add_u32 s80, s80, 524288
v_accvgpr_write_b32 a19, v251
v_mfma_f32_16x16x32_bf16 v[112:115], v[208:211], v[140:143], v[112:115]
ds_read_b64_tr_b16 v[208:209], v240 offset:42368
ds_read_b64_tr_b16 v[210:211], v240 offset:43392
v_mfma_f32_16x16x32_bf16 v[20:23], v[212:215], v[128:131], v[20:23]
v_mfma_f32_16x16x32_bf16 v[52:55], v[212:215], v[132:135], v[52:55]
buffer_load_dwordx4 v[188:191], v247, s[52:55], s91 offen offset:3072 sc1
v_mfma_f32_16x16x32_bf16 v[84:87], v[212:215], v[136:139], v[84:87]
s_add_u32 s91, s91, 4096
v_mfma_f32_16x16x32_bf16 v[116:119], v[212:215], v[140:143], v[116:119]
ds_read_b64_tr_b16 v[212:213], v241 offset:42368
ds_read_b64_tr_b16 v[214:215], v241 offset:43392
v_mfma_f32_16x16x32_bf16 v[24:27], v[216:219], v[128:131], v[24:27]
v_mfma_f32_16x16x32_bf16 v[56:59], v[216:219], v[132:135], v[56:59]
v_mfma_f32_16x16x32_bf16 v[88:91], v[216:219], v[136:139], v[88:91]
v_mfma_f32_16x16x32_bf16 v[120:123], v[216:219], v[140:143], v[120:123]
ds_read_b64_tr_b16 v[216:217], v242 offset:42368
ds_read_b64_tr_b16 v[218:219], v242 offset:43392
v_mfma_f32_16x16x32_bf16 v[28:31], v[220:223], v[128:131], v[28:31]
v_mfma_f32_16x16x32_bf16 v[60:63], v[220:223], v[132:135], v[60:63]
buffer_store_dwordx4 v[248:251], v255, s[48:51], s89 offen offset:3072 sc1
v_mfma_f32_16x16x32_bf16 v[92:95], v[220:223], v[136:139], v[92:95]
s_waitcnt vmcnt(15) lgkmcnt(2)
v_mfma_f32_16x16x32_bf16 v[124:127], v[220:223], v[140:143], v[124:127]
ds_read_b64_tr_b16 v[220:221], v243 offset:42368
ds_read_b64_tr_b16 v[222:223], v243 offset:43392
v_mfma_f32_16x16x32_bf16 v[0:3], v[192:195], v[144:147], v[0:3]
v_accvgpr_read_b32 v224, a88
v_accvgpr_read_b32 v225, a89
v_mfma_f32_16x16x32_bf16 v[32:35], v[192:195], v[148:151], v[32:35]
buffer_load_dwordx4 v[128:131], v244, s[52:55], s91 offen offset:0 sc1
v_mfma_f32_16x16x32_bf16 v[64:67], v[192:195], v[152:155], v[64:67]
v_accvgpr_read_b32 v226, a90
v_accvgpr_read_b32 v227, a91
v_mfma_f32_16x16x32_bf16 v[96:99], v[192:195], v[156:159], v[96:99]
v_accvgpr_read_b32 v228, a92
v_accvgpr_read_b32 v229, a93
v_mfma_f32_16x16x32_bf16 v[4:7], v[196:199], v[144:147], v[4:7]
s_waitcnt lgkmcnt(0)
s_barrier
v_mfma_f32_16x16x32_bf16 v[36:39], v[196:199], v[148:151], v[36:39]
buffer_load_dwordx4 v[132:135], v245, s[52:55], s91 offen offset:0 sc1
v_mfma_f32_16x16x32_bf16 v[68:71], v[196:199], v[152:155], v[68:71]
ds_read_b64_tr_b16 v[192:193], v240 offset:50688
ds_read_b64_tr_b16 v[194:195], v240 offset:51712
v_mfma_f32_16x16x32_bf16 v[100:103], v[196:199], v[156:159], v[100:103]
ds_read_b64_tr_b16 v[196:197], v241 offset:50688
ds_read_b64_tr_b16 v[198:199], v241 offset:51712
v_mfma_f32_16x16x32_bf16 v[8:11], v[200:203], v[144:147], v[8:11]
v_accvgpr_read_b32 v230, a94
v_accvgpr_read_b32 v231, a95
v_mfma_f32_16x16x32_bf16 v[40:43], v[200:203], v[148:151], v[40:43]
buffer_load_dwordx4 v[136:139], v246, s[52:55], s91 offen offset:0 sc1
v_mfma_f32_16x16x32_bf16 v[72:75], v[200:203], v[152:155], v[72:75]
v_cvt_pk_bf16_f32 v248, v224, v225
v_cvt_pk_bf16_f32 v249, v226, v227
v_mfma_f32_16x16x32_bf16 v[104:107], v[200:203], v[156:159], v[104:107]
ds_read_b64_tr_b16 v[200:201], v242 offset:50688
ds_read_b64_tr_b16 v[202:203], v242 offset:51712
v_mfma_f32_16x16x32_bf16 v[12:15], v[204:207], v[144:147], v[12:15]
s_add_u32 m0, s83, 33792
v_cvt_pk_bf16_f32 v250, v228, v229
v_mfma_f32_16x16x32_bf16 v[44:47], v[204:207], v[148:151], v[44:47]
buffer_load_dwordx4 v232, s[44:47], s80 offen lds
v_mfma_f32_16x16x32_bf16 v[76:79], v[204:207], v[152:155], v[76:79]
v_cvt_pk_bf16_f32 v251, v230, v231
v_accvgpr_write_b32 a20, v248
v_mfma_f32_16x16x32_bf16 v[108:111], v[204:207], v[156:159], v[108:111]
ds_read_b64_tr_b16 v[204:205], v243 offset:50688
ds_read_b64_tr_b16 v[206:207], v243 offset:51712
v_mfma_f32_16x16x32_bf16 v[16:19], v[208:211], v[144:147], v[16:19]
v_accvgpr_write_b32 a21, v249
v_accvgpr_write_b32 a22, v250
v_mfma_f32_16x16x32_bf16 v[48:51], v[208:211], v[148:151], v[48:51]
buffer_load_dwordx4 v[140:143], v247, s[52:55], s91 offen offset:0 sc1
v_mfma_f32_16x16x32_bf16 v[80:83], v[208:211], v[152:155], v[80:83]
v_accvgpr_write_b32 a23, v251
s_mov_b32 s89, 0
v_mfma_f32_16x16x32_bf16 v[112:115], v[208:211], v[156:159], v[112:115]
ds_read_b64_tr_b16 v[208:209], v240 offset:50816
ds_read_b64_tr_b16 v[210:211], v240 offset:51840
v_mfma_f32_16x16x32_bf16 v[20:23], v[212:215], v[144:147], v[20:23]
s_add_u32 m0, s83, 34816
v_mfma_f32_16x16x32_bf16 v[52:55], v[212:215], v[148:151], v[52:55]
buffer_load_dwordx4 v233, s[44:47], s80 offen lds
v_mfma_f32_16x16x32_bf16 v[84:87], v[212:215], v[152:155], v[84:87]
v_mfma_f32_16x16x32_bf16 v[116:119], v[212:215], v[156:159], v[116:119]
ds_read_b64_tr_b16 v[212:213], v241 offset:50816
ds_read_b64_tr_b16 v[214:215], v241 offset:51840
v_mfma_f32_16x16x32_bf16 v[24:27], v[216:219], v[144:147], v[24:27]
v_mfma_f32_16x16x32_bf16 v[56:59], v[216:219], v[148:151], v[56:59]
v_mfma_f32_16x16x32_bf16 v[88:91], v[216:219], v[152:155], v[88:91]
v_mfma_f32_16x16x32_bf16 v[120:123], v[216:219], v[156:159], v[120:123]
ds_read_b64_tr_b16 v[216:217], v242 offset:50816
ds_read_b64_tr_b16 v[218:219], v242 offset:51840
v_mfma_f32_16x16x32_bf16 v[28:31], v[220:223], v[144:147], v[28:31]
v_mfma_f32_16x16x32_bf16 v[60:63], v[220:223], v[148:151], v[60:63]
buffer_store_dwordx4 v[248:251], v255, s[48:51], s89 offen offset:0 sc1
v_mfma_f32_16x16x32_bf16 v[92:95], v[220:223], v[152:155], v[92:95]
s_waitcnt vmcnt(16) lgkmcnt(2)
v_mfma_f32_16x16x32_bf16 v[124:127], v[220:223], v[156:159], v[124:127]
ds_read_b64_tr_b16 v[220:221], v243 offset:50816
ds_read_b64_tr_b16 v[222:223], v243 offset:51840
v_mfma_f32_16x16x32_bf16 v[0:3], v[192:195], v[160:163], v[0:3]
v_accvgpr_read_b32 v224, a24
v_accvgpr_read_b32 v225, a25
v_mfma_f32_16x16x32_bf16 v[32:35], v[192:195], v[164:167], v[32:35]
buffer_load_dwordx4 v[144:147], v244, s[52:55], s91 offen offset:1024 sc1
v_mfma_f32_16x16x32_bf16 v[64:67], v[192:195], v[168:171], v[64:67]
v_accvgpr_read_b32 v226, a26
v_accvgpr_read_b32 v227, a27
v_mfma_f32_16x16x32_bf16 v[96:99], v[192:195], v[172:175], v[96:99]
ds_read_b64_tr_b16 v[192:193], v240 offset:59136
ds_read_b64_tr_b16 v[194:195], v240 offset:60160
v_mfma_f32_16x16x32_bf16 v[4:7], v[196:199], v[160:163], v[4:7]
v_accvgpr_read_b32 v228, a28
v_accvgpr_read_b32 v229, a29
v_mfma_f32_16x16x32_bf16 v[36:39], v[196:199], v[164:167], v[36:39]
buffer_load_dwordx4 v[148:151], v245, s[52:55], s91 offen offset:1024 sc1
v_mfma_f32_16x16x32_bf16 v[68:71], v[196:199], v[168:171], v[68:71]
s_waitcnt lgkmcnt(2)
v_accvgpr_read_b32 v230, a30
v_mfma_f32_16x16x32_bf16 v[100:103], v[196:199], v[172:175], v[100:103]
ds_read_b64_tr_b16 v[196:197], v241 offset:59136
ds_read_b64_tr_b16 v[198:199], v241 offset:60160
v_mfma_f32_16x16x32_bf16 v[8:11], v[200:203], v[160:163], v[8:11]
s_add_u32 m0, s83, 35968
v_accvgpr_read_b32 v231, a31
v_mfma_f32_16x16x32_bf16 v[40:43], v[200:203], v[164:167], v[40:43]
buffer_load_dwordx4 v234, s[44:47], s80 offen lds
v_mfma_f32_16x16x32_bf16 v[72:75], v[200:203], v[168:171], v[72:75]
v_cvt_pk_bf16_f32 v248, v224, v225
v_cvt_pk_bf16_f32 v249, v226, v227
v_mfma_f32_16x16x32_bf16 v[104:107], v[200:203], v[172:175], v[104:107]
ds_read_b64_tr_b16 v[200:201], v242 offset:59136
ds_read_b64_tr_b16 v[202:203], v242 offset:60160
v_mfma_f32_16x16x32_bf16 v[12:15], v[204:207], v[160:163], v[12:15]
v_cvt_pk_bf16_f32 v250, v228, v229
v_cvt_pk_bf16_f32 v251, v230, v231
v_mfma_f32_16x16x32_bf16 v[44:47], v[204:207], v[164:167], v[44:47]
buffer_load_dwordx4 v[152:155], v246, s[52:55], s91 offen offset:1024 sc1
v_mfma_f32_16x16x32_bf16 v[76:79], v[204:207], v[168:171], v[76:79]
v_accvgpr_write_b32 a24, v248
v_accvgpr_write_b32 a25, v249
v_mfma_f32_16x16x32_bf16 v[108:111], v[204:207], v[172:175], v[108:111]
ds_read_b64_tr_b16 v[204:205], v243 offset:59136
ds_read_b64_tr_b16 v[206:207], v243 offset:60160
v_mfma_f32_16x16x32_bf16 v[16:19], v[208:211], v[160:163], v[16:19]
s_add_u32 m0, s83, 36992
v_accvgpr_write_b32 a26, v250
v_mfma_f32_16x16x32_bf16 v[48:51], v[208:211], v[164:167], v[48:51]
buffer_load_dwordx4 v235, s[44:47], s80 offen lds
v_mfma_f32_16x16x32_bf16 v[80:83], v[208:211], v[168:171], v[80:83]
s_add_u32 s80, s80, 524288
v_accvgpr_write_b32 a27, v251
v_mfma_f32_16x16x32_bf16 v[112:115], v[208:211], v[172:175], v[112:115]
ds_read_b64_tr_b16 v[208:209], v240 offset:59264
ds_read_b64_tr_b16 v[210:211], v240 offset:60288
v_mfma_f32_16x16x32_bf16 v[20:23], v[212:215], v[160:163], v[20:23]
v_mfma_f32_16x16x32_bf16 v[52:55], v[212:215], v[164:167], v[52:55]
buffer_load_dwordx4 v[156:159], v247, s[52:55], s91 offen offset:1024 sc1
v_mfma_f32_16x16x32_bf16 v[84:87], v[212:215], v[168:171], v[84:87]
v_mfma_f32_16x16x32_bf16 v[116:119], v[212:215], v[172:175], v[116:119]
ds_read_b64_tr_b16 v[212:213], v241 offset:59264
ds_read_b64_tr_b16 v[214:215], v241 offset:60288
v_mfma_f32_16x16x32_bf16 v[24:27], v[216:219], v[160:163], v[24:27]
v_mfma_f32_16x16x32_bf16 v[56:59], v[216:219], v[164:167], v[56:59]
v_mfma_f32_16x16x32_bf16 v[88:91], v[216:219], v[168:171], v[88:91]
v_mfma_f32_16x16x32_bf16 v[120:123], v[216:219], v[172:175], v[120:123]
ds_read_b64_tr_b16 v[216:217], v242 offset:59264
ds_read_b64_tr_b16 v[218:219], v242 offset:60288
v_mfma_f32_16x16x32_bf16 v[28:31], v[220:223], v[160:163], v[28:31]
v_mfma_f32_16x16x32_bf16 v[60:63], v[220:223], v[164:167], v[60:63]
buffer_store_dwordx4 v[248:251], v255, s[48:51], s89 offen offset:1024 sc1
v_mfma_f32_16x16x32_bf16 v[92:95], v[220:223], v[168:171], v[92:95]
s_waitcnt vmcnt(15) lgkmcnt(2)
v_mfma_f32_16x16x32_bf16 v[124:127], v[220:223], v[172:175], v[124:127]
ds_read_b64_tr_b16 v[220:221], v243 offset:59264
ds_read_b64_tr_b16 v[222:223], v243 offset:60288
v_mfma_f32_16x16x32_bf16 v[0:3], v[192:195], v[176:179], v[0:3]
v_accvgpr_read_b32 v224, a240
v_accvgpr_read_b32 v225, a241
v_mfma_f32_16x16x32_bf16 v[32:35], v[192:195], v[180:183], v[32:35]
buffer_load_dwordx4 v[160:163], v244, s[52:55], s91 offen offset:2048 sc1
v_mfma_f32_16x16x32_bf16 v[64:67], v[192:195], v[184:187], v[64:67]
v_accvgpr_read_b32 v226, a242
v_accvgpr_read_b32 v227, a243
v_mfma_f32_16x16x32_bf16 v[96:99], v[192:195], v[188:191], v[96:99]
v_accvgpr_read_b32 v228, a244
v_accvgpr_read_b32 v229, a245
v_mfma_f32_16x16x32_bf16 v[4:7], v[196:199], v[176:179], v[4:7]
s_waitcnt lgkmcnt(0)
s_barrier
v_mfma_f32_16x16x32_bf16 v[36:39], v[196:199], v[180:183], v[36:39]
buffer_load_dwordx4 v[164:167], v245, s[52:55], s91 offen offset:2048 sc1
v_mfma_f32_16x16x32_bf16 v[68:71], v[196:199], v[184:187], v[68:71]
ds_read_b64_tr_b16 v[192:193], v240 offset:0
ds_read_b64_tr_b16 v[194:195], v240 offset:1024
v_mfma_f32_16x16x32_bf16 v[100:103], v[196:199], v[188:191], v[100:103]
ds_read_b64_tr_b16 v[196:197], v241 offset:0
ds_read_b64_tr_b16 v[198:199], v241 offset:1024
v_mfma_f32_16x16x32_bf16 v[8:11], v[200:203], v[176:179], v[8:11]
v_accvgpr_read_b32 v230, a246
v_accvgpr_read_b32 v231, a247
v_mfma_f32_16x16x32_bf16 v[40:43], v[200:203], v[180:183], v[40:43]
buffer_load_dwordx4 v[168:171], v246, s[52:55], s91 offen offset:2048 sc1
v_mfma_f32_16x16x32_bf16 v[72:75], v[200:203], v[184:187], v[72:75]
v_cvt_pk_bf16_f32 v248, v224, v225
v_cvt_pk_bf16_f32 v249, v226, v227
v_mfma_f32_16x16x32_bf16 v[104:107], v[200:203], v[188:191], v[104:107]
ds_read_b64_tr_b16 v[200:201], v242 offset:0
ds_read_b64_tr_b16 v[202:203], v242 offset:1024
v_mfma_f32_16x16x32_bf16 v[12:15], v[204:207], v[176:179], v[12:15]
s_add_u32 m0, s83, 50688
v_cvt_pk_bf16_f32 v250, v228, v229
v_mfma_f32_16x16x32_bf16 v[44:47], v[204:207], v[180:183], v[44:47]
buffer_load_dwordx4 v232, s[44:47], s80 offen lds
v_mfma_f32_16x16x32_bf16 v[76:79], v[204:207], v[184:187], v[76:79]
v_cvt_pk_bf16_f32 v251, v230, v231
v_accvgpr_write_b32 a28, v248
v_mfma_f32_16x16x32_bf16 v[108:111], v[204:207], v[188:191], v[108:111]
ds_read_b64_tr_b16 v[204:205], v243 offset:0
ds_read_b64_tr_b16 v[206:207], v243 offset:1024
v_mfma_f32_16x16x32_bf16 v[16:19], v[208:211], v[176:179], v[16:19]
v_accvgpr_write_b32 a29, v249
v_accvgpr_write_b32 a30, v250
v_mfma_f32_16x16x32_bf16 v[48:51], v[208:211], v[180:183], v[48:51]
buffer_load_dwordx4 v[172:175], v247, s[52:55], s91 offen offset:2048 sc1
v_mfma_f32_16x16x32_bf16 v[80:83], v[208:211], v[184:187], v[80:83]
v_accvgpr_write_b32 a31, v251
v_mfma_f32_16x16x32_bf16 v[112:115], v[208:211], v[188:191], v[112:115]
ds_read_b64_tr_b16 v[208:209], v240 offset:128
ds_read_b64_tr_b16 v[210:211], v240 offset:1152
v_mfma_f32_16x16x32_bf16 v[20:23], v[212:215], v[176:179], v[20:23]
s_add_u32 m0, s83, 51712
v_mfma_f32_16x16x32_bf16 v[52:55], v[212:215], v[180:183], v[52:55]
buffer_load_dwordx4 v233, s[44:47], s80 offen lds
v_mfma_f32_16x16x32_bf16 v[84:87], v[212:215], v[184:187], v[84:87]
v_mfma_f32_16x16x32_bf16 v[116:119], v[212:215], v[188:191], v[116:119]
ds_read_b64_tr_b16 v[212:213], v241 offset:128
ds_read_b64_tr_b16 v[214:215], v241 offset:1152
v_mfma_f32_16x16x32_bf16 v[24:27], v[216:219], v[176:179], v[24:27]
v_mfma_f32_16x16x32_bf16 v[56:59], v[216:219], v[180:183], v[56:59]
v_mfma_f32_16x16x32_bf16 v[88:91], v[216:219], v[184:187], v[88:91]
v_mfma_f32_16x16x32_bf16 v[120:123], v[216:219], v[188:191], v[120:123]
ds_read_b64_tr_b16 v[216:217], v242 offset:128
ds_read_b64_tr_b16 v[218:219], v242 offset:1152
v_mfma_f32_16x16x32_bf16 v[28:31], v[220:223], v[176:179], v[28:31]
v_mfma_f32_16x16x32_bf16 v[60:63], v[220:223], v[180:183], v[60:63]
buffer_store_dwordx4 v[248:251], v255, s[48:51], s89 offen offset:2048 sc1
v_mfma_f32_16x16x32_bf16 v[92:95], v[220:223], v[184:187], v[92:95]
s_waitcnt vmcnt(16) lgkmcnt(2)
v_mfma_f32_16x16x32_bf16 v[124:127], v[220:223], v[188:191], v[124:127]
ds_read_b64_tr_b16 v[220:221], v243 offset:128
ds_read_b64_tr_b16 v[222:223], v243 offset:1152
v_mfma_f32_16x16x32_bf16 v[0:3], v[192:195], v[128:131], v[0:3]
v_accvgpr_read_b32 v224, a32
v_accvgpr_read_b32 v225, a33
v_mfma_f32_16x16x32_bf16 v[32:35], v[192:195], v[132:135], v[32:35]
buffer_load_dwordx4 v[176:179], v244, s[52:55], s91 offen offset:3072 sc1
v_mfma_f32_16x16x32_bf16 v[64:67], v[192:195], v[136:139], v[64:67]
v_accvgpr_read_b32 v226, a34
v_accvgpr_read_b32 v227, a35
v_mfma_f32_16x16x32_bf16 v[96:99], v[192:195], v[140:143], v[96:99]
ds_read_b64_tr_b16 v[192:193], v240 offset:8448
ds_read_b64_tr_b16 v[194:195], v240 offset:9472
v_mfma_f32_16x16x32_bf16 v[4:7], v[196:199], v[128:131], v[4:7]
v_accvgpr_read_b32 v228, a36
v_accvgpr_read_b32 v229, a37
v_mfma_f32_16x16x32_bf16 v[36:39], v[196:199], v[132:135], v[36:39]
buffer_load_dwordx4 v[180:183], v245, s[52:55], s91 offen offset:3072 sc1
v_mfma_f32_16x16x32_bf16 v[68:71], v[196:199], v[136:139], v[68:71]
s_waitcnt lgkmcnt(2)
v_accvgpr_read_b32 v230, a38
v_mfma_f32_16x16x32_bf16 v[100:103], v[196:199], v[140:143], v[100:103]
ds_read_b64_tr_b16 v[196:197], v241 offset:8448
ds_read_b64_tr_b16 v[198:199], v241 offset:9472
v_mfma_f32_16x16x32_bf16 v[8:11], v[200:203], v[128:131], v[8:11]
s_add_u32 m0, s83, 52864
v_accvgpr_read_b32 v231, a39
v_mfma_f32_16x16x32_bf16 v[40:43], v[200:203], v[132:135], v[40:43]
buffer_load_dwordx4 v234, s[44:47], s80 offen lds
v_mfma_f32_16x16x32_bf16 v[72:75], v[200:203], v[136:139], v[72:75]
v_cvt_pk_bf16_f32 v248, v224, v225
v_cvt_pk_bf16_f32 v249, v226, v227
v_mfma_f32_16x16x32_bf16 v[104:107], v[200:203], v[140:143], v[104:107]
ds_read_b64_tr_b16 v[200:201], v242 offset:8448
ds_read_b64_tr_b16 v[202:203], v242 offset:9472
v_mfma_f32_16x16x32_bf16 v[12:15], v[204:207], v[128:131], v[12:15]
v_cvt_pk_bf16_f32 v250, v228, v229
v_cvt_pk_bf16_f32 v251, v230, v231
v_mfma_f32_16x16x32_bf16 v[44:47], v[204:207], v[132:135], v[44:47]
buffer_load_dwordx4 v[184:187], v246, s[52:55], s91 offen offset:3072 sc1
v_mfma_f32_16x16x32_bf16 v[76:79], v[204:207], v[136:139], v[76:79]
v_accvgpr_write_b32 a32, v248
v_accvgpr_write_b32 a33, v249
v_mfma_f32_16x16x32_bf16 v[108:111], v[204:207], v[140:143], v[108:111]
ds_read_b64_tr_b16 v[204:205], v243 offset:8448
ds_read_b64_tr_b16 v[206:207], v243 offset:9472
v_mfma_f32_16x16x32_bf16 v[16:19], v[208:211], v[128:131], v[16:19]
s_add_u32 m0, s83, 53888
v_accvgpr_write_b32 a34, v250
v_mfma_f32_16x16x32_bf16 v[48:51], v[208:211], v[132:135], v[48:51]
buffer_load_dwordx4 v235, s[44:47], s80 offen lds
v_mfma_f32_16x16x32_bf16 v[80:83], v[208:211], v[136:139], v[80:83]
v_accvgpr_write_b32 a35, v251
v_mfma_f32_16x16x32_bf16 v[112:115], v[208:211], v[140:143], v[112:115]
ds_read_b64_tr_b16 v[208:209], v240 offset:8576
ds_read_b64_tr_b16 v[210:211], v240 offset:9600
v_mfma_f32_16x16x32_bf16 v[20:23], v[212:215], v[128:131], v[20:23]
v_mfma_f32_16x16x32_bf16 v[52:55], v[212:215], v[132:135], v[52:55]
buffer_load_dwordx4 v[188:191], v247, s[52:55], s91 offen offset:3072 sc1
v_mfma_f32_16x16x32_bf16 v[84:87], v[212:215], v[136:139], v[84:87]
s_add_u32 s91, s91, 4096
v_mfma_f32_16x16x32_bf16 v[116:119], v[212:215], v[140:143], v[116:119]
ds_read_b64_tr_b16 v[212:213], v241 offset:8576
ds_read_b64_tr_b16 v[214:215], v241 offset:9600
v_mfma_f32_16x16x32_bf16 v[24:27], v[216:219], v[128:131], v[24:27]
v_mfma_f32_16x16x32_bf16 v[56:59], v[216:219], v[132:135], v[56:59]
v_mfma_f32_16x16x32_bf16 v[88:91], v[216:219], v[136:139], v[88:91]
v_mfma_f32_16x16x32_bf16 v[120:123], v[216:219], v[140:143], v[120:123]
ds_read_b64_tr_b16 v[216:217], v242 offset:8576
ds_read_b64_tr_b16 v[218:219], v242 offset:9600
v_mfma_f32_16x16x32_bf16 v[28:31], v[220:223], v[128:131], v[28:31]
v_mfma_f32_16x16x32_bf16 v[60:63], v[220:223], v[132:135], v[60:63]
buffer_store_dwordx4 v[248:251], v255, s[48:51], s89 offen offset:3072 sc1
v_mfma_f32_16x16x32_bf16 v[92:95], v[220:223], v[136:139], v[92:95]
s_waitcnt vmcnt(15) lgkmcnt(2)
v_mfma_f32_16x16x32_bf16 v[124:127], v[220:223], v[140:143], v[124:127]
ds_read_b64_tr_b16 v[220:221], v243 offset:8576
ds_read_b64_tr_b16 v[222:223], v243 offset:9600
v_mfma_f32_16x16x32_bf16 v[0:3], v[192:195], v[144:147], v[0:3]
v_accvgpr_read_b32 v224, a248
v_accvgpr_read_b32 v225, a249
v_mfma_f32_16x16x32_bf16 v[32:35], v[192:195], v[148:151], v[32:35]
buffer_load_dwordx4 v[128:131], v244, s[52:55], s91 offen offset:0 sc1
v_mfma_f32_16x16x32_bf16 v[64:67], v[192:195], v[152:155], v[64:67]
v_accvgpr_read_b32 v226, a250
v_accvgpr_read_b32 v227, a251
v_mfma_f32_16x16x32_bf16 v[96:99], v[192:195], v[156:159], v[96:99]
v_accvgpr_read_b32 v228, a252
v_accvgpr_read_b32 v229, a253
v_mfma_f32_16x16x32_bf16 v[4:7], v[196:199], v[144:147], v[4:7]
s_waitcnt lgkmcnt(0)
s_barrier
v_mfma_f32_16x16x32_bf16 v[36:39], v[196:199], v[148:151], v[36:39]
buffer_load_dwordx4 v[132:135], v245, s[52:55], s91 offen offset:0 sc1
v_mfma_f32_16x16x32_bf16 v[68:71], v[196:199], v[152:155], v[68:71]
ds_read_b64_tr_b16 v[192:193], v240 offset:16896
ds_read_b64_tr_b16 v[194:195], v240 offset:17920
v_mfma_f32_16x16x32_bf16 v[100:103], v[196:199], v[156:159], v[100:103]
ds_read_b64_tr_b16 v[196:197], v241 offset:16896
ds_read_b64_tr_b16 v[198:199], v241 offset:17920
v_mfma_f32_16x16x32_bf16 v[8:11], v[200:203], v[144:147], v[8:11]
v_accvgpr_read_b32 v230, a254
v_accvgpr_read_b32 v231, a255
v_mfma_f32_16x16x32_bf16 v[40:43], v[200:203], v[148:151], v[40:43]
buffer_load_dwordx4 v[136:139], v246, s[52:55], s91 offen offset:0 sc1
v_mfma_f32_16x16x32_bf16 v[72:75], v[200:203], v[152:155], v[72:75]
v_cvt_pk_bf16_f32 v248, v224, v225
v_cvt_pk_bf16_f32 v249, v226, v227
v_mfma_f32_16x16x32_bf16 v[104:107], v[200:203], v[156:159], v[104:107]
ds_read_b64_tr_b16 v[200:201], v242 offset:16896
ds_read_b64_tr_b16 v[202:203], v242 offset:17920
v_mfma_f32_16x16x32_bf16 v[12:15], v[204:207], v[144:147], v[12:15]
v_cvt_pk_bf16_f32 v250, v228, v229
v_cvt_pk_bf16_f32 v251, v230, v231
v_mfma_f32_16x16x32_bf16 v[44:47], v[204:207], v[148:151], v[44:47]
buffer_load_dwordx4 v[140:143], v247, s[52:55], s91 offen offset:0 sc1
v_mfma_f32_16x16x32_bf16 v[76:79], v[204:207], v[152:155], v[76:79]
v_accvgpr_write_b32 a36, v248
v_accvgpr_write_b32 a37, v249
v_mfma_f32_16x16x32_bf16 v[108:111], v[204:207], v[156:159], v[108:111]
ds_read_b64_tr_b16 v[204:205], v243 offset:16896
ds_read_b64_tr_b16 v[206:207], v243 offset:17920
v_mfma_f32_16x16x32_bf16 v[16:19], v[208:211], v[144:147], v[16:19]
v_accvgpr_write_b32 a38, v250
v_accvgpr_write_b32 a39, v251
v_mfma_f32_16x16x32_bf16 v[48:51], v[208:211], v[148:151], v[48:51]
s_mov_b32 s89, 4096
v_mfma_f32_16x16x32_bf16 v[80:83], v[208:211], v[152:155], v[80:83]
s_getpc_b64 s[92:93]
.La5_LCT2:
s_sub_u32 s92, s92, .La5_LCT2-.La5_LSTART
s_subb_u32 s93, s93, 0
v_mfma_f32_16x16x32_bf16 v[112:115], v[208:211], v[156:159], v[112:115]
ds_read_b64_tr_b16 v[208:209], v240 offset:17024
ds_read_b64_tr_b16 v[210:211], v240 offset:18048
v_mfma_f32_16x16x32_bf16 v[20:23], v[212:215], v[144:147], v[20:23]
s_and_b32 s93, s93, 0xffff
s_lshr_b32 s94, s2, 3
v_mfma_f32_16x16x32_bf16 v[52:55], v[212:215], v[148:151], v[52:55]
s_lshl2_add_u32 s94, s94, s3
s_lshl_b32 s94, s94, 10
v_mfma_f32_16x16x32_bf16 v[84:87], v[212:215], v[152:155], v[84:87]
v_lshrrev_b32 v233, 7, v255
v_lshlrev_b32 v232, 3, v255
v_mfma_f32_16x16x32_bf16 v[116:119], v[212:215], v[156:159], v[116:119]
ds_read_b64_tr_b16 v[212:213], v241 offset:17024
ds_read_b64_tr_b16 v[214:215], v241 offset:18048
v_mfma_f32_16x16x32_bf16 v[24:27], v[216:219], v[144:147], v[24:27]
v_lshl_add_u32 v232, v233, 28, v232
v_add_u32 v232, s94, v232
v_mfma_f32_16x16x32_bf16 v[56:59], v[216:219], v[148:151], v[56:59]
s_mov_b32 s94, .La5_LCODEEND-.La5_LSTART
s_mov_b32 s95, 0x20000
v_mfma_f32_16x16x32_bf16 v[88:91], v[216:219], v[152:155], v[88:91]
buffer_load_dword a253, v232, s[92:95], 0 offen
v_mfma_f32_16x16x32_bf16 v[120:123], v[216:219], v[156:159], v[120:123]
ds_read_b64_tr_b16 v[216:217], v242 offset:17024
ds_read_b64_tr_b16 v[218:219], v242 offset:18048
v_mfma_f32_16x16x32_bf16 v[28:31], v[220:223], v[144:147], v[28:31]
ds_write_b128 v238, a[12:15] offset:6144
v_mfma_f32_16x16x32_bf16 v[60:63], v[220:223], v[148:151], v[60:63]
buffer_store_dwordx4 v[248:251], v255, s[48:51], s89 offen offset:0 sc1
v_mfma_f32_16x16x32_bf16 v[92:95], v[220:223], v[152:155], v[92:95]
s_waitcnt vmcnt(15) lgkmcnt(3)
s_mov_b32 s80, 8192
v_mfma_f32_16x16x32_bf16 v[124:127], v[220:223], v[156:159], v[124:127]
ds_read_b64_tr_b16 v[220:221], v243 offset:17024
ds_read_b64_tr_b16 v[222:223], v243 offset:18048
v_mfma_f32_16x16x32_bf16 v[0:3], v[192:195], v[160:163], v[0:3]
v_accvgpr_read_b32 v224, a40
v_accvgpr_read_b32 v225, a41
v_mfma_f32_16x16x32_bf16 v[32:35], v[192:195], v[164:167], v[32:35]
buffer_load_dwordx4 a[192:195], v244, s[52:55], s91 offen offset:1024 sc1
v_mfma_f32_16x16x32_bf16 v[64:67], v[192:195], v[168:171], v[64:67]
v_accvgpr_read_b32 v226, a42
v_accvgpr_read_b32 v227, a43
v_mfma_f32_16x16x32_bf16 v[96:99], v[192:195], v[172:175], v[96:99]
ds_read_b64_tr_b16 v[192:193], v240 offset:25344
ds_read_b64_tr_b16 v[194:195], v240 offset:26368
v_mfma_f32_16x16x32_bf16 v[4:7], v[196:199], v[160:163], v[4:7]
v_accvgpr_read_b32 v228, a44
v_accvgpr_read_b32 v229, a45
v_mfma_f32_16x16x32_bf16 v[36:39], v[196:199], v[164:167], v[36:39]
buffer_load_dwordx4 a[196:199], v245, s[52:55], s91 offen offset:1024 sc1
.La5_LW1_10:
s_cbranch_execz .La5_LW1_R0
v_mfma_f32_16x16x32_bf16 v[68:71], v[196:199], v[168:171], v[68:71]
s_waitcnt lgkmcnt(2)
v_accvgpr_read_b32 v230, a46
v_mfma_f32_16x16x32_bf16 v[100:103], v[196:199], v[172:175], v[100:103]
ds_read_b64_tr_b16 v[196:197], v241 offset:25344
ds_read_b64_tr_b16 v[198:199], v241 offset:26368
v_mfma_f32_16x16x32_bf16 v[8:11], v[200:203], v[160:163], v[8:11]
v_accvgpr_read_b32 v231, a47
v_cvt_pk_bf16_f32 v248, v224, v225
v_mfma_f32_16x16x32_bf16 v[40:43], v[200:203], v[164:167], v[40:43]
buffer_load_dwordx4 a[200:203], v246, s[52:55], s91 offen offset:1024 sc1
v_mfma_f32_16x16x32_bf16 v[72:75], v[200:203], v[168:171], v[72:75]
v_cvt_pk_bf16_f32 v249, v226, v227
v_cvt_pk_bf16_f32 v250, v228, v229
v_mfma_f32_16x16x32_bf16 v[104:107], v[200:203], v[172:175], v[104:107]
ds_read_b64_tr_b16 v[200:201], v242 offset:25344
ds_read_b64_tr_b16 v[202:203], v242 offset:26368
v_mfma_f32_16x16x32_bf16 v[12:15], v[204:207], v[160:163], v[12:15]
v_cvt_pk_bf16_f32 v251, v230, v231
v_accvgpr_write_b32 a40, v248
v_mfma_f32_16x16x32_bf16 v[44:47], v[204:207], v[164:167], v[44:47]
buffer_load_dwordx4 a[204:207], v247, s[52:55], s91 offen offset:1024 sc1
v_mfma_f32_16x16x32_bf16 v[76:79], v[204:207], v[168:171], v[76:79]
v_accvgpr_write_b32 a41, v249
v_accvgpr_write_b32 a42, v250
v_mfma_f32_16x16x32_bf16 v[108:111], v[204:207], v[172:175], v[108:111]
ds_read_b64_tr_b16 v[204:205], v243 offset:25344
ds_read_b64_tr_b16 v[206:207], v243 offset:26368
v_mfma_f32_16x16x32_bf16 v[16:19], v[208:211], v[160:163], v[16:19]
v_accvgpr_write_b32 a43, v251
s_add_u32 s83, s83, 4294886912
v_mfma_f32_16x16x32_bf16 v[48:51], v[208:211], v[164:167], v[48:51]
buffer_load_dwordx4 a[96:99], v244, s[52:55], s91 offen offset:2048 sc1
v_mfma_f32_16x16x32_bf16 v[80:83], v[208:211], v[168:171], v[80:83]
s_lshl_b32 s92, s76, 21
s_add_u32 s92, s92, 54525952
v_mfma_f32_16x16x32_bf16 v[112:115], v[208:211], v[172:175], v[112:115]
ds_read_b64_tr_b16 v[208:209], v240 offset:25472
ds_read_b64_tr_b16 v[210:211], v240 offset:26496
v_mfma_f32_16x16x32_bf16 v[20:23], v[212:215], v[160:163], v[20:23]
s_add_u32 s44, s26, s92
s_addc_u32 s45, s27, 0
v_mfma_f32_16x16x32_bf16 v[52:55], v[212:215], v[164:167], v[52:55]
buffer_load_dwordx4 a[100:103], v245, s[52:55], s91 offen offset:2048 sc1
v_mfma_f32_16x16x32_bf16 v[84:87], v[212:215], v[168:171], v[84:87]
s_and_b32 s45, s45, 0xffff
s_mov_b32 s46, 2097152
v_mfma_f32_16x16x32_bf16 v[116:119], v[212:215], v[172:175], v[116:119]
ds_read_b64_tr_b16 v[212:213], v241 offset:25472
ds_read_b64_tr_b16 v[214:215], v241 offset:26496
v_mfma_f32_16x16x32_bf16 v[24:27], v[216:219], v[160:163], v[24:27]
s_mov_b32 s95, 4096
s_mov_b32 s96, 8192
v_mfma_f32_16x16x32_bf16 v[56:59], v[216:219], v[164:167], v[56:59]
buffer_load_dwordx4 a[104:107], v246, s[52:55], s91 offen offset:2048 sc1
v_mfma_f32_16x16x32_bf16 v[88:91], v[216:219], v[168:171], v[88:91]
s_mov_b32 s97, 12288
s_lshl_b32 s92, s3, 18
v_mfma_f32_16x16x32_bf16 v[120:123], v[216:219], v[172:175], v[120:123]
ds_read_b64_tr_b16 v[216:217], v242 offset:25472
ds_read_b64_tr_b16 v[218:219], v242 offset:26496
v_mfma_f32_16x16x32_bf16 v[28:31], v[220:223], v[160:163], v[28:31]
ds_write_b128 v238, a[16:19] offset:7168
v_mfma_f32_16x16x32_bf16 v[60:63], v[220:223], v[164:167], v[60:63]
buffer_store_dwordx4 v[248:251], v255, s[48:51], s89 offen offset:1024 sc1
v_mfma_f32_16x16x32_bf16 v[92:95], v[220:223], v[168:171], v[92:95]
s_waitcnt vmcnt(15) lgkmcnt(3)
s_sub_u32 s93, 1835008, s92
v_mfma_f32_16x16x32_bf16 v[124:127], v[220:223], v[172:175], v[124:127]
ds_read_b64_tr_b16 v[220:221], v243 offset:25472
ds_read_b64_tr_b16 v[222:223], v243 offset:26496
v_mfma_f32_16x16x32_bf16 v[0:3], v[192:195], v[176:179], v[0:3]
v_accvgpr_read_b32 v224, a48
v_accvgpr_read_b32 v225, a49
v_mfma_f32_16x16x32_bf16 v[32:35], v[192:195], v[180:183], v[32:35]
buffer_load_dwordx4 a[108:111], v247, s[52:55], s91 offen offset:2048 sc1
v_mfma_f32_16x16x32_bf16 v[64:67], v[192:195], v[184:187], v[64:67]
v_accvgpr_read_b32 v226, a50
v_accvgpr_read_b32 v227, a51
v_mfma_f32_16x16x32_bf16 v[96:99], v[192:195], v[188:191], v[96:99]
v_accvgpr_read_b32 v228, a52
v_accvgpr_read_b32 v229, a53
v_mfma_f32_16x16x32_bf16 v[4:7], v[196:199], v[176:179], v[4:7]
s_waitcnt lgkmcnt(0)
s_barrier
v_mfma_f32_16x16x32_bf16 v[36:39], v[196:199], v[180:183], v[36:39]
buffer_load_dwordx4 a[112:115], v244, s[52:55], s91 offen offset:3072 sc1
v_mfma_f32_16x16x32_bf16 v[68:71], v[196:199], v[184:187], v[68:71]
ds_read_b64_tr_b16 v[192:193], v240 offset:33792
ds_read_b64_tr_b16 v[194:195], v240 offset:34816
v_mfma_f32_16x16x32_bf16 v[100:103], v[196:199], v[188:191], v[100:103]
ds_read_b64_tr_b16 v[196:197], v241 offset:33792
ds_read_b64_tr_b16 v[198:199], v241 offset:34816
v_mfma_f32_16x16x32_bf16 v[8:11], v[200:203], v[176:179], v[8:11]
v_accvgpr_read_b32 v230, a54
v_accvgpr_read_b32 v231, a55
v_mfma_f32_16x16x32_bf16 v[40:43], v[200:203], v[180:183], v[40:43]
buffer_load_dwordx4 a[116:119], v245, s[52:55], s91 offen offset:3072 sc1
v_mfma_f32_16x16x32_bf16 v[72:75], v[200:203], v[184:187], v[72:75]
v_cvt_pk_bf16_f32 v248, v224, v225
v_cvt_pk_bf16_f32 v249, v226, v227
v_mfma_f32_16x16x32_bf16 v[104:107], v[200:203], v[188:191], v[104:107]
ds_read_b64_tr_b16 v[200:201], v242 offset:33792
ds_read_b64_tr_b16 v[202:203], v242 offset:34816
v_mfma_f32_16x16x32_bf16 v[12:15], v[204:207], v[176:179], v[12:15]
ds_write_b128 v238, a[20:23] offset:32768
v_mfma_f32_16x16x32_bf16 v[44:47], v[204:207], v[180:183], v[44:47]
buffer_load_dwordx4 a[120:123], v246, s[52:55], s91 offen offset:3072 sc1
v_mfma_f32_16x16x32_bf16 v[76:79], v[204:207], v[184:187], v[76:79]
v_cvt_pk_bf16_f32 v250, v228, v229
v_cvt_pk_bf16_f32 v251, v230, v231
v_mfma_f32_16x16x32_bf16 v[108:111], v[204:207], v[188:191], v[108:111]
ds_read_b64_tr_b16 v[204:205], v243 offset:33792
ds_read_b64_tr_b16 v[206:207], v243 offset:34816
v_mfma_f32_16x16x32_bf16 v[16:19], v[208:211], v[176:179], v[16:19]
v_accvgpr_write_b32 a44, v248
v_accvgpr_write_b32 a45, v249
v_mfma_f32_16x16x32_bf16 v[48:51], v[208:211], v[180:183], v[48:51]
buffer_load_dwordx4 a[124:127], v247, s[52:55], s91 offen offset:3072 sc1
v_mfma_f32_16x16x32_bf16 v[80:83], v[208:211], v[184:187], v[80:83]
v_accvgpr_write_b32 a46, v250
v_accvgpr_write_b32 a47, v251
v_mfma_f32_16x16x32_bf16 v[112:115], v[208:211], v[188:191], v[112:115]
ds_read_b64_tr_b16 v[208:209], v240 offset:33920
ds_read_b64_tr_b16 v[210:211], v240 offset:34944
v_mfma_f32_16x16x32_bf16 v[20:23], v[212:215], v[176:179], v[20:23]
v_mfma_f32_16x16x32_bf16 v[52:55], v[212:215], v[180:183], v[52:55]
v_mfma_f32_16x16x32_bf16 v[84:87], v[212:215], v[184:187], v[84:87]
v_mfma_f32_16x16x32_bf16 v[116:119], v[212:215], v[188:191], v[116:119]
ds_read_b64_tr_b16 v[212:213], v241 offset:33920
ds_read_b64_tr_b16 v[214:215], v241 offset:34944
v_mfma_f32_16x16x32_bf16 v[24:27], v[216:219], v[176:179], v[24:27]
v_mfma_f32_16x16x32_bf16 v[56:59], v[216:219], v[180:183], v[56:59]
v_mfma_f32_16x16x32_bf16 v[88:91], v[216:219], v[184:187], v[88:91]
v_mfma_f32_16x16x32_bf16 v[120:123], v[216:219], v[188:191], v[120:123]
ds_read_b64_tr_b16 v[216:217], v242 offset:33920
ds_read_b64_tr_b16 v[218:219], v242 offset:34944
v_mfma_f32_16x16x32_bf16 v[28:31], v[220:223], v[176:179], v[28:31]
ds_write_b128 v238, a[24:27] offset:33792
v_mfma_f32_16x16x32_bf16 v[60:63], v[220:223], v[180:183], v[60:63]
buffer_store_dwordx4 v[248:251], v255, s[48:51], s89 offen offset:2048 sc1
v_mfma_f32_16x16x32_bf16 v[92:95], v[220:223], v[184:187], v[92:95]
s_waitcnt vmcnt(16) lgkmcnt(3)
v_mfma_f32_16x16x32_bf16 v[124:127], v[220:223], v[188:191], v[124:127]
ds_read_b64_tr_b16 v[220:221], v243 offset:33920
ds_read_b64_tr_b16 v[222:223], v243 offset:34944
v_mfma_f32_16x16x32_bf16 v[0:3], v[192:195], v[128:131], v[0:3]
v_accvgpr_read_b32 v224, a56
v_accvgpr_read_b32 v225, a57
v_mfma_f32_16x16x32_bf16 v[32:35], v[192:195], v[132:135], v[32:35]
v_accvgpr_read_b32 v226, a58
v_accvgpr_read_b32 v227, a59
v_mfma_f32_16x16x32_bf16 v[64:67], v[192:195], v[136:139], v[64:67]
v_accvgpr_read_b32 v228, a60
v_accvgpr_read_b32 v229, a61
v_mfma_f32_16x16x32_bf16 v[96:99], v[192:195], v[140:143], v[96:99]
ds_read_b64_tr_b16 v[192:193], v240 offset:42240
ds_read_b64_tr_b16 v[194:195], v240 offset:43264
v_mfma_f32_16x16x32_bf16 v[4:7], v[196:199], v[128:131], v[4:7]
v_accvgpr_read_b32 v230, a62
v_accvgpr_read_b32 v231, a63
v_mfma_f32_16x16x32_bf16 v[36:39], v[196:199], v[132:135], v[36:39]
v_cvt_pk_bf16_f32 v248, v224, v225
v_cvt_pk_bf16_f32 v249, v226, v227
v_mfma_f32_16x16x32_bf16 v[68:71], v[196:199], v[136:139], v[68:71]
s_waitcnt lgkmcnt(2)
v_cvt_pk_bf16_f32 v250, v228, v229
v_mfma_f32_16x16x32_bf16 v[100:103], v[196:199], v[140:143], v[100:103]
ds_read_b64_tr_b16 v[196:197], v240 offset:50688
ds_read_b64_tr_b16 v[198:199], v240 offset:51712
v_mfma_f32_16x16x32_bf16 v[8:11], v[200:203], v[128:131], v[8:11]
v_cvt_pk_bf16_f32 v251, v230, v231
v_accvgpr_write_b32 a48, v248
v_mfma_f32_16x16x32_bf16 v[40:43], v[200:203], v[132:135], v[40:43]
v_accvgpr_write_b32 a49, v249
v_accvgpr_write_b32 a50, v250
v_mfma_f32_16x16x32_bf16 v[72:75], v[200:203], v[136:139], v[72:75]
v_accvgpr_write_b32 a51, v251
v_add_u32 v244, s92, v255
v_mfma_f32_16x16x32_bf16 v[104:107], v[200:203], v[140:143], v[104:107]
ds_read_b64_tr_b16 v[200:201], v240 offset:59136
ds_read_b64_tr_b16 v[202:203], v240 offset:60160
v_mfma_f32_16x16x32_bf16 v[12:15], v[204:207], v[128:131], v[12:15]
ds_write_b128 v238, a[28:31] offset:34816
v_mfma_f32_16x16x32_bf16 v[44:47], v[204:207], v[132:135], v[44:47]
v_add_u32 v246, s93, v255
v_add_u32 v245, 0x1000, v244
v_mfma_f32_16x16x32_bf16 v[76:79], v[204:207], v[136:139], v[76:79]
v_add_u32 v247, 0x1000, v246
s_lshl_b32 s91, s72, 13
v_mfma_f32_16x16x32_bf16 v[108:111], v[204:207], v[140:143], v[108:111]
ds_read_b64_tr_b16 v[204:205], v241 offset:42240
ds_read_b64_tr_b16 v[206:207], v241 offset:43264
v_mfma_f32_16x16x32_bf16 v[16:19], v[208:211], v[128:131], v[16:19]
s_lshl2_add_u32 s92, s76, s3
s_lshl3_add_u32 s92, s92, s20
v_mfma_f32_16x16x32_bf16 v[48:51], v[208:211], v[132:135], v[48:51]
v_mfma_f32_16x16x32_bf16 v[80:83], v[208:211], v[136:139], v[80:83]
v_mfma_f32_16x16x32_bf16 v[112:115], v[208:211], v[140:143], v[112:115]
ds_read_b64_tr_b16 v[208:209], v241 offset:50688
ds_read_b64_tr_b16 v[210:211], v241 offset:51712
v_mfma_f32_16x16x32_bf16 v[20:23], v[212:215], v[128:131], v[20:23]
v_mfma_f32_16x16x32_bf16 v[52:55], v[212:215], v[132:135], v[52:55]
v_mfma_f32_16x16x32_bf16 v[84:87], v[212:215], v[136:139], v[84:87]
v_mfma_f32_16x16x32_bf16 v[116:119], v[212:215], v[140:143], v[116:119]
ds_read_b64_tr_b16 v[212:213], v241 offset:59136
ds_read_b64_tr_b16 v[214:215], v241 offset:60160
v_mfma_f32_16x16x32_bf16 v[24:27], v[216:219], v[128:131], v[24:27]
v_mfma_f32_16x16x32_bf16 v[56:59], v[216:219], v[132:135], v[56:59]
v_mfma_f32_16x16x32_bf16 v[88:91], v[216:219], v[136:139], v[88:91]
v_mfma_f32_16x16x32_bf16 v[120:123], v[216:219], v[140:143], v[120:123]
ds_read_b64_tr_b16 v[216:217], v242 offset:42240
ds_read_b64_tr_b16 v[218:219], v242 offset:43264
v_mfma_f32_16x16x32_bf16 v[28:31], v[220:223], v[128:131], v[28:31]
ds_write_b128 v238, a[32:35] offset:35840
v_mfma_f32_16x16x32_bf16 v[60:63], v[220:223], v[132:135], v[60:63]
buffer_store_dwordx4 v[248:251], v255, s[48:51], s89 offen offset:3072 sc1
v_mfma_f32_16x16x32_bf16 v[92:95], v[220:223], v[136:139], v[92:95]
s_waitcnt vmcnt(2) lgkmcnt(3)
v_mfma_f32_16x16x32_bf16 v[124:127], v[220:223], v[140:143], v[124:127]
ds_read_b64_tr_b16 v[220:221], v242 offset:50688
ds_read_b64_tr_b16 v[222:223], v242 offset:51712
v_mfma_f32_16x16x32_bf16 v[0:3], v[192:195], a[192:195], v[0:3]
s_mov_b64 s[48:49], s[86:87]
v_mfma_f32_16x16x32_bf16 v[32:35], v[192:195], a[196:199], v[32:35]
s_bitcmp1_b32 21, s73
s_cselect_b32 s50, 16384, 0
s_cselect_b32 s86, s88, 1073741824
v_mfma_f32_16x16x32_bf16 v[64:67], v[192:195], a[200:203], v[64:67]
s_lshl_b32 s89, s3, 14
v_add_u32 v248, s89, v255
v_mfma_f32_16x16x32_bf16 v[96:99], v[192:195], a[204:207], v[96:99]
ds_read_b64_tr_b16 v[192:193], v242 offset:59136
ds_read_b64_tr_b16 v[194:195], v242 offset:60160
v_mfma_f32_16x16x32_bf16 v[0:3], v[196:199], a[96:99], v[0:3]
ds_write_b128 v238, a[36:39] offset:36864
v_mfma_f32_16x16x32_bf16 v[32:35], v[196:199], a[100:103], v[32:35]
s_add_u32 s86, s86, 262144
s_add_u32 s89, s89, 16384
v_mfma_f32_16x16x32_bf16 v[64:67], v[196:199], a[104:107], v[64:67]
s_waitcnt lgkmcnt(3)
s_lshl_b32 m0, s3, 12
v_mfma_f32_16x16x32_bf16 v[96:99], v[196:199], a[108:111], v[96:99]
ds_read_b64_tr_b16 v[196:197], v243 offset:42240
ds_read_b64_tr_b16 v[198:199], v243 offset:43264
v_mfma_f32_16x16x32_bf16 v[0:3], v[200:203], a[112:115], v[0:3]
s_add_u32 m0, m0, 131072
v_lshrrev_b32 v251, 2, v255
v_mfma_f32_16x16x32_bf16 v[32:35], v[200:203], a[116:119], v[32:35]
v_and_b32 v224, 16, v255
v_cmp_eq_u32 vcc, 0, v224
v_mfma_f32_16x16x32_bf16 v[64:67], v[200:203], a[120:123], v[64:67]
v_cndmask_b32 v249, v253, v252, vcc
v_mfma_f32_16x16x32_bf16 v[96:99], v[200:203], a[124:127], v[96:99]
ds_read_b64_tr_b16 v[200:201], v243 offset:50688
ds_read_b64_tr_b16 v[202:203], v243 offset:51712
v_mfma_f32_16x16x32_bf16 v[4:7], v[204:207], a[192:195], v[4:7]
ds_write_b128 v238, a[40:43] offset:37888
v_mfma_f32_16x16x32_bf16 v[36:39], v[204:207], a[196:199], v[36:39]
v_mfma_f32_16x16x32_bf16 v[68:71], v[204:207], a[200:203], v[68:71]
v_mfma_f32_16x16x32_bf16 v[100:103], v[204:207], a[204:207], v[100:103]
ds_read_b64_tr_b16 v[204:205], v243 offset:59136
ds_read_b64_tr_b16 v[206:207], v243 offset:60160
v_mfma_f32_16x16x32_bf16 v[4:7], v[208:211], a[96:99], v[4:7]
v_mfma_f32_16x16x32_bf16 v[36:39], v[208:211], a[100:103], v[36:39]
v_mfma_f32_16x16x32_bf16 v[68:71], v[208:211], a[104:107], v[68:71]
v_mfma_f32_16x16x32_bf16 v[100:103], v[208:211], a[108:111], v[100:103]
ds_read_b64_tr_b16 v[208:209], v240 offset:42368
ds_read_b64_tr_b16 v[210:211], v240 offset:43392
v_mfma_f32_16x16x32_bf16 v[4:7], v[212:215], a[112:115], v[4:7]
ds_write_b128 v238, a[44:47] offset:38912
v_mfma_f32_16x16x32_bf16 v[36:39], v[212:215], a[116:119], v[36:39]
v_mfma_f32_16x16x32_bf16 v[68:71], v[212:215], a[120:123], v[68:71]
v_mfma_f32_16x16x32_bf16 v[100:103], v[212:215], a[124:127], v[100:103]
ds_read_b64_tr_b16 v[212:213], v240 offset:50816
ds_read_b64_tr_b16 v[214:215], v240 offset:51840
v_mfma_f32_16x16x32_bf16 v[8:11], v[216:219], a[192:195], v[8:11]
v_mfma_f32_16x16x32_bf16 v[40:43], v[216:219], a[196:199], v[40:43]
v_mfma_f32_16x16x32_bf16 v[72:75], v[216:219], a[200:203], v[72:75]
v_cvt_pk_bf16_f32 v128, v0, v1
v_cvt_pk_bf16_f32 v129, v2, v3
v_mfma_f32_16x16x32_bf16 v[104:107], v[216:219], a[204:207], v[104:107]
ds_read_b64_tr_b16 v[216:217], v240 offset:59264
ds_read_b64_tr_b16 v[218:219], v240 offset:60288
v_mfma_f32_16x16x32_bf16 v[8:11], v[220:223], a[96:99], v[8:11]
v_cvt_pk_bf16_f32 v130, v4, v5
v_cvt_pk_bf16_f32 v131, v6, v7
v_mfma_f32_16x16x32_bf16 v[40:43], v[220:223], a[100:103], v[40:43]
v_cvt_pk_bf16_f32 v144, v32, v33
v_cvt_pk_bf16_f32 v145, v34, v35
v_mfma_f32_16x16x32_bf16 v[72:75], v[220:223], a[104:107], v[72:75]
s_waitcnt lgkmcnt(2)
v_cvt_pk_bf16_f32 v146, v36, v37
v_mfma_f32_16x16x32_bf16 v[104:107], v[220:223], a[108:111], v[104:107]
ds_read_b64_tr_b16 v[220:221], v241 offset:42368
ds_read_b64_tr_b16 v[222:223], v241 offset:43392
v_mfma_f32_16x16x32_bf16 v[8:11], v[192:195], a[112:115], v[8:11]
v_cvt_pk_bf16_f32 v147, v38, v39
v_cvt_pk_bf16_f32 v160, v64, v65
v_mfma_f32_16x16x32_bf16 v[40:43], v[192:195], a[116:119], v[40:43]
v_cvt_pk_bf16_f32 v161, v66, v67
v_cvt_pk_bf16_f32 v162, v68, v69
v_mfma_f32_16x16x32_bf16 v[72:75], v[192:195], a[120:123], v[72:75]
v_cvt_pk_bf16_f32 v163, v70, v71
v_cvt_pk_bf16_f32 v176, v96, v97
v_mfma_f32_16x16x32_bf16 v[104:107], v[192:195], a[124:127], v[104:107]
ds_read_b64_tr_b16 v[192:193], v241 offset:50816
ds_read_b64_tr_b16 v[194:195], v241 offset:51840
v_mfma_f32_16x16x32_bf16 v[12:15], v[196:199], a[192:195], v[12:15]
v_cvt_pk_bf16_f32 v177, v98, v99
v_cvt_pk_bf16_f32 v178, v100, v101
v_mfma_f32_16x16x32_bf16 v[44:47], v[196:199], a[196:199], v[44:47]
v_cvt_pk_bf16_f32 v179, v102, v103
v_mfma_f32_16x16x32_bf16 v[76:79], v[196:199], a[200:203], v[76:79]
s_waitcnt lgkmcnt(2)
v_mfma_f32_16x16x32_bf16 v[108:111], v[196:199], a[204:207], v[108:111]
ds_read_b64_tr_b16 v[196:197], v241 offset:59264
ds_read_b64_tr_b16 v[198:199], v241 offset:60288
v_mfma_f32_16x16x32_bf16 v[12:15], v[200:203], a[96:99], v[12:15]
v_mfma_f32_16x16x32_bf16 v[44:47], v[200:203], a[100:103], v[44:47]
v_mfma_f32_16x16x32_bf16 v[76:79], v[200:203], a[104:107], v[76:79]
v_mfma_f32_16x16x32_bf16 v[108:111], v[200:203], a[108:111], v[108:111]
ds_read_b64_tr_b16 v[200:201], v242 offset:42368
ds_read_b64_tr_b16 v[202:203], v242 offset:43392
v_mfma_f32_16x16x32_bf16 v[12:15], v[204:207], a[112:115], v[12:15]
v_mfma_f32_16x16x32_bf16 v[44:47], v[204:207], a[116:119], v[44:47]
v_mfma_f32_16x16x32_bf16 v[76:79], v[204:207], a[120:123], v[76:79]
v_mfma_f32_16x16x32_bf16 v[108:111], v[204:207], a[124:127], v[108:111]
ds_read_b64_tr_b16 v[204:205], v242 offset:50816
ds_read_b64_tr_b16 v[206:207], v242 offset:51840
v_mfma_f32_16x16x32_bf16 v[16:19], v[208:211], a[192:195], v[16:19]
v_mfma_f32_16x16x32_bf16 v[48:51], v[208:211], a[196:199], v[48:51]
buffer_store_dwordx4 v[128:131], v255, s[48:51], 0 offen offset:0 sc1
v_mfma_f32_16x16x32_bf16 v[80:83], v[208:211], a[200:203], v[80:83]
v_cvt_pk_bf16_f32 v132, v8, v9
v_cvt_pk_bf16_f32 v133, v10, v11
v_mfma_f32_16x16x32_bf16 v[112:115], v[208:211], a[204:207], v[112:115]
ds_read_b64_tr_b16 v[208:209], v242 offset:59264
ds_read_b64_tr_b16 v[210:211], v242 offset:60288
v_mfma_f32_16x16x32_bf16 v[16:19], v[212:215], a[96:99], v[16:19]
v_cvt_pk_bf16_f32 v134, v12, v13
v_cvt_pk_bf16_f32 v135, v14, v15
v_mfma_f32_16x16x32_bf16 v[48:51], v[212:215], a[100:103], v[48:51]
buffer_store_dwordx4 v[144:147], v255, s[48:51], s95 offen offset:0 sc1
v_mfma_f32_16x16x32_bf16 v[80:83], v[212:215], a[104:107], v[80:83]
v_cvt_pk_bf16_f32 v148, v40, v41
v_cvt_pk_bf16_f32 v149, v42, v43
v_mfma_f32_16x16x32_bf16 v[112:115], v[212:215], a[108:111], v[112:115]
ds_read_b64_tr_b16 v[212:213], v243 offset:42368
ds_read_b64_tr_b16 v[214:215], v243 offset:43392
v_mfma_f32_16x16x32_bf16 v[16:19], v[216:219], a[112:115], v[16:19]
v_cvt_pk_bf16_f32 v150, v44, v45
v_cvt_pk_bf16_f32 v151, v46, v47
v_mfma_f32_16x16x32_bf16 v[48:51], v[216:219], a[116:119], v[48:51]
buffer_store_dwordx4 v[160:163], v255, s[48:51], s96 offen offset:0 sc1
v_mfma_f32_16x16x32_bf16 v[80:83], v[216:219], a[120:123], v[80:83]
v_cvt_pk_bf16_f32 v164, v72, v73
v_cvt_pk_bf16_f32 v165, v74, v75
v_mfma_f32_16x16x32_bf16 v[112:115], v[216:219], a[124:127], v[112:115]
ds_read_b64_tr_b16 v[216:217], v243 offset:50816
ds_read_b64_tr_b16 v[218:219], v243 offset:51840
v_mfma_f32_16x16x32_bf16 v[20:23], v[220:223], a[192:195], v[20:23]
v_cvt_pk_bf16_f32 v166, v76, v77
v_cvt_pk_bf16_f32 v167, v78, v79
v_mfma_f32_16x16x32_bf16 v[52:55], v[220:223], a[196:199], v[52:55]
buffer_store_dwordx4 v[176:179], v255, s[48:51], s97 offen offset:0 sc1
v_mfma_f32_16x16x32_bf16 v[84:87], v[220:223], a[200:203], v[84:87]
s_waitcnt lgkmcnt(2)
v_cvt_pk_bf16_f32 v180, v104, v105
v_mfma_f32_16x16x32_bf16 v[116:119], v[220:223], a[204:207], v[116:119]
ds_read_b64_tr_b16 v[220:221], v243 offset:59264
ds_read_b64_tr_b16 v[222:223], v243 offset:60288
v_mfma_f32_16x16x32_bf16 v[20:23], v[192:195], a[96:99], v[20:23]
v_cvt_pk_bf16_f32 v181, v106, v107
v_cvt_pk_bf16_f32 v182, v108, v109
v_mfma_f32_16x16x32_bf16 v[52:55], v[192:195], a[100:103], v[52:55]
v_cvt_pk_bf16_f32 v183, v110, v111
v_accvgpr_read_b32 v32, a236
v_mfma_f32_16x16x32_bf16 v[84:87], v[192:195], a[104:107], v[84:87]
v_accvgpr_read_b32 v33, a237
v_accvgpr_read_b32 v34, a238
v_mfma_f32_16x16x32_bf16 v[116:119], v[192:195], a[108:111], v[116:119]
v_accvgpr_read_b32 v35, a239
v_mov_b32 v36, 0xff800000
v_mfma_f32_16x16x32_bf16 v[20:23], v[196:199], a[112:115], v[20:23]
s_waitcnt vmcnt(4) lgkmcnt(0)
s_barrier
v_mfma_f32_16x16x32_bf16 v[52:55], v[196:199], a[116:119], v[52:55]
buffer_store_dwordx2 v[252:253], off, s[64:67], s92 sc1
v_mfma_f32_16x16x32_bf16 v[84:87], v[196:199], a[120:123], v[84:87]
v_mov_b32 v37, 0xff800000
v_mov_b32 v38, 0xff800000
v_mfma_f32_16x16x32_bf16 v[116:119], v[196:199], a[124:127], v[116:119]
ds_read_b128 a[64:67], v248 offset:0
ds_read_b128 a[68:71], v248 offset:1024
v_mfma_f32_16x16x32_bf16 v[24:27], v[200:203], a[192:195], v[24:27]
v_mov_b32 v39, 0xff800000
v_mul_u32_u24 v241, 0x5555, v224
v_mfma_f32_16x16x32_bf16 v[56:59], v[200:203], a[196:199], v[56:59]
buffer_store_dwordx4 v[132:135], v255, s[48:51], 0 offen offset:1024 sc1
v_mfma_f32_16x16x32_bf16 v[88:91], v[200:203], a[200:203], v[88:91]
v_cvt_pk_bf16_f32 v136, v16, v17
v_cvt_pk_bf16_f32 v137, v18, v19
v_mfma_f32_16x16x32_bf16 v[120:123], v[200:203], a[204:207], v[120:123]
ds_read_b128 a[72:75], v248 offset:2048
ds_read_b128 a[76:79], v248 offset:3072
v_mfma_f32_16x16x32_bf16 v[24:27], v[204:207], a[96:99], v[24:27]
v_cvt_pk_bf16_f32 v138, v20, v21
v_cvt_pk_bf16_f32 v139, v22, v23
v_mfma_f32_16x16x32_bf16 v[56:59], v[204:207], a[100:103], v[56:59]
buffer_store_dwordx4 v[148:151], v255, s[48:51], s95 offen offset:1024 sc1
v_mfma_f32_16x16x32_bf16 v[88:91], v[204:207], a[104:107], v[88:91]
v_cvt_pk_bf16_f32 v152, v48, v49
v_cvt_pk_bf16_f32 v153, v50, v51
v_mfma_f32_16x16x32_bf16 v[120:123], v[204:207], a[108:111], v[120:123]
ds_read_b128 a[80:83], v248 offset:4096
ds_read_b128 a[84:87], v248 offset:5120
v_mfma_f32_16x16x32_bf16 v[24:27], v[208:211], a[112:115], v[24:27]
v_cvt_pk_bf16_f32 v154, v52, v53
v_cvt_pk_bf16_f32 v155, v54, v55
v_mfma_f32_16x16x32_bf16 v[56:59], v[208:211], a[116:119], v[56:59]
buffer_store_dwordx4 v[164:167], v255, s[48:51], s96 offen offset:1024 sc1
v_mfma_f32_16x16x32_bf16 v[88:91], v[208:211], a[120:123], v[88:91]
v_cvt_pk_bf16_f32 v168, v80, v81
v_cvt_pk_bf16_f32 v169, v82, v83
v_mfma_f32_16x16x32_bf16 v[120:123], v[208:211], a[124:127], v[120:123]
ds_read_b128 a[88:91], v248 offset:6144
ds_read_b128 a[92:95], v248 offset:7168
v_mfma_f32_16x16x32_bf16 v[28:31], v[212:215], a[192:195], v[28:31]
v_cvt_pk_bf16_f32 v170, v84, v85
v_cvt_pk_bf16_f32 v171, v86, v87
v_mfma_f32_16x16x32_bf16 v[60:63], v[212:215], a[196:199], v[60:63]
buffer_store_dwordx4 v[180:183], v255, s[48:51], s97 offen offset:1024 sc1
v_mfma_f32_16x16x32_bf16 v[92:95], v[212:215], a[200:203], v[92:95]
v_cvt_pk_bf16_f32 v184, v112, v113
v_cvt_pk_bf16_f32 v185, v114, v115
v_mfma_f32_16x16x32_bf16 v[124:127], v[212:215], a[204:207], v[124:127]
v_cvt_pk_bf16_f32 v186, v116, v117
v_cvt_pk_bf16_f32 v187, v118, v119
v_mfma_f32_16x16x32_bf16 v[28:31], v[216:219], a[96:99], v[28:31]
ds_write_b128 v238, a[48:51] offset:39936
v_mfma_f32_16x16x32_bf16 v[60:63], v[216:219], a[100:103], v[60:63]
v_lshrrev_b32 v241, 4, v241
v_add_u32 v241, 0x8888, v241
v_mfma_f32_16x16x32_bf16 v[92:95], v[216:219], a[104:107], v[92:95]
v_mfma_f32_16x16x32_bf16 v[124:127], v[216:219], a[108:111], v[124:127]
v_mfma_f32_16x16x32_bf16 v[28:31], v[220:223], a[112:115], v[28:31]
v_mfma_f32_16x16x32_bf16 v[60:63], v[220:223], a[116:119], v[60:63]
v_mfma_f32_16x16x32_bf16 v[92:95], v[220:223], a[120:123], v[92:95]
v_mfma_f32_16x16x32_bf16 v[124:127], v[220:223], a[124:127], v[124:127]
s_waitcnt lgkmcnt(1)
v_mfma_f32_16x16x32_bf16 v[208:211], a[64:67], v[160:163], v[32:35]
v_mfma_f32_16x16x32_bf16 v[216:219], a[64:67], v[176:179], 0
buffer_store_dwordx4 v[136:139], v255, s[48:51], 0 offen offset:2048 sc1
v_mfma_f32_16x16x32_bf16 v[212:215], a[80:83], v[160:163], v[36:39]
v_cvt_pk_bf16_f32 v172, v88, v89
v_cvt_pk_bf16_f32 v173, v90, v91
v_cvt_pk_bf16_f32 v174, v92, v93
v_mfma_f32_16x16x32_bf16 v[220:223], a[80:83], v[176:179], v[32:35]
v_cvt_pk_bf16_f32 v175, v94, v95
v_cvt_pk_bf16_f32 v188, v120, v121
v_cvt_pk_bf16_f32 v189, v122, v123
v_mfma_f32_16x16x32_bf16 v[208:211], a[68:71], v[164:167], v[208:211]
v_cvt_pk_bf16_f32 v190, v124, v125
v_cvt_pk_bf16_f32 v191, v126, v127
v_cvt_pk_bf16_f32 v140, v24, v25
v_mfma_f32_16x16x32_bf16 v[216:219], a[68:71], v[180:183], v[216:219]
buffer_store_dwordx4 v[152:155], v255, s[48:51], s95 offen offset:2048 sc1
v_cvt_pk_bf16_f32 v141, v26, v27
v_mfma_f32_16x16x32_bf16 v[212:215], a[84:87], v[164:167], v[212:215]
v_cvt_pk_bf16_f32 v142, v28, v29
v_cvt_pk_bf16_f32 v143, v30, v31
v_cvt_pk_bf16_f32 v156, v56, v57
v_mfma_f32_16x16x32_bf16 v[220:223], a[84:87], v[180:183], v[220:223]
v_cvt_pk_bf16_f32 v157, v58, v59
v_cvt_pk_bf16_f32 v158, v60, v61
v_cvt_pk_bf16_f32 v159, v62, v63
v_mfma_f32_16x16x32_bf16 v[208:211], a[72:75], v[168:171], v[208:211]
v_mfma_f32_16x16x32_bf16 v[216:219], a[72:75], v[184:187], v[216:219]
buffer_store_dwordx4 v[168:171], v255, s[48:51], s96 offen offset:2048 sc1
v_mfma_f32_16x16x32_bf16 v[212:215], a[88:91], v[168:171], v[212:215]
v_mfma_f32_16x16x32_bf16 v[220:223], a[88:91], v[184:187], v[220:223]
v_mfma_f32_16x16x32_bf16 v[208:211], a[76:79], v[172:175], v[208:211]
v_mfma_f32_16x16x32_bf16 v[216:219], a[76:79], v[188:191], v[216:219]
buffer_store_dwordx4 v[184:187], v255, s[48:51], s97 offen offset:2048 sc1
v_mfma_f32_16x16x32_bf16 v[212:215], a[92:95], v[172:175], v[212:215]
ds_read_b128 a[64:67], v248 offset:16384
v_mfma_f32_16x16x32_bf16 v[220:223], a[92:95], v[188:191], v[220:223]
ds_read_b128 a[96:99], v248 offset:8192
v_exp_f32 v0, v208
v_mfma_f32_16x16x32_bf16 v[64:67], a[232:235], a[232:235], 0
ds_read_b128 a[68:71], v248 offset:17408
v_exp_f32 v1, v209
v_mfma_f32_16x16x32_bf16 v[68:71], a[232:235], a[232:235], 0
ds_read_b128 a[100:103], v248 offset:9216
buffer_store_dwordx4 v[140:143], v255, s[48:51], 0 offen offset:3072 sc1
v_mfma_f32_16x16x32_bf16 v[72:75], a[232:235], a[232:235], 0
ds_read_b128 a[72:75], v248 offset:18432
v_exp_f32 v2, v210
v_mfma_f32_16x16x32_bf16 v[76:79], a[232:235], a[232:235], 0
ds_read_b128 a[104:107], v248 offset:10240
v_exp_f32 v3, v211
v_mfma_f32_16x16x32_bf16 v[80:83], a[232:235], a[232:235], 0
ds_read_b128 a[76:79], v248 offset:19456
v_exp_f32 v8, v216
v_mfma_f32_16x16x32_bf16 v[84:87], a[232:235], a[232:235], 0
ds_read_b128 a[108:111], v248 offset:11264
buffer_store_dwordx4 v[156:159], v255, s[48:51], s95 offen offset:3072 sc1
v_mfma_f32_16x16x32_bf16 v[88:91], a[232:235], a[232:235], 0
ds_read_b128 a[80:83], v248 offset:20480
v_exp_f32 v9, v217
v_mfma_f32_16x16x32_bf16 v[92:95], a[232:235], a[232:235], 0
ds_read_b128 a[112:115], v248 offset:12288
v_exp_f32 v10, v218
v_mfma_f32_16x16x32_bf16 v[96:99], a[232:235], a[232:235], 0
ds_read_b128 a[84:87], v248 offset:21504
v_exp_f32 v11, v219
v_mfma_f32_16x16x32_bf16 v[100:103], a[232:235], a[232:235], 0
ds_read_b128 a[116:119], v248 offset:13312
buffer_store_dwordx4 v[172:175], v255, s[48:51], s96 offen offset:3072 sc1
v_mfma_f32_16x16x32_bf16 v[104:107], a[232:235], a[232:235], 0
ds_read_b128 a[88:91], v248 offset:22528
v_exp_f32 v4, v212
v_mfma_f32_16x16x32_bf16 v[108:111], a[232:235], a[232:235], 0
ds_read_b128 a[120:123], v248 offset:14336
v_exp_f32 v5, v213
v_mfma_f32_16x16x32_bf16 v[112:115], a[232:235], a[232:235], 0
ds_read_b128 a[92:95], v248 offset:23552
v_exp_f32 v6, v214
v_mfma_f32_16x16x32_bf16 v[116:119], a[232:235], a[232:235], 0
ds_read_b128 a[124:127], v248 offset:15360
buffer_store_dwordx4 v[188:191], v255, s[48:51], s97 offen offset:3072 sc1
v_exp_f32 v7, v215
v_mfma_f32_16x16x32_bf16 v[120:123], a[232:235], a[232:235], 0
v_exp_f32 v12, v220
.La5_LW1_11:
s_cbranch_execz .La5_LW1_R0
v_mfma_f32_16x16x32_bf16 v[124:127], a[232:235], a[232:235], 0
v_accvgpr_write_b32 a192, 0
v_accvgpr_write_b32 a193, 0
v_accvgpr_write_b32 a194, 0
v_accvgpr_write_b32 a195, 0
v_accvgpr_write_b32 a196, 0
v_accvgpr_write_b32 a197, 0
v_accvgpr_write_b32 a198, 0
v_accvgpr_write_b32 a199, 0
v_add_u32 v248, 0x4000, v248
s_cmp_eq_u32 s3, 3
s_nop 0
s_nop 0
s_cbranch_scc1 .La5_LA_FH
.p2align 6
; ---- attention, 32 keys per window: S = K Q^T (causal mask through SrcC), P = exp2(S), O += V^T P, row sums l += 1 P; here the later 32 rows only
.La5_LA_HH:
s_waitcnt lgkmcnt(1)
v_mfma_f32_16x16x32_bf16 v[208:211], a[64:67], v[160:163], 0
v_exp_f32 v13, v221
v_mfma_f32_16x16x32_bf16 v[208:211], a[68:71], v[164:167], v[208:211]
v_cvt_pk_bf16_f32 v232, v0, v1
v_cvt_pk_bf16_f32 v233, v2, v3
v_mfma_f32_16x16x32_bf16 v[208:211], a[72:75], v[168:171], v[208:211]
v_exp_f32 v14, v222
v_mfma_f32_16x16x32_bf16 v[208:211], a[76:79], v[172:175], v[208:211]
v_cvt_pk_bf16_f32 v236, v8, v9
v_cvt_pk_bf16_f32 v237, v10, v11
v_mfma_f32_16x16x32_bf16 v[216:219], a[64:67], v[176:179], 0
v_exp_f32 v15, v223
v_mfma_f32_16x16x32_bf16 v[216:219], a[68:71], v[180:183], v[216:219]
v_cvt_pk_bf16_f32 v234, v4, v5
v_cvt_pk_bf16_f32 v235, v6, v7
v_mfma_f32_16x16x32_bf16 v[216:219], a[72:75], v[184:187], v[216:219]
v_exp_f32 v0, v208
v_mfma_f32_16x16x32_bf16 v[216:219], a[76:79], v[188:191], v[216:219]
v_cvt_pk_bf16_f32 v238, v12, v13
v_cvt_pk_bf16_f32 v239, v14, v15
v_mfma_f32_16x16x32_bf16 v[212:215], a[80:83], v[160:163], 0
v_exp_f32 v1, v209
v_mfma_f32_16x16x32_bf16 v[212:215], a[84:87], v[164:167], v[212:215]
ds_read_b128 a[64:67], v248 offset:16384
ds_read_b128 a[68:71], v248 offset:17408
v_mfma_f32_16x16x32_bf16 v[212:215], a[88:91], v[168:171], v[212:215]
v_exp_f32 v2, v210
v_mfma_f32_16x16x32_bf16 v[212:215], a[92:95], v[172:175], v[212:215]
ds_read_b128 a[72:75], v248 offset:18432
ds_read_b128 a[76:79], v248 offset:19456
v_mfma_f32_16x16x32_bf16 v[220:223], a[80:83], v[176:179], 0
v_exp_f32 v3, v211
v_mfma_f32_16x16x32_bf16 v[220:223], a[84:87], v[180:183], v[220:223]
ds_read_b128 a[80:83], v248 offset:20480
v_mfma_f32_16x16x32_bf16 v[220:223], a[88:91], v[184:187], v[220:223]
v_exp_f32 v8, v216
v_mfma_f32_16x16x32_bf16 v[220:223], a[92:95], v[188:191], v[220:223]
s_waitcnt lgkmcnt(5)
ds_read_b128 a[84:87], v248 offset:21504
v_mfma_f32_16x16x32_bf16 v[64:67], a[96:99], v[232:235], v[64:67]
v_exp_f32 v9, v217
v_mfma_f32_16x16x32_bf16 v[96:99], a[96:99], v[236:239], v[96:99]
ds_read_b128 a[96:99], v248 offset:8192
ds_read_b128 a[88:91], v248 offset:22528
v_mfma_f32_16x16x32_bf16 v[68:71], a[100:103], v[232:235], v[68:71]
v_exp_f32 v10, v218
v_mfma_f32_16x16x32_bf16 v[100:103], a[100:103], v[236:239], v[100:103]
ds_read_b128 a[100:103], v248 offset:9216
ds_read_b128 a[92:95], v248 offset:23552
v_mfma_f32_16x16x32_bf16 v[72:75], a[104:107], v[232:235], v[72:75]
v_exp_f32 v11, v219
v_mfma_f32_16x16x32_bf16 v[104:107], a[104:107], v[236:239], v[104:107]
ds_read_b128 a[104:107], v248 offset:10240
buffer_load_dword v250, v251, s[64:67], s20 offen sc1
v_mfma_f32_16x16x32_bf16 v[76:79], a[108:111], v[232:235], v[76:79]
v_exp_f32 v4, v212
v_mfma_f32_16x16x32_bf16 v[108:111], a[108:111], v[236:239], v[108:111]
ds_read_b128 a[108:111], v248 offset:11264
s_add_u32 s85, s85, 1
v_mfma_f32_16x16x32_bf16 v[80:83], a[112:115], v[232:235], v[80:83]
v_exp_f32 v5, v213
v_mfma_f32_16x16x32_bf16 v[112:115], a[112:115], v[236:239], v[112:115]
ds_read_b128 a[112:115], v248 offset:12288
s_add_u32 s89, s89, 32768
v_mfma_f32_16x16x32_bf16 v[84:87], a[116:119], v[232:235], v[84:87]
v_exp_f32 v6, v214
v_mfma_f32_16x16x32_bf16 v[116:119], a[116:119], v[236:239], v[116:119]
ds_read_b128 a[116:119], v248 offset:13312
s_add_u32 s92, s85, s3
v_mfma_f32_16x16x32_bf16 v[88:91], a[120:123], v[232:235], v[88:91]
v_exp_f32 v7, v215
v_mfma_f32_16x16x32_bf16 v[120:123], a[120:123], v[236:239], v[120:123]
ds_read_b128 a[120:123], v248 offset:14336
s_bitcmp1_b64 s[98:99], s85
v_mfma_f32_16x16x32_bf16 v[92:95], a[124:127], v[232:235], v[92:95]
v_exp_f32 v12, v220
v_mfma_f32_16x16x32_bf16 v[124:127], a[124:127], v[236:239], v[124:127]
ds_read_b128 a[124:127], v248 offset:15360
s_barrier
v_smfmac_f32_16x16x64_bf16 a[196:199], a[208:211], v[232:239], v241
s_waitcnt lgkmcnt(6)
v_mfma_f32_16x16x32_bf16 v[208:211], a[64:67], v[160:163], 0
v_exp_f32 v13, v221
v_mfma_f32_16x16x32_bf16 v[208:211], a[68:71], v[164:167], v[208:211]
v_cvt_pk_bf16_f32 v232, v0, v1
v_cvt_pk_bf16_f32 v233, v2, v3
v_mfma_f32_16x16x32_bf16 v[208:211], a[72:75], v[168:171], v[208:211]
v_exp_f32 v14, v222
v_mfma_f32_16x16x32_bf16 v[208:211], a[76:79], v[172:175], v[208:211]
v_cvt_pk_bf16_f32 v236, v8, v9
v_cvt_pk_bf16_f32 v237, v10, v11
v_mfma_f32_16x16x32_bf16 v[216:219], a[64:67], v[176:179], 0
v_exp_f32 v15, v223
v_mfma_f32_16x16x32_bf16 v[216:219], a[68:71], v[180:183], v[216:219]
v_cvt_pk_bf16_f32 v234, v4, v5
v_cvt_pk_bf16_f32 v235, v6, v7
v_mfma_f32_16x16x32_bf16 v[216:219], a[72:75], v[184:187], v[216:219]
v_exp_f32 v0, v208
v_mfma_f32_16x16x32_bf16 v[216:219], a[76:79], v[188:191], v[216:219]
v_cvt_pk_bf16_f32 v238, v12, v13
v_cvt_pk_bf16_f32 v239, v14, v15
v_mfma_f32_16x16x32_bf16 v[212:215], a[80:83], v[160:163], 0
v_exp_f32 v1, v209
v_mfma_f32_16x16x32_bf16 v[212:215], a[84:87], v[164:167], v[212:215]
ds_read_b128 a[64:67], v248 offset:32768
ds_read_b128 a[68:71], v248 offset:33792
v_mfma_f32_16x16x32_bf16 v[212:215], a[88:91], v[168:171], v[212:215]
v_exp_f32 v2, v210
v_mfma_f32_16x16x32_bf16 v[212:215], a[92:95], v[172:175], v[212:215]
ds_read_b128 a[72:75], v248 offset:34816
ds_read_b128 a[76:79], v248 offset:35840
v_mfma_f32_16x16x32_bf16 v[220:223], a[80:83], v[176:179], 0
v_exp_f32 v3, v211
v_mfma_f32_16x16x32_bf16 v[220:223], a[84:87], v[180:183], v[220:223]
ds_read_b128 a[80:83], v248 offset:36864
v_mfma_f32_16x16x32_bf16 v[220:223], a[88:91], v[184:187], v[220:223]
v_exp_f32 v8, v216
v_mfma_f32_16x16x32_bf16 v[220:223], a[92:95], v[188:191], v[220:223]
s_waitcnt lgkmcnt(5)
ds_read_b128 a[84:87], v248 offset:37888
v_mfma_f32_16x16x32_bf16 v[64:67], a[96:99], v[232:235], v[64:67]
v_exp_f32 v9, v217
v_mfma_f32_16x16x32_bf16 v[96:99], a[96:99], v[236:239], v[96:99]
ds_read_b128 a[96:99], v248 offset:24576
ds_read_b128 a[88:91], v248 offset:38912
v_mfma_f32_16x16x32_bf16 v[68:71], a[100:103], v[232:235], v[68:71]
v_exp_f32 v10, v218
v_mfma_f32_16x16x32_bf16 v[100:103], a[100:103], v[236:239], v[100:103]
ds_read_b128 a[100:103], v248 offset:25600
ds_read_b128 a[92:95], v248 offset:39936
v_mfma_f32_16x16x32_bf16 v[72:75], a[104:107], v[232:235], v[72:75]
v_exp_f32 v11, v219
v_mfma_f32_16x16x32_bf16 v[104:107], a[104:107], v[236:239], v[104:107]
ds_read_b128 a[104:107], v248 offset:26624
v_mfma_f32_16x16x32_bf16 v[76:79], a[108:111], v[232:235], v[76:79]
v_exp_f32 v4, v212
v_mfma_f32_16x16x32_bf16 v[108:111], a[108:111], v[236:239], v[108:111]
ds_read_b128 a[108:111], v248 offset:27648
v_mfma_f32_16x16x32_bf16 v[80:83], a[112:115], v[232:235], v[80:83]
v_exp_f32 v5, v213
v_mfma_f32_16x16x32_bf16 v[112:115], a[112:115], v[236:239], v[112:115]
ds_read_b128 a[112:115], v248 offset:28672
v_mfma_f32_16x16x32_bf16 v[84:87], a[116:119], v[232:235], v[84:87]
v_exp_f32 v6, v214
v_mfma_f32_16x16x32_bf16 v[116:119], a[116:119], v[236:239], v[116:119]
ds_read_b128 a[116:119], v248 offset:29696
s_waitcnt vmcnt(1)
buffer_store_dwordx2 v[252:253], off, s[64:67], s86 sc1
s_mov_b32 s86, 1073741824
v_mfma_f32_16x16x32_bf16 v[88:91], a[120:123], v[232:235], v[88:91]
v_exp_f32_e64 v7, v215
v_mfma_f32_16x16x32_bf16 v[120:123], a[120:123], v[236:239], v[120:123]
ds_read_b128 a[120:123], v248 offset:30720
v_mfma_f32_16x16x32_bf16 v[92:95], a[124:127], v[232:235], v[92:95]
v_exp_f32_e64 v12, v220
v_mfma_f32_16x16x32_bf16 v[124:127], a[124:127], v[236:239], v[124:127]
ds_read_b128 a[124:127], v248 offset:31744
v_smfmac_f32_16x16x64_bf16 a[196:199], a[208:211], v[232:239], v241
v_add_u32 v248, 0x8000, v248
s_cbranch_scc0 .La5_LA_HH1
s_call_b64 s[24:25], .La5_LA_EVENT
s_add_u32 s92, s85, s3
.La5_LA_HH1:
s_cmp_lt_u32 s92, 3
s_cbranch_scc1 .La5_LA_HH
; ---- first window with both row groups (diagonal of the earlier group)
.La5_LA_FH:
.La5_LW2_0:
s_cbranch_execz .La5_LW2_4
s_waitcnt lgkmcnt(9)
v_mfma_f32_16x16x32_bf16 v[192:195], a[64:67], v[128:131], v[32:35]
v_exp_f32 v13, v221
v_mfma_f32_16x16x32_bf16 v[192:195], a[68:71], v[132:135], v[192:195]
v_cvt_pk_bf16_f32 v232, v0, v1
v_cvt_pk_bf16_f32 v233, v2, v3
v_mfma_f32_16x16x32_bf16 v[192:195], a[72:75], v[136:139], v[192:195]
v_exp_f32 v14, v222
v_mfma_f32_16x16x32_bf16 v[192:195], a[76:79], v[140:143], v[192:195]
buffer_load_dwordx4 v255, s[60:63], s17 offen offset:0 sc1 lds
v_mfma_f32_16x16x32_bf16 v[200:203], a[64:67], v[144:147], 0
v_exp_f32 v15, v223
v_mfma_f32_16x16x32_bf16 v[200:203], a[68:71], v[148:151], v[200:203]
v_cvt_pk_bf16_f32 v236, v8, v9
v_cvt_pk_bf16_f32 v237, v10, v11
v_mfma_f32_16x16x32_bf16 v[200:203], a[72:75], v[152:155], v[200:203]
v_exp_f32 v192, v192
v_mfma_f32_16x16x32_bf16 v[200:203], a[76:79], v[156:159], v[200:203]
s_waitcnt lgkmcnt(1)
v_cvt_pk_bf16_f32 v234, v4, v5
v_mfma_f32_16x16x32_bf16 v[196:199], a[80:83], v[128:131], v[36:39]
v_exp_f32 v193, v193
v_mfma_f32_16x16x32_bf16 v[196:199], a[84:87], v[132:135], v[196:199]
v_cvt_pk_bf16_f32 v224, v192, v193
s_add_u32 s85, s85, 1
v_mfma_f32_16x16x32_bf16 v[196:199], a[88:91], v[136:139], v[196:199]
v_exp_f32 v194, v194
v_mfma_f32_16x16x32_bf16 v[196:199], a[92:95], v[140:143], v[196:199]
buffer_load_dwordx4 v255, s[60:63], s17 offen offset:1024 sc1 lds
v_mfma_f32_16x16x32_bf16 v[204:207], a[80:83], v[144:147], v[32:35]
v_exp_f32 v195, v195
v_mfma_f32_16x16x32_bf16 v[204:207], a[84:87], v[148:151], v[204:207]
v_cvt_pk_bf16_f32 v225, v194, v195
s_add_u32 s89, s89, 16384
v_mfma_f32_16x16x32_bf16 v[204:207], a[88:91], v[152:155], v[204:207]
v_exp_f32 v200, v200
v_mfma_f32_16x16x32_bf16 v[204:207], a[92:95], v[156:159], v[204:207]
buffer_load_dwordx4 v255, s[60:63], s17 offen offset:2048 sc1 lds
v_mfma_f32_16x16x32_bf16 v[208:211], a[64:67], v[160:163], 0
v_exp_f32 v201, v201
v_mfma_f32_16x16x32_bf16 v[208:211], a[68:71], v[164:167], v[208:211]
v_cvt_pk_bf16_f32 v228, v200, v201
s_and_b32 s89, s89, 0x1ffff
v_mfma_f32_16x16x32_bf16 v[208:211], a[72:75], v[168:171], v[208:211]
v_exp_f32 v202, v202
v_mfma_f32_16x16x32_bf16 v[208:211], a[76:79], v[172:175], v[208:211]
buffer_load_dwordx4 v255, s[60:63], s17 offen offset:3072 sc1 lds
v_mfma_f32_16x16x32_bf16 v[216:219], a[64:67], v[176:179], 0
v_exp_f32 v203, v203
v_mfma_f32_16x16x32_bf16 v[216:219], a[68:71], v[180:183], v[216:219]
v_cvt_pk_bf16_f32 v229, v202, v203
v_mfma_f32_16x16x32_bf16 v[216:219], a[72:75], v[184:187], v[216:219]
v_exp_f32 v196, v196
v_mfma_f32_16x16x32_bf16 v[216:219], a[76:79], v[188:191], v[216:219]
v_cvt_pk_bf16_f32 v235, v6, v7
v_cvt_pk_bf16_f32 v238, v12, v13
v_mfma_f32_16x16x32_bf16 v[212:215], a[80:83], v[160:163], 0
v_exp_f32 v197, v197
v_mfma_f32_16x16x32_bf16 v[212:215], a[84:87], v[164:167], v[212:215]
v_cvt_pk_bf16_f32 v226, v196, v197
s_add_u32 s17, s17, 8192
v_mfma_f32_16x16x32_bf16 v[212:215], a[88:91], v[168:171], v[212:215]
v_exp_f32 v198, v198
v_mfma_f32_16x16x32_bf16 v[212:215], a[92:95], v[172:175], v[212:215]
v_cvt_pk_bf16_f32 v239, v14, v15
s_waitcnt lgkmcnt(0)
v_mfma_f32_16x16x32_bf16 v[220:223], a[80:83], v[176:179], 0
v_exp_f32 v199, v199
v_mfma_f32_16x16x32_bf16 v[220:223], a[84:87], v[180:183], v[220:223]
v_cvt_pk_bf16_f32 v227, v198, v199
s_add_u32 m0, m0, 16384
v_mfma_f32_16x16x32_bf16 v[220:223], a[88:91], v[184:187], v[220:223]
v_exp_f32 v204, v204
v_mfma_f32_16x16x32_bf16 v[220:223], a[92:95], v[188:191], v[220:223]
s_waitcnt vmcnt(11)
s_barrier
v_mfma_f32_16x16x32_bf16 v[64:67], a[96:99], v[232:235], v[64:67]
v_exp_f32 v205, v205
v_mfma_f32_16x16x32_bf16 v[96:99], a[96:99], v[236:239], v[96:99]
ds_read_b128 a[96:99], v248 offset:8192
v_cvt_pk_bf16_f32 v230, v204, v205
v_mfma_f32_16x16x32_bf16 v[68:71], a[100:103], v[232:235], v[68:71]
v_exp_f32 v206, v206
v_mfma_f32_16x16x32_bf16 v[100:103], a[100:103], v[236:239], v[100:103]
ds_read_b128 a[100:103], v248 offset:9216
s_and_b32 m0, m0, s78
buffer_load_dword v250, v251, s[64:67], s20 offen sc1
v_mfma_f32_16x16x32_bf16 v[72:75], a[104:107], v[232:235], v[72:75]
v_exp_f32 v207, v207
v_mfma_f32_16x16x32_bf16 v[104:107], a[104:107], v[236:239], v[104:107]
ds_read_b128 a[104:107], v248 offset:10240
v_cvt_pk_bf16_f32 v231, v206, v207
v_mfma_f32_16x16x32_bf16 v[76:79], a[108:111], v[232:235], v[76:79]
v_exp_f32 v208, v208
v_mfma_f32_16x16x32_bf16 v[108:111], a[108:111], v[236:239], v[108:111]
ds_read_b128 a[108:111], v248 offset:11264
v_mfma_f32_16x16x32_bf16 v[80:83], a[112:115], v[232:235], v[80:83]
v_exp_f32 v209, v209
v_mfma_f32_16x16x32_bf16 v[112:115], a[112:115], v[236:239], v[112:115]
ds_read_b128 a[112:115], v248 offset:12288
v_mfma_f32_16x16x32_bf16 v[84:87], a[116:119], v[232:235], v[84:87]
v_exp_f32 v210, v210
v_mfma_f32_16x16x32_bf16 v[116:119], a[116:119], v[236:239], v[116:119]
ds_read_b128 a[116:119], v248 offset:13312
v_mfma_f32_16x16x32_bf16 v[88:91], a[120:123], v[232:235], v[88:91]
v_exp_f32 v211, v211
v_mfma_f32_16x16x32_bf16 v[120:123], a[120:123], v[236:239], v[120:123]
ds_read_b128 a[120:123], v248 offset:14336
s_bitcmp1_b64 s[98:99], s85
v_mfma_f32_16x16x32_bf16 v[92:95], a[124:127], v[232:235], v[92:95]
v_exp_f32 v216, v216
v_mfma_f32_16x16x32_bf16 v[124:127], a[124:127], v[236:239], v[124:127]
ds_read_b128 a[124:127], v248 offset:15360
v_or_b32 v248, s89, v255
v_smfmac_f32_16x16x64_bf16 a[196:199], a[208:211], v[232:239], v241
s_waitcnt lgkmcnt(4)
v_smfmac_f32_16x16x64_bf16 a[192:195], a[208:211], v[224:231], v241
ds_read_b128 a[64:67], v248 offset:0
v_mfma_f32_16x16x32_bf16 v[0:3], a[96:99], v[224:227], 0
v_exp_f32 v217, v217
v_mfma_f32_16x16x32_bf16 v[32:35], a[96:99], v[228:231], 0
ds_read_b128 a[68:71], v248 offset:1024
v_cvt_pk_bf16_f32 v232, v208, v209
v_mfma_f32_16x16x32_bf16 v[4:7], a[100:103], v[224:227], 0
v_exp_f32 v218, v218
v_mfma_f32_16x16x32_bf16 v[36:39], a[100:103], v[228:231], 0
ds_read_b128 a[72:75], v248 offset:2048
v_cvt_pk_bf16_f32 v233, v210, v211
v_mfma_f32_16x16x32_bf16 v[8:11], a[104:107], v[224:227], 0
v_exp_f32 v219, v219
v_mfma_f32_16x16x32_bf16 v[40:43], a[104:107], v[228:231], 0
ds_read_b128 a[76:79], v248 offset:3072
v_cvt_pk_bf16_f32 v236, v216, v217
v_mfma_f32_16x16x32_bf16 v[12:15], a[108:111], v[224:227], 0
v_exp_f32 v212, v212
v_mfma_f32_16x16x32_bf16 v[44:47], a[108:111], v[228:231], 0
ds_read_b128 a[80:83], v248 offset:4096
s_waitcnt lgkmcnt(5)
v_mfma_f32_16x16x32_bf16 v[16:19], a[112:115], v[224:227], 0
v_exp_f32 v213, v213
v_mfma_f32_16x16x32_bf16 v[48:51], a[112:115], v[228:231], 0
ds_read_b128 a[84:87], v248 offset:5120
v_cvt_pk_bf16_f32 v237, v218, v219
v_mfma_f32_16x16x32_bf16 v[20:23], a[116:119], v[224:227], 0
v_exp_f32 v214, v214
v_mfma_f32_16x16x32_bf16 v[52:55], a[116:119], v[228:231], 0
ds_read_b128 a[88:91], v248 offset:6144
v_cvt_pk_bf16_f32 v234, v212, v213
v_mfma_f32_16x16x32_bf16 v[24:27], a[120:123], v[224:227], 0
v_exp_f32 v215, v215
v_mfma_f32_16x16x32_bf16 v[56:59], a[120:123], v[228:231], 0
ds_read_b128 a[92:95], v248 offset:7168
v_cvt_pk_bf16_f32 v235, v214, v215
v_mfma_f32_16x16x32_bf16 v[28:31], a[124:127], v[224:227], 0
v_exp_f32_e64 v220, v220
v_mfma_f32_16x16x32_bf16 v[60:63], a[124:127], v[228:231], 0
s_waitcnt vmcnt(5)
buffer_store_dwordx2 v[252:253], off, s[64:67], s86 sc1
s_mov_b32 s86, 1073741824
s_cbranch_scc1 .La5_LA_FE0
.La5_LA_FS0:
s_waitcnt lgkmcnt(4)
v_mfma_f32_16x16x32_bf16 v[192:195], a[64:67], v[128:131], 0
v_exp_f32 v221, v221
v_mfma_f32_16x16x32_bf16 v[192:195], a[68:71], v[132:135], v[192:195]
v_cvt_pk_bf16_f32 v238, v220, v221
s_add_u32 s85, s85, 1
v_mfma_f32_16x16x32_bf16 v[192:195], a[72:75], v[136:139], v[192:195]
v_exp_f32 v222, v222
v_mfma_f32_16x16x32_bf16 v[192:195], a[76:79], v[140:143], v[192:195]
buffer_load_dwordx4 v255, s[60:63], s17 offen offset:0 sc1 lds
v_mfma_f32_16x16x32_bf16 v[200:203], a[64:67], v[144:147], 0
v_exp_f32 v223, v223
v_mfma_f32_16x16x32_bf16 v[200:203], a[68:71], v[148:151], v[200:203]
v_cvt_pk_bf16_f32 v239, v222, v223
s_add_u32 s89, s89, 16384
v_mfma_f32_16x16x32_bf16 v[200:203], a[72:75], v[152:155], v[200:203]
v_exp_f32 v192, v192
v_mfma_f32_16x16x32_bf16 v[200:203], a[76:79], v[156:159], v[200:203]
s_waitcnt lgkmcnt(0)
s_and_b32 s89, s89, 0x1ffff
buffer_load_dword v250, v251, s[64:67], s20 offen sc1
v_mfma_f32_16x16x32_bf16 v[196:199], a[80:83], v[128:131], 0
v_exp_f32 v193, v193
v_mfma_f32_16x16x32_bf16 v[196:199], a[84:87], v[132:135], v[196:199]
v_cvt_pk_bf16_f32 v224, v192, v193
v_mfma_f32_16x16x32_bf16 v[196:199], a[88:91], v[136:139], v[196:199]
v_exp_f32 v194, v194
v_mfma_f32_16x16x32_bf16 v[196:199], a[92:95], v[140:143], v[196:199]
buffer_load_dwordx4 v255, s[60:63], s17 offen offset:1024 sc1 lds
v_mfma_f32_16x16x32_bf16 v[204:207], a[80:83], v[144:147], 0
v_exp_f32 v195, v195
v_mfma_f32_16x16x32_bf16 v[204:207], a[84:87], v[148:151], v[204:207]
v_cvt_pk_bf16_f32 v225, v194, v195
v_mfma_f32_16x16x32_bf16 v[204:207], a[88:91], v[152:155], v[204:207]
v_exp_f32 v200, v200
v_mfma_f32_16x16x32_bf16 v[204:207], a[92:95], v[156:159], v[204:207]
buffer_load_dwordx4 v255, s[60:63], s17 offen offset:2048 sc1 lds
v_mfma_f32_16x16x32_bf16 v[208:211], a[64:67], v[160:163], 0
v_exp_f32 v201, v201
v_mfma_f32_16x16x32_bf16 v[208:211], a[68:71], v[164:167], v[208:211]
v_cvt_pk_bf16_f32 v228, v200, v201
v_mfma_f32_16x16x32_bf16 v[208:211], a[72:75], v[168:171], v[208:211]
v_exp_f32 v202, v202
v_mfma_f32_16x16x32_bf16 v[208:211], a[76:79], v[172:175], v[208:211]
buffer_load_dwordx4 v255, s[60:63], s17 offen offset:3072 sc1 lds
v_mfma_f32_16x16x32_bf16 v[216:219], a[64:67], v[176:179], 0
v_exp_f32 v203, v203
v_mfma_f32_16x16x32_bf16 v[216:219], a[68:71], v[180:183], v[216:219]
v_cvt_pk_bf16_f32 v229, v202, v203
v_mfma_f32_16x16x32_bf16 v[216:219], a[72:75], v[184:187], v[216:219]
v_exp_f32 v196, v196
v_mfma_f32_16x16x32_bf16 v[216:219], a[76:79], v[188:191], v[216:219]
buffer_load_dword a253, v254, s[36:39], s90 offen
v_mfma_f32_16x16x32_bf16 v[212:215], a[80:83], v[160:163], 0
v_exp_f32 v197, v197
v_mfma_f32_16x16x32_bf16 v[212:215], a[84:87], v[164:167], v[212:215]
v_cvt_pk_bf16_f32 v226, v196, v197
s_add_u32 s17, s17, 8192
v_mfma_f32_16x16x32_bf16 v[212:215], a[88:91], v[168:171], v[212:215]
v_exp_f32 v198, v198
v_mfma_f32_16x16x32_bf16 v[212:215], a[92:95], v[172:175], v[212:215]
s_add_u32 m0, m0, 16384
s_and_b32 m0, m0, s78
v_mfma_f32_16x16x32_bf16 v[220:223], a[80:83], v[176:179], 0
v_exp_f32 v199, v199
v_mfma_f32_16x16x32_bf16 v[220:223], a[84:87], v[180:183], v[220:223]
v_cvt_pk_bf16_f32 v227, v198, v199
s_add_u32 s90, s90, s21
v_mfma_f32_16x16x32_bf16 v[220:223], a[88:91], v[184:187], v[220:223]
v_exp_f32 v204, v204
v_mfma_f32_16x16x32_bf16 v[220:223], a[92:95], v[188:191], v[220:223]
s_waitcnt vmcnt(13)
s_barrier
v_mfma_f32_16x16x32_bf16 v[64:67], a[96:99], v[232:235], v[64:67]
v_exp_f32 v205, v205
v_mfma_f32_16x16x32_bf16 v[96:99], a[96:99], v[236:239], v[96:99]
ds_read_b128 a[96:99], v248 offset:8192
v_cvt_pk_bf16_f32 v230, v204, v205
v_mfma_f32_16x16x32_bf16 v[68:71], a[100:103], v[232:235], v[68:71]
v_exp_f32 v206, v206
v_mfma_f32_16x16x32_bf16 v[100:103], a[100:103], v[236:239], v[100:103]
ds_read_b128 a[100:103], v248 offset:9216
v_mfma_f32_16x16x32_bf16 v[72:75], a[104:107], v[232:235], v[72:75]
v_exp_f32 v207, v207
v_mfma_f32_16x16x32_bf16 v[104:107], a[104:107], v[236:239], v[104:107]
ds_read_b128 a[104:107], v248 offset:10240
v_cvt_pk_bf16_f32 v231, v206, v207
v_mfma_f32_16x16x32_bf16 v[76:79], a[108:111], v[232:235], v[76:79]
v_exp_f32 v208, v208
v_mfma_f32_16x16x32_bf16 v[108:111], a[108:111], v[236:239], v[108:111]
ds_read_b128 a[108:111], v248 offset:11264
v_mfma_f32_16x16x32_bf16 v[80:83], a[112:115], v[232:235], v[80:83]
v_exp_f32 v209, v209
v_mfma_f32_16x16x32_bf16 v[112:115], a[112:115], v[236:239], v[112:115]
ds_read_b128 a[112:115], v248 offset:12288
v_mfma_f32_16x16x32_bf16 v[84:87], a[116:119], v[232:235], v[84:87]
v_exp_f32 v210, v210
v_mfma_f32_16x16x32_bf16 v[116:119], a[116:119], v[236:239], v[116:119]
ds_read_b128 a[116:119], v248 offset:13312
v_mfma_f32_16x16x32_bf16 v[88:91], a[120:123], v[232:235], v[88:91]
v_exp_f32 v211, v211
v_mfma_f32_16x16x32_bf16 v[120:123], a[120:123], v[236:239], v[120:123]
ds_read_b128 a[120:123], v248 offset:14336
s_bitcmp1_b64 s[98:99], s85
v_mfma_f32_16x16x32_bf16 v[92:95], a[124:127], v[232:235], v[92:95]
v_exp_f32 v216, v216
v_mfma_f32_16x16x32_bf16 v[124:127], a[124:127], v[236:239], v[124:127]
ds_read_b128 a[124:127], v248 offset:15360
v_or_b32 v248, s89, v255
v_smfmac_f32_16x16x64_bf16 a[196:199], a[208:211], v[232:239], v241
s_waitcnt lgkmcnt(4)
v_smfmac_f32_16x16x64_bf16 a[192:195], a[208:211], v[224:231], v241
ds_read_b128 a[64:67], v248 offset:0
v_mfma_f32_16x16x32_bf16 v[0:3], a[96:99], v[224:227], v[0:3]
v_exp_f32 v217, v217
v_mfma_f32_16x16x32_bf16 v[32:35], a[96:99], v[228:231], v[32:35]
ds_read_b128 a[68:71], v248 offset:1024
v_cvt_pk_bf16_f32 v232, v208, v209
v_mfma_f32_16x16x32_bf16 v[4:7], a[100:103], v[224:227], v[4:7]
v_exp_f32 v218, v218
v_mfma_f32_16x16x32_bf16 v[36:39], a[100:103], v[228:231], v[36:39]
ds_read_b128 a[72:75], v248 offset:2048
v_cvt_pk_bf16_f32 v233, v210, v211
v_mfma_f32_16x16x32_bf16 v[8:11], a[104:107], v[224:227], v[8:11]
v_exp_f32 v219, v219
v_mfma_f32_16x16x32_bf16 v[40:43], a[104:107], v[228:231], v[40:43]
ds_read_b128 a[76:79], v248 offset:3072
v_cvt_pk_bf16_f32 v236, v216, v217
v_mfma_f32_16x16x32_bf16 v[12:15], a[108:111], v[224:227], v[12:15]
v_exp_f32 v212, v212
v_mfma_f32_16x16x32_bf16 v[44:47], a[108:111], v[228:231], v[44:47]
ds_read_b128 a[80:83], v248 offset:4096
s_waitcnt lgkmcnt(5)
v_mfma_f32_16x16x32_bf16 v[16:19], a[112:115], v[224:227], v[16:19]
v_exp_f32 v213, v213
v_mfma_f32_16x16x32_bf16 v[48:51], a[112:115], v[228:231], v[48:51]
ds_read_b128 a[84:87], v248 offset:5120
v_cvt_pk_bf16_f32 v237, v218, v219
v_mfma_f32_16x16x32_bf16 v[20:23], a[116:119], v[224:227], v[20:23]
v_exp_f32 v214, v214
v_mfma_f32_16x16x32_bf16 v[52:55], a[116:119], v[228:231], v[52:55]
ds_read_b128 a[88:91], v248 offset:6144
v_cvt_pk_bf16_f32 v234, v212, v213
v_mfma_f32_16x16x32_bf16 v[24:27], a[120:123], v[224:227], v[24:27]
v_exp_f32 v215, v215
v_mfma_f32_16x16x32_bf16 v[56:59], a[120:123], v[228:231], v[56:59]
ds_read_b128 a[92:95], v248 offset:7168
v_cvt_pk_bf16_f32 v235, v214, v215
v_mfma_f32_16x16x32_bf16 v[28:31], a[124:127], v[224:227], v[28:31]
v_exp_f32 v220, v220
v_mfma_f32_16x16x32_bf16 v[60:63], a[124:127], v[228:231], v[60:63]
s_cbranch_scc1 .La5_LA_F1E2
.p2align 6
; ---- steady windows
.La5_LA_F1:
s_waitcnt lgkmcnt(4)
v_mfma_f32_16x16x32_bf16 v[192:195], a[64:67], v[128:131], 0
v_exp_f32 v221, v221
v_mfma_f32_16x16x32_bf16 v[192:195], a[68:71], v[132:135], v[192:195]
v_cvt_pk_bf16_f32 v238, v220, v221
s_add_u32 s85, s85, 1
v_mfma_f32_16x16x32_bf16 v[192:195], a[72:75], v[136:139], v[192:195]
v_exp_f32 v222, v222
v_mfma_f32_16x16x32_bf16 v[192:195], a[76:79], v[140:143], v[192:195]
buffer_load_dwordx4 v255, s[60:63], s17 offen offset:0 sc1 lds
v_mfma_f32_16x16x32_bf16 v[200:203], a[64:67], v[144:147], 0
v_exp_f32 v223, v223
v_mfma_f32_16x16x32_bf16 v[200:203], a[68:71], v[148:151], v[200:203]
v_cvt_pk_bf16_f32 v239, v222, v223
s_add_u32 s89, s89, 16384
v_mfma_f32_16x16x32_bf16 v[200:203], a[72:75], v[152:155], v[200:203]
v_exp_f32 v192, v192
v_mfma_f32_16x16x32_bf16 v[200:203], a[76:79], v[156:159], v[200:203]
s_waitcnt lgkmcnt(0)
s_and_b32 s89, s89, 0x1ffff
v_mfma_f32_16x16x32_bf16 v[196:199], a[80:83], v[128:131], 0
v_exp_f32 v193, v193
v_mfma_f32_16x16x32_bf16 v[196:199], a[84:87], v[132:135], v[196:199]
v_cvt_pk_bf16_f32 v224, v192, v193
v_mfma_f32_16x16x32_bf16 v[196:199], a[88:91], v[136:139], v[196:199]
v_exp_f32 v194, v194
v_mfma_f32_16x16x32_bf16 v[196:199], a[92:95], v[140:143], v[196:199]
buffer_load_dwordx4 v255, s[60:63], s17 offen offset:1024 sc1 lds
v_mfma_f32_16x16x32_bf16 v[204:207], a[80:83], v[144:147], 0
v_exp_f32 v195, v195
v_mfma_f32_16x16x32_bf16 v[204:207], a[84:87], v[148:151], v[204:207]
v_cvt_pk_bf16_f32 v225, v194, v195
v_mfma_f32_16x16x32_bf16 v[204:207], a[88:91], v[152:155], v[204:207]
v_exp_f32 v200, v200
v_mfma_f32_16x16x32_bf16 v[204:207], a[92:95], v[156:159], v[204:207]
buffer_load_dwordx4 v255, s[60:63], s17 offen offset:2048 sc1 lds
v_mfma_f32_16x16x32_bf16 v[208:211], a[64:67], v[160:163], 0
v_exp_f32 v201, v201
v_mfma_f32_16x16x32_bf16 v[208:211], a[68:71], v[164:167], v[208:211]
v_cvt_pk_bf16_f32 v228, v200, v201
v_mfma_f32_16x16x32_bf16 v[208:211], a[72:75], v[168:171], v[208:211]
v_exp_f32 v202, v202
v_mfma_f32_16x16x32_bf16 v[208:211], a[76:79], v[172:175], v[208:211]
buffer_load_dwordx4 v255, s[60:63], s17 offen offset:3072 sc1 lds
v_mfma_f32_16x16x32_bf16 v[216:219], a[64:67], v[176:179], 0
v_exp_f32 v203, v203
v_mfma_f32_16x16x32_bf16 v[216:219], a[68:71], v[180:183], v[216:219]
v_cvt_pk_bf16_f32 v229, v202, v203
v_mfma_f32_16x16x32_bf16 v[216:219], a[72:75], v[184:187], v[216:219]
v_exp_f32 v196, v196
v_mfma_f32_16x16x32_bf16 v[216:219], a[76:79], v[188:191], v[216:219]
buffer_load_dword a253, v254, s[36:39], s90 offen
v_mfma_f32_16x16x32_bf16 v[212:215], a[80:83], v[160:163], 0
v_exp_f32 v197, v197
v_mfma_f32_16x16x32_bf16 v[212:215], a[84:87], v[164:167], v[212:215]
v_cvt_pk_bf16_f32 v226, v196, v197
s_add_u32 s17, s17, 8192
v_mfma_f32_16x16x32_bf16 v[212:215], a[88:91], v[168:171], v[212:215]
v_exp_f32 v198, v198
v_mfma_f32_16x16x32_bf16 v[212:215], a[92:95], v[172:175], v[212:215]
s_add_u32 m0, m0, 16384
s_and_b32 m0, m0, s78
v_mfma_f32_16x16x32_bf16 v[220:223], a[80:83], v[176:179], 0
v_exp_f32 v199, v199
v_mfma_f32_16x16x32_bf16 v[220:223], a[84:87], v[180:183], v[220:223]
v_cvt_pk_bf16_f32 v227, v198, v199
s_add_u32 s90, s90, s21
v_mfma_f32_16x16x32_bf16 v[220:223], a[88:91], v[184:187], v[220:223]
v_exp_f32 v204, v204
v_mfma_f32_16x16x32_bf16 v[220:223], a[92:95], v[188:191], v[220:223]
s_waitcnt vmcnt(11)
s_barrier
v_mfma_f32_16x16x32_bf16 v[64:67], a[96:99], v[232:235], v[64:67]
v_exp_f32 v205, v205
v_mfma_f32_16x16x32_bf16 v[96:99], a[96:99], v[236:239], v[96:99]
ds_read_b128 a[96:99], v248 offset:8192
v_cvt_pk_bf16_f32 v230, v204, v205
v_mfma_f32_16x16x32_bf16 v[68:71], a[100:103], v[232:235], v[68:71]
v_exp_f32 v206, v206
v_mfma_f32_16x16x32_bf16 v[100:103], a[100:103], v[236:239], v[100:103]
ds_read_b128 a[100:103], v248 offset:9216
v_mfma_f32_16x16x32_bf16 v[72:75], a[104:107], v[232:235], v[72:75]
v_exp_f32 v207, v207
v_mfma_f32_16x16x32_bf16 v[104:107], a[104:107], v[236:239], v[104:107]
ds_read_b128 a[104:107], v248 offset:10240
v_cvt_pk_bf16_f32 v231, v206, v207
v_mfma_f32_16x16x32_bf16 v[76:79], a[108:111], v[232:235], v[76:79]
v_exp_f32 v208, v208
v_mfma_f32_16x16x32_bf16 v[108:111], a[108:111], v[236:239], v[108:111]
ds_read_b128 a[108:111], v248 offset:11264
v_mfma_f32_16x16x32_bf16 v[80:83], a[112:115], v[232:235], v[80:83]
v_exp_f32 v209, v209
v_mfma_f32_16x16x32_bf16 v[112:115], a[112:115], v[236:239], v[112:115]
ds_read_b128 a[112:115], v248 offset:12288
.La5_LW2_1:
s_cbranch_execz .La5_LW2_5
v_mfma_f32_16x16x32_bf16 v[84:87], a[116:119], v[232:235], v[84:87]
v_exp_f32 v210, v210
v_mfma_f32_16x16x32_bf16 v[116:119], a[116:119], v[236:239], v[116:119]
ds_read_b128 a[116:119], v248 offset:13312
v_mfma_f32_16x16x32_bf16 v[88:91], a[120:123], v[232:235], v[88:91]
v_exp_f32 v211, v211
v_mfma_f32_16x16x32_bf16 v[120:123], a[120:123], v[236:239], v[120:123]
ds_read_b128 a[120:123], v248 offset:14336
s_bitcmp1_b64 s[98:99], s85
v_mfma_f32_16x16x32_bf16 v[92:95], a[124:127], v[232:235], v[92:95]
v_exp_f32 v216, v216
v_mfma_f32_16x16x32_bf16 v[124:127], a[124:127], v[236:239], v[124:127]
ds_read_b128 a[124:127], v248 offset:15360
v_or_b32 v248, s89, v255
v_smfmac_f32_16x16x64_bf16 a[196:199], a[208:211], v[232:239], v241
s_waitcnt lgkmcnt(4)
v_smfmac_f32_16x16x64_bf16 a[192:195], a[208:211], v[224:231], v241
ds_read_b128 a[64:67], v248 offset:0
v_mfma_f32_16x16x32_bf16 v[0:3], a[96:99], v[224:227], v[0:3]
v_exp_f32 v217, v217
v_mfma_f32_16x16x32_bf16 v[32:35], a[96:99], v[228:231], v[32:35]
ds_read_b128 a[68:71], v248 offset:1024
v_cvt_pk_bf16_f32 v232, v208, v209
v_mfma_f32_16x16x32_bf16 v[4:7], a[100:103], v[224:227], v[4:7]
v_exp_f32 v218, v218
v_mfma_f32_16x16x32_bf16 v[36:39], a[100:103], v[228:231], v[36:39]
ds_read_b128 a[72:75], v248 offset:2048
v_cvt_pk_bf16_f32 v233, v210, v211
v_mfma_f32_16x16x32_bf16 v[8:11], a[104:107], v[224:227], v[8:11]
v_exp_f32 v219, v219
v_mfma_f32_16x16x32_bf16 v[40:43], a[104:107], v[228:231], v[40:43]
ds_read_b128 a[76:79], v248 offset:3072
v_cvt_pk_bf16_f32 v236, v216, v217
v_mfma_f32_16x16x32_bf16 v[12:15], a[108:111], v[224:227], v[12:15]
v_exp_f32 v212, v212
v_mfma_f32_16x16x32_bf16 v[44:47], a[108:111], v[228:231], v[44:47]
ds_read_b128 a[80:83], v248 offset:4096
s_waitcnt lgkmcnt(5)
v_mfma_f32_16x16x32_bf16 v[16:19], a[112:115], v[224:227], v[16:19]
v_exp_f32 v213, v213
v_mfma_f32_16x16x32_bf16 v[48:51], a[112:115], v[228:231], v[48:51]
ds_read_b128 a[84:87], v248 offset:5120
v_cvt_pk_bf16_f32 v237, v218, v219
v_mfma_f32_16x16x32_bf16 v[20:23], a[116:119], v[224:227], v[20:23]
v_exp_f32 v214, v214
v_mfma_f32_16x16x32_bf16 v[52:55], a[116:119], v[228:231], v[52:55]
ds_read_b128 a[88:91], v248 offset:6144
v_cvt_pk_bf16_f32 v234, v212, v213
v_mfma_f32_16x16x32_bf16 v[24:27], a[120:123], v[224:227], v[24:27]
v_exp_f32 v215, v215
v_mfma_f32_16x16x32_bf16 v[56:59], a[120:123], v[228:231], v[56:59]
ds_read_b128 a[92:95], v248 offset:7168
v_cvt_pk_bf16_f32 v235, v214, v215
v_mfma_f32_16x16x32_bf16 v[28:31], a[124:127], v[224:227], v[28:31]
v_exp_f32 v220, v220
v_mfma_f32_16x16x32_bf16 v[60:63], a[124:127], v[228:231], v[60:63]
s_cbranch_scc1 .La5_LA_F1E2
s_waitcnt lgkmcnt(4)
v_mfma_f32_16x16x32_bf16 v[192:195], a[64:67], v[128:131], 0
v_exp_f32 v221, v221
v_mfma_f32_16x16x32_bf16 v[192:195], a[68:71], v[132:135], v[192:195]
v_cvt_pk_bf16_f32 v238, v220, v221
s_add_u32 s85, s85, 1
v_mfma_f32_16x16x32_bf16 v[192:195], a[72:75], v[136:139], v[192:195]
v_exp_f32 v222, v222
v_mfma_f32_16x16x32_bf16 v[192:195], a[76:79], v[140:143], v[192:195]
buffer_load_dwordx4 v255, s[60:63], s17 offen offset:0 sc1 lds
v_mfma_f32_16x16x32_bf16 v[200:203], a[64:67], v[144:147], 0
v_exp_f32 v223, v223
v_mfma_f32_16x16x32_bf16 v[200:203], a[68:71], v[148:151], v[200:203]
v_cvt_pk_bf16_f32 v239, v222, v223
s_add_u32 s89, s89, 16384
v_mfma_f32_16x16x32_bf16 v[200:203], a[72:75], v[152:155], v[200:203]
v_exp_f32 v192, v192
v_mfma_f32_16x16x32_bf16 v[200:203], a[76:79], v[156:159], v[200:203]
s_waitcnt lgkmcnt(0)
s_and_b32 s89, s89, 0x1ffff
v_mfma_f32_16x16x32_bf16 v[196:199], a[80:83], v[128:131], 0
v_exp_f32 v193, v193
v_mfma_f32_16x16x32_bf16 v[196:199], a[84:87], v[132:135], v[196:199]
v_cvt_pk_bf16_f32 v224, v192, v193
v_mfma_f32_16x16x32_bf16 v[196:199], a[88:91], v[136:139], v[196:199]
v_exp_f32 v194, v194
v_mfma_f32_16x16x32_bf16 v[196:199], a[92:95], v[140:143], v[196:199]
buffer_load_dwordx4 v255, s[60:63], s17 offen offset:1024 sc1 lds
v_mfma_f32_16x16x32_bf16 v[204:207], a[80:83], v[144:147], 0
v_exp_f32 v195, v195
v_mfma_f32_16x16x32_bf16 v[204:207], a[84:87], v[148:151], v[204:207]
v_cvt_pk_bf16_f32 v225, v194, v195
v_mfma_f32_16x16x32_bf16 v[204:207], a[88:91], v[152:155], v[204:207]
v_exp_f32 v200, v200
v_mfma_f32_16x16x32_bf16 v[204:207], a[92:95], v[156:159], v[204:207]
buffer_load_dwordx4 v255, s[60:63], s17 offen offset:2048 sc1 lds
v_mfma_f32_16x16x32_bf16 v[208:211], a[64:67], v[160:163], 0
v_exp_f32 v201, v201
v_mfma_f32_16x16x32_bf16 v[208:211], a[68:71], v[164:167], v[208:211]
v_cvt_pk_bf16_f32 v228, v200, v201
v_mfma_f32_16x16x32_bf16 v[208:211], a[72:75], v[168:171], v[208:211]
v_exp_f32 v202, v202
v_mfma_f32_16x16x32_bf16 v[208:211], a[76:79], v[172:175], v[208:211]
buffer_load_dwordx4 v255, s[60:63], s17 offen offset:3072 sc1 lds
v_mfma_f32_16x16x32_bf16 v[216:219], a[64:67], v[176:179], 0
v_exp_f32 v203, v203
v_mfma_f32_16x16x32_bf16 v[216:219], a[68:71], v[180:183], v[216:219]
v_cvt_pk_bf16_f32 v229, v202, v203
v_mfma_f32_16x16x32_bf16 v[216:219], a[72:75], v[184:187], v[216:219]
v_exp_f32 v196, v196
v_mfma_f32_16x16x32_bf16 v[216:219], a[76:79], v[188:191], v[216:219]
buffer_load_dword a253, v254, s[36:39], s90 offen
v_mfma_f32_16x16x32_bf16 v[212:215], a[80:83], v[160:163], 0
v_exp_f32 v197, v197
v_mfma_f32_16x16x32_bf16 v[212:215], a[84:87], v[164:167], v[212:215]
v_cvt_pk_bf16_f32 v226, v196, v197
s_add_u32 s17, s17, 8192
v_mfma_f32_16x16x32_bf16 v[212:215], a[88:91], v[168:171], v[212:215]
v_exp_f32 v198, v198
v_mfma_f32_16x16x32_bf16 v[212:215], a[92:95], v[172:175], v[212:215]
s_add_u32 m0, m0, 16384
s_and_b32 m0, m0, s78
v_mfma_f32_16x16x32_bf16 v[220:223], a[80:83], v[176:179], 0
v_exp_f32 v199, v199
v_mfma_f32_16x16x32_bf16 v[220:223], a[84:87], v[180:183], v[220:223]
v_cvt_pk_bf16_f32 v227, v198, v199
s_add_u32 s90, s90, s21
v_mfma_f32_16x16x32_bf16 v[220:223], a[88:91], v[184:187], v[220:223]
v_exp_f32 v204, v204
v_mfma_f32_16x16x32_bf16 v[220:223], a[92:95], v[188:191], v[220:223]
s_waitcnt vmcnt(11)
s_barrier
v_mfma_f32_16x16x32_bf16 v[64:67], a[96:99], v[232:235], v[64:67]
v_exp_f32 v205, v205
v_mfma_f32_16x16x32_bf16 v[96:99], a[96:99], v[236:239], v[96:99]
ds_read_b128 a[96:99], v248 offset:8192
v_cvt_pk_bf16_f32 v230, v204, v205
v_mfma_f32_16x16x32_bf16 v[68:71], a[100:103], v[232:235], v[68:71]
v_exp_f32 v206, v206
v_mfma_f32_16x16x32_bf16 v[100:103], a[100:103], v[236:239], v[100:103]
ds_read_b128 a[100:103], v248 offset:9216
v_mfma_f32_16x16x32_bf16 v[72:75], a[104:107], v[232:235], v[72:75]
v_exp_f32 v207, v207
v_mfma_f32_16x16x32_bf16 v[104:107], a[104:107], v[236:239], v[104:107]
ds_read_b128 a[104:107], v248 offset:10240
v_cvt_pk_bf16_f32 v231, v206, v207
v_mfma_f32_16x16x32_bf16 v[76:79], a[108:111], v[232:235], v[76:79]
v_exp_f32 v208, v208
v_mfma_f32_16x16x32_bf16 v[108:111], a[108:111], v[236:239], v[108:111]
ds_read_b128 a[108:111], v248 offset:11264
v_mfma_f32_16x16x32_bf16 v[80:83], a[112:115], v[232:235], v[80:83]
v_exp_f32 v209, v209
v_mfma_f32_16x16x32_bf16 v[112:115], a[112:115], v[236:239], v[112:115]
ds_read_b128 a[112:115], v248 offset:12288
v_mfma_f32_16x16x32_bf16 v[84:87], a[116:119], v[232:235], v[84:87]
v_exp_f32 v210, v210
v_mfma_f32_16x16x32_bf16 v[116:119], a[116:119], v[236:239], v[116:119]
ds_read_b128 a[116:119], v248 offset:13312
v_mfma_f32_16x16x32_bf16 v[88:91], a[120:123], v[232:235], v[88:91]
v_exp_f32 v211, v211
v_mfma_f32_16x16x32_bf16 v[120:123], a[120:123], v[236:239], v[120:123]
ds_read_b128 a[120:123], v248 offset:14336
s_bitcmp1_b64 s[98:99], s85
v_mfma_f32_16x16x32_bf16 v[92:95], a[124:127], v[232:235], v[92:95]
v_exp_f32 v216, v216
v_mfma_f32_16x16x32_bf16 v[124:127], a[124:127], v[236:239], v[124:127]
ds_read_b128 a[124:127], v248 offset:15360
v_or_b32 v248, s89, v255
v_smfmac_f32_16x16x64_bf16 a[196:199], a[208:211], v[232:239], v241
s_waitcnt lgkmcnt(4)
v_smfmac_f32_16x16x64_bf16 a[192:195], a[208:211], v[224:231], v241
ds_read_b128 a[64:67], v248 offset:0
v_mfma_f32_16x16x32_bf16 v[0:3], a[96:99], v[224:227], v[0:3]
v_exp_f32 v217, v217
v_mfma_f32_16x16x32_bf16 v[32:35], a[96:99], v[228:231], v[32:35]
ds_read_b128 a[68:71], v248 offset:1024
v_cvt_pk_bf16_f32 v232, v208, v209
v_mfma_f32_16x16x32_bf16 v[4:7], a[100:103], v[224:227], v[4:7]
v_exp_f32 v218, v218
v_mfma_f32_16x16x32_bf16 v[36:39], a[100:103], v[228:231], v[36:39]
ds_read_b128 a[72:75], v248 offset:2048
v_cvt_pk_bf16_f32 v233, v210, v211
v_mfma_f32_16x16x32_bf16 v[8:11], a[104:107], v[224:227], v[8:11]
v_exp_f32 v219, v219
v_mfma_f32_16x16x32_bf16 v[40:43], a[104:107], v[228:231], v[40:43]
ds_read_b128 a[76:79], v248 offset:3072
v_cvt_pk_bf16_f32 v236, v216, v217
v_mfma_f32_16x16x32_bf16 v[12:15], a[108:111], v[224:227], v[12:15]
v_exp_f32 v212, v212
v_mfma_f32_16x16x32_bf16 v[44:47], a[108:111], v[228:231], v[44:47]
ds_read_b128 a[80:83], v248 offset:4096
s_waitcnt lgkmcnt(5)
v_mfma_f32_16x16x32_bf16 v[16:19], a[112:115], v[224:227], v[16:19]
v_exp_f32 v213, v213
v_mfma_f32_16x16x32_bf16 v[48:51], a[112:115], v[228:231], v[48:51]
ds_read_b128 a[84:87], v248 offset:5120
v_cvt_pk_bf16_f32 v237, v218, v219
v_mfma_f32_16x16x32_bf16 v[20:23], a[116:119], v[224:227], v[20:23]
v_exp_f32 v214, v214
v_mfma_f32_16x16x32_bf16 v[52:55], a[116:119], v[228:231], v[52:55]
ds_read_b128 a[88:91], v248 offset:6144
v_cvt_pk_bf16_f32 v234, v212, v213
v_mfma_f32_16x16x32_bf16 v[24:27], a[120:123], v[224:227], v[24:27]
v_exp_f32 v215, v215
v_mfma_f32_16x16x32_bf16 v[56:59], a[120:123], v[228:231], v[56:59]
ds_read_b128 a[92:95], v248 offset:7168
v_cvt_pk_bf16_f32 v235, v214, v215
v_mfma_f32_16x16x32_bf16 v[28:31], a[124:127], v[224:227], v[28:31]
v_exp_f32 v220, v220
v_mfma_f32_16x16x32_bf16 v[60:63], a[124:127], v[228:231], v[60:63]
.La5_LA_F1E:
s_cbranch_scc0 .La5_LA_F1
.La5_LA_F1E2:
s_cmp_eq_u32 s85, s82
s_nop 0
s_nop 0
s_cbranch_scc1 .La5_LA_F1X
s_call_b64 s[24:25], .La5_LA_EVENT
s_branch .La5_LA_F1
.La5_LA_FE0:
.La5_LW2_2:
s_cbranch_execz .La5_LW2_6
s_cmp_eq_u32 s85, s82
s_cbranch_scc1 .La5_LA_F1X
s_nop 0
s_nop 0
s_call_b64 s[24:25], .La5_LA_EVENT
s_branch .La5_LA_FS0
; ---- hand-offs between windows
.La5_LA_EVENT:
.La5_LW2_3:
s_cbranch_execz .La5_LW2_7
s_cmp_lg_u32 s85, 2
s_cbranch_scc1 .La5_LA_E1
s_waitcnt vmcnt(1)
v_cmp_eq_u32 vcc, v250, v249
s_mov_b32 s17, s16
s_mov_b32 s78, 0x1ffff
s_lshl_b32 m0, s3, 12
s_cmp_eq_u64 vcc, -1
s_cbranch_scc0 .La5_LA_KSPIN
.La5_LA_KOK:
buffer_load_dwordx4 v255, s[60:63], s17 offen offset:0 sc1 lds
buffer_load_dwordx4 v255, s[60:63], s17 offen offset:1024 sc1 lds
buffer_load_dwordx4 v255, s[60:63], s17 offen offset:2048 sc1 lds
buffer_load_dwordx4 v255, s[60:63], s17 offen offset:3072 sc1 lds
s_add_u32 s17, s17, 8192
s_add_u32 m0, m0, 16384
s_mov_b32 s20, s19
s_cmp_lg_u32 s3, 0
s_cbranch_scc1 .La5_LA_POLL
s_nop 0
buffer_load_dwordx4 v255, s[60:63], s17 offen offset:0 sc1 lds
buffer_load_dwordx4 v255, s[60:63], s17 offen offset:1024 sc1 lds
buffer_load_dwordx4 v255, s[60:63], s17 offen offset:2048 sc1 lds
buffer_load_dwordx4 v255, s[60:63], s17 offen offset:3072 sc1 lds
s_add_u32 s17, s17, 8192
s_add_u32 m0, m0, 16384
.La5_LA_POLL:
buffer_load_dword v250, v251, s[64:67], s20 offen sc1
s_setpc_b64 s[24:25]
.La5_LA_E1:
s_lshr_b32 s92, s85, 1
s_lshl3_add_u32 s92, s92, s73
s_sub_u32 s92, s92, 64
s_cmp_lt_u32 s92, 5
s_cbranch_scc1 .La5_LA_LOK
s_cmp_lt_u32 s85, s79
s_cselect_b32 s17, 0, s17
s_cbranch_scc1 .La5_LA_POLL
s_cmp_lg_u32 s85, s79
s_cbranch_scc1 .La5_LA_E2
s_waitcnt vmcnt(1)
v_cmp_eq_u32 s[92:93], v250, v249
s_nop 1
s_and_b32 s92, s92, 3
s_cmp_eq_u32 s92, 3
s_cbranch_scc0 .La5_LA_QSPIN
.La5_LA_QOK:
s_mov_b32 s50, 16384
buffer_load_dwordx4 a[0:3], v255, s[48:51], 0 offen offset:0 sc1
buffer_load_dwordx4 a[4:7], v255, s[48:51], 0 offen offset:1024 sc1
buffer_load_dwordx4 a[8:11], v255, s[48:51], 0 offen offset:2048 sc1
buffer_load_dwordx4 a[12:15], v255, s[48:51], 0 offen offset:3072 sc1
buffer_load_dwordx4 a[16:19], v255, s[48:51], s95 offen offset:0 sc1
buffer_load_dwordx4 a[20:23], v255, s[48:51], s95 offen offset:1024 sc1
buffer_load_dwordx4 a[24:27], v255, s[48:51], s95 offen offset:2048 sc1
buffer_load_dwordx4 a[28:31], v255, s[48:51], s95 offen offset:3072 sc1
buffer_load_dwordx4 a[32:35], v255, s[48:51], s96 offen offset:0 sc1
buffer_load_dwordx4 a[36:39], v255, s[48:51], s96 offen offset:1024 sc1
buffer_load_dwordx4 a[40:43], v255, s[48:51], s96 offen offset:2048 sc1
buffer_load_dwordx4 a[44:47], v255, s[48:51], s96 offen offset:3072 sc1
buffer_load_dwordx4 a[48:51], v255, s[48:51], s97 offen offset:0 sc1
buffer_load_dwordx4 a[52:55], v255, s[48:51], s97 offen offset:1024 sc1
buffer_load_dwordx4 a[56:59], v255, s[48:51], s97 offen offset:2048 sc1
buffer_load_dwordx4 a[60:63], v255, s[48:51], s97 offen offset:3072 sc1
s_setpc_b64 s[24:25]
.La5_LA_E2:
v_accvgpr_read_b32 v242, a231
s_waitcnt vmcnt(5)
buffer_store_dwordx2 v[252:253], v242, s[64:67], s6 offen offset:0 sc1
s_setpc_b64 s[24:25]
.La5_LA_EL:
s_andn2_b32 s92, s87, 255
s_bitcmp1_b32 s85, 0
s_cbranch_scc1 .La5_LA_EL1
buffer_load_dword v250, v251, s[64:67], s92 offen sc1
s_setpc_b64 s[24:25]
.La5_LA_EL1:
s_nop 0
v_cmp_eq_u32 vcc, v250, v249
s_nop 1
s_or_b32 vcc_hi, vcc_hi, 0xff000000
s_cmp_eq_u32 vcc_hi, -1
s_cbranch_scc0 .La5_LA_LSPIN
.La5_LA_LOK:
s_cmp_eq_u32 s75, 1
s_cselect_b32 s17, 393216, s17
s_setpc_b64 s[24:25]
; ---- helper: own block finished: l is checked (.La5_LA_SLOW if outside [2^-60, 2^60] or NaN), O / l leaves; the partner-s rows start
.La5_LA_F1X:
s_cmp_eq_u32 s22, 0
s_cbranch_scc1 .La5_LA_DC
s_waitcnt lgkmcnt(4)
s_waitcnt vmcnt(5)
v_mfma_f32_16x16x32_bf16 v[192:195], a[64:67], a[0:3], 0
v_exp_f32 v221, v221
v_mfma_f32_16x16x32_bf16 v[192:195], a[68:71], a[4:7], v[192:195]
v_cvt_pk_bf16_f32 v238, v220, v221
s_add_u32 s89, s89, 16384
v_mfma_f32_16x16x32_bf16 v[192:195], a[72:75], a[8:11], v[192:195]
v_exp_f32 v222, v222
v_mfma_f32_16x16x32_bf16 v[192:195], a[76:79], a[12:15], v[192:195]
buffer_load_dwordx4 v255, s[60:63], s17 offen offset:0 sc1 lds
v_accvgpr_read_b32 v242, a192
v_accvgpr_read_b32 v240, a193
v_add_f32 v242, v242, v240
v_mfma_f32_16x16x32_bf16 v[200:203], a[64:67], a[16:19], 0
v_exp_f32 v223, v223
v_accvgpr_read_b32 v243, a194
v_accvgpr_read_b32 v240, a195
v_add_f32 v243, v243, v240
v_mfma_f32_16x16x32_bf16 v[200:203], a[68:71], a[20:23], v[200:203]
v_cvt_pk_bf16_f32 v239, v222, v223
s_and_b32 s89, s89, 0x1ffff
v_accvgpr_write_b32 a192, 0
v_accvgpr_write_b32 a193, 0
v_accvgpr_write_b32 a194, 0
v_mfma_f32_16x16x32_bf16 v[200:203], a[72:75], a[24:27], v[200:203]
v_exp_f32 v192, v192
v_accvgpr_write_b32 a195, 0
v_subrev_u32 v240, 562036736, v242
v_mfma_f32_16x16x32_bf16 v[200:203], a[76:79], a[28:31], v[200:203]
s_waitcnt lgkmcnt(0)
v_rcp_f32 v242, v242
v_cmp_le_u32 vcc, 1006632960, v240
v_subrev_u32 v240, 562036736, v243
v_mfma_f32_16x16x32_bf16 v[196:199], a[80:83], a[0:3], 0
v_exp_f32 v193, v193
s_mov_b64 s[92:93], vcc
v_cmp_le_u32 vcc, 1006632960, v240
v_mfma_f32_16x16x32_bf16 v[196:199], a[84:87], a[4:7], v[196:199]
v_cvt_pk_bf16_f32 v224, v192, v193
v_rcp_f32 v243, v243
v_mfma_f32_16x16x32_bf16 v[196:199], a[88:91], a[8:11], v[196:199]
v_exp_f32 v194, v194
s_or_b64 vcc, vcc, s[92:93]
s_cbranch_scc1 .La5_LA_SC1
.La5_LA_SR1:
v_mul_f32 v0, v0, v242
v_mfma_f32_16x16x32_bf16 v[196:199], a[92:95], a[12:15], v[196:199]
buffer_load_dwordx4 v255, s[60:63], s17 offen offset:1024 sc1 lds
v_mul_f32 v1, v1, v242
v_mul_f32 v2, v2, v242
v_mul_f32 v3, v3, v242
v_mfma_f32_16x16x32_bf16 v[204:207], a[80:83], a[16:19], 0
v_exp_f32 v195, v195
v_mul_f32 v4, v4, v242
v_mul_f32 v5, v5, v242
v_mul_f32 v6, v6, v242
v_mfma_f32_16x16x32_bf16 v[204:207], a[84:87], a[20:23], v[204:207]
v_cvt_pk_bf16_f32 v225, v194, v195
v_mul_f32 v7, v7, v242
v_cvt_pk_bf16_f32 v128, v0, v1
v_cvt_pk_bf16_f32 v129, v2, v3
v_cvt_pk_bf16_f32 v130, v4, v5
v_mfma_f32_16x16x32_bf16 v[204:207], a[88:91], a[24:27], v[204:207]
v_exp_f32 v200, v200
v_cvt_pk_bf16_f32 v131, v6, v7
v_mul_f32 v8, v8, v242
v_mul_f32 v9, v9, v242
v_mfma_f32_16x16x32_bf16 v[204:207], a[92:95], a[28:31], v[204:207]
buffer_load_dwordx4 v255, s[60:63], s17 offen offset:2048 sc1 lds
v_mul_f32 v10, v10, v242
v_mul_f32 v11, v11, v242
v_mul_f32 v12, v12, v242
v_mfma_f32_16x16x32_bf16 v[208:211], a[64:67], a[32:35], 0
v_exp_f32 v201, v201
v_mul_f32 v13, v13, v242
v_mul_f32 v14, v14, v242
v_mul_f32 v15, v15, v242
v_mfma_f32_16x16x32_bf16 v[208:211], a[68:71], a[36:39], v[208:211]
v_cvt_pk_bf16_f32 v228, v200, v201
v_cvt_pk_bf16_f32 v132, v8, v9
v_cvt_pk_bf16_f32 v133, v10, v11
v_cvt_pk_bf16_f32 v134, v12, v13
v_cvt_pk_bf16_f32 v135, v14, v15
v_mfma_f32_16x16x32_bf16 v[208:211], a[72:75], a[40:43], v[208:211]
v_exp_f32 v202, v202
v_mul_f32 v16, v16, v242
v_mul_f32 v17, v17, v242
v_mul_f32 v18, v18, v242
v_mfma_f32_16x16x32_bf16 v[208:211], a[76:79], a[44:47], v[208:211]
buffer_load_dwordx4 v255, s[60:63], s17 offen offset:3072 sc1 lds
v_mul_f32 v19, v19, v242
v_mul_f32 v20, v20, v242
v_mul_f32 v21, v21, v242
v_mfma_f32_16x16x32_bf16 v[216:219], a[64:67], a[48:51], 0
v_exp_f32 v203, v203
v_mul_f32 v22, v22, v242
v_mul_f32 v23, v23, v242
v_cvt_pk_bf16_f32 v136, v16, v17
v_mfma_f32_16x16x32_bf16 v[216:219], a[68:71], a[52:55], v[216:219]
v_cvt_pk_bf16_f32 v229, v202, v203
v_cvt_pk_bf16_f32 v137, v18, v19
v_cvt_pk_bf16_f32 v138, v20, v21
v_cvt_pk_bf16_f32 v139, v22, v23
v_mul_f32 v24, v24, v242
v_mfma_f32_16x16x32_bf16 v[216:219], a[72:75], a[56:59], v[216:219]
v_exp_f32 v196, v196
v_mul_f32 v25, v25, v242
v_mul_f32 v26, v26, v242
v_mul_f32 v27, v27, v242
v_mfma_f32_16x16x32_bf16 v[216:219], a[76:79], a[60:63], v[216:219]
buffer_load_dword a253, v254, s[36:39], s90 offen
s_add_u32 s90, s90, s21
v_mul_f32 v28, v28, v242
v_mul_f32 v29, v29, v242
v_mfma_f32_16x16x32_bf16 v[212:215], a[80:83], a[32:35], 0
v_exp_f32 v197, v197
s_add_u32 s17, s17, 8192
v_mul_f32 v30, v30, v242
v_mul_f32 v31, v31, v242
v_mfma_f32_16x16x32_bf16 v[212:215], a[84:87], a[36:39], v[212:215]
v_cvt_pk_bf16_f32 v226, v196, v197
s_add_u32 m0, m0, 16384
s_and_b32 m0, m0, s78
v_cvt_pk_bf16_f32 v140, v24, v25
v_cvt_pk_bf16_f32 v141, v26, v27
v_mfma_f32_16x16x32_bf16 v[212:215], a[88:91], a[40:43], v[212:215]
v_exp_f32 v198, v198
v_cvt_pk_bf16_f32 v142, v28, v29
v_cvt_pk_bf16_f32 v143, v30, v31
v_mul_f32 v32, v32, v243
v_mfma_f32_16x16x32_bf16 v[212:215], a[92:95], a[44:47], v[212:215]
buffer_store_dwordx4 v[128:131], v244, s[44:47], s91 offen offset:0 sc1
v_mul_f32 v33, v33, v243
v_mul_f32 v34, v34, v243
v_mul_f32 v35, v35, v243
v_mfma_f32_16x16x32_bf16 v[220:223], a[80:83], a[48:51], 0
v_exp_f32 v199, v199
v_mul_f32 v36, v36, v243
v_mul_f32 v37, v37, v243
v_mul_f32 v38, v38, v243
v_mfma_f32_16x16x32_bf16 v[220:223], a[84:87], a[52:55], v[220:223]
v_cvt_pk_bf16_f32 v227, v198, v199
v_mul_f32 v39, v39, v243
v_cvt_pk_bf16_f32 v144, v32, v33
v_cvt_pk_bf16_f32 v145, v34, v35
v_cvt_pk_bf16_f32 v146, v36, v37
v_mfma_f32_16x16x32_bf16 v[220:223], a[88:91], a[56:59], v[220:223]
v_exp_f32 v204, v204
v_cvt_pk_bf16_f32 v147, v38, v39
v_mul_f32 v40, v40, v243
v_mul_f32 v41, v41, v243
v_mfma_f32_16x16x32_bf16 v[220:223], a[92:95], a[60:63], v[220:223]
s_waitcnt vmcnt(12)
s_barrier
v_mul_f32 v42, v42, v243
v_mul_f32 v43, v43, v243
v_mul_f32 v44, v44, v243
v_mfma_f32_16x16x32_bf16 v[64:67], a[96:99], v[232:235], v[64:67]
v_exp_f32 v205, v205
v_mul_f32 v45, v45, v243
v_mul_f32 v46, v46, v243
v_mul_f32 v47, v47, v243
v_mfma_f32_16x16x32_bf16 v[96:99], a[96:99], v[236:239], v[96:99]
ds_read_b128 a[96:99], v248 offset:8192
v_cvt_pk_bf16_f32 v230, v204, v205
v_cvt_pk_bf16_f32 v148, v40, v41
v_cvt_pk_bf16_f32 v149, v42, v43
v_cvt_pk_bf16_f32 v150, v44, v45
v_mfma_f32_16x16x32_bf16 v[68:71], a[100:103], v[232:235], v[68:71]
v_exp_f32 v206, v206
v_cvt_pk_bf16_f32 v151, v46, v47
v_mul_f32 v48, v48, v243
v_mul_f32 v49, v49, v243
v_mfma_f32_16x16x32_bf16 v[100:103], a[100:103], v[236:239], v[100:103]
ds_read_b128 a[100:103], v248 offset:9216
buffer_store_dwordx4 v[132:135], v244, s[44:47], s91 offen offset:1024 sc1
v_mul_f32 v50, v50, v243
v_mul_f32 v51, v51, v243
v_mfma_f32_16x16x32_bf16 v[72:75], a[104:107], v[232:235], v[72:75]
v_exp_f32 v207, v207
v_mul_f32 v52, v52, v243
v_mul_f32 v53, v53, v243
v_mul_f32 v54, v54, v243
v_mfma_f32_16x16x32_bf16 v[104:107], a[104:107], v[236:239], v[104:107]
ds_read_b128 a[104:107], v248 offset:10240
v_cvt_pk_bf16_f32 v231, v206, v207
v_mul_f32 v55, v55, v243
v_cvt_pk_bf16_f32 v152, v48, v49
v_cvt_pk_bf16_f32 v153, v50, v51
v_mfma_f32_16x16x32_bf16 v[76:79], a[108:111], v[232:235], v[76:79]
v_exp_f32 v208, v208
v_cvt_pk_bf16_f32 v154, v52, v53
v_cvt_pk_bf16_f32 v155, v54, v55
v_mul_f32 v56, v56, v243
v_mfma_f32_16x16x32_bf16 v[108:111], a[108:111], v[236:239], v[108:111]
ds_read_b128 a[108:111], v248 offset:11264
v_mul_f32 v57, v57, v243
v_mul_f32 v58, v58, v243
v_mul_f32 v59, v59, v243
v_mul_f32 v60, v60, v243
v_mfma_f32_16x16x32_bf16 v[80:83], a[112:115], v[232:235], v[80:83]
v_exp_f32 v209, v209
v_mul_f32 v61, v61, v243
v_mul_f32 v62, v62, v243
v_mul_f32 v63, v63, v243
v_mfma_f32_16x16x32_bf16 v[112:115], a[112:115], v[236:239], v[112:115]
ds_read_b128 a[112:115], v248 offset:12288
buffer_store_dwordx4 v[136:139], v244, s[44:47], s91 offen offset:2048 sc1
v_cvt_pk_bf16_f32 v156, v56, v57
v_cvt_pk_bf16_f32 v157, v58, v59
v_mfma_f32_16x16x32_bf16 v[84:87], a[116:119], v[232:235], v[84:87]
v_exp_f32 v210, v210
v_cvt_pk_bf16_f32 v158, v60, v61
v_cvt_pk_bf16_f32 v159, v62, v63
v_mfma_f32_16x16x32_bf16 v[116:119], a[116:119], v[236:239], v[116:119]
ds_read_b128 a[116:119], v248 offset:13312
v_mfma_f32_16x16x32_bf16 v[88:91], a[120:123], v[232:235], v[88:91]
v_exp_f32 v211, v211
v_mfma_f32_16x16x32_bf16 v[120:123], a[120:123], v[236:239], v[120:123]
ds_read_b128 a[120:123], v248 offset:14336
buffer_store_dwordx4 v[140:143], v244, s[44:47], s91 offen offset:3072 sc1
v_mfma_f32_16x16x32_bf16 v[92:95], a[124:127], v[232:235], v[92:95]
v_exp_f32 v216, v216
v_mfma_f32_16x16x32_bf16 v[124:127], a[124:127], v[236:239], v[124:127]
ds_read_b128 a[124:127], v248 offset:15360
v_or_b32 v248, s89, v255
v_smfmac_f32_16x16x64_bf16 a[196:199], a[208:211], v[232:239], v241
s_waitcnt lgkmcnt(4)
v_smfmac_f32_16x16x64_bf16 a[192:195], a[208:211], v[224:231], v241
ds_read_b128 a[64:67], v248 offset:0
v_mfma_f32_16x16x32_bf16 v[0:3], a[96:99], v[224:227], 0
v_exp_f32 v217, v217
v_mfma_f32_16x16x32_bf16 v[32:35], a[96:99], v[228:231], 0
ds_read_b128 a[68:71], v248 offset:1024
v_cvt_pk_bf16_f32 v232, v208, v209
v_accvgpr_read_b32 v242, a196
v_accvgpr_read_b32 v240, a197
v_add_f32 v242, v242, v240
v_mfma_f32_16x16x32_bf16 v[4:7], a[100:103], v[224:227], 0
v_exp_f32 v218, v218
v_accvgpr_read_b32 v243, a198
v_accvgpr_read_b32 v240, a199
v_add_f32 v243, v243, v240
v_mfma_f32_16x16x32_bf16 v[36:39], a[100:103], v[228:231], 0
ds_read_b128 a[72:75], v248 offset:2048
v_cvt_pk_bf16_f32 v233, v210, v211
buffer_store_dwordx4 v[144:147], v245, s[44:47], s91 offen offset:0 sc1
v_accvgpr_write_b32 a196, 0
v_mfma_f32_16x16x32_bf16 v[8:11], a[104:107], v[224:227], 0
v_exp_f32 v219, v219
v_accvgpr_write_b32 a197, 0
v_accvgpr_write_b32 a198, 0
v_accvgpr_write_b32 a199, 0
v_mfma_f32_16x16x32_bf16 v[40:43], a[104:107], v[228:231], 0
ds_read_b128 a[76:79], v248 offset:3072
v_cvt_pk_bf16_f32 v236, v216, v217
v_subrev_u32 v240, 562036736, v242
v_rcp_f32 v242, v242
v_mfma_f32_16x16x32_bf16 v[12:15], a[108:111], v[224:227], 0
v_exp_f32 v212, v212
v_cmp_le_u32 vcc, 1006632960, v240
v_subrev_u32 v240, 562036736, v243
s_mov_b64 s[92:93], vcc
v_mfma_f32_16x16x32_bf16 v[44:47], a[108:111], v[228:231], 0
ds_read_b128 a[80:83], v248 offset:4096
s_waitcnt lgkmcnt(5)
v_cmp_le_u32 vcc, 1006632960, v240
v_rcp_f32 v243, v243
v_mfma_f32_16x16x32_bf16 v[16:19], a[112:115], v[224:227], 0
v_exp_f32 v213, v213
s_or_b64 vcc, vcc, s[92:93]
s_cbranch_scc1 .La5_LA_SC2
.La5_LA_SR2:
v_mul_f32 v64, v64, v242
v_mfma_f32_16x16x32_bf16 v[48:51], a[112:115], v[228:231], 0
ds_read_b128 a[84:87], v248 offset:5120
v_cvt_pk_bf16_f32 v237, v218, v219
v_mul_f32 v65, v65, v242
v_mul_f32 v66, v66, v242
v_mul_f32 v67, v67, v242
v_mfma_f32_16x16x32_bf16 v[20:23], a[116:119], v[224:227], 0
v_exp_f32 v214, v214
v_mul_f32 v68, v68, v242
v_mul_f32 v69, v69, v242
v_mul_f32 v70, v70, v242
v_mfma_f32_16x16x32_bf16 v[52:55], a[116:119], v[228:231], 0
ds_read_b128 a[88:91], v248 offset:6144
v_cvt_pk_bf16_f32 v234, v212, v213
buffer_store_dwordx4 v[148:151], v245, s[44:47], s91 offen offset:1024 sc1
v_mul_f32 v71, v71, v242
v_mfma_f32_16x16x32_bf16 v[24:27], a[120:123], v[224:227], 0
v_exp_f32 v215, v215
v_cvt_pk_bf16_f32 v160, v64, v65
v_cvt_pk_bf16_f32 v161, v66, v67
v_cvt_pk_bf16_f32 v162, v68, v69
v_mfma_f32_16x16x32_bf16 v[56:59], a[120:123], v[228:231], 0
ds_read_b128 a[92:95], v248 offset:7168
v_cvt_pk_bf16_f32 v235, v214, v215
v_cvt_pk_bf16_f32 v163, v70, v71
v_mul_f32 v72, v72, v242
v_mul_f32 v73, v73, v242
v_mfma_f32_16x16x32_bf16 v[28:31], a[124:127], v[224:227], 0
v_exp_f32 v220, v220
v_mul_f32 v74, v74, v242
v_mul_f32 v75, v75, v242
v_mul_f32 v76, v76, v242
v_mfma_f32_16x16x32_bf16 v[60:63], a[124:127], v[228:231], 0
s_add_u32 s85, s85, 1
v_mul_f32 v77, v77, v242
v_mul_f32 v78, v78, v242
v_mul_f32 v79, v79, v242
v_cvt_pk_bf16_f32 v164, v72, v73
s_waitcnt lgkmcnt(4)
v_mfma_f32_16x16x32_bf16 v[192:195], a[64:67], a[0:3], 0
v_exp_f32 v221, v221
v_cvt_pk_bf16_f32 v165, v74, v75
v_cvt_pk_bf16_f32 v166, v76, v77
v_cvt_pk_bf16_f32 v167, v78, v79
v_mfma_f32_16x16x32_bf16 v[192:195], a[68:71], a[4:7], v[192:195]
v_cvt_pk_bf16_f32 v238, v220, v221
s_add_u32 s89, s89, 16384
v_mul_f32 v80, v80, v242
v_mul_f32 v81, v81, v242
v_mul_f32 v82, v82, v242
v_mfma_f32_16x16x32_bf16 v[192:195], a[72:75], a[8:11], v[192:195]
v_exp_f32 v222, v222
v_mul_f32 v83, v83, v242
v_mul_f32 v84, v84, v242
v_mul_f32 v85, v85, v242
v_mfma_f32_16x16x32_bf16 v[192:195], a[76:79], a[12:15], v[192:195]
buffer_load_dwordx4 v255, s[60:63], s17 offen offset:0 sc1 lds
v_mul_f32 v86, v86, v242
v_mul_f32 v87, v87, v242
v_cvt_pk_bf16_f32 v168, v80, v81
v_mfma_f32_16x16x32_bf16 v[200:203], a[64:67], a[16:19], 0
v_exp_f32 v223, v223
v_cvt_pk_bf16_f32 v169, v82, v83
v_cvt_pk_bf16_f32 v170, v84, v85
v_cvt_pk_bf16_f32 v171, v86, v87
v_mfma_f32_16x16x32_bf16 v[200:203], a[68:71], a[20:23], v[200:203]
v_cvt_pk_bf16_f32 v239, v222, v223
s_and_b32 s89, s89, 0x1ffff
v_mul_f32 v88, v88, v242
.La5_LW2_4:
s_cbranch_execz .La5_LW2_8
v_mul_f32 v89, v89, v242
v_mul_f32 v90, v90, v242
v_mfma_f32_16x16x32_bf16 v[200:203], a[72:75], a[24:27], v[200:203]
v_exp_f32 v192, v192
v_mul_f32 v91, v91, v242
v_mul_f32 v92, v92, v242
v_mul_f32 v93, v93, v242
v_mfma_f32_16x16x32_bf16 v[200:203], a[76:79], a[28:31], v[200:203]
s_waitcnt lgkmcnt(0)
buffer_store_dwordx4 v[152:155], v245, s[44:47], s91 offen offset:2048 sc1
v_mul_f32 v94, v94, v242
v_mul_f32 v95, v95, v242
v_mfma_f32_16x16x32_bf16 v[196:199], a[80:83], a[0:3], 0
v_exp_f32 v193, v193
v_cvt_pk_bf16_f32 v172, v88, v89
v_cvt_pk_bf16_f32 v173, v90, v91
v_cvt_pk_bf16_f32 v174, v92, v93
v_mfma_f32_16x16x32_bf16 v[196:199], a[84:87], a[4:7], v[196:199]
v_cvt_pk_bf16_f32 v224, v192, v193
v_cvt_pk_bf16_f32 v175, v94, v95
v_mul_f32 v96, v96, v243
v_mul_f32 v97, v97, v243
v_mul_f32 v98, v98, v243
v_mfma_f32_16x16x32_bf16 v[196:199], a[88:91], a[8:11], v[196:199]
v_exp_f32 v194, v194
v_mul_f32 v99, v99, v243
v_mul_f32 v100, v100, v243
v_mul_f32 v101, v101, v243
v_mfma_f32_16x16x32_bf16 v[196:199], a[92:95], a[12:15], v[196:199]
buffer_load_dwordx4 v255, s[60:63], s17 offen offset:1024 sc1 lds
v_mul_f32 v102, v102, v243
v_mul_f32 v103, v103, v243
v_cvt_pk_bf16_f32 v176, v96, v97
v_mfma_f32_16x16x32_bf16 v[204:207], a[80:83], a[16:19], 0
v_exp_f32 v195, v195
v_cvt_pk_bf16_f32 v177, v98, v99
v_cvt_pk_bf16_f32 v178, v100, v101
v_cvt_pk_bf16_f32 v179, v102, v103
v_mfma_f32_16x16x32_bf16 v[204:207], a[84:87], a[20:23], v[204:207]
v_cvt_pk_bf16_f32 v225, v194, v195
buffer_store_dwordx4 v[156:159], v245, s[44:47], s91 offen offset:3072 sc1
v_mul_f32 v104, v104, v243
v_mul_f32 v105, v105, v243
v_mfma_f32_16x16x32_bf16 v[204:207], a[88:91], a[24:27], v[204:207]
v_exp_f32 v200, v200
v_mul_f32 v106, v106, v243
v_mul_f32 v107, v107, v243
v_mul_f32 v108, v108, v243
v_mfma_f32_16x16x32_bf16 v[204:207], a[92:95], a[28:31], v[204:207]
buffer_load_dwordx4 v255, s[60:63], s17 offen offset:2048 sc1 lds
v_mul_f32 v109, v109, v243
v_mul_f32 v110, v110, v243
v_mul_f32 v111, v111, v243
v_mfma_f32_16x16x32_bf16 v[208:211], a[64:67], a[32:35], 0
v_exp_f32 v201, v201
v_cvt_pk_bf16_f32 v180, v104, v105
v_cvt_pk_bf16_f32 v181, v106, v107
v_cvt_pk_bf16_f32 v182, v108, v109
v_mfma_f32_16x16x32_bf16 v[208:211], a[68:71], a[36:39], v[208:211]
v_cvt_pk_bf16_f32 v228, v200, v201
v_cvt_pk_bf16_f32 v183, v110, v111
v_mul_f32 v112, v112, v243
v_mul_f32 v113, v113, v243
v_mul_f32 v114, v114, v243
v_mfma_f32_16x16x32_bf16 v[208:211], a[72:75], a[40:43], v[208:211]
v_exp_f32 v202, v202
v_mul_f32 v115, v115, v243
v_mul_f32 v116, v116, v243
v_mul_f32 v117, v117, v243
v_mfma_f32_16x16x32_bf16 v[208:211], a[76:79], a[44:47], v[208:211]
buffer_load_dwordx4 v255, s[60:63], s17 offen offset:3072 sc1 lds
v_mul_f32 v118, v118, v243
v_mul_f32 v119, v119, v243
v_cvt_pk_bf16_f32 v184, v112, v113
v_mfma_f32_16x16x32_bf16 v[216:219], a[64:67], a[48:51], 0
v_exp_f32 v203, v203
v_cvt_pk_bf16_f32 v185, v114, v115
v_cvt_pk_bf16_f32 v186, v116, v117
v_cvt_pk_bf16_f32 v187, v118, v119
v_mfma_f32_16x16x32_bf16 v[216:219], a[68:71], a[52:55], v[216:219]
v_cvt_pk_bf16_f32 v229, v202, v203
v_mul_f32 v120, v120, v243
v_mul_f32 v121, v121, v243
v_mul_f32 v122, v122, v243
v_mul_f32 v123, v123, v243
v_mfma_f32_16x16x32_bf16 v[216:219], a[72:75], a[56:59], v[216:219]
v_exp_f32 v196, v196
v_mul_f32 v124, v124, v243
v_mul_f32 v125, v125, v243
v_mul_f32 v126, v126, v243
v_mfma_f32_16x16x32_bf16 v[216:219], a[76:79], a[60:63], v[216:219]
buffer_load_dword a253, v254, s[36:39], s90 offen
s_add_u32 s90, s90, s21
v_mul_f32 v127, v127, v243
v_cvt_pk_bf16_f32 v188, v120, v121
v_mfma_f32_16x16x32_bf16 v[212:215], a[80:83], a[32:35], 0
v_exp_f32 v197, v197
s_add_u32 s17, s17, 8192
v_cvt_pk_bf16_f32 v189, v122, v123
v_cvt_pk_bf16_f32 v190, v124, v125
v_mfma_f32_16x16x32_bf16 v[212:215], a[84:87], a[36:39], v[212:215]
v_cvt_pk_bf16_f32 v226, v196, v197
s_add_u32 m0, m0, 16384
s_and_b32 m0, m0, s78
v_cvt_pk_bf16_f32 v191, v126, v127
v_mfma_f32_16x16x32_bf16 v[212:215], a[88:91], a[40:43], v[212:215]
v_exp_f32 v198, v198
v_mfma_f32_16x16x32_bf16 v[212:215], a[92:95], a[44:47], v[212:215]
buffer_store_dwordx4 v[160:163], v246, s[44:47], s91 offen offset:0 sc1
v_mfma_f32_16x16x32_bf16 v[220:223], a[80:83], a[48:51], 0
v_exp_f32 v199, v199
v_mfma_f32_16x16x32_bf16 v[220:223], a[84:87], a[52:55], v[220:223]
v_cvt_pk_bf16_f32 v227, v198, v199
v_mfma_f32_16x16x32_bf16 v[220:223], a[88:91], a[56:59], v[220:223]
v_exp_f32 v204, v204
v_mfma_f32_16x16x32_bf16 v[220:223], a[92:95], a[60:63], v[220:223]
s_waitcnt vmcnt(20)
s_barrier
v_mfma_f32_16x16x32_bf16 v[64:67], a[96:99], v[232:235], 0
v_exp_f32 v205, v205
v_mfma_f32_16x16x32_bf16 v[96:99], a[96:99], v[236:239], 0
ds_read_b128 a[96:99], v248 offset:8192
v_cvt_pk_bf16_f32 v230, v204, v205
v_mfma_f32_16x16x32_bf16 v[68:71], a[100:103], v[232:235], 0
v_exp_f32 v206, v206
v_mfma_f32_16x16x32_bf16 v[100:103], a[100:103], v[236:239], 0
ds_read_b128 a[100:103], v248 offset:9216
buffer_store_dwordx4 v[164:167], v246, s[44:47], s91 offen offset:1024 sc1
v_mfma_f32_16x16x32_bf16 v[72:75], a[104:107], v[232:235], 0
v_exp_f32 v207, v207
v_mfma_f32_16x16x32_bf16 v[104:107], a[104:107], v[236:239], 0
ds_read_b128 a[104:107], v248 offset:10240
v_cvt_pk_bf16_f32 v231, v206, v207
v_mfma_f32_16x16x32_bf16 v[76:79], a[108:111], v[232:235], 0
v_exp_f32 v208, v208
v_mfma_f32_16x16x32_bf16 v[108:111], a[108:111], v[236:239], 0
ds_read_b128 a[108:111], v248 offset:11264
buffer_store_dwordx4 v[168:171], v246, s[44:47], s91 offen offset:2048 sc1
v_mfma_f32_16x16x32_bf16 v[80:83], a[112:115], v[232:235], 0
v_exp_f32 v209, v209
v_mfma_f32_16x16x32_bf16 v[112:115], a[112:115], v[236:239], 0
ds_read_b128 a[112:115], v248 offset:12288
v_mfma_f32_16x16x32_bf16 v[84:87], a[116:119], v[232:235], 0
v_exp_f32 v210, v210
v_mfma_f32_16x16x32_bf16 v[116:119], a[116:119], v[236:239], 0
ds_read_b128 a[116:119], v248 offset:13312
buffer_store_dwordx4 v[172:175], v246, s[44:47], s91 offen offset:3072 sc1
v_mfma_f32_16x16x32_bf16 v[88:91], a[120:123], v[232:235], 0
v_exp_f32 v211, v211
v_mfma_f32_16x16x32_bf16 v[120:123], a[120:123], v[236:239], 0
ds_read_b128 a[120:123], v248 offset:14336
v_mfma_f32_16x16x32_bf16 v[92:95], a[124:127], v[232:235], 0
v_exp_f32 v216, v216
v_mfma_f32_16x16x32_bf16 v[124:127], a[124:127], v[236:239], 0
ds_read_b128 a[124:127], v248 offset:15360
v_or_b32 v248, s89, v255
buffer_store_dwordx4 v[176:179], v247, s[44:47], s91 offen offset:0 sc1
v_smfmac_f32_16x16x64_bf16 a[196:199], a[208:211], v[232:239], v241
s_waitcnt lgkmcnt(4)
v_smfmac_f32_16x16x64_bf16 a[192:195], a[208:211], v[224:231], v241
ds_read_b128 a[64:67], v248 offset:0
v_mfma_f32_16x16x32_bf16 v[0:3], a[96:99], v[224:227], v[0:3]
v_exp_f32 v217, v217
v_mfma_f32_16x16x32_bf16 v[32:35], a[96:99], v[228:231], v[32:35]
ds_read_b128 a[68:71], v248 offset:1024
v_cvt_pk_bf16_f32 v232, v208, v209
v_mfma_f32_16x16x32_bf16 v[4:7], a[100:103], v[224:227], v[4:7]
v_exp_f32 v218, v218
v_mfma_f32_16x16x32_bf16 v[36:39], a[100:103], v[228:231], v[36:39]
ds_read_b128 a[72:75], v248 offset:2048
v_cvt_pk_bf16_f32 v233, v210, v211
buffer_store_dwordx4 v[180:183], v247, s[44:47], s91 offen offset:1024 sc1
v_mfma_f32_16x16x32_bf16 v[8:11], a[104:107], v[224:227], v[8:11]
v_exp_f32 v219, v219
v_mfma_f32_16x16x32_bf16 v[40:43], a[104:107], v[228:231], v[40:43]
ds_read_b128 a[76:79], v248 offset:3072
v_cvt_pk_bf16_f32 v236, v216, v217
v_mfma_f32_16x16x32_bf16 v[12:15], a[108:111], v[224:227], v[12:15]
v_exp_f32 v212, v212
v_mfma_f32_16x16x32_bf16 v[44:47], a[108:111], v[228:231], v[44:47]
ds_read_b128 a[80:83], v248 offset:4096
s_waitcnt lgkmcnt(5)
v_mfma_f32_16x16x32_bf16 v[16:19], a[112:115], v[224:227], v[16:19]
v_exp_f32 v213, v213
v_mfma_f32_16x16x32_bf16 v[48:51], a[112:115], v[228:231], v[48:51]
ds_read_b128 a[84:87], v248 offset:5120
v_cvt_pk_bf16_f32 v237, v218, v219
buffer_store_dwordx4 v[184:187], v247, s[44:47], s91 offen offset:2048 sc1
v_mfma_f32_16x16x32_bf16 v[20:23], a[116:119], v[224:227], v[20:23]
v_exp_f32 v214, v214
v_mfma_f32_16x16x32_bf16 v[52:55], a[116:119], v[228:231], v[52:55]
ds_read_b128 a[88:91], v248 offset:6144
v_cvt_pk_bf16_f32 v234, v212, v213
v_mfma_f32_16x16x32_bf16 v[24:27], a[120:123], v[224:227], v[24:27]
v_exp_f32 v215, v215
v_mfma_f32_16x16x32_bf16 v[56:59], a[120:123], v[228:231], v[56:59]
ds_read_b128 a[92:95], v248 offset:7168
v_cvt_pk_bf16_f32 v235, v214, v215
buffer_store_dwordx4 v[188:191], v247, s[44:47], s91 offen offset:3072 sc1
v_mfma_f32_16x16x32_bf16 v[28:31], a[124:127], v[224:227], v[28:31]
v_exp_f32 v220, v220
v_mfma_f32_16x16x32_bf16 v[60:63], a[124:127], v[228:231], v[60:63]
s_add_u32 s85, s85, 1
s_cmp_eq_u32 s85, 22
s_cbranch_scc1 .La5_LA_F3
.p2align 6
.La5_LA_F2:
s_waitcnt lgkmcnt(4)
v_mfma_f32_16x16x32_bf16 v[192:195], a[64:67], a[0:3], 0
v_exp_f32 v221, v221
v_mfma_f32_16x16x32_bf16 v[192:195], a[68:71], a[4:7], v[192:195]
v_cvt_pk_bf16_f32 v238, v220, v221
s_add_u32 s85, s85, 1
v_mfma_f32_16x16x32_bf16 v[192:195], a[72:75], a[8:11], v[192:195]
v_exp_f32 v222, v222
v_mfma_f32_16x16x32_bf16 v[192:195], a[76:79], a[12:15], v[192:195]
buffer_load_dwordx4 v255, s[60:63], s17 offen offset:0 sc1 lds
v_mfma_f32_16x16x32_bf16 v[200:203], a[64:67], a[16:19], 0
v_exp_f32 v223, v223
v_mfma_f32_16x16x32_bf16 v[200:203], a[68:71], a[20:23], v[200:203]
v_cvt_pk_bf16_f32 v239, v222, v223
s_add_u32 s89, s89, 16384
v_mfma_f32_16x16x32_bf16 v[200:203], a[72:75], a[24:27], v[200:203]
v_exp_f32 v192, v192
v_mfma_f32_16x16x32_bf16 v[200:203], a[76:79], a[28:31], v[200:203]
s_waitcnt lgkmcnt(0)
s_and_b32 s89, s89, 0x1ffff
v_mfma_f32_16x16x32_bf16 v[196:199], a[80:83], a[0:3], 0
v_exp_f32 v193, v193
v_mfma_f32_16x16x32_bf16 v[196:199], a[84:87], a[4:7], v[196:199]
v_cvt_pk_bf16_f32 v224, v192, v193
v_mfma_f32_16x16x32_bf16 v[196:199], a[88:91], a[8:11], v[196:199]
v_exp_f32 v194, v194
v_mfma_f32_16x16x32_bf16 v[196:199], a[92:95], a[12:15], v[196:199]
buffer_load_dwordx4 v255, s[60:63], s17 offen offset:1024 sc1 lds
v_mfma_f32_16x16x32_bf16 v[204:207], a[80:83], a[16:19], 0
v_exp_f32 v195, v195
v_mfma_f32_16x16x32_bf16 v[204:207], a[84:87], a[20:23], v[204:207]
v_cvt_pk_bf16_f32 v225, v194, v195
v_mfma_f32_16x16x32_bf16 v[204:207], a[88:91], a[24:27], v[204:207]
v_exp_f32 v200, v200
v_mfma_f32_16x16x32_bf16 v[204:207], a[92:95], a[28:31], v[204:207]
buffer_load_dwordx4 v255, s[60:63], s17 offen offset:2048 sc1 lds
v_mfma_f32_16x16x32_bf16 v[208:211], a[64:67], a[32:35], 0
v_exp_f32 v201, v201
v_mfma_f32_16x16x32_bf16 v[208:211], a[68:71], a[36:39], v[208:211]
v_cvt_pk_bf16_f32 v228, v200, v201
v_mfma_f32_16x16x32_bf16 v[208:211], a[72:75], a[40:43], v[208:211]
v_exp_f32 v202, v202
v_mfma_f32_16x16x32_bf16 v[208:211], a[76:79], a[44:47], v[208:211]
buffer_load_dwordx4 v255, s[60:63], s17 offen offset:3072 sc1 lds
v_mfma_f32_16x16x32_bf16 v[216:219], a[64:67], a[48:51], 0
v_exp_f32 v203, v203
v_mfma_f32_16x16x32_bf16 v[216:219], a[68:71], a[52:55], v[216:219]
v_cvt_pk_bf16_f32 v229, v202, v203
v_mfma_f32_16x16x32_bf16 v[216:219], a[72:75], a[56:59], v[216:219]
v_exp_f32 v196, v196
v_mfma_f32_16x16x32_bf16 v[216:219], a[76:79], a[60:63], v[216:219]
buffer_load_dword a253, v254, s[36:39], s90 offen
v_mfma_f32_16x16x32_bf16 v[212:215], a[80:83], a[32:35], 0
v_exp_f32 v197, v197
v_mfma_f32_16x16x32_bf16 v[212:215], a[84:87], a[36:39], v[212:215]
v_cvt_pk_bf16_f32 v226, v196, v197
s_add_u32 s17, s17, 8192
v_mfma_f32_16x16x32_bf16 v[212:215], a[88:91], a[40:43], v[212:215]
v_exp_f32 v198, v198
v_mfma_f32_16x16x32_bf16 v[212:215], a[92:95], a[44:47], v[212:215]
s_add_u32 m0, m0, 16384
s_and_b32 m0, m0, s78
v_mfma_f32_16x16x32_bf16 v[220:223], a[80:83], a[48:51], 0
v_exp_f32 v199, v199
v_mfma_f32_16x16x32_bf16 v[220:223], a[84:87], a[52:55], v[220:223]
v_cvt_pk_bf16_f32 v227, v198, v199
s_add_u32 s90, s90, s21
v_mfma_f32_16x16x32_bf16 v[220:223], a[88:91], a[56:59], v[220:223]
v_exp_f32 v204, v204
v_mfma_f32_16x16x32_bf16 v[220:223], a[92:95], a[60:63], v[220:223]
s_waitcnt vmcnt(11)
s_barrier
v_mfma_f32_16x16x32_bf16 v[64:67], a[96:99], v[232:235], v[64:67]
v_exp_f32 v205, v205
v_mfma_f32_16x16x32_bf16 v[96:99], a[96:99], v[236:239], v[96:99]
ds_read_b128 a[96:99], v248 offset:8192
v_cvt_pk_bf16_f32 v230, v204, v205
v_mfma_f32_16x16x32_bf16 v[68:71], a[100:103], v[232:235], v[68:71]
v_exp_f32 v206, v206
v_mfma_f32_16x16x32_bf16 v[100:103], a[100:103], v[236:239], v[100:103]
ds_read_b128 a[100:103], v248 offset:9216
v_mfma_f32_16x16x32_bf16 v[72:75], a[104:107], v[232:235], v[72:75]
v_exp_f32 v207, v207
v_mfma_f32_16x16x32_bf16 v[104:107], a[104:107], v[236:239], v[104:107]
ds_read_b128 a[104:107], v248 offset:10240
v_cvt_pk_bf16_f32 v231, v206, v207
v_mfma_f32_16x16x32_bf16 v[76:79], a[108:111], v[232:235], v[76:79]
v_exp_f32 v208, v208
v_mfma_f32_16x16x32_bf16 v[108:111], a[108:111], v[236:239], v[108:111]
ds_read_b128 a[108:111], v248 offset:11264
v_mfma_f32_16x16x32_bf16 v[80:83], a[112:115], v[232:235], v[80:83]
v_exp_f32 v209, v209
v_mfma_f32_16x16x32_bf16 v[112:115], a[112:115], v[236:239], v[112:115]
ds_read_b128 a[112:115], v248 offset:12288
v_mfma_f32_16x16x32_bf16 v[84:87], a[116:119], v[232:235], v[84:87]
v_exp_f32 v210, v210
v_mfma_f32_16x16x32_bf16 v[116:119], a[116:119], v[236:239], v[116:119]
ds_read_b128 a[116:119], v248 offset:13312
v_mfma_f32_16x16x32_bf16 v[88:91], a[120:123], v[232:235], v[88:91]
v_exp_f32 v211, v211
v_mfma_f32_16x16x32_bf16 v[120:123], a[120:123], v[236:239], v[120:123]
ds_read_b128 a[120:123], v248 offset:14336
s_bitcmp1_b64 s[98:99], s85
v_mfma_f32_16x16x32_bf16 v[92:95], a[124:127], v[232:235], v[92:95]
v_exp_f32 v216, v216
v_mfma_f32_16x16x32_bf16 v[124:127], a[124:127], v[236:239], v[124:127]
ds_read_b128 a[124:127], v248 offset:15360
v_or_b32 v248, s89, v255
v_smfmac_f32_16x16x64_bf16 a[196:199], a[208:211], v[232:239], v241
s_waitcnt lgkmcnt(4)
v_smfmac_f32_16x16x64_bf16 a[192:195], a[208:211], v[224:231], v241
ds_read_b128 a[64:67], v248 offset:0
v_mfma_f32_16x16x32_bf16 v[0:3], a[96:99], v[224:227], v[0:3]
v_exp_f32 v217, v217
v_mfma_f32_16x16x32_bf16 v[32:35], a[96:99], v[228:231], v[32:35]
ds_read_b128 a[68:71], v248 offset:1024
v_cvt_pk_bf16_f32 v232, v208, v209
v_mfma_f32_16x16x32_bf16 v[4:7], a[100:103], v[224:227], v[4:7]
v_exp_f32 v218, v218
v_mfma_f32_16x16x32_bf16 v[36:39], a[100:103], v[228:231], v[36:39]
ds_read_b128 a[72:75], v248 offset:2048
v_cvt_pk_bf16_f32 v233, v210, v211
v_mfma_f32_16x16x32_bf16 v[8:11], a[104:107], v[224:227], v[8:11]
v_exp_f32 v219, v219
v_mfma_f32_16x16x32_bf16 v[40:43], a[104:107], v[228:231], v[40:43]
ds_read_b128 a[76:79], v248 offset:3072
v_cvt_pk_bf16_f32 v236, v216, v217
v_mfma_f32_16x16x32_bf16 v[12:15], a[108:111], v[224:227], v[12:15]
v_exp_f32 v212, v212
v_mfma_f32_16x16x32_bf16 v[44:47], a[108:111], v[228:231], v[44:47]
ds_read_b128 a[80:83], v248 offset:4096
s_waitcnt lgkmcnt(5)
v_mfma_f32_16x16x32_bf16 v[16:19], a[112:115], v[224:227], v[16:19]
v_exp_f32 v213, v213
v_mfma_f32_16x16x32_bf16 v[48:51], a[112:115], v[228:231], v[48:51]
ds_read_b128 a[84:87], v248 offset:5120
v_cvt_pk_bf16_f32 v237, v218, v219
v_mfma_f32_16x16x32_bf16 v[20:23], a[116:119], v[224:227], v[20:23]
v_exp_f32 v214, v214
v_mfma_f32_16x16x32_bf16 v[52:55], a[116:119], v[228:231], v[52:55]
ds_read_b128 a[88:91], v248 offset:6144
v_cvt_pk_bf16_f32 v234, v212, v213
v_mfma_f32_16x16x32_bf16 v[24:27], a[120:123], v[224:227], v[24:27]
v_exp_f32 v215, v215
v_mfma_f32_16x16x32_bf16 v[56:59], a[120:123], v[228:231], v[56:59]
ds_read_b128 a[92:95], v248 offset:7168
v_cvt_pk_bf16_f32 v235, v214, v215
v_mfma_f32_16x16x32_bf16 v[28:31], a[124:127], v[224:227], v[28:31]
v_exp_f32 v220, v220
v_mfma_f32_16x16x32_bf16 v[60:63], a[124:127], v[228:231], v[60:63]
s_cbranch_scc1 .La5_LA_F2E2
s_waitcnt lgkmcnt(4)
v_mfma_f32_16x16x32_bf16 v[192:195], a[64:67], a[0:3], 0
v_exp_f32 v221, v221
v_mfma_f32_16x16x32_bf16 v[192:195], a[68:71], a[4:7], v[192:195]
v_cvt_pk_bf16_f32 v238, v220, v221
s_add_u32 s85, s85, 1
v_mfma_f32_16x16x32_bf16 v[192:195], a[72:75], a[8:11], v[192:195]
v_exp_f32 v222, v222
v_mfma_f32_16x16x32_bf16 v[192:195], a[76:79], a[12:15], v[192:195]
buffer_load_dwordx4 v255, s[60:63], s17 offen offset:0 sc1 lds
v_mfma_f32_16x16x32_bf16 v[200:203], a[64:67], a[16:19], 0
v_exp_f32 v223, v223
v_mfma_f32_16x16x32_bf16 v[200:203], a[68:71], a[20:23], v[200:203]
v_cvt_pk_bf16_f32 v239, v222, v223
s_add_u32 s89, s89, 16384
v_mfma_f32_16x16x32_bf16 v[200:203], a[72:75], a[24:27], v[200:203]
v_exp_f32 v192, v192
v_mfma_f32_16x16x32_bf16 v[200:203], a[76:79], a[28:31], v[200:203]
s_waitcnt lgkmcnt(0)
s_and_b32 s89, s89, 0x1ffff
v_mfma_f32_16x16x32_bf16 v[196:199], a[80:83], a[0:3], 0
v_exp_f32 v193, v193
v_mfma_f32_16x16x32_bf16 v[196:199], a[84:87], a[4:7], v[196:199]
v_cvt_pk_bf16_f32 v224, v192, v193
v_mfma_f32_16x16x32_bf16 v[196:199], a[88:91], a[8:11], v[196:199]
v_exp_f32 v194, v194
v_mfma_f32_16x16x32_bf16 v[196:199], a[92:95], a[12:15], v[196:199]
buffer_load_dwordx4 v255, s[60:63], s17 offen offset:1024 sc1 lds
v_mfma_f32_16x16x32_bf16 v[204:207], a[80:83], a[16:19], 0
v_exp_f32 v195, v195
v_mfma_f32_16x16x32_bf16 v[204:207], a[84:87], a[20:23], v[204:207]
v_cvt_pk_bf16_f32 v225, v194, v195
v_mfma_f32_16x16x32_bf16 v[204:207], a[88:91], a[24:27], v[204:207]
v_exp_f32 v200, v200
v_mfma_f32_16x16x32_bf16 v[204:207], a[92:95], a[28:31], v[204:207]
buffer_load_dwordx4 v255, s[60:63], s17 offen offset:2048 sc1 lds
v_mfma_f32_16x16x32_bf16 v[208:211], a[64:67], a[32:35], 0
v_exp_f32 v201, v201
v_mfma_f32_16x16x32_bf16 v[208:211], a[68:71], a[36:39], v[208:211]
v_cvt_pk_bf16_f32 v228, v200, v201
v_mfma_f32_16x16x32_bf16 v[208:211], a[72:75], a[40:43], v[208:211]
v_exp_f32 v202, v202
v_mfma_f32_16x16x32_bf16 v[208:211], a[76:79], a[44:47], v[208:211]
buffer_load_dwordx4 v255, s[60:63], s17 offen offset:3072 sc1 lds
v_mfma_f32_16x16x32_bf16 v[216:219], a[64:67], a[48:51], 0
v_exp_f32 v203, v203
v_mfma_f32_16x16x32_bf16 v[216:219], a[68:71], a[52:55], v[216:219]
v_cvt_pk_bf16_f32 v229, v202, v203
v_mfma_f32_16x16x32_bf16 v[216:219], a[72:75], a[56:59], v[216:219]
v_exp_f32 v196, v196
v_mfma_f32_16x16x32_bf16 v[216:219], a[76:79], a[60:63], v[216:219]
buffer_load_dword a253, v254, s[36:39], s90 offen
v_mfma_f32_16x16x32_bf16 v[212:215], a[80:83], a[32:35], 0
v_exp_f32 v197, v197
v_mfma_f32_16x16x32_bf16 v[212:215], a[84:87], a[36:39], v[212:215]
v_cvt_pk_bf16_f32 v226, v196, v197
s_add_u32 s17, s17, 8192
v_mfma_f32_16x16x32_bf16 v[212:215], a[88:91], a[40:43], v[212:215]
v_exp_f32 v198, v198
v_mfma_f32_16x16x32_bf16 v[212:215], a[92:95], a[44:47], v[212:215]
s_add_u32 m0, m0, 16384
s_and_b32 m0, m0, s78
v_mfma_f32_16x16x32_bf16 v[220:223], a[80:83], a[48:51], 0
v_exp_f32 v199, v199
v_mfma_f32_16x16x32_bf16 v[220:223], a[84:87], a[52:55], v[220:223]
v_cvt_pk_bf16_f32 v227, v198, v199
s_add_u32 s90, s90, s21
v_mfma_f32_16x16x32_bf16 v[220:223], a[88:91], a[56:59], v[220:223]
v_exp_f32 v204, v204
v_mfma_f32_16x16x32_bf16 v[220:223], a[92:95], a[60:63], v[220:223]
s_waitcnt vmcnt(11)
s_barrier
v_mfma_f32_16x16x32_bf16 v[64:67], a[96:99], v[232:235], v[64:67]
v_exp_f32 v205, v205
v_mfma_f32_16x16x32_bf16 v[96:99], a[96:99], v[236:239], v[96:99]
ds_read_b128 a[96:99], v248 offset:8192
v_cvt_pk_bf16_f32 v230, v204, v205
v_mfma_f32_16x16x32_bf16 v[68:71], a[100:103], v[232:235], v[68:71]
v_exp_f32 v206, v206
v_mfma_f32_16x16x32_bf16 v[100:103], a[100:103], v[236:239], v[100:103]
ds_read_b128 a[100:103], v248 offset:9216
v_mfma_f32_16x16x32_bf16 v[72:75], a[104:107], v[232:235], v[72:75]
v_exp_f32 v207, v207
v_mfma_f32_16x16x32_bf16 v[104:107], a[104:107], v[236:239], v[104:107]
ds_read_b128 a[104:107], v248 offset:10240
v_cvt_pk_bf16_f32 v231, v206, v207
v_mfma_f32_16x16x32_bf16 v[76:79], a[108:111], v[232:235], v[76:79]
v_exp_f32 v208, v208
v_mfma_f32_16x16x32_bf16 v[108:111], a[108:111], v[236:239], v[108:111]
ds_read_b128 a[108:111], v248 offset:11264
v_mfma_f32_16x16x32_bf16 v[80:83], a[112:115], v[232:235], v[80:83]
v_exp_f32 v209, v209
v_mfma_f32_16x16x32_bf16 v[112:115], a[112:115], v[236:239], v[112:115]
ds_read_b128 a[112:115], v248 offset:12288
v_mfma_f32_16x16x32_bf16 v[84:87], a[116:119], v[232:235], v[84:87]
v_exp_f32 v210, v210
v_mfma_f32_16x16x32_bf16 v[116:119], a[116:119], v[236:239], v[116:119]
ds_read_b128 a[116:119], v248 offset:13312
v_mfma_f32_16x16x32_bf16 v[88:91], a[120:123], v[232:235], v[88:91]
v_exp_f32 v211, v211
v_mfma_f32_16x16x32_bf16 v[120:123], a[120:123], v[236:239], v[120:123]
ds_read_b128 a[120:123], v248 offset:14336
s_bitcmp1_b64 s[98:99], s85
v_mfma_f32_16x16x32_bf16 v[92:95], a[124:127], v[232:235], v[92:95]
v_exp_f32 v216, v216
v_mfma_f32_16x16x32_bf16 v[124:127], a[124:127], v[236:239], v[124:127]
ds_read_b128 a[124:127], v248 offset:15360
v_or_b32 v248, s89, v255
v_smfmac_f32_16x16x64_bf16 a[196:199], a[208:211], v[232:239], v241
s_waitcnt lgkmcnt(4)
v_smfmac_f32_16x16x64_bf16 a[192:195], a[208:211], v[224:231], v241
ds_read_b128 a[64:67], v248 offset:0
v_mfma_f32_16x16x32_bf16 v[0:3], a[96:99], v[224:227], v[0:3]
v_exp_f32 v217, v217
v_mfma_f32_16x16x32_bf16 v[32:35], a[96:99], v[228:231], v[32:35]
ds_read_b128 a[68:71], v248 offset:1024
v_cvt_pk_bf16_f32 v232, v208, v209
v_mfma_f32_16x16x32_bf16 v[4:7], a[100:103], v[224:227], v[4:7]
v_exp_f32 v218, v218
v_mfma_f32_16x16x32_bf16 v[36:39], a[100:103], v[228:231], v[36:39]
ds_read_b128 a[72:75], v248 offset:2048
v_cvt_pk_bf16_f32 v233, v210, v211
v_mfma_f32_16x16x32_bf16 v[8:11], a[104:107], v[224:227], v[8:11]
v_exp_f32 v219, v219
v_mfma_f32_16x16x32_bf16 v[40:43], a[104:107], v[228:231], v[40:43]
ds_read_b128 a[76:79], v248 offset:3072
v_cvt_pk_bf16_f32 v236, v216, v217
v_mfma_f32_16x16x32_bf16 v[12:15], a[108:111], v[224:227], v[12:15]
v_exp_f32 v212, v212
v_mfma_f32_16x16x32_bf16 v[44:47], a[108:111], v[228:231], v[44:47]
ds_read_b128 a[80:83], v248 offset:4096
s_waitcnt lgkmcnt(5)
v_mfma_f32_16x16x32_bf16 v[16:19], a[112:115], v[224:227], v[16:19]
v_exp_f32 v213, v213
v_mfma_f32_16x16x32_bf16 v[48:51], a[112:115], v[228:231], v[48:51]
ds_read_b128 a[84:87], v248 offset:5120
v_cvt_pk_bf16_f32 v237, v218, v219
v_mfma_f32_16x16x32_bf16 v[20:23], a[116:119], v[224:227], v[20:23]
v_exp_f32 v214, v214
v_mfma_f32_16x16x32_bf16 v[52:55], a[116:119], v[228:231], v[52:55]
ds_read_b128 a[88:91], v248 offset:6144
v_cvt_pk_bf16_f32 v234, v212, v213
v_mfma_f32_16x16x32_bf16 v[24:27], a[120:123], v[224:227], v[24:27]
v_exp_f32 v215, v215
v_mfma_f32_16x16x32_bf16 v[56:59], a[120:123], v[228:231], v[56:59]
ds_read_b128 a[92:95], v248 offset:7168
v_cvt_pk_bf16_f32 v235, v214, v215
v_mfma_f32_16x16x32_bf16 v[28:31], a[124:127], v[224:227], v[28:31]
v_exp_f32 v220, v220
v_mfma_f32_16x16x32_bf16 v[60:63], a[124:127], v[228:231], v[60:63]
s_cbranch_scc0 .La5_LA_F2
.La5_LA_F2E2:
s_cmp_eq_u32 s85, 22
s_cbranch_scc1 .La5_LA_F3
s_nop 0
s_nop 0
s_call_b64 s[24:25], .La5_LA_EVENT
s_branch .La5_LA_F2
.La5_LA_F3:
.La5_LW2_5:
s_cbranch_execz .La5_LW2_9
s_waitcnt lgkmcnt(4)
v_mfma_f32_16x16x32_bf16 v[192:195], a[64:67], a[0:3], 0
v_exp_f32 v221, v221
v_mfma_f32_16x16x32_bf16 v[192:195], a[68:71], a[4:7], v[192:195]
v_cvt_pk_bf16_f32 v238, v220, v221
s_add_u32 s89, s89, 16384
v_mfma_f32_16x16x32_bf16 v[192:195], a[72:75], a[8:11], v[192:195]
v_exp_f32 v222, v222
v_mfma_f32_16x16x32_bf16 v[192:195], a[76:79], a[12:15], v[192:195]
buffer_load_dwordx4 v255, s[60:63], s17 offen offset:0 sc1 lds
v_mfma_f32_16x16x32_bf16 v[200:203], a[64:67], a[16:19], 0
v_exp_f32 v223, v223
v_mfma_f32_16x16x32_bf16 v[200:203], a[68:71], a[20:23], v[200:203]
v_cvt_pk_bf16_f32 v239, v222, v223
s_and_b32 s89, s89, 0x1ffff
v_mfma_f32_16x16x32_bf16 v[200:203], a[72:75], a[24:27], v[200:203]
v_exp_f32 v192, v192
v_mfma_f32_16x16x32_bf16 v[200:203], a[76:79], a[28:31], v[200:203]
s_waitcnt lgkmcnt(0)
v_mfma_f32_16x16x32_bf16 v[196:199], a[80:83], a[0:3], 0
v_exp_f32 v193, v193
v_mfma_f32_16x16x32_bf16 v[196:199], a[84:87], a[4:7], v[196:199]
v_cvt_pk_bf16_f32 v224, v192, v193
v_accvgpr_read_b32 v254, a229
v_mfma_f32_16x16x32_bf16 v[196:199], a[88:91], a[8:11], v[196:199]
v_exp_f32 v194, v194
v_mfma_f32_16x16x32_bf16 v[196:199], a[92:95], a[12:15], v[196:199]
buffer_load_dwordx4 v255, s[60:63], s17 offen offset:1024 sc1 lds
v_mfma_f32_16x16x32_bf16 v[204:207], a[80:83], a[16:19], 0
v_exp_f32 v195, v195
v_mfma_f32_16x16x32_bf16 v[204:207], a[84:87], a[20:23], v[204:207]
v_cvt_pk_bf16_f32 v225, v194, v195
v_mfma_f32_16x16x32_bf16 v[204:207], a[88:91], a[24:27], v[204:207]
v_exp_f32 v200, v200
v_mfma_f32_16x16x32_bf16 v[204:207], a[92:95], a[28:31], v[204:207]
buffer_load_dwordx4 v255, s[60:63], s17 offen offset:2048 sc1 lds
v_mfma_f32_16x16x32_bf16 v[208:211], a[64:67], a[32:35], 0
v_exp_f32 v201, v201
v_mfma_f32_16x16x32_bf16 v[208:211], a[68:71], a[36:39], v[208:211]
v_cvt_pk_bf16_f32 v228, v200, v201
v_mfma_f32_16x16x32_bf16 v[208:211], a[72:75], a[40:43], v[208:211]
v_exp_f32 v202, v202
v_mfma_f32_16x16x32_bf16 v[208:211], a[76:79], a[44:47], v[208:211]
buffer_load_dwordx4 v255, s[60:63], s17 offen offset:3072 sc1 lds
v_mfma_f32_16x16x32_bf16 v[216:219], a[64:67], a[48:51], 0
v_exp_f32 v203, v203
v_mfma_f32_16x16x32_bf16 v[216:219], a[68:71], a[52:55], v[216:219]
v_cvt_pk_bf16_f32 v229, v202, v203
s_mov_b32 s93, 0
v_mfma_f32_16x16x32_bf16 v[216:219], a[72:75], a[56:59], v[216:219]
v_exp_f32 v196, v196
v_mfma_f32_16x16x32_bf16 v[216:219], a[76:79], a[60:63], v[216:219]
buffer_load_dwordx4 a[128:131], v254, s[40:43], s93 offen
v_mfma_f32_16x16x32_bf16 v[212:215], a[80:83], a[32:35], 0
v_exp_f32 v197, v197
s_add_u32 s17, s17, 8192
v_mfma_f32_16x16x32_bf16 v[212:215], a[84:87], a[36:39], v[212:215]
v_cvt_pk_bf16_f32 v226, v196, v197
s_mov_b32 s93, 7168
s_add_u32 m0, m0, 16384
v_mfma_f32_16x16x32_bf16 v[212:215], a[88:91], a[40:43], v[212:215]
v_exp_f32 v198, v198
s_and_b32 m0, m0, s78
v_mfma_f32_16x16x32_bf16 v[212:215], a[92:95], a[44:47], v[212:215]
buffer_load_dwordx4 a[132:135], v254, s[40:43], s93 offen
v_mfma_f32_16x16x32_bf16 v[220:223], a[80:83], a[48:51], 0
v_exp_f32 v199, v199
v_mfma_f32_16x16x32_bf16 v[220:223], a[84:87], a[52:55], v[220:223]
v_cvt_pk_bf16_f32 v227, v198, v199
v_mfma_f32_16x16x32_bf16 v[220:223], a[88:91], a[56:59], v[220:223]
v_exp_f32 v204, v204
v_mfma_f32_16x16x32_bf16 v[220:223], a[92:95], a[60:63], v[220:223]
s_waitcnt vmcnt(12)
s_barrier
v_mfma_f32_16x16x32_bf16 v[64:67], a[96:99], v[232:235], v[64:67]
v_exp_f32 v205, v205
v_mfma_f32_16x16x32_bf16 v[96:99], a[96:99], v[236:239], v[96:99]
ds_read_b128 a[96:99], v248 offset:8192
v_cvt_pk_bf16_f32 v230, v204, v205
v_mfma_f32_16x16x32_bf16 v[68:71], a[100:103], v[232:235], v[68:71]
v_exp_f32 v206, v206
v_mfma_f32_16x16x32_bf16 v[100:103], a[100:103], v[236:239], v[100:103]
ds_read_b128 a[100:103], v248 offset:9216
v_mfma_f32_16x16x32_bf16 v[72:75], a[104:107], v[232:235], v[72:75]
v_exp_f32 v207, v207
v_mfma_f32_16x16x32_bf16 v[104:107], a[104:107], v[236:239], v[104:107]
ds_read_b128 a[104:107], v248 offset:10240
v_cvt_pk_bf16_f32 v231, v206, v207
s_mov_b32 s93, 14336
v_mfma_f32_16x16x32_bf16 v[76:79], a[108:111], v[232:235], v[76:79]
v_exp_f32 v208, v208
v_mfma_f32_16x16x32_bf16 v[108:111], a[108:111], v[236:239], v[108:111]
ds_read_b128 a[108:111], v248 offset:11264
buffer_load_dwordx4 a[136:139], v254, s[40:43], s93 offen
v_mfma_f32_16x16x32_bf16 v[80:83], a[112:115], v[232:235], v[80:83]
v_exp_f32 v209, v209
.La5_LW2_6:
s_cbranch_execz .La5_LW2_10
v_mfma_f32_16x16x32_bf16 v[112:115], a[112:115], v[236:239], v[112:115]
ds_read_b128 a[112:115], v248 offset:12288
v_mfma_f32_16x16x32_bf16 v[84:87], a[116:119], v[232:235], v[84:87]
v_exp_f32 v210, v210
v_mfma_f32_16x16x32_bf16 v[116:119], a[116:119], v[236:239], v[116:119]
ds_read_b128 a[116:119], v248 offset:13312
v_mfma_f32_16x16x32_bf16 v[88:91], a[120:123], v[232:235], v[88:91]
v_exp_f32 v211, v211
v_mfma_f32_16x16x32_bf16 v[120:123], a[120:123], v[236:239], v[120:123]
ds_read_b128 a[120:123], v248 offset:14336
v_mfma_f32_16x16x32_bf16 v[92:95], a[124:127], v[232:235], v[92:95]
v_exp_f32 v216, v216
v_mfma_f32_16x16x32_bf16 v[124:127], a[124:127], v[236:239], v[124:127]
ds_read_b128 a[124:127], v248 offset:15360
v_or_b32 v248, s89, v255
v_smfmac_f32_16x16x64_bf16 a[196:199], a[208:211], v[232:239], v241
s_waitcnt lgkmcnt(4)
v_smfmac_f32_16x16x64_bf16 a[192:195], a[208:211], v[224:231], v241
ds_read_b128 a[64:67], v248 offset:0
v_mfma_f32_16x16x32_bf16 v[0:3], a[96:99], v[224:227], v[0:3]
v_exp_f32 v217, v217
v_mfma_f32_16x16x32_bf16 v[32:35], a[96:99], v[228:231], v[32:35]
ds_read_b128 a[68:71], v248 offset:1024
v_cvt_pk_bf16_f32 v232, v208, v209
v_mfma_f32_16x16x32_bf16 v[4:7], a[100:103], v[224:227], v[4:7]
v_exp_f32 v218, v218
v_mfma_f32_16x16x32_bf16 v[36:39], a[100:103], v[228:231], v[36:39]
ds_read_b128 a[72:75], v248 offset:2048
v_cvt_pk_bf16_f32 v233, v210, v211
v_mfma_f32_16x16x32_bf16 v[8:11], a[104:107], v[224:227], v[8:11]
v_exp_f32 v219, v219
v_mfma_f32_16x16x32_bf16 v[40:43], a[104:107], v[228:231], v[40:43]
ds_read_b128 a[76:79], v248 offset:3072
v_cvt_pk_bf16_f32 v236, v216, v217
v_mfma_f32_16x16x32_bf16 v[12:15], a[108:111], v[224:227], v[12:15]
v_exp_f32 v212, v212
v_mfma_f32_16x16x32_bf16 v[44:47], a[108:111], v[228:231], v[44:47]
ds_read_b128 a[80:83], v248 offset:4096
s_waitcnt lgkmcnt(5)
v_mfma_f32_16x16x32_bf16 v[16:19], a[112:115], v[224:227], v[16:19]
v_exp_f32 v213, v213
v_mfma_f32_16x16x32_bf16 v[48:51], a[112:115], v[228:231], v[48:51]
ds_read_b128 a[84:87], v248 offset:5120
v_cvt_pk_bf16_f32 v237, v218, v219
v_mfma_f32_16x16x32_bf16 v[20:23], a[116:119], v[224:227], v[20:23]
v_exp_f32 v214, v214
v_mfma_f32_16x16x32_bf16 v[52:55], a[116:119], v[228:231], v[52:55]
ds_read_b128 a[88:91], v248 offset:6144
v_cvt_pk_bf16_f32 v234, v212, v213
v_mfma_f32_16x16x32_bf16 v[24:27], a[120:123], v[224:227], v[24:27]
v_exp_f32 v215, v215
v_mfma_f32_16x16x32_bf16 v[56:59], a[120:123], v[228:231], v[56:59]
ds_read_b128 a[92:95], v248 offset:7168
v_cvt_pk_bf16_f32 v235, v214, v215
v_mfma_f32_16x16x32_bf16 v[28:31], a[124:127], v[224:227], v[28:31]
v_exp_f32 v220, v220
v_mfma_f32_16x16x32_bf16 v[60:63], a[124:127], v[228:231], v[60:63]
s_waitcnt lgkmcnt(4)
v_mfma_f32_16x16x32_bf16 v[192:195], a[64:67], a[0:3], 0
v_exp_f32 v221, v221
v_mfma_f32_16x16x32_bf16 v[192:195], a[68:71], a[4:7], v[192:195]
v_cvt_pk_bf16_f32 v238, v220, v221
s_add_u32 s89, s89, 16384
v_mfma_f32_16x16x32_bf16 v[192:195], a[72:75], a[8:11], v[192:195]
v_exp_f32 v222, v222
v_mfma_f32_16x16x32_bf16 v[192:195], a[76:79], a[12:15], v[192:195]
buffer_load_dwordx4 v255, s[60:63], s17 offen offset:0 sc1 lds
v_mfma_f32_16x16x32_bf16 v[200:203], a[64:67], a[16:19], 0
v_exp_f32 v223, v223
v_mfma_f32_16x16x32_bf16 v[200:203], a[68:71], a[20:23], v[200:203]
v_cvt_pk_bf16_f32 v239, v222, v223
s_and_b32 s89, s89, 0x1ffff
v_mfma_f32_16x16x32_bf16 v[200:203], a[72:75], a[24:27], v[200:203]
v_exp_f32 v192, v192
v_mfma_f32_16x16x32_bf16 v[200:203], a[76:79], a[28:31], v[200:203]
s_waitcnt lgkmcnt(0)
v_mfma_f32_16x16x32_bf16 v[196:199], a[80:83], a[0:3], 0
v_exp_f32 v193, v193
v_mfma_f32_16x16x32_bf16 v[196:199], a[84:87], a[4:7], v[196:199]
v_cvt_pk_bf16_f32 v224, v192, v193
v_mfma_f32_16x16x32_bf16 v[196:199], a[88:91], a[8:11], v[196:199]
v_exp_f32 v194, v194
v_mfma_f32_16x16x32_bf16 v[196:199], a[92:95], a[12:15], v[196:199]
buffer_load_dwordx4 v255, s[60:63], s17 offen offset:1024 sc1 lds
v_mfma_f32_16x16x32_bf16 v[204:207], a[80:83], a[16:19], 0
v_exp_f32 v195, v195
v_mfma_f32_16x16x32_bf16 v[204:207], a[84:87], a[20:23], v[204:207]
v_cvt_pk_bf16_f32 v225, v194, v195
v_mfma_f32_16x16x32_bf16 v[204:207], a[88:91], a[24:27], v[204:207]
v_exp_f32 v200, v200
v_mfma_f32_16x16x32_bf16 v[204:207], a[92:95], a[28:31], v[204:207]
buffer_load_dwordx4 v255, s[60:63], s17 offen offset:2048 sc1 lds
v_mfma_f32_16x16x32_bf16 v[208:211], a[64:67], a[32:35], 0
v_exp_f32 v201, v201
v_mfma_f32_16x16x32_bf16 v[208:211], a[68:71], a[36:39], v[208:211]
v_cvt_pk_bf16_f32 v228, v200, v201
v_mfma_f32_16x16x32_bf16 v[208:211], a[72:75], a[40:43], v[208:211]
v_exp_f32 v202, v202
v_mfma_f32_16x16x32_bf16 v[208:211], a[76:79], a[44:47], v[208:211]
buffer_load_dwordx4 v255, s[60:63], s17 offen offset:3072 sc1 lds
v_mfma_f32_16x16x32_bf16 v[216:219], a[64:67], a[48:51], 0
v_exp_f32 v203, v203
v_mfma_f32_16x16x32_bf16 v[216:219], a[68:71], a[52:55], v[216:219]
v_cvt_pk_bf16_f32 v229, v202, v203
s_mov_b32 s93, 21504
v_mfma_f32_16x16x32_bf16 v[216:219], a[72:75], a[56:59], v[216:219]
v_exp_f32 v196, v196
v_mfma_f32_16x16x32_bf16 v[216:219], a[76:79], a[60:63], v[216:219]
buffer_load_dwordx4 a[140:143], v254, s[40:43], s93 offen
v_mfma_f32_16x16x32_bf16 v[212:215], a[80:83], a[32:35], 0
v_exp_f32 v197, v197
s_add_u32 s17, s17, 8192
v_mfma_f32_16x16x32_bf16 v[212:215], a[84:87], a[36:39], v[212:215]
v_cvt_pk_bf16_f32 v226, v196, v197
s_mov_b32 s93, 28672
s_add_u32 m0, m0, 16384
v_mfma_f32_16x16x32_bf16 v[212:215], a[88:91], a[40:43], v[212:215]
v_exp_f32 v198, v198
s_and_b32 m0, m0, s78
v_mfma_f32_16x16x32_bf16 v[212:215], a[92:95], a[44:47], v[212:215]
buffer_load_dwordx4 a[144:147], v254, s[40:43], s93 offen
v_mfma_f32_16x16x32_bf16 v[220:223], a[80:83], a[48:51], 0
v_exp_f32 v199, v199
v_mfma_f32_16x16x32_bf16 v[220:223], a[84:87], a[52:55], v[220:223]
v_cvt_pk_bf16_f32 v227, v198, v199
v_mfma_f32_16x16x32_bf16 v[220:223], a[88:91], a[56:59], v[220:223]
v_exp_f32 v204, v204
v_mfma_f32_16x16x32_bf16 v[220:223], a[92:95], a[60:63], v[220:223]
s_waitcnt vmcnt(14)
s_barrier
v_mfma_f32_16x16x32_bf16 v[64:67], a[96:99], v[232:235], v[64:67]
v_exp_f32 v205, v205
v_mfma_f32_16x16x32_bf16 v[96:99], a[96:99], v[236:239], v[96:99]
ds_read_b128 a[96:99], v248 offset:8192
v_cvt_pk_bf16_f32 v230, v204, v205
v_mfma_f32_16x16x32_bf16 v[68:71], a[100:103], v[232:235], v[68:71]
v_exp_f32 v206, v206
v_mfma_f32_16x16x32_bf16 v[100:103], a[100:103], v[236:239], v[100:103]
ds_read_b128 a[100:103], v248 offset:9216
v_mfma_f32_16x16x32_bf16 v[72:75], a[104:107], v[232:235], v[72:75]
v_exp_f32 v207, v207
v_mfma_f32_16x16x32_bf16 v[104:107], a[104:107], v[236:239], v[104:107]
ds_read_b128 a[104:107], v248 offset:10240
v_cvt_pk_bf16_f32 v231, v206, v207
s_mov_b32 s93, 35840
v_mfma_f32_16x16x32_bf16 v[76:79], a[108:111], v[232:235], v[76:79]
v_exp_f32 v208, v208
v_mfma_f32_16x16x32_bf16 v[108:111], a[108:111], v[236:239], v[108:111]
ds_read_b128 a[108:111], v248 offset:11264
buffer_load_dwordx4 a[148:151], v254, s[40:43], s93 offen
v_mfma_f32_16x16x32_bf16 v[80:83], a[112:115], v[232:235], v[80:83]
v_exp_f32 v209, v209
v_mfma_f32_16x16x32_bf16 v[112:115], a[112:115], v[236:239], v[112:115]
ds_read_b128 a[112:115], v248 offset:12288
v_accvgpr_read_b32 v242, a231
s_waitcnt vmcnt(14)
v_mfma_f32_16x16x32_bf16 v[84:87], a[116:119], v[232:235], v[84:87]
v_exp_f32 v210, v210
v_mfma_f32_16x16x32_bf16 v[116:119], a[116:119], v[236:239], v[116:119]
ds_read_b128 a[116:119], v248 offset:13312
buffer_store_dwordx2 v[252:253], v242, s[64:67], s6 offen offset:0 sc1
v_mfma_f32_16x16x32_bf16 v[88:91], a[120:123], v[232:235], v[88:91]
v_exp_f32 v211, v211
v_mfma_f32_16x16x32_bf16 v[120:123], a[120:123], v[236:239], v[120:123]
ds_read_b128 a[120:123], v248 offset:14336
v_mfma_f32_16x16x32_bf16 v[92:95], a[124:127], v[232:235], v[92:95]
v_exp_f32 v216, v216
v_mfma_f32_16x16x32_bf16 v[124:127], a[124:127], v[236:239], v[124:127]
ds_read_b128 a[124:127], v248 offset:15360
v_or_b32 v248, s89, v255
v_smfmac_f32_16x16x64_bf16 a[196:199], a[208:211], v[232:239], v241
s_waitcnt lgkmcnt(4)
v_smfmac_f32_16x16x64_bf16 a[192:195], a[208:211], v[224:231], v241
ds_read_b128 a[64:67], v248 offset:0
v_mfma_f32_16x16x32_bf16 v[0:3], a[96:99], v[224:227], v[0:3]
v_exp_f32 v217, v217
v_mfma_f32_16x16x32_bf16 v[32:35], a[96:99], v[228:231], v[32:35]
ds_read_b128 a[68:71], v248 offset:1024
v_cvt_pk_bf16_f32 v232, v208, v209
v_mfma_f32_16x16x32_bf16 v[4:7], a[100:103], v[224:227], v[4:7]
v_exp_f32 v218, v218
v_mfma_f32_16x16x32_bf16 v[36:39], a[100:103], v[228:231], v[36:39]
ds_read_b128 a[72:75], v248 offset:2048
v_cvt_pk_bf16_f32 v233, v210, v211
v_mfma_f32_16x16x32_bf16 v[8:11], a[104:107], v[224:227], v[8:11]
v_exp_f32 v219, v219
v_mfma_f32_16x16x32_bf16 v[40:43], a[104:107], v[228:231], v[40:43]
ds_read_b128 a[76:79], v248 offset:3072
v_cvt_pk_bf16_f32 v236, v216, v217
v_mfma_f32_16x16x32_bf16 v[12:15], a[108:111], v[224:227], v[12:15]
v_exp_f32 v212, v212
v_mfma_f32_16x16x32_bf16 v[44:47], a[108:111], v[228:231], v[44:47]
ds_read_b128 a[80:83], v248 offset:4096
s_waitcnt lgkmcnt(5)
v_mfma_f32_16x16x32_bf16 v[16:19], a[112:115], v[224:227], v[16:19]
v_exp_f32 v213, v213
v_mfma_f32_16x16x32_bf16 v[48:51], a[112:115], v[228:231], v[48:51]
ds_read_b128 a[84:87], v248 offset:5120
v_cvt_pk_bf16_f32 v237, v218, v219
v_mfma_f32_16x16x32_bf16 v[20:23], a[116:119], v[224:227], v[20:23]
v_exp_f32 v214, v214
v_mfma_f32_16x16x32_bf16 v[52:55], a[116:119], v[228:231], v[52:55]
ds_read_b128 a[88:91], v248 offset:6144
v_cvt_pk_bf16_f32 v234, v212, v213
v_mfma_f32_16x16x32_bf16 v[24:27], a[120:123], v[224:227], v[24:27]
v_exp_f32 v215, v215
v_mfma_f32_16x16x32_bf16 v[56:59], a[120:123], v[228:231], v[56:59]
ds_read_b128 a[92:95], v248 offset:7168
v_cvt_pk_bf16_f32 v235, v214, v215
v_mfma_f32_16x16x32_bf16 v[28:31], a[124:127], v[224:227], v[28:31]
v_exp_f32 v220, v220
v_mfma_f32_16x16x32_bf16 v[60:63], a[124:127], v[228:231], v[60:63]
s_waitcnt lgkmcnt(4)
v_mfma_f32_16x16x32_bf16 v[192:195], a[64:67], a[0:3], 0
v_exp_f32 v221, v221
v_mfma_f32_16x16x32_bf16 v[192:195], a[68:71], a[4:7], v[192:195]
v_cvt_pk_bf16_f32 v238, v220, v221
s_add_u32 s89, s89, 16384
v_mfma_f32_16x16x32_bf16 v[192:195], a[72:75], a[8:11], v[192:195]
v_exp_f32 v222, v222
v_mfma_f32_16x16x32_bf16 v[192:195], a[76:79], a[12:15], v[192:195]
buffer_load_dwordx4 v255, s[60:63], s17 offen offset:0 sc1 lds
v_mfma_f32_16x16x32_bf16 v[200:203], a[64:67], a[16:19], 0
v_exp_f32 v223, v223
v_mfma_f32_16x16x32_bf16 v[200:203], a[68:71], a[20:23], v[200:203]
v_cvt_pk_bf16_f32 v239, v222, v223
s_and_b32 s89, s89, 0x1ffff
v_mfma_f32_16x16x32_bf16 v[200:203], a[72:75], a[24:27], v[200:203]
v_exp_f32 v192, v192
v_mfma_f32_16x16x32_bf16 v[200:203], a[76:79], a[28:31], v[200:203]
s_waitcnt lgkmcnt(0)
v_mfma_f32_16x16x32_bf16 v[196:199], a[80:83], a[0:3], 0
v_exp_f32 v193, v193
v_mfma_f32_16x16x32_bf16 v[196:199], a[84:87], a[4:7], v[196:199]
v_cvt_pk_bf16_f32 v224, v192, v193
v_mfma_f32_16x16x32_bf16 v[196:199], a[88:91], a[8:11], v[196:199]
v_exp_f32 v194, v194
v_mfma_f32_16x16x32_bf16 v[196:199], a[92:95], a[12:15], v[196:199]
buffer_load_dwordx4 v255, s[60:63], s17 offen offset:1024 sc1 lds
v_mfma_f32_16x16x32_bf16 v[204:207], a[80:83], a[16:19], 0
v_exp_f32 v195, v195
v_mfma_f32_16x16x32_bf16 v[204:207], a[84:87], a[20:23], v[204:207]
v_cvt_pk_bf16_f32 v225, v194, v195
v_mfma_f32_16x16x32_bf16 v[204:207], a[88:91], a[24:27], v[204:207]
v_exp_f32 v200, v200
v_mfma_f32_16x16x32_bf16 v[204:207], a[92:95], a[28:31], v[204:207]
buffer_load_dwordx4 v255, s[60:63], s17 offen offset:2048 sc1 lds
v_mfma_f32_16x16x32_bf16 v[208:211], a[64:67], a[32:35], 0
v_exp_f32 v201, v201
v_mfma_f32_16x16x32_bf16 v[208:211], a[68:71], a[36:39], v[208:211]
v_cvt_pk_bf16_f32 v228, v200, v201
v_mfma_f32_16x16x32_bf16 v[208:211], a[72:75], a[40:43], v[208:211]
v_exp_f32 v202, v202
v_mfma_f32_16x16x32_bf16 v[208:211], a[76:79], a[44:47], v[208:211]
buffer_load_dwordx4 v255, s[60:63], s17 offen offset:3072 sc1 lds
v_mfma_f32_16x16x32_bf16 v[216:219], a[64:67], a[48:51], 0
v_exp_f32 v203, v203
v_mfma_f32_16x16x32_bf16 v[216:219], a[68:71], a[52:55], v[216:219]
v_cvt_pk_bf16_f32 v229, v202, v203
s_mov_b32 s93, 43008
v_mfma_f32_16x16x32_bf16 v[216:219], a[72:75], a[56:59], v[216:219]
v_exp_f32 v196, v196
v_mfma_f32_16x16x32_bf16 v[216:219], a[76:79], a[60:63], v[216:219]
buffer_load_dwordx4 a[152:155], v254, s[40:43], s93 offen
v_mfma_f32_16x16x32_bf16 v[212:215], a[80:83], a[32:35], 0
v_exp_f32 v197, v197
s_add_u32 s17, s17, 8192
v_mfma_f32_16x16x32_bf16 v[212:215], a[84:87], a[36:39], v[212:215]
v_cvt_pk_bf16_f32 v226, v196, v197
s_mov_b32 s93, 50176
s_add_u32 m0, m0, 16384
v_mfma_f32_16x16x32_bf16 v[212:215], a[88:91], a[40:43], v[212:215]
v_exp_f32 v198, v198
s_and_b32 m0, m0, s78
v_mfma_f32_16x16x32_bf16 v[212:215], a[92:95], a[44:47], v[212:215]
buffer_load_dwordx4 a[156:159], v254, s[40:43], s93 offen
v_mfma_f32_16x16x32_bf16 v[220:223], a[80:83], a[48:51], 0
v_exp_f32 v199, v199
v_mfma_f32_16x16x32_bf16 v[220:223], a[84:87], a[52:55], v[220:223]
v_cvt_pk_bf16_f32 v227, v198, v199
v_mfma_f32_16x16x32_bf16 v[220:223], a[88:91], a[56:59], v[220:223]
v_exp_f32 v204, v204
v_mfma_f32_16x16x32_bf16 v[220:223], a[92:95], a[60:63], v[220:223]
s_waitcnt vmcnt(17)
s_barrier
v_mfma_f32_16x16x32_bf16 v[64:67], a[96:99], v[232:235], v[64:67]
v_exp_f32 v205, v205
v_mfma_f32_16x16x32_bf16 v[96:99], a[96:99], v[236:239], v[96:99]
ds_read_b128 a[96:99], v248 offset:8192
v_cvt_pk_bf16_f32 v230, v204, v205
v_mfma_f32_16x16x32_bf16 v[68:71], a[100:103], v[232:235], v[68:71]
v_exp_f32 v206, v206
v_mfma_f32_16x16x32_bf16 v[100:103], a[100:103], v[236:239], v[100:103]
ds_read_b128 a[100:103], v248 offset:9216
v_mfma_f32_16x16x32_bf16 v[72:75], a[104:107], v[232:235], v[72:75]
v_exp_f32 v207, v207
v_mfma_f32_16x16x32_bf16 v[104:107], a[104:107], v[236:239], v[104:107]
ds_read_b128 a[104:107], v248 offset:10240
v_cvt_pk_bf16_f32 v231, v206, v207
s_mov_b32 s93, 57344
v_mfma_f32_16x16x32_bf16 v[76:79], a[108:111], v[232:235], v[76:79]
v_exp_f32 v208, v208
v_mfma_f32_16x16x32_bf16 v[108:111], a[108:111], v[236:239], v[108:111]
ds_read_b128 a[108:111], v248 offset:11264
buffer_load_dwordx4 a[160:163], v254, s[40:43], s93 offen
v_mfma_f32_16x16x32_bf16 v[80:83], a[112:115], v[232:235], v[80:83]
v_exp_f32 v209, v209
v_mfma_f32_16x16x32_bf16 v[112:115], a[112:115], v[236:239], v[112:115]
ds_read_b128 a[112:115], v248 offset:12288
v_mfma_f32_16x16x32_bf16 v[84:87], a[116:119], v[232:235], v[84:87]
v_exp_f32 v210, v210
v_mfma_f32_16x16x32_bf16 v[116:119], a[116:119], v[236:239], v[116:119]
ds_read_b128 a[116:119], v248 offset:13312
v_mfma_f32_16x16x32_bf16 v[88:91], a[120:123], v[232:235], v[88:91]
v_exp_f32 v211, v211
v_mfma_f32_16x16x32_bf16 v[120:123], a[120:123], v[236:239], v[120:123]
ds_read_b128 a[120:123], v248 offset:14336
v_mfma_f32_16x16x32_bf16 v[92:95], a[124:127], v[232:235], v[92:95]
v_exp_f32 v216, v216
v_mfma_f32_16x16x32_bf16 v[124:127], a[124:127], v[236:239], v[124:127]
ds_read_b128 a[124:127], v248 offset:15360
v_or_b32 v248, s89, v255
v_smfmac_f32_16x16x64_bf16 a[196:199], a[208:211], v[232:239], v241
s_waitcnt lgkmcnt(4)
v_smfmac_f32_16x16x64_bf16 a[192:195], a[208:211], v[224:231], v241
ds_read_b128 a[64:67], v248 offset:0
v_mfma_f32_16x16x32_bf16 v[0:3], a[96:99], v[224:227], v[0:3]
v_exp_f32 v217, v217
v_mfma_f32_16x16x32_bf16 v[32:35], a[96:99], v[228:231], v[32:35]
ds_read_b128 a[68:71], v248 offset:1024
v_cvt_pk_bf16_f32 v232, v208, v209
v_mfma_f32_16x16x32_bf16 v[4:7], a[100:103], v[224:227], v[4:7]
v_exp_f32 v218, v218
v_mfma_f32_16x16x32_bf16 v[36:39], a[100:103], v[228:231], v[36:39]
ds_read_b128 a[72:75], v248 offset:2048
v_cvt_pk_bf16_f32 v233, v210, v211
v_mfma_f32_16x16x32_bf16 v[8:11], a[104:107], v[224:227], v[8:11]
v_exp_f32 v219, v219
v_mfma_f32_16x16x32_bf16 v[40:43], a[104:107], v[228:231], v[40:43]
ds_read_b128 a[76:79], v248 offset:3072
v_cvt_pk_bf16_f32 v236, v216, v217
v_mfma_f32_16x16x32_bf16 v[12:15], a[108:111], v[224:227], v[12:15]
v_exp_f32 v212, v212
v_mfma_f32_16x16x32_bf16 v[44:47], a[108:111], v[228:231], v[44:47]
ds_read_b128 a[80:83], v248 offset:4096
s_waitcnt lgkmcnt(5)
v_mfma_f32_16x16x32_bf16 v[16:19], a[112:115], v[224:227], v[16:19]
v_exp_f32 v213, v213
v_mfma_f32_16x16x32_bf16 v[48:51], a[112:115], v[228:231], v[48:51]
ds_read_b128 a[84:87], v248 offset:5120
v_cvt_pk_bf16_f32 v237, v218, v219
v_mfma_f32_16x16x32_bf16 v[20:23], a[116:119], v[224:227], v[20:23]
v_exp_f32 v214, v214
v_mfma_f32_16x16x32_bf16 v[52:55], a[116:119], v[228:231], v[52:55]
ds_read_b128 a[88:91], v248 offset:6144
v_cvt_pk_bf16_f32 v234, v212, v213
v_mfma_f32_16x16x32_bf16 v[24:27], a[120:123], v[224:227], v[24:27]
v_exp_f32 v215, v215
v_mfma_f32_16x16x32_bf16 v[56:59], a[120:123], v[228:231], v[56:59]
ds_read_b128 a[92:95], v248 offset:7168
v_cvt_pk_bf16_f32 v235, v214, v215
v_mfma_f32_16x16x32_bf16 v[28:31], a[124:127], v[224:227], v[28:31]
v_exp_f32 v220, v220
v_mfma_f32_16x16x32_bf16 v[60:63], a[124:127], v[228:231], v[60:63]
s_waitcnt lgkmcnt(4)
v_mfma_f32_16x16x32_bf16 v[192:195], a[64:67], a[0:3], 0
v_exp_f32 v221, v221
v_mfma_f32_16x16x32_bf16 v[192:195], a[68:71], a[4:7], v[192:195]
v_cvt_pk_bf16_f32 v238, v220, v221
v_accvgpr_read_b32 v240, a228
v_mfma_f32_16x16x32_bf16 v[192:195], a[72:75], a[8:11], v[192:195]
v_exp_f32 v222, v222
v_mfma_f32_16x16x32_bf16 v[192:195], a[76:79], a[12:15], v[192:195]
s_add_u32 m0, s83, 16896
s_lshl_b32 s17, s72, 18
v_mfma_f32_16x16x32_bf16 v[200:203], a[64:67], a[16:19], 0
v_exp_f32 v223, v223
v_mfma_f32_16x16x32_bf16 v[200:203], a[68:71], a[20:23], v[200:203]
v_cvt_pk_bf16_f32 v239, v222, v223
s_add_u32 s89, s89, 16384
v_mfma_f32_16x16x32_bf16 v[200:203], a[72:75], a[24:27], v[200:203]
v_exp_f32 v192, v192
v_mfma_f32_16x16x32_bf16 v[200:203], a[76:79], a[28:31], v[200:203]
s_waitcnt lgkmcnt(0)
s_and_b32 s89, s89, 0x1ffff
v_mfma_f32_16x16x32_bf16 v[196:199], a[80:83], a[0:3], 0
v_exp_f32 v193, v193
v_mfma_f32_16x16x32_bf16 v[196:199], a[84:87], a[4:7], v[196:199]
v_cvt_pk_bf16_f32 v224, v192, v193
v_mfma_f32_16x16x32_bf16 v[196:199], a[88:91], a[8:11], v[196:199]
v_exp_f32 v194, v194
v_mfma_f32_16x16x32_bf16 v[196:199], a[92:95], a[12:15], v[196:199]
buffer_load_dwordx4 v240, s[56:59], s17 offen lds
v_mfma_f32_16x16x32_bf16 v[204:207], a[80:83], a[16:19], 0
v_exp_f32 v195, v195
s_add_u32 s17, s17, 32768
v_mfma_f32_16x16x32_bf16 v[204:207], a[84:87], a[20:23], v[204:207]
v_cvt_pk_bf16_f32 v225, v194, v195
s_add_u32 m0, m0, 1024
v_mfma_f32_16x16x32_bf16 v[204:207], a[88:91], a[24:27], v[204:207]
v_exp_f32 v200, v200
v_mfma_f32_16x16x32_bf16 v[204:207], a[92:95], a[28:31], v[204:207]
buffer_load_dwordx4 v240, s[56:59], s17 offen lds
v_mfma_f32_16x16x32_bf16 v[208:211], a[64:67], a[32:35], 0
v_exp_f32 v201, v201
s_add_u32 s17, s17, 4294942720
v_mfma_f32_16x16x32_bf16 v[208:211], a[68:71], a[36:39], v[208:211]
v_cvt_pk_bf16_f32 v228, v200, v201
s_add_u32 m0, m0, 1152
v_mfma_f32_16x16x32_bf16 v[208:211], a[72:75], a[40:43], v[208:211]
v_exp_f32 v202, v202
v_mfma_f32_16x16x32_bf16 v[208:211], a[76:79], a[44:47], v[208:211]
buffer_load_dwordx4 v240, s[56:59], s17 offen lds
v_mfma_f32_16x16x32_bf16 v[216:219], a[64:67], a[48:51], 0
v_exp_f32 v203, v203
s_add_u32 s17, s17, 32768
v_mfma_f32_16x16x32_bf16 v[216:219], a[68:71], a[52:55], v[216:219]
v_cvt_pk_bf16_f32 v229, v202, v203
s_add_u32 m0, m0, 1024
v_mfma_f32_16x16x32_bf16 v[216:219], a[72:75], a[56:59], v[216:219]
v_exp_f32 v196, v196
v_mfma_f32_16x16x32_bf16 v[216:219], a[76:79], a[60:63], v[216:219]
buffer_load_dwordx4 v240, s[56:59], s17 offen lds
v_mfma_f32_16x16x32_bf16 v[212:215], a[80:83], a[32:35], 0
v_exp_f32 v197, v197
s_add_u32 s17, s17, 90112
v_mfma_f32_16x16x32_bf16 v[212:215], a[84:87], a[36:39], v[212:215]
v_cvt_pk_bf16_f32 v226, v196, v197
s_mov_b32 s93, 64512
s_add_u32 m0, s83, 33792
v_mfma_f32_16x16x32_bf16 v[212:215], a[88:91], a[40:43], v[212:215]
v_exp_f32 v198, v198
v_mfma_f32_16x16x32_bf16 v[212:215], a[92:95], a[44:47], v[212:215]
buffer_load_dwordx4 a[164:167], v254, s[40:43], s93 offen
v_mfma_f32_16x16x32_bf16 v[220:223], a[80:83], a[48:51], 0
v_exp_f32 v199, v199
v_mfma_f32_16x16x32_bf16 v[220:223], a[84:87], a[52:55], v[220:223]
v_cvt_pk_bf16_f32 v227, v198, v199
v_mfma_f32_16x16x32_bf16 v[220:223], a[88:91], a[56:59], v[220:223]
v_exp_f32 v204, v204
v_mfma_f32_16x16x32_bf16 v[220:223], a[92:95], a[60:63], v[220:223]
s_waitcnt vmcnt(16)
s_barrier
v_mfma_f32_16x16x32_bf16 v[64:67], a[96:99], v[232:235], v[64:67]
v_exp_f32 v205, v205
v_mfma_f32_16x16x32_bf16 v[96:99], a[96:99], v[236:239], v[96:99]
ds_read_b128 a[96:99], v248 offset:8192
v_cvt_pk_bf16_f32 v230, v204, v205
v_mfma_f32_16x16x32_bf16 v[68:71], a[100:103], v[232:235], v[68:71]
v_exp_f32 v206, v206
v_mfma_f32_16x16x32_bf16 v[100:103], a[100:103], v[236:239], v[100:103]
ds_read_b128 a[100:103], v248 offset:9216
v_mfma_f32_16x16x32_bf16 v[72:75], a[104:107], v[232:235], v[72:75]
v_exp_f32 v207, v207
v_mfma_f32_16x16x32_bf16 v[104:107], a[104:107], v[236:239], v[104:107]
ds_read_b128 a[104:107], v248 offset:10240
v_cvt_pk_bf16_f32 v231, v206, v207
s_mov_b32 s93, 71680
v_mfma_f32_16x16x32_bf16 v[76:79], a[108:111], v[232:235], v[76:79]
v_exp_f32 v208, v208
v_mfma_f32_16x16x32_bf16 v[108:111], a[108:111], v[236:239], v[108:111]
ds_read_b128 a[108:111], v248 offset:11264
buffer_load_dwordx4 a[168:171], v254, s[40:43], s93 offen
v_mfma_f32_16x16x32_bf16 v[80:83], a[112:115], v[232:235], v[80:83]
v_exp_f32 v209, v209
v_mfma_f32_16x16x32_bf16 v[112:115], a[112:115], v[236:239], v[112:115]
ds_read_b128 a[112:115], v248 offset:12288
s_mov_b32 s93, 78848
v_mfma_f32_16x16x32_bf16 v[84:87], a[116:119], v[232:235], v[84:87]
v_exp_f32 v210, v210
v_mfma_f32_16x16x32_bf16 v[116:119], a[116:119], v[236:239], v[116:119]
ds_read_b128 a[116:119], v248 offset:13312
buffer_load_dwordx4 a[172:175], v254, s[40:43], s93 offen
v_mfma_f32_16x16x32_bf16 v[88:91], a[120:123], v[232:235], v[88:91]
v_exp_f32 v211, v211
v_mfma_f32_16x16x32_bf16 v[120:123], a[120:123], v[236:239], v[120:123]
ds_read_b128 a[120:123], v248 offset:14336
v_mfma_f32_16x16x32_bf16 v[92:95], a[124:127], v[232:235], v[92:95]
v_exp_f32 v216, v216
v_mfma_f32_16x16x32_bf16 v[124:127], a[124:127], v[236:239], v[124:127]
ds_read_b128 a[124:127], v248 offset:15360
v_or_b32 v248, s89, v255
v_smfmac_f32_16x16x64_bf16 a[196:199], a[208:211], v[232:239], v241
s_waitcnt lgkmcnt(4)
v_smfmac_f32_16x16x64_bf16 a[192:195], a[208:211], v[224:231], v241
ds_read_b128 a[64:67], v248 offset:0
v_mfma_f32_16x16x32_bf16 v[0:3], a[96:99], v[224:227], v[0:3]
v_exp_f32 v217, v217
v_mfma_f32_16x16x32_bf16 v[32:35], a[96:99], v[228:231], v[32:35]
ds_read_b128 a[68:71], v248 offset:1024
v_cvt_pk_bf16_f32 v232, v208, v209
v_mfma_f32_16x16x32_bf16 v[4:7], a[100:103], v[224:227], v[4:7]
v_exp_f32 v218, v218
v_mfma_f32_16x16x32_bf16 v[36:39], a[100:103], v[228:231], v[36:39]
ds_read_b128 a[72:75], v248 offset:2048
v_cvt_pk_bf16_f32 v233, v210, v211
v_mfma_f32_16x16x32_bf16 v[8:11], a[104:107], v[224:227], v[8:11]
v_exp_f32 v219, v219
v_mfma_f32_16x16x32_bf16 v[40:43], a[104:107], v[228:231], v[40:43]
ds_read_b128 a[76:79], v248 offset:3072
v_cvt_pk_bf16_f32 v236, v216, v217
v_mfma_f32_16x16x32_bf16 v[12:15], a[108:111], v[224:227], v[12:15]
v_exp_f32 v212, v212
v_mfma_f32_16x16x32_bf16 v[44:47], a[108:111], v[228:231], v[44:47]
ds_read_b128 a[80:83], v248 offset:4096
s_waitcnt lgkmcnt(5)
v_mfma_f32_16x16x32_bf16 v[16:19], a[112:115], v[224:227], v[16:19]
v_exp_f32 v213, v213
v_mfma_f32_16x16x32_bf16 v[48:51], a[112:115], v[228:231], v[48:51]
ds_read_b128 a[84:87], v248 offset:5120
v_cvt_pk_bf16_f32 v237, v218, v219
v_mfma_f32_16x16x32_bf16 v[20:23], a[116:119], v[224:227], v[20:23]
v_exp_f32 v214, v214
v_mfma_f32_16x16x32_bf16 v[52:55], a[116:119], v[228:231], v[52:55]
ds_read_b128 a[88:91], v248 offset:6144
v_cvt_pk_bf16_f32 v234, v212, v213
v_mfma_f32_16x16x32_bf16 v[24:27], a[120:123], v[224:227], v[24:27]
v_exp_f32 v215, v215
v_mfma_f32_16x16x32_bf16 v[56:59], a[120:123], v[228:231], v[56:59]
ds_read_b128 a[92:95], v248 offset:7168
v_cvt_pk_bf16_f32 v235, v214, v215
v_mfma_f32_16x16x32_bf16 v[28:31], a[124:127], v[224:227], v[28:31]
v_exp_f32 v220, v220
v_mfma_f32_16x16x32_bf16 v[60:63], a[124:127], v[228:231], v[60:63]
s_waitcnt lgkmcnt(4)
v_mfma_f32_16x16x32_bf16 v[192:195], a[64:67], a[0:3], 0
v_exp_f32 v221, v221
v_mfma_f32_16x16x32_bf16 v[192:195], a[68:71], a[4:7], v[192:195]
v_cvt_pk_bf16_f32 v238, v220, v221
s_add_u32 s89, s89, 16384
v_mfma_f32_16x16x32_bf16 v[192:195], a[72:75], a[8:11], v[192:195]
v_exp_f32 v222, v222
v_mfma_f32_16x16x32_bf16 v[192:195], a[76:79], a[12:15], v[192:195]
buffer_load_dwordx4 v240, s[56:59], s17 offen lds
v_mfma_f32_16x16x32_bf16 v[200:203], a[64:67], a[16:19], 0
v_exp_f32 v223, v223
s_add_u32 s17, s17, 32768
v_mfma_f32_16x16x32_bf16 v[200:203], a[68:71], a[20:23], v[200:203]
v_cvt_pk_bf16_f32 v239, v222, v223
s_add_u32 m0, m0, 1024
v_mfma_f32_16x16x32_bf16 v[200:203], a[72:75], a[24:27], v[200:203]
v_exp_f32 v192, v192
v_mfma_f32_16x16x32_bf16 v[200:203], a[76:79], a[28:31], v[200:203]
s_waitcnt lgkmcnt(0)
s_and_b32 s89, s89, 0x1ffff
v_mfma_f32_16x16x32_bf16 v[196:199], a[80:83], a[0:3], 0
v_exp_f32 v193, v193
v_mfma_f32_16x16x32_bf16 v[196:199], a[84:87], a[4:7], v[196:199]
v_cvt_pk_bf16_f32 v224, v192, v193
v_mfma_f32_16x16x32_bf16 v[196:199], a[88:91], a[8:11], v[196:199]
v_exp_f32 v194, v194
v_mfma_f32_16x16x32_bf16 v[196:199], a[92:95], a[12:15], v[196:199]
buffer_load_dwordx4 v240, s[56:59], s17 offen lds
v_mfma_f32_16x16x32_bf16 v[204:207], a[80:83], a[16:19], 0
v_exp_f32 v195, v195
s_add_u32 s17, s17, 4294942720
v_mfma_f32_16x16x32_bf16 v[204:207], a[84:87], a[20:23], v[204:207]
v_cvt_pk_bf16_f32 v225, v194, v195
s_add_u32 m0, m0, 1152
v_mfma_f32_16x16x32_bf16 v[204:207], a[88:91], a[24:27], v[204:207]
v_exp_f32 v200, v200
v_mfma_f32_16x16x32_bf16 v[204:207], a[92:95], a[28:31], v[204:207]
buffer_load_dwordx4 v240, s[56:59], s17 offen lds
v_mfma_f32_16x16x32_bf16 v[208:211], a[64:67], a[32:35], 0
v_exp_f32 v201, v201
s_add_u32 s17, s17, 32768
v_mfma_f32_16x16x32_bf16 v[208:211], a[68:71], a[36:39], v[208:211]
v_cvt_pk_bf16_f32 v228, v200, v201
s_add_u32 m0, m0, 1024
v_mfma_f32_16x16x32_bf16 v[208:211], a[72:75], a[40:43], v[208:211]
v_exp_f32 v202, v202
v_mfma_f32_16x16x32_bf16 v[208:211], a[76:79], a[44:47], v[208:211]
buffer_load_dwordx4 v240, s[56:59], s17 offen lds
v_mfma_f32_16x16x32_bf16 v[216:219], a[64:67], a[48:51], 0
v_exp_f32 v203, v203
v_mfma_f32_16x16x32_bf16 v[216:219], a[68:71], a[52:55], v[216:219]
v_cvt_pk_bf16_f32 v229, v202, v203
s_mov_b32 s93, 86016
v_mfma_f32_16x16x32_bf16 v[216:219], a[72:75], a[56:59], v[216:219]
v_exp_f32 v196, v196
v_mfma_f32_16x16x32_bf16 v[216:219], a[76:79], a[60:63], v[216:219]
buffer_load_dwordx4 a[176:179], v254, s[40:43], s93 offen
v_mfma_f32_16x16x32_bf16 v[212:215], a[80:83], a[32:35], 0
v_exp_f32 v197, v197
s_bfe_u32 s17, s4, 262148
v_mfma_f32_16x16x32_bf16 v[212:215], a[84:87], a[36:39], v[212:215]
v_cvt_pk_bf16_f32 v226, v196, v197
.La5_LW2_7:
s_cbranch_execz .La5_LW2_11
s_mov_b32 s93, 93184
s_xor_b32 s17, s17, s72
v_mfma_f32_16x16x32_bf16 v[212:215], a[88:91], a[40:43], v[212:215]
v_exp_f32 v198, v198
s_lshl_b32 s17, s17, 18
v_mfma_f32_16x16x32_bf16 v[212:215], a[92:95], a[44:47], v[212:215]
buffer_load_dwordx4 a[180:183], v254, s[40:43], s93 offen
s_add_u32 m0, s83, 50688
v_mfma_f32_16x16x32_bf16 v[220:223], a[80:83], a[48:51], 0
v_exp_f32 v199, v199
v_mfma_f32_16x16x32_bf16 v[220:223], a[84:87], a[52:55], v[220:223]
v_cvt_pk_bf16_f32 v227, v198, v199
v_mfma_f32_16x16x32_bf16 v[220:223], a[88:91], a[56:59], v[220:223]
v_exp_f32 v204, v204
v_mfma_f32_16x16x32_bf16 v[220:223], a[92:95], a[60:63], v[220:223]
s_waitcnt vmcnt(16)
s_barrier
v_mfma_f32_16x16x32_bf16 v[64:67], a[96:99], v[232:235], v[64:67]
v_exp_f32 v205, v205
v_mfma_f32_16x16x32_bf16 v[96:99], a[96:99], v[236:239], v[96:99]
ds_read_b128 a[96:99], v248 offset:8192
v_cvt_pk_bf16_f32 v230, v204, v205
v_mfma_f32_16x16x32_bf16 v[68:71], a[100:103], v[232:235], v[68:71]
v_exp_f32 v206, v206
v_mfma_f32_16x16x32_bf16 v[100:103], a[100:103], v[236:239], v[100:103]
ds_read_b128 a[100:103], v248 offset:9216
v_mfma_f32_16x16x32_bf16 v[72:75], a[104:107], v[232:235], v[72:75]
v_exp_f32 v207, v207
v_mfma_f32_16x16x32_bf16 v[104:107], a[104:107], v[236:239], v[104:107]
ds_read_b128 a[104:107], v248 offset:10240
v_cvt_pk_bf16_f32 v231, v206, v207
s_mov_b32 s93, 100352
v_mfma_f32_16x16x32_bf16 v[76:79], a[108:111], v[232:235], v[76:79]
v_exp_f32 v208, v208
v_mfma_f32_16x16x32_bf16 v[108:111], a[108:111], v[236:239], v[108:111]
ds_read_b128 a[108:111], v248 offset:11264
buffer_load_dwordx4 a[184:187], v254, s[40:43], s93 offen
v_mfma_f32_16x16x32_bf16 v[80:83], a[112:115], v[232:235], v[80:83]
v_exp_f32 v209, v209
v_mfma_f32_16x16x32_bf16 v[112:115], a[112:115], v[236:239], v[112:115]
ds_read_b128 a[112:115], v248 offset:12288
v_mfma_f32_16x16x32_bf16 v[84:87], a[116:119], v[232:235], v[84:87]
v_exp_f32 v210, v210
v_mfma_f32_16x16x32_bf16 v[116:119], a[116:119], v[236:239], v[116:119]
ds_read_b128 a[116:119], v248 offset:13312
v_mfma_f32_16x16x32_bf16 v[88:91], a[120:123], v[232:235], v[88:91]
v_exp_f32 v211, v211
v_mfma_f32_16x16x32_bf16 v[120:123], a[120:123], v[236:239], v[120:123]
ds_read_b128 a[120:123], v248 offset:14336
v_mfma_f32_16x16x32_bf16 v[92:95], a[124:127], v[232:235], v[92:95]
v_exp_f32 v216, v216
v_mfma_f32_16x16x32_bf16 v[124:127], a[124:127], v[236:239], v[124:127]
ds_read_b128 a[124:127], v248 offset:15360
v_or_b32 v248, s89, v255
v_smfmac_f32_16x16x64_bf16 a[196:199], a[208:211], v[232:239], v241
s_waitcnt lgkmcnt(4)
v_smfmac_f32_16x16x64_bf16 a[192:195], a[208:211], v[224:231], v241
ds_read_b128 a[64:67], v248 offset:0
v_mfma_f32_16x16x32_bf16 v[0:3], a[96:99], v[224:227], v[0:3]
v_exp_f32 v217, v217
v_mfma_f32_16x16x32_bf16 v[32:35], a[96:99], v[228:231], v[32:35]
ds_read_b128 a[68:71], v248 offset:1024
v_cvt_pk_bf16_f32 v232, v208, v209
v_mfma_f32_16x16x32_bf16 v[4:7], a[100:103], v[224:227], v[4:7]
v_exp_f32 v218, v218
v_mfma_f32_16x16x32_bf16 v[36:39], a[100:103], v[228:231], v[36:39]
ds_read_b128 a[72:75], v248 offset:2048
v_cvt_pk_bf16_f32 v233, v210, v211
v_mfma_f32_16x16x32_bf16 v[8:11], a[104:107], v[224:227], v[8:11]
v_exp_f32 v219, v219
v_mfma_f32_16x16x32_bf16 v[40:43], a[104:107], v[228:231], v[40:43]
ds_read_b128 a[76:79], v248 offset:3072
v_cvt_pk_bf16_f32 v236, v216, v217
v_mfma_f32_16x16x32_bf16 v[12:15], a[108:111], v[224:227], v[12:15]
v_exp_f32 v212, v212
v_mfma_f32_16x16x32_bf16 v[44:47], a[108:111], v[228:231], v[44:47]
ds_read_b128 a[80:83], v248 offset:4096
s_waitcnt lgkmcnt(5)
v_mfma_f32_16x16x32_bf16 v[16:19], a[112:115], v[224:227], v[16:19]
v_exp_f32 v213, v213
v_mfma_f32_16x16x32_bf16 v[48:51], a[112:115], v[228:231], v[48:51]
ds_read_b128 a[84:87], v248 offset:5120
v_cvt_pk_bf16_f32 v237, v218, v219
v_mfma_f32_16x16x32_bf16 v[20:23], a[116:119], v[224:227], v[20:23]
v_exp_f32 v214, v214
v_mfma_f32_16x16x32_bf16 v[52:55], a[116:119], v[228:231], v[52:55]
ds_read_b128 a[88:91], v248 offset:6144
v_cvt_pk_bf16_f32 v234, v212, v213
v_mfma_f32_16x16x32_bf16 v[24:27], a[120:123], v[224:227], v[24:27]
v_exp_f32 v215, v215
v_mfma_f32_16x16x32_bf16 v[56:59], a[120:123], v[228:231], v[56:59]
ds_read_b128 a[92:95], v248 offset:7168
v_cvt_pk_bf16_f32 v235, v214, v215
v_mfma_f32_16x16x32_bf16 v[28:31], a[124:127], v[224:227], v[28:31]
v_exp_f32 v220, v220
v_mfma_f32_16x16x32_bf16 v[60:63], a[124:127], v[228:231], v[60:63]
s_waitcnt lgkmcnt(4)
v_mfma_f32_16x16x32_bf16 v[192:195], a[64:67], a[0:3], 0
v_exp_f32 v221, v221
v_mfma_f32_16x16x32_bf16 v[192:195], a[68:71], a[4:7], v[192:195]
v_cvt_pk_bf16_f32 v238, v220, v221
v_mfma_f32_16x16x32_bf16 v[192:195], a[72:75], a[8:11], v[192:195]
v_exp_f32 v222, v222
v_mfma_f32_16x16x32_bf16 v[192:195], a[76:79], a[12:15], v[192:195]
buffer_load_dwordx4 v240, s[56:59], s17 offen lds
v_mfma_f32_16x16x32_bf16 v[200:203], a[64:67], a[16:19], 0
v_exp_f32 v223, v223
s_add_u32 s17, s17, 32768
v_mfma_f32_16x16x32_bf16 v[200:203], a[68:71], a[20:23], v[200:203]
v_cvt_pk_bf16_f32 v239, v222, v223
s_add_u32 m0, m0, 1024
v_mfma_f32_16x16x32_bf16 v[200:203], a[72:75], a[24:27], v[200:203]
v_exp_f32 v192, v192
v_mfma_f32_16x16x32_bf16 v[200:203], a[76:79], a[28:31], v[200:203]
s_waitcnt lgkmcnt(0)
v_mfma_f32_16x16x32_bf16 v[196:199], a[80:83], a[0:3], 0
v_exp_f32 v193, v193
v_mfma_f32_16x16x32_bf16 v[196:199], a[84:87], a[4:7], v[196:199]
v_cvt_pk_bf16_f32 v224, v192, v193
v_mfma_f32_16x16x32_bf16 v[196:199], a[88:91], a[8:11], v[196:199]
v_exp_f32 v194, v194
v_mfma_f32_16x16x32_bf16 v[196:199], a[92:95], a[12:15], v[196:199]
buffer_load_dwordx4 v240, s[56:59], s17 offen lds
v_mfma_f32_16x16x32_bf16 v[204:207], a[80:83], a[16:19], 0
v_exp_f32 v195, v195
s_add_u32 s17, s17, 4294942720
v_mfma_f32_16x16x32_bf16 v[204:207], a[84:87], a[20:23], v[204:207]
v_cvt_pk_bf16_f32 v225, v194, v195
s_add_u32 m0, m0, 1152
v_mfma_f32_16x16x32_bf16 v[204:207], a[88:91], a[24:27], v[204:207]
v_exp_f32 v200, v200
v_mfma_f32_16x16x32_bf16 v[204:207], a[92:95], a[28:31], v[204:207]
buffer_load_dwordx4 v240, s[56:59], s17 offen lds
v_mfma_f32_16x16x32_bf16 v[208:211], a[64:67], a[32:35], 0
v_exp_f32 v201, v201
s_add_u32 s17, s17, 32768
v_mfma_f32_16x16x32_bf16 v[208:211], a[68:71], a[36:39], v[208:211]
v_cvt_pk_bf16_f32 v228, v200, v201
s_add_u32 m0, m0, 1024
v_mfma_f32_16x16x32_bf16 v[208:211], a[72:75], a[40:43], v[208:211]
v_exp_f32 v202, v202
v_mfma_f32_16x16x32_bf16 v[208:211], a[76:79], a[44:47], v[208:211]
buffer_load_dwordx4 v240, s[56:59], s17 offen lds
v_mfma_f32_16x16x32_bf16 v[216:219], a[64:67], a[48:51], 0
v_exp_f32 v203, v203
v_mfma_f32_16x16x32_bf16 v[216:219], a[68:71], a[52:55], v[216:219]
v_cvt_pk_bf16_f32 v229, v202, v203
s_mov_b32 s93, 107520
v_mfma_f32_16x16x32_bf16 v[216:219], a[72:75], a[56:59], v[216:219]
v_exp_f32 v196, v196
v_mfma_f32_16x16x32_bf16 v[216:219], a[76:79], a[60:63], v[216:219]
buffer_load_dwordx4 a[188:191], v254, s[40:43], s93 offen
v_mfma_f32_16x16x32_bf16 v[212:215], a[80:83], a[32:35], 0
v_exp_f32 v197, v197
v_mfma_f32_16x16x32_bf16 v[212:215], a[84:87], a[36:39], v[212:215]
v_cvt_pk_bf16_f32 v226, v196, v197
v_mfma_f32_16x16x32_bf16 v[212:215], a[88:91], a[40:43], v[212:215]
v_exp_f32 v198, v198
v_mfma_f32_16x16x32_bf16 v[212:215], a[92:95], a[44:47], v[212:215]
v_mfma_f32_16x16x32_bf16 v[220:223], a[80:83], a[48:51], 0
v_exp_f32 v199, v199
v_mfma_f32_16x16x32_bf16 v[220:223], a[84:87], a[52:55], v[220:223]
v_cvt_pk_bf16_f32 v227, v198, v199
v_mfma_f32_16x16x32_bf16 v[220:223], a[88:91], a[56:59], v[220:223]
v_exp_f32 v204, v204
v_mfma_f32_16x16x32_bf16 v[220:223], a[92:95], a[60:63], v[220:223]
v_mfma_f32_16x16x32_bf16 v[64:67], a[96:99], v[232:235], v[64:67]
v_exp_f32 v205, v205
v_mfma_f32_16x16x32_bf16 v[96:99], a[96:99], v[236:239], v[96:99]
ds_read_b128 a[96:99], v248 offset:8192
v_cvt_pk_bf16_f32 v230, v204, v205
v_mfma_f32_16x16x32_bf16 v[68:71], a[100:103], v[232:235], v[68:71]
v_exp_f32 v206, v206
v_mfma_f32_16x16x32_bf16 v[100:103], a[100:103], v[236:239], v[100:103]
ds_read_b128 a[100:103], v248 offset:9216
v_mfma_f32_16x16x32_bf16 v[72:75], a[104:107], v[232:235], v[72:75]
v_exp_f32 v207, v207
v_mfma_f32_16x16x32_bf16 v[104:107], a[104:107], v[236:239], v[104:107]
ds_read_b128 a[104:107], v248 offset:10240
v_cvt_pk_bf16_f32 v231, v206, v207
v_mfma_f32_16x16x32_bf16 v[76:79], a[108:111], v[232:235], v[76:79]
v_exp_f32 v208, v208
v_mfma_f32_16x16x32_bf16 v[108:111], a[108:111], v[236:239], v[108:111]
ds_read_b128 a[108:111], v248 offset:11264
v_mfma_f32_16x16x32_bf16 v[80:83], a[112:115], v[232:235], v[80:83]
v_exp_f32 v209, v209
v_mfma_f32_16x16x32_bf16 v[112:115], a[112:115], v[236:239], v[112:115]
ds_read_b128 a[112:115], v248 offset:12288
v_mfma_f32_16x16x32_bf16 v[84:87], a[116:119], v[232:235], v[84:87]
v_exp_f32 v210, v210
v_mfma_f32_16x16x32_bf16 v[116:119], a[116:119], v[236:239], v[116:119]
ds_read_b128 a[116:119], v248 offset:13312
v_mfma_f32_16x16x32_bf16 v[88:91], a[120:123], v[232:235], v[88:91]
v_exp_f32 v211, v211
v_mfma_f32_16x16x32_bf16 v[120:123], a[120:123], v[236:239], v[120:123]
ds_read_b128 a[120:123], v248 offset:14336
v_mfma_f32_16x16x32_bf16 v[92:95], a[124:127], v[232:235], v[92:95]
v_exp_f32 v216, v216
v_mfma_f32_16x16x32_bf16 v[124:127], a[124:127], v[236:239], v[124:127]
ds_read_b128 a[124:127], v248 offset:15360
v_smfmac_f32_16x16x64_bf16 a[196:199], a[208:211], v[232:239], v241
s_waitcnt lgkmcnt(4)
v_smfmac_f32_16x16x64_bf16 a[192:195], a[208:211], v[224:231], v241
v_mfma_f32_16x16x32_bf16 v[0:3], a[96:99], v[224:227], v[0:3]
v_exp_f32 v217, v217
v_mfma_f32_16x16x32_bf16 v[32:35], a[96:99], v[228:231], v[32:35]
v_cvt_pk_bf16_f32 v232, v208, v209
v_mfma_f32_16x16x32_bf16 v[4:7], a[100:103], v[224:227], v[4:7]
v_exp_f32 v218, v218
v_mfma_f32_16x16x32_bf16 v[36:39], a[100:103], v[228:231], v[36:39]
v_cvt_pk_bf16_f32 v233, v210, v211
v_mfma_f32_16x16x32_bf16 v[8:11], a[104:107], v[224:227], v[8:11]
v_exp_f32 v219, v219
v_mfma_f32_16x16x32_bf16 v[40:43], a[104:107], v[228:231], v[40:43]
v_cvt_pk_bf16_f32 v236, v216, v217
v_mfma_f32_16x16x32_bf16 v[12:15], a[108:111], v[224:227], v[12:15]
v_exp_f32 v212, v212
v_mfma_f32_16x16x32_bf16 v[44:47], a[108:111], v[228:231], v[44:47]
s_waitcnt lgkmcnt(0)
v_mfma_f32_16x16x32_bf16 v[16:19], a[112:115], v[224:227], v[16:19]
v_exp_f32 v213, v213
v_mfma_f32_16x16x32_bf16 v[48:51], a[112:115], v[228:231], v[48:51]
v_cvt_pk_bf16_f32 v237, v218, v219
v_mfma_f32_16x16x32_bf16 v[20:23], a[116:119], v[224:227], v[20:23]
v_exp_f32 v214, v214
v_mfma_f32_16x16x32_bf16 v[52:55], a[116:119], v[228:231], v[52:55]
v_cvt_pk_bf16_f32 v234, v212, v213
v_mfma_f32_16x16x32_bf16 v[24:27], a[120:123], v[224:227], v[24:27]
v_exp_f32 v215, v215
v_mfma_f32_16x16x32_bf16 v[56:59], a[120:123], v[228:231], v[56:59]
v_cvt_pk_bf16_f32 v235, v214, v215
v_mfma_f32_16x16x32_bf16 v[28:31], a[124:127], v[224:227], v[28:31]
v_exp_f32 v220, v220
v_mfma_f32_16x16x32_bf16 v[60:63], a[124:127], v[228:231], v[60:63]
; ---- helper: partial O and l go to the partner
.La5_LA_DP:
v_exp_f32 v221, v221
v_exp_f32 v222, v222
v_exp_f32 v223, v223
v_cvt_pk_bf16_f32 v238, v220, v221
s_nop 0
v_cvt_pk_bf16_f32 v239, v222, v223
s_nop 1
v_smfmac_f32_16x16x64_bf16 a[196:199], a[208:211], v[232:239], v241
v_cvt_pk_bf16_f32 v0, v0, v1
v_cvt_pk_bf16_f32 v1, v2, v3
v_cvt_pk_bf16_f32 v2, v4, v5
v_cvt_pk_bf16_f32 v3, v6, v7
buffer_store_dwordx4 v[0:3], v255, s[68:71], 0 offen offset:0 sc1
v_mfma_f32_16x16x32_bf16 v[64:67], a[96:99], v[232:235], v[64:67]
v_cvt_pk_bf16_f32 v8, v8, v9
v_cvt_pk_bf16_f32 v9, v10, v11
v_cvt_pk_bf16_f32 v10, v12, v13
v_cvt_pk_bf16_f32 v11, v14, v15
buffer_store_dwordx4 v[8:11], v255, s[68:71], 0 offen offset:1024 sc1
v_mfma_f32_16x16x32_bf16 v[96:99], a[96:99], v[236:239], v[96:99]
v_accvgpr_read_b32 v240, a224
v_xor_b32 v241, 32, v240
v_cvt_pk_bf16_f32 v16, v16, v17
v_cvt_pk_bf16_f32 v17, v18, v19
v_cvt_pk_bf16_f32 v18, v20, v21
v_cvt_pk_bf16_f32 v19, v22, v23
v_mfma_f32_16x16x32_bf16 v[68:71], a[100:103], v[232:235], v[68:71]
v_xor_b32 v242, 64, v240
v_xor_b32 v243, 96, v240
buffer_store_dwordx4 v[16:19], v255, s[68:71], 0 offen offset:2048 sc1
v_cvt_pk_bf16_f32 v24, v24, v25
v_cvt_pk_bf16_f32 v25, v26, v27
v_mfma_f32_16x16x32_bf16 v[100:103], a[100:103], v[236:239], v[100:103]
v_cvt_pk_bf16_f32 v26, v28, v29
v_cvt_pk_bf16_f32 v27, v30, v31
buffer_store_dwordx4 v[24:27], v255, s[68:71], 0 offen offset:3072 sc1
v_cvt_pk_bf16_f32 v32, v32, v33
v_cvt_pk_bf16_f32 v33, v34, v35
v_mfma_f32_16x16x32_bf16 v[72:75], a[104:107], v[232:235], v[72:75]
v_cvt_pk_bf16_f32 v34, v36, v37
v_cvt_pk_bf16_f32 v35, v38, v39
buffer_store_dwordx4 v[32:35], v255, s[68:71], s95 offen offset:0 sc1
v_cvt_pk_bf16_f32 v40, v40, v41
v_cvt_pk_bf16_f32 v41, v42, v43
v_mfma_f32_16x16x32_bf16 v[104:107], a[104:107], v[236:239], v[104:107]
s_waitcnt vmcnt(13)
s_barrier
v_cvt_pk_bf16_f32 v42, v44, v45
v_cvt_pk_bf16_f32 v43, v46, v47
buffer_store_dwordx4 v[40:43], v255, s[68:71], s95 offen offset:1024 sc1
v_mfma_f32_16x16x32_bf16 v[76:79], a[108:111], v[232:235], v[76:79]
ds_read_b64_tr_b16 v[192:193], v240 offset:16896
ds_read_b64_tr_b16 v[194:195], v240 offset:17920
v_cvt_pk_bf16_f32 v48, v48, v49
v_cvt_pk_bf16_f32 v49, v50, v51
v_cvt_pk_bf16_f32 v50, v52, v53
v_cvt_pk_bf16_f32 v51, v54, v55
v_mfma_f32_16x16x32_bf16 v[108:111], a[108:111], v[236:239], v[108:111]
ds_read_b64_tr_b16 v[196:197], v241 offset:16896
ds_read_b64_tr_b16 v[198:199], v241 offset:17920
buffer_store_dwordx4 v[48:51], v255, s[68:71], s95 offen offset:2048 sc1
v_cvt_pk_bf16_f32 v56, v56, v57
v_cvt_pk_bf16_f32 v57, v58, v59
v_mfma_f32_16x16x32_bf16 v[80:83], a[112:115], v[232:235], v[80:83]
ds_read_b64_tr_b16 v[200:201], v242 offset:16896
ds_read_b64_tr_b16 v[202:203], v242 offset:17920
v_cvt_pk_bf16_f32 v58, v60, v61
v_cvt_pk_bf16_f32 v59, v62, v63
buffer_store_dwordx4 v[56:59], v255, s[68:71], s95 offen offset:3072 sc1
v_mfma_f32_16x16x32_bf16 v[112:115], a[112:115], v[236:239], v[112:115]
ds_read_b64_tr_b16 v[204:205], v243 offset:16896
ds_read_b64_tr_b16 v[206:207], v243 offset:17920
v_mfma_f32_16x16x32_bf16 v[84:87], a[116:119], v[232:235], v[84:87]
ds_read_b64_tr_b16 v[208:209], v240 offset:17024
ds_read_b64_tr_b16 v[210:211], v240 offset:18048
v_mfma_f32_16x16x32_bf16 v[116:119], a[116:119], v[236:239], v[116:119]
ds_read_b64_tr_b16 v[212:213], v241 offset:17024
ds_read_b64_tr_b16 v[214:215], v241 offset:18048
v_mfma_f32_16x16x32_bf16 v[88:91], a[120:123], v[232:235], v[88:91]
ds_read_b64_tr_b16 v[216:217], v242 offset:17024
ds_read_b64_tr_b16 v[218:219], v242 offset:18048
v_mfma_f32_16x16x32_bf16 v[120:123], a[120:123], v[236:239], v[120:123]
ds_read_b64_tr_b16 v[220:221], v243 offset:17024
ds_read_b64_tr_b16 v[222:223], v243 offset:18048
v_mfma_f32_16x16x32_bf16 v[92:95], a[124:127], v[232:235], v[92:95]
v_mfma_f32_16x16x32_bf16 v[124:127], a[124:127], v[236:239], v[124:127]
s_waitcnt lgkmcnt(14)
v_mfma_f32_16x16x32_bf16 v[0:3], v[192:195], v[128:131], 0
s_waitcnt lgkmcnt(12)
v_mfma_f32_16x16x32_bf16 v[32:35], v[192:195], v[144:147], 0
ds_read_b64_tr_b16 v[192:193], v240 offset:25344
ds_read_b64_tr_b16 v[194:195], v240 offset:26368
v_mfma_f32_16x16x32_bf16 v[4:7], v[196:199], v[128:131], 0
s_waitcnt lgkmcnt(12)
v_accvgpr_read_b32 v224, a192
v_accvgpr_read_b32 v228, a193
v_accvgpr_read_b32 v225, a194
v_accvgpr_read_b32 v229, a195
v_accvgpr_read_b32 v226, a196
v_mfma_f32_16x16x32_bf16 v[36:39], v[196:199], v[144:147], 0
ds_read_b64_tr_b16 v[196:197], v241 offset:25344
ds_read_b64_tr_b16 v[198:199], v241 offset:26368
v_accvgpr_read_b32 v230, a197
v_accvgpr_read_b32 v227, a198
v_accvgpr_read_b32 v231, a199
v_add_f32 v224, v224, v228
v_mfma_f32_16x16x32_bf16 v[8:11], v[200:203], v[128:131], 0
s_waitcnt lgkmcnt(12)
v_add_f32 v225, v225, v229
v_add_f32 v226, v226, v230
v_add_f32 v227, v227, v231
s_mov_b32 s92, 16384
buffer_store_dwordx4 v[224:227], v255, s[68:71], s92 offen sc1
v_mfma_f32_16x16x32_bf16 v[40:43], v[200:203], v[144:147], 0
ds_read_b64_tr_b16 v[200:201], v242 offset:25344
ds_read_b64_tr_b16 v[202:203], v242 offset:26368
v_cvt_pk_bf16_f32 v64, v64, v65
v_cvt_pk_bf16_f32 v65, v66, v67
v_cvt_pk_bf16_f32 v66, v68, v69
v_cvt_pk_bf16_f32 v67, v70, v71
v_mfma_f32_16x16x32_bf16 v[12:15], v[204:207], v[128:131], 0
s_waitcnt lgkmcnt(12)
buffer_store_dwordx4 v[64:67], v255, s[68:71], s96 offen offset:0 sc1
v_cvt_pk_bf16_f32 v72, v72, v73
v_cvt_pk_bf16_f32 v73, v74, v75
v_cvt_pk_bf16_f32 v74, v76, v77
v_mfma_f32_16x16x32_bf16 v[44:47], v[204:207], v[144:147], 0
ds_read_b64_tr_b16 v[204:205], v243 offset:25344
ds_read_b64_tr_b16 v[206:207], v243 offset:26368
v_cvt_pk_bf16_f32 v75, v78, v79
buffer_store_dwordx4 v[72:75], v255, s[68:71], s96 offen offset:1024 sc1
v_cvt_pk_bf16_f32 v80, v80, v81
v_mfma_f32_16x16x32_bf16 v[16:19], v[208:211], v[128:131], 0
s_waitcnt lgkmcnt(8)
v_cvt_pk_bf16_f32 v81, v82, v83
v_cvt_pk_bf16_f32 v82, v84, v85
v_cvt_pk_bf16_f32 v83, v86, v87
buffer_store_dwordx4 v[80:83], v255, s[68:71], s96 offen offset:2048 sc1
v_mfma_f32_16x16x32_bf16 v[48:51], v[208:211], v[144:147], 0
ds_read_b64_tr_b16 v[208:209], v240 offset:25472
ds_read_b64_tr_b16 v[210:211], v240 offset:26496
v_cvt_pk_bf16_f32 v88, v88, v89
v_cvt_pk_bf16_f32 v89, v90, v91
v_cvt_pk_bf16_f32 v90, v92, v93
v_cvt_pk_bf16_f32 v91, v94, v95
v_mfma_f32_16x16x32_bf16 v[20:23], v[212:215], v[128:131], 0
buffer_store_dwordx4 v[88:91], v255, s[68:71], s96 offen offset:3072 sc1
v_cvt_pk_bf16_f32 v96, v96, v97
v_cvt_pk_bf16_f32 v97, v98, v99
v_cvt_pk_bf16_f32 v98, v100, v101
v_cvt_pk_bf16_f32 v99, v102, v103
v_mfma_f32_16x16x32_bf16 v[52:55], v[212:215], v[144:147], 0
ds_read_b64_tr_b16 v[212:213], v241 offset:25472
ds_read_b64_tr_b16 v[214:215], v241 offset:26496
buffer_store_dwordx4 v[96:99], v255, s[68:71], s97 offen offset:0 sc1
v_cvt_pk_bf16_f32 v104, v104, v105
v_cvt_pk_bf16_f32 v105, v106, v107
v_mfma_f32_16x16x32_bf16 v[24:27], v[216:219], v[128:131], 0
v_cvt_pk_bf16_f32 v106, v108, v109
v_cvt_pk_bf16_f32 v107, v110, v111
buffer_store_dwordx4 v[104:107], v255, s[68:71], s97 offen offset:1024 sc1
v_cvt_pk_bf16_f32 v112, v112, v113
v_cvt_pk_bf16_f32 v113, v114, v115
v_mfma_f32_16x16x32_bf16 v[56:59], v[216:219], v[144:147], 0
ds_read_b64_tr_b16 v[216:217], v242 offset:25472
ds_read_b64_tr_b16 v[218:219], v242 offset:26496
v_cvt_pk_bf16_f32 v114, v116, v117
v_cvt_pk_bf16_f32 v115, v118, v119
buffer_store_dwordx4 v[112:115], v255, s[68:71], s97 offen offset:2048 sc1
v_mfma_f32_16x16x32_bf16 v[28:31], v[220:223], v[128:131], 0
s_waitcnt lgkmcnt(6)
v_cvt_pk_bf16_f32 v120, v120, v121
v_cvt_pk_bf16_f32 v121, v122, v123
v_cvt_pk_bf16_f32 v122, v124, v125
v_cvt_pk_bf16_f32 v123, v126, v127
buffer_store_dwordx4 v[120:123], v255, s[68:71], s97 offen offset:3072 sc1
v_mfma_f32_16x16x32_bf16 v[60:63], v[220:223], v[144:147], 0
ds_read_b64_tr_b16 v[220:221], v243 offset:25472
ds_read_b64_tr_b16 v[222:223], v243 offset:26496
v_mov_b32 v249, 0
s_add_u32 s6, s88, 393216
v_mfma_f32_16x16x32_bf16 v[0:3], v[192:195], v[132:135], v[0:3]
v_accvgpr_read_b32 v224, a228
v_add_u32 v225, 0x8000, v224
v_add_u32 v226, 0x2000, v224
v_add_u32 v227, 0xa000, v224
v_mfma_f32_16x16x32_bf16 v[32:35], v[192:195], v[148:151], v[32:35]
ds_read_b64_tr_b16 v[192:193], v240 offset:33792
ds_read_b64_tr_b16 v[194:195], v240 offset:34816
v_accvgpr_read_b32 v248, a225
v_accvgpr_read_b32 v228, a229
v_mfma_f32_16x16x32_bf16 v[4:7], v[196:199], v[132:135], v[4:7]
v_accvgpr_read_b32 v229, a230
v_mfma_f32_16x16x32_bf16 v[36:39], v[196:199], v[148:151], v[36:39]
ds_read_b64_tr_b16 v[196:197], v241 offset:33792
ds_read_b64_tr_b16 v[198:199], v241 offset:34816
v_mfma_f32_16x16x32_bf16 v[8:11], v[200:203], v[132:135], v[8:11]
v_mfma_f32_16x16x32_bf16 v[40:43], v[200:203], v[148:151], v[40:43]
ds_read_b64_tr_b16 v[200:201], v242 offset:33792
ds_read_b64_tr_b16 v[202:203], v242 offset:34816
v_mfma_f32_16x16x32_bf16 v[12:15], v[204:207], v[132:135], v[12:15]
s_waitcnt lgkmcnt(6)
v_mfma_f32_16x16x32_bf16 v[44:47], v[204:207], v[148:151], v[44:47]
ds_read_b64_tr_b16 v[204:205], v243 offset:33792
ds_read_b64_tr_b16 v[206:207], v243 offset:34816
v_mfma_f32_16x16x32_bf16 v[16:19], v[208:211], v[132:135], v[16:19]
v_mfma_f32_16x16x32_bf16 v[48:51], v[208:211], v[148:151], v[48:51]
ds_read_b64_tr_b16 v[208:209], v240 offset:33920
ds_read_b64_tr_b16 v[210:211], v240 offset:34944
v_mfma_f32_16x16x32_bf16 v[20:23], v[212:215], v[132:135], v[20:23]
v_mfma_f32_16x16x32_bf16 v[52:55], v[212:215], v[148:151], v[52:55]
ds_read_b64_tr_b16 v[212:213], v241 offset:33920
ds_read_b64_tr_b16 v[214:215], v241 offset:34944
v_mfma_f32_16x16x32_bf16 v[24:27], v[216:219], v[132:135], v[24:27]
v_mfma_f32_16x16x32_bf16 v[56:59], v[216:219], v[148:151], v[56:59]
ds_read_b64_tr_b16 v[216:217], v242 offset:33920
ds_read_b64_tr_b16 v[218:219], v242 offset:34944
v_mfma_f32_16x16x32_bf16 v[28:31], v[220:223], v[132:135], v[28:31]
s_waitcnt lgkmcnt(6)
v_mfma_f32_16x16x32_bf16 v[60:63], v[220:223], v[148:151], v[60:63]
ds_read_b64_tr_b16 v[220:221], v243 offset:33920
ds_read_b64_tr_b16 v[222:223], v243 offset:34944
v_mfma_f32_16x16x32_bf16 v[0:3], v[192:195], v[136:139], v[0:3]
v_mfma_f32_16x16x32_bf16 v[32:35], v[192:195], v[152:155], v[32:35]
ds_read_b64_tr_b16 v[192:193], v240 offset:42240
ds_read_b64_tr_b16 v[194:195], v240 offset:43264
v_mfma_f32_16x16x32_bf16 v[4:7], v[196:199], v[136:139], v[4:7]
v_mfma_f32_16x16x32_bf16 v[36:39], v[196:199], v[152:155], v[36:39]
ds_read_b64_tr_b16 v[196:197], v241 offset:42240
ds_read_b64_tr_b16 v[198:199], v241 offset:43264
v_mfma_f32_16x16x32_bf16 v[8:11], v[200:203], v[136:139], v[8:11]
v_mfma_f32_16x16x32_bf16 v[40:43], v[200:203], v[152:155], v[40:43]
ds_read_b64_tr_b16 v[200:201], v242 offset:42240
ds_read_b64_tr_b16 v[202:203], v242 offset:43264
v_mfma_f32_16x16x32_bf16 v[12:15], v[204:207], v[136:139], v[12:15]
s_waitcnt lgkmcnt(6)
s_waitcnt vmcnt(0)
buffer_store_dwordx2 v[252:253], v249, s[64:67], s6 offen offset:0 sc1
v_mfma_f32_16x16x32_bf16 v[44:47], v[204:207], v[152:155], v[44:47]
ds_read_b64_tr_b16 v[204:205], v243 offset:42240
ds_read_b64_tr_b16 v[206:207], v243 offset:43264
v_mfma_f32_16x16x32_bf16 v[16:19], v[208:211], v[136:139], v[16:19]
s_mov_b32 s93, 114688
buffer_load_dwordx4 a[192:195], v228, s[40:43], s93 offen
v_mfma_f32_16x16x32_bf16 v[48:51], v[208:211], v[152:155], v[48:51]
ds_read_b64_tr_b16 v[208:209], v240 offset:42368
ds_read_b64_tr_b16 v[210:211], v240 offset:43392
v_mfma_f32_16x16x32_bf16 v[20:23], v[212:215], v[136:139], v[20:23]
s_mov_b32 s93, 121856
buffer_load_dwordx4 a[196:199], v228, s[40:43], s93 offen
v_mfma_f32_16x16x32_bf16 v[52:55], v[212:215], v[152:155], v[52:55]
ds_read_b64_tr_b16 v[212:213], v241 offset:42368
ds_read_b64_tr_b16 v[214:215], v241 offset:43392
v_mfma_f32_16x16x32_bf16 v[24:27], v[216:219], v[136:139], v[24:27]
s_mov_b32 s93, 129024
buffer_load_dwordx4 a[200:203], v228, s[40:43], s93 offen
v_mfma_f32_16x16x32_bf16 v[56:59], v[216:219], v[152:155], v[56:59]
ds_read_b64_tr_b16 v[216:217], v242 offset:42368
ds_read_b64_tr_b16 v[218:219], v242 offset:43392
v_mfma_f32_16x16x32_bf16 v[28:31], v[220:223], v[136:139], v[28:31]
s_waitcnt lgkmcnt(6)
s_mov_b32 s93, 136192
buffer_load_dwordx4 a[204:207], v228, s[40:43], s93 offen
v_mfma_f32_16x16x32_bf16 v[60:63], v[220:223], v[152:155], v[60:63]
ds_read_b64_tr_b16 v[220:221], v243 offset:42368
ds_read_b64_tr_b16 v[222:223], v243 offset:43392
v_mfma_f32_16x16x32_bf16 v[0:3], v[192:195], v[140:143], v[0:3]
s_mov_b32 s93, 143360
buffer_load_dwordx4 a[208:211], v228, s[40:43], s93 offen
v_mfma_f32_16x16x32_bf16 v[32:35], v[192:195], v[156:159], v[32:35]
ds_read_b64_tr_b16 v[192:193], v240 offset:16896
ds_read_b64_tr_b16 v[194:195], v240 offset:17920
v_mfma_f32_16x16x32_bf16 v[4:7], v[196:199], v[140:143], v[4:7]
s_mov_b32 s93, 150528
buffer_load_dwordx4 a[212:215], v228, s[40:43], s93 offen
v_mfma_f32_16x16x32_bf16 v[36:39], v[196:199], v[156:159], v[36:39]
ds_read_b64_tr_b16 v[196:197], v241 offset:16896
ds_read_b64_tr_b16 v[198:199], v241 offset:17920
v_mfma_f32_16x16x32_bf16 v[8:11], v[200:203], v[140:143], v[8:11]
s_mov_b32 s93, 157696
buffer_load_dwordx4 a[216:219], v228, s[40:43], s93 offen
v_mfma_f32_16x16x32_bf16 v[40:43], v[200:203], v[156:159], v[40:43]
ds_read_b64_tr_b16 v[200:201], v242 offset:16896
ds_read_b64_tr_b16 v[202:203], v242 offset:17920
v_mfma_f32_16x16x32_bf16 v[12:15], v[204:207], v[140:143], v[12:15]
s_waitcnt lgkmcnt(6)
s_mov_b32 s93, 164864
buffer_load_dwordx4 a[220:223], v228, s[40:43], s93 offen
v_mfma_f32_16x16x32_bf16 v[44:47], v[204:207], v[156:159], v[44:47]
ds_read_b64_tr_b16 v[204:205], v243 offset:16896
ds_read_b64_tr_b16 v[206:207], v243 offset:17920
v_mfma_f32_16x16x32_bf16 v[16:19], v[208:211], v[140:143], v[16:19]
s_mov_b32 s93, 172032
buffer_load_dwordx4 a[224:227], v228, s[40:43], s93 offen
v_mfma_f32_16x16x32_bf16 v[48:51], v[208:211], v[156:159], v[48:51]
ds_read_b64_tr_b16 v[208:209], v240 offset:17024
ds_read_b64_tr_b16 v[210:211], v240 offset:18048
v_mfma_f32_16x16x32_bf16 v[20:23], v[212:215], v[140:143], v[20:23]
s_mov_b32 s93, 179200
buffer_load_dwordx4 a[228:231], v228, s[40:43], s93 offen
v_mfma_f32_16x16x32_bf16 v[52:55], v[212:215], v[156:159], v[52:55]
ds_read_b64_tr_b16 v[212:213], v241 offset:17024
ds_read_b64_tr_b16 v[214:215], v241 offset:18048
v_mfma_f32_16x16x32_bf16 v[24:27], v[216:219], v[140:143], v[24:27]
s_mov_b32 s93, 186368
buffer_load_dwordx4 a[232:235], v228, s[40:43], s93 offen
v_mfma_f32_16x16x32_bf16 v[56:59], v[216:219], v[156:159], v[56:59]
ds_read_b64_tr_b16 v[216:217], v242 offset:17024
ds_read_b64_tr_b16 v[218:219], v242 offset:18048
v_mfma_f32_16x16x32_bf16 v[28:31], v[220:223], v[140:143], v[28:31]
s_waitcnt lgkmcnt(6)
s_mov_b32 s93, 193536
buffer_load_dwordx4 a[236:239], v228, s[40:43], s93 offen
v_mfma_f32_16x16x32_bf16 v[60:63], v[220:223], v[156:159], v[60:63]
ds_read_b64_tr_b16 v[220:221], v243 offset:17024
ds_read_b64_tr_b16 v[222:223], v243 offset:18048
s_mov_b32 s93, 200704
buffer_load_dwordx4 a[240:243], v228, s[40:43], s93 offen
s_mov_b32 s93, 207872
buffer_load_dwordx4 a[244:247], v228, s[40:43], s93 offen
s_mov_b32 s93, 215040
buffer_load_dwordx4 a[248:251], v228, s[40:43], s93 offen
.La5_LW2_8:
s_cbranch_execz .La5_LW2_12
s_mov_b32 s93, 222208
buffer_load_dwordx4 a[252:255], v228, s[40:43], s93 offen
s_branch .La5_LO_START
; ---- owner: last window, partner-s part added, l checked, O / l leaves
.La5_LA_DC:
s_nop 0
s_nop 0
.La5_LW2_9:
s_cbranch_execz .La5_LW2_13
s_waitcnt lgkmcnt(4)
v_mfma_f32_16x16x32_bf16 v[192:195], a[64:67], v[128:131], 0
v_exp_f32 v221, v221
v_mfma_f32_16x16x32_bf16 v[192:195], a[68:71], v[132:135], v[192:195]
v_cvt_pk_bf16_f32 v238, v220, v221
s_add_u32 s89, s89, 16384
v_mfma_f32_16x16x32_bf16 v[192:195], a[72:75], v[136:139], v[192:195]
v_exp_f32 v222, v222
v_mfma_f32_16x16x32_bf16 v[192:195], a[76:79], v[140:143], v[192:195]
buffer_load_dwordx4 v255, s[60:63], s17 offen offset:0 sc1 lds
v_mfma_f32_16x16x32_bf16 v[200:203], a[64:67], v[144:147], 0
v_exp_f32 v223, v223
v_mfma_f32_16x16x32_bf16 v[200:203], a[68:71], v[148:151], v[200:203]
v_cvt_pk_bf16_f32 v239, v222, v223
s_and_b32 s89, s89, 0x1ffff
v_mfma_f32_16x16x32_bf16 v[200:203], a[72:75], v[152:155], v[200:203]
v_exp_f32 v192, v192
v_mfma_f32_16x16x32_bf16 v[200:203], a[76:79], v[156:159], v[200:203]
s_waitcnt lgkmcnt(0)
v_mfma_f32_16x16x32_bf16 v[196:199], a[80:83], v[128:131], 0
v_exp_f32 v193, v193
v_mfma_f32_16x16x32_bf16 v[196:199], a[84:87], v[132:135], v[196:199]
v_cvt_pk_bf16_f32 v224, v192, v193
v_accvgpr_read_b32 v254, a229
v_mfma_f32_16x16x32_bf16 v[196:199], a[88:91], v[136:139], v[196:199]
v_exp_f32 v194, v194
v_mfma_f32_16x16x32_bf16 v[196:199], a[92:95], v[140:143], v[196:199]
buffer_load_dwordx4 v255, s[60:63], s17 offen offset:1024 sc1 lds
v_mfma_f32_16x16x32_bf16 v[204:207], a[80:83], v[144:147], 0
v_exp_f32 v195, v195
v_mfma_f32_16x16x32_bf16 v[204:207], a[84:87], v[148:151], v[204:207]
v_cvt_pk_bf16_f32 v225, v194, v195
v_mfma_f32_16x16x32_bf16 v[204:207], a[88:91], v[152:155], v[204:207]
v_exp_f32 v200, v200
v_mfma_f32_16x16x32_bf16 v[204:207], a[92:95], v[156:159], v[204:207]
buffer_load_dwordx4 v255, s[60:63], s17 offen offset:2048 sc1 lds
v_mfma_f32_16x16x32_bf16 v[208:211], a[64:67], v[160:163], 0
v_exp_f32 v201, v201
v_mfma_f32_16x16x32_bf16 v[208:211], a[68:71], v[164:167], v[208:211]
v_cvt_pk_bf16_f32 v228, v200, v201
v_mfma_f32_16x16x32_bf16 v[208:211], a[72:75], v[168:171], v[208:211]
v_exp_f32 v202, v202
v_mfma_f32_16x16x32_bf16 v[208:211], a[76:79], v[172:175], v[208:211]
buffer_load_dwordx4 v255, s[60:63], s17 offen offset:3072 sc1 lds
v_mfma_f32_16x16x32_bf16 v[216:219], a[64:67], v[176:179], 0
v_exp_f32 v203, v203
v_mfma_f32_16x16x32_bf16 v[216:219], a[68:71], v[180:183], v[216:219]
v_cvt_pk_bf16_f32 v229, v202, v203
s_mov_b32 s93, 0
v_mfma_f32_16x16x32_bf16 v[216:219], a[72:75], v[184:187], v[216:219]
v_exp_f32 v196, v196
v_mfma_f32_16x16x32_bf16 v[216:219], a[76:79], v[188:191], v[216:219]
buffer_load_dwordx4 a[128:131], v254, s[40:43], s93 offen
v_mfma_f32_16x16x32_bf16 v[212:215], a[80:83], v[160:163], 0
v_exp_f32 v197, v197
s_add_u32 s17, s17, 8192
v_mfma_f32_16x16x32_bf16 v[212:215], a[84:87], v[164:167], v[212:215]
v_cvt_pk_bf16_f32 v226, v196, v197
s_mov_b32 s93, 7168
s_add_u32 m0, m0, 16384
v_mfma_f32_16x16x32_bf16 v[212:215], a[88:91], v[168:171], v[212:215]
v_exp_f32 v198, v198
s_and_b32 m0, m0, s78
v_mfma_f32_16x16x32_bf16 v[212:215], a[92:95], v[172:175], v[212:215]
buffer_load_dwordx4 a[132:135], v254, s[40:43], s93 offen
v_mfma_f32_16x16x32_bf16 v[220:223], a[80:83], v[176:179], 0
v_exp_f32 v199, v199
v_mfma_f32_16x16x32_bf16 v[220:223], a[84:87], v[180:183], v[220:223]
v_cvt_pk_bf16_f32 v227, v198, v199
v_mfma_f32_16x16x32_bf16 v[220:223], a[88:91], v[184:187], v[220:223]
v_exp_f32 v204, v204
v_mfma_f32_16x16x32_bf16 v[220:223], a[92:95], v[188:191], v[220:223]
s_waitcnt vmcnt(12)
s_barrier
v_mfma_f32_16x16x32_bf16 v[64:67], a[96:99], v[232:235], v[64:67]
v_exp_f32 v205, v205
v_mfma_f32_16x16x32_bf16 v[96:99], a[96:99], v[236:239], v[96:99]
ds_read_b128 a[96:99], v248 offset:8192
v_cvt_pk_bf16_f32 v230, v204, v205
v_mfma_f32_16x16x32_bf16 v[68:71], a[100:103], v[232:235], v[68:71]
v_exp_f32 v206, v206
v_mfma_f32_16x16x32_bf16 v[100:103], a[100:103], v[236:239], v[100:103]
ds_read_b128 a[100:103], v248 offset:9216
v_mfma_f32_16x16x32_bf16 v[72:75], a[104:107], v[232:235], v[72:75]
v_exp_f32 v207, v207
v_mfma_f32_16x16x32_bf16 v[104:107], a[104:107], v[236:239], v[104:107]
ds_read_b128 a[104:107], v248 offset:10240
v_cvt_pk_bf16_f32 v231, v206, v207
s_mov_b32 s93, 14336
v_mfma_f32_16x16x32_bf16 v[76:79], a[108:111], v[232:235], v[76:79]
v_exp_f32 v208, v208
v_mfma_f32_16x16x32_bf16 v[108:111], a[108:111], v[236:239], v[108:111]
ds_read_b128 a[108:111], v248 offset:11264
buffer_load_dwordx4 a[136:139], v254, s[40:43], s93 offen
v_mfma_f32_16x16x32_bf16 v[80:83], a[112:115], v[232:235], v[80:83]
v_exp_f32 v209, v209
v_mfma_f32_16x16x32_bf16 v[112:115], a[112:115], v[236:239], v[112:115]
ds_read_b128 a[112:115], v248 offset:12288
s_mov_b32 s93, 21504
v_mfma_f32_16x16x32_bf16 v[84:87], a[116:119], v[232:235], v[84:87]
v_exp_f32 v210, v210
v_mfma_f32_16x16x32_bf16 v[116:119], a[116:119], v[236:239], v[116:119]
ds_read_b128 a[116:119], v248 offset:13312
buffer_load_dwordx4 a[140:143], v254, s[40:43], s93 offen
v_mfma_f32_16x16x32_bf16 v[88:91], a[120:123], v[232:235], v[88:91]
v_exp_f32 v211, v211
v_mfma_f32_16x16x32_bf16 v[120:123], a[120:123], v[236:239], v[120:123]
ds_read_b128 a[120:123], v248 offset:14336
v_mfma_f32_16x16x32_bf16 v[92:95], a[124:127], v[232:235], v[92:95]
v_exp_f32 v216, v216
v_mfma_f32_16x16x32_bf16 v[124:127], a[124:127], v[236:239], v[124:127]
ds_read_b128 a[124:127], v248 offset:15360
v_or_b32 v248, s89, v255
v_smfmac_f32_16x16x64_bf16 a[196:199], a[208:211], v[232:239], v241
s_waitcnt lgkmcnt(4)
v_smfmac_f32_16x16x64_bf16 a[192:195], a[208:211], v[224:231], v241
ds_read_b128 a[64:67], v248 offset:0
v_mfma_f32_16x16x32_bf16 v[0:3], a[96:99], v[224:227], v[0:3]
v_exp_f32 v217, v217
v_mfma_f32_16x16x32_bf16 v[32:35], a[96:99], v[228:231], v[32:35]
ds_read_b128 a[68:71], v248 offset:1024
v_cvt_pk_bf16_f32 v232, v208, v209
v_mfma_f32_16x16x32_bf16 v[4:7], a[100:103], v[224:227], v[4:7]
v_exp_f32 v218, v218
v_mfma_f32_16x16x32_bf16 v[36:39], a[100:103], v[228:231], v[36:39]
ds_read_b128 a[72:75], v248 offset:2048
v_cvt_pk_bf16_f32 v233, v210, v211
v_mfma_f32_16x16x32_bf16 v[8:11], a[104:107], v[224:227], v[8:11]
v_exp_f32 v219, v219
v_mfma_f32_16x16x32_bf16 v[40:43], a[104:107], v[228:231], v[40:43]
ds_read_b128 a[76:79], v248 offset:3072
v_cvt_pk_bf16_f32 v236, v216, v217
v_mfma_f32_16x16x32_bf16 v[12:15], a[108:111], v[224:227], v[12:15]
v_exp_f32 v212, v212
v_mfma_f32_16x16x32_bf16 v[44:47], a[108:111], v[228:231], v[44:47]
ds_read_b128 a[80:83], v248 offset:4096
s_waitcnt lgkmcnt(5)
v_mfma_f32_16x16x32_bf16 v[16:19], a[112:115], v[224:227], v[16:19]
v_exp_f32 v213, v213
v_mfma_f32_16x16x32_bf16 v[48:51], a[112:115], v[228:231], v[48:51]
ds_read_b128 a[84:87], v248 offset:5120
v_cvt_pk_bf16_f32 v237, v218, v219
v_mfma_f32_16x16x32_bf16 v[20:23], a[116:119], v[224:227], v[20:23]
v_exp_f32 v214, v214
v_mfma_f32_16x16x32_bf16 v[52:55], a[116:119], v[228:231], v[52:55]
ds_read_b128 a[88:91], v248 offset:6144
v_cvt_pk_bf16_f32 v234, v212, v213
v_mfma_f32_16x16x32_bf16 v[24:27], a[120:123], v[224:227], v[24:27]
v_exp_f32 v215, v215
v_mfma_f32_16x16x32_bf16 v[56:59], a[120:123], v[228:231], v[56:59]
ds_read_b128 a[92:95], v248 offset:7168
v_cvt_pk_bf16_f32 v235, v214, v215
v_mfma_f32_16x16x32_bf16 v[28:31], a[124:127], v[224:227], v[28:31]
v_exp_f32 v220, v220
v_mfma_f32_16x16x32_bf16 v[60:63], a[124:127], v[228:231], v[60:63]
s_waitcnt lgkmcnt(4)
v_mfma_f32_16x16x32_bf16 v[192:195], a[64:67], v[128:131], 0
v_exp_f32 v221, v221
v_mfma_f32_16x16x32_bf16 v[192:195], a[68:71], v[132:135], v[192:195]
v_cvt_pk_bf16_f32 v238, v220, v221
s_add_u32 s89, s89, 16384
v_mfma_f32_16x16x32_bf16 v[192:195], a[72:75], v[136:139], v[192:195]
v_exp_f32 v222, v222
v_mfma_f32_16x16x32_bf16 v[192:195], a[76:79], v[140:143], v[192:195]
buffer_load_dwordx4 v255, s[60:63], s17 offen offset:0 sc1 lds
v_mfma_f32_16x16x32_bf16 v[200:203], a[64:67], v[144:147], 0
v_exp_f32 v223, v223
v_mfma_f32_16x16x32_bf16 v[200:203], a[68:71], v[148:151], v[200:203]
v_cvt_pk_bf16_f32 v239, v222, v223
s_and_b32 s89, s89, 0x1ffff
v_mfma_f32_16x16x32_bf16 v[200:203], a[72:75], v[152:155], v[200:203]
v_exp_f32 v192, v192
v_mfma_f32_16x16x32_bf16 v[200:203], a[76:79], v[156:159], v[200:203]
s_waitcnt lgkmcnt(0)
v_mfma_f32_16x16x32_bf16 v[196:199], a[80:83], v[128:131], 0
v_exp_f32 v193, v193
v_mfma_f32_16x16x32_bf16 v[196:199], a[84:87], v[132:135], v[196:199]
v_cvt_pk_bf16_f32 v224, v192, v193
v_mfma_f32_16x16x32_bf16 v[196:199], a[88:91], v[136:139], v[196:199]
v_exp_f32 v194, v194
v_mfma_f32_16x16x32_bf16 v[196:199], a[92:95], v[140:143], v[196:199]
buffer_load_dwordx4 v255, s[60:63], s17 offen offset:1024 sc1 lds
v_mfma_f32_16x16x32_bf16 v[204:207], a[80:83], v[144:147], 0
v_exp_f32 v195, v195
v_mfma_f32_16x16x32_bf16 v[204:207], a[84:87], v[148:151], v[204:207]
v_cvt_pk_bf16_f32 v225, v194, v195
v_mfma_f32_16x16x32_bf16 v[204:207], a[88:91], v[152:155], v[204:207]
v_exp_f32 v200, v200
v_mfma_f32_16x16x32_bf16 v[204:207], a[92:95], v[156:159], v[204:207]
buffer_load_dwordx4 v255, s[60:63], s17 offen offset:2048 sc1 lds
v_mfma_f32_16x16x32_bf16 v[208:211], a[64:67], v[160:163], 0
v_exp_f32 v201, v201
v_mfma_f32_16x16x32_bf16 v[208:211], a[68:71], v[164:167], v[208:211]
v_cvt_pk_bf16_f32 v228, v200, v201
v_mfma_f32_16x16x32_bf16 v[208:211], a[72:75], v[168:171], v[208:211]
v_exp_f32 v202, v202
v_mfma_f32_16x16x32_bf16 v[208:211], a[76:79], v[172:175], v[208:211]
buffer_load_dwordx4 v255, s[60:63], s17 offen offset:3072 sc1 lds
v_mfma_f32_16x16x32_bf16 v[216:219], a[64:67], v[176:179], 0
v_exp_f32 v203, v203
v_mfma_f32_16x16x32_bf16 v[216:219], a[68:71], v[180:183], v[216:219]
v_cvt_pk_bf16_f32 v229, v202, v203
s_mov_b32 s93, 28672
v_mfma_f32_16x16x32_bf16 v[216:219], a[72:75], v[184:187], v[216:219]
v_exp_f32 v196, v196
v_mfma_f32_16x16x32_bf16 v[216:219], a[76:79], v[188:191], v[216:219]
buffer_load_dwordx4 a[144:147], v254, s[40:43], s93 offen
v_mfma_f32_16x16x32_bf16 v[212:215], a[80:83], v[160:163], 0
v_exp_f32 v197, v197
s_add_u32 s17, s17, 8192
v_mfma_f32_16x16x32_bf16 v[212:215], a[84:87], v[164:167], v[212:215]
v_cvt_pk_bf16_f32 v226, v196, v197
s_mov_b32 s93, 35840
s_add_u32 m0, m0, 16384
v_mfma_f32_16x16x32_bf16 v[212:215], a[88:91], v[168:171], v[212:215]
v_exp_f32 v198, v198
s_and_b32 m0, m0, s78
v_mfma_f32_16x16x32_bf16 v[212:215], a[92:95], v[172:175], v[212:215]
buffer_load_dwordx4 a[148:151], v254, s[40:43], s93 offen
v_mfma_f32_16x16x32_bf16 v[220:223], a[80:83], v[176:179], 0
v_exp_f32 v199, v199
v_mfma_f32_16x16x32_bf16 v[220:223], a[84:87], v[180:183], v[220:223]
v_cvt_pk_bf16_f32 v227, v198, v199
v_mfma_f32_16x16x32_bf16 v[220:223], a[88:91], v[184:187], v[220:223]
v_exp_f32 v204, v204
v_mfma_f32_16x16x32_bf16 v[220:223], a[92:95], v[188:191], v[220:223]
s_waitcnt vmcnt(15)
s_barrier
v_mfma_f32_16x16x32_bf16 v[64:67], a[96:99], v[232:235], v[64:67]
v_exp_f32 v205, v205
v_mfma_f32_16x16x32_bf16 v[96:99], a[96:99], v[236:239], v[96:99]
ds_read_b128 a[96:99], v248 offset:8192
v_cvt_pk_bf16_f32 v230, v204, v205
v_mfma_f32_16x16x32_bf16 v[68:71], a[100:103], v[232:235], v[68:71]
v_exp_f32 v206, v206
v_mfma_f32_16x16x32_bf16 v[100:103], a[100:103], v[236:239], v[100:103]
ds_read_b128 a[100:103], v248 offset:9216
v_mfma_f32_16x16x32_bf16 v[72:75], a[104:107], v[232:235], v[72:75]
v_exp_f32 v207, v207
v_mfma_f32_16x16x32_bf16 v[104:107], a[104:107], v[236:239], v[104:107]
ds_read_b128 a[104:107], v248 offset:10240
v_cvt_pk_bf16_f32 v231, v206, v207
s_mov_b32 s93, 43008
v_mfma_f32_16x16x32_bf16 v[76:79], a[108:111], v[232:235], v[76:79]
v_exp_f32 v208, v208
v_mfma_f32_16x16x32_bf16 v[108:111], a[108:111], v[236:239], v[108:111]
ds_read_b128 a[108:111], v248 offset:11264
buffer_load_dwordx4 a[152:155], v254, s[40:43], s93 offen
v_mfma_f32_16x16x32_bf16 v[80:83], a[112:115], v[232:235], v[80:83]
v_exp_f32 v209, v209
v_mfma_f32_16x16x32_bf16 v[112:115], a[112:115], v[236:239], v[112:115]
ds_read_b128 a[112:115], v248 offset:12288
s_mov_b32 s93, 50176
v_mfma_f32_16x16x32_bf16 v[84:87], a[116:119], v[232:235], v[84:87]
v_exp_f32 v210, v210
v_mfma_f32_16x16x32_bf16 v[116:119], a[116:119], v[236:239], v[116:119]
ds_read_b128 a[116:119], v248 offset:13312
buffer_load_dwordx4 a[156:159], v254, s[40:43], s93 offen
v_mfma_f32_16x16x32_bf16 v[88:91], a[120:123], v[232:235], v[88:91]
v_exp_f32 v211, v211
v_mfma_f32_16x16x32_bf16 v[120:123], a[120:123], v[236:239], v[120:123]
ds_read_b128 a[120:123], v248 offset:14336
v_mfma_f32_16x16x32_bf16 v[92:95], a[124:127], v[232:235], v[92:95]
v_exp_f32 v216, v216
v_mfma_f32_16x16x32_bf16 v[124:127], a[124:127], v[236:239], v[124:127]
ds_read_b128 a[124:127], v248 offset:15360
v_or_b32 v248, s89, v255
v_smfmac_f32_16x16x64_bf16 a[196:199], a[208:211], v[232:239], v241
s_waitcnt lgkmcnt(4)
v_smfmac_f32_16x16x64_bf16 a[192:195], a[208:211], v[224:231], v241
ds_read_b128 a[64:67], v248 offset:0
v_mfma_f32_16x16x32_bf16 v[0:3], a[96:99], v[224:227], v[0:3]
v_exp_f32 v217, v217
v_mfma_f32_16x16x32_bf16 v[32:35], a[96:99], v[228:231], v[32:35]
ds_read_b128 a[68:71], v248 offset:1024
v_cvt_pk_bf16_f32 v232, v208, v209
v_mfma_f32_16x16x32_bf16 v[4:7], a[100:103], v[224:227], v[4:7]
v_exp_f32 v218, v218
v_mfma_f32_16x16x32_bf16 v[36:39], a[100:103], v[228:231], v[36:39]
ds_read_b128 a[72:75], v248 offset:2048
v_cvt_pk_bf16_f32 v233, v210, v211
v_mfma_f32_16x16x32_bf16 v[8:11], a[104:107], v[224:227], v[8:11]
v_exp_f32 v219, v219
v_mfma_f32_16x16x32_bf16 v[40:43], a[104:107], v[228:231], v[40:43]
ds_read_b128 a[76:79], v248 offset:3072
v_cvt_pk_bf16_f32 v236, v216, v217
v_mfma_f32_16x16x32_bf16 v[12:15], a[108:111], v[224:227], v[12:15]
v_exp_f32 v212, v212
v_mfma_f32_16x16x32_bf16 v[44:47], a[108:111], v[228:231], v[44:47]
ds_read_b128 a[80:83], v248 offset:4096
s_waitcnt lgkmcnt(5)
v_mfma_f32_16x16x32_bf16 v[16:19], a[112:115], v[224:227], v[16:19]
v_exp_f32 v213, v213
v_mfma_f32_16x16x32_bf16 v[48:51], a[112:115], v[228:231], v[48:51]
ds_read_b128 a[84:87], v248 offset:5120
v_cvt_pk_bf16_f32 v237, v218, v219
v_mfma_f32_16x16x32_bf16 v[20:23], a[116:119], v[224:227], v[20:23]
v_exp_f32 v214, v214
v_mfma_f32_16x16x32_bf16 v[52:55], a[116:119], v[228:231], v[52:55]
ds_read_b128 a[88:91], v248 offset:6144
v_cvt_pk_bf16_f32 v234, v212, v213
v_mfma_f32_16x16x32_bf16 v[24:27], a[120:123], v[224:227], v[24:27]
v_exp_f32 v215, v215
v_mfma_f32_16x16x32_bf16 v[56:59], a[120:123], v[228:231], v[56:59]
ds_read_b128 a[92:95], v248 offset:7168
v_cvt_pk_bf16_f32 v235, v214, v215
v_mfma_f32_16x16x32_bf16 v[28:31], a[124:127], v[224:227], v[28:31]
v_exp_f32 v220, v220
v_mfma_f32_16x16x32_bf16 v[60:63], a[124:127], v[228:231], v[60:63]
s_waitcnt lgkmcnt(4)
v_mfma_f32_16x16x32_bf16 v[192:195], a[64:67], v[128:131], 0
v_exp_f32 v221, v221
v_mfma_f32_16x16x32_bf16 v[192:195], a[68:71], v[132:135], v[192:195]
v_cvt_pk_bf16_f32 v238, v220, v221
s_add_u32 s89, s89, 16384
v_mfma_f32_16x16x32_bf16 v[192:195], a[72:75], v[136:139], v[192:195]
v_exp_f32 v222, v222
v_mfma_f32_16x16x32_bf16 v[192:195], a[76:79], v[140:143], v[192:195]
buffer_load_dwordx4 v255, s[60:63], s17 offen offset:0 sc1 lds
v_mfma_f32_16x16x32_bf16 v[200:203], a[64:67], v[144:147], 0
v_exp_f32 v223, v223
v_mfma_f32_16x16x32_bf16 v[200:203], a[68:71], v[148:151], v[200:203]
v_cvt_pk_bf16_f32 v239, v222, v223
s_and_b32 s89, s89, 0x1ffff
v_mfma_f32_16x16x32_bf16 v[200:203], a[72:75], v[152:155], v[200:203]
v_exp_f32 v192, v192
v_mfma_f32_16x16x32_bf16 v[200:203], a[76:79], v[156:159], v[200:203]
s_waitcnt lgkmcnt(0)
v_mfma_f32_16x16x32_bf16 v[196:199], a[80:83], v[128:131], 0
v_exp_f32 v193, v193
v_mfma_f32_16x16x32_bf16 v[196:199], a[84:87], v[132:135], v[196:199]
v_cvt_pk_bf16_f32 v224, v192, v193
v_mfma_f32_16x16x32_bf16 v[196:199], a[88:91], v[136:139], v[196:199]
v_exp_f32 v194, v194
v_mfma_f32_16x16x32_bf16 v[196:199], a[92:95], v[140:143], v[196:199]
buffer_load_dwordx4 v255, s[60:63], s17 offen offset:1024 sc1 lds
v_mfma_f32_16x16x32_bf16 v[204:207], a[80:83], v[144:147], 0
v_exp_f32 v195, v195
v_mfma_f32_16x16x32_bf16 v[204:207], a[84:87], v[148:151], v[204:207]
v_cvt_pk_bf16_f32 v225, v194, v195
v_mfma_f32_16x16x32_bf16 v[204:207], a[88:91], v[152:155], v[204:207]
v_exp_f32 v200, v200
v_mfma_f32_16x16x32_bf16 v[204:207], a[92:95], v[156:159], v[204:207]
buffer_load_dwordx4 v255, s[60:63], s17 offen offset:2048 sc1 lds
v_mfma_f32_16x16x32_bf16 v[208:211], a[64:67], v[160:163], 0
v_exp_f32 v201, v201
v_mfma_f32_16x16x32_bf16 v[208:211], a[68:71], v[164:167], v[208:211]
v_cvt_pk_bf16_f32 v228, v200, v201
v_mfma_f32_16x16x32_bf16 v[208:211], a[72:75], v[168:171], v[208:211]
v_exp_f32 v202, v202
v_mfma_f32_16x16x32_bf16 v[208:211], a[76:79], v[172:175], v[208:211]
buffer_load_dwordx4 v255, s[60:63], s17 offen offset:3072 sc1 lds
v_mfma_f32_16x16x32_bf16 v[216:219], a[64:67], v[176:179], 0
v_exp_f32 v203, v203
v_mfma_f32_16x16x32_bf16 v[216:219], a[68:71], v[180:183], v[216:219]
v_cvt_pk_bf16_f32 v229, v202, v203
s_mov_b32 s93, 57344
v_mfma_f32_16x16x32_bf16 v[216:219], a[72:75], v[184:187], v[216:219]
v_exp_f32 v196, v196
v_mfma_f32_16x16x32_bf16 v[216:219], a[76:79], v[188:191], v[216:219]
buffer_load_dwordx4 a[160:163], v254, s[40:43], s93 offen
v_mfma_f32_16x16x32_bf16 v[212:215], a[80:83], v[160:163], 0
v_exp_f32 v197, v197
s_add_u32 s17, s17, 8192
v_mfma_f32_16x16x32_bf16 v[212:215], a[84:87], v[164:167], v[212:215]
v_cvt_pk_bf16_f32 v226, v196, v197
s_mov_b32 s93, 64512
s_add_u32 m0, m0, 16384
v_mfma_f32_16x16x32_bf16 v[212:215], a[88:91], v[168:171], v[212:215]
v_exp_f32 v198, v198
s_and_b32 m0, m0, s78
v_mfma_f32_16x16x32_bf16 v[212:215], a[92:95], v[172:175], v[212:215]
buffer_load_dwordx4 a[164:167], v254, s[40:43], s93 offen
v_mfma_f32_16x16x32_bf16 v[220:223], a[80:83], v[176:179], 0
v_exp_f32 v199, v199
v_mfma_f32_16x16x32_bf16 v[220:223], a[84:87], v[180:183], v[220:223]
v_cvt_pk_bf16_f32 v227, v198, v199
v_mfma_f32_16x16x32_bf16 v[220:223], a[88:91], v[184:187], v[220:223]
v_exp_f32 v204, v204
v_mfma_f32_16x16x32_bf16 v[220:223], a[92:95], v[188:191], v[220:223]
s_waitcnt vmcnt(18)
s_barrier
v_mfma_f32_16x16x32_bf16 v[64:67], a[96:99], v[232:235], v[64:67]
v_exp_f32 v205, v205
v_mfma_f32_16x16x32_bf16 v[96:99], a[96:99], v[236:239], v[96:99]
ds_read_b128 a[96:99], v248 offset:8192
v_cvt_pk_bf16_f32 v230, v204, v205
v_mfma_f32_16x16x32_bf16 v[68:71], a[100:103], v[232:235], v[68:71]
v_exp_f32 v206, v206
v_mfma_f32_16x16x32_bf16 v[100:103], a[100:103], v[236:239], v[100:103]
ds_read_b128 a[100:103], v248 offset:9216
v_mfma_f32_16x16x32_bf16 v[72:75], a[104:107], v[232:235], v[72:75]
v_exp_f32 v207, v207
v_mfma_f32_16x16x32_bf16 v[104:107], a[104:107], v[236:239], v[104:107]
ds_read_b128 a[104:107], v248 offset:10240
v_cvt_pk_bf16_f32 v231, v206, v207
s_mov_b32 s93, 71680
v_mfma_f32_16x16x32_bf16 v[76:79], a[108:111], v[232:235], v[76:79]
v_exp_f32 v208, v208
v_mfma_f32_16x16x32_bf16 v[108:111], a[108:111], v[236:239], v[108:111]
ds_read_b128 a[108:111], v248 offset:11264
buffer_load_dwordx4 a[168:171], v254, s[40:43], s93 offen
v_mfma_f32_16x16x32_bf16 v[80:83], a[112:115], v[232:235], v[80:83]
v_exp_f32 v209, v209
v_mfma_f32_16x16x32_bf16 v[112:115], a[112:115], v[236:239], v[112:115]
ds_read_b128 a[112:115], v248 offset:12288
s_mov_b32 s93, 78848
v_mfma_f32_16x16x32_bf16 v[84:87], a[116:119], v[232:235], v[84:87]
v_exp_f32 v210, v210
v_mfma_f32_16x16x32_bf16 v[116:119], a[116:119], v[236:239], v[116:119]
ds_read_b128 a[116:119], v248 offset:13312
buffer_load_dwordx4 a[172:175], v254, s[40:43], s93 offen
v_mfma_f32_16x16x32_bf16 v[88:91], a[120:123], v[232:235], v[88:91]
v_exp_f32 v211, v211
v_mfma_f32_16x16x32_bf16 v[120:123], a[120:123], v[236:239], v[120:123]
ds_read_b128 a[120:123], v248 offset:14336
v_mfma_f32_16x16x32_bf16 v[92:95], a[124:127], v[232:235], v[92:95]
v_exp_f32 v216, v216
v_mfma_f32_16x16x32_bf16 v[124:127], a[124:127], v[236:239], v[124:127]
ds_read_b128 a[124:127], v248 offset:15360
v_or_b32 v248, s89, v255
v_smfmac_f32_16x16x64_bf16 a[196:199], a[208:211], v[232:239], v241
s_waitcnt lgkmcnt(4)
v_smfmac_f32_16x16x64_bf16 a[192:195], a[208:211], v[224:231], v241
ds_read_b128 a[64:67], v248 offset:0
v_mfma_f32_16x16x32_bf16 v[0:3], a[96:99], v[224:227], v[0:3]
v_exp_f32 v217, v217
v_mfma_f32_16x16x32_bf16 v[32:35], a[96:99], v[228:231], v[32:35]
ds_read_b128 a[68:71], v248 offset:1024
v_cvt_pk_bf16_f32 v232, v208, v209
v_mfma_f32_16x16x32_bf16 v[4:7], a[100:103], v[224:227], v[4:7]
v_exp_f32 v218, v218
v_mfma_f32_16x16x32_bf16 v[36:39], a[100:103], v[228:231], v[36:39]
ds_read_b128 a[72:75], v248 offset:2048
v_cvt_pk_bf16_f32 v233, v210, v211
v_mfma_f32_16x16x32_bf16 v[8:11], a[104:107], v[224:227], v[8:11]
v_exp_f32 v219, v219
v_mfma_f32_16x16x32_bf16 v[40:43], a[104:107], v[228:231], v[40:43]
ds_read_b128 a[76:79], v248 offset:3072
v_cvt_pk_bf16_f32 v236, v216, v217
v_mfma_f32_16x16x32_bf16 v[12:15], a[108:111], v[224:227], v[12:15]
v_exp_f32 v212, v212
v_mfma_f32_16x16x32_bf16 v[44:47], a[108:111], v[228:231], v[44:47]
ds_read_b128 a[80:83], v248 offset:4096
s_waitcnt lgkmcnt(5)
v_mfma_f32_16x16x32_bf16 v[16:19], a[112:115], v[224:227], v[16:19]
v_exp_f32 v213, v213
v_mfma_f32_16x16x32_bf16 v[48:51], a[112:115], v[228:231], v[48:51]
ds_read_b128 a[84:87], v248 offset:5120
v_cvt_pk_bf16_f32 v237, v218, v219
v_mfma_f32_16x16x32_bf16 v[20:23], a[116:119], v[224:227], v[20:23]
v_exp_f32 v214, v214
v_mfma_f32_16x16x32_bf16 v[52:55], a[116:119], v[228:231], v[52:55]
ds_read_b128 a[88:91], v248 offset:6144
v_cvt_pk_bf16_f32 v234, v212, v213
v_mfma_f32_16x16x32_bf16 v[24:27], a[120:123], v[224:227], v[24:27]
v_exp_f32 v215, v215
v_mfma_f32_16x16x32_bf16 v[56:59], a[120:123], v[228:231], v[56:59]
ds_read_b128 a[92:95], v248 offset:7168
v_cvt_pk_bf16_f32 v235, v214, v215
v_mfma_f32_16x16x32_bf16 v[28:31], a[124:127], v[224:227], v[28:31]
v_exp_f32 v220, v220
v_mfma_f32_16x16x32_bf16 v[60:63], a[124:127], v[228:231], v[60:63]
s_waitcnt lgkmcnt(4)
v_mfma_f32_16x16x32_bf16 v[192:195], a[64:67], v[128:131], 0
v_exp_f32 v221, v221
v_mfma_f32_16x16x32_bf16 v[192:195], a[68:71], v[132:135], v[192:195]
v_cvt_pk_bf16_f32 v238, v220, v221
v_accvgpr_read_b32 v240, a228
v_mfma_f32_16x16x32_bf16 v[192:195], a[72:75], v[136:139], v[192:195]
v_exp_f32 v222, v222
v_mfma_f32_16x16x32_bf16 v[192:195], a[76:79], v[140:143], v[192:195]
s_add_u32 m0, s83, 16896
s_lshl_b32 s17, s72, 18
v_mfma_f32_16x16x32_bf16 v[200:203], a[64:67], v[144:147], 0
v_exp_f32 v223, v223
v_mfma_f32_16x16x32_bf16 v[200:203], a[68:71], v[148:151], v[200:203]
v_cvt_pk_bf16_f32 v239, v222, v223
s_add_u32 s89, s89, 16384
v_mfma_f32_16x16x32_bf16 v[200:203], a[72:75], v[152:155], v[200:203]
v_exp_f32 v192, v192
v_mfma_f32_16x16x32_bf16 v[200:203], a[76:79], v[156:159], v[200:203]
s_waitcnt lgkmcnt(0)
s_and_b32 s89, s89, 0x1ffff
v_mfma_f32_16x16x32_bf16 v[196:199], a[80:83], v[128:131], 0
v_exp_f32 v193, v193
v_mfma_f32_16x16x32_bf16 v[196:199], a[84:87], v[132:135], v[196:199]
v_cvt_pk_bf16_f32 v224, v192, v193
v_mfma_f32_16x16x32_bf16 v[196:199], a[88:91], v[136:139], v[196:199]
v_exp_f32 v194, v194
v_mfma_f32_16x16x32_bf16 v[196:199], a[92:95], v[140:143], v[196:199]
buffer_load_dwordx4 v240, s[56:59], s17 offen lds
v_mfma_f32_16x16x32_bf16 v[204:207], a[80:83], v[144:147], 0
v_exp_f32 v195, v195
s_add_u32 s17, s17, 32768
v_mfma_f32_16x16x32_bf16 v[204:207], a[84:87], v[148:151], v[204:207]
v_cvt_pk_bf16_f32 v225, v194, v195
s_add_u32 m0, m0, 1024
v_mfma_f32_16x16x32_bf16 v[204:207], a[88:91], v[152:155], v[204:207]
v_exp_f32 v200, v200
v_mfma_f32_16x16x32_bf16 v[204:207], a[92:95], v[156:159], v[204:207]
buffer_load_dwordx4 v240, s[56:59], s17 offen lds
v_mfma_f32_16x16x32_bf16 v[208:211], a[64:67], v[160:163], 0
v_exp_f32 v201, v201
s_add_u32 s17, s17, 4294942720
v_mfma_f32_16x16x32_bf16 v[208:211], a[68:71], v[164:167], v[208:211]
v_cvt_pk_bf16_f32 v228, v200, v201
s_add_u32 m0, m0, 1152
v_mfma_f32_16x16x32_bf16 v[208:211], a[72:75], v[168:171], v[208:211]
v_exp_f32 v202, v202
v_mfma_f32_16x16x32_bf16 v[208:211], a[76:79], v[172:175], v[208:211]
buffer_load_dwordx4 v240, s[56:59], s17 offen lds
v_mfma_f32_16x16x32_bf16 v[216:219], a[64:67], v[176:179], 0
v_exp_f32 v203, v203
s_add_u32 s17, s17, 32768
v_mfma_f32_16x16x32_bf16 v[216:219], a[68:71], v[180:183], v[216:219]
v_cvt_pk_bf16_f32 v229, v202, v203
s_add_u32 m0, m0, 1024
v_mfma_f32_16x16x32_bf16 v[216:219], a[72:75], v[184:187], v[216:219]
v_exp_f32 v196, v196
v_mfma_f32_16x16x32_bf16 v[216:219], a[76:79], v[188:191], v[216:219]
buffer_load_dwordx4 v240, s[56:59], s17 offen lds
v_mfma_f32_16x16x32_bf16 v[212:215], a[80:83], v[160:163], 0
v_exp_f32 v197, v197
s_add_u32 s17, s17, 90112
v_mfma_f32_16x16x32_bf16 v[212:215], a[84:87], v[164:167], v[212:215]
v_cvt_pk_bf16_f32 v226, v196, v197
s_mov_b32 s93, 86016
s_add_u32 m0, s83, 33792
v_mfma_f32_16x16x32_bf16 v[212:215], a[88:91], v[168:171], v[212:215]
v_exp_f32 v198, v198
v_mfma_f32_16x16x32_bf16 v[212:215], a[92:95], v[172:175], v[212:215]
buffer_load_dwordx4 a[176:179], v254, s[40:43], s93 offen
v_mfma_f32_16x16x32_bf16 v[220:223], a[80:83], v[176:179], 0
v_exp_f32 v199, v199
v_mfma_f32_16x16x32_bf16 v[220:223], a[84:87], v[180:183], v[220:223]
v_cvt_pk_bf16_f32 v227, v198, v199
v_mfma_f32_16x16x32_bf16 v[220:223], a[88:91], v[184:187], v[220:223]
v_exp_f32 v204, v204
v_mfma_f32_16x16x32_bf16 v[220:223], a[92:95], v[188:191], v[220:223]
s_waitcnt vmcnt(17)
s_barrier
v_mfma_f32_16x16x32_bf16 v[64:67], a[96:99], v[232:235], v[64:67]
v_exp_f32 v205, v205
v_mfma_f32_16x16x32_bf16 v[96:99], a[96:99], v[236:239], v[96:99]
ds_read_b128 a[96:99], v248 offset:8192
v_cvt_pk_bf16_f32 v230, v204, v205
v_mfma_f32_16x16x32_bf16 v[68:71], a[100:103], v[232:235], v[68:71]
v_exp_f32 v206, v206
v_mfma_f32_16x16x32_bf16 v[100:103], a[100:103], v[236:239], v[100:103]
ds_read_b128 a[100:103], v248 offset:9216
v_mfma_f32_16x16x32_bf16 v[72:75], a[104:107], v[232:235], v[72:75]
v_exp_f32 v207, v207
v_mfma_f32_16x16x32_bf16 v[104:107], a[104:107], v[236:239], v[104:107]
ds_read_b128 a[104:107], v248 offset:10240
v_cvt_pk_bf16_f32 v231, v206, v207
s_mov_b32 s93, 93184
v_mfma_f32_16x16x32_bf16 v[76:79], a[108:111], v[232:235], v[76:79]
v_exp_f32 v208, v208
v_mfma_f32_16x16x32_bf16 v[108:111], a[108:111], v[236:239], v[108:111]
ds_read_b128 a[108:111], v248 offset:11264
buffer_load_dwordx4 a[180:183], v254, s[40:43], s93 offen
v_mfma_f32_16x16x32_bf16 v[80:83], a[112:115], v[232:235], v[80:83]
v_exp_f32 v209, v209
v_mfma_f32_16x16x32_bf16 v[112:115], a[112:115], v[236:239], v[112:115]
ds_read_b128 a[112:115], v248 offset:12288
s_mov_b32 s93, 100352
v_mfma_f32_16x16x32_bf16 v[84:87], a[116:119], v[232:235], v[84:87]
v_exp_f32 v210, v210
v_mfma_f32_16x16x32_bf16 v[116:119], a[116:119], v[236:239], v[116:119]
ds_read_b128 a[116:119], v248 offset:13312
buffer_load_dwordx4 a[184:187], v254, s[40:43], s93 offen
v_mfma_f32_16x16x32_bf16 v[88:91], a[120:123], v[232:235], v[88:91]
v_exp_f32 v211, v211
v_mfma_f32_16x16x32_bf16 v[120:123], a[120:123], v[236:239], v[120:123]
ds_read_b128 a[120:123], v248 offset:14336
v_mfma_f32_16x16x32_bf16 v[92:95], a[124:127], v[232:235], v[92:95]
v_exp_f32 v216, v216
v_mfma_f32_16x16x32_bf16 v[124:127], a[124:127], v[236:239], v[124:127]
ds_read_b128 a[124:127], v248 offset:15360
v_or_b32 v248, s89, v255
v_smfmac_f32_16x16x64_bf16 a[196:199], a[208:211], v[232:239], v241
s_waitcnt lgkmcnt(4)
v_smfmac_f32_16x16x64_bf16 a[192:195], a[208:211], v[224:231], v241
.La5_LW2_10:
s_cbranch_execz .La5_LW2_R0
ds_read_b128 a[64:67], v248 offset:0
v_mfma_f32_16x16x32_bf16 v[0:3], a[96:99], v[224:227], v[0:3]
v_exp_f32 v217, v217
v_mfma_f32_16x16x32_bf16 v[32:35], a[96:99], v[228:231], v[32:35]
ds_read_b128 a[68:71], v248 offset:1024
v_cvt_pk_bf16_f32 v232, v208, v209
v_mfma_f32_16x16x32_bf16 v[4:7], a[100:103], v[224:227], v[4:7]
v_exp_f32 v218, v218
v_mfma_f32_16x16x32_bf16 v[36:39], a[100:103], v[228:231], v[36:39]
ds_read_b128 a[72:75], v248 offset:2048
v_cvt_pk_bf16_f32 v233, v210, v211
v_mfma_f32_16x16x32_bf16 v[8:11], a[104:107], v[224:227], v[8:11]
v_exp_f32 v219, v219
v_mfma_f32_16x16x32_bf16 v[40:43], a[104:107], v[228:231], v[40:43]
ds_read_b128 a[76:79], v248 offset:3072
v_cvt_pk_bf16_f32 v236, v216, v217
v_mfma_f32_16x16x32_bf16 v[12:15], a[108:111], v[224:227], v[12:15]
v_exp_f32 v212, v212
v_mfma_f32_16x16x32_bf16 v[44:47], a[108:111], v[228:231], v[44:47]
ds_read_b128 a[80:83], v248 offset:4096
s_waitcnt lgkmcnt(5)
v_mfma_f32_16x16x32_bf16 v[16:19], a[112:115], v[224:227], v[16:19]
v_exp_f32 v213, v213
v_mfma_f32_16x16x32_bf16 v[48:51], a[112:115], v[228:231], v[48:51]
ds_read_b128 a[84:87], v248 offset:5120
v_cvt_pk_bf16_f32 v237, v218, v219
v_mfma_f32_16x16x32_bf16 v[20:23], a[116:119], v[224:227], v[20:23]
v_exp_f32 v214, v214
v_mfma_f32_16x16x32_bf16 v[52:55], a[116:119], v[228:231], v[52:55]
ds_read_b128 a[88:91], v248 offset:6144
v_cvt_pk_bf16_f32 v234, v212, v213
v_mfma_f32_16x16x32_bf16 v[24:27], a[120:123], v[224:227], v[24:27]
v_exp_f32 v215, v215
v_mfma_f32_16x16x32_bf16 v[56:59], a[120:123], v[228:231], v[56:59]
ds_read_b128 a[92:95], v248 offset:7168
v_cvt_pk_bf16_f32 v235, v214, v215
v_mfma_f32_16x16x32_bf16 v[28:31], a[124:127], v[224:227], v[28:31]
v_exp_f32 v220, v220
v_mfma_f32_16x16x32_bf16 v[60:63], a[124:127], v[228:231], v[60:63]
s_waitcnt lgkmcnt(4)
v_mfma_f32_16x16x32_bf16 v[192:195], a[64:67], v[128:131], 0
v_exp_f32 v221, v221
v_mfma_f32_16x16x32_bf16 v[192:195], a[68:71], v[132:135], v[192:195]
v_cvt_pk_bf16_f32 v238, v220, v221
s_add_u32 s89, s89, 16384
v_mfma_f32_16x16x32_bf16 v[192:195], a[72:75], v[136:139], v[192:195]
v_exp_f32 v222, v222
v_mfma_f32_16x16x32_bf16 v[192:195], a[76:79], v[140:143], v[192:195]
buffer_load_dwordx4 v240, s[56:59], s17 offen lds
v_mfma_f32_16x16x32_bf16 v[200:203], a[64:67], v[144:147], 0
v_exp_f32 v223, v223
s_add_u32 s17, s17, 32768
v_mfma_f32_16x16x32_bf16 v[200:203], a[68:71], v[148:151], v[200:203]
v_cvt_pk_bf16_f32 v239, v222, v223
s_add_u32 m0, m0, 1024
v_mfma_f32_16x16x32_bf16 v[200:203], a[72:75], v[152:155], v[200:203]
v_exp_f32 v192, v192
v_mfma_f32_16x16x32_bf16 v[200:203], a[76:79], v[156:159], v[200:203]
s_waitcnt lgkmcnt(0)
s_and_b32 s89, s89, 0x1ffff
v_mfma_f32_16x16x32_bf16 v[196:199], a[80:83], v[128:131], 0
v_exp_f32 v193, v193
v_mfma_f32_16x16x32_bf16 v[196:199], a[84:87], v[132:135], v[196:199]
v_cvt_pk_bf16_f32 v224, v192, v193
v_mfma_f32_16x16x32_bf16 v[196:199], a[88:91], v[136:139], v[196:199]
v_exp_f32 v194, v194
v_mfma_f32_16x16x32_bf16 v[196:199], a[92:95], v[140:143], v[196:199]
buffer_load_dwordx4 v240, s[56:59], s17 offen lds
v_mfma_f32_16x16x32_bf16 v[204:207], a[80:83], v[144:147], 0
v_exp_f32 v195, v195
s_add_u32 s17, s17, 4294942720
v_mfma_f32_16x16x32_bf16 v[204:207], a[84:87], v[148:151], v[204:207]
v_cvt_pk_bf16_f32 v225, v194, v195
s_add_u32 m0, m0, 1152
v_mfma_f32_16x16x32_bf16 v[204:207], a[88:91], v[152:155], v[204:207]
v_exp_f32 v200, v200
v_mfma_f32_16x16x32_bf16 v[204:207], a[92:95], v[156:159], v[204:207]
buffer_load_dwordx4 v240, s[56:59], s17 offen lds
v_mfma_f32_16x16x32_bf16 v[208:211], a[64:67], v[160:163], 0
v_exp_f32 v201, v201
s_add_u32 s17, s17, 32768
v_mfma_f32_16x16x32_bf16 v[208:211], a[68:71], v[164:167], v[208:211]
v_cvt_pk_bf16_f32 v228, v200, v201
s_add_u32 m0, m0, 1024
v_mfma_f32_16x16x32_bf16 v[208:211], a[72:75], v[168:171], v[208:211]
v_exp_f32 v202, v202
v_mfma_f32_16x16x32_bf16 v[208:211], a[76:79], v[172:175], v[208:211]
buffer_load_dwordx4 v240, s[56:59], s17 offen lds
v_mfma_f32_16x16x32_bf16 v[216:219], a[64:67], v[176:179], 0
v_exp_f32 v203, v203
v_mfma_f32_16x16x32_bf16 v[216:219], a[68:71], v[180:183], v[216:219]
v_cvt_pk_bf16_f32 v229, v202, v203
s_mov_b32 s93, 107520
v_mfma_f32_16x16x32_bf16 v[216:219], a[72:75], v[184:187], v[216:219]
v_exp_f32 v196, v196
v_mfma_f32_16x16x32_bf16 v[216:219], a[76:79], v[188:191], v[216:219]
buffer_load_dwordx4 a[188:191], v254, s[40:43], s93 offen
v_mfma_f32_16x16x32_bf16 v[212:215], a[80:83], v[160:163], 0
v_exp_f32 v197, v197
s_bfe_u32 s17, s4, 262148
v_mfma_f32_16x16x32_bf16 v[212:215], a[84:87], v[164:167], v[212:215]
v_cvt_pk_bf16_f32 v226, v196, v197
s_xor_b32 s17, s17, s72
s_lshl_b32 s17, s17, 18
v_mfma_f32_16x16x32_bf16 v[212:215], a[88:91], v[168:171], v[212:215]
v_exp_f32 v198, v198
s_add_u32 m0, s83, 50688
v_mfma_f32_16x16x32_bf16 v[212:215], a[92:95], v[172:175], v[212:215]
buffer_load_dword v250, v251, s[64:67], s20 offen sc1
v_mfma_f32_16x16x32_bf16 v[220:223], a[80:83], v[176:179], 0
v_exp_f32 v199, v199
v_mfma_f32_16x16x32_bf16 v[220:223], a[84:87], v[180:183], v[220:223]
v_cvt_pk_bf16_f32 v227, v198, v199
v_mfma_f32_16x16x32_bf16 v[220:223], a[88:91], v[184:187], v[220:223]
v_exp_f32 v204, v204
v_mfma_f32_16x16x32_bf16 v[220:223], a[92:95], v[188:191], v[220:223]
s_waitcnt vmcnt(17)
s_barrier
v_mfma_f32_16x16x32_bf16 v[64:67], a[96:99], v[232:235], v[64:67]
v_exp_f32 v205, v205
v_mfma_f32_16x16x32_bf16 v[96:99], a[96:99], v[236:239], v[96:99]
ds_read_b128 a[96:99], v248 offset:8192
v_cvt_pk_bf16_f32 v230, v204, v205
v_mfma_f32_16x16x32_bf16 v[68:71], a[100:103], v[232:235], v[68:71]
v_exp_f32 v206, v206
v_mfma_f32_16x16x32_bf16 v[100:103], a[100:103], v[236:239], v[100:103]
ds_read_b128 a[100:103], v248 offset:9216
v_mfma_f32_16x16x32_bf16 v[72:75], a[104:107], v[232:235], v[72:75]
v_exp_f32 v207, v207
v_mfma_f32_16x16x32_bf16 v[104:107], a[104:107], v[236:239], v[104:107]
ds_read_b128 a[104:107], v248 offset:10240
v_cvt_pk_bf16_f32 v231, v206, v207
v_mfma_f32_16x16x32_bf16 v[76:79], a[108:111], v[232:235], v[76:79]
v_exp_f32 v208, v208
v_mfma_f32_16x16x32_bf16 v[108:111], a[108:111], v[236:239], v[108:111]
ds_read_b128 a[108:111], v248 offset:11264
v_mfma_f32_16x16x32_bf16 v[80:83], a[112:115], v[232:235], v[80:83]
v_exp_f32 v209, v209
v_mfma_f32_16x16x32_bf16 v[112:115], a[112:115], v[236:239], v[112:115]
ds_read_b128 a[112:115], v248 offset:12288
v_mfma_f32_16x16x32_bf16 v[84:87], a[116:119], v[232:235], v[84:87]
v_exp_f32 v210, v210
v_mfma_f32_16x16x32_bf16 v[116:119], a[116:119], v[236:239], v[116:119]
ds_read_b128 a[116:119], v248 offset:13312
v_mfma_f32_16x16x32_bf16 v[88:91], a[120:123], v[232:235], v[88:91]
v_exp_f32 v211, v211
v_mfma_f32_16x16x32_bf16 v[120:123], a[120:123], v[236:239], v[120:123]
ds_read_b128 a[120:123], v248 offset:14336
v_mfma_f32_16x16x32_bf16 v[92:95], a[124:127], v[232:235], v[92:95]
v_exp_f32 v216, v216
v_mfma_f32_16x16x32_bf16 v[124:127], a[124:127], v[236:239], v[124:127]
ds_read_b128 a[124:127], v248 offset:15360
v_or_b32 v248, s89, v255
v_smfmac_f32_16x16x64_bf16 a[196:199], a[208:211], v[232:239], v241
s_waitcnt lgkmcnt(4)
v_smfmac_f32_16x16x64_bf16 a[192:195], a[208:211], v[224:231], v241
ds_read_b128 a[64:67], v248 offset:0
v_mfma_f32_16x16x32_bf16 v[0:3], a[96:99], v[224:227], v[0:3]
v_exp_f32 v217, v217
v_mfma_f32_16x16x32_bf16 v[32:35], a[96:99], v[228:231], v[32:35]
ds_read_b128 a[68:71], v248 offset:1024
v_cvt_pk_bf16_f32 v232, v208, v209
v_mfma_f32_16x16x32_bf16 v[4:7], a[100:103], v[224:227], v[4:7]
v_exp_f32 v218, v218
v_mfma_f32_16x16x32_bf16 v[36:39], a[100:103], v[228:231], v[36:39]
ds_read_b128 a[72:75], v248 offset:2048
v_cvt_pk_bf16_f32 v233, v210, v211
v_mfma_f32_16x16x32_bf16 v[8:11], a[104:107], v[224:227], v[8:11]
v_exp_f32 v219, v219
v_mfma_f32_16x16x32_bf16 v[40:43], a[104:107], v[228:231], v[40:43]
ds_read_b128 a[76:79], v248 offset:3072
v_cvt_pk_bf16_f32 v236, v216, v217
v_mfma_f32_16x16x32_bf16 v[12:15], a[108:111], v[224:227], v[12:15]
v_exp_f32 v212, v212
v_mfma_f32_16x16x32_bf16 v[44:47], a[108:111], v[228:231], v[44:47]
ds_read_b128 a[80:83], v248 offset:4096
s_waitcnt lgkmcnt(5)
v_mfma_f32_16x16x32_bf16 v[16:19], a[112:115], v[224:227], v[16:19]
v_exp_f32 v213, v213
v_mfma_f32_16x16x32_bf16 v[48:51], a[112:115], v[228:231], v[48:51]
ds_read_b128 a[84:87], v248 offset:5120
v_cvt_pk_bf16_f32 v237, v218, v219
v_mfma_f32_16x16x32_bf16 v[20:23], a[116:119], v[224:227], v[20:23]
v_exp_f32 v214, v214
v_mfma_f32_16x16x32_bf16 v[52:55], a[116:119], v[228:231], v[52:55]
ds_read_b128 a[88:91], v248 offset:6144
v_cvt_pk_bf16_f32 v234, v212, v213
v_mfma_f32_16x16x32_bf16 v[24:27], a[120:123], v[224:227], v[24:27]
v_exp_f32 v215, v215
v_mfma_f32_16x16x32_bf16 v[56:59], a[120:123], v[228:231], v[56:59]
ds_read_b128 a[92:95], v248 offset:7168
v_cvt_pk_bf16_f32 v235, v214, v215
v_mfma_f32_16x16x32_bf16 v[28:31], a[124:127], v[224:227], v[28:31]
v_exp_f32 v220, v220
v_mfma_f32_16x16x32_bf16 v[60:63], a[124:127], v[228:231], v[60:63]
s_waitcnt vmcnt(0)
v_cmp_eq_u32 s[92:93], v250, v249
s_nop 1
s_and_b32 s92, s92, 3
s_mov_b32 s94, 16384
s_andn2_b32 s92, s92, s23
s_cmp_eq_u32 s92, 3
s_cselect_b32 s70, 17408, 0
s_cselect_b32 s84, 3, s23
s_waitcnt lgkmcnt(4)
v_mfma_f32_16x16x32_bf16 v[192:195], a[64:67], v[128:131], 0
v_exp_f32 v221, v221
v_mfma_f32_16x16x32_bf16 v[192:195], a[68:71], v[132:135], v[192:195]
v_cvt_pk_bf16_f32 v238, v220, v221
buffer_load_dwordx4 a[220:223], v255, s[68:71], s94 offen sc1
v_mfma_f32_16x16x32_bf16 v[192:195], a[72:75], v[136:139], v[192:195]
v_exp_f32 v222, v222
v_mfma_f32_16x16x32_bf16 v[192:195], a[76:79], v[140:143], v[192:195]
buffer_load_dwordx4 v240, s[56:59], s17 offen lds
v_mfma_f32_16x16x32_bf16 v[200:203], a[64:67], v[144:147], 0
v_exp_f32 v223, v223
s_add_u32 s17, s17, 32768
v_mfma_f32_16x16x32_bf16 v[200:203], a[68:71], v[148:151], v[200:203]
v_cvt_pk_bf16_f32 v239, v222, v223
s_add_u32 m0, m0, 1024
buffer_load_dword v250, v251, s[64:67], s20 offen sc1
v_mfma_f32_16x16x32_bf16 v[200:203], a[72:75], v[152:155], v[200:203]
v_exp_f32 v192, v192
v_mfma_f32_16x16x32_bf16 v[200:203], a[76:79], v[156:159], v[200:203]
s_waitcnt lgkmcnt(0)
buffer_load_dwordx4 a[0:3], v255, s[68:71], 0 offen offset:0 sc1
v_mfma_f32_16x16x32_bf16 v[196:199], a[80:83], v[128:131], 0
v_exp_f32 v193, v193
v_mfma_f32_16x16x32_bf16 v[196:199], a[84:87], v[132:135], v[196:199]
v_cvt_pk_bf16_f32 v224, v192, v193
buffer_load_dwordx4 a[4:7], v255, s[68:71], 0 offen offset:1024 sc1
v_mfma_f32_16x16x32_bf16 v[196:199], a[88:91], v[136:139], v[196:199]
v_exp_f32 v194, v194
v_mfma_f32_16x16x32_bf16 v[196:199], a[92:95], v[140:143], v[196:199]
buffer_load_dwordx4 v240, s[56:59], s17 offen lds
v_mfma_f32_16x16x32_bf16 v[204:207], a[80:83], v[144:147], 0
v_exp_f32 v195, v195
s_add_u32 s17, s17, 4294942720
v_mfma_f32_16x16x32_bf16 v[204:207], a[84:87], v[148:151], v[204:207]
v_cvt_pk_bf16_f32 v225, v194, v195
s_add_u32 m0, m0, 1152
buffer_load_dwordx4 a[8:11], v255, s[68:71], 0 offen offset:2048 sc1
v_mfma_f32_16x16x32_bf16 v[204:207], a[88:91], v[152:155], v[204:207]
v_exp_f32 v200, v200
v_mfma_f32_16x16x32_bf16 v[204:207], a[92:95], v[156:159], v[204:207]
buffer_load_dwordx4 v240, s[56:59], s17 offen lds
v_mfma_f32_16x16x32_bf16 v[208:211], a[64:67], v[160:163], 0
v_exp_f32 v201, v201
s_add_u32 s17, s17, 32768
v_mfma_f32_16x16x32_bf16 v[208:211], a[68:71], v[164:167], v[208:211]
v_cvt_pk_bf16_f32 v228, v200, v201
s_add_u32 m0, m0, 1024
buffer_load_dwordx4 a[12:15], v255, s[68:71], 0 offen offset:3072 sc1
v_mfma_f32_16x16x32_bf16 v[208:211], a[72:75], v[168:171], v[208:211]
v_exp_f32 v202, v202
v_mfma_f32_16x16x32_bf16 v[208:211], a[76:79], v[172:175], v[208:211]
buffer_load_dwordx4 v240, s[56:59], s17 offen lds
v_mfma_f32_16x16x32_bf16 v[216:219], a[64:67], v[176:179], 0
v_exp_f32 v203, v203
v_mfma_f32_16x16x32_bf16 v[216:219], a[68:71], v[180:183], v[216:219]
v_cvt_pk_bf16_f32 v229, v202, v203
buffer_load_dwordx4 a[16:19], v255, s[68:71], s95 offen offset:0 sc1
v_mfma_f32_16x16x32_bf16 v[216:219], a[72:75], v[184:187], v[216:219]
v_exp_f32 v196, v196
v_mfma_f32_16x16x32_bf16 v[216:219], a[76:79], v[188:191], v[216:219]
buffer_load_dwordx4 a[20:23], v255, s[68:71], s95 offen offset:1024 sc1
v_mfma_f32_16x16x32_bf16 v[212:215], a[80:83], v[160:163], 0
v_exp_f32 v197, v197
v_mfma_f32_16x16x32_bf16 v[212:215], a[84:87], v[164:167], v[212:215]
v_cvt_pk_bf16_f32 v226, v196, v197
v_mfma_f32_16x16x32_bf16 v[212:215], a[88:91], v[168:171], v[212:215]
v_exp_f32 v198, v198
v_mfma_f32_16x16x32_bf16 v[212:215], a[92:95], v[172:175], v[212:215]
buffer_load_dwordx4 a[24:27], v255, s[68:71], s95 offen offset:2048 sc1
v_mfma_f32_16x16x32_bf16 v[220:223], a[80:83], v[176:179], 0
v_exp_f32 v199, v199
v_mfma_f32_16x16x32_bf16 v[220:223], a[84:87], v[180:183], v[220:223]
v_cvt_pk_bf16_f32 v227, v198, v199
buffer_load_dwordx4 a[28:31], v255, s[68:71], s95 offen offset:3072 sc1
v_mfma_f32_16x16x32_bf16 v[220:223], a[88:91], v[184:187], v[220:223]
v_exp_f32 v204, v204
v_mfma_f32_16x16x32_bf16 v[220:223], a[92:95], v[188:191], v[220:223]
buffer_load_dwordx4 a[32:35], v255, s[68:71], s96 offen offset:0 sc1
v_mfma_f32_16x16x32_bf16 v[64:67], a[96:99], v[232:235], v[64:67]
v_exp_f32 v205, v205
v_mfma_f32_16x16x32_bf16 v[96:99], a[96:99], v[236:239], v[96:99]
ds_read_b128 a[96:99], v248 offset:8192
v_cvt_pk_bf16_f32 v230, v204, v205
v_mfma_f32_16x16x32_bf16 v[68:71], a[100:103], v[232:235], v[68:71]
v_exp_f32 v206, v206
v_mfma_f32_16x16x32_bf16 v[100:103], a[100:103], v[236:239], v[100:103]
ds_read_b128 a[100:103], v248 offset:9216
buffer_load_dwordx4 a[36:39], v255, s[68:71], s96 offen offset:1024 sc1
v_mfma_f32_16x16x32_bf16 v[72:75], a[104:107], v[232:235], v[72:75]
v_exp_f32 v207, v207
v_mfma_f32_16x16x32_bf16 v[104:107], a[104:107], v[236:239], v[104:107]
ds_read_b128 a[104:107], v248 offset:10240
v_cvt_pk_bf16_f32 v231, v206, v207
v_mfma_f32_16x16x32_bf16 v[76:79], a[108:111], v[232:235], v[76:79]
v_exp_f32 v208, v208
v_mfma_f32_16x16x32_bf16 v[108:111], a[108:111], v[236:239], v[108:111]
ds_read_b128 a[108:111], v248 offset:11264
buffer_load_dwordx4 a[40:43], v255, s[68:71], s96 offen offset:2048 sc1
v_mfma_f32_16x16x32_bf16 v[80:83], a[112:115], v[232:235], v[80:83]
v_exp_f32 v209, v209
v_mfma_f32_16x16x32_bf16 v[112:115], a[112:115], v[236:239], v[112:115]
ds_read_b128 a[112:115], v248 offset:12288
buffer_load_dwordx4 a[44:47], v255, s[68:71], s96 offen offset:3072 sc1
v_mfma_f32_16x16x32_bf16 v[84:87], a[116:119], v[232:235], v[84:87]
v_exp_f32 v210, v210
v_mfma_f32_16x16x32_bf16 v[116:119], a[116:119], v[236:239], v[116:119]
ds_read_b128 a[116:119], v248 offset:13312
buffer_load_dwordx4 a[48:51], v255, s[68:71], s97 offen offset:0 sc1
v_mfma_f32_16x16x32_bf16 v[88:91], a[120:123], v[232:235], v[88:91]
v_exp_f32 v211, v211
v_mfma_f32_16x16x32_bf16 v[120:123], a[120:123], v[236:239], v[120:123]
ds_read_b128 a[120:123], v248 offset:14336
buffer_load_dwordx4 a[52:55], v255, s[68:71], s97 offen offset:1024 sc1
v_mfma_f32_16x16x32_bf16 v[92:95], a[124:127], v[232:235], v[92:95]
v_exp_f32 v216, v216
v_mfma_f32_16x16x32_bf16 v[124:127], a[124:127], v[236:239], v[124:127]
ds_read_b128 a[124:127], v248 offset:15360
buffer_load_dwordx4 a[56:59], v255, s[68:71], s97 offen offset:2048 sc1
v_smfmac_f32_16x16x64_bf16 a[196:199], a[208:211], v[232:239], v241
s_waitcnt lgkmcnt(4)
v_smfmac_f32_16x16x64_bf16 a[192:195], a[208:211], v[224:231], v241
buffer_load_dwordx4 a[60:63], v255, s[68:71], s97 offen offset:3072 sc1
v_mfma_f32_16x16x32_bf16 v[0:3], a[96:99], v[224:227], v[0:3]
v_exp_f32 v217, v217
v_mfma_f32_16x16x32_bf16 v[32:35], a[96:99], v[228:231], v[32:35]
v_cvt_pk_bf16_f32 v232, v208, v209
v_mfma_f32_16x16x32_bf16 v[4:7], a[100:103], v[224:227], v[4:7]
v_exp_f32 v218, v218
v_mfma_f32_16x16x32_bf16 v[36:39], a[100:103], v[228:231], v[36:39]
v_cvt_pk_bf16_f32 v233, v210, v211
v_mfma_f32_16x16x32_bf16 v[8:11], a[104:107], v[224:227], v[8:11]
v_exp_f32 v219, v219
v_mfma_f32_16x16x32_bf16 v[40:43], a[104:107], v[228:231], v[40:43]
v_cvt_pk_bf16_f32 v236, v216, v217
v_mfma_f32_16x16x32_bf16 v[12:15], a[108:111], v[224:227], v[12:15]
v_exp_f32 v212, v212
s_waitcnt vmcnt(19)
v_cmp_eq_u32 s[92:93], v250, v249
v_mfma_f32_16x16x32_bf16 v[44:47], a[108:111], v[228:231], v[44:47]
s_waitcnt lgkmcnt(0)
v_mfma_f32_16x16x32_bf16 v[16:19], a[112:115], v[224:227], v[16:19]
v_exp_f32 v213, v213
v_mfma_f32_16x16x32_bf16 v[48:51], a[112:115], v[228:231], v[48:51]
v_cvt_pk_bf16_f32 v237, v218, v219
s_and_b32 s92, s92, 3
s_sub_u32 s92, s92, s84
v_mfma_f32_16x16x32_bf16 v[20:23], a[116:119], v[224:227], v[20:23]
v_exp_f32 v214, v214
v_mfma_f32_16x16x32_bf16 v[52:55], a[116:119], v[228:231], v[52:55]
v_cvt_pk_bf16_f32 v234, v212, v213
v_mfma_f32_16x16x32_bf16 v[24:27], a[120:123], v[224:227], v[24:27]
v_exp_f32 v215, v215
v_mfma_f32_16x16x32_bf16 v[56:59], a[120:123], v[228:231], v[56:59]
v_cvt_pk_bf16_f32 v235, v214, v215
v_mfma_f32_16x16x32_bf16 v[28:31], a[124:127], v[224:227], v[28:31]
v_exp_f32 v220, v220
v_mfma_f32_16x16x32_bf16 v[60:63], a[124:127], v[228:231], v[60:63]
s_cmp_eq_u32 s92, 3
s_cbranch_scc1 .La5_LA_PS2
.La5_LA_PS3:
s_cmp_lg_u32 s84, 3
s_cbranch_scc1 .La5_LA_PSPIN
.La5_LA_DC1:
s_waitcnt vmcnt(8)
v_mfma_f32_16x16x32_bf16 v[0:3], a[214:217], a[0:3], v[0:3]
v_accvgpr_read_b32 v250, a192
v_accvgpr_read_b32 v251, a193
v_add_f32 v250, v250, v251
v_accvgpr_read_b32 v251, a220
v_add_f32 v250, v250, v251
v_accvgpr_read_b32 v249, a194
v_mfma_f32_16x16x32_bf16 v[4:7], a[212:215], a[0:3], v[4:7]
v_accvgpr_read_b32 v251, a195
v_add_f32 v249, v249, v251
v_accvgpr_read_b32 v251, a221
v_add_f32 v249, v249, v251
v_subrev_u32 v251, 562036736, v250
v_mfma_f32_16x16x32_bf16 v[8:11], a[214:217], a[4:7], v[8:11]
v_rcp_f32 v250, v250
v_cmp_le_u32 vcc, 1006632960, v251
v_subrev_u32 v251, 562036736, v249
s_mov_b64 s[92:93], vcc
v_cmp_le_u32 vcc, 1006632960, v251
v_mfma_f32_16x16x32_bf16 v[12:15], a[212:215], a[4:7], v[12:15]
v_rcp_f32 v249, v249
v_mfma_f32_16x16x32_bf16 v[16:19], a[214:217], a[8:11], v[16:19]
s_or_b64 vcc, vcc, s[92:93]
s_cbranch_scc1 .La5_LA_SC3
.La5_LA_SR3:
v_exp_f32 v221, v221
v_exp_f32 v222, v222
v_mfma_f32_16x16x32_bf16 v[20:23], a[212:215], a[8:11], v[20:23]
v_exp_f32 v223, v223
v_cvt_pk_bf16_f32 v238, v220, v221
v_mul_f32 v0, v0, v250
v_mul_f32 v1, v1, v250
v_mul_f32 v2, v2, v250
v_mfma_f32_16x16x32_bf16 v[24:27], a[214:217], a[12:15], v[24:27]
v_mul_f32 v3, v3, v250
v_mul_f32 v4, v4, v250
v_mul_f32 v5, v5, v250
v_mul_f32 v6, v6, v250
v_mul_f32 v7, v7, v250
v_cvt_pk_bf16_f32 v128, v0, v1
v_mfma_f32_16x16x32_bf16 v[28:31], a[212:215], a[12:15], v[28:31]
v_cvt_pk_bf16_f32 v129, v2, v3
v_cvt_pk_bf16_f32 v130, v4, v5
v_cvt_pk_bf16_f32 v131, v6, v7
buffer_store_dwordx4 v[128:131], v244, s[44:47], s91 offen offset:0 sc1
v_cvt_pk_bf16_f32 v239, v222, v223
v_mfma_f32_16x16x32_bf16 v[32:35], a[214:217], a[16:19], v[32:35]
v_mul_f32 v8, v8, v250
v_mul_f32 v9, v9, v250
v_mul_f32 v10, v10, v250
v_mul_f32 v11, v11, v250
v_mul_f32 v12, v12, v250
v_mul_f32 v13, v13, v250
v_mfma_f32_16x16x32_bf16 v[36:39], a[212:215], a[16:19], v[36:39]
v_mul_f32 v14, v14, v250
v_mul_f32 v15, v15, v250
v_cvt_pk_bf16_f32 v132, v8, v9
v_cvt_pk_bf16_f32 v133, v10, v11
v_cvt_pk_bf16_f32 v134, v12, v13
v_cvt_pk_bf16_f32 v135, v14, v15
v_mfma_f32_16x16x32_bf16 v[40:43], a[214:217], a[20:23], v[40:43]
buffer_store_dwordx4 v[132:135], v244, s[44:47], s91 offen offset:1024 sc1
v_mul_f32 v16, v16, v250
v_mul_f32 v17, v17, v250
v_mul_f32 v18, v18, v250
v_mul_f32 v19, v19, v250
v_mfma_f32_16x16x32_bf16 v[44:47], a[212:215], a[20:23], v[44:47]
v_mul_f32 v20, v20, v250
v_mul_f32 v21, v21, v250
v_mul_f32 v22, v22, v250
v_mul_f32 v23, v23, v250
v_cvt_pk_bf16_f32 v136, v16, v17
v_cvt_pk_bf16_f32 v137, v18, v19
v_mfma_f32_16x16x32_bf16 v[48:51], a[214:217], a[24:27], v[48:51]
v_cvt_pk_bf16_f32 v138, v20, v21
v_cvt_pk_bf16_f32 v139, v22, v23
buffer_store_dwordx4 v[136:139], v244, s[44:47], s91 offen offset:2048 sc1
v_mul_f32 v24, v24, v250
v_mul_f32 v25, v25, v250
v_mfma_f32_16x16x32_bf16 v[52:55], a[212:215], a[24:27], v[52:55]
v_mul_f32 v26, v26, v250
v_mul_f32 v27, v27, v250
v_mul_f32 v28, v28, v250
v_mul_f32 v29, v29, v250
v_mul_f32 v30, v30, v250
v_mul_f32 v31, v31, v250
v_mfma_f32_16x16x32_bf16 v[56:59], a[214:217], a[28:31], v[56:59]
v_cvt_pk_bf16_f32 v140, v24, v25
v_cvt_pk_bf16_f32 v141, v26, v27
v_cvt_pk_bf16_f32 v142, v28, v29
v_cvt_pk_bf16_f32 v143, v30, v31
buffer_store_dwordx4 v[140:143], v244, s[44:47], s91 offen offset:3072 sc1
v_mfma_f32_16x16x32_bf16 v[60:63], a[212:215], a[28:31], v[60:63]
v_mul_f32 v32, v32, v249
v_mul_f32 v33, v33, v249
v_mul_f32 v34, v34, v249
v_mul_f32 v35, v35, v249
v_mul_f32 v36, v36, v249
v_mul_f32 v37, v37, v249
v_smfmac_f32_16x16x64_bf16 a[196:199], a[208:211], v[232:239], v241
v_mul_f32 v38, v38, v249
v_mul_f32 v39, v39, v249
v_cvt_pk_bf16_f32 v144, v32, v33
v_cvt_pk_bf16_f32 v145, v34, v35
v_cvt_pk_bf16_f32 v146, v36, v37
v_mfma_f32_16x16x32_bf16 v[64:67], a[96:99], v[232:235], v[64:67]
v_cvt_pk_bf16_f32 v147, v38, v39
buffer_store_dwordx4 v[144:147], v245, s[44:47], s91 offen offset:0 sc1
v_mul_f32 v40, v40, v249
v_mul_f32 v41, v41, v249
v_mul_f32 v42, v42, v249
v_mfma_f32_16x16x32_bf16 v[96:99], a[96:99], v[236:239], v[96:99]
v_mul_f32 v43, v43, v249
v_mul_f32 v44, v44, v249
v_mul_f32 v45, v45, v249
v_mul_f32 v46, v46, v249
v_mul_f32 v47, v47, v249
v_cvt_pk_bf16_f32 v148, v40, v41
v_mfma_f32_16x16x32_bf16 v[68:71], a[100:103], v[232:235], v[68:71]
v_cvt_pk_bf16_f32 v149, v42, v43
v_cvt_pk_bf16_f32 v150, v44, v45
v_cvt_pk_bf16_f32 v151, v46, v47
buffer_store_dwordx4 v[148:151], v245, s[44:47], s91 offen offset:1024 sc1
v_mul_f32 v48, v48, v249
v_mfma_f32_16x16x32_bf16 v[100:103], a[100:103], v[236:239], v[100:103]
v_mul_f32 v49, v49, v249
v_mul_f32 v50, v50, v249
v_mul_f32 v51, v51, v249
v_mul_f32 v52, v52, v249
v_mul_f32 v53, v53, v249
v_mul_f32 v54, v54, v249
v_mfma_f32_16x16x32_bf16 v[72:75], a[104:107], v[232:235], v[72:75]
v_mul_f32 v55, v55, v249
v_cvt_pk_bf16_f32 v152, v48, v49
v_cvt_pk_bf16_f32 v153, v50, v51
v_cvt_pk_bf16_f32 v154, v52, v53
v_cvt_pk_bf16_f32 v155, v54, v55
buffer_store_dwordx4 v[152:155], v245, s[44:47], s91 offen offset:2048 sc1
v_mfma_f32_16x16x32_bf16 v[104:107], a[104:107], v[236:239], v[104:107]
v_mul_f32 v56, v56, v249
v_mul_f32 v57, v57, v249
v_mul_f32 v58, v58, v249
v_mul_f32 v59, v59, v249
v_mul_f32 v60, v60, v249
v_mul_f32 v61, v61, v249
v_mfma_f32_16x16x32_bf16 v[76:79], a[108:111], v[232:235], v[76:79]
v_mul_f32 v62, v62, v249
v_mul_f32 v63, v63, v249
v_cvt_pk_bf16_f32 v156, v56, v57
v_cvt_pk_bf16_f32 v157, v58, v59
v_cvt_pk_bf16_f32 v158, v60, v61
v_cvt_pk_bf16_f32 v159, v62, v63
v_mfma_f32_16x16x32_bf16 v[108:111], a[108:111], v[236:239], v[108:111]
buffer_store_dwordx4 v[156:159], v245, s[44:47], s91 offen offset:3072 sc1
v_accvgpr_read_b32 v240, a224
v_xor_b32 v241, 32, v240
v_xor_b32 v242, 64, v240
v_xor_b32 v243, 96, v240
v_mfma_f32_16x16x32_bf16 v[80:83], a[112:115], v[232:235], v[80:83]
v_mfma_f32_16x16x32_bf16 v[112:115], a[112:115], v[236:239], v[112:115]
v_mfma_f32_16x16x32_bf16 v[84:87], a[116:119], v[232:235], v[84:87]
v_mfma_f32_16x16x32_bf16 v[116:119], a[116:119], v[236:239], v[116:119]
v_mfma_f32_16x16x32_bf16 v[88:91], a[120:123], v[232:235], v[88:91]
v_mfma_f32_16x16x32_bf16 v[120:123], a[120:123], v[236:239], v[120:123]
v_mfma_f32_16x16x32_bf16 v[92:95], a[124:127], v[232:235], v[92:95]
v_mfma_f32_16x16x32_bf16 v[124:127], a[124:127], v[236:239], v[124:127]
s_nop 0
s_barrier
s_waitcnt vmcnt(8)
v_mfma_f32_16x16x32_bf16 v[64:67], a[214:217], a[32:35], v[64:67]
ds_read_b64_tr_b16 v[192:193], v240 offset:16896
ds_read_b64_tr_b16 v[194:195], v240 offset:17920
v_mfma_f32_16x16x32_bf16 v[68:71], a[212:215], a[32:35], v[68:71]
ds_read_b64_tr_b16 v[196:197], v241 offset:16896
ds_read_b64_tr_b16 v[198:199], v241 offset:17920
v_mfma_f32_16x16x32_bf16 v[72:75], a[214:217], a[36:39], v[72:75]
ds_read_b64_tr_b16 v[200:201], v242 offset:16896
ds_read_b64_tr_b16 v[202:203], v242 offset:17920
v_accvgpr_read_b32 v250, a196
v_accvgpr_read_b32 v251, a197
v_add_f32 v250, v250, v251
v_accvgpr_read_b32 v251, a222
v_mfma_f32_16x16x32_bf16 v[76:79], a[212:215], a[36:39], v[76:79]
ds_read_b64_tr_b16 v[204:205], v243 offset:16896
ds_read_b64_tr_b16 v[206:207], v243 offset:17920
v_add_f32 v250, v250, v251
v_accvgpr_read_b32 v249, a198
v_accvgpr_read_b32 v251, a199
v_add_f32 v249, v249, v251
v_mfma_f32_16x16x32_bf16 v[80:83], a[214:217], a[40:43], v[80:83]
ds_read_b64_tr_b16 v[208:209], v240 offset:17024
ds_read_b64_tr_b16 v[210:211], v240 offset:18048
v_accvgpr_read_b32 v251, a223
v_add_f32 v249, v249, v251
v_subrev_u32 v251, 562036736, v250
v_mfma_f32_16x16x32_bf16 v[84:87], a[212:215], a[40:43], v[84:87]
ds_read_b64_tr_b16 v[212:213], v241 offset:17024
ds_read_b64_tr_b16 v[214:215], v241 offset:18048
v_rcp_f32 v250, v250
v_cmp_le_u32 vcc, 1006632960, v251
v_subrev_u32 v251, 562036736, v249
v_mfma_f32_16x16x32_bf16 v[88:91], a[214:217], a[44:47], v[88:91]
ds_read_b64_tr_b16 v[216:217], v242 offset:17024
ds_read_b64_tr_b16 v[218:219], v242 offset:18048
s_mov_b64 s[92:93], vcc
v_cmp_le_u32 vcc, 1006632960, v251
v_rcp_f32_e64 v249, v249
v_mfma_f32_16x16x32_bf16 v[92:95], a[212:215], a[44:47], v[92:95]
ds_read_b64_tr_b16 v[220:221], v243 offset:17024
ds_read_b64_tr_b16 v[222:223], v243 offset:18048
s_or_b64 vcc, vcc, s[92:93]
s_cbranch_scc1 .La5_LA_SC4
.La5_LA_SR4:
v_mul_f32 v64, v64, v250
v_mul_f32 v65, v65, v250
v_mfma_f32_16x16x32_bf16 v[96:99], a[214:217], a[48:51], v[96:99]
v_mul_f32 v66, v66, v250
v_mul_f32 v67, v67, v250
v_mul_f32 v68, v68, v250
v_mul_f32 v69, v69, v250
v_mul_f32 v70, v70, v250
v_mul_f32 v71, v71, v250
v_mfma_f32_16x16x32_bf16 v[100:103], a[212:215], a[48:51], v[100:103]
v_cvt_pk_bf16_f32 v160, v64, v65
v_cvt_pk_bf16_f32 v161, v66, v67
v_cvt_pk_bf16_f32 v162, v68, v69
v_cvt_pk_bf16_f32 v163, v70, v71
buffer_store_dwordx4 v[160:163], v246, s[44:47], s91 offen offset:0 sc1
v_mfma_f32_16x16x32_bf16 v[104:107], a[214:217], a[52:55], v[104:107]
v_mul_f32 v72, v72, v250
v_mul_f32 v73, v73, v250
v_mul_f32 v74, v74, v250
v_mul_f32 v75, v75, v250
v_mul_f32 v76, v76, v250
v_mul_f32 v77, v77, v250
v_mfma_f32_16x16x32_bf16 v[108:111], a[212:215], a[52:55], v[108:111]
v_mul_f32 v78, v78, v250
v_mul_f32 v79, v79, v250
v_cvt_pk_bf16_f32 v164, v72, v73
v_cvt_pk_bf16_f32 v165, v74, v75
v_cvt_pk_bf16_f32 v166, v76, v77
v_cvt_pk_bf16_f32 v167, v78, v79
v_mfma_f32_16x16x32_bf16 v[112:115], a[214:217], a[56:59], v[112:115]
buffer_store_dwordx4 v[164:167], v246, s[44:47], s91 offen offset:1024 sc1
v_mul_f32 v80, v80, v250
v_mul_f32 v81, v81, v250
v_mul_f32 v82, v82, v250
v_mul_f32 v83, v83, v250
v_mfma_f32_16x16x32_bf16 v[116:119], a[212:215], a[56:59], v[116:119]
v_mul_f32 v84, v84, v250
v_mul_f32 v85, v85, v250
v_mul_f32 v86, v86, v250
.La5_LW2_11:
s_cbranch_execz .La5_LW2_R1
v_mul_f32 v87, v87, v250
v_cvt_pk_bf16_f32 v168, v80, v81
v_cvt_pk_bf16_f32 v169, v82, v83
v_mfma_f32_16x16x32_bf16 v[120:123], a[214:217], a[60:63], v[120:123]
v_cvt_pk_bf16_f32 v170, v84, v85
v_cvt_pk_bf16_f32 v171, v86, v87
buffer_store_dwordx4 v[168:171], v246, s[44:47], s91 offen offset:2048 sc1
v_mul_f32 v88, v88, v250
v_mul_f32 v89, v89, v250
v_mfma_f32_16x16x32_bf16 v[124:127], a[212:215], a[60:63], v[124:127]
s_waitcnt lgkmcnt(14)
v_mul_f32 v90, v90, v250
v_mul_f32 v91, v91, v250
v_mul_f32 v92, v92, v250
v_mul_f32 v93, v93, v250
v_mul_f32 v94, v94, v250
v_mfma_f32_16x16x32_bf16 v[0:3], v[192:195], v[128:131], 0
s_waitcnt lgkmcnt(12)
v_mul_f32 v95, v95, v250
v_cvt_pk_bf16_f32 v172, v88, v89
v_cvt_pk_bf16_f32 v173, v90, v91
v_cvt_pk_bf16_f32 v174, v92, v93
v_cvt_pk_bf16_f32 v175, v94, v95
v_mfma_f32_16x16x32_bf16 v[32:35], v[192:195], v[144:147], 0
ds_read_b64_tr_b16 v[192:193], v240 offset:25344
ds_read_b64_tr_b16 v[194:195], v240 offset:26368
buffer_store_dwordx4 v[172:175], v246, s[44:47], s91 offen offset:3072 sc1
v_mul_f32 v96, v96, v249
v_mul_f32 v97, v97, v249
v_mfma_f32_16x16x32_bf16 v[4:7], v[196:199], v[128:131], 0
s_waitcnt lgkmcnt(12)
v_mul_f32 v98, v98, v249
v_mul_f32 v99, v99, v249
v_mul_f32 v100, v100, v249
v_mul_f32 v101, v101, v249
v_mul_f32 v102, v102, v249
v_mfma_f32_16x16x32_bf16 v[36:39], v[196:199], v[144:147], 0
ds_read_b64_tr_b16 v[196:197], v241 offset:25344
ds_read_b64_tr_b16 v[198:199], v241 offset:26368
v_mul_f32 v103, v103, v249
v_cvt_pk_bf16_f32 v176, v96, v97
v_cvt_pk_bf16_f32 v177, v98, v99
v_cvt_pk_bf16_f32 v178, v100, v101
v_mfma_f32_16x16x32_bf16 v[8:11], v[200:203], v[128:131], 0
s_waitcnt lgkmcnt(12)
v_cvt_pk_bf16_f32 v179, v102, v103
buffer_store_dwordx4 v[176:179], v247, s[44:47], s91 offen offset:0 sc1
v_mul_f32 v104, v104, v249
v_mul_f32 v105, v105, v249
v_mfma_f32_16x16x32_bf16 v[40:43], v[200:203], v[144:147], 0
ds_read_b64_tr_b16 v[200:201], v242 offset:25344
ds_read_b64_tr_b16 v[202:203], v242 offset:26368
v_mul_f32 v106, v106, v249
v_mul_f32 v107, v107, v249
v_mul_f32 v108, v108, v249
v_mul_f32 v109, v109, v249
v_mfma_f32_16x16x32_bf16 v[12:15], v[204:207], v[128:131], 0
s_waitcnt lgkmcnt(12)
v_mul_f32 v110, v110, v249
v_mul_f32 v111, v111, v249
v_cvt_pk_bf16_f32 v180, v104, v105
v_cvt_pk_bf16_f32 v181, v106, v107
v_cvt_pk_bf16_f32 v182, v108, v109
v_mfma_f32_16x16x32_bf16 v[44:47], v[204:207], v[144:147], 0
ds_read_b64_tr_b16 v[204:205], v243 offset:25344
ds_read_b64_tr_b16 v[206:207], v243 offset:26368
v_cvt_pk_bf16_f32 v183, v110, v111
buffer_store_dwordx4 v[180:183], v247, s[44:47], s91 offen offset:1024 sc1
v_mul_f32 v112, v112, v249
v_mfma_f32_16x16x32_bf16 v[16:19], v[208:211], v[128:131], 0
s_waitcnt lgkmcnt(8)
v_mul_f32 v113, v113, v249
v_mul_f32 v114, v114, v249
v_mul_f32 v115, v115, v249
v_mul_f32 v116, v116, v249
v_mul_f32 v117, v117, v249
v_mfma_f32_16x16x32_bf16 v[48:51], v[208:211], v[144:147], 0
ds_read_b64_tr_b16 v[208:209], v240 offset:25472
ds_read_b64_tr_b16 v[210:211], v240 offset:26496
v_mul_f32 v118, v118, v249
v_mul_f32 v119, v119, v249
v_cvt_pk_bf16_f32 v184, v112, v113
v_cvt_pk_bf16_f32 v185, v114, v115
v_mfma_f32_16x16x32_bf16 v[20:23], v[212:215], v[128:131], 0
v_cvt_pk_bf16_f32 v186, v116, v117
v_cvt_pk_bf16_f32 v187, v118, v119
buffer_store_dwordx4 v[184:187], v247, s[44:47], s91 offen offset:2048 sc1
v_mul_f32 v120, v120, v249
v_mul_f32 v121, v121, v249
v_mfma_f32_16x16x32_bf16 v[52:55], v[212:215], v[144:147], 0
ds_read_b64_tr_b16 v[212:213], v241 offset:25472
ds_read_b64_tr_b16 v[214:215], v241 offset:26496
v_mul_f32 v122, v122, v249
v_mul_f32 v123, v123, v249
v_mul_f32 v124, v124, v249
v_mul_f32 v125, v125, v249
v_mfma_f32_16x16x32_bf16 v[24:27], v[216:219], v[128:131], 0
v_mul_f32 v126, v126, v249
v_mul_f32 v127, v127, v249
v_cvt_pk_bf16_f32 v188, v120, v121
v_cvt_pk_bf16_f32 v189, v122, v123
v_cvt_pk_bf16_f32 v190, v124, v125
v_cvt_pk_bf16_f32 v191, v126, v127
v_mfma_f32_16x16x32_bf16 v[56:59], v[216:219], v[144:147], 0
ds_read_b64_tr_b16 v[216:217], v242 offset:25472
ds_read_b64_tr_b16 v[218:219], v242 offset:26496
buffer_store_dwordx4 v[188:191], v247, s[44:47], s91 offen offset:3072 sc1
v_mfma_f32_16x16x32_bf16 v[28:31], v[220:223], v[128:131], 0
s_waitcnt lgkmcnt(6)
v_mfma_f32_16x16x32_bf16 v[60:63], v[220:223], v[144:147], 0
ds_read_b64_tr_b16 v[220:221], v243 offset:25472
ds_read_b64_tr_b16 v[222:223], v243 offset:26496
v_mfma_f32_16x16x32_bf16 v[0:3], v[192:195], v[132:135], v[0:3]
v_accvgpr_read_b32 v224, a228
v_add_u32 v225, 0x8000, v224
v_add_u32 v226, 0x2000, v224
v_add_u32 v227, 0xa000, v224
v_mfma_f32_16x16x32_bf16 v[32:35], v[192:195], v[148:151], v[32:35]
ds_read_b64_tr_b16 v[192:193], v240 offset:33792
ds_read_b64_tr_b16 v[194:195], v240 offset:34816
v_accvgpr_read_b32 v248, a225
v_accvgpr_read_b32 v228, a229
v_mfma_f32_16x16x32_bf16 v[4:7], v[196:199], v[132:135], v[4:7]
v_accvgpr_read_b32 v229, a230
v_mfma_f32_16x16x32_bf16 v[36:39], v[196:199], v[148:151], v[36:39]
ds_read_b64_tr_b16 v[196:197], v241 offset:33792
ds_read_b64_tr_b16 v[198:199], v241 offset:34816
v_mfma_f32_16x16x32_bf16 v[8:11], v[200:203], v[132:135], v[8:11]
v_mfma_f32_16x16x32_bf16 v[40:43], v[200:203], v[148:151], v[40:43]
ds_read_b64_tr_b16 v[200:201], v242 offset:33792
ds_read_b64_tr_b16 v[202:203], v242 offset:34816
v_mfma_f32_16x16x32_bf16 v[12:15], v[204:207], v[132:135], v[12:15]
s_waitcnt lgkmcnt(6)
v_mfma_f32_16x16x32_bf16 v[44:47], v[204:207], v[148:151], v[44:47]
ds_read_b64_tr_b16 v[204:205], v243 offset:33792
ds_read_b64_tr_b16 v[206:207], v243 offset:34816
v_mfma_f32_16x16x32_bf16 v[16:19], v[208:211], v[132:135], v[16:19]
v_mfma_f32_16x16x32_bf16 v[48:51], v[208:211], v[148:151], v[48:51]
ds_read_b64_tr_b16 v[208:209], v240 offset:33920
ds_read_b64_tr_b16 v[210:211], v240 offset:34944
v_mfma_f32_16x16x32_bf16 v[20:23], v[212:215], v[132:135], v[20:23]
v_mfma_f32_16x16x32_bf16 v[52:55], v[212:215], v[148:151], v[52:55]
ds_read_b64_tr_b16 v[212:213], v241 offset:33920
ds_read_b64_tr_b16 v[214:215], v241 offset:34944
v_mfma_f32_16x16x32_bf16 v[24:27], v[216:219], v[132:135], v[24:27]
v_mfma_f32_16x16x32_bf16 v[56:59], v[216:219], v[148:151], v[56:59]
ds_read_b64_tr_b16 v[216:217], v242 offset:33920
ds_read_b64_tr_b16 v[218:219], v242 offset:34944
v_mfma_f32_16x16x32_bf16 v[28:31], v[220:223], v[132:135], v[28:31]
s_waitcnt lgkmcnt(6)
v_mfma_f32_16x16x32_bf16 v[60:63], v[220:223], v[148:151], v[60:63]
ds_read_b64_tr_b16 v[220:221], v243 offset:33920
ds_read_b64_tr_b16 v[222:223], v243 offset:34944
v_mfma_f32_16x16x32_bf16 v[0:3], v[192:195], v[136:139], v[0:3]
v_mfma_f32_16x16x32_bf16 v[32:35], v[192:195], v[152:155], v[32:35]
ds_read_b64_tr_b16 v[192:193], v240 offset:42240
ds_read_b64_tr_b16 v[194:195], v240 offset:43264
v_mfma_f32_16x16x32_bf16 v[4:7], v[196:199], v[136:139], v[4:7]
v_mfma_f32_16x16x32_bf16 v[36:39], v[196:199], v[152:155], v[36:39]
ds_read_b64_tr_b16 v[196:197], v241 offset:42240
ds_read_b64_tr_b16 v[198:199], v241 offset:43264
v_mfma_f32_16x16x32_bf16 v[8:11], v[200:203], v[136:139], v[8:11]
v_mfma_f32_16x16x32_bf16 v[40:43], v[200:203], v[152:155], v[40:43]
ds_read_b64_tr_b16 v[200:201], v242 offset:42240
ds_read_b64_tr_b16 v[202:203], v242 offset:43264
v_mfma_f32_16x16x32_bf16 v[12:15], v[204:207], v[136:139], v[12:15]
s_waitcnt lgkmcnt(6)
v_mfma_f32_16x16x32_bf16 v[44:47], v[204:207], v[152:155], v[44:47]
ds_read_b64_tr_b16 v[204:205], v243 offset:42240
ds_read_b64_tr_b16 v[206:207], v243 offset:43264
v_mfma_f32_16x16x32_bf16 v[16:19], v[208:211], v[136:139], v[16:19]
v_mfma_f32_16x16x32_bf16 v[48:51], v[208:211], v[152:155], v[48:51]
ds_read_b64_tr_b16 v[208:209], v240 offset:42368
ds_read_b64_tr_b16 v[210:211], v240 offset:43392
v_mfma_f32_16x16x32_bf16 v[20:23], v[212:215], v[136:139], v[20:23]
v_mfma_f32_16x16x32_bf16 v[52:55], v[212:215], v[152:155], v[52:55]
ds_read_b64_tr_b16 v[212:213], v241 offset:42368
ds_read_b64_tr_b16 v[214:215], v241 offset:43392
v_accvgpr_read_b32 v249, a231
s_waitcnt vmcnt(0)
buffer_store_dwordx2 v[252:253], v249, s[64:67], s6 offen offset:0 sc1
v_mfma_f32_16x16x32_bf16 v[24:27], v[216:219], v[136:139], v[24:27]
v_mfma_f32_16x16x32_bf16 v[56:59], v[216:219], v[152:155], v[56:59]
ds_read_b64_tr_b16 v[216:217], v242 offset:42368
ds_read_b64_tr_b16 v[218:219], v242 offset:43392
v_mfma_f32_16x16x32_bf16 v[28:31], v[220:223], v[136:139], v[28:31]
s_waitcnt lgkmcnt(6)
v_mfma_f32_16x16x32_bf16 v[60:63], v[220:223], v[152:155], v[60:63]
ds_read_b64_tr_b16 v[220:221], v243 offset:42368
ds_read_b64_tr_b16 v[222:223], v243 offset:43392
v_mfma_f32_16x16x32_bf16 v[0:3], v[192:195], v[140:143], v[0:3]
v_mfma_f32_16x16x32_bf16 v[32:35], v[192:195], v[156:159], v[32:35]
ds_read_b64_tr_b16 v[192:193], v240 offset:16896
ds_read_b64_tr_b16 v[194:195], v240 offset:17920
v_mfma_f32_16x16x32_bf16 v[4:7], v[196:199], v[140:143], v[4:7]
v_mfma_f32_16x16x32_bf16 v[36:39], v[196:199], v[156:159], v[36:39]
ds_read_b64_tr_b16 v[196:197], v241 offset:16896
ds_read_b64_tr_b16 v[198:199], v241 offset:17920
v_mfma_f32_16x16x32_bf16 v[8:11], v[200:203], v[140:143], v[8:11]
v_mfma_f32_16x16x32_bf16 v[40:43], v[200:203], v[156:159], v[40:43]
ds_read_b64_tr_b16 v[200:201], v242 offset:16896
ds_read_b64_tr_b16 v[202:203], v242 offset:17920
v_mfma_f32_16x16x32_bf16 v[12:15], v[204:207], v[140:143], v[12:15]
s_waitcnt lgkmcnt(6)
v_mfma_f32_16x16x32_bf16 v[44:47], v[204:207], v[156:159], v[44:47]
ds_read_b64_tr_b16 v[204:205], v243 offset:16896
ds_read_b64_tr_b16 v[206:207], v243 offset:17920
v_mfma_f32_16x16x32_bf16 v[16:19], v[208:211], v[140:143], v[16:19]
v_mfma_f32_16x16x32_bf16 v[48:51], v[208:211], v[156:159], v[48:51]
ds_read_b64_tr_b16 v[208:209], v240 offset:17024
ds_read_b64_tr_b16 v[210:211], v240 offset:18048
v_mfma_f32_16x16x32_bf16 v[20:23], v[212:215], v[140:143], v[20:23]
v_mfma_f32_16x16x32_bf16 v[52:55], v[212:215], v[156:159], v[52:55]
ds_read_b64_tr_b16 v[212:213], v241 offset:17024
ds_read_b64_tr_b16 v[214:215], v241 offset:18048
v_mfma_f32_16x16x32_bf16 v[24:27], v[216:219], v[140:143], v[24:27]
v_mfma_f32_16x16x32_bf16 v[56:59], v[216:219], v[156:159], v[56:59]
ds_read_b64_tr_b16 v[216:217], v242 offset:17024
ds_read_b64_tr_b16 v[218:219], v242 offset:18048
v_mfma_f32_16x16x32_bf16 v[28:31], v[220:223], v[140:143], v[28:31]
s_waitcnt lgkmcnt(6)
v_mfma_f32_16x16x32_bf16 v[60:63], v[220:223], v[156:159], v[60:63]
ds_read_b64_tr_b16 v[220:221], v243 offset:17024
ds_read_b64_tr_b16 v[222:223], v243 offset:18048
s_cmp_lg_u32 s30, 0
s_cbranch_scc1 .La5_LO_W
; ---- O projection: 256 x 128 tile over eight heads
.La5_LO_START:
.La5_LW2_12:
s_cbranch_execz .La5_LW2_R0
v_mfma_f32_16x16x32_bf16 v[64:67], v[192:195], v[160:163], 0
buffer_load_dwordx2 v[250:251], v248, s[64:67], s5 offen sc1
v_mfma_f32_16x16x32_bf16 v[96:99], v[192:195], v[176:179], 0
ds_read_b64_tr_b16 v[192:193], v240 offset:25344
ds_read_b64_tr_b16 v[194:195], v240 offset:26368
v_mfma_f32_16x16x32_bf16 v[68:71], v[196:199], v[160:163], 0
s_add_u32 m0, s83, 0
s_bfe_u32 s80, s4, 262148
v_mfma_f32_16x16x32_bf16 v[100:103], v[196:199], v[176:179], 0
ds_read_b64_tr_b16 v[196:197], v241 offset:25344
ds_read_b64_tr_b16 v[198:199], v241 offset:26368
s_xor_b32 s80, s80, s72
s_lshl_b32 s80, s80, 18
s_add_u32 s80, s80, 131072
v_mfma_f32_16x16x32_bf16 v[72:75], v[200:203], v[160:163], 0
buffer_load_dwordx4 v224, s[56:59], s80 offen lds
v_mfma_f32_16x16x32_bf16 v[104:107], v[200:203], v[176:179], 0
ds_read_b64_tr_b16 v[200:201], v242 offset:25344
ds_read_b64_tr_b16 v[202:203], v242 offset:26368
v_mfma_f32_16x16x32_bf16 v[76:79], v[204:207], v[160:163], 0
s_add_u32 m0, s83, 1024
s_waitcnt lgkmcnt(6)
v_mfma_f32_16x16x32_bf16 v[108:111], v[204:207], v[176:179], 0
ds_read_b64_tr_b16 v[204:205], v243 offset:25344
ds_read_b64_tr_b16 v[206:207], v243 offset:26368
v_mfma_f32_16x16x32_bf16 v[80:83], v[208:211], v[160:163], 0
buffer_load_dwordx4 v225, s[56:59], s80 offen lds
v_mfma_f32_16x16x32_bf16 v[112:115], v[208:211], v[176:179], 0
ds_read_b64_tr_b16 v[208:209], v240 offset:25472
ds_read_b64_tr_b16 v[210:211], v240 offset:26496
v_mfma_f32_16x16x32_bf16 v[84:87], v[212:215], v[160:163], 0
s_add_u32 m0, s83, 2176
s_bfe_u32 s53, s4, 262148
v_mfma_f32_16x16x32_bf16 v[116:119], v[212:215], v[176:179], 0
ds_read_b64_tr_b16 v[212:213], v241 offset:25472
ds_read_b64_tr_b16 v[214:215], v241 offset:26496
v_mfma_f32_16x16x32_bf16 v[88:91], v[216:219], v[160:163], 0
buffer_load_dwordx4 v226, s[56:59], s80 offen lds
v_mfma_f32_16x16x32_bf16 v[120:123], v[216:219], v[176:179], 0
ds_read_b64_tr_b16 v[216:217], v242 offset:25472
ds_read_b64_tr_b16 v[218:219], v242 offset:26496
v_mfma_f32_16x16x32_bf16 v[92:95], v[220:223], v[160:163], 0
s_add_u32 m0, s83, 3200
s_waitcnt lgkmcnt(6)
v_mfma_f32_16x16x32_bf16 v[124:127], v[220:223], v[176:179], 0
ds_read_b64_tr_b16 v[220:221], v243 offset:25472
ds_read_b64_tr_b16 v[222:223], v243 offset:26496
v_mfma_f32_16x16x32_bf16 v[64:67], v[192:195], v[164:167], v[64:67]
buffer_load_dwordx4 v227, s[56:59], s80 offen lds
v_mfma_f32_16x16x32_bf16 v[96:99], v[192:195], v[180:183], v[96:99]
ds_read_b64_tr_b16 v[192:193], v240 offset:33792
ds_read_b64_tr_b16 v[194:195], v240 offset:34816
v_mfma_f32_16x16x32_bf16 v[68:71], v[196:199], v[164:167], v[68:71]
s_add_u32 m0, s83, 118272
s_bfe_u32 s80, s4, 262152
v_mfma_f32_16x16x32_bf16 v[100:103], v[196:199], v[180:183], v[100:103]
ds_read_b64_tr_b16 v[196:197], v241 offset:33792
ds_read_b64_tr_b16 v[198:199], v241 offset:34816
s_xor_b32 s80, s80, s72
s_lshl_b32 s80, s80, 18
v_mfma_f32_16x16x32_bf16 v[72:75], v[200:203], v[164:167], v[72:75]
buffer_load_dwordx4 v224, s[56:59], s80 offen lds
v_mfma_f32_16x16x32_bf16 v[104:107], v[200:203], v[180:183], v[104:107]
s_mov_b64 exec, 0
s_cmp_eq_u32 s3, 0
s_cbranch_scc1 .La5_LW3_0
s_cmp_eq_u32 s3, 1
s_cbranch_scc1 .La5_LW3_1
s_cmp_eq_u32 s3, 2
s_cbranch_scc1 .La5_LW3_2
s_branch .La5_LW3_3
.La5_LW3_R0:
s_mov_b64 exec, -1
ds_read_b64_tr_b16 v[200:201], v242 offset:33792
ds_read_b64_tr_b16 v[202:203], v242 offset:34816
v_mfma_f32_16x16x32_bf16 v[76:79], v[204:207], v[164:167], v[76:79]
s_waitcnt vmcnt(5)
v_cmp_eq_u64 vcc, v[250:251], v[252:253]
s_nop 0
s_bitcmp1_b32 vcc_lo, 1
s_nop 0
s_cbranch_scc0 .La5_LO_N1
.La5_LO_K1:
s_waitcnt lgkmcnt(6)
v_mfma_f32_16x16x32_bf16 v[108:111], v[204:207], v[180:183], v[108:111]
ds_read_b64_tr_b16 v[204:205], v243 offset:33792
ds_read_b64_tr_b16 v[206:207], v243 offset:34816
s_xor_b32 s53, s53, s72
s_lshl_b32 s53, s53, 13
v_mfma_f32_16x16x32_bf16 v[80:83], v[208:211], v[164:167], v[80:83]
buffer_load_dwordx4 a[0:3], v244, s[44:47], s53 offen offset:0 sc1
v_mfma_f32_16x16x32_bf16 v[112:115], v[208:211], v[180:183], v[112:115]
ds_read_b64_tr_b16 v[208:209], v240 offset:33920
ds_read_b64_tr_b16 v[210:211], v240 offset:34944
v_mfma_f32_16x16x32_bf16 v[84:87], v[212:215], v[164:167], v[84:87]
buffer_load_dwordx4 a[4:7], v245, s[44:47], s53 offen offset:0 sc1
v_mfma_f32_16x16x32_bf16 v[116:119], v[212:215], v[180:183], v[116:119]
ds_read_b64_tr_b16 v[212:213], v241 offset:33920
ds_read_b64_tr_b16 v[214:215], v241 offset:34944
v_mfma_f32_16x16x32_bf16 v[88:91], v[216:219], v[164:167], v[88:91]
buffer_load_dwordx4 a[8:11], v246, s[44:47], s53 offen offset:0 sc1
v_mfma_f32_16x16x32_bf16 v[120:123], v[216:219], v[180:183], v[120:123]
ds_read_b64_tr_b16 v[216:217], v242 offset:33920
ds_read_b64_tr_b16 v[218:219], v242 offset:34944
v_mfma_f32_16x16x32_bf16 v[92:95], v[220:223], v[164:167], v[92:95]
s_waitcnt lgkmcnt(6)
s_bfe_u32 s54, s4, 262152
v_mfma_f32_16x16x32_bf16 v[124:127], v[220:223], v[180:183], v[124:127]
ds_read_b64_tr_b16 v[220:221], v243 offset:33920
ds_read_b64_tr_b16 v[222:223], v243 offset:34944
v_mfma_f32_16x16x32_bf16 v[64:67], v[192:195], v[168:171], v[64:67]
buffer_load_dwordx4 a[12:15], v247, s[44:47], s53 offen offset:0 sc1
v_mfma_f32_16x16x32_bf16 v[96:99], v[192:195], v[184:187], v[96:99]
ds_read_b64_tr_b16 v[192:193], v240 offset:42240
ds_read_b64_tr_b16 v[194:195], v240 offset:43264
v_mfma_f32_16x16x32_bf16 v[68:71], v[196:199], v[168:171], v[68:71]
buffer_load_dwordx4 a[16:19], v244, s[44:47], s53 offen offset:1024 sc1
v_mfma_f32_16x16x32_bf16 v[100:103], v[196:199], v[184:187], v[100:103]
ds_read_b64_tr_b16 v[196:197], v241 offset:42240
ds_read_b64_tr_b16 v[198:199], v241 offset:43264
v_mfma_f32_16x16x32_bf16 v[72:75], v[200:203], v[168:171], v[72:75]
buffer_load_dwordx4 a[20:23], v245, s[44:47], s53 offen offset:1024 sc1
v_mfma_f32_16x16x32_bf16 v[104:107], v[200:203], v[184:187], v[104:107]
ds_read_b64_tr_b16 v[200:201], v242 offset:42240
ds_read_b64_tr_b16 v[202:203], v242 offset:43264
v_mfma_f32_16x16x32_bf16 v[76:79], v[204:207], v[168:171], v[76:79]
s_waitcnt lgkmcnt(6)
s_xor_b32 s54, s54, s72
v_mfma_f32_16x16x32_bf16 v[108:111], v[204:207], v[184:187], v[108:111]
ds_read_b64_tr_b16 v[204:205], v243 offset:42240
ds_read_b64_tr_b16 v[206:207], v243 offset:43264
v_mfma_f32_16x16x32_bf16 v[80:83], v[208:211], v[168:171], v[80:83]
buffer_load_dwordx4 a[24:27], v246, s[44:47], s53 offen offset:1024 sc1
v_mfma_f32_16x16x32_bf16 v[112:115], v[208:211], v[184:187], v[112:115]
ds_read_b64_tr_b16 v[208:209], v240 offset:42368
ds_read_b64_tr_b16 v[210:211], v240 offset:43392
v_mfma_f32_16x16x32_bf16 v[84:87], v[212:215], v[168:171], v[84:87]
buffer_load_dwordx4 a[28:31], v247, s[44:47], s53 offen offset:1024 sc1
s_add_u32 m0, s83, 119296
v_mfma_f32_16x16x32_bf16 v[116:119], v[212:215], v[184:187], v[116:119]
ds_read_b64_tr_b16 v[212:213], v241 offset:42368
ds_read_b64_tr_b16 v[214:215], v241 offset:43392
v_mfma_f32_16x16x32_bf16 v[88:91], v[216:219], v[168:171], v[88:91]
buffer_load_dwordx4 v225, s[56:59], s80 offen lds
v_mfma_f32_16x16x32_bf16 v[120:123], v[216:219], v[184:187], v[120:123]
ds_read_b64_tr_b16 v[216:217], v242 offset:42368
ds_read_b64_tr_b16 v[218:219], v242 offset:43392
v_mfma_f32_16x16x32_bf16 v[92:95], v[220:223], v[168:171], v[92:95]
s_add_u32 m0, s83, 120448
s_nop 0
s_barrier
s_waitcnt lgkmcnt(6)
v_mfma_f32_16x16x32_bf16 v[124:127], v[220:223], v[184:187], v[124:127]
ds_read_b64_tr_b16 v[220:221], v243 offset:42368
ds_read_b64_tr_b16 v[222:223], v243 offset:43392
v_mfma_f32_16x16x32_bf16 v[64:67], v[192:195], v[172:175], v[64:67]
buffer_load_dwordx4 v226, s[56:59], s80 offen lds
v_mfma_f32_16x16x32_bf16 v[96:99], v[192:195], v[188:191], v[96:99]
ds_read_b64_tr_b16 v[192:193], v240 offset:50688
ds_read_b64_tr_b16 v[194:195], v240 offset:51712
v_mfma_f32_16x16x32_bf16 v[68:71], v[196:199], v[172:175], v[68:71]
buffer_load_dwordx4 a[32:35], v244, s[44:47], s53 offen offset:2048 sc1
v_mfma_f32_16x16x32_bf16 v[100:103], v[196:199], v[188:191], v[100:103]
ds_read_b64_tr_b16 v[196:197], v241 offset:50688
ds_read_b64_tr_b16 v[198:199], v241 offset:51712
v_mfma_f32_16x16x32_bf16 v[72:75], v[200:203], v[172:175], v[72:75]
buffer_load_dwordx4 a[36:39], v245, s[44:47], s53 offen offset:2048 sc1
v_mfma_f32_16x16x32_bf16 v[104:107], v[200:203], v[188:191], v[104:107]
ds_read_b64_tr_b16 v[200:201], v242 offset:50688
ds_read_b64_tr_b16 v[202:203], v242 offset:51712
v_mfma_f32_16x16x32_bf16 v[76:79], v[204:207], v[172:175], v[76:79]
s_waitcnt lgkmcnt(6)
s_lshl_b32 s54, s54, 13
v_mfma_f32_16x16x32_bf16 v[108:111], v[204:207], v[188:191], v[108:111]
ds_read_b64_tr_b16 v[204:205], v243 offset:50688
ds_read_b64_tr_b16 v[206:207], v243 offset:51712
v_mfma_f32_16x16x32_bf16 v[80:83], v[208:211], v[172:175], v[80:83]
buffer_load_dwordx4 a[40:43], v246, s[44:47], s53 offen offset:2048 sc1
v_mfma_f32_16x16x32_bf16 v[112:115], v[208:211], v[188:191], v[112:115]
ds_read_b64_tr_b16 v[208:209], v240 offset:50816
ds_read_b64_tr_b16 v[210:211], v240 offset:51840
v_mfma_f32_16x16x32_bf16 v[84:87], v[212:215], v[172:175], v[84:87]
buffer_load_dwordx4 a[44:47], v247, s[44:47], s53 offen offset:2048 sc1
s_add_u32 m0, s83, 121472
v_mfma_f32_16x16x32_bf16 v[116:119], v[212:215], v[188:191], v[116:119]
ds_read_b64_tr_b16 v[212:213], v241 offset:50816
ds_read_b64_tr_b16 v[214:215], v241 offset:51840
v_mfma_f32_16x16x32_bf16 v[88:91], v[216:219], v[172:175], v[88:91]
buffer_load_dwordx4 v227, s[56:59], s80 offen lds
v_mfma_f32_16x16x32_bf16 v[120:123], v[216:219], v[188:191], v[120:123]
ds_read_b64_tr_b16 v[216:217], v242 offset:50816
ds_read_b64_tr_b16 v[218:219], v242 offset:51840
v_mfma_f32_16x16x32_bf16 v[92:95], v[220:223], v[172:175], v[92:95]
s_waitcnt vmcnt(11) lgkmcnt(6)
s_add_u32 s80, s80, 131072
v_mfma_f32_16x16x32_bf16 v[124:127], v[220:223], v[188:191], v[124:127]
ds_read_b64_tr_b16 v[220:221], v243 offset:50816
ds_read_b64_tr_b16 v[222:223], v243 offset:51840
v_mfma_f32_16x16x32_bf16 v[0:3], v[192:195], a[0:3], v[0:3]
s_add_u32 m0, s83, 67584
s_bfe_u32 s55, s4, 262156
v_mfma_f32_16x16x32_bf16 v[32:35], v[192:195], a[4:7], v[32:35]
buffer_load_dwordx4 v224, s[56:59], s80 offen lds
v_mfma_f32_16x16x32_bf16 v[64:67], v[192:195], a[8:11], v[64:67]
s_xor_b32 s55, s55, s72
s_lshl_b32 s55, s55, 13
v_mfma_f32_16x16x32_bf16 v[96:99], v[192:195], a[12:15], v[96:99]
ds_read_b64_tr_b16 v[192:193], v240 offset:59136
ds_read_b64_tr_b16 v[194:195], v240 offset:60160
v_mfma_f32_16x16x32_bf16 v[4:7], v[196:199], a[0:3], v[4:7]
s_add_u32 m0, s83, 68608
s_bfe_u32 s52, s4, 262160
v_mfma_f32_16x16x32_bf16 v[36:39], v[196:199], a[4:7], v[36:39]
buffer_load_dwordx4 v225, s[56:59], s80 offen lds
v_mfma_f32_16x16x32_bf16 v[68:71], v[196:199], a[8:11], v[68:71]
s_xor_b32 s52, s52, s72
s_lshl_b32 s52, s52, 13
v_mfma_f32_16x16x32_bf16 v[100:103], v[196:199], a[12:15], v[100:103]
ds_read_b64_tr_b16 v[196:197], v241 offset:59136
ds_read_b64_tr_b16 v[198:199], v241 offset:60160
v_mfma_f32_16x16x32_bf16 v[8:11], v[200:203], a[0:3], v[8:11]
v_mfma_f32_16x16x32_bf16 v[40:43], v[200:203], a[4:7], v[40:43]
buffer_load_dwordx2 v[250:251], v248, s[64:67], s5 offen sc1
v_mfma_f32_16x16x32_bf16 v[72:75], v[200:203], a[8:11], v[72:75]
v_mfma_f32_16x16x32_bf16 v[104:107], v[200:203], a[12:15], v[104:107]
ds_read_b64_tr_b16 v[200:201], v242 offset:59136
ds_read_b64_tr_b16 v[202:203], v242 offset:60160
v_mfma_f32_16x16x32_bf16 v[12:15], v[204:207], a[0:3], v[12:15]
s_add_u32 m0, s83, 69760
v_mfma_f32_16x16x32_bf16 v[44:47], v[204:207], a[4:7], v[44:47]
buffer_load_dwordx4 v226, s[56:59], s80 offen lds
v_mfma_f32_16x16x32_bf16 v[76:79], v[204:207], a[8:11], v[76:79]
s_waitcnt vmcnt(11) lgkmcnt(2)
v_mfma_f32_16x16x32_bf16 v[108:111], v[204:207], a[12:15], v[108:111]
ds_read_b64_tr_b16 v[204:205], v243 offset:59136
ds_read_b64_tr_b16 v[206:207], v243 offset:60160
v_mfma_f32_16x16x32_bf16 v[16:19], v[208:211], a[0:3], v[16:19]
s_add_u32 m0, s83, 70784
v_mfma_f32_16x16x32_bf16 v[48:51], v[208:211], a[4:7], v[48:51]
buffer_load_dwordx4 v227, s[56:59], s80 offen lds
v_mfma_f32_16x16x32_bf16 v[80:83], v[208:211], a[8:11], v[80:83]
s_bfe_u32 s80, s4, 262156
s_xor_b32 s80, s80, s72
v_mfma_f32_16x16x32_bf16 v[112:115], v[208:211], a[12:15], v[112:115]
ds_read_b64_tr_b16 v[208:209], v240 offset:59264
ds_read_b64_tr_b16 v[210:211], v240 offset:60288
v_mfma_f32_16x16x32_bf16 v[20:23], v[212:215], a[0:3], v[20:23]
s_lshl_b32 s80, s80, 18
v_mfma_f32_16x16x32_bf16 v[52:55], v[212:215], a[4:7], v[52:55]
buffer_load_dwordx4 a[48:51], v244, s[44:47], s53 offen offset:3072 sc1
v_mfma_f32_16x16x32_bf16 v[84:87], v[212:215], a[8:11], v[84:87]
v_mfma_f32_16x16x32_bf16 v[116:119], v[212:215], a[12:15], v[116:119]
ds_read_b64_tr_b16 v[212:213], v241 offset:59264
ds_read_b64_tr_b16 v[214:215], v241 offset:60288
v_mfma_f32_16x16x32_bf16 v[24:27], v[216:219], a[0:3], v[24:27]
v_mfma_f32_16x16x32_bf16 v[56:59], v[216:219], a[4:7], v[56:59]
buffer_load_dwordx4 a[52:55], v245, s[44:47], s53 offen offset:3072 sc1
v_mfma_f32_16x16x32_bf16 v[88:91], v[216:219], a[8:11], v[88:91]
v_mfma_f32_16x16x32_bf16 v[120:123], v[216:219], a[12:15], v[120:123]
ds_read_b64_tr_b16 v[216:217], v242 offset:59264
ds_read_b64_tr_b16 v[218:219], v242 offset:60288
v_mfma_f32_16x16x32_bf16 v[28:31], v[220:223], a[0:3], v[28:31]
v_mfma_f32_16x16x32_bf16 v[60:63], v[220:223], a[4:7], v[60:63]
buffer_load_dwordx4 a[0:3], v246, s[44:47], s53 offen offset:3072 sc1
v_mfma_f32_16x16x32_bf16 v[92:95], v[220:223], a[8:11], v[92:95]
v_mfma_f32_16x16x32_bf16 v[124:127], v[220:223], a[12:15], v[124:127]
ds_read_b64_tr_b16 v[220:221], v243 offset:59264
ds_read_b64_tr_b16 v[222:223], v243 offset:60288
v_mfma_f32_16x16x32_bf16 v[0:3], v[192:195], a[16:19], v[0:3]
v_mfma_f32_16x16x32_bf16 v[32:35], v[192:195], a[20:23], v[32:35]
buffer_load_dwordx4 a[4:7], v247, s[44:47], s53 offen offset:3072 sc1
v_mfma_f32_16x16x32_bf16 v[64:67], v[192:195], a[24:27], v[64:67]
s_bfe_u32 s53, s4, 262164
s_xor_b32 s53, s53, s72
v_mfma_f32_16x16x32_bf16 v[96:99], v[192:195], a[28:31], v[96:99]
s_lshl_b32 s53, s53, 13
v_mfma_f32_16x16x32_bf16 v[4:7], v[196:199], a[16:19], v[4:7]
s_add_u32 m0, s83, 84480
s_waitcnt lgkmcnt(0)
s_barrier
v_mfma_f32_16x16x32_bf16 v[36:39], v[196:199], a[20:23], v[36:39]
buffer_load_dwordx4 v224, s[56:59], s80 offen lds
v_mfma_f32_16x16x32_bf16 v[68:71], v[196:199], a[24:27], v[68:71]
ds_read_b64_tr_b16 v[192:193], v240 offset:0
ds_read_b64_tr_b16 v[194:195], v240 offset:1024
s_nop 0
v_mfma_f32_16x16x32_bf16 v[100:103], v[196:199], a[28:31], v[100:103]
ds_read_b64_tr_b16 v[196:197], v241 offset:0
ds_read_b64_tr_b16 v[198:199], v241 offset:1024
v_mfma_f32_16x16x32_bf16 v[8:11], v[200:203], a[16:19], v[8:11]
s_add_u32 m0, s83, 85504
v_mfma_f32_16x16x32_bf16 v[40:43], v[200:203], a[20:23], v[40:43]
buffer_load_dwordx4 v225, s[56:59], s80 offen lds
v_mfma_f32_16x16x32_bf16 v[72:75], v[200:203], a[24:27], v[72:75]
v_mfma_f32_16x16x32_bf16 v[104:107], v[200:203], a[28:31], v[104:107]
ds_read_b64_tr_b16 v[200:201], v242 offset:0
ds_read_b64_tr_b16 v[202:203], v242 offset:1024
v_mfma_f32_16x16x32_bf16 v[12:15], v[204:207], a[16:19], v[12:15]
s_add_u32 m0, s83, 86656
v_mfma_f32_16x16x32_bf16 v[44:47], v[204:207], a[20:23], v[44:47]
buffer_load_dwordx4 v226, s[56:59], s80 offen lds
v_mfma_f32_16x16x32_bf16 v[76:79], v[204:207], a[24:27], v[76:79]
v_mfma_f32_16x16x32_bf16 v[108:111], v[204:207], a[28:31], v[108:111]
ds_read_b64_tr_b16 v[204:205], v243 offset:0
ds_read_b64_tr_b16 v[206:207], v243 offset:1024
v_mfma_f32_16x16x32_bf16 v[16:19], v[208:211], a[16:19], v[16:19]
s_add_u32 m0, s83, 87680
v_mfma_f32_16x16x32_bf16 v[48:51], v[208:211], a[20:23], v[48:51]
buffer_load_dwordx4 v227, s[56:59], s80 offen lds
v_mfma_f32_16x16x32_bf16 v[80:83], v[208:211], a[24:27], v[80:83]
s_add_u32 s80, s80, 131072
s_waitcnt vmcnt(10)
v_mfma_f32_16x16x32_bf16 v[112:115], v[208:211], a[28:31], v[112:115]
ds_read_b64_tr_b16 v[208:209], v240 offset:128
ds_read_b64_tr_b16 v[210:211], v240 offset:1152
v_mfma_f32_16x16x32_bf16 v[20:23], v[212:215], a[16:19], v[20:23]
s_add_u32 m0, s83, 101376
v_cmp_eq_u64 vcc, v[250:251], v[252:253]
v_mfma_f32_16x16x32_bf16 v[52:55], v[212:215], a[20:23], v[52:55]
buffer_load_dwordx4 v224, s[56:59], s80 offen lds
v_mfma_f32_16x16x32_bf16 v[84:87], v[212:215], a[24:27], v[84:87]
s_bitcmp1_b32 vcc_lo, 2
s_cbranch_scc0 .La5_LO_N2
.La5_LO_K2:
v_mfma_f32_16x16x32_bf16 v[116:119], v[212:215], a[28:31], v[116:119]
ds_read_b64_tr_b16 v[212:213], v241 offset:128
ds_read_b64_tr_b16 v[214:215], v241 offset:1152
v_mfma_f32_16x16x32_bf16 v[24:27], v[216:219], a[16:19], v[24:27]
v_mfma_f32_16x16x32_bf16 v[56:59], v[216:219], a[20:23], v[56:59]
buffer_load_dwordx4 a[8:11], v244, s[44:47], s54 offen offset:0 sc1
v_mfma_f32_16x16x32_bf16 v[88:91], v[216:219], a[24:27], v[88:91]
v_mfma_f32_16x16x32_bf16 v[120:123], v[216:219], a[28:31], v[120:123]
ds_read_b64_tr_b16 v[216:217], v242 offset:128
ds_read_b64_tr_b16 v[218:219], v242 offset:1152
v_mfma_f32_16x16x32_bf16 v[28:31], v[220:223], a[16:19], v[28:31]
v_mfma_f32_16x16x32_bf16 v[60:63], v[220:223], a[20:23], v[60:63]
v_mfma_f32_16x16x32_bf16 v[92:95], v[220:223], a[24:27], v[92:95]
s_waitcnt lgkmcnt(2)
v_mfma_f32_16x16x32_bf16 v[124:127], v[220:223], a[28:31], v[124:127]
ds_read_b64_tr_b16 v[220:221], v243 offset:128
ds_read_b64_tr_b16 v[222:223], v243 offset:1152
v_mfma_f32_16x16x32_bf16 v[0:3], v[192:195], a[32:35], v[0:3]
.La5_LW3_0:
s_cbranch_execz .La5_LW3_4
v_mfma_f32_16x16x32_bf16 v[32:35], v[192:195], a[36:39], v[32:35]
buffer_load_dwordx4 a[12:15], v245, s[44:47], s54 offen offset:0 sc1
v_mfma_f32_16x16x32_bf16 v[64:67], v[192:195], a[40:43], v[64:67]
v_mfma_f32_16x16x32_bf16 v[96:99], v[192:195], a[44:47], v[96:99]
ds_read_b64_tr_b16 v[192:193], v240 offset:8448
ds_read_b64_tr_b16 v[194:195], v240 offset:9472
v_mfma_f32_16x16x32_bf16 v[4:7], v[196:199], a[32:35], v[4:7]
v_mfma_f32_16x16x32_bf16 v[36:39], v[196:199], a[36:39], v[36:39]
buffer_load_dwordx4 a[16:19], v246, s[44:47], s54 offen offset:0 sc1
v_mfma_f32_16x16x32_bf16 v[68:71], v[196:199], a[40:43], v[68:71]
v_mfma_f32_16x16x32_bf16 v[100:103], v[196:199], a[44:47], v[100:103]
ds_read_b64_tr_b16 v[196:197], v241 offset:8448
ds_read_b64_tr_b16 v[198:199], v241 offset:9472
v_mfma_f32_16x16x32_bf16 v[8:11], v[200:203], a[32:35], v[8:11]
v_mfma_f32_16x16x32_bf16 v[40:43], v[200:203], a[36:39], v[40:43]
buffer_load_dwordx4 a[20:23], v247, s[44:47], s54 offen offset:0 sc1
v_mfma_f32_16x16x32_bf16 v[72:75], v[200:203], a[40:43], v[72:75]
v_mfma_f32_16x16x32_bf16 v[104:107], v[200:203], a[44:47], v[104:107]
ds_read_b64_tr_b16 v[200:201], v242 offset:8448
ds_read_b64_tr_b16 v[202:203], v242 offset:9472
v_mfma_f32_16x16x32_bf16 v[12:15], v[204:207], a[32:35], v[12:15]
v_mfma_f32_16x16x32_bf16 v[44:47], v[204:207], a[36:39], v[44:47]
buffer_load_dwordx4 a[24:27], v244, s[44:47], s54 offen offset:1024 sc1
v_mfma_f32_16x16x32_bf16 v[76:79], v[204:207], a[40:43], v[76:79]
v_mfma_f32_16x16x32_bf16 v[108:111], v[204:207], a[44:47], v[108:111]
ds_read_b64_tr_b16 v[204:205], v243 offset:8448
ds_read_b64_tr_b16 v[206:207], v243 offset:9472
v_mfma_f32_16x16x32_bf16 v[16:19], v[208:211], a[32:35], v[16:19]
v_mfma_f32_16x16x32_bf16 v[48:51], v[208:211], a[36:39], v[48:51]
buffer_load_dwordx4 a[28:31], v245, s[44:47], s54 offen offset:1024 sc1
v_mfma_f32_16x16x32_bf16 v[80:83], v[208:211], a[40:43], v[80:83]
v_mfma_f32_16x16x32_bf16 v[112:115], v[208:211], a[44:47], v[112:115]
ds_read_b64_tr_b16 v[208:209], v240 offset:8576
ds_read_b64_tr_b16 v[210:211], v240 offset:9600
v_mfma_f32_16x16x32_bf16 v[20:23], v[212:215], a[32:35], v[20:23]
v_add_u32 v240, 67584, v240
v_mfma_f32_16x16x32_bf16 v[52:55], v[212:215], a[36:39], v[52:55]
buffer_load_dwordx4 a[56:59], v246, s[44:47], s54 offen offset:1024 sc1
v_mfma_f32_16x16x32_bf16 v[84:87], v[212:215], a[40:43], v[84:87]
s_waitcnt vmcnt(12) lgkmcnt(2)
v_mfma_f32_16x16x32_bf16 v[116:119], v[212:215], a[44:47], v[116:119]
ds_read_b64_tr_b16 v[212:213], v241 offset:8576
ds_read_b64_tr_b16 v[214:215], v241 offset:9600
v_mfma_f32_16x16x32_bf16 v[24:27], v[216:219], a[32:35], v[24:27]
v_add_u32 v241, 67584, v241
v_mfma_f32_16x16x32_bf16 v[56:59], v[216:219], a[36:39], v[56:59]
buffer_load_dwordx4 a[60:63], v247, s[44:47], s54 offen offset:1024 sc1
v_mfma_f32_16x16x32_bf16 v[88:91], v[216:219], a[40:43], v[88:91]
v_mfma_f32_16x16x32_bf16 v[120:123], v[216:219], a[44:47], v[120:123]
ds_read_b64_tr_b16 v[216:217], v242 offset:8576
ds_read_b64_tr_b16 v[218:219], v242 offset:9600
v_mfma_f32_16x16x32_bf16 v[28:31], v[220:223], a[32:35], v[28:31]
v_add_u32 v242, 67584, v242
v_mfma_f32_16x16x32_bf16 v[60:63], v[220:223], a[36:39], v[60:63]
v_mfma_f32_16x16x32_bf16 v[92:95], v[220:223], a[40:43], v[92:95]
v_mfma_f32_16x16x32_bf16 v[124:127], v[220:223], a[44:47], v[124:127]
ds_read_b64_tr_b16 v[220:221], v243 offset:8576
ds_read_b64_tr_b16 v[222:223], v243 offset:9600
v_mfma_f32_16x16x32_bf16 v[0:3], v[192:195], a[48:51], v[0:3]
s_add_u32 m0, s83, 102400
v_add_u32 v243, 67584, v243
v_mfma_f32_16x16x32_bf16 v[32:35], v[192:195], a[52:55], v[32:35]
buffer_load_dwordx4 v225, s[56:59], s80 offen lds
v_mfma_f32_16x16x32_bf16 v[64:67], v[192:195], a[0:3], v[64:67]
v_mfma_f32_16x16x32_bf16 v[96:99], v[192:195], a[4:7], v[96:99]
v_mfma_f32_16x16x32_bf16 v[4:7], v[196:199], a[48:51], v[4:7]
s_add_u32 m0, s83, 103552
s_waitcnt lgkmcnt(0)
s_barrier
v_mfma_f32_16x16x32_bf16 v[36:39], v[196:199], a[52:55], v[36:39]
buffer_load_dwordx4 v226, s[56:59], s80 offen lds
v_mfma_f32_16x16x32_bf16 v[68:71], v[196:199], a[0:3], v[68:71]
ds_read_b64_tr_b16 v[192:193], v240 offset:50688
ds_read_b64_tr_b16 v[194:195], v240 offset:51712
v_mfma_f32_16x16x32_bf16 v[100:103], v[196:199], a[4:7], v[100:103]
ds_read_b64_tr_b16 v[196:197], v241 offset:50688
ds_read_b64_tr_b16 v[198:199], v241 offset:51712
v_mfma_f32_16x16x32_bf16 v[8:11], v[200:203], a[48:51], v[8:11]
v_mfma_f32_16x16x32_bf16 v[40:43], v[200:203], a[52:55], v[40:43]
buffer_load_dwordx4 a[32:35], v244, s[44:47], s54 offen offset:2048 sc1
v_mfma_f32_16x16x32_bf16 v[72:75], v[200:203], a[0:3], v[72:75]
v_mfma_f32_16x16x32_bf16 v[104:107], v[200:203], a[4:7], v[104:107]
ds_read_b64_tr_b16 v[200:201], v242 offset:50688
ds_read_b64_tr_b16 v[202:203], v242 offset:51712
v_mfma_f32_16x16x32_bf16 v[12:15], v[204:207], a[48:51], v[12:15]
v_mfma_f32_16x16x32_bf16 v[44:47], v[204:207], a[52:55], v[44:47]
buffer_load_dwordx4 a[36:39], v245, s[44:47], s54 offen offset:2048 sc1
v_mfma_f32_16x16x32_bf16 v[76:79], v[204:207], a[0:3], v[76:79]
s_waitcnt vmcnt(8) lgkmcnt(2)
v_mfma_f32_16x16x32_bf16 v[108:111], v[204:207], a[4:7], v[108:111]
ds_read_b64_tr_b16 v[204:205], v243 offset:50688
ds_read_b64_tr_b16 v[206:207], v243 offset:51712
v_mfma_f32_16x16x32_bf16 v[16:19], v[208:211], a[48:51], v[16:19]
v_mfma_f32_16x16x32_bf16 v[48:51], v[208:211], a[52:55], v[48:51]
buffer_load_dwordx4 a[40:43], v246, s[44:47], s54 offen offset:2048 sc1
v_mfma_f32_16x16x32_bf16 v[80:83], v[208:211], a[0:3], v[80:83]
v_mfma_f32_16x16x32_bf16 v[112:115], v[208:211], a[4:7], v[112:115]
ds_read_b64_tr_b16 v[208:209], v240 offset:50816
ds_read_b64_tr_b16 v[210:211], v240 offset:51840
v_mfma_f32_16x16x32_bf16 v[20:23], v[212:215], a[48:51], v[20:23]
v_mfma_f32_16x16x32_bf16 v[52:55], v[212:215], a[52:55], v[52:55]
buffer_load_dwordx4 a[44:47], v247, s[44:47], s54 offen offset:2048 sc1
v_mfma_f32_16x16x32_bf16 v[84:87], v[212:215], a[0:3], v[84:87]
v_mfma_f32_16x16x32_bf16 v[116:119], v[212:215], a[4:7], v[116:119]
ds_read_b64_tr_b16 v[212:213], v241 offset:50816
ds_read_b64_tr_b16 v[214:215], v241 offset:51840
v_mfma_f32_16x16x32_bf16 v[24:27], v[216:219], a[48:51], v[24:27]
v_mfma_f32_16x16x32_bf16 v[56:59], v[216:219], a[52:55], v[56:59]
buffer_load_dwordx2 v[250:251], v248, s[64:67], s5 offen sc1
v_mfma_f32_16x16x32_bf16 v[88:91], v[216:219], a[0:3], v[88:91]
v_mfma_f32_16x16x32_bf16 v[120:123], v[216:219], a[4:7], v[120:123]
ds_read_b64_tr_b16 v[216:217], v242 offset:50816
ds_read_b64_tr_b16 v[218:219], v242 offset:51840
v_mfma_f32_16x16x32_bf16 v[28:31], v[220:223], a[48:51], v[28:31]
s_add_u32 m0, s83, 104576
v_mfma_f32_16x16x32_bf16 v[60:63], v[220:223], a[52:55], v[60:63]
buffer_load_dwordx4 v227, s[56:59], s80 offen lds
v_mfma_f32_16x16x32_bf16 v[92:95], v[220:223], a[0:3], v[92:95]
s_bfe_u32 s80, s4, 262160
s_xor_b32 s80, s80, s72
v_mfma_f32_16x16x32_bf16 v[124:127], v[220:223], a[4:7], v[124:127]
ds_read_b64_tr_b16 v[220:221], v243 offset:50816
ds_read_b64_tr_b16 v[222:223], v243 offset:51840
v_mfma_f32_16x16x32_bf16 v[0:3], v[192:195], a[8:11], v[0:3]
s_lshl_b32 s80, s80, 18
v_mfma_f32_16x16x32_bf16 v[32:35], v[192:195], a[12:15], v[32:35]
buffer_load_dwordx4 a[0:3], v244, s[44:47], s54 offen offset:3072 sc1
v_mfma_f32_16x16x32_bf16 v[64:67], v[192:195], a[16:19], v[64:67]
v_mfma_f32_16x16x32_bf16 v[96:99], v[192:195], a[20:23], v[96:99]
ds_read_b64_tr_b16 v[192:193], v240 offset:59136
ds_read_b64_tr_b16 v[194:195], v240 offset:60160
v_mfma_f32_16x16x32_bf16 v[4:7], v[196:199], a[8:11], v[4:7]
v_mfma_f32_16x16x32_bf16 v[36:39], v[196:199], a[12:15], v[36:39]
buffer_load_dwordx4 a[4:7], v245, s[44:47], s54 offen offset:3072 sc1
v_mfma_f32_16x16x32_bf16 v[68:71], v[196:199], a[16:19], v[68:71]
s_waitcnt lgkmcnt(2)
v_mfma_f32_16x16x32_bf16 v[100:103], v[196:199], a[20:23], v[100:103]
ds_read_b64_tr_b16 v[196:197], v241 offset:59136
ds_read_b64_tr_b16 v[198:199], v241 offset:60160
v_mfma_f32_16x16x32_bf16 v[8:11], v[200:203], a[8:11], v[8:11]
v_mfma_f32_16x16x32_bf16 v[40:43], v[200:203], a[12:15], v[40:43]
buffer_load_dwordx4 a[48:51], v246, s[44:47], s54 offen offset:3072 sc1
v_mfma_f32_16x16x32_bf16 v[72:75], v[200:203], a[16:19], v[72:75]
v_mfma_f32_16x16x32_bf16 v[104:107], v[200:203], a[20:23], v[104:107]
ds_read_b64_tr_b16 v[200:201], v242 offset:59136
ds_read_b64_tr_b16 v[202:203], v242 offset:60160
v_mfma_f32_16x16x32_bf16 v[12:15], v[204:207], a[8:11], v[12:15]
s_bitcmp1_b32 vcc_lo, 3
s_cbranch_scc0 .La5_LO_N3
.La5_LO_K3:
v_mfma_f32_16x16x32_bf16 v[44:47], v[204:207], a[12:15], v[44:47]
buffer_load_dwordx4 a[52:55], v247, s[44:47], s54 offen offset:3072 sc1
v_mfma_f32_16x16x32_bf16 v[76:79], v[204:207], a[16:19], v[76:79]
s_bfe_u32 s54, s4, 262168
s_xor_b32 s54, s54, s72
v_mfma_f32_16x16x32_bf16 v[108:111], v[204:207], a[20:23], v[108:111]
ds_read_b64_tr_b16 v[204:205], v243 offset:59136
ds_read_b64_tr_b16 v[206:207], v243 offset:60160
v_mfma_f32_16x16x32_bf16 v[16:19], v[208:211], a[8:11], v[16:19]
s_lshl_b32 s54, s54, 13
v_mfma_f32_16x16x32_bf16 v[48:51], v[208:211], a[12:15], v[48:51]
buffer_load_dwordx4 a[64:67], v244, s[44:47], s55 offen offset:0 sc1
v_mfma_f32_16x16x32_bf16 v[80:83], v[208:211], a[16:19], v[80:83]
v_mfma_f32_16x16x32_bf16 v[112:115], v[208:211], a[20:23], v[112:115]
ds_read_b64_tr_b16 v[208:209], v240 offset:59264
ds_read_b64_tr_b16 v[210:211], v240 offset:60288
v_mfma_f32_16x16x32_bf16 v[20:23], v[212:215], a[8:11], v[20:23]
v_mfma_f32_16x16x32_bf16 v[52:55], v[212:215], a[12:15], v[52:55]
buffer_load_dwordx4 a[68:71], v245, s[44:47], s55 offen offset:0 sc1
v_mfma_f32_16x16x32_bf16 v[84:87], v[212:215], a[16:19], v[84:87]
v_mfma_f32_16x16x32_bf16 v[116:119], v[212:215], a[20:23], v[116:119]
ds_read_b64_tr_b16 v[212:213], v241 offset:59264
ds_read_b64_tr_b16 v[214:215], v241 offset:60288
v_mfma_f32_16x16x32_bf16 v[24:27], v[216:219], a[8:11], v[24:27]
v_mfma_f32_16x16x32_bf16 v[56:59], v[216:219], a[12:15], v[56:59]
buffer_load_dwordx4 a[72:75], v246, s[44:47], s55 offen offset:0 sc1
v_mfma_f32_16x16x32_bf16 v[88:91], v[216:219], a[16:19], v[88:91]
v_mfma_f32_16x16x32_bf16 v[120:123], v[216:219], a[20:23], v[120:123]
ds_read_b64_tr_b16 v[216:217], v242 offset:59264
ds_read_b64_tr_b16 v[218:219], v242 offset:60288
v_mfma_f32_16x16x32_bf16 v[28:31], v[220:223], a[8:11], v[28:31]
v_mfma_f32_16x16x32_bf16 v[60:63], v[220:223], a[12:15], v[60:63]
v_mfma_f32_16x16x32_bf16 v[92:95], v[220:223], a[16:19], v[92:95]
s_waitcnt vmcnt(15) lgkmcnt(2)
v_mfma_f32_16x16x32_bf16 v[124:127], v[220:223], a[20:23], v[124:127]
ds_read_b64_tr_b16 v[220:221], v243 offset:59264
ds_read_b64_tr_b16 v[222:223], v243 offset:60288
v_mfma_f32_16x16x32_bf16 v[0:3], v[192:195], a[24:27], v[0:3]
v_mfma_f32_16x16x32_bf16 v[32:35], v[192:195], a[28:31], v[32:35]
buffer_load_dwordx4 a[8:11], v247, s[44:47], s55 offen offset:0 sc1
v_mfma_f32_16x16x32_bf16 v[64:67], v[192:195], a[56:59], v[64:67]
v_mfma_f32_16x16x32_bf16 v[96:99], v[192:195], a[60:63], v[96:99]
ds_read_b64_tr_b16 v[192:193], v240 offset:0
ds_read_b64_tr_b16 v[194:195], v240 offset:1024
v_mfma_f32_16x16x32_bf16 v[4:7], v[196:199], a[24:27], v[4:7]
v_mfma_f32_16x16x32_bf16 v[36:39], v[196:199], a[28:31], v[36:39]
buffer_load_dwordx4 a[12:15], v244, s[44:47], s55 offen offset:1024 sc1
v_mfma_f32_16x16x32_bf16 v[68:71], v[196:199], a[56:59], v[68:71]
v_mfma_f32_16x16x32_bf16 v[100:103], v[196:199], a[60:63], v[100:103]
ds_read_b64_tr_b16 v[196:197], v241 offset:0
ds_read_b64_tr_b16 v[198:199], v241 offset:1024
v_mfma_f32_16x16x32_bf16 v[8:11], v[200:203], a[24:27], v[8:11]
v_mfma_f32_16x16x32_bf16 v[40:43], v[200:203], a[28:31], v[40:43]
buffer_load_dwordx4 a[16:19], v245, s[44:47], s55 offen offset:1024 sc1
v_mfma_f32_16x16x32_bf16 v[72:75], v[200:203], a[56:59], v[72:75]
v_mfma_f32_16x16x32_bf16 v[104:107], v[200:203], a[60:63], v[104:107]
ds_read_b64_tr_b16 v[200:201], v242 offset:0
ds_read_b64_tr_b16 v[202:203], v242 offset:1024
v_mfma_f32_16x16x32_bf16 v[12:15], v[204:207], a[24:27], v[12:15]
s_load_dwordx2 s[16:17], s[0:1], 0
s_load_dwordx4 s[36:39], s[0:1], 80
v_mfma_f32_16x16x32_bf16 v[44:47], v[204:207], a[28:31], v[44:47]
buffer_load_dwordx4 a[20:23], v246, s[44:47], s55 offen offset:1024 sc1
v_mfma_f32_16x16x32_bf16 v[76:79], v[204:207], a[56:59], v[76:79]
v_mfma_f32_16x16x32_bf16 v[108:111], v[204:207], a[60:63], v[108:111]
ds_read_b64_tr_b16 v[204:205], v243 offset:0
ds_read_b64_tr_b16 v[206:207], v243 offset:1024
v_mfma_f32_16x16x32_bf16 v[16:19], v[208:211], a[24:27], v[16:19]
v_mfma_f32_16x16x32_bf16 v[48:51], v[208:211], a[28:31], v[48:51]
buffer_load_dwordx4 a[76:79], v247, s[44:47], s55 offen offset:1024 sc1
v_mfma_f32_16x16x32_bf16 v[80:83], v[208:211], a[56:59], v[80:83]
v_mfma_f32_16x16x32_bf16 v[112:115], v[208:211], a[60:63], v[112:115]
ds_read_b64_tr_b16 v[208:209], v240 offset:128
ds_read_b64_tr_b16 v[210:211], v240 offset:1152
v_mfma_f32_16x16x32_bf16 v[20:23], v[212:215], a[24:27], v[20:23]
s_add_u32 m0, s83, 16896
v_mfma_f32_16x16x32_bf16 v[52:55], v[212:215], a[28:31], v[52:55]
buffer_load_dwordx4 v224, s[56:59], s80 offen lds
v_mfma_f32_16x16x32_bf16 v[84:87], v[212:215], a[56:59], v[84:87]
s_waitcnt vmcnt(15) lgkmcnt(2)
v_mfma_f32_16x16x32_bf16 v[116:119], v[212:215], a[60:63], v[116:119]
ds_read_b64_tr_b16 v[212:213], v241 offset:128
ds_read_b64_tr_b16 v[214:215], v241 offset:1152
v_mfma_f32_16x16x32_bf16 v[24:27], v[216:219], a[24:27], v[24:27]
s_add_u32 m0, s83, 17920
v_mfma_f32_16x16x32_bf16 v[56:59], v[216:219], a[28:31], v[56:59]
buffer_load_dwordx4 v225, s[56:59], s80 offen lds
v_mfma_f32_16x16x32_bf16 v[88:91], v[216:219], a[56:59], v[88:91]
v_mfma_f32_16x16x32_bf16 v[120:123], v[216:219], a[60:63], v[120:123]
ds_read_b64_tr_b16 v[216:217], v242 offset:128
ds_read_b64_tr_b16 v[218:219], v242 offset:1152
v_mfma_f32_16x16x32_bf16 v[28:31], v[220:223], a[24:27], v[28:31]
v_mfma_f32_16x16x32_bf16 v[60:63], v[220:223], a[28:31], v[60:63]
v_mfma_f32_16x16x32_bf16 v[92:95], v[220:223], a[56:59], v[92:95]
v_mfma_f32_16x16x32_bf16 v[124:127], v[220:223], a[60:63], v[124:127]
ds_read_b64_tr_b16 v[220:221], v243 offset:128
ds_read_b64_tr_b16 v[222:223], v243 offset:1152
v_mfma_f32_16x16x32_bf16 v[0:3], v[192:195], a[32:35], v[0:3]
s_add_u32 m0, s83, 19072
v_mfma_f32_16x16x32_bf16 v[32:35], v[192:195], a[36:39], v[32:35]
buffer_load_dwordx4 v226, s[56:59], s80 offen lds
v_mfma_f32_16x16x32_bf16 v[64:67], v[192:195], a[40:43], v[64:67]
v_mfma_f32_16x16x32_bf16 v[96:99], v[192:195], a[44:47], v[96:99]
ds_read_b64_tr_b16 v[192:193], v240 offset:8448
ds_read_b64_tr_b16 v[194:195], v240 offset:9472
v_mfma_f32_16x16x32_bf16 v[4:7], v[196:199], a[32:35], v[4:7]
v_mfma_f32_16x16x32_bf16 v[36:39], v[196:199], a[36:39], v[36:39]
buffer_load_dwordx4 a[24:27], v244, s[44:47], s55 offen offset:2048 sc1
v_mfma_f32_16x16x32_bf16 v[68:71], v[196:199], a[40:43], v[68:71]
v_mfma_f32_16x16x32_bf16 v[100:103], v[196:199], a[44:47], v[100:103]
ds_read_b64_tr_b16 v[196:197], v241 offset:8448
ds_read_b64_tr_b16 v[198:199], v241 offset:9472
v_mfma_f32_16x16x32_bf16 v[8:11], v[200:203], a[32:35], v[8:11]
v_mfma_f32_16x16x32_bf16 v[40:43], v[200:203], a[36:39], v[40:43]
buffer_load_dwordx4 a[28:31], v245, s[44:47], s55 offen offset:2048 sc1
v_mfma_f32_16x16x32_bf16 v[72:75], v[200:203], a[40:43], v[72:75]
v_mfma_f32_16x16x32_bf16 v[104:107], v[200:203], a[44:47], v[104:107]
ds_read_b64_tr_b16 v[200:201], v242 offset:8448
ds_read_b64_tr_b16 v[202:203], v242 offset:9472
v_mfma_f32_16x16x32_bf16 v[12:15], v[204:207], a[32:35], v[12:15]
v_mfma_f32_16x16x32_bf16 v[44:47], v[204:207], a[36:39], v[44:47]
buffer_load_dwordx4 a[56:59], v246, s[44:47], s55 offen offset:2048 sc1
v_mfma_f32_16x16x32_bf16 v[76:79], v[204:207], a[40:43], v[76:79]
s_waitcnt vmcnt(14) lgkmcnt(2)
v_mfma_f32_16x16x32_bf16 v[108:111], v[204:207], a[44:47], v[108:111]
ds_read_b64_tr_b16 v[204:205], v243 offset:8448
ds_read_b64_tr_b16 v[206:207], v243 offset:9472
v_mfma_f32_16x16x32_bf16 v[16:19], v[208:211], a[32:35], v[16:19]
v_mfma_f32_16x16x32_bf16 v[48:51], v[208:211], a[36:39], v[48:51]
buffer_load_dwordx4 a[60:63], v247, s[44:47], s55 offen offset:2048 sc1
v_mfma_f32_16x16x32_bf16 v[80:83], v[208:211], a[40:43], v[80:83]
v_mfma_f32_16x16x32_bf16 v[112:115], v[208:211], a[44:47], v[112:115]
ds_read_b64_tr_b16 v[208:209], v240 offset:8576
ds_read_b64_tr_b16 v[210:211], v240 offset:9600
v_mfma_f32_16x16x32_bf16 v[20:23], v[212:215], a[32:35], v[20:23]
s_add_u32 m0, s83, 20096
v_mfma_f32_16x16x32_bf16 v[52:55], v[212:215], a[36:39], v[52:55]
buffer_load_dwordx4 v227, s[56:59], s80 offen lds
v_mfma_f32_16x16x32_bf16 v[84:87], v[212:215], a[40:43], v[84:87]
s_add_u32 s80, s80, 131072
v_mfma_f32_16x16x32_bf16 v[116:119], v[212:215], a[44:47], v[116:119]
ds_read_b64_tr_b16 v[212:213], v241 offset:8576
ds_read_b64_tr_b16 v[214:215], v241 offset:9600
v_mfma_f32_16x16x32_bf16 v[24:27], v[216:219], a[32:35], v[24:27]
s_add_u32 m0, s83, 33792
v_mfma_f32_16x16x32_bf16 v[56:59], v[216:219], a[36:39], v[56:59]
buffer_load_dwordx4 v224, s[56:59], s80 offen lds
v_mfma_f32_16x16x32_bf16 v[88:91], v[216:219], a[40:43], v[88:91]
v_mfma_f32_16x16x32_bf16 v[120:123], v[216:219], a[44:47], v[120:123]
ds_read_b64_tr_b16 v[216:217], v242 offset:8576
ds_read_b64_tr_b16 v[218:219], v242 offset:9600
v_mfma_f32_16x16x32_bf16 v[28:31], v[220:223], a[32:35], v[28:31]
v_mfma_f32_16x16x32_bf16 v[60:63], v[220:223], a[36:39], v[60:63]
s_nop 0
v_cmp_eq_u64 vcc, v[250:251], v[252:253]
v_mfma_f32_16x16x32_bf16 v[92:95], v[220:223], a[40:43], v[92:95]
v_mfma_f32_16x16x32_bf16 v[124:127], v[220:223], a[44:47], v[124:127]
ds_read_b64_tr_b16 v[220:221], v243 offset:8576
ds_read_b64_tr_b16 v[222:223], v243 offset:9600
v_mfma_f32_16x16x32_bf16 v[0:3], v[192:195], a[0:3], v[0:3]
s_add_u32 m0, s83, 34816
v_mfma_f32_16x16x32_bf16 v[32:35], v[192:195], a[4:7], v[32:35]
buffer_load_dwordx4 v225, s[56:59], s80 offen lds
v_mfma_f32_16x16x32_bf16 v[64:67], v[192:195], a[48:51], v[64:67]
v_mfma_f32_16x16x32_bf16 v[96:99], v[192:195], a[52:55], v[96:99]
v_mfma_f32_16x16x32_bf16 v[4:7], v[196:199], a[0:3], v[4:7]
s_add_u32 m0, s83, 35968
s_waitcnt lgkmcnt(0)
s_barrier
v_mfma_f32_16x16x32_bf16 v[36:39], v[196:199], a[4:7], v[36:39]
buffer_load_dwordx4 v226, s[56:59], s80 offen lds
v_mfma_f32_16x16x32_bf16 v[68:71], v[196:199], a[48:51], v[68:71]
ds_read_b64_tr_b16 v[192:193], v240 offset:16896
ds_read_b64_tr_b16 v[194:195], v240 offset:17920
s_nop 0
v_mfma_f32_16x16x32_bf16 v[100:103], v[196:199], a[52:55], v[100:103]
ds_read_b64_tr_b16 v[196:197], v241 offset:16896
ds_read_b64_tr_b16 v[198:199], v241 offset:17920
v_mfma_f32_16x16x32_bf16 v[8:11], v[200:203], a[0:3], v[8:11]
s_lshl_b32 s92, s76, 5
s_add_u32 s92, s92, s72
v_mfma_f32_16x16x32_bf16 v[40:43], v[200:203], a[4:7], v[40:43]
buffer_load_dwordx2 v[250:251], v248, s[64:67], s5 offen sc1
v_mfma_f32_16x16x32_bf16 v[72:75], v[200:203], a[48:51], v[72:75]
s_lshl2_add_u32 s92, s92, s3
s_lshl_b32 s92, s92, 12
v_mfma_f32_16x16x32_bf16 v[104:107], v[200:203], a[52:55], v[104:107]
ds_read_b64_tr_b16 v[200:201], v242 offset:16896
ds_read_b64_tr_b16 v[202:203], v242 offset:17920
v_mfma_f32_16x16x32_bf16 v[12:15], v[204:207], a[0:3], v[12:15]
s_add_u32 m0, s83, 36992
v_mfma_f32_16x16x32_bf16 v[44:47], v[204:207], a[4:7], v[44:47]
buffer_load_dwordx4 v227, s[56:59], s80 offen lds
v_mfma_f32_16x16x32_bf16 v[76:79], v[204:207], a[48:51], v[76:79]
s_bfe_u32 s80, s4, 262164
s_xor_b32 s80, s80, s72
v_mfma_f32_16x16x32_bf16 v[108:111], v[204:207], a[52:55], v[108:111]
ds_read_b64_tr_b16 v[204:205], v243 offset:16896
ds_read_b64_tr_b16 v[206:207], v243 offset:17920
v_mfma_f32_16x16x32_bf16 v[16:19], v[208:211], a[0:3], v[16:19]
s_lshl_b32 s80, s80, 18
v_mfma_f32_16x16x32_bf16 v[48:51], v[208:211], a[4:7], v[48:51]
buffer_load_dwordx4 a[32:35], v244, s[44:47], s55 offen offset:3072 sc1
v_mfma_f32_16x16x32_bf16 v[80:83], v[208:211], a[48:51], v[80:83]
s_add_u32 s68, s16, s92
s_addc_u32 s69, s17, 0
v_mfma_f32_16x16x32_bf16 v[112:115], v[208:211], a[52:55], v[112:115]
ds_read_b64_tr_b16 v[208:209], v240 offset:17024
ds_read_b64_tr_b16 v[210:211], v240 offset:18048
v_mfma_f32_16x16x32_bf16 v[20:23], v[212:215], a[0:3], v[20:23]
s_and_b32 s69, s69, 0xffff
s_mov_b32 s70, 4096
v_mfma_f32_16x16x32_bf16 v[52:55], v[212:215], a[4:7], v[52:55]
buffer_load_dwordx4 a[36:39], v245, s[44:47], s55 offen offset:3072 sc1
v_mfma_f32_16x16x32_bf16 v[84:87], v[212:215], a[48:51], v[84:87]
s_mov_b32 s71, 0x20000
v_mfma_f32_16x16x32_bf16 v[116:119], v[212:215], a[52:55], v[116:119]
ds_read_b64_tr_b16 v[212:213], v241 offset:17024
ds_read_b64_tr_b16 v[214:215], v241 offset:18048
v_mfma_f32_16x16x32_bf16 v[24:27], v[216:219], a[0:3], v[24:27]
s_add_u32 s48, s36, 0
s_addc_u32 s49, s37, 0
v_mfma_f32_16x16x32_bf16 v[56:59], v[216:219], a[4:7], v[56:59]
buffer_load_dwordx4 a[40:43], v246, s[44:47], s55 offen offset:3072 sc1
v_mfma_f32_16x16x32_bf16 v[88:91], v[216:219], a[48:51], v[88:91]
s_and_b32 s49, s49, 0xffff
s_mov_b32 s50, 2048
v_mfma_f32_16x16x32_bf16 v[120:123], v[216:219], a[52:55], v[120:123]
ds_read_b64_tr_b16 v[216:217], v242 offset:17024
ds_read_b64_tr_b16 v[218:219], v242 offset:18048
v_mfma_f32_16x16x32_bf16 v[28:31], v[220:223], a[0:3], v[28:31]
s_bitcmp1_b32 vcc_lo, 4
s_cbranch_scc0 .La5_LO_N4
.La5_LO_K4:
v_mfma_f32_16x16x32_bf16 v[60:63], v[220:223], a[4:7], v[60:63]
buffer_load_dwordx4 a[0:3], v247, s[44:47], s55 offen offset:3072 sc1
v_mfma_f32_16x16x32_bf16 v[92:95], v[220:223], a[48:51], v[92:95]
s_waitcnt vmcnt(21) lgkmcnt(2)
s_bfe_u32 s55, s4, 262172
v_mfma_f32_16x16x32_bf16 v[124:127], v[220:223], a[52:55], v[124:127]
ds_read_b64_tr_b16 v[220:221], v243 offset:17024
ds_read_b64_tr_b16 v[222:223], v243 offset:18048
v_mfma_f32_16x16x32_bf16 v[0:3], v[192:195], a[64:67], v[0:3]
s_xor_b32 s55, s55, s72
s_lshl_b32 s55, s55, 13
v_mfma_f32_16x16x32_bf16 v[32:35], v[192:195], a[68:71], v[32:35]
buffer_load_dwordx4 a[4:7], v244, s[44:47], s52 offen offset:0 sc1
v_mfma_f32_16x16x32_bf16 v[64:67], v[192:195], a[72:75], v[64:67]
s_mov_b32 s51, 0x20000
v_and_b32 v238, 0xf0, v255
v_mfma_f32_16x16x32_bf16 v[96:99], v[192:195], a[8:11], v[96:99]
ds_read_b64_tr_b16 v[192:193], v240 offset:25344
ds_read_b64_tr_b16 v[194:195], v240 offset:26368
v_mfma_f32_16x16x32_bf16 v[4:7], v[196:199], a[64:67], v[4:7]
v_lshlrev_b32 v238, 2, v238
v_bfe_u32 v230, v255, 8, 1
v_mfma_f32_16x16x32_bf16 v[36:39], v[196:199], a[68:71], v[36:39]
buffer_load_dwordx4 a[44:47], v245, s[44:47], s52 offen offset:0 sc1
v_mfma_f32_16x16x32_bf16 v[68:71], v[196:199], a[72:75], v[68:71]
v_lshl_add_u32 v238, v230, 5, v238
v_lshrrev_b32 v230, 9, v255
v_mfma_f32_16x16x32_bf16 v[100:103], v[196:199], a[8:11], v[100:103]
ds_read_b64_tr_b16 v[196:197], v241 offset:25344
ds_read_b64_tr_b16 v[198:199], v241 offset:26368
v_mfma_f32_16x16x32_bf16 v[8:11], v[200:203], a[64:67], v[8:11]
v_lshl_add_u32 v238, v230, 4, v238
s_lshr_b32 s23, s72, 3
v_mfma_f32_16x16x32_bf16 v[40:43], v[200:203], a[68:71], v[40:43]
buffer_load_dwordx4 a[48:51], v246, s[44:47], s52 offen offset:0 sc1
v_mfma_f32_16x16x32_bf16 v[72:75], v[200:203], a[72:75], v[72:75]
s_lshl3_add_u32 s92, s23, s76
s_lshl_b32 s92, s92, 19
v_mfma_f32_16x16x32_bf16 v[104:107], v[200:203], a[8:11], v[104:107]
ds_read_b64_tr_b16 v[200:201], v242 offset:25344
ds_read_b64_tr_b16 v[202:203], v242 offset:26368
v_mfma_f32_16x16x32_bf16 v[12:15], v[204:207], a[64:67], v[12:15]
s_lshl_b32 s97, s3, 16
s_add_u32 s94, s92, s97
v_mfma_f32_16x16x32_bf16 v[44:47], v[204:207], a[68:71], v[44:47]
buffer_load_dwordx4 a[52:55], v247, s[44:47], s52 offen offset:0 sc1
v_mfma_f32_16x16x32_bf16 v[76:79], v[204:207], a[72:75], v[76:79]
s_add_u32 s95, s94, 32768
s_sub_u32 s96, s92, s97
v_mfma_f32_16x16x32_bf16 v[108:111], v[204:207], a[8:11], v[108:111]
ds_read_b64_tr_b16 v[204:205], v243 offset:25344
ds_read_b64_tr_b16 v[206:207], v243 offset:26368
v_mfma_f32_16x16x32_bf16 v[16:19], v[208:211], a[64:67], v[16:19]
s_add_u32 s96, s96, 458752
s_add_u32 s97, s96, 32768
v_mfma_f32_16x16x32_bf16 v[48:51], v[208:211], a[68:71], v[48:51]
buffer_load_dwordx4 a[80:83], v244, s[44:47], s52 offen offset:1024 sc1
v_mfma_f32_16x16x32_bf16 v[80:83], v[208:211], a[72:75], v[80:83]
s_lshl3_add_u32 s92, s23, s101
s_lshl_b32 s85, s76, 14
v_mfma_f32_16x16x32_bf16 v[112:115], v[208:211], a[8:11], v[112:115]
ds_read_b64_tr_b16 v[208:209], v240 offset:25472
ds_read_b64_tr_b16 v[210:211], v240 offset:26496
v_mfma_f32_16x16x32_bf16 v[20:23], v[212:215], a[64:67], v[20:23]
s_lshl3_add_u32 s92, s92, s85
s_add_u32 s92, s92, 131072
v_mfma_f32_16x16x32_bf16 v[52:55], v[212:215], a[68:71], v[52:55]
buffer_load_dwordx4 a[84:87], v245, s[44:47], s52 offen offset:1024 sc1
v_mfma_f32_16x16x32_bf16 v[84:87], v[212:215], a[72:75], v[84:87]
s_waitcnt vmcnt(23) lgkmcnt(2)
v_mfma_f32_16x16x32_bf16 v[116:119], v[212:215], a[8:11], v[116:119]
ds_read_b64_tr_b16 v[212:213], v241 offset:25472
ds_read_b64_tr_b16 v[214:215], v241 offset:26496
v_mfma_f32_16x16x32_bf16 v[24:27], v[216:219], a[64:67], v[24:27]
v_mfma_f32_16x16x32_bf16 v[56:59], v[216:219], a[68:71], v[56:59]
buffer_load_dwordx4 a[88:91], v246, s[44:47], s52 offen offset:1024 sc1
v_mfma_f32_16x16x32_bf16 v[88:91], v[216:219], a[72:75], v[88:91]
v_mfma_f32_16x16x32_bf16 v[120:123], v[216:219], a[8:11], v[120:123]
ds_read_b64_tr_b16 v[216:217], v242 offset:25472
ds_read_b64_tr_b16 v[218:219], v242 offset:26496
v_mfma_f32_16x16x32_bf16 v[28:31], v[220:223], a[64:67], v[28:31]
v_mfma_f32_16x16x32_bf16 v[60:63], v[220:223], a[68:71], v[60:63]
v_mfma_f32_16x16x32_bf16 v[92:95], v[220:223], a[72:75], v[92:95]
v_mfma_f32_16x16x32_bf16 v[124:127], v[220:223], a[8:11], v[124:127]
ds_read_b64_tr_b16 v[220:221], v243 offset:25472
ds_read_b64_tr_b16 v[222:223], v243 offset:26496
v_mfma_f32_16x16x32_bf16 v[0:3], v[192:195], a[12:15], v[0:3]
v_mfma_f32_16x16x32_bf16 v[32:35], v[192:195], a[16:19], v[32:35]
buffer_load_dwordx4 a[8:11], v247, s[44:47], s52 offen offset:1024 sc1
v_mfma_f32_16x16x32_bf16 v[64:67], v[192:195], a[20:23], v[64:67]
v_mfma_f32_16x16x32_bf16 v[96:99], v[192:195], a[76:79], v[96:99]
ds_read_b64_tr_b16 v[192:193], v240 offset:33792
ds_read_b64_tr_b16 v[194:195], v240 offset:34816
v_mfma_f32_16x16x32_bf16 v[4:7], v[196:199], a[12:15], v[4:7]
s_add_u32 m0, s83, 50688
v_mfma_f32_16x16x32_bf16 v[36:39], v[196:199], a[16:19], v[36:39]
buffer_load_dwordx4 v224, s[56:59], s80 offen lds
v_mfma_f32_16x16x32_bf16 v[68:71], v[196:199], a[20:23], v[68:71]
v_mfma_f32_16x16x32_bf16 v[100:103], v[196:199], a[76:79], v[100:103]
ds_read_b64_tr_b16 v[196:197], v241 offset:33792
ds_read_b64_tr_b16 v[198:199], v241 offset:34816
v_mfma_f32_16x16x32_bf16 v[8:11], v[200:203], a[12:15], v[8:11]
s_add_u32 m0, s83, 51712
v_mfma_f32_16x16x32_bf16 v[40:43], v[200:203], a[16:19], v[40:43]
buffer_load_dwordx4 v225, s[56:59], s80 offen lds
v_mfma_f32_16x16x32_bf16 v[72:75], v[200:203], a[20:23], v[72:75]
v_mfma_f32_16x16x32_bf16 v[104:107], v[200:203], a[76:79], v[104:107]
ds_read_b64_tr_b16 v[200:201], v242 offset:33792
ds_read_b64_tr_b16 v[202:203], v242 offset:34816
v_mfma_f32_16x16x32_bf16 v[12:15], v[204:207], a[12:15], v[12:15]
s_add_u32 m0, s83, 52864
v_mfma_f32_16x16x32_bf16 v[44:47], v[204:207], a[16:19], v[44:47]
buffer_load_dwordx4 v226, s[56:59], s80 offen lds
v_mfma_f32_16x16x32_bf16 v[76:79], v[204:207], a[20:23], v[76:79]
s_waitcnt vmcnt(21) lgkmcnt(2)
v_mfma_f32_16x16x32_bf16 v[108:111], v[204:207], a[76:79], v[108:111]
ds_read_b64_tr_b16 v[204:205], v243 offset:33792
ds_read_b64_tr_b16 v[206:207], v243 offset:34816
v_mfma_f32_16x16x32_bf16 v[16:19], v[208:211], a[12:15], v[16:19]
v_mfma_f32_16x16x32_bf16 v[48:51], v[208:211], a[16:19], v[48:51]
buffer_load_dwordx4 a[64:67], v244, s[44:47], s52 offen offset:2048 sc1
v_mfma_f32_16x16x32_bf16 v[80:83], v[208:211], a[20:23], v[80:83]
v_mfma_f32_16x16x32_bf16 v[112:115], v[208:211], a[76:79], v[112:115]
ds_read_b64_tr_b16 v[208:209], v240 offset:33920
ds_read_b64_tr_b16 v[210:211], v240 offset:34944
v_mfma_f32_16x16x32_bf16 v[20:23], v[212:215], a[12:15], v[20:23]
v_mfma_f32_16x16x32_bf16 v[52:55], v[212:215], a[16:19], v[52:55]
buffer_load_dwordx4 a[68:71], v245, s[44:47], s52 offen offset:2048 sc1
v_mfma_f32_16x16x32_bf16 v[84:87], v[212:215], a[20:23], v[84:87]
v_mfma_f32_16x16x32_bf16 v[116:119], v[212:215], a[76:79], v[116:119]
ds_read_b64_tr_b16 v[212:213], v241 offset:33920
ds_read_b64_tr_b16 v[214:215], v241 offset:34944
v_mfma_f32_16x16x32_bf16 v[24:27], v[216:219], a[12:15], v[24:27]
v_mfma_f32_16x16x32_bf16 v[56:59], v[216:219], a[16:19], v[56:59]
buffer_load_dwordx4 a[72:75], v246, s[44:47], s52 offen offset:2048 sc1
v_mfma_f32_16x16x32_bf16 v[88:91], v[216:219], a[20:23], v[88:91]
v_mfma_f32_16x16x32_bf16 v[120:123], v[216:219], a[76:79], v[120:123]
ds_read_b64_tr_b16 v[216:217], v242 offset:33920
ds_read_b64_tr_b16 v[218:219], v242 offset:34944
v_mfma_f32_16x16x32_bf16 v[28:31], v[220:223], a[12:15], v[28:31]
v_mfma_f32_16x16x32_bf16 v[60:63], v[220:223], a[16:19], v[60:63]
v_mfma_f32_16x16x32_bf16 v[92:95], v[220:223], a[20:23], v[92:95]
v_mfma_f32_16x16x32_bf16 v[124:127], v[220:223], a[76:79], v[124:127]
ds_read_b64_tr_b16 v[220:221], v243 offset:33920
ds_read_b64_tr_b16 v[222:223], v243 offset:34944
v_mfma_f32_16x16x32_bf16 v[0:3], v[192:195], a[24:27], v[0:3]
v_mfma_f32_16x16x32_bf16 v[32:35], v[192:195], a[28:31], v[32:35]
buffer_load_dwordx4 a[12:15], v247, s[44:47], s52 offen offset:2048 sc1
v_mfma_f32_16x16x32_bf16 v[64:67], v[192:195], a[56:59], v[64:67]
v_mfma_f32_16x16x32_bf16 v[96:99], v[192:195], a[60:63], v[96:99]
ds_read_b64_tr_b16 v[192:193], v240 offset:42240
ds_read_b64_tr_b16 v[194:195], v240 offset:43264
v_mfma_f32_16x16x32_bf16 v[4:7], v[196:199], a[24:27], v[4:7]
.La5_LW3_1:
s_cbranch_execz .La5_LW3_R0
s_add_u32 m0, s83, 53888
v_mfma_f32_16x16x32_bf16 v[36:39], v[196:199], a[28:31], v[36:39]
buffer_load_dwordx4 v227, s[56:59], s80 offen lds
v_mfma_f32_16x16x32_bf16 v[68:71], v[196:199], a[56:59], v[68:71]
s_waitcnt lgkmcnt(2)
s_add_u32 s80, s80, 131072
v_mfma_f32_16x16x32_bf16 v[100:103], v[196:199], a[60:63], v[100:103]
ds_read_b64_tr_b16 v[196:197], v241 offset:42240
ds_read_b64_tr_b16 v[198:199], v241 offset:43264
v_mfma_f32_16x16x32_bf16 v[8:11], v[200:203], a[24:27], v[8:11]
s_add_u32 m0, s83, 0
v_mfma_f32_16x16x32_bf16 v[40:43], v[200:203], a[28:31], v[40:43]
buffer_load_dwordx4 v224, s[56:59], s80 offen lds
v_mfma_f32_16x16x32_bf16 v[72:75], v[200:203], a[56:59], v[72:75]
v_mfma_f32_16x16x32_bf16 v[104:107], v[200:203], a[60:63], v[104:107]
ds_read_b64_tr_b16 v[200:201], v242 offset:42240
ds_read_b64_tr_b16 v[202:203], v242 offset:43264
v_mfma_f32_16x16x32_bf16 v[12:15], v[204:207], a[24:27], v[12:15]
s_add_u32 m0, s83, 1024
v_mfma_f32_16x16x32_bf16 v[44:47], v[204:207], a[28:31], v[44:47]
buffer_load_dwordx4 v225, s[56:59], s80 offen lds
v_mfma_f32_16x16x32_bf16 v[76:79], v[204:207], a[56:59], v[76:79]
s_waitcnt vmcnt(23)
v_cmp_eq_u64 vcc, v[250:251], v[252:253]
v_mfma_f32_16x16x32_bf16 v[108:111], v[204:207], a[60:63], v[108:111]
ds_read_b64_tr_b16 v[204:205], v243 offset:42240
ds_read_b64_tr_b16 v[206:207], v243 offset:43264
v_mfma_f32_16x16x32_bf16 v[16:19], v[208:211], a[24:27], v[16:19]
s_add_u32 m0, s83, 2176
v_mfma_f32_16x16x32_bf16 v[48:51], v[208:211], a[28:31], v[48:51]
buffer_load_dwordx4 v226, s[56:59], s80 offen lds
v_mfma_f32_16x16x32_bf16 v[80:83], v[208:211], a[56:59], v[80:83]
v_mfma_f32_16x16x32_bf16 v[112:115], v[208:211], a[60:63], v[112:115]
ds_read_b64_tr_b16 v[208:209], v240 offset:42368
ds_read_b64_tr_b16 v[210:211], v240 offset:43392
v_mfma_f32_16x16x32_bf16 v[20:23], v[212:215], a[24:27], v[20:23]
s_add_u32 m0, s83, 3200
v_add_u32 v240, 4294899712, v240
v_mfma_f32_16x16x32_bf16 v[52:55], v[212:215], a[28:31], v[52:55]
buffer_load_dwordx4 v227, s[56:59], s80 offen lds
v_mfma_f32_16x16x32_bf16 v[84:87], v[212:215], a[56:59], v[84:87]
s_bfe_u32 s80, s4, 262168
s_xor_b32 s80, s80, s72
v_mfma_f32_16x16x32_bf16 v[116:119], v[212:215], a[60:63], v[116:119]
ds_read_b64_tr_b16 v[212:213], v241 offset:42368
ds_read_b64_tr_b16 v[214:215], v241 offset:43392
v_mfma_f32_16x16x32_bf16 v[24:27], v[216:219], a[24:27], v[24:27]
v_add_u32 v241, 4294899712, v241
s_lshl_b32 s80, s80, 18
v_mfma_f32_16x16x32_bf16 v[56:59], v[216:219], a[28:31], v[56:59]
buffer_load_dwordx2 v[250:251], v248, s[64:67], s5 offen sc1
v_mfma_f32_16x16x32_bf16 v[88:91], v[216:219], a[56:59], v[88:91]
v_mfma_f32_16x16x32_bf16 v[120:123], v[216:219], a[60:63], v[120:123]
ds_read_b64_tr_b16 v[216:217], v242 offset:42368
ds_read_b64_tr_b16 v[218:219], v242 offset:43392
v_mfma_f32_16x16x32_bf16 v[28:31], v[220:223], a[24:27], v[28:31]
v_add_u32 v242, 4294899712, v242
v_mfma_f32_16x16x32_bf16 v[60:63], v[220:223], a[28:31], v[60:63]
buffer_load_dwordx4 a[16:19], v244, s[44:47], s52 offen offset:3072 sc1
v_mfma_f32_16x16x32_bf16 v[92:95], v[220:223], a[56:59], v[92:95]
s_waitcnt vmcnt(22) lgkmcnt(2)
v_mfma_f32_16x16x32_bf16 v[124:127], v[220:223], a[60:63], v[124:127]
ds_read_b64_tr_b16 v[220:221], v243 offset:42368
ds_read_b64_tr_b16 v[222:223], v243 offset:43392
v_mfma_f32_16x16x32_bf16 v[0:3], v[192:195], a[32:35], v[0:3]
v_add_u32 v243, 4294899712, v243
v_mfma_f32_16x16x32_bf16 v[32:35], v[192:195], a[36:39], v[32:35]
buffer_load_dwordx4 a[20:23], v245, s[44:47], s52 offen offset:3072 sc1
v_mfma_f32_16x16x32_bf16 v[64:67], v[192:195], a[40:43], v[64:67]
v_mfma_f32_16x16x32_bf16 v[96:99], v[192:195], a[0:3], v[96:99]
v_mfma_f32_16x16x32_bf16 v[4:7], v[196:199], a[32:35], v[4:7]
s_waitcnt lgkmcnt(0)
s_barrier
v_mfma_f32_16x16x32_bf16 v[36:39], v[196:199], a[36:39], v[36:39]
buffer_load_dwordx4 a[24:27], v246, s[44:47], s52 offen offset:3072 sc1
v_mfma_f32_16x16x32_bf16 v[68:71], v[196:199], a[40:43], v[68:71]
ds_read_b64_tr_b16 v[192:193], v240 offset:16896
ds_read_b64_tr_b16 v[194:195], v240 offset:17920
v_mfma_f32_16x16x32_bf16 v[100:103], v[196:199], a[0:3], v[100:103]
ds_read_b64_tr_b16 v[196:197], v241 offset:16896
ds_read_b64_tr_b16 v[198:199], v241 offset:17920
v_mfma_f32_16x16x32_bf16 v[8:11], v[200:203], a[32:35], v[8:11]
s_bitcmp1_b32 vcc_lo, 5
s_cbranch_scc0 .La5_LO_N5
.La5_LO_K5:
v_mfma_f32_16x16x32_bf16 v[40:43], v[200:203], a[36:39], v[40:43]
buffer_load_dwordx4 a[28:31], v247, s[44:47], s52 offen offset:3072 sc1
v_mfma_f32_16x16x32_bf16 v[72:75], v[200:203], a[40:43], v[72:75]
v_mfma_f32_16x16x32_bf16 v[104:107], v[200:203], a[0:3], v[104:107]
ds_read_b64_tr_b16 v[200:201], v242 offset:16896
ds_read_b64_tr_b16 v[202:203], v242 offset:17920
v_mfma_f32_16x16x32_bf16 v[12:15], v[204:207], a[32:35], v[12:15]
v_mfma_f32_16x16x32_bf16 v[44:47], v[204:207], a[36:39], v[44:47]
buffer_load_dwordx4 a[56:59], v244, s[44:47], s53 offen offset:0 sc1
v_mfma_f32_16x16x32_bf16 v[76:79], v[204:207], a[40:43], v[76:79]
v_mfma_f32_16x16x32_bf16 v[108:111], v[204:207], a[0:3], v[108:111]
ds_read_b64_tr_b16 v[204:205], v243 offset:16896
ds_read_b64_tr_b16 v[206:207], v243 offset:17920
v_mfma_f32_16x16x32_bf16 v[16:19], v[208:211], a[32:35], v[16:19]
v_mfma_f32_16x16x32_bf16 v[48:51], v[208:211], a[36:39], v[48:51]
buffer_load_dwordx4 a[60:63], v245, s[44:47], s53 offen offset:0 sc1
v_mfma_f32_16x16x32_bf16 v[80:83], v[208:211], a[40:43], v[80:83]
v_mfma_f32_16x16x32_bf16 v[112:115], v[208:211], a[0:3], v[112:115]
ds_read_b64_tr_b16 v[208:209], v240 offset:17024
ds_read_b64_tr_b16 v[210:211], v240 offset:18048
v_mfma_f32_16x16x32_bf16 v[20:23], v[212:215], a[32:35], v[20:23]
v_mfma_f32_16x16x32_bf16 v[52:55], v[212:215], a[36:39], v[52:55]
buffer_load_dwordx4 a[76:79], v246, s[44:47], s53 offen offset:0 sc1
v_mfma_f32_16x16x32_bf16 v[84:87], v[212:215], a[40:43], v[84:87]
s_waitcnt vmcnt(24) lgkmcnt(2)
v_mfma_f32_16x16x32_bf16 v[116:119], v[212:215], a[0:3], v[116:119]
ds_read_b64_tr_b16 v[212:213], v241 offset:17024
ds_read_b64_tr_b16 v[214:215], v241 offset:18048
v_mfma_f32_16x16x32_bf16 v[24:27], v[216:219], a[32:35], v[24:27]
v_mfma_f32_16x16x32_bf16 v[56:59], v[216:219], a[36:39], v[56:59]
buffer_load_dwordx4 a[92:95], v247, s[44:47], s53 offen offset:0 sc1
v_mfma_f32_16x16x32_bf16 v[88:91], v[216:219], a[40:43], v[88:91]
v_mfma_f32_16x16x32_bf16 v[120:123], v[216:219], a[0:3], v[120:123]
ds_read_b64_tr_b16 v[216:217], v242 offset:17024
ds_read_b64_tr_b16 v[218:219], v242 offset:18048
v_mfma_f32_16x16x32_bf16 v[28:31], v[220:223], a[32:35], v[28:31]
v_mfma_f32_16x16x32_bf16 v[60:63], v[220:223], a[36:39], v[60:63]
v_mfma_f32_16x16x32_bf16 v[92:95], v[220:223], a[40:43], v[92:95]
v_mfma_f32_16x16x32_bf16 v[124:127], v[220:223], a[0:3], v[124:127]
ds_read_b64_tr_b16 v[220:221], v243 offset:17024
ds_read_b64_tr_b16 v[222:223], v243 offset:18048
v_mfma_f32_16x16x32_bf16 v[0:3], v[192:195], a[4:7], v[0:3]
v_mfma_f32_16x16x32_bf16 v[32:35], v[192:195], a[44:47], v[32:35]
buffer_load_dwordx4 a[0:3], v244, s[44:47], s53 offen offset:1024 sc1
v_mfma_f32_16x16x32_bf16 v[64:67], v[192:195], a[48:51], v[64:67]
v_mfma_f32_16x16x32_bf16 v[96:99], v[192:195], a[52:55], v[96:99]
ds_read_b64_tr_b16 v[192:193], v240 offset:25344
ds_read_b64_tr_b16 v[194:195], v240 offset:26368
v_mfma_f32_16x16x32_bf16 v[4:7], v[196:199], a[4:7], v[4:7]
v_mfma_f32_16x16x32_bf16 v[36:39], v[196:199], a[44:47], v[36:39]
buffer_load_dwordx4 a[32:35], v245, s[44:47], s53 offen offset:1024 sc1
v_mfma_f32_16x16x32_bf16 v[68:71], v[196:199], a[48:51], v[68:71]
v_mfma_f32_16x16x32_bf16 v[100:103], v[196:199], a[52:55], v[100:103]
ds_read_b64_tr_b16 v[196:197], v241 offset:25344
ds_read_b64_tr_b16 v[198:199], v241 offset:26368
v_mfma_f32_16x16x32_bf16 v[8:11], v[200:203], a[4:7], v[8:11]
v_mfma_f32_16x16x32_bf16 v[40:43], v[200:203], a[44:47], v[40:43]
buffer_load_dwordx4 a[36:39], v246, s[44:47], s53 offen offset:1024 sc1
v_mfma_f32_16x16x32_bf16 v[72:75], v[200:203], a[48:51], v[72:75]
v_mfma_f32_16x16x32_bf16 v[104:107], v[200:203], a[52:55], v[104:107]
ds_read_b64_tr_b16 v[200:201], v242 offset:25344
ds_read_b64_tr_b16 v[202:203], v242 offset:26368
v_mfma_f32_16x16x32_bf16 v[12:15], v[204:207], a[4:7], v[12:15]
v_mfma_f32_16x16x32_bf16 v[44:47], v[204:207], a[44:47], v[44:47]
buffer_load_dwordx4 a[40:43], v247, s[44:47], s53 offen offset:1024 sc1
v_mfma_f32_16x16x32_bf16 v[76:79], v[204:207], a[48:51], v[76:79]
s_waitcnt vmcnt(25) lgkmcnt(2)
v_mfma_f32_16x16x32_bf16 v[108:111], v[204:207], a[52:55], v[108:111]
ds_read_b64_tr_b16 v[204:205], v243 offset:25344
ds_read_b64_tr_b16 v[206:207], v243 offset:26368
v_mfma_f32_16x16x32_bf16 v[16:19], v[208:211], a[4:7], v[16:19]
s_add_u32 m0, s83, 118272
v_mfma_f32_16x16x32_bf16 v[48:51], v[208:211], a[44:47], v[48:51]
buffer_load_dwordx4 v224, s[56:59], s80 offen lds
v_mfma_f32_16x16x32_bf16 v[80:83], v[208:211], a[48:51], v[80:83]
v_mfma_f32_16x16x32_bf16 v[112:115], v[208:211], a[52:55], v[112:115]
ds_read_b64_tr_b16 v[208:209], v240 offset:25472
ds_read_b64_tr_b16 v[210:211], v240 offset:26496
v_mfma_f32_16x16x32_bf16 v[20:23], v[212:215], a[4:7], v[20:23]
s_add_u32 m0, s83, 119296
v_mfma_f32_16x16x32_bf16 v[52:55], v[212:215], a[44:47], v[52:55]
buffer_load_dwordx4 v225, s[56:59], s80 offen lds
v_mfma_f32_16x16x32_bf16 v[84:87], v[212:215], a[48:51], v[84:87]
v_mfma_f32_16x16x32_bf16 v[116:119], v[212:215], a[52:55], v[116:119]
ds_read_b64_tr_b16 v[212:213], v241 offset:25472
ds_read_b64_tr_b16 v[214:215], v241 offset:26496
v_mfma_f32_16x16x32_bf16 v[24:27], v[216:219], a[4:7], v[24:27]
s_add_u32 m0, s83, 120448
v_mfma_f32_16x16x32_bf16 v[56:59], v[216:219], a[44:47], v[56:59]
buffer_load_dwordx4 v226, s[56:59], s80 offen lds
v_mfma_f32_16x16x32_bf16 v[88:91], v[216:219], a[48:51], v[88:91]
v_mfma_f32_16x16x32_bf16 v[120:123], v[216:219], a[52:55], v[120:123]
ds_read_b64_tr_b16 v[216:217], v242 offset:25472
ds_read_b64_tr_b16 v[218:219], v242 offset:26496
v_mfma_f32_16x16x32_bf16 v[28:31], v[220:223], a[4:7], v[28:31]
v_mfma_f32_16x16x32_bf16 v[60:63], v[220:223], a[44:47], v[60:63]
v_mfma_f32_16x16x32_bf16 v[92:95], v[220:223], a[48:51], v[92:95]
v_mfma_f32_16x16x32_bf16 v[124:127], v[220:223], a[52:55], v[124:127]
ds_read_b64_tr_b16 v[220:221], v243 offset:25472
ds_read_b64_tr_b16 v[222:223], v243 offset:26496
v_mfma_f32_16x16x32_bf16 v[0:3], v[192:195], a[80:83], v[0:3]
v_mfma_f32_16x16x32_bf16 v[32:35], v[192:195], a[84:87], v[32:35]
buffer_load_dwordx4 a[4:7], v244, s[44:47], s53 offen offset:2048 sc1
v_mfma_f32_16x16x32_bf16 v[64:67], v[192:195], a[88:91], v[64:67]
v_mfma_f32_16x16x32_bf16 v[96:99], v[192:195], a[8:11], v[96:99]
ds_read_b64_tr_b16 v[192:193], v240 offset:33792
ds_read_b64_tr_b16 v[194:195], v240 offset:34816
v_mfma_f32_16x16x32_bf16 v[4:7], v[196:199], a[80:83], v[4:7]
v_mfma_f32_16x16x32_bf16 v[36:39], v[196:199], a[84:87], v[36:39]
buffer_load_dwordx4 a[44:47], v245, s[44:47], s53 offen offset:2048 sc1
v_mfma_f32_16x16x32_bf16 v[68:71], v[196:199], a[88:91], v[68:71]
s_waitcnt lgkmcnt(2)
v_mfma_f32_16x16x32_bf16 v[100:103], v[196:199], a[8:11], v[100:103]
ds_read_b64_tr_b16 v[196:197], v241 offset:33792
ds_read_b64_tr_b16 v[198:199], v241 offset:34816
v_mfma_f32_16x16x32_bf16 v[8:11], v[200:203], a[80:83], v[8:11]
v_mfma_f32_16x16x32_bf16 v[40:43], v[200:203], a[84:87], v[40:43]
buffer_load_dwordx4 a[48:51], v246, s[44:47], s53 offen offset:2048 sc1
v_mfma_f32_16x16x32_bf16 v[72:75], v[200:203], a[88:91], v[72:75]
v_mfma_f32_16x16x32_bf16 v[104:107], v[200:203], a[8:11], v[104:107]
ds_read_b64_tr_b16 v[200:201], v242 offset:33792
ds_read_b64_tr_b16 v[202:203], v242 offset:34816
v_mfma_f32_16x16x32_bf16 v[12:15], v[204:207], a[80:83], v[12:15]
v_mfma_f32_16x16x32_bf16 v[44:47], v[204:207], a[84:87], v[44:47]
buffer_load_dwordx4 a[52:55], v247, s[44:47], s53 offen offset:2048 sc1
v_mfma_f32_16x16x32_bf16 v[76:79], v[204:207], a[88:91], v[76:79]
v_mfma_f32_16x16x32_bf16 v[108:111], v[204:207], a[8:11], v[108:111]
ds_read_b64_tr_b16 v[204:205], v243 offset:33792
ds_read_b64_tr_b16 v[206:207], v243 offset:34816
v_mfma_f32_16x16x32_bf16 v[16:19], v[208:211], a[80:83], v[16:19]
s_add_u32 m0, s83, 121472
v_mfma_f32_16x16x32_bf16 v[48:51], v[208:211], a[84:87], v[48:51]
buffer_load_dwordx4 v227, s[56:59], s80 offen lds
v_mfma_f32_16x16x32_bf16 v[80:83], v[208:211], a[88:91], v[80:83]
s_add_u32 s80, s80, 131072
v_mfma_f32_16x16x32_bf16 v[112:115], v[208:211], a[8:11], v[112:115]
ds_read_b64_tr_b16 v[208:209], v240 offset:33920
ds_read_b64_tr_b16 v[210:211], v240 offset:34944
v_mfma_f32_16x16x32_bf16 v[20:23], v[212:215], a[80:83], v[20:23]
s_add_u32 m0, s83, 67584
v_mfma_f32_16x16x32_bf16 v[52:55], v[212:215], a[84:87], v[52:55]
buffer_load_dwordx4 v224, s[56:59], s80 offen lds
v_mfma_f32_16x16x32_bf16 v[84:87], v[212:215], a[88:91], v[84:87]
v_mfma_f32_16x16x32_bf16 v[116:119], v[212:215], a[8:11], v[116:119]
ds_read_b64_tr_b16 v[212:213], v241 offset:33920
ds_read_b64_tr_b16 v[214:215], v241 offset:34944
v_mfma_f32_16x16x32_bf16 v[24:27], v[216:219], a[80:83], v[24:27]
s_add_u32 m0, s83, 68608
v_mfma_f32_16x16x32_bf16 v[56:59], v[216:219], a[84:87], v[56:59]
buffer_load_dwordx4 v225, s[56:59], s80 offen lds
v_mfma_f32_16x16x32_bf16 v[88:91], v[216:219], a[88:91], v[88:91]
v_mfma_f32_16x16x32_bf16 v[120:123], v[216:219], a[8:11], v[120:123]
ds_read_b64_tr_b16 v[216:217], v242 offset:33920
ds_read_b64_tr_b16 v[218:219], v242 offset:34944
v_mfma_f32_16x16x32_bf16 v[28:31], v[220:223], a[80:83], v[28:31]
v_mfma_f32_16x16x32_bf16 v[60:63], v[220:223], a[84:87], v[60:63]
s_waitcnt vmcnt(22)
v_cmp_eq_u64 vcc, v[250:251], v[252:253]
v_mfma_f32_16x16x32_bf16 v[92:95], v[220:223], a[88:91], v[92:95]
s_waitcnt lgkmcnt(2)
v_mfma_f32_16x16x32_bf16 v[124:127], v[220:223], a[8:11], v[124:127]
ds_read_b64_tr_b16 v[220:221], v243 offset:33920
ds_read_b64_tr_b16 v[222:223], v243 offset:34944
v_mfma_f32_16x16x32_bf16 v[0:3], v[192:195], a[64:67], v[0:3]
s_add_u32 m0, s83, 69760
v_mfma_f32_16x16x32_bf16 v[32:35], v[192:195], a[68:71], v[32:35]
buffer_load_dwordx4 v226, s[56:59], s80 offen lds
v_mfma_f32_16x16x32_bf16 v[64:67], v[192:195], a[72:75], v[64:67]
v_mfma_f32_16x16x32_bf16 v[96:99], v[192:195], a[12:15], v[96:99]
ds_read_b64_tr_b16 v[192:193], v240 offset:42240
ds_read_b64_tr_b16 v[194:195], v240 offset:43264
v_mfma_f32_16x16x32_bf16 v[4:7], v[196:199], a[64:67], v[4:7]
s_add_u32 m0, s83, 70784
v_mfma_f32_16x16x32_bf16 v[36:39], v[196:199], a[68:71], v[36:39]
buffer_load_dwordx4 v227, s[56:59], s80 offen lds
v_mfma_f32_16x16x32_bf16 v[68:71], v[196:199], a[72:75], v[68:71]
s_bfe_u32 s80, s4, 262172
s_xor_b32 s80, s80, s72
v_mfma_f32_16x16x32_bf16 v[100:103], v[196:199], a[12:15], v[100:103]
ds_read_b64_tr_b16 v[196:197], v241 offset:42240
ds_read_b64_tr_b16 v[198:199], v241 offset:43264
v_mfma_f32_16x16x32_bf16 v[8:11], v[200:203], a[64:67], v[8:11]
s_lshl_b32 s80, s80, 18
v_mfma_f32_16x16x32_bf16 v[40:43], v[200:203], a[68:71], v[40:43]
buffer_load_dwordx2 v[250:251], v248, s[64:67], s5 offen sc1
v_mfma_f32_16x16x32_bf16 v[72:75], v[200:203], a[72:75], v[72:75]
v_mfma_f32_16x16x32_bf16 v[104:107], v[200:203], a[12:15], v[104:107]
ds_read_b64_tr_b16 v[200:201], v242 offset:42240
ds_read_b64_tr_b16 v[202:203], v242 offset:43264
v_mfma_f32_16x16x32_bf16 v[12:15], v[204:207], a[64:67], v[12:15]
s_add_u32 m0, s83, 84480
v_mfma_f32_16x16x32_bf16 v[44:47], v[204:207], a[68:71], v[44:47]
buffer_load_dwordx4 v224, s[56:59], s80 offen lds
v_mfma_f32_16x16x32_bf16 v[76:79], v[204:207], a[72:75], v[76:79]
v_mfma_f32_16x16x32_bf16 v[108:111], v[204:207], a[12:15], v[108:111]
ds_read_b64_tr_b16 v[204:205], v243 offset:42240
ds_read_b64_tr_b16 v[206:207], v243 offset:43264
v_mfma_f32_16x16x32_bf16 v[16:19], v[208:211], a[64:67], v[16:19]
s_add_u32 m0, s83, 85504
v_mfma_f32_16x16x32_bf16 v[48:51], v[208:211], a[68:71], v[48:51]
buffer_load_dwordx4 v225, s[56:59], s80 offen lds
v_mfma_f32_16x16x32_bf16 v[80:83], v[208:211], a[72:75], v[80:83]
v_mfma_f32_16x16x32_bf16 v[112:115], v[208:211], a[12:15], v[112:115]
ds_read_b64_tr_b16 v[208:209], v240 offset:42368
ds_read_b64_tr_b16 v[210:211], v240 offset:43392
v_mfma_f32_16x16x32_bf16 v[20:23], v[212:215], a[64:67], v[20:23]
s_add_u32 m0, s83, 86656
v_mfma_f32_16x16x32_bf16 v[52:55], v[212:215], a[68:71], v[52:55]
buffer_load_dwordx4 v226, s[56:59], s80 offen lds
v_mfma_f32_16x16x32_bf16 v[84:87], v[212:215], a[72:75], v[84:87]
s_waitcnt vmcnt(24) lgkmcnt(2)
v_mfma_f32_16x16x32_bf16 v[116:119], v[212:215], a[12:15], v[116:119]
ds_read_b64_tr_b16 v[212:213], v241 offset:42368
ds_read_b64_tr_b16 v[214:215], v241 offset:43392
v_mfma_f32_16x16x32_bf16 v[24:27], v[216:219], a[64:67], v[24:27]
s_add_u32 m0, s83, 87680
v_mfma_f32_16x16x32_bf16 v[56:59], v[216:219], a[68:71], v[56:59]
buffer_load_dwordx4 v227, s[56:59], s80 offen lds
v_mfma_f32_16x16x32_bf16 v[88:91], v[216:219], a[72:75], v[88:91]
s_add_u32 s80, s80, 131072
v_mfma_f32_16x16x32_bf16 v[120:123], v[216:219], a[12:15], v[120:123]
ds_read_b64_tr_b16 v[216:217], v242 offset:42368
ds_read_b64_tr_b16 v[218:219], v242 offset:43392
v_mfma_f32_16x16x32_bf16 v[28:31], v[220:223], a[64:67], v[28:31]
s_add_u32 m0, s83, 101376
v_mfma_f32_16x16x32_bf16 v[60:63], v[220:223], a[68:71], v[60:63]
buffer_load_dwordx4 v224, s[56:59], s80 offen lds
v_mfma_f32_16x16x32_bf16 v[92:95], v[220:223], a[72:75], v[92:95]
v_mfma_f32_16x16x32_bf16 v[124:127], v[220:223], a[12:15], v[124:127]
ds_read_b64_tr_b16 v[220:221], v243 offset:42368
ds_read_b64_tr_b16 v[222:223], v243 offset:43392
v_mfma_f32_16x16x32_bf16 v[0:3], v[192:195], a[16:19], v[0:3]
s_add_u32 m0, s83, 102400
v_mfma_f32_16x16x32_bf16 v[32:35], v[192:195], a[20:23], v[32:35]
buffer_load_dwordx4 v225, s[56:59], s80 offen lds
v_mfma_f32_16x16x32_bf16 v[64:67], v[192:195], a[24:27], v[64:67]
v_mfma_f32_16x16x32_bf16 v[96:99], v[192:195], a[28:31], v[96:99]
v_mfma_f32_16x16x32_bf16 v[4:7], v[196:199], a[16:19], v[4:7]
s_add_u32 m0, s83, 103552
s_waitcnt lgkmcnt(0)
s_barrier
v_mfma_f32_16x16x32_bf16 v[36:39], v[196:199], a[20:23], v[36:39]
buffer_load_dwordx4 v226, s[56:59], s80 offen lds
v_mfma_f32_16x16x32_bf16 v[68:71], v[196:199], a[24:27], v[68:71]
ds_read_b64_tr_b16 v[192:193], v240 offset:50688
ds_read_b64_tr_b16 v[194:195], v240 offset:51712
v_mfma_f32_16x16x32_bf16 v[100:103], v[196:199], a[28:31], v[100:103]
ds_read_b64_tr_b16 v[196:197], v241 offset:50688
ds_read_b64_tr_b16 v[198:199], v241 offset:51712
v_mfma_f32_16x16x32_bf16 v[8:11], v[200:203], a[16:19], v[8:11]
s_add_u32 m0, s83, 104576
v_mfma_f32_16x16x32_bf16 v[40:43], v[200:203], a[20:23], v[40:43]
buffer_load_dwordx4 v227, s[56:59], s80 offen lds
v_mfma_f32_16x16x32_bf16 v[72:75], v[200:203], a[24:27], v[72:75]
v_bfe_u32 v224, v255, 6, 1
v_lshrrev_b32 v225, 7, v255
v_mfma_f32_16x16x32_bf16 v[104:107], v[200:203], a[28:31], v[104:107]
ds_read_b64_tr_b16 v[200:201], v242 offset:50688
ds_read_b64_tr_b16 v[202:203], v242 offset:51712
v_mfma_f32_16x16x32_bf16 v[12:15], v[204:207], a[16:19], v[12:15]
v_and_b32 v226, 48, v255
v_lshl_add_u32 v225, v225, 6, v226
v_mfma_f32_16x16x32_bf16 v[44:47], v[204:207], a[20:23], v[44:47]
buffer_load_dwordx4 a[8:11], v244, s[44:47], s53 offen offset:3072 sc1
v_mfma_f32_16x16x32_bf16 v[76:79], v[204:207], a[24:27], v[76:79]
s_waitcnt vmcnt(26) lgkmcnt(2)
v_lshl_add_u32 v224, v224, 11, v225
v_mfma_f32_16x16x32_bf16 v[108:111], v[204:207], a[28:31], v[108:111]
ds_read_b64_tr_b16 v[204:205], v243 offset:50688
ds_read_b64_tr_b16 v[206:207], v243 offset:51712
v_mfma_f32_16x16x32_bf16 v[16:19], v[208:211], a[16:19], v[16:19]
v_mfma_f32_16x16x32_bf16 v[48:51], v[208:211], a[20:23], v[48:51]
buffer_load_dwordx4 a[12:15], v245, s[44:47], s53 offen offset:3072 sc1
v_mfma_f32_16x16x32_bf16 v[80:83], v[208:211], a[24:27], v[80:83]
v_mfma_f32_16x16x32_bf16 v[112:115], v[208:211], a[28:31], v[112:115]
ds_read_b64_tr_b16 v[208:209], v240 offset:50816
ds_read_b64_tr_b16 v[210:211], v240 offset:51840
v_mfma_f32_16x16x32_bf16 v[20:23], v[212:215], a[16:19], v[20:23]
v_mfma_f32_16x16x32_bf16 v[52:55], v[212:215], a[20:23], v[52:55]
buffer_load_dwordx4 a[64:67], v246, s[44:47], s53 offen offset:3072 sc1
v_mfma_f32_16x16x32_bf16 v[84:87], v[212:215], a[24:27], v[84:87]
v_mfma_f32_16x16x32_bf16 v[116:119], v[212:215], a[28:31], v[116:119]
ds_read_b64_tr_b16 v[212:213], v241 offset:50816
ds_read_b64_tr_b16 v[214:215], v241 offset:51840
v_mfma_f32_16x16x32_bf16 v[24:27], v[216:219], a[16:19], v[24:27]
v_mfma_f32_16x16x32_bf16 v[56:59], v[216:219], a[20:23], v[56:59]
buffer_load_dwordx4 a[68:71], v247, s[44:47], s53 offen offset:3072 sc1
v_mfma_f32_16x16x32_bf16 v[88:91], v[216:219], a[24:27], v[88:91]
v_mfma_f32_16x16x32_bf16 v[120:123], v[216:219], a[28:31], v[120:123]
ds_read_b64_tr_b16 v[216:217], v242 offset:50816
ds_read_b64_tr_b16 v[218:219], v242 offset:51840
v_mfma_f32_16x16x32_bf16 v[28:31], v[220:223], a[16:19], v[28:31]
s_bitcmp1_b32 vcc_lo, 6
s_cbranch_scc0 .La5_LO_N6
.La5_LO_K6:
v_mfma_f32_16x16x32_bf16 v[60:63], v[220:223], a[20:23], v[60:63]
v_mfma_f32_16x16x32_bf16 v[92:95], v[220:223], a[24:27], v[92:95]
v_mfma_f32_16x16x32_bf16 v[124:127], v[220:223], a[28:31], v[124:127]
ds_read_b64_tr_b16 v[220:221], v243 offset:50816
ds_read_b64_tr_b16 v[222:223], v243 offset:51840
v_mfma_f32_16x16x32_bf16 v[0:3], v[192:195], a[56:59], v[0:3]
v_mfma_f32_16x16x32_bf16 v[32:35], v[192:195], a[60:63], v[32:35]
buffer_load_dwordx4 a[16:19], v244, s[44:47], s54 offen offset:0 sc1
v_mfma_f32_16x16x32_bf16 v[64:67], v[192:195], a[76:79], v[64:67]
v_mfma_f32_16x16x32_bf16 v[96:99], v[192:195], a[92:95], v[96:99]
ds_read_b64_tr_b16 v[192:193], v240 offset:59136
ds_read_b64_tr_b16 v[194:195], v240 offset:60160
v_mfma_f32_16x16x32_bf16 v[4:7], v[196:199], a[56:59], v[4:7]
v_mfma_f32_16x16x32_bf16 v[36:39], v[196:199], a[60:63], v[36:39]
buffer_load_dwordx4 a[20:23], v245, s[44:47], s54 offen offset:0 sc1
v_mfma_f32_16x16x32_bf16 v[68:71], v[196:199], a[76:79], v[68:71]
s_waitcnt lgkmcnt(2)
v_mfma_f32_16x16x32_bf16 v[100:103], v[196:199], a[92:95], v[100:103]
ds_read_b64_tr_b16 v[196:197], v241 offset:59136
ds_read_b64_tr_b16 v[198:199], v241 offset:60160
v_mfma_f32_16x16x32_bf16 v[8:11], v[200:203], a[56:59], v[8:11]
v_mfma_f32_16x16x32_bf16 v[40:43], v[200:203], a[60:63], v[40:43]
buffer_load_dwordx4 a[24:27], v244, s[44:47], s54 offen offset:1024 sc1
v_mfma_f32_16x16x32_bf16 v[72:75], v[200:203], a[76:79], v[72:75]
v_mfma_f32_16x16x32_bf16 v[104:107], v[200:203], a[92:95], v[104:107]
ds_read_b64_tr_b16 v[200:201], v242 offset:59136
ds_read_b64_tr_b16 v[202:203], v242 offset:60160
v_mfma_f32_16x16x32_bf16 v[12:15], v[204:207], a[56:59], v[12:15]
v_mfma_f32_16x16x32_bf16 v[44:47], v[204:207], a[60:63], v[44:47]
buffer_load_dwordx4 a[28:31], v245, s[44:47], s54 offen offset:1024 sc1
v_mfma_f32_16x16x32_bf16 v[76:79], v[204:207], a[76:79], v[76:79]
v_mfma_f32_16x16x32_bf16 v[108:111], v[204:207], a[92:95], v[108:111]
ds_read_b64_tr_b16 v[204:205], v243 offset:59136
ds_read_b64_tr_b16 v[206:207], v243 offset:60160
v_mfma_f32_16x16x32_bf16 v[16:19], v[208:211], a[56:59], v[16:19]
v_mfma_f32_16x16x32_bf16 v[48:51], v[208:211], a[60:63], v[48:51]
buffer_load_dwordx4 a[72:75], v244, s[44:47], s54 offen offset:2048 sc1
v_mfma_f32_16x16x32_bf16 v[80:83], v[208:211], a[76:79], v[80:83]
v_mfma_f32_16x16x32_bf16 v[112:115], v[208:211], a[92:95], v[112:115]
ds_read_b64_tr_b16 v[208:209], v240 offset:59264
ds_read_b64_tr_b16 v[210:211], v240 offset:60288
v_mfma_f32_16x16x32_bf16 v[20:23], v[212:215], a[56:59], v[20:23]
v_mfma_f32_16x16x32_bf16 v[52:55], v[212:215], a[60:63], v[52:55]
buffer_load_dwordx4 a[80:83], v245, s[44:47], s54 offen offset:2048 sc1
v_mfma_f32_16x16x32_bf16 v[84:87], v[212:215], a[76:79], v[84:87]
v_mfma_f32_16x16x32_bf16 v[116:119], v[212:215], a[92:95], v[116:119]
ds_read_b64_tr_b16 v[212:213], v241 offset:59264
ds_read_b64_tr_b16 v[214:215], v241 offset:60288
v_mfma_f32_16x16x32_bf16 v[24:27], v[216:219], a[56:59], v[24:27]
v_mfma_f32_16x16x32_bf16 v[56:59], v[216:219], a[60:63], v[56:59]
buffer_load_dwordx4 a[84:87], v244, s[44:47], s54 offen offset:3072 sc1
v_mfma_f32_16x16x32_bf16 v[88:91], v[216:219], a[76:79], v[88:91]
v_mfma_f32_16x16x32_bf16 v[120:123], v[216:219], a[92:95], v[120:123]
ds_read_b64_tr_b16 v[216:217], v242 offset:59264
ds_read_b64_tr_b16 v[218:219], v242 offset:60288
v_mfma_f32_16x16x32_bf16 v[28:31], v[220:223], a[56:59], v[28:31]
v_mfma_f32_16x16x32_bf16 v[60:63], v[220:223], a[60:63], v[60:63]
v_mfma_f32_16x16x32_bf16 v[92:95], v[220:223], a[76:79], v[92:95]
s_waitcnt vmcnt(32) lgkmcnt(2)
v_mfma_f32_16x16x32_bf16 v[124:127], v[220:223], a[92:95], v[124:127]
ds_read_b64_tr_b16 v[220:221], v243 offset:59264
ds_read_b64_tr_b16 v[222:223], v243 offset:60288
v_mfma_f32_16x16x32_bf16 v[0:3], v[192:195], a[0:3], v[0:3]
s_bitcmp1_b32 vcc_lo, 7
s_nop 0
s_cbranch_scc0 .La5_LO_N7
.La5_LO_K7:
v_mfma_f32_16x16x32_bf16 v[32:35], v[192:195], a[32:35], v[32:35]
buffer_load_dwordx4 a[56:59], v245, s[44:47], s54 offen offset:3072 sc1
v_mfma_f32_16x16x32_bf16 v[64:67], v[192:195], a[36:39], v[64:67]
v_mfma_f32_16x16x32_bf16 v[96:99], v[192:195], a[40:43], v[96:99]
ds_read_b64_tr_b16 v[192:193], v240 offset:0
ds_read_b64_tr_b16 v[194:195], v240 offset:1024
v_mfma_f32_16x16x32_bf16 v[4:7], v[196:199], a[0:3], v[4:7]
v_mfma_f32_16x16x32_bf16 v[36:39], v[196:199], a[32:35], v[36:39]
buffer_load_dwordx4 a[60:63], v244, s[44:47], s55 offen offset:0 sc1
v_mfma_f32_16x16x32_bf16 v[68:71], v[196:199], a[36:39], v[68:71]
v_mfma_f32_16x16x32_bf16 v[100:103], v[196:199], a[40:43], v[100:103]
ds_read_b64_tr_b16 v[196:197], v241 offset:0
ds_read_b64_tr_b16 v[198:199], v241 offset:1024
v_mfma_f32_16x16x32_bf16 v[8:11], v[200:203], a[0:3], v[8:11]
v_mfma_f32_16x16x32_bf16 v[40:43], v[200:203], a[32:35], v[40:43]
buffer_load_dwordx4 a[76:79], v245, s[44:47], s55 offen offset:0 sc1
v_mfma_f32_16x16x32_bf16 v[72:75], v[200:203], a[36:39], v[72:75]
v_mfma_f32_16x16x32_bf16 v[104:107], v[200:203], a[40:43], v[104:107]
ds_read_b64_tr_b16 v[200:201], v242 offset:0
ds_read_b64_tr_b16 v[202:203], v242 offset:1024
v_mfma_f32_16x16x32_bf16 v[12:15], v[204:207], a[0:3], v[12:15]
v_mfma_f32_16x16x32_bf16 v[44:47], v[204:207], a[32:35], v[44:47]
buffer_load_dwordx4 a[88:91], v244, s[44:47], s55 offen offset:1024 sc1
v_mfma_f32_16x16x32_bf16 v[76:79], v[204:207], a[36:39], v[76:79]
s_waitcnt vmcnt(23)
v_cmp_eq_u64 vcc, v[250:251], v[252:253]
v_mfma_f32_16x16x32_bf16 v[108:111], v[204:207], a[40:43], v[108:111]
ds_read_b64_tr_b16 v[204:205], v243 offset:0
ds_read_b64_tr_b16 v[206:207], v243 offset:1024
v_mfma_f32_16x16x32_bf16 v[16:19], v[208:211], a[0:3], v[16:19]
v_mfma_f32_16x16x32_bf16 v[48:51], v[208:211], a[32:35], v[48:51]
buffer_load_dwordx4 a[92:95], v245, s[44:47], s55 offen offset:1024 sc1
v_mfma_f32_16x16x32_bf16 v[80:83], v[208:211], a[36:39], v[80:83]
v_mfma_f32_16x16x32_bf16 v[112:115], v[208:211], a[40:43], v[112:115]
ds_read_b64_tr_b16 v[208:209], v240 offset:128
ds_read_b64_tr_b16 v[210:211], v240 offset:1152
v_mfma_f32_16x16x32_bf16 v[20:23], v[212:215], a[0:3], v[20:23]
v_mfma_f32_16x16x32_bf16 v[52:55], v[212:215], a[32:35], v[52:55]
buffer_load_dwordx4 a[96:99], v244, s[44:47], s55 offen offset:2048 sc1
v_mfma_f32_16x16x32_bf16 v[84:87], v[212:215], a[36:39], v[84:87]
s_waitcnt lgkmcnt(2)
v_mfma_f32_16x16x32_bf16 v[116:119], v[212:215], a[40:43], v[116:119]
ds_read_b64_tr_b16 v[212:213], v241 offset:128
ds_read_b64_tr_b16 v[214:215], v241 offset:1152
v_mfma_f32_16x16x32_bf16 v[24:27], v[216:219], a[0:3], v[24:27]
v_mfma_f32_16x16x32_bf16 v[56:59], v[216:219], a[32:35], v[56:59]
buffer_load_dwordx4 a[100:103], v245, s[44:47], s55 offen offset:2048 sc1
v_mfma_f32_16x16x32_bf16 v[88:91], v[216:219], a[36:39], v[88:91]
v_mfma_f32_16x16x32_bf16 v[120:123], v[216:219], a[40:43], v[120:123]
ds_read_b64_tr_b16 v[216:217], v242 offset:128
ds_read_b64_tr_b16 v[218:219], v242 offset:1152
v_mfma_f32_16x16x32_bf16 v[28:31], v[220:223], a[0:3], v[28:31]
v_mfma_f32_16x16x32_bf16 v[60:63], v[220:223], a[32:35], v[60:63]
v_mfma_f32_16x16x32_bf16 v[92:95], v[220:223], a[36:39], v[92:95]
v_mfma_f32_16x16x32_bf16 v[124:127], v[220:223], a[40:43], v[124:127]
ds_read_b64_tr_b16 v[220:221], v243 offset:128
ds_read_b64_tr_b16 v[222:223], v243 offset:1152
v_mfma_f32_16x16x32_bf16 v[0:3], v[192:195], a[4:7], v[0:3]
v_mfma_f32_16x16x32_bf16 v[32:35], v[192:195], a[44:47], v[32:35]
buffer_load_dwordx4 a[0:3], v244, s[44:47], s55 offen offset:3072 sc1
v_mfma_f32_16x16x32_bf16 v[64:67], v[192:195], a[48:51], v[64:67]
v_mfma_f32_16x16x32_bf16 v[96:99], v[192:195], a[52:55], v[96:99]
ds_read_b64_tr_b16 v[192:193], v240 offset:8448
ds_read_b64_tr_b16 v[194:195], v240 offset:9472
v_mfma_f32_16x16x32_bf16 v[4:7], v[196:199], a[4:7], v[4:7]
v_mfma_f32_16x16x32_bf16 v[36:39], v[196:199], a[44:47], v[36:39]
buffer_load_dwordx4 a[32:35], v245, s[44:47], s55 offen offset:3072 sc1
v_mfma_f32_16x16x32_bf16 v[68:71], v[196:199], a[48:51], v[68:71]
v_mfma_f32_16x16x32_bf16 v[100:103], v[196:199], a[52:55], v[100:103]
ds_read_b64_tr_b16 v[196:197], v241 offset:8448
ds_read_b64_tr_b16 v[198:199], v241 offset:9472
v_mfma_f32_16x16x32_bf16 v[8:11], v[200:203], a[4:7], v[8:11]
v_mfma_f32_16x16x32_bf16 v[40:43], v[200:203], a[44:47], v[40:43]
v_mfma_f32_16x16x32_bf16 v[72:75], v[200:203], a[48:51], v[72:75]
v_mfma_f32_16x16x32_bf16 v[104:107], v[200:203], a[52:55], v[104:107]
ds_read_b64_tr_b16 v[200:201], v242 offset:8448
ds_read_b64_tr_b16 v[202:203], v242 offset:9472
v_mfma_f32_16x16x32_bf16 v[12:15], v[204:207], a[4:7], v[12:15]
v_mfma_f32_16x16x32_bf16 v[44:47], v[204:207], a[44:47], v[44:47]
v_mfma_f32_16x16x32_bf16 v[76:79], v[204:207], a[48:51], v[76:79]
s_waitcnt vmcnt(16) lgkmcnt(2)
v_mfma_f32_16x16x32_bf16 v[108:111], v[204:207], a[52:55], v[108:111]
ds_read_b64_tr_b16 v[204:205], v243 offset:8448
ds_read_b64_tr_b16 v[206:207], v243 offset:9472
v_mfma_f32_16x16x32_bf16 v[16:19], v[208:211], a[4:7], v[16:19]
v_mfma_f32_16x16x32_bf16 v[48:51], v[208:211], a[44:47], v[48:51]
.La5_LW3_2:
s_cbranch_execz .La5_LW3_R0
v_mfma_f32_16x16x32_bf16 v[80:83], v[208:211], a[48:51], v[80:83]
v_mfma_f32_16x16x32_bf16 v[112:115], v[208:211], a[52:55], v[112:115]
ds_read_b64_tr_b16 v[208:209], v240 offset:8576
ds_read_b64_tr_b16 v[210:211], v240 offset:9600
v_mfma_f32_16x16x32_bf16 v[20:23], v[212:215], a[4:7], v[20:23]
v_add_u32 v240, 67584, v240
v_mfma_f32_16x16x32_bf16 v[52:55], v[212:215], a[44:47], v[52:55]
v_mfma_f32_16x16x32_bf16 v[84:87], v[212:215], a[48:51], v[84:87]
v_mfma_f32_16x16x32_bf16 v[116:119], v[212:215], a[52:55], v[116:119]
ds_read_b64_tr_b16 v[212:213], v241 offset:8576
ds_read_b64_tr_b16 v[214:215], v241 offset:9600
v_mfma_f32_16x16x32_bf16 v[24:27], v[216:219], a[4:7], v[24:27]
v_add_u32 v241, 67584, v241
v_mfma_f32_16x16x32_bf16 v[56:59], v[216:219], a[44:47], v[56:59]
v_mfma_f32_16x16x32_bf16 v[88:91], v[216:219], a[48:51], v[88:91]
v_mfma_f32_16x16x32_bf16 v[120:123], v[216:219], a[52:55], v[120:123]
ds_read_b64_tr_b16 v[216:217], v242 offset:8576
ds_read_b64_tr_b16 v[218:219], v242 offset:9600
v_mfma_f32_16x16x32_bf16 v[28:31], v[220:223], a[4:7], v[28:31]
v_add_u32 v242, 67584, v242
v_mfma_f32_16x16x32_bf16 v[60:63], v[220:223], a[44:47], v[60:63]
v_mfma_f32_16x16x32_bf16 v[92:95], v[220:223], a[48:51], v[92:95]
v_mfma_f32_16x16x32_bf16 v[124:127], v[220:223], a[52:55], v[124:127]
ds_read_b64_tr_b16 v[220:221], v243 offset:8576
ds_read_b64_tr_b16 v[222:223], v243 offset:9600
v_mfma_f32_16x16x32_bf16 v[0:3], v[192:195], a[8:11], v[0:3]
v_add_u32 v243, 67584, v243
v_mfma_f32_16x16x32_bf16 v[32:35], v[192:195], a[12:15], v[32:35]
buffer_load_dwordx4 a[4:7], v246, s[44:47], s54 offen offset:0 sc1
v_mfma_f32_16x16x32_bf16 v[64:67], v[192:195], a[64:67], v[64:67]
v_mfma_f32_16x16x32_bf16 v[96:99], v[192:195], a[68:71], v[96:99]
v_mfma_f32_16x16x32_bf16 v[4:7], v[196:199], a[8:11], v[4:7]
s_waitcnt lgkmcnt(0)
s_barrier
v_mfma_f32_16x16x32_bf16 v[36:39], v[196:199], a[12:15], v[36:39]
buffer_load_dwordx4 a[36:39], v247, s[44:47], s54 offen offset:0 sc1
v_mfma_f32_16x16x32_bf16 v[68:71], v[196:199], a[64:67], v[68:71]
ds_read_b64_tr_b16 v[192:193], v240 offset:50688
ds_read_b64_tr_b16 v[194:195], v240 offset:51712
s_nop 0
v_mfma_f32_16x16x32_bf16 v[100:103], v[196:199], a[68:71], v[100:103]
ds_read_b64_tr_b16 v[196:197], v240 offset:59136
ds_read_b64_tr_b16 v[198:199], v240 offset:60160
v_mfma_f32_16x16x32_bf16 v[8:11], v[200:203], a[8:11], v[8:11]
v_mfma_f32_16x16x32_bf16 v[40:43], v[200:203], a[12:15], v[40:43]
buffer_load_dwordx4 a[40:43], v246, s[44:47], s54 offen offset:1024 sc1
v_mfma_f32_16x16x32_bf16 v[72:75], v[200:203], a[64:67], v[72:75]
v_mfma_f32_16x16x32_bf16 v[104:107], v[200:203], a[68:71], v[104:107]
ds_read_b64_tr_b16 v[200:201], v240 offset:0
ds_read_b64_tr_b16 v[202:203], v240 offset:1024
v_mfma_f32_16x16x32_bf16 v[12:15], v[204:207], a[8:11], v[12:15]
v_mfma_f32_16x16x32_bf16 v[44:47], v[204:207], a[12:15], v[44:47]
buffer_load_dwordx4 a[44:47], v247, s[44:47], s54 offen offset:1024 sc1
v_mfma_f32_16x16x32_bf16 v[76:79], v[204:207], a[64:67], v[76:79]
v_mfma_f32_16x16x32_bf16 v[108:111], v[204:207], a[68:71], v[108:111]
ds_read_b64_tr_b16 v[204:205], v240 offset:8448
ds_read_b64_tr_b16 v[206:207], v240 offset:9472
v_mfma_f32_16x16x32_bf16 v[16:19], v[208:211], a[8:11], v[16:19]
v_mfma_f32_16x16x32_bf16 v[48:51], v[208:211], a[12:15], v[48:51]
buffer_load_dwordx4 a[48:51], v246, s[44:47], s54 offen offset:2048 sc1
v_mfma_f32_16x16x32_bf16 v[80:83], v[208:211], a[64:67], v[80:83]
v_mfma_f32_16x16x32_bf16 v[112:115], v[208:211], a[68:71], v[112:115]
ds_read_b64_tr_b16 v[208:209], v240 offset:16896
ds_read_b64_tr_b16 v[210:211], v240 offset:17920
v_mfma_f32_16x16x32_bf16 v[20:23], v[212:215], a[8:11], v[20:23]
v_mfma_f32_16x16x32_bf16 v[52:55], v[212:215], a[12:15], v[52:55]
buffer_load_dwordx4 a[52:55], v247, s[44:47], s54 offen offset:2048 sc1
v_mfma_f32_16x16x32_bf16 v[84:87], v[212:215], a[64:67], v[84:87]
v_mfma_f32_16x16x32_bf16 v[116:119], v[212:215], a[68:71], v[116:119]
ds_read_b64_tr_b16 v[212:213], v240 offset:25344
ds_read_b64_tr_b16 v[214:215], v240 offset:26368
v_mfma_f32_16x16x32_bf16 v[24:27], v[216:219], a[8:11], v[24:27]
v_mfma_f32_16x16x32_bf16 v[56:59], v[216:219], a[12:15], v[56:59]
buffer_load_dwordx4 a[104:107], v246, s[44:47], s54 offen offset:3072 sc1
v_mfma_f32_16x16x32_bf16 v[88:91], v[216:219], a[64:67], v[88:91]
v_mfma_f32_16x16x32_bf16 v[120:123], v[216:219], a[68:71], v[120:123]
ds_read_b64_tr_b16 v[216:217], v240 offset:33792
ds_read_b64_tr_b16 v[218:219], v240 offset:34816
v_mfma_f32_16x16x32_bf16 v[28:31], v[220:223], a[8:11], v[28:31]
v_mfma_f32_16x16x32_bf16 v[60:63], v[220:223], a[12:15], v[60:63]
v_mfma_f32_16x16x32_bf16 v[92:95], v[220:223], a[64:67], v[92:95]
s_waitcnt vmcnt(15) lgkmcnt(6)
v_mfma_f32_16x16x32_bf16 v[124:127], v[220:223], a[68:71], v[124:127]
ds_read_b64_tr_b16 v[220:221], v240 offset:42240
ds_read_b64_tr_b16 v[222:223], v240 offset:43264
v_mfma_f32_16x16x32_bf16 v[0:3], v[192:195], a[16:19], v[0:3]
buffer_load_dwordx4 a[8:11], v247, s[44:47], s54 offen offset:3072 sc1
v_mfma_f32_16x16x32_bf16 v[32:35], v[192:195], a[20:23], v[32:35]
ds_read_b64_tr_b16 v[192:193], v241 offset:50688
ds_read_b64_tr_b16 v[194:195], v241 offset:51712
v_mfma_f32_16x16x32_bf16 v[0:3], v[196:199], a[24:27], v[0:3]
v_mfma_f32_16x16x32_bf16 v[32:35], v[196:199], a[28:31], v[32:35]
ds_read_b64_tr_b16 v[196:197], v241 offset:59136
ds_read_b64_tr_b16 v[198:199], v241 offset:60160
v_mfma_f32_16x16x32_bf16 v[0:3], v[200:203], a[72:75], v[0:3]
buffer_load_dwordx4 a[12:15], v246, s[44:47], s55 offen offset:0 sc1
v_mfma_f32_16x16x32_bf16 v[32:35], v[200:203], a[80:83], v[32:35]
ds_read_b64_tr_b16 v[200:201], v241 offset:0
ds_read_b64_tr_b16 v[202:203], v241 offset:1024
v_mfma_f32_16x16x32_bf16 v[0:3], v[204:207], a[84:87], v[0:3]
s_waitcnt vmcnt(9) lgkmcnt(6)
v_mfma_f32_16x16x32_bf16 v[32:35], v[204:207], a[56:59], v[32:35]
ds_read_b64_tr_b16 v[204:205], v241 offset:8448
ds_read_b64_tr_b16 v[206:207], v241 offset:9472
v_mfma_f32_16x16x32_bf16 v[0:3], v[208:211], a[60:63], v[0:3]
buffer_load_dwordx4 a[64:67], v247, s[44:47], s55 offen offset:0 sc1
v_mfma_f32_16x16x32_bf16 v[32:35], v[208:211], a[76:79], v[32:35]
ds_read_b64_tr_b16 v[208:209], v241 offset:16896
ds_read_b64_tr_b16 v[210:211], v241 offset:17920
v_mfma_f32_16x16x32_bf16 v[0:3], v[212:215], a[88:91], v[0:3]
v_mfma_f32_16x16x32_bf16 v[32:35], v[212:215], a[92:95], v[32:35]
ds_read_b64_tr_b16 v[212:213], v241 offset:25344
ds_read_b64_tr_b16 v[214:215], v241 offset:26368
v_mfma_f32_16x16x32_bf16 v[0:3], v[216:219], a[96:99], v[0:3]
buffer_load_dwordx4 a[68:71], v246, s[44:47], s55 offen offset:1024 sc1
v_mfma_f32_16x16x32_bf16 v[32:35], v[216:219], a[100:103], v[32:35]
ds_read_b64_tr_b16 v[216:217], v241 offset:33792
ds_read_b64_tr_b16 v[218:219], v241 offset:34816
v_mfma_f32_16x16x32_bf16 v[0:3], v[220:223], a[0:3], v[0:3]
s_waitcnt lgkmcnt(6)
v_mfma_f32_16x16x32_bf16 v[32:35], v[220:223], a[32:35], v[32:35]
ds_read_b64_tr_b16 v[220:221], v241 offset:42240
ds_read_b64_tr_b16 v[222:223], v241 offset:43264
v_mfma_f32_16x16x32_bf16 v[4:7], v[192:195], a[16:19], v[4:7]
buffer_load_dwordx4 a[108:111], v247, s[44:47], s55 offen offset:1024 sc1
v_mfma_f32_16x16x32_bf16 v[36:39], v[192:195], a[20:23], v[36:39]
ds_read_b64_tr_b16 v[192:193], v242 offset:50688
ds_read_b64_tr_b16 v[194:195], v242 offset:51712
v_mfma_f32_16x16x32_bf16 v[4:7], v[196:199], a[24:27], v[4:7]
v_mfma_f32_16x16x32_bf16 v[36:39], v[196:199], a[28:31], v[36:39]
ds_read_b64_tr_b16 v[196:197], v242 offset:59136
ds_read_b64_tr_b16 v[198:199], v242 offset:60160
v_mfma_f32_16x16x32_bf16 v[4:7], v[200:203], a[72:75], v[4:7]
buffer_load_dwordx4 a[112:115], v246, s[44:47], s55 offen offset:2048 sc1
v_mfma_f32_16x16x32_bf16 v[36:39], v[200:203], a[80:83], v[36:39]
ds_read_b64_tr_b16 v[200:201], v242 offset:0
ds_read_b64_tr_b16 v[202:203], v242 offset:1024
v_mfma_f32_16x16x32_bf16 v[4:7], v[204:207], a[84:87], v[4:7]
s_waitcnt lgkmcnt(6)
v_mfma_f32_16x16x32_bf16 v[36:39], v[204:207], a[56:59], v[36:39]
ds_read_b64_tr_b16 v[204:205], v242 offset:8448
ds_read_b64_tr_b16 v[206:207], v242 offset:9472
v_mfma_f32_16x16x32_bf16 v[4:7], v[208:211], a[60:63], v[4:7]
buffer_load_dwordx4 a[116:119], v247, s[44:47], s55 offen offset:2048 sc1
v_mfma_f32_16x16x32_bf16 v[36:39], v[208:211], a[76:79], v[36:39]
ds_read_b64_tr_b16 v[208:209], v242 offset:16896
ds_read_b64_tr_b16 v[210:211], v242 offset:17920
v_mfma_f32_16x16x32_bf16 v[4:7], v[212:215], a[88:91], v[4:7]
v_mfma_f32_16x16x32_bf16 v[36:39], v[212:215], a[92:95], v[36:39]
ds_read_b64_tr_b16 v[212:213], v242 offset:25344
ds_read_b64_tr_b16 v[214:215], v242 offset:26368
v_mfma_f32_16x16x32_bf16 v[4:7], v[216:219], a[96:99], v[4:7]
v_mfma_f32_16x16x32_bf16 v[36:39], v[216:219], a[100:103], v[36:39]
ds_read_b64_tr_b16 v[216:217], v242 offset:33792
ds_read_b64_tr_b16 v[218:219], v242 offset:34816
v_mfma_f32_16x16x32_bf16 v[4:7], v[220:223], a[0:3], v[4:7]
s_waitcnt lgkmcnt(6)
v_mfma_f32_16x16x32_bf16 v[36:39], v[220:223], a[32:35], v[36:39]
ds_read_b64_tr_b16 v[220:221], v242 offset:42240
ds_read_b64_tr_b16 v[222:223], v242 offset:43264
v_mfma_f32_16x16x32_bf16 v[8:11], v[192:195], a[16:19], v[8:11]
buffer_load_dwordx4 a[120:123], v246, s[44:47], s55 offen offset:3072 sc1
v_mfma_f32_16x16x32_bf16 v[40:43], v[192:195], a[20:23], v[40:43]
ds_read_b64_tr_b16 v[192:193], v243 offset:50688
ds_read_b64_tr_b16 v[194:195], v243 offset:51712
v_mfma_f32_16x16x32_bf16 v[8:11], v[196:199], a[24:27], v[8:11]
v_cvt_pk_bf16_f32 v230, v0, v1
v_cvt_pk_bf16_f32 v232, v4, v5
v_mfma_f32_16x16x32_bf16 v[40:43], v[196:199], a[28:31], v[40:43]
ds_read_b64_tr_b16 v[196:197], v243 offset:59136
ds_read_b64_tr_b16 v[198:199], v243 offset:60160
v_mfma_f32_16x16x32_bf16 v[8:11], v[200:203], a[72:75], v[8:11]
buffer_load_dwordx4 a[124:127], v247, s[44:47], s55 offen offset:3072 sc1
v_mfma_f32_16x16x32_bf16 v[40:43], v[200:203], a[80:83], v[40:43]
ds_read_b64_tr_b16 v[200:201], v243 offset:0
ds_read_b64_tr_b16 v[202:203], v243 offset:1024
v_mfma_f32_16x16x32_bf16 v[8:11], v[204:207], a[84:87], v[8:11]
s_waitcnt lgkmcnt(6)
v_bfe_u32 v250, v255, 6, 1
v_mfma_f32_16x16x32_bf16 v[40:43], v[204:207], a[56:59], v[40:43]
ds_read_b64_tr_b16 v[204:205], v243 offset:8448
ds_read_b64_tr_b16 v[206:207], v243 offset:9472
v_mfma_f32_16x16x32_bf16 v[8:11], v[208:211], a[60:63], v[8:11]
v_cvt_pk_bf16_f32 v231, v2, v3
v_cvt_pk_bf16_f32 v233, v6, v7
v_mfma_f32_16x16x32_bf16 v[40:43], v[208:211], a[76:79], v[40:43]
ds_read_b64_tr_b16 v[208:209], v243 offset:16896
ds_read_b64_tr_b16 v[210:211], v243 offset:17920
v_mfma_f32_16x16x32_bf16 v[8:11], v[212:215], a[88:91], v[8:11]
v_permlane16_swap_b32 v230, v232
v_permlane16_swap_b32 v231, v233
v_mfma_f32_16x16x32_bf16 v[40:43], v[212:215], a[92:95], v[40:43]
ds_read_b64_tr_b16 v[212:213], v243 offset:25344
ds_read_b64_tr_b16 v[214:215], v243 offset:26368
v_mfma_f32_16x16x32_bf16 v[8:11], v[216:219], a[96:99], v[8:11]
buffer_store_dwordx4 v[230:233], v238, s[32:35], s94 offen offset:0 sc1
v_mfma_f32_16x16x32_bf16 v[40:43], v[216:219], a[100:103], v[40:43]
ds_read_b64_tr_b16 v[216:217], v243 offset:33792
ds_read_b64_tr_b16 v[218:219], v243 offset:34816
v_mfma_f32_16x16x32_bf16 v[8:11], v[220:223], a[0:3], v[8:11]
s_waitcnt lgkmcnt(6)
v_mul_u32_u24 v250, 7, v250
v_mfma_f32_16x16x32_bf16 v[40:43], v[220:223], a[32:35], v[40:43]
ds_read_b64_tr_b16 v[220:221], v243 offset:42240
ds_read_b64_tr_b16 v[222:223], v243 offset:43264
v_mfma_f32_16x16x32_bf16 v[12:15], v[192:195], a[16:19], v[12:15]
v_cvt_pk_bf16_f32 v234, v32, v33
v_cvt_pk_bf16_f32 v236, v36, v37
v_mfma_f32_16x16x32_bf16 v[44:47], v[192:195], a[20:23], v[44:47]
ds_read_b64_tr_b16 v[192:193], v240 offset:50816
ds_read_b64_tr_b16 v[194:195], v240 offset:51840
v_mfma_f32_16x16x32_bf16 v[12:15], v[196:199], a[24:27], v[12:15]
v_cvt_pk_bf16_f32 v235, v34, v35
v_cvt_pk_bf16_f32 v237, v38, v39
v_mfma_f32_16x16x32_bf16 v[44:47], v[196:199], a[28:31], v[44:47]
ds_read_b64_tr_b16 v[196:197], v240 offset:59264
ds_read_b64_tr_b16 v[198:199], v240 offset:60288
v_mfma_f32_16x16x32_bf16 v[12:15], v[200:203], a[72:75], v[12:15]
v_permlane16_swap_b32 v234, v236
v_permlane16_swap_b32 v235, v237
v_mfma_f32_16x16x32_bf16 v[44:47], v[200:203], a[80:83], v[44:47]
ds_read_b64_tr_b16 v[200:201], v240 offset:128
ds_read_b64_tr_b16 v[202:203], v240 offset:1152
v_mfma_f32_16x16x32_bf16 v[12:15], v[204:207], a[84:87], v[12:15]
s_waitcnt lgkmcnt(6)
v_xor_b32 v250, s3, v250
v_mfma_f32_16x16x32_bf16 v[44:47], v[204:207], a[56:59], v[44:47]
ds_read_b64_tr_b16 v[204:205], v240 offset:8576
ds_read_b64_tr_b16 v[206:207], v240 offset:9600
v_mfma_f32_16x16x32_bf16 v[12:15], v[208:211], a[60:63], v[12:15]
buffer_store_dwordx4 v[234:237], v238, s[32:35], s95 offen offset:0 sc1
v_mfma_f32_16x16x32_bf16 v[44:47], v[208:211], a[76:79], v[44:47]
ds_read_b64_tr_b16 v[208:209], v240 offset:17024
ds_read_b64_tr_b16 v[210:211], v240 offset:18048
v_mfma_f32_16x16x32_bf16 v[12:15], v[212:215], a[88:91], v[12:15]
v_bfe_u32 v251, v255, 4, 2
v_lshl_add_u32 v250, v250, 2, v251
v_mfma_f32_16x16x32_bf16 v[44:47], v[212:215], a[92:95], v[44:47]
ds_read_b64_tr_b16 v[212:213], v240 offset:25472
ds_read_b64_tr_b16 v[214:215], v240 offset:26496
v_mfma_f32_16x16x32_bf16 v[12:15], v[216:219], a[96:99], v[12:15]
v_lshlrev_b32 v250, 9, v250
v_and_b32 v248, 0x1f0, v255
v_mfma_f32_16x16x32_bf16 v[44:47], v[216:219], a[100:103], v[44:47]
ds_read_b64_tr_b16 v[216:217], v240 offset:33920
ds_read_b64_tr_b16 v[218:219], v240 offset:34944
v_mfma_f32_16x16x32_bf16 v[12:15], v[220:223], a[0:3], v[12:15]
s_waitcnt lgkmcnt(6)
v_lshrrev_b32 v248, 1, v248
v_mfma_f32_16x16x32_bf16 v[44:47], v[220:223], a[32:35], v[44:47]
ds_read_b64_tr_b16 v[220:221], v240 offset:42368
ds_read_b64_tr_b16 v[222:223], v240 offset:43392
v_mfma_f32_16x16x32_bf16 v[16:19], v[192:195], a[16:19], v[16:19]
v_cvt_pk_bf16_f32 v230, v8, v9
v_cvt_pk_bf16_f32 v232, v12, v13
v_mfma_f32_16x16x32_bf16 v[48:51], v[192:195], a[20:23], v[48:51]
ds_read_b64_tr_b16 v[192:193], v241 offset:50816
ds_read_b64_tr_b16 v[194:195], v241 offset:51840
v_mfma_f32_16x16x32_bf16 v[16:19], v[196:199], a[24:27], v[16:19]
v_cvt_pk_bf16_f32 v231, v10, v11
v_cvt_pk_bf16_f32 v233, v14, v15
v_mfma_f32_16x16x32_bf16 v[48:51], v[196:199], a[28:31], v[48:51]
ds_read_b64_tr_b16 v[196:197], v241 offset:59264
ds_read_b64_tr_b16 v[198:199], v241 offset:60288
v_mfma_f32_16x16x32_bf16 v[16:19], v[200:203], a[72:75], v[16:19]
v_permlane16_swap_b32 v230, v232
v_permlane16_swap_b32 v231, v233
v_mfma_f32_16x16x32_bf16 v[48:51], v[200:203], a[80:83], v[48:51]
ds_read_b64_tr_b16 v[200:201], v241 offset:128
ds_read_b64_tr_b16 v[202:203], v241 offset:1152
v_mfma_f32_16x16x32_bf16 v[16:19], v[204:207], a[84:87], v[16:19]
s_waitcnt lgkmcnt(6)
v_mfma_f32_16x16x32_bf16 v[48:51], v[204:207], a[56:59], v[48:51]
ds_read_b64_tr_b16 v[204:205], v241 offset:8576
ds_read_b64_tr_b16 v[206:207], v241 offset:9600
v_mfma_f32_16x16x32_bf16 v[16:19], v[208:211], a[60:63], v[16:19]
buffer_store_dwordx4 v[230:233], v238, s[32:35], s94 offen offset:1024 sc1
v_mfma_f32_16x16x32_bf16 v[48:51], v[208:211], a[76:79], v[48:51]
ds_read_b64_tr_b16 v[208:209], v241 offset:17024
ds_read_b64_tr_b16 v[210:211], v241 offset:18048
v_mfma_f32_16x16x32_bf16 v[16:19], v[212:215], a[88:91], v[16:19]
v_cvt_pk_bf16_f32 v234, v40, v41
v_cvt_pk_bf16_f32 v236, v44, v45
v_mfma_f32_16x16x32_bf16 v[48:51], v[212:215], a[92:95], v[48:51]
ds_read_b64_tr_b16 v[212:213], v241 offset:25472
ds_read_b64_tr_b16 v[214:215], v241 offset:26496
v_mfma_f32_16x16x32_bf16 v[16:19], v[216:219], a[96:99], v[16:19]
v_cvt_pk_bf16_f32 v235, v42, v43
v_cvt_pk_bf16_f32 v237, v46, v47
v_mfma_f32_16x16x32_bf16 v[48:51], v[216:219], a[100:103], v[48:51]
ds_read_b64_tr_b16 v[216:217], v241 offset:33920
ds_read_b64_tr_b16 v[218:219], v241 offset:34944
v_mfma_f32_16x16x32_bf16 v[16:19], v[220:223], a[0:3], v[16:19]
s_waitcnt lgkmcnt(6)
v_mfma_f32_16x16x32_bf16 v[48:51], v[220:223], a[32:35], v[48:51]
ds_read_b64_tr_b16 v[220:221], v241 offset:42368
ds_read_b64_tr_b16 v[222:223], v241 offset:43392
v_mfma_f32_16x16x32_bf16 v[20:23], v[192:195], a[16:19], v[20:23]
v_permlane16_swap_b32 v234, v236
v_permlane16_swap_b32 v235, v237
v_mfma_f32_16x16x32_bf16 v[52:55], v[192:195], a[20:23], v[52:55]
ds_read_b64_tr_b16 v[192:193], v242 offset:50816
ds_read_b64_tr_b16 v[194:195], v242 offset:51840
v_mfma_f32_16x16x32_bf16 v[20:23], v[196:199], a[24:27], v[20:23]
v_mfma_f32_16x16x32_bf16 v[52:55], v[196:199], a[28:31], v[52:55]
ds_read_b64_tr_b16 v[196:197], v242 offset:59264
ds_read_b64_tr_b16 v[198:199], v242 offset:60288
v_mfma_f32_16x16x32_bf16 v[20:23], v[200:203], a[72:75], v[20:23]
buffer_store_dwordx4 v[234:237], v238, s[32:35], s95 offen offset:1024 sc1
v_mfma_f32_16x16x32_bf16 v[52:55], v[200:203], a[80:83], v[52:55]
ds_read_b64_tr_b16 v[200:201], v242 offset:128
ds_read_b64_tr_b16 v[202:203], v242 offset:1152
v_mfma_f32_16x16x32_bf16 v[20:23], v[204:207], a[84:87], v[20:23]
s_waitcnt lgkmcnt(6)
v_mfma_f32_16x16x32_bf16 v[52:55], v[204:207], a[56:59], v[52:55]
ds_read_b64_tr_b16 v[204:205], v242 offset:8576
ds_read_b64_tr_b16 v[206:207], v242 offset:9600
v_mfma_f32_16x16x32_bf16 v[20:23], v[208:211], a[60:63], v[20:23]
buffer_load_dwordx4 v[136:139], v224, s[68:71], 0 offen offset:0
v_mfma_f32_16x16x32_bf16 v[52:55], v[208:211], a[76:79], v[52:55]
ds_read_b64_tr_b16 v[208:209], v242 offset:17024
ds_read_b64_tr_b16 v[210:211], v242 offset:18048
v_mfma_f32_16x16x32_bf16 v[20:23], v[212:215], a[88:91], v[20:23]
v_mfma_f32_16x16x32_bf16 v[52:55], v[212:215], a[92:95], v[52:55]
ds_read_b64_tr_b16 v[212:213], v242 offset:25472
ds_read_b64_tr_b16 v[214:215], v242 offset:26496
v_mfma_f32_16x16x32_bf16 v[20:23], v[216:219], a[96:99], v[20:23]
buffer_load_dwordx4 v[140:143], v225, s[48:51], 0 offen offset:0
v_mfma_f32_16x16x32_bf16 v[52:55], v[216:219], a[100:103], v[52:55]
ds_read_b64_tr_b16 v[216:217], v242 offset:33920
ds_read_b64_tr_b16 v[218:219], v242 offset:34944
v_mfma_f32_16x16x32_bf16 v[20:23], v[220:223], a[0:3], v[20:23]
s_waitcnt lgkmcnt(6)
v_mfma_f32_16x16x32_bf16 v[52:55], v[220:223], a[32:35], v[52:55]
ds_read_b64_tr_b16 v[220:221], v242 offset:42368
ds_read_b64_tr_b16 v[222:223], v242 offset:43392
v_mfma_f32_16x16x32_bf16 v[24:27], v[192:195], a[16:19], v[24:27]
v_cvt_pk_bf16_f32 v230, v16, v17
v_cvt_pk_bf16_f32 v232, v20, v21
v_mfma_f32_16x16x32_bf16 v[56:59], v[192:195], a[20:23], v[56:59]
ds_read_b64_tr_b16 v[192:193], v243 offset:50816
ds_read_b64_tr_b16 v[194:195], v243 offset:51840
v_mfma_f32_16x16x32_bf16 v[24:27], v[196:199], a[24:27], v[24:27]
v_cvt_pk_bf16_f32 v231, v18, v19
v_cvt_pk_bf16_f32 v233, v22, v23
v_mfma_f32_16x16x32_bf16 v[56:59], v[196:199], a[28:31], v[56:59]
ds_read_b64_tr_b16 v[196:197], v243 offset:59264
ds_read_b64_tr_b16 v[198:199], v243 offset:60288
v_mfma_f32_16x16x32_bf16 v[24:27], v[200:203], a[72:75], v[24:27]
v_permlane16_swap_b32 v230, v232
v_permlane16_swap_b32 v231, v233
v_mfma_f32_16x16x32_bf16 v[56:59], v[200:203], a[80:83], v[56:59]
ds_read_b64_tr_b16 v[200:201], v243 offset:128
ds_read_b64_tr_b16 v[202:203], v243 offset:1152
v_mfma_f32_16x16x32_bf16 v[24:27], v[204:207], a[84:87], v[24:27]
s_waitcnt lgkmcnt(6)
v_mfma_f32_16x16x32_bf16 v[56:59], v[204:207], a[56:59], v[56:59]
ds_read_b64_tr_b16 v[204:205], v243 offset:8576
ds_read_b64_tr_b16 v[206:207], v243 offset:9600
v_mfma_f32_16x16x32_bf16 v[24:27], v[208:211], a[60:63], v[24:27]
buffer_store_dwordx4 v[230:233], v238, s[32:35], s94 offen offset:2048 sc1
v_mfma_f32_16x16x32_bf16 v[56:59], v[208:211], a[76:79], v[56:59]
ds_read_b64_tr_b16 v[208:209], v243 offset:17024
ds_read_b64_tr_b16 v[210:211], v243 offset:18048
v_mfma_f32_16x16x32_bf16 v[24:27], v[212:215], a[88:91], v[24:27]
v_cvt_pk_bf16_f32 v234, v48, v49
v_cvt_pk_bf16_f32 v236, v52, v53
v_mfma_f32_16x16x32_bf16 v[56:59], v[212:215], a[92:95], v[56:59]
ds_read_b64_tr_b16 v[212:213], v243 offset:25472
ds_read_b64_tr_b16 v[214:215], v243 offset:26496
v_mfma_f32_16x16x32_bf16 v[24:27], v[216:219], a[96:99], v[24:27]
v_cvt_pk_bf16_f32 v235, v50, v51
v_cvt_pk_bf16_f32 v237, v54, v55
v_mfma_f32_16x16x32_bf16 v[56:59], v[216:219], a[100:103], v[56:59]
ds_read_b64_tr_b16 v[216:217], v243 offset:33920
ds_read_b64_tr_b16 v[218:219], v243 offset:34944
v_mfma_f32_16x16x32_bf16 v[24:27], v[220:223], a[0:3], v[24:27]
s_waitcnt lgkmcnt(6)
v_mfma_f32_16x16x32_bf16 v[56:59], v[220:223], a[32:35], v[56:59]
ds_read_b64_tr_b16 v[220:221], v243 offset:42368
ds_read_b64_tr_b16 v[222:223], v243 offset:43392
v_mfma_f32_16x16x32_bf16 v[28:31], v[192:195], a[16:19], v[28:31]
v_permlane16_swap_b32 v234, v236
v_permlane16_swap_b32 v235, v237
v_mfma_f32_16x16x32_bf16 v[60:63], v[192:195], a[20:23], v[60:63]
ds_read_b64_tr_b16 v[192:193], v240 offset:50688
ds_read_b64_tr_b16 v[194:195], v240 offset:51712
v_mfma_f32_16x16x32_bf16 v[28:31], v[196:199], a[24:27], v[28:31]
v_mfma_f32_16x16x32_bf16 v[60:63], v[196:199], a[28:31], v[60:63]
ds_read_b64_tr_b16 v[196:197], v240 offset:59136
ds_read_b64_tr_b16 v[198:199], v240 offset:60160
v_mfma_f32_16x16x32_bf16 v[28:31], v[200:203], a[72:75], v[28:31]
buffer_store_dwordx4 v[234:237], v238, s[32:35], s95 offen offset:2048 sc1
v_mfma_f32_16x16x32_bf16 v[60:63], v[200:203], a[80:83], v[60:63]
ds_read_b64_tr_b16 v[200:201], v240 offset:0
ds_read_b64_tr_b16 v[202:203], v240 offset:1024
v_mfma_f32_16x16x32_bf16 v[28:31], v[204:207], a[84:87], v[28:31]
s_waitcnt lgkmcnt(6)
v_mfma_f32_16x16x32_bf16 v[60:63], v[204:207], a[56:59], v[60:63]
ds_read_b64_tr_b16 v[204:205], v240 offset:8448
ds_read_b64_tr_b16 v[206:207], v240 offset:9472
v_mfma_f32_16x16x32_bf16 v[28:31], v[208:211], a[60:63], v[28:31]
buffer_load_dwordx4 v[152:155], v224, s[68:71], 0 offen offset:512
v_mfma_f32_16x16x32_bf16 v[60:63], v[208:211], a[76:79], v[60:63]
ds_read_b64_tr_b16 v[208:209], v240 offset:16896
ds_read_b64_tr_b16 v[210:211], v240 offset:17920
v_mfma_f32_16x16x32_bf16 v[28:31], v[212:215], a[88:91], v[28:31]
v_mfma_f32_16x16x32_bf16 v[60:63], v[212:215], a[92:95], v[60:63]
ds_read_b64_tr_b16 v[212:213], v240 offset:25344
ds_read_b64_tr_b16 v[214:215], v240 offset:26368
v_mfma_f32_16x16x32_bf16 v[28:31], v[216:219], a[96:99], v[28:31]
buffer_load_dwordx4 v[156:159], v225, s[48:51], 0 offen offset:512
v_mfma_f32_16x16x32_bf16 v[60:63], v[216:219], a[100:103], v[60:63]
ds_read_b64_tr_b16 v[216:217], v240 offset:33792
ds_read_b64_tr_b16 v[218:219], v240 offset:34816
v_mfma_f32_16x16x32_bf16 v[28:31], v[220:223], a[0:3], v[28:31]
s_waitcnt vmcnt(18) lgkmcnt(6)
v_mfma_f32_16x16x32_bf16 v[60:63], v[220:223], a[32:35], v[60:63]
ds_read_b64_tr_b16 v[220:221], v240 offset:42240
ds_read_b64_tr_b16 v[222:223], v240 offset:43264
v_mfma_f32_16x16x32_bf16 v[64:67], v[192:195], a[4:7], v[64:67]
v_cvt_pk_bf16_f32 v230, v24, v25
v_cvt_pk_bf16_f32 v232, v28, v29
v_mfma_f32_16x16x32_bf16 v[96:99], v[192:195], a[36:39], v[96:99]
ds_read_b64_tr_b16 v[192:193], v241 offset:50688
ds_read_b64_tr_b16 v[194:195], v241 offset:51712
v_mfma_f32_16x16x32_bf16 v[64:67], v[196:199], a[40:43], v[64:67]
v_cvt_pk_bf16_f32 v231, v26, v27
v_cvt_pk_bf16_f32 v233, v30, v31
v_mfma_f32_16x16x32_bf16 v[96:99], v[196:199], a[44:47], v[96:99]
ds_read_b64_tr_b16 v[196:197], v241 offset:59136
ds_read_b64_tr_b16 v[198:199], v241 offset:60160
v_mfma_f32_16x16x32_bf16 v[64:67], v[200:203], a[48:51], v[64:67]
v_permlane16_swap_b32 v230, v232
v_permlane16_swap_b32 v231, v233
v_mfma_f32_16x16x32_bf16 v[96:99], v[200:203], a[52:55], v[96:99]
ds_read_b64_tr_b16 v[200:201], v241 offset:0
ds_read_b64_tr_b16 v[202:203], v241 offset:1024
v_mfma_f32_16x16x32_bf16 v[64:67], v[204:207], a[104:107], v[64:67]
s_waitcnt vmcnt(10) lgkmcnt(6)
v_mfma_f32_16x16x32_bf16 v[96:99], v[204:207], a[8:11], v[96:99]
ds_read_b64_tr_b16 v[204:205], v241 offset:8448
ds_read_b64_tr_b16 v[206:207], v241 offset:9472
v_mfma_f32_16x16x32_bf16 v[64:67], v[208:211], a[12:15], v[64:67]
buffer_store_dwordx4 v[230:233], v238, s[32:35], s94 offen offset:3072 sc1
v_mfma_f32_16x16x32_bf16 v[96:99], v[208:211], a[64:67], v[96:99]
ds_read_b64_tr_b16 v[208:209], v241 offset:16896
ds_read_b64_tr_b16 v[210:211], v241 offset:17920
v_mfma_f32_16x16x32_bf16 v[64:67], v[212:215], a[68:71], v[64:67]
v_cvt_pk_bf16_f32 v234, v56, v57
v_cvt_pk_bf16_f32 v236, v60, v61
v_mfma_f32_16x16x32_bf16 v[96:99], v[212:215], a[108:111], v[96:99]
ds_read_b64_tr_b16 v[212:213], v241 offset:25344
ds_read_b64_tr_b16 v[214:215], v241 offset:26368
v_mfma_f32_16x16x32_bf16 v[64:67], v[216:219], a[112:115], v[64:67]
v_cvt_pk_bf16_f32 v235, v58, v59
v_cvt_pk_bf16_f32 v237, v62, v63
v_mfma_f32_16x16x32_bf16 v[96:99], v[216:219], a[116:119], v[96:99]
ds_read_b64_tr_b16 v[216:217], v241 offset:33792
ds_read_b64_tr_b16 v[218:219], v241 offset:34816
v_mfma_f32_16x16x32_bf16 v[64:67], v[220:223], a[120:123], v[64:67]
s_waitcnt lgkmcnt(6)
v_mfma_f32_16x16x32_bf16 v[96:99], v[220:223], a[124:127], v[96:99]
ds_read_b64_tr_b16 v[220:221], v241 offset:42240
ds_read_b64_tr_b16 v[222:223], v241 offset:43264
v_mfma_f32_16x16x32_bf16 v[68:71], v[192:195], a[4:7], v[68:71]
v_permlane16_swap_b32 v234, v236
v_permlane16_swap_b32 v235, v237
v_mfma_f32_16x16x32_bf16 v[100:103], v[192:195], a[36:39], v[100:103]
ds_read_b64_tr_b16 v[192:193], v242 offset:50688
ds_read_b64_tr_b16 v[194:195], v242 offset:51712
v_mfma_f32_16x16x32_bf16 v[68:71], v[196:199], a[40:43], v[68:71]
v_mfma_f32_16x16x32_bf16 v[100:103], v[196:199], a[44:47], v[100:103]
ds_read_b64_tr_b16 v[196:197], v242 offset:59136
ds_read_b64_tr_b16 v[198:199], v242 offset:60160
v_mfma_f32_16x16x32_bf16 v[68:71], v[200:203], a[48:51], v[68:71]
buffer_store_dwordx4 v[234:237], v238, s[32:35], s95 offen offset:3072 sc1
v_mfma_f32_16x16x32_bf16 v[100:103], v[200:203], a[52:55], v[100:103]
ds_read_b64_tr_b16 v[200:201], v242 offset:0
ds_read_b64_tr_b16 v[202:203], v242 offset:1024
v_mfma_f32_16x16x32_bf16 v[68:71], v[204:207], a[104:107], v[68:71]
s_waitcnt lgkmcnt(6)
v_mfma_f32_16x16x32_bf16 v[100:103], v[204:207], a[8:11], v[100:103]
ds_read_b64_tr_b16 v[204:205], v242 offset:8448
ds_read_b64_tr_b16 v[206:207], v242 offset:9472
v_mfma_f32_16x16x32_bf16 v[68:71], v[208:211], a[12:15], v[68:71]
buffer_load_dwordx4 v[168:171], v224, s[68:71], 0 offen offset:1024
v_mfma_f32_16x16x32_bf16 v[100:103], v[208:211], a[64:67], v[100:103]
ds_read_b64_tr_b16 v[208:209], v242 offset:16896
ds_read_b64_tr_b16 v[210:211], v242 offset:17920
v_mfma_f32_16x16x32_bf16 v[68:71], v[212:215], a[68:71], v[68:71]
v_mfma_f32_16x16x32_bf16 v[100:103], v[212:215], a[108:111], v[100:103]
ds_read_b64_tr_b16 v[212:213], v242 offset:25344
ds_read_b64_tr_b16 v[214:215], v242 offset:26368
v_mfma_f32_16x16x32_bf16 v[68:71], v[216:219], a[112:115], v[68:71]
buffer_load_dwordx4 v[172:175], v225, s[48:51], 0 offen offset:1024
v_mfma_f32_16x16x32_bf16 v[100:103], v[216:219], a[116:119], v[100:103]
ds_read_b64_tr_b16 v[216:217], v242 offset:33792
ds_read_b64_tr_b16 v[218:219], v242 offset:34816
v_mfma_f32_16x16x32_bf16 v[68:71], v[220:223], a[120:123], v[68:71]
s_waitcnt lgkmcnt(6)
v_mfma_f32_16x16x32_bf16 v[100:103], v[220:223], a[124:127], v[100:103]
ds_read_b64_tr_b16 v[220:221], v242 offset:42240
ds_read_b64_tr_b16 v[222:223], v242 offset:43264
v_mfma_f32_16x16x32_bf16 v[72:75], v[192:195], a[4:7], v[72:75]
v_cvt_pk_bf16_f32 v230, v64, v65
v_cvt_pk_bf16_f32 v232, v68, v69
v_mfma_f32_16x16x32_bf16 v[104:107], v[192:195], a[36:39], v[104:107]
ds_read_b64_tr_b16 v[192:193], v243 offset:50688
ds_read_b64_tr_b16 v[194:195], v243 offset:51712
v_mfma_f32_16x16x32_bf16 v[72:75], v[196:199], a[40:43], v[72:75]
v_cvt_pk_bf16_f32 v231, v66, v67
v_cvt_pk_bf16_f32 v233, v70, v71
v_mfma_f32_16x16x32_bf16 v[104:107], v[196:199], a[44:47], v[104:107]
ds_read_b64_tr_b16 v[196:197], v243 offset:59136
ds_read_b64_tr_b16 v[198:199], v243 offset:60160
v_mfma_f32_16x16x32_bf16 v[72:75], v[200:203], a[48:51], v[72:75]
v_permlane16_swap_b32 v230, v232
v_permlane16_swap_b32 v231, v233
v_mfma_f32_16x16x32_bf16 v[104:107], v[200:203], a[52:55], v[104:107]
ds_read_b64_tr_b16 v[200:201], v243 offset:0
ds_read_b64_tr_b16 v[202:203], v243 offset:1024
v_mfma_f32_16x16x32_bf16 v[72:75], v[204:207], a[104:107], v[72:75]
s_waitcnt lgkmcnt(6)
v_mfma_f32_16x16x32_bf16 v[104:107], v[204:207], a[8:11], v[104:107]
ds_read_b64_tr_b16 v[204:205], v243 offset:8448
ds_read_b64_tr_b16 v[206:207], v243 offset:9472
v_mfma_f32_16x16x32_bf16 v[72:75], v[208:211], a[12:15], v[72:75]
buffer_store_dwordx4 v[230:233], v238, s[32:35], s96 offen offset:0 sc1
v_mfma_f32_16x16x32_bf16 v[104:107], v[208:211], a[64:67], v[104:107]
ds_read_b64_tr_b16 v[208:209], v243 offset:16896
ds_read_b64_tr_b16 v[210:211], v243 offset:17920
v_mfma_f32_16x16x32_bf16 v[72:75], v[212:215], a[68:71], v[72:75]
v_cvt_pk_bf16_f32 v234, v96, v97
v_cvt_pk_bf16_f32 v236, v100, v101
.La5_LW3_3:
s_cbranch_execz .La5_LW3_R0
v_mfma_f32_16x16x32_bf16 v[104:107], v[212:215], a[108:111], v[104:107]
ds_read_b64_tr_b16 v[212:213], v243 offset:25344
ds_read_b64_tr_b16 v[214:215], v243 offset:26368
v_mfma_f32_16x16x32_bf16 v[72:75], v[216:219], a[112:115], v[72:75]
v_cvt_pk_bf16_f32 v235, v98, v99
v_cvt_pk_bf16_f32 v237, v102, v103
v_mfma_f32_16x16x32_bf16 v[104:107], v[216:219], a[116:119], v[104:107]
ds_read_b64_tr_b16 v[216:217], v243 offset:33792
ds_read_b64_tr_b16 v[218:219], v243 offset:34816
v_mfma_f32_16x16x32_bf16 v[72:75], v[220:223], a[120:123], v[72:75]
s_waitcnt lgkmcnt(6)
v_mfma_f32_16x16x32_bf16 v[104:107], v[220:223], a[124:127], v[104:107]
ds_read_b64_tr_b16 v[220:221], v243 offset:42240
ds_read_b64_tr_b16 v[222:223], v243 offset:43264
v_mfma_f32_16x16x32_bf16 v[76:79], v[192:195], a[4:7], v[76:79]
v_permlane16_swap_b32 v234, v236
v_permlane16_swap_b32 v235, v237
v_mfma_f32_16x16x32_bf16 v[108:111], v[192:195], a[36:39], v[108:111]
ds_read_b64_tr_b16 v[192:193], v240 offset:50816
ds_read_b64_tr_b16 v[194:195], v240 offset:51840
v_mfma_f32_16x16x32_bf16 v[76:79], v[196:199], a[40:43], v[76:79]
v_mfma_f32_16x16x32_bf16 v[108:111], v[196:199], a[44:47], v[108:111]
ds_read_b64_tr_b16 v[196:197], v240 offset:59264
ds_read_b64_tr_b16 v[198:199], v240 offset:60288
v_mfma_f32_16x16x32_bf16 v[76:79], v[200:203], a[48:51], v[76:79]
buffer_store_dwordx4 v[234:237], v238, s[32:35], s97 offen offset:0 sc1
v_mfma_f32_16x16x32_bf16 v[108:111], v[200:203], a[52:55], v[108:111]
ds_read_b64_tr_b16 v[200:201], v240 offset:128
ds_read_b64_tr_b16 v[202:203], v240 offset:1152
v_mfma_f32_16x16x32_bf16 v[76:79], v[204:207], a[104:107], v[76:79]
s_waitcnt lgkmcnt(6)
v_mfma_f32_16x16x32_bf16 v[108:111], v[204:207], a[8:11], v[108:111]
ds_read_b64_tr_b16 v[204:205], v240 offset:8576
ds_read_b64_tr_b16 v[206:207], v240 offset:9600
v_mfma_f32_16x16x32_bf16 v[76:79], v[208:211], a[12:15], v[76:79]
buffer_load_dwordx4 v[184:187], v224, s[68:71], 0 offen offset:1536
v_mfma_f32_16x16x32_bf16 v[108:111], v[208:211], a[64:67], v[108:111]
ds_read_b64_tr_b16 v[208:209], v240 offset:17024
ds_read_b64_tr_b16 v[210:211], v240 offset:18048
v_mfma_f32_16x16x32_bf16 v[76:79], v[212:215], a[68:71], v[76:79]
v_mfma_f32_16x16x32_bf16 v[108:111], v[212:215], a[108:111], v[108:111]
ds_read_b64_tr_b16 v[212:213], v240 offset:25472
ds_read_b64_tr_b16 v[214:215], v240 offset:26496
v_mfma_f32_16x16x32_bf16 v[76:79], v[216:219], a[112:115], v[76:79]
buffer_load_dwordx4 v[188:191], v225, s[48:51], 0 offen offset:1536
v_mfma_f32_16x16x32_bf16 v[108:111], v[216:219], a[116:119], v[108:111]
ds_read_b64_tr_b16 v[216:217], v240 offset:33920
ds_read_b64_tr_b16 v[218:219], v240 offset:34944
v_mfma_f32_16x16x32_bf16 v[76:79], v[220:223], a[120:123], v[76:79]
s_waitcnt lgkmcnt(6)
v_mfma_f32_16x16x32_bf16 v[108:111], v[220:223], a[124:127], v[108:111]
ds_read_b64_tr_b16 v[220:221], v240 offset:42368
ds_read_b64_tr_b16 v[222:223], v240 offset:43392
v_mfma_f32_16x16x32_bf16 v[80:83], v[192:195], a[4:7], v[80:83]
v_cvt_pk_bf16_f32 v230, v72, v73
v_cvt_pk_bf16_f32 v232, v76, v77
v_mfma_f32_16x16x32_bf16 v[112:115], v[192:195], a[36:39], v[112:115]
ds_read_b64_tr_b16 v[192:193], v241 offset:50816
ds_read_b64_tr_b16 v[194:195], v241 offset:51840
v_mfma_f32_16x16x32_bf16 v[80:83], v[196:199], a[40:43], v[80:83]
v_cvt_pk_bf16_f32 v231, v74, v75
v_cvt_pk_bf16_f32 v233, v78, v79
v_mfma_f32_16x16x32_bf16 v[112:115], v[196:199], a[44:47], v[112:115]
ds_read_b64_tr_b16 v[196:197], v241 offset:59264
ds_read_b64_tr_b16 v[198:199], v241 offset:60288
v_mfma_f32_16x16x32_bf16 v[80:83], v[200:203], a[48:51], v[80:83]
v_permlane16_swap_b32 v230, v232
v_permlane16_swap_b32 v231, v233
v_mfma_f32_16x16x32_bf16 v[112:115], v[200:203], a[52:55], v[112:115]
ds_read_b64_tr_b16 v[200:201], v241 offset:128
ds_read_b64_tr_b16 v[202:203], v241 offset:1152
v_mfma_f32_16x16x32_bf16 v[80:83], v[204:207], a[104:107], v[80:83]
s_waitcnt lgkmcnt(6)
v_mfma_f32_16x16x32_bf16 v[112:115], v[204:207], a[8:11], v[112:115]
ds_read_b64_tr_b16 v[204:205], v241 offset:8576
ds_read_b64_tr_b16 v[206:207], v241 offset:9600
v_mfma_f32_16x16x32_bf16 v[80:83], v[208:211], a[12:15], v[80:83]
buffer_store_dwordx4 v[230:233], v238, s[32:35], s96 offen offset:1024 sc1
v_mfma_f32_16x16x32_bf16 v[112:115], v[208:211], a[64:67], v[112:115]
ds_read_b64_tr_b16 v[208:209], v241 offset:17024
ds_read_b64_tr_b16 v[210:211], v241 offset:18048
v_mfma_f32_16x16x32_bf16 v[80:83], v[212:215], a[68:71], v[80:83]
v_cvt_pk_bf16_f32 v234, v104, v105
v_cvt_pk_bf16_f32 v236, v108, v109
v_mfma_f32_16x16x32_bf16 v[112:115], v[212:215], a[108:111], v[112:115]
ds_read_b64_tr_b16 v[212:213], v241 offset:25472
ds_read_b64_tr_b16 v[214:215], v241 offset:26496
v_mfma_f32_16x16x32_bf16 v[80:83], v[216:219], a[112:115], v[80:83]
v_cvt_pk_bf16_f32 v235, v106, v107
v_cvt_pk_bf16_f32 v237, v110, v111
v_mfma_f32_16x16x32_bf16 v[112:115], v[216:219], a[116:119], v[112:115]
ds_read_b64_tr_b16 v[216:217], v241 offset:33920
ds_read_b64_tr_b16 v[218:219], v241 offset:34944
v_mfma_f32_16x16x32_bf16 v[80:83], v[220:223], a[120:123], v[80:83]
s_waitcnt lgkmcnt(6)
v_mfma_f32_16x16x32_bf16 v[112:115], v[220:223], a[124:127], v[112:115]
ds_read_b64_tr_b16 v[220:221], v241 offset:42368
ds_read_b64_tr_b16 v[222:223], v241 offset:43392
v_mfma_f32_16x16x32_bf16 v[84:87], v[192:195], a[4:7], v[84:87]
v_permlane16_swap_b32 v234, v236
v_permlane16_swap_b32 v235, v237
v_mfma_f32_16x16x32_bf16 v[116:119], v[192:195], a[36:39], v[116:119]
ds_read_b64_tr_b16 v[192:193], v242 offset:50816
ds_read_b64_tr_b16 v[194:195], v242 offset:51840
v_mfma_f32_16x16x32_bf16 v[84:87], v[196:199], a[40:43], v[84:87]
v_mfma_f32_16x16x32_bf16 v[116:119], v[196:199], a[44:47], v[116:119]
ds_read_b64_tr_b16 v[196:197], v242 offset:59264
ds_read_b64_tr_b16 v[198:199], v242 offset:60288
v_mfma_f32_16x16x32_bf16 v[84:87], v[200:203], a[48:51], v[84:87]
buffer_store_dwordx4 v[234:237], v238, s[32:35], s97 offen offset:1024 sc1
v_mfma_f32_16x16x32_bf16 v[116:119], v[200:203], a[52:55], v[116:119]
ds_read_b64_tr_b16 v[200:201], v242 offset:128
ds_read_b64_tr_b16 v[202:203], v242 offset:1152
v_mfma_f32_16x16x32_bf16 v[84:87], v[204:207], a[104:107], v[84:87]
s_waitcnt lgkmcnt(6)
v_mfma_f32_16x16x32_bf16 v[116:119], v[204:207], a[8:11], v[116:119]
ds_read_b64_tr_b16 v[204:205], v242 offset:8576
ds_read_b64_tr_b16 v[206:207], v242 offset:9600
v_mfma_f32_16x16x32_bf16 v[84:87], v[208:211], a[12:15], v[84:87]
v_mfma_f32_16x16x32_bf16 v[116:119], v[208:211], a[64:67], v[116:119]
ds_read_b64_tr_b16 v[208:209], v242 offset:17024
ds_read_b64_tr_b16 v[210:211], v242 offset:18048
v_mfma_f32_16x16x32_bf16 v[84:87], v[212:215], a[68:71], v[84:87]
v_mfma_f32_16x16x32_bf16 v[116:119], v[212:215], a[108:111], v[116:119]
ds_read_b64_tr_b16 v[212:213], v242 offset:25472
ds_read_b64_tr_b16 v[214:215], v242 offset:26496
v_mfma_f32_16x16x32_bf16 v[84:87], v[216:219], a[112:115], v[84:87]
v_mfma_f32_16x16x32_bf16 v[116:119], v[216:219], a[116:119], v[116:119]
ds_read_b64_tr_b16 v[216:217], v242 offset:33920
ds_read_b64_tr_b16 v[218:219], v242 offset:34944
v_mfma_f32_16x16x32_bf16 v[84:87], v[220:223], a[120:123], v[84:87]
s_waitcnt lgkmcnt(6)
v_mfma_f32_16x16x32_bf16 v[116:119], v[220:223], a[124:127], v[116:119]
ds_read_b64_tr_b16 v[220:221], v242 offset:42368
ds_read_b64_tr_b16 v[222:223], v242 offset:43392
v_mfma_f32_16x16x32_bf16 v[88:91], v[192:195], a[4:7], v[88:91]
v_cvt_pk_bf16_f32 v230, v80, v81
v_cvt_pk_bf16_f32 v232, v84, v85
v_mfma_f32_16x16x32_bf16 v[120:123], v[192:195], a[36:39], v[120:123]
ds_read_b64_tr_b16 v[192:193], v243 offset:50816
ds_read_b64_tr_b16 v[194:195], v243 offset:51840
v_mfma_f32_16x16x32_bf16 v[88:91], v[196:199], a[40:43], v[88:91]
v_cvt_pk_bf16_f32 v231, v82, v83
v_cvt_pk_bf16_f32 v233, v86, v87
v_mfma_f32_16x16x32_bf16 v[120:123], v[196:199], a[44:47], v[120:123]
ds_read_b64_tr_b16 v[196:197], v243 offset:59264
ds_read_b64_tr_b16 v[198:199], v243 offset:60288
v_mfma_f32_16x16x32_bf16 v[88:91], v[200:203], a[48:51], v[88:91]
v_permlane16_swap_b32 v230, v232
v_permlane16_swap_b32 v231, v233
v_mfma_f32_16x16x32_bf16 v[120:123], v[200:203], a[52:55], v[120:123]
ds_read_b64_tr_b16 v[200:201], v243 offset:128
ds_read_b64_tr_b16 v[202:203], v243 offset:1152
v_mfma_f32_16x16x32_bf16 v[88:91], v[204:207], a[104:107], v[88:91]
s_waitcnt lgkmcnt(6)
v_mfma_f32_16x16x32_bf16 v[120:123], v[204:207], a[8:11], v[120:123]
ds_read_b64_tr_b16 v[204:205], v243 offset:8576
ds_read_b64_tr_b16 v[206:207], v243 offset:9600
v_mfma_f32_16x16x32_bf16 v[88:91], v[208:211], a[12:15], v[88:91]
buffer_store_dwordx4 v[230:233], v238, s[32:35], s96 offen offset:2048 sc1
v_mfma_f32_16x16x32_bf16 v[120:123], v[208:211], a[64:67], v[120:123]
ds_read_b64_tr_b16 v[208:209], v243 offset:17024
ds_read_b64_tr_b16 v[210:211], v243 offset:18048
v_mfma_f32_16x16x32_bf16 v[88:91], v[212:215], a[68:71], v[88:91]
v_cvt_pk_bf16_f32 v234, v112, v113
v_cvt_pk_bf16_f32 v236, v116, v117
v_mfma_f32_16x16x32_bf16 v[120:123], v[212:215], a[108:111], v[120:123]
ds_read_b64_tr_b16 v[212:213], v243 offset:25472
ds_read_b64_tr_b16 v[214:215], v243 offset:26496
v_mfma_f32_16x16x32_bf16 v[88:91], v[216:219], a[112:115], v[88:91]
v_cvt_pk_bf16_f32 v235, v114, v115
v_cvt_pk_bf16_f32 v237, v118, v119
v_mfma_f32_16x16x32_bf16 v[120:123], v[216:219], a[116:119], v[120:123]
ds_read_b64_tr_b16 v[216:217], v243 offset:33920
ds_read_b64_tr_b16 v[218:219], v243 offset:34944
v_mfma_f32_16x16x32_bf16 v[88:91], v[220:223], a[120:123], v[88:91]
s_waitcnt lgkmcnt(6)
v_mfma_f32_16x16x32_bf16 v[120:123], v[220:223], a[124:127], v[120:123]
ds_read_b64_tr_b16 v[220:221], v243 offset:42368
ds_read_b64_tr_b16 v[222:223], v243 offset:43392
v_mfma_f32_16x16x32_bf16 v[92:95], v[192:195], a[4:7], v[92:95]
v_permlane16_swap_b32 v234, v236
v_permlane16_swap_b32 v235, v237
v_mfma_f32_16x16x32_bf16 v[124:127], v[192:195], a[36:39], v[124:127]
v_mfma_f32_16x16x32_bf16 v[92:95], v[196:199], a[40:43], v[92:95]
v_mfma_f32_16x16x32_bf16 v[124:127], v[196:199], a[44:47], v[124:127]
v_mfma_f32_16x16x32_bf16 v[92:95], v[200:203], a[48:51], v[92:95]
buffer_store_dwordx4 v[234:237], v238, s[32:35], s97 offen offset:2048 sc1
v_mfma_f32_16x16x32_bf16 v[124:127], v[200:203], a[52:55], v[124:127]
v_mfma_f32_16x16x32_bf16 v[92:95], v[204:207], a[104:107], v[92:95]
s_waitcnt lgkmcnt(0)
v_mfma_f32_16x16x32_bf16 v[124:127], v[204:207], a[8:11], v[124:127]
v_mfma_f32_16x16x32_bf16 v[92:95], v[208:211], a[12:15], v[92:95]
v_mfma_f32_16x16x32_bf16 v[124:127], v[208:211], a[64:67], v[124:127]
v_mfma_f32_16x16x32_bf16 v[92:95], v[212:215], a[68:71], v[92:95]
v_mfma_f32_16x16x32_bf16 v[124:127], v[212:215], a[108:111], v[124:127]
v_mfma_f32_16x16x32_bf16 v[92:95], v[216:219], a[112:115], v[92:95]
v_mfma_f32_16x16x32_bf16 v[124:127], v[216:219], a[116:119], v[124:127]
v_mfma_f32_16x16x32_bf16 v[92:95], v[220:223], a[120:123], v[92:95]
v_mfma_f32_16x16x32_bf16 v[124:127], v[220:223], a[124:127], v[124:127]
s_nop 7
v_cvt_pk_bf16_f32 v230, v88, v89
v_cvt_pk_bf16_f32 v232, v92, v93
v_cvt_pk_bf16_f32 v231, v90, v91
v_cvt_pk_bf16_f32 v233, v94, v95
v_cvt_pk_bf16_f32 v234, v120, v121
v_cvt_pk_bf16_f32 v236, v124, v125
v_cvt_pk_bf16_f32 v235, v122, v123
v_cvt_pk_bf16_f32 v237, v126, v127
v_permlane16_swap_b32 v230, v232
v_permlane16_swap_b32 v231, v233
buffer_store_dwordx4 v[230:233], v238, s[32:35], s96 offen offset:3072 sc1
v_permlane16_swap_b32 v234, v236
v_permlane16_swap_b32 v235, v237
buffer_store_dwordx4 v[234:237], v238, s[32:35], s97 offen offset:3072 sc1
s_lshl_b32 s91, s76, 5
s_add_u32 s91, s91, s72
s_lshl_b32 s20, s91, 9
s_add_u32 s20, s20, 131072
s_lshr_b32 s93, s91, 1
s_lshl_b32 s93, s93, 15
s_and_b32 s94, s91, 1
s_lshl2_add_u32 s94, s94, s3
s_lshl_b32 s94, s94, 7
s_add_u32 s97, s93, s94
s_add_u32 s32, s26, 71303168
s_addc_u32 s33, s27, 0
s_and_b32 s33, s33, 0xffff
s_mov_b32 s34, 16777216
s_mov_b32 s35, 0x20000
s_add_u32 s44, s26, 171966464
s_addc_u32 s45, s27, 0
s_and_b32 s45, s45, 0xffff
s_mov_b32 s46, 950272
s_mov_b32 s47, 0x20000
s_add_u32 s56, s38, 0
s_addc_u32 s57, s39, 0
s_and_b32 s57, s57, 0xffff
s_mov_b32 s58, 4194304
s_mov_b32 s59, 0x20000
s_add_u32 s68, s16, 0
s_addc_u32 s69, s17, 0
s_and_b32 s69, s69, 0xffff
s_mov_b32 s70, 4194304
s_mov_b32 s71, 0x20000
v_and_b32 v230, 0x70, v255
v_lshrrev_b32 v231, 7, v255
v_lshl_add_u32 v230, v231, 10, v230
v_add_u32 v240, s97, v230
v_add_u32 v241, 8192, v240
v_add_u32 v242, 16384, v240
v_add_u32 v243, 24576, v240
v_mov_b32 v237, 0x3f803f80
v_mov_b32 v236, 0
s_waitcnt vmcnt(6)
v_dot2_f32_bf16 v236, v140, v237, v236
v_dot2_f32_bf16 v236, v141, v237, v236
v_dot2_f32_bf16 v236, v142, v237, v236
v_dot2_f32_bf16 v236, v143, v237, v236
v_dot2_f32_bf16 v236, v156, v237, v236
v_dot2_f32_bf16 v236, v157, v237, v236
v_dot2_f32_bf16 v236, v158, v237, v236
v_dot2_f32_bf16 v236, v159, v237, v236
v_dot2_f32_bf16 v236, v172, v237, v236
v_dot2_f32_bf16 v236, v173, v237, v236
v_dot2_f32_bf16 v236, v174, v237, v236
v_dot2_f32_bf16 v236, v175, v237, v236
v_dot2_f32_bf16 v236, v188, v237, v236
v_dot2_f32_bf16 v236, v189, v237, v236
v_dot2_f32_bf16 v236, v190, v237, v236
v_dot2_f32_bf16 v236, v191, v237, v236
v_lshlrev_b32 v0, 16, v136
v_and_b32 v1, 0xffff0000, v136
v_lshlrev_b32 v2, 16, v137
v_and_b32 v3, 0xffff0000, v137
v_lshlrev_b32 v4, 16, v138
v_and_b32 v5, 0xffff0000, v138
v_lshlrev_b32 v6, 16, v139
v_and_b32 v7, 0xffff0000, v139
v_lshlrev_b32 v8, 16, v152
v_and_b32 v9, 0xffff0000, v152
v_lshlrev_b32 v10, 16, v153
v_and_b32 v11, 0xffff0000, v153
v_lshlrev_b32 v12, 16, v154
v_and_b32 v13, 0xffff0000, v154
v_lshlrev_b32 v14, 16, v155
v_and_b32 v15, 0xffff0000, v155
v_lshlrev_b32 v16, 16, v168
v_and_b32 v17, 0xffff0000, v168
v_lshlrev_b32 v18, 16, v169
v_and_b32 v19, 0xffff0000, v169
v_lshlrev_b32 v20, 16, v170
v_and_b32 v21, 0xffff0000, v170
v_lshlrev_b32 v22, 16, v171
v_and_b32 v23, 0xffff0000, v171
v_lshlrev_b32 v24, 16, v184
v_and_b32 v25, 0xffff0000, v184
v_lshlrev_b32 v26, 16, v185
v_and_b32 v27, 0xffff0000, v185
v_lshlrev_b32 v28, 16, v186
v_and_b32 v29, 0xffff0000, v186
v_lshlrev_b32 v30, 16, v187
v_and_b32 v31, 0xffff0000, v187
s_nop 1
v_add_f32_dpp v236, v236, v236 quad_perm:[1,0,3,2] row_mask:0xf bank_mask:0xf
s_nop 1
v_add_f32_dpp v236, v236, v236 quad_perm:[2,3,0,1] row_mask:0xf bank_mask:0xf
s_nop 1
v_add_f32_dpp v236, v236, v236 row_ror:8 row_mask:0xf bank_mask:0xf
v_mov_b32 v230, v236
s_nop 1
v_permlane16_swap_b32 v230, v236
s_nop 1
v_add_f32 v236, v236, v230
v_mov_b32 v230, v236
s_nop 1
v_permlane32_swap_b32 v230, v236
s_nop 1
v_add_f32 v236, v236, v230
s_mov_b64 exec, 0
s_cmp_eq_u32 s3, 0
s_cbranch_scc1 .La5_LW4_0
s_cmp_eq_u32 s3, 1
s_cbranch_scc1 .La5_LW4_1
s_cmp_eq_u32 s3, 2
s_cbranch_scc1 .La5_LW4_2
s_branch .La5_LW4_3
.La5_LW4_R0:
s_mov_b64 exec, -1
s_waitcnt vmcnt(0)
buffer_store_dwordx2 v[252:253], v250, s[64:67], s92 offen sc1
s_mov_b32 s85, 99
s_cmp_eq_u32 s30, 0
s_nop 0
s_nop 0
s_cbranch_scc1 .La5_LT_ROWS
buffer_load_dwordx2 v[250:251], v248, s[64:67], s20 offen sc1
s_mov_b32 s93, 0
buffer_load_dwordx4 a[64:67], v229, s[28:31], s93 offen offset:0
s_mov_b32 s93, 2048
buffer_load_dwordx4 a[68:71], v229, s[28:31], s93 offen offset:0
s_mov_b32 s93, 4096
buffer_load_dwordx4 a[72:75], v229, s[28:31], s93 offen offset:0
s_mov_b32 s93, 6144
buffer_load_dwordx4 a[76:79], v229, s[28:31], s93 offen offset:0
s_mov_b32 s93, 8192
buffer_load_dwordx4 a[80:83], v229, s[28:31], s93 offen offset:0
s_mov_b32 s93, 10240
buffer_load_dwordx4 a[84:87], v229, s[28:31], s93 offen offset:0
s_mov_b32 s93, 12288
buffer_load_dwordx4 a[88:91], v229, s[28:31], s93 offen offset:0
s_mov_b32 s93, 14336
buffer_load_dwordx4 a[92:95], v229, s[28:31], s93 offen offset:0
s_mov_b32 s85, 1
s_waitcnt vmcnt(8)
v_cmp_eq_u64 vcc, v[250:251], v[252:253]
s_nop 1
s_cmp_eq_u64 vcc, -1
s_nop 0
s_cbranch_scc1 .La5_LT_ROWS
buffer_load_dwordx2 v[250:251], v248, s[64:67], s20 offen sc1
s_mov_b32 s93, 0
buffer_load_dwordx4 a[96:99], v229, s[28:31], s93 offen offset:256
s_mov_b32 s93, 2048
buffer_load_dwordx4 a[100:103], v229, s[28:31], s93 offen offset:256
s_mov_b32 s93, 4096
buffer_load_dwordx4 a[104:107], v229, s[28:31], s93 offen offset:256
s_mov_b32 s93, 6144
buffer_load_dwordx4 a[108:111], v229, s[28:31], s93 offen offset:256
s_mov_b32 s93, 8192
buffer_load_dwordx4 a[112:115], v229, s[28:31], s93 offen offset:256
s_mov_b32 s93, 10240
buffer_load_dwordx4 a[116:119], v229, s[28:31], s93 offen offset:256
s_mov_b32 s93, 12288
buffer_load_dwordx4 a[120:123], v229, s[28:31], s93 offen offset:256
s_mov_b32 s93, 14336
buffer_load_dwordx4 a[124:127], v229, s[28:31], s93 offen offset:256
s_mov_b32 s85, 2
s_waitcnt vmcnt(8)
v_cmp_eq_u64 vcc, v[250:251], v[252:253]
s_nop 1
s_cmp_eq_u64 vcc, -1
s_nop 0
s_cbranch_scc1 .La5_LT_ROWS
; ---- h = x + attn, LayerNorm statistics, router score, two best of eight rows
.La5_LT_ROWS:
.La5_LW3_4:
s_cbranch_execz .La5_LW3_R0
s_mov_b32 s96, 0
.La5_LT_P3:
buffer_load_dwordx2 v[250:251], v248, s[64:67], s20 offen sc1
s_waitcnt vmcnt(0)
v_cmp_eq_u64 vcc, v[250:251], v[252:253]
s_add_u32 s96, s96, 1
s_lshr_b32 s92, vcc_lo, 1
s_and_b32 s92, s92, vcc_lo
s_lshr_b32 s93, s92, 2
s_and_b32 s92, s92, s93
s_lshr_b32 s93, s92, 4
s_and_b32 s92, s92, s93
s_lshr_b32 s93, s96, 20
s_mul_i32 s93, s93, 0x01010101
s_or_b32 s92, s92, s93
s_andn2_b32 s92, 0x01010101, s92
s_bcnt1_i32_b32 s93, s92
s_cmp_gt_u32 s93, 1
s_nop 0
s_nop 0
s_cbranch_scc1 .La5_LT_P3
s_or_b32 s93, s92, 0x01000000
s_ff1_i32_b32 s93, s93
s_lshl_b32 s80, s93, 19
s_xor_b32 s93, s80, 8388608
s_xor_b32 s94, s80, 12582912
s_xor_b32 s95, s80, 4194304
buffer_load_dwordx4 v[208:211], v240, s[32:35], s93 offen sc1
buffer_load_dwordx4 v[212:215], v241, s[32:35], s93 offen sc1
buffer_load_dwordx4 v[216:219], v242, s[32:35], s93 offen sc1
buffer_load_dwordx4 v[220:223], v243, s[32:35], s93 offen sc1
buffer_load_dwordx4 v[112:115], v240, s[32:35], s94 offen sc1
buffer_load_dwordx4 v[116:119], v241, s[32:35], s94 offen sc1
buffer_load_dwordx4 v[120:123], v242, s[32:35], s94 offen sc1
buffer_load_dwordx4 v[124:127], v243, s[32:35], s94 offen sc1
buffer_load_dwordx4 v[96:99], v240, s[32:35], s95 offen sc1
buffer_load_dwordx4 v[100:103], v241, s[32:35], s95 offen sc1
buffer_load_dwordx4 v[104:107], v242, s[32:35], s95 offen sc1
buffer_load_dwordx4 v[108:111], v243, s[32:35], s95 offen sc1
s_cmp_lg_u32 s92, 0
s_cbranch_scc1 .La5_LT_P4
buffer_load_dwordx4 v[192:195], v240, s[32:35], s80 offen sc1
buffer_load_dwordx4 v[196:199], v241, s[32:35], s80 offen sc1
buffer_load_dwordx4 v[200:203], v242, s[32:35], s80 offen sc1
buffer_load_dwordx4 v[204:207], v243, s[32:35], s80 offen sc1
.La5_LT_P4:
buffer_load_dwordx2 v[250:251], v248, s[64:67], s20 offen sc1
s_waitcnt vmcnt(8)
v_lshlrev_b32 v32, 16, v208
v_and_b32 v33, 0xffff0000, v208
v_lshlrev_b32 v230, 16, v112
v_and_b32 v231, 0xffff0000, v112
v_pk_add_f32 v[32:33], v[32:33], v[230:231]
v_lshlrev_b32 v34, 16, v209
v_and_b32 v35, 0xffff0000, v209
v_lshlrev_b32 v230, 16, v113
v_and_b32 v231, 0xffff0000, v113
v_pk_add_f32 v[34:35], v[34:35], v[230:231]
v_lshlrev_b32 v36, 16, v210
v_and_b32 v37, 0xffff0000, v210
v_lshlrev_b32 v230, 16, v114
v_and_b32 v231, 0xffff0000, v114
v_pk_add_f32 v[36:37], v[36:37], v[230:231]
v_lshlrev_b32 v38, 16, v211
v_and_b32 v39, 0xffff0000, v211
v_lshlrev_b32 v230, 16, v115
v_and_b32 v231, 0xffff0000, v115
v_pk_add_f32 v[38:39], v[38:39], v[230:231]
s_waitcnt vmcnt(7)
v_lshlrev_b32 v40, 16, v212
v_and_b32 v41, 0xffff0000, v212
v_lshlrev_b32 v230, 16, v116
v_and_b32 v231, 0xffff0000, v116
v_pk_add_f32 v[40:41], v[40:41], v[230:231]
v_lshlrev_b32 v42, 16, v213
v_and_b32 v43, 0xffff0000, v213
v_lshlrev_b32 v230, 16, v117
v_and_b32 v231, 0xffff0000, v117
v_pk_add_f32 v[42:43], v[42:43], v[230:231]
v_lshlrev_b32 v44, 16, v214
v_and_b32 v45, 0xffff0000, v214
v_lshlrev_b32 v230, 16, v118
v_and_b32 v231, 0xffff0000, v118
v_pk_add_f32 v[44:45], v[44:45], v[230:231]
v_lshlrev_b32 v46, 16, v215
v_and_b32 v47, 0xffff0000, v215
v_lshlrev_b32 v230, 16, v119
v_and_b32 v231, 0xffff0000, v119
v_pk_add_f32 v[46:47], v[46:47], v[230:231]
s_waitcnt vmcnt(6)
v_lshlrev_b32 v48, 16, v216
v_and_b32 v49, 0xffff0000, v216
v_lshlrev_b32 v230, 16, v120
v_and_b32 v231, 0xffff0000, v120
v_pk_add_f32 v[48:49], v[48:49], v[230:231]
v_lshlrev_b32 v50, 16, v217
v_and_b32 v51, 0xffff0000, v217
v_lshlrev_b32 v230, 16, v121
v_and_b32 v231, 0xffff0000, v121
v_pk_add_f32 v[50:51], v[50:51], v[230:231]
v_lshlrev_b32 v52, 16, v218
v_and_b32 v53, 0xffff0000, v218
v_lshlrev_b32 v230, 16, v122
v_and_b32 v231, 0xffff0000, v122
v_pk_add_f32 v[52:53], v[52:53], v[230:231]
v_lshlrev_b32 v54, 16, v219
v_and_b32 v55, 0xffff0000, v219
v_lshlrev_b32 v230, 16, v123
v_and_b32 v231, 0xffff0000, v123
v_pk_add_f32 v[54:55], v[54:55], v[230:231]
s_waitcnt vmcnt(5)
v_lshlrev_b32 v56, 16, v220
v_and_b32 v57, 0xffff0000, v220
v_lshlrev_b32 v230, 16, v124
v_and_b32 v231, 0xffff0000, v124
v_pk_add_f32 v[56:57], v[56:57], v[230:231]
v_lshlrev_b32 v58, 16, v221
v_and_b32 v59, 0xffff0000, v221
v_lshlrev_b32 v230, 16, v125
v_and_b32 v231, 0xffff0000, v125
v_pk_add_f32 v[58:59], v[58:59], v[230:231]
v_lshlrev_b32 v60, 16, v222
v_and_b32 v61, 0xffff0000, v222
v_lshlrev_b32 v230, 16, v126
v_and_b32 v231, 0xffff0000, v126
v_pk_add_f32 v[60:61], v[60:61], v[230:231]
v_lshlrev_b32 v62, 16, v223
v_and_b32 v63, 0xffff0000, v223
v_lshlrev_b32 v230, 16, v127
v_and_b32 v231, 0xffff0000, v127
v_pk_add_f32 v[62:63], v[62:63], v[230:231]
s_waitcnt vmcnt(1)
v_lshlrev_b32 v64, 16, v96
v_and_b32 v65, 0xffff0000, v96
v_lshlrev_b32 v66, 16, v97
v_and_b32 v67, 0xffff0000, v97
v_lshlrev_b32 v68, 16, v98
v_and_b32 v69, 0xffff0000, v98
v_lshlrev_b32 v70, 16, v99
v_and_b32 v71, 0xffff0000, v99
v_lshlrev_b32 v72, 16, v100
v_and_b32 v73, 0xffff0000, v100
v_lshlrev_b32 v74, 16, v101
v_and_b32 v75, 0xffff0000, v101
v_lshlrev_b32 v76, 16, v102
v_and_b32 v77, 0xffff0000, v102
v_lshlrev_b32 v78, 16, v103
v_and_b32 v79, 0xffff0000, v103
v_lshlrev_b32 v80, 16, v104
v_and_b32 v81, 0xffff0000, v104
v_lshlrev_b32 v82, 16, v105
v_and_b32 v83, 0xffff0000, v105
v_lshlrev_b32 v84, 16, v106
v_and_b32 v85, 0xffff0000, v106
v_lshlrev_b32 v86, 16, v107
v_and_b32 v87, 0xffff0000, v107
v_lshlrev_b32 v88, 16, v108
v_and_b32 v89, 0xffff0000, v108
v_lshlrev_b32 v90, 16, v109
v_and_b32 v91, 0xffff0000, v109
v_lshlrev_b32 v92, 16, v110
v_and_b32 v93, 0xffff0000, v110
v_lshlrev_b32 v94, 16, v111
v_and_b32 v95, 0xffff0000, v111
v_mov_b32 v232, 0
v_mov_b32 v233, 0
v_mov_b32 v234, 0
s_cmp_eq_u32 s92, 0
s_cbranch_scc1 .La5_LT_SUM
.La5_LT_P5:
s_waitcnt vmcnt(0)
v_cmp_eq_u64 vcc, v[250:251], v[252:253]
s_add_u32 s96, s96, 1
s_lshr_b32 s92, vcc_lo, 1
s_and_b32 s92, s92, vcc_lo
s_lshr_b32 s93, s92, 2
s_and_b32 s92, s92, s93
s_lshr_b32 s93, s92, 4
s_and_b32 s92, s92, s93
s_lshr_b32 s93, s96, 20
s_mul_i32 s93, s93, 0x01010101
s_or_b32 s92, s92, s93
s_andn2_b32 s92, 0x01010101, s92
s_cbranch_scc0 .La5_LT_P6
buffer_load_dwordx2 v[250:251], v248, s[64:67], s20 offen sc1
s_branch .La5_LT_P5
.La5_LT_P6:
buffer_load_dwordx4 v[192:195], v240, s[32:35], s80 offen sc1
buffer_load_dwordx4 v[196:199], v241, s[32:35], s80 offen sc1
buffer_load_dwordx4 v[200:203], v242, s[32:35], s80 offen sc1
buffer_load_dwordx4 v[204:207], v243, s[32:35], s80 offen sc1
.La5_LT_SUM:
s_waitcnt vmcnt(3)
v_lshlrev_b32 v208, 16, v192
v_and_b32 v209, 0xffff0000, v192
v_lshlrev_b32 v210, 16, v193
v_and_b32 v211, 0xffff0000, v193
v_lshlrev_b32 v212, 16, v194
v_and_b32 v213, 0xffff0000, v194
v_lshlrev_b32 v214, 16, v195
v_and_b32 v215, 0xffff0000, v195
v_pk_add_f32 v[208:209], v[208:209], v[64:65]
v_pk_add_f32 v[210:211], v[210:211], v[66:67]
v_pk_add_f32 v[212:213], v[212:213], v[68:69]
v_pk_add_f32 v[214:215], v[214:215], v[70:71]
v_pk_add_f32 v[208:209], v[208:209], v[32:33]
v_pk_add_f32 v[210:211], v[210:211], v[34:35]
v_pk_add_f32 v[212:213], v[212:213], v[36:37]
v_pk_add_f32 v[214:215], v[214:215], v[38:39]
v_pk_add_f32 v[208:209], v[208:209], v[0:1]
v_pk_add_f32 v[210:211], v[210:211], v[2:3]
v_pk_add_f32 v[212:213], v[212:213], v[4:5]
v_pk_add_f32 v[214:215], v[214:215], v[6:7]
v_cvt_pk_bf16_f32 v96, v208, v209
v_cvt_pk_bf16_f32 v97, v210, v211
v_cvt_pk_bf16_f32 v98, v212, v213
v_cvt_pk_bf16_f32 v99, v214, v215
v_dot2_f32_bf16 v232, v96, v237, v232
v_dot2_f32_bf16 v233, v96, v96, v233
v_dot2_f32_bf16 v234, v96, v140, v234
v_dot2_f32_bf16 v232, v97, v237, v232
v_dot2_f32_bf16 v233, v97, v97, v233
v_dot2_f32_bf16 v234, v97, v141, v234
v_dot2_f32_bf16 v232, v98, v237, v232
v_dot2_f32_bf16 v233, v98, v98, v233
v_dot2_f32_bf16 v234, v98, v142, v234
v_dot2_f32_bf16 v232, v99, v237, v232
v_dot2_f32_bf16 v233, v99, v99, v233
v_dot2_f32_bf16 v234, v99, v143, v234
s_waitcnt vmcnt(2)
v_lshlrev_b32 v208, 16, v196
v_and_b32 v209, 0xffff0000, v196
v_lshlrev_b32 v210, 16, v197
v_and_b32 v211, 0xffff0000, v197
.La5_LW4_0:
s_cbranch_execz .La5_LW4_4
v_lshlrev_b32 v212, 16, v198
v_and_b32 v213, 0xffff0000, v198
v_lshlrev_b32 v214, 16, v199
v_and_b32 v215, 0xffff0000, v199
v_pk_add_f32 v[208:209], v[208:209], v[72:73]
v_pk_add_f32 v[210:211], v[210:211], v[74:75]
v_pk_add_f32 v[212:213], v[212:213], v[76:77]
v_pk_add_f32 v[214:215], v[214:215], v[78:79]
v_pk_add_f32 v[208:209], v[208:209], v[40:41]
v_pk_add_f32 v[210:211], v[210:211], v[42:43]
v_pk_add_f32 v[212:213], v[212:213], v[44:45]
v_pk_add_f32 v[214:215], v[214:215], v[46:47]
v_pk_add_f32 v[208:209], v[208:209], v[8:9]
v_pk_add_f32 v[210:211], v[210:211], v[10:11]
v_pk_add_f32 v[212:213], v[212:213], v[12:13]
v_pk_add_f32 v[214:215], v[214:215], v[14:15]
v_cvt_pk_bf16_f32 v100, v208, v209
v_cvt_pk_bf16_f32 v101, v210, v211
v_cvt_pk_bf16_f32 v102, v212, v213
v_cvt_pk_bf16_f32 v103, v214, v215
v_dot2_f32_bf16 v232, v100, v237, v232
v_dot2_f32_bf16 v233, v100, v100, v233
v_dot2_f32_bf16 v234, v100, v156, v234
v_dot2_f32_bf16 v232, v101, v237, v232
v_dot2_f32_bf16 v233, v101, v101, v233
v_dot2_f32_bf16 v234, v101, v157, v234
v_dot2_f32_bf16 v232, v102, v237, v232
v_dot2_f32_bf16 v233, v102, v102, v233
v_dot2_f32_bf16 v234, v102, v158, v234
v_dot2_f32_bf16 v232, v103, v237, v232
v_dot2_f32_bf16 v233, v103, v103, v233
v_dot2_f32_bf16 v234, v103, v159, v234
s_waitcnt vmcnt(1)
v_lshlrev_b32 v208, 16, v200
v_and_b32 v209, 0xffff0000, v200
v_lshlrev_b32 v210, 16, v201
v_and_b32 v211, 0xffff0000, v201
v_lshlrev_b32 v212, 16, v202
v_and_b32 v213, 0xffff0000, v202
v_lshlrev_b32 v214, 16, v203
v_and_b32 v215, 0xffff0000, v203
v_pk_add_f32 v[208:209], v[208:209], v[80:81]
v_pk_add_f32 v[210:211], v[210:211], v[82:83]
v_pk_add_f32 v[212:213], v[212:213], v[84:85]
v_pk_add_f32 v[214:215], v[214:215], v[86:87]
v_pk_add_f32 v[208:209], v[208:209], v[48:49]
v_pk_add_f32 v[210:211], v[210:211], v[50:51]
v_pk_add_f32 v[212:213], v[212:213], v[52:53]
v_pk_add_f32 v[214:215], v[214:215], v[54:55]
v_pk_add_f32 v[208:209], v[208:209], v[16:17]
v_pk_add_f32 v[210:211], v[210:211], v[18:19]
v_pk_add_f32 v[212:213], v[212:213], v[20:21]
v_pk_add_f32 v[214:215], v[214:215], v[22:23]
v_cvt_pk_bf16_f32 v104, v208, v209
v_cvt_pk_bf16_f32 v105, v210, v211
v_cvt_pk_bf16_f32 v106, v212, v213
v_cvt_pk_bf16_f32 v107, v214, v215
v_dot2_f32_bf16 v232, v104, v237, v232
v_dot2_f32_bf16 v233, v104, v104, v233
v_dot2_f32_bf16 v234, v104, v172, v234
v_dot2_f32_bf16 v232, v105, v237, v232
v_dot2_f32_bf16 v233, v105, v105, v233
v_dot2_f32_bf16 v234, v105, v173, v234
v_dot2_f32_bf16 v232, v106, v237, v232
v_dot2_f32_bf16 v233, v106, v106, v233
v_dot2_f32_bf16 v234, v106, v174, v234
v_dot2_f32_bf16 v232, v107, v237, v232
v_dot2_f32_bf16 v233, v107, v107, v233
v_dot2_f32_bf16 v234, v107, v175, v234
s_waitcnt vmcnt(0)
v_lshlrev_b32 v208, 16, v204
v_and_b32 v209, 0xffff0000, v204
v_lshlrev_b32 v210, 16, v205
v_and_b32 v211, 0xffff0000, v205
v_lshlrev_b32 v212, 16, v206
v_and_b32 v213, 0xffff0000, v206
v_lshlrev_b32 v214, 16, v207
v_and_b32 v215, 0xffff0000, v207
v_pk_add_f32 v[208:209], v[208:209], v[88:89]
v_pk_add_f32 v[210:211], v[210:211], v[90:91]
v_pk_add_f32 v[212:213], v[212:213], v[92:93]
v_pk_add_f32 v[214:215], v[214:215], v[94:95]
v_pk_add_f32 v[208:209], v[208:209], v[56:57]
v_pk_add_f32 v[210:211], v[210:211], v[58:59]
v_pk_add_f32 v[212:213], v[212:213], v[60:61]
v_pk_add_f32 v[214:215], v[214:215], v[62:63]
v_pk_add_f32 v[208:209], v[208:209], v[24:25]
v_pk_add_f32 v[210:211], v[210:211], v[26:27]
v_pk_add_f32 v[212:213], v[212:213], v[28:29]
v_pk_add_f32 v[214:215], v[214:215], v[30:31]
v_cvt_pk_bf16_f32 v108, v208, v209
v_cvt_pk_bf16_f32 v109, v210, v211
v_cvt_pk_bf16_f32 v110, v212, v213
v_cvt_pk_bf16_f32 v111, v214, v215
v_dot2_f32_bf16 v232, v108, v237, v232
v_dot2_f32_bf16 v233, v108, v108, v233
v_dot2_f32_bf16 v234, v108, v188, v234
v_dot2_f32_bf16 v232, v109, v237, v232
v_dot2_f32_bf16 v233, v109, v109, v233
v_dot2_f32_bf16 v234, v109, v189, v234
v_dot2_f32_bf16 v232, v110, v237, v232
v_dot2_f32_bf16 v233, v110, v110, v233
v_dot2_f32_bf16 v234, v110, v190, v234
v_dot2_f32_bf16 v232, v111, v237, v232
v_dot2_f32_bf16 v233, v111, v111, v233
v_dot2_f32_bf16 v234, v111, v191, v234
s_waitcnt vmcnt(0)
s_lshl2_add_u32 s92, s91, s3
s_lshl_b32 s92, s92, 1
s_sub_u32 s92, 0xffff, s92
v_add_f32_dpp v232, v232, v232 quad_perm:[1,0,3,2] row_mask:0xf bank_mask:0xf
v_add_f32_dpp v233, v233, v233 quad_perm:[1,0,3,2] row_mask:0xf bank_mask:0xf
v_add_f32_dpp v234, v234, v234 quad_perm:[1,0,3,2] row_mask:0xf bank_mask:0xf
v_add_f32_dpp v232, v232, v232 quad_perm:[2,3,0,1] row_mask:0xf bank_mask:0xf
v_add_f32_dpp v233, v233, v233 quad_perm:[2,3,0,1] row_mask:0xf bank_mask:0xf
v_add_f32_dpp v234, v234, v234 quad_perm:[2,3,0,1] row_mask:0xf bank_mask:0xf
v_add_f32_dpp v232, v232, v232 row_ror:8 row_mask:0xf bank_mask:0xf
v_add_f32_dpp v233, v233, v233 row_ror:8 row_mask:0xf bank_mask:0xf
v_add_f32_dpp v234, v234, v234 row_ror:8 row_mask:0xf bank_mask:0xf
v_mov_b32 v230, v232
v_mov_b32 v231, v233
v_mov_b32 v235, v234
v_permlane16_swap_b32 v230, v232
v_permlane16_swap_b32 v231, v233
v_permlane16_swap_b32 v235, v234
v_add_f32 v232, v232, v230
v_add_f32 v233, v233, v231
v_add_f32 v234, v234, v235
v_mov_b32 v230, v232
v_mov_b32 v231, v233
v_mov_b32 v235, v234
v_permlane32_swap_b32 v230, v232
v_permlane32_swap_b32 v231, v233
v_permlane32_swap_b32 v235, v234
v_add_f32 v232, v232, v230
v_add_f32 v233, v233, v231
v_add_f32_e64 v234, v234, v235
v_mul_f32 v244, 0x3a800000, v232
v_mul_f32 v233, 0x3a800000, v233
v_fma_f32 v230, -v244, v244, v233
v_mul_f32 v233, 0x3a800000, v233
v_cmp_nge_f32 vcc, v230, v233
v_bfe_u32 v235, v255, 6, 1
v_sub_u32_e64 v235, s92, v235
s_cbranch_vccnz .La5_LT_VAR
.La5_LT_VOK:
v_cvt_pk_bf16_f32 v244, v244, v244
v_lshlrev_b32 v244, 16, v244
v_cvt_pk_bf16_f32 v230, v230, v230
v_lshlrev_b32 v230, 16, v230
v_fma_f32 v234, -v244, v236, v234
v_add_f32 v230, 0x3727c5ac, v230
v_cvt_pk_bf16_f32 v230, v230, v230
v_lshlrev_b32 v230, 16, v230
v_sqrt_f32 v230, v230
v_lshrrev_b32 v232, 3, v255
v_cvt_pk_bf16_f32 v230, v230, v230
v_lshlrev_b32 v231, 16, v230
v_rcp_f32 v246, v231
v_lshrrev_b32 v230, 16, v230
v_or_b32 v233, v244, v230
v_mul_f32 v234, v234, v246
s_lshl_b32 s93, s3, 4
v_cvt_pk_bf16_f32 v234, v234, v234
v_and_b32 v230, 8, v232
v_ashrrev_i32 v232, 31, v234
v_or_b32 v232, 0x80000000, v232
v_xor_b32 v232, v232, v234
v_and_b32 v232, 0xffff0000, v232
v_add_u32 v230, s93, v230
v_or_b32 v232, v232, v235
ds_write_b64 v230, v[232:233] offset:0
v_lshrrev_b32 v230, 1, v255
v_and_b32 v230, 56, v230
s_waitcnt lgkmcnt(0)
s_barrier
s_cmp_lg_u32 s3, 0
s_cbranch_scc1 .La5_LT_OUT
ds_read_b64 v[232:233], v230 offset:0
s_lshl_b32 s92, s91, 4
v_lshrrev_b32 v235, 7, v255
v_lshlrev_b32 v235, 12, v235
s_waitcnt lgkmcnt(0)
v_mov_b32 v234, v232
s_nop 1
v_max_u32_dpp v234, v234, v234 quad_perm:[1,0,3,2] row_mask:0xf bank_mask:0xf
s_nop 1
v_max_u32_dpp v234, v234, v234 quad_perm:[2,3,0,1] row_mask:0xf bank_mask:0xf
s_nop 1
v_max_u32_dpp v234, v234, v234 row_half_mirror row_mask:0xf bank_mask:0xf
s_nop 1
v_cmp_eq_u32 vcc, v232, v234
v_add_u32 v231, 8, v235
s_nop 0
v_cndmask_b32 v234, v232, 0, vcc
v_cndmask_b32 v235, v231, v235, vcc
s_nop 0
v_max_u32_dpp v234, v234, v234 quad_perm:[1,0,3,2] row_mask:0xf bank_mask:0xf
s_nop 1
v_max_u32_dpp v234, v234, v234 quad_perm:[2,3,0,1] row_mask:0xf bank_mask:0xf
s_nop 1
v_max_u32_dpp v234, v234, v234 row_half_mirror row_mask:0xf bank_mask:0xf
s_nop 1
v_cmp_ge_u32 vcc, v232, v234
s_nop 1
s_mov_b64 exec, vcc
buffer_store_dwordx2 v[232:233], v235, s[44:47], s92 offen sc1
s_mov_b64 exec, -1
; ---- out = h + .La5_LN(h) of the own rows; FFN weights into MFMA order
.La5_LT_OUT:
s_cmp_gt_u32 s85, 0
s_cbranch_scc1 .La5_LT_R0
s_mov_b32 s93, 0
buffer_load_dwordx4 a[64:67], v229, s[28:31], s93 offen offset:0
s_mov_b32 s93, 2048
buffer_load_dwordx4 a[68:71], v229, s[28:31], s93 offen offset:0
s_mov_b32 s93, 4096
buffer_load_dwordx4 a[72:75], v229, s[28:31], s93 offen offset:0
s_mov_b32 s93, 6144
buffer_load_dwordx4 a[76:79], v229, s[28:31], s93 offen offset:0
s_mov_b32 s93, 8192
buffer_load_dwordx4 a[80:83], v229, s[28:31], s93 offen offset:0
s_mov_b32 s93, 10240
buffer_load_dwordx4 a[84:87], v229, s[28:31], s93 offen offset:0
s_mov_b32 s93, 12288
buffer_load_dwordx4 a[88:91], v229, s[28:31], s93 offen offset:0
s_mov_b32 s93, 14336
buffer_load_dwordx4 a[92:95], v229, s[28:31], s93 offen offset:0
.La5_LT_R0:
s_cmp_gt_u32 s85, 1
s_cbranch_scc1 .La5_LT_R1
s_mov_b32 s93, 0
buffer_load_dwordx4 a[96:99], v229, s[28:31], s93 offen offset:256
s_mov_b32 s93, 2048
buffer_load_dwordx4 a[100:103], v229, s[28:31], s93 offen offset:256
s_mov_b32 s93, 4096
buffer_load_dwordx4 a[104:107], v229, s[28:31], s93 offen offset:256
s_mov_b32 s93, 6144
buffer_load_dwordx4 a[108:111], v229, s[28:31], s93 offen offset:256
s_mov_b32 s93, 8192
buffer_load_dwordx4 a[112:115], v229, s[28:31], s93 offen offset:256
s_mov_b32 s93, 10240
buffer_load_dwordx4 a[116:119], v229, s[28:31], s93 offen offset:256
s_mov_b32 s93, 12288
buffer_load_dwordx4 a[120:123], v229, s[28:31], s93 offen offset:256
s_mov_b32 s93, 14336
buffer_load_dwordx4 a[124:127], v229, s[28:31], s93 offen offset:256
.La5_LT_R1:
v_mul_f32 v244, v244, v246
v_mov_b32 v247, v246
v_xor_b32 v244, 0x80000000, v244
v_mov_b32 v245, v244
v_lshlrev_b32 v230, 16, v96
v_and_b32 v231, 0xffff0000, v96
v_pk_fma_f32 v[232:233], v[230:231], v[246:247], v[244:245]
v_cvt_pk_bf16_f32 v232, v232, v233
v_lshlrev_b32 v234, 16, v232
v_and_b32 v235, 0xffff0000, v232
v_pk_add_f32 v[230:231], v[230:231], v[234:235]
v_cvt_pk_bf16_f32 v192, v230, v231
v_lshlrev_b32 v230, 16, v97
v_and_b32 v231, 0xffff0000, v97
v_pk_fma_f32 v[232:233], v[230:231], v[246:247], v[244:245]
v_cvt_pk_bf16_f32 v232, v232, v233
v_lshlrev_b32 v234, 16, v232
v_and_b32 v235, 0xffff0000, v232
v_pk_add_f32 v[230:231], v[230:231], v[234:235]
v_cvt_pk_bf16_f32 v193, v230, v231
v_lshlrev_b32 v230, 16, v98
v_and_b32 v231, 0xffff0000, v98
v_pk_fma_f32 v[232:233], v[230:231], v[246:247], v[244:245]
v_cvt_pk_bf16_f32 v232, v232, v233
v_lshlrev_b32 v234, 16, v232
v_and_b32 v235, 0xffff0000, v232
v_pk_add_f32 v[230:231], v[230:231], v[234:235]
v_cvt_pk_bf16_f32 v194, v230, v231
v_lshlrev_b32 v230, 16, v99
v_and_b32 v231, 0xffff0000, v99
v_pk_fma_f32 v[232:233], v[230:231], v[246:247], v[244:245]
v_cvt_pk_bf16_f32 v232, v232, v233
v_lshlrev_b32 v234, 16, v232
v_and_b32 v235, 0xffff0000, v232
v_pk_add_f32 v[230:231], v[230:231], v[234:235]
v_cvt_pk_bf16_f32 v195, v230, v231
v_lshlrev_b32 v230, 16, v100
v_and_b32 v231, 0xffff0000, v100
v_pk_fma_f32 v[232:233], v[230:231], v[246:247], v[244:245]
v_cvt_pk_bf16_f32 v232, v232, v233
v_lshlrev_b32 v234, 16, v232
v_and_b32 v235, 0xffff0000, v232
v_pk_add_f32 v[230:231], v[230:231], v[234:235]
v_cvt_pk_bf16_f32 v196, v230, v231
v_lshlrev_b32 v230, 16, v101
v_and_b32 v231, 0xffff0000, v101
v_pk_fma_f32 v[232:233], v[230:231], v[246:247], v[244:245]
v_cvt_pk_bf16_f32 v232, v232, v233
v_lshlrev_b32 v234, 16, v232
v_and_b32 v235, 0xffff0000, v232
v_pk_add_f32 v[230:231], v[230:231], v[234:235]
v_cvt_pk_bf16_f32 v197, v230, v231
v_lshlrev_b32 v230, 16, v102
v_and_b32 v231, 0xffff0000, v102
v_pk_fma_f32 v[232:233], v[230:231], v[246:247], v[244:245]
v_cvt_pk_bf16_f32 v232, v232, v233
v_lshlrev_b32 v234, 16, v232
v_and_b32 v235, 0xffff0000, v232
v_pk_add_f32 v[230:231], v[230:231], v[234:235]
v_cvt_pk_bf16_f32 v198, v230, v231
v_lshlrev_b32 v230, 16, v103
v_and_b32 v231, 0xffff0000, v103
v_pk_fma_f32 v[232:233], v[230:231], v[246:247], v[244:245]
v_cvt_pk_bf16_f32 v232, v232, v233
v_lshlrev_b32 v234, 16, v232
v_and_b32 v235, 0xffff0000, v232
v_pk_add_f32 v[230:231], v[230:231], v[234:235]
v_cvt_pk_bf16_f32 v199, v230, v231
v_lshlrev_b32 v230, 16, v104
v_and_b32 v231, 0xffff0000, v104
v_pk_fma_f32 v[232:233], v[230:231], v[246:247], v[244:245]
v_cvt_pk_bf16_f32 v232, v232, v233
v_lshlrev_b32 v234, 16, v232
v_and_b32 v235, 0xffff0000, v232
v_pk_add_f32 v[230:231], v[230:231], v[234:235]
v_cvt_pk_bf16_f32 v200, v230, v231
v_lshlrev_b32 v230, 16, v105
v_and_b32 v231, 0xffff0000, v105
v_pk_fma_f32 v[232:233], v[230:231], v[246:247], v[244:245]
v_cvt_pk_bf16_f32 v232, v232, v233
v_lshlrev_b32 v234, 16, v232
v_and_b32 v235, 0xffff0000, v232
v_pk_add_f32 v[230:231], v[230:231], v[234:235]
v_cvt_pk_bf16_f32 v201, v230, v231
v_lshlrev_b32 v230, 16, v106
v_and_b32 v231, 0xffff0000, v106
v_pk_fma_f32 v[232:233], v[230:231], v[246:247], v[244:245]
v_cvt_pk_bf16_f32 v232, v232, v233
v_lshlrev_b32 v234, 16, v232
v_and_b32 v235, 0xffff0000, v232
v_pk_add_f32 v[230:231], v[230:231], v[234:235]
v_cvt_pk_bf16_f32 v202, v230, v231
v_lshlrev_b32 v230, 16, v107
v_and_b32 v231, 0xffff0000, v107
v_pk_fma_f32 v[232:233], v[230:231], v[246:247], v[244:245]
v_cvt_pk_bf16_f32 v232, v232, v233
v_lshlrev_b32 v234, 16, v232
v_and_b32 v235, 0xffff0000, v232
v_pk_add_f32 v[230:231], v[230:231], v[234:235]
v_cvt_pk_bf16_f32 v203, v230, v231
v_lshlrev_b32 v230, 16, v108
v_and_b32 v231, 0xffff0000, v108
v_pk_fma_f32 v[232:233], v[230:231], v[246:247], v[244:245]
v_cvt_pk_bf16_f32 v232, v232, v233
v_lshlrev_b32 v234, 16, v232
v_and_b32 v235, 0xffff0000, v232
v_pk_add_f32 v[230:231], v[230:231], v[234:235]
v_cvt_pk_bf16_f32 v204, v230, v231
v_lshlrev_b32 v230, 16, v109
v_and_b32 v231, 0xffff0000, v109
v_pk_fma_f32 v[232:233], v[230:231], v[246:247], v[244:245]
v_cvt_pk_bf16_f32 v232, v232, v233
v_lshlrev_b32 v234, 16, v232
v_and_b32 v235, 0xffff0000, v232
v_pk_add_f32 v[230:231], v[230:231], v[234:235]
v_cvt_pk_bf16_f32 v205, v230, v231
v_lshlrev_b32 v230, 16, v110
v_and_b32 v231, 0xffff0000, v110
v_pk_fma_f32 v[232:233], v[230:231], v[246:247], v[244:245]
v_cvt_pk_bf16_f32 v232, v232, v233
v_lshlrev_b32 v234, 16, v232
v_and_b32 v235, 0xffff0000, v232
v_pk_add_f32 v[230:231], v[230:231], v[234:235]
v_cvt_pk_bf16_f32 v206, v230, v231
v_lshlrev_b32 v230, 16, v111
v_and_b32 v231, 0xffff0000, v111
v_pk_fma_f32 v[232:233], v[230:231], v[246:247], v[244:245]
v_cvt_pk_bf16_f32 v232, v232, v233
v_lshlrev_b32 v234, 16, v232
v_and_b32 v235, 0xffff0000, v232
v_pk_add_f32 v[230:231], v[230:231], v[234:235]
v_cvt_pk_bf16_f32 v207, v230, v231
s_cmp_eq_u32 s30, 0
s_cbranch_scc1 .La5_LT_SEL
s_mov_b32 s94, 0x05040100
s_mov_b32 s95, 0x07060302
s_waitcnt vmcnt(0)
v_accvgpr_read_b32 v230, a128
v_accvgpr_read_b32 v231, a132
v_perm_b32 v0, v231, v230, s94
v_perm_b32 v4, v231, v230, s95
v_accvgpr_read_b32 v230, a136
v_accvgpr_read_b32 v231, a140
v_perm_b32 v1, v231, v230, s94
v_perm_b32 v5, v231, v230, s95
v_accvgpr_read_b32 v230, a144
v_accvgpr_read_b32 v231, a148
v_perm_b32 v2, v231, v230, s94
v_perm_b32 v6, v231, v230, s95
v_accvgpr_read_b32 v230, a152
v_accvgpr_read_b32 v231, a156
v_perm_b32 v3, v231, v230, s94
v_perm_b32 v7, v231, v230, s95
v_accvgpr_read_b32 v230, a129
v_accvgpr_read_b32 v231, a133
v_perm_b32 v8, v231, v230, s94
v_perm_b32 v12, v231, v230, s95
v_accvgpr_read_b32 v230, a137
v_accvgpr_read_b32 v231, a141
v_perm_b32 v9, v231, v230, s94
v_perm_b32 v13, v231, v230, s95
v_accvgpr_read_b32 v230, a145
v_accvgpr_read_b32 v231, a149
v_perm_b32 v10, v231, v230, s94
v_perm_b32 v14, v231, v230, s95
v_accvgpr_read_b32 v230, a153
v_accvgpr_read_b32 v231, a157
v_perm_b32 v11, v231, v230, s94
v_perm_b32 v15, v231, v230, s95
v_accvgpr_read_b32 v230, a130
v_accvgpr_read_b32 v231, a134
v_perm_b32 v16, v231, v230, s94
v_perm_b32 v20, v231, v230, s95
v_accvgpr_read_b32 v230, a138
v_accvgpr_read_b32 v231, a142
v_perm_b32 v17, v231, v230, s94
v_perm_b32 v21, v231, v230, s95
v_accvgpr_read_b32 v230, a146
v_accvgpr_read_b32 v231, a150
v_perm_b32 v18, v231, v230, s94
v_perm_b32 v22, v231, v230, s95
v_accvgpr_read_b32 v230, a154
v_accvgpr_read_b32 v231, a158
v_perm_b32 v19, v231, v230, s94
v_perm_b32 v23, v231, v230, s95
v_accvgpr_read_b32 v230, a131
v_accvgpr_read_b32 v231, a135
v_perm_b32 v24, v231, v230, s94
v_perm_b32 v28, v231, v230, s95
v_accvgpr_read_b32 v230, a139
v_accvgpr_read_b32 v231, a143
v_perm_b32 v25, v231, v230, s94
v_perm_b32 v29, v231, v230, s95
v_accvgpr_read_b32 v230, a147
v_accvgpr_read_b32 v231, a151
v_perm_b32 v26, v231, v230, s94
v_perm_b32 v30, v231, v230, s95
v_accvgpr_read_b32 v230, a155
v_accvgpr_read_b32 v231, a159
v_perm_b32 v27, v231, v230, s94
v_perm_b32 v31, v231, v230, s95
v_accvgpr_read_b32 v230, a160
v_accvgpr_read_b32 v231, a164
v_perm_b32 v32, v231, v230, s94
v_perm_b32 v36, v231, v230, s95
v_accvgpr_read_b32 v230, a168
v_accvgpr_read_b32 v231, a172
v_perm_b32 v33, v231, v230, s94
v_perm_b32 v37, v231, v230, s95
v_accvgpr_read_b32 v230, a176
v_accvgpr_read_b32 v231, a180
v_perm_b32 v34, v231, v230, s94
v_perm_b32 v38, v231, v230, s95
v_accvgpr_read_b32 v230, a184
v_accvgpr_read_b32 v231, a188
v_perm_b32 v35, v231, v230, s94
v_perm_b32 v39, v231, v230, s95
v_accvgpr_read_b32 v230, a161
v_accvgpr_read_b32 v231, a165
v_perm_b32 v40, v231, v230, s94
v_perm_b32 v44, v231, v230, s95
v_accvgpr_read_b32 v230, a169
v_accvgpr_read_b32 v231, a173
v_perm_b32 v41, v231, v230, s94
v_perm_b32 v45, v231, v230, s95
v_accvgpr_read_b32 v230, a177
v_accvgpr_read_b32 v231, a181
v_perm_b32 v42, v231, v230, s94
v_perm_b32 v46, v231, v230, s95
v_accvgpr_read_b32 v230, a185
v_accvgpr_read_b32 v231, a189
v_perm_b32 v43, v231, v230, s94
v_perm_b32 v47, v231, v230, s95
v_accvgpr_read_b32 v230, a162
v_accvgpr_read_b32 v231, a166
v_perm_b32 v48, v231, v230, s94
v_perm_b32 v52, v231, v230, s95
v_accvgpr_read_b32 v230, a170
v_accvgpr_read_b32 v231, a174
v_perm_b32 v49, v231, v230, s94
v_perm_b32 v53, v231, v230, s95
v_accvgpr_read_b32 v230, a178
v_accvgpr_read_b32 v231, a182
v_perm_b32 v50, v231, v230, s94
v_perm_b32 v54, v231, v230, s95
v_accvgpr_read_b32 v230, a186
v_accvgpr_read_b32 v231, a190
v_perm_b32 v51, v231, v230, s94
v_perm_b32 v55, v231, v230, s95
v_accvgpr_read_b32 v230, a163
v_accvgpr_read_b32 v231, a167
v_perm_b32 v56, v231, v230, s94
v_perm_b32 v60, v231, v230, s95
v_accvgpr_read_b32 v230, a171
v_accvgpr_read_b32 v231, a175
v_perm_b32 v57, v231, v230, s94
v_perm_b32 v61, v231, v230, s95
v_accvgpr_read_b32 v230, a179
v_accvgpr_read_b32 v231, a183
v_perm_b32 v58, v231, v230, s94
v_perm_b32 v62, v231, v230, s95
v_accvgpr_read_b32 v230, a187
v_accvgpr_read_b32 v231, a191
v_perm_b32 v59, v231, v230, s94
v_perm_b32 v63, v231, v230, s95
v_accvgpr_read_b32 v230, a192
v_accvgpr_read_b32 v231, a196
v_perm_b32 v64, v231, v230, s94
v_perm_b32 v68, v231, v230, s95
v_accvgpr_read_b32 v230, a200
v_accvgpr_read_b32 v231, a204
v_perm_b32 v65, v231, v230, s94
v_perm_b32 v69, v231, v230, s95
v_accvgpr_read_b32 v230, a208
v_accvgpr_read_b32 v231, a212
v_perm_b32 v66, v231, v230, s94
v_perm_b32 v70, v231, v230, s95
v_accvgpr_read_b32 v230, a216
v_accvgpr_read_b32 v231, a220
v_perm_b32 v67, v231, v230, s94
v_perm_b32 v71, v231, v230, s95
v_accvgpr_read_b32 v230, a193
v_accvgpr_read_b32 v231, a197
v_perm_b32 v72, v231, v230, s94
v_perm_b32 v76, v231, v230, s95
v_accvgpr_read_b32 v230, a201
v_accvgpr_read_b32 v231, a205
v_perm_b32 v73, v231, v230, s94
v_perm_b32 v77, v231, v230, s95
v_accvgpr_read_b32 v230, a209
v_accvgpr_read_b32 v231, a213
v_perm_b32 v74, v231, v230, s94
v_perm_b32 v78, v231, v230, s95
v_accvgpr_read_b32 v230, a217
v_accvgpr_read_b32 v231, a221
v_perm_b32 v75, v231, v230, s94
v_perm_b32 v79, v231, v230, s95
v_accvgpr_read_b32 v230, a194
v_accvgpr_read_b32 v231, a198
v_perm_b32 v80, v231, v230, s94
v_perm_b32 v84, v231, v230, s95
v_accvgpr_read_b32 v230, a202
v_accvgpr_read_b32 v231, a206
v_perm_b32 v81, v231, v230, s94
v_perm_b32 v85, v231, v230, s95
v_accvgpr_read_b32 v230, a210
v_accvgpr_read_b32 v231, a214
v_perm_b32 v82, v231, v230, s94
v_perm_b32 v86, v231, v230, s95
v_accvgpr_read_b32 v230, a218
v_accvgpr_read_b32 v231, a222
v_perm_b32 v83, v231, v230, s94
v_perm_b32 v87, v231, v230, s95
v_accvgpr_read_b32 v230, a195
v_accvgpr_read_b32 v231, a199
v_perm_b32 v88, v231, v230, s94
v_perm_b32 v92, v231, v230, s95
v_accvgpr_read_b32 v230, a203
v_accvgpr_read_b32 v231, a207
v_perm_b32 v89, v231, v230, s94
v_perm_b32 v93, v231, v230, s95
v_accvgpr_read_b32 v230, a211
v_accvgpr_read_b32 v231, a215
v_perm_b32 v90, v231, v230, s94
v_perm_b32 v94, v231, v230, s95
v_accvgpr_read_b32 v230, a219
v_accvgpr_read_b32 v231, a223
v_perm_b32 v91, v231, v230, s94
v_perm_b32 v95, v231, v230, s95
.La5_LW4_1:
s_cbranch_execz .La5_LW4_5
v_accvgpr_read_b32 v230, a224
v_accvgpr_read_b32 v231, a228
v_perm_b32 v96, v231, v230, s94
v_perm_b32 v100, v231, v230, s95
v_accvgpr_read_b32 v230, a232
v_accvgpr_read_b32 v231, a236
v_perm_b32 v97, v231, v230, s94
v_perm_b32 v101, v231, v230, s95
v_accvgpr_read_b32 v230, a240
v_accvgpr_read_b32 v231, a244
v_perm_b32 v98, v231, v230, s94
v_perm_b32 v102, v231, v230, s95
v_accvgpr_read_b32 v230, a248
v_accvgpr_read_b32 v231, a252
v_perm_b32 v99, v231, v230, s94
v_perm_b32 v103, v231, v230, s95
v_accvgpr_read_b32 v230, a225
v_accvgpr_read_b32 v231, a229
v_perm_b32 v104, v231, v230, s94
v_perm_b32 v108, v231, v230, s95
v_accvgpr_read_b32 v230, a233
v_accvgpr_read_b32 v231, a237
v_perm_b32 v105, v231, v230, s94
v_perm_b32 v109, v231, v230, s95
v_accvgpr_read_b32 v230, a241
v_accvgpr_read_b32 v231, a245
v_perm_b32 v106, v231, v230, s94
v_perm_b32 v110, v231, v230, s95
v_accvgpr_read_b32 v230, a249
v_accvgpr_read_b32 v231, a253
v_perm_b32 v107, v231, v230, s94
v_perm_b32 v111, v231, v230, s95
v_accvgpr_read_b32 v230, a226
v_accvgpr_read_b32 v231, a230
v_perm_b32 v112, v231, v230, s94
v_perm_b32 v116, v231, v230, s95
v_accvgpr_read_b32 v230, a234
v_accvgpr_read_b32 v231, a238
v_perm_b32 v113, v231, v230, s94
v_perm_b32 v117, v231, v230, s95
v_accvgpr_read_b32 v230, a242
v_accvgpr_read_b32 v231, a246
v_perm_b32 v114, v231, v230, s94
v_perm_b32 v118, v231, v230, s95
v_accvgpr_read_b32 v230, a250
v_accvgpr_read_b32 v231, a254
v_perm_b32 v115, v231, v230, s94
v_perm_b32 v119, v231, v230, s95
v_accvgpr_read_b32 v230, a227
v_accvgpr_read_b32 v231, a231
v_perm_b32 v120, v231, v230, s94
v_perm_b32 v124, v231, v230, s95
v_accvgpr_read_b32 v230, a235
v_accvgpr_read_b32 v231, a239
v_perm_b32 v121, v231, v230, s94
v_perm_b32 v125, v231, v230, s95
v_accvgpr_read_b32 v230, a243
v_accvgpr_read_b32 v231, a247
v_perm_b32 v122, v231, v230, s94
v_perm_b32 v126, v231, v230, s95
v_accvgpr_read_b32 v230, a251
v_accvgpr_read_b32 v231, a255
v_perm_b32 v123, v231, v230, s94
v_perm_b32 v127, v231, v230, s95
v_accvgpr_read_b32 v230, a64
v_accvgpr_read_b32 v231, a68
v_perm_b32 v128, v231, v230, s94
v_perm_b32 v132, v231, v230, s95
v_accvgpr_read_b32 v230, a72
v_accvgpr_read_b32 v231, a76
v_perm_b32 v129, v231, v230, s94
v_perm_b32 v133, v231, v230, s95
v_accvgpr_read_b32 v230, a80
v_accvgpr_read_b32 v231, a84
v_perm_b32 v130, v231, v230, s94
v_perm_b32 v134, v231, v230, s95
v_accvgpr_read_b32 v230, a88
v_accvgpr_read_b32 v231, a92
v_perm_b32 v131, v231, v230, s94
v_perm_b32 v135, v231, v230, s95
v_accvgpr_read_b32 v230, a65
v_accvgpr_read_b32 v231, a69
v_perm_b32 v136, v231, v230, s94
v_perm_b32 v140, v231, v230, s95
v_accvgpr_read_b32 v230, a73
v_accvgpr_read_b32 v231, a77
v_perm_b32 v137, v231, v230, s94
v_perm_b32 v141, v231, v230, s95
v_accvgpr_read_b32 v230, a81
v_accvgpr_read_b32 v231, a85
v_perm_b32 v138, v231, v230, s94
v_perm_b32 v142, v231, v230, s95
v_accvgpr_read_b32 v230, a89
v_accvgpr_read_b32 v231, a93
v_perm_b32 v139, v231, v230, s94
v_perm_b32 v143, v231, v230, s95
v_accvgpr_read_b32 v230, a66
v_accvgpr_read_b32 v231, a70
v_perm_b32 v144, v231, v230, s94
v_perm_b32 v148, v231, v230, s95
v_accvgpr_read_b32 v230, a74
v_accvgpr_read_b32 v231, a78
v_perm_b32 v145, v231, v230, s94
v_perm_b32 v149, v231, v230, s95
v_accvgpr_read_b32 v230, a82
v_accvgpr_read_b32 v231, a86
v_perm_b32 v146, v231, v230, s94
v_perm_b32 v150, v231, v230, s95
v_accvgpr_read_b32 v230, a90
v_accvgpr_read_b32 v231, a94
v_perm_b32 v147, v231, v230, s94
v_perm_b32 v151, v231, v230, s95
v_accvgpr_read_b32 v230, a67
v_accvgpr_read_b32 v231, a71
v_perm_b32 v152, v231, v230, s94
v_perm_b32 v156, v231, v230, s95
v_accvgpr_read_b32 v230, a75
v_accvgpr_read_b32 v231, a79
v_perm_b32 v153, v231, v230, s94
v_perm_b32 v157, v231, v230, s95
v_accvgpr_read_b32 v230, a83
v_accvgpr_read_b32 v231, a87
v_perm_b32 v154, v231, v230, s94
v_perm_b32 v158, v231, v230, s95
v_accvgpr_read_b32 v230, a91
v_accvgpr_read_b32 v231, a95
v_perm_b32 v155, v231, v230, s94
v_perm_b32 v159, v231, v230, s95
v_accvgpr_read_b32 v230, a96
v_accvgpr_read_b32 v231, a100
v_perm_b32 v160, v231, v230, s94
v_perm_b32 v164, v231, v230, s95
v_accvgpr_read_b32 v230, a104
v_accvgpr_read_b32 v231, a108
v_perm_b32 v161, v231, v230, s94
v_perm_b32 v165, v231, v230, s95
v_accvgpr_read_b32 v230, a112
v_accvgpr_read_b32 v231, a116
v_perm_b32 v162, v231, v230, s94
v_perm_b32 v166, v231, v230, s95
v_accvgpr_read_b32 v230, a120
v_accvgpr_read_b32 v231, a124
v_perm_b32 v163, v231, v230, s94
v_perm_b32 v167, v231, v230, s95
v_accvgpr_read_b32 v230, a97
v_accvgpr_read_b32 v231, a101
v_perm_b32 v168, v231, v230, s94
v_perm_b32 v172, v231, v230, s95
v_accvgpr_read_b32 v230, a105
v_accvgpr_read_b32 v231, a109
v_perm_b32 v169, v231, v230, s94
v_perm_b32 v173, v231, v230, s95
v_accvgpr_read_b32 v230, a113
v_accvgpr_read_b32 v231, a117
v_perm_b32 v170, v231, v230, s94
v_perm_b32 v174, v231, v230, s95
v_accvgpr_read_b32 v230, a121
v_accvgpr_read_b32 v231, a125
v_perm_b32 v171, v231, v230, s94
v_perm_b32 v175, v231, v230, s95
v_accvgpr_read_b32 v230, a98
v_accvgpr_read_b32 v231, a102
v_perm_b32 v176, v231, v230, s94
v_perm_b32 v180, v231, v230, s95
v_accvgpr_read_b32 v230, a106
v_accvgpr_read_b32 v231, a110
v_perm_b32 v177, v231, v230, s94
v_perm_b32 v181, v231, v230, s95
v_accvgpr_read_b32 v230, a114
v_accvgpr_read_b32 v231, a118
v_perm_b32 v178, v231, v230, s94
v_perm_b32 v182, v231, v230, s95
v_accvgpr_read_b32 v230, a122
v_accvgpr_read_b32 v231, a126
v_perm_b32 v179, v231, v230, s94
v_perm_b32 v183, v231, v230, s95
v_accvgpr_read_b32 v230, a99
v_accvgpr_read_b32 v231, a103
v_perm_b32 v184, v231, v230, s94
v_perm_b32 v188, v231, v230, s95
v_accvgpr_read_b32 v230, a107
v_accvgpr_read_b32 v231, a111
v_perm_b32 v185, v231, v230, s94
v_perm_b32 v189, v231, v230, s95
v_accvgpr_read_b32 v230, a115
v_accvgpr_read_b32 v231, a119
v_perm_b32 v186, v231, v230, s94
v_perm_b32 v190, v231, v230, s95
v_accvgpr_read_b32 v230, a123
v_accvgpr_read_b32 v231, a127
v_perm_b32 v187, v231, v230, s94
v_perm_b32 v191, v231, v230, s95
s_and_b32 s92, s3, 1
s_lshr_b32 s93, s3, 1
v_lshrrev_b32 v230, 6, v255
v_and_b32 v231, 48, v255
v_lshl_add_u32 v240, v230, 10, v231
s_lshl_b32 s96, s92, 14
v_add_u32 v240, s96, v240
s_lshl_b32 s96, s92, 10
v_add_u32 v241, s96, v255
s_lshl_b32 s97, s3, 10
v_add_u32 v208, s97, v255
v_bfe_u32 v230, v255, 4, 1
v_bfe_u32 v231, v255, 6, 2
v_lshrrev_b32 v232, 8, v255
v_lshlrev_b32 v209, 11, v230
v_lshl_add_u32 v209, v231, 8, v209
v_lshl_add_u32 v209, v232, 6, v209
v_add_u32 v209, s96, v209
v_bfe_u32 v233, v255, 4, 2
v_mul_u32_u24 v210, 640, v233
v_lshl_add_u32 v210, v231, 3, v210
s_mul_i32 s96, s93, 2560
s_lshl_b32 s92, s92, 5
s_add_u32 s96, s96, s92
v_add_u32 v210, s96, v210
v_cmp_eq_u32 s[98:99], v231, v232
v_mul_u32_u24 v211, 5, v255
s_lshl_b32 s96, s3, 7
v_lshrrev_b32 v212, 3, v255
v_and_b32 v212, 62, v212
v_add_u32 v212, s96, v212
v_lshlrev_b32 v234, 6, v230
v_lshl_add_u32 v234, v232, 4, v234
v_add_u32 v234, s96, v234
v_mov_b32 v235, 1024
v_cmp_eq_u32 vcc, 0, v231
s_nop 1
v_cndmask_b32 v216, v235, v234, vcc
v_cmp_eq_u32 vcc, 1, v231
s_nop 1
v_cndmask_b32 v217, v235, v234, vcc
v_cmp_eq_u32 vcc, 2, v231
s_nop 1
v_cndmask_b32 v218, v235, v234, vcc
v_cmp_eq_u32 vcc, 3, v231
s_nop 1
v_cndmask_b32 v219, v235, v234, vcc
v_mov_b32 v220, 0
v_mov_b32 v221, 0
v_mov_b32 v222, 0
v_mov_b32_e64 v223, 0
ds_write_b128 v235, v[220:223] offset:10240
s_lshl_b32 s92, s100, 13
s_add_u32 s80, s92, 32768
s_add_u32 s20, s80, 4096
; ---- top two rows of the sequence; SwiGLU slice of 32 hidden units
.La5_LT_SEL:
.La5_LW4_2:
s_cbranch_execz .La5_LW4_R0
s_and_b32 s97, s2, 7
s_lshl_b32 s97, s97, 12
s_cmp_lg_u32 s3, 0
s_cbranch_scc1 .La5_LT_S2
s_mov_b32 s96, 0
.La5_LT_S1:
s_cmp_lg_u32 s30, 0
s_nop 0
s_cbranch_scc1 .La5_LT_S0
s_sleep 8
.La5_LT_S0:
buffer_load_dwordx4 v[224:227], v255, s[44:47], s97 offen offset:0 sc1
buffer_load_dwordx4 v[232:235], v255, s[44:47], s97 offen offset:1024 sc1
buffer_load_dwordx4 v[244:247], v255, s[44:47], s97 offen offset:2048 sc1
buffer_load_dwordx4 v[248:251], v255, s[44:47], s97 offen offset:3072 sc1
s_add_u32 s96, s96, 1
s_lshr_b32 s92, s96, 20
s_waitcnt vmcnt(3)
v_min3_u32 v228, v224, v225, v226
v_min_u32 v228, v227, v228
s_waitcnt vmcnt(2)
v_min3_u32 v228, v232, v233, v228
v_min3_u32 v228, v234, v235, v228
s_waitcnt vmcnt(1)
v_min3_u32 v228, v244, v245, v228
v_min3_u32 v228, v246, v247, v228
s_waitcnt vmcnt(0)
v_min3_u32 v228, v248, v249, v228
v_min3_u32 v228, v250, v251, v228
v_cmp_ne_u32 vcc, 0, v228
v_max_u32 v229, v224, v232
v_max_u32 v230, v244, v248
s_cbranch_scc1 .La5_LT_S3
s_cmp_eq_u64 vcc, -1
s_cbranch_scc0 .La5_LT_S1
.La5_LT_S3:
v_max_u32 v228, v229, v230
v_min_u32 v229, v229, v230
v_mov_b32 v231, v228
v_min_u32 v224, v224, v232
v_min_u32 v244, v244, v248
v_max3_u32 v229, v229, v224, v244
v_max3_u32 v230, v226, v234, v246
v_max3_u32 v229, v229, v230, v250
s_nop 1
v_max_u32_dpp v231, v231, v231 row_shr:1 row_mask:0xf bank_mask:0xf
s_nop 1
v_max_u32_dpp v231, v231, v231 row_shr:2 row_mask:0xf bank_mask:0xf
s_nop 1
v_max_u32_dpp v231, v231, v231 row_shr:4 row_mask:0xf bank_mask:0xf
s_nop 1
v_max_u32_dpp v231, v231, v231 row_shr:8 row_mask:0xf bank_mask:0xf
s_nop 1
v_max_u32_dpp v231, v231, v231 row_bcast:15 row_mask:0xa bank_mask:0xf
s_nop 1
v_max_u32_dpp v231, v231, v231 row_bcast:31 row_mask:0xc bank_mask:0xf
s_nop 0
v_readlane_b32 s82, v231, 63
s_nop 1
v_cmp_eq_u32 vcc, s82, v228
v_mov_b32 v230, s82
v_mov_b32 v224, 0
v_cndmask_b32 v228, v228, v229, vcc
s_nop 1
v_max_u32_dpp v228, v228, v228 row_shr:1 row_mask:0xf bank_mask:0xf
s_nop 1
v_max_u32_dpp v228, v228, v228 row_shr:2 row_mask:0xf bank_mask:0xf
s_nop 1
v_max_u32_dpp v228, v228, v228 row_shr:4 row_mask:0xf bank_mask:0xf
s_nop 1
v_max_u32_dpp v228, v228, v228 row_shr:8 row_mask:0xf bank_mask:0xf
s_nop 1
v_max_u32_dpp v228, v228, v228 row_bcast:15 row_mask:0xa bank_mask:0xf
s_nop 1
v_max_u32_dpp v228, v228, v228 row_bcast:31 row_mask:0xc bank_mask:0xf
s_nop 0
v_readlane_b32 s81, v228, 63
s_nop 1
v_mov_b32 v231, s81
ds_write_b64 v224, v[230:231] offset:64
.La5_LT_S2:
v_mov_b32 v224, 0
s_waitcnt lgkmcnt(0)
s_barrier
ds_read_b64 v[228:229], v224 offset:64
s_waitcnt lgkmcnt(0)
v_readfirstlane_b32 s82, v228
v_readfirstlane_b32 s81, v229
s_nop 0
s_andn2_b32 s84, 0x7ff, s82
s_andn2_b32 s21, 0x7ff, s81
s_cmp_eq_u32 s30, 0
s_cbranch_scc1 .La5_LT_ST
s_cmp_lt_u32 s3, 2
s_cselect_b32 s91, s84, s21
s_cselect_b32 s85, s82, s81
s_lshr_b32 s92, s91, 4
s_lshl_b32 s92, s92, 15
s_and_b32 s93, s91, 15
s_lshl_b32 s93, s93, 6
s_add_u32 s92, s92, s93
s_add_u32 s93, s92, 4194304
s_add_u32 s94, s92, 8388608
s_add_u32 s95, s92, 12582912
buffer_load_dwordx4 v[224:227], v240, s[32:35], s92 offen sc1
buffer_load_dwordx4 v[228:231], v240, s[32:35], s93 offen sc1
buffer_load_dwordx4 v[232:235], v240, s[32:35], s94 offen sc1
buffer_load_dwordx4 v[244:247], v240, s[32:35], s95 offen sc1
s_lshl_b32 s96, s91, 11
s_lshr_b32 s92, s91, 3
s_lshl4_add_u32 s97, s92, s97
buffer_load_dwordx4 v[248:251], v241, s[68:71], s96 offen
buffer_load_dwordx4 v[220:223], off, s[44:47], s97 sc1
s_waitcnt vmcnt(4)
v_lshlrev_b32 v236, 16, v224
v_and_b32 v237, 0xffff0000, v224
v_lshlrev_b32 v238, 16, v228
v_and_b32 v239, 0xffff0000, v228
v_pk_add_f32 v[236:237], v[236:237], v[238:239]
v_lshlrev_b32 v242, 16, v225
v_and_b32 v243, 0xffff0000, v225
v_lshlrev_b32 v238, 16, v229
v_and_b32 v239, 0xffff0000, v229
v_pk_add_f32 v[242:243], v[242:243], v[238:239]
v_lshlrev_b32 v252, 16, v226
v_and_b32 v253, 0xffff0000, v226
v_lshlrev_b32 v238, 16, v230
v_and_b32 v239, 0xffff0000, v230
v_pk_add_f32 v[252:253], v[252:253], v[238:239]
v_lshlrev_b32 v214, 16, v227
v_and_b32 v215, 0xffff0000, v227
v_lshlrev_b32 v238, 16, v231
v_and_b32 v239, 0xffff0000, v231
v_pk_add_f32 v[214:215], v[214:215], v[238:239]
s_waitcnt vmcnt(2)
v_lshlrev_b32 v224, 16, v232
v_and_b32 v225, 0xffff0000, v232
v_lshlrev_b32 v226, 16, v244
v_and_b32 v227, 0xffff0000, v244
v_pk_add_f32 v[224:225], v[224:225], v[226:227]
v_pk_add_f32 v[236:237], v[236:237], v[224:225]
v_lshlrev_b32 v224, 16, v233
v_and_b32 v225, 0xffff0000, v233
v_lshlrev_b32 v226, 16, v245
v_and_b32 v227, 0xffff0000, v245
v_pk_add_f32 v[224:225], v[224:225], v[226:227]
v_pk_add_f32 v[242:243], v[242:243], v[224:225]
v_lshlrev_b32 v224, 16, v234
v_and_b32 v225, 0xffff0000, v234
v_lshlrev_b32 v226, 16, v246
v_and_b32 v227, 0xffff0000, v246
v_pk_add_f32 v[224:225], v[224:225], v[226:227]
v_pk_add_f32 v[252:253], v[252:253], v[224:225]
v_lshlrev_b32 v224, 16, v235
v_and_b32 v225, 0xffff0000, v235
v_lshlrev_b32 v226, 16, v247
v_and_b32 v227, 0xffff0000, v247
v_pk_add_f32 v[224:225], v[224:225], v[226:227]
v_pk_add_f32 v[214:215], v[214:215], v[224:225]
s_waitcnt vmcnt(0)
v_cmp_eq_u32 vcc, s85, v220
s_nop 1
v_cndmask_b32 v220, v223, v221, vcc
v_lshlrev_b32 v222, 16, v220
v_rcp_f32 v222, v222
v_and_b32 v220, 0xffff0000, v220
v_mul_f32 v220, v220, v222
v_mov_b32 v223, v222
v_xor_b32 v220, 0x80000000, v220
v_mov_b32 v221, v220
v_lshlrev_b32 v224, 16, v248
v_and_b32 v225, 0xffff0000, v248
v_pk_add_f32 v[236:237], v[236:237], v[224:225]
v_cvt_pk_bf16_f32 v224, v236, v237
v_lshlrev_b32 v226, 16, v224
v_and_b32 v227, 0xffff0000, v224
v_pk_fma_f32 v[226:227], v[226:227], v[222:223], v[220:221]
v_cvt_pk_bf16_f32 v228, v226, v227
v_lshlrev_b32 v224, 16, v249
v_and_b32 v225, 0xffff0000, v249
v_pk_add_f32 v[242:243], v[242:243], v[224:225]
v_cvt_pk_bf16_f32 v224, v242, v243
v_lshlrev_b32 v226, 16, v224
v_and_b32 v227, 0xffff0000, v224
v_pk_fma_f32 v[226:227], v[226:227], v[222:223], v[220:221]
v_cvt_pk_bf16_f32 v229, v226, v227
v_lshlrev_b32 v224, 16, v250
v_and_b32 v225, 0xffff0000, v250
v_pk_add_f32 v[252:253], v[252:253], v[224:225]
v_cvt_pk_bf16_f32 v224, v252, v253
v_lshlrev_b32 v226, 16, v224
v_and_b32 v227, 0xffff0000, v224
v_pk_fma_f32 v[226:227], v[226:227], v[222:223], v[220:221]
v_cvt_pk_bf16_f32 v230, v226, v227
v_lshlrev_b32 v224, 16, v251
v_and_b32 v225, 0xffff0000, v251
v_pk_add_f32 v[214:215], v[214:215], v[224:225]
v_cvt_pk_bf16_f32 v224, v214, v215
v_lshlrev_b32 v226, 16, v224
v_and_b32 v227, 0xffff0000, v224
v_pk_fma_f32 v[226:227], v[226:227], v[222:223], v[220:221]
v_cvt_pk_bf16_f32 v231, v226, v227
ds_write_b128 v208, v[228:231] offset:1024
s_waitcnt lgkmcnt(0)
s_barrier
ds_read_b128 v[224:227], v209 offset:1024
ds_read_b128 v[232:235], v209 offset:1040
ds_read_b128 v[244:247], v209 offset:1056
ds_read_b128 v[248:251], v209 offset:1072
s_waitcnt lgkmcnt(3)
v_mfma_f32_16x16x32_bf16 a[0:3], v[224:227], v[0:3], 0
v_mfma_f32_16x16x32_bf16 a[4:7], v[224:227], v[4:7], 0
v_mfma_f32_16x16x32_bf16 a[8:11], v[224:227], v[8:11], 0
v_mfma_f32_16x16x32_bf16 a[12:15], v[224:227], v[12:15], 0
v_mfma_f32_16x16x32_bf16 a[16:19], v[224:227], v[16:19], 0
v_mfma_f32_16x16x32_bf16 a[20:23], v[224:227], v[20:23], 0
v_mfma_f32_16x16x32_bf16 a[24:27], v[224:227], v[24:27], 0
v_mfma_f32_16x16x32_bf16 a[28:31], v[224:227], v[28:31], 0
s_waitcnt lgkmcnt(2)
v_mfma_f32_16x16x32_bf16 a[0:3], v[232:235], v[32:35], a[0:3]
v_mfma_f32_16x16x32_bf16 a[4:7], v[232:235], v[36:39], a[4:7]
v_mfma_f32_16x16x32_bf16 a[8:11], v[232:235], v[40:43], a[8:11]
v_mfma_f32_16x16x32_bf16 a[12:15], v[232:235], v[44:47], a[12:15]
v_mfma_f32_16x16x32_bf16 a[16:19], v[232:235], v[48:51], a[16:19]
v_mfma_f32_16x16x32_bf16 a[20:23], v[232:235], v[52:55], a[20:23]
v_mfma_f32_16x16x32_bf16 a[24:27], v[232:235], v[56:59], a[24:27]
v_mfma_f32_16x16x32_bf16 a[28:31], v[232:235], v[60:63], a[28:31]
s_waitcnt lgkmcnt(1)
v_mfma_f32_16x16x32_bf16 a[0:3], v[244:247], v[64:67], a[0:3]
v_mfma_f32_16x16x32_bf16 a[4:7], v[244:247], v[68:71], a[4:7]
v_mfma_f32_16x16x32_bf16 a[8:11], v[244:247], v[72:75], a[8:11]
v_mfma_f32_16x16x32_bf16 a[12:15], v[244:247], v[76:79], a[12:15]
v_mfma_f32_16x16x32_bf16 a[16:19], v[244:247], v[80:83], a[16:19]
v_mfma_f32_16x16x32_bf16 a[20:23], v[244:247], v[84:87], a[20:23]
v_mfma_f32_16x16x32_bf16 a[24:27], v[244:247], v[88:91], a[24:27]
v_mfma_f32_16x16x32_bf16 a[28:31], v[244:247], v[92:95], a[28:31]
s_waitcnt lgkmcnt(0)
v_mfma_f32_16x16x32_bf16 a[0:3], v[248:251], v[96:99], a[0:3]
v_mfma_f32_16x16x32_bf16 a[4:7], v[248:251], v[100:103], a[4:7]
v_mfma_f32_16x16x32_bf16 a[8:11], v[248:251], v[104:107], a[8:11]
v_mfma_f32_16x16x32_bf16 a[12:15], v[248:251], v[108:111], a[12:15]
v_mfma_f32_16x16x32_bf16 a[16:19], v[248:251], v[112:115], a[16:19]
v_mfma_f32_16x16x32_bf16 a[20:23], v[248:251], v[116:119], a[20:23]
v_mfma_f32_16x16x32_bf16 a[24:27], v[248:251], v[120:123], a[24:27]
v_mfma_f32_16x16x32_bf16 a[28:31], v[248:251], v[124:127], a[28:31]
s_mov_b64 exec, s[98:99]
ds_write_b64 v210, a[0:1] offset:5120
ds_write_b64 v210, a[4:5] offset:5200
ds_write_b64 v210, a[8:9] offset:5280
ds_write_b64 v210, a[12:13] offset:5360
ds_write_b64 v210, a[16:17] offset:5440
s_nop 3
ds_write_b64 v210, a[20:21] offset:5520
s_nop 3
ds_write_b64 v210, a[24:25] offset:5600
s_nop 3
ds_write_b64 v210, a[28:29] offset:5680
s_mov_b64 exec, -1
s_waitcnt lgkmcnt(0)
s_barrier
ds_read_b128 v[224:227], v211 offset:5120
ds_read_b128 v[232:235], v211 offset:5136
ds_read_b128 v[244:247], v211 offset:5152
ds_read_b128 v[248:251], v211 offset:5168
s_waitcnt lgkmcnt(2)
v_pk_add_f32 v[224:225], v[224:225], v[226:227]
v_pk_add_f32 v[232:233], v[232:233], v[234:235]
s_waitcnt lgkmcnt(0)
v_pk_add_f32 v[244:245], v[244:245], v[246:247]
v_pk_add_f32 v[248:249], v[248:249], v[250:251]
v_pk_add_f32 v[224:225], v[224:225], v[232:233]
v_pk_add_f32 v[244:245], v[244:245], v[248:249]
v_pk_add_f32 v[224:225], v[224:225], v[244:245]
v_cvt_pk_bf16_f32 v224, v224, v225
v_mov_b32 v226, v224
s_nop 1
v_permlane32_swap_b32 v224, v226
s_nop 1
v_lshlrev_b32 v228, 16, v224
v_and_b32 v229, 0xffff0000, v224
v_lshlrev_b32 v232, 16, v226
v_and_b32 v233, 0xffff0000, v226
v_mul_f32 v230, 0xbfb8aa3b, v228
v_mul_f32 v231, 0xbfb8aa3b, v229
v_exp_f32 v230, v230
v_exp_f32 v231, v231
v_add_f32 v230, 1.0, v230
v_add_f32 v231, 1.0, v231
v_rcp_f32 v230, v230
v_rcp_f32 v231, v231
s_nop 0
v_pk_mul_f32 v[228:229], v[228:229], v[230:231]
v_cvt_pk_bf16_f32 v228, v228, v229
v_lshlrev_b32 v230, 16, v228
v_and_b32 v231, 0xffff0000, v228
v_pk_mul_f32 v[230:231], v[230:231], v[232:233]
v_cvt_pk_bf16_f32 v228, v230, v231
ds_write_b16 v212, v228 offset:10240
ds_write_b16_d16_hi v212, v228 offset:10304
s_waitcnt lgkmcnt(0)
ds_read_b128 v[224:227], v216 offset:10240
ds_read_b128 v[232:235], v217 offset:10240
ds_read_b128 v[244:247], v218 offset:10240
ds_read_b128 v[248:251], v219 offset:10240
s_waitcnt lgkmcnt(3)
v_mfma_f32_16x16x32_bf16 v[0:3], v[224:227], v[128:131], 0
v_mfma_f32_16x16x32_bf16 v[4:7], v[224:227], v[132:135], 0
v_mfma_f32_16x16x32_bf16 v[8:11], v[224:227], v[136:139], 0
v_mfma_f32_16x16x32_bf16 v[12:15], v[224:227], v[140:143], 0
s_waitcnt lgkmcnt(2)
v_mfma_f32_16x16x32_bf16 v[0:3], v[232:235], v[144:147], v[0:3]
v_mfma_f32_16x16x32_bf16 v[4:7], v[232:235], v[148:151], v[4:7]
v_mfma_f32_16x16x32_bf16 v[8:11], v[232:235], v[152:155], v[8:11]
v_mfma_f32_16x16x32_bf16 v[12:15], v[232:235], v[156:159], v[12:15]
s_waitcnt lgkmcnt(1)
v_mfma_f32_16x16x32_bf16 v[0:3], v[244:247], v[160:163], v[0:3]
v_mfma_f32_16x16x32_bf16 v[4:7], v[244:247], v[164:167], v[4:7]
v_mfma_f32_16x16x32_bf16 v[8:11], v[244:247], v[168:171], v[8:11]
v_mfma_f32_16x16x32_bf16 v[12:15], v[244:247], v[172:175], v[12:15]
s_waitcnt lgkmcnt(0)
v_mfma_f32_16x16x32_bf16 v[0:3], v[248:251], v[176:179], v[0:3]
v_mfma_f32_16x16x32_bf16 v[4:7], v[248:251], v[180:183], v[4:7]
v_mfma_f32_16x16x32_bf16 v[8:11], v[248:251], v[184:187], v[8:11]
v_mfma_f32_16x16x32_bf16 v[12:15], v[248:251], v[188:191], v[12:15]
s_nop 7
v_or_b32 v16, 1, v0
v_or_b32 v20, 1, v1
v_or_b32 v17, 1, v4
v_or_b32 v21, 1, v5
v_or_b32 v18, 1, v8
v_or_b32 v22, 1, v9
v_or_b32 v19, 1, v12
v_or_b32 v23, 1, v13
buffer_store_dwordx4 v[16:19], v208, s[44:47], s80 offen sc1
buffer_store_dwordx4 v[20:23], v208, s[44:47], s20 offen sc1
; ---- rows that were not routed leave; final sums of the routed rows
.La5_LT_ST:
.La5_LW4_3:
s_cbranch_execz .La5_LW4_R0
s_lshl_b32 s92, s76, 5
s_add_u32 s92, s92, s72
s_lshl2_add_u32 s92, s92, s3
s_lshl_b32 s93, s92, 1
v_bfe_u32 v230, v255, 6, 1
v_add_u32 v231, s93, v230
v_cmp_ne_u32 vcc, s84, v231
v_cmp_ne_u32 s[94:95], s21, v231
v_lshrrev_b32 v232, 7, v255
v_and_b32 v233, 48, v255
v_lshl_add_u32 v232, v232, 6, v233
v_lshl_add_u32 v232, v230, 11, v232
s_lshl_b32 s92, s92, 12
s_and_b64 exec, vcc, s[94:95]
s_cbranch_scc0 .La5_LT_F0
buffer_store_dwordx4 v[192:195], v232, s[56:59], s92 offen offset:0 sc1
buffer_store_dwordx4 v[196:199], v232, s[56:59], s92 offen offset:512 sc1
buffer_store_dwordx4 v[200:203], v232, s[56:59], s92 offen offset:1024 sc1
buffer_store_dwordx4 v[204:207], v232, s[56:59], s92 offen offset:1536 sc1
.La5_LT_F0:
s_mov_b64 exec, -1
s_cmp_eq_u32 s75, 1
s_cbranch_scc1 .La5_LT_END
s_lshr_b32 s92, s2, 6
s_lshl2_add_u32 s92, s92, s74
s_and_b32 s93, s2, 7
s_lshl3_add_u32 s91, s92, s93
s_cmp_lt_u32 s91, 64
s_cselect_b32 s84, s84, s21
s_cselect_b32 s82, s82, s81
s_bfe_u32 s92, s91, 0x30003
s_and_b32 s93, s91, 3
s_lshl2_add_u32 s96, s92, s93
s_and_b32 s85, s91, 4
s_lshl_b32 s85, s85, 1
v_bfe_u32 v0, v255, 6, 2
v_bfe_u32 v1, v255, 4, 2
v_lshlrev_b32 v1, 1, v1
v_lshl_add_u32 v0, v0, 4, v1
.La5_LW4_4:
s_cbranch_execz .La5_LW4_R0
v_add_u32 v0, s85, v0
s_lshl_b32 s85, s96, 10
v_add_u32 v1, s85, v0
s_lshl_b32 s85, s96, 6
v_add_u32 v0, s85, v0
s_lshr_b32 s92, s84, 4
s_lshl_b32 s92, s92, 15
s_and_b32 s93, s84, 15
s_lshl_b32 s93, s93, 6
s_add_u32 s92, s92, s93
s_add_u32 s93, s92, 4194304
s_add_u32 s94, s92, 8388608
s_add_u32 s95, s92, 12582912
buffer_load_ushort v4, v1, s[32:35], s92 offen sc1
buffer_load_ushort v5, v1, s[32:35], s93 offen sc1
buffer_load_ushort v6, v1, s[32:35], s94 offen sc1
buffer_load_ushort v7, v1, s[32:35], s95 offen sc1
s_lshl_b32 s21, s84, 11
s_lshr_b32 s92, s84, 3
s_lshl4_add_u32 s97, s92, s97
buffer_load_ushort v8, v0, s[68:71], s21 offen
buffer_load_dwordx4 v[12:15], off, s[44:47], s97 sc1
v_lshrrev_b32 v2, 6, v255
v_lshlrev_b32 v2, 13, v2
v_and_b32 v3, 48, v255
v_add_u32 v2, v2, v3
s_lshl_b32 s92, s3, 17
s_lshl_b32 s93, s91, 6
s_add_u32 s92, s92, s93
s_add_u32 s94, s92, 32768
s_cmp_eq_u32 s3, 3
s_cselect_b32 s95, 0, 524288
s_cselect_b32 s96, 0, -1
s_add_u32 s95, s95, s94
s_ashr_i32 s92, s82, 31
s_orn2_b32 s92, 0x80000000, s92
s_xor_b32 s92, s92, s82
s_and_b32 s92, s92, 0xffff0000
v_mov_b32 v9, 0xbfb8aa3b
v_mul_f32 v9, s92, v9
v_exp_f32 v9, v9
v_lshrrev_b32 v3, 8, v255
s_lshl_b32 s92, s3, 4
v_add_f32 v9, 1.0, v9
v_lshl_add_u32 v3, v3, 2, s92
v_rcp_f32 v9, v9
v_and_b32 v10, 48, v255
v_mul_u32_u24 v10, 20, v10
v_add_u32 v3, v3, v10
v_cvt_pk_bf16_f32 v9, v9, v9
v_lshlrev_b32 v9, 16, v9
v_and_b32 v10, 0xf0, v255
v_mul_u32_u24 v10, 5, v10
s_waitcnt vmcnt(0)
v_cmp_eq_u32 vcc, s82, v12
s_nop 1
v_cndmask_b32 v12, v15, v13, vcc
v_lshlrev_b32 v14, 16, v12
v_rcp_f32 v14, v14
v_and_b32 v12, 0xffff0000, v12
v_mul_f32 v12, v12, v14
v_mov_b32 v15, v14
v_xor_b32 v12, 0x80000000, v12
v_mov_b32 v13, v12
v_lshlrev_b32 v4, 16, v4
v_lshlrev_b32 v5, 16, v5
v_lshlrev_b32 v6, 16, v6
v_lshlrev_b32 v7, 16, v7
v_lshlrev_b32 v8, 16, v8
v_add_f32 v4, v4, v5
v_add_f32 v6, v6, v7
v_add_f32 v4, v4, v6
v_add_f32 v4, v4, v8
v_cvt_pk_bf16_f32 v4, v4, v4
v_lshlrev_b32 v4, 16, v4
v_fma_f32 v5, v4, v14, v12
v_cvt_pk_bf16_f32 v5, v5, v5
v_lshlrev_b32 v5, 16, v5
s_sleep 40
s_mov_b32 s93, 0
.La5_LT_Y:
buffer_load_dwordx4 v[16:19], v2, s[44:47], s94 offen sc1
buffer_load_dwordx4 v[20:23], v2, s[44:47], s95 offen sc1
s_add_u32 s93, s93, 1
s_lshr_b32 s92, s93, 20
s_waitcnt vmcnt(0)
v_min3_u32 v24, v16, v17, v18
v_min3_u32 v25, v20, v21, v22
v_min3_u32 v24, v24, v19, v23
v_min_u32 v24, v24, v25
v_cmp_ne_u32 vcc, 0, v24
v_and_b32 v20, s96, v20
v_and_b32 v21, s96, v21
v_and_b32 v22, s96, v22
v_and_b32 v23, s96, v23
s_cbranch_scc1 .La5_LT_Y1
s_cmp_eq_u64 vcc, -1
s_cbranch_scc0 .La5_LT_Y
.La5_LT_Y1:
v_pk_add_f32 v[16:17], v[16:17], v[20:21]
v_pk_add_f32 v[18:19], v[18:19], v[22:23]
s_nop 0
v_add_f32_dpp v16, v16, v16 row_ror:4 row_mask:0xf bank_mask:0xf
v_add_f32_dpp v17, v17, v17 row_ror:4 row_mask:0xf bank_mask:0xf
v_add_f32_dpp v18, v18, v18 row_ror:4 row_mask:0xf bank_mask:0xf
v_add_f32_dpp v19, v19, v19 row_ror:4 row_mask:0xf bank_mask:0xf
v_add_f32_dpp v16, v16, v16 row_ror:8 row_mask:0xf bank_mask:0xf
v_add_f32_dpp v17, v17, v17 row_ror:8 row_mask:0xf bank_mask:0xf
v_add_f32_dpp v18, v18, v18 row_ror:8 row_mask:0xf bank_mask:0xf
v_add_f32_dpp v19, v19, v19 row_ror:8 row_mask:0xf bank_mask:0xf
ds_write_b32 v3, v16 offset:5120
ds_write_b32 v3, v17 offset:5200
ds_write_b32 v3, v18 offset:5280
ds_write_b32 v3, v19 offset:5360
s_waitcnt lgkmcnt(0)
s_barrier
ds_read_b128 v[16:19], v10 offset:5120
ds_read_b128 v[20:23], v10 offset:5136
ds_read_b128 v[24:27], v10 offset:5152
ds_read_b128 v[28:31], v10 offset:5168
s_waitcnt lgkmcnt(2)
v_pk_add_f32 v[16:17], v[16:17], v[18:19]
v_pk_add_f32 v[20:21], v[20:21], v[22:23]
s_waitcnt lgkmcnt(0)
v_pk_add_f32 v[24:25], v[24:25], v[26:27]
v_pk_add_f32 v[28:29], v[28:29], v[30:31]
v_pk_add_f32 v[16:17], v[16:17], v[20:21]
v_pk_add_f32 v[24:25], v[24:25], v[28:29]
v_pk_add_f32 v[16:17], v[16:17], v[24:25]
v_add_f32 v16, v16, v17
v_cvt_pk_bf16_f32 v16, v16, v16
v_lshlrev_b32 v16, 16, v16
v_mul_f32 v16, v16, v9
v_cvt_pk_bf16_f32 v16, v16, v16
v_lshlrev_b32 v16, 16, v16
v_add_f32 v16, v16, v5
v_cvt_pk_bf16_f32 v16, v16, v16
v_lshlrev_b32 v16, 16, v16
v_add_f32 v16, v16, v4
v_cvt_pk_bf16_f32 v16, v16, v16
s_cmp_lg_u32 s3, 0
s_cbranch_scc1 .La5_LT_END
s_mov_b64 exec, 0xffff
buffer_store_short v16, v0, s[56:59], s21 offen
s_mov_b64 exec, -1
.La5_LT_END:
.La5_LW4_5:
s_cbranch_execz .La5_LW4_R0
s_lshl2_add_u32 s92, s2, s3
s_cmp_lg_u32 s92, 992
s_nop 0
s_nop 0
s_cbranch_scc1 .La5_LT_E1
buffer_store_dwordx2 v[252:253], off, s[64:67], 0 sc1
.La5_LT_E1:
s_branch .La5_LEND
; ---- fallback: the earlier 32 rows against all their keys with the true running maximum (online softmax)
.La5_LA_SLOW0:
s_waitcnt lgkmcnt(0)
s_nop 7
s_nop 7
v_accvgpr_write_b32 a160, v192
v_accvgpr_write_b32 a161, v193
v_accvgpr_write_b32 a162, v194
v_accvgpr_write_b32 a163, v195
v_accvgpr_write_b32 a164, v196
v_accvgpr_write_b32 a165, v197
v_accvgpr_write_b32 a166, v198
v_accvgpr_write_b32 a167, v199
v_accvgpr_write_b32 a168, v200
v_accvgpr_write_b32 a169, v201
v_accvgpr_write_b32 a170, v202
v_accvgpr_write_b32 a171, v203
v_accvgpr_write_b32 a172, v204
v_accvgpr_write_b32 a173, v205
v_accvgpr_write_b32 a174, v206
v_accvgpr_write_b32 a175, v207
v_accvgpr_write_b32 a176, v208
v_accvgpr_write_b32 a177, v209
v_accvgpr_write_b32 a178, v210
v_accvgpr_write_b32 a179, v211
v_accvgpr_write_b32 a180, v212
v_accvgpr_write_b32 a181, v213
v_accvgpr_write_b32 a182, v214
v_accvgpr_write_b32 a183, v215
v_accvgpr_write_b32 a184, v216
v_accvgpr_write_b32 a185, v217
v_accvgpr_write_b32 a186, v218
v_accvgpr_write_b32 a187, v219
v_accvgpr_write_b32 a188, v220
v_accvgpr_write_b32 a189, v221
v_accvgpr_write_b32 a190, v222
v_accvgpr_write_b32 a191, v223
s_lshl_b32 s96, s72, 19
s_add_u32 s96, s96, 20971520
s_add_u32 s92, s26, s96
s_addc_u32 s93, s27, 0
s_and_b32 s93, s93, 0xffff
s_mov_b32 s94, 17301504
s_mov_b32 s95, 0x20000
v_mov_b32 v0, 0
v_mov_b32 v1, 0
v_mov_b32 v2, 0
v_mov_b32 v3, 0
v_mov_b32 v4, 0
v_mov_b32 v5, 0
v_mov_b32 v6, 0
v_mov_b32 v7, 0
v_mov_b32 v8, 0
v_mov_b32 v9, 0
v_mov_b32 v10, 0
v_mov_b32 v11, 0
v_mov_b32 v12, 0
v_mov_b32 v13, 0
v_mov_b32 v14, 0
v_mov_b32 v15, 0
v_mov_b32 v16, 0
v_mov_b32 v17, 0
v_mov_b32 v18, 0
v_mov_b32 v19, 0
v_mov_b32 v20, 0
v_mov_b32 v21, 0
v_mov_b32 v22, 0
v_mov_b32 v23, 0
v_mov_b32 v24, 0
v_mov_b32 v25, 0
v_mov_b32 v26, 0
v_mov_b32 v27, 0
v_mov_b32 v28, 0
v_mov_b32 v29, 0
v_mov_b32 v30, 0
v_mov_b32 v31, 0
v_mov_b32 v32, 0
v_mov_b32 v33, 0
v_mov_b32 v34, 0
v_mov_b32 v35, 0
v_mov_b32 v36, 0
v_mov_b32 v37, 0
v_mov_b32 v38, 0
v_mov_b32 v39, 0
v_mov_b32 v40, 0
v_mov_b32 v41, 0
v_mov_b32 v42, 0
v_mov_b32 v43, 0
v_mov_b32 v44, 0
v_mov_b32 v45, 0
v_mov_b32 v46, 0
v_mov_b32 v47, 0
v_mov_b32 v48, 0
v_mov_b32 v49, 0
v_mov_b32 v50, 0
v_mov_b32 v51, 0
v_mov_b32 v52, 0
v_mov_b32 v53, 0
v_mov_b32 v54, 0
v_mov_b32 v55, 0
v_mov_b32 v56, 0
v_mov_b32 v57, 0
v_mov_b32 v58, 0
v_mov_b32 v59, 0
v_mov_b32 v60, 0
v_mov_b32 v61, 0
v_mov_b32 v62, 0
v_mov_b32 v63, 0
v_mov_b32 v216, 0xff61b1e6
v_mov_b32 v217, 0xff61b1e6
v_mov_b32 v218, 0
v_mov_b32 v219, 0
s_mov_b32 s7, 0
.La5_LA_SLOW0_L:
s_lshl_b32 s96, s7, 13
s_add_u32 s97, s96, 4096
buffer_load_dwordx4 a[128:131], v255, s[92:95], s96 offen offset:0 sc1
buffer_load_dwordx4 a[132:135], v255, s[92:95], s96 offen offset:1024 sc1
buffer_load_dwordx4 a[136:139], v255, s[92:95], s96 offen offset:2048 sc1
buffer_load_dwordx4 a[140:143], v255, s[92:95], s96 offen offset:3072 sc1
buffer_load_dwordx4 a[144:147], v255, s[92:95], s97 offen offset:0 sc1
buffer_load_dwordx4 a[148:151], v255, s[92:95], s97 offen offset:1024 sc1
buffer_load_dwordx4 a[152:155], v255, s[92:95], s97 offen offset:2048 sc1
buffer_load_dwordx4 a[156:159], v255, s[92:95], s97 offen offset:3072 sc1
s_lshl_b32 s96, s76, 3
s_sub_u32 s96, s96, s7
s_add_u32 s96, s96, s3
s_lshl_b32 s96, s96, 5
v_bfe_u32 v220, v255, 4, 4
v_lshrrev_b32 v221, 8, v255
v_lshlrev_b32 v221, 2, v221
v_sub_u32 v220, v220, v221
v_add_u32 v220, s96, v220
v_mov_b32 v222, 0xceabfb8f
s_waitcnt vmcnt(0)
v_mfma_f32_16x16x32_bf16 v[192:195], a[128:131], v[128:131], 0
v_mfma_f32_16x16x32_bf16 v[192:195], a[132:135], v[132:135], v[192:195]
v_mfma_f32_16x16x32_bf16 v[192:195], a[136:139], v[136:139], v[192:195]
v_mfma_f32_16x16x32_bf16 v[192:195], a[140:143], v[140:143], v[192:195]
v_mfma_f32_16x16x32_bf16 v[196:199], a[144:147], v[128:131], 0
v_mfma_f32_16x16x32_bf16 v[196:199], a[148:151], v[132:135], v[196:199]
v_mfma_f32_16x16x32_bf16 v[196:199], a[152:155], v[136:139], v[196:199]
v_mfma_f32_16x16x32_bf16 v[196:199], a[156:159], v[140:143], v[196:199]
v_mfma_f32_16x16x32_bf16 v[200:203], a[128:131], v[144:147], 0
v_mfma_f32_16x16x32_bf16 v[200:203], a[132:135], v[148:151], v[200:203]
v_mfma_f32_16x16x32_bf16 v[200:203], a[136:139], v[152:155], v[200:203]
v_mfma_f32_16x16x32_bf16 v[200:203], a[140:143], v[156:159], v[200:203]
v_mfma_f32_16x16x32_bf16 v[204:207], a[144:147], v[144:147], 0
v_mfma_f32_16x16x32_bf16 v[204:207], a[148:151], v[148:151], v[204:207]
v_mfma_f32_16x16x32_bf16 v[204:207], a[152:155], v[152:155], v[204:207]
v_mfma_f32_16x16x32_bf16 v[204:207], a[156:159], v[156:159], v[204:207]
s_nop 7
s_nop 7
s_lshl_b32 s96, s7, 13
s_add_u32 s96, s96, 16777216
s_add_u32 s97, s96, 4096
buffer_load_dwordx4 a[128:131], v255, s[92:95], s96 offen offset:0 sc1
buffer_load_dwordx4 a[132:135], v255, s[92:95], s96 offen offset:1024 sc1
buffer_load_dwordx4 a[136:139], v255, s[92:95], s96 offen offset:2048 sc1
buffer_load_dwordx4 a[140:143], v255, s[92:95], s96 offen offset:3072 sc1
buffer_load_dwordx4 a[144:147], v255, s[92:95], s97 offen offset:0 sc1
buffer_load_dwordx4 a[148:151], v255, s[92:95], s97 offen offset:1024 sc1
buffer_load_dwordx4 a[152:155], v255, s[92:95], s97 offen offset:2048 sc1
buffer_load_dwordx4 a[156:159], v255, s[92:95], s97 offen offset:3072 sc1
v_cmp_gt_i32 vcc, 0, v220
s_nop 1
v_cndmask_b32 v192, v192, v222, vcc
v_cmp_gt_i32 vcc, 1, v220
s_nop 1
v_cndmask_b32 v193, v193, v222, vcc
v_cmp_gt_i32 vcc, 2, v220
s_nop 1
v_cndmask_b32 v194, v194, v222, vcc
v_cmp_gt_i32 vcc, 3, v220
s_nop 1
v_cndmask_b32 v195, v195, v222, vcc
v_cmp_gt_i32 vcc, 16, v220
s_nop 1
v_cndmask_b32 v196, v196, v222, vcc
v_cmp_gt_i32 vcc, 17, v220
s_nop 1
v_cndmask_b32 v197, v197, v222, vcc
v_cmp_gt_i32 vcc, 18, v220
s_nop 1
v_cndmask_b32 v198, v198, v222, vcc
v_cmp_gt_i32 vcc, 19, v220
s_nop 1
v_cndmask_b32 v199, v199, v222, vcc
v_cmp_gt_i32 vcc, -16, v220
s_nop 1
v_cndmask_b32 v200, v200, v222, vcc
v_cmp_gt_i32 vcc, -15, v220
s_nop 1
v_cndmask_b32 v201, v201, v222, vcc
v_cmp_gt_i32 vcc, -14, v220
s_nop 1
v_cndmask_b32 v202, v202, v222, vcc
v_cmp_gt_i32 vcc, -13, v220
s_nop 1
v_cndmask_b32 v203, v203, v222, vcc
v_cmp_gt_i32 vcc, 0, v220
s_nop 1
v_cndmask_b32 v204, v204, v222, vcc
v_cmp_gt_i32 vcc, 1, v220
s_nop 1
v_cndmask_b32 v205, v205, v222, vcc
v_cmp_gt_i32 vcc, 2, v220
s_nop 1
v_cndmask_b32 v206, v206, v222, vcc
v_cmp_gt_i32 vcc, 3, v220
s_nop 1
v_cndmask_b32 v207, v207, v222, vcc
v_max3_f32 v222, v192, v193, v216
v_max3_f32 v222, v194, v195, v222
v_max3_f32 v222, v196, v197, v222
v_max3_f32 v222, v198, v199, v222
v_mov_b32 v221, v222
s_nop 1
v_permlane16_swap_b32 v221, v222
s_nop 1
v_max_f32 v222, v222, v221
v_mov_b32 v221, v222
s_nop 1
v_permlane32_swap_b32 v221, v222
s_nop 1
v_max_f32 v222, v222, v221
v_sub_f32 v223, v216, v222
v_mov_b32 v216, v222
v_exp_f32 v223, v223
v_sub_f32 v192, v192, v222
v_sub_f32 v193, v193, v222
v_sub_f32 v194, v194, v222
v_sub_f32 v195, v195, v222
v_sub_f32 v196, v196, v222
v_sub_f32 v197, v197, v222
v_sub_f32 v198, v198, v222
v_sub_f32 v199, v199, v222
v_exp_f32 v192, v192
v_exp_f32 v193, v193
v_exp_f32 v194, v194
v_exp_f32 v195, v195
v_exp_f32 v196, v196
v_exp_f32 v197, v197
v_exp_f32 v198, v198
v_exp_f32 v199, v199
v_mul_f32 v218, v218, v223
v_add_f32 v218, v218, v192
v_add_f32 v218, v218, v193
v_add_f32 v218, v218, v194
v_add_f32 v218, v218, v195
v_add_f32 v218, v218, v196
v_add_f32 v218, v218, v197
v_add_f32 v218, v218, v198
v_add_f32 v218, v218, v199
v_cvt_pk_bf16_f32 v208, v192, v193
v_cvt_pk_bf16_f32 v209, v194, v195
v_cvt_pk_bf16_f32 v210, v196, v197
v_cvt_pk_bf16_f32 v211, v198, v199
v_mul_f32 v0, v0, v223
v_mul_f32 v1, v1, v223
v_mul_f32 v2, v2, v223
v_mul_f32 v3, v3, v223
v_mul_f32 v4, v4, v223
v_mul_f32 v5, v5, v223
v_mul_f32 v6, v6, v223
v_mul_f32 v7, v7, v223
v_mul_f32 v8, v8, v223
v_mul_f32 v9, v9, v223
v_mul_f32 v10, v10, v223
v_mul_f32 v11, v11, v223
v_mul_f32 v12, v12, v223
v_mul_f32 v13, v13, v223
v_mul_f32 v14, v14, v223
v_mul_f32 v15, v15, v223
v_mul_f32 v16, v16, v223
v_mul_f32 v17, v17, v223
v_mul_f32 v18, v18, v223
v_mul_f32 v19, v19, v223
v_mul_f32 v20, v20, v223
v_mul_f32 v21, v21, v223
v_mul_f32 v22, v22, v223
v_mul_f32 v23, v23, v223
v_mul_f32 v24, v24, v223
v_mul_f32 v25, v25, v223
v_mul_f32 v26, v26, v223
v_mul_f32 v27, v27, v223
v_mul_f32 v28, v28, v223
v_mul_f32 v29, v29, v223
v_mul_f32 v30, v30, v223
v_mul_f32 v31, v31, v223
v_max3_f32 v222, v200, v201, v217
v_max3_f32 v222, v202, v203, v222
v_max3_f32 v222, v204, v205, v222
v_max3_f32 v222, v206, v207, v222
v_mov_b32 v221, v222
s_nop 1
v_permlane16_swap_b32 v221, v222
s_nop 1
v_max_f32 v222, v222, v221
v_mov_b32 v221, v222
s_nop 1
v_permlane32_swap_b32 v221, v222
s_nop 1
v_max_f32 v222, v222, v221
v_sub_f32 v223, v217, v222
v_mov_b32 v217, v222
v_exp_f32 v223, v223
v_sub_f32 v200, v200, v222
v_sub_f32 v201, v201, v222
v_sub_f32 v202, v202, v222
v_sub_f32 v203, v203, v222
v_sub_f32 v204, v204, v222
v_sub_f32 v205, v205, v222
v_sub_f32 v206, v206, v222
v_sub_f32 v207, v207, v222
v_exp_f32 v200, v200
v_exp_f32 v201, v201
v_exp_f32 v202, v202
v_exp_f32 v203, v203
v_exp_f32 v204, v204
v_exp_f32 v205, v205
v_exp_f32 v206, v206
v_exp_f32 v207, v207
v_mul_f32 v219, v219, v223
v_add_f32 v219, v219, v200
v_add_f32 v219, v219, v201
v_add_f32 v219, v219, v202
v_add_f32 v219, v219, v203
v_add_f32 v219, v219, v204
v_add_f32 v219, v219, v205
v_add_f32 v219, v219, v206
v_add_f32 v219, v219, v207
v_cvt_pk_bf16_f32 v212, v200, v201
v_cvt_pk_bf16_f32 v213, v202, v203
v_cvt_pk_bf16_f32 v214, v204, v205
v_cvt_pk_bf16_f32 v215, v206, v207
v_mul_f32 v32, v32, v223
v_mul_f32 v33, v33, v223
v_mul_f32 v34, v34, v223
v_mul_f32 v35, v35, v223
v_mul_f32 v36, v36, v223
v_mul_f32 v37, v37, v223
v_mul_f32 v38, v38, v223
v_mul_f32 v39, v39, v223
v_mul_f32 v40, v40, v223
v_mul_f32 v41, v41, v223
v_mul_f32 v42, v42, v223
v_mul_f32 v43, v43, v223
v_mul_f32 v44, v44, v223
v_mul_f32 v45, v45, v223
v_mul_f32 v46, v46, v223
v_mul_f32 v47, v47, v223
v_mul_f32 v48, v48, v223
v_mul_f32 v49, v49, v223
v_mul_f32 v50, v50, v223
v_mul_f32 v51, v51, v223
v_mul_f32 v52, v52, v223
v_mul_f32 v53, v53, v223
v_mul_f32 v54, v54, v223
v_mul_f32 v55, v55, v223
v_mul_f32 v56, v56, v223
v_mul_f32 v57, v57, v223
v_mul_f32 v58, v58, v223
v_mul_f32 v59, v59, v223
v_mul_f32 v60, v60, v223
v_mul_f32 v61, v61, v223
v_mul_f32 v62, v62, v223
v_mul_f32 v63, v63, v223
s_waitcnt vmcnt(0)
v_mfma_f32_16x16x32_bf16 v[0:3], a[128:131], v[208:211], v[0:3]
v_mfma_f32_16x16x32_bf16 v[32:35], a[128:131], v[212:215], v[32:35]
v_mfma_f32_16x16x32_bf16 v[4:7], a[132:135], v[208:211], v[4:7]
v_mfma_f32_16x16x32_bf16 v[36:39], a[132:135], v[212:215], v[36:39]
v_mfma_f32_16x16x32_bf16 v[8:11], a[136:139], v[208:211], v[8:11]
v_mfma_f32_16x16x32_bf16 v[40:43], a[136:139], v[212:215], v[40:43]
v_mfma_f32_16x16x32_bf16 v[12:15], a[140:143], v[208:211], v[12:15]
v_mfma_f32_16x16x32_bf16 v[44:47], a[140:143], v[212:215], v[44:47]
v_mfma_f32_16x16x32_bf16 v[16:19], a[144:147], v[208:211], v[16:19]
v_mfma_f32_16x16x32_bf16 v[48:51], a[144:147], v[212:215], v[48:51]
v_mfma_f32_16x16x32_bf16 v[20:23], a[148:151], v[208:211], v[20:23]
v_mfma_f32_16x16x32_bf16 v[52:55], a[148:151], v[212:215], v[52:55]
v_mfma_f32_16x16x32_bf16 v[24:27], a[152:155], v[208:211], v[24:27]
v_mfma_f32_16x16x32_bf16 v[56:59], a[152:155], v[212:215], v[56:59]
v_mfma_f32_16x16x32_bf16 v[28:31], a[156:159], v[208:211], v[28:31]
v_mfma_f32_16x16x32_bf16 v[60:63], a[156:159], v[212:215], v[60:63]
s_nop 7
s_nop 7
s_add_u32 s7, s7, 1
s_lshr_b32 s96, s7, 3
s_cmp_le_u32 s96, s76
s_cbranch_scc1 .La5_LA_SLOW0_L
v_mov_b32 v221, v218
s_nop 1
v_permlane16_swap_b32 v221, v218
s_nop 1
v_add_f32 v218, v218, v221
v_mov_b32 v221, v218
s_nop 1
v_permlane32_swap_b32 v221, v218
s_nop 1
v_add_f32 v218, v218, v221
v_mov_b32 v221, v219
s_nop 1
v_permlane16_swap_b32 v221, v219
s_nop 1
v_add_f32 v219, v219, v221
v_mov_b32 v221, v219
s_nop 1
v_permlane32_swap_b32 v221, v219
s_nop 1
v_add_f32 v219, v219, v221
v_rcp_f32 v242, v218
v_rcp_f32 v243, v219
v_accvgpr_read_b32 v192, a160
v_accvgpr_read_b32 v193, a161
v_accvgpr_read_b32 v194, a162
v_accvgpr_read_b32 v195, a163
v_accvgpr_read_b32 v196, a164
v_accvgpr_read_b32 v197, a165
v_accvgpr_read_b32 v198, a166
v_accvgpr_read_b32 v199, a167
v_accvgpr_read_b32 v200, a168
v_accvgpr_read_b32 v201, a169
v_accvgpr_read_b32 v202, a170
v_accvgpr_read_b32 v203, a171
v_accvgpr_read_b32 v204, a172
v_accvgpr_read_b32 v205, a173
v_accvgpr_read_b32 v206, a174
v_accvgpr_read_b32 v207, a175
v_accvgpr_read_b32 v208, a176
v_accvgpr_read_b32 v209, a177
v_accvgpr_read_b32 v210, a178
v_accvgpr_read_b32 v211, a179
v_accvgpr_read_b32 v212, a180
v_accvgpr_read_b32 v213, a181
v_accvgpr_read_b32 v214, a182
v_accvgpr_read_b32 v215, a183
v_accvgpr_read_b32 v216, a184
v_accvgpr_read_b32 v217, a185
v_accvgpr_read_b32 v218, a186
v_accvgpr_read_b32 v219, a187
v_accvgpr_read_b32 v220, a188
v_accvgpr_read_b32 v221, a189
v_accvgpr_read_b32 v222, a190
v_accvgpr_read_b32 v223, a191
s_mov_b32 s95, 4096
s_mov_b32 s96, 8192
s_mov_b32 s97, 12288
s_mov_b64 vcc, 0
s_nop 1
s_setpc_b64 s[24:25]
; ---- fallback: the later 32 rows
.La5_LA_SLOW1:
s_waitcnt lgkmcnt(0)
s_nop 7
s_nop 7
v_accvgpr_write_b32 a160, v192
v_accvgpr_write_b32 a161, v193
v_accvgpr_write_b32 a162, v194
v_accvgpr_write_b32 a163, v195
v_accvgpr_write_b32 a164, v196
v_accvgpr_write_b32 a165, v197
v_accvgpr_write_b32 a166, v198
v_accvgpr_write_b32 a167, v199
v_accvgpr_write_b32 a168, v200
v_accvgpr_write_b32 a169, v201
v_accvgpr_write_b32 a170, v202
v_accvgpr_write_b32 a171, v203
v_accvgpr_write_b32 a172, v204
v_accvgpr_write_b32 a173, v205
v_accvgpr_write_b32 a174, v206
v_accvgpr_write_b32 a175, v207
v_accvgpr_write_b32 a176, v208
v_accvgpr_write_b32 a177, v209
v_accvgpr_write_b32 a178, v210
v_accvgpr_write_b32 a179, v211
v_accvgpr_write_b32 a180, v212
v_accvgpr_write_b32 a181, v213
v_accvgpr_write_b32 a182, v214
v_accvgpr_write_b32 a183, v215
v_accvgpr_write_b32 a184, v216
v_accvgpr_write_b32 a185, v217
v_accvgpr_write_b32 a186, v218
v_accvgpr_write_b32 a187, v219
v_accvgpr_write_b32 a188, v220
v_accvgpr_write_b32 a189, v221
v_accvgpr_write_b32 a190, v222
v_accvgpr_write_b32 a191, v223
s_lshl_b32 s96, s72, 19
s_add_u32 s96, s96, 20971520
s_add_u32 s92, s26, s96
s_addc_u32 s93, s27, 0
s_and_b32 s93, s93, 0xffff
s_mov_b32 s94, 17301504
s_mov_b32 s95, 0x20000
v_mov_b32 v64, 0
v_mov_b32 v65, 0
v_mov_b32 v66, 0
v_mov_b32 v67, 0
v_mov_b32 v68, 0
v_mov_b32 v69, 0
v_mov_b32 v70, 0
v_mov_b32 v71, 0
v_mov_b32 v72, 0
v_mov_b32 v73, 0
v_mov_b32 v74, 0
v_mov_b32 v75, 0
v_mov_b32 v76, 0
v_mov_b32 v77, 0
v_mov_b32 v78, 0
v_mov_b32 v79, 0
v_mov_b32 v80, 0
v_mov_b32 v81, 0
v_mov_b32 v82, 0
v_mov_b32 v83, 0
v_mov_b32 v84, 0
v_mov_b32 v85, 0
v_mov_b32 v86, 0
v_mov_b32 v87, 0
v_mov_b32 v88, 0
v_mov_b32 v89, 0
v_mov_b32 v90, 0
v_mov_b32 v91, 0
v_mov_b32 v92, 0
v_mov_b32 v93, 0
v_mov_b32 v94, 0
v_mov_b32 v95, 0
v_mov_b32 v96, 0
v_mov_b32 v97, 0
v_mov_b32 v98, 0
v_mov_b32 v99, 0
v_mov_b32 v100, 0
v_mov_b32 v101, 0
v_mov_b32 v102, 0
v_mov_b32 v103, 0
v_mov_b32 v104, 0
v_mov_b32 v105, 0
v_mov_b32 v106, 0
v_mov_b32 v107, 0
v_mov_b32 v108, 0
v_mov_b32 v109, 0
v_mov_b32 v110, 0
v_mov_b32 v111, 0
v_mov_b32 v112, 0
v_mov_b32 v113, 0
v_mov_b32 v114, 0
v_mov_b32 v115, 0
v_mov_b32 v116, 0
v_mov_b32 v117, 0
v_mov_b32 v118, 0
v_mov_b32 v119, 0
v_mov_b32 v120, 0
v_mov_b32 v121, 0
v_mov_b32 v122, 0
v_mov_b32 v123, 0
v_mov_b32 v124, 0
v_mov_b32 v125, 0
v_mov_b32 v126, 0
v_mov_b32 v127, 0
v_mov_b32 v216, 0xff61b1e6
v_mov_b32 v217, 0xff61b1e6
v_mov_b32 v218, 0
v_mov_b32 v219, 0
s_mov_b32 s7, 0
.La5_LA_SLOW1_L:
s_lshl_b32 s96, s7, 13
s_add_u32 s97, s96, 4096
buffer_load_dwordx4 a[128:131], v255, s[92:95], s96 offen offset:0 sc1
buffer_load_dwordx4 a[132:135], v255, s[92:95], s96 offen offset:1024 sc1
buffer_load_dwordx4 a[136:139], v255, s[92:95], s96 offen offset:2048 sc1
buffer_load_dwordx4 a[140:143], v255, s[92:95], s96 offen offset:3072 sc1
buffer_load_dwordx4 a[144:147], v255, s[92:95], s97 offen offset:0 sc1
buffer_load_dwordx4 a[148:151], v255, s[92:95], s97 offen offset:1024 sc1
buffer_load_dwordx4 a[152:155], v255, s[92:95], s97 offen offset:2048 sc1
buffer_load_dwordx4 a[156:159], v255, s[92:95], s97 offen offset:3072 sc1
s_lshl_b32 s96, s76, 3
s_sub_u32 s96, s96, s7
s_sub_u32 s96, s96, s3
s_add_u32 s96, s96, 7
s_lshl_b32 s96, s96, 5
v_bfe_u32 v220, v255, 4, 4
v_lshrrev_b32 v221, 8, v255
v_lshlrev_b32 v221, 2, v221
v_sub_u32 v220, v220, v221
v_add_u32 v220, s96, v220
v_mov_b32 v222, 0xceabfb8f
s_waitcnt vmcnt(0)
v_mfma_f32_16x16x32_bf16 v[192:195], a[128:131], v[160:163], 0
v_mfma_f32_16x16x32_bf16 v[192:195], a[132:135], v[164:167], v[192:195]
v_mfma_f32_16x16x32_bf16 v[192:195], a[136:139], v[168:171], v[192:195]
v_mfma_f32_16x16x32_bf16 v[192:195], a[140:143], v[172:175], v[192:195]
v_mfma_f32_16x16x32_bf16 v[196:199], a[144:147], v[160:163], 0
v_mfma_f32_16x16x32_bf16 v[196:199], a[148:151], v[164:167], v[196:199]
v_mfma_f32_16x16x32_bf16 v[196:199], a[152:155], v[168:171], v[196:199]
v_mfma_f32_16x16x32_bf16 v[196:199], a[156:159], v[172:175], v[196:199]
v_mfma_f32_16x16x32_bf16 v[200:203], a[128:131], v[176:179], 0
v_mfma_f32_16x16x32_bf16 v[200:203], a[132:135], v[180:183], v[200:203]
v_mfma_f32_16x16x32_bf16 v[200:203], a[136:139], v[184:187], v[200:203]
v_mfma_f32_16x16x32_bf16 v[200:203], a[140:143], v[188:191], v[200:203]
v_mfma_f32_16x16x32_bf16 v[204:207], a[144:147], v[176:179], 0
v_mfma_f32_16x16x32_bf16 v[204:207], a[148:151], v[180:183], v[204:207]
v_mfma_f32_16x16x32_bf16 v[204:207], a[152:155], v[184:187], v[204:207]
v_mfma_f32_16x16x32_bf16 v[204:207], a[156:159], v[188:191], v[204:207]
s_nop 7
s_nop 7
s_lshl_b32 s96, s7, 13
s_add_u32 s96, s96, 16777216
s_add_u32 s97, s96, 4096
buffer_load_dwordx4 a[128:131], v255, s[92:95], s96 offen offset:0 sc1
buffer_load_dwordx4 a[132:135], v255, s[92:95], s96 offen offset:1024 sc1
buffer_load_dwordx4 a[136:139], v255, s[92:95], s96 offen offset:2048 sc1
buffer_load_dwordx4 a[140:143], v255, s[92:95], s96 offen offset:3072 sc1
buffer_load_dwordx4 a[144:147], v255, s[92:95], s97 offen offset:0 sc1
buffer_load_dwordx4 a[148:151], v255, s[92:95], s97 offen offset:1024 sc1
buffer_load_dwordx4 a[152:155], v255, s[92:95], s97 offen offset:2048 sc1
buffer_load_dwordx4 a[156:159], v255, s[92:95], s97 offen offset:3072 sc1
v_cmp_gt_i32 vcc, 0, v220
s_nop 1
v_cndmask_b32 v192, v192, v222, vcc
v_cmp_gt_i32 vcc, 1, v220
s_nop 1
v_cndmask_b32 v193, v193, v222, vcc
v_cmp_gt_i32 vcc, 2, v220
s_nop 1
v_cndmask_b32 v194, v194, v222, vcc
v_cmp_gt_i32 vcc, 3, v220
s_nop 1
v_cndmask_b32 v195, v195, v222, vcc
v_cmp_gt_i32 vcc, 16, v220
s_nop 1
v_cndmask_b32 v196, v196, v222, vcc
v_cmp_gt_i32 vcc, 17, v220
s_nop 1
v_cndmask_b32 v197, v197, v222, vcc
v_cmp_gt_i32 vcc, 18, v220
s_nop 1
v_cndmask_b32 v198, v198, v222, vcc
v_cmp_gt_i32 vcc, 19, v220
s_nop 1
v_cndmask_b32 v199, v199, v222, vcc
v_cmp_gt_i32 vcc, -16, v220
s_nop 1
v_cndmask_b32 v200, v200, v222, vcc
v_cmp_gt_i32 vcc, -15, v220
s_nop 1
v_cndmask_b32 v201, v201, v222, vcc
v_cmp_gt_i32 vcc, -14, v220
s_nop 1
v_cndmask_b32 v202, v202, v222, vcc
v_cmp_gt_i32 vcc, -13, v220
s_nop 1
v_cndmask_b32 v203, v203, v222, vcc
v_cmp_gt_i32 vcc, 0, v220
s_nop 1
v_cndmask_b32 v204, v204, v222, vcc
v_cmp_gt_i32 vcc, 1, v220
s_nop 1
v_cndmask_b32 v205, v205, v222, vcc
v_cmp_gt_i32 vcc, 2, v220
s_nop 1
v_cndmask_b32 v206, v206, v222, vcc
v_cmp_gt_i32 vcc, 3, v220
s_nop 1
v_cndmask_b32 v207, v207, v222, vcc
v_max3_f32 v222, v192, v193, v216
v_max3_f32 v222, v194, v195, v222
v_max3_f32 v222, v196, v197, v222
v_max3_f32 v222, v198, v199, v222
v_mov_b32 v221, v222
s_nop 1
v_permlane16_swap_b32 v221, v222
s_nop 1
v_max_f32 v222, v222, v221
v_mov_b32 v221, v222
s_nop 1
v_permlane32_swap_b32 v221, v222
s_nop 1
v_max_f32 v222, v222, v221
v_sub_f32 v223, v216, v222
v_mov_b32 v216, v222
v_exp_f32 v223, v223
v_sub_f32 v192, v192, v222
v_sub_f32 v193, v193, v222
v_sub_f32 v194, v194, v222
v_sub_f32 v195, v195, v222
v_sub_f32 v196, v196, v222
v_sub_f32 v197, v197, v222
v_sub_f32 v198, v198, v222
v_sub_f32 v199, v199, v222
v_exp_f32 v192, v192
v_exp_f32 v193, v193
v_exp_f32 v194, v194
v_exp_f32 v195, v195
v_exp_f32 v196, v196
v_exp_f32 v197, v197
v_exp_f32 v198, v198
v_exp_f32 v199, v199
v_mul_f32 v218, v218, v223
v_add_f32 v218, v218, v192
v_add_f32 v218, v218, v193
v_add_f32 v218, v218, v194
v_add_f32 v218, v218, v195
v_add_f32 v218, v218, v196
v_add_f32 v218, v218, v197
v_add_f32 v218, v218, v198
v_add_f32 v218, v218, v199
v_cvt_pk_bf16_f32 v208, v192, v193
v_cvt_pk_bf16_f32 v209, v194, v195
v_cvt_pk_bf16_f32 v210, v196, v197
v_cvt_pk_bf16_f32 v211, v198, v199
v_mul_f32 v64, v64, v223
v_mul_f32 v65, v65, v223
v_mul_f32 v66, v66, v223
v_mul_f32 v67, v67, v223
v_mul_f32 v68, v68, v223
v_mul_f32 v69, v69, v223
v_mul_f32 v70, v70, v223
v_mul_f32 v71, v71, v223
v_mul_f32 v72, v72, v223
v_mul_f32 v73, v73, v223
v_mul_f32 v74, v74, v223
v_mul_f32 v75, v75, v223
v_mul_f32 v76, v76, v223
v_mul_f32 v77, v77, v223
v_mul_f32 v78, v78, v223
v_mul_f32 v79, v79, v223
v_mul_f32 v80, v80, v223
v_mul_f32 v81, v81, v223
v_mul_f32 v82, v82, v223
v_mul_f32 v83, v83, v223
v_mul_f32 v84, v84, v223
v_mul_f32 v85, v85, v223
v_mul_f32 v86, v86, v223
v_mul_f32 v87, v87, v223
v_mul_f32 v88, v88, v223
v_mul_f32 v89, v89, v223
v_mul_f32 v90, v90, v223
v_mul_f32 v91, v91, v223
v_mul_f32 v92, v92, v223
v_mul_f32 v93, v93, v223
v_mul_f32 v94, v94, v223
v_mul_f32 v95, v95, v223
v_max3_f32 v222, v200, v201, v217
v_max3_f32 v222, v202, v203, v222
v_max3_f32 v222, v204, v205, v222
v_max3_f32 v222, v206, v207, v222
v_mov_b32 v221, v222
s_nop 1
v_permlane16_swap_b32 v221, v222
s_nop 1
v_max_f32 v222, v222, v221
v_mov_b32 v221, v222
s_nop 1
v_permlane32_swap_b32 v221, v222
s_nop 1
v_max_f32 v222, v222, v221
v_sub_f32 v223, v217, v222
v_mov_b32 v217, v222
v_exp_f32 v223, v223
v_sub_f32 v200, v200, v222
v_sub_f32 v201, v201, v222
v_sub_f32 v202, v202, v222
v_sub_f32 v203, v203, v222
v_sub_f32 v204, v204, v222
v_sub_f32 v205, v205, v222
v_sub_f32 v206, v206, v222
v_sub_f32 v207, v207, v222
v_exp_f32 v200, v200
v_exp_f32 v201, v201
v_exp_f32 v202, v202
v_exp_f32 v203, v203
v_exp_f32 v204, v204
v_exp_f32 v205, v205
v_exp_f32 v206, v206
v_exp_f32 v207, v207
v_mul_f32 v219, v219, v223
v_add_f32 v219, v219, v200
v_add_f32 v219, v219, v201
v_add_f32 v219, v219, v202
v_add_f32 v219, v219, v203
v_add_f32 v219, v219, v204
v_add_f32 v219, v219, v205
v_add_f32 v219, v219, v206
v_add_f32 v219, v219, v207
v_cvt_pk_bf16_f32 v212, v200, v201
v_cvt_pk_bf16_f32 v213, v202, v203
v_cvt_pk_bf16_f32 v214, v204, v205
v_cvt_pk_bf16_f32 v215, v206, v207
v_mul_f32 v96, v96, v223
v_mul_f32 v97, v97, v223
v_mul_f32 v98, v98, v223
v_mul_f32 v99, v99, v223
v_mul_f32 v100, v100, v223
v_mul_f32 v101, v101, v223
v_mul_f32 v102, v102, v223
v_mul_f32 v103, v103, v223
v_mul_f32 v104, v104, v223
v_mul_f32 v105, v105, v223
v_mul_f32 v106, v106, v223
v_mul_f32 v107, v107, v223
v_mul_f32 v108, v108, v223
v_mul_f32 v109, v109, v223
v_mul_f32 v110, v110, v223
v_mul_f32 v111, v111, v223
v_mul_f32 v112, v112, v223
v_mul_f32 v113, v113, v223
v_mul_f32 v114, v114, v223
v_mul_f32 v115, v115, v223
v_mul_f32 v116, v116, v223
v_mul_f32 v117, v117, v223
v_mul_f32 v118, v118, v223
v_mul_f32 v119, v119, v223
v_mul_f32 v120, v120, v223
v_mul_f32 v121, v121, v223
v_mul_f32 v122, v122, v223
v_mul_f32 v123, v123, v223
v_mul_f32 v124, v124, v223
v_mul_f32 v125, v125, v223
v_mul_f32 v126, v126, v223
v_mul_f32 v127, v127, v223
s_waitcnt vmcnt(0)
v_mfma_f32_16x16x32_bf16 v[64:67], a[128:131], v[208:211], v[64:67]
v_mfma_f32_16x16x32_bf16 v[96:99], a[128:131], v[212:215], v[96:99]
v_mfma_f32_16x16x32_bf16 v[68:71], a[132:135], v[208:211], v[68:71]
v_mfma_f32_16x16x32_bf16 v[100:103], a[132:135], v[212:215], v[100:103]
v_mfma_f32_16x16x32_bf16 v[72:75], a[136:139], v[208:211], v[72:75]
v_mfma_f32_16x16x32_bf16 v[104:107], a[136:139], v[212:215], v[104:107]
v_mfma_f32_16x16x32_bf16 v[76:79], a[140:143], v[208:211], v[76:79]
v_mfma_f32_16x16x32_bf16 v[108:111], a[140:143], v[212:215], v[108:111]
v_mfma_f32_16x16x32_bf16 v[80:83], a[144:147], v[208:211], v[80:83]
v_mfma_f32_16x16x32_bf16 v[112:115], a[144:147], v[212:215], v[112:115]
v_mfma_f32_16x16x32_bf16 v[84:87], a[148:151], v[208:211], v[84:87]
v_mfma_f32_16x16x32_bf16 v[116:119], a[148:151], v[212:215], v[116:119]
v_mfma_f32_16x16x32_bf16 v[88:91], a[152:155], v[208:211], v[88:91]
v_mfma_f32_16x16x32_bf16 v[120:123], a[152:155], v[212:215], v[120:123]
v_mfma_f32_16x16x32_bf16 v[92:95], a[156:159], v[208:211], v[92:95]
v_mfma_f32_16x16x32_bf16 v[124:127], a[156:159], v[212:215], v[124:127]
s_nop 7
s_nop 7
s_add_u32 s7, s7, 1
s_lshr_b32 s96, s7, 3
s_cmp_le_u32 s96, s76
s_cbranch_scc1 .La5_LA_SLOW1_L
v_mov_b32 v221, v218
s_nop 1
v_permlane16_swap_b32 v221, v218
s_nop 1
v_add_f32 v218, v218, v221
v_mov_b32 v221, v218
s_nop 1
v_permlane32_swap_b32 v221, v218
s_nop 1
v_add_f32 v218, v218, v221
v_mov_b32 v221, v219
s_nop 1
v_permlane16_swap_b32 v221, v219
s_nop 1
v_add_f32 v219, v219, v221
v_mov_b32 v221, v219
s_nop 1
v_permlane32_swap_b32 v221, v219
s_nop 1
v_add_f32 v219, v219, v221
v_rcp_f32 v242, v218
v_rcp_f32 v243, v219
v_accvgpr_read_b32 v192, a160
v_accvgpr_read_b32 v193, a161
v_accvgpr_read_b32 v194, a162
v_accvgpr_read_b32 v195, a163
v_accvgpr_read_b32 v196, a164
v_accvgpr_read_b32 v197, a165
v_accvgpr_read_b32 v198, a166
v_accvgpr_read_b32 v199, a167
v_accvgpr_read_b32 v200, a168
v_accvgpr_read_b32 v201, a169
v_accvgpr_read_b32 v202, a170
v_accvgpr_read_b32 v203, a171
v_accvgpr_read_b32 v204, a172
v_accvgpr_read_b32 v205, a173
v_accvgpr_read_b32 v206, a174
v_accvgpr_read_b32 v207, a175
v_accvgpr_read_b32 v208, a176
v_accvgpr_read_b32 v209, a177
v_accvgpr_read_b32 v210, a178
v_accvgpr_read_b32 v211, a179
v_accvgpr_read_b32 v212, a180
v_accvgpr_read_b32 v213, a181
v_accvgpr_read_b32 v214, a182
v_accvgpr_read_b32 v215, a183
v_accvgpr_read_b32 v216, a184
v_accvgpr_read_b32 v217, a185
v_accvgpr_read_b32 v218, a186
v_accvgpr_read_b32 v219, a187
v_accvgpr_read_b32 v220, a188
v_accvgpr_read_b32 v221, a189
v_accvgpr_read_b32 v222, a190
v_accvgpr_read_b32 v223, a191
s_mov_b32 s95, 4096
s_mov_b32 s96, 8192
s_mov_b32 s97, 12288
s_mov_b64 vcc, 0
s_nop 1
s_setpc_b64 s[24:25]
.La5_LK_XSPIN:
s_mov_b32 s93, 0
.La5_LK_XSPIN_L:
s_add_u32 s93, s93, 1
s_cmp_gt_u32 s93, 2097152
s_cbranch_scc1 .La5_LK_XOK
s_sleep 1
buffer_load_dwordx4 v[72:75], v255, s[64:67], s89 offen offset:1024 sc1
s_waitcnt vmcnt(0)
v_cmp_eq_u64 vcc, v[72:73], v[252:253]
v_cmp_eq_u64 s[96:97], v[74:75], v[252:253]
s_nop 1
s_and_b64 vcc, vcc, s[96:97]
s_cmp_eq_u64 vcc, -1
s_cbranch_scc0 .La5_LK_XSPIN_L
s_branch .La5_LK_XOK
.La5_LV_XSPIN:
s_mov_b32 s93, 0
.La5_LV_XSPIN_L:
s_add_u32 s93, s93, 1
s_cmp_gt_u32 s93, 2097152
s_nop 0
s_nop 0
s_cbranch_scc1 .La5_LV_XOK
s_sleep 1
buffer_load_dwordx4 v[72:75], v255, s[64:67], s89 offen offset:1024 sc1
s_waitcnt vmcnt(0)
v_cmp_eq_u64 vcc, v[72:73], v[252:253]
v_cmp_eq_u64 s[96:97], v[74:75], v[252:253]
s_nop 1
s_and_b64 vcc, vcc, s[96:97]
s_cmp_eq_u64 vcc, -1
s_cbranch_scc0 .La5_LV_XSPIN_L
s_branch .La5_LV_XOK
.La5_LA_SC1:
s_call_b64 s[24:25], .La5_LA_SLOW0
s_branch .La5_LA_SR1
.La5_LA_SC2:
s_nop 0
s_call_b64 s[24:25], .La5_LA_SLOW1
s_branch .La5_LA_SR2
.La5_LA_PS2:
s_mov_b32 s84, 3
s_mov_b32 s70, 17408
s_waitcnt vmcnt(0)
buffer_load_dwordx4 a[220:223], v255, s[68:71], s94 offen sc1
buffer_load_dwordx4 a[0:3], v255, s[68:71], 0 offen offset:0 sc1
buffer_load_dwordx4 a[4:7], v255, s[68:71], 0 offen offset:1024 sc1
buffer_load_dwordx4 a[8:11], v255, s[68:71], 0 offen offset:2048 sc1
buffer_load_dwordx4 a[12:15], v255, s[68:71], 0 offen offset:3072 sc1
buffer_load_dwordx4 a[16:19], v255, s[68:71], s95 offen offset:0 sc1
buffer_load_dwordx4 a[20:23], v255, s[68:71], s95 offen offset:1024 sc1
buffer_load_dwordx4 a[24:27], v255, s[68:71], s95 offen offset:2048 sc1
buffer_load_dwordx4 a[28:31], v255, s[68:71], s95 offen offset:3072 sc1
buffer_load_dwordx4 a[32:35], v255, s[68:71], s96 offen offset:0 sc1
buffer_load_dwordx4 a[36:39], v255, s[68:71], s96 offen offset:1024 sc1
buffer_load_dwordx4 a[40:43], v255, s[68:71], s96 offen offset:2048 sc1
buffer_load_dwordx4 a[44:47], v255, s[68:71], s96 offen offset:3072 sc1
buffer_load_dwordx4 a[48:51], v255, s[68:71], s97 offen offset:0 sc1
buffer_load_dwordx4 a[52:55], v255, s[68:71], s97 offen offset:1024 sc1
buffer_load_dwordx4 a[56:59], v255, s[68:71], s97 offen offset:2048 sc1
buffer_load_dwordx4 a[60:63], v255, s[68:71], s97 offen offset:3072 sc1
s_branch .La5_LA_PS3
.La5_LA_PSPIN:
s_mov_b32 s93, 0
.La5_LA_PSPIN_L:
s_add_u32 s93, s93, 1
s_cmp_gt_u32 s93, 2097152
s_cbranch_scc1 .La5_LA_PSOK
s_sleep 1
buffer_load_dword v250, v251, s[64:67], s20 offen sc1
s_waitcnt vmcnt(0)
v_cmp_eq_u32 vcc, v250, v249
s_nop 1
s_and_b32 s92, vcc_lo, 3
s_cmp_eq_u32 s92, 3
s_nop 0
s_nop 0
s_cbranch_scc0 .La5_LA_PSPIN_L
s_branch .La5_LA_PSOK
.La5_LA_PSOK:
s_mov_b32 s70, 17408
buffer_load_dwordx4 a[220:223], v255, s[68:71], s94 offen sc1
buffer_load_dwordx4 a[0:3], v255, s[68:71], 0 offen offset:0 sc1
buffer_load_dwordx4 a[4:7], v255, s[68:71], 0 offen offset:1024 sc1
buffer_load_dwordx4 a[8:11], v255, s[68:71], 0 offen offset:2048 sc1
buffer_load_dwordx4 a[12:15], v255, s[68:71], 0 offen offset:3072 sc1
buffer_load_dwordx4 a[16:19], v255, s[68:71], s95 offen offset:0 sc1
buffer_load_dwordx4 a[20:23], v255, s[68:71], s95 offen offset:1024 sc1
buffer_load_dwordx4 a[24:27], v255, s[68:71], s95 offen offset:2048 sc1
buffer_load_dwordx4 a[28:31], v255, s[68:71], s95 offen offset:3072 sc1
buffer_load_dwordx4 a[32:35], v255, s[68:71], s96 offen offset:0 sc1
buffer_load_dwordx4 a[36:39], v255, s[68:71], s96 offen offset:1024 sc1
buffer_load_dwordx4 a[40:43], v255, s[68:71], s96 offen offset:2048 sc1
buffer_load_dwordx4 a[44:47], v255, s[68:71], s96 offen offset:3072 sc1
buffer_load_dwordx4 a[48:51], v255, s[68:71], s97 offen offset:0 sc1
buffer_load_dwordx4 a[52:55], v255, s[68:71], s97 offen offset:1024 sc1
buffer_load_dwordx4 a[56:59], v255, s[68:71], s97 offen offset:2048 sc1
buffer_load_dwordx4 a[60:63], v255, s[68:71], s97 offen offset:3072 sc1
s_branch .La5_LA_DC1
.La5_LO_N1:
s_nop 0
.La5_LW2_13:
s_cbranch_execz .La5_LW2_R1
s_mov_b32 s93, 0
.La5_LO_N1_L:
s_add_u32 s93, s93, 1
s_cmp_gt_u32 s93, 2097152
s_cbranch_scc1 .La5_LO_K1
s_sleep 1
buffer_load_dwordx2 v[250:251], v248, s[64:67], s5 offen sc1
s_waitcnt vmcnt(0)
v_cmp_eq_u64 vcc, v[250:251], v[252:253]
s_nop 1
s_bitcmp1_b32 vcc_lo, 1
s_cbranch_scc0 .La5_LO_N1_L
s_branch .La5_LO_K1
.La5_LO_N2:
s_mov_b32 s93, 0
.La5_LO_N2_L:
s_add_u32 s93, s93, 1
s_cmp_gt_u32 s93, 2097152
s_cbranch_scc1 .La5_LO_K2
s_sleep 1
buffer_load_dwordx2 v[250:251], v248, s[64:67], s5 offen sc1
s_waitcnt vmcnt(0)
v_cmp_eq_u64 vcc, v[250:251], v[252:253]
s_nop 1
s_bitcmp1_b32 vcc_lo, 2
s_cbranch_scc0 .La5_LO_N2_L
s_branch .La5_LO_K2
.La5_LO_N3:
s_mov_b32 s93, 0
.La5_LO_N3_L:
s_add_u32 s93, s93, 1
s_cmp_gt_u32 s93, 2097152
s_cbranch_scc1 .La5_LO_K3
s_sleep 1
buffer_load_dwordx2 v[250:251], v248, s[64:67], s5 offen sc1
s_waitcnt vmcnt(0)
v_cmp_eq_u64 vcc, v[250:251], v[252:253]
s_nop 1
s_bitcmp1_b32 vcc_lo, 3
s_cbranch_scc0 .La5_LO_N3_L
s_branch .La5_LO_K3
.La5_LO_N4:
s_mov_b32 s93, 0
.La5_LO_N4_L:
s_add_u32 s93, s93, 1
s_cmp_gt_u32 s93, 2097152
s_nop 0
s_cbranch_scc1 .La5_LO_K4
s_sleep 1
buffer_load_dwordx2 v[250:251], v248, s[64:67], s5 offen sc1
s_waitcnt vmcnt(0)
v_cmp_eq_u64 vcc, v[250:251], v[252:253]
s_nop 1
s_bitcmp1_b32 vcc_lo, 4
s_cbranch_scc0 .La5_LO_N4_L
s_branch .La5_LO_K4
.La5_LO_N5:
s_mov_b32 s93, 0
.La5_LO_N5_L:
s_add_u32 s93, s93, 1
s_cmp_gt_u32 s93, 2097152
s_nop 0
s_nop 0
s_cbranch_scc1 .La5_LO_K5
s_sleep 1
buffer_load_dwordx2 v[250:251], v248, s[64:67], s5 offen sc1
s_waitcnt vmcnt(0)
v_cmp_eq_u64 vcc, v[250:251], v[252:253]
s_nop 1
s_bitcmp1_b32 vcc_lo, 5
s_cbranch_scc0 .La5_LO_N5_L
s_branch .La5_LO_K5
.La5_LO_N6:
s_mov_b32 s93, 0
.La5_LO_N6_L:
s_add_u32 s93, s93, 1
s_cmp_gt_u32 s93, 2097152
s_nop 0
s_nop 0
s_cbranch_scc1 .La5_LO_K6
s_sleep 1
buffer_load_dwordx2 v[250:251], v248, s[64:67], s5 offen sc1
s_waitcnt vmcnt(0)
v_cmp_eq_u64 vcc, v[250:251], v[252:253]
s_nop 1
s_bitcmp1_b32 vcc_lo, 6
s_cbranch_scc0 .La5_LO_N6_L
s_branch .La5_LO_K6
.La5_LO_N7:
s_mov_b32 s93, 0
.La5_LO_N7_L:
s_add_u32 s93, s93, 1
s_cmp_gt_u32 s93, 2097152
s_nop 0
s_nop 0
s_cbranch_scc1 .La5_LO_K7
s_sleep 1
buffer_load_dwordx2 v[250:251], v248, s[64:67], s5 offen sc1
s_waitcnt vmcnt(0)
v_cmp_eq_u64 vcc, v[250:251], v[252:253]
s_nop 1
s_bitcmp1_b32 vcc_lo, 7
s_cbranch_scc0 .La5_LO_N7_L
s_branch .La5_LO_K7
.La5_LA_SC3:
s_call_b64 s[24:25], .La5_LA_SLOW0
v_accvgpr_write_b32 a0, 0
v_accvgpr_write_b32 a1, 0
v_accvgpr_write_b32 a2, 0
v_accvgpr_write_b32 a3, 0
v_accvgpr_write_b32 a4, 0
v_accvgpr_write_b32 a5, 0
v_accvgpr_write_b32 a6, 0
v_accvgpr_write_b32 a7, 0
v_accvgpr_write_b32 a8, 0
v_accvgpr_write_b32 a9, 0
v_accvgpr_write_b32 a10, 0
v_accvgpr_write_b32 a11, 0
v_accvgpr_write_b32 a12, 0
v_accvgpr_write_b32 a13, 0
v_accvgpr_write_b32 a14, 0
v_accvgpr_write_b32 a15, 0
v_accvgpr_write_b32 a16, 0
v_accvgpr_write_b32 a17, 0
v_accvgpr_write_b32 a18, 0
v_accvgpr_write_b32 a19, 0
v_accvgpr_write_b32 a20, 0
v_accvgpr_write_b32 a21, 0
v_accvgpr_write_b32 a22, 0
v_accvgpr_write_b32 a23, 0
v_accvgpr_write_b32 a24, 0
v_accvgpr_write_b32 a25, 0
v_accvgpr_write_b32 a26, 0
v_accvgpr_write_b32 a27, 0
v_accvgpr_write_b32 a28, 0
v_accvgpr_write_b32 a29, 0
v_accvgpr_write_b32 a30, 0
v_accvgpr_write_b32 a31, 0
s_nop 1
v_mov_b32 v250, v242
v_mov_b32 v249, v243
v_xor_b32 v242, 64, v240
v_xor_b32 v243, 96, v240
s_mov_b32 s93, 0
buffer_load_dwordx4 a[128:131], v254, s[40:43], s93 offen
s_mov_b32 s93, 7168
buffer_load_dwordx4 a[132:135], v254, s[40:43], s93 offen
s_mov_b32 s93, 14336
buffer_load_dwordx4 a[136:139], v254, s[40:43], s93 offen
s_mov_b32 s93, 21504
buffer_load_dwordx4 a[140:143], v254, s[40:43], s93 offen
s_mov_b32 s93, 28672
buffer_load_dwordx4 a[144:147], v254, s[40:43], s93 offen
s_mov_b32 s93, 35840
buffer_load_dwordx4 a[148:151], v254, s[40:43], s93 offen
s_mov_b32 s93, 43008
buffer_load_dwordx4 a[152:155], v254, s[40:43], s93 offen
s_mov_b32 s93, 50176
buffer_load_dwordx4 a[156:159], v254, s[40:43], s93 offen
s_mov_b32 s93, 57344
buffer_load_dwordx4 a[160:163], v254, s[40:43], s93 offen
s_mov_b32 s93, 64512
buffer_load_dwordx4 a[164:167], v254, s[40:43], s93 offen
s_mov_b32 s93, 71680
buffer_load_dwordx4 a[168:171], v254, s[40:43], s93 offen
s_mov_b32 s93, 78848
buffer_load_dwordx4 a[172:175], v254, s[40:43], s93 offen
s_mov_b32 s93, 86016
buffer_load_dwordx4 a[176:179], v254, s[40:43], s93 offen
s_mov_b32 s93, 93184
buffer_load_dwordx4 a[180:183], v254, s[40:43], s93 offen
s_mov_b32 s93, 100352
buffer_load_dwordx4 a[184:187], v254, s[40:43], s93 offen
s_mov_b32 s93, 107520
buffer_load_dwordx4 a[188:191], v254, s[40:43], s93 offen
s_branch .La5_LA_SR3
.La5_LA_SC4:
s_call_b64 s[24:25], .La5_LA_SLOW1
v_accvgpr_write_b32 a32, 0
v_accvgpr_write_b32 a33, 0
v_accvgpr_write_b32 a34, 0
v_accvgpr_write_b32 a35, 0
v_accvgpr_write_b32 a36, 0
v_accvgpr_write_b32 a37, 0
v_accvgpr_write_b32 a38, 0
v_accvgpr_write_b32 a39, 0
v_accvgpr_write_b32 a40, 0
v_accvgpr_write_b32 a41, 0
v_accvgpr_write_b32 a42, 0
v_accvgpr_write_b32 a43, 0
v_accvgpr_write_b32 a44, 0
v_accvgpr_write_b32 a45, 0
v_accvgpr_write_b32 a46, 0
v_accvgpr_write_b32 a47, 0
v_accvgpr_write_b32 a48, 0
v_accvgpr_write_b32 a49, 0
v_accvgpr_write_b32 a50, 0
v_accvgpr_write_b32 a51, 0
v_accvgpr_write_b32 a52, 0
v_accvgpr_write_b32 a53, 0
v_accvgpr_write_b32 a54, 0
v_accvgpr_write_b32 a55, 0
v_accvgpr_write_b32 a56, 0
v_accvgpr_write_b32 a57, 0
v_accvgpr_write_b32 a58, 0
v_accvgpr_write_b32 a59, 0
v_accvgpr_write_b32 a60, 0
v_accvgpr_write_b32 a61, 0
v_accvgpr_write_b32 a62, 0
v_accvgpr_write_b32 a63, 0
s_nop 1
v_mov_b32 v250, v242
v_mov_b32 v249, v243
v_xor_b32 v242, 64, v240
v_xor_b32 v243, 96, v240
s_mov_b32 s93, 0
buffer_load_dwordx4 a[128:131], v254, s[40:43], s93 offen
s_mov_b32 s93, 7168
buffer_load_dwordx4 a[132:135], v254, s[40:43], s93 offen
s_mov_b32 s93, 14336
buffer_load_dwordx4 a[136:139], v254, s[40:43], s93 offen
s_mov_b32 s93, 21504
buffer_load_dwordx4 a[140:143], v254, s[40:43], s93 offen
s_mov_b32 s93, 28672
buffer_load_dwordx4 a[144:147], v254, s[40:43], s93 offen
s_mov_b32 s93, 35840
buffer_load_dwordx4 a[148:151], v254, s[40:43], s93 offen
s_mov_b32 s93, 43008
buffer_load_dwordx4 a[152:155], v254, s[40:43], s93 offen
s_mov_b32 s93, 50176
buffer_load_dwordx4 a[156:159], v254, s[40:43], s93 offen
s_mov_b32 s93, 57344
buffer_load_dwordx4 a[160:163], v254, s[40:43], s93 offen
s_mov_b32 s93, 64512
buffer_load_dwordx4 a[164:167], v254, s[40:43], s93 offen
s_mov_b32 s93, 71680
buffer_load_dwordx4 a[168:171], v254, s[40:43], s93 offen
s_mov_b32 s93, 78848
buffer_load_dwordx4 a[172:175], v254, s[40:43], s93 offen
s_mov_b32 s93, 86016
buffer_load_dwordx4 a[176:179], v254, s[40:43], s93 offen
s_mov_b32 s93, 93184
buffer_load_dwordx4 a[180:183], v254, s[40:43], s93 offen
s_mov_b32 s93, 100352
buffer_load_dwordx4 a[184:187], v254, s[40:43], s93 offen
s_mov_b32 s93, 107520
buffer_load_dwordx4 a[188:191], v254, s[40:43], s93 offen
s_branch .La5_LA_SR4
.La5_LO_W:
s_mov_b32 s93, 114688
buffer_load_dwordx4 a[192:195], v228, s[40:43], s93 offen
s_mov_b32 s93, 121856
buffer_load_dwordx4 a[196:199], v228, s[40:43], s93 offen
s_mov_b32 s93, 129024
buffer_load_dwordx4 a[200:203], v228, s[40:43], s93 offen
s_mov_b32 s93, 136192
buffer_load_dwordx4 a[204:207], v228, s[40:43], s93 offen
s_mov_b32 s93, 143360
buffer_load_dwordx4 a[208:211], v228, s[40:43], s93 offen
s_mov_b32 s93, 150528
buffer_load_dwordx4 a[212:215], v228, s[40:43], s93 offen
s_mov_b32 s93, 157696
buffer_load_dwordx4 a[216:219], v228, s[40:43], s93 offen
s_mov_b32 s93, 164864
buffer_load_dwordx4 a[220:223], v228, s[40:43], s93 offen
s_mov_b32 s93, 172032
buffer_load_dwordx4 a[224:227], v228, s[40:43], s93 offen
s_mov_b32 s93, 179200
buffer_load_dwordx4 a[228:231], v228, s[40:43], s93 offen
s_mov_b32 s93, 186368
buffer_load_dwordx4 a[232:235], v228, s[40:43], s93 offen
s_mov_b32 s93, 193536
buffer_load_dwordx4 a[236:239], v228, s[40:43], s93 offen
s_mov_b32 s93, 200704
buffer_load_dwordx4 a[240:243], v228, s[40:43], s93 offen
s_mov_b32 s93, 207872
buffer_load_dwordx4 a[244:247], v228, s[40:43], s93 offen
s_mov_b32 s93, 215040
buffer_load_dwordx4 a[248:251], v228, s[40:43], s93 offen
s_mov_b32 s93, 222208
buffer_load_dwordx4 a[252:255], v228, s[40:43], s93 offen
s_branch .La5_LO_START
.La5_LT_VAR:
v_mov_b32 v230, 0
v_lshlrev_b32 v232, 16, v96
v_and_b32 v233, 0xffff0000, v96
v_sub_f32 v232, v232, v244
v_sub_f32 v233, v233, v244
v_fmac_f32 v230, v232, v232
v_fmac_f32 v230, v233, v233
v_lshlrev_b32 v232, 16, v97
v_and_b32 v233, 0xffff0000, v97
v_sub_f32 v232, v232, v244
v_sub_f32 v233, v233, v244
v_fmac_f32 v230, v232, v232
v_fmac_f32 v230, v233, v233
v_lshlrev_b32 v232, 16, v98
v_and_b32 v233, 0xffff0000, v98
v_sub_f32 v232, v232, v244
v_sub_f32 v233, v233, v244
v_fmac_f32 v230, v232, v232
v_fmac_f32 v230, v233, v233
v_lshlrev_b32 v232, 16, v99
v_and_b32 v233, 0xffff0000, v99
v_sub_f32 v232, v232, v244
v_sub_f32 v233, v233, v244
v_fmac_f32 v230, v232, v232
v_fmac_f32 v230, v233, v233
v_lshlrev_b32 v232, 16, v100
v_and_b32 v233, 0xffff0000, v100
v_sub_f32 v232, v232, v244
v_sub_f32 v233, v233, v244
v_fmac_f32 v230, v232, v232
v_fmac_f32 v230, v233, v233
v_lshlrev_b32 v232, 16, v101
v_and_b32 v233, 0xffff0000, v101
v_sub_f32 v232, v232, v244
v_sub_f32 v233, v233, v244
v_fmac_f32 v230, v232, v232
v_fmac_f32 v230, v233, v233
v_lshlrev_b32 v232, 16, v102
v_and_b32 v233, 0xffff0000, v102
v_sub_f32 v232, v232, v244
v_sub_f32 v233, v233, v244
v_fmac_f32 v230, v232, v232
v_fmac_f32 v230, v233, v233
v_lshlrev_b32 v232, 16, v103
v_and_b32 v233, 0xffff0000, v103
v_sub_f32 v232, v232, v244
v_sub_f32 v233, v233, v244
v_fmac_f32 v230, v232, v232
v_fmac_f32 v230, v233, v233
v_lshlrev_b32 v232, 16, v104
v_and_b32 v233, 0xffff0000, v104
v_sub_f32 v232, v232, v244
v_sub_f32 v233, v233, v244
v_fmac_f32 v230, v232, v232
v_fmac_f32 v230, v233, v233
v_lshlrev_b32 v232, 16, v105
v_and_b32 v233, 0xffff0000, v105
v_sub_f32 v232, v232, v244
v_sub_f32 v233, v233, v244
v_fmac_f32 v230, v232, v232
v_fmac_f32 v230, v233, v233
v_lshlrev_b32 v232, 16, v106
v_and_b32 v233, 0xffff0000, v106
v_sub_f32 v232, v232, v244
v_sub_f32 v233, v233, v244
v_fmac_f32 v230, v232, v232
v_fmac_f32 v230, v233, v233
v_lshlrev_b32 v232, 16, v107
v_and_b32 v233, 0xffff0000, v107
v_sub_f32 v232, v232, v244
v_sub_f32 v233, v233, v244
v_fmac_f32 v230, v232, v232
v_fmac_f32 v230, v233, v233
v_lshlrev_b32 v232, 16, v108
v_and_b32 v233, 0xffff0000, v108
v_sub_f32 v232, v232, v244
v_sub_f32 v233, v233, v244
v_fmac_f32 v230, v232, v232
v_fmac_f32 v230, v233, v233
v_lshlrev_b32 v232, 16, v109
v_and_b32 v233, 0xffff0000, v109
v_sub_f32 v232, v232, v244
v_sub_f32 v233, v233, v244
v_fmac_f32 v230, v232, v232
v_fmac_f32 v230, v233, v233
v_lshlrev_b32 v232, 16, v110
v_and_b32 v233, 0xffff0000, v110
v_sub_f32 v232, v232, v244
v_sub_f32 v233, v233, v244
v_fmac_f32 v230, v232, v232
v_fmac_f32 v230, v233, v233
v_lshlrev_b32 v232, 16, v111
v_and_b32 v233, 0xffff0000, v111
v_sub_f32 v232, v232, v244
v_sub_f32 v233, v233, v244
v_fmac_f32 v230, v232, v232
v_fmac_f32 v230, v233, v233
s_nop 1
v_add_f32_dpp v230, v230, v230 quad_perm:[1,0,3,2] row_mask:0xf bank_mask:0xf
s_nop 1
v_add_f32_dpp v230, v230, v230 quad_perm:[2,3,0,1] row_mask:0xf bank_mask:0xf
s_nop 1
v_add_f32_dpp v230, v230, v230 row_ror:8 row_mask:0xf bank_mask:0xf
v_mov_b32 v231, v230
s_nop 1
v_permlane16_swap_b32 v231, v230
s_nop 1
v_add_f32 v230, v230, v231
v_mov_b32 v231, v230
s_nop 1
v_permlane32_swap_b32 v231, v230
s_nop 1
v_add_f32 v230, v230, v231
v_mul_f32 v230, 0x3a800000, v230
s_branch .La5_LT_VOK
.La5_LA_LSPIN:
s_mov_b32 s93, 0
.La5_LA_LSPIN_L:
s_add_u32 s93, s93, 1
s_cmp_gt_u32 s93, 2097152
s_cbranch_scc1 .La5_LA_LOK
s_sleep 1
buffer_load_dword v250, v251, s[64:67], s92 offen sc1
s_waitcnt vmcnt(0)
v_cmp_eq_u32 vcc, v250, v249
s_nop 1
s_or_b32 vcc_hi, vcc_hi, 0xff000000
s_cmp_eq_u32 vcc_hi, -1
s_nop 0
s_nop 0
s_cbranch_scc0 .La5_LA_LSPIN_L
s_branch .La5_LA_LOK
.La5_LA_KSPIN:
s_mov_b32 s93, 0
.La5_LA_KSPIN_L:
s_add_u32 s93, s93, 1
s_cmp_gt_u32 s93, 2097152
s_nop 0
s_nop 0
s_cbranch_scc1 .La5_LA_KOK
s_sleep 1
buffer_load_dword v250, v251, s[64:67], s20 offen sc1
s_waitcnt vmcnt(0)
v_cmp_eq_u32 vcc, v250, v249
s_nop 1
s_cmp_eq_u64 vcc, -1
s_cbranch_scc0 .La5_LA_KSPIN_L
s_branch .La5_LA_KOK
.La5_LA_QSPIN:
s_mov_b32 s93, 0
.La5_LA_QSPIN_L:
s_add_u32 s93, s93, 1
s_cmp_gt_u32 s93, 2097152
s_nop 0
s_nop 0
s_cbranch_scc1 .La5_LA_QOK
s_sleep 1
buffer_load_dword v250, v251, s[64:67], s20 offen sc1
s_waitcnt vmcnt(0)
v_cmp_eq_u32 vcc, v250, v249
s_nop 1
s_and_b32 s92, vcc_lo, 3
s_cmp_eq_u32 s92, 3
s_cbranch_scc0 .La5_LA_QSPIN_L
s_branch .La5_LA_QOK
.La5_LEND:
.La5_LCODEEND:
)ASM"
        :: "{s[0:1]}"(__builtin_amdgcn_kernarg_segment_ptr()), "{s2}"(blockIdx.x), "{s3}"(__builtin_amdgcn_readfirstlane(threadIdx.x >> 6)), "{s[4:5]}"(DBGP)
        : "v0","v1","v2","v3","v4","v5","v6","v7","v8","v9","v10","v11","v12","v13","v14","v15","v16","v17","v18","v19","v20","v21","v22","v23","v24","v25","v26","v27","v28","v29","v30","v31","v32","v33","v34","v35","v36","v37","v38","v39","v40","v41","v42","v43","v44","v45","v46","v47","v48","v49","v50","v51","v52","v53","v54","v55","v56","v57","v58","v59","v60","v61","v62","v63","v64","v65","v66","v67","v68","v69","v70","v71","v72","v73","v74","v75","v76","v77","v78","v79","v80","v81","v82","v83","v84","v85","v86","v87","v88","v89","v90","v91","v92","v93","v94","v95","v96","v97","v98","v99","v100","v101","v102","v103","v104","v105","v106","v107","v108","v109","v110","v111","v112","v113","v114","v115","v116","v117","v118","v119","v120","v121","v122","v123","v124","v125","v126","v127","v128","v129","v130","v131","v132","v133","v134","v135","v136","v137","v138","v139","v140","v141","v142","v143","v144","v145","v146","v147","v148","v149","v150","v151","v152","v153","v154","v155","v156","v157","v158","v159","v160","v161","v162","v163","v164","v165","v166","v167","v168","v169","v170","v171","v172","v173","v174","v175","v176","v177","v178","v179","v180","v181","v182","v183","v184","v185","v186","v187","v188","v189","v190","v191","v192","v193","v194","v195","v196","v197","v198","v199","v200","v201","v202","v203","v204","v205","v206","v207","v208","v209","v210","v211","v212","v213","v214","v215","v216","v217","v218","v219","v220","v221","v222","v223","v224","v225","v226","v227","v228","v229","v230","v231","v232","v233","v234","v235","v236","v237","v238","v239","v240","v241","v242","v243","v244","v245","v246","v247","v248","v249","v250","v251","v252","v253","v254","v255","a0","a1","a2","a3","a4","a5","a6","a7","a8","a9","a10","a11","a12","a13","a14","a15","a16","a17","a18","a19","a20","a21","a22","a23","a24","a25","a26","a27","a28","a29","a30","a31","a32","a33","a34","a35","a36","a37","a38","a39","a40","a41","a42","a43","a44","a45","a46","a47","a48","a49","a50","a51","a52","a53","a54","a55","a56","a57","a58","a59","a60","a61","a62","a63","a64","a65","a66","a67","a68","a69","a70","a71","a72","a73","a74","a75","a76","a77","a78","a79","a80","a81","a82","a83","a84","a85","a86","a87","a88","a89","a90","a91","a92","a93","a94","a95","a96","a97","a98","a99","a100","a101","a102","a103","a104","a105","a106","a107","a108","a109","a110","a111","a112","a113","a114","a115","a116","a117","a118","a119","a120","a121","a122","a123","a124","a125","a126","a127","a128","a129","a130","a131","a132","a133","a134","a135","a136","a137","a138","a139","a140","a141","a142","a143","a144","a145","a146","a147","a148","a149","a150","a151","a152","a153","a154","a155","a156","a157","a158","a159","a160","a161","a162","a163","a164","a165","a166","a167","a168","a169","a170","a171","a172","a173","a174","a175","a176","a177","a178","a179","a180","a181","a182","a183","a184","a185","a186","a187","a188","a189","a190","a191","a192","a193","a194","a195","a196","a197","a198","a199","a200","a201","a202","a203","a204","a205","a206","a207","a208","a209","a210","a211","a212","a213","a214","a215","a216","a217","a218","a219","a220","a221","a222","a223","a224","a225","a226","a227","a228","a229","a230","a231","a232","a233","a234","a235","a236","a237","a238","a239","a240","a241","a242","a243","a244","a245","a246","a247","a248","a249","a250","a251","a252","a253","a254","a255","s6","s7","s8","s9","s10","s11","s12","s13","s14","s15","s16","s17","s18","s19","s20","s21","s22","s23","s24","s25","s26","s27","s28","s29","s30","s31","s32","s33","s34","s35","s36","s37","s38","s39","s40","s41","s42","s43","s44","s45","s46","s47","s48","s49","s50","s51","s52","s53","s54","s55","s56","s57","s58","s59","s60","s61","s62","s63","s64","s65","s66","s67","s68","s69","s70","s71","s72","s73","s74","s75","s76","s77","s78","s79","s80","s81","s82","s83","s84","s85","s86","s87","s88","s89","s90","s91","s92","s93","s94","s95","s96","s97","s98","s99","s100","s101","vcc","scc","m0","memory");
}

extern "C" void kernel_launch(void* const* d_in, const int* in_sizes, int n_in, void* d_out, int out_size, void* d_ws, size_t ws_size,
                              hipStream_t stream) {
    (void)hipFuncSetAttribute((const void*)fa_kernel, hipFuncAttributeMaxDynamicSharedMemorySize, LDS_BYTES);
    fa_kernel<<<256, 256, LDS_BYTES, stream>>>((const bf16*)d_in[0], (const bf16*)d_in[1], (const bf16*)d_in[2], (const bf16*)d_in[3], (const bf16*)d_in[4], (char*)d_ws,
                                               (const bf16*)d_in[5], (const bf16*)d_in[8], (const bf16*)d_in[9], (const bf16*)d_in[10], (const bf16*)d_in[7], (bf16*)d_out);
}
```

```cpp
#include <hip/hip_runtime.h>
#include <hip/hip_bf16.h>
#include <stdint.h>
#pragma clang diagnostic ignored "-Winline-asm"
#pragma clang diagnostic ignored "-Woverlength-strings"

typedef __hip_bfloat16 bf16;
#define DBGP ((unsigned*)nullptr)
constexpr int LDS_BYTES = 163840;
extern "C" __global__ __attribute__((aligned(4096))) __launch_bounds__(256, 1) void fa_kernel(const bf16* __restrict__ px, const bf16* __restrict__ ckv, const bf16* __restrict__ wq,
                                                    const bf16* __restrict__ wuk, const bf16* __restrict__ wuv, char* pws,
                                                    const bf16* __restrict__ pwo, const bf16* __restrict__ pwg, const bf16* __restrict__ pwu,
                                                    const bf16* __restrict__ pwd, const bf16* __restrict__ pwr, bf16* __restrict__ pout) {
    asm volatile(R"ASM(
; ---- roles: head = 4 xcd + (wg >> 6), j = (wg >> 3) & 7; odd j: block j >> 1 (then helps with block 7 - (j >> 1)), even j: block 7 - (j >> 1). First operands, addresses
.La5_LSTART:
s_load_dwordx16 s[16:31], s[0:1], 0
s_load_dwordx8 s[32:39], s[0:1], 64
.p2align 6
v_mbcnt_lo_u32_b32 v64, -1, 0
v_mbcnt_hi_u32_b32 v64, -1, v64
s_getpc_b64 s[92:93]
.La5_LPC:
s_sub_u32 s48, s92, .La5_LPC-.La5_LSTART
s_subb_u32 s49, s93, 0
s_and_b32 s49, s49, 0xffff
s_mov_b32 s50, .La5_LCODEEND-.La5_LSTART
s_mov_b32 s51, 0x20000
s_lshr_b32 s97, s2, 3
s_lshl2_add_u32 s87, s97, s3
v_lshlrev_b32 v165, 7, v64
s_lshl_b32 s87, s87, 13
buffer_load_dword v254, v165, s[48:51], s87 offen
s_and_b32 s92, s2, 7
s_lshr_b32 s93, s2, 6
s_lshl2_add_u32 s72, s92, s93
s_bfe_u32 s73, s2, 0x30003
s_lshr_b32 s74, s73, 1
s_xor_b32 s92, s74, 7
s_and_b32 s75, s73, 1
s_cselect_b32 s76, s74, s92
s_lshr_b32 s77, s3, 1
s_and_b32 s78, s3, 1
v_and_b32 v65, 15, v64
v_lshrrev_b32 v66, 4, v64
v_lshlrev_b32 v255, 4, v64
v_lshlrev_b32 v67, 1, v66
v_xor_b32 v67, v67, v65
v_lshlrev_b32 v67, 4, v67
v_lshl_add_u32 v67, v66, 13, v67
s_lshl_b32 s94, s78, 17
v_add_u32 v232, s94, v67
v_add_u32 v233, 32768, v232
v_add_u32 v234, 65536, v232
v_add_u32 v235, 98304, v232
v_and_b32 v67, 7, v64
v_xor_b32 v68, v67, v66
v_lshlrev_b32 v68, 4, v68
v_lshrrev_b32 v69, 3, v64
v_lshl_add_u32 v68, v69, 10, v68
s_lshl2_add_u32 s94, s3, s93
s_lshl_b32 s82, s94, 11
s_lshl_b32 s94, s94, 14
v_add_u32 v224, s94, v68
v_xor_b32 v225, 0x2040, v224
v_xor_b32 v226, 16384, v224
v_xor_b32 v227, 16384, v225
v_xor_b32 v228, 32768, v224
v_xor_b32 v229, 32768, v225
v_xor_b32 v230, 49152, v224
v_xor_b32 v231, 49152, v225
s_mul_i32 s94, s77, 8448
s_mul_i32 s95, s78, 4224
s_add_u32 s83, s94, s95
s_add_u32 s83, s83, 96256
v_lshlrev_b32 v167, 7, v67
v_lshl_add_u32 v167, v69, 13, v167
v_lshrrev_b32 v164, 5, v64
v_lshl_add_u32 v164, v164, 28, v167
v_lshl_add_u32 v165, v66, 28, v165
v_lshlrev_b32 v166, 10, v65
v_lshl_add_u32 v166, v66, 7, v166
v_lshrrev_b32 v68, 4, v64
v_lshl_add_u32 v166, v68, 28, v166
s_lshl1_add_u32 s97, s97, s78
s_lshl_b32 s97, s97, 15
s_lshl_b32 s96, s93, 8
s_sub_u32 s87, s87, s96
s_sub_u32 s97, s97, s96
s_add_u32 s86, s97, 0
s_lshl2_add_u32 s101, s72, s3
s_lshl_b32 s101, s101, 11
s_lshl2_add_u32 s100, s93, s3
s_lshl_b32 s100, s100, 14
v_lshlrev_b32 v118, 11, v67
s_lshl_b32 s93, s3, 9
v_lshl_add_u32 v119, v69, 4, v118
v_add_u32 v119, s93, v119
s_lshl_b32 s97, s2, 14
s_lshl_b32 s94, s72, 8
s_lshl_b32 s95, s76, 18
s_sub_u32 s46, 4194304, s94
s_mov_b32 s47, 0x20000
s_mov_b32 s42, 262144
s_mov_b32 s43, 0x20000
s_mov_b32 s62, 16384
s_mov_b32 s63, 0x20000
s_mov_b32 s79, 0
s_mov_b32 s80, 0
s_mov_b32 s81, 262144
s_cmp_eq_u32 s77, 0
s_waitcnt lgkmcnt(0)
s_cselect_b64 s[92:93], s[22:23], s[24:25]
s_add_u32 s44, s92, s94
s_addc_u32 s45, s93, 0
s_and_b32 s45, s45, 0xffff
s_add_u32 s40, s18, s95
s_addc_u32 s41, s19, 0
s_and_b32 s41, s41, 0xffff
s_add_u32 s60, s16, s97
s_addc_u32 s61, s17, 0
s_and_b32 s61, s61, 0xffff
buffer_load_dword v254, v164, s[44:47], s86 offen
s_add_u32 s86, s86, 2097152
buffer_load_dword v254, v165, s[40:43], s101 offen
buffer_load_dword v254, v166, s[40:43], s100 offen
s_add_u32 m0, s83, 0
s_nop 0
buffer_load_dwordx4 v232, s[44:47], s80 offen lds
s_add_u32 m0, s83, 1024
s_nop 0
buffer_load_dwordx4 v233, s[44:47], s80 offen lds
s_add_u32 m0, s83, 2176
s_nop 0
buffer_load_dwordx4 v234, s[44:47], s80 offen lds
s_add_u32 m0, s83, 3200
s_nop 0
buffer_load_dwordx4 v235, s[44:47], s80 offen lds
s_add_u32 s80, s80, s81
v_lshrrev_b32 v67, 1, v65
v_xor_b32 v67, v67, v66
v_lshlrev_b32 v67, 4, v67
v_lshl_add_u32 v67, v65, 7, v67
s_lshl_b32 s92, s77, 12
v_add_u32 v236, s92, v67
v_xor_b32 v237, 64, v236
v_bfe_u32 v120, v64, 2, 2
v_add_u32 v67, 1, v66
v_lshrrev_b32 v67, 1, v67
v_lshlrev_b32 v67, 7, v67
v_lshl_add_u32 v67, v66, 11, v67
v_lshl_add_u32 v67, v120, 8, v67
v_and_b32 v68, 3, v64
v_lshl_add_u32 v67, v68, 3, v67
s_mul_i32 s92, s78, 8448
v_lshl_add_u32 v67, v120, 5, v67
v_add_u32 v67, s92, v67
v_add_u32 v240, 96256, v67
v_xor_b32 v241, 32, v240
v_xor_b32 v242, 64, v240
v_xor_b32 v243, 96, v240
s_add_u32 s64, s26, 142606336
s_addc_u32 s65, s27, 0
s_and_b32 s65, s65, 0xffff
s_mov_b32 s66, 3145728
s_mov_b32 s67, 0x20000
s_mov_b32 s88, 4096
s_lshr_b32 s92, s2, 1
s_lshl_b32 s92, s92, 15
s_and_b32 s93, s2, 1
s_lshl_b32 s93, s93, 7
s_add_u32 s92, s92, s93
s_lshl_b32 s93, s3, 13
s_add_u32 s92, s92, s93
s_add_u32 s92, s92, 88080384
s_add_u32 s56, s26, s92
s_addc_u32 s57, s27, 0
s_and_b32 s57, s57, 0xffff
s_mov_b32 s58, 8576
s_mov_b32 s59, 0x20000
v_and_b32 v71, 0x380, v255
v_and_b32 v127, 0x70, v255
v_lshl_add_u32 v71, v71, 1, v127
v_xor_b32 v67, 3, v69
v_min_u32 v67, 1, v67
v_xor_b32 v67, 1, v67
s_mov_b32 s93, 0
v_mul_lo_u32 v67, v67, s93
v_add_u32 v168, v67, v255
v_and_b32 v67, 3, v69
v_xor_b32 v67, 1, v67
v_min_u32 v67, 1, v67
v_xor_b32 v67, 1, v67
v_and_b32 v67, s2, v67
v_mul_lo_u32 v67, v67, s93
v_add_u32 v71, v71, v67
s_lshl_b32 s92, s3, 3
v_lshlrev_b32 v126, 1, v127
v_or_b32 v126, 65536, v126
v_add_u32 v127, s92, v126
s_add_u32 s52, s26, 171966464
s_addc_u32 s53, s27, 0
s_and_b32 s53, s53, 0xffff
s_mov_b32 s54, 950272
s_mov_b32 s55, 0x20000
s_lshl2_add_u32 s95, s2, s3
s_lshl_b32 s91, s95, 3
s_lshl_b32 s95, s95, 10
s_lshl_b32 s89, s76, 10
v_lshl_add_u32 v167, v69, 28, v167
s_mov_b32 s96, s87
s_mov_b32 s84, 33792
s_mov_b32 s85, 0
s_add_u32 s97, s83, 67584
s_mov_b32 s90, s83
s_lshl_b32 s92, s72, 8
s_sub_u32 s93, 8388608, s92
s_add_u32 s68, s20, s92
s_addc_u32 s69, s21, 0
s_and_b32 s69, s69, 0xffff
s_mov_b32 s70, s93
s_mov_b32 s71, 0x20000
s_lshr_b32 s92, s20, 7
s_sub_u32 s92, 3, s92
s_bfe_u32 s99, s92, 0x20001
s_lshr_b32 s93, s2, 6
s_sub_u32 s99, s99, s93
s_lshl_b32 s92, s99, 8
s_lshr_b32 s93, s2, 3
s_lshl2_add_u32 s93, s93, s3
s_lshl_b32 s98, s93, 16
s_add_u32 s98, s98, s92
v_lshlrev_b32 v170, 4, v65
v_lshl_add_u32 v169, v66, 13, v170
v_lshl_add_u32 v170, v66, 9, v170
s_lshl_b32 s93, s93, 12
v_add_u32 v170, s93, v170
s_and_b32 s93, s2, 7
s_lshl_b32 s92, s93, 19
s_add_u32 s92, s92, 138412032
s_add_u32 s4, s26, s92
s_addc_u32 s5, s27, 0
s_and_b32 s5, s5, 0xffff
s_mov_b32 s6, 0
s_mov_b32 s100, 1073741824
s_add_u32 s101, s91, 65536
v_lshrrev_b32 v171, 1, v64
v_lshlrev_b32 v171, 8, v171
v_and_or_b32 v171, v255, 16, v171
s_lshl_b32 s93, s93, 5
s_add_u32 s93, s93, 64512
v_add_u32 v171, s93, v171
s_mov_b64 exec, 0
s_cmp_eq_u32 s3, 0
s_cbranch_scc1 .La5_LW1_0
s_cmp_eq_u32 s3, 1
s_cbranch_scc1 .La5_LW1_1
s_cmp_eq_u32 s3, 2
s_cbranch_scc1 .La5_LW1_2
s_branch .La5_LW1_3
.La5_LW1_R0:
s_mov_b64 exec, -1
s_xor_b32 m0, s82, 0
s_nop 0
buffer_load_dwordx4 v224, s[40:43], s79 offen lds
s_xor_b32 m0, s82, 1024
s_nop 0
buffer_load_dwordx4 v225, s[40:43], s79 offen lds
s_xor_b32 m0, s82, 2048
s_nop 0
buffer_load_dwordx4 v226, s[40:43], s79 offen lds
s_xor_b32 m0, s82, 3072
s_nop 0
buffer_load_dwordx4 v227, s[40:43], s79 offen lds
s_xor_b32 m0, s82, 4096
s_nop 0
buffer_load_dwordx4 v228, s[40:43], s79 offen lds
s_xor_b32 m0, s82, 5120
s_nop 0
buffer_load_dwordx4 v229, s[40:43], s79 offen lds
s_xor_b32 m0, s82, 6144
s_nop 0
buffer_load_dwordx4 v230, s[40:43], s79 offen lds
s_xor_b32 m0, s82, 7168
s_nop 0
buffer_load_dwordx4 v231, s[40:43], s79 offen lds
s_add_u32 s79, s79, 128
s_add_u32 m0, s83, 16896
s_nop 0
buffer_load_dwordx4 v232, s[44:47], s80 offen lds
s_add_u32 m0, s83, 17920
s_nop 0
buffer_load_dwordx4 v233, s[44:47], s80 offen lds
s_add_u32 m0, s83, 19072
s_nop 0
buffer_load_dwordx4 v234, s[44:47], s80 offen lds
s_add_u32 m0, s83, 20096
s_nop 0
buffer_load_dwordx4 v235, s[44:47], s80 offen lds
s_add_u32 s80, s80, s81
buffer_load_dwordx2 v[252:253], off, s[64:67], 0 sc1
s_waitcnt vmcnt(5)
s_barrier
ds_read_b128 v[128:131], v236 offset:24576
ds_read_b64_tr_b16 v[192:193], v240 offset:0
ds_read_b64_tr_b16 v[194:195], v240 offset:1024
ds_read_b64_tr_b16 v[196:197], v241 offset:0
ds_read_b64_tr_b16 v[198:199], v241 offset:1024
ds_read_b64_tr_b16 v[200:201], v242 offset:0
ds_read_b64_tr_b16 v[202:203], v242 offset:1024
ds_read_b64_tr_b16 v[204:205], v243 offset:0
ds_read_b64_tr_b16 v[206:207], v243 offset:1024
ds_read_b64_tr_b16 v[208:209], v240 offset:128
ds_read_b64_tr_b16 v[210:211], v240 offset:1152
ds_read_b64_tr_b16 v[212:213], v241 offset:128
ds_read_b64_tr_b16 v[214:215], v241 offset:1152
ds_read_b64_tr_b16 v[216:217], v242 offset:128
ds_read_b64_tr_b16 v[218:219], v242 offset:1152
ds_read_b64_tr_b16 v[220:221], v243 offset:128
ds_read_b64_tr_b16 v[222:223], v243 offset:1152
ds_read_b128 v[132:135], v236 offset:26624
ds_read_b128 v[136:139], v236 offset:16384
ds_read_b128 v[140:143], v236 offset:18432
ds_read_b128 v[144:147], v236 offset:8192
ds_read_b128 v[148:151], v236 offset:10240
ds_read_b128 v[152:155], v236 offset:0
ds_read_b128 v[156:159], v236 offset:2048
s_waitcnt lgkmcnt(0)
s_cmp_eq_u32 s78, 1
s_cbranch_scc1 .La5_LV_START
; ---- K tile (waves 0, 2): 256 keys x 128 columns, K = 512; LayerNorm of 8 rows of x in the gaps (softmax scale folded in)
.La5_LK_START:
s_waitcnt vmcnt(1)
s_barrier
v_mfma_f32_16x16x32_bf16 a[224:227], v[192:195], v[128:131], 0
ds_read_b128 v[0:3], v237 offset:24576
s_add_u32 m0, s83, 33792
v_mfma_f32_16x16x32_bf16 a[228:231], v[196:199], v[128:131], 0
buffer_load_dwordx4 v232, s[44:47], s80 offen lds
v_mfma_f32_16x16x32_bf16 a[232:235], v[200:203], v[128:131], 0
ds_read_b64_tr_b16 v[32:33], v240 offset:16896
v_mfma_f32_16x16x32_bf16 a[236:239], v[204:207], v[128:131], 0
v_mfma_f32_16x16x32_bf16 a[208:211], v[208:211], v[128:131], 0
ds_read_b64_tr_b16 v[34:35], v240 offset:17920
s_add_u32 m0, s83, 34816
v_mfma_f32_16x16x32_bf16 a[212:215], v[212:215], v[128:131], 0
buffer_load_dwordx4 v233, s[44:47], s80 offen lds
v_mfma_f32_16x16x32_bf16 a[128:131], v[216:219], v[128:131], 0
ds_read_b64_tr_b16 v[36:37], v241 offset:16896
v_mfma_f32_16x16x32_bf16 a[132:135], v[220:223], v[128:131], 0
v_mfma_f32_16x16x32_bf16 a[216:219], v[192:195], v[132:135], 0
ds_read_b64_tr_b16 v[38:39], v241 offset:17920
s_add_u32 m0, s83, 35968
v_mfma_f32_16x16x32_bf16 a[220:223], v[196:199], v[132:135], 0
buffer_load_dwordx4 v234, s[44:47], s80 offen lds
v_mfma_f32_16x16x32_bf16 a[136:139], v[200:203], v[132:135], 0
ds_read_b64_tr_b16 v[40:41], v242 offset:16896
v_mfma_f32_16x16x32_bf16 a[140:143], v[204:207], v[132:135], 0
v_mfma_f32_16x16x32_bf16 a[192:195], v[208:211], v[132:135], 0
ds_read_b64_tr_b16 v[42:43], v242 offset:17920
s_add_u32 m0, s83, 36992
v_mfma_f32_16x16x32_bf16 a[196:199], v[212:215], v[132:135], 0
buffer_load_dwordx4 v235, s[44:47], s80 offen lds
v_mfma_f32_16x16x32_bf16 a[144:147], v[216:219], v[132:135], 0
ds_read_b64_tr_b16 v[44:45], v243 offset:16896
v_mfma_f32_16x16x32_bf16 a[148:151], v[220:223], v[132:135], 0
s_add_u32 s80, s80, s81
v_mfma_f32_16x16x32_bf16 a[200:203], v[192:195], v[136:139], 0
ds_read_b64_tr_b16 v[46:47], v243 offset:17920
s_xor_b32 m0, s82, 32768
v_mfma_f32_16x16x32_bf16 a[204:207], v[196:199], v[136:139], 0
buffer_load_dwordx4 v224, s[40:43], s79 offen lds
v_mfma_f32_16x16x32_bf16 a[152:155], v[200:203], v[136:139], 0
ds_read_b64_tr_b16 v[48:49], v240 offset:17024
v_mfma_f32_16x16x32_bf16 a[156:159], v[204:207], v[136:139], 0
v_mfma_f32_16x16x32_bf16 a[96:99], v[208:211], v[136:139], 0
ds_read_b64_tr_b16 v[50:51], v240 offset:18048
s_xor_b32 m0, s82, 33792
v_mfma_f32_16x16x32_bf16 a[100:103], v[212:215], v[136:139], 0
buffer_load_dwordx4 v225, s[40:43], s79 offen lds
v_mfma_f32_16x16x32_bf16 a[160:163], v[216:219], v[136:139], 0
ds_read_b64_tr_b16 v[52:53], v241 offset:17024
v_mfma_f32_16x16x32_bf16 a[164:167], v[220:223], v[136:139], 0
v_mfma_f32_16x16x32_bf16 a[104:107], v[192:195], v[140:143], 0
ds_read_b64_tr_b16 v[54:55], v241 offset:18048
s_xor_b32 m0, s82, 34816
v_mfma_f32_16x16x32_bf16 a[108:111], v[196:199], v[140:143], 0
buffer_load_dwordx4 v226, s[40:43], s79 offen lds
v_mfma_f32_16x16x32_bf16 a[168:171], v[200:203], v[140:143], 0
ds_read_b64_tr_b16 v[56:57], v242 offset:17024
v_mfma_f32_16x16x32_bf16 a[172:175], v[204:207], v[140:143], 0
v_mfma_f32_16x16x32_bf16 a[112:115], v[208:211], v[140:143], 0
ds_read_b64_tr_b16 v[58:59], v242 offset:18048
s_xor_b32 m0, s82, 35840
v_mfma_f32_16x16x32_bf16 a[116:119], v[212:215], v[140:143], 0
buffer_load_dwordx4 v227, s[40:43], s79 offen lds
v_mfma_f32_16x16x32_bf16 a[176:179], v[216:219], v[140:143], 0
ds_read_b64_tr_b16 v[60:61], v243 offset:17024
v_mfma_f32_16x16x32_bf16 a[180:183], v[220:223], v[140:143], 0
v_mfma_f32_16x16x32_bf16 a[120:123], v[192:195], v[144:147], 0
ds_read_b64_tr_b16 v[62:63], v243 offset:18048
s_xor_b32 m0, s82, 36864
v_mfma_f32_16x16x32_bf16 a[124:127], v[196:199], v[144:147], 0
buffer_load_dwordx4 v228, s[40:43], s79 offen lds
v_mfma_f32_16x16x32_bf16 a[184:187], v[200:203], v[144:147], 0
ds_read_b128 v[4:7], v237 offset:26624
v_mfma_f32_16x16x32_bf16 a[188:191], v[204:207], v[144:147], 0
v_mfma_f32_16x16x32_bf16 a[64:67], v[208:211], v[144:147], 0
ds_read_b128 v[8:11], v237 offset:16384
s_xor_b32 m0, s82, 37888
v_mfma_f32_16x16x32_bf16 a[68:71], v[212:215], v[144:147], 0
buffer_load_dwordx4 v229, s[40:43], s79 offen lds
v_mfma_f32_16x16x32_bf16 a[0:3], v[216:219], v[144:147], 0
ds_read_b128 v[12:15], v237 offset:18432
v_mfma_f32_16x16x32_bf16 a[4:7], v[220:223], v[144:147], 0
v_mfma_f32_16x16x32_bf16 a[72:75], v[192:195], v[148:151], 0
ds_read_b128 v[16:19], v237 offset:8192
s_xor_b32 m0, s82, 38912
v_mfma_f32_16x16x32_bf16 a[76:79], v[196:199], v[148:151], 0
buffer_load_dwordx4 v230, s[40:43], s79 offen lds
v_mfma_f32_16x16x32_bf16 a[8:11], v[200:203], v[148:151], 0
ds_read_b128 v[20:23], v237 offset:10240
v_mfma_f32_16x16x32_bf16 a[12:15], v[204:207], v[148:151], 0
v_mfma_f32_16x16x32_bf16 a[80:83], v[208:211], v[148:151], 0
ds_read_b128 v[24:27], v237 offset:0
s_xor_b32 m0, s82, 39936
v_mfma_f32_16x16x32_bf16 a[84:87], v[212:215], v[148:151], 0
buffer_load_dwordx4 v231, s[40:43], s79 offen lds
v_mfma_f32_16x16x32_bf16 a[16:19], v[216:219], v[148:151], 0
ds_read_b128 v[28:31], v237 offset:2048
v_mfma_f32_16x16x32_bf16 a[20:23], v[220:223], v[148:151], 0
s_add_u32 s79, s79, 128
v_mfma_f32_16x16x32_bf16 a[88:91], v[192:195], v[152:155], 0
v_mfma_f32_16x16x32_bf16 a[92:95], v[196:199], v[152:155], 0
buffer_load_dword v70, v118, s[60:63], 0 offen
v_mfma_f32_16x16x32_bf16 a[24:27], v[200:203], v[152:155], 0
v_mfma_f32_16x16x32_bf16 a[28:31], v[204:207], v[152:155], 0
v_mfma_f32_16x16x32_bf16 a[240:243], v[208:211], v[152:155], 0
buffer_load_dwordx4 v[72:75], v119, s[60:63], 0 offen offset:0
v_mfma_f32_16x16x32_bf16 a[244:247], v[212:215], v[152:155], 0
v_mfma_f32_16x16x32_bf16 a[32:35], v[216:219], v[152:155], 0
v_mfma_f32_16x16x32_bf16 a[36:39], v[220:223], v[152:155], 0
buffer_load_dwordx4 v[76:79], v119, s[60:63], 0 offen offset:128
v_mfma_f32_16x16x32_bf16 a[248:251], v[192:195], v[156:159], 0
v_mfma_f32_16x16x32_bf16 a[252:255], v[196:199], v[156:159], 0
v_mfma_f32_16x16x32_bf16 a[40:43], v[200:203], v[156:159], 0
buffer_load_dwordx4 v[80:83], v119, s[60:63], 0 offen offset:256
v_mfma_f32_16x16x32_bf16 a[44:47], v[204:207], v[156:159], 0
v_mfma_f32_16x16x32_bf16 a[48:51], v[208:211], v[156:159], 0
v_mfma_f32_16x16x32_bf16 a[52:55], v[212:215], v[156:159], 0
buffer_load_dwordx4 v[84:87], v119, s[60:63], 0 offen offset:384
v_mfma_f32_16x16x32_bf16 a[56:59], v[216:219], v[156:159], 0
v_mfma_f32_16x16x32_bf16 a[60:63], v[220:223], v[156:159], 0
s_mov_b64 exec, 0
s_cmp_eq_u32 s3, 0
s_cbranch_scc1 .La5_LW2_0
s_branch .La5_LW2_2
.La5_LW2_R0:
s_mov_b64 exec, -1
s_waitcnt lgkmcnt(0)
s_waitcnt vmcnt(5)
s_barrier
v_mfma_f32_16x16x32_bf16 a[224:227], v[32:35], v[0:3], a[224:227]
ds_read_b128 v[128:131], v236 offset:57344
s_add_u32 m0, s83, 50688
v_mfma_f32_16x16x32_bf16 a[228:231], v[36:39], v[0:3], a[228:231]
buffer_load_dwordx4 v232, s[44:47], s80 offen lds
v_mfma_f32_16x16x32_bf16 a[232:235], v[40:43], v[0:3], a[232:235]
ds_read_b64_tr_b16 v[192:193], v240 offset:33792
v_mfma_f32_16x16x32_bf16 a[236:239], v[44:47], v[0:3], a[236:239]
v_mfma_f32_16x16x32_bf16 a[208:211], v[48:51], v[0:3], a[208:211]
ds_read_b64_tr_b16 v[194:195], v240 offset:34816
s_add_u32 m0, s83, 51712
v_mfma_f32_16x16x32_bf16 a[212:215], v[52:55], v[0:3], a[212:215]
buffer_load_dwordx4 v233, s[44:47], s80 offen lds
v_mfma_f32_16x16x32_bf16 a[128:131], v[56:59], v[0:3], a[128:131]
ds_read_b64_tr_b16 v[196:197], v241 offset:33792
v_mfma_f32_16x16x32_bf16 a[132:135], v[60:63], v[0:3], a[132:135]
v_mfma_f32_16x16x32_bf16 a[216:219], v[32:35], v[4:7], a[216:219]
ds_read_b64_tr_b16 v[198:199], v241 offset:34816
s_add_u32 m0, s83, 52864
v_mfma_f32_16x16x32_bf16 a[220:223], v[36:39], v[4:7], a[220:223]
buffer_load_dwordx4 v234, s[44:47], s80 offen lds
v_mfma_f32_16x16x32_bf16 a[136:139], v[40:43], v[4:7], a[136:139]
ds_read_b64_tr_b16 v[200:201], v242 offset:33792
v_mfma_f32_16x16x32_bf16 a[140:143], v[44:47], v[4:7], a[140:143]
v_mfma_f32_16x16x32_bf16 a[192:195], v[48:51], v[4:7], a[192:195]
ds_read_b64_tr_b16 v[202:203], v242 offset:34816
s_add_u32 m0, s83, 53888
v_mfma_f32_16x16x32_bf16 a[196:199], v[52:55], v[4:7], a[196:199]
buffer_load_dwordx4 v235, s[44:47], s80 offen lds
v_mfma_f32_16x16x32_bf16 a[144:147], v[56:59], v[4:7], a[144:147]
ds_read_b64_tr_b16 v[204:205], v243 offset:33792
v_mfma_f32_16x16x32_bf16 a[148:151], v[60:63], v[4:7], a[148:151]
s_add_u32 s80, s80, s81
v_mfma_f32_16x16x32_bf16 a[200:203], v[32:35], v[8:11], a[200:203]
ds_read_b64_tr_b16 v[206:207], v243 offset:34816
s_xor_b32 m0, s82, 0
v_mfma_f32_16x16x32_bf16 a[204:207], v[36:39], v[8:11], a[204:207]
buffer_load_dwordx4 v224, s[40:43], s79 offen lds
v_mfma_f32_16x16x32_bf16 a[152:155], v[40:43], v[8:11], a[152:155]
ds_read_b64_tr_b16 v[208:209], v240 offset:33920
v_mfma_f32_16x16x32_bf16 a[156:159], v[44:47], v[8:11], a[156:159]
v_mfma_f32_16x16x32_bf16 a[96:99], v[48:51], v[8:11], a[96:99]
ds_read_b64_tr_b16 v[210:211], v240 offset:34944
s_xor_b32 m0, s82, 1024
v_mfma_f32_16x16x32_bf16 a[100:103], v[52:55], v[8:11], a[100:103]
buffer_load_dwordx4 v225, s[40:43], s79 offen lds
v_mfma_f32_16x16x32_bf16 a[160:163], v[56:59], v[8:11], a[160:163]
ds_read_b64_tr_b16 v[212:213], v241 offset:33920
v_mfma_f32_16x16x32_bf16 a[164:167], v[60:63], v[8:11], a[164:167]
v_mfma_f32_16x16x32_bf16 a[104:107], v[32:35], v[12:15], a[104:107]
ds_read_b64_tr_b16 v[214:215], v241 offset:34944
s_xor_b32 m0, s82, 2048
v_mfma_f32_16x16x32_bf16 a[108:111], v[36:39], v[12:15], a[108:111]
buffer_load_dwordx4 v226, s[40:43], s79 offen lds
v_mfma_f32_16x16x32_bf16 a[168:171], v[40:43], v[12:15], a[168:171]
ds_read_b64_tr_b16 v[216:217], v242 offset:33920
v_mfma_f32_16x16x32_bf16 a[172:175], v[44:47], v[12:15], a[172:175]
v_mfma_f32_16x16x32_bf16 a[112:115], v[48:51], v[12:15], a[112:115]
ds_read_b64_tr_b16 v[218:219], v242 offset:34944
s_xor_b32 m0, s82, 3072
v_mfma_f32_16x16x32_bf16 a[116:119], v[52:55], v[12:15], a[116:119]
buffer_load_dwordx4 v227, s[40:43], s79 offen lds
v_mfma_f32_16x16x32_bf16 a[176:179], v[56:59], v[12:15], a[176:179]
ds_read_b64_tr_b16 v[220:221], v243 offset:33920
v_mfma_f32_16x16x32_bf16 a[180:183], v[60:63], v[12:15], a[180:183]
v_mfma_f32_16x16x32_bf16 a[120:123], v[32:35], v[16:19], a[120:123]
ds_read_b64_tr_b16 v[222:223], v243 offset:34944
s_xor_b32 m0, s82, 4096
v_mfma_f32_16x16x32_bf16 a[124:127], v[36:39], v[16:19], a[124:127]
buffer_load_dwordx4 v228, s[40:43], s79 offen lds
v_mfma_f32_16x16x32_bf16 a[184:187], v[40:43], v[16:19], a[184:187]
ds_read_b128 v[132:135], v236 offset:59392
v_mfma_f32_16x16x32_bf16 a[188:191], v[44:47], v[16:19], a[188:191]
v_mfma_f32_16x16x32_bf16 a[64:67], v[48:51], v[16:19], a[64:67]
ds_read_b128 v[136:139], v236 offset:49152
s_xor_b32 m0, s82, 5120
v_mfma_f32_16x16x32_bf16 a[68:71], v[52:55], v[16:19], a[68:71]
buffer_load_dwordx4 v229, s[40:43], s79 offen lds
v_mfma_f32_16x16x32_bf16 a[0:3], v[56:59], v[16:19], a[0:3]
ds_read_b128 v[140:143], v236 offset:51200
v_mfma_f32_16x16x32_bf16 a[4:7], v[60:63], v[16:19], a[4:7]
v_mfma_f32_16x16x32_bf16 a[72:75], v[32:35], v[20:23], a[72:75]
ds_read_b128 v[144:147], v236 offset:40960
s_xor_b32 m0, s82, 6144
v_mfma_f32_16x16x32_bf16 a[76:79], v[36:39], v[20:23], a[76:79]
buffer_load_dwordx4 v230, s[40:43], s79 offen lds
v_mfma_f32_16x16x32_bf16 a[8:11], v[40:43], v[20:23], a[8:11]
ds_read_b128 v[148:151], v236 offset:43008
v_mfma_f32_16x16x32_bf16 a[12:15], v[44:47], v[20:23], a[12:15]
v_mfma_f32_16x16x32_bf16 a[80:83], v[48:51], v[20:23], a[80:83]
ds_read_b128 v[152:155], v236 offset:32768
s_xor_b32 m0, s82, 7168
v_mfma_f32_16x16x32_bf16 a[84:87], v[52:55], v[20:23], a[84:87]
buffer_load_dwordx4 v231, s[40:43], s79 offen lds
v_mfma_f32_16x16x32_bf16 a[16:19], v[56:59], v[20:23], a[16:19]
ds_read_b128 v[156:159], v236 offset:34816
v_mfma_f32_16x16x32_bf16 a[20:23], v[60:63], v[20:23], a[20:23]
s_add_u32 s79, s79, 128
v_mfma_f32_16x16x32_bf16 a[88:91], v[32:35], v[24:27], a[88:91]
s_add_u32 m0, s83, 0
v_mfma_f32_16x16x32_bf16 a[92:95], v[36:39], v[24:27], a[92:95]
buffer_load_dwordx4 v232, s[44:47], s80 offen lds
v_mfma_f32_16x16x32_bf16 a[24:27], v[40:43], v[24:27], a[24:27]
v_mfma_f32_16x16x32_bf16 a[28:31], v[44:47], v[24:27], a[28:31]
v_mfma_f32_16x16x32_bf16 a[240:243], v[48:51], v[24:27], a[240:243]
s_add_u32 m0, s83, 1024
v_mfma_f32_16x16x32_bf16 a[244:247], v[52:55], v[24:27], a[244:247]
buffer_load_dwordx4 v233, s[44:47], s80 offen lds
v_mfma_f32_16x16x32_bf16 a[32:35], v[56:59], v[24:27], a[32:35]
v_mfma_f32_16x16x32_bf16 a[36:39], v[60:63], v[24:27], a[36:39]
v_mfma_f32_16x16x32_bf16 a[248:251], v[32:35], v[28:31], a[248:251]
s_add_u32 m0, s83, 2176
v_mfma_f32_16x16x32_bf16 a[252:255], v[36:39], v[28:31], a[252:255]
buffer_load_dwordx4 v234, s[44:47], s80 offen lds
v_mfma_f32_16x16x32_bf16 a[40:43], v[40:43], v[28:31], a[40:43]
v_mfma_f32_16x16x32_bf16 a[44:47], v[44:47], v[28:31], a[44:47]
v_mfma_f32_16x16x32_bf16 a[48:51], v[48:51], v[28:31], a[48:51]
s_add_u32 m0, s83, 3200
v_mfma_f32_16x16x32_bf16 a[52:55], v[52:55], v[28:31], a[52:55]
buffer_load_dwordx4 v235, s[44:47], s80 offen lds
v_mfma_f32_16x16x32_bf16 a[56:59], v[56:59], v[28:31], a[56:59]
v_mfma_f32_16x16x32_bf16 a[60:63], v[60:63], v[28:31], a[60:63]
s_add_u32 s80, s80, s81
s_waitcnt lgkmcnt(0)
s_waitcnt vmcnt(12)
s_barrier
v_mfma_f32_16x16x32_bf16 a[224:227], v[192:195], v[128:131], a[224:227]
ds_read_b128 v[0:3], v237 offset:57344
s_add_u32 m0, s83, 16896
v_mfma_f32_16x16x32_bf16 a[228:231], v[196:199], v[128:131], a[228:231]
buffer_load_dwordx4 v232, s[44:47], s80 offen lds
v_mfma_f32_16x16x32_bf16 a[232:235], v[200:203], v[128:131], a[232:235]
ds_read_b64_tr_b16 v[32:33], v240 offset:50688
s_nop 0
v_mfma_f32_16x16x32_bf16 a[236:239], v[204:207], v[128:131], a[236:239]
v_lshlrev_b32 v70, 16, v70
v_lshlrev_b32 v88, 16, v72
v_mfma_f32_16x16x32_bf16 a[208:211], v[208:211], v[128:131], a[208:211]
ds_read_b64_tr_b16 v[34:35], v240 offset:51712
v_and_b32 v89, 0xffff0000, v72
s_add_u32 m0, s83, 17920
v_mfma_f32_16x16x32_bf16 a[212:215], v[212:215], v[128:131], a[212:215]
buffer_load_dwordx4 v233, s[44:47], s80 offen lds
v_mfma_f32_16x16x32_bf16 a[128:131], v[216:219], v[128:131], a[128:131]
ds_read_b64_tr_b16 v[36:37], v241 offset:50688
v_sub_f32 v88, v88, v70
v_mfma_f32_16x16x32_bf16 a[132:135], v[220:223], v[128:131], a[132:135]
v_sub_f32 v89, v89, v70
v_add_f32 v120, v88, v89
v_mfma_f32_16x16x32_bf16 a[216:219], v[192:195], v[132:135], a[216:219]
ds_read_b64_tr_b16 v[38:39], v241 offset:51712
v_mul_f32 v122, v88, v88
s_add_u32 m0, s83, 19072
v_mfma_f32_16x16x32_bf16 a[220:223], v[196:199], v[132:135], a[220:223]
buffer_load_dwordx4 v234, s[44:47], s80 offen lds
v_mfma_f32_16x16x32_bf16 a[136:139], v[200:203], v[132:135], a[136:139]
ds_read_b64_tr_b16 v[40:41], v242 offset:50688
v_mul_f32 v123, v89, v89
v_mfma_f32_16x16x32_bf16 a[140:143], v[204:207], v[132:135], a[140:143]
v_lshlrev_b32 v90, 16, v73
v_and_b32 v91, 0xffff0000, v73
v_mfma_f32_16x16x32_bf16 a[192:195], v[208:211], v[132:135], a[192:195]
ds_read_b64_tr_b16 v[42:43], v242 offset:51712
v_sub_f32 v90, v90, v70
.La5_LW1_0:
s_cbranch_execz .La5_LW1_4
s_add_u32 m0, s83, 20096
v_mfma_f32_16x16x32_bf16 a[196:199], v[212:215], v[132:135], a[196:199]
buffer_load_dwordx4 v235, s[44:47], s80 offen lds
v_mfma_f32_16x16x32_bf16 a[144:147], v[216:219], v[132:135], a[144:147]
ds_read_b64_tr_b16 v[44:45], v243 offset:50688
v_sub_f32 v91, v91, v70
v_mfma_f32_16x16x32_bf16 a[148:151], v[220:223], v[132:135], a[148:151]
s_add_u32 s80, s80, s81
v_add_f32 v124, v90, v91
v_mfma_f32_16x16x32_bf16 a[200:203], v[192:195], v[136:139], a[200:203]
ds_read_b64_tr_b16 v[46:47], v243 offset:51712
v_fmac_f32 v122, v90, v90
s_add_u32 m0, s83, 33792
v_mfma_f32_16x16x32_bf16 a[204:207], v[196:199], v[136:139], a[204:207]
buffer_load_dwordx4 v232, s[44:47], s80 offen lds
v_mfma_f32_16x16x32_bf16 a[152:155], v[200:203], v[136:139], a[152:155]
ds_read_b64_tr_b16 v[48:49], v240 offset:50816
v_add_f32 v120, v120, v124
v_mfma_f32_16x16x32_bf16 a[156:159], v[204:207], v[136:139], a[156:159]
v_fmac_f32 v123, v91, v91
v_lshlrev_b32 v92, 16, v74
v_mfma_f32_16x16x32_bf16 a[96:99], v[208:211], v[136:139], a[96:99]
ds_read_b64_tr_b16 v[50:51], v240 offset:51840
v_and_b32 v93, 0xffff0000, v74
s_add_u32 m0, s83, 34816
v_mfma_f32_16x16x32_bf16 a[100:103], v[212:215], v[136:139], a[100:103]
buffer_load_dwordx4 v233, s[44:47], s80 offen lds
v_mfma_f32_16x16x32_bf16 a[160:163], v[216:219], v[136:139], a[160:163]
ds_read_b64_tr_b16 v[52:53], v241 offset:50816
v_sub_f32 v92, v92, v70
v_mfma_f32_16x16x32_bf16 a[164:167], v[220:223], v[136:139], a[164:167]
v_sub_f32 v93, v93, v70
v_add_f32 v124, v92, v93
v_mfma_f32_16x16x32_bf16 a[104:107], v[192:195], v[140:143], a[104:107]
ds_read_b64_tr_b16 v[54:55], v241 offset:51840
v_fmac_f32 v122, v92, v92
s_add_u32 m0, s83, 35968
v_mfma_f32_16x16x32_bf16 a[108:111], v[196:199], v[140:143], a[108:111]
buffer_load_dwordx4 v234, s[44:47], s80 offen lds
v_mfma_f32_16x16x32_bf16 a[168:171], v[200:203], v[140:143], a[168:171]
ds_read_b64_tr_b16 v[56:57], v242 offset:50816
v_add_f32 v120, v120, v124
v_mfma_f32_16x16x32_bf16 a[172:175], v[204:207], v[140:143], a[172:175]
v_fmac_f32 v123, v93, v93
v_lshlrev_b32 v94, 16, v75
v_mfma_f32_16x16x32_bf16 a[112:115], v[208:211], v[140:143], a[112:115]
ds_read_b64_tr_b16 v[58:59], v242 offset:51840
v_and_b32 v95, 0xffff0000, v75
s_add_u32 m0, s83, 36992
v_mfma_f32_16x16x32_bf16 a[116:119], v[212:215], v[140:143], a[116:119]
buffer_load_dwordx4 v235, s[44:47], s80 offen lds
v_mfma_f32_16x16x32_bf16 a[176:179], v[216:219], v[140:143], a[176:179]
ds_read_b64_tr_b16 v[60:61], v243 offset:50816
v_sub_f32 v94, v94, v70
v_mfma_f32_16x16x32_bf16 a[180:183], v[220:223], v[140:143], a[180:183]
s_add_u32 s80, s80, s81
v_sub_f32 v95, v95, v70
v_mfma_f32_16x16x32_bf16 a[120:123], v[192:195], v[144:147], a[120:123]
ds_read_b64_tr_b16 v[62:63], v243 offset:51840
v_add_f32 v124, v94, v95
v_mfma_f32_16x16x32_bf16 a[124:127], v[196:199], v[144:147], a[124:127]
buffer_load_dword v254, v164, s[44:47], s86 offen
v_fmac_f32 v122, v94, v94
v_mfma_f32_16x16x32_bf16 a[184:187], v[200:203], v[144:147], a[184:187]
ds_read_b128 v[4:7], v237 offset:59392
v_add_f32 v120, v120, v124
v_mfma_f32_16x16x32_bf16 a[188:191], v[204:207], v[144:147], a[188:191]
s_xor_b32 s82, s82, 32768
s_add_u32 s83, s83, 50688
v_mfma_f32_16x16x32_bf16 a[64:67], v[208:211], v[144:147], a[64:67]
ds_read_b128 v[8:11], v237 offset:49152
s_add_u32 s86, s86, 2097152
v_mfma_f32_16x16x32_bf16 a[68:71], v[212:215], v[144:147], a[68:71]
v_fmac_f32 v123, v95, v95
v_lshlrev_b32 v96, 16, v76
v_mfma_f32_16x16x32_bf16 a[0:3], v[216:219], v[144:147], a[0:3]
ds_read_b128 v[12:15], v237 offset:51200
v_and_b32 v97, 0xffff0000, v76
v_mfma_f32_16x16x32_bf16 a[4:7], v[220:223], v[144:147], a[4:7]
v_sub_f32 v96, v96, v70
v_sub_f32 v97, v97, v70
v_mfma_f32_16x16x32_bf16 a[72:75], v[192:195], v[148:151], a[72:75]
ds_read_b128 v[16:19], v237 offset:40960
v_add_f32 v124, v96, v97
v_mfma_f32_16x16x32_bf16 a[76:79], v[196:199], v[148:151], a[76:79]
v_fmac_f32 v122, v96, v96
v_add_f32 v120, v120, v124
v_mfma_f32_16x16x32_bf16 a[8:11], v[200:203], v[148:151], a[8:11]
ds_read_b128 v[20:23], v237 offset:43008
v_fmac_f32 v123, v97, v97
v_mfma_f32_16x16x32_bf16 a[12:15], v[204:207], v[148:151], a[12:15]
v_lshlrev_b32 v98, 16, v77
v_and_b32 v99, 0xffff0000, v77
v_mfma_f32_16x16x32_bf16 a[80:83], v[208:211], v[148:151], a[80:83]
ds_read_b128 v[24:27], v237 offset:32768
v_sub_f32 v98, v98, v70
v_mfma_f32_16x16x32_bf16 a[84:87], v[212:215], v[148:151], a[84:87]
v_sub_f32 v99, v99, v70
v_add_f32 v124, v98, v99
v_mfma_f32_16x16x32_bf16 a[16:19], v[216:219], v[148:151], a[16:19]
ds_read_b128 v[28:31], v237 offset:34816
v_fmac_f32 v122, v98, v98
v_mfma_f32_16x16x32_bf16 a[20:23], v[220:223], v[148:151], a[20:23]
v_add_f32 v120, v120, v124
v_fmac_f32 v123, v99, v99
v_mfma_f32_16x16x32_bf16 a[88:91], v[192:195], v[152:155], a[88:91]
v_lshlrev_b32 v100, 16, v78
v_and_b32 v101, 0xffff0000, v78
v_mfma_f32_16x16x32_bf16 a[92:95], v[196:199], v[152:155], a[92:95]
v_sub_f32 v100, v100, v70
v_sub_f32 v101, v101, v70
v_mfma_f32_16x16x32_bf16 a[24:27], v[200:203], v[152:155], a[24:27]
v_add_f32 v124, v100, v101
v_fmac_f32 v122, v100, v100
v_mfma_f32_16x16x32_bf16 a[28:31], v[204:207], v[152:155], a[28:31]
v_add_f32 v120, v120, v124
v_fmac_f32 v123, v101, v101
v_mfma_f32_16x16x32_bf16 a[240:243], v[208:211], v[152:155], a[240:243]
v_lshlrev_b32 v102, 16, v79
v_and_b32 v103, 0xffff0000, v79
v_mfma_f32_16x16x32_bf16 a[244:247], v[212:215], v[152:155], a[244:247]
v_sub_f32 v102, v102, v70
v_sub_f32 v103, v103, v70
v_mfma_f32_16x16x32_bf16 a[32:35], v[216:219], v[152:155], a[32:35]
v_add_f32 v124, v102, v103
v_fmac_f32 v122, v102, v102
v_mfma_f32_16x16x32_bf16 a[36:39], v[220:223], v[152:155], a[36:39]
v_add_f32 v120, v120, v124
v_fmac_f32 v123, v103, v103
v_mfma_f32_16x16x32_bf16 a[248:251], v[192:195], v[156:159], a[248:251]
v_lshlrev_b32 v104, 16, v80
v_and_b32 v105, 0xffff0000, v80
v_mfma_f32_16x16x32_bf16 a[252:255], v[196:199], v[156:159], a[252:255]
v_sub_f32 v104, v104, v70
v_sub_f32 v105, v105, v70
v_mfma_f32_16x16x32_bf16 a[40:43], v[200:203], v[156:159], a[40:43]
v_add_f32 v124, v104, v105
v_fmac_f32 v122, v104, v104
v_mfma_f32_16x16x32_bf16 a[44:47], v[204:207], v[156:159], a[44:47]
v_add_f32 v120, v120, v124
v_fmac_f32 v123, v105, v105
v_mfma_f32_16x16x32_bf16 a[48:51], v[208:211], v[156:159], a[48:51]
v_lshlrev_b32 v106, 16, v81
v_and_b32 v107, 0xffff0000, v81
v_mfma_f32_16x16x32_bf16 a[52:55], v[212:215], v[156:159], a[52:55]
v_sub_f32 v106, v106, v70
v_sub_f32 v107, v107, v70
v_mfma_f32_16x16x32_bf16 a[56:59], v[216:219], v[156:159], a[56:59]
v_add_f32 v124, v106, v107
v_fmac_f32 v122, v106, v106
v_mfma_f32_16x16x32_bf16 a[60:63], v[220:223], v[156:159], a[60:63]
v_add_f32 v120, v120, v124
v_fmac_f32 v123, v107, v107
s_waitcnt lgkmcnt(0)
s_waitcnt vmcnt(9)
s_barrier
v_mfma_f32_16x16x32_bf16 a[224:227], v[32:35], v[0:3], a[224:227]
ds_read_b128 v[128:131], v236 offset:24576
s_xor_b32 m0, s82, 0
v_mfma_f32_16x16x32_bf16 a[228:231], v[36:39], v[0:3], a[228:231]
buffer_load_dwordx4 v224, s[40:43], s79 offen lds
v_mfma_f32_16x16x32_bf16 a[232:235], v[40:43], v[0:3], a[232:235]
ds_read_b64_tr_b16 v[192:193], v240 offset:0
v_lshlrev_b32 v108, 16, v82
v_mfma_f32_16x16x32_bf16 a[236:239], v[44:47], v[0:3], a[236:239]
v_and_b32 v109, 0xffff0000, v82
v_sub_f32 v108, v108, v70
v_mfma_f32_16x16x32_bf16 a[208:211], v[48:51], v[0:3], a[208:211]
ds_read_b64_tr_b16 v[194:195], v240 offset:1024
v_sub_f32 v109, v109, v70
v_mfma_f32_16x16x32_bf16 a[212:215], v[52:55], v[0:3], a[212:215]
v_add_f32 v124, v108, v109
v_fmac_f32 v122, v108, v108
v_mfma_f32_16x16x32_bf16 a[128:131], v[56:59], v[0:3], a[128:131]
ds_read_b64_tr_b16 v[196:197], v241 offset:0
v_add_f32 v120, v120, v124
v_mfma_f32_16x16x32_bf16 a[132:135], v[60:63], v[0:3], a[132:135]
v_fmac_f32 v123, v109, v109
v_lshlrev_b32 v110, 16, v83
v_mfma_f32_16x16x32_bf16 a[216:219], v[32:35], v[4:7], a[216:219]
ds_read_b64_tr_b16 v[198:199], v241 offset:1024
v_and_b32 v111, 0xffff0000, v83
s_xor_b32 m0, s82, 1024
v_mfma_f32_16x16x32_bf16 a[220:223], v[36:39], v[4:7], a[220:223]
buffer_load_dwordx4 v225, s[40:43], s79 offen lds
v_mfma_f32_16x16x32_bf16 a[136:139], v[40:43], v[4:7], a[136:139]
ds_read_b64_tr_b16 v[200:201], v242 offset:0
v_sub_f32 v110, v110, v70
v_mfma_f32_16x16x32_bf16 a[140:143], v[44:47], v[4:7], a[140:143]
v_sub_f32 v111, v111, v70
v_add_f32 v124, v110, v111
v_mfma_f32_16x16x32_bf16 a[192:195], v[48:51], v[4:7], a[192:195]
ds_read_b64_tr_b16 v[202:203], v242 offset:1024
v_fmac_f32 v122, v110, v110
v_mfma_f32_16x16x32_bf16 a[196:199], v[52:55], v[4:7], a[196:199]
v_add_f32 v120, v120, v124
v_fmac_f32 v123, v111, v111
v_mfma_f32_16x16x32_bf16 a[144:147], v[56:59], v[4:7], a[144:147]
ds_read_b64_tr_b16 v[204:205], v243 offset:0
v_lshlrev_b32 v112, 16, v84
v_mfma_f32_16x16x32_bf16 a[148:151], v[60:63], v[4:7], a[148:151]
v_and_b32 v113, 0xffff0000, v84
v_sub_f32 v112, v112, v70
v_mfma_f32_16x16x32_bf16 a[200:203], v[32:35], v[8:11], a[200:203]
ds_read_b64_tr_b16 v[206:207], v243 offset:1024
v_sub_f32 v113, v113, v70
s_xor_b32 m0, s82, 2048
v_mfma_f32_16x16x32_bf16 a[204:207], v[36:39], v[8:11], a[204:207]
buffer_load_dwordx4 v226, s[40:43], s79 offen lds
v_mfma_f32_16x16x32_bf16 a[152:155], v[40:43], v[8:11], a[152:155]
ds_read_b64_tr_b16 v[208:209], v240 offset:128
v_add_f32 v124, v112, v113
v_mfma_f32_16x16x32_bf16 a[156:159], v[44:47], v[8:11], a[156:159]
v_fmac_f32 v122, v112, v112
v_add_f32 v120, v120, v124
v_mfma_f32_16x16x32_bf16 a[96:99], v[48:51], v[8:11], a[96:99]
ds_read_b64_tr_b16 v[210:211], v240 offset:1152
v_fmac_f32 v123, v113, v113
s_add_u32 m0, s83, 0
v_mfma_f32_16x16x32_bf16 a[100:103], v[52:55], v[8:11], a[100:103]
buffer_load_dwordx4 v232, s[44:47], s80 offen lds
v_mfma_f32_16x16x32_bf16 a[160:163], v[56:59], v[8:11], a[160:163]
ds_read_b64_tr_b16 v[212:213], v241 offset:128
v_lshlrev_b32 v114, 16, v85
v_mfma_f32_16x16x32_bf16 a[164:167], v[60:63], v[8:11], a[164:167]
v_and_b32 v115, 0xffff0000, v85
v_sub_f32 v114, v114, v70
v_mfma_f32_16x16x32_bf16 a[104:107], v[32:35], v[12:15], a[104:107]
ds_read_b64_tr_b16 v[214:215], v241 offset:1152
v_sub_f32 v115, v115, v70
s_xor_b32 m0, s82, 3072
v_mfma_f32_16x16x32_bf16 a[108:111], v[36:39], v[12:15], a[108:111]
buffer_load_dwordx4 v227, s[40:43], s79 offen lds
v_mfma_f32_16x16x32_bf16 a[168:171], v[40:43], v[12:15], a[168:171]
ds_read_b64_tr_b16 v[216:217], v242 offset:128
v_add_f32 v124, v114, v115
v_mfma_f32_16x16x32_bf16 a[172:175], v[44:47], v[12:15], a[172:175]
v_fmac_f32 v122, v114, v114
v_add_f32 v120, v120, v124
v_mfma_f32_16x16x32_bf16 a[112:115], v[48:51], v[12:15], a[112:115]
ds_read_b64_tr_b16 v[218:219], v242 offset:1152
v_fmac_f32 v123, v115, v115
s_add_u32 m0, s83, 1024
v_mfma_f32_16x16x32_bf16 a[116:119], v[52:55], v[12:15], a[116:119]
buffer_load_dwordx4 v233, s[44:47], s80 offen lds
v_mfma_f32_16x16x32_bf16 a[176:179], v[56:59], v[12:15], a[176:179]
ds_read_b64_tr_b16 v[220:221], v243 offset:128
v_lshlrev_b32 v116, 16, v86
v_mfma_f32_16x16x32_bf16 a[180:183], v[60:63], v[12:15], a[180:183]
v_and_b32 v117, 0xffff0000, v86
v_sub_f32 v116, v116, v70
v_mfma_f32_16x16x32_bf16 a[120:123], v[32:35], v[16:19], a[120:123]
ds_read_b64_tr_b16 v[222:223], v243 offset:1152
v_sub_f32 v117, v117, v70
s_xor_b32 m0, s82, 4096
v_mfma_f32_16x16x32_bf16 a[124:127], v[36:39], v[16:19], a[124:127]
buffer_load_dwordx4 v228, s[40:43], s79 offen lds
v_mfma_f32_16x16x32_bf16 a[184:187], v[40:43], v[16:19], a[184:187]
ds_read_b128 v[132:135], v236 offset:26624
v_add_f32 v124, v116, v117
v_mfma_f32_16x16x32_bf16 a[188:191], v[44:47], v[16:19], a[188:191]
v_fmac_f32 v122, v116, v116
v_add_f32 v120, v120, v124
v_mfma_f32_16x16x32_bf16 a[64:67], v[48:51], v[16:19], a[64:67]
ds_read_b128 v[136:139], v236 offset:16384
v_fmac_f32 v123, v117, v117
s_add_u32 m0, s83, 2176
v_mfma_f32_16x16x32_bf16 a[68:71], v[52:55], v[16:19], a[68:71]
buffer_load_dwordx4 v234, s[44:47], s80 offen lds
v_mfma_f32_16x16x32_bf16 a[0:3], v[56:59], v[16:19], a[0:3]
ds_read_b128 v[140:143], v236 offset:18432
v_lshlrev_b32 v118, 16, v87
v_mfma_f32_16x16x32_bf16 a[4:7], v[60:63], v[16:19], a[4:7]
v_and_b32 v119, 0xffff0000, v87
v_sub_f32 v118, v118, v70
v_mfma_f32_16x16x32_bf16 a[72:75], v[32:35], v[20:23], a[72:75]
ds_read_b128 v[144:147], v236 offset:8192
v_sub_f32 v119, v119, v70
s_xor_b32 m0, s82, 5120
v_mfma_f32_16x16x32_bf16 a[76:79], v[36:39], v[20:23], a[76:79]
buffer_load_dwordx4 v229, s[40:43], s79 offen lds
v_mfma_f32_16x16x32_bf16 a[8:11], v[40:43], v[20:23], a[8:11]
ds_read_b128 v[148:151], v236 offset:10240
v_add_f32 v124, v118, v119
v_mfma_f32_16x16x32_bf16 a[12:15], v[44:47], v[20:23], a[12:15]
v_fmac_f32 v122, v118, v118
v_add_f32 v120, v120, v124
v_mfma_f32_16x16x32_bf16 a[80:83], v[48:51], v[20:23], a[80:83]
ds_read_b128 v[152:155], v236 offset:0
v_fmac_f32 v123, v119, v119
s_add_u32 m0, s83, 3200
v_mfma_f32_16x16x32_bf16 a[84:87], v[52:55], v[20:23], a[84:87]
buffer_load_dwordx4 v235, s[44:47], s80 offen lds
v_mfma_f32_16x16x32_bf16 a[16:19], v[56:59], v[20:23], a[16:19]
ds_read_b128 v[156:159], v236 offset:2048
v_add_f32 v121, v122, v123
v_mfma_f32_16x16x32_bf16 a[20:23], v[60:63], v[20:23], a[20:23]
s_add_u32 s80, s80, s81
s_add_u32 s83, s83, 16896
v_mfma_f32_16x16x32_bf16 a[88:91], v[32:35], v[24:27], a[88:91]
v_add_f32_dpp v120, v120, v120 row_ror:8 row_mask:0xf bank_mask:0xf
s_xor_b32 m0, s82, 6144
v_mfma_f32_16x16x32_bf16 a[92:95], v[36:39], v[24:27], a[92:95]
buffer_load_dwordx4 v230, s[40:43], s79 offen lds
s_cmp_eq_u32 s83, s97
s_cselect_b32 s83, s90, s83
v_mfma_f32_16x16x32_bf16 a[24:27], v[40:43], v[24:27], a[24:27]
v_add_f32_dpp v121, v121, v121 row_ror:8 row_mask:0xf bank_mask:0xf
v_mov_b32 v124, v120
v_mfma_f32_16x16x32_bf16 a[28:31], v[44:47], v[24:27], a[28:31]
v_mov_b32 v125, v121
v_mfma_f32_16x16x32_bf16 a[240:243], v[48:51], v[24:27], a[240:243]
v_mfma_f32_16x16x32_bf16 a[244:247], v[52:55], v[24:27], a[244:247]
v_permlane16_swap_b32 v124, v120
v_permlane16_swap_b32 v125, v121
v_mfma_f32_16x16x32_bf16 a[32:35], v[56:59], v[24:27], a[32:35]
v_add_f32 v120, v120, v124
v_add_f32 v121, v121, v125
v_mfma_f32_16x16x32_bf16 a[36:39], v[60:63], v[24:27], a[36:39]
v_mov_b32 v124, v120
v_mov_b32 v125, v121
v_mfma_f32_16x16x32_bf16 a[248:251], v[32:35], v[28:31], a[248:251]
s_xor_b32 m0, s82, 7168
v_mfma_f32_16x16x32_bf16 a[252:255], v[36:39], v[28:31], a[252:255]
buffer_load_dwordx4 v231, s[40:43], s79 offen lds
v_mfma_f32_16x16x32_bf16 a[40:43], v[40:43], v[28:31], a[40:43]
v_permlane32_swap_b32 v124, v120
v_permlane32_swap_b32 v125, v121
v_mfma_f32_16x16x32_bf16 a[44:47], v[44:47], v[28:31], a[44:47]
s_add_u32 s79, s79, 128
s_xor_b32 s82, s82, 32768
v_mfma_f32_16x16x32_bf16 a[48:51], v[48:51], v[28:31], a[48:51]
v_add_f32 v120, v120, v124
v_add_f32 v121, v121, v125
v_mfma_f32_16x16x32_bf16 a[52:55], v[52:55], v[28:31], a[52:55]
ds_write_b64 v127, v[120:121]
s_nop 0
v_mfma_f32_16x16x32_bf16 a[56:59], v[56:59], v[28:31], a[56:59]
v_readfirstlane_b32 s92, v252
v_readfirstlane_b32 s93, v253
v_mfma_f32_16x16x32_bf16 a[60:63], v[60:63], v[28:31], a[60:63]
s_add_u32 s92, s92, 1
s_addc_u32 s93, s93, 0
s_waitcnt lgkmcnt(0)
s_waitcnt vmcnt(17)
s_barrier
v_mfma_f32_16x16x32_bf16 a[224:227], v[192:195], v[128:131], a[224:227]
ds_read_b128 v[0:3], v237 offset:24576
ds_read_b128 v[72:75], v126
v_mfma_f32_16x16x32_bf16 a[228:231], v[196:199], v[128:131], a[228:231]
ds_read_b128 v[76:79], v126 offset:16
v_mfma_f32_16x16x32_bf16 a[232:235], v[200:203], v[128:131], a[232:235]
ds_read_b64_tr_b16 v[32:33], v240 offset:16896
v_mfma_f32_16x16x32_bf16 a[236:239], v[204:207], v[128:131], a[236:239]
v_mfma_f32_16x16x32_bf16 a[208:211], v[208:211], v[128:131], a[208:211]
ds_read_b64_tr_b16 v[34:35], v240 offset:17920
v_mov_b32 v252, s92
v_mfma_f32_16x16x32_bf16 a[212:215], v[212:215], v[128:131], a[212:215]
v_mov_b32 v253, s93
v_mov_b32 v160, 0
v_mfma_f32_16x16x32_bf16 a[128:131], v[216:219], v[128:131], a[128:131]
ds_read_b64_tr_b16 v[36:37], v241 offset:16896
v_mov_b32 v161, 0
v_mfma_f32_16x16x32_bf16 a[132:135], v[220:223], v[128:131], a[132:135]
v_mov_b32 v162, 0
v_mov_b32 v163, 0
v_mfma_f32_16x16x32_bf16 a[216:219], v[192:195], v[132:135], a[216:219]
ds_read_b64_tr_b16 v[38:39], v241 offset:17920
buffer_store_dwordx4 v[160:163], v255, s[52:55], s95 offen sc1
v_mfma_f32_16x16x32_bf16 a[220:223], v[196:199], v[132:135], a[220:223]
s_waitcnt lgkmcnt(4)
v_add_f32 v72, v72, v74
v_mfma_f32_16x16x32_bf16 a[136:139], v[200:203], v[132:135], a[136:139]
ds_read_b64_tr_b16 v[40:41], v242 offset:16896
v_add_f32 v73, v73, v75
v_mfma_f32_16x16x32_bf16 a[140:143], v[204:207], v[132:135], a[140:143]
v_add_f32 v76, v76, v78
v_add_f32 v77, v77, v79
v_mfma_f32_16x16x32_bf16 a[192:195], v[208:211], v[132:135], a[192:195]
ds_read_b64_tr_b16 v[42:43], v242 offset:17920
v_add_f32 v72, v72, v76
v_mfma_f32_16x16x32_bf16 a[196:199], v[212:215], v[132:135], a[196:199]
v_add_f32 v73, v73, v77
v_mul_f32 v72, 0x3a800000, v72
v_mfma_f32_16x16x32_bf16 a[144:147], v[216:219], v[132:135], a[144:147]
ds_read_b64_tr_b16 v[44:45], v243 offset:16896
v_mul_f32 v73, 0x3a800000, v73
v_mfma_f32_16x16x32_bf16 a[148:151], v[220:223], v[132:135], a[148:151]
v_add_f32 v124, v70, v72
v_fma_f32 v125, -v72, v72, v73
v_mfma_f32_16x16x32_bf16 a[200:203], v[192:195], v[136:139], a[200:203]
ds_read_b64_tr_b16 v[46:47], v243 offset:17920
v_cvt_pk_bf16_f32 v124, v124, v124
v_mfma_f32_16x16x32_bf16 a[204:207], v[196:199], v[136:139], a[204:207]
v_lshlrev_b32 v124, 16, v124
v_max_f32 v125, 0, v125
v_mfma_f32_16x16x32_bf16 a[152:155], v[200:203], v[136:139], a[152:155]
ds_read_b64_tr_b16 v[48:49], v240 offset:17024
v_cvt_pk_bf16_f32 v125, v125, v125
v_mfma_f32_16x16x32_bf16 a[156:159], v[204:207], v[136:139], a[156:159]
v_lshlrev_b32 v125, 16, v125
v_add_f32 v125, 0x3727c5ac, v125
v_mfma_f32_16x16x32_bf16 a[96:99], v[208:211], v[136:139], a[96:99]
ds_read_b64_tr_b16 v[50:51], v240 offset:18048
v_cvt_pk_bf16_f32 v125, v125, v125
v_mfma_f32_16x16x32_bf16 a[100:103], v[212:215], v[136:139], a[100:103]
v_lshlrev_b32 v125, 16, v125
v_mfma_f32_16x16x32_bf16 a[160:163], v[216:219], v[136:139], a[160:163]
ds_read_b64_tr_b16 v[52:53], v241 offset:17024
v_mfma_f32_16x16x32_bf16 a[164:167], v[220:223], v[136:139], a[164:167]
v_sqrt_f32 v125, v125
v_mfma_f32_16x16x32_bf16 a[104:107], v[192:195], v[140:143], a[104:107]
ds_read_b64_tr_b16 v[54:55], v241 offset:18048
v_sub_f32 v124, v70, v124
v_mfma_f32_16x16x32_bf16 a[108:111], v[196:199], v[140:143], a[108:111]
v_cvt_pk_bf16_f32 v125, v125, v125
v_lshlrev_b32 v125, 16, v125
v_mfma_f32_16x16x32_bf16 a[168:171], v[200:203], v[140:143], a[168:171]
ds_read_b64_tr_b16 v[56:57], v242 offset:17024
v_mfma_f32_16x16x32_bf16 a[172:175], v[204:207], v[140:143], a[172:175]
v_rcp_f32 v125, v125
v_mfma_f32_16x16x32_bf16 a[112:115], v[208:211], v[140:143], a[112:115]
ds_read_b64_tr_b16 v[58:59], v242 offset:18048
v_mul_f32 v125, 0x3e0293ee, v125
v_mfma_f32_16x16x32_bf16 a[116:119], v[212:215], v[140:143], a[116:119]
v_mul_f32 v124, v124, v125
v_fma_f32 v88, v88, v125, v124
v_mfma_f32_16x16x32_bf16 a[176:179], v[216:219], v[140:143], a[176:179]
ds_read_b64_tr_b16 v[60:61], v243 offset:17024
v_fma_f32 v89, v89, v125, v124
v_mfma_f32_16x16x32_bf16 a[180:183], v[220:223], v[140:143], a[180:183]
v_cvt_pk_bf16_f32 v72, v88, v89
v_fma_f32 v90, v90, v125, v124
v_mfma_f32_16x16x32_bf16 a[120:123], v[192:195], v[144:147], a[120:123]
ds_read_b64_tr_b16 v[62:63], v243 offset:18048
v_fma_f32 v91, v91, v125, v124
s_add_u32 m0, s83, 0
v_mfma_f32_16x16x32_bf16 a[124:127], v[196:199], v[144:147], a[124:127]
buffer_load_dwordx4 v232, s[44:47], s80 offen lds
v_mfma_f32_16x16x32_bf16 a[184:187], v[200:203], v[144:147], a[184:187]
ds_read_b128 v[4:7], v237 offset:26624
v_cvt_pk_bf16_f32 v73, v90, v91
v_mfma_f32_16x16x32_bf16 a[188:191], v[204:207], v[144:147], a[188:191]
v_fma_f32 v92, v92, v125, v124
v_fma_f32 v93, v93, v125, v124
v_mfma_f32_16x16x32_bf16 a[64:67], v[208:211], v[144:147], a[64:67]
ds_read_b128 v[8:11], v237 offset:16384
v_cvt_pk_bf16_f32 v74, v92, v93
s_add_u32 m0, s83, 1024
v_mfma_f32_16x16x32_bf16 a[68:71], v[212:215], v[144:147], a[68:71]
buffer_load_dwordx4 v233, s[44:47], s80 offen lds
v_mfma_f32_16x16x32_bf16 a[0:3], v[216:219], v[144:147], a[0:3]
ds_read_b128 v[12:15], v237 offset:18432
v_fma_f32 v94, v94, v125, v124
v_mfma_f32_16x16x32_bf16 a[4:7], v[220:223], v[144:147], a[4:7]
v_fma_f32 v95, v95, v125, v124
v_cvt_pk_bf16_f32 v75, v94, v95
v_mfma_f32_16x16x32_bf16 a[72:75], v[192:195], v[148:151], a[72:75]
ds_read_b128 v[16:19], v237 offset:8192
buffer_store_dwordx4 v[72:75], v71, s[56:59], 0 offen offset:0 sc1
v_fma_f32 v96, v96, v125, v124
s_add_u32 m0, s83, 2176
v_mfma_f32_16x16x32_bf16 a[76:79], v[196:199], v[148:151], a[76:79]
buffer_load_dwordx4 v234, s[44:47], s80 offen lds
v_fma_f32 v97, v97, v125, v124
v_mfma_f32_16x16x32_bf16 a[8:11], v[200:203], v[148:151], a[8:11]
ds_read_b128 v[20:23], v237 offset:10240
v_cvt_pk_bf16_f32 v76, v96, v97
v_fma_f32 v98, v98, v125, v124
v_mfma_f32_16x16x32_bf16 a[12:15], v[204:207], v[148:151], a[12:15]
v_fma_f32 v99, v99, v125, v124
v_cvt_pk_bf16_f32 v77, v98, v99
v_fma_f32 v100, v100, v125, v124
v_mfma_f32_16x16x32_bf16 a[80:83], v[208:211], v[148:151], a[80:83]
ds_read_b128 v[24:27], v237 offset:0
v_fma_f32 v101, v101, v125, v124
v_cvt_pk_bf16_f32 v78, v100, v101
s_add_u32 m0, s83, 3200
v_mfma_f32_16x16x32_bf16 a[84:87], v[212:215], v[148:151], a[84:87]
buffer_load_dwordx4 v235, s[44:47], s80 offen lds
v_fma_f32 v102, v102, v125, v124
v_mfma_f32_16x16x32_bf16 a[16:19], v[216:219], v[148:151], a[16:19]
ds_read_b128 v[28:31], v237 offset:2048
v_fma_f32 v103, v103, v125, v124
v_cvt_pk_bf16_f32 v79, v102, v103
v_mfma_f32_16x16x32_bf16 a[20:23], v[220:223], v[148:151], a[20:23]
s_add_u32 s80, s80, s81
s_add_u32 s83, s83, 16896
buffer_store_dwordx4 v[76:79], v71, s[56:59], 0 offen offset:2048 sc1
v_mfma_f32_16x16x32_bf16 a[88:91], v[192:195], v[152:155], a[88:91]
v_fma_f32 v104, v104, v125, v124
v_fma_f32 v105, v105, v125, v124
v_cvt_pk_bf16_f32 v80, v104, v105
v_mfma_f32_16x16x32_bf16 a[92:95], v[196:199], v[152:155], a[92:95]
s_cmp_eq_u32 s83, s97
s_cselect_b32 s83, s90, s83
v_fma_f32 v106, v106, v125, v124
v_mfma_f32_16x16x32_bf16 a[24:27], v[200:203], v[152:155], a[24:27]
v_fma_f32 v107, v107, v125, v124
v_cvt_pk_bf16_f32 v81, v106, v107
v_fma_f32 v108, v108, v125, v124
v_mfma_f32_16x16x32_bf16 a[28:31], v[204:207], v[152:155], a[28:31]
v_fma_f32 v109, v109, v125, v124
v_cvt_pk_bf16_f32 v82, v108, v109
v_fma_f32 v110, v110, v125, v124
v_mfma_f32_16x16x32_bf16 a[240:243], v[208:211], v[152:155], a[240:243]
v_fma_f32 v111, v111, v125, v124
v_cvt_pk_bf16_f32 v83, v110, v111
buffer_store_dwordx4 v[80:83], v71, s[56:59], s88 offen offset:0 sc1
v_mfma_f32_16x16x32_bf16 a[244:247], v[212:215], v[152:155], a[244:247]
v_fma_f32 v112, v112, v125, v124
v_fma_f32 v113, v113, v125, v124
v_cvt_pk_bf16_f32 v84, v112, v113
v_mfma_f32_16x16x32_bf16 a[32:35], v[216:219], v[152:155], a[32:35]
v_fma_f32 v114, v114, v125, v124
v_fma_f32 v115, v115, v125, v124
v_cvt_pk_bf16_f32 v85, v114, v115
v_mfma_f32_16x16x32_bf16 a[36:39], v[220:223], v[152:155], a[36:39]
v_fma_f32 v116, v116, v125, v124
v_fma_f32 v117, v117, v125, v124
v_cvt_pk_bf16_f32 v86, v116, v117
v_mfma_f32_16x16x32_bf16 a[248:251], v[192:195], v[156:159], a[248:251]
v_fma_f32 v118, v118, v125, v124
v_fma_f32 v119, v119, v125, v124
v_cvt_pk_bf16_f32 v87, v118, v119
v_mfma_f32_16x16x32_bf16 a[252:255], v[196:199], v[156:159], a[252:255]
buffer_store_dwordx4 v[84:87], v71, s[56:59], s88 offen offset:2048 sc1
v_mfma_f32_16x16x32_bf16 a[40:43], v[200:203], v[156:159], a[40:43]
v_add_u32 v240, s84, v240
v_add_u32 v241, s84, v241
v_mfma_f32_16x16x32_bf16 a[44:47], v[204:207], v[156:159], a[44:47]
v_add_u32 v242, s84, v242
v_add_u32 v243, s84, v243
v_mfma_f32_16x16x32_bf16 a[48:51], v[208:211], v[156:159], a[48:51]
v_xor_b32 v236, 0x8000, v236
v_xor_b32 v237, 0x8000, v237
v_mfma_f32_16x16x32_bf16 a[52:55], v[212:215], v[156:159], a[52:55]
s_sub_u32 s84, 0, s84
v_mfma_f32_16x16x32_bf16 a[56:59], v[216:219], v[156:159], a[56:59]
v_mfma_f32_16x16x32_bf16 a[60:63], v[220:223], v[156:159], a[60:63]
s_waitcnt lgkmcnt(0)
.p2align 6
.La5_LK_LOOP:
s_waitcnt vmcnt(9)
s_barrier
v_mfma_f32_16x16x32_bf16 a[224:227], v[32:35], v[0:3], a[224:227]
ds_read_b128 v[128:131], v236 offset:24576
s_xor_b32 m0, s82, 0
v_mfma_f32_16x16x32_bf16 a[228:231], v[36:39], v[0:3], a[228:231]
buffer_load_dwordx4 v224, s[40:43], s79 offen lds
v_mfma_f32_16x16x32_bf16 a[232:235], v[40:43], v[0:3], a[232:235]
ds_read_b64_tr_b16 v[192:193], v240 offset:0
v_mfma_f32_16x16x32_bf16 a[236:239], v[44:47], v[0:3], a[236:239]
v_mfma_f32_16x16x32_bf16 a[208:211], v[48:51], v[0:3], a[208:211]
ds_read_b64_tr_b16 v[194:195], v240 offset:1024
s_xor_b32 m0, s82, 1024
v_mfma_f32_16x16x32_bf16 a[212:215], v[52:55], v[0:3], a[212:215]
buffer_load_dwordx4 v225, s[40:43], s79 offen lds
v_mfma_f32_16x16x32_bf16 a[128:131], v[56:59], v[0:3], a[128:131]
ds_read_b64_tr_b16 v[196:197], v241 offset:0
v_mfma_f32_16x16x32_bf16 a[132:135], v[60:63], v[0:3], a[132:135]
v_mfma_f32_16x16x32_bf16 a[216:219], v[32:35], v[4:7], a[216:219]
ds_read_b64_tr_b16 v[198:199], v241 offset:1024
s_xor_b32 m0, s82, 2048
v_mfma_f32_16x16x32_bf16 a[220:223], v[36:39], v[4:7], a[220:223]
buffer_load_dwordx4 v226, s[40:43], s79 offen lds
v_mfma_f32_16x16x32_bf16 a[136:139], v[40:43], v[4:7], a[136:139]
ds_read_b64_tr_b16 v[200:201], v242 offset:0
v_mfma_f32_16x16x32_bf16 a[140:143], v[44:47], v[4:7], a[140:143]
v_mfma_f32_16x16x32_bf16 a[192:195], v[48:51], v[4:7], a[192:195]
ds_read_b64_tr_b16 v[202:203], v242 offset:1024
s_xor_b32 m0, s82, 3072
v_mfma_f32_16x16x32_bf16 a[196:199], v[52:55], v[4:7], a[196:199]
buffer_load_dwordx4 v227, s[40:43], s79 offen lds
v_mfma_f32_16x16x32_bf16 a[144:147], v[56:59], v[4:7], a[144:147]
ds_read_b64_tr_b16 v[204:205], v243 offset:0
v_mfma_f32_16x16x32_bf16 a[148:151], v[60:63], v[4:7], a[148:151]
v_mfma_f32_16x16x32_bf16 a[200:203], v[32:35], v[8:11], a[200:203]
ds_read_b64_tr_b16 v[206:207], v243 offset:1024
s_xor_b32 m0, s82, 4096
v_mfma_f32_16x16x32_bf16 a[204:207], v[36:39], v[8:11], a[204:207]
buffer_load_dwordx4 v228, s[40:43], s79 offen lds
v_mfma_f32_16x16x32_bf16 a[152:155], v[40:43], v[8:11], a[152:155]
ds_read_b64_tr_b16 v[208:209], v240 offset:128
v_mfma_f32_16x16x32_bf16 a[156:159], v[44:47], v[8:11], a[156:159]
v_mfma_f32_16x16x32_bf16 a[96:99], v[48:51], v[8:11], a[96:99]
ds_read_b64_tr_b16 v[210:211], v240 offset:1152
s_xor_b32 m0, s82, 5120
v_mfma_f32_16x16x32_bf16 a[100:103], v[52:55], v[8:11], a[100:103]
buffer_load_dwordx4 v229, s[40:43], s79 offen lds
v_mfma_f32_16x16x32_bf16 a[160:163], v[56:59], v[8:11], a[160:163]
ds_read_b64_tr_b16 v[212:213], v241 offset:128
v_mfma_f32_16x16x32_bf16 a[164:167], v[60:63], v[8:11], a[164:167]
.La5_LW1_1:
s_cbranch_execz .La5_LW1_5
v_mfma_f32_16x16x32_bf16 a[104:107], v[32:35], v[12:15], a[104:107]
ds_read_b64_tr_b16 v[214:215], v241 offset:1152
s_xor_b32 m0, s82, 6144
v_mfma_f32_16x16x32_bf16 a[108:111], v[36:39], v[12:15], a[108:111]
buffer_load_dwordx4 v230, s[40:43], s79 offen lds
v_mfma_f32_16x16x32_bf16 a[168:171], v[40:43], v[12:15], a[168:171]
ds_read_b64_tr_b16 v[216:217], v242 offset:128
v_mfma_f32_16x16x32_bf16 a[172:175], v[44:47], v[12:15], a[172:175]
v_mfma_f32_16x16x32_bf16 a[112:115], v[48:51], v[12:15], a[112:115]
ds_read_b64_tr_b16 v[218:219], v242 offset:1152
s_xor_b32 m0, s82, 7168
v_mfma_f32_16x16x32_bf16 a[116:119], v[52:55], v[12:15], a[116:119]
buffer_load_dwordx4 v231, s[40:43], s79 offen lds
v_mfma_f32_16x16x32_bf16 a[176:179], v[56:59], v[12:15], a[176:179]
ds_read_b64_tr_b16 v[220:221], v243 offset:128
v_mfma_f32_16x16x32_bf16 a[180:183], v[60:63], v[12:15], a[180:183]
s_add_u32 s79, s79, 128
s_xor_b32 s82, s82, 32768
v_mfma_f32_16x16x32_bf16 a[120:123], v[32:35], v[16:19], a[120:123]
ds_read_b64_tr_b16 v[222:223], v243 offset:1152
s_add_u32 m0, s83, 0
v_mfma_f32_16x16x32_bf16 a[124:127], v[36:39], v[16:19], a[124:127]
buffer_load_dwordx4 v232, s[44:47], s80 offen lds
v_mfma_f32_16x16x32_bf16 a[184:187], v[40:43], v[16:19], a[184:187]
ds_read_b128 v[132:135], v236 offset:26624
v_mfma_f32_16x16x32_bf16 a[188:191], v[44:47], v[16:19], a[188:191]
v_mfma_f32_16x16x32_bf16 a[64:67], v[48:51], v[16:19], a[64:67]
ds_read_b128 v[136:139], v236 offset:16384
s_add_u32 m0, s83, 1024
v_mfma_f32_16x16x32_bf16 a[68:71], v[52:55], v[16:19], a[68:71]
buffer_load_dwordx4 v233, s[44:47], s80 offen lds
v_mfma_f32_16x16x32_bf16 a[0:3], v[56:59], v[16:19], a[0:3]
ds_read_b128 v[140:143], v236 offset:18432
v_mfma_f32_16x16x32_bf16 a[4:7], v[60:63], v[16:19], a[4:7]
v_mfma_f32_16x16x32_bf16 a[72:75], v[32:35], v[20:23], a[72:75]
ds_read_b128 v[144:147], v236 offset:8192
s_add_u32 m0, s83, 2176
v_mfma_f32_16x16x32_bf16 a[76:79], v[36:39], v[20:23], a[76:79]
buffer_load_dwordx4 v234, s[44:47], s80 offen lds
v_mfma_f32_16x16x32_bf16 a[8:11], v[40:43], v[20:23], a[8:11]
ds_read_b128 v[148:151], v236 offset:10240
v_mfma_f32_16x16x32_bf16 a[12:15], v[44:47], v[20:23], a[12:15]
v_mfma_f32_16x16x32_bf16 a[80:83], v[48:51], v[20:23], a[80:83]
ds_read_b128 v[152:155], v236 offset:0
s_add_u32 m0, s83, 3200
v_mfma_f32_16x16x32_bf16 a[84:87], v[52:55], v[20:23], a[84:87]
buffer_load_dwordx4 v235, s[44:47], s80 offen lds
v_mfma_f32_16x16x32_bf16 a[16:19], v[56:59], v[20:23], a[16:19]
ds_read_b128 v[156:159], v236 offset:2048
v_mfma_f32_16x16x32_bf16 a[20:23], v[60:63], v[20:23], a[20:23]
s_add_u32 s80, s80, s81
s_add_u32 s83, s83, 16896
v_mfma_f32_16x16x32_bf16 a[88:91], v[32:35], v[24:27], a[88:91]
v_mfma_f32_16x16x32_bf16 a[92:95], v[36:39], v[24:27], a[92:95]
s_cmp_eq_u32 s83, s97
s_cselect_b32 s83, s90, s83
v_mfma_f32_16x16x32_bf16 a[24:27], v[40:43], v[24:27], a[24:27]
v_mfma_f32_16x16x32_bf16 a[28:31], v[44:47], v[24:27], a[28:31]
buffer_load_dword v254, v167, s[68:71], s96 offen
s_add_u32 s96, s96, 1048576
v_mfma_f32_16x16x32_bf16 a[240:243], v[48:51], v[24:27], a[240:243]
v_mfma_f32_16x16x32_bf16 a[244:247], v[52:55], v[24:27], a[244:247]
v_mfma_f32_16x16x32_bf16 a[32:35], v[56:59], v[24:27], a[32:35]
v_mfma_f32_16x16x32_bf16 a[36:39], v[60:63], v[24:27], a[36:39]
buffer_load_dword v254, v167, s[68:71], s96 offen
s_add_u32 s96, s96, 1048576
v_mfma_f32_16x16x32_bf16 a[248:251], v[32:35], v[28:31], a[248:251]
s_lshl_b32 s92, s77, 18
s_cmp_eq_u32 s85, 3
v_mfma_f32_16x16x32_bf16 a[252:255], v[36:39], v[28:31], a[252:255]
s_cselect_b32 s44, s68, s44
s_cselect_b32 s45, s69, s45
v_mfma_f32_16x16x32_bf16 a[40:43], v[40:43], v[28:31], a[40:43]
s_cselect_b32 s46, s70, s46
s_cselect_b32 s81, 524288, s81
v_mfma_f32_16x16x32_bf16 a[44:47], v[44:47], v[28:31], a[44:47]
s_cselect_b32 s42, 0, s42
s_cselect_b32 s80, s92, s80
v_mfma_f32_16x16x32_bf16 a[48:51], v[48:51], v[28:31], a[48:51]
v_mfma_f32_16x16x32_bf16 a[52:55], v[52:55], v[28:31], a[52:55]
v_mfma_f32_16x16x32_bf16 a[56:59], v[56:59], v[28:31], a[56:59]
v_mfma_f32_16x16x32_bf16 a[60:63], v[60:63], v[28:31], a[60:63]
s_waitcnt lgkmcnt(0)
s_waitcnt vmcnt(22)
s_barrier
v_mfma_f32_16x16x32_bf16 a[224:227], v[192:195], v[128:131], a[224:227]
ds_read_b128 v[0:3], v237 offset:24576
v_mfma_f32_16x16x32_bf16 a[228:231], v[196:199], v[128:131], a[228:231]
s_waitcnt vmcnt(14)
buffer_store_dwordx2 v[252:253], off, s[64:67], s91 offset:1024 sc1
v_mfma_f32_16x16x32_bf16 a[232:235], v[200:203], v[128:131], a[232:235]
ds_read_b64_tr_b16 v[32:33], v240 offset:16896
v_mfma_f32_16x16x32_bf16 a[236:239], v[204:207], v[128:131], a[236:239]
s_cmp_eq_u32 s85, 4
s_cselect_b32 s92, s89, 1073741824
v_mfma_f32_16x16x32_bf16 a[208:211], v[208:211], v[128:131], a[208:211]
ds_read_b64_tr_b16 v[34:35], v240 offset:17920
v_mfma_f32_16x16x32_bf16 a[212:215], v[212:215], v[128:131], a[212:215]
buffer_load_dwordx4 v[72:75], v255, s[64:67], s92 offen offset:1024 sc1
v_mfma_f32_16x16x32_bf16 a[128:131], v[216:219], v[128:131], a[128:131]
ds_read_b64_tr_b16 v[36:37], v241 offset:16896
v_mfma_f32_16x16x32_bf16 a[132:135], v[220:223], v[128:131], a[132:135]
s_mov_b32 s91, 1073741824
v_mfma_f32_16x16x32_bf16 a[216:219], v[192:195], v[132:135], a[216:219]
ds_read_b64_tr_b16 v[38:39], v241 offset:17920
v_mfma_f32_16x16x32_bf16 a[220:223], v[196:199], v[132:135], a[220:223]
v_mfma_f32_16x16x32_bf16 a[136:139], v[200:203], v[132:135], a[136:139]
ds_read_b64_tr_b16 v[40:41], v242 offset:16896
s_lshl_b32 s92, s76, 19
v_mfma_f32_16x16x32_bf16 a[140:143], v[204:207], v[132:135], a[140:143]
s_add_u32 s92, s92, 88080384
v_mfma_f32_16x16x32_bf16 a[192:195], v[208:211], v[132:135], a[192:195]
ds_read_b64_tr_b16 v[42:43], v242 offset:17920
v_mfma_f32_16x16x32_bf16 a[196:199], v[212:215], v[132:135], a[196:199]
s_add_u32 s52, s26, s92
s_addc_u32 s53, s27, 0
v_mfma_f32_16x16x32_bf16 a[144:147], v[216:219], v[132:135], a[144:147]
ds_read_b64_tr_b16 v[44:45], v243 offset:16896
s_and_b32 s53, s53, 0xffff
v_mfma_f32_16x16x32_bf16 a[148:151], v[220:223], v[132:135], a[148:151]
s_mov_b32 s54, 524672
s_mov_b32 s55, 0x20000
v_mfma_f32_16x16x32_bf16 a[200:203], v[192:195], v[136:139], a[200:203]
ds_read_b64_tr_b16 v[46:47], v243 offset:17920
s_lshl_b32 s92, s3, 16
v_mfma_f32_16x16x32_bf16 a[204:207], v[196:199], v[136:139], a[204:207]
s_sub_u32 s93, 458752, s92
v_add_u32 v244, s92, v168
v_mfma_f32_16x16x32_bf16 a[152:155], v[200:203], v[136:139], a[152:155]
ds_read_b64_tr_b16 v[48:49], v240 offset:17024
v_add_u32 v246, s93, v168
v_mfma_f32_16x16x32_bf16 a[156:159], v[204:207], v[136:139], a[156:159]
v_add_u32 v245, 0x8000, v244
v_add_u32 v247, 0x8000, v246
v_mfma_f32_16x16x32_bf16 a[96:99], v[208:211], v[136:139], a[96:99]
ds_read_b64_tr_b16 v[50:51], v240 offset:18048
s_lshl_b32 s94, s72, 19
v_mfma_f32_16x16x32_bf16 a[100:103], v[212:215], v[136:139], a[100:103]
s_lshl_b32 s93, s76, 16
s_add_u32 s92, s94, s93
v_mfma_f32_16x16x32_bf16 a[160:163], v[216:219], v[136:139], a[160:163]
ds_read_b64_tr_b16 v[52:53], v241 offset:17024
s_lshl_b32 s93, s77, 13
v_mfma_f32_16x16x32_bf16 a[164:167], v[220:223], v[136:139], a[164:167]
s_add_u32 s92, s92, s93
s_add_u32 s92, s92, 20971520
v_mfma_f32_16x16x32_bf16 a[104:107], v[192:195], v[140:143], a[104:107]
ds_read_b64_tr_b16 v[54:55], v241 offset:18048
v_mfma_f32_16x16x32_bf16 a[108:111], v[196:199], v[140:143], a[108:111]
s_add_u32 s48, s26, s92
s_addc_u32 s49, s27, 0
v_mfma_f32_16x16x32_bf16 a[168:171], v[200:203], v[140:143], a[168:171]
ds_read_b64_tr_b16 v[56:57], v242 offset:17024
s_and_b32 s49, s49, 0xffff
v_mfma_f32_16x16x32_bf16 a[172:175], v[204:207], v[140:143], a[172:175]
s_mov_b32 s50, 57344
s_mov_b32 s51, 0x20000
v_mfma_f32_16x16x32_bf16 a[112:115], v[208:211], v[140:143], a[112:115]
ds_read_b64_tr_b16 v[58:59], v242 offset:18048
s_lshl_b32 s93, s77, 24
v_mfma_f32_16x16x32_bf16 a[116:119], v[212:215], v[140:143], a[116:119]
s_add_u32 s92, s94, s93
s_add_u32 s92, s92, 20971520
v_mfma_f32_16x16x32_bf16 a[176:179], v[216:219], v[140:143], a[176:179]
ds_read_b64_tr_b16 v[60:61], v243 offset:17024
v_mfma_f32_16x16x32_bf16 a[180:183], v[220:223], v[140:143], a[180:183]
s_add_u32 s60, s26, s92
s_addc_u32 s61, s27, 0
v_mfma_f32_16x16x32_bf16 a[120:123], v[192:195], v[144:147], a[120:123]
ds_read_b64_tr_b16 v[62:63], v243 offset:18048
s_and_b32 s61, s61, 0xffff
s_add_u32 m0, s83, 0
v_mfma_f32_16x16x32_bf16 a[124:127], v[196:199], v[144:147], a[124:127]
buffer_load_dwordx4 v232, s[44:47], s80 offen lds
v_mfma_f32_16x16x32_bf16 a[184:187], v[200:203], v[144:147], a[184:187]
ds_read_b128 v[4:7], v237 offset:26624
s_mov_b32 s62, 524288
v_mfma_f32_16x16x32_bf16 a[188:191], v[204:207], v[144:147], a[188:191]
s_mov_b32 s63, 0x20000
s_lshl_b32 s92, s77, 14
v_mfma_f32_16x16x32_bf16 a[64:67], v[208:211], v[144:147], a[64:67]
ds_read_b128 v[8:11], v237 offset:16384
s_sub_u32 s95, 16384, s92
s_add_u32 m0, s83, 1024
v_mfma_f32_16x16x32_bf16 a[68:71], v[212:215], v[144:147], a[68:71]
buffer_load_dwordx4 v233, s[44:47], s80 offen lds
v_mfma_f32_16x16x32_bf16 a[0:3], v[216:219], v[144:147], a[0:3]
ds_read_b128 v[12:15], v237 offset:18432
v_add_u32 v238, s95, v255
v_mfma_f32_16x16x32_bf16 a[4:7], v[220:223], v[144:147], a[4:7]
v_mfma_f32_16x16x32_bf16 a[72:75], v[192:195], v[148:151], a[72:75]
ds_read_b128 v[16:19], v237 offset:8192
s_add_u32 m0, s83, 2176
v_mfma_f32_16x16x32_bf16 a[76:79], v[196:199], v[148:151], a[76:79]
buffer_load_dwordx4 v234, s[44:47], s80 offen lds
v_mfma_f32_16x16x32_bf16 a[8:11], v[200:203], v[148:151], a[8:11]
ds_read_b128 v[20:23], v237 offset:10240
v_mfma_f32_16x16x32_bf16 a[12:15], v[204:207], v[148:151], a[12:15]
v_mfma_f32_16x16x32_bf16 a[80:83], v[208:211], v[148:151], a[80:83]
ds_read_b128 v[24:27], v237 offset:0
s_add_u32 m0, s83, 3200
v_mfma_f32_16x16x32_bf16 a[84:87], v[212:215], v[148:151], a[84:87]
buffer_load_dwordx4 v235, s[44:47], s80 offen lds
v_mfma_f32_16x16x32_bf16 a[16:19], v[216:219], v[148:151], a[16:19]
ds_read_b128 v[28:31], v237 offset:2048
v_mfma_f32_16x16x32_bf16 a[20:23], v[220:223], v[148:151], a[20:23]
s_add_u32 s80, s80, s81
s_add_u32 s83, s83, 16896
v_mfma_f32_16x16x32_bf16 a[88:91], v[192:195], v[152:155], a[88:91]
v_mfma_f32_16x16x32_bf16 a[92:95], v[196:199], v[152:155], a[92:95]
s_cmp_eq_u32 s83, s97
s_cselect_b32 s83, s90, s83
v_mfma_f32_16x16x32_bf16 a[24:27], v[200:203], v[152:155], a[24:27]
v_mfma_f32_16x16x32_bf16 a[28:31], v[204:207], v[152:155], a[28:31]
v_mfma_f32_16x16x32_bf16 a[240:243], v[208:211], v[152:155], a[240:243]
v_mfma_f32_16x16x32_bf16 a[244:247], v[212:215], v[152:155], a[244:247]
v_mfma_f32_16x16x32_bf16 a[32:35], v[216:219], v[152:155], a[32:35]
v_mfma_f32_16x16x32_bf16 a[36:39], v[220:223], v[152:155], a[36:39]
v_mfma_f32_16x16x32_bf16 a[248:251], v[192:195], v[156:159], a[248:251]
v_mfma_f32_16x16x32_bf16 a[252:255], v[196:199], v[156:159], a[252:255]
v_add_u32 v240, s84, v240
v_add_u32 v241, s84, v241
v_mfma_f32_16x16x32_bf16 a[40:43], v[200:203], v[156:159], a[40:43]
v_add_u32 v242, s84, v242
v_add_u32 v243, s84, v243
v_mfma_f32_16x16x32_bf16 a[44:47], v[204:207], v[156:159], a[44:47]
v_xor_b32 v236, 0x8000, v236
v_xor_b32 v237, 0x8000, v237
v_mfma_f32_16x16x32_bf16 a[48:51], v[208:211], v[156:159], a[48:51]
s_sub_u32 s84, 0, s84
s_add_u32 s85, s85, 1
v_mfma_f32_16x16x32_bf16 a[52:55], v[212:215], v[156:159], a[52:55]
s_cmp_lg_u32 s85, 5
v_mfma_f32_16x16x32_bf16 a[56:59], v[216:219], v[156:159], a[56:59]
v_mfma_f32_16x16x32_bf16 a[60:63], v[220:223], v[156:159], a[60:63]
s_waitcnt lgkmcnt(0)
s_cbranch_scc1 .La5_LK_LOOP
s_waitcnt vmcnt(12)
s_barrier
v_mfma_f32_16x16x32_bf16 a[224:227], v[32:35], v[0:3], a[224:227]
s_waitcnt vmcnt(4)
v_mfma_f32_16x16x32_bf16 a[228:231], v[36:39], v[0:3], a[228:231]
v_cmp_eq_u64 vcc, v[72:73], v[252:253]
v_cmp_eq_u64 s[96:97], v[74:75], v[252:253]
s_mov_b32 s91, 0
v_mfma_f32_16x16x32_bf16 a[232:235], v[40:43], v[0:3], a[232:235]
s_and_b64 vcc, vcc, s[96:97]
s_cmp_eq_u64 vcc, -1
s_cbranch_scc0 .La5_LK_XSPIN
.La5_LK_XOK:
v_mfma_f32_16x16x32_bf16 a[236:239], v[44:47], v[0:3], a[236:239]
s_mov_b32 s89, 49152
v_mfma_f32_16x16x32_bf16 a[208:211], v[48:51], v[0:3], a[208:211]
v_mfma_f32_16x16x32_bf16 a[212:215], v[52:55], v[0:3], a[212:215]
buffer_load_dwordx4 v[128:131], v244, s[52:55], s91 offen offset:0 sc1
v_mfma_f32_16x16x32_bf16 a[128:131], v[56:59], v[0:3], a[128:131]
v_mfma_f32_16x16x32_bf16 a[132:135], v[60:63], v[0:3], a[132:135]
v_mfma_f32_16x16x32_bf16 a[216:219], v[32:35], v[4:7], a[216:219]
ds_read_b64_tr_b16 v[192:193], v240 offset:0
v_mfma_f32_16x16x32_bf16 a[220:223], v[36:39], v[4:7], a[220:223]
buffer_load_dwordx4 v[132:135], v245, s[52:55], s91 offen offset:0 sc1
v_mfma_f32_16x16x32_bf16 a[136:139], v[40:43], v[4:7], a[136:139]
ds_read_b64_tr_b16 v[194:195], v240 offset:1024
v_mfma_f32_16x16x32_bf16 a[140:143], v[44:47], v[4:7], a[140:143]
v_mfma_f32_16x16x32_bf16 a[192:195], v[48:51], v[4:7], a[192:195]
ds_read_b64_tr_b16 v[196:197], v241 offset:0
v_mfma_f32_16x16x32_bf16 a[196:199], v[52:55], v[4:7], a[196:199]
buffer_load_dwordx4 v[136:139], v246, s[52:55], s91 offen offset:0 sc1
v_mfma_f32_16x16x32_bf16 a[144:147], v[56:59], v[4:7], a[144:147]
ds_read_b64_tr_b16 v[198:199], v241 offset:1024
v_accvgpr_read_b32 v224, a224
v_mfma_f32_16x16x32_bf16 a[148:151], v[60:63], v[4:7], a[148:151]
v_accvgpr_read_b32 v225, a225
v_accvgpr_read_b32 v226, a226
v_mfma_f32_16x16x32_bf16 a[200:203], v[32:35], v[8:11], a[200:203]
ds_read_b64_tr_b16 v[200:201], v242 offset:0
v_accvgpr_read_b32 v227, a227
v_mfma_f32_16x16x32_bf16 a[204:207], v[36:39], v[8:11], a[204:207]
buffer_load_dwordx4 v[140:143], v247, s[52:55], s91 offen offset:0 sc1
v_accvgpr_read_b32 v228, a228
v_mfma_f32_16x16x32_bf16 a[152:155], v[40:43], v[8:11], a[152:155]
ds_read_b64_tr_b16 v[202:203], v242 offset:1024
v_accvgpr_read_b32 v229, a229
v_mfma_f32_16x16x32_bf16 a[156:159], v[44:47], v[8:11], a[156:159]
v_accvgpr_read_b32 v230, a230
v_accvgpr_read_b32 v231, a231
v_mfma_f32_16x16x32_bf16 a[96:99], v[48:51], v[8:11], a[96:99]
ds_read_b64_tr_b16 v[204:205], v243 offset:0
v_cvt_pk_bf16_f32 v248, v224, v225
v_mfma_f32_16x16x32_bf16 a[100:103], v[52:55], v[8:11], a[100:103]
buffer_load_dwordx4 v[144:147], v244, s[52:55], s91 offen offset:1024 sc1
v_cvt_pk_bf16_f32 v249, v226, v227
v_mfma_f32_16x16x32_bf16 a[160:163], v[56:59], v[8:11], a[160:163]
ds_read_b64_tr_b16 v[206:207], v243 offset:1024
v_cvt_pk_bf16_f32 v250, v228, v229
v_mfma_f32_16x16x32_bf16 a[164:167], v[60:63], v[8:11], a[164:167]
v_cvt_pk_bf16_f32 v251, v230, v231
buffer_store_dwordx4 v[248:251], v255, s[48:51], s89 offen sc1
v_mfma_f32_16x16x32_bf16 a[104:107], v[32:35], v[12:15], a[104:107]
ds_read_b64_tr_b16 v[208:209], v240 offset:128
s_add_u32 s89, s89, 1024
v_mfma_f32_16x16x32_bf16 a[108:111], v[36:39], v[12:15], a[108:111]
buffer_load_dwordx4 v[148:151], v245, s[52:55], s91 offen offset:1024 sc1
ds_write_b128 v238, v[248:251] offset:0
v_mfma_f32_16x16x32_bf16 a[168:171], v[40:43], v[12:15], a[168:171]
ds_read_b64_tr_b16 v[210:211], v240 offset:1152
v_mfma_f32_16x16x32_bf16 a[172:175], v[44:47], v[12:15], a[172:175]
v_mfma_f32_16x16x32_bf16 a[112:115], v[48:51], v[12:15], a[112:115]
ds_read_b64_tr_b16 v[212:213], v241 offset:128
v_mfma_f32_16x16x32_bf16 a[116:119], v[52:55], v[12:15], a[116:119]
buffer_load_dwordx4 v[152:155], v246, s[52:55], s91 offen offset:1024 sc1
v_mfma_f32_16x16x32_bf16 a[176:179], v[56:59], v[12:15], a[176:179]
ds_read_b64_tr_b16 v[214:215], v241 offset:1152
v_accvgpr_read_b32 v224, a232
v_mfma_f32_16x16x32_bf16 a[180:183], v[60:63], v[12:15], a[180:183]
v_accvgpr_read_b32 v225, a233
v_accvgpr_read_b32 v226, a234
v_mfma_f32_16x16x32_bf16 a[120:123], v[32:35], v[16:19], a[120:123]
ds_read_b64_tr_b16 v[216:217], v242 offset:128
v_accvgpr_read_b32 v227, a235
s_add_u32 m0, s83, 0
v_mfma_f32_16x16x32_bf16 a[124:127], v[36:39], v[16:19], a[124:127]
buffer_load_dwordx4 v232, s[44:47], s80 offen lds
v_mfma_f32_16x16x32_bf16 a[184:187], v[40:43], v[16:19], a[184:187]
ds_read_b64_tr_b16 v[218:219], v242 offset:1152
v_accvgpr_read_b32 v228, a236
v_mfma_f32_16x16x32_bf16 a[188:191], v[44:47], v[16:19], a[188:191]
buffer_load_dwordx4 v[156:159], v247, s[52:55], s91 offen offset:1024 sc1
v_accvgpr_read_b32 v229, a237
v_mfma_f32_16x16x32_bf16 a[64:67], v[48:51], v[16:19], a[64:67]
ds_read_b64_tr_b16 v[220:221], v243 offset:128
v_accvgpr_read_b32 v230, a238
s_add_u32 m0, s83, 1024
v_mfma_f32_16x16x32_bf16 a[68:71], v[52:55], v[16:19], a[68:71]
buffer_load_dwordx4 v233, s[44:47], s80 offen lds
v_mfma_f32_16x16x32_bf16 a[0:3], v[56:59], v[16:19], a[0:3]
ds_read_b64_tr_b16 v[222:223], v243 offset:1152
v_accvgpr_read_b32 v231, a239
v_mfma_f32_16x16x32_bf16 a[4:7], v[60:63], v[16:19], a[4:7]
v_cvt_pk_bf16_f32 v248, v224, v225
v_cvt_pk_bf16_f32 v249, v226, v227
v_mfma_f32_16x16x32_bf16 a[72:75], v[32:35], v[20:23], a[72:75]
v_cvt_pk_bf16_f32 v250, v228, v229
v_cvt_pk_bf16_f32 v251, v230, v231
s_add_u32 m0, s83, 2176
v_mfma_f32_16x16x32_bf16 a[76:79], v[36:39], v[20:23], a[76:79]
buffer_load_dwordx4 v234, s[44:47], s80 offen lds
v_mfma_f32_16x16x32_bf16 a[8:11], v[40:43], v[20:23], a[8:11]
buffer_store_dwordx4 v[248:251], v255, s[48:51], s89 offen sc1
s_add_u32 s89, s89, 1024
v_mfma_f32_16x16x32_bf16 a[12:15], v[44:47], v[20:23], a[12:15]
buffer_load_dwordx4 v[160:163], v244, s[52:55], s91 offen offset:2048 sc1
ds_write_b128 v238, v[248:251] offset:1024
v_mfma_f32_16x16x32_bf16 a[80:83], v[48:51], v[20:23], a[80:83]
s_add_u32 m0, s83, 3200
v_mfma_f32_16x16x32_bf16 a[84:87], v[52:55], v[20:23], a[84:87]
buffer_load_dwordx4 v235, s[44:47], s80 offen lds
v_mfma_f32_16x16x32_bf16 a[16:19], v[56:59], v[20:23], a[16:19]
v_accvgpr_read_b32 v224, a208
v_accvgpr_read_b32 v225, a209
v_mfma_f32_16x16x32_bf16 a[20:23], v[60:63], v[20:23], a[20:23]
s_add_u32 s80, s80, s81
s_add_u32 s83, s83, 16896
v_mfma_f32_16x16x32_bf16 a[88:91], v[32:35], v[24:27], a[88:91]
v_accvgpr_read_b32 v226, a210
v_accvgpr_read_b32 v227, a211
v_mfma_f32_16x16x32_bf16 a[92:95], v[36:39], v[24:27], a[92:95]
s_mov_b32 s83, s90
v_accvgpr_read_b32 v228, a212
v_mfma_f32_16x16x32_bf16 a[24:27], v[40:43], v[24:27], a[24:27]
v_accvgpr_read_b32 v229, a213
v_accvgpr_read_b32 v230, a214
v_mfma_f32_16x16x32_bf16 a[28:31], v[44:47], v[24:27], a[28:31]
buffer_load_dwordx4 v[164:167], v245, s[52:55], s91 offen offset:2048 sc1
v_accvgpr_read_b32 v231, a215
v_mfma_f32_16x16x32_bf16 a[240:243], v[48:51], v[24:27], a[240:243]
v_cvt_pk_bf16_f32 v248, v224, v225
v_cvt_pk_bf16_f32 v249, v226, v227
v_mfma_f32_16x16x32_bf16 a[244:247], v[52:55], v[24:27], a[244:247]
v_cvt_pk_bf16_f32 v250, v228, v229
v_cvt_pk_bf16_f32 v251, v230, v231
v_mfma_f32_16x16x32_bf16 a[32:35], v[56:59], v[24:27], a[32:35]
buffer_store_dwordx4 v[248:251], v255, s[48:51], s89 offen sc1
s_add_u32 s89, s89, 1024
v_mfma_f32_16x16x32_bf16 a[36:39], v[60:63], v[24:27], a[36:39]
buffer_load_dwordx4 v[168:171], v246, s[52:55], s91 offen offset:2048 sc1
ds_write_b128 v238, v[248:251] offset:2048
v_mfma_f32_16x16x32_bf16 a[248:251], v[32:35], v[28:31], a[248:251]
v_mfma_f32_16x16x32_bf16 a[252:255], v[36:39], v[28:31], a[252:255]
v_mfma_f32_16x16x32_bf16 a[40:43], v[40:43], v[28:31], a[40:43]
v_mfma_f32_16x16x32_bf16 a[44:47], v[44:47], v[28:31], a[44:47]
buffer_load_dwordx4 v[172:175], v247, s[52:55], s91 offen offset:2048 sc1
v_mfma_f32_16x16x32_bf16 a[48:51], v[48:51], v[28:31], a[48:51]
v_mfma_f32_16x16x32_bf16 a[52:55], v[52:55], v[28:31], a[52:55]
v_mfma_f32_16x16x32_bf16 a[56:59], v[56:59], v[28:31], a[56:59]
v_mfma_f32_16x16x32_bf16 a[60:63], v[60:63], v[28:31], a[60:63]
s_branch .La5_LQ_START
; ---- V tile (waves 1, 3): the same with the operands exchanged (V comes out transposed)
.La5_LV_START:
.La5_LW1_2:
s_cbranch_execz .La5_LW1_6
s_waitcnt vmcnt(1)
s_barrier
v_mfma_f32_16x16x32_bf16 a[224:227], v[128:131], v[192:195], 0
ds_read_b128 v[0:3], v237 offset:24576
s_add_u32 m0, s83, 33792
v_mfma_f32_16x16x32_bf16 a[232:235], v[128:131], v[196:199], 0
buffer_load_dwordx4 v232, s[44:47], s80 offen lds
v_mfma_f32_16x16x32_bf16 a[208:211], v[128:131], v[200:203], 0
ds_read_b64_tr_b16 v[32:33], v240 offset:16896
v_mfma_f32_16x16x32_bf16 a[128:131], v[128:131], v[204:207], 0
v_mfma_f32_16x16x32_bf16 a[216:219], v[128:131], v[208:211], 0
ds_read_b64_tr_b16 v[34:35], v240 offset:17920
s_add_u32 m0, s83, 34816
v_mfma_f32_16x16x32_bf16 a[136:139], v[128:131], v[212:215], 0
buffer_load_dwordx4 v233, s[44:47], s80 offen lds
v_mfma_f32_16x16x32_bf16 a[192:195], v[128:131], v[216:219], 0
ds_read_b64_tr_b16 v[36:37], v241 offset:16896
v_mfma_f32_16x16x32_bf16 a[144:147], v[128:131], v[220:223], 0
v_mfma_f32_16x16x32_bf16 a[228:231], v[132:135], v[192:195], 0
ds_read_b64_tr_b16 v[38:39], v241 offset:17920
s_add_u32 m0, s83, 35968
v_mfma_f32_16x16x32_bf16 a[236:239], v[132:135], v[196:199], 0
buffer_load_dwordx4 v234, s[44:47], s80 offen lds
v_mfma_f32_16x16x32_bf16 a[212:215], v[132:135], v[200:203], 0
ds_read_b64_tr_b16 v[40:41], v242 offset:16896
v_mfma_f32_16x16x32_bf16 a[132:135], v[132:135], v[204:207], 0
v_mfma_f32_16x16x32_bf16 a[220:223], v[132:135], v[208:211], 0
ds_read_b64_tr_b16 v[42:43], v242 offset:17920
s_add_u32 m0, s83, 36992
v_mfma_f32_16x16x32_bf16 a[140:143], v[132:135], v[212:215], 0
buffer_load_dwordx4 v235, s[44:47], s80 offen lds
v_mfma_f32_16x16x32_bf16 a[196:199], v[132:135], v[216:219], 0
ds_read_b64_tr_b16 v[44:45], v243 offset:16896
v_mfma_f32_16x16x32_bf16 a[148:151], v[132:135], v[220:223], 0
s_add_u32 s80, s80, s81
v_mfma_f32_16x16x32_bf16 a[200:203], v[136:139], v[192:195], 0
ds_read_b64_tr_b16 v[46:47], v243 offset:17920
s_xor_b32 m0, s82, 32768
v_mfma_f32_16x16x32_bf16 a[152:155], v[136:139], v[196:199], 0
buffer_load_dwordx4 v224, s[40:43], s79 offen lds
v_mfma_f32_16x16x32_bf16 a[96:99], v[136:139], v[200:203], 0
ds_read_b64_tr_b16 v[48:49], v240 offset:17024
v_mfma_f32_16x16x32_bf16 a[160:163], v[136:139], v[204:207], 0
v_mfma_f32_16x16x32_bf16 a[104:107], v[136:139], v[208:211], 0
ds_read_b64_tr_b16 v[50:51], v240 offset:18048
s_xor_b32 m0, s82, 33792
v_mfma_f32_16x16x32_bf16 a[168:171], v[136:139], v[212:215], 0
buffer_load_dwordx4 v225, s[40:43], s79 offen lds
v_mfma_f32_16x16x32_bf16 a[112:115], v[136:139], v[216:219], 0
ds_read_b64_tr_b16 v[52:53], v241 offset:17024
v_mfma_f32_16x16x32_bf16 a[176:179], v[136:139], v[220:223], 0
v_mfma_f32_16x16x32_bf16 a[204:207], v[140:143], v[192:195], 0
ds_read_b64_tr_b16 v[54:55], v241 offset:18048
s_xor_b32 m0, s82, 34816
v_mfma_f32_16x16x32_bf16 a[156:159], v[140:143], v[196:199], 0
buffer_load_dwordx4 v226, s[40:43], s79 offen lds
v_mfma_f32_16x16x32_bf16 a[100:103], v[140:143], v[200:203], 0
ds_read_b64_tr_b16 v[56:57], v242 offset:17024
v_mfma_f32_16x16x32_bf16 a[164:167], v[140:143], v[204:207], 0
v_mfma_f32_16x16x32_bf16 a[108:111], v[140:143], v[208:211], 0
ds_read_b64_tr_b16 v[58:59], v242 offset:18048
s_xor_b32 m0, s82, 35840
v_mfma_f32_16x16x32_bf16 a[172:175], v[140:143], v[212:215], 0
buffer_load_dwordx4 v227, s[40:43], s79 offen lds
v_mfma_f32_16x16x32_bf16 a[116:119], v[140:143], v[216:219], 0
ds_read_b64_tr_b16 v[60:61], v243 offset:17024
v_mfma_f32_16x16x32_bf16 a[180:183], v[140:143], v[220:223], 0
v_mfma_f32_16x16x32_bf16 a[120:123], v[144:147], v[192:195], 0
ds_read_b64_tr_b16 v[62:63], v243 offset:18048
s_xor_b32 m0, s82, 36864
v_mfma_f32_16x16x32_bf16 a[184:187], v[144:147], v[196:199], 0
buffer_load_dwordx4 v228, s[40:43], s79 offen lds
v_mfma_f32_16x16x32_bf16 a[64:67], v[144:147], v[200:203], 0
ds_read_b128 v[4:7], v237 offset:26624
v_mfma_f32_16x16x32_bf16 a[0:3], v[144:147], v[204:207], 0
v_mfma_f32_16x16x32_bf16 a[72:75], v[144:147], v[208:211], 0
ds_read_b128 v[8:11], v237 offset:16384
s_xor_b32 m0, s82, 37888
v_mfma_f32_16x16x32_bf16 a[8:11], v[144:147], v[212:215], 0
buffer_load_dwordx4 v229, s[40:43], s79 offen lds
v_mfma_f32_16x16x32_bf16 a[80:83], v[144:147], v[216:219], 0
ds_read_b128 v[12:15], v237 offset:18432
v_mfma_f32_16x16x32_bf16 a[16:19], v[144:147], v[220:223], 0
v_mfma_f32_16x16x32_bf16 a[124:127], v[148:151], v[192:195], 0
ds_read_b128 v[16:19], v237 offset:8192
s_xor_b32 m0, s82, 38912
v_mfma_f32_16x16x32_bf16 a[188:191], v[148:151], v[196:199], 0
buffer_load_dwordx4 v230, s[40:43], s79 offen lds
v_mfma_f32_16x16x32_bf16 a[68:71], v[148:151], v[200:203], 0
ds_read_b128 v[20:23], v237 offset:10240
v_mfma_f32_16x16x32_bf16 a[4:7], v[148:151], v[204:207], 0
v_mfma_f32_16x16x32_bf16 a[76:79], v[148:151], v[208:211], 0
ds_read_b128 v[24:27], v237 offset:0
s_xor_b32 m0, s82, 39936
v_mfma_f32_16x16x32_bf16 a[12:15], v[148:151], v[212:215], 0
buffer_load_dwordx4 v231, s[40:43], s79 offen lds
v_mfma_f32_16x16x32_bf16 a[84:87], v[148:151], v[216:219], 0
ds_read_b128 v[28:31], v237 offset:2048
v_mfma_f32_16x16x32_bf16 a[20:23], v[148:151], v[220:223], 0
s_add_u32 s79, s79, 128
v_mfma_f32_16x16x32_bf16 a[88:91], v[152:155], v[192:195], 0
v_mfma_f32_16x16x32_bf16 a[24:27], v[152:155], v[196:199], 0
buffer_load_dword v70, v118, s[60:63], 0 offen
v_mfma_f32_16x16x32_bf16 a[240:243], v[152:155], v[200:203], 0
v_mfma_f32_16x16x32_bf16 a[32:35], v[152:155], v[204:207], 0
v_mfma_f32_16x16x32_bf16 a[248:251], v[152:155], v[208:211], 0
buffer_load_dwordx4 v[72:75], v119, s[60:63], 0 offen offset:0
v_mfma_f32_16x16x32_bf16 a[40:43], v[152:155], v[212:215], 0
v_mfma_f32_16x16x32_bf16 a[48:51], v[152:155], v[216:219], 0
v_mfma_f32_16x16x32_bf16 a[56:59], v[152:155], v[220:223], 0
buffer_load_dwordx4 v[76:79], v119, s[60:63], 0 offen offset:128
v_mfma_f32_16x16x32_bf16 a[92:95], v[156:159], v[192:195], 0
v_mfma_f32_16x16x32_bf16 a[28:31], v[156:159], v[196:199], 0
v_mfma_f32_16x16x32_bf16 a[244:247], v[156:159], v[200:203], 0
buffer_load_dwordx4 v[80:83], v119, s[60:63], 0 offen offset:256
v_mfma_f32_16x16x32_bf16 a[36:39], v[156:159], v[204:207], 0
v_mfma_f32_16x16x32_bf16 a[252:255], v[156:159], v[208:211], 0
v_mfma_f32_16x16x32_bf16 a[44:47], v[156:159], v[212:215], 0
buffer_load_dwordx4 v[84:87], v119, s[60:63], 0 offen offset:384
v_mfma_f32_16x16x32_bf16 a[52:55], v[156:159], v[216:219], 0
v_mfma_f32_16x16x32_bf16 a[60:63], v[156:159], v[220:223], 0
s_mov_b64 exec, 0
s_cmp_eq_u32 s3, 1
s_cbranch_scc1 .La5_LW2_1
s_branch .La5_LW2_3
.La5_LW2_R1:
s_mov_b64 exec, -1
s_waitcnt lgkmcnt(0)
s_waitcnt vmcnt(5)
s_barrier
v_mfma_f32_16x16x32_bf16 a[224:227], v[0:3], v[32:35], a[224:227]
ds_read_b128 v[128:131], v236 offset:57344
s_add_u32 m0, s83, 50688
v_mfma_f32_16x16x32_bf16 a[232:235], v[0:3], v[36:39], a[232:235]
buffer_load_dwordx4 v232, s[44:47], s80 offen lds
v_mfma_f32_16x16x32_bf16 a[208:211], v[0:3], v[40:43], a[208:211]
ds_read_b64_tr_b16 v[192:193], v240 offset:33792
v_mfma_f32_16x16x32_bf16 a[128:131], v[0:3], v[44:47], a[128:131]
v_mfma_f32_16x16x32_bf16 a[216:219], v[0:3], v[48:51], a[216:219]
ds_read_b64_tr_b16 v[194:195], v240 offset:34816
s_add_u32 m0, s83, 51712
v_mfma_f32_16x16x32_bf16 a[136:139], v[0:3], v[52:55], a[136:139]
buffer_load_dwordx4 v233, s[44:47], s80 offen lds
v_mfma_f32_16x16x32_bf16 a[192:195], v[0:3], v[56:59], a[192:195]
ds_read_b64_tr_b16 v[196:197], v241 offset:33792
v_mfma_f32_16x16x32_bf16 a[144:147], v[0:3], v[60:63], a[144:147]
v_mfma_f32_16x16x32_bf16 a[228:231], v[4:7], v[32:35], a[228:231]
ds_read_b64_tr_b16 v[198:199], v241 offset:34816
s_add_u32 m0, s83, 52864
v_mfma_f32_16x16x32_bf16 a[236:239], v[4:7], v[36:39], a[236:239]
buffer_load_dwordx4 v234, s[44:47], s80 offen lds
v_mfma_f32_16x16x32_bf16 a[212:215], v[4:7], v[40:43], a[212:215]
.La5_LW1_3:
s_cbranch_execz .La5_LW1_7
ds_read_b64_tr_b16 v[200:201], v242 offset:33792
v_mfma_f32_16x16x32_bf16 a[132:135], v[4:7], v[44:47], a[132:135]
v_mfma_f32_16x16x32_bf16 a[220:223], v[4:7], v[48:51], a[220:223]
ds_read_b64_tr_b16 v[202:203], v242 offset:34816
s_add_u32 m0, s83, 53888
v_mfma_f32_16x16x32_bf16 a[140:143], v[4:7], v[52:55], a[140:143]
buffer_load_dwordx4 v235, s[44:47], s80 offen lds
v_mfma_f32_16x16x32_bf16 a[196:199], v[4:7], v[56:59], a[196:199]
ds_read_b64_tr_b16 v[204:205], v243 offset:33792
v_mfma_f32_16x16x32_bf16 a[148:151], v[4:7], v[60:63], a[148:151]
s_add_u32 s80, s80, s81
v_mfma_f32_16x16x32_bf16 a[200:203], v[8:11], v[32:35], a[200:203]
ds_read_b64_tr_b16 v[206:207], v243 offset:34816
s_xor_b32 m0, s82, 0
v_mfma_f32_16x16x32_bf16 a[152:155], v[8:11], v[36:39], a[152:155]
buffer_load_dwordx4 v224, s[40:43], s79 offen lds
v_mfma_f32_16x16x32_bf16 a[96:99], v[8:11], v[40:43], a[96:99]
ds_read_b64_tr_b16 v[208:209], v240 offset:33920
v_mfma_f32_16x16x32_bf16 a[160:163], v[8:11], v[44:47], a[160:163]
v_mfma_f32_16x16x32_bf16 a[104:107], v[8:11], v[48:51], a[104:107]
ds_read_b64_tr_b16 v[210:211], v240 offset:34944
s_xor_b32 m0, s82, 1024
v_mfma_f32_16x16x32_bf16 a[168:171], v[8:11], v[52:55], a[168:171]
buffer_load_dwordx4 v225, s[40:43], s79 offen lds
v_mfma_f32_16x16x32_bf16 a[112:115], v[8:11], v[56:59], a[112:115]
ds_read_b64_tr_b16 v[212:213], v241 offset:33920
v_mfma_f32_16x16x32_bf16 a[176:179], v[8:11], v[60:63], a[176:179]
v_mfma_f32_16x16x32_bf16 a[204:207], v[12:15], v[32:35], a[204:207]
ds_read_b64_tr_b16 v[214:215], v241 offset:34944
s_xor_b32 m0, s82, 2048
v_mfma_f32_16x16x32_bf16 a[156:159], v[12:15], v[36:39], a[156:159]
buffer_load_dwordx4 v226, s[40:43], s79 offen lds
v_mfma_f32_16x16x32_bf16 a[100:103], v[12:15], v[40:43], a[100:103]
ds_read_b64_tr_b16 v[216:217], v242 offset:33920
v_mfma_f32_16x16x32_bf16 a[164:167], v[12:15], v[44:47], a[164:167]
v_mfma_f32_16x16x32_bf16 a[108:111], v[12:15], v[48:51], a[108:111]
ds_read_b64_tr_b16 v[218:219], v242 offset:34944
s_xor_b32 m0, s82, 3072
v_mfma_f32_16x16x32_bf16 a[172:175], v[12:15], v[52:55], a[172:175]
buffer_load_dwordx4 v227, s[40:43], s79 offen lds
v_mfma_f32_16x16x32_bf16 a[116:119], v[12:15], v[56:59], a[116:119]
ds_read_b64_tr_b16 v[220:221], v243 offset:33920
v_mfma_f32_16x16x32_bf16 a[180:183], v[12:15], v[60:63], a[180:183]
v_mfma_f32_16x16x32_bf16 a[120:123], v[16:19], v[32:35], a[120:123]
ds_read_b64_tr_b16 v[222:223], v243 offset:34944
s_xor_b32 m0, s82, 4096
v_mfma_f32_16x16x32_bf16 a[184:187], v[16:19], v[36:39], a[184:187]
buffer_load_dwordx4 v228, s[40:43], s79 offen lds
v_mfma_f32_16x16x32_bf16 a[64:67], v[16:19], v[40:43], a[64:67]
ds_read_b128 v[132:135], v236 offset:59392
v_mfma_f32_16x16x32_bf16 a[0:3], v[16:19], v[44:47], a[0:3]
v_mfma_f32_16x16x32_bf16 a[72:75], v[16:19], v[48:51], a[72:75]
ds_read_b128 v[136:139], v236 offset:49152
s_xor_b32 m0, s82, 5120
v_mfma_f32_16x16x32_bf16 a[8:11], v[16:19], v[52:55], a[8:11]
buffer_load_dwordx4 v229, s[40:43], s79 offen lds
v_mfma_f32_16x16x32_bf16 a[80:83], v[16:19], v[56:59], a[80:83]
ds_read_b128 v[140:143], v236 offset:51200
v_mfma_f32_16x16x32_bf16 a[16:19], v[16:19], v[60:63], a[16:19]
v_mfma_f32_16x16x32_bf16 a[124:127], v[20:23], v[32:35], a[124:127]
ds_read_b128 v[144:147], v236 offset:40960
s_xor_b32 m0, s82, 6144
v_mfma_f32_16x16x32_bf16 a[188:191], v[20:23], v[36:39], a[188:191]
buffer_load_dwordx4 v230, s[40:43], s79 offen lds
v_mfma_f32_16x16x32_bf16 a[68:71], v[20:23], v[40:43], a[68:71]
ds_read_b128 v[148:151], v236 offset:43008
v_mfma_f32_16x16x32_bf16 a[4:7], v[20:23], v[44:47], a[4:7]
v_mfma_f32_16x16x32_bf16 a[76:79], v[20:23], v[48:51], a[76:79]
ds_read_b128 v[152:155], v236 offset:32768
s_xor_b32 m0, s82, 7168
v_mfma_f32_16x16x32_bf16 a[12:15], v[20:23], v[52:55], a[12:15]
buffer_load_dwordx4 v231, s[40:43], s79 offen lds
v_mfma_f32_16x16x32_bf16 a[84:87], v[20:23], v[56:59], a[84:87]
ds_read_b128 v[156:159], v236 offset:34816
v_mfma_f32_16x16x32_bf16 a[20:23], v[20:23], v[60:63], a[20:23]
s_add_u32 s79, s79, 128
v_mfma_f32_16x16x32_bf16 a[88:91], v[24:27], v[32:35], a[88:91]
s_add_u32 m0, s83, 0
v_mfma_f32_16x16x32_bf16 a[24:27], v[24:27], v[36:39], a[24:27]
buffer_load_dwordx4 v232, s[44:47], s80 offen lds
v_mfma_f32_16x16x32_bf16 a[240:243], v[24:27], v[40:43], a[240:243]
v_mfma_f32_16x16x32_bf16 a[32:35], v[24:27], v[44:47], a[32:35]
v_mfma_f32_16x16x32_bf16 a[248:251], v[24:27], v[48:51], a[248:251]
s_add_u32 m0, s83, 1024
v_mfma_f32_16x16x32_bf16 a[40:43], v[24:27], v[52:55], a[40:43]
buffer_load_dwordx4 v233, s[44:47], s80 offen lds
v_mfma_f32_16x16x32_bf16 a[48:51], v[24:27], v[56:59], a[48:51]
v_mfma_f32_16x16x32_bf16 a[56:59], v[24:27], v[60:63], a[56:59]
v_mfma_f32_16x16x32_bf16 a[92:95], v[28:31], v[32:35], a[92:95]
s_add_u32 m0, s83, 2176
v_mfma_f32_16x16x32_bf16 a[28:31], v[28:31], v[36:39], a[28:31]
buffer_load_dwordx4 v234, s[44:47], s80 offen lds
v_mfma_f32_16x16x32_bf16 a[244:247], v[28:31], v[40:43], a[244:247]
v_mfma_f32_16x16x32_bf16 a[36:39], v[28:31], v[44:47], a[36:39]
v_mfma_f32_16x16x32_bf16 a[252:255], v[28:31], v[48:51], a[252:255]
s_add_u32 m0, s83, 3200
v_mfma_f32_16x16x32_bf16 a[44:47], v[28:31], v[52:55], a[44:47]
buffer_load_dwordx4 v235, s[44:47], s80 offen lds
v_mfma_f32_16x16x32_bf16 a[52:55], v[28:31], v[56:59], a[52:55]
v_mfma_f32_16x16x32_bf16 a[60:63], v[28:31], v[60:63], a[60:63]
s_add_u32 s80, s80, s81
s_waitcnt lgkmcnt(0)
s_waitcnt vmcnt(12)
s_barrier
v_mfma_f32_16x16x32_bf16 a[224:227], v[128:131], v[192:195], a[224:227]
ds_read_b128 v[0:3], v237 offset:57344
s_add_u32 m0, s83, 16896
v_mfma_f32_16x16x32_bf16 a[232:235], v[128:131], v[196:199], a[232:235]
buffer_load_dwordx4 v232, s[44:47], s80 offen lds
v_mfma_f32_16x16x32_bf16 a[208:211], v[128:131], v[200:203], a[208:211]
ds_read_b64_tr_b16 v[32:33], v240 offset:50688
s_nop 0
v_mfma_f32_16x16x32_bf16 a[128:131], v[128:131], v[204:207], a[128:131]
v_lshlrev_b32 v70, 16, v70
v_lshlrev_b32 v88, 16, v72
v_mfma_f32_16x16x32_bf16 a[216:219], v[128:131], v[208:211], a[216:219]
ds_read_b64_tr_b16 v[34:35], v240 offset:51712
v_and_b32 v89, 0xffff0000, v72
s_add_u32 m0, s83, 17920
v_mfma_f32_16x16x32_bf16 a[136:139], v[128:131], v[212:215], a[136:139]
buffer_load_dwordx4 v233, s[44:47], s80 offen lds
v_mfma_f32_16x16x32_bf16 a[192:195], v[128:131], v[216:219], a[192:195]
ds_read_b64_tr_b16 v[36:37], v241 offset:50688
v_sub_f32 v88, v88, v70
v_mfma_f32_16x16x32_bf16 a[144:147], v[128:131], v[220:223], a[144:147]
v_sub_f32 v89, v89, v70
v_add_f32 v120, v88, v89
v_mfma_f32_16x16x32_bf16 a[228:231], v[132:135], v[192:195], a[228:231]
ds_read_b64_tr_b16 v[38:39], v241 offset:51712
v_mul_f32 v122, v88, v88
s_add_u32 m0, s83, 19072
v_mfma_f32_16x16x32_bf16 a[236:239], v[132:135], v[196:199], a[236:239]
buffer_load_dwordx4 v234, s[44:47], s80 offen lds
v_mfma_f32_16x16x32_bf16 a[212:215], v[132:135], v[200:203], a[212:215]
ds_read_b64_tr_b16 v[40:41], v242 offset:50688
v_mul_f32 v123, v89, v89
v_mfma_f32_16x16x32_bf16 a[132:135], v[132:135], v[204:207], a[132:135]
v_lshlrev_b32 v90, 16, v73
v_and_b32 v91, 0xffff0000, v73
v_mfma_f32_16x16x32_bf16 a[220:223], v[132:135], v[208:211], a[220:223]
ds_read_b64_tr_b16 v[42:43], v242 offset:51712
v_sub_f32 v90, v90, v70
s_add_u32 m0, s83, 20096
v_mfma_f32_16x16x32_bf16 a[140:143], v[132:135], v[212:215], a[140:143]
buffer_load_dwordx4 v235, s[44:47], s80 offen lds
v_mfma_f32_16x16x32_bf16 a[196:199], v[132:135], v[216:219], a[196:199]
ds_read_b64_tr_b16 v[44:45], v243 offset:50688
v_sub_f32 v91, v91, v70
v_mfma_f32_16x16x32_bf16 a[148:151], v[132:135], v[220:223], a[148:151]
s_add_u32 s80, s80, s81
v_add_f32 v124, v90, v91
v_mfma_f32_16x16x32_bf16 a[200:203], v[136:139], v[192:195], a[200:203]
ds_read_b64_tr_b16 v[46:47], v243 offset:51712
v_fmac_f32 v122, v90, v90
s_add_u32 m0, s83, 33792
v_mfma_f32_16x16x32_bf16 a[152:155], v[136:139], v[196:199], a[152:155]
buffer_load_dwordx4 v232, s[44:47], s80 offen lds
v_mfma_f32_16x16x32_bf16 a[96:99], v[136:139], v[200:203], a[96:99]
ds_read_b64_tr_b16 v[48:49], v240 offset:50816
v_add_f32 v120, v120, v124
v_mfma_f32_16x16x32_bf16 a[160:163], v[136:139], v[204:207], a[160:163]
v_fmac_f32 v123, v91, v91
v_lshlrev_b32 v92, 16, v74
v_mfma_f32_16x16x32_bf16 a[104:107], v[136:139], v[208:211], a[104:107]
ds_read_b64_tr_b16 v[50:51], v240 offset:51840
v_and_b32 v93, 0xffff0000, v74
s_add_u32 m0, s83, 34816
v_mfma_f32_16x16x32_bf16 a[168:171], v[136:139], v[212:215], a[168:171]
buffer_load_dwordx4 v233, s[44:47], s80 offen lds
v_mfma_f32_16x16x32_bf16 a[112:115], v[136:139], v[216:219], a[112:115]
ds_read_b64_tr_b16 v[52:53], v241 offset:50816
v_sub_f32 v92, v92, v70
v_mfma_f32_16x16x32_bf16 a[176:179], v[136:139], v[220:223], a[176:179]
v_sub_f32 v93, v93, v70
v_add_f32 v124, v92, v93
v_mfma_f32_16x16x32_bf16 a[204:207], v[140:143], v[192:195], a[204:207]
ds_read_b64_tr_b16 v[54:55], v241 offset:51840
v_fmac_f32 v122, v92, v92
s_add_u32 m0, s83, 35968
v_mfma_f32_16x16x32_bf16 a[156:159], v[140:143], v[196:199], a[156:159]
buffer_load_dwordx4 v234, s[44:47], s80 offen lds
v_mfma_f32_16x16x32_bf16 a[100:103], v[140:143], v[200:203], a[100:103]
ds_read_b64_tr_b16 v[56:57], v242 offset:50816
v_add_f32 v120, v120, v124
v_mfma_f32_16x16x32_bf16 a[164:167], v[140:143], v[204:207], a[164:167]
v_fmac_f32 v123, v93, v93
v_lshlrev_b32 v94, 16, v75
v_mfma_f32_16x16x32_bf16 a[108:111], v[140:143], v[208:211], a[108:111]
ds_read_b64_tr_b16 v[58:59], v242 offset:51840
v_and_b32 v95, 0xffff0000, v75
s_add_u32 m0, s83, 36992
v_mfma_f32_16x16x32_bf16 a[172:175], v[140:143], v[212:215], a[172:175]
buffer_load_dwordx4 v235, s[44:47], s80 offen lds
v_mfma_f32_16x16x32_bf16 a[116:119], v[140:143], v[216:219], a[116:119]
ds_read_b64_tr_b16 v[60:61], v243 offset:50816
v_sub_f32 v94, v94, v70
v_mfma_f32_16x16x32_bf16 a[180:183], v[140:143], v[220:223], a[180:183]
s_add_u32 s80, s80, s81
v_sub_f32 v95, v95, v70
v_mfma_f32_16x16x32_bf16 a[120:123], v[144:147], v[192:195], a[120:123]
ds_read_b64_tr_b16 v[62:63], v243 offset:51840
v_add_f32 v124, v94, v95
v_mfma_f32_16x16x32_bf16 a[184:187], v[144:147], v[196:199], a[184:187]
buffer_load_dword v254, v164, s[44:47], s86 offen
v_fmac_f32 v122, v94, v94
v_mfma_f32_16x16x32_bf16 a[64:67], v[144:147], v[200:203], a[64:67]
ds_read_b128 v[4:7], v237 offset:59392
v_add_f32 v120, v120, v124
v_mfma_f32_16x16x32_bf16 a[0:3], v[144:147], v[204:207], a[0:3]
s_xor_b32 s82, s82, 32768
s_add_u32 s83, s83, 50688
v_mfma_f32_16x16x32_bf16 a[72:75], v[144:147], v[208:211], a[72:75]
ds_read_b128 v[8:11], v237 offset:49152
s_add_u32 s86, s86, 2097152
v_mfma_f32_16x16x32_bf16 a[8:11], v[144:147], v[212:215], a[8:11]
v_fmac_f32 v123, v95, v95
v_lshlrev_b32 v96, 16, v76
v_mfma_f32_16x16x32_bf16 a[80:83], v[144:147], v[216:219], a[80:83]
ds_read_b128 v[12:15], v237 offset:51200
v_and_b32 v97, 0xffff0000, v76
v_mfma_f32_16x16x32_bf16 a[16:19], v[144:147], v[220:223], a[16:19]
v_sub_f32 v96, v96, v70
v_sub_f32 v97, v97, v70
v_mfma_f32_16x16x32_bf16 a[124:127], v[148:151], v[192:195], a[124:127]
ds_read_b128 v[16:19], v237 offset:40960
v_add_f32 v124, v96, v97
v_mfma_f32_16x16x32_bf16 a[188:191], v[148:151], v[196:199], a[188:191]
v_fmac_f32 v122, v96, v96
v_add_f32 v120, v120, v124
v_mfma_f32_16x16x32_bf16 a[68:71], v[148:151], v[200:203], a[68:71]
ds_read_b128 v[20:23], v237 offset:43008
v_fmac_f32 v123, v97, v97
v_mfma_f32_16x16x32_bf16 a[4:7], v[148:151], v[204:207], a[4:7]
v_lshlrev_b32 v98, 16, v77
v_and_b32 v99, 0xffff0000, v77
v_mfma_f32_16x16x32_bf16 a[76:79], v[148:151], v[208:211], a[76:79]
ds_read_b128 v[24:27], v237 offset:32768
v_sub_f32 v98, v98, v70
v_mfma_f32_16x16x32_bf16 a[12:15], v[148:151], v[212:215], a[12:15]
v_sub_f32 v99, v99, v70
v_add_f32 v124, v98, v99
v_mfma_f32_16x16x32_bf16 a[84:87], v[148:151], v[216:219], a[84:87]
ds_read_b128 v[28:31], v237 offset:34816
v_fmac_f32 v122, v98, v98
v_mfma_f32_16x16x32_bf16 a[20:23], v[148:151], v[220:223], a[20:23]
v_add_f32 v120, v120, v124
v_fmac_f32 v123, v99, v99
v_mfma_f32_16x16x32_bf16 a[88:91], v[152:155], v[192:195], a[88:91]
v_lshlrev_b32 v100, 16, v78
v_and_b32 v101, 0xffff0000, v78
v_mfma_f32_16x16x32_bf16 a[24:27], v[152:155], v[196:199], a[24:27]
v_sub_f32 v100, v100, v70
v_sub_f32 v101, v101, v70
v_mfma_f32_16x16x32_bf16 a[240:243], v[152:155], v[200:203], a[240:243]
v_add_f32 v124, v100, v101
v_fmac_f32 v122, v100, v100
v_mfma_f32_16x16x32_bf16 a[32:35], v[152:155], v[204:207], a[32:35]
v_add_f32 v120, v120, v124
v_fmac_f32 v123, v101, v101
v_mfma_f32_16x16x32_bf16 a[248:251], v[152:155], v[208:211], a[248:251]
v_lshlrev_b32 v102, 16, v79
v_and_b32 v103, 0xffff0000, v79
v_mfma_f32_16x16x32_bf16 a[40:43], v[152:155], v[212:215], a[40:43]
v_sub_f32 v102, v102, v70
v_sub_f32 v103, v103, v70
v_mfma_f32_16x16x32_bf16 a[48:51], v[152:155], v[216:219], a[48:51]
v_add_f32 v124, v102, v103
v_fmac_f32 v122, v102, v102
v_mfma_f32_16x16x32_bf16 a[56:59], v[152:155], v[220:223], a[56:59]
v_add_f32 v120, v120, v124
v_fmac_f32 v123, v103, v103
v_mfma_f32_16x16x32_bf16 a[92:95], v[156:159], v[192:195], a[92:95]
v_lshlrev_b32 v104, 16, v80
v_and_b32 v105, 0xffff0000, v80
v_mfma_f32_16x16x32_bf16 a[28:31], v[156:159], v[196:199], a[28:31]
v_sub_f32 v104, v104, v70
v_sub_f32 v105, v105, v70
v_mfma_f32_16x16x32_bf16 a[244:247], v[156:159], v[200:203], a[244:247]
v_add_f32 v124, v104, v105
v_fmac_f32 v122, v104, v104
v_mfma_f32_16x16x32_bf16 a[36:39], v[156:159], v[204:207], a[36:39]
v_add_f32 v120, v120, v124
v_fmac_f32 v123, v105, v105
v_mfma_f32_16x16x32_bf16 a[252:255], v[156:159], v[208:211], a[252:255]
v_lshlrev_b32 v106, 16, v81
v_and_b32 v107, 0xffff0000, v81
v_mfma_f32_16x16x32_bf16 a[44:47], v[156:159], v[212:215], a[44:47]
v_sub_f32 v106, v106, v70
v_sub_f32 v107, v107, v70
v_mfma_f32_16x16x32_bf16 a[52:55], v[156:159], v[216:219], a[52:55]
v_add_f32 v124, v106, v107
v_fmac_f32 v122, v106, v106
v_mfma_f32_16x16x32_bf16 a[60:63], v[156:159], v[220:223], a[60:63]
v_add_f32 v120, v120, v124
v_fmac_f32 v123, v107, v107
s_waitcnt lgkmcnt(0)
s_waitcnt vmcnt(9)
s_barrier
v_mfma_f32_16x16x32_bf16 a[224:227], v[0:3], v[32:35], a[224:227]
ds_read_b128 v[128:131], v236 offset:24576
s_xor_b32 m0, s82, 0
v_mfma_f32_16x16x32_bf16 a[232:235], v[0:3], v[36:39], a[232:235]
buffer_load_dwordx4 v224, s[40:43], s79 offen lds
v_mfma_f32_16x16x32_bf16 a[208:211], v[0:3], v[40:43], a[208:211]
ds_read_b64_tr_b16 v[192:193], v240 offset:0
v_lshlrev_b32 v108, 16, v82
v_mfma_f32_16x16x32_bf16 a[128:131], v[0:3], v[44:47], a[128:131]
v_and_b32 v109, 0xffff0000, v82
v_sub_f32 v108, v108, v70
v_mfma_f32_16x16x32_bf16 a[216:219], v[0:3], v[48:51], a[216:219]
ds_read_b64_tr_b16 v[194:195], v240 offset:1024
v_sub_f32 v109, v109, v70
v_mfma_f32_16x16x32_bf16 a[136:139], v[0:3], v[52:55], a[136:139]
v_add_f32 v124, v108, v109
v_fmac_f32 v122, v108, v108
v_mfma_f32_16x16x32_bf16 a[192:195], v[0:3], v[56:59], a[192:195]
ds_read_b64_tr_b16 v[196:197], v241 offset:0
v_add_f32 v120, v120, v124
v_mfma_f32_16x16x32_bf16 a[144:147], v[0:3], v[60:63], a[144:147]
v_fmac_f32 v123, v109, v109
v_lshlrev_b32 v110, 16, v83
v_mfma_f32_16x16x32_bf16 a[228:231], v[4:7], v[32:35], a[228:231]
ds_read_b64_tr_b16 v[198:199], v241 offset:1024
v_and_b32 v111, 0xffff0000, v83
s_xor_b32 m0, s82, 1024
v_mfma_f32_16x16x32_bf16 a[236:239], v[4:7], v[36:39], a[236:239]
buffer_load_dwordx4 v225, s[40:43], s79 offen lds
v_mfma_f32_16x16x32_bf16 a[212:215], v[4:7], v[40:43], a[212:215]
ds_read_b64_tr_b16 v[200:201], v242 offset:0
v_sub_f32 v110, v110, v70
v_mfma_f32_16x16x32_bf16 a[132:135], v[4:7], v[44:47], a[132:135]
v_sub_f32 v111, v111, v70
v_add_f32 v124, v110, v111
v_mfma_f32_16x16x32_bf16 a[220:223], v[4:7], v[48:51], a[220:223]
ds_read_b64_tr_b16 v[202:203], v242 offset:1024
v_fmac_f32 v122, v110, v110
v_mfma_f32_16x16x32_bf16 a[140:143], v[4:7], v[52:55], a[140:143]
v_add_f32 v120, v120, v124
v_fmac_f32 v123, v111, v111
v_mfma_f32_16x16x32_bf16 a[196:199], v[4:7], v[56:59], a[196:199]
ds_read_b64_tr_b16 v[204:205], v243 offset:0
v_lshlrev_b32 v112, 16, v84
v_mfma_f32_16x16x32_bf16 a[148:151], v[4:7], v[60:63], a[148:151]
v_and_b32 v113, 0xffff0000, v84
v_sub_f32 v112, v112, v70
v_mfma_f32_16x16x32_bf16 a[200:203], v[8:11], v[32:35], a[200:203]
ds_read_b64_tr_b16 v[206:207], v243 offset:1024
v_sub_f32 v113, v113, v70
s_xor_b32 m0, s82, 2048
v_mfma_f32_16x16x32_bf16 a[152:155], v[8:11], v[36:39], a[152:155]
buffer_load_dwordx4 v226, s[40:43], s79 offen lds
v_mfma_f32_16x16x32_bf16 a[96:99], v[8:11], v[40:43], a[96:99]
ds_read_b64_tr_b16 v[208:209], v240 offset:128
v_add_f32 v124, v112, v113
v_mfma_f32_16x16x32_bf16 a[160:163], v[8:11], v[44:47], a[160:163]
v_fmac_f32 v122, v112, v112
v_add_f32 v120, v120, v124
v_mfma_f32_16x16x32_bf16 a[104:107], v[8:11], v[48:51], a[104:107]
ds_read_b64_tr_b16 v[210:211], v240 offset:1152
v_fmac_f32 v123, v113, v113
s_add_u32 m0, s83, 0
v_mfma_f32_16x16x32_bf16 a[168:171], v[8:11], v[52:55], a[168:171]
buffer_load_dwordx4 v232, s[44:47], s80 offen lds
v_mfma_f32_16x16x32_bf16 a[112:115], v[8:11], v[56:59], a[112:115]
ds_read_b64_tr_b16 v[212:213], v241 offset:128
v_lshlrev_b32 v114, 16, v85
v_mfma_f32_16x16x32_bf16 a[176:179], v[8:11], v[60:63], a[176:179]
v_and_b32 v115, 0xffff0000, v85
v_sub_f32 v114, v114, v70
v_mfma_f32_16x16x32_bf16 a[204:207], v[12:15], v[32:35], a[204:207]
ds_read_b64_tr_b16 v[214:215], v241 offset:1152
v_sub_f32 v115, v115, v70
s_xor_b32 m0, s82, 3072
v_mfma_f32_16x16x32_bf16 a[156:159], v[12:15], v[36:39], a[156:159]
buffer_load_dwordx4 v227, s[40:43], s79 offen lds
v_mfma_f32_16x16x32_bf16 a[100:103], v[12:15], v[40:43], a[100:103]
ds_read_b64_tr_b16 v[216:217], v242 offset:128
v_add_f32 v124, v114, v115
v_mfma_f32_16x16x32_bf16 a[164:167], v[12:15], v[44:47], a[164:167]
v_fmac_f32 v122, v114, v114
v_add_f32 v120, v120, v124
v_mfma_f32_16x16x32_bf16 a[108:111], v[12:15], v[48:51], a[108:111]
ds_read_b64_tr_b16 v[218:219], v242 offset:1152
v_fmac_f32 v123, v115, v115
s_add_u32 m0, s83, 1024
v_mfma_f32_16x16x32_bf16 a[172:175], v[12:15], v[52:55], a[172:175]
buffer_load_dwordx4 v233, s[44:47], s80 offen lds
v_mfma_f32_16x16x32_bf16 a[116:119], v[12:15], v[56:59], a[116:119]
ds_read_b64_tr_b16 v[220:221], v243 offset:128
v_lshlrev_b32 v116, 16, v86
v_mfma_f32_16x16x32_bf16 a[180:183], v[12:15], v[60:63], a[180:183]
v_and_b32 v117, 0xffff0000, v86
v_sub_f32 v116, v116, v70
v_mfma_f32_16x16x32_bf16 a[120:123], v[16:19], v[32:35], a[120:123]
ds_read_b64_tr_b16 v[222:223], v243 offset:1152
v_sub_f32 v117, v117, v70
s_xor_b32 m0, s82, 4096
v_mfma_f32_16x16x32_bf16 a[184:187], v[16:19], v[36:39], a[184:187]
buffer_load_dwordx4 v228, s[40:43], s79 offen lds
v_mfma_f32_16x16x32_bf16 a[64:67], v[16:19], v[40:43], a[64:67]
ds_read_b128 v[132:135], v236 offset:26624
v_add_f32 v124, v116, v117
v_mfma_f32_16x16x32_bf16 a[0:3], v[16:19], v[44:47], a[0:3]
v_fmac_f32 v122, v116, v116
v_add_f32 v120, v120, v124
v_mfma_f32_16x16x32_bf16 a[72:75], v[16:19], v[48:51], a[72:75]
ds_read_b128 v[136:139], v236 offset:16384
v_fmac_f32 v123, v117, v117
s_add_u32 m0, s83, 2176
v_mfma_f32_16x16x32_bf16 a[8:11], v[16:19], v[52:55], a[8:11]
buffer_load_dwordx4 v234, s[44:47], s80 offen lds
v_mfma_f32_16x16x32_bf16 a[80:83], v[16:19], v[56:59], a[80:83]
ds_read_b128 v[140:143], v236 offset:18432
v_lshlrev_b32 v118, 16, v87
v_mfma_f32_16x16x32_bf16 a[16:19], v[16:19], v[60:63], a[16:19]
v_and_b32 v119, 0xffff0000, v87
v_sub_f32 v118, v118, v70
v_mfma_f32_16x16x32_bf16 a[124:127], v[20:23], v[32:35], a[124:127]
ds_read_b128 v[144:147], v236 offset:8192
v_sub_f32 v119, v119, v70
s_xor_b32 m0, s82, 5120
v_mfma_f32_16x16x32_bf16 a[188:191], v[20:23], v[36:39], a[188:191]
buffer_load_dwordx4 v229, s[40:43], s79 offen lds
v_mfma_f32_16x16x32_bf16 a[68:71], v[20:23], v[40:43], a[68:71]
ds_read_b128 v[148:151], v236 offset:10240
v_add_f32 v124, v118, v119
v_mfma_f32_16x16x32_bf16 a[4:7], v[20:23], v[44:47], a[4:7]
v_fmac_f32 v122, v118, v118
v_add_f32 v120, v120, v124
v_mfma_f32_16x16x32_bf16 a[76:79], v[20:23], v[48:51], a[76:79]
ds_read_b128 v[152:155], v236 offset:0
v_fmac_f32 v123, v119, v119
s_add_u32 m0, s83, 3200
v_mfma_f32_16x16x32_bf16 a[12:15], v[20:23], v[52:55], a[12:15]
buffer_load_dwordx4 v235, s[44:47], s80 offen lds
v_mfma_f32_16x16x32_bf16 a[84:87], v[20:23], v[56:59], a[84:87]
ds_read_b128 v[156:159], v236 offset:2048
v_add_f32 v121, v122, v123
v_mfma_f32_16x16x32_bf16 a[20:23], v[20:23], v[60:63], a[20:23]
s_add_u32 s80, s80, s81
s_add_u32 s83, s83, 16896
v_mfma_f32_16x16x32_bf16 a[88:91], v[24:27], v[32:35], a[88:91]
v_add_f32_dpp v120, v120, v120 row_ror:8 row_mask:0xf bank_mask:0xf
s_xor_b32 m0, s82, 6144
v_mfma_f32_16x16x32_bf16 a[24:27], v[24:27], v[36:39], a[24:27]
buffer_load_dwordx4 v230, s[40:43], s79 offen lds
s_cmp_eq_u32 s83, s97
s_cselect_b32 s83, s90, s83
v_mfma_f32_16x16x32_bf16 a[240:243], v[24:27], v[40:43], a[240:243]
v_add_f32_dpp v121, v121, v121 row_ror:8 row_mask:0xf bank_mask:0xf
v_mov_b32 v124, v120
v_mfma_f32_16x16x32_bf16 a[32:35], v[24:27], v[44:47], a[32:35]
v_mov_b32 v125, v121
v_mfma_f32_16x16x32_bf16 a[248:251], v[24:27], v[48:51], a[248:251]
v_mfma_f32_16x16x32_bf16 a[40:43], v[24:27], v[52:55], a[40:43]
v_permlane16_swap_b32 v124, v120
v_permlane16_swap_b32 v125, v121
v_mfma_f32_16x16x32_bf16 a[48:51], v[24:27], v[56:59], a[48:51]
v_add_f32 v120, v120, v124
v_add_f32 v121, v121, v125
v_mfma_f32_16x16x32_bf16 a[56:59], v[24:27], v[60:63], a[56:59]
v_mov_b32 v124, v120
v_mov_b32 v125, v121
v_mfma_f32_16x16x32_bf16 a[92:95], v[28:31], v[32:35], a[92:95]
s_xor_b32 m0, s82, 7168
v_mfma_f32_16x16x32_bf16 a[28:31], v[28:31], v[36:39], a[28:31]
buffer_load_dwordx4 v231, s[40:43], s79 offen lds
v_mfma_f32_16x16x32_bf16 a[244:247], v[28:31], v[40:43], a[244:247]
v_permlane32_swap_b32 v124, v120
v_permlane32_swap_b32 v125, v121
v_mfma_f32_16x16x32_bf16 a[36:39], v[28:31], v[44:47], a[36:39]
s_add_u32 s79, s79, 128
s_xor_b32 s82, s82, 32768
v_mfma_f32_16x16x32_bf16 a[252:255], v[28:31], v[48:51], a[252:255]
v_add_f32 v120, v120, v124
v_add_f32 v121, v121, v125
v_mfma_f32_16x16x32_bf16 a[44:47], v[28:31], v[52:55], a[44:47]
ds_write_b64 v127, v[120:121]
s_nop 0
v_mfma_f32_16x16x32_bf16 a[52:55], v[28:31], v[56:59], a[52:55]
v_readfirstlane_b32 s92, v252
v_readfirstlane_b32 s93, v253
v_mfma_f32_16x16x32_bf16 a[60:63], v[28:31], v[60:63], a[60:63]
s_add_u32 s92, s92, 1
s_addc_u32 s93, s93, 0
s_waitcnt lgkmcnt(0)
s_waitcnt vmcnt(17)
s_barrier
v_mfma_f32_16x16x32_bf16 a[224:227], v[128:131], v[192:195], a[224:227]
ds_read_b128 v[0:3], v237 offset:24576
ds_read_b128 v[72:75], v126
v_mfma_f32_16x16x32_bf16 a[232:235], v[128:131], v[196:199], a[232:235]
ds_read_b128 v[76:79], v126 offset:16
v_mfma_f32_16x16x32_bf16 a[208:211], v[128:131], v[200:203], a[208:211]
ds_read_b64_tr_b16 v[32:33], v240 offset:16896
v_mfma_f32_16x16x32_bf16 a[128:131], v[128:131], v[204:207], a[128:131]
v_mfma_f32_16x16x32_bf16 a[216:219], v[128:131], v[208:211], a[216:219]
ds_read_b64_tr_b16 v[34:35], v240 offset:17920
v_mov_b32 v252, s92
v_mfma_f32_16x16x32_bf16 a[136:139], v[128:131], v[212:215], a[136:139]
v_mov_b32 v253, s93
v_mov_b32 v160, 0
v_mfma_f32_16x16x32_bf16 a[192:195], v[128:131], v[216:219], a[192:195]
ds_read_b64_tr_b16 v[36:37], v241 offset:16896
v_mov_b32 v161, 0
v_mfma_f32_16x16x32_bf16 a[144:147], v[128:131], v[220:223], a[144:147]
v_mov_b32 v162, 0
v_mov_b32 v163, 0
v_mfma_f32_16x16x32_bf16 a[228:231], v[132:135], v[192:195], a[228:231]
ds_read_b64_tr_b16 v[38:39], v241 offset:17920
buffer_store_dwordx4 v[160:163], v255, s[52:55], s95 offen sc1
v_mfma_f32_16x16x32_bf16 a[236:239], v[132:135], v[196:199], a[236:239]
s_waitcnt lgkmcnt(4)
v_add_f32 v72, v72, v74
v_mfma_f32_16x16x32_bf16 a[212:215], v[132:135], v[200:203], a[212:215]
ds_read_b64_tr_b16 v[40:41], v242 offset:16896
v_add_f32 v73, v73, v75
v_mfma_f32_16x16x32_bf16 a[132:135], v[132:135], v[204:207], a[132:135]
v_add_f32 v76, v76, v78
v_add_f32 v77, v77, v79
v_mfma_f32_16x16x32_bf16 a[220:223], v[132:135], v[208:211], a[220:223]
ds_read_b64_tr_b16 v[42:43], v242 offset:17920
v_add_f32 v72, v72, v76
v_mfma_f32_16x16x32_bf16 a[140:143], v[132:135], v[212:215], a[140:143]
v_add_f32 v73, v73, v77
v_mul_f32 v72, 0x3a800000, v72
v_mfma_f32_16x16x32_bf16 a[196:199], v[132:135], v[216:219], a[196:199]
ds_read_b64_tr_b16 v[44:45], v243 offset:16896
v_mul_f32 v73, 0x3a800000, v73
v_mfma_f32_16x16x32_bf16 a[148:151], v[132:135], v[220:223], a[148:151]
v_add_f32 v124, v70, v72
v_fma_f32 v125, -v72, v72, v73
v_mfma_f32_16x16x32_bf16 a[200:203], v[136:139], v[192:195], a[200:203]
ds_read_b64_tr_b16 v[46:47], v243 offset:17920
v_cvt_pk_bf16_f32 v124, v124, v124
v_mfma_f32_16x16x32_bf16 a[152:155], v[136:139], v[196:199], a[152:155]
v_lshlrev_b32 v124, 16, v124
v_max_f32 v125, 0, v125
v_mfma_f32_16x16x32_bf16 a[96:99], v[136:139], v[200:203], a[96:99]
ds_read_b64_tr_b16 v[48:49], v240 offset:17024
v_cvt_pk_bf16_f32 v125, v125, v125
v_mfma_f32_16x16x32_bf16 a[160:163], v[136:139], v[204:207], a[160:163]
v_lshlrev_b32 v125, 16, v125
v_add_f32 v125, 0x3727c5ac, v125
v_mfma_f32_16x16x32_bf16 a[104:107], v[136:139], v[208:211], a[104:107]
ds_read_b64_tr_b16 v[50:51], v240 offset:18048
v_cvt_pk_bf16_f32 v125, v125, v125
v_mfma_f32_16x16x32_bf16 a[168:171], v[136:139], v[212:215], a[168:171]
v_lshlrev_b32 v125, 16, v125
v_mfma_f32_16x16x32_bf16 a[112:115], v[136:139], v[216:219], a[112:115]
ds_read_b64_tr_b16 v[52:53], v241 offset:17024
v_mfma_f32_16x16x32_bf16 a[176:179], v[136:139], v[220:223], a[176:179]
v_sqrt_f32 v125, v125
v_mfma_f32_16x16x32_bf16 a[204:207], v[140:143], v[192:195], a[204:207]
ds_read_b64_tr_b16 v[54:55], v241 offset:18048
v_sub_f32 v124, v70, v124
v_mfma_f32_16x16x32_bf16 a[156:159], v[140:143], v[196:199], a[156:159]
v_cvt_pk_bf16_f32 v125, v125, v125
v_lshlrev_b32 v125, 16, v125
v_mfma_f32_16x16x32_bf16 a[100:103], v[140:143], v[200:203], a[100:103]
ds_read_b64_tr_b16 v[56:57], v242 offset:17024
v_mfma_f32_16x16x32_bf16 a[164:167], v[140:143], v[204:207], a[164:167]
v_rcp_f32 v125, v125
v_mfma_f32_16x16x32_bf16 a[108:111], v[140:143], v[208:211], a[108:111]
ds_read_b64_tr_b16 v[58:59], v242 offset:18048
v_mul_f32 v125, 0x3e0293ee, v125
v_mfma_f32_16x16x32_bf16 a[172:175], v[140:143], v[212:215], a[172:175]
v_mul_f32 v124, v124, v125
v_fma_f32 v88, v88, v125, v124
v_mfma_f32_16x16x32_bf16 a[116:119], v[140:143], v[216:219], a[116:119]
ds_read_b64_tr_b16 v[60:61], v243 offset:17024
v_fma_f32 v89, v89, v125, v124
v_mfma_f32_16x16x32_bf16 a[180:183], v[140:143], v[220:223], a[180:183]
v_cvt_pk_bf16_f32 v72, v88, v89
v_fma_f32 v90, v90, v125, v124
v_mfma_f32_16x16x32_bf16 a[120:123], v[144:147], v[192:195], a[120:123]
ds_read_b64_tr_b16 v[62:63], v243 offset:18048
v_fma_f32 v91, v91, v125, v124
s_add_u32 m0, s83, 0
v_mfma_f32_16x16x32_bf16 a[184:187], v[144:147], v[196:199], a[184:187]
buffer_load_dwordx4 v232, s[44:47], s80 offen lds
v_mfma_f32_16x16x32_bf16 a[64:67], v[144:147], v[200:203], a[64:67]
ds_read_b128 v[4:7], v237 offset:26624
v_cvt_pk_bf16_f32 v73, v90, v91
v_mfma_f32_16x16x32_bf16 a[0:3], v[144:147], v[204:207], a[0:3]
v_fma_f32 v92, v92, v125, v124
v_fma_f32 v93, v93, v125, v124
v_mfma_f32_16x16x32_bf16 a[72:75], v[144:147], v[208:211], a[72:75]
ds_read_b128 v[8:11], v237 offset:16384
v_cvt_pk_bf16_f32 v74, v92, v93
s_add_u32 m0, s83, 1024
v_mfma_f32_16x16x32_bf16 a[8:11], v[144:147], v[212:215], a[8:11]
buffer_load_dwordx4 v233, s[44:47], s80 offen lds
v_mfma_f32_16x16x32_bf16 a[80:83], v[144:147], v[216:219], a[80:83]
ds_read_b128 v[12:15], v237 offset:18432
v_fma_f32 v94, v94, v125, v124
v_mfma_f32_16x16x32_bf16 a[16:19], v[144:147], v[220:223], a[16:19]
v_fma_f32 v95, v95, v125, v124
v_cvt_pk_bf16_f32 v75, v94, v95
.La5_LW1_4:
s_cbranch_execz .La5_LW1_8
v_mfma_f32_16x16x32_bf16 a[124:127], v[148:151], v[192:195], a[124:127]
ds_read_b128 v[16:19], v237 offset:8192
buffer_store_dwordx4 v[72:75], v71, s[56:59], 0 offen offset:0 sc1
v_fma_f32 v96, v96, v125, v124
s_add_u32 m0, s83, 2176
v_mfma_f32_16x16x32_bf16 a[188:191], v[148:151], v[196:199], a[188:191]
buffer_load_dwordx4 v234, s[44:47], s80 offen lds
v_fma_f32 v97, v97, v125, v124
v_mfma_f32_16x16x32_bf16 a[68:71], v[148:151], v[200:203], a[68:71]
ds_read_b128 v[20:23], v237 offset:10240
v_cvt_pk_bf16_f32 v76, v96, v97
v_fma_f32 v98, v98, v125, v124
v_mfma_f32_16x16x32_bf16 a[4:7], v[148:151], v[204:207], a[4:7]
v_fma_f32 v99, v99, v125, v124
v_cvt_pk_bf16_f32 v77, v98, v99
v_fma_f32 v100, v100, v125, v124
v_mfma_f32_16x16x32_bf16 a[76:79], v[148:151], v[208:211], a[76:79]
ds_read_b128 v[24:27], v237 offset:0
v_fma_f32 v101, v101, v125, v124
v_cvt_pk_bf16_f32 v78, v100, v101
s_add_u32 m0, s83, 3200
v_mfma_f32_16x16x32_bf16 a[12:15], v[148:151], v[212:215], a[12:15]
buffer_load_dwordx4 v235, s[44:47], s80 offen lds
v_fma_f32 v102, v102, v125, v124
v_mfma_f32_16x16x32_bf16 a[84:87], v[148:151], v[216:219], a[84:87]
ds_read_b128 v[28:31], v237 offset:2048
v_fma_f32 v103, v103, v125, v124
v_cvt_pk_bf16_f32 v79, v102, v103
v_mfma_f32_16x16x32_bf16 a[20:23], v[148:151], v[220:223], a[20:23]
s_add_u32 s80, s80, s81
s_add_u32 s83, s83, 16896
buffer_store_dwordx4 v[76:79], v71, s[56:59], 0 offen offset:2048 sc1
v_mfma_f32_16x16x32_bf16 a[88:91], v[152:155], v[192:195], a[88:91]
v_fma_f32 v104, v104, v125, v124
v_fma_f32 v105, v105, v125, v124
v_cvt_pk_bf16_f32 v80, v104, v105
v_mfma_f32_16x16x32_bf16 a[24:27], v[152:155], v[196:199], a[24:27]
s_cmp_eq_u32 s83, s97
s_cselect_b32 s83, s90, s83
v_fma_f32 v106, v106, v125, v124
v_mfma_f32_16x16x32_bf16 a[240:243], v[152:155], v[200:203], a[240:243]
v_fma_f32 v107, v107, v125, v124
v_cvt_pk_bf16_f32 v81, v106, v107
v_fma_f32 v108, v108, v125, v124
v_mfma_f32_16x16x32_bf16 a[32:35], v[152:155], v[204:207], a[32:35]
v_fma_f32 v109, v109, v125, v124
v_cvt_pk_bf16_f32 v82, v108, v109
v_fma_f32 v110, v110, v125, v124
v_mfma_f32_16x16x32_bf16 a[248:251], v[152:155], v[208:211], a[248:251]
v_fma_f32 v111, v111, v125, v124
v_cvt_pk_bf16_f32 v83, v110, v111
buffer_store_dwordx4 v[80:83], v71, s[56:59], s88 offen offset:0 sc1
v_mfma_f32_16x16x32_bf16 a[40:43], v[152:155], v[212:215], a[40:43]
v_fma_f32 v112, v112, v125, v124
v_fma_f32 v113, v113, v125, v124
v_cvt_pk_bf16_f32 v84, v112, v113
v_mfma_f32_16x16x32_bf16 a[48:51], v[152:155], v[216:219], a[48:51]
v_fma_f32 v114, v114, v125, v124
v_fma_f32 v115, v115, v125, v124
v_cvt_pk_bf16_f32 v85, v114, v115
v_mfma_f32_16x16x32_bf16 a[56:59], v[152:155], v[220:223], a[56:59]
v_fma_f32 v116, v116, v125, v124
v_fma_f32 v117, v117, v125, v124
v_cvt_pk_bf16_f32 v86, v116, v117
v_mfma_f32_16x16x32_bf16 a[92:95], v[156:159], v[192:195], a[92:95]
v_fma_f32 v118, v118, v125, v124
v_fma_f32 v119, v119, v125, v124
v_cvt_pk_bf16_f32 v87, v118, v119
v_mfma_f32_16x16x32_bf16 a[28:31], v[156:159], v[196:199], a[28:31]
buffer_store_dwordx4 v[84:87], v71, s[56:59], s88 offen offset:2048 sc1
v_mfma_f32_16x16x32_bf16 a[244:247], v[156:159], v[200:203], a[244:247]
v_add_u32 v240, s84, v240
v_add_u32 v241, s84, v241
v_mfma_f32_16x16x32_bf16 a[36:39], v[156:159], v[204:207], a[36:39]
v_add_u32 v242, s84, v242
v_add_u32 v243, s84, v243
v_mfma_f32_16x16x32_bf16 a[252:255], v[156:159], v[208:211], a[252:255]
v_xor_b32 v236, 0x8000, v236
v_xor_b32 v237, 0x8000, v237
v_mfma_f32_16x16x32_bf16 a[44:47], v[156:159], v[212:215], a[44:47]
s_sub_u32 s84, 0, s84
v_mfma_f32_16x16x32_bf16 a[52:55], v[156:159], v[216:219], a[52:55]
v_mfma_f32_16x16x32_bf16 a[60:63], v[156:159], v[220:223], a[60:63]
s_waitcnt lgkmcnt(0)
.p2align 6
.La5_LV_LOOP:
s_waitcnt vmcnt(9)
s_barrier
v_mfma_f32_16x16x32_bf16 a[224:227], v[0:3], v[32:35], a[224:227]
ds_read_b128 v[128:131], v236 offset:24576
s_xor_b32 m0, s82, 0
v_mfma_f32_16x16x32_bf16 a[232:235], v[0:3], v[36:39], a[232:235]
buffer_load_dwordx4 v224, s[40:43], s79 offen lds
v_mfma_f32_16x16x32_bf16 a[208:211], v[0:3], v[40:43], a[208:211]
ds_read_b64_tr_b16 v[192:193], v240 offset:0
v_mfma_f32_16x16x32_bf16 a[128:131], v[0:3], v[44:47], a[128:131]
v_mfma_f32_16x16x32_bf16 a[216:219], v[0:3], v[48:51], a[216:219]
ds_read_b64_tr_b16 v[194:195], v240 offset:1024
s_xor_b32 m0, s82, 1024
v_mfma_f32_16x16x32_bf16 a[136:139], v[0:3], v[52:55], a[136:139]
buffer_load_dwordx4 v225, s[40:43], s79 offen lds
v_mfma_f32_16x16x32_bf16 a[192:195], v[0:3], v[56:59], a[192:195]
ds_read_b64_tr_b16 v[196:197], v241 offset:0
v_mfma_f32_16x16x32_bf16 a[144:147], v[0:3], v[60:63], a[144:147]
v_mfma_f32_16x16x32_bf16 a[228:231], v[4:7], v[32:35], a[228:231]
ds_read_b64_tr_b16 v[198:199], v241 offset:1024
s_xor_b32 m0, s82, 2048
v_mfma_f32_16x16x32_bf16 a[236:239], v[4:7], v[36:39], a[236:239]
buffer_load_dwordx4 v226, s[40:43], s79 offen lds
v_mfma_f32_16x16x32_bf16 a[212:215], v[4:7], v[40:43], a[212:215]
ds_read_b64_tr_b16 v[200:201], v242 offset:0
v_mfma_f32_16x16x32_bf16 a[132:135], v[4:7], v[44:47], a[132:135]
v_mfma_f32_16x16x32_bf16 a[220:223], v[4:7], v[48:51], a[220:223]
ds_read_b64_tr_b16 v[202:203], v242 offset:1024
s_xor_b32 m0, s82, 3072
v_mfma_f32_16x16x32_bf16 a[140:143], v[4:7], v[52:55], a[140:143]
buffer_load_dwordx4 v227, s[40:43], s79 offen lds
v_mfma_f32_16x16x32_bf16 a[196:199], v[4:7], v[56:59], a[196:199]
ds_read_b64_tr_b16 v[204:205], v243 offset:0
v_mfma_f32_16x16x32_bf16 a[148:151], v[4:7], v[60:63], a[148:151]
v_mfma_f32_16x16x32_bf16 a[200:203], v[8:11], v[32:35], a[200:203]
ds_read_b64_tr_b16 v[206:207], v243 offset:1024
s_xor_b32 m0, s82, 4096
v_mfma_f32_16x16x32_bf16 a[152:155], v[8:11], v[36:39], a[152:155]
buffer_load_dwordx4 v228, s[40:43], s79 offen lds
v_mfma_f32_16x16x32_bf16 a[96:99], v[8:11], v[40:43], a[96:99]
ds_read_b64_tr_b16 v[208:209], v240 offset:128
v_mfma_f32_16x16x32_bf16 a[160:163], v[8:11], v[44:47], a[160:163]
v_mfma_f32_16x16x32_bf16 a[104:107], v[8:11], v[48:51], a[104:107]
ds_read_b64_tr_b16 v[210:211], v240 offset:1152
s_xor_b32 m0, s82, 5120
v_mfma_f32_16x16x32_bf16 a[168:171], v[8:11], v[52:55], a[168:171]
buffer_load_dwordx4 v229, s[40:43], s79 offen lds
v_mfma_f32_16x16x32_bf16 a[112:115], v[8:11], v[56:59], a[112:115]
ds_read_b64_tr_b16 v[212:213], v241 offset:128
v_mfma_f32_16x16x32_bf16 a[176:179], v[8:11], v[60:63], a[176:179]
v_mfma_f32_16x16x32_bf16 a[204:207], v[12:15], v[32:35], a[204:207]
ds_read_b64_tr_b16 v[214:215], v241 offset:1152
s_xor_b32 m0, s82, 6144
v_mfma_f32_16x16x32_bf16 a[156:159], v[12:15], v[36:39], a[156:159]
buffer_load_dwordx4 v230, s[40:43], s79 offen lds
v_mfma_f32_16x16x32_bf16 a[100:103], v[12:15], v[40:43], a[100:103]
ds_read_b64_tr_b16 v[216:217], v242 offset:128
v_mfma_f32_16x16x32_bf16 a[164:167], v[12:15], v[44:47], a[164:167]
v_mfma_f32_16x16x32_bf16 a[108:111], v[12:15], v[48:51], a[108:111]
ds_read_b64_tr_b16 v[218:219], v242 offset:1152
s_xor_b32 m0, s82, 7168
v_mfma_f32_16x16x32_bf16 a[172:175], v[12:15], v[52:55], a[172:175]
buffer_load_dwordx4 v231, s[40:43], s79 offen lds
v_mfma_f32_16x16x32_bf16 a[116:119], v[12:15], v[56:59], a[116:119]
ds_read_b64_tr_b16 v[220:221], v243 offset:128
v_mfma_f32_16x16x32_bf16 a[180:183], v[12:15], v[60:63], a[180:183]
s_add_u32 s79, s79, 128
s_xor_b32 s82, s82, 32768
v_mfma_f32_16x16x32_bf16 a[120:123], v[16:19], v[32:35], a[120:123]
ds_read_b64_tr_b16 v[222:223], v243 offset:1152
s_add_u32 m0, s83, 0
v_mfma_f32_16x16x32_bf16 a[184:187], v[16:19], v[36:39], a[184:187]
buffer_load_dwordx4 v232, s[44:47], s80 offen lds
v_mfma_f32_16x16x32_bf16 a[64:67], v[16:19], v[40:43], a[64:67]
ds_read_b128 v[132:135], v236 offset:26624
v_mfma_f32_16x16x32_bf16 a[0:3], v[16:19], v[44:47], a[0:3]
v_mfma_f32_16x16x32_bf16 a[72:75], v[16:19], v[48:51], a[72:75]
ds_read_b128 v[136:139], v236 offset:16384
s_add_u32 m0, s83, 1024
v_mfma_f32_16x16x32_bf16 a[8:11], v[16:19], v[52:55], a[8:11]
buffer_load_dwordx4 v233, s[44:47], s80 offen lds
v_mfma_f32_16x16x32_bf16 a[80:83], v[16:19], v[56:59], a[80:83]
ds_read_b128 v[140:143], v236 offset:18432
v_mfma_f32_16x16x32_bf16 a[16:19], v[16:19], v[60:63], a[16:19]
v_mfma_f32_16x16x32_bf16 a[124:127], v[20:23], v[32:35], a[124:127]
ds_read_b128 v[144:147], v236 offset:8192
s_add_u32 m0, s83, 2176
v_mfma_f32_16x16x32_bf16 a[188:191], v[20:23], v[36:39], a[188:191]
buffer_load_dwordx4 v234, s[44:47], s80 offen lds
v_mfma_f32_16x16x32_bf16 a[68:71], v[20:23], v[40:43], a[68:71]
ds_read_b128 v[148:151], v236 offset:10240
v_mfma_f32_16x16x32_bf16 a[4:7], v[20:23], v[44:47], a[4:7]
v_mfma_f32_16x16x32_bf16 a[76:79], v[20:23], v[48:51], a[76:79]
ds_read_b128 v[152:155], v236 offset:0
s_add_u32 m0, s83, 3200
v_mfma_f32_16x16x32_bf16 a[12:15], v[20:23], v[52:55], a[12:15]
buffer_load_dwordx4 v235, s[44:47], s80 offen lds
v_mfma_f32_16x16x32_bf16 a[84:87], v[20:23], v[56:59], a[84:87]
ds_read_b128 v[156:159], v236 offset:2048
v_mfma_f32_16x16x32_bf16 a[20:23], v[20:23], v[60:63], a[20:23]
s_add_u32 s80, s80, s81
s_add_u32 s83, s83, 16896
v_mfma_f32_16x16x32_bf16 a[88:91], v[24:27], v[32:35], a[88:91]
v_mfma_f32_16x16x32_bf16 a[24:27], v[24:27], v[36:39], a[24:27]
s_cmp_eq_u32 s83, s97
s_cselect_b32 s83, s90, s83
v_mfma_f32_16x16x32_bf16 a[240:243], v[24:27], v[40:43], a[240:243]
v_mfma_f32_16x16x32_bf16 a[32:35], v[24:27], v[44:47], a[32:35]
buffer_load_dword v254, v167, s[68:71], s96 offen
s_add_u32 s96, s96, 1048576
v_mfma_f32_16x16x32_bf16 a[248:251], v[24:27], v[48:51], a[248:251]
v_mfma_f32_16x16x32_bf16 a[40:43], v[24:27], v[52:55], a[40:43]
v_mfma_f32_16x16x32_bf16 a[48:51], v[24:27], v[56:59], a[48:51]
v_mfma_f32_16x16x32_bf16 a[56:59], v[24:27], v[60:63], a[56:59]
buffer_load_dword v254, v167, s[68:71], s96 offen
s_add_u32 s96, s96, 1048576
v_mfma_f32_16x16x32_bf16 a[92:95], v[28:31], v[32:35], a[92:95]
s_lshl_b32 s92, s77, 18
s_cmp_eq_u32 s85, 3
v_mfma_f32_16x16x32_bf16 a[28:31], v[28:31], v[36:39], a[28:31]
s_cselect_b32 s44, s68, s44
s_cselect_b32 s45, s69, s45
v_mfma_f32_16x16x32_bf16 a[244:247], v[28:31], v[40:43], a[244:247]
s_cselect_b32 s46, s70, s46
s_cselect_b32 s81, 524288, s81
v_mfma_f32_16x16x32_bf16 a[36:39], v[28:31], v[44:47], a[36:39]
s_cselect_b32 s42, 0, s42
s_cselect_b32 s80, s92, s80
v_mfma_f32_16x16x32_bf16 a[252:255], v[28:31], v[48:51], a[252:255]
v_mfma_f32_16x16x32_bf16 a[44:47], v[28:31], v[52:55], a[44:47]
v_mfma_f32_16x16x32_bf16 a[52:55], v[28:31], v[56:59], a[52:55]
v_mfma_f32_16x16x32_bf16 a[60:63], v[28:31], v[60:63], a[60:63]
s_waitcnt lgkmcnt(0)
s_waitcnt vmcnt(22)
s_barrier
v_mfma_f32_16x16x32_bf16 a[224:227], v[128:131], v[192:195], a[224:227]
ds_read_b128 v[0:3], v237 offset:24576
v_mfma_f32_16x16x32_bf16 a[232:235], v[128:131], v[196:199], a[232:235]
s_waitcnt vmcnt(14)
buffer_store_dwordx2 v[252:253], off, s[64:67], s91 offset:1024 sc1
v_mfma_f32_16x16x32_bf16 a[208:211], v[128:131], v[200:203], a[208:211]
ds_read_b64_tr_b16 v[32:33], v240 offset:16896
v_mfma_f32_16x16x32_bf16 a[128:131], v[128:131], v[204:207], a[128:131]
s_cmp_eq_u32 s85, 4
s_cselect_b32 s92, s89, 1073741824
v_mfma_f32_16x16x32_bf16 a[216:219], v[128:131], v[208:211], a[216:219]
ds_read_b64_tr_b16 v[34:35], v240 offset:17920
v_mfma_f32_16x16x32_bf16 a[136:139], v[128:131], v[212:215], a[136:139]
buffer_load_dwordx4 v[72:75], v255, s[64:67], s92 offen offset:1024 sc1
v_mfma_f32_16x16x32_bf16 a[192:195], v[128:131], v[216:219], a[192:195]
ds_read_b64_tr_b16 v[36:37], v241 offset:16896
v_mfma_f32_16x16x32_bf16 a[144:147], v[128:131], v[220:223], a[144:147]
s_mov_b32 s91, 1073741824
v_mfma_f32_16x16x32_bf16 a[228:231], v[132:135], v[192:195], a[228:231]
ds_read_b64_tr_b16 v[38:39], v241 offset:17920
v_mfma_f32_16x16x32_bf16 a[236:239], v[132:135], v[196:199], a[236:239]
v_mfma_f32_16x16x32_bf16 a[212:215], v[132:135], v[200:203], a[212:215]
ds_read_b64_tr_b16 v[40:41], v242 offset:16896
s_lshl_b32 s92, s76, 19
v_mfma_f32_16x16x32_bf16 a[132:135], v[132:135], v[204:207], a[132:135]
s_add_u32 s92, s92, 88080384
v_mfma_f32_16x16x32_bf16 a[220:223], v[132:135], v[208:211], a[220:223]
ds_read_b64_tr_b16 v[42:43], v242 offset:17920
v_mfma_f32_16x16x32_bf16 a[140:143], v[132:135], v[212:215], a[140:143]
s_add_u32 s52, s26, s92
s_addc_u32 s53, s27, 0
v_mfma_f32_16x16x32_bf16 a[196:199], v[132:135], v[216:219], a[196:199]
ds_read_b64_tr_b16 v[44:45], v243 offset:16896
s_and_b32 s53, s53, 0xffff
v_mfma_f32_16x16x32_bf16 a[148:151], v[132:135], v[220:223], a[148:151]
s_mov_b32 s54, 524672
s_mov_b32 s55, 0x20000
v_mfma_f32_16x16x32_bf16 a[200:203], v[136:139], v[192:195], a[200:203]
ds_read_b64_tr_b16 v[46:47], v243 offset:17920
s_lshl_b32 s92, s3, 16
v_mfma_f32_16x16x32_bf16 a[152:155], v[136:139], v[196:199], a[152:155]
s_sub_u32 s93, 458752, s92
v_add_u32 v244, s92, v168
v_mfma_f32_16x16x32_bf16 a[96:99], v[136:139], v[200:203], a[96:99]
ds_read_b64_tr_b16 v[48:49], v240 offset:17024
v_add_u32 v246, s93, v168
v_mfma_f32_16x16x32_bf16 a[160:163], v[136:139], v[204:207], a[160:163]
v_add_u32 v245, 0x8000, v244
v_add_u32 v247, 0x8000, v246
v_mfma_f32_16x16x32_bf16 a[104:107], v[136:139], v[208:211], a[104:107]
ds_read_b64_tr_b16 v[50:51], v240 offset:18048
s_lshl_b32 s94, s72, 19
v_mfma_f32_16x16x32_bf16 a[168:171], v[136:139], v[212:215], a[168:171]
s_lshl_b32 s93, s76, 16
s_add_u32 s92, s94, s93
v_mfma_f32_16x16x32_bf16 a[112:115], v[136:139], v[216:219], a[112:115]
ds_read_b64_tr_b16 v[52:53], v241 offset:17024
s_lshl_b32 s93, s77, 13
v_mfma_f32_16x16x32_bf16 a[176:179], v[136:139], v[220:223], a[176:179]
s_add_u32 s92, s92, s93
s_add_u32 s92, s92, 37748736
v_mfma_f32_16x16x32_bf16 a[204:207], v[140:143], v[192:195], a[204:207]
ds_read_b64_tr_b16 v[54:55], v241 offset:18048
v_mfma_f32_16x16x32_bf16 a[156:159], v[140:143], v[196:199], a[156:159]
s_add_u32 s48, s26, s92
s_addc_u32 s49, s27, 0
v_mfma_f32_16x16x32_bf16 a[100:103], v[140:143], v[200:203], a[100:103]
ds_read_b64_tr_b16 v[56:57], v242 offset:17024
s_and_b32 s49, s49, 0xffff
v_mfma_f32_16x16x32_bf16 a[164:167], v[140:143], v[204:207], a[164:167]
s_mov_b32 s50, 57344
s_mov_b32 s51, 0x20000
v_mfma_f32_16x16x32_bf16 a[108:111], v[140:143], v[208:211], a[108:111]
ds_read_b64_tr_b16 v[58:59], v242 offset:18048
s_lshl_b32 s93, s77, 24
v_mfma_f32_16x16x32_bf16 a[172:175], v[140:143], v[212:215], a[172:175]
s_add_u32 s92, s94, s93
s_add_u32 s92, s92, 20975616
v_mfma_f32_16x16x32_bf16 a[116:119], v[140:143], v[216:219], a[116:119]
ds_read_b64_tr_b16 v[60:61], v243 offset:17024
v_mfma_f32_16x16x32_bf16 a[180:183], v[140:143], v[220:223], a[180:183]
s_add_u32 s60, s26, s92
s_addc_u32 s61, s27, 0
v_mfma_f32_16x16x32_bf16 a[120:123], v[144:147], v[192:195], a[120:123]
ds_read_b64_tr_b16 v[62:63], v243 offset:18048
s_and_b32 s61, s61, 0xffff
s_add_u32 m0, s83, 0
v_mfma_f32_16x16x32_bf16 a[184:187], v[144:147], v[196:199], a[184:187]
buffer_load_dwordx4 v232, s[44:47], s80 offen lds
v_mfma_f32_16x16x32_bf16 a[64:67], v[144:147], v[200:203], a[64:67]
ds_read_b128 v[4:7], v237 offset:26624
s_mov_b32 s62, 520192
v_mfma_f32_16x16x32_bf16 a[0:3], v[144:147], v[204:207], a[0:3]
s_mov_b32 s63, 0x20000
s_lshl_b32 s92, s77, 14
v_mfma_f32_16x16x32_bf16 a[72:75], v[144:147], v[208:211], a[72:75]
ds_read_b128 v[8:11], v237 offset:16384
s_sub_u32 s95, 24576, s92
s_add_u32 m0, s83, 1024
v_mfma_f32_16x16x32_bf16 a[8:11], v[144:147], v[212:215], a[8:11]
buffer_load_dwordx4 v233, s[44:47], s80 offen lds
v_mfma_f32_16x16x32_bf16 a[80:83], v[144:147], v[216:219], a[80:83]
ds_read_b128 v[12:15], v237 offset:18432
v_add_u32 v238, s95, v255
v_mfma_f32_16x16x32_bf16 a[16:19], v[144:147], v[220:223], a[16:19]
v_mfma_f32_16x16x32_bf16 a[124:127], v[148:151], v[192:195], a[124:127]
ds_read_b128 v[16:19], v237 offset:8192
s_add_u32 m0, s83, 2176
v_mfma_f32_16x16x32_bf16 a[188:191], v[148:151], v[196:199], a[188:191]
buffer_load_dwordx4 v234, s[44:47], s80 offen lds
v_mfma_f32_16x16x32_bf16 a[68:71], v[148:151], v[200:203], a[68:71]
ds_read_b128 v[20:23], v237 offset:10240
v_mfma_f32_16x16x32_bf16 a[4:7], v[148:151], v[204:207], a[4:7]
v_mfma_f32_16x16x32_bf16 a[76:79], v[148:151], v[208:211], a[76:79]
ds_read_b128 v[24:27], v237 offset:0
s_add_u32 m0, s83, 3200
v_mfma_f32_16x16x32_bf16 a[12:15], v[148:151], v[212:215], a[12:15]
buffer_load_dwordx4 v235, s[44:47], s80 offen lds
v_mfma_f32_16x16x32_bf16 a[84:87], v[148:151], v[216:219], a[84:87]
ds_read_b128 v[28:31], v237 offset:2048
v_mfma_f32_16x16x32_bf16 a[20:23], v[148:151], v[220:223], a[20:23]
s_add_u32 s80, s80, s81
s_add_u32 s83, s83, 16896
v_mfma_f32_16x16x32_bf16 a[88:91], v[152:155], v[192:195], a[88:91]
v_mfma_f32_16x16x32_bf16 a[24:27], v[152:155], v[196:199], a[24:27]
s_cmp_eq_u32 s83, s97
s_cselect_b32 s83, s90, s83
v_mfma_f32_16x16x32_bf16 a[240:243], v[152:155], v[200:203], a[240:243]
v_mfma_f32_16x16x32_bf16 a[32:35], v[152:155], v[204:207], a[32:35]
v_mfma_f32_16x16x32_bf16 a[248:251], v[152:155], v[208:211], a[248:251]
v_mfma_f32_16x16x32_bf16 a[40:43], v[152:155], v[212:215], a[40:43]
v_mfma_f32_16x16x32_bf16 a[48:51], v[152:155], v[216:219], a[48:51]
v_mfma_f32_16x16x32_bf16 a[56:59], v[152:155], v[220:223], a[56:59]
v_mfma_f32_16x16x32_bf16 a[92:95], v[156:159], v[192:195], a[92:95]
v_mfma_f32_16x16x32_bf16 a[28:31], v[156:159], v[196:199], a[28:31]
v_add_u32 v240, s84, v240
v_add_u32 v241, s84, v241
v_mfma_f32_16x16x32_bf16 a[244:247], v[156:159], v[200:203], a[244:247]
v_add_u32 v242, s84, v242
v_add_u32 v243, s84, v243
v_mfma_f32_16x16x32_bf16 a[36:39], v[156:159], v[204:207], a[36:39]
v_xor_b32 v236, 0x8000, v236
v_xor_b32 v237, 0x8000, v237
v_mfma_f32_16x16x32_bf16 a[252:255], v[156:159], v[208:211], a[252:255]
s_sub_u32 s84, 0, s84
s_add_u32 s85, s85, 1
v_mfma_f32_16x16x32_bf16 a[44:47], v[156:159], v[212:215], a[44:47]
s_cmp_lg_u32 s85, 5
v_mfma_f32_16x16x32_bf16 a[52:55], v[156:159], v[216:219], a[52:55]
v_mfma_f32_16x16x32_bf16 a[60:63], v[156:159], v[220:223], a[60:63]
s_waitcnt lgkmcnt(0)
s_cbranch_scc1 .La5_LV_LOOP
s_waitcnt vmcnt(12)
s_barrier
v_mfma_f32_16x16x32_bf16 a[224:227], v[0:3], v[32:35], a[224:227]
s_waitcnt vmcnt(4)
v_mfma_f32_16x16x32_bf16 a[232:235], v[0:3], v[36:39], a[232:235]
v_cmp_eq_u64 vcc, v[72:73], v[252:253]
v_cmp_eq_u64 s[96:97], v[74:75], v[252:253]
s_mov_b32 s91, 0
v_mfma_f32_16x16x32_bf16 a[208:211], v[0:3], v[40:43], a[208:211]
s_and_b64 vcc, vcc, s[96:97]
s_cmp_eq_u64 vcc, -1
s_cbranch_scc0 .La5_LV_XSPIN
.La5_LV_XOK:
v_mfma_f32_16x16x32_bf16 a[128:131], v[0:3], v[44:47], a[128:131]
s_mov_b32 s89, 49152
v_subrev_u32 v240, 8448, v240
v_subrev_u32 v241, 8448, v241
v_mfma_f32_16x16x32_bf16 a[216:219], v[0:3], v[48:51], a[216:219]
v_subrev_u32 v242, 8448, v242
v_subrev_u32 v243, 8448, v243
v_mfma_f32_16x16x32_bf16 a[136:139], v[0:3], v[52:55], a[136:139]
buffer_load_dwordx4 v[128:131], v244, s[52:55], s91 offen offset:0 sc1
v_mfma_f32_16x16x32_bf16 a[192:195], v[0:3], v[56:59], a[192:195]
v_mfma_f32_16x16x32_bf16 a[144:147], v[0:3], v[60:63], a[144:147]
v_mfma_f32_16x16x32_bf16 a[228:231], v[4:7], v[32:35], a[228:231]
ds_read_b64_tr_b16 v[192:193], v240 offset:0
v_mfma_f32_16x16x32_bf16 a[236:239], v[4:7], v[36:39], a[236:239]
buffer_load_dwordx4 v[132:135], v245, s[52:55], s91 offen offset:0 sc1
v_mfma_f32_16x16x32_bf16 a[212:215], v[4:7], v[40:43], a[212:215]
ds_read_b64_tr_b16 v[194:195], v240 offset:1024
v_mfma_f32_16x16x32_bf16 a[132:135], v[4:7], v[44:47], a[132:135]
v_mfma_f32_16x16x32_bf16 a[220:223], v[4:7], v[48:51], a[220:223]
ds_read_b64_tr_b16 v[196:197], v241 offset:0
v_mfma_f32_16x16x32_bf16 a[140:143], v[4:7], v[52:55], a[140:143]
buffer_load_dwordx4 v[136:139], v246, s[52:55], s91 offen offset:0 sc1
v_mfma_f32_16x16x32_bf16 a[196:199], v[4:7], v[56:59], a[196:199]
ds_read_b64_tr_b16 v[198:199], v241 offset:1024
v_accvgpr_read_b32 v224, a224
v_mfma_f32_16x16x32_bf16 a[148:151], v[4:7], v[60:63], a[148:151]
v_accvgpr_read_b32 v225, a225
v_accvgpr_read_b32 v226, a226
v_mfma_f32_16x16x32_bf16 a[200:203], v[8:11], v[32:35], a[200:203]
ds_read_b64_tr_b16 v[200:201], v242 offset:0
v_accvgpr_read_b32 v227, a227
v_mfma_f32_16x16x32_bf16 a[152:155], v[8:11], v[36:39], a[152:155]
buffer_load_dwordx4 v[140:143], v247, s[52:55], s91 offen offset:0 sc1
v_accvgpr_read_b32 v228, a228
v_mfma_f32_16x16x32_bf16 a[96:99], v[8:11], v[40:43], a[96:99]
ds_read_b64_tr_b16 v[202:203], v242 offset:1024
v_accvgpr_read_b32 v229, a229
v_mfma_f32_16x16x32_bf16 a[160:163], v[8:11], v[44:47], a[160:163]
v_accvgpr_read_b32 v230, a230
v_accvgpr_read_b32 v231, a231
v_mfma_f32_16x16x32_bf16 a[104:107], v[8:11], v[48:51], a[104:107]
ds_read_b64_tr_b16 v[204:205], v243 offset:0
v_cvt_pk_bf16_f32 v248, v224, v225
v_mfma_f32_16x16x32_bf16 a[168:171], v[8:11], v[52:55], a[168:171]
buffer_load_dwordx4 v[144:147], v244, s[52:55], s91 offen offset:1024 sc1
v_cvt_pk_bf16_f32 v249, v226, v227
v_mfma_f32_16x16x32_bf16 a[112:115], v[8:11], v[56:59], a[112:115]
ds_read_b64_tr_b16 v[206:207], v243 offset:1024
v_cvt_pk_bf16_f32 v250, v228, v229
v_mfma_f32_16x16x32_bf16 a[176:179], v[8:11], v[60:63], a[176:179]
v_cvt_pk_bf16_f32 v251, v230, v231
buffer_store_dwordx4 v[248:251], v255, s[48:51], s89 offen sc1
v_mfma_f32_16x16x32_bf16 a[204:207], v[12:15], v[32:35], a[204:207]
ds_read_b64_tr_b16 v[208:209], v240 offset:128
s_add_u32 s89, s89, 1024
v_mfma_f32_16x16x32_bf16 a[156:159], v[12:15], v[36:39], a[156:159]
buffer_load_dwordx4 v[148:151], v245, s[52:55], s91 offen offset:1024 sc1
ds_write_b128 v238, v[248:251] offset:0
v_mfma_f32_16x16x32_bf16 a[100:103], v[12:15], v[40:43], a[100:103]
ds_read_b64_tr_b16 v[210:211], v240 offset:1152
v_mfma_f32_16x16x32_bf16 a[164:167], v[12:15], v[44:47], a[164:167]
v_mfma_f32_16x16x32_bf16 a[108:111], v[12:15], v[48:51], a[108:111]
ds_read_b64_tr_b16 v[212:213], v241 offset:128
v_mfma_f32_16x16x32_bf16 a[172:175], v[12:15], v[52:55], a[172:175]
buffer_load_dwordx4 v[152:155], v246, s[52:55], s91 offen offset:1024 sc1
v_mfma_f32_16x16x32_bf16 a[116:119], v[12:15], v[56:59], a[116:119]
ds_read_b64_tr_b16 v[214:215], v241 offset:1152
v_accvgpr_read_b32 v224, a232
v_mfma_f32_16x16x32_bf16 a[180:183], v[12:15], v[60:63], a[180:183]
v_accvgpr_read_b32 v225, a233
v_accvgpr_read_b32 v226, a234
v_mfma_f32_16x16x32_bf16 a[120:123], v[16:19], v[32:35], a[120:123]
ds_read_b64_tr_b16 v[216:217], v242 offset:128
v_accvgpr_read_b32 v227, a235
s_add_u32 m0, s83, 0
v_mfma_f32_16x16x32_bf16 a[184:187], v[16:19], v[36:39], a[184:187]
buffer_load_dwordx4 v232, s[44:47], s80 offen lds
v_mfma_f32_16x16x32_bf16 a[64:67], v[16:19], v[40:43], a[64:67]
ds_read_b64_tr_b16 v[218:219], v242 offset:1152
v_accvgpr_read_b32 v228, a236
v_mfma_f32_16x16x32_bf16 a[0:3], v[16:19], v[44:47], a[0:3]
buffer_load_dwordx4 v[156:159], v247, s[52:55], s91 offen offset:1024 sc1
v_accvgpr_read_b32 v229, a237
v_mfma_f32_16x16x32_bf16 a[72:75], v[16:19], v[48:51], a[72:75]
ds_read_b64_tr_b16 v[220:221], v243 offset:128
v_accvgpr_read_b32 v230, a238
s_add_u32 m0, s83, 1024
v_mfma_f32_16x16x32_bf16 a[8:11], v[16:19], v[52:55], a[8:11]
buffer_load_dwordx4 v233, s[44:47], s80 offen lds
v_mfma_f32_16x16x32_bf16 a[80:83], v[16:19], v[56:59], a[80:83]
ds_read_b64_tr_b16 v[222:223], v243 offset:1152
v_accvgpr_read_b32 v231, a239
v_mfma_f32_16x16x32_bf16 a[16:19], v[16:19], v[60:63], a[16:19]
v_cvt_pk_bf16_f32 v248, v224, v225
v_cvt_pk_bf16_f32 v249, v226, v227
v_mfma_f32_16x16x32_bf16 a[124:127], v[20:23], v[32:35], a[124:127]
v_cvt_pk_bf16_f32 v250, v228, v229
v_cvt_pk_bf16_f32 v251, v230, v231
s_add_u32 m0, s83, 2176
v_mfma_f32_16x16x32_bf16 a[188:191], v[20:23], v[36:39], a[188:191]
buffer_load_dwordx4 v234, s[44:47], s80 offen lds
v_mfma_f32_16x16x32_bf16 a[68:71], v[20:23], v[40:43], a[68:71]
buffer_store_dwordx4 v[248:251], v255, s[48:51], s89 offen sc1
s_add_u32 s89, s89, 1024
v_mfma_f32_16x16x32_bf16 a[4:7], v[20:23], v[44:47], a[4:7]
buffer_load_dwordx4 v[160:163], v244, s[52:55], s91 offen offset:2048 sc1
ds_write_b128 v238, v[248:251] offset:1024
v_mfma_f32_16x16x32_bf16 a[76:79], v[20:23], v[48:51], a[76:79]
s_add_u32 m0, s83, 3200
v_mfma_f32_16x16x32_bf16 a[12:15], v[20:23], v[52:55], a[12:15]
buffer_load_dwordx4 v235, s[44:47], s80 offen lds
v_mfma_f32_16x16x32_bf16 a[84:87], v[20:23], v[56:59], a[84:87]
v_accvgpr_read_b32 v224, a208
v_accvgpr_read_b32 v225, a209
v_mfma_f32_16x16x32_bf16 a[20:23], v[20:23], v[60:63], a[20:23]
s_add_u32 s80, s80, s81
s_add_u32 s83, s83, 16896
v_mfma_f32_16x16x32_bf16 a[88:91], v[24:27], v[32:35], a[88:91]
v_accvgpr_read_b32 v226, a210
v_accvgpr_read_b32 v227, a211
v_mfma_f32_16x16x32_bf16 a[24:27], v[24:27], v[36:39], a[24:27]
s_mov_b32 s83, s90
v_accvgpr_read_b32 v228, a212
v_mfma_f32_16x16x32_bf16 a[240:243], v[24:27], v[40:43], a[240:243]
v_accvgpr_read_b32 v229, a213
v_accvgpr_read_b32 v230, a214
v_mfma_f32_16x16x32_bf16 a[32:35], v[24:27], v[44:47], a[32:35]
buffer_load_dwordx4 v[164:167], v245, s[52:55], s91 offen offset:2048 sc1
v_accvgpr_read_b32 v231, a215
v_mfma_f32_16x16x32_bf16 a[248:251], v[24:27], v[48:51], a[248:251]
v_cvt_pk_bf16_f32 v248, v224, v225
v_cvt_pk_bf16_f32 v249, v226, v227
v_mfma_f32_16x16x32_bf16 a[40:43], v[24:27], v[52:55], a[40:43]
v_cvt_pk_bf16_f32 v250, v228, v229
v_cvt_pk_bf16_f32 v251, v230, v231
v_mfma_f32_16x16x32_bf16 a[48:51], v[24:27], v[56:59], a[48:51]
buffer_store_dwordx4 v[248:251], v255, s[48:51], s89 offen sc1
s_add_u32 s89, s89, 1024
v_mfma_f32_16x16x32_bf16 a[56:59], v[24:27], v[60:63], a[56:59]
buffer_load_dwordx4 v[168:171], v246, s[52:55], s91 offen offset:2048 sc1
ds_write_b128 v238, v[248:251] offset:2048
v_mfma_f32_16x16x32_bf16 a[92:95], v[28:31], v[32:35], a[92:95]
v_mfma_f32_16x16x32_bf16 a[28:31], v[28:31], v[36:39], a[28:31]
v_mfma_f32_16x16x32_bf16 a[244:247], v[28:31], v[40:43], a[244:247]
v_mfma_f32_16x16x32_bf16 a[36:39], v[28:31], v[44:47], a[36:39]
buffer_load_dwordx4 v[172:175], v247, s[52:55], s91 offen offset:2048 sc1
v_mfma_f32_16x16x32_bf16 a[252:255], v[28:31], v[48:51], a[252:255]
v_mfma_f32_16x16x32_bf16 a[44:47], v[28:31], v[52:55], a[44:47]
v_mfma_f32_16x16x32_bf16 a[52:55], v[28:31], v[56:59], a[52:55]
v_mfma_f32_16x16x32_bf16 a[60:63], v[28:31], v[60:63], a[60:63]
; ---- Q tile: 256 rows x 128 columns, K = 1024, while K / V leave the accumulators (memory + .La5_LDS ring); behind it the first step on the diagonal
.La5_LQ_START:
.La5_LW1_5:
s_cbranch_execz .La5_LW1_9
s_waitcnt vmcnt(15) lgkmcnt(6)
v_mfma_f32_16x16x32_bf16 v[0:3], v[192:195], v[128:131], 0
v_accvgpr_read_b32 v224, a128
v_accvgpr_read_b32 v225, a129
s_mov_b32 s85, s95
v_mfma_f32_16x16x32_bf16 v[32:35], v[192:195], v[132:135], 0
buffer_load_dwordx4 v[176:179], v244, s[52:55], s91 offen offset:3072 sc1
v_mfma_f32_16x16x32_bf16 v[64:67], v[192:195], v[136:139], 0
v_accvgpr_read_b32 v226, a130
v_accvgpr_read_b32 v227, a131
v_mfma_f32_16x16x32_bf16 v[96:99], v[192:195], v[140:143], 0
ds_read_b64_tr_b16 v[192:193], v240 offset:8448
ds_read_b64_tr_b16 v[194:195], v240 offset:9472
v_mfma_f32_16x16x32_bf16 v[4:7], v[196:199], v[128:131], 0
v_accvgpr_read_b32 v228, a132
v_accvgpr_read_b32 v229, a133
.La5_LW1_6:
s_cbranch_execz .La5_LW1_10
v_mfma_f32_16x16x32_bf16 v[36:39], v[196:199], v[132:135], 0
buffer_load_dwordx4 v[180:183], v245, s[52:55], s91 offen offset:3072 sc1
v_mfma_f32_16x16x32_bf16 v[68:71], v[196:199], v[136:139], 0
s_waitcnt lgkmcnt(4)
v_accvgpr_read_b32 v230, a134
v_mfma_f32_16x16x32_bf16 v[100:103], v[196:199], v[140:143], 0
ds_read_b64_tr_b16 v[196:197], v241 offset:8448
ds_read_b64_tr_b16 v[198:199], v241 offset:9472
v_mfma_f32_16x16x32_bf16 v[8:11], v[200:203], v[128:131], 0
v_accvgpr_read_b32 v231, a135
v_cvt_pk_bf16_f32 v248, v224, v225
v_mfma_f32_16x16x32_bf16 v[40:43], v[200:203], v[132:135], 0
buffer_load_dwordx4 v[184:187], v246, s[52:55], s91 offen offset:3072 sc1
v_mfma_f32_16x16x32_bf16 v[72:75], v[200:203], v[136:139], 0
v_cvt_pk_bf16_f32 v249, v226, v227
v_cvt_pk_bf16_f32 v250, v228, v229
v_mfma_f32_16x16x32_bf16 v[104:107], v[200:203], v[140:143], 0
ds_read_b64_tr_b16 v[200:201], v242 offset:8448
ds_read_b64_tr_b16 v[202:203], v242 offset:9472
v_mfma_f32_16x16x32_bf16 v[12:15], v[204:207], v[128:131], 0
v_cvt_pk_bf16_f32 v251, v230, v231
s_and_b32 s97, s2, 7
v_mfma_f32_16x16x32_bf16 v[44:47], v[204:207], v[132:135], 0
buffer_load_dwordx4 v[188:191], v247, s[52:55], s91 offen offset:3072 sc1
v_mfma_f32_16x16x32_bf16 v[76:79], v[204:207], v[136:139], 0
s_add_u32 s91, s91, 4096
s_mul_i32 s95, s97, 7
v_mfma_f32_16x16x32_bf16 v[108:111], v[204:207], v[140:143], 0
ds_read_b64_tr_b16 v[204:205], v243 offset:8448
ds_read_b64_tr_b16 v[206:207], v243 offset:9472
v_mfma_f32_16x16x32_bf16 v[16:19], v[208:211], v[128:131], 0
s_lshr_b32 s96, s30, 7
s_add_u32 s95, s95, s96
v_mfma_f32_16x16x32_bf16 v[48:51], v[208:211], v[132:135], 0
s_sub_u32 s95, 3, s95
s_and_b32 s95, s95, 7
v_mfma_f32_16x16x32_bf16 v[80:83], v[208:211], v[136:139], 0
s_min_u32 s95, s95, 6
s_lshl_b32 s95, s95, 1
v_mfma_f32_16x16x32_bf16 v[112:115], v[208:211], v[140:143], 0
ds_read_b64_tr_b16 v[208:209], v240 offset:8576
ds_read_b64_tr_b16 v[210:211], v240 offset:9600
v_mfma_f32_16x16x32_bf16 v[20:23], v[212:215], v[128:131], 0
s_lshr_b32 s92, s2, 6
s_mul_i32 s93, s92, 3
v_mfma_f32_16x16x32_bf16 v[52:55], v[212:215], v[132:135], 0
s_add_u32 s93, s93, s74
v_mfma_f32_16x16x32_bf16 v[84:87], v[212:215], v[136:139], 0
s_cmp_ge_u32 s93, s95
s_cselect_b32 s96, 2, 0
v_mfma_f32_16x16x32_bf16 v[116:119], v[212:215], v[140:143], 0
ds_read_b64_tr_b16 v[212:213], v241 offset:8576
ds_read_b64_tr_b16 v[214:215], v241 offset:9600
v_mfma_f32_16x16x32_bf16 v[24:27], v[216:219], v[128:131], 0
s_add_u32 s93, s93, s96
s_add_u32 s95, s95, s92
v_mfma_f32_16x16x32_bf16 v[56:59], v[216:219], v[132:135], 0
s_cmp_lt_u32 s92, 2
s_cselect_b32 s94, s75, 0
v_mfma_f32_16x16x32_bf16 v[88:91], v[216:219], v[136:139], 0
s_cmp_eq_u32 s74, 3
s_cselect_b32 s92, s95, s93
s_cselect_b32 s94, s94, s75
v_mfma_f32_16x16x32_bf16 v[120:123], v[216:219], v[140:143], 0
ds_read_b64_tr_b16 v[216:217], v242 offset:8576
ds_read_b64_tr_b16 v[218:219], v242 offset:9600
v_mfma_f32_16x16x32_bf16 v[28:31], v[220:223], v[128:131], 0
ds_write_b128 v238, v[248:251] offset:3072
v_mfma_f32_16x16x32_bf16 v[60:63], v[220:223], v[132:135], 0
buffer_store_dwordx4 v[248:251], v255, s[48:51], s89 offen offset:0 sc1
v_mfma_f32_16x16x32_bf16 v[92:95], v[220:223], v[136:139], 0
s_waitcnt vmcnt(14) lgkmcnt(3)
s_mul_i32 s93, s97, 14
v_mfma_f32_16x16x32_bf16 v[124:127], v[220:223], v[140:143], 0
ds_read_b64_tr_b16 v[220:221], v243 offset:8576
ds_read_b64_tr_b16 v[222:223], v243 offset:9600
v_mfma_f32_16x16x32_bf16 v[0:3], v[192:195], v[144:147], v[0:3]
v_accvgpr_read_b32 v224, a216
v_accvgpr_read_b32 v225, a217
v_mfma_f32_16x16x32_bf16 v[32:35], v[192:195], v[148:151], v[32:35]
buffer_load_dwordx4 v[128:131], v244, s[52:55], s91 offen offset:0 sc1
v_mfma_f32_16x16x32_bf16 v[64:67], v[192:195], v[152:155], v[64:67]
v_accvgpr_read_b32 v226, a218
v_accvgpr_read_b32 v227, a219
v_mfma_f32_16x16x32_bf16 v[96:99], v[192:195], v[156:159], v[96:99]
v_accvgpr_read_b32 v228, a220
v_accvgpr_read_b32 v229, a221
v_mfma_f32_16x16x32_bf16 v[4:7], v[196:199], v[144:147], v[4:7]
s_waitcnt lgkmcnt(0)
s_barrier
v_mfma_f32_16x16x32_bf16 v[36:39], v[196:199], v[148:151], v[36:39]
buffer_load_dwordx4 v[132:135], v245, s[52:55], s91 offen offset:0 sc1
v_mfma_f32_16x16x32_bf16 v[68:71], v[196:199], v[152:155], v[68:71]
ds_read_b64_tr_b16 v[192:193], v240 offset:16896
ds_read_b64_tr_b16 v[194:195], v240 offset:17920
v_mfma_f32_16x16x32_bf16 v[100:103], v[196:199], v[156:159], v[100:103]
ds_read_b64_tr_b16 v[196:197], v241 offset:16896
ds_read_b64_tr_b16 v[198:199], v241 offset:17920
v_mfma_f32_16x16x32_bf16 v[8:11], v[200:203], v[144:147], v[8:11]
v_accvgpr_read_b32 v230, a222
v_accvgpr_read_b32 v231, a223
v_mfma_f32_16x16x32_bf16 v[40:43], v[200:203], v[148:151], v[40:43]
buffer_load_dwordx4 v[136:139], v246, s[52:55], s91 offen offset:0 sc1
v_mfma_f32_16x16x32_bf16 v[72:75], v[200:203], v[152:155], v[72:75]
v_cvt_pk_bf16_f32 v248, v224, v225
v_cvt_pk_bf16_f32 v249, v226, v227
v_mfma_f32_16x16x32_bf16 v[104:107], v[200:203], v[156:159], v[104:107]
ds_read_b64_tr_b16 v[200:201], v242 offset:16896
ds_read_b64_tr_b16 v[202:203], v242 offset:17920
v_mfma_f32_16x16x32_bf16 v[12:15], v[204:207], v[144:147], v[12:15]
s_add_u32 m0, s83, 0
v_cvt_pk_bf16_f32 v250, v228, v229
v_mfma_f32_16x16x32_bf16 v[44:47], v[204:207], v[148:151], v[44:47]
buffer_load_dwordx4 v232, s[44:47], s80 offen lds
v_mfma_f32_16x16x32_bf16 v[76:79], v[204:207], v[152:155], v[76:79]
v_cvt_pk_bf16_f32 v251, v230, v231
s_mov_b32 s89, 53248
v_mfma_f32_16x16x32_bf16 v[108:111], v[204:207], v[156:159], v[108:111]
ds_read_b64_tr_b16 v[204:205], v243 offset:16896
ds_read_b64_tr_b16 v[206:207], v243 offset:17920
v_mfma_f32_16x16x32_bf16 v[16:19], v[208:211], v[144:147], v[16:19]
s_add_u32 s92, s92, s93
s_mul_i32 s96, s92, s94
v_mfma_f32_16x16x32_bf16 v[48:51], v[208:211], v[148:151], v[48:51]
buffer_load_dwordx4 v[140:143], v247, s[52:55], s91 offen offset:0 sc1
v_mfma_f32_16x16x32_bf16 v[80:83], v[208:211], v[152:155], v[80:83]
s_mov_b32 s100, s96
s_lshr_b32 s92, s2, 3
v_mfma_f32_16x16x32_bf16 v[112:115], v[208:211], v[156:159], v[112:115]
ds_read_b64_tr_b16 v[208:209], v240 offset:17024
ds_read_b64_tr_b16 v[210:211], v240 offset:18048
v_mfma_f32_16x16x32_bf16 v[20:23], v[212:215], v[144:147], v[20:23]
s_add_u32 m0, s83, 1024
s_andn2_b32 s92, s92, 1
v_mfma_f32_16x16x32_bf16 v[52:55], v[212:215], v[148:151], v[52:55]
buffer_load_dwordx4 v233, s[44:47], s80 offen lds
v_mfma_f32_16x16x32_bf16 v[84:87], v[212:215], v[152:155], v[84:87]
s_and_b32 s93, s2, 7
s_lshl_b32 s95, s92, 16
v_mfma_f32_16x16x32_bf16 v[116:119], v[212:215], v[156:159], v[116:119]
ds_read_b64_tr_b16 v[212:213], v241 offset:17024
ds_read_b64_tr_b16 v[214:215], v241 offset:18048
v_mfma_f32_16x16x32_bf16 v[24:27], v[216:219], v[144:147], v[24:27]
s_lshr_b32 s97, s93, 1
s_lshl_b32 s97, s97, 21
v_mfma_f32_16x16x32_bf16 v[56:59], v[216:219], v[148:151], v[56:59]
s_add_u32 s95, s95, s97
s_and_b32 s97, s93, 1
v_mfma_f32_16x16x32_bf16 v[88:91], v[216:219], v[152:155], v[88:91]
s_lshl_b32 s97, s97, 10
s_add_u32 s95, s95, s97
v_mfma_f32_16x16x32_bf16 v[120:123], v[216:219], v[156:159], v[120:123]
ds_read_b64_tr_b16 v[216:217], v242 offset:17024
ds_read_b64_tr_b16 v[218:219], v242 offset:18048
v_mfma_f32_16x16x32_bf16 v[28:31], v[220:223], v[144:147], v[28:31]
ds_write_b128 v238, v[248:251] offset:4096
v_mfma_f32_16x16x32_bf16 v[60:63], v[220:223], v[148:151], v[60:63]
buffer_store_dwordx4 v[248:251], v255, s[48:51], s89 offen offset:0 sc1
v_mfma_f32_16x16x32_bf16 v[92:95], v[220:223], v[152:155], v[92:95]
s_waitcnt vmcnt(12) lgkmcnt(3)
s_mul_i32 s97, s92, 229376
v_mfma_f32_16x16x32_bf16 v[124:127], v[220:223], v[156:159], v[124:127]
ds_read_b64_tr_b16 v[220:221], v243 offset:17024
ds_read_b64_tr_b16 v[222:223], v243 offset:18048
v_mfma_f32_16x16x32_bf16 v[0:3], v[192:195], v[160:163], v[0:3]
v_accvgpr_read_b32 v224, a136
v_accvgpr_read_b32 v225, a137
v_mfma_f32_16x16x32_bf16 v[32:35], v[192:195], v[164:167], v[32:35]
buffer_load_dwordx4 v[144:147], v244, s[52:55], s91 offen offset:1024 sc1
v_mfma_f32_16x16x32_bf16 v[64:67], v[192:195], v[168:171], v[64:67]
v_accvgpr_read_b32 v226, a138
v_accvgpr_read_b32 v227, a139
v_mfma_f32_16x16x32_bf16 v[96:99], v[192:195], v[172:175], v[96:99]
ds_read_b64_tr_b16 v[192:193], v240 offset:25344
ds_read_b64_tr_b16 v[194:195], v240 offset:26368
v_mfma_f32_16x16x32_bf16 v[4:7], v[196:199], v[160:163], v[4:7]
v_accvgpr_read_b32 v228, a140
v_accvgpr_read_b32 v229, a141
v_mfma_f32_16x16x32_bf16 v[36:39], v[196:199], v[164:167], v[36:39]
buffer_load_dwordx4 v[148:151], v245, s[52:55], s91 offen offset:1024 sc1
v_mfma_f32_16x16x32_bf16 v[68:71], v[196:199], v[168:171], v[68:71]
s_waitcnt lgkmcnt(2)
v_accvgpr_read_b32 v230, a142
v_mfma_f32_16x16x32_bf16 v[100:103], v[196:199], v[172:175], v[100:103]
ds_read_b64_tr_b16 v[196:197], v241 offset:25344
ds_read_b64_tr_b16 v[198:199], v241 offset:26368
v_mfma_f32_16x16x32_bf16 v[8:11], v[200:203], v[160:163], v[8:11]
s_add_u32 m0, s83, 2176
v_accvgpr_read_b32 v231, a143
v_mfma_f32_16x16x32_bf16 v[40:43], v[200:203], v[164:167], v[40:43]
buffer_load_dwordx4 v234, s[44:47], s80 offen lds
v_mfma_f32_16x16x32_bf16 v[72:75], v[200:203], v[168:171], v[72:75]
v_cvt_pk_bf16_f32 v248, v224, v225
v_cvt_pk_bf16_f32 v249, v226, v227
v_mfma_f32_16x16x32_bf16 v[104:107], v[200:203], v[172:175], v[104:107]
ds_read_b64_tr_b16 v[200:201], v242 offset:25344
ds_read_b64_tr_b16 v[202:203], v242 offset:26368
v_mfma_f32_16x16x32_bf16 v[12:15], v[204:207], v[160:163], v[12:15]
v_cvt_pk_bf16_f32 v250, v228, v229
v_cvt_pk_bf16_f32 v251, v230, v231
v_mfma_f32_16x16x32_bf16 v[44:47], v[204:207], v[164:167], v[44:47]
buffer_load_dwordx4 v[152:155], v246, s[52:55], s91 offen offset:1024 sc1
v_mfma_f32_16x16x32_bf16 v[76:79], v[204:207], v[168:171], v[76:79]
s_mul_i32 s38, s93, 896
s_add_u32 s97, s97, s38
v_mfma_f32_16x16x32_bf16 v[108:111], v[204:207], v[172:175], v[108:111]
ds_read_b64_tr_b16 v[204:205], v243 offset:25344
ds_read_b64_tr_b16 v[206:207], v243 offset:26368
v_mfma_f32_16x16x32_bf16 v[16:19], v[208:211], v[160:163], v[16:19]
s_add_u32 m0, s83, 3200
v_mfma_f32_16x16x32_bf16 v[48:51], v[208:211], v[164:167], v[48:51]
buffer_load_dwordx4 v235, s[44:47], s80 offen lds
v_mfma_f32_16x16x32_bf16 v[80:83], v[208:211], v[168:171], v[80:83]
s_add_u32 s80, s80, 524288
v_mfma_f32_16x16x32_bf16 v[112:115], v[208:211], v[172:175], v[112:115]
ds_read_b64_tr_b16 v[208:209], v240 offset:25472
ds_read_b64_tr_b16 v[210:211], v240 offset:26496
v_mfma_f32_16x16x32_bf16 v[20:23], v[212:215], v[160:163], v[20:23]
s_cmp_eq_u32 s3, 0
s_cselect_b32 s95, s95, s97
s_cselect_b64 s[36:37], s[28:29], s[30:31]
s_cselect_b32 s21, 2, 7
v_mfma_f32_16x16x32_bf16 v[52:55], v[212:215], v[164:167], v[52:55]
buffer_load_dwordx4 v[156:159], v247, s[52:55], s91 offen offset:1024 sc1
v_mfma_f32_16x16x32_bf16 v[84:87], v[212:215], v[168:171], v[84:87]
s_cmp_eq_u32 s3, 2
s_cselect_b64 s[36:37], s[32:33], s[36:37]
v_mfma_f32_16x16x32_bf16 v[116:119], v[212:215], v[172:175], v[116:119]
ds_read_b64_tr_b16 v[212:213], v241 offset:25472
ds_read_b64_tr_b16 v[214:215], v241 offset:26496
v_mfma_f32_16x16x32_bf16 v[24:27], v[216:219], v[160:163], v[24:27]
s_lshl_b32 s93, s93, 5
s_add_u32 s92, s92, s93
v_mfma_f32_16x16x32_bf16 v[56:59], v[216:219], v[164:167], v[56:59]
s_mul_i32 s97, s92, 28672
v_mfma_f32_16x16x32_bf16 v[88:91], v[216:219], v[168:171], v[88:91]
s_cmp_eq_u32 s3, 3
s_cselect_b32 s95, s97, s95
s_cselect_b64 s[36:37], s[34:35], s[36:37]
s_cselect_b32 s21, 1, s21
s_cselect_b32 s97, 31, 39
v_mfma_f32_16x16x32_bf16 v[120:123], v[216:219], v[172:175], v[120:123]
ds_read_b64_tr_b16 v[216:217], v242 offset:25472
ds_read_b64_tr_b16 v[218:219], v242 offset:26496
v_mfma_f32_16x16x32_bf16 v[28:31], v[220:223], v[160:163], v[28:31]
ds_write_b128 v238, v[248:251] offset:5120
v_mfma_f32_16x16x32_bf16 v[60:63], v[220:223], v[164:167], v[60:63]
buffer_store_dwordx4 v[248:251], v255, s[48:51], s89 offen offset:1024 sc1
v_mfma_f32_16x16x32_bf16 v[92:95], v[220:223], v[168:171], v[92:95]
s_waitcnt vmcnt(15) lgkmcnt(3)
s_lshl_b32 s21, s21, 10
v_mfma_f32_16x16x32_bf16 v[124:127], v[220:223], v[172:175], v[124:127]
ds_read_b64_tr_b16 v[220:221], v243 offset:25472
ds_read_b64_tr_b16 v[222:223], v243 offset:26496
v_mfma_f32_16x16x32_bf16 v[0:3], v[192:195], v[176:179], v[0:3]
v_accvgpr_read_b32 v224, a192
v_accvgpr_read_b32 v225, a193
v_mfma_f32_16x16x32_bf16 v[32:35], v[192:195], v[180:183], v[32:35]
buffer_load_dwordx4 v[160:163], v244, s[52:55], s91 offen offset:2048 sc1
v_mfma_f32_16x16x32_bf16 v[64:67], v[192:195], v[184:187], v[64:67]
v_accvgpr_read_b32 v226, a194
v_accvgpr_read_b32 v227, a195
v_mfma_f32_16x16x32_bf16 v[96:99], v[192:195], v[188:191], v[96:99]
v_accvgpr_read_b32 v228, a196
v_accvgpr_read_b32 v229, a197
v_mfma_f32_16x16x32_bf16 v[4:7], v[196:199], v[176:179], v[4:7]
s_waitcnt lgkmcnt(0)
s_barrier
v_mfma_f32_16x16x32_bf16 v[36:39], v[196:199], v[180:183], v[36:39]
buffer_load_dwordx4 v[164:167], v245, s[52:55], s91 offen offset:2048 sc1
v_mfma_f32_16x16x32_bf16 v[68:71], v[196:199], v[184:187], v[68:71]
ds_read_b64_tr_b16 v[192:193], v240 offset:33792
ds_read_b64_tr_b16 v[194:195], v240 offset:34816
v_mfma_f32_16x16x32_bf16 v[100:103], v[196:199], v[188:191], v[100:103]
ds_read_b64_tr_b16 v[196:197], v241 offset:33792
ds_read_b64_tr_b16 v[198:199], v241 offset:34816
v_mfma_f32_16x16x32_bf16 v[8:11], v[200:203], v[176:179], v[8:11]
v_accvgpr_read_b32 v230, a198
v_accvgpr_read_b32 v231, a199
v_mfma_f32_16x16x32_bf16 v[40:43], v[200:203], v[180:183], v[40:43]
buffer_load_dwordx4 v[168:171], v246, s[52:55], s91 offen offset:2048 sc1
v_mfma_f32_16x16x32_bf16 v[72:75], v[200:203], v[184:187], v[72:75]
v_cvt_pk_bf16_f32 v248, v224, v225
v_cvt_pk_bf16_f32 v249, v226, v227
v_mfma_f32_16x16x32_bf16 v[104:107], v[200:203], v[188:191], v[104:107]
ds_read_b64_tr_b16 v[200:201], v242 offset:33792
ds_read_b64_tr_b16 v[202:203], v242 offset:34816
v_mfma_f32_16x16x32_bf16 v[12:15], v[204:207], v[176:179], v[12:15]
s_add_u32 m0, s83, 16896
v_cvt_pk_bf16_f32 v250, v228, v229
v_mfma_f32_16x16x32_bf16 v[44:47], v[204:207], v[180:183], v[44:47]
buffer_load_dwordx4 v232, s[44:47], s80 offen lds
v_mfma_f32_16x16x32_bf16 v[76:79], v[204:207], v[184:187], v[76:79]
v_cvt_pk_bf16_f32 v251, v230, v231
v_mfma_f32_16x16x32_bf16 v[108:111], v[204:207], v[188:191], v[108:111]
ds_read_b64_tr_b16 v[204:205], v243 offset:33792
ds_read_b64_tr_b16 v[206:207], v243 offset:34816
v_mfma_f32_16x16x32_bf16 v[16:19], v[208:211], v[176:179], v[16:19]
s_cmp_eq_u32 s75, 1
s_cselect_b32 s97, 23, s97
s_cselect_b32 s92, 0, 24
v_mfma_f32_16x16x32_bf16 v[48:51], v[208:211], v[180:183], v[48:51]
buffer_load_dwordx4 v[172:175], v247, s[52:55], s91 offen offset:2048 sc1
v_mfma_f32_16x16x32_bf16 v[80:83], v[208:211], v[184:187], v[80:83]
s_mul_i32 s92, s92, s21
s_add_u32 s95, s95, s92
v_mfma_f32_16x16x32_bf16 v[112:115], v[208:211], v[188:191], v[112:115]
ds_read_b64_tr_b16 v[208:209], v240 offset:33920
ds_read_b64_tr_b16 v[210:211], v240 offset:34944
v_mfma_f32_16x16x32_bf16 v[20:23], v[212:215], v[176:179], v[20:23]
s_add_u32 m0, s83, 17920
s_mul_i32 s97, s97, s21
v_mfma_f32_16x16x32_bf16 v[52:55], v[212:215], v[180:183], v[52:55]
buffer_load_dwordx4 v233, s[44:47], s80 offen lds
v_mfma_f32_16x16x32_bf16 v[84:87], v[212:215], v[184:187], v[84:87]
s_add_u32 s97, s97, 896
v_mfma_f32_16x16x32_bf16 v[116:119], v[212:215], v[188:191], v[116:119]
ds_read_b64_tr_b16 v[212:213], v241 offset:33920
ds_read_b64_tr_b16 v[214:215], v241 offset:34944
v_mfma_f32_16x16x32_bf16 v[24:27], v[216:219], v[176:179], v[24:27]
s_add_u32 s36, s36, s95
s_addc_u32 s37, s37, 0
v_mfma_f32_16x16x32_bf16 v[56:59], v[216:219], v[180:183], v[56:59]
s_and_b32 s37, s37, 0xffff
s_mov_b32 s38, s97
v_mfma_f32_16x16x32_bf16 v[88:91], v[216:219], v[184:187], v[88:91]
s_mov_b32 s39, 0x20000
s_lshl_b32 s93, s96, 6
v_mfma_f32_16x16x32_bf16 v[120:123], v[216:219], v[188:191], v[120:123]
ds_read_b64_tr_b16 v[216:217], v242 offset:33920
ds_read_b64_tr_b16 v[218:219], v242 offset:34944
v_mfma_f32_16x16x32_bf16 v[28:31], v[220:223], v[176:179], v[28:31]
ds_write_b128 v238, v[248:251] offset:6144
v_mfma_f32_16x16x32_bf16 v[60:63], v[220:223], v[180:183], v[60:63]
buffer_store_dwordx4 v[248:251], v255, s[48:51], s89 offen offset:2048 sc1
v_mfma_f32_16x16x32_bf16 v[92:95], v[220:223], v[184:187], v[92:95]
s_waitcnt vmcnt(16) lgkmcnt(3)
s_mul_i32 s95, s94, 7332928
v_mfma_f32_16x16x32_bf16 v[124:127], v[220:223], v[188:191], v[124:127]
ds_read_b64_tr_b16 v[220:221], v243 offset:33920
ds_read_b64_tr_b16 v[222:223], v243 offset:34944
v_mfma_f32_16x16x32_bf16 v[0:3], v[192:195], v[128:131], v[0:3]
v_accvgpr_read_b32 v224, a144
v_accvgpr_read_b32 v225, a145
v_mfma_f32_16x16x32_bf16 v[32:35], v[192:195], v[132:135], v[32:35]
buffer_load_dwordx4 v[176:179], v244, s[52:55], s91 offen offset:3072 sc1
v_mfma_f32_16x16x32_bf16 v[64:67], v[192:195], v[136:139], v[64:67]
v_accvgpr_read_b32 v226, a146
v_accvgpr_read_b32 v227, a147
v_mfma_f32_16x16x32_bf16 v[96:99], v[192:195], v[140:143], v[96:99]
ds_read_b64_tr_b16 v[192:193], v240 offset:42240
ds_read_b64_tr_b16 v[194:195], v240 offset:43264
v_mfma_f32_16x16x32_bf16 v[4:7], v[196:199], v[128:131], v[4:7]
v_accvgpr_read_b32 v228, a148
v_accvgpr_read_b32 v229, a149
v_mfma_f32_16x16x32_bf16 v[36:39], v[196:199], v[132:135], v[36:39]
buffer_load_dwordx4 v[180:183], v245, s[52:55], s91 offen offset:3072 sc1
v_mfma_f32_16x16x32_bf16 v[68:71], v[196:199], v[136:139], v[68:71]
s_waitcnt lgkmcnt(2)
v_accvgpr_read_b32 v230, a150
v_mfma_f32_16x16x32_bf16 v[100:103], v[196:199], v[140:143], v[100:103]
ds_read_b64_tr_b16 v[196:197], v241 offset:42240
ds_read_b64_tr_b16 v[198:199], v241 offset:43264
v_mfma_f32_16x16x32_bf16 v[8:11], v[200:203], v[128:131], v[8:11]
s_add_u32 m0, s83, 19072
v_accvgpr_read_b32 v231, a151
v_mfma_f32_16x16x32_bf16 v[40:43], v[200:203], v[132:135], v[40:43]
buffer_load_dwordx4 v234, s[44:47], s80 offen lds
v_mfma_f32_16x16x32_bf16 v[72:75], v[200:203], v[136:139], v[72:75]
v_cvt_pk_bf16_f32 v248, v224, v225
v_cvt_pk_bf16_f32 v249, v226, v227
v_mfma_f32_16x16x32_bf16 v[104:107], v[200:203], v[140:143], v[104:107]
ds_read_b64_tr_b16 v[200:201], v242 offset:42240
ds_read_b64_tr_b16 v[202:203], v242 offset:43264
v_mfma_f32_16x16x32_bf16 v[12:15], v[204:207], v[128:131], v[12:15]
v_cvt_pk_bf16_f32 v250, v228, v229
v_cvt_pk_bf16_f32 v251, v230, v231
v_mfma_f32_16x16x32_bf16 v[44:47], v[204:207], v[132:135], v[44:47]
buffer_load_dwordx4 v[184:187], v246, s[52:55], s91 offen offset:3072 sc1
v_mfma_f32_16x16x32_bf16 v[76:79], v[204:207], v[136:139], v[76:79]
s_cmp_eq_u32 s77, 0
s_cselect_b64 s[40:41], s[30:31], s[32:33]
v_mfma_f32_16x16x32_bf16 v[108:111], v[204:207], v[140:143], v[108:111]
ds_read_b64_tr_b16 v[204:205], v243 offset:42240
ds_read_b64_tr_b16 v[206:207], v243 offset:43264
v_mfma_f32_16x16x32_bf16 v[16:19], v[208:211], v[128:131], v[16:19]
s_add_u32 m0, s83, 20096
v_mfma_f32_16x16x32_bf16 v[48:51], v[208:211], v[132:135], v[48:51]
buffer_load_dwordx4 v235, s[44:47], s80 offen lds
v_mfma_f32_16x16x32_bf16 v[80:83], v[208:211], v[136:139], v[80:83]
s_add_u32 s80, s80, 524288
v_mfma_f32_16x16x32_bf16 v[112:115], v[208:211], v[140:143], v[112:115]
ds_read_b64_tr_b16 v[208:209], v240 offset:42368
ds_read_b64_tr_b16 v[210:211], v240 offset:43392
v_mfma_f32_16x16x32_bf16 v[20:23], v[212:215], v[128:131], v[20:23]
s_add_u32 s40, s40, s93
s_addc_u32 s41, s41, 0
v_mfma_f32_16x16x32_bf16 v[52:55], v[212:215], v[132:135], v[52:55]
buffer_load_dwordx4 v[188:191], v247, s[52:55], s91 offen offset:3072 sc1
v_mfma_f32_16x16x32_bf16 v[84:87], v[212:215], v[136:139], v[84:87]
s_add_u32 s91, s91, 4096
s_and_b32 s41, s41, 0xffff
v_mfma_f32_16x16x32_bf16 v[116:119], v[212:215], v[140:143], v[116:119]
ds_read_b64_tr_b16 v[212:213], v241 offset:42368
ds_read_b64_tr_b16 v[214:215], v241 offset:43392
v_mfma_f32_16x16x32_bf16 v[24:27], v[216:219], v[128:131], v[24:27]
s_mov_b32 s42, s95
s_mov_b32 s43, 0x20000
v_mfma_f32_16x16x32_bf16 v[56:59], v[216:219], v[132:135], v[56:59]
s_mov_b32 s92, s72
s_xor_b32 s92, s92, 1
v_mfma_f32_16x16x32_bf16 v[88:91], v[216:219], v[136:139], v[88:91]
s_and_b32 s101, s92, 7
s_lshl_b32 s92, s101, 8
v_mfma_f32_16x16x32_bf16 v[120:123], v[216:219], v[140:143], v[120:123]
ds_read_b64_tr_b16 v[216:217], v242 offset:42368
ds_read_b64_tr_b16 v[218:219], v242 offset:43392
v_mfma_f32_16x16x32_bf16 v[28:31], v[220:223], v[128:131], v[28:31]
ds_write_b128 v238, v[248:251] offset:7168
v_mfma_f32_16x16x32_bf16 v[60:63], v[220:223], v[132:135], v[60:63]
buffer_store_dwordx4 v[248:251], v255, s[48:51], s89 offen offset:3072 sc1
v_mfma_f32_16x16x32_bf16 v[92:95], v[220:223], v[136:139], v[92:95]
s_waitcnt vmcnt(15) lgkmcnt(3)
s_sub_u32 s93, 8388608, s92
v_mfma_f32_16x16x32_bf16 v[124:127], v[220:223], v[140:143], v[124:127]
ds_read_b64_tr_b16 v[220:221], v243 offset:42368
ds_read_b64_tr_b16 v[222:223], v243 offset:43392
v_mfma_f32_16x16x32_bf16 v[0:3], v[192:195], v[144:147], v[0:3]
v_accvgpr_read_b32 v224, a200
v_accvgpr_read_b32 v225, a201
v_mfma_f32_16x16x32_bf16 v[32:35], v[192:195], v[148:151], v[32:35]
buffer_load_dwordx4 v[128:131], v244, s[52:55], s91 offen offset:0 sc1
v_mfma_f32_16x16x32_bf16 v[64:67], v[192:195], v[152:155], v[64:67]
v_accvgpr_read_b32 v226, a202
v_accvgpr_read_b32 v227, a203
v_mfma_f32_16x16x32_bf16 v[96:99], v[192:195], v[156:159], v[96:99]
v_accvgpr_read_b32 v228, a204
v_accvgpr_read_b32 v229, a205
v_mfma_f32_16x16x32_bf16 v[4:7], v[196:199], v[144:147], v[4:7]
s_waitcnt lgkmcnt(0)
s_barrier
v_mfma_f32_16x16x32_bf16 v[36:39], v[196:199], v[148:151], v[36:39]
buffer_load_dwordx4 v[132:135], v245, s[52:55], s91 offen offset:0 sc1
v_mfma_f32_16x16x32_bf16 v[68:71], v[196:199], v[152:155], v[68:71]
ds_read_b64_tr_b16 v[192:193], v240 offset:50688
ds_read_b64_tr_b16 v[194:195], v240 offset:51712
v_mfma_f32_16x16x32_bf16 v[100:103], v[196:199], v[156:159], v[100:103]
ds_read_b64_tr_b16 v[196:197], v241 offset:50688
ds_read_b64_tr_b16 v[198:199], v241 offset:51712
v_mfma_f32_16x16x32_bf16 v[8:11], v[200:203], v[144:147], v[8:11]
v_accvgpr_read_b32 v230, a206
v_accvgpr_read_b32 v231, a207
v_mfma_f32_16x16x32_bf16 v[40:43], v[200:203], v[148:151], v[40:43]
buffer_load_dwordx4 v[136:139], v246, s[52:55], s91 offen offset:0 sc1
v_mfma_f32_16x16x32_bf16 v[72:75], v[200:203], v[152:155], v[72:75]
v_cvt_pk_bf16_f32 v248, v224, v225
v_cvt_pk_bf16_f32 v249, v226, v227
v_mfma_f32_16x16x32_bf16 v[104:107], v[200:203], v[156:159], v[104:107]
ds_read_b64_tr_b16 v[200:201], v242 offset:50688
ds_read_b64_tr_b16 v[202:203], v242 offset:51712
v_mfma_f32_16x16x32_bf16 v[12:15], v[204:207], v[144:147], v[12:15]
s_add_u32 m0, s83, 33792
v_cvt_pk_bf16_f32 v250, v228, v229
v_mfma_f32_16x16x32_bf16 v[44:47], v[204:207], v[148:151], v[44:47]
buffer_load_dwordx4 v232, s[44:47], s80 offen lds
v_mfma_f32_16x16x32_bf16 v[76:79], v[204:207], v[152:155], v[76:79]
v_cvt_pk_bf16_f32 v251, v230, v231
s_mov_b32 s89, 32768
v_mfma_f32_16x16x32_bf16 v[108:111], v[204:207], v[156:159], v[108:111]
ds_read_b64_tr_b16 v[204:205], v243 offset:50688
ds_read_b64_tr_b16 v[206:207], v243 offset:51712
v_mfma_f32_16x16x32_bf16 v[16:19], v[208:211], v[144:147], v[16:19]
s_add_u32 s56, s28, s92
s_addc_u32 s57, s29, 0
v_mfma_f32_16x16x32_bf16 v[48:51], v[208:211], v[148:151], v[48:51]
buffer_load_dwordx4 v[140:143], v247, s[52:55], s91 offen offset:0 sc1
v_mfma_f32_16x16x32_bf16 v[80:83], v[208:211], v[152:155], v[80:83]
s_and_b32 s57, s57, 0xffff
s_mov_b32 s58, s93
v_mfma_f32_16x16x32_bf16 v[112:115], v[208:211], v[156:159], v[112:115]
ds_read_b64_tr_b16 v[208:209], v240 offset:50816
ds_read_b64_tr_b16 v[210:211], v240 offset:51840
v_mfma_f32_16x16x32_bf16 v[20:23], v[212:215], v[144:147], v[20:23]
s_add_u32 m0, s83, 34816
s_mov_b32 s59, 0x20000
v_mfma_f32_16x16x32_bf16 v[52:55], v[212:215], v[148:151], v[52:55]
buffer_load_dwordx4 v233, s[44:47], s80 offen lds
v_mfma_f32_16x16x32_bf16 v[84:87], v[212:215], v[152:155], v[84:87]
s_lshl_b32 s93, s96, 16
s_lshl_b32 s95, s94, 16
v_mfma_f32_16x16x32_bf16 v[116:119], v[212:215], v[156:159], v[116:119]
ds_read_b64_tr_b16 v[212:213], v241 offset:50816
ds_read_b64_tr_b16 v[214:215], v241 offset:51840
v_mfma_f32_16x16x32_bf16 v[24:27], v[216:219], v[144:147], v[24:27]
s_add_u32 s28, s34, s93
s_addc_u32 s29, s35, 0
v_mfma_f32_16x16x32_bf16 v[56:59], v[216:219], v[148:151], v[56:59]
s_and_b32 s29, s29, 0xffff
s_mov_b32 s30, s95
v_mfma_f32_16x16x32_bf16 v[88:91], v[216:219], v[152:155], v[88:91]
s_mov_b32 s31, 0x20000
s_lshl_b32 s93, s101, 12
v_mfma_f32_16x16x32_bf16 v[120:123], v[216:219], v[156:159], v[120:123]
ds_read_b64_tr_b16 v[216:217], v242 offset:50816
ds_read_b64_tr_b16 v[218:219], v242 offset:51840
v_mfma_f32_16x16x32_bf16 v[28:31], v[220:223], v[144:147], v[28:31]
ds_write_b128 v238, v[248:251] offset:32768
v_mfma_f32_16x16x32_bf16 v[60:63], v[220:223], v[148:151], v[60:63]
buffer_store_dwordx4 v[248:251], v255, s[48:51], s89 offen offset:0 sc1
v_mfma_f32_16x16x32_bf16 v[92:95], v[220:223], v[152:155], v[92:95]
s_waitcnt vmcnt(16) lgkmcnt(3)
s_add_u32 s92, s93, 71303168
v_mfma_f32_16x16x32_bf16 v[124:127], v[220:223], v[156:159], v[124:127]
ds_read_b64_tr_b16 v[220:221], v243 offset:50816
ds_read_b64_tr_b16 v[222:223], v243 offset:51840
v_mfma_f32_16x16x32_bf16 v[0:3], v[192:195], v[160:163], v[0:3]
v_accvgpr_read_b32 v224, a152
v_accvgpr_read_b32 v225, a153
v_mfma_f32_16x16x32_bf16 v[32:35], v[192:195], v[164:167], v[32:35]
buffer_load_dwordx4 v[144:147], v244, s[52:55], s91 offen offset:1024 sc1
v_mfma_f32_16x16x32_bf16 v[64:67], v[192:195], v[168:171], v[64:67]
v_accvgpr_read_b32 v226, a154
v_accvgpr_read_b32 v227, a155
v_mfma_f32_16x16x32_bf16 v[96:99], v[192:195], v[172:175], v[96:99]
ds_read_b64_tr_b16 v[192:193], v240 offset:59136
ds_read_b64_tr_b16 v[194:195], v240 offset:60160
v_mfma_f32_16x16x32_bf16 v[4:7], v[196:199], v[160:163], v[4:7]
v_accvgpr_read_b32 v228, a156
v_accvgpr_read_b32 v229, a157
v_mfma_f32_16x16x32_bf16 v[36:39], v[196:199], v[164:167], v[36:39]
buffer_load_dwordx4 v[148:151], v245, s[52:55], s91 offen offset:1024 sc1
v_mfma_f32_16x16x32_bf16 v[68:71], v[196:199], v[168:171], v[68:71]
s_waitcnt lgkmcnt(2)
v_accvgpr_read_b32 v230, a158
v_mfma_f32_16x16x32_bf16 v[100:103], v[196:199], v[172:175], v[100:103]
ds_read_b64_tr_b16 v[196:197], v241 offset:59136
ds_read_b64_tr_b16 v[198:199], v241 offset:60160
v_mfma_f32_16x16x32_bf16 v[8:11], v[200:203], v[160:163], v[8:11]
s_add_u32 m0, s83, 35968
v_accvgpr_read_b32 v231, a159
v_mfma_f32_16x16x32_bf16 v[40:43], v[200:203], v[164:167], v[40:43]
buffer_load_dwordx4 v234, s[44:47], s80 offen lds
v_mfma_f32_16x16x32_bf16 v[72:75], v[200:203], v[168:171], v[72:75]
v_cvt_pk_bf16_f32 v248, v224, v225
v_cvt_pk_bf16_f32 v249, v226, v227
v_mfma_f32_16x16x32_bf16 v[104:107], v[200:203], v[172:175], v[104:107]
ds_read_b64_tr_b16 v[200:201], v242 offset:59136
ds_read_b64_tr_b16 v[202:203], v242 offset:60160
v_mfma_f32_16x16x32_bf16 v[12:15], v[204:207], v[160:163], v[12:15]
v_cvt_pk_bf16_f32 v250, v228, v229
v_cvt_pk_bf16_f32 v251, v230, v231
v_mfma_f32_16x16x32_bf16 v[44:47], v[204:207], v[164:167], v[44:47]
buffer_load_dwordx4 v[152:155], v246, s[52:55], s91 offen offset:1024 sc1
v_mfma_f32_16x16x32_bf16 v[76:79], v[204:207], v[168:171], v[76:79]
s_sub_u32 s93, 25165824, s93
v_mfma_f32_16x16x32_bf16 v[108:111], v[204:207], v[172:175], v[108:111]
ds_read_b64_tr_b16 v[204:205], v243 offset:59136
ds_read_b64_tr_b16 v[206:207], v243 offset:60160
.La5_LW1_7:
s_cbranch_execz .La5_LW1_11
v_mfma_f32_16x16x32_bf16 v[16:19], v[208:211], v[160:163], v[16:19]
s_add_u32 m0, s83, 36992
v_mfma_f32_16x16x32_bf16 v[48:51], v[208:211], v[164:167], v[48:51]
buffer_load_dwordx4 v235, s[44:47], s80 offen lds
v_mfma_f32_16x16x32_bf16 v[80:83], v[208:211], v[168:171], v[80:83]
s_add_u32 s80, s80, 524288
v_mfma_f32_16x16x32_bf16 v[112:115], v[208:211], v[172:175], v[112:115]
ds_read_b64_tr_b16 v[208:209], v240 offset:59264
ds_read_b64_tr_b16 v[210:211], v240 offset:60288
v_mfma_f32_16x16x32_bf16 v[20:23], v[212:215], v[160:163], v[20:23]
s_add_u32 s32, s26, s92
s_addc_u32 s33, s27, 0
v_mfma_f32_16x16x32_bf16 v[52:55], v[212:215], v[164:167], v[52:55]
buffer_load_dwordx4 v[156:159], v247, s[52:55], s91 offen offset:1024 sc1
v_mfma_f32_16x16x32_bf16 v[84:87], v[212:215], v[168:171], v[84:87]
s_and_b32 s33, s33, 0xffff
s_mov_b32 s34, s93
v_mfma_f32_16x16x32_bf16 v[116:119], v[212:215], v[172:175], v[116:119]
ds_read_b64_tr_b16 v[212:213], v241 offset:59264
ds_read_b64_tr_b16 v[214:215], v241 offset:60288
v_mfma_f32_16x16x32_bf16 v[24:27], v[216:219], v[160:163], v[24:27]
s_mov_b32 s35, 0x20000
s_lshl2_add_u32 s92, s72, s74
v_mfma_f32_16x16x32_bf16 v[56:59], v[216:219], v[164:167], v[56:59]
s_lshl2_add_u32 s92, s92, s3
s_lshl_b32 s88, s92, 8
v_mfma_f32_16x16x32_bf16 v[88:91], v[216:219], v[168:171], v[88:91]
s_mul_i32 s93, s92, 17408
s_lshl_b32 s92, s92, 14
v_mfma_f32_16x16x32_bf16 v[120:123], v[216:219], v[172:175], v[120:123]
ds_read_b64_tr_b16 v[216:217], v242 offset:59264
ds_read_b64_tr_b16 v[218:219], v242 offset:60288
v_mfma_f32_16x16x32_bf16 v[28:31], v[220:223], v[160:163], v[28:31]
ds_write_b128 v238, v[248:251] offset:33792
v_mfma_f32_16x16x32_bf16 v[60:63], v[220:223], v[164:167], v[60:63]
buffer_store_dwordx4 v[248:251], v255, s[48:51], s89 offen offset:1024 sc1
v_mfma_f32_16x16x32_bf16 v[92:95], v[220:223], v[168:171], v[92:95]
s_waitcnt vmcnt(15) lgkmcnt(3)
s_add_u32 s92, s92, 4194304
v_mfma_f32_16x16x32_bf16 v[124:127], v[220:223], v[172:175], v[124:127]
ds_read_b64_tr_b16 v[220:221], v243 offset:59264
ds_read_b64_tr_b16 v[222:223], v243 offset:60288
v_mfma_f32_16x16x32_bf16 v[0:3], v[192:195], v[176:179], v[0:3]
v_accvgpr_read_b32 v224, a96
v_accvgpr_read_b32 v225, a97
v_mfma_f32_16x16x32_bf16 v[32:35], v[192:195], v[180:183], v[32:35]
buffer_load_dwordx4 v[160:163], v244, s[52:55], s91 offen offset:2048 sc1
v_mfma_f32_16x16x32_bf16 v[64:67], v[192:195], v[184:187], v[64:67]
v_accvgpr_read_b32 v226, a98
v_accvgpr_read_b32 v227, a99
v_mfma_f32_16x16x32_bf16 v[96:99], v[192:195], v[188:191], v[96:99]
v_accvgpr_read_b32 v228, a100
v_accvgpr_read_b32 v229, a101
v_mfma_f32_16x16x32_bf16 v[4:7], v[196:199], v[176:179], v[4:7]
s_waitcnt lgkmcnt(0)
s_barrier
v_mfma_f32_16x16x32_bf16 v[36:39], v[196:199], v[180:183], v[36:39]
buffer_load_dwordx4 v[164:167], v245, s[52:55], s91 offen offset:2048 sc1
v_mfma_f32_16x16x32_bf16 v[68:71], v[196:199], v[184:187], v[68:71]
ds_read_b64_tr_b16 v[192:193], v240 offset:0
ds_read_b64_tr_b16 v[194:195], v240 offset:1024
v_mfma_f32_16x16x32_bf16 v[100:103], v[196:199], v[188:191], v[100:103]
ds_read_b64_tr_b16 v[196:197], v241 offset:0
ds_read_b64_tr_b16 v[198:199], v241 offset:1024
v_mfma_f32_16x16x32_bf16 v[8:11], v[200:203], v[176:179], v[8:11]
v_accvgpr_read_b32 v230, a102
v_accvgpr_read_b32 v231, a103
v_mfma_f32_16x16x32_bf16 v[40:43], v[200:203], v[180:183], v[40:43]
buffer_load_dwordx4 v[168:171], v246, s[52:55], s91 offen offset:2048 sc1
v_mfma_f32_16x16x32_bf16 v[72:75], v[200:203], v[184:187], v[72:75]
v_cvt_pk_bf16_f32 v248, v224, v225
v_cvt_pk_bf16_f32 v249, v226, v227
v_mfma_f32_16x16x32_bf16 v[104:107], v[200:203], v[188:191], v[104:107]
ds_read_b64_tr_b16 v[200:201], v242 offset:0
ds_read_b64_tr_b16 v[202:203], v242 offset:1024
v_mfma_f32_16x16x32_bf16 v[12:15], v[204:207], v[176:179], v[12:15]
s_add_u32 m0, s83, 50688
v_cvt_pk_bf16_f32 v250, v228, v229
v_mfma_f32_16x16x32_bf16 v[44:47], v[204:207], v[180:183], v[44:47]
buffer_load_dwordx4 v232, s[44:47], s80 offen lds
v_mfma_f32_16x16x32_bf16 v[76:79], v[204:207], v[184:187], v[76:79]
v_cvt_pk_bf16_f32 v251, v230, v231
v_mfma_f32_16x16x32_bf16 v[108:111], v[204:207], v[188:191], v[108:111]
ds_read_b64_tr_b16 v[204:205], v243 offset:0
ds_read_b64_tr_b16 v[206:207], v243 offset:1024
v_mfma_f32_16x16x32_bf16 v[16:19], v[208:211], v[176:179], v[16:19]
s_add_u32 s86, s26, s92
s_addc_u32 s87, s27, 0
v_mfma_f32_16x16x32_bf16 v[48:51], v[208:211], v[180:183], v[48:51]
buffer_load_dwordx4 v[172:175], v247, s[52:55], s91 offen offset:2048 sc1
v_mfma_f32_16x16x32_bf16 v[80:83], v[208:211], v[184:187], v[80:83]
s_and_b32 s87, s87, 0xffff
s_add_u32 s93, s93, 150994944
v_mfma_f32_16x16x32_bf16 v[112:115], v[208:211], v[188:191], v[112:115]
ds_read_b64_tr_b16 v[208:209], v240 offset:128
ds_read_b64_tr_b16 v[210:211], v240 offset:1152
v_mfma_f32_16x16x32_bf16 v[20:23], v[212:215], v[176:179], v[20:23]
s_add_u32 m0, s83, 51712
v_mfma_f32_16x16x32_bf16 v[52:55], v[212:215], v[180:183], v[52:55]
buffer_load_dwordx4 v233, s[44:47], s80 offen lds
v_mfma_f32_16x16x32_bf16 v[84:87], v[212:215], v[184:187], v[84:87]
s_add_u32 s68, s26, s93
s_addc_u32 s69, s27, 0
v_mfma_f32_16x16x32_bf16 v[116:119], v[212:215], v[188:191], v[116:119]
ds_read_b64_tr_b16 v[212:213], v241 offset:128
ds_read_b64_tr_b16 v[214:215], v241 offset:1152
v_mfma_f32_16x16x32_bf16 v[24:27], v[216:219], v[176:179], v[24:27]
s_and_b32 s69, s69, 0xffff
s_mov_b32 s70, 17408
v_mfma_f32_16x16x32_bf16 v[56:59], v[216:219], v[180:183], v[56:59]
s_mov_b32 s96, 152962078
s_mov_b32 s97, 371069214
v_mfma_f32_16x16x32_bf16 v[88:91], v[216:219], v[184:187], v[88:91]
s_lshl_b32 s95, s73, 3
s_lshr_b64 s[96:97], s[96:97], s95
v_mfma_f32_16x16x32_bf16 v[120:123], v[216:219], v[188:191], v[120:123]
ds_read_b64_tr_b16 v[216:217], v242 offset:128
ds_read_b64_tr_b16 v[218:219], v242 offset:1152
v_mfma_f32_16x16x32_bf16 v[28:31], v[220:223], v[176:179], v[28:31]
ds_write_b128 v238, v[248:251] offset:34816
v_mfma_f32_16x16x32_bf16 v[60:63], v[220:223], v[180:183], v[60:63]
buffer_store_dwordx4 v[248:251], v255, s[48:51], s89 offen offset:2048 sc1
v_mfma_f32_16x16x32_bf16 v[92:95], v[220:223], v[184:187], v[92:95]
s_waitcnt vmcnt(16) lgkmcnt(3)
s_and_b32 s82, s96, 255
v_mfma_f32_16x16x32_bf16 v[124:127], v[220:223], v[188:191], v[124:127]
ds_read_b64_tr_b16 v[220:221], v243 offset:128
ds_read_b64_tr_b16 v[222:223], v243 offset:1152
v_mfma_f32_16x16x32_bf16 v[0:3], v[192:195], v[128:131], v[0:3]
v_accvgpr_read_b32 v224, a160
v_accvgpr_read_b32 v225, a161
v_mfma_f32_16x16x32_bf16 v[32:35], v[192:195], v[132:135], v[32:35]
buffer_load_dwordx4 v[176:179], v244, s[52:55], s91 offen offset:3072 sc1
v_mfma_f32_16x16x32_bf16 v[64:67], v[192:195], v[136:139], v[64:67]
v_accvgpr_read_b32 v226, a162
v_accvgpr_read_b32 v227, a163
v_mfma_f32_16x16x32_bf16 v[96:99], v[192:195], v[140:143], v[96:99]
ds_read_b64_tr_b16 v[192:193], v240 offset:8448
ds_read_b64_tr_b16 v[194:195], v240 offset:9472
v_mfma_f32_16x16x32_bf16 v[4:7], v[196:199], v[128:131], v[4:7]
v_accvgpr_read_b32 v228, a164
v_accvgpr_read_b32 v229, a165
v_mfma_f32_16x16x32_bf16 v[36:39], v[196:199], v[132:135], v[36:39]
buffer_load_dwordx4 v[180:183], v245, s[52:55], s91 offen offset:3072 sc1
v_mfma_f32_16x16x32_bf16 v[68:71], v[196:199], v[136:139], v[68:71]
s_waitcnt lgkmcnt(2)
v_accvgpr_read_b32 v230, a166
v_mfma_f32_16x16x32_bf16 v[100:103], v[196:199], v[140:143], v[100:103]
ds_read_b64_tr_b16 v[196:197], v241 offset:8448
ds_read_b64_tr_b16 v[198:199], v241 offset:9472
v_mfma_f32_16x16x32_bf16 v[8:11], v[200:203], v[128:131], v[8:11]
s_add_u32 m0, s83, 52864
v_accvgpr_read_b32 v231, a167
v_mfma_f32_16x16x32_bf16 v[40:43], v[200:203], v[132:135], v[40:43]
buffer_load_dwordx4 v234, s[44:47], s80 offen lds
v_mfma_f32_16x16x32_bf16 v[72:75], v[200:203], v[136:139], v[72:75]
v_cvt_pk_bf16_f32 v248, v224, v225
v_cvt_pk_bf16_f32 v249, v226, v227
v_mfma_f32_16x16x32_bf16 v[104:107], v[200:203], v[140:143], v[104:107]
ds_read_b64_tr_b16 v[200:201], v242 offset:8448
ds_read_b64_tr_b16 v[202:203], v242 offset:9472
v_mfma_f32_16x16x32_bf16 v[12:15], v[204:207], v[128:131], v[12:15]
v_cvt_pk_bf16_f32 v250, v228, v229
v_cvt_pk_bf16_f32 v251, v230, v231
v_mfma_f32_16x16x32_bf16 v[44:47], v[204:207], v[132:135], v[44:47]
buffer_load_dwordx4 v[184:187], v246, s[52:55], s91 offen offset:3072 sc1
v_mfma_f32_16x16x32_bf16 v[76:79], v[204:207], v[136:139], v[76:79]
s_lshl_b32 s95, 1, s82
s_lshl_b32 s96, s75, 22
v_mfma_f32_16x16x32_bf16 v[108:111], v[204:207], v[140:143], v[108:111]
ds_read_b64_tr_b16 v[204:205], v243 offset:8448
ds_read_b64_tr_b16 v[206:207], v243 offset:9472
v_mfma_f32_16x16x32_bf16 v[16:19], v[208:211], v[128:131], v[16:19]
s_add_u32 m0, s83, 53888
s_or_b32 s95, s95, s96
v_mfma_f32_16x16x32_bf16 v[48:51], v[208:211], v[132:135], v[48:51]
buffer_load_dwordx4 v235, s[44:47], s80 offen lds
v_mfma_f32_16x16x32_bf16 v[80:83], v[208:211], v[136:139], v[80:83]
s_add_u32 s80, s80, 524288
v_mfma_f32_16x16x32_bf16 v[112:115], v[208:211], v[140:143], v[112:115]
ds_read_b64_tr_b16 v[208:209], v240 offset:8576
ds_read_b64_tr_b16 v[210:211], v240 offset:9600
v_mfma_f32_16x16x32_bf16 v[20:23], v[212:215], v[128:131], v[20:23]
s_cmp_eq_u32 s73, 1
s_cselect_b32 s96, 12, 4
s_cselect_b32 s79, 0, 0xff000000
v_mfma_f32_16x16x32_bf16 v[52:55], v[212:215], v[132:135], v[52:55]
buffer_load_dwordx4 v[188:191], v247, s[52:55], s91 offen offset:3072 sc1
v_mfma_f32_16x16x32_bf16 v[84:87], v[212:215], v[136:139], v[84:87]
s_add_u32 s91, s91, 4096
s_or_b32 s98, s95, s96
v_mfma_f32_16x16x32_bf16 v[116:119], v[212:215], v[140:143], v[116:119]
ds_read_b64_tr_b16 v[212:213], v241 offset:8576
ds_read_b64_tr_b16 v[214:215], v241 offset:9600
v_mfma_f32_16x16x32_bf16 v[24:27], v[216:219], v[128:131], v[24:27]
s_mov_b32 s99, 0
v_mfma_f32_16x16x32_bf16 v[56:59], v[216:219], v[132:135], v[56:59]
s_cmp_eq_u32 s74, 3
s_cselect_b32 s23, 3, 0
s_cselect_b32 s22, 0, s75
v_mfma_f32_16x16x32_bf16 v[88:91], v[216:219], v[136:139], v[88:91]
s_lshl_b32 s95, s76, 5
s_add_u32 s95, s95, s3
v_mfma_f32_16x16x32_bf16 v[120:123], v[216:219], v[140:143], v[120:123]
ds_read_b64_tr_b16 v[216:217], v242 offset:8576
ds_read_b64_tr_b16 v[218:219], v242 offset:9600
v_mfma_f32_16x16x32_bf16 v[28:31], v[220:223], v[128:131], v[28:31]
ds_write_b128 v238, v[248:251] offset:35840
v_mfma_f32_16x16x32_bf16 v[60:63], v[220:223], v[132:135], v[60:63]
buffer_store_dwordx4 v[248:251], v255, s[48:51], s89 offen offset:3072 sc1
v_mfma_f32_16x16x32_bf16 v[92:95], v[220:223], v[136:139], v[92:95]
s_waitcnt vmcnt(15) lgkmcnt(3)
s_lshl_b32 s95, s95, 5
v_mfma_f32_16x16x32_bf16 v[124:127], v[220:223], v[140:143], v[124:127]
ds_read_b64_tr_b16 v[220:221], v243 offset:8576
ds_read_b64_tr_b16 v[222:223], v243 offset:9600
v_mfma_f32_16x16x32_bf16 v[0:3], v[192:195], v[144:147], v[0:3]
v_accvgpr_read_b32 v224, a104
v_accvgpr_read_b32 v225, a105
v_mfma_f32_16x16x32_bf16 v[32:35], v[192:195], v[148:151], v[32:35]
buffer_load_dwordx4 v[128:131], v244, s[52:55], s91 offen offset:0 sc1
v_mfma_f32_16x16x32_bf16 v[64:67], v[192:195], v[152:155], v[64:67]
v_accvgpr_read_b32 v226, a106
v_accvgpr_read_b32 v227, a107
v_mfma_f32_16x16x32_bf16 v[96:99], v[192:195], v[156:159], v[96:99]
v_accvgpr_read_b32 v228, a108
v_accvgpr_read_b32 v229, a109
v_mfma_f32_16x16x32_bf16 v[4:7], v[196:199], v[144:147], v[4:7]
s_waitcnt lgkmcnt(0)
s_barrier
v_mfma_f32_16x16x32_bf16 v[36:39], v[196:199], v[148:151], v[36:39]
buffer_load_dwordx4 v[132:135], v245, s[52:55], s91 offen offset:0 sc1
v_mfma_f32_16x16x32_bf16 v[68:71], v[196:199], v[152:155], v[68:71]
ds_read_b64_tr_b16 v[192:193], v240 offset:16896
ds_read_b64_tr_b16 v[194:195], v240 offset:17920
v_mfma_f32_16x16x32_bf16 v[100:103], v[196:199], v[156:159], v[100:103]
ds_read_b64_tr_b16 v[196:197], v241 offset:16896
ds_read_b64_tr_b16 v[198:199], v241 offset:17920
v_mfma_f32_16x16x32_bf16 v[8:11], v[200:203], v[144:147], v[8:11]
v_accvgpr_read_b32 v230, a110
v_accvgpr_read_b32 v231, a111
v_mfma_f32_16x16x32_bf16 v[40:43], v[200:203], v[148:151], v[40:43]
buffer_load_dwordx4 v[136:139], v246, s[52:55], s91 offen offset:0 sc1
v_mfma_f32_16x16x32_bf16 v[72:75], v[200:203], v[152:155], v[72:75]
v_cvt_pk_bf16_f32 v248, v224, v225
v_cvt_pk_bf16_f32 v249, v226, v227
v_mfma_f32_16x16x32_bf16 v[104:107], v[200:203], v[156:159], v[104:107]
ds_read_b64_tr_b16 v[200:201], v242 offset:16896
ds_read_b64_tr_b16 v[202:203], v242 offset:17920
v_mfma_f32_16x16x32_bf16 v[12:15], v[204:207], v[144:147], v[12:15]
s_add_u32 m0, s83, 0
v_cvt_pk_bf16_f32 v250, v228, v229
v_mfma_f32_16x16x32_bf16 v[44:47], v[204:207], v[148:151], v[44:47]
buffer_load_dwordx4 v232, s[44:47], s80 offen lds
v_mfma_f32_16x16x32_bf16 v[76:79], v[204:207], v[152:155], v[76:79]
v_cvt_pk_bf16_f32 v251, v230, v231
s_mov_b32 s89, 36864
v_mfma_f32_16x16x32_bf16 v[108:111], v[204:207], v[156:159], v[108:111]
ds_read_b64_tr_b16 v[204:205], v243 offset:16896
ds_read_b64_tr_b16 v[206:207], v243 offset:17920
v_mfma_f32_16x16x32_bf16 v[16:19], v[208:211], v[144:147], v[16:19]
s_add_u32 s95, s95, s72
s_lshl_b32 s95, s95, 3
v_mfma_f32_16x16x32_bf16 v[48:51], v[208:211], v[148:151], v[48:51]
buffer_load_dwordx4 v[140:143], v247, s[52:55], s91 offen offset:0 sc1
v_mfma_f32_16x16x32_bf16 v[80:83], v[208:211], v[152:155], v[80:83]
s_add_u32 s6, s95, 1048576
v_and_b32 v239, 0x70, v255
v_mfma_f32_16x16x32_bf16 v[112:115], v[208:211], v[156:159], v[112:115]
ds_read_b64_tr_b16 v[208:209], v240 offset:17024
ds_read_b64_tr_b16 v[210:211], v240 offset:18048
v_mfma_f32_16x16x32_bf16 v[20:23], v[212:215], v[144:147], v[20:23]
s_add_u32 m0, s83, 1024
v_lshlrev_b32 v239, 6, v239
v_mfma_f32_16x16x32_bf16 v[52:55], v[212:215], v[148:151], v[52:55]
buffer_load_dwordx4 v233, s[44:47], s80 offen lds
v_mfma_f32_16x16x32_bf16 v[84:87], v[212:215], v[152:155], v[84:87]
v_accvgpr_write_b32 a231, v239
s_mul_i32 s95, s22, 0
v_mfma_f32_16x16x32_bf16 v[116:119], v[212:215], v[156:159], v[116:119]
ds_read_b64_tr_b16 v[212:213], v241 offset:17024
ds_read_b64_tr_b16 v[214:215], v241 offset:18048
v_mfma_f32_16x16x32_bf16 v[24:27], v[216:219], v[144:147], v[24:27]
s_sub_u32 s96, 2, s95
s_add_u32 s95, s95, 2
v_mfma_f32_16x16x32_bf16 v[56:59], v[216:219], v[148:151], v[56:59]
v_lshrrev_b32 v239, 7, v255
v_mul_u32_u24 v254, s21, v239
v_mfma_f32_16x16x32_bf16 v[88:91], v[216:219], v[152:155], v[88:91]
v_add_u32 v239, s96, v239
v_lshrrev_b32 v239, 2, v239
v_mfma_f32_16x16x32_bf16 v[120:123], v[216:219], v[156:159], v[120:123]
ds_read_b64_tr_b16 v[216:217], v242 offset:17024
ds_read_b64_tr_b16 v[218:219], v242 offset:18048
v_mfma_f32_16x16x32_bf16 v[28:31], v[220:223], v[144:147], v[28:31]
ds_write_b128 v238, v[248:251] offset:36864
v_mfma_f32_16x16x32_bf16 v[60:63], v[220:223], v[148:151], v[60:63]
buffer_store_dwordx4 v[248:251], v255, s[48:51], s89 offen offset:0 sc1
v_mfma_f32_16x16x32_bf16 v[92:95], v[220:223], v[152:155], v[92:95]
s_waitcnt vmcnt(16) lgkmcnt(3)
v_lshl_add_u32 v254, v239, 28, v254
v_mfma_f32_16x16x32_bf16 v[124:127], v[220:223], v[156:159], v[124:127]
ds_read_b64_tr_b16 v[220:221], v243 offset:17024
ds_read_b64_tr_b16 v[222:223], v243 offset:18048
v_mfma_f32_16x16x32_bf16 v[0:3], v[192:195], v[160:163], v[0:3]
v_accvgpr_read_b32 v224, a168
v_accvgpr_read_b32 v225, a169
v_mfma_f32_16x16x32_bf16 v[32:35], v[192:195], v[164:167], v[32:35]
buffer_load_dwordx4 v[144:147], v244, s[52:55], s91 offen offset:1024 sc1
v_mfma_f32_16x16x32_bf16 v[64:67], v[192:195], v[168:171], v[64:67]
v_accvgpr_read_b32 v226, a170
v_accvgpr_read_b32 v227, a171
v_mfma_f32_16x16x32_bf16 v[96:99], v[192:195], v[172:175], v[96:99]
ds_read_b64_tr_b16 v[192:193], v240 offset:25344
ds_read_b64_tr_b16 v[194:195], v240 offset:26368
v_mfma_f32_16x16x32_bf16 v[4:7], v[196:199], v[160:163], v[4:7]
v_accvgpr_read_b32 v228, a172
v_accvgpr_read_b32 v229, a173
v_mfma_f32_16x16x32_bf16 v[36:39], v[196:199], v[164:167], v[36:39]
buffer_load_dwordx4 v[148:151], v245, s[52:55], s91 offen offset:1024 sc1
v_mfma_f32_16x16x32_bf16 v[68:71], v[196:199], v[168:171], v[68:71]
s_waitcnt lgkmcnt(2)
v_accvgpr_read_b32 v230, a174
v_mfma_f32_16x16x32_bf16 v[100:103], v[196:199], v[172:175], v[100:103]
ds_read_b64_tr_b16 v[196:197], v241 offset:25344
ds_read_b64_tr_b16 v[198:199], v241 offset:26368
v_mfma_f32_16x16x32_bf16 v[8:11], v[200:203], v[160:163], v[8:11]
s_add_u32 m0, s83, 2176
v_accvgpr_read_b32 v231, a175
v_mfma_f32_16x16x32_bf16 v[40:43], v[200:203], v[164:167], v[40:43]
buffer_load_dwordx4 v234, s[44:47], s80 offen lds
v_mfma_f32_16x16x32_bf16 v[72:75], v[200:203], v[168:171], v[72:75]
v_cvt_pk_bf16_f32 v248, v224, v225
v_cvt_pk_bf16_f32 v249, v226, v227
v_mfma_f32_16x16x32_bf16 v[104:107], v[200:203], v[172:175], v[104:107]
ds_read_b64_tr_b16 v[200:201], v242 offset:25344
ds_read_b64_tr_b16 v[202:203], v242 offset:26368
v_mfma_f32_16x16x32_bf16 v[12:15], v[204:207], v[160:163], v[12:15]
v_cvt_pk_bf16_f32 v250, v228, v229
v_cvt_pk_bf16_f32 v251, v230, v231
v_mfma_f32_16x16x32_bf16 v[44:47], v[204:207], v[164:167], v[44:47]
buffer_load_dwordx4 v[152:155], v246, s[52:55], s91 offen offset:1024 sc1
v_mfma_f32_16x16x32_bf16 v[76:79], v[204:207], v[168:171], v[76:79]
v_and_b32 v239, 0x70, v255
v_lshl_add_u32 v254, v239, 3, v254
v_mfma_f32_16x16x32_bf16 v[108:111], v[204:207], v[172:175], v[108:111]
ds_read_b64_tr_b16 v[204:205], v243 offset:25344
ds_read_b64_tr_b16 v[206:207], v243 offset:26368
v_mfma_f32_16x16x32_bf16 v[16:19], v[208:211], v[160:163], v[16:19]
s_add_u32 m0, s83, 3200
s_mul_i32 s21, s21, s95
v_mfma_f32_16x16x32_bf16 v[48:51], v[208:211], v[164:167], v[48:51]
buffer_load_dwordx4 v235, s[44:47], s80 offen lds
v_mfma_f32_16x16x32_bf16 v[80:83], v[208:211], v[168:171], v[80:83]
s_add_u32 s80, s80, 524288
v_lshrrev_b32 v236, 8, v255
v_mfma_f32_16x16x32_bf16 v[112:115], v[208:211], v[172:175], v[112:115]
ds_read_b64_tr_b16 v[208:209], v240 offset:25472
ds_read_b64_tr_b16 v[210:211], v240 offset:26496
v_mfma_f32_16x16x32_bf16 v[20:23], v[212:215], v[160:163], v[20:23]
v_lshlrev_b32 v239, 5, v236
v_xor_b32 v239, v239, v255
v_mfma_f32_16x16x32_bf16 v[52:55], v[212:215], v[164:167], v[52:55]
buffer_load_dwordx4 v[156:159], v247, s[52:55], s91 offen offset:1024 sc1
v_mfma_f32_16x16x32_bf16 v[84:87], v[212:215], v[168:171], v[84:87]
v_and_b32 v239, 0xf0, v239
v_lshl_add_u32 v239, v236, 11, v239
v_mfma_f32_16x16x32_bf16 v[116:119], v[212:215], v[172:175], v[116:119]
ds_read_b64_tr_b16 v[212:213], v241 offset:25472
ds_read_b64_tr_b16 v[214:215], v241 offset:26496
v_mfma_f32_16x16x32_bf16 v[24:27], v[216:219], v[160:163], v[24:27]
s_lshl_b32 s92, s77, 16
s_lshl_b32 s93, s78, 14
v_mfma_f32_16x16x32_bf16 v[56:59], v[216:219], v[164:167], v[56:59]
s_add_u32 s92, s92, s93
v_add_u32 v239, s92, v239
v_mfma_f32_16x16x32_bf16 v[88:91], v[216:219], v[168:171], v[88:91]
v_accvgpr_write_b32 a228, v239
v_bfe_u32 v239, v255, 6, 2
v_mfma_f32_16x16x32_bf16 v[120:123], v[216:219], v[172:175], v[120:123]
ds_read_b64_tr_b16 v[216:217], v242 offset:25472
ds_read_b64_tr_b16 v[218:219], v242 offset:26496
v_mfma_f32_16x16x32_bf16 v[28:31], v[220:223], v[160:163], v[28:31]
ds_write_b128 v238, v[248:251] offset:37888
v_mfma_f32_16x16x32_bf16 v[60:63], v[220:223], v[164:167], v[60:63]
buffer_store_dwordx4 v[248:251], v255, s[48:51], s89 offen offset:1024 sc1
v_mfma_f32_16x16x32_bf16 v[92:95], v[220:223], v[168:171], v[92:95]
s_waitcnt vmcnt(15) lgkmcnt(3)
v_lshlrev_b32 v239, 7, v239
v_mfma_f32_16x16x32_bf16 v[124:127], v[220:223], v[172:175], v[124:127]
ds_read_b64_tr_b16 v[220:221], v243 offset:25472
ds_read_b64_tr_b16 v[222:223], v243 offset:26496
v_mfma_f32_16x16x32_bf16 v[0:3], v[192:195], v[176:179], v[0:3]
v_accvgpr_read_b32 v224, a112
v_accvgpr_read_b32 v225, a113
v_mfma_f32_16x16x32_bf16 v[32:35], v[192:195], v[180:183], v[32:35]
buffer_load_dwordx4 v[160:163], v244, s[52:55], s91 offen offset:2048 sc1
v_mfma_f32_16x16x32_bf16 v[64:67], v[192:195], v[184:187], v[64:67]
v_accvgpr_read_b32 v226, a114
v_accvgpr_read_b32 v227, a115
v_mfma_f32_16x16x32_bf16 v[96:99], v[192:195], v[188:191], v[96:99]
v_accvgpr_read_b32 v228, a116
v_accvgpr_read_b32 v229, a117
v_mfma_f32_16x16x32_bf16 v[4:7], v[196:199], v[176:179], v[4:7]
s_waitcnt lgkmcnt(0)
s_barrier
v_mfma_f32_16x16x32_bf16 v[36:39], v[196:199], v[180:183], v[36:39]
buffer_load_dwordx4 v[164:167], v245, s[52:55], s91 offen offset:2048 sc1
v_mfma_f32_16x16x32_bf16 v[68:71], v[196:199], v[184:187], v[68:71]
ds_read_b64_tr_b16 v[192:193], v240 offset:33792
ds_read_b64_tr_b16 v[194:195], v240 offset:34816
v_mfma_f32_16x16x32_bf16 v[100:103], v[196:199], v[188:191], v[100:103]
ds_read_b64_tr_b16 v[196:197], v241 offset:33792
ds_read_b64_tr_b16 v[198:199], v241 offset:34816
v_mfma_f32_16x16x32_bf16 v[8:11], v[200:203], v[176:179], v[8:11]
v_accvgpr_read_b32 v230, a118
v_accvgpr_read_b32 v231, a119
v_mfma_f32_16x16x32_bf16 v[40:43], v[200:203], v[180:183], v[40:43]
buffer_load_dwordx4 v[168:171], v246, s[52:55], s91 offen offset:2048 sc1
v_mfma_f32_16x16x32_bf16 v[72:75], v[200:203], v[184:187], v[72:75]
v_cvt_pk_bf16_f32 v248, v224, v225
v_cvt_pk_bf16_f32 v249, v226, v227
v_mfma_f32_16x16x32_bf16 v[104:107], v[200:203], v[188:191], v[104:107]
ds_read_b64_tr_b16 v[200:201], v242 offset:33792
ds_read_b64_tr_b16 v[202:203], v242 offset:34816
v_mfma_f32_16x16x32_bf16 v[12:15], v[204:207], v[176:179], v[12:15]
s_add_u32 m0, s83, 16896
v_cvt_pk_bf16_f32 v250, v228, v229
v_mfma_f32_16x16x32_bf16 v[44:47], v[204:207], v[180:183], v[44:47]
buffer_load_dwordx4 v232, s[44:47], s80 offen lds
v_mfma_f32_16x16x32_bf16 v[76:79], v[204:207], v[184:187], v[76:79]
v_cvt_pk_bf16_f32 v251, v230, v231
v_lshl_add_u32 v239, v236, 5, v239
v_mfma_f32_16x16x32_bf16 v[108:111], v[204:207], v[188:191], v[108:111]
ds_read_b64_tr_b16 v[204:205], v243 offset:33792
ds_read_b64_tr_b16 v[206:207], v243 offset:34816
v_mfma_f32_16x16x32_bf16 v[16:19], v[208:211], v[176:179], v[16:19]
s_lshl_b32 s92, s78, 9
v_add_u32 v239, s92, v239
v_mfma_f32_16x16x32_bf16 v[48:51], v[208:211], v[180:183], v[48:51]
buffer_load_dwordx4 v[172:175], v247, s[52:55], s91 offen offset:2048 sc1
v_mfma_f32_16x16x32_bf16 v[80:83], v[208:211], v[184:187], v[80:83]
v_mul_u32_u24 v239, 7168, v239
v_and_or_b32 v239, v255, 48, v239
v_mfma_f32_16x16x32_bf16 v[112:115], v[208:211], v[188:191], v[112:115]
ds_read_b64_tr_b16 v[208:209], v240 offset:33920
ds_read_b64_tr_b16 v[210:211], v240 offset:34944
v_mfma_f32_16x16x32_bf16 v[20:23], v[212:215], v[176:179], v[20:23]
s_add_u32 m0, s83, 17920
v_accvgpr_write_b32 a229, v239
v_mfma_f32_16x16x32_bf16 v[52:55], v[212:215], v[180:183], v[52:55]
buffer_load_dwordx4 v233, s[44:47], s80 offen lds
v_mfma_f32_16x16x32_bf16 v[84:87], v[212:215], v[184:187], v[84:87]
v_and_b32 v239, 0xf0, v255
v_lshl_add_u32 v239, v236, 14, v239
v_mfma_f32_16x16x32_bf16 v[116:119], v[212:215], v[188:191], v[116:119]
ds_read_b64_tr_b16 v[212:213], v241 offset:33920
ds_read_b64_tr_b16 v[214:215], v241 offset:34944
v_mfma_f32_16x16x32_bf16 v[24:27], v[216:219], v[176:179], v[24:27]
s_lshl_b32 s92, s3, 9
v_add_u32 v239, s92, v239
v_mfma_f32_16x16x32_bf16 v[56:59], v[216:219], v[180:183], v[56:59]
v_accvgpr_write_b32 a230, v239
v_add_u32 v239, 4294886912, v240
v_mfma_f32_16x16x32_bf16 v[88:91], v[216:219], v[184:187], v[88:91]
v_accvgpr_write_b32 a224, v239
v_bfe_u32 v239, v255, 4, 4
v_mfma_f32_16x16x32_bf16 v[120:123], v[216:219], v[188:191], v[120:123]
ds_read_b64_tr_b16 v[216:217], v242 offset:33920
ds_read_b64_tr_b16 v[218:219], v242 offset:34944
v_mfma_f32_16x16x32_bf16 v[28:31], v[220:223], v[176:179], v[28:31]
ds_write_b128 v238, v[248:251] offset:38912
v_mfma_f32_16x16x32_bf16 v[60:63], v[220:223], v[180:183], v[60:63]
buffer_store_dwordx4 v[248:251], v255, s[48:51], s89 offen offset:2048 sc1
v_mfma_f32_16x16x32_bf16 v[92:95], v[220:223], v[184:187], v[92:95]
s_waitcnt vmcnt(16) lgkmcnt(3)
v_lshlrev_b32 v236, 2, v236
v_mfma_f32_16x16x32_bf16 v[124:127], v[220:223], v[188:191], v[124:127]
ds_read_b64_tr_b16 v[220:221], v243 offset:33920
ds_read_b64_tr_b16 v[222:223], v243 offset:34944
v_mfma_f32_16x16x32_bf16 v[0:3], v[192:195], v[128:131], v[0:3]
v_accvgpr_read_b32 v224, a176
v_accvgpr_read_b32 v225, a177
v_mfma_f32_16x16x32_bf16 v[32:35], v[192:195], v[132:135], v[32:35]
buffer_load_dwordx4 v[176:179], v244, s[52:55], s91 offen offset:3072 sc1
v_mfma_f32_16x16x32_bf16 v[64:67], v[192:195], v[136:139], v[64:67]
v_accvgpr_read_b32 v226, a178
v_accvgpr_read_b32 v227, a179
v_mfma_f32_16x16x32_bf16 v[96:99], v[192:195], v[140:143], v[96:99]
ds_read_b64_tr_b16 v[192:193], v240 offset:42240
ds_read_b64_tr_b16 v[194:195], v240 offset:43264
v_mfma_f32_16x16x32_bf16 v[4:7], v[196:199], v[128:131], v[4:7]
v_accvgpr_read_b32 v228, a180
v_accvgpr_read_b32 v229, a181
v_mfma_f32_16x16x32_bf16 v[36:39], v[196:199], v[132:135], v[36:39]
buffer_load_dwordx4 v[180:183], v245, s[52:55], s91 offen offset:3072 sc1
v_mfma_f32_16x16x32_bf16 v[68:71], v[196:199], v[136:139], v[68:71]
s_waitcnt lgkmcnt(2)
v_accvgpr_read_b32 v230, a182
v_mfma_f32_16x16x32_bf16 v[100:103], v[196:199], v[140:143], v[100:103]
ds_read_b64_tr_b16 v[196:197], v241 offset:42240
ds_read_b64_tr_b16 v[198:199], v241 offset:43264
v_mfma_f32_16x16x32_bf16 v[8:11], v[200:203], v[128:131], v[8:11]
s_add_u32 m0, s83, 19072
v_accvgpr_read_b32 v231, a183
v_mfma_f32_16x16x32_bf16 v[40:43], v[200:203], v[132:135], v[40:43]
buffer_load_dwordx4 v234, s[44:47], s80 offen lds
v_mfma_f32_16x16x32_bf16 v[72:75], v[200:203], v[136:139], v[72:75]
v_cvt_pk_bf16_f32 v248, v224, v225
v_cvt_pk_bf16_f32 v249, v226, v227
v_mfma_f32_16x16x32_bf16 v[104:107], v[200:203], v[140:143], v[104:107]
ds_read_b64_tr_b16 v[200:201], v242 offset:42240
ds_read_b64_tr_b16 v[202:203], v242 offset:43264
v_mfma_f32_16x16x32_bf16 v[12:15], v[204:207], v[128:131], v[12:15]
v_cvt_pk_bf16_f32 v250, v228, v229
v_cvt_pk_bf16_f32 v251, v230, v231
v_mfma_f32_16x16x32_bf16 v[44:47], v[204:207], v[132:135], v[44:47]
buffer_load_dwordx4 v[184:187], v246, s[52:55], s91 offen offset:3072 sc1
v_mfma_f32_16x16x32_bf16 v[76:79], v[204:207], v[136:139], v[76:79]
v_sub_u32 v239, v239, v236
v_subrev_u32 v236, 0, v239
v_mfma_f32_16x16x32_bf16 v[108:111], v[204:207], v[140:143], v[108:111]
ds_read_b64_tr_b16 v[204:205], v243 offset:42240
ds_read_b64_tr_b16 v[206:207], v243 offset:43264
v_mfma_f32_16x16x32_bf16 v[16:19], v[208:211], v[128:131], v[16:19]
s_add_u32 m0, s83, 20096
v_ashrrev_i32 v236, 31, v236
v_mfma_f32_16x16x32_bf16 v[48:51], v[208:211], v[132:135], v[48:51]
buffer_load_dwordx4 v235, s[44:47], s80 offen lds
v_mfma_f32_16x16x32_bf16 v[80:83], v[208:211], v[136:139], v[80:83]
s_add_u32 s80, s80, 524288
v_and_b32 v236, 0xff800000, v236
v_mfma_f32_16x16x32_bf16 v[112:115], v[208:211], v[140:143], v[112:115]
ds_read_b64_tr_b16 v[208:209], v240 offset:42368
ds_read_b64_tr_b16 v[210:211], v240 offset:43392
v_mfma_f32_16x16x32_bf16 v[20:23], v[212:215], v[128:131], v[20:23]
v_accvgpr_write_b32 a236, v236
v_subrev_u32 v236, 1, v239
v_mfma_f32_16x16x32_bf16 v[52:55], v[212:215], v[132:135], v[52:55]
buffer_load_dwordx4 v[188:191], v247, s[52:55], s91 offen offset:3072 sc1
v_mfma_f32_16x16x32_bf16 v[84:87], v[212:215], v[136:139], v[84:87]
s_add_u32 s91, s91, 4096
v_ashrrev_i32 v236, 31, v236
v_mfma_f32_16x16x32_bf16 v[116:119], v[212:215], v[140:143], v[116:119]
ds_read_b64_tr_b16 v[212:213], v241 offset:42368
ds_read_b64_tr_b16 v[214:215], v241 offset:43392
v_mfma_f32_16x16x32_bf16 v[24:27], v[216:219], v[128:131], v[24:27]
.La5_LW1_8:
s_cbranch_execz .La5_LW1_R0
v_and_b32 v236, 0xff800000, v236
v_accvgpr_write_b32 a237, v236
v_mfma_f32_16x16x32_bf16 v[56:59], v[216:219], v[132:135], v[56:59]
v_subrev_u32 v236, 2, v239
v_ashrrev_i32 v236, 31, v236
v_mfma_f32_16x16x32_bf16 v[88:91], v[216:219], v[136:139], v[88:91]
v_and_b32 v236, 0xff800000, v236
v_accvgpr_write_b32 a238, v236
v_mfma_f32_16x16x32_bf16 v[120:123], v[216:219], v[140:143], v[120:123]
ds_read_b64_tr_b16 v[216:217], v242 offset:42368
ds_read_b64_tr_b16 v[218:219], v242 offset:43392
v_mfma_f32_16x16x32_bf16 v[28:31], v[220:223], v[128:131], v[28:31]
ds_write_b128 v238, v[248:251] offset:39936
v_mfma_f32_16x16x32_bf16 v[60:63], v[220:223], v[132:135], v[60:63]
buffer_store_dwordx4 v[248:251], v255, s[48:51], s89 offen offset:3072 sc1
v_mfma_f32_16x16x32_bf16 v[92:95], v[220:223], v[136:139], v[92:95]
s_waitcnt vmcnt(15) lgkmcnt(3)
v_subrev_u32 v236, 3, v239
v_mfma_f32_16x16x32_bf16 v[124:127], v[220:223], v[140:143], v[124:127]
ds_read_b64_tr_b16 v[220:221], v243 offset:42368
ds_read_b64_tr_b16 v[222:223], v243 offset:43392
v_mfma_f32_16x16x32_bf16 v[0:3], v[192:195], v[144:147], v[0:3]
v_accvgpr_read_b32 v224, a120
v_accvgpr_read_b32 v225, a121
v_mfma_f32_16x16x32_bf16 v[32:35], v[192:195], v[148:151], v[32:35]
buffer_load_dwordx4 v[128:131], v244, s[52:55], s91 offen offset:0 sc1
v_mfma_f32_16x16x32_bf16 v[64:67], v[192:195], v[152:155], v[64:67]
v_accvgpr_read_b32 v226, a122
v_accvgpr_read_b32 v227, a123
v_mfma_f32_16x16x32_bf16 v[96:99], v[192:195], v[156:159], v[96:99]
v_accvgpr_read_b32 v228, a124
v_accvgpr_read_b32 v229, a125
v_mfma_f32_16x16x32_bf16 v[4:7], v[196:199], v[144:147], v[4:7]
s_waitcnt lgkmcnt(0)
s_barrier
v_mfma_f32_16x16x32_bf16 v[36:39], v[196:199], v[148:151], v[36:39]
buffer_load_dwordx4 v[132:135], v245, s[52:55], s91 offen offset:0 sc1
v_mfma_f32_16x16x32_bf16 v[68:71], v[196:199], v[152:155], v[68:71]
ds_read_b64_tr_b16 v[192:193], v240 offset:50688
ds_read_b64_tr_b16 v[194:195], v240 offset:51712
v_mfma_f32_16x16x32_bf16 v[100:103], v[196:199], v[156:159], v[100:103]
ds_read_b64_tr_b16 v[196:197], v241 offset:50688
ds_read_b64_tr_b16 v[198:199], v241 offset:51712
v_mfma_f32_16x16x32_bf16 v[8:11], v[200:203], v[144:147], v[8:11]
v_accvgpr_read_b32 v230, a126
v_accvgpr_read_b32 v231, a127
v_mfma_f32_16x16x32_bf16 v[40:43], v[200:203], v[148:151], v[40:43]
buffer_load_dwordx4 v[136:139], v246, s[52:55], s91 offen offset:0 sc1
v_mfma_f32_16x16x32_bf16 v[72:75], v[200:203], v[152:155], v[72:75]
v_cvt_pk_bf16_f32 v248, v224, v225
v_cvt_pk_bf16_f32 v249, v226, v227
v_mfma_f32_16x16x32_bf16 v[104:107], v[200:203], v[156:159], v[104:107]
ds_read_b64_tr_b16 v[200:201], v242 offset:50688
ds_read_b64_tr_b16 v[202:203], v242 offset:51712
v_mfma_f32_16x16x32_bf16 v[12:15], v[204:207], v[144:147], v[12:15]
s_add_u32 m0, s83, 33792
v_cvt_pk_bf16_f32 v250, v228, v229
v_mfma_f32_16x16x32_bf16 v[44:47], v[204:207], v[148:151], v[44:47]
buffer_load_dwordx4 v232, s[44:47], s80 offen lds
v_mfma_f32_16x16x32_bf16 v[76:79], v[204:207], v[152:155], v[76:79]
v_cvt_pk_bf16_f32 v251, v230, v231
s_mov_b32 s89, 16384
v_mfma_f32_16x16x32_bf16 v[108:111], v[204:207], v[156:159], v[108:111]
ds_read_b64_tr_b16 v[204:205], v243 offset:50688
ds_read_b64_tr_b16 v[206:207], v243 offset:51712
v_mfma_f32_16x16x32_bf16 v[16:19], v[208:211], v[144:147], v[16:19]
s_add_u32 s85, s85, 65536
v_add_u32 v238, s85, v255
v_mfma_f32_16x16x32_bf16 v[48:51], v[208:211], v[148:151], v[48:51]
buffer_load_dwordx4 v[140:143], v247, s[52:55], s91 offen offset:0 sc1
v_mfma_f32_16x16x32_bf16 v[80:83], v[208:211], v[152:155], v[80:83]
s_mov_b32 s85, 0
v_ashrrev_i32 v236, 31, v236
v_mfma_f32_16x16x32_bf16 v[112:115], v[208:211], v[156:159], v[112:115]
ds_read_b64_tr_b16 v[208:209], v240 offset:50816
ds_read_b64_tr_b16 v[210:211], v240 offset:51840
v_mfma_f32_16x16x32_bf16 v[20:23], v[212:215], v[144:147], v[20:23]
s_add_u32 m0, s83, 34816
v_and_b32 v236, 0xff800000, v236
v_mfma_f32_16x16x32_bf16 v[52:55], v[212:215], v[148:151], v[52:55]
buffer_load_dwordx4 v233, s[44:47], s80 offen lds
v_mfma_f32_16x16x32_bf16 v[84:87], v[212:215], v[152:155], v[84:87]
v_accvgpr_write_b32 a239, v236
v_lshlrev_b32 v236, 4, v239
v_mfma_f32_16x16x32_bf16 v[116:119], v[212:215], v[156:159], v[116:119]
ds_read_b64_tr_b16 v[212:213], v241 offset:50816
ds_read_b64_tr_b16 v[214:215], v241 offset:51840
v_mfma_f32_16x16x32_bf16 v[24:27], v[216:219], v[144:147], v[24:27]
s_mov_b32 s92, 0x3f80
v_lshlrev_b32 v236, v236, s92
v_mfma_f32_16x16x32_bf16 v[56:59], v[216:219], v[148:151], v[56:59]
v_cmp_gt_u32 vcc, 2, v239
v_subrev_u32 v239, 2, v239
v_mfma_f32_16x16x32_bf16 v[88:91], v[216:219], v[152:155], v[88:91]
v_cmp_gt_u32 s[92:93], 2, v239
v_cndmask_b32 v239, 0, v236, vcc
v_mfma_f32_16x16x32_bf16 v[120:123], v[216:219], v[156:159], v[120:123]
ds_read_b64_tr_b16 v[216:217], v242 offset:50816
ds_read_b64_tr_b16 v[218:219], v242 offset:51840
v_mfma_f32_16x16x32_bf16 v[28:31], v[220:223], v[144:147], v[28:31]
ds_write_b128 v238, v[248:251] offset:0
v_mfma_f32_16x16x32_bf16 v[60:63], v[220:223], v[148:151], v[60:63]
buffer_store_dwordx4 v[248:251], v255, s[48:51], s89 offen offset:0 sc1
v_mfma_f32_16x16x32_bf16 v[92:95], v[220:223], v[152:155], v[92:95]
s_waitcnt vmcnt(16) lgkmcnt(3)
v_accvgpr_write_b32 a212, 0
v_mfma_f32_16x16x32_bf16 v[124:127], v[220:223], v[156:159], v[124:127]
ds_read_b64_tr_b16 v[220:221], v243 offset:50816
ds_read_b64_tr_b16 v[222:223], v243 offset:51840
v_mfma_f32_16x16x32_bf16 v[0:3], v[192:195], v[160:163], v[0:3]
v_accvgpr_read_b32 v224, a184
v_accvgpr_read_b32 v225, a185
v_mfma_f32_16x16x32_bf16 v[32:35], v[192:195], v[164:167], v[32:35]
buffer_load_dwordx4 v[144:147], v244, s[52:55], s91 offen offset:1024 sc1
v_mfma_f32_16x16x32_bf16 v[64:67], v[192:195], v[168:171], v[64:67]
v_accvgpr_read_b32 v226, a186
v_accvgpr_read_b32 v227, a187
v_mfma_f32_16x16x32_bf16 v[96:99], v[192:195], v[172:175], v[96:99]
ds_read_b64_tr_b16 v[192:193], v240 offset:59136
ds_read_b64_tr_b16 v[194:195], v240 offset:60160
v_mfma_f32_16x16x32_bf16 v[4:7], v[196:199], v[160:163], v[4:7]
v_accvgpr_read_b32 v228, a188
v_accvgpr_read_b32 v229, a189
v_mfma_f32_16x16x32_bf16 v[36:39], v[196:199], v[164:167], v[36:39]
buffer_load_dwordx4 v[148:151], v245, s[52:55], s91 offen offset:1024 sc1
v_mfma_f32_16x16x32_bf16 v[68:71], v[196:199], v[168:171], v[68:71]
s_waitcnt lgkmcnt(2)
v_accvgpr_read_b32 v230, a190
v_mfma_f32_16x16x32_bf16 v[100:103], v[196:199], v[172:175], v[100:103]
ds_read_b64_tr_b16 v[196:197], v241 offset:59136
ds_read_b64_tr_b16 v[198:199], v241 offset:60160
v_mfma_f32_16x16x32_bf16 v[8:11], v[200:203], v[160:163], v[8:11]
s_add_u32 m0, s83, 35968
v_accvgpr_read_b32 v231, a191
v_mfma_f32_16x16x32_bf16 v[40:43], v[200:203], v[164:167], v[40:43]
buffer_load_dwordx4 v234, s[44:47], s80 offen lds
v_mfma_f32_16x16x32_bf16 v[72:75], v[200:203], v[168:171], v[72:75]
v_cvt_pk_bf16_f32 v248, v224, v225
v_cvt_pk_bf16_f32 v249, v226, v227
v_mfma_f32_16x16x32_bf16 v[104:107], v[200:203], v[172:175], v[104:107]
ds_read_b64_tr_b16 v[200:201], v242 offset:59136
ds_read_b64_tr_b16 v[202:203], v242 offset:60160
v_mfma_f32_16x16x32_bf16 v[12:15], v[204:207], v[160:163], v[12:15]
v_cvt_pk_bf16_f32 v250, v228, v229
v_cvt_pk_bf16_f32 v251, v230, v231
v_mfma_f32_16x16x32_bf16 v[44:47], v[204:207], v[164:167], v[44:47]
buffer_load_dwordx4 v[152:155], v246, s[52:55], s91 offen offset:1024 sc1
v_mfma_f32_16x16x32_bf16 v[76:79], v[204:207], v[168:171], v[76:79]
v_accvgpr_write_b32 a214, v239
v_cndmask_b32 v239, 0, v236, s[92:93]
v_mfma_f32_16x16x32_bf16 v[108:111], v[204:207], v[172:175], v[108:111]
ds_read_b64_tr_b16 v[204:205], v243 offset:59136
ds_read_b64_tr_b16 v[206:207], v243 offset:60160
v_mfma_f32_16x16x32_bf16 v[16:19], v[208:211], v[160:163], v[16:19]
s_add_u32 m0, s83, 36992
v_mov_b32 v236, 0x3f803f80
v_mfma_f32_16x16x32_bf16 v[48:51], v[208:211], v[164:167], v[48:51]
buffer_load_dwordx4 v235, s[44:47], s80 offen lds
v_mfma_f32_16x16x32_bf16 v[80:83], v[208:211], v[168:171], v[80:83]
s_add_u32 s80, s80, 524288
v_accvgpr_write_b32 a215, v239
v_mfma_f32_16x16x32_bf16 v[112:115], v[208:211], v[172:175], v[112:115]
ds_read_b64_tr_b16 v[208:209], v240 offset:59264
ds_read_b64_tr_b16 v[210:211], v240 offset:60288
v_mfma_f32_16x16x32_bf16 v[20:23], v[212:215], v[160:163], v[20:23]
v_accvgpr_write_b32 a213, 0
v_accvgpr_write_b32 a216, 0
v_mfma_f32_16x16x32_bf16 v[52:55], v[212:215], v[164:167], v[52:55]
buffer_load_dwordx4 v[156:159], v247, s[52:55], s91 offen offset:1024 sc1
v_mfma_f32_16x16x32_bf16 v[84:87], v[212:215], v[168:171], v[84:87]
v_accvgpr_write_b32 a217, 0
v_lshrrev_b32 v239, 4, v255
v_mfma_f32_16x16x32_bf16 v[116:119], v[212:215], v[172:175], v[116:119]
ds_read_b64_tr_b16 v[212:213], v241 offset:59264
ds_read_b64_tr_b16 v[214:215], v241 offset:60288
v_mfma_f32_16x16x32_bf16 v[24:27], v[216:219], v[160:163], v[24:27]
v_xor_b32 v239, v239, v255
v_and_b32 v239, 32, v239
v_mfma_f32_16x16x32_bf16 v[56:59], v[216:219], v[164:167], v[56:59]
v_cmp_eq_u32 vcc, 0, v239
s_mov_b32 s78, 4294950911
v_mfma_f32_16x16x32_bf16 v[88:91], v[216:219], v[168:171], v[88:91]
s_mul_i32 s70, s70, s22
v_cndmask_b32 v236, 0, v236, vcc
v_mfma_f32_16x16x32_bf16 v[120:123], v[216:219], v[172:175], v[120:123]
ds_read_b64_tr_b16 v[216:217], v242 offset:59264
ds_read_b64_tr_b16 v[218:219], v242 offset:60288
v_mfma_f32_16x16x32_bf16 v[28:31], v[220:223], v[160:163], v[28:31]
ds_write_b128 v238, v[248:251] offset:1024
v_mfma_f32_16x16x32_bf16 v[60:63], v[220:223], v[164:167], v[60:63]
buffer_store_dwordx4 v[248:251], v255, s[48:51], s89 offen offset:1024 sc1
v_mfma_f32_16x16x32_bf16 v[92:95], v[220:223], v[168:171], v[92:95]
s_waitcnt vmcnt(15) lgkmcnt(3)
s_sub_u32 s92, 4294967295, s3
v_mfma_f32_16x16x32_bf16 v[124:127], v[220:223], v[172:175], v[124:127]
ds_read_b64_tr_b16 v[220:221], v243 offset:59264
ds_read_b64_tr_b16 v[222:223], v243 offset:60288
v_mfma_f32_16x16x32_bf16 v[0:3], v[192:195], v[176:179], v[0:3]
v_accvgpr_read_b32 v224, a64
v_accvgpr_read_b32 v225, a65
v_mfma_f32_16x16x32_bf16 v[32:35], v[192:195], v[180:183], v[32:35]
buffer_load_dwordx4 v[160:163], v244, s[52:55], s91 offen offset:2048 sc1
v_mfma_f32_16x16x32_bf16 v[64:67], v[192:195], v[184:187], v[64:67]
v_accvgpr_read_b32 v226, a66
v_accvgpr_read_b32 v227, a67
v_mfma_f32_16x16x32_bf16 v[96:99], v[192:195], v[188:191], v[96:99]
v_accvgpr_read_b32 v228, a68
v_accvgpr_read_b32 v229, a69
v_mfma_f32_16x16x32_bf16 v[4:7], v[196:199], v[176:179], v[4:7]
s_waitcnt lgkmcnt(0)
s_barrier
v_mfma_f32_16x16x32_bf16 v[36:39], v[196:199], v[180:183], v[36:39]
buffer_load_dwordx4 v[164:167], v245, s[52:55], s91 offen offset:2048 sc1
v_mfma_f32_16x16x32_bf16 v[68:71], v[196:199], v[184:187], v[68:71]
ds_read_b64_tr_b16 v[192:193], v240 offset:0
ds_read_b64_tr_b16 v[194:195], v240 offset:1024
v_mfma_f32_16x16x32_bf16 v[100:103], v[196:199], v[188:191], v[100:103]
ds_read_b64_tr_b16 v[196:197], v241 offset:0
ds_read_b64_tr_b16 v[198:199], v241 offset:1024
v_mfma_f32_16x16x32_bf16 v[8:11], v[200:203], v[176:179], v[8:11]
v_accvgpr_read_b32 v230, a70
v_accvgpr_read_b32 v231, a71
v_mfma_f32_16x16x32_bf16 v[40:43], v[200:203], v[180:183], v[40:43]
buffer_load_dwordx4 v[168:171], v246, s[52:55], s91 offen offset:2048 sc1
v_mfma_f32_16x16x32_bf16 v[72:75], v[200:203], v[184:187], v[72:75]
v_cvt_pk_bf16_f32 v248, v224, v225
v_cvt_pk_bf16_f32 v249, v226, v227
v_mfma_f32_16x16x32_bf16 v[104:107], v[200:203], v[188:191], v[104:107]
ds_read_b64_tr_b16 v[200:201], v242 offset:0
ds_read_b64_tr_b16 v[202:203], v242 offset:1024
v_mfma_f32_16x16x32_bf16 v[12:15], v[204:207], v[176:179], v[12:15]
s_add_u32 m0, s83, 50688
v_cvt_pk_bf16_f32 v250, v228, v229
v_mfma_f32_16x16x32_bf16 v[44:47], v[204:207], v[180:183], v[44:47]
buffer_load_dwordx4 v232, s[44:47], s80 offen lds
v_mfma_f32_16x16x32_bf16 v[76:79], v[204:207], v[184:187], v[76:79]
v_cvt_pk_bf16_f32 v251, v230, v231
s_mul_i32 s90, s92, s21
v_mfma_f32_16x16x32_bf16 v[108:111], v[204:207], v[188:191], v[108:111]
ds_read_b64_tr_b16 v[204:205], v243 offset:0
ds_read_b64_tr_b16 v[206:207], v243 offset:1024
v_mfma_f32_16x16x32_bf16 v[16:19], v[208:211], v[176:179], v[16:19]
s_mov_b32 s17, 1073741824
s_lshl_b32 s92, s73, 2
v_mfma_f32_16x16x32_bf16 v[48:51], v[208:211], v[180:183], v[48:51]
buffer_load_dwordx4 v[172:175], v247, s[52:55], s91 offen offset:2048 sc1
v_mfma_f32_16x16x32_bf16 v[80:83], v[208:211], v[184:187], v[80:83]
s_lshr_b32 s92, 66051, s92
s_and_b32 s92, s92, 3
v_mfma_f32_16x16x32_bf16 v[112:115], v[208:211], v[188:191], v[112:115]
ds_read_b64_tr_b16 v[208:209], v240 offset:128
ds_read_b64_tr_b16 v[210:211], v240 offset:1152
v_mfma_f32_16x16x32_bf16 v[20:23], v[212:215], v[176:179], v[20:23]
s_add_u32 m0, s83, 51712
s_lshl_b32 s16, s92, 16
v_mfma_f32_16x16x32_bf16 v[52:55], v[212:215], v[180:183], v[52:55]
buffer_load_dwordx4 v233, s[44:47], s80 offen lds
v_mfma_f32_16x16x32_bf16 v[84:87], v[212:215], v[184:187], v[84:87]
s_mul_i32 s92, s75, 4294836224
s_add_u32 s92, s92, s88
v_mfma_f32_16x16x32_bf16 v[116:119], v[212:215], v[188:191], v[116:119]
ds_read_b64_tr_b16 v[212:213], v241 offset:128
ds_read_b64_tr_b16 v[214:215], v241 offset:1152
v_mfma_f32_16x16x32_bf16 v[24:27], v[216:219], v[176:179], v[24:27]
s_add_u32 s19, s92, 393216
v_accvgpr_write_b32 a208, v236
v_mfma_f32_16x16x32_bf16 v[56:59], v[216:219], v[180:183], v[56:59]
v_accvgpr_write_b32 a209, v236
v_accvgpr_write_b32 a210, v236
v_mfma_f32_16x16x32_bf16 v[88:91], v[216:219], v[184:187], v[88:91]
v_accvgpr_write_b32 a211, v236
v_accvgpr_write_b32 a232, 0
v_mfma_f32_16x16x32_bf16 v[120:123], v[216:219], v[188:191], v[120:123]
ds_read_b64_tr_b16 v[216:217], v242 offset:128
ds_read_b64_tr_b16 v[218:219], v242 offset:1152
v_mfma_f32_16x16x32_bf16 v[28:31], v[220:223], v[176:179], v[28:31]
ds_write_b128 v238, v[248:251] offset:2048
v_mfma_f32_16x16x32_bf16 v[60:63], v[220:223], v[180:183], v[60:63]
buffer_store_dwordx4 v[248:251], v255, s[48:51], s89 offen offset:2048 sc1
v_mfma_f32_16x16x32_bf16 v[92:95], v[220:223], v[184:187], v[92:95]
s_waitcnt vmcnt(16) lgkmcnt(3)
v_accvgpr_write_b32 a233, 0
v_mfma_f32_16x16x32_bf16 v[124:127], v[220:223], v[188:191], v[124:127]
ds_read_b64_tr_b16 v[220:221], v243 offset:128
ds_read_b64_tr_b16 v[222:223], v243 offset:1152
v_mfma_f32_16x16x32_bf16 v[0:3], v[192:195], v[128:131], v[0:3]
v_accvgpr_read_b32 v224, a0
v_accvgpr_read_b32 v225, a1
v_mfma_f32_16x16x32_bf16 v[32:35], v[192:195], v[132:135], v[32:35]
buffer_load_dwordx4 v[176:179], v244, s[52:55], s91 offen offset:3072 sc1
v_mfma_f32_16x16x32_bf16 v[64:67], v[192:195], v[136:139], v[64:67]
v_accvgpr_read_b32 v226, a2
v_accvgpr_read_b32 v227, a3
v_mfma_f32_16x16x32_bf16 v[96:99], v[192:195], v[140:143], v[96:99]
ds_read_b64_tr_b16 v[192:193], v240 offset:8448
ds_read_b64_tr_b16 v[194:195], v240 offset:9472
v_mfma_f32_16x16x32_bf16 v[4:7], v[196:199], v[128:131], v[4:7]
v_accvgpr_read_b32 v228, a4
v_accvgpr_read_b32 v229, a5
v_mfma_f32_16x16x32_bf16 v[36:39], v[196:199], v[132:135], v[36:39]
buffer_load_dwordx4 v[180:183], v245, s[52:55], s91 offen offset:3072 sc1
v_mfma_f32_16x16x32_bf16 v[68:71], v[196:199], v[136:139], v[68:71]
s_waitcnt lgkmcnt(2)
v_accvgpr_read_b32 v230, a6
v_mfma_f32_16x16x32_bf16 v[100:103], v[196:199], v[140:143], v[100:103]
ds_read_b64_tr_b16 v[196:197], v241 offset:8448
ds_read_b64_tr_b16 v[198:199], v241 offset:9472
v_mfma_f32_16x16x32_bf16 v[8:11], v[200:203], v[128:131], v[8:11]
s_add_u32 m0, s83, 52864
v_accvgpr_read_b32 v231, a7
v_mfma_f32_16x16x32_bf16 v[40:43], v[200:203], v[132:135], v[40:43]
buffer_load_dwordx4 v234, s[44:47], s80 offen lds
v_mfma_f32_16x16x32_bf16 v[72:75], v[200:203], v[136:139], v[72:75]
v_cvt_pk_bf16_f32 v248, v224, v225
v_cvt_pk_bf16_f32 v249, v226, v227
v_mfma_f32_16x16x32_bf16 v[104:107], v[200:203], v[140:143], v[104:107]
ds_read_b64_tr_b16 v[200:201], v242 offset:8448
ds_read_b64_tr_b16 v[202:203], v242 offset:9472
v_mfma_f32_16x16x32_bf16 v[12:15], v[204:207], v[128:131], v[12:15]
v_cvt_pk_bf16_f32 v250, v228, v229
v_cvt_pk_bf16_f32 v251, v230, v231
v_mfma_f32_16x16x32_bf16 v[44:47], v[204:207], v[132:135], v[44:47]
buffer_load_dwordx4 v[184:187], v246, s[52:55], s91 offen offset:3072 sc1
v_mfma_f32_16x16x32_bf16 v[76:79], v[204:207], v[136:139], v[76:79]
v_accvgpr_write_b32 a234, 0
v_accvgpr_write_b32 a235, 0
v_mfma_f32_16x16x32_bf16 v[108:111], v[204:207], v[140:143], v[108:111]
ds_read_b64_tr_b16 v[204:205], v243 offset:8448
ds_read_b64_tr_b16 v[206:207], v243 offset:9472
v_mfma_f32_16x16x32_bf16 v[16:19], v[208:211], v[128:131], v[16:19]
s_add_u32 m0, s83, 53888
s_lshl_b32 s20, s72, 10
v_mfma_f32_16x16x32_bf16 v[48:51], v[208:211], v[132:135], v[48:51]
buffer_load_dwordx4 v235, s[44:47], s80 offen lds
v_mfma_f32_16x16x32_bf16 v[80:83], v[208:211], v[136:139], v[80:83]
s_add_u32 s80, s80, 524288
s_add_u32 s20, s20, 16384
v_mfma_f32_16x16x32_bf16 v[112:115], v[208:211], v[140:143], v[112:115]
ds_read_b64_tr_b16 v[208:209], v240 offset:8576
ds_read_b64_tr_b16 v[210:211], v240 offset:9600
v_mfma_f32_16x16x32_bf16 v[20:23], v[212:215], v[128:131], v[20:23]
s_mov_b32 s4, 1985221408
v_mfma_f32_16x16x32_bf16 v[52:55], v[212:215], v[132:135], v[52:55]
buffer_load_dwordx4 v[188:191], v247, s[52:55], s91 offen offset:3072 sc1
v_mfma_f32_16x16x32_bf16 v[84:87], v[212:215], v[136:139], v[84:87]
s_add_u32 s91, s91, 4096
v_mfma_f32_16x16x32_bf16 v[116:119], v[212:215], v[140:143], v[116:119]
ds_read_b64_tr_b16 v[212:213], v241 offset:8576
ds_read_b64_tr_b16 v[214:215], v241 offset:9600
v_mfma_f32_16x16x32_bf16 v[24:27], v[216:219], v[128:131], v[24:27]
s_cmp_ge_u32 s2, 64
s_cselect_b32 s4, 1985225488, s4
v_mfma_f32_16x16x32_bf16 v[56:59], v[216:219], v[132:135], v[56:59]
s_cmp_ge_u32 s2, 128
s_cselect_b32 s4, 1985229328, s4
v_mfma_f32_16x16x32_bf16 v[88:91], v[216:219], v[136:139], v[88:91]
s_cmp_ge_u32 s2, 192
s_cselect_b32 s4, 1985225008, s4
v_mfma_f32_16x16x32_bf16 v[120:123], v[216:219], v[140:143], v[120:123]
ds_read_b64_tr_b16 v[216:217], v242 offset:8576
ds_read_b64_tr_b16 v[218:219], v242 offset:9600
v_mfma_f32_16x16x32_bf16 v[28:31], v[220:223], v[128:131], v[28:31]
ds_write_b128 v238, v[248:251] offset:3072
v_mfma_f32_16x16x32_bf16 v[60:63], v[220:223], v[132:135], v[60:63]
buffer_store_dwordx4 v[248:251], v255, s[48:51], s89 offen offset:3072 sc1
v_mfma_f32_16x16x32_bf16 v[92:95], v[220:223], v[136:139], v[92:95]
s_waitcnt vmcnt(15) lgkmcnt(3)
v_bfe_u32 v236, v255, 4, 3
v_mfma_f32_16x16x32_bf16 v[124:127], v[220:223], v[140:143], v[124:127]
ds_read_b64_tr_b16 v[220:221], v243 offset:8576
ds_read_b64_tr_b16 v[222:223], v243 offset:9600
v_mfma_f32_16x16x32_bf16 v[0:3], v[192:195], v[144:147], v[0:3]
v_accvgpr_read_b32 v224, a72
v_accvgpr_read_b32 v225, a73
v_mfma_f32_16x16x32_bf16 v[32:35], v[192:195], v[148:151], v[32:35]
buffer_load_dwordx4 v[128:131], v244, s[52:55], s91 offen offset:0 sc1
v_mfma_f32_16x16x32_bf16 v[64:67], v[192:195], v[152:155], v[64:67]
v_accvgpr_read_b32 v226, a74
v_accvgpr_read_b32 v227, a75
v_mfma_f32_16x16x32_bf16 v[96:99], v[192:195], v[156:159], v[96:99]
v_accvgpr_read_b32 v228, a76
v_accvgpr_read_b32 v229, a77
v_mfma_f32_16x16x32_bf16 v[4:7], v[196:199], v[144:147], v[4:7]
s_waitcnt lgkmcnt(0)
s_barrier
v_mfma_f32_16x16x32_bf16 v[36:39], v[196:199], v[148:151], v[36:39]
buffer_load_dwordx4 v[132:135], v245, s[52:55], s91 offen offset:0 sc1
v_mfma_f32_16x16x32_bf16 v[68:71], v[196:199], v[152:155], v[68:71]
ds_read_b64_tr_b16 v[192:193], v240 offset:16896
ds_read_b64_tr_b16 v[194:195], v240 offset:17920
v_mfma_f32_16x16x32_bf16 v[100:103], v[196:199], v[156:159], v[100:103]
ds_read_b64_tr_b16 v[196:197], v241 offset:16896
ds_read_b64_tr_b16 v[198:199], v241 offset:17920
v_mfma_f32_16x16x32_bf16 v[8:11], v[200:203], v[144:147], v[8:11]
v_accvgpr_read_b32 v230, a78
v_accvgpr_read_b32 v231, a79
v_mfma_f32_16x16x32_bf16 v[40:43], v[200:203], v[148:151], v[40:43]
buffer_load_dwordx4 v[136:139], v246, s[52:55], s91 offen offset:0 sc1
v_mfma_f32_16x16x32_bf16 v[72:75], v[200:203], v[152:155], v[72:75]
v_cvt_pk_bf16_f32 v248, v224, v225
v_cvt_pk_bf16_f32 v249, v226, v227
v_mfma_f32_16x16x32_bf16 v[104:107], v[200:203], v[156:159], v[104:107]
ds_read_b64_tr_b16 v[200:201], v242 offset:16896
ds_read_b64_tr_b16 v[202:203], v242 offset:17920
v_mfma_f32_16x16x32_bf16 v[12:15], v[204:207], v[144:147], v[12:15]
s_add_u32 m0, s83, 0
v_cvt_pk_bf16_f32 v250, v228, v229
v_mfma_f32_16x16x32_bf16 v[44:47], v[204:207], v[148:151], v[44:47]
buffer_load_dwordx4 v232, s[44:47], s80 offen lds
v_mfma_f32_16x16x32_bf16 v[76:79], v[204:207], v[152:155], v[76:79]
v_cvt_pk_bf16_f32 v251, v230, v231
s_mov_b32 s89, 20480
v_mfma_f32_16x16x32_bf16 v[108:111], v[204:207], v[156:159], v[108:111]
ds_read_b64_tr_b16 v[204:205], v243 offset:16896
ds_read_b64_tr_b16 v[206:207], v243 offset:17920
v_mfma_f32_16x16x32_bf16 v[16:19], v[208:211], v[144:147], v[16:19]
v_lshlrev_b32 v236, 2, v236
v_lshrrev_b32 v236, v236, s4
v_mfma_f32_16x16x32_bf16 v[48:51], v[208:211], v[148:151], v[48:51]
buffer_load_dwordx4 v[140:143], v247, s[52:55], s91 offen offset:0 sc1
v_mfma_f32_16x16x32_bf16 v[80:83], v[208:211], v[152:155], v[80:83]
v_and_b32 v236, 15, v236
v_xor_b32 v236, s72, v236
v_mfma_f32_16x16x32_bf16 v[112:115], v[208:211], v[156:159], v[112:115]
ds_read_b64_tr_b16 v[208:209], v240 offset:17024
ds_read_b64_tr_b16 v[210:211], v240 offset:18048
v_mfma_f32_16x16x32_bf16 v[20:23], v[212:215], v[144:147], v[20:23]
s_add_u32 m0, s83, 1024
v_lshlrev_b32 v236, 3, v236
v_mfma_f32_16x16x32_bf16 v[52:55], v[212:215], v[148:151], v[52:55]
buffer_load_dwordx4 v233, s[44:47], s80 offen lds
v_mfma_f32_16x16x32_bf16 v[84:87], v[212:215], v[152:155], v[84:87]
v_accvgpr_write_b32 a225, v236
s_lshl3_add_u32 s5, s76, s101
v_mfma_f32_16x16x32_bf16 v[116:119], v[212:215], v[156:159], v[116:119]
ds_read_b64_tr_b16 v[212:213], v241 offset:17024
ds_read_b64_tr_b16 v[214:215], v241 offset:18048
v_mfma_f32_16x16x32_bf16 v[24:27], v[216:219], v[144:147], v[24:27]
s_lshl2_add_u32 s5, s5, s3
s_lshl_b32 s5, s5, 8
v_mfma_f32_16x16x32_bf16 v[56:59], v[216:219], v[148:151], v[56:59]
s_add_u32 s5, s5, 1048576
v_mfma_f32_16x16x32_bf16 v[88:91], v[216:219], v[152:155], v[88:91]
v_mfma_f32_16x16x32_bf16 v[120:123], v[216:219], v[156:159], v[120:123]
ds_read_b64_tr_b16 v[216:217], v242 offset:17024
ds_read_b64_tr_b16 v[218:219], v242 offset:18048
v_mfma_f32_16x16x32_bf16 v[28:31], v[220:223], v[144:147], v[28:31]
ds_write_b128 v238, v[248:251] offset:4096
v_mfma_f32_16x16x32_bf16 v[60:63], v[220:223], v[148:151], v[60:63]
buffer_store_dwordx4 v[248:251], v255, s[48:51], s89 offen offset:0 sc1
v_mfma_f32_16x16x32_bf16 v[92:95], v[220:223], v[152:155], v[92:95]
s_waitcnt vmcnt(16) lgkmcnt(3)
v_mfma_f32_16x16x32_bf16 v[124:127], v[220:223], v[156:159], v[124:127]
ds_read_b64_tr_b16 v[220:221], v243 offset:17024
ds_read_b64_tr_b16 v[222:223], v243 offset:18048
v_mfma_f32_16x16x32_bf16 v[0:3], v[192:195], v[160:163], v[0:3]
v_accvgpr_read_b32 v224, a8
v_accvgpr_read_b32 v225, a9
v_mfma_f32_16x16x32_bf16 v[32:35], v[192:195], v[164:167], v[32:35]
buffer_load_dwordx4 v[144:147], v244, s[52:55], s91 offen offset:1024 sc1
v_mfma_f32_16x16x32_bf16 v[64:67], v[192:195], v[168:171], v[64:67]
v_accvgpr_read_b32 v226, a10
v_accvgpr_read_b32 v227, a11
v_mfma_f32_16x16x32_bf16 v[96:99], v[192:195], v[172:175], v[96:99]
ds_read_b64_tr_b16 v[192:193], v240 offset:25344
ds_read_b64_tr_b16 v[194:195], v240 offset:26368
v_mfma_f32_16x16x32_bf16 v[4:7], v[196:199], v[160:163], v[4:7]
v_accvgpr_read_b32 v228, a12
v_accvgpr_read_b32 v229, a13
v_mfma_f32_16x16x32_bf16 v[36:39], v[196:199], v[164:167], v[36:39]
buffer_load_dwordx4 v[148:151], v245, s[52:55], s91 offen offset:1024 sc1
v_mfma_f32_16x16x32_bf16 v[68:71], v[196:199], v[168:171], v[68:71]
s_waitcnt lgkmcnt(2)
v_accvgpr_read_b32 v230, a14
v_mfma_f32_16x16x32_bf16 v[100:103], v[196:199], v[172:175], v[100:103]
ds_read_b64_tr_b16 v[196:197], v241 offset:25344
ds_read_b64_tr_b16 v[198:199], v241 offset:26368
v_mfma_f32_16x16x32_bf16 v[8:11], v[200:203], v[160:163], v[8:11]
s_add_u32 m0, s83, 2176
v_accvgpr_read_b32 v231, a15
v_mfma_f32_16x16x32_bf16 v[40:43], v[200:203], v[164:167], v[40:43]
buffer_load_dwordx4 v234, s[44:47], s80 offen lds
v_mfma_f32_16x16x32_bf16 v[72:75], v[200:203], v[168:171], v[72:75]
v_cvt_pk_bf16_f32 v248, v224, v225
v_cvt_pk_bf16_f32 v249, v226, v227
v_mfma_f32_16x16x32_bf16 v[104:107], v[200:203], v[172:175], v[104:107]
ds_read_b64_tr_b16 v[200:201], v242 offset:25344
ds_read_b64_tr_b16 v[202:203], v242 offset:26368
v_mfma_f32_16x16x32_bf16 v[12:15], v[204:207], v[160:163], v[12:15]
v_cvt_pk_bf16_f32 v250, v228, v229
v_cvt_pk_bf16_f32 v251, v230, v231
v_mfma_f32_16x16x32_bf16 v[44:47], v[204:207], v[164:167], v[44:47]
buffer_load_dwordx4 v[152:155], v246, s[52:55], s91 offen offset:1024 sc1
v_mfma_f32_16x16x32_bf16 v[76:79], v[204:207], v[168:171], v[76:79]
v_mfma_f32_16x16x32_bf16 v[108:111], v[204:207], v[172:175], v[108:111]
ds_read_b64_tr_b16 v[204:205], v243 offset:25344
ds_read_b64_tr_b16 v[206:207], v243 offset:26368
v_mfma_f32_16x16x32_bf16 v[16:19], v[208:211], v[160:163], v[16:19]
s_add_u32 m0, s83, 3200
v_mfma_f32_16x16x32_bf16 v[48:51], v[208:211], v[164:167], v[48:51]
buffer_load_dwordx4 v235, s[44:47], s80 offen lds
v_mfma_f32_16x16x32_bf16 v[80:83], v[208:211], v[168:171], v[80:83]
s_add_u32 s80, s80, 524288
v_mfma_f32_16x16x32_bf16 v[112:115], v[208:211], v[172:175], v[112:115]
ds_read_b64_tr_b16 v[208:209], v240 offset:25472
ds_read_b64_tr_b16 v[210:211], v240 offset:26496
v_mfma_f32_16x16x32_bf16 v[20:23], v[212:215], v[160:163], v[20:23]
v_mfma_f32_16x16x32_bf16 v[52:55], v[212:215], v[164:167], v[52:55]
buffer_load_dwordx4 v[156:159], v247, s[52:55], s91 offen offset:1024 sc1
v_mfma_f32_16x16x32_bf16 v[84:87], v[212:215], v[168:171], v[84:87]
v_mfma_f32_16x16x32_bf16 v[116:119], v[212:215], v[172:175], v[116:119]
ds_read_b64_tr_b16 v[212:213], v241 offset:25472
ds_read_b64_tr_b16 v[214:215], v241 offset:26496
v_mfma_f32_16x16x32_bf16 v[24:27], v[216:219], v[160:163], v[24:27]
v_mfma_f32_16x16x32_bf16 v[56:59], v[216:219], v[164:167], v[56:59]
v_mfma_f32_16x16x32_bf16 v[88:91], v[216:219], v[168:171], v[88:91]
v_mfma_f32_16x16x32_bf16 v[120:123], v[216:219], v[172:175], v[120:123]
ds_read_b64_tr_b16 v[216:217], v242 offset:25472
ds_read_b64_tr_b16 v[218:219], v242 offset:26496
v_mfma_f32_16x16x32_bf16 v[28:31], v[220:223], v[160:163], v[28:31]
ds_write_b128 v238, v[248:251] offset:5120
v_mfma_f32_16x16x32_bf16 v[60:63], v[220:223], v[164:167], v[60:63]
buffer_store_dwordx4 v[248:251], v255, s[48:51], s89 offen offset:1024 sc1
v_mfma_f32_16x16x32_bf16 v[92:95], v[220:223], v[168:171], v[92:95]
s_waitcnt vmcnt(15) lgkmcnt(3)
v_mfma_f32_16x16x32_bf16 v[124:127], v[220:223], v[172:175], v[124:127]
ds_read_b64_tr_b16 v[220:221], v243 offset:25472
ds_read_b64_tr_b16 v[222:223], v243 offset:26496
v_mfma_f32_16x16x32_bf16 v[0:3], v[192:195], v[176:179], v[0:3]
v_accvgpr_read_b32 v224, a80
v_accvgpr_read_b32 v225, a81
v_mfma_f32_16x16x32_bf16 v[32:35], v[192:195], v[180:183], v[32:35]
buffer_load_dwordx4 v[160:163], v244, s[52:55], s91 offen offset:2048 sc1
v_mfma_f32_16x16x32_bf16 v[64:67], v[192:195], v[184:187], v[64:67]
v_accvgpr_read_b32 v226, a82
v_accvgpr_read_b32 v227, a83
.La5_LW1_9:
s_cbranch_execz .La5_LW1_R0
v_mfma_f32_16x16x32_bf16 v[96:99], v[192:195], v[188:191], v[96:99]
v_accvgpr_read_b32 v228, a84
v_accvgpr_read_b32 v229, a85
v_mfma_f32_16x16x32_bf16 v[4:7], v[196:199], v[176:179], v[4:7]
s_waitcnt lgkmcnt(0)
s_barrier
v_mfma_f32_16x16x32_bf16 v[36:39], v[196:199], v[180:183], v[36:39]
buffer_load_dwordx4 v[164:167], v245, s[52:55], s91 offen offset:2048 sc1
v_mfma_f32_16x16x32_bf16 v[68:71], v[196:199], v[184:187], v[68:71]
ds_read_b64_tr_b16 v[192:193], v240 offset:33792
ds_read_b64_tr_b16 v[194:195], v240 offset:34816
v_mfma_f32_16x16x32_bf16 v[100:103], v[196:199], v[188:191], v[100:103]
ds_read_b64_tr_b16 v[196:197], v241 offset:33792
ds_read_b64_tr_b16 v[198:199], v241 offset:34816
v_mfma_f32_16x16x32_bf16 v[8:11], v[200:203], v[176:179], v[8:11]
v_accvgpr_read_b32 v230, a86
v_accvgpr_read_b32 v231, a87
v_mfma_f32_16x16x32_bf16 v[40:43], v[200:203], v[180:183], v[40:43]
buffer_load_dwordx4 v[168:171], v246, s[52:55], s91 offen offset:2048 sc1
v_mfma_f32_16x16x32_bf16 v[72:75], v[200:203], v[184:187], v[72:75]
v_cvt_pk_bf16_f32 v248, v224, v225
v_cvt_pk_bf16_f32 v249, v226, v227
v_mfma_f32_16x16x32_bf16 v[104:107], v[200:203], v[188:191], v[104:107]
ds_read_b64_tr_b16 v[200:201], v242 offset:33792
ds_read_b64_tr_b16 v[202:203], v242 offset:34816
v_mfma_f32_16x16x32_bf16 v[12:15], v[204:207], v[176:179], v[12:15]
s_add_u32 m0, s83, 16896
v_cvt_pk_bf16_f32 v250, v228, v229
v_mfma_f32_16x16x32_bf16 v[44:47], v[204:207], v[180:183], v[44:47]
buffer_load_dwordx4 v232, s[44:47], s80 offen lds
v_mfma_f32_16x16x32_bf16 v[76:79], v[204:207], v[184:187], v[76:79]
v_cvt_pk_bf16_f32 v251, v230, v231
v_accvgpr_write_b32 a12, v248
v_mfma_f32_16x16x32_bf16 v[108:111], v[204:207], v[188:191], v[108:111]
ds_read_b64_tr_b16 v[204:205], v243 offset:33792
ds_read_b64_tr_b16 v[206:207], v243 offset:34816
v_mfma_f32_16x16x32_bf16 v[16:19], v[208:211], v[176:179], v[16:19]
v_accvgpr_write_b32 a13, v249
v_accvgpr_write_b32 a14, v250
v_mfma_f32_16x16x32_bf16 v[48:51], v[208:211], v[180:183], v[48:51]
buffer_load_dwordx4 v[172:175], v247, s[52:55], s91 offen offset:2048 sc1
v_mfma_f32_16x16x32_bf16 v[80:83], v[208:211], v[184:187], v[80:83]
v_accvgpr_write_b32 a15, v251
v_mfma_f32_16x16x32_bf16 v[112:115], v[208:211], v[188:191], v[112:115]
ds_read_b64_tr_b16 v[208:209], v240 offset:33920
ds_read_b64_tr_b16 v[210:211], v240 offset:34944
v_mfma_f32_16x16x32_bf16 v[20:23], v[212:215], v[176:179], v[20:23]
s_add_u32 m0, s83, 17920
v_mfma_f32_16x16x32_bf16 v[52:55], v[212:215], v[180:183], v[52:55]
buffer_load_dwordx4 v233, s[44:47], s80 offen lds
v_mfma_f32_16x16x32_bf16 v[84:87], v[212:215], v[184:187], v[84:87]
v_mfma_f32_16x16x32_bf16 v[116:119], v[212:215], v[188:191], v[116:119]
ds_read_b64_tr_b16 v[212:213], v241 offset:33920
ds_read_b64_tr_b16 v[214:215], v241 offset:34944
v_mfma_f32_16x16x32_bf16 v[24:27], v[216:219], v[176:179], v[24:27]
v_mfma_f32_16x16x32_bf16 v[56:59], v[216:219], v[180:183], v[56:59]
v_mfma_f32_16x16x32_bf16 v[88:91], v[216:219], v[184:187], v[88:91]
v_mfma_f32_16x16x32_bf16 v[120:123], v[216:219], v[188:191], v[120:123]
ds_read_b64_tr_b16 v[216:217], v242 offset:33920
ds_read_b64_tr_b16 v[218:219], v242 offset:34944
v_mfma_f32_16x16x32_bf16 v[28:31], v[220:223], v[176:179], v[28:31]
v_mfma_f32_16x16x32_bf16 v[60:63], v[220:223], v[180:183], v[60:63]
buffer_store_dwordx4 v[248:251], v255, s[48:51], s89 offen offset:2048 sc1
v_mfma_f32_16x16x32_bf16 v[92:95], v[220:223], v[184:187], v[92:95]
s_waitcnt vmcnt(16) lgkmcnt(2)
v_mfma_f32_16x16x32_bf16 v[124:127], v[220:223], v[188:191], v[124:127]
ds_read_b64_tr_b16 v[220:221], v243 offset:33920
ds_read_b64_tr_b16 v[222:223], v243 offset:34944
v_mfma_f32_16x16x32_bf16 v[0:3], v[192:195], v[128:131], v[0:3]
v_accvgpr_read_b32 v224, a16
v_accvgpr_read_b32 v225, a17
v_mfma_f32_16x16x32_bf16 v[32:35], v[192:195], v[132:135], v[32:35]
buffer_load_dwordx4 v[176:179], v244, s[52:55], s91 offen offset:3072 sc1
v_mfma_f32_16x16x32_bf16 v[64:67], v[192:195], v[136:139], v[64:67]
v_accvgpr_read_b32 v226, a18
v_accvgpr_read_b32 v227, a19
v_mfma_f32_16x16x32_bf16 v[96:99], v[192:195], v[140:143], v[96:99]
ds_read_b64_tr_b16 v[192:193], v240 offset:42240
ds_read_b64_tr_b16 v[194:195], v240 offset:43264
v_mfma_f32_16x16x32_bf16 v[4:7], v[196:199], v[128:131], v[4:7]
v_accvgpr_read_b32 v228, a20
v_accvgpr_read_b32 v229, a21
v_mfma_f32_16x16x32_bf16 v[36:39], v[196:199], v[132:135], v[36:39]
buffer_load_dwordx4 v[180:183], v245, s[52:55], s91 offen offset:3072 sc1
v_mfma_f32_16x16x32_bf16 v[68:71], v[196:199], v[136:139], v[68:71]
s_waitcnt lgkmcnt(2)
v_accvgpr_read_b32 v230, a22
v_mfma_f32_16x16x32_bf16 v[100:103], v[196:199], v[140:143], v[100:103]
ds_read_b64_tr_b16 v[196:197], v241 offset:42240
ds_read_b64_tr_b16 v[198:199], v241 offset:43264
v_mfma_f32_16x16x32_bf16 v[8:11], v[200:203], v[128:131], v[8:11]
s_add_u32 m0, s83, 19072
v_accvgpr_read_b32 v231, a23
v_mfma_f32_16x16x32_bf16 v[40:43], v[200:203], v[132:135], v[40:43]
buffer_load_dwordx4 v234, s[44:47], s80 offen lds
v_mfma_f32_16x16x32_bf16 v[72:75], v[200:203], v[136:139], v[72:75]
v_cvt_pk_bf16_f32 v248, v224, v225
v_cvt_pk_bf16_f32 v249, v226, v227
v_mfma_f32_16x16x32_bf16 v[104:107], v[200:203], v[140:143], v[104:107]
ds_read_b64_tr_b16 v[200:201], v242 offset:42240
ds_read_b64_tr_b16 v[202:203], v242 offset:43264
v_mfma_f32_16x16x32_bf16 v[12:15], v[204:207], v[128:131], v[12:15]
v_cvt_pk_bf16_f32 v250, v228, v229
v_cvt_pk_bf16_f32 v251, v230, v231
v_mfma_f32_16x16x32_bf16 v[44:47], v[204:207], v[132:135], v[44:47]
buffer_load_dwordx4 v[184:187], v246, s[52:55], s91 offen offset:3072 sc1
v_mfma_f32_16x16x32_bf16 v[76:79], v[204:207], v[136:139], v[76:79]
v_accvgpr_write_b32 a16, v248
v_accvgpr_write_b32 a17, v249
v_mfma_f32_16x16x32_bf16 v[108:111], v[204:207], v[140:143], v[108:111]
ds_read_b64_tr_b16 v[204:205], v243 offset:42240
ds_read_b64_tr_b16 v[206:207], v243 offset:43264
v_mfma_f32_16x16x32_bf16 v[16:19], v[208:211], v[128:131], v[16:19]
s_add_u32 m0, s83, 20096
v_accvgpr_write_b32 a18, v250
v_mfma_f32_16x16x32_bf16 v[48:51], v[208:211], v[132:135], v[48:51]
buffer_load_dwordx4 v235, s[44:47], s80 offen lds
v_mfma_f32_16x16x32_bf16 v[80:83], v[208:211], v[136:139], v[80:83]
s_add_u32 s80, s80, 524288
v_accvgpr_write_b32 a19, v251
v_mfma_f32_16x16x32_bf16 v[112:115], v[208:211], v[140:143], v[112:115]
ds_read_b64_tr_b16 v[208:209], v240 offset:42368
ds_read_b64_tr_b16 v[210:211], v240 offset:43392
v_mfma_f32_16x16x32_bf16 v[20:23], v[212:215], v[128:131], v[20:23]
v_mfma_f32_16x16x32_bf16 v[52:55], v[212:215], v[132:135], v[52:55]
buffer_load_dwordx4 v[188:191], v247, s[52:55], s91 offen offset:3072 sc1
v_mfma_f32_16x16x32_bf16 v[84:87], v[212:215], v[136:139], v[84:87]
s_add_u32 s91, s91, 4096
v_mfma_f32_16x16x32_bf16 v[116:119], v[212:215], v[140:143], v[116:119]
ds_read_b64_tr_b16 v[212:213], v241 offset:42368
ds_read_b64_tr_b16 v[214:215], v241 offset:43392
v_mfma_f32_16x16x32_bf16 v[24:27], v[216:219], v[128:131], v[24:27]
v_mfma_f32_16x16x32_bf16 v[56:59], v[216:219], v[132:135], v[56:59]
v_mfma_f32_16x16x32_bf16 v[88:91], v[216:219], v[136:139], v[88:91]
v_mfma_f32_16x16x32_bf16 v[120:123], v[216:219], v[140:143], v[120:123]
ds_read_b64_tr_b16 v[216:217], v242 offset:42368
ds_read_b64_tr_b16 v[218:219], v242 offset:43392
v_mfma_f32_16x16x32_bf16 v[28:31], v[220:223], v[128:131], v[28:31]
v_mfma_f32_16x16x32_bf16 v[60:63], v[220:223], v[132:135], v[60:63]
buffer_store_dwordx4 v[248:251], v255, s[48:51], s89 offen offset:3072 sc1
v_mfma_f32_16x16x32_bf16 v[92:95], v[220:223], v[136:139], v[92:95]
s_waitcnt vmcnt(15) lgkmcnt(2)
v_mfma_f32_16x16x32_bf16 v[124:127], v[220:223], v[140:143], v[124:127]
ds_read_b64_tr_b16 v[220:221], v243 offset:42368
ds_read_b64_tr_b16 v[222:223], v243 offset:43392
v_mfma_f32_16x16x32_bf16 v[0:3], v[192:195], v[144:147], v[0:3]
v_accvgpr_read_b32 v224, a88
v_accvgpr_read_b32 v225, a89
v_mfma_f32_16x16x32_bf16 v[32:35], v[192:195], v[148:151], v[32:35]
buffer_load_dwordx4 v[128:131], v244, s[52:55], s91 offen offset:0 sc1
v_mfma_f32_16x16x32_bf16 v[64:67], v[192:195], v[152:155], v[64:67]
v_accvgpr_read_b32 v226, a90
v_accvgpr_read_b32 v227, a91
v_mfma_f32_16x16x32_bf16 v[96:99], v[192:195], v[156:159], v[96:99]
v_accvgpr_read_b32 v228, a92
v_accvgpr_read_b32 v229, a93
v_mfma_f32_16x16x32_bf16 v[4:7], v[196:199], v[144:147], v[4:7]
s_waitcnt lgkmcnt(0)
s_barrier
v_mfma_f32_16x16x32_bf16 v[36:39], v[196:199], v[148:151], v[36:39]
buffer_load_dwordx4 v[132:135], v245, s[52:55], s91 offen offset:0 sc1
v_mfma_f32_16x16x32_bf16 v[68:71], v[196:199], v[152:155], v[68:71]
ds_read_b64_tr_b16 v[192:193], v240 offset:50688
ds_read_b64_tr_b16 v[194:195], v240 offset:51712
v_mfma_f32_16x16x32_bf16 v[100:103], v[196:199], v[156:159], v[100:103]
ds_read_b64_tr_b16 v[196:197], v241 offset:50688
ds_read_b64_tr_b16 v[198:199], v241 offset:51712
v_mfma_f32_16x16x32_bf16 v[8:11], v[200:203], v[144:147], v[8:11]
v_accvgpr_read_b32 v230, a94
v_accvgpr_read_b32 v231, a95
v_mfma_f32_16x16x32_bf16 v[40:43], v[200:203], v[148:151], v[40:43]
buffer_load_dwordx4 v[136:139], v246, s[52:55], s91 offen offset:0 sc1
v_mfma_f32_16x16x32_bf16 v[72:75], v[200:203], v[152:155], v[72:75]
v_cvt_pk_bf16_f32 v248, v224, v225
v_cvt_pk_bf16_f32 v249, v226, v227
v_mfma_f32_16x16x32_bf16 v[104:107], v[200:203], v[156:159], v[104:107]
ds_read_b64_tr_b16 v[200:201], v242 offset:50688
ds_read_b64_tr_b16 v[202:203], v242 offset:51712
v_mfma_f32_16x16x32_bf16 v[12:15], v[204:207], v[144:147], v[12:15]
s_add_u32 m0, s83, 33792
v_cvt_pk_bf16_f32 v250, v228, v229
v_mfma_f32_16x16x32_bf16 v[44:47], v[204:207], v[148:151], v[44:47]
buffer_load_dwordx4 v232, s[44:47], s80 offen lds
v_mfma_f32_16x16x32_bf16 v[76:79], v[204:207], v[152:155], v[76:79]
v_cvt_pk_bf16_f32 v251, v230, v231
v_accvgpr_write_b32 a20, v248
v_mfma_f32_16x16x32_bf16 v[108:111], v[204:207], v[156:159], v[108:111]
ds_read_b64_tr_b16 v[204:205], v243 offset:50688
ds_read_b64_tr_b16 v[206:207], v243 offset:51712
v_mfma_f32_16x16x32_bf16 v[16:19], v[208:211], v[144:147], v[16:19]
v_accvgpr_write_b32 a21, v249
v_accvgpr_write_b32 a22, v250
v_mfma_f32_16x16x32_bf16 v[48:51], v[208:211], v[148:151], v[48:51]
buffer_load_dwordx4 v[140:143], v247, s[52:55], s91 offen offset:0 sc1
v_mfma_f32_16x16x32_bf16 v[80:83], v[208:211], v[152:155], v[80:83]
v_accvgpr_write_b32 a23, v251
s_mov_b32 s89, 0
v_mfma_f32_16x16x32_bf16 v[112:115], v[208:211], v[156:159], v[112:115]
ds_read_b64_tr_b16 v[208:209], v240 offset:50816
ds_read_b64_tr_b16 v[210:211], v240 offset:51840
v_mfma_f32_16x16x32_bf16 v[20:23], v[212:215], v[144:147], v[20:23]
s_add_u32 m0, s83, 34816
v_mfma_f32_16x16x32_bf16 v[52:55], v[212:215], v[148:151], v[52:55]
buffer_load_dwordx4 v233, s[44:47], s80 offen lds
v_mfma_f32_16x16x32_bf16 v[84:87], v[212:215], v[152:155], v[84:87]
v_mfma_f32_16x16x32_bf16 v[116:119], v[212:215], v[156:159], v[116:119]
ds_read_b64_tr_b16 v[212:213], v241 offset:50816
ds_read_b64_tr_b16 v[214:215], v241 offset:51840
v_mfma_f32_16x16x32_bf16 v[24:27], v[216:219], v[144:147], v[24:27]
v_mfma_f32_16x16x32_bf16 v[56:59], v[216:219], v[148:151], v[56:59]
v_mfma_f32_16x16x32_bf16 v[88:91], v[216:219], v[152:155], v[88:91]
v_mfma_f32_16x16x32_bf16 v[120:123], v[216:219], v[156:159], v[120:123]
ds_read_b64_tr_b16 v[216:217], v242 offset:50816
ds_read_b64_tr_b16 v[218:219], v242 offset:51840
v_mfma_f32_16x16x32_bf16 v[28:31], v[220:223], v[144:147], v[28:31]
v_mfma_f32_16x16x32_bf16 v[60:63], v[220:223], v[148:151], v[60:63]
buffer_store_dwordx4 v[248:251], v255, s[48:51], s89 offen offset:0 sc1
v_mfma_f32_16x16x32_bf16 v[92:95], v[220:223], v[152:155], v[92:95]
s_waitcnt vmcnt(16) lgkmcnt(2)
v_mfma_f32_16x16x32_bf16 v[124:127], v[220:223], v[156:159], v[124:127]
ds_read_b64_tr_b16 v[220:221], v243 offset:50816
ds_read_b64_tr_b16 v[222:223], v243 offset:51840
v_mfma_f32_16x16x32_bf16 v[0:3], v[192:195], v[160:163], v[0:3]
v_accvgpr_read_b32 v224, a24
v_accvgpr_read_b32 v225, a25
v_mfma_f32_16x16x32_bf16 v[32:35], v[192:195], v[164:167], v[32:35]
buffer_load_dwordx4 v[144:147], v244, s[52:55], s91 offen offset:1024 sc1
v_mfma_f32_16x16x32_bf16 v[64:67], v[192:195], v[168:171], v[64:67]
v_accvgpr_read_b32 v226, a26
v_accvgpr_read_b32 v227, a27
v_mfma_f32_16x16x32_bf16 v[96:99], v[192:195], v[172:175], v[96:99]
ds_read_b64_tr_b16 v[192:193], v240 offset:59136
ds_read_b64_tr_b16 v[194:195], v240 offset:60160
v_mfma_f32_16x16x32_bf16 v[4:7], v[196:199], v[160:163], v[4:7]
v_accvgpr_read_b32 v228, a28
v_accvgpr_read_b32 v229, a29
v_mfma_f32_16x16x32_bf16 v[36:39], v[196:199], v[164:167], v[36:39]
buffer_load_dwordx4 v[148:151], v245, s[52:55], s91 offen offset:1024 sc1
v_mfma_f32_16x16x32_bf16 v[68:71], v[196:199], v[168:171], v[68:71]
s_waitcnt lgkmcnt(2)
v_accvgpr_read_b32 v230, a30
v_mfma_f32_16x16x32_bf16 v[100:103], v[196:199], v[172:175], v[100:103]
ds_read_b64_tr_b16 v[196:197], v241 offset:59136
ds_read_b64_tr_b16 v[198:199], v241 offset:60160
v_mfma_f32_16x16x32_bf16 v[8:11], v[200:203], v[160:163], v[8:11]
s_add_u32 m0, s83, 35968
v_accvgpr_read_b32 v231, a31
v_mfma_f32_16x16x32_bf16 v[40:43], v[200:203], v[164:167], v[40:43]
buffer_load_dwordx4 v234, s[44:47], s80 offen lds
v_mfma_f32_16x16x32_bf16 v[72:75], v[200:203], v[168:171], v[72:75]
v_cvt_pk_bf16_f32 v248, v224, v225
v_cvt_pk_bf16_f32 v249, v226, v227
v_mfma_f32_16x16x32_bf16 v[104:107], v[200:203], v[172:175], v[104:107]
ds_read_b64_tr_b16 v[200:201], v242 offset:59136
ds_read_b64_tr_b16 v[202:203], v242 offset:60160
v_mfma_f32_16x16x32_bf16 v[12:15], v[204:207], v[160:163], v[12:15]
v_cvt_pk_bf16_f32 v250, v228, v229
v_cvt_pk_bf16_f32 v251, v230, v231
v_mfma_f32_16x16x32_bf16 v[44:47], v[204:207], v[164:167], v[44:47]
buffer_load_dwordx4 v[152:155], v246, s[52:55], s91 offen offset:1024 sc1
v_mfma_f32_16x16x32_bf16 v[76:79], v[204:207], v[168:171], v[76:79]
v_accvgpr_write_b32 a24, v248
v_accvgpr_write_b32 a25, v249
v_mfma_f32_16x16x32_bf16 v[108:111], v[204:207], v[172:175], v[108:111]
ds_read_b64_tr_b16 v[204:205], v243 offset:59136
ds_read_b64_tr_b16 v[206:207], v243 offset:60160
v_mfma_f32_16x16x32_bf16 v[16:19], v[208:211], v[160:163], v[16:19]
s_add_u32 m0, s83, 36992
v_accvgpr_write_b32 a26, v250
v_mfma_f32_16x16x32_bf16 v[48:51], v[208:211], v[164:167], v[48:51]
buffer_load_dwordx4 v235, s[44:47], s80 offen lds
v_mfma_f32_16x16x32_bf16 v[80:83], v[208:211], v[168:171], v[80:83]
s_add_u32 s80, s80, 524288
v_accvgpr_write_b32 a27, v251
v_mfma_f32_16x16x32_bf16 v[112:115], v[208:211], v[172:175], v[112:115]
ds_read_b64_tr_b16 v[208:209], v240 offset:59264
ds_read_b64_tr_b16 v[210:211], v240 offset:60288
v_mfma_f32_16x16x32_bf16 v[20:23], v[212:215], v[160:163], v[20:23]
v_mfma_f32_16x16x32_bf16 v[52:55], v[212:215], v[164:167], v[52:55]
buffer_load_dwordx4 v[156:159], v247, s[52:55], s91 offen offset:1024 sc1
v_mfma_f32_16x16x32_bf16 v[84:87], v[212:215], v[168:171], v[84:87]
v_mfma_f32_16x16x32_bf16 v[116:119], v[212:215], v[172:175], v[116:119]
ds_read_b64_tr_b16 v[212:213], v241 offset:59264
ds_read_b64_tr_b16 v[214:215], v241 offset:60288
v_mfma_f32_16x16x32_bf16 v[24:27], v[216:219], v[160:163], v[24:27]
v_mfma_f32_16x16x32_bf16 v[56:59], v[216:219], v[164:167], v[56:59]
v_mfma_f32_16x16x32_bf16 v[88:91], v[216:219], v[168:171], v[88:91]
v_mfma_f32_16x16x32_bf16 v[120:123], v[216:219], v[172:175], v[120:123]
ds_read_b64_tr_b16 v[216:217], v242 offset:59264
ds_read_b64_tr_b16 v[218:219], v242 offset:60288
v_mfma_f32_16x16x32_bf16 v[28:31], v[220:223], v[160:163], v[28:31]
v_mfma_f32_16x16x32_bf16 v[60:63], v[220:223], v[164:167], v[60:63]
buffer_store_dwordx4 v[248:251], v255, s[48:51], s89 offen offset:1024 sc1
v_mfma_f32_16x16x32_bf16 v[92:95], v[220:223], v[168:171], v[92:95]
s_waitcnt vmcnt(15) lgkmcnt(2)
v_mfma_f32_16x16x32_bf16 v[124:127], v[220:223], v[172:175], v[124:127]
ds_read_b64_tr_b16 v[220:221], v243 offset:59264
ds_read_b64_tr_b16 v[222:223], v243 offset:60288
v_mfma_f32_16x16x32_bf16 v[0:3], v[192:195], v[176:179], v[0:3]
v_accvgpr_read_b32 v224, a240
v_accvgpr_read_b32 v225, a241
v_mfma_f32_16x16x32_bf16 v[32:35], v[192:195], v[180:183], v[32:35]
buffer_load_dwordx4 v[160:163], v244, s[52:55], s91 offen offset:2048 sc1
v_mfma_f32_16x16x32_bf16 v[64:67], v[192:195], v[184:187], v[64:67]
v_accvgpr_read_b32 v226, a242
v_accvgpr_read_b32 v227, a243
v_mfma_f32_16x16x32_bf16 v[96:99], v[192:195], v[188:191], v[96:99]
v_accvgpr_read_b32 v228, a244
v_accvgpr_read_b32 v229, a245
v_mfma_f32_16x16x32_bf16 v[4:7], v[196:199], v[176:179], v[4:7]
s_waitcnt lgkmcnt(0)
s_barrier
v_mfma_f32_16x16x32_bf16 v[36:39], v[196:199], v[180:183], v[36:39]
buffer_load_dwordx4 v[164:167], v245, s[52:55], s91 offen offset:2048 sc1
v_mfma_f32_16x16x32_bf16 v[68:71], v[196:199], v[184:187], v[68:71]
ds_read_b64_tr_b16 v[192:193], v240 offset:0
ds_read_b64_tr_b16 v[194:195], v240 offset:1024
v_mfma_f32_16x16x32_bf16 v[100:103], v[196:199], v[188:191], v[100:103]
ds_read_b64_tr_b16 v[196:197], v241 offset:0
ds_read_b64_tr_b16 v[198:199], v241 offset:1024
v_mfma_f32_16x16x32_bf16 v[8:11], v[200:203], v[176:179], v[8:11]
v_accvgpr_read_b32 v230, a246
v_accvgpr_read_b32 v231, a247
v_mfma_f32_16x16x32_bf16 v[40:43], v[200:203], v[180:183], v[40:43]
buffer_load_dwordx4 v[168:171], v246, s[52:55], s91 offen offset:2048 sc1
v_mfma_f32_16x16x32_bf16 v[72:75], v[200:203], v[184:187], v[72:75]
v_cvt_pk_bf16_f32 v248, v224, v225
v_cvt_pk_bf16_f32 v249, v226, v227
v_mfma_f32_16x16x32_bf16 v[104:107], v[200:203], v[188:191], v[104:107]
ds_read_b64_tr_b16 v[200:201], v242 offset:0
ds_read_b64_tr_b16 v[202:203], v242 offset:1024
v_mfma_f32_16x16x32_bf16 v[12:15], v[204:207], v[176:179], v[12:15]
s_add_u32 m0, s83, 50688
v_cvt_pk_bf16_f32 v250, v228, v229
v_mfma_f32_16x16x32_bf16 v[44:47], v[204:207], v[180:183], v[44:47]
buffer_load_dwordx4 v232, s[44:47], s80 offen lds
v_mfma_f32_16x16x32_bf16 v[76:79], v[204:207], v[184:187], v[76:79]
v_cvt_pk_bf16_f32 v251, v230, v231
v_accvgpr_write_b32 a28, v248
v_mfma_f32_16x16x32_bf16 v[108:111], v[204:207], v[188:191], v[108:111]
ds_read_b64_tr_b16 v[204:205], v243 offset:0
ds_read_b64_tr_b16 v[206:207], v243 offset:1024
v_mfma_f32_16x16x32_bf16 v[16:19], v[208:211], v[176:179], v[16:19]
v_accvgpr_write_b32 a29, v249
v_accvgpr_write_b32 a30, v250
v_mfma_f32_16x16x32_bf16 v[48:51], v[208:211], v[180:183], v[48:51]
buffer_load_dwordx4 v[172:175], v247, s[52:55], s91 offen offset:2048 sc1
v_mfma_f32_16x16x32_bf16 v[80:83], v[208:211], v[184:187], v[80:83]
v_accvgpr_write_b32 a31, v251
v_mfma_f32_16x16x32_bf16 v[112:115], v[208:211], v[188:191], v[112:115]
ds_read_b64_tr_b16 v[208:209], v240 offset:128
ds_read_b64_tr_b16 v[210:211], v240 offset:1152
v_mfma_f32_16x16x32_bf16 v[20:23], v[212:215], v[176:179], v[20:23]
s_add_u32 m0, s83, 51712
v_mfma_f32_16x16x32_bf16 v[52:55], v[212:215], v[180:183], v[52:55]
buffer_load_dwordx4 v233, s[44:47], s80 offen lds
v_mfma_f32_16x16x32_bf16 v[84:87], v[212:215], v[184:187], v[84:87]
v_mfma_f32_16x16x32_bf16 v[116:119], v[212:215], v[188:191], v[116:119]
ds_read_b64_tr_b16 v[212:213], v241 offset:128
ds_read_b64_tr_b16 v[214:215], v241 offset:1152
v_mfma_f32_16x16x32_bf16 v[24:27], v[216:219], v[176:179], v[24:27]
v_mfma_f32_16x16x32_bf16 v[56:59], v[216:219], v[180:183], v[56:59]
v_mfma_f32_16x16x32_bf16 v[88:91], v[216:219], v[184:187], v[88:91]
v_mfma_f32_16x16x32_bf16 v[120:123], v[216:219], v[188:191], v[120:123]
ds_read_b64_tr_b16 v[216:217], v242 offset:128
ds_read_b64_tr_b16 v[218:219], v242 offset:1152
v_mfma_f32_16x16x32_bf16 v[28:31], v[220:223], v[176:179], v[28:31]
v_mfma_f32_16x16x32_bf16 v[60:63], v[220:223], v[180:183], v[60:63]
buffer_store_dwordx4 v[248:251], v255, s[48:51], s89 offen offset:2048 sc1
v_mfma_f32_16x16x32_bf16 v[92:95], v[220:223], v[184:187], v[92:95]
s_waitcnt vmcnt(16) lgkmcnt(2)
v_mfma_f32_16x16x32_bf16 v[124:127], v[220:223], v[188:191], v[124:127]
ds_read_b64_tr_b16 v[220:221], v243 offset:128
ds_read_b64_tr_b16 v[222:223], v243 offset:1152
v_mfma_f32_16x16x32_bf16 v[0:3], v[192:195], v[128:131], v[0:3]
v_accvgpr_read_b32 v224, a32
v_accvgpr_read_b32 v225, a33
v_mfma_f32_16x16x32_bf16 v[32:35], v[192:195], v[132:135], v[32:35]
buffer_load_dwordx4 v[176:179], v244, s[52:55], s91 offen offset:3072 sc1
v_mfma_f32_16x16x32_bf16 v[64:67], v[192:195], v[136:139], v[64:67]
v_accvgpr_read_b32 v226, a34
v_accvgpr_read_b32 v227, a35
v_mfma_f32_16x16x32_bf16 v[96:99], v[192:195], v[140:143], v[96:99]
ds_read_b64_tr_b16 v[192:193], v240 offset:8448
ds_read_b64_tr_b16 v[194:195], v240 offset:9472
v_mfma_f32_16x16x32_bf16 v[4:7], v[196:199], v[128:131], v[4:7]
v_accvgpr_read_b32 v228, a36
v_accvgpr_read_b32 v229, a37
v_mfma_f32_16x16x32_bf16 v[36:39], v[196:199], v[132:135], v[36:39]
buffer_load_dwordx4 v[180:183], v245, s[52:55], s91 offen offset:3072 sc1
v_mfma_f32_16x16x32_bf16 v[68:71], v[196:199], v[136:139], v[68:71]
s_waitcnt lgkmcnt(2)
v_accvgpr_read_b32 v230, a38
v_mfma_f32_16x16x32_bf16 v[100:103], v[196:199], v[140:143], v[100:103]
ds_read_b64_tr_b16 v[196:197], v241 offset:8448
ds_read_b64_tr_b16 v[198:199], v241 offset:9472
v_mfma_f32_16x16x32_bf16 v[8:11], v[200:203], v[128:131], v[8:11]
s_add_u32 m0, s83, 52864
v_accvgpr_read_b32 v231, a39
v_mfma_f32_16x16x32_bf16 v[40:43], v[200:203], v[132:135], v[40:43]
buffer_load_dwordx4 v234, s[44:47], s80 offen lds
v_mfma_f32_16x16x32_bf16 v[72:75], v[200:203], v[136:139], v[72:75]
v_cvt_pk_bf16_f32 v248, v224, v225
v_cvt_pk_bf16_f32 v249, v226, v227
v_mfma_f32_16x16x32_bf16 v[104:107], v[200:203], v[140:143], v[104:107]
ds_read_b64_tr_b16 v[200:201], v242 offset:8448
ds_read_b64_tr_b16 v[202:203], v242 offset:9472
v_mfma_f32_16x16x32_bf16 v[12:15], v[204:207], v[128:131], v[12:15]
v_cvt_pk_bf16_f32 v250, v228, v229
v_cvt_pk_bf16_f32 v251, v230, v231
v_mfma_f32_16x16x32_bf16 v[44:47], v[204:207], v[132:135], v[44:47]
buffer_load_dwordx4 v[184:187], v246, s[52:55], s91 offen offset:3072 sc1
v_mfma_f32_16x16x32_bf16 v[76:79], v[204:207], v[136:139], v[76:79]
v_accvgpr_write_b32 a32, v248
v_accvgpr_write_b32 a33, v249
v_mfma_f32_16x16x32_bf16 v[108:111], v[204:207], v[140:143], v[108:111]
ds_read_b64_tr_b16 v[204:205], v243 offset:8448
ds_read_b64_tr_b16 v[206:207], v243 offset:9472
v_mfma_f32_16x16x32_bf16 v[16:19], v[208:211], v[128:131], v[16:19]
s_add_u32 m0, s83, 53888
v_accvgpr_write_b32 a34, v250
v_mfma_f32_16x16x32_bf16 v[48:51], v[208:211], v[132:135], v[48:51]
buffer_load_dwordx4 v235, s[44:47], s80 offen lds
v_mfma_f32_16x16x32_bf16 v[80:83], v[208:211], v[136:139], v[80:83]
v_accvgpr_write_b32 a35, v251
v_mfma_f32_16x16x32_bf16 v[112:115], v[208:211], v[140:143], v[112:115]
ds_read_b64_tr_b16 v[208:209], v240 offset:8576
ds_read_b64_tr_b16 v[210:211], v240 offset:9600
v_mfma_f32_16x16x32_bf16 v[20:23], v[212:215], v[128:131], v[20:23]
v_mfma_f32_16x16x32_bf16 v[52:55], v[212:215], v[132:135], v[52:55]
buffer_load_dwordx4 v[188:191], v247, s[52:55], s91 offen offset:3072 sc1
v_mfma_f32_16x16x32_bf16 v[84:87], v[212:215], v[136:139], v[84:87]
s_add_u32 s91, s91, 4096
v_mfma_f32_16x16x32_bf16 v[116:119], v[212:215], v[140:143], v[116:119]
ds_read_b64_tr_b16 v[212:213], v241 offset:8576
ds_read_b64_tr_b16 v[214:215], v241 offset:9600
v_mfma_f32_16x16x32_bf16 v[24:27], v[216:219], v[128:131], v[24:27]
v_mfma_f32_16x16x32_bf16 v[56:59], v[216:219], v[132:135], v[56:59]
v_mfma_f32_16x16x32_bf16 v[88:91], v[216:219], v[136:139], v[88:91]
v_mfma_f32_16x16x32_bf16 v[120:123], v[216:219], v[140:143], v[120:123]
ds_read_b64_tr_b16 v[216:217], v242 offset:8576
ds_read_b64_tr_b16 v[218:219], v242 offset:9600
v_mfma_f32_16x16x32_bf16 v[28:31], v[220:223], v[128:131], v[28:31]
v_mfma_f32_16x16x32_bf16 v[60:63], v[220:223], v[132:135], v[60:63]
buffer_store_dwordx4 v[248:251], v255, s[48:51], s89 offen offset:3072 sc1
v_mfma_f32_16x16x32_bf16 v[92:95], v[220:223], v[136:139], v[92:95]
s_waitcnt vmcnt(15) lgkmcnt(2)
v_mfma_f32_16x16x32_bf16 v[124:127], v[220:223], v[140:143], v[124:127]
ds_read_b64_tr_b16 v[220:221], v243 offset:8576
ds_read_b64_tr_b16 v[222:223], v243 offset:9600
v_mfma_f32_16x16x32_bf16 v[0:3], v[192:195], v[144:147], v[0:3]
v_accvgpr_read_b32 v224, a248
v_accvgpr_read_b32 v225, a249
v_mfma_f32_16x16x32_bf16 v[32:35], v[192:195], v[148:151], v[32:35]
buffer_load_dwordx4 v[128:131], v244, s[52:55], s91 offen offset:0 sc1
v_mfma_f32_16x16x32_bf16 v[64:67], v[192:195], v[152:155], v[64:67]
v_accvgpr_read_b32 v226, a250
v_accvgpr_read_b32 v227, a251
v_mfma_f32_16x16x32_bf16 v[96:99], v[192:195], v[156:159], v[96:99]
v_accvgpr_read_b32 v228, a252
v_accvgpr_read_b32 v229, a253
v_mfma_f32_16x16x32_bf16 v[4:7], v[196:199], v[144:147], v[4:7]
s_waitcnt lgkmcnt(0)
s_barrier
v_mfma_f32_16x16x32_bf16 v[36:39], v[196:199], v[148:151], v[36:39]
buffer_load_dwordx4 v[132:135], v245, s[52:55], s91 offen offset:0 sc1
v_mfma_f32_16x16x32_bf16 v[68:71], v[196:199], v[152:155], v[68:71]
ds_read_b64_tr_b16 v[192:193], v240 offset:16896
ds_read_b64_tr_b16 v[194:195], v240 offset:17920
v_mfma_f32_16x16x32_bf16 v[100:103], v[196:199], v[156:159], v[100:103]
ds_read_b64_tr_b16 v[196:197], v241 offset:16896
ds_read_b64_tr_b16 v[198:199], v241 offset:17920
v_mfma_f32_16x16x32_bf16 v[8:11], v[200:203], v[144:147], v[8:11]
v_accvgpr_read_b32 v230, a254
v_accvgpr_read_b32 v231, a255
v_mfma_f32_16x16x32_bf16 v[40:43], v[200:203], v[148:151], v[40:43]
buffer_load_dwordx4 v[136:139], v246, s[52:55], s91 offen offset:0 sc1
v_mfma_f32_16x16x32_bf16 v[72:75], v[200:203], v[152:155], v[72:75]
v_cvt_pk_bf16_f32 v248, v224, v225
v_cvt_pk_bf16_f32 v249, v226, v227
v_mfma_f32_16x16x32_bf16 v[104:107], v[200:203], v[156:159], v[104:107]
ds_read_b64_tr_b16 v[200:201], v242 offset:16896
ds_read_b64_tr_b16 v[202:203], v242 offset:17920
v_mfma_f32_16x16x32_bf16 v[12:15], v[204:207], v[144:147], v[12:15]
v_cvt_pk_bf16_f32 v250, v228, v229
v_cvt_pk_bf16_f32 v251, v230, v231
v_mfma_f32_16x16x32_bf16 v[44:47], v[204:207], v[148:151], v[44:47]
buffer_load_dwordx4 v[140:143], v247, s[52:55], s91 offen offset:0 sc1
v_mfma_f32_16x16x32_bf16 v[76:79], v[204:207], v[152:155], v[76:79]
v_accvgpr_write_b32 a36, v248
v_accvgpr_write_b32 a37, v249
v_mfma_f32_16x16x32_bf16 v[108:111], v[204:207], v[156:159], v[108:111]
ds_read_b64_tr_b16 v[204:205], v243 offset:16896
ds_read_b64_tr_b16 v[206:207], v243 offset:17920
v_mfma_f32_16x16x32_bf16 v[16:19], v[208:211], v[144:147], v[16:19]
v_accvgpr_write_b32 a38, v250
v_accvgpr_write_b32 a39, v251
v_mfma_f32_16x16x32_bf16 v[48:51], v[208:211], v[148:151], v[48:51]
s_mov_b32 s89, 4096
v_mfma_f32_16x16x32_bf16 v[80:83], v[208:211], v[152:155], v[80:83]
s_getpc_b64 s[92:93]
.La5_LCT2:
s_sub_u32 s92, s92, .La5_LCT2-.La5_LSTART
s_subb_u32 s93, s93, 0
v_mfma_f32_16x16x32_bf16 v[112:115], v[208:211], v[156:159], v[112:115]
ds_read_b64_tr_b16 v[208:209], v240 offset:17024
ds_read_b64_tr_b16 v[210:211], v240 offset:18048
v_mfma_f32_16x16x32_bf16 v[20:23], v[212:215], v[144:147], v[20:23]
s_and_b32 s93, s93, 0xffff
s_lshr_b32 s94, s2, 3
v_mfma_f32_16x16x32_bf16 v[52:55], v[212:215], v[148:151], v[52:55]
s_lshl2_add_u32 s94, s94, s3
s_lshl_b32 s94, s94, 10
v_mfma_f32_16x16x32_bf16 v[84:87], v[212:215], v[152:155], v[84:87]
v_lshrrev_b32 v233, 7, v255
v_lshlrev_b32 v232, 3, v255
v_mfma_f32_16x16x32_bf16 v[116:119], v[212:215], v[156:159], v[116:119]
ds_read_b64_tr_b16 v[212:213], v241 offset:17024
ds_read_b64_tr_b16 v[214:215], v241 offset:18048
v_mfma_f32_16x16x32_bf16 v[24:27], v[216:219], v[144:147], v[24:27]
v_lshl_add_u32 v232, v233, 28, v232
v_add_u32 v232, s94, v232
v_mfma_f32_16x16x32_bf16 v[56:59], v[216:219], v[148:151], v[56:59]
s_mov_b32 s94, .La5_LCODEEND-.La5_LSTART
s_mov_b32 s95, 0x20000
v_mfma_f32_16x16x32_bf16 v[88:91], v[216:219], v[152:155], v[88:91]
.La5_LW1_10:
s_cbranch_execz .La5_LW1_R0
buffer_load_dword a253, v232, s[92:95], 0 offen
v_mfma_f32_16x16x32_bf16 v[120:123], v[216:219], v[156:159], v[120:123]
ds_read_b64_tr_b16 v[216:217], v242 offset:17024
ds_read_b64_tr_b16 v[218:219], v242 offset:18048
v_mfma_f32_16x16x32_bf16 v[28:31], v[220:223], v[144:147], v[28:31]
ds_write_b128 v238, a[12:15] offset:6144
v_mfma_f32_16x16x32_bf16 v[60:63], v[220:223], v[148:151], v[60:63]
buffer_store_dwordx4 v[248:251], v255, s[48:51], s89 offen offset:0 sc1
v_mfma_f32_16x16x32_bf16 v[92:95], v[220:223], v[152:155], v[92:95]
s_waitcnt vmcnt(15) lgkmcnt(3)
s_mov_b32 s80, 8192
v_mfma_f32_16x16x32_bf16 v[124:127], v[220:223], v[156:159], v[124:127]
ds_read_b64_tr_b16 v[220:221], v243 offset:17024
ds_read_b64_tr_b16 v[222:223], v243 offset:18048
v_mfma_f32_16x16x32_bf16 v[0:3], v[192:195], v[160:163], v[0:3]
v_accvgpr_read_b32 v224, a40
v_accvgpr_read_b32 v225, a41
v_mfma_f32_16x16x32_bf16 v[32:35], v[192:195], v[164:167], v[32:35]
buffer_load_dwordx4 a[192:195], v244, s[52:55], s91 offen offset:1024 sc1
v_mfma_f32_16x16x32_bf16 v[64:67], v[192:195], v[168:171], v[64:67]
v_accvgpr_read_b32 v226, a42
v_accvgpr_read_b32 v227, a43
v_mfma_f32_16x16x32_bf16 v[96:99], v[192:195], v[172:175], v[96:99]
ds_read_b64_tr_b16 v[192:193], v240 offset:25344
ds_read_b64_tr_b16 v[194:195], v240 offset:26368
v_mfma_f32_16x16x32_bf16 v[4:7], v[196:199], v[160:163], v[4:7]
v_accvgpr_read_b32 v228, a44
v_accvgpr_read_b32 v229, a45
v_mfma_f32_16x16x32_bf16 v[36:39], v[196:199], v[164:167], v[36:39]
buffer_load_dwordx4 a[196:199], v245, s[52:55], s91 offen offset:1024 sc1
v_mfma_f32_16x16x32_bf16 v[68:71], v[196:199], v[168:171], v[68:71]
s_waitcnt lgkmcnt(2)
v_accvgpr_read_b32 v230, a46
v_mfma_f32_16x16x32_bf16 v[100:103], v[196:199], v[172:175], v[100:103]
ds_read_b64_tr_b16 v[196:197], v241 offset:25344
ds_read_b64_tr_b16 v[198:199], v241 offset:26368
v_mfma_f32_16x16x32_bf16 v[8:11], v[200:203], v[160:163], v[8:11]
v_accvgpr_read_b32 v231, a47
v_cvt_pk_bf16_f32 v248, v224, v225
v_mfma_f32_16x16x32_bf16 v[40:43], v[200:203], v[164:167], v[40:43]
buffer_load_dwordx4 a[200:203], v246, s[52:55], s91 offen offset:1024 sc1
v_mfma_f32_16x16x32_bf16 v[72:75], v[200:203], v[168:171], v[72:75]
v_cvt_pk_bf16_f32 v249, v226, v227
v_cvt_pk_bf16_f32 v250, v228, v229
v_mfma_f32_16x16x32_bf16 v[104:107], v[200:203], v[172:175], v[104:107]
ds_read_b64_tr_b16 v[200:201], v242 offset:25344
ds_read_b64_tr_b16 v[202:203], v242 offset:26368
v_mfma_f32_16x16x32_bf16 v[12:15], v[204:207], v[160:163], v[12:15]
v_cvt_pk_bf16_f32 v251, v230, v231
v_accvgpr_write_b32 a40, v248
v_mfma_f32_16x16x32_bf16 v[44:47], v[204:207], v[164:167], v[44:47]
buffer_load_dwordx4 a[204:207], v247, s[52:55], s91 offen offset:1024 sc1
v_mfma_f32_16x16x32_bf16 v[76:79], v[204:207], v[168:171], v[76:79]
v_accvgpr_write_b32 a41, v249
v_accvgpr_write_b32 a42, v250
v_mfma_f32_16x16x32_bf16 v[108:111], v[204:207], v[172:175], v[108:111]
ds_read_b64_tr_b16 v[204:205], v243 offset:25344
ds_read_b64_tr_b16 v[206:207], v243 offset:26368
v_mfma_f32_16x16x32_bf16 v[16:19], v[208:211], v[160:163], v[16:19]
v_accvgpr_write_b32 a43, v251
s_add_u32 s83, s83, 4294886912
v_mfma_f32_16x16x32_bf16 v[48:51], v[208:211], v[164:167], v[48:51]
buffer_load_dwordx4 a[96:99], v244, s[52:55], s91 offen offset:2048 sc1
v_mfma_f32_16x16x32_bf16 v[80:83], v[208:211], v[168:171], v[80:83]
s_lshl_b32 s92, s76, 21
s_add_u32 s92, s92, 54525952
v_mfma_f32_16x16x32_bf16 v[112:115], v[208:211], v[172:175], v[112:115]
ds_read_b64_tr_b16 v[208:209], v240 offset:25472
ds_read_b64_tr_b16 v[210:211], v240 offset:26496
v_mfma_f32_16x16x32_bf16 v[20:23], v[212:215], v[160:163], v[20:23]
s_add_u32 s44, s26, s92
s_addc_u32 s45, s27, 0
v_mfma_f32_16x16x32_bf16 v[52:55], v[212:215], v[164:167], v[52:55]
buffer_load_dwordx4 a[100:103], v245, s[52:55], s91 offen offset:2048 sc1
v_mfma_f32_16x16x32_bf16 v[84:87], v[212:215], v[168:171], v[84:87]
s_and_b32 s45, s45, 0xffff
s_mov_b32 s46, 2097536
v_mfma_f32_16x16x32_bf16 v[116:119], v[212:215], v[172:175], v[116:119]
ds_read_b64_tr_b16 v[212:213], v241 offset:25472
ds_read_b64_tr_b16 v[214:215], v241 offset:26496
v_mfma_f32_16x16x32_bf16 v[24:27], v[216:219], v[160:163], v[24:27]
s_mov_b32 s95, 4096
s_mov_b32 s96, 8192
v_mfma_f32_16x16x32_bf16 v[56:59], v[216:219], v[164:167], v[56:59]
buffer_load_dwordx4 a[104:107], v246, s[52:55], s91 offen offset:2048 sc1
v_mfma_f32_16x16x32_bf16 v[88:91], v[216:219], v[168:171], v[88:91]
s_mov_b32 s97, 12288
s_mul_i32 s92, s3, 196608
v_mfma_f32_16x16x32_bf16 v[120:123], v[216:219], v[172:175], v[120:123]
ds_read_b64_tr_b16 v[216:217], v242 offset:25472
ds_read_b64_tr_b16 v[218:219], v242 offset:26496
v_mfma_f32_16x16x32_bf16 v[28:31], v[220:223], v[160:163], v[28:31]
ds_write_b128 v238, a[16:19] offset:7168
v_mfma_f32_16x16x32_bf16 v[60:63], v[220:223], v[164:167], v[60:63]
buffer_store_dwordx4 v[248:251], v255, s[48:51], s89 offen offset:1024 sc1
v_mfma_f32_16x16x32_bf16 v[92:95], v[220:223], v[168:171], v[92:95]
s_waitcnt vmcnt(15) lgkmcnt(3)
s_sub_u32 s93, 1376256, s92
v_mfma_f32_16x16x32_bf16 v[124:127], v[220:223], v[172:175], v[124:127]
ds_read_b64_tr_b16 v[220:221], v243 offset:25472
ds_read_b64_tr_b16 v[222:223], v243 offset:26496
v_mfma_f32_16x16x32_bf16 v[0:3], v[192:195], v[176:179], v[0:3]
v_accvgpr_read_b32 v224, a48
v_accvgpr_read_b32 v225, a49
v_mfma_f32_16x16x32_bf16 v[32:35], v[192:195], v[180:183], v[32:35]
buffer_load_dwordx4 a[108:111], v247, s[52:55], s91 offen offset:2048 sc1
v_mfma_f32_16x16x32_bf16 v[64:67], v[192:195], v[184:187], v[64:67]
v_accvgpr_read_b32 v226, a50
v_accvgpr_read_b32 v227, a51
v_mfma_f32_16x16x32_bf16 v[96:99], v[192:195], v[188:191], v[96:99]
v_accvgpr_read_b32 v228, a52
v_accvgpr_read_b32 v229, a53
v_mfma_f32_16x16x32_bf16 v[4:7], v[196:199], v[176:179], v[4:7]
s_waitcnt lgkmcnt(0)
s_barrier
v_mfma_f32_16x16x32_bf16 v[36:39], v[196:199], v[180:183], v[36:39]
buffer_load_dwordx4 a[112:115], v244, s[52:55], s91 offen offset:3072 sc1
v_mfma_f32_16x16x32_bf16 v[68:71], v[196:199], v[184:187], v[68:71]
ds_read_b64_tr_b16 v[192:193], v240 offset:33792
ds_read_b64_tr_b16 v[194:195], v240 offset:34816
v_mfma_f32_16x16x32_bf16 v[100:103], v[196:199], v[188:191], v[100:103]
ds_read_b64_tr_b16 v[196:197], v241 offset:33792
ds_read_b64_tr_b16 v[198:199], v241 offset:34816
v_mfma_f32_16x16x32_bf16 v[8:11], v[200:203], v[176:179], v[8:11]
v_accvgpr_read_b32 v230, a54
v_accvgpr_read_b32 v231, a55
v_mfma_f32_16x16x32_bf16 v[40:43], v[200:203], v[180:183], v[40:43]
buffer_load_dwordx4 a[116:119], v245, s[52:55], s91 offen offset:3072 sc1
v_mfma_f32_16x16x32_bf16 v[72:75], v[200:203], v[184:187], v[72:75]
v_cvt_pk_bf16_f32 v248, v224, v225
v_cvt_pk_bf16_f32 v249, v226, v227
v_mfma_f32_16x16x32_bf16 v[104:107], v[200:203], v[188:191], v[104:107]
ds_read_b64_tr_b16 v[200:201], v242 offset:33792
ds_read_b64_tr_b16 v[202:203], v242 offset:34816
v_mfma_f32_16x16x32_bf16 v[12:15], v[204:207], v[176:179], v[12:15]
ds_write_b128 v238, a[20:23] offset:32768
v_mfma_f32_16x16x32_bf16 v[44:47], v[204:207], v[180:183], v[44:47]
buffer_load_dwordx4 a[120:123], v246, s[52:55], s91 offen offset:3072 sc1
v_mfma_f32_16x16x32_bf16 v[76:79], v[204:207], v[184:187], v[76:79]
v_cvt_pk_bf16_f32 v250, v228, v229
v_cvt_pk_bf16_f32 v251, v230, v231
v_mfma_f32_16x16x32_bf16 v[108:111], v[204:207], v[188:191], v[108:111]
ds_read_b64_tr_b16 v[204:205], v243 offset:33792
ds_read_b64_tr_b16 v[206:207], v243 offset:34816
v_mfma_f32_16x16x32_bf16 v[16:19], v[208:211], v[176:179], v[16:19]
v_accvgpr_write_b32 a44, v248
v_accvgpr_write_b32 a45, v249
v_mfma_f32_16x16x32_bf16 v[48:51], v[208:211], v[180:183], v[48:51]
buffer_load_dwordx4 a[124:127], v247, s[52:55], s91 offen offset:3072 sc1
v_mfma_f32_16x16x32_bf16 v[80:83], v[208:211], v[184:187], v[80:83]
v_accvgpr_write_b32 a46, v250
v_accvgpr_write_b32 a47, v251
v_mfma_f32_16x16x32_bf16 v[112:115], v[208:211], v[188:191], v[112:115]
ds_read_b64_tr_b16 v[208:209], v240 offset:33920
ds_read_b64_tr_b16 v[210:211], v240 offset:34944
v_mfma_f32_16x16x32_bf16 v[20:23], v[212:215], v[176:179], v[20:23]
v_mfma_f32_16x16x32_bf16 v[52:55], v[212:215], v[180:183], v[52:55]
v_mfma_f32_16x16x32_bf16 v[84:87], v[212:215], v[184:187], v[84:87]
v_mfma_f32_16x16x32_bf16 v[116:119], v[212:215], v[188:191], v[116:119]
ds_read_b64_tr_b16 v[212:213], v241 offset:33920
ds_read_b64_tr_b16 v[214:215], v241 offset:34944
v_mfma_f32_16x16x32_bf16 v[24:27], v[216:219], v[176:179], v[24:27]
v_mfma_f32_16x16x32_bf16 v[56:59], v[216:219], v[180:183], v[56:59]
v_mfma_f32_16x16x32_bf16 v[88:91], v[216:219], v[184:187], v[88:91]
v_mfma_f32_16x16x32_bf16 v[120:123], v[216:219], v[188:191], v[120:123]
ds_read_b64_tr_b16 v[216:217], v242 offset:33920
ds_read_b64_tr_b16 v[218:219], v242 offset:34944
v_mfma_f32_16x16x32_bf16 v[28:31], v[220:223], v[176:179], v[28:31]
ds_write_b128 v238, a[24:27] offset:33792
v_mfma_f32_16x16x32_bf16 v[60:63], v[220:223], v[180:183], v[60:63]
buffer_store_dwordx4 v[248:251], v255, s[48:51], s89 offen offset:2048 sc1
v_mfma_f32_16x16x32_bf16 v[92:95], v[220:223], v[184:187], v[92:95]
s_waitcnt vmcnt(16) lgkmcnt(3)
v_mfma_f32_16x16x32_bf16 v[124:127], v[220:223], v[188:191], v[124:127]
ds_read_b64_tr_b16 v[220:221], v243 offset:33920
ds_read_b64_tr_b16 v[222:223], v243 offset:34944
v_mfma_f32_16x16x32_bf16 v[0:3], v[192:195], v[128:131], v[0:3]
v_accvgpr_read_b32 v224, a56
v_accvgpr_read_b32 v225, a57
v_mfma_f32_16x16x32_bf16 v[32:35], v[192:195], v[132:135], v[32:35]
v_accvgpr_read_b32 v226, a58
v_accvgpr_read_b32 v227, a59
v_mfma_f32_16x16x32_bf16 v[64:67], v[192:195], v[136:139], v[64:67]
v_accvgpr_read_b32 v228, a60
v_accvgpr_read_b32 v229, a61
v_mfma_f32_16x16x32_bf16 v[96:99], v[192:195], v[140:143], v[96:99]
ds_read_b64_tr_b16 v[192:193], v240 offset:42240
ds_read_b64_tr_b16 v[194:195], v240 offset:43264
v_mfma_f32_16x16x32_bf16 v[4:7], v[196:199], v[128:131], v[4:7]
v_accvgpr_read_b32 v230, a62
v_accvgpr_read_b32 v231, a63
v_mfma_f32_16x16x32_bf16 v[36:39], v[196:199], v[132:135], v[36:39]
v_cvt_pk_bf16_f32 v248, v224, v225
v_cvt_pk_bf16_f32 v249, v226, v227
v_mfma_f32_16x16x32_bf16 v[68:71], v[196:199], v[136:139], v[68:71]
s_waitcnt lgkmcnt(2)
v_cvt_pk_bf16_f32 v250, v228, v229
v_mfma_f32_16x16x32_bf16 v[100:103], v[196:199], v[140:143], v[100:103]
ds_read_b64_tr_b16 v[196:197], v240 offset:50688
ds_read_b64_tr_b16 v[198:199], v240 offset:51712
v_mfma_f32_16x16x32_bf16 v[8:11], v[200:203], v[128:131], v[8:11]
v_cvt_pk_bf16_f32 v251, v230, v231
v_accvgpr_write_b32 a48, v248
v_mfma_f32_16x16x32_bf16 v[40:43], v[200:203], v[132:135], v[40:43]
v_accvgpr_write_b32 a49, v249
v_accvgpr_write_b32 a50, v250
v_mfma_f32_16x16x32_bf16 v[72:75], v[200:203], v[136:139], v[72:75]
v_accvgpr_write_b32 a51, v251
v_add_u32 v244, s92, v244
v_mfma_f32_16x16x32_bf16 v[104:107], v[200:203], v[140:143], v[104:107]
ds_read_b64_tr_b16 v[200:201], v240 offset:59136
ds_read_b64_tr_b16 v[202:203], v240 offset:60160
v_mfma_f32_16x16x32_bf16 v[12:15], v[204:207], v[128:131], v[12:15]
ds_write_b128 v238, a[28:31] offset:34816
v_mfma_f32_16x16x32_bf16 v[44:47], v[204:207], v[132:135], v[44:47]
v_add_u32 v246, s93, v246
v_add_u32 v245, 0x1000, v244
v_mfma_f32_16x16x32_bf16 v[76:79], v[204:207], v[136:139], v[76:79]
v_add_u32 v247, 0x1000, v246
s_lshl_b32 s91, s72, 13
v_mfma_f32_16x16x32_bf16 v[108:111], v[204:207], v[140:143], v[108:111]
ds_read_b64_tr_b16 v[204:205], v241 offset:42240
ds_read_b64_tr_b16 v[206:207], v241 offset:43264
v_mfma_f32_16x16x32_bf16 v[16:19], v[208:211], v[128:131], v[16:19]
s_lshl4_add_u32 s92, s76, s3
s_lshl3_add_u32 s92, s92, s20
v_mfma_f32_16x16x32_bf16 v[48:51], v[208:211], v[132:135], v[48:51]
v_mfma_f32_16x16x32_bf16 v[80:83], v[208:211], v[136:139], v[80:83]
v_mfma_f32_16x16x32_bf16 v[112:115], v[208:211], v[140:143], v[112:115]
ds_read_b64_tr_b16 v[208:209], v241 offset:50688
ds_read_b64_tr_b16 v[210:211], v241 offset:51712
v_mfma_f32_16x16x32_bf16 v[20:23], v[212:215], v[128:131], v[20:23]
v_mfma_f32_16x16x32_bf16 v[52:55], v[212:215], v[132:135], v[52:55]
v_mfma_f32_16x16x32_bf16 v[84:87], v[212:215], v[136:139], v[84:87]
v_mfma_f32_16x16x32_bf16 v[116:119], v[212:215], v[140:143], v[116:119]
ds_read_b64_tr_b16 v[212:213], v241 offset:59136
ds_read_b64_tr_b16 v[214:215], v241 offset:60160
v_mfma_f32_16x16x32_bf16 v[24:27], v[216:219], v[128:131], v[24:27]
v_mfma_f32_16x16x32_bf16 v[56:59], v[216:219], v[132:135], v[56:59]
v_mfma_f32_16x16x32_bf16 v[88:91], v[216:219], v[136:139], v[88:91]
v_mfma_f32_16x16x32_bf16 v[120:123], v[216:219], v[140:143], v[120:123]
ds_read_b64_tr_b16 v[216:217], v242 offset:42240
ds_read_b64_tr_b16 v[218:219], v242 offset:43264
v_mfma_f32_16x16x32_bf16 v[28:31], v[220:223], v[128:131], v[28:31]
ds_write_b128 v238, a[32:35] offset:35840
v_mfma_f32_16x16x32_bf16 v[60:63], v[220:223], v[132:135], v[60:63]
buffer_store_dwordx4 v[248:251], v255, s[48:51], s89 offen offset:3072 sc1
v_mfma_f32_16x16x32_bf16 v[92:95], v[220:223], v[136:139], v[92:95]
s_waitcnt vmcnt(2) lgkmcnt(3)
v_mfma_f32_16x16x32_bf16 v[124:127], v[220:223], v[140:143], v[124:127]
ds_read_b64_tr_b16 v[220:221], v242 offset:50688
ds_read_b64_tr_b16 v[222:223], v242 offset:51712
v_mfma_f32_16x16x32_bf16 v[0:3], v[192:195], a[192:195], v[0:3]
s_mov_b64 s[48:49], s[86:87]
v_mfma_f32_16x16x32_bf16 v[32:35], v[192:195], a[196:199], v[32:35]
s_bitcmp1_b32 21, s73
s_cselect_b32 s50, 16384, 0
s_cselect_b32 s86, s88, 1073741824
v_mfma_f32_16x16x32_bf16 v[64:67], v[192:195], a[200:203], v[64:67]
s_lshl_b32 s89, s3, 14
v_add_u32 v248, s89, v255
v_mfma_f32_16x16x32_bf16 v[96:99], v[192:195], a[204:207], v[96:99]
ds_read_b64_tr_b16 v[192:193], v242 offset:59136
ds_read_b64_tr_b16 v[194:195], v242 offset:60160
v_mfma_f32_16x16x32_bf16 v[0:3], v[196:199], a[96:99], v[0:3]
ds_write_b128 v238, a[36:39] offset:36864
v_mfma_f32_16x16x32_bf16 v[32:35], v[196:199], a[100:103], v[32:35]
s_add_u32 s86, s86, 262144
v_mfma_f32_16x16x32_bf16 v[64:67], v[196:199], a[104:107], v[64:67]
s_waitcnt lgkmcnt(3)
v_mfma_f32_16x16x32_bf16 v[96:99], v[196:199], a[108:111], v[96:99]
ds_read_b64_tr_b16 v[196:197], v243 offset:42240
ds_read_b64_tr_b16 v[198:199], v243 offset:43264
v_mfma_f32_16x16x32_bf16 v[0:3], v[200:203], a[112:115], v[0:3]
s_cmp_eq_u32 s73, 0
s_cselect_b32 s86, s92, s86
s_cselect_b32 s92, 1073741824, s92
v_mfma_f32_16x16x32_bf16 v[32:35], v[200:203], a[116:119], v[32:35]
s_add_u32 s89, s89, 16384
s_lshl_b32 m0, s3, 12
v_mfma_f32_16x16x32_bf16 v[64:67], v[200:203], a[120:123], v[64:67]
s_add_u32 m0, m0, 131072
v_bfe_u32 v251, v255, 2, 5
v_mfma_f32_16x16x32_bf16 v[96:99], v[200:203], a[124:127], v[96:99]
ds_read_b64_tr_b16 v[200:201], v243 offset:50688
ds_read_b64_tr_b16 v[202:203], v243 offset:51712
v_mfma_f32_16x16x32_bf16 v[4:7], v[204:207], a[192:195], v[4:7]
ds_write_b128 v238, a[40:43] offset:37888
v_mfma_f32_16x16x32_bf16 v[36:39], v[204:207], a[196:199], v[36:39]
v_and_b32 v224, 0x380, v255
v_add_u32 v251, v251, v224
v_mfma_f32_16x16x32_bf16 v[68:71], v[204:207], a[200:203], v[68:71]
v_and_b32 v224, 16, v255
v_cmp_eq_u32 vcc, 0, v224
v_mfma_f32_16x16x32_bf16 v[100:103], v[204:207], a[204:207], v[100:103]
ds_read_b64_tr_b16 v[204:205], v243 offset:59136
ds_read_b64_tr_b16 v[206:207], v243 offset:60160
v_mfma_f32_16x16x32_bf16 v[4:7], v[208:211], a[96:99], v[4:7]
v_cndmask_b32 v249, v253, v252, vcc
v_mfma_f32_16x16x32_bf16 v[36:39], v[208:211], a[100:103], v[36:39]
v_mfma_f32_16x16x32_bf16 v[68:71], v[208:211], a[104:107], v[68:71]
v_mfma_f32_16x16x32_bf16 v[100:103], v[208:211], a[108:111], v[100:103]
ds_read_b64_tr_b16 v[208:209], v240 offset:42368
ds_read_b64_tr_b16 v[210:211], v240 offset:43392
v_mfma_f32_16x16x32_bf16 v[4:7], v[212:215], a[112:115], v[4:7]
ds_write_b128 v238, a[44:47] offset:38912
v_mfma_f32_16x16x32_bf16 v[36:39], v[212:215], a[116:119], v[36:39]
v_mfma_f32_16x16x32_bf16 v[68:71], v[212:215], a[120:123], v[68:71]
v_mfma_f32_16x16x32_bf16 v[100:103], v[212:215], a[124:127], v[100:103]
ds_read_b64_tr_b16 v[212:213], v240 offset:50816
ds_read_b64_tr_b16 v[214:215], v240 offset:51840
v_mfma_f32_16x16x32_bf16 v[8:11], v[216:219], a[192:195], v[8:11]
v_mfma_f32_16x16x32_bf16 v[40:43], v[216:219], a[196:199], v[40:43]
v_mfma_f32_16x16x32_bf16 v[72:75], v[216:219], a[200:203], v[72:75]
v_cvt_pk_bf16_f32 v128, v0, v1
v_cvt_pk_bf16_f32 v129, v2, v3
v_mfma_f32_16x16x32_bf16 v[104:107], v[216:219], a[204:207], v[104:107]
ds_read_b64_tr_b16 v[216:217], v240 offset:59264
ds_read_b64_tr_b16 v[218:219], v240 offset:60288
v_mfma_f32_16x16x32_bf16 v[8:11], v[220:223], a[96:99], v[8:11]
v_cvt_pk_bf16_f32 v130, v4, v5
v_cvt_pk_bf16_f32 v131, v6, v7
v_mfma_f32_16x16x32_bf16 v[40:43], v[220:223], a[100:103], v[40:43]
v_cvt_pk_bf16_f32 v144, v32, v33
v_cvt_pk_bf16_f32 v145, v34, v35
v_mfma_f32_16x16x32_bf16 v[72:75], v[220:223], a[104:107], v[72:75]
s_waitcnt lgkmcnt(2)
v_cvt_pk_bf16_f32 v146, v36, v37
v_mfma_f32_16x16x32_bf16 v[104:107], v[220:223], a[108:111], v[104:107]
ds_read_b64_tr_b16 v[220:221], v241 offset:42368
ds_read_b64_tr_b16 v[222:223], v241 offset:43392
v_mfma_f32_16x16x32_bf16 v[8:11], v[192:195], a[112:115], v[8:11]
v_cvt_pk_bf16_f32 v147, v38, v39
v_cvt_pk_bf16_f32 v160, v64, v65
v_mfma_f32_16x16x32_bf16 v[40:43], v[192:195], a[116:119], v[40:43]
v_cvt_pk_bf16_f32 v161, v66, v67
v_cvt_pk_bf16_f32 v162, v68, v69
v_mfma_f32_16x16x32_bf16 v[72:75], v[192:195], a[120:123], v[72:75]
v_cvt_pk_bf16_f32 v163, v70, v71
v_cvt_pk_bf16_f32 v176, v96, v97
v_mfma_f32_16x16x32_bf16 v[104:107], v[192:195], a[124:127], v[104:107]
ds_read_b64_tr_b16 v[192:193], v241 offset:50816
ds_read_b64_tr_b16 v[194:195], v241 offset:51840
v_mfma_f32_16x16x32_bf16 v[12:15], v[196:199], a[192:195], v[12:15]
v_cvt_pk_bf16_f32 v177, v98, v99
v_cvt_pk_bf16_f32 v178, v100, v101
v_mfma_f32_16x16x32_bf16 v[44:47], v[196:199], a[196:199], v[44:47]
v_cvt_pk_bf16_f32 v179, v102, v103
v_mfma_f32_16x16x32_bf16 v[76:79], v[196:199], a[200:203], v[76:79]
s_waitcnt lgkmcnt(2)
v_mfma_f32_16x16x32_bf16 v[108:111], v[196:199], a[204:207], v[108:111]
ds_read_b64_tr_b16 v[196:197], v241 offset:59264
ds_read_b64_tr_b16 v[198:199], v241 offset:60288
v_mfma_f32_16x16x32_bf16 v[12:15], v[200:203], a[96:99], v[12:15]
v_mfma_f32_16x16x32_bf16 v[44:47], v[200:203], a[100:103], v[44:47]
v_mfma_f32_16x16x32_bf16 v[76:79], v[200:203], a[104:107], v[76:79]
v_mfma_f32_16x16x32_bf16 v[108:111], v[200:203], a[108:111], v[108:111]
ds_read_b64_tr_b16 v[200:201], v242 offset:42368
ds_read_b64_tr_b16 v[202:203], v242 offset:43392
v_mfma_f32_16x16x32_bf16 v[12:15], v[204:207], a[112:115], v[12:15]
v_mfma_f32_16x16x32_bf16 v[44:47], v[204:207], a[116:119], v[44:47]
v_mfma_f32_16x16x32_bf16 v[76:79], v[204:207], a[120:123], v[76:79]
v_mfma_f32_16x16x32_bf16 v[108:111], v[204:207], a[124:127], v[108:111]
ds_read_b64_tr_b16 v[204:205], v242 offset:50816
ds_read_b64_tr_b16 v[206:207], v242 offset:51840
v_mfma_f32_16x16x32_bf16 v[16:19], v[208:211], a[192:195], v[16:19]
v_mfma_f32_16x16x32_bf16 v[48:51], v[208:211], a[196:199], v[48:51]
buffer_store_dwordx4 v[128:131], v255, s[48:51], 0 offen offset:0 sc1
v_mfma_f32_16x16x32_bf16 v[80:83], v[208:211], a[200:203], v[80:83]
v_cvt_pk_bf16_f32 v132, v8, v9
v_cvt_pk_bf16_f32 v133, v10, v11
v_mfma_f32_16x16x32_bf16 v[112:115], v[208:211], a[204:207], v[112:115]
ds_read_b64_tr_b16 v[208:209], v242 offset:59264
ds_read_b64_tr_b16 v[210:211], v242 offset:60288
v_mfma_f32_16x16x32_bf16 v[16:19], v[212:215], a[96:99], v[16:19]
v_cvt_pk_bf16_f32 v134, v12, v13
v_cvt_pk_bf16_f32 v135, v14, v15
v_mfma_f32_16x16x32_bf16 v[48:51], v[212:215], a[100:103], v[48:51]
buffer_store_dwordx4 v[144:147], v255, s[48:51], s95 offen offset:0 sc1
v_mfma_f32_16x16x32_bf16 v[80:83], v[212:215], a[104:107], v[80:83]
v_cvt_pk_bf16_f32 v148, v40, v41
v_cvt_pk_bf16_f32 v149, v42, v43
v_mfma_f32_16x16x32_bf16 v[112:115], v[212:215], a[108:111], v[112:115]
ds_read_b64_tr_b16 v[212:213], v243 offset:42368
ds_read_b64_tr_b16 v[214:215], v243 offset:43392
v_mfma_f32_16x16x32_bf16 v[16:19], v[216:219], a[112:115], v[16:19]
v_cvt_pk_bf16_f32 v150, v44, v45
v_cvt_pk_bf16_f32 v151, v46, v47
v_mfma_f32_16x16x32_bf16 v[48:51], v[216:219], a[116:119], v[48:51]
buffer_store_dwordx4 v[160:163], v255, s[48:51], s96 offen offset:0 sc1
v_mfma_f32_16x16x32_bf16 v[80:83], v[216:219], a[120:123], v[80:83]
v_cvt_pk_bf16_f32 v164, v72, v73
v_cvt_pk_bf16_f32 v165, v74, v75
v_mfma_f32_16x16x32_bf16 v[112:115], v[216:219], a[124:127], v[112:115]
ds_read_b64_tr_b16 v[216:217], v243 offset:50816
ds_read_b64_tr_b16 v[218:219], v243 offset:51840
v_mfma_f32_16x16x32_bf16 v[20:23], v[220:223], a[192:195], v[20:23]
v_cvt_pk_bf16_f32 v166, v76, v77
v_cvt_pk_bf16_f32 v167, v78, v79
v_mfma_f32_16x16x32_bf16 v[52:55], v[220:223], a[196:199], v[52:55]
buffer_store_dwordx4 v[176:179], v255, s[48:51], s97 offen offset:0 sc1
v_mfma_f32_16x16x32_bf16 v[84:87], v[220:223], a[200:203], v[84:87]
s_waitcnt lgkmcnt(2)
v_cvt_pk_bf16_f32 v180, v104, v105
v_mfma_f32_16x16x32_bf16 v[116:119], v[220:223], a[204:207], v[116:119]
ds_read_b64_tr_b16 v[220:221], v243 offset:59264
ds_read_b64_tr_b16 v[222:223], v243 offset:60288
v_mfma_f32_16x16x32_bf16 v[20:23], v[192:195], a[96:99], v[20:23]
v_cvt_pk_bf16_f32 v181, v106, v107
v_cvt_pk_bf16_f32 v182, v108, v109
v_mfma_f32_16x16x32_bf16 v[52:55], v[192:195], a[100:103], v[52:55]
v_cvt_pk_bf16_f32 v183, v110, v111
v_accvgpr_read_b32 v32, a236
v_mfma_f32_16x16x32_bf16 v[84:87], v[192:195], a[104:107], v[84:87]
v_accvgpr_read_b32 v33, a237
v_accvgpr_read_b32 v34, a238
v_mfma_f32_16x16x32_bf16 v[116:119], v[192:195], a[108:111], v[116:119]
v_accvgpr_read_b32 v35, a239
v_mov_b32 v36, 0xff800000
v_mfma_f32_16x16x32_bf16 v[20:23], v[196:199], a[112:115], v[20:23]
s_waitcnt vmcnt(4) lgkmcnt(0)
s_barrier
v_mfma_f32_16x16x32_bf16 v[52:55], v[196:199], a[116:119], v[52:55]
buffer_store_dwordx2 v[252:253], off, s[64:67], s92 sc1
v_mfma_f32_16x16x32_bf16 v[84:87], v[196:199], a[120:123], v[84:87]
v_mov_b32 v37, 0xff800000
v_mov_b32 v38, 0xff800000
v_mfma_f32_16x16x32_bf16 v[116:119], v[196:199], a[124:127], v[116:119]
ds_read_b128 a[64:67], v248 offset:0
ds_read_b128 a[68:71], v248 offset:1024
v_mfma_f32_16x16x32_bf16 v[24:27], v[200:203], a[192:195], v[24:27]
v_mov_b32 v39, 0xff800000
v_mul_u32_u24 v241, 0x5555, v224
v_mfma_f32_16x16x32_bf16 v[56:59], v[200:203], a[196:199], v[56:59]
buffer_store_dwordx4 v[132:135], v255, s[48:51], 0 offen offset:1024 sc1
v_mfma_f32_16x16x32_bf16 v[88:91], v[200:203], a[200:203], v[88:91]
v_cvt_pk_bf16_f32 v136, v16, v17
v_cvt_pk_bf16_f32 v137, v18, v19
v_mfma_f32_16x16x32_bf16 v[120:123], v[200:203], a[204:207], v[120:123]
ds_read_b128 a[72:75], v248 offset:2048
ds_read_b128 a[76:79], v248 offset:3072
v_mfma_f32_16x16x32_bf16 v[24:27], v[204:207], a[96:99], v[24:27]
v_cvt_pk_bf16_f32 v138, v20, v21
v_cvt_pk_bf16_f32 v139, v22, v23
v_mfma_f32_16x16x32_bf16 v[56:59], v[204:207], a[100:103], v[56:59]
buffer_store_dwordx4 v[148:151], v255, s[48:51], s95 offen offset:1024 sc1
v_mfma_f32_16x16x32_bf16 v[88:91], v[204:207], a[104:107], v[88:91]
v_cvt_pk_bf16_f32 v152, v48, v49
v_cvt_pk_bf16_f32 v153, v50, v51
v_mfma_f32_16x16x32_bf16 v[120:123], v[204:207], a[108:111], v[120:123]
ds_read_b128 a[80:83], v248 offset:4096
ds_read_b128 a[84:87], v248 offset:5120
v_mfma_f32_16x16x32_bf16 v[24:27], v[208:211], a[112:115], v[24:27]
v_cvt_pk_bf16_f32 v154, v52, v53
v_cvt_pk_bf16_f32 v155, v54, v55
v_mfma_f32_16x16x32_bf16 v[56:59], v[208:211], a[116:119], v[56:59]
buffer_store_dwordx4 v[164:167], v255, s[48:51], s96 offen offset:1024 sc1
v_mfma_f32_16x16x32_bf16 v[88:91], v[208:211], a[120:123], v[88:91]
v_cvt_pk_bf16_f32 v168, v80, v81
v_cvt_pk_bf16_f32 v169, v82, v83
v_mfma_f32_16x16x32_bf16 v[120:123], v[208:211], a[124:127], v[120:123]
ds_read_b128 a[88:91], v248 offset:6144
ds_read_b128 a[92:95], v248 offset:7168
v_mfma_f32_16x16x32_bf16 v[28:31], v[212:215], a[192:195], v[28:31]
v_cvt_pk_bf16_f32 v170, v84, v85
v_cvt_pk_bf16_f32 v171, v86, v87
v_mfma_f32_16x16x32_bf16 v[60:63], v[212:215], a[196:199], v[60:63]
buffer_store_dwordx4 v[180:183], v255, s[48:51], s97 offen offset:1024 sc1
v_mfma_f32_16x16x32_bf16 v[92:95], v[212:215], a[200:203], v[92:95]
v_cvt_pk_bf16_f32 v184, v112, v113
v_cvt_pk_bf16_f32 v185, v114, v115
v_mfma_f32_16x16x32_bf16 v[124:127], v[212:215], a[204:207], v[124:127]
v_cvt_pk_bf16_f32 v186, v116, v117
v_cvt_pk_bf16_f32 v187, v118, v119
v_mfma_f32_16x16x32_bf16 v[28:31], v[216:219], a[96:99], v[28:31]
ds_write_b128 v238, a[48:51] offset:39936
v_mfma_f32_16x16x32_bf16 v[60:63], v[216:219], a[100:103], v[60:63]
v_lshrrev_b32 v241, 4, v241
v_add_u32 v241, 0x8888, v241
v_mfma_f32_16x16x32_bf16 v[92:95], v[216:219], a[104:107], v[92:95]
v_mfma_f32_16x16x32_bf16 v[124:127], v[216:219], a[108:111], v[124:127]
v_mfma_f32_16x16x32_bf16 v[28:31], v[220:223], a[112:115], v[28:31]
v_mfma_f32_16x16x32_bf16 v[60:63], v[220:223], a[116:119], v[60:63]
v_mfma_f32_16x16x32_bf16 v[92:95], v[220:223], a[120:123], v[92:95]
v_mfma_f32_16x16x32_bf16 v[124:127], v[220:223], a[124:127], v[124:127]
s_waitcnt lgkmcnt(1)
v_mfma_f32_16x16x32_bf16 v[208:211], a[64:67], v[160:163], v[32:35]
v_mfma_f32_16x16x32_bf16 v[216:219], a[64:67], v[176:179], 0
buffer_store_dwordx4 v[136:139], v255, s[48:51], 0 offen offset:2048 sc1
v_mfma_f32_16x16x32_bf16 v[212:215], a[80:83], v[160:163], v[36:39]
v_cvt_pk_bf16_f32 v172, v88, v89
v_cvt_pk_bf16_f32 v173, v90, v91
v_cvt_pk_bf16_f32 v174, v92, v93
v_mfma_f32_16x16x32_bf16 v[220:223], a[80:83], v[176:179], v[32:35]
v_cvt_pk_bf16_f32 v175, v94, v95
v_cvt_pk_bf16_f32 v188, v120, v121
v_cvt_pk_bf16_f32 v189, v122, v123
v_mfma_f32_16x16x32_bf16 v[208:211], a[68:71], v[164:167], v[208:211]
v_cvt_pk_bf16_f32 v190, v124, v125
v_cvt_pk_bf16_f32 v191, v126, v127
v_cvt_pk_bf16_f32 v140, v24, v25
v_mfma_f32_16x16x32_bf16 v[216:219], a[68:71], v[180:183], v[216:219]
buffer_store_dwordx4 v[152:155], v255, s[48:51], s95 offen offset:2048 sc1
v_cvt_pk_bf16_f32 v141, v26, v27
v_mfma_f32_16x16x32_bf16 v[212:215], a[84:87], v[164:167], v[212:215]
v_cvt_pk_bf16_f32 v142, v28, v29
v_cvt_pk_bf16_f32 v143, v30, v31
v_cvt_pk_bf16_f32 v156, v56, v57
v_mfma_f32_16x16x32_bf16 v[220:223], a[84:87], v[180:183], v[220:223]
v_cvt_pk_bf16_f32 v157, v58, v59
v_cvt_pk_bf16_f32 v158, v60, v61
v_cvt_pk_bf16_f32 v159, v62, v63
v_mfma_f32_16x16x32_bf16 v[208:211], a[72:75], v[168:171], v[208:211]
v_mfma_f32_16x16x32_bf16 v[216:219], a[72:75], v[184:187], v[216:219]
buffer_store_dwordx4 v[168:171], v255, s[48:51], s96 offen offset:2048 sc1
v_mfma_f32_16x16x32_bf16 v[212:215], a[88:91], v[168:171], v[212:215]
v_mfma_f32_16x16x32_bf16 v[220:223], a[88:91], v[184:187], v[220:223]
v_mfma_f32_16x16x32_bf16 v[208:211], a[76:79], v[172:175], v[208:211]
v_mfma_f32_16x16x32_bf16 v[216:219], a[76:79], v[188:191], v[216:219]
buffer_store_dwordx4 v[184:187], v255, s[48:51], s97 offen offset:2048 sc1
v_mfma_f32_16x16x32_bf16 v[212:215], a[92:95], v[172:175], v[212:215]
ds_read_b128 a[64:67], v248 offset:16384
v_mfma_f32_16x16x32_bf16 v[220:223], a[92:95], v[188:191], v[220:223]
ds_read_b128 a[96:99], v248 offset:8192
v_exp_f32 v0, v208
v_mfma_f32_16x16x32_bf16 v[64:67], a[232:235], a[232:235], 0
ds_read_b128 a[68:71], v248 offset:17408
v_exp_f32 v1, v209
v_mfma_f32_16x16x32_bf16 v[68:71], a[232:235], a[232:235], 0
ds_read_b128 a[100:103], v248 offset:9216
buffer_store_dwordx4 v[140:143], v255, s[48:51], 0 offen offset:3072 sc1
.La5_LW1_11:
s_cbranch_execz .La5_LW1_R0
v_mfma_f32_16x16x32_bf16 v[72:75], a[232:235], a[232:235], 0
ds_read_b128 a[72:75], v248 offset:18432
v_exp_f32 v2, v210
v_mfma_f32_16x16x32_bf16 v[76:79], a[232:235], a[232:235], 0
ds_read_b128 a[104:107], v248 offset:10240
v_exp_f32 v3, v211
v_mfma_f32_16x16x32_bf16 v[80:83], a[232:235], a[232:235], 0
ds_read_b128 a[76:79], v248 offset:19456
v_exp_f32 v8, v216
v_mfma_f32_16x16x32_bf16 v[84:87], a[232:235], a[232:235], 0
ds_read_b128 a[108:111], v248 offset:11264
buffer_store_dwordx4 v[156:159], v255, s[48:51], s95 offen offset:3072 sc1
v_mfma_f32_16x16x32_bf16 v[88:91], a[232:235], a[232:235], 0
ds_read_b128 a[80:83], v248 offset:20480
v_exp_f32 v9, v217
v_mfma_f32_16x16x32_bf16 v[92:95], a[232:235], a[232:235], 0
ds_read_b128 a[112:115], v248 offset:12288
v_exp_f32 v10, v218
v_mfma_f32_16x16x32_bf16 v[96:99], a[232:235], a[232:235], 0
ds_read_b128 a[84:87], v248 offset:21504
v_exp_f32 v11, v219
v_mfma_f32_16x16x32_bf16 v[100:103], a[232:235], a[232:235], 0
ds_read_b128 a[116:119], v248 offset:13312
buffer_store_dwordx4 v[172:175], v255, s[48:51], s96 offen offset:3072 sc1
v_mfma_f32_16x16x32_bf16 v[104:107], a[232:235], a[232:235], 0
ds_read_b128 a[88:91], v248 offset:22528
v_exp_f32 v4, v212
v_mfma_f32_16x16x32_bf16 v[108:111], a[232:235], a[232:235], 0
ds_read_b128 a[120:123], v248 offset:14336
v_exp_f32 v5, v213
v_mfma_f32_16x16x32_bf16 v[112:115], a[232:235], a[232:235], 0
ds_read_b128 a[92:95], v248 offset:23552
v_exp_f32 v6, v214
v_mfma_f32_16x16x32_bf16 v[116:119], a[232:235], a[232:235], 0
ds_read_b128 a[124:127], v248 offset:15360
buffer_store_dwordx4 v[188:191], v255, s[48:51], s97 offen offset:3072 sc1
v_exp_f32 v7, v215
v_mfma_f32_16x16x32_bf16 v[120:123], a[232:235], a[232:235], 0
v_exp_f32 v12, v220
v_mfma_f32_16x16x32_bf16 v[124:127], a[232:235], a[232:235], 0
v_accvgpr_write_b32 a192, 0
v_accvgpr_write_b32 a193, 0
v_accvgpr_write_b32 a194, 0
v_accvgpr_write_b32 a195, 0
v_accvgpr_write_b32 a196, 0
v_accvgpr_write_b32 a197, 0
v_accvgpr_write_b32 a198, 0
v_accvgpr_write_b32 a199, 0
v_add_u32 v248, 0x4000, v248
s_cmp_eq_u32 s3, 3
s_cbranch_scc1 .La5_LA_FH
.p2align 6
; ---- attention, 32 keys per window: S = K Q^T (causal mask through SrcC), P = exp2(S), O += V^T P, row sums l += 1 P; here the later 32 rows only
.La5_LA_HH:
s_waitcnt lgkmcnt(1)
v_mfma_f32_16x16x32_bf16 v[208:211], a[64:67], v[160:163], 0
v_exp_f32 v13, v221
v_mfma_f32_16x16x32_bf16 v[208:211], a[68:71], v[164:167], v[208:211]
v_cvt_pk_bf16_f32 v232, v0, v1
v_cvt_pk_bf16_f32 v233, v2, v3
v_mfma_f32_16x16x32_bf16 v[208:211], a[72:75], v[168:171], v[208:211]
v_exp_f32 v14, v222
v_mfma_f32_16x16x32_bf16 v[208:211], a[76:79], v[172:175], v[208:211]
v_cvt_pk_bf16_f32 v236, v8, v9
v_cvt_pk_bf16_f32 v237, v10, v11
v_mfma_f32_16x16x32_bf16 v[216:219], a[64:67], v[176:179], 0
v_exp_f32 v15, v223
v_mfma_f32_16x16x32_bf16 v[216:219], a[68:71], v[180:183], v[216:219]
v_cvt_pk_bf16_f32 v234, v4, v5
v_cvt_pk_bf16_f32 v235, v6, v7
v_mfma_f32_16x16x32_bf16 v[216:219], a[72:75], v[184:187], v[216:219]
v_exp_f32 v0, v208
v_mfma_f32_16x16x32_bf16 v[216:219], a[76:79], v[188:191], v[216:219]
v_cvt_pk_bf16_f32 v238, v12, v13
v_cvt_pk_bf16_f32 v239, v14, v15
v_mfma_f32_16x16x32_bf16 v[212:215], a[80:83], v[160:163], 0
v_exp_f32 v1, v209
v_mfma_f32_16x16x32_bf16 v[212:215], a[84:87], v[164:167], v[212:215]
ds_read_b128 a[64:67], v248 offset:16384
ds_read_b128 a[68:71], v248 offset:17408
v_mfma_f32_16x16x32_bf16 v[212:215], a[88:91], v[168:171], v[212:215]
v_exp_f32 v2, v210
v_mfma_f32_16x16x32_bf16 v[212:215], a[92:95], v[172:175], v[212:215]
ds_read_b128 a[72:75], v248 offset:18432
ds_read_b128 a[76:79], v248 offset:19456
v_mfma_f32_16x16x32_bf16 v[220:223], a[80:83], v[176:179], 0
v_exp_f32 v3, v211
v_mfma_f32_16x16x32_bf16 v[220:223], a[84:87], v[180:183], v[220:223]
ds_read_b128 a[80:83], v248 offset:20480
v_mfma_f32_16x16x32_bf16 v[220:223], a[88:91], v[184:187], v[220:223]
v_exp_f32 v8, v216
v_mfma_f32_16x16x32_bf16 v[220:223], a[92:95], v[188:191], v[220:223]
s_waitcnt lgkmcnt(5)
ds_read_b128 a[84:87], v248 offset:21504
v_mfma_f32_16x16x32_bf16 v[64:67], a[96:99], v[232:235], v[64:67]
v_exp_f32 v9, v217
v_mfma_f32_16x16x32_bf16 v[96:99], a[96:99], v[236:239], v[96:99]
ds_read_b128 a[96:99], v248 offset:8192
ds_read_b128 a[88:91], v248 offset:22528
v_mfma_f32_16x16x32_bf16 v[68:71], a[100:103], v[232:235], v[68:71]
v_exp_f32 v10, v218
v_mfma_f32_16x16x32_bf16 v[100:103], a[100:103], v[236:239], v[100:103]
ds_read_b128 a[100:103], v248 offset:9216
ds_read_b128 a[92:95], v248 offset:23552
v_mfma_f32_16x16x32_bf16 v[72:75], a[104:107], v[232:235], v[72:75]
v_exp_f32 v11, v219
v_mfma_f32_16x16x32_bf16 v[104:107], a[104:107], v[236:239], v[104:107]
ds_read_b128 a[104:107], v248 offset:10240
v_mfma_f32_16x16x32_bf16 v[76:79], a[108:111], v[232:235], v[76:79]
v_exp_f32 v4, v212
v_mfma_f32_16x16x32_bf16 v[108:111], a[108:111], v[236:239], v[108:111]
ds_read_b128 a[108:111], v248 offset:11264
s_add_u32 s85, s85, 1
v_mfma_f32_16x16x32_bf16 v[80:83], a[112:115], v[232:235], v[80:83]
v_exp_f32 v5, v213
v_mfma_f32_16x16x32_bf16 v[112:115], a[112:115], v[236:239], v[112:115]
ds_read_b128 a[112:115], v248 offset:12288
s_add_u32 s89, s89, 32768
v_mfma_f32_16x16x32_bf16 v[84:87], a[116:119], v[232:235], v[84:87]
v_exp_f32 v6, v214
v_mfma_f32_16x16x32_bf16 v[116:119], a[116:119], v[236:239], v[116:119]
ds_read_b128 a[116:119], v248 offset:13312
s_add_u32 s92, s85, s3
v_mfma_f32_16x16x32_bf16 v[88:91], a[120:123], v[232:235], v[88:91]
v_exp_f32 v7, v215
v_mfma_f32_16x16x32_bf16 v[120:123], a[120:123], v[236:239], v[120:123]
ds_read_b128 a[120:123], v248 offset:14336
buffer_load_dword v250, v251, s[64:67], s20 offen sc1
v_mfma_f32_16x16x32_bf16 v[92:95], a[124:127], v[232:235], v[92:95]
v_exp_f32 v12, v220
v_mfma_f32_16x16x32_bf16 v[124:127], a[124:127], v[236:239], v[124:127]
ds_read_b128 a[124:127], v248 offset:15360
s_barrier
v_smfmac_f32_16x16x64_bf16 a[196:199], a[208:211], v[232:239], v241
s_waitcnt lgkmcnt(6)
v_mfma_f32_16x16x32_bf16 v[208:211], a[64:67], v[160:163], 0
v_exp_f32 v13, v221
v_mfma_f32_16x16x32_bf16 v[208:211], a[68:71], v[164:167], v[208:211]
v_cvt_pk_bf16_f32 v232, v0, v1
v_cvt_pk_bf16_f32 v233, v2, v3
v_mfma_f32_16x16x32_bf16 v[208:211], a[72:75], v[168:171], v[208:211]
v_exp_f32 v14, v222
v_mfma_f32_16x16x32_bf16 v[208:211], a[76:79], v[172:175], v[208:211]
v_cvt_pk_bf16_f32 v236, v8, v9
v_cvt_pk_bf16_f32 v237, v10, v11
v_mfma_f32_16x16x32_bf16 v[216:219], a[64:67], v[176:179], 0
v_exp_f32 v15, v223
v_mfma_f32_16x16x32_bf16 v[216:219], a[68:71], v[180:183], v[216:219]
v_cvt_pk_bf16_f32 v234, v4, v5
v_cvt_pk_bf16_f32 v235, v6, v7
v_mfma_f32_16x16x32_bf16 v[216:219], a[72:75], v[184:187], v[216:219]
v_exp_f32 v0, v208
v_mfma_f32_16x16x32_bf16 v[216:219], a[76:79], v[188:191], v[216:219]
v_cvt_pk_bf16_f32 v238, v12, v13
v_cvt_pk_bf16_f32 v239, v14, v15
v_mfma_f32_16x16x32_bf16 v[212:215], a[80:83], v[160:163], 0
v_exp_f32 v1, v209
v_mfma_f32_16x16x32_bf16 v[212:215], a[84:87], v[164:167], v[212:215]
ds_read_b128 a[64:67], v248 offset:32768
ds_read_b128 a[68:71], v248 offset:33792
v_mfma_f32_16x16x32_bf16 v[212:215], a[88:91], v[168:171], v[212:215]
v_exp_f32 v2, v210
v_mfma_f32_16x16x32_bf16 v[212:215], a[92:95], v[172:175], v[212:215]
ds_read_b128 a[72:75], v248 offset:34816
ds_read_b128 a[76:79], v248 offset:35840
v_mfma_f32_16x16x32_bf16 v[220:223], a[80:83], v[176:179], 0
v_exp_f32 v3, v211
v_mfma_f32_16x16x32_bf16 v[220:223], a[84:87], v[180:183], v[220:223]
ds_read_b128 a[80:83], v248 offset:36864
s_bitcmp1_b64 s[98:99], s85
v_mfma_f32_16x16x32_bf16 v[220:223], a[88:91], v[184:187], v[220:223]
v_exp_f32 v8, v216
v_mfma_f32_16x16x32_bf16 v[220:223], a[92:95], v[188:191], v[220:223]
s_waitcnt lgkmcnt(5)
ds_read_b128 a[84:87], v248 offset:37888
v_mfma_f32_16x16x32_bf16 v[64:67], a[96:99], v[232:235], v[64:67]
v_exp_f32 v9, v217
v_mfma_f32_16x16x32_bf16 v[96:99], a[96:99], v[236:239], v[96:99]
ds_read_b128 a[96:99], v248 offset:24576
ds_read_b128 a[88:91], v248 offset:38912
v_mfma_f32_16x16x32_bf16 v[68:71], a[100:103], v[232:235], v[68:71]
v_exp_f32 v10, v218
v_mfma_f32_16x16x32_bf16 v[100:103], a[100:103], v[236:239], v[100:103]
ds_read_b128 a[100:103], v248 offset:25600
ds_read_b128 a[92:95], v248 offset:39936
v_mfma_f32_16x16x32_bf16 v[72:75], a[104:107], v[232:235], v[72:75]
v_exp_f32 v11, v219
v_mfma_f32_16x16x32_bf16 v[104:107], a[104:107], v[236:239], v[104:107]
ds_read_b128 a[104:107], v248 offset:26624
v_mfma_f32_16x16x32_bf16 v[76:79], a[108:111], v[232:235], v[76:79]
v_exp_f32 v4, v212
v_mfma_f32_16x16x32_bf16 v[108:111], a[108:111], v[236:239], v[108:111]
ds_read_b128 a[108:111], v248 offset:27648
v_mfma_f32_16x16x32_bf16 v[80:83], a[112:115], v[232:235], v[80:83]
v_exp_f32 v5, v213
v_mfma_f32_16x16x32_bf16 v[112:115], a[112:115], v[236:239], v[112:115]
ds_read_b128 a[112:115], v248 offset:28672
v_mfma_f32_16x16x32_bf16 v[84:87], a[116:119], v[232:235], v[84:87]
v_exp_f32 v6, v214
v_mfma_f32_16x16x32_bf16 v[116:119], a[116:119], v[236:239], v[116:119]
ds_read_b128 a[116:119], v248 offset:29696
s_waitcnt vmcnt(1)
buffer_store_dwordx2 v[252:253], off, s[64:67], s86 sc1
s_mov_b32 s86, 1073741824
v_mfma_f32_16x16x32_bf16 v[88:91], a[120:123], v[232:235], v[88:91]
v_exp_f32_e64 v7, v215
v_mfma_f32_16x16x32_bf16 v[120:123], a[120:123], v[236:239], v[120:123]
ds_read_b128 a[120:123], v248 offset:30720
v_mfma_f32_16x16x32_bf16 v[92:95], a[124:127], v[232:235], v[92:95]
v_exp_f32_e64 v12, v220
v_mfma_f32_16x16x32_bf16 v[124:127], a[124:127], v[236:239], v[124:127]
ds_read_b128 a[124:127], v248 offset:31744
v_smfmac_f32_16x16x64_bf16 a[196:199], a[208:211], v[232:239], v241
v_add_u32 v248, 0x8000, v248
s_cbranch_scc0 .La5_LA_HH1
s_call_b64 s[24:25], .La5_LA_EVENT
s_add_u32 s92, s85, s3
.La5_LA_HH1:
s_cmp_lt_u32 s92, 3
s_cbranch_scc1 .La5_LA_HH
; ---- first window with both row groups (diagonal of the earlier group)
.La5_LA_FH:
.La5_LW2_0:
s_cbranch_execz .La5_LW2_4
s_waitcnt lgkmcnt(9)
v_mfma_f32_16x16x32_bf16 v[192:195], a[64:67], v[128:131], v[32:35]
v_exp_f32 v13, v221
v_mfma_f32_16x16x32_bf16 v[192:195], a[68:71], v[132:135], v[192:195]
v_cvt_pk_bf16_f32 v232, v0, v1
v_cvt_pk_bf16_f32 v233, v2, v3
v_mfma_f32_16x16x32_bf16 v[192:195], a[72:75], v[136:139], v[192:195]
v_exp_f32 v14, v222
v_mfma_f32_16x16x32_bf16 v[192:195], a[76:79], v[140:143], v[192:195]
buffer_load_dwordx4 v255, s[60:63], s17 offen offset:0 sc1 lds
v_mfma_f32_16x16x32_bf16 v[200:203], a[64:67], v[144:147], 0
v_exp_f32 v15, v223
v_mfma_f32_16x16x32_bf16 v[200:203], a[68:71], v[148:151], v[200:203]
v_cvt_pk_bf16_f32 v236, v8, v9
v_cvt_pk_bf16_f32 v237, v10, v11
v_mfma_f32_16x16x32_bf16 v[200:203], a[72:75], v[152:155], v[200:203]
v_exp_f32 v192, v192
v_mfma_f32_16x16x32_bf16 v[200:203], a[76:79], v[156:159], v[200:203]
s_waitcnt lgkmcnt(1)
v_cvt_pk_bf16_f32 v234, v4, v5
v_mfma_f32_16x16x32_bf16 v[196:199], a[80:83], v[128:131], v[36:39]
v_exp_f32 v193, v193
v_mfma_f32_16x16x32_bf16 v[196:199], a[84:87], v[132:135], v[196:199]
v_cvt_pk_bf16_f32 v224, v192, v193
s_add_u32 s85, s85, 1
v_mfma_f32_16x16x32_bf16 v[196:199], a[88:91], v[136:139], v[196:199]
v_exp_f32 v194, v194
v_mfma_f32_16x16x32_bf16 v[196:199], a[92:95], v[140:143], v[196:199]
buffer_load_dwordx4 v255, s[60:63], s17 offen offset:1024 sc1 lds
v_mfma_f32_16x16x32_bf16 v[204:207], a[80:83], v[144:147], v[32:35]
v_exp_f32 v195, v195
v_mfma_f32_16x16x32_bf16 v[204:207], a[84:87], v[148:151], v[204:207]
v_cvt_pk_bf16_f32 v225, v194, v195
s_add_u32 s89, s89, 16384
v_mfma_f32_16x16x32_bf16 v[204:207], a[88:91], v[152:155], v[204:207]
v_exp_f32 v200, v200
v_mfma_f32_16x16x32_bf16 v[204:207], a[92:95], v[156:159], v[204:207]
buffer_load_dwordx4 v255, s[60:63], s17 offen offset:2048 sc1 lds
v_mfma_f32_16x16x32_bf16 v[208:211], a[64:67], v[160:163], 0
v_exp_f32 v201, v201
v_mfma_f32_16x16x32_bf16 v[208:211], a[68:71], v[164:167], v[208:211]
v_cvt_pk_bf16_f32 v228, v200, v201
s_and_b32 s89, s89, 0x1ffff
v_mfma_f32_16x16x32_bf16 v[208:211], a[72:75], v[168:171], v[208:211]
v_exp_f32 v202, v202
v_mfma_f32_16x16x32_bf16 v[208:211], a[76:79], v[172:175], v[208:211]
buffer_load_dwordx4 v255, s[60:63], s17 offen offset:3072 sc1 lds
v_mfma_f32_16x16x32_bf16 v[216:219], a[64:67], v[176:179], 0
v_exp_f32 v203, v203
v_mfma_f32_16x16x32_bf16 v[216:219], a[68:71], v[180:183], v[216:219]
v_cvt_pk_bf16_f32 v229, v202, v203
v_mfma_f32_16x16x32_bf16 v[216:219], a[72:75], v[184:187], v[216:219]
v_exp_f32 v196, v196
v_mfma_f32_16x16x32_bf16 v[216:219], a[76:79], v[188:191], v[216:219]
v_cvt_pk_bf16_f32 v235, v6, v7
v_cvt_pk_bf16_f32 v238, v12, v13
v_mfma_f32_16x16x32_bf16 v[212:215], a[80:83], v[160:163], 0
v_exp_f32 v197, v197
s_add_u32 s17, s17, 8192
v_mfma_f32_16x16x32_bf16 v[212:215], a[84:87], v[164:167], v[212:215]
v_cvt_pk_bf16_f32 v226, v196, v197
s_add_u32 m0, m0, 16384
s_and_b32 m0, m0, s78
v_mfma_f32_16x16x32_bf16 v[212:215], a[88:91], v[168:171], v[212:215]
v_exp_f32 v198, v198
v_mfma_f32_16x16x32_bf16 v[212:215], a[92:95], v[172:175], v[212:215]
v_cvt_pk_bf16_f32 v239, v14, v15
s_waitcnt lgkmcnt(0)
v_mfma_f32_16x16x32_bf16 v[220:223], a[80:83], v[176:179], 0
v_exp_f32 v199, v199
v_mfma_f32_16x16x32_bf16 v[220:223], a[84:87], v[180:183], v[220:223]
v_cvt_pk_bf16_f32 v227, v198, v199
v_mfma_f32_16x16x32_bf16 v[220:223], a[88:91], v[184:187], v[220:223]
v_exp_f32 v204, v204
v_mfma_f32_16x16x32_bf16 v[220:223], a[92:95], v[188:191], v[220:223]
s_waitcnt vmcnt(11)
s_barrier
v_mfma_f32_16x16x32_bf16 v[64:67], a[96:99], v[232:235], v[64:67]
v_exp_f32 v205, v205
v_mfma_f32_16x16x32_bf16 v[96:99], a[96:99], v[236:239], v[96:99]
ds_read_b128 a[96:99], v248 offset:8192
v_cvt_pk_bf16_f32 v230, v204, v205
v_mfma_f32_16x16x32_bf16 v[68:71], a[100:103], v[232:235], v[68:71]
v_exp_f32 v206, v206
v_mfma_f32_16x16x32_bf16 v[100:103], a[100:103], v[236:239], v[100:103]
ds_read_b128 a[100:103], v248 offset:9216
buffer_load_dword v250, v251, s[64:67], s20 offen sc1
v_mfma_f32_16x16x32_bf16 v[72:75], a[104:107], v[232:235], v[72:75]
v_exp_f32 v207, v207
v_mfma_f32_16x16x32_bf16 v[104:107], a[104:107], v[236:239], v[104:107]
ds_read_b128 a[104:107], v248 offset:10240
v_cvt_pk_bf16_f32 v231, v206, v207
v_mfma_f32_16x16x32_bf16 v[76:79], a[108:111], v[232:235], v[76:79]
v_exp_f32 v208, v208
v_mfma_f32_16x16x32_bf16 v[108:111], a[108:111], v[236:239], v[108:111]
ds_read_b128 a[108:111], v248 offset:11264
v_mfma_f32_16x16x32_bf16 v[80:83], a[112:115], v[232:235], v[80:83]
v_exp_f32 v209, v209
v_mfma_f32_16x16x32_bf16 v[112:115], a[112:115], v[236:239], v[112:115]
ds_read_b128 a[112:115], v248 offset:12288
v_mfma_f32_16x16x32_bf16 v[84:87], a[116:119], v[232:235], v[84:87]
v_exp_f32 v210, v210
v_mfma_f32_16x16x32_bf16 v[116:119], a[116:119], v[236:239], v[116:119]
ds_read_b128 a[116:119], v248 offset:13312
v_mfma_f32_16x16x32_bf16 v[88:91], a[120:123], v[232:235], v[88:91]
v_exp_f32 v211, v211
v_mfma_f32_16x16x32_bf16 v[120:123], a[120:123], v[236:239], v[120:123]
ds_read_b128 a[120:123], v248 offset:14336
s_bitcmp1_b64 s[98:99], s85
v_mfma_f32_16x16x32_bf16 v[92:95], a[124:127], v[232:235], v[92:95]
v_exp_f32 v216, v216
v_mfma_f32_16x16x32_bf16 v[124:127], a[124:127], v[236:239], v[124:127]
ds_read_b128 a[124:127], v248 offset:15360
v_or_b32 v248, s89, v255
v_smfmac_f32_16x16x64_bf16 a[196:199], a[208:211], v[232:239], v241
s_waitcnt lgkmcnt(4)
v_smfmac_f32_16x16x64_bf16 a[192:195], a[208:211], v[224:231], v241
ds_read_b128 a[64:67], v248 offset:0
v_mfma_f32_16x16x32_bf16 v[0:3], a[96:99], v[224:227], 0
v_exp_f32 v217, v217
v_mfma_f32_16x16x32_bf16 v[32:35], a[96:99], v[228:231], 0
ds_read_b128 a[68:71], v248 offset:1024
v_cvt_pk_bf16_f32 v232, v208, v209
v_mfma_f32_16x16x32_bf16 v[4:7], a[100:103], v[224:227], 0
v_exp_f32 v218, v218
v_mfma_f32_16x16x32_bf16 v[36:39], a[100:103], v[228:231], 0
ds_read_b128 a[72:75], v248 offset:2048
v_cvt_pk_bf16_f32 v233, v210, v211
v_mfma_f32_16x16x32_bf16 v[8:11], a[104:107], v[224:227], 0
v_exp_f32 v219, v219
v_mfma_f32_16x16x32_bf16 v[40:43], a[104:107], v[228:231], 0
ds_read_b128 a[76:79], v248 offset:3072
v_cvt_pk_bf16_f32 v236, v216, v217
v_mfma_f32_16x16x32_bf16 v[12:15], a[108:111], v[224:227], 0
v_exp_f32 v212, v212
v_mfma_f32_16x16x32_bf16 v[44:47], a[108:111], v[228:231], 0
ds_read_b128 a[80:83], v248 offset:4096
s_waitcnt lgkmcnt(5)
v_mfma_f32_16x16x32_bf16 v[16:19], a[112:115], v[224:227], 0
v_exp_f32 v213, v213
v_mfma_f32_16x16x32_bf16 v[48:51], a[112:115], v[228:231], 0
ds_read_b128 a[84:87], v248 offset:5120
v_cvt_pk_bf16_f32 v237, v218, v219
v_mfma_f32_16x16x32_bf16 v[20:23], a[116:119], v[224:227], 0
v_exp_f32 v214, v214
v_mfma_f32_16x16x32_bf16 v[52:55], a[116:119], v[228:231], 0
ds_read_b128 a[88:91], v248 offset:6144
v_cvt_pk_bf16_f32 v234, v212, v213
v_mfma_f32_16x16x32_bf16 v[24:27], a[120:123], v[224:227], 0
v_exp_f32 v215, v215
v_mfma_f32_16x16x32_bf16 v[56:59], a[120:123], v[228:231], 0
ds_read_b128 a[92:95], v248 offset:7168
v_cvt_pk_bf16_f32 v235, v214, v215
v_mfma_f32_16x16x32_bf16 v[28:31], a[124:127], v[224:227], 0
v_exp_f32_e64 v220, v220
v_mfma_f32_16x16x32_bf16 v[60:63], a[124:127], v[228:231], 0
s_waitcnt vmcnt(5)
buffer_store_dwordx2 v[252:253], off, s[64:67], s86 sc1
s_mov_b32 s86, 1073741824
s_cbranch_scc1 .La5_LA_FE0
.La5_LA_FS0:
s_waitcnt lgkmcnt(4)
v_mfma_f32_16x16x32_bf16 v[192:195], a[64:67], v[128:131], 0
v_exp_f32 v221, v221
v_mfma_f32_16x16x32_bf16 v[192:195], a[68:71], v[132:135], v[192:195]
v_cvt_pk_bf16_f32 v238, v220, v221
s_add_u32 s85, s85, 1
v_mfma_f32_16x16x32_bf16 v[192:195], a[72:75], v[136:139], v[192:195]
v_exp_f32 v222, v222
v_mfma_f32_16x16x32_bf16 v[192:195], a[76:79], v[140:143], v[192:195]
buffer_load_dwordx4 v255, s[60:63], s17 offen offset:0 sc1 lds
v_mfma_f32_16x16x32_bf16 v[200:203], a[64:67], v[144:147], 0
v_exp_f32 v223, v223
v_mfma_f32_16x16x32_bf16 v[200:203], a[68:71], v[148:151], v[200:203]
v_cvt_pk_bf16_f32 v239, v222, v223
s_add_u32 s89, s89, 16384
v_mfma_f32_16x16x32_bf16 v[200:203], a[72:75], v[152:155], v[200:203]
v_exp_f32 v192, v192
v_mfma_f32_16x16x32_bf16 v[200:203], a[76:79], v[156:159], v[200:203]
s_waitcnt lgkmcnt(0)
s_and_b32 s89, s89, 0x1ffff
v_mfma_f32_16x16x32_bf16 v[196:199], a[80:83], v[128:131], 0
v_exp_f32 v193, v193
v_mfma_f32_16x16x32_bf16 v[196:199], a[84:87], v[132:135], v[196:199]
v_cvt_pk_bf16_f32 v224, v192, v193
v_mfma_f32_16x16x32_bf16 v[196:199], a[88:91], v[136:139], v[196:199]
v_exp_f32 v194, v194
v_mfma_f32_16x16x32_bf16 v[196:199], a[92:95], v[140:143], v[196:199]
buffer_load_dwordx4 v255, s[60:63], s17 offen offset:1024 sc1 lds
v_mfma_f32_16x16x32_bf16 v[204:207], a[80:83], v[144:147], 0
v_exp_f32 v195, v195
v_mfma_f32_16x16x32_bf16 v[204:207], a[84:87], v[148:151], v[204:207]
v_cvt_pk_bf16_f32 v225, v194, v195
v_mfma_f32_16x16x32_bf16 v[204:207], a[88:91], v[152:155], v[204:207]
v_exp_f32 v200, v200
v_mfma_f32_16x16x32_bf16 v[204:207], a[92:95], v[156:159], v[204:207]
buffer_load_dwordx4 v255, s[60:63], s17 offen offset:2048 sc1 lds
v_mfma_f32_16x16x32_bf16 v[208:211], a[64:67], v[160:163], 0
v_exp_f32 v201, v201
v_mfma_f32_16x16x32_bf16 v[208:211], a[68:71], v[164:167], v[208:211]
v_cvt_pk_bf16_f32 v228, v200, v201
v_mfma_f32_16x16x32_bf16 v[208:211], a[72:75], v[168:171], v[208:211]
v_exp_f32 v202, v202
v_mfma_f32_16x16x32_bf16 v[208:211], a[76:79], v[172:175], v[208:211]
buffer_load_dwordx4 v255, s[60:63], s17 offen offset:3072 sc1 lds
v_mfma_f32_16x16x32_bf16 v[216:219], a[64:67], v[176:179], 0
v_exp_f32 v203, v203
v_mfma_f32_16x16x32_bf16 v[216:219], a[68:71], v[180:183], v[216:219]
v_cvt_pk_bf16_f32 v229, v202, v203
v_mfma_f32_16x16x32_bf16 v[216:219], a[72:75], v[184:187], v[216:219]
v_exp_f32 v196, v196
v_mfma_f32_16x16x32_bf16 v[216:219], a[76:79], v[188:191], v[216:219]
buffer_load_dword a253, v254, s[36:39], s90 offen
v_mfma_f32_16x16x32_bf16 v[212:215], a[80:83], v[160:163], 0
v_exp_f32 v197, v197
v_mfma_f32_16x16x32_bf16 v[212:215], a[84:87], v[164:167], v[212:215]
v_cvt_pk_bf16_f32 v226, v196, v197
s_add_u32 s17, s17, 8192
v_mfma_f32_16x16x32_bf16 v[212:215], a[88:91], v[168:171], v[212:215]
v_exp_f32 v198, v198
v_mfma_f32_16x16x32_bf16 v[212:215], a[92:95], v[172:175], v[212:215]
s_add_u32 m0, m0, 16384
s_and_b32 m0, m0, s78
v_mfma_f32_16x16x32_bf16 v[220:223], a[80:83], v[176:179], 0
v_exp_f32 v199, v199
v_mfma_f32_16x16x32_bf16 v[220:223], a[84:87], v[180:183], v[220:223]
v_cvt_pk_bf16_f32 v227, v198, v199
s_add_u32 s90, s90, s21
v_mfma_f32_16x16x32_bf16 v[220:223], a[88:91], v[184:187], v[220:223]
v_exp_f32 v204, v204
v_mfma_f32_16x16x32_bf16 v[220:223], a[92:95], v[188:191], v[220:223]
s_waitcnt vmcnt(12)
s_barrier
v_mfma_f32_16x16x32_bf16 v[64:67], a[96:99], v[232:235], v[64:67]
v_exp_f32 v205, v205
v_mfma_f32_16x16x32_bf16 v[96:99], a[96:99], v[236:239], v[96:99]
ds_read_b128 a[96:99], v248 offset:8192
v_cvt_pk_bf16_f32 v230, v204, v205
v_mfma_f32_16x16x32_bf16 v[68:71], a[100:103], v[232:235], v[68:71]
v_exp_f32 v206, v206
v_mfma_f32_16x16x32_bf16 v[100:103], a[100:103], v[236:239], v[100:103]
ds_read_b128 a[100:103], v248 offset:9216
buffer_load_dword v250, v251, s[64:67], s20 offen sc1
v_mfma_f32_16x16x32_bf16 v[72:75], a[104:107], v[232:235], v[72:75]
v_exp_f32 v207, v207
v_mfma_f32_16x16x32_bf16 v[104:107], a[104:107], v[236:239], v[104:107]
ds_read_b128 a[104:107], v248 offset:10240
v_cvt_pk_bf16_f32 v231, v206, v207
v_mfma_f32_16x16x32_bf16 v[76:79], a[108:111], v[232:235], v[76:79]
v_exp_f32 v208, v208
v_mfma_f32_16x16x32_bf16 v[108:111], a[108:111], v[236:239], v[108:111]
ds_read_b128 a[108:111], v248 offset:11264
v_mfma_f32_16x16x32_bf16 v[80:83], a[112:115], v[232:235], v[80:83]
v_exp_f32 v209, v209
v_mfma_f32_16x16x32_bf16 v[112:115], a[112:115], v[236:239], v[112:115]
ds_read_b128 a[112:115], v248 offset:12288
v_mfma_f32_16x16x32_bf16 v[84:87], a[116:119], v[232:235], v[84:87]
v_exp_f32 v210, v210
v_mfma_f32_16x16x32_bf16 v[116:119], a[116:119], v[236:239], v[116:119]
ds_read_b128 a[116:119], v248 offset:13312
v_mfma_f32_16x16x32_bf16 v[88:91], a[120:123], v[232:235], v[88:91]
v_exp_f32 v211, v211
v_mfma_f32_16x16x32_bf16 v[120:123], a[120:123], v[236:239], v[120:123]
ds_read_b128 a[120:123], v248 offset:14336
s_bitcmp1_b64 s[98:99], s85
v_mfma_f32_16x16x32_bf16 v[92:95], a[124:127], v[232:235], v[92:95]
v_exp_f32 v216, v216
v_mfma_f32_16x16x32_bf16 v[124:127], a[124:127], v[236:239], v[124:127]
ds_read_b128 a[124:127], v248 offset:15360
v_or_b32 v248, s89, v255
v_smfmac_f32_16x16x64_bf16 a[196:199], a[208:211], v[232:239], v241
s_waitcnt lgkmcnt(4)
v_smfmac_f32_16x16x64_bf16 a[192:195], a[208:211], v[224:231], v241
ds_read_b128 a[64:67], v248 offset:0
v_mfma_f32_16x16x32_bf16 v[0:3], a[96:99], v[224:227], v[0:3]
v_exp_f32 v217, v217
v_mfma_f32_16x16x32_bf16 v[32:35], a[96:99], v[228:231], v[32:35]
ds_read_b128 a[68:71], v248 offset:1024
v_cvt_pk_bf16_f32 v232, v208, v209
v_mfma_f32_16x16x32_bf16 v[4:7], a[100:103], v[224:227], v[4:7]
v_exp_f32 v218, v218
v_mfma_f32_16x16x32_bf16 v[36:39], a[100:103], v[228:231], v[36:39]
ds_read_b128 a[72:75], v248 offset:2048
v_cvt_pk_bf16_f32 v233, v210, v211
v_mfma_f32_16x16x32_bf16 v[8:11], a[104:107], v[224:227], v[8:11]
v_exp_f32 v219, v219
v_mfma_f32_16x16x32_bf16 v[40:43], a[104:107], v[228:231], v[40:43]
ds_read_b128 a[76:79], v248 offset:3072
v_cvt_pk_bf16_f32 v236, v216, v217
v_mfma_f32_16x16x32_bf16 v[12:15], a[108:111], v[224:227], v[12:15]
v_exp_f32 v212, v212
v_mfma_f32_16x16x32_bf16 v[44:47], a[108:111], v[228:231], v[44:47]
ds_read_b128 a[80:83], v248 offset:4096
s_waitcnt lgkmcnt(5)
v_mfma_f32_16x16x32_bf16 v[16:19], a[112:115], v[224:227], v[16:19]
v_exp_f32 v213, v213
v_mfma_f32_16x16x32_bf16 v[48:51], a[112:115], v[228:231], v[48:51]
ds_read_b128 a[84:87], v248 offset:5120
v_cvt_pk_bf16_f32 v237, v218, v219
v_mfma_f32_16x16x32_bf16 v[20:23], a[116:119], v[224:227], v[20:23]
v_exp_f32 v214, v214
v_mfma_f32_16x16x32_bf16 v[52:55], a[116:119], v[228:231], v[52:55]
ds_read_b128 a[88:91], v248 offset:6144
v_cvt_pk_bf16_f32 v234, v212, v213
v_mfma_f32_16x16x32_bf16 v[24:27], a[120:123], v[224:227], v[24:27]
v_exp_f32 v215, v215
v_mfma_f32_16x16x32_bf16 v[56:59], a[120:123], v[228:231], v[56:59]
ds_read_b128 a[92:95], v248 offset:7168
v_cvt_pk_bf16_f32 v235, v214, v215
v_mfma_f32_16x16x32_bf16 v[28:31], a[124:127], v[224:227], v[28:31]
v_exp_f32 v220, v220
v_mfma_f32_16x16x32_bf16 v[60:63], a[124:127], v[228:231], v[60:63]
s_cbranch_scc1 .La5_LA_F1E2
.p2align 6
; ---- steady windows
.La5_LA_F1:
s_waitcnt lgkmcnt(4)
v_mfma_f32_16x16x32_bf16 v[192:195], a[64:67], v[128:131], 0
v_exp_f32 v221, v221
v_mfma_f32_16x16x32_bf16 v[192:195], a[68:71], v[132:135], v[192:195]
v_cvt_pk_bf16_f32 v238, v220, v221
s_add_u32 s85, s85, 1
v_mfma_f32_16x16x32_bf16 v[192:195], a[72:75], v[136:139], v[192:195]
v_exp_f32 v222, v222
v_mfma_f32_16x16x32_bf16 v[192:195], a[76:79], v[140:143], v[192:195]
buffer_load_dwordx4 v255, s[60:63], s17 offen offset:0 sc1 lds
v_mfma_f32_16x16x32_bf16 v[200:203], a[64:67], v[144:147], 0
v_exp_f32 v223, v223
v_mfma_f32_16x16x32_bf16 v[200:203], a[68:71], v[148:151], v[200:203]
v_cvt_pk_bf16_f32 v239, v222, v223
s_add_u32 s89, s89, 16384
v_mfma_f32_16x16x32_bf16 v[200:203], a[72:75], v[152:155], v[200:203]
v_exp_f32 v192, v192
v_mfma_f32_16x16x32_bf16 v[200:203], a[76:79], v[156:159], v[200:203]
s_waitcnt lgkmcnt(0)
s_and_b32 s89, s89, 0x1ffff
v_mfma_f32_16x16x32_bf16 v[196:199], a[80:83], v[128:131], 0
v_exp_f32 v193, v193
v_mfma_f32_16x16x32_bf16 v[196:199], a[84:87], v[132:135], v[196:199]
v_cvt_pk_bf16_f32 v224, v192, v193
v_mfma_f32_16x16x32_bf16 v[196:199], a[88:91], v[136:139], v[196:199]
v_exp_f32 v194, v194
v_mfma_f32_16x16x32_bf16 v[196:199], a[92:95], v[140:143], v[196:199]
buffer_load_dwordx4 v255, s[60:63], s17 offen offset:1024 sc1 lds
v_mfma_f32_16x16x32_bf16 v[204:207], a[80:83], v[144:147], 0
v_exp_f32 v195, v195
v_mfma_f32_16x16x32_bf16 v[204:207], a[84:87], v[148:151], v[204:207]
v_cvt_pk_bf16_f32 v225, v194, v195
v_mfma_f32_16x16x32_bf16 v[204:207], a[88:91], v[152:155], v[204:207]
v_exp_f32 v200, v200
v_mfma_f32_16x16x32_bf16 v[204:207], a[92:95], v[156:159], v[204:207]
buffer_load_dwordx4 v255, s[60:63], s17 offen offset:2048 sc1 lds
v_mfma_f32_16x16x32_bf16 v[208:211], a[64:67], v[160:163], 0
v_exp_f32 v201, v201
v_mfma_f32_16x16x32_bf16 v[208:211], a[68:71], v[164:167], v[208:211]
v_cvt_pk_bf16_f32 v228, v200, v201
v_mfma_f32_16x16x32_bf16 v[208:211], a[72:75], v[168:171], v[208:211]
v_exp_f32 v202, v202
v_mfma_f32_16x16x32_bf16 v[208:211], a[76:79], v[172:175], v[208:211]
buffer_load_dwordx4 v255, s[60:63], s17 offen offset:3072 sc1 lds
v_mfma_f32_16x16x32_bf16 v[216:219], a[64:67], v[176:179], 0
v_exp_f32 v203, v203
v_mfma_f32_16x16x32_bf16 v[216:219], a[68:71], v[180:183], v[216:219]
v_cvt_pk_bf16_f32 v229, v202, v203
v_mfma_f32_16x16x32_bf16 v[216:219], a[72:75], v[184:187], v[216:219]
v_exp_f32 v196, v196
v_mfma_f32_16x16x32_bf16 v[216:219], a[76:79], v[188:191], v[216:219]
buffer_load_dword a253, v254, s[36:39], s90 offen
v_mfma_f32_16x16x32_bf16 v[212:215], a[80:83], v[160:163], 0
v_exp_f32 v197, v197
v_mfma_f32_16x16x32_bf16 v[212:215], a[84:87], v[164:167], v[212:215]
v_cvt_pk_bf16_f32 v226, v196, v197
.La5_LW2_1:
s_cbranch_execz .La5_LW2_5
s_add_u32 s17, s17, 8192
v_mfma_f32_16x16x32_bf16 v[212:215], a[88:91], v[168:171], v[212:215]
v_exp_f32 v198, v198
v_mfma_f32_16x16x32_bf16 v[212:215], a[92:95], v[172:175], v[212:215]
s_add_u32 m0, m0, 16384
s_and_b32 m0, m0, s78
v_mfma_f32_16x16x32_bf16 v[220:223], a[80:83], v[176:179], 0
v_exp_f32 v199, v199
v_mfma_f32_16x16x32_bf16 v[220:223], a[84:87], v[180:183], v[220:223]
v_cvt_pk_bf16_f32 v227, v198, v199
s_add_u32 s90, s90, s21
v_mfma_f32_16x16x32_bf16 v[220:223], a[88:91], v[184:187], v[220:223]
v_exp_f32 v204, v204
v_mfma_f32_16x16x32_bf16 v[220:223], a[92:95], v[188:191], v[220:223]
s_waitcnt vmcnt(11)
s_barrier
v_mfma_f32_16x16x32_bf16 v[64:67], a[96:99], v[232:235], v[64:67]
v_exp_f32 v205, v205
v_mfma_f32_16x16x32_bf16 v[96:99], a[96:99], v[236:239], v[96:99]
ds_read_b128 a[96:99], v248 offset:8192
v_cvt_pk_bf16_f32 v230, v204, v205
v_mfma_f32_16x16x32_bf16 v[68:71], a[100:103], v[232:235], v[68:71]
v_exp_f32 v206, v206
v_mfma_f32_16x16x32_bf16 v[100:103], a[100:103], v[236:239], v[100:103]
ds_read_b128 a[100:103], v248 offset:9216
v_mfma_f32_16x16x32_bf16 v[72:75], a[104:107], v[232:235], v[72:75]
v_exp_f32 v207, v207
v_mfma_f32_16x16x32_bf16 v[104:107], a[104:107], v[236:239], v[104:107]
ds_read_b128 a[104:107], v248 offset:10240
v_cvt_pk_bf16_f32 v231, v206, v207
v_mfma_f32_16x16x32_bf16 v[76:79], a[108:111], v[232:235], v[76:79]
v_exp_f32 v208, v208
v_mfma_f32_16x16x32_bf16 v[108:111], a[108:111], v[236:239], v[108:111]
ds_read_b128 a[108:111], v248 offset:11264
v_mfma_f32_16x16x32_bf16 v[80:83], a[112:115], v[232:235], v[80:83]
v_exp_f32 v209, v209
v_mfma_f32_16x16x32_bf16 v[112:115], a[112:115], v[236:239], v[112:115]
ds_read_b128 a[112:115], v248 offset:12288
v_mfma_f32_16x16x32_bf16 v[84:87], a[116:119], v[232:235], v[84:87]
v_exp_f32 v210, v210
v_mfma_f32_16x16x32_bf16 v[116:119], a[116:119], v[236:239], v[116:119]
ds_read_b128 a[116:119], v248 offset:13312
v_mfma_f32_16x16x32_bf16 v[88:91], a[120:123], v[232:235], v[88:91]
v_exp_f32 v211, v211
v_mfma_f32_16x16x32_bf16 v[120:123], a[120:123], v[236:239], v[120:123]
ds_read_b128 a[120:123], v248 offset:14336
s_bitcmp1_b64 s[98:99], s85
v_mfma_f32_16x16x32_bf16 v[92:95], a[124:127], v[232:235], v[92:95]
v_exp_f32 v216, v216
v_mfma_f32_16x16x32_bf16 v[124:127], a[124:127], v[236:239], v[124:127]
ds_read_b128 a[124:127], v248 offset:15360
v_or_b32 v248, s89, v255
v_smfmac_f32_16x16x64_bf16 a[196:199], a[208:211], v[232:239], v241
s_waitcnt lgkmcnt(4)
v_smfmac_f32_16x16x64_bf16 a[192:195], a[208:211], v[224:231], v241
ds_read_b128 a[64:67], v248 offset:0
v_mfma_f32_16x16x32_bf16 v[0:3], a[96:99], v[224:227], v[0:3]
v_exp_f32 v217, v217
v_mfma_f32_16x16x32_bf16 v[32:35], a[96:99], v[228:231], v[32:35]
ds_read_b128 a[68:71], v248 offset:1024
v_cvt_pk_bf16_f32 v232, v208, v209
v_mfma_f32_16x16x32_bf16 v[4:7], a[100:103], v[224:227], v[4:7]
v_exp_f32 v218, v218
v_mfma_f32_16x16x32_bf16 v[36:39], a[100:103], v[228:231], v[36:39]
ds_read_b128 a[72:75], v248 offset:2048
v_cvt_pk_bf16_f32 v233, v210, v211
v_mfma_f32_16x16x32_bf16 v[8:11], a[104:107], v[224:227], v[8:11]
v_exp_f32 v219, v219
v_mfma_f32_16x16x32_bf16 v[40:43], a[104:107], v[228:231], v[40:43]
ds_read_b128 a[76:79], v248 offset:3072
v_cvt_pk_bf16_f32 v236, v216, v217
v_mfma_f32_16x16x32_bf16 v[12:15], a[108:111], v[224:227], v[12:15]
v_exp_f32 v212, v212
v_mfma_f32_16x16x32_bf16 v[44:47], a[108:111], v[228:231], v[44:47]
ds_read_b128 a[80:83], v248 offset:4096
s_waitcnt lgkmcnt(5)
v_mfma_f32_16x16x32_bf16 v[16:19], a[112:115], v[224:227], v[16:19]
v_exp_f32 v213, v213
v_mfma_f32_16x16x32_bf16 v[48:51], a[112:115], v[228:231], v[48:51]
ds_read_b128 a[84:87], v248 offset:5120
v_cvt_pk_bf16_f32 v237, v218, v219
v_mfma_f32_16x16x32_bf16 v[20:23], a[116:119], v[224:227], v[20:23]
v_exp_f32 v214, v214
v_mfma_f32_16x16x32_bf16 v[52:55], a[116:119], v[228:231], v[52:55]
ds_read_b128 a[88:91], v248 offset:6144
v_cvt_pk_bf16_f32 v234, v212, v213
v_mfma_f32_16x16x32_bf16 v[24:27], a[120:123], v[224:227], v[24:27]
v_exp_f32 v215, v215
v_mfma_f32_16x16x32_bf16 v[56:59], a[120:123], v[228:231], v[56:59]
ds_read_b128 a[92:95], v248 offset:7168
v_cvt_pk_bf16_f32 v235, v214, v215
v_mfma_f32_16x16x32_bf16 v[28:31], a[124:127], v[224:227], v[28:31]
v_exp_f32 v220, v220
v_mfma_f32_16x16x32_bf16 v[60:63], a[124:127], v[228:231], v[60:63]
s_cbranch_scc1 .La5_LA_F1E2
s_waitcnt lgkmcnt(4)
v_mfma_f32_16x16x32_bf16 v[192:195], a[64:67], v[128:131], 0
v_exp_f32 v221, v221
v_mfma_f32_16x16x32_bf16 v[192:195], a[68:71], v[132:135], v[192:195]
v_cvt_pk_bf16_f32 v238, v220, v221
s_add_u32 s85, s85, 1
v_mfma_f32_16x16x32_bf16 v[192:195], a[72:75], v[136:139], v[192:195]
v_exp_f32 v222, v222
v_mfma_f32_16x16x32_bf16 v[192:195], a[76:79], v[140:143], v[192:195]
buffer_load_dwordx4 v255, s[60:63], s17 offen offset:0 sc1 lds
v_mfma_f32_16x16x32_bf16 v[200:203], a[64:67], v[144:147], 0
v_exp_f32 v223, v223
v_mfma_f32_16x16x32_bf16 v[200:203], a[68:71], v[148:151], v[200:203]
v_cvt_pk_bf16_f32 v239, v222, v223
s_add_u32 s89, s89, 16384
v_mfma_f32_16x16x32_bf16 v[200:203], a[72:75], v[152:155], v[200:203]
v_exp_f32 v192, v192
v_mfma_f32_16x16x32_bf16 v[200:203], a[76:79], v[156:159], v[200:203]
s_waitcnt lgkmcnt(0)
s_and_b32 s89, s89, 0x1ffff
v_mfma_f32_16x16x32_bf16 v[196:199], a[80:83], v[128:131], 0
v_exp_f32 v193, v193
v_mfma_f32_16x16x32_bf16 v[196:199], a[84:87], v[132:135], v[196:199]
v_cvt_pk_bf16_f32 v224, v192, v193
v_mfma_f32_16x16x32_bf16 v[196:199], a[88:91], v[136:139], v[196:199]
v_exp_f32 v194, v194
v_mfma_f32_16x16x32_bf16 v[196:199], a[92:95], v[140:143], v[196:199]
buffer_load_dwordx4 v255, s[60:63], s17 offen offset:1024 sc1 lds
v_mfma_f32_16x16x32_bf16 v[204:207], a[80:83], v[144:147], 0
v_exp_f32 v195, v195
v_mfma_f32_16x16x32_bf16 v[204:207], a[84:87], v[148:151], v[204:207]
v_cvt_pk_bf16_f32 v225, v194, v195
v_mfma_f32_16x16x32_bf16 v[204:207], a[88:91], v[152:155], v[204:207]
v_exp_f32 v200, v200
v_mfma_f32_16x16x32_bf16 v[204:207], a[92:95], v[156:159], v[204:207]
buffer_load_dwordx4 v255, s[60:63], s17 offen offset:2048 sc1 lds
v_mfma_f32_16x16x32_bf16 v[208:211], a[64:67], v[160:163], 0
v_exp_f32 v201, v201
v_mfma_f32_16x16x32_bf16 v[208:211], a[68:71], v[164:167], v[208:211]
v_cvt_pk_bf16_f32 v228, v200, v201
v_mfma_f32_16x16x32_bf16 v[208:211], a[72:75], v[168:171], v[208:211]
v_exp_f32 v202, v202
v_mfma_f32_16x16x32_bf16 v[208:211], a[76:79], v[172:175], v[208:211]
buffer_load_dwordx4 v255, s[60:63], s17 offen offset:3072 sc1 lds
v_mfma_f32_16x16x32_bf16 v[216:219], a[64:67], v[176:179], 0
v_exp_f32 v203, v203
v_mfma_f32_16x16x32_bf16 v[216:219], a[68:71], v[180:183], v[216:219]
v_cvt_pk_bf16_f32 v229, v202, v203
v_mfma_f32_16x16x32_bf16 v[216:219], a[72:75], v[184:187], v[216:219]
v_exp_f32 v196, v196
v_mfma_f32_16x16x32_bf16 v[216:219], a[76:79], v[188:191], v[216:219]
buffer_load_dword a253, v254, s[36:39], s90 offen
v_mfma_f32_16x16x32_bf16 v[212:215], a[80:83], v[160:163], 0
v_exp_f32 v197, v197
v_mfma_f32_16x16x32_bf16 v[212:215], a[84:87], v[164:167], v[212:215]
v_cvt_pk_bf16_f32 v226, v196, v197
s_add_u32 s17, s17, 8192
v_mfma_f32_16x16x32_bf16 v[212:215], a[88:91], v[168:171], v[212:215]
v_exp_f32 v198, v198
v_mfma_f32_16x16x32_bf16 v[212:215], a[92:95], v[172:175], v[212:215]
s_add_u32 m0, m0, 16384
s_and_b32 m0, m0, s78
v_mfma_f32_16x16x32_bf16 v[220:223], a[80:83], v[176:179], 0
v_exp_f32 v199, v199
v_mfma_f32_16x16x32_bf16 v[220:223], a[84:87], v[180:183], v[220:223]
v_cvt_pk_bf16_f32 v227, v198, v199
s_add_u32 s90, s90, s21
v_mfma_f32_16x16x32_bf16 v[220:223], a[88:91], v[184:187], v[220:223]
v_exp_f32 v204, v204
v_mfma_f32_16x16x32_bf16 v[220:223], a[92:95], v[188:191], v[220:223]
s_waitcnt vmcnt(11)
s_barrier
v_mfma_f32_16x16x32_bf16 v[64:67], a[96:99], v[232:235], v[64:67]
v_exp_f32 v205, v205
v_mfma_f32_16x16x32_bf16 v[96:99], a[96:99], v[236:239], v[96:99]
ds_read_b128 a[96:99], v248 offset:8192
v_cvt_pk_bf16_f32 v230, v204, v205
v_mfma_f32_16x16x32_bf16 v[68:71], a[100:103], v[232:235], v[68:71]
v_exp_f32 v206, v206
v_mfma_f32_16x16x32_bf16 v[100:103], a[100:103], v[236:239], v[100:103]
ds_read_b128 a[100:103], v248 offset:9216
v_mfma_f32_16x16x32_bf16 v[72:75], a[104:107], v[232:235], v[72:75]
v_exp_f32 v207, v207
v_mfma_f32_16x16x32_bf16 v[104:107], a[104:107], v[236:239], v[104:107]
ds_read_b128 a[104:107], v248 offset:10240
v_cvt_pk_bf16_f32 v231, v206, v207
v_mfma_f32_16x16x32_bf16 v[76:79], a[108:111], v[232:235], v[76:79]
v_exp_f32 v208, v208
v_mfma_f32_16x16x32_bf16 v[108:111], a[108:111], v[236:239], v[108:111]
ds_read_b128 a[108:111], v248 offset:11264
v_mfma_f32_16x16x32_bf16 v[80:83], a[112:115], v[232:235], v[80:83]
v_exp_f32 v209, v209
v_mfma_f32_16x16x32_bf16 v[112:115], a[112:115], v[236:239], v[112:115]
ds_read_b128 a[112:115], v248 offset:12288
v_mfma_f32_16x16x32_bf16 v[84:87], a[116:119], v[232:235], v[84:87]
v_exp_f32 v210, v210
v_mfma_f32_16x16x32_bf16 v[116:119], a[116:119], v[236:239], v[116:119]
ds_read_b128 a[116:119], v248 offset:13312
v_mfma_f32_16x16x32_bf16 v[88:91], a[120:123], v[232:235], v[88:91]
v_exp_f32 v211, v211
v_mfma_f32_16x16x32_bf16 v[120:123], a[120:123], v[236:239], v[120:123]
ds_read_b128 a[120:123], v248 offset:14336
s_bitcmp1_b64 s[98:99], s85
v_mfma_f32_16x16x32_bf16 v[92:95], a[124:127], v[232:235], v[92:95]
v_exp_f32 v216, v216
v_mfma_f32_16x16x32_bf16 v[124:127], a[124:127], v[236:239], v[124:127]
ds_read_b128 a[124:127], v248 offset:15360
v_or_b32 v248, s89, v255
v_smfmac_f32_16x16x64_bf16 a[196:199], a[208:211], v[232:239], v241
s_waitcnt lgkmcnt(4)
v_smfmac_f32_16x16x64_bf16 a[192:195], a[208:211], v[224:231], v241
ds_read_b128 a[64:67], v248 offset:0
v_mfma_f32_16x16x32_bf16 v[0:3], a[96:99], v[224:227], v[0:3]
v_exp_f32 v217, v217
v_mfma_f32_16x16x32_bf16 v[32:35], a[96:99], v[228:231], v[32:35]
ds_read_b128 a[68:71], v248 offset:1024
v_cvt_pk_bf16_f32 v232, v208, v209
v_mfma_f32_16x16x32_bf16 v[4:7], a[100:103], v[224:227], v[4:7]
v_exp_f32 v218, v218
v_mfma_f32_16x16x32_bf16 v[36:39], a[100:103], v[228:231], v[36:39]
ds_read_b128 a[72:75], v248 offset:2048
v_cvt_pk_bf16_f32 v233, v210, v211
v_mfma_f32_16x16x32_bf16 v[8:11], a[104:107], v[224:227], v[8:11]
v_exp_f32 v219, v219
v_mfma_f32_16x16x32_bf16 v[40:43], a[104:107], v[228:231], v[40:43]
ds_read_b128 a[76:79], v248 offset:3072
v_cvt_pk_bf16_f32 v236, v216, v217
v_mfma_f32_16x16x32_bf16 v[12:15], a[108:111], v[224:227], v[12:15]
v_exp_f32 v212, v212
v_mfma_f32_16x16x32_bf16 v[44:47], a[108:111], v[228:231], v[44:47]
ds_read_b128 a[80:83], v248 offset:4096
s_waitcnt lgkmcnt(5)
v_mfma_f32_16x16x32_bf16 v[16:19], a[112:115], v[224:227], v[16:19]
v_exp_f32 v213, v213
v_mfma_f32_16x16x32_bf16 v[48:51], a[112:115], v[228:231], v[48:51]
ds_read_b128 a[84:87], v248 offset:5120
v_cvt_pk_bf16_f32 v237, v218, v219
v_mfma_f32_16x16x32_bf16 v[20:23], a[116:119], v[224:227], v[20:23]
v_exp_f32 v214, v214
v_mfma_f32_16x16x32_bf16 v[52:55], a[116:119], v[228:231], v[52:55]
ds_read_b128 a[88:91], v248 offset:6144
v_cvt_pk_bf16_f32 v234, v212, v213
v_mfma_f32_16x16x32_bf16 v[24:27], a[120:123], v[224:227], v[24:27]
v_exp_f32 v215, v215
v_mfma_f32_16x16x32_bf16 v[56:59], a[120:123], v[228:231], v[56:59]
ds_read_b128 a[92:95], v248 offset:7168
v_cvt_pk_bf16_f32 v235, v214, v215
v_mfma_f32_16x16x32_bf16 v[28:31], a[124:127], v[224:227], v[28:31]
v_exp_f32 v220, v220
v_mfma_f32_16x16x32_bf16 v[60:63], a[124:127], v[228:231], v[60:63]
.La5_LA_F1E:
s_cbranch_scc0 .La5_LA_F1
.La5_LA_F1E2:
s_cmp_eq_u32 s85, s82
s_nop 0
s_nop 0
s_cbranch_scc1 .La5_LA_F1X
s_call_b64 s[24:25], .La5_LA_EVENT
s_branch .La5_LA_F1
.La5_LA_FE0:
.La5_LW2_2:
s_cbranch_execz .La5_LW2_6
s_cmp_eq_u32 s85, s82
s_cbranch_scc1 .La5_LA_F1X
s_nop 0
s_nop 0
s_call_b64 s[24:25], .La5_LA_EVENT
s_branch .La5_LA_FS0
; ---- hand-offs between windows
.La5_LA_EVENT:
s_cmp_lg_u32 s85, 2
s_cbranch_scc1 .La5_LA_E1
s_waitcnt vmcnt(0)
v_cmp_eq_u32 vcc, v250, v249
s_mov_b32 s17, s16
s_mov_b32 s78, 0x1ffff
s_lshl_b32 m0, s3, 12
s_or_b32 vcc_hi, vcc_hi, s79
s_cmp_eq_u64 vcc, -1
s_cbranch_scc0 .La5_LA_KSPIN
.La5_LA_KOK:
buffer_load_dwordx4 v255, s[60:63], s17 offen offset:0 sc1 lds
buffer_load_dwordx4 v255, s[60:63], s17 offen offset:1024 sc1 lds
buffer_load_dwordx4 v255, s[60:63], s17 offen offset:2048 sc1 lds
buffer_load_dwordx4 v255, s[60:63], s17 offen offset:3072 sc1 lds
s_add_u32 s17, s17, 8192
s_add_u32 m0, m0, 16384
s_mov_b32 s20, s19
v_and_b32 v251, 4, v251
s_cmp_lg_u32 s3, 0
s_cbranch_scc1 .La5_LA_POLL
s_nop 0
buffer_load_dwordx4 v255, s[60:63], s17 offen offset:0 sc1 lds
buffer_load_dwordx4 v255, s[60:63], s17 offen offset:1024 sc1 lds
buffer_load_dwordx4 v255, s[60:63], s17 offen offset:2048 sc1 lds
buffer_load_dwordx4 v255, s[60:63], s17 offen offset:3072 sc1 lds
s_add_u32 s17, s17, 8192
s_add_u32 m0, m0, 16384
.La5_LA_POLL:
s_cmp_eq_u32 s73, 1
s_cbranch_scc1 .La5_LA_E0
buffer_load_dword v250, v251, s[64:67], s20 offen sc1
s_setpc_b64 s[24:25]
.La5_LA_E0:
s_mov_b32 s50, 16384
buffer_load_dwordx4 a[0:3], v255, s[48:51], 0 offen offset:0 sc1
buffer_load_dwordx4 a[4:7], v255, s[48:51], 0 offen offset:1024 sc1
buffer_load_dwordx4 a[8:11], v255, s[48:51], 0 offen offset:2048 sc1
buffer_load_dwordx4 a[12:15], v255, s[48:51], 0 offen offset:3072 sc1
buffer_load_dwordx4 a[16:19], v255, s[48:51], s95 offen offset:0 sc1
buffer_load_dwordx4 a[20:23], v255, s[48:51], s95 offen offset:1024 sc1
s_setpc_b64 s[24:25]
.La5_LA_E1:
buffer_load_dwordx4 a[24:27], v255, s[48:51], s95 offen offset:2048 sc1
buffer_load_dwordx4 a[28:31], v255, s[48:51], s95 offen offset:3072 sc1
buffer_load_dwordx4 a[32:35], v255, s[48:51], s96 offen offset:0 sc1
buffer_load_dwordx4 a[36:39], v255, s[48:51], s96 offen offset:1024 sc1
buffer_load_dwordx4 a[40:43], v255, s[48:51], s96 offen offset:2048 sc1
buffer_load_dwordx4 a[44:47], v255, s[48:51], s96 offen offset:3072 sc1
buffer_load_dwordx4 a[48:51], v255, s[48:51], s97 offen offset:0 sc1
buffer_load_dwordx4 a[52:55], v255, s[48:51], s97 offen offset:1024 sc1
buffer_load_dwordx4 a[56:59], v255, s[48:51], s97 offen offset:2048 sc1
buffer_load_dwordx4 a[60:63], v255, s[48:51], s97 offen offset:3072 sc1
s_setpc_b64 s[24:25]
; ---- helper: own block finished: l is checked (.La5_LA_SLOW if outside [2^-60, 2^60] or NaN), O / l leaves; the partner-s rows start
.La5_LA_F1X:
s_cmp_eq_u32 s22, 0
s_cbranch_scc1 .La5_LA_DC
s_cmp_eq_u32 s73, 1
s_nop 0
s_nop 0
s_cbranch_scc1 .La5_LA_F1Y
s_nop 0
v_cmp_eq_u32 vcc, v250, v249
s_mov_b32 s17, 0
s_mov_b32 s50, 16384
s_and_b32 s92, vcc_lo, 3
s_cmp_eq_u32 s92, 3
s_cbranch_scc0 .La5_LA_QSPIN
.La5_LA_QOK:
s_waitcnt lgkmcnt(4)
v_mfma_f32_16x16x32_bf16 v[192:195], a[64:67], v[128:131], 0
v_exp_f32 v221, v221
v_mfma_f32_16x16x32_bf16 v[192:195], a[68:71], v[132:135], v[192:195]
v_cvt_pk_bf16_f32 v238, v220, v221
s_add_u32 s85, s85, 1
v_mfma_f32_16x16x32_bf16 v[192:195], a[72:75], v[136:139], v[192:195]
v_exp_f32 v222, v222
v_mfma_f32_16x16x32_bf16 v[192:195], a[76:79], v[140:143], v[192:195]
buffer_load_dwordx4 v255, s[60:63], s17 offen offset:0 sc1 lds
v_mfma_f32_16x16x32_bf16 v[200:203], a[64:67], v[144:147], 0
v_exp_f32 v223, v223
v_mfma_f32_16x16x32_bf16 v[200:203], a[68:71], v[148:151], v[200:203]
v_cvt_pk_bf16_f32 v239, v222, v223
s_add_u32 s89, s89, 16384
v_mfma_f32_16x16x32_bf16 v[200:203], a[72:75], v[152:155], v[200:203]
v_exp_f32 v192, v192
v_mfma_f32_16x16x32_bf16 v[200:203], a[76:79], v[156:159], v[200:203]
s_waitcnt lgkmcnt(0)
s_and_b32 s89, s89, 0x1ffff
v_mfma_f32_16x16x32_bf16 v[196:199], a[80:83], v[128:131], 0
v_exp_f32 v193, v193
v_mfma_f32_16x16x32_bf16 v[196:199], a[84:87], v[132:135], v[196:199]
v_cvt_pk_bf16_f32 v224, v192, v193
v_mfma_f32_16x16x32_bf16 v[196:199], a[88:91], v[136:139], v[196:199]
v_exp_f32 v194, v194
v_mfma_f32_16x16x32_bf16 v[196:199], a[92:95], v[140:143], v[196:199]
buffer_load_dwordx4 v255, s[60:63], s17 offen offset:1024 sc1 lds
v_mfma_f32_16x16x32_bf16 v[204:207], a[80:83], v[144:147], 0
v_exp_f32 v195, v195
v_mfma_f32_16x16x32_bf16 v[204:207], a[84:87], v[148:151], v[204:207]
v_cvt_pk_bf16_f32 v225, v194, v195
v_mfma_f32_16x16x32_bf16 v[204:207], a[88:91], v[152:155], v[204:207]
v_exp_f32 v200, v200
v_mfma_f32_16x16x32_bf16 v[204:207], a[92:95], v[156:159], v[204:207]
buffer_load_dwordx4 v255, s[60:63], s17 offen offset:2048 sc1 lds
v_mfma_f32_16x16x32_bf16 v[208:211], a[64:67], v[160:163], 0
v_exp_f32 v201, v201
v_mfma_f32_16x16x32_bf16 v[208:211], a[68:71], v[164:167], v[208:211]
v_cvt_pk_bf16_f32 v228, v200, v201
v_mfma_f32_16x16x32_bf16 v[208:211], a[72:75], v[168:171], v[208:211]
v_exp_f32 v202, v202
v_mfma_f32_16x16x32_bf16 v[208:211], a[76:79], v[172:175], v[208:211]
buffer_load_dwordx4 v255, s[60:63], s17 offen offset:3072 sc1 lds
v_mfma_f32_16x16x32_bf16 v[216:219], a[64:67], v[176:179], 0
v_exp_f32 v203, v203
v_mfma_f32_16x16x32_bf16 v[216:219], a[68:71], v[180:183], v[216:219]
v_cvt_pk_bf16_f32 v229, v202, v203
v_mfma_f32_16x16x32_bf16 v[216:219], a[72:75], v[184:187], v[216:219]
v_exp_f32 v196, v196
v_mfma_f32_16x16x32_bf16 v[216:219], a[76:79], v[188:191], v[216:219]
buffer_load_dwordx4 a[0:3], v255, s[48:51], 0 offen offset:0 sc1
v_mfma_f32_16x16x32_bf16 v[212:215], a[80:83], v[160:163], 0
v_exp_f32 v197, v197
v_mfma_f32_16x16x32_bf16 v[212:215], a[84:87], v[164:167], v[212:215]
v_cvt_pk_bf16_f32 v226, v196, v197
s_add_u32 s17, s17, 8192
v_mfma_f32_16x16x32_bf16 v[212:215], a[88:91], v[168:171], v[212:215]
v_exp_f32 v198, v198
v_mfma_f32_16x16x32_bf16 v[212:215], a[92:95], v[172:175], v[212:215]
s_add_u32 m0, m0, 16384
s_and_b32 m0, m0, s78
buffer_load_dwordx4 a[4:7], v255, s[48:51], 0 offen offset:1024 sc1
v_mfma_f32_16x16x32_bf16 v[220:223], a[80:83], v[176:179], 0
v_exp_f32 v199, v199
v_mfma_f32_16x16x32_bf16 v[220:223], a[84:87], v[180:183], v[220:223]
v_cvt_pk_bf16_f32 v227, v198, v199
v_mfma_f32_16x16x32_bf16 v[220:223], a[88:91], v[184:187], v[220:223]
v_exp_f32 v204, v204
v_mfma_f32_16x16x32_bf16 v[220:223], a[92:95], v[188:191], v[220:223]
s_waitcnt vmcnt(12)
s_barrier
v_mfma_f32_16x16x32_bf16 v[64:67], a[96:99], v[232:235], v[64:67]
v_exp_f32 v205, v205
v_mfma_f32_16x16x32_bf16 v[96:99], a[96:99], v[236:239], v[96:99]
ds_read_b128 a[96:99], v248 offset:8192
v_cvt_pk_bf16_f32 v230, v204, v205
v_mfma_f32_16x16x32_bf16 v[68:71], a[100:103], v[232:235], v[68:71]
v_exp_f32 v206, v206
v_mfma_f32_16x16x32_bf16 v[100:103], a[100:103], v[236:239], v[100:103]
ds_read_b128 a[100:103], v248 offset:9216
buffer_load_dwordx4 a[8:11], v255, s[48:51], 0 offen offset:2048 sc1
v_mfma_f32_16x16x32_bf16 v[72:75], a[104:107], v[232:235], v[72:75]
v_exp_f32 v207, v207
v_mfma_f32_16x16x32_bf16 v[104:107], a[104:107], v[236:239], v[104:107]
ds_read_b128 a[104:107], v248 offset:10240
v_cvt_pk_bf16_f32 v231, v206, v207
v_mfma_f32_16x16x32_bf16 v[76:79], a[108:111], v[232:235], v[76:79]
v_exp_f32 v208, v208
v_mfma_f32_16x16x32_bf16 v[108:111], a[108:111], v[236:239], v[108:111]
ds_read_b128 a[108:111], v248 offset:11264
buffer_load_dwordx4 a[12:15], v255, s[48:51], 0 offen offset:3072 sc1
v_mfma_f32_16x16x32_bf16 v[80:83], a[112:115], v[232:235], v[80:83]
v_exp_f32 v209, v209
v_mfma_f32_16x16x32_bf16 v[112:115], a[112:115], v[236:239], v[112:115]
ds_read_b128 a[112:115], v248 offset:12288
v_mfma_f32_16x16x32_bf16 v[84:87], a[116:119], v[232:235], v[84:87]
v_exp_f32 v210, v210
v_mfma_f32_16x16x32_bf16 v[116:119], a[116:119], v[236:239], v[116:119]
ds_read_b128 a[116:119], v248 offset:13312
buffer_load_dwordx4 a[16:19], v255, s[48:51], s95 offen offset:0 sc1
v_mfma_f32_16x16x32_bf16 v[88:91], a[120:123], v[232:235], v[88:91]
v_exp_f32 v211, v211
v_mfma_f32_16x16x32_bf16 v[120:123], a[120:123], v[236:239], v[120:123]
ds_read_b128 a[120:123], v248 offset:14336
s_bitcmp1_b64 s[98:99], s85
v_mfma_f32_16x16x32_bf16 v[92:95], a[124:127], v[232:235], v[92:95]
v_exp_f32 v216, v216
v_mfma_f32_16x16x32_bf16 v[124:127], a[124:127], v[236:239], v[124:127]
ds_read_b128 a[124:127], v248 offset:15360
v_or_b32 v248, s89, v255
v_smfmac_f32_16x16x64_bf16 a[196:199], a[208:211], v[232:239], v241
s_waitcnt lgkmcnt(4)
v_smfmac_f32_16x16x64_bf16 a[192:195], a[208:211], v[224:231], v241
ds_read_b128 a[64:67], v248 offset:0
v_mfma_f32_16x16x32_bf16 v[0:3], a[96:99], v[224:227], v[0:3]
v_exp_f32 v217, v217
v_mfma_f32_16x16x32_bf16 v[32:35], a[96:99], v[228:231], v[32:35]
ds_read_b128 a[68:71], v248 offset:1024
v_cvt_pk_bf16_f32 v232, v208, v209
v_mfma_f32_16x16x32_bf16 v[4:7], a[100:103], v[224:227], v[4:7]
v_exp_f32 v218, v218
v_mfma_f32_16x16x32_bf16 v[36:39], a[100:103], v[228:231], v[36:39]
ds_read_b128 a[72:75], v248 offset:2048
v_cvt_pk_bf16_f32 v233, v210, v211
v_mfma_f32_16x16x32_bf16 v[8:11], a[104:107], v[224:227], v[8:11]
v_exp_f32 v219, v219
v_mfma_f32_16x16x32_bf16 v[40:43], a[104:107], v[228:231], v[40:43]
ds_read_b128 a[76:79], v248 offset:3072
v_cvt_pk_bf16_f32 v236, v216, v217
v_mfma_f32_16x16x32_bf16 v[12:15], a[108:111], v[224:227], v[12:15]
v_exp_f32 v212, v212
v_mfma_f32_16x16x32_bf16 v[44:47], a[108:111], v[228:231], v[44:47]
ds_read_b128 a[80:83], v248 offset:4096
s_waitcnt lgkmcnt(5)
v_mfma_f32_16x16x32_bf16 v[16:19], a[112:115], v[224:227], v[16:19]
v_exp_f32 v213, v213
v_mfma_f32_16x16x32_bf16 v[48:51], a[112:115], v[228:231], v[48:51]
ds_read_b128 a[84:87], v248 offset:5120
v_cvt_pk_bf16_f32 v237, v218, v219
v_mfma_f32_16x16x32_bf16 v[20:23], a[116:119], v[224:227], v[20:23]
v_exp_f32 v214, v214
v_mfma_f32_16x16x32_bf16 v[52:55], a[116:119], v[228:231], v[52:55]
ds_read_b128 a[88:91], v248 offset:6144
v_cvt_pk_bf16_f32 v234, v212, v213
v_mfma_f32_16x16x32_bf16 v[24:27], a[120:123], v[224:227], v[24:27]
v_exp_f32 v215, v215
v_mfma_f32_16x16x32_bf16 v[56:59], a[120:123], v[228:231], v[56:59]
ds_read_b128 a[92:95], v248 offset:7168
v_cvt_pk_bf16_f32 v235, v214, v215
v_mfma_f32_16x16x32_bf16 v[28:31], a[124:127], v[224:227], v[28:31]
v_exp_f32 v220, v220
v_mfma_f32_16x16x32_bf16 v[60:63], a[124:127], v[228:231], v[60:63]
buffer_load_dwordx4 a[20:23], v255, s[48:51], s95 offen offset:1024 sc1
s_waitcnt lgkmcnt(4)
v_mfma_f32_16x16x32_bf16 v[192:195], a[64:67], v[128:131], 0
v_exp_f32 v221, v221
v_mfma_f32_16x16x32_bf16 v[192:195], a[68:71], v[132:135], v[192:195]
v_cvt_pk_bf16_f32 v238, v220, v221
s_add_u32 s85, s85, 1
v_mfma_f32_16x16x32_bf16 v[192:195], a[72:75], v[136:139], v[192:195]
v_exp_f32 v222, v222
v_mfma_f32_16x16x32_bf16 v[192:195], a[76:79], v[140:143], v[192:195]
buffer_load_dwordx4 v255, s[60:63], s17 offen offset:0 sc1 lds
v_mfma_f32_16x16x32_bf16 v[200:203], a[64:67], v[144:147], 0
v_exp_f32 v223, v223
v_mfma_f32_16x16x32_bf16 v[200:203], a[68:71], v[148:151], v[200:203]
v_cvt_pk_bf16_f32 v239, v222, v223
s_add_u32 s89, s89, 16384
v_mfma_f32_16x16x32_bf16 v[200:203], a[72:75], v[152:155], v[200:203]
v_exp_f32 v192, v192
v_mfma_f32_16x16x32_bf16 v[200:203], a[76:79], v[156:159], v[200:203]
s_waitcnt lgkmcnt(0)
s_and_b32 s89, s89, 0x1ffff
buffer_load_dwordx4 a[24:27], v255, s[48:51], s95 offen offset:2048 sc1
v_mfma_f32_16x16x32_bf16 v[196:199], a[80:83], v[128:131], 0
v_exp_f32 v193, v193
v_mfma_f32_16x16x32_bf16 v[196:199], a[84:87], v[132:135], v[196:199]
v_cvt_pk_bf16_f32 v224, v192, v193
v_mfma_f32_16x16x32_bf16 v[196:199], a[88:91], v[136:139], v[196:199]
v_exp_f32 v194, v194
v_mfma_f32_16x16x32_bf16 v[196:199], a[92:95], v[140:143], v[196:199]
buffer_load_dwordx4 v255, s[60:63], s17 offen offset:1024 sc1 lds
v_mfma_f32_16x16x32_bf16 v[204:207], a[80:83], v[144:147], 0
v_exp_f32 v195, v195
v_mfma_f32_16x16x32_bf16 v[204:207], a[84:87], v[148:151], v[204:207]
v_cvt_pk_bf16_f32 v225, v194, v195
v_mfma_f32_16x16x32_bf16 v[204:207], a[88:91], v[152:155], v[204:207]
v_exp_f32 v200, v200
v_mfma_f32_16x16x32_bf16 v[204:207], a[92:95], v[156:159], v[204:207]
buffer_load_dwordx4 v255, s[60:63], s17 offen offset:2048 sc1 lds
v_mfma_f32_16x16x32_bf16 v[208:211], a[64:67], v[160:163], 0
v_exp_f32 v201, v201
v_mfma_f32_16x16x32_bf16 v[208:211], a[68:71], v[164:167], v[208:211]
v_cvt_pk_bf16_f32 v228, v200, v201
v_mfma_f32_16x16x32_bf16 v[208:211], a[72:75], v[168:171], v[208:211]
v_exp_f32 v202, v202
v_mfma_f32_16x16x32_bf16 v[208:211], a[76:79], v[172:175], v[208:211]
buffer_load_dwordx4 v255, s[60:63], s17 offen offset:3072 sc1 lds
v_mfma_f32_16x16x32_bf16 v[216:219], a[64:67], v[176:179], 0
v_exp_f32 v203, v203
v_mfma_f32_16x16x32_bf16 v[216:219], a[68:71], v[180:183], v[216:219]
v_cvt_pk_bf16_f32 v229, v202, v203
v_mfma_f32_16x16x32_bf16 v[216:219], a[72:75], v[184:187], v[216:219]
v_exp_f32 v196, v196
v_mfma_f32_16x16x32_bf16 v[216:219], a[76:79], v[188:191], v[216:219]
buffer_load_dwordx4 a[28:31], v255, s[48:51], s95 offen offset:3072 sc1
v_mfma_f32_16x16x32_bf16 v[212:215], a[80:83], v[160:163], 0
v_exp_f32 v197, v197
v_mfma_f32_16x16x32_bf16 v[212:215], a[84:87], v[164:167], v[212:215]
v_cvt_pk_bf16_f32 v226, v196, v197
s_add_u32 s17, s17, 8192
v_mfma_f32_16x16x32_bf16 v[212:215], a[88:91], v[168:171], v[212:215]
v_exp_f32 v198, v198
v_mfma_f32_16x16x32_bf16 v[212:215], a[92:95], v[172:175], v[212:215]
s_add_u32 m0, m0, 16384
s_and_b32 m0, m0, s78
buffer_load_dwordx4 a[32:35], v255, s[48:51], s96 offen offset:0 sc1
v_mfma_f32_16x16x32_bf16 v[220:223], a[80:83], v[176:179], 0
v_exp_f32 v199, v199
v_mfma_f32_16x16x32_bf16 v[220:223], a[84:87], v[180:183], v[220:223]
v_cvt_pk_bf16_f32 v227, v198, v199
v_mfma_f32_16x16x32_bf16 v[220:223], a[88:91], v[184:187], v[220:223]
v_exp_f32 v204, v204
v_mfma_f32_16x16x32_bf16 v[220:223], a[92:95], v[188:191], v[220:223]
s_waitcnt vmcnt(18)
s_barrier
v_mfma_f32_16x16x32_bf16 v[64:67], a[96:99], v[232:235], v[64:67]
v_exp_f32 v205, v205
v_mfma_f32_16x16x32_bf16 v[96:99], a[96:99], v[236:239], v[96:99]
ds_read_b128 a[96:99], v248 offset:8192
v_cvt_pk_bf16_f32 v230, v204, v205
v_mfma_f32_16x16x32_bf16 v[68:71], a[100:103], v[232:235], v[68:71]
v_exp_f32 v206, v206
v_mfma_f32_16x16x32_bf16 v[100:103], a[100:103], v[236:239], v[100:103]
ds_read_b128 a[100:103], v248 offset:9216
buffer_load_dwordx4 a[36:39], v255, s[48:51], s96 offen offset:1024 sc1
v_mfma_f32_16x16x32_bf16 v[72:75], a[104:107], v[232:235], v[72:75]
v_exp_f32 v207, v207
v_mfma_f32_16x16x32_bf16 v[104:107], a[104:107], v[236:239], v[104:107]
ds_read_b128 a[104:107], v248 offset:10240
v_cvt_pk_bf16_f32 v231, v206, v207
v_mfma_f32_16x16x32_bf16 v[76:79], a[108:111], v[232:235], v[76:79]
v_exp_f32 v208, v208
v_mfma_f32_16x16x32_bf16 v[108:111], a[108:111], v[236:239], v[108:111]
ds_read_b128 a[108:111], v248 offset:11264
buffer_load_dwordx4 a[40:43], v255, s[48:51], s96 offen offset:2048 sc1
v_mfma_f32_16x16x32_bf16 v[80:83], a[112:115], v[232:235], v[80:83]
v_exp_f32 v209, v209
v_mfma_f32_16x16x32_bf16 v[112:115], a[112:115], v[236:239], v[112:115]
ds_read_b128 a[112:115], v248 offset:12288
v_mfma_f32_16x16x32_bf16 v[84:87], a[116:119], v[232:235], v[84:87]
v_exp_f32 v210, v210
v_mfma_f32_16x16x32_bf16 v[116:119], a[116:119], v[236:239], v[116:119]
ds_read_b128 a[116:119], v248 offset:13312
buffer_load_dwordx4 a[44:47], v255, s[48:51], s96 offen offset:3072 sc1
v_mfma_f32_16x16x32_bf16 v[88:91], a[120:123], v[232:235], v[88:91]
v_exp_f32 v211, v211
v_mfma_f32_16x16x32_bf16 v[120:123], a[120:123], v[236:239], v[120:123]
ds_read_b128 a[120:123], v248 offset:14336
s_bitcmp1_b64 s[98:99], s85
v_mfma_f32_16x16x32_bf16 v[92:95], a[124:127], v[232:235], v[92:95]
v_exp_f32 v216, v216
v_mfma_f32_16x16x32_bf16 v[124:127], a[124:127], v[236:239], v[124:127]
ds_read_b128 a[124:127], v248 offset:15360
v_or_b32 v248, s89, v255
v_smfmac_f32_16x16x64_bf16 a[196:199], a[208:211], v[232:239], v241
s_waitcnt lgkmcnt(4)
v_smfmac_f32_16x16x64_bf16 a[192:195], a[208:211], v[224:231], v241
ds_read_b128 a[64:67], v248 offset:0
v_mfma_f32_16x16x32_bf16 v[0:3], a[96:99], v[224:227], v[0:3]
v_exp_f32 v217, v217
v_mfma_f32_16x16x32_bf16 v[32:35], a[96:99], v[228:231], v[32:35]
ds_read_b128 a[68:71], v248 offset:1024
v_cvt_pk_bf16_f32 v232, v208, v209
v_mfma_f32_16x16x32_bf16 v[4:7], a[100:103], v[224:227], v[4:7]
v_exp_f32 v218, v218
v_mfma_f32_16x16x32_bf16 v[36:39], a[100:103], v[228:231], v[36:39]
ds_read_b128 a[72:75], v248 offset:2048
v_cvt_pk_bf16_f32 v233, v210, v211
v_mfma_f32_16x16x32_bf16 v[8:11], a[104:107], v[224:227], v[8:11]
v_exp_f32 v219, v219
v_mfma_f32_16x16x32_bf16 v[40:43], a[104:107], v[228:231], v[40:43]
ds_read_b128 a[76:79], v248 offset:3072
v_cvt_pk_bf16_f32 v236, v216, v217
v_mfma_f32_16x16x32_bf16 v[12:15], a[108:111], v[224:227], v[12:15]
v_exp_f32 v212, v212
.La5_LW2_3:
s_cbranch_execz .La5_LW2_7
v_mfma_f32_16x16x32_bf16 v[44:47], a[108:111], v[228:231], v[44:47]
ds_read_b128 a[80:83], v248 offset:4096
s_waitcnt lgkmcnt(5)
v_mfma_f32_16x16x32_bf16 v[16:19], a[112:115], v[224:227], v[16:19]
v_exp_f32 v213, v213
v_mfma_f32_16x16x32_bf16 v[48:51], a[112:115], v[228:231], v[48:51]
ds_read_b128 a[84:87], v248 offset:5120
v_cvt_pk_bf16_f32 v237, v218, v219
v_mfma_f32_16x16x32_bf16 v[20:23], a[116:119], v[224:227], v[20:23]
v_exp_f32 v214, v214
v_mfma_f32_16x16x32_bf16 v[52:55], a[116:119], v[228:231], v[52:55]
ds_read_b128 a[88:91], v248 offset:6144
v_cvt_pk_bf16_f32 v234, v212, v213
v_mfma_f32_16x16x32_bf16 v[24:27], a[120:123], v[224:227], v[24:27]
v_exp_f32 v215, v215
v_mfma_f32_16x16x32_bf16 v[56:59], a[120:123], v[228:231], v[56:59]
ds_read_b128 a[92:95], v248 offset:7168
v_cvt_pk_bf16_f32 v235, v214, v215
v_mfma_f32_16x16x32_bf16 v[28:31], a[124:127], v[224:227], v[28:31]
v_exp_f32 v220, v220
v_mfma_f32_16x16x32_bf16 v[60:63], a[124:127], v[228:231], v[60:63]
buffer_load_dwordx4 a[48:51], v255, s[48:51], s97 offen offset:0 sc1
s_waitcnt lgkmcnt(4)
v_mfma_f32_16x16x32_bf16 v[192:195], a[64:67], v[128:131], 0
v_exp_f32 v221, v221
v_mfma_f32_16x16x32_bf16 v[192:195], a[68:71], v[132:135], v[192:195]
v_cvt_pk_bf16_f32 v238, v220, v221
s_add_u32 s85, s85, 1
v_mfma_f32_16x16x32_bf16 v[192:195], a[72:75], v[136:139], v[192:195]
v_exp_f32 v222, v222
v_mfma_f32_16x16x32_bf16 v[192:195], a[76:79], v[140:143], v[192:195]
buffer_load_dwordx4 v255, s[60:63], s17 offen offset:0 sc1 lds
v_mfma_f32_16x16x32_bf16 v[200:203], a[64:67], v[144:147], 0
v_exp_f32 v223, v223
v_mfma_f32_16x16x32_bf16 v[200:203], a[68:71], v[148:151], v[200:203]
v_cvt_pk_bf16_f32 v239, v222, v223
s_add_u32 s89, s89, 16384
v_mfma_f32_16x16x32_bf16 v[200:203], a[72:75], v[152:155], v[200:203]
v_exp_f32 v192, v192
v_mfma_f32_16x16x32_bf16 v[200:203], a[76:79], v[156:159], v[200:203]
s_waitcnt lgkmcnt(0)
s_and_b32 s89, s89, 0x1ffff
buffer_load_dwordx4 a[52:55], v255, s[48:51], s97 offen offset:1024 sc1
v_mfma_f32_16x16x32_bf16 v[196:199], a[80:83], v[128:131], 0
v_exp_f32 v193, v193
v_mfma_f32_16x16x32_bf16 v[196:199], a[84:87], v[132:135], v[196:199]
v_cvt_pk_bf16_f32 v224, v192, v193
v_mfma_f32_16x16x32_bf16 v[196:199], a[88:91], v[136:139], v[196:199]
v_exp_f32 v194, v194
v_mfma_f32_16x16x32_bf16 v[196:199], a[92:95], v[140:143], v[196:199]
buffer_load_dwordx4 v255, s[60:63], s17 offen offset:1024 sc1 lds
v_mfma_f32_16x16x32_bf16 v[204:207], a[80:83], v[144:147], 0
v_exp_f32 v195, v195
v_mfma_f32_16x16x32_bf16 v[204:207], a[84:87], v[148:151], v[204:207]
v_cvt_pk_bf16_f32 v225, v194, v195
v_mfma_f32_16x16x32_bf16 v[204:207], a[88:91], v[152:155], v[204:207]
v_exp_f32 v200, v200
v_mfma_f32_16x16x32_bf16 v[204:207], a[92:95], v[156:159], v[204:207]
buffer_load_dwordx4 v255, s[60:63], s17 offen offset:2048 sc1 lds
v_mfma_f32_16x16x32_bf16 v[208:211], a[64:67], v[160:163], 0
v_exp_f32 v201, v201
v_mfma_f32_16x16x32_bf16 v[208:211], a[68:71], v[164:167], v[208:211]
v_cvt_pk_bf16_f32 v228, v200, v201
v_mfma_f32_16x16x32_bf16 v[208:211], a[72:75], v[168:171], v[208:211]
v_exp_f32 v202, v202
v_mfma_f32_16x16x32_bf16 v[208:211], a[76:79], v[172:175], v[208:211]
buffer_load_dwordx4 v255, s[60:63], s17 offen offset:3072 sc1 lds
v_mfma_f32_16x16x32_bf16 v[216:219], a[64:67], v[176:179], 0
v_exp_f32 v203, v203
v_mfma_f32_16x16x32_bf16 v[216:219], a[68:71], v[180:183], v[216:219]
v_cvt_pk_bf16_f32 v229, v202, v203
v_mfma_f32_16x16x32_bf16 v[216:219], a[72:75], v[184:187], v[216:219]
v_exp_f32 v196, v196
v_mfma_f32_16x16x32_bf16 v[216:219], a[76:79], v[188:191], v[216:219]
buffer_load_dwordx4 a[56:59], v255, s[48:51], s97 offen offset:2048 sc1
v_mfma_f32_16x16x32_bf16 v[212:215], a[80:83], v[160:163], 0
v_exp_f32 v197, v197
v_mfma_f32_16x16x32_bf16 v[212:215], a[84:87], v[164:167], v[212:215]
v_cvt_pk_bf16_f32 v226, v196, v197
s_add_u32 s17, s17, 8192
v_mfma_f32_16x16x32_bf16 v[212:215], a[88:91], v[168:171], v[212:215]
v_exp_f32 v198, v198
v_mfma_f32_16x16x32_bf16 v[212:215], a[92:95], v[172:175], v[212:215]
s_add_u32 m0, m0, 16384
s_and_b32 m0, m0, s78
buffer_load_dwordx4 a[60:63], v255, s[48:51], s97 offen offset:3072 sc1
v_mfma_f32_16x16x32_bf16 v[220:223], a[80:83], v[176:179], 0
v_exp_f32 v199, v199
v_mfma_f32_16x16x32_bf16 v[220:223], a[84:87], v[180:183], v[220:223]
v_cvt_pk_bf16_f32 v227, v198, v199
v_mfma_f32_16x16x32_bf16 v[220:223], a[88:91], v[184:187], v[220:223]
v_exp_f32 v204, v204
v_mfma_f32_16x16x32_bf16 v[220:223], a[92:95], v[188:191], v[220:223]
s_waitcnt vmcnt(24)
s_barrier
v_mfma_f32_16x16x32_bf16 v[64:67], a[96:99], v[232:235], v[64:67]
v_exp_f32 v205, v205
v_mfma_f32_16x16x32_bf16 v[96:99], a[96:99], v[236:239], v[96:99]
ds_read_b128 a[96:99], v248 offset:8192
v_cvt_pk_bf16_f32 v230, v204, v205
v_mfma_f32_16x16x32_bf16 v[68:71], a[100:103], v[232:235], v[68:71]
v_exp_f32 v206, v206
v_mfma_f32_16x16x32_bf16 v[100:103], a[100:103], v[236:239], v[100:103]
ds_read_b128 a[100:103], v248 offset:9216
v_mfma_f32_16x16x32_bf16 v[72:75], a[104:107], v[232:235], v[72:75]
v_exp_f32 v207, v207
v_mfma_f32_16x16x32_bf16 v[104:107], a[104:107], v[236:239], v[104:107]
ds_read_b128 a[104:107], v248 offset:10240
v_cvt_pk_bf16_f32 v231, v206, v207
v_mfma_f32_16x16x32_bf16 v[76:79], a[108:111], v[232:235], v[76:79]
v_exp_f32 v208, v208
v_mfma_f32_16x16x32_bf16 v[108:111], a[108:111], v[236:239], v[108:111]
ds_read_b128 a[108:111], v248 offset:11264
v_mfma_f32_16x16x32_bf16 v[80:83], a[112:115], v[232:235], v[80:83]
v_exp_f32 v209, v209
v_mfma_f32_16x16x32_bf16 v[112:115], a[112:115], v[236:239], v[112:115]
ds_read_b128 a[112:115], v248 offset:12288
v_mfma_f32_16x16x32_bf16 v[84:87], a[116:119], v[232:235], v[84:87]
v_exp_f32 v210, v210
v_mfma_f32_16x16x32_bf16 v[116:119], a[116:119], v[236:239], v[116:119]
ds_read_b128 a[116:119], v248 offset:13312
v_mfma_f32_16x16x32_bf16 v[88:91], a[120:123], v[232:235], v[88:91]
v_exp_f32 v211, v211
v_mfma_f32_16x16x32_bf16 v[120:123], a[120:123], v[236:239], v[120:123]
ds_read_b128 a[120:123], v248 offset:14336
s_bitcmp1_b64 s[98:99], s85
v_mfma_f32_16x16x32_bf16 v[92:95], a[124:127], v[232:235], v[92:95]
v_exp_f32 v216, v216
v_mfma_f32_16x16x32_bf16 v[124:127], a[124:127], v[236:239], v[124:127]
ds_read_b128 a[124:127], v248 offset:15360
v_or_b32 v248, s89, v255
v_smfmac_f32_16x16x64_bf16 a[196:199], a[208:211], v[232:239], v241
s_waitcnt lgkmcnt(4)
v_smfmac_f32_16x16x64_bf16 a[192:195], a[208:211], v[224:231], v241
ds_read_b128 a[64:67], v248 offset:0
v_mfma_f32_16x16x32_bf16 v[0:3], a[96:99], v[224:227], v[0:3]
v_exp_f32 v217, v217
v_mfma_f32_16x16x32_bf16 v[32:35], a[96:99], v[228:231], v[32:35]
ds_read_b128 a[68:71], v248 offset:1024
v_cvt_pk_bf16_f32 v232, v208, v209
v_mfma_f32_16x16x32_bf16 v[4:7], a[100:103], v[224:227], v[4:7]
v_exp_f32 v218, v218
v_mfma_f32_16x16x32_bf16 v[36:39], a[100:103], v[228:231], v[36:39]
ds_read_b128 a[72:75], v248 offset:2048
v_cvt_pk_bf16_f32 v233, v210, v211
v_mfma_f32_16x16x32_bf16 v[8:11], a[104:107], v[224:227], v[8:11]
v_exp_f32 v219, v219
v_mfma_f32_16x16x32_bf16 v[40:43], a[104:107], v[228:231], v[40:43]
ds_read_b128 a[76:79], v248 offset:3072
v_cvt_pk_bf16_f32 v236, v216, v217
v_mfma_f32_16x16x32_bf16 v[12:15], a[108:111], v[224:227], v[12:15]
v_exp_f32 v212, v212
v_mfma_f32_16x16x32_bf16 v[44:47], a[108:111], v[228:231], v[44:47]
ds_read_b128 a[80:83], v248 offset:4096
s_waitcnt lgkmcnt(5)
v_mfma_f32_16x16x32_bf16 v[16:19], a[112:115], v[224:227], v[16:19]
v_exp_f32 v213, v213
v_mfma_f32_16x16x32_bf16 v[48:51], a[112:115], v[228:231], v[48:51]
ds_read_b128 a[84:87], v248 offset:5120
v_cvt_pk_bf16_f32 v237, v218, v219
v_mfma_f32_16x16x32_bf16 v[20:23], a[116:119], v[224:227], v[20:23]
v_exp_f32 v214, v214
v_mfma_f32_16x16x32_bf16 v[52:55], a[116:119], v[228:231], v[52:55]
ds_read_b128 a[88:91], v248 offset:6144
v_cvt_pk_bf16_f32 v234, v212, v213
v_mfma_f32_16x16x32_bf16 v[24:27], a[120:123], v[224:227], v[24:27]
v_exp_f32 v215, v215
v_mfma_f32_16x16x32_bf16 v[56:59], a[120:123], v[228:231], v[56:59]
ds_read_b128 a[92:95], v248 offset:7168
v_cvt_pk_bf16_f32 v235, v214, v215
v_mfma_f32_16x16x32_bf16 v[28:31], a[124:127], v[224:227], v[28:31]
v_exp_f32 v220, v220
v_mfma_f32_16x16x32_bf16 v[60:63], a[124:127], v[228:231], v[60:63]
.La5_LA_F1Y:
.La5_LW2_4:
s_cbranch_execz .La5_LW2_8
s_waitcnt lgkmcnt(4)
s_waitcnt vmcnt(0)
v_mfma_f32_16x16x32_bf16 v[192:195], a[64:67], a[0:3], 0
v_exp_f32 v221, v221
v_mfma_f32_16x16x32_bf16 v[192:195], a[68:71], a[4:7], v[192:195]
v_cvt_pk_bf16_f32 v238, v220, v221
s_add_u32 s89, s89, 16384
v_mfma_f32_16x16x32_bf16 v[192:195], a[72:75], a[8:11], v[192:195]
v_exp_f32 v222, v222
v_mfma_f32_16x16x32_bf16 v[192:195], a[76:79], a[12:15], v[192:195]
buffer_load_dwordx4 v255, s[60:63], s17 offen offset:0 sc1 lds
v_accvgpr_read_b32 v242, a192
v_accvgpr_read_b32 v240, a193
v_add_f32 v242, v242, v240
v_mfma_f32_16x16x32_bf16 v[200:203], a[64:67], a[16:19], 0
v_exp_f32 v223, v223
v_accvgpr_read_b32 v243, a194
v_accvgpr_read_b32 v240, a195
v_add_f32 v243, v243, v240
v_mfma_f32_16x16x32_bf16 v[200:203], a[68:71], a[20:23], v[200:203]
v_cvt_pk_bf16_f32 v239, v222, v223
s_and_b32 s89, s89, 0x1ffff
v_accvgpr_write_b32 a192, 0
v_accvgpr_write_b32 a193, 0
v_accvgpr_write_b32 a194, 0
v_mfma_f32_16x16x32_bf16 v[200:203], a[72:75], a[24:27], v[200:203]
v_exp_f32 v192, v192
v_accvgpr_write_b32 a195, 0
v_subrev_u32 v240, 562036736, v242
v_mfma_f32_16x16x32_bf16 v[200:203], a[76:79], a[28:31], v[200:203]
s_waitcnt lgkmcnt(0)
v_rcp_f32 v242, v242
v_cmp_le_u32 vcc, 1006632960, v240
v_subrev_u32 v240, 562036736, v243
v_mfma_f32_16x16x32_bf16 v[196:199], a[80:83], a[0:3], 0
v_exp_f32 v193, v193
s_mov_b64 s[92:93], vcc
v_cmp_le_u32 vcc, 1006632960, v240
v_mfma_f32_16x16x32_bf16 v[196:199], a[84:87], a[4:7], v[196:199]
v_cvt_pk_bf16_f32 v224, v192, v193
v_rcp_f32_e64 v243, v243
v_mfma_f32_16x16x32_bf16 v[196:199], a[88:91], a[8:11], v[196:199]
v_exp_f32_e64 v194, v194
s_or_b64 vcc, vcc, s[92:93]
s_cbranch_scc1 .La5_LA_SC1
.La5_LA_SR1:
v_mul_f32 v0, v0, v242
v_mfma_f32_16x16x32_bf16 v[196:199], a[92:95], a[12:15], v[196:199]
buffer_load_dwordx4 v255, s[60:63], s17 offen offset:1024 sc1 lds
v_mul_f32 v1, v1, v242
v_mul_f32 v2, v2, v242
v_mul_f32 v3, v3, v242
v_mfma_f32_16x16x32_bf16 v[204:207], a[80:83], a[16:19], 0
v_exp_f32 v195, v195
v_mul_f32 v4, v4, v242
v_mul_f32 v5, v5, v242
v_mul_f32 v6, v6, v242
v_mfma_f32_16x16x32_bf16 v[204:207], a[84:87], a[20:23], v[204:207]
v_cvt_pk_bf16_f32 v225, v194, v195
v_mul_f32 v7, v7, v242
v_cvt_pk_bf16_f32 v128, v0, v1
v_cvt_pk_bf16_f32 v129, v2, v3
v_cvt_pk_bf16_f32 v130, v4, v5
v_mfma_f32_16x16x32_bf16 v[204:207], a[88:91], a[24:27], v[204:207]
v_exp_f32 v200, v200
v_cvt_pk_bf16_f32 v131, v6, v7
v_mul_f32 v8, v8, v242
v_mul_f32 v9, v9, v242
v_mfma_f32_16x16x32_bf16 v[204:207], a[92:95], a[28:31], v[204:207]
buffer_load_dwordx4 v255, s[60:63], s17 offen offset:2048 sc1 lds
v_mul_f32 v10, v10, v242
v_mul_f32 v11, v11, v242
v_mul_f32 v12, v12, v242
v_mfma_f32_16x16x32_bf16 v[208:211], a[64:67], a[32:35], 0
v_exp_f32 v201, v201
v_mul_f32 v13, v13, v242
v_mul_f32 v14, v14, v242
v_mul_f32 v15, v15, v242
v_mfma_f32_16x16x32_bf16 v[208:211], a[68:71], a[36:39], v[208:211]
v_cvt_pk_bf16_f32 v228, v200, v201
v_cvt_pk_bf16_f32 v132, v8, v9
v_cvt_pk_bf16_f32 v133, v10, v11
v_cvt_pk_bf16_f32 v134, v12, v13
v_cvt_pk_bf16_f32 v135, v14, v15
v_mfma_f32_16x16x32_bf16 v[208:211], a[72:75], a[40:43], v[208:211]
v_exp_f32 v202, v202
v_mul_f32 v16, v16, v242
v_mul_f32 v17, v17, v242
v_mul_f32 v18, v18, v242
v_mfma_f32_16x16x32_bf16 v[208:211], a[76:79], a[44:47], v[208:211]
buffer_load_dwordx4 v255, s[60:63], s17 offen offset:3072 sc1 lds
v_mul_f32 v19, v19, v242
v_mul_f32 v20, v20, v242
v_mul_f32 v21, v21, v242
v_mfma_f32_16x16x32_bf16 v[216:219], a[64:67], a[48:51], 0
v_exp_f32 v203, v203
v_mul_f32 v22, v22, v242
v_mul_f32 v23, v23, v242
v_cvt_pk_bf16_f32 v136, v16, v17
v_mfma_f32_16x16x32_bf16 v[216:219], a[68:71], a[52:55], v[216:219]
v_cvt_pk_bf16_f32 v229, v202, v203
v_cvt_pk_bf16_f32 v137, v18, v19
v_cvt_pk_bf16_f32 v138, v20, v21
v_cvt_pk_bf16_f32 v139, v22, v23
v_mul_f32 v24, v24, v242
v_mfma_f32_16x16x32_bf16 v[216:219], a[72:75], a[56:59], v[216:219]
v_exp_f32 v196, v196
v_mul_f32 v25, v25, v242
v_mul_f32 v26, v26, v242
v_mul_f32 v27, v27, v242
v_mfma_f32_16x16x32_bf16 v[216:219], a[76:79], a[60:63], v[216:219]
v_mul_f32 v28, v28, v242
v_mul_f32 v29, v29, v242
v_mul_f32 v30, v30, v242
v_mul_f32 v31, v31, v242
v_cvt_pk_bf16_f32 v140, v24, v25
v_mfma_f32_16x16x32_bf16 v[212:215], a[80:83], a[32:35], 0
v_exp_f32 v197, v197
s_add_u32 s17, s17, 8192
v_cvt_pk_bf16_f32 v141, v26, v27
v_cvt_pk_bf16_f32 v142, v28, v29
v_mfma_f32_16x16x32_bf16 v[212:215], a[84:87], a[36:39], v[212:215]
v_cvt_pk_bf16_f32 v226, v196, v197
s_add_u32 m0, m0, 16384
s_and_b32 m0, m0, s78
v_cvt_pk_bf16_f32 v143, v30, v31
v_mul_f32 v32, v32, v243
v_mfma_f32_16x16x32_bf16 v[212:215], a[88:91], a[40:43], v[212:215]
v_exp_f32 v198, v198
v_mul_f32 v33, v33, v243
v_mul_f32 v34, v34, v243
v_mul_f32 v35, v35, v243
v_mfma_f32_16x16x32_bf16 v[212:215], a[92:95], a[44:47], v[212:215]
buffer_store_dwordx4 v[128:131], v244, s[44:47], s91 offen offset:0 sc1
v_mul_f32 v36, v36, v243
v_mul_f32 v37, v37, v243
v_mul_f32 v38, v38, v243
v_mfma_f32_16x16x32_bf16 v[220:223], a[80:83], a[48:51], 0
v_exp_f32 v199, v199
v_mul_f32 v39, v39, v243
v_cvt_pk_bf16_f32 v144, v32, v33
v_cvt_pk_bf16_f32 v145, v34, v35
v_mfma_f32_16x16x32_bf16 v[220:223], a[84:87], a[52:55], v[220:223]
v_cvt_pk_bf16_f32 v227, v198, v199
v_cvt_pk_bf16_f32 v146, v36, v37
v_cvt_pk_bf16_f32 v147, v38, v39
v_mul_f32 v40, v40, v243
v_mul_f32 v41, v41, v243
v_mfma_f32_16x16x32_bf16 v[220:223], a[88:91], a[56:59], v[220:223]
v_exp_f32 v204, v204
v_mul_f32 v42, v42, v243
v_mul_f32 v43, v43, v243
v_mul_f32 v44, v44, v243
v_mfma_f32_16x16x32_bf16 v[220:223], a[92:95], a[60:63], v[220:223]
s_nop 0
s_barrier
v_mul_f32 v45, v45, v243
v_mul_f32 v46, v46, v243
v_mul_f32 v47, v47, v243
v_mfma_f32_16x16x32_bf16 v[64:67], a[96:99], v[232:235], v[64:67]
v_exp_f32 v205, v205
v_cvt_pk_bf16_f32 v148, v40, v41
v_cvt_pk_bf16_f32 v149, v42, v43
v_cvt_pk_bf16_f32 v150, v44, v45
v_mfma_f32_16x16x32_bf16 v[96:99], a[96:99], v[236:239], v[96:99]
ds_read_b128 a[96:99], v248 offset:8192
v_cvt_pk_bf16_f32 v230, v204, v205
v_cvt_pk_bf16_f32 v151, v46, v47
v_mul_f32 v48, v48, v243
v_mul_f32 v49, v49, v243
v_mfma_f32_16x16x32_bf16 v[68:71], a[100:103], v[232:235], v[68:71]
v_exp_f32 v206, v206
v_mul_f32 v50, v50, v243
v_mul_f32 v51, v51, v243
v_mul_f32 v52, v52, v243
v_mfma_f32_16x16x32_bf16 v[100:103], a[100:103], v[236:239], v[100:103]
ds_read_b128 a[100:103], v248 offset:9216
buffer_store_dwordx4 v[132:135], v244, s[44:47], s91 offen offset:1024 sc1
v_mul_f32 v53, v53, v243
v_mul_f32 v54, v54, v243
v_mfma_f32_16x16x32_bf16 v[72:75], a[104:107], v[232:235], v[72:75]
v_exp_f32 v207, v207
v_mul_f32 v55, v55, v243
v_cvt_pk_bf16_f32 v152, v48, v49
v_cvt_pk_bf16_f32 v153, v50, v51
v_mfma_f32_16x16x32_bf16 v[104:107], a[104:107], v[236:239], v[104:107]
ds_read_b128 a[104:107], v248 offset:10240
v_cvt_pk_bf16_f32 v231, v206, v207
v_cvt_pk_bf16_f32 v154, v52, v53
v_cvt_pk_bf16_f32 v155, v54, v55
v_mul_f32 v56, v56, v243
v_mfma_f32_16x16x32_bf16 v[76:79], a[108:111], v[232:235], v[76:79]
v_exp_f32 v208, v208
v_mul_f32 v57, v57, v243
v_mul_f32 v58, v58, v243
v_mul_f32 v59, v59, v243
v_mfma_f32_16x16x32_bf16 v[108:111], a[108:111], v[236:239], v[108:111]
ds_read_b128 a[108:111], v248 offset:11264
v_mul_f32 v60, v60, v243
v_mul_f32 v61, v61, v243
v_mul_f32 v62, v62, v243
v_mul_f32 v63, v63, v243
v_mfma_f32_16x16x32_bf16 v[80:83], a[112:115], v[232:235], v[80:83]
v_exp_f32 v209, v209
v_cvt_pk_bf16_f32 v156, v56, v57
v_cvt_pk_bf16_f32 v157, v58, v59
v_cvt_pk_bf16_f32 v158, v60, v61
v_mfma_f32_16x16x32_bf16 v[112:115], a[112:115], v[236:239], v[112:115]
ds_read_b128 a[112:115], v248 offset:12288
buffer_store_dwordx4 v[136:139], v244, s[44:47], s91 offen offset:2048 sc1
v_cvt_pk_bf16_f32 v159, v62, v63
v_mfma_f32_16x16x32_bf16 v[84:87], a[116:119], v[232:235], v[84:87]
v_exp_f32 v210, v210
v_mfma_f32_16x16x32_bf16 v[116:119], a[116:119], v[236:239], v[116:119]
ds_read_b128 a[116:119], v248 offset:13312
v_mfma_f32_16x16x32_bf16 v[88:91], a[120:123], v[232:235], v[88:91]
v_exp_f32 v211, v211
v_mfma_f32_16x16x32_bf16 v[120:123], a[120:123], v[236:239], v[120:123]
ds_read_b128 a[120:123], v248 offset:14336
buffer_store_dwordx4 v[140:143], v244, s[44:47], s91 offen offset:3072 sc1
v_mfma_f32_16x16x32_bf16 v[92:95], a[124:127], v[232:235], v[92:95]
v_exp_f32 v216, v216
v_mfma_f32_16x16x32_bf16 v[124:127], a[124:127], v[236:239], v[124:127]
ds_read_b128 a[124:127], v248 offset:15360
v_or_b32 v248, s89, v255
v_smfmac_f32_16x16x64_bf16 a[196:199], a[208:211], v[232:239], v241
s_waitcnt lgkmcnt(4)
v_smfmac_f32_16x16x64_bf16 a[192:195], a[208:211], v[224:231], v241
ds_read_b128 a[64:67], v248 offset:0
v_mfma_f32_16x16x32_bf16 v[0:3], a[96:99], v[224:227], 0
v_exp_f32 v217, v217
v_mfma_f32_16x16x32_bf16 v[32:35], a[96:99], v[228:231], 0
ds_read_b128 a[68:71], v248 offset:1024
v_cvt_pk_bf16_f32 v232, v208, v209
v_accvgpr_read_b32 v242, a196
v_accvgpr_read_b32 v240, a197
v_add_f32 v242, v242, v240
v_mfma_f32_16x16x32_bf16 v[4:7], a[100:103], v[224:227], 0
v_exp_f32 v218, v218
v_accvgpr_read_b32 v243, a198
v_accvgpr_read_b32 v240, a199
v_add_f32 v243, v243, v240
v_mfma_f32_16x16x32_bf16 v[36:39], a[100:103], v[228:231], 0
ds_read_b128 a[72:75], v248 offset:2048
v_cvt_pk_bf16_f32 v233, v210, v211
buffer_store_dwordx4 v[144:147], v245, s[44:47], s91 offen offset:0 sc1
v_accvgpr_write_b32 a196, 0
v_mfma_f32_16x16x32_bf16 v[8:11], a[104:107], v[224:227], 0
v_exp_f32 v219, v219
v_accvgpr_write_b32 a197, 0
v_accvgpr_write_b32 a198, 0
v_accvgpr_write_b32 a199, 0
v_mfma_f32_16x16x32_bf16 v[40:43], a[104:107], v[228:231], 0
ds_read_b128 a[76:79], v248 offset:3072
v_cvt_pk_bf16_f32 v236, v216, v217
v_subrev_u32 v240, 562036736, v242
v_rcp_f32 v242, v242
v_mfma_f32_16x16x32_bf16 v[12:15], a[108:111], v[224:227], 0
v_exp_f32 v212, v212
v_cmp_le_u32 vcc, 1006632960, v240
v_subrev_u32 v240, 562036736, v243
s_mov_b64 s[92:93], vcc
v_mfma_f32_16x16x32_bf16 v[44:47], a[108:111], v[228:231], 0
ds_read_b128 a[80:83], v248 offset:4096
s_waitcnt lgkmcnt(5)
v_cmp_le_u32 vcc, 1006632960, v240
v_rcp_f32_e64 v243, v243
v_mfma_f32_16x16x32_bf16 v[16:19], a[112:115], v[224:227], 0
v_exp_f32_e64 v213, v213
s_or_b64 vcc, vcc, s[92:93]
s_cbranch_scc1 .La5_LA_SC2
.La5_LA_SR2:
v_mul_f32 v64, v64, v242
v_mfma_f32_16x16x32_bf16 v[48:51], a[112:115], v[228:231], 0
ds_read_b128 a[84:87], v248 offset:5120
v_cvt_pk_bf16_f32 v237, v218, v219
v_mul_f32 v65, v65, v242
v_mul_f32 v66, v66, v242
v_mul_f32 v67, v67, v242
v_mfma_f32_16x16x32_bf16 v[20:23], a[116:119], v[224:227], 0
v_exp_f32 v214, v214
v_mul_f32 v68, v68, v242
v_mul_f32 v69, v69, v242
v_mul_f32 v70, v70, v242
v_mfma_f32_16x16x32_bf16 v[52:55], a[116:119], v[228:231], 0
ds_read_b128 a[88:91], v248 offset:6144
v_cvt_pk_bf16_f32 v234, v212, v213
buffer_store_dwordx4 v[148:151], v245, s[44:47], s91 offen offset:1024 sc1
v_mul_f32 v71, v71, v242
v_mfma_f32_16x16x32_bf16 v[24:27], a[120:123], v[224:227], 0
v_exp_f32 v215, v215
v_cvt_pk_bf16_f32 v160, v64, v65
v_cvt_pk_bf16_f32 v161, v66, v67
v_cvt_pk_bf16_f32 v162, v68, v69
v_mfma_f32_16x16x32_bf16 v[56:59], a[120:123], v[228:231], 0
ds_read_b128 a[92:95], v248 offset:7168
v_cvt_pk_bf16_f32 v235, v214, v215
v_cvt_pk_bf16_f32 v163, v70, v71
v_mul_f32 v72, v72, v242
v_mul_f32 v73, v73, v242
v_mfma_f32_16x16x32_bf16 v[28:31], a[124:127], v[224:227], 0
v_exp_f32 v220, v220
v_mul_f32 v74, v74, v242
v_mul_f32 v75, v75, v242
v_mul_f32 v76, v76, v242
v_mfma_f32_16x16x32_bf16 v[60:63], a[124:127], v[228:231], 0
s_add_u32 s85, s85, 1
v_mul_f32 v77, v77, v242
v_mul_f32 v78, v78, v242
v_mul_f32 v79, v79, v242
v_cvt_pk_bf16_f32 v164, v72, v73
s_waitcnt lgkmcnt(4)
v_mfma_f32_16x16x32_bf16 v[192:195], a[64:67], a[0:3], 0
v_exp_f32 v221, v221
v_cvt_pk_bf16_f32 v165, v74, v75
v_cvt_pk_bf16_f32 v166, v76, v77
v_cvt_pk_bf16_f32 v167, v78, v79
v_mfma_f32_16x16x32_bf16 v[192:195], a[68:71], a[4:7], v[192:195]
v_cvt_pk_bf16_f32 v238, v220, v221
s_add_u32 s89, s89, 16384
v_mul_f32 v80, v80, v242
v_mul_f32 v81, v81, v242
v_mul_f32 v82, v82, v242
v_mfma_f32_16x16x32_bf16 v[192:195], a[72:75], a[8:11], v[192:195]
v_exp_f32 v222, v222
v_mul_f32 v83, v83, v242
v_mul_f32 v84, v84, v242
v_mul_f32 v85, v85, v242
v_mfma_f32_16x16x32_bf16 v[192:195], a[76:79], a[12:15], v[192:195]
buffer_load_dwordx4 v255, s[60:63], s17 offen offset:0 sc1 lds
v_mul_f32 v86, v86, v242
v_mul_f32 v87, v87, v242
v_cvt_pk_bf16_f32 v168, v80, v81
v_mfma_f32_16x16x32_bf16 v[200:203], a[64:67], a[16:19], 0
v_exp_f32 v223, v223
v_cvt_pk_bf16_f32 v169, v82, v83
v_cvt_pk_bf16_f32 v170, v84, v85
v_cvt_pk_bf16_f32 v171, v86, v87
v_mfma_f32_16x16x32_bf16 v[200:203], a[68:71], a[20:23], v[200:203]
v_cvt_pk_bf16_f32 v239, v222, v223
s_and_b32 s89, s89, 0x1ffff
v_mul_f32 v88, v88, v242
v_mul_f32 v89, v89, v242
v_mul_f32 v90, v90, v242
v_mfma_f32_16x16x32_bf16 v[200:203], a[72:75], a[24:27], v[200:203]
v_exp_f32 v192, v192
v_mul_f32 v91, v91, v242
v_mul_f32 v92, v92, v242
v_mul_f32 v93, v93, v242
v_mfma_f32_16x16x32_bf16 v[200:203], a[76:79], a[28:31], v[200:203]
s_waitcnt lgkmcnt(0)
buffer_store_dwordx4 v[152:155], v245, s[44:47], s91 offen offset:2048 sc1
v_mul_f32 v94, v94, v242
v_mul_f32 v95, v95, v242
v_mfma_f32_16x16x32_bf16 v[196:199], a[80:83], a[0:3], 0
v_exp_f32 v193, v193
v_cvt_pk_bf16_f32 v172, v88, v89
v_cvt_pk_bf16_f32 v173, v90, v91
v_cvt_pk_bf16_f32 v174, v92, v93
v_mfma_f32_16x16x32_bf16 v[196:199], a[84:87], a[4:7], v[196:199]
v_cvt_pk_bf16_f32 v224, v192, v193
v_cvt_pk_bf16_f32 v175, v94, v95
v_mul_f32 v96, v96, v243
v_mul_f32 v97, v97, v243
v_mul_f32 v98, v98, v243
v_mfma_f32_16x16x32_bf16 v[196:199], a[88:91], a[8:11], v[196:199]
v_exp_f32 v194, v194
v_mul_f32 v99, v99, v243
v_mul_f32 v100, v100, v243
v_mul_f32 v101, v101, v243
v_mfma_f32_16x16x32_bf16 v[196:199], a[92:95], a[12:15], v[196:199]
buffer_load_dwordx4 v255, s[60:63], s17 offen offset:1024 sc1 lds
v_mul_f32 v102, v102, v243
v_mul_f32 v103, v103, v243
v_cvt_pk_bf16_f32 v176, v96, v97
v_mfma_f32_16x16x32_bf16 v[204:207], a[80:83], a[16:19], 0
v_exp_f32 v195, v195
v_cvt_pk_bf16_f32 v177, v98, v99
v_cvt_pk_bf16_f32 v178, v100, v101
v_cvt_pk_bf16_f32 v179, v102, v103
v_mfma_f32_16x16x32_bf16 v[204:207], a[84:87], a[20:23], v[204:207]
v_cvt_pk_bf16_f32 v225, v194, v195
buffer_store_dwordx4 v[156:159], v245, s[44:47], s91 offen offset:3072 sc1
v_mul_f32 v104, v104, v243
v_mul_f32 v105, v105, v243
v_mfma_f32_16x16x32_bf16 v[204:207], a[88:91], a[24:27], v[204:207]
v_exp_f32 v200, v200
v_mul_f32 v106, v106, v243
v_mul_f32 v107, v107, v243
v_mul_f32 v108, v108, v243
v_mfma_f32_16x16x32_bf16 v[204:207], a[92:95], a[28:31], v[204:207]
buffer_load_dwordx4 v255, s[60:63], s17 offen offset:2048 sc1 lds
v_mul_f32 v109, v109, v243
v_mul_f32 v110, v110, v243
v_mul_f32 v111, v111, v243
v_mfma_f32_16x16x32_bf16 v[208:211], a[64:67], a[32:35], 0
v_exp_f32 v201, v201
v_cvt_pk_bf16_f32 v180, v104, v105
v_cvt_pk_bf16_f32 v181, v106, v107
v_cvt_pk_bf16_f32 v182, v108, v109
v_mfma_f32_16x16x32_bf16 v[208:211], a[68:71], a[36:39], v[208:211]
v_cvt_pk_bf16_f32 v228, v200, v201
v_cvt_pk_bf16_f32 v183, v110, v111
v_mul_f32 v112, v112, v243
v_mul_f32 v113, v113, v243
v_mul_f32 v114, v114, v243
v_mfma_f32_16x16x32_bf16 v[208:211], a[72:75], a[40:43], v[208:211]
v_exp_f32 v202, v202
v_mul_f32 v115, v115, v243
v_mul_f32 v116, v116, v243
v_mul_f32 v117, v117, v243
v_mfma_f32_16x16x32_bf16 v[208:211], a[76:79], a[44:47], v[208:211]
buffer_load_dwordx4 v255, s[60:63], s17 offen offset:3072 sc1 lds
v_mul_f32 v118, v118, v243
v_mul_f32 v119, v119, v243
v_cvt_pk_bf16_f32 v184, v112, v113
v_mfma_f32_16x16x32_bf16 v[216:219], a[64:67], a[48:51], 0
v_exp_f32 v203, v203
v_cvt_pk_bf16_f32 v185, v114, v115
v_cvt_pk_bf16_f32 v186, v116, v117
v_cvt_pk_bf16_f32 v187, v118, v119
v_mfma_f32_16x16x32_bf16 v[216:219], a[68:71], a[52:55], v[216:219]
v_cvt_pk_bf16_f32 v229, v202, v203
v_mul_f32 v120, v120, v243
v_mul_f32 v121, v121, v243
v_mul_f32 v122, v122, v243
v_mul_f32 v123, v123, v243
v_mfma_f32_16x16x32_bf16 v[216:219], a[72:75], a[56:59], v[216:219]
v_exp_f32 v196, v196
v_mul_f32 v124, v124, v243
v_mul_f32 v125, v125, v243
v_mul_f32 v126, v126, v243
v_mfma_f32_16x16x32_bf16 v[216:219], a[76:79], a[60:63], v[216:219]
v_mul_f32 v127, v127, v243
v_cvt_pk_bf16_f32 v188, v120, v121
v_cvt_pk_bf16_f32 v189, v122, v123
v_cvt_pk_bf16_f32 v190, v124, v125
v_cvt_pk_bf16_f32 v191, v126, v127
v_mfma_f32_16x16x32_bf16 v[212:215], a[80:83], a[32:35], 0
v_exp_f32 v197, v197
s_add_u32 s17, s17, 8192
v_mfma_f32_16x16x32_bf16 v[212:215], a[84:87], a[36:39], v[212:215]
v_cvt_pk_bf16_f32 v226, v196, v197
s_add_u32 m0, m0, 16384
s_and_b32 m0, m0, s78
v_mfma_f32_16x16x32_bf16 v[212:215], a[88:91], a[40:43], v[212:215]
v_exp_f32 v198, v198
v_mfma_f32_16x16x32_bf16 v[212:215], a[92:95], a[44:47], v[212:215]
buffer_store_dwordx4 v[160:163], v246, s[44:47], s91 offen offset:0 sc1
v_mfma_f32_16x16x32_bf16 v[220:223], a[80:83], a[48:51], 0
v_exp_f32 v199, v199
v_mfma_f32_16x16x32_bf16 v[220:223], a[84:87], a[52:55], v[220:223]
v_cvt_pk_bf16_f32 v227, v198, v199
v_mfma_f32_16x16x32_bf16 v[220:223], a[88:91], a[56:59], v[220:223]
v_exp_f32 v204, v204
v_mfma_f32_16x16x32_bf16 v[220:223], a[92:95], a[60:63], v[220:223]
s_waitcnt vmcnt(18)
s_barrier
v_mfma_f32_16x16x32_bf16 v[64:67], a[96:99], v[232:235], 0
v_exp_f32 v205, v205
v_mfma_f32_16x16x32_bf16 v[96:99], a[96:99], v[236:239], 0
ds_read_b128 a[96:99], v248 offset:8192
v_cvt_pk_bf16_f32 v230, v204, v205
v_mfma_f32_16x16x32_bf16 v[68:71], a[100:103], v[232:235], 0
v_exp_f32 v206, v206
.La5_LW2_5:
s_cbranch_execz .La5_LW2_9
v_mfma_f32_16x16x32_bf16 v[100:103], a[100:103], v[236:239], 0
ds_read_b128 a[100:103], v248 offset:9216
buffer_store_dwordx4 v[164:167], v246, s[44:47], s91 offen offset:1024 sc1
v_mfma_f32_16x16x32_bf16 v[72:75], a[104:107], v[232:235], 0
v_exp_f32 v207, v207
v_mfma_f32_16x16x32_bf16 v[104:107], a[104:107], v[236:239], 0
ds_read_b128 a[104:107], v248 offset:10240
v_cvt_pk_bf16_f32 v231, v206, v207
v_mfma_f32_16x16x32_bf16 v[76:79], a[108:111], v[232:235], 0
v_exp_f32 v208, v208
v_mfma_f32_16x16x32_bf16 v[108:111], a[108:111], v[236:239], 0
ds_read_b128 a[108:111], v248 offset:11264
buffer_store_dwordx4 v[168:171], v246, s[44:47], s91 offen offset:2048 sc1
v_mfma_f32_16x16x32_bf16 v[80:83], a[112:115], v[232:235], 0
v_exp_f32 v209, v209
v_mfma_f32_16x16x32_bf16 v[112:115], a[112:115], v[236:239], 0
ds_read_b128 a[112:115], v248 offset:12288
v_mfma_f32_16x16x32_bf16 v[84:87], a[116:119], v[232:235], 0
v_exp_f32 v210, v210
v_mfma_f32_16x16x32_bf16 v[116:119], a[116:119], v[236:239], 0
ds_read_b128 a[116:119], v248 offset:13312
buffer_store_dwordx4 v[172:175], v246, s[44:47], s91 offen offset:3072 sc1
v_mfma_f32_16x16x32_bf16 v[88:91], a[120:123], v[232:235], 0
v_exp_f32 v211, v211
v_mfma_f32_16x16x32_bf16 v[120:123], a[120:123], v[236:239], 0
ds_read_b128 a[120:123], v248 offset:14336
v_mfma_f32_16x16x32_bf16 v[92:95], a[124:127], v[232:235], 0
v_exp_f32 v216, v216
v_mfma_f32_16x16x32_bf16 v[124:127], a[124:127], v[236:239], 0
ds_read_b128 a[124:127], v248 offset:15360
v_or_b32 v248, s89, v255
buffer_store_dwordx4 v[176:179], v247, s[44:47], s91 offen offset:0 sc1
v_smfmac_f32_16x16x64_bf16 a[196:199], a[208:211], v[232:239], v241
s_waitcnt lgkmcnt(4)
v_smfmac_f32_16x16x64_bf16 a[192:195], a[208:211], v[224:231], v241
ds_read_b128 a[64:67], v248 offset:0
v_mfma_f32_16x16x32_bf16 v[0:3], a[96:99], v[224:227], v[0:3]
v_exp_f32 v217, v217
v_mfma_f32_16x16x32_bf16 v[32:35], a[96:99], v[228:231], v[32:35]
ds_read_b128 a[68:71], v248 offset:1024
v_cvt_pk_bf16_f32 v232, v208, v209
v_mfma_f32_16x16x32_bf16 v[4:7], a[100:103], v[224:227], v[4:7]
v_exp_f32 v218, v218
v_mfma_f32_16x16x32_bf16 v[36:39], a[100:103], v[228:231], v[36:39]
ds_read_b128 a[72:75], v248 offset:2048
v_cvt_pk_bf16_f32 v233, v210, v211
buffer_store_dwordx4 v[180:183], v247, s[44:47], s91 offen offset:1024 sc1
v_mfma_f32_16x16x32_bf16 v[8:11], a[104:107], v[224:227], v[8:11]
v_exp_f32 v219, v219
v_mfma_f32_16x16x32_bf16 v[40:43], a[104:107], v[228:231], v[40:43]
ds_read_b128 a[76:79], v248 offset:3072
v_cvt_pk_bf16_f32 v236, v216, v217
v_mfma_f32_16x16x32_bf16 v[12:15], a[108:111], v[224:227], v[12:15]
v_exp_f32 v212, v212
v_mfma_f32_16x16x32_bf16 v[44:47], a[108:111], v[228:231], v[44:47]
ds_read_b128 a[80:83], v248 offset:4096
s_waitcnt lgkmcnt(5)
v_mfma_f32_16x16x32_bf16 v[16:19], a[112:115], v[224:227], v[16:19]
v_exp_f32 v213, v213
v_mfma_f32_16x16x32_bf16 v[48:51], a[112:115], v[228:231], v[48:51]
ds_read_b128 a[84:87], v248 offset:5120
v_cvt_pk_bf16_f32 v237, v218, v219
buffer_store_dwordx4 v[184:187], v247, s[44:47], s91 offen offset:2048 sc1
v_mfma_f32_16x16x32_bf16 v[20:23], a[116:119], v[224:227], v[20:23]
v_exp_f32 v214, v214
v_mfma_f32_16x16x32_bf16 v[52:55], a[116:119], v[228:231], v[52:55]
ds_read_b128 a[88:91], v248 offset:6144
v_cvt_pk_bf16_f32 v234, v212, v213
v_mfma_f32_16x16x32_bf16 v[24:27], a[120:123], v[224:227], v[24:27]
v_exp_f32 v215, v215
v_mfma_f32_16x16x32_bf16 v[56:59], a[120:123], v[228:231], v[56:59]
ds_read_b128 a[92:95], v248 offset:7168
v_cvt_pk_bf16_f32 v235, v214, v215
buffer_store_dwordx4 v[188:191], v247, s[44:47], s91 offen offset:3072 sc1
v_mfma_f32_16x16x32_bf16 v[28:31], a[124:127], v[224:227], v[28:31]
v_exp_f32 v220, v220
v_mfma_f32_16x16x32_bf16 v[60:63], a[124:127], v[228:231], v[60:63]
s_add_u32 s85, s85, 1
s_cmp_eq_u32 s85, 22
s_cbranch_scc1 .La5_LA_F3
.p2align 6
.La5_LA_F2:
s_waitcnt lgkmcnt(4)
v_mfma_f32_16x16x32_bf16 v[192:195], a[64:67], a[0:3], 0
v_exp_f32 v221, v221
v_mfma_f32_16x16x32_bf16 v[192:195], a[68:71], a[4:7], v[192:195]
v_cvt_pk_bf16_f32 v238, v220, v221
s_add_u32 s85, s85, 1
v_mfma_f32_16x16x32_bf16 v[192:195], a[72:75], a[8:11], v[192:195]
v_exp_f32 v222, v222
v_mfma_f32_16x16x32_bf16 v[192:195], a[76:79], a[12:15], v[192:195]
buffer_load_dwordx4 v255, s[60:63], s17 offen offset:0 sc1 lds
v_mfma_f32_16x16x32_bf16 v[200:203], a[64:67], a[16:19], 0
v_exp_f32 v223, v223
v_mfma_f32_16x16x32_bf16 v[200:203], a[68:71], a[20:23], v[200:203]
v_cvt_pk_bf16_f32 v239, v222, v223
s_add_u32 s89, s89, 16384
v_mfma_f32_16x16x32_bf16 v[200:203], a[72:75], a[24:27], v[200:203]
v_exp_f32 v192, v192
v_mfma_f32_16x16x32_bf16 v[200:203], a[76:79], a[28:31], v[200:203]
s_waitcnt lgkmcnt(0)
s_and_b32 s89, s89, 0x1ffff
v_mfma_f32_16x16x32_bf16 v[196:199], a[80:83], a[0:3], 0
v_exp_f32 v193, v193
v_mfma_f32_16x16x32_bf16 v[196:199], a[84:87], a[4:7], v[196:199]
v_cvt_pk_bf16_f32 v224, v192, v193
v_mfma_f32_16x16x32_bf16 v[196:199], a[88:91], a[8:11], v[196:199]
v_exp_f32 v194, v194
v_mfma_f32_16x16x32_bf16 v[196:199], a[92:95], a[12:15], v[196:199]
buffer_load_dwordx4 v255, s[60:63], s17 offen offset:1024 sc1 lds
v_mfma_f32_16x16x32_bf16 v[204:207], a[80:83], a[16:19], 0
v_exp_f32 v195, v195
v_mfma_f32_16x16x32_bf16 v[204:207], a[84:87], a[20:23], v[204:207]
v_cvt_pk_bf16_f32 v225, v194, v195
v_mfma_f32_16x16x32_bf16 v[204:207], a[88:91], a[24:27], v[204:207]
v_exp_f32 v200, v200
v_mfma_f32_16x16x32_bf16 v[204:207], a[92:95], a[28:31], v[204:207]
buffer_load_dwordx4 v255, s[60:63], s17 offen offset:2048 sc1 lds
v_mfma_f32_16x16x32_bf16 v[208:211], a[64:67], a[32:35], 0
v_exp_f32 v201, v201
v_mfma_f32_16x16x32_bf16 v[208:211], a[68:71], a[36:39], v[208:211]
v_cvt_pk_bf16_f32 v228, v200, v201
v_mfma_f32_16x16x32_bf16 v[208:211], a[72:75], a[40:43], v[208:211]
v_exp_f32 v202, v202
v_mfma_f32_16x16x32_bf16 v[208:211], a[76:79], a[44:47], v[208:211]
buffer_load_dwordx4 v255, s[60:63], s17 offen offset:3072 sc1 lds
v_mfma_f32_16x16x32_bf16 v[216:219], a[64:67], a[48:51], 0
v_exp_f32 v203, v203
v_mfma_f32_16x16x32_bf16 v[216:219], a[68:71], a[52:55], v[216:219]
v_cvt_pk_bf16_f32 v229, v202, v203
v_mfma_f32_16x16x32_bf16 v[216:219], a[72:75], a[56:59], v[216:219]
v_exp_f32 v196, v196
v_mfma_f32_16x16x32_bf16 v[216:219], a[76:79], a[60:63], v[216:219]
buffer_load_dword a253, v254, s[36:39], s90 offen
v_mfma_f32_16x16x32_bf16 v[212:215], a[80:83], a[32:35], 0
v_exp_f32 v197, v197
v_mfma_f32_16x16x32_bf16 v[212:215], a[84:87], a[36:39], v[212:215]
v_cvt_pk_bf16_f32 v226, v196, v197
s_add_u32 s17, s17, 8192
v_mfma_f32_16x16x32_bf16 v[212:215], a[88:91], a[40:43], v[212:215]
v_exp_f32 v198, v198
v_mfma_f32_16x16x32_bf16 v[212:215], a[92:95], a[44:47], v[212:215]
s_add_u32 m0, m0, 16384
s_and_b32 m0, m0, s78
v_mfma_f32_16x16x32_bf16 v[220:223], a[80:83], a[48:51], 0
v_exp_f32 v199, v199
v_mfma_f32_16x16x32_bf16 v[220:223], a[84:87], a[52:55], v[220:223]
v_cvt_pk_bf16_f32 v227, v198, v199
s_add_u32 s90, s90, s21
v_mfma_f32_16x16x32_bf16 v[220:223], a[88:91], a[56:59], v[220:223]
v_exp_f32 v204, v204
v_mfma_f32_16x16x32_bf16 v[220:223], a[92:95], a[60:63], v[220:223]
s_waitcnt vmcnt(11)
s_barrier
v_mfma_f32_16x16x32_bf16 v[64:67], a[96:99], v[232:235], v[64:67]
v_exp_f32 v205, v205
v_mfma_f32_16x16x32_bf16 v[96:99], a[96:99], v[236:239], v[96:99]
ds_read_b128 a[96:99], v248 offset:8192
v_cvt_pk_bf16_f32 v230, v204, v205
v_mfma_f32_16x16x32_bf16 v[68:71], a[100:103], v[232:235], v[68:71]
v_exp_f32 v206, v206
v_mfma_f32_16x16x32_bf16 v[100:103], a[100:103], v[236:239], v[100:103]
ds_read_b128 a[100:103], v248 offset:9216
v_mfma_f32_16x16x32_bf16 v[72:75], a[104:107], v[232:235], v[72:75]
v_exp_f32 v207, v207
v_mfma_f32_16x16x32_bf16 v[104:107], a[104:107], v[236:239], v[104:107]
ds_read_b128 a[104:107], v248 offset:10240
v_cvt_pk_bf16_f32 v231, v206, v207
v_mfma_f32_16x16x32_bf16 v[76:79], a[108:111], v[232:235], v[76:79]
v_exp_f32 v208, v208
v_mfma_f32_16x16x32_bf16 v[108:111], a[108:111], v[236:239], v[108:111]
ds_read_b128 a[108:111], v248 offset:11264
v_mfma_f32_16x16x32_bf16 v[80:83], a[112:115], v[232:235], v[80:83]
v_exp_f32 v209, v209
v_mfma_f32_16x16x32_bf16 v[112:115], a[112:115], v[236:239], v[112:115]
ds_read_b128 a[112:115], v248 offset:12288
v_mfma_f32_16x16x32_bf16 v[84:87], a[116:119], v[232:235], v[84:87]
v_exp_f32 v210, v210
v_mfma_f32_16x16x32_bf16 v[116:119], a[116:119], v[236:239], v[116:119]
ds_read_b128 a[116:119], v248 offset:13312
v_mfma_f32_16x16x32_bf16 v[88:91], a[120:123], v[232:235], v[88:91]
v_exp_f32 v211, v211
v_mfma_f32_16x16x32_bf16 v[120:123], a[120:123], v[236:239], v[120:123]
ds_read_b128 a[120:123], v248 offset:14336
s_bitcmp1_b64 s[98:99], s85
v_mfma_f32_16x16x32_bf16 v[92:95], a[124:127], v[232:235], v[92:95]
v_exp_f32 v216, v216
v_mfma_f32_16x16x32_bf16 v[124:127], a[124:127], v[236:239], v[124:127]
ds_read_b128 a[124:127], v248 offset:15360
v_or_b32 v248, s89, v255
v_smfmac_f32_16x16x64_bf16 a[196:199], a[208:211], v[232:239], v241
s_waitcnt lgkmcnt(4)
v_smfmac_f32_16x16x64_bf16 a[192:195], a[208:211], v[224:231], v241
ds_read_b128 a[64:67], v248 offset:0
v_mfma_f32_16x16x32_bf16 v[0:3], a[96:99], v[224:227], v[0:3]
v_exp_f32 v217, v217
v_mfma_f32_16x16x32_bf16 v[32:35], a[96:99], v[228:231], v[32:35]
ds_read_b128 a[68:71], v248 offset:1024
v_cvt_pk_bf16_f32 v232, v208, v209
v_mfma_f32_16x16x32_bf16 v[4:7], a[100:103], v[224:227], v[4:7]
v_exp_f32 v218, v218
v_mfma_f32_16x16x32_bf16 v[36:39], a[100:103], v[228:231], v[36:39]
ds_read_b128 a[72:75], v248 offset:2048
v_cvt_pk_bf16_f32 v233, v210, v211
v_mfma_f32_16x16x32_bf16 v[8:11], a[104:107], v[224:227], v[8:11]
v_exp_f32 v219, v219
v_mfma_f32_16x16x32_bf16 v[40:43], a[104:107], v[228:231], v[40:43]
ds_read_b128 a[76:79], v248 offset:3072
v_cvt_pk_bf16_f32 v236, v216, v217
v_mfma_f32_16x16x32_bf16 v[12:15], a[108:111], v[224:227], v[12:15]
v_exp_f32 v212, v212
v_mfma_f32_16x16x32_bf16 v[44:47], a[108:111], v[228:231], v[44:47]
ds_read_b128 a[80:83], v248 offset:4096
s_waitcnt lgkmcnt(5)
v_mfma_f32_16x16x32_bf16 v[16:19], a[112:115], v[224:227], v[16:19]
v_exp_f32 v213, v213
v_mfma_f32_16x16x32_bf16 v[48:51], a[112:115], v[228:231], v[48:51]
ds_read_b128 a[84:87], v248 offset:5120
v_cvt_pk_bf16_f32 v237, v218, v219
v_mfma_f32_16x16x32_bf16 v[20:23], a[116:119], v[224:227], v[20:23]
v_exp_f32 v214, v214
v_mfma_f32_16x16x32_bf16 v[52:55], a[116:119], v[228:231], v[52:55]
ds_read_b128 a[88:91], v248 offset:6144
v_cvt_pk_bf16_f32 v234, v212, v213
v_mfma_f32_16x16x32_bf16 v[24:27], a[120:123], v[224:227], v[24:27]
v_exp_f32 v215, v215
v_mfma_f32_16x16x32_bf16 v[56:59], a[120:123], v[228:231], v[56:59]
ds_read_b128 a[92:95], v248 offset:7168
v_cvt_pk_bf16_f32 v235, v214, v215
v_mfma_f32_16x16x32_bf16 v[28:31], a[124:127], v[224:227], v[28:31]
v_exp_f32 v220, v220
v_mfma_f32_16x16x32_bf16 v[60:63], a[124:127], v[228:231], v[60:63]
s_cbranch_scc1 .La5_LA_F2E2
s_waitcnt lgkmcnt(4)
v_mfma_f32_16x16x32_bf16 v[192:195], a[64:67], a[0:3], 0
v_exp_f32 v221, v221
v_mfma_f32_16x16x32_bf16 v[192:195], a[68:71], a[4:7], v[192:195]
v_cvt_pk_bf16_f32 v238, v220, v221
s_add_u32 s85, s85, 1
v_mfma_f32_16x16x32_bf16 v[192:195], a[72:75], a[8:11], v[192:195]
v_exp_f32 v222, v222
v_mfma_f32_16x16x32_bf16 v[192:195], a[76:79], a[12:15], v[192:195]
buffer_load_dwordx4 v255, s[60:63], s17 offen offset:0 sc1 lds
v_mfma_f32_16x16x32_bf16 v[200:203], a[64:67], a[16:19], 0
v_exp_f32 v223, v223
v_mfma_f32_16x16x32_bf16 v[200:203], a[68:71], a[20:23], v[200:203]
v_cvt_pk_bf16_f32 v239, v222, v223
s_add_u32 s89, s89, 16384
v_mfma_f32_16x16x32_bf16 v[200:203], a[72:75], a[24:27], v[200:203]
v_exp_f32 v192, v192
v_mfma_f32_16x16x32_bf16 v[200:203], a[76:79], a[28:31], v[200:203]
s_waitcnt lgkmcnt(0)
s_and_b32 s89, s89, 0x1ffff
v_mfma_f32_16x16x32_bf16 v[196:199], a[80:83], a[0:3], 0
v_exp_f32 v193, v193
v_mfma_f32_16x16x32_bf16 v[196:199], a[84:87], a[4:7], v[196:199]
v_cvt_pk_bf16_f32 v224, v192, v193
v_mfma_f32_16x16x32_bf16 v[196:199], a[88:91], a[8:11], v[196:199]
v_exp_f32 v194, v194
v_mfma_f32_16x16x32_bf16 v[196:199], a[92:95], a[12:15], v[196:199]
buffer_load_dwordx4 v255, s[60:63], s17 offen offset:1024 sc1 lds
v_mfma_f32_16x16x32_bf16 v[204:207], a[80:83], a[16:19], 0
v_exp_f32 v195, v195
v_mfma_f32_16x16x32_bf16 v[204:207], a[84:87], a[20:23], v[204:207]
v_cvt_pk_bf16_f32 v225, v194, v195
v_mfma_f32_16x16x32_bf16 v[204:207], a[88:91], a[24:27], v[204:207]
v_exp_f32 v200, v200
v_mfma_f32_16x16x32_bf16 v[204:207], a[92:95], a[28:31], v[204:207]
buffer_load_dwordx4 v255, s[60:63], s17 offen offset:2048 sc1 lds
v_mfma_f32_16x16x32_bf16 v[208:211], a[64:67], a[32:35], 0
v_exp_f32 v201, v201
v_mfma_f32_16x16x32_bf16 v[208:211], a[68:71], a[36:39], v[208:211]
v_cvt_pk_bf16_f32 v228, v200, v201
v_mfma_f32_16x16x32_bf16 v[208:211], a[72:75], a[40:43], v[208:211]
v_exp_f32 v202, v202
v_mfma_f32_16x16x32_bf16 v[208:211], a[76:79], a[44:47], v[208:211]
buffer_load_dwordx4 v255, s[60:63], s17 offen offset:3072 sc1 lds
v_mfma_f32_16x16x32_bf16 v[216:219], a[64:67], a[48:51], 0
v_exp_f32 v203, v203
v_mfma_f32_16x16x32_bf16 v[216:219], a[68:71], a[52:55], v[216:219]
v_cvt_pk_bf16_f32 v229, v202, v203
v_mfma_f32_16x16x32_bf16 v[216:219], a[72:75], a[56:59], v[216:219]
v_exp_f32 v196, v196
v_mfma_f32_16x16x32_bf16 v[216:219], a[76:79], a[60:63], v[216:219]
buffer_load_dword a253, v254, s[36:39], s90 offen
v_mfma_f32_16x16x32_bf16 v[212:215], a[80:83], a[32:35], 0
v_exp_f32 v197, v197
v_mfma_f32_16x16x32_bf16 v[212:215], a[84:87], a[36:39], v[212:215]
v_cvt_pk_bf16_f32 v226, v196, v197
s_add_u32 s17, s17, 8192
v_mfma_f32_16x16x32_bf16 v[212:215], a[88:91], a[40:43], v[212:215]
v_exp_f32 v198, v198
v_mfma_f32_16x16x32_bf16 v[212:215], a[92:95], a[44:47], v[212:215]
s_add_u32 m0, m0, 16384
s_and_b32 m0, m0, s78
v_mfma_f32_16x16x32_bf16 v[220:223], a[80:83], a[48:51], 0
v_exp_f32 v199, v199
v_mfma_f32_16x16x32_bf16 v[220:223], a[84:87], a[52:55], v[220:223]
v_cvt_pk_bf16_f32 v227, v198, v199
s_add_u32 s90, s90, s21
v_mfma_f32_16x16x32_bf16 v[220:223], a[88:91], a[56:59], v[220:223]
v_exp_f32 v204, v204
v_mfma_f32_16x16x32_bf16 v[220:223], a[92:95], a[60:63], v[220:223]
s_waitcnt vmcnt(11)
s_barrier
v_mfma_f32_16x16x32_bf16 v[64:67], a[96:99], v[232:235], v[64:67]
v_exp_f32 v205, v205
v_mfma_f32_16x16x32_bf16 v[96:99], a[96:99], v[236:239], v[96:99]
ds_read_b128 a[96:99], v248 offset:8192
v_cvt_pk_bf16_f32 v230, v204, v205
v_mfma_f32_16x16x32_bf16 v[68:71], a[100:103], v[232:235], v[68:71]
v_exp_f32 v206, v206
v_mfma_f32_16x16x32_bf16 v[100:103], a[100:103], v[236:239], v[100:103]
ds_read_b128 a[100:103], v248 offset:9216
v_mfma_f32_16x16x32_bf16 v[72:75], a[104:107], v[232:235], v[72:75]
v_exp_f32 v207, v207
v_mfma_f32_16x16x32_bf16 v[104:107], a[104:107], v[236:239], v[104:107]
ds_read_b128 a[104:107], v248 offset:10240
v_cvt_pk_bf16_f32 v231, v206, v207
v_mfma_f32_16x16x32_bf16 v[76:79], a[108:111], v[232:235], v[76:79]
v_exp_f32 v208, v208
v_mfma_f32_16x16x32_bf16 v[108:111], a[108:111], v[236:239], v[108:111]
ds_read_b128 a[108:111], v248 offset:11264
v_mfma_f32_16x16x32_bf16 v[80:83], a[112:115], v[232:235], v[80:83]
v_exp_f32 v209, v209
v_mfma_f32_16x16x32_bf16 v[112:115], a[112:115], v[236:239], v[112:115]
ds_read_b128 a[112:115], v248 offset:12288
v_mfma_f32_16x16x32_bf16 v[84:87], a[116:119], v[232:235], v[84:87]
v_exp_f32 v210, v210
v_mfma_f32_16x16x32_bf16 v[116:119], a[116:119], v[236:239], v[116:119]
ds_read_b128 a[116:119], v248 offset:13312
v_mfma_f32_16x16x32_bf16 v[88:91], a[120:123], v[232:235], v[88:91]
v_exp_f32 v211, v211
v_mfma_f32_16x16x32_bf16 v[120:123], a[120:123], v[236:239], v[120:123]
ds_read_b128 a[120:123], v248 offset:14336
s_bitcmp1_b64 s[98:99], s85
v_mfma_f32_16x16x32_bf16 v[92:95], a[124:127], v[232:235], v[92:95]
v_exp_f32 v216, v216
v_mfma_f32_16x16x32_bf16 v[124:127], a[124:127], v[236:239], v[124:127]
ds_read_b128 a[124:127], v248 offset:15360
v_or_b32 v248, s89, v255
v_smfmac_f32_16x16x64_bf16 a[196:199], a[208:211], v[232:239], v241
s_waitcnt lgkmcnt(4)
v_smfmac_f32_16x16x64_bf16 a[192:195], a[208:211], v[224:231], v241
ds_read_b128 a[64:67], v248 offset:0
v_mfma_f32_16x16x32_bf16 v[0:3], a[96:99], v[224:227], v[0:3]
v_exp_f32 v217, v217
v_mfma_f32_16x16x32_bf16 v[32:35], a[96:99], v[228:231], v[32:35]
ds_read_b128 a[68:71], v248 offset:1024
v_cvt_pk_bf16_f32 v232, v208, v209
v_mfma_f32_16x16x32_bf16 v[4:7], a[100:103], v[224:227], v[4:7]
v_exp_f32 v218, v218
v_mfma_f32_16x16x32_bf16 v[36:39], a[100:103], v[228:231], v[36:39]
ds_read_b128 a[72:75], v248 offset:2048
v_cvt_pk_bf16_f32 v233, v210, v211
v_mfma_f32_16x16x32_bf16 v[8:11], a[104:107], v[224:227], v[8:11]
v_exp_f32 v219, v219
v_mfma_f32_16x16x32_bf16 v[40:43], a[104:107], v[228:231], v[40:43]
ds_read_b128 a[76:79], v248 offset:3072
v_cvt_pk_bf16_f32 v236, v216, v217
v_mfma_f32_16x16x32_bf16 v[12:15], a[108:111], v[224:227], v[12:15]
v_exp_f32 v212, v212
v_mfma_f32_16x16x32_bf16 v[44:47], a[108:111], v[228:231], v[44:47]
ds_read_b128 a[80:83], v248 offset:4096
s_waitcnt lgkmcnt(5)
v_mfma_f32_16x16x32_bf16 v[16:19], a[112:115], v[224:227], v[16:19]
v_exp_f32 v213, v213
v_mfma_f32_16x16x32_bf16 v[48:51], a[112:115], v[228:231], v[48:51]
ds_read_b128 a[84:87], v248 offset:5120
v_cvt_pk_bf16_f32 v237, v218, v219
v_mfma_f32_16x16x32_bf16 v[20:23], a[116:119], v[224:227], v[20:23]
v_exp_f32 v214, v214
v_mfma_f32_16x16x32_bf16 v[52:55], a[116:119], v[228:231], v[52:55]
ds_read_b128 a[88:91], v248 offset:6144
v_cvt_pk_bf16_f32 v234, v212, v213
v_mfma_f32_16x16x32_bf16 v[24:27], a[120:123], v[224:227], v[24:27]
v_exp_f32 v215, v215
v_mfma_f32_16x16x32_bf16 v[56:59], a[120:123], v[228:231], v[56:59]
ds_read_b128 a[92:95], v248 offset:7168
v_cvt_pk_bf16_f32 v235, v214, v215
v_mfma_f32_16x16x32_bf16 v[28:31], a[124:127], v[224:227], v[28:31]
v_exp_f32 v220, v220
v_mfma_f32_16x16x32_bf16 v[60:63], a[124:127], v[228:231], v[60:63]
s_cbranch_scc0 .La5_LA_F2
.La5_LA_F2E2:
.La5_LA_F3:
.La5_LW2_6:
s_cbranch_execz .La5_LW2_10
s_waitcnt lgkmcnt(4)
v_mfma_f32_16x16x32_bf16 v[192:195], a[64:67], a[0:3], 0
v_exp_f32 v221, v221
v_mfma_f32_16x16x32_bf16 v[192:195], a[68:71], a[4:7], v[192:195]
v_cvt_pk_bf16_f32 v238, v220, v221
s_add_u32 s89, s89, 16384
v_mfma_f32_16x16x32_bf16 v[192:195], a[72:75], a[8:11], v[192:195]
v_exp_f32 v222, v222
v_mfma_f32_16x16x32_bf16 v[192:195], a[76:79], a[12:15], v[192:195]
buffer_load_dwordx4 v255, s[60:63], s17 offen offset:0 sc1 lds
v_mfma_f32_16x16x32_bf16 v[200:203], a[64:67], a[16:19], 0
v_exp_f32 v223, v223
v_mfma_f32_16x16x32_bf16 v[200:203], a[68:71], a[20:23], v[200:203]
v_cvt_pk_bf16_f32 v239, v222, v223
s_and_b32 s89, s89, 0x1ffff
v_mfma_f32_16x16x32_bf16 v[200:203], a[72:75], a[24:27], v[200:203]
v_exp_f32 v192, v192
v_mfma_f32_16x16x32_bf16 v[200:203], a[76:79], a[28:31], v[200:203]
s_waitcnt lgkmcnt(0)
v_mfma_f32_16x16x32_bf16 v[196:199], a[80:83], a[0:3], 0
v_exp_f32 v193, v193
v_mfma_f32_16x16x32_bf16 v[196:199], a[84:87], a[4:7], v[196:199]
v_cvt_pk_bf16_f32 v224, v192, v193
v_accvgpr_read_b32 v254, a229
v_mfma_f32_16x16x32_bf16 v[196:199], a[88:91], a[8:11], v[196:199]
v_exp_f32 v194, v194
v_mfma_f32_16x16x32_bf16 v[196:199], a[92:95], a[12:15], v[196:199]
buffer_load_dwordx4 v255, s[60:63], s17 offen offset:1024 sc1 lds
v_mfma_f32_16x16x32_bf16 v[204:207], a[80:83], a[16:19], 0
v_exp_f32 v195, v195
v_mfma_f32_16x16x32_bf16 v[204:207], a[84:87], a[20:23], v[204:207]
v_cvt_pk_bf16_f32 v225, v194, v195
v_mfma_f32_16x16x32_bf16 v[204:207], a[88:91], a[24:27], v[204:207]
v_exp_f32 v200, v200
v_mfma_f32_16x16x32_bf16 v[204:207], a[92:95], a[28:31], v[204:207]
buffer_load_dwordx4 v255, s[60:63], s17 offen offset:2048 sc1 lds
v_mfma_f32_16x16x32_bf16 v[208:211], a[64:67], a[32:35], 0
v_exp_f32 v201, v201
v_mfma_f32_16x16x32_bf16 v[208:211], a[68:71], a[36:39], v[208:211]
v_cvt_pk_bf16_f32 v228, v200, v201
v_mfma_f32_16x16x32_bf16 v[208:211], a[72:75], a[40:43], v[208:211]
v_exp_f32 v202, v202
v_mfma_f32_16x16x32_bf16 v[208:211], a[76:79], a[44:47], v[208:211]
buffer_load_dwordx4 v255, s[60:63], s17 offen offset:3072 sc1 lds
v_mfma_f32_16x16x32_bf16 v[216:219], a[64:67], a[48:51], 0
v_exp_f32 v203, v203
v_mfma_f32_16x16x32_bf16 v[216:219], a[68:71], a[52:55], v[216:219]
v_cvt_pk_bf16_f32 v229, v202, v203
s_mov_b32 s93, 0
v_mfma_f32_16x16x32_bf16 v[216:219], a[72:75], a[56:59], v[216:219]
v_exp_f32 v196, v196
v_mfma_f32_16x16x32_bf16 v[216:219], a[76:79], a[60:63], v[216:219]
buffer_load_dwordx4 a[128:131], v254, s[40:43], s93 offen
v_mfma_f32_16x16x32_bf16 v[212:215], a[80:83], a[32:35], 0
v_exp_f32 v197, v197
s_add_u32 s17, s17, 8192
v_mfma_f32_16x16x32_bf16 v[212:215], a[84:87], a[36:39], v[212:215]
v_cvt_pk_bf16_f32 v226, v196, v197
s_mov_b32 s93, 7168
s_add_u32 m0, m0, 16384
v_mfma_f32_16x16x32_bf16 v[212:215], a[88:91], a[40:43], v[212:215]
v_exp_f32 v198, v198
s_and_b32 m0, m0, s78
v_mfma_f32_16x16x32_bf16 v[212:215], a[92:95], a[44:47], v[212:215]
buffer_load_dwordx4 a[132:135], v254, s[40:43], s93 offen
v_mfma_f32_16x16x32_bf16 v[220:223], a[80:83], a[48:51], 0
v_exp_f32 v199, v199
v_mfma_f32_16x16x32_bf16 v[220:223], a[84:87], a[52:55], v[220:223]
v_cvt_pk_bf16_f32 v227, v198, v199
v_mfma_f32_16x16x32_bf16 v[220:223], a[88:91], a[56:59], v[220:223]
v_exp_f32 v204, v204
v_mfma_f32_16x16x32_bf16 v[220:223], a[92:95], a[60:63], v[220:223]
s_waitcnt vmcnt(12)
s_barrier
v_mfma_f32_16x16x32_bf16 v[64:67], a[96:99], v[232:235], v[64:67]
v_exp_f32 v205, v205
v_mfma_f32_16x16x32_bf16 v[96:99], a[96:99], v[236:239], v[96:99]
ds_read_b128 a[96:99], v248 offset:8192
v_cvt_pk_bf16_f32 v230, v204, v205
v_mfma_f32_16x16x32_bf16 v[68:71], a[100:103], v[232:235], v[68:71]
v_exp_f32 v206, v206
v_mfma_f32_16x16x32_bf16 v[100:103], a[100:103], v[236:239], v[100:103]
ds_read_b128 a[100:103], v248 offset:9216
v_mfma_f32_16x16x32_bf16 v[72:75], a[104:107], v[232:235], v[72:75]
v_exp_f32 v207, v207
v_mfma_f32_16x16x32_bf16 v[104:107], a[104:107], v[236:239], v[104:107]
ds_read_b128 a[104:107], v248 offset:10240
v_cvt_pk_bf16_f32 v231, v206, v207
s_mov_b32 s93, 14336
v_mfma_f32_16x16x32_bf16 v[76:79], a[108:111], v[232:235], v[76:79]
v_exp_f32 v208, v208
v_mfma_f32_16x16x32_bf16 v[108:111], a[108:111], v[236:239], v[108:111]
ds_read_b128 a[108:111], v248 offset:11264
buffer_load_dwordx4 a[136:139], v254, s[40:43], s93 offen
v_mfma_f32_16x16x32_bf16 v[80:83], a[112:115], v[232:235], v[80:83]
v_exp_f32 v209, v209
v_mfma_f32_16x16x32_bf16 v[112:115], a[112:115], v[236:239], v[112:115]
ds_read_b128 a[112:115], v248 offset:12288
v_mfma_f32_16x16x32_bf16 v[84:87], a[116:119], v[232:235], v[84:87]
v_exp_f32 v210, v210
v_mfma_f32_16x16x32_bf16 v[116:119], a[116:119], v[236:239], v[116:119]
ds_read_b128 a[116:119], v248 offset:13312
v_mfma_f32_16x16x32_bf16 v[88:91], a[120:123], v[232:235], v[88:91]
v_exp_f32 v211, v211
v_mfma_f32_16x16x32_bf16 v[120:123], a[120:123], v[236:239], v[120:123]
ds_read_b128 a[120:123], v248 offset:14336
v_mfma_f32_16x16x32_bf16 v[92:95], a[124:127], v[232:235], v[92:95]
v_exp_f32 v216, v216
v_mfma_f32_16x16x32_bf16 v[124:127], a[124:127], v[236:239], v[124:127]
ds_read_b128 a[124:127], v248 offset:15360
v_or_b32 v248, s89, v255
v_smfmac_f32_16x16x64_bf16 a[196:199], a[208:211], v[232:239], v241
s_waitcnt lgkmcnt(4)
v_smfmac_f32_16x16x64_bf16 a[192:195], a[208:211], v[224:231], v241
ds_read_b128 a[64:67], v248 offset:0
v_mfma_f32_16x16x32_bf16 v[0:3], a[96:99], v[224:227], v[0:3]
v_exp_f32 v217, v217
v_mfma_f32_16x16x32_bf16 v[32:35], a[96:99], v[228:231], v[32:35]
ds_read_b128 a[68:71], v248 offset:1024
v_cvt_pk_bf16_f32 v232, v208, v209
v_mfma_f32_16x16x32_bf16 v[4:7], a[100:103], v[224:227], v[4:7]
v_exp_f32 v218, v218
v_mfma_f32_16x16x32_bf16 v[36:39], a[100:103], v[228:231], v[36:39]
ds_read_b128 a[72:75], v248 offset:2048
v_cvt_pk_bf16_f32 v233, v210, v211
v_mfma_f32_16x16x32_bf16 v[8:11], a[104:107], v[224:227], v[8:11]
v_exp_f32 v219, v219
v_mfma_f32_16x16x32_bf16 v[40:43], a[104:107], v[228:231], v[40:43]
ds_read_b128 a[76:79], v248 offset:3072
v_cvt_pk_bf16_f32 v236, v216, v217
v_mfma_f32_16x16x32_bf16 v[12:15], a[108:111], v[224:227], v[12:15]
v_exp_f32 v212, v212
v_mfma_f32_16x16x32_bf16 v[44:47], a[108:111], v[228:231], v[44:47]
ds_read_b128 a[80:83], v248 offset:4096
s_waitcnt lgkmcnt(5)
v_mfma_f32_16x16x32_bf16 v[16:19], a[112:115], v[224:227], v[16:19]
v_exp_f32 v213, v213
v_mfma_f32_16x16x32_bf16 v[48:51], a[112:115], v[228:231], v[48:51]
ds_read_b128 a[84:87], v248 offset:5120
v_cvt_pk_bf16_f32 v237, v218, v219
v_mfma_f32_16x16x32_bf16 v[20:23], a[116:119], v[224:227], v[20:23]
v_exp_f32 v214, v214
v_mfma_f32_16x16x32_bf16 v[52:55], a[116:119], v[228:231], v[52:55]
ds_read_b128 a[88:91], v248 offset:6144
v_cvt_pk_bf16_f32 v234, v212, v213
v_mfma_f32_16x16x32_bf16 v[24:27], a[120:123], v[224:227], v[24:27]
v_exp_f32 v215, v215
v_mfma_f32_16x16x32_bf16 v[56:59], a[120:123], v[228:231], v[56:59]
ds_read_b128 a[92:95], v248 offset:7168
v_cvt_pk_bf16_f32 v235, v214, v215
v_mfma_f32_16x16x32_bf16 v[28:31], a[124:127], v[224:227], v[28:31]
v_exp_f32 v220, v220
v_mfma_f32_16x16x32_bf16 v[60:63], a[124:127], v[228:231], v[60:63]
s_waitcnt lgkmcnt(4)
v_mfma_f32_16x16x32_bf16 v[192:195], a[64:67], a[0:3], 0
v_exp_f32 v221, v221
v_mfma_f32_16x16x32_bf16 v[192:195], a[68:71], a[4:7], v[192:195]
v_cvt_pk_bf16_f32 v238, v220, v221
s_add_u32 s89, s89, 16384
v_mfma_f32_16x16x32_bf16 v[192:195], a[72:75], a[8:11], v[192:195]
v_exp_f32 v222, v222
v_mfma_f32_16x16x32_bf16 v[192:195], a[76:79], a[12:15], v[192:195]
buffer_load_dwordx4 v255, s[60:63], s17 offen offset:0 sc1 lds
v_mfma_f32_16x16x32_bf16 v[200:203], a[64:67], a[16:19], 0
v_exp_f32 v223, v223
v_mfma_f32_16x16x32_bf16 v[200:203], a[68:71], a[20:23], v[200:203]
v_cvt_pk_bf16_f32 v239, v222, v223
s_and_b32 s89, s89, 0x1ffff
v_mfma_f32_16x16x32_bf16 v[200:203], a[72:75], a[24:27], v[200:203]
v_exp_f32 v192, v192
v_mfma_f32_16x16x32_bf16 v[200:203], a[76:79], a[28:31], v[200:203]
s_waitcnt lgkmcnt(0)
v_mfma_f32_16x16x32_bf16 v[196:199], a[80:83], a[0:3], 0
v_exp_f32 v193, v193
v_mfma_f32_16x16x32_bf16 v[196:199], a[84:87], a[4:7], v[196:199]
v_cvt_pk_bf16_f32 v224, v192, v193
v_mfma_f32_16x16x32_bf16 v[196:199], a[88:91], a[8:11], v[196:199]
v_exp_f32 v194, v194
v_mfma_f32_16x16x32_bf16 v[196:199], a[92:95], a[12:15], v[196:199]
buffer_load_dwordx4 v255, s[60:63], s17 offen offset:1024 sc1 lds
v_mfma_f32_16x16x32_bf16 v[204:207], a[80:83], a[16:19], 0
v_exp_f32 v195, v195
v_mfma_f32_16x16x32_bf16 v[204:207], a[84:87], a[20:23], v[204:207]
v_cvt_pk_bf16_f32 v225, v194, v195
v_mfma_f32_16x16x32_bf16 v[204:207], a[88:91], a[24:27], v[204:207]
v_exp_f32 v200, v200
v_mfma_f32_16x16x32_bf16 v[204:207], a[92:95], a[28:31], v[204:207]
buffer_load_dwordx4 v255, s[60:63], s17 offen offset:2048 sc1 lds
v_mfma_f32_16x16x32_bf16 v[208:211], a[64:67], a[32:35], 0
v_exp_f32 v201, v201
v_mfma_f32_16x16x32_bf16 v[208:211], a[68:71], a[36:39], v[208:211]
v_cvt_pk_bf16_f32 v228, v200, v201
v_mfma_f32_16x16x32_bf16 v[208:211], a[72:75], a[40:43], v[208:211]
v_exp_f32 v202, v202
v_mfma_f32_16x16x32_bf16 v[208:211], a[76:79], a[44:47], v[208:211]
buffer_load_dwordx4 v255, s[60:63], s17 offen offset:3072 sc1 lds
v_mfma_f32_16x16x32_bf16 v[216:219], a[64:67], a[48:51], 0
v_exp_f32 v203, v203
v_mfma_f32_16x16x32_bf16 v[216:219], a[68:71], a[52:55], v[216:219]
v_cvt_pk_bf16_f32 v229, v202, v203
s_mov_b32 s93, 21504
v_mfma_f32_16x16x32_bf16 v[216:219], a[72:75], a[56:59], v[216:219]
v_exp_f32 v196, v196
v_mfma_f32_16x16x32_bf16 v[216:219], a[76:79], a[60:63], v[216:219]
buffer_load_dwordx4 a[140:143], v254, s[40:43], s93 offen
v_mfma_f32_16x16x32_bf16 v[212:215], a[80:83], a[32:35], 0
v_exp_f32 v197, v197
s_add_u32 s17, s17, 8192
v_mfma_f32_16x16x32_bf16 v[212:215], a[84:87], a[36:39], v[212:215]
v_cvt_pk_bf16_f32 v226, v196, v197
.La5_LW2_7:
s_cbranch_execz .La5_LW2_11
s_mov_b32 s93, 28672
s_add_u32 m0, m0, 16384
v_mfma_f32_16x16x32_bf16 v[212:215], a[88:91], a[40:43], v[212:215]
v_exp_f32 v198, v198
s_and_b32 m0, m0, s78
v_mfma_f32_16x16x32_bf16 v[212:215], a[92:95], a[44:47], v[212:215]
buffer_load_dwordx4 a[144:147], v254, s[40:43], s93 offen
v_mfma_f32_16x16x32_bf16 v[220:223], a[80:83], a[48:51], 0
v_exp_f32 v199, v199
v_mfma_f32_16x16x32_bf16 v[220:223], a[84:87], a[52:55], v[220:223]
v_cvt_pk_bf16_f32 v227, v198, v199
v_mfma_f32_16x16x32_bf16 v[220:223], a[88:91], a[56:59], v[220:223]
v_exp_f32 v204, v204
v_mfma_f32_16x16x32_bf16 v[220:223], a[92:95], a[60:63], v[220:223]
s_waitcnt vmcnt(14)
s_barrier
v_mfma_f32_16x16x32_bf16 v[64:67], a[96:99], v[232:235], v[64:67]
v_exp_f32 v205, v205
v_mfma_f32_16x16x32_bf16 v[96:99], a[96:99], v[236:239], v[96:99]
ds_read_b128 a[96:99], v248 offset:8192
v_cvt_pk_bf16_f32 v230, v204, v205
v_mfma_f32_16x16x32_bf16 v[68:71], a[100:103], v[232:235], v[68:71]
v_exp_f32 v206, v206
v_mfma_f32_16x16x32_bf16 v[100:103], a[100:103], v[236:239], v[100:103]
ds_read_b128 a[100:103], v248 offset:9216
v_mfma_f32_16x16x32_bf16 v[72:75], a[104:107], v[232:235], v[72:75]
v_exp_f32 v207, v207
v_mfma_f32_16x16x32_bf16 v[104:107], a[104:107], v[236:239], v[104:107]
ds_read_b128 a[104:107], v248 offset:10240
v_cvt_pk_bf16_f32 v231, v206, v207
s_mov_b32 s93, 35840
v_mfma_f32_16x16x32_bf16 v[76:79], a[108:111], v[232:235], v[76:79]
v_exp_f32 v208, v208
v_mfma_f32_16x16x32_bf16 v[108:111], a[108:111], v[236:239], v[108:111]
ds_read_b128 a[108:111], v248 offset:11264
buffer_load_dwordx4 a[148:151], v254, s[40:43], s93 offen
v_mfma_f32_16x16x32_bf16 v[80:83], a[112:115], v[232:235], v[80:83]
v_exp_f32 v209, v209
v_mfma_f32_16x16x32_bf16 v[112:115], a[112:115], v[236:239], v[112:115]
ds_read_b128 a[112:115], v248 offset:12288
v_mfma_f32_16x16x32_bf16 v[84:87], a[116:119], v[232:235], v[84:87]
v_exp_f32 v210, v210
v_mfma_f32_16x16x32_bf16 v[116:119], a[116:119], v[236:239], v[116:119]
ds_read_b128 a[116:119], v248 offset:13312
v_accvgpr_read_b32 v240, a231
s_waitcnt vmcnt(14)
v_mfma_f32_16x16x32_bf16 v[88:91], a[120:123], v[232:235], v[88:91]
v_exp_f32 v211, v211
v_mfma_f32_16x16x32_bf16 v[120:123], a[120:123], v[236:239], v[120:123]
ds_read_b128 a[120:123], v248 offset:14336
v_mfma_f32_16x16x32_bf16 v[92:95], a[124:127], v[232:235], v[92:95]
v_exp_f32 v216, v216
v_mfma_f32_16x16x32_bf16 v[124:127], a[124:127], v[236:239], v[124:127]
ds_read_b128 a[124:127], v248 offset:15360
v_or_b32 v248, s89, v255
v_smfmac_f32_16x16x64_bf16 a[196:199], a[208:211], v[232:239], v241
s_waitcnt lgkmcnt(4)
v_smfmac_f32_16x16x64_bf16 a[192:195], a[208:211], v[224:231], v241
ds_read_b128 a[64:67], v248 offset:0
v_mfma_f32_16x16x32_bf16 v[0:3], a[96:99], v[224:227], v[0:3]
v_exp_f32 v217, v217
v_mfma_f32_16x16x32_bf16 v[32:35], a[96:99], v[228:231], v[32:35]
ds_read_b128 a[68:71], v248 offset:1024
v_cvt_pk_bf16_f32 v232, v208, v209
v_mfma_f32_16x16x32_bf16 v[4:7], a[100:103], v[224:227], v[4:7]
v_exp_f32 v218, v218
v_mfma_f32_16x16x32_bf16 v[36:39], a[100:103], v[228:231], v[36:39]
ds_read_b128 a[72:75], v248 offset:2048
v_cvt_pk_bf16_f32 v233, v210, v211
v_mfma_f32_16x16x32_bf16 v[8:11], a[104:107], v[224:227], v[8:11]
v_exp_f32 v219, v219
v_mfma_f32_16x16x32_bf16 v[40:43], a[104:107], v[228:231], v[40:43]
ds_read_b128 a[76:79], v248 offset:3072
v_cvt_pk_bf16_f32 v236, v216, v217
v_mfma_f32_16x16x32_bf16 v[12:15], a[108:111], v[224:227], v[12:15]
v_exp_f32 v212, v212
v_mfma_f32_16x16x32_bf16 v[44:47], a[108:111], v[228:231], v[44:47]
ds_read_b128 a[80:83], v248 offset:4096
s_waitcnt lgkmcnt(5)
v_mfma_f32_16x16x32_bf16 v[16:19], a[112:115], v[224:227], v[16:19]
v_exp_f32 v213, v213
v_mfma_f32_16x16x32_bf16 v[48:51], a[112:115], v[228:231], v[48:51]
ds_read_b128 a[84:87], v248 offset:5120
v_cvt_pk_bf16_f32 v237, v218, v219
v_mfma_f32_16x16x32_bf16 v[20:23], a[116:119], v[224:227], v[20:23]
v_exp_f32 v214, v214
v_mfma_f32_16x16x32_bf16 v[52:55], a[116:119], v[228:231], v[52:55]
ds_read_b128 a[88:91], v248 offset:6144
v_cvt_pk_bf16_f32 v234, v212, v213
v_mfma_f32_16x16x32_bf16 v[24:27], a[120:123], v[224:227], v[24:27]
v_exp_f32 v215, v215
v_mfma_f32_16x16x32_bf16 v[56:59], a[120:123], v[228:231], v[56:59]
ds_read_b128 a[92:95], v248 offset:7168
v_cvt_pk_bf16_f32 v235, v214, v215
v_mfma_f32_16x16x32_bf16 v[28:31], a[124:127], v[224:227], v[28:31]
v_exp_f32 v220, v220
v_mfma_f32_16x16x32_bf16 v[60:63], a[124:127], v[228:231], v[60:63]
buffer_store_dwordx2 v[252:253], v240, s[64:67], s6 offen offset:0 sc1
s_waitcnt lgkmcnt(4)
v_mfma_f32_16x16x32_bf16 v[192:195], a[64:67], a[0:3], 0
v_exp_f32 v221, v221
v_mfma_f32_16x16x32_bf16 v[192:195], a[68:71], a[4:7], v[192:195]
v_cvt_pk_bf16_f32 v238, v220, v221
s_add_u32 s89, s89, 16384
v_mfma_f32_16x16x32_bf16 v[192:195], a[72:75], a[8:11], v[192:195]
v_exp_f32 v222, v222
v_mfma_f32_16x16x32_bf16 v[192:195], a[76:79], a[12:15], v[192:195]
buffer_load_dwordx4 v255, s[60:63], s17 offen offset:0 sc1 lds
v_mfma_f32_16x16x32_bf16 v[200:203], a[64:67], a[16:19], 0
v_exp_f32 v223, v223
v_mfma_f32_16x16x32_bf16 v[200:203], a[68:71], a[20:23], v[200:203]
v_cvt_pk_bf16_f32 v239, v222, v223
s_and_b32 s89, s89, 0x1ffff
v_mfma_f32_16x16x32_bf16 v[200:203], a[72:75], a[24:27], v[200:203]
v_exp_f32 v192, v192
v_mfma_f32_16x16x32_bf16 v[200:203], a[76:79], a[28:31], v[200:203]
s_waitcnt lgkmcnt(0)
v_mfma_f32_16x16x32_bf16 v[196:199], a[80:83], a[0:3], 0
v_exp_f32 v193, v193
v_mfma_f32_16x16x32_bf16 v[196:199], a[84:87], a[4:7], v[196:199]
v_cvt_pk_bf16_f32 v224, v192, v193
v_mfma_f32_16x16x32_bf16 v[196:199], a[88:91], a[8:11], v[196:199]
v_exp_f32 v194, v194
v_mfma_f32_16x16x32_bf16 v[196:199], a[92:95], a[12:15], v[196:199]
buffer_load_dwordx4 v255, s[60:63], s17 offen offset:1024 sc1 lds
v_mfma_f32_16x16x32_bf16 v[204:207], a[80:83], a[16:19], 0
v_exp_f32 v195, v195
v_mfma_f32_16x16x32_bf16 v[204:207], a[84:87], a[20:23], v[204:207]
v_cvt_pk_bf16_f32 v225, v194, v195
v_mfma_f32_16x16x32_bf16 v[204:207], a[88:91], a[24:27], v[204:207]
v_exp_f32 v200, v200
v_mfma_f32_16x16x32_bf16 v[204:207], a[92:95], a[28:31], v[204:207]
buffer_load_dwordx4 v255, s[60:63], s17 offen offset:2048 sc1 lds
v_mfma_f32_16x16x32_bf16 v[208:211], a[64:67], a[32:35], 0
v_exp_f32 v201, v201
v_mfma_f32_16x16x32_bf16 v[208:211], a[68:71], a[36:39], v[208:211]
v_cvt_pk_bf16_f32 v228, v200, v201
v_mfma_f32_16x16x32_bf16 v[208:211], a[72:75], a[40:43], v[208:211]
v_exp_f32 v202, v202
v_mfma_f32_16x16x32_bf16 v[208:211], a[76:79], a[44:47], v[208:211]
buffer_load_dwordx4 v255, s[60:63], s17 offen offset:3072 sc1 lds
v_mfma_f32_16x16x32_bf16 v[216:219], a[64:67], a[48:51], 0
v_exp_f32 v203, v203
v_mfma_f32_16x16x32_bf16 v[216:219], a[68:71], a[52:55], v[216:219]
v_cvt_pk_bf16_f32 v229, v202, v203
s_mov_b32 s93, 43008
v_mfma_f32_16x16x32_bf16 v[216:219], a[72:75], a[56:59], v[216:219]
v_exp_f32 v196, v196
v_mfma_f32_16x16x32_bf16 v[216:219], a[76:79], a[60:63], v[216:219]
buffer_load_dwordx4 a[152:155], v254, s[40:43], s93 offen
v_mfma_f32_16x16x32_bf16 v[212:215], a[80:83], a[32:35], 0
v_exp_f32 v197, v197
s_add_u32 s17, s17, 8192
v_mfma_f32_16x16x32_bf16 v[212:215], a[84:87], a[36:39], v[212:215]
v_cvt_pk_bf16_f32 v226, v196, v197
s_mov_b32 s93, 50176
s_add_u32 m0, m0, 16384
v_mfma_f32_16x16x32_bf16 v[212:215], a[88:91], a[40:43], v[212:215]
v_exp_f32 v198, v198
s_and_b32 m0, m0, s78
v_mfma_f32_16x16x32_bf16 v[212:215], a[92:95], a[44:47], v[212:215]
buffer_load_dwordx4 a[156:159], v254, s[40:43], s93 offen
v_mfma_f32_16x16x32_bf16 v[220:223], a[80:83], a[48:51], 0
v_exp_f32 v199, v199
v_mfma_f32_16x16x32_bf16 v[220:223], a[84:87], a[52:55], v[220:223]
v_cvt_pk_bf16_f32 v227, v198, v199
v_mfma_f32_16x16x32_bf16 v[220:223], a[88:91], a[56:59], v[220:223]
v_exp_f32 v204, v204
v_mfma_f32_16x16x32_bf16 v[220:223], a[92:95], a[60:63], v[220:223]
s_waitcnt vmcnt(17)
s_barrier
v_mfma_f32_16x16x32_bf16 v[64:67], a[96:99], v[232:235], v[64:67]
v_exp_f32 v205, v205
v_mfma_f32_16x16x32_bf16 v[96:99], a[96:99], v[236:239], v[96:99]
ds_read_b128 a[96:99], v248 offset:8192
v_cvt_pk_bf16_f32 v230, v204, v205
v_mfma_f32_16x16x32_bf16 v[68:71], a[100:103], v[232:235], v[68:71]
v_exp_f32 v206, v206
v_mfma_f32_16x16x32_bf16 v[100:103], a[100:103], v[236:239], v[100:103]
ds_read_b128 a[100:103], v248 offset:9216
v_mfma_f32_16x16x32_bf16 v[72:75], a[104:107], v[232:235], v[72:75]
v_exp_f32 v207, v207
v_mfma_f32_16x16x32_bf16 v[104:107], a[104:107], v[236:239], v[104:107]
ds_read_b128 a[104:107], v248 offset:10240
v_cvt_pk_bf16_f32 v231, v206, v207
s_mov_b32 s93, 57344
v_mfma_f32_16x16x32_bf16 v[76:79], a[108:111], v[232:235], v[76:79]
v_exp_f32 v208, v208
v_mfma_f32_16x16x32_bf16 v[108:111], a[108:111], v[236:239], v[108:111]
ds_read_b128 a[108:111], v248 offset:11264
buffer_load_dwordx4 a[160:163], v254, s[40:43], s93 offen
v_mfma_f32_16x16x32_bf16 v[80:83], a[112:115], v[232:235], v[80:83]
v_exp_f32 v209, v209
v_mfma_f32_16x16x32_bf16 v[112:115], a[112:115], v[236:239], v[112:115]
ds_read_b128 a[112:115], v248 offset:12288
v_mfma_f32_16x16x32_bf16 v[84:87], a[116:119], v[232:235], v[84:87]
v_exp_f32 v210, v210
v_mfma_f32_16x16x32_bf16 v[116:119], a[116:119], v[236:239], v[116:119]
ds_read_b128 a[116:119], v248 offset:13312
v_mfma_f32_16x16x32_bf16 v[88:91], a[120:123], v[232:235], v[88:91]
v_exp_f32 v211, v211
v_mfma_f32_16x16x32_bf16 v[120:123], a[120:123], v[236:239], v[120:123]
ds_read_b128 a[120:123], v248 offset:14336
v_mfma_f32_16x16x32_bf16 v[92:95], a[124:127], v[232:235], v[92:95]
v_exp_f32 v216, v216
v_mfma_f32_16x16x32_bf16 v[124:127], a[124:127], v[236:239], v[124:127]
ds_read_b128 a[124:127], v248 offset:15360
v_or_b32 v248, s89, v255
v_smfmac_f32_16x16x64_bf16 a[196:199], a[208:211], v[232:239], v241
s_waitcnt lgkmcnt(4)
v_smfmac_f32_16x16x64_bf16 a[192:195], a[208:211], v[224:231], v241
ds_read_b128 a[64:67], v248 offset:0
v_mfma_f32_16x16x32_bf16 v[0:3], a[96:99], v[224:227], v[0:3]
v_exp_f32 v217, v217
v_mfma_f32_16x16x32_bf16 v[32:35], a[96:99], v[228:231], v[32:35]
ds_read_b128 a[68:71], v248 offset:1024
v_cvt_pk_bf16_f32 v232, v208, v209
v_mfma_f32_16x16x32_bf16 v[4:7], a[100:103], v[224:227], v[4:7]
v_exp_f32 v218, v218
v_mfma_f32_16x16x32_bf16 v[36:39], a[100:103], v[228:231], v[36:39]
ds_read_b128 a[72:75], v248 offset:2048
v_cvt_pk_bf16_f32 v233, v210, v211
v_mfma_f32_16x16x32_bf16 v[8:11], a[104:107], v[224:227], v[8:11]
v_exp_f32 v219, v219
v_mfma_f32_16x16x32_bf16 v[40:43], a[104:107], v[228:231], v[40:43]
ds_read_b128 a[76:79], v248 offset:3072
v_cvt_pk_bf16_f32 v236, v216, v217
v_mfma_f32_16x16x32_bf16 v[12:15], a[108:111], v[224:227], v[12:15]
v_exp_f32 v212, v212
v_mfma_f32_16x16x32_bf16 v[44:47], a[108:111], v[228:231], v[44:47]
ds_read_b128 a[80:83], v248 offset:4096
s_waitcnt lgkmcnt(5)
v_mfma_f32_16x16x32_bf16 v[16:19], a[112:115], v[224:227], v[16:19]
v_exp_f32 v213, v213
v_mfma_f32_16x16x32_bf16 v[48:51], a[112:115], v[228:231], v[48:51]
ds_read_b128 a[84:87], v248 offset:5120
v_cvt_pk_bf16_f32 v237, v218, v219
v_mfma_f32_16x16x32_bf16 v[20:23], a[116:119], v[224:227], v[20:23]
v_exp_f32 v214, v214
v_mfma_f32_16x16x32_bf16 v[52:55], a[116:119], v[228:231], v[52:55]
ds_read_b128 a[88:91], v248 offset:6144
v_cvt_pk_bf16_f32 v234, v212, v213
v_mfma_f32_16x16x32_bf16 v[24:27], a[120:123], v[224:227], v[24:27]
v_exp_f32 v215, v215
v_mfma_f32_16x16x32_bf16 v[56:59], a[120:123], v[228:231], v[56:59]
ds_read_b128 a[92:95], v248 offset:7168
v_cvt_pk_bf16_f32 v235, v214, v215
v_mfma_f32_16x16x32_bf16 v[28:31], a[124:127], v[224:227], v[28:31]
v_exp_f32 v220, v220
v_mfma_f32_16x16x32_bf16 v[60:63], a[124:127], v[228:231], v[60:63]
s_waitcnt lgkmcnt(4)
v_mfma_f32_16x16x32_bf16 v[192:195], a[64:67], a[0:3], 0
v_exp_f32 v221, v221
v_mfma_f32_16x16x32_bf16 v[192:195], a[68:71], a[4:7], v[192:195]
v_cvt_pk_bf16_f32 v238, v220, v221
v_accvgpr_read_b32 v240, a228
v_mfma_f32_16x16x32_bf16 v[192:195], a[72:75], a[8:11], v[192:195]
v_exp_f32 v222, v222
v_mfma_f32_16x16x32_bf16 v[192:195], a[76:79], a[12:15], v[192:195]
s_add_u32 m0, s83, 16896
s_lshl_b32 s17, s72, 18
v_mfma_f32_16x16x32_bf16 v[200:203], a[64:67], a[16:19], 0
v_exp_f32 v223, v223
v_mfma_f32_16x16x32_bf16 v[200:203], a[68:71], a[20:23], v[200:203]
v_cvt_pk_bf16_f32 v239, v222, v223
s_add_u32 s89, s89, 16384
v_mfma_f32_16x16x32_bf16 v[200:203], a[72:75], a[24:27], v[200:203]
v_exp_f32 v192, v192
v_mfma_f32_16x16x32_bf16 v[200:203], a[76:79], a[28:31], v[200:203]
s_waitcnt lgkmcnt(0)
s_and_b32 s89, s89, 0x1ffff
v_mfma_f32_16x16x32_bf16 v[196:199], a[80:83], a[0:3], 0
v_exp_f32 v193, v193
v_mfma_f32_16x16x32_bf16 v[196:199], a[84:87], a[4:7], v[196:199]
v_cvt_pk_bf16_f32 v224, v192, v193
v_mfma_f32_16x16x32_bf16 v[196:199], a[88:91], a[8:11], v[196:199]
v_exp_f32 v194, v194
v_mfma_f32_16x16x32_bf16 v[196:199], a[92:95], a[12:15], v[196:199]
buffer_load_dwordx4 v240, s[56:59], s17 offen lds
v_mfma_f32_16x16x32_bf16 v[204:207], a[80:83], a[16:19], 0
v_exp_f32 v195, v195
s_add_u32 s17, s17, 32768
v_mfma_f32_16x16x32_bf16 v[204:207], a[84:87], a[20:23], v[204:207]
v_cvt_pk_bf16_f32 v225, v194, v195
s_add_u32 m0, m0, 1024
v_mfma_f32_16x16x32_bf16 v[204:207], a[88:91], a[24:27], v[204:207]
v_exp_f32 v200, v200
v_mfma_f32_16x16x32_bf16 v[204:207], a[92:95], a[28:31], v[204:207]
buffer_load_dwordx4 v240, s[56:59], s17 offen lds
v_mfma_f32_16x16x32_bf16 v[208:211], a[64:67], a[32:35], 0
v_exp_f32 v201, v201
s_add_u32 s17, s17, 4294942720
v_mfma_f32_16x16x32_bf16 v[208:211], a[68:71], a[36:39], v[208:211]
v_cvt_pk_bf16_f32 v228, v200, v201
s_add_u32 m0, m0, 1152
v_mfma_f32_16x16x32_bf16 v[208:211], a[72:75], a[40:43], v[208:211]
v_exp_f32 v202, v202
v_mfma_f32_16x16x32_bf16 v[208:211], a[76:79], a[44:47], v[208:211]
buffer_load_dwordx4 v240, s[56:59], s17 offen lds
v_mfma_f32_16x16x32_bf16 v[216:219], a[64:67], a[48:51], 0
v_exp_f32 v203, v203
s_add_u32 s17, s17, 32768
v_mfma_f32_16x16x32_bf16 v[216:219], a[68:71], a[52:55], v[216:219]
v_cvt_pk_bf16_f32 v229, v202, v203
s_add_u32 m0, m0, 1024
v_mfma_f32_16x16x32_bf16 v[216:219], a[72:75], a[56:59], v[216:219]
v_exp_f32 v196, v196
v_mfma_f32_16x16x32_bf16 v[216:219], a[76:79], a[60:63], v[216:219]
buffer_load_dwordx4 v240, s[56:59], s17 offen lds
v_mfma_f32_16x16x32_bf16 v[212:215], a[80:83], a[32:35], 0
v_exp_f32 v197, v197
s_add_u32 s17, s17, 90112
v_mfma_f32_16x16x32_bf16 v[212:215], a[84:87], a[36:39], v[212:215]
v_cvt_pk_bf16_f32 v226, v196, v197
s_mov_b32 s93, 64512
s_add_u32 m0, s83, 33792
v_mfma_f32_16x16x32_bf16 v[212:215], a[88:91], a[40:43], v[212:215]
v_exp_f32 v198, v198
v_mfma_f32_16x16x32_bf16 v[212:215], a[92:95], a[44:47], v[212:215]
buffer_load_dwordx4 a[164:167], v254, s[40:43], s93 offen
v_mfma_f32_16x16x32_bf16 v[220:223], a[80:83], a[48:51], 0
v_exp_f32 v199, v199
v_mfma_f32_16x16x32_bf16 v[220:223], a[84:87], a[52:55], v[220:223]
v_cvt_pk_bf16_f32 v227, v198, v199
v_mfma_f32_16x16x32_bf16 v[220:223], a[88:91], a[56:59], v[220:223]
v_exp_f32 v204, v204
v_mfma_f32_16x16x32_bf16 v[220:223], a[92:95], a[60:63], v[220:223]
s_waitcnt vmcnt(16)
s_barrier
v_mfma_f32_16x16x32_bf16 v[64:67], a[96:99], v[232:235], v[64:67]
v_exp_f32 v205, v205
v_mfma_f32_16x16x32_bf16 v[96:99], a[96:99], v[236:239], v[96:99]
ds_read_b128 a[96:99], v248 offset:8192
v_cvt_pk_bf16_f32 v230, v204, v205
v_mfma_f32_16x16x32_bf16 v[68:71], a[100:103], v[232:235], v[68:71]
v_exp_f32 v206, v206
v_mfma_f32_16x16x32_bf16 v[100:103], a[100:103], v[236:239], v[100:103]
ds_read_b128 a[100:103], v248 offset:9216
v_mfma_f32_16x16x32_bf16 v[72:75], a[104:107], v[232:235], v[72:75]
v_exp_f32 v207, v207
v_mfma_f32_16x16x32_bf16 v[104:107], a[104:107], v[236:239], v[104:107]
ds_read_b128 a[104:107], v248 offset:10240
v_cvt_pk_bf16_f32 v231, v206, v207
s_mov_b32 s93, 71680
v_mfma_f32_16x16x32_bf16 v[76:79], a[108:111], v[232:235], v[76:79]
v_exp_f32 v208, v208
v_mfma_f32_16x16x32_bf16 v[108:111], a[108:111], v[236:239], v[108:111]
ds_read_b128 a[108:111], v248 offset:11264
buffer_load_dwordx4 a[168:171], v254, s[40:43], s93 offen
v_mfma_f32_16x16x32_bf16 v[80:83], a[112:115], v[232:235], v[80:83]
v_exp_f32 v209, v209
v_mfma_f32_16x16x32_bf16 v[112:115], a[112:115], v[236:239], v[112:115]
ds_read_b128 a[112:115], v248 offset:12288
s_mov_b32 s93, 78848
v_mfma_f32_16x16x32_bf16 v[84:87], a[116:119], v[232:235], v[84:87]
v_exp_f32 v210, v210
v_mfma_f32_16x16x32_bf16 v[116:119], a[116:119], v[236:239], v[116:119]
ds_read_b128 a[116:119], v248 offset:13312
buffer_load_dwordx4 a[172:175], v254, s[40:43], s93 offen
v_mfma_f32_16x16x32_bf16 v[88:91], a[120:123], v[232:235], v[88:91]
v_exp_f32 v211, v211
v_mfma_f32_16x16x32_bf16 v[120:123], a[120:123], v[236:239], v[120:123]
ds_read_b128 a[120:123], v248 offset:14336
v_mfma_f32_16x16x32_bf16 v[92:95], a[124:127], v[232:235], v[92:95]
v_exp_f32 v216, v216
v_mfma_f32_16x16x32_bf16 v[124:127], a[124:127], v[236:239], v[124:127]
ds_read_b128 a[124:127], v248 offset:15360
v_or_b32 v248, s89, v255
v_smfmac_f32_16x16x64_bf16 a[196:199], a[208:211], v[232:239], v241
s_waitcnt lgkmcnt(4)
v_smfmac_f32_16x16x64_bf16 a[192:195], a[208:211], v[224:231], v241
ds_read_b128 a[64:67], v248 offset:0
v_mfma_f32_16x16x32_bf16 v[0:3], a[96:99], v[224:227], v[0:3]
v_exp_f32 v217, v217
v_mfma_f32_16x16x32_bf16 v[32:35], a[96:99], v[228:231], v[32:35]
ds_read_b128 a[68:71], v248 offset:1024
v_cvt_pk_bf16_f32 v232, v208, v209
v_mfma_f32_16x16x32_bf16 v[4:7], a[100:103], v[224:227], v[4:7]
v_exp_f32 v218, v218
v_mfma_f32_16x16x32_bf16 v[36:39], a[100:103], v[228:231], v[36:39]
ds_read_b128 a[72:75], v248 offset:2048
v_cvt_pk_bf16_f32 v233, v210, v211
v_mfma_f32_16x16x32_bf16 v[8:11], a[104:107], v[224:227], v[8:11]
v_exp_f32 v219, v219
v_mfma_f32_16x16x32_bf16 v[40:43], a[104:107], v[228:231], v[40:43]
ds_read_b128 a[76:79], v248 offset:3072
v_cvt_pk_bf16_f32 v236, v216, v217
v_mfma_f32_16x16x32_bf16 v[12:15], a[108:111], v[224:227], v[12:15]
v_exp_f32 v212, v212
v_mfma_f32_16x16x32_bf16 v[44:47], a[108:111], v[228:231], v[44:47]
ds_read_b128 a[80:83], v248 offset:4096
s_waitcnt lgkmcnt(5)
v_mfma_f32_16x16x32_bf16 v[16:19], a[112:115], v[224:227], v[16:19]
v_exp_f32 v213, v213
v_mfma_f32_16x16x32_bf16 v[48:51], a[112:115], v[228:231], v[48:51]
ds_read_b128 a[84:87], v248 offset:5120
v_cvt_pk_bf16_f32 v237, v218, v219
v_mfma_f32_16x16x32_bf16 v[20:23], a[116:119], v[224:227], v[20:23]
v_exp_f32 v214, v214
v_mfma_f32_16x16x32_bf16 v[52:55], a[116:119], v[228:231], v[52:55]
ds_read_b128 a[88:91], v248 offset:6144
v_cvt_pk_bf16_f32 v234, v212, v213
v_mfma_f32_16x16x32_bf16 v[24:27], a[120:123], v[224:227], v[24:27]
v_exp_f32 v215, v215
v_mfma_f32_16x16x32_bf16 v[56:59], a[120:123], v[228:231], v[56:59]
ds_read_b128 a[92:95], v248 offset:7168
v_cvt_pk_bf16_f32 v235, v214, v215
v_mfma_f32_16x16x32_bf16 v[28:31], a[124:127], v[224:227], v[28:31]
v_exp_f32 v220, v220
v_mfma_f32_16x16x32_bf16 v[60:63], a[124:127], v[228:231], v[60:63]
s_waitcnt lgkmcnt(4)
v_mfma_f32_16x16x32_bf16 v[192:195], a[64:67], a[0:3], 0
v_exp_f32 v221, v221
v_mfma_f32_16x16x32_bf16 v[192:195], a[68:71], a[4:7], v[192:195]
v_cvt_pk_bf16_f32 v238, v220, v221
s_add_u32 s89, s89, 16384
v_mfma_f32_16x16x32_bf16 v[192:195], a[72:75], a[8:11], v[192:195]
v_exp_f32 v222, v222
v_mfma_f32_16x16x32_bf16 v[192:195], a[76:79], a[12:15], v[192:195]
buffer_load_dwordx4 v240, s[56:59], s17 offen lds
v_mfma_f32_16x16x32_bf16 v[200:203], a[64:67], a[16:19], 0
v_exp_f32 v223, v223
s_add_u32 s17, s17, 32768
v_mfma_f32_16x16x32_bf16 v[200:203], a[68:71], a[20:23], v[200:203]
v_cvt_pk_bf16_f32 v239, v222, v223
s_add_u32 m0, m0, 1024
v_mfma_f32_16x16x32_bf16 v[200:203], a[72:75], a[24:27], v[200:203]
v_exp_f32 v192, v192
v_mfma_f32_16x16x32_bf16 v[200:203], a[76:79], a[28:31], v[200:203]
s_waitcnt lgkmcnt(0)
s_and_b32 s89, s89, 0x1ffff
v_mfma_f32_16x16x32_bf16 v[196:199], a[80:83], a[0:3], 0
v_exp_f32 v193, v193
v_mfma_f32_16x16x32_bf16 v[196:199], a[84:87], a[4:7], v[196:199]
v_cvt_pk_bf16_f32 v224, v192, v193
v_mfma_f32_16x16x32_bf16 v[196:199], a[88:91], a[8:11], v[196:199]
v_exp_f32 v194, v194
v_mfma_f32_16x16x32_bf16 v[196:199], a[92:95], a[12:15], v[196:199]
buffer_load_dwordx4 v240, s[56:59], s17 offen lds
v_mfma_f32_16x16x32_bf16 v[204:207], a[80:83], a[16:19], 0
v_exp_f32 v195, v195
s_add_u32 s17, s17, 4294942720
v_mfma_f32_16x16x32_bf16 v[204:207], a[84:87], a[20:23], v[204:207]
v_cvt_pk_bf16_f32 v225, v194, v195
s_add_u32 m0, m0, 1152
v_mfma_f32_16x16x32_bf16 v[204:207], a[88:91], a[24:27], v[204:207]
v_exp_f32 v200, v200
v_mfma_f32_16x16x32_bf16 v[204:207], a[92:95], a[28:31], v[204:207]
buffer_load_dwordx4 v240, s[56:59], s17 offen lds
v_mfma_f32_16x16x32_bf16 v[208:211], a[64:67], a[32:35], 0
v_exp_f32 v201, v201
s_add_u32 s17, s17, 32768
v_mfma_f32_16x16x32_bf16 v[208:211], a[68:71], a[36:39], v[208:211]
v_cvt_pk_bf16_f32 v228, v200, v201
s_add_u32 m0, m0, 1024
v_mfma_f32_16x16x32_bf16 v[208:211], a[72:75], a[40:43], v[208:211]
v_exp_f32 v202, v202
v_mfma_f32_16x16x32_bf16 v[208:211], a[76:79], a[44:47], v[208:211]
buffer_load_dwordx4 v240, s[56:59], s17 offen lds
v_mfma_f32_16x16x32_bf16 v[216:219], a[64:67], a[48:51], 0
v_exp_f32 v203, v203
v_mfma_f32_16x16x32_bf16 v[216:219], a[68:71], a[52:55], v[216:219]
v_cvt_pk_bf16_f32 v229, v202, v203
s_mov_b32 s93, 86016
v_mfma_f32_16x16x32_bf16 v[216:219], a[72:75], a[56:59], v[216:219]
v_exp_f32 v196, v196
v_mfma_f32_16x16x32_bf16 v[216:219], a[76:79], a[60:63], v[216:219]
buffer_load_dwordx4 a[176:179], v254, s[40:43], s93 offen
v_mfma_f32_16x16x32_bf16 v[212:215], a[80:83], a[32:35], 0
v_exp_f32 v197, v197
s_bfe_u32 s17, s4, 262148
v_mfma_f32_16x16x32_bf16 v[212:215], a[84:87], a[36:39], v[212:215]
v_cvt_pk_bf16_f32 v226, v196, v197
s_mov_b32 s93, 93184
s_xor_b32 s17, s17, s72
v_mfma_f32_16x16x32_bf16 v[212:215], a[88:91], a[40:43], v[212:215]
v_exp_f32 v198, v198
s_lshl_b32 s17, s17, 18
v_mfma_f32_16x16x32_bf16 v[212:215], a[92:95], a[44:47], v[212:215]
buffer_load_dwordx4 a[180:183], v254, s[40:43], s93 offen
s_add_u32 m0, s83, 50688
v_mfma_f32_16x16x32_bf16 v[220:223], a[80:83], a[48:51], 0
v_exp_f32 v199, v199
v_mfma_f32_16x16x32_bf16 v[220:223], a[84:87], a[52:55], v[220:223]
v_cvt_pk_bf16_f32 v227, v198, v199
v_mfma_f32_16x16x32_bf16 v[220:223], a[88:91], a[56:59], v[220:223]
v_exp_f32 v204, v204
v_mfma_f32_16x16x32_bf16 v[220:223], a[92:95], a[60:63], v[220:223]
s_waitcnt vmcnt(16)
s_barrier
v_mfma_f32_16x16x32_bf16 v[64:67], a[96:99], v[232:235], v[64:67]
v_exp_f32 v205, v205
v_mfma_f32_16x16x32_bf16 v[96:99], a[96:99], v[236:239], v[96:99]
ds_read_b128 a[96:99], v248 offset:8192
v_cvt_pk_bf16_f32 v230, v204, v205
v_mfma_f32_16x16x32_bf16 v[68:71], a[100:103], v[232:235], v[68:71]
v_exp_f32 v206, v206
v_mfma_f32_16x16x32_bf16 v[100:103], a[100:103], v[236:239], v[100:103]
ds_read_b128 a[100:103], v248 offset:9216
v_mfma_f32_16x16x32_bf16 v[72:75], a[104:107], v[232:235], v[72:75]
v_exp_f32 v207, v207
v_mfma_f32_16x16x32_bf16 v[104:107], a[104:107], v[236:239], v[104:107]
ds_read_b128 a[104:107], v248 offset:10240
v_cvt_pk_bf16_f32 v231, v206, v207
s_mov_b32 s93, 100352
v_mfma_f32_16x16x32_bf16 v[76:79], a[108:111], v[232:235], v[76:79]
v_exp_f32 v208, v208
v_mfma_f32_16x16x32_bf16 v[108:111], a[108:111], v[236:239], v[108:111]
ds_read_b128 a[108:111], v248 offset:11264
buffer_load_dwordx4 a[184:187], v254, s[40:43], s93 offen
v_mfma_f32_16x16x32_bf16 v[80:83], a[112:115], v[232:235], v[80:83]
v_exp_f32 v209, v209
v_mfma_f32_16x16x32_bf16 v[112:115], a[112:115], v[236:239], v[112:115]
ds_read_b128 a[112:115], v248 offset:12288
v_mfma_f32_16x16x32_bf16 v[84:87], a[116:119], v[232:235], v[84:87]
v_exp_f32 v210, v210
v_mfma_f32_16x16x32_bf16 v[116:119], a[116:119], v[236:239], v[116:119]
ds_read_b128 a[116:119], v248 offset:13312
v_mfma_f32_16x16x32_bf16 v[88:91], a[120:123], v[232:235], v[88:91]
v_exp_f32 v211, v211
v_mfma_f32_16x16x32_bf16 v[120:123], a[120:123], v[236:239], v[120:123]
ds_read_b128 a[120:123], v248 offset:14336
v_mfma_f32_16x16x32_bf16 v[92:95], a[124:127], v[232:235], v[92:95]
v_exp_f32 v216, v216
v_mfma_f32_16x16x32_bf16 v[124:127], a[124:127], v[236:239], v[124:127]
ds_read_b128 a[124:127], v248 offset:15360
v_or_b32 v248, s89, v255
v_smfmac_f32_16x16x64_bf16 a[196:199], a[208:211], v[232:239], v241
s_waitcnt lgkmcnt(4)
v_smfmac_f32_16x16x64_bf16 a[192:195], a[208:211], v[224:231], v241
ds_read_b128 a[64:67], v248 offset:0
v_mfma_f32_16x16x32_bf16 v[0:3], a[96:99], v[224:227], v[0:3]
v_exp_f32 v217, v217
v_mfma_f32_16x16x32_bf16 v[32:35], a[96:99], v[228:231], v[32:35]
ds_read_b128 a[68:71], v248 offset:1024
v_cvt_pk_bf16_f32 v232, v208, v209
v_mfma_f32_16x16x32_bf16 v[4:7], a[100:103], v[224:227], v[4:7]
v_exp_f32 v218, v218
v_mfma_f32_16x16x32_bf16 v[36:39], a[100:103], v[228:231], v[36:39]
ds_read_b128 a[72:75], v248 offset:2048
v_cvt_pk_bf16_f32 v233, v210, v211
v_mfma_f32_16x16x32_bf16 v[8:11], a[104:107], v[224:227], v[8:11]
v_exp_f32 v219, v219
v_mfma_f32_16x16x32_bf16 v[40:43], a[104:107], v[228:231], v[40:43]
ds_read_b128 a[76:79], v248 offset:3072
v_cvt_pk_bf16_f32 v236, v216, v217
v_mfma_f32_16x16x32_bf16 v[12:15], a[108:111], v[224:227], v[12:15]
v_exp_f32 v212, v212
v_mfma_f32_16x16x32_bf16 v[44:47], a[108:111], v[228:231], v[44:47]
ds_read_b128 a[80:83], v248 offset:4096
s_waitcnt lgkmcnt(5)
v_mfma_f32_16x16x32_bf16 v[16:19], a[112:115], v[224:227], v[16:19]
v_exp_f32 v213, v213
v_mfma_f32_16x16x32_bf16 v[48:51], a[112:115], v[228:231], v[48:51]
ds_read_b128 a[84:87], v248 offset:5120
v_cvt_pk_bf16_f32 v237, v218, v219
v_mfma_f32_16x16x32_bf16 v[20:23], a[116:119], v[224:227], v[20:23]
v_exp_f32 v214, v214
v_mfma_f32_16x16x32_bf16 v[52:55], a[116:119], v[228:231], v[52:55]
ds_read_b128 a[88:91], v248 offset:6144
v_cvt_pk_bf16_f32 v234, v212, v213
v_mfma_f32_16x16x32_bf16 v[24:27], a[120:123], v[224:227], v[24:27]
v_exp_f32 v215, v215
v_mfma_f32_16x16x32_bf16 v[56:59], a[120:123], v[228:231], v[56:59]
ds_read_b128 a[92:95], v248 offset:7168
v_cvt_pk_bf16_f32 v235, v214, v215
v_mfma_f32_16x16x32_bf16 v[28:31], a[124:127], v[224:227], v[28:31]
v_exp_f32 v220, v220
v_mfma_f32_16x16x32_bf16 v[60:63], a[124:127], v[228:231], v[60:63]
s_waitcnt lgkmcnt(4)
v_mfma_f32_16x16x32_bf16 v[192:195], a[64:67], a[0:3], 0
v_exp_f32 v221, v221
v_mfma_f32_16x16x32_bf16 v[192:195], a[68:71], a[4:7], v[192:195]
v_cvt_pk_bf16_f32 v238, v220, v221
v_mfma_f32_16x16x32_bf16 v[192:195], a[72:75], a[8:11], v[192:195]
v_exp_f32 v222, v222
v_mfma_f32_16x16x32_bf16 v[192:195], a[76:79], a[12:15], v[192:195]
buffer_load_dwordx4 v240, s[56:59], s17 offen lds
v_mfma_f32_16x16x32_bf16 v[200:203], a[64:67], a[16:19], 0
v_exp_f32 v223, v223
s_add_u32 s17, s17, 32768
v_mfma_f32_16x16x32_bf16 v[200:203], a[68:71], a[20:23], v[200:203]
v_cvt_pk_bf16_f32 v239, v222, v223
s_add_u32 m0, m0, 1024
v_mfma_f32_16x16x32_bf16 v[200:203], a[72:75], a[24:27], v[200:203]
v_exp_f32 v192, v192
v_mfma_f32_16x16x32_bf16 v[200:203], a[76:79], a[28:31], v[200:203]
.La5_LW2_8:
s_cbranch_execz .La5_LW2_12
s_waitcnt lgkmcnt(0)
v_mfma_f32_16x16x32_bf16 v[196:199], a[80:83], a[0:3], 0
v_exp_f32 v193, v193
v_mfma_f32_16x16x32_bf16 v[196:199], a[84:87], a[4:7], v[196:199]
v_cvt_pk_bf16_f32 v224, v192, v193
v_mfma_f32_16x16x32_bf16 v[196:199], a[88:91], a[8:11], v[196:199]
v_exp_f32 v194, v194
v_mfma_f32_16x16x32_bf16 v[196:199], a[92:95], a[12:15], v[196:199]
buffer_load_dwordx4 v240, s[56:59], s17 offen lds
v_mfma_f32_16x16x32_bf16 v[204:207], a[80:83], a[16:19], 0
v_exp_f32 v195, v195
s_add_u32 s17, s17, 4294942720
v_mfma_f32_16x16x32_bf16 v[204:207], a[84:87], a[20:23], v[204:207]
v_cvt_pk_bf16_f32 v225, v194, v195
s_add_u32 m0, m0, 1152
v_mfma_f32_16x16x32_bf16 v[204:207], a[88:91], a[24:27], v[204:207]
v_exp_f32 v200, v200
v_mfma_f32_16x16x32_bf16 v[204:207], a[92:95], a[28:31], v[204:207]
buffer_load_dwordx4 v240, s[56:59], s17 offen lds
v_mfma_f32_16x16x32_bf16 v[208:211], a[64:67], a[32:35], 0
v_exp_f32 v201, v201
s_add_u32 s17, s17, 32768
v_mfma_f32_16x16x32_bf16 v[208:211], a[68:71], a[36:39], v[208:211]
v_cvt_pk_bf16_f32 v228, v200, v201
s_add_u32 m0, m0, 1024
v_mfma_f32_16x16x32_bf16 v[208:211], a[72:75], a[40:43], v[208:211]
v_exp_f32 v202, v202
v_mfma_f32_16x16x32_bf16 v[208:211], a[76:79], a[44:47], v[208:211]
buffer_load_dwordx4 v240, s[56:59], s17 offen lds
v_mfma_f32_16x16x32_bf16 v[216:219], a[64:67], a[48:51], 0
v_exp_f32 v203, v203
v_mfma_f32_16x16x32_bf16 v[216:219], a[68:71], a[52:55], v[216:219]
v_cvt_pk_bf16_f32 v229, v202, v203
s_mov_b32 s93, 107520
v_mfma_f32_16x16x32_bf16 v[216:219], a[72:75], a[56:59], v[216:219]
v_exp_f32 v196, v196
v_mfma_f32_16x16x32_bf16 v[216:219], a[76:79], a[60:63], v[216:219]
buffer_load_dwordx4 a[188:191], v254, s[40:43], s93 offen
v_mfma_f32_16x16x32_bf16 v[212:215], a[80:83], a[32:35], 0
v_exp_f32 v197, v197
v_mfma_f32_16x16x32_bf16 v[212:215], a[84:87], a[36:39], v[212:215]
v_cvt_pk_bf16_f32 v226, v196, v197
v_mfma_f32_16x16x32_bf16 v[212:215], a[88:91], a[40:43], v[212:215]
v_exp_f32 v198, v198
v_mfma_f32_16x16x32_bf16 v[212:215], a[92:95], a[44:47], v[212:215]
v_mfma_f32_16x16x32_bf16 v[220:223], a[80:83], a[48:51], 0
v_exp_f32 v199, v199
v_mfma_f32_16x16x32_bf16 v[220:223], a[84:87], a[52:55], v[220:223]
v_cvt_pk_bf16_f32 v227, v198, v199
v_mfma_f32_16x16x32_bf16 v[220:223], a[88:91], a[56:59], v[220:223]
v_exp_f32 v204, v204
v_mfma_f32_16x16x32_bf16 v[220:223], a[92:95], a[60:63], v[220:223]
v_mfma_f32_16x16x32_bf16 v[64:67], a[96:99], v[232:235], v[64:67]
v_exp_f32 v205, v205
v_mfma_f32_16x16x32_bf16 v[96:99], a[96:99], v[236:239], v[96:99]
ds_read_b128 a[96:99], v248 offset:8192
v_cvt_pk_bf16_f32 v230, v204, v205
v_mfma_f32_16x16x32_bf16 v[68:71], a[100:103], v[232:235], v[68:71]
v_exp_f32 v206, v206
v_mfma_f32_16x16x32_bf16 v[100:103], a[100:103], v[236:239], v[100:103]
ds_read_b128 a[100:103], v248 offset:9216
v_mfma_f32_16x16x32_bf16 v[72:75], a[104:107], v[232:235], v[72:75]
v_exp_f32 v207, v207
v_mfma_f32_16x16x32_bf16 v[104:107], a[104:107], v[236:239], v[104:107]
ds_read_b128 a[104:107], v248 offset:10240
v_cvt_pk_bf16_f32 v231, v206, v207
v_mfma_f32_16x16x32_bf16 v[76:79], a[108:111], v[232:235], v[76:79]
v_exp_f32 v208, v208
v_mfma_f32_16x16x32_bf16 v[108:111], a[108:111], v[236:239], v[108:111]
ds_read_b128 a[108:111], v248 offset:11264
v_mfma_f32_16x16x32_bf16 v[80:83], a[112:115], v[232:235], v[80:83]
v_exp_f32 v209, v209
v_mfma_f32_16x16x32_bf16 v[112:115], a[112:115], v[236:239], v[112:115]
ds_read_b128 a[112:115], v248 offset:12288
v_mfma_f32_16x16x32_bf16 v[84:87], a[116:119], v[232:235], v[84:87]
v_exp_f32 v210, v210
v_mfma_f32_16x16x32_bf16 v[116:119], a[116:119], v[236:239], v[116:119]
ds_read_b128 a[116:119], v248 offset:13312
v_mfma_f32_16x16x32_bf16 v[88:91], a[120:123], v[232:235], v[88:91]
v_exp_f32 v211, v211
v_mfma_f32_16x16x32_bf16 v[120:123], a[120:123], v[236:239], v[120:123]
ds_read_b128 a[120:123], v248 offset:14336
v_mfma_f32_16x16x32_bf16 v[92:95], a[124:127], v[232:235], v[92:95]
v_exp_f32 v216, v216
v_mfma_f32_16x16x32_bf16 v[124:127], a[124:127], v[236:239], v[124:127]
ds_read_b128 a[124:127], v248 offset:15360
v_smfmac_f32_16x16x64_bf16 a[196:199], a[208:211], v[232:239], v241
s_waitcnt lgkmcnt(4)
v_smfmac_f32_16x16x64_bf16 a[192:195], a[208:211], v[224:231], v241
v_mfma_f32_16x16x32_bf16 v[0:3], a[96:99], v[224:227], v[0:3]
v_exp_f32 v217, v217
v_mfma_f32_16x16x32_bf16 v[32:35], a[96:99], v[228:231], v[32:35]
v_cvt_pk_bf16_f32 v232, v208, v209
v_mfma_f32_16x16x32_bf16 v[4:7], a[100:103], v[224:227], v[4:7]
v_exp_f32 v218, v218
v_mfma_f32_16x16x32_bf16 v[36:39], a[100:103], v[228:231], v[36:39]
v_cvt_pk_bf16_f32 v233, v210, v211
v_mfma_f32_16x16x32_bf16 v[8:11], a[104:107], v[224:227], v[8:11]
v_exp_f32 v219, v219
v_mfma_f32_16x16x32_bf16 v[40:43], a[104:107], v[228:231], v[40:43]
v_cvt_pk_bf16_f32 v236, v216, v217
v_mfma_f32_16x16x32_bf16 v[12:15], a[108:111], v[224:227], v[12:15]
v_exp_f32 v212, v212
v_mfma_f32_16x16x32_bf16 v[44:47], a[108:111], v[228:231], v[44:47]
s_waitcnt lgkmcnt(0)
v_mfma_f32_16x16x32_bf16 v[16:19], a[112:115], v[224:227], v[16:19]
v_exp_f32 v213, v213
v_mfma_f32_16x16x32_bf16 v[48:51], a[112:115], v[228:231], v[48:51]
v_cvt_pk_bf16_f32 v237, v218, v219
v_mfma_f32_16x16x32_bf16 v[20:23], a[116:119], v[224:227], v[20:23]
v_exp_f32 v214, v214
v_mfma_f32_16x16x32_bf16 v[52:55], a[116:119], v[228:231], v[52:55]
v_cvt_pk_bf16_f32 v234, v212, v213
v_mfma_f32_16x16x32_bf16 v[24:27], a[120:123], v[224:227], v[24:27]
v_exp_f32 v215, v215
v_mfma_f32_16x16x32_bf16 v[56:59], a[120:123], v[228:231], v[56:59]
v_cvt_pk_bf16_f32 v235, v214, v215
v_mfma_f32_16x16x32_bf16 v[28:31], a[124:127], v[224:227], v[28:31]
v_exp_f32 v220, v220
v_mfma_f32_16x16x32_bf16 v[60:63], a[124:127], v[228:231], v[60:63]
; ---- helper: partial O and l go to the partner
.La5_LA_DP:
v_exp_f32 v221, v221
v_exp_f32 v222, v222
v_exp_f32 v223, v223
v_cvt_pk_bf16_f32 v238, v220, v221
s_nop 0
v_cvt_pk_bf16_f32 v239, v222, v223
s_nop 1
v_smfmac_f32_16x16x64_bf16 a[196:199], a[208:211], v[232:239], v241
v_cvt_pk_bf16_f32 v0, v0, v1
v_cvt_pk_bf16_f32 v1, v2, v3
v_cvt_pk_bf16_f32 v2, v4, v5
v_cvt_pk_bf16_f32 v3, v6, v7
buffer_store_dwordx4 v[0:3], v255, s[68:71], 0 offen offset:0 sc1
v_mfma_f32_16x16x32_bf16 v[64:67], a[96:99], v[232:235], v[64:67]
v_cvt_pk_bf16_f32 v8, v8, v9
v_cvt_pk_bf16_f32 v9, v10, v11
v_cvt_pk_bf16_f32 v10, v12, v13
v_cvt_pk_bf16_f32 v11, v14, v15
buffer_store_dwordx4 v[8:11], v255, s[68:71], 0 offen offset:1024 sc1
v_mfma_f32_16x16x32_bf16 v[96:99], a[96:99], v[236:239], v[96:99]
v_accvgpr_read_b32 v240, a224
v_xor_b32 v241, 32, v240
v_cvt_pk_bf16_f32 v16, v16, v17
v_cvt_pk_bf16_f32 v17, v18, v19
v_cvt_pk_bf16_f32 v18, v20, v21
v_cvt_pk_bf16_f32 v19, v22, v23
v_mfma_f32_16x16x32_bf16 v[68:71], a[100:103], v[232:235], v[68:71]
v_xor_b32 v242, 64, v240
v_xor_b32 v243, 96, v240
buffer_store_dwordx4 v[16:19], v255, s[68:71], 0 offen offset:2048 sc1
v_cvt_pk_bf16_f32 v24, v24, v25
v_cvt_pk_bf16_f32 v25, v26, v27
v_mfma_f32_16x16x32_bf16 v[100:103], a[100:103], v[236:239], v[100:103]
v_cvt_pk_bf16_f32 v26, v28, v29
v_cvt_pk_bf16_f32 v27, v30, v31
buffer_store_dwordx4 v[24:27], v255, s[68:71], 0 offen offset:3072 sc1
v_cvt_pk_bf16_f32 v32, v32, v33
v_cvt_pk_bf16_f32 v33, v34, v35
v_mfma_f32_16x16x32_bf16 v[72:75], a[104:107], v[232:235], v[72:75]
v_cvt_pk_bf16_f32 v34, v36, v37
v_cvt_pk_bf16_f32 v35, v38, v39
buffer_store_dwordx4 v[32:35], v255, s[68:71], s95 offen offset:0 sc1
v_cvt_pk_bf16_f32 v40, v40, v41
v_cvt_pk_bf16_f32 v41, v42, v43
v_mfma_f32_16x16x32_bf16 v[104:107], a[104:107], v[236:239], v[104:107]
s_waitcnt vmcnt(13)
s_barrier
v_cvt_pk_bf16_f32 v42, v44, v45
v_cvt_pk_bf16_f32 v43, v46, v47
buffer_store_dwordx4 v[40:43], v255, s[68:71], s95 offen offset:1024 sc1
v_mfma_f32_16x16x32_bf16 v[76:79], a[108:111], v[232:235], v[76:79]
ds_read_b64_tr_b16 v[192:193], v240 offset:16896
ds_read_b64_tr_b16 v[194:195], v240 offset:17920
v_cvt_pk_bf16_f32 v48, v48, v49
v_cvt_pk_bf16_f32 v49, v50, v51
v_cvt_pk_bf16_f32 v50, v52, v53
v_cvt_pk_bf16_f32 v51, v54, v55
v_mfma_f32_16x16x32_bf16 v[108:111], a[108:111], v[236:239], v[108:111]
ds_read_b64_tr_b16 v[196:197], v241 offset:16896
ds_read_b64_tr_b16 v[198:199], v241 offset:17920
buffer_store_dwordx4 v[48:51], v255, s[68:71], s95 offen offset:2048 sc1
v_cvt_pk_bf16_f32 v56, v56, v57
v_cvt_pk_bf16_f32 v57, v58, v59
v_mfma_f32_16x16x32_bf16 v[80:83], a[112:115], v[232:235], v[80:83]
ds_read_b64_tr_b16 v[200:201], v242 offset:16896
ds_read_b64_tr_b16 v[202:203], v242 offset:17920
v_cvt_pk_bf16_f32 v58, v60, v61
v_cvt_pk_bf16_f32 v59, v62, v63
buffer_store_dwordx4 v[56:59], v255, s[68:71], s95 offen offset:3072 sc1
v_mfma_f32_16x16x32_bf16 v[112:115], a[112:115], v[236:239], v[112:115]
ds_read_b64_tr_b16 v[204:205], v243 offset:16896
ds_read_b64_tr_b16 v[206:207], v243 offset:17920
v_mfma_f32_16x16x32_bf16 v[84:87], a[116:119], v[232:235], v[84:87]
ds_read_b64_tr_b16 v[208:209], v240 offset:17024
ds_read_b64_tr_b16 v[210:211], v240 offset:18048
v_mfma_f32_16x16x32_bf16 v[116:119], a[116:119], v[236:239], v[116:119]
ds_read_b64_tr_b16 v[212:213], v241 offset:17024
ds_read_b64_tr_b16 v[214:215], v241 offset:18048
v_mfma_f32_16x16x32_bf16 v[88:91], a[120:123], v[232:235], v[88:91]
ds_read_b64_tr_b16 v[216:217], v242 offset:17024
ds_read_b64_tr_b16 v[218:219], v242 offset:18048
v_mfma_f32_16x16x32_bf16 v[120:123], a[120:123], v[236:239], v[120:123]
ds_read_b64_tr_b16 v[220:221], v243 offset:17024
ds_read_b64_tr_b16 v[222:223], v243 offset:18048
v_mfma_f32_16x16x32_bf16 v[92:95], a[124:127], v[232:235], v[92:95]
v_mfma_f32_16x16x32_bf16 v[124:127], a[124:127], v[236:239], v[124:127]
s_waitcnt lgkmcnt(14)
v_mfma_f32_16x16x32_bf16 v[0:3], v[192:195], v[128:131], 0
s_waitcnt lgkmcnt(12)
v_mfma_f32_16x16x32_bf16 v[32:35], v[192:195], v[144:147], 0
ds_read_b64_tr_b16 v[192:193], v240 offset:25344
ds_read_b64_tr_b16 v[194:195], v240 offset:26368
v_mfma_f32_16x16x32_bf16 v[4:7], v[196:199], v[128:131], 0
s_waitcnt lgkmcnt(12)
v_accvgpr_read_b32 v224, a192
v_accvgpr_read_b32 v228, a193
v_accvgpr_read_b32 v225, a194
v_accvgpr_read_b32 v229, a195
v_accvgpr_read_b32 v226, a196
v_mfma_f32_16x16x32_bf16 v[36:39], v[196:199], v[144:147], 0
ds_read_b64_tr_b16 v[196:197], v241 offset:25344
ds_read_b64_tr_b16 v[198:199], v241 offset:26368
v_accvgpr_read_b32 v230, a197
v_accvgpr_read_b32 v227, a198
v_accvgpr_read_b32 v231, a199
v_add_f32 v224, v224, v228
v_mfma_f32_16x16x32_bf16 v[8:11], v[200:203], v[128:131], 0
s_waitcnt lgkmcnt(12)
v_add_f32 v225, v225, v229
v_add_f32 v226, v226, v230
v_add_f32 v227, v227, v231
s_mov_b32 s92, 16384
buffer_store_dwordx4 v[224:227], v255, s[68:71], s92 offen sc1
v_mfma_f32_16x16x32_bf16 v[40:43], v[200:203], v[144:147], 0
ds_read_b64_tr_b16 v[200:201], v242 offset:25344
ds_read_b64_tr_b16 v[202:203], v242 offset:26368
v_cvt_pk_bf16_f32 v64, v64, v65
v_cvt_pk_bf16_f32 v65, v66, v67
v_cvt_pk_bf16_f32 v66, v68, v69
v_cvt_pk_bf16_f32 v67, v70, v71
v_mfma_f32_16x16x32_bf16 v[12:15], v[204:207], v[128:131], 0
s_waitcnt lgkmcnt(12)
buffer_store_dwordx4 v[64:67], v255, s[68:71], s96 offen offset:0 sc1
v_cvt_pk_bf16_f32 v72, v72, v73
v_cvt_pk_bf16_f32 v73, v74, v75
v_cvt_pk_bf16_f32 v74, v76, v77
v_mfma_f32_16x16x32_bf16 v[44:47], v[204:207], v[144:147], 0
ds_read_b64_tr_b16 v[204:205], v243 offset:25344
ds_read_b64_tr_b16 v[206:207], v243 offset:26368
v_cvt_pk_bf16_f32 v75, v78, v79
buffer_store_dwordx4 v[72:75], v255, s[68:71], s96 offen offset:1024 sc1
v_cvt_pk_bf16_f32 v80, v80, v81
v_mfma_f32_16x16x32_bf16 v[16:19], v[208:211], v[128:131], 0
s_waitcnt lgkmcnt(8)
v_cvt_pk_bf16_f32 v81, v82, v83
v_cvt_pk_bf16_f32 v82, v84, v85
v_cvt_pk_bf16_f32 v83, v86, v87
buffer_store_dwordx4 v[80:83], v255, s[68:71], s96 offen offset:2048 sc1
v_mfma_f32_16x16x32_bf16 v[48:51], v[208:211], v[144:147], 0
ds_read_b64_tr_b16 v[208:209], v240 offset:25472
ds_read_b64_tr_b16 v[210:211], v240 offset:26496
v_cvt_pk_bf16_f32 v88, v88, v89
v_cvt_pk_bf16_f32 v89, v90, v91
v_cvt_pk_bf16_f32 v90, v92, v93
v_cvt_pk_bf16_f32 v91, v94, v95
v_mfma_f32_16x16x32_bf16 v[20:23], v[212:215], v[128:131], 0
buffer_store_dwordx4 v[88:91], v255, s[68:71], s96 offen offset:3072 sc1
v_cvt_pk_bf16_f32 v96, v96, v97
v_cvt_pk_bf16_f32 v97, v98, v99
v_cvt_pk_bf16_f32 v98, v100, v101
v_cvt_pk_bf16_f32 v99, v102, v103
v_mfma_f32_16x16x32_bf16 v[52:55], v[212:215], v[144:147], 0
ds_read_b64_tr_b16 v[212:213], v241 offset:25472
ds_read_b64_tr_b16 v[214:215], v241 offset:26496
buffer_store_dwordx4 v[96:99], v255, s[68:71], s97 offen offset:0 sc1
v_cvt_pk_bf16_f32 v104, v104, v105
v_cvt_pk_bf16_f32 v105, v106, v107
v_mfma_f32_16x16x32_bf16 v[24:27], v[216:219], v[128:131], 0
v_cvt_pk_bf16_f32 v106, v108, v109
v_cvt_pk_bf16_f32 v107, v110, v111
buffer_store_dwordx4 v[104:107], v255, s[68:71], s97 offen offset:1024 sc1
v_cvt_pk_bf16_f32 v112, v112, v113
v_cvt_pk_bf16_f32 v113, v114, v115
v_mfma_f32_16x16x32_bf16 v[56:59], v[216:219], v[144:147], 0
ds_read_b64_tr_b16 v[216:217], v242 offset:25472
ds_read_b64_tr_b16 v[218:219], v242 offset:26496
v_cvt_pk_bf16_f32 v114, v116, v117
v_cvt_pk_bf16_f32 v115, v118, v119
buffer_store_dwordx4 v[112:115], v255, s[68:71], s97 offen offset:2048 sc1
v_mfma_f32_16x16x32_bf16 v[28:31], v[220:223], v[128:131], 0
s_waitcnt lgkmcnt(6)
v_cvt_pk_bf16_f32 v120, v120, v121
v_cvt_pk_bf16_f32 v121, v122, v123
v_cvt_pk_bf16_f32 v122, v124, v125
v_cvt_pk_bf16_f32 v123, v126, v127
buffer_store_dwordx4 v[120:123], v255, s[68:71], s97 offen offset:3072 sc1
v_mfma_f32_16x16x32_bf16 v[60:63], v[220:223], v[144:147], 0
ds_read_b64_tr_b16 v[220:221], v243 offset:25472
ds_read_b64_tr_b16 v[222:223], v243 offset:26496
v_mov_b32_e64 v249, 0
s_add_u32 s6, s88, 393216
v_mfma_f32_16x16x32_bf16 v[0:3], v[192:195], v[132:135], v[0:3]
v_accvgpr_read_b32 v224, a228
v_add_u32 v225, 0x8000, v224
v_add_u32 v226, 0x2000, v224
v_add_u32 v227, 0xa000, v224
v_mfma_f32_16x16x32_bf16 v[32:35], v[192:195], v[148:151], v[32:35]
ds_read_b64_tr_b16 v[192:193], v240 offset:33792
ds_read_b64_tr_b16 v[194:195], v240 offset:34816
v_accvgpr_read_b32 v248, a225
v_accvgpr_read_b32 v228, a229
v_mfma_f32_16x16x32_bf16 v[4:7], v[196:199], v[132:135], v[4:7]
v_accvgpr_read_b32 v229, a230
v_mfma_f32_16x16x32_bf16 v[36:39], v[196:199], v[148:151], v[36:39]
ds_read_b64_tr_b16 v[196:197], v241 offset:33792
ds_read_b64_tr_b16 v[198:199], v241 offset:34816
v_mfma_f32_16x16x32_bf16 v[8:11], v[200:203], v[132:135], v[8:11]
v_mfma_f32_16x16x32_bf16 v[40:43], v[200:203], v[148:151], v[40:43]
ds_read_b64_tr_b16 v[200:201], v242 offset:33792
ds_read_b64_tr_b16 v[202:203], v242 offset:34816
v_mfma_f32_16x16x32_bf16 v[12:15], v[204:207], v[132:135], v[12:15]
s_waitcnt lgkmcnt(6)
v_mfma_f32_16x16x32_bf16 v[44:47], v[204:207], v[148:151], v[44:47]
ds_read_b64_tr_b16 v[204:205], v243 offset:33792
ds_read_b64_tr_b16 v[206:207], v243 offset:34816
v_mfma_f32_16x16x32_bf16 v[16:19], v[208:211], v[132:135], v[16:19]
s_waitcnt vmcnt(0)
buffer_store_dwordx2 v[252:253], v249, s[64:67], s6 offen offset:0 sc1
v_mfma_f32_16x16x32_bf16 v[48:51], v[208:211], v[148:151], v[48:51]
ds_read_b64_tr_b16 v[208:209], v240 offset:33920
ds_read_b64_tr_b16 v[210:211], v240 offset:34944
v_mfma_f32_16x16x32_bf16 v[20:23], v[212:215], v[132:135], v[20:23]
s_mov_b32 s93, 114688
buffer_load_dwordx4 a[192:195], v228, s[40:43], s93 offen
v_mfma_f32_16x16x32_bf16 v[52:55], v[212:215], v[148:151], v[52:55]
ds_read_b64_tr_b16 v[212:213], v241 offset:33920
ds_read_b64_tr_b16 v[214:215], v241 offset:34944
v_mfma_f32_16x16x32_bf16 v[24:27], v[216:219], v[132:135], v[24:27]
s_mov_b32 s93, 121856
buffer_load_dwordx4 a[196:199], v228, s[40:43], s93 offen
v_mfma_f32_16x16x32_bf16 v[56:59], v[216:219], v[148:151], v[56:59]
ds_read_b64_tr_b16 v[216:217], v242 offset:33920
ds_read_b64_tr_b16 v[218:219], v242 offset:34944
v_mfma_f32_16x16x32_bf16 v[28:31], v[220:223], v[132:135], v[28:31]
s_waitcnt lgkmcnt(6)
s_mov_b32 s93, 129024
buffer_load_dwordx4 a[200:203], v228, s[40:43], s93 offen
v_mfma_f32_16x16x32_bf16 v[60:63], v[220:223], v[148:151], v[60:63]
ds_read_b64_tr_b16 v[220:221], v243 offset:33920
ds_read_b64_tr_b16 v[222:223], v243 offset:34944
v_mfma_f32_16x16x32_bf16 v[0:3], v[192:195], v[136:139], v[0:3]
s_mov_b32 s93, 136192
buffer_load_dwordx4 a[204:207], v228, s[40:43], s93 offen
v_mfma_f32_16x16x32_bf16 v[32:35], v[192:195], v[152:155], v[32:35]
ds_read_b64_tr_b16 v[192:193], v240 offset:42240
ds_read_b64_tr_b16 v[194:195], v240 offset:43264
v_mfma_f32_16x16x32_bf16 v[4:7], v[196:199], v[136:139], v[4:7]
s_mov_b32 s93, 143360
buffer_load_dwordx4 a[208:211], v228, s[40:43], s93 offen
v_mfma_f32_16x16x32_bf16 v[36:39], v[196:199], v[152:155], v[36:39]
ds_read_b64_tr_b16 v[196:197], v241 offset:42240
ds_read_b64_tr_b16 v[198:199], v241 offset:43264
v_mfma_f32_16x16x32_bf16 v[8:11], v[200:203], v[136:139], v[8:11]
s_mov_b32 s93, 150528
buffer_load_dwordx4 a[212:215], v228, s[40:43], s93 offen
v_mfma_f32_16x16x32_bf16 v[40:43], v[200:203], v[152:155], v[40:43]
ds_read_b64_tr_b16 v[200:201], v242 offset:42240
ds_read_b64_tr_b16 v[202:203], v242 offset:43264
v_mfma_f32_16x16x32_bf16 v[12:15], v[204:207], v[136:139], v[12:15]
s_waitcnt lgkmcnt(6)
s_mov_b32 s93, 157696
buffer_load_dwordx4 a[216:219], v228, s[40:43], s93 offen
v_mfma_f32_16x16x32_bf16 v[44:47], v[204:207], v[152:155], v[44:47]
ds_read_b64_tr_b16 v[204:205], v243 offset:42240
ds_read_b64_tr_b16 v[206:207], v243 offset:43264
v_mfma_f32_16x16x32_bf16 v[16:19], v[208:211], v[136:139], v[16:19]
s_mov_b32 s93, 164864
buffer_load_dwordx4 a[220:223], v228, s[40:43], s93 offen
v_mfma_f32_16x16x32_bf16 v[48:51], v[208:211], v[152:155], v[48:51]
ds_read_b64_tr_b16 v[208:209], v240 offset:42368
ds_read_b64_tr_b16 v[210:211], v240 offset:43392
v_mfma_f32_16x16x32_bf16 v[20:23], v[212:215], v[136:139], v[20:23]
s_mov_b32 s93, 172032
buffer_load_dwordx4 a[224:227], v228, s[40:43], s93 offen
v_mfma_f32_16x16x32_bf16 v[52:55], v[212:215], v[152:155], v[52:55]
ds_read_b64_tr_b16 v[212:213], v241 offset:42368
ds_read_b64_tr_b16 v[214:215], v241 offset:43392
v_mfma_f32_16x16x32_bf16 v[24:27], v[216:219], v[136:139], v[24:27]
s_mov_b32 s93, 179200
buffer_load_dwordx4 a[228:231], v228, s[40:43], s93 offen
v_mfma_f32_16x16x32_bf16 v[56:59], v[216:219], v[152:155], v[56:59]
ds_read_b64_tr_b16 v[216:217], v242 offset:42368
ds_read_b64_tr_b16 v[218:219], v242 offset:43392
v_mfma_f32_16x16x32_bf16 v[28:31], v[220:223], v[136:139], v[28:31]
s_waitcnt lgkmcnt(6)
s_mov_b32 s93, 186368
buffer_load_dwordx4 a[232:235], v228, s[40:43], s93 offen
v_mfma_f32_16x16x32_bf16 v[60:63], v[220:223], v[152:155], v[60:63]
ds_read_b64_tr_b16 v[220:221], v243 offset:42368
ds_read_b64_tr_b16 v[222:223], v243 offset:43392
v_mfma_f32_16x16x32_bf16 v[0:3], v[192:195], v[140:143], v[0:3]
s_mov_b32 s93, 193536
buffer_load_dwordx4 a[236:239], v228, s[40:43], s93 offen
v_mfma_f32_16x16x32_bf16 v[32:35], v[192:195], v[156:159], v[32:35]
ds_read_b64_tr_b16 v[192:193], v240 offset:16896
ds_read_b64_tr_b16 v[194:195], v240 offset:17920
v_mfma_f32_16x16x32_bf16 v[4:7], v[196:199], v[140:143], v[4:7]
s_mov_b32 s93, 200704
buffer_load_dwordx4 a[240:243], v228, s[40:43], s93 offen
v_mfma_f32_16x16x32_bf16 v[36:39], v[196:199], v[156:159], v[36:39]
ds_read_b64_tr_b16 v[196:197], v241 offset:16896
ds_read_b64_tr_b16 v[198:199], v241 offset:17920
v_mfma_f32_16x16x32_bf16 v[8:11], v[200:203], v[140:143], v[8:11]
s_mov_b32 s93, 207872
buffer_load_dwordx4 a[244:247], v228, s[40:43], s93 offen
v_mfma_f32_16x16x32_bf16 v[40:43], v[200:203], v[156:159], v[40:43]
ds_read_b64_tr_b16 v[200:201], v242 offset:16896
ds_read_b64_tr_b16 v[202:203], v242 offset:17920
v_mfma_f32_16x16x32_bf16 v[12:15], v[204:207], v[140:143], v[12:15]
s_waitcnt lgkmcnt(6)
s_mov_b32 s93, 215040
buffer_load_dwordx4 a[248:251], v228, s[40:43], s93 offen
v_mfma_f32_16x16x32_bf16 v[44:47], v[204:207], v[156:159], v[44:47]
ds_read_b64_tr_b16 v[204:205], v243 offset:16896
ds_read_b64_tr_b16 v[206:207], v243 offset:17920
v_mfma_f32_16x16x32_bf16 v[16:19], v[208:211], v[140:143], v[16:19]
s_mov_b32 s93, 222208
buffer_load_dwordx4 a[252:255], v228, s[40:43], s93 offen
v_mfma_f32_16x16x32_bf16 v[48:51], v[208:211], v[156:159], v[48:51]
ds_read_b64_tr_b16 v[208:209], v240 offset:17024
ds_read_b64_tr_b16 v[210:211], v240 offset:18048
v_mfma_f32_16x16x32_bf16 v[20:23], v[212:215], v[140:143], v[20:23]
v_mfma_f32_16x16x32_bf16 v[52:55], v[212:215], v[156:159], v[52:55]
ds_read_b64_tr_b16 v[212:213], v241 offset:17024
ds_read_b64_tr_b16 v[214:215], v241 offset:18048
v_mfma_f32_16x16x32_bf16 v[24:27], v[216:219], v[140:143], v[24:27]
v_mfma_f32_16x16x32_bf16 v[56:59], v[216:219], v[156:159], v[56:59]
ds_read_b64_tr_b16 v[216:217], v242 offset:17024
ds_read_b64_tr_b16 v[218:219], v242 offset:18048
v_mfma_f32_16x16x32_bf16 v[28:31], v[220:223], v[140:143], v[28:31]
s_waitcnt lgkmcnt(6)
v_mfma_f32_16x16x32_bf16 v[60:63], v[220:223], v[156:159], v[60:63]
ds_read_b64_tr_b16 v[220:221], v243 offset:17024
ds_read_b64_tr_b16 v[222:223], v243 offset:18048
s_branch .La5_LO_START
; ---- owner: last window, partner-s part added, l checked, O / l leaves
.La5_LA_DC:
.La5_LW2_9:
s_cbranch_execz .La5_LW2_13
s_waitcnt lgkmcnt(4)
v_mfma_f32_16x16x32_bf16 v[192:195], a[64:67], v[128:131], 0
v_exp_f32 v221, v221
v_mfma_f32_16x16x32_bf16 v[192:195], a[68:71], v[132:135], v[192:195]
v_cvt_pk_bf16_f32 v238, v220, v221
s_add_u32 s89, s89, 16384
v_mfma_f32_16x16x32_bf16 v[192:195], a[72:75], v[136:139], v[192:195]
v_exp_f32 v222, v222
v_mfma_f32_16x16x32_bf16 v[192:195], a[76:79], v[140:143], v[192:195]
buffer_load_dwordx4 v255, s[60:63], s17 offen offset:0 sc1 lds
v_mfma_f32_16x16x32_bf16 v[200:203], a[64:67], v[144:147], 0
v_exp_f32 v223, v223
v_mfma_f32_16x16x32_bf16 v[200:203], a[68:71], v[148:151], v[200:203]
v_cvt_pk_bf16_f32 v239, v222, v223
s_and_b32 s89, s89, 0x1ffff
v_mfma_f32_16x16x32_bf16 v[200:203], a[72:75], v[152:155], v[200:203]
v_exp_f32 v192, v192
v_mfma_f32_16x16x32_bf16 v[200:203], a[76:79], v[156:159], v[200:203]
s_waitcnt lgkmcnt(0)
v_mfma_f32_16x16x32_bf16 v[196:199], a[80:83], v[128:131], 0
v_exp_f32 v193, v193
v_mfma_f32_16x16x32_bf16 v[196:199], a[84:87], v[132:135], v[196:199]
v_cvt_pk_bf16_f32 v224, v192, v193
v_accvgpr_read_b32 v254, a229
v_mfma_f32_16x16x32_bf16 v[196:199], a[88:91], v[136:139], v[196:199]
v_exp_f32 v194, v194
v_mfma_f32_16x16x32_bf16 v[196:199], a[92:95], v[140:143], v[196:199]
buffer_load_dwordx4 v255, s[60:63], s17 offen offset:1024 sc1 lds
v_mfma_f32_16x16x32_bf16 v[204:207], a[80:83], v[144:147], 0
v_exp_f32 v195, v195
v_mfma_f32_16x16x32_bf16 v[204:207], a[84:87], v[148:151], v[204:207]
v_cvt_pk_bf16_f32 v225, v194, v195
v_mfma_f32_16x16x32_bf16 v[204:207], a[88:91], v[152:155], v[204:207]
v_exp_f32 v200, v200
v_mfma_f32_16x16x32_bf16 v[204:207], a[92:95], v[156:159], v[204:207]
buffer_load_dwordx4 v255, s[60:63], s17 offen offset:2048 sc1 lds
v_mfma_f32_16x16x32_bf16 v[208:211], a[64:67], v[160:163], 0
v_exp_f32 v201, v201
v_mfma_f32_16x16x32_bf16 v[208:211], a[68:71], v[164:167], v[208:211]
v_cvt_pk_bf16_f32 v228, v200, v201
v_mfma_f32_16x16x32_bf16 v[208:211], a[72:75], v[168:171], v[208:211]
v_exp_f32 v202, v202
v_mfma_f32_16x16x32_bf16 v[208:211], a[76:79], v[172:175], v[208:211]
buffer_load_dwordx4 v255, s[60:63], s17 offen offset:3072 sc1 lds
v_mfma_f32_16x16x32_bf16 v[216:219], a[64:67], v[176:179], 0
v_exp_f32 v203, v203
v_mfma_f32_16x16x32_bf16 v[216:219], a[68:71], v[180:183], v[216:219]
v_cvt_pk_bf16_f32 v229, v202, v203
s_mov_b32 s93, 0
v_mfma_f32_16x16x32_bf16 v[216:219], a[72:75], v[184:187], v[216:219]
v_exp_f32 v196, v196
v_mfma_f32_16x16x32_bf16 v[216:219], a[76:79], v[188:191], v[216:219]
buffer_load_dwordx4 a[128:131], v254, s[40:43], s93 offen
v_mfma_f32_16x16x32_bf16 v[212:215], a[80:83], v[160:163], 0
v_exp_f32 v197, v197
s_add_u32 s17, s17, 8192
v_mfma_f32_16x16x32_bf16 v[212:215], a[84:87], v[164:167], v[212:215]
v_cvt_pk_bf16_f32 v226, v196, v197
s_mov_b32 s93, 7168
s_add_u32 m0, m0, 16384
v_mfma_f32_16x16x32_bf16 v[212:215], a[88:91], v[168:171], v[212:215]
v_exp_f32 v198, v198
s_and_b32 m0, m0, s78
v_mfma_f32_16x16x32_bf16 v[212:215], a[92:95], v[172:175], v[212:215]
buffer_load_dwordx4 a[132:135], v254, s[40:43], s93 offen
v_mfma_f32_16x16x32_bf16 v[220:223], a[80:83], v[176:179], 0
v_exp_f32 v199, v199
v_mfma_f32_16x16x32_bf16 v[220:223], a[84:87], v[180:183], v[220:223]
v_cvt_pk_bf16_f32 v227, v198, v199
v_mfma_f32_16x16x32_bf16 v[220:223], a[88:91], v[184:187], v[220:223]
v_exp_f32 v204, v204
v_mfma_f32_16x16x32_bf16 v[220:223], a[92:95], v[188:191], v[220:223]
s_waitcnt vmcnt(12)
s_barrier
v_mfma_f32_16x16x32_bf16 v[64:67], a[96:99], v[232:235], v[64:67]
v_exp_f32 v205, v205
v_mfma_f32_16x16x32_bf16 v[96:99], a[96:99], v[236:239], v[96:99]
ds_read_b128 a[96:99], v248 offset:8192
v_cvt_pk_bf16_f32 v230, v204, v205
v_mfma_f32_16x16x32_bf16 v[68:71], a[100:103], v[232:235], v[68:71]
v_exp_f32 v206, v206
v_mfma_f32_16x16x32_bf16 v[100:103], a[100:103], v[236:239], v[100:103]
ds_read_b128 a[100:103], v248 offset:9216
v_mfma_f32_16x16x32_bf16 v[72:75], a[104:107], v[232:235], v[72:75]
v_exp_f32 v207, v207
v_mfma_f32_16x16x32_bf16 v[104:107], a[104:107], v[236:239], v[104:107]
ds_read_b128 a[104:107], v248 offset:10240
v_cvt_pk_bf16_f32 v231, v206, v207
s_mov_b32 s93, 14336
v_mfma_f32_16x16x32_bf16 v[76:79], a[108:111], v[232:235], v[76:79]
v_exp_f32 v208, v208
v_mfma_f32_16x16x32_bf16 v[108:111], a[108:111], v[236:239], v[108:111]
ds_read_b128 a[108:111], v248 offset:11264
buffer_load_dwordx4 a[136:139], v254, s[40:43], s93 offen
v_mfma_f32_16x16x32_bf16 v[80:83], a[112:115], v[232:235], v[80:83]
v_exp_f32 v209, v209
v_mfma_f32_16x16x32_bf16 v[112:115], a[112:115], v[236:239], v[112:115]
ds_read_b128 a[112:115], v248 offset:12288
s_mov_b32 s93, 21504
v_mfma_f32_16x16x32_bf16 v[84:87], a[116:119], v[232:235], v[84:87]
v_exp_f32 v210, v210
v_mfma_f32_16x16x32_bf16 v[116:119], a[116:119], v[236:239], v[116:119]
ds_read_b128 a[116:119], v248 offset:13312
buffer_load_dwordx4 a[140:143], v254, s[40:43], s93 offen
v_mfma_f32_16x16x32_bf16 v[88:91], a[120:123], v[232:235], v[88:91]
v_exp_f32 v211, v211
v_mfma_f32_16x16x32_bf16 v[120:123], a[120:123], v[236:239], v[120:123]
ds_read_b128 a[120:123], v248 offset:14336
v_mfma_f32_16x16x32_bf16 v[92:95], a[124:127], v[232:235], v[92:95]
v_exp_f32 v216, v216
v_mfma_f32_16x16x32_bf16 v[124:127], a[124:127], v[236:239], v[124:127]
.La5_LW2_10:
s_cbranch_execz .La5_LW2_14
ds_read_b128 a[124:127], v248 offset:15360
v_or_b32 v248, s89, v255
v_smfmac_f32_16x16x64_bf16 a[196:199], a[208:211], v[232:239], v241
s_waitcnt lgkmcnt(4)
v_smfmac_f32_16x16x64_bf16 a[192:195], a[208:211], v[224:231], v241
ds_read_b128 a[64:67], v248 offset:0
v_mfma_f32_16x16x32_bf16 v[0:3], a[96:99], v[224:227], v[0:3]
v_exp_f32 v217, v217
v_mfma_f32_16x16x32_bf16 v[32:35], a[96:99], v[228:231], v[32:35]
ds_read_b128 a[68:71], v248 offset:1024
v_cvt_pk_bf16_f32 v232, v208, v209
v_mfma_f32_16x16x32_bf16 v[4:7], a[100:103], v[224:227], v[4:7]
v_exp_f32 v218, v218
v_mfma_f32_16x16x32_bf16 v[36:39], a[100:103], v[228:231], v[36:39]
ds_read_b128 a[72:75], v248 offset:2048
v_cvt_pk_bf16_f32 v233, v210, v211
v_mfma_f32_16x16x32_bf16 v[8:11], a[104:107], v[224:227], v[8:11]
v_exp_f32 v219, v219
v_mfma_f32_16x16x32_bf16 v[40:43], a[104:107], v[228:231], v[40:43]
ds_read_b128 a[76:79], v248 offset:3072
v_cvt_pk_bf16_f32 v236, v216, v217
v_mfma_f32_16x16x32_bf16 v[12:15], a[108:111], v[224:227], v[12:15]
v_exp_f32 v212, v212
v_mfma_f32_16x16x32_bf16 v[44:47], a[108:111], v[228:231], v[44:47]
ds_read_b128 a[80:83], v248 offset:4096
s_waitcnt lgkmcnt(5)
v_mfma_f32_16x16x32_bf16 v[16:19], a[112:115], v[224:227], v[16:19]
v_exp_f32 v213, v213
v_mfma_f32_16x16x32_bf16 v[48:51], a[112:115], v[228:231], v[48:51]
ds_read_b128 a[84:87], v248 offset:5120
v_cvt_pk_bf16_f32 v237, v218, v219
v_mfma_f32_16x16x32_bf16 v[20:23], a[116:119], v[224:227], v[20:23]
v_exp_f32 v214, v214
v_mfma_f32_16x16x32_bf16 v[52:55], a[116:119], v[228:231], v[52:55]
ds_read_b128 a[88:91], v248 offset:6144
v_cvt_pk_bf16_f32 v234, v212, v213
v_mfma_f32_16x16x32_bf16 v[24:27], a[120:123], v[224:227], v[24:27]
v_exp_f32 v215, v215
v_mfma_f32_16x16x32_bf16 v[56:59], a[120:123], v[228:231], v[56:59]
ds_read_b128 a[92:95], v248 offset:7168
v_cvt_pk_bf16_f32 v235, v214, v215
v_mfma_f32_16x16x32_bf16 v[28:31], a[124:127], v[224:227], v[28:31]
v_exp_f32 v220, v220
v_mfma_f32_16x16x32_bf16 v[60:63], a[124:127], v[228:231], v[60:63]
s_waitcnt lgkmcnt(4)
v_mfma_f32_16x16x32_bf16 v[192:195], a[64:67], v[128:131], 0
v_exp_f32 v221, v221
v_mfma_f32_16x16x32_bf16 v[192:195], a[68:71], v[132:135], v[192:195]
v_cvt_pk_bf16_f32 v238, v220, v221
s_add_u32 s89, s89, 16384
v_mfma_f32_16x16x32_bf16 v[192:195], a[72:75], v[136:139], v[192:195]
v_exp_f32 v222, v222
v_mfma_f32_16x16x32_bf16 v[192:195], a[76:79], v[140:143], v[192:195]
buffer_load_dwordx4 v255, s[60:63], s17 offen offset:0 sc1 lds
v_mfma_f32_16x16x32_bf16 v[200:203], a[64:67], v[144:147], 0
v_exp_f32 v223, v223
v_mfma_f32_16x16x32_bf16 v[200:203], a[68:71], v[148:151], v[200:203]
v_cvt_pk_bf16_f32 v239, v222, v223
s_and_b32 s89, s89, 0x1ffff
v_mfma_f32_16x16x32_bf16 v[200:203], a[72:75], v[152:155], v[200:203]
v_exp_f32 v192, v192
v_mfma_f32_16x16x32_bf16 v[200:203], a[76:79], v[156:159], v[200:203]
s_waitcnt lgkmcnt(0)
v_mfma_f32_16x16x32_bf16 v[196:199], a[80:83], v[128:131], 0
v_exp_f32 v193, v193
v_mfma_f32_16x16x32_bf16 v[196:199], a[84:87], v[132:135], v[196:199]
v_cvt_pk_bf16_f32 v224, v192, v193
v_mfma_f32_16x16x32_bf16 v[196:199], a[88:91], v[136:139], v[196:199]
v_exp_f32 v194, v194
v_mfma_f32_16x16x32_bf16 v[196:199], a[92:95], v[140:143], v[196:199]
buffer_load_dwordx4 v255, s[60:63], s17 offen offset:1024 sc1 lds
v_mfma_f32_16x16x32_bf16 v[204:207], a[80:83], v[144:147], 0
v_exp_f32 v195, v195
v_mfma_f32_16x16x32_bf16 v[204:207], a[84:87], v[148:151], v[204:207]
v_cvt_pk_bf16_f32 v225, v194, v195
v_mfma_f32_16x16x32_bf16 v[204:207], a[88:91], v[152:155], v[204:207]
v_exp_f32 v200, v200
v_mfma_f32_16x16x32_bf16 v[204:207], a[92:95], v[156:159], v[204:207]
buffer_load_dwordx4 v255, s[60:63], s17 offen offset:2048 sc1 lds
v_mfma_f32_16x16x32_bf16 v[208:211], a[64:67], v[160:163], 0
v_exp_f32 v201, v201
v_mfma_f32_16x16x32_bf16 v[208:211], a[68:71], v[164:167], v[208:211]
v_cvt_pk_bf16_f32 v228, v200, v201
v_mfma_f32_16x16x32_bf16 v[208:211], a[72:75], v[168:171], v[208:211]
v_exp_f32 v202, v202
v_mfma_f32_16x16x32_bf16 v[208:211], a[76:79], v[172:175], v[208:211]
buffer_load_dwordx4 v255, s[60:63], s17 offen offset:3072 sc1 lds
v_mfma_f32_16x16x32_bf16 v[216:219], a[64:67], v[176:179], 0
v_exp_f32 v203, v203
v_mfma_f32_16x16x32_bf16 v[216:219], a[68:71], v[180:183], v[216:219]
v_cvt_pk_bf16_f32 v229, v202, v203
s_mov_b32 s93, 28672
v_mfma_f32_16x16x32_bf16 v[216:219], a[72:75], v[184:187], v[216:219]
v_exp_f32 v196, v196
v_mfma_f32_16x16x32_bf16 v[216:219], a[76:79], v[188:191], v[216:219]
buffer_load_dwordx4 a[144:147], v254, s[40:43], s93 offen
v_mfma_f32_16x16x32_bf16 v[212:215], a[80:83], v[160:163], 0
v_exp_f32 v197, v197
s_add_u32 s17, s17, 8192
v_mfma_f32_16x16x32_bf16 v[212:215], a[84:87], v[164:167], v[212:215]
v_cvt_pk_bf16_f32 v226, v196, v197
s_mov_b32 s93, 35840
s_add_u32 m0, m0, 16384
v_mfma_f32_16x16x32_bf16 v[212:215], a[88:91], v[168:171], v[212:215]
v_exp_f32 v198, v198
s_and_b32 m0, m0, s78
v_mfma_f32_16x16x32_bf16 v[212:215], a[92:95], v[172:175], v[212:215]
buffer_load_dwordx4 a[148:151], v254, s[40:43], s93 offen
v_mfma_f32_16x16x32_bf16 v[220:223], a[80:83], v[176:179], 0
v_exp_f32 v199, v199
v_mfma_f32_16x16x32_bf16 v[220:223], a[84:87], v[180:183], v[220:223]
v_cvt_pk_bf16_f32 v227, v198, v199
v_mfma_f32_16x16x32_bf16 v[220:223], a[88:91], v[184:187], v[220:223]
v_exp_f32 v204, v204
v_mfma_f32_16x16x32_bf16 v[220:223], a[92:95], v[188:191], v[220:223]
s_waitcnt vmcnt(15)
s_barrier
v_mfma_f32_16x16x32_bf16 v[64:67], a[96:99], v[232:235], v[64:67]
v_exp_f32 v205, v205
v_mfma_f32_16x16x32_bf16 v[96:99], a[96:99], v[236:239], v[96:99]
ds_read_b128 a[96:99], v248 offset:8192
v_cvt_pk_bf16_f32 v230, v204, v205
v_mfma_f32_16x16x32_bf16 v[68:71], a[100:103], v[232:235], v[68:71]
v_exp_f32 v206, v206
v_mfma_f32_16x16x32_bf16 v[100:103], a[100:103], v[236:239], v[100:103]
ds_read_b128 a[100:103], v248 offset:9216
v_mfma_f32_16x16x32_bf16 v[72:75], a[104:107], v[232:235], v[72:75]
v_exp_f32 v207, v207
v_mfma_f32_16x16x32_bf16 v[104:107], a[104:107], v[236:239], v[104:107]
ds_read_b128 a[104:107], v248 offset:10240
v_cvt_pk_bf16_f32 v231, v206, v207
s_mov_b32 s93, 43008
v_mfma_f32_16x16x32_bf16 v[76:79], a[108:111], v[232:235], v[76:79]
v_exp_f32 v208, v208
v_mfma_f32_16x16x32_bf16 v[108:111], a[108:111], v[236:239], v[108:111]
ds_read_b128 a[108:111], v248 offset:11264
buffer_load_dwordx4 a[152:155], v254, s[40:43], s93 offen
v_mfma_f32_16x16x32_bf16 v[80:83], a[112:115], v[232:235], v[80:83]
v_exp_f32 v209, v209
v_mfma_f32_16x16x32_bf16 v[112:115], a[112:115], v[236:239], v[112:115]
ds_read_b128 a[112:115], v248 offset:12288
s_mov_b32 s93, 50176
v_mfma_f32_16x16x32_bf16 v[84:87], a[116:119], v[232:235], v[84:87]
v_exp_f32 v210, v210
v_mfma_f32_16x16x32_bf16 v[116:119], a[116:119], v[236:239], v[116:119]
ds_read_b128 a[116:119], v248 offset:13312
buffer_load_dwordx4 a[156:159], v254, s[40:43], s93 offen
v_mfma_f32_16x16x32_bf16 v[88:91], a[120:123], v[232:235], v[88:91]
v_exp_f32 v211, v211
v_mfma_f32_16x16x32_bf16 v[120:123], a[120:123], v[236:239], v[120:123]
ds_read_b128 a[120:123], v248 offset:14336
v_mfma_f32_16x16x32_bf16 v[92:95], a[124:127], v[232:235], v[92:95]
v_exp_f32 v216, v216
v_mfma_f32_16x16x32_bf16 v[124:127], a[124:127], v[236:239], v[124:127]
ds_read_b128 a[124:127], v248 offset:15360
v_or_b32 v248, s89, v255
v_smfmac_f32_16x16x64_bf16 a[196:199], a[208:211], v[232:239], v241
s_waitcnt lgkmcnt(4)
v_smfmac_f32_16x16x64_bf16 a[192:195], a[208:211], v[224:231], v241
ds_read_b128 a[64:67], v248 offset:0
v_mfma_f32_16x16x32_bf16 v[0:3], a[96:99], v[224:227], v[0:3]
v_exp_f32 v217, v217
v_mfma_f32_16x16x32_bf16 v[32:35], a[96:99], v[228:231], v[32:35]
ds_read_b128 a[68:71], v248 offset:1024
v_cvt_pk_bf16_f32 v232, v208, v209
v_mfma_f32_16x16x32_bf16 v[4:7], a[100:103], v[224:227], v[4:7]
v_exp_f32 v218, v218
v_mfma_f32_16x16x32_bf16 v[36:39], a[100:103], v[228:231], v[36:39]
ds_read_b128 a[72:75], v248 offset:2048
v_cvt_pk_bf16_f32 v233, v210, v211
v_mfma_f32_16x16x32_bf16 v[8:11], a[104:107], v[224:227], v[8:11]
v_exp_f32 v219, v219
v_mfma_f32_16x16x32_bf16 v[40:43], a[104:107], v[228:231], v[40:43]
ds_read_b128 a[76:79], v248 offset:3072
v_cvt_pk_bf16_f32 v236, v216, v217
v_mfma_f32_16x16x32_bf16 v[12:15], a[108:111], v[224:227], v[12:15]
v_exp_f32 v212, v212
v_mfma_f32_16x16x32_bf16 v[44:47], a[108:111], v[228:231], v[44:47]
ds_read_b128 a[80:83], v248 offset:4096
s_waitcnt lgkmcnt(5)
v_mfma_f32_16x16x32_bf16 v[16:19], a[112:115], v[224:227], v[16:19]
v_exp_f32 v213, v213
v_mfma_f32_16x16x32_bf16 v[48:51], a[112:115], v[228:231], v[48:51]
ds_read_b128 a[84:87], v248 offset:5120
v_cvt_pk_bf16_f32 v237, v218, v219
v_mfma_f32_16x16x32_bf16 v[20:23], a[116:119], v[224:227], v[20:23]
v_exp_f32 v214, v214
v_mfma_f32_16x16x32_bf16 v[52:55], a[116:119], v[228:231], v[52:55]
ds_read_b128 a[88:91], v248 offset:6144
v_cvt_pk_bf16_f32 v234, v212, v213
v_mfma_f32_16x16x32_bf16 v[24:27], a[120:123], v[224:227], v[24:27]
v_exp_f32 v215, v215
v_mfma_f32_16x16x32_bf16 v[56:59], a[120:123], v[228:231], v[56:59]
ds_read_b128 a[92:95], v248 offset:7168
v_cvt_pk_bf16_f32 v235, v214, v215
v_mfma_f32_16x16x32_bf16 v[28:31], a[124:127], v[224:227], v[28:31]
v_exp_f32 v220, v220
v_mfma_f32_16x16x32_bf16 v[60:63], a[124:127], v[228:231], v[60:63]
s_waitcnt lgkmcnt(4)
v_mfma_f32_16x16x32_bf16 v[192:195], a[64:67], v[128:131], 0
v_exp_f32 v221, v221
v_mfma_f32_16x16x32_bf16 v[192:195], a[68:71], v[132:135], v[192:195]
v_cvt_pk_bf16_f32 v238, v220, v221
s_add_u32 s89, s89, 16384
v_mfma_f32_16x16x32_bf16 v[192:195], a[72:75], v[136:139], v[192:195]
v_exp_f32 v222, v222
v_mfma_f32_16x16x32_bf16 v[192:195], a[76:79], v[140:143], v[192:195]
buffer_load_dwordx4 v255, s[60:63], s17 offen offset:0 sc1 lds
v_mfma_f32_16x16x32_bf16 v[200:203], a[64:67], v[144:147], 0
v_exp_f32 v223, v223
v_mfma_f32_16x16x32_bf16 v[200:203], a[68:71], v[148:151], v[200:203]
v_cvt_pk_bf16_f32 v239, v222, v223
s_and_b32 s89, s89, 0x1ffff
v_mfma_f32_16x16x32_bf16 v[200:203], a[72:75], v[152:155], v[200:203]
v_exp_f32 v192, v192
v_mfma_f32_16x16x32_bf16 v[200:203], a[76:79], v[156:159], v[200:203]
s_waitcnt lgkmcnt(0)
v_mfma_f32_16x16x32_bf16 v[196:199], a[80:83], v[128:131], 0
v_exp_f32 v193, v193
v_mfma_f32_16x16x32_bf16 v[196:199], a[84:87], v[132:135], v[196:199]
v_cvt_pk_bf16_f32 v224, v192, v193
v_mfma_f32_16x16x32_bf16 v[196:199], a[88:91], v[136:139], v[196:199]
v_exp_f32 v194, v194
v_mfma_f32_16x16x32_bf16 v[196:199], a[92:95], v[140:143], v[196:199]
buffer_load_dwordx4 v255, s[60:63], s17 offen offset:1024 sc1 lds
v_mfma_f32_16x16x32_bf16 v[204:207], a[80:83], v[144:147], 0
v_exp_f32 v195, v195
v_mfma_f32_16x16x32_bf16 v[204:207], a[84:87], v[148:151], v[204:207]
v_cvt_pk_bf16_f32 v225, v194, v195
v_mfma_f32_16x16x32_bf16 v[204:207], a[88:91], v[152:155], v[204:207]
v_exp_f32 v200, v200
v_mfma_f32_16x16x32_bf16 v[204:207], a[92:95], v[156:159], v[204:207]
buffer_load_dwordx4 v255, s[60:63], s17 offen offset:2048 sc1 lds
v_mfma_f32_16x16x32_bf16 v[208:211], a[64:67], v[160:163], 0
v_exp_f32 v201, v201
v_mfma_f32_16x16x32_bf16 v[208:211], a[68:71], v[164:167], v[208:211]
v_cvt_pk_bf16_f32 v228, v200, v201
v_mfma_f32_16x16x32_bf16 v[208:211], a[72:75], v[168:171], v[208:211]
v_exp_f32 v202, v202
v_mfma_f32_16x16x32_bf16 v[208:211], a[76:79], v[172:175], v[208:211]
buffer_load_dwordx4 v255, s[60:63], s17 offen offset:3072 sc1 lds
v_mfma_f32_16x16x32_bf16 v[216:219], a[64:67], v[176:179], 0
v_exp_f32 v203, v203
v_mfma_f32_16x16x32_bf16 v[216:219], a[68:71], v[180:183], v[216:219]
v_cvt_pk_bf16_f32 v229, v202, v203
s_mov_b32 s93, 57344
v_mfma_f32_16x16x32_bf16 v[216:219], a[72:75], v[184:187], v[216:219]
v_exp_f32 v196, v196
v_mfma_f32_16x16x32_bf16 v[216:219], a[76:79], v[188:191], v[216:219]
buffer_load_dwordx4 a[160:163], v254, s[40:43], s93 offen
v_mfma_f32_16x16x32_bf16 v[212:215], a[80:83], v[160:163], 0
v_exp_f32 v197, v197
s_add_u32 s17, s17, 8192
v_mfma_f32_16x16x32_bf16 v[212:215], a[84:87], v[164:167], v[212:215]
v_cvt_pk_bf16_f32 v226, v196, v197
s_mov_b32 s93, 64512
s_add_u32 m0, m0, 16384
v_mfma_f32_16x16x32_bf16 v[212:215], a[88:91], v[168:171], v[212:215]
v_exp_f32 v198, v198
s_and_b32 m0, m0, s78
v_mfma_f32_16x16x32_bf16 v[212:215], a[92:95], v[172:175], v[212:215]
buffer_load_dwordx4 a[164:167], v254, s[40:43], s93 offen
v_mfma_f32_16x16x32_bf16 v[220:223], a[80:83], v[176:179], 0
v_exp_f32 v199, v199
v_mfma_f32_16x16x32_bf16 v[220:223], a[84:87], v[180:183], v[220:223]
v_cvt_pk_bf16_f32 v227, v198, v199
v_mfma_f32_16x16x32_bf16 v[220:223], a[88:91], v[184:187], v[220:223]
v_exp_f32 v204, v204
v_mfma_f32_16x16x32_bf16 v[220:223], a[92:95], v[188:191], v[220:223]
s_waitcnt vmcnt(18)
s_barrier
v_mfma_f32_16x16x32_bf16 v[64:67], a[96:99], v[232:235], v[64:67]
v_exp_f32 v205, v205
v_mfma_f32_16x16x32_bf16 v[96:99], a[96:99], v[236:239], v[96:99]
ds_read_b128 a[96:99], v248 offset:8192
v_cvt_pk_bf16_f32 v230, v204, v205
v_mfma_f32_16x16x32_bf16 v[68:71], a[100:103], v[232:235], v[68:71]
v_exp_f32 v206, v206
v_mfma_f32_16x16x32_bf16 v[100:103], a[100:103], v[236:239], v[100:103]
ds_read_b128 a[100:103], v248 offset:9216
v_mfma_f32_16x16x32_bf16 v[72:75], a[104:107], v[232:235], v[72:75]
v_exp_f32 v207, v207
v_mfma_f32_16x16x32_bf16 v[104:107], a[104:107], v[236:239], v[104:107]
ds_read_b128 a[104:107], v248 offset:10240
v_cvt_pk_bf16_f32 v231, v206, v207
s_mov_b32 s93, 71680
v_mfma_f32_16x16x32_bf16 v[76:79], a[108:111], v[232:235], v[76:79]
v_exp_f32 v208, v208
v_mfma_f32_16x16x32_bf16 v[108:111], a[108:111], v[236:239], v[108:111]
ds_read_b128 a[108:111], v248 offset:11264
buffer_load_dwordx4 a[168:171], v254, s[40:43], s93 offen
v_mfma_f32_16x16x32_bf16 v[80:83], a[112:115], v[232:235], v[80:83]
v_exp_f32 v209, v209
v_mfma_f32_16x16x32_bf16 v[112:115], a[112:115], v[236:239], v[112:115]
ds_read_b128 a[112:115], v248 offset:12288
s_mov_b32 s93, 78848
v_mfma_f32_16x16x32_bf16 v[84:87], a[116:119], v[232:235], v[84:87]
v_exp_f32 v210, v210
v_mfma_f32_16x16x32_bf16 v[116:119], a[116:119], v[236:239], v[116:119]
ds_read_b128 a[116:119], v248 offset:13312
buffer_load_dwordx4 a[172:175], v254, s[40:43], s93 offen
v_mfma_f32_16x16x32_bf16 v[88:91], a[120:123], v[232:235], v[88:91]
v_exp_f32 v211, v211
v_mfma_f32_16x16x32_bf16 v[120:123], a[120:123], v[236:239], v[120:123]
ds_read_b128 a[120:123], v248 offset:14336
v_mfma_f32_16x16x32_bf16 v[92:95], a[124:127], v[232:235], v[92:95]
v_exp_f32 v216, v216
v_mfma_f32_16x16x32_bf16 v[124:127], a[124:127], v[236:239], v[124:127]
ds_read_b128 a[124:127], v248 offset:15360
v_or_b32 v248, s89, v255
v_smfmac_f32_16x16x64_bf16 a[196:199], a[208:211], v[232:239], v241
s_waitcnt lgkmcnt(4)
v_smfmac_f32_16x16x64_bf16 a[192:195], a[208:211], v[224:231], v241
ds_read_b128 a[64:67], v248 offset:0
v_mfma_f32_16x16x32_bf16 v[0:3], a[96:99], v[224:227], v[0:3]
v_exp_f32 v217, v217
v_mfma_f32_16x16x32_bf16 v[32:35], a[96:99], v[228:231], v[32:35]
ds_read_b128 a[68:71], v248 offset:1024
v_cvt_pk_bf16_f32 v232, v208, v209
v_mfma_f32_16x16x32_bf16 v[4:7], a[100:103], v[224:227], v[4:7]
v_exp_f32 v218, v218
v_mfma_f32_16x16x32_bf16 v[36:39], a[100:103], v[228:231], v[36:39]
ds_read_b128 a[72:75], v248 offset:2048
v_cvt_pk_bf16_f32 v233, v210, v211
v_mfma_f32_16x16x32_bf16 v[8:11], a[104:107], v[224:227], v[8:11]
v_exp_f32 v219, v219
v_mfma_f32_16x16x32_bf16 v[40:43], a[104:107], v[228:231], v[40:43]
ds_read_b128 a[76:79], v248 offset:3072
v_cvt_pk_bf16_f32 v236, v216, v217
v_mfma_f32_16x16x32_bf16 v[12:15], a[108:111], v[224:227], v[12:15]
v_exp_f32 v212, v212
v_mfma_f32_16x16x32_bf16 v[44:47], a[108:111], v[228:231], v[44:47]
ds_read_b128 a[80:83], v248 offset:4096
s_waitcnt lgkmcnt(5)
v_mfma_f32_16x16x32_bf16 v[16:19], a[112:115], v[224:227], v[16:19]
v_exp_f32 v213, v213
v_mfma_f32_16x16x32_bf16 v[48:51], a[112:115], v[228:231], v[48:51]
ds_read_b128 a[84:87], v248 offset:5120
v_cvt_pk_bf16_f32 v237, v218, v219
v_mfma_f32_16x16x32_bf16 v[20:23], a[116:119], v[224:227], v[20:23]
v_exp_f32 v214, v214
v_mfma_f32_16x16x32_bf16 v[52:55], a[116:119], v[228:231], v[52:55]
ds_read_b128 a[88:91], v248 offset:6144
v_cvt_pk_bf16_f32 v234, v212, v213
v_mfma_f32_16x16x32_bf16 v[24:27], a[120:123], v[224:227], v[24:27]
v_exp_f32 v215, v215
v_mfma_f32_16x16x32_bf16 v[56:59], a[120:123], v[228:231], v[56:59]
ds_read_b128 a[92:95], v248 offset:7168
v_cvt_pk_bf16_f32 v235, v214, v215
v_mfma_f32_16x16x32_bf16 v[28:31], a[124:127], v[224:227], v[28:31]
v_exp_f32 v220, v220
v_mfma_f32_16x16x32_bf16 v[60:63], a[124:127], v[228:231], v[60:63]
s_waitcnt lgkmcnt(4)
v_mfma_f32_16x16x32_bf16 v[192:195], a[64:67], v[128:131], 0
v_exp_f32 v221, v221
v_mfma_f32_16x16x32_bf16 v[192:195], a[68:71], v[132:135], v[192:195]
v_cvt_pk_bf16_f32 v238, v220, v221
v_accvgpr_read_b32 v240, a228
v_mfma_f32_16x16x32_bf16 v[192:195], a[72:75], v[136:139], v[192:195]
v_exp_f32 v222, v222
v_mfma_f32_16x16x32_bf16 v[192:195], a[76:79], v[140:143], v[192:195]
s_add_u32 m0, s83, 16896
s_lshl_b32 s17, s72, 18
v_mfma_f32_16x16x32_bf16 v[200:203], a[64:67], v[144:147], 0
v_exp_f32 v223, v223
v_mfma_f32_16x16x32_bf16 v[200:203], a[68:71], v[148:151], v[200:203]
v_cvt_pk_bf16_f32 v239, v222, v223
s_add_u32 s89, s89, 16384
v_mfma_f32_16x16x32_bf16 v[200:203], a[72:75], v[152:155], v[200:203]
v_exp_f32 v192, v192
v_mfma_f32_16x16x32_bf16 v[200:203], a[76:79], v[156:159], v[200:203]
s_waitcnt lgkmcnt(0)
s_and_b32 s89, s89, 0x1ffff
v_mfma_f32_16x16x32_bf16 v[196:199], a[80:83], v[128:131], 0
v_exp_f32 v193, v193
v_mfma_f32_16x16x32_bf16 v[196:199], a[84:87], v[132:135], v[196:199]
v_cvt_pk_bf16_f32 v224, v192, v193
v_mfma_f32_16x16x32_bf16 v[196:199], a[88:91], v[136:139], v[196:199]
v_exp_f32 v194, v194
v_mfma_f32_16x16x32_bf16 v[196:199], a[92:95], v[140:143], v[196:199]
buffer_load_dwordx4 v240, s[56:59], s17 offen lds
v_mfma_f32_16x16x32_bf16 v[204:207], a[80:83], v[144:147], 0
v_exp_f32 v195, v195
s_add_u32 s17, s17, 32768
v_mfma_f32_16x16x32_bf16 v[204:207], a[84:87], v[148:151], v[204:207]
v_cvt_pk_bf16_f32 v225, v194, v195
s_add_u32 m0, m0, 1024
v_mfma_f32_16x16x32_bf16 v[204:207], a[88:91], v[152:155], v[204:207]
v_exp_f32 v200, v200
v_mfma_f32_16x16x32_bf16 v[204:207], a[92:95], v[156:159], v[204:207]
buffer_load_dwordx4 v240, s[56:59], s17 offen lds
v_mfma_f32_16x16x32_bf16 v[208:211], a[64:67], v[160:163], 0
v_exp_f32 v201, v201
s_add_u32 s17, s17, 4294942720
v_mfma_f32_16x16x32_bf16 v[208:211], a[68:71], v[164:167], v[208:211]
v_cvt_pk_bf16_f32 v228, v200, v201
s_add_u32 m0, m0, 1152
v_mfma_f32_16x16x32_bf16 v[208:211], a[72:75], v[168:171], v[208:211]
v_exp_f32 v202, v202
v_mfma_f32_16x16x32_bf16 v[208:211], a[76:79], v[172:175], v[208:211]
buffer_load_dwordx4 v240, s[56:59], s17 offen lds
v_mfma_f32_16x16x32_bf16 v[216:219], a[64:67], v[176:179], 0
v_exp_f32 v203, v203
s_add_u32 s17, s17, 32768
v_mfma_f32_16x16x32_bf16 v[216:219], a[68:71], v[180:183], v[216:219]
v_cvt_pk_bf16_f32 v229, v202, v203
s_add_u32 m0, m0, 1024
v_mfma_f32_16x16x32_bf16 v[216:219], a[72:75], v[184:187], v[216:219]
v_exp_f32 v196, v196
v_mfma_f32_16x16x32_bf16 v[216:219], a[76:79], v[188:191], v[216:219]
buffer_load_dwordx4 v240, s[56:59], s17 offen lds
v_mfma_f32_16x16x32_bf16 v[212:215], a[80:83], v[160:163], 0
v_exp_f32 v197, v197
s_add_u32 s17, s17, 90112
v_mfma_f32_16x16x32_bf16 v[212:215], a[84:87], v[164:167], v[212:215]
v_cvt_pk_bf16_f32 v226, v196, v197
s_mov_b32 s93, 86016
s_add_u32 m0, s83, 33792
v_mfma_f32_16x16x32_bf16 v[212:215], a[88:91], v[168:171], v[212:215]
v_exp_f32 v198, v198
v_mfma_f32_16x16x32_bf16 v[212:215], a[92:95], v[172:175], v[212:215]
buffer_load_dwordx4 a[176:179], v254, s[40:43], s93 offen
v_mfma_f32_16x16x32_bf16 v[220:223], a[80:83], v[176:179], 0
v_exp_f32 v199, v199
v_mfma_f32_16x16x32_bf16 v[220:223], a[84:87], v[180:183], v[220:223]
v_cvt_pk_bf16_f32 v227, v198, v199
v_mfma_f32_16x16x32_bf16 v[220:223], a[88:91], v[184:187], v[220:223]
v_exp_f32 v204, v204
v_mfma_f32_16x16x32_bf16 v[220:223], a[92:95], v[188:191], v[220:223]
s_waitcnt vmcnt(17)
s_barrier
v_mfma_f32_16x16x32_bf16 v[64:67], a[96:99], v[232:235], v[64:67]
v_exp_f32 v205, v205
v_mfma_f32_16x16x32_bf16 v[96:99], a[96:99], v[236:239], v[96:99]
ds_read_b128 a[96:99], v248 offset:8192
v_cvt_pk_bf16_f32 v230, v204, v205
v_mfma_f32_16x16x32_bf16 v[68:71], a[100:103], v[232:235], v[68:71]
v_exp_f32 v206, v206
v_mfma_f32_16x16x32_bf16 v[100:103], a[100:103], v[236:239], v[100:103]
ds_read_b128 a[100:103], v248 offset:9216
v_mfma_f32_16x16x32_bf16 v[72:75], a[104:107], v[232:235], v[72:75]
v_exp_f32 v207, v207
v_mfma_f32_16x16x32_bf16 v[104:107], a[104:107], v[236:239], v[104:107]
ds_read_b128 a[104:107], v248 offset:10240
v_cvt_pk_bf16_f32 v231, v206, v207
s_mov_b32 s93, 93184
v_mfma_f32_16x16x32_bf16 v[76:79], a[108:111], v[232:235], v[76:79]
v_exp_f32 v208, v208
v_mfma_f32_16x16x32_bf16 v[108:111], a[108:111], v[236:239], v[108:111]
ds_read_b128 a[108:111], v248 offset:11264
buffer_load_dwordx4 a[180:183], v254, s[40:43], s93 offen
v_mfma_f32_16x16x32_bf16 v[80:83], a[112:115], v[232:235], v[80:83]
v_exp_f32 v209, v209
v_mfma_f32_16x16x32_bf16 v[112:115], a[112:115], v[236:239], v[112:115]
ds_read_b128 a[112:115], v248 offset:12288
s_mov_b32 s93, 100352
v_mfma_f32_16x16x32_bf16 v[84:87], a[116:119], v[232:235], v[84:87]
v_exp_f32 v210, v210
v_mfma_f32_16x16x32_bf16 v[116:119], a[116:119], v[236:239], v[116:119]
ds_read_b128 a[116:119], v248 offset:13312
buffer_load_dwordx4 a[184:187], v254, s[40:43], s93 offen
v_mfma_f32_16x16x32_bf16 v[88:91], a[120:123], v[232:235], v[88:91]
v_exp_f32 v211, v211
v_mfma_f32_16x16x32_bf16 v[120:123], a[120:123], v[236:239], v[120:123]
ds_read_b128 a[120:123], v248 offset:14336
v_mfma_f32_16x16x32_bf16 v[92:95], a[124:127], v[232:235], v[92:95]
v_exp_f32 v216, v216
v_mfma_f32_16x16x32_bf16 v[124:127], a[124:127], v[236:239], v[124:127]
ds_read_b128 a[124:127], v248 offset:15360
v_or_b32 v248, s89, v255
v_smfmac_f32_16x16x64_bf16 a[196:199], a[208:211], v[232:239], v241
s_waitcnt lgkmcnt(4)
v_smfmac_f32_16x16x64_bf16 a[192:195], a[208:211], v[224:231], v241
ds_read_b128 a[64:67], v248 offset:0
v_mfma_f32_16x16x32_bf16 v[0:3], a[96:99], v[224:227], v[0:3]
v_exp_f32 v217, v217
v_mfma_f32_16x16x32_bf16 v[32:35], a[96:99], v[228:231], v[32:35]
ds_read_b128 a[68:71], v248 offset:1024
v_cvt_pk_bf16_f32 v232, v208, v209
v_mfma_f32_16x16x32_bf16 v[4:7], a[100:103], v[224:227], v[4:7]
v_exp_f32 v218, v218
v_mfma_f32_16x16x32_bf16 v[36:39], a[100:103], v[228:231], v[36:39]
ds_read_b128 a[72:75], v248 offset:2048
v_cvt_pk_bf16_f32 v233, v210, v211
v_mfma_f32_16x16x32_bf16 v[8:11], a[104:107], v[224:227], v[8:11]
v_exp_f32 v219, v219
v_mfma_f32_16x16x32_bf16 v[40:43], a[104:107], v[228:231], v[40:43]
ds_read_b128 a[76:79], v248 offset:3072
v_cvt_pk_bf16_f32 v236, v216, v217
v_mfma_f32_16x16x32_bf16 v[12:15], a[108:111], v[224:227], v[12:15]
v_exp_f32 v212, v212
v_mfma_f32_16x16x32_bf16 v[44:47], a[108:111], v[228:231], v[44:47]
ds_read_b128 a[80:83], v248 offset:4096
s_waitcnt lgkmcnt(5)
v_mfma_f32_16x16x32_bf16 v[16:19], a[112:115], v[224:227], v[16:19]
v_exp_f32 v213, v213
v_mfma_f32_16x16x32_bf16 v[48:51], a[112:115], v[228:231], v[48:51]
ds_read_b128 a[84:87], v248 offset:5120
v_cvt_pk_bf16_f32 v237, v218, v219
v_mfma_f32_16x16x32_bf16 v[20:23], a[116:119], v[224:227], v[20:23]
v_exp_f32 v214, v214
v_mfma_f32_16x16x32_bf16 v[52:55], a[116:119], v[228:231], v[52:55]
ds_read_b128 a[88:91], v248 offset:6144
v_cvt_pk_bf16_f32 v234, v212, v213
v_mfma_f32_16x16x32_bf16 v[24:27], a[120:123], v[224:227], v[24:27]
v_exp_f32 v215, v215
v_mfma_f32_16x16x32_bf16 v[56:59], a[120:123], v[228:231], v[56:59]
ds_read_b128 a[92:95], v248 offset:7168
v_cvt_pk_bf16_f32 v235, v214, v215
v_mfma_f32_16x16x32_bf16 v[28:31], a[124:127], v[224:227], v[28:31]
v_exp_f32 v220, v220
v_mfma_f32_16x16x32_bf16 v[60:63], a[124:127], v[228:231], v[60:63]
buffer_load_dword v250, v251, s[64:67], s20 offen sc1
s_waitcnt lgkmcnt(4)
v_mfma_f32_16x16x32_bf16 v[192:195], a[64:67], v[128:131], 0
v_exp_f32 v221, v221
v_mfma_f32_16x16x32_bf16 v[192:195], a[68:71], v[132:135], v[192:195]
v_cvt_pk_bf16_f32 v238, v220, v221
s_add_u32 s89, s89, 16384
v_mfma_f32_16x16x32_bf16 v[192:195], a[72:75], v[136:139], v[192:195]
v_exp_f32 v222, v222
v_mfma_f32_16x16x32_bf16 v[192:195], a[76:79], v[140:143], v[192:195]
buffer_load_dwordx4 v240, s[56:59], s17 offen lds
v_mfma_f32_16x16x32_bf16 v[200:203], a[64:67], v[144:147], 0
v_exp_f32 v223, v223
s_add_u32 s17, s17, 32768
v_mfma_f32_16x16x32_bf16 v[200:203], a[68:71], v[148:151], v[200:203]
v_cvt_pk_bf16_f32 v239, v222, v223
s_add_u32 m0, m0, 1024
v_mfma_f32_16x16x32_bf16 v[200:203], a[72:75], v[152:155], v[200:203]
v_exp_f32 v192, v192
v_mfma_f32_16x16x32_bf16 v[200:203], a[76:79], v[156:159], v[200:203]
s_waitcnt lgkmcnt(0)
s_and_b32 s89, s89, 0x1ffff
v_mfma_f32_16x16x32_bf16 v[196:199], a[80:83], v[128:131], 0
v_exp_f32 v193, v193
v_mfma_f32_16x16x32_bf16 v[196:199], a[84:87], v[132:135], v[196:199]
v_cvt_pk_bf16_f32 v224, v192, v193
v_mfma_f32_16x16x32_bf16 v[196:199], a[88:91], v[136:139], v[196:199]
v_exp_f32 v194, v194
v_mfma_f32_16x16x32_bf16 v[196:199], a[92:95], v[140:143], v[196:199]
buffer_load_dwordx4 v240, s[56:59], s17 offen lds
v_mfma_f32_16x16x32_bf16 v[204:207], a[80:83], v[144:147], 0
v_exp_f32 v195, v195
s_add_u32 s17, s17, 4294942720
v_mfma_f32_16x16x32_bf16 v[204:207], a[84:87], v[148:151], v[204:207]
v_cvt_pk_bf16_f32 v225, v194, v195
s_add_u32 m0, m0, 1152
v_mfma_f32_16x16x32_bf16 v[204:207], a[88:91], v[152:155], v[204:207]
v_exp_f32 v200, v200
v_mfma_f32_16x16x32_bf16 v[204:207], a[92:95], v[156:159], v[204:207]
buffer_load_dwordx4 v240, s[56:59], s17 offen lds
v_mfma_f32_16x16x32_bf16 v[208:211], a[64:67], v[160:163], 0
v_exp_f32 v201, v201
s_add_u32 s17, s17, 32768
v_mfma_f32_16x16x32_bf16 v[208:211], a[68:71], v[164:167], v[208:211]
v_cvt_pk_bf16_f32 v228, v200, v201
s_add_u32 m0, m0, 1024
v_mfma_f32_16x16x32_bf16 v[208:211], a[72:75], v[168:171], v[208:211]
v_exp_f32 v202, v202
v_mfma_f32_16x16x32_bf16 v[208:211], a[76:79], v[172:175], v[208:211]
buffer_load_dwordx4 v240, s[56:59], s17 offen lds
v_mfma_f32_16x16x32_bf16 v[216:219], a[64:67], v[176:179], 0
v_exp_f32 v203, v203
v_mfma_f32_16x16x32_bf16 v[216:219], a[68:71], v[180:183], v[216:219]
v_cvt_pk_bf16_f32 v229, v202, v203
s_mov_b32 s93, 107520
v_mfma_f32_16x16x32_bf16 v[216:219], a[72:75], v[184:187], v[216:219]
v_exp_f32 v196, v196
v_mfma_f32_16x16x32_bf16 v[216:219], a[76:79], v[188:191], v[216:219]
buffer_load_dwordx4 a[188:191], v254, s[40:43], s93 offen
v_mfma_f32_16x16x32_bf16 v[212:215], a[80:83], v[160:163], 0
v_exp_f32 v197, v197
s_bfe_u32 s17, s4, 262148
v_mfma_f32_16x16x32_bf16 v[212:215], a[84:87], v[164:167], v[212:215]
v_cvt_pk_bf16_f32 v226, v196, v197
s_xor_b32 s17, s17, s72
s_lshl_b32 s17, s17, 18
v_mfma_f32_16x16x32_bf16 v[212:215], a[88:91], v[168:171], v[212:215]
v_exp_f32 v198, v198
s_add_u32 m0, s83, 50688
v_mfma_f32_16x16x32_bf16 v[212:215], a[92:95], v[172:175], v[212:215]
v_mfma_f32_16x16x32_bf16 v[220:223], a[80:83], v[176:179], 0
v_exp_f32 v199, v199
v_mfma_f32_16x16x32_bf16 v[220:223], a[84:87], v[180:183], v[220:223]
v_cvt_pk_bf16_f32 v227, v198, v199
.La5_LW2_11:
s_cbranch_execz .La5_LW2_R1
s_waitcnt vmcnt(5)
v_cmp_eq_u32 s[92:93], v250, v249
s_mov_b32 s94, 16384
v_mfma_f32_16x16x32_bf16 v[220:223], a[88:91], v[184:187], v[220:223]
v_exp_f32 v204, v204
v_mfma_f32_16x16x32_bf16 v[220:223], a[92:95], v[188:191], v[220:223]
s_nop 0
s_barrier
v_mfma_f32_16x16x32_bf16 v[64:67], a[96:99], v[232:235], v[64:67]
v_exp_f32 v205, v205
v_mfma_f32_16x16x32_bf16 v[96:99], a[96:99], v[236:239], v[96:99]
ds_read_b128 a[96:99], v248 offset:8192
v_cvt_pk_bf16_f32 v230, v204, v205
s_and_b32 s92, s92, 3
s_andn2_b32 s92, s92, s23
s_cmp_eq_u32 s92, 3
s_cselect_b32 s70, 17408, 0
s_cselect_b32 s84, 3, s23
v_mfma_f32_16x16x32_bf16 v[68:71], a[100:103], v[232:235], v[68:71]
v_exp_f32 v206, v206
v_mfma_f32_16x16x32_bf16 v[100:103], a[100:103], v[236:239], v[100:103]
ds_read_b128 a[100:103], v248 offset:9216
buffer_load_dwordx4 a[220:223], v255, s[68:71], s94 offen sc1
v_mfma_f32_16x16x32_bf16 v[72:75], a[104:107], v[232:235], v[72:75]
v_exp_f32 v207, v207
v_mfma_f32_16x16x32_bf16 v[104:107], a[104:107], v[236:239], v[104:107]
ds_read_b128 a[104:107], v248 offset:10240
v_cvt_pk_bf16_f32 v231, v206, v207
v_mfma_f32_16x16x32_bf16 v[76:79], a[108:111], v[232:235], v[76:79]
v_exp_f32 v208, v208
v_mfma_f32_16x16x32_bf16 v[108:111], a[108:111], v[236:239], v[108:111]
ds_read_b128 a[108:111], v248 offset:11264
buffer_load_dwordx4 a[0:3], v255, s[68:71], 0 offen offset:0 sc1
v_mfma_f32_16x16x32_bf16 v[80:83], a[112:115], v[232:235], v[80:83]
v_exp_f32 v209, v209
v_mfma_f32_16x16x32_bf16 v[112:115], a[112:115], v[236:239], v[112:115]
ds_read_b128 a[112:115], v248 offset:12288
v_mfma_f32_16x16x32_bf16 v[84:87], a[116:119], v[232:235], v[84:87]
v_exp_f32 v210, v210
v_mfma_f32_16x16x32_bf16 v[116:119], a[116:119], v[236:239], v[116:119]
ds_read_b128 a[116:119], v248 offset:13312
buffer_load_dwordx4 a[4:7], v255, s[68:71], 0 offen offset:1024 sc1
v_mfma_f32_16x16x32_bf16 v[88:91], a[120:123], v[232:235], v[88:91]
v_exp_f32 v211, v211
v_mfma_f32_16x16x32_bf16 v[120:123], a[120:123], v[236:239], v[120:123]
ds_read_b128 a[120:123], v248 offset:14336
v_mfma_f32_16x16x32_bf16 v[92:95], a[124:127], v[232:235], v[92:95]
v_exp_f32 v216, v216
v_mfma_f32_16x16x32_bf16 v[124:127], a[124:127], v[236:239], v[124:127]
ds_read_b128 a[124:127], v248 offset:15360
v_or_b32 v248, s89, v255
buffer_load_dword v250, v251, s[64:67], s20 offen sc1
v_smfmac_f32_16x16x64_bf16 a[196:199], a[208:211], v[232:239], v241
s_waitcnt lgkmcnt(4)
v_smfmac_f32_16x16x64_bf16 a[192:195], a[208:211], v[224:231], v241
ds_read_b128 a[64:67], v248 offset:0
v_mfma_f32_16x16x32_bf16 v[0:3], a[96:99], v[224:227], v[0:3]
v_exp_f32 v217, v217
v_mfma_f32_16x16x32_bf16 v[32:35], a[96:99], v[228:231], v[32:35]
ds_read_b128 a[68:71], v248 offset:1024
v_cvt_pk_bf16_f32 v232, v208, v209
buffer_load_dwordx4 a[8:11], v255, s[68:71], 0 offen offset:2048 sc1
v_mfma_f32_16x16x32_bf16 v[4:7], a[100:103], v[224:227], v[4:7]
v_exp_f32 v218, v218
v_mfma_f32_16x16x32_bf16 v[36:39], a[100:103], v[228:231], v[36:39]
ds_read_b128 a[72:75], v248 offset:2048
v_cvt_pk_bf16_f32 v233, v210, v211
v_mfma_f32_16x16x32_bf16 v[8:11], a[104:107], v[224:227], v[8:11]
v_exp_f32 v219, v219
v_mfma_f32_16x16x32_bf16 v[40:43], a[104:107], v[228:231], v[40:43]
ds_read_b128 a[76:79], v248 offset:3072
v_cvt_pk_bf16_f32 v236, v216, v217
buffer_load_dwordx4 a[12:15], v255, s[68:71], 0 offen offset:3072 sc1
v_mfma_f32_16x16x32_bf16 v[12:15], a[108:111], v[224:227], v[12:15]
v_exp_f32 v212, v212
v_mfma_f32_16x16x32_bf16 v[44:47], a[108:111], v[228:231], v[44:47]
ds_read_b128 a[80:83], v248 offset:4096
s_waitcnt lgkmcnt(5)
v_mfma_f32_16x16x32_bf16 v[16:19], a[112:115], v[224:227], v[16:19]
v_exp_f32 v213, v213
v_mfma_f32_16x16x32_bf16 v[48:51], a[112:115], v[228:231], v[48:51]
ds_read_b128 a[84:87], v248 offset:5120
v_cvt_pk_bf16_f32 v237, v218, v219
buffer_load_dwordx4 a[16:19], v255, s[68:71], s95 offen offset:0 sc1
v_mfma_f32_16x16x32_bf16 v[20:23], a[116:119], v[224:227], v[20:23]
v_exp_f32 v214, v214
v_mfma_f32_16x16x32_bf16 v[52:55], a[116:119], v[228:231], v[52:55]
ds_read_b128 a[88:91], v248 offset:6144
v_cvt_pk_bf16_f32 v234, v212, v213
v_mfma_f32_16x16x32_bf16 v[24:27], a[120:123], v[224:227], v[24:27]
v_exp_f32 v215, v215
v_mfma_f32_16x16x32_bf16 v[56:59], a[120:123], v[228:231], v[56:59]
ds_read_b128 a[92:95], v248 offset:7168
v_cvt_pk_bf16_f32 v235, v214, v215
buffer_load_dwordx4 a[20:23], v255, s[68:71], s95 offen offset:1024 sc1
v_mfma_f32_16x16x32_bf16 v[28:31], a[124:127], v[224:227], v[28:31]
v_exp_f32 v220, v220
v_mfma_f32_16x16x32_bf16 v[60:63], a[124:127], v[228:231], v[60:63]
buffer_load_dwordx4 a[24:27], v255, s[68:71], s95 offen offset:2048 sc1
s_waitcnt lgkmcnt(4)
v_mfma_f32_16x16x32_bf16 v[192:195], a[64:67], v[128:131], 0
v_exp_f32 v221, v221
v_mfma_f32_16x16x32_bf16 v[192:195], a[68:71], v[132:135], v[192:195]
v_cvt_pk_bf16_f32 v238, v220, v221
v_mfma_f32_16x16x32_bf16 v[192:195], a[72:75], v[136:139], v[192:195]
v_exp_f32 v222, v222
v_mfma_f32_16x16x32_bf16 v[192:195], a[76:79], v[140:143], v[192:195]
buffer_load_dwordx4 v240, s[56:59], s17 offen lds
v_mfma_f32_16x16x32_bf16 v[200:203], a[64:67], v[144:147], 0
v_exp_f32 v223, v223
s_add_u32 s17, s17, 32768
v_mfma_f32_16x16x32_bf16 v[200:203], a[68:71], v[148:151], v[200:203]
v_cvt_pk_bf16_f32 v239, v222, v223
s_add_u32 m0, m0, 1024
v_mfma_f32_16x16x32_bf16 v[200:203], a[72:75], v[152:155], v[200:203]
v_exp_f32 v192, v192
v_mfma_f32_16x16x32_bf16 v[200:203], a[76:79], v[156:159], v[200:203]
s_waitcnt lgkmcnt(0)
v_mfma_f32_16x16x32_bf16 v[196:199], a[80:83], v[128:131], 0
v_exp_f32 v193, v193
v_mfma_f32_16x16x32_bf16 v[196:199], a[84:87], v[132:135], v[196:199]
v_cvt_pk_bf16_f32 v224, v192, v193
s_waitcnt vmcnt(6)
v_cmp_eq_u32 s[92:93], v250, v249
v_mfma_f32_16x16x32_bf16 v[196:199], a[88:91], v[136:139], v[196:199]
v_exp_f32 v194, v194
v_mfma_f32_16x16x32_bf16 v[196:199], a[92:95], v[140:143], v[196:199]
buffer_load_dwordx4 v240, s[56:59], s17 offen lds
v_mfma_f32_16x16x32_bf16 v[204:207], a[80:83], v[144:147], 0
v_exp_f32 v195, v195
s_add_u32 s17, s17, 4294942720
v_mfma_f32_16x16x32_bf16 v[204:207], a[84:87], v[148:151], v[204:207]
v_cvt_pk_bf16_f32 v225, v194, v195
s_add_u32 m0, m0, 1152
s_and_b32 s92, s92, 3
s_sub_u32 s92, s92, s84
s_cmp_eq_u32 s92, 3
s_cbranch_scc1 .La5_LA_PS0
.La5_LA_PS1:
v_mfma_f32_16x16x32_bf16 v[204:207], a[88:91], v[152:155], v[204:207]
v_exp_f32 v200, v200
v_mfma_f32_16x16x32_bf16 v[204:207], a[92:95], v[156:159], v[204:207]
buffer_load_dwordx4 v240, s[56:59], s17 offen lds
v_mfma_f32_16x16x32_bf16 v[208:211], a[64:67], v[160:163], 0
v_exp_f32 v201, v201
s_add_u32 s17, s17, 32768
v_mfma_f32_16x16x32_bf16 v[208:211], a[68:71], v[164:167], v[208:211]
v_cvt_pk_bf16_f32 v228, v200, v201
s_add_u32 m0, m0, 1024
buffer_load_dwordx4 a[28:31], v255, s[68:71], s95 offen offset:3072 sc1
v_mfma_f32_16x16x32_bf16 v[208:211], a[72:75], v[168:171], v[208:211]
v_exp_f32 v202, v202
v_mfma_f32_16x16x32_bf16 v[208:211], a[76:79], v[172:175], v[208:211]
buffer_load_dwordx4 v240, s[56:59], s17 offen lds
v_mfma_f32_16x16x32_bf16 v[216:219], a[64:67], v[176:179], 0
v_exp_f32 v203, v203
v_mfma_f32_16x16x32_bf16 v[216:219], a[68:71], v[180:183], v[216:219]
v_cvt_pk_bf16_f32 v229, v202, v203
buffer_load_dwordx4 a[32:35], v255, s[68:71], s96 offen offset:0 sc1
v_mfma_f32_16x16x32_bf16 v[216:219], a[72:75], v[184:187], v[216:219]
v_exp_f32 v196, v196
v_mfma_f32_16x16x32_bf16 v[216:219], a[76:79], v[188:191], v[216:219]
v_mfma_f32_16x16x32_bf16 v[212:215], a[80:83], v[160:163], 0
v_exp_f32 v197, v197
v_mfma_f32_16x16x32_bf16 v[212:215], a[84:87], v[164:167], v[212:215]
v_cvt_pk_bf16_f32 v226, v196, v197
buffer_load_dword v250, v251, s[64:67], s20 offen sc1
v_mfma_f32_16x16x32_bf16 v[212:215], a[88:91], v[168:171], v[212:215]
v_exp_f32 v198, v198
v_mfma_f32_16x16x32_bf16 v[212:215], a[92:95], v[172:175], v[212:215]
buffer_load_dwordx4 a[36:39], v255, s[68:71], s96 offen offset:1024 sc1
v_mfma_f32_16x16x32_bf16 v[220:223], a[80:83], v[176:179], 0
v_exp_f32 v199, v199
v_mfma_f32_16x16x32_bf16 v[220:223], a[84:87], v[180:183], v[220:223]
v_cvt_pk_bf16_f32 v227, v198, v199
v_mfma_f32_16x16x32_bf16 v[220:223], a[88:91], v[184:187], v[220:223]
v_exp_f32 v204, v204
v_mfma_f32_16x16x32_bf16 v[220:223], a[92:95], v[188:191], v[220:223]
buffer_load_dwordx4 a[40:43], v255, s[68:71], s96 offen offset:2048 sc1
v_mfma_f32_16x16x32_bf16 v[64:67], a[96:99], v[232:235], v[64:67]
v_exp_f32 v205, v205
v_mfma_f32_16x16x32_bf16 v[96:99], a[96:99], v[236:239], v[96:99]
ds_read_b128 a[96:99], v248 offset:8192
v_cvt_pk_bf16_f32 v230, v204, v205
v_mfma_f32_16x16x32_bf16 v[68:71], a[100:103], v[232:235], v[68:71]
v_exp_f32 v206, v206
v_mfma_f32_16x16x32_bf16 v[100:103], a[100:103], v[236:239], v[100:103]
ds_read_b128 a[100:103], v248 offset:9216
buffer_load_dwordx4 a[44:47], v255, s[68:71], s96 offen offset:3072 sc1
v_mfma_f32_16x16x32_bf16 v[72:75], a[104:107], v[232:235], v[72:75]
v_exp_f32 v207, v207
v_mfma_f32_16x16x32_bf16 v[104:107], a[104:107], v[236:239], v[104:107]
ds_read_b128 a[104:107], v248 offset:10240
v_cvt_pk_bf16_f32 v231, v206, v207
v_mfma_f32_16x16x32_bf16 v[76:79], a[108:111], v[232:235], v[76:79]
v_exp_f32 v208, v208
v_mfma_f32_16x16x32_bf16 v[108:111], a[108:111], v[236:239], v[108:111]
ds_read_b128 a[108:111], v248 offset:11264
buffer_load_dwordx4 a[48:51], v255, s[68:71], s97 offen offset:0 sc1
v_mfma_f32_16x16x32_bf16 v[80:83], a[112:115], v[232:235], v[80:83]
v_exp_f32 v209, v209
v_mfma_f32_16x16x32_bf16 v[112:115], a[112:115], v[236:239], v[112:115]
ds_read_b128 a[112:115], v248 offset:12288
v_mfma_f32_16x16x32_bf16 v[84:87], a[116:119], v[232:235], v[84:87]
v_exp_f32 v210, v210
v_mfma_f32_16x16x32_bf16 v[116:119], a[116:119], v[236:239], v[116:119]
ds_read_b128 a[116:119], v248 offset:13312
buffer_load_dwordx4 a[52:55], v255, s[68:71], s97 offen offset:1024 sc1
v_mfma_f32_16x16x32_bf16 v[88:91], a[120:123], v[232:235], v[88:91]
v_exp_f32 v211, v211
v_mfma_f32_16x16x32_bf16 v[120:123], a[120:123], v[236:239], v[120:123]
ds_read_b128 a[120:123], v248 offset:14336
v_mfma_f32_16x16x32_bf16 v[92:95], a[124:127], v[232:235], v[92:95]
v_exp_f32 v216, v216
v_mfma_f32_16x16x32_bf16 v[124:127], a[124:127], v[236:239], v[124:127]
ds_read_b128 a[124:127], v248 offset:15360
buffer_load_dwordx4 a[56:59], v255, s[68:71], s97 offen offset:2048 sc1
v_smfmac_f32_16x16x64_bf16 a[196:199], a[208:211], v[232:239], v241
s_waitcnt lgkmcnt(4)
v_smfmac_f32_16x16x64_bf16 a[192:195], a[208:211], v[224:231], v241
buffer_load_dwordx4 a[60:63], v255, s[68:71], s97 offen offset:3072 sc1
v_mfma_f32_16x16x32_bf16 v[0:3], a[96:99], v[224:227], v[0:3]
v_exp_f32 v217, v217
v_mfma_f32_16x16x32_bf16 v[32:35], a[96:99], v[228:231], v[32:35]
v_cvt_pk_bf16_f32 v232, v208, v209
v_mfma_f32_16x16x32_bf16 v[4:7], a[100:103], v[224:227], v[4:7]
v_exp_f32 v218, v218
v_mfma_f32_16x16x32_bf16 v[36:39], a[100:103], v[228:231], v[36:39]
v_cvt_pk_bf16_f32 v233, v210, v211
v_mfma_f32_16x16x32_bf16 v[8:11], a[104:107], v[224:227], v[8:11]
v_exp_f32 v219, v219
v_mfma_f32_16x16x32_bf16 v[40:43], a[104:107], v[228:231], v[40:43]
v_cvt_pk_bf16_f32 v236, v216, v217
v_mfma_f32_16x16x32_bf16 v[12:15], a[108:111], v[224:227], v[12:15]
v_exp_f32 v212, v212
s_waitcnt vmcnt(7)
v_cmp_eq_u32 s[92:93], v250, v249
v_mfma_f32_16x16x32_bf16 v[44:47], a[108:111], v[228:231], v[44:47]
s_waitcnt lgkmcnt(0)
v_mfma_f32_16x16x32_bf16 v[16:19], a[112:115], v[224:227], v[16:19]
v_exp_f32 v213, v213
v_mfma_f32_16x16x32_bf16 v[48:51], a[112:115], v[228:231], v[48:51]
v_cvt_pk_bf16_f32 v237, v218, v219
s_and_b32 s92, s92, 3
s_sub_u32 s92, s92, s84
v_mfma_f32_16x16x32_bf16 v[20:23], a[116:119], v[224:227], v[20:23]
v_exp_f32 v214, v214
v_mfma_f32_16x16x32_bf16 v[52:55], a[116:119], v[228:231], v[52:55]
v_cvt_pk_bf16_f32 v234, v212, v213
v_mfma_f32_16x16x32_bf16 v[24:27], a[120:123], v[224:227], v[24:27]
v_exp_f32 v215, v215
v_mfma_f32_16x16x32_bf16 v[56:59], a[120:123], v[228:231], v[56:59]
v_cvt_pk_bf16_f32 v235, v214, v215
v_mfma_f32_16x16x32_bf16 v[28:31], a[124:127], v[224:227], v[28:31]
v_exp_f32 v220, v220
v_mfma_f32_16x16x32_bf16 v[60:63], a[124:127], v[228:231], v[60:63]
s_cmp_eq_u32 s92, 3
s_cbranch_scc1 .La5_LA_PS2
.La5_LA_PS3:
s_cmp_lg_u32 s84, 3
s_cbranch_scc1 .La5_LA_PSPIN
.La5_LA_DC1:
s_waitcnt vmcnt(8)
v_mfma_f32_16x16x32_bf16 v[0:3], a[214:217], a[0:3], v[0:3]
v_accvgpr_read_b32 v250, a192
v_accvgpr_read_b32 v251, a193
v_add_f32 v250, v250, v251
v_accvgpr_read_b32 v251, a220
v_add_f32 v250, v250, v251
v_accvgpr_read_b32 v249, a194
v_mfma_f32_16x16x32_bf16 v[4:7], a[212:215], a[0:3], v[4:7]
v_accvgpr_read_b32 v251, a195
v_add_f32 v249, v249, v251
v_accvgpr_read_b32 v251, a221
v_add_f32 v249, v249, v251
v_subrev_u32 v251, 562036736, v250
v_mfma_f32_16x16x32_bf16 v[8:11], a[214:217], a[4:7], v[8:11]
v_rcp_f32 v250, v250
v_cmp_le_u32 vcc, 1006632960, v251
v_subrev_u32 v251, 562036736, v249
s_mov_b64 s[92:93], vcc
v_cmp_le_u32 vcc, 1006632960, v251
v_mfma_f32_16x16x32_bf16 v[12:15], a[212:215], a[4:7], v[12:15]
v_rcp_f32 v249, v249
v_mfma_f32_16x16x32_bf16 v[16:19], a[214:217], a[8:11], v[16:19]
s_or_b64 vcc, vcc, s[92:93]
s_cbranch_scc1 .La5_LA_SC3
.La5_LA_SR3:
v_exp_f32 v221, v221
v_exp_f32 v222, v222
v_mfma_f32_16x16x32_bf16 v[20:23], a[212:215], a[8:11], v[20:23]
v_exp_f32 v223, v223
v_cvt_pk_bf16_f32 v238, v220, v221
v_mul_f32 v0, v0, v250
v_mul_f32 v1, v1, v250
v_mul_f32 v2, v2, v250
v_mfma_f32_16x16x32_bf16 v[24:27], a[214:217], a[12:15], v[24:27]
v_mul_f32 v3, v3, v250
v_mul_f32 v4, v4, v250
v_mul_f32 v5, v5, v250
v_mul_f32 v6, v6, v250
v_mul_f32 v7, v7, v250
v_cvt_pk_bf16_f32 v128, v0, v1
v_mfma_f32_16x16x32_bf16 v[28:31], a[212:215], a[12:15], v[28:31]
v_cvt_pk_bf16_f32 v129, v2, v3
v_cvt_pk_bf16_f32 v130, v4, v5
v_cvt_pk_bf16_f32 v131, v6, v7
buffer_store_dwordx4 v[128:131], v244, s[44:47], s91 offen offset:0 sc1
v_cvt_pk_bf16_f32 v239, v222, v223
v_mfma_f32_16x16x32_bf16 v[32:35], a[214:217], a[16:19], v[32:35]
v_mul_f32 v8, v8, v250
v_mul_f32 v9, v9, v250
v_mul_f32 v10, v10, v250
v_mul_f32 v11, v11, v250
v_mul_f32 v12, v12, v250
v_mul_f32 v13, v13, v250
v_mfma_f32_16x16x32_bf16 v[36:39], a[212:215], a[16:19], v[36:39]
v_mul_f32 v14, v14, v250
v_mul_f32 v15, v15, v250
v_cvt_pk_bf16_f32 v132, v8, v9
v_cvt_pk_bf16_f32 v133, v10, v11
v_cvt_pk_bf16_f32 v134, v12, v13
v_cvt_pk_bf16_f32 v135, v14, v15
v_mfma_f32_16x16x32_bf16 v[40:43], a[214:217], a[20:23], v[40:43]
buffer_store_dwordx4 v[132:135], v244, s[44:47], s91 offen offset:1024 sc1
v_mul_f32 v16, v16, v250
v_mul_f32 v17, v17, v250
v_mul_f32 v18, v18, v250
v_mul_f32 v19, v19, v250
v_mfma_f32_16x16x32_bf16 v[44:47], a[212:215], a[20:23], v[44:47]
v_mul_f32 v20, v20, v250
v_mul_f32 v21, v21, v250
v_mul_f32 v22, v22, v250
v_mul_f32 v23, v23, v250
v_cvt_pk_bf16_f32 v136, v16, v17
v_cvt_pk_bf16_f32 v137, v18, v19
v_mfma_f32_16x16x32_bf16 v[48:51], a[214:217], a[24:27], v[48:51]
v_cvt_pk_bf16_f32 v138, v20, v21
v_cvt_pk_bf16_f32 v139, v22, v23
buffer_store_dwordx4 v[136:139], v244, s[44:47], s91 offen offset:2048 sc1
v_mul_f32 v24, v24, v250
v_mul_f32 v25, v25, v250
v_mfma_f32_16x16x32_bf16 v[52:55], a[212:215], a[24:27], v[52:55]
v_mul_f32 v26, v26, v250
v_mul_f32 v27, v27, v250
v_mul_f32 v28, v28, v250
v_mul_f32 v29, v29, v250
v_mul_f32 v30, v30, v250
v_mul_f32 v31, v31, v250
v_mfma_f32_16x16x32_bf16 v[56:59], a[214:217], a[28:31], v[56:59]
v_cvt_pk_bf16_f32 v140, v24, v25
v_cvt_pk_bf16_f32 v141, v26, v27
v_cvt_pk_bf16_f32 v142, v28, v29
v_cvt_pk_bf16_f32 v143, v30, v31
buffer_store_dwordx4 v[140:143], v244, s[44:47], s91 offen offset:3072 sc1
v_mfma_f32_16x16x32_bf16 v[60:63], a[212:215], a[28:31], v[60:63]
v_mul_f32 v32, v32, v249
v_mul_f32 v33, v33, v249
v_mul_f32 v34, v34, v249
v_mul_f32 v35, v35, v249
v_mul_f32 v36, v36, v249
v_mul_f32 v37, v37, v249
v_smfmac_f32_16x16x64_bf16 a[196:199], a[208:211], v[232:239], v241
v_mul_f32 v38, v38, v249
v_mul_f32 v39, v39, v249
v_cvt_pk_bf16_f32 v144, v32, v33
v_cvt_pk_bf16_f32 v145, v34, v35
v_cvt_pk_bf16_f32 v146, v36, v37
v_mfma_f32_16x16x32_bf16 v[64:67], a[96:99], v[232:235], v[64:67]
v_cvt_pk_bf16_f32 v147, v38, v39
buffer_store_dwordx4 v[144:147], v245, s[44:47], s91 offen offset:0 sc1
v_mul_f32 v40, v40, v249
v_mul_f32 v41, v41, v249
v_mul_f32 v42, v42, v249
v_mfma_f32_16x16x32_bf16 v[96:99], a[96:99], v[236:239], v[96:99]
v_mul_f32 v43, v43, v249
v_mul_f32 v44, v44, v249
v_mul_f32 v45, v45, v249
v_mul_f32 v46, v46, v249
v_mul_f32 v47, v47, v249
v_cvt_pk_bf16_f32 v148, v40, v41
v_mfma_f32_16x16x32_bf16 v[68:71], a[100:103], v[232:235], v[68:71]
v_cvt_pk_bf16_f32 v149, v42, v43
v_cvt_pk_bf16_f32 v150, v44, v45
v_cvt_pk_bf16_f32 v151, v46, v47
buffer_store_dwordx4 v[148:151], v245, s[44:47], s91 offen offset:1024 sc1
v_mul_f32 v48, v48, v249
v_mfma_f32_16x16x32_bf16 v[100:103], a[100:103], v[236:239], v[100:103]
v_mul_f32 v49, v49, v249
v_mul_f32 v50, v50, v249
v_mul_f32 v51, v51, v249
v_mul_f32 v52, v52, v249
v_mul_f32 v53, v53, v249
v_mul_f32 v54, v54, v249
v_mfma_f32_16x16x32_bf16 v[72:75], a[104:107], v[232:235], v[72:75]
v_mul_f32 v55, v55, v249
v_cvt_pk_bf16_f32 v152, v48, v49
v_cvt_pk_bf16_f32 v153, v50, v51
v_cvt_pk_bf16_f32 v154, v52, v53
v_cvt_pk_bf16_f32 v155, v54, v55
buffer_store_dwordx4 v[152:155], v245, s[44:47], s91 offen offset:2048 sc1
v_mfma_f32_16x16x32_bf16 v[104:107], a[104:107], v[236:239], v[104:107]
v_mul_f32 v56, v56, v249
v_mul_f32 v57, v57, v249
v_mul_f32 v58, v58, v249
v_mul_f32 v59, v59, v249
v_mul_f32 v60, v60, v249
v_mul_f32 v61, v61, v249
v_mfma_f32_16x16x32_bf16 v[76:79], a[108:111], v[232:235], v[76:79]
v_mul_f32 v62, v62, v249
v_mul_f32 v63, v63, v249
v_cvt_pk_bf16_f32 v156, v56, v57
v_cvt_pk_bf16_f32 v157, v58, v59
v_cvt_pk_bf16_f32 v158, v60, v61
v_cvt_pk_bf16_f32 v159, v62, v63
v_mfma_f32_16x16x32_bf16 v[108:111], a[108:111], v[236:239], v[108:111]
buffer_store_dwordx4 v[156:159], v245, s[44:47], s91 offen offset:3072 sc1
v_accvgpr_read_b32 v240, a224
v_xor_b32 v241, 32, v240
v_xor_b32 v242, 64, v240
v_xor_b32 v243, 96, v240
v_mfma_f32_16x16x32_bf16 v[80:83], a[112:115], v[232:235], v[80:83]
v_mfma_f32_16x16x32_bf16 v[112:115], a[112:115], v[236:239], v[112:115]
v_mfma_f32_16x16x32_bf16 v[84:87], a[116:119], v[232:235], v[84:87]
v_mfma_f32_16x16x32_bf16 v[116:119], a[116:119], v[236:239], v[116:119]
v_mfma_f32_16x16x32_bf16 v[88:91], a[120:123], v[232:235], v[88:91]
v_mfma_f32_16x16x32_bf16 v[120:123], a[120:123], v[236:239], v[120:123]
v_mfma_f32_16x16x32_bf16 v[92:95], a[124:127], v[232:235], v[92:95]
v_mfma_f32_16x16x32_bf16 v[124:127], a[124:127], v[236:239], v[124:127]
s_nop 0
s_barrier
s_waitcnt vmcnt(8)
v_mfma_f32_16x16x32_bf16 v[64:67], a[214:217], a[32:35], v[64:67]
ds_read_b64_tr_b16 v[192:193], v240 offset:16896
ds_read_b64_tr_b16 v[194:195], v240 offset:17920
v_mfma_f32_16x16x32_bf16 v[68:71], a[212:215], a[32:35], v[68:71]
ds_read_b64_tr_b16 v[196:197], v241 offset:16896
ds_read_b64_tr_b16 v[198:199], v241 offset:17920
v_mfma_f32_16x16x32_bf16 v[72:75], a[214:217], a[36:39], v[72:75]
ds_read_b64_tr_b16 v[200:201], v242 offset:16896
ds_read_b64_tr_b16 v[202:203], v242 offset:17920
v_accvgpr_read_b32 v250, a196
v_accvgpr_read_b32 v251, a197
v_add_f32 v250, v250, v251
v_accvgpr_read_b32 v251, a222
v_mfma_f32_16x16x32_bf16 v[76:79], a[212:215], a[36:39], v[76:79]
ds_read_b64_tr_b16 v[204:205], v243 offset:16896
ds_read_b64_tr_b16 v[206:207], v243 offset:17920
v_add_f32 v250, v250, v251
v_accvgpr_read_b32 v249, a198
v_accvgpr_read_b32 v251, a199
v_add_f32 v249, v249, v251
v_mfma_f32_16x16x32_bf16 v[80:83], a[214:217], a[40:43], v[80:83]
ds_read_b64_tr_b16 v[208:209], v240 offset:17024
ds_read_b64_tr_b16 v[210:211], v240 offset:18048
v_accvgpr_read_b32 v251, a223
v_add_f32 v249, v249, v251
v_subrev_u32 v251, 562036736, v250
v_mfma_f32_16x16x32_bf16 v[84:87], a[212:215], a[40:43], v[84:87]
ds_read_b64_tr_b16 v[212:213], v241 offset:17024
ds_read_b64_tr_b16 v[214:215], v241 offset:18048
v_rcp_f32 v250, v250
v_cmp_le_u32 vcc, 1006632960, v251
v_subrev_u32 v251, 562036736, v249
v_mfma_f32_16x16x32_bf16 v[88:91], a[214:217], a[44:47], v[88:91]
ds_read_b64_tr_b16 v[216:217], v242 offset:17024
ds_read_b64_tr_b16 v[218:219], v242 offset:18048
s_mov_b64 s[92:93], vcc
v_cmp_le_u32 vcc, 1006632960, v251
v_rcp_f32 v249, v249
v_mfma_f32_16x16x32_bf16 v[92:95], a[212:215], a[44:47], v[92:95]
ds_read_b64_tr_b16 v[220:221], v243 offset:17024
ds_read_b64_tr_b16 v[222:223], v243 offset:18048
s_or_b64 vcc, vcc, s[92:93]
s_cbranch_scc1 .La5_LA_SC4
.La5_LA_SR4:
v_mul_f32 v64, v64, v250
v_mul_f32 v65, v65, v250
v_mfma_f32_16x16x32_bf16 v[96:99], a[214:217], a[48:51], v[96:99]
v_mul_f32 v66, v66, v250
v_mul_f32 v67, v67, v250
v_mul_f32 v68, v68, v250
v_mul_f32 v69, v69, v250
v_mul_f32 v70, v70, v250
v_mul_f32 v71, v71, v250
v_mfma_f32_16x16x32_bf16 v[100:103], a[212:215], a[48:51], v[100:103]
v_cvt_pk_bf16_f32 v160, v64, v65
v_cvt_pk_bf16_f32 v161, v66, v67
v_cvt_pk_bf16_f32 v162, v68, v69
v_cvt_pk_bf16_f32 v163, v70, v71
buffer_store_dwordx4 v[160:163], v246, s[44:47], s91 offen offset:0 sc1
v_mfma_f32_16x16x32_bf16 v[104:107], a[214:217], a[52:55], v[104:107]
v_mul_f32 v72, v72, v250
v_mul_f32 v73, v73, v250
v_mul_f32 v74, v74, v250
v_mul_f32 v75, v75, v250
v_mul_f32 v76, v76, v250
v_mul_f32 v77, v77, v250
v_mfma_f32_16x16x32_bf16 v[108:111], a[212:215], a[52:55], v[108:111]
v_mul_f32 v78, v78, v250
v_mul_f32 v79, v79, v250
v_cvt_pk_bf16_f32 v164, v72, v73
v_cvt_pk_bf16_f32 v165, v74, v75
v_cvt_pk_bf16_f32 v166, v76, v77
v_cvt_pk_bf16_f32 v167, v78, v79
v_mfma_f32_16x16x32_bf16 v[112:115], a[214:217], a[56:59], v[112:115]
buffer_store_dwordx4 v[164:167], v246, s[44:47], s91 offen offset:1024 sc1
v_mul_f32 v80, v80, v250
v_mul_f32 v81, v81, v250
v_mul_f32 v82, v82, v250
v_mul_f32 v83, v83, v250
v_mfma_f32_16x16x32_bf16 v[116:119], a[212:215], a[56:59], v[116:119]
v_mul_f32 v84, v84, v250
v_mul_f32 v85, v85, v250
v_mul_f32 v86, v86, v250
v_mul_f32 v87, v87, v250
v_cvt_pk_bf16_f32 v168, v80, v81
v_cvt_pk_bf16_f32 v169, v82, v83
v_mfma_f32_16x16x32_bf16 v[120:123], a[214:217], a[60:63], v[120:123]
v_cvt_pk_bf16_f32 v170, v84, v85
v_cvt_pk_bf16_f32 v171, v86, v87
buffer_store_dwordx4 v[168:171], v246, s[44:47], s91 offen offset:2048 sc1
v_mul_f32 v88, v88, v250
v_mul_f32 v89, v89, v250
v_mfma_f32_16x16x32_bf16 v[124:127], a[212:215], a[60:63], v[124:127]
s_waitcnt lgkmcnt(14)
v_mul_f32 v90, v90, v250
v_mul_f32 v91, v91, v250
v_mul_f32 v92, v92, v250
v_mul_f32 v93, v93, v250
v_mul_f32 v94, v94, v250
v_mfma_f32_16x16x32_bf16 v[0:3], v[192:195], v[128:131], 0
s_waitcnt lgkmcnt(12)
v_mul_f32 v95, v95, v250
v_cvt_pk_bf16_f32 v172, v88, v89
v_cvt_pk_bf16_f32 v173, v90, v91
v_cvt_pk_bf16_f32 v174, v92, v93
v_cvt_pk_bf16_f32 v175, v94, v95
v_mfma_f32_16x16x32_bf16 v[32:35], v[192:195], v[144:147], 0
ds_read_b64_tr_b16 v[192:193], v240 offset:25344
ds_read_b64_tr_b16 v[194:195], v240 offset:26368
buffer_store_dwordx4 v[172:175], v246, s[44:47], s91 offen offset:3072 sc1
v_mul_f32 v96, v96, v249
v_mul_f32 v97, v97, v249
v_mfma_f32_16x16x32_bf16 v[4:7], v[196:199], v[128:131], 0
s_waitcnt lgkmcnt(12)
v_mul_f32 v98, v98, v249
v_mul_f32 v99, v99, v249
v_mul_f32 v100, v100, v249
v_mul_f32 v101, v101, v249
v_mul_f32 v102, v102, v249
v_mfma_f32_16x16x32_bf16 v[36:39], v[196:199], v[144:147], 0
ds_read_b64_tr_b16 v[196:197], v241 offset:25344
ds_read_b64_tr_b16 v[198:199], v241 offset:26368
v_mul_f32 v103, v103, v249
v_cvt_pk_bf16_f32 v176, v96, v97
v_cvt_pk_bf16_f32 v177, v98, v99
v_cvt_pk_bf16_f32 v178, v100, v101
v_mfma_f32_16x16x32_bf16 v[8:11], v[200:203], v[128:131], 0
s_waitcnt lgkmcnt(12)
v_cvt_pk_bf16_f32 v179, v102, v103
buffer_store_dwordx4 v[176:179], v247, s[44:47], s91 offen offset:0 sc1
v_mul_f32 v104, v104, v249
v_mul_f32 v105, v105, v249
v_mfma_f32_16x16x32_bf16 v[40:43], v[200:203], v[144:147], 0
ds_read_b64_tr_b16 v[200:201], v242 offset:25344
ds_read_b64_tr_b16 v[202:203], v242 offset:26368
v_mul_f32 v106, v106, v249
v_mul_f32 v107, v107, v249
v_mul_f32 v108, v108, v249
v_mul_f32 v109, v109, v249
v_mfma_f32_16x16x32_bf16 v[12:15], v[204:207], v[128:131], 0
s_waitcnt lgkmcnt(12)
v_mul_f32 v110, v110, v249
v_mul_f32 v111, v111, v249
v_cvt_pk_bf16_f32 v180, v104, v105
v_cvt_pk_bf16_f32 v181, v106, v107
v_cvt_pk_bf16_f32 v182, v108, v109
v_mfma_f32_16x16x32_bf16 v[44:47], v[204:207], v[144:147], 0
ds_read_b64_tr_b16 v[204:205], v243 offset:25344
ds_read_b64_tr_b16 v[206:207], v243 offset:26368
v_cvt_pk_bf16_f32 v183, v110, v111
buffer_store_dwordx4 v[180:183], v247, s[44:47], s91 offen offset:1024 sc1
v_mul_f32 v112, v112, v249
v_mfma_f32_16x16x32_bf16 v[16:19], v[208:211], v[128:131], 0
s_waitcnt lgkmcnt(8)
v_mul_f32 v113, v113, v249
v_mul_f32 v114, v114, v249
v_mul_f32 v115, v115, v249
v_mul_f32 v116, v116, v249
v_mul_f32 v117, v117, v249
v_mfma_f32_16x16x32_bf16 v[48:51], v[208:211], v[144:147], 0
ds_read_b64_tr_b16 v[208:209], v240 offset:25472
ds_read_b64_tr_b16 v[210:211], v240 offset:26496
v_mul_f32 v118, v118, v249
v_mul_f32 v119, v119, v249
v_cvt_pk_bf16_f32 v184, v112, v113
v_cvt_pk_bf16_f32 v185, v114, v115
v_mfma_f32_16x16x32_bf16 v[20:23], v[212:215], v[128:131], 0
v_cvt_pk_bf16_f32 v186, v116, v117
v_cvt_pk_bf16_f32 v187, v118, v119
buffer_store_dwordx4 v[184:187], v247, s[44:47], s91 offen offset:2048 sc1
v_mul_f32 v120, v120, v249
v_mul_f32 v121, v121, v249
v_mfma_f32_16x16x32_bf16 v[52:55], v[212:215], v[144:147], 0
ds_read_b64_tr_b16 v[212:213], v241 offset:25472
ds_read_b64_tr_b16 v[214:215], v241 offset:26496
v_mul_f32 v122, v122, v249
v_mul_f32 v123, v123, v249
v_mul_f32 v124, v124, v249
v_mul_f32 v125, v125, v249
v_mfma_f32_16x16x32_bf16 v[24:27], v[216:219], v[128:131], 0
v_mul_f32 v126, v126, v249
v_mul_f32 v127, v127, v249
v_cvt_pk_bf16_f32 v188, v120, v121
v_cvt_pk_bf16_f32 v189, v122, v123
v_cvt_pk_bf16_f32 v190, v124, v125
v_cvt_pk_bf16_f32 v191, v126, v127
v_mfma_f32_16x16x32_bf16 v[56:59], v[216:219], v[144:147], 0
ds_read_b64_tr_b16 v[216:217], v242 offset:25472
ds_read_b64_tr_b16 v[218:219], v242 offset:26496
buffer_store_dwordx4 v[188:191], v247, s[44:47], s91 offen offset:3072 sc1
v_mfma_f32_16x16x32_bf16 v[28:31], v[220:223], v[128:131], 0
s_waitcnt lgkmcnt(6)
v_mfma_f32_16x16x32_bf16 v[60:63], v[220:223], v[144:147], 0
ds_read_b64_tr_b16 v[220:221], v243 offset:25472
ds_read_b64_tr_b16 v[222:223], v243 offset:26496
v_mfma_f32_16x16x32_bf16 v[0:3], v[192:195], v[132:135], v[0:3]
v_accvgpr_read_b32 v224, a228
v_add_u32 v225, 0x8000, v224
v_add_u32 v226, 0x2000, v224
v_add_u32 v227, 0xa000, v224
v_mfma_f32_16x16x32_bf16 v[32:35], v[192:195], v[148:151], v[32:35]
ds_read_b64_tr_b16 v[192:193], v240 offset:33792
ds_read_b64_tr_b16 v[194:195], v240 offset:34816
.La5_LW2_12:
s_cbranch_execz .La5_LW2_R0
v_accvgpr_read_b32 v248, a225
v_accvgpr_read_b32 v228, a229
v_mfma_f32_16x16x32_bf16 v[4:7], v[196:199], v[132:135], v[4:7]
v_accvgpr_read_b32 v229, a230
v_mfma_f32_16x16x32_bf16 v[36:39], v[196:199], v[148:151], v[36:39]
ds_read_b64_tr_b16 v[196:197], v241 offset:33792
ds_read_b64_tr_b16 v[198:199], v241 offset:34816
v_mfma_f32_16x16x32_bf16 v[8:11], v[200:203], v[132:135], v[8:11]
v_mfma_f32_16x16x32_bf16 v[40:43], v[200:203], v[148:151], v[40:43]
ds_read_b64_tr_b16 v[200:201], v242 offset:33792
ds_read_b64_tr_b16 v[202:203], v242 offset:34816
v_mfma_f32_16x16x32_bf16 v[12:15], v[204:207], v[132:135], v[12:15]
s_waitcnt lgkmcnt(6)
v_mfma_f32_16x16x32_bf16 v[44:47], v[204:207], v[148:151], v[44:47]
ds_read_b64_tr_b16 v[204:205], v243 offset:33792
ds_read_b64_tr_b16 v[206:207], v243 offset:34816
v_mfma_f32_16x16x32_bf16 v[16:19], v[208:211], v[132:135], v[16:19]
v_mfma_f32_16x16x32_bf16 v[48:51], v[208:211], v[148:151], v[48:51]
ds_read_b64_tr_b16 v[208:209], v240 offset:33920
ds_read_b64_tr_b16 v[210:211], v240 offset:34944
v_mfma_f32_16x16x32_bf16 v[20:23], v[212:215], v[132:135], v[20:23]
v_mfma_f32_16x16x32_bf16 v[52:55], v[212:215], v[148:151], v[52:55]
ds_read_b64_tr_b16 v[212:213], v241 offset:33920
ds_read_b64_tr_b16 v[214:215], v241 offset:34944
v_mfma_f32_16x16x32_bf16 v[24:27], v[216:219], v[132:135], v[24:27]
v_mfma_f32_16x16x32_bf16 v[56:59], v[216:219], v[148:151], v[56:59]
ds_read_b64_tr_b16 v[216:217], v242 offset:33920
ds_read_b64_tr_b16 v[218:219], v242 offset:34944
v_mfma_f32_16x16x32_bf16 v[28:31], v[220:223], v[132:135], v[28:31]
s_waitcnt lgkmcnt(6)
v_mfma_f32_16x16x32_bf16 v[60:63], v[220:223], v[148:151], v[60:63]
ds_read_b64_tr_b16 v[220:221], v243 offset:33920
ds_read_b64_tr_b16 v[222:223], v243 offset:34944
v_mfma_f32_16x16x32_bf16 v[0:3], v[192:195], v[136:139], v[0:3]
v_mfma_f32_16x16x32_bf16 v[32:35], v[192:195], v[152:155], v[32:35]
ds_read_b64_tr_b16 v[192:193], v240 offset:42240
ds_read_b64_tr_b16 v[194:195], v240 offset:43264
v_mfma_f32_16x16x32_bf16 v[4:7], v[196:199], v[136:139], v[4:7]
v_mfma_f32_16x16x32_bf16 v[36:39], v[196:199], v[152:155], v[36:39]
ds_read_b64_tr_b16 v[196:197], v241 offset:42240
ds_read_b64_tr_b16 v[198:199], v241 offset:43264
v_mfma_f32_16x16x32_bf16 v[8:11], v[200:203], v[136:139], v[8:11]
v_mfma_f32_16x16x32_bf16 v[40:43], v[200:203], v[152:155], v[40:43]
ds_read_b64_tr_b16 v[200:201], v242 offset:42240
ds_read_b64_tr_b16 v[202:203], v242 offset:43264
v_mfma_f32_16x16x32_bf16 v[12:15], v[204:207], v[136:139], v[12:15]
s_waitcnt lgkmcnt(6)
v_mfma_f32_16x16x32_bf16 v[44:47], v[204:207], v[152:155], v[44:47]
ds_read_b64_tr_b16 v[204:205], v243 offset:42240
ds_read_b64_tr_b16 v[206:207], v243 offset:43264
v_mfma_f32_16x16x32_bf16 v[16:19], v[208:211], v[136:139], v[16:19]
v_mfma_f32_16x16x32_bf16 v[48:51], v[208:211], v[152:155], v[48:51]
ds_read_b64_tr_b16 v[208:209], v240 offset:42368
ds_read_b64_tr_b16 v[210:211], v240 offset:43392
v_mfma_f32_16x16x32_bf16 v[20:23], v[212:215], v[136:139], v[20:23]
v_mfma_f32_16x16x32_bf16 v[52:55], v[212:215], v[152:155], v[52:55]
ds_read_b64_tr_b16 v[212:213], v241 offset:42368
ds_read_b64_tr_b16 v[214:215], v241 offset:43392
v_accvgpr_read_b32 v249, a231
s_waitcnt vmcnt(0)
buffer_store_dwordx2 v[252:253], v249, s[64:67], s6 offen offset:0 sc1
v_mfma_f32_16x16x32_bf16 v[24:27], v[216:219], v[136:139], v[24:27]
v_mfma_f32_16x16x32_bf16 v[56:59], v[216:219], v[152:155], v[56:59]
ds_read_b64_tr_b16 v[216:217], v242 offset:42368
ds_read_b64_tr_b16 v[218:219], v242 offset:43392
v_mfma_f32_16x16x32_bf16 v[28:31], v[220:223], v[136:139], v[28:31]
s_waitcnt lgkmcnt(6)
v_mfma_f32_16x16x32_bf16 v[60:63], v[220:223], v[152:155], v[60:63]
ds_read_b64_tr_b16 v[220:221], v243 offset:42368
ds_read_b64_tr_b16 v[222:223], v243 offset:43392
v_mfma_f32_16x16x32_bf16 v[0:3], v[192:195], v[140:143], v[0:3]
v_mfma_f32_16x16x32_bf16 v[32:35], v[192:195], v[156:159], v[32:35]
ds_read_b64_tr_b16 v[192:193], v240 offset:16896
ds_read_b64_tr_b16 v[194:195], v240 offset:17920
v_mfma_f32_16x16x32_bf16 v[4:7], v[196:199], v[140:143], v[4:7]
v_mfma_f32_16x16x32_bf16 v[36:39], v[196:199], v[156:159], v[36:39]
ds_read_b64_tr_b16 v[196:197], v241 offset:16896
ds_read_b64_tr_b16 v[198:199], v241 offset:17920
v_mfma_f32_16x16x32_bf16 v[8:11], v[200:203], v[140:143], v[8:11]
v_mfma_f32_16x16x32_bf16 v[40:43], v[200:203], v[156:159], v[40:43]
ds_read_b64_tr_b16 v[200:201], v242 offset:16896
ds_read_b64_tr_b16 v[202:203], v242 offset:17920
v_mfma_f32_16x16x32_bf16 v[12:15], v[204:207], v[140:143], v[12:15]
s_waitcnt lgkmcnt(6)
v_mfma_f32_16x16x32_bf16 v[44:47], v[204:207], v[156:159], v[44:47]
ds_read_b64_tr_b16 v[204:205], v243 offset:16896
ds_read_b64_tr_b16 v[206:207], v243 offset:17920
v_mfma_f32_16x16x32_bf16 v[16:19], v[208:211], v[140:143], v[16:19]
v_mfma_f32_16x16x32_bf16 v[48:51], v[208:211], v[156:159], v[48:51]
ds_read_b64_tr_b16 v[208:209], v240 offset:17024
ds_read_b64_tr_b16 v[210:211], v240 offset:18048
v_mfma_f32_16x16x32_bf16 v[20:23], v[212:215], v[140:143], v[20:23]
v_mfma_f32_16x16x32_bf16 v[52:55], v[212:215], v[156:159], v[52:55]
ds_read_b64_tr_b16 v[212:213], v241 offset:17024
ds_read_b64_tr_b16 v[214:215], v241 offset:18048
v_mfma_f32_16x16x32_bf16 v[24:27], v[216:219], v[140:143], v[24:27]
v_mfma_f32_16x16x32_bf16 v[56:59], v[216:219], v[156:159], v[56:59]
ds_read_b64_tr_b16 v[216:217], v242 offset:17024
ds_read_b64_tr_b16 v[218:219], v242 offset:18048
v_mfma_f32_16x16x32_bf16 v[28:31], v[220:223], v[140:143], v[28:31]
s_waitcnt lgkmcnt(6)
v_mfma_f32_16x16x32_bf16 v[60:63], v[220:223], v[156:159], v[60:63]
ds_read_b64_tr_b16 v[220:221], v243 offset:17024
ds_read_b64_tr_b16 v[222:223], v243 offset:18048
s_cmp_lg_u32 s30, 0
s_cbranch_scc1 .La5_LO_W
; ---- O projection: 256 x 128 tile over eight heads
.La5_LO_START:
.La5_LW2_13:
s_cbranch_execz .La5_LW2_R1
v_mfma_f32_16x16x32_bf16 v[64:67], v[192:195], v[160:163], 0
buffer_load_dwordx2 v[250:251], v248, s[64:67], s5 offen sc1
v_mfma_f32_16x16x32_bf16 v[96:99], v[192:195], v[176:179], 0
ds_read_b64_tr_b16 v[192:193], v240 offset:25344
ds_read_b64_tr_b16 v[194:195], v240 offset:26368
v_mfma_f32_16x16x32_bf16 v[68:71], v[196:199], v[160:163], 0
s_add_u32 m0, s83, 0
s_bfe_u32 s80, s4, 262148
v_mfma_f32_16x16x32_bf16 v[100:103], v[196:199], v[176:179], 0
ds_read_b64_tr_b16 v[196:197], v241 offset:25344
ds_read_b64_tr_b16 v[198:199], v241 offset:26368
s_xor_b32 s80, s80, s72
s_lshl_b32 s80, s80, 18
s_add_u32 s80, s80, 131072
v_mfma_f32_16x16x32_bf16 v[72:75], v[200:203], v[160:163], 0
buffer_load_dwordx4 v224, s[56:59], s80 offen lds
v_mfma_f32_16x16x32_bf16 v[104:107], v[200:203], v[176:179], 0
ds_read_b64_tr_b16 v[200:201], v242 offset:25344
ds_read_b64_tr_b16 v[202:203], v242 offset:26368
v_mfma_f32_16x16x32_bf16 v[76:79], v[204:207], v[160:163], 0
s_add_u32 m0, s83, 1024
s_waitcnt lgkmcnt(6)
v_mfma_f32_16x16x32_bf16 v[108:111], v[204:207], v[176:179], 0
ds_read_b64_tr_b16 v[204:205], v243 offset:25344
ds_read_b64_tr_b16 v[206:207], v243 offset:26368
v_mfma_f32_16x16x32_bf16 v[80:83], v[208:211], v[160:163], 0
buffer_load_dwordx4 v225, s[56:59], s80 offen lds
v_mfma_f32_16x16x32_bf16 v[112:115], v[208:211], v[176:179], 0
ds_read_b64_tr_b16 v[208:209], v240 offset:25472
ds_read_b64_tr_b16 v[210:211], v240 offset:26496
v_mfma_f32_16x16x32_bf16 v[84:87], v[212:215], v[160:163], 0
s_add_u32 m0, s83, 2176
s_bfe_u32 s53, s4, 262148
v_mfma_f32_16x16x32_bf16 v[116:119], v[212:215], v[176:179], 0
ds_read_b64_tr_b16 v[212:213], v241 offset:25472
ds_read_b64_tr_b16 v[214:215], v241 offset:26496
v_mfma_f32_16x16x32_bf16 v[88:91], v[216:219], v[160:163], 0
buffer_load_dwordx4 v226, s[56:59], s80 offen lds
v_mfma_f32_16x16x32_bf16 v[120:123], v[216:219], v[176:179], 0
ds_read_b64_tr_b16 v[216:217], v242 offset:25472
ds_read_b64_tr_b16 v[218:219], v242 offset:26496
v_mfma_f32_16x16x32_bf16 v[92:95], v[220:223], v[160:163], 0
s_add_u32 m0, s83, 3200
s_waitcnt lgkmcnt(6)
v_mfma_f32_16x16x32_bf16 v[124:127], v[220:223], v[176:179], 0
ds_read_b64_tr_b16 v[220:221], v243 offset:25472
ds_read_b64_tr_b16 v[222:223], v243 offset:26496
v_mfma_f32_16x16x32_bf16 v[64:67], v[192:195], v[164:167], v[64:67]
buffer_load_dwordx4 v227, s[56:59], s80 offen lds
v_mfma_f32_16x16x32_bf16 v[96:99], v[192:195], v[180:183], v[96:99]
ds_read_b64_tr_b16 v[192:193], v240 offset:33792
ds_read_b64_tr_b16 v[194:195], v240 offset:34816
v_mfma_f32_16x16x32_bf16 v[68:71], v[196:199], v[164:167], v[68:71]
s_add_u32 m0, s83, 118272
s_bfe_u32 s80, s4, 262152
v_mfma_f32_16x16x32_bf16 v[100:103], v[196:199], v[180:183], v[100:103]
ds_read_b64_tr_b16 v[196:197], v241 offset:33792
ds_read_b64_tr_b16 v[198:199], v241 offset:34816
s_xor_b32 s80, s80, s72
s_lshl_b32 s80, s80, 18
v_mfma_f32_16x16x32_bf16 v[72:75], v[200:203], v[164:167], v[72:75]
buffer_load_dwordx4 v224, s[56:59], s80 offen lds
v_mfma_f32_16x16x32_bf16 v[104:107], v[200:203], v[180:183], v[104:107]
s_mov_b64 exec, 0
s_cmp_eq_u32 s3, 0
s_cbranch_scc1 .La5_LW3_0
s_cmp_eq_u32 s3, 1
s_cbranch_scc1 .La5_LW3_1
s_cmp_eq_u32 s3, 2
s_cbranch_scc1 .La5_LW3_2
s_branch .La5_LW3_3
.La5_LW3_R0:
s_mov_b64 exec, -1
ds_read_b64_tr_b16 v[200:201], v242 offset:33792
ds_read_b64_tr_b16 v[202:203], v242 offset:34816
v_mfma_f32_16x16x32_bf16 v[76:79], v[204:207], v[164:167], v[76:79]
s_waitcnt vmcnt(5)
v_cmp_eq_u64 vcc, v[250:251], v[252:253]
s_nop 0
s_bitcmp1_b32 vcc_lo, 1
s_nop 0
s_cbranch_scc0 .La5_LO_N1
.La5_LO_K1:
s_waitcnt lgkmcnt(6)
v_mfma_f32_16x16x32_bf16 v[108:111], v[204:207], v[180:183], v[108:111]
ds_read_b64_tr_b16 v[204:205], v243 offset:33792
ds_read_b64_tr_b16 v[206:207], v243 offset:34816
s_xor_b32 s53, s53, s72
s_lshl_b32 s53, s53, 13
v_mfma_f32_16x16x32_bf16 v[80:83], v[208:211], v[164:167], v[80:83]
buffer_load_dwordx4 a[0:3], v244, s[44:47], s53 offen offset:0 sc1
v_mfma_f32_16x16x32_bf16 v[112:115], v[208:211], v[180:183], v[112:115]
ds_read_b64_tr_b16 v[208:209], v240 offset:33920
ds_read_b64_tr_b16 v[210:211], v240 offset:34944
v_mfma_f32_16x16x32_bf16 v[84:87], v[212:215], v[164:167], v[84:87]
buffer_load_dwordx4 a[4:7], v245, s[44:47], s53 offen offset:0 sc1
v_mfma_f32_16x16x32_bf16 v[116:119], v[212:215], v[180:183], v[116:119]
ds_read_b64_tr_b16 v[212:213], v241 offset:33920
ds_read_b64_tr_b16 v[214:215], v241 offset:34944
v_mfma_f32_16x16x32_bf16 v[88:91], v[216:219], v[164:167], v[88:91]
buffer_load_dwordx4 a[8:11], v246, s[44:47], s53 offen offset:0 sc1
v_mfma_f32_16x16x32_bf16 v[120:123], v[216:219], v[180:183], v[120:123]
ds_read_b64_tr_b16 v[216:217], v242 offset:33920
ds_read_b64_tr_b16 v[218:219], v242 offset:34944
v_mfma_f32_16x16x32_bf16 v[92:95], v[220:223], v[164:167], v[92:95]
s_waitcnt lgkmcnt(6)
s_bfe_u32 s54, s4, 262152
v_mfma_f32_16x16x32_bf16 v[124:127], v[220:223], v[180:183], v[124:127]
ds_read_b64_tr_b16 v[220:221], v243 offset:33920
ds_read_b64_tr_b16 v[222:223], v243 offset:34944
v_mfma_f32_16x16x32_bf16 v[64:67], v[192:195], v[168:171], v[64:67]
buffer_load_dwordx4 a[12:15], v247, s[44:47], s53 offen offset:0 sc1
v_mfma_f32_16x16x32_bf16 v[96:99], v[192:195], v[184:187], v[96:99]
ds_read_b64_tr_b16 v[192:193], v240 offset:42240
ds_read_b64_tr_b16 v[194:195], v240 offset:43264
v_mfma_f32_16x16x32_bf16 v[68:71], v[196:199], v[168:171], v[68:71]
buffer_load_dwordx4 a[16:19], v244, s[44:47], s53 offen offset:1024 sc1
v_mfma_f32_16x16x32_bf16 v[100:103], v[196:199], v[184:187], v[100:103]
ds_read_b64_tr_b16 v[196:197], v241 offset:42240
ds_read_b64_tr_b16 v[198:199], v241 offset:43264
v_mfma_f32_16x16x32_bf16 v[72:75], v[200:203], v[168:171], v[72:75]
buffer_load_dwordx4 a[20:23], v245, s[44:47], s53 offen offset:1024 sc1
v_mfma_f32_16x16x32_bf16 v[104:107], v[200:203], v[184:187], v[104:107]
ds_read_b64_tr_b16 v[200:201], v242 offset:42240
ds_read_b64_tr_b16 v[202:203], v242 offset:43264
v_mfma_f32_16x16x32_bf16 v[76:79], v[204:207], v[168:171], v[76:79]
s_waitcnt lgkmcnt(6)
s_xor_b32 s54, s54, s72
v_mfma_f32_16x16x32_bf16 v[108:111], v[204:207], v[184:187], v[108:111]
ds_read_b64_tr_b16 v[204:205], v243 offset:42240
ds_read_b64_tr_b16 v[206:207], v243 offset:43264
v_mfma_f32_16x16x32_bf16 v[80:83], v[208:211], v[168:171], v[80:83]
buffer_load_dwordx4 a[24:27], v246, s[44:47], s53 offen offset:1024 sc1
v_mfma_f32_16x16x32_bf16 v[112:115], v[208:211], v[184:187], v[112:115]
ds_read_b64_tr_b16 v[208:209], v240 offset:42368
ds_read_b64_tr_b16 v[210:211], v240 offset:43392
v_mfma_f32_16x16x32_bf16 v[84:87], v[212:215], v[168:171], v[84:87]
buffer_load_dwordx4 a[28:31], v247, s[44:47], s53 offen offset:1024 sc1
s_add_u32 m0, s83, 119296
v_mfma_f32_16x16x32_bf16 v[116:119], v[212:215], v[184:187], v[116:119]
ds_read_b64_tr_b16 v[212:213], v241 offset:42368
ds_read_b64_tr_b16 v[214:215], v241 offset:43392
v_mfma_f32_16x16x32_bf16 v[88:91], v[216:219], v[168:171], v[88:91]
buffer_load_dwordx4 v225, s[56:59], s80 offen lds
v_mfma_f32_16x16x32_bf16 v[120:123], v[216:219], v[184:187], v[120:123]
ds_read_b64_tr_b16 v[216:217], v242 offset:42368
ds_read_b64_tr_b16 v[218:219], v242 offset:43392
v_mfma_f32_16x16x32_bf16 v[92:95], v[220:223], v[168:171], v[92:95]
s_add_u32 m0, s83, 120448
s_nop 0
s_barrier
s_waitcnt lgkmcnt(6)
v_mfma_f32_16x16x32_bf16 v[124:127], v[220:223], v[184:187], v[124:127]
ds_read_b64_tr_b16 v[220:221], v243 offset:42368
ds_read_b64_tr_b16 v[222:223], v243 offset:43392
v_mfma_f32_16x16x32_bf16 v[64:67], v[192:195], v[172:175], v[64:67]
buffer_load_dwordx4 v226, s[56:59], s80 offen lds
v_mfma_f32_16x16x32_bf16 v[96:99], v[192:195], v[188:191], v[96:99]
ds_read_b64_tr_b16 v[192:193], v240 offset:50688
ds_read_b64_tr_b16 v[194:195], v240 offset:51712
v_mfma_f32_16x16x32_bf16 v[68:71], v[196:199], v[172:175], v[68:71]
buffer_load_dwordx4 a[32:35], v244, s[44:47], s53 offen offset:2048 sc1
v_mfma_f32_16x16x32_bf16 v[100:103], v[196:199], v[188:191], v[100:103]
ds_read_b64_tr_b16 v[196:197], v241 offset:50688
ds_read_b64_tr_b16 v[198:199], v241 offset:51712
v_mfma_f32_16x16x32_bf16 v[72:75], v[200:203], v[172:175], v[72:75]
buffer_load_dwordx4 a[36:39], v245, s[44:47], s53 offen offset:2048 sc1
v_mfma_f32_16x16x32_bf16 v[104:107], v[200:203], v[188:191], v[104:107]
ds_read_b64_tr_b16 v[200:201], v242 offset:50688
ds_read_b64_tr_b16 v[202:203], v242 offset:51712
v_mfma_f32_16x16x32_bf16 v[76:79], v[204:207], v[172:175], v[76:79]
s_waitcnt lgkmcnt(6)
s_lshl_b32 s54, s54, 13
v_mfma_f32_16x16x32_bf16 v[108:111], v[204:207], v[188:191], v[108:111]
ds_read_b64_tr_b16 v[204:205], v243 offset:50688
ds_read_b64_tr_b16 v[206:207], v243 offset:51712
v_mfma_f32_16x16x32_bf16 v[80:83], v[208:211], v[172:175], v[80:83]
buffer_load_dwordx4 a[40:43], v246, s[44:47], s53 offen offset:2048 sc1
v_mfma_f32_16x16x32_bf16 v[112:115], v[208:211], v[188:191], v[112:115]
ds_read_b64_tr_b16 v[208:209], v240 offset:50816
ds_read_b64_tr_b16 v[210:211], v240 offset:51840
v_mfma_f32_16x16x32_bf16 v[84:87], v[212:215], v[172:175], v[84:87]
buffer_load_dwordx4 a[44:47], v247, s[44:47], s53 offen offset:2048 sc1
s_add_u32 m0, s83, 121472
v_mfma_f32_16x16x32_bf16 v[116:119], v[212:215], v[188:191], v[116:119]
ds_read_b64_tr_b16 v[212:213], v241 offset:50816
ds_read_b64_tr_b16 v[214:215], v241 offset:51840
v_mfma_f32_16x16x32_bf16 v[88:91], v[216:219], v[172:175], v[88:91]
buffer_load_dwordx4 v227, s[56:59], s80 offen lds
v_mfma_f32_16x16x32_bf16 v[120:123], v[216:219], v[188:191], v[120:123]
ds_read_b64_tr_b16 v[216:217], v242 offset:50816
ds_read_b64_tr_b16 v[218:219], v242 offset:51840
v_mfma_f32_16x16x32_bf16 v[92:95], v[220:223], v[172:175], v[92:95]
s_waitcnt vmcnt(11) lgkmcnt(6)
s_add_u32 s80, s80, 131072
v_mfma_f32_16x16x32_bf16 v[124:127], v[220:223], v[188:191], v[124:127]
ds_read_b64_tr_b16 v[220:221], v243 offset:50816
ds_read_b64_tr_b16 v[222:223], v243 offset:51840
v_mfma_f32_16x16x32_bf16 v[0:3], v[192:195], a[0:3], v[0:3]
s_add_u32 m0, s83, 67584
s_bfe_u32 s55, s4, 262156
v_mfma_f32_16x16x32_bf16 v[32:35], v[192:195], a[4:7], v[32:35]
buffer_load_dwordx4 v224, s[56:59], s80 offen lds
v_mfma_f32_16x16x32_bf16 v[64:67], v[192:195], a[8:11], v[64:67]
s_xor_b32 s55, s55, s72
s_lshl_b32 s55, s55, 13
v_mfma_f32_16x16x32_bf16 v[96:99], v[192:195], a[12:15], v[96:99]
ds_read_b64_tr_b16 v[192:193], v240 offset:59136
ds_read_b64_tr_b16 v[194:195], v240 offset:60160
v_mfma_f32_16x16x32_bf16 v[4:7], v[196:199], a[0:3], v[4:7]
s_add_u32 m0, s83, 68608
s_bfe_u32 s52, s4, 262160
v_mfma_f32_16x16x32_bf16 v[36:39], v[196:199], a[4:7], v[36:39]
buffer_load_dwordx4 v225, s[56:59], s80 offen lds
v_mfma_f32_16x16x32_bf16 v[68:71], v[196:199], a[8:11], v[68:71]
s_xor_b32 s52, s52, s72
s_lshl_b32 s52, s52, 13
v_mfma_f32_16x16x32_bf16 v[100:103], v[196:199], a[12:15], v[100:103]
ds_read_b64_tr_b16 v[196:197], v241 offset:59136
ds_read_b64_tr_b16 v[198:199], v241 offset:60160
v_mfma_f32_16x16x32_bf16 v[8:11], v[200:203], a[0:3], v[8:11]
v_mfma_f32_16x16x32_bf16 v[40:43], v[200:203], a[4:7], v[40:43]
buffer_load_dwordx2 v[250:251], v248, s[64:67], s5 offen sc1
v_mfma_f32_16x16x32_bf16 v[72:75], v[200:203], a[8:11], v[72:75]
v_mfma_f32_16x16x32_bf16 v[104:107], v[200:203], a[12:15], v[104:107]
ds_read_b64_tr_b16 v[200:201], v242 offset:59136
ds_read_b64_tr_b16 v[202:203], v242 offset:60160
v_mfma_f32_16x16x32_bf16 v[12:15], v[204:207], a[0:3], v[12:15]
s_add_u32 m0, s83, 69760
v_mfma_f32_16x16x32_bf16 v[44:47], v[204:207], a[4:7], v[44:47]
buffer_load_dwordx4 v226, s[56:59], s80 offen lds
v_mfma_f32_16x16x32_bf16 v[76:79], v[204:207], a[8:11], v[76:79]
s_waitcnt vmcnt(11) lgkmcnt(2)
v_mfma_f32_16x16x32_bf16 v[108:111], v[204:207], a[12:15], v[108:111]
ds_read_b64_tr_b16 v[204:205], v243 offset:59136
ds_read_b64_tr_b16 v[206:207], v243 offset:60160
v_mfma_f32_16x16x32_bf16 v[16:19], v[208:211], a[0:3], v[16:19]
s_add_u32 m0, s83, 70784
v_mfma_f32_16x16x32_bf16 v[48:51], v[208:211], a[4:7], v[48:51]
buffer_load_dwordx4 v227, s[56:59], s80 offen lds
v_mfma_f32_16x16x32_bf16 v[80:83], v[208:211], a[8:11], v[80:83]
s_bfe_u32 s80, s4, 262156
s_xor_b32 s80, s80, s72
v_mfma_f32_16x16x32_bf16 v[112:115], v[208:211], a[12:15], v[112:115]
ds_read_b64_tr_b16 v[208:209], v240 offset:59264
ds_read_b64_tr_b16 v[210:211], v240 offset:60288
v_mfma_f32_16x16x32_bf16 v[20:23], v[212:215], a[0:3], v[20:23]
s_lshl_b32 s80, s80, 18
v_mfma_f32_16x16x32_bf16 v[52:55], v[212:215], a[4:7], v[52:55]
buffer_load_dwordx4 a[48:51], v244, s[44:47], s53 offen offset:3072 sc1
v_mfma_f32_16x16x32_bf16 v[84:87], v[212:215], a[8:11], v[84:87]
v_mfma_f32_16x16x32_bf16 v[116:119], v[212:215], a[12:15], v[116:119]
ds_read_b64_tr_b16 v[212:213], v241 offset:59264
ds_read_b64_tr_b16 v[214:215], v241 offset:60288
v_mfma_f32_16x16x32_bf16 v[24:27], v[216:219], a[0:3], v[24:27]
v_mfma_f32_16x16x32_bf16 v[56:59], v[216:219], a[4:7], v[56:59]
buffer_load_dwordx4 a[52:55], v245, s[44:47], s53 offen offset:3072 sc1
v_mfma_f32_16x16x32_bf16 v[88:91], v[216:219], a[8:11], v[88:91]
v_mfma_f32_16x16x32_bf16 v[120:123], v[216:219], a[12:15], v[120:123]
ds_read_b64_tr_b16 v[216:217], v242 offset:59264
ds_read_b64_tr_b16 v[218:219], v242 offset:60288
v_mfma_f32_16x16x32_bf16 v[28:31], v[220:223], a[0:3], v[28:31]
v_mfma_f32_16x16x32_bf16 v[60:63], v[220:223], a[4:7], v[60:63]
buffer_load_dwordx4 a[0:3], v246, s[44:47], s53 offen offset:3072 sc1
v_mfma_f32_16x16x32_bf16 v[92:95], v[220:223], a[8:11], v[92:95]
v_mfma_f32_16x16x32_bf16 v[124:127], v[220:223], a[12:15], v[124:127]
ds_read_b64_tr_b16 v[220:221], v243 offset:59264
ds_read_b64_tr_b16 v[222:223], v243 offset:60288
v_mfma_f32_16x16x32_bf16 v[0:3], v[192:195], a[16:19], v[0:3]
v_mfma_f32_16x16x32_bf16 v[32:35], v[192:195], a[20:23], v[32:35]
buffer_load_dwordx4 a[4:7], v247, s[44:47], s53 offen offset:3072 sc1
v_mfma_f32_16x16x32_bf16 v[64:67], v[192:195], a[24:27], v[64:67]
s_bfe_u32 s53, s4, 262164
s_xor_b32 s53, s53, s72
v_mfma_f32_16x16x32_bf16 v[96:99], v[192:195], a[28:31], v[96:99]
s_lshl_b32 s53, s53, 13
v_mfma_f32_16x16x32_bf16 v[4:7], v[196:199], a[16:19], v[4:7]
s_add_u32 m0, s83, 84480
s_waitcnt lgkmcnt(0)
s_barrier
v_mfma_f32_16x16x32_bf16 v[36:39], v[196:199], a[20:23], v[36:39]
buffer_load_dwordx4 v224, s[56:59], s80 offen lds
v_mfma_f32_16x16x32_bf16 v[68:71], v[196:199], a[24:27], v[68:71]
ds_read_b64_tr_b16 v[192:193], v240 offset:0
ds_read_b64_tr_b16 v[194:195], v240 offset:1024
s_nop 0
v_mfma_f32_16x16x32_bf16 v[100:103], v[196:199], a[28:31], v[100:103]
ds_read_b64_tr_b16 v[196:197], v241 offset:0
ds_read_b64_tr_b16 v[198:199], v241 offset:1024
v_mfma_f32_16x16x32_bf16 v[8:11], v[200:203], a[16:19], v[8:11]
s_add_u32 m0, s83, 85504
v_mfma_f32_16x16x32_bf16 v[40:43], v[200:203], a[20:23], v[40:43]
buffer_load_dwordx4 v225, s[56:59], s80 offen lds
v_mfma_f32_16x16x32_bf16 v[72:75], v[200:203], a[24:27], v[72:75]
v_mfma_f32_16x16x32_bf16 v[104:107], v[200:203], a[28:31], v[104:107]
ds_read_b64_tr_b16 v[200:201], v242 offset:0
ds_read_b64_tr_b16 v[202:203], v242 offset:1024
v_mfma_f32_16x16x32_bf16 v[12:15], v[204:207], a[16:19], v[12:15]
s_add_u32 m0, s83, 86656
v_mfma_f32_16x16x32_bf16 v[44:47], v[204:207], a[20:23], v[44:47]
buffer_load_dwordx4 v226, s[56:59], s80 offen lds
v_mfma_f32_16x16x32_bf16 v[76:79], v[204:207], a[24:27], v[76:79]
v_mfma_f32_16x16x32_bf16 v[108:111], v[204:207], a[28:31], v[108:111]
ds_read_b64_tr_b16 v[204:205], v243 offset:0
ds_read_b64_tr_b16 v[206:207], v243 offset:1024
v_mfma_f32_16x16x32_bf16 v[16:19], v[208:211], a[16:19], v[16:19]
s_add_u32 m0, s83, 87680
v_mfma_f32_16x16x32_bf16 v[48:51], v[208:211], a[20:23], v[48:51]
buffer_load_dwordx4 v227, s[56:59], s80 offen lds
v_mfma_f32_16x16x32_bf16 v[80:83], v[208:211], a[24:27], v[80:83]
s_add_u32 s80, s80, 131072
s_waitcnt vmcnt(10)
v_mfma_f32_16x16x32_bf16 v[112:115], v[208:211], a[28:31], v[112:115]
ds_read_b64_tr_b16 v[208:209], v240 offset:128
ds_read_b64_tr_b16 v[210:211], v240 offset:1152
v_mfma_f32_16x16x32_bf16 v[20:23], v[212:215], a[16:19], v[20:23]
s_add_u32 m0, s83, 101376
v_cmp_eq_u64 vcc, v[250:251], v[252:253]
v_mfma_f32_16x16x32_bf16 v[52:55], v[212:215], a[20:23], v[52:55]
buffer_load_dwordx4 v224, s[56:59], s80 offen lds
v_mfma_f32_16x16x32_bf16 v[84:87], v[212:215], a[24:27], v[84:87]
s_bitcmp1_b32 vcc_lo, 2
s_cbranch_scc0 .La5_LO_N2
.La5_LO_K2:
v_mfma_f32_16x16x32_bf16 v[116:119], v[212:215], a[28:31], v[116:119]
ds_read_b64_tr_b16 v[212:213], v241 offset:128
ds_read_b64_tr_b16 v[214:215], v241 offset:1152
v_mfma_f32_16x16x32_bf16 v[24:27], v[216:219], a[16:19], v[24:27]
v_mfma_f32_16x16x32_bf16 v[56:59], v[216:219], a[20:23], v[56:59]
buffer_load_dwordx4 a[8:11], v244, s[44:47], s54 offen offset:0 sc1
v_mfma_f32_16x16x32_bf16 v[88:91], v[216:219], a[24:27], v[88:91]
v_mfma_f32_16x16x32_bf16 v[120:123], v[216:219], a[28:31], v[120:123]
ds_read_b64_tr_b16 v[216:217], v242 offset:128
ds_read_b64_tr_b16 v[218:219], v242 offset:1152
v_mfma_f32_16x16x32_bf16 v[28:31], v[220:223], a[16:19], v[28:31]
v_mfma_f32_16x16x32_bf16 v[60:63], v[220:223], a[20:23], v[60:63]
v_mfma_f32_16x16x32_bf16 v[92:95], v[220:223], a[24:27], v[92:95]
s_waitcnt lgkmcnt(2)
v_mfma_f32_16x16x32_bf16 v[124:127], v[220:223], a[28:31], v[124:127]
ds_read_b64_tr_b16 v[220:221], v243 offset:128
ds_read_b64_tr_b16 v[222:223], v243 offset:1152
v_mfma_f32_16x16x32_bf16 v[0:3], v[192:195], a[32:35], v[0:3]
v_mfma_f32_16x16x32_bf16 v[32:35], v[192:195], a[36:39], v[32:35]
buffer_load_dwordx4 a[12:15], v245, s[44:47], s54 offen offset:0 sc1
v_mfma_f32_16x16x32_bf16 v[64:67], v[192:195], a[40:43], v[64:67]
v_mfma_f32_16x16x32_bf16 v[96:99], v[192:195], a[44:47], v[96:99]
ds_read_b64_tr_b16 v[192:193], v240 offset:8448
ds_read_b64_tr_b16 v[194:195], v240 offset:9472
v_mfma_f32_16x16x32_bf16 v[4:7], v[196:199], a[32:35], v[4:7]
v_mfma_f32_16x16x32_bf16 v[36:39], v[196:199], a[36:39], v[36:39]
buffer_load_dwordx4 a[16:19], v246, s[44:47], s54 offen offset:0 sc1
v_mfma_f32_16x16x32_bf16 v[68:71], v[196:199], a[40:43], v[68:71]
v_mfma_f32_16x16x32_bf16 v[100:103], v[196:199], a[44:47], v[100:103]
ds_read_b64_tr_b16 v[196:197], v241 offset:8448
ds_read_b64_tr_b16 v[198:199], v241 offset:9472
v_mfma_f32_16x16x32_bf16 v[8:11], v[200:203], a[32:35], v[8:11]
s_mov_b32 s86, 0x05040100
s_mov_b32 s87, 0x07060302
v_mfma_f32_16x16x32_bf16 v[40:43], v[200:203], a[36:39], v[40:43]
buffer_load_dwordx4 a[20:23], v247, s[44:47], s54 offen offset:0 sc1
v_mfma_f32_16x16x32_bf16 v[72:75], v[200:203], a[40:43], v[72:75]
s_nop 0
v_accvgpr_read_b32 v230, a128
v_mfma_f32_16x16x32_bf16 v[104:107], v[200:203], a[44:47], v[104:107]
ds_read_b64_tr_b16 v[200:201], v242 offset:8448
ds_read_b64_tr_b16 v[202:203], v242 offset:9472
v_mfma_f32_16x16x32_bf16 v[12:15], v[204:207], a[32:35], v[12:15]
v_accvgpr_read_b32 v231, a132
v_perm_b32 v232, v231, v230, s86
v_mfma_f32_16x16x32_bf16 v[44:47], v[204:207], a[36:39], v[44:47]
buffer_load_dwordx4 a[24:27], v244, s[44:47], s54 offen offset:1024 sc1
v_mfma_f32_16x16x32_bf16 v[76:79], v[204:207], a[40:43], v[76:79]
v_perm_b32 v233, v231, v230, s87
v_accvgpr_write_b32 a96, v232
v_mfma_f32_16x16x32_bf16 v[108:111], v[204:207], a[44:47], v[108:111]
ds_read_b64_tr_b16 v[204:205], v243 offset:8448
ds_read_b64_tr_b16 v[206:207], v243 offset:9472
v_mfma_f32_16x16x32_bf16 v[16:19], v[208:211], a[32:35], v[16:19]
v_accvgpr_write_b32 a100, v233
v_accvgpr_read_b32 v234, a136
v_mfma_f32_16x16x32_bf16 v[48:51], v[208:211], a[36:39], v[48:51]
buffer_load_dwordx4 a[28:31], v245, s[44:47], s54 offen offset:1024 sc1
v_mfma_f32_16x16x32_bf16 v[80:83], v[208:211], a[40:43], v[80:83]
v_accvgpr_read_b32 v235, a140
v_perm_b32 v236, v235, v234, s86
v_mfma_f32_16x16x32_bf16 v[112:115], v[208:211], a[44:47], v[112:115]
ds_read_b64_tr_b16 v[208:209], v240 offset:8576
ds_read_b64_tr_b16 v[210:211], v240 offset:9600
v_mfma_f32_16x16x32_bf16 v[20:23], v[212:215], a[32:35], v[20:23]
v_add_u32 v240, 67584, v240
v_perm_b32 v237, v235, v234, s87
v_mfma_f32_16x16x32_bf16 v[52:55], v[212:215], a[36:39], v[52:55]
buffer_load_dwordx4 a[56:59], v246, s[44:47], s54 offen offset:1024 sc1
v_mfma_f32_16x16x32_bf16 v[84:87], v[212:215], a[40:43], v[84:87]
s_waitcnt vmcnt(12) lgkmcnt(2)
v_accvgpr_write_b32 a97, v236
v_mfma_f32_16x16x32_bf16 v[116:119], v[212:215], a[44:47], v[116:119]
ds_read_b64_tr_b16 v[212:213], v241 offset:8576
ds_read_b64_tr_b16 v[214:215], v241 offset:9600
v_mfma_f32_16x16x32_bf16 v[24:27], v[216:219], a[32:35], v[24:27]
v_add_u32 v241, 67584, v241
v_accvgpr_write_b32 a101, v237
v_mfma_f32_16x16x32_bf16 v[56:59], v[216:219], a[36:39], v[56:59]
buffer_load_dwordx4 a[60:63], v247, s[44:47], s54 offen offset:1024 sc1
v_mfma_f32_16x16x32_bf16 v[88:91], v[216:219], a[40:43], v[88:91]
v_accvgpr_read_b32 v230, a144
v_accvgpr_read_b32 v231, a148
v_mfma_f32_16x16x32_bf16 v[120:123], v[216:219], a[44:47], v[120:123]
ds_read_b64_tr_b16 v[216:217], v242 offset:8576
ds_read_b64_tr_b16 v[218:219], v242 offset:9600
v_mfma_f32_16x16x32_bf16 v[28:31], v[220:223], a[32:35], v[28:31]
v_add_u32 v242, 67584, v242
v_perm_b32 v232, v231, v230, s86
v_mfma_f32_16x16x32_bf16 v[60:63], v[220:223], a[36:39], v[60:63]
v_perm_b32 v233, v231, v230, s87
v_accvgpr_write_b32 a98, v232
v_mfma_f32_16x16x32_bf16 v[92:95], v[220:223], a[40:43], v[92:95]
v_accvgpr_write_b32 a102, v233
v_accvgpr_read_b32 v234, a152
v_mfma_f32_16x16x32_bf16 v[124:127], v[220:223], a[44:47], v[124:127]
ds_read_b64_tr_b16 v[220:221], v243 offset:8576
ds_read_b64_tr_b16 v[222:223], v243 offset:9600
v_mfma_f32_16x16x32_bf16 v[0:3], v[192:195], a[48:51], v[0:3]
s_add_u32 m0, s83, 102400
v_add_u32 v243, 67584, v243
v_mfma_f32_16x16x32_bf16 v[32:35], v[192:195], a[52:55], v[32:35]
buffer_load_dwordx4 v225, s[56:59], s80 offen lds
v_mfma_f32_16x16x32_bf16 v[64:67], v[192:195], a[0:3], v[64:67]
v_accvgpr_read_b32 v235, a156
v_perm_b32 v236, v235, v234, s86
v_mfma_f32_16x16x32_bf16 v[96:99], v[192:195], a[4:7], v[96:99]
v_perm_b32 v237, v235, v234, s87
v_accvgpr_write_b32 a99, v236
v_mfma_f32_16x16x32_bf16 v[4:7], v[196:199], a[48:51], v[4:7]
s_add_u32 m0, s83, 103552
s_waitcnt lgkmcnt(0)
s_barrier
v_mfma_f32_16x16x32_bf16 v[36:39], v[196:199], a[52:55], v[36:39]
buffer_load_dwordx4 v226, s[56:59], s80 offen lds
v_mfma_f32_16x16x32_bf16 v[68:71], v[196:199], a[0:3], v[68:71]
.La5_LW3_0:
s_cbranch_execz .La5_LW3_4
ds_read_b64_tr_b16 v[192:193], v240 offset:50688
ds_read_b64_tr_b16 v[194:195], v240 offset:51712
v_mfma_f32_16x16x32_bf16 v[100:103], v[196:199], a[4:7], v[100:103]
ds_read_b64_tr_b16 v[196:197], v241 offset:50688
ds_read_b64_tr_b16 v[198:199], v241 offset:51712
v_mfma_f32_16x16x32_bf16 v[8:11], v[200:203], a[48:51], v[8:11]
v_accvgpr_write_b32 a103, v237
v_accvgpr_read_b32 v230, a129
v_mfma_f32_16x16x32_bf16 v[40:43], v[200:203], a[52:55], v[40:43]
buffer_load_dwordx4 a[32:35], v244, s[44:47], s54 offen offset:2048 sc1
v_mfma_f32_16x16x32_bf16 v[72:75], v[200:203], a[0:3], v[72:75]
v_accvgpr_read_b32 v231, a133
v_perm_b32 v232, v231, v230, s86
v_mfma_f32_16x16x32_bf16 v[104:107], v[200:203], a[4:7], v[104:107]
ds_read_b64_tr_b16 v[200:201], v242 offset:50688
ds_read_b64_tr_b16 v[202:203], v242 offset:51712
v_mfma_f32_16x16x32_bf16 v[12:15], v[204:207], a[48:51], v[12:15]
v_perm_b32 v233, v231, v230, s87
v_accvgpr_write_b32 a104, v232
v_mfma_f32_16x16x32_bf16 v[44:47], v[204:207], a[52:55], v[44:47]
buffer_load_dwordx4 a[36:39], v245, s[44:47], s54 offen offset:2048 sc1
v_mfma_f32_16x16x32_bf16 v[76:79], v[204:207], a[0:3], v[76:79]
s_waitcnt vmcnt(8) lgkmcnt(2)
v_accvgpr_write_b32 a108, v233
v_mfma_f32_16x16x32_bf16 v[108:111], v[204:207], a[4:7], v[108:111]
ds_read_b64_tr_b16 v[204:205], v243 offset:50688
ds_read_b64_tr_b16 v[206:207], v243 offset:51712
v_mfma_f32_16x16x32_bf16 v[16:19], v[208:211], a[48:51], v[16:19]
v_accvgpr_read_b32 v234, a137
v_accvgpr_read_b32 v235, a141
v_mfma_f32_16x16x32_bf16 v[48:51], v[208:211], a[52:55], v[48:51]
buffer_load_dwordx4 a[40:43], v246, s[44:47], s54 offen offset:2048 sc1
v_mfma_f32_16x16x32_bf16 v[80:83], v[208:211], a[0:3], v[80:83]
v_perm_b32 v236, v235, v234, s86
v_perm_b32 v237, v235, v234, s87
v_mfma_f32_16x16x32_bf16 v[112:115], v[208:211], a[4:7], v[112:115]
ds_read_b64_tr_b16 v[208:209], v240 offset:50816
ds_read_b64_tr_b16 v[210:211], v240 offset:51840
v_mfma_f32_16x16x32_bf16 v[20:23], v[212:215], a[48:51], v[20:23]
v_accvgpr_write_b32 a105, v236
v_accvgpr_write_b32 a109, v237
v_mfma_f32_16x16x32_bf16 v[52:55], v[212:215], a[52:55], v[52:55]
buffer_load_dwordx4 a[44:47], v247, s[44:47], s54 offen offset:2048 sc1
v_mfma_f32_16x16x32_bf16 v[84:87], v[212:215], a[0:3], v[84:87]
v_accvgpr_read_b32 v230, a145
v_accvgpr_read_b32 v231, a149
v_mfma_f32_16x16x32_bf16 v[116:119], v[212:215], a[4:7], v[116:119]
ds_read_b64_tr_b16 v[212:213], v241 offset:50816
ds_read_b64_tr_b16 v[214:215], v241 offset:51840
v_mfma_f32_16x16x32_bf16 v[24:27], v[216:219], a[48:51], v[24:27]
v_perm_b32 v232, v231, v230, s86
v_perm_b32 v233, v231, v230, s87
v_mfma_f32_16x16x32_bf16 v[56:59], v[216:219], a[52:55], v[56:59]
buffer_load_dwordx2 v[250:251], v248, s[64:67], s5 offen sc1
v_mfma_f32_16x16x32_bf16 v[88:91], v[216:219], a[0:3], v[88:91]
v_accvgpr_write_b32 a106, v232
v_accvgpr_write_b32 a110, v233
v_mfma_f32_16x16x32_bf16 v[120:123], v[216:219], a[4:7], v[120:123]
ds_read_b64_tr_b16 v[216:217], v242 offset:50816
ds_read_b64_tr_b16 v[218:219], v242 offset:51840
v_mfma_f32_16x16x32_bf16 v[28:31], v[220:223], a[48:51], v[28:31]
s_add_u32 m0, s83, 104576
v_accvgpr_read_b32 v234, a153
v_mfma_f32_16x16x32_bf16 v[60:63], v[220:223], a[52:55], v[60:63]
buffer_load_dwordx4 v227, s[56:59], s80 offen lds
v_mfma_f32_16x16x32_bf16 v[92:95], v[220:223], a[0:3], v[92:95]
s_bfe_u32 s80, s4, 262160
s_xor_b32 s80, s80, s72
v_mfma_f32_16x16x32_bf16 v[124:127], v[220:223], a[4:7], v[124:127]
ds_read_b64_tr_b16 v[220:221], v243 offset:50816
ds_read_b64_tr_b16 v[222:223], v243 offset:51840
v_mfma_f32_16x16x32_bf16 v[0:3], v[192:195], a[8:11], v[0:3]
s_lshl_b32 s80, s80, 18
v_accvgpr_read_b32 v235, a157
v_mfma_f32_16x16x32_bf16 v[32:35], v[192:195], a[12:15], v[32:35]
buffer_load_dwordx4 a[0:3], v244, s[44:47], s54 offen offset:3072 sc1
v_mfma_f32_16x16x32_bf16 v[64:67], v[192:195], a[16:19], v[64:67]
v_perm_b32 v236, v235, v234, s86
v_perm_b32 v237, v235, v234, s87
v_mfma_f32_16x16x32_bf16 v[96:99], v[192:195], a[20:23], v[96:99]
ds_read_b64_tr_b16 v[192:193], v240 offset:59136
ds_read_b64_tr_b16 v[194:195], v240 offset:60160
v_mfma_f32_16x16x32_bf16 v[4:7], v[196:199], a[8:11], v[4:7]
v_accvgpr_write_b32 a107, v236
v_accvgpr_write_b32 a111, v237
v_mfma_f32_16x16x32_bf16 v[36:39], v[196:199], a[12:15], v[36:39]
buffer_load_dwordx4 a[4:7], v245, s[44:47], s54 offen offset:3072 sc1
v_mfma_f32_16x16x32_bf16 v[68:71], v[196:199], a[16:19], v[68:71]
s_waitcnt lgkmcnt(2)
v_accvgpr_read_b32 v230, a130
v_mfma_f32_16x16x32_bf16 v[100:103], v[196:199], a[20:23], v[100:103]
ds_read_b64_tr_b16 v[196:197], v241 offset:59136
ds_read_b64_tr_b16 v[198:199], v241 offset:60160
v_mfma_f32_16x16x32_bf16 v[8:11], v[200:203], a[8:11], v[8:11]
v_accvgpr_read_b32 v231, a134
v_perm_b32 v232, v231, v230, s86
v_mfma_f32_16x16x32_bf16 v[40:43], v[200:203], a[12:15], v[40:43]
buffer_load_dwordx4 a[48:51], v246, s[44:47], s54 offen offset:3072 sc1
v_mfma_f32_16x16x32_bf16 v[72:75], v[200:203], a[16:19], v[72:75]
v_perm_b32 v233, v231, v230, s87
v_accvgpr_write_b32 a112, v232
v_mfma_f32_16x16x32_bf16 v[104:107], v[200:203], a[20:23], v[104:107]
ds_read_b64_tr_b16 v[200:201], v242 offset:59136
ds_read_b64_tr_b16 v[202:203], v242 offset:60160
v_mfma_f32_16x16x32_bf16 v[12:15], v[204:207], a[8:11], v[12:15]
s_bitcmp1_b32 vcc_lo, 3
s_cbranch_scc0 .La5_LO_N3
.La5_LO_K3:
v_mfma_f32_16x16x32_bf16 v[44:47], v[204:207], a[12:15], v[44:47]
buffer_load_dwordx4 a[52:55], v247, s[44:47], s54 offen offset:3072 sc1
v_mfma_f32_16x16x32_bf16 v[76:79], v[204:207], a[16:19], v[76:79]
s_bfe_u32 s54, s4, 262168
s_xor_b32 s54, s54, s72
v_mfma_f32_16x16x32_bf16 v[108:111], v[204:207], a[20:23], v[108:111]
ds_read_b64_tr_b16 v[204:205], v243 offset:59136
ds_read_b64_tr_b16 v[206:207], v243 offset:60160
v_mfma_f32_16x16x32_bf16 v[16:19], v[208:211], a[8:11], v[16:19]
s_lshl_b32 s54, s54, 13
v_accvgpr_write_b32 a116, v233
v_mfma_f32_16x16x32_bf16 v[48:51], v[208:211], a[12:15], v[48:51]
buffer_load_dwordx4 a[64:67], v244, s[44:47], s55 offen offset:0 sc1
v_mfma_f32_16x16x32_bf16 v[80:83], v[208:211], a[16:19], v[80:83]
v_accvgpr_read_b32 v234, a138
v_accvgpr_read_b32 v235, a142
v_mfma_f32_16x16x32_bf16 v[112:115], v[208:211], a[20:23], v[112:115]
ds_read_b64_tr_b16 v[208:209], v240 offset:59264
ds_read_b64_tr_b16 v[210:211], v240 offset:60288
v_mfma_f32_16x16x32_bf16 v[20:23], v[212:215], a[8:11], v[20:23]
v_perm_b32 v236, v235, v234, s86
v_perm_b32 v237, v235, v234, s87
v_mfma_f32_16x16x32_bf16 v[52:55], v[212:215], a[12:15], v[52:55]
buffer_load_dwordx4 a[68:71], v245, s[44:47], s55 offen offset:0 sc1
v_mfma_f32_16x16x32_bf16 v[84:87], v[212:215], a[16:19], v[84:87]
v_accvgpr_write_b32 a113, v236
v_accvgpr_write_b32 a117, v237
v_mfma_f32_16x16x32_bf16 v[116:119], v[212:215], a[20:23], v[116:119]
ds_read_b64_tr_b16 v[212:213], v241 offset:59264
ds_read_b64_tr_b16 v[214:215], v241 offset:60288
v_mfma_f32_16x16x32_bf16 v[24:27], v[216:219], a[8:11], v[24:27]
v_accvgpr_read_b32 v230, a146
v_accvgpr_read_b32 v231, a150
v_mfma_f32_16x16x32_bf16 v[56:59], v[216:219], a[12:15], v[56:59]
buffer_load_dwordx4 a[72:75], v246, s[44:47], s55 offen offset:0 sc1
v_mfma_f32_16x16x32_bf16 v[88:91], v[216:219], a[16:19], v[88:91]
v_perm_b32 v232, v231, v230, s86
v_perm_b32 v233, v231, v230, s87
v_mfma_f32_16x16x32_bf16 v[120:123], v[216:219], a[20:23], v[120:123]
ds_read_b64_tr_b16 v[216:217], v242 offset:59264
ds_read_b64_tr_b16 v[218:219], v242 offset:60288
v_mfma_f32_16x16x32_bf16 v[28:31], v[220:223], a[8:11], v[28:31]
v_accvgpr_write_b32 a114, v232
v_accvgpr_write_b32 a118, v233
v_mfma_f32_16x16x32_bf16 v[60:63], v[220:223], a[12:15], v[60:63]
v_accvgpr_read_b32 v234, a154
v_accvgpr_read_b32 v235, a158
v_mfma_f32_16x16x32_bf16 v[92:95], v[220:223], a[16:19], v[92:95]
s_waitcnt vmcnt(15) lgkmcnt(2)
v_perm_b32 v236, v235, v234, s86
v_mfma_f32_16x16x32_bf16 v[124:127], v[220:223], a[20:23], v[124:127]
ds_read_b64_tr_b16 v[220:221], v243 offset:59264
ds_read_b64_tr_b16 v[222:223], v243 offset:60288
v_mfma_f32_16x16x32_bf16 v[0:3], v[192:195], a[24:27], v[0:3]
v_perm_b32 v237, v235, v234, s87
v_accvgpr_write_b32 a115, v236
v_mfma_f32_16x16x32_bf16 v[32:35], v[192:195], a[28:31], v[32:35]
buffer_load_dwordx4 a[8:11], v247, s[44:47], s55 offen offset:0 sc1
v_mfma_f32_16x16x32_bf16 v[64:67], v[192:195], a[56:59], v[64:67]
v_accvgpr_write_b32 a119, v237
v_accvgpr_read_b32 v230, a131
v_mfma_f32_16x16x32_bf16 v[96:99], v[192:195], a[60:63], v[96:99]
ds_read_b64_tr_b16 v[192:193], v240 offset:0
ds_read_b64_tr_b16 v[194:195], v240 offset:1024
v_mfma_f32_16x16x32_bf16 v[4:7], v[196:199], a[24:27], v[4:7]
v_accvgpr_read_b32 v231, a135
v_perm_b32 v232, v231, v230, s86
v_mfma_f32_16x16x32_bf16 v[36:39], v[196:199], a[28:31], v[36:39]
buffer_load_dwordx4 a[12:15], v244, s[44:47], s55 offen offset:1024 sc1
v_mfma_f32_16x16x32_bf16 v[68:71], v[196:199], a[56:59], v[68:71]
v_perm_b32 v233, v231, v230, s87
v_accvgpr_write_b32 a120, v232
v_mfma_f32_16x16x32_bf16 v[100:103], v[196:199], a[60:63], v[100:103]
ds_read_b64_tr_b16 v[196:197], v241 offset:0
ds_read_b64_tr_b16 v[198:199], v241 offset:1024
v_mfma_f32_16x16x32_bf16 v[8:11], v[200:203], a[24:27], v[8:11]
v_accvgpr_write_b32 a124, v233
v_accvgpr_read_b32 v234, a139
v_mfma_f32_16x16x32_bf16 v[40:43], v[200:203], a[28:31], v[40:43]
buffer_load_dwordx4 a[16:19], v245, s[44:47], s55 offen offset:1024 sc1
v_mfma_f32_16x16x32_bf16 v[72:75], v[200:203], a[56:59], v[72:75]
v_accvgpr_read_b32 v235, a143
v_perm_b32 v236, v235, v234, s86
v_mfma_f32_16x16x32_bf16 v[104:107], v[200:203], a[60:63], v[104:107]
ds_read_b64_tr_b16 v[200:201], v242 offset:0
ds_read_b64_tr_b16 v[202:203], v242 offset:1024
v_mfma_f32_16x16x32_bf16 v[12:15], v[204:207], a[24:27], v[12:15]
s_load_dwordx2 s[16:17], s[0:1], 0
s_load_dwordx4 s[36:39], s[0:1], 80
v_mfma_f32_16x16x32_bf16 v[44:47], v[204:207], a[28:31], v[44:47]
buffer_load_dwordx4 a[20:23], v246, s[44:47], s55 offen offset:1024 sc1
v_mfma_f32_16x16x32_bf16 v[76:79], v[204:207], a[56:59], v[76:79]
v_perm_b32 v237, v235, v234, s87
v_accvgpr_write_b32 a121, v236
v_mfma_f32_16x16x32_bf16 v[108:111], v[204:207], a[60:63], v[108:111]
ds_read_b64_tr_b16 v[204:205], v243 offset:0
ds_read_b64_tr_b16 v[206:207], v243 offset:1024
v_mfma_f32_16x16x32_bf16 v[16:19], v[208:211], a[24:27], v[16:19]
v_accvgpr_write_b32 a125, v237
v_accvgpr_read_b32 v230, a147
v_mfma_f32_16x16x32_bf16 v[48:51], v[208:211], a[28:31], v[48:51]
buffer_load_dwordx4 a[76:79], v247, s[44:47], s55 offen offset:1024 sc1
v_mfma_f32_16x16x32_bf16 v[80:83], v[208:211], a[56:59], v[80:83]
v_accvgpr_read_b32 v231, a151
v_perm_b32 v232, v231, v230, s86
v_mfma_f32_16x16x32_bf16 v[112:115], v[208:211], a[60:63], v[112:115]
ds_read_b64_tr_b16 v[208:209], v240 offset:128
ds_read_b64_tr_b16 v[210:211], v240 offset:1152
v_mfma_f32_16x16x32_bf16 v[20:23], v[212:215], a[24:27], v[20:23]
s_add_u32 m0, s83, 16896
v_perm_b32 v233, v231, v230, s87
v_mfma_f32_16x16x32_bf16 v[52:55], v[212:215], a[28:31], v[52:55]
buffer_load_dwordx4 v224, s[56:59], s80 offen lds
v_mfma_f32_16x16x32_bf16 v[84:87], v[212:215], a[56:59], v[84:87]
s_waitcnt vmcnt(15) lgkmcnt(2)
v_accvgpr_write_b32 a122, v232
v_mfma_f32_16x16x32_bf16 v[116:119], v[212:215], a[60:63], v[116:119]
ds_read_b64_tr_b16 v[212:213], v241 offset:128
ds_read_b64_tr_b16 v[214:215], v241 offset:1152
v_mfma_f32_16x16x32_bf16 v[24:27], v[216:219], a[24:27], v[24:27]
s_add_u32 m0, s83, 17920
v_accvgpr_write_b32 a126, v233
v_mfma_f32_16x16x32_bf16 v[56:59], v[216:219], a[28:31], v[56:59]
buffer_load_dwordx4 v225, s[56:59], s80 offen lds
v_mfma_f32_16x16x32_bf16 v[88:91], v[216:219], a[56:59], v[88:91]
v_accvgpr_read_b32 v234, a155
v_accvgpr_read_b32 v235, a159
v_mfma_f32_16x16x32_bf16 v[120:123], v[216:219], a[60:63], v[120:123]
ds_read_b64_tr_b16 v[216:217], v242 offset:128
ds_read_b64_tr_b16 v[218:219], v242 offset:1152
v_mfma_f32_16x16x32_bf16 v[28:31], v[220:223], a[24:27], v[28:31]
v_perm_b32 v236, v235, v234, s86
v_perm_b32 v237, v235, v234, s87
v_mfma_f32_16x16x32_bf16 v[60:63], v[220:223], a[28:31], v[60:63]
v_accvgpr_write_b32 a123, v236
v_accvgpr_write_b32 a127, v237
v_mfma_f32_16x16x32_bf16 v[92:95], v[220:223], a[56:59], v[92:95]
v_accvgpr_read_b32 v230, a160
v_accvgpr_read_b32 v231, a164
v_mfma_f32_16x16x32_bf16 v[124:127], v[220:223], a[60:63], v[124:127]
ds_read_b64_tr_b16 v[220:221], v243 offset:128
ds_read_b64_tr_b16 v[222:223], v243 offset:1152
v_mfma_f32_16x16x32_bf16 v[0:3], v[192:195], a[32:35], v[0:3]
s_add_u32 m0, s83, 19072
v_perm_b32 v232, v231, v230, s86
v_mfma_f32_16x16x32_bf16 v[32:35], v[192:195], a[36:39], v[32:35]
buffer_load_dwordx4 v226, s[56:59], s80 offen lds
v_mfma_f32_16x16x32_bf16 v[64:67], v[192:195], a[40:43], v[64:67]
v_perm_b32 v233, v231, v230, s87
v_accvgpr_write_b32 a128, v232
v_mfma_f32_16x16x32_bf16 v[96:99], v[192:195], a[44:47], v[96:99]
ds_read_b64_tr_b16 v[192:193], v240 offset:8448
ds_read_b64_tr_b16 v[194:195], v240 offset:9472
v_mfma_f32_16x16x32_bf16 v[4:7], v[196:199], a[32:35], v[4:7]
v_accvgpr_write_b32 a132, v233
v_accvgpr_read_b32 v234, a168
v_mfma_f32_16x16x32_bf16 v[36:39], v[196:199], a[36:39], v[36:39]
buffer_load_dwordx4 a[24:27], v244, s[44:47], s55 offen offset:2048 sc1
v_mfma_f32_16x16x32_bf16 v[68:71], v[196:199], a[40:43], v[68:71]
v_accvgpr_read_b32 v235, a172
v_perm_b32 v236, v235, v234, s86
v_mfma_f32_16x16x32_bf16 v[100:103], v[196:199], a[44:47], v[100:103]
ds_read_b64_tr_b16 v[196:197], v241 offset:8448
ds_read_b64_tr_b16 v[198:199], v241 offset:9472
v_mfma_f32_16x16x32_bf16 v[8:11], v[200:203], a[32:35], v[8:11]
v_perm_b32 v237, v235, v234, s87
v_accvgpr_write_b32 a129, v236
v_mfma_f32_16x16x32_bf16 v[40:43], v[200:203], a[36:39], v[40:43]
buffer_load_dwordx4 a[28:31], v245, s[44:47], s55 offen offset:2048 sc1
v_mfma_f32_16x16x32_bf16 v[72:75], v[200:203], a[40:43], v[72:75]
v_accvgpr_write_b32 a133, v237
v_accvgpr_read_b32 v230, a176
v_mfma_f32_16x16x32_bf16 v[104:107], v[200:203], a[44:47], v[104:107]
ds_read_b64_tr_b16 v[200:201], v242 offset:8448
ds_read_b64_tr_b16 v[202:203], v242 offset:9472
v_mfma_f32_16x16x32_bf16 v[12:15], v[204:207], a[32:35], v[12:15]
v_accvgpr_read_b32 v231, a180
v_perm_b32 v232, v231, v230, s86
v_mfma_f32_16x16x32_bf16 v[44:47], v[204:207], a[36:39], v[44:47]
buffer_load_dwordx4 a[56:59], v246, s[44:47], s55 offen offset:2048 sc1
v_mfma_f32_16x16x32_bf16 v[76:79], v[204:207], a[40:43], v[76:79]
s_waitcnt vmcnt(14) lgkmcnt(2)
v_perm_b32 v233, v231, v230, s87
v_mfma_f32_16x16x32_bf16 v[108:111], v[204:207], a[44:47], v[108:111]
ds_read_b64_tr_b16 v[204:205], v243 offset:8448
ds_read_b64_tr_b16 v[206:207], v243 offset:9472
v_mfma_f32_16x16x32_bf16 v[16:19], v[208:211], a[32:35], v[16:19]
v_accvgpr_write_b32 a130, v232
v_accvgpr_write_b32 a134, v233
v_mfma_f32_16x16x32_bf16 v[48:51], v[208:211], a[36:39], v[48:51]
buffer_load_dwordx4 a[60:63], v247, s[44:47], s55 offen offset:2048 sc1
v_mfma_f32_16x16x32_bf16 v[80:83], v[208:211], a[40:43], v[80:83]
v_accvgpr_read_b32 v234, a184
v_accvgpr_read_b32 v235, a188
v_mfma_f32_16x16x32_bf16 v[112:115], v[208:211], a[44:47], v[112:115]
ds_read_b64_tr_b16 v[208:209], v240 offset:8576
ds_read_b64_tr_b16 v[210:211], v240 offset:9600
v_mfma_f32_16x16x32_bf16 v[20:23], v[212:215], a[32:35], v[20:23]
s_add_u32 m0, s83, 20096
v_perm_b32 v236, v235, v234, s86
v_mfma_f32_16x16x32_bf16 v[52:55], v[212:215], a[36:39], v[52:55]
buffer_load_dwordx4 v227, s[56:59], s80 offen lds
v_mfma_f32_16x16x32_bf16 v[84:87], v[212:215], a[40:43], v[84:87]
s_add_u32 s80, s80, 131072
v_perm_b32 v237, v235, v234, s87
v_mfma_f32_16x16x32_bf16 v[116:119], v[212:215], a[44:47], v[116:119]
ds_read_b64_tr_b16 v[212:213], v241 offset:8576
ds_read_b64_tr_b16 v[214:215], v241 offset:9600
v_mfma_f32_16x16x32_bf16 v[24:27], v[216:219], a[32:35], v[24:27]
s_add_u32 m0, s83, 33792
v_accvgpr_write_b32 a131, v236
v_mfma_f32_16x16x32_bf16 v[56:59], v[216:219], a[36:39], v[56:59]
buffer_load_dwordx4 v224, s[56:59], s80 offen lds
v_mfma_f32_16x16x32_bf16 v[88:91], v[216:219], a[40:43], v[88:91]
v_accvgpr_write_b32 a135, v237
v_accvgpr_read_b32 v230, a161
v_mfma_f32_16x16x32_bf16 v[120:123], v[216:219], a[44:47], v[120:123]
ds_read_b64_tr_b16 v[216:217], v242 offset:8576
ds_read_b64_tr_b16 v[218:219], v242 offset:9600
v_mfma_f32_16x16x32_bf16 v[28:31], v[220:223], a[32:35], v[28:31]
v_accvgpr_read_b32 v231, a165
v_perm_b32 v232, v231, v230, s86
v_mfma_f32_16x16x32_bf16 v[60:63], v[220:223], a[36:39], v[60:63]
s_nop 0
v_cmp_eq_u64 vcc, v[250:251], v[252:253]
v_mfma_f32_16x16x32_bf16 v[92:95], v[220:223], a[40:43], v[92:95]
v_perm_b32 v233, v231, v230, s87
v_accvgpr_write_b32 a136, v232
v_mfma_f32_16x16x32_bf16 v[124:127], v[220:223], a[44:47], v[124:127]
ds_read_b64_tr_b16 v[220:221], v243 offset:8576
ds_read_b64_tr_b16 v[222:223], v243 offset:9600
v_mfma_f32_16x16x32_bf16 v[0:3], v[192:195], a[0:3], v[0:3]
s_add_u32 m0, s83, 34816
v_accvgpr_write_b32 a140, v233
v_mfma_f32_16x16x32_bf16 v[32:35], v[192:195], a[4:7], v[32:35]
buffer_load_dwordx4 v225, s[56:59], s80 offen lds
v_mfma_f32_16x16x32_bf16 v[64:67], v[192:195], a[48:51], v[64:67]
v_accvgpr_read_b32 v234, a169
v_accvgpr_read_b32 v235, a173
v_mfma_f32_16x16x32_bf16 v[96:99], v[192:195], a[52:55], v[96:99]
v_perm_b32 v236, v235, v234, s86
v_perm_b32 v237, v235, v234, s87
v_mfma_f32_16x16x32_bf16 v[4:7], v[196:199], a[0:3], v[4:7]
s_add_u32 m0, s83, 35968
s_waitcnt lgkmcnt(0)
s_barrier
v_mfma_f32_16x16x32_bf16 v[36:39], v[196:199], a[4:7], v[36:39]
buffer_load_dwordx4 v226, s[56:59], s80 offen lds
v_mfma_f32_16x16x32_bf16 v[68:71], v[196:199], a[48:51], v[68:71]
ds_read_b64_tr_b16 v[192:193], v240 offset:16896
ds_read_b64_tr_b16 v[194:195], v240 offset:17920
s_nop 0
v_mfma_f32_16x16x32_bf16 v[100:103], v[196:199], a[52:55], v[100:103]
ds_read_b64_tr_b16 v[196:197], v241 offset:16896
ds_read_b64_tr_b16 v[198:199], v241 offset:17920
v_mfma_f32_16x16x32_bf16 v[8:11], v[200:203], a[0:3], v[8:11]
s_lshl_b32 s92, s76, 5
s_add_u32 s92, s92, s72
v_mfma_f32_16x16x32_bf16 v[40:43], v[200:203], a[4:7], v[40:43]
buffer_load_dwordx2 v[250:251], v248, s[64:67], s5 offen sc1
v_mfma_f32_16x16x32_bf16 v[72:75], v[200:203], a[48:51], v[72:75]
s_lshl2_add_u32 s92, s92, s3
s_lshl_b32 s92, s92, 12
v_mfma_f32_16x16x32_bf16 v[104:107], v[200:203], a[52:55], v[104:107]
ds_read_b64_tr_b16 v[200:201], v242 offset:16896
ds_read_b64_tr_b16 v[202:203], v242 offset:17920
v_mfma_f32_16x16x32_bf16 v[12:15], v[204:207], a[0:3], v[12:15]
s_add_u32 m0, s83, 36992
v_accvgpr_write_b32 a137, v236
v_mfma_f32_16x16x32_bf16 v[44:47], v[204:207], a[4:7], v[44:47]
buffer_load_dwordx4 v227, s[56:59], s80 offen lds
v_mfma_f32_16x16x32_bf16 v[76:79], v[204:207], a[48:51], v[76:79]
s_bfe_u32 s80, s4, 262164
s_xor_b32 s80, s80, s72
v_mfma_f32_16x16x32_bf16 v[108:111], v[204:207], a[52:55], v[108:111]
ds_read_b64_tr_b16 v[204:205], v243 offset:16896
ds_read_b64_tr_b16 v[206:207], v243 offset:17920
v_mfma_f32_16x16x32_bf16 v[16:19], v[208:211], a[0:3], v[16:19]
s_lshl_b32 s80, s80, 18
v_accvgpr_write_b32 a141, v237
v_mfma_f32_16x16x32_bf16 v[48:51], v[208:211], a[4:7], v[48:51]
buffer_load_dwordx4 a[32:35], v244, s[44:47], s55 offen offset:3072 sc1
v_mfma_f32_16x16x32_bf16 v[80:83], v[208:211], a[48:51], v[80:83]
s_add_u32 s68, s16, s92
s_addc_u32 s69, s17, 0
v_mfma_f32_16x16x32_bf16 v[112:115], v[208:211], a[52:55], v[112:115]
ds_read_b64_tr_b16 v[208:209], v240 offset:17024
ds_read_b64_tr_b16 v[210:211], v240 offset:18048
v_mfma_f32_16x16x32_bf16 v[20:23], v[212:215], a[0:3], v[20:23]
s_and_b32 s69, s69, 0xffff
s_mov_b32 s70, 4096
v_mfma_f32_16x16x32_bf16 v[52:55], v[212:215], a[4:7], v[52:55]
buffer_load_dwordx4 a[36:39], v245, s[44:47], s55 offen offset:3072 sc1
v_mfma_f32_16x16x32_bf16 v[84:87], v[212:215], a[48:51], v[84:87]
s_mov_b32 s71, 0x20000
v_accvgpr_read_b32 v230, a177
v_mfma_f32_16x16x32_bf16 v[116:119], v[212:215], a[52:55], v[116:119]
ds_read_b64_tr_b16 v[212:213], v241 offset:17024
ds_read_b64_tr_b16 v[214:215], v241 offset:18048
v_mfma_f32_16x16x32_bf16 v[24:27], v[216:219], a[0:3], v[24:27]
s_add_u32 s48, s36, 0
s_addc_u32 s49, s37, 0
v_mfma_f32_16x16x32_bf16 v[56:59], v[216:219], a[4:7], v[56:59]
buffer_load_dwordx4 a[40:43], v246, s[44:47], s55 offen offset:3072 sc1
v_mfma_f32_16x16x32_bf16 v[88:91], v[216:219], a[48:51], v[88:91]
s_and_b32 s49, s49, 0xffff
s_mov_b32 s50, 2048
v_mfma_f32_16x16x32_bf16 v[120:123], v[216:219], a[52:55], v[120:123]
ds_read_b64_tr_b16 v[216:217], v242 offset:17024
ds_read_b64_tr_b16 v[218:219], v242 offset:18048
v_mfma_f32_16x16x32_bf16 v[28:31], v[220:223], a[0:3], v[28:31]
s_bitcmp1_b32 vcc_lo, 4
s_cbranch_scc0 .La5_LO_N4
.La5_LO_K4:
v_mfma_f32_16x16x32_bf16 v[60:63], v[220:223], a[4:7], v[60:63]
buffer_load_dwordx4 a[0:3], v247, s[44:47], s55 offen offset:3072 sc1
v_mfma_f32_16x16x32_bf16 v[92:95], v[220:223], a[48:51], v[92:95]
s_waitcnt vmcnt(21) lgkmcnt(2)
s_bfe_u32 s55, s4, 262172
v_mfma_f32_16x16x32_bf16 v[124:127], v[220:223], a[52:55], v[124:127]
ds_read_b64_tr_b16 v[220:221], v243 offset:17024
ds_read_b64_tr_b16 v[222:223], v243 offset:18048
v_mfma_f32_16x16x32_bf16 v[0:3], v[192:195], a[64:67], v[0:3]
s_xor_b32 s55, s55, s72
s_lshl_b32 s55, s55, 13
v_mfma_f32_16x16x32_bf16 v[32:35], v[192:195], a[68:71], v[32:35]
buffer_load_dwordx4 a[4:7], v244, s[44:47], s52 offen offset:0 sc1
v_mfma_f32_16x16x32_bf16 v[64:67], v[192:195], a[72:75], v[64:67]
s_mov_b32 s51, 0x20000
v_and_b32 v238, 0xf0, v255
v_mfma_f32_16x16x32_bf16 v[96:99], v[192:195], a[8:11], v[96:99]
ds_read_b64_tr_b16 v[192:193], v240 offset:25344
ds_read_b64_tr_b16 v[194:195], v240 offset:26368
v_mfma_f32_16x16x32_bf16 v[4:7], v[196:199], a[64:67], v[4:7]
v_lshlrev_b32 v238, 2, v238
v_bfe_u32 v249, v255, 8, 1
v_mfma_f32_16x16x32_bf16 v[36:39], v[196:199], a[68:71], v[36:39]
buffer_load_dwordx4 a[44:47], v245, s[44:47], s52 offen offset:0 sc1
v_mfma_f32_16x16x32_bf16 v[68:71], v[196:199], a[72:75], v[68:71]
v_lshl_add_u32 v238, v249, 5, v238
v_lshrrev_b32 v249, 9, v255
v_mfma_f32_16x16x32_bf16 v[100:103], v[196:199], a[8:11], v[100:103]
ds_read_b64_tr_b16 v[196:197], v241 offset:25344
ds_read_b64_tr_b16 v[198:199], v241 offset:26368
v_mfma_f32_16x16x32_bf16 v[8:11], v[200:203], a[64:67], v[8:11]
v_lshl_add_u32 v238, v249, 4, v238
s_lshr_b32 s23, s72, 3
v_mfma_f32_16x16x32_bf16 v[40:43], v[200:203], a[68:71], v[40:43]
buffer_load_dwordx4 a[48:51], v246, s[44:47], s52 offen offset:0 sc1
v_mfma_f32_16x16x32_bf16 v[72:75], v[200:203], a[72:75], v[72:75]
s_lshl3_add_u32 s92, s23, s76
s_lshl_b32 s92, s92, 19
v_mfma_f32_16x16x32_bf16 v[104:107], v[200:203], a[8:11], v[104:107]
ds_read_b64_tr_b16 v[200:201], v242 offset:25344
ds_read_b64_tr_b16 v[202:203], v242 offset:26368
v_mfma_f32_16x16x32_bf16 v[12:15], v[204:207], a[64:67], v[12:15]
s_lshl_b32 s97, s3, 16
s_add_u32 s94, s92, s97
v_mfma_f32_16x16x32_bf16 v[44:47], v[204:207], a[68:71], v[44:47]
buffer_load_dwordx4 a[52:55], v247, s[44:47], s52 offen offset:0 sc1
v_mfma_f32_16x16x32_bf16 v[76:79], v[204:207], a[72:75], v[76:79]
s_add_u32 s95, s94, 32768
s_sub_u32 s96, s92, s97
v_mfma_f32_16x16x32_bf16 v[108:111], v[204:207], a[8:11], v[108:111]
ds_read_b64_tr_b16 v[204:205], v243 offset:25344
ds_read_b64_tr_b16 v[206:207], v243 offset:26368
v_mfma_f32_16x16x32_bf16 v[16:19], v[208:211], a[64:67], v[16:19]
s_add_u32 s96, s96, 458752
s_add_u32 s97, s96, 32768
v_mfma_f32_16x16x32_bf16 v[48:51], v[208:211], a[68:71], v[48:51]
buffer_load_dwordx4 v[136:139], v244, s[44:47], s52 offen offset:1024 sc1
v_mfma_f32_16x16x32_bf16 v[80:83], v[208:211], a[72:75], v[80:83]
s_lshl3_add_u32 s92, s23, s101
s_lshl_b32 s85, s76, 14
v_mfma_f32_16x16x32_bf16 v[112:115], v[208:211], a[8:11], v[112:115]
ds_read_b64_tr_b16 v[208:209], v240 offset:25472
ds_read_b64_tr_b16 v[210:211], v240 offset:26496
v_mfma_f32_16x16x32_bf16 v[20:23], v[212:215], a[64:67], v[20:23]
s_lshl_b32 s92, s92, 3
s_add_u32 s92, s92, s85
v_mfma_f32_16x16x32_bf16 v[52:55], v[212:215], a[68:71], v[52:55]
buffer_load_dwordx4 v[140:143], v245, s[44:47], s52 offen offset:1024 sc1
v_mfma_f32_16x16x32_bf16 v[84:87], v[212:215], a[72:75], v[84:87]
s_waitcnt vmcnt(23) lgkmcnt(2)
s_add_u32 s92, s92, 2097152
v_mfma_f32_16x16x32_bf16 v[116:119], v[212:215], a[8:11], v[116:119]
ds_read_b64_tr_b16 v[212:213], v241 offset:25472
ds_read_b64_tr_b16 v[214:215], v241 offset:26496
v_mfma_f32_16x16x32_bf16 v[24:27], v[216:219], a[64:67], v[24:27]
v_accvgpr_read_b32 v231, a181
v_perm_b32 v232, v231, v230, s86
v_mfma_f32_16x16x32_bf16 v[56:59], v[216:219], a[68:71], v[56:59]
buffer_load_dwordx4 v[152:155], v246, s[44:47], s52 offen offset:1024 sc1
v_mfma_f32_16x16x32_bf16 v[88:91], v[216:219], a[72:75], v[88:91]
v_perm_b32 v233, v231, v230, s87
v_accvgpr_write_b32 a138, v232
v_mfma_f32_16x16x32_bf16 v[120:123], v[216:219], a[8:11], v[120:123]
ds_read_b64_tr_b16 v[216:217], v242 offset:25472
ds_read_b64_tr_b16 v[218:219], v242 offset:26496
v_mfma_f32_16x16x32_bf16 v[28:31], v[220:223], a[64:67], v[28:31]
v_accvgpr_write_b32 a142, v233
v_accvgpr_read_b32 v234, a185
v_mfma_f32_16x16x32_bf16 v[60:63], v[220:223], a[68:71], v[60:63]
v_accvgpr_read_b32 v235, a189
v_perm_b32 v236, v235, v234, s86
v_mfma_f32_16x16x32_bf16 v[92:95], v[220:223], a[72:75], v[92:95]
v_perm_b32 v237, v235, v234, s87
v_accvgpr_write_b32 a139, v236
v_mfma_f32_16x16x32_bf16 v[124:127], v[220:223], a[8:11], v[124:127]
ds_read_b64_tr_b16 v[220:221], v243 offset:25472
ds_read_b64_tr_b16 v[222:223], v243 offset:26496
v_mfma_f32_16x16x32_bf16 v[0:3], v[192:195], a[12:15], v[0:3]
v_accvgpr_write_b32 a143, v237
v_accvgpr_read_b32 v230, a162
v_mfma_f32_16x16x32_bf16 v[32:35], v[192:195], a[16:19], v[32:35]
buffer_load_dwordx4 v[156:159], v247, s[44:47], s52 offen offset:1024 sc1
v_mfma_f32_16x16x32_bf16 v[64:67], v[192:195], a[20:23], v[64:67]
v_accvgpr_read_b32 v231, a166
v_perm_b32 v232, v231, v230, s86
v_mfma_f32_16x16x32_bf16 v[96:99], v[192:195], a[76:79], v[96:99]
ds_read_b64_tr_b16 v[192:193], v240 offset:33792
ds_read_b64_tr_b16 v[194:195], v240 offset:34816
v_mfma_f32_16x16x32_bf16 v[4:7], v[196:199], a[12:15], v[4:7]
s_add_u32 m0, s83, 50688
v_perm_b32 v233, v231, v230, s87
v_mfma_f32_16x16x32_bf16 v[36:39], v[196:199], a[16:19], v[36:39]
buffer_load_dwordx4 v224, s[56:59], s80 offen lds
v_mfma_f32_16x16x32_bf16 v[68:71], v[196:199], a[20:23], v[68:71]
.La5_LW3_1:
s_cbranch_execz .La5_LW3_5
v_accvgpr_write_b32 a144, v232
v_accvgpr_write_b32 a148, v233
v_mfma_f32_16x16x32_bf16 v[100:103], v[196:199], a[76:79], v[100:103]
ds_read_b64_tr_b16 v[196:197], v241 offset:33792
ds_read_b64_tr_b16 v[198:199], v241 offset:34816
v_mfma_f32_16x16x32_bf16 v[8:11], v[200:203], a[12:15], v[8:11]
s_add_u32 m0, s83, 51712
v_accvgpr_read_b32 v234, a170
v_mfma_f32_16x16x32_bf16 v[40:43], v[200:203], a[16:19], v[40:43]
buffer_load_dwordx4 v225, s[56:59], s80 offen lds
v_mfma_f32_16x16x32_bf16 v[72:75], v[200:203], a[20:23], v[72:75]
v_accvgpr_read_b32 v235, a174
v_perm_b32 v236, v235, v234, s86
v_mfma_f32_16x16x32_bf16 v[104:107], v[200:203], a[76:79], v[104:107]
ds_read_b64_tr_b16 v[200:201], v242 offset:33792
ds_read_b64_tr_b16 v[202:203], v242 offset:34816
v_mfma_f32_16x16x32_bf16 v[12:15], v[204:207], a[12:15], v[12:15]
s_add_u32 m0, s83, 52864
v_perm_b32 v237, v235, v234, s87
v_mfma_f32_16x16x32_bf16 v[44:47], v[204:207], a[16:19], v[44:47]
buffer_load_dwordx4 v226, s[56:59], s80 offen lds
v_mfma_f32_16x16x32_bf16 v[76:79], v[204:207], a[20:23], v[76:79]
s_waitcnt vmcnt(21) lgkmcnt(2)
v_accvgpr_write_b32 a145, v236
v_mfma_f32_16x16x32_bf16 v[108:111], v[204:207], a[76:79], v[108:111]
ds_read_b64_tr_b16 v[204:205], v243 offset:33792
ds_read_b64_tr_b16 v[206:207], v243 offset:34816
v_mfma_f32_16x16x32_bf16 v[16:19], v[208:211], a[12:15], v[16:19]
v_accvgpr_write_b32 a149, v237
v_accvgpr_read_b32 v230, a178
v_mfma_f32_16x16x32_bf16 v[48:51], v[208:211], a[16:19], v[48:51]
buffer_load_dwordx4 v[168:171], v244, s[44:47], s52 offen offset:2048 sc1
v_mfma_f32_16x16x32_bf16 v[80:83], v[208:211], a[20:23], v[80:83]
v_accvgpr_read_b32 v231, a182
v_perm_b32 v232, v231, v230, s86
v_mfma_f32_16x16x32_bf16 v[112:115], v[208:211], a[76:79], v[112:115]
ds_read_b64_tr_b16 v[208:209], v240 offset:33920
ds_read_b64_tr_b16 v[210:211], v240 offset:34944
v_mfma_f32_16x16x32_bf16 v[20:23], v[212:215], a[12:15], v[20:23]
v_perm_b32 v233, v231, v230, s87
v_accvgpr_write_b32 a146, v232
v_mfma_f32_16x16x32_bf16 v[52:55], v[212:215], a[16:19], v[52:55]
buffer_load_dwordx4 v[172:175], v245, s[44:47], s52 offen offset:2048 sc1
v_mfma_f32_16x16x32_bf16 v[84:87], v[212:215], a[20:23], v[84:87]
v_accvgpr_write_b32 a150, v233
v_accvgpr_read_b32 v234, a186
v_mfma_f32_16x16x32_bf16 v[116:119], v[212:215], a[76:79], v[116:119]
ds_read_b64_tr_b16 v[212:213], v241 offset:33920
ds_read_b64_tr_b16 v[214:215], v241 offset:34944
v_mfma_f32_16x16x32_bf16 v[24:27], v[216:219], a[12:15], v[24:27]
v_accvgpr_read_b32 v235, a190
v_perm_b32 v236, v235, v234, s86
v_mfma_f32_16x16x32_bf16 v[56:59], v[216:219], a[16:19], v[56:59]
buffer_load_dwordx4 v[184:187], v246, s[44:47], s52 offen offset:2048 sc1
v_mfma_f32_16x16x32_bf16 v[88:91], v[216:219], a[20:23], v[88:91]
v_perm_b32 v237, v235, v234, s87
v_accvgpr_write_b32 a147, v236
v_mfma_f32_16x16x32_bf16 v[120:123], v[216:219], a[76:79], v[120:123]
ds_read_b64_tr_b16 v[216:217], v242 offset:33920
ds_read_b64_tr_b16 v[218:219], v242 offset:34944
v_mfma_f32_16x16x32_bf16 v[28:31], v[220:223], a[12:15], v[28:31]
v_accvgpr_write_b32 a151, v237
v_accvgpr_read_b32 v230, a163
v_mfma_f32_16x16x32_bf16 v[60:63], v[220:223], a[16:19], v[60:63]
v_accvgpr_read_b32 v231, a167
v_perm_b32 v232, v231, v230, s86
v_mfma_f32_16x16x32_bf16 v[92:95], v[220:223], a[20:23], v[92:95]
v_perm_b32 v233, v231, v230, s87
v_accvgpr_write_b32 a152, v232
v_mfma_f32_16x16x32_bf16 v[124:127], v[220:223], a[76:79], v[124:127]
ds_read_b64_tr_b16 v[220:221], v243 offset:33920
ds_read_b64_tr_b16 v[222:223], v243 offset:34944
v_mfma_f32_16x16x32_bf16 v[0:3], v[192:195], a[24:27], v[0:3]
v_accvgpr_write_b32 a156, v233
v_accvgpr_read_b32 v234, a171
v_mfma_f32_16x16x32_bf16 v[32:35], v[192:195], a[28:31], v[32:35]
buffer_load_dwordx4 v[188:191], v247, s[44:47], s52 offen offset:2048 sc1
v_mfma_f32_16x16x32_bf16 v[64:67], v[192:195], a[56:59], v[64:67]
v_accvgpr_read_b32 v235, a175
v_perm_b32 v236, v235, v234, s86
v_mfma_f32_16x16x32_bf16 v[96:99], v[192:195], a[60:63], v[96:99]
ds_read_b64_tr_b16 v[192:193], v240 offset:42240
ds_read_b64_tr_b16 v[194:195], v240 offset:43264
v_mfma_f32_16x16x32_bf16 v[4:7], v[196:199], a[24:27], v[4:7]
s_add_u32 m0, s83, 53888
v_perm_b32 v237, v235, v234, s87
v_mfma_f32_16x16x32_bf16 v[36:39], v[196:199], a[28:31], v[36:39]
buffer_load_dwordx4 v227, s[56:59], s80 offen lds
v_mfma_f32_16x16x32_bf16 v[68:71], v[196:199], a[56:59], v[68:71]
s_waitcnt lgkmcnt(2)
s_add_u32 s80, s80, 131072
v_mfma_f32_16x16x32_bf16 v[100:103], v[196:199], a[60:63], v[100:103]
ds_read_b64_tr_b16 v[196:197], v241 offset:42240
ds_read_b64_tr_b16 v[198:199], v241 offset:43264
v_mfma_f32_16x16x32_bf16 v[8:11], v[200:203], a[24:27], v[8:11]
s_add_u32 m0, s83, 0
v_accvgpr_write_b32 a153, v236
v_mfma_f32_16x16x32_bf16 v[40:43], v[200:203], a[28:31], v[40:43]
buffer_load_dwordx4 v224, s[56:59], s80 offen lds
v_mfma_f32_16x16x32_bf16 v[72:75], v[200:203], a[56:59], v[72:75]
v_accvgpr_write_b32 a157, v237
v_accvgpr_read_b32 v230, a179
v_mfma_f32_16x16x32_bf16 v[104:107], v[200:203], a[60:63], v[104:107]
ds_read_b64_tr_b16 v[200:201], v242 offset:42240
ds_read_b64_tr_b16 v[202:203], v242 offset:43264
v_mfma_f32_16x16x32_bf16 v[12:15], v[204:207], a[24:27], v[12:15]
s_add_u32 m0, s83, 1024
v_accvgpr_read_b32 v231, a183
v_mfma_f32_16x16x32_bf16 v[44:47], v[204:207], a[28:31], v[44:47]
buffer_load_dwordx4 v225, s[56:59], s80 offen lds
v_mfma_f32_16x16x32_bf16 v[76:79], v[204:207], a[56:59], v[76:79]
s_waitcnt vmcnt(23)
v_cmp_eq_u64 vcc, v[250:251], v[252:253]
v_mfma_f32_16x16x32_bf16 v[108:111], v[204:207], a[60:63], v[108:111]
ds_read_b64_tr_b16 v[204:205], v243 offset:42240
ds_read_b64_tr_b16 v[206:207], v243 offset:43264
v_mfma_f32_16x16x32_bf16 v[16:19], v[208:211], a[24:27], v[16:19]
s_add_u32 m0, s83, 2176
v_perm_b32 v232, v231, v230, s86
v_mfma_f32_16x16x32_bf16 v[48:51], v[208:211], a[28:31], v[48:51]
buffer_load_dwordx4 v226, s[56:59], s80 offen lds
v_mfma_f32_16x16x32_bf16 v[80:83], v[208:211], a[56:59], v[80:83]
v_perm_b32 v233, v231, v230, s87
v_accvgpr_write_b32 a154, v232
v_mfma_f32_16x16x32_bf16 v[112:115], v[208:211], a[60:63], v[112:115]
ds_read_b64_tr_b16 v[208:209], v240 offset:42368
ds_read_b64_tr_b16 v[210:211], v240 offset:43392
v_mfma_f32_16x16x32_bf16 v[20:23], v[212:215], a[24:27], v[20:23]
s_add_u32 m0, s83, 3200
v_add_u32 v240, 4294899712, v240
v_mfma_f32_16x16x32_bf16 v[52:55], v[212:215], a[28:31], v[52:55]
buffer_load_dwordx4 v227, s[56:59], s80 offen lds
v_mfma_f32_16x16x32_bf16 v[84:87], v[212:215], a[56:59], v[84:87]
s_bfe_u32 s80, s4, 262168
s_xor_b32 s80, s80, s72
v_mfma_f32_16x16x32_bf16 v[116:119], v[212:215], a[60:63], v[116:119]
ds_read_b64_tr_b16 v[212:213], v241 offset:42368
ds_read_b64_tr_b16 v[214:215], v241 offset:43392
v_mfma_f32_16x16x32_bf16 v[24:27], v[216:219], a[24:27], v[24:27]
v_add_u32 v241, 4294899712, v241
s_lshl_b32 s80, s80, 18
v_mfma_f32_16x16x32_bf16 v[56:59], v[216:219], a[28:31], v[56:59]
buffer_load_dwordx2 v[250:251], v248, s[64:67], s5 offen sc1
v_mfma_f32_16x16x32_bf16 v[88:91], v[216:219], a[56:59], v[88:91]
v_accvgpr_write_b32 a158, v233
v_accvgpr_read_b32 v234, a187
v_mfma_f32_16x16x32_bf16 v[120:123], v[216:219], a[60:63], v[120:123]
ds_read_b64_tr_b16 v[216:217], v242 offset:42368
ds_read_b64_tr_b16 v[218:219], v242 offset:43392
v_mfma_f32_16x16x32_bf16 v[28:31], v[220:223], a[24:27], v[28:31]
v_add_u32 v242, 4294899712, v242
v_accvgpr_read_b32 v235, a191
v_mfma_f32_16x16x32_bf16 v[60:63], v[220:223], a[28:31], v[60:63]
buffer_load_dwordx4 a[8:11], v244, s[44:47], s52 offen offset:3072 sc1
v_mfma_f32_16x16x32_bf16 v[92:95], v[220:223], a[56:59], v[92:95]
s_waitcnt vmcnt(22) lgkmcnt(2)
v_perm_b32 v236, v235, v234, s86
v_mfma_f32_16x16x32_bf16 v[124:127], v[220:223], a[60:63], v[124:127]
ds_read_b64_tr_b16 v[220:221], v243 offset:42368
ds_read_b64_tr_b16 v[222:223], v243 offset:43392
v_mfma_f32_16x16x32_bf16 v[0:3], v[192:195], a[32:35], v[0:3]
v_add_u32 v243, 4294899712, v243
v_perm_b32 v237, v235, v234, s87
v_mfma_f32_16x16x32_bf16 v[32:35], v[192:195], a[36:39], v[32:35]
buffer_load_dwordx4 a[12:15], v245, s[44:47], s52 offen offset:3072 sc1
v_mfma_f32_16x16x32_bf16 v[64:67], v[192:195], a[40:43], v[64:67]
v_accvgpr_write_b32 a155, v236
v_accvgpr_write_b32 a159, v237
v_mfma_f32_16x16x32_bf16 v[96:99], v[192:195], a[0:3], v[96:99]
v_accvgpr_read_b32 v230, a192
v_accvgpr_read_b32 v231, a196
v_mfma_f32_16x16x32_bf16 v[4:7], v[196:199], a[32:35], v[4:7]
s_waitcnt lgkmcnt(0)
s_barrier
v_mfma_f32_16x16x32_bf16 v[36:39], v[196:199], a[36:39], v[36:39]
buffer_load_dwordx4 a[16:19], v246, s[44:47], s52 offen offset:3072 sc1
v_mfma_f32_16x16x32_bf16 v[68:71], v[196:199], a[40:43], v[68:71]
ds_read_b64_tr_b16 v[192:193], v240 offset:16896
ds_read_b64_tr_b16 v[194:195], v240 offset:17920
v_mfma_f32_16x16x32_bf16 v[100:103], v[196:199], a[0:3], v[100:103]
ds_read_b64_tr_b16 v[196:197], v241 offset:16896
ds_read_b64_tr_b16 v[198:199], v241 offset:17920
v_mfma_f32_16x16x32_bf16 v[8:11], v[200:203], a[32:35], v[8:11]
s_bitcmp1_b32 vcc_lo, 5
s_nop 0
s_cbranch_scc0 .La5_LO_N5
.La5_LO_K5:
v_mfma_f32_16x16x32_bf16 v[40:43], v[200:203], a[36:39], v[40:43]
buffer_load_dwordx4 a[20:23], v247, s[44:47], s52 offen offset:3072 sc1
v_mfma_f32_16x16x32_bf16 v[72:75], v[200:203], a[40:43], v[72:75]
v_perm_b32 v232, v231, v230, s86
v_perm_b32 v233, v231, v230, s87
v_mfma_f32_16x16x32_bf16 v[104:107], v[200:203], a[0:3], v[104:107]
ds_read_b64_tr_b16 v[200:201], v242 offset:16896
ds_read_b64_tr_b16 v[202:203], v242 offset:17920
v_mfma_f32_16x16x32_bf16 v[12:15], v[204:207], a[32:35], v[12:15]
v_accvgpr_write_b32 a160, v232
v_accvgpr_write_b32 a164, v233
v_mfma_f32_16x16x32_bf16 v[44:47], v[204:207], a[36:39], v[44:47]
buffer_load_dwordx4 a[24:27], v244, s[44:47], s53 offen offset:0 sc1
v_mfma_f32_16x16x32_bf16 v[76:79], v[204:207], a[40:43], v[76:79]
v_accvgpr_read_b32 v234, a200
v_accvgpr_read_b32 v235, a204
v_mfma_f32_16x16x32_bf16 v[108:111], v[204:207], a[0:3], v[108:111]
ds_read_b64_tr_b16 v[204:205], v243 offset:16896
ds_read_b64_tr_b16 v[206:207], v243 offset:17920
v_mfma_f32_16x16x32_bf16 v[16:19], v[208:211], a[32:35], v[16:19]
v_perm_b32 v236, v235, v234, s86
v_perm_b32 v237, v235, v234, s87
v_mfma_f32_16x16x32_bf16 v[48:51], v[208:211], a[36:39], v[48:51]
buffer_load_dwordx4 a[28:31], v245, s[44:47], s53 offen offset:0 sc1
v_mfma_f32_16x16x32_bf16 v[80:83], v[208:211], a[40:43], v[80:83]
v_accvgpr_write_b32 a161, v236
v_accvgpr_write_b32 a165, v237
v_mfma_f32_16x16x32_bf16 v[112:115], v[208:211], a[0:3], v[112:115]
ds_read_b64_tr_b16 v[208:209], v240 offset:17024
ds_read_b64_tr_b16 v[210:211], v240 offset:18048
v_mfma_f32_16x16x32_bf16 v[20:23], v[212:215], a[32:35], v[20:23]
v_accvgpr_read_b32 v230, a208
v_accvgpr_read_b32 v231, a212
v_mfma_f32_16x16x32_bf16 v[52:55], v[212:215], a[36:39], v[52:55]
buffer_load_dwordx4 a[56:59], v246, s[44:47], s53 offen offset:0 sc1
v_mfma_f32_16x16x32_bf16 v[84:87], v[212:215], a[40:43], v[84:87]
s_waitcnt vmcnt(24) lgkmcnt(2)
v_perm_b32 v232, v231, v230, s86
v_mfma_f32_16x16x32_bf16 v[116:119], v[212:215], a[0:3], v[116:119]
ds_read_b64_tr_b16 v[212:213], v241 offset:17024
ds_read_b64_tr_b16 v[214:215], v241 offset:18048
v_mfma_f32_16x16x32_bf16 v[24:27], v[216:219], a[32:35], v[24:27]
v_perm_b32 v233, v231, v230, s87
v_accvgpr_write_b32 a162, v232
v_mfma_f32_16x16x32_bf16 v[56:59], v[216:219], a[36:39], v[56:59]
buffer_load_dwordx4 a[60:63], v247, s[44:47], s53 offen offset:0 sc1
v_mfma_f32_16x16x32_bf16 v[88:91], v[216:219], a[40:43], v[88:91]
v_accvgpr_write_b32 a166, v233
v_accvgpr_read_b32 v234, a216
v_mfma_f32_16x16x32_bf16 v[120:123], v[216:219], a[0:3], v[120:123]
ds_read_b64_tr_b16 v[216:217], v242 offset:17024
ds_read_b64_tr_b16 v[218:219], v242 offset:18048
v_mfma_f32_16x16x32_bf16 v[28:31], v[220:223], a[32:35], v[28:31]
v_accvgpr_read_b32 v235, a220
v_perm_b32 v236, v235, v234, s86
v_mfma_f32_16x16x32_bf16 v[60:63], v[220:223], a[36:39], v[60:63]
v_perm_b32 v237, v235, v234, s87
v_accvgpr_write_b32 a163, v236
v_mfma_f32_16x16x32_bf16 v[92:95], v[220:223], a[40:43], v[92:95]
v_accvgpr_write_b32 a167, v237
v_accvgpr_read_b32 v230, a193
v_mfma_f32_16x16x32_bf16 v[124:127], v[220:223], a[0:3], v[124:127]
ds_read_b64_tr_b16 v[220:221], v243 offset:17024
ds_read_b64_tr_b16 v[222:223], v243 offset:18048
v_mfma_f32_16x16x32_bf16 v[0:3], v[192:195], a[4:7], v[0:3]
v_accvgpr_read_b32 v231, a197
v_perm_b32 v232, v231, v230, s86
v_mfma_f32_16x16x32_bf16 v[32:35], v[192:195], a[44:47], v[32:35]
buffer_load_dwordx4 a[0:3], v244, s[44:47], s53 offen offset:1024 sc1
v_mfma_f32_16x16x32_bf16 v[64:67], v[192:195], a[48:51], v[64:67]
v_perm_b32 v233, v231, v230, s87
v_accvgpr_write_b32 a168, v232
v_mfma_f32_16x16x32_bf16 v[96:99], v[192:195], a[52:55], v[96:99]
ds_read_b64_tr_b16 v[192:193], v240 offset:25344
ds_read_b64_tr_b16 v[194:195], v240 offset:26368
v_mfma_f32_16x16x32_bf16 v[4:7], v[196:199], a[4:7], v[4:7]
v_accvgpr_write_b32 a172, v233
v_accvgpr_read_b32 v234, a201
v_mfma_f32_16x16x32_bf16 v[36:39], v[196:199], a[44:47], v[36:39]
buffer_load_dwordx4 a[32:35], v245, s[44:47], s53 offen offset:1024 sc1
v_mfma_f32_16x16x32_bf16 v[68:71], v[196:199], a[48:51], v[68:71]
v_accvgpr_read_b32 v235, a205
v_perm_b32 v236, v235, v234, s86
v_mfma_f32_16x16x32_bf16 v[100:103], v[196:199], a[52:55], v[100:103]
ds_read_b64_tr_b16 v[196:197], v241 offset:25344
ds_read_b64_tr_b16 v[198:199], v241 offset:26368
v_mfma_f32_16x16x32_bf16 v[8:11], v[200:203], a[4:7], v[8:11]
v_perm_b32 v237, v235, v234, s87
v_accvgpr_write_b32 a169, v236
v_mfma_f32_16x16x32_bf16 v[40:43], v[200:203], a[44:47], v[40:43]
buffer_load_dwordx4 a[36:39], v246, s[44:47], s53 offen offset:1024 sc1
v_mfma_f32_16x16x32_bf16 v[72:75], v[200:203], a[48:51], v[72:75]
v_accvgpr_write_b32 a173, v237
v_accvgpr_read_b32 v230, a209
v_mfma_f32_16x16x32_bf16 v[104:107], v[200:203], a[52:55], v[104:107]
ds_read_b64_tr_b16 v[200:201], v242 offset:25344
ds_read_b64_tr_b16 v[202:203], v242 offset:26368
v_mfma_f32_16x16x32_bf16 v[12:15], v[204:207], a[4:7], v[12:15]
v_accvgpr_read_b32 v231, a213
v_perm_b32 v232, v231, v230, s86
v_mfma_f32_16x16x32_bf16 v[44:47], v[204:207], a[44:47], v[44:47]
buffer_load_dwordx4 a[40:43], v247, s[44:47], s53 offen offset:1024 sc1
v_mfma_f32_16x16x32_bf16 v[76:79], v[204:207], a[48:51], v[76:79]
s_waitcnt vmcnt(25) lgkmcnt(2)
v_perm_b32 v233, v231, v230, s87
v_mfma_f32_16x16x32_bf16 v[108:111], v[204:207], a[52:55], v[108:111]
ds_read_b64_tr_b16 v[204:205], v243 offset:25344
ds_read_b64_tr_b16 v[206:207], v243 offset:26368
v_mfma_f32_16x16x32_bf16 v[16:19], v[208:211], a[4:7], v[16:19]
s_add_u32 m0, s83, 118272
v_accvgpr_write_b32 a170, v232
v_mfma_f32_16x16x32_bf16 v[48:51], v[208:211], a[44:47], v[48:51]
buffer_load_dwordx4 v224, s[56:59], s80 offen lds
v_mfma_f32_16x16x32_bf16 v[80:83], v[208:211], a[48:51], v[80:83]
v_accvgpr_write_b32 a174, v233
v_accvgpr_read_b32 v234, a217
v_mfma_f32_16x16x32_bf16 v[112:115], v[208:211], a[52:55], v[112:115]
ds_read_b64_tr_b16 v[208:209], v240 offset:25472
ds_read_b64_tr_b16 v[210:211], v240 offset:26496
v_mfma_f32_16x16x32_bf16 v[20:23], v[212:215], a[4:7], v[20:23]
s_add_u32 m0, s83, 119296
v_accvgpr_read_b32 v235, a221
v_mfma_f32_16x16x32_bf16 v[52:55], v[212:215], a[44:47], v[52:55]
buffer_load_dwordx4 v225, s[56:59], s80 offen lds
v_mfma_f32_16x16x32_bf16 v[84:87], v[212:215], a[48:51], v[84:87]
v_perm_b32 v236, v235, v234, s86
v_perm_b32 v237, v235, v234, s87
v_mfma_f32_16x16x32_bf16 v[116:119], v[212:215], a[52:55], v[116:119]
ds_read_b64_tr_b16 v[212:213], v241 offset:25472
ds_read_b64_tr_b16 v[214:215], v241 offset:26496
v_mfma_f32_16x16x32_bf16 v[24:27], v[216:219], a[4:7], v[24:27]
s_add_u32 m0, s83, 120448
v_accvgpr_write_b32 a171, v236
v_mfma_f32_16x16x32_bf16 v[56:59], v[216:219], a[44:47], v[56:59]
buffer_load_dwordx4 v226, s[56:59], s80 offen lds
v_mfma_f32_16x16x32_bf16 v[88:91], v[216:219], a[48:51], v[88:91]
v_accvgpr_write_b32 a175, v237
v_accvgpr_read_b32 v230, a194
v_mfma_f32_16x16x32_bf16 v[120:123], v[216:219], a[52:55], v[120:123]
ds_read_b64_tr_b16 v[216:217], v242 offset:25472
ds_read_b64_tr_b16 v[218:219], v242 offset:26496
v_mfma_f32_16x16x32_bf16 v[28:31], v[220:223], a[4:7], v[28:31]
v_accvgpr_read_b32 v231, a198
v_perm_b32 v232, v231, v230, s86
v_mfma_f32_16x16x32_bf16 v[60:63], v[220:223], a[44:47], v[60:63]
v_perm_b32 v233, v231, v230, s87
v_accvgpr_write_b32 a176, v232
v_mfma_f32_16x16x32_bf16 v[92:95], v[220:223], a[48:51], v[92:95]
v_accvgpr_write_b32 a180, v233
v_accvgpr_read_b32 v234, a202
v_mfma_f32_16x16x32_bf16 v[124:127], v[220:223], a[52:55], v[124:127]
ds_read_b64_tr_b16 v[220:221], v243 offset:25472
ds_read_b64_tr_b16 v[222:223], v243 offset:26496
v_mfma_f32_16x16x32_bf16 v[0:3], v[192:195], v[136:139], v[0:3]
v_accvgpr_read_b32 v235, a206
v_perm_b32 v236, v235, v234, s86
v_mfma_f32_16x16x32_bf16 v[32:35], v[192:195], v[140:143], v[32:35]
buffer_load_dwordx4 a[4:7], v244, s[44:47], s53 offen offset:2048 sc1
v_mfma_f32_16x16x32_bf16 v[64:67], v[192:195], v[152:155], v[64:67]
v_perm_b32 v237, v235, v234, s87
v_accvgpr_write_b32 a177, v236
v_mfma_f32_16x16x32_bf16 v[96:99], v[192:195], v[156:159], v[96:99]
ds_read_b64_tr_b16 v[192:193], v240 offset:33792
ds_read_b64_tr_b16 v[194:195], v240 offset:34816
v_mfma_f32_16x16x32_bf16 v[4:7], v[196:199], v[136:139], v[4:7]
v_accvgpr_write_b32 a181, v237
v_accvgpr_read_b32 v230, a210
v_mfma_f32_16x16x32_bf16 v[36:39], v[196:199], v[140:143], v[36:39]
buffer_load_dwordx4 a[44:47], v245, s[44:47], s53 offen offset:2048 sc1
v_mfma_f32_16x16x32_bf16 v[68:71], v[196:199], v[152:155], v[68:71]
s_waitcnt lgkmcnt(2)
v_accvgpr_read_b32 v231, a214
v_mfma_f32_16x16x32_bf16 v[100:103], v[196:199], v[156:159], v[100:103]
ds_read_b64_tr_b16 v[196:197], v241 offset:33792
ds_read_b64_tr_b16 v[198:199], v241 offset:34816
v_mfma_f32_16x16x32_bf16 v[8:11], v[200:203], v[136:139], v[8:11]
v_perm_b32 v232, v231, v230, s86
v_perm_b32 v233, v231, v230, s87
v_mfma_f32_16x16x32_bf16 v[40:43], v[200:203], v[140:143], v[40:43]
buffer_load_dwordx4 a[48:51], v246, s[44:47], s53 offen offset:2048 sc1
v_mfma_f32_16x16x32_bf16 v[72:75], v[200:203], v[152:155], v[72:75]
v_accvgpr_write_b32 a178, v232
v_accvgpr_write_b32 a182, v233
v_mfma_f32_16x16x32_bf16 v[104:107], v[200:203], v[156:159], v[104:107]
ds_read_b64_tr_b16 v[200:201], v242 offset:33792
ds_read_b64_tr_b16 v[202:203], v242 offset:34816
v_mfma_f32_16x16x32_bf16 v[12:15], v[204:207], v[136:139], v[12:15]
v_accvgpr_read_b32 v234, a218
v_accvgpr_read_b32 v235, a222
v_mfma_f32_16x16x32_bf16 v[44:47], v[204:207], v[140:143], v[44:47]
buffer_load_dwordx4 a[52:55], v247, s[44:47], s53 offen offset:2048 sc1
v_mfma_f32_16x16x32_bf16 v[76:79], v[204:207], v[152:155], v[76:79]
v_perm_b32 v236, v235, v234, s86
v_perm_b32 v237, v235, v234, s87
v_mfma_f32_16x16x32_bf16 v[108:111], v[204:207], v[156:159], v[108:111]
ds_read_b64_tr_b16 v[204:205], v243 offset:33792
ds_read_b64_tr_b16 v[206:207], v243 offset:34816
v_mfma_f32_16x16x32_bf16 v[16:19], v[208:211], v[136:139], v[16:19]
s_add_u32 m0, s83, 121472
v_accvgpr_write_b32 a179, v236
v_mfma_f32_16x16x32_bf16 v[48:51], v[208:211], v[140:143], v[48:51]
buffer_load_dwordx4 v227, s[56:59], s80 offen lds
v_mfma_f32_16x16x32_bf16 v[80:83], v[208:211], v[152:155], v[80:83]
s_add_u32 s80, s80, 131072
v_accvgpr_write_b32 a183, v237
v_mfma_f32_16x16x32_bf16 v[112:115], v[208:211], v[156:159], v[112:115]
ds_read_b64_tr_b16 v[208:209], v240 offset:33920
ds_read_b64_tr_b16 v[210:211], v240 offset:34944
v_mfma_f32_16x16x32_bf16 v[20:23], v[212:215], v[136:139], v[20:23]
s_add_u32 m0, s83, 67584
v_accvgpr_read_b32 v230, a195
v_mfma_f32_16x16x32_bf16 v[52:55], v[212:215], v[140:143], v[52:55]
buffer_load_dwordx4 v224, s[56:59], s80 offen lds
v_mfma_f32_16x16x32_bf16 v[84:87], v[212:215], v[152:155], v[84:87]
v_accvgpr_read_b32 v231, a199
v_perm_b32 v232, v231, v230, s86
v_mfma_f32_16x16x32_bf16 v[116:119], v[212:215], v[156:159], v[116:119]
ds_read_b64_tr_b16 v[212:213], v241 offset:33920
ds_read_b64_tr_b16 v[214:215], v241 offset:34944
v_mfma_f32_16x16x32_bf16 v[24:27], v[216:219], v[136:139], v[24:27]
s_add_u32 m0, s83, 68608
v_perm_b32 v233, v231, v230, s87
v_mfma_f32_16x16x32_bf16 v[56:59], v[216:219], v[140:143], v[56:59]
buffer_load_dwordx4 v225, s[56:59], s80 offen lds
v_mfma_f32_16x16x32_bf16 v[88:91], v[216:219], v[152:155], v[88:91]
v_accvgpr_write_b32 a184, v232
v_accvgpr_write_b32 a188, v233
v_mfma_f32_16x16x32_bf16 v[120:123], v[216:219], v[156:159], v[120:123]
ds_read_b64_tr_b16 v[216:217], v242 offset:33920
ds_read_b64_tr_b16 v[218:219], v242 offset:34944
v_mfma_f32_16x16x32_bf16 v[28:31], v[220:223], v[136:139], v[28:31]
v_accvgpr_read_b32 v234, a203
v_accvgpr_read_b32 v235, a207
v_mfma_f32_16x16x32_bf16 v[60:63], v[220:223], v[140:143], v[60:63]
s_waitcnt vmcnt(22)
v_cmp_eq_u64 vcc, v[250:251], v[252:253]
v_mfma_f32_16x16x32_bf16 v[92:95], v[220:223], v[152:155], v[92:95]
s_waitcnt lgkmcnt(2)
v_perm_b32 v236, v235, v234, s86
v_mfma_f32_16x16x32_bf16 v[124:127], v[220:223], v[156:159], v[124:127]
ds_read_b64_tr_b16 v[220:221], v243 offset:33920
ds_read_b64_tr_b16 v[222:223], v243 offset:34944
v_mfma_f32_16x16x32_bf16 v[0:3], v[192:195], v[168:171], v[0:3]
s_add_u32 m0, s83, 69760
v_perm_b32 v237, v235, v234, s87
v_mfma_f32_16x16x32_bf16 v[32:35], v[192:195], v[172:175], v[32:35]
buffer_load_dwordx4 v226, s[56:59], s80 offen lds
v_mfma_f32_16x16x32_bf16 v[64:67], v[192:195], v[184:187], v[64:67]
v_accvgpr_write_b32 a185, v236
v_accvgpr_write_b32 a189, v237
v_mfma_f32_16x16x32_bf16 v[96:99], v[192:195], v[188:191], v[96:99]
ds_read_b64_tr_b16 v[192:193], v240 offset:42240
ds_read_b64_tr_b16 v[194:195], v240 offset:43264
v_mfma_f32_16x16x32_bf16 v[4:7], v[196:199], v[168:171], v[4:7]
s_add_u32 m0, s83, 70784
v_accvgpr_read_b32 v230, a211
v_mfma_f32_16x16x32_bf16 v[36:39], v[196:199], v[172:175], v[36:39]
buffer_load_dwordx4 v227, s[56:59], s80 offen lds
v_mfma_f32_16x16x32_bf16 v[68:71], v[196:199], v[184:187], v[68:71]
s_bfe_u32 s80, s4, 262172
s_xor_b32 s80, s80, s72
v_mfma_f32_16x16x32_bf16 v[100:103], v[196:199], v[188:191], v[100:103]
ds_read_b64_tr_b16 v[196:197], v241 offset:42240
ds_read_b64_tr_b16 v[198:199], v241 offset:43264
v_mfma_f32_16x16x32_bf16 v[8:11], v[200:203], v[168:171], v[8:11]
s_lshl_b32 s80, s80, 18
v_accvgpr_read_b32 v231, a215
v_mfma_f32_16x16x32_bf16 v[40:43], v[200:203], v[172:175], v[40:43]
buffer_load_dwordx2 v[250:251], v248, s[64:67], s5 offen sc1
v_mfma_f32_16x16x32_bf16 v[72:75], v[200:203], v[184:187], v[72:75]
v_perm_b32 v232, v231, v230, s86
v_perm_b32 v233, v231, v230, s87
v_mfma_f32_16x16x32_bf16 v[104:107], v[200:203], v[188:191], v[104:107]
ds_read_b64_tr_b16 v[200:201], v242 offset:42240
ds_read_b64_tr_b16 v[202:203], v242 offset:43264
v_mfma_f32_16x16x32_bf16 v[12:15], v[204:207], v[168:171], v[12:15]
s_add_u32 m0, s83, 84480
v_accvgpr_write_b32 a186, v232
v_mfma_f32_16x16x32_bf16 v[44:47], v[204:207], v[172:175], v[44:47]
buffer_load_dwordx4 v224, s[56:59], s80 offen lds
v_mfma_f32_16x16x32_bf16 v[76:79], v[204:207], v[184:187], v[76:79]
v_accvgpr_write_b32 a190, v233
v_accvgpr_read_b32 v234, a219
v_mfma_f32_16x16x32_bf16 v[108:111], v[204:207], v[188:191], v[108:111]
ds_read_b64_tr_b16 v[204:205], v243 offset:42240
ds_read_b64_tr_b16 v[206:207], v243 offset:43264
v_mfma_f32_16x16x32_bf16 v[16:19], v[208:211], v[168:171], v[16:19]
s_add_u32 m0, s83, 85504
v_accvgpr_read_b32 v235, a223
v_mfma_f32_16x16x32_bf16 v[48:51], v[208:211], v[172:175], v[48:51]
buffer_load_dwordx4 v225, s[56:59], s80 offen lds
v_mfma_f32_16x16x32_bf16 v[80:83], v[208:211], v[184:187], v[80:83]
v_perm_b32 v236, v235, v234, s86
v_perm_b32 v237, v235, v234, s87
v_mfma_f32_16x16x32_bf16 v[112:115], v[208:211], v[188:191], v[112:115]
ds_read_b64_tr_b16 v[208:209], v240 offset:42368
ds_read_b64_tr_b16 v[210:211], v240 offset:43392
v_mfma_f32_16x16x32_bf16 v[20:23], v[212:215], v[168:171], v[20:23]
s_add_u32 m0, s83, 86656
v_accvgpr_write_b32 a187, v236
v_mfma_f32_16x16x32_bf16 v[52:55], v[212:215], v[172:175], v[52:55]
buffer_load_dwordx4 v226, s[56:59], s80 offen lds
v_mfma_f32_16x16x32_bf16 v[84:87], v[212:215], v[184:187], v[84:87]
s_waitcnt vmcnt(24) lgkmcnt(2)
v_accvgpr_write_b32 a191, v237
v_mfma_f32_16x16x32_bf16 v[116:119], v[212:215], v[188:191], v[116:119]
ds_read_b64_tr_b16 v[212:213], v241 offset:42368
ds_read_b64_tr_b16 v[214:215], v241 offset:43392
v_mfma_f32_16x16x32_bf16 v[24:27], v[216:219], v[168:171], v[24:27]
s_add_u32 m0, s83, 87680
v_accvgpr_read_b32 v230, a224
v_mfma_f32_16x16x32_bf16 v[56:59], v[216:219], v[172:175], v[56:59]
buffer_load_dwordx4 v227, s[56:59], s80 offen lds
v_mfma_f32_16x16x32_bf16 v[88:91], v[216:219], v[184:187], v[88:91]
s_add_u32 s80, s80, 131072
v_accvgpr_read_b32 v231, a228
v_mfma_f32_16x16x32_bf16 v[120:123], v[216:219], v[188:191], v[120:123]
ds_read_b64_tr_b16 v[216:217], v242 offset:42368
ds_read_b64_tr_b16 v[218:219], v242 offset:43392
v_mfma_f32_16x16x32_bf16 v[28:31], v[220:223], v[168:171], v[28:31]
s_add_u32 m0, s83, 101376
v_perm_b32 v232, v231, v230, s86
v_mfma_f32_16x16x32_bf16 v[60:63], v[220:223], v[172:175], v[60:63]
buffer_load_dwordx4 v224, s[56:59], s80 offen lds
v_mfma_f32_16x16x32_bf16 v[92:95], v[220:223], v[184:187], v[92:95]
v_perm_b32 v233, v231, v230, s87
v_accvgpr_write_b32 a192, v232
v_mfma_f32_16x16x32_bf16 v[124:127], v[220:223], v[188:191], v[124:127]
ds_read_b64_tr_b16 v[220:221], v243 offset:42368
ds_read_b64_tr_b16 v[222:223], v243 offset:43392
v_mfma_f32_16x16x32_bf16 v[0:3], v[192:195], a[8:11], v[0:3]
s_add_u32 m0, s83, 102400
v_accvgpr_write_b32 a196, v233
v_mfma_f32_16x16x32_bf16 v[32:35], v[192:195], a[12:15], v[32:35]
buffer_load_dwordx4 v225, s[56:59], s80 offen lds
v_mfma_f32_16x16x32_bf16 v[64:67], v[192:195], a[16:19], v[64:67]
.La5_LW3_2:
s_cbranch_execz .La5_LW3_R0
v_accvgpr_read_b32 v234, a232
v_accvgpr_read_b32 v235, a236
v_mfma_f32_16x16x32_bf16 v[96:99], v[192:195], a[20:23], v[96:99]
v_perm_b32 v236, v235, v234, s86
v_perm_b32 v237, v235, v234, s87
v_mfma_f32_16x16x32_bf16 v[4:7], v[196:199], a[8:11], v[4:7]
s_add_u32 m0, s83, 103552
s_waitcnt lgkmcnt(0)
s_barrier
v_mfma_f32_16x16x32_bf16 v[36:39], v[196:199], a[12:15], v[36:39]
buffer_load_dwordx4 v226, s[56:59], s80 offen lds
v_mfma_f32_16x16x32_bf16 v[68:71], v[196:199], a[16:19], v[68:71]
ds_read_b64_tr_b16 v[192:193], v240 offset:50688
ds_read_b64_tr_b16 v[194:195], v240 offset:51712
v_mfma_f32_16x16x32_bf16 v[100:103], v[196:199], a[20:23], v[100:103]
ds_read_b64_tr_b16 v[196:197], v241 offset:50688
ds_read_b64_tr_b16 v[198:199], v241 offset:51712
v_mfma_f32_16x16x32_bf16 v[8:11], v[200:203], a[8:11], v[8:11]
s_add_u32 m0, s83, 104576
v_accvgpr_write_b32 a193, v236
v_mfma_f32_16x16x32_bf16 v[40:43], v[200:203], a[12:15], v[40:43]
buffer_load_dwordx4 v227, s[56:59], s80 offen lds
v_mfma_f32_16x16x32_bf16 v[72:75], v[200:203], a[16:19], v[72:75]
v_bfe_u32 v224, v255, 6, 1
v_lshrrev_b32 v225, 7, v255
v_mfma_f32_16x16x32_bf16 v[104:107], v[200:203], a[20:23], v[104:107]
ds_read_b64_tr_b16 v[200:201], v242 offset:50688
ds_read_b64_tr_b16 v[202:203], v242 offset:51712
v_mfma_f32_16x16x32_bf16 v[12:15], v[204:207], a[8:11], v[12:15]
v_and_b32_e64 v226, 48, v255
v_lshl_add_u32 v225, v225, 6, v226
v_mfma_f32_16x16x32_bf16 v[44:47], v[204:207], a[12:15], v[44:47]
buffer_load_dwordx4 v[136:139], v244, s[44:47], s53 offen offset:3072 sc1
v_mfma_f32_16x16x32_bf16 v[76:79], v[204:207], a[16:19], v[76:79]
s_waitcnt vmcnt(26) lgkmcnt(2)
v_lshl_add_u32 v224, v224, 11, v225
v_mfma_f32_16x16x32_bf16 v[108:111], v[204:207], a[20:23], v[108:111]
ds_read_b64_tr_b16 v[204:205], v243 offset:50688
ds_read_b64_tr_b16 v[206:207], v243 offset:51712
v_mfma_f32_16x16x32_bf16 v[16:19], v[208:211], a[8:11], v[16:19]
v_accvgpr_write_b32 a197, v237
v_accvgpr_read_b32 v230, a240
v_mfma_f32_16x16x32_bf16 v[48:51], v[208:211], a[12:15], v[48:51]
buffer_load_dwordx4 v[140:143], v245, s[44:47], s53 offen offset:3072 sc1
v_mfma_f32_16x16x32_bf16 v[80:83], v[208:211], a[16:19], v[80:83]
v_accvgpr_read_b32 v231, a244
v_perm_b32 v232, v231, v230, s86
v_mfma_f32_16x16x32_bf16 v[112:115], v[208:211], a[20:23], v[112:115]
ds_read_b64_tr_b16 v[208:209], v240 offset:50816
ds_read_b64_tr_b16 v[210:211], v240 offset:51840
v_mfma_f32_16x16x32_bf16 v[20:23], v[212:215], a[8:11], v[20:23]
v_perm_b32 v233, v231, v230, s87
v_accvgpr_write_b32 a194, v232
v_mfma_f32_16x16x32_bf16 v[52:55], v[212:215], a[12:15], v[52:55]
buffer_load_dwordx4 v[152:155], v246, s[44:47], s53 offen offset:3072 sc1
v_mfma_f32_16x16x32_bf16 v[84:87], v[212:215], a[16:19], v[84:87]
v_accvgpr_write_b32 a198, v233
v_accvgpr_read_b32 v234, a248
v_mfma_f32_16x16x32_bf16 v[116:119], v[212:215], a[20:23], v[116:119]
ds_read_b64_tr_b16 v[212:213], v241 offset:50816
ds_read_b64_tr_b16 v[214:215], v241 offset:51840
v_mfma_f32_16x16x32_bf16 v[24:27], v[216:219], a[8:11], v[24:27]
v_accvgpr_read_b32 v235, a252
v_perm_b32 v236, v235, v234, s86
v_mfma_f32_16x16x32_bf16 v[56:59], v[216:219], a[12:15], v[56:59]
buffer_load_dwordx4 v[156:159], v247, s[44:47], s53 offen offset:3072 sc1
v_mfma_f32_16x16x32_bf16 v[88:91], v[216:219], a[16:19], v[88:91]
v_perm_b32 v237, v235, v234, s87
v_accvgpr_write_b32 a195, v236
v_mfma_f32_16x16x32_bf16 v[120:123], v[216:219], a[20:23], v[120:123]
ds_read_b64_tr_b16 v[216:217], v242 offset:50816
ds_read_b64_tr_b16 v[218:219], v242 offset:51840
v_mfma_f32_16x16x32_bf16 v[28:31], v[220:223], a[8:11], v[28:31]
s_bitcmp1_b32 vcc_lo, 6
s_cbranch_scc0 .La5_LO_N6
.La5_LO_K6:
v_mfma_f32_16x16x32_bf16 v[60:63], v[220:223], a[12:15], v[60:63]
v_accvgpr_write_b32 a199, v237
v_accvgpr_read_b32 v230, a225
v_mfma_f32_16x16x32_bf16 v[92:95], v[220:223], a[16:19], v[92:95]
v_accvgpr_read_b32 v231, a229
v_perm_b32 v232, v231, v230, s86
v_mfma_f32_16x16x32_bf16 v[124:127], v[220:223], a[20:23], v[124:127]
ds_read_b64_tr_b16 v[220:221], v243 offset:50816
ds_read_b64_tr_b16 v[222:223], v243 offset:51840
v_mfma_f32_16x16x32_bf16 v[0:3], v[192:195], a[24:27], v[0:3]
v_perm_b32 v233, v231, v230, s87
v_accvgpr_write_b32 a200, v232
v_mfma_f32_16x16x32_bf16 v[32:35], v[192:195], a[28:31], v[32:35]
buffer_load_dwordx4 a[64:67], v244, s[44:47], s54 offen offset:0 sc1
v_mfma_f32_16x16x32_bf16 v[64:67], v[192:195], a[56:59], v[64:67]
v_accvgpr_write_b32 a204, v233
v_accvgpr_read_b32 v234, a233
v_mfma_f32_16x16x32_bf16 v[96:99], v[192:195], a[60:63], v[96:99]
ds_read_b64_tr_b16 v[192:193], v240 offset:59136
ds_read_b64_tr_b16 v[194:195], v240 offset:60160
v_mfma_f32_16x16x32_bf16 v[4:7], v[196:199], a[24:27], v[4:7]
v_accvgpr_read_b32 v235, a237
v_perm_b32 v236, v235, v234, s86
v_mfma_f32_16x16x32_bf16 v[36:39], v[196:199], a[28:31], v[36:39]
buffer_load_dwordx4 a[68:71], v245, s[44:47], s54 offen offset:0 sc1
v_mfma_f32_16x16x32_bf16 v[68:71], v[196:199], a[56:59], v[68:71]
s_waitcnt lgkmcnt(2)
v_perm_b32 v237, v235, v234, s87
v_mfma_f32_16x16x32_bf16 v[100:103], v[196:199], a[60:63], v[100:103]
ds_read_b64_tr_b16 v[196:197], v241 offset:59136
ds_read_b64_tr_b16 v[198:199], v241 offset:60160
v_mfma_f32_16x16x32_bf16 v[8:11], v[200:203], a[24:27], v[8:11]
v_accvgpr_write_b32 a201, v236
v_accvgpr_write_b32 a205, v237
v_mfma_f32_16x16x32_bf16 v[40:43], v[200:203], a[28:31], v[40:43]
buffer_load_dwordx4 a[72:75], v244, s[44:47], s54 offen offset:1024 sc1
v_mfma_f32_16x16x32_bf16 v[72:75], v[200:203], a[56:59], v[72:75]
v_accvgpr_read_b32 v230, a241
v_accvgpr_read_b32 v231, a245
v_mfma_f32_16x16x32_bf16 v[104:107], v[200:203], a[60:63], v[104:107]
ds_read_b64_tr_b16 v[200:201], v242 offset:59136
ds_read_b64_tr_b16 v[202:203], v242 offset:60160
v_mfma_f32_16x16x32_bf16 v[12:15], v[204:207], a[24:27], v[12:15]
v_perm_b32 v232, v231, v230, s86
v_perm_b32 v233, v231, v230, s87
v_mfma_f32_16x16x32_bf16 v[44:47], v[204:207], a[28:31], v[44:47]
buffer_load_dwordx4 a[76:79], v245, s[44:47], s54 offen offset:1024 sc1
v_mfma_f32_16x16x32_bf16 v[76:79], v[204:207], a[56:59], v[76:79]
v_accvgpr_write_b32 a202, v232
v_accvgpr_write_b32 a206, v233
v_mfma_f32_16x16x32_bf16 v[108:111], v[204:207], a[60:63], v[108:111]
ds_read_b64_tr_b16 v[204:205], v243 offset:59136
ds_read_b64_tr_b16 v[206:207], v243 offset:60160
v_mfma_f32_16x16x32_bf16 v[16:19], v[208:211], a[24:27], v[16:19]
v_accvgpr_read_b32 v234, a249
v_accvgpr_read_b32 v235, a253
v_mfma_f32_16x16x32_bf16 v[48:51], v[208:211], a[28:31], v[48:51]
buffer_load_dwordx4 a[80:83], v244, s[44:47], s54 offen offset:2048 sc1
v_mfma_f32_16x16x32_bf16 v[80:83], v[208:211], a[56:59], v[80:83]
v_perm_b32 v236, v235, v234, s86
v_perm_b32 v237, v235, v234, s87
v_mfma_f32_16x16x32_bf16 v[112:115], v[208:211], a[60:63], v[112:115]
ds_read_b64_tr_b16 v[208:209], v240 offset:59264
ds_read_b64_tr_b16 v[210:211], v240 offset:60288
v_mfma_f32_16x16x32_bf16 v[20:23], v[212:215], a[24:27], v[20:23]
v_accvgpr_write_b32 a203, v236
v_accvgpr_write_b32 a207, v237
v_mfma_f32_16x16x32_bf16 v[52:55], v[212:215], a[28:31], v[52:55]
buffer_load_dwordx4 a[84:87], v245, s[44:47], s54 offen offset:2048 sc1
v_mfma_f32_16x16x32_bf16 v[84:87], v[212:215], a[56:59], v[84:87]
v_accvgpr_read_b32 v230, a226
v_accvgpr_read_b32 v231, a230
v_mfma_f32_16x16x32_bf16 v[116:119], v[212:215], a[60:63], v[116:119]
ds_read_b64_tr_b16 v[212:213], v241 offset:59264
ds_read_b64_tr_b16 v[214:215], v241 offset:60288
v_mfma_f32_16x16x32_bf16 v[24:27], v[216:219], a[24:27], v[24:27]
v_perm_b32 v232, v231, v230, s86
v_perm_b32 v233, v231, v230, s87
v_mfma_f32_16x16x32_bf16 v[56:59], v[216:219], a[28:31], v[56:59]
buffer_load_dwordx4 a[88:91], v244, s[44:47], s54 offen offset:3072 sc1
v_mfma_f32_16x16x32_bf16 v[88:91], v[216:219], a[56:59], v[88:91]
v_accvgpr_write_b32 a208, v232
v_accvgpr_write_b32 a212, v233
v_mfma_f32_16x16x32_bf16 v[120:123], v[216:219], a[60:63], v[120:123]
ds_read_b64_tr_b16 v[216:217], v242 offset:59264
ds_read_b64_tr_b16 v[218:219], v242 offset:60288
v_mfma_f32_16x16x32_bf16 v[28:31], v[220:223], a[24:27], v[28:31]
v_accvgpr_read_b32 v234, a234
v_accvgpr_read_b32 v235, a238
v_mfma_f32_16x16x32_bf16 v[60:63], v[220:223], a[28:31], v[60:63]
v_perm_b32 v236, v235, v234, s86
v_perm_b32 v237, v235, v234, s87
v_mfma_f32_16x16x32_bf16 v[92:95], v[220:223], a[56:59], v[92:95]
s_waitcnt vmcnt(32) lgkmcnt(2)
v_accvgpr_write_b32 a209, v236
v_mfma_f32_16x16x32_bf16 v[124:127], v[220:223], a[60:63], v[124:127]
ds_read_b64_tr_b16 v[220:221], v243 offset:59264
ds_read_b64_tr_b16 v[222:223], v243 offset:60288
v_mfma_f32_16x16x32_bf16 v[0:3], v[192:195], a[0:3], v[0:3]
s_bitcmp1_b32 vcc_lo, 7
s_nop 0
s_nop 0
s_cbranch_scc0 .La5_LO_N7
.La5_LO_K7:
v_mfma_f32_16x16x32_bf16 v[32:35], v[192:195], a[32:35], v[32:35]
buffer_load_dwordx4 a[92:95], v245, s[44:47], s54 offen offset:3072 sc1
v_mfma_f32_16x16x32_bf16 v[64:67], v[192:195], a[36:39], v[64:67]
v_accvgpr_write_b32 a213, v237
v_accvgpr_read_b32 v230, a242
v_mfma_f32_16x16x32_bf16 v[96:99], v[192:195], a[40:43], v[96:99]
ds_read_b64_tr_b16 v[192:193], v240 offset:0
ds_read_b64_tr_b16 v[194:195], v240 offset:1024
v_mfma_f32_16x16x32_bf16 v[4:7], v[196:199], a[0:3], v[4:7]
v_accvgpr_read_b32 v231, a246
v_perm_b32 v232, v231, v230, s86
v_mfma_f32_16x16x32_bf16 v[36:39], v[196:199], a[32:35], v[36:39]
buffer_load_dwordx4 a[8:11], v244, s[44:47], s55 offen offset:0 sc1
v_mfma_f32_16x16x32_bf16 v[68:71], v[196:199], a[36:39], v[68:71]
v_perm_b32 v233, v231, v230, s87
v_accvgpr_write_b32 a210, v232
v_mfma_f32_16x16x32_bf16 v[100:103], v[196:199], a[40:43], v[100:103]
ds_read_b64_tr_b16 v[196:197], v241 offset:0
ds_read_b64_tr_b16 v[198:199], v241 offset:1024
v_mfma_f32_16x16x32_bf16 v[8:11], v[200:203], a[0:3], v[8:11]
v_accvgpr_write_b32 a214, v233
v_accvgpr_read_b32 v234, a250
v_mfma_f32_16x16x32_bf16 v[40:43], v[200:203], a[32:35], v[40:43]
buffer_load_dwordx4 a[12:15], v245, s[44:47], s55 offen offset:0 sc1
v_mfma_f32_16x16x32_bf16 v[72:75], v[200:203], a[36:39], v[72:75]
v_accvgpr_read_b32 v235, a254
v_perm_b32 v236, v235, v234, s86
v_mfma_f32_16x16x32_bf16 v[104:107], v[200:203], a[40:43], v[104:107]
ds_read_b64_tr_b16 v[200:201], v242 offset:0
ds_read_b64_tr_b16 v[202:203], v242 offset:1024
v_mfma_f32_16x16x32_bf16 v[12:15], v[204:207], a[0:3], v[12:15]
v_perm_b32 v237, v235, v234, s87
v_accvgpr_write_b32 a211, v236
v_mfma_f32_16x16x32_bf16 v[44:47], v[204:207], a[32:35], v[44:47]
buffer_load_dwordx4 a[16:19], v244, s[44:47], s55 offen offset:1024 sc1
v_mfma_f32_16x16x32_bf16 v[76:79], v[204:207], a[36:39], v[76:79]
s_waitcnt vmcnt(23)
v_cmp_eq_u64 vcc, v[250:251], v[252:253]
v_mfma_f32_16x16x32_bf16 v[108:111], v[204:207], a[40:43], v[108:111]
ds_read_b64_tr_b16 v[204:205], v243 offset:0
ds_read_b64_tr_b16 v[206:207], v243 offset:1024
v_mfma_f32_16x16x32_bf16 v[16:19], v[208:211], a[0:3], v[16:19]
v_accvgpr_write_b32 a215, v237
v_accvgpr_read_b32 v230, a227
v_mfma_f32_16x16x32_bf16 v[48:51], v[208:211], a[32:35], v[48:51]
buffer_load_dwordx4 a[20:23], v245, s[44:47], s55 offen offset:1024 sc1
v_mfma_f32_16x16x32_bf16 v[80:83], v[208:211], a[36:39], v[80:83]
v_accvgpr_read_b32 v231, a231
v_perm_b32 v232, v231, v230, s86
v_mfma_f32_16x16x32_bf16 v[112:115], v[208:211], a[40:43], v[112:115]
ds_read_b64_tr_b16 v[208:209], v240 offset:128
ds_read_b64_tr_b16 v[210:211], v240 offset:1152
v_mfma_f32_16x16x32_bf16 v[20:23], v[212:215], a[0:3], v[20:23]
v_perm_b32 v233, v231, v230, s87
v_accvgpr_write_b32 a216, v232
v_mfma_f32_16x16x32_bf16 v[52:55], v[212:215], a[32:35], v[52:55]
buffer_load_dwordx4 a[24:27], v244, s[44:47], s55 offen offset:2048 sc1
v_mfma_f32_16x16x32_bf16 v[84:87], v[212:215], a[36:39], v[84:87]
s_waitcnt lgkmcnt(2)
v_accvgpr_write_b32 a220, v233
v_mfma_f32_16x16x32_bf16 v[116:119], v[212:215], a[40:43], v[116:119]
ds_read_b64_tr_b16 v[212:213], v241 offset:128
ds_read_b64_tr_b16 v[214:215], v241 offset:1152
v_mfma_f32_16x16x32_bf16 v[24:27], v[216:219], a[0:3], v[24:27]
v_accvgpr_read_b32 v234, a235
v_accvgpr_read_b32 v235, a239
v_mfma_f32_16x16x32_bf16 v[56:59], v[216:219], a[32:35], v[56:59]
buffer_load_dwordx4 a[28:31], v245, s[44:47], s55 offen offset:2048 sc1
v_mfma_f32_16x16x32_bf16 v[88:91], v[216:219], a[36:39], v[88:91]
v_perm_b32 v236, v235, v234, s86
v_perm_b32 v237, v235, v234, s87
v_mfma_f32_16x16x32_bf16 v[120:123], v[216:219], a[40:43], v[120:123]
ds_read_b64_tr_b16 v[216:217], v242 offset:128
ds_read_b64_tr_b16 v[218:219], v242 offset:1152
v_mfma_f32_16x16x32_bf16 v[28:31], v[220:223], a[0:3], v[28:31]
v_accvgpr_write_b32 a217, v236
v_accvgpr_write_b32 a221, v237
v_mfma_f32_16x16x32_bf16 v[60:63], v[220:223], a[32:35], v[60:63]
v_accvgpr_read_b32 v230, a243
v_accvgpr_read_b32 v231, a247
v_mfma_f32_16x16x32_bf16 v[92:95], v[220:223], a[36:39], v[92:95]
v_perm_b32 v232, v231, v230, s86
v_perm_b32 v233, v231, v230, s87
v_mfma_f32_16x16x32_bf16 v[124:127], v[220:223], a[40:43], v[124:127]
ds_read_b64_tr_b16 v[220:221], v243 offset:128
ds_read_b64_tr_b16 v[222:223], v243 offset:1152
v_mfma_f32_16x16x32_bf16 v[0:3], v[192:195], a[4:7], v[0:3]
v_accvgpr_write_b32 a218, v232
v_accvgpr_write_b32 a222, v233
v_mfma_f32_16x16x32_bf16 v[32:35], v[192:195], a[44:47], v[32:35]
buffer_load_dwordx4 a[0:3], v244, s[44:47], s55 offen offset:3072 sc1
v_mfma_f32_16x16x32_bf16 v[64:67], v[192:195], a[48:51], v[64:67]
v_accvgpr_read_b32 v234, a251
v_accvgpr_read_b32 v235, a255
v_mfma_f32_16x16x32_bf16 v[96:99], v[192:195], a[52:55], v[96:99]
ds_read_b64_tr_b16 v[192:193], v240 offset:8448
ds_read_b64_tr_b16 v[194:195], v240 offset:9472
v_mfma_f32_16x16x32_bf16 v[4:7], v[196:199], a[4:7], v[4:7]
v_perm_b32 v236, v235, v234, s86
v_perm_b32 v237, v235, v234, s87
v_mfma_f32_16x16x32_bf16 v[36:39], v[196:199], a[44:47], v[36:39]
buffer_load_dwordx4 a[32:35], v245, s[44:47], s55 offen offset:3072 sc1
v_mfma_f32_16x16x32_bf16 v[68:71], v[196:199], a[48:51], v[68:71]
v_accvgpr_write_b32 a219, v236
v_accvgpr_write_b32 a223, v237
v_mfma_f32_16x16x32_bf16 v[100:103], v[196:199], a[52:55], v[100:103]
ds_read_b64_tr_b16 v[196:197], v241 offset:8448
ds_read_b64_tr_b16 v[198:199], v241 offset:9472
v_mfma_f32_16x16x32_bf16 v[8:11], v[200:203], a[4:7], v[8:11]
v_mfma_f32_16x16x32_bf16 v[40:43], v[200:203], a[44:47], v[40:43]
v_mfma_f32_16x16x32_bf16 v[72:75], v[200:203], a[48:51], v[72:75]
v_mfma_f32_16x16x32_bf16 v[104:107], v[200:203], a[52:55], v[104:107]
ds_read_b64_tr_b16 v[200:201], v242 offset:8448
ds_read_b64_tr_b16 v[202:203], v242 offset:9472
v_mfma_f32_16x16x32_bf16 v[12:15], v[204:207], a[4:7], v[12:15]
v_mfma_f32_16x16x32_bf16 v[44:47], v[204:207], a[44:47], v[44:47]
v_mfma_f32_16x16x32_bf16 v[76:79], v[204:207], a[48:51], v[76:79]
s_waitcnt vmcnt(16) lgkmcnt(2)
v_mfma_f32_16x16x32_bf16 v[108:111], v[204:207], a[52:55], v[108:111]
ds_read_b64_tr_b16 v[204:205], v243 offset:8448
ds_read_b64_tr_b16 v[206:207], v243 offset:9472
v_mfma_f32_16x16x32_bf16 v[16:19], v[208:211], a[4:7], v[16:19]
v_mfma_f32_16x16x32_bf16 v[48:51], v[208:211], a[44:47], v[48:51]
v_mfma_f32_16x16x32_bf16 v[80:83], v[208:211], a[48:51], v[80:83]
v_mfma_f32_16x16x32_bf16 v[112:115], v[208:211], a[52:55], v[112:115]
ds_read_b64_tr_b16 v[208:209], v240 offset:8576
ds_read_b64_tr_b16 v[210:211], v240 offset:9600
v_mfma_f32_16x16x32_bf16 v[20:23], v[212:215], a[4:7], v[20:23]
v_add_u32 v240, 67584, v240
v_mfma_f32_16x16x32_bf16 v[52:55], v[212:215], a[44:47], v[52:55]
v_mfma_f32_16x16x32_bf16 v[84:87], v[212:215], a[48:51], v[84:87]
v_mfma_f32_16x16x32_bf16 v[116:119], v[212:215], a[52:55], v[116:119]
ds_read_b64_tr_b16 v[212:213], v241 offset:8576
ds_read_b64_tr_b16 v[214:215], v241 offset:9600
v_mfma_f32_16x16x32_bf16 v[24:27], v[216:219], a[4:7], v[24:27]
v_add_u32 v241, 67584, v241
v_mfma_f32_16x16x32_bf16 v[56:59], v[216:219], a[44:47], v[56:59]
v_mfma_f32_16x16x32_bf16 v[88:91], v[216:219], a[48:51], v[88:91]
v_mfma_f32_16x16x32_bf16 v[120:123], v[216:219], a[52:55], v[120:123]
ds_read_b64_tr_b16 v[216:217], v242 offset:8576
ds_read_b64_tr_b16 v[218:219], v242 offset:9600
v_mfma_f32_16x16x32_bf16 v[28:31], v[220:223], a[4:7], v[28:31]
v_add_u32 v242, 67584, v242
v_mfma_f32_16x16x32_bf16 v[60:63], v[220:223], a[44:47], v[60:63]
v_mfma_f32_16x16x32_bf16 v[92:95], v[220:223], a[48:51], v[92:95]
v_mfma_f32_16x16x32_bf16 v[124:127], v[220:223], a[52:55], v[124:127]
ds_read_b64_tr_b16 v[220:221], v243 offset:8576
ds_read_b64_tr_b16 v[222:223], v243 offset:9600
v_mfma_f32_16x16x32_bf16 v[0:3], v[192:195], v[136:139], v[0:3]
v_add_u32 v243, 67584, v243
v_mfma_f32_16x16x32_bf16 v[32:35], v[192:195], v[140:143], v[32:35]
buffer_load_dwordx4 a[4:7], v246, s[44:47], s54 offen offset:0 sc1
v_mfma_f32_16x16x32_bf16 v[64:67], v[192:195], v[152:155], v[64:67]
v_mfma_f32_16x16x32_bf16 v[96:99], v[192:195], v[156:159], v[96:99]
v_mfma_f32_16x16x32_bf16 v[4:7], v[196:199], v[136:139], v[4:7]
s_waitcnt lgkmcnt(0)
s_barrier
v_mfma_f32_16x16x32_bf16 v[36:39], v[196:199], v[140:143], v[36:39]
buffer_load_dwordx4 a[36:39], v247, s[44:47], s54 offen offset:0 sc1
v_mfma_f32_16x16x32_bf16 v[68:71], v[196:199], v[152:155], v[68:71]
ds_read_b64_tr_b16 v[192:193], v240 offset:50688
ds_read_b64_tr_b16 v[194:195], v240 offset:51712
s_nop 0
v_mfma_f32_16x16x32_bf16 v[100:103], v[196:199], v[156:159], v[100:103]
ds_read_b64_tr_b16 v[196:197], v240 offset:59136
ds_read_b64_tr_b16 v[198:199], v240 offset:60160
v_mfma_f32_16x16x32_bf16 v[8:11], v[200:203], v[136:139], v[8:11]
v_mfma_f32_16x16x32_bf16 v[40:43], v[200:203], v[140:143], v[40:43]
buffer_load_dwordx4 a[40:43], v246, s[44:47], s54 offen offset:1024 sc1
v_mfma_f32_16x16x32_bf16 v[72:75], v[200:203], v[152:155], v[72:75]
v_mfma_f32_16x16x32_bf16 v[104:107], v[200:203], v[156:159], v[104:107]
ds_read_b64_tr_b16 v[200:201], v240 offset:0
ds_read_b64_tr_b16 v[202:203], v240 offset:1024
v_mfma_f32_16x16x32_bf16 v[12:15], v[204:207], v[136:139], v[12:15]
v_mfma_f32_16x16x32_bf16 v[44:47], v[204:207], v[140:143], v[44:47]
buffer_load_dwordx4 a[44:47], v247, s[44:47], s54 offen offset:1024 sc1
v_mfma_f32_16x16x32_bf16 v[76:79], v[204:207], v[152:155], v[76:79]
v_mfma_f32_16x16x32_bf16 v[108:111], v[204:207], v[156:159], v[108:111]
ds_read_b64_tr_b16 v[204:205], v240 offset:8448
ds_read_b64_tr_b16 v[206:207], v240 offset:9472
v_mfma_f32_16x16x32_bf16 v[16:19], v[208:211], v[136:139], v[16:19]
v_mfma_f32_16x16x32_bf16 v[48:51], v[208:211], v[140:143], v[48:51]
buffer_load_dwordx4 a[48:51], v246, s[44:47], s54 offen offset:2048 sc1
v_mfma_f32_16x16x32_bf16 v[80:83], v[208:211], v[152:155], v[80:83]
v_mfma_f32_16x16x32_bf16 v[112:115], v[208:211], v[156:159], v[112:115]
ds_read_b64_tr_b16 v[208:209], v240 offset:16896
ds_read_b64_tr_b16 v[210:211], v240 offset:17920
v_mfma_f32_16x16x32_bf16 v[20:23], v[212:215], v[136:139], v[20:23]
v_mfma_f32_16x16x32_bf16 v[52:55], v[212:215], v[140:143], v[52:55]
buffer_load_dwordx4 a[52:55], v247, s[44:47], s54 offen offset:2048 sc1
v_mfma_f32_16x16x32_bf16 v[84:87], v[212:215], v[152:155], v[84:87]
v_mfma_f32_16x16x32_bf16 v[116:119], v[212:215], v[156:159], v[116:119]
ds_read_b64_tr_b16 v[212:213], v240 offset:25344
ds_read_b64_tr_b16 v[214:215], v240 offset:26368
v_mfma_f32_16x16x32_bf16 v[24:27], v[216:219], v[136:139], v[24:27]
v_mfma_f32_16x16x32_bf16 v[56:59], v[216:219], v[140:143], v[56:59]
buffer_load_dwordx4 a[56:59], v246, s[44:47], s54 offen offset:3072 sc1
v_mfma_f32_16x16x32_bf16 v[88:91], v[216:219], v[152:155], v[88:91]
v_mfma_f32_16x16x32_bf16 v[120:123], v[216:219], v[156:159], v[120:123]
ds_read_b64_tr_b16 v[216:217], v240 offset:33792
ds_read_b64_tr_b16 v[218:219], v240 offset:34816
v_mfma_f32_16x16x32_bf16 v[28:31], v[220:223], v[136:139], v[28:31]
v_mfma_f32_16x16x32_bf16 v[60:63], v[220:223], v[140:143], v[60:63]
v_mfma_f32_16x16x32_bf16 v[92:95], v[220:223], v[152:155], v[92:95]
s_waitcnt vmcnt(15) lgkmcnt(6)
v_mfma_f32_16x16x32_bf16 v[124:127], v[220:223], v[156:159], v[124:127]
ds_read_b64_tr_b16 v[220:221], v240 offset:42240
ds_read_b64_tr_b16 v[222:223], v240 offset:43264
v_mfma_f32_16x16x32_bf16 v[0:3], v[192:195], a[64:67], v[0:3]
buffer_load_dwordx4 a[60:63], v247, s[44:47], s54 offen offset:3072 sc1
v_mfma_f32_16x16x32_bf16 v[32:35], v[192:195], a[68:71], v[32:35]
ds_read_b64_tr_b16 v[192:193], v241 offset:50688
ds_read_b64_tr_b16 v[194:195], v241 offset:51712
v_mfma_f32_16x16x32_bf16 v[0:3], v[196:199], a[72:75], v[0:3]
v_mfma_f32_16x16x32_bf16 v[32:35], v[196:199], a[76:79], v[32:35]
ds_read_b64_tr_b16 v[196:197], v241 offset:59136
ds_read_b64_tr_b16 v[198:199], v241 offset:60160
v_mfma_f32_16x16x32_bf16 v[0:3], v[200:203], a[80:83], v[0:3]
buffer_load_dwordx4 v[128:131], v246, s[44:47], s55 offen offset:0 sc1
v_mfma_f32_16x16x32_bf16 v[32:35], v[200:203], a[84:87], v[32:35]
ds_read_b64_tr_b16 v[200:201], v241 offset:0
ds_read_b64_tr_b16 v[202:203], v241 offset:1024
v_mfma_f32_16x16x32_bf16 v[0:3], v[204:207], a[88:91], v[0:3]
s_waitcnt vmcnt(9) lgkmcnt(6)
v_mfma_f32_16x16x32_bf16 v[32:35], v[204:207], a[92:95], v[32:35]
ds_read_b64_tr_b16 v[204:205], v241 offset:8448
ds_read_b64_tr_b16 v[206:207], v241 offset:9472
v_mfma_f32_16x16x32_bf16 v[0:3], v[208:211], a[8:11], v[0:3]
buffer_load_dwordx4 v[132:135], v247, s[44:47], s55 offen offset:0 sc1
v_mfma_f32_16x16x32_bf16 v[32:35], v[208:211], a[12:15], v[32:35]
ds_read_b64_tr_b16 v[208:209], v241 offset:16896
ds_read_b64_tr_b16 v[210:211], v241 offset:17920
v_mfma_f32_16x16x32_bf16 v[0:3], v[212:215], a[16:19], v[0:3]
v_mfma_f32_16x16x32_bf16 v[32:35], v[212:215], a[20:23], v[32:35]
ds_read_b64_tr_b16 v[212:213], v241 offset:25344
ds_read_b64_tr_b16 v[214:215], v241 offset:26368
v_mfma_f32_16x16x32_bf16 v[0:3], v[216:219], a[24:27], v[0:3]
buffer_load_dwordx4 v[144:147], v246, s[44:47], s55 offen offset:1024 sc1
v_mfma_f32_16x16x32_bf16 v[32:35], v[216:219], a[28:31], v[32:35]
ds_read_b64_tr_b16 v[216:217], v241 offset:33792
ds_read_b64_tr_b16 v[218:219], v241 offset:34816
v_mfma_f32_16x16x32_bf16 v[0:3], v[220:223], a[0:3], v[0:3]
s_waitcnt lgkmcnt(6)
v_mfma_f32_16x16x32_bf16 v[32:35], v[220:223], a[32:35], v[32:35]
ds_read_b64_tr_b16 v[220:221], v241 offset:42240
ds_read_b64_tr_b16 v[222:223], v241 offset:43264
v_mfma_f32_16x16x32_bf16 v[4:7], v[192:195], a[64:67], v[4:7]
buffer_load_dwordx4 v[148:151], v247, s[44:47], s55 offen offset:1024 sc1
v_mfma_f32_16x16x32_bf16 v[36:39], v[192:195], a[68:71], v[36:39]
ds_read_b64_tr_b16 v[192:193], v242 offset:50688
ds_read_b64_tr_b16 v[194:195], v242 offset:51712
v_mfma_f32_16x16x32_bf16 v[4:7], v[196:199], a[72:75], v[4:7]
v_mfma_f32_16x16x32_bf16 v[36:39], v[196:199], a[76:79], v[36:39]
ds_read_b64_tr_b16 v[196:197], v242 offset:59136
ds_read_b64_tr_b16 v[198:199], v242 offset:60160
v_mfma_f32_16x16x32_bf16 v[4:7], v[200:203], a[80:83], v[4:7]
buffer_load_dwordx4 v[160:163], v246, s[44:47], s55 offen offset:2048 sc1
v_mfma_f32_16x16x32_bf16 v[36:39], v[200:203], a[84:87], v[36:39]
ds_read_b64_tr_b16 v[200:201], v242 offset:0
ds_read_b64_tr_b16 v[202:203], v242 offset:1024
v_mfma_f32_16x16x32_bf16 v[4:7], v[204:207], a[88:91], v[4:7]
s_waitcnt lgkmcnt(6)
v_mfma_f32_16x16x32_bf16 v[36:39], v[204:207], a[92:95], v[36:39]
ds_read_b64_tr_b16 v[204:205], v242 offset:8448
ds_read_b64_tr_b16 v[206:207], v242 offset:9472
v_mfma_f32_16x16x32_bf16 v[4:7], v[208:211], a[8:11], v[4:7]
buffer_load_dwordx4 v[164:167], v247, s[44:47], s55 offen offset:2048 sc1
v_mfma_f32_16x16x32_bf16 v[36:39], v[208:211], a[12:15], v[36:39]
ds_read_b64_tr_b16 v[208:209], v242 offset:16896
ds_read_b64_tr_b16 v[210:211], v242 offset:17920
v_mfma_f32_16x16x32_bf16 v[4:7], v[212:215], a[16:19], v[4:7]
v_mfma_f32_16x16x32_bf16 v[36:39], v[212:215], a[20:23], v[36:39]
ds_read_b64_tr_b16 v[212:213], v242 offset:25344
ds_read_b64_tr_b16 v[214:215], v242 offset:26368
v_mfma_f32_16x16x32_bf16 v[4:7], v[216:219], a[24:27], v[4:7]
v_mfma_f32_16x16x32_bf16 v[36:39], v[216:219], a[28:31], v[36:39]
ds_read_b64_tr_b16 v[216:217], v242 offset:33792
ds_read_b64_tr_b16 v[218:219], v242 offset:34816
v_mfma_f32_16x16x32_bf16 v[4:7], v[220:223], a[0:3], v[4:7]
s_waitcnt lgkmcnt(6)
v_mfma_f32_16x16x32_bf16 v[36:39], v[220:223], a[32:35], v[36:39]
ds_read_b64_tr_b16 v[220:221], v242 offset:42240
ds_read_b64_tr_b16 v[222:223], v242 offset:43264
v_mfma_f32_16x16x32_bf16 v[8:11], v[192:195], a[64:67], v[8:11]
buffer_load_dwordx4 v[176:179], v246, s[44:47], s55 offen offset:3072 sc1
v_mfma_f32_16x16x32_bf16 v[40:43], v[192:195], a[68:71], v[40:43]
ds_read_b64_tr_b16 v[192:193], v243 offset:50688
ds_read_b64_tr_b16 v[194:195], v243 offset:51712
v_mfma_f32_16x16x32_bf16 v[8:11], v[196:199], a[72:75], v[8:11]
v_cvt_pk_bf16_f32 v230, v0, v1
v_cvt_pk_bf16_f32 v232, v4, v5
v_mfma_f32_16x16x32_bf16 v[40:43], v[196:199], a[76:79], v[40:43]
ds_read_b64_tr_b16 v[196:197], v243 offset:59136
ds_read_b64_tr_b16 v[198:199], v243 offset:60160
v_mfma_f32_16x16x32_bf16 v[8:11], v[200:203], a[80:83], v[8:11]
buffer_load_dwordx4 v[180:183], v247, s[44:47], s55 offen offset:3072 sc1
v_mfma_f32_16x16x32_bf16 v[40:43], v[200:203], a[84:87], v[40:43]
ds_read_b64_tr_b16 v[200:201], v243 offset:0
ds_read_b64_tr_b16 v[202:203], v243 offset:1024
v_mfma_f32_16x16x32_bf16 v[8:11], v[204:207], a[88:91], v[8:11]
s_waitcnt lgkmcnt(6)
v_bfe_u32 v250, v255, 6, 1
v_mfma_f32_16x16x32_bf16 v[40:43], v[204:207], a[92:95], v[40:43]
ds_read_b64_tr_b16 v[204:205], v243 offset:8448
ds_read_b64_tr_b16 v[206:207], v243 offset:9472
v_mfma_f32_16x16x32_bf16 v[8:11], v[208:211], a[8:11], v[8:11]
v_cvt_pk_bf16_f32 v231, v2, v3
v_cvt_pk_bf16_f32 v233, v6, v7
v_mfma_f32_16x16x32_bf16 v[40:43], v[208:211], a[12:15], v[40:43]
ds_read_b64_tr_b16 v[208:209], v243 offset:16896
ds_read_b64_tr_b16 v[210:211], v243 offset:17920
v_mfma_f32_16x16x32_bf16 v[8:11], v[212:215], a[16:19], v[8:11]
v_permlane16_swap_b32 v230, v232
v_permlane16_swap_b32 v231, v233
v_mfma_f32_16x16x32_bf16 v[40:43], v[212:215], a[20:23], v[40:43]
ds_read_b64_tr_b16 v[212:213], v243 offset:25344
ds_read_b64_tr_b16 v[214:215], v243 offset:26368
v_mfma_f32_16x16x32_bf16 v[8:11], v[216:219], a[24:27], v[8:11]
buffer_store_dwordx4 v[230:233], v238, s[32:35], s94 offen offset:0 sc1
v_mfma_f32_16x16x32_bf16 v[40:43], v[216:219], a[28:31], v[40:43]
ds_read_b64_tr_b16 v[216:217], v243 offset:33792
ds_read_b64_tr_b16 v[218:219], v243 offset:34816
v_mfma_f32_16x16x32_bf16 v[8:11], v[220:223], a[0:3], v[8:11]
s_waitcnt lgkmcnt(6)
v_mul_u32_u24 v250, 7, v250
v_mfma_f32_16x16x32_bf16 v[40:43], v[220:223], a[32:35], v[40:43]
ds_read_b64_tr_b16 v[220:221], v243 offset:42240
ds_read_b64_tr_b16 v[222:223], v243 offset:43264
v_mfma_f32_16x16x32_bf16 v[12:15], v[192:195], a[64:67], v[12:15]
v_cvt_pk_bf16_f32 v234, v32, v33
v_cvt_pk_bf16_f32 v236, v36, v37
v_mfma_f32_16x16x32_bf16 v[44:47], v[192:195], a[68:71], v[44:47]
ds_read_b64_tr_b16 v[192:193], v240 offset:50816
ds_read_b64_tr_b16 v[194:195], v240 offset:51840
v_mfma_f32_16x16x32_bf16 v[12:15], v[196:199], a[72:75], v[12:15]
v_cvt_pk_bf16_f32 v235, v34, v35
v_cvt_pk_bf16_f32 v237, v38, v39
v_mfma_f32_16x16x32_bf16 v[44:47], v[196:199], a[76:79], v[44:47]
ds_read_b64_tr_b16 v[196:197], v240 offset:59264
ds_read_b64_tr_b16 v[198:199], v240 offset:60288
v_mfma_f32_16x16x32_bf16 v[12:15], v[200:203], a[80:83], v[12:15]
v_permlane16_swap_b32 v234, v236
v_permlane16_swap_b32 v235, v237
v_mfma_f32_16x16x32_bf16 v[44:47], v[200:203], a[84:87], v[44:47]
ds_read_b64_tr_b16 v[200:201], v240 offset:128
ds_read_b64_tr_b16 v[202:203], v240 offset:1152
v_mfma_f32_16x16x32_bf16 v[12:15], v[204:207], a[88:91], v[12:15]
s_waitcnt lgkmcnt(6)
v_xor_b32 v250, s3, v250
v_mfma_f32_16x16x32_bf16 v[44:47], v[204:207], a[92:95], v[44:47]
.La5_LW3_3:
s_cbranch_execz .La5_LW3_R0
ds_read_b64_tr_b16 v[204:205], v240 offset:8576
ds_read_b64_tr_b16 v[206:207], v240 offset:9600
v_mfma_f32_16x16x32_bf16 v[12:15], v[208:211], a[8:11], v[12:15]
buffer_store_dwordx4 v[234:237], v238, s[32:35], s95 offen offset:0 sc1
v_mfma_f32_16x16x32_bf16 v[44:47], v[208:211], a[12:15], v[44:47]
ds_read_b64_tr_b16 v[208:209], v240 offset:17024
ds_read_b64_tr_b16 v[210:211], v240 offset:18048
v_mfma_f32_16x16x32_bf16 v[12:15], v[212:215], a[16:19], v[12:15]
v_bfe_u32 v251, v255, 4, 2
v_lshl_add_u32 v250, v250, 2, v251
v_mfma_f32_16x16x32_bf16 v[44:47], v[212:215], a[20:23], v[44:47]
ds_read_b64_tr_b16 v[212:213], v240 offset:25472
ds_read_b64_tr_b16 v[214:215], v240 offset:26496
v_mfma_f32_16x16x32_bf16 v[12:15], v[216:219], a[24:27], v[12:15]
buffer_load_dwordx4 v[136:139], v224, s[68:71], 0 offen offset:0
v_mfma_f32_16x16x32_bf16 v[44:47], v[216:219], a[28:31], v[44:47]
ds_read_b64_tr_b16 v[216:217], v240 offset:33920
ds_read_b64_tr_b16 v[218:219], v240 offset:34944
v_mfma_f32_16x16x32_bf16 v[12:15], v[220:223], a[0:3], v[12:15]
s_waitcnt lgkmcnt(6)
v_lshlrev_b32 v250, 9, v250
v_mfma_f32_16x16x32_bf16 v[44:47], v[220:223], a[32:35], v[44:47]
ds_read_b64_tr_b16 v[220:221], v240 offset:42368
ds_read_b64_tr_b16 v[222:223], v240 offset:43392
v_mfma_f32_16x16x32_bf16 v[16:19], v[192:195], a[64:67], v[16:19]
v_cvt_pk_bf16_f32 v230, v8, v9
v_cvt_pk_bf16_f32 v232, v12, v13
v_mfma_f32_16x16x32_bf16 v[48:51], v[192:195], a[68:71], v[48:51]
ds_read_b64_tr_b16 v[192:193], v241 offset:50816
ds_read_b64_tr_b16 v[194:195], v241 offset:51840
v_mfma_f32_16x16x32_bf16 v[16:19], v[196:199], a[72:75], v[16:19]
v_cvt_pk_bf16_f32 v231, v10, v11
v_cvt_pk_bf16_f32 v233, v14, v15
v_mfma_f32_16x16x32_bf16 v[48:51], v[196:199], a[76:79], v[48:51]
ds_read_b64_tr_b16 v[196:197], v241 offset:59264
ds_read_b64_tr_b16 v[198:199], v241 offset:60288
v_mfma_f32_16x16x32_bf16 v[16:19], v[200:203], a[80:83], v[16:19]
v_permlane16_swap_b32 v230, v232
v_permlane16_swap_b32 v231, v233
v_mfma_f32_16x16x32_bf16 v[48:51], v[200:203], a[84:87], v[48:51]
ds_read_b64_tr_b16 v[200:201], v241 offset:128
ds_read_b64_tr_b16 v[202:203], v241 offset:1152
v_mfma_f32_16x16x32_bf16 v[16:19], v[204:207], a[88:91], v[16:19]
s_waitcnt lgkmcnt(6)
v_and_b32 v248, 0x1f0, v255
v_mfma_f32_16x16x32_bf16 v[48:51], v[204:207], a[92:95], v[48:51]
ds_read_b64_tr_b16 v[204:205], v241 offset:8576
ds_read_b64_tr_b16 v[206:207], v241 offset:9600
v_mfma_f32_16x16x32_bf16 v[16:19], v[208:211], a[8:11], v[16:19]
buffer_store_dwordx4 v[230:233], v238, s[32:35], s94 offen offset:1024 sc1
v_mfma_f32_16x16x32_bf16 v[48:51], v[208:211], a[12:15], v[48:51]
ds_read_b64_tr_b16 v[208:209], v241 offset:17024
ds_read_b64_tr_b16 v[210:211], v241 offset:18048
v_mfma_f32_16x16x32_bf16 v[16:19], v[212:215], a[16:19], v[16:19]
v_cvt_pk_bf16_f32 v234, v40, v41
v_cvt_pk_bf16_f32 v236, v44, v45
v_mfma_f32_16x16x32_bf16 v[48:51], v[212:215], a[20:23], v[48:51]
ds_read_b64_tr_b16 v[212:213], v241 offset:25472
ds_read_b64_tr_b16 v[214:215], v241 offset:26496
v_mfma_f32_16x16x32_bf16 v[16:19], v[216:219], a[24:27], v[16:19]
v_cvt_pk_bf16_f32 v235, v42, v43
v_cvt_pk_bf16_f32 v237, v46, v47
v_mfma_f32_16x16x32_bf16 v[48:51], v[216:219], a[28:31], v[48:51]
ds_read_b64_tr_b16 v[216:217], v241 offset:33920
ds_read_b64_tr_b16 v[218:219], v241 offset:34944
v_mfma_f32_16x16x32_bf16 v[16:19], v[220:223], a[0:3], v[16:19]
s_waitcnt lgkmcnt(6)
v_lshrrev_b32 v248, 1, v248
v_mfma_f32_16x16x32_bf16 v[48:51], v[220:223], a[32:35], v[48:51]
ds_read_b64_tr_b16 v[220:221], v241 offset:42368
ds_read_b64_tr_b16 v[222:223], v241 offset:43392
v_mfma_f32_16x16x32_bf16 v[20:23], v[192:195], a[64:67], v[20:23]
v_permlane16_swap_b32 v234, v236
v_permlane16_swap_b32 v235, v237
v_mfma_f32_16x16x32_bf16 v[52:55], v[192:195], a[68:71], v[52:55]
ds_read_b64_tr_b16 v[192:193], v242 offset:50816
ds_read_b64_tr_b16 v[194:195], v242 offset:51840
v_mfma_f32_16x16x32_bf16 v[20:23], v[196:199], a[72:75], v[20:23]
v_mfma_f32_16x16x32_bf16 v[52:55], v[196:199], a[76:79], v[52:55]
ds_read_b64_tr_b16 v[196:197], v242 offset:59264
ds_read_b64_tr_b16 v[198:199], v242 offset:60288
v_mfma_f32_16x16x32_bf16 v[20:23], v[200:203], a[80:83], v[20:23]
buffer_store_dwordx4 v[234:237], v238, s[32:35], s95 offen offset:1024 sc1
v_mfma_f32_16x16x32_bf16 v[52:55], v[200:203], a[84:87], v[52:55]
ds_read_b64_tr_b16 v[200:201], v242 offset:128
ds_read_b64_tr_b16 v[202:203], v242 offset:1152
v_mfma_f32_16x16x32_bf16 v[20:23], v[204:207], a[88:91], v[20:23]
s_waitcnt lgkmcnt(6)
v_mfma_f32_16x16x32_bf16 v[52:55], v[204:207], a[92:95], v[52:55]
ds_read_b64_tr_b16 v[204:205], v242 offset:8576
ds_read_b64_tr_b16 v[206:207], v242 offset:9600
v_mfma_f32_16x16x32_bf16 v[20:23], v[208:211], a[8:11], v[20:23]
buffer_load_dwordx4 v[140:143], v225, s[48:51], 0 offen offset:0
v_mfma_f32_16x16x32_bf16 v[52:55], v[208:211], a[12:15], v[52:55]
ds_read_b64_tr_b16 v[208:209], v242 offset:17024
ds_read_b64_tr_b16 v[210:211], v242 offset:18048
v_mfma_f32_16x16x32_bf16 v[20:23], v[212:215], a[16:19], v[20:23]
v_mfma_f32_16x16x32_bf16 v[52:55], v[212:215], a[20:23], v[52:55]
ds_read_b64_tr_b16 v[212:213], v242 offset:25472
ds_read_b64_tr_b16 v[214:215], v242 offset:26496
v_mfma_f32_16x16x32_bf16 v[20:23], v[216:219], a[24:27], v[20:23]
buffer_load_dwordx4 v[152:155], v224, s[68:71], 0 offen offset:512
v_mfma_f32_16x16x32_bf16 v[52:55], v[216:219], a[28:31], v[52:55]
ds_read_b64_tr_b16 v[216:217], v242 offset:33920
ds_read_b64_tr_b16 v[218:219], v242 offset:34944
v_mfma_f32_16x16x32_bf16 v[20:23], v[220:223], a[0:3], v[20:23]
s_waitcnt lgkmcnt(6)
s_mov_b32 s93, 0
buffer_load_dwordx4 a[224:227], v229, s[28:31], s93 offen offset:0
v_mfma_f32_16x16x32_bf16 v[52:55], v[220:223], a[32:35], v[52:55]
ds_read_b64_tr_b16 v[220:221], v242 offset:42368
ds_read_b64_tr_b16 v[222:223], v242 offset:43392
v_mfma_f32_16x16x32_bf16 v[24:27], v[192:195], a[64:67], v[24:27]
v_cvt_pk_bf16_f32 v230, v16, v17
v_cvt_pk_bf16_f32 v232, v20, v21
v_mfma_f32_16x16x32_bf16 v[56:59], v[192:195], a[68:71], v[56:59]
ds_read_b64_tr_b16 v[192:193], v243 offset:50816
ds_read_b64_tr_b16 v[194:195], v243 offset:51840
v_mfma_f32_16x16x32_bf16 v[24:27], v[196:199], a[72:75], v[24:27]
v_cvt_pk_bf16_f32 v231, v18, v19
v_cvt_pk_bf16_f32 v233, v22, v23
v_mfma_f32_16x16x32_bf16 v[56:59], v[196:199], a[76:79], v[56:59]
ds_read_b64_tr_b16 v[196:197], v243 offset:59264
ds_read_b64_tr_b16 v[198:199], v243 offset:60288
v_mfma_f32_16x16x32_bf16 v[24:27], v[200:203], a[80:83], v[24:27]
v_permlane16_swap_b32 v230, v232
v_permlane16_swap_b32 v231, v233
v_mfma_f32_16x16x32_bf16 v[56:59], v[200:203], a[84:87], v[56:59]
ds_read_b64_tr_b16 v[200:201], v243 offset:128
ds_read_b64_tr_b16 v[202:203], v243 offset:1152
v_mfma_f32_16x16x32_bf16 v[24:27], v[204:207], a[88:91], v[24:27]
s_waitcnt lgkmcnt(6)
buffer_load_dwordx4 a[228:231], v229, s[28:31], s93 offen offset:2048
v_mfma_f32_16x16x32_bf16 v[56:59], v[204:207], a[92:95], v[56:59]
ds_read_b64_tr_b16 v[204:205], v243 offset:8576
ds_read_b64_tr_b16 v[206:207], v243 offset:9600
v_mfma_f32_16x16x32_bf16 v[24:27], v[208:211], a[8:11], v[24:27]
buffer_store_dwordx4 v[230:233], v238, s[32:35], s94 offen offset:2048 sc1
v_mfma_f32_16x16x32_bf16 v[56:59], v[208:211], a[12:15], v[56:59]
ds_read_b64_tr_b16 v[208:209], v243 offset:17024
ds_read_b64_tr_b16 v[210:211], v243 offset:18048
v_mfma_f32_16x16x32_bf16 v[24:27], v[212:215], a[16:19], v[24:27]
v_cvt_pk_bf16_f32 v234, v48, v49
v_cvt_pk_bf16_f32 v236, v52, v53
v_mfma_f32_16x16x32_bf16 v[56:59], v[212:215], a[20:23], v[56:59]
ds_read_b64_tr_b16 v[212:213], v243 offset:25472
ds_read_b64_tr_b16 v[214:215], v243 offset:26496
v_mfma_f32_16x16x32_bf16 v[24:27], v[216:219], a[24:27], v[24:27]
v_cvt_pk_bf16_f32 v235, v50, v51
v_cvt_pk_bf16_f32 v237, v54, v55
v_mfma_f32_16x16x32_bf16 v[56:59], v[216:219], a[28:31], v[56:59]
ds_read_b64_tr_b16 v[216:217], v243 offset:33920
ds_read_b64_tr_b16 v[218:219], v243 offset:34944
v_mfma_f32_16x16x32_bf16 v[24:27], v[220:223], a[0:3], v[24:27]
s_waitcnt lgkmcnt(6)
s_mov_b32 s93, 4096
buffer_load_dwordx4 a[232:235], v229, s[28:31], s93 offen offset:0
v_mfma_f32_16x16x32_bf16 v[56:59], v[220:223], a[32:35], v[56:59]
ds_read_b64_tr_b16 v[220:221], v243 offset:42368
ds_read_b64_tr_b16 v[222:223], v243 offset:43392
v_mfma_f32_16x16x32_bf16 v[28:31], v[192:195], a[64:67], v[28:31]
v_permlane16_swap_b32 v234, v236
v_permlane16_swap_b32 v235, v237
v_mfma_f32_16x16x32_bf16 v[60:63], v[192:195], a[68:71], v[60:63]
ds_read_b64_tr_b16 v[192:193], v240 offset:50688
ds_read_b64_tr_b16 v[194:195], v240 offset:51712
v_mfma_f32_16x16x32_bf16 v[28:31], v[196:199], a[72:75], v[28:31]
buffer_load_dwordx4 a[236:239], v229, s[28:31], s93 offen offset:2048
v_mfma_f32_16x16x32_bf16 v[60:63], v[196:199], a[76:79], v[60:63]
ds_read_b64_tr_b16 v[196:197], v240 offset:59136
ds_read_b64_tr_b16 v[198:199], v240 offset:60160
v_mfma_f32_16x16x32_bf16 v[28:31], v[200:203], a[80:83], v[28:31]
buffer_store_dwordx4 v[234:237], v238, s[32:35], s95 offen offset:2048 sc1
v_mfma_f32_16x16x32_bf16 v[60:63], v[200:203], a[84:87], v[60:63]
ds_read_b64_tr_b16 v[200:201], v240 offset:0
ds_read_b64_tr_b16 v[202:203], v240 offset:1024
v_mfma_f32_16x16x32_bf16 v[28:31], v[204:207], a[88:91], v[28:31]
s_waitcnt lgkmcnt(6)
s_mov_b32 s93, 8192
buffer_load_dwordx4 a[240:243], v229, s[28:31], s93 offen offset:0
v_mfma_f32_16x16x32_bf16 v[60:63], v[204:207], a[92:95], v[60:63]
ds_read_b64_tr_b16 v[204:205], v240 offset:8448
ds_read_b64_tr_b16 v[206:207], v240 offset:9472
v_mfma_f32_16x16x32_bf16 v[28:31], v[208:211], a[8:11], v[28:31]
buffer_load_dwordx4 v[156:159], v225, s[48:51], 0 offen offset:512
v_mfma_f32_16x16x32_bf16 v[60:63], v[208:211], a[12:15], v[60:63]
ds_read_b64_tr_b16 v[208:209], v240 offset:16896
ds_read_b64_tr_b16 v[210:211], v240 offset:17920
v_mfma_f32_16x16x32_bf16 v[28:31], v[212:215], a[16:19], v[28:31]
buffer_load_dwordx4 a[244:247], v229, s[28:31], s93 offen offset:2048
v_mfma_f32_16x16x32_bf16 v[60:63], v[212:215], a[20:23], v[60:63]
ds_read_b64_tr_b16 v[212:213], v240 offset:25344
ds_read_b64_tr_b16 v[214:215], v240 offset:26368
v_mfma_f32_16x16x32_bf16 v[28:31], v[216:219], a[24:27], v[28:31]
buffer_load_dwordx4 v[168:171], v224, s[68:71], 0 offen offset:1024
v_mfma_f32_16x16x32_bf16 v[60:63], v[216:219], a[28:31], v[60:63]
ds_read_b64_tr_b16 v[216:217], v240 offset:33792
ds_read_b64_tr_b16 v[218:219], v240 offset:34816
v_mfma_f32_16x16x32_bf16 v[28:31], v[220:223], a[0:3], v[28:31]
s_waitcnt vmcnt(25) lgkmcnt(6)
s_mov_b32 s93, 12288
buffer_load_dwordx4 a[248:251], v229, s[28:31], s93 offen offset:0
v_mfma_f32_16x16x32_bf16 v[60:63], v[220:223], a[32:35], v[60:63]
ds_read_b64_tr_b16 v[220:221], v240 offset:42240
ds_read_b64_tr_b16 v[222:223], v240 offset:43264
v_mfma_f32_16x16x32_bf16 v[64:67], v[192:195], a[4:7], v[64:67]
v_cvt_pk_bf16_f32 v230, v24, v25
v_cvt_pk_bf16_f32 v232, v28, v29
v_mfma_f32_16x16x32_bf16 v[96:99], v[192:195], a[36:39], v[96:99]
ds_read_b64_tr_b16 v[192:193], v241 offset:50688
ds_read_b64_tr_b16 v[194:195], v241 offset:51712
v_mfma_f32_16x16x32_bf16 v[64:67], v[196:199], a[40:43], v[64:67]
v_cvt_pk_bf16_f32 v231, v26, v27
v_cvt_pk_bf16_f32 v233, v30, v31
v_mfma_f32_16x16x32_bf16 v[96:99], v[196:199], a[44:47], v[96:99]
ds_read_b64_tr_b16 v[196:197], v241 offset:59136
ds_read_b64_tr_b16 v[198:199], v241 offset:60160
v_mfma_f32_16x16x32_bf16 v[64:67], v[200:203], a[48:51], v[64:67]
v_permlane16_swap_b32 v230, v232
v_permlane16_swap_b32 v231, v233
v_mfma_f32_16x16x32_bf16 v[96:99], v[200:203], a[52:55], v[96:99]
ds_read_b64_tr_b16 v[200:201], v241 offset:0
ds_read_b64_tr_b16 v[202:203], v241 offset:1024
v_mfma_f32_16x16x32_bf16 v[64:67], v[204:207], a[56:59], v[64:67]
s_waitcnt vmcnt(18) lgkmcnt(6)
buffer_load_dwordx4 a[252:255], v229, s[28:31], s93 offen offset:2048
v_mfma_f32_16x16x32_bf16 v[96:99], v[204:207], a[60:63], v[96:99]
ds_read_b64_tr_b16 v[204:205], v241 offset:8448
ds_read_b64_tr_b16 v[206:207], v241 offset:9472
v_mfma_f32_16x16x32_bf16 v[64:67], v[208:211], v[128:131], v[64:67]
buffer_store_dwordx4 v[230:233], v238, s[32:35], s94 offen offset:3072 sc1
v_mfma_f32_16x16x32_bf16 v[96:99], v[208:211], v[132:135], v[96:99]
ds_read_b64_tr_b16 v[208:209], v241 offset:16896
ds_read_b64_tr_b16 v[210:211], v241 offset:17920
v_mfma_f32_16x16x32_bf16 v[64:67], v[212:215], v[144:147], v[64:67]
v_cvt_pk_bf16_f32 v234, v56, v57
v_cvt_pk_bf16_f32 v236, v60, v61
v_mfma_f32_16x16x32_bf16 v[96:99], v[212:215], v[148:151], v[96:99]
ds_read_b64_tr_b16 v[212:213], v241 offset:25344
ds_read_b64_tr_b16 v[214:215], v241 offset:26368
v_mfma_f32_16x16x32_bf16 v[64:67], v[216:219], v[160:163], v[64:67]
v_cvt_pk_bf16_f32 v235, v58, v59
v_cvt_pk_bf16_f32 v237, v62, v63
v_mfma_f32_16x16x32_bf16 v[96:99], v[216:219], v[164:167], v[96:99]
ds_read_b64_tr_b16 v[216:217], v241 offset:33792
ds_read_b64_tr_b16 v[218:219], v241 offset:34816
v_mfma_f32_16x16x32_bf16 v[64:67], v[220:223], v[176:179], v[64:67]
s_waitcnt lgkmcnt(6)
s_mov_b32 s93, 0
buffer_load_dwordx4 a[64:67], v229, s[28:31], s93 offen offset:256
v_mfma_f32_16x16x32_bf16 v[96:99], v[220:223], v[180:183], v[96:99]
ds_read_b64_tr_b16 v[220:221], v241 offset:42240
ds_read_b64_tr_b16 v[222:223], v241 offset:43264
v_mfma_f32_16x16x32_bf16 v[68:71], v[192:195], a[4:7], v[68:71]
v_permlane16_swap_b32 v234, v236
v_permlane16_swap_b32 v235, v237
v_mfma_f32_16x16x32_bf16 v[100:103], v[192:195], a[36:39], v[100:103]
ds_read_b64_tr_b16 v[192:193], v242 offset:50688
ds_read_b64_tr_b16 v[194:195], v242 offset:51712
v_mfma_f32_16x16x32_bf16 v[68:71], v[196:199], a[40:43], v[68:71]
buffer_load_dwordx4 a[68:71], v229, s[28:31], s93 offen offset:2304
v_mfma_f32_16x16x32_bf16 v[100:103], v[196:199], a[44:47], v[100:103]
ds_read_b64_tr_b16 v[196:197], v242 offset:59136
ds_read_b64_tr_b16 v[198:199], v242 offset:60160
v_mfma_f32_16x16x32_bf16 v[68:71], v[200:203], a[48:51], v[68:71]
buffer_store_dwordx4 v[234:237], v238, s[32:35], s95 offen offset:3072 sc1
v_mfma_f32_16x16x32_bf16 v[100:103], v[200:203], a[52:55], v[100:103]
ds_read_b64_tr_b16 v[200:201], v242 offset:0
ds_read_b64_tr_b16 v[202:203], v242 offset:1024
v_mfma_f32_16x16x32_bf16 v[68:71], v[204:207], a[56:59], v[68:71]
s_waitcnt lgkmcnt(6)
s_mov_b32 s93, 4096
buffer_load_dwordx4 a[72:75], v229, s[28:31], s93 offen offset:256
v_mfma_f32_16x16x32_bf16 v[100:103], v[204:207], a[60:63], v[100:103]
ds_read_b64_tr_b16 v[204:205], v242 offset:8448
ds_read_b64_tr_b16 v[206:207], v242 offset:9472
v_mfma_f32_16x16x32_bf16 v[68:71], v[208:211], v[128:131], v[68:71]
buffer_load_dwordx4 v[172:175], v225, s[48:51], 0 offen offset:1024
v_mfma_f32_16x16x32_bf16 v[100:103], v[208:211], v[132:135], v[100:103]
ds_read_b64_tr_b16 v[208:209], v242 offset:16896
ds_read_b64_tr_b16 v[210:211], v242 offset:17920
v_mfma_f32_16x16x32_bf16 v[68:71], v[212:215], v[144:147], v[68:71]
buffer_load_dwordx4 a[76:79], v229, s[28:31], s93 offen offset:2304
v_mfma_f32_16x16x32_bf16 v[100:103], v[212:215], v[148:151], v[100:103]
ds_read_b64_tr_b16 v[212:213], v242 offset:25344
ds_read_b64_tr_b16 v[214:215], v242 offset:26368
v_mfma_f32_16x16x32_bf16 v[68:71], v[216:219], v[160:163], v[68:71]
buffer_load_dwordx4 v[184:187], v224, s[68:71], 0 offen offset:1536
v_mfma_f32_16x16x32_bf16 v[100:103], v[216:219], v[164:167], v[100:103]
ds_read_b64_tr_b16 v[216:217], v242 offset:33792
ds_read_b64_tr_b16 v[218:219], v242 offset:34816
v_mfma_f32_16x16x32_bf16 v[68:71], v[220:223], v[176:179], v[68:71]
s_waitcnt lgkmcnt(6)
s_mov_b32 s93, 8192
buffer_load_dwordx4 a[80:83], v229, s[28:31], s93 offen offset:256
v_mfma_f32_16x16x32_bf16 v[100:103], v[220:223], v[180:183], v[100:103]
ds_read_b64_tr_b16 v[220:221], v242 offset:42240
ds_read_b64_tr_b16 v[222:223], v242 offset:43264
v_mfma_f32_16x16x32_bf16 v[72:75], v[192:195], a[4:7], v[72:75]
v_cvt_pk_bf16_f32 v230, v64, v65
v_cvt_pk_bf16_f32 v232, v68, v69
v_mfma_f32_16x16x32_bf16 v[104:107], v[192:195], a[36:39], v[104:107]
ds_read_b64_tr_b16 v[192:193], v243 offset:50688
ds_read_b64_tr_b16 v[194:195], v243 offset:51712
v_mfma_f32_16x16x32_bf16 v[72:75], v[196:199], a[40:43], v[72:75]
v_cvt_pk_bf16_f32 v231, v66, v67
v_cvt_pk_bf16_f32 v233, v70, v71
v_mfma_f32_16x16x32_bf16 v[104:107], v[196:199], a[44:47], v[104:107]
ds_read_b64_tr_b16 v[196:197], v243 offset:59136
ds_read_b64_tr_b16 v[198:199], v243 offset:60160
v_mfma_f32_16x16x32_bf16 v[72:75], v[200:203], a[48:51], v[72:75]
v_permlane16_swap_b32 v230, v232
v_permlane16_swap_b32 v231, v233
v_mfma_f32_16x16x32_bf16 v[104:107], v[200:203], a[52:55], v[104:107]
ds_read_b64_tr_b16 v[200:201], v243 offset:0
ds_read_b64_tr_b16 v[202:203], v243 offset:1024
v_mfma_f32_16x16x32_bf16 v[72:75], v[204:207], a[56:59], v[72:75]
s_waitcnt lgkmcnt(6)
buffer_load_dwordx4 a[84:87], v229, s[28:31], s93 offen offset:2304
v_mfma_f32_16x16x32_bf16 v[104:107], v[204:207], a[60:63], v[104:107]
ds_read_b64_tr_b16 v[204:205], v243 offset:8448
ds_read_b64_tr_b16 v[206:207], v243 offset:9472
v_mfma_f32_16x16x32_bf16 v[72:75], v[208:211], v[128:131], v[72:75]
buffer_store_dwordx4 v[230:233], v238, s[32:35], s96 offen offset:0 sc1
v_mfma_f32_16x16x32_bf16 v[104:107], v[208:211], v[132:135], v[104:107]
ds_read_b64_tr_b16 v[208:209], v243 offset:16896
ds_read_b64_tr_b16 v[210:211], v243 offset:17920
v_mfma_f32_16x16x32_bf16 v[72:75], v[212:215], v[144:147], v[72:75]
v_cvt_pk_bf16_f32 v234, v96, v97
v_cvt_pk_bf16_f32 v236, v100, v101
v_mfma_f32_16x16x32_bf16 v[104:107], v[212:215], v[148:151], v[104:107]
ds_read_b64_tr_b16 v[212:213], v243 offset:25344
ds_read_b64_tr_b16 v[214:215], v243 offset:26368
v_mfma_f32_16x16x32_bf16 v[72:75], v[216:219], v[160:163], v[72:75]
v_cvt_pk_bf16_f32 v235, v98, v99
v_cvt_pk_bf16_f32 v237, v102, v103
v_mfma_f32_16x16x32_bf16 v[104:107], v[216:219], v[164:167], v[104:107]
ds_read_b64_tr_b16 v[216:217], v243 offset:33792
ds_read_b64_tr_b16 v[218:219], v243 offset:34816
v_mfma_f32_16x16x32_bf16 v[72:75], v[220:223], v[176:179], v[72:75]
s_waitcnt lgkmcnt(6)
s_mov_b32 s93, 12288
buffer_load_dwordx4 a[88:91], v229, s[28:31], s93 offen offset:256
v_mfma_f32_16x16x32_bf16 v[104:107], v[220:223], v[180:183], v[104:107]
ds_read_b64_tr_b16 v[220:221], v243 offset:42240
ds_read_b64_tr_b16 v[222:223], v243 offset:43264
v_mfma_f32_16x16x32_bf16 v[76:79], v[192:195], a[4:7], v[76:79]
v_permlane16_swap_b32 v234, v236
v_permlane16_swap_b32 v235, v237
v_mfma_f32_16x16x32_bf16 v[108:111], v[192:195], a[36:39], v[108:111]
ds_read_b64_tr_b16 v[192:193], v240 offset:50816
ds_read_b64_tr_b16 v[194:195], v240 offset:51840
v_mfma_f32_16x16x32_bf16 v[76:79], v[196:199], a[40:43], v[76:79]
buffer_load_dwordx4 a[92:95], v229, s[28:31], s93 offen offset:2304
v_mfma_f32_16x16x32_bf16 v[108:111], v[196:199], a[44:47], v[108:111]
ds_read_b64_tr_b16 v[196:197], v240 offset:59264
ds_read_b64_tr_b16 v[198:199], v240 offset:60288
v_mfma_f32_16x16x32_bf16 v[76:79], v[200:203], a[48:51], v[76:79]
buffer_store_dwordx4 v[234:237], v238, s[32:35], s97 offen offset:0 sc1
v_mfma_f32_16x16x32_bf16 v[108:111], v[200:203], a[52:55], v[108:111]
ds_read_b64_tr_b16 v[200:201], v240 offset:128
ds_read_b64_tr_b16 v[202:203], v240 offset:1152
v_mfma_f32_16x16x32_bf16 v[76:79], v[204:207], a[56:59], v[76:79]
s_waitcnt lgkmcnt(6)
v_mfma_f32_16x16x32_bf16 v[108:111], v[204:207], a[60:63], v[108:111]
ds_read_b64_tr_b16 v[204:205], v240 offset:8576
ds_read_b64_tr_b16 v[206:207], v240 offset:9600
v_mfma_f32_16x16x32_bf16 v[76:79], v[208:211], v[128:131], v[76:79]
buffer_load_dwordx4 v[188:191], v225, s[48:51], 0 offen offset:1536
v_mfma_f32_16x16x32_bf16 v[108:111], v[208:211], v[132:135], v[108:111]
ds_read_b64_tr_b16 v[208:209], v240 offset:17024
ds_read_b64_tr_b16 v[210:211], v240 offset:18048
v_mfma_f32_16x16x32_bf16 v[76:79], v[212:215], v[144:147], v[76:79]
v_mfma_f32_16x16x32_bf16 v[108:111], v[212:215], v[148:151], v[108:111]
ds_read_b64_tr_b16 v[212:213], v240 offset:25472
ds_read_b64_tr_b16 v[214:215], v240 offset:26496
v_mfma_f32_16x16x32_bf16 v[76:79], v[216:219], v[160:163], v[76:79]
v_mfma_f32_16x16x32_bf16 v[108:111], v[216:219], v[164:167], v[108:111]
ds_read_b64_tr_b16 v[216:217], v240 offset:33920
ds_read_b64_tr_b16 v[218:219], v240 offset:34944
v_mfma_f32_16x16x32_bf16 v[76:79], v[220:223], v[176:179], v[76:79]
s_waitcnt lgkmcnt(6)
v_mfma_f32_16x16x32_bf16 v[108:111], v[220:223], v[180:183], v[108:111]
ds_read_b64_tr_b16 v[220:221], v240 offset:42368
ds_read_b64_tr_b16 v[222:223], v240 offset:43392
v_mfma_f32_16x16x32_bf16 v[80:83], v[192:195], a[4:7], v[80:83]
v_cvt_pk_bf16_f32 v230, v72, v73
v_cvt_pk_bf16_f32 v232, v76, v77
v_mfma_f32_16x16x32_bf16 v[112:115], v[192:195], a[36:39], v[112:115]
ds_read_b64_tr_b16 v[192:193], v241 offset:50816
ds_read_b64_tr_b16 v[194:195], v241 offset:51840
v_mfma_f32_16x16x32_bf16 v[80:83], v[196:199], a[40:43], v[80:83]
v_cvt_pk_bf16_f32 v231, v74, v75
v_cvt_pk_bf16_f32 v233, v78, v79
v_mfma_f32_16x16x32_bf16 v[112:115], v[196:199], a[44:47], v[112:115]
ds_read_b64_tr_b16 v[196:197], v241 offset:59264
ds_read_b64_tr_b16 v[198:199], v241 offset:60288
v_mfma_f32_16x16x32_bf16 v[80:83], v[200:203], a[48:51], v[80:83]
v_permlane16_swap_b32 v230, v232
v_permlane16_swap_b32 v231, v233
v_mfma_f32_16x16x32_bf16 v[112:115], v[200:203], a[52:55], v[112:115]
ds_read_b64_tr_b16 v[200:201], v241 offset:128
ds_read_b64_tr_b16 v[202:203], v241 offset:1152
v_mfma_f32_16x16x32_bf16 v[80:83], v[204:207], a[56:59], v[80:83]
s_waitcnt lgkmcnt(6)
v_mfma_f32_16x16x32_bf16 v[112:115], v[204:207], a[60:63], v[112:115]
ds_read_b64_tr_b16 v[204:205], v241 offset:8576
ds_read_b64_tr_b16 v[206:207], v241 offset:9600
v_mfma_f32_16x16x32_bf16 v[80:83], v[208:211], v[128:131], v[80:83]
buffer_store_dwordx4 v[230:233], v238, s[32:35], s96 offen offset:1024 sc1
v_mfma_f32_16x16x32_bf16 v[112:115], v[208:211], v[132:135], v[112:115]
ds_read_b64_tr_b16 v[208:209], v241 offset:17024
ds_read_b64_tr_b16 v[210:211], v241 offset:18048
v_mfma_f32_16x16x32_bf16 v[80:83], v[212:215], v[144:147], v[80:83]
v_cvt_pk_bf16_f32 v234, v104, v105
v_cvt_pk_bf16_f32 v236, v108, v109
v_mfma_f32_16x16x32_bf16 v[112:115], v[212:215], v[148:151], v[112:115]
ds_read_b64_tr_b16 v[212:213], v241 offset:25472
ds_read_b64_tr_b16 v[214:215], v241 offset:26496
v_mfma_f32_16x16x32_bf16 v[80:83], v[216:219], v[160:163], v[80:83]
v_cvt_pk_bf16_f32 v235, v106, v107
v_cvt_pk_bf16_f32 v237, v110, v111
v_mfma_f32_16x16x32_bf16 v[112:115], v[216:219], v[164:167], v[112:115]
ds_read_b64_tr_b16 v[216:217], v241 offset:33920
ds_read_b64_tr_b16 v[218:219], v241 offset:34944
v_mfma_f32_16x16x32_bf16 v[80:83], v[220:223], v[176:179], v[80:83]
s_waitcnt lgkmcnt(6)
v_mfma_f32_16x16x32_bf16 v[112:115], v[220:223], v[180:183], v[112:115]
ds_read_b64_tr_b16 v[220:221], v241 offset:42368
ds_read_b64_tr_b16 v[222:223], v241 offset:43392
v_mfma_f32_16x16x32_bf16 v[84:87], v[192:195], a[4:7], v[84:87]
v_permlane16_swap_b32 v234, v236
v_permlane16_swap_b32 v235, v237
v_mfma_f32_16x16x32_bf16 v[116:119], v[192:195], a[36:39], v[116:119]
ds_read_b64_tr_b16 v[192:193], v242 offset:50816
ds_read_b64_tr_b16 v[194:195], v242 offset:51840
v_mfma_f32_16x16x32_bf16 v[84:87], v[196:199], a[40:43], v[84:87]
v_mfma_f32_16x16x32_bf16 v[116:119], v[196:199], a[44:47], v[116:119]
ds_read_b64_tr_b16 v[196:197], v242 offset:59264
ds_read_b64_tr_b16 v[198:199], v242 offset:60288
v_mfma_f32_16x16x32_bf16 v[84:87], v[200:203], a[48:51], v[84:87]
buffer_store_dwordx4 v[234:237], v238, s[32:35], s97 offen offset:1024 sc1
v_mfma_f32_16x16x32_bf16 v[116:119], v[200:203], a[52:55], v[116:119]
ds_read_b64_tr_b16 v[200:201], v242 offset:128
ds_read_b64_tr_b16 v[202:203], v242 offset:1152
v_mfma_f32_16x16x32_bf16 v[84:87], v[204:207], a[56:59], v[84:87]
s_waitcnt lgkmcnt(6)
v_mfma_f32_16x16x32_bf16 v[116:119], v[204:207], a[60:63], v[116:119]
ds_read_b64_tr_b16 v[204:205], v242 offset:8576
ds_read_b64_tr_b16 v[206:207], v242 offset:9600
v_mfma_f32_16x16x32_bf16 v[84:87], v[208:211], v[128:131], v[84:87]
v_mfma_f32_16x16x32_bf16 v[116:119], v[208:211], v[132:135], v[116:119]
ds_read_b64_tr_b16 v[208:209], v242 offset:17024
ds_read_b64_tr_b16 v[210:211], v242 offset:18048
v_mfma_f32_16x16x32_bf16 v[84:87], v[212:215], v[144:147], v[84:87]
v_mfma_f32_16x16x32_bf16 v[116:119], v[212:215], v[148:151], v[116:119]
ds_read_b64_tr_b16 v[212:213], v242 offset:25472
ds_read_b64_tr_b16 v[214:215], v242 offset:26496
v_mfma_f32_16x16x32_bf16 v[84:87], v[216:219], v[160:163], v[84:87]
v_mfma_f32_16x16x32_bf16 v[116:119], v[216:219], v[164:167], v[116:119]
ds_read_b64_tr_b16 v[216:217], v242 offset:33920
ds_read_b64_tr_b16 v[218:219], v242 offset:34944
v_mfma_f32_16x16x32_bf16 v[84:87], v[220:223], v[176:179], v[84:87]
s_waitcnt lgkmcnt(6)
v_mfma_f32_16x16x32_bf16 v[116:119], v[220:223], v[180:183], v[116:119]
ds_read_b64_tr_b16 v[220:221], v242 offset:42368
ds_read_b64_tr_b16 v[222:223], v242 offset:43392
v_mfma_f32_16x16x32_bf16 v[88:91], v[192:195], a[4:7], v[88:91]
v_cvt_pk_bf16_f32 v230, v80, v81
v_cvt_pk_bf16_f32 v232, v84, v85
v_mfma_f32_16x16x32_bf16 v[120:123], v[192:195], a[36:39], v[120:123]
ds_read_b64_tr_b16 v[192:193], v243 offset:50816
ds_read_b64_tr_b16 v[194:195], v243 offset:51840
v_mfma_f32_16x16x32_bf16 v[88:91], v[196:199], a[40:43], v[88:91]
v_cvt_pk_bf16_f32 v231, v82, v83
v_cvt_pk_bf16_f32 v233, v86, v87
v_mfma_f32_16x16x32_bf16 v[120:123], v[196:199], a[44:47], v[120:123]
ds_read_b64_tr_b16 v[196:197], v243 offset:59264
ds_read_b64_tr_b16 v[198:199], v243 offset:60288
v_mfma_f32_16x16x32_bf16 v[88:91], v[200:203], a[48:51], v[88:91]
v_permlane16_swap_b32 v230, v232
v_permlane16_swap_b32 v231, v233
v_mfma_f32_16x16x32_bf16 v[120:123], v[200:203], a[52:55], v[120:123]
ds_read_b64_tr_b16 v[200:201], v243 offset:128
ds_read_b64_tr_b16 v[202:203], v243 offset:1152
v_mfma_f32_16x16x32_bf16 v[88:91], v[204:207], a[56:59], v[88:91]
s_waitcnt lgkmcnt(6)
v_mfma_f32_16x16x32_bf16 v[120:123], v[204:207], a[60:63], v[120:123]
ds_read_b64_tr_b16 v[204:205], v243 offset:8576
ds_read_b64_tr_b16 v[206:207], v243 offset:9600
v_mfma_f32_16x16x32_bf16 v[88:91], v[208:211], v[128:131], v[88:91]
buffer_store_dwordx4 v[230:233], v238, s[32:35], s96 offen offset:2048 sc1
v_mfma_f32_16x16x32_bf16 v[120:123], v[208:211], v[132:135], v[120:123]
ds_read_b64_tr_b16 v[208:209], v243 offset:17024
ds_read_b64_tr_b16 v[210:211], v243 offset:18048
v_mfma_f32_16x16x32_bf16 v[88:91], v[212:215], v[144:147], v[88:91]
v_cvt_pk_bf16_f32 v234, v112, v113
v_cvt_pk_bf16_f32 v236, v116, v117
v_mfma_f32_16x16x32_bf16 v[120:123], v[212:215], v[148:151], v[120:123]
ds_read_b64_tr_b16 v[212:213], v243 offset:25472
ds_read_b64_tr_b16 v[214:215], v243 offset:26496
v_mfma_f32_16x16x32_bf16 v[88:91], v[216:219], v[160:163], v[88:91]
v_cvt_pk_bf16_f32 v235, v114, v115
v_cvt_pk_bf16_f32 v237, v118, v119
v_mfma_f32_16x16x32_bf16 v[120:123], v[216:219], v[164:167], v[120:123]
ds_read_b64_tr_b16 v[216:217], v243 offset:33920
ds_read_b64_tr_b16 v[218:219], v243 offset:34944
v_mfma_f32_16x16x32_bf16 v[88:91], v[220:223], v[176:179], v[88:91]
s_waitcnt lgkmcnt(6)
v_mfma_f32_16x16x32_bf16 v[120:123], v[220:223], v[180:183], v[120:123]
ds_read_b64_tr_b16 v[220:221], v243 offset:42368
ds_read_b64_tr_b16 v[222:223], v243 offset:43392
v_mfma_f32_16x16x32_bf16 v[92:95], v[192:195], a[4:7], v[92:95]
v_permlane16_swap_b32 v234, v236
v_permlane16_swap_b32 v235, v237
v_mfma_f32_16x16x32_bf16 v[124:127], v[192:195], a[36:39], v[124:127]
v_mfma_f32_16x16x32_bf16 v[92:95], v[196:199], a[40:43], v[92:95]
v_mfma_f32_16x16x32_bf16 v[124:127], v[196:199], a[44:47], v[124:127]
v_mfma_f32_16x16x32_bf16 v[92:95], v[200:203], a[48:51], v[92:95]
buffer_store_dwordx4 v[234:237], v238, s[32:35], s97 offen offset:2048 sc1
v_mfma_f32_16x16x32_bf16 v[124:127], v[200:203], a[52:55], v[124:127]
v_mfma_f32_16x16x32_bf16 v[92:95], v[204:207], a[56:59], v[92:95]
s_waitcnt lgkmcnt(0)
v_mfma_f32_16x16x32_bf16 v[124:127], v[204:207], a[60:63], v[124:127]
v_mfma_f32_16x16x32_bf16 v[92:95], v[208:211], v[128:131], v[92:95]
v_mfma_f32_16x16x32_bf16 v[124:127], v[208:211], v[132:135], v[124:127]
v_mfma_f32_16x16x32_bf16 v[92:95], v[212:215], v[144:147], v[92:95]
v_mfma_f32_16x16x32_bf16 v[124:127], v[212:215], v[148:151], v[124:127]
v_mfma_f32_16x16x32_bf16 v[92:95], v[216:219], v[160:163], v[92:95]
v_mfma_f32_16x16x32_bf16 v[124:127], v[216:219], v[164:167], v[124:127]
v_mfma_f32_16x16x32_bf16 v[92:95], v[220:223], v[176:179], v[92:95]
v_mfma_f32_16x16x32_bf16 v[124:127], v[220:223], v[180:183], v[124:127]
s_nop 7
v_cvt_pk_bf16_f32 v230, v88, v89
v_cvt_pk_bf16_f32 v232, v92, v93
v_cvt_pk_bf16_f32 v231, v90, v91
v_cvt_pk_bf16_f32 v233, v94, v95
v_cvt_pk_bf16_f32 v234, v120, v121
.La5_LW3_4:
s_cbranch_execz .La5_LW3_R0
v_cvt_pk_bf16_f32 v236, v124, v125
v_cvt_pk_bf16_f32 v235, v122, v123
v_cvt_pk_bf16_f32 v237, v126, v127
v_permlane16_swap_b32 v230, v232
v_permlane16_swap_b32 v231, v233
buffer_store_dwordx4 v[230:233], v238, s[32:35], s96 offen offset:3072 sc1
v_permlane16_swap_b32 v234, v236
v_permlane16_swap_b32 v235, v237
buffer_store_dwordx4 v[234:237], v238, s[32:35], s97 offen offset:3072 sc1
s_lshl_b32 s91, s76, 5
s_add_u32 s91, s91, s72
s_lshl_b32 s20, s91, 9
s_add_u32 s20, s20, 2097152
s_lshr_b32 s93, s91, 1
s_lshl_b32 s93, s93, 15
s_and_b32 s94, s91, 1
s_lshl2_add_u32 s94, s94, s3
s_lshl_b32 s94, s94, 7
s_add_u32 s97, s93, s94
s_add_u32 s32, s26, 71303168
s_addc_u32 s33, s27, 0
s_and_b32 s33, s33, 0xffff
s_mov_b32 s34, 16777216
s_mov_b32 s35, 0x20000
s_add_u32 s44, s26, 171966464
s_addc_u32 s45, s27, 0
s_and_b32 s45, s45, 0xffff
s_mov_b32 s46, 950272
s_mov_b32 s47, 0x20000
s_add_u32 s56, s38, 0
s_addc_u32 s57, s39, 0
s_and_b32 s57, s57, 0xffff
s_mov_b32 s58, 4194304
s_mov_b32 s59, 0x20000
s_add_u32 s68, s16, 0
s_addc_u32 s69, s17, 0
s_and_b32 s69, s69, 0xffff
s_mov_b32 s70, 4194304
s_mov_b32 s71, 0x20000
v_and_b32 v230, 0x70, v255
v_lshrrev_b32 v231, 7, v255
v_lshl_add_u32 v230, v231, 10, v230
v_add_u32 v240, s97, v230
v_add_u32 v241, 8192, v240
v_add_u32 v242, 16384, v240
v_add_u32 v243, 24576, v240
v_mov_b32 v237, 0x3f803f80
v_mov_b32 v236, 0
s_waitcnt vmcnt(6)
v_dot2_f32_bf16 v236, v140, v237, v236
v_dot2_f32_bf16 v236, v141, v237, v236
v_dot2_f32_bf16 v236, v142, v237, v236
v_dot2_f32_bf16 v236, v143, v237, v236
v_dot2_f32_bf16 v236, v156, v237, v236
v_dot2_f32_bf16 v236, v157, v237, v236
v_dot2_f32_bf16 v236, v158, v237, v236
v_dot2_f32_bf16 v236, v159, v237, v236
v_dot2_f32_bf16 v236, v172, v237, v236
v_dot2_f32_bf16 v236, v173, v237, v236
v_dot2_f32_bf16 v236, v174, v237, v236
v_dot2_f32_bf16 v236, v175, v237, v236
v_dot2_f32_bf16 v236, v188, v237, v236
v_dot2_f32_bf16 v236, v189, v237, v236
v_dot2_f32_bf16 v236, v190, v237, v236
v_dot2_f32_bf16 v236, v191, v237, v236
v_lshlrev_b32 v0, 16, v136
v_and_b32 v1, 0xffff0000, v136
v_lshlrev_b32 v2, 16, v137
v_and_b32 v3, 0xffff0000, v137
v_lshlrev_b32 v4, 16, v138
v_and_b32 v5, 0xffff0000, v138
v_lshlrev_b32 v6, 16, v139
v_and_b32 v7, 0xffff0000, v139
v_lshlrev_b32 v8, 16, v152
v_and_b32 v9, 0xffff0000, v152
v_lshlrev_b32 v10, 16, v153
v_and_b32 v11, 0xffff0000, v153
v_lshlrev_b32 v12, 16, v154
v_and_b32 v13, 0xffff0000, v154
v_lshlrev_b32 v14, 16, v155
v_and_b32 v15, 0xffff0000, v155
v_lshlrev_b32 v16, 16, v168
v_and_b32 v17, 0xffff0000, v168
v_lshlrev_b32 v18, 16, v169
v_and_b32 v19, 0xffff0000, v169
v_lshlrev_b32 v20, 16, v170
v_and_b32 v21, 0xffff0000, v170
v_lshlrev_b32 v22, 16, v171
v_and_b32 v23, 0xffff0000, v171
v_lshlrev_b32 v24, 16, v184
v_and_b32 v25, 0xffff0000, v184
v_lshlrev_b32 v26, 16, v185
v_and_b32 v27, 0xffff0000, v185
v_lshlrev_b32 v28, 16, v186
v_and_b32 v29, 0xffff0000, v186
v_lshlrev_b32 v30, 16, v187
v_and_b32 v31, 0xffff0000, v187
s_nop 1
v_add_f32_dpp v236, v236, v236 quad_perm:[1,0,3,2] row_mask:0xf bank_mask:0xf
s_nop 1
v_add_f32_dpp v236, v236, v236 quad_perm:[2,3,0,1] row_mask:0xf bank_mask:0xf
s_nop 1
v_add_f32_dpp v236, v236, v236 row_ror:8 row_mask:0xf bank_mask:0xf
v_mov_b32 v230, v236
s_nop 1
v_permlane16_swap_b32 v230, v236
s_nop 1
v_add_f32 v236, v236, v230
v_mov_b32 v230, v236
s_nop 1
v_permlane32_swap_b32 v230, v236
s_nop 1
v_add_f32 v236, v236, v230
s_mov_b64 exec, 0
s_cmp_eq_u32 s3, 0
s_cbranch_scc1 .La5_LW4_0
s_cmp_eq_u32 s3, 1
s_cbranch_scc1 .La5_LW4_1
s_cmp_eq_u32 s3, 2
s_cbranch_scc1 .La5_LW4_2
s_branch .La5_LW4_3
.La5_LW4_R0:
s_mov_b64 exec, -1
s_waitcnt vmcnt(0)
buffer_store_dwordx2 v[252:253], v250, s[64:67], s92 offen sc1
s_mov_b32 s85, 99
s_cmp_eq_u32 s30, 0
s_nop 0
s_nop 0
s_cbranch_scc1 .La5_LT_ROWS
; ---- h = x + attn, LayerNorm statistics, router score, two best of eight rows
.La5_LT_ROWS:
.La5_LW3_5:
s_cbranch_execz .La5_LW3_R0
s_mov_b32 s96, 0
.La5_LT_P3:
buffer_load_dwordx2 v[250:251], v248, s[64:67], s20 offen sc1
s_waitcnt vmcnt(0)
.La5_LT_P2:
v_cmp_eq_u64 vcc, v[250:251], v[252:253]
s_add_u32 s96, s96, 1
s_lshr_b32 s92, vcc_lo, 1
s_and_b32 s92, s92, vcc_lo
s_lshr_b32 s93, s92, 2
s_and_b32 s92, s92, s93
s_lshr_b32 s93, s92, 4
s_and_b32 s92, s92, s93
s_lshr_b32 s93, s96, 20
s_mul_i32 s93, s93, 0x01010101
s_or_b32 s92, s92, s93
s_andn2_b32 s92, 0x01010101, s92
s_bcnt1_i32_b32 s93, s92
s_cmp_gt_u32 s93, 1
s_nop 0
s_nop 0
s_cbranch_scc1 .La5_LT_P3
s_or_b32 s93, s92, 0x01000000
s_ff1_i32_b32 s93, s93
s_lshl_b32 s80, s93, 19
s_xor_b32 s93, s80, 8388608
s_xor_b32 s94, s80, 12582912
s_xor_b32 s95, s80, 4194304
buffer_load_dwordx4 v[208:211], v240, s[32:35], s93 offen sc1
buffer_load_dwordx4 v[212:215], v241, s[32:35], s93 offen sc1
buffer_load_dwordx4 v[216:219], v242, s[32:35], s93 offen sc1
buffer_load_dwordx4 v[220:223], v243, s[32:35], s93 offen sc1
buffer_load_dwordx4 v[112:115], v240, s[32:35], s94 offen sc1
buffer_load_dwordx4 v[116:119], v241, s[32:35], s94 offen sc1
buffer_load_dwordx4 v[120:123], v242, s[32:35], s94 offen sc1
buffer_load_dwordx4 v[124:127], v243, s[32:35], s94 offen sc1
buffer_load_dwordx4 v[96:99], v240, s[32:35], s95 offen sc1
buffer_load_dwordx4 v[100:103], v241, s[32:35], s95 offen sc1
buffer_load_dwordx4 v[104:107], v242, s[32:35], s95 offen sc1
buffer_load_dwordx4 v[108:111], v243, s[32:35], s95 offen sc1
s_cmp_lg_u32 s92, 0
s_cbranch_scc1 .La5_LT_P4
buffer_load_dwordx4 v[192:195], v240, s[32:35], s80 offen sc1
buffer_load_dwordx4 v[196:199], v241, s[32:35], s80 offen sc1
buffer_load_dwordx4 v[200:203], v242, s[32:35], s80 offen sc1
buffer_load_dwordx4 v[204:207], v243, s[32:35], s80 offen sc1
.La5_LT_P4:
buffer_load_dwordx2 v[250:251], v248, s[64:67], s20 offen sc1
s_waitcnt vmcnt(8)
v_lshlrev_b32 v32, 16, v208
v_and_b32 v33, 0xffff0000, v208
v_lshlrev_b32 v230, 16, v112
v_and_b32 v231, 0xffff0000, v112
v_pk_add_f32 v[32:33], v[32:33], v[230:231]
v_lshlrev_b32 v34, 16, v209
v_and_b32 v35, 0xffff0000, v209
v_lshlrev_b32 v230, 16, v113
v_and_b32 v231, 0xffff0000, v113
v_pk_add_f32 v[34:35], v[34:35], v[230:231]
v_lshlrev_b32 v36, 16, v210
v_and_b32 v37, 0xffff0000, v210
v_lshlrev_b32 v230, 16, v114
v_and_b32 v231, 0xffff0000, v114
v_pk_add_f32 v[36:37], v[36:37], v[230:231]
v_lshlrev_b32 v38, 16, v211
v_and_b32 v39, 0xffff0000, v211
v_lshlrev_b32 v230, 16, v115
v_and_b32 v231, 0xffff0000, v115
v_pk_add_f32 v[38:39], v[38:39], v[230:231]
s_waitcnt vmcnt(7)
v_lshlrev_b32 v40, 16, v212
v_and_b32 v41, 0xffff0000, v212
v_lshlrev_b32 v230, 16, v116
v_and_b32 v231, 0xffff0000, v116
v_pk_add_f32 v[40:41], v[40:41], v[230:231]
v_lshlrev_b32 v42, 16, v213
v_and_b32 v43, 0xffff0000, v213
v_lshlrev_b32 v230, 16, v117
v_and_b32 v231, 0xffff0000, v117
v_pk_add_f32 v[42:43], v[42:43], v[230:231]
v_lshlrev_b32 v44, 16, v214
v_and_b32 v45, 0xffff0000, v214
v_lshlrev_b32 v230, 16, v118
v_and_b32 v231, 0xffff0000, v118
v_pk_add_f32 v[44:45], v[44:45], v[230:231]
v_lshlrev_b32 v46, 16, v215
v_and_b32 v47, 0xffff0000, v215
v_lshlrev_b32 v230, 16, v119
v_and_b32 v231, 0xffff0000, v119
v_pk_add_f32 v[46:47], v[46:47], v[230:231]
s_waitcnt vmcnt(6)
v_lshlrev_b32 v48, 16, v216
v_and_b32 v49, 0xffff0000, v216
v_lshlrev_b32 v230, 16, v120
v_and_b32 v231, 0xffff0000, v120
v_pk_add_f32 v[48:49], v[48:49], v[230:231]
v_lshlrev_b32 v50, 16, v217
v_and_b32 v51, 0xffff0000, v217
v_lshlrev_b32 v230, 16, v121
v_and_b32 v231, 0xffff0000, v121
v_pk_add_f32 v[50:51], v[50:51], v[230:231]
v_lshlrev_b32 v52, 16, v218
v_and_b32 v53, 0xffff0000, v218
v_lshlrev_b32 v230, 16, v122
v_and_b32 v231, 0xffff0000, v122
v_pk_add_f32 v[52:53], v[52:53], v[230:231]
v_lshlrev_b32 v54, 16, v219
v_and_b32 v55, 0xffff0000, v219
v_lshlrev_b32 v230, 16, v123
v_and_b32 v231, 0xffff0000, v123
v_pk_add_f32 v[54:55], v[54:55], v[230:231]
s_waitcnt vmcnt(5)
v_lshlrev_b32 v56, 16, v220
v_and_b32 v57, 0xffff0000, v220
v_lshlrev_b32 v230, 16, v124
v_and_b32 v231, 0xffff0000, v124
v_pk_add_f32 v[56:57], v[56:57], v[230:231]
v_lshlrev_b32 v58, 16, v221
v_and_b32 v59, 0xffff0000, v221
v_lshlrev_b32 v230, 16, v125
v_and_b32 v231, 0xffff0000, v125
v_pk_add_f32 v[58:59], v[58:59], v[230:231]
v_lshlrev_b32 v60, 16, v222
v_and_b32 v61, 0xffff0000, v222
v_lshlrev_b32 v230, 16, v126
v_and_b32 v231, 0xffff0000, v126
v_pk_add_f32 v[60:61], v[60:61], v[230:231]
v_lshlrev_b32 v62, 16, v223
v_and_b32 v63, 0xffff0000, v223
v_lshlrev_b32 v230, 16, v127
v_and_b32 v231, 0xffff0000, v127
v_pk_add_f32 v[62:63], v[62:63], v[230:231]
s_waitcnt vmcnt(1)
v_lshlrev_b32 v64, 16, v96
v_and_b32 v65, 0xffff0000, v96
v_lshlrev_b32 v66, 16, v97
v_and_b32 v67, 0xffff0000, v97
v_lshlrev_b32 v68, 16, v98
v_and_b32 v69, 0xffff0000, v98
v_lshlrev_b32 v70, 16, v99
v_and_b32 v71, 0xffff0000, v99
v_lshlrev_b32 v72, 16, v100
v_and_b32 v73, 0xffff0000, v100
v_lshlrev_b32 v74, 16, v101
v_and_b32 v75, 0xffff0000, v101
v_lshlrev_b32 v76, 16, v102
v_and_b32 v77, 0xffff0000, v102
v_lshlrev_b32 v78, 16, v103
v_and_b32 v79, 0xffff0000, v103
v_lshlrev_b32 v80, 16, v104
v_and_b32 v81, 0xffff0000, v104
v_lshlrev_b32 v82, 16, v105
v_and_b32 v83, 0xffff0000, v105
v_lshlrev_b32 v84, 16, v106
v_and_b32 v85, 0xffff0000, v106
v_lshlrev_b32 v86, 16, v107
v_and_b32 v87, 0xffff0000, v107
v_lshlrev_b32 v88, 16, v108
v_and_b32 v89, 0xffff0000, v108
v_lshlrev_b32 v90, 16, v109
v_and_b32 v91, 0xffff0000, v109
v_lshlrev_b32 v92, 16, v110
v_and_b32 v93, 0xffff0000, v110
v_lshlrev_b32 v94, 16, v111
v_and_b32 v95, 0xffff0000, v111
v_mov_b32 v232, 0
v_mov_b32 v233, 0
v_mov_b32 v234, 0
s_cmp_eq_u32 s92, 0
s_cbranch_scc1 .La5_LT_SUM
.La5_LT_P5:
s_waitcnt vmcnt(0)
v_cmp_eq_u64 vcc, v[250:251], v[252:253]
s_add_u32 s96, s96, 1
s_lshr_b32 s92, vcc_lo, 1
s_and_b32 s92, s92, vcc_lo
s_lshr_b32 s93, s92, 2
s_and_b32 s92, s92, s93
s_lshr_b32 s93, s92, 4
s_and_b32 s92, s92, s93
s_lshr_b32 s93, s96, 20
s_mul_i32 s93, s93, 0x01010101
s_or_b32 s92, s92, s93
s_andn2_b32 s92, 0x01010101, s92
s_cbranch_scc0 .La5_LT_P6
buffer_load_dwordx2 v[250:251], v248, s[64:67], s20 offen sc1
s_branch .La5_LT_P5
.La5_LT_P6:
buffer_load_dwordx4 v[192:195], v240, s[32:35], s80 offen sc1
buffer_load_dwordx4 v[196:199], v241, s[32:35], s80 offen sc1
buffer_load_dwordx4 v[200:203], v242, s[32:35], s80 offen sc1
buffer_load_dwordx4 v[204:207], v243, s[32:35], s80 offen sc1
.La5_LT_SUM:
s_waitcnt vmcnt(3)
v_lshlrev_b32 v208, 16, v192
v_and_b32 v209, 0xffff0000, v192
v_lshlrev_b32 v210, 16, v193
v_and_b32 v211, 0xffff0000, v193
v_lshlrev_b32 v212, 16, v194
v_and_b32 v213, 0xffff0000, v194
v_lshlrev_b32 v214, 16, v195
v_and_b32 v215, 0xffff0000, v195
v_pk_add_f32 v[208:209], v[208:209], v[64:65]
v_pk_add_f32 v[210:211], v[210:211], v[66:67]
v_pk_add_f32 v[212:213], v[212:213], v[68:69]
v_pk_add_f32 v[214:215], v[214:215], v[70:71]
v_pk_add_f32 v[208:209], v[208:209], v[32:33]
v_pk_add_f32 v[210:211], v[210:211], v[34:35]
v_pk_add_f32 v[212:213], v[212:213], v[36:37]
v_pk_add_f32 v[214:215], v[214:215], v[38:39]
v_pk_add_f32 v[208:209], v[208:209], v[0:1]
v_pk_add_f32 v[210:211], v[210:211], v[2:3]
v_pk_add_f32 v[212:213], v[212:213], v[4:5]
v_pk_add_f32 v[214:215], v[214:215], v[6:7]
v_cvt_pk_bf16_f32 v96, v208, v209
v_cvt_pk_bf16_f32 v97, v210, v211
v_cvt_pk_bf16_f32 v98, v212, v213
v_cvt_pk_bf16_f32 v99, v214, v215
v_dot2_f32_bf16 v232, v96, v237, v232
v_dot2_f32_bf16 v233, v96, v96, v233
v_dot2_f32_bf16 v234, v96, v140, v234
v_dot2_f32_bf16 v232, v97, v237, v232
v_dot2_f32_bf16 v233, v97, v97, v233
v_dot2_f32_bf16 v234, v97, v141, v234
v_dot2_f32_bf16 v232, v98, v237, v232
v_dot2_f32_bf16 v233, v98, v98, v233
v_dot2_f32_bf16 v234, v98, v142, v234
v_dot2_f32_bf16 v232, v99, v237, v232
v_dot2_f32_bf16 v233, v99, v99, v233
v_dot2_f32_bf16 v234, v99, v143, v234
s_waitcnt vmcnt(2)
v_lshlrev_b32 v208, 16, v196
v_and_b32 v209, 0xffff0000, v196
v_lshlrev_b32 v210, 16, v197
v_and_b32 v211, 0xffff0000, v197
v_lshlrev_b32 v212, 16, v198
v_and_b32 v213, 0xffff0000, v198
v_lshlrev_b32 v214, 16, v199
v_and_b32 v215, 0xffff0000, v199
v_pk_add_f32 v[208:209], v[208:209], v[72:73]
v_pk_add_f32 v[210:211], v[210:211], v[74:75]
v_pk_add_f32 v[212:213], v[212:213], v[76:77]
v_pk_add_f32 v[214:215], v[214:215], v[78:79]
v_pk_add_f32 v[208:209], v[208:209], v[40:41]
v_pk_add_f32 v[210:211], v[210:211], v[42:43]
v_pk_add_f32 v[212:213], v[212:213], v[44:45]
v_pk_add_f32 v[214:215], v[214:215], v[46:47]
v_pk_add_f32 v[208:209], v[208:209], v[8:9]
v_pk_add_f32 v[210:211], v[210:211], v[10:11]
v_pk_add_f32 v[212:213], v[212:213], v[12:13]
v_pk_add_f32 v[214:215], v[214:215], v[14:15]
v_cvt_pk_bf16_f32 v100, v208, v209
v_cvt_pk_bf16_f32 v101, v210, v211
v_cvt_pk_bf16_f32 v102, v212, v213
v_cvt_pk_bf16_f32 v103, v214, v215
v_dot2_f32_bf16 v232, v100, v237, v232
v_dot2_f32_bf16 v233, v100, v100, v233
v_dot2_f32_bf16 v234, v100, v156, v234
v_dot2_f32_bf16 v232, v101, v237, v232
v_dot2_f32_bf16 v233, v101, v101, v233
v_dot2_f32_bf16 v234, v101, v157, v234
v_dot2_f32_bf16 v232, v102, v237, v232
v_dot2_f32_bf16 v233, v102, v102, v233
v_dot2_f32_bf16 v234, v102, v158, v234
v_dot2_f32_bf16 v232, v103, v237, v232
v_dot2_f32_bf16 v233, v103, v103, v233
v_dot2_f32_bf16 v234, v103, v159, v234
s_waitcnt vmcnt(1)
v_lshlrev_b32 v208, 16, v200
v_and_b32 v209, 0xffff0000, v200
v_lshlrev_b32 v210, 16, v201
v_and_b32 v211, 0xffff0000, v201
v_lshlrev_b32 v212, 16, v202
v_and_b32 v213, 0xffff0000, v202
v_lshlrev_b32 v214, 16, v203
v_and_b32 v215, 0xffff0000, v203
v_pk_add_f32 v[208:209], v[208:209], v[80:81]
v_pk_add_f32 v[210:211], v[210:211], v[82:83]
v_pk_add_f32 v[212:213], v[212:213], v[84:85]
v_pk_add_f32 v[214:215], v[214:215], v[86:87]
v_pk_add_f32 v[208:209], v[208:209], v[48:49]
v_pk_add_f32 v[210:211], v[210:211], v[50:51]
v_pk_add_f32 v[212:213], v[212:213], v[52:53]
v_pk_add_f32 v[214:215], v[214:215], v[54:55]
v_pk_add_f32 v[208:209], v[208:209], v[16:17]
v_pk_add_f32 v[210:211], v[210:211], v[18:19]
v_pk_add_f32 v[212:213], v[212:213], v[20:21]
v_pk_add_f32 v[214:215], v[214:215], v[22:23]
v_cvt_pk_bf16_f32 v104, v208, v209
v_cvt_pk_bf16_f32 v105, v210, v211
v_cvt_pk_bf16_f32 v106, v212, v213
v_cvt_pk_bf16_f32 v107, v214, v215
v_dot2_f32_bf16 v232, v104, v237, v232
v_dot2_f32_bf16 v233, v104, v104, v233
v_dot2_f32_bf16 v234, v104, v172, v234
v_dot2_f32_bf16 v232, v105, v237, v232
v_dot2_f32_bf16 v233, v105, v105, v233
v_dot2_f32_bf16 v234, v105, v173, v234
v_dot2_f32_bf16 v232, v106, v237, v232
v_dot2_f32_bf16 v233, v106, v106, v233
v_dot2_f32_bf16 v234, v106, v174, v234
v_dot2_f32_bf16 v232, v107, v237, v232
v_dot2_f32_bf16 v233, v107, v107, v233
v_dot2_f32_bf16 v234, v107, v175, v234
s_waitcnt vmcnt(0)
v_lshlrev_b32 v208, 16, v204
v_and_b32 v209, 0xffff0000, v204
v_lshlrev_b32 v210, 16, v205
v_and_b32 v211, 0xffff0000, v205
v_lshlrev_b32 v212, 16, v206
v_and_b32 v213, 0xffff0000, v206
v_lshlrev_b32 v214, 16, v207
v_and_b32 v215, 0xffff0000, v207
v_pk_add_f32 v[208:209], v[208:209], v[88:89]
v_pk_add_f32 v[210:211], v[210:211], v[90:91]
v_pk_add_f32 v[212:213], v[212:213], v[92:93]
v_pk_add_f32 v[214:215], v[214:215], v[94:95]
v_pk_add_f32 v[208:209], v[208:209], v[56:57]
v_pk_add_f32 v[210:211], v[210:211], v[58:59]
v_pk_add_f32 v[212:213], v[212:213], v[60:61]
v_pk_add_f32 v[214:215], v[214:215], v[62:63]
v_pk_add_f32 v[208:209], v[208:209], v[24:25]
v_pk_add_f32 v[210:211], v[210:211], v[26:27]
v_pk_add_f32 v[212:213], v[212:213], v[28:29]
v_pk_add_f32 v[214:215], v[214:215], v[30:31]
v_cvt_pk_bf16_f32 v108, v208, v209
v_cvt_pk_bf16_f32 v109, v210, v211
v_cvt_pk_bf16_f32 v110, v212, v213
v_cvt_pk_bf16_f32 v111, v214, v215
v_dot2_f32_bf16 v232, v108, v237, v232
v_dot2_f32_bf16 v233, v108, v108, v233
v_dot2_f32_bf16 v234, v108, v188, v234
v_dot2_f32_bf16 v232, v109, v237, v232
v_dot2_f32_bf16 v233, v109, v109, v233
v_dot2_f32_bf16 v234, v109, v189, v234
v_dot2_f32_bf16 v232, v110, v237, v232
v_dot2_f32_bf16 v233, v110, v110, v233
v_dot2_f32_bf16 v234, v110, v190, v234
v_dot2_f32_bf16 v232, v111, v237, v232
v_dot2_f32_bf16 v233, v111, v111, v233
v_dot2_f32_bf16 v234, v111, v191, v234
s_waitcnt vmcnt(0)
s_lshl2_add_u32 s92, s91, s3
s_lshl_b32 s92, s92, 1
s_sub_u32 s92, 0xffff, s92
v_add_f32_dpp v232, v232, v232 quad_perm:[1,0,3,2] row_mask:0xf bank_mask:0xf
v_add_f32_dpp v233, v233, v233 quad_perm:[1,0,3,2] row_mask:0xf bank_mask:0xf
v_add_f32_dpp v234, v234, v234 quad_perm:[1,0,3,2] row_mask:0xf bank_mask:0xf
v_add_f32_dpp v232, v232, v232 quad_perm:[2,3,0,1] row_mask:0xf bank_mask:0xf
v_add_f32_dpp v233, v233, v233 quad_perm:[2,3,0,1] row_mask:0xf bank_mask:0xf
v_add_f32_dpp v234, v234, v234 quad_perm:[2,3,0,1] row_mask:0xf bank_mask:0xf
v_add_f32_dpp v232, v232, v232 row_ror:8 row_mask:0xf bank_mask:0xf
v_add_f32_dpp v233, v233, v233 row_ror:8 row_mask:0xf bank_mask:0xf
v_add_f32_dpp v234, v234, v234 row_ror:8 row_mask:0xf bank_mask:0xf
v_mov_b32 v230, v232
v_mov_b32 v231, v233
v_mov_b32 v235, v234
v_permlane16_swap_b32 v230, v232
v_permlane16_swap_b32 v231, v233
v_permlane16_swap_b32 v235, v234
v_add_f32 v232, v232, v230
v_add_f32 v233, v233, v231
v_add_f32 v234, v234, v235
v_mov_b32 v230, v232
v_mov_b32 v231, v233
v_mov_b32 v235, v234
v_permlane32_swap_b32 v230, v232
v_permlane32_swap_b32 v231, v233
v_permlane32_swap_b32 v235, v234
v_add_f32 v232, v232, v230
v_add_f32 v233, v233, v231
v_add_f32 v234, v234, v235
v_mul_f32 v244, 0x3a800000, v232
v_mul_f32 v233, 0x3a800000, v233
v_fma_f32 v230, -v244, v244, v233
v_mul_f32 v233, 0x3a800000, v233
v_cmp_nge_f32 vcc, v230, v233
v_bfe_u32 v235, v255, 6, 1
v_sub_u32 v235, s92, v235
s_cbranch_vccnz .La5_LT_VAR
.La5_LT_VOK:
v_cvt_pk_bf16_f32 v244, v244, v244
v_lshlrev_b32 v244, 16, v244
v_cvt_pk_bf16_f32 v230, v230, v230
v_lshlrev_b32 v230, 16, v230
v_fma_f32 v234, -v244, v236, v234
v_add_f32 v230, 0x3727c5ac, v230
v_cvt_pk_bf16_f32 v230, v230, v230
v_lshlrev_b32 v230, 16, v230
v_sqrt_f32 v230, v230
v_lshrrev_b32 v232, 3, v255
v_cvt_pk_bf16_f32 v230, v230, v230
v_lshlrev_b32 v231, 16, v230
v_rcp_f32 v246, v231
v_lshrrev_b32 v230, 16, v230
v_or_b32 v233, v244, v230
v_mul_f32 v234, v234, v246
s_lshl_b32 s93, s3, 4
v_cvt_pk_bf16_f32 v234, v234, v234
v_and_b32 v230, 8, v232
v_ashrrev_i32 v232, 31, v234
v_or_b32 v232, 0x80000000, v232
v_xor_b32 v232, v232, v234
v_and_b32 v232, 0xffff0000, v232
v_add_u32 v230, s93, v230
v_or_b32 v232, v232, v235
ds_write_b64 v230, v[232:233] offset:0
v_lshrrev_b32 v230, 1, v255
v_and_b32_e64 v230, 56, v230
s_waitcnt lgkmcnt(0)
s_barrier
s_cmp_lg_u32 s3, 0
s_cbranch_scc1 .La5_LT_OUT
ds_read_b64 v[232:233], v230 offset:0
s_lshl3_add_u32 s92, s72, s76
s_lshl_b32 s92, s92, 4
v_lshrrev_b32 v235, 7, v255
v_lshlrev_b32 v235, 12, v235
s_waitcnt lgkmcnt(0)
v_mov_b32 v234, v232
s_nop 1
v_max_u32_dpp v234, v234, v234 quad_perm:[1,0,3,2] row_mask:0xf bank_mask:0xf
s_nop 1
v_max_u32_dpp v234, v234, v234 quad_perm:[2,3,0,1] row_mask:0xf bank_mask:0xf
s_nop 1
v_max_u32_dpp v234, v234, v234 row_half_mirror row_mask:0xf bank_mask:0xf
s_nop 1
v_cmp_eq_u32 vcc, v232, v234
v_add_u32 v231, 8, v235
s_nop 0
v_cndmask_b32 v234, v232, 0, vcc
v_cndmask_b32 v235, v231, v235, vcc
s_nop 0
v_max_u32_dpp v234, v234, v234 quad_perm:[1,0,3,2] row_mask:0xf bank_mask:0xf
s_nop 1
v_max_u32_dpp v234, v234, v234 quad_perm:[2,3,0,1] row_mask:0xf bank_mask:0xf
s_nop 1
v_max_u32_dpp v234, v234, v234 row_half_mirror row_mask:0xf bank_mask:0xf
s_nop 1
v_cmp_ge_u32 vcc, v232, v234
s_nop 1
s_mov_b64 exec, vcc
buffer_store_dwordx2 v[232:233], v235, s[44:47], s92 offen sc1
s_mov_b64 exec, -1
; ---- out = h + .La5_LN(h) of the own rows; FFN weights into MFMA order
.La5_LT_OUT:
v_mul_f32 v244, v244, v246
v_mov_b32 v247, v246
v_xor_b32 v244, 0x80000000, v244
v_mov_b32 v245, v244
v_lshlrev_b32 v230, 16, v96
v_and_b32 v231, 0xffff0000, v96
v_pk_fma_f32 v[232:233], v[230:231], v[246:247], v[244:245]
v_cvt_pk_bf16_f32 v232, v232, v233
v_lshlrev_b32 v234, 16, v232
v_and_b32 v235, 0xffff0000, v232
v_pk_add_f32 v[230:231], v[230:231], v[234:235]
v_cvt_pk_bf16_f32 v192, v230, v231
v_lshlrev_b32 v230, 16, v97
v_and_b32 v231, 0xffff0000, v97
v_pk_fma_f32 v[232:233], v[230:231], v[246:247], v[244:245]
v_cvt_pk_bf16_f32 v232, v232, v233
v_lshlrev_b32 v234, 16, v232
v_and_b32 v235, 0xffff0000, v232
v_pk_add_f32 v[230:231], v[230:231], v[234:235]
v_cvt_pk_bf16_f32 v193, v230, v231
v_lshlrev_b32 v230, 16, v98
v_and_b32 v231, 0xffff0000, v98
v_pk_fma_f32 v[232:233], v[230:231], v[246:247], v[244:245]
v_cvt_pk_bf16_f32 v232, v232, v233
v_lshlrev_b32 v234, 16, v232
v_and_b32 v235, 0xffff0000, v232
v_pk_add_f32 v[230:231], v[230:231], v[234:235]
v_cvt_pk_bf16_f32 v194, v230, v231
v_lshlrev_b32 v230, 16, v99
v_and_b32 v231, 0xffff0000, v99
v_pk_fma_f32 v[232:233], v[230:231], v[246:247], v[244:245]
v_cvt_pk_bf16_f32 v232, v232, v233
v_lshlrev_b32 v234, 16, v232
v_and_b32 v235, 0xffff0000, v232
v_pk_add_f32 v[230:231], v[230:231], v[234:235]
v_cvt_pk_bf16_f32 v195, v230, v231
v_lshlrev_b32 v230, 16, v100
v_and_b32 v231, 0xffff0000, v100
v_pk_fma_f32 v[232:233], v[230:231], v[246:247], v[244:245]
v_cvt_pk_bf16_f32 v232, v232, v233
v_lshlrev_b32 v234, 16, v232
v_and_b32 v235, 0xffff0000, v232
v_pk_add_f32 v[230:231], v[230:231], v[234:235]
v_cvt_pk_bf16_f32 v196, v230, v231
v_lshlrev_b32 v230, 16, v101
v_and_b32 v231, 0xffff0000, v101
v_pk_fma_f32 v[232:233], v[230:231], v[246:247], v[244:245]
v_cvt_pk_bf16_f32 v232, v232, v233
v_lshlrev_b32 v234, 16, v232
v_and_b32 v235, 0xffff0000, v232
v_pk_add_f32 v[230:231], v[230:231], v[234:235]
v_cvt_pk_bf16_f32 v197, v230, v231
v_lshlrev_b32 v230, 16, v102
v_and_b32 v231, 0xffff0000, v102
v_pk_fma_f32 v[232:233], v[230:231], v[246:247], v[244:245]
v_cvt_pk_bf16_f32 v232, v232, v233
v_lshlrev_b32 v234, 16, v232
v_and_b32 v235, 0xffff0000, v232
v_pk_add_f32 v[230:231], v[230:231], v[234:235]
v_cvt_pk_bf16_f32 v198, v230, v231
v_lshlrev_b32 v230, 16, v103
v_and_b32 v231, 0xffff0000, v103
v_pk_fma_f32 v[232:233], v[230:231], v[246:247], v[244:245]
v_cvt_pk_bf16_f32 v232, v232, v233
v_lshlrev_b32 v234, 16, v232
v_and_b32 v235, 0xffff0000, v232
v_pk_add_f32 v[230:231], v[230:231], v[234:235]
v_cvt_pk_bf16_f32 v199, v230, v231
v_lshlrev_b32 v230, 16, v104
v_and_b32 v231, 0xffff0000, v104
v_pk_fma_f32 v[232:233], v[230:231], v[246:247], v[244:245]
v_cvt_pk_bf16_f32 v232, v232, v233
v_lshlrev_b32 v234, 16, v232
v_and_b32 v235, 0xffff0000, v232
v_pk_add_f32 v[230:231], v[230:231], v[234:235]
v_cvt_pk_bf16_f32 v200, v230, v231
v_lshlrev_b32 v230, 16, v105
v_and_b32 v231, 0xffff0000, v105
v_pk_fma_f32 v[232:233], v[230:231], v[246:247], v[244:245]
.La5_LW4_0:
s_cbranch_execz .La5_LW4_4
v_cvt_pk_bf16_f32 v232, v232, v233
v_lshlrev_b32 v234, 16, v232
v_and_b32 v235, 0xffff0000, v232
v_pk_add_f32 v[230:231], v[230:231], v[234:235]
v_cvt_pk_bf16_f32 v201, v230, v231
v_lshlrev_b32 v230, 16, v106
v_and_b32 v231, 0xffff0000, v106
v_pk_fma_f32 v[232:233], v[230:231], v[246:247], v[244:245]
v_cvt_pk_bf16_f32 v232, v232, v233
v_lshlrev_b32 v234, 16, v232
v_and_b32 v235, 0xffff0000, v232
v_pk_add_f32 v[230:231], v[230:231], v[234:235]
v_cvt_pk_bf16_f32 v202, v230, v231
v_lshlrev_b32 v230, 16, v107
v_and_b32 v231, 0xffff0000, v107
v_pk_fma_f32 v[232:233], v[230:231], v[246:247], v[244:245]
v_cvt_pk_bf16_f32 v232, v232, v233
v_lshlrev_b32 v234, 16, v232
v_and_b32 v235, 0xffff0000, v232
v_pk_add_f32 v[230:231], v[230:231], v[234:235]
v_cvt_pk_bf16_f32 v203, v230, v231
v_lshlrev_b32 v230, 16, v108
v_and_b32 v231, 0xffff0000, v108
v_pk_fma_f32 v[232:233], v[230:231], v[246:247], v[244:245]
v_cvt_pk_bf16_f32 v232, v232, v233
v_lshlrev_b32 v234, 16, v232
v_and_b32 v235, 0xffff0000, v232
v_pk_add_f32 v[230:231], v[230:231], v[234:235]
v_cvt_pk_bf16_f32 v204, v230, v231
v_lshlrev_b32 v230, 16, v109
v_and_b32 v231, 0xffff0000, v109
v_pk_fma_f32 v[232:233], v[230:231], v[246:247], v[244:245]
v_cvt_pk_bf16_f32 v232, v232, v233
v_lshlrev_b32 v234, 16, v232
v_and_b32 v235, 0xffff0000, v232
v_pk_add_f32 v[230:231], v[230:231], v[234:235]
v_cvt_pk_bf16_f32 v205, v230, v231
v_lshlrev_b32 v230, 16, v110
v_and_b32 v231, 0xffff0000, v110
v_pk_fma_f32 v[232:233], v[230:231], v[246:247], v[244:245]
v_cvt_pk_bf16_f32 v232, v232, v233
v_lshlrev_b32 v234, 16, v232
v_and_b32 v235, 0xffff0000, v232
v_pk_add_f32 v[230:231], v[230:231], v[234:235]
v_cvt_pk_bf16_f32 v206, v230, v231
v_lshlrev_b32 v230, 16, v111
v_and_b32 v231, 0xffff0000, v111
v_pk_fma_f32 v[232:233], v[230:231], v[246:247], v[244:245]
v_cvt_pk_bf16_f32 v232, v232, v233
v_lshlrev_b32 v234, 16, v232
v_and_b32 v235, 0xffff0000, v232
v_pk_add_f32 v[230:231], v[230:231], v[234:235]
v_cvt_pk_bf16_f32 v207, v230, v231
s_cmp_eq_u32 s30, 0
s_cbranch_scc1 .La5_LT_SEL
s_mov_b32 s94, 0x05040100
s_mov_b32 s95, 0x07060302
s_waitcnt vmcnt(0)
v_accvgpr_read_b32 v230, a224
v_accvgpr_read_b32 v231, a228
v_perm_b32 v128, v231, v230, s94
v_perm_b32 v132, v231, v230, s95
v_accvgpr_read_b32 v230, a232
v_accvgpr_read_b32 v231, a236
v_perm_b32 v129, v231, v230, s94
v_perm_b32 v133, v231, v230, s95
v_accvgpr_read_b32 v230, a240
v_accvgpr_read_b32 v231, a244
v_perm_b32 v130, v231, v230, s94
v_perm_b32 v134, v231, v230, s95
v_accvgpr_read_b32 v230, a248
v_accvgpr_read_b32 v231, a252
v_perm_b32 v131, v231, v230, s94
v_perm_b32 v135, v231, v230, s95
v_accvgpr_read_b32 v230, a225
v_accvgpr_read_b32 v231, a229
v_perm_b32 v136, v231, v230, s94
v_perm_b32 v140, v231, v230, s95
v_accvgpr_read_b32 v230, a233
v_accvgpr_read_b32 v231, a237
v_perm_b32 v137, v231, v230, s94
v_perm_b32 v141, v231, v230, s95
v_accvgpr_read_b32 v230, a241
v_accvgpr_read_b32 v231, a245
v_perm_b32 v138, v231, v230, s94
v_perm_b32 v142, v231, v230, s95
v_accvgpr_read_b32 v230, a249
v_accvgpr_read_b32 v231, a253
v_perm_b32 v139, v231, v230, s94
v_perm_b32 v143, v231, v230, s95
v_accvgpr_read_b32 v230, a226
v_accvgpr_read_b32 v231, a230
v_perm_b32 v144, v231, v230, s94
v_perm_b32 v148, v231, v230, s95
v_accvgpr_read_b32 v230, a234
v_accvgpr_read_b32 v231, a238
v_perm_b32 v145, v231, v230, s94
v_perm_b32 v149, v231, v230, s95
v_accvgpr_read_b32 v230, a242
v_accvgpr_read_b32 v231, a246
v_perm_b32 v146, v231, v230, s94
v_perm_b32 v150, v231, v230, s95
v_accvgpr_read_b32 v230, a250
v_accvgpr_read_b32 v231, a254
v_perm_b32 v147, v231, v230, s94
v_perm_b32 v151, v231, v230, s95
v_accvgpr_read_b32 v230, a227
v_accvgpr_read_b32 v231, a231
v_perm_b32 v152, v231, v230, s94
v_perm_b32 v156, v231, v230, s95
v_accvgpr_read_b32 v230, a235
v_accvgpr_read_b32 v231, a239
v_perm_b32 v153, v231, v230, s94
v_perm_b32 v157, v231, v230, s95
v_accvgpr_read_b32 v230, a243
v_accvgpr_read_b32 v231, a247
v_perm_b32 v154, v231, v230, s94
v_perm_b32 v158, v231, v230, s95
v_accvgpr_read_b32 v230, a251
v_accvgpr_read_b32 v231, a255
v_perm_b32 v155, v231, v230, s94
v_perm_b32 v159, v231, v230, s95
v_accvgpr_read_b32 v230, a64
v_accvgpr_read_b32 v231, a68
v_perm_b32 v160, v231, v230, s94
v_perm_b32 v164, v231, v230, s95
v_accvgpr_read_b32 v230, a72
v_accvgpr_read_b32 v231, a76
v_perm_b32 v161, v231, v230, s94
v_perm_b32 v165, v231, v230, s95
v_accvgpr_read_b32 v230, a80
v_accvgpr_read_b32 v231, a84
v_perm_b32 v162, v231, v230, s94
v_perm_b32 v166, v231, v230, s95
v_accvgpr_read_b32 v230, a88
v_accvgpr_read_b32 v231, a92
v_perm_b32 v163, v231, v230, s94
v_perm_b32 v167, v231, v230, s95
v_accvgpr_read_b32 v230, a65
v_accvgpr_read_b32 v231, a69
v_perm_b32 v168, v231, v230, s94
v_perm_b32 v172, v231, v230, s95
v_accvgpr_read_b32 v230, a73
v_accvgpr_read_b32 v231, a77
v_perm_b32 v169, v231, v230, s94
v_perm_b32 v173, v231, v230, s95
v_accvgpr_read_b32 v230, a81
v_accvgpr_read_b32 v231, a85
v_perm_b32 v170, v231, v230, s94
v_perm_b32 v174, v231, v230, s95
v_accvgpr_read_b32 v230, a89
v_accvgpr_read_b32 v231, a93
v_perm_b32 v171, v231, v230, s94
v_perm_b32 v175, v231, v230, s95
v_accvgpr_read_b32 v230, a66
v_accvgpr_read_b32 v231, a70
v_perm_b32 v176, v231, v230, s94
v_perm_b32 v180, v231, v230, s95
v_accvgpr_read_b32 v230, a74
v_accvgpr_read_b32 v231, a78
v_perm_b32 v177, v231, v230, s94
v_perm_b32 v181, v231, v230, s95
v_accvgpr_read_b32 v230, a82
v_accvgpr_read_b32 v231, a86
v_perm_b32 v178, v231, v230, s94
v_perm_b32 v182, v231, v230, s95
v_accvgpr_read_b32 v230, a90
v_accvgpr_read_b32 v231, a94
v_perm_b32 v179, v231, v230, s94
v_perm_b32 v183, v231, v230, s95
v_accvgpr_read_b32 v230, a67
v_accvgpr_read_b32 v231, a71
v_perm_b32 v184, v231, v230, s94
v_perm_b32 v188, v231, v230, s95
v_accvgpr_read_b32 v230, a75
v_accvgpr_read_b32 v231, a79
v_perm_b32 v185, v231, v230, s94
v_perm_b32 v189, v231, v230, s95
v_accvgpr_read_b32 v230, a83
v_accvgpr_read_b32 v231, a87
v_perm_b32 v186, v231, v230, s94
v_perm_b32 v190, v231, v230, s95
v_accvgpr_read_b32 v230, a91
v_accvgpr_read_b32 v231, a95
v_perm_b32 v187, v231, v230, s94
v_perm_b32 v191, v231, v230, s95
s_and_b32 s92, s3, 1
s_lshr_b32 s93, s3, 1
v_lshrrev_b32 v230, 6, v255
v_and_b32 v231, 48, v255
v_lshl_add_u32 v240, v230, 10, v231
s_lshl_b32 s96, s92, 14
v_add_u32 v240, s96, v240
s_lshl_b32 s96, s92, 10
v_add_u32 v241, s96, v255
s_lshl_b32 s97, s3, 10
v_add_u32 v208, s97, v255
v_bfe_u32 v230, v255, 4, 1
v_bfe_u32 v231, v255, 6, 2
v_lshrrev_b32 v232, 8, v255
v_lshlrev_b32 v209, 11, v230
v_lshl_add_u32 v209, v231, 8, v209
v_lshl_add_u32 v209, v232, 6, v209
v_add_u32 v209, s96, v209
v_bfe_u32 v233, v255, 4, 2
v_mul_u32_u24 v210, 640, v233
v_lshl_add_u32 v210, v231, 3, v210
s_mul_i32 s96, s93, 2560
s_lshl_b32 s92, s92, 5
s_add_u32 s96, s96, s92
v_add_u32 v210, s96, v210
v_cmp_eq_u32 s[98:99], v231, v232
v_mul_u32_u24 v211, 5, v255
s_lshl_b32 s96, s3, 7
v_lshrrev_b32 v212, 3, v255
v_and_b32 v212, 62, v212
v_add_u32 v212, s96, v212
v_lshlrev_b32 v234, 6, v230
v_lshl_add_u32 v234, v232, 4, v234
v_add_u32 v234, s96, v234
v_mov_b32 v235, 1024
v_cmp_eq_u32 vcc, 0, v231
s_nop 1
v_cndmask_b32 v216, v235, v234, vcc
v_cmp_eq_u32 vcc, 1, v231
s_nop 1
v_cndmask_b32 v217, v235, v234, vcc
v_cmp_eq_u32 vcc, 2, v231
s_nop 1
v_cndmask_b32 v218, v235, v234, vcc
v_cmp_eq_u32 vcc, 3, v231
s_nop 1
v_cndmask_b32 v219, v235, v234, vcc
v_mov_b32 v220, 0
v_mov_b32 v221, 0
v_mov_b32 v222, 0
v_mov_b32 v223, 0
ds_write_b128 v235, v[220:223] offset:10240
s_lshl_b32 s92, s100, 13
s_add_u32 s80, s92, 32768
s_add_u32 s20, s80, 4096
; ---- top two rows of the sequence; SwiGLU slice of 32 hidden units
.La5_LT_SEL:
.La5_LW4_1:
s_cbranch_execz .La5_LW4_R0
s_and_b32 s97, s2, 7
s_lshl_b32 s97, s97, 12
s_cmp_lg_u32 s3, 0
s_cbranch_scc1 .La5_LT_S2
s_mov_b32 s96, 0
.La5_LT_S1:
s_cmp_lg_u32 s30, 0
s_cbranch_scc1 .La5_LT_S0
s_sleep 8
.La5_LT_S0:
buffer_load_dwordx4 v[224:227], v255, s[44:47], s97 offen offset:0 sc1
buffer_load_dwordx4 v[232:235], v255, s[44:47], s97 offen offset:1024 sc1
buffer_load_dwordx4 v[244:247], v255, s[44:47], s97 offen offset:2048 sc1
buffer_load_dwordx4 v[248:251], v255, s[44:47], s97 offen offset:3072 sc1
s_add_u32 s96, s96, 1
s_lshr_b32 s92, s96, 20
s_waitcnt vmcnt(3)
v_min3_u32 v228, v224, v225, v226
v_min_u32 v228, v227, v228
s_waitcnt vmcnt(2)
v_min3_u32 v228, v232, v233, v228
v_min3_u32 v228, v234, v235, v228
s_waitcnt vmcnt(1)
v_min3_u32 v228, v244, v245, v228
v_min3_u32 v228, v246, v247, v228
s_waitcnt vmcnt(0)
v_min3_u32 v228, v248, v249, v228
v_min3_u32 v228, v250, v251, v228
v_cmp_ne_u32 vcc, 0, v228
v_max_u32 v229, v224, v232
v_max_u32 v230, v244, v248
s_cbranch_scc1 .La5_LT_S3
s_cmp_eq_u64 vcc, -1
s_nop 0
s_cbranch_scc0 .La5_LT_S1
.La5_LT_S3:
v_max_u32 v228, v229, v230
v_min_u32 v229, v229, v230
v_mov_b32 v231, v228
v_min_u32 v224, v224, v232
v_min_u32 v244, v244, v248
v_max3_u32 v229, v229, v224, v244
v_max3_u32 v230, v226, v234, v246
v_max3_u32 v229, v229, v230, v250
s_nop 1
v_max_u32_dpp v231, v231, v231 row_shr:1 row_mask:0xf bank_mask:0xf
s_nop 1
v_max_u32_dpp v231, v231, v231 row_shr:2 row_mask:0xf bank_mask:0xf
s_nop 1
v_max_u32_dpp v231, v231, v231 row_shr:4 row_mask:0xf bank_mask:0xf
s_nop 1
v_max_u32_dpp v231, v231, v231 row_shr:8 row_mask:0xf bank_mask:0xf
s_nop 1
v_max_u32_dpp v231, v231, v231 row_bcast:15 row_mask:0xa bank_mask:0xf
s_nop 1
v_max_u32_dpp v231, v231, v231 row_bcast:31 row_mask:0xc bank_mask:0xf
s_nop 0
v_readlane_b32 s82, v231, 63
s_nop 1
v_cmp_eq_u32 vcc, s82, v228
v_mov_b32 v230, s82
v_mov_b32 v224, 0
v_cndmask_b32 v228, v228, v229, vcc
s_nop 1
v_max_u32_dpp v228, v228, v228 row_shr:1 row_mask:0xf bank_mask:0xf
s_nop 1
v_max_u32_dpp v228, v228, v228 row_shr:2 row_mask:0xf bank_mask:0xf
s_nop 1
v_max_u32_dpp v228, v228, v228 row_shr:4 row_mask:0xf bank_mask:0xf
s_nop 1
v_max_u32_dpp v228, v228, v228 row_shr:8 row_mask:0xf bank_mask:0xf
s_nop 1
v_max_u32_dpp v228, v228, v228 row_bcast:15 row_mask:0xa bank_mask:0xf
s_nop 1
v_max_u32_dpp v228, v228, v228 row_bcast:31 row_mask:0xc bank_mask:0xf
s_nop 0
v_readlane_b32 s81, v228, 63
s_nop 1
v_mov_b32 v231, s81
ds_write_b64 v224, v[230:231] offset:64
.La5_LT_S2:
v_mov_b32 v224, 0
s_waitcnt lgkmcnt(0)
s_barrier
ds_read_b64 v[228:229], v224 offset:64
s_waitcnt lgkmcnt(0)
v_readfirstlane_b32 s82, v228
v_readfirstlane_b32 s81, v229
s_nop 0
s_andn2_b32 s84, 0x7ff, s82
s_andn2_b32 s21, 0x7ff, s81
s_cmp_eq_u32 s30, 0
s_cbranch_scc1 .La5_LT_ST
s_cmp_lt_u32 s3, 2
s_cselect_b32 s91, s84, s21
s_cselect_b32 s85, s82, s81
s_lshr_b32 s92, s91, 4
s_lshl_b32 s92, s92, 15
s_and_b32 s93, s91, 15
s_lshl_b32 s93, s93, 6
s_add_u32 s92, s92, s93
s_add_u32 s93, s92, 4194304
s_add_u32 s94, s92, 8388608
s_add_u32 s95, s92, 12582912
buffer_load_dwordx4 v[224:227], v240, s[32:35], s92 offen sc1
buffer_load_dwordx4 v[228:231], v240, s[32:35], s93 offen sc1
buffer_load_dwordx4 v[232:235], v240, s[32:35], s94 offen sc1
buffer_load_dwordx4 v[244:247], v240, s[32:35], s95 offen sc1
s_lshl_b32 s96, s91, 11
s_and_b32 s92, s91, 0xf8
s_lshl4_add_u32 s97, s92, s97
s_lshr_b32 s92, s91, 8
s_lshl4_add_u32 s97, s92, s97
buffer_load_dwordx4 v[248:251], v241, s[68:71], s96 offen
buffer_load_dwordx4 v[220:223], off, s[44:47], s97 sc1
s_waitcnt vmcnt(4)
v_lshlrev_b32 v236, 16, v224
v_and_b32 v237, 0xffff0000, v224
v_lshlrev_b32 v238, 16, v228
v_and_b32 v239, 0xffff0000, v228
v_pk_add_f32 v[236:237], v[236:237], v[238:239]
v_lshlrev_b32 v242, 16, v225
v_and_b32 v243, 0xffff0000, v225
v_lshlrev_b32 v238, 16, v229
v_and_b32 v239, 0xffff0000, v229
v_pk_add_f32 v[242:243], v[242:243], v[238:239]
v_lshlrev_b32 v252, 16, v226
v_and_b32 v253, 0xffff0000, v226
v_lshlrev_b32 v238, 16, v230
v_and_b32 v239, 0xffff0000, v230
v_pk_add_f32 v[252:253], v[252:253], v[238:239]
v_lshlrev_b32 v214, 16, v227
v_and_b32 v215, 0xffff0000, v227
v_lshlrev_b32 v238, 16, v231
v_and_b32 v239, 0xffff0000, v231
v_pk_add_f32 v[214:215], v[214:215], v[238:239]
s_waitcnt vmcnt(2)
v_lshlrev_b32 v224, 16, v232
v_and_b32 v225, 0xffff0000, v232
v_lshlrev_b32 v226, 16, v244
v_and_b32 v227, 0xffff0000, v244
v_pk_add_f32 v[224:225], v[224:225], v[226:227]
v_pk_add_f32 v[236:237], v[236:237], v[224:225]
v_lshlrev_b32 v224, 16, v233
v_and_b32 v225, 0xffff0000, v233
v_lshlrev_b32 v226, 16, v245
v_and_b32 v227, 0xffff0000, v245
v_pk_add_f32 v[224:225], v[224:225], v[226:227]
v_pk_add_f32 v[242:243], v[242:243], v[224:225]
v_lshlrev_b32 v224, 16, v234
v_and_b32 v225, 0xffff0000, v234
v_lshlrev_b32 v226, 16, v246
v_and_b32 v227, 0xffff0000, v246
v_pk_add_f32 v[224:225], v[224:225], v[226:227]
v_pk_add_f32 v[252:253], v[252:253], v[224:225]
v_lshlrev_b32 v224, 16, v235
v_and_b32 v225, 0xffff0000, v235
v_lshlrev_b32 v226, 16, v247
v_and_b32 v227, 0xffff0000, v247
v_pk_add_f32 v[224:225], v[224:225], v[226:227]
v_pk_add_f32 v[214:215], v[214:215], v[224:225]
s_waitcnt vmcnt(0)
v_cmp_eq_u32 vcc, s85, v220
s_nop 1
v_cndmask_b32 v220, v223, v221, vcc
v_lshlrev_b32 v222, 16, v220
v_rcp_f32 v222, v222
v_and_b32 v220, 0xffff0000, v220
v_mul_f32 v220, v220, v222
v_mov_b32 v223, v222
v_xor_b32 v220, 0x80000000, v220
v_mov_b32 v221, v220
v_lshlrev_b32 v224, 16, v248
v_and_b32 v225, 0xffff0000, v248
v_pk_add_f32 v[236:237], v[236:237], v[224:225]
v_cvt_pk_bf16_f32 v224, v236, v237
v_lshlrev_b32 v226, 16, v224
v_and_b32 v227, 0xffff0000, v224
v_pk_fma_f32 v[226:227], v[226:227], v[222:223], v[220:221]
v_cvt_pk_bf16_f32 v228, v226, v227
v_lshlrev_b32 v224, 16, v249
v_and_b32 v225, 0xffff0000, v249
v_pk_add_f32 v[242:243], v[242:243], v[224:225]
v_cvt_pk_bf16_f32 v224, v242, v243
v_lshlrev_b32 v226, 16, v224
v_and_b32 v227, 0xffff0000, v224
v_pk_fma_f32 v[226:227], v[226:227], v[222:223], v[220:221]
v_cvt_pk_bf16_f32 v229, v226, v227
v_lshlrev_b32 v224, 16, v250
v_and_b32 v225, 0xffff0000, v250
v_pk_add_f32 v[252:253], v[252:253], v[224:225]
v_cvt_pk_bf16_f32 v224, v252, v253
v_lshlrev_b32 v226, 16, v224
v_and_b32 v227, 0xffff0000, v224
v_pk_fma_f32 v[226:227], v[226:227], v[222:223], v[220:221]
v_cvt_pk_bf16_f32 v230, v226, v227
v_lshlrev_b32 v224, 16, v251
v_and_b32 v225, 0xffff0000, v251
v_pk_add_f32 v[214:215], v[214:215], v[224:225]
v_cvt_pk_bf16_f32 v224, v214, v215
v_lshlrev_b32 v226, 16, v224
v_and_b32 v227, 0xffff0000, v224
v_pk_fma_f32 v[226:227], v[226:227], v[222:223], v[220:221]
v_cvt_pk_bf16_f32 v231, v226, v227
ds_write_b128 v208, v[228:231] offset:1024
s_waitcnt lgkmcnt(0)
s_barrier
ds_read_b128 v[224:227], v209 offset:1024
ds_read_b128 v[232:235], v209 offset:1040
ds_read_b128 v[244:247], v209 offset:1056
ds_read_b128 v[248:251], v209 offset:1072
s_waitcnt lgkmcnt(3)
v_mfma_f32_16x16x32_bf16 a[0:3], v[224:227], a[96:99], 0
v_mfma_f32_16x16x32_bf16 a[4:7], v[224:227], a[100:103], 0
v_mfma_f32_16x16x32_bf16 a[8:11], v[224:227], a[104:107], 0
v_mfma_f32_16x16x32_bf16 a[12:15], v[224:227], a[108:111], 0
v_mfma_f32_16x16x32_bf16 a[16:19], v[224:227], a[112:115], 0
v_mfma_f32_16x16x32_bf16 a[20:23], v[224:227], a[116:119], 0
v_mfma_f32_16x16x32_bf16 a[24:27], v[224:227], a[120:123], 0
v_mfma_f32_16x16x32_bf16 a[28:31], v[224:227], a[124:127], 0
s_waitcnt lgkmcnt(2)
v_mfma_f32_16x16x32_bf16 a[0:3], v[232:235], a[128:131], a[0:3]
v_mfma_f32_16x16x32_bf16 a[4:7], v[232:235], a[132:135], a[4:7]
v_mfma_f32_16x16x32_bf16 a[8:11], v[232:235], a[136:139], a[8:11]
v_mfma_f32_16x16x32_bf16 a[12:15], v[232:235], a[140:143], a[12:15]
v_mfma_f32_16x16x32_bf16 a[16:19], v[232:235], a[144:147], a[16:19]
v_mfma_f32_16x16x32_bf16 a[20:23], v[232:235], a[148:151], a[20:23]
v_mfma_f32_16x16x32_bf16 a[24:27], v[232:235], a[152:155], a[24:27]
v_mfma_f32_16x16x32_bf16 a[28:31], v[232:235], a[156:159], a[28:31]
s_waitcnt lgkmcnt(1)
v_mfma_f32_16x16x32_bf16 a[0:3], v[244:247], a[160:163], a[0:3]
v_mfma_f32_16x16x32_bf16 a[4:7], v[244:247], a[164:167], a[4:7]
v_mfma_f32_16x16x32_bf16 a[8:11], v[244:247], a[168:171], a[8:11]
v_mfma_f32_16x16x32_bf16 a[12:15], v[244:247], a[172:175], a[12:15]
v_mfma_f32_16x16x32_bf16 a[16:19], v[244:247], a[176:179], a[16:19]
v_mfma_f32_16x16x32_bf16 a[20:23], v[244:247], a[180:183], a[20:23]
v_mfma_f32_16x16x32_bf16 a[24:27], v[244:247], a[184:187], a[24:27]
v_mfma_f32_16x16x32_bf16 a[28:31], v[244:247], a[188:191], a[28:31]
s_waitcnt lgkmcnt(0)
v_mfma_f32_16x16x32_bf16 a[0:3], v[248:251], a[192:195], a[0:3]
v_mfma_f32_16x16x32_bf16 a[4:7], v[248:251], a[196:199], a[4:7]
v_mfma_f32_16x16x32_bf16 a[8:11], v[248:251], a[200:203], a[8:11]
v_mfma_f32_16x16x32_bf16 a[12:15], v[248:251], a[204:207], a[12:15]
v_mfma_f32_16x16x32_bf16 a[16:19], v[248:251], a[208:211], a[16:19]
v_mfma_f32_16x16x32_bf16 a[20:23], v[248:251], a[212:215], a[20:23]
v_mfma_f32_16x16x32_bf16 a[24:27], v[248:251], a[216:219], a[24:27]
v_mfma_f32_16x16x32_bf16 a[28:31], v[248:251], a[220:223], a[28:31]
s_mov_b64 exec, s[98:99]
ds_write_b64 v210, a[0:1] offset:5120
ds_write_b64 v210, a[4:5] offset:5200
ds_write_b64 v210, a[8:9] offset:5280
ds_write_b64 v210, a[12:13] offset:5360
ds_write_b64 v210, a[16:17] offset:5440
s_nop 3
ds_write_b64 v210, a[20:21] offset:5520
s_nop 3
ds_write_b64 v210, a[24:25] offset:5600
s_nop 3
ds_write_b64 v210, a[28:29] offset:5680
s_mov_b64 exec, -1
s_waitcnt lgkmcnt(0)
s_barrier
ds_read_b128 v[224:227], v211 offset:5120
ds_read_b128 v[232:235], v211 offset:5136
ds_read_b128 v[244:247], v211 offset:5152
ds_read_b128 v[248:251], v211 offset:5168
s_waitcnt lgkmcnt(2)
v_pk_add_f32 v[224:225], v[224:225], v[226:227]
v_pk_add_f32 v[232:233], v[232:233], v[234:235]
s_waitcnt lgkmcnt(0)
v_pk_add_f32 v[244:245], v[244:245], v[246:247]
v_pk_add_f32 v[248:249], v[248:249], v[250:251]
v_pk_add_f32 v[224:225], v[224:225], v[232:233]
v_pk_add_f32 v[244:245], v[244:245], v[248:249]
v_pk_add_f32 v[224:225], v[224:225], v[244:245]
v_cvt_pk_bf16_f32 v224, v224, v225
v_mov_b32 v226, v224
s_nop 1
v_permlane32_swap_b32 v224, v226
s_nop 1
v_lshlrev_b32 v228, 16, v224
v_and_b32 v229, 0xffff0000, v224
v_lshlrev_b32 v232, 16, v226
v_and_b32 v233, 0xffff0000, v226
v_mul_f32 v230, 0xbfb8aa3b, v228
v_mul_f32 v231, 0xbfb8aa3b, v229
v_exp_f32 v230, v230
v_exp_f32 v231, v231
v_add_f32 v230, 1.0, v230
v_add_f32 v231, 1.0, v231
v_rcp_f32 v230, v230
v_rcp_f32 v231, v231
s_nop 0
v_pk_mul_f32 v[228:229], v[228:229], v[230:231]
v_cvt_pk_bf16_f32 v228, v228, v229
v_lshlrev_b32 v230, 16, v228
v_and_b32 v231, 0xffff0000, v228
v_pk_mul_f32 v[230:231], v[230:231], v[232:233]
v_cvt_pk_bf16_f32 v228, v230, v231
ds_write_b16 v212, v228 offset:10240
ds_write_b16_d16_hi v212, v228 offset:10304
s_waitcnt lgkmcnt(0)
ds_read_b128 v[224:227], v216 offset:10240
ds_read_b128 v[232:235], v217 offset:10240
ds_read_b128 v[244:247], v218 offset:10240
ds_read_b128 v[248:251], v219 offset:10240
s_waitcnt lgkmcnt(3)
v_mfma_f32_16x16x32_bf16 v[0:3], v[224:227], v[128:131], 0
v_mfma_f32_16x16x32_bf16 v[4:7], v[224:227], v[132:135], 0
v_mfma_f32_16x16x32_bf16 v[8:11], v[224:227], v[136:139], 0
v_mfma_f32_16x16x32_bf16 v[12:15], v[224:227], v[140:143], 0
s_waitcnt lgkmcnt(2)
v_mfma_f32_16x16x32_bf16 v[0:3], v[232:235], v[144:147], v[0:3]
v_mfma_f32_16x16x32_bf16 v[4:7], v[232:235], v[148:151], v[4:7]
v_mfma_f32_16x16x32_bf16 v[8:11], v[232:235], v[152:155], v[8:11]
v_mfma_f32_16x16x32_bf16 v[12:15], v[232:235], v[156:159], v[12:15]
s_waitcnt lgkmcnt(1)
v_mfma_f32_16x16x32_bf16 v[0:3], v[244:247], v[160:163], v[0:3]
v_mfma_f32_16x16x32_bf16 v[4:7], v[244:247], v[164:167], v[4:7]
v_mfma_f32_16x16x32_bf16 v[8:11], v[244:247], v[168:171], v[8:11]
v_mfma_f32_16x16x32_bf16 v[12:15], v[244:247], v[172:175], v[12:15]
s_waitcnt lgkmcnt(0)
v_mfma_f32_16x16x32_bf16 v[0:3], v[248:251], v[176:179], v[0:3]
v_mfma_f32_16x16x32_bf16 v[4:7], v[248:251], v[180:183], v[4:7]
v_mfma_f32_16x16x32_bf16 v[8:11], v[248:251], v[184:187], v[8:11]
v_mfma_f32_16x16x32_bf16 v[12:15], v[248:251], v[188:191], v[12:15]
s_nop 7
v_or_b32 v16, 1, v0
v_or_b32 v20, 1, v1
v_or_b32 v17, 1, v4
v_or_b32 v21, 1, v5
v_or_b32 v18, 1, v8
v_or_b32 v22, 1, v9
v_or_b32 v19, 1, v12
v_or_b32 v23, 1, v13
buffer_store_dwordx4 v[16:19], v208, s[44:47], s80 offen sc1
buffer_store_dwordx4 v[20:23], v208, s[44:47], s20 offen sc1
; ---- rows that were not routed leave; final sums of the routed rows
.La5_LT_ST:
.La5_LW4_2:
s_cbranch_execz .La5_LW4_R0
s_lshl_b32 s92, s76, 5
s_add_u32 s92, s92, s72
s_lshl2_add_u32 s92, s92, s3
s_lshl_b32 s93, s92, 1
v_bfe_u32 v230, v255, 6, 1
v_add_u32 v231, s93, v230
v_cmp_ne_u32 vcc, s84, v231
v_cmp_ne_u32 s[94:95], s21, v231
v_lshrrev_b32 v232, 7, v255
v_and_b32_e64 v233, 48, v255
v_lshl_add_u32 v232, v232, 6, v233
v_lshl_add_u32 v232, v230, 11, v232
s_lshl_b32 s92, s92, 12
s_and_b64 exec, vcc, s[94:95]
s_cbranch_scc0 .La5_LT_F0
buffer_store_dwordx4 v[192:195], v232, s[56:59], s92 offen offset:0 sc1
buffer_store_dwordx4 v[196:199], v232, s[56:59], s92 offen offset:512 sc1
buffer_store_dwordx4 v[200:203], v232, s[56:59], s92 offen offset:1024 sc1
buffer_store_dwordx4 v[204:207], v232, s[56:59], s92 offen offset:1536 sc1
.La5_LT_F0:
s_mov_b64 exec, -1
s_cmp_eq_u32 s75, 1
s_cbranch_scc1 .La5_LT_END
s_lshr_b32 s92, s2, 6
s_lshl2_add_u32 s92, s92, s74
s_and_b32 s93, s2, 7
s_lshl3_add_u32 s91, s92, s93
s_cmp_lt_u32 s91, 64
s_cselect_b32 s84, s84, s21
s_cselect_b32 s82, s82, s81
s_bfe_u32 s92, s91, 0x30003
s_and_b32 s93, s91, 3
s_lshl2_add_u32 s96, s92, s93
s_and_b32 s85, s91, 4
s_lshl_b32 s85, s85, 1
v_bfe_u32 v0, v255, 6, 2
v_bfe_u32 v1, v255, 4, 2
v_lshlrev_b32 v1, 1, v1
v_lshl_add_u32 v0, v0, 4, v1
v_add_u32 v0, s85, v0
s_lshl_b32 s85, s96, 10
v_add_u32 v1, s85, v0
s_lshl_b32 s85, s96, 6
v_add_u32 v0, s85, v0
s_lshr_b32 s92, s84, 4
s_lshl_b32 s92, s92, 15
s_and_b32 s93, s84, 15
s_lshl_b32 s93, s93, 6
s_add_u32 s92, s92, s93
s_add_u32 s93, s92, 4194304
s_add_u32 s94, s92, 8388608
s_add_u32 s95, s92, 12582912
buffer_load_ushort v4, v1, s[32:35], s92 offen sc1
buffer_load_ushort v5, v1, s[32:35], s93 offen sc1
buffer_load_ushort v6, v1, s[32:35], s94 offen sc1
buffer_load_ushort v7, v1, s[32:35], s95 offen sc1
.La5_LW4_3:
s_cbranch_execz .La5_LW4_R0
s_lshl_b32 s21, s84, 11
s_and_b32 s92, s84, 0xf8
s_lshl4_add_u32 s97, s92, s97
s_lshr_b32 s92, s84, 8
s_lshl4_add_u32 s97, s92, s97
buffer_load_ushort v8, v0, s[68:71], s21 offen
buffer_load_dwordx4 v[12:15], off, s[44:47], s97 sc1
v_lshrrev_b32 v2, 6, v255
v_lshlrev_b32 v2, 13, v2
v_and_b32 v3, 48, v255
v_add_u32 v2, v2, v3
s_lshl_b32 s92, s3, 17
s_lshl_b32 s93, s91, 6
s_add_u32 s92, s92, s93
s_add_u32 s94, s92, 32768
s_cmp_eq_u32 s3, 3
s_cselect_b32 s95, 0, 524288
s_cselect_b32 s96, 0, -1
s_add_u32 s95, s95, s94
s_ashr_i32 s92, s82, 31
s_orn2_b32 s92, 0x80000000, s92
s_xor_b32 s92, s92, s82
s_and_b32 s92, s92, 0xffff0000
v_mov_b32 v9, 0xbfb8aa3b
v_mul_f32 v9, s92, v9
v_exp_f32 v9, v9
v_lshrrev_b32 v3, 8, v255
s_lshl_b32 s92, s3, 4
v_add_f32 v9, 1.0, v9
v_lshl_add_u32 v3, v3, 2, s92
v_rcp_f32 v9, v9
v_and_b32 v10, 48, v255
v_mul_u32_u24 v10, 20, v10
v_add_u32 v3, v3, v10
v_cvt_pk_bf16_f32 v9, v9, v9
v_lshlrev_b32 v9, 16, v9
v_and_b32 v10, 0xf0, v255
v_mul_u32_u24 v10, 5, v10
s_waitcnt vmcnt(0)
v_cmp_eq_u32 vcc, s82, v12
s_nop 1
v_cndmask_b32 v12, v15, v13, vcc
v_lshlrev_b32 v14, 16, v12
v_rcp_f32 v14, v14
v_and_b32 v12, 0xffff0000, v12
v_mul_f32 v12, v12, v14
v_mov_b32 v15, v14
v_xor_b32 v12, 0x80000000, v12
v_mov_b32 v13, v12
v_lshlrev_b32 v4, 16, v4
v_lshlrev_b32 v5, 16, v5
v_lshlrev_b32 v6, 16, v6
v_lshlrev_b32 v7, 16, v7
v_lshlrev_b32 v8, 16, v8
v_add_f32 v4, v4, v5
v_add_f32 v6, v6, v7
v_add_f32 v4, v4, v6
v_add_f32 v4, v4, v8
v_cvt_pk_bf16_f32 v4, v4, v4
v_lshlrev_b32 v4, 16, v4
v_fma_f32 v5, v4, v14, v12
v_cvt_pk_bf16_f32 v5, v5, v5
v_lshlrev_b32 v5, 16, v5
s_sleep 40
s_mov_b32 s93, 0
.La5_LT_Y:
buffer_load_dwordx4 v[16:19], v2, s[44:47], s94 offen sc1
buffer_load_dwordx4 v[20:23], v2, s[44:47], s95 offen sc1
s_add_u32 s93, s93, 1
s_lshr_b32 s92, s93, 20
s_waitcnt vmcnt(0)
v_min3_u32 v24, v16, v17, v18
v_min3_u32 v25, v20, v21, v22
v_min3_u32 v24, v24, v19, v23
v_min_u32 v24, v24, v25
v_cmp_ne_u32 vcc, 0, v24
v_and_b32 v20, s96, v20
v_and_b32 v21, s96, v21
v_and_b32 v22, s96, v22
v_and_b32 v23, s96, v23
s_cbranch_scc1 .La5_LT_Y1
s_cmp_eq_u64 vcc, -1
s_nop 0
s_cbranch_scc0 .La5_LT_Y
.La5_LT_Y1:
v_pk_add_f32 v[16:17], v[16:17], v[20:21]
v_pk_add_f32 v[18:19], v[18:19], v[22:23]
s_nop 0
v_add_f32_dpp v16, v16, v16 row_ror:4 row_mask:0xf bank_mask:0xf
v_add_f32_dpp v17, v17, v17 row_ror:4 row_mask:0xf bank_mask:0xf
v_add_f32_dpp v18, v18, v18 row_ror:4 row_mask:0xf bank_mask:0xf
v_add_f32_dpp v19, v19, v19 row_ror:4 row_mask:0xf bank_mask:0xf
v_add_f32_dpp v16, v16, v16 row_ror:8 row_mask:0xf bank_mask:0xf
v_add_f32_dpp v17, v17, v17 row_ror:8 row_mask:0xf bank_mask:0xf
v_add_f32_dpp v18, v18, v18 row_ror:8 row_mask:0xf bank_mask:0xf
v_add_f32_dpp v19, v19, v19 row_ror:8 row_mask:0xf bank_mask:0xf
ds_write_b32 v3, v16 offset:5120
ds_write_b32 v3, v17 offset:5200
ds_write_b32 v3, v18 offset:5280
ds_write_b32 v3, v19 offset:5360
s_waitcnt lgkmcnt(0)
s_barrier
ds_read_b128 v[16:19], v10 offset:5120
ds_read_b128 v[20:23], v10 offset:5136
ds_read_b128 v[24:27], v10 offset:5152
ds_read_b128 v[28:31], v10 offset:5168
s_waitcnt lgkmcnt(2)
v_pk_add_f32 v[16:17], v[16:17], v[18:19]
v_pk_add_f32 v[20:21], v[20:21], v[22:23]
s_waitcnt lgkmcnt(0)
v_pk_add_f32 v[24:25], v[24:25], v[26:27]
v_pk_add_f32 v[28:29], v[28:29], v[30:31]
v_pk_add_f32 v[16:17], v[16:17], v[20:21]
v_pk_add_f32 v[24:25], v[24:25], v[28:29]
v_pk_add_f32 v[16:17], v[16:17], v[24:25]
v_add_f32 v16, v16, v17
v_cvt_pk_bf16_f32 v16, v16, v16
v_lshlrev_b32 v16, 16, v16
v_mul_f32 v16, v16, v9
v_cvt_pk_bf16_f32 v16, v16, v16
v_lshlrev_b32 v16, 16, v16
v_add_f32 v16, v16, v5
v_cvt_pk_bf16_f32 v16, v16, v16
v_lshlrev_b32 v16, 16, v16
v_add_f32 v16, v16, v4
v_cvt_pk_bf16_f32 v16, v16, v16
s_cmp_lg_u32 s3, 0
s_cbranch_scc1 .La5_LT_END
s_mov_b64 exec, 0xffff
buffer_store_short v16, v0, s[56:59], s21 offen
s_mov_b64 exec, -1
.La5_LT_END:
s_nop 0
s_nop 0
.La5_LW4_4:
s_cbranch_execz .La5_LW4_R0
s_lshl2_add_u32 s92, s2, s3
s_cmp_lg_u32 s92, 992
s_cbranch_scc1 .La5_LT_E1
buffer_store_dwordx2 v[252:253], off, s[64:67], 0 sc1
.La5_LT_E1:
s_branch .La5_LEND
; ---- fallback: the earlier 32 rows against all their keys with the true running maximum (online softmax)
.La5_LA_SLOW0:
s_waitcnt lgkmcnt(0)
s_nop 7
s_nop 7
v_accvgpr_write_b32 a160, v192
v_accvgpr_write_b32 a161, v193
v_accvgpr_write_b32 a162, v194
v_accvgpr_write_b32 a163, v195
v_accvgpr_write_b32 a164, v196
v_accvgpr_write_b32 a165, v197
v_accvgpr_write_b32 a166, v198
v_accvgpr_write_b32 a167, v199
v_accvgpr_write_b32 a168, v200
v_accvgpr_write_b32 a169, v201
v_accvgpr_write_b32 a170, v202
v_accvgpr_write_b32 a171, v203
v_accvgpr_write_b32 a172, v204
v_accvgpr_write_b32 a173, v205
v_accvgpr_write_b32 a174, v206
v_accvgpr_write_b32 a175, v207
v_accvgpr_write_b32 a176, v208
v_accvgpr_write_b32 a177, v209
v_accvgpr_write_b32 a178, v210
v_accvgpr_write_b32 a179, v211
v_accvgpr_write_b32 a180, v212
v_accvgpr_write_b32 a181, v213
v_accvgpr_write_b32 a182, v214
v_accvgpr_write_b32 a183, v215
v_accvgpr_write_b32 a184, v216
v_accvgpr_write_b32 a185, v217
v_accvgpr_write_b32 a186, v218
v_accvgpr_write_b32 a187, v219
v_accvgpr_write_b32 a188, v220
v_accvgpr_write_b32 a189, v221
v_accvgpr_write_b32 a190, v222
v_accvgpr_write_b32 a191, v223
s_lshl_b32 s96, s72, 19
s_add_u32 s96, s96, 20971520
s_add_u32 s92, s26, s96
s_addc_u32 s93, s27, 0
s_and_b32 s93, s93, 0xffff
s_mov_b32 s94, 17301504
s_mov_b32 s95, 0x20000
v_mov_b32 v0, 0
v_mov_b32 v1, 0
v_mov_b32 v2, 0
v_mov_b32 v3, 0
v_mov_b32 v4, 0
v_mov_b32 v5, 0
v_mov_b32 v6, 0
v_mov_b32 v7, 0
v_mov_b32 v8, 0
v_mov_b32 v9, 0
v_mov_b32 v10, 0
v_mov_b32 v11, 0
v_mov_b32 v12, 0
v_mov_b32 v13, 0
v_mov_b32 v14, 0
v_mov_b32 v15, 0
v_mov_b32 v16, 0
v_mov_b32 v17, 0
v_mov_b32 v18, 0
v_mov_b32 v19, 0
v_mov_b32 v20, 0
v_mov_b32 v21, 0
v_mov_b32 v22, 0
v_mov_b32 v23, 0
v_mov_b32 v24, 0
v_mov_b32 v25, 0
v_mov_b32 v26, 0
v_mov_b32 v27, 0
v_mov_b32 v28, 0
v_mov_b32 v29, 0
v_mov_b32 v30, 0
v_mov_b32 v31, 0
v_mov_b32 v32, 0
v_mov_b32 v33, 0
v_mov_b32 v34, 0
v_mov_b32 v35, 0
v_mov_b32 v36, 0
v_mov_b32 v37, 0
v_mov_b32 v38, 0
v_mov_b32 v39, 0
v_mov_b32 v40, 0
v_mov_b32 v41, 0
v_mov_b32 v42, 0
v_mov_b32 v43, 0
v_mov_b32 v44, 0
v_mov_b32 v45, 0
v_mov_b32 v46, 0
v_mov_b32 v47, 0
v_mov_b32 v48, 0
v_mov_b32 v49, 0
v_mov_b32 v50, 0
v_mov_b32 v51, 0
v_mov_b32 v52, 0
v_mov_b32 v53, 0
v_mov_b32 v54, 0
v_mov_b32 v55, 0
v_mov_b32 v56, 0
v_mov_b32 v57, 0
v_mov_b32 v58, 0
v_mov_b32 v59, 0
v_mov_b32 v60, 0
v_mov_b32 v61, 0
v_mov_b32 v62, 0
v_mov_b32 v63, 0
v_mov_b32 v216, 0xff61b1e6
v_mov_b32 v217, 0xff61b1e6
v_mov_b32 v218, 0
v_mov_b32 v219, 0
s_mov_b32 s7, 0
.La5_LA_SLOW0_L:
s_lshl_b32 s96, s7, 13
s_add_u32 s97, s96, 4096
buffer_load_dwordx4 a[128:131], v255, s[92:95], s96 offen offset:0 sc1
buffer_load_dwordx4 a[132:135], v255, s[92:95], s96 offen offset:1024 sc1
buffer_load_dwordx4 a[136:139], v255, s[92:95], s96 offen offset:2048 sc1
buffer_load_dwordx4 a[140:143], v255, s[92:95], s96 offen offset:3072 sc1
buffer_load_dwordx4 a[144:147], v255, s[92:95], s97 offen offset:0 sc1
buffer_load_dwordx4 a[148:151], v255, s[92:95], s97 offen offset:1024 sc1
buffer_load_dwordx4 a[152:155], v255, s[92:95], s97 offen offset:2048 sc1
buffer_load_dwordx4 a[156:159], v255, s[92:95], s97 offen offset:3072 sc1
s_lshl_b32 s96, s76, 3
s_sub_u32 s96, s96, s7
s_add_u32 s96, s96, s3
s_lshl_b32 s96, s96, 5
v_bfe_u32 v220, v255, 4, 4
v_lshrrev_b32 v221, 8, v255
v_lshlrev_b32 v221, 2, v221
v_sub_u32 v220, v220, v221
v_add_u32 v220, s96, v220
v_mov_b32 v222, 0xceabfb8f
s_waitcnt vmcnt(0)
v_mfma_f32_16x16x32_bf16 v[192:195], a[128:131], v[128:131], 0
v_mfma_f32_16x16x32_bf16 v[192:195], a[132:135], v[132:135], v[192:195]
v_mfma_f32_16x16x32_bf16 v[192:195], a[136:139], v[136:139], v[192:195]
v_mfma_f32_16x16x32_bf16 v[192:195], a[140:143], v[140:143], v[192:195]
v_mfma_f32_16x16x32_bf16 v[196:199], a[144:147], v[128:131], 0
v_mfma_f32_16x16x32_bf16 v[196:199], a[148:151], v[132:135], v[196:199]
v_mfma_f32_16x16x32_bf16 v[196:199], a[152:155], v[136:139], v[196:199]
v_mfma_f32_16x16x32_bf16 v[196:199], a[156:159], v[140:143], v[196:199]
v_mfma_f32_16x16x32_bf16 v[200:203], a[128:131], v[144:147], 0
v_mfma_f32_16x16x32_bf16 v[200:203], a[132:135], v[148:151], v[200:203]
v_mfma_f32_16x16x32_bf16 v[200:203], a[136:139], v[152:155], v[200:203]
v_mfma_f32_16x16x32_bf16 v[200:203], a[140:143], v[156:159], v[200:203]
v_mfma_f32_16x16x32_bf16 v[204:207], a[144:147], v[144:147], 0
v_mfma_f32_16x16x32_bf16 v[204:207], a[148:151], v[148:151], v[204:207]
v_mfma_f32_16x16x32_bf16 v[204:207], a[152:155], v[152:155], v[204:207]
v_mfma_f32_16x16x32_bf16 v[204:207], a[156:159], v[156:159], v[204:207]
s_nop 7
s_nop 7
s_lshl_b32 s96, s7, 13
s_add_u32 s96, s96, 16777216
s_add_u32 s97, s96, 4096
buffer_load_dwordx4 a[128:131], v255, s[92:95], s96 offen offset:0 sc1
buffer_load_dwordx4 a[132:135], v255, s[92:95], s96 offen offset:1024 sc1
buffer_load_dwordx4 a[136:139], v255, s[92:95], s96 offen offset:2048 sc1
buffer_load_dwordx4 a[140:143], v255, s[92:95], s96 offen offset:3072 sc1
buffer_load_dwordx4 a[144:147], v255, s[92:95], s97 offen offset:0 sc1
buffer_load_dwordx4 a[148:151], v255, s[92:95], s97 offen offset:1024 sc1
buffer_load_dwordx4 a[152:155], v255, s[92:95], s97 offen offset:2048 sc1
buffer_load_dwordx4 a[156:159], v255, s[92:95], s97 offen offset:3072 sc1
v_cmp_gt_i32 vcc, 0, v220
s_nop 1
v_cndmask_b32 v192, v192, v222, vcc
v_cmp_gt_i32 vcc, 1, v220
s_nop 1
v_cndmask_b32 v193, v193, v222, vcc
v_cmp_gt_i32 vcc, 2, v220
s_nop 1
v_cndmask_b32 v194, v194, v222, vcc
v_cmp_gt_i32 vcc, 3, v220
s_nop 1
v_cndmask_b32 v195, v195, v222, vcc
v_cmp_gt_i32 vcc, 16, v220
s_nop 1
v_cndmask_b32 v196, v196, v222, vcc
v_cmp_gt_i32 vcc, 17, v220
s_nop 1
v_cndmask_b32 v197, v197, v222, vcc
v_cmp_gt_i32 vcc, 18, v220
s_nop 1
v_cndmask_b32 v198, v198, v222, vcc
v_cmp_gt_i32 vcc, 19, v220
s_nop 1
v_cndmask_b32 v199, v199, v222, vcc
v_cmp_gt_i32 vcc, -16, v220
s_nop 1
v_cndmask_b32 v200, v200, v222, vcc
v_cmp_gt_i32 vcc, -15, v220
s_nop 1
v_cndmask_b32 v201, v201, v222, vcc
v_cmp_gt_i32 vcc, -14, v220
s_nop 1
v_cndmask_b32 v202, v202, v222, vcc
v_cmp_gt_i32 vcc, -13, v220
s_nop 1
v_cndmask_b32 v203, v203, v222, vcc
v_cmp_gt_i32 vcc, 0, v220
s_nop 1
v_cndmask_b32 v204, v204, v222, vcc
v_cmp_gt_i32 vcc, 1, v220
s_nop 1
v_cndmask_b32 v205, v205, v222, vcc
v_cmp_gt_i32 vcc, 2, v220
s_nop 1
v_cndmask_b32 v206, v206, v222, vcc
v_cmp_gt_i32 vcc, 3, v220
s_nop 1
v_cndmask_b32 v207, v207, v222, vcc
v_max3_f32 v222, v192, v193, v216
v_max3_f32 v222, v194, v195, v222
v_max3_f32 v222, v196, v197, v222
v_max3_f32 v222, v198, v199, v222
v_mov_b32 v221, v222
s_nop 1
v_permlane16_swap_b32 v221, v222
s_nop 1
v_max_f32 v222, v222, v221
v_mov_b32 v221, v222
s_nop 1
v_permlane32_swap_b32 v221, v222
s_nop 1
v_max_f32 v222, v222, v221
v_sub_f32 v223, v216, v222
v_mov_b32 v216, v222
v_exp_f32 v223, v223
v_sub_f32 v192, v192, v222
v_sub_f32 v193, v193, v222
v_sub_f32 v194, v194, v222
v_sub_f32 v195, v195, v222
v_sub_f32 v196, v196, v222
v_sub_f32 v197, v197, v222
v_sub_f32 v198, v198, v222
v_sub_f32 v199, v199, v222
v_exp_f32 v192, v192
v_exp_f32 v193, v193
v_exp_f32 v194, v194
v_exp_f32 v195, v195
v_exp_f32 v196, v196
v_exp_f32 v197, v197
v_exp_f32 v198, v198
v_exp_f32 v199, v199
v_mul_f32 v218, v218, v223
v_add_f32 v218, v218, v192
v_add_f32 v218, v218, v193
v_add_f32 v218, v218, v194
v_add_f32 v218, v218, v195
v_add_f32 v218, v218, v196
v_add_f32 v218, v218, v197
v_add_f32 v218, v218, v198
v_add_f32 v218, v218, v199
v_cvt_pk_bf16_f32 v208, v192, v193
v_cvt_pk_bf16_f32 v209, v194, v195
v_cvt_pk_bf16_f32 v210, v196, v197
v_cvt_pk_bf16_f32 v211, v198, v199
v_mul_f32 v0, v0, v223
v_mul_f32 v1, v1, v223
v_mul_f32 v2, v2, v223
v_mul_f32 v3, v3, v223
v_mul_f32 v4, v4, v223
v_mul_f32 v5, v5, v223
v_mul_f32 v6, v6, v223
v_mul_f32 v7, v7, v223
v_mul_f32 v8, v8, v223
v_mul_f32 v9, v9, v223
v_mul_f32 v10, v10, v223
v_mul_f32 v11, v11, v223
v_mul_f32 v12, v12, v223
v_mul_f32 v13, v13, v223
v_mul_f32 v14, v14, v223
v_mul_f32 v15, v15, v223
v_mul_f32 v16, v16, v223
v_mul_f32 v17, v17, v223
v_mul_f32 v18, v18, v223
v_mul_f32 v19, v19, v223
v_mul_f32 v20, v20, v223
v_mul_f32 v21, v21, v223
v_mul_f32 v22, v22, v223
v_mul_f32 v23, v23, v223
v_mul_f32 v24, v24, v223
v_mul_f32 v25, v25, v223
v_mul_f32 v26, v26, v223
v_mul_f32 v27, v27, v223
v_mul_f32 v28, v28, v223
v_mul_f32 v29, v29, v223
v_mul_f32 v30, v30, v223
v_mul_f32 v31, v31, v223
v_max3_f32 v222, v200, v201, v217
v_max3_f32 v222, v202, v203, v222
v_max3_f32 v222, v204, v205, v222
v_max3_f32 v222, v206, v207, v222
v_mov_b32 v221, v222
s_nop 1
v_permlane16_swap_b32 v221, v222
s_nop 1
v_max_f32 v222, v222, v221
v_mov_b32 v221, v222
s_nop 1
v_permlane32_swap_b32 v221, v222
s_nop 1
v_max_f32 v222, v222, v221
v_sub_f32 v223, v217, v222
v_mov_b32 v217, v222
v_exp_f32 v223, v223
v_sub_f32 v200, v200, v222
v_sub_f32 v201, v201, v222
v_sub_f32 v202, v202, v222
v_sub_f32 v203, v203, v222
v_sub_f32 v204, v204, v222
v_sub_f32 v205, v205, v222
v_sub_f32 v206, v206, v222
v_sub_f32 v207, v207, v222
v_exp_f32 v200, v200
v_exp_f32 v201, v201
v_exp_f32 v202, v202
v_exp_f32 v203, v203
v_exp_f32 v204, v204
v_exp_f32 v205, v205
v_exp_f32 v206, v206
v_exp_f32 v207, v207
v_mul_f32 v219, v219, v223
v_add_f32 v219, v219, v200
v_add_f32 v219, v219, v201
v_add_f32 v219, v219, v202
v_add_f32 v219, v219, v203
v_add_f32 v219, v219, v204
v_add_f32 v219, v219, v205
v_add_f32 v219, v219, v206
v_add_f32 v219, v219, v207
v_cvt_pk_bf16_f32 v212, v200, v201
v_cvt_pk_bf16_f32 v213, v202, v203
v_cvt_pk_bf16_f32 v214, v204, v205
v_cvt_pk_bf16_f32 v215, v206, v207
v_mul_f32 v32, v32, v223
v_mul_f32 v33, v33, v223
v_mul_f32 v34, v34, v223
v_mul_f32 v35, v35, v223
v_mul_f32 v36, v36, v223
v_mul_f32 v37, v37, v223
v_mul_f32 v38, v38, v223
v_mul_f32 v39, v39, v223
v_mul_f32 v40, v40, v223
v_mul_f32 v41, v41, v223
v_mul_f32 v42, v42, v223
v_mul_f32 v43, v43, v223
v_mul_f32 v44, v44, v223
v_mul_f32 v45, v45, v223
v_mul_f32 v46, v46, v223
v_mul_f32 v47, v47, v223
v_mul_f32 v48, v48, v223
v_mul_f32 v49, v49, v223
v_mul_f32 v50, v50, v223
v_mul_f32 v51, v51, v223
v_mul_f32 v52, v52, v223
v_mul_f32 v53, v53, v223
v_mul_f32 v54, v54, v223
v_mul_f32 v55, v55, v223
v_mul_f32 v56, v56, v223
v_mul_f32 v57, v57, v223
v_mul_f32 v58, v58, v223
v_mul_f32 v59, v59, v223
v_mul_f32 v60, v60, v223
v_mul_f32 v61, v61, v223
v_mul_f32_e64 v62, v62, v223
v_mul_f32_e64 v63, v63, v223
s_waitcnt vmcnt(0)
v_mfma_f32_16x16x32_bf16 v[0:3], a[128:131], v[208:211], v[0:3]
v_mfma_f32_16x16x32_bf16 v[32:35], a[128:131], v[212:215], v[32:35]
v_mfma_f32_16x16x32_bf16 v[4:7], a[132:135], v[208:211], v[4:7]
v_mfma_f32_16x16x32_bf16 v[36:39], a[132:135], v[212:215], v[36:39]
v_mfma_f32_16x16x32_bf16 v[8:11], a[136:139], v[208:211], v[8:11]
v_mfma_f32_16x16x32_bf16 v[40:43], a[136:139], v[212:215], v[40:43]
v_mfma_f32_16x16x32_bf16 v[12:15], a[140:143], v[208:211], v[12:15]
v_mfma_f32_16x16x32_bf16 v[44:47], a[140:143], v[212:215], v[44:47]
v_mfma_f32_16x16x32_bf16 v[16:19], a[144:147], v[208:211], v[16:19]
v_mfma_f32_16x16x32_bf16 v[48:51], a[144:147], v[212:215], v[48:51]
v_mfma_f32_16x16x32_bf16 v[20:23], a[148:151], v[208:211], v[20:23]
v_mfma_f32_16x16x32_bf16 v[52:55], a[148:151], v[212:215], v[52:55]
v_mfma_f32_16x16x32_bf16 v[24:27], a[152:155], v[208:211], v[24:27]
v_mfma_f32_16x16x32_bf16 v[56:59], a[152:155], v[212:215], v[56:59]
v_mfma_f32_16x16x32_bf16 v[28:31], a[156:159], v[208:211], v[28:31]
v_mfma_f32_16x16x32_bf16 v[60:63], a[156:159], v[212:215], v[60:63]
s_nop 7
s_nop 7
s_add_u32 s7, s7, 1
s_lshr_b32 s96, s7, 3
s_cmp_le_u32 s96, s76
s_cbranch_scc1 .La5_LA_SLOW0_L
v_mov_b32 v221, v218
s_nop 1
v_permlane16_swap_b32 v221, v218
s_nop 1
v_add_f32 v218, v218, v221
v_mov_b32 v221, v218
s_nop 1
v_permlane32_swap_b32 v221, v218
s_nop 1
v_add_f32 v218, v218, v221
v_mov_b32 v221, v219
s_nop 1
v_permlane16_swap_b32 v221, v219
s_nop 1
v_add_f32 v219, v219, v221
v_mov_b32 v221, v219
s_nop 1
v_permlane32_swap_b32 v221, v219
s_nop 1
v_add_f32 v219, v219, v221
v_rcp_f32 v242, v218
v_rcp_f32 v243, v219
v_accvgpr_read_b32 v192, a160
v_accvgpr_read_b32 v193, a161
v_accvgpr_read_b32 v194, a162
v_accvgpr_read_b32 v195, a163
v_accvgpr_read_b32 v196, a164
v_accvgpr_read_b32 v197, a165
v_accvgpr_read_b32 v198, a166
v_accvgpr_read_b32 v199, a167
v_accvgpr_read_b32 v200, a168
v_accvgpr_read_b32 v201, a169
v_accvgpr_read_b32 v202, a170
v_accvgpr_read_b32 v203, a171
v_accvgpr_read_b32 v204, a172
v_accvgpr_read_b32 v205, a173
v_accvgpr_read_b32 v206, a174
v_accvgpr_read_b32 v207, a175
v_accvgpr_read_b32 v208, a176
v_accvgpr_read_b32 v209, a177
v_accvgpr_read_b32 v210, a178
v_accvgpr_read_b32 v211, a179
v_accvgpr_read_b32 v212, a180
v_accvgpr_read_b32 v213, a181
v_accvgpr_read_b32 v214, a182
v_accvgpr_read_b32 v215, a183
v_accvgpr_read_b32 v216, a184
v_accvgpr_read_b32 v217, a185
v_accvgpr_read_b32 v218, a186
v_accvgpr_read_b32 v219, a187
v_accvgpr_read_b32 v220, a188
v_accvgpr_read_b32 v221, a189
v_accvgpr_read_b32 v222, a190
v_accvgpr_read_b32 v223, a191
s_mov_b32 s95, 4096
s_mov_b32 s96, 8192
s_mov_b32 s97, 12288
s_mov_b64 vcc, 0
s_nop 1
s_setpc_b64 s[24:25]
; ---- fallback: the later 32 rows
.La5_LA_SLOW1:
s_waitcnt lgkmcnt(0)
s_nop 7
s_nop 7
v_accvgpr_write_b32 a160, v192
v_accvgpr_write_b32 a161, v193
v_accvgpr_write_b32 a162, v194
v_accvgpr_write_b32 a163, v195
v_accvgpr_write_b32 a164, v196
v_accvgpr_write_b32 a165, v197
v_accvgpr_write_b32 a166, v198
v_accvgpr_write_b32 a167, v199
v_accvgpr_write_b32 a168, v200
v_accvgpr_write_b32 a169, v201
v_accvgpr_write_b32 a170, v202
v_accvgpr_write_b32 a171, v203
v_accvgpr_write_b32 a172, v204
v_accvgpr_write_b32 a173, v205
v_accvgpr_write_b32 a174, v206
v_accvgpr_write_b32 a175, v207
v_accvgpr_write_b32 a176, v208
v_accvgpr_write_b32 a177, v209
v_accvgpr_write_b32 a178, v210
v_accvgpr_write_b32 a179, v211
v_accvgpr_write_b32 a180, v212
v_accvgpr_write_b32 a181, v213
v_accvgpr_write_b32 a182, v214
v_accvgpr_write_b32 a183, v215
v_accvgpr_write_b32 a184, v216
v_accvgpr_write_b32 a185, v217
v_accvgpr_write_b32 a186, v218
v_accvgpr_write_b32 a187, v219
v_accvgpr_write_b32 a188, v220
v_accvgpr_write_b32 a189, v221
v_accvgpr_write_b32 a190, v222
v_accvgpr_write_b32 a191, v223
s_lshl_b32 s96, s72, 19
s_add_u32 s96, s96, 20971520
s_add_u32 s92, s26, s96
s_addc_u32 s93, s27, 0
s_and_b32 s93, s93, 0xffff
s_mov_b32 s94, 17301504
s_mov_b32 s95, 0x20000
v_mov_b32 v64, 0
v_mov_b32 v65, 0
v_mov_b32 v66, 0
v_mov_b32 v67, 0
v_mov_b32 v68, 0
v_mov_b32 v69, 0
v_mov_b32 v70, 0
v_mov_b32 v71, 0
v_mov_b32 v72, 0
v_mov_b32 v73, 0
v_mov_b32 v74, 0
v_mov_b32 v75, 0
v_mov_b32 v76, 0
v_mov_b32 v77, 0
v_mov_b32 v78, 0
v_mov_b32 v79, 0
v_mov_b32 v80, 0
v_mov_b32 v81, 0
v_mov_b32 v82, 0
v_mov_b32 v83, 0
v_mov_b32 v84, 0
v_mov_b32 v85, 0
v_mov_b32 v86, 0
v_mov_b32 v87, 0
v_mov_b32 v88, 0
v_mov_b32 v89, 0
v_mov_b32 v90, 0
v_mov_b32 v91, 0
v_mov_b32 v92, 0
v_mov_b32 v93, 0
v_mov_b32 v94, 0
v_mov_b32 v95, 0
v_mov_b32 v96, 0
v_mov_b32 v97, 0
v_mov_b32 v98, 0
v_mov_b32 v99, 0
v_mov_b32 v100, 0
v_mov_b32 v101, 0
v_mov_b32 v102, 0
v_mov_b32 v103, 0
v_mov_b32 v104, 0
v_mov_b32 v105, 0
v_mov_b32 v106, 0
v_mov_b32 v107, 0
v_mov_b32 v108, 0
v_mov_b32 v109, 0
v_mov_b32 v110, 0
v_mov_b32 v111, 0
v_mov_b32 v112, 0
v_mov_b32 v113, 0
v_mov_b32 v114, 0
v_mov_b32 v115, 0
v_mov_b32 v116, 0
v_mov_b32 v117, 0
v_mov_b32 v118, 0
v_mov_b32 v119, 0
v_mov_b32 v120, 0
v_mov_b32 v121, 0
v_mov_b32 v122, 0
v_mov_b32 v123, 0
v_mov_b32 v124, 0
v_mov_b32 v125, 0
v_mov_b32 v126, 0
v_mov_b32 v127, 0
v_mov_b32 v216, 0xff61b1e6
v_mov_b32 v217, 0xff61b1e6
v_mov_b32 v218, 0
v_mov_b32 v219, 0
s_mov_b32 s7, 0
.La5_LA_SLOW1_L:
s_lshl_b32 s96, s7, 13
s_add_u32 s97, s96, 4096
buffer_load_dwordx4 a[128:131], v255, s[92:95], s96 offen offset:0 sc1
buffer_load_dwordx4 a[132:135], v255, s[92:95], s96 offen offset:1024 sc1
buffer_load_dwordx4 a[136:139], v255, s[92:95], s96 offen offset:2048 sc1
buffer_load_dwordx4 a[140:143], v255, s[92:95], s96 offen offset:3072 sc1
buffer_load_dwordx4 a[144:147], v255, s[92:95], s97 offen offset:0 sc1
buffer_load_dwordx4 a[148:151], v255, s[92:95], s97 offen offset:1024 sc1
buffer_load_dwordx4 a[152:155], v255, s[92:95], s97 offen offset:2048 sc1
buffer_load_dwordx4 a[156:159], v255, s[92:95], s97 offen offset:3072 sc1
s_lshl_b32 s96, s76, 3
s_sub_u32 s96, s96, s7
s_sub_u32 s96, s96, s3
s_add_u32 s96, s96, 7
s_lshl_b32 s96, s96, 5
v_bfe_u32 v220, v255, 4, 4
v_lshrrev_b32 v221, 8, v255
v_lshlrev_b32 v221, 2, v221
v_sub_u32 v220, v220, v221
v_add_u32 v220, s96, v220
v_mov_b32 v222, 0xceabfb8f
s_waitcnt vmcnt(0)
v_mfma_f32_16x16x32_bf16 v[192:195], a[128:131], v[160:163], 0
v_mfma_f32_16x16x32_bf16 v[192:195], a[132:135], v[164:167], v[192:195]
v_mfma_f32_16x16x32_bf16 v[192:195], a[136:139], v[168:171], v[192:195]
v_mfma_f32_16x16x32_bf16 v[192:195], a[140:143], v[172:175], v[192:195]
v_mfma_f32_16x16x32_bf16 v[196:199], a[144:147], v[160:163], 0
v_mfma_f32_16x16x32_bf16 v[196:199], a[148:151], v[164:167], v[196:199]
v_mfma_f32_16x16x32_bf16 v[196:199], a[152:155], v[168:171], v[196:199]
v_mfma_f32_16x16x32_bf16 v[196:199], a[156:159], v[172:175], v[196:199]
v_mfma_f32_16x16x32_bf16 v[200:203], a[128:131], v[176:179], 0
v_mfma_f32_16x16x32_bf16 v[200:203], a[132:135], v[180:183], v[200:203]
v_mfma_f32_16x16x32_bf16 v[200:203], a[136:139], v[184:187], v[200:203]
v_mfma_f32_16x16x32_bf16 v[200:203], a[140:143], v[188:191], v[200:203]
v_mfma_f32_16x16x32_bf16 v[204:207], a[144:147], v[176:179], 0
v_mfma_f32_16x16x32_bf16 v[204:207], a[148:151], v[180:183], v[204:207]
v_mfma_f32_16x16x32_bf16 v[204:207], a[152:155], v[184:187], v[204:207]
v_mfma_f32_16x16x32_bf16 v[204:207], a[156:159], v[188:191], v[204:207]
s_nop 7
s_nop 7
s_lshl_b32 s96, s7, 13
s_add_u32 s96, s96, 16777216
s_add_u32 s97, s96, 4096
buffer_load_dwordx4 a[128:131], v255, s[92:95], s96 offen offset:0 sc1
buffer_load_dwordx4 a[132:135], v255, s[92:95], s96 offen offset:1024 sc1
buffer_load_dwordx4 a[136:139], v255, s[92:95], s96 offen offset:2048 sc1
buffer_load_dwordx4 a[140:143], v255, s[92:95], s96 offen offset:3072 sc1
buffer_load_dwordx4 a[144:147], v255, s[92:95], s97 offen offset:0 sc1
buffer_load_dwordx4 a[148:151], v255, s[92:95], s97 offen offset:1024 sc1
buffer_load_dwordx4 a[152:155], v255, s[92:95], s97 offen offset:2048 sc1
buffer_load_dwordx4 a[156:159], v255, s[92:95], s97 offen offset:3072 sc1
v_cmp_gt_i32 vcc, 0, v220
s_nop 1
v_cndmask_b32 v192, v192, v222, vcc
v_cmp_gt_i32 vcc, 1, v220
s_nop 1
v_cndmask_b32 v193, v193, v222, vcc
v_cmp_gt_i32 vcc, 2, v220
s_nop 1
v_cndmask_b32 v194, v194, v222, vcc
v_cmp_gt_i32 vcc, 3, v220
s_nop 1
v_cndmask_b32 v195, v195, v222, vcc
v_cmp_gt_i32 vcc, 16, v220
s_nop 1
v_cndmask_b32 v196, v196, v222, vcc
v_cmp_gt_i32 vcc, 17, v220
s_nop 1
v_cndmask_b32 v197, v197, v222, vcc
v_cmp_gt_i32 vcc, 18, v220
s_nop 1
v_cndmask_b32 v198, v198, v222, vcc
v_cmp_gt_i32 vcc, 19, v220
s_nop 1
v_cndmask_b32 v199, v199, v222, vcc
v_cmp_gt_i32 vcc, -16, v220
s_nop 1
v_cndmask_b32 v200, v200, v222, vcc
v_cmp_gt_i32 vcc, -15, v220
s_nop 1
v_cndmask_b32 v201, v201, v222, vcc
v_cmp_gt_i32 vcc, -14, v220
s_nop 1
v_cndmask_b32 v202, v202, v222, vcc
v_cmp_gt_i32 vcc, -13, v220
s_nop 1
v_cndmask_b32 v203, v203, v222, vcc
v_cmp_gt_i32 vcc, 0, v220
s_nop 1
v_cndmask_b32 v204, v204, v222, vcc
v_cmp_gt_i32 vcc, 1, v220
s_nop 1
v_cndmask_b32 v205, v205, v222, vcc
v_cmp_gt_i32 vcc, 2, v220
s_nop 1
v_cndmask_b32 v206, v206, v222, vcc
v_cmp_gt_i32 vcc, 3, v220
s_nop 1
v_cndmask_b32 v207, v207, v222, vcc
v_max3_f32 v222, v192, v193, v216
v_max3_f32 v222, v194, v195, v222
v_max3_f32 v222, v196, v197, v222
v_max3_f32 v222, v198, v199, v222
v_mov_b32 v221, v222
s_nop 1
v_permlane16_swap_b32 v221, v222
s_nop 1
v_max_f32 v222, v222, v221
v_mov_b32 v221, v222
s_nop 1
v_permlane32_swap_b32 v221, v222
s_nop 1
v_max_f32 v222, v222, v221
v_sub_f32 v223, v216, v222
v_mov_b32 v216, v222
v_exp_f32 v223, v223
v_sub_f32 v192, v192, v222
v_sub_f32 v193, v193, v222
v_sub_f32 v194, v194, v222
v_sub_f32 v195, v195, v222
v_sub_f32 v196, v196, v222
v_sub_f32 v197, v197, v222
v_sub_f32 v198, v198, v222
v_sub_f32 v199, v199, v222
v_exp_f32 v192, v192
v_exp_f32 v193, v193
v_exp_f32 v194, v194
v_exp_f32 v195, v195
v_exp_f32 v196, v196
v_exp_f32 v197, v197
v_exp_f32 v198, v198
v_exp_f32 v199, v199
v_mul_f32 v218, v218, v223
v_add_f32 v218, v218, v192
v_add_f32 v218, v218, v193
v_add_f32 v218, v218, v194
v_add_f32 v218, v218, v195
v_add_f32 v218, v218, v196
v_add_f32 v218, v218, v197
v_add_f32 v218, v218, v198
v_add_f32 v218, v218, v199
v_cvt_pk_bf16_f32 v208, v192, v193
v_cvt_pk_bf16_f32 v209, v194, v195
v_cvt_pk_bf16_f32 v210, v196, v197
v_cvt_pk_bf16_f32 v211, v198, v199
v_mul_f32 v64, v64, v223
v_mul_f32 v65, v65, v223
v_mul_f32 v66, v66, v223
v_mul_f32 v67, v67, v223
v_mul_f32 v68, v68, v223
v_mul_f32 v69, v69, v223
v_mul_f32 v70, v70, v223
v_mul_f32 v71, v71, v223
v_mul_f32 v72, v72, v223
v_mul_f32 v73, v73, v223
v_mul_f32 v74, v74, v223
v_mul_f32 v75, v75, v223
v_mul_f32 v76, v76, v223
v_mul_f32 v77, v77, v223
v_mul_f32 v78, v78, v223
v_mul_f32 v79, v79, v223
v_mul_f32 v80, v80, v223
v_mul_f32 v81, v81, v223
v_mul_f32 v82, v82, v223
v_mul_f32 v83, v83, v223
v_mul_f32 v84, v84, v223
v_mul_f32 v85, v85, v223
v_mul_f32 v86, v86, v223
v_mul_f32 v87, v87, v223
v_mul_f32 v88, v88, v223
v_mul_f32 v89, v89, v223
v_mul_f32 v90, v90, v223
v_mul_f32 v91, v91, v223
v_mul_f32 v92, v92, v223
v_mul_f32 v93, v93, v223
v_mul_f32 v94, v94, v223
v_mul_f32 v95, v95, v223
v_max3_f32 v222, v200, v201, v217
v_max3_f32 v222, v202, v203, v222
v_max3_f32 v222, v204, v205, v222
v_max3_f32 v222, v206, v207, v222
v_mov_b32 v221, v222
s_nop 1
v_permlane16_swap_b32 v221, v222
s_nop 1
v_max_f32 v222, v222, v221
v_mov_b32 v221, v222
s_nop 1
v_permlane32_swap_b32 v221, v222
s_nop 1
v_max_f32 v222, v222, v221
v_sub_f32 v223, v217, v222
v_mov_b32 v217, v222
v_exp_f32 v223, v223
v_sub_f32 v200, v200, v222
v_sub_f32 v201, v201, v222
v_sub_f32 v202, v202, v222
v_sub_f32 v203, v203, v222
v_sub_f32 v204, v204, v222
v_sub_f32 v205, v205, v222
v_sub_f32 v206, v206, v222
v_sub_f32 v207, v207, v222
v_exp_f32 v200, v200
v_exp_f32 v201, v201
v_exp_f32 v202, v202
v_exp_f32 v203, v203
v_exp_f32 v204, v204
v_exp_f32 v205, v205
v_exp_f32 v206, v206
v_exp_f32 v207, v207
v_mul_f32 v219, v219, v223
v_add_f32 v219, v219, v200
v_add_f32 v219, v219, v201
v_add_f32 v219, v219, v202
v_add_f32 v219, v219, v203
v_add_f32 v219, v219, v204
v_add_f32 v219, v219, v205
v_add_f32 v219, v219, v206
v_add_f32 v219, v219, v207
v_cvt_pk_bf16_f32 v212, v200, v201
v_cvt_pk_bf16_f32 v213, v202, v203
v_cvt_pk_bf16_f32 v214, v204, v205
v_cvt_pk_bf16_f32 v215, v206, v207
v_mul_f32 v96, v96, v223
v_mul_f32 v97, v97, v223
v_mul_f32 v98, v98, v223
v_mul_f32 v99, v99, v223
v_mul_f32 v100, v100, v223
v_mul_f32 v101, v101, v223
v_mul_f32 v102, v102, v223
v_mul_f32 v103, v103, v223
v_mul_f32 v104, v104, v223
v_mul_f32 v105, v105, v223
v_mul_f32 v106, v106, v223
v_mul_f32 v107, v107, v223
v_mul_f32 v108, v108, v223
v_mul_f32 v109, v109, v223
v_mul_f32 v110, v110, v223
v_mul_f32 v111, v111, v223
v_mul_f32 v112, v112, v223
v_mul_f32 v113, v113, v223
v_mul_f32 v114, v114, v223
v_mul_f32 v115, v115, v223
v_mul_f32 v116, v116, v223
v_mul_f32 v117, v117, v223
v_mul_f32 v118, v118, v223
v_mul_f32 v119, v119, v223
v_mul_f32 v120, v120, v223
v_mul_f32 v121, v121, v223
v_mul_f32 v122, v122, v223
v_mul_f32 v123, v123, v223
v_mul_f32 v124, v124, v223
v_mul_f32 v125, v125, v223
v_mul_f32 v126, v126, v223
v_mul_f32_e64 v127, v127, v223
s_waitcnt vmcnt(0)
v_mfma_f32_16x16x32_bf16 v[64:67], a[128:131], v[208:211], v[64:67]
v_mfma_f32_16x16x32_bf16 v[96:99], a[128:131], v[212:215], v[96:99]
v_mfma_f32_16x16x32_bf16 v[68:71], a[132:135], v[208:211], v[68:71]
v_mfma_f32_16x16x32_bf16 v[100:103], a[132:135], v[212:215], v[100:103]
v_mfma_f32_16x16x32_bf16 v[72:75], a[136:139], v[208:211], v[72:75]
v_mfma_f32_16x16x32_bf16 v[104:107], a[136:139], v[212:215], v[104:107]
v_mfma_f32_16x16x32_bf16 v[76:79], a[140:143], v[208:211], v[76:79]
v_mfma_f32_16x16x32_bf16 v[108:111], a[140:143], v[212:215], v[108:111]
v_mfma_f32_16x16x32_bf16 v[80:83], a[144:147], v[208:211], v[80:83]
v_mfma_f32_16x16x32_bf16 v[112:115], a[144:147], v[212:215], v[112:115]
v_mfma_f32_16x16x32_bf16 v[84:87], a[148:151], v[208:211], v[84:87]
v_mfma_f32_16x16x32_bf16 v[116:119], a[148:151], v[212:215], v[116:119]
v_mfma_f32_16x16x32_bf16 v[88:91], a[152:155], v[208:211], v[88:91]
v_mfma_f32_16x16x32_bf16 v[120:123], a[152:155], v[212:215], v[120:123]
v_mfma_f32_16x16x32_bf16 v[92:95], a[156:159], v[208:211], v[92:95]
v_mfma_f32_16x16x32_bf16 v[124:127], a[156:159], v[212:215], v[124:127]
s_nop 7
s_nop 7
s_add_u32 s7, s7, 1
s_lshr_b32 s96, s7, 3
s_cmp_le_u32 s96, s76
s_cbranch_scc1 .La5_LA_SLOW1_L
v_mov_b32 v221, v218
s_nop 1
v_permlane16_swap_b32 v221, v218
s_nop 1
v_add_f32 v218, v218, v221
v_mov_b32 v221, v218
s_nop 1
v_permlane32_swap_b32 v221, v218
s_nop 1
v_add_f32 v218, v218, v221
v_mov_b32 v221, v219
s_nop 1
v_permlane16_swap_b32 v221, v219
s_nop 1
v_add_f32 v219, v219, v221
v_mov_b32 v221, v219
s_nop 1
v_permlane32_swap_b32 v221, v219
s_nop 1
v_add_f32 v219, v219, v221
v_rcp_f32 v242, v218
v_rcp_f32 v243, v219
v_accvgpr_read_b32 v192, a160
v_accvgpr_read_b32 v193, a161
v_accvgpr_read_b32 v194, a162
v_accvgpr_read_b32 v195, a163
v_accvgpr_read_b32 v196, a164
v_accvgpr_read_b32 v197, a165
v_accvgpr_read_b32 v198, a166
v_accvgpr_read_b32 v199, a167
v_accvgpr_read_b32 v200, a168
v_accvgpr_read_b32 v201, a169
v_accvgpr_read_b32 v202, a170
v_accvgpr_read_b32 v203, a171
v_accvgpr_read_b32 v204, a172
v_accvgpr_read_b32 v205, a173
v_accvgpr_read_b32 v206, a174
v_accvgpr_read_b32 v207, a175
v_accvgpr_read_b32 v208, a176
v_accvgpr_read_b32 v209, a177
v_accvgpr_read_b32 v210, a178
v_accvgpr_read_b32 v211, a179
v_accvgpr_read_b32 v212, a180
v_accvgpr_read_b32 v213, a181
v_accvgpr_read_b32 v214, a182
v_accvgpr_read_b32 v215, a183
v_accvgpr_read_b32 v216, a184
v_accvgpr_read_b32 v217, a185
v_accvgpr_read_b32 v218, a186
v_accvgpr_read_b32 v219, a187
v_accvgpr_read_b32 v220, a188
v_accvgpr_read_b32 v221, a189
v_accvgpr_read_b32 v222, a190
v_accvgpr_read_b32 v223, a191
s_mov_b32 s95, 4096
s_mov_b32 s96, 8192
s_mov_b32 s97, 12288
s_mov_b64 vcc, 0
s_nop 1
s_setpc_b64 s[24:25]
.La5_LK_XSPIN:
s_mov_b32 s93, 0
.La5_LK_XSPIN_L:
s_add_u32 s93, s93, 1
s_cmp_gt_u32 s93, 2097152
s_cbranch_scc1 .La5_LK_XOK
s_sleep 8
buffer_load_dwordx4 v[72:75], v255, s[64:67], s89 offen offset:1024 sc1
s_waitcnt vmcnt(0)
v_cmp_eq_u64 vcc, v[72:73], v[252:253]
v_cmp_eq_u64 s[96:97], v[74:75], v[252:253]
s_nop 1
s_and_b64 vcc, vcc, s[96:97]
s_cmp_eq_u64 vcc, -1
s_nop 0
s_cbranch_scc0 .La5_LK_XSPIN_L
s_branch .La5_LK_XOK
.La5_LV_XSPIN:
s_mov_b32 s93, 0
.La5_LV_XSPIN_L:
s_add_u32 s93, s93, 1
s_cmp_gt_u32 s93, 2097152
s_nop 0
s_nop 0
s_cbranch_scc1 .La5_LV_XOK
s_sleep 8
buffer_load_dwordx4 v[72:75], v255, s[64:67], s89 offen offset:1024 sc1
s_waitcnt vmcnt(0)
v_cmp_eq_u64 vcc, v[72:73], v[252:253]
v_cmp_eq_u64 s[96:97], v[74:75], v[252:253]
s_nop 1
s_and_b64 vcc, vcc, s[96:97]
s_cmp_eq_u64 vcc, -1
s_cbranch_scc0 .La5_LV_XSPIN_L
s_branch .La5_LV_XOK
.La5_LA_SC1:
s_call_b64 s[24:25], .La5_LA_SLOW0
s_branch .La5_LA_SR1
.La5_LA_SC2:
s_nop 0
s_call_b64 s[24:25], .La5_LA_SLOW1
s_branch .La5_LA_SR2
.La5_LA_PS0:
s_mov_b32 s84, 3
s_mov_b32 s70, 17408
s_waitcnt vmcnt(2)
buffer_load_dwordx4 a[220:223], v255, s[68:71], s94 offen sc1
buffer_load_dwordx4 a[0:3], v255, s[68:71], 0 offen offset:0 sc1
buffer_load_dwordx4 a[4:7], v255, s[68:71], 0 offen offset:1024 sc1
buffer_load_dwordx4 a[8:11], v255, s[68:71], 0 offen offset:2048 sc1
buffer_load_dwordx4 a[12:15], v255, s[68:71], 0 offen offset:3072 sc1
buffer_load_dwordx4 a[16:19], v255, s[68:71], s95 offen offset:0 sc1
buffer_load_dwordx4 a[20:23], v255, s[68:71], s95 offen offset:1024 sc1
buffer_load_dwordx4 a[24:27], v255, s[68:71], s95 offen offset:2048 sc1
s_branch .La5_LA_PS1
.La5_LA_PS2:
s_mov_b32 s84, 3
s_mov_b32 s70, 17408
s_waitcnt vmcnt(0)
buffer_load_dwordx4 a[220:223], v255, s[68:71], s94 offen sc1
buffer_load_dwordx4 a[0:3], v255, s[68:71], 0 offen offset:0 sc1
buffer_load_dwordx4 a[4:7], v255, s[68:71], 0 offen offset:1024 sc1
buffer_load_dwordx4 a[8:11], v255, s[68:71], 0 offen offset:2048 sc1
buffer_load_dwordx4 a[12:15], v255, s[68:71], 0 offen offset:3072 sc1
buffer_load_dwordx4 a[16:19], v255, s[68:71], s95 offen offset:0 sc1
buffer_load_dwordx4 a[20:23], v255, s[68:71], s95 offen offset:1024 sc1
buffer_load_dwordx4 a[24:27], v255, s[68:71], s95 offen offset:2048 sc1
buffer_load_dwordx4 a[28:31], v255, s[68:71], s95 offen offset:3072 sc1
buffer_load_dwordx4 a[32:35], v255, s[68:71], s96 offen offset:0 sc1
buffer_load_dwordx4 a[36:39], v255, s[68:71], s96 offen offset:1024 sc1
buffer_load_dwordx4 a[40:43], v255, s[68:71], s96 offen offset:2048 sc1
buffer_load_dwordx4 a[44:47], v255, s[68:71], s96 offen offset:3072 sc1
buffer_load_dwordx4 a[48:51], v255, s[68:71], s97 offen offset:0 sc1
buffer_load_dwordx4 a[52:55], v255, s[68:71], s97 offen offset:1024 sc1
buffer_load_dwordx4 a[56:59], v255, s[68:71], s97 offen offset:2048 sc1
buffer_load_dwordx4 a[60:63], v255, s[68:71], s97 offen offset:3072 sc1
s_branch .La5_LA_PS3
.La5_LA_PSPIN:
s_mov_b32 s93, 0
.La5_LA_PSPIN_L:
s_add_u32 s93, s93, 1
s_cmp_gt_u32 s93, 2097152
s_cbranch_scc1 .La5_LA_PSOK
s_sleep 8
buffer_load_dword v250, v251, s[64:67], s20 offen sc1
s_waitcnt vmcnt(0)
v_cmp_eq_u32 vcc, v250, v249
s_nop 1
s_and_b32 s92, vcc_lo, 3
s_cmp_eq_u32 s92, 3
s_cbranch_scc0 .La5_LA_PSPIN_L
s_branch .La5_LA_PSOK
.La5_LA_PSOK:
s_mov_b32 s70, 17408
buffer_load_dwordx4 a[220:223], v255, s[68:71], s94 offen sc1
buffer_load_dwordx4 a[0:3], v255, s[68:71], 0 offen offset:0 sc1
buffer_load_dwordx4 a[4:7], v255, s[68:71], 0 offen offset:1024 sc1
buffer_load_dwordx4 a[8:11], v255, s[68:71], 0 offen offset:2048 sc1
buffer_load_dwordx4 a[12:15], v255, s[68:71], 0 offen offset:3072 sc1
buffer_load_dwordx4 a[16:19], v255, s[68:71], s95 offen offset:0 sc1
buffer_load_dwordx4 a[20:23], v255, s[68:71], s95 offen offset:1024 sc1
buffer_load_dwordx4 a[24:27], v255, s[68:71], s95 offen offset:2048 sc1
buffer_load_dwordx4 a[28:31], v255, s[68:71], s95 offen offset:3072 sc1
buffer_load_dwordx4 a[32:35], v255, s[68:71], s96 offen offset:0 sc1
buffer_load_dwordx4 a[36:39], v255, s[68:71], s96 offen offset:1024 sc1
buffer_load_dwordx4 a[40:43], v255, s[68:71], s96 offen offset:2048 sc1
buffer_load_dwordx4 a[44:47], v255, s[68:71], s96 offen offset:3072 sc1
buffer_load_dwordx4 a[48:51], v255, s[68:71], s97 offen offset:0 sc1
buffer_load_dwordx4 a[52:55], v255, s[68:71], s97 offen offset:1024 sc1
buffer_load_dwordx4 a[56:59], v255, s[68:71], s97 offen offset:2048 sc1
buffer_load_dwordx4 a[60:63], v255, s[68:71], s97 offen offset:3072 sc1
s_branch .La5_LA_DC1
.La5_LO_N1:
.La5_LW2_14:
s_cbranch_execz .La5_LW2_R0
s_mov_b32 s93, 0
.La5_LO_N1_L:
s_add_u32 s93, s93, 1
s_cmp_gt_u32 s93, 2097152
s_nop 0
s_cbranch_scc1 .La5_LO_K1
s_sleep 8
buffer_load_dwordx2 v[250:251], v248, s[64:67], s5 offen sc1
s_waitcnt vmcnt(0)
v_cmp_eq_u64 vcc, v[250:251], v[252:253]
s_nop 1
s_bitcmp1_b32 vcc_lo, 1
s_cbranch_scc0 .La5_LO_N1_L
s_branch .La5_LO_K1
.La5_LO_N2:
s_mov_b32 s93, 0
.La5_LO_N2_L:
s_add_u32 s93, s93, 1
s_cmp_gt_u32 s93, 2097152
s_nop 0
s_nop 0
s_cbranch_scc1 .La5_LO_K2
s_sleep 8
buffer_load_dwordx2 v[250:251], v248, s[64:67], s5 offen sc1
s_waitcnt vmcnt(0)
v_cmp_eq_u64 vcc, v[250:251], v[252:253]
s_nop 1
s_bitcmp1_b32 vcc_lo, 2
s_cbranch_scc0 .La5_LO_N2_L
s_branch .La5_LO_K2
.La5_LO_N3:
s_mov_b32 s93, 0
.La5_LO_N3_L:
s_add_u32 s93, s93, 1
s_cmp_gt_u32 s93, 2097152
s_nop 0
s_nop 0
s_cbranch_scc1 .La5_LO_K3
s_sleep 8
buffer_load_dwordx2 v[250:251], v248, s[64:67], s5 offen sc1
s_waitcnt vmcnt(0)
v_cmp_eq_u64 vcc, v[250:251], v[252:253]
s_nop 1
s_bitcmp1_b32 vcc_lo, 3
s_cbranch_scc0 .La5_LO_N3_L
s_branch .La5_LO_K3
.La5_LO_N4:
s_mov_b32 s93, 0
.La5_LO_N4_L:
s_add_u32 s93, s93, 1
s_cmp_gt_u32 s93, 2097152
s_nop 0
s_nop 0
s_cbranch_scc1 .La5_LO_K4
s_sleep 8
buffer_load_dwordx2 v[250:251], v248, s[64:67], s5 offen sc1
s_waitcnt vmcnt(0)
v_cmp_eq_u64 vcc, v[250:251], v[252:253]
s_nop 1
s_bitcmp1_b32 vcc_lo, 4
s_cbranch_scc0 .La5_LO_N4_L
s_branch .La5_LO_K4
.La5_LO_N5:
s_mov_b32 s93, 0
.La5_LO_N5_L:
s_add_u32 s93, s93, 1
s_cmp_gt_u32 s93, 2097152
s_nop 0
s_nop 0
s_cbranch_scc1 .La5_LO_K5
s_sleep 8
buffer_load_dwordx2 v[250:251], v248, s[64:67], s5 offen sc1
s_waitcnt vmcnt(0)
v_cmp_eq_u64 vcc, v[250:251], v[252:253]
s_nop 1
s_bitcmp1_b32 vcc_lo, 5
s_cbranch_scc0 .La5_LO_N5_L
s_branch .La5_LO_K5
.La5_LO_N6:
s_mov_b32 s93, 0
.La5_LO_N6_L:
s_add_u32 s93, s93, 1
s_cmp_gt_u32 s93, 2097152
s_nop 0
s_nop 0
s_cbranch_scc1 .La5_LO_K6
s_sleep 8
buffer_load_dwordx2 v[250:251], v248, s[64:67], s5 offen sc1
s_waitcnt vmcnt(0)
v_cmp_eq_u64 vcc, v[250:251], v[252:253]
s_nop 1
s_bitcmp1_b32 vcc_lo, 6
s_cbranch_scc0 .La5_LO_N6_L
s_branch .La5_LO_K6
.La5_LO_N7:
s_mov_b32 s93, 0
.La5_LO_N7_L:
s_add_u32 s93, s93, 1
s_cmp_gt_u32 s93, 2097152
s_nop 0
s_nop 0
s_cbranch_scc1 .La5_LO_K7
s_sleep 8
buffer_load_dwordx2 v[250:251], v248, s[64:67], s5 offen sc1
s_waitcnt vmcnt(0)
v_cmp_eq_u64 vcc, v[250:251], v[252:253]
s_nop 1
s_bitcmp1_b32 vcc_lo, 7
s_cbranch_scc0 .La5_LO_N7_L
s_branch .La5_LO_K7
.La5_LA_SC3:
s_call_b64 s[24:25], .La5_LA_SLOW0
v_accvgpr_write_b32 a0, 0
v_accvgpr_write_b32 a1, 0
v_accvgpr_write_b32 a2, 0
v_accvgpr_write_b32 a3, 0
v_accvgpr_write_b32 a4, 0
v_accvgpr_write_b32 a5, 0
v_accvgpr_write_b32 a6, 0
v_accvgpr_write_b32 a7, 0
v_accvgpr_write_b32 a8, 0
v_accvgpr_write_b32 a9, 0
v_accvgpr_write_b32 a10, 0
v_accvgpr_write_b32 a11, 0
v_accvgpr_write_b32 a12, 0
v_accvgpr_write_b32 a13, 0
v_accvgpr_write_b32 a14, 0
v_accvgpr_write_b32 a15, 0
v_accvgpr_write_b32 a16, 0
v_accvgpr_write_b32 a17, 0
v_accvgpr_write_b32 a18, 0
v_accvgpr_write_b32 a19, 0
v_accvgpr_write_b32 a20, 0
v_accvgpr_write_b32 a21, 0
v_accvgpr_write_b32 a22, 0
v_accvgpr_write_b32 a23, 0
v_accvgpr_write_b32 a24, 0
v_accvgpr_write_b32 a25, 0
v_accvgpr_write_b32 a26, 0
v_accvgpr_write_b32 a27, 0
v_accvgpr_write_b32 a28, 0
v_accvgpr_write_b32 a29, 0
v_accvgpr_write_b32 a30, 0
v_accvgpr_write_b32 a31, 0
s_nop 1
v_mov_b32 v250, v242
v_mov_b32 v249, v243
v_xor_b32 v242, 64, v240
v_xor_b32 v243, 96, v240
s_mov_b32 s93, 0
buffer_load_dwordx4 a[128:131], v254, s[40:43], s93 offen
s_mov_b32 s93, 7168
buffer_load_dwordx4 a[132:135], v254, s[40:43], s93 offen
s_mov_b32 s93, 14336
buffer_load_dwordx4 a[136:139], v254, s[40:43], s93 offen
s_mov_b32 s93, 21504
buffer_load_dwordx4 a[140:143], v254, s[40:43], s93 offen
s_mov_b32 s93, 28672
buffer_load_dwordx4 a[144:147], v254, s[40:43], s93 offen
s_mov_b32 s93, 35840
buffer_load_dwordx4 a[148:151], v254, s[40:43], s93 offen
s_mov_b32 s93, 43008
buffer_load_dwordx4 a[152:155], v254, s[40:43], s93 offen
s_mov_b32 s93, 50176
buffer_load_dwordx4 a[156:159], v254, s[40:43], s93 offen
s_mov_b32 s93, 57344
buffer_load_dwordx4 a[160:163], v254, s[40:43], s93 offen
s_mov_b32 s93, 64512
buffer_load_dwordx4 a[164:167], v254, s[40:43], s93 offen
s_mov_b32 s93, 71680
buffer_load_dwordx4 a[168:171], v254, s[40:43], s93 offen
s_mov_b32 s93, 78848
buffer_load_dwordx4 a[172:175], v254, s[40:43], s93 offen
s_mov_b32 s93, 86016
buffer_load_dwordx4 a[176:179], v254, s[40:43], s93 offen
s_mov_b32 s93, 93184
buffer_load_dwordx4 a[180:183], v254, s[40:43], s93 offen
s_mov_b32 s93, 100352
buffer_load_dwordx4 a[184:187], v254, s[40:43], s93 offen
s_mov_b32 s93, 107520
buffer_load_dwordx4 a[188:191], v254, s[40:43], s93 offen
s_branch .La5_LA_SR3
.La5_LA_SC4:
s_call_b64 s[24:25], .La5_LA_SLOW1
v_accvgpr_write_b32 a32, 0
v_accvgpr_write_b32 a33, 0
v_accvgpr_write_b32 a34, 0
v_accvgpr_write_b32 a35, 0
v_accvgpr_write_b32 a36, 0
v_accvgpr_write_b32 a37, 0
v_accvgpr_write_b32 a38, 0
v_accvgpr_write_b32 a39, 0
v_accvgpr_write_b32 a40, 0
v_accvgpr_write_b32 a41, 0
v_accvgpr_write_b32 a42, 0
v_accvgpr_write_b32 a43, 0
v_accvgpr_write_b32 a44, 0
v_accvgpr_write_b32 a45, 0
v_accvgpr_write_b32 a46, 0
v_accvgpr_write_b32 a47, 0
v_accvgpr_write_b32 a48, 0
v_accvgpr_write_b32 a49, 0
v_accvgpr_write_b32 a50, 0
v_accvgpr_write_b32 a51, 0
v_accvgpr_write_b32 a52, 0
v_accvgpr_write_b32 a53, 0
v_accvgpr_write_b32 a54, 0
v_accvgpr_write_b32 a55, 0
v_accvgpr_write_b32 a56, 0
v_accvgpr_write_b32 a57, 0
v_accvgpr_write_b32 a58, 0
v_accvgpr_write_b32 a59, 0
v_accvgpr_write_b32 a60, 0
v_accvgpr_write_b32 a61, 0
v_accvgpr_write_b32 a62, 0
v_accvgpr_write_b32 a63, 0
s_nop 1
v_mov_b32 v250, v242
v_mov_b32 v249, v243
v_xor_b32 v242, 64, v240
v_xor_b32 v243, 96, v240
s_mov_b32 s93, 0
buffer_load_dwordx4 a[128:131], v254, s[40:43], s93 offen
s_mov_b32 s93, 7168
buffer_load_dwordx4 a[132:135], v254, s[40:43], s93 offen
s_mov_b32 s93, 14336
buffer_load_dwordx4 a[136:139], v254, s[40:43], s93 offen
s_mov_b32 s93, 21504
buffer_load_dwordx4 a[140:143], v254, s[40:43], s93 offen
s_mov_b32 s93, 28672
buffer_load_dwordx4 a[144:147], v254, s[40:43], s93 offen
s_mov_b32 s93, 35840
buffer_load_dwordx4 a[148:151], v254, s[40:43], s93 offen
s_mov_b32 s93, 43008
buffer_load_dwordx4 a[152:155], v254, s[40:43], s93 offen
s_mov_b32 s93, 50176
buffer_load_dwordx4 a[156:159], v254, s[40:43], s93 offen
s_mov_b32 s93, 57344
buffer_load_dwordx4 a[160:163], v254, s[40:43], s93 offen
s_mov_b32 s93, 64512
buffer_load_dwordx4 a[164:167], v254, s[40:43], s93 offen
s_mov_b32 s93, 71680
buffer_load_dwordx4 a[168:171], v254, s[40:43], s93 offen
s_mov_b32 s93, 78848
buffer_load_dwordx4 a[172:175], v254, s[40:43], s93 offen
s_mov_b32 s93, 86016
buffer_load_dwordx4 a[176:179], v254, s[40:43], s93 offen
s_mov_b32 s93, 93184
buffer_load_dwordx4 a[180:183], v254, s[40:43], s93 offen
s_mov_b32 s93, 100352
buffer_load_dwordx4 a[184:187], v254, s[40:43], s93 offen
s_mov_b32 s93, 107520
buffer_load_dwordx4 a[188:191], v254, s[40:43], s93 offen
s_branch .La5_LA_SR4
.La5_LO_W:
s_mov_b32 s93, 114688
buffer_load_dwordx4 a[192:195], v228, s[40:43], s93 offen
s_mov_b32 s93, 121856
buffer_load_dwordx4 a[196:199], v228, s[40:43], s93 offen
s_mov_b32 s93, 129024
buffer_load_dwordx4 a[200:203], v228, s[40:43], s93 offen
s_mov_b32 s93, 136192
buffer_load_dwordx4 a[204:207], v228, s[40:43], s93 offen
s_mov_b32 s93, 143360
buffer_load_dwordx4 a[208:211], v228, s[40:43], s93 offen
s_mov_b32 s93, 150528
buffer_load_dwordx4 a[212:215], v228, s[40:43], s93 offen
s_mov_b32 s93, 157696
buffer_load_dwordx4 a[216:219], v228, s[40:43], s93 offen
s_mov_b32 s93, 164864
buffer_load_dwordx4 a[220:223], v228, s[40:43], s93 offen
s_mov_b32 s93, 172032
buffer_load_dwordx4 a[224:227], v228, s[40:43], s93 offen
s_mov_b32 s93, 179200
buffer_load_dwordx4 a[228:231], v228, s[40:43], s93 offen
s_mov_b32 s93, 186368
buffer_load_dwordx4 a[232:235], v228, s[40:43], s93 offen
s_mov_b32 s93, 193536
buffer_load_dwordx4 a[236:239], v228, s[40:43], s93 offen
s_mov_b32 s93, 200704
buffer_load_dwordx4 a[240:243], v228, s[40:43], s93 offen
s_mov_b32 s93, 207872
buffer_load_dwordx4 a[244:247], v228, s[40:43], s93 offen
s_mov_b32 s93, 215040
buffer_load_dwordx4 a[248:251], v228, s[40:43], s93 offen
s_mov_b32 s93, 222208
buffer_load_dwordx4 a[252:255], v228, s[40:43], s93 offen
s_branch .La5_LO_START
.La5_LT_VAR:
v_mov_b32 v230, 0
v_lshlrev_b32 v232, 16, v96
v_and_b32 v233, 0xffff0000, v96
v_sub_f32 v232, v232, v244
v_sub_f32 v233, v233, v244
v_fmac_f32 v230, v232, v232
v_fmac_f32 v230, v233, v233
v_lshlrev_b32 v232, 16, v97
v_and_b32 v233, 0xffff0000, v97
v_sub_f32 v232, v232, v244
v_sub_f32 v233, v233, v244
v_fmac_f32 v230, v232, v232
v_fmac_f32 v230, v233, v233
v_lshlrev_b32 v232, 16, v98
v_and_b32 v233, 0xffff0000, v98
v_sub_f32 v232, v232, v244
v_sub_f32 v233, v233, v244
v_fmac_f32 v230, v232, v232
v_fmac_f32 v230, v233, v233
v_lshlrev_b32 v232, 16, v99
v_and_b32 v233, 0xffff0000, v99
v_sub_f32 v232, v232, v244
v_sub_f32 v233, v233, v244
v_fmac_f32 v230, v232, v232
v_fmac_f32 v230, v233, v233
v_lshlrev_b32 v232, 16, v100
v_and_b32 v233, 0xffff0000, v100
v_sub_f32 v232, v232, v244
v_sub_f32 v233, v233, v244
v_fmac_f32 v230, v232, v232
v_fmac_f32 v230, v233, v233
v_lshlrev_b32 v232, 16, v101
v_and_b32 v233, 0xffff0000, v101
v_sub_f32 v232, v232, v244
v_sub_f32 v233, v233, v244
v_fmac_f32 v230, v232, v232
v_fmac_f32 v230, v233, v233
v_lshlrev_b32 v232, 16, v102
v_and_b32 v233, 0xffff0000, v102
v_sub_f32 v232, v232, v244
v_sub_f32 v233, v233, v244
v_fmac_f32 v230, v232, v232
v_fmac_f32 v230, v233, v233
v_lshlrev_b32 v232, 16, v103
v_and_b32 v233, 0xffff0000, v103
v_sub_f32 v232, v232, v244
v_sub_f32 v233, v233, v244
v_fmac_f32 v230, v232, v232
v_fmac_f32 v230, v233, v233
v_lshlrev_b32 v232, 16, v104
v_and_b32 v233, 0xffff0000, v104
v_sub_f32 v232, v232, v244
v_sub_f32 v233, v233, v244
v_fmac_f32 v230, v232, v232
v_fmac_f32 v230, v233, v233
v_lshlrev_b32 v232, 16, v105
v_and_b32 v233, 0xffff0000, v105
v_sub_f32 v232, v232, v244
v_sub_f32 v233, v233, v244
v_fmac_f32 v230, v232, v232
v_fmac_f32 v230, v233, v233
v_lshlrev_b32 v232, 16, v106
v_and_b32 v233, 0xffff0000, v106
v_sub_f32 v232, v232, v244
v_sub_f32 v233, v233, v244
v_fmac_f32 v230, v232, v232
v_fmac_f32 v230, v233, v233
v_lshlrev_b32 v232, 16, v107
v_and_b32 v233, 0xffff0000, v107
v_sub_f32 v232, v232, v244
v_sub_f32 v233, v233, v244
v_fmac_f32 v230, v232, v232
v_fmac_f32 v230, v233, v233
v_lshlrev_b32 v232, 16, v108
v_and_b32 v233, 0xffff0000, v108
v_sub_f32 v232, v232, v244
v_sub_f32 v233, v233, v244
v_fmac_f32 v230, v232, v232
v_fmac_f32 v230, v233, v233
v_lshlrev_b32 v232, 16, v109
v_and_b32 v233, 0xffff0000, v109
v_sub_f32 v232, v232, v244
v_sub_f32 v233, v233, v244
v_fmac_f32 v230, v232, v232
v_fmac_f32 v230, v233, v233
v_lshlrev_b32 v232, 16, v110
v_and_b32 v233, 0xffff0000, v110
v_sub_f32 v232, v232, v244
v_sub_f32 v233, v233, v244
v_fmac_f32 v230, v232, v232
v_fmac_f32 v230, v233, v233
v_lshlrev_b32 v232, 16, v111
v_and_b32 v233, 0xffff0000, v111
v_sub_f32 v232, v232, v244
v_sub_f32 v233, v233, v244
v_fmac_f32 v230, v232, v232
v_fmac_f32 v230, v233, v233
s_nop 1
v_add_f32_dpp v230, v230, v230 quad_perm:[1,0,3,2] row_mask:0xf bank_mask:0xf
s_nop 1
v_add_f32_dpp v230, v230, v230 quad_perm:[2,3,0,1] row_mask:0xf bank_mask:0xf
s_nop 1
v_add_f32_dpp v230, v230, v230 row_ror:8 row_mask:0xf bank_mask:0xf
v_mov_b32 v231, v230
s_nop 1
v_permlane16_swap_b32 v231, v230
s_nop 1
v_add_f32 v230, v230, v231
v_mov_b32 v231, v230
s_nop 1
v_permlane32_swap_b32 v231, v230
s_nop 1
v_add_f32 v230, v230, v231
v_mul_f32 v230, 0x3a800000, v230
s_branch .La5_LT_VOK
.La5_LA_KSPIN:
s_mov_b32 s93, 0
.La5_LA_KSPIN_L:
s_add_u32 s93, s93, 1
s_cmp_gt_u32 s93, 2097152
s_cbranch_scc1 .La5_LA_KOK
s_sleep 8
buffer_load_dword v250, v251, s[64:67], s20 offen sc1
s_waitcnt vmcnt(0)
v_cmp_eq_u32 vcc, v250, v249
s_nop 1
s_or_b32 vcc_hi, vcc_hi, s79
s_cmp_eq_u64 vcc, -1
s_cbranch_scc0 .La5_LA_KSPIN_L
s_branch .La5_LA_KOK
.La5_LA_QSPIN:
s_mov_b32 s93, 0
.La5_LA_QSPIN_L:
s_add_u32 s93, s93, 1
s_cmp_gt_u32 s93, 2097152
s_cbranch_scc1 .La5_LA_QOK
s_sleep 8
buffer_load_dword v250, v251, s[64:67], s20 offen sc1
s_waitcnt vmcnt(0)
v_cmp_eq_u32 vcc, v250, v249
s_nop 1
s_and_b32 s92, vcc_lo, 3
s_cmp_eq_u32 s92, 3
s_cbranch_scc0 .La5_LA_QSPIN_L
s_branch .La5_LA_QOK
.La5_LEND:
.La5_LCODEEND:
)ASM"
        :: "{s[0:1]}"(__builtin_amdgcn_kernarg_segment_ptr()), "{s2}"(blockIdx.x), "{s3}"(__builtin_amdgcn_readfirstlane(threadIdx.x >> 6)), "{s[4:5]}"(DBGP)
        : "v0","v1","v2","v3","v4","v5","v6","v7","v8","v9","v10","v11","v12","v13","v14","v15","v16","v17","v18","v19","v20","v21","v22","v23","v24","v25","v26","v27","v28","v29","v30","v31","v32","v33","v34","v35","v36","v37","v38","v39","v40","v41","v42","v43","v44","v45","v46","v47","v48","v49","v50","v51","v52","v53","v54","v55","v56","v57","v58","v59","v60","v61","v62","v63","v64","v65","v66","v67","v68","v69","v70","v71","v72","v73","v74","v75","v76","v77","v78","v79","v80","v81","v82","v83","v84","v85","v86","v87","v88","v89","v90","v91","v92","v93","v94","v95","v96","v97","v98","v99","v100","v101","v102","v103","v104","v105","v106","v107","v108","v109","v110","v111","v112","v113","v114","v115","v116","v117","v118","v119","v120","v121","v122","v123","v124","v125","v126","v127","v128","v129","v130","v131","v132","v133","v134","v135","v136","v137","v138","v139","v140","v141","v142","v143","v144","v145","v146","v147","v148","v149","v150","v151","v152","v153","v154","v155","v156","v157","v158","v159","v160","v161","v162","v163","v164","v165","v166","v167","v168","v169","v170","v171","v172","v173","v174","v175","v176","v177","v178","v179","v180","v181","v182","v183","v184","v185","v186","v187","v188","v189","v190","v191","v192","v193","v194","v195","v196","v197","v198","v199","v200","v201","v202","v203","v204","v205","v206","v207","v208","v209","v210","v211","v212","v213","v214","v215","v216","v217","v218","v219","v220","v221","v222","v223","v224","v225","v226","v227","v228","v229","v230","v231","v232","v233","v234","v235","v236","v237","v238","v239","v240","v241","v242","v243","v244","v245","v246","v247","v248","v249","v250","v251","v252","v253","v254","v255","a0","a1","a2","a3","a4","a5","a6","a7","a8","a9","a10","a11","a12","a13","a14","a15","a16","a17","a18","a19","a20","a21","a22","a23","a24","a25","a26","a27","a28","a29","a30","a31","a32","a33","a34","a35","a36","a37","a38","a39","a40","a41","a42","a43","a44","a45","a46","a47","a48","a49","a50","a51","a52","a53","a54","a55","a56","a57","a58","a59","a60","a61","a62","a63","a64","a65","a66","a67","a68","a69","a70","a71","a72","a73","a74","a75","a76","a77","a78","a79","a80","a81","a82","a83","a84","a85","a86","a87","a88","a89","a90","a91","a92","a93","a94","a95","a96","a97","a98","a99","a100","a101","a102","a103","a104","a105","a106","a107","a108","a109","a110","a111","a112","a113","a114","a115","a116","a117","a118","a119","a120","a121","a122","a123","a124","a125","a126","a127","a128","a129","a130","a131","a132","a133","a134","a135","a136","a137","a138","a139","a140","a141","a142","a143","a144","a145","a146","a147","a148","a149","a150","a151","a152","a153","a154","a155","a156","a157","a158","a159","a160","a161","a162","a163","a164","a165","a166","a167","a168","a169","a170","a171","a172","a173","a174","a175","a176","a177","a178","a179","a180","a181","a182","a183","a184","a185","a186","a187","a188","a189","a190","a191","a192","a193","a194","a195","a196","a197","a198","a199","a200","a201","a202","a203","a204","a205","a206","a207","a208","a209","a210","a211","a212","a213","a214","a215","a216","a217","a218","a219","a220","a221","a222","a223","a224","a225","a226","a227","a228","a229","a230","a231","a232","a233","a234","a235","a236","a237","a238","a239","a240","a241","a242","a243","a244","a245","a246","a247","a248","a249","a250","a251","a252","a253","a254","a255","s6","s7","s8","s9","s10","s11","s12","s13","s14","s15","s16","s17","s18","s19","s20","s21","s22","s23","s24","s25","s26","s27","s28","s29","s30","s31","s32","s33","s34","s35","s36","s37","s38","s39","s40","s41","s42","s43","s44","s45","s46","s47","s48","s49","s50","s51","s52","s53","s54","s55","s56","s57","s58","s59","s60","s61","s62","s63","s64","s65","s66","s67","s68","s69","s70","s71","s72","s73","s74","s75","s76","s77","s78","s79","s80","s81","s82","s83","s84","s85","s86","s87","s88","s89","s90","s91","s92","s93","s94","s95","s96","s97","s98","s99","s100","s101","vcc","scc","m0","memory");
}

extern "C" void kernel_launch(void* const* d_in, const int* in_sizes, int n_in, void* d_out, int out_size, void* d_ws, size_t ws_size,
                              hipStream_t stream) {
    (void)hipFuncSetAttribute((const void*)fa_kernel, hipFuncAttributeMaxDynamicSharedMemorySize, LDS_BYTES);
    fa_kernel<<<256, 256, LDS_BYTES, stream>>>((const bf16*)d_in[0], (const bf16*)d_in[1], (const bf16*)d_in[2], (const bf16*)d_in[3], (const bf16*)d_in[4], (char*)d_ws,
                                               (const bf16*)d_in[5], (const bf16*)d_in[8], (const bf16*)d_in[9], (const bf16*)d_in[10], (const bf16*)d_in[7], (bf16*)d_out);
}
```

```cpp
#include <hip/hip_runtime.h>
#include <hip/hip_bf16.h>
#include <stdint.h>
#pragma clang diagnostic ignored "-Winline-asm"
#pragma clang diagnostic ignored "-Woverlength-strings"

typedef __hip_bfloat16 bf16;
#define DBGP ((unsigned*)nullptr)
constexpr int LDS_BYTES = 163840;
extern "C" __global__ __attribute__((aligned(4096))) __launch_bounds__(256, 1) void fa_kernel(const bf16* __restrict__ px, const bf16* __restrict__ ckv, const bf16* __restrict__ wq,
                                                    const bf16* __restrict__ wuk, const bf16* __restrict__ wuv, char* pws,
                                                    const bf16* __restrict__ pwo, const bf16* __restrict__ pwg, const bf16* __restrict__ pwu,
                                                    const bf16* __restrict__ pwd, const bf16* __restrict__ pwr, bf16* __restrict__ pout) {
    asm volatile(R"ASM(
; ---- roles: head = 4 xcd + (wg >> 6), j = (wg >> 3) & 7; odd j: block j >> 1 (then helps with block 7 - (j >> 1)), even j: block 7 - (j >> 1). First operands, addresses
.La5_LSTART:
s_load_dwordx16 s[16:31], s[0:1], 0
s_load_dwordx8 s[32:39], s[0:1], 64
.p2align 6
v_mbcnt_lo_u32_b32 v64, -1, 0
v_mbcnt_hi_u32_b32 v64, -1, v64
s_getpc_b64 s[92:93]
.La5_LPC:
s_sub_u32 s48, s92, .La5_LPC-.La5_LSTART
s_subb_u32 s49, s93, 0
s_and_b32 s49, s49, 0xffff
s_mov_b32 s50, .La5_LCODEEND-.La5_LSTART
s_mov_b32 s51, 0x20000
s_lshr_b32 s97, s2, 3
s_lshl2_add_u32 s87, s97, s3
v_lshlrev_b32 v165, 7, v64
s_lshl_b32 s87, s87, 13
buffer_load_dword v254, v165, s[48:51], s87 offen
s_and_b32 s92, s2, 7
s_lshr_b32 s93, s2, 6
s_lshl2_add_u32 s72, s92, s93
s_bfe_u32 s73, s2, 0x30003
s_lshr_b32 s74, s73, 1
s_xor_b32 s92, s74, 7
s_and_b32 s75, s73, 1
s_cselect_b32 s76, s74, s92
s_lshr_b32 s77, s3, 1
s_and_b32 s78, s3, 1
v_and_b32 v65, 15, v64
v_lshrrev_b32 v66, 4, v64
v_lshlrev_b32 v255, 4, v64
v_lshlrev_b32 v67, 1, v66
v_xor_b32 v67, v67, v65
v_lshlrev_b32 v67, 4, v67
v_lshl_add_u32 v67, v66, 13, v67
s_lshl_b32 s94, s78, 17
v_add_u32 v232, s94, v67
v_add_u32 v233, 32768, v232
v_add_u32 v234, 65536, v232
v_add_u32 v235, 98304, v232
v_and_b32 v67, 7, v64
v_xor_b32 v68, v67, v66
v_lshlrev_b32 v68, 4, v68
v_lshrrev_b32 v69, 3, v64
v_lshl_add_u32 v68, v69, 10, v68
s_lshl2_add_u32 s94, s3, s93
s_lshl_b32 s82, s94, 11
s_lshl_b32 s94, s94, 14
v_add_u32 v224, s94, v68
v_xor_b32 v225, 0x2040, v224
v_xor_b32 v226, 16384, v224
v_xor_b32 v227, 16384, v225
v_xor_b32 v228, 32768, v224
v_xor_b32 v229, 32768, v225
v_xor_b32 v230, 49152, v224
v_xor_b32 v231, 49152, v225
s_mul_i32 s94, s77, 8448
s_mul_i32 s95, s78, 4224
s_add_u32 s83, s94, s95
s_add_u32 s83, s83, 96256
v_lshlrev_b32 v167, 7, v67
v_lshl_add_u32 v167, v69, 13, v167
v_lshrrev_b32 v164, 5, v64
v_lshl_add_u32 v164, v164, 28, v167
v_lshl_add_u32 v165, v66, 28, v165
v_lshlrev_b32 v166, 10, v65
v_lshl_add_u32 v166, v66, 7, v166
v_lshrrev_b32 v68, 4, v64
v_lshl_add_u32 v166, v68, 28, v166
s_lshl1_add_u32 s97, s97, s78
s_lshl_b32 s7, s97, 13
s_lshl_b32 s97, s97, 15
s_lshl_b32 s96, s93, 8
s_sub_u32 s7, s7, s96
s_sub_u32 s87, s87, s96
s_sub_u32 s97, s97, s96
s_add_u32 s86, s97, 2097152
s_lshl2_add_u32 s101, s72, s3
s_lshl_b32 s101, s101, 11
s_lshl2_add_u32 s100, s93, s3
s_lshl_b32 s100, s100, 14
v_lshlrev_b32 v118, 11, v67
s_lshl_b32 s93, s3, 9
v_lshl_add_u32 v119, v69, 4, v118
v_add_u32 v119, s93, v119
s_lshl_b32 s97, s2, 14
s_lshl_b32 s94, s72, 8
s_lshl_b32 s95, s76, 18
s_sub_u32 s46, 4194304, s94
s_mov_b32 s47, 0x20000
s_mov_b32 s42, 262144
s_mov_b32 s43, 0x20000
s_mov_b32 s62, 16384
s_mov_b32 s63, 0x20000
s_mov_b32 s79, 512
s_mov_b32 s80, 2097152
s_add_u32 s100, s100, 512
s_mov_b32 s81, 262144
s_cmp_eq_u32 s77, 0
s_waitcnt lgkmcnt(0)
s_cselect_b64 s[92:93], s[22:23], s[24:25]
s_add_u32 s44, s92, s94
s_addc_u32 s45, s93, 0
s_and_b32 s45, s45, 0xffff
s_add_u32 s40, s18, s95
s_addc_u32 s41, s19, 0
s_and_b32 s41, s41, 0xffff
s_add_u32 s60, s16, s97
s_addc_u32 s61, s17, 0
s_and_b32 s61, s61, 0xffff
buffer_load_dword v254, v164, s[44:47], s86 offen
s_sub_u32 s86, s86, 2097152
buffer_load_dword v254, v165, s[40:43], s101 offen
buffer_load_dword v254, v166, s[40:43], s100 offen
s_add_u32 m0, s83, 0
s_nop 0
buffer_load_dwordx4 v232, s[44:47], s80 offen lds
s_add_u32 m0, s83, 1024
s_nop 0
buffer_load_dwordx4 v233, s[44:47], s80 offen lds
s_add_u32 m0, s83, 2176
s_nop 0
buffer_load_dwordx4 v234, s[44:47], s80 offen lds
s_add_u32 m0, s83, 3200
s_nop 0
buffer_load_dwordx4 v235, s[44:47], s80 offen lds
s_add_u32 s80, s80, s81
s_and_b32 s80, s80, 4194303
v_lshrrev_b32 v67, 1, v65
v_xor_b32 v67, v67, v66
v_lshlrev_b32 v67, 4, v67
v_lshl_add_u32 v67, v65, 7, v67
s_lshl_b32 s92, s77, 12
v_add_u32 v236, s92, v67
v_xor_b32 v237, 64, v236
v_bfe_u32 v120, v64, 2, 2
v_add_u32 v67, 1, v66
v_lshrrev_b32 v67, 1, v67
v_lshlrev_b32 v67, 7, v67
v_lshl_add_u32 v67, v66, 11, v67
v_lshl_add_u32 v67, v120, 8, v67
v_and_b32 v68, 3, v64
v_lshl_add_u32 v67, v68, 3, v67
s_mul_i32 s92, s78, 8448
v_lshl_add_u32 v67, v120, 5, v67
v_add_u32 v67, s92, v67
v_add_u32 v240, 96256, v67
v_xor_b32 v241, 32, v240
v_xor_b32 v242, 64, v240
v_xor_b32 v243, 96, v240
s_add_u32 s64, s26, 142606336
s_addc_u32 s65, s27, 0
s_and_b32 s65, s65, 0xffff
s_mov_b32 s66, 3145728
s_mov_b32 s67, 0x20000
s_mov_b32 s88, 4096
s_lshr_b32 s92, s2, 1
s_lshl_b32 s92, s92, 15
s_and_b32 s93, s2, 1
s_lshl_b32 s93, s93, 7
s_add_u32 s92, s92, s93
s_lshl_b32 s93, s3, 13
s_add_u32 s92, s92, s93
s_add_u32 s92, s92, 88080384
s_add_u32 s56, s26, s92
s_addc_u32 s57, s27, 0
s_and_b32 s57, s57, 0xffff
s_mov_b32 s58, 8576
s_mov_b32 s59, 0x20000
v_and_b32 v71, 0x380, v255
v_and_b32 v127, 0x70, v255
v_lshl_add_u32 v71, v71, 1, v127
v_xor_b32 v67, 3, v69
v_min_u32 v67, 1, v67
v_xor_b32 v67, 1, v67
s_mov_b32 s93, 0
v_mul_lo_u32 v67, v67, s93
v_add_u32 v168, v67, v255
v_and_b32 v67, 3, v69
v_xor_b32 v67, 1, v67
v_min_u32 v67, 1, v67
v_xor_b32 v67, 1, v67
v_and_b32 v67, s2, v67
v_mul_lo_u32 v67, v67, s93
v_add_u32 v71, v71, v67
s_lshl_b32 s92, s3, 3
v_lshlrev_b32 v126, 1, v127
v_or_b32 v126, 65536, v126
v_add_u32 v127, s92, v126
s_add_u32 s52, s26, 171966464
s_addc_u32 s53, s27, 0
s_and_b32 s53, s53, 0xffff
s_mov_b32 s54, 950272
s_mov_b32 s55, 0x20000
s_lshl2_add_u32 s95, s2, s3
s_lshl_b32 s91, s95, 3
s_lshl_b32 s95, s95, 10
s_lshl_b32 s89, s76, 10
v_lshl_add_u32 v167, v69, 28, v167
s_mov_b32 s96, s87
s_mov_b32 s84, 33792
s_mov_b32 s85, 0
s_add_u32 s97, s83, 67584
s_mov_b32 s90, s83
s_lshl_b32 s92, s72, 8
s_sub_u32 s93, 8388608, s92
s_add_u32 s68, s20, s92
s_addc_u32 s69, s21, 0
s_and_b32 s69, s69, 0xffff
s_mov_b32 s70, s93
s_mov_b32 s71, 0x20000
s_lshr_b32 s92, s20, 7
s_sub_u32 s92, 3, s92
s_bfe_u32 s99, s92, 0x20001
s_lshr_b32 s93, s2, 6
s_sub_u32 s99, s99, s93
s_lshl_b32 s92, s99, 8
s_lshr_b32 s93, s2, 3
s_lshl2_add_u32 s93, s93, s3
s_lshl_b32 s98, s93, 16
s_add_u32 s98, s98, s92
v_lshlrev_b32 v170, 4, v65
v_lshl_add_u32 v169, v66, 13, v170
v_lshl_add_u32 v170, v66, 9, v170
s_lshl_b32 s93, s93, 12
v_add_u32 v170, s93, v170
s_and_b32 s93, s2, 7
s_lshl_b32 s92, s93, 19
s_add_u32 s92, s92, 138412032
s_add_u32 s4, s26, s92
s_addc_u32 s5, s27, 0
s_and_b32 s5, s5, 0xffff
s_mov_b32 s6, 0
s_mov_b32 s100, 1073741824
s_add_u32 s101, s91, 65536
v_lshrrev_b32 v171, 1, v64
v_lshlrev_b32 v171, 8, v171
v_and_or_b32 v171, v255, 16, v171
s_lshl_b32 s93, s93, 5
s_add_u32 s93, s93, 64512
v_add_u32 v171, s93, v171
s_mov_b64 exec, 0
s_cmp_eq_u32 s3, 0
s_cbranch_scc1 .La5_LW1_0
s_cmp_eq_u32 s3, 1
s_cbranch_scc1 .La5_LW1_1
s_cmp_eq_u32 s3, 2
s_cbranch_scc1 .La5_LW1_2
s_branch .La5_LW1_3
.La5_LW1_R0:
s_mov_b64 exec, -1
s_xor_b32 m0, s82, 0
s_nop 0
buffer_load_dwordx4 v224, s[40:43], s79 offen lds
s_xor_b32 m0, s82, 1024
s_nop 0
buffer_load_dwordx4 v225, s[40:43], s79 offen lds
s_xor_b32 m0, s82, 2048
s_nop 0
buffer_load_dwordx4 v226, s[40:43], s79 offen lds
s_xor_b32 m0, s82, 3072
s_nop 0
buffer_load_dwordx4 v227, s[40:43], s79 offen lds
s_xor_b32 m0, s82, 4096
s_nop 0
buffer_load_dwordx4 v228, s[40:43], s79 offen lds
s_xor_b32 m0, s82, 5120
s_nop 0
buffer_load_dwordx4 v229, s[40:43], s79 offen lds
s_xor_b32 m0, s82, 6144
s_nop 0
buffer_load_dwordx4 v230, s[40:43], s79 offen lds
s_xor_b32 m0, s82, 7168
s_nop 0
buffer_load_dwordx4 v231, s[40:43], s79 offen lds
s_add_u32 s79, s79, 128
s_and_b32 s79, s79, 1023
s_add_u32 m0, s83, 16896
s_nop 0
buffer_load_dwordx4 v232, s[44:47], s80 offen lds
s_add_u32 m0, s83, 17920
s_nop 0
buffer_load_dwordx4 v233, s[44:47], s80 offen lds
s_add_u32 m0, s83, 19072
s_nop 0
buffer_load_dwordx4 v234, s[44:47], s80 offen lds
s_add_u32 m0, s83, 20096
s_nop 0
buffer_load_dwordx4 v235, s[44:47], s80 offen lds
s_add_u32 s80, s80, s81
s_and_b32 s80, s80, 4194303
buffer_load_dwordx2 v[252:253], off, s[64:67], 0 sc1
s_waitcnt vmcnt(5)
s_barrier
ds_read_b128 v[128:131], v236 offset:24576
ds_read_b64_tr_b16 v[192:193], v240 offset:0
ds_read_b64_tr_b16 v[194:195], v240 offset:1024
ds_read_b64_tr_b16 v[196:197], v241 offset:0
ds_read_b64_tr_b16 v[198:199], v241 offset:1024
ds_read_b64_tr_b16 v[200:201], v242 offset:0
ds_read_b64_tr_b16 v[202:203], v242 offset:1024
ds_read_b64_tr_b16 v[204:205], v243 offset:0
ds_read_b64_tr_b16 v[206:207], v243 offset:1024
ds_read_b64_tr_b16 v[208:209], v240 offset:128
ds_read_b64_tr_b16 v[210:211], v240 offset:1152
ds_read_b64_tr_b16 v[212:213], v241 offset:128
ds_read_b64_tr_b16 v[214:215], v241 offset:1152
ds_read_b64_tr_b16 v[216:217], v242 offset:128
ds_read_b64_tr_b16 v[218:219], v242 offset:1152
ds_read_b64_tr_b16 v[220:221], v243 offset:128
ds_read_b64_tr_b16 v[222:223], v243 offset:1152
ds_read_b128 v[132:135], v236 offset:26624
ds_read_b128 v[136:139], v236 offset:16384
ds_read_b128 v[140:143], v236 offset:18432
ds_read_b128 v[144:147], v236 offset:8192
ds_read_b128 v[148:151], v236 offset:10240
ds_read_b128 v[152:155], v236 offset:0
ds_read_b128 v[156:159], v236 offset:2048
s_waitcnt lgkmcnt(0)
s_cmp_eq_u32 s78, 1
s_nop 0
s_nop 0
s_cbranch_scc1 .La5_LV_START
; ---- K tile (waves 0, 2): 256 keys x 128 columns, K = 512; LayerNorm of 8 rows of x in the gaps (softmax scale folded in)
.La5_LK_START:
s_waitcnt vmcnt(1)
s_barrier
v_mfma_f32_16x16x32_bf16 a[224:227], v[192:195], v[128:131], 0
ds_read_b128 v[0:3], v237 offset:24576
s_add_u32 m0, s83, 33792
v_mfma_f32_16x16x32_bf16 a[228:231], v[196:199], v[128:131], 0
buffer_load_dwordx4 v232, s[44:47], s80 offen lds
v_mfma_f32_16x16x32_bf16 a[232:235], v[200:203], v[128:131], 0
ds_read_b64_tr_b16 v[32:33], v240 offset:16896
v_mfma_f32_16x16x32_bf16 a[236:239], v[204:207], v[128:131], 0
v_mfma_f32_16x16x32_bf16 a[208:211], v[208:211], v[128:131], 0
ds_read_b64_tr_b16 v[34:35], v240 offset:17920
s_add_u32 m0, s83, 34816
v_mfma_f32_16x16x32_bf16 a[212:215], v[212:215], v[128:131], 0
buffer_load_dwordx4 v233, s[44:47], s80 offen lds
v_mfma_f32_16x16x32_bf16 a[128:131], v[216:219], v[128:131], 0
ds_read_b64_tr_b16 v[36:37], v241 offset:16896
v_mfma_f32_16x16x32_bf16 a[132:135], v[220:223], v[128:131], 0
v_mfma_f32_16x16x32_bf16 a[216:219], v[192:195], v[132:135], 0
ds_read_b64_tr_b16 v[38:39], v241 offset:17920
s_add_u32 m0, s83, 35968
v_mfma_f32_16x16x32_bf16 a[220:223], v[196:199], v[132:135], 0
buffer_load_dwordx4 v234, s[44:47], s80 offen lds
v_mfma_f32_16x16x32_bf16 a[136:139], v[200:203], v[132:135], 0
ds_read_b64_tr_b16 v[40:41], v242 offset:16896
v_mfma_f32_16x16x32_bf16 a[140:143], v[204:207], v[132:135], 0
v_mfma_f32_16x16x32_bf16 a[192:195], v[208:211], v[132:135], 0
ds_read_b64_tr_b16 v[42:43], v242 offset:17920
s_add_u32 m0, s83, 36992
v_mfma_f32_16x16x32_bf16 a[196:199], v[212:215], v[132:135], 0
buffer_load_dwordx4 v235, s[44:47], s80 offen lds
v_mfma_f32_16x16x32_bf16 a[144:147], v[216:219], v[132:135], 0
ds_read_b64_tr_b16 v[44:45], v243 offset:16896
v_mfma_f32_16x16x32_bf16 a[148:151], v[220:223], v[132:135], 0
s_add_u32 s80, s80, s81
s_and_b32 s80, s80, 4194303
v_mfma_f32_16x16x32_bf16 a[200:203], v[192:195], v[136:139], 0
ds_read_b64_tr_b16 v[46:47], v243 offset:17920
s_xor_b32 m0, s82, 32768
v_mfma_f32_16x16x32_bf16 a[204:207], v[196:199], v[136:139], 0
buffer_load_dwordx4 v224, s[40:43], s79 offen lds
v_mfma_f32_16x16x32_bf16 a[152:155], v[200:203], v[136:139], 0
ds_read_b64_tr_b16 v[48:49], v240 offset:17024
v_mfma_f32_16x16x32_bf16 a[156:159], v[204:207], v[136:139], 0
v_mfma_f32_16x16x32_bf16 a[96:99], v[208:211], v[136:139], 0
ds_read_b64_tr_b16 v[50:51], v240 offset:18048
s_xor_b32 m0, s82, 33792
v_mfma_f32_16x16x32_bf16 a[100:103], v[212:215], v[136:139], 0
buffer_load_dwordx4 v225, s[40:43], s79 offen lds
v_mfma_f32_16x16x32_bf16 a[160:163], v[216:219], v[136:139], 0
ds_read_b64_tr_b16 v[52:53], v241 offset:17024
v_mfma_f32_16x16x32_bf16 a[164:167], v[220:223], v[136:139], 0
v_mfma_f32_16x16x32_bf16 a[104:107], v[192:195], v[140:143], 0
ds_read_b64_tr_b16 v[54:55], v241 offset:18048
s_xor_b32 m0, s82, 34816
v_mfma_f32_16x16x32_bf16 a[108:111], v[196:199], v[140:143], 0
buffer_load_dwordx4 v226, s[40:43], s79 offen lds
v_mfma_f32_16x16x32_bf16 a[168:171], v[200:203], v[140:143], 0
ds_read_b64_tr_b16 v[56:57], v242 offset:17024
v_mfma_f32_16x16x32_bf16 a[172:175], v[204:207], v[140:143], 0
v_mfma_f32_16x16x32_bf16 a[112:115], v[208:211], v[140:143], 0
ds_read_b64_tr_b16 v[58:59], v242 offset:18048
s_xor_b32 m0, s82, 35840
v_mfma_f32_16x16x32_bf16 a[116:119], v[212:215], v[140:143], 0
buffer_load_dwordx4 v227, s[40:43], s79 offen lds
v_mfma_f32_16x16x32_bf16 a[176:179], v[216:219], v[140:143], 0
ds_read_b64_tr_b16 v[60:61], v243 offset:17024
v_mfma_f32_16x16x32_bf16 a[180:183], v[220:223], v[140:143], 0
v_mfma_f32_16x16x32_bf16 a[120:123], v[192:195], v[144:147], 0
ds_read_b64_tr_b16 v[62:63], v243 offset:18048
s_xor_b32 m0, s82, 36864
v_mfma_f32_16x16x32_bf16 a[124:127], v[196:199], v[144:147], 0
buffer_load_dwordx4 v228, s[40:43], s79 offen lds
v_mfma_f32_16x16x32_bf16 a[184:187], v[200:203], v[144:147], 0
ds_read_b128 v[4:7], v237 offset:26624
v_mfma_f32_16x16x32_bf16 a[188:191], v[204:207], v[144:147], 0
v_mfma_f32_16x16x32_bf16 a[64:67], v[208:211], v[144:147], 0
ds_read_b128 v[8:11], v237 offset:16384
s_xor_b32 m0, s82, 37888
v_mfma_f32_16x16x32_bf16 a[68:71], v[212:215], v[144:147], 0
buffer_load_dwordx4 v229, s[40:43], s79 offen lds
v_mfma_f32_16x16x32_bf16 a[0:3], v[216:219], v[144:147], 0
ds_read_b128 v[12:15], v237 offset:18432
v_mfma_f32_16x16x32_bf16 a[4:7], v[220:223], v[144:147], 0
v_mfma_f32_16x16x32_bf16 a[72:75], v[192:195], v[148:151], 0
ds_read_b128 v[16:19], v237 offset:8192
s_xor_b32 m0, s82, 38912
v_mfma_f32_16x16x32_bf16 a[76:79], v[196:199], v[148:151], 0
buffer_load_dwordx4 v230, s[40:43], s79 offen lds
v_mfma_f32_16x16x32_bf16 a[8:11], v[200:203], v[148:151], 0
ds_read_b128 v[20:23], v237 offset:10240
v_mfma_f32_16x16x32_bf16 a[12:15], v[204:207], v[148:151], 0
v_mfma_f32_16x16x32_bf16 a[80:83], v[208:211], v[148:151], 0
ds_read_b128 v[24:27], v237 offset:0
s_xor_b32 m0, s82, 39936
v_mfma_f32_16x16x32_bf16 a[84:87], v[212:215], v[148:151], 0
buffer_load_dwordx4 v231, s[40:43], s79 offen lds
v_mfma_f32_16x16x32_bf16 a[16:19], v[216:219], v[148:151], 0
ds_read_b128 v[28:31], v237 offset:2048
v_mfma_f32_16x16x32_bf16 a[20:23], v[220:223], v[148:151], 0
s_add_u32 s79, s79, 128
s_and_b32 s79, s79, 1023
v_mfma_f32_16x16x32_bf16 a[88:91], v[192:195], v[152:155], 0
v_mfma_f32_16x16x32_bf16 a[92:95], v[196:199], v[152:155], 0
buffer_load_dword v70, v118, s[60:63], 0 offen
v_mfma_f32_16x16x32_bf16 a[24:27], v[200:203], v[152:155], 0
v_mfma_f32_16x16x32_bf16 a[28:31], v[204:207], v[152:155], 0
v_mfma_f32_16x16x32_bf16 a[240:243], v[208:211], v[152:155], 0
buffer_load_dwordx4 v[72:75], v119, s[60:63], 0 offen offset:0
v_mfma_f32_16x16x32_bf16 a[244:247], v[212:215], v[152:155], 0
v_mfma_f32_16x16x32_bf16 a[32:35], v[216:219], v[152:155], 0
v_mfma_f32_16x16x32_bf16 a[36:39], v[220:223], v[152:155], 0
buffer_load_dwordx4 v[76:79], v119, s[60:63], 0 offen offset:128
v_mfma_f32_16x16x32_bf16 a[248:251], v[192:195], v[156:159], 0
v_mfma_f32_16x16x32_bf16 a[252:255], v[196:199], v[156:159], 0
v_mfma_f32_16x16x32_bf16 a[40:43], v[200:203], v[156:159], 0
buffer_load_dwordx4 v[80:83], v119, s[60:63], 0 offen offset:256
v_mfma_f32_16x16x32_bf16 a[44:47], v[204:207], v[156:159], 0
v_mfma_f32_16x16x32_bf16 a[48:51], v[208:211], v[156:159], 0
v_mfma_f32_16x16x32_bf16 a[52:55], v[212:215], v[156:159], 0
buffer_load_dwordx4 v[84:87], v119, s[60:63], 0 offen offset:384
v_mfma_f32_16x16x32_bf16 a[56:59], v[216:219], v[156:159], 0
v_mfma_f32_16x16x32_bf16 a[60:63], v[220:223], v[156:159], 0
s_mov_b64 exec, 0
s_cmp_eq_u32 s3, 0
s_cbranch_scc1 .La5_LW2_0
s_branch .La5_LW2_2
.La5_LW2_R0:
s_mov_b64 exec, -1
s_waitcnt lgkmcnt(0)
s_waitcnt vmcnt(5)
s_barrier
v_mfma_f32_16x16x32_bf16 a[224:227], v[32:35], v[0:3], a[224:227]
ds_read_b128 v[128:131], v236 offset:57344
s_add_u32 m0, s83, 50688
v_mfma_f32_16x16x32_bf16 a[228:231], v[36:39], v[0:3], a[228:231]
buffer_load_dwordx4 v232, s[44:47], s80 offen lds
v_mfma_f32_16x16x32_bf16 a[232:235], v[40:43], v[0:3], a[232:235]
ds_read_b64_tr_b16 v[192:193], v240 offset:33792
v_mfma_f32_16x16x32_bf16 a[236:239], v[44:47], v[0:3], a[236:239]
v_mfma_f32_16x16x32_bf16 a[208:211], v[48:51], v[0:3], a[208:211]
ds_read_b64_tr_b16 v[194:195], v240 offset:34816
s_add_u32 m0, s83, 51712
v_mfma_f32_16x16x32_bf16 a[212:215], v[52:55], v[0:3], a[212:215]
buffer_load_dwordx4 v233, s[44:47], s80 offen lds
v_mfma_f32_16x16x32_bf16 a[128:131], v[56:59], v[0:3], a[128:131]
ds_read_b64_tr_b16 v[196:197], v241 offset:33792
v_mfma_f32_16x16x32_bf16 a[132:135], v[60:63], v[0:3], a[132:135]
v_mfma_f32_16x16x32_bf16 a[216:219], v[32:35], v[4:7], a[216:219]
ds_read_b64_tr_b16 v[198:199], v241 offset:34816
s_add_u32 m0, s83, 52864
v_mfma_f32_16x16x32_bf16 a[220:223], v[36:39], v[4:7], a[220:223]
buffer_load_dwordx4 v234, s[44:47], s80 offen lds
v_mfma_f32_16x16x32_bf16 a[136:139], v[40:43], v[4:7], a[136:139]
ds_read_b64_tr_b16 v[200:201], v242 offset:33792
v_mfma_f32_16x16x32_bf16 a[140:143], v[44:47], v[4:7], a[140:143]
v_mfma_f32_16x16x32_bf16 a[192:195], v[48:51], v[4:7], a[192:195]
ds_read_b64_tr_b16 v[202:203], v242 offset:34816
s_add_u32 m0, s83, 53888
v_mfma_f32_16x16x32_bf16 a[196:199], v[52:55], v[4:7], a[196:199]
buffer_load_dwordx4 v235, s[44:47], s80 offen lds
v_mfma_f32_16x16x32_bf16 a[144:147], v[56:59], v[4:7], a[144:147]
ds_read_b64_tr_b16 v[204:205], v243 offset:33792
v_mfma_f32_16x16x32_bf16 a[148:151], v[60:63], v[4:7], a[148:151]
s_add_u32 s80, s80, s81
s_and_b32 s80, s80, 4194303
v_mfma_f32_16x16x32_bf16 a[200:203], v[32:35], v[8:11], a[200:203]
ds_read_b64_tr_b16 v[206:207], v243 offset:34816
s_xor_b32 m0, s82, 0
v_mfma_f32_16x16x32_bf16 a[204:207], v[36:39], v[8:11], a[204:207]
buffer_load_dwordx4 v224, s[40:43], s79 offen lds
v_mfma_f32_16x16x32_bf16 a[152:155], v[40:43], v[8:11], a[152:155]
ds_read_b64_tr_b16 v[208:209], v240 offset:33920
v_mfma_f32_16x16x32_bf16 a[156:159], v[44:47], v[8:11], a[156:159]
v_mfma_f32_16x16x32_bf16 a[96:99], v[48:51], v[8:11], a[96:99]
ds_read_b64_tr_b16 v[210:211], v240 offset:34944
s_xor_b32 m0, s82, 1024
v_mfma_f32_16x16x32_bf16 a[100:103], v[52:55], v[8:11], a[100:103]
buffer_load_dwordx4 v225, s[40:43], s79 offen lds
v_mfma_f32_16x16x32_bf16 a[160:163], v[56:59], v[8:11], a[160:163]
ds_read_b64_tr_b16 v[212:213], v241 offset:33920
v_mfma_f32_16x16x32_bf16 a[164:167], v[60:63], v[8:11], a[164:167]
v_mfma_f32_16x16x32_bf16 a[104:107], v[32:35], v[12:15], a[104:107]
ds_read_b64_tr_b16 v[214:215], v241 offset:34944
s_xor_b32 m0, s82, 2048
v_mfma_f32_16x16x32_bf16 a[108:111], v[36:39], v[12:15], a[108:111]
buffer_load_dwordx4 v226, s[40:43], s79 offen lds
v_mfma_f32_16x16x32_bf16 a[168:171], v[40:43], v[12:15], a[168:171]
ds_read_b64_tr_b16 v[216:217], v242 offset:33920
v_mfma_f32_16x16x32_bf16 a[172:175], v[44:47], v[12:15], a[172:175]
v_mfma_f32_16x16x32_bf16 a[112:115], v[48:51], v[12:15], a[112:115]
ds_read_b64_tr_b16 v[218:219], v242 offset:34944
s_xor_b32 m0, s82, 3072
v_mfma_f32_16x16x32_bf16 a[116:119], v[52:55], v[12:15], a[116:119]
buffer_load_dwordx4 v227, s[40:43], s79 offen lds
v_mfma_f32_16x16x32_bf16 a[176:179], v[56:59], v[12:15], a[176:179]
ds_read_b64_tr_b16 v[220:221], v243 offset:33920
v_mfma_f32_16x16x32_bf16 a[180:183], v[60:63], v[12:15], a[180:183]
v_mfma_f32_16x16x32_bf16 a[120:123], v[32:35], v[16:19], a[120:123]
ds_read_b64_tr_b16 v[222:223], v243 offset:34944
s_xor_b32 m0, s82, 4096
v_mfma_f32_16x16x32_bf16 a[124:127], v[36:39], v[16:19], a[124:127]
buffer_load_dwordx4 v228, s[40:43], s79 offen lds
v_mfma_f32_16x16x32_bf16 a[184:187], v[40:43], v[16:19], a[184:187]
ds_read_b128 v[132:135], v236 offset:59392
v_mfma_f32_16x16x32_bf16 a[188:191], v[44:47], v[16:19], a[188:191]
v_mfma_f32_16x16x32_bf16 a[64:67], v[48:51], v[16:19], a[64:67]
ds_read_b128 v[136:139], v236 offset:49152
s_xor_b32 m0, s82, 5120
v_mfma_f32_16x16x32_bf16 a[68:71], v[52:55], v[16:19], a[68:71]
buffer_load_dwordx4 v229, s[40:43], s79 offen lds
v_mfma_f32_16x16x32_bf16 a[0:3], v[56:59], v[16:19], a[0:3]
ds_read_b128 v[140:143], v236 offset:51200
v_mfma_f32_16x16x32_bf16 a[4:7], v[60:63], v[16:19], a[4:7]
v_mfma_f32_16x16x32_bf16 a[72:75], v[32:35], v[20:23], a[72:75]
ds_read_b128 v[144:147], v236 offset:40960
s_xor_b32 m0, s82, 6144
v_mfma_f32_16x16x32_bf16 a[76:79], v[36:39], v[20:23], a[76:79]
buffer_load_dwordx4 v230, s[40:43], s79 offen lds
v_mfma_f32_16x16x32_bf16 a[8:11], v[40:43], v[20:23], a[8:11]
ds_read_b128 v[148:151], v236 offset:43008
v_mfma_f32_16x16x32_bf16 a[12:15], v[44:47], v[20:23], a[12:15]
v_mfma_f32_16x16x32_bf16 a[80:83], v[48:51], v[20:23], a[80:83]
ds_read_b128 v[152:155], v236 offset:32768
s_xor_b32 m0, s82, 7168
v_mfma_f32_16x16x32_bf16 a[84:87], v[52:55], v[20:23], a[84:87]
buffer_load_dwordx4 v231, s[40:43], s79 offen lds
v_mfma_f32_16x16x32_bf16 a[16:19], v[56:59], v[20:23], a[16:19]
ds_read_b128 v[156:159], v236 offset:34816
s_waitcnt vmcnt(12)
v_mfma_f32_16x16x32_bf16 a[20:23], v[60:63], v[20:23], a[20:23]
s_add_u32 s79, s79, 128
s_and_b32 s79, s79, 1023
v_mfma_f32_16x16x32_bf16 a[88:91], v[32:35], v[24:27], a[88:91]
v_lshlrev_b32 v70, 16, v70
v_lshlrev_b32 v88, 16, v72
s_add_u32 m0, s83, 0
v_mfma_f32_16x16x32_bf16 a[92:95], v[36:39], v[24:27], a[92:95]
buffer_load_dwordx4 v232, s[44:47], s80 offen lds
v_mfma_f32_16x16x32_bf16 a[24:27], v[40:43], v[24:27], a[24:27]
v_and_b32 v89, 0xffff0000, v72
v_sub_f32 v88, v88, v70
v_mfma_f32_16x16x32_bf16 a[28:31], v[44:47], v[24:27], a[28:31]
v_sub_f32 v89, v89, v70
v_add_f32 v120, v88, v89
v_mfma_f32_16x16x32_bf16 a[240:243], v[48:51], v[24:27], a[240:243]
v_mul_f32 v122, v88, v88
v_mul_f32 v123, v89, v89
s_add_u32 m0, s83, 1024
v_mfma_f32_16x16x32_bf16 a[244:247], v[52:55], v[24:27], a[244:247]
buffer_load_dwordx4 v233, s[44:47], s80 offen lds
v_mfma_f32_16x16x32_bf16 a[32:35], v[56:59], v[24:27], a[32:35]
v_lshlrev_b32 v90, 16, v73
v_and_b32 v91, 0xffff0000, v73
v_mfma_f32_16x16x32_bf16 a[36:39], v[60:63], v[24:27], a[36:39]
v_sub_f32 v90, v90, v70
v_sub_f32 v91, v91, v70
v_mfma_f32_16x16x32_bf16 a[248:251], v[32:35], v[28:31], a[248:251]
v_add_f32 v124, v90, v91
v_fmac_f32 v122, v90, v90
s_add_u32 m0, s83, 2176
v_mfma_f32_16x16x32_bf16 a[252:255], v[36:39], v[28:31], a[252:255]
buffer_load_dwordx4 v234, s[44:47], s80 offen lds
v_mfma_f32_16x16x32_bf16 a[40:43], v[40:43], v[28:31], a[40:43]
v_add_f32 v120, v120, v124
v_fmac_f32 v123, v91, v91
v_mfma_f32_16x16x32_bf16 a[44:47], v[44:47], v[28:31], a[44:47]
v_lshlrev_b32 v92, 16, v74
v_and_b32 v93, 0xffff0000, v74
v_mfma_f32_16x16x32_bf16 a[48:51], v[48:51], v[28:31], a[48:51]
v_sub_f32 v92, v92, v70
v_sub_f32 v93, v93, v70
s_add_u32 m0, s83, 3200
v_mfma_f32_16x16x32_bf16 a[52:55], v[52:55], v[28:31], a[52:55]
buffer_load_dwordx4 v235, s[44:47], s80 offen lds
v_mfma_f32_16x16x32_bf16 a[56:59], v[56:59], v[28:31], a[56:59]
v_add_f32 v124, v92, v93
v_fmac_f32 v122, v92, v92
v_mfma_f32_16x16x32_bf16 a[60:63], v[60:63], v[28:31], a[60:63]
s_add_u32 s80, s80, s81
s_and_b32 s80, s80, 4194303
s_waitcnt lgkmcnt(0)
s_waitcnt vmcnt(12)
s_barrier
v_mfma_f32_16x16x32_bf16 a[224:227], v[192:195], v[128:131], a[224:227]
ds_read_b128 v[0:3], v237 offset:57344
s_add_u32 m0, s83, 16896
v_mfma_f32_16x16x32_bf16 a[228:231], v[196:199], v[128:131], a[228:231]
buffer_load_dwordx4 v232, s[44:47], s80 offen lds
v_mfma_f32_16x16x32_bf16 a[232:235], v[200:203], v[128:131], a[232:235]
ds_read_b64_tr_b16 v[32:33], v240 offset:50688
v_add_f32 v120, v120, v124
.La5_LW1_0:
s_cbranch_execz .La5_LW1_4
v_mfma_f32_16x16x32_bf16 a[236:239], v[204:207], v[128:131], a[236:239]
v_fmac_f32 v123, v93, v93
v_lshlrev_b32 v94, 16, v75
v_mfma_f32_16x16x32_bf16 a[208:211], v[208:211], v[128:131], a[208:211]
ds_read_b64_tr_b16 v[34:35], v240 offset:51712
v_and_b32 v95, 0xffff0000, v75
s_add_u32 m0, s83, 17920
v_mfma_f32_16x16x32_bf16 a[212:215], v[212:215], v[128:131], a[212:215]
buffer_load_dwordx4 v233, s[44:47], s80 offen lds
v_mfma_f32_16x16x32_bf16 a[128:131], v[216:219], v[128:131], a[128:131]
ds_read_b64_tr_b16 v[36:37], v241 offset:50688
v_sub_f32 v94, v94, v70
v_mfma_f32_16x16x32_bf16 a[132:135], v[220:223], v[128:131], a[132:135]
v_sub_f32 v95, v95, v70
v_add_f32 v124, v94, v95
v_mfma_f32_16x16x32_bf16 a[216:219], v[192:195], v[132:135], a[216:219]
ds_read_b64_tr_b16 v[38:39], v241 offset:51712
v_fmac_f32 v122, v94, v94
s_add_u32 m0, s83, 19072
v_mfma_f32_16x16x32_bf16 a[220:223], v[196:199], v[132:135], a[220:223]
buffer_load_dwordx4 v234, s[44:47], s80 offen lds
v_mfma_f32_16x16x32_bf16 a[136:139], v[200:203], v[132:135], a[136:139]
ds_read_b64_tr_b16 v[40:41], v242 offset:50688
v_add_f32 v120, v120, v124
v_mfma_f32_16x16x32_bf16 a[140:143], v[204:207], v[132:135], a[140:143]
v_fmac_f32 v123, v95, v95
v_lshlrev_b32 v96, 16, v76
v_mfma_f32_16x16x32_bf16 a[192:195], v[208:211], v[132:135], a[192:195]
ds_read_b64_tr_b16 v[42:43], v242 offset:51712
v_and_b32 v97, 0xffff0000, v76
s_add_u32 m0, s83, 20096
v_mfma_f32_16x16x32_bf16 a[196:199], v[212:215], v[132:135], a[196:199]
buffer_load_dwordx4 v235, s[44:47], s80 offen lds
v_mfma_f32_16x16x32_bf16 a[144:147], v[216:219], v[132:135], a[144:147]
ds_read_b64_tr_b16 v[44:45], v243 offset:50688
v_sub_f32 v96, v96, v70
v_mfma_f32_16x16x32_bf16 a[148:151], v[220:223], v[132:135], a[148:151]
s_add_u32 s80, s80, s81
s_and_b32 s80, s80, 4194303
v_mfma_f32_16x16x32_bf16 a[200:203], v[192:195], v[136:139], a[200:203]
ds_read_b64_tr_b16 v[46:47], v243 offset:51712
v_sub_f32 v97, v97, v70
s_add_u32 m0, s83, 33792
v_mfma_f32_16x16x32_bf16 a[204:207], v[196:199], v[136:139], a[204:207]
buffer_load_dwordx4 v232, s[44:47], s80 offen lds
v_mfma_f32_16x16x32_bf16 a[152:155], v[200:203], v[136:139], a[152:155]
ds_read_b64_tr_b16 v[48:49], v240 offset:50816
v_add_f32 v124, v96, v97
v_mfma_f32_16x16x32_bf16 a[156:159], v[204:207], v[136:139], a[156:159]
v_fmac_f32 v122, v96, v96
v_add_f32 v120, v120, v124
v_mfma_f32_16x16x32_bf16 a[96:99], v[208:211], v[136:139], a[96:99]
ds_read_b64_tr_b16 v[50:51], v240 offset:51840
v_fmac_f32 v123, v97, v97
s_add_u32 m0, s83, 34816
v_mfma_f32_16x16x32_bf16 a[100:103], v[212:215], v[136:139], a[100:103]
buffer_load_dwordx4 v233, s[44:47], s80 offen lds
v_mfma_f32_16x16x32_bf16 a[160:163], v[216:219], v[136:139], a[160:163]
ds_read_b64_tr_b16 v[52:53], v241 offset:50816
v_lshlrev_b32 v98, 16, v77
v_mfma_f32_16x16x32_bf16 a[164:167], v[220:223], v[136:139], a[164:167]
v_and_b32 v99, 0xffff0000, v77
v_sub_f32 v98, v98, v70
v_mfma_f32_16x16x32_bf16 a[104:107], v[192:195], v[140:143], a[104:107]
ds_read_b64_tr_b16 v[54:55], v241 offset:51840
v_sub_f32 v99, v99, v70
s_add_u32 m0, s83, 35968
v_mfma_f32_16x16x32_bf16 a[108:111], v[196:199], v[140:143], a[108:111]
buffer_load_dwordx4 v234, s[44:47], s80 offen lds
v_mfma_f32_16x16x32_bf16 a[168:171], v[200:203], v[140:143], a[168:171]
ds_read_b64_tr_b16 v[56:57], v242 offset:50816
v_add_f32 v124, v98, v99
v_mfma_f32_16x16x32_bf16 a[172:175], v[204:207], v[140:143], a[172:175]
v_fmac_f32 v122, v98, v98
v_add_f32 v120, v120, v124
v_mfma_f32_16x16x32_bf16 a[112:115], v[208:211], v[140:143], a[112:115]
ds_read_b64_tr_b16 v[58:59], v242 offset:51840
v_fmac_f32 v123, v99, v99
s_add_u32 m0, s83, 36992
v_mfma_f32_16x16x32_bf16 a[116:119], v[212:215], v[140:143], a[116:119]
buffer_load_dwordx4 v235, s[44:47], s80 offen lds
v_mfma_f32_16x16x32_bf16 a[176:179], v[216:219], v[140:143], a[176:179]
ds_read_b64_tr_b16 v[60:61], v243 offset:50816
v_lshlrev_b32 v100, 16, v78
v_mfma_f32_16x16x32_bf16 a[180:183], v[220:223], v[140:143], a[180:183]
s_add_u32 s80, s80, s81
s_and_b32 s80, s80, 4194303
v_mfma_f32_16x16x32_bf16 a[120:123], v[192:195], v[144:147], a[120:123]
ds_read_b64_tr_b16 v[62:63], v243 offset:51840
v_and_b32 v101, 0xffff0000, v78
v_mfma_f32_16x16x32_bf16 a[124:127], v[196:199], v[144:147], a[124:127]
buffer_load_dword v254, v164, s[44:47], s86 offen
v_sub_f32 v100, v100, v70
v_mfma_f32_16x16x32_bf16 a[184:187], v[200:203], v[144:147], a[184:187]
ds_read_b128 v[4:7], v237 offset:59392
v_sub_f32 v101, v101, v70
v_mfma_f32_16x16x32_bf16 a[188:191], v[204:207], v[144:147], a[188:191]
s_xor_b32 s82, s82, 32768
s_add_u32 s83, s83, 50688
v_mfma_f32_16x16x32_bf16 a[64:67], v[208:211], v[144:147], a[64:67]
ds_read_b128 v[8:11], v237 offset:49152
s_sub_u32 s86, s86, 2097152
v_mfma_f32_16x16x32_bf16 a[68:71], v[212:215], v[144:147], a[68:71]
v_add_f32 v124, v100, v101
v_fmac_f32 v122, v100, v100
v_mfma_f32_16x16x32_bf16 a[0:3], v[216:219], v[144:147], a[0:3]
ds_read_b128 v[12:15], v237 offset:51200
v_add_f32 v120, v120, v124
v_mfma_f32_16x16x32_bf16 a[4:7], v[220:223], v[144:147], a[4:7]
v_fmac_f32 v123, v101, v101
v_lshlrev_b32 v102, 16, v79
v_mfma_f32_16x16x32_bf16 a[72:75], v[192:195], v[148:151], a[72:75]
ds_read_b128 v[16:19], v237 offset:40960
v_and_b32 v103, 0xffff0000, v79
v_mfma_f32_16x16x32_bf16 a[76:79], v[196:199], v[148:151], a[76:79]
v_sub_f32 v102, v102, v70
v_sub_f32 v103, v103, v70
v_mfma_f32_16x16x32_bf16 a[8:11], v[200:203], v[148:151], a[8:11]
ds_read_b128 v[20:23], v237 offset:43008
v_add_f32 v124, v102, v103
v_mfma_f32_16x16x32_bf16 a[12:15], v[204:207], v[148:151], a[12:15]
v_fmac_f32 v122, v102, v102
v_add_f32 v120, v120, v124
v_mfma_f32_16x16x32_bf16 a[80:83], v[208:211], v[148:151], a[80:83]
ds_read_b128 v[24:27], v237 offset:32768
v_fmac_f32 v123, v103, v103
v_mfma_f32_16x16x32_bf16 a[84:87], v[212:215], v[148:151], a[84:87]
v_lshlrev_b32 v104, 16, v80
v_and_b32 v105, 0xffff0000, v80
v_mfma_f32_16x16x32_bf16 a[16:19], v[216:219], v[148:151], a[16:19]
ds_read_b128 v[28:31], v237 offset:34816
v_sub_f32 v104, v104, v70
v_mfma_f32_16x16x32_bf16 a[20:23], v[220:223], v[148:151], a[20:23]
v_sub_f32 v105, v105, v70
v_add_f32 v124, v104, v105
v_mfma_f32_16x16x32_bf16 a[88:91], v[192:195], v[152:155], a[88:91]
v_fmac_f32 v122, v104, v104
v_add_f32 v120, v120, v124
v_mfma_f32_16x16x32_bf16 a[92:95], v[196:199], v[152:155], a[92:95]
v_fmac_f32 v123, v105, v105
v_lshlrev_b32 v106, 16, v81
v_mfma_f32_16x16x32_bf16 a[24:27], v[200:203], v[152:155], a[24:27]
v_and_b32 v107, 0xffff0000, v81
v_sub_f32 v106, v106, v70
v_mfma_f32_16x16x32_bf16 a[28:31], v[204:207], v[152:155], a[28:31]
v_sub_f32 v107, v107, v70
v_add_f32 v124, v106, v107
v_mfma_f32_16x16x32_bf16 a[240:243], v[208:211], v[152:155], a[240:243]
v_fmac_f32 v122, v106, v106
v_add_f32 v120, v120, v124
v_mfma_f32_16x16x32_bf16 a[244:247], v[212:215], v[152:155], a[244:247]
v_fmac_f32 v123, v107, v107
v_lshlrev_b32 v108, 16, v82
v_mfma_f32_16x16x32_bf16 a[32:35], v[216:219], v[152:155], a[32:35]
v_and_b32 v109, 0xffff0000, v82
v_sub_f32 v108, v108, v70
v_mfma_f32_16x16x32_bf16 a[36:39], v[220:223], v[152:155], a[36:39]
v_sub_f32 v109, v109, v70
v_add_f32 v124, v108, v109
v_mfma_f32_16x16x32_bf16 a[248:251], v[192:195], v[156:159], a[248:251]
v_fmac_f32 v122, v108, v108
v_add_f32 v120, v120, v124
v_mfma_f32_16x16x32_bf16 a[252:255], v[196:199], v[156:159], a[252:255]
v_fmac_f32 v123, v109, v109
v_lshlrev_b32 v110, 16, v83
v_mfma_f32_16x16x32_bf16 a[40:43], v[200:203], v[156:159], a[40:43]
v_and_b32 v111, 0xffff0000, v83
v_sub_f32 v110, v110, v70
v_mfma_f32_16x16x32_bf16 a[44:47], v[204:207], v[156:159], a[44:47]
v_sub_f32 v111, v111, v70
v_add_f32 v124, v110, v111
v_mfma_f32_16x16x32_bf16 a[48:51], v[208:211], v[156:159], a[48:51]
v_fmac_f32 v122, v110, v110
v_add_f32 v120, v120, v124
v_mfma_f32_16x16x32_bf16 a[52:55], v[212:215], v[156:159], a[52:55]
v_fmac_f32 v123, v111, v111
v_lshlrev_b32 v112, 16, v84
v_mfma_f32_16x16x32_bf16 a[56:59], v[216:219], v[156:159], a[56:59]
v_and_b32 v113, 0xffff0000, v84
v_sub_f32 v112, v112, v70
v_mfma_f32_16x16x32_bf16 a[60:63], v[220:223], v[156:159], a[60:63]
v_sub_f32 v113, v113, v70
v_add_f32 v124, v112, v113
s_waitcnt lgkmcnt(0)
s_waitcnt vmcnt(9)
s_barrier
v_mfma_f32_16x16x32_bf16 a[224:227], v[32:35], v[0:3], a[224:227]
ds_read_b128 v[128:131], v236 offset:24576
s_xor_b32 m0, s82, 0
v_mfma_f32_16x16x32_bf16 a[228:231], v[36:39], v[0:3], a[228:231]
buffer_load_dwordx4 v224, s[40:43], s79 offen lds
v_mfma_f32_16x16x32_bf16 a[232:235], v[40:43], v[0:3], a[232:235]
ds_read_b64_tr_b16 v[192:193], v240 offset:0
v_fmac_f32 v122, v112, v112
v_mfma_f32_16x16x32_bf16 a[236:239], v[44:47], v[0:3], a[236:239]
v_add_f32 v120, v120, v124
v_fmac_f32 v123, v113, v113
v_mfma_f32_16x16x32_bf16 a[208:211], v[48:51], v[0:3], a[208:211]
ds_read_b64_tr_b16 v[194:195], v240 offset:1024
v_lshlrev_b32 v114, 16, v85
s_xor_b32 m0, s82, 1024
v_mfma_f32_16x16x32_bf16 a[212:215], v[52:55], v[0:3], a[212:215]
buffer_load_dwordx4 v225, s[40:43], s79 offen lds
v_mfma_f32_16x16x32_bf16 a[128:131], v[56:59], v[0:3], a[128:131]
ds_read_b64_tr_b16 v[196:197], v241 offset:0
v_and_b32 v115, 0xffff0000, v85
v_mfma_f32_16x16x32_bf16 a[132:135], v[60:63], v[0:3], a[132:135]
v_sub_f32 v114, v114, v70
v_sub_f32 v115, v115, v70
v_mfma_f32_16x16x32_bf16 a[216:219], v[32:35], v[4:7], a[216:219]
ds_read_b64_tr_b16 v[198:199], v241 offset:1024
v_add_f32 v124, v114, v115
s_xor_b32 m0, s82, 2048
v_mfma_f32_16x16x32_bf16 a[220:223], v[36:39], v[4:7], a[220:223]
buffer_load_dwordx4 v226, s[40:43], s79 offen lds
v_mfma_f32_16x16x32_bf16 a[136:139], v[40:43], v[4:7], a[136:139]
ds_read_b64_tr_b16 v[200:201], v242 offset:0
v_fmac_f32 v122, v114, v114
v_mfma_f32_16x16x32_bf16 a[140:143], v[44:47], v[4:7], a[140:143]
v_add_f32 v120, v120, v124
v_fmac_f32 v123, v115, v115
v_mfma_f32_16x16x32_bf16 a[192:195], v[48:51], v[4:7], a[192:195]
ds_read_b64_tr_b16 v[202:203], v242 offset:1024
v_lshlrev_b32 v116, 16, v86
s_xor_b32 m0, s82, 3072
v_mfma_f32_16x16x32_bf16 a[196:199], v[52:55], v[4:7], a[196:199]
buffer_load_dwordx4 v227, s[40:43], s79 offen lds
v_mfma_f32_16x16x32_bf16 a[144:147], v[56:59], v[4:7], a[144:147]
ds_read_b64_tr_b16 v[204:205], v243 offset:0
v_and_b32 v117, 0xffff0000, v86
v_mfma_f32_16x16x32_bf16 a[148:151], v[60:63], v[4:7], a[148:151]
v_sub_f32 v116, v116, v70
v_sub_f32 v117, v117, v70
v_mfma_f32_16x16x32_bf16 a[200:203], v[32:35], v[8:11], a[200:203]
ds_read_b64_tr_b16 v[206:207], v243 offset:1024
v_add_f32 v124, v116, v117
s_xor_b32 m0, s82, 4096
v_mfma_f32_16x16x32_bf16 a[204:207], v[36:39], v[8:11], a[204:207]
buffer_load_dwordx4 v228, s[40:43], s79 offen lds
v_mfma_f32_16x16x32_bf16 a[152:155], v[40:43], v[8:11], a[152:155]
ds_read_b64_tr_b16 v[208:209], v240 offset:128
v_fmac_f32 v122, v116, v116
v_mfma_f32_16x16x32_bf16 a[156:159], v[44:47], v[8:11], a[156:159]
v_add_f32 v120, v120, v124
v_fmac_f32 v123, v117, v117
v_mfma_f32_16x16x32_bf16 a[96:99], v[48:51], v[8:11], a[96:99]
ds_read_b64_tr_b16 v[210:211], v240 offset:1152
v_lshlrev_b32 v118, 16, v87
s_xor_b32 m0, s82, 5120
v_mfma_f32_16x16x32_bf16 a[100:103], v[52:55], v[8:11], a[100:103]
buffer_load_dwordx4 v229, s[40:43], s79 offen lds
v_mfma_f32_16x16x32_bf16 a[160:163], v[56:59], v[8:11], a[160:163]
ds_read_b64_tr_b16 v[212:213], v241 offset:128
v_and_b32 v119, 0xffff0000, v87
v_mfma_f32_16x16x32_bf16 a[164:167], v[60:63], v[8:11], a[164:167]
v_sub_f32 v118, v118, v70
v_sub_f32 v119, v119, v70
v_mfma_f32_16x16x32_bf16 a[104:107], v[32:35], v[12:15], a[104:107]
ds_read_b64_tr_b16 v[214:215], v241 offset:1152
v_add_f32 v124, v118, v119
s_xor_b32 m0, s82, 6144
v_mfma_f32_16x16x32_bf16 a[108:111], v[36:39], v[12:15], a[108:111]
buffer_load_dwordx4 v230, s[40:43], s79 offen lds
v_mfma_f32_16x16x32_bf16 a[168:171], v[40:43], v[12:15], a[168:171]
ds_read_b64_tr_b16 v[216:217], v242 offset:128
v_fmac_f32 v122, v118, v118
v_mfma_f32_16x16x32_bf16 a[172:175], v[44:47], v[12:15], a[172:175]
v_add_f32 v120, v120, v124
v_fmac_f32 v123, v119, v119
v_mfma_f32_16x16x32_bf16 a[112:115], v[48:51], v[12:15], a[112:115]
ds_read_b64_tr_b16 v[218:219], v242 offset:1152
v_add_f32 v121, v122, v123
s_xor_b32 m0, s82, 7168
v_mfma_f32_16x16x32_bf16 a[116:119], v[52:55], v[12:15], a[116:119]
buffer_load_dwordx4 v231, s[40:43], s79 offen lds
v_mfma_f32_16x16x32_bf16 a[176:179], v[56:59], v[12:15], a[176:179]
ds_read_b64_tr_b16 v[220:221], v243 offset:128
v_add_f32_dpp v120, v120, v120 row_ror:8 row_mask:0xf bank_mask:0xf
v_mfma_f32_16x16x32_bf16 a[180:183], v[60:63], v[12:15], a[180:183]
s_add_u32 s79, s79, 128
s_and_b32 s79, s79, 1023
s_xor_b32 s82, s82, 32768
v_mfma_f32_16x16x32_bf16 a[120:123], v[32:35], v[16:19], a[120:123]
ds_read_b64_tr_b16 v[222:223], v243 offset:1152
v_add_f32_dpp v121, v121, v121 row_ror:8 row_mask:0xf bank_mask:0xf
s_add_u32 m0, s83, 0
v_mfma_f32_16x16x32_bf16 a[124:127], v[36:39], v[16:19], a[124:127]
buffer_load_dwordx4 v232, s[44:47], s80 offen lds
v_mfma_f32_16x16x32_bf16 a[184:187], v[40:43], v[16:19], a[184:187]
ds_read_b128 v[132:135], v236 offset:26624
v_mov_b32 v124, v120
v_mfma_f32_16x16x32_bf16 a[188:191], v[44:47], v[16:19], a[188:191]
v_mov_b32 v125, v121
v_mfma_f32_16x16x32_bf16 a[64:67], v[48:51], v[16:19], a[64:67]
ds_read_b128 v[136:139], v236 offset:16384
s_add_u32 m0, s83, 1024
v_mfma_f32_16x16x32_bf16 a[68:71], v[52:55], v[16:19], a[68:71]
buffer_load_dwordx4 v233, s[44:47], s80 offen lds
v_mfma_f32_16x16x32_bf16 a[0:3], v[56:59], v[16:19], a[0:3]
ds_read_b128 v[140:143], v236 offset:18432
v_permlane16_swap_b32 v124, v120
v_mfma_f32_16x16x32_bf16 a[4:7], v[60:63], v[16:19], a[4:7]
v_permlane16_swap_b32 v125, v121
v_mfma_f32_16x16x32_bf16 a[72:75], v[32:35], v[20:23], a[72:75]
ds_read_b128 v[144:147], v236 offset:8192
v_add_f32 v120, v120, v124
s_add_u32 m0, s83, 2176
v_mfma_f32_16x16x32_bf16 a[76:79], v[36:39], v[20:23], a[76:79]
buffer_load_dwordx4 v234, s[44:47], s80 offen lds
v_mfma_f32_16x16x32_bf16 a[8:11], v[40:43], v[20:23], a[8:11]
ds_read_b128 v[148:151], v236 offset:10240
v_add_f32 v121, v121, v125
v_mfma_f32_16x16x32_bf16 a[12:15], v[44:47], v[20:23], a[12:15]
v_mov_b32 v124, v120
v_mov_b32 v125, v121
v_mfma_f32_16x16x32_bf16 a[80:83], v[48:51], v[20:23], a[80:83]
ds_read_b128 v[152:155], v236 offset:0
s_add_u32 m0, s83, 3200
v_mfma_f32_16x16x32_bf16 a[84:87], v[52:55], v[20:23], a[84:87]
buffer_load_dwordx4 v235, s[44:47], s80 offen lds
v_mfma_f32_16x16x32_bf16 a[16:19], v[56:59], v[20:23], a[16:19]
ds_read_b128 v[156:159], v236 offset:2048
v_permlane32_swap_b32 v124, v120
v_mfma_f32_16x16x32_bf16 a[20:23], v[60:63], v[20:23], a[20:23]
s_add_u32 s80, s80, s81
s_and_b32 s80, s80, 4194303
s_add_u32 s83, s83, 16896
v_mfma_f32_16x16x32_bf16 a[88:91], v[32:35], v[24:27], a[88:91]
v_permlane32_swap_b32 v125, v121
v_mfma_f32_16x16x32_bf16 a[92:95], v[36:39], v[24:27], a[92:95]
s_cmp_eq_u32 s83, s97
s_cselect_b32 s83, s90, s83
v_mfma_f32_16x16x32_bf16 a[24:27], v[40:43], v[24:27], a[24:27]
v_add_f32 v120, v120, v124
v_add_f32 v121, v121, v125
v_mfma_f32_16x16x32_bf16 a[28:31], v[44:47], v[24:27], a[28:31]
ds_write_b64 v127, v[120:121]
s_nop 0
v_mfma_f32_16x16x32_bf16 a[240:243], v[48:51], v[24:27], a[240:243]
v_readfirstlane_b32 s92, v252
v_readfirstlane_b32 s93, v253
v_mfma_f32_16x16x32_bf16 a[244:247], v[52:55], v[24:27], a[244:247]
s_add_u32 s92, s92, 1
s_addc_u32 s93, s93, 0
v_mfma_f32_16x16x32_bf16 a[32:35], v[56:59], v[24:27], a[32:35]
v_mov_b32 v252, s92
v_mov_b32 v253, s93
v_mfma_f32_16x16x32_bf16 a[36:39], v[60:63], v[24:27], a[36:39]
v_mov_b32 v160, 0
v_mov_b32 v161, 0
v_mfma_f32_16x16x32_bf16 a[248:251], v[32:35], v[28:31], a[248:251]
v_mov_b32 v162, 0
v_mov_b32 v163, 0
v_mfma_f32_16x16x32_bf16 a[252:255], v[36:39], v[28:31], a[252:255]
buffer_store_dwordx4 v[160:163], v255, s[52:55], s95 offen sc1
v_mfma_f32_16x16x32_bf16 a[40:43], v[40:43], v[28:31], a[40:43]
v_mfma_f32_16x16x32_bf16 a[44:47], v[44:47], v[28:31], a[44:47]
v_mfma_f32_16x16x32_bf16 a[48:51], v[48:51], v[28:31], a[48:51]
v_mfma_f32_16x16x32_bf16 a[52:55], v[52:55], v[28:31], a[52:55]
v_mfma_f32_16x16x32_bf16 a[56:59], v[56:59], v[28:31], a[56:59]
v_mfma_f32_16x16x32_bf16 a[60:63], v[60:63], v[28:31], a[60:63]
s_waitcnt lgkmcnt(0)
s_waitcnt vmcnt(18)
s_barrier
v_mfma_f32_16x16x32_bf16 a[224:227], v[192:195], v[128:131], a[224:227]
ds_read_b128 v[0:3], v237 offset:24576
ds_read_b128 v[72:75], v126
v_mfma_f32_16x16x32_bf16 a[228:231], v[196:199], v[128:131], a[228:231]
ds_read_b128 v[76:79], v126 offset:16
v_mfma_f32_16x16x32_bf16 a[232:235], v[200:203], v[128:131], a[232:235]
ds_read_b64_tr_b16 v[32:33], v240 offset:16896
v_mfma_f32_16x16x32_bf16 a[236:239], v[204:207], v[128:131], a[236:239]
v_mfma_f32_16x16x32_bf16 a[208:211], v[208:211], v[128:131], a[208:211]
ds_read_b64_tr_b16 v[34:35], v240 offset:17920
s_waitcnt lgkmcnt(2)
v_mfma_f32_16x16x32_bf16 a[212:215], v[212:215], v[128:131], a[212:215]
v_add_f32 v72, v72, v74
v_add_f32 v73, v73, v75
v_mfma_f32_16x16x32_bf16 a[128:131], v[216:219], v[128:131], a[128:131]
ds_read_b64_tr_b16 v[36:37], v241 offset:16896
v_add_f32 v76, v76, v78
v_mfma_f32_16x16x32_bf16 a[132:135], v[220:223], v[128:131], a[132:135]
v_add_f32 v77, v77, v79
v_add_f32 v72, v72, v76
v_mfma_f32_16x16x32_bf16 a[216:219], v[192:195], v[132:135], a[216:219]
ds_read_b64_tr_b16 v[38:39], v241 offset:17920
v_add_f32 v73, v73, v77
v_mfma_f32_16x16x32_bf16 a[220:223], v[196:199], v[132:135], a[220:223]
v_mul_f32 v72, 0x3a800000, v72
v_mul_f32 v73, 0x3a800000, v73
v_mfma_f32_16x16x32_bf16 a[136:139], v[200:203], v[132:135], a[136:139]
ds_read_b64_tr_b16 v[40:41], v242 offset:16896
v_add_f32 v124, v70, v72
v_mfma_f32_16x16x32_bf16 a[140:143], v[204:207], v[132:135], a[140:143]
v_fma_f32 v125, -v72, v72, v73
v_cvt_pk_bf16_f32 v124, v124, v124
v_mfma_f32_16x16x32_bf16 a[192:195], v[208:211], v[132:135], a[192:195]
ds_read_b64_tr_b16 v[42:43], v242 offset:17920
v_lshlrev_b32 v124, 16, v124
v_mfma_f32_16x16x32_bf16 a[196:199], v[212:215], v[132:135], a[196:199]
v_max_f32 v125, 0, v125
v_cvt_pk_bf16_f32 v125, v125, v125
v_mfma_f32_16x16x32_bf16 a[144:147], v[216:219], v[132:135], a[144:147]
ds_read_b64_tr_b16 v[44:45], v243 offset:16896
v_lshlrev_b32 v125, 16, v125
v_mfma_f32_16x16x32_bf16 a[148:151], v[220:223], v[132:135], a[148:151]
v_add_f32 v125, 0x3727c5ac, v125
v_cvt_pk_bf16_f32 v125, v125, v125
v_mfma_f32_16x16x32_bf16 a[200:203], v[192:195], v[136:139], a[200:203]
ds_read_b64_tr_b16 v[46:47], v243 offset:17920
v_lshlrev_b32 v125, 16, v125
v_mfma_f32_16x16x32_bf16 a[204:207], v[196:199], v[136:139], a[204:207]
v_sqrt_f32 v125, v125
v_mfma_f32_16x16x32_bf16 a[152:155], v[200:203], v[136:139], a[152:155]
ds_read_b64_tr_b16 v[48:49], v240 offset:17024
v_sub_f32 v124, v70, v124
v_mfma_f32_16x16x32_bf16 a[156:159], v[204:207], v[136:139], a[156:159]
v_cvt_pk_bf16_f32 v125, v125, v125
v_lshlrev_b32 v125, 16, v125
v_mfma_f32_16x16x32_bf16 a[96:99], v[208:211], v[136:139], a[96:99]
ds_read_b64_tr_b16 v[50:51], v240 offset:18048
v_mfma_f32_16x16x32_bf16 a[100:103], v[212:215], v[136:139], a[100:103]
v_rcp_f32 v125, v125
v_mfma_f32_16x16x32_bf16 a[160:163], v[216:219], v[136:139], a[160:163]
ds_read_b64_tr_b16 v[52:53], v241 offset:17024
v_mul_f32 v125, 0x3e0293ee, v125
v_mfma_f32_16x16x32_bf16 a[164:167], v[220:223], v[136:139], a[164:167]
v_mul_f32 v124, v124, v125
v_fma_f32 v88, v88, v125, v124
v_mfma_f32_16x16x32_bf16 a[104:107], v[192:195], v[140:143], a[104:107]
ds_read_b64_tr_b16 v[54:55], v241 offset:18048
v_fma_f32 v89, v89, v125, v124
v_mfma_f32_16x16x32_bf16 a[108:111], v[196:199], v[140:143], a[108:111]
v_cvt_pk_bf16_f32 v72, v88, v89
v_fma_f32 v90, v90, v125, v124
v_mfma_f32_16x16x32_bf16 a[168:171], v[200:203], v[140:143], a[168:171]
ds_read_b64_tr_b16 v[56:57], v242 offset:17024
v_fma_f32 v91, v91, v125, v124
v_mfma_f32_16x16x32_bf16 a[172:175], v[204:207], v[140:143], a[172:175]
v_cvt_pk_bf16_f32 v73, v90, v91
v_fma_f32 v92, v92, v125, v124
v_mfma_f32_16x16x32_bf16 a[112:115], v[208:211], v[140:143], a[112:115]
ds_read_b64_tr_b16 v[58:59], v242 offset:18048
v_fma_f32 v93, v93, v125, v124
v_mfma_f32_16x16x32_bf16 a[116:119], v[212:215], v[140:143], a[116:119]
v_cvt_pk_bf16_f32 v74, v92, v93
v_fma_f32 v94, v94, v125, v124
v_mfma_f32_16x16x32_bf16 a[176:179], v[216:219], v[140:143], a[176:179]
ds_read_b64_tr_b16 v[60:61], v243 offset:17024
v_fma_f32 v95, v95, v125, v124
v_mfma_f32_16x16x32_bf16 a[180:183], v[220:223], v[140:143], a[180:183]
v_cvt_pk_bf16_f32 v75, v94, v95
buffer_store_dwordx4 v[72:75], v71, s[56:59], 0 offen offset:0 sc1
v_mfma_f32_16x16x32_bf16 a[120:123], v[192:195], v[144:147], a[120:123]
ds_read_b64_tr_b16 v[62:63], v243 offset:18048
v_fma_f32 v96, v96, v125, v124
s_add_u32 m0, s83, 0
v_mfma_f32_16x16x32_bf16 a[124:127], v[196:199], v[144:147], a[124:127]
buffer_load_dwordx4 v232, s[44:47], s80 offen lds
v_mfma_f32_16x16x32_bf16 a[184:187], v[200:203], v[144:147], a[184:187]
ds_read_b128 v[4:7], v237 offset:26624
v_fma_f32 v97, v97, v125, v124
v_mfma_f32_16x16x32_bf16 a[188:191], v[204:207], v[144:147], a[188:191]
v_cvt_pk_bf16_f32 v76, v96, v97
v_fma_f32 v98, v98, v125, v124
v_mfma_f32_16x16x32_bf16 a[64:67], v[208:211], v[144:147], a[64:67]
ds_read_b128 v[8:11], v237 offset:16384
v_fma_f32 v99, v99, v125, v124
s_add_u32 m0, s83, 1024
v_mfma_f32_16x16x32_bf16 a[68:71], v[212:215], v[144:147], a[68:71]
buffer_load_dwordx4 v233, s[44:47], s80 offen lds
v_mfma_f32_16x16x32_bf16 a[0:3], v[216:219], v[144:147], a[0:3]
ds_read_b128 v[12:15], v237 offset:18432
v_cvt_pk_bf16_f32 v77, v98, v99
v_mfma_f32_16x16x32_bf16 a[4:7], v[220:223], v[144:147], a[4:7]
v_fma_f32 v100, v100, v125, v124
v_fma_f32 v101, v101, v125, v124
v_mfma_f32_16x16x32_bf16 a[72:75], v[192:195], v[148:151], a[72:75]
ds_read_b128 v[16:19], v237 offset:8192
v_cvt_pk_bf16_f32 v78, v100, v101
v_fma_f32 v102, v102, v125, v124
s_add_u32 m0, s83, 2176
v_mfma_f32_16x16x32_bf16 a[76:79], v[196:199], v[148:151], a[76:79]
buffer_load_dwordx4 v234, s[44:47], s80 offen lds
v_fma_f32 v103, v103, v125, v124
v_mfma_f32_16x16x32_bf16 a[8:11], v[200:203], v[148:151], a[8:11]
ds_read_b128 v[20:23], v237 offset:10240
v_cvt_pk_bf16_f32 v79, v102, v103
buffer_store_dwordx4 v[76:79], v71, s[56:59], 0 offen offset:2048 sc1
v_mfma_f32_16x16x32_bf16 a[12:15], v[204:207], v[148:151], a[12:15]
v_fma_f32 v104, v104, v125, v124
v_fma_f32 v105, v105, v125, v124
v_cvt_pk_bf16_f32 v80, v104, v105
v_mfma_f32_16x16x32_bf16 a[80:83], v[208:211], v[148:151], a[80:83]
ds_read_b128 v[24:27], v237 offset:0
v_fma_f32 v106, v106, v125, v124
v_fma_f32 v107, v107, v125, v124
s_add_u32 m0, s83, 3200
v_mfma_f32_16x16x32_bf16 a[84:87], v[212:215], v[148:151], a[84:87]
buffer_load_dwordx4 v235, s[44:47], s80 offen lds
v_cvt_pk_bf16_f32 v81, v106, v107
v_mfma_f32_16x16x32_bf16 a[16:19], v[216:219], v[148:151], a[16:19]
ds_read_b128 v[28:31], v237 offset:2048
v_fma_f32 v108, v108, v125, v124
v_fma_f32 v109, v109, v125, v124
v_mfma_f32_16x16x32_bf16 a[20:23], v[220:223], v[148:151], a[20:23]
s_add_u32 s80, s80, s81
s_and_b32 s80, s80, 4194303
s_add_u32 s83, s83, 16896
v_mfma_f32_16x16x32_bf16 a[88:91], v[192:195], v[152:155], a[88:91]
v_cvt_pk_bf16_f32 v82, v108, v109
v_fma_f32 v110, v110, v125, v124
v_fma_f32 v111, v111, v125, v124
v_mfma_f32_16x16x32_bf16 a[92:95], v[196:199], v[152:155], a[92:95]
s_cmp_eq_u32 s83, s97
s_cselect_b32 s83, s90, s83
v_cvt_pk_bf16_f32 v83, v110, v111
v_mfma_f32_16x16x32_bf16 a[24:27], v[200:203], v[152:155], a[24:27]
buffer_store_dwordx4 v[80:83], v71, s[56:59], s88 offen offset:0 sc1
v_fma_f32 v112, v112, v125, v124
v_fma_f32 v113, v113, v125, v124
v_mfma_f32_16x16x32_bf16 a[28:31], v[204:207], v[152:155], a[28:31]
v_cvt_pk_bf16_f32 v84, v112, v113
v_fma_f32 v114, v114, v125, v124
v_fma_f32 v115, v115, v125, v124
v_mfma_f32_16x16x32_bf16 a[240:243], v[208:211], v[152:155], a[240:243]
v_cvt_pk_bf16_f32 v85, v114, v115
v_fma_f32 v116, v116, v125, v124
v_fma_f32 v117, v117, v125, v124
v_mfma_f32_16x16x32_bf16 a[244:247], v[212:215], v[152:155], a[244:247]
v_cvt_pk_bf16_f32 v86, v116, v117
v_fma_f32 v118, v118, v125, v124
v_fma_f32 v119, v119, v125, v124
v_mfma_f32_16x16x32_bf16 a[32:35], v[216:219], v[152:155], a[32:35]
v_cvt_pk_bf16_f32 v87, v118, v119
buffer_store_dwordx4 v[84:87], v71, s[56:59], s88 offen offset:2048 sc1
v_mfma_f32_16x16x32_bf16 a[36:39], v[220:223], v[152:155], a[36:39]
v_mfma_f32_16x16x32_bf16 a[248:251], v[192:195], v[156:159], a[248:251]
v_mfma_f32_16x16x32_bf16 a[252:255], v[196:199], v[156:159], a[252:255]
v_mfma_f32_16x16x32_bf16 a[40:43], v[200:203], v[156:159], a[40:43]
v_add_u32 v240, s84, v240
v_add_u32 v241, s84, v241
v_mfma_f32_16x16x32_bf16 a[44:47], v[204:207], v[156:159], a[44:47]
v_add_u32 v242, s84, v242
v_add_u32 v243, s84, v243
v_mfma_f32_16x16x32_bf16 a[48:51], v[208:211], v[156:159], a[48:51]
v_xor_b32 v236, 0x8000, v236
v_xor_b32 v237, 0x8000, v237
v_mfma_f32_16x16x32_bf16 a[52:55], v[212:215], v[156:159], a[52:55]
s_sub_u32 s84, 0, s84
v_mfma_f32_16x16x32_bf16 a[56:59], v[216:219], v[156:159], a[56:59]
v_mfma_f32_16x16x32_bf16 a[60:63], v[220:223], v[156:159], a[60:63]
s_waitcnt lgkmcnt(0)
.p2align 6
.La5_LK_LOOP:
s_waitcnt vmcnt(12)
s_barrier
v_mfma_f32_16x16x32_bf16 a[224:227], v[32:35], v[0:3], a[224:227]
ds_read_b128 v[128:131], v236 offset:24576
s_xor_b32 m0, s82, 0
v_mfma_f32_16x16x32_bf16 a[228:231], v[36:39], v[0:3], a[228:231]
buffer_load_dwordx4 v224, s[40:43], s79 offen lds
v_mfma_f32_16x16x32_bf16 a[232:235], v[40:43], v[0:3], a[232:235]
ds_read_b64_tr_b16 v[192:193], v240 offset:0
v_mfma_f32_16x16x32_bf16 a[236:239], v[44:47], v[0:3], a[236:239]
v_mfma_f32_16x16x32_bf16 a[208:211], v[48:51], v[0:3], a[208:211]
ds_read_b64_tr_b16 v[194:195], v240 offset:1024
s_xor_b32 m0, s82, 1024
v_mfma_f32_16x16x32_bf16 a[212:215], v[52:55], v[0:3], a[212:215]
buffer_load_dwordx4 v225, s[40:43], s79 offen lds
v_mfma_f32_16x16x32_bf16 a[128:131], v[56:59], v[0:3], a[128:131]
ds_read_b64_tr_b16 v[196:197], v241 offset:0
v_mfma_f32_16x16x32_bf16 a[132:135], v[60:63], v[0:3], a[132:135]
v_mfma_f32_16x16x32_bf16 a[216:219], v[32:35], v[4:7], a[216:219]
ds_read_b64_tr_b16 v[198:199], v241 offset:1024
s_xor_b32 m0, s82, 2048
v_mfma_f32_16x16x32_bf16 a[220:223], v[36:39], v[4:7], a[220:223]
buffer_load_dwordx4 v226, s[40:43], s79 offen lds
v_mfma_f32_16x16x32_bf16 a[136:139], v[40:43], v[4:7], a[136:139]
ds_read_b64_tr_b16 v[200:201], v242 offset:0
v_mfma_f32_16x16x32_bf16 a[140:143], v[44:47], v[4:7], a[140:143]
v_mfma_f32_16x16x32_bf16 a[192:195], v[48:51], v[4:7], a[192:195]
ds_read_b64_tr_b16 v[202:203], v242 offset:1024
s_xor_b32 m0, s82, 3072
v_mfma_f32_16x16x32_bf16 a[196:199], v[52:55], v[4:7], a[196:199]
buffer_load_dwordx4 v227, s[40:43], s79 offen lds
v_mfma_f32_16x16x32_bf16 a[144:147], v[56:59], v[4:7], a[144:147]
ds_read_b64_tr_b16 v[204:205], v243 offset:0
v_mfma_f32_16x16x32_bf16 a[148:151], v[60:63], v[4:7], a[148:151]
.La5_LW1_1:
s_cbranch_execz .La5_LW1_5
v_mfma_f32_16x16x32_bf16 a[200:203], v[32:35], v[8:11], a[200:203]
ds_read_b64_tr_b16 v[206:207], v243 offset:1024
s_xor_b32 m0, s82, 4096
v_mfma_f32_16x16x32_bf16 a[204:207], v[36:39], v[8:11], a[204:207]
buffer_load_dwordx4 v228, s[40:43], s79 offen lds
v_mfma_f32_16x16x32_bf16 a[152:155], v[40:43], v[8:11], a[152:155]
ds_read_b64_tr_b16 v[208:209], v240 offset:128
v_mfma_f32_16x16x32_bf16 a[156:159], v[44:47], v[8:11], a[156:159]
v_mfma_f32_16x16x32_bf16 a[96:99], v[48:51], v[8:11], a[96:99]
ds_read_b64_tr_b16 v[210:211], v240 offset:1152
s_xor_b32 m0, s82, 5120
v_mfma_f32_16x16x32_bf16 a[100:103], v[52:55], v[8:11], a[100:103]
buffer_load_dwordx4 v229, s[40:43], s79 offen lds
v_mfma_f32_16x16x32_bf16 a[160:163], v[56:59], v[8:11], a[160:163]
ds_read_b64_tr_b16 v[212:213], v241 offset:128
v_mfma_f32_16x16x32_bf16 a[164:167], v[60:63], v[8:11], a[164:167]
v_mfma_f32_16x16x32_bf16 a[104:107], v[32:35], v[12:15], a[104:107]
ds_read_b64_tr_b16 v[214:215], v241 offset:1152
s_xor_b32 m0, s82, 6144
v_mfma_f32_16x16x32_bf16 a[108:111], v[36:39], v[12:15], a[108:111]
buffer_load_dwordx4 v230, s[40:43], s79 offen lds
v_mfma_f32_16x16x32_bf16 a[168:171], v[40:43], v[12:15], a[168:171]
ds_read_b64_tr_b16 v[216:217], v242 offset:128
v_mfma_f32_16x16x32_bf16 a[172:175], v[44:47], v[12:15], a[172:175]
v_mfma_f32_16x16x32_bf16 a[112:115], v[48:51], v[12:15], a[112:115]
ds_read_b64_tr_b16 v[218:219], v242 offset:1152
s_xor_b32 m0, s82, 7168
v_mfma_f32_16x16x32_bf16 a[116:119], v[52:55], v[12:15], a[116:119]
buffer_load_dwordx4 v231, s[40:43], s79 offen lds
v_mfma_f32_16x16x32_bf16 a[176:179], v[56:59], v[12:15], a[176:179]
ds_read_b64_tr_b16 v[220:221], v243 offset:128
v_mfma_f32_16x16x32_bf16 a[180:183], v[60:63], v[12:15], a[180:183]
s_add_u32 s79, s79, 128
s_and_b32 s79, s79, 1023
s_xor_b32 s82, s82, 32768
v_mfma_f32_16x16x32_bf16 a[120:123], v[32:35], v[16:19], a[120:123]
ds_read_b64_tr_b16 v[222:223], v243 offset:1152
s_add_u32 m0, s83, 0
v_mfma_f32_16x16x32_bf16 a[124:127], v[36:39], v[16:19], a[124:127]
buffer_load_dwordx4 v232, s[44:47], s80 offen lds
v_mfma_f32_16x16x32_bf16 a[184:187], v[40:43], v[16:19], a[184:187]
ds_read_b128 v[132:135], v236 offset:26624
v_mfma_f32_16x16x32_bf16 a[188:191], v[44:47], v[16:19], a[188:191]
v_mfma_f32_16x16x32_bf16 a[64:67], v[48:51], v[16:19], a[64:67]
ds_read_b128 v[136:139], v236 offset:16384
s_add_u32 m0, s83, 1024
v_mfma_f32_16x16x32_bf16 a[68:71], v[52:55], v[16:19], a[68:71]
buffer_load_dwordx4 v233, s[44:47], s80 offen lds
v_mfma_f32_16x16x32_bf16 a[0:3], v[56:59], v[16:19], a[0:3]
ds_read_b128 v[140:143], v236 offset:18432
v_mfma_f32_16x16x32_bf16 a[4:7], v[60:63], v[16:19], a[4:7]
v_mfma_f32_16x16x32_bf16 a[72:75], v[32:35], v[20:23], a[72:75]
ds_read_b128 v[144:147], v236 offset:8192
s_add_u32 m0, s83, 2176
v_mfma_f32_16x16x32_bf16 a[76:79], v[36:39], v[20:23], a[76:79]
buffer_load_dwordx4 v234, s[44:47], s80 offen lds
v_mfma_f32_16x16x32_bf16 a[8:11], v[40:43], v[20:23], a[8:11]
ds_read_b128 v[148:151], v236 offset:10240
v_mfma_f32_16x16x32_bf16 a[12:15], v[44:47], v[20:23], a[12:15]
v_mfma_f32_16x16x32_bf16 a[80:83], v[48:51], v[20:23], a[80:83]
ds_read_b128 v[152:155], v236 offset:0
s_add_u32 m0, s83, 3200
v_mfma_f32_16x16x32_bf16 a[84:87], v[52:55], v[20:23], a[84:87]
buffer_load_dwordx4 v235, s[44:47], s80 offen lds
v_mfma_f32_16x16x32_bf16 a[16:19], v[56:59], v[20:23], a[16:19]
ds_read_b128 v[156:159], v236 offset:2048
v_mfma_f32_16x16x32_bf16 a[20:23], v[60:63], v[20:23], a[20:23]
s_add_u32 s80, s80, s81
s_and_b32 s80, s80, 4194303
s_add_u32 s83, s83, 16896
v_mfma_f32_16x16x32_bf16 a[88:91], v[32:35], v[24:27], a[88:91]
v_mfma_f32_16x16x32_bf16 a[92:95], v[36:39], v[24:27], a[92:95]
s_cmp_eq_u32 s83, s97
s_cselect_b32 s83, s90, s83
v_mfma_f32_16x16x32_bf16 a[24:27], v[40:43], v[24:27], a[24:27]
v_mfma_f32_16x16x32_bf16 a[28:31], v[44:47], v[24:27], a[28:31]
buffer_load_dword v254, v167, s[68:71], s96 offen
s_add_u32 s96, s96, 1048576
v_mfma_f32_16x16x32_bf16 a[240:243], v[48:51], v[24:27], a[240:243]
v_mfma_f32_16x16x32_bf16 a[244:247], v[52:55], v[24:27], a[244:247]
v_mfma_f32_16x16x32_bf16 a[32:35], v[56:59], v[24:27], a[32:35]
v_mfma_f32_16x16x32_bf16 a[36:39], v[60:63], v[24:27], a[36:39]
buffer_load_dword v254, v167, s[68:71], s96 offen
s_add_u32 s96, s96, 1048576
v_mfma_f32_16x16x32_bf16 a[248:251], v[32:35], v[28:31], a[248:251]
s_lshl_b32 s92, s77, 18
s_cmp_eq_u32 s85, 3
v_mfma_f32_16x16x32_bf16 a[252:255], v[36:39], v[28:31], a[252:255]
s_cselect_b32 s44, s68, s44
s_cselect_b32 s45, s69, s45
v_mfma_f32_16x16x32_bf16 a[40:43], v[40:43], v[28:31], a[40:43]
s_cselect_b32 s46, s70, s46
s_cselect_b32 s81, 524288, s81
v_mfma_f32_16x16x32_bf16 a[44:47], v[44:47], v[28:31], a[44:47]
s_cselect_b32 s42, 0, s42
s_cselect_b32 s80, s92, s80
v_mfma_f32_16x16x32_bf16 a[48:51], v[48:51], v[28:31], a[48:51]
v_mfma_f32_16x16x32_bf16 a[52:55], v[52:55], v[28:31], a[52:55]
v_mfma_f32_16x16x32_bf16 a[56:59], v[56:59], v[28:31], a[56:59]
v_mfma_f32_16x16x32_bf16 a[60:63], v[60:63], v[28:31], a[60:63]
s_waitcnt lgkmcnt(0)
s_waitcnt vmcnt(22)
s_barrier
v_mfma_f32_16x16x32_bf16 a[224:227], v[192:195], v[128:131], a[224:227]
ds_read_b128 v[0:3], v237 offset:24576
v_mfma_f32_16x16x32_bf16 a[228:231], v[196:199], v[128:131], a[228:231]
s_waitcnt vmcnt(14)
buffer_store_dwordx2 v[252:253], off, s[64:67], s91 offset:1024 sc1
v_mfma_f32_16x16x32_bf16 a[232:235], v[200:203], v[128:131], a[232:235]
ds_read_b64_tr_b16 v[32:33], v240 offset:16896
v_mfma_f32_16x16x32_bf16 a[236:239], v[204:207], v[128:131], a[236:239]
s_cmp_eq_u32 s85, 4
s_cselect_b32 s92, s89, 1073741824
v_mfma_f32_16x16x32_bf16 a[208:211], v[208:211], v[128:131], a[208:211]
ds_read_b64_tr_b16 v[34:35], v240 offset:17920
v_mfma_f32_16x16x32_bf16 a[212:215], v[212:215], v[128:131], a[212:215]
buffer_load_dwordx4 v[72:75], v255, s[64:67], s92 offen offset:1024 sc1
v_mfma_f32_16x16x32_bf16 a[128:131], v[216:219], v[128:131], a[128:131]
ds_read_b64_tr_b16 v[36:37], v241 offset:16896
v_mfma_f32_16x16x32_bf16 a[132:135], v[220:223], v[128:131], a[132:135]
s_mov_b32 s91, 1073741824
v_mfma_f32_16x16x32_bf16 a[216:219], v[192:195], v[132:135], a[216:219]
ds_read_b64_tr_b16 v[38:39], v241 offset:17920
v_mfma_f32_16x16x32_bf16 a[220:223], v[196:199], v[132:135], a[220:223]
v_mfma_f32_16x16x32_bf16 a[136:139], v[200:203], v[132:135], a[136:139]
ds_read_b64_tr_b16 v[40:41], v242 offset:16896
s_lshl_b32 s92, s76, 19
v_mfma_f32_16x16x32_bf16 a[140:143], v[204:207], v[132:135], a[140:143]
s_add_u32 s92, s92, 88080384
v_mfma_f32_16x16x32_bf16 a[192:195], v[208:211], v[132:135], a[192:195]
ds_read_b64_tr_b16 v[42:43], v242 offset:17920
v_mfma_f32_16x16x32_bf16 a[196:199], v[212:215], v[132:135], a[196:199]
s_add_u32 s52, s26, s92
s_addc_u32 s53, s27, 0
v_mfma_f32_16x16x32_bf16 a[144:147], v[216:219], v[132:135], a[144:147]
ds_read_b64_tr_b16 v[44:45], v243 offset:16896
s_and_b32 s53, s53, 0xffff
v_mfma_f32_16x16x32_bf16 a[148:151], v[220:223], v[132:135], a[148:151]
s_mov_b32 s54, 524672
s_mov_b32 s55, 0x20000
v_mfma_f32_16x16x32_bf16 a[200:203], v[192:195], v[136:139], a[200:203]
ds_read_b64_tr_b16 v[46:47], v243 offset:17920
s_lshl_b32 s92, s3, 16
v_mfma_f32_16x16x32_bf16 a[204:207], v[196:199], v[136:139], a[204:207]
s_sub_u32 s93, 458752, s92
v_add_u32 v244, s92, v168
v_mfma_f32_16x16x32_bf16 a[152:155], v[200:203], v[136:139], a[152:155]
ds_read_b64_tr_b16 v[48:49], v240 offset:17024
v_add_u32 v246, s93, v168
v_mfma_f32_16x16x32_bf16 a[156:159], v[204:207], v[136:139], a[156:159]
v_add_u32 v245, 0x8000, v244
v_add_u32 v247, 0x8000, v246
v_mfma_f32_16x16x32_bf16 a[96:99], v[208:211], v[136:139], a[96:99]
ds_read_b64_tr_b16 v[50:51], v240 offset:18048
s_lshl_b32 s94, s72, 19
v_mfma_f32_16x16x32_bf16 a[100:103], v[212:215], v[136:139], a[100:103]
s_lshl_b32 s93, s76, 16
s_add_u32 s92, s94, s93
v_mfma_f32_16x16x32_bf16 a[160:163], v[216:219], v[136:139], a[160:163]
ds_read_b64_tr_b16 v[52:53], v241 offset:17024
s_lshl_b32 s93, s77, 13
v_mfma_f32_16x16x32_bf16 a[164:167], v[220:223], v[136:139], a[164:167]
s_add_u32 s92, s92, s93
s_add_u32 s92, s92, 20971520
v_mfma_f32_16x16x32_bf16 a[104:107], v[192:195], v[140:143], a[104:107]
ds_read_b64_tr_b16 v[54:55], v241 offset:18048
v_mfma_f32_16x16x32_bf16 a[108:111], v[196:199], v[140:143], a[108:111]
s_add_u32 s48, s26, s92
s_addc_u32 s49, s27, 0
v_mfma_f32_16x16x32_bf16 a[168:171], v[200:203], v[140:143], a[168:171]
ds_read_b64_tr_b16 v[56:57], v242 offset:17024
s_and_b32 s49, s49, 0xffff
v_mfma_f32_16x16x32_bf16 a[172:175], v[204:207], v[140:143], a[172:175]
s_mov_b32 s50, 57344
s_mov_b32 s51, 0x20000
v_mfma_f32_16x16x32_bf16 a[112:115], v[208:211], v[140:143], a[112:115]
ds_read_b64_tr_b16 v[58:59], v242 offset:18048
s_lshl_b32 s93, s77, 24
v_mfma_f32_16x16x32_bf16 a[116:119], v[212:215], v[140:143], a[116:119]
s_add_u32 s92, s94, s93
s_add_u32 s92, s92, 20971520
v_mfma_f32_16x16x32_bf16 a[176:179], v[216:219], v[140:143], a[176:179]
ds_read_b64_tr_b16 v[60:61], v243 offset:17024
v_mfma_f32_16x16x32_bf16 a[180:183], v[220:223], v[140:143], a[180:183]
s_add_u32 s60, s26, s92
s_addc_u32 s61, s27, 0
v_mfma_f32_16x16x32_bf16 a[120:123], v[192:195], v[144:147], a[120:123]
ds_read_b64_tr_b16 v[62:63], v243 offset:18048
s_and_b32 s61, s61, 0xffff
s_add_u32 m0, s83, 0
v_mfma_f32_16x16x32_bf16 a[124:127], v[196:199], v[144:147], a[124:127]
buffer_load_dwordx4 v232, s[44:47], s80 offen lds
v_mfma_f32_16x16x32_bf16 a[184:187], v[200:203], v[144:147], a[184:187]
ds_read_b128 v[4:7], v237 offset:26624
s_mov_b32 s62, 524288
v_mfma_f32_16x16x32_bf16 a[188:191], v[204:207], v[144:147], a[188:191]
s_mov_b32 s63, 0x20000
s_lshl_b32 s92, s77, 14
v_mfma_f32_16x16x32_bf16 a[64:67], v[208:211], v[144:147], a[64:67]
ds_read_b128 v[8:11], v237 offset:16384
s_sub_u32 s95, 16384, s92
s_add_u32 m0, s83, 1024
v_mfma_f32_16x16x32_bf16 a[68:71], v[212:215], v[144:147], a[68:71]
buffer_load_dwordx4 v233, s[44:47], s80 offen lds
v_mfma_f32_16x16x32_bf16 a[0:3], v[216:219], v[144:147], a[0:3]
ds_read_b128 v[12:15], v237 offset:18432
v_add_u32 v238, s95, v255
v_mfma_f32_16x16x32_bf16 a[4:7], v[220:223], v[144:147], a[4:7]
v_mfma_f32_16x16x32_bf16 a[72:75], v[192:195], v[148:151], a[72:75]
ds_read_b128 v[16:19], v237 offset:8192
s_add_u32 m0, s83, 2176
v_mfma_f32_16x16x32_bf16 a[76:79], v[196:199], v[148:151], a[76:79]
buffer_load_dwordx4 v234, s[44:47], s80 offen lds
v_mfma_f32_16x16x32_bf16 a[8:11], v[200:203], v[148:151], a[8:11]
ds_read_b128 v[20:23], v237 offset:10240
v_mfma_f32_16x16x32_bf16 a[12:15], v[204:207], v[148:151], a[12:15]
v_mfma_f32_16x16x32_bf16 a[80:83], v[208:211], v[148:151], a[80:83]
ds_read_b128 v[24:27], v237 offset:0
s_add_u32 m0, s83, 3200
v_mfma_f32_16x16x32_bf16 a[84:87], v[212:215], v[148:151], a[84:87]
buffer_load_dwordx4 v235, s[44:47], s80 offen lds
v_mfma_f32_16x16x32_bf16 a[16:19], v[216:219], v[148:151], a[16:19]
ds_read_b128 v[28:31], v237 offset:2048
v_mfma_f32_16x16x32_bf16 a[20:23], v[220:223], v[148:151], a[20:23]
s_add_u32 s80, s80, s81
s_and_b32 s80, s80, 4194303
s_add_u32 s83, s83, 16896
v_mfma_f32_16x16x32_bf16 a[88:91], v[192:195], v[152:155], a[88:91]
v_mfma_f32_16x16x32_bf16 a[92:95], v[196:199], v[152:155], a[92:95]
s_cmp_eq_u32 s83, s97
s_cselect_b32 s83, s90, s83
v_mfma_f32_16x16x32_bf16 a[24:27], v[200:203], v[152:155], a[24:27]
v_mfma_f32_16x16x32_bf16 a[28:31], v[204:207], v[152:155], a[28:31]
v_mfma_f32_16x16x32_bf16 a[240:243], v[208:211], v[152:155], a[240:243]
v_mfma_f32_16x16x32_bf16 a[244:247], v[212:215], v[152:155], a[244:247]
v_mfma_f32_16x16x32_bf16 a[32:35], v[216:219], v[152:155], a[32:35]
v_mfma_f32_16x16x32_bf16 a[36:39], v[220:223], v[152:155], a[36:39]
v_mfma_f32_16x16x32_bf16 a[248:251], v[192:195], v[156:159], a[248:251]
v_mfma_f32_16x16x32_bf16 a[252:255], v[196:199], v[156:159], a[252:255]
v_add_u32 v240, s84, v240
v_add_u32 v241, s84, v241
v_mfma_f32_16x16x32_bf16 a[40:43], v[200:203], v[156:159], a[40:43]
v_add_u32 v242, s84, v242
v_add_u32_e64 v243, s84, v243
v_mfma_f32_16x16x32_bf16 a[44:47], v[204:207], v[156:159], a[44:47]
v_xor_b32 v236, 0x8000, v236
v_xor_b32 v237, 0x8000, v237
v_mfma_f32_16x16x32_bf16 a[48:51], v[208:211], v[156:159], a[48:51]
s_sub_u32 s84, 0, s84
s_add_u32 s85, s85, 1
v_mfma_f32_16x16x32_bf16 a[52:55], v[212:215], v[156:159], a[52:55]
s_cmp_lg_u32 s85, 5
v_mfma_f32_16x16x32_bf16 a[56:59], v[216:219], v[156:159], a[56:59]
v_mfma_f32_16x16x32_bf16 a[60:63], v[220:223], v[156:159], a[60:63]
s_waitcnt lgkmcnt(0)
s_cbranch_scc1 .La5_LK_LOOP
s_waitcnt vmcnt(12)
s_barrier
v_mfma_f32_16x16x32_bf16 a[224:227], v[32:35], v[0:3], a[224:227]
s_waitcnt vmcnt(4)
v_mfma_f32_16x16x32_bf16 a[228:231], v[36:39], v[0:3], a[228:231]
v_cmp_eq_u64 vcc, v[72:73], v[252:253]
v_cmp_eq_u64 s[96:97], v[74:75], v[252:253]
s_mov_b32 s91, 0
v_mfma_f32_16x16x32_bf16 a[232:235], v[40:43], v[0:3], a[232:235]
s_and_b64 vcc, vcc, s[96:97]
s_cmp_eq_u64 vcc, -1
s_cbranch_scc0 .La5_LK_XSPIN
.La5_LK_XOK:
v_mfma_f32_16x16x32_bf16 a[236:239], v[44:47], v[0:3], a[236:239]
s_mov_b32 s89, 49152
v_mfma_f32_16x16x32_bf16 a[208:211], v[48:51], v[0:3], a[208:211]
v_mfma_f32_16x16x32_bf16 a[212:215], v[52:55], v[0:3], a[212:215]
buffer_load_dwordx4 v[128:131], v244, s[52:55], s91 offen offset:0 sc1
v_mfma_f32_16x16x32_bf16 a[128:131], v[56:59], v[0:3], a[128:131]
v_mfma_f32_16x16x32_bf16 a[132:135], v[60:63], v[0:3], a[132:135]
v_mfma_f32_16x16x32_bf16 a[216:219], v[32:35], v[4:7], a[216:219]
ds_read_b64_tr_b16 v[192:193], v240 offset:0
v_mfma_f32_16x16x32_bf16 a[220:223], v[36:39], v[4:7], a[220:223]
buffer_load_dwordx4 v[132:135], v245, s[52:55], s91 offen offset:0 sc1
v_mfma_f32_16x16x32_bf16 a[136:139], v[40:43], v[4:7], a[136:139]
ds_read_b64_tr_b16 v[194:195], v240 offset:1024
v_mfma_f32_16x16x32_bf16 a[140:143], v[44:47], v[4:7], a[140:143]
v_mfma_f32_16x16x32_bf16 a[192:195], v[48:51], v[4:7], a[192:195]
ds_read_b64_tr_b16 v[196:197], v241 offset:0
v_mfma_f32_16x16x32_bf16 a[196:199], v[52:55], v[4:7], a[196:199]
buffer_load_dwordx4 v[136:139], v246, s[52:55], s91 offen offset:0 sc1
v_mfma_f32_16x16x32_bf16 a[144:147], v[56:59], v[4:7], a[144:147]
ds_read_b64_tr_b16 v[198:199], v241 offset:1024
v_accvgpr_read_b32 v224, a224
v_mfma_f32_16x16x32_bf16 a[148:151], v[60:63], v[4:7], a[148:151]
v_accvgpr_read_b32 v225, a225
v_accvgpr_read_b32 v226, a226
v_mfma_f32_16x16x32_bf16 a[200:203], v[32:35], v[8:11], a[200:203]
ds_read_b64_tr_b16 v[200:201], v242 offset:0
v_accvgpr_read_b32 v227, a227
v_mfma_f32_16x16x32_bf16 a[204:207], v[36:39], v[8:11], a[204:207]
buffer_load_dwordx4 v[140:143], v247, s[52:55], s91 offen offset:0 sc1
v_accvgpr_read_b32 v228, a228
v_mfma_f32_16x16x32_bf16 a[152:155], v[40:43], v[8:11], a[152:155]
ds_read_b64_tr_b16 v[202:203], v242 offset:1024
v_accvgpr_read_b32 v229, a229
v_mfma_f32_16x16x32_bf16 a[156:159], v[44:47], v[8:11], a[156:159]
v_accvgpr_read_b32 v230, a230
v_accvgpr_read_b32 v231, a231
v_mfma_f32_16x16x32_bf16 a[96:99], v[48:51], v[8:11], a[96:99]
ds_read_b64_tr_b16 v[204:205], v243 offset:0
v_cvt_pk_bf16_f32 v248, v224, v225
v_mfma_f32_16x16x32_bf16 a[100:103], v[52:55], v[8:11], a[100:103]
buffer_load_dwordx4 v[144:147], v244, s[52:55], s91 offen offset:1024 sc1
v_cvt_pk_bf16_f32 v249, v226, v227
v_mfma_f32_16x16x32_bf16 a[160:163], v[56:59], v[8:11], a[160:163]
ds_read_b64_tr_b16 v[206:207], v243 offset:1024
v_cvt_pk_bf16_f32 v250, v228, v229
v_mfma_f32_16x16x32_bf16 a[164:167], v[60:63], v[8:11], a[164:167]
v_cvt_pk_bf16_f32 v251, v230, v231
buffer_store_dwordx4 v[248:251], v255, s[48:51], s89 offen sc1
v_mfma_f32_16x16x32_bf16 a[104:107], v[32:35], v[12:15], a[104:107]
ds_read_b64_tr_b16 v[208:209], v240 offset:128
s_add_u32 s89, s89, 1024
v_mfma_f32_16x16x32_bf16 a[108:111], v[36:39], v[12:15], a[108:111]
buffer_load_dwordx4 v[148:151], v245, s[52:55], s91 offen offset:1024 sc1
ds_write_b128 v238, v[248:251] offset:0
v_mfma_f32_16x16x32_bf16 a[168:171], v[40:43], v[12:15], a[168:171]
ds_read_b64_tr_b16 v[210:211], v240 offset:1152
v_mfma_f32_16x16x32_bf16 a[172:175], v[44:47], v[12:15], a[172:175]
v_mfma_f32_16x16x32_bf16 a[112:115], v[48:51], v[12:15], a[112:115]
ds_read_b64_tr_b16 v[212:213], v241 offset:128
v_mfma_f32_16x16x32_bf16 a[116:119], v[52:55], v[12:15], a[116:119]
buffer_load_dwordx4 v[152:155], v246, s[52:55], s91 offen offset:1024 sc1
v_mfma_f32_16x16x32_bf16 a[176:179], v[56:59], v[12:15], a[176:179]
ds_read_b64_tr_b16 v[214:215], v241 offset:1152
v_accvgpr_read_b32 v224, a232
v_mfma_f32_16x16x32_bf16 a[180:183], v[60:63], v[12:15], a[180:183]
v_accvgpr_read_b32 v225, a233
v_accvgpr_read_b32 v226, a234
v_mfma_f32_16x16x32_bf16 a[120:123], v[32:35], v[16:19], a[120:123]
ds_read_b64_tr_b16 v[216:217], v242 offset:128
v_accvgpr_read_b32 v227, a235
s_add_u32 m0, s83, 0
v_mfma_f32_16x16x32_bf16 a[124:127], v[36:39], v[16:19], a[124:127]
buffer_load_dwordx4 v232, s[44:47], s80 offen lds
v_mfma_f32_16x16x32_bf16 a[184:187], v[40:43], v[16:19], a[184:187]
ds_read_b64_tr_b16 v[218:219], v242 offset:1152
v_accvgpr_read_b32 v228, a236
v_mfma_f32_16x16x32_bf16 a[188:191], v[44:47], v[16:19], a[188:191]
buffer_load_dwordx4 v[156:159], v247, s[52:55], s91 offen offset:1024 sc1
v_accvgpr_read_b32 v229, a237
v_mfma_f32_16x16x32_bf16 a[64:67], v[48:51], v[16:19], a[64:67]
ds_read_b64_tr_b16 v[220:221], v243 offset:128
v_accvgpr_read_b32 v230, a238
s_add_u32 m0, s83, 1024
v_mfma_f32_16x16x32_bf16 a[68:71], v[52:55], v[16:19], a[68:71]
buffer_load_dwordx4 v233, s[44:47], s80 offen lds
v_mfma_f32_16x16x32_bf16 a[0:3], v[56:59], v[16:19], a[0:3]
ds_read_b64_tr_b16 v[222:223], v243 offset:1152
v_accvgpr_read_b32 v231, a239
v_mfma_f32_16x16x32_bf16 a[4:7], v[60:63], v[16:19], a[4:7]
v_cvt_pk_bf16_f32 v248, v224, v225
v_cvt_pk_bf16_f32 v249, v226, v227
v_mfma_f32_16x16x32_bf16 a[72:75], v[32:35], v[20:23], a[72:75]
v_cvt_pk_bf16_f32 v250, v228, v229
v_cvt_pk_bf16_f32 v251, v230, v231
s_add_u32 m0, s83, 2176
v_mfma_f32_16x16x32_bf16 a[76:79], v[36:39], v[20:23], a[76:79]
buffer_load_dwordx4 v234, s[44:47], s80 offen lds
v_mfma_f32_16x16x32_bf16 a[8:11], v[40:43], v[20:23], a[8:11]
buffer_store_dwordx4 v[248:251], v255, s[48:51], s89 offen sc1
s_add_u32 s89, s89, 1024
v_mfma_f32_16x16x32_bf16 a[12:15], v[44:47], v[20:23], a[12:15]
buffer_load_dwordx4 v[160:163], v244, s[52:55], s91 offen offset:2048 sc1
ds_write_b128 v238, v[248:251] offset:1024
v_mfma_f32_16x16x32_bf16 a[80:83], v[48:51], v[20:23], a[80:83]
s_add_u32 m0, s83, 3200
v_mfma_f32_16x16x32_bf16 a[84:87], v[52:55], v[20:23], a[84:87]
buffer_load_dwordx4 v235, s[44:47], s80 offen lds
v_mfma_f32_16x16x32_bf16 a[16:19], v[56:59], v[20:23], a[16:19]
v_accvgpr_read_b32 v224, a208
v_accvgpr_read_b32 v225, a209
v_mfma_f32_16x16x32_bf16 a[20:23], v[60:63], v[20:23], a[20:23]
s_add_u32 s80, s80, s81
s_and_b32 s80, s80, 4194303
s_add_u32 s83, s83, 16896
v_mfma_f32_16x16x32_bf16 a[88:91], v[32:35], v[24:27], a[88:91]
v_accvgpr_read_b32 v226, a210
v_accvgpr_read_b32 v227, a211
v_mfma_f32_16x16x32_bf16 a[92:95], v[36:39], v[24:27], a[92:95]
s_mov_b32 s83, s90
v_accvgpr_read_b32 v228, a212
v_mfma_f32_16x16x32_bf16 a[24:27], v[40:43], v[24:27], a[24:27]
v_accvgpr_read_b32 v229, a213
v_accvgpr_read_b32 v230, a214
v_mfma_f32_16x16x32_bf16 a[28:31], v[44:47], v[24:27], a[28:31]
buffer_load_dwordx4 v[164:167], v245, s[52:55], s91 offen offset:2048 sc1
v_accvgpr_read_b32 v231, a215
v_mfma_f32_16x16x32_bf16 a[240:243], v[48:51], v[24:27], a[240:243]
v_cvt_pk_bf16_f32 v248, v224, v225
v_cvt_pk_bf16_f32 v249, v226, v227
v_mfma_f32_16x16x32_bf16 a[244:247], v[52:55], v[24:27], a[244:247]
v_cvt_pk_bf16_f32 v250, v228, v229
v_cvt_pk_bf16_f32 v251, v230, v231
v_mfma_f32_16x16x32_bf16 a[32:35], v[56:59], v[24:27], a[32:35]
buffer_store_dwordx4 v[248:251], v255, s[48:51], s89 offen sc1
s_add_u32 s89, s89, 1024
v_mfma_f32_16x16x32_bf16 a[36:39], v[60:63], v[24:27], a[36:39]
buffer_load_dwordx4 v[168:171], v246, s[52:55], s91 offen offset:2048 sc1
ds_write_b128 v238, v[248:251] offset:2048
v_mfma_f32_16x16x32_bf16 a[248:251], v[32:35], v[28:31], a[248:251]
v_mfma_f32_16x16x32_bf16 a[252:255], v[36:39], v[28:31], a[252:255]
v_mfma_f32_16x16x32_bf16 a[40:43], v[40:43], v[28:31], a[40:43]
v_mfma_f32_16x16x32_bf16 a[44:47], v[44:47], v[28:31], a[44:47]
buffer_load_dwordx4 v[172:175], v247, s[52:55], s91 offen offset:2048 sc1
v_mfma_f32_16x16x32_bf16 a[48:51], v[48:51], v[28:31], a[48:51]
v_mfma_f32_16x16x32_bf16 a[52:55], v[52:55], v[28:31], a[52:55]
v_mfma_f32_16x16x32_bf16 a[56:59], v[56:59], v[28:31], a[56:59]
v_mfma_f32_16x16x32_bf16 a[60:63], v[60:63], v[28:31], a[60:63]
s_branch .La5_LQ_START
; ---- V tile (waves 1, 3): the same with the operands exchanged (V comes out transposed)
.La5_LV_START:
.La5_LW1_2:
s_cbranch_execz .La5_LW1_6
s_waitcnt vmcnt(1)
s_barrier
v_mfma_f32_16x16x32_bf16 a[224:227], v[128:131], v[192:195], 0
ds_read_b128 v[0:3], v237 offset:24576
s_add_u32 m0, s83, 33792
v_mfma_f32_16x16x32_bf16 a[232:235], v[128:131], v[196:199], 0
buffer_load_dwordx4 v232, s[44:47], s80 offen lds
v_mfma_f32_16x16x32_bf16 a[208:211], v[128:131], v[200:203], 0
ds_read_b64_tr_b16 v[32:33], v240 offset:16896
v_mfma_f32_16x16x32_bf16 a[128:131], v[128:131], v[204:207], 0
v_mfma_f32_16x16x32_bf16 a[216:219], v[128:131], v[208:211], 0
ds_read_b64_tr_b16 v[34:35], v240 offset:17920
s_add_u32 m0, s83, 34816
v_mfma_f32_16x16x32_bf16 a[136:139], v[128:131], v[212:215], 0
buffer_load_dwordx4 v233, s[44:47], s80 offen lds
v_mfma_f32_16x16x32_bf16 a[192:195], v[128:131], v[216:219], 0
ds_read_b64_tr_b16 v[36:37], v241 offset:16896
v_mfma_f32_16x16x32_bf16 a[144:147], v[128:131], v[220:223], 0
v_mfma_f32_16x16x32_bf16 a[228:231], v[132:135], v[192:195], 0
ds_read_b64_tr_b16 v[38:39], v241 offset:17920
s_add_u32 m0, s83, 35968
v_mfma_f32_16x16x32_bf16 a[236:239], v[132:135], v[196:199], 0
buffer_load_dwordx4 v234, s[44:47], s80 offen lds
v_mfma_f32_16x16x32_bf16 a[212:215], v[132:135], v[200:203], 0
ds_read_b64_tr_b16 v[40:41], v242 offset:16896
v_mfma_f32_16x16x32_bf16 a[132:135], v[132:135], v[204:207], 0
v_mfma_f32_16x16x32_bf16 a[220:223], v[132:135], v[208:211], 0
ds_read_b64_tr_b16 v[42:43], v242 offset:17920
s_add_u32 m0, s83, 36992
v_mfma_f32_16x16x32_bf16 a[140:143], v[132:135], v[212:215], 0
buffer_load_dwordx4 v235, s[44:47], s80 offen lds
v_mfma_f32_16x16x32_bf16 a[196:199], v[132:135], v[216:219], 0
ds_read_b64_tr_b16 v[44:45], v243 offset:16896
v_mfma_f32_16x16x32_bf16 a[148:151], v[132:135], v[220:223], 0
s_add_u32 s80, s80, s81
s_and_b32 s80, s80, 4194303
v_mfma_f32_16x16x32_bf16 a[200:203], v[136:139], v[192:195], 0
ds_read_b64_tr_b16 v[46:47], v243 offset:17920
s_xor_b32 m0, s82, 32768
v_mfma_f32_16x16x32_bf16 a[152:155], v[136:139], v[196:199], 0
buffer_load_dwordx4 v224, s[40:43], s79 offen lds
v_mfma_f32_16x16x32_bf16 a[96:99], v[136:139], v[200:203], 0
ds_read_b64_tr_b16 v[48:49], v240 offset:17024
v_mfma_f32_16x16x32_bf16 a[160:163], v[136:139], v[204:207], 0
v_mfma_f32_16x16x32_bf16 a[104:107], v[136:139], v[208:211], 0
ds_read_b64_tr_b16 v[50:51], v240 offset:18048
s_xor_b32 m0, s82, 33792
v_mfma_f32_16x16x32_bf16 a[168:171], v[136:139], v[212:215], 0
buffer_load_dwordx4 v225, s[40:43], s79 offen lds
v_mfma_f32_16x16x32_bf16 a[112:115], v[136:139], v[216:219], 0
ds_read_b64_tr_b16 v[52:53], v241 offset:17024
v_mfma_f32_16x16x32_bf16 a[176:179], v[136:139], v[220:223], 0
v_mfma_f32_16x16x32_bf16 a[204:207], v[140:143], v[192:195], 0
ds_read_b64_tr_b16 v[54:55], v241 offset:18048
s_xor_b32 m0, s82, 34816
v_mfma_f32_16x16x32_bf16 a[156:159], v[140:143], v[196:199], 0
buffer_load_dwordx4 v226, s[40:43], s79 offen lds
v_mfma_f32_16x16x32_bf16 a[100:103], v[140:143], v[200:203], 0
ds_read_b64_tr_b16 v[56:57], v242 offset:17024
v_mfma_f32_16x16x32_bf16 a[164:167], v[140:143], v[204:207], 0
v_mfma_f32_16x16x32_bf16 a[108:111], v[140:143], v[208:211], 0
ds_read_b64_tr_b16 v[58:59], v242 offset:18048
s_xor_b32 m0, s82, 35840
v_mfma_f32_16x16x32_bf16 a[172:175], v[140:143], v[212:215], 0
buffer_load_dwordx4 v227, s[40:43], s79 offen lds
v_mfma_f32_16x16x32_bf16 a[116:119], v[140:143], v[216:219], 0
ds_read_b64_tr_b16 v[60:61], v243 offset:17024
v_mfma_f32_16x16x32_bf16 a[180:183], v[140:143], v[220:223], 0
v_mfma_f32_16x16x32_bf16 a[120:123], v[144:147], v[192:195], 0
ds_read_b64_tr_b16 v[62:63], v243 offset:18048
s_xor_b32 m0, s82, 36864
v_mfma_f32_16x16x32_bf16 a[184:187], v[144:147], v[196:199], 0
buffer_load_dwordx4 v228, s[40:43], s79 offen lds
v_mfma_f32_16x16x32_bf16 a[64:67], v[144:147], v[200:203], 0
ds_read_b128 v[4:7], v237 offset:26624
v_mfma_f32_16x16x32_bf16 a[0:3], v[144:147], v[204:207], 0
v_mfma_f32_16x16x32_bf16 a[72:75], v[144:147], v[208:211], 0
ds_read_b128 v[8:11], v237 offset:16384
s_xor_b32 m0, s82, 37888
v_mfma_f32_16x16x32_bf16 a[8:11], v[144:147], v[212:215], 0
buffer_load_dwordx4 v229, s[40:43], s79 offen lds
v_mfma_f32_16x16x32_bf16 a[80:83], v[144:147], v[216:219], 0
ds_read_b128 v[12:15], v237 offset:18432
v_mfma_f32_16x16x32_bf16 a[16:19], v[144:147], v[220:223], 0
v_mfma_f32_16x16x32_bf16 a[124:127], v[148:151], v[192:195], 0
ds_read_b128 v[16:19], v237 offset:8192
s_xor_b32 m0, s82, 38912
v_mfma_f32_16x16x32_bf16 a[188:191], v[148:151], v[196:199], 0
buffer_load_dwordx4 v230, s[40:43], s79 offen lds
v_mfma_f32_16x16x32_bf16 a[68:71], v[148:151], v[200:203], 0
ds_read_b128 v[20:23], v237 offset:10240
v_mfma_f32_16x16x32_bf16 a[4:7], v[148:151], v[204:207], 0
v_mfma_f32_16x16x32_bf16 a[76:79], v[148:151], v[208:211], 0
ds_read_b128 v[24:27], v237 offset:0
s_xor_b32 m0, s82, 39936
v_mfma_f32_16x16x32_bf16 a[12:15], v[148:151], v[212:215], 0
buffer_load_dwordx4 v231, s[40:43], s79 offen lds
v_mfma_f32_16x16x32_bf16 a[84:87], v[148:151], v[216:219], 0
ds_read_b128 v[28:31], v237 offset:2048
v_mfma_f32_16x16x32_bf16 a[20:23], v[148:151], v[220:223], 0
s_add_u32 s79, s79, 128
s_and_b32 s79, s79, 1023
v_mfma_f32_16x16x32_bf16 a[88:91], v[152:155], v[192:195], 0
v_mfma_f32_16x16x32_bf16 a[24:27], v[152:155], v[196:199], 0
buffer_load_dword v70, v118, s[60:63], 0 offen
v_mfma_f32_16x16x32_bf16 a[240:243], v[152:155], v[200:203], 0
v_mfma_f32_16x16x32_bf16 a[32:35], v[152:155], v[204:207], 0
v_mfma_f32_16x16x32_bf16 a[248:251], v[152:155], v[208:211], 0
buffer_load_dwordx4 v[72:75], v119, s[60:63], 0 offen offset:0
v_mfma_f32_16x16x32_bf16 a[40:43], v[152:155], v[212:215], 0
v_mfma_f32_16x16x32_bf16 a[48:51], v[152:155], v[216:219], 0
v_mfma_f32_16x16x32_bf16 a[56:59], v[152:155], v[220:223], 0
buffer_load_dwordx4 v[76:79], v119, s[60:63], 0 offen offset:128
v_mfma_f32_16x16x32_bf16 a[92:95], v[156:159], v[192:195], 0
v_mfma_f32_16x16x32_bf16 a[28:31], v[156:159], v[196:199], 0
v_mfma_f32_16x16x32_bf16 a[244:247], v[156:159], v[200:203], 0
buffer_load_dwordx4 v[80:83], v119, s[60:63], 0 offen offset:256
v_mfma_f32_16x16x32_bf16 a[36:39], v[156:159], v[204:207], 0
v_mfma_f32_16x16x32_bf16 a[252:255], v[156:159], v[208:211], 0
v_mfma_f32_16x16x32_bf16 a[44:47], v[156:159], v[212:215], 0
buffer_load_dwordx4 v[84:87], v119, s[60:63], 0 offen offset:384
v_mfma_f32_16x16x32_bf16 a[52:55], v[156:159], v[216:219], 0
v_mfma_f32_16x16x32_bf16 a[60:63], v[156:159], v[220:223], 0
s_mov_b64 exec, 0
s_cmp_eq_u32 s3, 1
s_cbranch_scc1 .La5_LW2_1
s_branch .La5_LW2_3
.La5_LW2_R1:
s_mov_b64 exec, -1
s_waitcnt lgkmcnt(0)
s_waitcnt vmcnt(5)
.La5_LW1_3:
s_cbranch_execz .La5_LW1_7
s_barrier
v_mfma_f32_16x16x32_bf16 a[224:227], v[0:3], v[32:35], a[224:227]
ds_read_b128 v[128:131], v236 offset:57344
s_add_u32 m0, s83, 50688
v_mfma_f32_16x16x32_bf16 a[232:235], v[0:3], v[36:39], a[232:235]
buffer_load_dwordx4 v232, s[44:47], s80 offen lds
v_mfma_f32_16x16x32_bf16 a[208:211], v[0:3], v[40:43], a[208:211]
ds_read_b64_tr_b16 v[192:193], v240 offset:33792
v_mfma_f32_16x16x32_bf16 a[128:131], v[0:3], v[44:47], a[128:131]
v_mfma_f32_16x16x32_bf16 a[216:219], v[0:3], v[48:51], a[216:219]
ds_read_b64_tr_b16 v[194:195], v240 offset:34816
s_add_u32 m0, s83, 51712
v_mfma_f32_16x16x32_bf16 a[136:139], v[0:3], v[52:55], a[136:139]
buffer_load_dwordx4 v233, s[44:47], s80 offen lds
v_mfma_f32_16x16x32_bf16 a[192:195], v[0:3], v[56:59], a[192:195]
ds_read_b64_tr_b16 v[196:197], v241 offset:33792
v_mfma_f32_16x16x32_bf16 a[144:147], v[0:3], v[60:63], a[144:147]
v_mfma_f32_16x16x32_bf16 a[228:231], v[4:7], v[32:35], a[228:231]
ds_read_b64_tr_b16 v[198:199], v241 offset:34816
s_add_u32 m0, s83, 52864
v_mfma_f32_16x16x32_bf16 a[236:239], v[4:7], v[36:39], a[236:239]
buffer_load_dwordx4 v234, s[44:47], s80 offen lds
v_mfma_f32_16x16x32_bf16 a[212:215], v[4:7], v[40:43], a[212:215]
ds_read_b64_tr_b16 v[200:201], v242 offset:33792
v_mfma_f32_16x16x32_bf16 a[132:135], v[4:7], v[44:47], a[132:135]
v_mfma_f32_16x16x32_bf16 a[220:223], v[4:7], v[48:51], a[220:223]
ds_read_b64_tr_b16 v[202:203], v242 offset:34816
s_add_u32 m0, s83, 53888
v_mfma_f32_16x16x32_bf16 a[140:143], v[4:7], v[52:55], a[140:143]
buffer_load_dwordx4 v235, s[44:47], s80 offen lds
v_mfma_f32_16x16x32_bf16 a[196:199], v[4:7], v[56:59], a[196:199]
ds_read_b64_tr_b16 v[204:205], v243 offset:33792
v_mfma_f32_16x16x32_bf16 a[148:151], v[4:7], v[60:63], a[148:151]
s_add_u32 s80, s80, s81
s_and_b32 s80, s80, 4194303
v_mfma_f32_16x16x32_bf16 a[200:203], v[8:11], v[32:35], a[200:203]
ds_read_b64_tr_b16 v[206:207], v243 offset:34816
s_xor_b32 m0, s82, 0
v_mfma_f32_16x16x32_bf16 a[152:155], v[8:11], v[36:39], a[152:155]
buffer_load_dwordx4 v224, s[40:43], s79 offen lds
v_mfma_f32_16x16x32_bf16 a[96:99], v[8:11], v[40:43], a[96:99]
ds_read_b64_tr_b16 v[208:209], v240 offset:33920
v_mfma_f32_16x16x32_bf16 a[160:163], v[8:11], v[44:47], a[160:163]
v_mfma_f32_16x16x32_bf16 a[104:107], v[8:11], v[48:51], a[104:107]
ds_read_b64_tr_b16 v[210:211], v240 offset:34944
s_xor_b32 m0, s82, 1024
v_mfma_f32_16x16x32_bf16 a[168:171], v[8:11], v[52:55], a[168:171]
buffer_load_dwordx4 v225, s[40:43], s79 offen lds
v_mfma_f32_16x16x32_bf16 a[112:115], v[8:11], v[56:59], a[112:115]
ds_read_b64_tr_b16 v[212:213], v241 offset:33920
v_mfma_f32_16x16x32_bf16 a[176:179], v[8:11], v[60:63], a[176:179]
v_mfma_f32_16x16x32_bf16 a[204:207], v[12:15], v[32:35], a[204:207]
ds_read_b64_tr_b16 v[214:215], v241 offset:34944
s_xor_b32 m0, s82, 2048
v_mfma_f32_16x16x32_bf16 a[156:159], v[12:15], v[36:39], a[156:159]
buffer_load_dwordx4 v226, s[40:43], s79 offen lds
v_mfma_f32_16x16x32_bf16 a[100:103], v[12:15], v[40:43], a[100:103]
ds_read_b64_tr_b16 v[216:217], v242 offset:33920
v_mfma_f32_16x16x32_bf16 a[164:167], v[12:15], v[44:47], a[164:167]
v_mfma_f32_16x16x32_bf16 a[108:111], v[12:15], v[48:51], a[108:111]
ds_read_b64_tr_b16 v[218:219], v242 offset:34944
s_xor_b32 m0, s82, 3072
v_mfma_f32_16x16x32_bf16 a[172:175], v[12:15], v[52:55], a[172:175]
buffer_load_dwordx4 v227, s[40:43], s79 offen lds
v_mfma_f32_16x16x32_bf16 a[116:119], v[12:15], v[56:59], a[116:119]
ds_read_b64_tr_b16 v[220:221], v243 offset:33920
v_mfma_f32_16x16x32_bf16 a[180:183], v[12:15], v[60:63], a[180:183]
v_mfma_f32_16x16x32_bf16 a[120:123], v[16:19], v[32:35], a[120:123]
ds_read_b64_tr_b16 v[222:223], v243 offset:34944
s_xor_b32 m0, s82, 4096
v_mfma_f32_16x16x32_bf16 a[184:187], v[16:19], v[36:39], a[184:187]
buffer_load_dwordx4 v228, s[40:43], s79 offen lds
v_mfma_f32_16x16x32_bf16 a[64:67], v[16:19], v[40:43], a[64:67]
ds_read_b128 v[132:135], v236 offset:59392
v_mfma_f32_16x16x32_bf16 a[0:3], v[16:19], v[44:47], a[0:3]
v_mfma_f32_16x16x32_bf16 a[72:75], v[16:19], v[48:51], a[72:75]
ds_read_b128 v[136:139], v236 offset:49152
s_xor_b32 m0, s82, 5120
v_mfma_f32_16x16x32_bf16 a[8:11], v[16:19], v[52:55], a[8:11]
buffer_load_dwordx4 v229, s[40:43], s79 offen lds
v_mfma_f32_16x16x32_bf16 a[80:83], v[16:19], v[56:59], a[80:83]
ds_read_b128 v[140:143], v236 offset:51200
v_mfma_f32_16x16x32_bf16 a[16:19], v[16:19], v[60:63], a[16:19]
v_mfma_f32_16x16x32_bf16 a[124:127], v[20:23], v[32:35], a[124:127]
ds_read_b128 v[144:147], v236 offset:40960
s_xor_b32 m0, s82, 6144
v_mfma_f32_16x16x32_bf16 a[188:191], v[20:23], v[36:39], a[188:191]
buffer_load_dwordx4 v230, s[40:43], s79 offen lds
v_mfma_f32_16x16x32_bf16 a[68:71], v[20:23], v[40:43], a[68:71]
ds_read_b128 v[148:151], v236 offset:43008
v_mfma_f32_16x16x32_bf16 a[4:7], v[20:23], v[44:47], a[4:7]
v_mfma_f32_16x16x32_bf16 a[76:79], v[20:23], v[48:51], a[76:79]
ds_read_b128 v[152:155], v236 offset:32768
s_xor_b32 m0, s82, 7168
v_mfma_f32_16x16x32_bf16 a[12:15], v[20:23], v[52:55], a[12:15]
buffer_load_dwordx4 v231, s[40:43], s79 offen lds
v_mfma_f32_16x16x32_bf16 a[84:87], v[20:23], v[56:59], a[84:87]
ds_read_b128 v[156:159], v236 offset:34816
s_waitcnt vmcnt(12)
v_mfma_f32_16x16x32_bf16 a[20:23], v[20:23], v[60:63], a[20:23]
s_add_u32 s79, s79, 128
s_and_b32 s79, s79, 1023
v_mfma_f32_16x16x32_bf16 a[88:91], v[24:27], v[32:35], a[88:91]
v_lshlrev_b32 v70, 16, v70
v_lshlrev_b32 v88, 16, v72
s_add_u32 m0, s83, 0
v_mfma_f32_16x16x32_bf16 a[24:27], v[24:27], v[36:39], a[24:27]
buffer_load_dwordx4 v232, s[44:47], s80 offen lds
v_mfma_f32_16x16x32_bf16 a[240:243], v[24:27], v[40:43], a[240:243]
v_and_b32 v89, 0xffff0000, v72
v_sub_f32 v88, v88, v70
v_mfma_f32_16x16x32_bf16 a[32:35], v[24:27], v[44:47], a[32:35]
v_sub_f32 v89, v89, v70
v_add_f32 v120, v88, v89
v_mfma_f32_16x16x32_bf16 a[248:251], v[24:27], v[48:51], a[248:251]
v_mul_f32 v122, v88, v88
v_mul_f32 v123, v89, v89
s_add_u32 m0, s83, 1024
v_mfma_f32_16x16x32_bf16 a[40:43], v[24:27], v[52:55], a[40:43]
buffer_load_dwordx4 v233, s[44:47], s80 offen lds
v_mfma_f32_16x16x32_bf16 a[48:51], v[24:27], v[56:59], a[48:51]
v_lshlrev_b32 v90, 16, v73
v_and_b32 v91, 0xffff0000, v73
v_mfma_f32_16x16x32_bf16 a[56:59], v[24:27], v[60:63], a[56:59]
v_sub_f32 v90, v90, v70
v_sub_f32 v91, v91, v70
v_mfma_f32_16x16x32_bf16 a[92:95], v[28:31], v[32:35], a[92:95]
v_add_f32 v124, v90, v91
v_fmac_f32 v122, v90, v90
s_add_u32 m0, s83, 2176
v_mfma_f32_16x16x32_bf16 a[28:31], v[28:31], v[36:39], a[28:31]
buffer_load_dwordx4 v234, s[44:47], s80 offen lds
v_mfma_f32_16x16x32_bf16 a[244:247], v[28:31], v[40:43], a[244:247]
v_add_f32 v120, v120, v124
v_fmac_f32 v123, v91, v91
v_mfma_f32_16x16x32_bf16 a[36:39], v[28:31], v[44:47], a[36:39]
v_lshlrev_b32 v92, 16, v74
v_and_b32 v93, 0xffff0000, v74
v_mfma_f32_16x16x32_bf16 a[252:255], v[28:31], v[48:51], a[252:255]
v_sub_f32 v92, v92, v70
v_sub_f32 v93, v93, v70
s_add_u32 m0, s83, 3200
v_mfma_f32_16x16x32_bf16 a[44:47], v[28:31], v[52:55], a[44:47]
buffer_load_dwordx4 v235, s[44:47], s80 offen lds
v_mfma_f32_16x16x32_bf16 a[52:55], v[28:31], v[56:59], a[52:55]
v_add_f32 v124, v92, v93
v_fmac_f32 v122, v92, v92
v_mfma_f32_16x16x32_bf16 a[60:63], v[28:31], v[60:63], a[60:63]
s_add_u32 s80, s80, s81
s_and_b32 s80, s80, 4194303
s_waitcnt lgkmcnt(0)
s_waitcnt vmcnt(12)
s_barrier
v_mfma_f32_16x16x32_bf16 a[224:227], v[128:131], v[192:195], a[224:227]
ds_read_b128 v[0:3], v237 offset:57344
s_add_u32 m0, s83, 16896
v_mfma_f32_16x16x32_bf16 a[232:235], v[128:131], v[196:199], a[232:235]
buffer_load_dwordx4 v232, s[44:47], s80 offen lds
v_mfma_f32_16x16x32_bf16 a[208:211], v[128:131], v[200:203], a[208:211]
ds_read_b64_tr_b16 v[32:33], v240 offset:50688
v_add_f32 v120, v120, v124
v_mfma_f32_16x16x32_bf16 a[128:131], v[128:131], v[204:207], a[128:131]
v_fmac_f32 v123, v93, v93
v_lshlrev_b32 v94, 16, v75
v_mfma_f32_16x16x32_bf16 a[216:219], v[128:131], v[208:211], a[216:219]
ds_read_b64_tr_b16 v[34:35], v240 offset:51712
v_and_b32 v95, 0xffff0000, v75
s_add_u32 m0, s83, 17920
v_mfma_f32_16x16x32_bf16 a[136:139], v[128:131], v[212:215], a[136:139]
buffer_load_dwordx4 v233, s[44:47], s80 offen lds
v_mfma_f32_16x16x32_bf16 a[192:195], v[128:131], v[216:219], a[192:195]
ds_read_b64_tr_b16 v[36:37], v241 offset:50688
v_sub_f32 v94, v94, v70
v_mfma_f32_16x16x32_bf16 a[144:147], v[128:131], v[220:223], a[144:147]
v_sub_f32 v95, v95, v70
v_add_f32 v124, v94, v95
v_mfma_f32_16x16x32_bf16 a[228:231], v[132:135], v[192:195], a[228:231]
ds_read_b64_tr_b16 v[38:39], v241 offset:51712
v_fmac_f32 v122, v94, v94
s_add_u32 m0, s83, 19072
v_mfma_f32_16x16x32_bf16 a[236:239], v[132:135], v[196:199], a[236:239]
buffer_load_dwordx4 v234, s[44:47], s80 offen lds
v_mfma_f32_16x16x32_bf16 a[212:215], v[132:135], v[200:203], a[212:215]
ds_read_b64_tr_b16 v[40:41], v242 offset:50688
v_add_f32 v120, v120, v124
v_mfma_f32_16x16x32_bf16 a[132:135], v[132:135], v[204:207], a[132:135]
v_fmac_f32 v123, v95, v95
v_lshlrev_b32 v96, 16, v76
v_mfma_f32_16x16x32_bf16 a[220:223], v[132:135], v[208:211], a[220:223]
ds_read_b64_tr_b16 v[42:43], v242 offset:51712
v_and_b32 v97, 0xffff0000, v76
s_add_u32 m0, s83, 20096
v_mfma_f32_16x16x32_bf16 a[140:143], v[132:135], v[212:215], a[140:143]
buffer_load_dwordx4 v235, s[44:47], s80 offen lds
v_mfma_f32_16x16x32_bf16 a[196:199], v[132:135], v[216:219], a[196:199]
ds_read_b64_tr_b16 v[44:45], v243 offset:50688
v_sub_f32 v96, v96, v70
v_mfma_f32_16x16x32_bf16 a[148:151], v[132:135], v[220:223], a[148:151]
s_add_u32 s80, s80, s81
s_and_b32 s80, s80, 4194303
v_mfma_f32_16x16x32_bf16 a[200:203], v[136:139], v[192:195], a[200:203]
ds_read_b64_tr_b16 v[46:47], v243 offset:51712
v_sub_f32 v97, v97, v70
s_add_u32 m0, s83, 33792
v_mfma_f32_16x16x32_bf16 a[152:155], v[136:139], v[196:199], a[152:155]
buffer_load_dwordx4 v232, s[44:47], s80 offen lds
v_mfma_f32_16x16x32_bf16 a[96:99], v[136:139], v[200:203], a[96:99]
ds_read_b64_tr_b16 v[48:49], v240 offset:50816
v_add_f32 v124, v96, v97
v_mfma_f32_16x16x32_bf16 a[160:163], v[136:139], v[204:207], a[160:163]
v_fmac_f32 v122, v96, v96
v_add_f32 v120, v120, v124
v_mfma_f32_16x16x32_bf16 a[104:107], v[136:139], v[208:211], a[104:107]
ds_read_b64_tr_b16 v[50:51], v240 offset:51840
v_fmac_f32 v123, v97, v97
s_add_u32 m0, s83, 34816
v_mfma_f32_16x16x32_bf16 a[168:171], v[136:139], v[212:215], a[168:171]
buffer_load_dwordx4 v233, s[44:47], s80 offen lds
v_mfma_f32_16x16x32_bf16 a[112:115], v[136:139], v[216:219], a[112:115]
ds_read_b64_tr_b16 v[52:53], v241 offset:50816
v_lshlrev_b32 v98, 16, v77
v_mfma_f32_16x16x32_bf16 a[176:179], v[136:139], v[220:223], a[176:179]
v_and_b32 v99, 0xffff0000, v77
v_sub_f32 v98, v98, v70
v_mfma_f32_16x16x32_bf16 a[204:207], v[140:143], v[192:195], a[204:207]
ds_read_b64_tr_b16 v[54:55], v241 offset:51840
v_sub_f32 v99, v99, v70
s_add_u32 m0, s83, 35968
v_mfma_f32_16x16x32_bf16 a[156:159], v[140:143], v[196:199], a[156:159]
buffer_load_dwordx4 v234, s[44:47], s80 offen lds
v_mfma_f32_16x16x32_bf16 a[100:103], v[140:143], v[200:203], a[100:103]
ds_read_b64_tr_b16 v[56:57], v242 offset:50816
v_add_f32 v124, v98, v99
v_mfma_f32_16x16x32_bf16 a[164:167], v[140:143], v[204:207], a[164:167]
v_fmac_f32 v122, v98, v98
v_add_f32 v120, v120, v124
v_mfma_f32_16x16x32_bf16 a[108:111], v[140:143], v[208:211], a[108:111]
ds_read_b64_tr_b16 v[58:59], v242 offset:51840
v_fmac_f32 v123, v99, v99
s_add_u32 m0, s83, 36992
v_mfma_f32_16x16x32_bf16 a[172:175], v[140:143], v[212:215], a[172:175]
buffer_load_dwordx4 v235, s[44:47], s80 offen lds
v_mfma_f32_16x16x32_bf16 a[116:119], v[140:143], v[216:219], a[116:119]
ds_read_b64_tr_b16 v[60:61], v243 offset:50816
v_lshlrev_b32 v100, 16, v78
v_mfma_f32_16x16x32_bf16 a[180:183], v[140:143], v[220:223], a[180:183]
s_add_u32 s80, s80, s81
s_and_b32 s80, s80, 4194303
v_mfma_f32_16x16x32_bf16 a[120:123], v[144:147], v[192:195], a[120:123]
ds_read_b64_tr_b16 v[62:63], v243 offset:51840
v_and_b32 v101, 0xffff0000, v78
v_mfma_f32_16x16x32_bf16 a[184:187], v[144:147], v[196:199], a[184:187]
buffer_load_dword v254, v164, s[44:47], s86 offen
v_sub_f32 v100, v100, v70
v_mfma_f32_16x16x32_bf16 a[64:67], v[144:147], v[200:203], a[64:67]
ds_read_b128 v[4:7], v237 offset:59392
v_sub_f32 v101, v101, v70
v_mfma_f32_16x16x32_bf16 a[0:3], v[144:147], v[204:207], a[0:3]
s_xor_b32 s82, s82, 32768
s_add_u32 s83, s83, 50688
v_mfma_f32_16x16x32_bf16 a[72:75], v[144:147], v[208:211], a[72:75]
ds_read_b128 v[8:11], v237 offset:49152
s_sub_u32 s86, s86, 2097152
v_mfma_f32_16x16x32_bf16 a[8:11], v[144:147], v[212:215], a[8:11]
v_add_f32 v124, v100, v101
v_fmac_f32 v122, v100, v100
v_mfma_f32_16x16x32_bf16 a[80:83], v[144:147], v[216:219], a[80:83]
ds_read_b128 v[12:15], v237 offset:51200
v_add_f32 v120, v120, v124
v_mfma_f32_16x16x32_bf16 a[16:19], v[144:147], v[220:223], a[16:19]
v_fmac_f32 v123, v101, v101
v_lshlrev_b32 v102, 16, v79
v_mfma_f32_16x16x32_bf16 a[124:127], v[148:151], v[192:195], a[124:127]
ds_read_b128 v[16:19], v237 offset:40960
v_and_b32 v103, 0xffff0000, v79
v_mfma_f32_16x16x32_bf16 a[188:191], v[148:151], v[196:199], a[188:191]
v_sub_f32 v102, v102, v70
v_sub_f32 v103, v103, v70
v_mfma_f32_16x16x32_bf16 a[68:71], v[148:151], v[200:203], a[68:71]
ds_read_b128 v[20:23], v237 offset:43008
v_add_f32 v124, v102, v103
v_mfma_f32_16x16x32_bf16 a[4:7], v[148:151], v[204:207], a[4:7]
v_fmac_f32 v122, v102, v102
v_add_f32 v120, v120, v124
v_mfma_f32_16x16x32_bf16 a[76:79], v[148:151], v[208:211], a[76:79]
ds_read_b128 v[24:27], v237 offset:32768
v_fmac_f32 v123, v103, v103
v_mfma_f32_16x16x32_bf16 a[12:15], v[148:151], v[212:215], a[12:15]
v_lshlrev_b32 v104, 16, v80
v_and_b32 v105, 0xffff0000, v80
v_mfma_f32_16x16x32_bf16 a[84:87], v[148:151], v[216:219], a[84:87]
ds_read_b128 v[28:31], v237 offset:34816
v_sub_f32 v104, v104, v70
v_mfma_f32_16x16x32_bf16 a[20:23], v[148:151], v[220:223], a[20:23]
v_sub_f32 v105, v105, v70
v_add_f32 v124, v104, v105
v_mfma_f32_16x16x32_bf16 a[88:91], v[152:155], v[192:195], a[88:91]
v_fmac_f32 v122, v104, v104
v_add_f32 v120, v120, v124
v_mfma_f32_16x16x32_bf16 a[24:27], v[152:155], v[196:199], a[24:27]
v_fmac_f32 v123, v105, v105
v_lshlrev_b32 v106, 16, v81
v_mfma_f32_16x16x32_bf16 a[240:243], v[152:155], v[200:203], a[240:243]
v_and_b32 v107, 0xffff0000, v81
v_sub_f32 v106, v106, v70
v_mfma_f32_16x16x32_bf16 a[32:35], v[152:155], v[204:207], a[32:35]
v_sub_f32 v107, v107, v70
v_add_f32 v124, v106, v107
v_mfma_f32_16x16x32_bf16 a[248:251], v[152:155], v[208:211], a[248:251]
v_fmac_f32 v122, v106, v106
v_add_f32 v120, v120, v124
v_mfma_f32_16x16x32_bf16 a[40:43], v[152:155], v[212:215], a[40:43]
v_fmac_f32 v123, v107, v107
v_lshlrev_b32 v108, 16, v82
v_mfma_f32_16x16x32_bf16 a[48:51], v[152:155], v[216:219], a[48:51]
v_and_b32 v109, 0xffff0000, v82
v_sub_f32 v108, v108, v70
v_mfma_f32_16x16x32_bf16 a[56:59], v[152:155], v[220:223], a[56:59]
v_sub_f32 v109, v109, v70
v_add_f32 v124, v108, v109
v_mfma_f32_16x16x32_bf16 a[92:95], v[156:159], v[192:195], a[92:95]
v_fmac_f32 v122, v108, v108
v_add_f32 v120, v120, v124
v_mfma_f32_16x16x32_bf16 a[28:31], v[156:159], v[196:199], a[28:31]
v_fmac_f32 v123, v109, v109
v_lshlrev_b32 v110, 16, v83
v_mfma_f32_16x16x32_bf16 a[244:247], v[156:159], v[200:203], a[244:247]
v_and_b32 v111, 0xffff0000, v83
v_sub_f32 v110, v110, v70
v_mfma_f32_16x16x32_bf16 a[36:39], v[156:159], v[204:207], a[36:39]
v_sub_f32 v111, v111, v70
v_add_f32 v124, v110, v111
v_mfma_f32_16x16x32_bf16 a[252:255], v[156:159], v[208:211], a[252:255]
v_fmac_f32 v122, v110, v110
v_add_f32 v120, v120, v124
v_mfma_f32_16x16x32_bf16 a[44:47], v[156:159], v[212:215], a[44:47]
v_fmac_f32 v123, v111, v111
v_lshlrev_b32 v112, 16, v84
v_mfma_f32_16x16x32_bf16 a[52:55], v[156:159], v[216:219], a[52:55]
v_and_b32 v113, 0xffff0000, v84
v_sub_f32 v112, v112, v70
v_mfma_f32_16x16x32_bf16 a[60:63], v[156:159], v[220:223], a[60:63]
v_sub_f32 v113, v113, v70
v_add_f32 v124, v112, v113
s_waitcnt lgkmcnt(0)
s_waitcnt vmcnt(9)
s_barrier
v_mfma_f32_16x16x32_bf16 a[224:227], v[0:3], v[32:35], a[224:227]
ds_read_b128 v[128:131], v236 offset:24576
s_xor_b32 m0, s82, 0
v_mfma_f32_16x16x32_bf16 a[232:235], v[0:3], v[36:39], a[232:235]
buffer_load_dwordx4 v224, s[40:43], s79 offen lds
v_mfma_f32_16x16x32_bf16 a[208:211], v[0:3], v[40:43], a[208:211]
ds_read_b64_tr_b16 v[192:193], v240 offset:0
v_fmac_f32 v122, v112, v112
v_mfma_f32_16x16x32_bf16 a[128:131], v[0:3], v[44:47], a[128:131]
v_add_f32 v120, v120, v124
v_fmac_f32 v123, v113, v113
v_mfma_f32_16x16x32_bf16 a[216:219], v[0:3], v[48:51], a[216:219]
ds_read_b64_tr_b16 v[194:195], v240 offset:1024
v_lshlrev_b32 v114, 16, v85
s_xor_b32 m0, s82, 1024
v_mfma_f32_16x16x32_bf16 a[136:139], v[0:3], v[52:55], a[136:139]
buffer_load_dwordx4 v225, s[40:43], s79 offen lds
v_mfma_f32_16x16x32_bf16 a[192:195], v[0:3], v[56:59], a[192:195]
ds_read_b64_tr_b16 v[196:197], v241 offset:0
v_and_b32 v115, 0xffff0000, v85
v_mfma_f32_16x16x32_bf16 a[144:147], v[0:3], v[60:63], a[144:147]
v_sub_f32 v114, v114, v70
v_sub_f32 v115, v115, v70
v_mfma_f32_16x16x32_bf16 a[228:231], v[4:7], v[32:35], a[228:231]
ds_read_b64_tr_b16 v[198:199], v241 offset:1024
v_add_f32 v124, v114, v115
s_xor_b32 m0, s82, 2048
v_mfma_f32_16x16x32_bf16 a[236:239], v[4:7], v[36:39], a[236:239]
buffer_load_dwordx4 v226, s[40:43], s79 offen lds
v_mfma_f32_16x16x32_bf16 a[212:215], v[4:7], v[40:43], a[212:215]
ds_read_b64_tr_b16 v[200:201], v242 offset:0
v_fmac_f32 v122, v114, v114
v_mfma_f32_16x16x32_bf16 a[132:135], v[4:7], v[44:47], a[132:135]
v_add_f32 v120, v120, v124
v_fmac_f32 v123, v115, v115
v_mfma_f32_16x16x32_bf16 a[220:223], v[4:7], v[48:51], a[220:223]
ds_read_b64_tr_b16 v[202:203], v242 offset:1024
v_lshlrev_b32 v116, 16, v86
s_xor_b32 m0, s82, 3072
v_mfma_f32_16x16x32_bf16 a[140:143], v[4:7], v[52:55], a[140:143]
buffer_load_dwordx4 v227, s[40:43], s79 offen lds
v_mfma_f32_16x16x32_bf16 a[196:199], v[4:7], v[56:59], a[196:199]
ds_read_b64_tr_b16 v[204:205], v243 offset:0
v_and_b32 v117, 0xffff0000, v86
v_mfma_f32_16x16x32_bf16 a[148:151], v[4:7], v[60:63], a[148:151]
v_sub_f32 v116, v116, v70
v_sub_f32 v117, v117, v70
v_mfma_f32_16x16x32_bf16 a[200:203], v[8:11], v[32:35], a[200:203]
ds_read_b64_tr_b16 v[206:207], v243 offset:1024
v_add_f32 v124, v116, v117
s_xor_b32 m0, s82, 4096
v_mfma_f32_16x16x32_bf16 a[152:155], v[8:11], v[36:39], a[152:155]
buffer_load_dwordx4 v228, s[40:43], s79 offen lds
v_mfma_f32_16x16x32_bf16 a[96:99], v[8:11], v[40:43], a[96:99]
ds_read_b64_tr_b16 v[208:209], v240 offset:128
v_fmac_f32 v122, v116, v116
v_mfma_f32_16x16x32_bf16 a[160:163], v[8:11], v[44:47], a[160:163]
v_add_f32 v120, v120, v124
v_fmac_f32 v123, v117, v117
v_mfma_f32_16x16x32_bf16 a[104:107], v[8:11], v[48:51], a[104:107]
ds_read_b64_tr_b16 v[210:211], v240 offset:1152
v_lshlrev_b32 v118, 16, v87
s_xor_b32 m0, s82, 5120
v_mfma_f32_16x16x32_bf16 a[168:171], v[8:11], v[52:55], a[168:171]
buffer_load_dwordx4 v229, s[40:43], s79 offen lds
v_mfma_f32_16x16x32_bf16 a[112:115], v[8:11], v[56:59], a[112:115]
ds_read_b64_tr_b16 v[212:213], v241 offset:128
v_and_b32 v119, 0xffff0000, v87
v_mfma_f32_16x16x32_bf16 a[176:179], v[8:11], v[60:63], a[176:179]
v_sub_f32 v118, v118, v70
v_sub_f32 v119, v119, v70
v_mfma_f32_16x16x32_bf16 a[204:207], v[12:15], v[32:35], a[204:207]
ds_read_b64_tr_b16 v[214:215], v241 offset:1152
v_add_f32 v124, v118, v119
s_xor_b32 m0, s82, 6144
v_mfma_f32_16x16x32_bf16 a[156:159], v[12:15], v[36:39], a[156:159]
buffer_load_dwordx4 v230, s[40:43], s79 offen lds
v_mfma_f32_16x16x32_bf16 a[100:103], v[12:15], v[40:43], a[100:103]
ds_read_b64_tr_b16 v[216:217], v242 offset:128
v_fmac_f32 v122, v118, v118
v_mfma_f32_16x16x32_bf16 a[164:167], v[12:15], v[44:47], a[164:167]
v_add_f32 v120, v120, v124
v_fmac_f32 v123, v119, v119
v_mfma_f32_16x16x32_bf16 a[108:111], v[12:15], v[48:51], a[108:111]
ds_read_b64_tr_b16 v[218:219], v242 offset:1152
v_add_f32 v121, v122, v123
s_xor_b32 m0, s82, 7168
v_mfma_f32_16x16x32_bf16 a[172:175], v[12:15], v[52:55], a[172:175]
buffer_load_dwordx4 v231, s[40:43], s79 offen lds
v_mfma_f32_16x16x32_bf16 a[116:119], v[12:15], v[56:59], a[116:119]
ds_read_b64_tr_b16 v[220:221], v243 offset:128
v_add_f32_dpp v120, v120, v120 row_ror:8 row_mask:0xf bank_mask:0xf
v_mfma_f32_16x16x32_bf16 a[180:183], v[12:15], v[60:63], a[180:183]
s_add_u32 s79, s79, 128
s_and_b32 s79, s79, 1023
s_xor_b32 s82, s82, 32768
v_mfma_f32_16x16x32_bf16 a[120:123], v[16:19], v[32:35], a[120:123]
ds_read_b64_tr_b16 v[222:223], v243 offset:1152
v_add_f32_dpp v121, v121, v121 row_ror:8 row_mask:0xf bank_mask:0xf
s_add_u32 m0, s83, 0
v_mfma_f32_16x16x32_bf16 a[184:187], v[16:19], v[36:39], a[184:187]
buffer_load_dwordx4 v232, s[44:47], s80 offen lds
v_mfma_f32_16x16x32_bf16 a[64:67], v[16:19], v[40:43], a[64:67]
ds_read_b128 v[132:135], v236 offset:26624
v_mov_b32 v124, v120
v_mfma_f32_16x16x32_bf16 a[0:3], v[16:19], v[44:47], a[0:3]
v_mov_b32 v125, v121
v_mfma_f32_16x16x32_bf16 a[72:75], v[16:19], v[48:51], a[72:75]
ds_read_b128 v[136:139], v236 offset:16384
s_add_u32 m0, s83, 1024
v_mfma_f32_16x16x32_bf16 a[8:11], v[16:19], v[52:55], a[8:11]
buffer_load_dwordx4 v233, s[44:47], s80 offen lds
v_mfma_f32_16x16x32_bf16 a[80:83], v[16:19], v[56:59], a[80:83]
ds_read_b128 v[140:143], v236 offset:18432
v_permlane16_swap_b32 v124, v120
v_mfma_f32_16x16x32_bf16 a[16:19], v[16:19], v[60:63], a[16:19]
v_permlane16_swap_b32 v125, v121
v_mfma_f32_16x16x32_bf16 a[124:127], v[20:23], v[32:35], a[124:127]
ds_read_b128 v[144:147], v236 offset:8192
v_add_f32 v120, v120, v124
s_add_u32 m0, s83, 2176
v_mfma_f32_16x16x32_bf16 a[188:191], v[20:23], v[36:39], a[188:191]
buffer_load_dwordx4 v234, s[44:47], s80 offen lds
v_mfma_f32_16x16x32_bf16 a[68:71], v[20:23], v[40:43], a[68:71]
ds_read_b128 v[148:151], v236 offset:10240
v_add_f32 v121, v121, v125
v_mfma_f32_16x16x32_bf16 a[4:7], v[20:23], v[44:47], a[4:7]
v_mov_b32 v124, v120
v_mov_b32 v125, v121
v_mfma_f32_16x16x32_bf16 a[76:79], v[20:23], v[48:51], a[76:79]
ds_read_b128 v[152:155], v236 offset:0
s_add_u32 m0, s83, 3200
v_mfma_f32_16x16x32_bf16 a[12:15], v[20:23], v[52:55], a[12:15]
buffer_load_dwordx4 v235, s[44:47], s80 offen lds
v_mfma_f32_16x16x32_bf16 a[84:87], v[20:23], v[56:59], a[84:87]
ds_read_b128 v[156:159], v236 offset:2048
v_permlane32_swap_b32 v124, v120
v_mfma_f32_16x16x32_bf16 a[20:23], v[20:23], v[60:63], a[20:23]
s_add_u32 s80, s80, s81
s_and_b32 s80, s80, 4194303
s_add_u32 s83, s83, 16896
v_mfma_f32_16x16x32_bf16 a[88:91], v[24:27], v[32:35], a[88:91]
v_permlane32_swap_b32 v125, v121
v_mfma_f32_16x16x32_bf16 a[24:27], v[24:27], v[36:39], a[24:27]
s_cmp_eq_u32 s83, s97
s_cselect_b32 s83, s90, s83
v_mfma_f32_16x16x32_bf16 a[240:243], v[24:27], v[40:43], a[240:243]
v_add_f32 v120, v120, v124
v_add_f32 v121, v121, v125
v_mfma_f32_16x16x32_bf16 a[32:35], v[24:27], v[44:47], a[32:35]
ds_write_b64 v127, v[120:121]
s_nop 0
v_mfma_f32_16x16x32_bf16 a[248:251], v[24:27], v[48:51], a[248:251]
v_readfirstlane_b32 s92, v252
v_readfirstlane_b32 s93, v253
v_mfma_f32_16x16x32_bf16 a[40:43], v[24:27], v[52:55], a[40:43]
s_add_u32 s92, s92, 1
s_addc_u32 s93, s93, 0
v_mfma_f32_16x16x32_bf16 a[48:51], v[24:27], v[56:59], a[48:51]
v_mov_b32 v252, s92
v_mov_b32 v253, s93
v_mfma_f32_16x16x32_bf16 a[56:59], v[24:27], v[60:63], a[56:59]
v_mov_b32 v160, 0
v_mov_b32 v161, 0
v_mfma_f32_16x16x32_bf16 a[92:95], v[28:31], v[32:35], a[92:95]
v_mov_b32 v162, 0
v_mov_b32 v163, 0
v_mfma_f32_16x16x32_bf16 a[28:31], v[28:31], v[36:39], a[28:31]
buffer_store_dwordx4 v[160:163], v255, s[52:55], s95 offen sc1
v_mfma_f32_16x16x32_bf16 a[244:247], v[28:31], v[40:43], a[244:247]
v_mfma_f32_16x16x32_bf16 a[36:39], v[28:31], v[44:47], a[36:39]
v_mfma_f32_16x16x32_bf16 a[252:255], v[28:31], v[48:51], a[252:255]
v_mfma_f32_16x16x32_bf16 a[44:47], v[28:31], v[52:55], a[44:47]
v_mfma_f32_16x16x32_bf16 a[52:55], v[28:31], v[56:59], a[52:55]
v_mfma_f32_16x16x32_bf16 a[60:63], v[28:31], v[60:63], a[60:63]
s_waitcnt lgkmcnt(0)
s_waitcnt vmcnt(18)
s_barrier
v_mfma_f32_16x16x32_bf16 a[224:227], v[128:131], v[192:195], a[224:227]
ds_read_b128 v[0:3], v237 offset:24576
ds_read_b128 v[72:75], v126
v_mfma_f32_16x16x32_bf16 a[232:235], v[128:131], v[196:199], a[232:235]
ds_read_b128 v[76:79], v126 offset:16
v_mfma_f32_16x16x32_bf16 a[208:211], v[128:131], v[200:203], a[208:211]
ds_read_b64_tr_b16 v[32:33], v240 offset:16896
v_mfma_f32_16x16x32_bf16 a[128:131], v[128:131], v[204:207], a[128:131]
v_mfma_f32_16x16x32_bf16 a[216:219], v[128:131], v[208:211], a[216:219]
ds_read_b64_tr_b16 v[34:35], v240 offset:17920
s_waitcnt lgkmcnt(2)
v_mfma_f32_16x16x32_bf16 a[136:139], v[128:131], v[212:215], a[136:139]
v_add_f32 v72, v72, v74
v_add_f32 v73, v73, v75
v_mfma_f32_16x16x32_bf16 a[192:195], v[128:131], v[216:219], a[192:195]
ds_read_b64_tr_b16 v[36:37], v241 offset:16896
v_add_f32 v76, v76, v78
v_mfma_f32_16x16x32_bf16 a[144:147], v[128:131], v[220:223], a[144:147]
v_add_f32 v77, v77, v79
v_add_f32 v72, v72, v76
v_mfma_f32_16x16x32_bf16 a[228:231], v[132:135], v[192:195], a[228:231]
ds_read_b64_tr_b16 v[38:39], v241 offset:17920
v_add_f32 v73, v73, v77
v_mfma_f32_16x16x32_bf16 a[236:239], v[132:135], v[196:199], a[236:239]
v_mul_f32 v72, 0x3a800000, v72
v_mul_f32 v73, 0x3a800000, v73
v_mfma_f32_16x16x32_bf16 a[212:215], v[132:135], v[200:203], a[212:215]
ds_read_b64_tr_b16 v[40:41], v242 offset:16896
v_add_f32 v124, v70, v72
v_mfma_f32_16x16x32_bf16 a[132:135], v[132:135], v[204:207], a[132:135]
v_fma_f32 v125, -v72, v72, v73
v_cvt_pk_bf16_f32 v124, v124, v124
v_mfma_f32_16x16x32_bf16 a[220:223], v[132:135], v[208:211], a[220:223]
ds_read_b64_tr_b16 v[42:43], v242 offset:17920
v_lshlrev_b32 v124, 16, v124
v_mfma_f32_16x16x32_bf16 a[140:143], v[132:135], v[212:215], a[140:143]
v_max_f32 v125, 0, v125
v_cvt_pk_bf16_f32 v125, v125, v125
v_mfma_f32_16x16x32_bf16 a[196:199], v[132:135], v[216:219], a[196:199]
ds_read_b64_tr_b16 v[44:45], v243 offset:16896
v_lshlrev_b32 v125, 16, v125
v_mfma_f32_16x16x32_bf16 a[148:151], v[132:135], v[220:223], a[148:151]
v_add_f32 v125, 0x3727c5ac, v125
v_cvt_pk_bf16_f32 v125, v125, v125
v_mfma_f32_16x16x32_bf16 a[200:203], v[136:139], v[192:195], a[200:203]
ds_read_b64_tr_b16 v[46:47], v243 offset:17920
v_lshlrev_b32 v125, 16, v125
v_mfma_f32_16x16x32_bf16 a[152:155], v[136:139], v[196:199], a[152:155]
v_sqrt_f32 v125, v125
v_mfma_f32_16x16x32_bf16 a[96:99], v[136:139], v[200:203], a[96:99]
ds_read_b64_tr_b16 v[48:49], v240 offset:17024
v_sub_f32 v124, v70, v124
v_mfma_f32_16x16x32_bf16 a[160:163], v[136:139], v[204:207], a[160:163]
v_cvt_pk_bf16_f32 v125, v125, v125
v_lshlrev_b32 v125, 16, v125
v_mfma_f32_16x16x32_bf16 a[104:107], v[136:139], v[208:211], a[104:107]
ds_read_b64_tr_b16 v[50:51], v240 offset:18048
v_mfma_f32_16x16x32_bf16 a[168:171], v[136:139], v[212:215], a[168:171]
v_rcp_f32 v125, v125
v_mfma_f32_16x16x32_bf16 a[112:115], v[136:139], v[216:219], a[112:115]
ds_read_b64_tr_b16 v[52:53], v241 offset:17024
v_mul_f32 v125, 0x3e0293ee, v125
v_mfma_f32_16x16x32_bf16 a[176:179], v[136:139], v[220:223], a[176:179]
v_mul_f32 v124, v124, v125
v_fma_f32 v88, v88, v125, v124
v_mfma_f32_16x16x32_bf16 a[204:207], v[140:143], v[192:195], a[204:207]
ds_read_b64_tr_b16 v[54:55], v241 offset:18048
v_fma_f32 v89, v89, v125, v124
v_mfma_f32_16x16x32_bf16 a[156:159], v[140:143], v[196:199], a[156:159]
v_cvt_pk_bf16_f32 v72, v88, v89
v_fma_f32 v90, v90, v125, v124
v_mfma_f32_16x16x32_bf16 a[100:103], v[140:143], v[200:203], a[100:103]
ds_read_b64_tr_b16 v[56:57], v242 offset:17024
v_fma_f32 v91, v91, v125, v124
.La5_LW1_4:
s_cbranch_execz .La5_LW1_8
v_mfma_f32_16x16x32_bf16 a[164:167], v[140:143], v[204:207], a[164:167]
v_cvt_pk_bf16_f32 v73, v90, v91
v_fma_f32 v92, v92, v125, v124
v_mfma_f32_16x16x32_bf16 a[108:111], v[140:143], v[208:211], a[108:111]
ds_read_b64_tr_b16 v[58:59], v242 offset:18048
v_fma_f32 v93, v93, v125, v124
v_mfma_f32_16x16x32_bf16 a[172:175], v[140:143], v[212:215], a[172:175]
v_cvt_pk_bf16_f32 v74, v92, v93
v_fma_f32 v94, v94, v125, v124
v_mfma_f32_16x16x32_bf16 a[116:119], v[140:143], v[216:219], a[116:119]
ds_read_b64_tr_b16 v[60:61], v243 offset:17024
v_fma_f32 v95, v95, v125, v124
v_mfma_f32_16x16x32_bf16 a[180:183], v[140:143], v[220:223], a[180:183]
v_cvt_pk_bf16_f32 v75, v94, v95
buffer_store_dwordx4 v[72:75], v71, s[56:59], 0 offen offset:0 sc1
v_mfma_f32_16x16x32_bf16 a[120:123], v[144:147], v[192:195], a[120:123]
ds_read_b64_tr_b16 v[62:63], v243 offset:18048
v_fma_f32 v96, v96, v125, v124
s_add_u32 m0, s83, 0
v_mfma_f32_16x16x32_bf16 a[184:187], v[144:147], v[196:199], a[184:187]
buffer_load_dwordx4 v232, s[44:47], s80 offen lds
v_mfma_f32_16x16x32_bf16 a[64:67], v[144:147], v[200:203], a[64:67]
ds_read_b128 v[4:7], v237 offset:26624
v_fma_f32 v97, v97, v125, v124
v_mfma_f32_16x16x32_bf16 a[0:3], v[144:147], v[204:207], a[0:3]
v_cvt_pk_bf16_f32 v76, v96, v97
v_fma_f32 v98, v98, v125, v124
v_mfma_f32_16x16x32_bf16 a[72:75], v[144:147], v[208:211], a[72:75]
ds_read_b128 v[8:11], v237 offset:16384
v_fma_f32 v99, v99, v125, v124
s_add_u32 m0, s83, 1024
v_mfma_f32_16x16x32_bf16 a[8:11], v[144:147], v[212:215], a[8:11]
buffer_load_dwordx4 v233, s[44:47], s80 offen lds
v_mfma_f32_16x16x32_bf16 a[80:83], v[144:147], v[216:219], a[80:83]
ds_read_b128 v[12:15], v237 offset:18432
v_cvt_pk_bf16_f32 v77, v98, v99
v_mfma_f32_16x16x32_bf16 a[16:19], v[144:147], v[220:223], a[16:19]
v_fma_f32 v100, v100, v125, v124
v_fma_f32 v101, v101, v125, v124
v_mfma_f32_16x16x32_bf16 a[124:127], v[148:151], v[192:195], a[124:127]
ds_read_b128 v[16:19], v237 offset:8192
v_cvt_pk_bf16_f32 v78, v100, v101
v_fma_f32 v102, v102, v125, v124
s_add_u32 m0, s83, 2176
v_mfma_f32_16x16x32_bf16 a[188:191], v[148:151], v[196:199], a[188:191]
buffer_load_dwordx4 v234, s[44:47], s80 offen lds
v_fma_f32 v103, v103, v125, v124
v_mfma_f32_16x16x32_bf16 a[68:71], v[148:151], v[200:203], a[68:71]
ds_read_b128 v[20:23], v237 offset:10240
v_cvt_pk_bf16_f32 v79, v102, v103
buffer_store_dwordx4 v[76:79], v71, s[56:59], 0 offen offset:2048 sc1
v_mfma_f32_16x16x32_bf16 a[4:7], v[148:151], v[204:207], a[4:7]
v_fma_f32 v104, v104, v125, v124
v_fma_f32 v105, v105, v125, v124
v_cvt_pk_bf16_f32 v80, v104, v105
v_mfma_f32_16x16x32_bf16 a[76:79], v[148:151], v[208:211], a[76:79]
ds_read_b128 v[24:27], v237 offset:0
v_fma_f32 v106, v106, v125, v124
v_fma_f32 v107, v107, v125, v124
s_add_u32 m0, s83, 3200
v_mfma_f32_16x16x32_bf16 a[12:15], v[148:151], v[212:215], a[12:15]
buffer_load_dwordx4 v235, s[44:47], s80 offen lds
v_cvt_pk_bf16_f32 v81, v106, v107
v_mfma_f32_16x16x32_bf16 a[84:87], v[148:151], v[216:219], a[84:87]
ds_read_b128 v[28:31], v237 offset:2048
v_fma_f32 v108, v108, v125, v124
v_fma_f32 v109, v109, v125, v124
v_mfma_f32_16x16x32_bf16 a[20:23], v[148:151], v[220:223], a[20:23]
s_add_u32 s80, s80, s81
s_and_b32 s80, s80, 4194303
s_add_u32 s83, s83, 16896
v_mfma_f32_16x16x32_bf16 a[88:91], v[152:155], v[192:195], a[88:91]
v_cvt_pk_bf16_f32 v82, v108, v109
v_fma_f32 v110, v110, v125, v124
v_fma_f32 v111, v111, v125, v124
v_mfma_f32_16x16x32_bf16 a[24:27], v[152:155], v[196:199], a[24:27]
s_cmp_eq_u32 s83, s97
s_cselect_b32 s83, s90, s83
v_cvt_pk_bf16_f32 v83, v110, v111
v_mfma_f32_16x16x32_bf16 a[240:243], v[152:155], v[200:203], a[240:243]
buffer_store_dwordx4 v[80:83], v71, s[56:59], s88 offen offset:0 sc1
v_fma_f32 v112, v112, v125, v124
v_fma_f32 v113, v113, v125, v124
v_mfma_f32_16x16x32_bf16 a[32:35], v[152:155], v[204:207], a[32:35]
v_cvt_pk_bf16_f32 v84, v112, v113
v_fma_f32 v114, v114, v125, v124
v_fma_f32 v115, v115, v125, v124
v_mfma_f32_16x16x32_bf16 a[248:251], v[152:155], v[208:211], a[248:251]
v_cvt_pk_bf16_f32 v85, v114, v115
v_fma_f32 v116, v116, v125, v124
v_fma_f32 v117, v117, v125, v124
v_mfma_f32_16x16x32_bf16 a[40:43], v[152:155], v[212:215], a[40:43]
v_cvt_pk_bf16_f32 v86, v116, v117
v_fma_f32 v118, v118, v125, v124
v_fma_f32 v119, v119, v125, v124
v_mfma_f32_16x16x32_bf16 a[48:51], v[152:155], v[216:219], a[48:51]
v_cvt_pk_bf16_f32 v87, v118, v119
buffer_store_dwordx4 v[84:87], v71, s[56:59], s88 offen offset:2048 sc1
v_mfma_f32_16x16x32_bf16 a[56:59], v[152:155], v[220:223], a[56:59]
v_mfma_f32_16x16x32_bf16 a[92:95], v[156:159], v[192:195], a[92:95]
v_mfma_f32_16x16x32_bf16 a[28:31], v[156:159], v[196:199], a[28:31]
v_mfma_f32_16x16x32_bf16 a[244:247], v[156:159], v[200:203], a[244:247]
v_add_u32 v240, s84, v240
v_add_u32 v241, s84, v241
v_mfma_f32_16x16x32_bf16 a[36:39], v[156:159], v[204:207], a[36:39]
v_add_u32 v242, s84, v242
v_add_u32 v243, s84, v243
v_mfma_f32_16x16x32_bf16 a[252:255], v[156:159], v[208:211], a[252:255]
v_xor_b32 v236, 0x8000, v236
v_xor_b32 v237, 0x8000, v237
v_mfma_f32_16x16x32_bf16 a[44:47], v[156:159], v[212:215], a[44:47]
s_sub_u32 s84, 0, s84
v_mfma_f32_16x16x32_bf16 a[52:55], v[156:159], v[216:219], a[52:55]
v_mfma_f32_16x16x32_bf16 a[60:63], v[156:159], v[220:223], a[60:63]
s_waitcnt lgkmcnt(0)
.p2align 6
.La5_LV_LOOP:
s_waitcnt vmcnt(12)
s_barrier
v_mfma_f32_16x16x32_bf16 a[224:227], v[0:3], v[32:35], a[224:227]
ds_read_b128 v[128:131], v236 offset:24576
s_xor_b32 m0, s82, 0
v_mfma_f32_16x16x32_bf16 a[232:235], v[0:3], v[36:39], a[232:235]
buffer_load_dwordx4 v224, s[40:43], s79 offen lds
v_mfma_f32_16x16x32_bf16 a[208:211], v[0:3], v[40:43], a[208:211]
ds_read_b64_tr_b16 v[192:193], v240 offset:0
v_mfma_f32_16x16x32_bf16 a[128:131], v[0:3], v[44:47], a[128:131]
v_mfma_f32_16x16x32_bf16 a[216:219], v[0:3], v[48:51], a[216:219]
ds_read_b64_tr_b16 v[194:195], v240 offset:1024
s_xor_b32 m0, s82, 1024
v_mfma_f32_16x16x32_bf16 a[136:139], v[0:3], v[52:55], a[136:139]
buffer_load_dwordx4 v225, s[40:43], s79 offen lds
v_mfma_f32_16x16x32_bf16 a[192:195], v[0:3], v[56:59], a[192:195]
ds_read_b64_tr_b16 v[196:197], v241 offset:0
v_mfma_f32_16x16x32_bf16 a[144:147], v[0:3], v[60:63], a[144:147]
v_mfma_f32_16x16x32_bf16 a[228:231], v[4:7], v[32:35], a[228:231]
ds_read_b64_tr_b16 v[198:199], v241 offset:1024
s_xor_b32 m0, s82, 2048
v_mfma_f32_16x16x32_bf16 a[236:239], v[4:7], v[36:39], a[236:239]
buffer_load_dwordx4 v226, s[40:43], s79 offen lds
v_mfma_f32_16x16x32_bf16 a[212:215], v[4:7], v[40:43], a[212:215]
ds_read_b64_tr_b16 v[200:201], v242 offset:0
v_mfma_f32_16x16x32_bf16 a[132:135], v[4:7], v[44:47], a[132:135]
v_mfma_f32_16x16x32_bf16 a[220:223], v[4:7], v[48:51], a[220:223]
ds_read_b64_tr_b16 v[202:203], v242 offset:1024
s_xor_b32 m0, s82, 3072
v_mfma_f32_16x16x32_bf16 a[140:143], v[4:7], v[52:55], a[140:143]
buffer_load_dwordx4 v227, s[40:43], s79 offen lds
v_mfma_f32_16x16x32_bf16 a[196:199], v[4:7], v[56:59], a[196:199]
ds_read_b64_tr_b16 v[204:205], v243 offset:0
v_mfma_f32_16x16x32_bf16 a[148:151], v[4:7], v[60:63], a[148:151]
v_mfma_f32_16x16x32_bf16 a[200:203], v[8:11], v[32:35], a[200:203]
ds_read_b64_tr_b16 v[206:207], v243 offset:1024
s_xor_b32 m0, s82, 4096
v_mfma_f32_16x16x32_bf16 a[152:155], v[8:11], v[36:39], a[152:155]
buffer_load_dwordx4 v228, s[40:43], s79 offen lds
v_mfma_f32_16x16x32_bf16 a[96:99], v[8:11], v[40:43], a[96:99]
ds_read_b64_tr_b16 v[208:209], v240 offset:128
v_mfma_f32_16x16x32_bf16 a[160:163], v[8:11], v[44:47], a[160:163]
v_mfma_f32_16x16x32_bf16 a[104:107], v[8:11], v[48:51], a[104:107]
ds_read_b64_tr_b16 v[210:211], v240 offset:1152
s_xor_b32 m0, s82, 5120
v_mfma_f32_16x16x32_bf16 a[168:171], v[8:11], v[52:55], a[168:171]
buffer_load_dwordx4 v229, s[40:43], s79 offen lds
v_mfma_f32_16x16x32_bf16 a[112:115], v[8:11], v[56:59], a[112:115]
ds_read_b64_tr_b16 v[212:213], v241 offset:128
v_mfma_f32_16x16x32_bf16 a[176:179], v[8:11], v[60:63], a[176:179]
v_mfma_f32_16x16x32_bf16 a[204:207], v[12:15], v[32:35], a[204:207]
ds_read_b64_tr_b16 v[214:215], v241 offset:1152
s_xor_b32 m0, s82, 6144
v_mfma_f32_16x16x32_bf16 a[156:159], v[12:15], v[36:39], a[156:159]
buffer_load_dwordx4 v230, s[40:43], s79 offen lds
v_mfma_f32_16x16x32_bf16 a[100:103], v[12:15], v[40:43], a[100:103]
ds_read_b64_tr_b16 v[216:217], v242 offset:128
v_mfma_f32_16x16x32_bf16 a[164:167], v[12:15], v[44:47], a[164:167]
v_mfma_f32_16x16x32_bf16 a[108:111], v[12:15], v[48:51], a[108:111]
ds_read_b64_tr_b16 v[218:219], v242 offset:1152
s_xor_b32 m0, s82, 7168
v_mfma_f32_16x16x32_bf16 a[172:175], v[12:15], v[52:55], a[172:175]
buffer_load_dwordx4 v231, s[40:43], s79 offen lds
v_mfma_f32_16x16x32_bf16 a[116:119], v[12:15], v[56:59], a[116:119]
ds_read_b64_tr_b16 v[220:221], v243 offset:128
v_mfma_f32_16x16x32_bf16 a[180:183], v[12:15], v[60:63], a[180:183]
s_add_u32 s79, s79, 128
s_and_b32 s79, s79, 1023
s_xor_b32 s82, s82, 32768
v_mfma_f32_16x16x32_bf16 a[120:123], v[16:19], v[32:35], a[120:123]
ds_read_b64_tr_b16 v[222:223], v243 offset:1152
s_add_u32 m0, s83, 0
v_mfma_f32_16x16x32_bf16 a[184:187], v[16:19], v[36:39], a[184:187]
buffer_load_dwordx4 v232, s[44:47], s80 offen lds
v_mfma_f32_16x16x32_bf16 a[64:67], v[16:19], v[40:43], a[64:67]
ds_read_b128 v[132:135], v236 offset:26624
v_mfma_f32_16x16x32_bf16 a[0:3], v[16:19], v[44:47], a[0:3]
v_mfma_f32_16x16x32_bf16 a[72:75], v[16:19], v[48:51], a[72:75]
ds_read_b128 v[136:139], v236 offset:16384
s_add_u32 m0, s83, 1024
v_mfma_f32_16x16x32_bf16 a[8:11], v[16:19], v[52:55], a[8:11]
buffer_load_dwordx4 v233, s[44:47], s80 offen lds
v_mfma_f32_16x16x32_bf16 a[80:83], v[16:19], v[56:59], a[80:83]
ds_read_b128 v[140:143], v236 offset:18432
v_mfma_f32_16x16x32_bf16 a[16:19], v[16:19], v[60:63], a[16:19]
v_mfma_f32_16x16x32_bf16 a[124:127], v[20:23], v[32:35], a[124:127]
ds_read_b128 v[144:147], v236 offset:8192
s_add_u32 m0, s83, 2176
v_mfma_f32_16x16x32_bf16 a[188:191], v[20:23], v[36:39], a[188:191]
buffer_load_dwordx4 v234, s[44:47], s80 offen lds
v_mfma_f32_16x16x32_bf16 a[68:71], v[20:23], v[40:43], a[68:71]
ds_read_b128 v[148:151], v236 offset:10240
v_mfma_f32_16x16x32_bf16 a[4:7], v[20:23], v[44:47], a[4:7]
v_mfma_f32_16x16x32_bf16 a[76:79], v[20:23], v[48:51], a[76:79]
ds_read_b128 v[152:155], v236 offset:0
s_add_u32 m0, s83, 3200
v_mfma_f32_16x16x32_bf16 a[12:15], v[20:23], v[52:55], a[12:15]
buffer_load_dwordx4 v235, s[44:47], s80 offen lds
v_mfma_f32_16x16x32_bf16 a[84:87], v[20:23], v[56:59], a[84:87]
ds_read_b128 v[156:159], v236 offset:2048
v_mfma_f32_16x16x32_bf16 a[20:23], v[20:23], v[60:63], a[20:23]
s_add_u32 s80, s80, s81
s_and_b32 s80, s80, 4194303
s_add_u32 s83, s83, 16896
v_mfma_f32_16x16x32_bf16 a[88:91], v[24:27], v[32:35], a[88:91]
v_mfma_f32_16x16x32_bf16 a[24:27], v[24:27], v[36:39], a[24:27]
s_cmp_eq_u32 s83, s97
s_cselect_b32 s83, s90, s83
v_mfma_f32_16x16x32_bf16 a[240:243], v[24:27], v[40:43], a[240:243]
v_mfma_f32_16x16x32_bf16 a[32:35], v[24:27], v[44:47], a[32:35]
buffer_load_dword v254, v167, s[68:71], s96 offen
s_add_u32 s96, s96, 1048576
v_mfma_f32_16x16x32_bf16 a[248:251], v[24:27], v[48:51], a[248:251]
v_mfma_f32_16x16x32_bf16 a[40:43], v[24:27], v[52:55], a[40:43]
v_mfma_f32_16x16x32_bf16 a[48:51], v[24:27], v[56:59], a[48:51]
v_mfma_f32_16x16x32_bf16 a[56:59], v[24:27], v[60:63], a[56:59]
buffer_load_dword v254, v167, s[68:71], s96 offen
s_add_u32 s96, s96, 1048576
v_mfma_f32_16x16x32_bf16 a[92:95], v[28:31], v[32:35], a[92:95]
s_lshl_b32 s92, s77, 18
s_cmp_eq_u32 s85, 3
v_mfma_f32_16x16x32_bf16 a[28:31], v[28:31], v[36:39], a[28:31]
s_cselect_b32 s44, s68, s44
s_cselect_b32 s45, s69, s45
v_mfma_f32_16x16x32_bf16 a[244:247], v[28:31], v[40:43], a[244:247]
s_cselect_b32 s46, s70, s46
s_cselect_b32 s81, 524288, s81
v_mfma_f32_16x16x32_bf16 a[36:39], v[28:31], v[44:47], a[36:39]
s_cselect_b32 s42, 0, s42
s_cselect_b32 s80, s92, s80
v_mfma_f32_16x16x32_bf16 a[252:255], v[28:31], v[48:51], a[252:255]
v_mfma_f32_16x16x32_bf16 a[44:47], v[28:31], v[52:55], a[44:47]
v_mfma_f32_16x16x32_bf16 a[52:55], v[28:31], v[56:59], a[52:55]
v_mfma_f32_16x16x32_bf16 a[60:63], v[28:31], v[60:63], a[60:63]
s_waitcnt lgkmcnt(0)
s_waitcnt vmcnt(22)
s_barrier
v_mfma_f32_16x16x32_bf16 a[224:227], v[128:131], v[192:195], a[224:227]
ds_read_b128 v[0:3], v237 offset:24576
v_mfma_f32_16x16x32_bf16 a[232:235], v[128:131], v[196:199], a[232:235]
s_waitcnt vmcnt(14)
buffer_store_dwordx2 v[252:253], off, s[64:67], s91 offset:1024 sc1
v_mfma_f32_16x16x32_bf16 a[208:211], v[128:131], v[200:203], a[208:211]
ds_read_b64_tr_b16 v[32:33], v240 offset:16896
v_mfma_f32_16x16x32_bf16 a[128:131], v[128:131], v[204:207], a[128:131]
s_cmp_eq_u32 s85, 4
s_cselect_b32 s92, s89, 1073741824
v_mfma_f32_16x16x32_bf16 a[216:219], v[128:131], v[208:211], a[216:219]
ds_read_b64_tr_b16 v[34:35], v240 offset:17920
v_mfma_f32_16x16x32_bf16 a[136:139], v[128:131], v[212:215], a[136:139]
buffer_load_dwordx4 v[72:75], v255, s[64:67], s92 offen offset:1024 sc1
v_mfma_f32_16x16x32_bf16 a[192:195], v[128:131], v[216:219], a[192:195]
ds_read_b64_tr_b16 v[36:37], v241 offset:16896
v_mfma_f32_16x16x32_bf16 a[144:147], v[128:131], v[220:223], a[144:147]
s_mov_b32 s91, 1073741824
v_mfma_f32_16x16x32_bf16 a[228:231], v[132:135], v[192:195], a[228:231]
ds_read_b64_tr_b16 v[38:39], v241 offset:17920
v_mfma_f32_16x16x32_bf16 a[236:239], v[132:135], v[196:199], a[236:239]
v_mfma_f32_16x16x32_bf16 a[212:215], v[132:135], v[200:203], a[212:215]
ds_read_b64_tr_b16 v[40:41], v242 offset:16896
s_lshl_b32 s92, s76, 19
v_mfma_f32_16x16x32_bf16 a[132:135], v[132:135], v[204:207], a[132:135]
s_add_u32 s92, s92, 88080384
v_mfma_f32_16x16x32_bf16 a[220:223], v[132:135], v[208:211], a[220:223]
ds_read_b64_tr_b16 v[42:43], v242 offset:17920
v_mfma_f32_16x16x32_bf16 a[140:143], v[132:135], v[212:215], a[140:143]
s_add_u32 s52, s26, s92
s_addc_u32 s53, s27, 0
v_mfma_f32_16x16x32_bf16 a[196:199], v[132:135], v[216:219], a[196:199]
ds_read_b64_tr_b16 v[44:45], v243 offset:16896
s_and_b32 s53, s53, 0xffff
v_mfma_f32_16x16x32_bf16 a[148:151], v[132:135], v[220:223], a[148:151]
s_mov_b32 s54, 524672
s_mov_b32 s55, 0x20000
v_mfma_f32_16x16x32_bf16 a[200:203], v[136:139], v[192:195], a[200:203]
ds_read_b64_tr_b16 v[46:47], v243 offset:17920
s_lshl_b32 s92, s3, 16
v_mfma_f32_16x16x32_bf16 a[152:155], v[136:139], v[196:199], a[152:155]
s_sub_u32 s93, 458752, s92
v_add_u32 v244, s92, v168
v_mfma_f32_16x16x32_bf16 a[96:99], v[136:139], v[200:203], a[96:99]
ds_read_b64_tr_b16 v[48:49], v240 offset:17024
v_add_u32 v246, s93, v168
v_mfma_f32_16x16x32_bf16 a[160:163], v[136:139], v[204:207], a[160:163]
v_add_u32 v245, 0x8000, v244
v_add_u32 v247, 0x8000, v246
v_mfma_f32_16x16x32_bf16 a[104:107], v[136:139], v[208:211], a[104:107]
ds_read_b64_tr_b16 v[50:51], v240 offset:18048
s_lshl_b32 s94, s72, 19
v_mfma_f32_16x16x32_bf16 a[168:171], v[136:139], v[212:215], a[168:171]
s_lshl_b32 s93, s76, 16
s_add_u32 s92, s94, s93
v_mfma_f32_16x16x32_bf16 a[112:115], v[136:139], v[216:219], a[112:115]
ds_read_b64_tr_b16 v[52:53], v241 offset:17024
s_lshl_b32 s93, s77, 13
v_mfma_f32_16x16x32_bf16 a[176:179], v[136:139], v[220:223], a[176:179]
s_add_u32 s92, s92, s93
s_add_u32 s92, s92, 37748736
v_mfma_f32_16x16x32_bf16 a[204:207], v[140:143], v[192:195], a[204:207]
ds_read_b64_tr_b16 v[54:55], v241 offset:18048
v_mfma_f32_16x16x32_bf16 a[156:159], v[140:143], v[196:199], a[156:159]
s_add_u32 s48, s26, s92
s_addc_u32 s49, s27, 0
v_mfma_f32_16x16x32_bf16 a[100:103], v[140:143], v[200:203], a[100:103]
ds_read_b64_tr_b16 v[56:57], v242 offset:17024
s_and_b32 s49, s49, 0xffff
v_mfma_f32_16x16x32_bf16 a[164:167], v[140:143], v[204:207], a[164:167]
s_mov_b32 s50, 57344
s_mov_b32 s51, 0x20000
v_mfma_f32_16x16x32_bf16 a[108:111], v[140:143], v[208:211], a[108:111]
ds_read_b64_tr_b16 v[58:59], v242 offset:18048
s_lshl_b32 s93, s77, 24
v_mfma_f32_16x16x32_bf16 a[172:175], v[140:143], v[212:215], a[172:175]
s_add_u32 s92, s94, s93
s_add_u32 s92, s92, 20975616
v_mfma_f32_16x16x32_bf16 a[116:119], v[140:143], v[216:219], a[116:119]
ds_read_b64_tr_b16 v[60:61], v243 offset:17024
v_mfma_f32_16x16x32_bf16 a[180:183], v[140:143], v[220:223], a[180:183]
s_add_u32 s60, s26, s92
s_addc_u32 s61, s27, 0
v_mfma_f32_16x16x32_bf16 a[120:123], v[144:147], v[192:195], a[120:123]
ds_read_b64_tr_b16 v[62:63], v243 offset:18048
s_and_b32 s61, s61, 0xffff
s_add_u32 m0, s83, 0
v_mfma_f32_16x16x32_bf16 a[184:187], v[144:147], v[196:199], a[184:187]
buffer_load_dwordx4 v232, s[44:47], s80 offen lds
v_mfma_f32_16x16x32_bf16 a[64:67], v[144:147], v[200:203], a[64:67]
ds_read_b128 v[4:7], v237 offset:26624
s_mov_b32 s62, 520192
v_mfma_f32_16x16x32_bf16 a[0:3], v[144:147], v[204:207], a[0:3]
s_mov_b32 s63, 0x20000
s_lshl_b32 s92, s77, 14
v_mfma_f32_16x16x32_bf16 a[72:75], v[144:147], v[208:211], a[72:75]
ds_read_b128 v[8:11], v237 offset:16384
s_sub_u32 s95, 24576, s92
s_add_u32 m0, s83, 1024
v_mfma_f32_16x16x32_bf16 a[8:11], v[144:147], v[212:215], a[8:11]
buffer_load_dwordx4 v233, s[44:47], s80 offen lds
v_mfma_f32_16x16x32_bf16 a[80:83], v[144:147], v[216:219], a[80:83]
ds_read_b128 v[12:15], v237 offset:18432
v_add_u32 v238, s95, v255
v_mfma_f32_16x16x32_bf16 a[16:19], v[144:147], v[220:223], a[16:19]
v_mfma_f32_16x16x32_bf16 a[124:127], v[148:151], v[192:195], a[124:127]
ds_read_b128 v[16:19], v237 offset:8192
s_add_u32 m0, s83, 2176
v_mfma_f32_16x16x32_bf16 a[188:191], v[148:151], v[196:199], a[188:191]
buffer_load_dwordx4 v234, s[44:47], s80 offen lds
v_mfma_f32_16x16x32_bf16 a[68:71], v[148:151], v[200:203], a[68:71]
ds_read_b128 v[20:23], v237 offset:10240
v_mfma_f32_16x16x32_bf16 a[4:7], v[148:151], v[204:207], a[4:7]
v_mfma_f32_16x16x32_bf16 a[76:79], v[148:151], v[208:211], a[76:79]
ds_read_b128 v[24:27], v237 offset:0
s_add_u32 m0, s83, 3200
v_mfma_f32_16x16x32_bf16 a[12:15], v[148:151], v[212:215], a[12:15]
buffer_load_dwordx4 v235, s[44:47], s80 offen lds
v_mfma_f32_16x16x32_bf16 a[84:87], v[148:151], v[216:219], a[84:87]
ds_read_b128 v[28:31], v237 offset:2048
v_mfma_f32_16x16x32_bf16 a[20:23], v[148:151], v[220:223], a[20:23]
s_add_u32 s80, s80, s81
s_and_b32 s80, s80, 4194303
s_add_u32 s83, s83, 16896
v_mfma_f32_16x16x32_bf16 a[88:91], v[152:155], v[192:195], a[88:91]
v_mfma_f32_16x16x32_bf16 a[24:27], v[152:155], v[196:199], a[24:27]
s_cmp_eq_u32 s83, s97
s_cselect_b32 s83, s90, s83
v_mfma_f32_16x16x32_bf16 a[240:243], v[152:155], v[200:203], a[240:243]
v_mfma_f32_16x16x32_bf16 a[32:35], v[152:155], v[204:207], a[32:35]
v_mfma_f32_16x16x32_bf16 a[248:251], v[152:155], v[208:211], a[248:251]
v_mfma_f32_16x16x32_bf16 a[40:43], v[152:155], v[212:215], a[40:43]
v_mfma_f32_16x16x32_bf16 a[48:51], v[152:155], v[216:219], a[48:51]
v_mfma_f32_16x16x32_bf16 a[56:59], v[152:155], v[220:223], a[56:59]
v_mfma_f32_16x16x32_bf16 a[92:95], v[156:159], v[192:195], a[92:95]
v_mfma_f32_16x16x32_bf16 a[28:31], v[156:159], v[196:199], a[28:31]
v_add_u32 v240, s84, v240
v_add_u32 v241, s84, v241
v_mfma_f32_16x16x32_bf16 a[244:247], v[156:159], v[200:203], a[244:247]
v_add_u32_e64 v242, s84, v242
v_add_u32_e64 v243, s84, v243
v_mfma_f32_16x16x32_bf16 a[36:39], v[156:159], v[204:207], a[36:39]
v_xor_b32 v236, 0x8000, v236
v_xor_b32 v237, 0x8000, v237
v_mfma_f32_16x16x32_bf16 a[252:255], v[156:159], v[208:211], a[252:255]
s_sub_u32 s84, 0, s84
s_add_u32 s85, s85, 1
v_mfma_f32_16x16x32_bf16 a[44:47], v[156:159], v[212:215], a[44:47]
s_cmp_lg_u32 s85, 5
v_mfma_f32_16x16x32_bf16 a[52:55], v[156:159], v[216:219], a[52:55]
v_mfma_f32_16x16x32_bf16 a[60:63], v[156:159], v[220:223], a[60:63]
s_waitcnt lgkmcnt(0)
s_cbranch_scc1 .La5_LV_LOOP
s_waitcnt vmcnt(12)
s_barrier
v_mfma_f32_16x16x32_bf16 a[224:227], v[0:3], v[32:35], a[224:227]
s_waitcnt vmcnt(4)
v_mfma_f32_16x16x32_bf16 a[232:235], v[0:3], v[36:39], a[232:235]
v_cmp_eq_u64 vcc, v[72:73], v[252:253]
v_cmp_eq_u64 s[96:97], v[74:75], v[252:253]
s_mov_b32 s91, 0
v_mfma_f32_16x16x32_bf16 a[208:211], v[0:3], v[40:43], a[208:211]
s_and_b64 vcc, vcc, s[96:97]
s_cmp_eq_u64 vcc, -1
s_cbranch_scc0 .La5_LV_XSPIN
.La5_LV_XOK:
v_mfma_f32_16x16x32_bf16 a[128:131], v[0:3], v[44:47], a[128:131]
s_mov_b32 s89, 49152
v_subrev_u32 v240, 8448, v240
v_subrev_u32 v241, 8448, v241
v_mfma_f32_16x16x32_bf16 a[216:219], v[0:3], v[48:51], a[216:219]
v_subrev_u32 v242, 8448, v242
v_subrev_u32 v243, 8448, v243
v_mfma_f32_16x16x32_bf16 a[136:139], v[0:3], v[52:55], a[136:139]
buffer_load_dwordx4 v[128:131], v244, s[52:55], s91 offen offset:0 sc1
v_mfma_f32_16x16x32_bf16 a[192:195], v[0:3], v[56:59], a[192:195]
v_mfma_f32_16x16x32_bf16 a[144:147], v[0:3], v[60:63], a[144:147]
v_mfma_f32_16x16x32_bf16 a[228:231], v[4:7], v[32:35], a[228:231]
ds_read_b64_tr_b16 v[192:193], v240 offset:0
v_mfma_f32_16x16x32_bf16 a[236:239], v[4:7], v[36:39], a[236:239]
buffer_load_dwordx4 v[132:135], v245, s[52:55], s91 offen offset:0 sc1
v_mfma_f32_16x16x32_bf16 a[212:215], v[4:7], v[40:43], a[212:215]
ds_read_b64_tr_b16 v[194:195], v240 offset:1024
v_mfma_f32_16x16x32_bf16 a[132:135], v[4:7], v[44:47], a[132:135]
v_mfma_f32_16x16x32_bf16 a[220:223], v[4:7], v[48:51], a[220:223]
ds_read_b64_tr_b16 v[196:197], v241 offset:0
v_mfma_f32_16x16x32_bf16 a[140:143], v[4:7], v[52:55], a[140:143]
buffer_load_dwordx4 v[136:139], v246, s[52:55], s91 offen offset:0 sc1
v_mfma_f32_16x16x32_bf16 a[196:199], v[4:7], v[56:59], a[196:199]
ds_read_b64_tr_b16 v[198:199], v241 offset:1024
v_accvgpr_read_b32 v224, a224
v_mfma_f32_16x16x32_bf16 a[148:151], v[4:7], v[60:63], a[148:151]
v_accvgpr_read_b32 v225, a225
v_accvgpr_read_b32 v226, a226
v_mfma_f32_16x16x32_bf16 a[200:203], v[8:11], v[32:35], a[200:203]
ds_read_b64_tr_b16 v[200:201], v242 offset:0
v_accvgpr_read_b32 v227, a227
v_mfma_f32_16x16x32_bf16 a[152:155], v[8:11], v[36:39], a[152:155]
buffer_load_dwordx4 v[140:143], v247, s[52:55], s91 offen offset:0 sc1
v_accvgpr_read_b32 v228, a228
v_mfma_f32_16x16x32_bf16 a[96:99], v[8:11], v[40:43], a[96:99]
ds_read_b64_tr_b16 v[202:203], v242 offset:1024
v_accvgpr_read_b32 v229, a229
v_mfma_f32_16x16x32_bf16 a[160:163], v[8:11], v[44:47], a[160:163]
v_accvgpr_read_b32 v230, a230
v_accvgpr_read_b32 v231, a231
v_mfma_f32_16x16x32_bf16 a[104:107], v[8:11], v[48:51], a[104:107]
ds_read_b64_tr_b16 v[204:205], v243 offset:0
v_cvt_pk_bf16_f32 v248, v224, v225
v_mfma_f32_16x16x32_bf16 a[168:171], v[8:11], v[52:55], a[168:171]
buffer_load_dwordx4 v[144:147], v244, s[52:55], s91 offen offset:1024 sc1
v_cvt_pk_bf16_f32 v249, v226, v227
v_mfma_f32_16x16x32_bf16 a[112:115], v[8:11], v[56:59], a[112:115]
ds_read_b64_tr_b16 v[206:207], v243 offset:1024
v_cvt_pk_bf16_f32 v250, v228, v229
v_mfma_f32_16x16x32_bf16 a[176:179], v[8:11], v[60:63], a[176:179]
v_cvt_pk_bf16_f32 v251, v230, v231
buffer_store_dwordx4 v[248:251], v255, s[48:51], s89 offen sc1
v_mfma_f32_16x16x32_bf16 a[204:207], v[12:15], v[32:35], a[204:207]
ds_read_b64_tr_b16 v[208:209], v240 offset:128
s_add_u32 s89, s89, 1024
v_mfma_f32_16x16x32_bf16 a[156:159], v[12:15], v[36:39], a[156:159]
buffer_load_dwordx4 v[148:151], v245, s[52:55], s91 offen offset:1024 sc1
ds_write_b128 v238, v[248:251] offset:0
v_mfma_f32_16x16x32_bf16 a[100:103], v[12:15], v[40:43], a[100:103]
ds_read_b64_tr_b16 v[210:211], v240 offset:1152
v_mfma_f32_16x16x32_bf16 a[164:167], v[12:15], v[44:47], a[164:167]
v_mfma_f32_16x16x32_bf16 a[108:111], v[12:15], v[48:51], a[108:111]
ds_read_b64_tr_b16 v[212:213], v241 offset:128
v_mfma_f32_16x16x32_bf16 a[172:175], v[12:15], v[52:55], a[172:175]
buffer_load_dwordx4 v[152:155], v246, s[52:55], s91 offen offset:1024 sc1
v_mfma_f32_16x16x32_bf16 a[116:119], v[12:15], v[56:59], a[116:119]
ds_read_b64_tr_b16 v[214:215], v241 offset:1152
v_accvgpr_read_b32 v224, a232
v_mfma_f32_16x16x32_bf16 a[180:183], v[12:15], v[60:63], a[180:183]
v_accvgpr_read_b32 v225, a233
v_accvgpr_read_b32 v226, a234
v_mfma_f32_16x16x32_bf16 a[120:123], v[16:19], v[32:35], a[120:123]
ds_read_b64_tr_b16 v[216:217], v242 offset:128
v_accvgpr_read_b32 v227, a235
s_add_u32 m0, s83, 0
v_mfma_f32_16x16x32_bf16 a[184:187], v[16:19], v[36:39], a[184:187]
buffer_load_dwordx4 v232, s[44:47], s80 offen lds
v_mfma_f32_16x16x32_bf16 a[64:67], v[16:19], v[40:43], a[64:67]
ds_read_b64_tr_b16 v[218:219], v242 offset:1152
v_accvgpr_read_b32 v228, a236
v_mfma_f32_16x16x32_bf16 a[0:3], v[16:19], v[44:47], a[0:3]
buffer_load_dwordx4 v[156:159], v247, s[52:55], s91 offen offset:1024 sc1
v_accvgpr_read_b32 v229, a237
v_mfma_f32_16x16x32_bf16 a[72:75], v[16:19], v[48:51], a[72:75]
ds_read_b64_tr_b16 v[220:221], v243 offset:128
v_accvgpr_read_b32 v230, a238
s_add_u32 m0, s83, 1024
v_mfma_f32_16x16x32_bf16 a[8:11], v[16:19], v[52:55], a[8:11]
buffer_load_dwordx4 v233, s[44:47], s80 offen lds
v_mfma_f32_16x16x32_bf16 a[80:83], v[16:19], v[56:59], a[80:83]
ds_read_b64_tr_b16 v[222:223], v243 offset:1152
v_accvgpr_read_b32 v231, a239
v_mfma_f32_16x16x32_bf16 a[16:19], v[16:19], v[60:63], a[16:19]
v_cvt_pk_bf16_f32 v248, v224, v225
v_cvt_pk_bf16_f32 v249, v226, v227
v_mfma_f32_16x16x32_bf16 a[124:127], v[20:23], v[32:35], a[124:127]
v_cvt_pk_bf16_f32 v250, v228, v229
v_cvt_pk_bf16_f32 v251, v230, v231
s_add_u32 m0, s83, 2176
v_mfma_f32_16x16x32_bf16 a[188:191], v[20:23], v[36:39], a[188:191]
buffer_load_dwordx4 v234, s[44:47], s80 offen lds
v_mfma_f32_16x16x32_bf16 a[68:71], v[20:23], v[40:43], a[68:71]
buffer_store_dwordx4 v[248:251], v255, s[48:51], s89 offen sc1
s_add_u32 s89, s89, 1024
v_mfma_f32_16x16x32_bf16 a[4:7], v[20:23], v[44:47], a[4:7]
buffer_load_dwordx4 v[160:163], v244, s[52:55], s91 offen offset:2048 sc1
ds_write_b128 v238, v[248:251] offset:1024
v_mfma_f32_16x16x32_bf16 a[76:79], v[20:23], v[48:51], a[76:79]
s_add_u32 m0, s83, 3200
v_mfma_f32_16x16x32_bf16 a[12:15], v[20:23], v[52:55], a[12:15]
buffer_load_dwordx4 v235, s[44:47], s80 offen lds
v_mfma_f32_16x16x32_bf16 a[84:87], v[20:23], v[56:59], a[84:87]
v_accvgpr_read_b32 v224, a208
v_accvgpr_read_b32 v225, a209
v_mfma_f32_16x16x32_bf16 a[20:23], v[20:23], v[60:63], a[20:23]
s_add_u32 s80, s80, s81
s_and_b32 s80, s80, 4194303
s_add_u32 s83, s83, 16896
v_mfma_f32_16x16x32_bf16 a[88:91], v[24:27], v[32:35], a[88:91]
v_accvgpr_read_b32 v226, a210
v_accvgpr_read_b32 v227, a211
v_mfma_f32_16x16x32_bf16 a[24:27], v[24:27], v[36:39], a[24:27]
s_mov_b32 s83, s90
v_accvgpr_read_b32 v228, a212
v_mfma_f32_16x16x32_bf16 a[240:243], v[24:27], v[40:43], a[240:243]
v_accvgpr_read_b32 v229, a213
v_accvgpr_read_b32 v230, a214
v_mfma_f32_16x16x32_bf16 a[32:35], v[24:27], v[44:47], a[32:35]
buffer_load_dwordx4 v[164:167], v245, s[52:55], s91 offen offset:2048 sc1
.La5_LW1_5:
s_cbranch_execz .La5_LW1_9
v_accvgpr_read_b32 v231, a215
v_mfma_f32_16x16x32_bf16 a[248:251], v[24:27], v[48:51], a[248:251]
v_cvt_pk_bf16_f32 v248, v224, v225
v_cvt_pk_bf16_f32 v249, v226, v227
v_mfma_f32_16x16x32_bf16 a[40:43], v[24:27], v[52:55], a[40:43]
v_cvt_pk_bf16_f32 v250, v228, v229
v_cvt_pk_bf16_f32 v251, v230, v231
v_mfma_f32_16x16x32_bf16 a[48:51], v[24:27], v[56:59], a[48:51]
buffer_store_dwordx4 v[248:251], v255, s[48:51], s89 offen sc1
s_add_u32 s89, s89, 1024
v_mfma_f32_16x16x32_bf16 a[56:59], v[24:27], v[60:63], a[56:59]
buffer_load_dwordx4 v[168:171], v246, s[52:55], s91 offen offset:2048 sc1
ds_write_b128 v238, v[248:251] offset:2048
v_mfma_f32_16x16x32_bf16 a[92:95], v[28:31], v[32:35], a[92:95]
v_mfma_f32_16x16x32_bf16 a[28:31], v[28:31], v[36:39], a[28:31]
v_mfma_f32_16x16x32_bf16 a[244:247], v[28:31], v[40:43], a[244:247]
v_mfma_f32_16x16x32_bf16 a[36:39], v[28:31], v[44:47], a[36:39]
buffer_load_dwordx4 v[172:175], v247, s[52:55], s91 offen offset:2048 sc1
v_mfma_f32_16x16x32_bf16 a[252:255], v[28:31], v[48:51], a[252:255]
v_mfma_f32_16x16x32_bf16 a[44:47], v[28:31], v[52:55], a[44:47]
v_mfma_f32_16x16x32_bf16 a[52:55], v[28:31], v[56:59], a[52:55]
v_mfma_f32_16x16x32_bf16 a[60:63], v[28:31], v[60:63], a[60:63]
; ---- Q tile: 256 rows x 128 columns, K = 1024, while K / V leave the accumulators (memory + .La5_LDS ring); behind it the first step on the diagonal
.La5_LQ_START:
.La5_LW1_6:
s_cbranch_execz .La5_LW1_10
s_waitcnt vmcnt(15) lgkmcnt(6)
v_mfma_f32_16x16x32_bf16 v[0:3], v[192:195], v[128:131], 0
v_accvgpr_read_b32 v224, a128
v_accvgpr_read_b32 v225, a129
s_mov_b32 s85, s95
v_mfma_f32_16x16x32_bf16 v[32:35], v[192:195], v[132:135], 0
buffer_load_dwordx4 v[176:179], v244, s[52:55], s91 offen offset:3072 sc1
v_mfma_f32_16x16x32_bf16 v[64:67], v[192:195], v[136:139], 0
v_accvgpr_read_b32 v226, a130
v_accvgpr_read_b32 v227, a131
v_mfma_f32_16x16x32_bf16 v[96:99], v[192:195], v[140:143], 0
ds_read_b64_tr_b16 v[192:193], v240 offset:8448
ds_read_b64_tr_b16 v[194:195], v240 offset:9472
v_mfma_f32_16x16x32_bf16 v[4:7], v[196:199], v[128:131], 0
v_accvgpr_read_b32 v228, a132
v_accvgpr_read_b32 v229, a133
v_mfma_f32_16x16x32_bf16 v[36:39], v[196:199], v[132:135], 0
buffer_load_dwordx4 v[180:183], v245, s[52:55], s91 offen offset:3072 sc1
v_mfma_f32_16x16x32_bf16 v[68:71], v[196:199], v[136:139], 0
s_waitcnt lgkmcnt(4)
v_accvgpr_read_b32 v230, a134
v_mfma_f32_16x16x32_bf16 v[100:103], v[196:199], v[140:143], 0
ds_read_b64_tr_b16 v[196:197], v241 offset:8448
ds_read_b64_tr_b16 v[198:199], v241 offset:9472
v_mfma_f32_16x16x32_bf16 v[8:11], v[200:203], v[128:131], 0
v_accvgpr_read_b32 v231, a135
v_cvt_pk_bf16_f32 v248, v224, v225
v_mfma_f32_16x16x32_bf16 v[40:43], v[200:203], v[132:135], 0
buffer_load_dwordx4 v[184:187], v246, s[52:55], s91 offen offset:3072 sc1
v_mfma_f32_16x16x32_bf16 v[72:75], v[200:203], v[136:139], 0
v_cvt_pk_bf16_f32 v249, v226, v227
v_cvt_pk_bf16_f32 v250, v228, v229
v_mfma_f32_16x16x32_bf16 v[104:107], v[200:203], v[140:143], 0
ds_read_b64_tr_b16 v[200:201], v242 offset:8448
ds_read_b64_tr_b16 v[202:203], v242 offset:9472
v_mfma_f32_16x16x32_bf16 v[12:15], v[204:207], v[128:131], 0
v_cvt_pk_bf16_f32 v251, v230, v231
v_accvgpr_write_b32 a128, v248
v_mfma_f32_16x16x32_bf16 v[44:47], v[204:207], v[132:135], 0
buffer_load_dwordx4 v[188:191], v247, s[52:55], s91 offen offset:3072 sc1
v_mfma_f32_16x16x32_bf16 v[76:79], v[204:207], v[136:139], 0
s_add_u32 s91, s91, 4096
v_accvgpr_write_b32 a129, v249
v_mfma_f32_16x16x32_bf16 v[108:111], v[204:207], v[140:143], 0
ds_read_b64_tr_b16 v[204:205], v243 offset:8448
ds_read_b64_tr_b16 v[206:207], v243 offset:9472
v_mfma_f32_16x16x32_bf16 v[16:19], v[208:211], v[128:131], 0
v_accvgpr_write_b32 a130, v250
v_accvgpr_write_b32 a131, v251
v_mfma_f32_16x16x32_bf16 v[48:51], v[208:211], v[132:135], 0
s_and_b32 s97, s2, 7
s_mul_i32 s95, s97, 7
v_mfma_f32_16x16x32_bf16 v[80:83], v[208:211], v[136:139], 0
s_lshr_b32 s96, s30, 7
s_add_u32 s95, s95, s96
v_mfma_f32_16x16x32_bf16 v[112:115], v[208:211], v[140:143], 0
ds_read_b64_tr_b16 v[208:209], v240 offset:8576
ds_read_b64_tr_b16 v[210:211], v240 offset:9600
v_mfma_f32_16x16x32_bf16 v[20:23], v[212:215], v[128:131], 0
s_sub_u32 s95, 3, s95
s_and_b32 s95, s95, 7
v_mfma_f32_16x16x32_bf16 v[52:55], v[212:215], v[132:135], 0
s_min_u32 s95, s95, 6
s_lshl_b32 s95, s95, 1
v_mfma_f32_16x16x32_bf16 v[84:87], v[212:215], v[136:139], 0
s_lshr_b32 s92, s2, 6
s_mul_i32 s93, s92, 3
v_mfma_f32_16x16x32_bf16 v[116:119], v[212:215], v[140:143], 0
ds_read_b64_tr_b16 v[212:213], v241 offset:8576
ds_read_b64_tr_b16 v[214:215], v241 offset:9600
v_mfma_f32_16x16x32_bf16 v[24:27], v[216:219], v[128:131], 0
s_add_u32 s93, s93, s74
v_mfma_f32_16x16x32_bf16 v[56:59], v[216:219], v[132:135], 0
s_cmp_ge_u32 s93, s95
s_cselect_b32 s96, 2, 0
v_mfma_f32_16x16x32_bf16 v[88:91], v[216:219], v[136:139], 0
s_add_u32 s93, s93, s96
s_add_u32 s95, s95, s92
v_mfma_f32_16x16x32_bf16 v[120:123], v[216:219], v[140:143], 0
ds_read_b64_tr_b16 v[216:217], v242 offset:8576
ds_read_b64_tr_b16 v[218:219], v242 offset:9600
v_mfma_f32_16x16x32_bf16 v[28:31], v[220:223], v[128:131], 0
ds_write_b128 v238, v[248:251] offset:3072
v_mfma_f32_16x16x32_bf16 v[60:63], v[220:223], v[132:135], 0
buffer_store_dwordx4 v[248:251], v255, s[48:51], s89 offen offset:0 sc1
v_mfma_f32_16x16x32_bf16 v[92:95], v[220:223], v[136:139], 0
s_waitcnt vmcnt(14) lgkmcnt(3)
v_mfma_f32_16x16x32_bf16 v[124:127], v[220:223], v[140:143], 0
ds_read_b64_tr_b16 v[220:221], v243 offset:8576
ds_read_b64_tr_b16 v[222:223], v243 offset:9600
v_mfma_f32_16x16x32_bf16 v[0:3], v[192:195], v[144:147], v[0:3]
v_accvgpr_read_b32 v224, a216
v_accvgpr_read_b32 v225, a217
v_mfma_f32_16x16x32_bf16 v[32:35], v[192:195], v[148:151], v[32:35]
buffer_load_dwordx4 v[128:131], v244, s[52:55], s91 offen offset:0 sc1
v_mfma_f32_16x16x32_bf16 v[64:67], v[192:195], v[152:155], v[64:67]
v_accvgpr_read_b32 v226, a218
v_accvgpr_read_b32 v227, a219
v_mfma_f32_16x16x32_bf16 v[96:99], v[192:195], v[156:159], v[96:99]
v_accvgpr_read_b32 v228, a220
v_accvgpr_read_b32 v229, a221
v_mfma_f32_16x16x32_bf16 v[4:7], v[196:199], v[144:147], v[4:7]
s_waitcnt lgkmcnt(0)
s_barrier
v_mfma_f32_16x16x32_bf16 v[36:39], v[196:199], v[148:151], v[36:39]
buffer_load_dwordx4 v[132:135], v245, s[52:55], s91 offen offset:0 sc1
v_mfma_f32_16x16x32_bf16 v[68:71], v[196:199], v[152:155], v[68:71]
ds_read_b64_tr_b16 v[192:193], v240 offset:16896
ds_read_b64_tr_b16 v[194:195], v240 offset:17920
v_mfma_f32_16x16x32_bf16 v[100:103], v[196:199], v[156:159], v[100:103]
ds_read_b64_tr_b16 v[196:197], v241 offset:16896
ds_read_b64_tr_b16 v[198:199], v241 offset:17920
v_mfma_f32_16x16x32_bf16 v[8:11], v[200:203], v[144:147], v[8:11]
v_accvgpr_read_b32 v230, a222
v_accvgpr_read_b32 v231, a223
v_mfma_f32_16x16x32_bf16 v[40:43], v[200:203], v[148:151], v[40:43]
buffer_load_dwordx4 v[136:139], v246, s[52:55], s91 offen offset:0 sc1
v_mfma_f32_16x16x32_bf16 v[72:75], v[200:203], v[152:155], v[72:75]
v_cvt_pk_bf16_f32 v248, v224, v225
v_cvt_pk_bf16_f32 v249, v226, v227
v_mfma_f32_16x16x32_bf16 v[104:107], v[200:203], v[156:159], v[104:107]
ds_read_b64_tr_b16 v[200:201], v242 offset:16896
ds_read_b64_tr_b16 v[202:203], v242 offset:17920
v_mfma_f32_16x16x32_bf16 v[12:15], v[204:207], v[144:147], v[12:15]
s_add_u32 m0, s83, 0
v_cvt_pk_bf16_f32 v250, v228, v229
v_mfma_f32_16x16x32_bf16 v[44:47], v[204:207], v[148:151], v[44:47]
buffer_load_dwordx4 v232, s[44:47], s80 offen lds
v_mfma_f32_16x16x32_bf16 v[76:79], v[204:207], v[152:155], v[76:79]
v_cvt_pk_bf16_f32 v251, v230, v231
v_accvgpr_write_b32 a132, v248
v_mfma_f32_16x16x32_bf16 v[108:111], v[204:207], v[156:159], v[108:111]
ds_read_b64_tr_b16 v[204:205], v243 offset:16896
ds_read_b64_tr_b16 v[206:207], v243 offset:17920
v_mfma_f32_16x16x32_bf16 v[16:19], v[208:211], v[144:147], v[16:19]
v_accvgpr_write_b32 a133, v249
v_accvgpr_write_b32 a134, v250
v_mfma_f32_16x16x32_bf16 v[48:51], v[208:211], v[148:151], v[48:51]
buffer_load_dwordx4 v[140:143], v247, s[52:55], s91 offen offset:0 sc1
v_mfma_f32_16x16x32_bf16 v[80:83], v[208:211], v[152:155], v[80:83]
v_accvgpr_write_b32 a135, v251
v_mfma_f32_16x16x32_bf16 v[112:115], v[208:211], v[156:159], v[112:115]
ds_read_b64_tr_b16 v[208:209], v240 offset:17024
ds_read_b64_tr_b16 v[210:211], v240 offset:18048
v_mfma_f32_16x16x32_bf16 v[20:23], v[212:215], v[144:147], v[20:23]
s_add_u32 m0, s83, 1024
v_mfma_f32_16x16x32_bf16 v[52:55], v[212:215], v[148:151], v[52:55]
buffer_load_dwordx4 v233, s[44:47], s80 offen lds
v_mfma_f32_16x16x32_bf16 v[84:87], v[212:215], v[152:155], v[84:87]
s_cmp_lt_u32 s92, 2
s_cselect_b32 s94, s75, 0
v_mfma_f32_16x16x32_bf16 v[116:119], v[212:215], v[156:159], v[116:119]
ds_read_b64_tr_b16 v[212:213], v241 offset:17024
ds_read_b64_tr_b16 v[214:215], v241 offset:18048
v_mfma_f32_16x16x32_bf16 v[24:27], v[216:219], v[144:147], v[24:27]
s_cmp_eq_u32 s74, 3
s_cselect_b32 s92, s95, s93
s_cselect_b32 s94, s94, s75
v_mfma_f32_16x16x32_bf16 v[56:59], v[216:219], v[148:151], v[56:59]
s_mul_i32 s93, s97, 14
s_add_u32 s92, s92, s93
v_mfma_f32_16x16x32_bf16 v[88:91], v[216:219], v[152:155], v[88:91]
s_mul_i32 s96, s92, s94
s_mov_b32 s100, s96
v_mfma_f32_16x16x32_bf16 v[120:123], v[216:219], v[156:159], v[120:123]
ds_read_b64_tr_b16 v[216:217], v242 offset:17024
ds_read_b64_tr_b16 v[218:219], v242 offset:18048
v_mfma_f32_16x16x32_bf16 v[28:31], v[220:223], v[144:147], v[28:31]
ds_write_b128 v238, v[248:251] offset:4096
v_mfma_f32_16x16x32_bf16 v[60:63], v[220:223], v[148:151], v[60:63]
s_lshr_b32 s92, s2, 3
s_andn2_b32 s92, s92, 1
v_mfma_f32_16x16x32_bf16 v[92:95], v[220:223], v[152:155], v[92:95]
s_waitcnt vmcnt(11) lgkmcnt(3)
s_and_b32 s93, s2, 7
v_mfma_f32_16x16x32_bf16 v[124:127], v[220:223], v[156:159], v[124:127]
ds_read_b64_tr_b16 v[220:221], v243 offset:17024
ds_read_b64_tr_b16 v[222:223], v243 offset:18048
v_mfma_f32_16x16x32_bf16 v[0:3], v[192:195], v[160:163], v[0:3]
v_accvgpr_read_b32 v224, a136
v_accvgpr_read_b32 v225, a137
v_mfma_f32_16x16x32_bf16 v[32:35], v[192:195], v[164:167], v[32:35]
buffer_load_dwordx4 v[144:147], v244, s[52:55], s91 offen offset:1024 sc1
v_mfma_f32_16x16x32_bf16 v[64:67], v[192:195], v[168:171], v[64:67]
v_accvgpr_read_b32 v226, a138
v_accvgpr_read_b32 v227, a139
v_mfma_f32_16x16x32_bf16 v[96:99], v[192:195], v[172:175], v[96:99]
ds_read_b64_tr_b16 v[192:193], v240 offset:25344
ds_read_b64_tr_b16 v[194:195], v240 offset:26368
v_mfma_f32_16x16x32_bf16 v[4:7], v[196:199], v[160:163], v[4:7]
v_accvgpr_read_b32 v228, a140
v_accvgpr_read_b32 v229, a141
v_mfma_f32_16x16x32_bf16 v[36:39], v[196:199], v[164:167], v[36:39]
buffer_load_dwordx4 v[148:151], v245, s[52:55], s91 offen offset:1024 sc1
v_mfma_f32_16x16x32_bf16 v[68:71], v[196:199], v[168:171], v[68:71]
s_waitcnt lgkmcnt(2)
v_accvgpr_read_b32 v230, a142
v_mfma_f32_16x16x32_bf16 v[100:103], v[196:199], v[172:175], v[100:103]
ds_read_b64_tr_b16 v[196:197], v241 offset:25344
ds_read_b64_tr_b16 v[198:199], v241 offset:26368
v_mfma_f32_16x16x32_bf16 v[8:11], v[200:203], v[160:163], v[8:11]
s_add_u32 m0, s83, 2176
v_accvgpr_read_b32 v231, a143
v_mfma_f32_16x16x32_bf16 v[40:43], v[200:203], v[164:167], v[40:43]
buffer_load_dwordx4 v234, s[44:47], s80 offen lds
v_mfma_f32_16x16x32_bf16 v[72:75], v[200:203], v[168:171], v[72:75]
v_cvt_pk_bf16_f32 v248, v224, v225
v_cvt_pk_bf16_f32 v249, v226, v227
v_mfma_f32_16x16x32_bf16 v[104:107], v[200:203], v[172:175], v[104:107]
ds_read_b64_tr_b16 v[200:201], v242 offset:25344
ds_read_b64_tr_b16 v[202:203], v242 offset:26368
v_mfma_f32_16x16x32_bf16 v[12:15], v[204:207], v[160:163], v[12:15]
v_cvt_pk_bf16_f32 v250, v228, v229
v_cvt_pk_bf16_f32 v251, v230, v231
v_mfma_f32_16x16x32_bf16 v[44:47], v[204:207], v[164:167], v[44:47]
buffer_load_dwordx4 v[152:155], v246, s[52:55], s91 offen offset:1024 sc1
v_mfma_f32_16x16x32_bf16 v[76:79], v[204:207], v[168:171], v[76:79]
v_accvgpr_write_b32 a136, v248
v_accvgpr_write_b32 a137, v249
v_mfma_f32_16x16x32_bf16 v[108:111], v[204:207], v[172:175], v[108:111]
ds_read_b64_tr_b16 v[204:205], v243 offset:25344
ds_read_b64_tr_b16 v[206:207], v243 offset:26368
v_mfma_f32_16x16x32_bf16 v[16:19], v[208:211], v[160:163], v[16:19]
s_add_u32 m0, s83, 3200
v_accvgpr_write_b32 a138, v250
v_mfma_f32_16x16x32_bf16 v[48:51], v[208:211], v[164:167], v[48:51]
buffer_load_dwordx4 v235, s[44:47], s80 offen lds
v_mfma_f32_16x16x32_bf16 v[80:83], v[208:211], v[168:171], v[80:83]
s_add_u32 s80, s80, 524288
v_accvgpr_write_b32 a139, v251
v_mfma_f32_16x16x32_bf16 v[112:115], v[208:211], v[172:175], v[112:115]
ds_read_b64_tr_b16 v[208:209], v240 offset:25472
ds_read_b64_tr_b16 v[210:211], v240 offset:26496
v_mfma_f32_16x16x32_bf16 v[20:23], v[212:215], v[160:163], v[20:23]
s_lshl_b32 s95, s92, 16
s_lshr_b32 s97, s93, 1
v_mfma_f32_16x16x32_bf16 v[52:55], v[212:215], v[164:167], v[52:55]
buffer_load_dwordx4 v[156:159], v247, s[52:55], s91 offen offset:1024 sc1
v_mfma_f32_16x16x32_bf16 v[84:87], v[212:215], v[168:171], v[84:87]
s_lshl_b32 s97, s97, 21
s_add_u32 s95, s95, s97
v_mfma_f32_16x16x32_bf16 v[116:119], v[212:215], v[172:175], v[116:119]
ds_read_b64_tr_b16 v[212:213], v241 offset:25472
ds_read_b64_tr_b16 v[214:215], v241 offset:26496
v_mfma_f32_16x16x32_bf16 v[24:27], v[216:219], v[160:163], v[24:27]
s_and_b32 s97, s93, 1
s_lshl_b32 s97, s97, 10
v_mfma_f32_16x16x32_bf16 v[56:59], v[216:219], v[164:167], v[56:59]
s_add_u32 s95, s95, s97
s_mul_i32 s97, s92, 229376
v_mfma_f32_16x16x32_bf16 v[88:91], v[216:219], v[168:171], v[88:91]
s_mov_b32 s18, 53248
s_mul_i32 s38, s93, 896
v_mfma_f32_16x16x32_bf16 v[120:123], v[216:219], v[172:175], v[120:123]
ds_read_b64_tr_b16 v[216:217], v242 offset:25472
ds_read_b64_tr_b16 v[218:219], v242 offset:26496
v_mfma_f32_16x16x32_bf16 v[28:31], v[220:223], v[160:163], v[28:31]
ds_write_b128 v238, v[248:251] offset:5120
v_mfma_f32_16x16x32_bf16 v[60:63], v[220:223], v[164:167], v[60:63]
buffer_store_dwordx4 a[132:135], v255, s[48:51], s18 offen offset:0 sc1
v_mfma_f32_16x16x32_bf16 v[92:95], v[220:223], v[168:171], v[92:95]
s_waitcnt vmcnt(14) lgkmcnt(3)
s_add_u32 s97, s97, s38
v_mfma_f32_16x16x32_bf16 v[124:127], v[220:223], v[172:175], v[124:127]
ds_read_b64_tr_b16 v[220:221], v243 offset:25472
ds_read_b64_tr_b16 v[222:223], v243 offset:26496
v_mfma_f32_16x16x32_bf16 v[0:3], v[192:195], v[176:179], v[0:3]
v_accvgpr_read_b32 v224, a192
v_accvgpr_read_b32 v225, a193
v_mfma_f32_16x16x32_bf16 v[32:35], v[192:195], v[180:183], v[32:35]
buffer_load_dwordx4 v[160:163], v244, s[52:55], s91 offen offset:2048 sc1
v_mfma_f32_16x16x32_bf16 v[64:67], v[192:195], v[184:187], v[64:67]
v_accvgpr_read_b32 v226, a194
v_accvgpr_read_b32 v227, a195
v_mfma_f32_16x16x32_bf16 v[96:99], v[192:195], v[188:191], v[96:99]
v_accvgpr_read_b32 v228, a196
v_accvgpr_read_b32 v229, a197
v_mfma_f32_16x16x32_bf16 v[4:7], v[196:199], v[176:179], v[4:7]
s_waitcnt lgkmcnt(0)
s_barrier
v_mfma_f32_16x16x32_bf16 v[36:39], v[196:199], v[180:183], v[36:39]
buffer_load_dwordx4 v[164:167], v245, s[52:55], s91 offen offset:2048 sc1
v_mfma_f32_16x16x32_bf16 v[68:71], v[196:199], v[184:187], v[68:71]
ds_read_b64_tr_b16 v[192:193], v240 offset:33792
ds_read_b64_tr_b16 v[194:195], v240 offset:34816
v_mfma_f32_16x16x32_bf16 v[100:103], v[196:199], v[188:191], v[100:103]
ds_read_b64_tr_b16 v[196:197], v241 offset:33792
ds_read_b64_tr_b16 v[198:199], v241 offset:34816
v_mfma_f32_16x16x32_bf16 v[8:11], v[200:203], v[176:179], v[8:11]
v_accvgpr_read_b32 v230, a198
v_accvgpr_read_b32 v231, a199
v_mfma_f32_16x16x32_bf16 v[40:43], v[200:203], v[180:183], v[40:43]
buffer_load_dwordx4 v[168:171], v246, s[52:55], s91 offen offset:2048 sc1
v_mfma_f32_16x16x32_bf16 v[72:75], v[200:203], v[184:187], v[72:75]
v_cvt_pk_bf16_f32 v248, v224, v225
v_cvt_pk_bf16_f32 v249, v226, v227
v_mfma_f32_16x16x32_bf16 v[104:107], v[200:203], v[188:191], v[104:107]
ds_read_b64_tr_b16 v[200:201], v242 offset:33792
ds_read_b64_tr_b16 v[202:203], v242 offset:34816
v_mfma_f32_16x16x32_bf16 v[12:15], v[204:207], v[176:179], v[12:15]
s_add_u32 m0, s83, 16896
v_cvt_pk_bf16_f32 v250, v228, v229
v_mfma_f32_16x16x32_bf16 v[44:47], v[204:207], v[180:183], v[44:47]
buffer_load_dwordx4 v232, s[44:47], s80 offen lds
v_mfma_f32_16x16x32_bf16 v[76:79], v[204:207], v[184:187], v[76:79]
v_cvt_pk_bf16_f32 v251, v230, v231
v_accvgpr_write_b32 a140, v248
v_mfma_f32_16x16x32_bf16 v[108:111], v[204:207], v[188:191], v[108:111]
ds_read_b64_tr_b16 v[204:205], v243 offset:33792
ds_read_b64_tr_b16 v[206:207], v243 offset:34816
v_mfma_f32_16x16x32_bf16 v[16:19], v[208:211], v[176:179], v[16:19]
v_accvgpr_write_b32 a141, v249
v_accvgpr_write_b32 a142, v250
v_mfma_f32_16x16x32_bf16 v[48:51], v[208:211], v[180:183], v[48:51]
buffer_load_dwordx4 v[172:175], v247, s[52:55], s91 offen offset:2048 sc1
v_mfma_f32_16x16x32_bf16 v[80:83], v[208:211], v[184:187], v[80:83]
v_accvgpr_write_b32 a143, v251
v_mfma_f32_16x16x32_bf16 v[112:115], v[208:211], v[188:191], v[112:115]
ds_read_b64_tr_b16 v[208:209], v240 offset:33920
ds_read_b64_tr_b16 v[210:211], v240 offset:34944
v_mfma_f32_16x16x32_bf16 v[20:23], v[212:215], v[176:179], v[20:23]
s_add_u32 m0, s83, 17920
v_mfma_f32_16x16x32_bf16 v[52:55], v[212:215], v[180:183], v[52:55]
buffer_load_dwordx4 v233, s[44:47], s80 offen lds
v_mfma_f32_16x16x32_bf16 v[84:87], v[212:215], v[184:187], v[84:87]
s_cmp_eq_u32 s3, 0
s_cselect_b32 s95, s95, s97
s_cselect_b64 s[36:37], s[28:29], s[30:31]
s_cselect_b32 s21, 2, 7
v_mfma_f32_16x16x32_bf16 v[116:119], v[212:215], v[188:191], v[116:119]
ds_read_b64_tr_b16 v[212:213], v241 offset:33920
ds_read_b64_tr_b16 v[214:215], v241 offset:34944
v_mfma_f32_16x16x32_bf16 v[24:27], v[216:219], v[176:179], v[24:27]
s_cmp_eq_u32 s3, 2
s_cselect_b64 s[36:37], s[32:33], s[36:37]
v_mfma_f32_16x16x32_bf16 v[56:59], v[216:219], v[180:183], v[56:59]
s_lshl_b32 s93, s93, 5
s_add_u32 s92, s92, s93
v_mfma_f32_16x16x32_bf16 v[88:91], v[216:219], v[184:187], v[88:91]
s_mul_i32 s97, s92, 28672
v_mfma_f32_16x16x32_bf16 v[120:123], v[216:219], v[188:191], v[120:123]
ds_read_b64_tr_b16 v[216:217], v242 offset:33920
ds_read_b64_tr_b16 v[218:219], v242 offset:34944
v_mfma_f32_16x16x32_bf16 v[28:31], v[220:223], v[176:179], v[28:31]
ds_write_b128 v238, v[248:251] offset:6144
v_mfma_f32_16x16x32_bf16 v[60:63], v[220:223], v[180:183], v[60:63]
buffer_store_dwordx4 a[136:139], v255, s[48:51], s18 offen offset:1024 sc1
v_mfma_f32_16x16x32_bf16 v[92:95], v[220:223], v[184:187], v[92:95]
s_waitcnt vmcnt(15) lgkmcnt(3)
v_mfma_f32_16x16x32_bf16 v[124:127], v[220:223], v[188:191], v[124:127]
ds_read_b64_tr_b16 v[220:221], v243 offset:33920
ds_read_b64_tr_b16 v[222:223], v243 offset:34944
v_mfma_f32_16x16x32_bf16 v[0:3], v[192:195], v[128:131], v[0:3]
v_accvgpr_read_b32 v224, a144
v_accvgpr_read_b32 v225, a145
v_mfma_f32_16x16x32_bf16 v[32:35], v[192:195], v[132:135], v[32:35]
buffer_load_dwordx4 v[176:179], v244, s[52:55], s91 offen offset:3072 sc1
v_mfma_f32_16x16x32_bf16 v[64:67], v[192:195], v[136:139], v[64:67]
v_accvgpr_read_b32 v226, a146
v_accvgpr_read_b32 v227, a147
v_mfma_f32_16x16x32_bf16 v[96:99], v[192:195], v[140:143], v[96:99]
ds_read_b64_tr_b16 v[192:193], v240 offset:42240
ds_read_b64_tr_b16 v[194:195], v240 offset:43264
v_mfma_f32_16x16x32_bf16 v[4:7], v[196:199], v[128:131], v[4:7]
v_accvgpr_read_b32 v228, a148
v_accvgpr_read_b32 v229, a149
v_mfma_f32_16x16x32_bf16 v[36:39], v[196:199], v[132:135], v[36:39]
buffer_load_dwordx4 v[180:183], v245, s[52:55], s91 offen offset:3072 sc1
v_mfma_f32_16x16x32_bf16 v[68:71], v[196:199], v[136:139], v[68:71]
s_waitcnt lgkmcnt(2)
v_accvgpr_read_b32 v230, a150
v_mfma_f32_16x16x32_bf16 v[100:103], v[196:199], v[140:143], v[100:103]
ds_read_b64_tr_b16 v[196:197], v241 offset:42240
ds_read_b64_tr_b16 v[198:199], v241 offset:43264
v_mfma_f32_16x16x32_bf16 v[8:11], v[200:203], v[128:131], v[8:11]
s_add_u32 m0, s83, 19072
v_accvgpr_read_b32 v231, a151
v_mfma_f32_16x16x32_bf16 v[40:43], v[200:203], v[132:135], v[40:43]
buffer_load_dwordx4 v234, s[44:47], s80 offen lds
v_mfma_f32_16x16x32_bf16 v[72:75], v[200:203], v[136:139], v[72:75]
v_cvt_pk_bf16_f32 v248, v224, v225
v_cvt_pk_bf16_f32 v249, v226, v227
v_mfma_f32_16x16x32_bf16 v[104:107], v[200:203], v[140:143], v[104:107]
ds_read_b64_tr_b16 v[200:201], v242 offset:42240
ds_read_b64_tr_b16 v[202:203], v242 offset:43264
v_mfma_f32_16x16x32_bf16 v[12:15], v[204:207], v[128:131], v[12:15]
v_cvt_pk_bf16_f32 v250, v228, v229
v_cvt_pk_bf16_f32 v251, v230, v231
v_mfma_f32_16x16x32_bf16 v[44:47], v[204:207], v[132:135], v[44:47]
buffer_load_dwordx4 v[184:187], v246, s[52:55], s91 offen offset:3072 sc1
v_mfma_f32_16x16x32_bf16 v[76:79], v[204:207], v[136:139], v[76:79]
v_accvgpr_write_b32 a144, v248
v_accvgpr_write_b32 a145, v249
v_mfma_f32_16x16x32_bf16 v[108:111], v[204:207], v[140:143], v[108:111]
ds_read_b64_tr_b16 v[204:205], v243 offset:42240
ds_read_b64_tr_b16 v[206:207], v243 offset:43264
v_mfma_f32_16x16x32_bf16 v[16:19], v[208:211], v[128:131], v[16:19]
s_add_u32 m0, s83, 20096
v_accvgpr_write_b32 a146, v250
v_mfma_f32_16x16x32_bf16 v[48:51], v[208:211], v[132:135], v[48:51]
buffer_load_dwordx4 v235, s[44:47], s80 offen lds
v_mfma_f32_16x16x32_bf16 v[80:83], v[208:211], v[136:139], v[80:83]
s_add_u32 s80, s80, 524288
v_accvgpr_write_b32 a147, v251
v_mfma_f32_16x16x32_bf16 v[112:115], v[208:211], v[140:143], v[112:115]
ds_read_b64_tr_b16 v[208:209], v240 offset:42368
ds_read_b64_tr_b16 v[210:211], v240 offset:43392
v_mfma_f32_16x16x32_bf16 v[20:23], v[212:215], v[128:131], v[20:23]
s_cmp_eq_u32 s3, 3
s_cselect_b32 s95, s97, s95
s_cselect_b64 s[36:37], s[34:35], s[36:37]
s_cselect_b32 s21, 1, s21
s_cselect_b32 s97, 31, 39
v_mfma_f32_16x16x32_bf16 v[52:55], v[212:215], v[132:135], v[52:55]
buffer_load_dwordx4 v[188:191], v247, s[52:55], s91 offen offset:3072 sc1
v_mfma_f32_16x16x32_bf16 v[84:87], v[212:215], v[136:139], v[84:87]
s_add_u32 s91, s91, 4096
s_lshl_b32 s21, s21, 10
v_mfma_f32_16x16x32_bf16 v[116:119], v[212:215], v[140:143], v[116:119]
ds_read_b64_tr_b16 v[212:213], v241 offset:42368
ds_read_b64_tr_b16 v[214:215], v241 offset:43392
v_mfma_f32_16x16x32_bf16 v[24:27], v[216:219], v[128:131], v[24:27]
s_cmp_eq_u32 s75, 1
s_cselect_b32 s97, 23, s97
s_cselect_b32 s92, 0, 24
v_mfma_f32_16x16x32_bf16 v[56:59], v[216:219], v[132:135], v[56:59]
s_mul_i32 s92, s92, s21
s_add_u32 s95, s95, s92
v_mfma_f32_16x16x32_bf16 v[88:91], v[216:219], v[136:139], v[88:91]
s_mul_i32 s97, s97, s21
s_add_u32 s97, s97, 896
v_mfma_f32_16x16x32_bf16 v[120:123], v[216:219], v[140:143], v[120:123]
ds_read_b64_tr_b16 v[216:217], v242 offset:42368
ds_read_b64_tr_b16 v[218:219], v242 offset:43392
v_mfma_f32_16x16x32_bf16 v[28:31], v[220:223], v[128:131], v[28:31]
ds_write_b128 v238, v[248:251] offset:7168
v_mfma_f32_16x16x32_bf16 v[60:63], v[220:223], v[132:135], v[60:63]
buffer_store_dwordx4 a[140:143], v255, s[48:51], s18 offen offset:2048 sc1
v_mfma_f32_16x16x32_bf16 v[92:95], v[220:223], v[136:139], v[92:95]
s_waitcnt vmcnt(15) lgkmcnt(3)
v_mfma_f32_16x16x32_bf16 v[124:127], v[220:223], v[140:143], v[124:127]
ds_read_b64_tr_b16 v[220:221], v243 offset:42368
ds_read_b64_tr_b16 v[222:223], v243 offset:43392
v_mfma_f32_16x16x32_bf16 v[0:3], v[192:195], v[144:147], v[0:3]
v_accvgpr_read_b32 v224, a200
v_accvgpr_read_b32 v225, a201
v_mfma_f32_16x16x32_bf16 v[32:35], v[192:195], v[148:151], v[32:35]
buffer_load_dwordx4 v[128:131], v244, s[52:55], s91 offen offset:0 sc1
v_mfma_f32_16x16x32_bf16 v[64:67], v[192:195], v[152:155], v[64:67]
v_accvgpr_read_b32 v226, a202
v_accvgpr_read_b32 v227, a203
v_mfma_f32_16x16x32_bf16 v[96:99], v[192:195], v[156:159], v[96:99]
v_accvgpr_read_b32 v228, a204
v_accvgpr_read_b32 v229, a205
v_mfma_f32_16x16x32_bf16 v[4:7], v[196:199], v[144:147], v[4:7]
s_waitcnt lgkmcnt(0)
s_barrier
v_mfma_f32_16x16x32_bf16 v[36:39], v[196:199], v[148:151], v[36:39]
buffer_load_dwordx4 v[132:135], v245, s[52:55], s91 offen offset:0 sc1
v_mfma_f32_16x16x32_bf16 v[68:71], v[196:199], v[152:155], v[68:71]
ds_read_b64_tr_b16 v[192:193], v240 offset:50688
ds_read_b64_tr_b16 v[194:195], v240 offset:51712
v_mfma_f32_16x16x32_bf16 v[100:103], v[196:199], v[156:159], v[100:103]
ds_read_b64_tr_b16 v[196:197], v241 offset:50688
ds_read_b64_tr_b16 v[198:199], v241 offset:51712
v_mfma_f32_16x16x32_bf16 v[8:11], v[200:203], v[144:147], v[8:11]
v_accvgpr_read_b32 v230, a206
v_accvgpr_read_b32 v231, a207
v_mfma_f32_16x16x32_bf16 v[40:43], v[200:203], v[148:151], v[40:43]
buffer_load_dwordx4 v[136:139], v246, s[52:55], s91 offen offset:0 sc1
v_mfma_f32_16x16x32_bf16 v[72:75], v[200:203], v[152:155], v[72:75]
v_cvt_pk_bf16_f32 v248, v224, v225
v_cvt_pk_bf16_f32 v249, v226, v227
v_mfma_f32_16x16x32_bf16 v[104:107], v[200:203], v[156:159], v[104:107]
ds_read_b64_tr_b16 v[200:201], v242 offset:50688
ds_read_b64_tr_b16 v[202:203], v242 offset:51712
v_mfma_f32_16x16x32_bf16 v[12:15], v[204:207], v[144:147], v[12:15]
s_add_u32 m0, s83, 33792
v_cvt_pk_bf16_f32 v250, v228, v229
v_mfma_f32_16x16x32_bf16 v[44:47], v[204:207], v[148:151], v[44:47]
buffer_load_dwordx4 v232, s[44:47], s80 offen lds
v_mfma_f32_16x16x32_bf16 v[76:79], v[204:207], v[152:155], v[76:79]
v_cvt_pk_bf16_f32 v251, v230, v231
v_accvgpr_write_b32 a148, v248
v_mfma_f32_16x16x32_bf16 v[108:111], v[204:207], v[156:159], v[108:111]
ds_read_b64_tr_b16 v[204:205], v243 offset:50688
ds_read_b64_tr_b16 v[206:207], v243 offset:51712
v_mfma_f32_16x16x32_bf16 v[16:19], v[208:211], v[144:147], v[16:19]
v_accvgpr_write_b32 a149, v249
v_accvgpr_write_b32 a150, v250
v_mfma_f32_16x16x32_bf16 v[48:51], v[208:211], v[148:151], v[48:51]
buffer_load_dwordx4 v[140:143], v247, s[52:55], s91 offen offset:0 sc1
v_mfma_f32_16x16x32_bf16 v[80:83], v[208:211], v[152:155], v[80:83]
v_accvgpr_write_b32 a151, v251
v_mfma_f32_16x16x32_bf16 v[112:115], v[208:211], v[156:159], v[112:115]
ds_read_b64_tr_b16 v[208:209], v240 offset:50816
ds_read_b64_tr_b16 v[210:211], v240 offset:51840
v_mfma_f32_16x16x32_bf16 v[20:23], v[212:215], v[144:147], v[20:23]
s_add_u32 m0, s83, 34816
v_mfma_f32_16x16x32_bf16 v[52:55], v[212:215], v[148:151], v[52:55]
buffer_load_dwordx4 v233, s[44:47], s80 offen lds
v_mfma_f32_16x16x32_bf16 v[84:87], v[212:215], v[152:155], v[84:87]
s_add_u32 s36, s36, s95
s_addc_u32 s37, s37, 0
v_mfma_f32_16x16x32_bf16 v[116:119], v[212:215], v[156:159], v[116:119]
ds_read_b64_tr_b16 v[212:213], v241 offset:50816
ds_read_b64_tr_b16 v[214:215], v241 offset:51840
v_mfma_f32_16x16x32_bf16 v[24:27], v[216:219], v[144:147], v[24:27]
s_and_b32 s37, s37, 0xffff
s_mov_b32 s38, s97
v_mfma_f32_16x16x32_bf16 v[56:59], v[216:219], v[148:151], v[56:59]
s_mov_b32 s39, 0x20000
s_lshl_b32 s93, s96, 6
v_mfma_f32_16x16x32_bf16 v[88:91], v[216:219], v[152:155], v[88:91]
s_mul_i32 s95, s94, 7332928
v_mfma_f32_16x16x32_bf16 v[120:123], v[216:219], v[156:159], v[120:123]
ds_read_b64_tr_b16 v[216:217], v242 offset:50816
ds_read_b64_tr_b16 v[218:219], v242 offset:51840
v_mfma_f32_16x16x32_bf16 v[28:31], v[220:223], v[144:147], v[28:31]
ds_write_b128 v238, v[248:251] offset:32768
v_mfma_f32_16x16x32_bf16 v[60:63], v[220:223], v[148:151], v[60:63]
s_cmp_eq_u32 s77, 0
s_cselect_b64 s[40:41], s[30:31], s[32:33]
v_mfma_f32_16x16x32_bf16 v[92:95], v[220:223], v[152:155], v[92:95]
s_waitcnt vmcnt(15) lgkmcnt(3)
v_mfma_f32_16x16x32_bf16 v[124:127], v[220:223], v[156:159], v[124:127]
ds_read_b64_tr_b16 v[220:221], v243 offset:50816
ds_read_b64_tr_b16 v[222:223], v243 offset:51840
v_mfma_f32_16x16x32_bf16 v[0:3], v[192:195], v[160:163], v[0:3]
.La5_LW1_7:
s_cbranch_execz .La5_LW1_11
v_accvgpr_read_b32 v224, a152
v_accvgpr_read_b32 v225, a153
v_mfma_f32_16x16x32_bf16 v[32:35], v[192:195], v[164:167], v[32:35]
buffer_load_dwordx4 v[144:147], v244, s[52:55], s91 offen offset:1024 sc1
v_mfma_f32_16x16x32_bf16 v[64:67], v[192:195], v[168:171], v[64:67]
v_accvgpr_read_b32 v226, a154
v_accvgpr_read_b32 v227, a155
v_mfma_f32_16x16x32_bf16 v[96:99], v[192:195], v[172:175], v[96:99]
ds_read_b64_tr_b16 v[192:193], v240 offset:59136
ds_read_b64_tr_b16 v[194:195], v240 offset:60160
v_mfma_f32_16x16x32_bf16 v[4:7], v[196:199], v[160:163], v[4:7]
v_accvgpr_read_b32 v228, a156
v_accvgpr_read_b32 v229, a157
v_mfma_f32_16x16x32_bf16 v[36:39], v[196:199], v[164:167], v[36:39]
buffer_load_dwordx4 v[148:151], v245, s[52:55], s91 offen offset:1024 sc1
v_mfma_f32_16x16x32_bf16 v[68:71], v[196:199], v[168:171], v[68:71]
s_waitcnt lgkmcnt(2)
v_accvgpr_read_b32 v230, a158
v_mfma_f32_16x16x32_bf16 v[100:103], v[196:199], v[172:175], v[100:103]
ds_read_b64_tr_b16 v[196:197], v241 offset:59136
ds_read_b64_tr_b16 v[198:199], v241 offset:60160
v_mfma_f32_16x16x32_bf16 v[8:11], v[200:203], v[160:163], v[8:11]
s_add_u32 m0, s83, 35968
v_accvgpr_read_b32 v231, a159
v_mfma_f32_16x16x32_bf16 v[40:43], v[200:203], v[164:167], v[40:43]
buffer_load_dwordx4 v234, s[44:47], s80 offen lds
v_mfma_f32_16x16x32_bf16 v[72:75], v[200:203], v[168:171], v[72:75]
v_cvt_pk_bf16_f32 v248, v224, v225
v_cvt_pk_bf16_f32 v249, v226, v227
v_mfma_f32_16x16x32_bf16 v[104:107], v[200:203], v[172:175], v[104:107]
ds_read_b64_tr_b16 v[200:201], v242 offset:59136
ds_read_b64_tr_b16 v[202:203], v242 offset:60160
v_mfma_f32_16x16x32_bf16 v[12:15], v[204:207], v[160:163], v[12:15]
v_cvt_pk_bf16_f32 v250, v228, v229
v_cvt_pk_bf16_f32 v251, v230, v231
v_mfma_f32_16x16x32_bf16 v[44:47], v[204:207], v[164:167], v[44:47]
buffer_load_dwordx4 v[152:155], v246, s[52:55], s91 offen offset:1024 sc1
v_mfma_f32_16x16x32_bf16 v[76:79], v[204:207], v[168:171], v[76:79]
v_accvgpr_write_b32 a152, v248
v_accvgpr_write_b32 a153, v249
v_mfma_f32_16x16x32_bf16 v[108:111], v[204:207], v[172:175], v[108:111]
ds_read_b64_tr_b16 v[204:205], v243 offset:59136
ds_read_b64_tr_b16 v[206:207], v243 offset:60160
v_mfma_f32_16x16x32_bf16 v[16:19], v[208:211], v[160:163], v[16:19]
s_add_u32 m0, s83, 36992
v_accvgpr_write_b32 a154, v250
v_mfma_f32_16x16x32_bf16 v[48:51], v[208:211], v[164:167], v[48:51]
buffer_load_dwordx4 v235, s[44:47], s80 offen lds
v_mfma_f32_16x16x32_bf16 v[80:83], v[208:211], v[168:171], v[80:83]
s_add_u32 s80, s80, 524288
v_accvgpr_write_b32 a155, v251
v_mfma_f32_16x16x32_bf16 v[112:115], v[208:211], v[172:175], v[112:115]
ds_read_b64_tr_b16 v[208:209], v240 offset:59264
ds_read_b64_tr_b16 v[210:211], v240 offset:60288
v_mfma_f32_16x16x32_bf16 v[20:23], v[212:215], v[160:163], v[20:23]
s_add_u32 s40, s40, s93
s_addc_u32 s41, s41, 0
v_mfma_f32_16x16x32_bf16 v[52:55], v[212:215], v[164:167], v[52:55]
buffer_load_dwordx4 v[156:159], v247, s[52:55], s91 offen offset:1024 sc1
v_mfma_f32_16x16x32_bf16 v[84:87], v[212:215], v[168:171], v[84:87]
s_and_b32 s41, s41, 0xffff
s_mov_b32 s42, s95
v_mfma_f32_16x16x32_bf16 v[116:119], v[212:215], v[172:175], v[116:119]
ds_read_b64_tr_b16 v[212:213], v241 offset:59264
ds_read_b64_tr_b16 v[214:215], v241 offset:60288
v_mfma_f32_16x16x32_bf16 v[24:27], v[216:219], v[160:163], v[24:27]
s_mov_b32 s43, 0x20000
s_mov_b32 s92, s72
v_mfma_f32_16x16x32_bf16 v[56:59], v[216:219], v[164:167], v[56:59]
s_xor_b32 s92, s92, 1
s_and_b32 s101, s92, 7
v_mfma_f32_16x16x32_bf16 v[88:91], v[216:219], v[168:171], v[88:91]
s_lshl_b32 s92, s101, 8
s_sub_u32 s93, 8388608, s92
v_mfma_f32_16x16x32_bf16 v[120:123], v[216:219], v[172:175], v[120:123]
ds_read_b64_tr_b16 v[216:217], v242 offset:59264
ds_read_b64_tr_b16 v[218:219], v242 offset:60288
v_mfma_f32_16x16x32_bf16 v[28:31], v[220:223], v[160:163], v[28:31]
ds_write_b128 v238, v[248:251] offset:33792
v_mfma_f32_16x16x32_bf16 v[60:63], v[220:223], v[164:167], v[60:63]
buffer_store_dwordx4 a[144:147], v255, s[48:51], s18 offen offset:3072 sc1
v_mfma_f32_16x16x32_bf16 v[92:95], v[220:223], v[168:171], v[92:95]
s_waitcnt vmcnt(14) lgkmcnt(3)
v_mfma_f32_16x16x32_bf16 v[124:127], v[220:223], v[172:175], v[124:127]
ds_read_b64_tr_b16 v[220:221], v243 offset:59264
ds_read_b64_tr_b16 v[222:223], v243 offset:60288
v_mfma_f32_16x16x32_bf16 v[0:3], v[192:195], v[176:179], v[0:3]
v_accvgpr_read_b32 v224, a96
v_accvgpr_read_b32 v225, a97
v_mfma_f32_16x16x32_bf16 v[32:35], v[192:195], v[180:183], v[32:35]
buffer_load_dwordx4 v[160:163], v244, s[52:55], s91 offen offset:2048 sc1
v_mfma_f32_16x16x32_bf16 v[64:67], v[192:195], v[184:187], v[64:67]
v_accvgpr_read_b32 v226, a98
v_accvgpr_read_b32 v227, a99
v_mfma_f32_16x16x32_bf16 v[96:99], v[192:195], v[188:191], v[96:99]
v_accvgpr_read_b32 v228, a100
v_accvgpr_read_b32 v229, a101
v_mfma_f32_16x16x32_bf16 v[4:7], v[196:199], v[176:179], v[4:7]
s_waitcnt lgkmcnt(0)
s_barrier
v_mfma_f32_16x16x32_bf16 v[36:39], v[196:199], v[180:183], v[36:39]
buffer_load_dwordx4 v[164:167], v245, s[52:55], s91 offen offset:2048 sc1
v_mfma_f32_16x16x32_bf16 v[68:71], v[196:199], v[184:187], v[68:71]
ds_read_b64_tr_b16 v[192:193], v240 offset:0
ds_read_b64_tr_b16 v[194:195], v240 offset:1024
v_mfma_f32_16x16x32_bf16 v[100:103], v[196:199], v[188:191], v[100:103]
ds_read_b64_tr_b16 v[196:197], v241 offset:0
ds_read_b64_tr_b16 v[198:199], v241 offset:1024
v_mfma_f32_16x16x32_bf16 v[8:11], v[200:203], v[176:179], v[8:11]
v_accvgpr_read_b32 v230, a102
v_accvgpr_read_b32 v231, a103
v_mfma_f32_16x16x32_bf16 v[40:43], v[200:203], v[180:183], v[40:43]
buffer_load_dwordx4 v[168:171], v246, s[52:55], s91 offen offset:2048 sc1
v_mfma_f32_16x16x32_bf16 v[72:75], v[200:203], v[184:187], v[72:75]
v_cvt_pk_bf16_f32 v248, v224, v225
v_cvt_pk_bf16_f32 v249, v226, v227
v_mfma_f32_16x16x32_bf16 v[104:107], v[200:203], v[188:191], v[104:107]
ds_read_b64_tr_b16 v[200:201], v242 offset:0
ds_read_b64_tr_b16 v[202:203], v242 offset:1024
v_mfma_f32_16x16x32_bf16 v[12:15], v[204:207], v[176:179], v[12:15]
s_add_u32 m0, s83, 50688
v_cvt_pk_bf16_f32 v250, v228, v229
v_mfma_f32_16x16x32_bf16 v[44:47], v[204:207], v[180:183], v[44:47]
buffer_load_dwordx4 v232, s[44:47], s80 offen lds
v_mfma_f32_16x16x32_bf16 v[76:79], v[204:207], v[184:187], v[76:79]
v_cvt_pk_bf16_f32 v251, v230, v231
v_accvgpr_write_b32 a156, v248
v_mfma_f32_16x16x32_bf16 v[108:111], v[204:207], v[188:191], v[108:111]
ds_read_b64_tr_b16 v[204:205], v243 offset:0
ds_read_b64_tr_b16 v[206:207], v243 offset:1024
v_mfma_f32_16x16x32_bf16 v[16:19], v[208:211], v[176:179], v[16:19]
v_accvgpr_write_b32 a157, v249
v_accvgpr_write_b32 a158, v250
v_mfma_f32_16x16x32_bf16 v[48:51], v[208:211], v[180:183], v[48:51]
buffer_load_dwordx4 v[172:175], v247, s[52:55], s91 offen offset:2048 sc1
v_mfma_f32_16x16x32_bf16 v[80:83], v[208:211], v[184:187], v[80:83]
v_accvgpr_write_b32 a159, v251
v_mfma_f32_16x16x32_bf16 v[112:115], v[208:211], v[188:191], v[112:115]
ds_read_b64_tr_b16 v[208:209], v240 offset:128
ds_read_b64_tr_b16 v[210:211], v240 offset:1152
v_mfma_f32_16x16x32_bf16 v[20:23], v[212:215], v[176:179], v[20:23]
s_add_u32 m0, s83, 51712
v_mfma_f32_16x16x32_bf16 v[52:55], v[212:215], v[180:183], v[52:55]
buffer_load_dwordx4 v233, s[44:47], s80 offen lds
v_mfma_f32_16x16x32_bf16 v[84:87], v[212:215], v[184:187], v[84:87]
s_add_u32 s56, s28, s92
s_addc_u32 s57, s29, 0
v_mfma_f32_16x16x32_bf16 v[116:119], v[212:215], v[188:191], v[116:119]
ds_read_b64_tr_b16 v[212:213], v241 offset:128
ds_read_b64_tr_b16 v[214:215], v241 offset:1152
v_mfma_f32_16x16x32_bf16 v[24:27], v[216:219], v[176:179], v[24:27]
s_and_b32 s57, s57, 0xffff
s_mov_b32 s58, s93
v_mfma_f32_16x16x32_bf16 v[56:59], v[216:219], v[180:183], v[56:59]
s_mov_b32 s59, 0x20000
s_lshl_b32 s93, s96, 16
v_mfma_f32_16x16x32_bf16 v[88:91], v[216:219], v[184:187], v[88:91]
s_mov_b32 s18, 32768
s_lshl_b32 s95, s94, 16
v_mfma_f32_16x16x32_bf16 v[120:123], v[216:219], v[188:191], v[120:123]
ds_read_b64_tr_b16 v[216:217], v242 offset:128
ds_read_b64_tr_b16 v[218:219], v242 offset:1152
v_mfma_f32_16x16x32_bf16 v[28:31], v[220:223], v[176:179], v[28:31]
ds_write_b128 v238, v[248:251] offset:34816
v_mfma_f32_16x16x32_bf16 v[60:63], v[220:223], v[180:183], v[60:63]
buffer_store_dwordx4 a[148:151], v255, s[48:51], s18 offen offset:0 sc1
v_mfma_f32_16x16x32_bf16 v[92:95], v[220:223], v[184:187], v[92:95]
s_waitcnt vmcnt(15) lgkmcnt(3)
v_mfma_f32_16x16x32_bf16 v[124:127], v[220:223], v[188:191], v[124:127]
ds_read_b64_tr_b16 v[220:221], v243 offset:128
ds_read_b64_tr_b16 v[222:223], v243 offset:1152
v_mfma_f32_16x16x32_bf16 v[0:3], v[192:195], v[128:131], v[0:3]
v_accvgpr_read_b32 v224, a160
v_accvgpr_read_b32 v225, a161
v_mfma_f32_16x16x32_bf16 v[32:35], v[192:195], v[132:135], v[32:35]
buffer_load_dwordx4 v[176:179], v244, s[52:55], s91 offen offset:3072 sc1
v_mfma_f32_16x16x32_bf16 v[64:67], v[192:195], v[136:139], v[64:67]
v_accvgpr_read_b32 v226, a162
v_accvgpr_read_b32 v227, a163
v_mfma_f32_16x16x32_bf16 v[96:99], v[192:195], v[140:143], v[96:99]
ds_read_b64_tr_b16 v[192:193], v240 offset:8448
ds_read_b64_tr_b16 v[194:195], v240 offset:9472
v_mfma_f32_16x16x32_bf16 v[4:7], v[196:199], v[128:131], v[4:7]
v_accvgpr_read_b32 v228, a164
v_accvgpr_read_b32 v229, a165
v_mfma_f32_16x16x32_bf16 v[36:39], v[196:199], v[132:135], v[36:39]
buffer_load_dwordx4 v[180:183], v245, s[52:55], s91 offen offset:3072 sc1
v_mfma_f32_16x16x32_bf16 v[68:71], v[196:199], v[136:139], v[68:71]
s_waitcnt lgkmcnt(2)
v_accvgpr_read_b32 v230, a166
v_mfma_f32_16x16x32_bf16 v[100:103], v[196:199], v[140:143], v[100:103]
ds_read_b64_tr_b16 v[196:197], v241 offset:8448
ds_read_b64_tr_b16 v[198:199], v241 offset:9472
v_mfma_f32_16x16x32_bf16 v[8:11], v[200:203], v[128:131], v[8:11]
s_add_u32 m0, s83, 52864
v_accvgpr_read_b32 v231, a167
v_mfma_f32_16x16x32_bf16 v[40:43], v[200:203], v[132:135], v[40:43]
buffer_load_dwordx4 v234, s[44:47], s80 offen lds
v_mfma_f32_16x16x32_bf16 v[72:75], v[200:203], v[136:139], v[72:75]
v_cvt_pk_bf16_f32 v248, v224, v225
v_cvt_pk_bf16_f32 v249, v226, v227
v_mfma_f32_16x16x32_bf16 v[104:107], v[200:203], v[140:143], v[104:107]
ds_read_b64_tr_b16 v[200:201], v242 offset:8448
ds_read_b64_tr_b16 v[202:203], v242 offset:9472
v_mfma_f32_16x16x32_bf16 v[12:15], v[204:207], v[128:131], v[12:15]
v_cvt_pk_bf16_f32 v250, v228, v229
v_cvt_pk_bf16_f32 v251, v230, v231
v_mfma_f32_16x16x32_bf16 v[44:47], v[204:207], v[132:135], v[44:47]
buffer_load_dwordx4 v[184:187], v246, s[52:55], s91 offen offset:3072 sc1
v_mfma_f32_16x16x32_bf16 v[76:79], v[204:207], v[136:139], v[76:79]
v_accvgpr_write_b32 a160, v248
v_accvgpr_write_b32 a161, v249
v_mfma_f32_16x16x32_bf16 v[108:111], v[204:207], v[140:143], v[108:111]
ds_read_b64_tr_b16 v[204:205], v243 offset:8448
ds_read_b64_tr_b16 v[206:207], v243 offset:9472
v_mfma_f32_16x16x32_bf16 v[16:19], v[208:211], v[128:131], v[16:19]
s_add_u32 m0, s83, 53888
v_accvgpr_write_b32 a162, v250
v_mfma_f32_16x16x32_bf16 v[48:51], v[208:211], v[132:135], v[48:51]
buffer_load_dwordx4 v235, s[44:47], s80 offen lds
v_mfma_f32_16x16x32_bf16 v[80:83], v[208:211], v[136:139], v[80:83]
s_add_u32 s80, s80, 524288
v_accvgpr_write_b32 a163, v251
v_mfma_f32_16x16x32_bf16 v[112:115], v[208:211], v[140:143], v[112:115]
ds_read_b64_tr_b16 v[208:209], v240 offset:8576
ds_read_b64_tr_b16 v[210:211], v240 offset:9600
v_mfma_f32_16x16x32_bf16 v[20:23], v[212:215], v[128:131], v[20:23]
s_add_u32 s28, s34, s93
s_addc_u32 s29, s35, 0
v_mfma_f32_16x16x32_bf16 v[52:55], v[212:215], v[132:135], v[52:55]
buffer_load_dwordx4 v[188:191], v247, s[52:55], s91 offen offset:3072 sc1
v_mfma_f32_16x16x32_bf16 v[84:87], v[212:215], v[136:139], v[84:87]
s_add_u32 s91, s91, 4096
s_and_b32 s29, s29, 0xffff
v_mfma_f32_16x16x32_bf16 v[116:119], v[212:215], v[140:143], v[116:119]
ds_read_b64_tr_b16 v[212:213], v241 offset:8576
ds_read_b64_tr_b16 v[214:215], v241 offset:9600
v_mfma_f32_16x16x32_bf16 v[24:27], v[216:219], v[128:131], v[24:27]
s_mov_b32 s30, s95
s_mov_b32 s31, 0x20000
v_mfma_f32_16x16x32_bf16 v[56:59], v[216:219], v[132:135], v[56:59]
s_lshl_b32 s93, s101, 12
s_add_u32 s92, s93, 71303168
v_mfma_f32_16x16x32_bf16 v[88:91], v[216:219], v[136:139], v[88:91]
s_sub_u32 s93, 25165824, s93
v_mfma_f32_16x16x32_bf16 v[120:123], v[216:219], v[140:143], v[120:123]
ds_read_b64_tr_b16 v[216:217], v242 offset:8576
ds_read_b64_tr_b16 v[218:219], v242 offset:9600
v_mfma_f32_16x16x32_bf16 v[28:31], v[220:223], v[128:131], v[28:31]
ds_write_b128 v238, v[248:251] offset:35840
v_mfma_f32_16x16x32_bf16 v[60:63], v[220:223], v[132:135], v[60:63]
buffer_store_dwordx4 a[152:155], v255, s[48:51], s18 offen offset:1024 sc1
v_mfma_f32_16x16x32_bf16 v[92:95], v[220:223], v[136:139], v[92:95]
s_waitcnt vmcnt(15) lgkmcnt(3)
v_mfma_f32_16x16x32_bf16 v[124:127], v[220:223], v[140:143], v[124:127]
ds_read_b64_tr_b16 v[220:221], v243 offset:8576
ds_read_b64_tr_b16 v[222:223], v243 offset:9600
v_mfma_f32_16x16x32_bf16 v[0:3], v[192:195], v[144:147], v[0:3]
v_accvgpr_read_b32 v224, a104
v_accvgpr_read_b32 v225, a105
v_mfma_f32_16x16x32_bf16 v[32:35], v[192:195], v[148:151], v[32:35]
buffer_load_dwordx4 v[128:131], v244, s[52:55], s91 offen offset:0 sc1
v_mfma_f32_16x16x32_bf16 v[64:67], v[192:195], v[152:155], v[64:67]
v_accvgpr_read_b32 v226, a106
v_accvgpr_read_b32 v227, a107
v_mfma_f32_16x16x32_bf16 v[96:99], v[192:195], v[156:159], v[96:99]
v_accvgpr_read_b32 v228, a108
v_accvgpr_read_b32 v229, a109
v_mfma_f32_16x16x32_bf16 v[4:7], v[196:199], v[144:147], v[4:7]
s_waitcnt lgkmcnt(0)
s_barrier
v_mfma_f32_16x16x32_bf16 v[36:39], v[196:199], v[148:151], v[36:39]
buffer_load_dwordx4 v[132:135], v245, s[52:55], s91 offen offset:0 sc1
v_mfma_f32_16x16x32_bf16 v[68:71], v[196:199], v[152:155], v[68:71]
ds_read_b64_tr_b16 v[192:193], v240 offset:16896
ds_read_b64_tr_b16 v[194:195], v240 offset:17920
v_mfma_f32_16x16x32_bf16 v[100:103], v[196:199], v[156:159], v[100:103]
ds_read_b64_tr_b16 v[196:197], v241 offset:16896
ds_read_b64_tr_b16 v[198:199], v241 offset:17920
v_mfma_f32_16x16x32_bf16 v[8:11], v[200:203], v[144:147], v[8:11]
v_accvgpr_read_b32 v230, a110
v_accvgpr_read_b32 v231, a111
v_mfma_f32_16x16x32_bf16 v[40:43], v[200:203], v[148:151], v[40:43]
buffer_load_dwordx4 v[136:139], v246, s[52:55], s91 offen offset:0 sc1
v_mfma_f32_16x16x32_bf16 v[72:75], v[200:203], v[152:155], v[72:75]
v_cvt_pk_bf16_f32 v248, v224, v225
v_cvt_pk_bf16_f32 v249, v226, v227
v_mfma_f32_16x16x32_bf16 v[104:107], v[200:203], v[156:159], v[104:107]
ds_read_b64_tr_b16 v[200:201], v242 offset:16896
ds_read_b64_tr_b16 v[202:203], v242 offset:17920
v_mfma_f32_16x16x32_bf16 v[12:15], v[204:207], v[144:147], v[12:15]
s_add_u32 m0, s83, 0
v_cvt_pk_bf16_f32 v250, v228, v229
v_mfma_f32_16x16x32_bf16 v[44:47], v[204:207], v[148:151], v[44:47]
buffer_load_dwordx4 v232, s[44:47], s80 offen lds
v_mfma_f32_16x16x32_bf16 v[76:79], v[204:207], v[152:155], v[76:79]
v_cvt_pk_bf16_f32 v251, v230, v231
v_accvgpr_write_b32 a164, v248
v_mfma_f32_16x16x32_bf16 v[108:111], v[204:207], v[156:159], v[108:111]
ds_read_b64_tr_b16 v[204:205], v243 offset:16896
ds_read_b64_tr_b16 v[206:207], v243 offset:17920
v_mfma_f32_16x16x32_bf16 v[16:19], v[208:211], v[144:147], v[16:19]
v_accvgpr_write_b32 a165, v249
v_accvgpr_write_b32 a166, v250
v_mfma_f32_16x16x32_bf16 v[48:51], v[208:211], v[148:151], v[48:51]
buffer_load_dwordx4 v[140:143], v247, s[52:55], s91 offen offset:0 sc1
v_mfma_f32_16x16x32_bf16 v[80:83], v[208:211], v[152:155], v[80:83]
v_accvgpr_write_b32 a167, v251
v_mfma_f32_16x16x32_bf16 v[112:115], v[208:211], v[156:159], v[112:115]
ds_read_b64_tr_b16 v[208:209], v240 offset:17024
ds_read_b64_tr_b16 v[210:211], v240 offset:18048
v_mfma_f32_16x16x32_bf16 v[20:23], v[212:215], v[144:147], v[20:23]
s_add_u32 m0, s83, 1024
v_mfma_f32_16x16x32_bf16 v[52:55], v[212:215], v[148:151], v[52:55]
buffer_load_dwordx4 v233, s[44:47], s80 offen lds
v_mfma_f32_16x16x32_bf16 v[84:87], v[212:215], v[152:155], v[84:87]
s_add_u32 s32, s26, s92
s_addc_u32 s33, s27, 0
v_mfma_f32_16x16x32_bf16 v[116:119], v[212:215], v[156:159], v[116:119]
ds_read_b64_tr_b16 v[212:213], v241 offset:17024
ds_read_b64_tr_b16 v[214:215], v241 offset:18048
v_mfma_f32_16x16x32_bf16 v[24:27], v[216:219], v[144:147], v[24:27]
s_and_b32 s33, s33, 0xffff
s_mov_b32 s34, s93
v_mfma_f32_16x16x32_bf16 v[56:59], v[216:219], v[148:151], v[56:59]
s_mov_b32 s35, 0x20000
s_lshl2_add_u32 s92, s72, s74
v_mfma_f32_16x16x32_bf16 v[88:91], v[216:219], v[152:155], v[88:91]
s_lshl2_add_u32 s92, s92, s3
s_lshl_b32 s88, s92, 8
v_mfma_f32_16x16x32_bf16 v[120:123], v[216:219], v[156:159], v[120:123]
ds_read_b64_tr_b16 v[216:217], v242 offset:17024
ds_read_b64_tr_b16 v[218:219], v242 offset:18048
v_mfma_f32_16x16x32_bf16 v[28:31], v[220:223], v[144:147], v[28:31]
ds_write_b128 v238, v[248:251] offset:36864
v_mfma_f32_16x16x32_bf16 v[60:63], v[220:223], v[148:151], v[60:63]
buffer_store_dwordx4 a[156:159], v255, s[48:51], s18 offen offset:2048 sc1
v_mfma_f32_16x16x32_bf16 v[92:95], v[220:223], v[152:155], v[92:95]
s_waitcnt vmcnt(16) lgkmcnt(3)
s_mul_i32 s93, s92, 17408
v_mfma_f32_16x16x32_bf16 v[124:127], v[220:223], v[156:159], v[124:127]
ds_read_b64_tr_b16 v[220:221], v243 offset:17024
ds_read_b64_tr_b16 v[222:223], v243 offset:18048
v_mfma_f32_16x16x32_bf16 v[0:3], v[192:195], v[160:163], v[0:3]
v_accvgpr_read_b32 v224, a168
v_accvgpr_read_b32 v225, a169
v_mfma_f32_16x16x32_bf16 v[32:35], v[192:195], v[164:167], v[32:35]
buffer_load_dwordx4 v[144:147], v244, s[52:55], s91 offen offset:1024 sc1
v_mfma_f32_16x16x32_bf16 v[64:67], v[192:195], v[168:171], v[64:67]
v_accvgpr_read_b32 v226, a170
v_accvgpr_read_b32 v227, a171
v_mfma_f32_16x16x32_bf16 v[96:99], v[192:195], v[172:175], v[96:99]
ds_read_b64_tr_b16 v[192:193], v240 offset:25344
ds_read_b64_tr_b16 v[194:195], v240 offset:26368
v_mfma_f32_16x16x32_bf16 v[4:7], v[196:199], v[160:163], v[4:7]
v_accvgpr_read_b32 v228, a172
v_accvgpr_read_b32 v229, a173
v_mfma_f32_16x16x32_bf16 v[36:39], v[196:199], v[164:167], v[36:39]
buffer_load_dwordx4 v[148:151], v245, s[52:55], s91 offen offset:1024 sc1
v_mfma_f32_16x16x32_bf16 v[68:71], v[196:199], v[168:171], v[68:71]
s_waitcnt lgkmcnt(2)
v_accvgpr_read_b32 v230, a174
v_mfma_f32_16x16x32_bf16 v[100:103], v[196:199], v[172:175], v[100:103]
ds_read_b64_tr_b16 v[196:197], v241 offset:25344
ds_read_b64_tr_b16 v[198:199], v241 offset:26368
v_mfma_f32_16x16x32_bf16 v[8:11], v[200:203], v[160:163], v[8:11]
s_add_u32 m0, s83, 2176
v_accvgpr_read_b32 v231, a175
v_mfma_f32_16x16x32_bf16 v[40:43], v[200:203], v[164:167], v[40:43]
buffer_load_dwordx4 v234, s[44:47], s80 offen lds
v_mfma_f32_16x16x32_bf16 v[72:75], v[200:203], v[168:171], v[72:75]
v_cvt_pk_bf16_f32 v248, v224, v225
v_cvt_pk_bf16_f32 v249, v226, v227
v_mfma_f32_16x16x32_bf16 v[104:107], v[200:203], v[172:175], v[104:107]
ds_read_b64_tr_b16 v[200:201], v242 offset:25344
ds_read_b64_tr_b16 v[202:203], v242 offset:26368
v_mfma_f32_16x16x32_bf16 v[12:15], v[204:207], v[160:163], v[12:15]
v_cvt_pk_bf16_f32 v250, v228, v229
v_cvt_pk_bf16_f32 v251, v230, v231
v_mfma_f32_16x16x32_bf16 v[44:47], v[204:207], v[164:167], v[44:47]
buffer_load_dwordx4 v[152:155], v246, s[52:55], s91 offen offset:1024 sc1
v_mfma_f32_16x16x32_bf16 v[76:79], v[204:207], v[168:171], v[76:79]
v_accvgpr_write_b32 a168, v248
v_accvgpr_write_b32 a169, v249
v_mfma_f32_16x16x32_bf16 v[108:111], v[204:207], v[172:175], v[108:111]
ds_read_b64_tr_b16 v[204:205], v243 offset:25344
ds_read_b64_tr_b16 v[206:207], v243 offset:26368
v_mfma_f32_16x16x32_bf16 v[16:19], v[208:211], v[160:163], v[16:19]
s_add_u32 m0, s83, 3200
v_accvgpr_write_b32 a170, v250
v_mfma_f32_16x16x32_bf16 v[48:51], v[208:211], v[164:167], v[48:51]
buffer_load_dwordx4 v235, s[44:47], s80 offen lds
v_mfma_f32_16x16x32_bf16 v[80:83], v[208:211], v[168:171], v[80:83]
s_add_u32 s80, s80, 524288
v_accvgpr_write_b32 a171, v251
v_mfma_f32_16x16x32_bf16 v[112:115], v[208:211], v[172:175], v[112:115]
ds_read_b64_tr_b16 v[208:209], v240 offset:25472
ds_read_b64_tr_b16 v[210:211], v240 offset:26496
v_mfma_f32_16x16x32_bf16 v[20:23], v[212:215], v[160:163], v[20:23]
s_lshl_b32 s92, s92, 14
s_add_u32 s92, s92, 4194304
v_mfma_f32_16x16x32_bf16 v[52:55], v[212:215], v[164:167], v[52:55]
buffer_load_dwordx4 v[156:159], v247, s[52:55], s91 offen offset:1024 sc1
v_mfma_f32_16x16x32_bf16 v[84:87], v[212:215], v[168:171], v[84:87]
s_add_u32 s86, s26, s92
s_addc_u32 s87, s27, 0
v_mfma_f32_16x16x32_bf16 v[116:119], v[212:215], v[172:175], v[116:119]
ds_read_b64_tr_b16 v[212:213], v241 offset:25472
ds_read_b64_tr_b16 v[214:215], v241 offset:26496
v_mfma_f32_16x16x32_bf16 v[24:27], v[216:219], v[160:163], v[24:27]
s_and_b32 s87, s87, 0xffff
s_add_u32 s93, s93, 150994944
v_mfma_f32_16x16x32_bf16 v[56:59], v[216:219], v[164:167], v[56:59]
s_add_u32 s68, s26, s93
s_addc_u32 s69, s27, 0
v_mfma_f32_16x16x32_bf16 v[88:91], v[216:219], v[168:171], v[88:91]
s_and_b32 s69, s69, 0xffff
s_mov_b32 s70, 17408
v_mfma_f32_16x16x32_bf16 v[120:123], v[216:219], v[172:175], v[120:123]
ds_read_b64_tr_b16 v[216:217], v242 offset:25472
ds_read_b64_tr_b16 v[218:219], v242 offset:26496
v_mfma_f32_16x16x32_bf16 v[28:31], v[220:223], v[160:163], v[28:31]
ds_write_b128 v238, v[248:251] offset:37888
v_mfma_f32_16x16x32_bf16 v[60:63], v[220:223], v[164:167], v[60:63]
buffer_store_dwordx4 a[160:163], v255, s[48:51], s18 offen offset:3072 sc1
v_mfma_f32_16x16x32_bf16 v[92:95], v[220:223], v[168:171], v[92:95]
s_waitcnt vmcnt(15) lgkmcnt(3)
s_mov_b32 s96, 152962078
v_mfma_f32_16x16x32_bf16 v[124:127], v[220:223], v[172:175], v[124:127]
ds_read_b64_tr_b16 v[220:221], v243 offset:25472
ds_read_b64_tr_b16 v[222:223], v243 offset:26496
v_mfma_f32_16x16x32_bf16 v[0:3], v[192:195], v[176:179], v[0:3]
v_accvgpr_read_b32 v224, a112
v_accvgpr_read_b32 v225, a113
v_mfma_f32_16x16x32_bf16 v[32:35], v[192:195], v[180:183], v[32:35]
buffer_load_dwordx4 v[160:163], v244, s[52:55], s91 offen offset:2048 sc1
v_mfma_f32_16x16x32_bf16 v[64:67], v[192:195], v[184:187], v[64:67]
v_accvgpr_read_b32 v226, a114
v_accvgpr_read_b32 v227, a115
v_mfma_f32_16x16x32_bf16 v[96:99], v[192:195], v[188:191], v[96:99]
v_accvgpr_read_b32 v228, a116
v_accvgpr_read_b32 v229, a117
v_mfma_f32_16x16x32_bf16 v[4:7], v[196:199], v[176:179], v[4:7]
s_waitcnt lgkmcnt(0)
s_barrier
v_mfma_f32_16x16x32_bf16 v[36:39], v[196:199], v[180:183], v[36:39]
buffer_load_dwordx4 v[164:167], v245, s[52:55], s91 offen offset:2048 sc1
v_mfma_f32_16x16x32_bf16 v[68:71], v[196:199], v[184:187], v[68:71]
ds_read_b64_tr_b16 v[192:193], v240 offset:33792
ds_read_b64_tr_b16 v[194:195], v240 offset:34816
v_mfma_f32_16x16x32_bf16 v[100:103], v[196:199], v[188:191], v[100:103]
ds_read_b64_tr_b16 v[196:197], v241 offset:33792
ds_read_b64_tr_b16 v[198:199], v241 offset:34816
v_mfma_f32_16x16x32_bf16 v[8:11], v[200:203], v[176:179], v[8:11]
v_accvgpr_read_b32 v230, a118
v_accvgpr_read_b32 v231, a119
v_mfma_f32_16x16x32_bf16 v[40:43], v[200:203], v[180:183], v[40:43]
buffer_load_dwordx4 v[168:171], v246, s[52:55], s91 offen offset:2048 sc1
v_mfma_f32_16x16x32_bf16 v[72:75], v[200:203], v[184:187], v[72:75]
v_cvt_pk_bf16_f32 v248, v224, v225
v_cvt_pk_bf16_f32 v249, v226, v227
v_mfma_f32_16x16x32_bf16 v[104:107], v[200:203], v[188:191], v[104:107]
ds_read_b64_tr_b16 v[200:201], v242 offset:33792
ds_read_b64_tr_b16 v[202:203], v242 offset:34816
v_mfma_f32_16x16x32_bf16 v[12:15], v[204:207], v[176:179], v[12:15]
s_add_u32 m0, s83, 16896
v_cvt_pk_bf16_f32 v250, v228, v229
v_mfma_f32_16x16x32_bf16 v[44:47], v[204:207], v[180:183], v[44:47]
buffer_load_dwordx4 v232, s[44:47], s80 offen lds
v_mfma_f32_16x16x32_bf16 v[76:79], v[204:207], v[184:187], v[76:79]
v_cvt_pk_bf16_f32 v251, v230, v231
v_accvgpr_write_b32 a172, v248
v_mfma_f32_16x16x32_bf16 v[108:111], v[204:207], v[188:191], v[108:111]
ds_read_b64_tr_b16 v[204:205], v243 offset:33792
ds_read_b64_tr_b16 v[206:207], v243 offset:34816
v_mfma_f32_16x16x32_bf16 v[16:19], v[208:211], v[176:179], v[16:19]
v_accvgpr_write_b32 a173, v249
v_accvgpr_write_b32 a174, v250
v_mfma_f32_16x16x32_bf16 v[48:51], v[208:211], v[180:183], v[48:51]
buffer_load_dwordx4 v[172:175], v247, s[52:55], s91 offen offset:2048 sc1
v_mfma_f32_16x16x32_bf16 v[80:83], v[208:211], v[184:187], v[80:83]
v_accvgpr_write_b32 a175, v251
s_mov_b32 s97, 371069214
v_mfma_f32_16x16x32_bf16 v[112:115], v[208:211], v[188:191], v[112:115]
ds_read_b64_tr_b16 v[208:209], v240 offset:33920
ds_read_b64_tr_b16 v[210:211], v240 offset:34944
v_mfma_f32_16x16x32_bf16 v[20:23], v[212:215], v[176:179], v[20:23]
s_add_u32 m0, s83, 17920
s_lshl_b32 s95, s73, 3
v_mfma_f32_16x16x32_bf16 v[52:55], v[212:215], v[180:183], v[52:55]
buffer_load_dwordx4 v233, s[44:47], s80 offen lds
v_mfma_f32_16x16x32_bf16 v[84:87], v[212:215], v[184:187], v[84:87]
s_lshr_b64 s[96:97], s[96:97], s95
s_and_b32 s82, s96, 255
v_mfma_f32_16x16x32_bf16 v[116:119], v[212:215], v[188:191], v[116:119]
ds_read_b64_tr_b16 v[212:213], v241 offset:33920
ds_read_b64_tr_b16 v[214:215], v241 offset:34944
v_mfma_f32_16x16x32_bf16 v[24:27], v[216:219], v[176:179], v[24:27]
s_lshl_b32 s95, 1, s82
s_lshl_b32 s96, s75, 22
v_mfma_f32_16x16x32_bf16 v[56:59], v[216:219], v[180:183], v[56:59]
s_or_b32 s95, s95, s96
v_mfma_f32_16x16x32_bf16 v[88:91], v[216:219], v[184:187], v[88:91]
s_cmp_eq_u32 s73, 1
s_cselect_b32 s96, 12, 4
s_cselect_b32 s79, 0, 0xff000000
v_mfma_f32_16x16x32_bf16 v[120:123], v[216:219], v[188:191], v[120:123]
ds_read_b64_tr_b16 v[216:217], v242 offset:33920
ds_read_b64_tr_b16 v[218:219], v242 offset:34944
v_mfma_f32_16x16x32_bf16 v[28:31], v[220:223], v[176:179], v[28:31]
ds_write_b128 v238, v[248:251] offset:38912
v_mfma_f32_16x16x32_bf16 v[60:63], v[220:223], v[180:183], v[60:63]
s_or_b32 s98, s95, s96
s_mov_b32 s99, 0
v_mfma_f32_16x16x32_bf16 v[92:95], v[220:223], v[184:187], v[92:95]
s_waitcnt vmcnt(15) lgkmcnt(3)
v_mfma_f32_16x16x32_bf16 v[124:127], v[220:223], v[188:191], v[124:127]
ds_read_b64_tr_b16 v[220:221], v243 offset:33920
ds_read_b64_tr_b16 v[222:223], v243 offset:34944
v_mfma_f32_16x16x32_bf16 v[0:3], v[192:195], v[128:131], v[0:3]
v_accvgpr_read_b32 v224, a176
v_accvgpr_read_b32 v225, a177
v_mfma_f32_16x16x32_bf16 v[32:35], v[192:195], v[132:135], v[32:35]
buffer_load_dwordx4 v[176:179], v244, s[52:55], s91 offen offset:3072 sc1
v_mfma_f32_16x16x32_bf16 v[64:67], v[192:195], v[136:139], v[64:67]
v_accvgpr_read_b32 v226, a178
v_accvgpr_read_b32 v227, a179
v_mfma_f32_16x16x32_bf16 v[96:99], v[192:195], v[140:143], v[96:99]
ds_read_b64_tr_b16 v[192:193], v240 offset:42240
ds_read_b64_tr_b16 v[194:195], v240 offset:43264
v_mfma_f32_16x16x32_bf16 v[4:7], v[196:199], v[128:131], v[4:7]
v_accvgpr_read_b32 v228, a180
v_accvgpr_read_b32 v229, a181
v_mfma_f32_16x16x32_bf16 v[36:39], v[196:199], v[132:135], v[36:39]
buffer_load_dwordx4 v[180:183], v245, s[52:55], s91 offen offset:3072 sc1
v_mfma_f32_16x16x32_bf16 v[68:71], v[196:199], v[136:139], v[68:71]
s_waitcnt lgkmcnt(2)
v_accvgpr_read_b32 v230, a182
.La5_LW1_8:
s_cbranch_execz .La5_LW1_R0
v_mfma_f32_16x16x32_bf16 v[100:103], v[196:199], v[140:143], v[100:103]
ds_read_b64_tr_b16 v[196:197], v241 offset:42240
ds_read_b64_tr_b16 v[198:199], v241 offset:43264
v_mfma_f32_16x16x32_bf16 v[8:11], v[200:203], v[128:131], v[8:11]
s_add_u32 m0, s83, 19072
v_accvgpr_read_b32 v231, a183
v_mfma_f32_16x16x32_bf16 v[40:43], v[200:203], v[132:135], v[40:43]
buffer_load_dwordx4 v234, s[44:47], s80 offen lds
v_mfma_f32_16x16x32_bf16 v[72:75], v[200:203], v[136:139], v[72:75]
v_cvt_pk_bf16_f32 v248, v224, v225
v_cvt_pk_bf16_f32 v249, v226, v227
v_mfma_f32_16x16x32_bf16 v[104:107], v[200:203], v[140:143], v[104:107]
ds_read_b64_tr_b16 v[200:201], v242 offset:42240
ds_read_b64_tr_b16 v[202:203], v242 offset:43264
v_mfma_f32_16x16x32_bf16 v[12:15], v[204:207], v[128:131], v[12:15]
v_cvt_pk_bf16_f32 v250, v228, v229
v_cvt_pk_bf16_f32 v251, v230, v231
v_mfma_f32_16x16x32_bf16 v[44:47], v[204:207], v[132:135], v[44:47]
buffer_load_dwordx4 v[184:187], v246, s[52:55], s91 offen offset:3072 sc1
v_mfma_f32_16x16x32_bf16 v[76:79], v[204:207], v[136:139], v[76:79]
v_accvgpr_write_b32 a176, v248
v_accvgpr_write_b32 a177, v249
v_mfma_f32_16x16x32_bf16 v[108:111], v[204:207], v[140:143], v[108:111]
ds_read_b64_tr_b16 v[204:205], v243 offset:42240
ds_read_b64_tr_b16 v[206:207], v243 offset:43264
v_mfma_f32_16x16x32_bf16 v[16:19], v[208:211], v[128:131], v[16:19]
s_add_u32 m0, s83, 20096
v_accvgpr_write_b32 a178, v250
v_mfma_f32_16x16x32_bf16 v[48:51], v[208:211], v[132:135], v[48:51]
buffer_load_dwordx4 v235, s[44:47], s80 offen lds
v_mfma_f32_16x16x32_bf16 v[80:83], v[208:211], v[136:139], v[80:83]
s_add_u32 s80, s80, 524288
v_accvgpr_write_b32 a179, v251
v_mfma_f32_16x16x32_bf16 v[112:115], v[208:211], v[140:143], v[112:115]
ds_read_b64_tr_b16 v[208:209], v240 offset:42368
ds_read_b64_tr_b16 v[210:211], v240 offset:43392
v_mfma_f32_16x16x32_bf16 v[20:23], v[212:215], v[128:131], v[20:23]
s_cmp_eq_u32 s74, 3
s_cselect_b32 s23, 3, 0
s_cselect_b32 s22, 0, s75
v_mfma_f32_16x16x32_bf16 v[52:55], v[212:215], v[132:135], v[52:55]
buffer_load_dwordx4 v[188:191], v247, s[52:55], s91 offen offset:3072 sc1
v_mfma_f32_16x16x32_bf16 v[84:87], v[212:215], v[136:139], v[84:87]
s_add_u32 s91, s91, 4096
s_lshl_b32 s95, s76, 5
v_mfma_f32_16x16x32_bf16 v[116:119], v[212:215], v[140:143], v[116:119]
ds_read_b64_tr_b16 v[212:213], v241 offset:42368
ds_read_b64_tr_b16 v[214:215], v241 offset:43392
v_mfma_f32_16x16x32_bf16 v[24:27], v[216:219], v[128:131], v[24:27]
s_add_u32 s95, s95, s3
s_lshl_b32 s95, s95, 5
v_mfma_f32_16x16x32_bf16 v[56:59], v[216:219], v[132:135], v[56:59]
s_add_u32 s95, s95, s72
s_lshl_b32 s95, s95, 3
v_mfma_f32_16x16x32_bf16 v[88:91], v[216:219], v[136:139], v[88:91]
s_mov_b32 s18, 36864
s_add_u32 s6, s95, 1048576
v_mfma_f32_16x16x32_bf16 v[120:123], v[216:219], v[140:143], v[120:123]
ds_read_b64_tr_b16 v[216:217], v242 offset:42368
ds_read_b64_tr_b16 v[218:219], v242 offset:43392
v_mfma_f32_16x16x32_bf16 v[28:31], v[220:223], v[128:131], v[28:31]
ds_write_b128 v238, v[248:251] offset:39936
v_mfma_f32_16x16x32_bf16 v[60:63], v[220:223], v[132:135], v[60:63]
buffer_store_dwordx4 a[164:167], v255, s[48:51], s18 offen offset:0 sc1
v_mfma_f32_16x16x32_bf16 v[92:95], v[220:223], v[136:139], v[92:95]
s_waitcnt vmcnt(14) lgkmcnt(3)
v_and_b32 v239, 0x70, v255
v_mfma_f32_16x16x32_bf16 v[124:127], v[220:223], v[140:143], v[124:127]
ds_read_b64_tr_b16 v[220:221], v243 offset:42368
ds_read_b64_tr_b16 v[222:223], v243 offset:43392
v_mfma_f32_16x16x32_bf16 v[0:3], v[192:195], v[144:147], v[0:3]
v_accvgpr_read_b32 v224, a120
v_accvgpr_read_b32 v225, a121
v_mfma_f32_16x16x32_bf16 v[32:35], v[192:195], v[148:151], v[32:35]
buffer_load_dwordx4 v[128:131], v244, s[52:55], s91 offen offset:0 sc1
v_mfma_f32_16x16x32_bf16 v[64:67], v[192:195], v[152:155], v[64:67]
v_accvgpr_read_b32 v226, a122
v_accvgpr_read_b32 v227, a123
v_mfma_f32_16x16x32_bf16 v[96:99], v[192:195], v[156:159], v[96:99]
v_accvgpr_read_b32 v228, a124
v_accvgpr_read_b32 v229, a125
v_mfma_f32_16x16x32_bf16 v[4:7], v[196:199], v[144:147], v[4:7]
s_waitcnt lgkmcnt(0)
s_barrier
v_mfma_f32_16x16x32_bf16 v[36:39], v[196:199], v[148:151], v[36:39]
buffer_load_dwordx4 v[132:135], v245, s[52:55], s91 offen offset:0 sc1
v_mfma_f32_16x16x32_bf16 v[68:71], v[196:199], v[152:155], v[68:71]
ds_read_b64_tr_b16 v[192:193], v240 offset:50688
ds_read_b64_tr_b16 v[194:195], v240 offset:51712
v_mfma_f32_16x16x32_bf16 v[100:103], v[196:199], v[156:159], v[100:103]
ds_read_b64_tr_b16 v[196:197], v241 offset:50688
ds_read_b64_tr_b16 v[198:199], v241 offset:51712
v_mfma_f32_16x16x32_bf16 v[8:11], v[200:203], v[144:147], v[8:11]
v_accvgpr_read_b32 v230, a126
v_accvgpr_read_b32 v231, a127
v_mfma_f32_16x16x32_bf16 v[40:43], v[200:203], v[148:151], v[40:43]
buffer_load_dwordx4 v[136:139], v246, s[52:55], s91 offen offset:0 sc1
v_mfma_f32_16x16x32_bf16 v[72:75], v[200:203], v[152:155], v[72:75]
v_cvt_pk_bf16_f32 v248, v224, v225
v_cvt_pk_bf16_f32 v249, v226, v227
v_mfma_f32_16x16x32_bf16 v[104:107], v[200:203], v[156:159], v[104:107]
ds_read_b64_tr_b16 v[200:201], v242 offset:50688
ds_read_b64_tr_b16 v[202:203], v242 offset:51712
v_mfma_f32_16x16x32_bf16 v[12:15], v[204:207], v[144:147], v[12:15]
s_add_u32 m0, s83, 33792
v_cvt_pk_bf16_f32 v250, v228, v229
v_mfma_f32_16x16x32_bf16 v[44:47], v[204:207], v[148:151], v[44:47]
buffer_load_dwordx4 v232, s[44:47], s80 offen lds
v_mfma_f32_16x16x32_bf16 v[76:79], v[204:207], v[152:155], v[76:79]
v_cvt_pk_bf16_f32 v251, v230, v231
v_accvgpr_write_b32 a180, v248
v_mfma_f32_16x16x32_bf16 v[108:111], v[204:207], v[156:159], v[108:111]
ds_read_b64_tr_b16 v[204:205], v243 offset:50688
ds_read_b64_tr_b16 v[206:207], v243 offset:51712
v_mfma_f32_16x16x32_bf16 v[16:19], v[208:211], v[144:147], v[16:19]
v_accvgpr_write_b32 a181, v249
v_accvgpr_write_b32 a182, v250
v_mfma_f32_16x16x32_bf16 v[48:51], v[208:211], v[148:151], v[48:51]
buffer_load_dwordx4 v[140:143], v247, s[52:55], s91 offen offset:0 sc1
v_mfma_f32_16x16x32_bf16 v[80:83], v[208:211], v[152:155], v[80:83]
v_accvgpr_write_b32 a183, v251
s_add_u32 s85, s85, 65536
v_mfma_f32_16x16x32_bf16 v[112:115], v[208:211], v[156:159], v[112:115]
ds_read_b64_tr_b16 v[208:209], v240 offset:50816
ds_read_b64_tr_b16 v[210:211], v240 offset:51840
v_mfma_f32_16x16x32_bf16 v[20:23], v[212:215], v[144:147], v[20:23]
s_add_u32 m0, s83, 34816
v_add_u32 v238, s85, v255
v_mfma_f32_16x16x32_bf16 v[52:55], v[212:215], v[148:151], v[52:55]
buffer_load_dwordx4 v233, s[44:47], s80 offen lds
v_mfma_f32_16x16x32_bf16 v[84:87], v[212:215], v[152:155], v[84:87]
s_mov_b32 s85, 0
v_lshlrev_b32 v239, 6, v239
v_mfma_f32_16x16x32_bf16 v[116:119], v[212:215], v[156:159], v[116:119]
ds_read_b64_tr_b16 v[212:213], v241 offset:50816
ds_read_b64_tr_b16 v[214:215], v241 offset:51840
v_mfma_f32_16x16x32_bf16 v[24:27], v[216:219], v[144:147], v[24:27]
v_accvgpr_write_b32 a231, v239
s_mul_i32 s95, s22, 0
v_mfma_f32_16x16x32_bf16 v[56:59], v[216:219], v[148:151], v[56:59]
s_sub_u32 s96, 2, s95
s_add_u32 s95, s95, 2
v_mfma_f32_16x16x32_bf16 v[88:91], v[216:219], v[152:155], v[88:91]
v_lshrrev_b32 v239, 7, v255
v_mul_u32_u24 v254, s21, v239
v_mfma_f32_16x16x32_bf16 v[120:123], v[216:219], v[156:159], v[120:123]
ds_read_b64_tr_b16 v[216:217], v242 offset:50816
ds_read_b64_tr_b16 v[218:219], v242 offset:51840
v_mfma_f32_16x16x32_bf16 v[28:31], v[220:223], v[144:147], v[28:31]
ds_write_b128 v238, v[248:251] offset:0
v_mfma_f32_16x16x32_bf16 v[60:63], v[220:223], v[148:151], v[60:63]
buffer_store_dwordx4 a[168:171], v255, s[48:51], s18 offen offset:1024 sc1
v_mfma_f32_16x16x32_bf16 v[92:95], v[220:223], v[152:155], v[92:95]
s_waitcnt vmcnt(15) lgkmcnt(3)
v_add_u32 v239, s96, v239
v_mfma_f32_16x16x32_bf16 v[124:127], v[220:223], v[156:159], v[124:127]
ds_read_b64_tr_b16 v[220:221], v243 offset:50816
ds_read_b64_tr_b16 v[222:223], v243 offset:51840
v_mfma_f32_16x16x32_bf16 v[0:3], v[192:195], v[160:163], v[0:3]
v_accvgpr_read_b32 v224, a184
v_accvgpr_read_b32 v225, a185
v_mfma_f32_16x16x32_bf16 v[32:35], v[192:195], v[164:167], v[32:35]
buffer_load_dwordx4 v[144:147], v244, s[52:55], s91 offen offset:1024 sc1
v_mfma_f32_16x16x32_bf16 v[64:67], v[192:195], v[168:171], v[64:67]
v_accvgpr_read_b32 v226, a186
v_accvgpr_read_b32 v227, a187
v_mfma_f32_16x16x32_bf16 v[96:99], v[192:195], v[172:175], v[96:99]
ds_read_b64_tr_b16 v[192:193], v240 offset:59136
ds_read_b64_tr_b16 v[194:195], v240 offset:60160
v_mfma_f32_16x16x32_bf16 v[4:7], v[196:199], v[160:163], v[4:7]
v_accvgpr_read_b32 v228, a188
v_accvgpr_read_b32 v229, a189
v_mfma_f32_16x16x32_bf16 v[36:39], v[196:199], v[164:167], v[36:39]
buffer_load_dwordx4 v[148:151], v245, s[52:55], s91 offen offset:1024 sc1
v_mfma_f32_16x16x32_bf16 v[68:71], v[196:199], v[168:171], v[68:71]
s_waitcnt lgkmcnt(2)
v_accvgpr_read_b32 v230, a190
v_mfma_f32_16x16x32_bf16 v[100:103], v[196:199], v[172:175], v[100:103]
ds_read_b64_tr_b16 v[196:197], v241 offset:59136
ds_read_b64_tr_b16 v[198:199], v241 offset:60160
v_mfma_f32_16x16x32_bf16 v[8:11], v[200:203], v[160:163], v[8:11]
s_add_u32 m0, s83, 35968
v_accvgpr_read_b32 v231, a191
v_mfma_f32_16x16x32_bf16 v[40:43], v[200:203], v[164:167], v[40:43]
buffer_load_dwordx4 v234, s[44:47], s80 offen lds
v_mfma_f32_16x16x32_bf16 v[72:75], v[200:203], v[168:171], v[72:75]
v_cvt_pk_bf16_f32 v248, v224, v225
v_cvt_pk_bf16_f32 v249, v226, v227
v_mfma_f32_16x16x32_bf16 v[104:107], v[200:203], v[172:175], v[104:107]
ds_read_b64_tr_b16 v[200:201], v242 offset:59136
ds_read_b64_tr_b16 v[202:203], v242 offset:60160
v_mfma_f32_16x16x32_bf16 v[12:15], v[204:207], v[160:163], v[12:15]
v_cvt_pk_bf16_f32 v250, v228, v229
v_cvt_pk_bf16_f32 v251, v230, v231
v_mfma_f32_16x16x32_bf16 v[44:47], v[204:207], v[164:167], v[44:47]
buffer_load_dwordx4 v[152:155], v246, s[52:55], s91 offen offset:1024 sc1
v_mfma_f32_16x16x32_bf16 v[76:79], v[204:207], v[168:171], v[76:79]
v_accvgpr_write_b32 a184, v248
v_accvgpr_write_b32 a185, v249
v_mfma_f32_16x16x32_bf16 v[108:111], v[204:207], v[172:175], v[108:111]
ds_read_b64_tr_b16 v[204:205], v243 offset:59136
ds_read_b64_tr_b16 v[206:207], v243 offset:60160
v_mfma_f32_16x16x32_bf16 v[16:19], v[208:211], v[160:163], v[16:19]
s_add_u32 m0, s83, 36992
v_accvgpr_write_b32 a186, v250
v_mfma_f32_16x16x32_bf16 v[48:51], v[208:211], v[164:167], v[48:51]
buffer_load_dwordx4 v235, s[44:47], s80 offen lds
v_mfma_f32_16x16x32_bf16 v[80:83], v[208:211], v[168:171], v[80:83]
s_add_u32 s80, s80, 524288
v_accvgpr_write_b32 a187, v251
v_mfma_f32_16x16x32_bf16 v[112:115], v[208:211], v[172:175], v[112:115]
ds_read_b64_tr_b16 v[208:209], v240 offset:59264
ds_read_b64_tr_b16 v[210:211], v240 offset:60288
v_mfma_f32_16x16x32_bf16 v[20:23], v[212:215], v[160:163], v[20:23]
v_lshrrev_b32 v239, 2, v239
v_lshl_add_u32 v254, v239, 28, v254
v_mfma_f32_16x16x32_bf16 v[52:55], v[212:215], v[164:167], v[52:55]
buffer_load_dwordx4 v[156:159], v247, s[52:55], s91 offen offset:1024 sc1
v_mfma_f32_16x16x32_bf16 v[84:87], v[212:215], v[168:171], v[84:87]
v_and_b32 v239, 0x70, v255
v_lshl_add_u32 v254, v239, 3, v254
v_mfma_f32_16x16x32_bf16 v[116:119], v[212:215], v[172:175], v[116:119]
ds_read_b64_tr_b16 v[212:213], v241 offset:59264
ds_read_b64_tr_b16 v[214:215], v241 offset:60288
v_mfma_f32_16x16x32_bf16 v[24:27], v[216:219], v[160:163], v[24:27]
s_mul_i32 s21, s21, s95
v_lshrrev_b32 v236, 8, v255
v_mfma_f32_16x16x32_bf16 v[56:59], v[216:219], v[164:167], v[56:59]
v_lshlrev_b32 v239, 5, v236
v_xor_b32 v239, v239, v255
v_mfma_f32_16x16x32_bf16 v[88:91], v[216:219], v[168:171], v[88:91]
v_and_b32 v239, 0xf0, v239
v_lshl_add_u32 v239, v236, 11, v239
v_mfma_f32_16x16x32_bf16 v[120:123], v[216:219], v[172:175], v[120:123]
ds_read_b64_tr_b16 v[216:217], v242 offset:59264
ds_read_b64_tr_b16 v[218:219], v242 offset:60288
v_mfma_f32_16x16x32_bf16 v[28:31], v[220:223], v[160:163], v[28:31]
ds_write_b128 v238, v[248:251] offset:1024
v_mfma_f32_16x16x32_bf16 v[60:63], v[220:223], v[164:167], v[60:63]
buffer_store_dwordx4 a[172:175], v255, s[48:51], s18 offen offset:2048 sc1
v_mfma_f32_16x16x32_bf16 v[92:95], v[220:223], v[168:171], v[92:95]
s_waitcnt vmcnt(15) lgkmcnt(3)
s_lshl_b32 s92, s77, 16
v_mfma_f32_16x16x32_bf16 v[124:127], v[220:223], v[172:175], v[124:127]
ds_read_b64_tr_b16 v[220:221], v243 offset:59264
ds_read_b64_tr_b16 v[222:223], v243 offset:60288
v_mfma_f32_16x16x32_bf16 v[0:3], v[192:195], v[176:179], v[0:3]
v_accvgpr_read_b32 v224, a64
v_accvgpr_read_b32 v225, a65
v_mfma_f32_16x16x32_bf16 v[32:35], v[192:195], v[180:183], v[32:35]
buffer_load_dwordx4 v[160:163], v244, s[52:55], s91 offen offset:2048 sc1
v_mfma_f32_16x16x32_bf16 v[64:67], v[192:195], v[184:187], v[64:67]
v_accvgpr_read_b32 v226, a66
v_accvgpr_read_b32 v227, a67
v_mfma_f32_16x16x32_bf16 v[96:99], v[192:195], v[188:191], v[96:99]
v_accvgpr_read_b32 v228, a68
v_accvgpr_read_b32 v229, a69
v_mfma_f32_16x16x32_bf16 v[4:7], v[196:199], v[176:179], v[4:7]
s_waitcnt lgkmcnt(0)
s_barrier
v_mfma_f32_16x16x32_bf16 v[36:39], v[196:199], v[180:183], v[36:39]
buffer_load_dwordx4 v[164:167], v245, s[52:55], s91 offen offset:2048 sc1
v_mfma_f32_16x16x32_bf16 v[68:71], v[196:199], v[184:187], v[68:71]
ds_read_b64_tr_b16 v[192:193], v240 offset:0
ds_read_b64_tr_b16 v[194:195], v240 offset:1024
v_mfma_f32_16x16x32_bf16 v[100:103], v[196:199], v[188:191], v[100:103]
ds_read_b64_tr_b16 v[196:197], v241 offset:0
ds_read_b64_tr_b16 v[198:199], v241 offset:1024
v_mfma_f32_16x16x32_bf16 v[8:11], v[200:203], v[176:179], v[8:11]
v_accvgpr_read_b32 v230, a70
v_accvgpr_read_b32 v231, a71
v_mfma_f32_16x16x32_bf16 v[40:43], v[200:203], v[180:183], v[40:43]
buffer_load_dwordx4 v[168:171], v246, s[52:55], s91 offen offset:2048 sc1
v_mfma_f32_16x16x32_bf16 v[72:75], v[200:203], v[184:187], v[72:75]
v_cvt_pk_bf16_f32 v248, v224, v225
v_cvt_pk_bf16_f32 v249, v226, v227
v_mfma_f32_16x16x32_bf16 v[104:107], v[200:203], v[188:191], v[104:107]
ds_read_b64_tr_b16 v[200:201], v242 offset:0
ds_read_b64_tr_b16 v[202:203], v242 offset:1024
v_mfma_f32_16x16x32_bf16 v[12:15], v[204:207], v[176:179], v[12:15]
s_add_u32 m0, s83, 50688
v_cvt_pk_bf16_f32 v250, v228, v229
v_mfma_f32_16x16x32_bf16 v[44:47], v[204:207], v[180:183], v[44:47]
buffer_load_dwordx4 v232, s[44:47], s80 offen lds
v_mfma_f32_16x16x32_bf16 v[76:79], v[204:207], v[184:187], v[76:79]
v_cvt_pk_bf16_f32 v251, v230, v231
v_accvgpr_write_b32 a188, v248
v_mfma_f32_16x16x32_bf16 v[108:111], v[204:207], v[188:191], v[108:111]
ds_read_b64_tr_b16 v[204:205], v243 offset:0
ds_read_b64_tr_b16 v[206:207], v243 offset:1024
v_mfma_f32_16x16x32_bf16 v[16:19], v[208:211], v[176:179], v[16:19]
v_accvgpr_write_b32 a189, v249
v_accvgpr_write_b32 a190, v250
v_mfma_f32_16x16x32_bf16 v[48:51], v[208:211], v[180:183], v[48:51]
buffer_load_dwordx4 v[172:175], v247, s[52:55], s91 offen offset:2048 sc1
v_mfma_f32_16x16x32_bf16 v[80:83], v[208:211], v[184:187], v[80:83]
v_accvgpr_write_b32 a191, v251
s_lshl_b32 s93, s78, 14
v_mfma_f32_16x16x32_bf16 v[112:115], v[208:211], v[188:191], v[112:115]
ds_read_b64_tr_b16 v[208:209], v240 offset:128
ds_read_b64_tr_b16 v[210:211], v240 offset:1152
v_mfma_f32_16x16x32_bf16 v[20:23], v[212:215], v[176:179], v[20:23]
s_add_u32 m0, s83, 51712
s_add_u32 s92, s92, s93
v_mfma_f32_16x16x32_bf16 v[52:55], v[212:215], v[180:183], v[52:55]
buffer_load_dwordx4 v233, s[44:47], s80 offen lds
v_mfma_f32_16x16x32_bf16 v[84:87], v[212:215], v[184:187], v[84:87]
v_add_u32 v239, s92, v239
v_accvgpr_write_b32 a228, v239
v_mfma_f32_16x16x32_bf16 v[116:119], v[212:215], v[188:191], v[116:119]
ds_read_b64_tr_b16 v[212:213], v241 offset:128
ds_read_b64_tr_b16 v[214:215], v241 offset:1152
v_mfma_f32_16x16x32_bf16 v[24:27], v[216:219], v[176:179], v[24:27]
v_bfe_u32 v239, v255, 6, 2
v_lshlrev_b32 v239, 7, v239
v_mfma_f32_16x16x32_bf16 v[56:59], v[216:219], v[180:183], v[56:59]
v_lshl_add_u32 v239, v236, 5, v239
s_lshl_b32 s92, s78, 9
v_mfma_f32_16x16x32_bf16 v[88:91], v[216:219], v[184:187], v[88:91]
v_add_u32 v239, s92, v239
v_mul_u32_u24 v239, 7168, v239
v_mfma_f32_16x16x32_bf16 v[120:123], v[216:219], v[188:191], v[120:123]
ds_read_b64_tr_b16 v[216:217], v242 offset:128
ds_read_b64_tr_b16 v[218:219], v242 offset:1152
v_mfma_f32_16x16x32_bf16 v[28:31], v[220:223], v[176:179], v[28:31]
ds_write_b128 v238, v[248:251] offset:2048
v_mfma_f32_16x16x32_bf16 v[60:63], v[220:223], v[180:183], v[60:63]
buffer_store_dwordx4 a[176:179], v255, s[48:51], s18 offen offset:3072 sc1
v_mfma_f32_16x16x32_bf16 v[92:95], v[220:223], v[184:187], v[92:95]
s_waitcnt vmcnt(16) lgkmcnt(3)
v_and_or_b32 v239, v255, 48, v239
v_mfma_f32_16x16x32_bf16 v[124:127], v[220:223], v[188:191], v[124:127]
ds_read_b64_tr_b16 v[220:221], v243 offset:128
ds_read_b64_tr_b16 v[222:223], v243 offset:1152
v_mfma_f32_16x16x32_bf16 v[0:3], v[192:195], v[128:131], v[0:3]
v_accvgpr_read_b32 v224, a0
v_accvgpr_read_b32 v225, a1
v_mfma_f32_16x16x32_bf16 v[32:35], v[192:195], v[132:135], v[32:35]
buffer_load_dwordx4 v[176:179], v244, s[52:55], s91 offen offset:3072 sc1
v_mfma_f32_16x16x32_bf16 v[64:67], v[192:195], v[136:139], v[64:67]
v_accvgpr_read_b32 v226, a2
v_accvgpr_read_b32 v227, a3
v_mfma_f32_16x16x32_bf16 v[96:99], v[192:195], v[140:143], v[96:99]
ds_read_b64_tr_b16 v[192:193], v240 offset:8448
ds_read_b64_tr_b16 v[194:195], v240 offset:9472
v_mfma_f32_16x16x32_bf16 v[4:7], v[196:199], v[128:131], v[4:7]
v_accvgpr_read_b32 v228, a4
v_accvgpr_read_b32 v229, a5
v_mfma_f32_16x16x32_bf16 v[36:39], v[196:199], v[132:135], v[36:39]
buffer_load_dwordx4 v[180:183], v245, s[52:55], s91 offen offset:3072 sc1
v_mfma_f32_16x16x32_bf16 v[68:71], v[196:199], v[136:139], v[68:71]
s_waitcnt lgkmcnt(2)
v_accvgpr_read_b32 v230, a6
v_mfma_f32_16x16x32_bf16 v[100:103], v[196:199], v[140:143], v[100:103]
ds_read_b64_tr_b16 v[196:197], v241 offset:8448
ds_read_b64_tr_b16 v[198:199], v241 offset:9472
v_mfma_f32_16x16x32_bf16 v[8:11], v[200:203], v[128:131], v[8:11]
s_add_u32 m0, s83, 52864
v_accvgpr_read_b32 v231, a7
v_mfma_f32_16x16x32_bf16 v[40:43], v[200:203], v[132:135], v[40:43]
buffer_load_dwordx4 v234, s[44:47], s80 offen lds
v_mfma_f32_16x16x32_bf16 v[72:75], v[200:203], v[136:139], v[72:75]
v_cvt_pk_bf16_f32 v248, v224, v225
v_cvt_pk_bf16_f32 v249, v226, v227
v_mfma_f32_16x16x32_bf16 v[104:107], v[200:203], v[140:143], v[104:107]
ds_read_b64_tr_b16 v[200:201], v242 offset:8448
ds_read_b64_tr_b16 v[202:203], v242 offset:9472
v_mfma_f32_16x16x32_bf16 v[12:15], v[204:207], v[128:131], v[12:15]
v_cvt_pk_bf16_f32 v250, v228, v229
v_cvt_pk_bf16_f32 v251, v230, v231
v_mfma_f32_16x16x32_bf16 v[44:47], v[204:207], v[132:135], v[44:47]
buffer_load_dwordx4 v[184:187], v246, s[52:55], s91 offen offset:3072 sc1
v_mfma_f32_16x16x32_bf16 v[76:79], v[204:207], v[136:139], v[76:79]
v_accvgpr_write_b32 a0, v248
v_accvgpr_write_b32 a1, v249
v_mfma_f32_16x16x32_bf16 v[108:111], v[204:207], v[140:143], v[108:111]
ds_read_b64_tr_b16 v[204:205], v243 offset:8448
ds_read_b64_tr_b16 v[206:207], v243 offset:9472
v_mfma_f32_16x16x32_bf16 v[16:19], v[208:211], v[128:131], v[16:19]
s_add_u32 m0, s83, 53888
v_accvgpr_write_b32 a2, v250
v_mfma_f32_16x16x32_bf16 v[48:51], v[208:211], v[132:135], v[48:51]
buffer_load_dwordx4 v235, s[44:47], s80 offen lds
v_mfma_f32_16x16x32_bf16 v[80:83], v[208:211], v[136:139], v[80:83]
s_add_u32 s80, s80, 524288
v_accvgpr_write_b32 a3, v251
v_mfma_f32_16x16x32_bf16 v[112:115], v[208:211], v[140:143], v[112:115]
ds_read_b64_tr_b16 v[208:209], v240 offset:8576
ds_read_b64_tr_b16 v[210:211], v240 offset:9600
v_mfma_f32_16x16x32_bf16 v[20:23], v[212:215], v[128:131], v[20:23]
v_accvgpr_write_b32 a229, v239
v_and_b32 v239, 0xf0, v255
v_mfma_f32_16x16x32_bf16 v[52:55], v[212:215], v[132:135], v[52:55]
buffer_load_dwordx4 v[188:191], v247, s[52:55], s91 offen offset:3072 sc1
v_mfma_f32_16x16x32_bf16 v[84:87], v[212:215], v[136:139], v[84:87]
s_add_u32 s91, s91, 4096
v_lshl_add_u32 v239, v236, 14, v239
v_mfma_f32_16x16x32_bf16 v[116:119], v[212:215], v[140:143], v[116:119]
ds_read_b64_tr_b16 v[212:213], v241 offset:8576
ds_read_b64_tr_b16 v[214:215], v241 offset:9600
v_mfma_f32_16x16x32_bf16 v[24:27], v[216:219], v[128:131], v[24:27]
s_lshl_b32 s92, s3, 9
v_add_u32 v239, s92, v239
v_mfma_f32_16x16x32_bf16 v[56:59], v[216:219], v[132:135], v[56:59]
v_accvgpr_write_b32 a230, v239
v_add_u32 v239, 4294886912, v240
v_mfma_f32_16x16x32_bf16 v[88:91], v[216:219], v[136:139], v[88:91]
s_mov_b32 s18, 16384
v_accvgpr_write_b32 a224, v239
v_mfma_f32_16x16x32_bf16 v[120:123], v[216:219], v[140:143], v[120:123]
ds_read_b64_tr_b16 v[216:217], v242 offset:8576
ds_read_b64_tr_b16 v[218:219], v242 offset:9600
v_mfma_f32_16x16x32_bf16 v[28:31], v[220:223], v[128:131], v[28:31]
ds_write_b128 v238, v[248:251] offset:3072
v_mfma_f32_16x16x32_bf16 v[60:63], v[220:223], v[132:135], v[60:63]
buffer_store_dwordx4 a[180:183], v255, s[48:51], s18 offen offset:0 sc1
v_mfma_f32_16x16x32_bf16 v[92:95], v[220:223], v[136:139], v[92:95]
s_waitcnt vmcnt(15) lgkmcnt(3)
v_bfe_u32 v239, v255, 4, 4
v_mfma_f32_16x16x32_bf16 v[124:127], v[220:223], v[140:143], v[124:127]
ds_read_b64_tr_b16 v[220:221], v243 offset:8576
ds_read_b64_tr_b16 v[222:223], v243 offset:9600
v_mfma_f32_16x16x32_bf16 v[0:3], v[192:195], v[144:147], v[0:3]
v_accvgpr_read_b32 v224, a72
v_accvgpr_read_b32 v225, a73
v_mfma_f32_16x16x32_bf16 v[32:35], v[192:195], v[148:151], v[32:35]
buffer_load_dwordx4 v[128:131], v244, s[52:55], s91 offen offset:0 sc1
v_mfma_f32_16x16x32_bf16 v[64:67], v[192:195], v[152:155], v[64:67]
v_accvgpr_read_b32 v226, a74
v_accvgpr_read_b32 v227, a75
v_mfma_f32_16x16x32_bf16 v[96:99], v[192:195], v[156:159], v[96:99]
v_accvgpr_read_b32 v228, a76
v_accvgpr_read_b32 v229, a77
v_mfma_f32_16x16x32_bf16 v[4:7], v[196:199], v[144:147], v[4:7]
s_waitcnt lgkmcnt(0)
s_barrier
v_mfma_f32_16x16x32_bf16 v[36:39], v[196:199], v[148:151], v[36:39]
buffer_load_dwordx4 v[132:135], v245, s[52:55], s91 offen offset:0 sc1
v_mfma_f32_16x16x32_bf16 v[68:71], v[196:199], v[152:155], v[68:71]
ds_read_b64_tr_b16 v[192:193], v240 offset:16896
ds_read_b64_tr_b16 v[194:195], v240 offset:17920
v_mfma_f32_16x16x32_bf16 v[100:103], v[196:199], v[156:159], v[100:103]
ds_read_b64_tr_b16 v[196:197], v241 offset:16896
ds_read_b64_tr_b16 v[198:199], v241 offset:17920
v_mfma_f32_16x16x32_bf16 v[8:11], v[200:203], v[144:147], v[8:11]
v_accvgpr_read_b32 v230, a78
v_accvgpr_read_b32 v231, a79
v_mfma_f32_16x16x32_bf16 v[40:43], v[200:203], v[148:151], v[40:43]
buffer_load_dwordx4 v[136:139], v246, s[52:55], s91 offen offset:0 sc1
v_mfma_f32_16x16x32_bf16 v[72:75], v[200:203], v[152:155], v[72:75]
v_cvt_pk_bf16_f32 v248, v224, v225
v_cvt_pk_bf16_f32 v249, v226, v227
v_mfma_f32_16x16x32_bf16 v[104:107], v[200:203], v[156:159], v[104:107]
ds_read_b64_tr_b16 v[200:201], v242 offset:16896
ds_read_b64_tr_b16 v[202:203], v242 offset:17920
v_mfma_f32_16x16x32_bf16 v[12:15], v[204:207], v[144:147], v[12:15]
s_add_u32 m0, s83, 0
v_cvt_pk_bf16_f32 v250, v228, v229
v_mfma_f32_16x16x32_bf16 v[44:47], v[204:207], v[148:151], v[44:47]
buffer_load_dwordx4 v232, s[44:47], s80 offen lds
v_mfma_f32_16x16x32_bf16 v[76:79], v[204:207], v[152:155], v[76:79]
v_cvt_pk_bf16_f32 v251, v230, v231
v_accvgpr_write_b32 a4, v248
v_mfma_f32_16x16x32_bf16 v[108:111], v[204:207], v[156:159], v[108:111]
ds_read_b64_tr_b16 v[204:205], v243 offset:16896
ds_read_b64_tr_b16 v[206:207], v243 offset:17920
v_mfma_f32_16x16x32_bf16 v[16:19], v[208:211], v[144:147], v[16:19]
v_accvgpr_write_b32 a5, v249
v_accvgpr_write_b32 a6, v250
v_mfma_f32_16x16x32_bf16 v[48:51], v[208:211], v[148:151], v[48:51]
buffer_load_dwordx4 v[140:143], v247, s[52:55], s91 offen offset:0 sc1
v_mfma_f32_16x16x32_bf16 v[80:83], v[208:211], v[152:155], v[80:83]
v_accvgpr_write_b32 a7, v251
v_lshlrev_b32 v236, 2, v236
v_mfma_f32_16x16x32_bf16 v[112:115], v[208:211], v[156:159], v[112:115]
ds_read_b64_tr_b16 v[208:209], v240 offset:17024
ds_read_b64_tr_b16 v[210:211], v240 offset:18048
v_mfma_f32_16x16x32_bf16 v[20:23], v[212:215], v[144:147], v[20:23]
s_add_u32 m0, s83, 1024
v_sub_u32 v239, v239, v236
v_mfma_f32_16x16x32_bf16 v[52:55], v[212:215], v[148:151], v[52:55]
buffer_load_dwordx4 v233, s[44:47], s80 offen lds
v_mfma_f32_16x16x32_bf16 v[84:87], v[212:215], v[152:155], v[84:87]
v_subrev_u32 v236, 0, v239
v_ashrrev_i32 v236, 31, v236
v_mfma_f32_16x16x32_bf16 v[116:119], v[212:215], v[156:159], v[116:119]
ds_read_b64_tr_b16 v[212:213], v241 offset:17024
ds_read_b64_tr_b16 v[214:215], v241 offset:18048
v_mfma_f32_16x16x32_bf16 v[24:27], v[216:219], v[144:147], v[24:27]
v_and_b32 v236, 0xff800000, v236
v_accvgpr_write_b32 a236, v236
v_mfma_f32_16x16x32_bf16 v[56:59], v[216:219], v[148:151], v[56:59]
v_subrev_u32 v236, 1, v239
v_ashrrev_i32 v236, 31, v236
v_mfma_f32_16x16x32_bf16 v[88:91], v[216:219], v[152:155], v[88:91]
v_and_b32 v236, 0xff800000, v236
v_accvgpr_write_b32 a237, v236
v_mfma_f32_16x16x32_bf16 v[120:123], v[216:219], v[156:159], v[120:123]
ds_read_b64_tr_b16 v[216:217], v242 offset:17024
ds_read_b64_tr_b16 v[218:219], v242 offset:18048
v_mfma_f32_16x16x32_bf16 v[28:31], v[220:223], v[144:147], v[28:31]
ds_write_b128 v238, v[248:251] offset:4096
v_mfma_f32_16x16x32_bf16 v[60:63], v[220:223], v[148:151], v[60:63]
v_subrev_u32 v236, 2, v239
v_ashrrev_i32 v236, 31, v236
v_mfma_f32_16x16x32_bf16 v[92:95], v[220:223], v[152:155], v[92:95]
s_waitcnt vmcnt(15) lgkmcnt(3)
v_and_b32 v236, 0xff800000, v236
v_mfma_f32_16x16x32_bf16 v[124:127], v[220:223], v[156:159], v[124:127]
ds_read_b64_tr_b16 v[220:221], v243 offset:17024
ds_read_b64_tr_b16 v[222:223], v243 offset:18048
v_mfma_f32_16x16x32_bf16 v[0:3], v[192:195], v[160:163], v[0:3]
v_accvgpr_read_b32 v224, a8
v_accvgpr_read_b32 v225, a9
v_mfma_f32_16x16x32_bf16 v[32:35], v[192:195], v[164:167], v[32:35]
buffer_load_dwordx4 v[144:147], v244, s[52:55], s91 offen offset:1024 sc1
v_mfma_f32_16x16x32_bf16 v[64:67], v[192:195], v[168:171], v[64:67]
v_accvgpr_read_b32 v226, a10
v_accvgpr_read_b32 v227, a11
v_mfma_f32_16x16x32_bf16 v[96:99], v[192:195], v[172:175], v[96:99]
ds_read_b64_tr_b16 v[192:193], v240 offset:25344
ds_read_b64_tr_b16 v[194:195], v240 offset:26368
v_mfma_f32_16x16x32_bf16 v[4:7], v[196:199], v[160:163], v[4:7]
v_accvgpr_read_b32 v228, a12
v_accvgpr_read_b32 v229, a13
v_mfma_f32_16x16x32_bf16 v[36:39], v[196:199], v[164:167], v[36:39]
buffer_load_dwordx4 v[148:151], v245, s[52:55], s91 offen offset:1024 sc1
v_mfma_f32_16x16x32_bf16 v[68:71], v[196:199], v[168:171], v[68:71]
s_waitcnt lgkmcnt(2)
v_accvgpr_read_b32 v230, a14
v_mfma_f32_16x16x32_bf16 v[100:103], v[196:199], v[172:175], v[100:103]
ds_read_b64_tr_b16 v[196:197], v241 offset:25344
ds_read_b64_tr_b16 v[198:199], v241 offset:26368
v_mfma_f32_16x16x32_bf16 v[8:11], v[200:203], v[160:163], v[8:11]
s_add_u32 m0, s83, 2176
v_accvgpr_read_b32 v231, a15
v_mfma_f32_16x16x32_bf16 v[40:43], v[200:203], v[164:167], v[40:43]
buffer_load_dwordx4 v234, s[44:47], s80 offen lds
v_mfma_f32_16x16x32_bf16 v[72:75], v[200:203], v[168:171], v[72:75]
.La5_LW1_9:
s_cbranch_execz .La5_LW1_R0
v_cvt_pk_bf16_f32 v248, v224, v225
v_cvt_pk_bf16_f32 v249, v226, v227
v_mfma_f32_16x16x32_bf16 v[104:107], v[200:203], v[172:175], v[104:107]
ds_read_b64_tr_b16 v[200:201], v242 offset:25344
ds_read_b64_tr_b16 v[202:203], v242 offset:26368
v_mfma_f32_16x16x32_bf16 v[12:15], v[204:207], v[160:163], v[12:15]
v_cvt_pk_bf16_f32 v250, v228, v229
v_cvt_pk_bf16_f32 v251, v230, v231
v_mfma_f32_16x16x32_bf16 v[44:47], v[204:207], v[164:167], v[44:47]
buffer_load_dwordx4 v[152:155], v246, s[52:55], s91 offen offset:1024 sc1
v_mfma_f32_16x16x32_bf16 v[76:79], v[204:207], v[168:171], v[76:79]
v_accvgpr_write_b32 a8, v248
v_accvgpr_write_b32 a9, v249
v_mfma_f32_16x16x32_bf16 v[108:111], v[204:207], v[172:175], v[108:111]
ds_read_b64_tr_b16 v[204:205], v243 offset:25344
ds_read_b64_tr_b16 v[206:207], v243 offset:26368
v_mfma_f32_16x16x32_bf16 v[16:19], v[208:211], v[160:163], v[16:19]
s_add_u32 m0, s83, 3200
v_accvgpr_write_b32 a10, v250
v_mfma_f32_16x16x32_bf16 v[48:51], v[208:211], v[164:167], v[48:51]
buffer_load_dwordx4 v235, s[44:47], s80 offen lds
v_mfma_f32_16x16x32_bf16 v[80:83], v[208:211], v[168:171], v[80:83]
s_add_u32 s80, s80, 524288
v_accvgpr_write_b32 a11, v251
v_mfma_f32_16x16x32_bf16 v[112:115], v[208:211], v[172:175], v[112:115]
ds_read_b64_tr_b16 v[208:209], v240 offset:25472
ds_read_b64_tr_b16 v[210:211], v240 offset:26496
v_mfma_f32_16x16x32_bf16 v[20:23], v[212:215], v[160:163], v[20:23]
v_accvgpr_write_b32 a238, v236
v_subrev_u32 v236, 3, v239
v_mfma_f32_16x16x32_bf16 v[52:55], v[212:215], v[164:167], v[52:55]
buffer_load_dwordx4 v[156:159], v247, s[52:55], s91 offen offset:1024 sc1
v_mfma_f32_16x16x32_bf16 v[84:87], v[212:215], v[168:171], v[84:87]
v_ashrrev_i32 v236, 31, v236
v_and_b32 v236, 0xff800000, v236
v_mfma_f32_16x16x32_bf16 v[116:119], v[212:215], v[172:175], v[116:119]
ds_read_b64_tr_b16 v[212:213], v241 offset:25472
ds_read_b64_tr_b16 v[214:215], v241 offset:26496
v_mfma_f32_16x16x32_bf16 v[24:27], v[216:219], v[160:163], v[24:27]
v_accvgpr_write_b32 a239, v236
v_lshlrev_b32 v236, 4, v239
v_mfma_f32_16x16x32_bf16 v[56:59], v[216:219], v[164:167], v[56:59]
s_mov_b32 s92, 0x3f80
v_lshlrev_b32 v236, v236, s92
v_mfma_f32_16x16x32_bf16 v[88:91], v[216:219], v[168:171], v[88:91]
v_cmp_gt_u32 vcc, 2, v239
v_subrev_u32 v239, 2, v239
v_mfma_f32_16x16x32_bf16 v[120:123], v[216:219], v[172:175], v[120:123]
ds_read_b64_tr_b16 v[216:217], v242 offset:25472
ds_read_b64_tr_b16 v[218:219], v242 offset:26496
v_mfma_f32_16x16x32_bf16 v[28:31], v[220:223], v[160:163], v[28:31]
ds_write_b128 v238, v[248:251] offset:5120
v_mfma_f32_16x16x32_bf16 v[60:63], v[220:223], v[164:167], v[60:63]
buffer_store_dwordx4 a[184:187], v255, s[48:51], s18 offen offset:1024 sc1
v_mfma_f32_16x16x32_bf16 v[92:95], v[220:223], v[168:171], v[92:95]
s_waitcnt vmcnt(14) lgkmcnt(3)
v_cmp_gt_u32 s[92:93], 2, v239
v_mfma_f32_16x16x32_bf16 v[124:127], v[220:223], v[172:175], v[124:127]
ds_read_b64_tr_b16 v[220:221], v243 offset:25472
ds_read_b64_tr_b16 v[222:223], v243 offset:26496
v_mfma_f32_16x16x32_bf16 v[0:3], v[192:195], v[176:179], v[0:3]
v_accvgpr_read_b32 v224, a80
v_accvgpr_read_b32 v225, a81
v_mfma_f32_16x16x32_bf16 v[32:35], v[192:195], v[180:183], v[32:35]
buffer_load_dwordx4 v[160:163], v244, s[52:55], s91 offen offset:2048 sc1
v_mfma_f32_16x16x32_bf16 v[64:67], v[192:195], v[184:187], v[64:67]
v_accvgpr_read_b32 v226, a82
v_accvgpr_read_b32 v227, a83
v_mfma_f32_16x16x32_bf16 v[96:99], v[192:195], v[188:191], v[96:99]
v_accvgpr_read_b32 v228, a84
v_accvgpr_read_b32 v229, a85
v_mfma_f32_16x16x32_bf16 v[4:7], v[196:199], v[176:179], v[4:7]
s_waitcnt lgkmcnt(0)
s_barrier
v_mfma_f32_16x16x32_bf16 v[36:39], v[196:199], v[180:183], v[36:39]
buffer_load_dwordx4 v[164:167], v245, s[52:55], s91 offen offset:2048 sc1
v_mfma_f32_16x16x32_bf16 v[68:71], v[196:199], v[184:187], v[68:71]
ds_read_b64_tr_b16 v[192:193], v240 offset:33792
ds_read_b64_tr_b16 v[194:195], v240 offset:34816
v_mfma_f32_16x16x32_bf16 v[100:103], v[196:199], v[188:191], v[100:103]
ds_read_b64_tr_b16 v[196:197], v241 offset:33792
ds_read_b64_tr_b16 v[198:199], v241 offset:34816
v_mfma_f32_16x16x32_bf16 v[8:11], v[200:203], v[176:179], v[8:11]
v_accvgpr_read_b32 v230, a86
v_accvgpr_read_b32 v231, a87
v_mfma_f32_16x16x32_bf16 v[40:43], v[200:203], v[180:183], v[40:43]
buffer_load_dwordx4 v[168:171], v246, s[52:55], s91 offen offset:2048 sc1
v_mfma_f32_16x16x32_bf16 v[72:75], v[200:203], v[184:187], v[72:75]
v_cvt_pk_bf16_f32 v248, v224, v225
v_cvt_pk_bf16_f32 v249, v226, v227
v_mfma_f32_16x16x32_bf16 v[104:107], v[200:203], v[188:191], v[104:107]
ds_read_b64_tr_b16 v[200:201], v242 offset:33792
ds_read_b64_tr_b16 v[202:203], v242 offset:34816
v_mfma_f32_16x16x32_bf16 v[12:15], v[204:207], v[176:179], v[12:15]
s_add_u32 m0, s83, 16896
v_cvt_pk_bf16_f32 v250, v228, v229
v_mfma_f32_16x16x32_bf16 v[44:47], v[204:207], v[180:183], v[44:47]
buffer_load_dwordx4 v232, s[44:47], s80 offen lds
v_mfma_f32_16x16x32_bf16 v[76:79], v[204:207], v[184:187], v[76:79]
v_cvt_pk_bf16_f32 v251, v230, v231
v_accvgpr_write_b32 a12, v248
v_mfma_f32_16x16x32_bf16 v[108:111], v[204:207], v[188:191], v[108:111]
ds_read_b64_tr_b16 v[204:205], v243 offset:33792
ds_read_b64_tr_b16 v[206:207], v243 offset:34816
v_mfma_f32_16x16x32_bf16 v[16:19], v[208:211], v[176:179], v[16:19]
v_accvgpr_write_b32 a13, v249
v_accvgpr_write_b32 a14, v250
v_mfma_f32_16x16x32_bf16 v[48:51], v[208:211], v[180:183], v[48:51]
buffer_load_dwordx4 v[172:175], v247, s[52:55], s91 offen offset:2048 sc1
v_mfma_f32_16x16x32_bf16 v[80:83], v[208:211], v[184:187], v[80:83]
v_accvgpr_write_b32 a15, v251
v_cndmask_b32 v239, 0, v236, vcc
v_mfma_f32_16x16x32_bf16 v[112:115], v[208:211], v[188:191], v[112:115]
ds_read_b64_tr_b16 v[208:209], v240 offset:33920
ds_read_b64_tr_b16 v[210:211], v240 offset:34944
v_mfma_f32_16x16x32_bf16 v[20:23], v[212:215], v[176:179], v[20:23]
s_add_u32 m0, s83, 17920
v_accvgpr_write_b32 a212, 0
v_mfma_f32_16x16x32_bf16 v[52:55], v[212:215], v[180:183], v[52:55]
buffer_load_dwordx4 v233, s[44:47], s80 offen lds
v_mfma_f32_16x16x32_bf16 v[84:87], v[212:215], v[184:187], v[84:87]
v_accvgpr_write_b32 a214, v239
v_cndmask_b32 v239, 0, v236, s[92:93]
v_mfma_f32_16x16x32_bf16 v[116:119], v[212:215], v[188:191], v[116:119]
ds_read_b64_tr_b16 v[212:213], v241 offset:33920
ds_read_b64_tr_b16 v[214:215], v241 offset:34944
v_mfma_f32_16x16x32_bf16 v[24:27], v[216:219], v[176:179], v[24:27]
v_mov_b32 v236, 0x3f803f80
v_accvgpr_write_b32 a215, v239
v_mfma_f32_16x16x32_bf16 v[56:59], v[216:219], v[180:183], v[56:59]
v_accvgpr_write_b32 a213, 0
v_accvgpr_write_b32 a216, 0
v_mfma_f32_16x16x32_bf16 v[88:91], v[216:219], v[184:187], v[88:91]
v_accvgpr_write_b32 a217, 0
v_lshrrev_b32 v239, 4, v255
v_mfma_f32_16x16x32_bf16 v[120:123], v[216:219], v[188:191], v[120:123]
ds_read_b64_tr_b16 v[216:217], v242 offset:33920
ds_read_b64_tr_b16 v[218:219], v242 offset:34944
v_mfma_f32_16x16x32_bf16 v[28:31], v[220:223], v[176:179], v[28:31]
v_xor_b32 v239, v239, v255
v_and_b32 v239, 32, v239
v_mfma_f32_16x16x32_bf16 v[60:63], v[220:223], v[180:183], v[60:63]
buffer_store_dwordx4 a[188:191], v255, s[48:51], s18 offen offset:2048 sc1
v_mfma_f32_16x16x32_bf16 v[92:95], v[220:223], v[184:187], v[92:95]
s_waitcnt vmcnt(15) lgkmcnt(2)
v_cmp_eq_u32 vcc, 0, v239
v_mfma_f32_16x16x32_bf16 v[124:127], v[220:223], v[188:191], v[124:127]
ds_read_b64_tr_b16 v[220:221], v243 offset:33920
ds_read_b64_tr_b16 v[222:223], v243 offset:34944
v_mfma_f32_16x16x32_bf16 v[0:3], v[192:195], v[128:131], v[0:3]
v_accvgpr_read_b32 v224, a16
v_accvgpr_read_b32 v225, a17
v_mfma_f32_16x16x32_bf16 v[32:35], v[192:195], v[132:135], v[32:35]
buffer_load_dwordx4 v[176:179], v244, s[52:55], s91 offen offset:3072 sc1
v_mfma_f32_16x16x32_bf16 v[64:67], v[192:195], v[136:139], v[64:67]
v_accvgpr_read_b32 v226, a18
v_accvgpr_read_b32 v227, a19
v_mfma_f32_16x16x32_bf16 v[96:99], v[192:195], v[140:143], v[96:99]
ds_read_b64_tr_b16 v[192:193], v240 offset:42240
ds_read_b64_tr_b16 v[194:195], v240 offset:43264
v_mfma_f32_16x16x32_bf16 v[4:7], v[196:199], v[128:131], v[4:7]
v_accvgpr_read_b32 v228, a20
v_accvgpr_read_b32 v229, a21
v_mfma_f32_16x16x32_bf16 v[36:39], v[196:199], v[132:135], v[36:39]
buffer_load_dwordx4 v[180:183], v245, s[52:55], s91 offen offset:3072 sc1
v_mfma_f32_16x16x32_bf16 v[68:71], v[196:199], v[136:139], v[68:71]
s_waitcnt lgkmcnt(2)
v_accvgpr_read_b32 v230, a22
v_mfma_f32_16x16x32_bf16 v[100:103], v[196:199], v[140:143], v[100:103]
ds_read_b64_tr_b16 v[196:197], v241 offset:42240
ds_read_b64_tr_b16 v[198:199], v241 offset:43264
v_mfma_f32_16x16x32_bf16 v[8:11], v[200:203], v[128:131], v[8:11]
s_add_u32 m0, s83, 19072
v_accvgpr_read_b32 v231, a23
v_mfma_f32_16x16x32_bf16 v[40:43], v[200:203], v[132:135], v[40:43]
buffer_load_dwordx4 v234, s[44:47], s80 offen lds
v_mfma_f32_16x16x32_bf16 v[72:75], v[200:203], v[136:139], v[72:75]
v_cvt_pk_bf16_f32 v248, v224, v225
v_cvt_pk_bf16_f32 v249, v226, v227
v_mfma_f32_16x16x32_bf16 v[104:107], v[200:203], v[140:143], v[104:107]
ds_read_b64_tr_b16 v[200:201], v242 offset:42240
ds_read_b64_tr_b16 v[202:203], v242 offset:43264
v_mfma_f32_16x16x32_bf16 v[12:15], v[204:207], v[128:131], v[12:15]
v_cvt_pk_bf16_f32 v250, v228, v229
v_cvt_pk_bf16_f32 v251, v230, v231
v_mfma_f32_16x16x32_bf16 v[44:47], v[204:207], v[132:135], v[44:47]
buffer_load_dwordx4 v[184:187], v246, s[52:55], s91 offen offset:3072 sc1
v_mfma_f32_16x16x32_bf16 v[76:79], v[204:207], v[136:139], v[76:79]
v_accvgpr_write_b32 a16, v248
v_accvgpr_write_b32 a17, v249
v_mfma_f32_16x16x32_bf16 v[108:111], v[204:207], v[140:143], v[108:111]
ds_read_b64_tr_b16 v[204:205], v243 offset:42240
ds_read_b64_tr_b16 v[206:207], v243 offset:43264
v_mfma_f32_16x16x32_bf16 v[16:19], v[208:211], v[128:131], v[16:19]
s_add_u32 m0, s83, 20096
v_accvgpr_write_b32 a18, v250
v_mfma_f32_16x16x32_bf16 v[48:51], v[208:211], v[132:135], v[48:51]
buffer_load_dwordx4 v235, s[44:47], s80 offen lds
v_mfma_f32_16x16x32_bf16 v[80:83], v[208:211], v[136:139], v[80:83]
s_add_u32 s80, s80, 524288
v_accvgpr_write_b32 a19, v251
v_mfma_f32_16x16x32_bf16 v[112:115], v[208:211], v[140:143], v[112:115]
ds_read_b64_tr_b16 v[208:209], v240 offset:42368
ds_read_b64_tr_b16 v[210:211], v240 offset:43392
v_mfma_f32_16x16x32_bf16 v[20:23], v[212:215], v[128:131], v[20:23]
s_mov_b32 s78, 4294950911
s_mul_i32 s70, s70, s22
v_mfma_f32_16x16x32_bf16 v[52:55], v[212:215], v[132:135], v[52:55]
buffer_load_dwordx4 v[188:191], v247, s[52:55], s91 offen offset:3072 sc1
v_mfma_f32_16x16x32_bf16 v[84:87], v[212:215], v[136:139], v[84:87]
s_add_u32 s91, s91, 4096
v_cndmask_b32 v236, 0, v236, vcc
v_mfma_f32_16x16x32_bf16 v[116:119], v[212:215], v[140:143], v[116:119]
ds_read_b64_tr_b16 v[212:213], v241 offset:42368
ds_read_b64_tr_b16 v[214:215], v241 offset:43392
v_mfma_f32_16x16x32_bf16 v[24:27], v[216:219], v[128:131], v[24:27]
s_sub_u32 s92, 4294967295, s3
s_mul_i32 s90, s92, s21
v_mfma_f32_16x16x32_bf16 v[56:59], v[216:219], v[132:135], v[56:59]
s_mov_b32 s17, 1073741824
s_lshl_b32 s92, s73, 2
v_mfma_f32_16x16x32_bf16 v[88:91], v[216:219], v[136:139], v[88:91]
s_lshr_b32 s92, 66051, s92
s_and_b32 s92, s92, 3
v_mfma_f32_16x16x32_bf16 v[120:123], v[216:219], v[140:143], v[120:123]
ds_read_b64_tr_b16 v[216:217], v242 offset:42368
ds_read_b64_tr_b16 v[218:219], v242 offset:43392
v_mfma_f32_16x16x32_bf16 v[28:31], v[220:223], v[128:131], v[28:31]
s_lshl_b32 s16, s92, 16
s_mul_i32 s92, s75, 4294836224
v_mfma_f32_16x16x32_bf16 v[60:63], v[220:223], v[132:135], v[60:63]
buffer_store_dwordx4 a[0:3], v255, s[48:51], s18 offen offset:3072 sc1
v_mfma_f32_16x16x32_bf16 v[92:95], v[220:223], v[136:139], v[92:95]
s_waitcnt vmcnt(15) lgkmcnt(2)
s_add_u32 s92, s92, s88
v_mfma_f32_16x16x32_bf16 v[124:127], v[220:223], v[140:143], v[124:127]
ds_read_b64_tr_b16 v[220:221], v243 offset:42368
ds_read_b64_tr_b16 v[222:223], v243 offset:43392
v_mfma_f32_16x16x32_bf16 v[0:3], v[192:195], v[144:147], v[0:3]
v_accvgpr_read_b32 v224, a88
v_accvgpr_read_b32 v225, a89
v_mfma_f32_16x16x32_bf16 v[32:35], v[192:195], v[148:151], v[32:35]
buffer_load_dwordx4 v[128:131], v244, s[52:55], s91 offen offset:0 sc1
v_mfma_f32_16x16x32_bf16 v[64:67], v[192:195], v[152:155], v[64:67]
v_accvgpr_read_b32 v226, a90
v_accvgpr_read_b32 v227, a91
v_mfma_f32_16x16x32_bf16 v[96:99], v[192:195], v[156:159], v[96:99]
v_accvgpr_read_b32 v228, a92
v_accvgpr_read_b32 v229, a93
v_mfma_f32_16x16x32_bf16 v[4:7], v[196:199], v[144:147], v[4:7]
s_waitcnt lgkmcnt(0)
s_barrier
v_mfma_f32_16x16x32_bf16 v[36:39], v[196:199], v[148:151], v[36:39]
buffer_load_dwordx4 v[132:135], v245, s[52:55], s91 offen offset:0 sc1
v_mfma_f32_16x16x32_bf16 v[68:71], v[196:199], v[152:155], v[68:71]
ds_read_b64_tr_b16 v[192:193], v240 offset:50688
ds_read_b64_tr_b16 v[194:195], v240 offset:51712
v_mfma_f32_16x16x32_bf16 v[100:103], v[196:199], v[156:159], v[100:103]
ds_read_b64_tr_b16 v[196:197], v241 offset:50688
ds_read_b64_tr_b16 v[198:199], v241 offset:51712
v_mfma_f32_16x16x32_bf16 v[8:11], v[200:203], v[144:147], v[8:11]
v_accvgpr_read_b32 v230, a94
v_accvgpr_read_b32 v231, a95
v_mfma_f32_16x16x32_bf16 v[40:43], v[200:203], v[148:151], v[40:43]
buffer_load_dwordx4 v[136:139], v246, s[52:55], s91 offen offset:0 sc1
v_mfma_f32_16x16x32_bf16 v[72:75], v[200:203], v[152:155], v[72:75]
v_cvt_pk_bf16_f32 v248, v224, v225
v_cvt_pk_bf16_f32 v249, v226, v227
v_mfma_f32_16x16x32_bf16 v[104:107], v[200:203], v[156:159], v[104:107]
ds_read_b64_tr_b16 v[200:201], v242 offset:50688
ds_read_b64_tr_b16 v[202:203], v242 offset:51712
v_mfma_f32_16x16x32_bf16 v[12:15], v[204:207], v[144:147], v[12:15]
s_add_u32 m0, s83, 33792
v_cvt_pk_bf16_f32 v250, v228, v229
v_mfma_f32_16x16x32_bf16 v[44:47], v[204:207], v[148:151], v[44:47]
buffer_load_dwordx4 v232, s[44:47], s80 offen lds
v_mfma_f32_16x16x32_bf16 v[76:79], v[204:207], v[152:155], v[76:79]
v_cvt_pk_bf16_f32 v251, v230, v231
v_accvgpr_write_b32 a20, v248
v_mfma_f32_16x16x32_bf16 v[108:111], v[204:207], v[156:159], v[108:111]
ds_read_b64_tr_b16 v[204:205], v243 offset:50688
ds_read_b64_tr_b16 v[206:207], v243 offset:51712
v_mfma_f32_16x16x32_bf16 v[16:19], v[208:211], v[144:147], v[16:19]
v_accvgpr_write_b32 a21, v249
v_accvgpr_write_b32 a22, v250
v_mfma_f32_16x16x32_bf16 v[48:51], v[208:211], v[148:151], v[48:51]
buffer_load_dwordx4 v[140:143], v247, s[52:55], s91 offen offset:0 sc1
v_mfma_f32_16x16x32_bf16 v[80:83], v[208:211], v[152:155], v[80:83]
v_accvgpr_write_b32 a23, v251
s_add_u32 s19, s92, 393216
v_mfma_f32_16x16x32_bf16 v[112:115], v[208:211], v[156:159], v[112:115]
ds_read_b64_tr_b16 v[208:209], v240 offset:50816
ds_read_b64_tr_b16 v[210:211], v240 offset:51840
v_mfma_f32_16x16x32_bf16 v[20:23], v[212:215], v[144:147], v[20:23]
s_add_u32 m0, s83, 34816
v_accvgpr_write_b32 a208, v236
v_mfma_f32_16x16x32_bf16 v[52:55], v[212:215], v[148:151], v[52:55]
buffer_load_dwordx4 v233, s[44:47], s80 offen lds
v_mfma_f32_16x16x32_bf16 v[84:87], v[212:215], v[152:155], v[84:87]
v_accvgpr_write_b32 a209, v236
v_accvgpr_write_b32 a210, v236
v_mfma_f32_16x16x32_bf16 v[116:119], v[212:215], v[156:159], v[116:119]
ds_read_b64_tr_b16 v[212:213], v241 offset:50816
ds_read_b64_tr_b16 v[214:215], v241 offset:51840
v_mfma_f32_16x16x32_bf16 v[24:27], v[216:219], v[144:147], v[24:27]
v_accvgpr_write_b32 a211, v236
v_accvgpr_write_b32 a232, 0
v_mfma_f32_16x16x32_bf16 v[56:59], v[216:219], v[148:151], v[56:59]
v_accvgpr_write_b32 a233, 0
v_accvgpr_write_b32 a234, 0
v_mfma_f32_16x16x32_bf16 v[88:91], v[216:219], v[152:155], v[88:91]
s_mov_b32 s18, 20480
v_accvgpr_write_b32 a235, 0
v_mfma_f32_16x16x32_bf16 v[120:123], v[216:219], v[156:159], v[120:123]
ds_read_b64_tr_b16 v[216:217], v242 offset:50816
ds_read_b64_tr_b16 v[218:219], v242 offset:51840
v_mfma_f32_16x16x32_bf16 v[28:31], v[220:223], v[144:147], v[28:31]
s_lshl_b32 s20, s72, 10
s_add_u32 s20, s20, 16384
v_mfma_f32_16x16x32_bf16 v[60:63], v[220:223], v[148:151], v[60:63]
buffer_store_dwordx4 a[4:7], v255, s[48:51], s18 offen offset:0 sc1
v_mfma_f32_16x16x32_bf16 v[92:95], v[220:223], v[152:155], v[92:95]
s_waitcnt vmcnt(16) lgkmcnt(2)
s_mov_b32 s4, 1985221408
v_mfma_f32_16x16x32_bf16 v[124:127], v[220:223], v[156:159], v[124:127]
ds_read_b64_tr_b16 v[220:221], v243 offset:50816
ds_read_b64_tr_b16 v[222:223], v243 offset:51840
v_mfma_f32_16x16x32_bf16 v[0:3], v[192:195], v[160:163], v[0:3]
v_accvgpr_read_b32 v224, a24
v_accvgpr_read_b32 v225, a25
v_mfma_f32_16x16x32_bf16 v[32:35], v[192:195], v[164:167], v[32:35]
buffer_load_dwordx4 v[144:147], v244, s[52:55], s91 offen offset:1024 sc1
v_mfma_f32_16x16x32_bf16 v[64:67], v[192:195], v[168:171], v[64:67]
v_accvgpr_read_b32 v226, a26
v_accvgpr_read_b32 v227, a27
v_mfma_f32_16x16x32_bf16 v[96:99], v[192:195], v[172:175], v[96:99]
ds_read_b64_tr_b16 v[192:193], v240 offset:59136
ds_read_b64_tr_b16 v[194:195], v240 offset:60160
v_mfma_f32_16x16x32_bf16 v[4:7], v[196:199], v[160:163], v[4:7]
v_accvgpr_read_b32 v228, a28
v_accvgpr_read_b32 v229, a29
v_mfma_f32_16x16x32_bf16 v[36:39], v[196:199], v[164:167], v[36:39]
buffer_load_dwordx4 v[148:151], v245, s[52:55], s91 offen offset:1024 sc1
v_mfma_f32_16x16x32_bf16 v[68:71], v[196:199], v[168:171], v[68:71]
s_waitcnt lgkmcnt(2)
v_accvgpr_read_b32 v230, a30
v_mfma_f32_16x16x32_bf16 v[100:103], v[196:199], v[172:175], v[100:103]
ds_read_b64_tr_b16 v[196:197], v241 offset:59136
ds_read_b64_tr_b16 v[198:199], v241 offset:60160
v_mfma_f32_16x16x32_bf16 v[8:11], v[200:203], v[160:163], v[8:11]
s_add_u32 m0, s83, 35968
v_accvgpr_read_b32 v231, a31
v_mfma_f32_16x16x32_bf16 v[40:43], v[200:203], v[164:167], v[40:43]
buffer_load_dwordx4 v234, s[44:47], s80 offen lds
v_mfma_f32_16x16x32_bf16 v[72:75], v[200:203], v[168:171], v[72:75]
v_cvt_pk_bf16_f32 v248, v224, v225
v_cvt_pk_bf16_f32 v249, v226, v227
v_mfma_f32_16x16x32_bf16 v[104:107], v[200:203], v[172:175], v[104:107]
ds_read_b64_tr_b16 v[200:201], v242 offset:59136
ds_read_b64_tr_b16 v[202:203], v242 offset:60160
v_mfma_f32_16x16x32_bf16 v[12:15], v[204:207], v[160:163], v[12:15]
v_cvt_pk_bf16_f32 v250, v228, v229
v_cvt_pk_bf16_f32 v251, v230, v231
v_mfma_f32_16x16x32_bf16 v[44:47], v[204:207], v[164:167], v[44:47]
buffer_load_dwordx4 v[152:155], v246, s[52:55], s91 offen offset:1024 sc1
v_mfma_f32_16x16x32_bf16 v[76:79], v[204:207], v[168:171], v[76:79]
v_accvgpr_write_b32 a24, v248
v_accvgpr_write_b32 a25, v249
v_mfma_f32_16x16x32_bf16 v[108:111], v[204:207], v[172:175], v[108:111]
ds_read_b64_tr_b16 v[204:205], v243 offset:59136
ds_read_b64_tr_b16 v[206:207], v243 offset:60160
v_mfma_f32_16x16x32_bf16 v[16:19], v[208:211], v[160:163], v[16:19]
s_add_u32 m0, s83, 36992
v_accvgpr_write_b32 a26, v250
v_mfma_f32_16x16x32_bf16 v[48:51], v[208:211], v[164:167], v[48:51]
buffer_load_dwordx4 v235, s[44:47], s80 offen lds
v_mfma_f32_16x16x32_bf16 v[80:83], v[208:211], v[168:171], v[80:83]
s_add_u32 s80, s80, 524288
v_accvgpr_write_b32 a27, v251
v_mfma_f32_16x16x32_bf16 v[112:115], v[208:211], v[172:175], v[112:115]
ds_read_b64_tr_b16 v[208:209], v240 offset:59264
ds_read_b64_tr_b16 v[210:211], v240 offset:60288
v_mfma_f32_16x16x32_bf16 v[20:23], v[212:215], v[160:163], v[20:23]
s_cmp_ge_u32 s2, 64
s_cselect_b32 s4, 1985225488, s4
v_mfma_f32_16x16x32_bf16 v[52:55], v[212:215], v[164:167], v[52:55]
buffer_load_dwordx4 v[156:159], v247, s[52:55], s91 offen offset:1024 sc1
v_mfma_f32_16x16x32_bf16 v[84:87], v[212:215], v[168:171], v[84:87]
s_cmp_ge_u32 s2, 128
s_cselect_b32 s4, 1985229328, s4
v_mfma_f32_16x16x32_bf16 v[116:119], v[212:215], v[172:175], v[116:119]
ds_read_b64_tr_b16 v[212:213], v241 offset:59264
ds_read_b64_tr_b16 v[214:215], v241 offset:60288
v_mfma_f32_16x16x32_bf16 v[24:27], v[216:219], v[160:163], v[24:27]
s_cmp_ge_u32 s2, 192
s_cselect_b32 s4, 1985225008, s4
v_mfma_f32_16x16x32_bf16 v[56:59], v[216:219], v[164:167], v[56:59]
v_bfe_u32 v236, v255, 4, 3
v_lshlrev_b32 v236, 2, v236
v_mfma_f32_16x16x32_bf16 v[88:91], v[216:219], v[168:171], v[88:91]
v_lshrrev_b32 v236, v236, s4
v_and_b32 v236, 15, v236
v_mfma_f32_16x16x32_bf16 v[120:123], v[216:219], v[172:175], v[120:123]
ds_read_b64_tr_b16 v[216:217], v242 offset:59264
ds_read_b64_tr_b16 v[218:219], v242 offset:60288
v_mfma_f32_16x16x32_bf16 v[28:31], v[220:223], v[160:163], v[28:31]
v_xor_b32 v236, s72, v236
v_lshlrev_b32 v236, 3, v236
v_mfma_f32_16x16x32_bf16 v[60:63], v[220:223], v[164:167], v[60:63]
buffer_store_dwordx4 a[8:11], v255, s[48:51], s18 offen offset:1024 sc1
v_mfma_f32_16x16x32_bf16 v[92:95], v[220:223], v[168:171], v[92:95]
s_waitcnt vmcnt(15) lgkmcnt(2)
v_accvgpr_write_b32 a225, v236
v_mfma_f32_16x16x32_bf16 v[124:127], v[220:223], v[172:175], v[124:127]
ds_read_b64_tr_b16 v[220:221], v243 offset:59264
ds_read_b64_tr_b16 v[222:223], v243 offset:60288
v_mfma_f32_16x16x32_bf16 v[0:3], v[192:195], v[176:179], v[0:3]
v_accvgpr_read_b32 v224, a240
v_accvgpr_read_b32 v225, a241
v_mfma_f32_16x16x32_bf16 v[32:35], v[192:195], v[180:183], v[32:35]
buffer_load_dwordx4 v[160:163], v244, s[52:55], s91 offen offset:2048 sc1
v_mfma_f32_16x16x32_bf16 v[64:67], v[192:195], v[184:187], v[64:67]
v_accvgpr_read_b32 v226, a242
v_accvgpr_read_b32 v227, a243
v_mfma_f32_16x16x32_bf16 v[96:99], v[192:195], v[188:191], v[96:99]
v_accvgpr_read_b32 v228, a244
v_accvgpr_read_b32 v229, a245
v_mfma_f32_16x16x32_bf16 v[4:7], v[196:199], v[176:179], v[4:7]
s_waitcnt lgkmcnt(0)
s_barrier
v_mfma_f32_16x16x32_bf16 v[36:39], v[196:199], v[180:183], v[36:39]
buffer_load_dwordx4 v[164:167], v245, s[52:55], s91 offen offset:2048 sc1
v_mfma_f32_16x16x32_bf16 v[68:71], v[196:199], v[184:187], v[68:71]
ds_read_b64_tr_b16 v[192:193], v240 offset:0
ds_read_b64_tr_b16 v[194:195], v240 offset:1024
v_mfma_f32_16x16x32_bf16 v[100:103], v[196:199], v[188:191], v[100:103]
ds_read_b64_tr_b16 v[196:197], v241 offset:0
ds_read_b64_tr_b16 v[198:199], v241 offset:1024
v_mfma_f32_16x16x32_bf16 v[8:11], v[200:203], v[176:179], v[8:11]
v_accvgpr_read_b32 v230, a246
v_accvgpr_read_b32 v231, a247
v_mfma_f32_16x16x32_bf16 v[40:43], v[200:203], v[180:183], v[40:43]
buffer_load_dwordx4 v[168:171], v246, s[52:55], s91 offen offset:2048 sc1
v_mfma_f32_16x16x32_bf16 v[72:75], v[200:203], v[184:187], v[72:75]
v_cvt_pk_bf16_f32 v248, v224, v225
v_cvt_pk_bf16_f32 v249, v226, v227
v_mfma_f32_16x16x32_bf16 v[104:107], v[200:203], v[188:191], v[104:107]
ds_read_b64_tr_b16 v[200:201], v242 offset:0
ds_read_b64_tr_b16 v[202:203], v242 offset:1024
v_mfma_f32_16x16x32_bf16 v[12:15], v[204:207], v[176:179], v[12:15]
s_add_u32 m0, s83, 50688
v_cvt_pk_bf16_f32 v250, v228, v229
v_mfma_f32_16x16x32_bf16 v[44:47], v[204:207], v[180:183], v[44:47]
buffer_load_dwordx4 v232, s[44:47], s80 offen lds
v_mfma_f32_16x16x32_bf16 v[76:79], v[204:207], v[184:187], v[76:79]
v_cvt_pk_bf16_f32 v251, v230, v231
v_accvgpr_write_b32 a28, v248
v_mfma_f32_16x16x32_bf16 v[108:111], v[204:207], v[188:191], v[108:111]
ds_read_b64_tr_b16 v[204:205], v243 offset:0
ds_read_b64_tr_b16 v[206:207], v243 offset:1024
v_mfma_f32_16x16x32_bf16 v[16:19], v[208:211], v[176:179], v[16:19]
v_accvgpr_write_b32 a29, v249
v_accvgpr_write_b32 a30, v250
v_mfma_f32_16x16x32_bf16 v[48:51], v[208:211], v[180:183], v[48:51]
buffer_load_dwordx4 v[172:175], v247, s[52:55], s91 offen offset:2048 sc1
v_mfma_f32_16x16x32_bf16 v[80:83], v[208:211], v[184:187], v[80:83]
v_accvgpr_write_b32 a31, v251
s_lshl3_add_u32 s5, s76, s101
v_mfma_f32_16x16x32_bf16 v[112:115], v[208:211], v[188:191], v[112:115]
ds_read_b64_tr_b16 v[208:209], v240 offset:128
ds_read_b64_tr_b16 v[210:211], v240 offset:1152
v_mfma_f32_16x16x32_bf16 v[20:23], v[212:215], v[176:179], v[20:23]
s_add_u32 m0, s83, 51712
s_lshl2_add_u32 s5, s5, s3
v_mfma_f32_16x16x32_bf16 v[52:55], v[212:215], v[180:183], v[52:55]
buffer_load_dwordx4 v233, s[44:47], s80 offen lds
v_mfma_f32_16x16x32_bf16 v[84:87], v[212:215], v[184:187], v[84:87]
s_lshl_b32 s5, s5, 8
s_add_u32 s5, s5, 1048576
v_mfma_f32_16x16x32_bf16 v[116:119], v[212:215], v[188:191], v[116:119]
ds_read_b64_tr_b16 v[212:213], v241 offset:128
ds_read_b64_tr_b16 v[214:215], v241 offset:1152
v_mfma_f32_16x16x32_bf16 v[24:27], v[216:219], v[176:179], v[24:27]
v_mfma_f32_16x16x32_bf16 v[56:59], v[216:219], v[180:183], v[56:59]
v_mfma_f32_16x16x32_bf16 v[88:91], v[216:219], v[184:187], v[88:91]
v_mfma_f32_16x16x32_bf16 v[120:123], v[216:219], v[188:191], v[120:123]
ds_read_b64_tr_b16 v[216:217], v242 offset:128
ds_read_b64_tr_b16 v[218:219], v242 offset:1152
v_mfma_f32_16x16x32_bf16 v[28:31], v[220:223], v[176:179], v[28:31]
v_mfma_f32_16x16x32_bf16 v[60:63], v[220:223], v[180:183], v[60:63]
v_mfma_f32_16x16x32_bf16 v[92:95], v[220:223], v[184:187], v[92:95]
s_waitcnt vmcnt(15) lgkmcnt(2)
v_mfma_f32_16x16x32_bf16 v[124:127], v[220:223], v[188:191], v[124:127]
ds_read_b64_tr_b16 v[220:221], v243 offset:128
ds_read_b64_tr_b16 v[222:223], v243 offset:1152
v_mfma_f32_16x16x32_bf16 v[0:3], v[192:195], v[128:131], v[0:3]
v_accvgpr_read_b32 v224, a32
v_accvgpr_read_b32 v225, a33
v_mfma_f32_16x16x32_bf16 v[32:35], v[192:195], v[132:135], v[32:35]
buffer_load_dwordx4 v[176:179], v244, s[52:55], s91 offen offset:3072 sc1
v_mfma_f32_16x16x32_bf16 v[64:67], v[192:195], v[136:139], v[64:67]
v_accvgpr_read_b32 v226, a34
v_accvgpr_read_b32 v227, a35
v_mfma_f32_16x16x32_bf16 v[96:99], v[192:195], v[140:143], v[96:99]
ds_read_b64_tr_b16 v[192:193], v240 offset:8448
ds_read_b64_tr_b16 v[194:195], v240 offset:9472
v_mfma_f32_16x16x32_bf16 v[4:7], v[196:199], v[128:131], v[4:7]
v_accvgpr_read_b32 v228, a36
v_accvgpr_read_b32 v229, a37
v_mfma_f32_16x16x32_bf16 v[36:39], v[196:199], v[132:135], v[36:39]
buffer_load_dwordx4 v[180:183], v245, s[52:55], s91 offen offset:3072 sc1
v_mfma_f32_16x16x32_bf16 v[68:71], v[196:199], v[136:139], v[68:71]
s_waitcnt lgkmcnt(2)
v_accvgpr_read_b32 v230, a38
v_mfma_f32_16x16x32_bf16 v[100:103], v[196:199], v[140:143], v[100:103]
ds_read_b64_tr_b16 v[196:197], v241 offset:8448
ds_read_b64_tr_b16 v[198:199], v241 offset:9472
v_mfma_f32_16x16x32_bf16 v[8:11], v[200:203], v[128:131], v[8:11]
s_add_u32 m0, s83, 52864
v_accvgpr_read_b32 v231, a39
v_mfma_f32_16x16x32_bf16 v[40:43], v[200:203], v[132:135], v[40:43]
buffer_load_dwordx4 v234, s[44:47], s80 offen lds
v_mfma_f32_16x16x32_bf16 v[72:75], v[200:203], v[136:139], v[72:75]
v_cvt_pk_bf16_f32 v248, v224, v225
v_cvt_pk_bf16_f32 v249, v226, v227
v_mfma_f32_16x16x32_bf16 v[104:107], v[200:203], v[140:143], v[104:107]
ds_read_b64_tr_b16 v[200:201], v242 offset:8448
ds_read_b64_tr_b16 v[202:203], v242 offset:9472
v_mfma_f32_16x16x32_bf16 v[12:15], v[204:207], v[128:131], v[12:15]
v_cvt_pk_bf16_f32 v250, v228, v229
v_cvt_pk_bf16_f32 v251, v230, v231
v_mfma_f32_16x16x32_bf16 v[44:47], v[204:207], v[132:135], v[44:47]
buffer_load_dwordx4 v[184:187], v246, s[52:55], s91 offen offset:3072 sc1
.La5_LW1_10:
s_cbranch_execz .La5_LW1_R0
v_mfma_f32_16x16x32_bf16 v[76:79], v[204:207], v[136:139], v[76:79]
v_accvgpr_write_b32 a32, v248
v_accvgpr_write_b32 a33, v249
v_mfma_f32_16x16x32_bf16 v[108:111], v[204:207], v[140:143], v[108:111]
ds_read_b64_tr_b16 v[204:205], v243 offset:8448
ds_read_b64_tr_b16 v[206:207], v243 offset:9472
v_mfma_f32_16x16x32_bf16 v[16:19], v[208:211], v[128:131], v[16:19]
s_add_u32 m0, s83, 53888
v_accvgpr_write_b32 a34, v250
v_mfma_f32_16x16x32_bf16 v[48:51], v[208:211], v[132:135], v[48:51]
buffer_load_dwordx4 v235, s[44:47], s80 offen lds
v_mfma_f32_16x16x32_bf16 v[80:83], v[208:211], v[136:139], v[80:83]
v_accvgpr_write_b32 a35, v251
v_mfma_f32_16x16x32_bf16 v[112:115], v[208:211], v[140:143], v[112:115]
ds_read_b64_tr_b16 v[208:209], v240 offset:8576
ds_read_b64_tr_b16 v[210:211], v240 offset:9600
v_mfma_f32_16x16x32_bf16 v[20:23], v[212:215], v[128:131], v[20:23]
v_mfma_f32_16x16x32_bf16 v[52:55], v[212:215], v[132:135], v[52:55]
buffer_load_dwordx4 v[188:191], v247, s[52:55], s91 offen offset:3072 sc1
v_mfma_f32_16x16x32_bf16 v[84:87], v[212:215], v[136:139], v[84:87]
s_add_u32 s91, s91, 4096
v_mfma_f32_16x16x32_bf16 v[116:119], v[212:215], v[140:143], v[116:119]
ds_read_b64_tr_b16 v[212:213], v241 offset:8576
ds_read_b64_tr_b16 v[214:215], v241 offset:9600
v_mfma_f32_16x16x32_bf16 v[24:27], v[216:219], v[128:131], v[24:27]
v_mfma_f32_16x16x32_bf16 v[56:59], v[216:219], v[132:135], v[56:59]
v_mfma_f32_16x16x32_bf16 v[88:91], v[216:219], v[136:139], v[88:91]
v_mfma_f32_16x16x32_bf16 v[120:123], v[216:219], v[140:143], v[120:123]
ds_read_b64_tr_b16 v[216:217], v242 offset:8576
ds_read_b64_tr_b16 v[218:219], v242 offset:9600
v_mfma_f32_16x16x32_bf16 v[28:31], v[220:223], v[128:131], v[28:31]
v_mfma_f32_16x16x32_bf16 v[60:63], v[220:223], v[132:135], v[60:63]
buffer_store_dwordx4 a[12:15], v255, s[48:51], s18 offen offset:2048 sc1
v_mfma_f32_16x16x32_bf16 v[92:95], v[220:223], v[136:139], v[92:95]
s_waitcnt vmcnt(14) lgkmcnt(2)
v_mfma_f32_16x16x32_bf16 v[124:127], v[220:223], v[140:143], v[124:127]
ds_read_b64_tr_b16 v[220:221], v243 offset:8576
ds_read_b64_tr_b16 v[222:223], v243 offset:9600
v_mfma_f32_16x16x32_bf16 v[0:3], v[192:195], v[144:147], v[0:3]
v_accvgpr_read_b32 v224, a248
v_accvgpr_read_b32 v225, a249
v_mfma_f32_16x16x32_bf16 v[32:35], v[192:195], v[148:151], v[32:35]
buffer_load_dwordx4 v[128:131], v244, s[52:55], s91 offen offset:0 sc1
v_mfma_f32_16x16x32_bf16 v[64:67], v[192:195], v[152:155], v[64:67]
v_accvgpr_read_b32 v226, a250
v_accvgpr_read_b32 v227, a251
v_mfma_f32_16x16x32_bf16 v[96:99], v[192:195], v[156:159], v[96:99]
v_accvgpr_read_b32 v228, a252
v_accvgpr_read_b32 v229, a253
v_mfma_f32_16x16x32_bf16 v[4:7], v[196:199], v[144:147], v[4:7]
s_waitcnt lgkmcnt(0)
s_barrier
v_mfma_f32_16x16x32_bf16 v[36:39], v[196:199], v[148:151], v[36:39]
buffer_load_dwordx4 v[132:135], v245, s[52:55], s91 offen offset:0 sc1
v_mfma_f32_16x16x32_bf16 v[68:71], v[196:199], v[152:155], v[68:71]
ds_read_b64_tr_b16 v[192:193], v240 offset:16896
ds_read_b64_tr_b16 v[194:195], v240 offset:17920
v_mfma_f32_16x16x32_bf16 v[100:103], v[196:199], v[156:159], v[100:103]
ds_read_b64_tr_b16 v[196:197], v241 offset:16896
ds_read_b64_tr_b16 v[198:199], v241 offset:17920
v_mfma_f32_16x16x32_bf16 v[8:11], v[200:203], v[144:147], v[8:11]
v_accvgpr_read_b32 v230, a254
v_accvgpr_read_b32 v231, a255
v_mfma_f32_16x16x32_bf16 v[40:43], v[200:203], v[148:151], v[40:43]
buffer_load_dwordx4 v[136:139], v246, s[52:55], s91 offen offset:0 sc1
v_mfma_f32_16x16x32_bf16 v[72:75], v[200:203], v[152:155], v[72:75]
v_cvt_pk_bf16_f32 v248, v224, v225
v_cvt_pk_bf16_f32 v249, v226, v227
v_mfma_f32_16x16x32_bf16 v[104:107], v[200:203], v[156:159], v[104:107]
ds_read_b64_tr_b16 v[200:201], v242 offset:16896
ds_read_b64_tr_b16 v[202:203], v242 offset:17920
v_mfma_f32_16x16x32_bf16 v[12:15], v[204:207], v[144:147], v[12:15]
v_cvt_pk_bf16_f32 v250, v228, v229
v_cvt_pk_bf16_f32 v251, v230, v231
v_mfma_f32_16x16x32_bf16 v[44:47], v[204:207], v[148:151], v[44:47]
buffer_load_dwordx4 v[140:143], v247, s[52:55], s91 offen offset:0 sc1
v_mfma_f32_16x16x32_bf16 v[76:79], v[204:207], v[152:155], v[76:79]
v_accvgpr_write_b32 a36, v248
v_accvgpr_write_b32 a37, v249
v_mfma_f32_16x16x32_bf16 v[108:111], v[204:207], v[156:159], v[108:111]
ds_read_b64_tr_b16 v[204:205], v243 offset:16896
ds_read_b64_tr_b16 v[206:207], v243 offset:17920
v_mfma_f32_16x16x32_bf16 v[16:19], v[208:211], v[144:147], v[16:19]
v_accvgpr_write_b32 a38, v250
v_accvgpr_write_b32 a39, v251
v_mfma_f32_16x16x32_bf16 v[48:51], v[208:211], v[148:151], v[48:51]
s_getpc_b64 s[92:93]
.La5_LCT2:
s_sub_u32 s92, s92, .La5_LCT2-.La5_LSTART
s_subb_u32 s93, s93, 0
v_mfma_f32_16x16x32_bf16 v[80:83], v[208:211], v[152:155], v[80:83]
s_and_b32 s93, s93, 0xffff
s_lshr_b32 s94, s2, 3
v_mfma_f32_16x16x32_bf16 v[112:115], v[208:211], v[156:159], v[112:115]
ds_read_b64_tr_b16 v[208:209], v240 offset:17024
ds_read_b64_tr_b16 v[210:211], v240 offset:18048
v_mfma_f32_16x16x32_bf16 v[20:23], v[212:215], v[144:147], v[20:23]
s_lshl2_add_u32 s94, s94, s3
s_lshl_b32 s94, s94, 10
v_mfma_f32_16x16x32_bf16 v[52:55], v[212:215], v[148:151], v[52:55]
v_lshrrev_b32 v233, 7, v255
v_lshlrev_b32 v232, 3, v255
v_mfma_f32_16x16x32_bf16 v[84:87], v[212:215], v[152:155], v[84:87]
v_lshl_add_u32 v232, v233, 28, v232
v_add_u32 v232, s94, v232
v_mfma_f32_16x16x32_bf16 v[116:119], v[212:215], v[156:159], v[116:119]
ds_read_b64_tr_b16 v[212:213], v241 offset:17024
ds_read_b64_tr_b16 v[214:215], v241 offset:18048
v_mfma_f32_16x16x32_bf16 v[24:27], v[216:219], v[144:147], v[24:27]
s_mov_b32 s94, .La5_LCODEEND-.La5_LSTART
s_mov_b32 s95, 0x20000
v_mfma_f32_16x16x32_bf16 v[56:59], v[216:219], v[148:151], v[56:59]
buffer_load_dword a253, v232, s[92:95], 0 offen
v_mfma_f32_16x16x32_bf16 v[88:91], v[216:219], v[152:155], v[88:91]
s_mov_b32 s80, 8192
s_add_u32 s83, s83, 4294886912
v_mfma_f32_16x16x32_bf16 v[120:123], v[216:219], v[156:159], v[120:123]
ds_read_b64_tr_b16 v[216:217], v242 offset:17024
ds_read_b64_tr_b16 v[218:219], v242 offset:18048
v_mfma_f32_16x16x32_bf16 v[28:31], v[220:223], v[144:147], v[28:31]
ds_write_b128 v238, a[12:15] offset:6144
v_mfma_f32_16x16x32_bf16 v[60:63], v[220:223], v[148:151], v[60:63]
buffer_store_dwordx4 a[16:19], v255, s[48:51], s18 offen offset:3072 sc1
v_mfma_f32_16x16x32_bf16 v[92:95], v[220:223], v[152:155], v[92:95]
s_waitcnt vmcnt(14) lgkmcnt(3)
s_lshl_b32 s92, s76, 21
v_mfma_f32_16x16x32_bf16 v[124:127], v[220:223], v[156:159], v[124:127]
ds_read_b64_tr_b16 v[220:221], v243 offset:17024
ds_read_b64_tr_b16 v[222:223], v243 offset:18048
v_mfma_f32_16x16x32_bf16 v[0:3], v[192:195], v[160:163], v[0:3]
v_accvgpr_read_b32 v224, a40
v_accvgpr_read_b32 v225, a41
v_mfma_f32_16x16x32_bf16 v[32:35], v[192:195], v[164:167], v[32:35]
buffer_load_dwordx4 a[192:195], v244, s[52:55], s91 offen offset:1024 sc1
v_mfma_f32_16x16x32_bf16 v[64:67], v[192:195], v[168:171], v[64:67]
v_accvgpr_read_b32 v226, a42
v_accvgpr_read_b32 v227, a43
v_mfma_f32_16x16x32_bf16 v[96:99], v[192:195], v[172:175], v[96:99]
ds_read_b64_tr_b16 v[192:193], v240 offset:25344
ds_read_b64_tr_b16 v[194:195], v240 offset:26368
v_mfma_f32_16x16x32_bf16 v[4:7], v[196:199], v[160:163], v[4:7]
v_accvgpr_read_b32 v228, a44
v_accvgpr_read_b32 v229, a45
v_mfma_f32_16x16x32_bf16 v[36:39], v[196:199], v[164:167], v[36:39]
buffer_load_dwordx4 a[196:199], v245, s[52:55], s91 offen offset:1024 sc1
v_mfma_f32_16x16x32_bf16 v[68:71], v[196:199], v[168:171], v[68:71]
s_waitcnt lgkmcnt(2)
v_accvgpr_read_b32 v230, a46
v_mfma_f32_16x16x32_bf16 v[100:103], v[196:199], v[172:175], v[100:103]
ds_read_b64_tr_b16 v[196:197], v241 offset:25344
ds_read_b64_tr_b16 v[198:199], v241 offset:26368
v_mfma_f32_16x16x32_bf16 v[8:11], v[200:203], v[160:163], v[8:11]
v_accvgpr_read_b32 v231, a47
v_cvt_pk_bf16_f32 v248, v224, v225
v_mfma_f32_16x16x32_bf16 v[40:43], v[200:203], v[164:167], v[40:43]
buffer_load_dwordx4 a[200:203], v246, s[52:55], s91 offen offset:1024 sc1
v_mfma_f32_16x16x32_bf16 v[72:75], v[200:203], v[168:171], v[72:75]
v_cvt_pk_bf16_f32 v249, v226, v227
v_cvt_pk_bf16_f32 v250, v228, v229
v_mfma_f32_16x16x32_bf16 v[104:107], v[200:203], v[172:175], v[104:107]
ds_read_b64_tr_b16 v[200:201], v242 offset:25344
ds_read_b64_tr_b16 v[202:203], v242 offset:26368
v_mfma_f32_16x16x32_bf16 v[12:15], v[204:207], v[160:163], v[12:15]
v_cvt_pk_bf16_f32 v251, v230, v231
v_accvgpr_write_b32 a40, v248
v_mfma_f32_16x16x32_bf16 v[44:47], v[204:207], v[164:167], v[44:47]
buffer_load_dwordx4 a[204:207], v247, s[52:55], s91 offen offset:1024 sc1
v_mfma_f32_16x16x32_bf16 v[76:79], v[204:207], v[168:171], v[76:79]
v_accvgpr_write_b32 a41, v249
v_accvgpr_write_b32 a42, v250
v_mfma_f32_16x16x32_bf16 v[108:111], v[204:207], v[172:175], v[108:111]
ds_read_b64_tr_b16 v[204:205], v243 offset:25344
ds_read_b64_tr_b16 v[206:207], v243 offset:26368
v_mfma_f32_16x16x32_bf16 v[16:19], v[208:211], v[160:163], v[16:19]
v_accvgpr_write_b32 a43, v251
s_add_u32 s92, s92, 54525952
v_mfma_f32_16x16x32_bf16 v[48:51], v[208:211], v[164:167], v[48:51]
buffer_load_dwordx4 a[96:99], v244, s[52:55], s91 offen offset:2048 sc1
v_mfma_f32_16x16x32_bf16 v[80:83], v[208:211], v[168:171], v[80:83]
s_add_u32 s44, s26, s92
s_addc_u32 s45, s27, 0
v_mfma_f32_16x16x32_bf16 v[112:115], v[208:211], v[172:175], v[112:115]
ds_read_b64_tr_b16 v[208:209], v240 offset:25472
ds_read_b64_tr_b16 v[210:211], v240 offset:26496
v_mfma_f32_16x16x32_bf16 v[20:23], v[212:215], v[160:163], v[20:23]
s_and_b32 s45, s45, 0xffff
s_mov_b32 s46, 2097536
v_mfma_f32_16x16x32_bf16 v[52:55], v[212:215], v[164:167], v[52:55]
buffer_load_dwordx4 a[100:103], v245, s[52:55], s91 offen offset:2048 sc1
v_mfma_f32_16x16x32_bf16 v[84:87], v[212:215], v[168:171], v[84:87]
s_mov_b32 s95, 4096
s_mov_b32 s96, 8192
v_mfma_f32_16x16x32_bf16 v[116:119], v[212:215], v[172:175], v[116:119]
ds_read_b64_tr_b16 v[212:213], v241 offset:25472
ds_read_b64_tr_b16 v[214:215], v241 offset:26496
v_mfma_f32_16x16x32_bf16 v[24:27], v[216:219], v[160:163], v[24:27]
s_mov_b32 s97, 12288
s_mul_i32 s92, s3, 196608
v_mfma_f32_16x16x32_bf16 v[56:59], v[216:219], v[164:167], v[56:59]
buffer_load_dwordx4 a[104:107], v246, s[52:55], s91 offen offset:2048 sc1
v_mfma_f32_16x16x32_bf16 v[88:91], v[216:219], v[168:171], v[88:91]
s_mov_b32 s18, 0
s_sub_u32 s93, 1376256, s92
v_mfma_f32_16x16x32_bf16 v[120:123], v[216:219], v[172:175], v[120:123]
ds_read_b64_tr_b16 v[216:217], v242 offset:25472
ds_read_b64_tr_b16 v[218:219], v242 offset:26496
v_mfma_f32_16x16x32_bf16 v[28:31], v[220:223], v[160:163], v[28:31]
ds_write_b128 v238, a[16:19] offset:7168
v_mfma_f32_16x16x32_bf16 v[60:63], v[220:223], v[164:167], v[60:63]
buffer_store_dwordx4 a[20:23], v255, s[48:51], s18 offen offset:0 sc1
v_mfma_f32_16x16x32_bf16 v[92:95], v[220:223], v[168:171], v[92:95]
s_waitcnt vmcnt(15) lgkmcnt(3)
v_mfma_f32_16x16x32_bf16 v[124:127], v[220:223], v[172:175], v[124:127]
ds_read_b64_tr_b16 v[220:221], v243 offset:25472
ds_read_b64_tr_b16 v[222:223], v243 offset:26496
v_mfma_f32_16x16x32_bf16 v[0:3], v[192:195], v[176:179], v[0:3]
v_accvgpr_read_b32 v224, a48
v_accvgpr_read_b32 v225, a49
v_mfma_f32_16x16x32_bf16 v[32:35], v[192:195], v[180:183], v[32:35]
buffer_load_dwordx4 a[108:111], v247, s[52:55], s91 offen offset:2048 sc1
v_mfma_f32_16x16x32_bf16 v[64:67], v[192:195], v[184:187], v[64:67]
v_accvgpr_read_b32 v226, a50
v_accvgpr_read_b32 v227, a51
v_mfma_f32_16x16x32_bf16 v[96:99], v[192:195], v[188:191], v[96:99]
v_accvgpr_read_b32 v228, a52
v_accvgpr_read_b32 v229, a53
v_mfma_f32_16x16x32_bf16 v[4:7], v[196:199], v[176:179], v[4:7]
s_waitcnt lgkmcnt(0)
s_barrier
v_mfma_f32_16x16x32_bf16 v[36:39], v[196:199], v[180:183], v[36:39]
buffer_load_dwordx4 a[112:115], v244, s[52:55], s91 offen offset:3072 sc1
v_mfma_f32_16x16x32_bf16 v[68:71], v[196:199], v[184:187], v[68:71]
ds_read_b64_tr_b16 v[192:193], v240 offset:33792
ds_read_b64_tr_b16 v[194:195], v240 offset:34816
v_mfma_f32_16x16x32_bf16 v[100:103], v[196:199], v[188:191], v[100:103]
ds_read_b64_tr_b16 v[196:197], v241 offset:33792
ds_read_b64_tr_b16 v[198:199], v241 offset:34816
v_mfma_f32_16x16x32_bf16 v[8:11], v[200:203], v[176:179], v[8:11]
v_accvgpr_read_b32 v230, a54
v_accvgpr_read_b32 v231, a55
v_mfma_f32_16x16x32_bf16 v[40:43], v[200:203], v[180:183], v[40:43]
buffer_load_dwordx4 a[116:119], v245, s[52:55], s91 offen offset:3072 sc1
v_mfma_f32_16x16x32_bf16 v[72:75], v[200:203], v[184:187], v[72:75]
v_cvt_pk_bf16_f32 v248, v224, v225
v_cvt_pk_bf16_f32 v249, v226, v227
v_mfma_f32_16x16x32_bf16 v[104:107], v[200:203], v[188:191], v[104:107]
ds_read_b64_tr_b16 v[200:201], v242 offset:33792
ds_read_b64_tr_b16 v[202:203], v242 offset:34816
v_mfma_f32_16x16x32_bf16 v[12:15], v[204:207], v[176:179], v[12:15]
ds_write_b128 v238, a[20:23] offset:32768
v_mfma_f32_16x16x32_bf16 v[44:47], v[204:207], v[180:183], v[44:47]
buffer_load_dwordx4 a[120:123], v246, s[52:55], s91 offen offset:3072 sc1
v_mfma_f32_16x16x32_bf16 v[76:79], v[204:207], v[184:187], v[76:79]
v_cvt_pk_bf16_f32 v250, v228, v229
v_cvt_pk_bf16_f32 v251, v230, v231
v_mfma_f32_16x16x32_bf16 v[108:111], v[204:207], v[188:191], v[108:111]
ds_read_b64_tr_b16 v[204:205], v243 offset:33792
ds_read_b64_tr_b16 v[206:207], v243 offset:34816
v_mfma_f32_16x16x32_bf16 v[16:19], v[208:211], v[176:179], v[16:19]
v_accvgpr_write_b32 a44, v248
v_accvgpr_write_b32 a45, v249
v_mfma_f32_16x16x32_bf16 v[48:51], v[208:211], v[180:183], v[48:51]
buffer_load_dwordx4 a[124:127], v247, s[52:55], s91 offen offset:3072 sc1
v_mfma_f32_16x16x32_bf16 v[80:83], v[208:211], v[184:187], v[80:83]
v_accvgpr_write_b32 a46, v250
v_accvgpr_write_b32 a47, v251
v_mfma_f32_16x16x32_bf16 v[112:115], v[208:211], v[188:191], v[112:115]
ds_read_b64_tr_b16 v[208:209], v240 offset:33920
ds_read_b64_tr_b16 v[210:211], v240 offset:34944
v_mfma_f32_16x16x32_bf16 v[20:23], v[212:215], v[176:179], v[20:23]
v_mfma_f32_16x16x32_bf16 v[52:55], v[212:215], v[180:183], v[52:55]
v_mfma_f32_16x16x32_bf16 v[84:87], v[212:215], v[184:187], v[84:87]
v_mfma_f32_16x16x32_bf16 v[116:119], v[212:215], v[188:191], v[116:119]
ds_read_b64_tr_b16 v[212:213], v241 offset:33920
ds_read_b64_tr_b16 v[214:215], v241 offset:34944
v_mfma_f32_16x16x32_bf16 v[24:27], v[216:219], v[176:179], v[24:27]
v_mfma_f32_16x16x32_bf16 v[56:59], v[216:219], v[180:183], v[56:59]
buffer_store_dwordx4 a[24:27], v255, s[48:51], s18 offen offset:1024 sc1
v_mfma_f32_16x16x32_bf16 v[88:91], v[216:219], v[184:187], v[88:91]
v_mfma_f32_16x16x32_bf16 v[120:123], v[216:219], v[188:191], v[120:123]
ds_read_b64_tr_b16 v[216:217], v242 offset:33920
ds_read_b64_tr_b16 v[218:219], v242 offset:34944
v_mfma_f32_16x16x32_bf16 v[28:31], v[220:223], v[176:179], v[28:31]
ds_write_b128 v238, a[24:27] offset:33792
v_mfma_f32_16x16x32_bf16 v[60:63], v[220:223], v[180:183], v[60:63]
buffer_store_dwordx4 a[28:31], v255, s[48:51], s18 offen offset:2048 sc1
v_mfma_f32_16x16x32_bf16 v[92:95], v[220:223], v[184:187], v[92:95]
s_waitcnt vmcnt(17) lgkmcnt(3)
v_mfma_f32_16x16x32_bf16 v[124:127], v[220:223], v[188:191], v[124:127]
ds_read_b64_tr_b16 v[220:221], v243 offset:33920
ds_read_b64_tr_b16 v[222:223], v243 offset:34944
v_mfma_f32_16x16x32_bf16 v[0:3], v[192:195], v[128:131], v[0:3]
v_accvgpr_read_b32 v224, a56
v_accvgpr_read_b32 v225, a57
v_mfma_f32_16x16x32_bf16 v[32:35], v[192:195], v[132:135], v[32:35]
v_accvgpr_read_b32 v226, a58
v_accvgpr_read_b32 v227, a59
v_mfma_f32_16x16x32_bf16 v[64:67], v[192:195], v[136:139], v[64:67]
v_accvgpr_read_b32 v228, a60
v_accvgpr_read_b32 v229, a61
v_mfma_f32_16x16x32_bf16 v[96:99], v[192:195], v[140:143], v[96:99]
ds_read_b64_tr_b16 v[192:193], v240 offset:42240
ds_read_b64_tr_b16 v[194:195], v240 offset:43264
v_mfma_f32_16x16x32_bf16 v[4:7], v[196:199], v[128:131], v[4:7]
v_accvgpr_read_b32 v230, a62
v_accvgpr_read_b32 v231, a63
v_mfma_f32_16x16x32_bf16 v[36:39], v[196:199], v[132:135], v[36:39]
v_cvt_pk_bf16_f32 v248, v224, v225
v_cvt_pk_bf16_f32 v249, v226, v227
v_mfma_f32_16x16x32_bf16 v[68:71], v[196:199], v[136:139], v[68:71]
s_waitcnt lgkmcnt(2)
v_cvt_pk_bf16_f32 v250, v228, v229
v_mfma_f32_16x16x32_bf16 v[100:103], v[196:199], v[140:143], v[100:103]
ds_read_b64_tr_b16 v[196:197], v240 offset:50688
ds_read_b64_tr_b16 v[198:199], v240 offset:51712
v_mfma_f32_16x16x32_bf16 v[8:11], v[200:203], v[128:131], v[8:11]
v_cvt_pk_bf16_f32 v251, v230, v231
v_accvgpr_write_b32 a48, v248
v_mfma_f32_16x16x32_bf16 v[40:43], v[200:203], v[132:135], v[40:43]
v_accvgpr_write_b32 a49, v249
v_accvgpr_write_b32 a50, v250
v_mfma_f32_16x16x32_bf16 v[72:75], v[200:203], v[136:139], v[72:75]
v_accvgpr_write_b32 a51, v251
v_add_u32 v244, s92, v244
v_mfma_f32_16x16x32_bf16 v[104:107], v[200:203], v[140:143], v[104:107]
ds_read_b64_tr_b16 v[200:201], v240 offset:59136
ds_read_b64_tr_b16 v[202:203], v240 offset:60160
v_mfma_f32_16x16x32_bf16 v[12:15], v[204:207], v[128:131], v[12:15]
ds_write_b128 v238, a[28:31] offset:34816
v_mfma_f32_16x16x32_bf16 v[44:47], v[204:207], v[132:135], v[44:47]
v_add_u32 v246, s93, v246
v_add_u32 v245, 0x1000, v244
v_mfma_f32_16x16x32_bf16 v[76:79], v[204:207], v[136:139], v[76:79]
v_add_u32 v247, 0x1000, v246
s_lshl_b32 s91, s72, 13
v_mfma_f32_16x16x32_bf16 v[108:111], v[204:207], v[140:143], v[108:111]
ds_read_b64_tr_b16 v[204:205], v241 offset:42240
ds_read_b64_tr_b16 v[206:207], v241 offset:43264
v_mfma_f32_16x16x32_bf16 v[16:19], v[208:211], v[128:131], v[16:19]
s_lshl4_add_u32 s92, s76, s3
s_lshl3_add_u32 s92, s92, s20
v_mfma_f32_16x16x32_bf16 v[48:51], v[208:211], v[132:135], v[48:51]
s_bitset1_b32 s20, 30
v_mfma_f32_16x16x32_bf16 v[80:83], v[208:211], v[136:139], v[80:83]
v_mfma_f32_16x16x32_bf16 v[112:115], v[208:211], v[140:143], v[112:115]
ds_read_b64_tr_b16 v[208:209], v241 offset:50688
ds_read_b64_tr_b16 v[210:211], v241 offset:51712
v_mfma_f32_16x16x32_bf16 v[20:23], v[212:215], v[128:131], v[20:23]
v_mfma_f32_16x16x32_bf16 v[52:55], v[212:215], v[132:135], v[52:55]
v_mfma_f32_16x16x32_bf16 v[84:87], v[212:215], v[136:139], v[84:87]
v_mfma_f32_16x16x32_bf16 v[116:119], v[212:215], v[140:143], v[116:119]
ds_read_b64_tr_b16 v[212:213], v241 offset:59136
ds_read_b64_tr_b16 v[214:215], v241 offset:60160
v_mfma_f32_16x16x32_bf16 v[24:27], v[216:219], v[128:131], v[24:27]
v_mfma_f32_16x16x32_bf16 v[56:59], v[216:219], v[132:135], v[56:59]
buffer_store_dwordx4 a[32:35], v255, s[48:51], s18 offen offset:3072 sc1
v_mfma_f32_16x16x32_bf16 v[88:91], v[216:219], v[136:139], v[88:91]
s_mov_b32 s18, 4096
v_mfma_f32_16x16x32_bf16 v[120:123], v[216:219], v[140:143], v[120:123]
ds_read_b64_tr_b16 v[216:217], v242 offset:42240
ds_read_b64_tr_b16 v[218:219], v242 offset:43264
v_mfma_f32_16x16x32_bf16 v[28:31], v[220:223], v[128:131], v[28:31]
ds_write_b128 v238, a[32:35] offset:35840
v_mfma_f32_16x16x32_bf16 v[60:63], v[220:223], v[132:135], v[60:63]
buffer_store_dwordx4 a[36:39], v255, s[48:51], s18 offen offset:0 sc1
v_mfma_f32_16x16x32_bf16 v[92:95], v[220:223], v[136:139], v[92:95]
s_waitcnt vmcnt(4) lgkmcnt(3)
v_mfma_f32_16x16x32_bf16 v[124:127], v[220:223], v[140:143], v[124:127]
ds_read_b64_tr_b16 v[220:221], v242 offset:50688
ds_read_b64_tr_b16 v[222:223], v242 offset:51712
v_mfma_f32_16x16x32_bf16 v[0:3], v[192:195], a[192:195], v[0:3]
s_mov_b64 s[52:53], s[86:87]
v_mfma_f32_16x16x32_bf16 v[32:35], v[192:195], a[196:199], v[32:35]
s_bitcmp1_b32 21, s73
s_cselect_b32 s54, 16384, 0
s_cselect_b32 s86, s88, 1073741824
v_mfma_f32_16x16x32_bf16 v[64:67], v[192:195], a[200:203], v[64:67]
s_lshl_b32 s89, s3, 14
v_add_u32 v248, s89, v255
v_mfma_f32_16x16x32_bf16 v[96:99], v[192:195], a[204:207], v[96:99]
ds_read_b64_tr_b16 v[192:193], v242 offset:59136
ds_read_b64_tr_b16 v[194:195], v242 offset:60160
v_mfma_f32_16x16x32_bf16 v[0:3], v[196:199], a[96:99], v[0:3]
ds_write_b128 v238, a[36:39] offset:36864
v_mfma_f32_16x16x32_bf16 v[32:35], v[196:199], a[100:103], v[32:35]
s_add_u32 s86, s86, 262144
v_mfma_f32_16x16x32_bf16 v[64:67], v[196:199], a[104:107], v[64:67]
s_waitcnt lgkmcnt(3)
v_mfma_f32_16x16x32_bf16 v[96:99], v[196:199], a[108:111], v[96:99]
ds_read_b64_tr_b16 v[196:197], v243 offset:42240
ds_read_b64_tr_b16 v[198:199], v243 offset:43264
v_mfma_f32_16x16x32_bf16 v[0:3], v[200:203], a[112:115], v[0:3]
s_cmp_eq_u32 s73, 0
s_cselect_b32 s86, s92, s86
s_cselect_b32 s92, 1073741824, s92
v_mfma_f32_16x16x32_bf16 v[32:35], v[200:203], a[116:119], v[32:35]
s_mov_b32 s87, s92
s_add_u32 s89, s89, 16384
v_mfma_f32_16x16x32_bf16 v[64:67], v[200:203], a[120:123], v[64:67]
s_lshl_b32 m0, s3, 12
s_add_u32 m0, m0, 131072
v_mfma_f32_16x16x32_bf16 v[96:99], v[200:203], a[124:127], v[96:99]
ds_read_b64_tr_b16 v[200:201], v243 offset:50688
ds_read_b64_tr_b16 v[202:203], v243 offset:51712
v_mfma_f32_16x16x32_bf16 v[4:7], v[204:207], a[192:195], v[4:7]
ds_write_b128 v238, a[40:43] offset:37888
v_mfma_f32_16x16x32_bf16 v[36:39], v[204:207], a[196:199], v[36:39]
v_bfe_u32 v251, v255, 2, 5
v_and_b32 v224, 0x380, v255
v_mfma_f32_16x16x32_bf16 v[68:71], v[204:207], a[200:203], v[68:71]
v_add_u32 v251, v251, v224
v_and_b32 v224, 16, v255
v_mfma_f32_16x16x32_bf16 v[100:103], v[204:207], a[204:207], v[100:103]
ds_read_b64_tr_b16 v[204:205], v243 offset:59136
ds_read_b64_tr_b16 v[206:207], v243 offset:60160
v_mfma_f32_16x16x32_bf16 v[4:7], v[208:211], a[96:99], v[4:7]
v_cmp_eq_u32 vcc, 0, v224
v_mfma_f32_16x16x32_bf16 v[36:39], v[208:211], a[100:103], v[36:39]
v_cndmask_b32 v249, v253, v252, vcc
v_mfma_f32_16x16x32_bf16 v[68:71], v[208:211], a[104:107], v[68:71]
v_mfma_f32_16x16x32_bf16 v[100:103], v[208:211], a[108:111], v[100:103]
ds_read_b64_tr_b16 v[208:209], v240 offset:42368
ds_read_b64_tr_b16 v[210:211], v240 offset:43392
v_mfma_f32_16x16x32_bf16 v[4:7], v[212:215], a[112:115], v[4:7]
ds_write_b128 v238, a[44:47] offset:38912
v_mfma_f32_16x16x32_bf16 v[36:39], v[212:215], a[116:119], v[36:39]
buffer_store_dwordx4 a[40:43], v255, s[48:51], s18 offen offset:1024 sc1
v_mfma_f32_16x16x32_bf16 v[68:71], v[212:215], a[120:123], v[68:71]
v_mfma_f32_16x16x32_bf16 v[100:103], v[212:215], a[124:127], v[100:103]
ds_read_b64_tr_b16 v[212:213], v240 offset:50816
ds_read_b64_tr_b16 v[214:215], v240 offset:51840
v_mfma_f32_16x16x32_bf16 v[8:11], v[216:219], a[192:195], v[8:11]
v_mfma_f32_16x16x32_bf16 v[40:43], v[216:219], a[196:199], v[40:43]
v_mfma_f32_16x16x32_bf16 v[72:75], v[216:219], a[200:203], v[72:75]
v_cvt_pk_bf16_f32 v128, v0, v1
v_cvt_pk_bf16_f32 v129, v2, v3
v_mfma_f32_16x16x32_bf16 v[104:107], v[216:219], a[204:207], v[104:107]
ds_read_b64_tr_b16 v[216:217], v240 offset:59264
ds_read_b64_tr_b16 v[218:219], v240 offset:60288
v_mfma_f32_16x16x32_bf16 v[8:11], v[220:223], a[96:99], v[8:11]
v_cvt_pk_bf16_f32 v130, v4, v5
v_cvt_pk_bf16_f32 v131, v6, v7
v_mfma_f32_16x16x32_bf16 v[40:43], v[220:223], a[100:103], v[40:43]
buffer_store_dwordx4 a[44:47], v255, s[48:51], s18 offen offset:2048 sc1
v_mfma_f32_16x16x32_bf16 v[72:75], v[220:223], a[104:107], v[72:75]
s_waitcnt lgkmcnt(2)
v_cvt_pk_bf16_f32 v144, v32, v33
v_mfma_f32_16x16x32_bf16 v[104:107], v[220:223], a[108:111], v[104:107]
ds_read_b64_tr_b16 v[220:221], v241 offset:42368
ds_read_b64_tr_b16 v[222:223], v241 offset:43392
v_mfma_f32_16x16x32_bf16 v[8:11], v[192:195], a[112:115], v[8:11]
v_cvt_pk_bf16_f32 v145, v34, v35
v_cvt_pk_bf16_f32 v146, v36, v37
v_mfma_f32_16x16x32_bf16 v[40:43], v[192:195], a[116:119], v[40:43]
v_cvt_pk_bf16_f32 v147, v38, v39
v_cvt_pk_bf16_f32 v160, v64, v65
v_mfma_f32_16x16x32_bf16 v[72:75], v[192:195], a[120:123], v[72:75]
v_cvt_pk_bf16_f32 v161, v66, v67
v_cvt_pk_bf16_f32 v162, v68, v69
v_mfma_f32_16x16x32_bf16 v[104:107], v[192:195], a[124:127], v[104:107]
ds_read_b64_tr_b16 v[192:193], v241 offset:50816
ds_read_b64_tr_b16 v[194:195], v241 offset:51840
v_mfma_f32_16x16x32_bf16 v[12:15], v[196:199], a[192:195], v[12:15]
v_cvt_pk_bf16_f32 v163, v70, v71
v_cvt_pk_bf16_f32 v176, v96, v97
v_mfma_f32_16x16x32_bf16 v[44:47], v[196:199], a[196:199], v[44:47]
v_cvt_pk_bf16_f32 v177, v98, v99
v_cvt_pk_bf16_f32 v178, v100, v101
v_mfma_f32_16x16x32_bf16 v[76:79], v[196:199], a[200:203], v[76:79]
s_waitcnt lgkmcnt(2)
v_cvt_pk_bf16_f32 v179, v102, v103
v_mfma_f32_16x16x32_bf16 v[108:111], v[196:199], a[204:207], v[108:111]
ds_read_b64_tr_b16 v[196:197], v241 offset:59264
ds_read_b64_tr_b16 v[198:199], v241 offset:60288
v_mfma_f32_16x16x32_bf16 v[12:15], v[200:203], a[96:99], v[12:15]
v_mfma_f32_16x16x32_bf16 v[44:47], v[200:203], a[100:103], v[44:47]
v_mfma_f32_16x16x32_bf16 v[76:79], v[200:203], a[104:107], v[76:79]
v_mfma_f32_16x16x32_bf16 v[108:111], v[200:203], a[108:111], v[108:111]
ds_read_b64_tr_b16 v[200:201], v242 offset:42368
ds_read_b64_tr_b16 v[202:203], v242 offset:43392
v_mfma_f32_16x16x32_bf16 v[12:15], v[204:207], a[112:115], v[12:15]
v_mfma_f32_16x16x32_bf16 v[44:47], v[204:207], a[116:119], v[44:47]
buffer_store_dwordx4 a[48:51], v255, s[48:51], s18 offen offset:3072 sc1
v_mfma_f32_16x16x32_bf16 v[76:79], v[204:207], a[120:123], v[76:79]
v_mfma_f32_16x16x32_bf16 v[108:111], v[204:207], a[124:127], v[108:111]
ds_read_b64_tr_b16 v[204:205], v242 offset:50816
ds_read_b64_tr_b16 v[206:207], v242 offset:51840
v_mfma_f32_16x16x32_bf16 v[16:19], v[208:211], a[192:195], v[16:19]
v_mfma_f32_16x16x32_bf16 v[48:51], v[208:211], a[196:199], v[48:51]
buffer_store_dwordx4 v[128:131], v255, s[52:55], 0 offen offset:0 sc1
v_mfma_f32_16x16x32_bf16 v[80:83], v[208:211], a[200:203], v[80:83]
v_cvt_pk_bf16_f32 v132, v8, v9
v_cvt_pk_bf16_f32 v133, v10, v11
v_mfma_f32_16x16x32_bf16 v[112:115], v[208:211], a[204:207], v[112:115]
ds_read_b64_tr_b16 v[208:209], v242 offset:59264
ds_read_b64_tr_b16 v[210:211], v242 offset:60288
v_mfma_f32_16x16x32_bf16 v[16:19], v[212:215], a[96:99], v[16:19]
v_cvt_pk_bf16_f32 v134, v12, v13
v_cvt_pk_bf16_f32 v135, v14, v15
v_mfma_f32_16x16x32_bf16 v[48:51], v[212:215], a[100:103], v[48:51]
buffer_store_dwordx4 v[144:147], v255, s[52:55], s95 offen offset:0 sc1
v_mfma_f32_16x16x32_bf16 v[80:83], v[212:215], a[104:107], v[80:83]
v_cvt_pk_bf16_f32 v148, v40, v41
v_cvt_pk_bf16_f32 v149, v42, v43
v_mfma_f32_16x16x32_bf16 v[112:115], v[212:215], a[108:111], v[112:115]
ds_read_b64_tr_b16 v[212:213], v243 offset:42368
ds_read_b64_tr_b16 v[214:215], v243 offset:43392
v_mfma_f32_16x16x32_bf16 v[16:19], v[216:219], a[112:115], v[16:19]
v_cvt_pk_bf16_f32 v150, v44, v45
v_cvt_pk_bf16_f32 v151, v46, v47
v_mfma_f32_16x16x32_bf16 v[48:51], v[216:219], a[116:119], v[48:51]
buffer_store_dwordx4 v[160:163], v255, s[52:55], s96 offen offset:0 sc1
v_mfma_f32_16x16x32_bf16 v[80:83], v[216:219], a[120:123], v[80:83]
v_cvt_pk_bf16_f32 v164, v72, v73
v_cvt_pk_bf16_f32 v165, v74, v75
v_mfma_f32_16x16x32_bf16 v[112:115], v[216:219], a[124:127], v[112:115]
ds_read_b64_tr_b16 v[216:217], v243 offset:50816
ds_read_b64_tr_b16 v[218:219], v243 offset:51840
v_mfma_f32_16x16x32_bf16 v[20:23], v[220:223], a[192:195], v[20:23]
v_cvt_pk_bf16_f32 v166, v76, v77
v_cvt_pk_bf16_f32 v167, v78, v79
v_mfma_f32_16x16x32_bf16 v[52:55], v[220:223], a[196:199], v[52:55]
buffer_store_dwordx4 v[176:179], v255, s[52:55], s97 offen offset:0 sc1
v_mfma_f32_16x16x32_bf16 v[84:87], v[220:223], a[200:203], v[84:87]
s_waitcnt lgkmcnt(2)
v_cvt_pk_bf16_f32 v180, v104, v105
v_mfma_f32_16x16x32_bf16 v[116:119], v[220:223], a[204:207], v[116:119]
ds_read_b64_tr_b16 v[220:221], v243 offset:59264
ds_read_b64_tr_b16 v[222:223], v243 offset:60288
v_mfma_f32_16x16x32_bf16 v[20:23], v[192:195], a[96:99], v[20:23]
v_cvt_pk_bf16_f32 v181, v106, v107
v_cvt_pk_bf16_f32 v182, v108, v109
v_mfma_f32_16x16x32_bf16 v[52:55], v[192:195], a[100:103], v[52:55]
v_cvt_pk_bf16_f32 v183, v110, v111
v_accvgpr_read_b32 v32, a236
v_mfma_f32_16x16x32_bf16 v[84:87], v[192:195], a[104:107], v[84:87]
.La5_LW1_11:
s_cbranch_execz .La5_LW1_R0
v_accvgpr_read_b32 v33, a237
v_accvgpr_read_b32 v34, a238
v_mfma_f32_16x16x32_bf16 v[116:119], v[192:195], a[108:111], v[116:119]
v_accvgpr_read_b32 v35, a239
v_mov_b32 v36, 0xff800000
v_mfma_f32_16x16x32_bf16 v[20:23], v[196:199], a[112:115], v[20:23]
s_waitcnt lgkmcnt(0)
s_barrier
v_mfma_f32_16x16x32_bf16 v[52:55], v[196:199], a[116:119], v[52:55]
v_mov_b32 v37, 0xff800000
v_mov_b32 v38, 0xff800000
v_mfma_f32_16x16x32_bf16 v[84:87], v[196:199], a[120:123], v[84:87]
v_mov_b32 v39, 0xff800000
v_mul_u32_u24 v241, 0x5555, v224
v_mfma_f32_16x16x32_bf16 v[116:119], v[196:199], a[124:127], v[116:119]
ds_read_b128 a[64:67], v248 offset:0
ds_read_b128 a[68:71], v248 offset:1024
v_mfma_f32_16x16x32_bf16 v[24:27], v[200:203], a[192:195], v[24:27]
v_lshrrev_b32 v241, 4, v241
v_add_u32 v241, 0x8888, v241
v_mfma_f32_16x16x32_bf16 v[56:59], v[200:203], a[196:199], v[56:59]
buffer_store_dwordx4 v[132:135], v255, s[52:55], 0 offen offset:1024 sc1
v_mfma_f32_16x16x32_bf16 v[88:91], v[200:203], a[200:203], v[88:91]
v_cvt_pk_bf16_f32 v136, v16, v17
v_cvt_pk_bf16_f32 v137, v18, v19
v_mfma_f32_16x16x32_bf16 v[120:123], v[200:203], a[204:207], v[120:123]
ds_read_b128 a[72:75], v248 offset:2048
ds_read_b128 a[76:79], v248 offset:3072
v_mfma_f32_16x16x32_bf16 v[24:27], v[204:207], a[96:99], v[24:27]
v_cvt_pk_bf16_f32 v138, v20, v21
v_cvt_pk_bf16_f32 v139, v22, v23
v_mfma_f32_16x16x32_bf16 v[56:59], v[204:207], a[100:103], v[56:59]
buffer_store_dwordx4 v[148:151], v255, s[52:55], s95 offen offset:1024 sc1
v_mfma_f32_16x16x32_bf16 v[88:91], v[204:207], a[104:107], v[88:91]
v_cvt_pk_bf16_f32 v152, v48, v49
v_cvt_pk_bf16_f32 v153, v50, v51
v_mfma_f32_16x16x32_bf16 v[120:123], v[204:207], a[108:111], v[120:123]
ds_read_b128 a[80:83], v248 offset:4096
ds_read_b128 a[84:87], v248 offset:5120
v_mfma_f32_16x16x32_bf16 v[24:27], v[208:211], a[112:115], v[24:27]
v_cvt_pk_bf16_f32 v154, v52, v53
v_cvt_pk_bf16_f32 v155, v54, v55
v_mfma_f32_16x16x32_bf16 v[56:59], v[208:211], a[116:119], v[56:59]
buffer_store_dwordx4 v[164:167], v255, s[52:55], s96 offen offset:1024 sc1
v_mfma_f32_16x16x32_bf16 v[88:91], v[208:211], a[120:123], v[88:91]
v_cvt_pk_bf16_f32 v168, v80, v81
v_cvt_pk_bf16_f32 v169, v82, v83
v_mfma_f32_16x16x32_bf16 v[120:123], v[208:211], a[124:127], v[120:123]
ds_read_b128 a[88:91], v248 offset:6144
ds_read_b128 a[92:95], v248 offset:7168
v_mfma_f32_16x16x32_bf16 v[28:31], v[212:215], a[192:195], v[28:31]
v_cvt_pk_bf16_f32 v170, v84, v85
v_cvt_pk_bf16_f32 v171, v86, v87
v_mfma_f32_16x16x32_bf16 v[60:63], v[212:215], a[196:199], v[60:63]
buffer_store_dwordx4 v[180:183], v255, s[52:55], s97 offen offset:1024 sc1
v_mfma_f32_16x16x32_bf16 v[92:95], v[212:215], a[200:203], v[92:95]
v_cvt_pk_bf16_f32 v184, v112, v113
v_cvt_pk_bf16_f32 v185, v114, v115
v_mfma_f32_16x16x32_bf16 v[124:127], v[212:215], a[204:207], v[124:127]
v_cvt_pk_bf16_f32 v186, v116, v117
v_cvt_pk_bf16_f32 v187, v118, v119
v_mfma_f32_16x16x32_bf16 v[28:31], v[216:219], a[96:99], v[28:31]
ds_write_b128 v238, a[48:51] offset:39936
v_mfma_f32_16x16x32_bf16 v[60:63], v[216:219], a[100:103], v[60:63]
v_mfma_f32_16x16x32_bf16 v[92:95], v[216:219], a[104:107], v[92:95]
v_mfma_f32_16x16x32_bf16 v[124:127], v[216:219], a[108:111], v[124:127]
v_mfma_f32_16x16x32_bf16 v[28:31], v[220:223], a[112:115], v[28:31]
v_mfma_f32_16x16x32_bf16 v[60:63], v[220:223], a[116:119], v[60:63]
v_mfma_f32_16x16x32_bf16 v[92:95], v[220:223], a[120:123], v[92:95]
v_mfma_f32_16x16x32_bf16 v[124:127], v[220:223], a[124:127], v[124:127]
s_waitcnt lgkmcnt(1)
v_mfma_f32_16x16x32_bf16 v[208:211], a[64:67], v[160:163], v[32:35]
v_mfma_f32_16x16x32_bf16 v[216:219], a[64:67], v[176:179], 0
buffer_store_dwordx4 v[136:139], v255, s[52:55], 0 offen offset:2048 sc1
v_mfma_f32_16x16x32_bf16 v[212:215], a[80:83], v[160:163], v[36:39]
v_cvt_pk_bf16_f32 v172, v88, v89
v_cvt_pk_bf16_f32 v173, v90, v91
v_cvt_pk_bf16_f32 v174, v92, v93
v_mfma_f32_16x16x32_bf16 v[220:223], a[80:83], v[176:179], v[32:35]
v_cvt_pk_bf16_f32 v175, v94, v95
v_cvt_pk_bf16_f32 v188, v120, v121
v_cvt_pk_bf16_f32 v189, v122, v123
v_mfma_f32_16x16x32_bf16 v[208:211], a[68:71], v[164:167], v[208:211]
v_cvt_pk_bf16_f32 v190, v124, v125
v_cvt_pk_bf16_f32 v191, v126, v127
v_cvt_pk_bf16_f32 v140, v24, v25
v_mfma_f32_16x16x32_bf16 v[216:219], a[68:71], v[180:183], v[216:219]
buffer_store_dwordx4 v[152:155], v255, s[52:55], s95 offen offset:2048 sc1
v_cvt_pk_bf16_f32 v141, v26, v27
v_mfma_f32_16x16x32_bf16 v[212:215], a[84:87], v[164:167], v[212:215]
v_cvt_pk_bf16_f32 v142, v28, v29
v_cvt_pk_bf16_f32 v143, v30, v31
v_cvt_pk_bf16_f32 v156, v56, v57
v_mfma_f32_16x16x32_bf16 v[220:223], a[84:87], v[180:183], v[220:223]
v_cvt_pk_bf16_f32 v157, v58, v59
v_cvt_pk_bf16_f32 v158, v60, v61
v_cvt_pk_bf16_f32 v159, v62, v63
v_mfma_f32_16x16x32_bf16 v[208:211], a[72:75], v[168:171], v[208:211]
v_mfma_f32_16x16x32_bf16 v[216:219], a[72:75], v[184:187], v[216:219]
buffer_store_dwordx4 v[168:171], v255, s[52:55], s96 offen offset:2048 sc1
v_mfma_f32_16x16x32_bf16 v[212:215], a[88:91], v[168:171], v[212:215]
v_mfma_f32_16x16x32_bf16 v[220:223], a[88:91], v[184:187], v[220:223]
v_mfma_f32_16x16x32_bf16 v[208:211], a[76:79], v[172:175], v[208:211]
v_mfma_f32_16x16x32_bf16 v[216:219], a[76:79], v[188:191], v[216:219]
buffer_store_dwordx4 v[184:187], v255, s[52:55], s97 offen offset:2048 sc1
v_mfma_f32_16x16x32_bf16 v[212:215], a[92:95], v[172:175], v[212:215]
ds_read_b128 a[64:67], v248 offset:16384
v_mfma_f32_16x16x32_bf16 v[220:223], a[92:95], v[188:191], v[220:223]
ds_read_b128 a[96:99], v248 offset:8192
v_exp_f32 v0, v208
s_waitcnt vmcnt(12)
buffer_store_dwordx2 v[252:253], off, s[64:67], s87 sc1
v_mfma_f32_16x16x32_bf16 v[64:67], a[232:235], a[232:235], 0
ds_read_b128 a[68:71], v248 offset:17408
v_exp_f32 v1, v209
v_mfma_f32_16x16x32_bf16 v[68:71], a[232:235], a[232:235], 0
ds_read_b128 a[100:103], v248 offset:9216
buffer_store_dwordx4 v[140:143], v255, s[52:55], 0 offen offset:3072 sc1
v_mfma_f32_16x16x32_bf16 v[72:75], a[232:235], a[232:235], 0
ds_read_b128 a[72:75], v248 offset:18432
v_exp_f32 v2, v210
v_mfma_f32_16x16x32_bf16 v[76:79], a[232:235], a[232:235], 0
ds_read_b128 a[104:107], v248 offset:10240
v_exp_f32 v3, v211
v_mfma_f32_16x16x32_bf16 v[80:83], a[232:235], a[232:235], 0
ds_read_b128 a[76:79], v248 offset:19456
v_exp_f32 v8, v216
v_mfma_f32_16x16x32_bf16 v[84:87], a[232:235], a[232:235], 0
ds_read_b128 a[108:111], v248 offset:11264
buffer_store_dwordx4 v[156:159], v255, s[52:55], s95 offen offset:3072 sc1
v_mfma_f32_16x16x32_bf16 v[88:91], a[232:235], a[232:235], 0
ds_read_b128 a[80:83], v248 offset:20480
v_exp_f32 v9, v217
v_mfma_f32_16x16x32_bf16 v[92:95], a[232:235], a[232:235], 0
ds_read_b128 a[112:115], v248 offset:12288
v_exp_f32 v10, v218
v_mfma_f32_16x16x32_bf16 v[96:99], a[232:235], a[232:235], 0
ds_read_b128 a[84:87], v248 offset:21504
v_exp_f32 v11, v219
v_mfma_f32_16x16x32_bf16 v[100:103], a[232:235], a[232:235], 0
ds_read_b128 a[116:119], v248 offset:13312
buffer_store_dwordx4 v[172:175], v255, s[52:55], s96 offen offset:3072 sc1
v_mfma_f32_16x16x32_bf16 v[104:107], a[232:235], a[232:235], 0
ds_read_b128 a[88:91], v248 offset:22528
v_exp_f32 v4, v212
v_mfma_f32_16x16x32_bf16 v[108:111], a[232:235], a[232:235], 0
ds_read_b128 a[120:123], v248 offset:14336
v_exp_f32 v5, v213
v_mfma_f32_16x16x32_bf16 v[112:115], a[232:235], a[232:235], 0
ds_read_b128 a[92:95], v248 offset:23552
v_exp_f32 v6, v214
v_mfma_f32_16x16x32_bf16 v[116:119], a[232:235], a[232:235], 0
ds_read_b128 a[124:127], v248 offset:15360
buffer_store_dwordx4 v[188:191], v255, s[52:55], s97 offen offset:3072 sc1
v_exp_f32 v7, v215
v_mfma_f32_16x16x32_bf16 v[120:123], a[232:235], a[232:235], 0
v_exp_f32_e64 v12, v220
v_mfma_f32_16x16x32_bf16 v[124:127], a[232:235], a[232:235], 0
v_accvgpr_write_b32 a192, 0
v_accvgpr_write_b32 a193, 0
v_accvgpr_write_b32 a194, 0
v_accvgpr_write_b32 a195, 0
v_accvgpr_write_b32 a196, 0
v_accvgpr_write_b32 a197, 0
v_accvgpr_write_b32 a198, 0
v_accvgpr_write_b32 a199, 0
v_add_u32 v248, 0x4000, v248
s_cmp_eq_u32 s3, 3
s_cbranch_scc1 .La5_LA_FH
.p2align 6
; ---- attention, 32 keys per window: S = K Q^T (causal mask through SrcC), P = exp2(S), O += V^T P, row sums l += 1 P; here the later 32 rows only
.La5_LA_HH:
s_waitcnt lgkmcnt(1)
v_mfma_f32_16x16x32_bf16 v[208:211], a[64:67], v[160:163], 0
v_exp_f32 v13, v221
v_mfma_f32_16x16x32_bf16 v[208:211], a[68:71], v[164:167], v[208:211]
v_cvt_pk_bf16_f32 v232, v0, v1
v_cvt_pk_bf16_f32 v233, v2, v3
v_mfma_f32_16x16x32_bf16 v[208:211], a[72:75], v[168:171], v[208:211]
v_exp_f32 v14, v222
v_mfma_f32_16x16x32_bf16 v[208:211], a[76:79], v[172:175], v[208:211]
v_cvt_pk_bf16_f32 v236, v8, v9
v_cvt_pk_bf16_f32 v237, v10, v11
v_mfma_f32_16x16x32_bf16 v[216:219], a[64:67], v[176:179], 0
v_exp_f32 v15, v223
v_mfma_f32_16x16x32_bf16 v[216:219], a[68:71], v[180:183], v[216:219]
v_cvt_pk_bf16_f32 v234, v4, v5
v_cvt_pk_bf16_f32 v235, v6, v7
v_mfma_f32_16x16x32_bf16 v[216:219], a[72:75], v[184:187], v[216:219]
v_exp_f32 v0, v208
v_mfma_f32_16x16x32_bf16 v[216:219], a[76:79], v[188:191], v[216:219]
v_cvt_pk_bf16_f32 v238, v12, v13
v_cvt_pk_bf16_f32 v239, v14, v15
v_mfma_f32_16x16x32_bf16 v[212:215], a[80:83], v[160:163], 0
v_exp_f32 v1, v209
v_mfma_f32_16x16x32_bf16 v[212:215], a[84:87], v[164:167], v[212:215]
ds_read_b128 a[64:67], v248 offset:16384
ds_read_b128 a[68:71], v248 offset:17408
v_mfma_f32_16x16x32_bf16 v[212:215], a[88:91], v[168:171], v[212:215]
v_exp_f32 v2, v210
v_mfma_f32_16x16x32_bf16 v[212:215], a[92:95], v[172:175], v[212:215]
ds_read_b128 a[72:75], v248 offset:18432
ds_read_b128 a[76:79], v248 offset:19456
v_mfma_f32_16x16x32_bf16 v[220:223], a[80:83], v[176:179], 0
v_exp_f32 v3, v211
v_mfma_f32_16x16x32_bf16 v[220:223], a[84:87], v[180:183], v[220:223]
ds_read_b128 a[80:83], v248 offset:20480
v_mfma_f32_16x16x32_bf16 v[220:223], a[88:91], v[184:187], v[220:223]
v_exp_f32 v8, v216
v_mfma_f32_16x16x32_bf16 v[220:223], a[92:95], v[188:191], v[220:223]
s_waitcnt lgkmcnt(5)
ds_read_b128 a[84:87], v248 offset:21504
v_mfma_f32_16x16x32_bf16 v[64:67], a[96:99], v[232:235], v[64:67]
v_exp_f32 v9, v217
v_mfma_f32_16x16x32_bf16 v[96:99], a[96:99], v[236:239], v[96:99]
ds_read_b128 a[96:99], v248 offset:8192
ds_read_b128 a[88:91], v248 offset:22528
v_mfma_f32_16x16x32_bf16 v[68:71], a[100:103], v[232:235], v[68:71]
v_exp_f32 v10, v218
v_mfma_f32_16x16x32_bf16 v[100:103], a[100:103], v[236:239], v[100:103]
ds_read_b128 a[100:103], v248 offset:9216
ds_read_b128 a[92:95], v248 offset:23552
v_mfma_f32_16x16x32_bf16 v[72:75], a[104:107], v[232:235], v[72:75]
v_exp_f32 v11, v219
v_mfma_f32_16x16x32_bf16 v[104:107], a[104:107], v[236:239], v[104:107]
ds_read_b128 a[104:107], v248 offset:10240
v_mfma_f32_16x16x32_bf16 v[76:79], a[108:111], v[232:235], v[76:79]
v_exp_f32 v4, v212
v_mfma_f32_16x16x32_bf16 v[108:111], a[108:111], v[236:239], v[108:111]
ds_read_b128 a[108:111], v248 offset:11264
s_add_u32 s85, s85, 1
v_mfma_f32_16x16x32_bf16 v[80:83], a[112:115], v[232:235], v[80:83]
v_exp_f32 v5, v213
v_mfma_f32_16x16x32_bf16 v[112:115], a[112:115], v[236:239], v[112:115]
ds_read_b128 a[112:115], v248 offset:12288
s_add_u32 s89, s89, 32768
v_mfma_f32_16x16x32_bf16 v[84:87], a[116:119], v[232:235], v[84:87]
v_exp_f32 v6, v214
v_mfma_f32_16x16x32_bf16 v[116:119], a[116:119], v[236:239], v[116:119]
ds_read_b128 a[116:119], v248 offset:13312
s_add_u32 s92, s85, s3
v_mfma_f32_16x16x32_bf16 v[88:91], a[120:123], v[232:235], v[88:91]
v_exp_f32 v7, v215
v_mfma_f32_16x16x32_bf16 v[120:123], a[120:123], v[236:239], v[120:123]
ds_read_b128 a[120:123], v248 offset:14336
buffer_load_dword v250, v251, s[64:67], s20 offen sc1
v_mfma_f32_16x16x32_bf16 v[92:95], a[124:127], v[232:235], v[92:95]
v_exp_f32 v12, v220
v_mfma_f32_16x16x32_bf16 v[124:127], a[124:127], v[236:239], v[124:127]
ds_read_b128 a[124:127], v248 offset:15360
s_barrier
v_smfmac_f32_16x16x64_bf16 a[196:199], a[208:211], v[232:239], v241
s_waitcnt lgkmcnt(6)
v_mfma_f32_16x16x32_bf16 v[208:211], a[64:67], v[160:163], 0
v_exp_f32 v13, v221
v_mfma_f32_16x16x32_bf16 v[208:211], a[68:71], v[164:167], v[208:211]
v_cvt_pk_bf16_f32 v232, v0, v1
v_cvt_pk_bf16_f32 v233, v2, v3
v_mfma_f32_16x16x32_bf16 v[208:211], a[72:75], v[168:171], v[208:211]
v_exp_f32 v14, v222
v_mfma_f32_16x16x32_bf16 v[208:211], a[76:79], v[172:175], v[208:211]
v_cvt_pk_bf16_f32 v236, v8, v9
v_cvt_pk_bf16_f32 v237, v10, v11
v_mfma_f32_16x16x32_bf16 v[216:219], a[64:67], v[176:179], 0
v_exp_f32 v15, v223
v_mfma_f32_16x16x32_bf16 v[216:219], a[68:71], v[180:183], v[216:219]
v_cvt_pk_bf16_f32 v234, v4, v5
v_cvt_pk_bf16_f32 v235, v6, v7
v_mfma_f32_16x16x32_bf16 v[216:219], a[72:75], v[184:187], v[216:219]
v_exp_f32 v0, v208
v_mfma_f32_16x16x32_bf16 v[216:219], a[76:79], v[188:191], v[216:219]
v_cvt_pk_bf16_f32 v238, v12, v13
v_cvt_pk_bf16_f32 v239, v14, v15
v_mfma_f32_16x16x32_bf16 v[212:215], a[80:83], v[160:163], 0
v_exp_f32 v1, v209
v_mfma_f32_16x16x32_bf16 v[212:215], a[84:87], v[164:167], v[212:215]
ds_read_b128 a[64:67], v248 offset:32768
ds_read_b128 a[68:71], v248 offset:33792
v_mfma_f32_16x16x32_bf16 v[212:215], a[88:91], v[168:171], v[212:215]
v_exp_f32 v2, v210
v_mfma_f32_16x16x32_bf16 v[212:215], a[92:95], v[172:175], v[212:215]
ds_read_b128 a[72:75], v248 offset:34816
ds_read_b128 a[76:79], v248 offset:35840
v_mfma_f32_16x16x32_bf16 v[220:223], a[80:83], v[176:179], 0
v_exp_f32 v3, v211
v_mfma_f32_16x16x32_bf16 v[220:223], a[84:87], v[180:183], v[220:223]
ds_read_b128 a[80:83], v248 offset:36864
s_bitcmp1_b64 s[98:99], s85
v_mfma_f32_16x16x32_bf16 v[220:223], a[88:91], v[184:187], v[220:223]
v_exp_f32 v8, v216
v_mfma_f32_16x16x32_bf16 v[220:223], a[92:95], v[188:191], v[220:223]
s_waitcnt lgkmcnt(5)
ds_read_b128 a[84:87], v248 offset:37888
v_mfma_f32_16x16x32_bf16 v[64:67], a[96:99], v[232:235], v[64:67]
v_exp_f32 v9, v217
v_mfma_f32_16x16x32_bf16 v[96:99], a[96:99], v[236:239], v[96:99]
ds_read_b128 a[96:99], v248 offset:24576
ds_read_b128 a[88:91], v248 offset:38912
v_mfma_f32_16x16x32_bf16 v[68:71], a[100:103], v[232:235], v[68:71]
v_exp_f32 v10, v218
v_mfma_f32_16x16x32_bf16 v[100:103], a[100:103], v[236:239], v[100:103]
ds_read_b128 a[100:103], v248 offset:25600
ds_read_b128 a[92:95], v248 offset:39936
v_mfma_f32_16x16x32_bf16 v[72:75], a[104:107], v[232:235], v[72:75]
v_exp_f32 v11, v219
v_mfma_f32_16x16x32_bf16 v[104:107], a[104:107], v[236:239], v[104:107]
ds_read_b128 a[104:107], v248 offset:26624
v_mfma_f32_16x16x32_bf16 v[76:79], a[108:111], v[232:235], v[76:79]
v_exp_f32 v4, v212
v_mfma_f32_16x16x32_bf16 v[108:111], a[108:111], v[236:239], v[108:111]
ds_read_b128 a[108:111], v248 offset:27648
v_mfma_f32_16x16x32_bf16 v[80:83], a[112:115], v[232:235], v[80:83]
v_exp_f32 v5, v213
v_mfma_f32_16x16x32_bf16 v[112:115], a[112:115], v[236:239], v[112:115]
ds_read_b128 a[112:115], v248 offset:28672
v_mfma_f32_16x16x32_bf16 v[84:87], a[116:119], v[232:235], v[84:87]
v_exp_f32 v6, v214
v_mfma_f32_16x16x32_bf16 v[116:119], a[116:119], v[236:239], v[116:119]
ds_read_b128 a[116:119], v248 offset:29696
s_waitcnt vmcnt(1)
buffer_store_dwordx2 v[252:253], off, s[64:67], s86 sc1
s_mov_b32 s86, 1073741824
s_bitset0_b32 s20, 30
v_mfma_f32_16x16x32_bf16 v[88:91], a[120:123], v[232:235], v[88:91]
v_exp_f32 v7, v215
v_mfma_f32_16x16x32_bf16 v[120:123], a[120:123], v[236:239], v[120:123]
ds_read_b128 a[120:123], v248 offset:30720
v_mfma_f32_16x16x32_bf16 v[92:95], a[124:127], v[232:235], v[92:95]
v_exp_f32_e64 v12, v220
v_mfma_f32_16x16x32_bf16 v[124:127], a[124:127], v[236:239], v[124:127]
ds_read_b128 a[124:127], v248 offset:31744
v_smfmac_f32_16x16x64_bf16 a[196:199], a[208:211], v[232:239], v241
v_add_u32 v248, 0x8000, v248
s_cbranch_scc0 .La5_LA_HH1
s_call_b64 s[24:25], .La5_LA_EVENT
s_add_u32 s92, s85, s3
.La5_LA_HH1:
s_cmp_lt_u32 s92, 3
s_cbranch_scc1 .La5_LA_HH
; ---- first window with both row groups (diagonal of the earlier group)
.La5_LA_FH:
.La5_LW2_0:
s_cbranch_execz .La5_LW2_4
s_waitcnt lgkmcnt(9)
v_mfma_f32_16x16x32_bf16 v[192:195], a[64:67], v[128:131], v[32:35]
v_exp_f32 v13, v221
v_mfma_f32_16x16x32_bf16 v[192:195], a[68:71], v[132:135], v[192:195]
v_cvt_pk_bf16_f32 v232, v0, v1
v_cvt_pk_bf16_f32 v233, v2, v3
v_mfma_f32_16x16x32_bf16 v[192:195], a[72:75], v[136:139], v[192:195]
v_exp_f32 v14, v222
v_mfma_f32_16x16x32_bf16 v[192:195], a[76:79], v[140:143], v[192:195]
buffer_load_dwordx4 v255, s[60:63], s17 offen offset:0 sc1 lds
v_mfma_f32_16x16x32_bf16 v[200:203], a[64:67], v[144:147], 0
v_exp_f32 v15, v223
v_mfma_f32_16x16x32_bf16 v[200:203], a[68:71], v[148:151], v[200:203]
v_cvt_pk_bf16_f32 v236, v8, v9
v_cvt_pk_bf16_f32 v237, v10, v11
v_mfma_f32_16x16x32_bf16 v[200:203], a[72:75], v[152:155], v[200:203]
v_exp_f32 v192, v192
v_mfma_f32_16x16x32_bf16 v[200:203], a[76:79], v[156:159], v[200:203]
s_waitcnt lgkmcnt(1)
v_cvt_pk_bf16_f32 v234, v4, v5
v_mfma_f32_16x16x32_bf16 v[196:199], a[80:83], v[128:131], v[36:39]
v_exp_f32 v193, v193
v_mfma_f32_16x16x32_bf16 v[196:199], a[84:87], v[132:135], v[196:199]
v_cvt_pk_bf16_f32 v224, v192, v193
s_add_u32 s85, s85, 1
v_mfma_f32_16x16x32_bf16 v[196:199], a[88:91], v[136:139], v[196:199]
v_exp_f32 v194, v194
v_mfma_f32_16x16x32_bf16 v[196:199], a[92:95], v[140:143], v[196:199]
buffer_load_dwordx4 v255, s[60:63], s17 offen offset:1024 sc1 lds
v_mfma_f32_16x16x32_bf16 v[204:207], a[80:83], v[144:147], v[32:35]
v_exp_f32 v195, v195
v_mfma_f32_16x16x32_bf16 v[204:207], a[84:87], v[148:151], v[204:207]
v_cvt_pk_bf16_f32 v225, v194, v195
s_add_u32 s89, s89, 16384
v_mfma_f32_16x16x32_bf16 v[204:207], a[88:91], v[152:155], v[204:207]
v_exp_f32 v200, v200
v_mfma_f32_16x16x32_bf16 v[204:207], a[92:95], v[156:159], v[204:207]
buffer_load_dwordx4 v255, s[60:63], s17 offen offset:2048 sc1 lds
v_mfma_f32_16x16x32_bf16 v[208:211], a[64:67], v[160:163], 0
v_exp_f32 v201, v201
v_mfma_f32_16x16x32_bf16 v[208:211], a[68:71], v[164:167], v[208:211]
v_cvt_pk_bf16_f32 v228, v200, v201
s_and_b32 s89, s89, 0x1ffff
v_mfma_f32_16x16x32_bf16 v[208:211], a[72:75], v[168:171], v[208:211]
v_exp_f32 v202, v202
v_mfma_f32_16x16x32_bf16 v[208:211], a[76:79], v[172:175], v[208:211]
buffer_load_dwordx4 v255, s[60:63], s17 offen offset:3072 sc1 lds
v_mfma_f32_16x16x32_bf16 v[216:219], a[64:67], v[176:179], 0
v_exp_f32 v203, v203
v_mfma_f32_16x16x32_bf16 v[216:219], a[68:71], v[180:183], v[216:219]
v_cvt_pk_bf16_f32 v229, v202, v203
v_mfma_f32_16x16x32_bf16 v[216:219], a[72:75], v[184:187], v[216:219]
v_exp_f32 v196, v196
v_mfma_f32_16x16x32_bf16 v[216:219], a[76:79], v[188:191], v[216:219]
v_cvt_pk_bf16_f32 v235, v6, v7
v_cvt_pk_bf16_f32 v238, v12, v13
v_mfma_f32_16x16x32_bf16 v[212:215], a[80:83], v[160:163], 0
v_exp_f32 v197, v197
s_add_u32 s17, s17, 8192
v_mfma_f32_16x16x32_bf16 v[212:215], a[84:87], v[164:167], v[212:215]
v_cvt_pk_bf16_f32 v226, v196, v197
s_add_u32 m0, m0, 16384
s_and_b32 m0, m0, s78
v_mfma_f32_16x16x32_bf16 v[212:215], a[88:91], v[168:171], v[212:215]
v_exp_f32 v198, v198
v_mfma_f32_16x16x32_bf16 v[212:215], a[92:95], v[172:175], v[212:215]
v_cvt_pk_bf16_f32 v239, v14, v15
s_waitcnt lgkmcnt(0)
v_mfma_f32_16x16x32_bf16 v[220:223], a[80:83], v[176:179], 0
v_exp_f32 v199, v199
v_mfma_f32_16x16x32_bf16 v[220:223], a[84:87], v[180:183], v[220:223]
v_cvt_pk_bf16_f32 v227, v198, v199
v_mfma_f32_16x16x32_bf16 v[220:223], a[88:91], v[184:187], v[220:223]
v_exp_f32 v204, v204
v_mfma_f32_16x16x32_bf16 v[220:223], a[92:95], v[188:191], v[220:223]
s_waitcnt vmcnt(11)
s_barrier
v_mfma_f32_16x16x32_bf16 v[64:67], a[96:99], v[232:235], v[64:67]
v_exp_f32 v205, v205
v_mfma_f32_16x16x32_bf16 v[96:99], a[96:99], v[236:239], v[96:99]
ds_read_b128 a[96:99], v248 offset:8192
v_cvt_pk_bf16_f32 v230, v204, v205
v_mfma_f32_16x16x32_bf16 v[68:71], a[100:103], v[232:235], v[68:71]
v_exp_f32 v206, v206
v_mfma_f32_16x16x32_bf16 v[100:103], a[100:103], v[236:239], v[100:103]
ds_read_b128 a[100:103], v248 offset:9216
buffer_load_dword v250, v251, s[64:67], s20 offen sc1
v_mfma_f32_16x16x32_bf16 v[72:75], a[104:107], v[232:235], v[72:75]
v_exp_f32 v207, v207
v_mfma_f32_16x16x32_bf16 v[104:107], a[104:107], v[236:239], v[104:107]
ds_read_b128 a[104:107], v248 offset:10240
v_cvt_pk_bf16_f32 v231, v206, v207
v_mfma_f32_16x16x32_bf16 v[76:79], a[108:111], v[232:235], v[76:79]
v_exp_f32 v208, v208
v_mfma_f32_16x16x32_bf16 v[108:111], a[108:111], v[236:239], v[108:111]
ds_read_b128 a[108:111], v248 offset:11264
v_mfma_f32_16x16x32_bf16 v[80:83], a[112:115], v[232:235], v[80:83]
v_exp_f32 v209, v209
v_mfma_f32_16x16x32_bf16 v[112:115], a[112:115], v[236:239], v[112:115]
ds_read_b128 a[112:115], v248 offset:12288
v_mfma_f32_16x16x32_bf16 v[84:87], a[116:119], v[232:235], v[84:87]
v_exp_f32 v210, v210
v_mfma_f32_16x16x32_bf16 v[116:119], a[116:119], v[236:239], v[116:119]
ds_read_b128 a[116:119], v248 offset:13312
v_mfma_f32_16x16x32_bf16 v[88:91], a[120:123], v[232:235], v[88:91]
v_exp_f32 v211, v211
v_mfma_f32_16x16x32_bf16 v[120:123], a[120:123], v[236:239], v[120:123]
ds_read_b128 a[120:123], v248 offset:14336
s_bitcmp1_b64 s[98:99], s85
v_mfma_f32_16x16x32_bf16 v[92:95], a[124:127], v[232:235], v[92:95]
v_exp_f32 v216, v216
v_mfma_f32_16x16x32_bf16 v[124:127], a[124:127], v[236:239], v[124:127]
ds_read_b128 a[124:127], v248 offset:15360
v_or_b32 v248, s89, v255
v_smfmac_f32_16x16x64_bf16 a[196:199], a[208:211], v[232:239], v241
s_waitcnt lgkmcnt(4)
v_smfmac_f32_16x16x64_bf16 a[192:195], a[208:211], v[224:231], v241
ds_read_b128 a[64:67], v248 offset:0
v_mfma_f32_16x16x32_bf16 v[0:3], a[96:99], v[224:227], 0
v_exp_f32 v217, v217
v_mfma_f32_16x16x32_bf16 v[32:35], a[96:99], v[228:231], 0
ds_read_b128 a[68:71], v248 offset:1024
v_cvt_pk_bf16_f32 v232, v208, v209
v_mfma_f32_16x16x32_bf16 v[4:7], a[100:103], v[224:227], 0
v_exp_f32 v218, v218
v_mfma_f32_16x16x32_bf16 v[36:39], a[100:103], v[228:231], 0
ds_read_b128 a[72:75], v248 offset:2048
v_cvt_pk_bf16_f32 v233, v210, v211
v_mfma_f32_16x16x32_bf16 v[8:11], a[104:107], v[224:227], 0
v_exp_f32 v219, v219
v_mfma_f32_16x16x32_bf16 v[40:43], a[104:107], v[228:231], 0
ds_read_b128 a[76:79], v248 offset:3072
v_cvt_pk_bf16_f32 v236, v216, v217
v_mfma_f32_16x16x32_bf16 v[12:15], a[108:111], v[224:227], 0
v_exp_f32 v212, v212
v_mfma_f32_16x16x32_bf16 v[44:47], a[108:111], v[228:231], 0
ds_read_b128 a[80:83], v248 offset:4096
s_waitcnt lgkmcnt(5)
v_mfma_f32_16x16x32_bf16 v[16:19], a[112:115], v[224:227], 0
v_exp_f32 v213, v213
v_mfma_f32_16x16x32_bf16 v[48:51], a[112:115], v[228:231], 0
ds_read_b128 a[84:87], v248 offset:5120
v_cvt_pk_bf16_f32 v237, v218, v219
v_mfma_f32_16x16x32_bf16 v[20:23], a[116:119], v[224:227], 0
v_exp_f32 v214, v214
v_mfma_f32_16x16x32_bf16 v[52:55], a[116:119], v[228:231], 0
ds_read_b128 a[88:91], v248 offset:6144
v_cvt_pk_bf16_f32 v234, v212, v213
v_mfma_f32_16x16x32_bf16 v[24:27], a[120:123], v[224:227], 0
v_exp_f32 v215, v215
v_mfma_f32_16x16x32_bf16 v[56:59], a[120:123], v[228:231], 0
ds_read_b128 a[92:95], v248 offset:7168
v_cvt_pk_bf16_f32 v235, v214, v215
v_mfma_f32_16x16x32_bf16 v[28:31], a[124:127], v[224:227], 0
v_exp_f32 v220, v220
v_mfma_f32_16x16x32_bf16 v[60:63], a[124:127], v[228:231], 0
s_waitcnt vmcnt(5)
buffer_store_dwordx2 v[252:253], off, s[64:67], s86 sc1
s_mov_b32 s86, 1073741824
s_bitset0_b32 s20, 30
s_cbranch_scc1 .La5_LA_FE0
.La5_LA_FS0:
s_waitcnt lgkmcnt(4)
v_mfma_f32_16x16x32_bf16 v[192:195], a[64:67], v[128:131], 0
v_exp_f32 v221, v221
v_mfma_f32_16x16x32_bf16 v[192:195], a[68:71], v[132:135], v[192:195]
v_cvt_pk_bf16_f32 v238, v220, v221
s_add_u32 s85, s85, 1
v_mfma_f32_16x16x32_bf16 v[192:195], a[72:75], v[136:139], v[192:195]
v_exp_f32 v222, v222
v_mfma_f32_16x16x32_bf16 v[192:195], a[76:79], v[140:143], v[192:195]
buffer_load_dwordx4 v255, s[60:63], s17 offen offset:0 sc1 lds
v_mfma_f32_16x16x32_bf16 v[200:203], a[64:67], v[144:147], 0
v_exp_f32 v223, v223
v_mfma_f32_16x16x32_bf16 v[200:203], a[68:71], v[148:151], v[200:203]
v_cvt_pk_bf16_f32 v239, v222, v223
s_add_u32 s89, s89, 16384
v_mfma_f32_16x16x32_bf16 v[200:203], a[72:75], v[152:155], v[200:203]
v_exp_f32 v192, v192
v_mfma_f32_16x16x32_bf16 v[200:203], a[76:79], v[156:159], v[200:203]
s_waitcnt lgkmcnt(0)
s_and_b32 s89, s89, 0x1ffff
v_mfma_f32_16x16x32_bf16 v[196:199], a[80:83], v[128:131], 0
v_exp_f32 v193, v193
v_mfma_f32_16x16x32_bf16 v[196:199], a[84:87], v[132:135], v[196:199]
v_cvt_pk_bf16_f32 v224, v192, v193
v_mfma_f32_16x16x32_bf16 v[196:199], a[88:91], v[136:139], v[196:199]
v_exp_f32 v194, v194
v_mfma_f32_16x16x32_bf16 v[196:199], a[92:95], v[140:143], v[196:199]
buffer_load_dwordx4 v255, s[60:63], s17 offen offset:1024 sc1 lds
v_mfma_f32_16x16x32_bf16 v[204:207], a[80:83], v[144:147], 0
v_exp_f32 v195, v195
v_mfma_f32_16x16x32_bf16 v[204:207], a[84:87], v[148:151], v[204:207]
v_cvt_pk_bf16_f32 v225, v194, v195
v_mfma_f32_16x16x32_bf16 v[204:207], a[88:91], v[152:155], v[204:207]
v_exp_f32 v200, v200
v_mfma_f32_16x16x32_bf16 v[204:207], a[92:95], v[156:159], v[204:207]
buffer_load_dwordx4 v255, s[60:63], s17 offen offset:2048 sc1 lds
v_mfma_f32_16x16x32_bf16 v[208:211], a[64:67], v[160:163], 0
v_exp_f32 v201, v201
v_mfma_f32_16x16x32_bf16 v[208:211], a[68:71], v[164:167], v[208:211]
v_cvt_pk_bf16_f32 v228, v200, v201
v_mfma_f32_16x16x32_bf16 v[208:211], a[72:75], v[168:171], v[208:211]
v_exp_f32 v202, v202
v_mfma_f32_16x16x32_bf16 v[208:211], a[76:79], v[172:175], v[208:211]
buffer_load_dwordx4 v255, s[60:63], s17 offen offset:3072 sc1 lds
v_mfma_f32_16x16x32_bf16 v[216:219], a[64:67], v[176:179], 0
v_exp_f32 v203, v203
v_mfma_f32_16x16x32_bf16 v[216:219], a[68:71], v[180:183], v[216:219]
v_cvt_pk_bf16_f32 v229, v202, v203
v_mfma_f32_16x16x32_bf16 v[216:219], a[72:75], v[184:187], v[216:219]
v_exp_f32 v196, v196
v_mfma_f32_16x16x32_bf16 v[216:219], a[76:79], v[188:191], v[216:219]
buffer_load_dword a253, v254, s[36:39], s90 offen
v_mfma_f32_16x16x32_bf16 v[212:215], a[80:83], v[160:163], 0
v_exp_f32 v197, v197
v_mfma_f32_16x16x32_bf16 v[212:215], a[84:87], v[164:167], v[212:215]
v_cvt_pk_bf16_f32 v226, v196, v197
s_add_u32 s17, s17, 8192
v_mfma_f32_16x16x32_bf16 v[212:215], a[88:91], v[168:171], v[212:215]
v_exp_f32 v198, v198
v_mfma_f32_16x16x32_bf16 v[212:215], a[92:95], v[172:175], v[212:215]
s_add_u32 m0, m0, 16384
s_and_b32 m0, m0, s78
v_mfma_f32_16x16x32_bf16 v[220:223], a[80:83], v[176:179], 0
v_exp_f32 v199, v199
v_mfma_f32_16x16x32_bf16 v[220:223], a[84:87], v[180:183], v[220:223]
v_cvt_pk_bf16_f32 v227, v198, v199
s_add_u32 s90, s90, s21
v_mfma_f32_16x16x32_bf16 v[220:223], a[88:91], v[184:187], v[220:223]
v_exp_f32 v204, v204
v_mfma_f32_16x16x32_bf16 v[220:223], a[92:95], v[188:191], v[220:223]
s_waitcnt vmcnt(12)
s_barrier
v_mfma_f32_16x16x32_bf16 v[64:67], a[96:99], v[232:235], v[64:67]
v_exp_f32 v205, v205
v_mfma_f32_16x16x32_bf16 v[96:99], a[96:99], v[236:239], v[96:99]
ds_read_b128 a[96:99], v248 offset:8192
.La5_LW2_1:
s_cbranch_execz .La5_LW2_5
v_cvt_pk_bf16_f32 v230, v204, v205
v_mfma_f32_16x16x32_bf16 v[68:71], a[100:103], v[232:235], v[68:71]
v_exp_f32 v206, v206
v_mfma_f32_16x16x32_bf16 v[100:103], a[100:103], v[236:239], v[100:103]
ds_read_b128 a[100:103], v248 offset:9216
buffer_load_dword v250, v251, s[64:67], s20 offen sc1
v_mfma_f32_16x16x32_bf16 v[72:75], a[104:107], v[232:235], v[72:75]
v_exp_f32 v207, v207
v_mfma_f32_16x16x32_bf16 v[104:107], a[104:107], v[236:239], v[104:107]
ds_read_b128 a[104:107], v248 offset:10240
v_cvt_pk_bf16_f32 v231, v206, v207
v_mfma_f32_16x16x32_bf16 v[76:79], a[108:111], v[232:235], v[76:79]
v_exp_f32 v208, v208
v_mfma_f32_16x16x32_bf16 v[108:111], a[108:111], v[236:239], v[108:111]
ds_read_b128 a[108:111], v248 offset:11264
v_mfma_f32_16x16x32_bf16 v[80:83], a[112:115], v[232:235], v[80:83]
v_exp_f32 v209, v209
v_mfma_f32_16x16x32_bf16 v[112:115], a[112:115], v[236:239], v[112:115]
ds_read_b128 a[112:115], v248 offset:12288
v_mfma_f32_16x16x32_bf16 v[84:87], a[116:119], v[232:235], v[84:87]
v_exp_f32 v210, v210
v_mfma_f32_16x16x32_bf16 v[116:119], a[116:119], v[236:239], v[116:119]
ds_read_b128 a[116:119], v248 offset:13312
v_mfma_f32_16x16x32_bf16 v[88:91], a[120:123], v[232:235], v[88:91]
v_exp_f32 v211, v211
v_mfma_f32_16x16x32_bf16 v[120:123], a[120:123], v[236:239], v[120:123]
ds_read_b128 a[120:123], v248 offset:14336
s_bitcmp1_b64 s[98:99], s85
v_mfma_f32_16x16x32_bf16 v[92:95], a[124:127], v[232:235], v[92:95]
v_exp_f32 v216, v216
v_mfma_f32_16x16x32_bf16 v[124:127], a[124:127], v[236:239], v[124:127]
ds_read_b128 a[124:127], v248 offset:15360
v_or_b32 v248, s89, v255
v_smfmac_f32_16x16x64_bf16 a[196:199], a[208:211], v[232:239], v241
s_waitcnt lgkmcnt(4)
v_smfmac_f32_16x16x64_bf16 a[192:195], a[208:211], v[224:231], v241
ds_read_b128 a[64:67], v248 offset:0
v_mfma_f32_16x16x32_bf16 v[0:3], a[96:99], v[224:227], v[0:3]
v_exp_f32 v217, v217
v_mfma_f32_16x16x32_bf16 v[32:35], a[96:99], v[228:231], v[32:35]
ds_read_b128 a[68:71], v248 offset:1024
v_cvt_pk_bf16_f32 v232, v208, v209
v_mfma_f32_16x16x32_bf16 v[4:7], a[100:103], v[224:227], v[4:7]
v_exp_f32 v218, v218
v_mfma_f32_16x16x32_bf16 v[36:39], a[100:103], v[228:231], v[36:39]
ds_read_b128 a[72:75], v248 offset:2048
v_cvt_pk_bf16_f32 v233, v210, v211
v_mfma_f32_16x16x32_bf16 v[8:11], a[104:107], v[224:227], v[8:11]
v_exp_f32 v219, v219
v_mfma_f32_16x16x32_bf16 v[40:43], a[104:107], v[228:231], v[40:43]
ds_read_b128 a[76:79], v248 offset:3072
v_cvt_pk_bf16_f32 v236, v216, v217
v_mfma_f32_16x16x32_bf16 v[12:15], a[108:111], v[224:227], v[12:15]
v_exp_f32 v212, v212
v_mfma_f32_16x16x32_bf16 v[44:47], a[108:111], v[228:231], v[44:47]
ds_read_b128 a[80:83], v248 offset:4096
s_waitcnt lgkmcnt(5)
v_mfma_f32_16x16x32_bf16 v[16:19], a[112:115], v[224:227], v[16:19]
v_exp_f32 v213, v213
v_mfma_f32_16x16x32_bf16 v[48:51], a[112:115], v[228:231], v[48:51]
ds_read_b128 a[84:87], v248 offset:5120
v_cvt_pk_bf16_f32 v237, v218, v219
v_mfma_f32_16x16x32_bf16 v[20:23], a[116:119], v[224:227], v[20:23]
v_exp_f32 v214, v214
v_mfma_f32_16x16x32_bf16 v[52:55], a[116:119], v[228:231], v[52:55]
ds_read_b128 a[88:91], v248 offset:6144
v_cvt_pk_bf16_f32 v234, v212, v213
v_mfma_f32_16x16x32_bf16 v[24:27], a[120:123], v[224:227], v[24:27]
v_exp_f32 v215, v215
v_mfma_f32_16x16x32_bf16 v[56:59], a[120:123], v[228:231], v[56:59]
ds_read_b128 a[92:95], v248 offset:7168
v_cvt_pk_bf16_f32 v235, v214, v215
v_mfma_f32_16x16x32_bf16 v[28:31], a[124:127], v[224:227], v[28:31]
v_exp_f32 v220, v220
v_mfma_f32_16x16x32_bf16 v[60:63], a[124:127], v[228:231], v[60:63]
s_cbranch_scc1 .La5_LA_F1E2
.p2align 6
; ---- steady windows
.La5_LA_F1:
s_waitcnt lgkmcnt(4)
v_mfma_f32_16x16x32_bf16 v[192:195], a[64:67], v[128:131], 0
v_exp_f32 v221, v221
v_mfma_f32_16x16x32_bf16 v[192:195], a[68:71], v[132:135], v[192:195]
v_cvt_pk_bf16_f32 v238, v220, v221
s_add_u32 s85, s85, 1
v_mfma_f32_16x16x32_bf16 v[192:195], a[72:75], v[136:139], v[192:195]
v_exp_f32 v222, v222
v_mfma_f32_16x16x32_bf16 v[192:195], a[76:79], v[140:143], v[192:195]
buffer_load_dwordx4 v255, s[60:63], s17 offen offset:0 sc1 lds
v_mfma_f32_16x16x32_bf16 v[200:203], a[64:67], v[144:147], 0
v_exp_f32 v223, v223
v_mfma_f32_16x16x32_bf16 v[200:203], a[68:71], v[148:151], v[200:203]
v_cvt_pk_bf16_f32 v239, v222, v223
s_add_u32 s89, s89, 16384
v_mfma_f32_16x16x32_bf16 v[200:203], a[72:75], v[152:155], v[200:203]
v_exp_f32 v192, v192
v_mfma_f32_16x16x32_bf16 v[200:203], a[76:79], v[156:159], v[200:203]
s_waitcnt lgkmcnt(0)
s_and_b32 s89, s89, 0x1ffff
v_mfma_f32_16x16x32_bf16 v[196:199], a[80:83], v[128:131], 0
v_exp_f32 v193, v193
v_mfma_f32_16x16x32_bf16 v[196:199], a[84:87], v[132:135], v[196:199]
v_cvt_pk_bf16_f32 v224, v192, v193
v_mfma_f32_16x16x32_bf16 v[196:199], a[88:91], v[136:139], v[196:199]
v_exp_f32 v194, v194
v_mfma_f32_16x16x32_bf16 v[196:199], a[92:95], v[140:143], v[196:199]
buffer_load_dwordx4 v255, s[60:63], s17 offen offset:1024 sc1 lds
v_mfma_f32_16x16x32_bf16 v[204:207], a[80:83], v[144:147], 0
v_exp_f32 v195, v195
v_mfma_f32_16x16x32_bf16 v[204:207], a[84:87], v[148:151], v[204:207]
v_cvt_pk_bf16_f32 v225, v194, v195
v_mfma_f32_16x16x32_bf16 v[204:207], a[88:91], v[152:155], v[204:207]
v_exp_f32 v200, v200
v_mfma_f32_16x16x32_bf16 v[204:207], a[92:95], v[156:159], v[204:207]
buffer_load_dwordx4 v255, s[60:63], s17 offen offset:2048 sc1 lds
v_mfma_f32_16x16x32_bf16 v[208:211], a[64:67], v[160:163], 0
v_exp_f32 v201, v201
v_mfma_f32_16x16x32_bf16 v[208:211], a[68:71], v[164:167], v[208:211]
v_cvt_pk_bf16_f32 v228, v200, v201
v_mfma_f32_16x16x32_bf16 v[208:211], a[72:75], v[168:171], v[208:211]
v_exp_f32 v202, v202
v_mfma_f32_16x16x32_bf16 v[208:211], a[76:79], v[172:175], v[208:211]
buffer_load_dwordx4 v255, s[60:63], s17 offen offset:3072 sc1 lds
v_mfma_f32_16x16x32_bf16 v[216:219], a[64:67], v[176:179], 0
v_exp_f32 v203, v203
v_mfma_f32_16x16x32_bf16 v[216:219], a[68:71], v[180:183], v[216:219]
v_cvt_pk_bf16_f32 v229, v202, v203
v_mfma_f32_16x16x32_bf16 v[216:219], a[72:75], v[184:187], v[216:219]
v_exp_f32 v196, v196
v_mfma_f32_16x16x32_bf16 v[216:219], a[76:79], v[188:191], v[216:219]
buffer_load_dword a253, v254, s[36:39], s90 offen
v_mfma_f32_16x16x32_bf16 v[212:215], a[80:83], v[160:163], 0
v_exp_f32 v197, v197
v_mfma_f32_16x16x32_bf16 v[212:215], a[84:87], v[164:167], v[212:215]
v_cvt_pk_bf16_f32 v226, v196, v197
s_add_u32 s17, s17, 8192
v_mfma_f32_16x16x32_bf16 v[212:215], a[88:91], v[168:171], v[212:215]
v_exp_f32 v198, v198
v_mfma_f32_16x16x32_bf16 v[212:215], a[92:95], v[172:175], v[212:215]
s_add_u32 m0, m0, 16384
s_and_b32 m0, m0, s78
v_mfma_f32_16x16x32_bf16 v[220:223], a[80:83], v[176:179], 0
v_exp_f32 v199, v199
v_mfma_f32_16x16x32_bf16 v[220:223], a[84:87], v[180:183], v[220:223]
v_cvt_pk_bf16_f32 v227, v198, v199
s_add_u32 s90, s90, s21
v_mfma_f32_16x16x32_bf16 v[220:223], a[88:91], v[184:187], v[220:223]
v_exp_f32 v204, v204
v_mfma_f32_16x16x32_bf16 v[220:223], a[92:95], v[188:191], v[220:223]
s_waitcnt vmcnt(11)
s_barrier
v_mfma_f32_16x16x32_bf16 v[64:67], a[96:99], v[232:235], v[64:67]
v_exp_f32 v205, v205
v_mfma_f32_16x16x32_bf16 v[96:99], a[96:99], v[236:239], v[96:99]
ds_read_b128 a[96:99], v248 offset:8192
v_cvt_pk_bf16_f32 v230, v204, v205
v_mfma_f32_16x16x32_bf16 v[68:71], a[100:103], v[232:235], v[68:71]
v_exp_f32 v206, v206
v_mfma_f32_16x16x32_bf16 v[100:103], a[100:103], v[236:239], v[100:103]
ds_read_b128 a[100:103], v248 offset:9216
v_mfma_f32_16x16x32_bf16 v[72:75], a[104:107], v[232:235], v[72:75]
v_exp_f32 v207, v207
v_mfma_f32_16x16x32_bf16 v[104:107], a[104:107], v[236:239], v[104:107]
ds_read_b128 a[104:107], v248 offset:10240
v_cvt_pk_bf16_f32 v231, v206, v207
v_mfma_f32_16x16x32_bf16 v[76:79], a[108:111], v[232:235], v[76:79]
v_exp_f32 v208, v208
v_mfma_f32_16x16x32_bf16 v[108:111], a[108:111], v[236:239], v[108:111]
ds_read_b128 a[108:111], v248 offset:11264
v_mfma_f32_16x16x32_bf16 v[80:83], a[112:115], v[232:235], v[80:83]
v_exp_f32 v209, v209
v_mfma_f32_16x16x32_bf16 v[112:115], a[112:115], v[236:239], v[112:115]
ds_read_b128 a[112:115], v248 offset:12288
v_mfma_f32_16x16x32_bf16 v[84:87], a[116:119], v[232:235], v[84:87]
v_exp_f32 v210, v210
v_mfma_f32_16x16x32_bf16 v[116:119], a[116:119], v[236:239], v[116:119]
ds_read_b128 a[116:119], v248 offset:13312
v_mfma_f32_16x16x32_bf16 v[88:91], a[120:123], v[232:235], v[88:91]
v_exp_f32 v211, v211
v_mfma_f32_16x16x32_bf16 v[120:123], a[120:123], v[236:239], v[120:123]
ds_read_b128 a[120:123], v248 offset:14336
s_bitcmp1_b64 s[98:99], s85
v_mfma_f32_16x16x32_bf16 v[92:95], a[124:127], v[232:235], v[92:95]
v_exp_f32 v216, v216
v_mfma_f32_16x16x32_bf16 v[124:127], a[124:127], v[236:239], v[124:127]
ds_read_b128 a[124:127], v248 offset:15360
v_or_b32 v248, s89, v255
v_smfmac_f32_16x16x64_bf16 a[196:199], a[208:211], v[232:239], v241
s_waitcnt lgkmcnt(4)
v_smfmac_f32_16x16x64_bf16 a[192:195], a[208:211], v[224:231], v241
ds_read_b128 a[64:67], v248 offset:0
v_mfma_f32_16x16x32_bf16 v[0:3], a[96:99], v[224:227], v[0:3]
v_exp_f32 v217, v217
v_mfma_f32_16x16x32_bf16 v[32:35], a[96:99], v[228:231], v[32:35]
ds_read_b128 a[68:71], v248 offset:1024
v_cvt_pk_bf16_f32 v232, v208, v209
v_mfma_f32_16x16x32_bf16 v[4:7], a[100:103], v[224:227], v[4:7]
v_exp_f32 v218, v218
v_mfma_f32_16x16x32_bf16 v[36:39], a[100:103], v[228:231], v[36:39]
ds_read_b128 a[72:75], v248 offset:2048
v_cvt_pk_bf16_f32 v233, v210, v211
v_mfma_f32_16x16x32_bf16 v[8:11], a[104:107], v[224:227], v[8:11]
v_exp_f32 v219, v219
v_mfma_f32_16x16x32_bf16 v[40:43], a[104:107], v[228:231], v[40:43]
ds_read_b128 a[76:79], v248 offset:3072
v_cvt_pk_bf16_f32 v236, v216, v217
v_mfma_f32_16x16x32_bf16 v[12:15], a[108:111], v[224:227], v[12:15]
v_exp_f32 v212, v212
v_mfma_f32_16x16x32_bf16 v[44:47], a[108:111], v[228:231], v[44:47]
ds_read_b128 a[80:83], v248 offset:4096
s_waitcnt lgkmcnt(5)
v_mfma_f32_16x16x32_bf16 v[16:19], a[112:115], v[224:227], v[16:19]
v_exp_f32 v213, v213
v_mfma_f32_16x16x32_bf16 v[48:51], a[112:115], v[228:231], v[48:51]
ds_read_b128 a[84:87], v248 offset:5120
v_cvt_pk_bf16_f32 v237, v218, v219
v_mfma_f32_16x16x32_bf16 v[20:23], a[116:119], v[224:227], v[20:23]
v_exp_f32 v214, v214
v_mfma_f32_16x16x32_bf16 v[52:55], a[116:119], v[228:231], v[52:55]
ds_read_b128 a[88:91], v248 offset:6144
v_cvt_pk_bf16_f32 v234, v212, v213
v_mfma_f32_16x16x32_bf16 v[24:27], a[120:123], v[224:227], v[24:27]
v_exp_f32 v215, v215
v_mfma_f32_16x16x32_bf16 v[56:59], a[120:123], v[228:231], v[56:59]
ds_read_b128 a[92:95], v248 offset:7168
v_cvt_pk_bf16_f32 v235, v214, v215
v_mfma_f32_16x16x32_bf16 v[28:31], a[124:127], v[224:227], v[28:31]
v_exp_f32 v220, v220
v_mfma_f32_16x16x32_bf16 v[60:63], a[124:127], v[228:231], v[60:63]
s_cbranch_scc1 .La5_LA_F1E2
s_waitcnt lgkmcnt(4)
v_mfma_f32_16x16x32_bf16 v[192:195], a[64:67], v[128:131], 0
v_exp_f32 v221, v221
v_mfma_f32_16x16x32_bf16 v[192:195], a[68:71], v[132:135], v[192:195]
v_cvt_pk_bf16_f32 v238, v220, v221
s_add_u32 s85, s85, 1
v_mfma_f32_16x16x32_bf16 v[192:195], a[72:75], v[136:139], v[192:195]
v_exp_f32 v222, v222
v_mfma_f32_16x16x32_bf16 v[192:195], a[76:79], v[140:143], v[192:195]
buffer_load_dwordx4 v255, s[60:63], s17 offen offset:0 sc1 lds
v_mfma_f32_16x16x32_bf16 v[200:203], a[64:67], v[144:147], 0
v_exp_f32 v223, v223
v_mfma_f32_16x16x32_bf16 v[200:203], a[68:71], v[148:151], v[200:203]
v_cvt_pk_bf16_f32 v239, v222, v223
s_add_u32 s89, s89, 16384
v_mfma_f32_16x16x32_bf16 v[200:203], a[72:75], v[152:155], v[200:203]
v_exp_f32 v192, v192
v_mfma_f32_16x16x32_bf16 v[200:203], a[76:79], v[156:159], v[200:203]
s_waitcnt lgkmcnt(0)
s_and_b32 s89, s89, 0x1ffff
v_mfma_f32_16x16x32_bf16 v[196:199], a[80:83], v[128:131], 0
v_exp_f32 v193, v193
v_mfma_f32_16x16x32_bf16 v[196:199], a[84:87], v[132:135], v[196:199]
v_cvt_pk_bf16_f32 v224, v192, v193
v_mfma_f32_16x16x32_bf16 v[196:199], a[88:91], v[136:139], v[196:199]
v_exp_f32 v194, v194
v_mfma_f32_16x16x32_bf16 v[196:199], a[92:95], v[140:143], v[196:199]
buffer_load_dwordx4 v255, s[60:63], s17 offen offset:1024 sc1 lds
v_mfma_f32_16x16x32_bf16 v[204:207], a[80:83], v[144:147], 0
v_exp_f32 v195, v195
v_mfma_f32_16x16x32_bf16 v[204:207], a[84:87], v[148:151], v[204:207]
v_cvt_pk_bf16_f32 v225, v194, v195
v_mfma_f32_16x16x32_bf16 v[204:207], a[88:91], v[152:155], v[204:207]
v_exp_f32 v200, v200
v_mfma_f32_16x16x32_bf16 v[204:207], a[92:95], v[156:159], v[204:207]
buffer_load_dwordx4 v255, s[60:63], s17 offen offset:2048 sc1 lds
v_mfma_f32_16x16x32_bf16 v[208:211], a[64:67], v[160:163], 0
v_exp_f32 v201, v201
v_mfma_f32_16x16x32_bf16 v[208:211], a[68:71], v[164:167], v[208:211]
v_cvt_pk_bf16_f32 v228, v200, v201
v_mfma_f32_16x16x32_bf16 v[208:211], a[72:75], v[168:171], v[208:211]
v_exp_f32 v202, v202
v_mfma_f32_16x16x32_bf16 v[208:211], a[76:79], v[172:175], v[208:211]
buffer_load_dwordx4 v255, s[60:63], s17 offen offset:3072 sc1 lds
v_mfma_f32_16x16x32_bf16 v[216:219], a[64:67], v[176:179], 0
v_exp_f32 v203, v203
v_mfma_f32_16x16x32_bf16 v[216:219], a[68:71], v[180:183], v[216:219]
v_cvt_pk_bf16_f32 v229, v202, v203
v_mfma_f32_16x16x32_bf16 v[216:219], a[72:75], v[184:187], v[216:219]
v_exp_f32 v196, v196
v_mfma_f32_16x16x32_bf16 v[216:219], a[76:79], v[188:191], v[216:219]
buffer_load_dword a253, v254, s[36:39], s90 offen
v_mfma_f32_16x16x32_bf16 v[212:215], a[80:83], v[160:163], 0
v_exp_f32 v197, v197
v_mfma_f32_16x16x32_bf16 v[212:215], a[84:87], v[164:167], v[212:215]
v_cvt_pk_bf16_f32 v226, v196, v197
s_add_u32 s17, s17, 8192
v_mfma_f32_16x16x32_bf16 v[212:215], a[88:91], v[168:171], v[212:215]
v_exp_f32 v198, v198
v_mfma_f32_16x16x32_bf16 v[212:215], a[92:95], v[172:175], v[212:215]
s_add_u32 m0, m0, 16384
s_and_b32 m0, m0, s78
v_mfma_f32_16x16x32_bf16 v[220:223], a[80:83], v[176:179], 0
v_exp_f32 v199, v199
v_mfma_f32_16x16x32_bf16 v[220:223], a[84:87], v[180:183], v[220:223]
v_cvt_pk_bf16_f32 v227, v198, v199
s_add_u32 s90, s90, s21
v_mfma_f32_16x16x32_bf16 v[220:223], a[88:91], v[184:187], v[220:223]
v_exp_f32 v204, v204
v_mfma_f32_16x16x32_bf16 v[220:223], a[92:95], v[188:191], v[220:223]
s_waitcnt vmcnt(11)
s_barrier
v_mfma_f32_16x16x32_bf16 v[64:67], a[96:99], v[232:235], v[64:67]
v_exp_f32 v205, v205
v_mfma_f32_16x16x32_bf16 v[96:99], a[96:99], v[236:239], v[96:99]
ds_read_b128 a[96:99], v248 offset:8192
v_cvt_pk_bf16_f32 v230, v204, v205
v_mfma_f32_16x16x32_bf16 v[68:71], a[100:103], v[232:235], v[68:71]
v_exp_f32 v206, v206
v_mfma_f32_16x16x32_bf16 v[100:103], a[100:103], v[236:239], v[100:103]
ds_read_b128 a[100:103], v248 offset:9216
v_mfma_f32_16x16x32_bf16 v[72:75], a[104:107], v[232:235], v[72:75]
v_exp_f32 v207, v207
v_mfma_f32_16x16x32_bf16 v[104:107], a[104:107], v[236:239], v[104:107]
ds_read_b128 a[104:107], v248 offset:10240
v_cvt_pk_bf16_f32 v231, v206, v207
v_mfma_f32_16x16x32_bf16 v[76:79], a[108:111], v[232:235], v[76:79]
v_exp_f32 v208, v208
v_mfma_f32_16x16x32_bf16 v[108:111], a[108:111], v[236:239], v[108:111]
ds_read_b128 a[108:111], v248 offset:11264
v_mfma_f32_16x16x32_bf16 v[80:83], a[112:115], v[232:235], v[80:83]
v_exp_f32 v209, v209
v_mfma_f32_16x16x32_bf16 v[112:115], a[112:115], v[236:239], v[112:115]
ds_read_b128 a[112:115], v248 offset:12288
v_mfma_f32_16x16x32_bf16 v[84:87], a[116:119], v[232:235], v[84:87]
v_exp_f32 v210, v210
v_mfma_f32_16x16x32_bf16 v[116:119], a[116:119], v[236:239], v[116:119]
ds_read_b128 a[116:119], v248 offset:13312
v_mfma_f32_16x16x32_bf16 v[88:91], a[120:123], v[232:235], v[88:91]
v_exp_f32 v211, v211
v_mfma_f32_16x16x32_bf16 v[120:123], a[120:123], v[236:239], v[120:123]
ds_read_b128 a[120:123], v248 offset:14336
s_bitcmp1_b64 s[98:99], s85
v_mfma_f32_16x16x32_bf16 v[92:95], a[124:127], v[232:235], v[92:95]
v_exp_f32 v216, v216
v_mfma_f32_16x16x32_bf16 v[124:127], a[124:127], v[236:239], v[124:127]
ds_read_b128 a[124:127], v248 offset:15360
v_or_b32 v248, s89, v255
v_smfmac_f32_16x16x64_bf16 a[196:199], a[208:211], v[232:239], v241
s_waitcnt lgkmcnt(4)
v_smfmac_f32_16x16x64_bf16 a[192:195], a[208:211], v[224:231], v241
ds_read_b128 a[64:67], v248 offset:0
v_mfma_f32_16x16x32_bf16 v[0:3], a[96:99], v[224:227], v[0:3]
v_exp_f32 v217, v217
v_mfma_f32_16x16x32_bf16 v[32:35], a[96:99], v[228:231], v[32:35]
ds_read_b128 a[68:71], v248 offset:1024
v_cvt_pk_bf16_f32 v232, v208, v209
v_mfma_f32_16x16x32_bf16 v[4:7], a[100:103], v[224:227], v[4:7]
v_exp_f32 v218, v218
v_mfma_f32_16x16x32_bf16 v[36:39], a[100:103], v[228:231], v[36:39]
ds_read_b128 a[72:75], v248 offset:2048
v_cvt_pk_bf16_f32 v233, v210, v211
v_mfma_f32_16x16x32_bf16 v[8:11], a[104:107], v[224:227], v[8:11]
v_exp_f32 v219, v219
v_mfma_f32_16x16x32_bf16 v[40:43], a[104:107], v[228:231], v[40:43]
ds_read_b128 a[76:79], v248 offset:3072
v_cvt_pk_bf16_f32 v236, v216, v217
v_mfma_f32_16x16x32_bf16 v[12:15], a[108:111], v[224:227], v[12:15]
v_exp_f32 v212, v212
v_mfma_f32_16x16x32_bf16 v[44:47], a[108:111], v[228:231], v[44:47]
ds_read_b128 a[80:83], v248 offset:4096
s_waitcnt lgkmcnt(5)
v_mfma_f32_16x16x32_bf16 v[16:19], a[112:115], v[224:227], v[16:19]
v_exp_f32 v213, v213
v_mfma_f32_16x16x32_bf16 v[48:51], a[112:115], v[228:231], v[48:51]
ds_read_b128 a[84:87], v248 offset:5120
v_cvt_pk_bf16_f32 v237, v218, v219
v_mfma_f32_16x16x32_bf16 v[20:23], a[116:119], v[224:227], v[20:23]
v_exp_f32 v214, v214
v_mfma_f32_16x16x32_bf16 v[52:55], a[116:119], v[228:231], v[52:55]
ds_read_b128 a[88:91], v248 offset:6144
v_cvt_pk_bf16_f32 v234, v212, v213
v_mfma_f32_16x16x32_bf16 v[24:27], a[120:123], v[224:227], v[24:27]
v_exp_f32 v215, v215
v_mfma_f32_16x16x32_bf16 v[56:59], a[120:123], v[228:231], v[56:59]
ds_read_b128 a[92:95], v248 offset:7168
v_cvt_pk_bf16_f32 v235, v214, v215
v_mfma_f32_16x16x32_bf16 v[28:31], a[124:127], v[224:227], v[28:31]
v_exp_f32 v220, v220
v_mfma_f32_16x16x32_bf16 v[60:63], a[124:127], v[228:231], v[60:63]
.La5_LA_F1E:
s_cbranch_scc0 .La5_LA_F1
.La5_LA_F1E2:
s_cmp_eq_u32 s85, s82
s_cbranch_scc1 .La5_LA_F1X
s_nop 0
s_nop 0
s_call_b64 s[24:25], .La5_LA_EVENT
s_branch .La5_LA_F1
.La5_LA_FE0:
.La5_LW2_2:
s_cbranch_execz .La5_LW2_6
s_cmp_eq_u32 s85, s82
s_cbranch_scc1 .La5_LA_F1X
s_call_b64 s[24:25], .La5_LA_EVENT
s_branch .La5_LA_FS0
; ---- hand-offs between windows
.La5_LA_EVENT:
s_cmp_lg_u32 s85, 2
s_cbranch_scc1 .La5_LA_E1
s_waitcnt vmcnt(0)
v_cmp_eq_u32 vcc, v250, v249
s_mov_b32 s17, s16
s_mov_b32 s78, 0x1ffff
s_lshl_b32 m0, s3, 12
s_or_b32 vcc_hi, vcc_hi, s79
s_cmp_eq_u64 vcc, -1
s_cbranch_scc0 .La5_LA_KSPIN
.La5_LA_KOK:
buffer_load_dwordx4 v255, s[60:63], s17 offen offset:0 sc1 lds
buffer_load_dwordx4 v255, s[60:63], s17 offen offset:1024 sc1 lds
buffer_load_dwordx4 v255, s[60:63], s17 offen offset:2048 sc1 lds
buffer_load_dwordx4 v255, s[60:63], s17 offen offset:3072 sc1 lds
s_add_u32 s17, s17, 8192
s_add_u32 m0, m0, 16384
s_mov_b32 s20, s19
v_and_b32 v251, 4, v251
s_cmp_lg_u32 s3, 0
s_cbranch_scc1 .La5_LA_POLL
s_nop 0
buffer_load_dwordx4 v255, s[60:63], s17 offen offset:0 sc1 lds
buffer_load_dwordx4 v255, s[60:63], s17 offen offset:1024 sc1 lds
buffer_load_dwordx4 v255, s[60:63], s17 offen offset:2048 sc1 lds
buffer_load_dwordx4 v255, s[60:63], s17 offen offset:3072 sc1 lds
s_add_u32 s17, s17, 8192
s_add_u32 m0, m0, 16384
.La5_LA_POLL:
s_cmp_eq_u32 s73, 1
s_cbranch_scc1 .La5_LA_E0
buffer_load_dword v250, v251, s[64:67], s20 offen sc1
s_setpc_b64 s[24:25]
.La5_LA_E0:
s_mov_b32 s54, 16384
buffer_load_dwordx4 a[0:3], v255, s[52:55], 0 offen offset:0 sc1
buffer_load_dwordx4 a[4:7], v255, s[52:55], 0 offen offset:1024 sc1
buffer_load_dwordx4 a[8:11], v255, s[52:55], 0 offen offset:2048 sc1
buffer_load_dwordx4 a[12:15], v255, s[52:55], 0 offen offset:3072 sc1
buffer_load_dwordx4 a[16:19], v255, s[52:55], s95 offen offset:0 sc1
buffer_load_dwordx4 a[20:23], v255, s[52:55], s95 offen offset:1024 sc1
s_setpc_b64 s[24:25]
.La5_LA_E1:
buffer_load_dwordx4 a[24:27], v255, s[52:55], s95 offen offset:2048 sc1
buffer_load_dwordx4 a[28:31], v255, s[52:55], s95 offen offset:3072 sc1
buffer_load_dwordx4 a[32:35], v255, s[52:55], s96 offen offset:0 sc1
buffer_load_dwordx4 a[36:39], v255, s[52:55], s96 offen offset:1024 sc1
buffer_load_dwordx4 a[40:43], v255, s[52:55], s96 offen offset:2048 sc1
buffer_load_dwordx4 a[44:47], v255, s[52:55], s96 offen offset:3072 sc1
buffer_load_dwordx4 a[48:51], v255, s[52:55], s97 offen offset:0 sc1
buffer_load_dwordx4 a[52:55], v255, s[52:55], s97 offen offset:1024 sc1
buffer_load_dwordx4 a[56:59], v255, s[52:55], s97 offen offset:2048 sc1
buffer_load_dwordx4 a[60:63], v255, s[52:55], s97 offen offset:3072 sc1
s_setpc_b64 s[24:25]
; ---- helper: own block finished: l is checked (.La5_LA_SLOW if outside [2^-60, 2^60] or NaN), O / l leaves; the partner-s rows start
.La5_LA_F1X:
s_cmp_eq_u32 s22, 0
s_cbranch_scc1 .La5_LA_DC
s_cmp_eq_u32 s73, 1
s_cbranch_scc1 .La5_LA_F1Y
s_nop 0
v_cmp_eq_u32 vcc, v250, v249
s_mov_b32 s17, 0
s_mov_b32 s54, 16384
s_and_b32 s92, vcc_lo, 3
s_cmp_eq_u32 s92, 3
s_cbranch_scc0 .La5_LA_QSPIN
.La5_LA_QOK:
s_waitcnt lgkmcnt(4)
v_mfma_f32_16x16x32_bf16 v[192:195], a[64:67], v[128:131], 0
v_exp_f32 v221, v221
v_mfma_f32_16x16x32_bf16 v[192:195], a[68:71], v[132:135], v[192:195]
v_cvt_pk_bf16_f32 v238, v220, v221
s_add_u32 s85, s85, 1
v_mfma_f32_16x16x32_bf16 v[192:195], a[72:75], v[136:139], v[192:195]
v_exp_f32 v222, v222
v_mfma_f32_16x16x32_bf16 v[192:195], a[76:79], v[140:143], v[192:195]
buffer_load_dwordx4 v255, s[60:63], s17 offen offset:0 sc1 lds
v_mfma_f32_16x16x32_bf16 v[200:203], a[64:67], v[144:147], 0
v_exp_f32 v223, v223
v_mfma_f32_16x16x32_bf16 v[200:203], a[68:71], v[148:151], v[200:203]
v_cvt_pk_bf16_f32 v239, v222, v223
s_add_u32 s89, s89, 16384
v_mfma_f32_16x16x32_bf16 v[200:203], a[72:75], v[152:155], v[200:203]
v_exp_f32 v192, v192
v_mfma_f32_16x16x32_bf16 v[200:203], a[76:79], v[156:159], v[200:203]
s_waitcnt lgkmcnt(0)
s_and_b32 s89, s89, 0x1ffff
v_mfma_f32_16x16x32_bf16 v[196:199], a[80:83], v[128:131], 0
v_exp_f32 v193, v193
v_mfma_f32_16x16x32_bf16 v[196:199], a[84:87], v[132:135], v[196:199]
v_cvt_pk_bf16_f32 v224, v192, v193
v_mfma_f32_16x16x32_bf16 v[196:199], a[88:91], v[136:139], v[196:199]
v_exp_f32 v194, v194
v_mfma_f32_16x16x32_bf16 v[196:199], a[92:95], v[140:143], v[196:199]
buffer_load_dwordx4 v255, s[60:63], s17 offen offset:1024 sc1 lds
v_mfma_f32_16x16x32_bf16 v[204:207], a[80:83], v[144:147], 0
v_exp_f32 v195, v195
v_mfma_f32_16x16x32_bf16 v[204:207], a[84:87], v[148:151], v[204:207]
v_cvt_pk_bf16_f32 v225, v194, v195
v_mfma_f32_16x16x32_bf16 v[204:207], a[88:91], v[152:155], v[204:207]
v_exp_f32 v200, v200
v_mfma_f32_16x16x32_bf16 v[204:207], a[92:95], v[156:159], v[204:207]
buffer_load_dwordx4 v255, s[60:63], s17 offen offset:2048 sc1 lds
v_mfma_f32_16x16x32_bf16 v[208:211], a[64:67], v[160:163], 0
v_exp_f32 v201, v201
v_mfma_f32_16x16x32_bf16 v[208:211], a[68:71], v[164:167], v[208:211]
v_cvt_pk_bf16_f32 v228, v200, v201
v_mfma_f32_16x16x32_bf16 v[208:211], a[72:75], v[168:171], v[208:211]
v_exp_f32 v202, v202
v_mfma_f32_16x16x32_bf16 v[208:211], a[76:79], v[172:175], v[208:211]
buffer_load_dwordx4 v255, s[60:63], s17 offen offset:3072 sc1 lds
v_mfma_f32_16x16x32_bf16 v[216:219], a[64:67], v[176:179], 0
v_exp_f32 v203, v203
v_mfma_f32_16x16x32_bf16 v[216:219], a[68:71], v[180:183], v[216:219]
v_cvt_pk_bf16_f32 v229, v202, v203
v_mfma_f32_16x16x32_bf16 v[216:219], a[72:75], v[184:187], v[216:219]
v_exp_f32 v196, v196
v_mfma_f32_16x16x32_bf16 v[216:219], a[76:79], v[188:191], v[216:219]
buffer_load_dwordx4 a[0:3], v255, s[52:55], 0 offen offset:0 sc1
v_mfma_f32_16x16x32_bf16 v[212:215], a[80:83], v[160:163], 0
v_exp_f32 v197, v197
v_mfma_f32_16x16x32_bf16 v[212:215], a[84:87], v[164:167], v[212:215]
v_cvt_pk_bf16_f32 v226, v196, v197
s_add_u32 s17, s17, 8192
v_mfma_f32_16x16x32_bf16 v[212:215], a[88:91], v[168:171], v[212:215]
v_exp_f32 v198, v198
v_mfma_f32_16x16x32_bf16 v[212:215], a[92:95], v[172:175], v[212:215]
s_add_u32 m0, m0, 16384
s_and_b32 m0, m0, s78
buffer_load_dwordx4 a[4:7], v255, s[52:55], 0 offen offset:1024 sc1
v_mfma_f32_16x16x32_bf16 v[220:223], a[80:83], v[176:179], 0
v_exp_f32 v199, v199
v_mfma_f32_16x16x32_bf16 v[220:223], a[84:87], v[180:183], v[220:223]
v_cvt_pk_bf16_f32 v227, v198, v199
v_mfma_f32_16x16x32_bf16 v[220:223], a[88:91], v[184:187], v[220:223]
v_exp_f32 v204, v204
v_mfma_f32_16x16x32_bf16 v[220:223], a[92:95], v[188:191], v[220:223]
s_waitcnt vmcnt(12)
s_barrier
v_mfma_f32_16x16x32_bf16 v[64:67], a[96:99], v[232:235], v[64:67]
v_exp_f32 v205, v205
v_mfma_f32_16x16x32_bf16 v[96:99], a[96:99], v[236:239], v[96:99]
ds_read_b128 a[96:99], v248 offset:8192
v_cvt_pk_bf16_f32 v230, v204, v205
v_mfma_f32_16x16x32_bf16 v[68:71], a[100:103], v[232:235], v[68:71]
v_exp_f32 v206, v206
v_mfma_f32_16x16x32_bf16 v[100:103], a[100:103], v[236:239], v[100:103]
ds_read_b128 a[100:103], v248 offset:9216
buffer_load_dwordx4 a[8:11], v255, s[52:55], 0 offen offset:2048 sc1
v_mfma_f32_16x16x32_bf16 v[72:75], a[104:107], v[232:235], v[72:75]
v_exp_f32 v207, v207
v_mfma_f32_16x16x32_bf16 v[104:107], a[104:107], v[236:239], v[104:107]
ds_read_b128 a[104:107], v248 offset:10240
v_cvt_pk_bf16_f32 v231, v206, v207
v_mfma_f32_16x16x32_bf16 v[76:79], a[108:111], v[232:235], v[76:79]
v_exp_f32 v208, v208
v_mfma_f32_16x16x32_bf16 v[108:111], a[108:111], v[236:239], v[108:111]
ds_read_b128 a[108:111], v248 offset:11264
buffer_load_dwordx4 a[12:15], v255, s[52:55], 0 offen offset:3072 sc1
v_mfma_f32_16x16x32_bf16 v[80:83], a[112:115], v[232:235], v[80:83]
v_exp_f32 v209, v209
v_mfma_f32_16x16x32_bf16 v[112:115], a[112:115], v[236:239], v[112:115]
ds_read_b128 a[112:115], v248 offset:12288
v_mfma_f32_16x16x32_bf16 v[84:87], a[116:119], v[232:235], v[84:87]
v_exp_f32 v210, v210
v_mfma_f32_16x16x32_bf16 v[116:119], a[116:119], v[236:239], v[116:119]
ds_read_b128 a[116:119], v248 offset:13312
buffer_load_dwordx4 a[16:19], v255, s[52:55], s95 offen offset:0 sc1
v_mfma_f32_16x16x32_bf16 v[88:91], a[120:123], v[232:235], v[88:91]
v_exp_f32 v211, v211
v_mfma_f32_16x16x32_bf16 v[120:123], a[120:123], v[236:239], v[120:123]
ds_read_b128 a[120:123], v248 offset:14336
s_bitcmp1_b64 s[98:99], s85
v_mfma_f32_16x16x32_bf16 v[92:95], a[124:127], v[232:235], v[92:95]
v_exp_f32 v216, v216
v_mfma_f32_16x16x32_bf16 v[124:127], a[124:127], v[236:239], v[124:127]
ds_read_b128 a[124:127], v248 offset:15360
v_or_b32 v248, s89, v255
v_smfmac_f32_16x16x64_bf16 a[196:199], a[208:211], v[232:239], v241
s_waitcnt lgkmcnt(4)
v_smfmac_f32_16x16x64_bf16 a[192:195], a[208:211], v[224:231], v241
ds_read_b128 a[64:67], v248 offset:0
v_mfma_f32_16x16x32_bf16 v[0:3], a[96:99], v[224:227], v[0:3]
v_exp_f32 v217, v217
v_mfma_f32_16x16x32_bf16 v[32:35], a[96:99], v[228:231], v[32:35]
ds_read_b128 a[68:71], v248 offset:1024
v_cvt_pk_bf16_f32 v232, v208, v209
v_mfma_f32_16x16x32_bf16 v[4:7], a[100:103], v[224:227], v[4:7]
v_exp_f32 v218, v218
v_mfma_f32_16x16x32_bf16 v[36:39], a[100:103], v[228:231], v[36:39]
ds_read_b128 a[72:75], v248 offset:2048
v_cvt_pk_bf16_f32 v233, v210, v211
v_mfma_f32_16x16x32_bf16 v[8:11], a[104:107], v[224:227], v[8:11]
v_exp_f32 v219, v219
v_mfma_f32_16x16x32_bf16 v[40:43], a[104:107], v[228:231], v[40:43]
ds_read_b128 a[76:79], v248 offset:3072
v_cvt_pk_bf16_f32 v236, v216, v217
v_mfma_f32_16x16x32_bf16 v[12:15], a[108:111], v[224:227], v[12:15]
v_exp_f32 v212, v212
v_mfma_f32_16x16x32_bf16 v[44:47], a[108:111], v[228:231], v[44:47]
ds_read_b128 a[80:83], v248 offset:4096
s_waitcnt lgkmcnt(5)
v_mfma_f32_16x16x32_bf16 v[16:19], a[112:115], v[224:227], v[16:19]
v_exp_f32 v213, v213
v_mfma_f32_16x16x32_bf16 v[48:51], a[112:115], v[228:231], v[48:51]
ds_read_b128 a[84:87], v248 offset:5120
v_cvt_pk_bf16_f32 v237, v218, v219
v_mfma_f32_16x16x32_bf16 v[20:23], a[116:119], v[224:227], v[20:23]
v_exp_f32 v214, v214
v_mfma_f32_16x16x32_bf16 v[52:55], a[116:119], v[228:231], v[52:55]
ds_read_b128 a[88:91], v248 offset:6144
v_cvt_pk_bf16_f32 v234, v212, v213
v_mfma_f32_16x16x32_bf16 v[24:27], a[120:123], v[224:227], v[24:27]
v_exp_f32 v215, v215
v_mfma_f32_16x16x32_bf16 v[56:59], a[120:123], v[228:231], v[56:59]
ds_read_b128 a[92:95], v248 offset:7168
v_cvt_pk_bf16_f32 v235, v214, v215
v_mfma_f32_16x16x32_bf16 v[28:31], a[124:127], v[224:227], v[28:31]
.La5_LW2_3:
s_cbranch_execz .La5_LW2_7
v_exp_f32 v220, v220
v_mfma_f32_16x16x32_bf16 v[60:63], a[124:127], v[228:231], v[60:63]
buffer_load_dwordx4 a[20:23], v255, s[52:55], s95 offen offset:1024 sc1
s_waitcnt lgkmcnt(4)
v_mfma_f32_16x16x32_bf16 v[192:195], a[64:67], v[128:131], 0
v_exp_f32 v221, v221
v_mfma_f32_16x16x32_bf16 v[192:195], a[68:71], v[132:135], v[192:195]
v_cvt_pk_bf16_f32 v238, v220, v221
s_add_u32 s85, s85, 1
v_mfma_f32_16x16x32_bf16 v[192:195], a[72:75], v[136:139], v[192:195]
v_exp_f32 v222, v222
v_mfma_f32_16x16x32_bf16 v[192:195], a[76:79], v[140:143], v[192:195]
buffer_load_dwordx4 v255, s[60:63], s17 offen offset:0 sc1 lds
v_mfma_f32_16x16x32_bf16 v[200:203], a[64:67], v[144:147], 0
v_exp_f32 v223, v223
v_mfma_f32_16x16x32_bf16 v[200:203], a[68:71], v[148:151], v[200:203]
v_cvt_pk_bf16_f32 v239, v222, v223
s_add_u32 s89, s89, 16384
v_mfma_f32_16x16x32_bf16 v[200:203], a[72:75], v[152:155], v[200:203]
v_exp_f32 v192, v192
v_mfma_f32_16x16x32_bf16 v[200:203], a[76:79], v[156:159], v[200:203]
s_waitcnt lgkmcnt(0)
s_and_b32 s89, s89, 0x1ffff
buffer_load_dwordx4 a[24:27], v255, s[52:55], s95 offen offset:2048 sc1
v_mfma_f32_16x16x32_bf16 v[196:199], a[80:83], v[128:131], 0
v_exp_f32 v193, v193
v_mfma_f32_16x16x32_bf16 v[196:199], a[84:87], v[132:135], v[196:199]
v_cvt_pk_bf16_f32 v224, v192, v193
v_mfma_f32_16x16x32_bf16 v[196:199], a[88:91], v[136:139], v[196:199]
v_exp_f32 v194, v194
v_mfma_f32_16x16x32_bf16 v[196:199], a[92:95], v[140:143], v[196:199]
buffer_load_dwordx4 v255, s[60:63], s17 offen offset:1024 sc1 lds
v_mfma_f32_16x16x32_bf16 v[204:207], a[80:83], v[144:147], 0
v_exp_f32 v195, v195
v_mfma_f32_16x16x32_bf16 v[204:207], a[84:87], v[148:151], v[204:207]
v_cvt_pk_bf16_f32 v225, v194, v195
v_mfma_f32_16x16x32_bf16 v[204:207], a[88:91], v[152:155], v[204:207]
v_exp_f32 v200, v200
v_mfma_f32_16x16x32_bf16 v[204:207], a[92:95], v[156:159], v[204:207]
buffer_load_dwordx4 v255, s[60:63], s17 offen offset:2048 sc1 lds
v_mfma_f32_16x16x32_bf16 v[208:211], a[64:67], v[160:163], 0
v_exp_f32 v201, v201
v_mfma_f32_16x16x32_bf16 v[208:211], a[68:71], v[164:167], v[208:211]
v_cvt_pk_bf16_f32 v228, v200, v201
v_mfma_f32_16x16x32_bf16 v[208:211], a[72:75], v[168:171], v[208:211]
v_exp_f32 v202, v202
v_mfma_f32_16x16x32_bf16 v[208:211], a[76:79], v[172:175], v[208:211]
buffer_load_dwordx4 v255, s[60:63], s17 offen offset:3072 sc1 lds
v_mfma_f32_16x16x32_bf16 v[216:219], a[64:67], v[176:179], 0
v_exp_f32 v203, v203
v_mfma_f32_16x16x32_bf16 v[216:219], a[68:71], v[180:183], v[216:219]
v_cvt_pk_bf16_f32 v229, v202, v203
v_mfma_f32_16x16x32_bf16 v[216:219], a[72:75], v[184:187], v[216:219]
v_exp_f32 v196, v196
v_mfma_f32_16x16x32_bf16 v[216:219], a[76:79], v[188:191], v[216:219]
buffer_load_dwordx4 a[28:31], v255, s[52:55], s95 offen offset:3072 sc1
v_mfma_f32_16x16x32_bf16 v[212:215], a[80:83], v[160:163], 0
v_exp_f32 v197, v197
v_mfma_f32_16x16x32_bf16 v[212:215], a[84:87], v[164:167], v[212:215]
v_cvt_pk_bf16_f32 v226, v196, v197
s_add_u32 s17, s17, 8192
v_mfma_f32_16x16x32_bf16 v[212:215], a[88:91], v[168:171], v[212:215]
v_exp_f32 v198, v198
v_mfma_f32_16x16x32_bf16 v[212:215], a[92:95], v[172:175], v[212:215]
s_add_u32 m0, m0, 16384
s_and_b32 m0, m0, s78
buffer_load_dwordx4 a[32:35], v255, s[52:55], s96 offen offset:0 sc1
v_mfma_f32_16x16x32_bf16 v[220:223], a[80:83], v[176:179], 0
v_exp_f32 v199, v199
v_mfma_f32_16x16x32_bf16 v[220:223], a[84:87], v[180:183], v[220:223]
v_cvt_pk_bf16_f32 v227, v198, v199
v_mfma_f32_16x16x32_bf16 v[220:223], a[88:91], v[184:187], v[220:223]
v_exp_f32 v204, v204
v_mfma_f32_16x16x32_bf16 v[220:223], a[92:95], v[188:191], v[220:223]
s_waitcnt vmcnt(18)
s_barrier
v_mfma_f32_16x16x32_bf16 v[64:67], a[96:99], v[232:235], v[64:67]
v_exp_f32 v205, v205
v_mfma_f32_16x16x32_bf16 v[96:99], a[96:99], v[236:239], v[96:99]
ds_read_b128 a[96:99], v248 offset:8192
v_cvt_pk_bf16_f32 v230, v204, v205
v_mfma_f32_16x16x32_bf16 v[68:71], a[100:103], v[232:235], v[68:71]
v_exp_f32 v206, v206
v_mfma_f32_16x16x32_bf16 v[100:103], a[100:103], v[236:239], v[100:103]
ds_read_b128 a[100:103], v248 offset:9216
buffer_load_dwordx4 a[36:39], v255, s[52:55], s96 offen offset:1024 sc1
v_mfma_f32_16x16x32_bf16 v[72:75], a[104:107], v[232:235], v[72:75]
v_exp_f32 v207, v207
v_mfma_f32_16x16x32_bf16 v[104:107], a[104:107], v[236:239], v[104:107]
ds_read_b128 a[104:107], v248 offset:10240
v_cvt_pk_bf16_f32 v231, v206, v207
v_mfma_f32_16x16x32_bf16 v[76:79], a[108:111], v[232:235], v[76:79]
v_exp_f32 v208, v208
v_mfma_f32_16x16x32_bf16 v[108:111], a[108:111], v[236:239], v[108:111]
ds_read_b128 a[108:111], v248 offset:11264
buffer_load_dwordx4 a[40:43], v255, s[52:55], s96 offen offset:2048 sc1
v_mfma_f32_16x16x32_bf16 v[80:83], a[112:115], v[232:235], v[80:83]
v_exp_f32 v209, v209
v_mfma_f32_16x16x32_bf16 v[112:115], a[112:115], v[236:239], v[112:115]
ds_read_b128 a[112:115], v248 offset:12288
v_mfma_f32_16x16x32_bf16 v[84:87], a[116:119], v[232:235], v[84:87]
v_exp_f32 v210, v210
v_mfma_f32_16x16x32_bf16 v[116:119], a[116:119], v[236:239], v[116:119]
ds_read_b128 a[116:119], v248 offset:13312
buffer_load_dwordx4 a[44:47], v255, s[52:55], s96 offen offset:3072 sc1
v_mfma_f32_16x16x32_bf16 v[88:91], a[120:123], v[232:235], v[88:91]
v_exp_f32 v211, v211
v_mfma_f32_16x16x32_bf16 v[120:123], a[120:123], v[236:239], v[120:123]
ds_read_b128 a[120:123], v248 offset:14336
s_bitcmp1_b64 s[98:99], s85
v_mfma_f32_16x16x32_bf16 v[92:95], a[124:127], v[232:235], v[92:95]
v_exp_f32 v216, v216
v_mfma_f32_16x16x32_bf16 v[124:127], a[124:127], v[236:239], v[124:127]
ds_read_b128 a[124:127], v248 offset:15360
v_or_b32 v248, s89, v255
v_smfmac_f32_16x16x64_bf16 a[196:199], a[208:211], v[232:239], v241
s_waitcnt lgkmcnt(4)
v_smfmac_f32_16x16x64_bf16 a[192:195], a[208:211], v[224:231], v241
ds_read_b128 a[64:67], v248 offset:0
v_mfma_f32_16x16x32_bf16 v[0:3], a[96:99], v[224:227], v[0:3]
v_exp_f32 v217, v217
v_mfma_f32_16x16x32_bf16 v[32:35], a[96:99], v[228:231], v[32:35]
ds_read_b128 a[68:71], v248 offset:1024
v_cvt_pk_bf16_f32 v232, v208, v209
v_mfma_f32_16x16x32_bf16 v[4:7], a[100:103], v[224:227], v[4:7]
v_exp_f32 v218, v218
v_mfma_f32_16x16x32_bf16 v[36:39], a[100:103], v[228:231], v[36:39]
ds_read_b128 a[72:75], v248 offset:2048
v_cvt_pk_bf16_f32 v233, v210, v211
v_mfma_f32_16x16x32_bf16 v[8:11], a[104:107], v[224:227], v[8:11]
v_exp_f32 v219, v219
v_mfma_f32_16x16x32_bf16 v[40:43], a[104:107], v[228:231], v[40:43]
ds_read_b128 a[76:79], v248 offset:3072
v_cvt_pk_bf16_f32 v236, v216, v217
v_mfma_f32_16x16x32_bf16 v[12:15], a[108:111], v[224:227], v[12:15]
v_exp_f32 v212, v212
v_mfma_f32_16x16x32_bf16 v[44:47], a[108:111], v[228:231], v[44:47]
ds_read_b128 a[80:83], v248 offset:4096
s_waitcnt lgkmcnt(5)
v_mfma_f32_16x16x32_bf16 v[16:19], a[112:115], v[224:227], v[16:19]
v_exp_f32 v213, v213
v_mfma_f32_16x16x32_bf16 v[48:51], a[112:115], v[228:231], v[48:51]
ds_read_b128 a[84:87], v248 offset:5120
v_cvt_pk_bf16_f32 v237, v218, v219
v_mfma_f32_16x16x32_bf16 v[20:23], a[116:119], v[224:227], v[20:23]
v_exp_f32 v214, v214
v_mfma_f32_16x16x32_bf16 v[52:55], a[116:119], v[228:231], v[52:55]
ds_read_b128 a[88:91], v248 offset:6144
v_cvt_pk_bf16_f32 v234, v212, v213
v_mfma_f32_16x16x32_bf16 v[24:27], a[120:123], v[224:227], v[24:27]
v_exp_f32 v215, v215
v_mfma_f32_16x16x32_bf16 v[56:59], a[120:123], v[228:231], v[56:59]
ds_read_b128 a[92:95], v248 offset:7168
v_cvt_pk_bf16_f32 v235, v214, v215
v_mfma_f32_16x16x32_bf16 v[28:31], a[124:127], v[224:227], v[28:31]
v_exp_f32 v220, v220
v_mfma_f32_16x16x32_bf16 v[60:63], a[124:127], v[228:231], v[60:63]
buffer_load_dwordx4 a[48:51], v255, s[52:55], s97 offen offset:0 sc1
s_waitcnt lgkmcnt(4)
v_mfma_f32_16x16x32_bf16 v[192:195], a[64:67], v[128:131], 0
v_exp_f32 v221, v221
v_mfma_f32_16x16x32_bf16 v[192:195], a[68:71], v[132:135], v[192:195]
v_cvt_pk_bf16_f32 v238, v220, v221
s_add_u32 s85, s85, 1
v_mfma_f32_16x16x32_bf16 v[192:195], a[72:75], v[136:139], v[192:195]
v_exp_f32 v222, v222
v_mfma_f32_16x16x32_bf16 v[192:195], a[76:79], v[140:143], v[192:195]
buffer_load_dwordx4 v255, s[60:63], s17 offen offset:0 sc1 lds
v_mfma_f32_16x16x32_bf16 v[200:203], a[64:67], v[144:147], 0
v_exp_f32 v223, v223
v_mfma_f32_16x16x32_bf16 v[200:203], a[68:71], v[148:151], v[200:203]
v_cvt_pk_bf16_f32 v239, v222, v223
s_add_u32 s89, s89, 16384
v_mfma_f32_16x16x32_bf16 v[200:203], a[72:75], v[152:155], v[200:203]
v_exp_f32 v192, v192
v_mfma_f32_16x16x32_bf16 v[200:203], a[76:79], v[156:159], v[200:203]
s_waitcnt lgkmcnt(0)
s_and_b32 s89, s89, 0x1ffff
buffer_load_dwordx4 a[52:55], v255, s[52:55], s97 offen offset:1024 sc1
v_mfma_f32_16x16x32_bf16 v[196:199], a[80:83], v[128:131], 0
v_exp_f32 v193, v193
v_mfma_f32_16x16x32_bf16 v[196:199], a[84:87], v[132:135], v[196:199]
v_cvt_pk_bf16_f32 v224, v192, v193
v_mfma_f32_16x16x32_bf16 v[196:199], a[88:91], v[136:139], v[196:199]
v_exp_f32 v194, v194
v_mfma_f32_16x16x32_bf16 v[196:199], a[92:95], v[140:143], v[196:199]
buffer_load_dwordx4 v255, s[60:63], s17 offen offset:1024 sc1 lds
v_mfma_f32_16x16x32_bf16 v[204:207], a[80:83], v[144:147], 0
v_exp_f32 v195, v195
v_mfma_f32_16x16x32_bf16 v[204:207], a[84:87], v[148:151], v[204:207]
v_cvt_pk_bf16_f32 v225, v194, v195
v_mfma_f32_16x16x32_bf16 v[204:207], a[88:91], v[152:155], v[204:207]
v_exp_f32 v200, v200
v_mfma_f32_16x16x32_bf16 v[204:207], a[92:95], v[156:159], v[204:207]
buffer_load_dwordx4 v255, s[60:63], s17 offen offset:2048 sc1 lds
v_mfma_f32_16x16x32_bf16 v[208:211], a[64:67], v[160:163], 0
v_exp_f32 v201, v201
v_mfma_f32_16x16x32_bf16 v[208:211], a[68:71], v[164:167], v[208:211]
v_cvt_pk_bf16_f32 v228, v200, v201
v_mfma_f32_16x16x32_bf16 v[208:211], a[72:75], v[168:171], v[208:211]
v_exp_f32 v202, v202
v_mfma_f32_16x16x32_bf16 v[208:211], a[76:79], v[172:175], v[208:211]
buffer_load_dwordx4 v255, s[60:63], s17 offen offset:3072 sc1 lds
v_mfma_f32_16x16x32_bf16 v[216:219], a[64:67], v[176:179], 0
v_exp_f32 v203, v203
v_mfma_f32_16x16x32_bf16 v[216:219], a[68:71], v[180:183], v[216:219]
v_cvt_pk_bf16_f32 v229, v202, v203
v_mfma_f32_16x16x32_bf16 v[216:219], a[72:75], v[184:187], v[216:219]
v_exp_f32 v196, v196
v_mfma_f32_16x16x32_bf16 v[216:219], a[76:79], v[188:191], v[216:219]
buffer_load_dwordx4 a[56:59], v255, s[52:55], s97 offen offset:2048 sc1
v_mfma_f32_16x16x32_bf16 v[212:215], a[80:83], v[160:163], 0
v_exp_f32 v197, v197
v_mfma_f32_16x16x32_bf16 v[212:215], a[84:87], v[164:167], v[212:215]
v_cvt_pk_bf16_f32 v226, v196, v197
s_add_u32 s17, s17, 8192
v_mfma_f32_16x16x32_bf16 v[212:215], a[88:91], v[168:171], v[212:215]
v_exp_f32 v198, v198
v_mfma_f32_16x16x32_bf16 v[212:215], a[92:95], v[172:175], v[212:215]
s_add_u32 m0, m0, 16384
s_and_b32 m0, m0, s78
buffer_load_dwordx4 a[60:63], v255, s[52:55], s97 offen offset:3072 sc1
v_mfma_f32_16x16x32_bf16 v[220:223], a[80:83], v[176:179], 0
v_exp_f32 v199, v199
v_mfma_f32_16x16x32_bf16 v[220:223], a[84:87], v[180:183], v[220:223]
v_cvt_pk_bf16_f32 v227, v198, v199
v_mfma_f32_16x16x32_bf16 v[220:223], a[88:91], v[184:187], v[220:223]
v_exp_f32 v204, v204
v_mfma_f32_16x16x32_bf16 v[220:223], a[92:95], v[188:191], v[220:223]
s_waitcnt vmcnt(24)
s_barrier
v_mfma_f32_16x16x32_bf16 v[64:67], a[96:99], v[232:235], v[64:67]
v_exp_f32 v205, v205
v_mfma_f32_16x16x32_bf16 v[96:99], a[96:99], v[236:239], v[96:99]
ds_read_b128 a[96:99], v248 offset:8192
v_cvt_pk_bf16_f32 v230, v204, v205
v_mfma_f32_16x16x32_bf16 v[68:71], a[100:103], v[232:235], v[68:71]
v_exp_f32 v206, v206
v_mfma_f32_16x16x32_bf16 v[100:103], a[100:103], v[236:239], v[100:103]
ds_read_b128 a[100:103], v248 offset:9216
v_mfma_f32_16x16x32_bf16 v[72:75], a[104:107], v[232:235], v[72:75]
v_exp_f32 v207, v207
v_mfma_f32_16x16x32_bf16 v[104:107], a[104:107], v[236:239], v[104:107]
ds_read_b128 a[104:107], v248 offset:10240
v_cvt_pk_bf16_f32 v231, v206, v207
v_mfma_f32_16x16x32_bf16 v[76:79], a[108:111], v[232:235], v[76:79]
v_exp_f32 v208, v208
v_mfma_f32_16x16x32_bf16 v[108:111], a[108:111], v[236:239], v[108:111]
ds_read_b128 a[108:111], v248 offset:11264
v_mfma_f32_16x16x32_bf16 v[80:83], a[112:115], v[232:235], v[80:83]
v_exp_f32 v209, v209
v_mfma_f32_16x16x32_bf16 v[112:115], a[112:115], v[236:239], v[112:115]
ds_read_b128 a[112:115], v248 offset:12288
v_mfma_f32_16x16x32_bf16 v[84:87], a[116:119], v[232:235], v[84:87]
v_exp_f32 v210, v210
v_mfma_f32_16x16x32_bf16 v[116:119], a[116:119], v[236:239], v[116:119]
ds_read_b128 a[116:119], v248 offset:13312
v_mfma_f32_16x16x32_bf16 v[88:91], a[120:123], v[232:235], v[88:91]
v_exp_f32 v211, v211
v_mfma_f32_16x16x32_bf16 v[120:123], a[120:123], v[236:239], v[120:123]
ds_read_b128 a[120:123], v248 offset:14336
s_bitcmp1_b64 s[98:99], s85
v_mfma_f32_16x16x32_bf16 v[92:95], a[124:127], v[232:235], v[92:95]
v_exp_f32 v216, v216
v_mfma_f32_16x16x32_bf16 v[124:127], a[124:127], v[236:239], v[124:127]
ds_read_b128 a[124:127], v248 offset:15360
v_or_b32 v248, s89, v255
v_smfmac_f32_16x16x64_bf16 a[196:199], a[208:211], v[232:239], v241
s_waitcnt lgkmcnt(4)
v_smfmac_f32_16x16x64_bf16 a[192:195], a[208:211], v[224:231], v241
ds_read_b128 a[64:67], v248 offset:0
v_mfma_f32_16x16x32_bf16 v[0:3], a[96:99], v[224:227], v[0:3]
v_exp_f32 v217, v217
v_mfma_f32_16x16x32_bf16 v[32:35], a[96:99], v[228:231], v[32:35]
ds_read_b128 a[68:71], v248 offset:1024
v_cvt_pk_bf16_f32 v232, v208, v209
v_mfma_f32_16x16x32_bf16 v[4:7], a[100:103], v[224:227], v[4:7]
v_exp_f32 v218, v218
v_mfma_f32_16x16x32_bf16 v[36:39], a[100:103], v[228:231], v[36:39]
ds_read_b128 a[72:75], v248 offset:2048
v_cvt_pk_bf16_f32 v233, v210, v211
v_mfma_f32_16x16x32_bf16 v[8:11], a[104:107], v[224:227], v[8:11]
v_exp_f32 v219, v219
v_mfma_f32_16x16x32_bf16 v[40:43], a[104:107], v[228:231], v[40:43]
ds_read_b128 a[76:79], v248 offset:3072
v_cvt_pk_bf16_f32 v236, v216, v217
v_mfma_f32_16x16x32_bf16 v[12:15], a[108:111], v[224:227], v[12:15]
v_exp_f32 v212, v212
v_mfma_f32_16x16x32_bf16 v[44:47], a[108:111], v[228:231], v[44:47]
ds_read_b128 a[80:83], v248 offset:4096
s_waitcnt lgkmcnt(5)
v_mfma_f32_16x16x32_bf16 v[16:19], a[112:115], v[224:227], v[16:19]
v_exp_f32 v213, v213
v_mfma_f32_16x16x32_bf16 v[48:51], a[112:115], v[228:231], v[48:51]
ds_read_b128 a[84:87], v248 offset:5120
v_cvt_pk_bf16_f32 v237, v218, v219
v_mfma_f32_16x16x32_bf16 v[20:23], a[116:119], v[224:227], v[20:23]
v_exp_f32 v214, v214
v_mfma_f32_16x16x32_bf16 v[52:55], a[116:119], v[228:231], v[52:55]
ds_read_b128 a[88:91], v248 offset:6144
v_cvt_pk_bf16_f32 v234, v212, v213
v_mfma_f32_16x16x32_bf16 v[24:27], a[120:123], v[224:227], v[24:27]
v_exp_f32 v215, v215
v_mfma_f32_16x16x32_bf16 v[56:59], a[120:123], v[228:231], v[56:59]
ds_read_b128 a[92:95], v248 offset:7168
v_cvt_pk_bf16_f32 v235, v214, v215
v_mfma_f32_16x16x32_bf16 v[28:31], a[124:127], v[224:227], v[28:31]
v_exp_f32_e64 v220, v220
v_mfma_f32_16x16x32_bf16 v[60:63], a[124:127], v[228:231], v[60:63]
.La5_LA_F1Y:
.La5_LW2_4:
s_cbranch_execz .La5_LW2_8
s_waitcnt lgkmcnt(4)
s_waitcnt vmcnt(0)
v_mfma_f32_16x16x32_bf16 v[192:195], a[64:67], a[0:3], 0
v_exp_f32 v221, v221
v_mfma_f32_16x16x32_bf16 v[192:195], a[68:71], a[4:7], v[192:195]
v_cvt_pk_bf16_f32 v238, v220, v221
s_add_u32 s89, s89, 16384
v_mfma_f32_16x16x32_bf16 v[192:195], a[72:75], a[8:11], v[192:195]
v_exp_f32 v222, v222
v_mfma_f32_16x16x32_bf16 v[192:195], a[76:79], a[12:15], v[192:195]
buffer_load_dwordx4 v255, s[60:63], s17 offen offset:0 sc1 lds
v_accvgpr_read_b32 v242, a192
v_accvgpr_read_b32 v240, a193
v_add_f32 v242, v242, v240
v_mfma_f32_16x16x32_bf16 v[200:203], a[64:67], a[16:19], 0
v_exp_f32 v223, v223
v_accvgpr_read_b32 v243, a194
v_accvgpr_read_b32 v240, a195
v_add_f32 v243, v243, v240
v_mfma_f32_16x16x32_bf16 v[200:203], a[68:71], a[20:23], v[200:203]
v_cvt_pk_bf16_f32 v239, v222, v223
s_and_b32 s89, s89, 0x1ffff
v_accvgpr_write_b32 a192, 0
v_accvgpr_write_b32 a193, 0
v_accvgpr_write_b32 a194, 0
v_mfma_f32_16x16x32_bf16 v[200:203], a[72:75], a[24:27], v[200:203]
v_exp_f32 v192, v192
v_accvgpr_write_b32 a195, 0
v_subrev_u32 v240, 562036736, v242
v_mfma_f32_16x16x32_bf16 v[200:203], a[76:79], a[28:31], v[200:203]
s_waitcnt lgkmcnt(0)
v_rcp_f32 v242, v242
v_cmp_le_u32 vcc, 1006632960, v240
v_subrev_u32 v240, 562036736, v243
v_mfma_f32_16x16x32_bf16 v[196:199], a[80:83], a[0:3], 0
v_exp_f32 v193, v193
s_mov_b64 s[92:93], vcc
v_cmp_le_u32 vcc, 1006632960, v240
v_mfma_f32_16x16x32_bf16 v[196:199], a[84:87], a[4:7], v[196:199]
v_cvt_pk_bf16_f32 v224, v192, v193
v_rcp_f32 v243, v243
v_mfma_f32_16x16x32_bf16 v[196:199], a[88:91], a[8:11], v[196:199]
v_exp_f32 v194, v194
s_or_b64 vcc, vcc, s[92:93]
s_cbranch_scc1 .La5_LA_SC1
.La5_LA_SR1:
v_mul_f32 v0, v0, v242
v_mfma_f32_16x16x32_bf16 v[196:199], a[92:95], a[12:15], v[196:199]
buffer_load_dwordx4 v255, s[60:63], s17 offen offset:1024 sc1 lds
v_mul_f32 v1, v1, v242
v_mul_f32 v2, v2, v242
v_mul_f32 v3, v3, v242
v_mfma_f32_16x16x32_bf16 v[204:207], a[80:83], a[16:19], 0
v_exp_f32 v195, v195
v_mul_f32 v4, v4, v242
v_mul_f32 v5, v5, v242
v_mul_f32 v6, v6, v242
v_mfma_f32_16x16x32_bf16 v[204:207], a[84:87], a[20:23], v[204:207]
v_cvt_pk_bf16_f32 v225, v194, v195
v_mul_f32 v7, v7, v242
v_cvt_pk_bf16_f32 v128, v0, v1
v_cvt_pk_bf16_f32 v129, v2, v3
v_cvt_pk_bf16_f32 v130, v4, v5
v_mfma_f32_16x16x32_bf16 v[204:207], a[88:91], a[24:27], v[204:207]
v_exp_f32 v200, v200
v_cvt_pk_bf16_f32 v131, v6, v7
v_mul_f32 v8, v8, v242
v_mul_f32 v9, v9, v242
v_mfma_f32_16x16x32_bf16 v[204:207], a[92:95], a[28:31], v[204:207]
buffer_load_dwordx4 v255, s[60:63], s17 offen offset:2048 sc1 lds
v_mul_f32 v10, v10, v242
v_mul_f32 v11, v11, v242
v_mul_f32 v12, v12, v242
v_mfma_f32_16x16x32_bf16 v[208:211], a[64:67], a[32:35], 0
v_exp_f32 v201, v201
v_mul_f32 v13, v13, v242
v_mul_f32 v14, v14, v242
v_mul_f32 v15, v15, v242
v_mfma_f32_16x16x32_bf16 v[208:211], a[68:71], a[36:39], v[208:211]
v_cvt_pk_bf16_f32 v228, v200, v201
v_cvt_pk_bf16_f32 v132, v8, v9
v_cvt_pk_bf16_f32 v133, v10, v11
v_cvt_pk_bf16_f32 v134, v12, v13
v_cvt_pk_bf16_f32 v135, v14, v15
v_mfma_f32_16x16x32_bf16 v[208:211], a[72:75], a[40:43], v[208:211]
v_exp_f32 v202, v202
v_mul_f32 v16, v16, v242
v_mul_f32 v17, v17, v242
v_mul_f32 v18, v18, v242
v_mfma_f32_16x16x32_bf16 v[208:211], a[76:79], a[44:47], v[208:211]
buffer_load_dwordx4 v255, s[60:63], s17 offen offset:3072 sc1 lds
v_mul_f32 v19, v19, v242
v_mul_f32 v20, v20, v242
v_mul_f32 v21, v21, v242
v_mfma_f32_16x16x32_bf16 v[216:219], a[64:67], a[48:51], 0
v_exp_f32 v203, v203
v_mul_f32 v22, v22, v242
v_mul_f32 v23, v23, v242
v_cvt_pk_bf16_f32 v136, v16, v17
v_mfma_f32_16x16x32_bf16 v[216:219], a[68:71], a[52:55], v[216:219]
v_cvt_pk_bf16_f32 v229, v202, v203
v_cvt_pk_bf16_f32 v137, v18, v19
v_cvt_pk_bf16_f32 v138, v20, v21
v_cvt_pk_bf16_f32 v139, v22, v23
v_mul_f32 v24, v24, v242
v_mfma_f32_16x16x32_bf16 v[216:219], a[72:75], a[56:59], v[216:219]
v_exp_f32 v196, v196
v_mul_f32 v25, v25, v242
v_mul_f32 v26, v26, v242
v_mul_f32 v27, v27, v242
v_mfma_f32_16x16x32_bf16 v[216:219], a[76:79], a[60:63], v[216:219]
v_mul_f32 v28, v28, v242
v_mul_f32 v29, v29, v242
v_mul_f32 v30, v30, v242
v_mul_f32 v31, v31, v242
v_cvt_pk_bf16_f32 v140, v24, v25
v_mfma_f32_16x16x32_bf16 v[212:215], a[80:83], a[32:35], 0
v_exp_f32 v197, v197
s_add_u32 s17, s17, 8192
v_cvt_pk_bf16_f32 v141, v26, v27
v_cvt_pk_bf16_f32 v142, v28, v29
v_mfma_f32_16x16x32_bf16 v[212:215], a[84:87], a[36:39], v[212:215]
v_cvt_pk_bf16_f32 v226, v196, v197
s_add_u32 m0, m0, 16384
s_and_b32 m0, m0, s78
v_cvt_pk_bf16_f32 v143, v30, v31
v_mul_f32 v32, v32, v243
v_mfma_f32_16x16x32_bf16 v[212:215], a[88:91], a[40:43], v[212:215]
v_exp_f32 v198, v198
v_mul_f32 v33, v33, v243
v_mul_f32 v34, v34, v243
v_mul_f32 v35, v35, v243
v_mfma_f32_16x16x32_bf16 v[212:215], a[92:95], a[44:47], v[212:215]
buffer_store_dwordx4 v[128:131], v244, s[44:47], s91 offen offset:0 sc1
v_mul_f32 v36, v36, v243
v_mul_f32 v37, v37, v243
v_mul_f32 v38, v38, v243
v_mfma_f32_16x16x32_bf16 v[220:223], a[80:83], a[48:51], 0
v_exp_f32 v199, v199
v_mul_f32 v39, v39, v243
v_cvt_pk_bf16_f32 v144, v32, v33
v_cvt_pk_bf16_f32 v145, v34, v35
v_mfma_f32_16x16x32_bf16 v[220:223], a[84:87], a[52:55], v[220:223]
v_cvt_pk_bf16_f32 v227, v198, v199
v_cvt_pk_bf16_f32 v146, v36, v37
v_cvt_pk_bf16_f32 v147, v38, v39
v_mul_f32 v40, v40, v243
v_mul_f32 v41, v41, v243
v_mfma_f32_16x16x32_bf16 v[220:223], a[88:91], a[56:59], v[220:223]
v_exp_f32 v204, v204
v_mul_f32 v42, v42, v243
v_mul_f32 v43, v43, v243
v_mul_f32 v44, v44, v243
v_mfma_f32_16x16x32_bf16 v[220:223], a[92:95], a[60:63], v[220:223]
s_nop 0
s_barrier
v_mul_f32 v45, v45, v243
v_mul_f32 v46, v46, v243
v_mul_f32 v47, v47, v243
v_mfma_f32_16x16x32_bf16 v[64:67], a[96:99], v[232:235], v[64:67]
v_exp_f32 v205, v205
v_cvt_pk_bf16_f32 v148, v40, v41
v_cvt_pk_bf16_f32 v149, v42, v43
v_cvt_pk_bf16_f32 v150, v44, v45
v_mfma_f32_16x16x32_bf16 v[96:99], a[96:99], v[236:239], v[96:99]
ds_read_b128 a[96:99], v248 offset:8192
v_cvt_pk_bf16_f32 v230, v204, v205
v_cvt_pk_bf16_f32 v151, v46, v47
v_mul_f32 v48, v48, v243
v_mul_f32 v49, v49, v243
v_mfma_f32_16x16x32_bf16 v[68:71], a[100:103], v[232:235], v[68:71]
v_exp_f32 v206, v206
v_mul_f32 v50, v50, v243
v_mul_f32 v51, v51, v243
v_mul_f32 v52, v52, v243
v_mfma_f32_16x16x32_bf16 v[100:103], a[100:103], v[236:239], v[100:103]
ds_read_b128 a[100:103], v248 offset:9216
buffer_store_dwordx4 v[132:135], v244, s[44:47], s91 offen offset:1024 sc1
v_mul_f32 v53, v53, v243
v_mul_f32 v54, v54, v243
v_mfma_f32_16x16x32_bf16 v[72:75], a[104:107], v[232:235], v[72:75]
v_exp_f32 v207, v207
v_mul_f32 v55, v55, v243
v_cvt_pk_bf16_f32 v152, v48, v49
v_cvt_pk_bf16_f32 v153, v50, v51
v_mfma_f32_16x16x32_bf16 v[104:107], a[104:107], v[236:239], v[104:107]
ds_read_b128 a[104:107], v248 offset:10240
v_cvt_pk_bf16_f32 v231, v206, v207
v_cvt_pk_bf16_f32 v154, v52, v53
v_cvt_pk_bf16_f32 v155, v54, v55
v_mul_f32 v56, v56, v243
v_mfma_f32_16x16x32_bf16 v[76:79], a[108:111], v[232:235], v[76:79]
v_exp_f32 v208, v208
v_mul_f32 v57, v57, v243
v_mul_f32 v58, v58, v243
v_mul_f32 v59, v59, v243
v_mfma_f32_16x16x32_bf16 v[108:111], a[108:111], v[236:239], v[108:111]
ds_read_b128 a[108:111], v248 offset:11264
v_mul_f32 v60, v60, v243
v_mul_f32 v61, v61, v243
v_mul_f32 v62, v62, v243
v_mul_f32 v63, v63, v243
v_mfma_f32_16x16x32_bf16 v[80:83], a[112:115], v[232:235], v[80:83]
v_exp_f32 v209, v209
v_cvt_pk_bf16_f32 v156, v56, v57
v_cvt_pk_bf16_f32 v157, v58, v59
v_cvt_pk_bf16_f32 v158, v60, v61
v_mfma_f32_16x16x32_bf16 v[112:115], a[112:115], v[236:239], v[112:115]
ds_read_b128 a[112:115], v248 offset:12288
buffer_store_dwordx4 v[136:139], v244, s[44:47], s91 offen offset:2048 sc1
v_cvt_pk_bf16_f32 v159, v62, v63
v_mfma_f32_16x16x32_bf16 v[84:87], a[116:119], v[232:235], v[84:87]
v_exp_f32 v210, v210
v_mfma_f32_16x16x32_bf16 v[116:119], a[116:119], v[236:239], v[116:119]
ds_read_b128 a[116:119], v248 offset:13312
v_mfma_f32_16x16x32_bf16 v[88:91], a[120:123], v[232:235], v[88:91]
v_exp_f32 v211, v211
v_mfma_f32_16x16x32_bf16 v[120:123], a[120:123], v[236:239], v[120:123]
ds_read_b128 a[120:123], v248 offset:14336
buffer_store_dwordx4 v[140:143], v244, s[44:47], s91 offen offset:3072 sc1
v_mfma_f32_16x16x32_bf16 v[92:95], a[124:127], v[232:235], v[92:95]
v_exp_f32 v216, v216
v_mfma_f32_16x16x32_bf16 v[124:127], a[124:127], v[236:239], v[124:127]
ds_read_b128 a[124:127], v248 offset:15360
v_or_b32 v248, s89, v255
v_smfmac_f32_16x16x64_bf16 a[196:199], a[208:211], v[232:239], v241
s_waitcnt lgkmcnt(4)
v_smfmac_f32_16x16x64_bf16 a[192:195], a[208:211], v[224:231], v241
ds_read_b128 a[64:67], v248 offset:0
v_mfma_f32_16x16x32_bf16 v[0:3], a[96:99], v[224:227], 0
v_exp_f32 v217, v217
v_mfma_f32_16x16x32_bf16 v[32:35], a[96:99], v[228:231], 0
ds_read_b128 a[68:71], v248 offset:1024
v_cvt_pk_bf16_f32 v232, v208, v209
v_accvgpr_read_b32 v242, a196
v_accvgpr_read_b32 v240, a197
v_add_f32 v242, v242, v240
v_mfma_f32_16x16x32_bf16 v[4:7], a[100:103], v[224:227], 0
v_exp_f32 v218, v218
v_accvgpr_read_b32 v243, a198
v_accvgpr_read_b32 v240, a199
v_add_f32 v243, v243, v240
v_mfma_f32_16x16x32_bf16 v[36:39], a[100:103], v[228:231], 0
ds_read_b128 a[72:75], v248 offset:2048
v_cvt_pk_bf16_f32 v233, v210, v211
buffer_store_dwordx4 v[144:147], v245, s[44:47], s91 offen offset:0 sc1
v_accvgpr_write_b32 a196, 0
v_mfma_f32_16x16x32_bf16 v[8:11], a[104:107], v[224:227], 0
v_exp_f32 v219, v219
v_accvgpr_write_b32 a197, 0
v_accvgpr_write_b32 a198, 0
v_accvgpr_write_b32 a199, 0
v_mfma_f32_16x16x32_bf16 v[40:43], a[104:107], v[228:231], 0
ds_read_b128 a[76:79], v248 offset:3072
v_cvt_pk_bf16_f32 v236, v216, v217
v_subrev_u32 v240, 562036736, v242
v_rcp_f32 v242, v242
v_mfma_f32_16x16x32_bf16 v[12:15], a[108:111], v[224:227], 0
v_exp_f32 v212, v212
v_cmp_le_u32 vcc, 1006632960, v240
v_subrev_u32 v240, 562036736, v243
s_mov_b64 s[92:93], vcc
v_mfma_f32_16x16x32_bf16 v[44:47], a[108:111], v[228:231], 0
ds_read_b128 a[80:83], v248 offset:4096
s_waitcnt lgkmcnt(5)
v_cmp_le_u32 vcc, 1006632960, v240
v_rcp_f32 v243, v243
v_mfma_f32_16x16x32_bf16 v[16:19], a[112:115], v[224:227], 0
v_exp_f32 v213, v213
s_or_b64 vcc, vcc, s[92:93]
s_cbranch_scc1 .La5_LA_SC2
.La5_LA_SR2:
v_mul_f32 v64, v64, v242
v_mfma_f32_16x16x32_bf16 v[48:51], a[112:115], v[228:231], 0
ds_read_b128 a[84:87], v248 offset:5120
v_cvt_pk_bf16_f32 v237, v218, v219
v_mul_f32 v65, v65, v242
v_mul_f32 v66, v66, v242
v_mul_f32 v67, v67, v242
v_mfma_f32_16x16x32_bf16 v[20:23], a[116:119], v[224:227], 0
v_exp_f32 v214, v214
v_mul_f32 v68, v68, v242
v_mul_f32 v69, v69, v242
v_mul_f32 v70, v70, v242
v_mfma_f32_16x16x32_bf16 v[52:55], a[116:119], v[228:231], 0
ds_read_b128 a[88:91], v248 offset:6144
v_cvt_pk_bf16_f32 v234, v212, v213
buffer_store_dwordx4 v[148:151], v245, s[44:47], s91 offen offset:1024 sc1
v_mul_f32 v71, v71, v242
v_mfma_f32_16x16x32_bf16 v[24:27], a[120:123], v[224:227], 0
v_exp_f32 v215, v215
v_cvt_pk_bf16_f32 v160, v64, v65
v_cvt_pk_bf16_f32 v161, v66, v67
v_cvt_pk_bf16_f32 v162, v68, v69
v_mfma_f32_16x16x32_bf16 v[56:59], a[120:123], v[228:231], 0
ds_read_b128 a[92:95], v248 offset:7168
v_cvt_pk_bf16_f32 v235, v214, v215
v_cvt_pk_bf16_f32 v163, v70, v71
v_mul_f32 v72, v72, v242
v_mul_f32 v73, v73, v242
v_mfma_f32_16x16x32_bf16 v[28:31], a[124:127], v[224:227], 0
v_exp_f32 v220, v220
v_mul_f32 v74, v74, v242
v_mul_f32 v75, v75, v242
v_mul_f32 v76, v76, v242
v_mfma_f32_16x16x32_bf16 v[60:63], a[124:127], v[228:231], 0
s_add_u32 s85, s85, 1
v_mul_f32 v77, v77, v242
v_mul_f32 v78, v78, v242
v_mul_f32 v79, v79, v242
v_cvt_pk_bf16_f32 v164, v72, v73
s_waitcnt lgkmcnt(4)
v_mfma_f32_16x16x32_bf16 v[192:195], a[64:67], a[0:3], 0
v_exp_f32 v221, v221
v_cvt_pk_bf16_f32 v165, v74, v75
.La5_LW2_5:
s_cbranch_execz .La5_LW2_9
v_cvt_pk_bf16_f32 v166, v76, v77
v_cvt_pk_bf16_f32 v167, v78, v79
v_mfma_f32_16x16x32_bf16 v[192:195], a[68:71], a[4:7], v[192:195]
v_cvt_pk_bf16_f32 v238, v220, v221
s_add_u32 s89, s89, 16384
v_mul_f32 v80, v80, v242
v_mul_f32 v81, v81, v242
v_mul_f32 v82, v82, v242
v_mfma_f32_16x16x32_bf16 v[192:195], a[72:75], a[8:11], v[192:195]
v_exp_f32 v222, v222
v_mul_f32 v83, v83, v242
v_mul_f32 v84, v84, v242
v_mul_f32 v85, v85, v242
v_mfma_f32_16x16x32_bf16 v[192:195], a[76:79], a[12:15], v[192:195]
buffer_load_dwordx4 v255, s[60:63], s17 offen offset:0 sc1 lds
v_mul_f32 v86, v86, v242
v_mul_f32 v87, v87, v242
v_cvt_pk_bf16_f32 v168, v80, v81
v_mfma_f32_16x16x32_bf16 v[200:203], a[64:67], a[16:19], 0
v_exp_f32 v223, v223
v_cvt_pk_bf16_f32 v169, v82, v83
v_cvt_pk_bf16_f32 v170, v84, v85
v_cvt_pk_bf16_f32 v171, v86, v87
v_mfma_f32_16x16x32_bf16 v[200:203], a[68:71], a[20:23], v[200:203]
v_cvt_pk_bf16_f32 v239, v222, v223
s_and_b32 s89, s89, 0x1ffff
v_mul_f32 v88, v88, v242
v_mul_f32 v89, v89, v242
v_mul_f32 v90, v90, v242
v_mfma_f32_16x16x32_bf16 v[200:203], a[72:75], a[24:27], v[200:203]
v_exp_f32 v192, v192
v_mul_f32 v91, v91, v242
v_mul_f32 v92, v92, v242
v_mul_f32 v93, v93, v242
v_mfma_f32_16x16x32_bf16 v[200:203], a[76:79], a[28:31], v[200:203]
s_waitcnt lgkmcnt(0)
buffer_store_dwordx4 v[152:155], v245, s[44:47], s91 offen offset:2048 sc1
v_mul_f32 v94, v94, v242
v_mul_f32 v95, v95, v242
v_mfma_f32_16x16x32_bf16 v[196:199], a[80:83], a[0:3], 0
v_exp_f32 v193, v193
v_cvt_pk_bf16_f32 v172, v88, v89
v_cvt_pk_bf16_f32 v173, v90, v91
v_cvt_pk_bf16_f32 v174, v92, v93
v_mfma_f32_16x16x32_bf16 v[196:199], a[84:87], a[4:7], v[196:199]
v_cvt_pk_bf16_f32 v224, v192, v193
v_cvt_pk_bf16_f32 v175, v94, v95
v_mul_f32 v96, v96, v243
v_mul_f32 v97, v97, v243
v_mul_f32 v98, v98, v243
v_mfma_f32_16x16x32_bf16 v[196:199], a[88:91], a[8:11], v[196:199]
v_exp_f32 v194, v194
v_mul_f32 v99, v99, v243
v_mul_f32 v100, v100, v243
v_mul_f32 v101, v101, v243
v_mfma_f32_16x16x32_bf16 v[196:199], a[92:95], a[12:15], v[196:199]
buffer_load_dwordx4 v255, s[60:63], s17 offen offset:1024 sc1 lds
v_mul_f32 v102, v102, v243
v_mul_f32 v103, v103, v243
v_cvt_pk_bf16_f32 v176, v96, v97
v_mfma_f32_16x16x32_bf16 v[204:207], a[80:83], a[16:19], 0
v_exp_f32 v195, v195
v_cvt_pk_bf16_f32 v177, v98, v99
v_cvt_pk_bf16_f32 v178, v100, v101
v_cvt_pk_bf16_f32 v179, v102, v103
v_mfma_f32_16x16x32_bf16 v[204:207], a[84:87], a[20:23], v[204:207]
v_cvt_pk_bf16_f32 v225, v194, v195
buffer_store_dwordx4 v[156:159], v245, s[44:47], s91 offen offset:3072 sc1
v_mul_f32 v104, v104, v243
v_mul_f32 v105, v105, v243
v_mfma_f32_16x16x32_bf16 v[204:207], a[88:91], a[24:27], v[204:207]
v_exp_f32 v200, v200
v_mul_f32 v106, v106, v243
v_mul_f32 v107, v107, v243
v_mul_f32 v108, v108, v243
v_mfma_f32_16x16x32_bf16 v[204:207], a[92:95], a[28:31], v[204:207]
buffer_load_dwordx4 v255, s[60:63], s17 offen offset:2048 sc1 lds
v_mul_f32 v109, v109, v243
v_mul_f32 v110, v110, v243
v_mul_f32 v111, v111, v243
v_mfma_f32_16x16x32_bf16 v[208:211], a[64:67], a[32:35], 0
v_exp_f32 v201, v201
v_cvt_pk_bf16_f32 v180, v104, v105
v_cvt_pk_bf16_f32 v181, v106, v107
v_cvt_pk_bf16_f32 v182, v108, v109
v_mfma_f32_16x16x32_bf16 v[208:211], a[68:71], a[36:39], v[208:211]
v_cvt_pk_bf16_f32 v228, v200, v201
v_cvt_pk_bf16_f32 v183, v110, v111
v_mul_f32 v112, v112, v243
v_mul_f32 v113, v113, v243
v_mul_f32 v114, v114, v243
v_mfma_f32_16x16x32_bf16 v[208:211], a[72:75], a[40:43], v[208:211]
v_exp_f32 v202, v202
v_mul_f32 v115, v115, v243
v_mul_f32 v116, v116, v243
v_mul_f32 v117, v117, v243
v_mfma_f32_16x16x32_bf16 v[208:211], a[76:79], a[44:47], v[208:211]
buffer_load_dwordx4 v255, s[60:63], s17 offen offset:3072 sc1 lds
v_mul_f32 v118, v118, v243
v_mul_f32 v119, v119, v243
v_cvt_pk_bf16_f32 v184, v112, v113
v_mfma_f32_16x16x32_bf16 v[216:219], a[64:67], a[48:51], 0
v_exp_f32 v203, v203
v_cvt_pk_bf16_f32 v185, v114, v115
v_cvt_pk_bf16_f32 v186, v116, v117
v_cvt_pk_bf16_f32 v187, v118, v119
v_mfma_f32_16x16x32_bf16 v[216:219], a[68:71], a[52:55], v[216:219]
v_cvt_pk_bf16_f32 v229, v202, v203
v_mul_f32 v120, v120, v243
v_mul_f32 v121, v121, v243
v_mul_f32 v122, v122, v243
v_mul_f32 v123, v123, v243
v_mfma_f32_16x16x32_bf16 v[216:219], a[72:75], a[56:59], v[216:219]
v_exp_f32 v196, v196
v_mul_f32 v124, v124, v243
v_mul_f32 v125, v125, v243
v_mul_f32 v126, v126, v243
v_mfma_f32_16x16x32_bf16 v[216:219], a[76:79], a[60:63], v[216:219]
v_mul_f32 v127, v127, v243
v_cvt_pk_bf16_f32 v188, v120, v121
v_cvt_pk_bf16_f32 v189, v122, v123
v_cvt_pk_bf16_f32 v190, v124, v125
v_cvt_pk_bf16_f32 v191, v126, v127
v_mfma_f32_16x16x32_bf16 v[212:215], a[80:83], a[32:35], 0
v_exp_f32 v197, v197
s_add_u32 s17, s17, 8192
v_mfma_f32_16x16x32_bf16 v[212:215], a[84:87], a[36:39], v[212:215]
v_cvt_pk_bf16_f32 v226, v196, v197
s_add_u32 m0, m0, 16384
s_and_b32 m0, m0, s78
v_mfma_f32_16x16x32_bf16 v[212:215], a[88:91], a[40:43], v[212:215]
v_exp_f32 v198, v198
v_mfma_f32_16x16x32_bf16 v[212:215], a[92:95], a[44:47], v[212:215]
buffer_store_dwordx4 v[160:163], v246, s[44:47], s91 offen offset:0 sc1
v_mfma_f32_16x16x32_bf16 v[220:223], a[80:83], a[48:51], 0
v_exp_f32 v199, v199
v_mfma_f32_16x16x32_bf16 v[220:223], a[84:87], a[52:55], v[220:223]
v_cvt_pk_bf16_f32 v227, v198, v199
v_mfma_f32_16x16x32_bf16 v[220:223], a[88:91], a[56:59], v[220:223]
v_exp_f32 v204, v204
v_mfma_f32_16x16x32_bf16 v[220:223], a[92:95], a[60:63], v[220:223]
s_waitcnt vmcnt(18)
s_barrier
v_mfma_f32_16x16x32_bf16 v[64:67], a[96:99], v[232:235], 0
v_exp_f32 v205, v205
v_mfma_f32_16x16x32_bf16 v[96:99], a[96:99], v[236:239], 0
ds_read_b128 a[96:99], v248 offset:8192
v_cvt_pk_bf16_f32 v230, v204, v205
v_mfma_f32_16x16x32_bf16 v[68:71], a[100:103], v[232:235], 0
v_exp_f32 v206, v206
v_mfma_f32_16x16x32_bf16 v[100:103], a[100:103], v[236:239], 0
ds_read_b128 a[100:103], v248 offset:9216
buffer_store_dwordx4 v[164:167], v246, s[44:47], s91 offen offset:1024 sc1
v_mfma_f32_16x16x32_bf16 v[72:75], a[104:107], v[232:235], 0
v_exp_f32 v207, v207
v_mfma_f32_16x16x32_bf16 v[104:107], a[104:107], v[236:239], 0
ds_read_b128 a[104:107], v248 offset:10240
v_cvt_pk_bf16_f32 v231, v206, v207
v_mfma_f32_16x16x32_bf16 v[76:79], a[108:111], v[232:235], 0
v_exp_f32 v208, v208
v_mfma_f32_16x16x32_bf16 v[108:111], a[108:111], v[236:239], 0
ds_read_b128 a[108:111], v248 offset:11264
buffer_store_dwordx4 v[168:171], v246, s[44:47], s91 offen offset:2048 sc1
v_mfma_f32_16x16x32_bf16 v[80:83], a[112:115], v[232:235], 0
v_exp_f32 v209, v209
v_mfma_f32_16x16x32_bf16 v[112:115], a[112:115], v[236:239], 0
ds_read_b128 a[112:115], v248 offset:12288
v_mfma_f32_16x16x32_bf16 v[84:87], a[116:119], v[232:235], 0
v_exp_f32 v210, v210
v_mfma_f32_16x16x32_bf16 v[116:119], a[116:119], v[236:239], 0
ds_read_b128 a[116:119], v248 offset:13312
buffer_store_dwordx4 v[172:175], v246, s[44:47], s91 offen offset:3072 sc1
v_mfma_f32_16x16x32_bf16 v[88:91], a[120:123], v[232:235], 0
v_exp_f32 v211, v211
v_mfma_f32_16x16x32_bf16 v[120:123], a[120:123], v[236:239], 0
ds_read_b128 a[120:123], v248 offset:14336
v_mfma_f32_16x16x32_bf16 v[92:95], a[124:127], v[232:235], 0
v_exp_f32 v216, v216
v_mfma_f32_16x16x32_bf16 v[124:127], a[124:127], v[236:239], 0
ds_read_b128 a[124:127], v248 offset:15360
v_or_b32 v248, s89, v255
buffer_store_dwordx4 v[176:179], v247, s[44:47], s91 offen offset:0 sc1
v_smfmac_f32_16x16x64_bf16 a[196:199], a[208:211], v[232:239], v241
s_waitcnt lgkmcnt(4)
v_smfmac_f32_16x16x64_bf16 a[192:195], a[208:211], v[224:231], v241
ds_read_b128 a[64:67], v248 offset:0
v_mfma_f32_16x16x32_bf16 v[0:3], a[96:99], v[224:227], v[0:3]
v_exp_f32 v217, v217
v_mfma_f32_16x16x32_bf16 v[32:35], a[96:99], v[228:231], v[32:35]
ds_read_b128 a[68:71], v248 offset:1024
v_cvt_pk_bf16_f32 v232, v208, v209
v_mfma_f32_16x16x32_bf16 v[4:7], a[100:103], v[224:227], v[4:7]
v_exp_f32 v218, v218
v_mfma_f32_16x16x32_bf16 v[36:39], a[100:103], v[228:231], v[36:39]
ds_read_b128 a[72:75], v248 offset:2048
v_cvt_pk_bf16_f32 v233, v210, v211
buffer_store_dwordx4 v[180:183], v247, s[44:47], s91 offen offset:1024 sc1
v_mfma_f32_16x16x32_bf16 v[8:11], a[104:107], v[224:227], v[8:11]
v_exp_f32 v219, v219
v_mfma_f32_16x16x32_bf16 v[40:43], a[104:107], v[228:231], v[40:43]
ds_read_b128 a[76:79], v248 offset:3072
v_cvt_pk_bf16_f32 v236, v216, v217
v_mfma_f32_16x16x32_bf16 v[12:15], a[108:111], v[224:227], v[12:15]
v_exp_f32 v212, v212
v_mfma_f32_16x16x32_bf16 v[44:47], a[108:111], v[228:231], v[44:47]
ds_read_b128 a[80:83], v248 offset:4096
s_waitcnt lgkmcnt(5)
v_mfma_f32_16x16x32_bf16 v[16:19], a[112:115], v[224:227], v[16:19]
v_exp_f32 v213, v213
v_mfma_f32_16x16x32_bf16 v[48:51], a[112:115], v[228:231], v[48:51]
ds_read_b128 a[84:87], v248 offset:5120
v_cvt_pk_bf16_f32 v237, v218, v219
buffer_store_dwordx4 v[184:187], v247, s[44:47], s91 offen offset:2048 sc1
v_mfma_f32_16x16x32_bf16 v[20:23], a[116:119], v[224:227], v[20:23]
v_exp_f32 v214, v214
v_mfma_f32_16x16x32_bf16 v[52:55], a[116:119], v[228:231], v[52:55]
ds_read_b128 a[88:91], v248 offset:6144
v_cvt_pk_bf16_f32 v234, v212, v213
v_mfma_f32_16x16x32_bf16 v[24:27], a[120:123], v[224:227], v[24:27]
v_exp_f32 v215, v215
v_mfma_f32_16x16x32_bf16 v[56:59], a[120:123], v[228:231], v[56:59]
ds_read_b128 a[92:95], v248 offset:7168
v_cvt_pk_bf16_f32 v235, v214, v215
buffer_store_dwordx4 v[188:191], v247, s[44:47], s91 offen offset:3072 sc1
v_mfma_f32_16x16x32_bf16 v[28:31], a[124:127], v[224:227], v[28:31]
v_exp_f32 v220, v220
v_mfma_f32_16x16x32_bf16 v[60:63], a[124:127], v[228:231], v[60:63]
s_add_u32 s85, s85, 1
s_cmp_eq_u32 s85, 22
s_cbranch_scc1 .La5_LA_F3
.p2align 6
.La5_LA_F2:
s_waitcnt lgkmcnt(4)
v_mfma_f32_16x16x32_bf16 v[192:195], a[64:67], a[0:3], 0
v_exp_f32 v221, v221
v_mfma_f32_16x16x32_bf16 v[192:195], a[68:71], a[4:7], v[192:195]
v_cvt_pk_bf16_f32 v238, v220, v221
s_add_u32 s85, s85, 1
v_mfma_f32_16x16x32_bf16 v[192:195], a[72:75], a[8:11], v[192:195]
v_exp_f32 v222, v222
v_mfma_f32_16x16x32_bf16 v[192:195], a[76:79], a[12:15], v[192:195]
buffer_load_dwordx4 v255, s[60:63], s17 offen offset:0 sc1 lds
v_mfma_f32_16x16x32_bf16 v[200:203], a[64:67], a[16:19], 0
v_exp_f32 v223, v223
v_mfma_f32_16x16x32_bf16 v[200:203], a[68:71], a[20:23], v[200:203]
v_cvt_pk_bf16_f32 v239, v222, v223
s_add_u32 s89, s89, 16384
v_mfma_f32_16x16x32_bf16 v[200:203], a[72:75], a[24:27], v[200:203]
v_exp_f32 v192, v192
v_mfma_f32_16x16x32_bf16 v[200:203], a[76:79], a[28:31], v[200:203]
s_waitcnt lgkmcnt(0)
s_and_b32 s89, s89, 0x1ffff
v_mfma_f32_16x16x32_bf16 v[196:199], a[80:83], a[0:3], 0
v_exp_f32 v193, v193
v_mfma_f32_16x16x32_bf16 v[196:199], a[84:87], a[4:7], v[196:199]
v_cvt_pk_bf16_f32 v224, v192, v193
v_mfma_f32_16x16x32_bf16 v[196:199], a[88:91], a[8:11], v[196:199]
v_exp_f32 v194, v194
v_mfma_f32_16x16x32_bf16 v[196:199], a[92:95], a[12:15], v[196:199]
buffer_load_dwordx4 v255, s[60:63], s17 offen offset:1024 sc1 lds
v_mfma_f32_16x16x32_bf16 v[204:207], a[80:83], a[16:19], 0
v_exp_f32 v195, v195
v_mfma_f32_16x16x32_bf16 v[204:207], a[84:87], a[20:23], v[204:207]
v_cvt_pk_bf16_f32 v225, v194, v195
v_mfma_f32_16x16x32_bf16 v[204:207], a[88:91], a[24:27], v[204:207]
v_exp_f32 v200, v200
v_mfma_f32_16x16x32_bf16 v[204:207], a[92:95], a[28:31], v[204:207]
buffer_load_dwordx4 v255, s[60:63], s17 offen offset:2048 sc1 lds
v_mfma_f32_16x16x32_bf16 v[208:211], a[64:67], a[32:35], 0
v_exp_f32 v201, v201
v_mfma_f32_16x16x32_bf16 v[208:211], a[68:71], a[36:39], v[208:211]
v_cvt_pk_bf16_f32 v228, v200, v201
v_mfma_f32_16x16x32_bf16 v[208:211], a[72:75], a[40:43], v[208:211]
v_exp_f32 v202, v202
v_mfma_f32_16x16x32_bf16 v[208:211], a[76:79], a[44:47], v[208:211]
buffer_load_dwordx4 v255, s[60:63], s17 offen offset:3072 sc1 lds
v_mfma_f32_16x16x32_bf16 v[216:219], a[64:67], a[48:51], 0
v_exp_f32 v203, v203
v_mfma_f32_16x16x32_bf16 v[216:219], a[68:71], a[52:55], v[216:219]
v_cvt_pk_bf16_f32 v229, v202, v203
v_mfma_f32_16x16x32_bf16 v[216:219], a[72:75], a[56:59], v[216:219]
v_exp_f32 v196, v196
v_mfma_f32_16x16x32_bf16 v[216:219], a[76:79], a[60:63], v[216:219]
buffer_load_dword a253, v254, s[36:39], s90 offen
v_mfma_f32_16x16x32_bf16 v[212:215], a[80:83], a[32:35], 0
v_exp_f32 v197, v197
v_mfma_f32_16x16x32_bf16 v[212:215], a[84:87], a[36:39], v[212:215]
v_cvt_pk_bf16_f32 v226, v196, v197
s_add_u32 s17, s17, 8192
v_mfma_f32_16x16x32_bf16 v[212:215], a[88:91], a[40:43], v[212:215]
v_exp_f32 v198, v198
v_mfma_f32_16x16x32_bf16 v[212:215], a[92:95], a[44:47], v[212:215]
s_add_u32 m0, m0, 16384
s_and_b32 m0, m0, s78
v_mfma_f32_16x16x32_bf16 v[220:223], a[80:83], a[48:51], 0
v_exp_f32 v199, v199
v_mfma_f32_16x16x32_bf16 v[220:223], a[84:87], a[52:55], v[220:223]
v_cvt_pk_bf16_f32 v227, v198, v199
s_add_u32 s90, s90, s21
v_mfma_f32_16x16x32_bf16 v[220:223], a[88:91], a[56:59], v[220:223]
v_exp_f32 v204, v204
v_mfma_f32_16x16x32_bf16 v[220:223], a[92:95], a[60:63], v[220:223]
s_waitcnt vmcnt(11)
s_barrier
v_mfma_f32_16x16x32_bf16 v[64:67], a[96:99], v[232:235], v[64:67]
v_exp_f32 v205, v205
v_mfma_f32_16x16x32_bf16 v[96:99], a[96:99], v[236:239], v[96:99]
ds_read_b128 a[96:99], v248 offset:8192
v_cvt_pk_bf16_f32 v230, v204, v205
v_mfma_f32_16x16x32_bf16 v[68:71], a[100:103], v[232:235], v[68:71]
v_exp_f32 v206, v206
v_mfma_f32_16x16x32_bf16 v[100:103], a[100:103], v[236:239], v[100:103]
ds_read_b128 a[100:103], v248 offset:9216
v_mfma_f32_16x16x32_bf16 v[72:75], a[104:107], v[232:235], v[72:75]
v_exp_f32 v207, v207
v_mfma_f32_16x16x32_bf16 v[104:107], a[104:107], v[236:239], v[104:107]
ds_read_b128 a[104:107], v248 offset:10240
v_cvt_pk_bf16_f32 v231, v206, v207
v_mfma_f32_16x16x32_bf16 v[76:79], a[108:111], v[232:235], v[76:79]
v_exp_f32 v208, v208
v_mfma_f32_16x16x32_bf16 v[108:111], a[108:111], v[236:239], v[108:111]
ds_read_b128 a[108:111], v248 offset:11264
v_mfma_f32_16x16x32_bf16 v[80:83], a[112:115], v[232:235], v[80:83]
v_exp_f32 v209, v209
v_mfma_f32_16x16x32_bf16 v[112:115], a[112:115], v[236:239], v[112:115]
ds_read_b128 a[112:115], v248 offset:12288
v_mfma_f32_16x16x32_bf16 v[84:87], a[116:119], v[232:235], v[84:87]
v_exp_f32 v210, v210
v_mfma_f32_16x16x32_bf16 v[116:119], a[116:119], v[236:239], v[116:119]
ds_read_b128 a[116:119], v248 offset:13312
v_mfma_f32_16x16x32_bf16 v[88:91], a[120:123], v[232:235], v[88:91]
v_exp_f32 v211, v211
v_mfma_f32_16x16x32_bf16 v[120:123], a[120:123], v[236:239], v[120:123]
ds_read_b128 a[120:123], v248 offset:14336
s_bitcmp1_b64 s[98:99], s85
v_mfma_f32_16x16x32_bf16 v[92:95], a[124:127], v[232:235], v[92:95]
v_exp_f32 v216, v216
v_mfma_f32_16x16x32_bf16 v[124:127], a[124:127], v[236:239], v[124:127]
ds_read_b128 a[124:127], v248 offset:15360
v_or_b32 v248, s89, v255
v_smfmac_f32_16x16x64_bf16 a[196:199], a[208:211], v[232:239], v241
s_waitcnt lgkmcnt(4)
v_smfmac_f32_16x16x64_bf16 a[192:195], a[208:211], v[224:231], v241
ds_read_b128 a[64:67], v248 offset:0
v_mfma_f32_16x16x32_bf16 v[0:3], a[96:99], v[224:227], v[0:3]
v_exp_f32 v217, v217
v_mfma_f32_16x16x32_bf16 v[32:35], a[96:99], v[228:231], v[32:35]
ds_read_b128 a[68:71], v248 offset:1024
v_cvt_pk_bf16_f32 v232, v208, v209
v_mfma_f32_16x16x32_bf16 v[4:7], a[100:103], v[224:227], v[4:7]
v_exp_f32 v218, v218
v_mfma_f32_16x16x32_bf16 v[36:39], a[100:103], v[228:231], v[36:39]
ds_read_b128 a[72:75], v248 offset:2048
v_cvt_pk_bf16_f32 v233, v210, v211
v_mfma_f32_16x16x32_bf16 v[8:11], a[104:107], v[224:227], v[8:11]
v_exp_f32 v219, v219
v_mfma_f32_16x16x32_bf16 v[40:43], a[104:107], v[228:231], v[40:43]
ds_read_b128 a[76:79], v248 offset:3072
v_cvt_pk_bf16_f32 v236, v216, v217
v_mfma_f32_16x16x32_bf16 v[12:15], a[108:111], v[224:227], v[12:15]
v_exp_f32 v212, v212
v_mfma_f32_16x16x32_bf16 v[44:47], a[108:111], v[228:231], v[44:47]
ds_read_b128 a[80:83], v248 offset:4096
s_waitcnt lgkmcnt(5)
v_mfma_f32_16x16x32_bf16 v[16:19], a[112:115], v[224:227], v[16:19]
v_exp_f32 v213, v213
v_mfma_f32_16x16x32_bf16 v[48:51], a[112:115], v[228:231], v[48:51]
ds_read_b128 a[84:87], v248 offset:5120
v_cvt_pk_bf16_f32 v237, v218, v219
v_mfma_f32_16x16x32_bf16 v[20:23], a[116:119], v[224:227], v[20:23]
v_exp_f32 v214, v214
v_mfma_f32_16x16x32_bf16 v[52:55], a[116:119], v[228:231], v[52:55]
ds_read_b128 a[88:91], v248 offset:6144
v_cvt_pk_bf16_f32 v234, v212, v213
v_mfma_f32_16x16x32_bf16 v[24:27], a[120:123], v[224:227], v[24:27]
v_exp_f32 v215, v215
v_mfma_f32_16x16x32_bf16 v[56:59], a[120:123], v[228:231], v[56:59]
ds_read_b128 a[92:95], v248 offset:7168
v_cvt_pk_bf16_f32 v235, v214, v215
v_mfma_f32_16x16x32_bf16 v[28:31], a[124:127], v[224:227], v[28:31]
v_exp_f32 v220, v220
v_mfma_f32_16x16x32_bf16 v[60:63], a[124:127], v[228:231], v[60:63]
s_cbranch_scc1 .La5_LA_F2E2
s_waitcnt lgkmcnt(4)
v_mfma_f32_16x16x32_bf16 v[192:195], a[64:67], a[0:3], 0
v_exp_f32 v221, v221
v_mfma_f32_16x16x32_bf16 v[192:195], a[68:71], a[4:7], v[192:195]
v_cvt_pk_bf16_f32 v238, v220, v221
s_add_u32 s85, s85, 1
v_mfma_f32_16x16x32_bf16 v[192:195], a[72:75], a[8:11], v[192:195]
v_exp_f32 v222, v222
v_mfma_f32_16x16x32_bf16 v[192:195], a[76:79], a[12:15], v[192:195]
buffer_load_dwordx4 v255, s[60:63], s17 offen offset:0 sc1 lds
v_mfma_f32_16x16x32_bf16 v[200:203], a[64:67], a[16:19], 0
v_exp_f32 v223, v223
v_mfma_f32_16x16x32_bf16 v[200:203], a[68:71], a[20:23], v[200:203]
v_cvt_pk_bf16_f32 v239, v222, v223
s_add_u32 s89, s89, 16384
v_mfma_f32_16x16x32_bf16 v[200:203], a[72:75], a[24:27], v[200:203]
v_exp_f32 v192, v192
v_mfma_f32_16x16x32_bf16 v[200:203], a[76:79], a[28:31], v[200:203]
s_waitcnt lgkmcnt(0)
s_and_b32 s89, s89, 0x1ffff
v_mfma_f32_16x16x32_bf16 v[196:199], a[80:83], a[0:3], 0
v_exp_f32 v193, v193
v_mfma_f32_16x16x32_bf16 v[196:199], a[84:87], a[4:7], v[196:199]
v_cvt_pk_bf16_f32 v224, v192, v193
v_mfma_f32_16x16x32_bf16 v[196:199], a[88:91], a[8:11], v[196:199]
v_exp_f32 v194, v194
v_mfma_f32_16x16x32_bf16 v[196:199], a[92:95], a[12:15], v[196:199]
buffer_load_dwordx4 v255, s[60:63], s17 offen offset:1024 sc1 lds
v_mfma_f32_16x16x32_bf16 v[204:207], a[80:83], a[16:19], 0
v_exp_f32 v195, v195
v_mfma_f32_16x16x32_bf16 v[204:207], a[84:87], a[20:23], v[204:207]
v_cvt_pk_bf16_f32 v225, v194, v195
v_mfma_f32_16x16x32_bf16 v[204:207], a[88:91], a[24:27], v[204:207]
v_exp_f32 v200, v200
v_mfma_f32_16x16x32_bf16 v[204:207], a[92:95], a[28:31], v[204:207]
buffer_load_dwordx4 v255, s[60:63], s17 offen offset:2048 sc1 lds
v_mfma_f32_16x16x32_bf16 v[208:211], a[64:67], a[32:35], 0
v_exp_f32 v201, v201
v_mfma_f32_16x16x32_bf16 v[208:211], a[68:71], a[36:39], v[208:211]
v_cvt_pk_bf16_f32 v228, v200, v201
v_mfma_f32_16x16x32_bf16 v[208:211], a[72:75], a[40:43], v[208:211]
v_exp_f32 v202, v202
v_mfma_f32_16x16x32_bf16 v[208:211], a[76:79], a[44:47], v[208:211]
buffer_load_dwordx4 v255, s[60:63], s17 offen offset:3072 sc1 lds
v_mfma_f32_16x16x32_bf16 v[216:219], a[64:67], a[48:51], 0
v_exp_f32 v203, v203
v_mfma_f32_16x16x32_bf16 v[216:219], a[68:71], a[52:55], v[216:219]
v_cvt_pk_bf16_f32 v229, v202, v203
v_mfma_f32_16x16x32_bf16 v[216:219], a[72:75], a[56:59], v[216:219]
v_exp_f32 v196, v196
v_mfma_f32_16x16x32_bf16 v[216:219], a[76:79], a[60:63], v[216:219]
buffer_load_dword a253, v254, s[36:39], s90 offen
v_mfma_f32_16x16x32_bf16 v[212:215], a[80:83], a[32:35], 0
v_exp_f32 v197, v197
v_mfma_f32_16x16x32_bf16 v[212:215], a[84:87], a[36:39], v[212:215]
v_cvt_pk_bf16_f32 v226, v196, v197
s_add_u32 s17, s17, 8192
v_mfma_f32_16x16x32_bf16 v[212:215], a[88:91], a[40:43], v[212:215]
v_exp_f32 v198, v198
v_mfma_f32_16x16x32_bf16 v[212:215], a[92:95], a[44:47], v[212:215]
s_add_u32 m0, m0, 16384
s_and_b32 m0, m0, s78
v_mfma_f32_16x16x32_bf16 v[220:223], a[80:83], a[48:51], 0
v_exp_f32 v199, v199
v_mfma_f32_16x16x32_bf16 v[220:223], a[84:87], a[52:55], v[220:223]
v_cvt_pk_bf16_f32 v227, v198, v199
s_add_u32 s90, s90, s21
v_mfma_f32_16x16x32_bf16 v[220:223], a[88:91], a[56:59], v[220:223]
v_exp_f32 v204, v204
v_mfma_f32_16x16x32_bf16 v[220:223], a[92:95], a[60:63], v[220:223]
s_waitcnt vmcnt(11)
s_barrier
v_mfma_f32_16x16x32_bf16 v[64:67], a[96:99], v[232:235], v[64:67]
v_exp_f32 v205, v205
v_mfma_f32_16x16x32_bf16 v[96:99], a[96:99], v[236:239], v[96:99]
ds_read_b128 a[96:99], v248 offset:8192
v_cvt_pk_bf16_f32 v230, v204, v205
v_mfma_f32_16x16x32_bf16 v[68:71], a[100:103], v[232:235], v[68:71]
v_exp_f32 v206, v206
v_mfma_f32_16x16x32_bf16 v[100:103], a[100:103], v[236:239], v[100:103]
ds_read_b128 a[100:103], v248 offset:9216
v_mfma_f32_16x16x32_bf16 v[72:75], a[104:107], v[232:235], v[72:75]
v_exp_f32 v207, v207
v_mfma_f32_16x16x32_bf16 v[104:107], a[104:107], v[236:239], v[104:107]
ds_read_b128 a[104:107], v248 offset:10240
v_cvt_pk_bf16_f32 v231, v206, v207
v_mfma_f32_16x16x32_bf16 v[76:79], a[108:111], v[232:235], v[76:79]
v_exp_f32 v208, v208
v_mfma_f32_16x16x32_bf16 v[108:111], a[108:111], v[236:239], v[108:111]
ds_read_b128 a[108:111], v248 offset:11264
v_mfma_f32_16x16x32_bf16 v[80:83], a[112:115], v[232:235], v[80:83]
v_exp_f32 v209, v209
v_mfma_f32_16x16x32_bf16 v[112:115], a[112:115], v[236:239], v[112:115]
ds_read_b128 a[112:115], v248 offset:12288
v_mfma_f32_16x16x32_bf16 v[84:87], a[116:119], v[232:235], v[84:87]
v_exp_f32 v210, v210
v_mfma_f32_16x16x32_bf16 v[116:119], a[116:119], v[236:239], v[116:119]
ds_read_b128 a[116:119], v248 offset:13312
v_mfma_f32_16x16x32_bf16 v[88:91], a[120:123], v[232:235], v[88:91]
v_exp_f32 v211, v211
v_mfma_f32_16x16x32_bf16 v[120:123], a[120:123], v[236:239], v[120:123]
ds_read_b128 a[120:123], v248 offset:14336
s_bitcmp1_b64 s[98:99], s85
v_mfma_f32_16x16x32_bf16 v[92:95], a[124:127], v[232:235], v[92:95]
v_exp_f32 v216, v216
v_mfma_f32_16x16x32_bf16 v[124:127], a[124:127], v[236:239], v[124:127]
ds_read_b128 a[124:127], v248 offset:15360
v_or_b32 v248, s89, v255
v_smfmac_f32_16x16x64_bf16 a[196:199], a[208:211], v[232:239], v241
s_waitcnt lgkmcnt(4)
v_smfmac_f32_16x16x64_bf16 a[192:195], a[208:211], v[224:231], v241
ds_read_b128 a[64:67], v248 offset:0
v_mfma_f32_16x16x32_bf16 v[0:3], a[96:99], v[224:227], v[0:3]
v_exp_f32 v217, v217
v_mfma_f32_16x16x32_bf16 v[32:35], a[96:99], v[228:231], v[32:35]
ds_read_b128 a[68:71], v248 offset:1024
v_cvt_pk_bf16_f32 v232, v208, v209
v_mfma_f32_16x16x32_bf16 v[4:7], a[100:103], v[224:227], v[4:7]
v_exp_f32 v218, v218
v_mfma_f32_16x16x32_bf16 v[36:39], a[100:103], v[228:231], v[36:39]
ds_read_b128 a[72:75], v248 offset:2048
v_cvt_pk_bf16_f32 v233, v210, v211
v_mfma_f32_16x16x32_bf16 v[8:11], a[104:107], v[224:227], v[8:11]
v_exp_f32 v219, v219
v_mfma_f32_16x16x32_bf16 v[40:43], a[104:107], v[228:231], v[40:43]
ds_read_b128 a[76:79], v248 offset:3072
v_cvt_pk_bf16_f32 v236, v216, v217
v_mfma_f32_16x16x32_bf16 v[12:15], a[108:111], v[224:227], v[12:15]
v_exp_f32 v212, v212
v_mfma_f32_16x16x32_bf16 v[44:47], a[108:111], v[228:231], v[44:47]
ds_read_b128 a[80:83], v248 offset:4096
s_waitcnt lgkmcnt(5)
v_mfma_f32_16x16x32_bf16 v[16:19], a[112:115], v[224:227], v[16:19]
v_exp_f32 v213, v213
v_mfma_f32_16x16x32_bf16 v[48:51], a[112:115], v[228:231], v[48:51]
ds_read_b128 a[84:87], v248 offset:5120
v_cvt_pk_bf16_f32 v237, v218, v219
v_mfma_f32_16x16x32_bf16 v[20:23], a[116:119], v[224:227], v[20:23]
v_exp_f32 v214, v214
v_mfma_f32_16x16x32_bf16 v[52:55], a[116:119], v[228:231], v[52:55]
ds_read_b128 a[88:91], v248 offset:6144
v_cvt_pk_bf16_f32 v234, v212, v213
v_mfma_f32_16x16x32_bf16 v[24:27], a[120:123], v[224:227], v[24:27]
v_exp_f32 v215, v215
v_mfma_f32_16x16x32_bf16 v[56:59], a[120:123], v[228:231], v[56:59]
ds_read_b128 a[92:95], v248 offset:7168
v_cvt_pk_bf16_f32 v235, v214, v215
v_mfma_f32_16x16x32_bf16 v[28:31], a[124:127], v[224:227], v[28:31]
v_exp_f32 v220, v220
v_mfma_f32_16x16x32_bf16 v[60:63], a[124:127], v[228:231], v[60:63]
s_cbranch_scc0 .La5_LA_F2
.La5_LA_F2E2:
.La5_LA_F3:
.La5_LW2_6:
s_cbranch_execz .La5_LW2_10
s_waitcnt lgkmcnt(4)
v_mfma_f32_16x16x32_bf16 v[192:195], a[64:67], a[0:3], 0
v_exp_f32 v221, v221
v_mfma_f32_16x16x32_bf16 v[192:195], a[68:71], a[4:7], v[192:195]
v_cvt_pk_bf16_f32 v238, v220, v221
s_add_u32 s89, s89, 16384
v_mfma_f32_16x16x32_bf16 v[192:195], a[72:75], a[8:11], v[192:195]
v_exp_f32 v222, v222
v_mfma_f32_16x16x32_bf16 v[192:195], a[76:79], a[12:15], v[192:195]
buffer_load_dwordx4 v255, s[60:63], s17 offen offset:0 sc1 lds
v_mfma_f32_16x16x32_bf16 v[200:203], a[64:67], a[16:19], 0
v_exp_f32 v223, v223
v_mfma_f32_16x16x32_bf16 v[200:203], a[68:71], a[20:23], v[200:203]
v_cvt_pk_bf16_f32 v239, v222, v223
s_and_b32 s89, s89, 0x1ffff
v_mfma_f32_16x16x32_bf16 v[200:203], a[72:75], a[24:27], v[200:203]
v_exp_f32 v192, v192
v_mfma_f32_16x16x32_bf16 v[200:203], a[76:79], a[28:31], v[200:203]
s_waitcnt lgkmcnt(0)
v_mfma_f32_16x16x32_bf16 v[196:199], a[80:83], a[0:3], 0
v_exp_f32 v193, v193
v_mfma_f32_16x16x32_bf16 v[196:199], a[84:87], a[4:7], v[196:199]
v_cvt_pk_bf16_f32 v224, v192, v193
v_accvgpr_read_b32 v254, a229
v_mfma_f32_16x16x32_bf16 v[196:199], a[88:91], a[8:11], v[196:199]
v_exp_f32 v194, v194
v_mfma_f32_16x16x32_bf16 v[196:199], a[92:95], a[12:15], v[196:199]
buffer_load_dwordx4 v255, s[60:63], s17 offen offset:1024 sc1 lds
v_mfma_f32_16x16x32_bf16 v[204:207], a[80:83], a[16:19], 0
v_exp_f32 v195, v195
v_mfma_f32_16x16x32_bf16 v[204:207], a[84:87], a[20:23], v[204:207]
v_cvt_pk_bf16_f32 v225, v194, v195
v_mfma_f32_16x16x32_bf16 v[204:207], a[88:91], a[24:27], v[204:207]
v_exp_f32 v200, v200
v_mfma_f32_16x16x32_bf16 v[204:207], a[92:95], a[28:31], v[204:207]
buffer_load_dwordx4 v255, s[60:63], s17 offen offset:2048 sc1 lds
v_mfma_f32_16x16x32_bf16 v[208:211], a[64:67], a[32:35], 0
v_exp_f32 v201, v201
v_mfma_f32_16x16x32_bf16 v[208:211], a[68:71], a[36:39], v[208:211]
v_cvt_pk_bf16_f32 v228, v200, v201
v_mfma_f32_16x16x32_bf16 v[208:211], a[72:75], a[40:43], v[208:211]
v_exp_f32 v202, v202
v_mfma_f32_16x16x32_bf16 v[208:211], a[76:79], a[44:47], v[208:211]
buffer_load_dwordx4 v255, s[60:63], s17 offen offset:3072 sc1 lds
v_mfma_f32_16x16x32_bf16 v[216:219], a[64:67], a[48:51], 0
v_exp_f32 v203, v203
v_mfma_f32_16x16x32_bf16 v[216:219], a[68:71], a[52:55], v[216:219]
v_cvt_pk_bf16_f32 v229, v202, v203
s_mov_b32 s93, 0
v_mfma_f32_16x16x32_bf16 v[216:219], a[72:75], a[56:59], v[216:219]
v_exp_f32 v196, v196
v_mfma_f32_16x16x32_bf16 v[216:219], a[76:79], a[60:63], v[216:219]
buffer_load_dwordx4 a[128:131], v254, s[40:43], s93 offen
v_mfma_f32_16x16x32_bf16 v[212:215], a[80:83], a[32:35], 0
v_exp_f32 v197, v197
s_add_u32 s17, s17, 8192
v_mfma_f32_16x16x32_bf16 v[212:215], a[84:87], a[36:39], v[212:215]
v_cvt_pk_bf16_f32 v226, v196, v197
s_mov_b32 s93, 7168
s_add_u32 m0, m0, 16384
v_mfma_f32_16x16x32_bf16 v[212:215], a[88:91], a[40:43], v[212:215]
v_exp_f32 v198, v198
s_and_b32 m0, m0, s78
v_mfma_f32_16x16x32_bf16 v[212:215], a[92:95], a[44:47], v[212:215]
buffer_load_dwordx4 a[132:135], v254, s[40:43], s93 offen
v_mfma_f32_16x16x32_bf16 v[220:223], a[80:83], a[48:51], 0
v_exp_f32 v199, v199
v_mfma_f32_16x16x32_bf16 v[220:223], a[84:87], a[52:55], v[220:223]
v_cvt_pk_bf16_f32 v227, v198, v199
v_mfma_f32_16x16x32_bf16 v[220:223], a[88:91], a[56:59], v[220:223]
v_exp_f32 v204, v204
v_mfma_f32_16x16x32_bf16 v[220:223], a[92:95], a[60:63], v[220:223]
.La5_LW2_7:
s_cbranch_execz .La5_LW2_11
s_waitcnt vmcnt(12)
s_barrier
v_mfma_f32_16x16x32_bf16 v[64:67], a[96:99], v[232:235], v[64:67]
v_exp_f32 v205, v205
v_mfma_f32_16x16x32_bf16 v[96:99], a[96:99], v[236:239], v[96:99]
ds_read_b128 a[96:99], v248 offset:8192
v_cvt_pk_bf16_f32 v230, v204, v205
v_mfma_f32_16x16x32_bf16 v[68:71], a[100:103], v[232:235], v[68:71]
v_exp_f32 v206, v206
v_mfma_f32_16x16x32_bf16 v[100:103], a[100:103], v[236:239], v[100:103]
ds_read_b128 a[100:103], v248 offset:9216
v_mfma_f32_16x16x32_bf16 v[72:75], a[104:107], v[232:235], v[72:75]
v_exp_f32 v207, v207
v_mfma_f32_16x16x32_bf16 v[104:107], a[104:107], v[236:239], v[104:107]
ds_read_b128 a[104:107], v248 offset:10240
v_cvt_pk_bf16_f32 v231, v206, v207
s_mov_b32 s93, 14336
v_mfma_f32_16x16x32_bf16 v[76:79], a[108:111], v[232:235], v[76:79]
v_exp_f32 v208, v208
v_mfma_f32_16x16x32_bf16 v[108:111], a[108:111], v[236:239], v[108:111]
ds_read_b128 a[108:111], v248 offset:11264
buffer_load_dwordx4 a[136:139], v254, s[40:43], s93 offen
v_mfma_f32_16x16x32_bf16 v[80:83], a[112:115], v[232:235], v[80:83]
v_exp_f32 v209, v209
v_mfma_f32_16x16x32_bf16 v[112:115], a[112:115], v[236:239], v[112:115]
ds_read_b128 a[112:115], v248 offset:12288
v_mfma_f32_16x16x32_bf16 v[84:87], a[116:119], v[232:235], v[84:87]
v_exp_f32 v210, v210
v_mfma_f32_16x16x32_bf16 v[116:119], a[116:119], v[236:239], v[116:119]
ds_read_b128 a[116:119], v248 offset:13312
v_mfma_f32_16x16x32_bf16 v[88:91], a[120:123], v[232:235], v[88:91]
v_exp_f32 v211, v211
v_mfma_f32_16x16x32_bf16 v[120:123], a[120:123], v[236:239], v[120:123]
ds_read_b128 a[120:123], v248 offset:14336
v_mfma_f32_16x16x32_bf16 v[92:95], a[124:127], v[232:235], v[92:95]
v_exp_f32 v216, v216
v_mfma_f32_16x16x32_bf16 v[124:127], a[124:127], v[236:239], v[124:127]
ds_read_b128 a[124:127], v248 offset:15360
v_or_b32 v248, s89, v255
v_smfmac_f32_16x16x64_bf16 a[196:199], a[208:211], v[232:239], v241
s_waitcnt lgkmcnt(4)
v_smfmac_f32_16x16x64_bf16 a[192:195], a[208:211], v[224:231], v241
ds_read_b128 a[64:67], v248 offset:0
v_mfma_f32_16x16x32_bf16 v[0:3], a[96:99], v[224:227], v[0:3]
v_exp_f32 v217, v217
v_mfma_f32_16x16x32_bf16 v[32:35], a[96:99], v[228:231], v[32:35]
ds_read_b128 a[68:71], v248 offset:1024
v_cvt_pk_bf16_f32 v232, v208, v209
v_mfma_f32_16x16x32_bf16 v[4:7], a[100:103], v[224:227], v[4:7]
v_exp_f32 v218, v218
v_mfma_f32_16x16x32_bf16 v[36:39], a[100:103], v[228:231], v[36:39]
ds_read_b128 a[72:75], v248 offset:2048
v_cvt_pk_bf16_f32 v233, v210, v211
v_mfma_f32_16x16x32_bf16 v[8:11], a[104:107], v[224:227], v[8:11]
v_exp_f32 v219, v219
v_mfma_f32_16x16x32_bf16 v[40:43], a[104:107], v[228:231], v[40:43]
ds_read_b128 a[76:79], v248 offset:3072
v_cvt_pk_bf16_f32 v236, v216, v217
v_mfma_f32_16x16x32_bf16 v[12:15], a[108:111], v[224:227], v[12:15]
v_exp_f32 v212, v212
v_mfma_f32_16x16x32_bf16 v[44:47], a[108:111], v[228:231], v[44:47]
ds_read_b128 a[80:83], v248 offset:4096
s_waitcnt lgkmcnt(5)
v_mfma_f32_16x16x32_bf16 v[16:19], a[112:115], v[224:227], v[16:19]
v_exp_f32 v213, v213
v_mfma_f32_16x16x32_bf16 v[48:51], a[112:115], v[228:231], v[48:51]
ds_read_b128 a[84:87], v248 offset:5120
v_cvt_pk_bf16_f32 v237, v218, v219
v_mfma_f32_16x16x32_bf16 v[20:23], a[116:119], v[224:227], v[20:23]
v_exp_f32 v214, v214
v_mfma_f32_16x16x32_bf16 v[52:55], a[116:119], v[228:231], v[52:55]
ds_read_b128 a[88:91], v248 offset:6144
v_cvt_pk_bf16_f32 v234, v212, v213
v_mfma_f32_16x16x32_bf16 v[24:27], a[120:123], v[224:227], v[24:27]
v_exp_f32 v215, v215
v_mfma_f32_16x16x32_bf16 v[56:59], a[120:123], v[228:231], v[56:59]
ds_read_b128 a[92:95], v248 offset:7168
v_cvt_pk_bf16_f32 v235, v214, v215
v_mfma_f32_16x16x32_bf16 v[28:31], a[124:127], v[224:227], v[28:31]
v_exp_f32 v220, v220
v_mfma_f32_16x16x32_bf16 v[60:63], a[124:127], v[228:231], v[60:63]
s_waitcnt lgkmcnt(4)
v_mfma_f32_16x16x32_bf16 v[192:195], a[64:67], a[0:3], 0
v_exp_f32 v221, v221
v_mfma_f32_16x16x32_bf16 v[192:195], a[68:71], a[4:7], v[192:195]
v_cvt_pk_bf16_f32 v238, v220, v221
s_add_u32 s89, s89, 16384
v_mfma_f32_16x16x32_bf16 v[192:195], a[72:75], a[8:11], v[192:195]
v_exp_f32 v222, v222
v_mfma_f32_16x16x32_bf16 v[192:195], a[76:79], a[12:15], v[192:195]
buffer_load_dwordx4 v255, s[60:63], s17 offen offset:0 sc1 lds
v_mfma_f32_16x16x32_bf16 v[200:203], a[64:67], a[16:19], 0
v_exp_f32 v223, v223
v_mfma_f32_16x16x32_bf16 v[200:203], a[68:71], a[20:23], v[200:203]
v_cvt_pk_bf16_f32 v239, v222, v223
s_and_b32 s89, s89, 0x1ffff
v_mfma_f32_16x16x32_bf16 v[200:203], a[72:75], a[24:27], v[200:203]
v_exp_f32 v192, v192
v_mfma_f32_16x16x32_bf16 v[200:203], a[76:79], a[28:31], v[200:203]
s_waitcnt lgkmcnt(0)
v_mfma_f32_16x16x32_bf16 v[196:199], a[80:83], a[0:3], 0
v_exp_f32 v193, v193
v_mfma_f32_16x16x32_bf16 v[196:199], a[84:87], a[4:7], v[196:199]
v_cvt_pk_bf16_f32 v224, v192, v193
v_mfma_f32_16x16x32_bf16 v[196:199], a[88:91], a[8:11], v[196:199]
v_exp_f32 v194, v194
v_mfma_f32_16x16x32_bf16 v[196:199], a[92:95], a[12:15], v[196:199]
buffer_load_dwordx4 v255, s[60:63], s17 offen offset:1024 sc1 lds
v_mfma_f32_16x16x32_bf16 v[204:207], a[80:83], a[16:19], 0
v_exp_f32 v195, v195
v_mfma_f32_16x16x32_bf16 v[204:207], a[84:87], a[20:23], v[204:207]
v_cvt_pk_bf16_f32 v225, v194, v195
v_mfma_f32_16x16x32_bf16 v[204:207], a[88:91], a[24:27], v[204:207]
v_exp_f32 v200, v200
v_mfma_f32_16x16x32_bf16 v[204:207], a[92:95], a[28:31], v[204:207]
buffer_load_dwordx4 v255, s[60:63], s17 offen offset:2048 sc1 lds
v_mfma_f32_16x16x32_bf16 v[208:211], a[64:67], a[32:35], 0
v_exp_f32 v201, v201
v_mfma_f32_16x16x32_bf16 v[208:211], a[68:71], a[36:39], v[208:211]
v_cvt_pk_bf16_f32 v228, v200, v201
v_mfma_f32_16x16x32_bf16 v[208:211], a[72:75], a[40:43], v[208:211]
v_exp_f32 v202, v202
v_mfma_f32_16x16x32_bf16 v[208:211], a[76:79], a[44:47], v[208:211]
buffer_load_dwordx4 v255, s[60:63], s17 offen offset:3072 sc1 lds
v_mfma_f32_16x16x32_bf16 v[216:219], a[64:67], a[48:51], 0
v_exp_f32 v203, v203
v_mfma_f32_16x16x32_bf16 v[216:219], a[68:71], a[52:55], v[216:219]
v_cvt_pk_bf16_f32 v229, v202, v203
s_mov_b32 s93, 21504
v_mfma_f32_16x16x32_bf16 v[216:219], a[72:75], a[56:59], v[216:219]
v_exp_f32 v196, v196
v_mfma_f32_16x16x32_bf16 v[216:219], a[76:79], a[60:63], v[216:219]
buffer_load_dwordx4 a[140:143], v254, s[40:43], s93 offen
v_mfma_f32_16x16x32_bf16 v[212:215], a[80:83], a[32:35], 0
v_exp_f32 v197, v197
s_add_u32 s17, s17, 8192
v_mfma_f32_16x16x32_bf16 v[212:215], a[84:87], a[36:39], v[212:215]
v_cvt_pk_bf16_f32 v226, v196, v197
s_mov_b32 s93, 28672
s_add_u32 m0, m0, 16384
v_mfma_f32_16x16x32_bf16 v[212:215], a[88:91], a[40:43], v[212:215]
v_exp_f32 v198, v198
s_and_b32 m0, m0, s78
v_mfma_f32_16x16x32_bf16 v[212:215], a[92:95], a[44:47], v[212:215]
buffer_load_dwordx4 a[144:147], v254, s[40:43], s93 offen
v_mfma_f32_16x16x32_bf16 v[220:223], a[80:83], a[48:51], 0
v_exp_f32 v199, v199
v_mfma_f32_16x16x32_bf16 v[220:223], a[84:87], a[52:55], v[220:223]
v_cvt_pk_bf16_f32 v227, v198, v199
v_mfma_f32_16x16x32_bf16 v[220:223], a[88:91], a[56:59], v[220:223]
v_exp_f32 v204, v204
v_mfma_f32_16x16x32_bf16 v[220:223], a[92:95], a[60:63], v[220:223]
s_waitcnt vmcnt(14)
s_barrier
v_mfma_f32_16x16x32_bf16 v[64:67], a[96:99], v[232:235], v[64:67]
v_exp_f32 v205, v205
v_mfma_f32_16x16x32_bf16 v[96:99], a[96:99], v[236:239], v[96:99]
ds_read_b128 a[96:99], v248 offset:8192
v_cvt_pk_bf16_f32 v230, v204, v205
v_mfma_f32_16x16x32_bf16 v[68:71], a[100:103], v[232:235], v[68:71]
v_exp_f32 v206, v206
v_mfma_f32_16x16x32_bf16 v[100:103], a[100:103], v[236:239], v[100:103]
ds_read_b128 a[100:103], v248 offset:9216
v_mfma_f32_16x16x32_bf16 v[72:75], a[104:107], v[232:235], v[72:75]
v_exp_f32 v207, v207
v_mfma_f32_16x16x32_bf16 v[104:107], a[104:107], v[236:239], v[104:107]
ds_read_b128 a[104:107], v248 offset:10240
v_cvt_pk_bf16_f32 v231, v206, v207
s_mov_b32 s93, 35840
v_mfma_f32_16x16x32_bf16 v[76:79], a[108:111], v[232:235], v[76:79]
v_exp_f32 v208, v208
v_mfma_f32_16x16x32_bf16 v[108:111], a[108:111], v[236:239], v[108:111]
ds_read_b128 a[108:111], v248 offset:11264
buffer_load_dwordx4 a[148:151], v254, s[40:43], s93 offen
v_mfma_f32_16x16x32_bf16 v[80:83], a[112:115], v[232:235], v[80:83]
v_exp_f32 v209, v209
v_mfma_f32_16x16x32_bf16 v[112:115], a[112:115], v[236:239], v[112:115]
ds_read_b128 a[112:115], v248 offset:12288
v_mfma_f32_16x16x32_bf16 v[84:87], a[116:119], v[232:235], v[84:87]
v_exp_f32 v210, v210
v_mfma_f32_16x16x32_bf16 v[116:119], a[116:119], v[236:239], v[116:119]
ds_read_b128 a[116:119], v248 offset:13312
v_accvgpr_read_b32 v240, a231
s_waitcnt vmcnt(14)
v_mfma_f32_16x16x32_bf16 v[88:91], a[120:123], v[232:235], v[88:91]
v_exp_f32 v211, v211
v_mfma_f32_16x16x32_bf16 v[120:123], a[120:123], v[236:239], v[120:123]
ds_read_b128 a[120:123], v248 offset:14336
v_mfma_f32_16x16x32_bf16 v[92:95], a[124:127], v[232:235], v[92:95]
v_exp_f32 v216, v216
v_mfma_f32_16x16x32_bf16 v[124:127], a[124:127], v[236:239], v[124:127]
ds_read_b128 a[124:127], v248 offset:15360
v_or_b32 v248, s89, v255
v_smfmac_f32_16x16x64_bf16 a[196:199], a[208:211], v[232:239], v241
s_waitcnt lgkmcnt(4)
v_smfmac_f32_16x16x64_bf16 a[192:195], a[208:211], v[224:231], v241
ds_read_b128 a[64:67], v248 offset:0
v_mfma_f32_16x16x32_bf16 v[0:3], a[96:99], v[224:227], v[0:3]
v_exp_f32 v217, v217
v_mfma_f32_16x16x32_bf16 v[32:35], a[96:99], v[228:231], v[32:35]
ds_read_b128 a[68:71], v248 offset:1024
v_cvt_pk_bf16_f32 v232, v208, v209
v_mfma_f32_16x16x32_bf16 v[4:7], a[100:103], v[224:227], v[4:7]
v_exp_f32 v218, v218
v_mfma_f32_16x16x32_bf16 v[36:39], a[100:103], v[228:231], v[36:39]
ds_read_b128 a[72:75], v248 offset:2048
v_cvt_pk_bf16_f32 v233, v210, v211
v_mfma_f32_16x16x32_bf16 v[8:11], a[104:107], v[224:227], v[8:11]
v_exp_f32 v219, v219
v_mfma_f32_16x16x32_bf16 v[40:43], a[104:107], v[228:231], v[40:43]
ds_read_b128 a[76:79], v248 offset:3072
v_cvt_pk_bf16_f32 v236, v216, v217
v_mfma_f32_16x16x32_bf16 v[12:15], a[108:111], v[224:227], v[12:15]
v_exp_f32 v212, v212
v_mfma_f32_16x16x32_bf16 v[44:47], a[108:111], v[228:231], v[44:47]
ds_read_b128 a[80:83], v248 offset:4096
s_waitcnt lgkmcnt(5)
v_mfma_f32_16x16x32_bf16 v[16:19], a[112:115], v[224:227], v[16:19]
v_exp_f32 v213, v213
v_mfma_f32_16x16x32_bf16 v[48:51], a[112:115], v[228:231], v[48:51]
ds_read_b128 a[84:87], v248 offset:5120
v_cvt_pk_bf16_f32 v237, v218, v219
v_mfma_f32_16x16x32_bf16 v[20:23], a[116:119], v[224:227], v[20:23]
v_exp_f32 v214, v214
v_mfma_f32_16x16x32_bf16 v[52:55], a[116:119], v[228:231], v[52:55]
ds_read_b128 a[88:91], v248 offset:6144
v_cvt_pk_bf16_f32 v234, v212, v213
v_mfma_f32_16x16x32_bf16 v[24:27], a[120:123], v[224:227], v[24:27]
v_exp_f32 v215, v215
v_mfma_f32_16x16x32_bf16 v[56:59], a[120:123], v[228:231], v[56:59]
ds_read_b128 a[92:95], v248 offset:7168
v_cvt_pk_bf16_f32 v235, v214, v215
v_mfma_f32_16x16x32_bf16 v[28:31], a[124:127], v[224:227], v[28:31]
v_exp_f32 v220, v220
v_mfma_f32_16x16x32_bf16 v[60:63], a[124:127], v[228:231], v[60:63]
buffer_store_dwordx2 v[252:253], v240, s[64:67], s6 offen offset:0 sc1
s_waitcnt lgkmcnt(4)
v_mfma_f32_16x16x32_bf16 v[192:195], a[64:67], a[0:3], 0
v_exp_f32 v221, v221
v_mfma_f32_16x16x32_bf16 v[192:195], a[68:71], a[4:7], v[192:195]
v_cvt_pk_bf16_f32 v238, v220, v221
s_add_u32 s89, s89, 16384
v_mfma_f32_16x16x32_bf16 v[192:195], a[72:75], a[8:11], v[192:195]
v_exp_f32 v222, v222
v_mfma_f32_16x16x32_bf16 v[192:195], a[76:79], a[12:15], v[192:195]
buffer_load_dwordx4 v255, s[60:63], s17 offen offset:0 sc1 lds
v_mfma_f32_16x16x32_bf16 v[200:203], a[64:67], a[16:19], 0
v_exp_f32 v223, v223
v_mfma_f32_16x16x32_bf16 v[200:203], a[68:71], a[20:23], v[200:203]
v_cvt_pk_bf16_f32 v239, v222, v223
s_and_b32 s89, s89, 0x1ffff
v_mfma_f32_16x16x32_bf16 v[200:203], a[72:75], a[24:27], v[200:203]
v_exp_f32 v192, v192
v_mfma_f32_16x16x32_bf16 v[200:203], a[76:79], a[28:31], v[200:203]
s_waitcnt lgkmcnt(0)
v_mfma_f32_16x16x32_bf16 v[196:199], a[80:83], a[0:3], 0
v_exp_f32 v193, v193
v_mfma_f32_16x16x32_bf16 v[196:199], a[84:87], a[4:7], v[196:199]
v_cvt_pk_bf16_f32 v224, v192, v193
v_mfma_f32_16x16x32_bf16 v[196:199], a[88:91], a[8:11], v[196:199]
v_exp_f32 v194, v194
v_mfma_f32_16x16x32_bf16 v[196:199], a[92:95], a[12:15], v[196:199]
buffer_load_dwordx4 v255, s[60:63], s17 offen offset:1024 sc1 lds
v_mfma_f32_16x16x32_bf16 v[204:207], a[80:83], a[16:19], 0
v_exp_f32 v195, v195
v_mfma_f32_16x16x32_bf16 v[204:207], a[84:87], a[20:23], v[204:207]
v_cvt_pk_bf16_f32 v225, v194, v195
v_mfma_f32_16x16x32_bf16 v[204:207], a[88:91], a[24:27], v[204:207]
v_exp_f32 v200, v200
v_mfma_f32_16x16x32_bf16 v[204:207], a[92:95], a[28:31], v[204:207]
buffer_load_dwordx4 v255, s[60:63], s17 offen offset:2048 sc1 lds
v_mfma_f32_16x16x32_bf16 v[208:211], a[64:67], a[32:35], 0
v_exp_f32 v201, v201
v_mfma_f32_16x16x32_bf16 v[208:211], a[68:71], a[36:39], v[208:211]
v_cvt_pk_bf16_f32 v228, v200, v201
v_mfma_f32_16x16x32_bf16 v[208:211], a[72:75], a[40:43], v[208:211]
v_exp_f32 v202, v202
v_mfma_f32_16x16x32_bf16 v[208:211], a[76:79], a[44:47], v[208:211]
buffer_load_dwordx4 v255, s[60:63], s17 offen offset:3072 sc1 lds
v_mfma_f32_16x16x32_bf16 v[216:219], a[64:67], a[48:51], 0
v_exp_f32 v203, v203
v_mfma_f32_16x16x32_bf16 v[216:219], a[68:71], a[52:55], v[216:219]
v_cvt_pk_bf16_f32 v229, v202, v203
s_mov_b32 s93, 43008
v_mfma_f32_16x16x32_bf16 v[216:219], a[72:75], a[56:59], v[216:219]
v_exp_f32 v196, v196
v_mfma_f32_16x16x32_bf16 v[216:219], a[76:79], a[60:63], v[216:219]
buffer_load_dwordx4 a[152:155], v254, s[40:43], s93 offen
v_mfma_f32_16x16x32_bf16 v[212:215], a[80:83], a[32:35], 0
v_exp_f32 v197, v197
s_add_u32 s17, s17, 8192
v_mfma_f32_16x16x32_bf16 v[212:215], a[84:87], a[36:39], v[212:215]
v_cvt_pk_bf16_f32 v226, v196, v197
s_mov_b32 s93, 50176
s_add_u32 m0, m0, 16384
v_mfma_f32_16x16x32_bf16 v[212:215], a[88:91], a[40:43], v[212:215]
v_exp_f32 v198, v198
s_and_b32 m0, m0, s78
v_mfma_f32_16x16x32_bf16 v[212:215], a[92:95], a[44:47], v[212:215]
buffer_load_dwordx4 a[156:159], v254, s[40:43], s93 offen
v_mfma_f32_16x16x32_bf16 v[220:223], a[80:83], a[48:51], 0
v_exp_f32 v199, v199
v_mfma_f32_16x16x32_bf16 v[220:223], a[84:87], a[52:55], v[220:223]
v_cvt_pk_bf16_f32 v227, v198, v199
v_mfma_f32_16x16x32_bf16 v[220:223], a[88:91], a[56:59], v[220:223]
v_exp_f32 v204, v204
v_mfma_f32_16x16x32_bf16 v[220:223], a[92:95], a[60:63], v[220:223]
s_waitcnt vmcnt(17)
s_barrier
v_mfma_f32_16x16x32_bf16 v[64:67], a[96:99], v[232:235], v[64:67]
v_exp_f32 v205, v205
v_mfma_f32_16x16x32_bf16 v[96:99], a[96:99], v[236:239], v[96:99]
ds_read_b128 a[96:99], v248 offset:8192
v_cvt_pk_bf16_f32 v230, v204, v205
v_mfma_f32_16x16x32_bf16 v[68:71], a[100:103], v[232:235], v[68:71]
v_exp_f32 v206, v206
v_mfma_f32_16x16x32_bf16 v[100:103], a[100:103], v[236:239], v[100:103]
ds_read_b128 a[100:103], v248 offset:9216
v_mfma_f32_16x16x32_bf16 v[72:75], a[104:107], v[232:235], v[72:75]
v_exp_f32 v207, v207
v_mfma_f32_16x16x32_bf16 v[104:107], a[104:107], v[236:239], v[104:107]
ds_read_b128 a[104:107], v248 offset:10240
v_cvt_pk_bf16_f32 v231, v206, v207
s_mov_b32 s93, 57344
v_mfma_f32_16x16x32_bf16 v[76:79], a[108:111], v[232:235], v[76:79]
v_exp_f32 v208, v208
v_mfma_f32_16x16x32_bf16 v[108:111], a[108:111], v[236:239], v[108:111]
ds_read_b128 a[108:111], v248 offset:11264
buffer_load_dwordx4 a[160:163], v254, s[40:43], s93 offen
v_mfma_f32_16x16x32_bf16 v[80:83], a[112:115], v[232:235], v[80:83]
v_exp_f32 v209, v209
v_mfma_f32_16x16x32_bf16 v[112:115], a[112:115], v[236:239], v[112:115]
ds_read_b128 a[112:115], v248 offset:12288
v_mfma_f32_16x16x32_bf16 v[84:87], a[116:119], v[232:235], v[84:87]
v_exp_f32 v210, v210
v_mfma_f32_16x16x32_bf16 v[116:119], a[116:119], v[236:239], v[116:119]
ds_read_b128 a[116:119], v248 offset:13312
v_mfma_f32_16x16x32_bf16 v[88:91], a[120:123], v[232:235], v[88:91]
v_exp_f32 v211, v211
v_mfma_f32_16x16x32_bf16 v[120:123], a[120:123], v[236:239], v[120:123]
ds_read_b128 a[120:123], v248 offset:14336
v_mfma_f32_16x16x32_bf16 v[92:95], a[124:127], v[232:235], v[92:95]
v_exp_f32 v216, v216
v_mfma_f32_16x16x32_bf16 v[124:127], a[124:127], v[236:239], v[124:127]
ds_read_b128 a[124:127], v248 offset:15360
v_or_b32 v248, s89, v255
v_smfmac_f32_16x16x64_bf16 a[196:199], a[208:211], v[232:239], v241
s_waitcnt lgkmcnt(4)
v_smfmac_f32_16x16x64_bf16 a[192:195], a[208:211], v[224:231], v241
ds_read_b128 a[64:67], v248 offset:0
v_mfma_f32_16x16x32_bf16 v[0:3], a[96:99], v[224:227], v[0:3]
v_exp_f32 v217, v217
v_mfma_f32_16x16x32_bf16 v[32:35], a[96:99], v[228:231], v[32:35]
ds_read_b128 a[68:71], v248 offset:1024
v_cvt_pk_bf16_f32 v232, v208, v209
v_mfma_f32_16x16x32_bf16 v[4:7], a[100:103], v[224:227], v[4:7]
v_exp_f32 v218, v218
v_mfma_f32_16x16x32_bf16 v[36:39], a[100:103], v[228:231], v[36:39]
ds_read_b128 a[72:75], v248 offset:2048
v_cvt_pk_bf16_f32 v233, v210, v211
v_mfma_f32_16x16x32_bf16 v[8:11], a[104:107], v[224:227], v[8:11]
v_exp_f32 v219, v219
v_mfma_f32_16x16x32_bf16 v[40:43], a[104:107], v[228:231], v[40:43]
ds_read_b128 a[76:79], v248 offset:3072
v_cvt_pk_bf16_f32 v236, v216, v217
v_mfma_f32_16x16x32_bf16 v[12:15], a[108:111], v[224:227], v[12:15]
v_exp_f32 v212, v212
v_mfma_f32_16x16x32_bf16 v[44:47], a[108:111], v[228:231], v[44:47]
ds_read_b128 a[80:83], v248 offset:4096
s_waitcnt lgkmcnt(5)
v_mfma_f32_16x16x32_bf16 v[16:19], a[112:115], v[224:227], v[16:19]
v_exp_f32 v213, v213
v_mfma_f32_16x16x32_bf16 v[48:51], a[112:115], v[228:231], v[48:51]
ds_read_b128 a[84:87], v248 offset:5120
v_cvt_pk_bf16_f32 v237, v218, v219
v_mfma_f32_16x16x32_bf16 v[20:23], a[116:119], v[224:227], v[20:23]
v_exp_f32 v214, v214
v_mfma_f32_16x16x32_bf16 v[52:55], a[116:119], v[228:231], v[52:55]
ds_read_b128 a[88:91], v248 offset:6144
v_cvt_pk_bf16_f32 v234, v212, v213
v_mfma_f32_16x16x32_bf16 v[24:27], a[120:123], v[224:227], v[24:27]
v_exp_f32 v215, v215
v_mfma_f32_16x16x32_bf16 v[56:59], a[120:123], v[228:231], v[56:59]
ds_read_b128 a[92:95], v248 offset:7168
v_cvt_pk_bf16_f32 v235, v214, v215
v_mfma_f32_16x16x32_bf16 v[28:31], a[124:127], v[224:227], v[28:31]
v_exp_f32 v220, v220
v_mfma_f32_16x16x32_bf16 v[60:63], a[124:127], v[228:231], v[60:63]
s_waitcnt lgkmcnt(4)
v_mfma_f32_16x16x32_bf16 v[192:195], a[64:67], a[0:3], 0
v_exp_f32 v221, v221
v_mfma_f32_16x16x32_bf16 v[192:195], a[68:71], a[4:7], v[192:195]
v_cvt_pk_bf16_f32 v238, v220, v221
v_accvgpr_read_b32 v240, a228
v_mfma_f32_16x16x32_bf16 v[192:195], a[72:75], a[8:11], v[192:195]
v_exp_f32 v222, v222
v_mfma_f32_16x16x32_bf16 v[192:195], a[76:79], a[12:15], v[192:195]
s_add_u32 m0, s83, 16896
s_lshl_b32 s17, s72, 18
v_mfma_f32_16x16x32_bf16 v[200:203], a[64:67], a[16:19], 0
v_exp_f32 v223, v223
v_mfma_f32_16x16x32_bf16 v[200:203], a[68:71], a[20:23], v[200:203]
v_cvt_pk_bf16_f32 v239, v222, v223
s_add_u32 s89, s89, 16384
v_mfma_f32_16x16x32_bf16 v[200:203], a[72:75], a[24:27], v[200:203]
v_exp_f32 v192, v192
v_mfma_f32_16x16x32_bf16 v[200:203], a[76:79], a[28:31], v[200:203]
s_waitcnt lgkmcnt(0)
s_and_b32 s89, s89, 0x1ffff
v_mfma_f32_16x16x32_bf16 v[196:199], a[80:83], a[0:3], 0
v_exp_f32 v193, v193
v_mfma_f32_16x16x32_bf16 v[196:199], a[84:87], a[4:7], v[196:199]
v_cvt_pk_bf16_f32 v224, v192, v193
v_mfma_f32_16x16x32_bf16 v[196:199], a[88:91], a[8:11], v[196:199]
v_exp_f32 v194, v194
v_mfma_f32_16x16x32_bf16 v[196:199], a[92:95], a[12:15], v[196:199]
buffer_load_dwordx4 v240, s[56:59], s17 offen lds
v_mfma_f32_16x16x32_bf16 v[204:207], a[80:83], a[16:19], 0
v_exp_f32 v195, v195
s_add_u32 s17, s17, 32768
v_mfma_f32_16x16x32_bf16 v[204:207], a[84:87], a[20:23], v[204:207]
v_cvt_pk_bf16_f32 v225, v194, v195
s_add_u32 m0, m0, 1024
v_mfma_f32_16x16x32_bf16 v[204:207], a[88:91], a[24:27], v[204:207]
v_exp_f32 v200, v200
v_mfma_f32_16x16x32_bf16 v[204:207], a[92:95], a[28:31], v[204:207]
buffer_load_dwordx4 v240, s[56:59], s17 offen lds
v_mfma_f32_16x16x32_bf16 v[208:211], a[64:67], a[32:35], 0
v_exp_f32 v201, v201
s_add_u32 s17, s17, 4294942720
v_mfma_f32_16x16x32_bf16 v[208:211], a[68:71], a[36:39], v[208:211]
v_cvt_pk_bf16_f32 v228, v200, v201
s_add_u32 m0, m0, 1152
v_mfma_f32_16x16x32_bf16 v[208:211], a[72:75], a[40:43], v[208:211]
v_exp_f32 v202, v202
v_mfma_f32_16x16x32_bf16 v[208:211], a[76:79], a[44:47], v[208:211]
buffer_load_dwordx4 v240, s[56:59], s17 offen lds
v_mfma_f32_16x16x32_bf16 v[216:219], a[64:67], a[48:51], 0
v_exp_f32 v203, v203
s_add_u32 s17, s17, 32768
v_mfma_f32_16x16x32_bf16 v[216:219], a[68:71], a[52:55], v[216:219]
v_cvt_pk_bf16_f32 v229, v202, v203
s_add_u32 m0, m0, 1024
v_mfma_f32_16x16x32_bf16 v[216:219], a[72:75], a[56:59], v[216:219]
v_exp_f32 v196, v196
v_mfma_f32_16x16x32_bf16 v[216:219], a[76:79], a[60:63], v[216:219]
buffer_load_dwordx4 v240, s[56:59], s17 offen lds
v_mfma_f32_16x16x32_bf16 v[212:215], a[80:83], a[32:35], 0
v_exp_f32 v197, v197
s_add_u32 s17, s17, 90112
v_mfma_f32_16x16x32_bf16 v[212:215], a[84:87], a[36:39], v[212:215]
v_cvt_pk_bf16_f32 v226, v196, v197
s_mov_b32 s93, 64512
s_add_u32 m0, s83, 33792
v_mfma_f32_16x16x32_bf16 v[212:215], a[88:91], a[40:43], v[212:215]
v_exp_f32 v198, v198
v_mfma_f32_16x16x32_bf16 v[212:215], a[92:95], a[44:47], v[212:215]
buffer_load_dwordx4 a[164:167], v254, s[40:43], s93 offen
v_mfma_f32_16x16x32_bf16 v[220:223], a[80:83], a[48:51], 0
v_exp_f32 v199, v199
v_mfma_f32_16x16x32_bf16 v[220:223], a[84:87], a[52:55], v[220:223]
v_cvt_pk_bf16_f32 v227, v198, v199
v_mfma_f32_16x16x32_bf16 v[220:223], a[88:91], a[56:59], v[220:223]
v_exp_f32 v204, v204
v_mfma_f32_16x16x32_bf16 v[220:223], a[92:95], a[60:63], v[220:223]
s_waitcnt vmcnt(16)
s_barrier
v_mfma_f32_16x16x32_bf16 v[64:67], a[96:99], v[232:235], v[64:67]
v_exp_f32 v205, v205
v_mfma_f32_16x16x32_bf16 v[96:99], a[96:99], v[236:239], v[96:99]
ds_read_b128 a[96:99], v248 offset:8192
v_cvt_pk_bf16_f32 v230, v204, v205
v_mfma_f32_16x16x32_bf16 v[68:71], a[100:103], v[232:235], v[68:71]
v_exp_f32 v206, v206
v_mfma_f32_16x16x32_bf16 v[100:103], a[100:103], v[236:239], v[100:103]
ds_read_b128 a[100:103], v248 offset:9216
v_mfma_f32_16x16x32_bf16 v[72:75], a[104:107], v[232:235], v[72:75]
v_exp_f32 v207, v207
v_mfma_f32_16x16x32_bf16 v[104:107], a[104:107], v[236:239], v[104:107]
ds_read_b128 a[104:107], v248 offset:10240
v_cvt_pk_bf16_f32 v231, v206, v207
s_mov_b32 s93, 71680
v_mfma_f32_16x16x32_bf16 v[76:79], a[108:111], v[232:235], v[76:79]
v_exp_f32 v208, v208
v_mfma_f32_16x16x32_bf16 v[108:111], a[108:111], v[236:239], v[108:111]
ds_read_b128 a[108:111], v248 offset:11264
buffer_load_dwordx4 a[168:171], v254, s[40:43], s93 offen
v_mfma_f32_16x16x32_bf16 v[80:83], a[112:115], v[232:235], v[80:83]
v_exp_f32 v209, v209
v_mfma_f32_16x16x32_bf16 v[112:115], a[112:115], v[236:239], v[112:115]
ds_read_b128 a[112:115], v248 offset:12288
s_mov_b32 s93, 78848
v_mfma_f32_16x16x32_bf16 v[84:87], a[116:119], v[232:235], v[84:87]
v_exp_f32 v210, v210
v_mfma_f32_16x16x32_bf16 v[116:119], a[116:119], v[236:239], v[116:119]
ds_read_b128 a[116:119], v248 offset:13312
buffer_load_dwordx4 a[172:175], v254, s[40:43], s93 offen
v_mfma_f32_16x16x32_bf16 v[88:91], a[120:123], v[232:235], v[88:91]
v_exp_f32 v211, v211
v_mfma_f32_16x16x32_bf16 v[120:123], a[120:123], v[236:239], v[120:123]
ds_read_b128 a[120:123], v248 offset:14336
v_mfma_f32_16x16x32_bf16 v[92:95], a[124:127], v[232:235], v[92:95]
v_exp_f32 v216, v216
v_mfma_f32_16x16x32_bf16 v[124:127], a[124:127], v[236:239], v[124:127]
ds_read_b128 a[124:127], v248 offset:15360
v_or_b32 v248, s89, v255
v_smfmac_f32_16x16x64_bf16 a[196:199], a[208:211], v[232:239], v241
s_waitcnt lgkmcnt(4)
v_smfmac_f32_16x16x64_bf16 a[192:195], a[208:211], v[224:231], v241
ds_read_b128 a[64:67], v248 offset:0
v_mfma_f32_16x16x32_bf16 v[0:3], a[96:99], v[224:227], v[0:3]
v_exp_f32 v217, v217
v_mfma_f32_16x16x32_bf16 v[32:35], a[96:99], v[228:231], v[32:35]
ds_read_b128 a[68:71], v248 offset:1024
v_cvt_pk_bf16_f32 v232, v208, v209
v_mfma_f32_16x16x32_bf16 v[4:7], a[100:103], v[224:227], v[4:7]
v_exp_f32 v218, v218
v_mfma_f32_16x16x32_bf16 v[36:39], a[100:103], v[228:231], v[36:39]
ds_read_b128 a[72:75], v248 offset:2048
v_cvt_pk_bf16_f32 v233, v210, v211
v_mfma_f32_16x16x32_bf16 v[8:11], a[104:107], v[224:227], v[8:11]
v_exp_f32 v219, v219
v_mfma_f32_16x16x32_bf16 v[40:43], a[104:107], v[228:231], v[40:43]
ds_read_b128 a[76:79], v248 offset:3072
v_cvt_pk_bf16_f32 v236, v216, v217
v_mfma_f32_16x16x32_bf16 v[12:15], a[108:111], v[224:227], v[12:15]
v_exp_f32 v212, v212
v_mfma_f32_16x16x32_bf16 v[44:47], a[108:111], v[228:231], v[44:47]
ds_read_b128 a[80:83], v248 offset:4096
s_waitcnt lgkmcnt(5)
v_mfma_f32_16x16x32_bf16 v[16:19], a[112:115], v[224:227], v[16:19]
v_exp_f32 v213, v213
v_mfma_f32_16x16x32_bf16 v[48:51], a[112:115], v[228:231], v[48:51]
ds_read_b128 a[84:87], v248 offset:5120
v_cvt_pk_bf16_f32 v237, v218, v219
v_mfma_f32_16x16x32_bf16 v[20:23], a[116:119], v[224:227], v[20:23]
v_exp_f32 v214, v214
v_mfma_f32_16x16x32_bf16 v[52:55], a[116:119], v[228:231], v[52:55]
ds_read_b128 a[88:91], v248 offset:6144
v_cvt_pk_bf16_f32 v234, v212, v213
v_mfma_f32_16x16x32_bf16 v[24:27], a[120:123], v[224:227], v[24:27]
v_exp_f32 v215, v215
v_mfma_f32_16x16x32_bf16 v[56:59], a[120:123], v[228:231], v[56:59]
ds_read_b128 a[92:95], v248 offset:7168
v_cvt_pk_bf16_f32 v235, v214, v215
v_mfma_f32_16x16x32_bf16 v[28:31], a[124:127], v[224:227], v[28:31]
v_exp_f32 v220, v220
v_mfma_f32_16x16x32_bf16 v[60:63], a[124:127], v[228:231], v[60:63]
s_waitcnt lgkmcnt(4)
v_mfma_f32_16x16x32_bf16 v[192:195], a[64:67], a[0:3], 0
v_exp_f32 v221, v221
v_mfma_f32_16x16x32_bf16 v[192:195], a[68:71], a[4:7], v[192:195]
v_cvt_pk_bf16_f32 v238, v220, v221
s_add_u32 s89, s89, 16384
v_mfma_f32_16x16x32_bf16 v[192:195], a[72:75], a[8:11], v[192:195]
v_exp_f32 v222, v222
v_mfma_f32_16x16x32_bf16 v[192:195], a[76:79], a[12:15], v[192:195]
buffer_load_dwordx4 v240, s[56:59], s17 offen lds
v_mfma_f32_16x16x32_bf16 v[200:203], a[64:67], a[16:19], 0
v_exp_f32 v223, v223
s_add_u32 s17, s17, 32768
v_mfma_f32_16x16x32_bf16 v[200:203], a[68:71], a[20:23], v[200:203]
v_cvt_pk_bf16_f32 v239, v222, v223
s_add_u32 m0, m0, 1024
v_mfma_f32_16x16x32_bf16 v[200:203], a[72:75], a[24:27], v[200:203]
v_exp_f32 v192, v192
v_mfma_f32_16x16x32_bf16 v[200:203], a[76:79], a[28:31], v[200:203]
s_waitcnt lgkmcnt(0)
s_and_b32 s89, s89, 0x1ffff
v_mfma_f32_16x16x32_bf16 v[196:199], a[80:83], a[0:3], 0
v_exp_f32 v193, v193
v_mfma_f32_16x16x32_bf16 v[196:199], a[84:87], a[4:7], v[196:199]
v_cvt_pk_bf16_f32 v224, v192, v193
v_mfma_f32_16x16x32_bf16 v[196:199], a[88:91], a[8:11], v[196:199]
v_exp_f32 v194, v194
v_mfma_f32_16x16x32_bf16 v[196:199], a[92:95], a[12:15], v[196:199]
buffer_load_dwordx4 v240, s[56:59], s17 offen lds
v_mfma_f32_16x16x32_bf16 v[204:207], a[80:83], a[16:19], 0
v_exp_f32 v195, v195
s_add_u32 s17, s17, 4294942720
v_mfma_f32_16x16x32_bf16 v[204:207], a[84:87], a[20:23], v[204:207]
v_cvt_pk_bf16_f32 v225, v194, v195
s_add_u32 m0, m0, 1152
v_mfma_f32_16x16x32_bf16 v[204:207], a[88:91], a[24:27], v[204:207]
v_exp_f32 v200, v200
v_mfma_f32_16x16x32_bf16 v[204:207], a[92:95], a[28:31], v[204:207]
buffer_load_dwordx4 v240, s[56:59], s17 offen lds
.La5_LW2_8:
s_cbranch_execz .La5_LW2_12
v_mfma_f32_16x16x32_bf16 v[208:211], a[64:67], a[32:35], 0
v_exp_f32 v201, v201
s_add_u32 s17, s17, 32768
v_mfma_f32_16x16x32_bf16 v[208:211], a[68:71], a[36:39], v[208:211]
v_cvt_pk_bf16_f32 v228, v200, v201
s_add_u32 m0, m0, 1024
v_mfma_f32_16x16x32_bf16 v[208:211], a[72:75], a[40:43], v[208:211]
v_exp_f32 v202, v202
v_mfma_f32_16x16x32_bf16 v[208:211], a[76:79], a[44:47], v[208:211]
buffer_load_dwordx4 v240, s[56:59], s17 offen lds
v_mfma_f32_16x16x32_bf16 v[216:219], a[64:67], a[48:51], 0
v_exp_f32 v203, v203
v_mfma_f32_16x16x32_bf16 v[216:219], a[68:71], a[52:55], v[216:219]
v_cvt_pk_bf16_f32 v229, v202, v203
s_mov_b32 s93, 86016
v_mfma_f32_16x16x32_bf16 v[216:219], a[72:75], a[56:59], v[216:219]
v_exp_f32 v196, v196
v_mfma_f32_16x16x32_bf16 v[216:219], a[76:79], a[60:63], v[216:219]
buffer_load_dwordx4 a[176:179], v254, s[40:43], s93 offen
v_mfma_f32_16x16x32_bf16 v[212:215], a[80:83], a[32:35], 0
v_exp_f32 v197, v197
s_bfe_u32 s17, s4, 262148
v_mfma_f32_16x16x32_bf16 v[212:215], a[84:87], a[36:39], v[212:215]
v_cvt_pk_bf16_f32 v226, v196, v197
s_mov_b32 s93, 93184
s_xor_b32 s17, s17, s72
v_mfma_f32_16x16x32_bf16 v[212:215], a[88:91], a[40:43], v[212:215]
v_exp_f32 v198, v198
s_lshl_b32 s17, s17, 18
v_mfma_f32_16x16x32_bf16 v[212:215], a[92:95], a[44:47], v[212:215]
buffer_load_dwordx4 a[180:183], v254, s[40:43], s93 offen
s_add_u32 m0, s83, 50688
v_mfma_f32_16x16x32_bf16 v[220:223], a[80:83], a[48:51], 0
v_exp_f32 v199, v199
v_mfma_f32_16x16x32_bf16 v[220:223], a[84:87], a[52:55], v[220:223]
v_cvt_pk_bf16_f32 v227, v198, v199
v_mfma_f32_16x16x32_bf16 v[220:223], a[88:91], a[56:59], v[220:223]
v_exp_f32 v204, v204
v_mfma_f32_16x16x32_bf16 v[220:223], a[92:95], a[60:63], v[220:223]
s_waitcnt vmcnt(16)
s_barrier
v_mfma_f32_16x16x32_bf16 v[64:67], a[96:99], v[232:235], v[64:67]
v_exp_f32 v205, v205
v_mfma_f32_16x16x32_bf16 v[96:99], a[96:99], v[236:239], v[96:99]
ds_read_b128 a[96:99], v248 offset:8192
v_cvt_pk_bf16_f32 v230, v204, v205
v_mfma_f32_16x16x32_bf16 v[68:71], a[100:103], v[232:235], v[68:71]
v_exp_f32 v206, v206
v_mfma_f32_16x16x32_bf16 v[100:103], a[100:103], v[236:239], v[100:103]
ds_read_b128 a[100:103], v248 offset:9216
v_mfma_f32_16x16x32_bf16 v[72:75], a[104:107], v[232:235], v[72:75]
v_exp_f32 v207, v207
v_mfma_f32_16x16x32_bf16 v[104:107], a[104:107], v[236:239], v[104:107]
ds_read_b128 a[104:107], v248 offset:10240
v_cvt_pk_bf16_f32 v231, v206, v207
s_mov_b32 s93, 100352
v_mfma_f32_16x16x32_bf16 v[76:79], a[108:111], v[232:235], v[76:79]
v_exp_f32 v208, v208
v_mfma_f32_16x16x32_bf16 v[108:111], a[108:111], v[236:239], v[108:111]
ds_read_b128 a[108:111], v248 offset:11264
buffer_load_dwordx4 a[184:187], v254, s[40:43], s93 offen
v_mfma_f32_16x16x32_bf16 v[80:83], a[112:115], v[232:235], v[80:83]
v_exp_f32 v209, v209
v_mfma_f32_16x16x32_bf16 v[112:115], a[112:115], v[236:239], v[112:115]
ds_read_b128 a[112:115], v248 offset:12288
v_mfma_f32_16x16x32_bf16 v[84:87], a[116:119], v[232:235], v[84:87]
v_exp_f32 v210, v210
v_mfma_f32_16x16x32_bf16 v[116:119], a[116:119], v[236:239], v[116:119]
ds_read_b128 a[116:119], v248 offset:13312
v_mfma_f32_16x16x32_bf16 v[88:91], a[120:123], v[232:235], v[88:91]
v_exp_f32 v211, v211
v_mfma_f32_16x16x32_bf16 v[120:123], a[120:123], v[236:239], v[120:123]
ds_read_b128 a[120:123], v248 offset:14336
v_mfma_f32_16x16x32_bf16 v[92:95], a[124:127], v[232:235], v[92:95]
v_exp_f32 v216, v216
v_mfma_f32_16x16x32_bf16 v[124:127], a[124:127], v[236:239], v[124:127]
ds_read_b128 a[124:127], v248 offset:15360
v_or_b32 v248, s89, v255
v_smfmac_f32_16x16x64_bf16 a[196:199], a[208:211], v[232:239], v241
s_waitcnt lgkmcnt(4)
v_smfmac_f32_16x16x64_bf16 a[192:195], a[208:211], v[224:231], v241
ds_read_b128 a[64:67], v248 offset:0
v_mfma_f32_16x16x32_bf16 v[0:3], a[96:99], v[224:227], v[0:3]
v_exp_f32 v217, v217
v_mfma_f32_16x16x32_bf16 v[32:35], a[96:99], v[228:231], v[32:35]
ds_read_b128 a[68:71], v248 offset:1024
v_cvt_pk_bf16_f32 v232, v208, v209
v_mfma_f32_16x16x32_bf16 v[4:7], a[100:103], v[224:227], v[4:7]
v_exp_f32 v218, v218
v_mfma_f32_16x16x32_bf16 v[36:39], a[100:103], v[228:231], v[36:39]
ds_read_b128 a[72:75], v248 offset:2048
v_cvt_pk_bf16_f32 v233, v210, v211
v_mfma_f32_16x16x32_bf16 v[8:11], a[104:107], v[224:227], v[8:11]
v_exp_f32 v219, v219
v_mfma_f32_16x16x32_bf16 v[40:43], a[104:107], v[228:231], v[40:43]
ds_read_b128 a[76:79], v248 offset:3072
v_cvt_pk_bf16_f32 v236, v216, v217
v_mfma_f32_16x16x32_bf16 v[12:15], a[108:111], v[224:227], v[12:15]
v_exp_f32 v212, v212
v_mfma_f32_16x16x32_bf16 v[44:47], a[108:111], v[228:231], v[44:47]
ds_read_b128 a[80:83], v248 offset:4096
s_waitcnt lgkmcnt(5)
v_mfma_f32_16x16x32_bf16 v[16:19], a[112:115], v[224:227], v[16:19]
v_exp_f32 v213, v213
v_mfma_f32_16x16x32_bf16 v[48:51], a[112:115], v[228:231], v[48:51]
ds_read_b128 a[84:87], v248 offset:5120
v_cvt_pk_bf16_f32 v237, v218, v219
v_mfma_f32_16x16x32_bf16 v[20:23], a[116:119], v[224:227], v[20:23]
v_exp_f32 v214, v214
v_mfma_f32_16x16x32_bf16 v[52:55], a[116:119], v[228:231], v[52:55]
ds_read_b128 a[88:91], v248 offset:6144
v_cvt_pk_bf16_f32 v234, v212, v213
v_mfma_f32_16x16x32_bf16 v[24:27], a[120:123], v[224:227], v[24:27]
v_exp_f32 v215, v215
v_mfma_f32_16x16x32_bf16 v[56:59], a[120:123], v[228:231], v[56:59]
ds_read_b128 a[92:95], v248 offset:7168
v_cvt_pk_bf16_f32 v235, v214, v215
v_mfma_f32_16x16x32_bf16 v[28:31], a[124:127], v[224:227], v[28:31]
v_exp_f32 v220, v220
v_mfma_f32_16x16x32_bf16 v[60:63], a[124:127], v[228:231], v[60:63]
s_waitcnt lgkmcnt(4)
v_mfma_f32_16x16x32_bf16 v[192:195], a[64:67], a[0:3], 0
v_exp_f32 v221, v221
v_mfma_f32_16x16x32_bf16 v[192:195], a[68:71], a[4:7], v[192:195]
v_cvt_pk_bf16_f32 v238, v220, v221
v_mfma_f32_16x16x32_bf16 v[192:195], a[72:75], a[8:11], v[192:195]
v_exp_f32 v222, v222
v_mfma_f32_16x16x32_bf16 v[192:195], a[76:79], a[12:15], v[192:195]
buffer_load_dwordx4 v240, s[56:59], s17 offen lds
v_mfma_f32_16x16x32_bf16 v[200:203], a[64:67], a[16:19], 0
v_exp_f32 v223, v223
s_add_u32 s17, s17, 32768
v_mfma_f32_16x16x32_bf16 v[200:203], a[68:71], a[20:23], v[200:203]
v_cvt_pk_bf16_f32 v239, v222, v223
s_add_u32 m0, m0, 1024
v_mfma_f32_16x16x32_bf16 v[200:203], a[72:75], a[24:27], v[200:203]
v_exp_f32 v192, v192
v_mfma_f32_16x16x32_bf16 v[200:203], a[76:79], a[28:31], v[200:203]
s_waitcnt lgkmcnt(0)
v_mfma_f32_16x16x32_bf16 v[196:199], a[80:83], a[0:3], 0
v_exp_f32 v193, v193
v_mfma_f32_16x16x32_bf16 v[196:199], a[84:87], a[4:7], v[196:199]
v_cvt_pk_bf16_f32 v224, v192, v193
v_mfma_f32_16x16x32_bf16 v[196:199], a[88:91], a[8:11], v[196:199]
v_exp_f32 v194, v194
v_mfma_f32_16x16x32_bf16 v[196:199], a[92:95], a[12:15], v[196:199]
buffer_load_dwordx4 v240, s[56:59], s17 offen lds
v_mfma_f32_16x16x32_bf16 v[204:207], a[80:83], a[16:19], 0
v_exp_f32 v195, v195
s_add_u32 s17, s17, 4294942720
v_mfma_f32_16x16x32_bf16 v[204:207], a[84:87], a[20:23], v[204:207]
v_cvt_pk_bf16_f32 v225, v194, v195
s_add_u32 m0, m0, 1152
v_mfma_f32_16x16x32_bf16 v[204:207], a[88:91], a[24:27], v[204:207]
v_exp_f32 v200, v200
v_mfma_f32_16x16x32_bf16 v[204:207], a[92:95], a[28:31], v[204:207]
buffer_load_dwordx4 v240, s[56:59], s17 offen lds
v_mfma_f32_16x16x32_bf16 v[208:211], a[64:67], a[32:35], 0
v_exp_f32 v201, v201
s_add_u32 s17, s17, 32768
v_mfma_f32_16x16x32_bf16 v[208:211], a[68:71], a[36:39], v[208:211]
v_cvt_pk_bf16_f32 v228, v200, v201
s_add_u32 m0, m0, 1024
v_mfma_f32_16x16x32_bf16 v[208:211], a[72:75], a[40:43], v[208:211]
v_exp_f32 v202, v202
v_mfma_f32_16x16x32_bf16 v[208:211], a[76:79], a[44:47], v[208:211]
buffer_load_dwordx4 v240, s[56:59], s17 offen lds
v_mfma_f32_16x16x32_bf16 v[216:219], a[64:67], a[48:51], 0
v_exp_f32 v203, v203
v_mfma_f32_16x16x32_bf16 v[216:219], a[68:71], a[52:55], v[216:219]
v_cvt_pk_bf16_f32 v229, v202, v203
s_mov_b32 s93, 107520
v_mfma_f32_16x16x32_bf16 v[216:219], a[72:75], a[56:59], v[216:219]
v_exp_f32 v196, v196
v_mfma_f32_16x16x32_bf16 v[216:219], a[76:79], a[60:63], v[216:219]
buffer_load_dwordx4 a[188:191], v254, s[40:43], s93 offen
v_mfma_f32_16x16x32_bf16 v[212:215], a[80:83], a[32:35], 0
v_exp_f32 v197, v197
v_mfma_f32_16x16x32_bf16 v[212:215], a[84:87], a[36:39], v[212:215]
v_cvt_pk_bf16_f32 v226, v196, v197
v_mfma_f32_16x16x32_bf16 v[212:215], a[88:91], a[40:43], v[212:215]
v_exp_f32 v198, v198
v_mfma_f32_16x16x32_bf16 v[212:215], a[92:95], a[44:47], v[212:215]
v_mfma_f32_16x16x32_bf16 v[220:223], a[80:83], a[48:51], 0
v_exp_f32 v199, v199
v_mfma_f32_16x16x32_bf16 v[220:223], a[84:87], a[52:55], v[220:223]
v_cvt_pk_bf16_f32 v227, v198, v199
v_mfma_f32_16x16x32_bf16 v[220:223], a[88:91], a[56:59], v[220:223]
v_exp_f32 v204, v204
v_mfma_f32_16x16x32_bf16 v[220:223], a[92:95], a[60:63], v[220:223]
v_mfma_f32_16x16x32_bf16 v[64:67], a[96:99], v[232:235], v[64:67]
v_exp_f32 v205, v205
v_mfma_f32_16x16x32_bf16 v[96:99], a[96:99], v[236:239], v[96:99]
ds_read_b128 a[96:99], v248 offset:8192
v_cvt_pk_bf16_f32 v230, v204, v205
v_mfma_f32_16x16x32_bf16 v[68:71], a[100:103], v[232:235], v[68:71]
v_exp_f32 v206, v206
v_mfma_f32_16x16x32_bf16 v[100:103], a[100:103], v[236:239], v[100:103]
ds_read_b128 a[100:103], v248 offset:9216
v_mfma_f32_16x16x32_bf16 v[72:75], a[104:107], v[232:235], v[72:75]
v_exp_f32 v207, v207
v_mfma_f32_16x16x32_bf16 v[104:107], a[104:107], v[236:239], v[104:107]
ds_read_b128 a[104:107], v248 offset:10240
v_cvt_pk_bf16_f32 v231, v206, v207
v_mfma_f32_16x16x32_bf16 v[76:79], a[108:111], v[232:235], v[76:79]
v_exp_f32 v208, v208
v_mfma_f32_16x16x32_bf16 v[108:111], a[108:111], v[236:239], v[108:111]
ds_read_b128 a[108:111], v248 offset:11264
v_mfma_f32_16x16x32_bf16 v[80:83], a[112:115], v[232:235], v[80:83]
v_exp_f32 v209, v209
v_mfma_f32_16x16x32_bf16 v[112:115], a[112:115], v[236:239], v[112:115]
ds_read_b128 a[112:115], v248 offset:12288
v_mfma_f32_16x16x32_bf16 v[84:87], a[116:119], v[232:235], v[84:87]
v_exp_f32 v210, v210
v_mfma_f32_16x16x32_bf16 v[116:119], a[116:119], v[236:239], v[116:119]
ds_read_b128 a[116:119], v248 offset:13312
v_mfma_f32_16x16x32_bf16 v[88:91], a[120:123], v[232:235], v[88:91]
v_exp_f32 v211, v211
v_mfma_f32_16x16x32_bf16 v[120:123], a[120:123], v[236:239], v[120:123]
ds_read_b128 a[120:123], v248 offset:14336
v_mfma_f32_16x16x32_bf16 v[92:95], a[124:127], v[232:235], v[92:95]
v_exp_f32 v216, v216
v_mfma_f32_16x16x32_bf16 v[124:127], a[124:127], v[236:239], v[124:127]
ds_read_b128 a[124:127], v248 offset:15360
v_smfmac_f32_16x16x64_bf16 a[196:199], a[208:211], v[232:239], v241
s_waitcnt lgkmcnt(4)
v_smfmac_f32_16x16x64_bf16 a[192:195], a[208:211], v[224:231], v241
v_mfma_f32_16x16x32_bf16 v[0:3], a[96:99], v[224:227], v[0:3]
v_exp_f32 v217, v217
v_mfma_f32_16x16x32_bf16 v[32:35], a[96:99], v[228:231], v[32:35]
v_cvt_pk_bf16_f32 v232, v208, v209
v_mfma_f32_16x16x32_bf16 v[4:7], a[100:103], v[224:227], v[4:7]
v_exp_f32 v218, v218
v_mfma_f32_16x16x32_bf16 v[36:39], a[100:103], v[228:231], v[36:39]
v_cvt_pk_bf16_f32 v233, v210, v211
v_mfma_f32_16x16x32_bf16 v[8:11], a[104:107], v[224:227], v[8:11]
v_exp_f32 v219, v219
v_mfma_f32_16x16x32_bf16 v[40:43], a[104:107], v[228:231], v[40:43]
v_cvt_pk_bf16_f32 v236, v216, v217
v_mfma_f32_16x16x32_bf16 v[12:15], a[108:111], v[224:227], v[12:15]
v_exp_f32 v212, v212
v_mfma_f32_16x16x32_bf16 v[44:47], a[108:111], v[228:231], v[44:47]
s_waitcnt lgkmcnt(0)
v_mfma_f32_16x16x32_bf16 v[16:19], a[112:115], v[224:227], v[16:19]
v_exp_f32 v213, v213
v_mfma_f32_16x16x32_bf16 v[48:51], a[112:115], v[228:231], v[48:51]
v_cvt_pk_bf16_f32 v237, v218, v219
v_mfma_f32_16x16x32_bf16 v[20:23], a[116:119], v[224:227], v[20:23]
v_exp_f32 v214, v214
v_mfma_f32_16x16x32_bf16 v[52:55], a[116:119], v[228:231], v[52:55]
v_cvt_pk_bf16_f32 v234, v212, v213
v_mfma_f32_16x16x32_bf16 v[24:27], a[120:123], v[224:227], v[24:27]
v_exp_f32 v215, v215
v_mfma_f32_16x16x32_bf16 v[56:59], a[120:123], v[228:231], v[56:59]
v_cvt_pk_bf16_f32 v235, v214, v215
v_mfma_f32_16x16x32_bf16 v[28:31], a[124:127], v[224:227], v[28:31]
v_exp_f32 v220, v220
v_mfma_f32_16x16x32_bf16 v[60:63], a[124:127], v[228:231], v[60:63]
; ---- helper: partial O and l go to the partner
.La5_LA_DP:
v_exp_f32 v221, v221
v_exp_f32 v222, v222
v_exp_f32 v223, v223
v_cvt_pk_bf16_f32 v238, v220, v221
s_nop 0
v_cvt_pk_bf16_f32 v239, v222, v223
s_nop 1
v_smfmac_f32_16x16x64_bf16 a[196:199], a[208:211], v[232:239], v241
v_cvt_pk_bf16_f32 v0, v0, v1
v_cvt_pk_bf16_f32 v1, v2, v3
v_cvt_pk_bf16_f32 v2, v4, v5
v_cvt_pk_bf16_f32 v3, v6, v7
buffer_store_dwordx4 v[0:3], v255, s[68:71], 0 offen offset:0 sc1
v_mfma_f32_16x16x32_bf16 v[64:67], a[96:99], v[232:235], v[64:67]
v_cvt_pk_bf16_f32 v8, v8, v9
v_cvt_pk_bf16_f32 v9, v10, v11
v_cvt_pk_bf16_f32 v10, v12, v13
v_cvt_pk_bf16_f32 v11, v14, v15
buffer_store_dwordx4 v[8:11], v255, s[68:71], 0 offen offset:1024 sc1
v_mfma_f32_16x16x32_bf16 v[96:99], a[96:99], v[236:239], v[96:99]
v_accvgpr_read_b32 v240, a224
v_xor_b32 v241, 32, v240
v_cvt_pk_bf16_f32 v16, v16, v17
v_cvt_pk_bf16_f32 v17, v18, v19
v_cvt_pk_bf16_f32 v18, v20, v21
v_cvt_pk_bf16_f32 v19, v22, v23
v_mfma_f32_16x16x32_bf16 v[68:71], a[100:103], v[232:235], v[68:71]
v_xor_b32 v242, 64, v240
v_xor_b32 v243, 96, v240
buffer_store_dwordx4 v[16:19], v255, s[68:71], 0 offen offset:2048 sc1
v_cvt_pk_bf16_f32 v24, v24, v25
v_cvt_pk_bf16_f32 v25, v26, v27
v_mfma_f32_16x16x32_bf16 v[100:103], a[100:103], v[236:239], v[100:103]
v_cvt_pk_bf16_f32 v26, v28, v29
v_cvt_pk_bf16_f32 v27, v30, v31
buffer_store_dwordx4 v[24:27], v255, s[68:71], 0 offen offset:3072 sc1
v_cvt_pk_bf16_f32 v32, v32, v33
v_cvt_pk_bf16_f32 v33, v34, v35
v_mfma_f32_16x16x32_bf16 v[72:75], a[104:107], v[232:235], v[72:75]
v_cvt_pk_bf16_f32 v34, v36, v37
v_cvt_pk_bf16_f32 v35, v38, v39
buffer_store_dwordx4 v[32:35], v255, s[68:71], s95 offen offset:0 sc1
v_cvt_pk_bf16_f32 v40, v40, v41
v_cvt_pk_bf16_f32 v41, v42, v43
v_mfma_f32_16x16x32_bf16 v[104:107], a[104:107], v[236:239], v[104:107]
s_waitcnt vmcnt(13)
s_barrier
v_cvt_pk_bf16_f32 v42, v44, v45
v_cvt_pk_bf16_f32 v43, v46, v47
buffer_store_dwordx4 v[40:43], v255, s[68:71], s95 offen offset:1024 sc1
v_mfma_f32_16x16x32_bf16 v[76:79], a[108:111], v[232:235], v[76:79]
ds_read_b64_tr_b16 v[192:193], v240 offset:16896
ds_read_b64_tr_b16 v[194:195], v240 offset:17920
v_cvt_pk_bf16_f32 v48, v48, v49
v_cvt_pk_bf16_f32 v49, v50, v51
v_cvt_pk_bf16_f32 v50, v52, v53
v_cvt_pk_bf16_f32 v51, v54, v55
v_mfma_f32_16x16x32_bf16 v[108:111], a[108:111], v[236:239], v[108:111]
ds_read_b64_tr_b16 v[196:197], v241 offset:16896
ds_read_b64_tr_b16 v[198:199], v241 offset:17920
buffer_store_dwordx4 v[48:51], v255, s[68:71], s95 offen offset:2048 sc1
v_cvt_pk_bf16_f32 v56, v56, v57
v_cvt_pk_bf16_f32 v57, v58, v59
v_mfma_f32_16x16x32_bf16 v[80:83], a[112:115], v[232:235], v[80:83]
ds_read_b64_tr_b16 v[200:201], v242 offset:16896
ds_read_b64_tr_b16 v[202:203], v242 offset:17920
v_cvt_pk_bf16_f32 v58, v60, v61
v_cvt_pk_bf16_f32 v59, v62, v63
buffer_store_dwordx4 v[56:59], v255, s[68:71], s95 offen offset:3072 sc1
v_mfma_f32_16x16x32_bf16 v[112:115], a[112:115], v[236:239], v[112:115]
ds_read_b64_tr_b16 v[204:205], v243 offset:16896
ds_read_b64_tr_b16 v[206:207], v243 offset:17920
v_mfma_f32_16x16x32_bf16 v[84:87], a[116:119], v[232:235], v[84:87]
ds_read_b64_tr_b16 v[208:209], v240 offset:17024
ds_read_b64_tr_b16 v[210:211], v240 offset:18048
v_mfma_f32_16x16x32_bf16 v[116:119], a[116:119], v[236:239], v[116:119]
ds_read_b64_tr_b16 v[212:213], v241 offset:17024
ds_read_b64_tr_b16 v[214:215], v241 offset:18048
v_mfma_f32_16x16x32_bf16 v[88:91], a[120:123], v[232:235], v[88:91]
ds_read_b64_tr_b16 v[216:217], v242 offset:17024
ds_read_b64_tr_b16 v[218:219], v242 offset:18048
v_mfma_f32_16x16x32_bf16 v[120:123], a[120:123], v[236:239], v[120:123]
ds_read_b64_tr_b16 v[220:221], v243 offset:17024
ds_read_b64_tr_b16 v[222:223], v243 offset:18048
v_mfma_f32_16x16x32_bf16 v[92:95], a[124:127], v[232:235], v[92:95]
v_mfma_f32_16x16x32_bf16 v[124:127], a[124:127], v[236:239], v[124:127]
s_waitcnt lgkmcnt(14)
v_mfma_f32_16x16x32_bf16 v[0:3], v[192:195], v[128:131], 0
s_waitcnt lgkmcnt(12)
v_mfma_f32_16x16x32_bf16 v[32:35], v[192:195], v[144:147], 0
ds_read_b64_tr_b16 v[192:193], v240 offset:25344
ds_read_b64_tr_b16 v[194:195], v240 offset:26368
v_mfma_f32_16x16x32_bf16 v[4:7], v[196:199], v[128:131], 0
s_waitcnt lgkmcnt(12)
v_accvgpr_read_b32 v224, a192
v_accvgpr_read_b32 v228, a193
v_accvgpr_read_b32 v225, a194
v_accvgpr_read_b32 v229, a195
v_accvgpr_read_b32 v226, a196
v_mfma_f32_16x16x32_bf16 v[36:39], v[196:199], v[144:147], 0
ds_read_b64_tr_b16 v[196:197], v241 offset:25344
ds_read_b64_tr_b16 v[198:199], v241 offset:26368
v_accvgpr_read_b32 v230, a197
v_accvgpr_read_b32 v227, a198
v_accvgpr_read_b32 v231, a199
v_add_f32 v224, v224, v228
v_mfma_f32_16x16x32_bf16 v[8:11], v[200:203], v[128:131], 0
s_waitcnt lgkmcnt(12)
v_add_f32 v225, v225, v229
v_add_f32 v226, v226, v230
v_add_f32 v227, v227, v231
s_mov_b32 s92, 16384
buffer_store_dwordx4 v[224:227], v255, s[68:71], s92 offen sc1
v_mfma_f32_16x16x32_bf16 v[40:43], v[200:203], v[144:147], 0
ds_read_b64_tr_b16 v[200:201], v242 offset:25344
ds_read_b64_tr_b16 v[202:203], v242 offset:26368
v_cvt_pk_bf16_f32 v64, v64, v65
v_cvt_pk_bf16_f32 v65, v66, v67
v_cvt_pk_bf16_f32 v66, v68, v69
v_cvt_pk_bf16_f32 v67, v70, v71
v_mfma_f32_16x16x32_bf16 v[12:15], v[204:207], v[128:131], 0
s_waitcnt lgkmcnt(12)
buffer_store_dwordx4 v[64:67], v255, s[68:71], s96 offen offset:0 sc1
v_cvt_pk_bf16_f32 v72, v72, v73
v_cvt_pk_bf16_f32 v73, v74, v75
v_cvt_pk_bf16_f32 v74, v76, v77
v_mfma_f32_16x16x32_bf16 v[44:47], v[204:207], v[144:147], 0
ds_read_b64_tr_b16 v[204:205], v243 offset:25344
ds_read_b64_tr_b16 v[206:207], v243 offset:26368
v_cvt_pk_bf16_f32 v75, v78, v79
buffer_store_dwordx4 v[72:75], v255, s[68:71], s96 offen offset:1024 sc1
v_cvt_pk_bf16_f32 v80, v80, v81
v_mfma_f32_16x16x32_bf16 v[16:19], v[208:211], v[128:131], 0
s_waitcnt lgkmcnt(8)
v_cvt_pk_bf16_f32 v81, v82, v83
v_cvt_pk_bf16_f32 v82, v84, v85
v_cvt_pk_bf16_f32 v83, v86, v87
buffer_store_dwordx4 v[80:83], v255, s[68:71], s96 offen offset:2048 sc1
v_mfma_f32_16x16x32_bf16 v[48:51], v[208:211], v[144:147], 0
ds_read_b64_tr_b16 v[208:209], v240 offset:25472
ds_read_b64_tr_b16 v[210:211], v240 offset:26496
v_cvt_pk_bf16_f32 v88, v88, v89
v_cvt_pk_bf16_f32 v89, v90, v91
v_cvt_pk_bf16_f32 v90, v92, v93
v_cvt_pk_bf16_f32 v91, v94, v95
v_mfma_f32_16x16x32_bf16 v[20:23], v[212:215], v[128:131], 0
buffer_store_dwordx4 v[88:91], v255, s[68:71], s96 offen offset:3072 sc1
v_cvt_pk_bf16_f32 v96, v96, v97
v_cvt_pk_bf16_f32 v97, v98, v99
v_cvt_pk_bf16_f32 v98, v100, v101
v_cvt_pk_bf16_f32 v99, v102, v103
v_mfma_f32_16x16x32_bf16 v[52:55], v[212:215], v[144:147], 0
ds_read_b64_tr_b16 v[212:213], v241 offset:25472
ds_read_b64_tr_b16 v[214:215], v241 offset:26496
buffer_store_dwordx4 v[96:99], v255, s[68:71], s97 offen offset:0 sc1
v_cvt_pk_bf16_f32 v104, v104, v105
v_cvt_pk_bf16_f32 v105, v106, v107
v_mfma_f32_16x16x32_bf16 v[24:27], v[216:219], v[128:131], 0
v_cvt_pk_bf16_f32 v106, v108, v109
v_cvt_pk_bf16_f32 v107, v110, v111
buffer_store_dwordx4 v[104:107], v255, s[68:71], s97 offen offset:1024 sc1
v_cvt_pk_bf16_f32 v112, v112, v113
v_cvt_pk_bf16_f32 v113, v114, v115
v_mfma_f32_16x16x32_bf16 v[56:59], v[216:219], v[144:147], 0
ds_read_b64_tr_b16 v[216:217], v242 offset:25472
ds_read_b64_tr_b16 v[218:219], v242 offset:26496
v_cvt_pk_bf16_f32 v114, v116, v117
v_cvt_pk_bf16_f32 v115, v118, v119
buffer_store_dwordx4 v[112:115], v255, s[68:71], s97 offen offset:2048 sc1
v_mfma_f32_16x16x32_bf16 v[28:31], v[220:223], v[128:131], 0
s_waitcnt lgkmcnt(6)
v_cvt_pk_bf16_f32 v120, v120, v121
v_cvt_pk_bf16_f32 v121, v122, v123
v_cvt_pk_bf16_f32 v122, v124, v125
v_cvt_pk_bf16_f32 v123, v126, v127
buffer_store_dwordx4 v[120:123], v255, s[68:71], s97 offen offset:3072 sc1
v_mfma_f32_16x16x32_bf16 v[60:63], v[220:223], v[144:147], 0
ds_read_b64_tr_b16 v[220:221], v243 offset:25472
ds_read_b64_tr_b16 v[222:223], v243 offset:26496
v_mov_b32 v249, 0
s_add_u32 s6, s88, 393216
v_mfma_f32_16x16x32_bf16 v[0:3], v[192:195], v[132:135], v[0:3]
v_accvgpr_read_b32 v224, a228
v_add_u32 v225, 0x8000, v224
v_add_u32 v226, 0x2000, v224
v_add_u32 v227, 0xa000, v224
v_mfma_f32_16x16x32_bf16 v[32:35], v[192:195], v[148:151], v[32:35]
ds_read_b64_tr_b16 v[192:193], v240 offset:33792
ds_read_b64_tr_b16 v[194:195], v240 offset:34816
v_accvgpr_read_b32 v248, a225
v_accvgpr_read_b32 v228, a229
v_mfma_f32_16x16x32_bf16 v[4:7], v[196:199], v[132:135], v[4:7]
v_accvgpr_read_b32 v229, a230
v_mfma_f32_16x16x32_bf16 v[36:39], v[196:199], v[148:151], v[36:39]
ds_read_b64_tr_b16 v[196:197], v241 offset:33792
ds_read_b64_tr_b16 v[198:199], v241 offset:34816
v_mfma_f32_16x16x32_bf16 v[8:11], v[200:203], v[132:135], v[8:11]
v_mfma_f32_16x16x32_bf16 v[40:43], v[200:203], v[148:151], v[40:43]
ds_read_b64_tr_b16 v[200:201], v242 offset:33792
ds_read_b64_tr_b16 v[202:203], v242 offset:34816
v_mfma_f32_16x16x32_bf16 v[12:15], v[204:207], v[132:135], v[12:15]
s_waitcnt lgkmcnt(6)
v_mfma_f32_16x16x32_bf16 v[44:47], v[204:207], v[148:151], v[44:47]
ds_read_b64_tr_b16 v[204:205], v243 offset:33792
ds_read_b64_tr_b16 v[206:207], v243 offset:34816
v_mfma_f32_16x16x32_bf16 v[16:19], v[208:211], v[132:135], v[16:19]
s_waitcnt vmcnt(0)
buffer_store_dwordx2 v[252:253], v249, s[64:67], s6 offen offset:0 sc1
v_mfma_f32_16x16x32_bf16 v[48:51], v[208:211], v[148:151], v[48:51]
ds_read_b64_tr_b16 v[208:209], v240 offset:33920
ds_read_b64_tr_b16 v[210:211], v240 offset:34944
v_mfma_f32_16x16x32_bf16 v[20:23], v[212:215], v[132:135], v[20:23]
s_mov_b32 s93, 114688
buffer_load_dwordx4 a[192:195], v228, s[40:43], s93 offen
v_mfma_f32_16x16x32_bf16 v[52:55], v[212:215], v[148:151], v[52:55]
ds_read_b64_tr_b16 v[212:213], v241 offset:33920
ds_read_b64_tr_b16 v[214:215], v241 offset:34944
v_mfma_f32_16x16x32_bf16 v[24:27], v[216:219], v[132:135], v[24:27]
s_mov_b32 s93, 121856
buffer_load_dwordx4 a[196:199], v228, s[40:43], s93 offen
v_mfma_f32_16x16x32_bf16 v[56:59], v[216:219], v[148:151], v[56:59]
ds_read_b64_tr_b16 v[216:217], v242 offset:33920
ds_read_b64_tr_b16 v[218:219], v242 offset:34944
v_mfma_f32_16x16x32_bf16 v[28:31], v[220:223], v[132:135], v[28:31]
s_waitcnt lgkmcnt(6)
s_mov_b32 s93, 129024
buffer_load_dwordx4 a[200:203], v228, s[40:43], s93 offen
v_mfma_f32_16x16x32_bf16 v[60:63], v[220:223], v[148:151], v[60:63]
ds_read_b64_tr_b16 v[220:221], v243 offset:33920
ds_read_b64_tr_b16 v[222:223], v243 offset:34944
v_mfma_f32_16x16x32_bf16 v[0:3], v[192:195], v[136:139], v[0:3]
s_mov_b32 s93, 136192
buffer_load_dwordx4 a[204:207], v228, s[40:43], s93 offen
v_mfma_f32_16x16x32_bf16 v[32:35], v[192:195], v[152:155], v[32:35]
ds_read_b64_tr_b16 v[192:193], v240 offset:42240
ds_read_b64_tr_b16 v[194:195], v240 offset:43264
v_mfma_f32_16x16x32_bf16 v[4:7], v[196:199], v[136:139], v[4:7]
s_mov_b32 s93, 143360
buffer_load_dwordx4 a[208:211], v228, s[40:43], s93 offen
v_mfma_f32_16x16x32_bf16 v[36:39], v[196:199], v[152:155], v[36:39]
ds_read_b64_tr_b16 v[196:197], v241 offset:42240
ds_read_b64_tr_b16 v[198:199], v241 offset:43264
v_mfma_f32_16x16x32_bf16 v[8:11], v[200:203], v[136:139], v[8:11]
s_mov_b32 s93, 150528
buffer_load_dwordx4 a[212:215], v228, s[40:43], s93 offen
v_mfma_f32_16x16x32_bf16 v[40:43], v[200:203], v[152:155], v[40:43]
ds_read_b64_tr_b16 v[200:201], v242 offset:42240
ds_read_b64_tr_b16 v[202:203], v242 offset:43264
v_mfma_f32_16x16x32_bf16 v[12:15], v[204:207], v[136:139], v[12:15]
s_waitcnt lgkmcnt(6)
s_mov_b32 s93, 157696
buffer_load_dwordx4 a[216:219], v228, s[40:43], s93 offen
v_mfma_f32_16x16x32_bf16 v[44:47], v[204:207], v[152:155], v[44:47]
ds_read_b64_tr_b16 v[204:205], v243 offset:42240
ds_read_b64_tr_b16 v[206:207], v243 offset:43264
v_mfma_f32_16x16x32_bf16 v[16:19], v[208:211], v[136:139], v[16:19]
s_mov_b32 s93, 164864
buffer_load_dwordx4 a[220:223], v228, s[40:43], s93 offen
v_mfma_f32_16x16x32_bf16 v[48:51], v[208:211], v[152:155], v[48:51]
ds_read_b64_tr_b16 v[208:209], v240 offset:42368
ds_read_b64_tr_b16 v[210:211], v240 offset:43392
v_mfma_f32_16x16x32_bf16 v[20:23], v[212:215], v[136:139], v[20:23]
s_mov_b32 s93, 172032
buffer_load_dwordx4 a[224:227], v228, s[40:43], s93 offen
v_mfma_f32_16x16x32_bf16 v[52:55], v[212:215], v[152:155], v[52:55]
ds_read_b64_tr_b16 v[212:213], v241 offset:42368
ds_read_b64_tr_b16 v[214:215], v241 offset:43392
v_mfma_f32_16x16x32_bf16 v[24:27], v[216:219], v[136:139], v[24:27]
s_mov_b32 s93, 179200
buffer_load_dwordx4 a[228:231], v228, s[40:43], s93 offen
v_mfma_f32_16x16x32_bf16 v[56:59], v[216:219], v[152:155], v[56:59]
ds_read_b64_tr_b16 v[216:217], v242 offset:42368
ds_read_b64_tr_b16 v[218:219], v242 offset:43392
v_mfma_f32_16x16x32_bf16 v[28:31], v[220:223], v[136:139], v[28:31]
s_waitcnt lgkmcnt(6)
s_mov_b32 s93, 186368
buffer_load_dwordx4 a[232:235], v228, s[40:43], s93 offen
v_mfma_f32_16x16x32_bf16 v[60:63], v[220:223], v[152:155], v[60:63]
ds_read_b64_tr_b16 v[220:221], v243 offset:42368
ds_read_b64_tr_b16 v[222:223], v243 offset:43392
v_mfma_f32_16x16x32_bf16 v[0:3], v[192:195], v[140:143], v[0:3]
s_mov_b32 s93, 193536
buffer_load_dwordx4 a[236:239], v228, s[40:43], s93 offen
v_mfma_f32_16x16x32_bf16 v[32:35], v[192:195], v[156:159], v[32:35]
ds_read_b64_tr_b16 v[192:193], v240 offset:16896
ds_read_b64_tr_b16 v[194:195], v240 offset:17920
v_mfma_f32_16x16x32_bf16 v[4:7], v[196:199], v[140:143], v[4:7]
s_mov_b32 s93, 200704
buffer_load_dwordx4 a[240:243], v228, s[40:43], s93 offen
v_mfma_f32_16x16x32_bf16 v[36:39], v[196:199], v[156:159], v[36:39]
ds_read_b64_tr_b16 v[196:197], v241 offset:16896
ds_read_b64_tr_b16 v[198:199], v241 offset:17920
v_mfma_f32_16x16x32_bf16 v[8:11], v[200:203], v[140:143], v[8:11]
s_mov_b32 s93, 207872
buffer_load_dwordx4 a[244:247], v228, s[40:43], s93 offen
v_mfma_f32_16x16x32_bf16 v[40:43], v[200:203], v[156:159], v[40:43]
ds_read_b64_tr_b16 v[200:201], v242 offset:16896
ds_read_b64_tr_b16 v[202:203], v242 offset:17920
v_mfma_f32_16x16x32_bf16 v[12:15], v[204:207], v[140:143], v[12:15]
s_waitcnt lgkmcnt(6)
s_mov_b32 s93, 215040
.La5_LW2_9:
s_cbranch_execz .La5_LW2_13
buffer_load_dwordx4 a[248:251], v228, s[40:43], s93 offen
v_mfma_f32_16x16x32_bf16 v[44:47], v[204:207], v[156:159], v[44:47]
ds_read_b64_tr_b16 v[204:205], v243 offset:16896
ds_read_b64_tr_b16 v[206:207], v243 offset:17920
v_mfma_f32_16x16x32_bf16 v[16:19], v[208:211], v[140:143], v[16:19]
s_mov_b32 s93, 222208
buffer_load_dwordx4 a[252:255], v228, s[40:43], s93 offen
v_mfma_f32_16x16x32_bf16 v[48:51], v[208:211], v[156:159], v[48:51]
ds_read_b64_tr_b16 v[208:209], v240 offset:17024
ds_read_b64_tr_b16 v[210:211], v240 offset:18048
v_mfma_f32_16x16x32_bf16 v[20:23], v[212:215], v[140:143], v[20:23]
v_mfma_f32_16x16x32_bf16 v[52:55], v[212:215], v[156:159], v[52:55]
ds_read_b64_tr_b16 v[212:213], v241 offset:17024
ds_read_b64_tr_b16 v[214:215], v241 offset:18048
v_mfma_f32_16x16x32_bf16 v[24:27], v[216:219], v[140:143], v[24:27]
v_mfma_f32_16x16x32_bf16 v[56:59], v[216:219], v[156:159], v[56:59]
ds_read_b64_tr_b16 v[216:217], v242 offset:17024
ds_read_b64_tr_b16 v[218:219], v242 offset:18048
v_mfma_f32_16x16x32_bf16 v[28:31], v[220:223], v[140:143], v[28:31]
s_waitcnt lgkmcnt(6)
v_mfma_f32_16x16x32_bf16 v[60:63], v[220:223], v[156:159], v[60:63]
ds_read_b64_tr_b16 v[220:221], v243 offset:17024
ds_read_b64_tr_b16 v[222:223], v243 offset:18048
s_branch .La5_LO_START
; ---- owner: last window, partner-s part added, l checked, O / l leaves
.La5_LA_DC:
.La5_LW2_10:
s_cbranch_execz .La5_LW2_14
s_waitcnt lgkmcnt(4)
v_mfma_f32_16x16x32_bf16 v[192:195], a[64:67], v[128:131], 0
v_exp_f32 v221, v221
v_mfma_f32_16x16x32_bf16 v[192:195], a[68:71], v[132:135], v[192:195]
v_cvt_pk_bf16_f32 v238, v220, v221
s_add_u32 s89, s89, 16384
v_mfma_f32_16x16x32_bf16 v[192:195], a[72:75], v[136:139], v[192:195]
v_exp_f32 v222, v222
v_mfma_f32_16x16x32_bf16 v[192:195], a[76:79], v[140:143], v[192:195]
buffer_load_dwordx4 v255, s[60:63], s17 offen offset:0 sc1 lds
v_mfma_f32_16x16x32_bf16 v[200:203], a[64:67], v[144:147], 0
v_exp_f32 v223, v223
v_mfma_f32_16x16x32_bf16 v[200:203], a[68:71], v[148:151], v[200:203]
v_cvt_pk_bf16_f32 v239, v222, v223
s_and_b32 s89, s89, 0x1ffff
v_mfma_f32_16x16x32_bf16 v[200:203], a[72:75], v[152:155], v[200:203]
v_exp_f32 v192, v192
v_mfma_f32_16x16x32_bf16 v[200:203], a[76:79], v[156:159], v[200:203]
s_waitcnt lgkmcnt(0)
v_mfma_f32_16x16x32_bf16 v[196:199], a[80:83], v[128:131], 0
v_exp_f32 v193, v193
v_mfma_f32_16x16x32_bf16 v[196:199], a[84:87], v[132:135], v[196:199]
v_cvt_pk_bf16_f32 v224, v192, v193
v_accvgpr_read_b32 v254, a229
v_mfma_f32_16x16x32_bf16 v[196:199], a[88:91], v[136:139], v[196:199]
v_exp_f32 v194, v194
v_mfma_f32_16x16x32_bf16 v[196:199], a[92:95], v[140:143], v[196:199]
buffer_load_dwordx4 v255, s[60:63], s17 offen offset:1024 sc1 lds
v_mfma_f32_16x16x32_bf16 v[204:207], a[80:83], v[144:147], 0
v_exp_f32 v195, v195
v_mfma_f32_16x16x32_bf16 v[204:207], a[84:87], v[148:151], v[204:207]
v_cvt_pk_bf16_f32 v225, v194, v195
v_mfma_f32_16x16x32_bf16 v[204:207], a[88:91], v[152:155], v[204:207]
v_exp_f32 v200, v200
v_mfma_f32_16x16x32_bf16 v[204:207], a[92:95], v[156:159], v[204:207]
buffer_load_dwordx4 v255, s[60:63], s17 offen offset:2048 sc1 lds
v_mfma_f32_16x16x32_bf16 v[208:211], a[64:67], v[160:163], 0
v_exp_f32 v201, v201
v_mfma_f32_16x16x32_bf16 v[208:211], a[68:71], v[164:167], v[208:211]
v_cvt_pk_bf16_f32 v228, v200, v201
v_mfma_f32_16x16x32_bf16 v[208:211], a[72:75], v[168:171], v[208:211]
v_exp_f32 v202, v202
v_mfma_f32_16x16x32_bf16 v[208:211], a[76:79], v[172:175], v[208:211]
buffer_load_dwordx4 v255, s[60:63], s17 offen offset:3072 sc1 lds
v_mfma_f32_16x16x32_bf16 v[216:219], a[64:67], v[176:179], 0
v_exp_f32 v203, v203
v_mfma_f32_16x16x32_bf16 v[216:219], a[68:71], v[180:183], v[216:219]
v_cvt_pk_bf16_f32 v229, v202, v203
s_mov_b32 s93, 0
v_mfma_f32_16x16x32_bf16 v[216:219], a[72:75], v[184:187], v[216:219]
v_exp_f32 v196, v196
v_mfma_f32_16x16x32_bf16 v[216:219], a[76:79], v[188:191], v[216:219]
buffer_load_dwordx4 a[128:131], v254, s[40:43], s93 offen
v_mfma_f32_16x16x32_bf16 v[212:215], a[80:83], v[160:163], 0
v_exp_f32 v197, v197
s_add_u32 s17, s17, 8192
v_mfma_f32_16x16x32_bf16 v[212:215], a[84:87], v[164:167], v[212:215]
v_cvt_pk_bf16_f32 v226, v196, v197
s_mov_b32 s93, 7168
s_add_u32 m0, m0, 16384
v_mfma_f32_16x16x32_bf16 v[212:215], a[88:91], v[168:171], v[212:215]
v_exp_f32 v198, v198
s_and_b32 m0, m0, s78
v_mfma_f32_16x16x32_bf16 v[212:215], a[92:95], v[172:175], v[212:215]
buffer_load_dwordx4 a[132:135], v254, s[40:43], s93 offen
v_mfma_f32_16x16x32_bf16 v[220:223], a[80:83], v[176:179], 0
v_exp_f32 v199, v199
v_mfma_f32_16x16x32_bf16 v[220:223], a[84:87], v[180:183], v[220:223]
v_cvt_pk_bf16_f32 v227, v198, v199
v_mfma_f32_16x16x32_bf16 v[220:223], a[88:91], v[184:187], v[220:223]
v_exp_f32 v204, v204
v_mfma_f32_16x16x32_bf16 v[220:223], a[92:95], v[188:191], v[220:223]
s_waitcnt vmcnt(12)
s_barrier
v_mfma_f32_16x16x32_bf16 v[64:67], a[96:99], v[232:235], v[64:67]
v_exp_f32 v205, v205
v_mfma_f32_16x16x32_bf16 v[96:99], a[96:99], v[236:239], v[96:99]
ds_read_b128 a[96:99], v248 offset:8192
v_cvt_pk_bf16_f32 v230, v204, v205
v_mfma_f32_16x16x32_bf16 v[68:71], a[100:103], v[232:235], v[68:71]
v_exp_f32 v206, v206
v_mfma_f32_16x16x32_bf16 v[100:103], a[100:103], v[236:239], v[100:103]
ds_read_b128 a[100:103], v248 offset:9216
v_mfma_f32_16x16x32_bf16 v[72:75], a[104:107], v[232:235], v[72:75]
v_exp_f32 v207, v207
v_mfma_f32_16x16x32_bf16 v[104:107], a[104:107], v[236:239], v[104:107]
ds_read_b128 a[104:107], v248 offset:10240
v_cvt_pk_bf16_f32 v231, v206, v207
s_mov_b32 s93, 14336
v_mfma_f32_16x16x32_bf16 v[76:79], a[108:111], v[232:235], v[76:79]
v_exp_f32 v208, v208
v_mfma_f32_16x16x32_bf16 v[108:111], a[108:111], v[236:239], v[108:111]
ds_read_b128 a[108:111], v248 offset:11264
buffer_load_dwordx4 a[136:139], v254, s[40:43], s93 offen
v_mfma_f32_16x16x32_bf16 v[80:83], a[112:115], v[232:235], v[80:83]
v_exp_f32 v209, v209
v_mfma_f32_16x16x32_bf16 v[112:115], a[112:115], v[236:239], v[112:115]
ds_read_b128 a[112:115], v248 offset:12288
s_mov_b32 s93, 21504
v_mfma_f32_16x16x32_bf16 v[84:87], a[116:119], v[232:235], v[84:87]
v_exp_f32 v210, v210
v_mfma_f32_16x16x32_bf16 v[116:119], a[116:119], v[236:239], v[116:119]
ds_read_b128 a[116:119], v248 offset:13312
buffer_load_dwordx4 a[140:143], v254, s[40:43], s93 offen
v_mfma_f32_16x16x32_bf16 v[88:91], a[120:123], v[232:235], v[88:91]
v_exp_f32 v211, v211
v_mfma_f32_16x16x32_bf16 v[120:123], a[120:123], v[236:239], v[120:123]
ds_read_b128 a[120:123], v248 offset:14336
v_mfma_f32_16x16x32_bf16 v[92:95], a[124:127], v[232:235], v[92:95]
v_exp_f32 v216, v216
v_mfma_f32_16x16x32_bf16 v[124:127], a[124:127], v[236:239], v[124:127]
ds_read_b128 a[124:127], v248 offset:15360
v_or_b32 v248, s89, v255
v_smfmac_f32_16x16x64_bf16 a[196:199], a[208:211], v[232:239], v241
s_waitcnt lgkmcnt(4)
v_smfmac_f32_16x16x64_bf16 a[192:195], a[208:211], v[224:231], v241
ds_read_b128 a[64:67], v248 offset:0
v_mfma_f32_16x16x32_bf16 v[0:3], a[96:99], v[224:227], v[0:3]
v_exp_f32 v217, v217
v_mfma_f32_16x16x32_bf16 v[32:35], a[96:99], v[228:231], v[32:35]
ds_read_b128 a[68:71], v248 offset:1024
v_cvt_pk_bf16_f32 v232, v208, v209
v_mfma_f32_16x16x32_bf16 v[4:7], a[100:103], v[224:227], v[4:7]
v_exp_f32 v218, v218
v_mfma_f32_16x16x32_bf16 v[36:39], a[100:103], v[228:231], v[36:39]
ds_read_b128 a[72:75], v248 offset:2048
v_cvt_pk_bf16_f32 v233, v210, v211
v_mfma_f32_16x16x32_bf16 v[8:11], a[104:107], v[224:227], v[8:11]
v_exp_f32 v219, v219
v_mfma_f32_16x16x32_bf16 v[40:43], a[104:107], v[228:231], v[40:43]
ds_read_b128 a[76:79], v248 offset:3072
v_cvt_pk_bf16_f32 v236, v216, v217
v_mfma_f32_16x16x32_bf16 v[12:15], a[108:111], v[224:227], v[12:15]
v_exp_f32 v212, v212
v_mfma_f32_16x16x32_bf16 v[44:47], a[108:111], v[228:231], v[44:47]
ds_read_b128 a[80:83], v248 offset:4096
s_waitcnt lgkmcnt(5)
v_mfma_f32_16x16x32_bf16 v[16:19], a[112:115], v[224:227], v[16:19]
v_exp_f32 v213, v213
v_mfma_f32_16x16x32_bf16 v[48:51], a[112:115], v[228:231], v[48:51]
ds_read_b128 a[84:87], v248 offset:5120
v_cvt_pk_bf16_f32 v237, v218, v219
v_mfma_f32_16x16x32_bf16 v[20:23], a[116:119], v[224:227], v[20:23]
v_exp_f32 v214, v214
v_mfma_f32_16x16x32_bf16 v[52:55], a[116:119], v[228:231], v[52:55]
ds_read_b128 a[88:91], v248 offset:6144
v_cvt_pk_bf16_f32 v234, v212, v213
v_mfma_f32_16x16x32_bf16 v[24:27], a[120:123], v[224:227], v[24:27]
v_exp_f32 v215, v215
v_mfma_f32_16x16x32_bf16 v[56:59], a[120:123], v[228:231], v[56:59]
ds_read_b128 a[92:95], v248 offset:7168
v_cvt_pk_bf16_f32 v235, v214, v215
v_mfma_f32_16x16x32_bf16 v[28:31], a[124:127], v[224:227], v[28:31]
v_exp_f32 v220, v220
v_mfma_f32_16x16x32_bf16 v[60:63], a[124:127], v[228:231], v[60:63]
s_waitcnt lgkmcnt(4)
v_mfma_f32_16x16x32_bf16 v[192:195], a[64:67], v[128:131], 0
v_exp_f32 v221, v221
v_mfma_f32_16x16x32_bf16 v[192:195], a[68:71], v[132:135], v[192:195]
v_cvt_pk_bf16_f32 v238, v220, v221
s_add_u32 s89, s89, 16384
v_mfma_f32_16x16x32_bf16 v[192:195], a[72:75], v[136:139], v[192:195]
v_exp_f32 v222, v222
v_mfma_f32_16x16x32_bf16 v[192:195], a[76:79], v[140:143], v[192:195]
buffer_load_dwordx4 v255, s[60:63], s17 offen offset:0 sc1 lds
v_mfma_f32_16x16x32_bf16 v[200:203], a[64:67], v[144:147], 0
v_exp_f32 v223, v223
v_mfma_f32_16x16x32_bf16 v[200:203], a[68:71], v[148:151], v[200:203]
v_cvt_pk_bf16_f32 v239, v222, v223
s_and_b32 s89, s89, 0x1ffff
v_mfma_f32_16x16x32_bf16 v[200:203], a[72:75], v[152:155], v[200:203]
v_exp_f32 v192, v192
v_mfma_f32_16x16x32_bf16 v[200:203], a[76:79], v[156:159], v[200:203]
s_waitcnt lgkmcnt(0)
v_mfma_f32_16x16x32_bf16 v[196:199], a[80:83], v[128:131], 0
v_exp_f32 v193, v193
v_mfma_f32_16x16x32_bf16 v[196:199], a[84:87], v[132:135], v[196:199]
v_cvt_pk_bf16_f32 v224, v192, v193
v_mfma_f32_16x16x32_bf16 v[196:199], a[88:91], v[136:139], v[196:199]
v_exp_f32 v194, v194
v_mfma_f32_16x16x32_bf16 v[196:199], a[92:95], v[140:143], v[196:199]
buffer_load_dwordx4 v255, s[60:63], s17 offen offset:1024 sc1 lds
v_mfma_f32_16x16x32_bf16 v[204:207], a[80:83], v[144:147], 0
v_exp_f32 v195, v195
v_mfma_f32_16x16x32_bf16 v[204:207], a[84:87], v[148:151], v[204:207]
v_cvt_pk_bf16_f32 v225, v194, v195
v_mfma_f32_16x16x32_bf16 v[204:207], a[88:91], v[152:155], v[204:207]
v_exp_f32 v200, v200
v_mfma_f32_16x16x32_bf16 v[204:207], a[92:95], v[156:159], v[204:207]
buffer_load_dwordx4 v255, s[60:63], s17 offen offset:2048 sc1 lds
v_mfma_f32_16x16x32_bf16 v[208:211], a[64:67], v[160:163], 0
v_exp_f32 v201, v201
v_mfma_f32_16x16x32_bf16 v[208:211], a[68:71], v[164:167], v[208:211]
v_cvt_pk_bf16_f32 v228, v200, v201
v_mfma_f32_16x16x32_bf16 v[208:211], a[72:75], v[168:171], v[208:211]
v_exp_f32 v202, v202
v_mfma_f32_16x16x32_bf16 v[208:211], a[76:79], v[172:175], v[208:211]
buffer_load_dwordx4 v255, s[60:63], s17 offen offset:3072 sc1 lds
v_mfma_f32_16x16x32_bf16 v[216:219], a[64:67], v[176:179], 0
v_exp_f32 v203, v203
v_mfma_f32_16x16x32_bf16 v[216:219], a[68:71], v[180:183], v[216:219]
v_cvt_pk_bf16_f32 v229, v202, v203
s_mov_b32 s93, 28672
v_mfma_f32_16x16x32_bf16 v[216:219], a[72:75], v[184:187], v[216:219]
v_exp_f32 v196, v196
v_mfma_f32_16x16x32_bf16 v[216:219], a[76:79], v[188:191], v[216:219]
buffer_load_dwordx4 a[144:147], v254, s[40:43], s93 offen
v_mfma_f32_16x16x32_bf16 v[212:215], a[80:83], v[160:163], 0
v_exp_f32 v197, v197
s_add_u32 s17, s17, 8192
v_mfma_f32_16x16x32_bf16 v[212:215], a[84:87], v[164:167], v[212:215]
v_cvt_pk_bf16_f32 v226, v196, v197
s_mov_b32 s93, 35840
s_add_u32 m0, m0, 16384
v_mfma_f32_16x16x32_bf16 v[212:215], a[88:91], v[168:171], v[212:215]
v_exp_f32 v198, v198
s_and_b32 m0, m0, s78
v_mfma_f32_16x16x32_bf16 v[212:215], a[92:95], v[172:175], v[212:215]
buffer_load_dwordx4 a[148:151], v254, s[40:43], s93 offen
v_mfma_f32_16x16x32_bf16 v[220:223], a[80:83], v[176:179], 0
v_exp_f32 v199, v199
v_mfma_f32_16x16x32_bf16 v[220:223], a[84:87], v[180:183], v[220:223]
v_cvt_pk_bf16_f32 v227, v198, v199
v_mfma_f32_16x16x32_bf16 v[220:223], a[88:91], v[184:187], v[220:223]
v_exp_f32 v204, v204
v_mfma_f32_16x16x32_bf16 v[220:223], a[92:95], v[188:191], v[220:223]
s_waitcnt vmcnt(15)
s_barrier
v_mfma_f32_16x16x32_bf16 v[64:67], a[96:99], v[232:235], v[64:67]
v_exp_f32 v205, v205
v_mfma_f32_16x16x32_bf16 v[96:99], a[96:99], v[236:239], v[96:99]
ds_read_b128 a[96:99], v248 offset:8192
v_cvt_pk_bf16_f32 v230, v204, v205
v_mfma_f32_16x16x32_bf16 v[68:71], a[100:103], v[232:235], v[68:71]
v_exp_f32 v206, v206
v_mfma_f32_16x16x32_bf16 v[100:103], a[100:103], v[236:239], v[100:103]
ds_read_b128 a[100:103], v248 offset:9216
v_mfma_f32_16x16x32_bf16 v[72:75], a[104:107], v[232:235], v[72:75]
v_exp_f32 v207, v207
v_mfma_f32_16x16x32_bf16 v[104:107], a[104:107], v[236:239], v[104:107]
ds_read_b128 a[104:107], v248 offset:10240
v_cvt_pk_bf16_f32 v231, v206, v207
s_mov_b32 s93, 43008
v_mfma_f32_16x16x32_bf16 v[76:79], a[108:111], v[232:235], v[76:79]
v_exp_f32 v208, v208
v_mfma_f32_16x16x32_bf16 v[108:111], a[108:111], v[236:239], v[108:111]
ds_read_b128 a[108:111], v248 offset:11264
buffer_load_dwordx4 a[152:155], v254, s[40:43], s93 offen
v_mfma_f32_16x16x32_bf16 v[80:83], a[112:115], v[232:235], v[80:83]
v_exp_f32 v209, v209
v_mfma_f32_16x16x32_bf16 v[112:115], a[112:115], v[236:239], v[112:115]
ds_read_b128 a[112:115], v248 offset:12288
s_mov_b32 s93, 50176
v_mfma_f32_16x16x32_bf16 v[84:87], a[116:119], v[232:235], v[84:87]
v_exp_f32 v210, v210
v_mfma_f32_16x16x32_bf16 v[116:119], a[116:119], v[236:239], v[116:119]
ds_read_b128 a[116:119], v248 offset:13312
buffer_load_dwordx4 a[156:159], v254, s[40:43], s93 offen
v_mfma_f32_16x16x32_bf16 v[88:91], a[120:123], v[232:235], v[88:91]
v_exp_f32 v211, v211
v_mfma_f32_16x16x32_bf16 v[120:123], a[120:123], v[236:239], v[120:123]
ds_read_b128 a[120:123], v248 offset:14336
v_mfma_f32_16x16x32_bf16 v[92:95], a[124:127], v[232:235], v[92:95]
v_exp_f32 v216, v216
v_mfma_f32_16x16x32_bf16 v[124:127], a[124:127], v[236:239], v[124:127]
ds_read_b128 a[124:127], v248 offset:15360
v_or_b32 v248, s89, v255
v_smfmac_f32_16x16x64_bf16 a[196:199], a[208:211], v[232:239], v241
s_waitcnt lgkmcnt(4)
v_smfmac_f32_16x16x64_bf16 a[192:195], a[208:211], v[224:231], v241
ds_read_b128 a[64:67], v248 offset:0
v_mfma_f32_16x16x32_bf16 v[0:3], a[96:99], v[224:227], v[0:3]
v_exp_f32 v217, v217
v_mfma_f32_16x16x32_bf16 v[32:35], a[96:99], v[228:231], v[32:35]
ds_read_b128 a[68:71], v248 offset:1024
v_cvt_pk_bf16_f32 v232, v208, v209
v_mfma_f32_16x16x32_bf16 v[4:7], a[100:103], v[224:227], v[4:7]
v_exp_f32 v218, v218
v_mfma_f32_16x16x32_bf16 v[36:39], a[100:103], v[228:231], v[36:39]
ds_read_b128 a[72:75], v248 offset:2048
v_cvt_pk_bf16_f32 v233, v210, v211
v_mfma_f32_16x16x32_bf16 v[8:11], a[104:107], v[224:227], v[8:11]
v_exp_f32 v219, v219
v_mfma_f32_16x16x32_bf16 v[40:43], a[104:107], v[228:231], v[40:43]
ds_read_b128 a[76:79], v248 offset:3072
v_cvt_pk_bf16_f32 v236, v216, v217
v_mfma_f32_16x16x32_bf16 v[12:15], a[108:111], v[224:227], v[12:15]
v_exp_f32 v212, v212
v_mfma_f32_16x16x32_bf16 v[44:47], a[108:111], v[228:231], v[44:47]
ds_read_b128 a[80:83], v248 offset:4096
s_waitcnt lgkmcnt(5)
v_mfma_f32_16x16x32_bf16 v[16:19], a[112:115], v[224:227], v[16:19]
v_exp_f32 v213, v213
v_mfma_f32_16x16x32_bf16 v[48:51], a[112:115], v[228:231], v[48:51]
ds_read_b128 a[84:87], v248 offset:5120
v_cvt_pk_bf16_f32 v237, v218, v219
v_mfma_f32_16x16x32_bf16 v[20:23], a[116:119], v[224:227], v[20:23]
v_exp_f32 v214, v214
v_mfma_f32_16x16x32_bf16 v[52:55], a[116:119], v[228:231], v[52:55]
ds_read_b128 a[88:91], v248 offset:6144
v_cvt_pk_bf16_f32 v234, v212, v213
v_mfma_f32_16x16x32_bf16 v[24:27], a[120:123], v[224:227], v[24:27]
v_exp_f32 v215, v215
v_mfma_f32_16x16x32_bf16 v[56:59], a[120:123], v[228:231], v[56:59]
ds_read_b128 a[92:95], v248 offset:7168
v_cvt_pk_bf16_f32 v235, v214, v215
v_mfma_f32_16x16x32_bf16 v[28:31], a[124:127], v[224:227], v[28:31]
v_exp_f32 v220, v220
v_mfma_f32_16x16x32_bf16 v[60:63], a[124:127], v[228:231], v[60:63]
s_waitcnt lgkmcnt(4)
v_mfma_f32_16x16x32_bf16 v[192:195], a[64:67], v[128:131], 0
v_exp_f32 v221, v221
v_mfma_f32_16x16x32_bf16 v[192:195], a[68:71], v[132:135], v[192:195]
v_cvt_pk_bf16_f32 v238, v220, v221
s_add_u32 s89, s89, 16384
v_mfma_f32_16x16x32_bf16 v[192:195], a[72:75], v[136:139], v[192:195]
v_exp_f32 v222, v222
v_mfma_f32_16x16x32_bf16 v[192:195], a[76:79], v[140:143], v[192:195]
buffer_load_dwordx4 v255, s[60:63], s17 offen offset:0 sc1 lds
v_mfma_f32_16x16x32_bf16 v[200:203], a[64:67], v[144:147], 0
v_exp_f32 v223, v223
v_mfma_f32_16x16x32_bf16 v[200:203], a[68:71], v[148:151], v[200:203]
v_cvt_pk_bf16_f32 v239, v222, v223
s_and_b32 s89, s89, 0x1ffff
v_mfma_f32_16x16x32_bf16 v[200:203], a[72:75], v[152:155], v[200:203]
v_exp_f32 v192, v192
v_mfma_f32_16x16x32_bf16 v[200:203], a[76:79], v[156:159], v[200:203]
s_waitcnt lgkmcnt(0)
v_mfma_f32_16x16x32_bf16 v[196:199], a[80:83], v[128:131], 0
v_exp_f32 v193, v193
v_mfma_f32_16x16x32_bf16 v[196:199], a[84:87], v[132:135], v[196:199]
v_cvt_pk_bf16_f32 v224, v192, v193
v_mfma_f32_16x16x32_bf16 v[196:199], a[88:91], v[136:139], v[196:199]
v_exp_f32 v194, v194
v_mfma_f32_16x16x32_bf16 v[196:199], a[92:95], v[140:143], v[196:199]
buffer_load_dwordx4 v255, s[60:63], s17 offen offset:1024 sc1 lds
v_mfma_f32_16x16x32_bf16 v[204:207], a[80:83], v[144:147], 0
v_exp_f32 v195, v195
v_mfma_f32_16x16x32_bf16 v[204:207], a[84:87], v[148:151], v[204:207]
v_cvt_pk_bf16_f32 v225, v194, v195
v_mfma_f32_16x16x32_bf16 v[204:207], a[88:91], v[152:155], v[204:207]
v_exp_f32 v200, v200
v_mfma_f32_16x16x32_bf16 v[204:207], a[92:95], v[156:159], v[204:207]
buffer_load_dwordx4 v255, s[60:63], s17 offen offset:2048 sc1 lds
v_mfma_f32_16x16x32_bf16 v[208:211], a[64:67], v[160:163], 0
v_exp_f32 v201, v201
v_mfma_f32_16x16x32_bf16 v[208:211], a[68:71], v[164:167], v[208:211]
v_cvt_pk_bf16_f32 v228, v200, v201
v_mfma_f32_16x16x32_bf16 v[208:211], a[72:75], v[168:171], v[208:211]
v_exp_f32 v202, v202
v_mfma_f32_16x16x32_bf16 v[208:211], a[76:79], v[172:175], v[208:211]
buffer_load_dwordx4 v255, s[60:63], s17 offen offset:3072 sc1 lds
v_mfma_f32_16x16x32_bf16 v[216:219], a[64:67], v[176:179], 0
v_exp_f32 v203, v203
v_mfma_f32_16x16x32_bf16 v[216:219], a[68:71], v[180:183], v[216:219]
v_cvt_pk_bf16_f32 v229, v202, v203
s_mov_b32 s93, 57344
v_mfma_f32_16x16x32_bf16 v[216:219], a[72:75], v[184:187], v[216:219]
v_exp_f32 v196, v196
v_mfma_f32_16x16x32_bf16 v[216:219], a[76:79], v[188:191], v[216:219]
buffer_load_dwordx4 a[160:163], v254, s[40:43], s93 offen
v_mfma_f32_16x16x32_bf16 v[212:215], a[80:83], v[160:163], 0
v_exp_f32 v197, v197
s_add_u32 s17, s17, 8192
v_mfma_f32_16x16x32_bf16 v[212:215], a[84:87], v[164:167], v[212:215]
v_cvt_pk_bf16_f32 v226, v196, v197
s_mov_b32 s93, 64512
s_add_u32 m0, m0, 16384
v_mfma_f32_16x16x32_bf16 v[212:215], a[88:91], v[168:171], v[212:215]
v_exp_f32 v198, v198
s_and_b32 m0, m0, s78
v_mfma_f32_16x16x32_bf16 v[212:215], a[92:95], v[172:175], v[212:215]
buffer_load_dwordx4 a[164:167], v254, s[40:43], s93 offen
v_mfma_f32_16x16x32_bf16 v[220:223], a[80:83], v[176:179], 0
v_exp_f32 v199, v199
v_mfma_f32_16x16x32_bf16 v[220:223], a[84:87], v[180:183], v[220:223]
v_cvt_pk_bf16_f32 v227, v198, v199
v_mfma_f32_16x16x32_bf16 v[220:223], a[88:91], v[184:187], v[220:223]
v_exp_f32 v204, v204
v_mfma_f32_16x16x32_bf16 v[220:223], a[92:95], v[188:191], v[220:223]
s_waitcnt vmcnt(18)
s_barrier
v_mfma_f32_16x16x32_bf16 v[64:67], a[96:99], v[232:235], v[64:67]
v_exp_f32 v205, v205
v_mfma_f32_16x16x32_bf16 v[96:99], a[96:99], v[236:239], v[96:99]
ds_read_b128 a[96:99], v248 offset:8192
v_cvt_pk_bf16_f32 v230, v204, v205
v_mfma_f32_16x16x32_bf16 v[68:71], a[100:103], v[232:235], v[68:71]
v_exp_f32 v206, v206
v_mfma_f32_16x16x32_bf16 v[100:103], a[100:103], v[236:239], v[100:103]
ds_read_b128 a[100:103], v248 offset:9216
v_mfma_f32_16x16x32_bf16 v[72:75], a[104:107], v[232:235], v[72:75]
v_exp_f32 v207, v207
v_mfma_f32_16x16x32_bf16 v[104:107], a[104:107], v[236:239], v[104:107]
ds_read_b128 a[104:107], v248 offset:10240
v_cvt_pk_bf16_f32 v231, v206, v207
s_mov_b32 s93, 71680
v_mfma_f32_16x16x32_bf16 v[76:79], a[108:111], v[232:235], v[76:79]
v_exp_f32 v208, v208
v_mfma_f32_16x16x32_bf16 v[108:111], a[108:111], v[236:239], v[108:111]
ds_read_b128 a[108:111], v248 offset:11264
buffer_load_dwordx4 a[168:171], v254, s[40:43], s93 offen
v_mfma_f32_16x16x32_bf16 v[80:83], a[112:115], v[232:235], v[80:83]
v_exp_f32 v209, v209
v_mfma_f32_16x16x32_bf16 v[112:115], a[112:115], v[236:239], v[112:115]
ds_read_b128 a[112:115], v248 offset:12288
s_mov_b32 s93, 78848
v_mfma_f32_16x16x32_bf16 v[84:87], a[116:119], v[232:235], v[84:87]
v_exp_f32 v210, v210
v_mfma_f32_16x16x32_bf16 v[116:119], a[116:119], v[236:239], v[116:119]
ds_read_b128 a[116:119], v248 offset:13312
buffer_load_dwordx4 a[172:175], v254, s[40:43], s93 offen
v_mfma_f32_16x16x32_bf16 v[88:91], a[120:123], v[232:235], v[88:91]
v_exp_f32 v211, v211
v_mfma_f32_16x16x32_bf16 v[120:123], a[120:123], v[236:239], v[120:123]
ds_read_b128 a[120:123], v248 offset:14336
v_mfma_f32_16x16x32_bf16 v[92:95], a[124:127], v[232:235], v[92:95]
v_exp_f32 v216, v216
v_mfma_f32_16x16x32_bf16 v[124:127], a[124:127], v[236:239], v[124:127]
ds_read_b128 a[124:127], v248 offset:15360
v_or_b32 v248, s89, v255
v_smfmac_f32_16x16x64_bf16 a[196:199], a[208:211], v[232:239], v241
s_waitcnt lgkmcnt(4)
v_smfmac_f32_16x16x64_bf16 a[192:195], a[208:211], v[224:231], v241
ds_read_b128 a[64:67], v248 offset:0
v_mfma_f32_16x16x32_bf16 v[0:3], a[96:99], v[224:227], v[0:3]
v_exp_f32 v217, v217
v_mfma_f32_16x16x32_bf16 v[32:35], a[96:99], v[228:231], v[32:35]
ds_read_b128 a[68:71], v248 offset:1024
v_cvt_pk_bf16_f32 v232, v208, v209
v_mfma_f32_16x16x32_bf16 v[4:7], a[100:103], v[224:227], v[4:7]
v_exp_f32 v218, v218
v_mfma_f32_16x16x32_bf16 v[36:39], a[100:103], v[228:231], v[36:39]
ds_read_b128 a[72:75], v248 offset:2048
v_cvt_pk_bf16_f32 v233, v210, v211
v_mfma_f32_16x16x32_bf16 v[8:11], a[104:107], v[224:227], v[8:11]
v_exp_f32 v219, v219
v_mfma_f32_16x16x32_bf16 v[40:43], a[104:107], v[228:231], v[40:43]
ds_read_b128 a[76:79], v248 offset:3072
v_cvt_pk_bf16_f32 v236, v216, v217
v_mfma_f32_16x16x32_bf16 v[12:15], a[108:111], v[224:227], v[12:15]
v_exp_f32 v212, v212
v_mfma_f32_16x16x32_bf16 v[44:47], a[108:111], v[228:231], v[44:47]
ds_read_b128 a[80:83], v248 offset:4096
s_waitcnt lgkmcnt(5)
v_mfma_f32_16x16x32_bf16 v[16:19], a[112:115], v[224:227], v[16:19]
v_exp_f32 v213, v213
v_mfma_f32_16x16x32_bf16 v[48:51], a[112:115], v[228:231], v[48:51]
ds_read_b128 a[84:87], v248 offset:5120
v_cvt_pk_bf16_f32 v237, v218, v219
v_mfma_f32_16x16x32_bf16 v[20:23], a[116:119], v[224:227], v[20:23]
v_exp_f32 v214, v214
v_mfma_f32_16x16x32_bf16 v[52:55], a[116:119], v[228:231], v[52:55]
ds_read_b128 a[88:91], v248 offset:6144
v_cvt_pk_bf16_f32 v234, v212, v213
v_mfma_f32_16x16x32_bf16 v[24:27], a[120:123], v[224:227], v[24:27]
v_exp_f32 v215, v215
v_mfma_f32_16x16x32_bf16 v[56:59], a[120:123], v[228:231], v[56:59]
ds_read_b128 a[92:95], v248 offset:7168
v_cvt_pk_bf16_f32 v235, v214, v215
v_mfma_f32_16x16x32_bf16 v[28:31], a[124:127], v[224:227], v[28:31]
v_exp_f32 v220, v220
v_mfma_f32_16x16x32_bf16 v[60:63], a[124:127], v[228:231], v[60:63]
s_waitcnt lgkmcnt(4)
v_mfma_f32_16x16x32_bf16 v[192:195], a[64:67], v[128:131], 0
v_exp_f32 v221, v221
v_mfma_f32_16x16x32_bf16 v[192:195], a[68:71], v[132:135], v[192:195]
v_cvt_pk_bf16_f32 v238, v220, v221
v_accvgpr_read_b32 v240, a228
v_mfma_f32_16x16x32_bf16 v[192:195], a[72:75], v[136:139], v[192:195]
v_exp_f32 v222, v222
v_mfma_f32_16x16x32_bf16 v[192:195], a[76:79], v[140:143], v[192:195]
s_add_u32 m0, s83, 16896
s_lshl_b32 s17, s72, 18
v_mfma_f32_16x16x32_bf16 v[200:203], a[64:67], v[144:147], 0
v_exp_f32 v223, v223
v_mfma_f32_16x16x32_bf16 v[200:203], a[68:71], v[148:151], v[200:203]
v_cvt_pk_bf16_f32 v239, v222, v223
s_add_u32 s89, s89, 16384
v_mfma_f32_16x16x32_bf16 v[200:203], a[72:75], v[152:155], v[200:203]
v_exp_f32 v192, v192
v_mfma_f32_16x16x32_bf16 v[200:203], a[76:79], v[156:159], v[200:203]
s_waitcnt lgkmcnt(0)
s_and_b32 s89, s89, 0x1ffff
v_mfma_f32_16x16x32_bf16 v[196:199], a[80:83], v[128:131], 0
v_exp_f32 v193, v193
v_mfma_f32_16x16x32_bf16 v[196:199], a[84:87], v[132:135], v[196:199]
v_cvt_pk_bf16_f32 v224, v192, v193
v_mfma_f32_16x16x32_bf16 v[196:199], a[88:91], v[136:139], v[196:199]
v_exp_f32 v194, v194
v_mfma_f32_16x16x32_bf16 v[196:199], a[92:95], v[140:143], v[196:199]
buffer_load_dwordx4 v240, s[56:59], s17 offen lds
v_mfma_f32_16x16x32_bf16 v[204:207], a[80:83], v[144:147], 0
v_exp_f32 v195, v195
s_add_u32 s17, s17, 32768
v_mfma_f32_16x16x32_bf16 v[204:207], a[84:87], v[148:151], v[204:207]
v_cvt_pk_bf16_f32 v225, v194, v195
s_add_u32 m0, m0, 1024
v_mfma_f32_16x16x32_bf16 v[204:207], a[88:91], v[152:155], v[204:207]
v_exp_f32 v200, v200
v_mfma_f32_16x16x32_bf16 v[204:207], a[92:95], v[156:159], v[204:207]
buffer_load_dwordx4 v240, s[56:59], s17 offen lds
v_mfma_f32_16x16x32_bf16 v[208:211], a[64:67], v[160:163], 0
v_exp_f32 v201, v201
s_add_u32 s17, s17, 4294942720
v_mfma_f32_16x16x32_bf16 v[208:211], a[68:71], v[164:167], v[208:211]
v_cvt_pk_bf16_f32 v228, v200, v201
s_add_u32 m0, m0, 1152
v_mfma_f32_16x16x32_bf16 v[208:211], a[72:75], v[168:171], v[208:211]
v_exp_f32 v202, v202
v_mfma_f32_16x16x32_bf16 v[208:211], a[76:79], v[172:175], v[208:211]
buffer_load_dwordx4 v240, s[56:59], s17 offen lds
v_mfma_f32_16x16x32_bf16 v[216:219], a[64:67], v[176:179], 0
v_exp_f32 v203, v203
s_add_u32 s17, s17, 32768
v_mfma_f32_16x16x32_bf16 v[216:219], a[68:71], v[180:183], v[216:219]
v_cvt_pk_bf16_f32 v229, v202, v203
s_add_u32 m0, m0, 1024
v_mfma_f32_16x16x32_bf16 v[216:219], a[72:75], v[184:187], v[216:219]
v_exp_f32 v196, v196
v_mfma_f32_16x16x32_bf16 v[216:219], a[76:79], v[188:191], v[216:219]
buffer_load_dwordx4 v240, s[56:59], s17 offen lds
v_mfma_f32_16x16x32_bf16 v[212:215], a[80:83], v[160:163], 0
v_exp_f32 v197, v197
s_add_u32 s17, s17, 90112
v_mfma_f32_16x16x32_bf16 v[212:215], a[84:87], v[164:167], v[212:215]
v_cvt_pk_bf16_f32 v226, v196, v197
s_mov_b32 s93, 86016
s_add_u32 m0, s83, 33792
v_mfma_f32_16x16x32_bf16 v[212:215], a[88:91], v[168:171], v[212:215]
v_exp_f32 v198, v198
v_mfma_f32_16x16x32_bf16 v[212:215], a[92:95], v[172:175], v[212:215]
buffer_load_dwordx4 a[176:179], v254, s[40:43], s93 offen
v_mfma_f32_16x16x32_bf16 v[220:223], a[80:83], v[176:179], 0
v_exp_f32 v199, v199
v_mfma_f32_16x16x32_bf16 v[220:223], a[84:87], v[180:183], v[220:223]
v_cvt_pk_bf16_f32 v227, v198, v199
v_mfma_f32_16x16x32_bf16 v[220:223], a[88:91], v[184:187], v[220:223]
v_exp_f32 v204, v204
v_mfma_f32_16x16x32_bf16 v[220:223], a[92:95], v[188:191], v[220:223]
s_waitcnt vmcnt(17)
s_barrier
v_mfma_f32_16x16x32_bf16 v[64:67], a[96:99], v[232:235], v[64:67]
v_exp_f32 v205, v205
v_mfma_f32_16x16x32_bf16 v[96:99], a[96:99], v[236:239], v[96:99]
ds_read_b128 a[96:99], v248 offset:8192
v_cvt_pk_bf16_f32 v230, v204, v205
v_mfma_f32_16x16x32_bf16 v[68:71], a[100:103], v[232:235], v[68:71]
v_exp_f32 v206, v206
v_mfma_f32_16x16x32_bf16 v[100:103], a[100:103], v[236:239], v[100:103]
ds_read_b128 a[100:103], v248 offset:9216
v_mfma_f32_16x16x32_bf16 v[72:75], a[104:107], v[232:235], v[72:75]
v_exp_f32 v207, v207
v_mfma_f32_16x16x32_bf16 v[104:107], a[104:107], v[236:239], v[104:107]
ds_read_b128 a[104:107], v248 offset:10240
v_cvt_pk_bf16_f32 v231, v206, v207
s_mov_b32 s93, 93184
v_mfma_f32_16x16x32_bf16 v[76:79], a[108:111], v[232:235], v[76:79]
v_exp_f32 v208, v208
v_mfma_f32_16x16x32_bf16 v[108:111], a[108:111], v[236:239], v[108:111]
ds_read_b128 a[108:111], v248 offset:11264
.La5_LW2_11:
s_cbranch_execz .La5_LW2_R1
buffer_load_dwordx4 a[180:183], v254, s[40:43], s93 offen
v_mfma_f32_16x16x32_bf16 v[80:83], a[112:115], v[232:235], v[80:83]
v_exp_f32 v209, v209
v_mfma_f32_16x16x32_bf16 v[112:115], a[112:115], v[236:239], v[112:115]
ds_read_b128 a[112:115], v248 offset:12288
s_mov_b32 s93, 100352
v_mfma_f32_16x16x32_bf16 v[84:87], a[116:119], v[232:235], v[84:87]
v_exp_f32 v210, v210
v_mfma_f32_16x16x32_bf16 v[116:119], a[116:119], v[236:239], v[116:119]
ds_read_b128 a[116:119], v248 offset:13312
buffer_load_dwordx4 a[184:187], v254, s[40:43], s93 offen
v_mfma_f32_16x16x32_bf16 v[88:91], a[120:123], v[232:235], v[88:91]
v_exp_f32 v211, v211
v_mfma_f32_16x16x32_bf16 v[120:123], a[120:123], v[236:239], v[120:123]
ds_read_b128 a[120:123], v248 offset:14336
v_mfma_f32_16x16x32_bf16 v[92:95], a[124:127], v[232:235], v[92:95]
v_exp_f32 v216, v216
v_mfma_f32_16x16x32_bf16 v[124:127], a[124:127], v[236:239], v[124:127]
ds_read_b128 a[124:127], v248 offset:15360
v_or_b32 v248, s89, v255
v_smfmac_f32_16x16x64_bf16 a[196:199], a[208:211], v[232:239], v241
s_waitcnt lgkmcnt(4)
v_smfmac_f32_16x16x64_bf16 a[192:195], a[208:211], v[224:231], v241
ds_read_b128 a[64:67], v248 offset:0
v_mfma_f32_16x16x32_bf16 v[0:3], a[96:99], v[224:227], v[0:3]
v_exp_f32 v217, v217
v_mfma_f32_16x16x32_bf16 v[32:35], a[96:99], v[228:231], v[32:35]
ds_read_b128 a[68:71], v248 offset:1024
v_cvt_pk_bf16_f32 v232, v208, v209
v_mfma_f32_16x16x32_bf16 v[4:7], a[100:103], v[224:227], v[4:7]
v_exp_f32 v218, v218
v_mfma_f32_16x16x32_bf16 v[36:39], a[100:103], v[228:231], v[36:39]
ds_read_b128 a[72:75], v248 offset:2048
v_cvt_pk_bf16_f32 v233, v210, v211
v_mfma_f32_16x16x32_bf16 v[8:11], a[104:107], v[224:227], v[8:11]
v_exp_f32 v219, v219
v_mfma_f32_16x16x32_bf16 v[40:43], a[104:107], v[228:231], v[40:43]
ds_read_b128 a[76:79], v248 offset:3072
v_cvt_pk_bf16_f32 v236, v216, v217
v_mfma_f32_16x16x32_bf16 v[12:15], a[108:111], v[224:227], v[12:15]
v_exp_f32 v212, v212
v_mfma_f32_16x16x32_bf16 v[44:47], a[108:111], v[228:231], v[44:47]
ds_read_b128 a[80:83], v248 offset:4096
s_waitcnt lgkmcnt(5)
v_mfma_f32_16x16x32_bf16 v[16:19], a[112:115], v[224:227], v[16:19]
v_exp_f32 v213, v213
v_mfma_f32_16x16x32_bf16 v[48:51], a[112:115], v[228:231], v[48:51]
ds_read_b128 a[84:87], v248 offset:5120
v_cvt_pk_bf16_f32 v237, v218, v219
v_mfma_f32_16x16x32_bf16 v[20:23], a[116:119], v[224:227], v[20:23]
v_exp_f32 v214, v214
v_mfma_f32_16x16x32_bf16 v[52:55], a[116:119], v[228:231], v[52:55]
ds_read_b128 a[88:91], v248 offset:6144
v_cvt_pk_bf16_f32 v234, v212, v213
v_mfma_f32_16x16x32_bf16 v[24:27], a[120:123], v[224:227], v[24:27]
v_exp_f32 v215, v215
v_mfma_f32_16x16x32_bf16 v[56:59], a[120:123], v[228:231], v[56:59]
ds_read_b128 a[92:95], v248 offset:7168
v_cvt_pk_bf16_f32 v235, v214, v215
v_mfma_f32_16x16x32_bf16 v[28:31], a[124:127], v[224:227], v[28:31]
v_exp_f32 v220, v220
v_mfma_f32_16x16x32_bf16 v[60:63], a[124:127], v[228:231], v[60:63]
buffer_load_dword v250, v251, s[64:67], s20 offen sc1
s_waitcnt lgkmcnt(4)
v_mfma_f32_16x16x32_bf16 v[192:195], a[64:67], v[128:131], 0
v_exp_f32 v221, v221
v_mfma_f32_16x16x32_bf16 v[192:195], a[68:71], v[132:135], v[192:195]
v_cvt_pk_bf16_f32 v238, v220, v221
s_add_u32 s89, s89, 16384
v_mfma_f32_16x16x32_bf16 v[192:195], a[72:75], v[136:139], v[192:195]
v_exp_f32 v222, v222
v_mfma_f32_16x16x32_bf16 v[192:195], a[76:79], v[140:143], v[192:195]
buffer_load_dwordx4 v240, s[56:59], s17 offen lds
v_mfma_f32_16x16x32_bf16 v[200:203], a[64:67], v[144:147], 0
v_exp_f32 v223, v223
s_add_u32 s17, s17, 32768
v_mfma_f32_16x16x32_bf16 v[200:203], a[68:71], v[148:151], v[200:203]
v_cvt_pk_bf16_f32 v239, v222, v223
s_add_u32 m0, m0, 1024
v_mfma_f32_16x16x32_bf16 v[200:203], a[72:75], v[152:155], v[200:203]
v_exp_f32 v192, v192
v_mfma_f32_16x16x32_bf16 v[200:203], a[76:79], v[156:159], v[200:203]
s_waitcnt lgkmcnt(0)
s_and_b32 s89, s89, 0x1ffff
v_mfma_f32_16x16x32_bf16 v[196:199], a[80:83], v[128:131], 0
v_exp_f32 v193, v193
v_mfma_f32_16x16x32_bf16 v[196:199], a[84:87], v[132:135], v[196:199]
v_cvt_pk_bf16_f32 v224, v192, v193
v_mfma_f32_16x16x32_bf16 v[196:199], a[88:91], v[136:139], v[196:199]
v_exp_f32 v194, v194
v_mfma_f32_16x16x32_bf16 v[196:199], a[92:95], v[140:143], v[196:199]
buffer_load_dwordx4 v240, s[56:59], s17 offen lds
v_mfma_f32_16x16x32_bf16 v[204:207], a[80:83], v[144:147], 0
v_exp_f32 v195, v195
s_add_u32 s17, s17, 4294942720
v_mfma_f32_16x16x32_bf16 v[204:207], a[84:87], v[148:151], v[204:207]
v_cvt_pk_bf16_f32 v225, v194, v195
s_add_u32 m0, m0, 1152
v_mfma_f32_16x16x32_bf16 v[204:207], a[88:91], v[152:155], v[204:207]
v_exp_f32 v200, v200
v_mfma_f32_16x16x32_bf16 v[204:207], a[92:95], v[156:159], v[204:207]
buffer_load_dwordx4 v240, s[56:59], s17 offen lds
v_mfma_f32_16x16x32_bf16 v[208:211], a[64:67], v[160:163], 0
v_exp_f32 v201, v201
s_add_u32 s17, s17, 32768
v_mfma_f32_16x16x32_bf16 v[208:211], a[68:71], v[164:167], v[208:211]
v_cvt_pk_bf16_f32 v228, v200, v201
s_add_u32 m0, m0, 1024
v_mfma_f32_16x16x32_bf16 v[208:211], a[72:75], v[168:171], v[208:211]
v_exp_f32 v202, v202
v_mfma_f32_16x16x32_bf16 v[208:211], a[76:79], v[172:175], v[208:211]
buffer_load_dwordx4 v240, s[56:59], s17 offen lds
v_mfma_f32_16x16x32_bf16 v[216:219], a[64:67], v[176:179], 0
v_exp_f32 v203, v203
v_mfma_f32_16x16x32_bf16 v[216:219], a[68:71], v[180:183], v[216:219]
v_cvt_pk_bf16_f32 v229, v202, v203
s_mov_b32 s93, 107520
v_mfma_f32_16x16x32_bf16 v[216:219], a[72:75], v[184:187], v[216:219]
v_exp_f32 v196, v196
v_mfma_f32_16x16x32_bf16 v[216:219], a[76:79], v[188:191], v[216:219]
buffer_load_dwordx4 a[188:191], v254, s[40:43], s93 offen
v_mfma_f32_16x16x32_bf16 v[212:215], a[80:83], v[160:163], 0
v_exp_f32 v197, v197
s_bfe_u32 s17, s4, 262148
v_mfma_f32_16x16x32_bf16 v[212:215], a[84:87], v[164:167], v[212:215]
v_cvt_pk_bf16_f32 v226, v196, v197
s_xor_b32 s17, s17, s72
s_lshl_b32 s17, s17, 18
v_mfma_f32_16x16x32_bf16 v[212:215], a[88:91], v[168:171], v[212:215]
v_exp_f32 v198, v198
s_add_u32 m0, s83, 50688
v_mfma_f32_16x16x32_bf16 v[212:215], a[92:95], v[172:175], v[212:215]
v_mfma_f32_16x16x32_bf16 v[220:223], a[80:83], v[176:179], 0
v_exp_f32 v199, v199
v_mfma_f32_16x16x32_bf16 v[220:223], a[84:87], v[180:183], v[220:223]
v_cvt_pk_bf16_f32 v227, v198, v199
s_waitcnt vmcnt(5)
v_cmp_eq_u32 s[92:93], v250, v249
s_mov_b32 s94, 16384
v_mfma_f32_16x16x32_bf16 v[220:223], a[88:91], v[184:187], v[220:223]
v_exp_f32 v204, v204
v_mfma_f32_16x16x32_bf16 v[220:223], a[92:95], v[188:191], v[220:223]
s_nop 0
s_barrier
v_mfma_f32_16x16x32_bf16 v[64:67], a[96:99], v[232:235], v[64:67]
v_exp_f32 v205, v205
v_mfma_f32_16x16x32_bf16 v[96:99], a[96:99], v[236:239], v[96:99]
ds_read_b128 a[96:99], v248 offset:8192
v_cvt_pk_bf16_f32 v230, v204, v205
s_and_b32 s92, s92, 3
s_andn2_b32 s92, s92, s23
s_cmp_eq_u32 s92, 3
s_cselect_b32 s70, 17408, 0
s_cselect_b32 s84, 3, s23
v_mfma_f32_16x16x32_bf16 v[68:71], a[100:103], v[232:235], v[68:71]
v_exp_f32 v206, v206
v_mfma_f32_16x16x32_bf16 v[100:103], a[100:103], v[236:239], v[100:103]
ds_read_b128 a[100:103], v248 offset:9216
buffer_load_dwordx4 a[220:223], v255, s[68:71], s94 offen sc1
v_mfma_f32_16x16x32_bf16 v[72:75], a[104:107], v[232:235], v[72:75]
v_exp_f32 v207, v207
v_mfma_f32_16x16x32_bf16 v[104:107], a[104:107], v[236:239], v[104:107]
ds_read_b128 a[104:107], v248 offset:10240
v_cvt_pk_bf16_f32 v231, v206, v207
v_mfma_f32_16x16x32_bf16 v[76:79], a[108:111], v[232:235], v[76:79]
v_exp_f32 v208, v208
v_mfma_f32_16x16x32_bf16 v[108:111], a[108:111], v[236:239], v[108:111]
ds_read_b128 a[108:111], v248 offset:11264
buffer_load_dwordx4 a[0:3], v255, s[68:71], 0 offen offset:0 sc1
v_mfma_f32_16x16x32_bf16 v[80:83], a[112:115], v[232:235], v[80:83]
v_exp_f32 v209, v209
v_mfma_f32_16x16x32_bf16 v[112:115], a[112:115], v[236:239], v[112:115]
ds_read_b128 a[112:115], v248 offset:12288
v_mfma_f32_16x16x32_bf16 v[84:87], a[116:119], v[232:235], v[84:87]
v_exp_f32 v210, v210
v_mfma_f32_16x16x32_bf16 v[116:119], a[116:119], v[236:239], v[116:119]
ds_read_b128 a[116:119], v248 offset:13312
buffer_load_dwordx4 a[4:7], v255, s[68:71], 0 offen offset:1024 sc1
v_mfma_f32_16x16x32_bf16 v[88:91], a[120:123], v[232:235], v[88:91]
v_exp_f32 v211, v211
v_mfma_f32_16x16x32_bf16 v[120:123], a[120:123], v[236:239], v[120:123]
ds_read_b128 a[120:123], v248 offset:14336
v_mfma_f32_16x16x32_bf16 v[92:95], a[124:127], v[232:235], v[92:95]
v_exp_f32 v216, v216
v_mfma_f32_16x16x32_bf16 v[124:127], a[124:127], v[236:239], v[124:127]
ds_read_b128 a[124:127], v248 offset:15360
v_or_b32 v248, s89, v255
buffer_load_dword v250, v251, s[64:67], s20 offen sc1
v_smfmac_f32_16x16x64_bf16 a[196:199], a[208:211], v[232:239], v241
s_waitcnt lgkmcnt(4)
v_smfmac_f32_16x16x64_bf16 a[192:195], a[208:211], v[224:231], v241
ds_read_b128 a[64:67], v248 offset:0
v_mfma_f32_16x16x32_bf16 v[0:3], a[96:99], v[224:227], v[0:3]
v_exp_f32 v217, v217
v_mfma_f32_16x16x32_bf16 v[32:35], a[96:99], v[228:231], v[32:35]
ds_read_b128 a[68:71], v248 offset:1024
v_cvt_pk_bf16_f32 v232, v208, v209
buffer_load_dwordx4 a[8:11], v255, s[68:71], 0 offen offset:2048 sc1
v_mfma_f32_16x16x32_bf16 v[4:7], a[100:103], v[224:227], v[4:7]
v_exp_f32 v218, v218
v_mfma_f32_16x16x32_bf16 v[36:39], a[100:103], v[228:231], v[36:39]
ds_read_b128 a[72:75], v248 offset:2048
v_cvt_pk_bf16_f32 v233, v210, v211
v_mfma_f32_16x16x32_bf16 v[8:11], a[104:107], v[224:227], v[8:11]
v_exp_f32 v219, v219
v_mfma_f32_16x16x32_bf16 v[40:43], a[104:107], v[228:231], v[40:43]
ds_read_b128 a[76:79], v248 offset:3072
v_cvt_pk_bf16_f32 v236, v216, v217
buffer_load_dwordx4 a[12:15], v255, s[68:71], 0 offen offset:3072 sc1
v_mfma_f32_16x16x32_bf16 v[12:15], a[108:111], v[224:227], v[12:15]
v_exp_f32 v212, v212
v_mfma_f32_16x16x32_bf16 v[44:47], a[108:111], v[228:231], v[44:47]
ds_read_b128 a[80:83], v248 offset:4096
s_waitcnt lgkmcnt(5)
v_mfma_f32_16x16x32_bf16 v[16:19], a[112:115], v[224:227], v[16:19]
v_exp_f32 v213, v213
v_mfma_f32_16x16x32_bf16 v[48:51], a[112:115], v[228:231], v[48:51]
ds_read_b128 a[84:87], v248 offset:5120
v_cvt_pk_bf16_f32 v237, v218, v219
buffer_load_dwordx4 a[16:19], v255, s[68:71], s95 offen offset:0 sc1
v_mfma_f32_16x16x32_bf16 v[20:23], a[116:119], v[224:227], v[20:23]
v_exp_f32 v214, v214
v_mfma_f32_16x16x32_bf16 v[52:55], a[116:119], v[228:231], v[52:55]
ds_read_b128 a[88:91], v248 offset:6144
v_cvt_pk_bf16_f32 v234, v212, v213
v_mfma_f32_16x16x32_bf16 v[24:27], a[120:123], v[224:227], v[24:27]
v_exp_f32 v215, v215
v_mfma_f32_16x16x32_bf16 v[56:59], a[120:123], v[228:231], v[56:59]
ds_read_b128 a[92:95], v248 offset:7168
v_cvt_pk_bf16_f32 v235, v214, v215
buffer_load_dwordx4 a[20:23], v255, s[68:71], s95 offen offset:1024 sc1
v_mfma_f32_16x16x32_bf16 v[28:31], a[124:127], v[224:227], v[28:31]
v_exp_f32 v220, v220
v_mfma_f32_16x16x32_bf16 v[60:63], a[124:127], v[228:231], v[60:63]
buffer_load_dwordx4 a[24:27], v255, s[68:71], s95 offen offset:2048 sc1
s_waitcnt lgkmcnt(4)
v_mfma_f32_16x16x32_bf16 v[192:195], a[64:67], v[128:131], 0
v_exp_f32 v221, v221
v_mfma_f32_16x16x32_bf16 v[192:195], a[68:71], v[132:135], v[192:195]
v_cvt_pk_bf16_f32 v238, v220, v221
v_mfma_f32_16x16x32_bf16 v[192:195], a[72:75], v[136:139], v[192:195]
v_exp_f32 v222, v222
v_mfma_f32_16x16x32_bf16 v[192:195], a[76:79], v[140:143], v[192:195]
buffer_load_dwordx4 v240, s[56:59], s17 offen lds
v_mfma_f32_16x16x32_bf16 v[200:203], a[64:67], v[144:147], 0
v_exp_f32 v223, v223
s_add_u32 s17, s17, 32768
v_mfma_f32_16x16x32_bf16 v[200:203], a[68:71], v[148:151], v[200:203]
v_cvt_pk_bf16_f32 v239, v222, v223
s_add_u32 m0, m0, 1024
v_mfma_f32_16x16x32_bf16 v[200:203], a[72:75], v[152:155], v[200:203]
v_exp_f32 v192, v192
v_mfma_f32_16x16x32_bf16 v[200:203], a[76:79], v[156:159], v[200:203]
s_waitcnt lgkmcnt(0)
v_mfma_f32_16x16x32_bf16 v[196:199], a[80:83], v[128:131], 0
v_exp_f32 v193, v193
v_mfma_f32_16x16x32_bf16 v[196:199], a[84:87], v[132:135], v[196:199]
v_cvt_pk_bf16_f32 v224, v192, v193
s_waitcnt vmcnt(6)
v_cmp_eq_u32 s[92:93], v250, v249
v_mfma_f32_16x16x32_bf16 v[196:199], a[88:91], v[136:139], v[196:199]
v_exp_f32 v194, v194
v_mfma_f32_16x16x32_bf16 v[196:199], a[92:95], v[140:143], v[196:199]
buffer_load_dwordx4 v240, s[56:59], s17 offen lds
v_mfma_f32_16x16x32_bf16 v[204:207], a[80:83], v[144:147], 0
v_exp_f32 v195, v195
s_add_u32 s17, s17, 4294942720
v_mfma_f32_16x16x32_bf16 v[204:207], a[84:87], v[148:151], v[204:207]
v_cvt_pk_bf16_f32 v225, v194, v195
s_add_u32 m0, m0, 1152
s_and_b32 s92, s92, 3
s_sub_u32 s92, s92, s84
s_cmp_eq_u32 s92, 3
s_cbranch_scc1 .La5_LA_PS0
.La5_LA_PS1:
v_mfma_f32_16x16x32_bf16 v[204:207], a[88:91], v[152:155], v[204:207]
v_exp_f32 v200, v200
v_mfma_f32_16x16x32_bf16 v[204:207], a[92:95], v[156:159], v[204:207]
buffer_load_dwordx4 v240, s[56:59], s17 offen lds
v_mfma_f32_16x16x32_bf16 v[208:211], a[64:67], v[160:163], 0
v_exp_f32 v201, v201
s_add_u32 s17, s17, 32768
v_mfma_f32_16x16x32_bf16 v[208:211], a[68:71], v[164:167], v[208:211]
v_cvt_pk_bf16_f32 v228, v200, v201
s_add_u32 m0, m0, 1024
buffer_load_dwordx4 a[28:31], v255, s[68:71], s95 offen offset:3072 sc1
v_mfma_f32_16x16x32_bf16 v[208:211], a[72:75], v[168:171], v[208:211]
v_exp_f32 v202, v202
v_mfma_f32_16x16x32_bf16 v[208:211], a[76:79], v[172:175], v[208:211]
buffer_load_dwordx4 v240, s[56:59], s17 offen lds
v_mfma_f32_16x16x32_bf16 v[216:219], a[64:67], v[176:179], 0
v_exp_f32 v203, v203
v_mfma_f32_16x16x32_bf16 v[216:219], a[68:71], v[180:183], v[216:219]
v_cvt_pk_bf16_f32 v229, v202, v203
buffer_load_dwordx4 a[32:35], v255, s[68:71], s96 offen offset:0 sc1
v_mfma_f32_16x16x32_bf16 v[216:219], a[72:75], v[184:187], v[216:219]
v_exp_f32 v196, v196
v_mfma_f32_16x16x32_bf16 v[216:219], a[76:79], v[188:191], v[216:219]
v_mfma_f32_16x16x32_bf16 v[212:215], a[80:83], v[160:163], 0
v_exp_f32 v197, v197
v_mfma_f32_16x16x32_bf16 v[212:215], a[84:87], v[164:167], v[212:215]
v_cvt_pk_bf16_f32 v226, v196, v197
buffer_load_dword v250, v251, s[64:67], s20 offen sc1
v_mfma_f32_16x16x32_bf16 v[212:215], a[88:91], v[168:171], v[212:215]
v_exp_f32 v198, v198
v_mfma_f32_16x16x32_bf16 v[212:215], a[92:95], v[172:175], v[212:215]
buffer_load_dwordx4 a[36:39], v255, s[68:71], s96 offen offset:1024 sc1
v_mfma_f32_16x16x32_bf16 v[220:223], a[80:83], v[176:179], 0
v_exp_f32 v199, v199
v_mfma_f32_16x16x32_bf16 v[220:223], a[84:87], v[180:183], v[220:223]
v_cvt_pk_bf16_f32 v227, v198, v199
v_mfma_f32_16x16x32_bf16 v[220:223], a[88:91], v[184:187], v[220:223]
v_exp_f32 v204, v204
v_mfma_f32_16x16x32_bf16 v[220:223], a[92:95], v[188:191], v[220:223]
buffer_load_dwordx4 a[40:43], v255, s[68:71], s96 offen offset:2048 sc1
v_mfma_f32_16x16x32_bf16 v[64:67], a[96:99], v[232:235], v[64:67]
v_exp_f32 v205, v205
v_mfma_f32_16x16x32_bf16 v[96:99], a[96:99], v[236:239], v[96:99]
ds_read_b128 a[96:99], v248 offset:8192
v_cvt_pk_bf16_f32 v230, v204, v205
v_mfma_f32_16x16x32_bf16 v[68:71], a[100:103], v[232:235], v[68:71]
v_exp_f32 v206, v206
v_mfma_f32_16x16x32_bf16 v[100:103], a[100:103], v[236:239], v[100:103]
ds_read_b128 a[100:103], v248 offset:9216
buffer_load_dwordx4 a[44:47], v255, s[68:71], s96 offen offset:3072 sc1
v_mfma_f32_16x16x32_bf16 v[72:75], a[104:107], v[232:235], v[72:75]
v_exp_f32 v207, v207
v_mfma_f32_16x16x32_bf16 v[104:107], a[104:107], v[236:239], v[104:107]
ds_read_b128 a[104:107], v248 offset:10240
v_cvt_pk_bf16_f32 v231, v206, v207
v_mfma_f32_16x16x32_bf16 v[76:79], a[108:111], v[232:235], v[76:79]
v_exp_f32 v208, v208
v_mfma_f32_16x16x32_bf16 v[108:111], a[108:111], v[236:239], v[108:111]
ds_read_b128 a[108:111], v248 offset:11264
buffer_load_dwordx4 a[48:51], v255, s[68:71], s97 offen offset:0 sc1
v_mfma_f32_16x16x32_bf16 v[80:83], a[112:115], v[232:235], v[80:83]
v_exp_f32 v209, v209
v_mfma_f32_16x16x32_bf16 v[112:115], a[112:115], v[236:239], v[112:115]
ds_read_b128 a[112:115], v248 offset:12288
v_mfma_f32_16x16x32_bf16 v[84:87], a[116:119], v[232:235], v[84:87]
v_exp_f32 v210, v210
v_mfma_f32_16x16x32_bf16 v[116:119], a[116:119], v[236:239], v[116:119]
ds_read_b128 a[116:119], v248 offset:13312
buffer_load_dwordx4 a[52:55], v255, s[68:71], s97 offen offset:1024 sc1
v_mfma_f32_16x16x32_bf16 v[88:91], a[120:123], v[232:235], v[88:91]
v_exp_f32 v211, v211
v_mfma_f32_16x16x32_bf16 v[120:123], a[120:123], v[236:239], v[120:123]
ds_read_b128 a[120:123], v248 offset:14336
v_mfma_f32_16x16x32_bf16 v[92:95], a[124:127], v[232:235], v[92:95]
v_exp_f32 v216, v216
v_mfma_f32_16x16x32_bf16 v[124:127], a[124:127], v[236:239], v[124:127]
ds_read_b128 a[124:127], v248 offset:15360
buffer_load_dwordx4 a[56:59], v255, s[68:71], s97 offen offset:2048 sc1
v_smfmac_f32_16x16x64_bf16 a[196:199], a[208:211], v[232:239], v241
s_waitcnt lgkmcnt(4)
v_smfmac_f32_16x16x64_bf16 a[192:195], a[208:211], v[224:231], v241
buffer_load_dwordx4 a[60:63], v255, s[68:71], s97 offen offset:3072 sc1
v_mfma_f32_16x16x32_bf16 v[0:3], a[96:99], v[224:227], v[0:3]
v_exp_f32 v217, v217
v_mfma_f32_16x16x32_bf16 v[32:35], a[96:99], v[228:231], v[32:35]
v_cvt_pk_bf16_f32 v232, v208, v209
v_mfma_f32_16x16x32_bf16 v[4:7], a[100:103], v[224:227], v[4:7]
v_exp_f32 v218, v218
v_mfma_f32_16x16x32_bf16 v[36:39], a[100:103], v[228:231], v[36:39]
v_cvt_pk_bf16_f32 v233, v210, v211
v_mfma_f32_16x16x32_bf16 v[8:11], a[104:107], v[224:227], v[8:11]
v_exp_f32 v219, v219
v_mfma_f32_16x16x32_bf16 v[40:43], a[104:107], v[228:231], v[40:43]
v_cvt_pk_bf16_f32 v236, v216, v217
v_mfma_f32_16x16x32_bf16 v[12:15], a[108:111], v[224:227], v[12:15]
v_exp_f32 v212, v212
s_waitcnt vmcnt(7)
v_cmp_eq_u32 s[92:93], v250, v249
v_mfma_f32_16x16x32_bf16 v[44:47], a[108:111], v[228:231], v[44:47]
s_waitcnt lgkmcnt(0)
v_mfma_f32_16x16x32_bf16 v[16:19], a[112:115], v[224:227], v[16:19]
v_exp_f32 v213, v213
v_mfma_f32_16x16x32_bf16 v[48:51], a[112:115], v[228:231], v[48:51]
v_cvt_pk_bf16_f32 v237, v218, v219
s_and_b32 s92, s92, 3
s_sub_u32 s92, s92, s84
v_mfma_f32_16x16x32_bf16 v[20:23], a[116:119], v[224:227], v[20:23]
v_exp_f32 v214, v214
v_mfma_f32_16x16x32_bf16 v[52:55], a[116:119], v[228:231], v[52:55]
v_cvt_pk_bf16_f32 v234, v212, v213
v_mfma_f32_16x16x32_bf16 v[24:27], a[120:123], v[224:227], v[24:27]
v_exp_f32 v215, v215
v_mfma_f32_16x16x32_bf16 v[56:59], a[120:123], v[228:231], v[56:59]
v_cvt_pk_bf16_f32 v235, v214, v215
v_mfma_f32_16x16x32_bf16 v[28:31], a[124:127], v[224:227], v[28:31]
v_exp_f32 v220, v220
v_mfma_f32_16x16x32_bf16 v[60:63], a[124:127], v[228:231], v[60:63]
s_cmp_eq_u32 s92, 3
s_cbranch_scc1 .La5_LA_PS2
.La5_LA_PS3:
s_cmp_lg_u32 s84, 3
s_cbranch_scc1 .La5_LA_PSPIN
.La5_LA_DC1:
s_waitcnt vmcnt(8)
v_mfma_f32_16x16x32_bf16 v[0:3], a[214:217], a[0:3], v[0:3]
v_accvgpr_read_b32 v250, a192
v_accvgpr_read_b32 v251, a193
v_add_f32 v250, v250, v251
v_accvgpr_read_b32 v251, a220
v_add_f32 v250, v250, v251
v_accvgpr_read_b32 v249, a194
v_mfma_f32_16x16x32_bf16 v[4:7], a[212:215], a[0:3], v[4:7]
v_accvgpr_read_b32 v251, a195
v_add_f32 v249, v249, v251
v_accvgpr_read_b32 v251, a221
v_add_f32 v249, v249, v251
v_subrev_u32 v251, 562036736, v250
v_mfma_f32_16x16x32_bf16 v[8:11], a[214:217], a[4:7], v[8:11]
v_rcp_f32 v250, v250
v_cmp_le_u32 vcc, 1006632960, v251
v_subrev_u32 v251, 562036736, v249
s_mov_b64 s[92:93], vcc
v_cmp_le_u32 vcc, 1006632960, v251
v_mfma_f32_16x16x32_bf16 v[12:15], a[212:215], a[4:7], v[12:15]
v_rcp_f32 v249, v249
v_mfma_f32_16x16x32_bf16 v[16:19], a[214:217], a[8:11], v[16:19]
s_or_b64 vcc, vcc, s[92:93]
s_cbranch_scc1 .La5_LA_SC3
.La5_LA_SR3:
v_exp_f32 v221, v221
v_exp_f32 v222, v222
v_mfma_f32_16x16x32_bf16 v[20:23], a[212:215], a[8:11], v[20:23]
v_exp_f32 v223, v223
v_cvt_pk_bf16_f32 v238, v220, v221
v_mul_f32 v0, v0, v250
v_mul_f32 v1, v1, v250
v_mul_f32 v2, v2, v250
v_mfma_f32_16x16x32_bf16 v[24:27], a[214:217], a[12:15], v[24:27]
v_mul_f32 v3, v3, v250
v_mul_f32 v4, v4, v250
v_mul_f32 v5, v5, v250
v_mul_f32 v6, v6, v250
v_mul_f32 v7, v7, v250
v_cvt_pk_bf16_f32 v128, v0, v1
v_mfma_f32_16x16x32_bf16 v[28:31], a[212:215], a[12:15], v[28:31]
v_cvt_pk_bf16_f32 v129, v2, v3
v_cvt_pk_bf16_f32 v130, v4, v5
v_cvt_pk_bf16_f32 v131, v6, v7
buffer_store_dwordx4 v[128:131], v244, s[44:47], s91 offen offset:0 sc1
v_cvt_pk_bf16_f32 v239, v222, v223
v_mfma_f32_16x16x32_bf16 v[32:35], a[214:217], a[16:19], v[32:35]
v_mul_f32 v8, v8, v250
v_mul_f32 v9, v9, v250
v_mul_f32 v10, v10, v250
v_mul_f32 v11, v11, v250
v_mul_f32 v12, v12, v250
v_mul_f32 v13, v13, v250
v_mfma_f32_16x16x32_bf16 v[36:39], a[212:215], a[16:19], v[36:39]
v_mul_f32 v14, v14, v250
v_mul_f32 v15, v15, v250
v_cvt_pk_bf16_f32 v132, v8, v9
v_cvt_pk_bf16_f32 v133, v10, v11
v_cvt_pk_bf16_f32 v134, v12, v13
v_cvt_pk_bf16_f32 v135, v14, v15
v_mfma_f32_16x16x32_bf16 v[40:43], a[214:217], a[20:23], v[40:43]
buffer_store_dwordx4 v[132:135], v244, s[44:47], s91 offen offset:1024 sc1
v_mul_f32 v16, v16, v250
v_mul_f32 v17, v17, v250
v_mul_f32 v18, v18, v250
v_mul_f32 v19, v19, v250
v_mfma_f32_16x16x32_bf16 v[44:47], a[212:215], a[20:23], v[44:47]
v_mul_f32 v20, v20, v250
v_mul_f32 v21, v21, v250
v_mul_f32 v22, v22, v250
v_mul_f32 v23, v23, v250
v_cvt_pk_bf16_f32 v136, v16, v17
v_cvt_pk_bf16_f32 v137, v18, v19
v_mfma_f32_16x16x32_bf16 v[48:51], a[214:217], a[24:27], v[48:51]
v_cvt_pk_bf16_f32 v138, v20, v21
v_cvt_pk_bf16_f32 v139, v22, v23
buffer_store_dwordx4 v[136:139], v244, s[44:47], s91 offen offset:2048 sc1
v_mul_f32 v24, v24, v250
v_mul_f32 v25, v25, v250
v_mfma_f32_16x16x32_bf16 v[52:55], a[212:215], a[24:27], v[52:55]
v_mul_f32 v26, v26, v250
v_mul_f32 v27, v27, v250
v_mul_f32 v28, v28, v250
v_mul_f32 v29, v29, v250
v_mul_f32 v30, v30, v250
v_mul_f32 v31, v31, v250
v_mfma_f32_16x16x32_bf16 v[56:59], a[214:217], a[28:31], v[56:59]
v_cvt_pk_bf16_f32 v140, v24, v25
v_cvt_pk_bf16_f32 v141, v26, v27
v_cvt_pk_bf16_f32 v142, v28, v29
v_cvt_pk_bf16_f32 v143, v30, v31
buffer_store_dwordx4 v[140:143], v244, s[44:47], s91 offen offset:3072 sc1
v_mfma_f32_16x16x32_bf16 v[60:63], a[212:215], a[28:31], v[60:63]
v_mul_f32 v32, v32, v249
v_mul_f32 v33, v33, v249
v_mul_f32 v34, v34, v249
v_mul_f32 v35, v35, v249
v_mul_f32 v36, v36, v249
v_mul_f32 v37, v37, v249
v_smfmac_f32_16x16x64_bf16 a[196:199], a[208:211], v[232:239], v241
v_mul_f32 v38, v38, v249
v_mul_f32 v39, v39, v249
v_cvt_pk_bf16_f32 v144, v32, v33
v_cvt_pk_bf16_f32 v145, v34, v35
v_cvt_pk_bf16_f32 v146, v36, v37
v_mfma_f32_16x16x32_bf16 v[64:67], a[96:99], v[232:235], v[64:67]
v_cvt_pk_bf16_f32 v147, v38, v39
buffer_store_dwordx4 v[144:147], v245, s[44:47], s91 offen offset:0 sc1
v_mul_f32 v40, v40, v249
v_mul_f32 v41, v41, v249
v_mul_f32 v42, v42, v249
v_mfma_f32_16x16x32_bf16 v[96:99], a[96:99], v[236:239], v[96:99]
v_mul_f32 v43, v43, v249
v_mul_f32 v44, v44, v249
v_mul_f32 v45, v45, v249
v_mul_f32 v46, v46, v249
v_mul_f32 v47, v47, v249
v_cvt_pk_bf16_f32 v148, v40, v41
v_mfma_f32_16x16x32_bf16 v[68:71], a[100:103], v[232:235], v[68:71]
v_cvt_pk_bf16_f32 v149, v42, v43
v_cvt_pk_bf16_f32 v150, v44, v45
v_cvt_pk_bf16_f32 v151, v46, v47
buffer_store_dwordx4 v[148:151], v245, s[44:47], s91 offen offset:1024 sc1
v_mul_f32 v48, v48, v249
v_mfma_f32_16x16x32_bf16 v[100:103], a[100:103], v[236:239], v[100:103]
v_mul_f32 v49, v49, v249
v_mul_f32 v50, v50, v249
v_mul_f32 v51, v51, v249
v_mul_f32 v52, v52, v249
v_mul_f32 v53, v53, v249
v_mul_f32 v54, v54, v249
v_mfma_f32_16x16x32_bf16 v[72:75], a[104:107], v[232:235], v[72:75]
v_mul_f32 v55, v55, v249
v_cvt_pk_bf16_f32 v152, v48, v49
v_cvt_pk_bf16_f32 v153, v50, v51
v_cvt_pk_bf16_f32 v154, v52, v53
v_cvt_pk_bf16_f32 v155, v54, v55
buffer_store_dwordx4 v[152:155], v245, s[44:47], s91 offen offset:2048 sc1
v_mfma_f32_16x16x32_bf16 v[104:107], a[104:107], v[236:239], v[104:107]
v_mul_f32 v56, v56, v249
v_mul_f32 v57, v57, v249
v_mul_f32 v58, v58, v249
v_mul_f32 v59, v59, v249
v_mul_f32 v60, v60, v249
v_mul_f32 v61, v61, v249
v_mfma_f32_16x16x32_bf16 v[76:79], a[108:111], v[232:235], v[76:79]
v_mul_f32 v62, v62, v249
v_mul_f32 v63, v63, v249
v_cvt_pk_bf16_f32 v156, v56, v57
v_cvt_pk_bf16_f32 v157, v58, v59
v_cvt_pk_bf16_f32 v158, v60, v61
v_cvt_pk_bf16_f32 v159, v62, v63
v_mfma_f32_16x16x32_bf16 v[108:111], a[108:111], v[236:239], v[108:111]
buffer_store_dwordx4 v[156:159], v245, s[44:47], s91 offen offset:3072 sc1
v_accvgpr_read_b32 v240, a224
v_xor_b32 v241, 32, v240
v_xor_b32 v242, 64, v240
v_xor_b32 v243, 96, v240
v_mfma_f32_16x16x32_bf16 v[80:83], a[112:115], v[232:235], v[80:83]
v_mfma_f32_16x16x32_bf16 v[112:115], a[112:115], v[236:239], v[112:115]
v_mfma_f32_16x16x32_bf16 v[84:87], a[116:119], v[232:235], v[84:87]
v_mfma_f32_16x16x32_bf16 v[116:119], a[116:119], v[236:239], v[116:119]
v_mfma_f32_16x16x32_bf16 v[88:91], a[120:123], v[232:235], v[88:91]
v_mfma_f32_16x16x32_bf16 v[120:123], a[120:123], v[236:239], v[120:123]
v_mfma_f32_16x16x32_bf16 v[92:95], a[124:127], v[232:235], v[92:95]
v_mfma_f32_16x16x32_bf16 v[124:127], a[124:127], v[236:239], v[124:127]
s_nop 0
s_barrier
s_waitcnt vmcnt(8)
v_mfma_f32_16x16x32_bf16 v[64:67], a[214:217], a[32:35], v[64:67]
ds_read_b64_tr_b16 v[192:193], v240 offset:16896
ds_read_b64_tr_b16 v[194:195], v240 offset:17920
v_mfma_f32_16x16x32_bf16 v[68:71], a[212:215], a[32:35], v[68:71]
ds_read_b64_tr_b16 v[196:197], v241 offset:16896
ds_read_b64_tr_b16 v[198:199], v241 offset:17920
v_mfma_f32_16x16x32_bf16 v[72:75], a[214:217], a[36:39], v[72:75]
ds_read_b64_tr_b16 v[200:201], v242 offset:16896
ds_read_b64_tr_b16 v[202:203], v242 offset:17920
v_accvgpr_read_b32 v250, a196
v_accvgpr_read_b32 v251, a197
v_add_f32 v250, v250, v251
v_accvgpr_read_b32 v251, a222
v_mfma_f32_16x16x32_bf16 v[76:79], a[212:215], a[36:39], v[76:79]
ds_read_b64_tr_b16 v[204:205], v243 offset:16896
ds_read_b64_tr_b16 v[206:207], v243 offset:17920
v_add_f32 v250, v250, v251
v_accvgpr_read_b32 v249, a198
v_accvgpr_read_b32 v251, a199
v_add_f32 v249, v249, v251
v_mfma_f32_16x16x32_bf16 v[80:83], a[214:217], a[40:43], v[80:83]
ds_read_b64_tr_b16 v[208:209], v240 offset:17024
ds_read_b64_tr_b16 v[210:211], v240 offset:18048
v_accvgpr_read_b32 v251, a223
v_add_f32 v249, v249, v251
v_subrev_u32 v251, 562036736, v250
v_mfma_f32_16x16x32_bf16 v[84:87], a[212:215], a[40:43], v[84:87]
ds_read_b64_tr_b16 v[212:213], v241 offset:17024
ds_read_b64_tr_b16 v[214:215], v241 offset:18048
v_rcp_f32 v250, v250
v_cmp_le_u32 vcc, 1006632960, v251
v_subrev_u32 v251, 562036736, v249
v_mfma_f32_16x16x32_bf16 v[88:91], a[214:217], a[44:47], v[88:91]
ds_read_b64_tr_b16 v[216:217], v242 offset:17024
ds_read_b64_tr_b16 v[218:219], v242 offset:18048
s_mov_b64 s[92:93], vcc
v_cmp_le_u32 vcc, 1006632960, v251
v_rcp_f32_e64 v249, v249
v_mfma_f32_16x16x32_bf16 v[92:95], a[212:215], a[44:47], v[92:95]
ds_read_b64_tr_b16 v[220:221], v243 offset:17024
ds_read_b64_tr_b16 v[222:223], v243 offset:18048
s_or_b64 vcc, vcc, s[92:93]
s_cbranch_scc1 .La5_LA_SC4
.La5_LA_SR4:
v_mul_f32 v64, v64, v250
v_mul_f32 v65, v65, v250
v_mfma_f32_16x16x32_bf16 v[96:99], a[214:217], a[48:51], v[96:99]
v_mul_f32 v66, v66, v250
v_mul_f32 v67, v67, v250
v_mul_f32 v68, v68, v250
.La5_LW2_12:
s_cbranch_execz .La5_LW2_R0
v_mul_f32 v69, v69, v250
v_mul_f32 v70, v70, v250
v_mul_f32 v71, v71, v250
v_mfma_f32_16x16x32_bf16 v[100:103], a[212:215], a[48:51], v[100:103]
v_cvt_pk_bf16_f32 v160, v64, v65
v_cvt_pk_bf16_f32 v161, v66, v67
v_cvt_pk_bf16_f32 v162, v68, v69
v_cvt_pk_bf16_f32 v163, v70, v71
buffer_store_dwordx4 v[160:163], v246, s[44:47], s91 offen offset:0 sc1
v_mfma_f32_16x16x32_bf16 v[104:107], a[214:217], a[52:55], v[104:107]
v_mul_f32 v72, v72, v250
v_mul_f32 v73, v73, v250
v_mul_f32 v74, v74, v250
v_mul_f32 v75, v75, v250
v_mul_f32 v76, v76, v250
v_mul_f32 v77, v77, v250
v_mfma_f32_16x16x32_bf16 v[108:111], a[212:215], a[52:55], v[108:111]
v_mul_f32 v78, v78, v250
v_mul_f32 v79, v79, v250
v_cvt_pk_bf16_f32 v164, v72, v73
v_cvt_pk_bf16_f32 v165, v74, v75
v_cvt_pk_bf16_f32 v166, v76, v77
v_cvt_pk_bf16_f32 v167, v78, v79
v_mfma_f32_16x16x32_bf16 v[112:115], a[214:217], a[56:59], v[112:115]
buffer_store_dwordx4 v[164:167], v246, s[44:47], s91 offen offset:1024 sc1
v_mul_f32 v80, v80, v250
v_mul_f32 v81, v81, v250
v_mul_f32 v82, v82, v250
v_mul_f32 v83, v83, v250
v_mfma_f32_16x16x32_bf16 v[116:119], a[212:215], a[56:59], v[116:119]
v_mul_f32 v84, v84, v250
v_mul_f32 v85, v85, v250
v_mul_f32 v86, v86, v250
v_mul_f32 v87, v87, v250
v_cvt_pk_bf16_f32 v168, v80, v81
v_cvt_pk_bf16_f32 v169, v82, v83
v_mfma_f32_16x16x32_bf16 v[120:123], a[214:217], a[60:63], v[120:123]
v_cvt_pk_bf16_f32 v170, v84, v85
v_cvt_pk_bf16_f32 v171, v86, v87
buffer_store_dwordx4 v[168:171], v246, s[44:47], s91 offen offset:2048 sc1
v_mul_f32 v88, v88, v250
v_mul_f32 v89, v89, v250
v_mfma_f32_16x16x32_bf16 v[124:127], a[212:215], a[60:63], v[124:127]
s_waitcnt lgkmcnt(14)
v_mul_f32 v90, v90, v250
v_mul_f32 v91, v91, v250
v_mul_f32 v92, v92, v250
v_mul_f32 v93, v93, v250
v_mul_f32 v94, v94, v250
v_mfma_f32_16x16x32_bf16 v[0:3], v[192:195], v[128:131], 0
s_waitcnt lgkmcnt(12)
v_mul_f32 v95, v95, v250
v_cvt_pk_bf16_f32 v172, v88, v89
v_cvt_pk_bf16_f32 v173, v90, v91
v_cvt_pk_bf16_f32 v174, v92, v93
v_cvt_pk_bf16_f32 v175, v94, v95
v_mfma_f32_16x16x32_bf16 v[32:35], v[192:195], v[144:147], 0
ds_read_b64_tr_b16 v[192:193], v240 offset:25344
ds_read_b64_tr_b16 v[194:195], v240 offset:26368
buffer_store_dwordx4 v[172:175], v246, s[44:47], s91 offen offset:3072 sc1
v_mul_f32 v96, v96, v249
v_mul_f32 v97, v97, v249
v_mfma_f32_16x16x32_bf16 v[4:7], v[196:199], v[128:131], 0
s_waitcnt lgkmcnt(12)
v_mul_f32 v98, v98, v249
v_mul_f32 v99, v99, v249
v_mul_f32 v100, v100, v249
v_mul_f32 v101, v101, v249
v_mul_f32 v102, v102, v249
v_mfma_f32_16x16x32_bf16 v[36:39], v[196:199], v[144:147], 0
ds_read_b64_tr_b16 v[196:197], v241 offset:25344
ds_read_b64_tr_b16 v[198:199], v241 offset:26368
v_mul_f32 v103, v103, v249
v_cvt_pk_bf16_f32 v176, v96, v97
v_cvt_pk_bf16_f32 v177, v98, v99
v_cvt_pk_bf16_f32 v178, v100, v101
v_mfma_f32_16x16x32_bf16 v[8:11], v[200:203], v[128:131], 0
s_waitcnt lgkmcnt(12)
v_cvt_pk_bf16_f32 v179, v102, v103
buffer_store_dwordx4 v[176:179], v247, s[44:47], s91 offen offset:0 sc1
v_mul_f32 v104, v104, v249
v_mul_f32 v105, v105, v249
v_mfma_f32_16x16x32_bf16 v[40:43], v[200:203], v[144:147], 0
ds_read_b64_tr_b16 v[200:201], v242 offset:25344
ds_read_b64_tr_b16 v[202:203], v242 offset:26368
v_mul_f32 v106, v106, v249
v_mul_f32 v107, v107, v249
v_mul_f32 v108, v108, v249
v_mul_f32 v109, v109, v249
v_mfma_f32_16x16x32_bf16 v[12:15], v[204:207], v[128:131], 0
s_waitcnt lgkmcnt(12)
v_mul_f32 v110, v110, v249
v_mul_f32 v111, v111, v249
v_cvt_pk_bf16_f32 v180, v104, v105
v_cvt_pk_bf16_f32 v181, v106, v107
v_cvt_pk_bf16_f32 v182, v108, v109
v_mfma_f32_16x16x32_bf16 v[44:47], v[204:207], v[144:147], 0
ds_read_b64_tr_b16 v[204:205], v243 offset:25344
ds_read_b64_tr_b16 v[206:207], v243 offset:26368
v_cvt_pk_bf16_f32 v183, v110, v111
buffer_store_dwordx4 v[180:183], v247, s[44:47], s91 offen offset:1024 sc1
v_mul_f32 v112, v112, v249
v_mfma_f32_16x16x32_bf16 v[16:19], v[208:211], v[128:131], 0
s_waitcnt lgkmcnt(8)
v_mul_f32 v113, v113, v249
v_mul_f32 v114, v114, v249
v_mul_f32 v115, v115, v249
v_mul_f32 v116, v116, v249
v_mul_f32 v117, v117, v249
v_mfma_f32_16x16x32_bf16 v[48:51], v[208:211], v[144:147], 0
ds_read_b64_tr_b16 v[208:209], v240 offset:25472
ds_read_b64_tr_b16 v[210:211], v240 offset:26496
v_mul_f32 v118, v118, v249
v_mul_f32 v119, v119, v249
v_cvt_pk_bf16_f32 v184, v112, v113
v_cvt_pk_bf16_f32 v185, v114, v115
v_mfma_f32_16x16x32_bf16 v[20:23], v[212:215], v[128:131], 0
v_cvt_pk_bf16_f32 v186, v116, v117
v_cvt_pk_bf16_f32 v187, v118, v119
buffer_store_dwordx4 v[184:187], v247, s[44:47], s91 offen offset:2048 sc1
v_mul_f32 v120, v120, v249
v_mul_f32 v121, v121, v249
v_mfma_f32_16x16x32_bf16 v[52:55], v[212:215], v[144:147], 0
ds_read_b64_tr_b16 v[212:213], v241 offset:25472
ds_read_b64_tr_b16 v[214:215], v241 offset:26496
v_mul_f32 v122, v122, v249
v_mul_f32 v123, v123, v249
v_mul_f32 v124, v124, v249
v_mul_f32 v125, v125, v249
v_mfma_f32_16x16x32_bf16 v[24:27], v[216:219], v[128:131], 0
v_mul_f32 v126, v126, v249
v_mul_f32 v127, v127, v249
v_cvt_pk_bf16_f32 v188, v120, v121
v_cvt_pk_bf16_f32 v189, v122, v123
v_cvt_pk_bf16_f32 v190, v124, v125
v_cvt_pk_bf16_f32 v191, v126, v127
v_mfma_f32_16x16x32_bf16 v[56:59], v[216:219], v[144:147], 0
ds_read_b64_tr_b16 v[216:217], v242 offset:25472
ds_read_b64_tr_b16 v[218:219], v242 offset:26496
buffer_store_dwordx4 v[188:191], v247, s[44:47], s91 offen offset:3072 sc1
v_mfma_f32_16x16x32_bf16 v[28:31], v[220:223], v[128:131], 0
s_waitcnt lgkmcnt(6)
v_mfma_f32_16x16x32_bf16 v[60:63], v[220:223], v[144:147], 0
ds_read_b64_tr_b16 v[220:221], v243 offset:25472
ds_read_b64_tr_b16 v[222:223], v243 offset:26496
v_mfma_f32_16x16x32_bf16 v[0:3], v[192:195], v[132:135], v[0:3]
v_accvgpr_read_b32 v224, a228
v_add_u32 v225, 0x8000, v224
v_add_u32 v226, 0x2000, v224
v_add_u32 v227, 0xa000, v224
v_mfma_f32_16x16x32_bf16 v[32:35], v[192:195], v[148:151], v[32:35]
ds_read_b64_tr_b16 v[192:193], v240 offset:33792
ds_read_b64_tr_b16 v[194:195], v240 offset:34816
v_accvgpr_read_b32 v248, a225
v_accvgpr_read_b32 v228, a229
v_mfma_f32_16x16x32_bf16 v[4:7], v[196:199], v[132:135], v[4:7]
v_accvgpr_read_b32 v229, a230
v_mfma_f32_16x16x32_bf16 v[36:39], v[196:199], v[148:151], v[36:39]
ds_read_b64_tr_b16 v[196:197], v241 offset:33792
ds_read_b64_tr_b16 v[198:199], v241 offset:34816
v_mfma_f32_16x16x32_bf16 v[8:11], v[200:203], v[132:135], v[8:11]
v_mfma_f32_16x16x32_bf16 v[40:43], v[200:203], v[148:151], v[40:43]
ds_read_b64_tr_b16 v[200:201], v242 offset:33792
ds_read_b64_tr_b16 v[202:203], v242 offset:34816
v_mfma_f32_16x16x32_bf16 v[12:15], v[204:207], v[132:135], v[12:15]
s_waitcnt lgkmcnt(6)
v_mfma_f32_16x16x32_bf16 v[44:47], v[204:207], v[148:151], v[44:47]
ds_read_b64_tr_b16 v[204:205], v243 offset:33792
ds_read_b64_tr_b16 v[206:207], v243 offset:34816
v_mfma_f32_16x16x32_bf16 v[16:19], v[208:211], v[132:135], v[16:19]
v_mfma_f32_16x16x32_bf16 v[48:51], v[208:211], v[148:151], v[48:51]
ds_read_b64_tr_b16 v[208:209], v240 offset:33920
ds_read_b64_tr_b16 v[210:211], v240 offset:34944
v_mfma_f32_16x16x32_bf16 v[20:23], v[212:215], v[132:135], v[20:23]
v_mfma_f32_16x16x32_bf16 v[52:55], v[212:215], v[148:151], v[52:55]
ds_read_b64_tr_b16 v[212:213], v241 offset:33920
ds_read_b64_tr_b16 v[214:215], v241 offset:34944
v_mfma_f32_16x16x32_bf16 v[24:27], v[216:219], v[132:135], v[24:27]
v_mfma_f32_16x16x32_bf16 v[56:59], v[216:219], v[148:151], v[56:59]
ds_read_b64_tr_b16 v[216:217], v242 offset:33920
ds_read_b64_tr_b16 v[218:219], v242 offset:34944
v_mfma_f32_16x16x32_bf16 v[28:31], v[220:223], v[132:135], v[28:31]
s_waitcnt lgkmcnt(6)
v_mfma_f32_16x16x32_bf16 v[60:63], v[220:223], v[148:151], v[60:63]
ds_read_b64_tr_b16 v[220:221], v243 offset:33920
ds_read_b64_tr_b16 v[222:223], v243 offset:34944
v_mfma_f32_16x16x32_bf16 v[0:3], v[192:195], v[136:139], v[0:3]
v_mfma_f32_16x16x32_bf16 v[32:35], v[192:195], v[152:155], v[32:35]
ds_read_b64_tr_b16 v[192:193], v240 offset:42240
ds_read_b64_tr_b16 v[194:195], v240 offset:43264
v_mfma_f32_16x16x32_bf16 v[4:7], v[196:199], v[136:139], v[4:7]
v_mfma_f32_16x16x32_bf16 v[36:39], v[196:199], v[152:155], v[36:39]
ds_read_b64_tr_b16 v[196:197], v241 offset:42240
ds_read_b64_tr_b16 v[198:199], v241 offset:43264
v_mfma_f32_16x16x32_bf16 v[8:11], v[200:203], v[136:139], v[8:11]
v_mfma_f32_16x16x32_bf16 v[40:43], v[200:203], v[152:155], v[40:43]
ds_read_b64_tr_b16 v[200:201], v242 offset:42240
ds_read_b64_tr_b16 v[202:203], v242 offset:43264
v_mfma_f32_16x16x32_bf16 v[12:15], v[204:207], v[136:139], v[12:15]
s_waitcnt lgkmcnt(6)
v_mfma_f32_16x16x32_bf16 v[44:47], v[204:207], v[152:155], v[44:47]
ds_read_b64_tr_b16 v[204:205], v243 offset:42240
ds_read_b64_tr_b16 v[206:207], v243 offset:43264
v_mfma_f32_16x16x32_bf16 v[16:19], v[208:211], v[136:139], v[16:19]
v_mfma_f32_16x16x32_bf16 v[48:51], v[208:211], v[152:155], v[48:51]
ds_read_b64_tr_b16 v[208:209], v240 offset:42368
ds_read_b64_tr_b16 v[210:211], v240 offset:43392
v_mfma_f32_16x16x32_bf16 v[20:23], v[212:215], v[136:139], v[20:23]
v_mfma_f32_16x16x32_bf16 v[52:55], v[212:215], v[152:155], v[52:55]
ds_read_b64_tr_b16 v[212:213], v241 offset:42368
ds_read_b64_tr_b16 v[214:215], v241 offset:43392
v_accvgpr_read_b32 v249, a231
s_waitcnt vmcnt(0)
buffer_store_dwordx2 v[252:253], v249, s[64:67], s6 offen offset:0 sc1
v_mfma_f32_16x16x32_bf16 v[24:27], v[216:219], v[136:139], v[24:27]
v_mfma_f32_16x16x32_bf16 v[56:59], v[216:219], v[152:155], v[56:59]
ds_read_b64_tr_b16 v[216:217], v242 offset:42368
ds_read_b64_tr_b16 v[218:219], v242 offset:43392
v_mfma_f32_16x16x32_bf16 v[28:31], v[220:223], v[136:139], v[28:31]
s_waitcnt lgkmcnt(6)
v_mfma_f32_16x16x32_bf16 v[60:63], v[220:223], v[152:155], v[60:63]
ds_read_b64_tr_b16 v[220:221], v243 offset:42368
ds_read_b64_tr_b16 v[222:223], v243 offset:43392
v_mfma_f32_16x16x32_bf16 v[0:3], v[192:195], v[140:143], v[0:3]
v_mfma_f32_16x16x32_bf16 v[32:35], v[192:195], v[156:159], v[32:35]
ds_read_b64_tr_b16 v[192:193], v240 offset:16896
ds_read_b64_tr_b16 v[194:195], v240 offset:17920
v_mfma_f32_16x16x32_bf16 v[4:7], v[196:199], v[140:143], v[4:7]
v_mfma_f32_16x16x32_bf16 v[36:39], v[196:199], v[156:159], v[36:39]
ds_read_b64_tr_b16 v[196:197], v241 offset:16896
ds_read_b64_tr_b16 v[198:199], v241 offset:17920
v_mfma_f32_16x16x32_bf16 v[8:11], v[200:203], v[140:143], v[8:11]
v_mfma_f32_16x16x32_bf16 v[40:43], v[200:203], v[156:159], v[40:43]
ds_read_b64_tr_b16 v[200:201], v242 offset:16896
ds_read_b64_tr_b16 v[202:203], v242 offset:17920
v_mfma_f32_16x16x32_bf16 v[12:15], v[204:207], v[140:143], v[12:15]
s_waitcnt lgkmcnt(6)
v_mfma_f32_16x16x32_bf16 v[44:47], v[204:207], v[156:159], v[44:47]
ds_read_b64_tr_b16 v[204:205], v243 offset:16896
ds_read_b64_tr_b16 v[206:207], v243 offset:17920
v_mfma_f32_16x16x32_bf16 v[16:19], v[208:211], v[140:143], v[16:19]
v_mfma_f32_16x16x32_bf16 v[48:51], v[208:211], v[156:159], v[48:51]
ds_read_b64_tr_b16 v[208:209], v240 offset:17024
ds_read_b64_tr_b16 v[210:211], v240 offset:18048
v_mfma_f32_16x16x32_bf16 v[20:23], v[212:215], v[140:143], v[20:23]
v_mfma_f32_16x16x32_bf16 v[52:55], v[212:215], v[156:159], v[52:55]
ds_read_b64_tr_b16 v[212:213], v241 offset:17024
ds_read_b64_tr_b16 v[214:215], v241 offset:18048
v_mfma_f32_16x16x32_bf16 v[24:27], v[216:219], v[140:143], v[24:27]
v_mfma_f32_16x16x32_bf16 v[56:59], v[216:219], v[156:159], v[56:59]
ds_read_b64_tr_b16 v[216:217], v242 offset:17024
ds_read_b64_tr_b16 v[218:219], v242 offset:18048
v_mfma_f32_16x16x32_bf16 v[28:31], v[220:223], v[140:143], v[28:31]
s_waitcnt lgkmcnt(6)
v_mfma_f32_16x16x32_bf16 v[60:63], v[220:223], v[156:159], v[60:63]
ds_read_b64_tr_b16 v[220:221], v243 offset:17024
ds_read_b64_tr_b16 v[222:223], v243 offset:18048
s_cmp_lg_u32 s30, 0
s_cbranch_scc1 .La5_LO_W
; ---- O projection: 256 x 128 tile over eight heads
.La5_LO_START:
.La5_LW2_13:
s_cbranch_execz .La5_LW2_R1
v_mfma_f32_16x16x32_bf16 v[64:67], v[192:195], v[160:163], 0
buffer_load_dwordx2 v[250:251], v248, s[64:67], s5 offen sc1
v_mfma_f32_16x16x32_bf16 v[96:99], v[192:195], v[176:179], 0
ds_read_b64_tr_b16 v[192:193], v240 offset:25344
ds_read_b64_tr_b16 v[194:195], v240 offset:26368
v_mfma_f32_16x16x32_bf16 v[68:71], v[196:199], v[160:163], 0
s_add_u32 m0, s83, 0
s_bfe_u32 s80, s4, 262148
v_mfma_f32_16x16x32_bf16 v[100:103], v[196:199], v[176:179], 0
ds_read_b64_tr_b16 v[196:197], v241 offset:25344
ds_read_b64_tr_b16 v[198:199], v241 offset:26368
s_xor_b32 s80, s80, s72
s_lshl_b32 s80, s80, 18
s_add_u32 s80, s80, 131072
v_mfma_f32_16x16x32_bf16 v[72:75], v[200:203], v[160:163], 0
buffer_load_dwordx4 v224, s[56:59], s80 offen lds
v_mfma_f32_16x16x32_bf16 v[104:107], v[200:203], v[176:179], 0
ds_read_b64_tr_b16 v[200:201], v242 offset:25344
ds_read_b64_tr_b16 v[202:203], v242 offset:26368
v_mfma_f32_16x16x32_bf16 v[76:79], v[204:207], v[160:163], 0
s_add_u32 m0, s83, 1024
s_waitcnt lgkmcnt(6)
v_mfma_f32_16x16x32_bf16 v[108:111], v[204:207], v[176:179], 0
ds_read_b64_tr_b16 v[204:205], v243 offset:25344
ds_read_b64_tr_b16 v[206:207], v243 offset:26368
v_mfma_f32_16x16x32_bf16 v[80:83], v[208:211], v[160:163], 0
buffer_load_dwordx4 v225, s[56:59], s80 offen lds
v_mfma_f32_16x16x32_bf16 v[112:115], v[208:211], v[176:179], 0
ds_read_b64_tr_b16 v[208:209], v240 offset:25472
ds_read_b64_tr_b16 v[210:211], v240 offset:26496
v_mfma_f32_16x16x32_bf16 v[84:87], v[212:215], v[160:163], 0
s_add_u32 m0, s83, 2176
s_bfe_u32 s53, s4, 262148
v_mfma_f32_16x16x32_bf16 v[116:119], v[212:215], v[176:179], 0
ds_read_b64_tr_b16 v[212:213], v241 offset:25472
ds_read_b64_tr_b16 v[214:215], v241 offset:26496
v_mfma_f32_16x16x32_bf16 v[88:91], v[216:219], v[160:163], 0
buffer_load_dwordx4 v226, s[56:59], s80 offen lds
v_mfma_f32_16x16x32_bf16 v[120:123], v[216:219], v[176:179], 0
ds_read_b64_tr_b16 v[216:217], v242 offset:25472
ds_read_b64_tr_b16 v[218:219], v242 offset:26496
v_mfma_f32_16x16x32_bf16 v[92:95], v[220:223], v[160:163], 0
s_add_u32 m0, s83, 3200
s_waitcnt lgkmcnt(6)
v_mfma_f32_16x16x32_bf16 v[124:127], v[220:223], v[176:179], 0
ds_read_b64_tr_b16 v[220:221], v243 offset:25472
ds_read_b64_tr_b16 v[222:223], v243 offset:26496
v_mfma_f32_16x16x32_bf16 v[64:67], v[192:195], v[164:167], v[64:67]
buffer_load_dwordx4 v227, s[56:59], s80 offen lds
v_mfma_f32_16x16x32_bf16 v[96:99], v[192:195], v[180:183], v[96:99]
ds_read_b64_tr_b16 v[192:193], v240 offset:33792
ds_read_b64_tr_b16 v[194:195], v240 offset:34816
v_mfma_f32_16x16x32_bf16 v[68:71], v[196:199], v[164:167], v[68:71]
s_add_u32 m0, s83, 118272
s_bfe_u32 s80, s4, 262152
v_mfma_f32_16x16x32_bf16 v[100:103], v[196:199], v[180:183], v[100:103]
ds_read_b64_tr_b16 v[196:197], v241 offset:33792
ds_read_b64_tr_b16 v[198:199], v241 offset:34816
s_xor_b32 s80, s80, s72
s_lshl_b32 s80, s80, 18
v_mfma_f32_16x16x32_bf16 v[72:75], v[200:203], v[164:167], v[72:75]
buffer_load_dwordx4 v224, s[56:59], s80 offen lds
v_mfma_f32_16x16x32_bf16 v[104:107], v[200:203], v[180:183], v[104:107]
s_mov_b64 exec, 0
s_cmp_eq_u32 s3, 0
s_cbranch_scc1 .La5_LW3_0
s_cmp_eq_u32 s3, 1
s_cbranch_scc1 .La5_LW3_1
s_cmp_eq_u32 s3, 2
s_cbranch_scc1 .La5_LW3_2
s_branch .La5_LW3_3
.La5_LW3_R0:
s_mov_b64 exec, -1
ds_read_b64_tr_b16 v[200:201], v242 offset:33792
ds_read_b64_tr_b16 v[202:203], v242 offset:34816
v_mfma_f32_16x16x32_bf16 v[76:79], v[204:207], v[164:167], v[76:79]
s_waitcnt vmcnt(5)
v_cmp_eq_u64 vcc, v[250:251], v[252:253]
s_nop 0
s_bitcmp1_b32 vcc_lo, 1
s_nop 0
s_cbranch_scc0 .La5_LO_N1
.La5_LO_K1:
s_waitcnt lgkmcnt(6)
v_mfma_f32_16x16x32_bf16 v[108:111], v[204:207], v[180:183], v[108:111]
ds_read_b64_tr_b16 v[204:205], v243 offset:33792
ds_read_b64_tr_b16 v[206:207], v243 offset:34816
s_xor_b32 s53, s53, s72
s_lshl_b32 s53, s53, 13
v_mfma_f32_16x16x32_bf16 v[80:83], v[208:211], v[164:167], v[80:83]
buffer_load_dwordx4 a[0:3], v244, s[44:47], s53 offen offset:0 sc1
v_mfma_f32_16x16x32_bf16 v[112:115], v[208:211], v[180:183], v[112:115]
ds_read_b64_tr_b16 v[208:209], v240 offset:33920
ds_read_b64_tr_b16 v[210:211], v240 offset:34944
v_mfma_f32_16x16x32_bf16 v[84:87], v[212:215], v[164:167], v[84:87]
buffer_load_dwordx4 a[4:7], v245, s[44:47], s53 offen offset:0 sc1
v_mfma_f32_16x16x32_bf16 v[116:119], v[212:215], v[180:183], v[116:119]
ds_read_b64_tr_b16 v[212:213], v241 offset:33920
ds_read_b64_tr_b16 v[214:215], v241 offset:34944
v_mfma_f32_16x16x32_bf16 v[88:91], v[216:219], v[164:167], v[88:91]
buffer_load_dwordx4 a[8:11], v246, s[44:47], s53 offen offset:0 sc1
v_mfma_f32_16x16x32_bf16 v[120:123], v[216:219], v[180:183], v[120:123]
ds_read_b64_tr_b16 v[216:217], v242 offset:33920
ds_read_b64_tr_b16 v[218:219], v242 offset:34944
v_mfma_f32_16x16x32_bf16 v[92:95], v[220:223], v[164:167], v[92:95]
s_waitcnt lgkmcnt(6)
s_bfe_u32 s54, s4, 262152
v_mfma_f32_16x16x32_bf16 v[124:127], v[220:223], v[180:183], v[124:127]
ds_read_b64_tr_b16 v[220:221], v243 offset:33920
ds_read_b64_tr_b16 v[222:223], v243 offset:34944
v_mfma_f32_16x16x32_bf16 v[64:67], v[192:195], v[168:171], v[64:67]
buffer_load_dwordx4 a[12:15], v247, s[44:47], s53 offen offset:0 sc1
v_mfma_f32_16x16x32_bf16 v[96:99], v[192:195], v[184:187], v[96:99]
ds_read_b64_tr_b16 v[192:193], v240 offset:42240
ds_read_b64_tr_b16 v[194:195], v240 offset:43264
v_mfma_f32_16x16x32_bf16 v[68:71], v[196:199], v[168:171], v[68:71]
buffer_load_dwordx4 a[16:19], v244, s[44:47], s53 offen offset:1024 sc1
v_mfma_f32_16x16x32_bf16 v[100:103], v[196:199], v[184:187], v[100:103]
ds_read_b64_tr_b16 v[196:197], v241 offset:42240
ds_read_b64_tr_b16 v[198:199], v241 offset:43264
v_mfma_f32_16x16x32_bf16 v[72:75], v[200:203], v[168:171], v[72:75]
buffer_load_dwordx4 a[20:23], v245, s[44:47], s53 offen offset:1024 sc1
v_mfma_f32_16x16x32_bf16 v[104:107], v[200:203], v[184:187], v[104:107]
ds_read_b64_tr_b16 v[200:201], v242 offset:42240
ds_read_b64_tr_b16 v[202:203], v242 offset:43264
v_mfma_f32_16x16x32_bf16 v[76:79], v[204:207], v[168:171], v[76:79]
s_waitcnt lgkmcnt(6)
s_xor_b32 s54, s54, s72
v_mfma_f32_16x16x32_bf16 v[108:111], v[204:207], v[184:187], v[108:111]
ds_read_b64_tr_b16 v[204:205], v243 offset:42240
ds_read_b64_tr_b16 v[206:207], v243 offset:43264
v_mfma_f32_16x16x32_bf16 v[80:83], v[208:211], v[168:171], v[80:83]
buffer_load_dwordx4 a[24:27], v246, s[44:47], s53 offen offset:1024 sc1
v_mfma_f32_16x16x32_bf16 v[112:115], v[208:211], v[184:187], v[112:115]
ds_read_b64_tr_b16 v[208:209], v240 offset:42368
ds_read_b64_tr_b16 v[210:211], v240 offset:43392
v_mfma_f32_16x16x32_bf16 v[84:87], v[212:215], v[168:171], v[84:87]
buffer_load_dwordx4 a[28:31], v247, s[44:47], s53 offen offset:1024 sc1
s_add_u32 m0, s83, 119296
v_mfma_f32_16x16x32_bf16 v[116:119], v[212:215], v[184:187], v[116:119]
ds_read_b64_tr_b16 v[212:213], v241 offset:42368
ds_read_b64_tr_b16 v[214:215], v241 offset:43392
v_mfma_f32_16x16x32_bf16 v[88:91], v[216:219], v[168:171], v[88:91]
buffer_load_dwordx4 v225, s[56:59], s80 offen lds
v_mfma_f32_16x16x32_bf16 v[120:123], v[216:219], v[184:187], v[120:123]
ds_read_b64_tr_b16 v[216:217], v242 offset:42368
ds_read_b64_tr_b16 v[218:219], v242 offset:43392
v_mfma_f32_16x16x32_bf16 v[92:95], v[220:223], v[168:171], v[92:95]
s_add_u32 m0, s83, 120448
s_nop 0
s_barrier
s_waitcnt lgkmcnt(6)
v_mfma_f32_16x16x32_bf16 v[124:127], v[220:223], v[184:187], v[124:127]
ds_read_b64_tr_b16 v[220:221], v243 offset:42368
ds_read_b64_tr_b16 v[222:223], v243 offset:43392
v_mfma_f32_16x16x32_bf16 v[64:67], v[192:195], v[172:175], v[64:67]
buffer_load_dwordx4 v226, s[56:59], s80 offen lds
v_mfma_f32_16x16x32_bf16 v[96:99], v[192:195], v[188:191], v[96:99]
ds_read_b64_tr_b16 v[192:193], v240 offset:50688
ds_read_b64_tr_b16 v[194:195], v240 offset:51712
v_mfma_f32_16x16x32_bf16 v[68:71], v[196:199], v[172:175], v[68:71]
buffer_load_dwordx4 a[32:35], v244, s[44:47], s53 offen offset:2048 sc1
v_mfma_f32_16x16x32_bf16 v[100:103], v[196:199], v[188:191], v[100:103]
ds_read_b64_tr_b16 v[196:197], v241 offset:50688
ds_read_b64_tr_b16 v[198:199], v241 offset:51712
v_mfma_f32_16x16x32_bf16 v[72:75], v[200:203], v[172:175], v[72:75]
buffer_load_dwordx4 a[36:39], v245, s[44:47], s53 offen offset:2048 sc1
v_mfma_f32_16x16x32_bf16 v[104:107], v[200:203], v[188:191], v[104:107]
ds_read_b64_tr_b16 v[200:201], v242 offset:50688
ds_read_b64_tr_b16 v[202:203], v242 offset:51712
v_mfma_f32_16x16x32_bf16 v[76:79], v[204:207], v[172:175], v[76:79]
s_waitcnt lgkmcnt(6)
s_lshl_b32 s54, s54, 13
v_mfma_f32_16x16x32_bf16 v[108:111], v[204:207], v[188:191], v[108:111]
ds_read_b64_tr_b16 v[204:205], v243 offset:50688
ds_read_b64_tr_b16 v[206:207], v243 offset:51712
v_mfma_f32_16x16x32_bf16 v[80:83], v[208:211], v[172:175], v[80:83]
buffer_load_dwordx4 a[40:43], v246, s[44:47], s53 offen offset:2048 sc1
v_mfma_f32_16x16x32_bf16 v[112:115], v[208:211], v[188:191], v[112:115]
ds_read_b64_tr_b16 v[208:209], v240 offset:50816
ds_read_b64_tr_b16 v[210:211], v240 offset:51840
v_mfma_f32_16x16x32_bf16 v[84:87], v[212:215], v[172:175], v[84:87]
buffer_load_dwordx4 a[44:47], v247, s[44:47], s53 offen offset:2048 sc1
s_add_u32 m0, s83, 121472
v_mfma_f32_16x16x32_bf16 v[116:119], v[212:215], v[188:191], v[116:119]
ds_read_b64_tr_b16 v[212:213], v241 offset:50816
ds_read_b64_tr_b16 v[214:215], v241 offset:51840
v_mfma_f32_16x16x32_bf16 v[88:91], v[216:219], v[172:175], v[88:91]
buffer_load_dwordx4 v227, s[56:59], s80 offen lds
v_mfma_f32_16x16x32_bf16 v[120:123], v[216:219], v[188:191], v[120:123]
ds_read_b64_tr_b16 v[216:217], v242 offset:50816
ds_read_b64_tr_b16 v[218:219], v242 offset:51840
v_mfma_f32_16x16x32_bf16 v[92:95], v[220:223], v[172:175], v[92:95]
s_waitcnt vmcnt(11) lgkmcnt(6)
s_add_u32 s80, s80, 131072
v_mfma_f32_16x16x32_bf16 v[124:127], v[220:223], v[188:191], v[124:127]
ds_read_b64_tr_b16 v[220:221], v243 offset:50816
ds_read_b64_tr_b16 v[222:223], v243 offset:51840
v_mfma_f32_16x16x32_bf16 v[0:3], v[192:195], a[0:3], v[0:3]
s_add_u32 m0, s83, 67584
s_bfe_u32 s55, s4, 262156
v_mfma_f32_16x16x32_bf16 v[32:35], v[192:195], a[4:7], v[32:35]
buffer_load_dwordx4 v224, s[56:59], s80 offen lds
v_mfma_f32_16x16x32_bf16 v[64:67], v[192:195], a[8:11], v[64:67]
s_xor_b32 s55, s55, s72
s_lshl_b32 s55, s55, 13
v_mfma_f32_16x16x32_bf16 v[96:99], v[192:195], a[12:15], v[96:99]
ds_read_b64_tr_b16 v[192:193], v240 offset:59136
ds_read_b64_tr_b16 v[194:195], v240 offset:60160
v_mfma_f32_16x16x32_bf16 v[4:7], v[196:199], a[0:3], v[4:7]
s_add_u32 m0, s83, 68608
s_bfe_u32 s52, s4, 262160
v_mfma_f32_16x16x32_bf16 v[36:39], v[196:199], a[4:7], v[36:39]
buffer_load_dwordx4 v225, s[56:59], s80 offen lds
v_mfma_f32_16x16x32_bf16 v[68:71], v[196:199], a[8:11], v[68:71]
s_xor_b32 s52, s52, s72
s_lshl_b32 s52, s52, 13
v_mfma_f32_16x16x32_bf16 v[100:103], v[196:199], a[12:15], v[100:103]
ds_read_b64_tr_b16 v[196:197], v241 offset:59136
ds_read_b64_tr_b16 v[198:199], v241 offset:60160
v_mfma_f32_16x16x32_bf16 v[8:11], v[200:203], a[0:3], v[8:11]
v_mfma_f32_16x16x32_bf16 v[40:43], v[200:203], a[4:7], v[40:43]
buffer_load_dwordx2 v[250:251], v248, s[64:67], s5 offen sc1
v_mfma_f32_16x16x32_bf16 v[72:75], v[200:203], a[8:11], v[72:75]
v_mfma_f32_16x16x32_bf16 v[104:107], v[200:203], a[12:15], v[104:107]
ds_read_b64_tr_b16 v[200:201], v242 offset:59136
ds_read_b64_tr_b16 v[202:203], v242 offset:60160
v_mfma_f32_16x16x32_bf16 v[12:15], v[204:207], a[0:3], v[12:15]
s_add_u32 m0, s83, 69760
v_mfma_f32_16x16x32_bf16 v[44:47], v[204:207], a[4:7], v[44:47]
buffer_load_dwordx4 v226, s[56:59], s80 offen lds
v_mfma_f32_16x16x32_bf16 v[76:79], v[204:207], a[8:11], v[76:79]
s_waitcnt vmcnt(11) lgkmcnt(2)
v_mfma_f32_16x16x32_bf16 v[108:111], v[204:207], a[12:15], v[108:111]
ds_read_b64_tr_b16 v[204:205], v243 offset:59136
ds_read_b64_tr_b16 v[206:207], v243 offset:60160
v_mfma_f32_16x16x32_bf16 v[16:19], v[208:211], a[0:3], v[16:19]
s_add_u32 m0, s83, 70784
v_mfma_f32_16x16x32_bf16 v[48:51], v[208:211], a[4:7], v[48:51]
buffer_load_dwordx4 v227, s[56:59], s80 offen lds
v_mfma_f32_16x16x32_bf16 v[80:83], v[208:211], a[8:11], v[80:83]
s_bfe_u32 s80, s4, 262156
s_xor_b32 s80, s80, s72
v_mfma_f32_16x16x32_bf16 v[112:115], v[208:211], a[12:15], v[112:115]
ds_read_b64_tr_b16 v[208:209], v240 offset:59264
ds_read_b64_tr_b16 v[210:211], v240 offset:60288
v_mfma_f32_16x16x32_bf16 v[20:23], v[212:215], a[0:3], v[20:23]
s_lshl_b32 s80, s80, 18
v_mfma_f32_16x16x32_bf16 v[52:55], v[212:215], a[4:7], v[52:55]
buffer_load_dwordx4 a[48:51], v244, s[44:47], s53 offen offset:3072 sc1
v_mfma_f32_16x16x32_bf16 v[84:87], v[212:215], a[8:11], v[84:87]
v_mfma_f32_16x16x32_bf16 v[116:119], v[212:215], a[12:15], v[116:119]
ds_read_b64_tr_b16 v[212:213], v241 offset:59264
ds_read_b64_tr_b16 v[214:215], v241 offset:60288
v_mfma_f32_16x16x32_bf16 v[24:27], v[216:219], a[0:3], v[24:27]
v_mfma_f32_16x16x32_bf16 v[56:59], v[216:219], a[4:7], v[56:59]
buffer_load_dwordx4 a[52:55], v245, s[44:47], s53 offen offset:3072 sc1
v_mfma_f32_16x16x32_bf16 v[88:91], v[216:219], a[8:11], v[88:91]
v_mfma_f32_16x16x32_bf16 v[120:123], v[216:219], a[12:15], v[120:123]
ds_read_b64_tr_b16 v[216:217], v242 offset:59264
ds_read_b64_tr_b16 v[218:219], v242 offset:60288
v_mfma_f32_16x16x32_bf16 v[28:31], v[220:223], a[0:3], v[28:31]
v_mfma_f32_16x16x32_bf16 v[60:63], v[220:223], a[4:7], v[60:63]
buffer_load_dwordx4 a[0:3], v246, s[44:47], s53 offen offset:3072 sc1
v_mfma_f32_16x16x32_bf16 v[92:95], v[220:223], a[8:11], v[92:95]
v_mfma_f32_16x16x32_bf16 v[124:127], v[220:223], a[12:15], v[124:127]
ds_read_b64_tr_b16 v[220:221], v243 offset:59264
ds_read_b64_tr_b16 v[222:223], v243 offset:60288
v_mfma_f32_16x16x32_bf16 v[0:3], v[192:195], a[16:19], v[0:3]
v_mfma_f32_16x16x32_bf16 v[32:35], v[192:195], a[20:23], v[32:35]
buffer_load_dwordx4 a[4:7], v247, s[44:47], s53 offen offset:3072 sc1
v_mfma_f32_16x16x32_bf16 v[64:67], v[192:195], a[24:27], v[64:67]
s_bfe_u32 s53, s4, 262164
s_xor_b32 s53, s53, s72
v_mfma_f32_16x16x32_bf16 v[96:99], v[192:195], a[28:31], v[96:99]
s_lshl_b32 s53, s53, 13
v_mfma_f32_16x16x32_bf16 v[4:7], v[196:199], a[16:19], v[4:7]
s_add_u32 m0, s83, 84480
s_waitcnt lgkmcnt(0)
s_barrier
v_mfma_f32_16x16x32_bf16 v[36:39], v[196:199], a[20:23], v[36:39]
buffer_load_dwordx4 v224, s[56:59], s80 offen lds
v_mfma_f32_16x16x32_bf16 v[68:71], v[196:199], a[24:27], v[68:71]
ds_read_b64_tr_b16 v[192:193], v240 offset:0
ds_read_b64_tr_b16 v[194:195], v240 offset:1024
s_nop 0
v_mfma_f32_16x16x32_bf16 v[100:103], v[196:199], a[28:31], v[100:103]
ds_read_b64_tr_b16 v[196:197], v241 offset:0
ds_read_b64_tr_b16 v[198:199], v241 offset:1024
v_mfma_f32_16x16x32_bf16 v[8:11], v[200:203], a[16:19], v[8:11]
s_add_u32 m0, s83, 85504
v_mfma_f32_16x16x32_bf16 v[40:43], v[200:203], a[20:23], v[40:43]
buffer_load_dwordx4 v225, s[56:59], s80 offen lds
v_mfma_f32_16x16x32_bf16 v[72:75], v[200:203], a[24:27], v[72:75]
v_mfma_f32_16x16x32_bf16 v[104:107], v[200:203], a[28:31], v[104:107]
ds_read_b64_tr_b16 v[200:201], v242 offset:0
ds_read_b64_tr_b16 v[202:203], v242 offset:1024
v_mfma_f32_16x16x32_bf16 v[12:15], v[204:207], a[16:19], v[12:15]
s_add_u32 m0, s83, 86656
v_mfma_f32_16x16x32_bf16 v[44:47], v[204:207], a[20:23], v[44:47]
buffer_load_dwordx4 v226, s[56:59], s80 offen lds
v_mfma_f32_16x16x32_bf16 v[76:79], v[204:207], a[24:27], v[76:79]
v_mfma_f32_16x16x32_bf16 v[108:111], v[204:207], a[28:31], v[108:111]
ds_read_b64_tr_b16 v[204:205], v243 offset:0
ds_read_b64_tr_b16 v[206:207], v243 offset:1024
v_mfma_f32_16x16x32_bf16 v[16:19], v[208:211], a[16:19], v[16:19]
s_add_u32 m0, s83, 87680
v_mfma_f32_16x16x32_bf16 v[48:51], v[208:211], a[20:23], v[48:51]
buffer_load_dwordx4 v227, s[56:59], s80 offen lds
v_mfma_f32_16x16x32_bf16 v[80:83], v[208:211], a[24:27], v[80:83]
s_add_u32 s80, s80, 131072
s_waitcnt vmcnt(10)
v_mfma_f32_16x16x32_bf16 v[112:115], v[208:211], a[28:31], v[112:115]
ds_read_b64_tr_b16 v[208:209], v240 offset:128
ds_read_b64_tr_b16 v[210:211], v240 offset:1152
.La5_LW3_0:
s_cbranch_execz .La5_LW3_4
v_mfma_f32_16x16x32_bf16 v[20:23], v[212:215], a[16:19], v[20:23]
s_add_u32 m0, s83, 101376
v_cmp_eq_u64 vcc, v[250:251], v[252:253]
v_mfma_f32_16x16x32_bf16 v[52:55], v[212:215], a[20:23], v[52:55]
buffer_load_dwordx4 v224, s[56:59], s80 offen lds
v_mfma_f32_16x16x32_bf16 v[84:87], v[212:215], a[24:27], v[84:87]
s_bitcmp1_b32 vcc_lo, 2
s_cbranch_scc0 .La5_LO_N2
.La5_LO_K2:
v_mfma_f32_16x16x32_bf16 v[116:119], v[212:215], a[28:31], v[116:119]
ds_read_b64_tr_b16 v[212:213], v241 offset:128
ds_read_b64_tr_b16 v[214:215], v241 offset:1152
v_mfma_f32_16x16x32_bf16 v[24:27], v[216:219], a[16:19], v[24:27]
v_mfma_f32_16x16x32_bf16 v[56:59], v[216:219], a[20:23], v[56:59]
buffer_load_dwordx4 a[8:11], v244, s[44:47], s54 offen offset:0 sc1
v_mfma_f32_16x16x32_bf16 v[88:91], v[216:219], a[24:27], v[88:91]
v_mfma_f32_16x16x32_bf16 v[120:123], v[216:219], a[28:31], v[120:123]
ds_read_b64_tr_b16 v[216:217], v242 offset:128
ds_read_b64_tr_b16 v[218:219], v242 offset:1152
v_mfma_f32_16x16x32_bf16 v[28:31], v[220:223], a[16:19], v[28:31]
v_mfma_f32_16x16x32_bf16 v[60:63], v[220:223], a[20:23], v[60:63]
v_mfma_f32_16x16x32_bf16 v[92:95], v[220:223], a[24:27], v[92:95]
s_waitcnt lgkmcnt(2)
v_mfma_f32_16x16x32_bf16 v[124:127], v[220:223], a[28:31], v[124:127]
ds_read_b64_tr_b16 v[220:221], v243 offset:128
ds_read_b64_tr_b16 v[222:223], v243 offset:1152
v_mfma_f32_16x16x32_bf16 v[0:3], v[192:195], a[32:35], v[0:3]
v_mfma_f32_16x16x32_bf16 v[32:35], v[192:195], a[36:39], v[32:35]
buffer_load_dwordx4 a[12:15], v245, s[44:47], s54 offen offset:0 sc1
v_mfma_f32_16x16x32_bf16 v[64:67], v[192:195], a[40:43], v[64:67]
v_mfma_f32_16x16x32_bf16 v[96:99], v[192:195], a[44:47], v[96:99]
ds_read_b64_tr_b16 v[192:193], v240 offset:8448
ds_read_b64_tr_b16 v[194:195], v240 offset:9472
v_mfma_f32_16x16x32_bf16 v[4:7], v[196:199], a[32:35], v[4:7]
v_mfma_f32_16x16x32_bf16 v[36:39], v[196:199], a[36:39], v[36:39]
buffer_load_dwordx4 a[16:19], v246, s[44:47], s54 offen offset:0 sc1
v_mfma_f32_16x16x32_bf16 v[68:71], v[196:199], a[40:43], v[68:71]
v_mfma_f32_16x16x32_bf16 v[100:103], v[196:199], a[44:47], v[100:103]
ds_read_b64_tr_b16 v[196:197], v241 offset:8448
ds_read_b64_tr_b16 v[198:199], v241 offset:9472
v_mfma_f32_16x16x32_bf16 v[8:11], v[200:203], a[32:35], v[8:11]
s_mov_b32 s86, 0x05040100
s_mov_b32 s87, 0x07060302
v_mfma_f32_16x16x32_bf16 v[40:43], v[200:203], a[36:39], v[40:43]
buffer_load_dwordx4 a[20:23], v247, s[44:47], s54 offen offset:0 sc1
v_mfma_f32_16x16x32_bf16 v[72:75], v[200:203], a[40:43], v[72:75]
s_nop 0
v_accvgpr_read_b32 v230, a128
v_mfma_f32_16x16x32_bf16 v[104:107], v[200:203], a[44:47], v[104:107]
ds_read_b64_tr_b16 v[200:201], v242 offset:8448
ds_read_b64_tr_b16 v[202:203], v242 offset:9472
v_mfma_f32_16x16x32_bf16 v[12:15], v[204:207], a[32:35], v[12:15]
v_accvgpr_read_b32 v231, a132
v_perm_b32 v232, v231, v230, s86
v_mfma_f32_16x16x32_bf16 v[44:47], v[204:207], a[36:39], v[44:47]
buffer_load_dwordx4 a[24:27], v244, s[44:47], s54 offen offset:1024 sc1
v_mfma_f32_16x16x32_bf16 v[76:79], v[204:207], a[40:43], v[76:79]
v_perm_b32 v233, v231, v230, s87
v_accvgpr_write_b32 a96, v232
v_mfma_f32_16x16x32_bf16 v[108:111], v[204:207], a[44:47], v[108:111]
ds_read_b64_tr_b16 v[204:205], v243 offset:8448
ds_read_b64_tr_b16 v[206:207], v243 offset:9472
v_mfma_f32_16x16x32_bf16 v[16:19], v[208:211], a[32:35], v[16:19]
v_accvgpr_write_b32 a100, v233
v_accvgpr_read_b32 v234, a136
v_mfma_f32_16x16x32_bf16 v[48:51], v[208:211], a[36:39], v[48:51]
buffer_load_dwordx4 a[28:31], v245, s[44:47], s54 offen offset:1024 sc1
v_mfma_f32_16x16x32_bf16 v[80:83], v[208:211], a[40:43], v[80:83]
v_accvgpr_read_b32 v235, a140
v_perm_b32 v236, v235, v234, s86
v_mfma_f32_16x16x32_bf16 v[112:115], v[208:211], a[44:47], v[112:115]
ds_read_b64_tr_b16 v[208:209], v240 offset:8576
ds_read_b64_tr_b16 v[210:211], v240 offset:9600
v_mfma_f32_16x16x32_bf16 v[20:23], v[212:215], a[32:35], v[20:23]
v_add_u32 v240, 67584, v240
v_perm_b32 v237, v235, v234, s87
v_mfma_f32_16x16x32_bf16 v[52:55], v[212:215], a[36:39], v[52:55]
buffer_load_dwordx4 a[56:59], v246, s[44:47], s54 offen offset:1024 sc1
v_mfma_f32_16x16x32_bf16 v[84:87], v[212:215], a[40:43], v[84:87]
s_waitcnt vmcnt(12) lgkmcnt(2)
v_accvgpr_write_b32 a97, v236
v_mfma_f32_16x16x32_bf16 v[116:119], v[212:215], a[44:47], v[116:119]
ds_read_b64_tr_b16 v[212:213], v241 offset:8576
ds_read_b64_tr_b16 v[214:215], v241 offset:9600
v_mfma_f32_16x16x32_bf16 v[24:27], v[216:219], a[32:35], v[24:27]
v_add_u32 v241, 67584, v241
v_accvgpr_write_b32 a101, v237
v_mfma_f32_16x16x32_bf16 v[56:59], v[216:219], a[36:39], v[56:59]
buffer_load_dwordx4 a[60:63], v247, s[44:47], s54 offen offset:1024 sc1
v_mfma_f32_16x16x32_bf16 v[88:91], v[216:219], a[40:43], v[88:91]
v_accvgpr_read_b32 v230, a144
v_accvgpr_read_b32 v231, a148
v_mfma_f32_16x16x32_bf16 v[120:123], v[216:219], a[44:47], v[120:123]
ds_read_b64_tr_b16 v[216:217], v242 offset:8576
ds_read_b64_tr_b16 v[218:219], v242 offset:9600
v_mfma_f32_16x16x32_bf16 v[28:31], v[220:223], a[32:35], v[28:31]
v_add_u32 v242, 67584, v242
v_perm_b32 v232, v231, v230, s86
v_mfma_f32_16x16x32_bf16 v[60:63], v[220:223], a[36:39], v[60:63]
v_perm_b32 v233, v231, v230, s87
v_accvgpr_write_b32 a98, v232
v_mfma_f32_16x16x32_bf16 v[92:95], v[220:223], a[40:43], v[92:95]
v_accvgpr_write_b32 a102, v233
v_accvgpr_read_b32 v234, a152
v_mfma_f32_16x16x32_bf16 v[124:127], v[220:223], a[44:47], v[124:127]
ds_read_b64_tr_b16 v[220:221], v243 offset:8576
ds_read_b64_tr_b16 v[222:223], v243 offset:9600
v_mfma_f32_16x16x32_bf16 v[0:3], v[192:195], a[48:51], v[0:3]
s_add_u32 m0, s83, 102400
v_add_u32 v243, 67584, v243
v_mfma_f32_16x16x32_bf16 v[32:35], v[192:195], a[52:55], v[32:35]
buffer_load_dwordx4 v225, s[56:59], s80 offen lds
v_mfma_f32_16x16x32_bf16 v[64:67], v[192:195], a[0:3], v[64:67]
v_accvgpr_read_b32 v235, a156
v_perm_b32 v236, v235, v234, s86
v_mfma_f32_16x16x32_bf16 v[96:99], v[192:195], a[4:7], v[96:99]
v_perm_b32 v237, v235, v234, s87
v_accvgpr_write_b32 a99, v236
v_mfma_f32_16x16x32_bf16 v[4:7], v[196:199], a[48:51], v[4:7]
s_add_u32 m0, s83, 103552
s_waitcnt lgkmcnt(0)
s_barrier
v_mfma_f32_16x16x32_bf16 v[36:39], v[196:199], a[52:55], v[36:39]
buffer_load_dwordx4 v226, s[56:59], s80 offen lds
v_mfma_f32_16x16x32_bf16 v[68:71], v[196:199], a[0:3], v[68:71]
ds_read_b64_tr_b16 v[192:193], v240 offset:50688
ds_read_b64_tr_b16 v[194:195], v240 offset:51712
v_mfma_f32_16x16x32_bf16 v[100:103], v[196:199], a[4:7], v[100:103]
ds_read_b64_tr_b16 v[196:197], v241 offset:50688
ds_read_b64_tr_b16 v[198:199], v241 offset:51712
v_mfma_f32_16x16x32_bf16 v[8:11], v[200:203], a[48:51], v[8:11]
v_accvgpr_write_b32 a103, v237
v_accvgpr_read_b32 v230, a129
v_mfma_f32_16x16x32_bf16 v[40:43], v[200:203], a[52:55], v[40:43]
buffer_load_dwordx4 a[32:35], v244, s[44:47], s54 offen offset:2048 sc1
v_mfma_f32_16x16x32_bf16 v[72:75], v[200:203], a[0:3], v[72:75]
v_accvgpr_read_b32 v231, a133
v_perm_b32 v232, v231, v230, s86
v_mfma_f32_16x16x32_bf16 v[104:107], v[200:203], a[4:7], v[104:107]
ds_read_b64_tr_b16 v[200:201], v242 offset:50688
ds_read_b64_tr_b16 v[202:203], v242 offset:51712
v_mfma_f32_16x16x32_bf16 v[12:15], v[204:207], a[48:51], v[12:15]
v_perm_b32 v233, v231, v230, s87
v_accvgpr_write_b32 a104, v232
v_mfma_f32_16x16x32_bf16 v[44:47], v[204:207], a[52:55], v[44:47]
buffer_load_dwordx4 a[36:39], v245, s[44:47], s54 offen offset:2048 sc1
v_mfma_f32_16x16x32_bf16 v[76:79], v[204:207], a[0:3], v[76:79]
s_waitcnt vmcnt(8) lgkmcnt(2)
v_accvgpr_write_b32 a108, v233
v_mfma_f32_16x16x32_bf16 v[108:111], v[204:207], a[4:7], v[108:111]
ds_read_b64_tr_b16 v[204:205], v243 offset:50688
ds_read_b64_tr_b16 v[206:207], v243 offset:51712
v_mfma_f32_16x16x32_bf16 v[16:19], v[208:211], a[48:51], v[16:19]
v_accvgpr_read_b32 v234, a137
v_accvgpr_read_b32 v235, a141
v_mfma_f32_16x16x32_bf16 v[48:51], v[208:211], a[52:55], v[48:51]
buffer_load_dwordx4 a[40:43], v246, s[44:47], s54 offen offset:2048 sc1
v_mfma_f32_16x16x32_bf16 v[80:83], v[208:211], a[0:3], v[80:83]
v_perm_b32 v236, v235, v234, s86
v_perm_b32 v237, v235, v234, s87
v_mfma_f32_16x16x32_bf16 v[112:115], v[208:211], a[4:7], v[112:115]
ds_read_b64_tr_b16 v[208:209], v240 offset:50816
ds_read_b64_tr_b16 v[210:211], v240 offset:51840
v_mfma_f32_16x16x32_bf16 v[20:23], v[212:215], a[48:51], v[20:23]
v_accvgpr_write_b32 a105, v236
v_accvgpr_write_b32 a109, v237
v_mfma_f32_16x16x32_bf16 v[52:55], v[212:215], a[52:55], v[52:55]
buffer_load_dwordx4 a[44:47], v247, s[44:47], s54 offen offset:2048 sc1
v_mfma_f32_16x16x32_bf16 v[84:87], v[212:215], a[0:3], v[84:87]
v_accvgpr_read_b32 v230, a145
v_accvgpr_read_b32 v231, a149
v_mfma_f32_16x16x32_bf16 v[116:119], v[212:215], a[4:7], v[116:119]
ds_read_b64_tr_b16 v[212:213], v241 offset:50816
ds_read_b64_tr_b16 v[214:215], v241 offset:51840
v_mfma_f32_16x16x32_bf16 v[24:27], v[216:219], a[48:51], v[24:27]
v_perm_b32 v232, v231, v230, s86
v_perm_b32 v233, v231, v230, s87
v_mfma_f32_16x16x32_bf16 v[56:59], v[216:219], a[52:55], v[56:59]
buffer_load_dwordx2 v[250:251], v248, s[64:67], s5 offen sc1
v_mfma_f32_16x16x32_bf16 v[88:91], v[216:219], a[0:3], v[88:91]
v_accvgpr_write_b32 a106, v232
v_accvgpr_write_b32 a110, v233
v_mfma_f32_16x16x32_bf16 v[120:123], v[216:219], a[4:7], v[120:123]
ds_read_b64_tr_b16 v[216:217], v242 offset:50816
ds_read_b64_tr_b16 v[218:219], v242 offset:51840
v_mfma_f32_16x16x32_bf16 v[28:31], v[220:223], a[48:51], v[28:31]
s_add_u32 m0, s83, 104576
v_accvgpr_read_b32 v234, a153
v_mfma_f32_16x16x32_bf16 v[60:63], v[220:223], a[52:55], v[60:63]
buffer_load_dwordx4 v227, s[56:59], s80 offen lds
v_mfma_f32_16x16x32_bf16 v[92:95], v[220:223], a[0:3], v[92:95]
s_bfe_u32 s80, s4, 262160
s_xor_b32 s80, s80, s72
v_mfma_f32_16x16x32_bf16 v[124:127], v[220:223], a[4:7], v[124:127]
ds_read_b64_tr_b16 v[220:221], v243 offset:50816
ds_read_b64_tr_b16 v[222:223], v243 offset:51840
v_mfma_f32_16x16x32_bf16 v[0:3], v[192:195], a[8:11], v[0:3]
s_lshl_b32 s80, s80, 18
v_accvgpr_read_b32 v235, a157
v_mfma_f32_16x16x32_bf16 v[32:35], v[192:195], a[12:15], v[32:35]
buffer_load_dwordx4 a[0:3], v244, s[44:47], s54 offen offset:3072 sc1
v_mfma_f32_16x16x32_bf16 v[64:67], v[192:195], a[16:19], v[64:67]
v_perm_b32 v236, v235, v234, s86
v_perm_b32 v237, v235, v234, s87
v_mfma_f32_16x16x32_bf16 v[96:99], v[192:195], a[20:23], v[96:99]
ds_read_b64_tr_b16 v[192:193], v240 offset:59136
ds_read_b64_tr_b16 v[194:195], v240 offset:60160
v_mfma_f32_16x16x32_bf16 v[4:7], v[196:199], a[8:11], v[4:7]
v_accvgpr_write_b32 a107, v236
v_accvgpr_write_b32 a111, v237
v_mfma_f32_16x16x32_bf16 v[36:39], v[196:199], a[12:15], v[36:39]
buffer_load_dwordx4 a[4:7], v245, s[44:47], s54 offen offset:3072 sc1
v_mfma_f32_16x16x32_bf16 v[68:71], v[196:199], a[16:19], v[68:71]
s_waitcnt lgkmcnt(2)
v_accvgpr_read_b32 v230, a130
v_mfma_f32_16x16x32_bf16 v[100:103], v[196:199], a[20:23], v[100:103]
ds_read_b64_tr_b16 v[196:197], v241 offset:59136
ds_read_b64_tr_b16 v[198:199], v241 offset:60160
v_mfma_f32_16x16x32_bf16 v[8:11], v[200:203], a[8:11], v[8:11]
v_accvgpr_read_b32 v231, a134
v_perm_b32 v232, v231, v230, s86
v_mfma_f32_16x16x32_bf16 v[40:43], v[200:203], a[12:15], v[40:43]
buffer_load_dwordx4 a[48:51], v246, s[44:47], s54 offen offset:3072 sc1
v_mfma_f32_16x16x32_bf16 v[72:75], v[200:203], a[16:19], v[72:75]
v_perm_b32 v233, v231, v230, s87
v_accvgpr_write_b32 a112, v232
v_mfma_f32_16x16x32_bf16 v[104:107], v[200:203], a[20:23], v[104:107]
ds_read_b64_tr_b16 v[200:201], v242 offset:59136
ds_read_b64_tr_b16 v[202:203], v242 offset:60160
v_mfma_f32_16x16x32_bf16 v[12:15], v[204:207], a[8:11], v[12:15]
s_bitcmp1_b32 vcc_lo, 3
s_cbranch_scc0 .La5_LO_N3
.La5_LO_K3:
v_mfma_f32_16x16x32_bf16 v[44:47], v[204:207], a[12:15], v[44:47]
buffer_load_dwordx4 a[52:55], v247, s[44:47], s54 offen offset:3072 sc1
v_mfma_f32_16x16x32_bf16 v[76:79], v[204:207], a[16:19], v[76:79]
s_bfe_u32 s54, s4, 262168
s_xor_b32 s54, s54, s72
v_mfma_f32_16x16x32_bf16 v[108:111], v[204:207], a[20:23], v[108:111]
ds_read_b64_tr_b16 v[204:205], v243 offset:59136
ds_read_b64_tr_b16 v[206:207], v243 offset:60160
v_mfma_f32_16x16x32_bf16 v[16:19], v[208:211], a[8:11], v[16:19]
s_lshl_b32 s54, s54, 13
v_accvgpr_write_b32 a116, v233
v_mfma_f32_16x16x32_bf16 v[48:51], v[208:211], a[12:15], v[48:51]
buffer_load_dwordx4 a[64:67], v244, s[44:47], s55 offen offset:0 sc1
v_mfma_f32_16x16x32_bf16 v[80:83], v[208:211], a[16:19], v[80:83]
v_accvgpr_read_b32 v234, a138
v_accvgpr_read_b32 v235, a142
v_mfma_f32_16x16x32_bf16 v[112:115], v[208:211], a[20:23], v[112:115]
ds_read_b64_tr_b16 v[208:209], v240 offset:59264
ds_read_b64_tr_b16 v[210:211], v240 offset:60288
v_mfma_f32_16x16x32_bf16 v[20:23], v[212:215], a[8:11], v[20:23]
v_perm_b32 v236, v235, v234, s86
v_perm_b32 v237, v235, v234, s87
v_mfma_f32_16x16x32_bf16 v[52:55], v[212:215], a[12:15], v[52:55]
buffer_load_dwordx4 a[68:71], v245, s[44:47], s55 offen offset:0 sc1
v_mfma_f32_16x16x32_bf16 v[84:87], v[212:215], a[16:19], v[84:87]
v_accvgpr_write_b32 a113, v236
v_accvgpr_write_b32 a117, v237
v_mfma_f32_16x16x32_bf16 v[116:119], v[212:215], a[20:23], v[116:119]
ds_read_b64_tr_b16 v[212:213], v241 offset:59264
ds_read_b64_tr_b16 v[214:215], v241 offset:60288
v_mfma_f32_16x16x32_bf16 v[24:27], v[216:219], a[8:11], v[24:27]
v_accvgpr_read_b32 v230, a146
v_accvgpr_read_b32 v231, a150
v_mfma_f32_16x16x32_bf16 v[56:59], v[216:219], a[12:15], v[56:59]
buffer_load_dwordx4 a[72:75], v246, s[44:47], s55 offen offset:0 sc1
v_mfma_f32_16x16x32_bf16 v[88:91], v[216:219], a[16:19], v[88:91]
v_perm_b32 v232, v231, v230, s86
v_perm_b32 v233, v231, v230, s87
v_mfma_f32_16x16x32_bf16 v[120:123], v[216:219], a[20:23], v[120:123]
ds_read_b64_tr_b16 v[216:217], v242 offset:59264
ds_read_b64_tr_b16 v[218:219], v242 offset:60288
v_mfma_f32_16x16x32_bf16 v[28:31], v[220:223], a[8:11], v[28:31]
v_accvgpr_write_b32 a114, v232
v_accvgpr_write_b32 a118, v233
v_mfma_f32_16x16x32_bf16 v[60:63], v[220:223], a[12:15], v[60:63]
v_accvgpr_read_b32 v234, a154
v_accvgpr_read_b32 v235, a158
v_mfma_f32_16x16x32_bf16 v[92:95], v[220:223], a[16:19], v[92:95]
s_waitcnt vmcnt(15) lgkmcnt(2)
v_perm_b32 v236, v235, v234, s86
v_mfma_f32_16x16x32_bf16 v[124:127], v[220:223], a[20:23], v[124:127]
ds_read_b64_tr_b16 v[220:221], v243 offset:59264
ds_read_b64_tr_b16 v[222:223], v243 offset:60288
v_mfma_f32_16x16x32_bf16 v[0:3], v[192:195], a[24:27], v[0:3]
v_perm_b32 v237, v235, v234, s87
v_accvgpr_write_b32 a115, v236
v_mfma_f32_16x16x32_bf16 v[32:35], v[192:195], a[28:31], v[32:35]
buffer_load_dwordx4 a[8:11], v247, s[44:47], s55 offen offset:0 sc1
v_mfma_f32_16x16x32_bf16 v[64:67], v[192:195], a[56:59], v[64:67]
v_accvgpr_write_b32 a119, v237
v_accvgpr_read_b32 v230, a131
v_mfma_f32_16x16x32_bf16 v[96:99], v[192:195], a[60:63], v[96:99]
ds_read_b64_tr_b16 v[192:193], v240 offset:0
ds_read_b64_tr_b16 v[194:195], v240 offset:1024
v_mfma_f32_16x16x32_bf16 v[4:7], v[196:199], a[24:27], v[4:7]
v_accvgpr_read_b32 v231, a135
v_perm_b32 v232, v231, v230, s86
v_mfma_f32_16x16x32_bf16 v[36:39], v[196:199], a[28:31], v[36:39]
buffer_load_dwordx4 a[12:15], v244, s[44:47], s55 offen offset:1024 sc1
v_mfma_f32_16x16x32_bf16 v[68:71], v[196:199], a[56:59], v[68:71]
v_perm_b32 v233, v231, v230, s87
v_accvgpr_write_b32 a120, v232
v_mfma_f32_16x16x32_bf16 v[100:103], v[196:199], a[60:63], v[100:103]
ds_read_b64_tr_b16 v[196:197], v241 offset:0
ds_read_b64_tr_b16 v[198:199], v241 offset:1024
v_mfma_f32_16x16x32_bf16 v[8:11], v[200:203], a[24:27], v[8:11]
v_accvgpr_write_b32 a124, v233
v_accvgpr_read_b32 v234, a139
v_mfma_f32_16x16x32_bf16 v[40:43], v[200:203], a[28:31], v[40:43]
buffer_load_dwordx4 a[16:19], v245, s[44:47], s55 offen offset:1024 sc1
v_mfma_f32_16x16x32_bf16 v[72:75], v[200:203], a[56:59], v[72:75]
v_accvgpr_read_b32 v235, a143
v_perm_b32 v236, v235, v234, s86
v_mfma_f32_16x16x32_bf16 v[104:107], v[200:203], a[60:63], v[104:107]
ds_read_b64_tr_b16 v[200:201], v242 offset:0
ds_read_b64_tr_b16 v[202:203], v242 offset:1024
v_mfma_f32_16x16x32_bf16 v[12:15], v[204:207], a[24:27], v[12:15]
s_load_dwordx2 s[16:17], s[0:1], 0
s_load_dwordx4 s[36:39], s[0:1], 80
v_mfma_f32_16x16x32_bf16 v[44:47], v[204:207], a[28:31], v[44:47]
buffer_load_dwordx4 a[20:23], v246, s[44:47], s55 offen offset:1024 sc1
v_mfma_f32_16x16x32_bf16 v[76:79], v[204:207], a[56:59], v[76:79]
v_perm_b32 v237, v235, v234, s87
v_accvgpr_write_b32 a121, v236
v_mfma_f32_16x16x32_bf16 v[108:111], v[204:207], a[60:63], v[108:111]
ds_read_b64_tr_b16 v[204:205], v243 offset:0
ds_read_b64_tr_b16 v[206:207], v243 offset:1024
v_mfma_f32_16x16x32_bf16 v[16:19], v[208:211], a[24:27], v[16:19]
v_accvgpr_write_b32 a125, v237
v_accvgpr_read_b32 v230, a147
v_mfma_f32_16x16x32_bf16 v[48:51], v[208:211], a[28:31], v[48:51]
buffer_load_dwordx4 a[76:79], v247, s[44:47], s55 offen offset:1024 sc1
v_mfma_f32_16x16x32_bf16 v[80:83], v[208:211], a[56:59], v[80:83]
v_accvgpr_read_b32 v231, a151
v_perm_b32 v232, v231, v230, s86
v_mfma_f32_16x16x32_bf16 v[112:115], v[208:211], a[60:63], v[112:115]
ds_read_b64_tr_b16 v[208:209], v240 offset:128
ds_read_b64_tr_b16 v[210:211], v240 offset:1152
v_mfma_f32_16x16x32_bf16 v[20:23], v[212:215], a[24:27], v[20:23]
s_add_u32 m0, s83, 16896
v_perm_b32 v233, v231, v230, s87
v_mfma_f32_16x16x32_bf16 v[52:55], v[212:215], a[28:31], v[52:55]
buffer_load_dwordx4 v224, s[56:59], s80 offen lds
v_mfma_f32_16x16x32_bf16 v[84:87], v[212:215], a[56:59], v[84:87]
s_waitcnt vmcnt(15) lgkmcnt(2)
v_accvgpr_write_b32 a122, v232
v_mfma_f32_16x16x32_bf16 v[116:119], v[212:215], a[60:63], v[116:119]
ds_read_b64_tr_b16 v[212:213], v241 offset:128
ds_read_b64_tr_b16 v[214:215], v241 offset:1152
v_mfma_f32_16x16x32_bf16 v[24:27], v[216:219], a[24:27], v[24:27]
s_add_u32 m0, s83, 17920
v_accvgpr_write_b32 a126, v233
v_mfma_f32_16x16x32_bf16 v[56:59], v[216:219], a[28:31], v[56:59]
buffer_load_dwordx4 v225, s[56:59], s80 offen lds
v_mfma_f32_16x16x32_bf16 v[88:91], v[216:219], a[56:59], v[88:91]
v_accvgpr_read_b32 v234, a155
v_accvgpr_read_b32 v235, a159
v_mfma_f32_16x16x32_bf16 v[120:123], v[216:219], a[60:63], v[120:123]
ds_read_b64_tr_b16 v[216:217], v242 offset:128
ds_read_b64_tr_b16 v[218:219], v242 offset:1152
v_mfma_f32_16x16x32_bf16 v[28:31], v[220:223], a[24:27], v[28:31]
v_perm_b32 v236, v235, v234, s86
v_perm_b32 v237, v235, v234, s87
v_mfma_f32_16x16x32_bf16 v[60:63], v[220:223], a[28:31], v[60:63]
v_accvgpr_write_b32 a123, v236
v_accvgpr_write_b32 a127, v237
v_mfma_f32_16x16x32_bf16 v[92:95], v[220:223], a[56:59], v[92:95]
v_accvgpr_read_b32 v230, a160
v_accvgpr_read_b32 v231, a164
v_mfma_f32_16x16x32_bf16 v[124:127], v[220:223], a[60:63], v[124:127]
ds_read_b64_tr_b16 v[220:221], v243 offset:128
ds_read_b64_tr_b16 v[222:223], v243 offset:1152
v_mfma_f32_16x16x32_bf16 v[0:3], v[192:195], a[32:35], v[0:3]
s_add_u32 m0, s83, 19072
v_perm_b32 v232, v231, v230, s86
v_mfma_f32_16x16x32_bf16 v[32:35], v[192:195], a[36:39], v[32:35]
buffer_load_dwordx4 v226, s[56:59], s80 offen lds
v_mfma_f32_16x16x32_bf16 v[64:67], v[192:195], a[40:43], v[64:67]
v_perm_b32 v233, v231, v230, s87
v_accvgpr_write_b32 a128, v232
v_mfma_f32_16x16x32_bf16 v[96:99], v[192:195], a[44:47], v[96:99]
ds_read_b64_tr_b16 v[192:193], v240 offset:8448
ds_read_b64_tr_b16 v[194:195], v240 offset:9472
v_mfma_f32_16x16x32_bf16 v[4:7], v[196:199], a[32:35], v[4:7]
v_accvgpr_write_b32 a132, v233
v_accvgpr_read_b32 v234, a168
v_mfma_f32_16x16x32_bf16 v[36:39], v[196:199], a[36:39], v[36:39]
buffer_load_dwordx4 a[24:27], v244, s[44:47], s55 offen offset:2048 sc1
v_mfma_f32_16x16x32_bf16 v[68:71], v[196:199], a[40:43], v[68:71]
v_accvgpr_read_b32 v235, a172
v_perm_b32 v236, v235, v234, s86
v_mfma_f32_16x16x32_bf16 v[100:103], v[196:199], a[44:47], v[100:103]
ds_read_b64_tr_b16 v[196:197], v241 offset:8448
ds_read_b64_tr_b16 v[198:199], v241 offset:9472
v_mfma_f32_16x16x32_bf16 v[8:11], v[200:203], a[32:35], v[8:11]
v_perm_b32 v237, v235, v234, s87
v_accvgpr_write_b32 a129, v236
v_mfma_f32_16x16x32_bf16 v[40:43], v[200:203], a[36:39], v[40:43]
buffer_load_dwordx4 a[28:31], v245, s[44:47], s55 offen offset:2048 sc1
v_mfma_f32_16x16x32_bf16 v[72:75], v[200:203], a[40:43], v[72:75]
v_accvgpr_write_b32 a133, v237
v_accvgpr_read_b32 v230, a176
v_mfma_f32_16x16x32_bf16 v[104:107], v[200:203], a[44:47], v[104:107]
ds_read_b64_tr_b16 v[200:201], v242 offset:8448
ds_read_b64_tr_b16 v[202:203], v242 offset:9472
v_mfma_f32_16x16x32_bf16 v[12:15], v[204:207], a[32:35], v[12:15]
v_accvgpr_read_b32 v231, a180
v_perm_b32 v232, v231, v230, s86
v_mfma_f32_16x16x32_bf16 v[44:47], v[204:207], a[36:39], v[44:47]
buffer_load_dwordx4 a[56:59], v246, s[44:47], s55 offen offset:2048 sc1
v_mfma_f32_16x16x32_bf16 v[76:79], v[204:207], a[40:43], v[76:79]
s_waitcnt vmcnt(14) lgkmcnt(2)
v_perm_b32 v233, v231, v230, s87
v_mfma_f32_16x16x32_bf16 v[108:111], v[204:207], a[44:47], v[108:111]
ds_read_b64_tr_b16 v[204:205], v243 offset:8448
ds_read_b64_tr_b16 v[206:207], v243 offset:9472
v_mfma_f32_16x16x32_bf16 v[16:19], v[208:211], a[32:35], v[16:19]
v_accvgpr_write_b32 a130, v232
v_accvgpr_write_b32 a134, v233
v_mfma_f32_16x16x32_bf16 v[48:51], v[208:211], a[36:39], v[48:51]
buffer_load_dwordx4 a[60:63], v247, s[44:47], s55 offen offset:2048 sc1
v_mfma_f32_16x16x32_bf16 v[80:83], v[208:211], a[40:43], v[80:83]
v_accvgpr_read_b32 v234, a184
v_accvgpr_read_b32 v235, a188
v_mfma_f32_16x16x32_bf16 v[112:115], v[208:211], a[44:47], v[112:115]
ds_read_b64_tr_b16 v[208:209], v240 offset:8576
ds_read_b64_tr_b16 v[210:211], v240 offset:9600
v_mfma_f32_16x16x32_bf16 v[20:23], v[212:215], a[32:35], v[20:23]
s_add_u32 m0, s83, 20096
v_perm_b32 v236, v235, v234, s86
v_mfma_f32_16x16x32_bf16 v[52:55], v[212:215], a[36:39], v[52:55]
buffer_load_dwordx4 v227, s[56:59], s80 offen lds
v_mfma_f32_16x16x32_bf16 v[84:87], v[212:215], a[40:43], v[84:87]
s_add_u32 s80, s80, 131072
v_perm_b32 v237, v235, v234, s87
v_mfma_f32_16x16x32_bf16 v[116:119], v[212:215], a[44:47], v[116:119]
ds_read_b64_tr_b16 v[212:213], v241 offset:8576
ds_read_b64_tr_b16 v[214:215], v241 offset:9600
v_mfma_f32_16x16x32_bf16 v[24:27], v[216:219], a[32:35], v[24:27]
s_add_u32 m0, s83, 33792
v_accvgpr_write_b32 a131, v236
v_mfma_f32_16x16x32_bf16 v[56:59], v[216:219], a[36:39], v[56:59]
buffer_load_dwordx4 v224, s[56:59], s80 offen lds
v_mfma_f32_16x16x32_bf16 v[88:91], v[216:219], a[40:43], v[88:91]
v_accvgpr_write_b32 a135, v237
v_accvgpr_read_b32 v230, a161
v_mfma_f32_16x16x32_bf16 v[120:123], v[216:219], a[44:47], v[120:123]
ds_read_b64_tr_b16 v[216:217], v242 offset:8576
ds_read_b64_tr_b16 v[218:219], v242 offset:9600
v_mfma_f32_16x16x32_bf16 v[28:31], v[220:223], a[32:35], v[28:31]
v_accvgpr_read_b32 v231, a165
v_perm_b32 v232, v231, v230, s86
v_mfma_f32_16x16x32_bf16 v[60:63], v[220:223], a[36:39], v[60:63]
s_nop 0
v_cmp_eq_u64 vcc, v[250:251], v[252:253]
v_mfma_f32_16x16x32_bf16 v[92:95], v[220:223], a[40:43], v[92:95]
v_perm_b32 v233, v231, v230, s87
v_accvgpr_write_b32 a136, v232
v_mfma_f32_16x16x32_bf16 v[124:127], v[220:223], a[44:47], v[124:127]
ds_read_b64_tr_b16 v[220:221], v243 offset:8576
ds_read_b64_tr_b16 v[222:223], v243 offset:9600
v_mfma_f32_16x16x32_bf16 v[0:3], v[192:195], a[0:3], v[0:3]
s_add_u32 m0, s83, 34816
v_accvgpr_write_b32 a140, v233
v_mfma_f32_16x16x32_bf16 v[32:35], v[192:195], a[4:7], v[32:35]
buffer_load_dwordx4 v225, s[56:59], s80 offen lds
v_mfma_f32_16x16x32_bf16 v[64:67], v[192:195], a[48:51], v[64:67]
v_accvgpr_read_b32 v234, a169
v_accvgpr_read_b32 v235, a173
v_mfma_f32_16x16x32_bf16 v[96:99], v[192:195], a[52:55], v[96:99]
v_perm_b32 v236, v235, v234, s86
v_perm_b32 v237, v235, v234, s87
v_mfma_f32_16x16x32_bf16 v[4:7], v[196:199], a[0:3], v[4:7]
s_add_u32 m0, s83, 35968
s_waitcnt lgkmcnt(0)
s_barrier
v_mfma_f32_16x16x32_bf16 v[36:39], v[196:199], a[4:7], v[36:39]
buffer_load_dwordx4 v226, s[56:59], s80 offen lds
v_mfma_f32_16x16x32_bf16 v[68:71], v[196:199], a[48:51], v[68:71]
ds_read_b64_tr_b16 v[192:193], v240 offset:16896
ds_read_b64_tr_b16 v[194:195], v240 offset:17920
s_nop 0
v_mfma_f32_16x16x32_bf16 v[100:103], v[196:199], a[52:55], v[100:103]
ds_read_b64_tr_b16 v[196:197], v241 offset:16896
ds_read_b64_tr_b16 v[198:199], v241 offset:17920
v_mfma_f32_16x16x32_bf16 v[8:11], v[200:203], a[0:3], v[8:11]
s_lshl_b32 s92, s76, 5
s_add_u32 s92, s92, s72
v_mfma_f32_16x16x32_bf16 v[40:43], v[200:203], a[4:7], v[40:43]
buffer_load_dwordx2 v[250:251], v248, s[64:67], s5 offen sc1
v_mfma_f32_16x16x32_bf16 v[72:75], v[200:203], a[48:51], v[72:75]
s_lshl2_add_u32 s92, s92, s3
s_lshl_b32 s92, s92, 12
v_mfma_f32_16x16x32_bf16 v[104:107], v[200:203], a[52:55], v[104:107]
ds_read_b64_tr_b16 v[200:201], v242 offset:16896
ds_read_b64_tr_b16 v[202:203], v242 offset:17920
v_mfma_f32_16x16x32_bf16 v[12:15], v[204:207], a[0:3], v[12:15]
s_add_u32 m0, s83, 36992
v_accvgpr_write_b32 a137, v236
v_mfma_f32_16x16x32_bf16 v[44:47], v[204:207], a[4:7], v[44:47]
buffer_load_dwordx4 v227, s[56:59], s80 offen lds
v_mfma_f32_16x16x32_bf16 v[76:79], v[204:207], a[48:51], v[76:79]
s_bfe_u32 s80, s4, 262164
s_xor_b32 s80, s80, s72
v_mfma_f32_16x16x32_bf16 v[108:111], v[204:207], a[52:55], v[108:111]
ds_read_b64_tr_b16 v[204:205], v243 offset:16896
ds_read_b64_tr_b16 v[206:207], v243 offset:17920
v_mfma_f32_16x16x32_bf16 v[16:19], v[208:211], a[0:3], v[16:19]
s_lshl_b32 s80, s80, 18
v_accvgpr_write_b32 a141, v237
v_mfma_f32_16x16x32_bf16 v[48:51], v[208:211], a[4:7], v[48:51]
buffer_load_dwordx4 a[32:35], v244, s[44:47], s55 offen offset:3072 sc1
v_mfma_f32_16x16x32_bf16 v[80:83], v[208:211], a[48:51], v[80:83]
s_add_u32 s68, s16, s92
s_addc_u32 s69, s17, 0
v_mfma_f32_16x16x32_bf16 v[112:115], v[208:211], a[52:55], v[112:115]
ds_read_b64_tr_b16 v[208:209], v240 offset:17024
ds_read_b64_tr_b16 v[210:211], v240 offset:18048
v_mfma_f32_16x16x32_bf16 v[20:23], v[212:215], a[0:3], v[20:23]
s_and_b32 s69, s69, 0xffff
s_mov_b32 s70, 4096
v_mfma_f32_16x16x32_bf16 v[52:55], v[212:215], a[4:7], v[52:55]
buffer_load_dwordx4 a[36:39], v245, s[44:47], s55 offen offset:3072 sc1
v_mfma_f32_16x16x32_bf16 v[84:87], v[212:215], a[48:51], v[84:87]
s_mov_b32 s71, 0x20000
v_accvgpr_read_b32 v230, a177
v_mfma_f32_16x16x32_bf16 v[116:119], v[212:215], a[52:55], v[116:119]
.La5_LW3_1:
s_cbranch_execz .La5_LW3_5
ds_read_b64_tr_b16 v[212:213], v241 offset:17024
ds_read_b64_tr_b16 v[214:215], v241 offset:18048
v_mfma_f32_16x16x32_bf16 v[24:27], v[216:219], a[0:3], v[24:27]
s_add_u32 s48, s36, 0
s_addc_u32 s49, s37, 0
v_mfma_f32_16x16x32_bf16 v[56:59], v[216:219], a[4:7], v[56:59]
buffer_load_dwordx4 a[40:43], v246, s[44:47], s55 offen offset:3072 sc1
v_mfma_f32_16x16x32_bf16 v[88:91], v[216:219], a[48:51], v[88:91]
s_and_b32 s49, s49, 0xffff
s_mov_b32 s50, 2048
v_mfma_f32_16x16x32_bf16 v[120:123], v[216:219], a[52:55], v[120:123]
ds_read_b64_tr_b16 v[216:217], v242 offset:17024
ds_read_b64_tr_b16 v[218:219], v242 offset:18048
v_mfma_f32_16x16x32_bf16 v[28:31], v[220:223], a[0:3], v[28:31]
s_bitcmp1_b32 vcc_lo, 4
s_cbranch_scc0 .La5_LO_N4
.La5_LO_K4:
v_mfma_f32_16x16x32_bf16 v[60:63], v[220:223], a[4:7], v[60:63]
buffer_load_dwordx4 a[0:3], v247, s[44:47], s55 offen offset:3072 sc1
v_mfma_f32_16x16x32_bf16 v[92:95], v[220:223], a[48:51], v[92:95]
s_waitcnt vmcnt(21) lgkmcnt(2)
s_bfe_u32 s55, s4, 262172
v_mfma_f32_16x16x32_bf16 v[124:127], v[220:223], a[52:55], v[124:127]
ds_read_b64_tr_b16 v[220:221], v243 offset:17024
ds_read_b64_tr_b16 v[222:223], v243 offset:18048
v_mfma_f32_16x16x32_bf16 v[0:3], v[192:195], a[64:67], v[0:3]
s_xor_b32 s55, s55, s72
s_lshl_b32 s55, s55, 13
v_mfma_f32_16x16x32_bf16 v[32:35], v[192:195], a[68:71], v[32:35]
buffer_load_dwordx4 a[4:7], v244, s[44:47], s52 offen offset:0 sc1
v_mfma_f32_16x16x32_bf16 v[64:67], v[192:195], a[72:75], v[64:67]
s_mov_b32 s51, 0x20000
v_and_b32 v238, 0xf0, v255
v_mfma_f32_16x16x32_bf16 v[96:99], v[192:195], a[8:11], v[96:99]
ds_read_b64_tr_b16 v[192:193], v240 offset:25344
ds_read_b64_tr_b16 v[194:195], v240 offset:26368
v_mfma_f32_16x16x32_bf16 v[4:7], v[196:199], a[64:67], v[4:7]
v_lshlrev_b32 v238, 2, v238
v_bfe_u32 v249, v255, 8, 1
v_mfma_f32_16x16x32_bf16 v[36:39], v[196:199], a[68:71], v[36:39]
buffer_load_dwordx4 a[44:47], v245, s[44:47], s52 offen offset:0 sc1
v_mfma_f32_16x16x32_bf16 v[68:71], v[196:199], a[72:75], v[68:71]
v_lshl_add_u32 v238, v249, 5, v238
v_lshrrev_b32_e64 v249, 9, v255
v_mfma_f32_16x16x32_bf16 v[100:103], v[196:199], a[8:11], v[100:103]
ds_read_b64_tr_b16 v[196:197], v241 offset:25344
ds_read_b64_tr_b16 v[198:199], v241 offset:26368
v_mfma_f32_16x16x32_bf16 v[8:11], v[200:203], a[64:67], v[8:11]
v_lshl_add_u32 v238, v249, 4, v238
s_lshr_b32 s23, s72, 3
v_mfma_f32_16x16x32_bf16 v[40:43], v[200:203], a[68:71], v[40:43]
buffer_load_dwordx4 a[48:51], v246, s[44:47], s52 offen offset:0 sc1
v_mfma_f32_16x16x32_bf16 v[72:75], v[200:203], a[72:75], v[72:75]
s_lshl3_add_u32 s92, s23, s76
s_lshl_b32 s92, s92, 19
v_mfma_f32_16x16x32_bf16 v[104:107], v[200:203], a[8:11], v[104:107]
ds_read_b64_tr_b16 v[200:201], v242 offset:25344
ds_read_b64_tr_b16 v[202:203], v242 offset:26368
v_mfma_f32_16x16x32_bf16 v[12:15], v[204:207], a[64:67], v[12:15]
s_lshl_b32 s97, s3, 16
s_add_u32 s94, s92, s97
v_mfma_f32_16x16x32_bf16 v[44:47], v[204:207], a[68:71], v[44:47]
buffer_load_dwordx4 a[52:55], v247, s[44:47], s52 offen offset:0 sc1
v_mfma_f32_16x16x32_bf16 v[76:79], v[204:207], a[72:75], v[76:79]
s_add_u32 s95, s94, 32768
s_sub_u32 s96, s92, s97
v_mfma_f32_16x16x32_bf16 v[108:111], v[204:207], a[8:11], v[108:111]
ds_read_b64_tr_b16 v[204:205], v243 offset:25344
ds_read_b64_tr_b16 v[206:207], v243 offset:26368
v_mfma_f32_16x16x32_bf16 v[16:19], v[208:211], a[64:67], v[16:19]
s_add_u32 s96, s96, 458752
s_add_u32 s97, s96, 32768
v_mfma_f32_16x16x32_bf16 v[48:51], v[208:211], a[68:71], v[48:51]
buffer_load_dwordx4 v[136:139], v244, s[44:47], s52 offen offset:1024 sc1
v_mfma_f32_16x16x32_bf16 v[80:83], v[208:211], a[72:75], v[80:83]
s_lshl3_add_u32 s92, s23, s101
s_lshl_b32 s85, s76, 14
v_mfma_f32_16x16x32_bf16 v[112:115], v[208:211], a[8:11], v[112:115]
ds_read_b64_tr_b16 v[208:209], v240 offset:25472
ds_read_b64_tr_b16 v[210:211], v240 offset:26496
v_mfma_f32_16x16x32_bf16 v[20:23], v[212:215], a[64:67], v[20:23]
s_lshl_b32 s92, s92, 3
s_add_u32 s92, s92, s85
v_mfma_f32_16x16x32_bf16 v[52:55], v[212:215], a[68:71], v[52:55]
buffer_load_dwordx4 v[140:143], v245, s[44:47], s52 offen offset:1024 sc1
v_mfma_f32_16x16x32_bf16 v[84:87], v[212:215], a[72:75], v[84:87]
s_waitcnt vmcnt(23) lgkmcnt(2)
s_add_u32 s92, s92, 2097152
v_mfma_f32_16x16x32_bf16 v[116:119], v[212:215], a[8:11], v[116:119]
ds_read_b64_tr_b16 v[212:213], v241 offset:25472
ds_read_b64_tr_b16 v[214:215], v241 offset:26496
v_mfma_f32_16x16x32_bf16 v[24:27], v[216:219], a[64:67], v[24:27]
v_accvgpr_read_b32 v231, a181
v_perm_b32 v232, v231, v230, s86
v_mfma_f32_16x16x32_bf16 v[56:59], v[216:219], a[68:71], v[56:59]
buffer_load_dwordx4 v[152:155], v246, s[44:47], s52 offen offset:1024 sc1
v_mfma_f32_16x16x32_bf16 v[88:91], v[216:219], a[72:75], v[88:91]
v_perm_b32 v233, v231, v230, s87
v_accvgpr_write_b32 a138, v232
v_mfma_f32_16x16x32_bf16 v[120:123], v[216:219], a[8:11], v[120:123]
ds_read_b64_tr_b16 v[216:217], v242 offset:25472
ds_read_b64_tr_b16 v[218:219], v242 offset:26496
v_mfma_f32_16x16x32_bf16 v[28:31], v[220:223], a[64:67], v[28:31]
v_accvgpr_write_b32 a142, v233
v_accvgpr_read_b32 v234, a185
v_mfma_f32_16x16x32_bf16 v[60:63], v[220:223], a[68:71], v[60:63]
v_accvgpr_read_b32 v235, a189
v_perm_b32 v236, v235, v234, s86
v_mfma_f32_16x16x32_bf16 v[92:95], v[220:223], a[72:75], v[92:95]
v_perm_b32 v237, v235, v234, s87
v_accvgpr_write_b32 a139, v236
v_mfma_f32_16x16x32_bf16 v[124:127], v[220:223], a[8:11], v[124:127]
ds_read_b64_tr_b16 v[220:221], v243 offset:25472
ds_read_b64_tr_b16 v[222:223], v243 offset:26496
v_mfma_f32_16x16x32_bf16 v[0:3], v[192:195], a[12:15], v[0:3]
v_accvgpr_write_b32 a143, v237
v_accvgpr_read_b32 v230, a162
v_mfma_f32_16x16x32_bf16 v[32:35], v[192:195], a[16:19], v[32:35]
buffer_load_dwordx4 v[156:159], v247, s[44:47], s52 offen offset:1024 sc1
v_mfma_f32_16x16x32_bf16 v[64:67], v[192:195], a[20:23], v[64:67]
v_accvgpr_read_b32 v231, a166
v_perm_b32 v232, v231, v230, s86
v_mfma_f32_16x16x32_bf16 v[96:99], v[192:195], a[76:79], v[96:99]
ds_read_b64_tr_b16 v[192:193], v240 offset:33792
ds_read_b64_tr_b16 v[194:195], v240 offset:34816
v_mfma_f32_16x16x32_bf16 v[4:7], v[196:199], a[12:15], v[4:7]
s_add_u32 m0, s83, 50688
v_perm_b32 v233, v231, v230, s87
v_mfma_f32_16x16x32_bf16 v[36:39], v[196:199], a[16:19], v[36:39]
buffer_load_dwordx4 v224, s[56:59], s80 offen lds
v_mfma_f32_16x16x32_bf16 v[68:71], v[196:199], a[20:23], v[68:71]
v_accvgpr_write_b32 a144, v232
v_accvgpr_write_b32 a148, v233
v_mfma_f32_16x16x32_bf16 v[100:103], v[196:199], a[76:79], v[100:103]
ds_read_b64_tr_b16 v[196:197], v241 offset:33792
ds_read_b64_tr_b16 v[198:199], v241 offset:34816
v_mfma_f32_16x16x32_bf16 v[8:11], v[200:203], a[12:15], v[8:11]
s_add_u32 m0, s83, 51712
v_accvgpr_read_b32 v234, a170
v_mfma_f32_16x16x32_bf16 v[40:43], v[200:203], a[16:19], v[40:43]
buffer_load_dwordx4 v225, s[56:59], s80 offen lds
v_mfma_f32_16x16x32_bf16 v[72:75], v[200:203], a[20:23], v[72:75]
v_accvgpr_read_b32 v235, a174
v_perm_b32 v236, v235, v234, s86
v_mfma_f32_16x16x32_bf16 v[104:107], v[200:203], a[76:79], v[104:107]
ds_read_b64_tr_b16 v[200:201], v242 offset:33792
ds_read_b64_tr_b16 v[202:203], v242 offset:34816
v_mfma_f32_16x16x32_bf16 v[12:15], v[204:207], a[12:15], v[12:15]
s_add_u32 m0, s83, 52864
v_perm_b32 v237, v235, v234, s87
v_mfma_f32_16x16x32_bf16 v[44:47], v[204:207], a[16:19], v[44:47]
buffer_load_dwordx4 v226, s[56:59], s80 offen lds
v_mfma_f32_16x16x32_bf16 v[76:79], v[204:207], a[20:23], v[76:79]
s_waitcnt vmcnt(21) lgkmcnt(2)
v_accvgpr_write_b32 a145, v236
v_mfma_f32_16x16x32_bf16 v[108:111], v[204:207], a[76:79], v[108:111]
ds_read_b64_tr_b16 v[204:205], v243 offset:33792
ds_read_b64_tr_b16 v[206:207], v243 offset:34816
v_mfma_f32_16x16x32_bf16 v[16:19], v[208:211], a[12:15], v[16:19]
v_accvgpr_write_b32 a149, v237
v_accvgpr_read_b32 v230, a178
v_mfma_f32_16x16x32_bf16 v[48:51], v[208:211], a[16:19], v[48:51]
buffer_load_dwordx4 v[168:171], v244, s[44:47], s52 offen offset:2048 sc1
v_mfma_f32_16x16x32_bf16 v[80:83], v[208:211], a[20:23], v[80:83]
v_accvgpr_read_b32 v231, a182
v_perm_b32 v232, v231, v230, s86
v_mfma_f32_16x16x32_bf16 v[112:115], v[208:211], a[76:79], v[112:115]
ds_read_b64_tr_b16 v[208:209], v240 offset:33920
ds_read_b64_tr_b16 v[210:211], v240 offset:34944
v_mfma_f32_16x16x32_bf16 v[20:23], v[212:215], a[12:15], v[20:23]
v_perm_b32 v233, v231, v230, s87
v_accvgpr_write_b32 a146, v232
v_mfma_f32_16x16x32_bf16 v[52:55], v[212:215], a[16:19], v[52:55]
buffer_load_dwordx4 v[172:175], v245, s[44:47], s52 offen offset:2048 sc1
v_mfma_f32_16x16x32_bf16 v[84:87], v[212:215], a[20:23], v[84:87]
v_accvgpr_write_b32 a150, v233
v_accvgpr_read_b32 v234, a186
v_mfma_f32_16x16x32_bf16 v[116:119], v[212:215], a[76:79], v[116:119]
ds_read_b64_tr_b16 v[212:213], v241 offset:33920
ds_read_b64_tr_b16 v[214:215], v241 offset:34944
v_mfma_f32_16x16x32_bf16 v[24:27], v[216:219], a[12:15], v[24:27]
v_accvgpr_read_b32 v235, a190
v_perm_b32 v236, v235, v234, s86
v_mfma_f32_16x16x32_bf16 v[56:59], v[216:219], a[16:19], v[56:59]
buffer_load_dwordx4 v[184:187], v246, s[44:47], s52 offen offset:2048 sc1
v_mfma_f32_16x16x32_bf16 v[88:91], v[216:219], a[20:23], v[88:91]
v_perm_b32 v237, v235, v234, s87
v_accvgpr_write_b32 a147, v236
v_mfma_f32_16x16x32_bf16 v[120:123], v[216:219], a[76:79], v[120:123]
ds_read_b64_tr_b16 v[216:217], v242 offset:33920
ds_read_b64_tr_b16 v[218:219], v242 offset:34944
v_mfma_f32_16x16x32_bf16 v[28:31], v[220:223], a[12:15], v[28:31]
v_accvgpr_write_b32 a151, v237
v_accvgpr_read_b32 v230, a163
v_mfma_f32_16x16x32_bf16 v[60:63], v[220:223], a[16:19], v[60:63]
v_accvgpr_read_b32 v231, a167
v_perm_b32 v232, v231, v230, s86
v_mfma_f32_16x16x32_bf16 v[92:95], v[220:223], a[20:23], v[92:95]
v_perm_b32 v233, v231, v230, s87
v_accvgpr_write_b32 a152, v232
v_mfma_f32_16x16x32_bf16 v[124:127], v[220:223], a[76:79], v[124:127]
ds_read_b64_tr_b16 v[220:221], v243 offset:33920
ds_read_b64_tr_b16 v[222:223], v243 offset:34944
v_mfma_f32_16x16x32_bf16 v[0:3], v[192:195], a[24:27], v[0:3]
v_accvgpr_write_b32 a156, v233
v_accvgpr_read_b32 v234, a171
v_mfma_f32_16x16x32_bf16 v[32:35], v[192:195], a[28:31], v[32:35]
buffer_load_dwordx4 v[188:191], v247, s[44:47], s52 offen offset:2048 sc1
v_mfma_f32_16x16x32_bf16 v[64:67], v[192:195], a[56:59], v[64:67]
v_accvgpr_read_b32 v235, a175
v_perm_b32 v236, v235, v234, s86
v_mfma_f32_16x16x32_bf16 v[96:99], v[192:195], a[60:63], v[96:99]
ds_read_b64_tr_b16 v[192:193], v240 offset:42240
ds_read_b64_tr_b16 v[194:195], v240 offset:43264
v_mfma_f32_16x16x32_bf16 v[4:7], v[196:199], a[24:27], v[4:7]
s_add_u32 m0, s83, 53888
v_perm_b32 v237, v235, v234, s87
v_mfma_f32_16x16x32_bf16 v[36:39], v[196:199], a[28:31], v[36:39]
buffer_load_dwordx4 v227, s[56:59], s80 offen lds
v_mfma_f32_16x16x32_bf16 v[68:71], v[196:199], a[56:59], v[68:71]
s_waitcnt lgkmcnt(2)
s_add_u32 s80, s80, 131072
v_mfma_f32_16x16x32_bf16 v[100:103], v[196:199], a[60:63], v[100:103]
ds_read_b64_tr_b16 v[196:197], v241 offset:42240
ds_read_b64_tr_b16 v[198:199], v241 offset:43264
v_mfma_f32_16x16x32_bf16 v[8:11], v[200:203], a[24:27], v[8:11]
s_add_u32 m0, s83, 0
v_accvgpr_write_b32 a153, v236
v_mfma_f32_16x16x32_bf16 v[40:43], v[200:203], a[28:31], v[40:43]
buffer_load_dwordx4 v224, s[56:59], s80 offen lds
v_mfma_f32_16x16x32_bf16 v[72:75], v[200:203], a[56:59], v[72:75]
v_accvgpr_write_b32 a157, v237
v_accvgpr_read_b32 v230, a179
v_mfma_f32_16x16x32_bf16 v[104:107], v[200:203], a[60:63], v[104:107]
ds_read_b64_tr_b16 v[200:201], v242 offset:42240
ds_read_b64_tr_b16 v[202:203], v242 offset:43264
v_mfma_f32_16x16x32_bf16 v[12:15], v[204:207], a[24:27], v[12:15]
s_add_u32 m0, s83, 1024
v_accvgpr_read_b32 v231, a183
v_mfma_f32_16x16x32_bf16 v[44:47], v[204:207], a[28:31], v[44:47]
buffer_load_dwordx4 v225, s[56:59], s80 offen lds
v_mfma_f32_16x16x32_bf16 v[76:79], v[204:207], a[56:59], v[76:79]
s_waitcnt vmcnt(23)
v_cmp_eq_u64 vcc, v[250:251], v[252:253]
v_mfma_f32_16x16x32_bf16 v[108:111], v[204:207], a[60:63], v[108:111]
ds_read_b64_tr_b16 v[204:205], v243 offset:42240
ds_read_b64_tr_b16 v[206:207], v243 offset:43264
v_mfma_f32_16x16x32_bf16 v[16:19], v[208:211], a[24:27], v[16:19]
s_add_u32 m0, s83, 2176
v_perm_b32 v232, v231, v230, s86
v_mfma_f32_16x16x32_bf16 v[48:51], v[208:211], a[28:31], v[48:51]
buffer_load_dwordx4 v226, s[56:59], s80 offen lds
v_mfma_f32_16x16x32_bf16 v[80:83], v[208:211], a[56:59], v[80:83]
v_perm_b32 v233, v231, v230, s87
v_accvgpr_write_b32 a154, v232
v_mfma_f32_16x16x32_bf16 v[112:115], v[208:211], a[60:63], v[112:115]
ds_read_b64_tr_b16 v[208:209], v240 offset:42368
ds_read_b64_tr_b16 v[210:211], v240 offset:43392
v_mfma_f32_16x16x32_bf16 v[20:23], v[212:215], a[24:27], v[20:23]
s_add_u32 m0, s83, 3200
v_add_u32 v240, 4294899712, v240
v_mfma_f32_16x16x32_bf16 v[52:55], v[212:215], a[28:31], v[52:55]
buffer_load_dwordx4 v227, s[56:59], s80 offen lds
v_mfma_f32_16x16x32_bf16 v[84:87], v[212:215], a[56:59], v[84:87]
s_bfe_u32 s80, s4, 262168
s_xor_b32 s80, s80, s72
v_mfma_f32_16x16x32_bf16 v[116:119], v[212:215], a[60:63], v[116:119]
ds_read_b64_tr_b16 v[212:213], v241 offset:42368
ds_read_b64_tr_b16 v[214:215], v241 offset:43392
v_mfma_f32_16x16x32_bf16 v[24:27], v[216:219], a[24:27], v[24:27]
v_add_u32 v241, 4294899712, v241
s_lshl_b32 s80, s80, 18
v_mfma_f32_16x16x32_bf16 v[56:59], v[216:219], a[28:31], v[56:59]
buffer_load_dwordx2 v[250:251], v248, s[64:67], s5 offen sc1
v_mfma_f32_16x16x32_bf16 v[88:91], v[216:219], a[56:59], v[88:91]
v_accvgpr_write_b32 a158, v233
v_accvgpr_read_b32 v234, a187
v_mfma_f32_16x16x32_bf16 v[120:123], v[216:219], a[60:63], v[120:123]
ds_read_b64_tr_b16 v[216:217], v242 offset:42368
ds_read_b64_tr_b16 v[218:219], v242 offset:43392
v_mfma_f32_16x16x32_bf16 v[28:31], v[220:223], a[24:27], v[28:31]
v_add_u32 v242, 4294899712, v242
v_accvgpr_read_b32 v235, a191
v_mfma_f32_16x16x32_bf16 v[60:63], v[220:223], a[28:31], v[60:63]
buffer_load_dwordx4 a[8:11], v244, s[44:47], s52 offen offset:3072 sc1
v_mfma_f32_16x16x32_bf16 v[92:95], v[220:223], a[56:59], v[92:95]
s_waitcnt vmcnt(22) lgkmcnt(2)
v_perm_b32 v236, v235, v234, s86
v_mfma_f32_16x16x32_bf16 v[124:127], v[220:223], a[60:63], v[124:127]
ds_read_b64_tr_b16 v[220:221], v243 offset:42368
ds_read_b64_tr_b16 v[222:223], v243 offset:43392
v_mfma_f32_16x16x32_bf16 v[0:3], v[192:195], a[32:35], v[0:3]
v_add_u32 v243, 4294899712, v243
v_perm_b32 v237, v235, v234, s87
v_mfma_f32_16x16x32_bf16 v[32:35], v[192:195], a[36:39], v[32:35]
buffer_load_dwordx4 a[12:15], v245, s[44:47], s52 offen offset:3072 sc1
v_mfma_f32_16x16x32_bf16 v[64:67], v[192:195], a[40:43], v[64:67]
v_accvgpr_write_b32 a155, v236
v_accvgpr_write_b32 a159, v237
v_mfma_f32_16x16x32_bf16 v[96:99], v[192:195], a[0:3], v[96:99]
v_accvgpr_read_b32 v230, a192
v_accvgpr_read_b32 v231, a196
v_mfma_f32_16x16x32_bf16 v[4:7], v[196:199], a[32:35], v[4:7]
s_waitcnt lgkmcnt(0)
s_barrier
v_mfma_f32_16x16x32_bf16 v[36:39], v[196:199], a[36:39], v[36:39]
buffer_load_dwordx4 a[16:19], v246, s[44:47], s52 offen offset:3072 sc1
v_mfma_f32_16x16x32_bf16 v[68:71], v[196:199], a[40:43], v[68:71]
ds_read_b64_tr_b16 v[192:193], v240 offset:16896
ds_read_b64_tr_b16 v[194:195], v240 offset:17920
v_mfma_f32_16x16x32_bf16 v[100:103], v[196:199], a[0:3], v[100:103]
ds_read_b64_tr_b16 v[196:197], v241 offset:16896
ds_read_b64_tr_b16 v[198:199], v241 offset:17920
v_mfma_f32_16x16x32_bf16 v[8:11], v[200:203], a[32:35], v[8:11]
s_bitcmp1_b32 vcc_lo, 5
s_cbranch_scc0 .La5_LO_N5
.La5_LO_K5:
v_mfma_f32_16x16x32_bf16 v[40:43], v[200:203], a[36:39], v[40:43]
buffer_load_dwordx4 a[20:23], v247, s[44:47], s52 offen offset:3072 sc1
v_mfma_f32_16x16x32_bf16 v[72:75], v[200:203], a[40:43], v[72:75]
v_perm_b32 v232, v231, v230, s86
v_perm_b32 v233, v231, v230, s87
v_mfma_f32_16x16x32_bf16 v[104:107], v[200:203], a[0:3], v[104:107]
ds_read_b64_tr_b16 v[200:201], v242 offset:16896
ds_read_b64_tr_b16 v[202:203], v242 offset:17920
v_mfma_f32_16x16x32_bf16 v[12:15], v[204:207], a[32:35], v[12:15]
v_accvgpr_write_b32 a160, v232
v_accvgpr_write_b32 a164, v233
v_mfma_f32_16x16x32_bf16 v[44:47], v[204:207], a[36:39], v[44:47]
buffer_load_dwordx4 a[24:27], v244, s[44:47], s53 offen offset:0 sc1
v_mfma_f32_16x16x32_bf16 v[76:79], v[204:207], a[40:43], v[76:79]
v_accvgpr_read_b32 v234, a200
v_accvgpr_read_b32 v235, a204
v_mfma_f32_16x16x32_bf16 v[108:111], v[204:207], a[0:3], v[108:111]
ds_read_b64_tr_b16 v[204:205], v243 offset:16896
ds_read_b64_tr_b16 v[206:207], v243 offset:17920
v_mfma_f32_16x16x32_bf16 v[16:19], v[208:211], a[32:35], v[16:19]
v_perm_b32 v236, v235, v234, s86
v_perm_b32 v237, v235, v234, s87
v_mfma_f32_16x16x32_bf16 v[48:51], v[208:211], a[36:39], v[48:51]
buffer_load_dwordx4 a[28:31], v245, s[44:47], s53 offen offset:0 sc1
v_mfma_f32_16x16x32_bf16 v[80:83], v[208:211], a[40:43], v[80:83]
v_accvgpr_write_b32 a161, v236
v_accvgpr_write_b32 a165, v237
v_mfma_f32_16x16x32_bf16 v[112:115], v[208:211], a[0:3], v[112:115]
ds_read_b64_tr_b16 v[208:209], v240 offset:17024
ds_read_b64_tr_b16 v[210:211], v240 offset:18048
v_mfma_f32_16x16x32_bf16 v[20:23], v[212:215], a[32:35], v[20:23]
v_accvgpr_read_b32 v230, a208
v_accvgpr_read_b32 v231, a212
v_mfma_f32_16x16x32_bf16 v[52:55], v[212:215], a[36:39], v[52:55]
buffer_load_dwordx4 a[56:59], v246, s[44:47], s53 offen offset:0 sc1
v_mfma_f32_16x16x32_bf16 v[84:87], v[212:215], a[40:43], v[84:87]
s_waitcnt vmcnt(24) lgkmcnt(2)
v_perm_b32 v232, v231, v230, s86
v_mfma_f32_16x16x32_bf16 v[116:119], v[212:215], a[0:3], v[116:119]
ds_read_b64_tr_b16 v[212:213], v241 offset:17024
ds_read_b64_tr_b16 v[214:215], v241 offset:18048
v_mfma_f32_16x16x32_bf16 v[24:27], v[216:219], a[32:35], v[24:27]
v_perm_b32 v233, v231, v230, s87
v_accvgpr_write_b32 a162, v232
v_mfma_f32_16x16x32_bf16 v[56:59], v[216:219], a[36:39], v[56:59]
buffer_load_dwordx4 a[60:63], v247, s[44:47], s53 offen offset:0 sc1
v_mfma_f32_16x16x32_bf16 v[88:91], v[216:219], a[40:43], v[88:91]
v_accvgpr_write_b32 a166, v233
v_accvgpr_read_b32 v234, a216
v_mfma_f32_16x16x32_bf16 v[120:123], v[216:219], a[0:3], v[120:123]
ds_read_b64_tr_b16 v[216:217], v242 offset:17024
ds_read_b64_tr_b16 v[218:219], v242 offset:18048
v_mfma_f32_16x16x32_bf16 v[28:31], v[220:223], a[32:35], v[28:31]
v_accvgpr_read_b32 v235, a220
v_perm_b32 v236, v235, v234, s86
v_mfma_f32_16x16x32_bf16 v[60:63], v[220:223], a[36:39], v[60:63]
v_perm_b32 v237, v235, v234, s87
v_accvgpr_write_b32 a163, v236
v_mfma_f32_16x16x32_bf16 v[92:95], v[220:223], a[40:43], v[92:95]
v_accvgpr_write_b32 a167, v237
v_accvgpr_read_b32 v230, a193
v_mfma_f32_16x16x32_bf16 v[124:127], v[220:223], a[0:3], v[124:127]
ds_read_b64_tr_b16 v[220:221], v243 offset:17024
ds_read_b64_tr_b16 v[222:223], v243 offset:18048
v_mfma_f32_16x16x32_bf16 v[0:3], v[192:195], a[4:7], v[0:3]
v_accvgpr_read_b32 v231, a197
v_perm_b32 v232, v231, v230, s86
v_mfma_f32_16x16x32_bf16 v[32:35], v[192:195], a[44:47], v[32:35]
buffer_load_dwordx4 a[0:3], v244, s[44:47], s53 offen offset:1024 sc1
v_mfma_f32_16x16x32_bf16 v[64:67], v[192:195], a[48:51], v[64:67]
v_perm_b32 v233, v231, v230, s87
v_accvgpr_write_b32 a168, v232
v_mfma_f32_16x16x32_bf16 v[96:99], v[192:195], a[52:55], v[96:99]
ds_read_b64_tr_b16 v[192:193], v240 offset:25344
ds_read_b64_tr_b16 v[194:195], v240 offset:26368
v_mfma_f32_16x16x32_bf16 v[4:7], v[196:199], a[4:7], v[4:7]
v_accvgpr_write_b32 a172, v233
v_accvgpr_read_b32 v234, a201
v_mfma_f32_16x16x32_bf16 v[36:39], v[196:199], a[44:47], v[36:39]
buffer_load_dwordx4 a[32:35], v245, s[44:47], s53 offen offset:1024 sc1
v_mfma_f32_16x16x32_bf16 v[68:71], v[196:199], a[48:51], v[68:71]
v_accvgpr_read_b32 v235, a205
v_perm_b32 v236, v235, v234, s86
v_mfma_f32_16x16x32_bf16 v[100:103], v[196:199], a[52:55], v[100:103]
ds_read_b64_tr_b16 v[196:197], v241 offset:25344
ds_read_b64_tr_b16 v[198:199], v241 offset:26368
v_mfma_f32_16x16x32_bf16 v[8:11], v[200:203], a[4:7], v[8:11]
v_perm_b32 v237, v235, v234, s87
v_accvgpr_write_b32 a169, v236
v_mfma_f32_16x16x32_bf16 v[40:43], v[200:203], a[44:47], v[40:43]
buffer_load_dwordx4 a[36:39], v246, s[44:47], s53 offen offset:1024 sc1
v_mfma_f32_16x16x32_bf16 v[72:75], v[200:203], a[48:51], v[72:75]
v_accvgpr_write_b32 a173, v237
v_accvgpr_read_b32 v230, a209
v_mfma_f32_16x16x32_bf16 v[104:107], v[200:203], a[52:55], v[104:107]
ds_read_b64_tr_b16 v[200:201], v242 offset:25344
ds_read_b64_tr_b16 v[202:203], v242 offset:26368
v_mfma_f32_16x16x32_bf16 v[12:15], v[204:207], a[4:7], v[12:15]
v_accvgpr_read_b32 v231, a213
v_perm_b32 v232, v231, v230, s86
v_mfma_f32_16x16x32_bf16 v[44:47], v[204:207], a[44:47], v[44:47]
buffer_load_dwordx4 a[40:43], v247, s[44:47], s53 offen offset:1024 sc1
v_mfma_f32_16x16x32_bf16 v[76:79], v[204:207], a[48:51], v[76:79]
s_waitcnt vmcnt(25) lgkmcnt(2)
v_perm_b32 v233, v231, v230, s87
v_mfma_f32_16x16x32_bf16 v[108:111], v[204:207], a[52:55], v[108:111]
ds_read_b64_tr_b16 v[204:205], v243 offset:25344
ds_read_b64_tr_b16 v[206:207], v243 offset:26368
v_mfma_f32_16x16x32_bf16 v[16:19], v[208:211], a[4:7], v[16:19]
s_add_u32 m0, s83, 118272
v_accvgpr_write_b32 a170, v232
v_mfma_f32_16x16x32_bf16 v[48:51], v[208:211], a[44:47], v[48:51]
buffer_load_dwordx4 v224, s[56:59], s80 offen lds
v_mfma_f32_16x16x32_bf16 v[80:83], v[208:211], a[48:51], v[80:83]
v_accvgpr_write_b32 a174, v233
v_accvgpr_read_b32 v234, a217
v_mfma_f32_16x16x32_bf16 v[112:115], v[208:211], a[52:55], v[112:115]
ds_read_b64_tr_b16 v[208:209], v240 offset:25472
ds_read_b64_tr_b16 v[210:211], v240 offset:26496
v_mfma_f32_16x16x32_bf16 v[20:23], v[212:215], a[4:7], v[20:23]
s_add_u32 m0, s83, 119296
v_accvgpr_read_b32 v235, a221
v_mfma_f32_16x16x32_bf16 v[52:55], v[212:215], a[44:47], v[52:55]
buffer_load_dwordx4 v225, s[56:59], s80 offen lds
v_mfma_f32_16x16x32_bf16 v[84:87], v[212:215], a[48:51], v[84:87]
v_perm_b32 v236, v235, v234, s86
v_perm_b32 v237, v235, v234, s87
v_mfma_f32_16x16x32_bf16 v[116:119], v[212:215], a[52:55], v[116:119]
ds_read_b64_tr_b16 v[212:213], v241 offset:25472
ds_read_b64_tr_b16 v[214:215], v241 offset:26496
v_mfma_f32_16x16x32_bf16 v[24:27], v[216:219], a[4:7], v[24:27]
s_add_u32 m0, s83, 120448
v_accvgpr_write_b32 a171, v236
v_mfma_f32_16x16x32_bf16 v[56:59], v[216:219], a[44:47], v[56:59]
buffer_load_dwordx4 v226, s[56:59], s80 offen lds
v_mfma_f32_16x16x32_bf16 v[88:91], v[216:219], a[48:51], v[88:91]
v_accvgpr_write_b32 a175, v237
v_accvgpr_read_b32 v230, a194
v_mfma_f32_16x16x32_bf16 v[120:123], v[216:219], a[52:55], v[120:123]
ds_read_b64_tr_b16 v[216:217], v242 offset:25472
ds_read_b64_tr_b16 v[218:219], v242 offset:26496
v_mfma_f32_16x16x32_bf16 v[28:31], v[220:223], a[4:7], v[28:31]
v_accvgpr_read_b32 v231, a198
v_perm_b32 v232, v231, v230, s86
v_mfma_f32_16x16x32_bf16 v[60:63], v[220:223], a[44:47], v[60:63]
v_perm_b32 v233, v231, v230, s87
v_accvgpr_write_b32 a176, v232
v_mfma_f32_16x16x32_bf16 v[92:95], v[220:223], a[48:51], v[92:95]
v_accvgpr_write_b32 a180, v233
v_accvgpr_read_b32 v234, a202
v_mfma_f32_16x16x32_bf16 v[124:127], v[220:223], a[52:55], v[124:127]
ds_read_b64_tr_b16 v[220:221], v243 offset:25472
ds_read_b64_tr_b16 v[222:223], v243 offset:26496
v_mfma_f32_16x16x32_bf16 v[0:3], v[192:195], v[136:139], v[0:3]
v_accvgpr_read_b32 v235, a206
v_perm_b32 v236, v235, v234, s86
v_mfma_f32_16x16x32_bf16 v[32:35], v[192:195], v[140:143], v[32:35]
buffer_load_dwordx4 a[4:7], v244, s[44:47], s53 offen offset:2048 sc1
v_mfma_f32_16x16x32_bf16 v[64:67], v[192:195], v[152:155], v[64:67]
v_perm_b32 v237, v235, v234, s87
v_accvgpr_write_b32 a177, v236
v_mfma_f32_16x16x32_bf16 v[96:99], v[192:195], v[156:159], v[96:99]
ds_read_b64_tr_b16 v[192:193], v240 offset:33792
ds_read_b64_tr_b16 v[194:195], v240 offset:34816
v_mfma_f32_16x16x32_bf16 v[4:7], v[196:199], v[136:139], v[4:7]
v_accvgpr_write_b32 a181, v237
v_accvgpr_read_b32 v230, a210
v_mfma_f32_16x16x32_bf16 v[36:39], v[196:199], v[140:143], v[36:39]
buffer_load_dwordx4 a[44:47], v245, s[44:47], s53 offen offset:2048 sc1
v_mfma_f32_16x16x32_bf16 v[68:71], v[196:199], v[152:155], v[68:71]
s_waitcnt lgkmcnt(2)
v_accvgpr_read_b32 v231, a214
v_mfma_f32_16x16x32_bf16 v[100:103], v[196:199], v[156:159], v[100:103]
ds_read_b64_tr_b16 v[196:197], v241 offset:33792
ds_read_b64_tr_b16 v[198:199], v241 offset:34816
v_mfma_f32_16x16x32_bf16 v[8:11], v[200:203], v[136:139], v[8:11]
v_perm_b32 v232, v231, v230, s86
v_perm_b32 v233, v231, v230, s87
v_mfma_f32_16x16x32_bf16 v[40:43], v[200:203], v[140:143], v[40:43]
buffer_load_dwordx4 a[48:51], v246, s[44:47], s53 offen offset:2048 sc1
v_mfma_f32_16x16x32_bf16 v[72:75], v[200:203], v[152:155], v[72:75]
v_accvgpr_write_b32 a178, v232
v_accvgpr_write_b32 a182, v233
v_mfma_f32_16x16x32_bf16 v[104:107], v[200:203], v[156:159], v[104:107]
ds_read_b64_tr_b16 v[200:201], v242 offset:33792
ds_read_b64_tr_b16 v[202:203], v242 offset:34816
v_mfma_f32_16x16x32_bf16 v[12:15], v[204:207], v[136:139], v[12:15]
v_accvgpr_read_b32 v234, a218
v_accvgpr_read_b32 v235, a222
v_mfma_f32_16x16x32_bf16 v[44:47], v[204:207], v[140:143], v[44:47]
buffer_load_dwordx4 a[52:55], v247, s[44:47], s53 offen offset:2048 sc1
v_mfma_f32_16x16x32_bf16 v[76:79], v[204:207], v[152:155], v[76:79]
v_perm_b32 v236, v235, v234, s86
v_perm_b32 v237, v235, v234, s87
v_mfma_f32_16x16x32_bf16 v[108:111], v[204:207], v[156:159], v[108:111]
ds_read_b64_tr_b16 v[204:205], v243 offset:33792
ds_read_b64_tr_b16 v[206:207], v243 offset:34816
v_mfma_f32_16x16x32_bf16 v[16:19], v[208:211], v[136:139], v[16:19]
s_add_u32 m0, s83, 121472
v_accvgpr_write_b32 a179, v236
v_mfma_f32_16x16x32_bf16 v[48:51], v[208:211], v[140:143], v[48:51]
buffer_load_dwordx4 v227, s[56:59], s80 offen lds
v_mfma_f32_16x16x32_bf16 v[80:83], v[208:211], v[152:155], v[80:83]
s_add_u32 s80, s80, 131072
v_accvgpr_write_b32 a183, v237
v_mfma_f32_16x16x32_bf16 v[112:115], v[208:211], v[156:159], v[112:115]
ds_read_b64_tr_b16 v[208:209], v240 offset:33920
ds_read_b64_tr_b16 v[210:211], v240 offset:34944
v_mfma_f32_16x16x32_bf16 v[20:23], v[212:215], v[136:139], v[20:23]
s_add_u32 m0, s83, 67584
v_accvgpr_read_b32 v230, a195
v_mfma_f32_16x16x32_bf16 v[52:55], v[212:215], v[140:143], v[52:55]
buffer_load_dwordx4 v224, s[56:59], s80 offen lds
.La5_LW3_2:
s_cbranch_execz .La5_LW3_R0
v_mfma_f32_16x16x32_bf16 v[84:87], v[212:215], v[152:155], v[84:87]
v_accvgpr_read_b32 v231, a199
v_perm_b32 v232, v231, v230, s86
v_mfma_f32_16x16x32_bf16 v[116:119], v[212:215], v[156:159], v[116:119]
ds_read_b64_tr_b16 v[212:213], v241 offset:33920
ds_read_b64_tr_b16 v[214:215], v241 offset:34944
v_mfma_f32_16x16x32_bf16 v[24:27], v[216:219], v[136:139], v[24:27]
s_add_u32 m0, s83, 68608
v_perm_b32 v233, v231, v230, s87
v_mfma_f32_16x16x32_bf16 v[56:59], v[216:219], v[140:143], v[56:59]
buffer_load_dwordx4 v225, s[56:59], s80 offen lds
v_mfma_f32_16x16x32_bf16 v[88:91], v[216:219], v[152:155], v[88:91]
v_accvgpr_write_b32 a184, v232
v_accvgpr_write_b32 a188, v233
v_mfma_f32_16x16x32_bf16 v[120:123], v[216:219], v[156:159], v[120:123]
ds_read_b64_tr_b16 v[216:217], v242 offset:33920
ds_read_b64_tr_b16 v[218:219], v242 offset:34944
v_mfma_f32_16x16x32_bf16 v[28:31], v[220:223], v[136:139], v[28:31]
v_accvgpr_read_b32 v234, a203
v_accvgpr_read_b32 v235, a207
v_mfma_f32_16x16x32_bf16 v[60:63], v[220:223], v[140:143], v[60:63]
s_waitcnt vmcnt(22)
v_cmp_eq_u64 vcc, v[250:251], v[252:253]
v_mfma_f32_16x16x32_bf16 v[92:95], v[220:223], v[152:155], v[92:95]
s_waitcnt lgkmcnt(2)
v_perm_b32 v236, v235, v234, s86
v_mfma_f32_16x16x32_bf16 v[124:127], v[220:223], v[156:159], v[124:127]
ds_read_b64_tr_b16 v[220:221], v243 offset:33920
ds_read_b64_tr_b16 v[222:223], v243 offset:34944
v_mfma_f32_16x16x32_bf16 v[0:3], v[192:195], v[168:171], v[0:3]
s_add_u32 m0, s83, 69760
v_perm_b32 v237, v235, v234, s87
v_mfma_f32_16x16x32_bf16 v[32:35], v[192:195], v[172:175], v[32:35]
buffer_load_dwordx4 v226, s[56:59], s80 offen lds
v_mfma_f32_16x16x32_bf16 v[64:67], v[192:195], v[184:187], v[64:67]
v_accvgpr_write_b32 a185, v236
v_accvgpr_write_b32 a189, v237
v_mfma_f32_16x16x32_bf16 v[96:99], v[192:195], v[188:191], v[96:99]
ds_read_b64_tr_b16 v[192:193], v240 offset:42240
ds_read_b64_tr_b16 v[194:195], v240 offset:43264
v_mfma_f32_16x16x32_bf16 v[4:7], v[196:199], v[168:171], v[4:7]
s_add_u32 m0, s83, 70784
v_accvgpr_read_b32 v230, a211
v_mfma_f32_16x16x32_bf16 v[36:39], v[196:199], v[172:175], v[36:39]
buffer_load_dwordx4 v227, s[56:59], s80 offen lds
v_mfma_f32_16x16x32_bf16 v[68:71], v[196:199], v[184:187], v[68:71]
s_bfe_u32 s80, s4, 262172
s_xor_b32 s80, s80, s72
v_mfma_f32_16x16x32_bf16 v[100:103], v[196:199], v[188:191], v[100:103]
ds_read_b64_tr_b16 v[196:197], v241 offset:42240
ds_read_b64_tr_b16 v[198:199], v241 offset:43264
v_mfma_f32_16x16x32_bf16 v[8:11], v[200:203], v[168:171], v[8:11]
s_lshl_b32 s80, s80, 18
v_accvgpr_read_b32 v231, a215
v_mfma_f32_16x16x32_bf16 v[40:43], v[200:203], v[172:175], v[40:43]
buffer_load_dwordx2 v[250:251], v248, s[64:67], s5 offen sc1
v_mfma_f32_16x16x32_bf16 v[72:75], v[200:203], v[184:187], v[72:75]
v_perm_b32 v232, v231, v230, s86
v_perm_b32 v233, v231, v230, s87
v_mfma_f32_16x16x32_bf16 v[104:107], v[200:203], v[188:191], v[104:107]
ds_read_b64_tr_b16 v[200:201], v242 offset:42240
ds_read_b64_tr_b16 v[202:203], v242 offset:43264
v_mfma_f32_16x16x32_bf16 v[12:15], v[204:207], v[168:171], v[12:15]
s_add_u32 m0, s83, 84480
v_accvgpr_write_b32 a186, v232
v_mfma_f32_16x16x32_bf16 v[44:47], v[204:207], v[172:175], v[44:47]
buffer_load_dwordx4 v224, s[56:59], s80 offen lds
v_mfma_f32_16x16x32_bf16 v[76:79], v[204:207], v[184:187], v[76:79]
v_accvgpr_write_b32 a190, v233
v_accvgpr_read_b32 v234, a219
v_mfma_f32_16x16x32_bf16 v[108:111], v[204:207], v[188:191], v[108:111]
ds_read_b64_tr_b16 v[204:205], v243 offset:42240
ds_read_b64_tr_b16 v[206:207], v243 offset:43264
v_mfma_f32_16x16x32_bf16 v[16:19], v[208:211], v[168:171], v[16:19]
s_add_u32 m0, s83, 85504
v_accvgpr_read_b32 v235, a223
v_mfma_f32_16x16x32_bf16 v[48:51], v[208:211], v[172:175], v[48:51]
buffer_load_dwordx4 v225, s[56:59], s80 offen lds
v_mfma_f32_16x16x32_bf16 v[80:83], v[208:211], v[184:187], v[80:83]
v_perm_b32 v236, v235, v234, s86
v_perm_b32 v237, v235, v234, s87
v_mfma_f32_16x16x32_bf16 v[112:115], v[208:211], v[188:191], v[112:115]
ds_read_b64_tr_b16 v[208:209], v240 offset:42368
ds_read_b64_tr_b16 v[210:211], v240 offset:43392
v_mfma_f32_16x16x32_bf16 v[20:23], v[212:215], v[168:171], v[20:23]
s_add_u32 m0, s83, 86656
v_accvgpr_write_b32 a187, v236
v_mfma_f32_16x16x32_bf16 v[52:55], v[212:215], v[172:175], v[52:55]
buffer_load_dwordx4 v226, s[56:59], s80 offen lds
v_mfma_f32_16x16x32_bf16 v[84:87], v[212:215], v[184:187], v[84:87]
s_waitcnt vmcnt(24) lgkmcnt(2)
v_accvgpr_write_b32 a191, v237
v_mfma_f32_16x16x32_bf16 v[116:119], v[212:215], v[188:191], v[116:119]
ds_read_b64_tr_b16 v[212:213], v241 offset:42368
ds_read_b64_tr_b16 v[214:215], v241 offset:43392
v_mfma_f32_16x16x32_bf16 v[24:27], v[216:219], v[168:171], v[24:27]
s_add_u32 m0, s83, 87680
v_accvgpr_read_b32 v230, a224
v_mfma_f32_16x16x32_bf16 v[56:59], v[216:219], v[172:175], v[56:59]
buffer_load_dwordx4 v227, s[56:59], s80 offen lds
v_mfma_f32_16x16x32_bf16 v[88:91], v[216:219], v[184:187], v[88:91]
s_add_u32 s80, s80, 131072
v_accvgpr_read_b32 v231, a228
v_mfma_f32_16x16x32_bf16 v[120:123], v[216:219], v[188:191], v[120:123]
ds_read_b64_tr_b16 v[216:217], v242 offset:42368
ds_read_b64_tr_b16 v[218:219], v242 offset:43392
v_mfma_f32_16x16x32_bf16 v[28:31], v[220:223], v[168:171], v[28:31]
s_add_u32 m0, s83, 101376
v_perm_b32 v232, v231, v230, s86
v_mfma_f32_16x16x32_bf16 v[60:63], v[220:223], v[172:175], v[60:63]
buffer_load_dwordx4 v224, s[56:59], s80 offen lds
v_mfma_f32_16x16x32_bf16 v[92:95], v[220:223], v[184:187], v[92:95]
v_perm_b32 v233, v231, v230, s87
v_accvgpr_write_b32 a192, v232
v_mfma_f32_16x16x32_bf16 v[124:127], v[220:223], v[188:191], v[124:127]
ds_read_b64_tr_b16 v[220:221], v243 offset:42368
ds_read_b64_tr_b16 v[222:223], v243 offset:43392
v_mfma_f32_16x16x32_bf16 v[0:3], v[192:195], a[8:11], v[0:3]
s_add_u32 m0, s83, 102400
v_accvgpr_write_b32 a196, v233
v_mfma_f32_16x16x32_bf16 v[32:35], v[192:195], a[12:15], v[32:35]
buffer_load_dwordx4 v225, s[56:59], s80 offen lds
v_mfma_f32_16x16x32_bf16 v[64:67], v[192:195], a[16:19], v[64:67]
v_accvgpr_read_b32 v234, a232
v_accvgpr_read_b32 v235, a236
v_mfma_f32_16x16x32_bf16 v[96:99], v[192:195], a[20:23], v[96:99]
v_perm_b32 v236, v235, v234, s86
v_perm_b32 v237, v235, v234, s87
v_mfma_f32_16x16x32_bf16 v[4:7], v[196:199], a[8:11], v[4:7]
s_add_u32 m0, s83, 103552
s_waitcnt lgkmcnt(0)
s_barrier
v_mfma_f32_16x16x32_bf16 v[36:39], v[196:199], a[12:15], v[36:39]
buffer_load_dwordx4 v226, s[56:59], s80 offen lds
v_mfma_f32_16x16x32_bf16 v[68:71], v[196:199], a[16:19], v[68:71]
ds_read_b64_tr_b16 v[192:193], v240 offset:50688
ds_read_b64_tr_b16 v[194:195], v240 offset:51712
v_mfma_f32_16x16x32_bf16 v[100:103], v[196:199], a[20:23], v[100:103]
ds_read_b64_tr_b16 v[196:197], v241 offset:50688
ds_read_b64_tr_b16 v[198:199], v241 offset:51712
v_mfma_f32_16x16x32_bf16 v[8:11], v[200:203], a[8:11], v[8:11]
s_add_u32 m0, s83, 104576
v_accvgpr_write_b32 a193, v236
v_mfma_f32_16x16x32_bf16 v[40:43], v[200:203], a[12:15], v[40:43]
buffer_load_dwordx4 v227, s[56:59], s80 offen lds
v_mfma_f32_16x16x32_bf16 v[72:75], v[200:203], a[16:19], v[72:75]
v_bfe_u32 v224, v255, 6, 1
v_lshrrev_b32 v225, 7, v255
v_mfma_f32_16x16x32_bf16 v[104:107], v[200:203], a[20:23], v[104:107]
ds_read_b64_tr_b16 v[200:201], v242 offset:50688
ds_read_b64_tr_b16 v[202:203], v242 offset:51712
v_mfma_f32_16x16x32_bf16 v[12:15], v[204:207], a[8:11], v[12:15]
v_and_b32_e64 v226, 48, v255
v_lshl_add_u32 v225, v225, 6, v226
v_mfma_f32_16x16x32_bf16 v[44:47], v[204:207], a[12:15], v[44:47]
buffer_load_dwordx4 v[136:139], v244, s[44:47], s53 offen offset:3072 sc1
v_mfma_f32_16x16x32_bf16 v[76:79], v[204:207], a[16:19], v[76:79]
s_waitcnt vmcnt(26) lgkmcnt(2)
v_lshl_add_u32 v224, v224, 11, v225
v_mfma_f32_16x16x32_bf16 v[108:111], v[204:207], a[20:23], v[108:111]
ds_read_b64_tr_b16 v[204:205], v243 offset:50688
ds_read_b64_tr_b16 v[206:207], v243 offset:51712
v_mfma_f32_16x16x32_bf16 v[16:19], v[208:211], a[8:11], v[16:19]
v_accvgpr_write_b32 a197, v237
v_accvgpr_read_b32 v230, a240
v_mfma_f32_16x16x32_bf16 v[48:51], v[208:211], a[12:15], v[48:51]
buffer_load_dwordx4 v[140:143], v245, s[44:47], s53 offen offset:3072 sc1
v_mfma_f32_16x16x32_bf16 v[80:83], v[208:211], a[16:19], v[80:83]
v_accvgpr_read_b32 v231, a244
v_perm_b32 v232, v231, v230, s86
v_mfma_f32_16x16x32_bf16 v[112:115], v[208:211], a[20:23], v[112:115]
ds_read_b64_tr_b16 v[208:209], v240 offset:50816
ds_read_b64_tr_b16 v[210:211], v240 offset:51840
v_mfma_f32_16x16x32_bf16 v[20:23], v[212:215], a[8:11], v[20:23]
v_perm_b32 v233, v231, v230, s87
v_accvgpr_write_b32 a194, v232
v_mfma_f32_16x16x32_bf16 v[52:55], v[212:215], a[12:15], v[52:55]
buffer_load_dwordx4 v[152:155], v246, s[44:47], s53 offen offset:3072 sc1
v_mfma_f32_16x16x32_bf16 v[84:87], v[212:215], a[16:19], v[84:87]
v_accvgpr_write_b32 a198, v233
v_accvgpr_read_b32 v234, a248
v_mfma_f32_16x16x32_bf16 v[116:119], v[212:215], a[20:23], v[116:119]
ds_read_b64_tr_b16 v[212:213], v241 offset:50816
ds_read_b64_tr_b16 v[214:215], v241 offset:51840
v_mfma_f32_16x16x32_bf16 v[24:27], v[216:219], a[8:11], v[24:27]
v_accvgpr_read_b32 v235, a252
v_perm_b32 v236, v235, v234, s86
v_mfma_f32_16x16x32_bf16 v[56:59], v[216:219], a[12:15], v[56:59]
buffer_load_dwordx4 v[156:159], v247, s[44:47], s53 offen offset:3072 sc1
v_mfma_f32_16x16x32_bf16 v[88:91], v[216:219], a[16:19], v[88:91]
v_perm_b32 v237, v235, v234, s87
v_accvgpr_write_b32 a195, v236
v_mfma_f32_16x16x32_bf16 v[120:123], v[216:219], a[20:23], v[120:123]
ds_read_b64_tr_b16 v[216:217], v242 offset:50816
ds_read_b64_tr_b16 v[218:219], v242 offset:51840
v_mfma_f32_16x16x32_bf16 v[28:31], v[220:223], a[8:11], v[28:31]
s_bitcmp1_b32 vcc_lo, 6
s_cbranch_scc0 .La5_LO_N6
.La5_LO_K6:
v_mfma_f32_16x16x32_bf16 v[60:63], v[220:223], a[12:15], v[60:63]
v_accvgpr_write_b32 a199, v237
v_accvgpr_read_b32 v230, a225
v_mfma_f32_16x16x32_bf16 v[92:95], v[220:223], a[16:19], v[92:95]
v_accvgpr_read_b32 v231, a229
v_perm_b32 v232, v231, v230, s86
v_mfma_f32_16x16x32_bf16 v[124:127], v[220:223], a[20:23], v[124:127]
ds_read_b64_tr_b16 v[220:221], v243 offset:50816
ds_read_b64_tr_b16 v[222:223], v243 offset:51840
v_mfma_f32_16x16x32_bf16 v[0:3], v[192:195], a[24:27], v[0:3]
v_perm_b32 v233, v231, v230, s87
v_accvgpr_write_b32 a200, v232
v_mfma_f32_16x16x32_bf16 v[32:35], v[192:195], a[28:31], v[32:35]
buffer_load_dwordx4 a[64:67], v244, s[44:47], s54 offen offset:0 sc1
v_mfma_f32_16x16x32_bf16 v[64:67], v[192:195], a[56:59], v[64:67]
v_accvgpr_write_b32 a204, v233
v_accvgpr_read_b32 v234, a233
v_mfma_f32_16x16x32_bf16 v[96:99], v[192:195], a[60:63], v[96:99]
ds_read_b64_tr_b16 v[192:193], v240 offset:59136
ds_read_b64_tr_b16 v[194:195], v240 offset:60160
v_mfma_f32_16x16x32_bf16 v[4:7], v[196:199], a[24:27], v[4:7]
v_accvgpr_read_b32 v235, a237
v_perm_b32 v236, v235, v234, s86
v_mfma_f32_16x16x32_bf16 v[36:39], v[196:199], a[28:31], v[36:39]
buffer_load_dwordx4 a[68:71], v245, s[44:47], s54 offen offset:0 sc1
v_mfma_f32_16x16x32_bf16 v[68:71], v[196:199], a[56:59], v[68:71]
s_waitcnt lgkmcnt(2)
v_perm_b32 v237, v235, v234, s87
v_mfma_f32_16x16x32_bf16 v[100:103], v[196:199], a[60:63], v[100:103]
ds_read_b64_tr_b16 v[196:197], v241 offset:59136
ds_read_b64_tr_b16 v[198:199], v241 offset:60160
v_mfma_f32_16x16x32_bf16 v[8:11], v[200:203], a[24:27], v[8:11]
v_accvgpr_write_b32 a201, v236
v_accvgpr_write_b32 a205, v237
v_mfma_f32_16x16x32_bf16 v[40:43], v[200:203], a[28:31], v[40:43]
buffer_load_dwordx4 a[72:75], v244, s[44:47], s54 offen offset:1024 sc1
v_mfma_f32_16x16x32_bf16 v[72:75], v[200:203], a[56:59], v[72:75]
v_accvgpr_read_b32 v230, a241
v_accvgpr_read_b32 v231, a245
v_mfma_f32_16x16x32_bf16 v[104:107], v[200:203], a[60:63], v[104:107]
ds_read_b64_tr_b16 v[200:201], v242 offset:59136
ds_read_b64_tr_b16 v[202:203], v242 offset:60160
v_mfma_f32_16x16x32_bf16 v[12:15], v[204:207], a[24:27], v[12:15]
v_perm_b32 v232, v231, v230, s86
v_perm_b32 v233, v231, v230, s87
v_mfma_f32_16x16x32_bf16 v[44:47], v[204:207], a[28:31], v[44:47]
buffer_load_dwordx4 a[76:79], v245, s[44:47], s54 offen offset:1024 sc1
v_mfma_f32_16x16x32_bf16 v[76:79], v[204:207], a[56:59], v[76:79]
v_accvgpr_write_b32 a202, v232
v_accvgpr_write_b32 a206, v233
v_mfma_f32_16x16x32_bf16 v[108:111], v[204:207], a[60:63], v[108:111]
ds_read_b64_tr_b16 v[204:205], v243 offset:59136
ds_read_b64_tr_b16 v[206:207], v243 offset:60160
v_mfma_f32_16x16x32_bf16 v[16:19], v[208:211], a[24:27], v[16:19]
v_accvgpr_read_b32 v234, a249
v_accvgpr_read_b32 v235, a253
v_mfma_f32_16x16x32_bf16 v[48:51], v[208:211], a[28:31], v[48:51]
buffer_load_dwordx4 a[80:83], v244, s[44:47], s54 offen offset:2048 sc1
v_mfma_f32_16x16x32_bf16 v[80:83], v[208:211], a[56:59], v[80:83]
v_perm_b32 v236, v235, v234, s86
v_perm_b32 v237, v235, v234, s87
v_mfma_f32_16x16x32_bf16 v[112:115], v[208:211], a[60:63], v[112:115]
ds_read_b64_tr_b16 v[208:209], v240 offset:59264
ds_read_b64_tr_b16 v[210:211], v240 offset:60288
v_mfma_f32_16x16x32_bf16 v[20:23], v[212:215], a[24:27], v[20:23]
v_accvgpr_write_b32 a203, v236
v_accvgpr_write_b32 a207, v237
v_mfma_f32_16x16x32_bf16 v[52:55], v[212:215], a[28:31], v[52:55]
buffer_load_dwordx4 a[84:87], v245, s[44:47], s54 offen offset:2048 sc1
v_mfma_f32_16x16x32_bf16 v[84:87], v[212:215], a[56:59], v[84:87]
v_accvgpr_read_b32 v230, a226
v_accvgpr_read_b32 v231, a230
v_mfma_f32_16x16x32_bf16 v[116:119], v[212:215], a[60:63], v[116:119]
ds_read_b64_tr_b16 v[212:213], v241 offset:59264
ds_read_b64_tr_b16 v[214:215], v241 offset:60288
v_mfma_f32_16x16x32_bf16 v[24:27], v[216:219], a[24:27], v[24:27]
v_perm_b32 v232, v231, v230, s86
v_perm_b32 v233, v231, v230, s87
v_mfma_f32_16x16x32_bf16 v[56:59], v[216:219], a[28:31], v[56:59]
buffer_load_dwordx4 a[88:91], v244, s[44:47], s54 offen offset:3072 sc1
v_mfma_f32_16x16x32_bf16 v[88:91], v[216:219], a[56:59], v[88:91]
v_accvgpr_write_b32 a208, v232
v_accvgpr_write_b32 a212, v233
v_mfma_f32_16x16x32_bf16 v[120:123], v[216:219], a[60:63], v[120:123]
ds_read_b64_tr_b16 v[216:217], v242 offset:59264
ds_read_b64_tr_b16 v[218:219], v242 offset:60288
v_mfma_f32_16x16x32_bf16 v[28:31], v[220:223], a[24:27], v[28:31]
v_accvgpr_read_b32 v234, a234
v_accvgpr_read_b32 v235, a238
v_mfma_f32_16x16x32_bf16 v[60:63], v[220:223], a[28:31], v[60:63]
v_perm_b32 v236, v235, v234, s86
v_perm_b32 v237, v235, v234, s87
v_mfma_f32_16x16x32_bf16 v[92:95], v[220:223], a[56:59], v[92:95]
s_waitcnt vmcnt(32) lgkmcnt(2)
v_accvgpr_write_b32 a209, v236
v_mfma_f32_16x16x32_bf16 v[124:127], v[220:223], a[60:63], v[124:127]
ds_read_b64_tr_b16 v[220:221], v243 offset:59264
ds_read_b64_tr_b16 v[222:223], v243 offset:60288
v_mfma_f32_16x16x32_bf16 v[0:3], v[192:195], a[0:3], v[0:3]
s_bitcmp1_b32 vcc_lo, 7
s_nop 0
s_nop 0
s_cbranch_scc0 .La5_LO_N7
.La5_LO_K7:
v_mfma_f32_16x16x32_bf16 v[32:35], v[192:195], a[32:35], v[32:35]
buffer_load_dwordx4 a[92:95], v245, s[44:47], s54 offen offset:3072 sc1
v_mfma_f32_16x16x32_bf16 v[64:67], v[192:195], a[36:39], v[64:67]
v_accvgpr_write_b32 a213, v237
v_accvgpr_read_b32 v230, a242
v_mfma_f32_16x16x32_bf16 v[96:99], v[192:195], a[40:43], v[96:99]
ds_read_b64_tr_b16 v[192:193], v240 offset:0
ds_read_b64_tr_b16 v[194:195], v240 offset:1024
v_mfma_f32_16x16x32_bf16 v[4:7], v[196:199], a[0:3], v[4:7]
v_accvgpr_read_b32 v231, a246
v_perm_b32 v232, v231, v230, s86
v_mfma_f32_16x16x32_bf16 v[36:39], v[196:199], a[32:35], v[36:39]
buffer_load_dwordx4 a[8:11], v244, s[44:47], s55 offen offset:0 sc1
v_mfma_f32_16x16x32_bf16 v[68:71], v[196:199], a[36:39], v[68:71]
v_perm_b32 v233, v231, v230, s87
v_accvgpr_write_b32 a210, v232
v_mfma_f32_16x16x32_bf16 v[100:103], v[196:199], a[40:43], v[100:103]
ds_read_b64_tr_b16 v[196:197], v241 offset:0
ds_read_b64_tr_b16 v[198:199], v241 offset:1024
v_mfma_f32_16x16x32_bf16 v[8:11], v[200:203], a[0:3], v[8:11]
v_accvgpr_write_b32 a214, v233
v_accvgpr_read_b32 v234, a250
v_mfma_f32_16x16x32_bf16 v[40:43], v[200:203], a[32:35], v[40:43]
buffer_load_dwordx4 a[12:15], v245, s[44:47], s55 offen offset:0 sc1
v_mfma_f32_16x16x32_bf16 v[72:75], v[200:203], a[36:39], v[72:75]
v_accvgpr_read_b32 v235, a254
v_perm_b32 v236, v235, v234, s86
v_mfma_f32_16x16x32_bf16 v[104:107], v[200:203], a[40:43], v[104:107]
ds_read_b64_tr_b16 v[200:201], v242 offset:0
ds_read_b64_tr_b16 v[202:203], v242 offset:1024
v_mfma_f32_16x16x32_bf16 v[12:15], v[204:207], a[0:3], v[12:15]
v_perm_b32 v237, v235, v234, s87
v_accvgpr_write_b32 a211, v236
v_mfma_f32_16x16x32_bf16 v[44:47], v[204:207], a[32:35], v[44:47]
buffer_load_dwordx4 a[16:19], v244, s[44:47], s55 offen offset:1024 sc1
v_mfma_f32_16x16x32_bf16 v[76:79], v[204:207], a[36:39], v[76:79]
s_waitcnt vmcnt(23)
v_cmp_eq_u64 vcc, v[250:251], v[252:253]
v_mfma_f32_16x16x32_bf16 v[108:111], v[204:207], a[40:43], v[108:111]
ds_read_b64_tr_b16 v[204:205], v243 offset:0
ds_read_b64_tr_b16 v[206:207], v243 offset:1024
v_mfma_f32_16x16x32_bf16 v[16:19], v[208:211], a[0:3], v[16:19]
v_accvgpr_write_b32 a215, v237
v_accvgpr_read_b32 v230, a227
v_mfma_f32_16x16x32_bf16 v[48:51], v[208:211], a[32:35], v[48:51]
buffer_load_dwordx4 a[20:23], v245, s[44:47], s55 offen offset:1024 sc1
v_mfma_f32_16x16x32_bf16 v[80:83], v[208:211], a[36:39], v[80:83]
v_accvgpr_read_b32 v231, a231
v_perm_b32 v232, v231, v230, s86
v_mfma_f32_16x16x32_bf16 v[112:115], v[208:211], a[40:43], v[112:115]
ds_read_b64_tr_b16 v[208:209], v240 offset:128
ds_read_b64_tr_b16 v[210:211], v240 offset:1152
v_mfma_f32_16x16x32_bf16 v[20:23], v[212:215], a[0:3], v[20:23]
v_perm_b32 v233, v231, v230, s87
v_accvgpr_write_b32 a216, v232
v_mfma_f32_16x16x32_bf16 v[52:55], v[212:215], a[32:35], v[52:55]
buffer_load_dwordx4 a[24:27], v244, s[44:47], s55 offen offset:2048 sc1
v_mfma_f32_16x16x32_bf16 v[84:87], v[212:215], a[36:39], v[84:87]
s_waitcnt lgkmcnt(2)
v_accvgpr_write_b32 a220, v233
v_mfma_f32_16x16x32_bf16 v[116:119], v[212:215], a[40:43], v[116:119]
ds_read_b64_tr_b16 v[212:213], v241 offset:128
ds_read_b64_tr_b16 v[214:215], v241 offset:1152
v_mfma_f32_16x16x32_bf16 v[24:27], v[216:219], a[0:3], v[24:27]
v_accvgpr_read_b32 v234, a235
v_accvgpr_read_b32 v235, a239
v_mfma_f32_16x16x32_bf16 v[56:59], v[216:219], a[32:35], v[56:59]
buffer_load_dwordx4 a[28:31], v245, s[44:47], s55 offen offset:2048 sc1
v_mfma_f32_16x16x32_bf16 v[88:91], v[216:219], a[36:39], v[88:91]
v_perm_b32 v236, v235, v234, s86
v_perm_b32 v237, v235, v234, s87
v_mfma_f32_16x16x32_bf16 v[120:123], v[216:219], a[40:43], v[120:123]
ds_read_b64_tr_b16 v[216:217], v242 offset:128
ds_read_b64_tr_b16 v[218:219], v242 offset:1152
v_mfma_f32_16x16x32_bf16 v[28:31], v[220:223], a[0:3], v[28:31]
v_accvgpr_write_b32 a217, v236
v_accvgpr_write_b32 a221, v237
v_mfma_f32_16x16x32_bf16 v[60:63], v[220:223], a[32:35], v[60:63]
v_accvgpr_read_b32 v230, a243
v_accvgpr_read_b32 v231, a247
v_mfma_f32_16x16x32_bf16 v[92:95], v[220:223], a[36:39], v[92:95]
v_perm_b32 v232, v231, v230, s86
v_perm_b32 v233, v231, v230, s87
v_mfma_f32_16x16x32_bf16 v[124:127], v[220:223], a[40:43], v[124:127]
ds_read_b64_tr_b16 v[220:221], v243 offset:128
ds_read_b64_tr_b16 v[222:223], v243 offset:1152
v_mfma_f32_16x16x32_bf16 v[0:3], v[192:195], a[4:7], v[0:3]
v_accvgpr_write_b32 a218, v232
v_accvgpr_write_b32 a222, v233
v_mfma_f32_16x16x32_bf16 v[32:35], v[192:195], a[44:47], v[32:35]
buffer_load_dwordx4 a[0:3], v244, s[44:47], s55 offen offset:3072 sc1
v_mfma_f32_16x16x32_bf16 v[64:67], v[192:195], a[48:51], v[64:67]
v_accvgpr_read_b32 v234, a251
v_accvgpr_read_b32 v235, a255
v_mfma_f32_16x16x32_bf16 v[96:99], v[192:195], a[52:55], v[96:99]
ds_read_b64_tr_b16 v[192:193], v240 offset:8448
ds_read_b64_tr_b16 v[194:195], v240 offset:9472
v_mfma_f32_16x16x32_bf16 v[4:7], v[196:199], a[4:7], v[4:7]
v_perm_b32 v236, v235, v234, s86
v_perm_b32 v237, v235, v234, s87
v_mfma_f32_16x16x32_bf16 v[36:39], v[196:199], a[44:47], v[36:39]
buffer_load_dwordx4 a[32:35], v245, s[44:47], s55 offen offset:3072 sc1
v_mfma_f32_16x16x32_bf16 v[68:71], v[196:199], a[48:51], v[68:71]
v_accvgpr_write_b32 a219, v236
v_accvgpr_write_b32 a223, v237
v_mfma_f32_16x16x32_bf16 v[100:103], v[196:199], a[52:55], v[100:103]
ds_read_b64_tr_b16 v[196:197], v241 offset:8448
ds_read_b64_tr_b16 v[198:199], v241 offset:9472
v_mfma_f32_16x16x32_bf16 v[8:11], v[200:203], a[4:7], v[8:11]
v_mfma_f32_16x16x32_bf16 v[40:43], v[200:203], a[44:47], v[40:43]
v_mfma_f32_16x16x32_bf16 v[72:75], v[200:203], a[48:51], v[72:75]
v_mfma_f32_16x16x32_bf16 v[104:107], v[200:203], a[52:55], v[104:107]
ds_read_b64_tr_b16 v[200:201], v242 offset:8448
ds_read_b64_tr_b16 v[202:203], v242 offset:9472
v_mfma_f32_16x16x32_bf16 v[12:15], v[204:207], a[4:7], v[12:15]
v_mfma_f32_16x16x32_bf16 v[44:47], v[204:207], a[44:47], v[44:47]
v_mfma_f32_16x16x32_bf16 v[76:79], v[204:207], a[48:51], v[76:79]
s_waitcnt vmcnt(16) lgkmcnt(2)
v_mfma_f32_16x16x32_bf16 v[108:111], v[204:207], a[52:55], v[108:111]
ds_read_b64_tr_b16 v[204:205], v243 offset:8448
ds_read_b64_tr_b16 v[206:207], v243 offset:9472
v_mfma_f32_16x16x32_bf16 v[16:19], v[208:211], a[4:7], v[16:19]
v_mfma_f32_16x16x32_bf16 v[48:51], v[208:211], a[44:47], v[48:51]
v_mfma_f32_16x16x32_bf16 v[80:83], v[208:211], a[48:51], v[80:83]
v_mfma_f32_16x16x32_bf16 v[112:115], v[208:211], a[52:55], v[112:115]
ds_read_b64_tr_b16 v[208:209], v240 offset:8576
ds_read_b64_tr_b16 v[210:211], v240 offset:9600
v_mfma_f32_16x16x32_bf16 v[20:23], v[212:215], a[4:7], v[20:23]
v_add_u32 v240, 67584, v240
v_mfma_f32_16x16x32_bf16 v[52:55], v[212:215], a[44:47], v[52:55]
v_mfma_f32_16x16x32_bf16 v[84:87], v[212:215], a[48:51], v[84:87]
v_mfma_f32_16x16x32_bf16 v[116:119], v[212:215], a[52:55], v[116:119]
ds_read_b64_tr_b16 v[212:213], v241 offset:8576
ds_read_b64_tr_b16 v[214:215], v241 offset:9600
v_mfma_f32_16x16x32_bf16 v[24:27], v[216:219], a[4:7], v[24:27]
v_add_u32 v241, 67584, v241
v_mfma_f32_16x16x32_bf16 v[56:59], v[216:219], a[44:47], v[56:59]
v_mfma_f32_16x16x32_bf16 v[88:91], v[216:219], a[48:51], v[88:91]
v_mfma_f32_16x16x32_bf16 v[120:123], v[216:219], a[52:55], v[120:123]
ds_read_b64_tr_b16 v[216:217], v242 offset:8576
ds_read_b64_tr_b16 v[218:219], v242 offset:9600
v_mfma_f32_16x16x32_bf16 v[28:31], v[220:223], a[4:7], v[28:31]
v_add_u32 v242, 67584, v242
v_mfma_f32_16x16x32_bf16 v[60:63], v[220:223], a[44:47], v[60:63]
v_mfma_f32_16x16x32_bf16 v[92:95], v[220:223], a[48:51], v[92:95]
v_mfma_f32_16x16x32_bf16 v[124:127], v[220:223], a[52:55], v[124:127]
ds_read_b64_tr_b16 v[220:221], v243 offset:8576
ds_read_b64_tr_b16 v[222:223], v243 offset:9600
v_mfma_f32_16x16x32_bf16 v[0:3], v[192:195], v[136:139], v[0:3]
v_add_u32 v243, 67584, v243
v_mfma_f32_16x16x32_bf16 v[32:35], v[192:195], v[140:143], v[32:35]
buffer_load_dwordx4 a[4:7], v246, s[44:47], s54 offen offset:0 sc1
v_mfma_f32_16x16x32_bf16 v[64:67], v[192:195], v[152:155], v[64:67]
v_mfma_f32_16x16x32_bf16 v[96:99], v[192:195], v[156:159], v[96:99]
v_mfma_f32_16x16x32_bf16 v[4:7], v[196:199], v[136:139], v[4:7]
s_waitcnt lgkmcnt(0)
s_barrier
v_mfma_f32_16x16x32_bf16 v[36:39], v[196:199], v[140:143], v[36:39]
buffer_load_dwordx4 a[36:39], v247, s[44:47], s54 offen offset:0 sc1
v_mfma_f32_16x16x32_bf16 v[68:71], v[196:199], v[152:155], v[68:71]
ds_read_b64_tr_b16 v[192:193], v240 offset:50688
ds_read_b64_tr_b16 v[194:195], v240 offset:51712
s_nop 0
v_mfma_f32_16x16x32_bf16 v[100:103], v[196:199], v[156:159], v[100:103]
ds_read_b64_tr_b16 v[196:197], v240 offset:59136
ds_read_b64_tr_b16 v[198:199], v240 offset:60160
v_mfma_f32_16x16x32_bf16 v[8:11], v[200:203], v[136:139], v[8:11]
v_mfma_f32_16x16x32_bf16 v[40:43], v[200:203], v[140:143], v[40:43]
buffer_load_dwordx4 a[40:43], v246, s[44:47], s54 offen offset:1024 sc1
v_mfma_f32_16x16x32_bf16 v[72:75], v[200:203], v[152:155], v[72:75]
v_mfma_f32_16x16x32_bf16 v[104:107], v[200:203], v[156:159], v[104:107]
ds_read_b64_tr_b16 v[200:201], v240 offset:0
ds_read_b64_tr_b16 v[202:203], v240 offset:1024
v_mfma_f32_16x16x32_bf16 v[12:15], v[204:207], v[136:139], v[12:15]
v_mfma_f32_16x16x32_bf16 v[44:47], v[204:207], v[140:143], v[44:47]
buffer_load_dwordx4 a[44:47], v247, s[44:47], s54 offen offset:1024 sc1
v_mfma_f32_16x16x32_bf16 v[76:79], v[204:207], v[152:155], v[76:79]
v_mfma_f32_16x16x32_bf16 v[108:111], v[204:207], v[156:159], v[108:111]
ds_read_b64_tr_b16 v[204:205], v240 offset:8448
ds_read_b64_tr_b16 v[206:207], v240 offset:9472
v_mfma_f32_16x16x32_bf16 v[16:19], v[208:211], v[136:139], v[16:19]
v_mfma_f32_16x16x32_bf16 v[48:51], v[208:211], v[140:143], v[48:51]
buffer_load_dwordx4 a[48:51], v246, s[44:47], s54 offen offset:2048 sc1
v_mfma_f32_16x16x32_bf16 v[80:83], v[208:211], v[152:155], v[80:83]
v_mfma_f32_16x16x32_bf16 v[112:115], v[208:211], v[156:159], v[112:115]
ds_read_b64_tr_b16 v[208:209], v240 offset:16896
ds_read_b64_tr_b16 v[210:211], v240 offset:17920
v_mfma_f32_16x16x32_bf16 v[20:23], v[212:215], v[136:139], v[20:23]
v_mfma_f32_16x16x32_bf16 v[52:55], v[212:215], v[140:143], v[52:55]
buffer_load_dwordx4 a[52:55], v247, s[44:47], s54 offen offset:2048 sc1
v_mfma_f32_16x16x32_bf16 v[84:87], v[212:215], v[152:155], v[84:87]
v_mfma_f32_16x16x32_bf16 v[116:119], v[212:215], v[156:159], v[116:119]
ds_read_b64_tr_b16 v[212:213], v240 offset:25344
ds_read_b64_tr_b16 v[214:215], v240 offset:26368
v_mfma_f32_16x16x32_bf16 v[24:27], v[216:219], v[136:139], v[24:27]
v_mfma_f32_16x16x32_bf16 v[56:59], v[216:219], v[140:143], v[56:59]
buffer_load_dwordx4 a[56:59], v246, s[44:47], s54 offen offset:3072 sc1
v_mfma_f32_16x16x32_bf16 v[88:91], v[216:219], v[152:155], v[88:91]
v_mfma_f32_16x16x32_bf16 v[120:123], v[216:219], v[156:159], v[120:123]
ds_read_b64_tr_b16 v[216:217], v240 offset:33792
ds_read_b64_tr_b16 v[218:219], v240 offset:34816
v_mfma_f32_16x16x32_bf16 v[28:31], v[220:223], v[136:139], v[28:31]
v_mfma_f32_16x16x32_bf16 v[60:63], v[220:223], v[140:143], v[60:63]
v_mfma_f32_16x16x32_bf16 v[92:95], v[220:223], v[152:155], v[92:95]
s_waitcnt vmcnt(15) lgkmcnt(6)
v_mfma_f32_16x16x32_bf16 v[124:127], v[220:223], v[156:159], v[124:127]
ds_read_b64_tr_b16 v[220:221], v240 offset:42240
ds_read_b64_tr_b16 v[222:223], v240 offset:43264
v_mfma_f32_16x16x32_bf16 v[0:3], v[192:195], a[64:67], v[0:3]
buffer_load_dwordx4 a[60:63], v247, s[44:47], s54 offen offset:3072 sc1
v_mfma_f32_16x16x32_bf16 v[32:35], v[192:195], a[68:71], v[32:35]
ds_read_b64_tr_b16 v[192:193], v241 offset:50688
ds_read_b64_tr_b16 v[194:195], v241 offset:51712
v_mfma_f32_16x16x32_bf16 v[0:3], v[196:199], a[72:75], v[0:3]
v_mfma_f32_16x16x32_bf16 v[32:35], v[196:199], a[76:79], v[32:35]
ds_read_b64_tr_b16 v[196:197], v241 offset:59136
ds_read_b64_tr_b16 v[198:199], v241 offset:60160
v_mfma_f32_16x16x32_bf16 v[0:3], v[200:203], a[80:83], v[0:3]
buffer_load_dwordx4 v[128:131], v246, s[44:47], s55 offen offset:0 sc1
v_mfma_f32_16x16x32_bf16 v[32:35], v[200:203], a[84:87], v[32:35]
ds_read_b64_tr_b16 v[200:201], v241 offset:0
ds_read_b64_tr_b16 v[202:203], v241 offset:1024
v_mfma_f32_16x16x32_bf16 v[0:3], v[204:207], a[88:91], v[0:3]
s_waitcnt vmcnt(9) lgkmcnt(6)
.La5_LW3_3:
s_cbranch_execz .La5_LW3_R0
v_mfma_f32_16x16x32_bf16 v[32:35], v[204:207], a[92:95], v[32:35]
ds_read_b64_tr_b16 v[204:205], v241 offset:8448
ds_read_b64_tr_b16 v[206:207], v241 offset:9472
v_mfma_f32_16x16x32_bf16 v[0:3], v[208:211], a[8:11], v[0:3]
buffer_load_dwordx4 v[132:135], v247, s[44:47], s55 offen offset:0 sc1
v_mfma_f32_16x16x32_bf16 v[32:35], v[208:211], a[12:15], v[32:35]
ds_read_b64_tr_b16 v[208:209], v241 offset:16896
ds_read_b64_tr_b16 v[210:211], v241 offset:17920
v_mfma_f32_16x16x32_bf16 v[0:3], v[212:215], a[16:19], v[0:3]
v_mfma_f32_16x16x32_bf16 v[32:35], v[212:215], a[20:23], v[32:35]
ds_read_b64_tr_b16 v[212:213], v241 offset:25344
ds_read_b64_tr_b16 v[214:215], v241 offset:26368
v_mfma_f32_16x16x32_bf16 v[0:3], v[216:219], a[24:27], v[0:3]
buffer_load_dwordx4 v[144:147], v246, s[44:47], s55 offen offset:1024 sc1
v_mfma_f32_16x16x32_bf16 v[32:35], v[216:219], a[28:31], v[32:35]
ds_read_b64_tr_b16 v[216:217], v241 offset:33792
ds_read_b64_tr_b16 v[218:219], v241 offset:34816
v_mfma_f32_16x16x32_bf16 v[0:3], v[220:223], a[0:3], v[0:3]
s_waitcnt lgkmcnt(6)
v_mfma_f32_16x16x32_bf16 v[32:35], v[220:223], a[32:35], v[32:35]
ds_read_b64_tr_b16 v[220:221], v241 offset:42240
ds_read_b64_tr_b16 v[222:223], v241 offset:43264
v_mfma_f32_16x16x32_bf16 v[4:7], v[192:195], a[64:67], v[4:7]
buffer_load_dwordx4 v[148:151], v247, s[44:47], s55 offen offset:1024 sc1
v_mfma_f32_16x16x32_bf16 v[36:39], v[192:195], a[68:71], v[36:39]
ds_read_b64_tr_b16 v[192:193], v242 offset:50688
ds_read_b64_tr_b16 v[194:195], v242 offset:51712
v_mfma_f32_16x16x32_bf16 v[4:7], v[196:199], a[72:75], v[4:7]
v_mfma_f32_16x16x32_bf16 v[36:39], v[196:199], a[76:79], v[36:39]
ds_read_b64_tr_b16 v[196:197], v242 offset:59136
ds_read_b64_tr_b16 v[198:199], v242 offset:60160
v_mfma_f32_16x16x32_bf16 v[4:7], v[200:203], a[80:83], v[4:7]
buffer_load_dwordx4 v[160:163], v246, s[44:47], s55 offen offset:2048 sc1
v_mfma_f32_16x16x32_bf16 v[36:39], v[200:203], a[84:87], v[36:39]
ds_read_b64_tr_b16 v[200:201], v242 offset:0
ds_read_b64_tr_b16 v[202:203], v242 offset:1024
v_mfma_f32_16x16x32_bf16 v[4:7], v[204:207], a[88:91], v[4:7]
s_waitcnt lgkmcnt(6)
v_mfma_f32_16x16x32_bf16 v[36:39], v[204:207], a[92:95], v[36:39]
ds_read_b64_tr_b16 v[204:205], v242 offset:8448
ds_read_b64_tr_b16 v[206:207], v242 offset:9472
v_mfma_f32_16x16x32_bf16 v[4:7], v[208:211], a[8:11], v[4:7]
buffer_load_dwordx4 v[164:167], v247, s[44:47], s55 offen offset:2048 sc1
v_mfma_f32_16x16x32_bf16 v[36:39], v[208:211], a[12:15], v[36:39]
ds_read_b64_tr_b16 v[208:209], v242 offset:16896
ds_read_b64_tr_b16 v[210:211], v242 offset:17920
v_mfma_f32_16x16x32_bf16 v[4:7], v[212:215], a[16:19], v[4:7]
v_mfma_f32_16x16x32_bf16 v[36:39], v[212:215], a[20:23], v[36:39]
ds_read_b64_tr_b16 v[212:213], v242 offset:25344
ds_read_b64_tr_b16 v[214:215], v242 offset:26368
v_mfma_f32_16x16x32_bf16 v[4:7], v[216:219], a[24:27], v[4:7]
v_mfma_f32_16x16x32_bf16 v[36:39], v[216:219], a[28:31], v[36:39]
ds_read_b64_tr_b16 v[216:217], v242 offset:33792
ds_read_b64_tr_b16 v[218:219], v242 offset:34816
v_mfma_f32_16x16x32_bf16 v[4:7], v[220:223], a[0:3], v[4:7]
s_waitcnt lgkmcnt(6)
v_mfma_f32_16x16x32_bf16 v[36:39], v[220:223], a[32:35], v[36:39]
ds_read_b64_tr_b16 v[220:221], v242 offset:42240
ds_read_b64_tr_b16 v[222:223], v242 offset:43264
v_mfma_f32_16x16x32_bf16 v[8:11], v[192:195], a[64:67], v[8:11]
buffer_load_dwordx4 v[176:179], v246, s[44:47], s55 offen offset:3072 sc1
v_mfma_f32_16x16x32_bf16 v[40:43], v[192:195], a[68:71], v[40:43]
ds_read_b64_tr_b16 v[192:193], v243 offset:50688
ds_read_b64_tr_b16 v[194:195], v243 offset:51712
v_mfma_f32_16x16x32_bf16 v[8:11], v[196:199], a[72:75], v[8:11]
v_cvt_pk_bf16_f32 v230, v0, v1
v_cvt_pk_bf16_f32 v232, v4, v5
v_mfma_f32_16x16x32_bf16 v[40:43], v[196:199], a[76:79], v[40:43]
ds_read_b64_tr_b16 v[196:197], v243 offset:59136
ds_read_b64_tr_b16 v[198:199], v243 offset:60160
v_mfma_f32_16x16x32_bf16 v[8:11], v[200:203], a[80:83], v[8:11]
buffer_load_dwordx4 v[180:183], v247, s[44:47], s55 offen offset:3072 sc1
v_mfma_f32_16x16x32_bf16 v[40:43], v[200:203], a[84:87], v[40:43]
ds_read_b64_tr_b16 v[200:201], v243 offset:0
ds_read_b64_tr_b16 v[202:203], v243 offset:1024
v_mfma_f32_16x16x32_bf16 v[8:11], v[204:207], a[88:91], v[8:11]
s_waitcnt lgkmcnt(6)
v_bfe_u32 v250, v255, 6, 1
v_mfma_f32_16x16x32_bf16 v[40:43], v[204:207], a[92:95], v[40:43]
ds_read_b64_tr_b16 v[204:205], v243 offset:8448
ds_read_b64_tr_b16 v[206:207], v243 offset:9472
v_mfma_f32_16x16x32_bf16 v[8:11], v[208:211], a[8:11], v[8:11]
v_cvt_pk_bf16_f32 v231, v2, v3
v_cvt_pk_bf16_f32 v233, v6, v7
v_mfma_f32_16x16x32_bf16 v[40:43], v[208:211], a[12:15], v[40:43]
ds_read_b64_tr_b16 v[208:209], v243 offset:16896
ds_read_b64_tr_b16 v[210:211], v243 offset:17920
v_mfma_f32_16x16x32_bf16 v[8:11], v[212:215], a[16:19], v[8:11]
v_permlane16_swap_b32 v230, v232
v_permlane16_swap_b32 v231, v233
v_mfma_f32_16x16x32_bf16 v[40:43], v[212:215], a[20:23], v[40:43]
ds_read_b64_tr_b16 v[212:213], v243 offset:25344
ds_read_b64_tr_b16 v[214:215], v243 offset:26368
v_mfma_f32_16x16x32_bf16 v[8:11], v[216:219], a[24:27], v[8:11]
buffer_store_dwordx4 v[230:233], v238, s[32:35], s94 offen offset:0 sc1
v_mfma_f32_16x16x32_bf16 v[40:43], v[216:219], a[28:31], v[40:43]
ds_read_b64_tr_b16 v[216:217], v243 offset:33792
ds_read_b64_tr_b16 v[218:219], v243 offset:34816
v_mfma_f32_16x16x32_bf16 v[8:11], v[220:223], a[0:3], v[8:11]
s_waitcnt lgkmcnt(6)
v_mul_u32_u24 v250, 7, v250
v_mfma_f32_16x16x32_bf16 v[40:43], v[220:223], a[32:35], v[40:43]
ds_read_b64_tr_b16 v[220:221], v243 offset:42240
ds_read_b64_tr_b16 v[222:223], v243 offset:43264
v_mfma_f32_16x16x32_bf16 v[12:15], v[192:195], a[64:67], v[12:15]
v_cvt_pk_bf16_f32 v234, v32, v33
v_cvt_pk_bf16_f32 v236, v36, v37
v_mfma_f32_16x16x32_bf16 v[44:47], v[192:195], a[68:71], v[44:47]
ds_read_b64_tr_b16 v[192:193], v240 offset:50816
ds_read_b64_tr_b16 v[194:195], v240 offset:51840
v_mfma_f32_16x16x32_bf16 v[12:15], v[196:199], a[72:75], v[12:15]
v_cvt_pk_bf16_f32 v235, v34, v35
v_cvt_pk_bf16_f32 v237, v38, v39
v_mfma_f32_16x16x32_bf16 v[44:47], v[196:199], a[76:79], v[44:47]
ds_read_b64_tr_b16 v[196:197], v240 offset:59264
ds_read_b64_tr_b16 v[198:199], v240 offset:60288
v_mfma_f32_16x16x32_bf16 v[12:15], v[200:203], a[80:83], v[12:15]
v_permlane16_swap_b32 v234, v236
v_permlane16_swap_b32 v235, v237
v_mfma_f32_16x16x32_bf16 v[44:47], v[200:203], a[84:87], v[44:47]
ds_read_b64_tr_b16 v[200:201], v240 offset:128
ds_read_b64_tr_b16 v[202:203], v240 offset:1152
v_mfma_f32_16x16x32_bf16 v[12:15], v[204:207], a[88:91], v[12:15]
s_waitcnt lgkmcnt(6)
v_xor_b32 v250, s3, v250
v_mfma_f32_16x16x32_bf16 v[44:47], v[204:207], a[92:95], v[44:47]
ds_read_b64_tr_b16 v[204:205], v240 offset:8576
ds_read_b64_tr_b16 v[206:207], v240 offset:9600
v_mfma_f32_16x16x32_bf16 v[12:15], v[208:211], a[8:11], v[12:15]
buffer_store_dwordx4 v[234:237], v238, s[32:35], s95 offen offset:0 sc1
v_mfma_f32_16x16x32_bf16 v[44:47], v[208:211], a[12:15], v[44:47]
ds_read_b64_tr_b16 v[208:209], v240 offset:17024
ds_read_b64_tr_b16 v[210:211], v240 offset:18048
v_mfma_f32_16x16x32_bf16 v[12:15], v[212:215], a[16:19], v[12:15]
v_bfe_u32 v251, v255, 4, 2
v_lshl_add_u32 v250, v250, 2, v251
v_mfma_f32_16x16x32_bf16 v[44:47], v[212:215], a[20:23], v[44:47]
ds_read_b64_tr_b16 v[212:213], v240 offset:25472
ds_read_b64_tr_b16 v[214:215], v240 offset:26496
v_mfma_f32_16x16x32_bf16 v[12:15], v[216:219], a[24:27], v[12:15]
buffer_load_dwordx4 v[136:139], v224, s[68:71], 0 offen offset:0
v_mfma_f32_16x16x32_bf16 v[44:47], v[216:219], a[28:31], v[44:47]
ds_read_b64_tr_b16 v[216:217], v240 offset:33920
ds_read_b64_tr_b16 v[218:219], v240 offset:34944
v_mfma_f32_16x16x32_bf16 v[12:15], v[220:223], a[0:3], v[12:15]
s_waitcnt lgkmcnt(6)
v_lshlrev_b32 v250, 9, v250
v_mfma_f32_16x16x32_bf16 v[44:47], v[220:223], a[32:35], v[44:47]
ds_read_b64_tr_b16 v[220:221], v240 offset:42368
ds_read_b64_tr_b16 v[222:223], v240 offset:43392
v_mfma_f32_16x16x32_bf16 v[16:19], v[192:195], a[64:67], v[16:19]
v_cvt_pk_bf16_f32 v230, v8, v9
v_cvt_pk_bf16_f32 v232, v12, v13
v_mfma_f32_16x16x32_bf16 v[48:51], v[192:195], a[68:71], v[48:51]
ds_read_b64_tr_b16 v[192:193], v241 offset:50816
ds_read_b64_tr_b16 v[194:195], v241 offset:51840
v_mfma_f32_16x16x32_bf16 v[16:19], v[196:199], a[72:75], v[16:19]
v_cvt_pk_bf16_f32 v231, v10, v11
v_cvt_pk_bf16_f32 v233, v14, v15
v_mfma_f32_16x16x32_bf16 v[48:51], v[196:199], a[76:79], v[48:51]
ds_read_b64_tr_b16 v[196:197], v241 offset:59264
ds_read_b64_tr_b16 v[198:199], v241 offset:60288
v_mfma_f32_16x16x32_bf16 v[16:19], v[200:203], a[80:83], v[16:19]
v_permlane16_swap_b32 v230, v232
v_permlane16_swap_b32 v231, v233
v_mfma_f32_16x16x32_bf16 v[48:51], v[200:203], a[84:87], v[48:51]
ds_read_b64_tr_b16 v[200:201], v241 offset:128
ds_read_b64_tr_b16 v[202:203], v241 offset:1152
v_mfma_f32_16x16x32_bf16 v[16:19], v[204:207], a[88:91], v[16:19]
s_waitcnt lgkmcnt(6)
v_and_b32 v248, 0x1f0, v255
v_mfma_f32_16x16x32_bf16 v[48:51], v[204:207], a[92:95], v[48:51]
ds_read_b64_tr_b16 v[204:205], v241 offset:8576
ds_read_b64_tr_b16 v[206:207], v241 offset:9600
v_mfma_f32_16x16x32_bf16 v[16:19], v[208:211], a[8:11], v[16:19]
buffer_store_dwordx4 v[230:233], v238, s[32:35], s94 offen offset:1024 sc1
v_mfma_f32_16x16x32_bf16 v[48:51], v[208:211], a[12:15], v[48:51]
ds_read_b64_tr_b16 v[208:209], v241 offset:17024
ds_read_b64_tr_b16 v[210:211], v241 offset:18048
v_mfma_f32_16x16x32_bf16 v[16:19], v[212:215], a[16:19], v[16:19]
v_cvt_pk_bf16_f32 v234, v40, v41
v_cvt_pk_bf16_f32 v236, v44, v45
v_mfma_f32_16x16x32_bf16 v[48:51], v[212:215], a[20:23], v[48:51]
ds_read_b64_tr_b16 v[212:213], v241 offset:25472
ds_read_b64_tr_b16 v[214:215], v241 offset:26496
v_mfma_f32_16x16x32_bf16 v[16:19], v[216:219], a[24:27], v[16:19]
v_cvt_pk_bf16_f32 v235, v42, v43
v_cvt_pk_bf16_f32 v237, v46, v47
v_mfma_f32_16x16x32_bf16 v[48:51], v[216:219], a[28:31], v[48:51]
ds_read_b64_tr_b16 v[216:217], v241 offset:33920
ds_read_b64_tr_b16 v[218:219], v241 offset:34944
v_mfma_f32_16x16x32_bf16 v[16:19], v[220:223], a[0:3], v[16:19]
s_waitcnt lgkmcnt(6)
v_lshrrev_b32 v248, 1, v248
v_mfma_f32_16x16x32_bf16 v[48:51], v[220:223], a[32:35], v[48:51]
ds_read_b64_tr_b16 v[220:221], v241 offset:42368
ds_read_b64_tr_b16 v[222:223], v241 offset:43392
v_mfma_f32_16x16x32_bf16 v[20:23], v[192:195], a[64:67], v[20:23]
v_permlane16_swap_b32 v234, v236
v_permlane16_swap_b32 v235, v237
v_mfma_f32_16x16x32_bf16 v[52:55], v[192:195], a[68:71], v[52:55]
ds_read_b64_tr_b16 v[192:193], v242 offset:50816
ds_read_b64_tr_b16 v[194:195], v242 offset:51840
v_mfma_f32_16x16x32_bf16 v[20:23], v[196:199], a[72:75], v[20:23]
v_mfma_f32_16x16x32_bf16 v[52:55], v[196:199], a[76:79], v[52:55]
ds_read_b64_tr_b16 v[196:197], v242 offset:59264
ds_read_b64_tr_b16 v[198:199], v242 offset:60288
v_mfma_f32_16x16x32_bf16 v[20:23], v[200:203], a[80:83], v[20:23]
buffer_store_dwordx4 v[234:237], v238, s[32:35], s95 offen offset:1024 sc1
v_mfma_f32_16x16x32_bf16 v[52:55], v[200:203], a[84:87], v[52:55]
ds_read_b64_tr_b16 v[200:201], v242 offset:128
ds_read_b64_tr_b16 v[202:203], v242 offset:1152
v_mfma_f32_16x16x32_bf16 v[20:23], v[204:207], a[88:91], v[20:23]
s_waitcnt lgkmcnt(6)
v_mfma_f32_16x16x32_bf16 v[52:55], v[204:207], a[92:95], v[52:55]
ds_read_b64_tr_b16 v[204:205], v242 offset:8576
ds_read_b64_tr_b16 v[206:207], v242 offset:9600
v_mfma_f32_16x16x32_bf16 v[20:23], v[208:211], a[8:11], v[20:23]
buffer_load_dwordx4 v[140:143], v225, s[48:51], 0 offen offset:0
v_mfma_f32_16x16x32_bf16 v[52:55], v[208:211], a[12:15], v[52:55]
ds_read_b64_tr_b16 v[208:209], v242 offset:17024
ds_read_b64_tr_b16 v[210:211], v242 offset:18048
v_mfma_f32_16x16x32_bf16 v[20:23], v[212:215], a[16:19], v[20:23]
v_mfma_f32_16x16x32_bf16 v[52:55], v[212:215], a[20:23], v[52:55]
ds_read_b64_tr_b16 v[212:213], v242 offset:25472
ds_read_b64_tr_b16 v[214:215], v242 offset:26496
v_mfma_f32_16x16x32_bf16 v[20:23], v[216:219], a[24:27], v[20:23]
buffer_load_dwordx4 v[152:155], v224, s[68:71], 0 offen offset:512
v_mfma_f32_16x16x32_bf16 v[52:55], v[216:219], a[28:31], v[52:55]
ds_read_b64_tr_b16 v[216:217], v242 offset:33920
ds_read_b64_tr_b16 v[218:219], v242 offset:34944
v_mfma_f32_16x16x32_bf16 v[20:23], v[220:223], a[0:3], v[20:23]
s_waitcnt lgkmcnt(6)
s_mov_b32 s93, 0
buffer_load_dwordx4 a[224:227], v229, s[28:31], s93 offen offset:0
v_mfma_f32_16x16x32_bf16 v[52:55], v[220:223], a[32:35], v[52:55]
ds_read_b64_tr_b16 v[220:221], v242 offset:42368
ds_read_b64_tr_b16 v[222:223], v242 offset:43392
v_mfma_f32_16x16x32_bf16 v[24:27], v[192:195], a[64:67], v[24:27]
v_cvt_pk_bf16_f32 v230, v16, v17
v_cvt_pk_bf16_f32 v232, v20, v21
v_mfma_f32_16x16x32_bf16 v[56:59], v[192:195], a[68:71], v[56:59]
ds_read_b64_tr_b16 v[192:193], v243 offset:50816
ds_read_b64_tr_b16 v[194:195], v243 offset:51840
v_mfma_f32_16x16x32_bf16 v[24:27], v[196:199], a[72:75], v[24:27]
v_cvt_pk_bf16_f32 v231, v18, v19
v_cvt_pk_bf16_f32 v233, v22, v23
v_mfma_f32_16x16x32_bf16 v[56:59], v[196:199], a[76:79], v[56:59]
ds_read_b64_tr_b16 v[196:197], v243 offset:59264
ds_read_b64_tr_b16 v[198:199], v243 offset:60288
v_mfma_f32_16x16x32_bf16 v[24:27], v[200:203], a[80:83], v[24:27]
v_permlane16_swap_b32 v230, v232
v_permlane16_swap_b32 v231, v233
v_mfma_f32_16x16x32_bf16 v[56:59], v[200:203], a[84:87], v[56:59]
ds_read_b64_tr_b16 v[200:201], v243 offset:128
ds_read_b64_tr_b16 v[202:203], v243 offset:1152
v_mfma_f32_16x16x32_bf16 v[24:27], v[204:207], a[88:91], v[24:27]
s_waitcnt lgkmcnt(6)
buffer_load_dwordx4 a[228:231], v229, s[28:31], s93 offen offset:2048
v_mfma_f32_16x16x32_bf16 v[56:59], v[204:207], a[92:95], v[56:59]
ds_read_b64_tr_b16 v[204:205], v243 offset:8576
ds_read_b64_tr_b16 v[206:207], v243 offset:9600
v_mfma_f32_16x16x32_bf16 v[24:27], v[208:211], a[8:11], v[24:27]
buffer_store_dwordx4 v[230:233], v238, s[32:35], s94 offen offset:2048 sc1
v_mfma_f32_16x16x32_bf16 v[56:59], v[208:211], a[12:15], v[56:59]
ds_read_b64_tr_b16 v[208:209], v243 offset:17024
ds_read_b64_tr_b16 v[210:211], v243 offset:18048
v_mfma_f32_16x16x32_bf16 v[24:27], v[212:215], a[16:19], v[24:27]
v_cvt_pk_bf16_f32 v234, v48, v49
v_cvt_pk_bf16_f32 v236, v52, v53
v_mfma_f32_16x16x32_bf16 v[56:59], v[212:215], a[20:23], v[56:59]
ds_read_b64_tr_b16 v[212:213], v243 offset:25472
ds_read_b64_tr_b16 v[214:215], v243 offset:26496
v_mfma_f32_16x16x32_bf16 v[24:27], v[216:219], a[24:27], v[24:27]
v_cvt_pk_bf16_f32 v235, v50, v51
v_cvt_pk_bf16_f32 v237, v54, v55
v_mfma_f32_16x16x32_bf16 v[56:59], v[216:219], a[28:31], v[56:59]
ds_read_b64_tr_b16 v[216:217], v243 offset:33920
ds_read_b64_tr_b16 v[218:219], v243 offset:34944
v_mfma_f32_16x16x32_bf16 v[24:27], v[220:223], a[0:3], v[24:27]
s_waitcnt lgkmcnt(6)
s_mov_b32 s93, 4096
buffer_load_dwordx4 a[232:235], v229, s[28:31], s93 offen offset:0
v_mfma_f32_16x16x32_bf16 v[56:59], v[220:223], a[32:35], v[56:59]
ds_read_b64_tr_b16 v[220:221], v243 offset:42368
ds_read_b64_tr_b16 v[222:223], v243 offset:43392
v_mfma_f32_16x16x32_bf16 v[28:31], v[192:195], a[64:67], v[28:31]
v_permlane16_swap_b32 v234, v236
v_permlane16_swap_b32 v235, v237
v_mfma_f32_16x16x32_bf16 v[60:63], v[192:195], a[68:71], v[60:63]
ds_read_b64_tr_b16 v[192:193], v240 offset:50688
ds_read_b64_tr_b16 v[194:195], v240 offset:51712
v_mfma_f32_16x16x32_bf16 v[28:31], v[196:199], a[72:75], v[28:31]
buffer_load_dwordx4 a[236:239], v229, s[28:31], s93 offen offset:2048
v_mfma_f32_16x16x32_bf16 v[60:63], v[196:199], a[76:79], v[60:63]
ds_read_b64_tr_b16 v[196:197], v240 offset:59136
ds_read_b64_tr_b16 v[198:199], v240 offset:60160
v_mfma_f32_16x16x32_bf16 v[28:31], v[200:203], a[80:83], v[28:31]
buffer_store_dwordx4 v[234:237], v238, s[32:35], s95 offen offset:2048 sc1
v_mfma_f32_16x16x32_bf16 v[60:63], v[200:203], a[84:87], v[60:63]
ds_read_b64_tr_b16 v[200:201], v240 offset:0
ds_read_b64_tr_b16 v[202:203], v240 offset:1024
v_mfma_f32_16x16x32_bf16 v[28:31], v[204:207], a[88:91], v[28:31]
s_waitcnt lgkmcnt(6)
s_mov_b32 s93, 8192
buffer_load_dwordx4 a[240:243], v229, s[28:31], s93 offen offset:0
v_mfma_f32_16x16x32_bf16 v[60:63], v[204:207], a[92:95], v[60:63]
ds_read_b64_tr_b16 v[204:205], v240 offset:8448
ds_read_b64_tr_b16 v[206:207], v240 offset:9472
v_mfma_f32_16x16x32_bf16 v[28:31], v[208:211], a[8:11], v[28:31]
buffer_load_dwordx4 v[156:159], v225, s[48:51], 0 offen offset:512
v_mfma_f32_16x16x32_bf16 v[60:63], v[208:211], a[12:15], v[60:63]
ds_read_b64_tr_b16 v[208:209], v240 offset:16896
ds_read_b64_tr_b16 v[210:211], v240 offset:17920
v_mfma_f32_16x16x32_bf16 v[28:31], v[212:215], a[16:19], v[28:31]
buffer_load_dwordx4 a[244:247], v229, s[28:31], s93 offen offset:2048
v_mfma_f32_16x16x32_bf16 v[60:63], v[212:215], a[20:23], v[60:63]
ds_read_b64_tr_b16 v[212:213], v240 offset:25344
ds_read_b64_tr_b16 v[214:215], v240 offset:26368
v_mfma_f32_16x16x32_bf16 v[28:31], v[216:219], a[24:27], v[28:31]
buffer_load_dwordx4 v[168:171], v224, s[68:71], 0 offen offset:1024
v_mfma_f32_16x16x32_bf16 v[60:63], v[216:219], a[28:31], v[60:63]
ds_read_b64_tr_b16 v[216:217], v240 offset:33792
ds_read_b64_tr_b16 v[218:219], v240 offset:34816
v_mfma_f32_16x16x32_bf16 v[28:31], v[220:223], a[0:3], v[28:31]
s_waitcnt vmcnt(25) lgkmcnt(6)
s_mov_b32 s93, 12288
buffer_load_dwordx4 a[248:251], v229, s[28:31], s93 offen offset:0
v_mfma_f32_16x16x32_bf16 v[60:63], v[220:223], a[32:35], v[60:63]
ds_read_b64_tr_b16 v[220:221], v240 offset:42240
ds_read_b64_tr_b16 v[222:223], v240 offset:43264
v_mfma_f32_16x16x32_bf16 v[64:67], v[192:195], a[4:7], v[64:67]
v_cvt_pk_bf16_f32 v230, v24, v25
v_cvt_pk_bf16_f32 v232, v28, v29
v_mfma_f32_16x16x32_bf16 v[96:99], v[192:195], a[36:39], v[96:99]
ds_read_b64_tr_b16 v[192:193], v241 offset:50688
ds_read_b64_tr_b16 v[194:195], v241 offset:51712
v_mfma_f32_16x16x32_bf16 v[64:67], v[196:199], a[40:43], v[64:67]
v_cvt_pk_bf16_f32 v231, v26, v27
v_cvt_pk_bf16_f32 v233, v30, v31
v_mfma_f32_16x16x32_bf16 v[96:99], v[196:199], a[44:47], v[96:99]
ds_read_b64_tr_b16 v[196:197], v241 offset:59136
ds_read_b64_tr_b16 v[198:199], v241 offset:60160
v_mfma_f32_16x16x32_bf16 v[64:67], v[200:203], a[48:51], v[64:67]
v_permlane16_swap_b32 v230, v232
v_permlane16_swap_b32 v231, v233
v_mfma_f32_16x16x32_bf16 v[96:99], v[200:203], a[52:55], v[96:99]
ds_read_b64_tr_b16 v[200:201], v241 offset:0
ds_read_b64_tr_b16 v[202:203], v241 offset:1024
v_mfma_f32_16x16x32_bf16 v[64:67], v[204:207], a[56:59], v[64:67]
s_waitcnt vmcnt(18) lgkmcnt(6)
buffer_load_dwordx4 a[252:255], v229, s[28:31], s93 offen offset:2048
v_mfma_f32_16x16x32_bf16 v[96:99], v[204:207], a[60:63], v[96:99]
ds_read_b64_tr_b16 v[204:205], v241 offset:8448
ds_read_b64_tr_b16 v[206:207], v241 offset:9472
v_mfma_f32_16x16x32_bf16 v[64:67], v[208:211], v[128:131], v[64:67]
buffer_store_dwordx4 v[230:233], v238, s[32:35], s94 offen offset:3072 sc1
v_mfma_f32_16x16x32_bf16 v[96:99], v[208:211], v[132:135], v[96:99]
ds_read_b64_tr_b16 v[208:209], v241 offset:16896
ds_read_b64_tr_b16 v[210:211], v241 offset:17920
v_mfma_f32_16x16x32_bf16 v[64:67], v[212:215], v[144:147], v[64:67]
v_cvt_pk_bf16_f32 v234, v56, v57
v_cvt_pk_bf16_f32 v236, v60, v61
v_mfma_f32_16x16x32_bf16 v[96:99], v[212:215], v[148:151], v[96:99]
ds_read_b64_tr_b16 v[212:213], v241 offset:25344
ds_read_b64_tr_b16 v[214:215], v241 offset:26368
v_mfma_f32_16x16x32_bf16 v[64:67], v[216:219], v[160:163], v[64:67]
v_cvt_pk_bf16_f32 v235, v58, v59
v_cvt_pk_bf16_f32 v237, v62, v63
v_mfma_f32_16x16x32_bf16 v[96:99], v[216:219], v[164:167], v[96:99]
ds_read_b64_tr_b16 v[216:217], v241 offset:33792
ds_read_b64_tr_b16 v[218:219], v241 offset:34816
v_mfma_f32_16x16x32_bf16 v[64:67], v[220:223], v[176:179], v[64:67]
s_waitcnt lgkmcnt(6)
s_mov_b32 s93, 0
buffer_load_dwordx4 a[64:67], v229, s[28:31], s93 offen offset:256
v_mfma_f32_16x16x32_bf16 v[96:99], v[220:223], v[180:183], v[96:99]
ds_read_b64_tr_b16 v[220:221], v241 offset:42240
ds_read_b64_tr_b16 v[222:223], v241 offset:43264
v_mfma_f32_16x16x32_bf16 v[68:71], v[192:195], a[4:7], v[68:71]
v_permlane16_swap_b32 v234, v236
v_permlane16_swap_b32 v235, v237
v_mfma_f32_16x16x32_bf16 v[100:103], v[192:195], a[36:39], v[100:103]
ds_read_b64_tr_b16 v[192:193], v242 offset:50688
ds_read_b64_tr_b16 v[194:195], v242 offset:51712
v_mfma_f32_16x16x32_bf16 v[68:71], v[196:199], a[40:43], v[68:71]
buffer_load_dwordx4 a[68:71], v229, s[28:31], s93 offen offset:2304
v_mfma_f32_16x16x32_bf16 v[100:103], v[196:199], a[44:47], v[100:103]
ds_read_b64_tr_b16 v[196:197], v242 offset:59136
ds_read_b64_tr_b16 v[198:199], v242 offset:60160
v_mfma_f32_16x16x32_bf16 v[68:71], v[200:203], a[48:51], v[68:71]
buffer_store_dwordx4 v[234:237], v238, s[32:35], s95 offen offset:3072 sc1
v_mfma_f32_16x16x32_bf16 v[100:103], v[200:203], a[52:55], v[100:103]
ds_read_b64_tr_b16 v[200:201], v242 offset:0
ds_read_b64_tr_b16 v[202:203], v242 offset:1024
v_mfma_f32_16x16x32_bf16 v[68:71], v[204:207], a[56:59], v[68:71]
s_waitcnt lgkmcnt(6)
s_mov_b32 s93, 4096
buffer_load_dwordx4 a[72:75], v229, s[28:31], s93 offen offset:256
v_mfma_f32_16x16x32_bf16 v[100:103], v[204:207], a[60:63], v[100:103]
ds_read_b64_tr_b16 v[204:205], v242 offset:8448
ds_read_b64_tr_b16 v[206:207], v242 offset:9472
v_mfma_f32_16x16x32_bf16 v[68:71], v[208:211], v[128:131], v[68:71]
buffer_load_dwordx4 v[172:175], v225, s[48:51], 0 offen offset:1024
v_mfma_f32_16x16x32_bf16 v[100:103], v[208:211], v[132:135], v[100:103]
ds_read_b64_tr_b16 v[208:209], v242 offset:16896
ds_read_b64_tr_b16 v[210:211], v242 offset:17920
v_mfma_f32_16x16x32_bf16 v[68:71], v[212:215], v[144:147], v[68:71]
buffer_load_dwordx4 a[76:79], v229, s[28:31], s93 offen offset:2304
v_mfma_f32_16x16x32_bf16 v[100:103], v[212:215], v[148:151], v[100:103]
ds_read_b64_tr_b16 v[212:213], v242 offset:25344
ds_read_b64_tr_b16 v[214:215], v242 offset:26368
v_mfma_f32_16x16x32_bf16 v[68:71], v[216:219], v[160:163], v[68:71]
buffer_load_dwordx4 v[184:187], v224, s[68:71], 0 offen offset:1536
v_mfma_f32_16x16x32_bf16 v[100:103], v[216:219], v[164:167], v[100:103]
ds_read_b64_tr_b16 v[216:217], v242 offset:33792
ds_read_b64_tr_b16 v[218:219], v242 offset:34816
v_mfma_f32_16x16x32_bf16 v[68:71], v[220:223], v[176:179], v[68:71]
s_waitcnt lgkmcnt(6)
s_mov_b32 s93, 8192
buffer_load_dwordx4 a[80:83], v229, s[28:31], s93 offen offset:256
v_mfma_f32_16x16x32_bf16 v[100:103], v[220:223], v[180:183], v[100:103]
ds_read_b64_tr_b16 v[220:221], v242 offset:42240
ds_read_b64_tr_b16 v[222:223], v242 offset:43264
v_mfma_f32_16x16x32_bf16 v[72:75], v[192:195], a[4:7], v[72:75]
v_cvt_pk_bf16_f32 v230, v64, v65
v_cvt_pk_bf16_f32 v232, v68, v69
v_mfma_f32_16x16x32_bf16 v[104:107], v[192:195], a[36:39], v[104:107]
ds_read_b64_tr_b16 v[192:193], v243 offset:50688
ds_read_b64_tr_b16 v[194:195], v243 offset:51712
v_mfma_f32_16x16x32_bf16 v[72:75], v[196:199], a[40:43], v[72:75]
v_cvt_pk_bf16_f32 v231, v66, v67
v_cvt_pk_bf16_f32 v233, v70, v71
v_mfma_f32_16x16x32_bf16 v[104:107], v[196:199], a[44:47], v[104:107]
ds_read_b64_tr_b16 v[196:197], v243 offset:59136
ds_read_b64_tr_b16 v[198:199], v243 offset:60160
v_mfma_f32_16x16x32_bf16 v[72:75], v[200:203], a[48:51], v[72:75]
v_permlane16_swap_b32 v230, v232
v_permlane16_swap_b32 v231, v233
v_mfma_f32_16x16x32_bf16 v[104:107], v[200:203], a[52:55], v[104:107]
ds_read_b64_tr_b16 v[200:201], v243 offset:0
ds_read_b64_tr_b16 v[202:203], v243 offset:1024
v_mfma_f32_16x16x32_bf16 v[72:75], v[204:207], a[56:59], v[72:75]
s_waitcnt lgkmcnt(6)
buffer_load_dwordx4 a[84:87], v229, s[28:31], s93 offen offset:2304
v_mfma_f32_16x16x32_bf16 v[104:107], v[204:207], a[60:63], v[104:107]
ds_read_b64_tr_b16 v[204:205], v243 offset:8448
ds_read_b64_tr_b16 v[206:207], v243 offset:9472
v_mfma_f32_16x16x32_bf16 v[72:75], v[208:211], v[128:131], v[72:75]
buffer_store_dwordx4 v[230:233], v238, s[32:35], s96 offen offset:0 sc1
v_mfma_f32_16x16x32_bf16 v[104:107], v[208:211], v[132:135], v[104:107]
ds_read_b64_tr_b16 v[208:209], v243 offset:16896
ds_read_b64_tr_b16 v[210:211], v243 offset:17920
v_mfma_f32_16x16x32_bf16 v[72:75], v[212:215], v[144:147], v[72:75]
v_cvt_pk_bf16_f32 v234, v96, v97
v_cvt_pk_bf16_f32 v236, v100, v101
v_mfma_f32_16x16x32_bf16 v[104:107], v[212:215], v[148:151], v[104:107]
ds_read_b64_tr_b16 v[212:213], v243 offset:25344
ds_read_b64_tr_b16 v[214:215], v243 offset:26368
v_mfma_f32_16x16x32_bf16 v[72:75], v[216:219], v[160:163], v[72:75]
v_cvt_pk_bf16_f32 v235, v98, v99
v_cvt_pk_bf16_f32 v237, v102, v103
v_mfma_f32_16x16x32_bf16 v[104:107], v[216:219], v[164:167], v[104:107]
ds_read_b64_tr_b16 v[216:217], v243 offset:33792
ds_read_b64_tr_b16 v[218:219], v243 offset:34816
v_mfma_f32_16x16x32_bf16 v[72:75], v[220:223], v[176:179], v[72:75]
s_waitcnt lgkmcnt(6)
s_mov_b32 s93, 12288
buffer_load_dwordx4 a[88:91], v229, s[28:31], s93 offen offset:256
v_mfma_f32_16x16x32_bf16 v[104:107], v[220:223], v[180:183], v[104:107]
ds_read_b64_tr_b16 v[220:221], v243 offset:42240
ds_read_b64_tr_b16 v[222:223], v243 offset:43264
v_mfma_f32_16x16x32_bf16 v[76:79], v[192:195], a[4:7], v[76:79]
v_permlane16_swap_b32 v234, v236
v_permlane16_swap_b32 v235, v237
v_mfma_f32_16x16x32_bf16 v[108:111], v[192:195], a[36:39], v[108:111]
ds_read_b64_tr_b16 v[192:193], v240 offset:50816
ds_read_b64_tr_b16 v[194:195], v240 offset:51840
v_mfma_f32_16x16x32_bf16 v[76:79], v[196:199], a[40:43], v[76:79]
buffer_load_dwordx4 a[92:95], v229, s[28:31], s93 offen offset:2304
v_mfma_f32_16x16x32_bf16 v[108:111], v[196:199], a[44:47], v[108:111]
ds_read_b64_tr_b16 v[196:197], v240 offset:59264
ds_read_b64_tr_b16 v[198:199], v240 offset:60288
v_mfma_f32_16x16x32_bf16 v[76:79], v[200:203], a[48:51], v[76:79]
buffer_store_dwordx4 v[234:237], v238, s[32:35], s97 offen offset:0 sc1
v_mfma_f32_16x16x32_bf16 v[108:111], v[200:203], a[52:55], v[108:111]
ds_read_b64_tr_b16 v[200:201], v240 offset:128
ds_read_b64_tr_b16 v[202:203], v240 offset:1152
v_mfma_f32_16x16x32_bf16 v[76:79], v[204:207], a[56:59], v[76:79]
s_waitcnt lgkmcnt(6)
v_mfma_f32_16x16x32_bf16 v[108:111], v[204:207], a[60:63], v[108:111]
ds_read_b64_tr_b16 v[204:205], v240 offset:8576
ds_read_b64_tr_b16 v[206:207], v240 offset:9600
v_mfma_f32_16x16x32_bf16 v[76:79], v[208:211], v[128:131], v[76:79]
buffer_load_dwordx4 v[188:191], v225, s[48:51], 0 offen offset:1536
v_mfma_f32_16x16x32_bf16 v[108:111], v[208:211], v[132:135], v[108:111]
ds_read_b64_tr_b16 v[208:209], v240 offset:17024
ds_read_b64_tr_b16 v[210:211], v240 offset:18048
v_mfma_f32_16x16x32_bf16 v[76:79], v[212:215], v[144:147], v[76:79]
v_mfma_f32_16x16x32_bf16 v[108:111], v[212:215], v[148:151], v[108:111]
ds_read_b64_tr_b16 v[212:213], v240 offset:25472
ds_read_b64_tr_b16 v[214:215], v240 offset:26496
v_mfma_f32_16x16x32_bf16 v[76:79], v[216:219], v[160:163], v[76:79]
v_mfma_f32_16x16x32_bf16 v[108:111], v[216:219], v[164:167], v[108:111]
ds_read_b64_tr_b16 v[216:217], v240 offset:33920
ds_read_b64_tr_b16 v[218:219], v240 offset:34944
v_mfma_f32_16x16x32_bf16 v[76:79], v[220:223], v[176:179], v[76:79]
s_waitcnt lgkmcnt(6)
v_mfma_f32_16x16x32_bf16 v[108:111], v[220:223], v[180:183], v[108:111]
ds_read_b64_tr_b16 v[220:221], v240 offset:42368
ds_read_b64_tr_b16 v[222:223], v240 offset:43392
v_mfma_f32_16x16x32_bf16 v[80:83], v[192:195], a[4:7], v[80:83]
v_cvt_pk_bf16_f32 v230, v72, v73
v_cvt_pk_bf16_f32 v232, v76, v77
v_mfma_f32_16x16x32_bf16 v[112:115], v[192:195], a[36:39], v[112:115]
ds_read_b64_tr_b16 v[192:193], v241 offset:50816
ds_read_b64_tr_b16 v[194:195], v241 offset:51840
v_mfma_f32_16x16x32_bf16 v[80:83], v[196:199], a[40:43], v[80:83]
v_cvt_pk_bf16_f32 v231, v74, v75
v_cvt_pk_bf16_f32 v233, v78, v79
v_mfma_f32_16x16x32_bf16 v[112:115], v[196:199], a[44:47], v[112:115]
ds_read_b64_tr_b16 v[196:197], v241 offset:59264
ds_read_b64_tr_b16 v[198:199], v241 offset:60288
v_mfma_f32_16x16x32_bf16 v[80:83], v[200:203], a[48:51], v[80:83]
v_permlane16_swap_b32 v230, v232
v_permlane16_swap_b32 v231, v233
v_mfma_f32_16x16x32_bf16 v[112:115], v[200:203], a[52:55], v[112:115]
ds_read_b64_tr_b16 v[200:201], v241 offset:128
ds_read_b64_tr_b16 v[202:203], v241 offset:1152
v_mfma_f32_16x16x32_bf16 v[80:83], v[204:207], a[56:59], v[80:83]
s_waitcnt lgkmcnt(6)
v_mfma_f32_16x16x32_bf16 v[112:115], v[204:207], a[60:63], v[112:115]
ds_read_b64_tr_b16 v[204:205], v241 offset:8576
ds_read_b64_tr_b16 v[206:207], v241 offset:9600
v_mfma_f32_16x16x32_bf16 v[80:83], v[208:211], v[128:131], v[80:83]
buffer_store_dwordx4 v[230:233], v238, s[32:35], s96 offen offset:1024 sc1
v_mfma_f32_16x16x32_bf16 v[112:115], v[208:211], v[132:135], v[112:115]
ds_read_b64_tr_b16 v[208:209], v241 offset:17024
.La5_LW3_4:
s_cbranch_execz .La5_LW3_R0
ds_read_b64_tr_b16 v[210:211], v241 offset:18048
v_mfma_f32_16x16x32_bf16 v[80:83], v[212:215], v[144:147], v[80:83]
v_cvt_pk_bf16_f32 v234, v104, v105
v_cvt_pk_bf16_f32 v236, v108, v109
v_mfma_f32_16x16x32_bf16 v[112:115], v[212:215], v[148:151], v[112:115]
ds_read_b64_tr_b16 v[212:213], v241 offset:25472
ds_read_b64_tr_b16 v[214:215], v241 offset:26496
v_mfma_f32_16x16x32_bf16 v[80:83], v[216:219], v[160:163], v[80:83]
v_cvt_pk_bf16_f32 v235, v106, v107
v_cvt_pk_bf16_f32 v237, v110, v111
v_mfma_f32_16x16x32_bf16 v[112:115], v[216:219], v[164:167], v[112:115]
ds_read_b64_tr_b16 v[216:217], v241 offset:33920
ds_read_b64_tr_b16 v[218:219], v241 offset:34944
v_mfma_f32_16x16x32_bf16 v[80:83], v[220:223], v[176:179], v[80:83]
s_waitcnt lgkmcnt(6)
v_mfma_f32_16x16x32_bf16 v[112:115], v[220:223], v[180:183], v[112:115]
ds_read_b64_tr_b16 v[220:221], v241 offset:42368
ds_read_b64_tr_b16 v[222:223], v241 offset:43392
v_mfma_f32_16x16x32_bf16 v[84:87], v[192:195], a[4:7], v[84:87]
v_permlane16_swap_b32 v234, v236
v_permlane16_swap_b32 v235, v237
v_mfma_f32_16x16x32_bf16 v[116:119], v[192:195], a[36:39], v[116:119]
ds_read_b64_tr_b16 v[192:193], v242 offset:50816
ds_read_b64_tr_b16 v[194:195], v242 offset:51840
v_mfma_f32_16x16x32_bf16 v[84:87], v[196:199], a[40:43], v[84:87]
v_mfma_f32_16x16x32_bf16 v[116:119], v[196:199], a[44:47], v[116:119]
ds_read_b64_tr_b16 v[196:197], v242 offset:59264
ds_read_b64_tr_b16 v[198:199], v242 offset:60288
v_mfma_f32_16x16x32_bf16 v[84:87], v[200:203], a[48:51], v[84:87]
buffer_store_dwordx4 v[234:237], v238, s[32:35], s97 offen offset:1024 sc1
v_mfma_f32_16x16x32_bf16 v[116:119], v[200:203], a[52:55], v[116:119]
ds_read_b64_tr_b16 v[200:201], v242 offset:128
ds_read_b64_tr_b16 v[202:203], v242 offset:1152
v_mfma_f32_16x16x32_bf16 v[84:87], v[204:207], a[56:59], v[84:87]
s_waitcnt lgkmcnt(6)
v_mfma_f32_16x16x32_bf16 v[116:119], v[204:207], a[60:63], v[116:119]
ds_read_b64_tr_b16 v[204:205], v242 offset:8576
ds_read_b64_tr_b16 v[206:207], v242 offset:9600
v_mfma_f32_16x16x32_bf16 v[84:87], v[208:211], v[128:131], v[84:87]
v_mfma_f32_16x16x32_bf16 v[116:119], v[208:211], v[132:135], v[116:119]
ds_read_b64_tr_b16 v[208:209], v242 offset:17024
ds_read_b64_tr_b16 v[210:211], v242 offset:18048
v_mfma_f32_16x16x32_bf16 v[84:87], v[212:215], v[144:147], v[84:87]
v_mfma_f32_16x16x32_bf16 v[116:119], v[212:215], v[148:151], v[116:119]
ds_read_b64_tr_b16 v[212:213], v242 offset:25472
ds_read_b64_tr_b16 v[214:215], v242 offset:26496
v_mfma_f32_16x16x32_bf16 v[84:87], v[216:219], v[160:163], v[84:87]
v_mfma_f32_16x16x32_bf16 v[116:119], v[216:219], v[164:167], v[116:119]
ds_read_b64_tr_b16 v[216:217], v242 offset:33920
ds_read_b64_tr_b16 v[218:219], v242 offset:34944
v_mfma_f32_16x16x32_bf16 v[84:87], v[220:223], v[176:179], v[84:87]
s_waitcnt lgkmcnt(6)
v_mfma_f32_16x16x32_bf16 v[116:119], v[220:223], v[180:183], v[116:119]
ds_read_b64_tr_b16 v[220:221], v242 offset:42368
ds_read_b64_tr_b16 v[222:223], v242 offset:43392
v_mfma_f32_16x16x32_bf16 v[88:91], v[192:195], a[4:7], v[88:91]
v_cvt_pk_bf16_f32 v230, v80, v81
v_cvt_pk_bf16_f32 v232, v84, v85
v_mfma_f32_16x16x32_bf16 v[120:123], v[192:195], a[36:39], v[120:123]
ds_read_b64_tr_b16 v[192:193], v243 offset:50816
ds_read_b64_tr_b16 v[194:195], v243 offset:51840
v_mfma_f32_16x16x32_bf16 v[88:91], v[196:199], a[40:43], v[88:91]
v_cvt_pk_bf16_f32 v231, v82, v83
v_cvt_pk_bf16_f32 v233, v86, v87
v_mfma_f32_16x16x32_bf16 v[120:123], v[196:199], a[44:47], v[120:123]
ds_read_b64_tr_b16 v[196:197], v243 offset:59264
ds_read_b64_tr_b16 v[198:199], v243 offset:60288
v_mfma_f32_16x16x32_bf16 v[88:91], v[200:203], a[48:51], v[88:91]
v_permlane16_swap_b32 v230, v232
v_permlane16_swap_b32 v231, v233
v_mfma_f32_16x16x32_bf16 v[120:123], v[200:203], a[52:55], v[120:123]
ds_read_b64_tr_b16 v[200:201], v243 offset:128
ds_read_b64_tr_b16 v[202:203], v243 offset:1152
v_mfma_f32_16x16x32_bf16 v[88:91], v[204:207], a[56:59], v[88:91]
s_waitcnt lgkmcnt(6)
v_mfma_f32_16x16x32_bf16 v[120:123], v[204:207], a[60:63], v[120:123]
ds_read_b64_tr_b16 v[204:205], v243 offset:8576
ds_read_b64_tr_b16 v[206:207], v243 offset:9600
v_mfma_f32_16x16x32_bf16 v[88:91], v[208:211], v[128:131], v[88:91]
buffer_store_dwordx4 v[230:233], v238, s[32:35], s96 offen offset:2048 sc1
v_mfma_f32_16x16x32_bf16 v[120:123], v[208:211], v[132:135], v[120:123]
ds_read_b64_tr_b16 v[208:209], v243 offset:17024
ds_read_b64_tr_b16 v[210:211], v243 offset:18048
v_mfma_f32_16x16x32_bf16 v[88:91], v[212:215], v[144:147], v[88:91]
v_cvt_pk_bf16_f32 v234, v112, v113
v_cvt_pk_bf16_f32 v236, v116, v117
v_mfma_f32_16x16x32_bf16 v[120:123], v[212:215], v[148:151], v[120:123]
ds_read_b64_tr_b16 v[212:213], v243 offset:25472
ds_read_b64_tr_b16 v[214:215], v243 offset:26496
v_mfma_f32_16x16x32_bf16 v[88:91], v[216:219], v[160:163], v[88:91]
v_cvt_pk_bf16_f32 v235, v114, v115
v_cvt_pk_bf16_f32 v237, v118, v119
v_mfma_f32_16x16x32_bf16 v[120:123], v[216:219], v[164:167], v[120:123]
ds_read_b64_tr_b16 v[216:217], v243 offset:33920
ds_read_b64_tr_b16 v[218:219], v243 offset:34944
v_mfma_f32_16x16x32_bf16 v[88:91], v[220:223], v[176:179], v[88:91]
s_waitcnt lgkmcnt(6)
v_mfma_f32_16x16x32_bf16 v[120:123], v[220:223], v[180:183], v[120:123]
ds_read_b64_tr_b16 v[220:221], v243 offset:42368
ds_read_b64_tr_b16 v[222:223], v243 offset:43392
v_mfma_f32_16x16x32_bf16 v[92:95], v[192:195], a[4:7], v[92:95]
v_permlane16_swap_b32 v234, v236
v_permlane16_swap_b32 v235, v237
v_mfma_f32_16x16x32_bf16 v[124:127], v[192:195], a[36:39], v[124:127]
v_mfma_f32_16x16x32_bf16 v[92:95], v[196:199], a[40:43], v[92:95]
v_mfma_f32_16x16x32_bf16 v[124:127], v[196:199], a[44:47], v[124:127]
v_mfma_f32_16x16x32_bf16 v[92:95], v[200:203], a[48:51], v[92:95]
buffer_store_dwordx4 v[234:237], v238, s[32:35], s97 offen offset:2048 sc1
v_mfma_f32_16x16x32_bf16 v[124:127], v[200:203], a[52:55], v[124:127]
v_mfma_f32_16x16x32_bf16 v[92:95], v[204:207], a[56:59], v[92:95]
s_waitcnt lgkmcnt(0)
v_mfma_f32_16x16x32_bf16 v[124:127], v[204:207], a[60:63], v[124:127]
v_mfma_f32_16x16x32_bf16 v[92:95], v[208:211], v[128:131], v[92:95]
v_mfma_f32_16x16x32_bf16 v[124:127], v[208:211], v[132:135], v[124:127]
v_mfma_f32_16x16x32_bf16 v[92:95], v[212:215], v[144:147], v[92:95]
v_mfma_f32_16x16x32_bf16 v[124:127], v[212:215], v[148:151], v[124:127]
v_mfma_f32_16x16x32_bf16 v[92:95], v[216:219], v[160:163], v[92:95]
v_mfma_f32_16x16x32_bf16 v[124:127], v[216:219], v[164:167], v[124:127]
v_mfma_f32_16x16x32_bf16 v[92:95], v[220:223], v[176:179], v[92:95]
v_mfma_f32_16x16x32_bf16 v[124:127], v[220:223], v[180:183], v[124:127]
s_nop 7
v_cvt_pk_bf16_f32 v230, v88, v89
v_cvt_pk_bf16_f32 v232, v92, v93
v_cvt_pk_bf16_f32 v231, v90, v91
v_cvt_pk_bf16_f32 v233, v94, v95
v_cvt_pk_bf16_f32 v234, v120, v121
v_cvt_pk_bf16_f32 v236, v124, v125
v_cvt_pk_bf16_f32 v235, v122, v123
v_cvt_pk_bf16_f32 v237, v126, v127
v_permlane16_swap_b32 v230, v232
v_permlane16_swap_b32 v231, v233
buffer_store_dwordx4 v[230:233], v238, s[32:35], s96 offen offset:3072 sc1
v_permlane16_swap_b32 v234, v236
v_permlane16_swap_b32 v235, v237
buffer_store_dwordx4 v[234:237], v238, s[32:35], s97 offen offset:3072 sc1
s_lshl_b32 s91, s76, 5
s_add_u32 s91, s91, s72
s_lshl_b32 s20, s91, 9
s_add_u32 s20, s20, 2097152
s_lshr_b32 s93, s91, 1
s_lshl_b32 s93, s93, 15
s_and_b32 s94, s91, 1
s_lshl2_add_u32 s94, s94, s3
s_lshl_b32 s94, s94, 7
s_add_u32 s97, s93, s94
s_add_u32 s32, s26, 71303168
s_addc_u32 s33, s27, 0
s_and_b32 s33, s33, 0xffff
s_mov_b32 s34, 16777216
s_mov_b32 s35, 0x20000
s_add_u32 s44, s26, 171966464
s_addc_u32 s45, s27, 0
s_and_b32 s45, s45, 0xffff
s_mov_b32 s46, 950272
s_mov_b32 s47, 0x20000
s_add_u32 s56, s38, 0
s_addc_u32 s57, s39, 0
s_and_b32 s57, s57, 0xffff
s_mov_b32 s58, 4194304
s_mov_b32 s59, 0x20000
s_add_u32 s68, s16, 0
s_addc_u32 s69, s17, 0
s_and_b32 s69, s69, 0xffff
s_mov_b32 s70, 4194304
s_mov_b32 s71, 0x20000
v_and_b32 v230, 0x70, v255
v_lshrrev_b32 v231, 7, v255
v_lshl_add_u32 v230, v231, 10, v230
v_add_u32 v240, s97, v230
v_add_u32 v241, 8192, v240
v_add_u32 v242, 16384, v240
v_add_u32 v243, 24576, v240
v_mov_b32 v237, 0x3f803f80
v_mov_b32 v236, 0
s_waitcnt vmcnt(6)
v_dot2_f32_bf16 v236, v140, v237, v236
v_dot2_f32_bf16 v236, v141, v237, v236
v_dot2_f32_bf16 v236, v142, v237, v236
v_dot2_f32_bf16 v236, v143, v237, v236
v_dot2_f32_bf16 v236, v156, v237, v236
v_dot2_f32_bf16 v236, v157, v237, v236
v_dot2_f32_bf16 v236, v158, v237, v236
v_dot2_f32_bf16 v236, v159, v237, v236
v_dot2_f32_bf16 v236, v172, v237, v236
v_dot2_f32_bf16 v236, v173, v237, v236
v_dot2_f32_bf16 v236, v174, v237, v236
v_dot2_f32_bf16 v236, v175, v237, v236
v_dot2_f32_bf16 v236, v188, v237, v236
v_dot2_f32_bf16 v236, v189, v237, v236
v_dot2_f32_bf16 v236, v190, v237, v236
v_dot2_f32_bf16 v236, v191, v237, v236
v_lshlrev_b32 v0, 16, v136
v_and_b32 v1, 0xffff0000, v136
v_lshlrev_b32 v2, 16, v137
v_and_b32 v3, 0xffff0000, v137
v_lshlrev_b32 v4, 16, v138
v_and_b32 v5, 0xffff0000, v138
v_lshlrev_b32 v6, 16, v139
v_and_b32 v7, 0xffff0000, v139
v_lshlrev_b32 v8, 16, v152
v_and_b32 v9, 0xffff0000, v152
v_lshlrev_b32 v10, 16, v153
v_and_b32 v11, 0xffff0000, v153
v_lshlrev_b32 v12, 16, v154
v_and_b32 v13, 0xffff0000, v154
v_lshlrev_b32 v14, 16, v155
v_and_b32 v15, 0xffff0000, v155
v_lshlrev_b32 v16, 16, v168
v_and_b32 v17, 0xffff0000, v168
v_lshlrev_b32 v18, 16, v169
v_and_b32 v19, 0xffff0000, v169
v_lshlrev_b32 v20, 16, v170
v_and_b32 v21, 0xffff0000, v170
v_lshlrev_b32 v22, 16, v171
v_and_b32 v23, 0xffff0000, v171
v_lshlrev_b32 v24, 16, v184
v_and_b32 v25, 0xffff0000, v184
v_lshlrev_b32 v26, 16, v185
v_and_b32 v27, 0xffff0000, v185
v_lshlrev_b32 v28, 16, v186
v_and_b32 v29, 0xffff0000, v186
v_lshlrev_b32 v30, 16, v187
v_and_b32 v31, 0xffff0000, v187
s_nop 1
v_add_f32_dpp v236, v236, v236 quad_perm:[1,0,3,2] row_mask:0xf bank_mask:0xf
s_nop 1
v_add_f32_dpp v236, v236, v236 quad_perm:[2,3,0,1] row_mask:0xf bank_mask:0xf
s_nop 1
v_add_f32_dpp v236, v236, v236 row_ror:8 row_mask:0xf bank_mask:0xf
v_mov_b32 v230, v236
s_nop 1
v_permlane16_swap_b32 v230, v236
s_nop 1
v_add_f32 v236, v236, v230
v_mov_b32 v230, v236
s_nop 1
v_permlane32_swap_b32 v230, v236
s_nop 1
v_add_f32 v236, v236, v230
s_mov_b64 exec, 0
s_cmp_eq_u32 s3, 0
s_cbranch_scc1 .La5_LW4_0
s_cmp_eq_u32 s3, 1
s_cbranch_scc1 .La5_LW4_1
s_cmp_eq_u32 s3, 2
s_cbranch_scc1 .La5_LW4_2
s_branch .La5_LW4_3
.La5_LW4_R0:
s_mov_b64 exec, -1
s_waitcnt vmcnt(0)
buffer_store_dwordx2 v[252:253], v250, s[64:67], s92 offen sc1
s_mov_b32 s85, 99
s_cmp_eq_u32 s30, 0
s_nop 0
s_nop 0
s_cbranch_scc1 .La5_LT_ROWS
; ---- h = x + attn, LayerNorm statistics, router score, two best of eight rows
.La5_LT_ROWS:
.La5_LW3_5:
s_cbranch_execz .La5_LW3_R0
s_mov_b32 s96, 0
.La5_LT_P3:
buffer_load_dwordx2 v[250:251], v248, s[64:67], s20 offen sc1
s_waitcnt vmcnt(0)
.La5_LT_P2:
v_cmp_eq_u64 vcc, v[250:251], v[252:253]
s_add_u32 s96, s96, 1
s_lshr_b32 s92, vcc_lo, 1
s_and_b32 s92, s92, vcc_lo
s_lshr_b32 s93, s92, 2
s_and_b32 s92, s92, s93
s_lshr_b32 s93, s92, 4
s_and_b32 s92, s92, s93
s_lshr_b32 s93, s96, 20
s_mul_i32 s93, s93, 0x01010101
s_or_b32 s92, s92, s93
s_andn2_b32 s92, 0x01010101, s92
s_bcnt1_i32_b32 s93, s92
s_cmp_gt_u32 s93, 1
s_nop 0
s_nop 0
s_cbranch_scc1 .La5_LT_P3
s_or_b32 s93, s92, 0x01000000
s_ff1_i32_b32 s93, s93
s_lshl_b32 s80, s93, 19
s_xor_b32 s93, s80, 8388608
s_xor_b32 s94, s80, 12582912
s_xor_b32 s95, s80, 4194304
buffer_load_dwordx4 v[208:211], v240, s[32:35], s93 offen sc1
buffer_load_dwordx4 v[212:215], v241, s[32:35], s93 offen sc1
buffer_load_dwordx4 v[216:219], v242, s[32:35], s93 offen sc1
buffer_load_dwordx4 v[220:223], v243, s[32:35], s93 offen sc1
buffer_load_dwordx4 v[112:115], v240, s[32:35], s94 offen sc1
buffer_load_dwordx4 v[116:119], v241, s[32:35], s94 offen sc1
buffer_load_dwordx4 v[120:123], v242, s[32:35], s94 offen sc1
buffer_load_dwordx4 v[124:127], v243, s[32:35], s94 offen sc1
buffer_load_dwordx4 v[96:99], v240, s[32:35], s95 offen sc1
buffer_load_dwordx4 v[100:103], v241, s[32:35], s95 offen sc1
buffer_load_dwordx4 v[104:107], v242, s[32:35], s95 offen sc1
buffer_load_dwordx4 v[108:111], v243, s[32:35], s95 offen sc1
s_cmp_lg_u32 s92, 0
s_cbranch_scc1 .La5_LT_P4
buffer_load_dwordx4 v[192:195], v240, s[32:35], s80 offen sc1
buffer_load_dwordx4 v[196:199], v241, s[32:35], s80 offen sc1
buffer_load_dwordx4 v[200:203], v242, s[32:35], s80 offen sc1
buffer_load_dwordx4 v[204:207], v243, s[32:35], s80 offen sc1
.La5_LT_P4:
buffer_load_dwordx2 v[250:251], v248, s[64:67], s20 offen sc1
s_waitcnt vmcnt(8)
v_lshlrev_b32 v32, 16, v208
v_and_b32 v33, 0xffff0000, v208
v_lshlrev_b32 v230, 16, v112
v_and_b32 v231, 0xffff0000, v112
v_pk_add_f32 v[32:33], v[32:33], v[230:231]
v_lshlrev_b32 v34, 16, v209
v_and_b32 v35, 0xffff0000, v209
v_lshlrev_b32 v230, 16, v113
v_and_b32 v231, 0xffff0000, v113
v_pk_add_f32 v[34:35], v[34:35], v[230:231]
v_lshlrev_b32 v36, 16, v210
v_and_b32 v37, 0xffff0000, v210
v_lshlrev_b32 v230, 16, v114
v_and_b32 v231, 0xffff0000, v114
v_pk_add_f32 v[36:37], v[36:37], v[230:231]
v_lshlrev_b32 v38, 16, v211
v_and_b32 v39, 0xffff0000, v211
v_lshlrev_b32 v230, 16, v115
v_and_b32 v231, 0xffff0000, v115
v_pk_add_f32 v[38:39], v[38:39], v[230:231]
s_waitcnt vmcnt(7)
v_lshlrev_b32 v40, 16, v212
v_and_b32 v41, 0xffff0000, v212
v_lshlrev_b32 v230, 16, v116
v_and_b32 v231, 0xffff0000, v116
v_pk_add_f32 v[40:41], v[40:41], v[230:231]
v_lshlrev_b32 v42, 16, v213
v_and_b32 v43, 0xffff0000, v213
v_lshlrev_b32 v230, 16, v117
v_and_b32 v231, 0xffff0000, v117
v_pk_add_f32 v[42:43], v[42:43], v[230:231]
v_lshlrev_b32 v44, 16, v214
v_and_b32 v45, 0xffff0000, v214
v_lshlrev_b32 v230, 16, v118
v_and_b32 v231, 0xffff0000, v118
v_pk_add_f32 v[44:45], v[44:45], v[230:231]
v_lshlrev_b32 v46, 16, v215
v_and_b32 v47, 0xffff0000, v215
v_lshlrev_b32 v230, 16, v119
v_and_b32 v231, 0xffff0000, v119
v_pk_add_f32 v[46:47], v[46:47], v[230:231]
s_waitcnt vmcnt(6)
v_lshlrev_b32 v48, 16, v216
v_and_b32 v49, 0xffff0000, v216
v_lshlrev_b32 v230, 16, v120
v_and_b32 v231, 0xffff0000, v120
v_pk_add_f32 v[48:49], v[48:49], v[230:231]
v_lshlrev_b32 v50, 16, v217
v_and_b32 v51, 0xffff0000, v217
v_lshlrev_b32 v230, 16, v121
v_and_b32 v231, 0xffff0000, v121
v_pk_add_f32 v[50:51], v[50:51], v[230:231]
v_lshlrev_b32 v52, 16, v218
v_and_b32 v53, 0xffff0000, v218
v_lshlrev_b32 v230, 16, v122
v_and_b32 v231, 0xffff0000, v122
v_pk_add_f32 v[52:53], v[52:53], v[230:231]
v_lshlrev_b32 v54, 16, v219
v_and_b32 v55, 0xffff0000, v219
v_lshlrev_b32 v230, 16, v123
v_and_b32 v231, 0xffff0000, v123
v_pk_add_f32 v[54:55], v[54:55], v[230:231]
s_waitcnt vmcnt(5)
v_lshlrev_b32 v56, 16, v220
v_and_b32 v57, 0xffff0000, v220
v_lshlrev_b32 v230, 16, v124
v_and_b32 v231, 0xffff0000, v124
v_pk_add_f32 v[56:57], v[56:57], v[230:231]
v_lshlrev_b32 v58, 16, v221
v_and_b32 v59, 0xffff0000, v221
v_lshlrev_b32 v230, 16, v125
v_and_b32 v231, 0xffff0000, v125
v_pk_add_f32 v[58:59], v[58:59], v[230:231]
v_lshlrev_b32 v60, 16, v222
v_and_b32 v61, 0xffff0000, v222
v_lshlrev_b32 v230, 16, v126
v_and_b32 v231, 0xffff0000, v126
v_pk_add_f32 v[60:61], v[60:61], v[230:231]
v_lshlrev_b32 v62, 16, v223
v_and_b32 v63, 0xffff0000, v223
v_lshlrev_b32 v230, 16, v127
v_and_b32 v231, 0xffff0000, v127
v_pk_add_f32 v[62:63], v[62:63], v[230:231]
s_waitcnt vmcnt(1)
v_lshlrev_b32 v64, 16, v96
v_and_b32 v65, 0xffff0000, v96
v_lshlrev_b32 v66, 16, v97
v_and_b32 v67, 0xffff0000, v97
v_lshlrev_b32 v68, 16, v98
v_and_b32 v69, 0xffff0000, v98
v_lshlrev_b32 v70, 16, v99
v_and_b32 v71, 0xffff0000, v99
v_lshlrev_b32 v72, 16, v100
v_and_b32 v73, 0xffff0000, v100
v_lshlrev_b32 v74, 16, v101
v_and_b32 v75, 0xffff0000, v101
v_lshlrev_b32 v76, 16, v102
v_and_b32 v77, 0xffff0000, v102
v_lshlrev_b32 v78, 16, v103
v_and_b32 v79, 0xffff0000, v103
v_lshlrev_b32 v80, 16, v104
v_and_b32 v81, 0xffff0000, v104
v_lshlrev_b32 v82, 16, v105
v_and_b32 v83, 0xffff0000, v105
v_lshlrev_b32 v84, 16, v106
v_and_b32 v85, 0xffff0000, v106
v_lshlrev_b32 v86, 16, v107
v_and_b32 v87, 0xffff0000, v107
v_lshlrev_b32 v88, 16, v108
v_and_b32 v89, 0xffff0000, v108
v_lshlrev_b32 v90, 16, v109
v_and_b32 v91, 0xffff0000, v109
v_lshlrev_b32 v92, 16, v110
v_and_b32 v93, 0xffff0000, v110
v_lshlrev_b32 v94, 16, v111
v_and_b32 v95, 0xffff0000, v111
v_mov_b32 v232, 0
v_mov_b32 v233, 0
v_mov_b32 v234, 0
s_cmp_eq_u32 s92, 0
s_cbranch_scc1 .La5_LT_SUM
.La5_LT_P5:
s_waitcnt vmcnt(0)
v_cmp_eq_u64 vcc, v[250:251], v[252:253]
s_add_u32 s96, s96, 1
s_lshr_b32 s92, vcc_lo, 1
s_and_b32 s92, s92, vcc_lo
s_lshr_b32 s93, s92, 2
s_and_b32 s92, s92, s93
s_lshr_b32 s93, s92, 4
s_and_b32 s92, s92, s93
s_lshr_b32 s93, s96, 20
s_mul_i32 s93, s93, 0x01010101
s_or_b32 s92, s92, s93
s_andn2_b32 s92, 0x01010101, s92
s_cbranch_scc0 .La5_LT_P6
buffer_load_dwordx2 v[250:251], v248, s[64:67], s20 offen sc1
s_branch .La5_LT_P5
.La5_LT_P6:
buffer_load_dwordx4 v[192:195], v240, s[32:35], s80 offen sc1
buffer_load_dwordx4 v[196:199], v241, s[32:35], s80 offen sc1
buffer_load_dwordx4 v[200:203], v242, s[32:35], s80 offen sc1
buffer_load_dwordx4 v[204:207], v243, s[32:35], s80 offen sc1
.La5_LT_SUM:
s_waitcnt vmcnt(3)
v_lshlrev_b32 v208, 16, v192
v_and_b32 v209, 0xffff0000, v192
v_lshlrev_b32 v210, 16, v193
v_and_b32 v211, 0xffff0000, v193
v_lshlrev_b32 v212, 16, v194
v_and_b32 v213, 0xffff0000, v194
v_lshlrev_b32 v214, 16, v195
v_and_b32 v215, 0xffff0000, v195
v_pk_add_f32 v[208:209], v[208:209], v[64:65]
v_pk_add_f32 v[210:211], v[210:211], v[66:67]
v_pk_add_f32 v[212:213], v[212:213], v[68:69]
v_pk_add_f32 v[214:215], v[214:215], v[70:71]
v_pk_add_f32 v[208:209], v[208:209], v[32:33]
v_pk_add_f32 v[210:211], v[210:211], v[34:35]
v_pk_add_f32 v[212:213], v[212:213], v[36:37]
v_pk_add_f32 v[214:215], v[214:215], v[38:39]
v_pk_add_f32 v[208:209], v[208:209], v[0:1]
v_pk_add_f32 v[210:211], v[210:211], v[2:3]
v_pk_add_f32 v[212:213], v[212:213], v[4:5]
v_pk_add_f32 v[214:215], v[214:215], v[6:7]
v_cvt_pk_bf16_f32 v96, v208, v209
v_cvt_pk_bf16_f32 v97, v210, v211
v_cvt_pk_bf16_f32 v98, v212, v213
v_cvt_pk_bf16_f32 v99, v214, v215
v_dot2_f32_bf16 v232, v96, v237, v232
v_dot2_f32_bf16 v233, v96, v96, v233
v_dot2_f32_bf16 v234, v96, v140, v234
v_dot2_f32_bf16 v232, v97, v237, v232
v_dot2_f32_bf16 v233, v97, v97, v233
v_dot2_f32_bf16 v234, v97, v141, v234
v_dot2_f32_bf16 v232, v98, v237, v232
v_dot2_f32_bf16 v233, v98, v98, v233
v_dot2_f32_bf16 v234, v98, v142, v234
v_dot2_f32_bf16 v232, v99, v237, v232
v_dot2_f32_bf16 v233, v99, v99, v233
v_dot2_f32_bf16 v234, v99, v143, v234
s_waitcnt vmcnt(2)
v_lshlrev_b32 v208, 16, v196
v_and_b32 v209, 0xffff0000, v196
v_lshlrev_b32 v210, 16, v197
v_and_b32 v211, 0xffff0000, v197
v_lshlrev_b32 v212, 16, v198
v_and_b32 v213, 0xffff0000, v198
v_lshlrev_b32 v214, 16, v199
v_and_b32 v215, 0xffff0000, v199
v_pk_add_f32 v[208:209], v[208:209], v[72:73]
v_pk_add_f32 v[210:211], v[210:211], v[74:75]
v_pk_add_f32 v[212:213], v[212:213], v[76:77]
v_pk_add_f32 v[214:215], v[214:215], v[78:79]
v_pk_add_f32 v[208:209], v[208:209], v[40:41]
v_pk_add_f32 v[210:211], v[210:211], v[42:43]
v_pk_add_f32 v[212:213], v[212:213], v[44:45]
v_pk_add_f32 v[214:215], v[214:215], v[46:47]
v_pk_add_f32 v[208:209], v[208:209], v[8:9]
v_pk_add_f32 v[210:211], v[210:211], v[10:11]
v_pk_add_f32 v[212:213], v[212:213], v[12:13]
v_pk_add_f32 v[214:215], v[214:215], v[14:15]
v_cvt_pk_bf16_f32 v100, v208, v209
v_cvt_pk_bf16_f32 v101, v210, v211
v_cvt_pk_bf16_f32 v102, v212, v213
v_cvt_pk_bf16_f32 v103, v214, v215
v_dot2_f32_bf16 v232, v100, v237, v232
v_dot2_f32_bf16 v233, v100, v100, v233
v_dot2_f32_bf16 v234, v100, v156, v234
v_dot2_f32_bf16 v232, v101, v237, v232
v_dot2_f32_bf16 v233, v101, v101, v233
v_dot2_f32_bf16 v234, v101, v157, v234
v_dot2_f32_bf16 v232, v102, v237, v232
v_dot2_f32_bf16 v233, v102, v102, v233
v_dot2_f32_bf16 v234, v102, v158, v234
v_dot2_f32_bf16 v232, v103, v237, v232
v_dot2_f32_bf16 v233, v103, v103, v233
v_dot2_f32_bf16 v234, v103, v159, v234
s_waitcnt vmcnt(1)
v_lshlrev_b32 v208, 16, v200
v_and_b32 v209, 0xffff0000, v200
v_lshlrev_b32 v210, 16, v201
v_and_b32 v211, 0xffff0000, v201
v_lshlrev_b32 v212, 16, v202
v_and_b32 v213, 0xffff0000, v202
v_lshlrev_b32 v214, 16, v203
v_and_b32 v215, 0xffff0000, v203
v_pk_add_f32 v[208:209], v[208:209], v[80:81]
v_pk_add_f32 v[210:211], v[210:211], v[82:83]
v_pk_add_f32 v[212:213], v[212:213], v[84:85]
v_pk_add_f32 v[214:215], v[214:215], v[86:87]
v_pk_add_f32 v[208:209], v[208:209], v[48:49]
v_pk_add_f32 v[210:211], v[210:211], v[50:51]
v_pk_add_f32 v[212:213], v[212:213], v[52:53]
v_pk_add_f32 v[214:215], v[214:215], v[54:55]
v_pk_add_f32 v[208:209], v[208:209], v[16:17]
v_pk_add_f32 v[210:211], v[210:211], v[18:19]
v_pk_add_f32 v[212:213], v[212:213], v[20:21]
v_pk_add_f32 v[214:215], v[214:215], v[22:23]
v_cvt_pk_bf16_f32 v104, v208, v209
v_cvt_pk_bf16_f32 v105, v210, v211
v_cvt_pk_bf16_f32 v106, v212, v213
v_cvt_pk_bf16_f32 v107, v214, v215
v_dot2_f32_bf16 v232, v104, v237, v232
v_dot2_f32_bf16 v233, v104, v104, v233
v_dot2_f32_bf16 v234, v104, v172, v234
v_dot2_f32_bf16 v232, v105, v237, v232
v_dot2_f32_bf16 v233, v105, v105, v233
v_dot2_f32_bf16 v234, v105, v173, v234
v_dot2_f32_bf16 v232, v106, v237, v232
v_dot2_f32_bf16 v233, v106, v106, v233
v_dot2_f32_bf16 v234, v106, v174, v234
v_dot2_f32_bf16 v232, v107, v237, v232
v_dot2_f32_bf16 v233, v107, v107, v233
v_dot2_f32_bf16 v234, v107, v175, v234
s_waitcnt vmcnt(0)
v_lshlrev_b32 v208, 16, v204
v_and_b32 v209, 0xffff0000, v204
v_lshlrev_b32 v210, 16, v205
v_and_b32 v211, 0xffff0000, v205
v_lshlrev_b32 v212, 16, v206
v_and_b32 v213, 0xffff0000, v206
v_lshlrev_b32 v214, 16, v207
v_and_b32 v215, 0xffff0000, v207
v_pk_add_f32 v[208:209], v[208:209], v[88:89]
v_pk_add_f32 v[210:211], v[210:211], v[90:91]
v_pk_add_f32 v[212:213], v[212:213], v[92:93]
v_pk_add_f32 v[214:215], v[214:215], v[94:95]
v_pk_add_f32 v[208:209], v[208:209], v[56:57]
v_pk_add_f32 v[210:211], v[210:211], v[58:59]
v_pk_add_f32 v[212:213], v[212:213], v[60:61]
v_pk_add_f32 v[214:215], v[214:215], v[62:63]
v_pk_add_f32 v[208:209], v[208:209], v[24:25]
v_pk_add_f32 v[210:211], v[210:211], v[26:27]
v_pk_add_f32 v[212:213], v[212:213], v[28:29]
v_pk_add_f32 v[214:215], v[214:215], v[30:31]
v_cvt_pk_bf16_f32 v108, v208, v209
v_cvt_pk_bf16_f32 v109, v210, v211
v_cvt_pk_bf16_f32 v110, v212, v213
v_cvt_pk_bf16_f32 v111, v214, v215
v_dot2_f32_bf16 v232, v108, v237, v232
v_dot2_f32_bf16 v233, v108, v108, v233
v_dot2_f32_bf16 v234, v108, v188, v234
v_dot2_f32_bf16 v232, v109, v237, v232
v_dot2_f32_bf16 v233, v109, v109, v233
v_dot2_f32_bf16 v234, v109, v189, v234
v_dot2_f32_bf16 v232, v110, v237, v232
v_dot2_f32_bf16 v233, v110, v110, v233
v_dot2_f32_bf16 v234, v110, v190, v234
v_dot2_f32_bf16 v232, v111, v237, v232
v_dot2_f32_bf16 v233, v111, v111, v233
v_dot2_f32_bf16 v234, v111, v191, v234
s_waitcnt vmcnt(0)
s_lshl2_add_u32 s92, s91, s3
s_lshl_b32 s92, s92, 1
s_sub_u32 s92, 0xffff, s92
v_add_f32_dpp v232, v232, v232 quad_perm:[1,0,3,2] row_mask:0xf bank_mask:0xf
v_add_f32_dpp v233, v233, v233 quad_perm:[1,0,3,2] row_mask:0xf bank_mask:0xf
v_add_f32_dpp v234, v234, v234 quad_perm:[1,0,3,2] row_mask:0xf bank_mask:0xf
v_add_f32_dpp v232, v232, v232 quad_perm:[2,3,0,1] row_mask:0xf bank_mask:0xf
v_add_f32_dpp v233, v233, v233 quad_perm:[2,3,0,1] row_mask:0xf bank_mask:0xf
v_add_f32_dpp v234, v234, v234 quad_perm:[2,3,0,1] row_mask:0xf bank_mask:0xf
v_add_f32_dpp v232, v232, v232 row_ror:8 row_mask:0xf bank_mask:0xf
v_add_f32_dpp v233, v233, v233 row_ror:8 row_mask:0xf bank_mask:0xf
v_add_f32_dpp v234, v234, v234 row_ror:8 row_mask:0xf bank_mask:0xf
v_mov_b32 v230, v232
v_mov_b32 v231, v233
v_mov_b32 v235, v234
v_permlane16_swap_b32 v230, v232
v_permlane16_swap_b32 v231, v233
v_permlane16_swap_b32 v235, v234
v_add_f32 v232, v232, v230
v_add_f32 v233, v233, v231
v_add_f32 v234, v234, v235
.La5_LW4_0:
s_cbranch_execz .La5_LW4_4
v_mov_b32 v230, v232
v_mov_b32 v231, v233
v_mov_b32 v235, v234
v_permlane32_swap_b32 v230, v232
v_permlane32_swap_b32 v231, v233
v_permlane32_swap_b32 v235, v234
v_add_f32 v232, v232, v230
v_add_f32 v233, v233, v231
v_add_f32_e64 v234, v234, v235
v_mul_f32 v244, 0x3a800000, v232
v_mul_f32 v233, 0x3a800000, v233
v_fma_f32 v230, -v244, v244, v233
v_mul_f32 v233, 0x3a800000, v233
v_cmp_nge_f32 vcc, v230, v233
v_bfe_u32 v235, v255, 6, 1
v_sub_u32_e64 v235, s92, v235
s_cbranch_vccnz .La5_LT_VAR
.La5_LT_VOK:
v_cvt_pk_bf16_f32 v244, v244, v244
v_lshlrev_b32 v244, 16, v244
v_cvt_pk_bf16_f32 v230, v230, v230
v_lshlrev_b32 v230, 16, v230
v_fma_f32 v234, -v244, v236, v234
v_add_f32 v230, 0x3727c5ac, v230
v_cvt_pk_bf16_f32 v230, v230, v230
v_lshlrev_b32 v230, 16, v230
v_sqrt_f32 v230, v230
v_lshrrev_b32 v232, 3, v255
v_cvt_pk_bf16_f32 v230, v230, v230
v_lshlrev_b32 v231, 16, v230
v_rcp_f32 v246, v231
v_lshrrev_b32 v230, 16, v230
v_or_b32 v233, v244, v230
v_mul_f32 v234, v234, v246
s_lshl_b32 s93, s3, 4
v_cvt_pk_bf16_f32 v234, v234, v234
v_and_b32 v230, 8, v232
v_ashrrev_i32 v232, 31, v234
v_or_b32 v232, 0x80000000, v232
v_xor_b32 v232, v232, v234
v_and_b32 v232, 0xffff0000, v232
v_add_u32 v230, s93, v230
v_or_b32 v232, v232, v235
ds_write_b64 v230, v[232:233] offset:0
v_lshrrev_b32 v230, 1, v255
v_and_b32 v230, 56, v230
s_waitcnt lgkmcnt(0)
s_barrier
s_cmp_lg_u32 s3, 0
s_cbranch_scc1 .La5_LT_OUT
ds_read_b64 v[232:233], v230 offset:0
s_lshl3_add_u32 s92, s72, s76
s_lshl_b32 s92, s92, 4
v_lshrrev_b32 v235, 7, v255
v_lshlrev_b32 v235, 12, v235
s_waitcnt lgkmcnt(0)
v_mov_b32 v234, v232
s_nop 1
v_max_u32_dpp v234, v234, v234 quad_perm:[1,0,3,2] row_mask:0xf bank_mask:0xf
s_nop 1
v_max_u32_dpp v234, v234, v234 quad_perm:[2,3,0,1] row_mask:0xf bank_mask:0xf
s_nop 1
v_max_u32_dpp v234, v234, v234 row_half_mirror row_mask:0xf bank_mask:0xf
s_nop 1
v_cmp_eq_u32 vcc, v232, v234
v_add_u32 v231, 8, v235
s_nop 0
v_cndmask_b32 v234, v232, 0, vcc
v_cndmask_b32 v235, v231, v235, vcc
s_nop 0
v_max_u32_dpp v234, v234, v234 quad_perm:[1,0,3,2] row_mask:0xf bank_mask:0xf
s_nop 1
v_max_u32_dpp v234, v234, v234 quad_perm:[2,3,0,1] row_mask:0xf bank_mask:0xf
s_nop 1
v_max_u32_dpp v234, v234, v234 row_half_mirror row_mask:0xf bank_mask:0xf
s_nop 1
v_cmp_ge_u32 vcc, v232, v234
s_nop 1
s_mov_b64 exec, vcc
buffer_store_dwordx2 v[232:233], v235, s[44:47], s92 offen sc1
s_mov_b64 exec, -1
; ---- out = h + .La5_LN(h) of the own rows; FFN weights into MFMA order
.La5_LT_OUT:
v_mul_f32 v244, v244, v246
v_mov_b32 v247, v246
v_xor_b32 v244, 0x80000000, v244
v_mov_b32 v245, v244
v_lshlrev_b32 v230, 16, v96
v_and_b32 v231, 0xffff0000, v96
v_pk_fma_f32 v[232:233], v[230:231], v[246:247], v[244:245]
v_cvt_pk_bf16_f32 v232, v232, v233
v_lshlrev_b32 v234, 16, v232
v_and_b32 v235, 0xffff0000, v232
v_pk_add_f32 v[230:231], v[230:231], v[234:235]
v_cvt_pk_bf16_f32 v192, v230, v231
v_lshlrev_b32 v230, 16, v97
v_and_b32 v231, 0xffff0000, v97
v_pk_fma_f32 v[232:233], v[230:231], v[246:247], v[244:245]
v_cvt_pk_bf16_f32 v232, v232, v233
v_lshlrev_b32 v234, 16, v232
v_and_b32 v235, 0xffff0000, v232
v_pk_add_f32 v[230:231], v[230:231], v[234:235]
v_cvt_pk_bf16_f32 v193, v230, v231
v_lshlrev_b32 v230, 16, v98
v_and_b32 v231, 0xffff0000, v98
v_pk_fma_f32 v[232:233], v[230:231], v[246:247], v[244:245]
v_cvt_pk_bf16_f32 v232, v232, v233
v_lshlrev_b32 v234, 16, v232
v_and_b32 v235, 0xffff0000, v232
v_pk_add_f32 v[230:231], v[230:231], v[234:235]
v_cvt_pk_bf16_f32 v194, v230, v231
v_lshlrev_b32 v230, 16, v99
v_and_b32 v231, 0xffff0000, v99
v_pk_fma_f32 v[232:233], v[230:231], v[246:247], v[244:245]
v_cvt_pk_bf16_f32 v232, v232, v233
v_lshlrev_b32 v234, 16, v232
v_and_b32 v235, 0xffff0000, v232
v_pk_add_f32 v[230:231], v[230:231], v[234:235]
v_cvt_pk_bf16_f32 v195, v230, v231
v_lshlrev_b32 v230, 16, v100
v_and_b32 v231, 0xffff0000, v100
v_pk_fma_f32 v[232:233], v[230:231], v[246:247], v[244:245]
v_cvt_pk_bf16_f32 v232, v232, v233
v_lshlrev_b32 v234, 16, v232
v_and_b32 v235, 0xffff0000, v232
v_pk_add_f32 v[230:231], v[230:231], v[234:235]
v_cvt_pk_bf16_f32 v196, v230, v231
v_lshlrev_b32 v230, 16, v101
v_and_b32 v231, 0xffff0000, v101
v_pk_fma_f32 v[232:233], v[230:231], v[246:247], v[244:245]
v_cvt_pk_bf16_f32 v232, v232, v233
v_lshlrev_b32 v234, 16, v232
v_and_b32 v235, 0xffff0000, v232
v_pk_add_f32 v[230:231], v[230:231], v[234:235]
v_cvt_pk_bf16_f32 v197, v230, v231
v_lshlrev_b32 v230, 16, v102
v_and_b32 v231, 0xffff0000, v102
v_pk_fma_f32 v[232:233], v[230:231], v[246:247], v[244:245]
v_cvt_pk_bf16_f32 v232, v232, v233
v_lshlrev_b32 v234, 16, v232
v_and_b32 v235, 0xffff0000, v232
v_pk_add_f32 v[230:231], v[230:231], v[234:235]
v_cvt_pk_bf16_f32 v198, v230, v231
v_lshlrev_b32 v230, 16, v103
v_and_b32 v231, 0xffff0000, v103
v_pk_fma_f32 v[232:233], v[230:231], v[246:247], v[244:245]
v_cvt_pk_bf16_f32 v232, v232, v233
v_lshlrev_b32 v234, 16, v232
v_and_b32 v235, 0xffff0000, v232
v_pk_add_f32 v[230:231], v[230:231], v[234:235]
v_cvt_pk_bf16_f32 v199, v230, v231
v_lshlrev_b32 v230, 16, v104
v_and_b32 v231, 0xffff0000, v104
v_pk_fma_f32 v[232:233], v[230:231], v[246:247], v[244:245]
v_cvt_pk_bf16_f32 v232, v232, v233
v_lshlrev_b32 v234, 16, v232
v_and_b32 v235, 0xffff0000, v232
v_pk_add_f32 v[230:231], v[230:231], v[234:235]
v_cvt_pk_bf16_f32 v200, v230, v231
v_lshlrev_b32 v230, 16, v105
v_and_b32 v231, 0xffff0000, v105
v_pk_fma_f32 v[232:233], v[230:231], v[246:247], v[244:245]
v_cvt_pk_bf16_f32 v232, v232, v233
v_lshlrev_b32 v234, 16, v232
v_and_b32 v235, 0xffff0000, v232
v_pk_add_f32 v[230:231], v[230:231], v[234:235]
v_cvt_pk_bf16_f32 v201, v230, v231
v_lshlrev_b32 v230, 16, v106
v_and_b32 v231, 0xffff0000, v106
v_pk_fma_f32 v[232:233], v[230:231], v[246:247], v[244:245]
v_cvt_pk_bf16_f32 v232, v232, v233
v_lshlrev_b32 v234, 16, v232
v_and_b32 v235, 0xffff0000, v232
v_pk_add_f32 v[230:231], v[230:231], v[234:235]
v_cvt_pk_bf16_f32 v202, v230, v231
v_lshlrev_b32 v230, 16, v107
v_and_b32 v231, 0xffff0000, v107
v_pk_fma_f32 v[232:233], v[230:231], v[246:247], v[244:245]
v_cvt_pk_bf16_f32 v232, v232, v233
v_lshlrev_b32 v234, 16, v232
v_and_b32 v235, 0xffff0000, v232
v_pk_add_f32 v[230:231], v[230:231], v[234:235]
v_cvt_pk_bf16_f32 v203, v230, v231
v_lshlrev_b32 v230, 16, v108
v_and_b32 v231, 0xffff0000, v108
v_pk_fma_f32 v[232:233], v[230:231], v[246:247], v[244:245]
v_cvt_pk_bf16_f32 v232, v232, v233
v_lshlrev_b32 v234, 16, v232
v_and_b32 v235, 0xffff0000, v232
v_pk_add_f32 v[230:231], v[230:231], v[234:235]
v_cvt_pk_bf16_f32 v204, v230, v231
v_lshlrev_b32 v230, 16, v109
v_and_b32 v231, 0xffff0000, v109
v_pk_fma_f32 v[232:233], v[230:231], v[246:247], v[244:245]
v_cvt_pk_bf16_f32 v232, v232, v233
v_lshlrev_b32 v234, 16, v232
v_and_b32 v235, 0xffff0000, v232
v_pk_add_f32 v[230:231], v[230:231], v[234:235]
v_cvt_pk_bf16_f32 v205, v230, v231
v_lshlrev_b32 v230, 16, v110
v_and_b32 v231, 0xffff0000, v110
v_pk_fma_f32 v[232:233], v[230:231], v[246:247], v[244:245]
v_cvt_pk_bf16_f32 v232, v232, v233
v_lshlrev_b32 v234, 16, v232
v_and_b32 v235, 0xffff0000, v232
v_pk_add_f32 v[230:231], v[230:231], v[234:235]
v_cvt_pk_bf16_f32 v206, v230, v231
v_lshlrev_b32 v230, 16, v111
v_and_b32 v231, 0xffff0000, v111
v_pk_fma_f32 v[232:233], v[230:231], v[246:247], v[244:245]
v_cvt_pk_bf16_f32 v232, v232, v233
v_lshlrev_b32 v234, 16, v232
v_and_b32 v235, 0xffff0000, v232
v_pk_add_f32 v[230:231], v[230:231], v[234:235]
v_cvt_pk_bf16_f32 v207, v230, v231
s_cmp_eq_u32 s30, 0
s_cbranch_scc1 .La5_LT_SEL
s_mov_b32 s94, 0x05040100
s_mov_b32 s95, 0x07060302
s_waitcnt vmcnt(0)
v_accvgpr_read_b32 v230, a224
v_accvgpr_read_b32 v231, a228
v_perm_b32 v128, v231, v230, s94
v_perm_b32 v132, v231, v230, s95
v_accvgpr_read_b32 v230, a232
v_accvgpr_read_b32 v231, a236
v_perm_b32 v129, v231, v230, s94
v_perm_b32 v133, v231, v230, s95
v_accvgpr_read_b32 v230, a240
v_accvgpr_read_b32 v231, a244
v_perm_b32 v130, v231, v230, s94
v_perm_b32 v134, v231, v230, s95
v_accvgpr_read_b32 v230, a248
v_accvgpr_read_b32 v231, a252
v_perm_b32 v131, v231, v230, s94
v_perm_b32 v135, v231, v230, s95
v_accvgpr_read_b32 v230, a225
v_accvgpr_read_b32 v231, a229
v_perm_b32 v136, v231, v230, s94
v_perm_b32 v140, v231, v230, s95
v_accvgpr_read_b32 v230, a233
v_accvgpr_read_b32 v231, a237
v_perm_b32 v137, v231, v230, s94
v_perm_b32 v141, v231, v230, s95
v_accvgpr_read_b32 v230, a241
v_accvgpr_read_b32 v231, a245
v_perm_b32 v138, v231, v230, s94
v_perm_b32 v142, v231, v230, s95
v_accvgpr_read_b32 v230, a249
v_accvgpr_read_b32 v231, a253
v_perm_b32 v139, v231, v230, s94
v_perm_b32 v143, v231, v230, s95
v_accvgpr_read_b32 v230, a226
v_accvgpr_read_b32 v231, a230
v_perm_b32 v144, v231, v230, s94
v_perm_b32 v148, v231, v230, s95
v_accvgpr_read_b32 v230, a234
v_accvgpr_read_b32 v231, a238
v_perm_b32 v145, v231, v230, s94
v_perm_b32 v149, v231, v230, s95
v_accvgpr_read_b32 v230, a242
v_accvgpr_read_b32 v231, a246
v_perm_b32 v146, v231, v230, s94
v_perm_b32 v150, v231, v230, s95
v_accvgpr_read_b32 v230, a250
v_accvgpr_read_b32 v231, a254
v_perm_b32 v147, v231, v230, s94
v_perm_b32 v151, v231, v230, s95
v_accvgpr_read_b32 v230, a227
v_accvgpr_read_b32 v231, a231
v_perm_b32 v152, v231, v230, s94
v_perm_b32 v156, v231, v230, s95
v_accvgpr_read_b32 v230, a235
v_accvgpr_read_b32 v231, a239
v_perm_b32 v153, v231, v230, s94
v_perm_b32 v157, v231, v230, s95
v_accvgpr_read_b32 v230, a243
v_accvgpr_read_b32 v231, a247
v_perm_b32 v154, v231, v230, s94
v_perm_b32 v158, v231, v230, s95
v_accvgpr_read_b32 v230, a251
v_accvgpr_read_b32 v231, a255
v_perm_b32 v155, v231, v230, s94
v_perm_b32 v159, v231, v230, s95
v_accvgpr_read_b32 v230, a64
v_accvgpr_read_b32 v231, a68
v_perm_b32 v160, v231, v230, s94
v_perm_b32 v164, v231, v230, s95
v_accvgpr_read_b32 v230, a72
v_accvgpr_read_b32 v231, a76
v_perm_b32 v161, v231, v230, s94
v_perm_b32 v165, v231, v230, s95
v_accvgpr_read_b32 v230, a80
v_accvgpr_read_b32 v231, a84
v_perm_b32 v162, v231, v230, s94
v_perm_b32 v166, v231, v230, s95
v_accvgpr_read_b32 v230, a88
v_accvgpr_read_b32 v231, a92
v_perm_b32 v163, v231, v230, s94
v_perm_b32 v167, v231, v230, s95
v_accvgpr_read_b32 v230, a65
v_accvgpr_read_b32 v231, a69
v_perm_b32 v168, v231, v230, s94
v_perm_b32 v172, v231, v230, s95
v_accvgpr_read_b32 v230, a73
v_accvgpr_read_b32 v231, a77
v_perm_b32 v169, v231, v230, s94
v_perm_b32 v173, v231, v230, s95
v_accvgpr_read_b32 v230, a81
v_accvgpr_read_b32 v231, a85
v_perm_b32 v170, v231, v230, s94
v_perm_b32 v174, v231, v230, s95
v_accvgpr_read_b32 v230, a89
v_accvgpr_read_b32 v231, a93
v_perm_b32 v171, v231, v230, s94
v_perm_b32 v175, v231, v230, s95
v_accvgpr_read_b32 v230, a66
v_accvgpr_read_b32 v231, a70
v_perm_b32 v176, v231, v230, s94
v_perm_b32 v180, v231, v230, s95
v_accvgpr_read_b32 v230, a74
v_accvgpr_read_b32 v231, a78
v_perm_b32 v177, v231, v230, s94
v_perm_b32 v181, v231, v230, s95
v_accvgpr_read_b32 v230, a82
v_accvgpr_read_b32 v231, a86
v_perm_b32 v178, v231, v230, s94
v_perm_b32 v182, v231, v230, s95
v_accvgpr_read_b32 v230, a90
v_accvgpr_read_b32 v231, a94
v_perm_b32 v179, v231, v230, s94
v_perm_b32 v183, v231, v230, s95
v_accvgpr_read_b32 v230, a67
v_accvgpr_read_b32 v231, a71
v_perm_b32 v184, v231, v230, s94
v_perm_b32 v188, v231, v230, s95
v_accvgpr_read_b32 v230, a75
v_accvgpr_read_b32 v231, a79
v_perm_b32 v185, v231, v230, s94
v_perm_b32 v189, v231, v230, s95
v_accvgpr_read_b32 v230, a83
v_accvgpr_read_b32 v231, a87
v_perm_b32 v186, v231, v230, s94
v_perm_b32 v190, v231, v230, s95
v_accvgpr_read_b32 v230, a91
v_accvgpr_read_b32 v231, a95
v_perm_b32 v187, v231, v230, s94
v_perm_b32 v191, v231, v230, s95
s_and_b32 s92, s3, 1
s_lshr_b32 s93, s3, 1
v_lshrrev_b32 v230, 6, v255
v_and_b32 v231, 48, v255
v_lshl_add_u32 v240, v230, 10, v231
s_lshl_b32 s96, s92, 14
v_add_u32 v240, s96, v240
s_lshl_b32 s96, s92, 10
v_add_u32 v241, s96, v255
s_lshl_b32 s97, s3, 10
v_add_u32 v208, s97, v255
v_bfe_u32 v230, v255, 4, 1
v_bfe_u32 v231, v255, 6, 2
v_lshrrev_b32 v232, 8, v255
v_lshlrev_b32 v209, 11, v230
v_lshl_add_u32 v209, v231, 8, v209
v_lshl_add_u32 v209, v232, 6, v209
v_add_u32 v209, s96, v209
v_bfe_u32 v233, v255, 4, 2
v_mul_u32_u24 v210, 640, v233
v_lshl_add_u32 v210, v231, 3, v210
s_mul_i32 s96, s93, 2560
s_lshl_b32 s92, s92, 5
s_add_u32 s96, s96, s92
v_add_u32 v210, s96, v210
v_cmp_eq_u32 s[98:99], v231, v232
v_mul_u32_u24 v211, 5, v255
s_lshl_b32 s96, s3, 7
v_lshrrev_b32 v212, 3, v255
v_and_b32 v212, 62, v212
v_add_u32 v212, s96, v212
v_lshlrev_b32 v234, 6, v230
v_lshl_add_u32 v234, v232, 4, v234
v_add_u32 v234, s96, v234
v_mov_b32 v235, 1024
v_cmp_eq_u32 vcc, 0, v231
s_nop 1
v_cndmask_b32 v216, v235, v234, vcc
v_cmp_eq_u32 vcc, 1, v231
s_nop 1
v_cndmask_b32 v217, v235, v234, vcc
v_cmp_eq_u32 vcc, 2, v231
s_nop 1
v_cndmask_b32 v218, v235, v234, vcc
v_cmp_eq_u32 vcc, 3, v231
s_nop 1
v_cndmask_b32 v219, v235, v234, vcc
v_mov_b32 v220, 0
v_mov_b32 v221, 0
v_mov_b32 v222, 0
v_mov_b32 v223, 0
ds_write_b128 v235, v[220:223] offset:10240
s_lshl_b32 s92, s100, 13
s_add_u32 s80, s92, 32768
s_add_u32 s20, s80, 4096
; ---- top two rows of the sequence; SwiGLU slice of 32 hidden units
.La5_LT_SEL:
.La5_LW4_1:
s_cbranch_execz .La5_LW4_R0
s_and_b32 s97, s2, 7
s_lshl_b32 s97, s97, 12
s_cmp_lg_u32 s3, 0
s_cbranch_scc1 .La5_LT_S2
s_mov_b32 s96, 0
.La5_LT_S1:
s_cmp_lg_u32 s30, 0
s_cbranch_scc1 .La5_LT_S0
s_sleep 24
.La5_LT_S0:
buffer_load_dwordx4 v[224:227], v255, s[44:47], s97 offen offset:0 sc1
buffer_load_dwordx4 v[232:235], v255, s[44:47], s97 offen offset:1024 sc1
buffer_load_dwordx4 v[244:247], v255, s[44:47], s97 offen offset:2048 sc1
buffer_load_dwordx4 v[248:251], v255, s[44:47], s97 offen offset:3072 sc1
s_add_u32 s96, s96, 1
s_lshr_b32 s92, s96, 20
s_waitcnt vmcnt(3)
v_min3_u32 v228, v224, v225, v226
v_min_u32 v228, v227, v228
s_waitcnt vmcnt(2)
v_min3_u32 v228, v232, v233, v228
v_min3_u32 v228, v234, v235, v228
s_waitcnt vmcnt(1)
v_min3_u32 v228, v244, v245, v228
v_min3_u32 v228, v246, v247, v228
s_waitcnt vmcnt(0)
v_min3_u32 v228, v248, v249, v228
v_min3_u32 v228, v250, v251, v228
v_cmp_ne_u32 vcc, 0, v228
v_max_u32 v229, v224, v232
v_max_u32 v230, v244, v248
s_nop 0
s_nop 0
s_cbranch_scc1 .La5_LT_S3
s_cmp_eq_u64 vcc, -1
s_cbranch_scc0 .La5_LT_S1
.La5_LT_S3:
v_max_u32 v228, v229, v230
v_min_u32 v229, v229, v230
v_mov_b32 v231, v228
v_min_u32 v224, v224, v232
v_min_u32 v244, v244, v248
v_max3_u32 v229, v229, v224, v244
v_max3_u32 v230, v226, v234, v246
v_max3_u32 v229, v229, v230, v250
s_nop 1
v_max_u32_dpp v231, v231, v231 row_shr:1 row_mask:0xf bank_mask:0xf
s_nop 1
v_max_u32_dpp v231, v231, v231 row_shr:2 row_mask:0xf bank_mask:0xf
s_nop 1
v_max_u32_dpp v231, v231, v231 row_shr:4 row_mask:0xf bank_mask:0xf
s_nop 1
v_max_u32_dpp v231, v231, v231 row_shr:8 row_mask:0xf bank_mask:0xf
s_nop 1
v_max_u32_dpp v231, v231, v231 row_bcast:15 row_mask:0xa bank_mask:0xf
s_nop 1
v_max_u32_dpp v231, v231, v231 row_bcast:31 row_mask:0xc bank_mask:0xf
s_nop 0
v_readlane_b32 s82, v231, 63
s_nop 1
v_cmp_eq_u32 vcc, s82, v228
v_mov_b32 v230, s82
v_mov_b32 v224, 0
v_cndmask_b32 v228, v228, v229, vcc
s_nop 1
v_max_u32_dpp v228, v228, v228 row_shr:1 row_mask:0xf bank_mask:0xf
s_nop 1
v_max_u32_dpp v228, v228, v228 row_shr:2 row_mask:0xf bank_mask:0xf
s_nop 1
v_max_u32_dpp v228, v228, v228 row_shr:4 row_mask:0xf bank_mask:0xf
s_nop 1
v_max_u32_dpp v228, v228, v228 row_shr:8 row_mask:0xf bank_mask:0xf
s_nop 1
v_max_u32_dpp v228, v228, v228 row_bcast:15 row_mask:0xa bank_mask:0xf
s_nop 1
v_max_u32_dpp v228, v228, v228 row_bcast:31 row_mask:0xc bank_mask:0xf
s_nop 0
v_readlane_b32 s81, v228, 63
s_nop 1
v_mov_b32 v231, s81
ds_write_b64 v224, v[230:231] offset:64
.La5_LT_S2:
v_mov_b32_e64 v224, 0
s_waitcnt lgkmcnt(0)
s_barrier
ds_read_b64 v[228:229], v224 offset:64
s_waitcnt lgkmcnt(0)
v_readfirstlane_b32 s82, v228
v_readfirstlane_b32 s81, v229
s_nop 0
s_andn2_b32 s84, 0x7ff, s82
s_andn2_b32 s21, 0x7ff, s81
s_cmp_eq_u32 s30, 0
s_cbranch_scc1 .La5_LT_ST
s_cmp_lt_u32 s3, 2
s_cselect_b32 s91, s84, s21
s_cselect_b32 s85, s82, s81
s_lshr_b32 s92, s91, 4
s_lshl_b32 s92, s92, 15
s_and_b32 s93, s91, 15
s_lshl_b32 s93, s93, 6
s_add_u32 s92, s92, s93
s_add_u32 s93, s92, 4194304
s_add_u32 s94, s92, 8388608
s_add_u32 s95, s92, 12582912
buffer_load_dwordx4 v[224:227], v240, s[32:35], s92 offen sc1
buffer_load_dwordx4 v[228:231], v240, s[32:35], s93 offen sc1
buffer_load_dwordx4 v[232:235], v240, s[32:35], s94 offen sc1
buffer_load_dwordx4 v[244:247], v240, s[32:35], s95 offen sc1
s_lshl_b32 s96, s91, 11
s_and_b32 s92, s91, 0xf8
s_lshl4_add_u32 s97, s92, s97
s_lshr_b32 s92, s91, 8
s_lshl4_add_u32 s97, s92, s97
buffer_load_dwordx4 v[248:251], v241, s[68:71], s96 offen
buffer_load_dwordx4 v[220:223], off, s[44:47], s97 sc1
s_waitcnt vmcnt(4)
v_lshlrev_b32 v236, 16, v224
v_and_b32 v237, 0xffff0000, v224
v_lshlrev_b32 v238, 16, v228
v_and_b32 v239, 0xffff0000, v228
v_pk_add_f32 v[236:237], v[236:237], v[238:239]
v_lshlrev_b32 v242, 16, v225
v_and_b32 v243, 0xffff0000, v225
v_lshlrev_b32 v238, 16, v229
v_and_b32 v239, 0xffff0000, v229
v_pk_add_f32 v[242:243], v[242:243], v[238:239]
v_lshlrev_b32 v252, 16, v226
v_and_b32 v253, 0xffff0000, v226
v_lshlrev_b32 v238, 16, v230
v_and_b32 v239, 0xffff0000, v230
v_pk_add_f32 v[252:253], v[252:253], v[238:239]
v_lshlrev_b32 v214, 16, v227
v_and_b32 v215, 0xffff0000, v227
v_lshlrev_b32 v238, 16, v231
v_and_b32 v239, 0xffff0000, v231
v_pk_add_f32 v[214:215], v[214:215], v[238:239]
s_waitcnt vmcnt(2)
v_lshlrev_b32 v224, 16, v232
v_and_b32 v225, 0xffff0000, v232
v_lshlrev_b32 v226, 16, v244
v_and_b32 v227, 0xffff0000, v244
v_pk_add_f32 v[224:225], v[224:225], v[226:227]
v_pk_add_f32 v[236:237], v[236:237], v[224:225]
v_lshlrev_b32 v224, 16, v233
v_and_b32 v225, 0xffff0000, v233
v_lshlrev_b32 v226, 16, v245
v_and_b32 v227, 0xffff0000, v245
v_pk_add_f32 v[224:225], v[224:225], v[226:227]
v_pk_add_f32 v[242:243], v[242:243], v[224:225]
v_lshlrev_b32 v224, 16, v234
v_and_b32 v225, 0xffff0000, v234
v_lshlrev_b32 v226, 16, v246
v_and_b32 v227, 0xffff0000, v246
v_pk_add_f32 v[224:225], v[224:225], v[226:227]
v_pk_add_f32 v[252:253], v[252:253], v[224:225]
v_lshlrev_b32 v224, 16, v235
v_and_b32 v225, 0xffff0000, v235
v_lshlrev_b32 v226, 16, v247
v_and_b32 v227, 0xffff0000, v247
v_pk_add_f32 v[224:225], v[224:225], v[226:227]
v_pk_add_f32 v[214:215], v[214:215], v[224:225]
s_waitcnt vmcnt(0)
v_cmp_eq_u32 vcc, s85, v220
s_nop 1
v_cndmask_b32 v220, v223, v221, vcc
v_lshlrev_b32 v222, 16, v220
v_rcp_f32 v222, v222
v_and_b32 v220, 0xffff0000, v220
v_mul_f32 v220, v220, v222
v_mov_b32 v223, v222
v_xor_b32 v220, 0x80000000, v220
v_mov_b32 v221, v220
v_lshlrev_b32 v224, 16, v248
v_and_b32 v225, 0xffff0000, v248
v_pk_add_f32 v[236:237], v[236:237], v[224:225]
v_cvt_pk_bf16_f32 v224, v236, v237
v_lshlrev_b32 v226, 16, v224
v_and_b32 v227, 0xffff0000, v224
v_pk_fma_f32 v[226:227], v[226:227], v[222:223], v[220:221]
v_cvt_pk_bf16_f32 v228, v226, v227
v_lshlrev_b32 v224, 16, v249
v_and_b32 v225, 0xffff0000, v249
v_pk_add_f32 v[242:243], v[242:243], v[224:225]
v_cvt_pk_bf16_f32 v224, v242, v243
v_lshlrev_b32 v226, 16, v224
v_and_b32 v227, 0xffff0000, v224
v_pk_fma_f32 v[226:227], v[226:227], v[222:223], v[220:221]
v_cvt_pk_bf16_f32 v229, v226, v227
v_lshlrev_b32 v224, 16, v250
v_and_b32 v225, 0xffff0000, v250
v_pk_add_f32 v[252:253], v[252:253], v[224:225]
v_cvt_pk_bf16_f32 v224, v252, v253
v_lshlrev_b32 v226, 16, v224
v_and_b32 v227, 0xffff0000, v224
v_pk_fma_f32 v[226:227], v[226:227], v[222:223], v[220:221]
v_cvt_pk_bf16_f32 v230, v226, v227
v_lshlrev_b32 v224, 16, v251
v_and_b32 v225, 0xffff0000, v251
v_pk_add_f32 v[214:215], v[214:215], v[224:225]
v_cvt_pk_bf16_f32 v224, v214, v215
v_lshlrev_b32 v226, 16, v224
v_and_b32 v227, 0xffff0000, v224
v_pk_fma_f32 v[226:227], v[226:227], v[222:223], v[220:221]
v_cvt_pk_bf16_f32 v231, v226, v227
ds_write_b128 v208, v[228:231] offset:1024
s_waitcnt lgkmcnt(0)
s_barrier
ds_read_b128 v[224:227], v209 offset:1024
ds_read_b128 v[232:235], v209 offset:1040
ds_read_b128 v[244:247], v209 offset:1056
ds_read_b128 v[248:251], v209 offset:1072
s_waitcnt lgkmcnt(3)
v_mfma_f32_16x16x32_bf16 a[0:3], v[224:227], a[96:99], 0
v_mfma_f32_16x16x32_bf16 a[4:7], v[224:227], a[100:103], 0
v_mfma_f32_16x16x32_bf16 a[8:11], v[224:227], a[104:107], 0
v_mfma_f32_16x16x32_bf16 a[12:15], v[224:227], a[108:111], 0
v_mfma_f32_16x16x32_bf16 a[16:19], v[224:227], a[112:115], 0
v_mfma_f32_16x16x32_bf16 a[20:23], v[224:227], a[116:119], 0
v_mfma_f32_16x16x32_bf16 a[24:27], v[224:227], a[120:123], 0
v_mfma_f32_16x16x32_bf16 a[28:31], v[224:227], a[124:127], 0
s_waitcnt lgkmcnt(2)
v_mfma_f32_16x16x32_bf16 a[0:3], v[232:235], a[128:131], a[0:3]
v_mfma_f32_16x16x32_bf16 a[4:7], v[232:235], a[132:135], a[4:7]
v_mfma_f32_16x16x32_bf16 a[8:11], v[232:235], a[136:139], a[8:11]
v_mfma_f32_16x16x32_bf16 a[12:15], v[232:235], a[140:143], a[12:15]
v_mfma_f32_16x16x32_bf16 a[16:19], v[232:235], a[144:147], a[16:19]
v_mfma_f32_16x16x32_bf16 a[20:23], v[232:235], a[148:151], a[20:23]
v_mfma_f32_16x16x32_bf16 a[24:27], v[232:235], a[152:155], a[24:27]
v_mfma_f32_16x16x32_bf16 a[28:31], v[232:235], a[156:159], a[28:31]
s_waitcnt lgkmcnt(1)
v_mfma_f32_16x16x32_bf16 a[0:3], v[244:247], a[160:163], a[0:3]
v_mfma_f32_16x16x32_bf16 a[4:7], v[244:247], a[164:167], a[4:7]
v_mfma_f32_16x16x32_bf16 a[8:11], v[244:247], a[168:171], a[8:11]
v_mfma_f32_16x16x32_bf16 a[12:15], v[244:247], a[172:175], a[12:15]
v_mfma_f32_16x16x32_bf16 a[16:19], v[244:247], a[176:179], a[16:19]
.La5_LW4_2:
s_cbranch_execz .La5_LW4_R0
v_mfma_f32_16x16x32_bf16 a[20:23], v[244:247], a[180:183], a[20:23]
v_mfma_f32_16x16x32_bf16 a[24:27], v[244:247], a[184:187], a[24:27]
v_mfma_f32_16x16x32_bf16 a[28:31], v[244:247], a[188:191], a[28:31]
s_waitcnt lgkmcnt(0)
v_mfma_f32_16x16x32_bf16 a[0:3], v[248:251], a[192:195], a[0:3]
v_mfma_f32_16x16x32_bf16 a[4:7], v[248:251], a[196:199], a[4:7]
v_mfma_f32_16x16x32_bf16 a[8:11], v[248:251], a[200:203], a[8:11]
v_mfma_f32_16x16x32_bf16 a[12:15], v[248:251], a[204:207], a[12:15]
v_mfma_f32_16x16x32_bf16 a[16:19], v[248:251], a[208:211], a[16:19]
v_mfma_f32_16x16x32_bf16 a[20:23], v[248:251], a[212:215], a[20:23]
v_mfma_f32_16x16x32_bf16 a[24:27], v[248:251], a[216:219], a[24:27]
v_mfma_f32_16x16x32_bf16 a[28:31], v[248:251], a[220:223], a[28:31]
s_mov_b64 exec, s[98:99]
ds_write_b64 v210, a[0:1] offset:5120
ds_write_b64 v210, a[4:5] offset:5200
ds_write_b64 v210, a[8:9] offset:5280
ds_write_b64 v210, a[12:13] offset:5360
ds_write_b64 v210, a[16:17] offset:5440
s_nop 3
ds_write_b64 v210, a[20:21] offset:5520
s_nop 3
ds_write_b64 v210, a[24:25] offset:5600
s_nop 3
ds_write_b64 v210, a[28:29] offset:5680
s_mov_b64 exec, -1
s_waitcnt lgkmcnt(0)
s_barrier
ds_read_b128 v[224:227], v211 offset:5120
ds_read_b128 v[232:235], v211 offset:5136
ds_read_b128 v[244:247], v211 offset:5152
ds_read_b128 v[248:251], v211 offset:5168
s_waitcnt lgkmcnt(2)
v_pk_add_f32 v[224:225], v[224:225], v[226:227]
v_pk_add_f32 v[232:233], v[232:233], v[234:235]
s_waitcnt lgkmcnt(0)
v_pk_add_f32 v[244:245], v[244:245], v[246:247]
v_pk_add_f32 v[248:249], v[248:249], v[250:251]
v_pk_add_f32 v[224:225], v[224:225], v[232:233]
v_pk_add_f32 v[244:245], v[244:245], v[248:249]
v_pk_add_f32 v[224:225], v[224:225], v[244:245]
v_cvt_pk_bf16_f32 v224, v224, v225
v_mov_b32 v226, v224
s_nop 1
v_permlane32_swap_b32 v224, v226
s_nop 1
v_lshlrev_b32 v228, 16, v224
v_and_b32 v229, 0xffff0000, v224
v_lshlrev_b32 v232, 16, v226
v_and_b32 v233, 0xffff0000, v226
v_mul_f32 v230, 0xbfb8aa3b, v228
v_mul_f32 v231, 0xbfb8aa3b, v229
v_exp_f32 v230, v230
v_exp_f32 v231, v231
v_add_f32 v230, 1.0, v230
v_add_f32 v231, 1.0, v231
v_rcp_f32 v230, v230
v_rcp_f32 v231, v231
s_nop 0
v_pk_mul_f32 v[228:229], v[228:229], v[230:231]
v_cvt_pk_bf16_f32 v228, v228, v229
v_lshlrev_b32 v230, 16, v228
v_and_b32 v231, 0xffff0000, v228
v_pk_mul_f32 v[230:231], v[230:231], v[232:233]
v_cvt_pk_bf16_f32 v228, v230, v231
ds_write_b16 v212, v228 offset:10240
ds_write_b16_d16_hi v212, v228 offset:10304
s_waitcnt lgkmcnt(0)
ds_read_b128 v[224:227], v216 offset:10240
ds_read_b128 v[232:235], v217 offset:10240
ds_read_b128 v[244:247], v218 offset:10240
ds_read_b128 v[248:251], v219 offset:10240
s_waitcnt lgkmcnt(3)
v_mfma_f32_16x16x32_bf16 v[0:3], v[224:227], v[128:131], 0
v_mfma_f32_16x16x32_bf16 v[4:7], v[224:227], v[132:135], 0
v_mfma_f32_16x16x32_bf16 v[8:11], v[224:227], v[136:139], 0
v_mfma_f32_16x16x32_bf16 v[12:15], v[224:227], v[140:143], 0
s_waitcnt lgkmcnt(2)
v_mfma_f32_16x16x32_bf16 v[0:3], v[232:235], v[144:147], v[0:3]
v_mfma_f32_16x16x32_bf16 v[4:7], v[232:235], v[148:151], v[4:7]
v_mfma_f32_16x16x32_bf16 v[8:11], v[232:235], v[152:155], v[8:11]
v_mfma_f32_16x16x32_bf16 v[12:15], v[232:235], v[156:159], v[12:15]
s_waitcnt lgkmcnt(1)
v_mfma_f32_16x16x32_bf16 v[0:3], v[244:247], v[160:163], v[0:3]
v_mfma_f32_16x16x32_bf16 v[4:7], v[244:247], v[164:167], v[4:7]
v_mfma_f32_16x16x32_bf16 v[8:11], v[244:247], v[168:171], v[8:11]
v_mfma_f32_16x16x32_bf16 v[12:15], v[244:247], v[172:175], v[12:15]
s_waitcnt lgkmcnt(0)
v_mfma_f32_16x16x32_bf16 v[0:3], v[248:251], v[176:179], v[0:3]
v_mfma_f32_16x16x32_bf16 v[4:7], v[248:251], v[180:183], v[4:7]
v_mfma_f32_16x16x32_bf16 v[8:11], v[248:251], v[184:187], v[8:11]
v_mfma_f32_16x16x32_bf16 v[12:15], v[248:251], v[188:191], v[12:15]
s_nop 7
v_or_b32 v16, 1, v0
v_or_b32 v20, 1, v1
v_or_b32 v17, 1, v4
v_or_b32 v21, 1, v5
v_or_b32 v18, 1, v8
v_or_b32 v22, 1, v9
v_or_b32 v19, 1, v12
v_or_b32 v23, 1, v13
buffer_store_dwordx4 v[16:19], v208, s[44:47], s80 offen sc1
buffer_store_dwordx4 v[20:23], v208, s[44:47], s20 offen sc1
; ---- rows that were not routed leave; final sums of the routed rows
.La5_LT_ST:
.La5_LW4_3:
s_cbranch_execz .La5_LW4_R0
s_lshl_b32 s92, s76, 5
s_add_u32 s92, s92, s72
s_lshl2_add_u32 s92, s92, s3
s_lshl_b32 s93, s92, 1
v_bfe_u32 v230, v255, 6, 1
v_add_u32 v231, s93, v230
v_cmp_ne_u32 vcc, s84, v231
v_cmp_ne_u32 s[94:95], s21, v231
v_lshrrev_b32 v232, 7, v255
v_and_b32 v233, 48, v255
v_lshl_add_u32 v232, v232, 6, v233
v_lshl_add_u32 v232, v230, 11, v232
s_lshl_b32 s92, s92, 12
s_and_b64 exec, vcc, s[94:95]
s_cbranch_scc0 .La5_LT_F0
buffer_store_dwordx4 v[192:195], v232, s[56:59], s92 offen offset:0 sc1
buffer_store_dwordx4 v[196:199], v232, s[56:59], s92 offen offset:512 sc1
buffer_store_dwordx4 v[200:203], v232, s[56:59], s92 offen offset:1024 sc1
buffer_store_dwordx4 v[204:207], v232, s[56:59], s92 offen offset:1536 sc1
.La5_LT_F0:
s_mov_b64 exec, -1
s_cmp_eq_u32 s75, 1
s_nop 0
s_nop 0
s_cbranch_scc1 .La5_LT_END
s_lshr_b32 s92, s2, 6
s_lshl2_add_u32 s92, s92, s74
s_and_b32 s93, s2, 7
s_lshl3_add_u32 s91, s92, s93
s_cmp_lt_u32 s91, 64
s_cselect_b32 s84, s84, s21
s_cselect_b32 s82, s82, s81
s_bfe_u32 s92, s91, 0x30003
s_and_b32 s93, s91, 3
s_lshl2_add_u32 s96, s92, s93
s_and_b32 s85, s91, 4
s_lshl_b32 s85, s85, 1
v_bfe_u32 v0, v255, 6, 2
v_bfe_u32 v1, v255, 4, 2
v_lshlrev_b32 v1, 1, v1
v_lshl_add_u32 v0, v0, 4, v1
v_add_u32 v0, s85, v0
s_lshl_b32 s85, s96, 10
v_add_u32 v1, s85, v0
s_lshl_b32 s85, s96, 6
v_add_u32 v0, s85, v0
s_lshr_b32 s92, s84, 4
s_lshl_b32 s92, s92, 15
s_and_b32 s93, s84, 15
s_lshl_b32 s93, s93, 6
s_add_u32 s92, s92, s93
s_add_u32 s93, s92, 4194304
s_add_u32 s94, s92, 8388608
s_add_u32 s95, s92, 12582912
buffer_load_ushort v4, v1, s[32:35], s92 offen sc1
buffer_load_ushort v5, v1, s[32:35], s93 offen sc1
buffer_load_ushort v6, v1, s[32:35], s94 offen sc1
buffer_load_ushort v7, v1, s[32:35], s95 offen sc1
s_lshl_b32 s21, s84, 11
s_and_b32 s92, s84, 0xf8
s_lshl4_add_u32 s97, s92, s97
s_lshr_b32 s92, s84, 8
s_lshl4_add_u32 s97, s92, s97
buffer_load_ushort v8, v0, s[68:71], s21 offen
buffer_load_dwordx4 v[12:15], off, s[44:47], s97 sc1
v_lshrrev_b32 v2, 6, v255
v_lshlrev_b32 v2, 13, v2
v_and_b32 v3, 48, v255
v_add_u32 v2, v2, v3
s_lshl_b32 s92, s3, 17
s_lshl_b32 s93, s91, 6
s_add_u32 s92, s92, s93
s_add_u32 s94, s92, 32768
s_cmp_eq_u32 s3, 3
s_cselect_b32 s95, 0, 524288
s_cselect_b32 s96, 0, -1
s_add_u32 s95, s95, s94
s_ashr_i32 s92, s82, 31
s_orn2_b32 s92, 0x80000000, s92
s_xor_b32 s92, s92, s82
s_and_b32 s92, s92, 0xffff0000
v_mov_b32 v9, 0xbfb8aa3b
v_mul_f32 v9, s92, v9
v_exp_f32 v9, v9
v_lshrrev_b32 v3, 8, v255
s_lshl_b32 s92, s3, 4
v_add_f32 v9, 1.0, v9
v_lshl_add_u32 v3, v3, 2, s92
v_rcp_f32 v9, v9
v_and_b32 v10, 48, v255
v_mul_u32_u24 v10, 20, v10
v_add_u32 v3, v3, v10
v_cvt_pk_bf16_f32 v9, v9, v9
v_lshlrev_b32 v9, 16, v9
v_and_b32 v10, 0xf0, v255
v_mul_u32_u24 v10, 5, v10
s_waitcnt vmcnt(0)
v_cmp_eq_u32 vcc, s82, v12
s_nop 1
v_cndmask_b32 v12, v15, v13, vcc
v_lshlrev_b32 v14, 16, v12
v_rcp_f32 v14, v14
v_and_b32 v12, 0xffff0000, v12
v_mul_f32 v12, v12, v14
v_mov_b32 v15, v14
v_xor_b32 v12, 0x80000000, v12
v_mov_b32 v13, v12
v_lshlrev_b32 v4, 16, v4
v_lshlrev_b32 v5, 16, v5
v_lshlrev_b32 v6, 16, v6
v_lshlrev_b32 v7, 16, v7
v_lshlrev_b32 v8, 16, v8
v_add_f32 v4, v4, v5
v_add_f32 v6, v6, v7
v_add_f32 v4, v4, v6
v_add_f32 v4, v4, v8
v_cvt_pk_bf16_f32 v4, v4, v4
v_lshlrev_b32 v4, 16, v4
v_fma_f32 v5, v4, v14, v12
v_cvt_pk_bf16_f32 v5, v5, v5
v_lshlrev_b32 v5, 16, v5
s_sleep 40
s_mov_b32 s93, 0
.La5_LT_Y:
buffer_load_dwordx4 v[16:19], v2, s[44:47], s94 offen sc1
buffer_load_dwordx4 v[20:23], v2, s[44:47], s95 offen sc1
s_add_u32 s93, s93, 1
s_lshr_b32 s92, s93, 20
s_waitcnt vmcnt(0)
v_min3_u32 v24, v16, v17, v18
v_min3_u32 v25, v20, v21, v22
v_min3_u32 v24, v24, v19, v23
v_min_u32 v24, v24, v25
v_cmp_ne_u32 vcc, 0, v24
v_and_b32 v20, s96, v20
v_and_b32 v21, s96, v21
v_and_b32 v22, s96, v22
v_and_b32 v23, s96, v23
s_cbranch_scc1 .La5_LT_Y1
s_cmp_eq_u64 vcc, -1
s_cbranch_scc0 .La5_LT_Y
.La5_LT_Y1:
v_pk_add_f32 v[16:17], v[16:17], v[20:21]
v_pk_add_f32 v[18:19], v[18:19], v[22:23]
s_nop 0
v_add_f32_dpp v16, v16, v16 row_ror:4 row_mask:0xf bank_mask:0xf
v_add_f32_dpp v17, v17, v17 row_ror:4 row_mask:0xf bank_mask:0xf
v_add_f32_dpp v18, v18, v18 row_ror:4 row_mask:0xf bank_mask:0xf
v_add_f32_dpp v19, v19, v19 row_ror:4 row_mask:0xf bank_mask:0xf
v_add_f32_dpp v16, v16, v16 row_ror:8 row_mask:0xf bank_mask:0xf
v_add_f32_dpp v17, v17, v17 row_ror:8 row_mask:0xf bank_mask:0xf
v_add_f32_dpp v18, v18, v18 row_ror:8 row_mask:0xf bank_mask:0xf
v_add_f32_dpp v19, v19, v19 row_ror:8 row_mask:0xf bank_mask:0xf
ds_write_b32 v3, v16 offset:5120
ds_write_b32 v3, v17 offset:5200
ds_write_b32 v3, v18 offset:5280
ds_write_b32 v3, v19 offset:5360
s_waitcnt lgkmcnt(0)
s_barrier
ds_read_b128 v[16:19], v10 offset:5120
ds_read_b128 v[20:23], v10 offset:5136
ds_read_b128 v[24:27], v10 offset:5152
ds_read_b128 v[28:31], v10 offset:5168
s_waitcnt lgkmcnt(2)
v_pk_add_f32 v[16:17], v[16:17], v[18:19]
v_pk_add_f32 v[20:21], v[20:21], v[22:23]
s_waitcnt lgkmcnt(0)
v_pk_add_f32 v[24:25], v[24:25], v[26:27]
v_pk_add_f32 v[28:29], v[28:29], v[30:31]
v_pk_add_f32 v[16:17], v[16:17], v[20:21]
v_pk_add_f32 v[24:25], v[24:25], v[28:29]
v_pk_add_f32 v[16:17], v[16:17], v[24:25]
v_add_f32 v16, v16, v17
v_cvt_pk_bf16_f32 v16, v16, v16
v_lshlrev_b32 v16, 16, v16
v_mul_f32 v16, v16, v9
v_cvt_pk_bf16_f32 v16, v16, v16
v_lshlrev_b32 v16, 16, v16
v_add_f32 v16, v16, v5
v_cvt_pk_bf16_f32 v16, v16, v16
v_lshlrev_b32 v16, 16, v16
v_add_f32 v16, v16, v4
v_cvt_pk_bf16_f32 v16, v16, v16
s_cmp_lg_u32 s3, 0
s_cbranch_scc1 .La5_LT_END
s_mov_b64 exec, 0xffff
buffer_store_short v16, v0, s[56:59], s21 offen
s_mov_b64 exec, -1
.La5_LT_END:
.La5_LW4_4:
s_cbranch_execz .La5_LW4_R0
s_lshl2_add_u32 s92, s2, s3
s_cmp_lg_u32 s92, 992
s_cbranch_scc1 .La5_LT_E1
buffer_store_dwordx2 v[252:253], off, s[64:67], 0 sc1
.La5_LT_E1:
s_branch .La5_LEND
; ---- fallback: the earlier 32 rows against all their keys with the true running maximum (online softmax)
.La5_LA_SLOW0:
s_waitcnt lgkmcnt(0)
s_nop 7
s_nop 7
v_accvgpr_write_b32 a160, v192
v_accvgpr_write_b32 a161, v193
v_accvgpr_write_b32 a162, v194
v_accvgpr_write_b32 a163, v195
v_accvgpr_write_b32 a164, v196
v_accvgpr_write_b32 a165, v197
v_accvgpr_write_b32 a166, v198
v_accvgpr_write_b32 a167, v199
v_accvgpr_write_b32 a168, v200
v_accvgpr_write_b32 a169, v201
v_accvgpr_write_b32 a170, v202
v_accvgpr_write_b32 a171, v203
v_accvgpr_write_b32 a172, v204
v_accvgpr_write_b32 a173, v205
v_accvgpr_write_b32 a174, v206
v_accvgpr_write_b32 a175, v207
v_accvgpr_write_b32 a176, v208
v_accvgpr_write_b32 a177, v209
v_accvgpr_write_b32 a178, v210
v_accvgpr_write_b32 a179, v211
v_accvgpr_write_b32 a180, v212
v_accvgpr_write_b32 a181, v213
v_accvgpr_write_b32 a182, v214
v_accvgpr_write_b32 a183, v215
v_accvgpr_write_b32 a184, v216
v_accvgpr_write_b32 a185, v217
v_accvgpr_write_b32 a186, v218
v_accvgpr_write_b32 a187, v219
v_accvgpr_write_b32 a188, v220
v_accvgpr_write_b32 a189, v221
v_accvgpr_write_b32 a190, v222
v_accvgpr_write_b32 a191, v223
s_lshl_b32 s96, s72, 19
s_add_u32 s96, s96, 20971520
s_add_u32 s92, s26, s96
s_addc_u32 s93, s27, 0
s_and_b32 s93, s93, 0xffff
s_mov_b32 s94, 17301504
s_mov_b32 s95, 0x20000
v_mov_b32 v0, 0
v_mov_b32 v1, 0
v_mov_b32 v2, 0
v_mov_b32 v3, 0
v_mov_b32 v4, 0
v_mov_b32 v5, 0
v_mov_b32 v6, 0
v_mov_b32 v7, 0
v_mov_b32 v8, 0
v_mov_b32 v9, 0
v_mov_b32 v10, 0
v_mov_b32 v11, 0
v_mov_b32 v12, 0
v_mov_b32 v13, 0
v_mov_b32 v14, 0
v_mov_b32 v15, 0
v_mov_b32 v16, 0
v_mov_b32 v17, 0
v_mov_b32 v18, 0
v_mov_b32 v19, 0
v_mov_b32 v20, 0
v_mov_b32 v21, 0
v_mov_b32 v22, 0
v_mov_b32 v23, 0
v_mov_b32 v24, 0
v_mov_b32 v25, 0
v_mov_b32 v26, 0
v_mov_b32 v27, 0
v_mov_b32 v28, 0
v_mov_b32 v29, 0
v_mov_b32 v30, 0
v_mov_b32 v31, 0
v_mov_b32 v32, 0
v_mov_b32 v33, 0
v_mov_b32 v34, 0
v_mov_b32 v35, 0
v_mov_b32 v36, 0
v_mov_b32 v37, 0
v_mov_b32 v38, 0
v_mov_b32 v39, 0
v_mov_b32 v40, 0
v_mov_b32 v41, 0
v_mov_b32 v42, 0
v_mov_b32 v43, 0
v_mov_b32 v44, 0
v_mov_b32 v45, 0
v_mov_b32 v46, 0
v_mov_b32 v47, 0
v_mov_b32 v48, 0
v_mov_b32 v49, 0
v_mov_b32 v50, 0
v_mov_b32 v51, 0
v_mov_b32 v52, 0
v_mov_b32 v53, 0
v_mov_b32 v54, 0
v_mov_b32 v55, 0
v_mov_b32 v56, 0
v_mov_b32 v57, 0
v_mov_b32 v58, 0
v_mov_b32 v59, 0
v_mov_b32 v60, 0
v_mov_b32 v61, 0
v_mov_b32 v62, 0
v_mov_b32 v63, 0
v_mov_b32 v216, 0xff61b1e6
v_mov_b32 v217, 0xff61b1e6
v_mov_b32 v218, 0
v_mov_b32 v219, 0
s_mov_b32 s7, 0
.La5_LA_SLOW0_L:
s_lshl_b32 s96, s7, 13
s_add_u32 s97, s96, 4096
buffer_load_dwordx4 a[128:131], v255, s[92:95], s96 offen offset:0 sc1
buffer_load_dwordx4 a[132:135], v255, s[92:95], s96 offen offset:1024 sc1
buffer_load_dwordx4 a[136:139], v255, s[92:95], s96 offen offset:2048 sc1
buffer_load_dwordx4 a[140:143], v255, s[92:95], s96 offen offset:3072 sc1
buffer_load_dwordx4 a[144:147], v255, s[92:95], s97 offen offset:0 sc1
buffer_load_dwordx4 a[148:151], v255, s[92:95], s97 offen offset:1024 sc1
buffer_load_dwordx4 a[152:155], v255, s[92:95], s97 offen offset:2048 sc1
buffer_load_dwordx4 a[156:159], v255, s[92:95], s97 offen offset:3072 sc1
s_lshl_b32 s96, s76, 3
s_sub_u32 s96, s96, s7
s_add_u32 s96, s96, s3
s_lshl_b32 s96, s96, 5
v_bfe_u32 v220, v255, 4, 4
v_lshrrev_b32 v221, 8, v255
v_lshlrev_b32 v221, 2, v221
v_sub_u32 v220, v220, v221
v_add_u32 v220, s96, v220
v_mov_b32 v222, 0xceabfb8f
s_waitcnt vmcnt(0)
v_mfma_f32_16x16x32_bf16 v[192:195], a[128:131], v[128:131], 0
v_mfma_f32_16x16x32_bf16 v[192:195], a[132:135], v[132:135], v[192:195]
v_mfma_f32_16x16x32_bf16 v[192:195], a[136:139], v[136:139], v[192:195]
v_mfma_f32_16x16x32_bf16 v[192:195], a[140:143], v[140:143], v[192:195]
v_mfma_f32_16x16x32_bf16 v[196:199], a[144:147], v[128:131], 0
v_mfma_f32_16x16x32_bf16 v[196:199], a[148:151], v[132:135], v[196:199]
v_mfma_f32_16x16x32_bf16 v[196:199], a[152:155], v[136:139], v[196:199]
v_mfma_f32_16x16x32_bf16 v[196:199], a[156:159], v[140:143], v[196:199]
v_mfma_f32_16x16x32_bf16 v[200:203], a[128:131], v[144:147], 0
v_mfma_f32_16x16x32_bf16 v[200:203], a[132:135], v[148:151], v[200:203]
v_mfma_f32_16x16x32_bf16 v[200:203], a[136:139], v[152:155], v[200:203]
v_mfma_f32_16x16x32_bf16 v[200:203], a[140:143], v[156:159], v[200:203]
v_mfma_f32_16x16x32_bf16 v[204:207], a[144:147], v[144:147], 0
v_mfma_f32_16x16x32_bf16 v[204:207], a[148:151], v[148:151], v[204:207]
v_mfma_f32_16x16x32_bf16 v[204:207], a[152:155], v[152:155], v[204:207]
v_mfma_f32_16x16x32_bf16 v[204:207], a[156:159], v[156:159], v[204:207]
s_nop 7
s_nop 7
s_lshl_b32 s96, s7, 13
s_add_u32 s96, s96, 16777216
s_add_u32 s97, s96, 4096
buffer_load_dwordx4 a[128:131], v255, s[92:95], s96 offen offset:0 sc1
buffer_load_dwordx4 a[132:135], v255, s[92:95], s96 offen offset:1024 sc1
buffer_load_dwordx4 a[136:139], v255, s[92:95], s96 offen offset:2048 sc1
buffer_load_dwordx4 a[140:143], v255, s[92:95], s96 offen offset:3072 sc1
buffer_load_dwordx4 a[144:147], v255, s[92:95], s97 offen offset:0 sc1
buffer_load_dwordx4 a[148:151], v255, s[92:95], s97 offen offset:1024 sc1
buffer_load_dwordx4 a[152:155], v255, s[92:95], s97 offen offset:2048 sc1
buffer_load_dwordx4 a[156:159], v255, s[92:95], s97 offen offset:3072 sc1
v_cmp_gt_i32 vcc, 0, v220
s_nop 1
v_cndmask_b32 v192, v192, v222, vcc
v_cmp_gt_i32 vcc, 1, v220
s_nop 1
v_cndmask_b32 v193, v193, v222, vcc
v_cmp_gt_i32 vcc, 2, v220
s_nop 1
v_cndmask_b32 v194, v194, v222, vcc
v_cmp_gt_i32 vcc, 3, v220
s_nop 1
v_cndmask_b32 v195, v195, v222, vcc
v_cmp_gt_i32 vcc, 16, v220
s_nop 1
v_cndmask_b32 v196, v196, v222, vcc
v_cmp_gt_i32 vcc, 17, v220
s_nop 1
v_cndmask_b32 v197, v197, v222, vcc
v_cmp_gt_i32 vcc, 18, v220
s_nop 1
v_cndmask_b32 v198, v198, v222, vcc
v_cmp_gt_i32 vcc, 19, v220
s_nop 1
v_cndmask_b32 v199, v199, v222, vcc
v_cmp_gt_i32 vcc, -16, v220
s_nop 1
v_cndmask_b32 v200, v200, v222, vcc
v_cmp_gt_i32 vcc, -15, v220
s_nop 1
v_cndmask_b32 v201, v201, v222, vcc
v_cmp_gt_i32 vcc, -14, v220
s_nop 1
v_cndmask_b32 v202, v202, v222, vcc
v_cmp_gt_i32 vcc, -13, v220
s_nop 1
v_cndmask_b32 v203, v203, v222, vcc
v_cmp_gt_i32 vcc, 0, v220
s_nop 1
v_cndmask_b32 v204, v204, v222, vcc
v_cmp_gt_i32 vcc, 1, v220
s_nop 1
v_cndmask_b32 v205, v205, v222, vcc
v_cmp_gt_i32 vcc, 2, v220
s_nop 1
v_cndmask_b32 v206, v206, v222, vcc
v_cmp_gt_i32 vcc, 3, v220
s_nop 1
v_cndmask_b32 v207, v207, v222, vcc
v_max3_f32 v222, v192, v193, v216
v_max3_f32 v222, v194, v195, v222
v_max3_f32 v222, v196, v197, v222
v_max3_f32 v222, v198, v199, v222
v_mov_b32 v221, v222
s_nop 1
v_permlane16_swap_b32 v221, v222
s_nop 1
v_max_f32 v222, v222, v221
v_mov_b32 v221, v222
s_nop 1
v_permlane32_swap_b32 v221, v222
s_nop 1
v_max_f32 v222, v222, v221
v_sub_f32 v223, v216, v222
v_mov_b32 v216, v222
v_exp_f32 v223, v223
v_sub_f32 v192, v192, v222
v_sub_f32 v193, v193, v222
v_sub_f32 v194, v194, v222
v_sub_f32 v195, v195, v222
v_sub_f32 v196, v196, v222
v_sub_f32 v197, v197, v222
v_sub_f32 v198, v198, v222
v_sub_f32 v199, v199, v222
v_exp_f32 v192, v192
v_exp_f32 v193, v193
v_exp_f32 v194, v194
v_exp_f32 v195, v195
v_exp_f32 v196, v196
v_exp_f32 v197, v197
v_exp_f32 v198, v198
v_exp_f32 v199, v199
v_mul_f32 v218, v218, v223
v_add_f32 v218, v218, v192
v_add_f32 v218, v218, v193
v_add_f32 v218, v218, v194
v_add_f32 v218, v218, v195
v_add_f32 v218, v218, v196
v_add_f32 v218, v218, v197
v_add_f32 v218, v218, v198
v_add_f32 v218, v218, v199
v_cvt_pk_bf16_f32 v208, v192, v193
v_cvt_pk_bf16_f32 v209, v194, v195
v_cvt_pk_bf16_f32 v210, v196, v197
v_cvt_pk_bf16_f32 v211, v198, v199
v_mul_f32 v0, v0, v223
v_mul_f32 v1, v1, v223
v_mul_f32 v2, v2, v223
v_mul_f32 v3, v3, v223
v_mul_f32 v4, v4, v223
v_mul_f32 v5, v5, v223
v_mul_f32 v6, v6, v223
v_mul_f32 v7, v7, v223
v_mul_f32 v8, v8, v223
v_mul_f32 v9, v9, v223
v_mul_f32 v10, v10, v223
v_mul_f32 v11, v11, v223
v_mul_f32 v12, v12, v223
v_mul_f32 v13, v13, v223
v_mul_f32 v14, v14, v223
v_mul_f32 v15, v15, v223
v_mul_f32 v16, v16, v223
v_mul_f32 v17, v17, v223
v_mul_f32 v18, v18, v223
v_mul_f32 v19, v19, v223
v_mul_f32 v20, v20, v223
v_mul_f32 v21, v21, v223
v_mul_f32 v22, v22, v223
v_mul_f32 v23, v23, v223
v_mul_f32 v24, v24, v223
v_mul_f32 v25, v25, v223
v_mul_f32 v26, v26, v223
v_mul_f32 v27, v27, v223
v_mul_f32 v28, v28, v223
v_mul_f32 v29, v29, v223
v_mul_f32 v30, v30, v223
v_mul_f32 v31, v31, v223
v_max3_f32 v222, v200, v201, v217
v_max3_f32 v222, v202, v203, v222
v_max3_f32 v222, v204, v205, v222
v_max3_f32 v222, v206, v207, v222
v_mov_b32 v221, v222
s_nop 1
v_permlane16_swap_b32 v221, v222
s_nop 1
v_max_f32 v222, v222, v221
v_mov_b32 v221, v222
s_nop 1
v_permlane32_swap_b32 v221, v222
s_nop 1
v_max_f32 v222, v222, v221
v_sub_f32 v223, v217, v222
v_mov_b32 v217, v222
v_exp_f32 v223, v223
v_sub_f32 v200, v200, v222
v_sub_f32 v201, v201, v222
v_sub_f32 v202, v202, v222
v_sub_f32 v203, v203, v222
v_sub_f32 v204, v204, v222
v_sub_f32 v205, v205, v222
v_sub_f32 v206, v206, v222
v_sub_f32 v207, v207, v222
v_exp_f32 v200, v200
v_exp_f32 v201, v201
v_exp_f32 v202, v202
v_exp_f32 v203, v203
v_exp_f32 v204, v204
v_exp_f32 v205, v205
v_exp_f32 v206, v206
v_exp_f32 v207, v207
v_mul_f32 v219, v219, v223
v_add_f32 v219, v219, v200
v_add_f32 v219, v219, v201
v_add_f32 v219, v219, v202
v_add_f32 v219, v219, v203
v_add_f32 v219, v219, v204
v_add_f32 v219, v219, v205
v_add_f32 v219, v219, v206
v_add_f32 v219, v219, v207
v_cvt_pk_bf16_f32 v212, v200, v201
v_cvt_pk_bf16_f32 v213, v202, v203
v_cvt_pk_bf16_f32 v214, v204, v205
v_cvt_pk_bf16_f32 v215, v206, v207
v_mul_f32 v32, v32, v223
v_mul_f32 v33, v33, v223
v_mul_f32 v34, v34, v223
v_mul_f32 v35, v35, v223
v_mul_f32 v36, v36, v223
v_mul_f32 v37, v37, v223
v_mul_f32 v38, v38, v223
v_mul_f32 v39, v39, v223
v_mul_f32 v40, v40, v223
v_mul_f32 v41, v41, v223
v_mul_f32 v42, v42, v223
v_mul_f32 v43, v43, v223
v_mul_f32 v44, v44, v223
v_mul_f32 v45, v45, v223
v_mul_f32 v46, v46, v223
v_mul_f32 v47, v47, v223
v_mul_f32 v48, v48, v223
v_mul_f32 v49, v49, v223
v_mul_f32 v50, v50, v223
v_mul_f32 v51, v51, v223
v_mul_f32 v52, v52, v223
v_mul_f32 v53, v53, v223
v_mul_f32 v54, v54, v223
v_mul_f32 v55, v55, v223
v_mul_f32 v56, v56, v223
v_mul_f32 v57, v57, v223
v_mul_f32 v58, v58, v223
v_mul_f32 v59, v59, v223
v_mul_f32 v60, v60, v223
v_mul_f32 v61, v61, v223
v_mul_f32 v62, v62, v223
v_mul_f32_e64 v63, v63, v223
s_waitcnt vmcnt(0)
v_mfma_f32_16x16x32_bf16 v[0:3], a[128:131], v[208:211], v[0:3]
v_mfma_f32_16x16x32_bf16 v[32:35], a[128:131], v[212:215], v[32:35]
v_mfma_f32_16x16x32_bf16 v[4:7], a[132:135], v[208:211], v[4:7]
v_mfma_f32_16x16x32_bf16 v[36:39], a[132:135], v[212:215], v[36:39]
v_mfma_f32_16x16x32_bf16 v[8:11], a[136:139], v[208:211], v[8:11]
v_mfma_f32_16x16x32_bf16 v[40:43], a[136:139], v[212:215], v[40:43]
v_mfma_f32_16x16x32_bf16 v[12:15], a[140:143], v[208:211], v[12:15]
v_mfma_f32_16x16x32_bf16 v[44:47], a[140:143], v[212:215], v[44:47]
v_mfma_f32_16x16x32_bf16 v[16:19], a[144:147], v[208:211], v[16:19]
v_mfma_f32_16x16x32_bf16 v[48:51], a[144:147], v[212:215], v[48:51]
v_mfma_f32_16x16x32_bf16 v[20:23], a[148:151], v[208:211], v[20:23]
v_mfma_f32_16x16x32_bf16 v[52:55], a[148:151], v[212:215], v[52:55]
v_mfma_f32_16x16x32_bf16 v[24:27], a[152:155], v[208:211], v[24:27]
v_mfma_f32_16x16x32_bf16 v[56:59], a[152:155], v[212:215], v[56:59]
v_mfma_f32_16x16x32_bf16 v[28:31], a[156:159], v[208:211], v[28:31]
v_mfma_f32_16x16x32_bf16 v[60:63], a[156:159], v[212:215], v[60:63]
s_nop 7
s_nop 7
s_add_u32 s7, s7, 1
s_lshr_b32 s96, s7, 3
s_cmp_le_u32 s96, s76
s_cbranch_scc1 .La5_LA_SLOW0_L
v_mov_b32 v221, v218
s_nop 1
v_permlane16_swap_b32 v221, v218
s_nop 1
v_add_f32 v218, v218, v221
v_mov_b32 v221, v218
s_nop 1
v_permlane32_swap_b32 v221, v218
s_nop 1
v_add_f32 v218, v218, v221
v_mov_b32 v221, v219
s_nop 1
v_permlane16_swap_b32 v221, v219
s_nop 1
v_add_f32 v219, v219, v221
v_mov_b32 v221, v219
s_nop 1
v_permlane32_swap_b32 v221, v219
s_nop 1
v_add_f32 v219, v219, v221
v_rcp_f32 v242, v218
v_rcp_f32 v243, v219
v_accvgpr_read_b32 v192, a160
v_accvgpr_read_b32 v193, a161
v_accvgpr_read_b32 v194, a162
v_accvgpr_read_b32 v195, a163
v_accvgpr_read_b32 v196, a164
v_accvgpr_read_b32 v197, a165
v_accvgpr_read_b32 v198, a166
v_accvgpr_read_b32 v199, a167
v_accvgpr_read_b32 v200, a168
v_accvgpr_read_b32 v201, a169
v_accvgpr_read_b32 v202, a170
v_accvgpr_read_b32 v203, a171
v_accvgpr_read_b32 v204, a172
v_accvgpr_read_b32 v205, a173
v_accvgpr_read_b32 v206, a174
v_accvgpr_read_b32 v207, a175
v_accvgpr_read_b32 v208, a176
v_accvgpr_read_b32 v209, a177
v_accvgpr_read_b32 v210, a178
v_accvgpr_read_b32 v211, a179
v_accvgpr_read_b32 v212, a180
v_accvgpr_read_b32 v213, a181
v_accvgpr_read_b32 v214, a182
v_accvgpr_read_b32 v215, a183
v_accvgpr_read_b32 v216, a184
v_accvgpr_read_b32 v217, a185
v_accvgpr_read_b32 v218, a186
v_accvgpr_read_b32 v219, a187
v_accvgpr_read_b32 v220, a188
v_accvgpr_read_b32 v221, a189
v_accvgpr_read_b32 v222, a190
v_accvgpr_read_b32 v223, a191
s_mov_b32 s95, 4096
s_mov_b32 s96, 8192
s_mov_b32 s97, 12288
s_mov_b64 vcc, 0
s_nop 1
s_setpc_b64 s[24:25]
; ---- fallback: the later 32 rows
.La5_LA_SLOW1:
s_waitcnt lgkmcnt(0)
s_nop 7
s_nop 7
v_accvgpr_write_b32 a160, v192
v_accvgpr_write_b32 a161, v193
v_accvgpr_write_b32 a162, v194
v_accvgpr_write_b32 a163, v195
v_accvgpr_write_b32 a164, v196
v_accvgpr_write_b32 a165, v197
v_accvgpr_write_b32 a166, v198
v_accvgpr_write_b32 a167, v199
v_accvgpr_write_b32 a168, v200
v_accvgpr_write_b32 a169, v201
v_accvgpr_write_b32 a170, v202
v_accvgpr_write_b32 a171, v203
v_accvgpr_write_b32 a172, v204
v_accvgpr_write_b32 a173, v205
v_accvgpr_write_b32 a174, v206
v_accvgpr_write_b32 a175, v207
v_accvgpr_write_b32 a176, v208
v_accvgpr_write_b32 a177, v209
v_accvgpr_write_b32 a178, v210
v_accvgpr_write_b32 a179, v211
v_accvgpr_write_b32 a180, v212
v_accvgpr_write_b32 a181, v213
v_accvgpr_write_b32 a182, v214
v_accvgpr_write_b32 a183, v215
v_accvgpr_write_b32 a184, v216
v_accvgpr_write_b32 a185, v217
v_accvgpr_write_b32 a186, v218
v_accvgpr_write_b32 a187, v219
v_accvgpr_write_b32 a188, v220
v_accvgpr_write_b32 a189, v221
v_accvgpr_write_b32 a190, v222
v_accvgpr_write_b32 a191, v223
s_lshl_b32 s96, s72, 19
s_add_u32 s96, s96, 20971520
s_add_u32 s92, s26, s96
s_addc_u32 s93, s27, 0
s_and_b32 s93, s93, 0xffff
s_mov_b32 s94, 17301504
s_mov_b32 s95, 0x20000
v_mov_b32 v64, 0
v_mov_b32 v65, 0
v_mov_b32 v66, 0
v_mov_b32 v67, 0
v_mov_b32 v68, 0
v_mov_b32 v69, 0
v_mov_b32 v70, 0
v_mov_b32 v71, 0
v_mov_b32 v72, 0
v_mov_b32 v73, 0
v_mov_b32 v74, 0
v_mov_b32 v75, 0
v_mov_b32 v76, 0
v_mov_b32 v77, 0
v_mov_b32 v78, 0
v_mov_b32 v79, 0
v_mov_b32 v80, 0
v_mov_b32 v81, 0
v_mov_b32 v82, 0
v_mov_b32 v83, 0
v_mov_b32 v84, 0
v_mov_b32 v85, 0
v_mov_b32 v86, 0
v_mov_b32 v87, 0
v_mov_b32 v88, 0
v_mov_b32 v89, 0
v_mov_b32 v90, 0
v_mov_b32 v91, 0
v_mov_b32 v92, 0
v_mov_b32 v93, 0
v_mov_b32 v94, 0
v_mov_b32 v95, 0
v_mov_b32 v96, 0
v_mov_b32 v97, 0
v_mov_b32 v98, 0
v_mov_b32 v99, 0
v_mov_b32 v100, 0
v_mov_b32 v101, 0
v_mov_b32 v102, 0
v_mov_b32 v103, 0
v_mov_b32 v104, 0
v_mov_b32 v105, 0
v_mov_b32 v106, 0
v_mov_b32 v107, 0
v_mov_b32 v108, 0
v_mov_b32 v109, 0
v_mov_b32 v110, 0
v_mov_b32 v111, 0
v_mov_b32 v112, 0
v_mov_b32 v113, 0
v_mov_b32 v114, 0
v_mov_b32 v115, 0
v_mov_b32 v116, 0
v_mov_b32 v117, 0
v_mov_b32 v118, 0
v_mov_b32 v119, 0
v_mov_b32 v120, 0
v_mov_b32 v121, 0
v_mov_b32 v122, 0
v_mov_b32 v123, 0
v_mov_b32 v124, 0
v_mov_b32 v125, 0
v_mov_b32 v126, 0
v_mov_b32 v127, 0
v_mov_b32 v216, 0xff61b1e6
v_mov_b32 v217, 0xff61b1e6
v_mov_b32 v218, 0
v_mov_b32 v219, 0
s_mov_b32 s7, 0
.La5_LA_SLOW1_L:
s_lshl_b32 s96, s7, 13
s_add_u32 s97, s96, 4096
buffer_load_dwordx4 a[128:131], v255, s[92:95], s96 offen offset:0 sc1
buffer_load_dwordx4 a[132:135], v255, s[92:95], s96 offen offset:1024 sc1
buffer_load_dwordx4 a[136:139], v255, s[92:95], s96 offen offset:2048 sc1
buffer_load_dwordx4 a[140:143], v255, s[92:95], s96 offen offset:3072 sc1
buffer_load_dwordx4 a[144:147], v255, s[92:95], s97 offen offset:0 sc1
buffer_load_dwordx4 a[148:151], v255, s[92:95], s97 offen offset:1024 sc1
buffer_load_dwordx4 a[152:155], v255, s[92:95], s97 offen offset:2048 sc1
buffer_load_dwordx4 a[156:159], v255, s[92:95], s97 offen offset:3072 sc1
s_lshl_b32 s96, s76, 3
s_sub_u32 s96, s96, s7
s_sub_u32 s96, s96, s3
s_add_u32 s96, s96, 7
s_lshl_b32 s96, s96, 5
v_bfe_u32 v220, v255, 4, 4
v_lshrrev_b32 v221, 8, v255
v_lshlrev_b32 v221, 2, v221
v_sub_u32 v220, v220, v221
v_add_u32 v220, s96, v220
v_mov_b32 v222, 0xceabfb8f
s_waitcnt vmcnt(0)
v_mfma_f32_16x16x32_bf16 v[192:195], a[128:131], v[160:163], 0
v_mfma_f32_16x16x32_bf16 v[192:195], a[132:135], v[164:167], v[192:195]
v_mfma_f32_16x16x32_bf16 v[192:195], a[136:139], v[168:171], v[192:195]
v_mfma_f32_16x16x32_bf16 v[192:195], a[140:143], v[172:175], v[192:195]
v_mfma_f32_16x16x32_bf16 v[196:199], a[144:147], v[160:163], 0
v_mfma_f32_16x16x32_bf16 v[196:199], a[148:151], v[164:167], v[196:199]
v_mfma_f32_16x16x32_bf16 v[196:199], a[152:155], v[168:171], v[196:199]
v_mfma_f32_16x16x32_bf16 v[196:199], a[156:159], v[172:175], v[196:199]
v_mfma_f32_16x16x32_bf16 v[200:203], a[128:131], v[176:179], 0
v_mfma_f32_16x16x32_bf16 v[200:203], a[132:135], v[180:183], v[200:203]
v_mfma_f32_16x16x32_bf16 v[200:203], a[136:139], v[184:187], v[200:203]
v_mfma_f32_16x16x32_bf16 v[200:203], a[140:143], v[188:191], v[200:203]
v_mfma_f32_16x16x32_bf16 v[204:207], a[144:147], v[176:179], 0
v_mfma_f32_16x16x32_bf16 v[204:207], a[148:151], v[180:183], v[204:207]
v_mfma_f32_16x16x32_bf16 v[204:207], a[152:155], v[184:187], v[204:207]
v_mfma_f32_16x16x32_bf16 v[204:207], a[156:159], v[188:191], v[204:207]
s_nop 7
s_nop 7
s_lshl_b32 s96, s7, 13
s_add_u32 s96, s96, 16777216
s_add_u32 s97, s96, 4096
buffer_load_dwordx4 a[128:131], v255, s[92:95], s96 offen offset:0 sc1
buffer_load_dwordx4 a[132:135], v255, s[92:95], s96 offen offset:1024 sc1
buffer_load_dwordx4 a[136:139], v255, s[92:95], s96 offen offset:2048 sc1
buffer_load_dwordx4 a[140:143], v255, s[92:95], s96 offen offset:3072 sc1
buffer_load_dwordx4 a[144:147], v255, s[92:95], s97 offen offset:0 sc1
buffer_load_dwordx4 a[148:151], v255, s[92:95], s97 offen offset:1024 sc1
buffer_load_dwordx4 a[152:155], v255, s[92:95], s97 offen offset:2048 sc1
buffer_load_dwordx4 a[156:159], v255, s[92:95], s97 offen offset:3072 sc1
v_cmp_gt_i32 vcc, 0, v220
s_nop 1
v_cndmask_b32 v192, v192, v222, vcc
v_cmp_gt_i32 vcc, 1, v220
s_nop 1
v_cndmask_b32 v193, v193, v222, vcc
v_cmp_gt_i32 vcc, 2, v220
s_nop 1
v_cndmask_b32 v194, v194, v222, vcc
v_cmp_gt_i32 vcc, 3, v220
s_nop 1
v_cndmask_b32 v195, v195, v222, vcc
v_cmp_gt_i32 vcc, 16, v220
s_nop 1
v_cndmask_b32 v196, v196, v222, vcc
v_cmp_gt_i32 vcc, 17, v220
s_nop 1
v_cndmask_b32 v197, v197, v222, vcc
v_cmp_gt_i32 vcc, 18, v220
s_nop 1
v_cndmask_b32 v198, v198, v222, vcc
v_cmp_gt_i32 vcc, 19, v220
s_nop 1
v_cndmask_b32 v199, v199, v222, vcc
v_cmp_gt_i32 vcc, -16, v220
s_nop 1
v_cndmask_b32 v200, v200, v222, vcc
v_cmp_gt_i32 vcc, -15, v220
s_nop 1
v_cndmask_b32 v201, v201, v222, vcc
v_cmp_gt_i32 vcc, -14, v220
s_nop 1
v_cndmask_b32 v202, v202, v222, vcc
v_cmp_gt_i32 vcc, -13, v220
s_nop 1
v_cndmask_b32 v203, v203, v222, vcc
v_cmp_gt_i32 vcc, 0, v220
s_nop 1
v_cndmask_b32 v204, v204, v222, vcc
v_cmp_gt_i32 vcc, 1, v220
s_nop 1
v_cndmask_b32 v205, v205, v222, vcc
v_cmp_gt_i32 vcc, 2, v220
s_nop 1
v_cndmask_b32 v206, v206, v222, vcc
v_cmp_gt_i32 vcc, 3, v220
s_nop 1
v_cndmask_b32 v207, v207, v222, vcc
v_max3_f32 v222, v192, v193, v216
v_max3_f32 v222, v194, v195, v222
v_max3_f32 v222, v196, v197, v222
v_max3_f32 v222, v198, v199, v222
v_mov_b32 v221, v222
s_nop 1
v_permlane16_swap_b32 v221, v222
s_nop 1
v_max_f32 v222, v222, v221
v_mov_b32 v221, v222
s_nop 1
v_permlane32_swap_b32 v221, v222
s_nop 1
v_max_f32 v222, v222, v221
v_sub_f32 v223, v216, v222
v_mov_b32 v216, v222
v_exp_f32 v223, v223
v_sub_f32 v192, v192, v222
v_sub_f32 v193, v193, v222
v_sub_f32 v194, v194, v222
v_sub_f32 v195, v195, v222
v_sub_f32 v196, v196, v222
v_sub_f32 v197, v197, v222
v_sub_f32 v198, v198, v222
v_sub_f32 v199, v199, v222
v_exp_f32 v192, v192
v_exp_f32 v193, v193
v_exp_f32 v194, v194
v_exp_f32 v195, v195
v_exp_f32 v196, v196
v_exp_f32 v197, v197
v_exp_f32 v198, v198
v_exp_f32 v199, v199
v_mul_f32 v218, v218, v223
v_add_f32 v218, v218, v192
v_add_f32 v218, v218, v193
v_add_f32 v218, v218, v194
v_add_f32 v218, v218, v195
v_add_f32 v218, v218, v196
v_add_f32 v218, v218, v197
v_add_f32 v218, v218, v198
v_add_f32 v218, v218, v199
v_cvt_pk_bf16_f32 v208, v192, v193
v_cvt_pk_bf16_f32 v209, v194, v195
v_cvt_pk_bf16_f32 v210, v196, v197
v_cvt_pk_bf16_f32 v211, v198, v199
v_mul_f32 v64, v64, v223
v_mul_f32 v65, v65, v223
v_mul_f32 v66, v66, v223
v_mul_f32 v67, v67, v223
v_mul_f32 v68, v68, v223
v_mul_f32 v69, v69, v223
v_mul_f32 v70, v70, v223
v_mul_f32 v71, v71, v223
v_mul_f32 v72, v72, v223
v_mul_f32 v73, v73, v223
v_mul_f32 v74, v74, v223
v_mul_f32 v75, v75, v223
v_mul_f32 v76, v76, v223
v_mul_f32 v77, v77, v223
v_mul_f32 v78, v78, v223
v_mul_f32 v79, v79, v223
v_mul_f32 v80, v80, v223
v_mul_f32 v81, v81, v223
v_mul_f32 v82, v82, v223
v_mul_f32 v83, v83, v223
v_mul_f32 v84, v84, v223
v_mul_f32 v85, v85, v223
v_mul_f32 v86, v86, v223
v_mul_f32 v87, v87, v223
v_mul_f32 v88, v88, v223
v_mul_f32 v89, v89, v223
v_mul_f32 v90, v90, v223
v_mul_f32 v91, v91, v223
v_mul_f32 v92, v92, v223
v_mul_f32 v93, v93, v223
v_mul_f32 v94, v94, v223
v_mul_f32 v95, v95, v223
v_max3_f32 v222, v200, v201, v217
v_max3_f32 v222, v202, v203, v222
v_max3_f32 v222, v204, v205, v222
v_max3_f32 v222, v206, v207, v222
v_mov_b32 v221, v222
s_nop 1
v_permlane16_swap_b32 v221, v222
s_nop 1
v_max_f32 v222, v222, v221
v_mov_b32 v221, v222
s_nop 1
v_permlane32_swap_b32 v221, v222
s_nop 1
v_max_f32 v222, v222, v221
v_sub_f32 v223, v217, v222
v_mov_b32 v217, v222
v_exp_f32 v223, v223
v_sub_f32 v200, v200, v222
v_sub_f32 v201, v201, v222
v_sub_f32 v202, v202, v222
v_sub_f32 v203, v203, v222
v_sub_f32 v204, v204, v222
v_sub_f32 v205, v205, v222
v_sub_f32 v206, v206, v222
v_sub_f32 v207, v207, v222
v_exp_f32 v200, v200
v_exp_f32 v201, v201
v_exp_f32 v202, v202
v_exp_f32 v203, v203
v_exp_f32 v204, v204
v_exp_f32 v205, v205
v_exp_f32 v206, v206
v_exp_f32 v207, v207
v_mul_f32 v219, v219, v223
v_add_f32 v219, v219, v200
v_add_f32 v219, v219, v201
v_add_f32 v219, v219, v202
v_add_f32 v219, v219, v203
v_add_f32 v219, v219, v204
v_add_f32 v219, v219, v205
v_add_f32 v219, v219, v206
v_add_f32 v219, v219, v207
v_cvt_pk_bf16_f32 v212, v200, v201
v_cvt_pk_bf16_f32 v213, v202, v203
v_cvt_pk_bf16_f32 v214, v204, v205
v_cvt_pk_bf16_f32 v215, v206, v207
v_mul_f32 v96, v96, v223
v_mul_f32 v97, v97, v223
v_mul_f32 v98, v98, v223
v_mul_f32 v99, v99, v223
v_mul_f32 v100, v100, v223
v_mul_f32 v101, v101, v223
v_mul_f32 v102, v102, v223
v_mul_f32 v103, v103, v223
v_mul_f32 v104, v104, v223
v_mul_f32 v105, v105, v223
v_mul_f32 v106, v106, v223
v_mul_f32 v107, v107, v223
v_mul_f32 v108, v108, v223
v_mul_f32 v109, v109, v223
v_mul_f32 v110, v110, v223
v_mul_f32 v111, v111, v223
v_mul_f32 v112, v112, v223
v_mul_f32 v113, v113, v223
v_mul_f32 v114, v114, v223
v_mul_f32 v115, v115, v223
v_mul_f32 v116, v116, v223
v_mul_f32 v117, v117, v223
v_mul_f32 v118, v118, v223
v_mul_f32 v119, v119, v223
v_mul_f32 v120, v120, v223
v_mul_f32 v121, v121, v223
v_mul_f32 v122, v122, v223
v_mul_f32 v123, v123, v223
v_mul_f32 v124, v124, v223
v_mul_f32 v125, v125, v223
v_mul_f32 v126, v126, v223
v_mul_f32_e64 v127, v127, v223
s_waitcnt vmcnt(0)
v_mfma_f32_16x16x32_bf16 v[64:67], a[128:131], v[208:211], v[64:67]
v_mfma_f32_16x16x32_bf16 v[96:99], a[128:131], v[212:215], v[96:99]
v_mfma_f32_16x16x32_bf16 v[68:71], a[132:135], v[208:211], v[68:71]
v_mfma_f32_16x16x32_bf16 v[100:103], a[132:135], v[212:215], v[100:103]
v_mfma_f32_16x16x32_bf16 v[72:75], a[136:139], v[208:211], v[72:75]
v_mfma_f32_16x16x32_bf16 v[104:107], a[136:139], v[212:215], v[104:107]
v_mfma_f32_16x16x32_bf16 v[76:79], a[140:143], v[208:211], v[76:79]
v_mfma_f32_16x16x32_bf16 v[108:111], a[140:143], v[212:215], v[108:111]
v_mfma_f32_16x16x32_bf16 v[80:83], a[144:147], v[208:211], v[80:83]
v_mfma_f32_16x16x32_bf16 v[112:115], a[144:147], v[212:215], v[112:115]
v_mfma_f32_16x16x32_bf16 v[84:87], a[148:151], v[208:211], v[84:87]
v_mfma_f32_16x16x32_bf16 v[116:119], a[148:151], v[212:215], v[116:119]
v_mfma_f32_16x16x32_bf16 v[88:91], a[152:155], v[208:211], v[88:91]
v_mfma_f32_16x16x32_bf16 v[120:123], a[152:155], v[212:215], v[120:123]
v_mfma_f32_16x16x32_bf16 v[92:95], a[156:159], v[208:211], v[92:95]
v_mfma_f32_16x16x32_bf16 v[124:127], a[156:159], v[212:215], v[124:127]
s_nop 7
s_nop 7
s_add_u32 s7, s7, 1
s_lshr_b32 s96, s7, 3
s_cmp_le_u32 s96, s76
s_cbranch_scc1 .La5_LA_SLOW1_L
v_mov_b32 v221, v218
s_nop 1
v_permlane16_swap_b32 v221, v218
s_nop 1
v_add_f32 v218, v218, v221
v_mov_b32 v221, v218
s_nop 1
v_permlane32_swap_b32 v221, v218
s_nop 1
v_add_f32 v218, v218, v221
v_mov_b32 v221, v219
s_nop 1
v_permlane16_swap_b32 v221, v219
s_nop 1
v_add_f32 v219, v219, v221
v_mov_b32 v221, v219
s_nop 1
v_permlane32_swap_b32 v221, v219
s_nop 1
v_add_f32 v219, v219, v221
v_rcp_f32 v242, v218
v_rcp_f32 v243, v219
v_accvgpr_read_b32 v192, a160
v_accvgpr_read_b32 v193, a161
v_accvgpr_read_b32 v194, a162
v_accvgpr_read_b32 v195, a163
v_accvgpr_read_b32 v196, a164
v_accvgpr_read_b32 v197, a165
v_accvgpr_read_b32 v198, a166
v_accvgpr_read_b32 v199, a167
v_accvgpr_read_b32 v200, a168
v_accvgpr_read_b32 v201, a169
v_accvgpr_read_b32 v202, a170
v_accvgpr_read_b32 v203, a171
v_accvgpr_read_b32 v204, a172
v_accvgpr_read_b32 v205, a173
v_accvgpr_read_b32 v206, a174
v_accvgpr_read_b32 v207, a175
v_accvgpr_read_b32 v208, a176
v_accvgpr_read_b32 v209, a177
v_accvgpr_read_b32 v210, a178
v_accvgpr_read_b32 v211, a179
v_accvgpr_read_b32 v212, a180
v_accvgpr_read_b32 v213, a181
v_accvgpr_read_b32 v214, a182
v_accvgpr_read_b32 v215, a183
v_accvgpr_read_b32 v216, a184
v_accvgpr_read_b32 v217, a185
v_accvgpr_read_b32 v218, a186
v_accvgpr_read_b32 v219, a187
v_accvgpr_read_b32 v220, a188
v_accvgpr_read_b32 v221, a189
v_accvgpr_read_b32 v222, a190
v_accvgpr_read_b32 v223, a191
s_mov_b32 s95, 4096
s_mov_b32 s96, 8192
s_mov_b32 s97, 12288
s_mov_b64 vcc, 0
s_nop 1
s_setpc_b64 s[24:25]
.La5_LK_XSPIN:
s_mov_b32 s93, 0
.La5_LK_XSPIN_L:
s_add_u32 s93, s93, 1
s_cmp_gt_u32 s93, 2097152
s_cbranch_scc1 .La5_LK_XOK
s_sleep 8
buffer_load_dwordx4 v[72:75], v255, s[64:67], s89 offen offset:1024 sc1
s_waitcnt vmcnt(0)
v_cmp_eq_u64 vcc, v[72:73], v[252:253]
v_cmp_eq_u64 s[96:97], v[74:75], v[252:253]
s_nop 1
s_and_b64 vcc, vcc, s[96:97]
s_cmp_eq_u64 vcc, -1
s_nop 0
s_cbranch_scc0 .La5_LK_XSPIN_L
s_branch .La5_LK_XOK
.La5_LV_XSPIN:
s_mov_b32 s93, 0
.La5_LV_XSPIN_L:
s_add_u32 s93, s93, 1
s_cmp_gt_u32 s93, 2097152
s_nop 0
s_nop 0
s_cbranch_scc1 .La5_LV_XOK
s_sleep 8
buffer_load_dwordx4 v[72:75], v255, s[64:67], s89 offen offset:1024 sc1
s_waitcnt vmcnt(0)
v_cmp_eq_u64 vcc, v[72:73], v[252:253]
v_cmp_eq_u64 s[96:97], v[74:75], v[252:253]
s_nop 1
s_and_b64 vcc, vcc, s[96:97]
s_cmp_eq_u64 vcc, -1
s_cbranch_scc0 .La5_LV_XSPIN_L
s_branch .La5_LV_XOK
.La5_LA_SC1:
s_call_b64 s[24:25], .La5_LA_SLOW0
s_branch .La5_LA_SR1
.La5_LA_SC2:
s_nop 0
s_call_b64 s[24:25], .La5_LA_SLOW1
s_branch .La5_LA_SR2
.La5_LA_PS0:
s_mov_b32 s84, 3
s_mov_b32 s70, 17408
s_waitcnt vmcnt(2)
buffer_load_dwordx4 a[220:223], v255, s[68:71], s94 offen sc1
buffer_load_dwordx4 a[0:3], v255, s[68:71], 0 offen offset:0 sc1
buffer_load_dwordx4 a[4:7], v255, s[68:71], 0 offen offset:1024 sc1
buffer_load_dwordx4 a[8:11], v255, s[68:71], 0 offen offset:2048 sc1
buffer_load_dwordx4 a[12:15], v255, s[68:71], 0 offen offset:3072 sc1
buffer_load_dwordx4 a[16:19], v255, s[68:71], s95 offen offset:0 sc1
buffer_load_dwordx4 a[20:23], v255, s[68:71], s95 offen offset:1024 sc1
buffer_load_dwordx4 a[24:27], v255, s[68:71], s95 offen offset:2048 sc1
s_branch .La5_LA_PS1
.La5_LA_PS2:
s_mov_b32 s84, 3
s_mov_b32 s70, 17408
s_waitcnt vmcnt(0)
buffer_load_dwordx4 a[220:223], v255, s[68:71], s94 offen sc1
buffer_load_dwordx4 a[0:3], v255, s[68:71], 0 offen offset:0 sc1
buffer_load_dwordx4 a[4:7], v255, s[68:71], 0 offen offset:1024 sc1
buffer_load_dwordx4 a[8:11], v255, s[68:71], 0 offen offset:2048 sc1
buffer_load_dwordx4 a[12:15], v255, s[68:71], 0 offen offset:3072 sc1
buffer_load_dwordx4 a[16:19], v255, s[68:71], s95 offen offset:0 sc1
buffer_load_dwordx4 a[20:23], v255, s[68:71], s95 offen offset:1024 sc1
buffer_load_dwordx4 a[24:27], v255, s[68:71], s95 offen offset:2048 sc1
buffer_load_dwordx4 a[28:31], v255, s[68:71], s95 offen offset:3072 sc1
buffer_load_dwordx4 a[32:35], v255, s[68:71], s96 offen offset:0 sc1
buffer_load_dwordx4 a[36:39], v255, s[68:71], s96 offen offset:1024 sc1
buffer_load_dwordx4 a[40:43], v255, s[68:71], s96 offen offset:2048 sc1
buffer_load_dwordx4 a[44:47], v255, s[68:71], s96 offen offset:3072 sc1
buffer_load_dwordx4 a[48:51], v255, s[68:71], s97 offen offset:0 sc1
buffer_load_dwordx4 a[52:55], v255, s[68:71], s97 offen offset:1024 sc1
buffer_load_dwordx4 a[56:59], v255, s[68:71], s97 offen offset:2048 sc1
buffer_load_dwordx4 a[60:63], v255, s[68:71], s97 offen offset:3072 sc1
s_branch .La5_LA_PS3
.La5_LA_PSPIN:
s_mov_b32 s93, 0
.La5_LA_PSPIN_L:
s_add_u32 s93, s93, 1
s_cmp_gt_u32 s93, 2097152
s_cbranch_scc1 .La5_LA_PSOK
s_sleep 8
buffer_load_dword v250, v251, s[64:67], s20 offen sc1
s_waitcnt vmcnt(0)
v_cmp_eq_u32 vcc, v250, v249
s_nop 1
s_and_b32 s92, vcc_lo, 3
s_cmp_eq_u32 s92, 3
s_cbranch_scc0 .La5_LA_PSPIN_L
s_branch .La5_LA_PSOK
.La5_LA_PSOK:
s_mov_b32 s70, 17408
buffer_load_dwordx4 a[220:223], v255, s[68:71], s94 offen sc1
buffer_load_dwordx4 a[0:3], v255, s[68:71], 0 offen offset:0 sc1
buffer_load_dwordx4 a[4:7], v255, s[68:71], 0 offen offset:1024 sc1
buffer_load_dwordx4 a[8:11], v255, s[68:71], 0 offen offset:2048 sc1
buffer_load_dwordx4 a[12:15], v255, s[68:71], 0 offen offset:3072 sc1
buffer_load_dwordx4 a[16:19], v255, s[68:71], s95 offen offset:0 sc1
buffer_load_dwordx4 a[20:23], v255, s[68:71], s95 offen offset:1024 sc1
buffer_load_dwordx4 a[24:27], v255, s[68:71], s95 offen offset:2048 sc1
buffer_load_dwordx4 a[28:31], v255, s[68:71], s95 offen offset:3072 sc1
buffer_load_dwordx4 a[32:35], v255, s[68:71], s96 offen offset:0 sc1
buffer_load_dwordx4 a[36:39], v255, s[68:71], s96 offen offset:1024 sc1
buffer_load_dwordx4 a[40:43], v255, s[68:71], s96 offen offset:2048 sc1
buffer_load_dwordx4 a[44:47], v255, s[68:71], s96 offen offset:3072 sc1
buffer_load_dwordx4 a[48:51], v255, s[68:71], s97 offen offset:0 sc1
buffer_load_dwordx4 a[52:55], v255, s[68:71], s97 offen offset:1024 sc1
buffer_load_dwordx4 a[56:59], v255, s[68:71], s97 offen offset:2048 sc1
buffer_load_dwordx4 a[60:63], v255, s[68:71], s97 offen offset:3072 sc1
s_branch .La5_LA_DC1
.La5_LO_N1:
.La5_LW2_14:
s_cbranch_execz .La5_LW2_R0
s_mov_b32 s93, 0
.La5_LO_N1_L:
s_add_u32 s93, s93, 1
s_cmp_gt_u32 s93, 2097152
s_nop 0
s_cbranch_scc1 .La5_LO_K1
s_sleep 8
buffer_load_dwordx2 v[250:251], v248, s[64:67], s5 offen sc1
s_waitcnt vmcnt(0)
v_cmp_eq_u64 vcc, v[250:251], v[252:253]
s_nop 1
s_bitcmp1_b32 vcc_lo, 1
s_cbranch_scc0 .La5_LO_N1_L
s_branch .La5_LO_K1
.La5_LO_N2:
s_mov_b32 s93, 0
.La5_LO_N2_L:
s_add_u32 s93, s93, 1
s_cmp_gt_u32 s93, 2097152
s_nop 0
s_nop 0
s_cbranch_scc1 .La5_LO_K2
s_sleep 8
buffer_load_dwordx2 v[250:251], v248, s[64:67], s5 offen sc1
s_waitcnt vmcnt(0)
v_cmp_eq_u64 vcc, v[250:251], v[252:253]
s_nop 1
s_bitcmp1_b32 vcc_lo, 2
s_cbranch_scc0 .La5_LO_N2_L
s_branch .La5_LO_K2
.La5_LO_N3:
s_mov_b32 s93, 0
.La5_LO_N3_L:
s_add_u32 s93, s93, 1
s_cmp_gt_u32 s93, 2097152
s_nop 0
s_nop 0
s_cbranch_scc1 .La5_LO_K3
s_sleep 8
buffer_load_dwordx2 v[250:251], v248, s[64:67], s5 offen sc1
s_waitcnt vmcnt(0)
v_cmp_eq_u64 vcc, v[250:251], v[252:253]
s_nop 1
s_bitcmp1_b32 vcc_lo, 3
s_cbranch_scc0 .La5_LO_N3_L
s_branch .La5_LO_K3
.La5_LO_N4:
s_mov_b32 s93, 0
.La5_LO_N4_L:
s_add_u32 s93, s93, 1
s_cmp_gt_u32 s93, 2097152
s_nop 0
s_nop 0
s_cbranch_scc1 .La5_LO_K4
s_sleep 8
buffer_load_dwordx2 v[250:251], v248, s[64:67], s5 offen sc1
s_waitcnt vmcnt(0)
v_cmp_eq_u64 vcc, v[250:251], v[252:253]
s_nop 1
s_bitcmp1_b32 vcc_lo, 4
s_cbranch_scc0 .La5_LO_N4_L
s_branch .La5_LO_K4
.La5_LO_N5:
s_mov_b32 s93, 0
.La5_LO_N5_L:
s_add_u32 s93, s93, 1
s_cmp_gt_u32 s93, 2097152
s_nop 0
s_nop 0
s_cbranch_scc1 .La5_LO_K5
s_sleep 8
buffer_load_dwordx2 v[250:251], v248, s[64:67], s5 offen sc1
s_waitcnt vmcnt(0)
v_cmp_eq_u64 vcc, v[250:251], v[252:253]
s_nop 1
s_bitcmp1_b32 vcc_lo, 5
s_cbranch_scc0 .La5_LO_N5_L
s_branch .La5_LO_K5
.La5_LO_N6:
s_mov_b32 s93, 0
.La5_LO_N6_L:
s_add_u32 s93, s93, 1
s_cmp_gt_u32 s93, 2097152
s_nop 0
s_nop 0
s_cbranch_scc1 .La5_LO_K6
s_sleep 8
buffer_load_dwordx2 v[250:251], v248, s[64:67], s5 offen sc1
s_waitcnt vmcnt(0)
v_cmp_eq_u64 vcc, v[250:251], v[252:253]
s_nop 1
s_bitcmp1_b32 vcc_lo, 6
s_cbranch_scc0 .La5_LO_N6_L
s_branch .La5_LO_K6
.La5_LO_N7:
s_mov_b32 s93, 0
.La5_LO_N7_L:
s_add_u32 s93, s93, 1
s_cmp_gt_u32 s93, 2097152
s_nop 0
s_nop 0
s_cbranch_scc1 .La5_LO_K7
s_sleep 8
buffer_load_dwordx2 v[250:251], v248, s[64:67], s5 offen sc1
s_waitcnt vmcnt(0)
v_cmp_eq_u64 vcc, v[250:251], v[252:253]
s_nop 1
s_bitcmp1_b32 vcc_lo, 7
s_cbranch_scc0 .La5_LO_N7_L
s_branch .La5_LO_K7
.La5_LA_SC3:
s_call_b64 s[24:25], .La5_LA_SLOW0
v_accvgpr_write_b32 a0, 0
v_accvgpr_write_b32 a1, 0
v_accvgpr_write_b32 a2, 0
v_accvgpr_write_b32 a3, 0
v_accvgpr_write_b32 a4, 0
v_accvgpr_write_b32 a5, 0
v_accvgpr_write_b32 a6, 0
v_accvgpr_write_b32 a7, 0
v_accvgpr_write_b32 a8, 0
v_accvgpr_write_b32 a9, 0
v_accvgpr_write_b32 a10, 0
v_accvgpr_write_b32 a11, 0
v_accvgpr_write_b32 a12, 0
v_accvgpr_write_b32 a13, 0
v_accvgpr_write_b32 a14, 0
v_accvgpr_write_b32 a15, 0
v_accvgpr_write_b32 a16, 0
v_accvgpr_write_b32 a17, 0
v_accvgpr_write_b32 a18, 0
v_accvgpr_write_b32 a19, 0
v_accvgpr_write_b32 a20, 0
v_accvgpr_write_b32 a21, 0
v_accvgpr_write_b32 a22, 0
v_accvgpr_write_b32 a23, 0
v_accvgpr_write_b32 a24, 0
v_accvgpr_write_b32 a25, 0
v_accvgpr_write_b32 a26, 0
v_accvgpr_write_b32 a27, 0
v_accvgpr_write_b32 a28, 0
v_accvgpr_write_b32 a29, 0
v_accvgpr_write_b32 a30, 0
v_accvgpr_write_b32 a31, 0
s_nop 1
v_mov_b32 v250, v242
v_mov_b32 v249, v243
v_xor_b32 v242, 64, v240
v_xor_b32 v243, 96, v240
s_mov_b32 s93, 0
buffer_load_dwordx4 a[128:131], v254, s[40:43], s93 offen
s_mov_b32 s93, 7168
buffer_load_dwordx4 a[132:135], v254, s[40:43], s93 offen
s_mov_b32 s93, 14336
buffer_load_dwordx4 a[136:139], v254, s[40:43], s93 offen
s_mov_b32 s93, 21504
buffer_load_dwordx4 a[140:143], v254, s[40:43], s93 offen
s_mov_b32 s93, 28672
buffer_load_dwordx4 a[144:147], v254, s[40:43], s93 offen
s_mov_b32 s93, 35840
buffer_load_dwordx4 a[148:151], v254, s[40:43], s93 offen
s_mov_b32 s93, 43008
buffer_load_dwordx4 a[152:155], v254, s[40:43], s93 offen
s_mov_b32 s93, 50176
buffer_load_dwordx4 a[156:159], v254, s[40:43], s93 offen
s_mov_b32 s93, 57344
buffer_load_dwordx4 a[160:163], v254, s[40:43], s93 offen
s_mov_b32 s93, 64512
buffer_load_dwordx4 a[164:167], v254, s[40:43], s93 offen
s_mov_b32 s93, 71680
buffer_load_dwordx4 a[168:171], v254, s[40:43], s93 offen
s_mov_b32 s93, 78848
buffer_load_dwordx4 a[172:175], v254, s[40:43], s93 offen
s_mov_b32 s93, 86016
buffer_load_dwordx4 a[176:179], v254, s[40:43], s93 offen
s_mov_b32 s93, 93184
buffer_load_dwordx4 a[180:183], v254, s[40:43], s93 offen
s_mov_b32 s93, 100352
buffer_load_dwordx4 a[184:187], v254, s[40:43], s93 offen
s_mov_b32 s93, 107520
buffer_load_dwordx4 a[188:191], v254, s[40:43], s93 offen
s_branch .La5_LA_SR3
.La5_LA_SC4:
s_call_b64 s[24:25], .La5_LA_SLOW1
v_accvgpr_write_b32 a32, 0
v_accvgpr_write_b32 a33, 0
v_accvgpr_write_b32 a34, 0
v_accvgpr_write_b32 a35, 0
v_accvgpr_write_b32 a36, 0
v_accvgpr_write_b32 a37, 0
v_accvgpr_write_b32 a38, 0
v_accvgpr_write_b32 a39, 0
v_accvgpr_write_b32 a40, 0
v_accvgpr_write_b32 a41, 0
v_accvgpr_write_b32 a42, 0
v_accvgpr_write_b32 a43, 0
v_accvgpr_write_b32 a44, 0
v_accvgpr_write_b32 a45, 0
v_accvgpr_write_b32 a46, 0
v_accvgpr_write_b32 a47, 0
v_accvgpr_write_b32 a48, 0
v_accvgpr_write_b32 a49, 0
v_accvgpr_write_b32 a50, 0
v_accvgpr_write_b32 a51, 0
v_accvgpr_write_b32 a52, 0
v_accvgpr_write_b32 a53, 0
v_accvgpr_write_b32 a54, 0
v_accvgpr_write_b32 a55, 0
v_accvgpr_write_b32 a56, 0
v_accvgpr_write_b32 a57, 0
v_accvgpr_write_b32 a58, 0
v_accvgpr_write_b32 a59, 0
v_accvgpr_write_b32 a60, 0
v_accvgpr_write_b32 a61, 0
v_accvgpr_write_b32 a62, 0
v_accvgpr_write_b32 a63, 0
s_nop 1
v_mov_b32 v250, v242
v_mov_b32 v249, v243
v_xor_b32 v242, 64, v240
v_xor_b32 v243, 96, v240
s_mov_b32 s93, 0
buffer_load_dwordx4 a[128:131], v254, s[40:43], s93 offen
s_mov_b32 s93, 7168
buffer_load_dwordx4 a[132:135], v254, s[40:43], s93 offen
s_mov_b32 s93, 14336
buffer_load_dwordx4 a[136:139], v254, s[40:43], s93 offen
s_mov_b32 s93, 21504
buffer_load_dwordx4 a[140:143], v254, s[40:43], s93 offen
s_mov_b32 s93, 28672
buffer_load_dwordx4 a[144:147], v254, s[40:43], s93 offen
s_mov_b32 s93, 35840
buffer_load_dwordx4 a[148:151], v254, s[40:43], s93 offen
s_mov_b32 s93, 43008
buffer_load_dwordx4 a[152:155], v254, s[40:43], s93 offen
s_mov_b32 s93, 50176
buffer_load_dwordx4 a[156:159], v254, s[40:43], s93 offen
s_mov_b32 s93, 57344
buffer_load_dwordx4 a[160:163], v254, s[40:43], s93 offen
s_mov_b32 s93, 64512
buffer_load_dwordx4 a[164:167], v254, s[40:43], s93 offen
s_mov_b32 s93, 71680
buffer_load_dwordx4 a[168:171], v254, s[40:43], s93 offen
s_mov_b32 s93, 78848
buffer_load_dwordx4 a[172:175], v254, s[40:43], s93 offen
s_mov_b32 s93, 86016
buffer_load_dwordx4 a[176:179], v254, s[40:43], s93 offen
s_mov_b32 s93, 93184
buffer_load_dwordx4 a[180:183], v254, s[40:43], s93 offen
s_mov_b32 s93, 100352
buffer_load_dwordx4 a[184:187], v254, s[40:43], s93 offen
s_mov_b32 s93, 107520
buffer_load_dwordx4 a[188:191], v254, s[40:43], s93 offen
s_branch .La5_LA_SR4
.La5_LO_W:
s_mov_b32 s93, 114688
buffer_load_dwordx4 a[192:195], v228, s[40:43], s93 offen
s_mov_b32 s93, 121856
buffer_load_dwordx4 a[196:199], v228, s[40:43], s93 offen
s_mov_b32 s93, 129024
buffer_load_dwordx4 a[200:203], v228, s[40:43], s93 offen
s_mov_b32 s93, 136192
buffer_load_dwordx4 a[204:207], v228, s[40:43], s93 offen
s_mov_b32 s93, 143360
buffer_load_dwordx4 a[208:211], v228, s[40:43], s93 offen
s_mov_b32 s93, 150528
buffer_load_dwordx4 a[212:215], v228, s[40:43], s93 offen
s_mov_b32 s93, 157696
buffer_load_dwordx4 a[216:219], v228, s[40:43], s93 offen
s_mov_b32 s93, 164864
buffer_load_dwordx4 a[220:223], v228, s[40:43], s93 offen
s_mov_b32 s93, 172032
buffer_load_dwordx4 a[224:227], v228, s[40:43], s93 offen
s_mov_b32 s93, 179200
buffer_load_dwordx4 a[228:231], v228, s[40:43], s93 offen
s_mov_b32 s93, 186368
buffer_load_dwordx4 a[232:235], v228, s[40:43], s93 offen
s_mov_b32 s93, 193536
buffer_load_dwordx4 a[236:239], v228, s[40:43], s93 offen
s_mov_b32 s93, 200704
buffer_load_dwordx4 a[240:243], v228, s[40:43], s93 offen
s_mov_b32 s93, 207872
buffer_load_dwordx4 a[244:247], v228, s[40:43], s93 offen
s_mov_b32 s93, 215040
buffer_load_dwordx4 a[248:251], v228, s[40:43], s93 offen
s_mov_b32 s93, 222208
buffer_load_dwordx4 a[252:255], v228, s[40:43], s93 offen
s_branch .La5_LO_START
.La5_LT_VAR:
v_mov_b32 v230, 0
v_lshlrev_b32 v232, 16, v96
v_and_b32 v233, 0xffff0000, v96
v_sub_f32 v232, v232, v244
v_sub_f32 v233, v233, v244
v_fmac_f32 v230, v232, v232
v_fmac_f32 v230, v233, v233
v_lshlrev_b32 v232, 16, v97
v_and_b32 v233, 0xffff0000, v97
v_sub_f32 v232, v232, v244
v_sub_f32 v233, v233, v244
v_fmac_f32 v230, v232, v232
v_fmac_f32 v230, v233, v233
v_lshlrev_b32 v232, 16, v98
v_and_b32 v233, 0xffff0000, v98
v_sub_f32 v232, v232, v244
v_sub_f32 v233, v233, v244
v_fmac_f32 v230, v232, v232
v_fmac_f32 v230, v233, v233
v_lshlrev_b32 v232, 16, v99
v_and_b32 v233, 0xffff0000, v99
v_sub_f32 v232, v232, v244
v_sub_f32 v233, v233, v244
v_fmac_f32 v230, v232, v232
v_fmac_f32 v230, v233, v233
v_lshlrev_b32 v232, 16, v100
v_and_b32 v233, 0xffff0000, v100
v_sub_f32 v232, v232, v244
v_sub_f32 v233, v233, v244
v_fmac_f32 v230, v232, v232
v_fmac_f32 v230, v233, v233
v_lshlrev_b32 v232, 16, v101
v_and_b32 v233, 0xffff0000, v101
v_sub_f32 v232, v232, v244
v_sub_f32 v233, v233, v244
v_fmac_f32 v230, v232, v232
v_fmac_f32 v230, v233, v233
v_lshlrev_b32 v232, 16, v102
v_and_b32 v233, 0xffff0000, v102
v_sub_f32 v232, v232, v244
v_sub_f32 v233, v233, v244
v_fmac_f32 v230, v232, v232
v_fmac_f32 v230, v233, v233
v_lshlrev_b32 v232, 16, v103
v_and_b32 v233, 0xffff0000, v103
v_sub_f32 v232, v232, v244
v_sub_f32 v233, v233, v244
v_fmac_f32 v230, v232, v232
v_fmac_f32 v230, v233, v233
v_lshlrev_b32 v232, 16, v104
v_and_b32 v233, 0xffff0000, v104
v_sub_f32 v232, v232, v244
v_sub_f32 v233, v233, v244
v_fmac_f32 v230, v232, v232
v_fmac_f32 v230, v233, v233
v_lshlrev_b32 v232, 16, v105
v_and_b32 v233, 0xffff0000, v105
v_sub_f32 v232, v232, v244
v_sub_f32 v233, v233, v244
v_fmac_f32 v230, v232, v232
v_fmac_f32 v230, v233, v233
v_lshlrev_b32 v232, 16, v106
v_and_b32 v233, 0xffff0000, v106
v_sub_f32 v232, v232, v244
v_sub_f32 v233, v233, v244
v_fmac_f32 v230, v232, v232
v_fmac_f32 v230, v233, v233
v_lshlrev_b32 v232, 16, v107
v_and_b32 v233, 0xffff0000, v107
v_sub_f32 v232, v232, v244
v_sub_f32 v233, v233, v244
v_fmac_f32 v230, v232, v232
v_fmac_f32 v230, v233, v233
v_lshlrev_b32 v232, 16, v108
v_and_b32 v233, 0xffff0000, v108
v_sub_f32 v232, v232, v244
v_sub_f32 v233, v233, v244
v_fmac_f32 v230, v232, v232
v_fmac_f32 v230, v233, v233
v_lshlrev_b32 v232, 16, v109
v_and_b32 v233, 0xffff0000, v109
v_sub_f32 v232, v232, v244
v_sub_f32 v233, v233, v244
v_fmac_f32 v230, v232, v232
v_fmac_f32 v230, v233, v233
v_lshlrev_b32 v232, 16, v110
v_and_b32 v233, 0xffff0000, v110
v_sub_f32 v232, v232, v244
v_sub_f32 v233, v233, v244
v_fmac_f32 v230, v232, v232
v_fmac_f32 v230, v233, v233
v_lshlrev_b32 v232, 16, v111
v_and_b32 v233, 0xffff0000, v111
v_sub_f32 v232, v232, v244
v_sub_f32 v233, v233, v244
v_fmac_f32 v230, v232, v232
v_fmac_f32 v230, v233, v233
s_nop 1
v_add_f32_dpp v230, v230, v230 quad_perm:[1,0,3,2] row_mask:0xf bank_mask:0xf
s_nop 1
v_add_f32_dpp v230, v230, v230 quad_perm:[2,3,0,1] row_mask:0xf bank_mask:0xf
s_nop 1
v_add_f32_dpp v230, v230, v230 row_ror:8 row_mask:0xf bank_mask:0xf
v_mov_b32 v231, v230
s_nop 1
v_permlane16_swap_b32 v231, v230
s_nop 1
v_add_f32 v230, v230, v231
v_mov_b32 v231, v230
s_nop 1
v_permlane32_swap_b32 v231, v230
s_nop 1
v_add_f32 v230, v230, v231
v_mul_f32 v230, 0x3a800000, v230
s_branch .La5_LT_VOK
.La5_LA_KSPIN:
s_mov_b32 s93, 0
.La5_LA_KSPIN_L:
s_add_u32 s93, s93, 1
s_cmp_gt_u32 s93, 2097152
s_cbranch_scc1 .La5_LA_KOK
s_sleep 8
buffer_load_dword v250, v251, s[64:67], s20 offen sc1
s_waitcnt vmcnt(0)
v_cmp_eq_u32 vcc, v250, v249
s_nop 1
s_or_b32 vcc_hi, vcc_hi, s79
s_cmp_eq_u64 vcc, -1
s_cbranch_scc0 .La5_LA_KSPIN_L
s_branch .La5_LA_KOK
.La5_LA_QSPIN:
s_mov_b32 s93, 0
.La5_LA_QSPIN_L:
s_add_u32 s93, s93, 1
s_cmp_gt_u32 s93, 2097152
s_cbranch_scc1 .La5_LA_QOK
s_sleep 8
buffer_load_dword v250, v251, s[64:67], s20 offen sc1
s_waitcnt vmcnt(0)
v_cmp_eq_u32 vcc, v250, v249
s_nop 1
s_and_b32 s92, vcc_lo, 3
s_cmp_eq_u32 s92, 3
s_cbranch_scc0 .La5_LA_QSPIN_L
s_branch .La5_LA_QOK
.La5_LEND:
.La5_LCODEEND:
)ASM"
        :: "{s[0:1]}"(__builtin_amdgcn_kernarg_segment_ptr()), "{s2}"(blockIdx.x), "{s3}"(__builtin_amdgcn_readfirstlane(threadIdx.x >> 6)), "{s[4:5]}"(DBGP)
        : "v0","v1","v2","v3","v4","v5","v6","v7","v8","v9","v10","v11","v12","v13","v14","v15","v16","v17","v18","v19","v20","v21","v22","v23","v24","v25","v26","v27","v28","v29","v30","v31","v32","v33","v34","v35","v36","v37","v38","v39","v40","v41","v42","v43","v44","v45","v46","v47","v48","v49","v50","v51","v52","v53","v54","v55","v56","v57","v58","v59","v60","v61","v62","v63","v64","v65","v66","v67","v68","v69","v70","v71","v72","v73","v74","v75","v76","v77","v78","v79","v80","v81","v82","v83","v84","v85","v86","v87","v88","v89","v90","v91","v92","v93","v94","v95","v96","v97","v98","v99","v100","v101","v102","v103","v104","v105","v106","v107","v108","v109","v110","v111","v112","v113","v114","v115","v116","v117","v118","v119","v120","v121","v122","v123","v124","v125","v126","v127","v128","v129","v130","v131","v132","v133","v134","v135","v136","v137","v138","v139","v140","v141","v142","v143","v144","v145","v146","v147","v148","v149","v150","v151","v152","v153","v154","v155","v156","v157","v158","v159","v160","v161","v162","v163","v164","v165","v166","v167","v168","v169","v170","v171","v172","v173","v174","v175","v176","v177","v178","v179","v180","v181","v182","v183","v184","v185","v186","v187","v188","v189","v190","v191","v192","v193","v194","v195","v196","v197","v198","v199","v200","v201","v202","v203","v204","v205","v206","v207","v208","v209","v210","v211","v212","v213","v214","v215","v216","v217","v218","v219","v220","v221","v222","v223","v224","v225","v226","v227","v228","v229","v230","v231","v232","v233","v234","v235","v236","v237","v238","v239","v240","v241","v242","v243","v244","v245","v246","v247","v248","v249","v250","v251","v252","v253","v254","v255","a0","a1","a2","a3","a4","a5","a6","a7","a8","a9","a10","a11","a12","a13","a14","a15","a16","a17","a18","a19","a20","a21","a22","a23","a24","a25","a26","a27","a28","a29","a30","a31","a32","a33","a34","a35","a36","a37","a38","a39","a40","a41","a42","a43","a44","a45","a46","a47","a48","a49","a50","a51","a52","a53","a54","a55","a56","a57","a58","a59","a60","a61","a62","a63","a64","a65","a66","a67","a68","a69","a70","a71","a72","a73","a74","a75","a76","a77","a78","a79","a80","a81","a82","a83","a84","a85","a86","a87","a88","a89","a90","a91","a92","a93","a94","a95","a96","a97","a98","a99","a100","a101","a102","a103","a104","a105","a106","a107","a108","a109","a110","a111","a112","a113","a114","a115","a116","a117","a118","a119","a120","a121","a122","a123","a124","a125","a126","a127","a128","a129","a130","a131","a132","a133","a134","a135","a136","a137","a138","a139","a140","a141","a142","a143","a144","a145","a146","a147","a148","a149","a150","a151","a152","a153","a154","a155","a156","a157","a158","a159","a160","a161","a162","a163","a164","a165","a166","a167","a168","a169","a170","a171","a172","a173","a174","a175","a176","a177","a178","a179","a180","a181","a182","a183","a184","a185","a186","a187","a188","a189","a190","a191","a192","a193","a194","a195","a196","a197","a198","a199","a200","a201","a202","a203","a204","a205","a206","a207","a208","a209","a210","a211","a212","a213","a214","a215","a216","a217","a218","a219","a220","a221","a222","a223","a224","a225","a226","a227","a228","a229","a230","a231","a232","a233","a234","a235","a236","a237","a238","a239","a240","a241","a242","a243","a244","a245","a246","a247","a248","a249","a250","a251","a252","a253","a254","a255","s6","s7","s8","s9","s10","s11","s12","s13","s14","s15","s16","s17","s18","s19","s20","s21","s22","s23","s24","s25","s26","s27","s28","s29","s30","s31","s32","s33","s34","s35","s36","s37","s38","s39","s40","s41","s42","s43","s44","s45","s46","s47","s48","s49","s50","s51","s52","s53","s54","s55","s56","s57","s58","s59","s60","s61","s62","s63","s64","s65","s66","s67","s68","s69","s70","s71","s72","s73","s74","s75","s76","s77","s78","s79","s80","s81","s82","s83","s84","s85","s86","s87","s88","s89","s90","s91","s92","s93","s94","s95","s96","s97","s98","s99","s100","s101","vcc","scc","m0","memory");
}

extern "C" void kernel_launch(void* const* d_in, const int* in_sizes, int n_in, void* d_out, int out_size, void* d_ws, size_t ws_size,
                              hipStream_t stream) {
    (void)hipFuncSetAttribute((const void*)fa_kernel, hipFuncAttributeMaxDynamicSharedMemorySize, LDS_BYTES);
    fa_kernel<<<256, 256, LDS_BYTES, stream>>>((const bf16*)d_in[0], (const bf16*)d_in[1], (const bf16*)d_in[2], (const bf16*)d_in[3], (const bf16*)d_in[4], (char*)d_ws,
                                               (const bf16*)d_in[5], (const bf16*)d_in[8], (const bf16*)d_in[9], (const bf16*)d_in[10], (const bf16*)d_in[7], (bf16*)d_out);
}
```

```cpp
#include <hip/hip_runtime.h>
#include <hip/hip_bf16.h>
#include <stdint.h>
#pragma clang diagnostic ignored "-Winline-asm"
#pragma clang diagnostic ignored "-Woverlength-strings"

typedef __hip_bfloat16 bf16;
#define DBGP ((unsigned*)nullptr)
constexpr int LDS_BYTES = 163840;
extern "C" __global__ __attribute__((aligned(4096))) __launch_bounds__(256, 1) void fa_kernel(const bf16* __restrict__ px, const bf16* __restrict__ ckv, const bf16* __restrict__ wq,
                                                    const bf16* __restrict__ wuk, const bf16* __restrict__ wuv, char* pws,
                                                    const bf16* __restrict__ pwo, const bf16* __restrict__ pwg, const bf16* __restrict__ pwu,
                                                    const bf16* __restrict__ pwd, const bf16* __restrict__ pwr, bf16* __restrict__ pout) {
    asm volatile(R"ASM(
; ---- roles: head = 4 xcd + (wg >> 6), j = (wg >> 3) & 7; odd j: block j >> 1 (then helps with block 7 - (j >> 1)), even j: block 7 - (j >> 1). First operands, addresses
.La5_LSTART:
s_load_dwordx16 s[16:31], s[0:1], 0
s_load_dwordx8 s[32:39], s[0:1], 64
.p2align 6
v_mbcnt_lo_u32_b32 v64, -1, 0
v_mbcnt_hi_u32_b32 v64, -1, v64
s_getpc_b64 s[92:93]
.La5_LPC:
s_sub_u32 s48, s92, .La5_LPC-.La5_LSTART
s_subb_u32 s49, s93, 0
s_and_b32 s49, s49, 0xffff
s_mov_b32 s50, .La5_LCODEEND-.La5_LSTART
s_mov_b32 s51, 0x20000
s_lshr_b32 s97, s2, 3
s_lshl2_add_u32 s87, s97, s3
v_lshlrev_b32 v133, 7, v64
s_lshl_b32 s87, s87, 13
buffer_load_dword v254, v133, s[48:51], s87 offen
s_and_b32 s92, s2, 7
s_lshr_b32 s93, s2, 6
s_lshl2_add_u32 s72, s92, s93
s_bfe_u32 s73, s2, 0x30003
s_lshr_b32 s74, s73, 1
s_xor_b32 s92, s74, 7
s_and_b32 s75, s73, 1
s_cselect_b32 s76, s74, s92
s_lshr_b32 s77, s3, 1
s_and_b32 s78, s3, 1
v_and_b32 v65, 15, v64
v_lshrrev_b32 v66, 4, v64
v_lshlrev_b32 v255, 4, v64
v_lshlrev_b32 v67, 1, v66
v_xor_b32 v67, v67, v65
v_lshlrev_b32 v67, 4, v67
v_lshl_add_u32 v67, v66, 13, v67
s_lshl_b32 s94, s78, 17
v_add_u32 v232, s94, v67
v_add_u32 v233, 32768, v232
v_add_u32 v234, 65536, v232
v_add_u32 v235, 98304, v232
v_and_b32 v67, 7, v64
v_xor_b32 v68, v67, v66
v_lshlrev_b32 v68, 4, v68
v_lshrrev_b32 v69, 3, v64
v_lshl_add_u32 v68, v69, 10, v68
s_lshl2_add_u32 s94, s3, s93
s_lshl_b32 s82, s94, 11
s_lshl_b32 s94, s94, 14
v_add_u32 v224, s94, v68
v_xor_b32 v225, 0x2040, v224
v_xor_b32 v226, 16384, v224
v_xor_b32 v227, 16384, v225
v_xor_b32 v228, 32768, v224
v_xor_b32 v229, 32768, v225
v_xor_b32 v230, 49152, v224
v_xor_b32 v231, 49152, v225
s_mul_i32 s94, s77, 8448
s_mul_i32 s95, s78, 4224
s_add_u32 s83, s94, s95
s_add_u32 s83, s83, 96256
v_lshlrev_b32 v135, 7, v67
v_lshl_add_u32 v135, v69, 13, v135
v_lshrrev_b32 v132, 5, v64
v_lshl_add_u32 v132, v132, 28, v135
v_lshl_add_u32 v133, v66, 28, v133
v_lshlrev_b32 v134, 10, v65
v_lshl_add_u32 v134, v66, 7, v134
v_lshrrev_b32 v68, 4, v64
v_lshl_add_u32 v134, v68, 28, v134
s_lshl1_add_u32 s97, s97, s78
s_lshl_b32 s7, s97, 13
s_lshl_b32 s97, s97, 15
s_lshl_b32 s96, s93, 8
s_sub_u32 s7, s7, s96
s_sub_u32 s87, s87, s96
s_sub_u32 s97, s97, s96
s_add_u32 s86, s97, 2097152
s_lshl2_add_u32 s101, s72, s3
s_lshl_b32 s101, s101, 11
s_lshl2_add_u32 s100, s93, s3
s_lshl_b32 s100, s100, 14
v_lshlrev_b32 v118, 11, v67
s_lshl_b32 s93, s3, 9
v_lshl_add_u32 v119, v69, 4, v118
v_add_u32 v119, s93, v119
s_lshl_b32 s97, s2, 14
s_lshl_b32 s94, s72, 8
s_lshl_b32 s95, s76, 18
s_sub_u32 s46, 4194304, s94
s_mov_b32 s47, 0x20000
s_mov_b32 s42, 262144
s_mov_b32 s43, 0x20000
s_mov_b32 s62, 16384
s_mov_b32 s63, 0x20000
s_mov_b32 s79, 512
s_mov_b32 s80, 2097152
s_add_u32 s100, s100, 512
s_mov_b32 s81, 262144
s_cmp_eq_u32 s77, 0
s_waitcnt lgkmcnt(0)
s_cselect_b64 s[92:93], s[22:23], s[24:25]
s_add_u32 s44, s92, s94
s_addc_u32 s45, s93, 0
s_and_b32 s45, s45, 0xffff
s_add_u32 s40, s18, s95
s_addc_u32 s41, s19, 0
s_and_b32 s41, s41, 0xffff
s_add_u32 s60, s16, s97
s_addc_u32 s61, s17, 0
s_and_b32 s61, s61, 0xffff
buffer_load_dword v254, v132, s[44:47], s86 offen
s_sub_u32 s86, s86, 2097152
buffer_load_dword v254, v133, s[40:43], s101 offen
buffer_load_dword v254, v134, s[40:43], s100 offen
s_add_u32 m0, s83, 0
s_nop 0
buffer_load_dwordx4 v232, s[44:47], s80 offen lds
s_add_u32 m0, s83, 1024
s_nop 0
buffer_load_dwordx4 v233, s[44:47], s80 offen lds
s_add_u32 m0, s83, 2176
s_nop 0
buffer_load_dwordx4 v234, s[44:47], s80 offen lds
s_add_u32 m0, s83, 3200
s_nop 0
buffer_load_dwordx4 v235, s[44:47], s80 offen lds
s_add_u32 s80, s80, s81
s_and_b32 s80, s80, 4194303
v_lshrrev_b32 v67, 1, v65
v_xor_b32 v67, v67, v66
v_lshlrev_b32 v67, 4, v67
v_lshl_add_u32 v67, v65, 7, v67
s_lshl_b32 s92, s77, 12
v_add_u32 v236, s92, v67
v_xor_b32 v237, 64, v236
v_bfe_u32 v120, v64, 2, 2
v_add_u32 v67, 1, v66
v_lshrrev_b32 v67, 1, v67
v_lshlrev_b32 v67, 7, v67
v_lshl_add_u32 v67, v66, 11, v67
v_lshl_add_u32 v67, v120, 8, v67
v_and_b32 v68, 3, v64
v_lshl_add_u32 v67, v68, 3, v67
s_mul_i32 s92, s78, 8448
v_lshl_add_u32 v67, v120, 5, v67
v_add_u32 v67, s92, v67
v_add_u32 v240, 96256, v67
v_xor_b32 v241, 32, v240
v_xor_b32 v242, 64, v240
v_xor_b32 v243, 96, v240
s_add_u32 s64, s26, 142606336
s_addc_u32 s65, s27, 0
s_and_b32 s65, s65, 0xffff
s_mov_b32 s66, 3145728
s_mov_b32 s67, 0x20000
s_mov_b32 s88, 4096
s_lshr_b32 s92, s2, 1
s_lshl_b32 s92, s92, 15
s_and_b32 s93, s2, 1
s_lshl_b32 s93, s93, 7
s_add_u32 s92, s92, s93
s_lshl_b32 s93, s3, 13
s_add_u32 s92, s92, s93
s_add_u32 s92, s92, 88080384
s_add_u32 s56, s26, s92
s_addc_u32 s57, s27, 0
s_and_b32 s57, s57, 0xffff
s_mov_b32 s58, 8576
s_mov_b32 s59, 0x20000
v_and_b32 v71, 0x380, v255
v_and_b32 v127, 0x70, v255
v_lshl_add_u32 v71, v71, 1, v127
v_xor_b32 v67, 3, v69
v_min_u32 v67, 1, v67
v_xor_b32 v67, 1, v67
s_mov_b32 s93, 0
v_mul_lo_u32 v67, v67, s93
v_add_u32 v136, v67, v255
v_and_b32 v67, 3, v69
v_xor_b32 v67, 1, v67
v_min_u32 v67, 1, v67
v_xor_b32 v67, 1, v67
v_and_b32 v67, s2, v67
v_mul_lo_u32 v67, v67, s93
v_add_u32 v71, v71, v67
s_lshl_b32 s92, s3, 3
v_lshlrev_b32 v126, 1, v127
v_or_b32 v126, 65536, v126
v_add_u32 v127, s92, v126
s_add_u32 s52, s26, 171966464
s_addc_u32 s53, s27, 0
s_and_b32 s53, s53, 0xffff
s_mov_b32 s54, 950272
s_mov_b32 s55, 0x20000
s_lshl2_add_u32 s95, s2, s3
s_lshl_b32 s91, s95, 3
s_lshl_b32 s95, s95, 10
s_lshl_b32 s89, s76, 10
v_lshl_add_u32 v135, v69, 28, v135
s_mov_b32 s96, s87
s_mov_b32 s84, 33792
s_mov_b32 s85, 0
s_add_u32 s97, s83, 67584
s_mov_b32 s90, s83
s_lshl_b32 s92, s72, 8
s_sub_u32 s93, 8388608, s92
s_add_u32 s68, s20, s92
s_addc_u32 s69, s21, 0
s_and_b32 s69, s69, 0xffff
s_mov_b32 s70, s93
s_mov_b32 s71, 0x20000
s_lshr_b32 s92, s20, 7
s_sub_u32 s92, 3, s92
s_bfe_u32 s99, s92, 0x20001
s_lshr_b32 s93, s2, 6
s_sub_u32 s99, s99, s93
s_lshl_b32 s92, s99, 8
s_lshr_b32 s93, s2, 3
s_lshl2_add_u32 s93, s93, s3
s_lshl_b32 s98, s93, 16
s_add_u32 s98, s98, s92
v_lshlrev_b32 v138, 4, v65
v_lshl_add_u32 v137, v66, 13, v138
v_lshl_add_u32 v138, v66, 9, v138
s_lshl_b32 s93, s93, 12
v_add_u32 v138, s93, v138
s_and_b32 s93, s2, 7
s_lshl_b32 s92, s93, 19
s_add_u32 s92, s92, 138412032
s_add_u32 s4, s26, s92
s_addc_u32 s5, s27, 0
s_and_b32 s5, s5, 0xffff
s_mov_b32 s6, 0
s_mov_b32 s100, 1073741824
s_add_u32 s101, s91, 65536
v_lshrrev_b32 v139, 1, v64
v_lshlrev_b32 v139, 8, v139
v_and_or_b32 v139, v255, 16, v139
s_lshl_b32 s93, s93, 5
s_add_u32 s93, s93, 64512
v_add_u32 v139, s93, v139
s_mov_b64 exec, 0
s_cmp_eq_u32 s3, 0
s_cbranch_scc1 .La5_LW1_0
s_cmp_eq_u32 s3, 1
s_cbranch_scc1 .La5_LW1_1
s_cmp_eq_u32 s3, 2
s_cbranch_scc1 .La5_LW1_2
s_branch .La5_LW1_3
.La5_LW1_R0:
s_mov_b64 exec, -1
s_xor_b32 m0, s82, 0
s_nop 0
buffer_load_dwordx4 v224, s[40:43], s79 offen lds
s_xor_b32 m0, s82, 1024
s_nop 0
buffer_load_dwordx4 v225, s[40:43], s79 offen lds
s_xor_b32 m0, s82, 2048
s_nop 0
buffer_load_dwordx4 v226, s[40:43], s79 offen lds
s_xor_b32 m0, s82, 3072
s_nop 0
buffer_load_dwordx4 v227, s[40:43], s79 offen lds
s_xor_b32 m0, s82, 4096
s_nop 0
buffer_load_dwordx4 v228, s[40:43], s79 offen lds
s_xor_b32 m0, s82, 5120
s_nop 0
buffer_load_dwordx4 v229, s[40:43], s79 offen lds
s_xor_b32 m0, s82, 6144
s_nop 0
buffer_load_dwordx4 v230, s[40:43], s79 offen lds
s_xor_b32 m0, s82, 7168
s_nop 0
buffer_load_dwordx4 v231, s[40:43], s79 offen lds
s_add_u32 s79, s79, 128
s_and_b32 s79, s79, 1023
s_add_u32 m0, s83, 16896
s_nop 0
buffer_load_dwordx4 v232, s[44:47], s80 offen lds
s_add_u32 m0, s83, 17920
s_nop 0
buffer_load_dwordx4 v233, s[44:47], s80 offen lds
s_add_u32 m0, s83, 19072
s_nop 0
buffer_load_dwordx4 v234, s[44:47], s80 offen lds
s_add_u32 m0, s83, 20096
s_nop 0
buffer_load_dwordx4 v235, s[44:47], s80 offen lds
s_add_u32 s80, s80, s81
s_and_b32 s80, s80, 4194303
buffer_load_dwordx2 v[252:253], off, s[64:67], 0 sc1
s_waitcnt vmcnt(1)
s_barrier
ds_read_b128 v[160:163], v236 offset:24576
ds_read_b128 v[164:167], v237 offset:24576
ds_read_b64_tr_b16 v[192:193], v240 offset:0
ds_read_b64_tr_b16 v[194:195], v240 offset:1024
ds_read_b64_tr_b16 v[196:197], v240 offset:16896
ds_read_b64_tr_b16 v[198:199], v240 offset:17920
ds_read_b64_tr_b16 v[200:201], v241 offset:0
ds_read_b64_tr_b16 v[202:203], v241 offset:1024
ds_read_b64_tr_b16 v[204:205], v241 offset:16896
ds_read_b64_tr_b16 v[206:207], v241 offset:17920
ds_read_b64_tr_b16 v[208:209], v242 offset:0
ds_read_b64_tr_b16 v[210:211], v242 offset:1024
ds_read_b64_tr_b16 v[212:213], v242 offset:16896
ds_read_b64_tr_b16 v[214:215], v242 offset:17920
ds_read_b64_tr_b16 v[216:217], v243 offset:0
ds_read_b64_tr_b16 v[218:219], v243 offset:1024
ds_read_b64_tr_b16 v[220:221], v243 offset:16896
ds_read_b64_tr_b16 v[222:223], v243 offset:17920
ds_read_b128 v[168:171], v236 offset:26624
ds_read_b128 v[172:175], v237 offset:26624
ds_read_b128 v[176:179], v236 offset:16384
ds_read_b128 v[180:183], v237 offset:16384
ds_read_b128 v[184:187], v236 offset:18432
ds_read_b128 v[188:191], v237 offset:18432
s_waitcnt lgkmcnt(0)
s_cmp_eq_u32 s78, 1
s_nop 0
s_nop 0
s_cbranch_scc1 .La5_LV_START
; ---- K tile (waves 0, 2): 256 keys x 128 columns, K = 512; LayerNorm of 8 rows of x in the gaps (softmax scale folded in)
.La5_LK_START:
v_mfma_f32_16x16x32_bf16 a[96:99], v[192:195], v[160:163], 0
s_add_u32 m0, s83, 33792
v_mfma_f32_16x16x32_bf16 a[96:99], v[196:199], v[164:167], a[96:99]
buffer_load_dwordx4 v232, s[44:47], s80 offen lds
v_mfma_f32_16x16x32_bf16 a[100:103], v[204:207], v[164:167], 0
ds_read_b128 v[0:3], v236 offset:8192
ds_read_b128 v[4:7], v237 offset:8192
s_add_u32 m0, s83, 34816
v_mfma_f32_16x16x32_bf16 a[100:103], v[200:203], v[160:163], a[100:103]
ds_read_b128 v[8:11], v236 offset:10240
ds_read_b128 v[12:15], v237 offset:10240
buffer_load_dwordx4 v233, s[44:47], s80 offen lds
v_mfma_f32_16x16x32_bf16 a[104:107], v[208:211], v[160:163], 0
s_add_u32 m0, s83, 35968
v_mfma_f32_16x16x32_bf16 a[104:107], v[212:215], v[164:167], a[104:107]
buffer_load_dwordx4 v234, s[44:47], s80 offen lds
v_mfma_f32_16x16x32_bf16 a[108:111], v[220:223], v[164:167], 0
ds_read_b128 v[16:19], v236 offset:0
ds_read_b128 v[20:23], v237 offset:0
s_add_u32 m0, s83, 36992
v_mfma_f32_16x16x32_bf16 a[108:111], v[216:219], v[160:163], a[108:111]
ds_read_b128 v[24:27], v236 offset:2048
ds_read_b128 v[28:31], v237 offset:2048
buffer_load_dwordx4 v235, s[44:47], s80 offen lds
v_mfma_f32_16x16x32_bf16 a[140:143], v[216:219], v[168:171], 0
s_mov_b32 s80, 2883584
v_mfma_f32_16x16x32_bf16 a[140:143], v[220:223], v[172:175], a[140:143]
v_mfma_f32_16x16x32_bf16 a[136:139], v[212:215], v[172:175], 0
s_xor_b32 m0, s82, 32768
v_mfma_f32_16x16x32_bf16 a[136:139], v[208:211], v[168:171], a[136:139]
ds_read_b64_tr_b16 v[32:33], v240 offset:128
ds_read_b64_tr_b16 v[34:35], v240 offset:1152
buffer_load_dwordx4 v224, s[40:43], s79 offen lds
v_mfma_f32_16x16x32_bf16 a[124:127], v[200:203], v[168:171], 0
s_xor_b32 m0, s82, 33792
v_mfma_f32_16x16x32_bf16 a[124:127], v[204:207], v[172:175], a[124:127]
buffer_load_dwordx4 v225, s[40:43], s79 offen lds
v_mfma_f32_16x16x32_bf16 a[120:123], v[196:199], v[172:175], 0
s_xor_b32 m0, s82, 34816
v_mfma_f32_16x16x32_bf16 a[120:123], v[192:195], v[168:171], a[120:123]
ds_read_b64_tr_b16 v[36:37], v240 offset:17024
ds_read_b64_tr_b16 v[38:39], v240 offset:18048
buffer_load_dwordx4 v226, s[40:43], s79 offen lds
v_mfma_f32_16x16x32_bf16 a[232:235], v[192:195], v[176:179], 0
s_xor_b32 m0, s82, 35840
v_mfma_f32_16x16x32_bf16 a[232:235], v[196:199], v[180:183], a[232:235]
buffer_load_dwordx4 v227, s[40:43], s79 offen lds
v_mfma_f32_16x16x32_bf16 a[236:239], v[204:207], v[180:183], 0
s_xor_b32 m0, s82, 36864
v_mfma_f32_16x16x32_bf16 a[236:239], v[200:203], v[176:179], a[236:239]
ds_read_b64_tr_b16 v[40:41], v241 offset:128
ds_read_b64_tr_b16 v[42:43], v241 offset:1152
buffer_load_dwordx4 v228, s[40:43], s79 offen lds
v_mfma_f32_16x16x32_bf16 a[152:155], v[208:211], v[176:179], 0
s_xor_b32 m0, s82, 37888
v_mfma_f32_16x16x32_bf16 a[152:155], v[212:215], v[180:183], a[152:155]
buffer_load_dwordx4 v229, s[40:43], s79 offen lds
v_mfma_f32_16x16x32_bf16 a[156:159], v[220:223], v[180:183], 0
s_xor_b32 m0, s82, 38912
v_mfma_f32_16x16x32_bf16 a[156:159], v[216:219], v[176:179], a[156:159]
buffer_load_dwordx4 v230, s[40:43], s79 offen lds
v_mfma_f32_16x16x32_bf16 a[172:175], v[216:219], v[184:187], 0
s_xor_b32 m0, s82, 39936
v_mfma_f32_16x16x32_bf16 a[172:175], v[220:223], v[188:191], a[172:175]
buffer_load_dwordx4 v231, s[40:43], s79 offen lds
v_mfma_f32_16x16x32_bf16 a[168:171], v[212:215], v[188:191], 0
s_mov_b32 s79, 768
v_mfma_f32_16x16x32_bf16 a[168:171], v[208:211], v[184:187], a[168:171]
ds_read_b64_tr_b16 v[44:45], v241 offset:17024
ds_read_b64_tr_b16 v[46:47], v241 offset:18048
v_mfma_f32_16x16x32_bf16 a[220:223], v[200:203], v[184:187], 0
s_add_u32 m0, s83, 50688
v_mfma_f32_16x16x32_bf16 a[220:223], v[204:207], v[188:191], a[220:223]
buffer_load_dwordx4 v232, s[44:47], s80 offen lds
v_mfma_f32_16x16x32_bf16 a[216:219], v[196:199], v[188:191], 0
s_waitcnt lgkmcnt(8)
s_add_u32 m0, s83, 51712
v_mfma_f32_16x16x32_bf16 a[216:219], v[192:195], v[184:187], a[216:219]
buffer_load_dwordx4 v233, s[44:47], s80 offen lds
v_mfma_f32_16x16x32_bf16 a[200:203], v[192:195], v[0:3], 0
s_add_u32 m0, s83, 52864
v_mfma_f32_16x16x32_bf16 a[200:203], v[196:199], v[4:7], a[200:203]
buffer_load_dwordx4 v234, s[44:47], s80 offen lds
v_mfma_f32_16x16x32_bf16 a[204:207], v[204:207], v[4:7], 0
s_add_u32 m0, s83, 53888
v_mfma_f32_16x16x32_bf16 a[204:207], v[200:203], v[0:3], a[204:207]
ds_read_b64_tr_b16 v[48:49], v242 offset:128
ds_read_b64_tr_b16 v[50:51], v242 offset:1152
buffer_load_dwordx4 v235, s[44:47], s80 offen lds
v_mfma_f32_16x16x32_bf16 a[184:187], v[208:211], v[0:3], 0
s_mov_b32 s80, 3145728
v_mfma_f32_16x16x32_bf16 a[184:187], v[212:215], v[4:7], a[184:187]
v_mfma_f32_16x16x32_bf16 a[188:191], v[220:223], v[4:7], 0
v_mfma_f32_16x16x32_bf16 a[188:191], v[216:219], v[0:3], a[188:191]
v_mfma_f32_16x16x32_bf16 a[12:15], v[216:219], v[8:11], 0
v_mfma_f32_16x16x32_bf16 a[12:15], v[220:223], v[12:15], a[12:15]
buffer_load_dword v70, v118, s[60:63], 0 offen
v_mfma_f32_16x16x32_bf16 a[8:11], v[212:215], v[12:15], 0
v_mfma_f32_16x16x32_bf16 a[8:11], v[208:211], v[8:11], a[8:11]
ds_read_b64_tr_b16 v[52:53], v242 offset:17024
ds_read_b64_tr_b16 v[54:55], v242 offset:18048
v_mfma_f32_16x16x32_bf16 a[76:79], v[200:203], v[8:11], 0
v_mfma_f32_16x16x32_bf16 a[76:79], v[204:207], v[12:15], a[76:79]
buffer_load_dwordx4 v[72:75], v119, s[60:63], 0 offen offset:0
v_mfma_f32_16x16x32_bf16 a[72:75], v[196:199], v[12:15], 0
v_mfma_f32_16x16x32_bf16 a[72:75], v[192:195], v[8:11], a[72:75]
ds_read_b64_tr_b16 v[56:57], v243 offset:128
ds_read_b64_tr_b16 v[58:59], v243 offset:1152
v_mfma_f32_16x16x32_bf16 a[88:91], v[192:195], v[16:19], 0
v_mfma_f32_16x16x32_bf16 a[88:91], v[196:199], v[20:23], a[88:91]
buffer_load_dwordx4 v[76:79], v119, s[60:63], 0 offen offset:128
v_mfma_f32_16x16x32_bf16 a[92:95], v[204:207], v[20:23], 0
v_mfma_f32_16x16x32_bf16 a[92:95], v[200:203], v[16:19], a[92:95]
ds_read_b64_tr_b16 v[60:61], v243 offset:17024
ds_read_b64_tr_b16 v[62:63], v243 offset:18048
v_mfma_f32_16x16x32_bf16 a[24:27], v[208:211], v[16:19], 0
v_mfma_f32_16x16x32_bf16 a[24:27], v[212:215], v[20:23], a[24:27]
buffer_load_dwordx4 v[80:83], v119, s[60:63], 0 offen offset:256
v_mfma_f32_16x16x32_bf16 a[28:31], v[220:223], v[20:23], 0
v_mfma_f32_16x16x32_bf16 a[28:31], v[216:219], v[16:19], a[28:31]
v_mfma_f32_16x16x32_bf16 a[44:47], v[216:219], v[24:27], 0
v_mfma_f32_16x16x32_bf16 a[44:47], v[220:223], v[28:31], a[44:47]
buffer_load_dwordx4 v[84:87], v119, s[60:63], 0 offen offset:384
v_mfma_f32_16x16x32_bf16 a[40:43], v[212:215], v[28:31], 0
v_mfma_f32_16x16x32_bf16 a[40:43], v[208:211], v[24:27], a[40:43]
v_mfma_f32_16x16x32_bf16 a[252:255], v[200:203], v[24:27], 0
v_mfma_f32_16x16x32_bf16 a[252:255], v[204:207], v[28:31], a[252:255]
v_mfma_f32_16x16x32_bf16 a[248:251], v[196:199], v[28:31], 0
v_mfma_f32_16x16x32_bf16 a[248:251], v[192:195], v[24:27], a[248:251]
s_mov_b64 exec, 0
s_cmp_eq_u32 s3, 0
s_cbranch_scc1 .La5_LW2_0
s_branch .La5_LW2_R0
.La5_LW2_R0:
s_mov_b64 exec, -1
s_waitcnt vmcnt(5) lgkmcnt(0)
s_barrier
v_mfma_f32_16x16x32_bf16 a[112:115], v[32:35], v[160:163], 0
v_mfma_f32_16x16x32_bf16 a[112:115], v[36:39], v[164:167], a[112:115]
v_mfma_f32_16x16x32_bf16 a[116:119], v[44:47], v[164:167], 0
ds_read_b64_tr_b16 v[192:193], v240 offset:33792
ds_read_b64_tr_b16 v[194:195], v240 offset:34816
v_mfma_f32_16x16x32_bf16 a[116:119], v[40:43], v[160:163], a[116:119]
ds_read_b64_tr_b16 v[196:197], v240 offset:50688
ds_read_b64_tr_b16 v[198:199], v240 offset:51712
v_mfma_f32_16x16x32_bf16 a[128:131], v[48:51], v[160:163], 0
v_mfma_f32_16x16x32_bf16 a[128:131], v[52:55], v[164:167], a[128:131]
v_mfma_f32_16x16x32_bf16 a[132:135], v[60:63], v[164:167], 0
v_mfma_f32_16x16x32_bf16 a[132:135], v[56:59], v[160:163], a[132:135]
ds_read_b128 v[160:163], v236 offset:57344
ds_read_b128 v[164:167], v237 offset:57344
v_mfma_f32_16x16x32_bf16 a[148:151], v[56:59], v[168:171], 0
v_mfma_f32_16x16x32_bf16 a[148:151], v[60:63], v[172:175], a[148:151]
v_mfma_f32_16x16x32_bf16 a[144:147], v[52:55], v[172:175], 0
v_mfma_f32_16x16x32_bf16 a[144:147], v[48:51], v[168:171], a[144:147]
ds_read_b64_tr_b16 v[200:201], v241 offset:33792
ds_read_b64_tr_b16 v[202:203], v241 offset:34816
v_mfma_f32_16x16x32_bf16 a[228:231], v[40:43], v[168:171], 0
v_mfma_f32_16x16x32_bf16 a[228:231], v[44:47], v[172:175], a[228:231]
v_mfma_f32_16x16x32_bf16 a[224:227], v[36:39], v[172:175], 0
v_mfma_f32_16x16x32_bf16 a[224:227], v[32:35], v[168:171], a[224:227]
ds_read_b128 v[168:171], v236 offset:59392
ds_read_b128 v[172:175], v237 offset:59392
v_mfma_f32_16x16x32_bf16 a[208:211], v[32:35], v[176:179], 0
s_xor_b32 m0, s82, 0
v_mfma_f32_16x16x32_bf16 a[208:211], v[36:39], v[180:183], a[208:211]
buffer_load_dwordx4 v224, s[40:43], s79 offen lds
v_mfma_f32_16x16x32_bf16 a[212:215], v[44:47], v[180:183], 0
v_mfma_f32_16x16x32_bf16 a[212:215], v[40:43], v[176:179], a[212:215]
ds_read_b64_tr_b16 v[204:205], v241 offset:50688
ds_read_b64_tr_b16 v[206:207], v241 offset:51712
s_xor_b32 m0, s82, 1024
v_mfma_f32_16x16x32_bf16 a[160:163], v[48:51], v[176:179], 0
buffer_load_dwordx4 v225, s[40:43], s79 offen lds
v_mfma_f32_16x16x32_bf16 a[160:163], v[52:55], v[180:183], a[160:163]
v_mfma_f32_16x16x32_bf16 a[164:167], v[60:63], v[180:183], 0
s_xor_b32 m0, s82, 2048
v_mfma_f32_16x16x32_bf16 a[164:167], v[56:59], v[176:179], a[164:167]
ds_read_b128 v[176:179], v236 offset:49152
ds_read_b128 v[180:183], v237 offset:49152
buffer_load_dwordx4 v226, s[40:43], s79 offen lds
v_mfma_f32_16x16x32_bf16 a[180:183], v[56:59], v[184:187], 0
v_mfma_f32_16x16x32_bf16 a[180:183], v[60:63], v[188:191], a[180:183]
s_xor_b32 m0, s82, 3072
v_mfma_f32_16x16x32_bf16 a[176:179], v[52:55], v[188:191], 0
buffer_load_dwordx4 v227, s[40:43], s79 offen lds
v_mfma_f32_16x16x32_bf16 a[176:179], v[48:51], v[184:187], a[176:179]
ds_read_b64_tr_b16 v[208:209], v242 offset:33792
ds_read_b64_tr_b16 v[210:211], v242 offset:34816
v_mfma_f32_16x16x32_bf16 a[196:199], v[40:43], v[184:187], 0
s_xor_b32 m0, s82, 4096
v_mfma_f32_16x16x32_bf16 a[196:199], v[44:47], v[188:191], a[196:199]
buffer_load_dwordx4 v228, s[40:43], s79 offen lds
v_mfma_f32_16x16x32_bf16 a[192:195], v[36:39], v[188:191], 0
v_mfma_f32_16x16x32_bf16 a[192:195], v[32:35], v[184:187], a[192:195]
ds_read_b128 v[184:187], v236 offset:51200
ds_read_b128 v[188:191], v237 offset:51200
s_xor_b32 m0, s82, 5120
v_mfma_f32_16x16x32_bf16 a[64:67], v[32:35], v[0:3], 0
buffer_load_dwordx4 v229, s[40:43], s79 offen lds
v_mfma_f32_16x16x32_bf16 a[64:67], v[36:39], v[4:7], a[64:67]
v_mfma_f32_16x16x32_bf16 a[68:71], v[44:47], v[4:7], 0
s_xor_b32 m0, s82, 6144
v_mfma_f32_16x16x32_bf16 a[68:71], v[40:43], v[0:3], a[68:71]
ds_read_b64_tr_b16 v[212:213], v242 offset:50688
ds_read_b64_tr_b16 v[214:215], v242 offset:51712
buffer_load_dwordx4 v230, s[40:43], s79 offen lds
v_mfma_f32_16x16x32_bf16 a[0:3], v[48:51], v[0:3], 0
v_mfma_f32_16x16x32_bf16 a[0:3], v[52:55], v[4:7], a[0:3]
s_xor_b32 m0, s82, 7168
v_mfma_f32_16x16x32_bf16 a[4:7], v[60:63], v[4:7], 0
buffer_load_dwordx4 v231, s[40:43], s79 offen lds
v_mfma_f32_16x16x32_bf16 a[4:7], v[56:59], v[0:3], a[4:7]
ds_read_b128 v[0:3], v236 offset:40960
ds_read_b128 v[4:7], v237 offset:40960
s_mov_b32 s79, 896
v_mfma_f32_16x16x32_bf16 a[20:23], v[56:59], v[8:11], 0
v_mfma_f32_16x16x32_bf16 a[20:23], v[60:63], v[12:15], a[20:23]
s_add_u32 m0, s83, 0
v_mfma_f32_16x16x32_bf16 a[16:19], v[52:55], v[12:15], 0
buffer_load_dwordx4 v232, s[44:47], s80 offen lds
v_mfma_f32_16x16x32_bf16 a[16:19], v[48:51], v[8:11], a[16:19]
ds_read_b64_tr_b16 v[216:217], v243 offset:33792
ds_read_b64_tr_b16 v[218:219], v243 offset:34816
v_mfma_f32_16x16x32_bf16 a[84:87], v[40:43], v[8:11], 0
s_add_u32 m0, s83, 1024
v_mfma_f32_16x16x32_bf16 a[84:87], v[44:47], v[12:15], a[84:87]
buffer_load_dwordx4 v233, s[44:47], s80 offen lds
v_mfma_f32_16x16x32_bf16 a[80:83], v[36:39], v[12:15], 0
s_waitcnt vmcnt(10)
v_lshlrev_b32 v70, 16, v70
v_mfma_f32_16x16x32_bf16 a[80:83], v[32:35], v[8:11], a[80:83]
ds_read_b128 v[8:11], v236 offset:43008
ds_read_b128 v[12:15], v237 offset:43008
s_add_u32 m0, s83, 2176
v_mfma_f32_16x16x32_bf16 a[240:243], v[32:35], v[16:19], 0
buffer_load_dwordx4 v234, s[44:47], s80 offen lds
v_mfma_f32_16x16x32_bf16 a[240:243], v[36:39], v[20:23], a[240:243]
v_lshlrev_b32 v88, 16, v72
v_and_b32 v89, 0xffff0000, v72
v_mfma_f32_16x16x32_bf16 a[244:247], v[44:47], v[20:23], 0
s_add_u32 m0, s83, 3200
v_sub_f32 v88, v88, v70
v_mfma_f32_16x16x32_bf16 a[244:247], v[40:43], v[16:19], a[244:247]
ds_read_b64_tr_b16 v[220:221], v243 offset:50688
ds_read_b64_tr_b16 v[222:223], v243 offset:51712
buffer_load_dwordx4 v235, s[44:47], s80 offen lds
v_mfma_f32_16x16x32_bf16 a[32:35], v[48:51], v[16:19], 0
s_mov_b32 s80, 3407872
v_sub_f32 v89, v89, v70
v_mfma_f32_16x16x32_bf16 a[32:35], v[52:55], v[20:23], a[32:35]
v_add_f32 v120, v88, v89
v_mul_f32 v122, v88, v88
v_mfma_f32_16x16x32_bf16 a[36:39], v[60:63], v[20:23], 0
s_add_u32 m0, s83, 16896
v_mul_f32 v123, v89, v89
v_mfma_f32_16x16x32_bf16 a[36:39], v[56:59], v[16:19], a[36:39]
ds_read_b128 v[16:19], v236 offset:32768
ds_read_b128 v[20:23], v237 offset:32768
buffer_load_dwordx4 v232, s[44:47], s80 offen lds
v_mfma_f32_16x16x32_bf16 a[60:63], v[56:59], v[24:27], 0
v_lshlrev_b32 v90, 16, v73
v_and_b32 v91, 0xffff0000, v73
v_mfma_f32_16x16x32_bf16 a[60:63], v[60:63], v[28:31], a[60:63]
s_add_u32 m0, s83, 17920
v_sub_f32 v90, v90, v70
v_mfma_f32_16x16x32_bf16 a[56:59], v[52:55], v[28:31], 0
buffer_load_dwordx4 v233, s[44:47], s80 offen lds
v_mfma_f32_16x16x32_bf16 a[56:59], v[48:51], v[24:27], a[56:59]
v_sub_f32 v91, v91, v70
v_add_f32 v124, v90, v91
v_mfma_f32_16x16x32_bf16 a[52:55], v[40:43], v[24:27], 0
s_add_u32 m0, s83, 19072
v_fmac_f32 v122, v90, v90
v_mfma_f32_16x16x32_bf16 a[52:55], v[44:47], v[28:31], a[52:55]
buffer_load_dwordx4 v234, s[44:47], s80 offen lds
v_mfma_f32_16x16x32_bf16 a[48:51], v[36:39], v[28:31], 0
s_waitcnt lgkmcnt(2)
v_add_f32 v120, v120, v124
v_mfma_f32_16x16x32_bf16 a[48:51], v[32:35], v[24:27], a[48:51]
ds_read_b128 v[24:27], v236 offset:34816
ds_read_b128 v[28:31], v237 offset:34816
s_add_u32 m0, s83, 20096
v_mfma_f32_16x16x32_bf16 a[96:99], v[192:195], v[160:163], a[96:99]
buffer_load_dwordx4 v235, s[44:47], s80 offen lds
.La5_LW1_0:
s_cbranch_execz .La5_LW1_4
v_mfma_f32_16x16x32_bf16 a[96:99], v[196:199], v[164:167], a[96:99]
s_mov_b32 s80, 3670016
v_fmac_f32 v123, v91, v91
v_mfma_f32_16x16x32_bf16 a[100:103], v[204:207], v[164:167], a[100:103]
v_lshlrev_b32 v92, 16, v74
v_and_b32 v93, 0xffff0000, v74
v_mfma_f32_16x16x32_bf16 a[100:103], v[200:203], v[160:163], a[100:103]
ds_read_b64_tr_b16 v[32:33], v240 offset:33920
ds_read_b64_tr_b16 v[34:35], v240 offset:34944
v_mfma_f32_16x16x32_bf16 a[104:107], v[208:211], v[160:163], a[104:107]
v_sub_f32 v92, v92, v70
v_sub_f32 v93, v93, v70
v_mfma_f32_16x16x32_bf16 a[104:107], v[212:215], v[164:167], a[104:107]
v_add_f32 v124, v92, v93
v_fmac_f32 v122, v92, v92
v_mfma_f32_16x16x32_bf16 a[108:111], v[220:223], v[164:167], a[108:111]
s_waitcnt lgkmcnt(2)
s_barrier
v_mfma_f32_16x16x32_bf16 a[108:111], v[216:219], v[160:163], a[108:111]
v_add_f32 v120, v120, v124
v_fmac_f32 v123, v93, v93
v_mfma_f32_16x16x32_bf16 a[140:143], v[216:219], v[168:171], a[140:143]
s_xor_b32 m0, s82, 32768
v_lshlrev_b32 v94, 16, v75
v_mfma_f32_16x16x32_bf16 a[140:143], v[220:223], v[172:175], a[140:143]
buffer_load_dwordx4 v224, s[40:43], s79 offen lds
v_mfma_f32_16x16x32_bf16 a[136:139], v[212:215], v[172:175], a[136:139]
v_and_b32 v95, 0xffff0000, v75
v_sub_f32 v94, v94, v70
v_mfma_f32_16x16x32_bf16 a[136:139], v[208:211], v[168:171], a[136:139]
ds_read_b64_tr_b16 v[36:37], v240 offset:50816
ds_read_b64_tr_b16 v[38:39], v240 offset:51840
v_mfma_f32_16x16x32_bf16 a[124:127], v[200:203], v[168:171], a[124:127]
s_xor_b32 m0, s82, 33792
v_sub_f32 v95, v95, v70
v_mfma_f32_16x16x32_bf16 a[124:127], v[204:207], v[172:175], a[124:127]
buffer_load_dwordx4 v225, s[40:43], s79 offen lds
v_mfma_f32_16x16x32_bf16 a[120:123], v[196:199], v[172:175], a[120:123]
v_add_f32 v124, v94, v95
v_fmac_f32 v122, v94, v94
v_mfma_f32_16x16x32_bf16 a[120:123], v[192:195], v[168:171], a[120:123]
v_add_f32 v120, v120, v124
v_fmac_f32 v123, v95, v95
v_mfma_f32_16x16x32_bf16 a[232:235], v[192:195], v[176:179], a[232:235]
s_xor_b32 m0, s82, 34816
v_lshlrev_b32 v96, 16, v76
v_mfma_f32_16x16x32_bf16 a[232:235], v[196:199], v[180:183], a[232:235]
buffer_load_dwordx4 v226, s[40:43], s79 offen lds
v_mfma_f32_16x16x32_bf16 a[236:239], v[204:207], v[180:183], a[236:239]
v_and_b32 v97, 0xffff0000, v76
v_sub_f32 v96, v96, v70
v_mfma_f32_16x16x32_bf16 a[236:239], v[200:203], v[176:179], a[236:239]
ds_read_b64_tr_b16 v[40:41], v241 offset:33920
ds_read_b64_tr_b16 v[42:43], v241 offset:34944
v_mfma_f32_16x16x32_bf16 a[152:155], v[208:211], v[176:179], a[152:155]
s_xor_b32 m0, s82, 35840
v_sub_f32 v97, v97, v70
v_mfma_f32_16x16x32_bf16 a[152:155], v[212:215], v[180:183], a[152:155]
buffer_load_dwordx4 v227, s[40:43], s79 offen lds
v_mfma_f32_16x16x32_bf16 a[156:159], v[220:223], v[180:183], a[156:159]
v_add_f32 v124, v96, v97
v_fmac_f32 v122, v96, v96
v_mfma_f32_16x16x32_bf16 a[156:159], v[216:219], v[176:179], a[156:159]
v_add_f32 v120, v120, v124
v_fmac_f32 v123, v97, v97
v_mfma_f32_16x16x32_bf16 a[172:175], v[216:219], v[184:187], a[172:175]
s_xor_b32 m0, s82, 36864
v_lshlrev_b32 v98, 16, v77
v_mfma_f32_16x16x32_bf16 a[172:175], v[220:223], v[188:191], a[172:175]
buffer_load_dwordx4 v228, s[40:43], s79 offen lds
v_mfma_f32_16x16x32_bf16 a[168:171], v[212:215], v[188:191], a[168:171]
v_and_b32 v99, 0xffff0000, v77
v_sub_f32 v98, v98, v70
v_mfma_f32_16x16x32_bf16 a[168:171], v[208:211], v[184:187], a[168:171]
ds_read_b64_tr_b16 v[44:45], v241 offset:50816
ds_read_b64_tr_b16 v[46:47], v241 offset:51840
v_mfma_f32_16x16x32_bf16 a[220:223], v[200:203], v[184:187], a[220:223]
s_xor_b32 m0, s82, 37888
v_sub_f32 v99, v99, v70
v_mfma_f32_16x16x32_bf16 a[220:223], v[204:207], v[188:191], a[220:223]
buffer_load_dwordx4 v229, s[40:43], s79 offen lds
v_mfma_f32_16x16x32_bf16 a[216:219], v[196:199], v[188:191], a[216:219]
v_add_f32 v124, v98, v99
v_fmac_f32 v122, v98, v98
v_mfma_f32_16x16x32_bf16 a[216:219], v[192:195], v[184:187], a[216:219]
v_add_f32 v120, v120, v124
v_fmac_f32 v123, v99, v99
v_mfma_f32_16x16x32_bf16 a[200:203], v[192:195], v[0:3], a[200:203]
s_xor_b32 m0, s82, 38912
v_lshlrev_b32 v100, 16, v78
v_mfma_f32_16x16x32_bf16 a[200:203], v[196:199], v[4:7], a[200:203]
buffer_load_dwordx4 v230, s[40:43], s79 offen lds
v_mfma_f32_16x16x32_bf16 a[204:207], v[204:207], v[4:7], a[204:207]
v_and_b32 v101, 0xffff0000, v78
v_sub_f32 v100, v100, v70
v_mfma_f32_16x16x32_bf16 a[204:207], v[200:203], v[0:3], a[204:207]
ds_read_b64_tr_b16 v[48:49], v242 offset:33920
ds_read_b64_tr_b16 v[50:51], v242 offset:34944
v_mfma_f32_16x16x32_bf16 a[184:187], v[208:211], v[0:3], a[184:187]
s_xor_b32 m0, s82, 39936
v_sub_f32 v101, v101, v70
v_mfma_f32_16x16x32_bf16 a[184:187], v[212:215], v[4:7], a[184:187]
buffer_load_dwordx4 v231, s[40:43], s79 offen lds
v_mfma_f32_16x16x32_bf16 a[188:191], v[220:223], v[4:7], a[188:191]
s_mov_b32 s79, 0
v_add_f32 v124, v100, v101
v_mfma_f32_16x16x32_bf16 a[188:191], v[216:219], v[0:3], a[188:191]
v_fmac_f32 v122, v100, v100
v_add_f32 v120, v120, v124
v_mfma_f32_16x16x32_bf16 a[12:15], v[216:219], v[8:11], a[12:15]
v_fmac_f32 v123, v101, v101
v_lshlrev_b32 v102, 16, v79
v_mfma_f32_16x16x32_bf16 a[12:15], v[220:223], v[12:15], a[12:15]
buffer_load_dword v254, v132, s[44:47], s86 offen
v_mfma_f32_16x16x32_bf16 a[8:11], v[212:215], v[12:15], a[8:11]
s_sub_u32 s86, s86, 2097152
v_and_b32 v103, 0xffff0000, v79
v_mfma_f32_16x16x32_bf16 a[8:11], v[208:211], v[8:11], a[8:11]
ds_read_b64_tr_b16 v[52:53], v242 offset:50816
ds_read_b64_tr_b16 v[54:55], v242 offset:51840
v_mfma_f32_16x16x32_bf16 a[76:79], v[200:203], v[8:11], a[76:79]
v_sub_f32 v102, v102, v70
v_sub_f32 v103, v103, v70
v_mfma_f32_16x16x32_bf16 a[76:79], v[204:207], v[12:15], a[76:79]
v_add_f32 v124, v102, v103
v_fmac_f32 v122, v102, v102
v_mfma_f32_16x16x32_bf16 a[72:75], v[196:199], v[12:15], a[72:75]
v_add_f32 v120, v120, v124
v_fmac_f32 v123, v103, v103
v_mfma_f32_16x16x32_bf16 a[72:75], v[192:195], v[8:11], a[72:75]
ds_read_b64_tr_b16 v[56:57], v243 offset:33920
ds_read_b64_tr_b16 v[58:59], v243 offset:34944
v_mfma_f32_16x16x32_bf16 a[88:91], v[192:195], v[16:19], a[88:91]
v_lshlrev_b32 v104, 16, v80
v_and_b32 v105, 0xffff0000, v80
v_mfma_f32_16x16x32_bf16 a[88:91], v[196:199], v[20:23], a[88:91]
v_sub_f32 v104, v104, v70
v_sub_f32 v105, v105, v70
v_mfma_f32_16x16x32_bf16 a[92:95], v[204:207], v[20:23], a[92:95]
v_add_f32 v124, v104, v105
v_fmac_f32 v122, v104, v104
v_mfma_f32_16x16x32_bf16 a[92:95], v[200:203], v[16:19], a[92:95]
ds_read_b64_tr_b16 v[60:61], v243 offset:50816
ds_read_b64_tr_b16 v[62:63], v243 offset:51840
v_mfma_f32_16x16x32_bf16 a[24:27], v[208:211], v[16:19], a[24:27]
v_add_f32 v120, v120, v124
v_fmac_f32 v123, v105, v105
v_mfma_f32_16x16x32_bf16 a[24:27], v[212:215], v[20:23], a[24:27]
v_lshlrev_b32 v106, 16, v81
v_and_b32 v107, 0xffff0000, v81
v_mfma_f32_16x16x32_bf16 a[28:31], v[220:223], v[20:23], a[28:31]
v_sub_f32 v106, v106, v70
v_sub_f32 v107, v107, v70
v_mfma_f32_16x16x32_bf16 a[28:31], v[216:219], v[16:19], a[28:31]
v_add_f32 v124, v106, v107
v_fmac_f32 v122, v106, v106
v_mfma_f32_16x16x32_bf16 a[44:47], v[216:219], v[24:27], a[44:47]
v_add_f32 v120, v120, v124
v_fmac_f32 v123, v107, v107
v_mfma_f32_16x16x32_bf16 a[44:47], v[220:223], v[28:31], a[44:47]
v_lshlrev_b32 v108, 16, v82
v_and_b32 v109, 0xffff0000, v82
v_mfma_f32_16x16x32_bf16 a[40:43], v[212:215], v[28:31], a[40:43]
v_sub_f32 v108, v108, v70
v_sub_f32 v109, v109, v70
v_mfma_f32_16x16x32_bf16 a[40:43], v[208:211], v[24:27], a[40:43]
v_add_f32 v124, v108, v109
v_fmac_f32 v122, v108, v108
v_mfma_f32_16x16x32_bf16 a[252:255], v[200:203], v[24:27], a[252:255]
v_add_f32 v120, v120, v124
v_fmac_f32 v123, v109, v109
v_mfma_f32_16x16x32_bf16 a[252:255], v[204:207], v[28:31], a[252:255]
v_lshlrev_b32 v110, 16, v83
v_and_b32 v111, 0xffff0000, v83
v_mfma_f32_16x16x32_bf16 a[248:251], v[196:199], v[28:31], a[248:251]
s_waitcnt vmcnt(9) lgkmcnt(0)
s_barrier
v_mfma_f32_16x16x32_bf16 a[248:251], v[192:195], v[24:27], a[248:251]
v_sub_f32 v110, v110, v70
v_sub_f32 v111, v111, v70
v_mfma_f32_16x16x32_bf16 a[112:115], v[32:35], v[160:163], a[112:115]
v_add_f32 v124, v110, v111
v_fmac_f32 v122, v110, v110
v_mfma_f32_16x16x32_bf16 a[112:115], v[36:39], v[164:167], a[112:115]
v_add_f32 v120, v120, v124
v_fmac_f32 v123, v111, v111
v_mfma_f32_16x16x32_bf16 a[116:119], v[44:47], v[164:167], a[116:119]
ds_read_b64_tr_b16 v[192:193], v240 offset:0
ds_read_b64_tr_b16 v[194:195], v240 offset:1024
v_mfma_f32_16x16x32_bf16 a[116:119], v[40:43], v[160:163], a[116:119]
ds_read_b64_tr_b16 v[196:197], v240 offset:16896
ds_read_b64_tr_b16 v[198:199], v240 offset:17920
v_mfma_f32_16x16x32_bf16 a[128:131], v[48:51], v[160:163], a[128:131]
v_lshlrev_b32 v112, 16, v84
v_and_b32 v113, 0xffff0000, v84
v_mfma_f32_16x16x32_bf16 a[128:131], v[52:55], v[164:167], a[128:131]
v_sub_f32 v112, v112, v70
v_sub_f32 v113, v113, v70
v_mfma_f32_16x16x32_bf16 a[132:135], v[60:63], v[164:167], a[132:135]
v_add_f32 v124, v112, v113
v_fmac_f32 v122, v112, v112
v_mfma_f32_16x16x32_bf16 a[132:135], v[56:59], v[160:163], a[132:135]
ds_read_b128 v[160:163], v236 offset:24576
ds_read_b128 v[164:167], v237 offset:24576
v_mfma_f32_16x16x32_bf16 a[148:151], v[56:59], v[168:171], a[148:151]
v_add_f32 v120, v120, v124
v_fmac_f32 v123, v113, v113
v_mfma_f32_16x16x32_bf16 a[148:151], v[60:63], v[172:175], a[148:151]
v_lshlrev_b32 v114, 16, v85
v_and_b32 v115, 0xffff0000, v85
v_mfma_f32_16x16x32_bf16 a[144:147], v[52:55], v[172:175], a[144:147]
v_sub_f32 v114, v114, v70
v_sub_f32 v115, v115, v70
v_mfma_f32_16x16x32_bf16 a[144:147], v[48:51], v[168:171], a[144:147]
ds_read_b64_tr_b16 v[200:201], v241 offset:0
ds_read_b64_tr_b16 v[202:203], v241 offset:1024
v_mfma_f32_16x16x32_bf16 a[228:231], v[40:43], v[168:171], a[228:231]
v_add_f32 v124, v114, v115
v_fmac_f32 v122, v114, v114
v_mfma_f32_16x16x32_bf16 a[228:231], v[44:47], v[172:175], a[228:231]
v_add_f32 v120, v120, v124
v_fmac_f32 v123, v115, v115
v_mfma_f32_16x16x32_bf16 a[224:227], v[36:39], v[172:175], a[224:227]
v_lshlrev_b32 v116, 16, v86
v_and_b32 v117, 0xffff0000, v86
v_mfma_f32_16x16x32_bf16 a[224:227], v[32:35], v[168:171], a[224:227]
ds_read_b128 v[168:171], v236 offset:26624
ds_read_b128 v[172:175], v237 offset:26624
v_mfma_f32_16x16x32_bf16 a[208:211], v[32:35], v[176:179], a[208:211]
v_sub_f32 v116, v116, v70
v_sub_f32 v117, v117, v70
v_mfma_f32_16x16x32_bf16 a[208:211], v[36:39], v[180:183], a[208:211]
v_add_f32 v124, v116, v117
v_fmac_f32 v122, v116, v116
v_mfma_f32_16x16x32_bf16 a[212:215], v[44:47], v[180:183], a[212:215]
v_add_f32 v120, v120, v124
v_fmac_f32 v123, v117, v117
v_mfma_f32_16x16x32_bf16 a[212:215], v[40:43], v[176:179], a[212:215]
ds_read_b64_tr_b16 v[204:205], v241 offset:16896
ds_read_b64_tr_b16 v[206:207], v241 offset:17920
v_mfma_f32_16x16x32_bf16 a[160:163], v[48:51], v[176:179], a[160:163]
v_lshlrev_b32 v118, 16, v87
v_and_b32 v119, 0xffff0000, v87
v_mfma_f32_16x16x32_bf16 a[160:163], v[52:55], v[180:183], a[160:163]
v_sub_f32 v118, v118, v70
v_sub_f32 v119, v119, v70
v_mfma_f32_16x16x32_bf16 a[164:167], v[60:63], v[180:183], a[164:167]
v_add_f32 v124, v118, v119
v_fmac_f32 v122, v118, v118
v_mfma_f32_16x16x32_bf16 a[164:167], v[56:59], v[176:179], a[164:167]
ds_read_b128 v[176:179], v236 offset:16384
ds_read_b128 v[180:183], v237 offset:16384
v_mfma_f32_16x16x32_bf16 a[180:183], v[56:59], v[184:187], a[180:183]
v_add_f32 v120, v120, v124
v_fmac_f32 v123, v119, v119
v_mfma_f32_16x16x32_bf16 a[180:183], v[60:63], v[188:191], a[180:183]
v_add_f32 v121, v122, v123
v_mfma_f32_16x16x32_bf16 a[176:179], v[52:55], v[188:191], a[176:179]
v_add_f32_dpp v120, v120, v120 row_ror:8 row_mask:0xf bank_mask:0xf
v_mfma_f32_16x16x32_bf16 a[176:179], v[48:51], v[184:187], a[176:179]
ds_read_b64_tr_b16 v[208:209], v242 offset:0
ds_read_b64_tr_b16 v[210:211], v242 offset:1024
v_mfma_f32_16x16x32_bf16 a[196:199], v[40:43], v[184:187], a[196:199]
s_add_u32 m0, s83, 33792
v_add_f32_dpp v121, v121, v121 row_ror:8 row_mask:0xf bank_mask:0xf
v_mfma_f32_16x16x32_bf16 a[196:199], v[44:47], v[188:191], a[196:199]
buffer_load_dwordx4 v232, s[44:47], s80 offen lds
v_mfma_f32_16x16x32_bf16 a[192:195], v[36:39], v[188:191], a[192:195]
v_mov_b32 v124, v120
v_mov_b32 v125, v121
v_mfma_f32_16x16x32_bf16 a[192:195], v[32:35], v[184:187], a[192:195]
ds_read_b128 v[184:187], v236 offset:18432
ds_read_b128 v[188:191], v237 offset:18432
v_mfma_f32_16x16x32_bf16 a[64:67], v[32:35], v[0:3], a[64:67]
s_add_u32 m0, s83, 34816
v_permlane16_swap_b32 v124, v120
v_mfma_f32_16x16x32_bf16 a[64:67], v[36:39], v[4:7], a[64:67]
buffer_load_dwordx4 v233, s[44:47], s80 offen lds
v_mfma_f32_16x16x32_bf16 a[68:71], v[44:47], v[4:7], a[68:71]
v_permlane16_swap_b32 v125, v121
v_mfma_f32_16x16x32_bf16 a[68:71], v[40:43], v[0:3], a[68:71]
ds_read_b64_tr_b16 v[212:213], v242 offset:16896
ds_read_b64_tr_b16 v[214:215], v242 offset:17920
v_mfma_f32_16x16x32_bf16 a[0:3], v[48:51], v[0:3], a[0:3]
s_add_u32 m0, s83, 35968
v_add_f32 v120, v120, v124
v_mfma_f32_16x16x32_bf16 a[0:3], v[52:55], v[4:7], a[0:3]
buffer_load_dwordx4 v234, s[44:47], s80 offen lds
v_mfma_f32_16x16x32_bf16 a[4:7], v[60:63], v[4:7], a[4:7]
v_add_f32 v121, v121, v125
v_mov_b32 v124, v120
v_mfma_f32_16x16x32_bf16 a[4:7], v[56:59], v[0:3], a[4:7]
ds_read_b128 v[0:3], v236 offset:8192
ds_read_b128 v[4:7], v237 offset:8192
v_mfma_f32_16x16x32_bf16 a[20:23], v[56:59], v[8:11], a[20:23]
s_add_u32 m0, s83, 36992
v_mov_b32 v125, v121
v_mfma_f32_16x16x32_bf16 a[20:23], v[60:63], v[12:15], a[20:23]
buffer_load_dwordx4 v235, s[44:47], s80 offen lds
v_mfma_f32_16x16x32_bf16 a[16:19], v[52:55], v[12:15], a[16:19]
s_mov_b32 s80, 3932160
v_permlane32_swap_b32 v124, v120
v_mfma_f32_16x16x32_bf16 a[16:19], v[48:51], v[8:11], a[16:19]
ds_read_b64_tr_b16 v[216:217], v243 offset:0
ds_read_b64_tr_b16 v[218:219], v243 offset:1024
v_mfma_f32_16x16x32_bf16 a[84:87], v[40:43], v[8:11], a[84:87]
s_add_u32 m0, s83, 50688
v_permlane32_swap_b32 v125, v121
v_mfma_f32_16x16x32_bf16 a[84:87], v[44:47], v[12:15], a[84:87]
buffer_load_dwordx4 v232, s[44:47], s80 offen lds
v_mfma_f32_16x16x32_bf16 a[80:83], v[36:39], v[12:15], a[80:83]
v_add_f32 v120, v120, v124
v_add_f32 v121, v121, v125
v_mfma_f32_16x16x32_bf16 a[80:83], v[32:35], v[8:11], a[80:83]
ds_read_b128 v[8:11], v236 offset:10240
ds_read_b128 v[12:15], v237 offset:10240
v_mfma_f32_16x16x32_bf16 a[240:243], v[32:35], v[16:19], a[240:243]
s_add_u32 m0, s83, 51712
ds_write_b64 v127, v[120:121]
v_mfma_f32_16x16x32_bf16 a[240:243], v[36:39], v[20:23], a[240:243]
buffer_load_dwordx4 v233, s[44:47], s80 offen lds
v_mfma_f32_16x16x32_bf16 a[244:247], v[44:47], v[20:23], a[244:247]
s_nop 0
v_readfirstlane_b32 s92, v252
v_mfma_f32_16x16x32_bf16 a[244:247], v[40:43], v[16:19], a[244:247]
ds_read_b64_tr_b16 v[220:221], v243 offset:16896
ds_read_b64_tr_b16 v[222:223], v243 offset:17920
v_mfma_f32_16x16x32_bf16 a[32:35], v[48:51], v[16:19], a[32:35]
s_add_u32 m0, s83, 52864
v_readfirstlane_b32 s93, v253
v_mfma_f32_16x16x32_bf16 a[32:35], v[52:55], v[20:23], a[32:35]
buffer_load_dwordx4 v234, s[44:47], s80 offen lds
v_mfma_f32_16x16x32_bf16 a[36:39], v[60:63], v[20:23], a[36:39]
s_add_u32 s92, s92, 1
s_addc_u32 s93, s93, 0
v_mfma_f32_16x16x32_bf16 a[36:39], v[56:59], v[16:19], a[36:39]
ds_read_b128 v[16:19], v236 offset:0
ds_read_b128 v[20:23], v237 offset:0
v_mfma_f32_16x16x32_bf16 a[60:63], v[56:59], v[24:27], a[60:63]
s_add_u32 m0, s83, 53888
v_mov_b32 v252, s92
v_mfma_f32_16x16x32_bf16 a[60:63], v[60:63], v[28:31], a[60:63]
buffer_load_dwordx4 v235, s[44:47], s80 offen lds
v_mfma_f32_16x16x32_bf16 a[56:59], v[52:55], v[28:31], a[56:59]
s_mov_b32 s80, 0
v_mov_b32 v253, s93
v_mfma_f32_16x16x32_bf16 a[56:59], v[48:51], v[24:27], a[56:59]
v_mov_b32 v128, 0
v_mov_b32 v129, 0
v_mfma_f32_16x16x32_bf16 a[52:55], v[40:43], v[24:27], a[52:55]
v_mov_b32 v130, 0
v_mov_b32 v131, 0
v_mfma_f32_16x16x32_bf16 a[52:55], v[44:47], v[28:31], a[52:55]
buffer_store_dwordx4 v[128:131], v255, s[52:55], s95 offen sc1
v_mfma_f32_16x16x32_bf16 a[48:51], v[36:39], v[28:31], a[48:51]
s_waitcnt lgkmcnt(2)
v_mfma_f32_16x16x32_bf16 a[48:51], v[32:35], v[24:27], a[48:51]
ds_read_b128 v[24:27], v236 offset:2048
ds_read_b128 v[28:31], v237 offset:2048
v_mfma_f32_16x16x32_bf16 a[96:99], v[192:195], v[160:163], a[96:99]
v_mfma_f32_16x16x32_bf16 a[96:99], v[196:199], v[164:167], a[96:99]
v_mfma_f32_16x16x32_bf16 a[100:103], v[204:207], v[164:167], a[100:103]
v_mfma_f32_16x16x32_bf16 a[100:103], v[200:203], v[160:163], a[100:103]
ds_read_b64_tr_b16 v[32:33], v240 offset:128
ds_read_b64_tr_b16 v[34:35], v240 offset:1152
v_mfma_f32_16x16x32_bf16 a[104:107], v[208:211], v[160:163], a[104:107]
v_mfma_f32_16x16x32_bf16 a[104:107], v[212:215], v[164:167], a[104:107]
v_mfma_f32_16x16x32_bf16 a[108:111], v[220:223], v[164:167], a[108:111]
s_waitcnt lgkmcnt(2)
s_barrier
v_mfma_f32_16x16x32_bf16 a[108:111], v[216:219], v[160:163], a[108:111]
ds_read_b128 v[72:75], v126
ds_read_b128 v[76:79], v126 offset:16
v_mfma_f32_16x16x32_bf16 a[140:143], v[216:219], v[168:171], a[140:143]
s_xor_b32 m0, s82, 0
v_mfma_f32_16x16x32_bf16 a[140:143], v[220:223], v[172:175], a[140:143]
buffer_load_dwordx4 v224, s[40:43], s79 offen lds
v_mfma_f32_16x16x32_bf16 a[136:139], v[212:215], v[172:175], a[136:139]
v_mfma_f32_16x16x32_bf16 a[136:139], v[208:211], v[168:171], a[136:139]
ds_read_b64_tr_b16 v[36:37], v240 offset:17024
ds_read_b64_tr_b16 v[38:39], v240 offset:18048
v_mfma_f32_16x16x32_bf16 a[124:127], v[200:203], v[168:171], a[124:127]
s_xor_b32 m0, s82, 1024
v_mfma_f32_16x16x32_bf16 a[124:127], v[204:207], v[172:175], a[124:127]
buffer_load_dwordx4 v225, s[40:43], s79 offen lds
v_mfma_f32_16x16x32_bf16 a[120:123], v[196:199], v[172:175], a[120:123]
s_waitcnt lgkmcnt(2)
v_add_f32 v72, v72, v74
v_mfma_f32_16x16x32_bf16 a[120:123], v[192:195], v[168:171], a[120:123]
v_add_f32 v73, v73, v75
v_add_f32 v76, v76, v78
v_mfma_f32_16x16x32_bf16 a[232:235], v[192:195], v[176:179], a[232:235]
s_xor_b32 m0, s82, 2048
v_add_f32 v77, v77, v79
v_mfma_f32_16x16x32_bf16 a[232:235], v[196:199], v[180:183], a[232:235]
buffer_load_dwordx4 v226, s[40:43], s79 offen lds
v_mfma_f32_16x16x32_bf16 a[236:239], v[204:207], v[180:183], a[236:239]
v_add_f32 v72, v72, v76
v_add_f32 v73, v73, v77
v_mfma_f32_16x16x32_bf16 a[236:239], v[200:203], v[176:179], a[236:239]
ds_read_b64_tr_b16 v[40:41], v241 offset:128
ds_read_b64_tr_b16 v[42:43], v241 offset:1152
v_mfma_f32_16x16x32_bf16 a[152:155], v[208:211], v[176:179], a[152:155]
s_xor_b32 m0, s82, 3072
v_mul_f32 v72, 0x3a800000, v72
v_mfma_f32_16x16x32_bf16 a[152:155], v[212:215], v[180:183], a[152:155]
buffer_load_dwordx4 v227, s[40:43], s79 offen lds
v_mfma_f32_16x16x32_bf16 a[156:159], v[220:223], v[180:183], a[156:159]
v_mul_f32 v73, 0x3a800000, v73
v_add_f32 v124, v70, v72
v_mfma_f32_16x16x32_bf16 a[156:159], v[216:219], v[176:179], a[156:159]
v_fma_f32 v125, -v72, v72, v73
v_cvt_pk_bf16_f32 v124, v124, v124
v_mfma_f32_16x16x32_bf16 a[172:175], v[216:219], v[184:187], a[172:175]
s_xor_b32 m0, s82, 4096
v_lshlrev_b32 v124, 16, v124
v_mfma_f32_16x16x32_bf16 a[172:175], v[220:223], v[188:191], a[172:175]
buffer_load_dwordx4 v228, s[40:43], s79 offen lds
v_mfma_f32_16x16x32_bf16 a[168:171], v[212:215], v[188:191], a[168:171]
v_max_f32 v125, 0, v125
v_cvt_pk_bf16_f32 v125, v125, v125
v_mfma_f32_16x16x32_bf16 a[168:171], v[208:211], v[184:187], a[168:171]
ds_read_b64_tr_b16 v[44:45], v241 offset:17024
ds_read_b64_tr_b16 v[46:47], v241 offset:18048
v_mfma_f32_16x16x32_bf16 a[220:223], v[200:203], v[184:187], a[220:223]
s_xor_b32 m0, s82, 5120
v_lshlrev_b32 v125, 16, v125
v_mfma_f32_16x16x32_bf16 a[220:223], v[204:207], v[188:191], a[220:223]
buffer_load_dwordx4 v229, s[40:43], s79 offen lds
v_mfma_f32_16x16x32_bf16 a[216:219], v[196:199], v[188:191], a[216:219]
v_add_f32 v125, 0x3727c5ac, v125
v_cvt_pk_bf16_f32 v125, v125, v125
v_mfma_f32_16x16x32_bf16 a[216:219], v[192:195], v[184:187], a[216:219]
v_lshlrev_b32 v125, 16, v125
v_mfma_f32_16x16x32_bf16 a[200:203], v[192:195], v[0:3], a[200:203]
s_xor_b32 m0, s82, 6144
v_mfma_f32_16x16x32_bf16 a[200:203], v[196:199], v[4:7], a[200:203]
buffer_load_dwordx4 v230, s[40:43], s79 offen lds
v_mfma_f32_16x16x32_bf16 a[204:207], v[204:207], v[4:7], a[204:207]
v_sqrt_f32 v125, v125
v_mfma_f32_16x16x32_bf16 a[204:207], v[200:203], v[0:3], a[204:207]
ds_read_b64_tr_b16 v[48:49], v242 offset:128
ds_read_b64_tr_b16 v[50:51], v242 offset:1152
v_mfma_f32_16x16x32_bf16 a[184:187], v[208:211], v[0:3], a[184:187]
s_xor_b32 m0, s82, 7168
v_sub_f32 v124, v70, v124
v_mfma_f32_16x16x32_bf16 a[184:187], v[212:215], v[4:7], a[184:187]
buffer_load_dwordx4 v231, s[40:43], s79 offen lds
v_mfma_f32_16x16x32_bf16 a[188:191], v[220:223], v[4:7], a[188:191]
s_mov_b32 s79, 128
v_cvt_pk_bf16_f32 v125, v125, v125
v_mfma_f32_16x16x32_bf16 a[188:191], v[216:219], v[0:3], a[188:191]
v_lshlrev_b32 v125, 16, v125
v_mfma_f32_16x16x32_bf16 a[12:15], v[216:219], v[8:11], a[12:15]
v_rcp_f32 v125, v125
v_mfma_f32_16x16x32_bf16 a[12:15], v[220:223], v[12:15], a[12:15]
v_mul_f32 v125, 0x3e0293ee, v125
v_mul_f32 v124, v124, v125
v_mfma_f32_16x16x32_bf16 a[8:11], v[212:215], v[12:15], a[8:11]
v_fma_f32 v88, v88, v125, v124
v_fma_f32 v89, v89, v125, v124
v_mfma_f32_16x16x32_bf16 a[8:11], v[208:211], v[8:11], a[8:11]
ds_read_b64_tr_b16 v[52:53], v242 offset:17024
ds_read_b64_tr_b16 v[54:55], v242 offset:18048
v_mfma_f32_16x16x32_bf16 a[76:79], v[200:203], v[8:11], a[76:79]
v_cvt_pk_bf16_f32 v72, v88, v89
v_fma_f32 v90, v90, v125, v124
v_mfma_f32_16x16x32_bf16 a[76:79], v[204:207], v[12:15], a[76:79]
v_fma_f32 v91, v91, v125, v124
v_cvt_pk_bf16_f32 v73, v90, v91
v_mfma_f32_16x16x32_bf16 a[72:75], v[196:199], v[12:15], a[72:75]
v_fma_f32 v92, v92, v125, v124
v_fma_f32 v93, v93, v125, v124
v_mfma_f32_16x16x32_bf16 a[72:75], v[192:195], v[8:11], a[72:75]
ds_read_b64_tr_b16 v[56:57], v243 offset:128
ds_read_b64_tr_b16 v[58:59], v243 offset:1152
v_mfma_f32_16x16x32_bf16 a[88:91], v[192:195], v[16:19], a[88:91]
v_cvt_pk_bf16_f32 v74, v92, v93
v_fma_f32 v94, v94, v125, v124
v_mfma_f32_16x16x32_bf16 a[88:91], v[196:199], v[20:23], a[88:91]
v_fma_f32 v95, v95, v125, v124
v_cvt_pk_bf16_f32 v75, v94, v95
v_mfma_f32_16x16x32_bf16 a[92:95], v[204:207], v[20:23], a[92:95]
buffer_store_dwordx4 v[72:75], v71, s[56:59], 0 offen offset:0 sc1
v_mfma_f32_16x16x32_bf16 a[92:95], v[200:203], v[16:19], a[92:95]
ds_read_b64_tr_b16 v[60:61], v243 offset:17024
ds_read_b64_tr_b16 v[62:63], v243 offset:18048
v_mfma_f32_16x16x32_bf16 a[24:27], v[208:211], v[16:19], a[24:27]
v_fma_f32 v96, v96, v125, v124
v_fma_f32 v97, v97, v125, v124
v_mfma_f32_16x16x32_bf16 a[24:27], v[212:215], v[20:23], a[24:27]
v_cvt_pk_bf16_f32 v76, v96, v97
v_fma_f32 v98, v98, v125, v124
v_mfma_f32_16x16x32_bf16 a[28:31], v[220:223], v[20:23], a[28:31]
v_fma_f32 v99, v99, v125, v124
v_cvt_pk_bf16_f32 v77, v98, v99
v_mfma_f32_16x16x32_bf16 a[28:31], v[216:219], v[16:19], a[28:31]
v_fma_f32 v100, v100, v125, v124
v_fma_f32 v101, v101, v125, v124
v_mfma_f32_16x16x32_bf16 a[44:47], v[216:219], v[24:27], a[44:47]
v_cvt_pk_bf16_f32 v78, v100, v101
v_fma_f32 v102, v102, v125, v124
v_mfma_f32_16x16x32_bf16 a[44:47], v[220:223], v[28:31], a[44:47]
v_fma_f32 v103, v103, v125, v124
v_cvt_pk_bf16_f32 v79, v102, v103
v_mfma_f32_16x16x32_bf16 a[40:43], v[212:215], v[28:31], a[40:43]
buffer_store_dwordx4 v[76:79], v71, s[56:59], 0 offen offset:2048 sc1
v_mfma_f32_16x16x32_bf16 a[40:43], v[208:211], v[24:27], a[40:43]
v_fma_f32 v104, v104, v125, v124
v_fma_f32 v105, v105, v125, v124
v_mfma_f32_16x16x32_bf16 a[252:255], v[200:203], v[24:27], a[252:255]
v_cvt_pk_bf16_f32 v80, v104, v105
v_fma_f32 v106, v106, v125, v124
v_mfma_f32_16x16x32_bf16 a[252:255], v[204:207], v[28:31], a[252:255]
v_fma_f32 v107, v107, v125, v124
v_cvt_pk_bf16_f32 v81, v106, v107
v_mfma_f32_16x16x32_bf16 a[248:251], v[196:199], v[28:31], a[248:251]
s_waitcnt vmcnt(11) lgkmcnt(0)
s_barrier
v_mfma_f32_16x16x32_bf16 a[248:251], v[192:195], v[24:27], a[248:251]
v_fma_f32 v108, v108, v125, v124
v_fma_f32 v109, v109, v125, v124
v_mfma_f32_16x16x32_bf16 a[112:115], v[32:35], v[160:163], a[112:115]
v_cvt_pk_bf16_f32 v82, v108, v109
v_fma_f32 v110, v110, v125, v124
v_mfma_f32_16x16x32_bf16 a[112:115], v[36:39], v[164:167], a[112:115]
v_fma_f32 v111, v111, v125, v124
v_cvt_pk_bf16_f32 v83, v110, v111
v_mfma_f32_16x16x32_bf16 a[116:119], v[44:47], v[164:167], a[116:119]
ds_read_b64_tr_b16 v[192:193], v240 offset:33792
ds_read_b64_tr_b16 v[194:195], v240 offset:34816
v_mfma_f32_16x16x32_bf16 a[116:119], v[40:43], v[160:163], a[116:119]
ds_read_b64_tr_b16 v[196:197], v240 offset:50688
ds_read_b64_tr_b16 v[198:199], v240 offset:51712
v_mfma_f32_16x16x32_bf16 a[128:131], v[48:51], v[160:163], a[128:131]
buffer_store_dwordx4 v[80:83], v71, s[56:59], s88 offen offset:0 sc1
v_mfma_f32_16x16x32_bf16 a[128:131], v[52:55], v[164:167], a[128:131]
v_fma_f32 v112, v112, v125, v124
v_fma_f32 v113, v113, v125, v124
v_mfma_f32_16x16x32_bf16 a[132:135], v[60:63], v[164:167], a[132:135]
v_cvt_pk_bf16_f32 v84, v112, v113
v_fma_f32 v114, v114, v125, v124
v_mfma_f32_16x16x32_bf16 a[132:135], v[56:59], v[160:163], a[132:135]
ds_read_b128 v[160:163], v236 offset:57344
ds_read_b128 v[164:167], v237 offset:57344
v_mfma_f32_16x16x32_bf16 a[148:151], v[56:59], v[168:171], a[148:151]
v_fma_f32 v115, v115, v125, v124
v_cvt_pk_bf16_f32 v85, v114, v115
v_mfma_f32_16x16x32_bf16 a[148:151], v[60:63], v[172:175], a[148:151]
v_fma_f32 v116, v116, v125, v124
v_fma_f32 v117, v117, v125, v124
v_mfma_f32_16x16x32_bf16 a[144:147], v[52:55], v[172:175], a[144:147]
v_cvt_pk_bf16_f32 v86, v116, v117
v_fma_f32 v118, v118, v125, v124
v_mfma_f32_16x16x32_bf16 a[144:147], v[48:51], v[168:171], a[144:147]
ds_read_b64_tr_b16 v[200:201], v241 offset:33792
ds_read_b64_tr_b16 v[202:203], v241 offset:34816
v_mfma_f32_16x16x32_bf16 a[228:231], v[40:43], v[168:171], a[228:231]
v_fma_f32 v119, v119, v125, v124
v_cvt_pk_bf16_f32 v87, v118, v119
v_mfma_f32_16x16x32_bf16 a[228:231], v[44:47], v[172:175], a[228:231]
buffer_store_dwordx4 v[84:87], v71, s[56:59], s88 offen offset:2048 sc1
v_mfma_f32_16x16x32_bf16 a[224:227], v[36:39], v[172:175], a[224:227]
v_mfma_f32_16x16x32_bf16 a[224:227], v[32:35], v[168:171], a[224:227]
ds_read_b128 v[168:171], v236 offset:59392
ds_read_b128 v[172:175], v237 offset:59392
v_mfma_f32_16x16x32_bf16 a[208:211], v[32:35], v[176:179], a[208:211]
v_mfma_f32_16x16x32_bf16 a[208:211], v[36:39], v[180:183], a[208:211]
v_mfma_f32_16x16x32_bf16 a[212:215], v[44:47], v[180:183], a[212:215]
v_mfma_f32_16x16x32_bf16 a[212:215], v[40:43], v[176:179], a[212:215]
ds_read_b64_tr_b16 v[204:205], v241 offset:50688
ds_read_b64_tr_b16 v[206:207], v241 offset:51712
v_mfma_f32_16x16x32_bf16 a[160:163], v[48:51], v[176:179], a[160:163]
v_mfma_f32_16x16x32_bf16 a[160:163], v[52:55], v[180:183], a[160:163]
v_mfma_f32_16x16x32_bf16 a[164:167], v[60:63], v[180:183], a[164:167]
v_mfma_f32_16x16x32_bf16 a[164:167], v[56:59], v[176:179], a[164:167]
ds_read_b128 v[176:179], v236 offset:49152
ds_read_b128 v[180:183], v237 offset:49152
v_mfma_f32_16x16x32_bf16 a[180:183], v[56:59], v[184:187], a[180:183]
v_mfma_f32_16x16x32_bf16 a[180:183], v[60:63], v[188:191], a[180:183]
v_mfma_f32_16x16x32_bf16 a[176:179], v[52:55], v[188:191], a[176:179]
v_mfma_f32_16x16x32_bf16 a[176:179], v[48:51], v[184:187], a[176:179]
ds_read_b64_tr_b16 v[208:209], v242 offset:33792
ds_read_b64_tr_b16 v[210:211], v242 offset:34816
v_mfma_f32_16x16x32_bf16 a[196:199], v[40:43], v[184:187], a[196:199]
s_add_u32 m0, s83, 0
v_mfma_f32_16x16x32_bf16 a[196:199], v[44:47], v[188:191], a[196:199]
buffer_load_dwordx4 v232, s[44:47], s80 offen lds
.La5_LW1_1:
s_cbranch_execz .La5_LW1_5
v_mfma_f32_16x16x32_bf16 a[192:195], v[36:39], v[188:191], a[192:195]
v_mfma_f32_16x16x32_bf16 a[192:195], v[32:35], v[184:187], a[192:195]
ds_read_b128 v[184:187], v236 offset:51200
ds_read_b128 v[188:191], v237 offset:51200
v_mfma_f32_16x16x32_bf16 a[64:67], v[32:35], v[0:3], a[64:67]
s_add_u32 m0, s83, 1024
v_mfma_f32_16x16x32_bf16 a[64:67], v[36:39], v[4:7], a[64:67]
buffer_load_dwordx4 v233, s[44:47], s80 offen lds
v_mfma_f32_16x16x32_bf16 a[68:71], v[44:47], v[4:7], a[68:71]
v_mfma_f32_16x16x32_bf16 a[68:71], v[40:43], v[0:3], a[68:71]
ds_read_b64_tr_b16 v[212:213], v242 offset:50688
ds_read_b64_tr_b16 v[214:215], v242 offset:51712
v_mfma_f32_16x16x32_bf16 a[0:3], v[48:51], v[0:3], a[0:3]
s_add_u32 m0, s83, 2176
v_mfma_f32_16x16x32_bf16 a[0:3], v[52:55], v[4:7], a[0:3]
buffer_load_dwordx4 v234, s[44:47], s80 offen lds
v_mfma_f32_16x16x32_bf16 a[4:7], v[60:63], v[4:7], a[4:7]
v_mfma_f32_16x16x32_bf16 a[4:7], v[56:59], v[0:3], a[4:7]
ds_read_b128 v[0:3], v236 offset:40960
ds_read_b128 v[4:7], v237 offset:40960
v_mfma_f32_16x16x32_bf16 a[20:23], v[56:59], v[8:11], a[20:23]
s_add_u32 m0, s83, 3200
v_mfma_f32_16x16x32_bf16 a[20:23], v[60:63], v[12:15], a[20:23]
buffer_load_dwordx4 v235, s[44:47], s80 offen lds
v_mfma_f32_16x16x32_bf16 a[16:19], v[52:55], v[12:15], a[16:19]
s_mov_b32 s80, 262144
v_mfma_f32_16x16x32_bf16 a[16:19], v[48:51], v[8:11], a[16:19]
ds_read_b64_tr_b16 v[216:217], v243 offset:33792
ds_read_b64_tr_b16 v[218:219], v243 offset:34816
v_mfma_f32_16x16x32_bf16 a[84:87], v[40:43], v[8:11], a[84:87]
s_add_u32 m0, s83, 16896
v_mfma_f32_16x16x32_bf16 a[84:87], v[44:47], v[12:15], a[84:87]
buffer_load_dwordx4 v232, s[44:47], s80 offen lds
v_mfma_f32_16x16x32_bf16 a[80:83], v[36:39], v[12:15], a[80:83]
v_mfma_f32_16x16x32_bf16 a[80:83], v[32:35], v[8:11], a[80:83]
ds_read_b128 v[8:11], v236 offset:43008
ds_read_b128 v[12:15], v237 offset:43008
v_mfma_f32_16x16x32_bf16 a[240:243], v[32:35], v[16:19], a[240:243]
s_add_u32 m0, s83, 17920
v_mfma_f32_16x16x32_bf16 a[240:243], v[36:39], v[20:23], a[240:243]
buffer_load_dwordx4 v233, s[44:47], s80 offen lds
v_mfma_f32_16x16x32_bf16 a[244:247], v[44:47], v[20:23], a[244:247]
v_mfma_f32_16x16x32_bf16 a[244:247], v[40:43], v[16:19], a[244:247]
ds_read_b64_tr_b16 v[220:221], v243 offset:50688
ds_read_b64_tr_b16 v[222:223], v243 offset:51712
v_mfma_f32_16x16x32_bf16 a[32:35], v[48:51], v[16:19], a[32:35]
s_add_u32 m0, s83, 19072
v_mfma_f32_16x16x32_bf16 a[32:35], v[52:55], v[20:23], a[32:35]
buffer_load_dwordx4 v234, s[44:47], s80 offen lds
v_mfma_f32_16x16x32_bf16 a[36:39], v[60:63], v[20:23], a[36:39]
v_mfma_f32_16x16x32_bf16 a[36:39], v[56:59], v[16:19], a[36:39]
ds_read_b128 v[16:19], v236 offset:32768
ds_read_b128 v[20:23], v237 offset:32768
v_mfma_f32_16x16x32_bf16 a[60:63], v[56:59], v[24:27], a[60:63]
s_add_u32 m0, s83, 20096
v_mfma_f32_16x16x32_bf16 a[60:63], v[60:63], v[28:31], a[60:63]
buffer_load_dwordx4 v235, s[44:47], s80 offen lds
v_mfma_f32_16x16x32_bf16 a[56:59], v[52:55], v[28:31], a[56:59]
s_mov_b32 s80, 524288
v_mfma_f32_16x16x32_bf16 a[56:59], v[48:51], v[24:27], a[56:59]
v_mfma_f32_16x16x32_bf16 a[52:55], v[40:43], v[24:27], a[52:55]
v_mfma_f32_16x16x32_bf16 a[52:55], v[44:47], v[28:31], a[52:55]
v_mfma_f32_16x16x32_bf16 a[48:51], v[36:39], v[28:31], a[48:51]
s_waitcnt lgkmcnt(2)
v_mfma_f32_16x16x32_bf16 a[48:51], v[32:35], v[24:27], a[48:51]
ds_read_b128 v[24:27], v236 offset:34816
ds_read_b128 v[28:31], v237 offset:34816
v_mfma_f32_16x16x32_bf16 a[96:99], v[192:195], v[160:163], a[96:99]
v_mfma_f32_16x16x32_bf16 a[96:99], v[196:199], v[164:167], a[96:99]
v_mfma_f32_16x16x32_bf16 a[100:103], v[204:207], v[164:167], a[100:103]
v_mfma_f32_16x16x32_bf16 a[100:103], v[200:203], v[160:163], a[100:103]
ds_read_b64_tr_b16 v[32:33], v240 offset:33920
ds_read_b64_tr_b16 v[34:35], v240 offset:34944
v_mfma_f32_16x16x32_bf16 a[104:107], v[208:211], v[160:163], a[104:107]
v_mfma_f32_16x16x32_bf16 a[104:107], v[212:215], v[164:167], a[104:107]
v_mfma_f32_16x16x32_bf16 a[108:111], v[220:223], v[164:167], a[108:111]
s_waitcnt lgkmcnt(2)
s_barrier
v_mfma_f32_16x16x32_bf16 a[108:111], v[216:219], v[160:163], a[108:111]
v_mfma_f32_16x16x32_bf16 a[140:143], v[216:219], v[168:171], a[140:143]
s_xor_b32 m0, s82, 32768
v_mfma_f32_16x16x32_bf16 a[140:143], v[220:223], v[172:175], a[140:143]
buffer_load_dwordx4 v224, s[40:43], s79 offen lds
v_mfma_f32_16x16x32_bf16 a[136:139], v[212:215], v[172:175], a[136:139]
v_mfma_f32_16x16x32_bf16 a[136:139], v[208:211], v[168:171], a[136:139]
ds_read_b64_tr_b16 v[36:37], v240 offset:50816
ds_read_b64_tr_b16 v[38:39], v240 offset:51840
v_mfma_f32_16x16x32_bf16 a[124:127], v[200:203], v[168:171], a[124:127]
s_xor_b32 m0, s82, 33792
v_mfma_f32_16x16x32_bf16 a[124:127], v[204:207], v[172:175], a[124:127]
buffer_load_dwordx4 v225, s[40:43], s79 offen lds
v_mfma_f32_16x16x32_bf16 a[120:123], v[196:199], v[172:175], a[120:123]
v_mfma_f32_16x16x32_bf16 a[120:123], v[192:195], v[168:171], a[120:123]
v_mfma_f32_16x16x32_bf16 a[232:235], v[192:195], v[176:179], a[232:235]
s_xor_b32 m0, s82, 34816
v_mfma_f32_16x16x32_bf16 a[232:235], v[196:199], v[180:183], a[232:235]
buffer_load_dwordx4 v226, s[40:43], s79 offen lds
v_mfma_f32_16x16x32_bf16 a[236:239], v[204:207], v[180:183], a[236:239]
v_mfma_f32_16x16x32_bf16 a[236:239], v[200:203], v[176:179], a[236:239]
ds_read_b64_tr_b16 v[40:41], v241 offset:33920
ds_read_b64_tr_b16 v[42:43], v241 offset:34944
v_mfma_f32_16x16x32_bf16 a[152:155], v[208:211], v[176:179], a[152:155]
s_xor_b32 m0, s82, 35840
v_mfma_f32_16x16x32_bf16 a[152:155], v[212:215], v[180:183], a[152:155]
buffer_load_dwordx4 v227, s[40:43], s79 offen lds
v_mfma_f32_16x16x32_bf16 a[156:159], v[220:223], v[180:183], a[156:159]
v_mfma_f32_16x16x32_bf16 a[156:159], v[216:219], v[176:179], a[156:159]
v_mfma_f32_16x16x32_bf16 a[172:175], v[216:219], v[184:187], a[172:175]
s_xor_b32 m0, s82, 36864
v_mfma_f32_16x16x32_bf16 a[172:175], v[220:223], v[188:191], a[172:175]
buffer_load_dwordx4 v228, s[40:43], s79 offen lds
v_mfma_f32_16x16x32_bf16 a[168:171], v[212:215], v[188:191], a[168:171]
v_mfma_f32_16x16x32_bf16 a[168:171], v[208:211], v[184:187], a[168:171]
ds_read_b64_tr_b16 v[44:45], v241 offset:50816
ds_read_b64_tr_b16 v[46:47], v241 offset:51840
v_mfma_f32_16x16x32_bf16 a[220:223], v[200:203], v[184:187], a[220:223]
s_xor_b32 m0, s82, 37888
v_mfma_f32_16x16x32_bf16 a[220:223], v[204:207], v[188:191], a[220:223]
buffer_load_dwordx4 v229, s[40:43], s79 offen lds
v_mfma_f32_16x16x32_bf16 a[216:219], v[196:199], v[188:191], a[216:219]
v_mfma_f32_16x16x32_bf16 a[216:219], v[192:195], v[184:187], a[216:219]
v_mfma_f32_16x16x32_bf16 a[200:203], v[192:195], v[0:3], a[200:203]
s_xor_b32 m0, s82, 38912
v_mfma_f32_16x16x32_bf16 a[200:203], v[196:199], v[4:7], a[200:203]
buffer_load_dwordx4 v230, s[40:43], s79 offen lds
v_mfma_f32_16x16x32_bf16 a[204:207], v[204:207], v[4:7], a[204:207]
v_mfma_f32_16x16x32_bf16 a[204:207], v[200:203], v[0:3], a[204:207]
ds_read_b64_tr_b16 v[48:49], v242 offset:33920
ds_read_b64_tr_b16 v[50:51], v242 offset:34944
v_mfma_f32_16x16x32_bf16 a[184:187], v[208:211], v[0:3], a[184:187]
s_xor_b32 m0, s82, 39936
v_mfma_f32_16x16x32_bf16 a[184:187], v[212:215], v[4:7], a[184:187]
buffer_load_dwordx4 v231, s[40:43], s79 offen lds
v_mfma_f32_16x16x32_bf16 a[188:191], v[220:223], v[4:7], a[188:191]
s_mov_b32 s79, 256
v_mfma_f32_16x16x32_bf16 a[188:191], v[216:219], v[0:3], a[188:191]
v_mfma_f32_16x16x32_bf16 a[12:15], v[216:219], v[8:11], a[12:15]
v_mfma_f32_16x16x32_bf16 a[12:15], v[220:223], v[12:15], a[12:15]
buffer_load_dword v254, v135, s[68:71], s96 offen
v_mfma_f32_16x16x32_bf16 a[8:11], v[212:215], v[12:15], a[8:11]
s_add_u32 s96, s96, 1048576
v_mfma_f32_16x16x32_bf16 a[8:11], v[208:211], v[8:11], a[8:11]
ds_read_b64_tr_b16 v[52:53], v242 offset:50816
ds_read_b64_tr_b16 v[54:55], v242 offset:51840
v_mfma_f32_16x16x32_bf16 a[76:79], v[200:203], v[8:11], a[76:79]
v_mfma_f32_16x16x32_bf16 a[76:79], v[204:207], v[12:15], a[76:79]
buffer_load_dword v254, v135, s[68:71], s96 offen
v_mfma_f32_16x16x32_bf16 a[72:75], v[196:199], v[12:15], a[72:75]
s_add_u32 s96, s96, 1048576
v_mfma_f32_16x16x32_bf16 a[72:75], v[192:195], v[8:11], a[72:75]
ds_read_b64_tr_b16 v[56:57], v243 offset:33920
ds_read_b64_tr_b16 v[58:59], v243 offset:34944
v_mfma_f32_16x16x32_bf16 a[88:91], v[192:195], v[16:19], a[88:91]
v_mfma_f32_16x16x32_bf16 a[88:91], v[196:199], v[20:23], a[88:91]
s_waitcnt vmcnt(18)
buffer_store_dwordx2 v[252:253], off, s[64:67], s91 offset:1024 sc1
v_mfma_f32_16x16x32_bf16 a[92:95], v[204:207], v[20:23], a[92:95]
s_mov_b32 s91, 0
s_lshl_b32 s92, s76, 19
v_mfma_f32_16x16x32_bf16 a[92:95], v[200:203], v[16:19], a[92:95]
ds_read_b64_tr_b16 v[60:61], v243 offset:50816
ds_read_b64_tr_b16 v[62:63], v243 offset:51840
v_mfma_f32_16x16x32_bf16 a[24:27], v[208:211], v[16:19], a[24:27]
s_add_u32 s92, s92, 88080384
v_mfma_f32_16x16x32_bf16 a[24:27], v[212:215], v[20:23], a[24:27]
s_add_u32 s52, s26, s92
s_addc_u32 s53, s27, 0
v_mfma_f32_16x16x32_bf16 a[28:31], v[220:223], v[20:23], a[28:31]
s_and_b32 s53, s53, 0xffff
s_mov_b32 s54, 524672
v_mfma_f32_16x16x32_bf16 a[28:31], v[216:219], v[16:19], a[28:31]
s_mov_b32 s55, 0x20000
s_lshl_b32 s92, s3, 16
v_mfma_f32_16x16x32_bf16 a[44:47], v[216:219], v[24:27], a[44:47]
s_sub_u32 s93, 458752, s92
v_add_u32 v244, s92, v136
v_mfma_f32_16x16x32_bf16 a[44:47], v[220:223], v[28:31], a[44:47]
v_add_u32 v246, s93, v136
v_add_u32 v245, 0x8000, v244
v_mfma_f32_16x16x32_bf16 a[40:43], v[212:215], v[28:31], a[40:43]
v_add_u32 v247, 0x8000, v246
s_lshl_b32 s94, s72, 19
v_mfma_f32_16x16x32_bf16 a[40:43], v[208:211], v[24:27], a[40:43]
s_lshl_b32 s93, s76, 16
s_add_u32 s92, s94, s93
v_mfma_f32_16x16x32_bf16 a[252:255], v[200:203], v[24:27], a[252:255]
s_lshl_b32 s93, s77, 13
s_add_u32 s92, s92, s93
v_mfma_f32_16x16x32_bf16 a[252:255], v[204:207], v[28:31], a[252:255]
s_add_u32 s92, s92, 20971520
v_mfma_f32_16x16x32_bf16 a[248:251], v[196:199], v[28:31], a[248:251]
s_waitcnt vmcnt(11) lgkmcnt(0)
s_barrier
v_mfma_f32_16x16x32_bf16 a[248:251], v[192:195], v[24:27], a[248:251]
s_add_u32 s48, s26, s92
s_addc_u32 s49, s27, 0
v_mfma_f32_16x16x32_bf16 a[112:115], v[32:35], v[160:163], a[112:115]
s_and_b32 s49, s49, 0xffff
s_mov_b32 s50, 57344
v_mfma_f32_16x16x32_bf16 a[112:115], v[36:39], v[164:167], a[112:115]
s_mov_b32 s51, 0x20000
s_lshl_b32 s93, s77, 24
v_mfma_f32_16x16x32_bf16 a[116:119], v[44:47], v[164:167], a[116:119]
ds_read_b64_tr_b16 v[192:193], v240 offset:0
ds_read_b64_tr_b16 v[194:195], v240 offset:1024
v_mfma_f32_16x16x32_bf16 a[116:119], v[40:43], v[160:163], a[116:119]
ds_read_b64_tr_b16 v[196:197], v240 offset:16896
ds_read_b64_tr_b16 v[198:199], v240 offset:17920
v_mfma_f32_16x16x32_bf16 a[128:131], v[48:51], v[160:163], a[128:131]
s_add_u32 s92, s94, s93
s_add_u32 s92, s92, 20971520
v_mfma_f32_16x16x32_bf16 a[128:131], v[52:55], v[164:167], a[128:131]
s_add_u32 s60, s26, s92
s_addc_u32 s61, s27, 0
v_mfma_f32_16x16x32_bf16 a[132:135], v[60:63], v[164:167], a[132:135]
s_and_b32 s61, s61, 0xffff
s_mov_b32 s62, 524288
v_mfma_f32_16x16x32_bf16 a[132:135], v[56:59], v[160:163], a[132:135]
ds_read_b128 v[160:163], v236 offset:24576
ds_read_b128 v[164:167], v237 offset:24576
v_mfma_f32_16x16x32_bf16 a[148:151], v[56:59], v[168:171], a[148:151]
s_mov_b32 s63, 0x20000
s_lshl_b32 s92, s77, 14
v_mfma_f32_16x16x32_bf16 a[148:151], v[60:63], v[172:175], a[148:151]
s_sub_u32 s95, 16384, s92
v_add_u32 v238, s95, v255
v_mfma_f32_16x16x32_bf16 a[144:147], v[52:55], v[172:175], a[144:147]
v_mfma_f32_16x16x32_bf16 a[144:147], v[48:51], v[168:171], a[144:147]
ds_read_b64_tr_b16 v[200:201], v241 offset:0
ds_read_b64_tr_b16 v[202:203], v241 offset:1024
v_mfma_f32_16x16x32_bf16 a[228:231], v[40:43], v[168:171], a[228:231]
v_mfma_f32_16x16x32_bf16 a[228:231], v[44:47], v[172:175], a[228:231]
v_mfma_f32_16x16x32_bf16 a[224:227], v[36:39], v[172:175], a[224:227]
v_mfma_f32_16x16x32_bf16 a[224:227], v[32:35], v[168:171], a[224:227]
ds_read_b128 v[168:171], v236 offset:26624
ds_read_b128 v[172:175], v237 offset:26624
v_mfma_f32_16x16x32_bf16 a[208:211], v[32:35], v[176:179], a[208:211]
v_mfma_f32_16x16x32_bf16 a[208:211], v[36:39], v[180:183], a[208:211]
v_mfma_f32_16x16x32_bf16 a[212:215], v[44:47], v[180:183], a[212:215]
v_mfma_f32_16x16x32_bf16 a[212:215], v[40:43], v[176:179], a[212:215]
ds_read_b64_tr_b16 v[204:205], v241 offset:16896
ds_read_b64_tr_b16 v[206:207], v241 offset:17920
v_mfma_f32_16x16x32_bf16 a[160:163], v[48:51], v[176:179], a[160:163]
v_mfma_f32_16x16x32_bf16 a[160:163], v[52:55], v[180:183], a[160:163]
v_mfma_f32_16x16x32_bf16 a[164:167], v[60:63], v[180:183], a[164:167]
v_mfma_f32_16x16x32_bf16 a[164:167], v[56:59], v[176:179], a[164:167]
ds_read_b128 v[176:179], v236 offset:16384
ds_read_b128 v[180:183], v237 offset:16384
v_mfma_f32_16x16x32_bf16 a[180:183], v[56:59], v[184:187], a[180:183]
v_mfma_f32_16x16x32_bf16 a[180:183], v[60:63], v[188:191], a[180:183]
v_mfma_f32_16x16x32_bf16 a[176:179], v[52:55], v[188:191], a[176:179]
v_mfma_f32_16x16x32_bf16 a[176:179], v[48:51], v[184:187], a[176:179]
ds_read_b64_tr_b16 v[208:209], v242 offset:0
ds_read_b64_tr_b16 v[210:211], v242 offset:1024
v_mfma_f32_16x16x32_bf16 a[196:199], v[40:43], v[184:187], a[196:199]
s_add_u32 m0, s83, 33792
v_mfma_f32_16x16x32_bf16 a[196:199], v[44:47], v[188:191], a[196:199]
buffer_load_dwordx4 v232, s[44:47], s80 offen lds
v_mfma_f32_16x16x32_bf16 a[192:195], v[36:39], v[188:191], a[192:195]
v_mfma_f32_16x16x32_bf16 a[192:195], v[32:35], v[184:187], a[192:195]
ds_read_b128 v[184:187], v236 offset:18432
ds_read_b128 v[188:191], v237 offset:18432
v_mfma_f32_16x16x32_bf16 a[64:67], v[32:35], v[0:3], a[64:67]
s_add_u32 m0, s83, 34816
v_mfma_f32_16x16x32_bf16 a[64:67], v[36:39], v[4:7], a[64:67]
buffer_load_dwordx4 v233, s[44:47], s80 offen lds
v_mfma_f32_16x16x32_bf16 a[68:71], v[44:47], v[4:7], a[68:71]
v_mfma_f32_16x16x32_bf16 a[68:71], v[40:43], v[0:3], a[68:71]
ds_read_b64_tr_b16 v[212:213], v242 offset:16896
ds_read_b64_tr_b16 v[214:215], v242 offset:17920
v_mfma_f32_16x16x32_bf16 a[0:3], v[48:51], v[0:3], a[0:3]
s_add_u32 m0, s83, 35968
v_mfma_f32_16x16x32_bf16 a[0:3], v[52:55], v[4:7], a[0:3]
buffer_load_dwordx4 v234, s[44:47], s80 offen lds
v_mfma_f32_16x16x32_bf16 a[4:7], v[60:63], v[4:7], a[4:7]
v_mfma_f32_16x16x32_bf16 a[4:7], v[56:59], v[0:3], a[4:7]
ds_read_b128 v[0:3], v236 offset:8192
ds_read_b128 v[4:7], v237 offset:8192
v_mfma_f32_16x16x32_bf16 a[20:23], v[56:59], v[8:11], a[20:23]
s_add_u32 m0, s83, 36992
v_mfma_f32_16x16x32_bf16 a[20:23], v[60:63], v[12:15], a[20:23]
buffer_load_dwordx4 v235, s[44:47], s80 offen lds
v_mfma_f32_16x16x32_bf16 a[16:19], v[52:55], v[12:15], a[16:19]
s_mov_b32 s80, 786432
v_mfma_f32_16x16x32_bf16 a[16:19], v[48:51], v[8:11], a[16:19]
ds_read_b64_tr_b16 v[216:217], v243 offset:0
ds_read_b64_tr_b16 v[218:219], v243 offset:1024
v_mfma_f32_16x16x32_bf16 a[84:87], v[40:43], v[8:11], a[84:87]
s_add_u32 m0, s83, 50688
v_mfma_f32_16x16x32_bf16 a[84:87], v[44:47], v[12:15], a[84:87]
buffer_load_dwordx4 v232, s[44:47], s80 offen lds
v_mfma_f32_16x16x32_bf16 a[80:83], v[36:39], v[12:15], a[80:83]
v_mfma_f32_16x16x32_bf16 a[80:83], v[32:35], v[8:11], a[80:83]
ds_read_b128 v[8:11], v236 offset:10240
ds_read_b128 v[12:15], v237 offset:10240
v_mfma_f32_16x16x32_bf16 a[240:243], v[32:35], v[16:19], a[240:243]
s_add_u32 m0, s83, 51712
v_mfma_f32_16x16x32_bf16 a[240:243], v[36:39], v[20:23], a[240:243]
buffer_load_dwordx4 v233, s[44:47], s80 offen lds
v_mfma_f32_16x16x32_bf16 a[244:247], v[44:47], v[20:23], a[244:247]
v_mfma_f32_16x16x32_bf16 a[244:247], v[40:43], v[16:19], a[244:247]
ds_read_b64_tr_b16 v[220:221], v243 offset:16896
ds_read_b64_tr_b16 v[222:223], v243 offset:17920
v_mfma_f32_16x16x32_bf16 a[32:35], v[48:51], v[16:19], a[32:35]
s_add_u32 m0, s83, 52864
v_mfma_f32_16x16x32_bf16 a[32:35], v[52:55], v[20:23], a[32:35]
buffer_load_dwordx4 v234, s[44:47], s80 offen lds
v_mfma_f32_16x16x32_bf16 a[36:39], v[60:63], v[20:23], a[36:39]
v_mfma_f32_16x16x32_bf16 a[36:39], v[56:59], v[16:19], a[36:39]
ds_read_b128 v[16:19], v236 offset:0
ds_read_b128 v[20:23], v237 offset:0
v_mfma_f32_16x16x32_bf16 a[60:63], v[56:59], v[24:27], a[60:63]
s_add_u32 m0, s83, 53888
v_mfma_f32_16x16x32_bf16 a[60:63], v[60:63], v[28:31], a[60:63]
buffer_load_dwordx4 v235, s[44:47], s80 offen lds
v_mfma_f32_16x16x32_bf16 a[56:59], v[52:55], v[28:31], a[56:59]
s_mov_b32 s80, 1048576
v_mfma_f32_16x16x32_bf16 a[56:59], v[48:51], v[24:27], a[56:59]
v_mfma_f32_16x16x32_bf16 a[52:55], v[40:43], v[24:27], a[52:55]
v_mfma_f32_16x16x32_bf16 a[52:55], v[44:47], v[28:31], a[52:55]
v_mfma_f32_16x16x32_bf16 a[48:51], v[36:39], v[28:31], a[48:51]
s_waitcnt lgkmcnt(2)
v_mfma_f32_16x16x32_bf16 a[48:51], v[32:35], v[24:27], a[48:51]
ds_read_b128 v[24:27], v236 offset:2048
ds_read_b128 v[28:31], v237 offset:2048
v_mfma_f32_16x16x32_bf16 a[96:99], v[192:195], v[160:163], a[96:99]
v_mfma_f32_16x16x32_bf16 a[96:99], v[196:199], v[164:167], a[96:99]
v_mfma_f32_16x16x32_bf16 a[100:103], v[204:207], v[164:167], a[100:103]
v_mfma_f32_16x16x32_bf16 a[100:103], v[200:203], v[160:163], a[100:103]
ds_read_b64_tr_b16 v[32:33], v240 offset:128
ds_read_b64_tr_b16 v[34:35], v240 offset:1152
v_mfma_f32_16x16x32_bf16 a[104:107], v[208:211], v[160:163], a[104:107]
v_mfma_f32_16x16x32_bf16 a[104:107], v[212:215], v[164:167], a[104:107]
v_mfma_f32_16x16x32_bf16 a[108:111], v[220:223], v[164:167], a[108:111]
s_waitcnt lgkmcnt(2)
s_barrier
v_mfma_f32_16x16x32_bf16 a[108:111], v[216:219], v[160:163], a[108:111]
v_mfma_f32_16x16x32_bf16 a[140:143], v[216:219], v[168:171], a[140:143]
s_xor_b32 m0, s82, 0
v_mfma_f32_16x16x32_bf16 a[140:143], v[220:223], v[172:175], a[140:143]
buffer_load_dwordx4 v224, s[40:43], s79 offen lds
v_mfma_f32_16x16x32_bf16 a[136:139], v[212:215], v[172:175], a[136:139]
v_mfma_f32_16x16x32_bf16 a[136:139], v[208:211], v[168:171], a[136:139]
ds_read_b64_tr_b16 v[36:37], v240 offset:17024
ds_read_b64_tr_b16 v[38:39], v240 offset:18048
v_mfma_f32_16x16x32_bf16 a[124:127], v[200:203], v[168:171], a[124:127]
s_xor_b32 m0, s82, 1024
v_mfma_f32_16x16x32_bf16 a[124:127], v[204:207], v[172:175], a[124:127]
buffer_load_dwordx4 v225, s[40:43], s79 offen lds
v_mfma_f32_16x16x32_bf16 a[120:123], v[196:199], v[172:175], a[120:123]
v_mfma_f32_16x16x32_bf16 a[120:123], v[192:195], v[168:171], a[120:123]
v_mfma_f32_16x16x32_bf16 a[232:235], v[192:195], v[176:179], a[232:235]
s_xor_b32 m0, s82, 2048
v_mfma_f32_16x16x32_bf16 a[232:235], v[196:199], v[180:183], a[232:235]
buffer_load_dwordx4 v226, s[40:43], s79 offen lds
v_mfma_f32_16x16x32_bf16 a[236:239], v[204:207], v[180:183], a[236:239]
v_mfma_f32_16x16x32_bf16 a[236:239], v[200:203], v[176:179], a[236:239]
ds_read_b64_tr_b16 v[40:41], v241 offset:128
ds_read_b64_tr_b16 v[42:43], v241 offset:1152
v_mfma_f32_16x16x32_bf16 a[152:155], v[208:211], v[176:179], a[152:155]
s_xor_b32 m0, s82, 3072
v_mfma_f32_16x16x32_bf16 a[152:155], v[212:215], v[180:183], a[152:155]
buffer_load_dwordx4 v227, s[40:43], s79 offen lds
v_mfma_f32_16x16x32_bf16 a[156:159], v[220:223], v[180:183], a[156:159]
v_mfma_f32_16x16x32_bf16 a[156:159], v[216:219], v[176:179], a[156:159]
v_mfma_f32_16x16x32_bf16 a[172:175], v[216:219], v[184:187], a[172:175]
s_xor_b32 m0, s82, 4096
v_mfma_f32_16x16x32_bf16 a[172:175], v[220:223], v[188:191], a[172:175]
buffer_load_dwordx4 v228, s[40:43], s79 offen lds
v_mfma_f32_16x16x32_bf16 a[168:171], v[212:215], v[188:191], a[168:171]
v_mfma_f32_16x16x32_bf16 a[168:171], v[208:211], v[184:187], a[168:171]
ds_read_b64_tr_b16 v[44:45], v241 offset:17024
ds_read_b64_tr_b16 v[46:47], v241 offset:18048
v_mfma_f32_16x16x32_bf16 a[220:223], v[200:203], v[184:187], a[220:223]
s_xor_b32 m0, s82, 5120
v_mfma_f32_16x16x32_bf16 a[220:223], v[204:207], v[188:191], a[220:223]
buffer_load_dwordx4 v229, s[40:43], s79 offen lds
v_mfma_f32_16x16x32_bf16 a[216:219], v[196:199], v[188:191], a[216:219]
v_mfma_f32_16x16x32_bf16 a[216:219], v[192:195], v[184:187], a[216:219]
v_mfma_f32_16x16x32_bf16 a[200:203], v[192:195], v[0:3], a[200:203]
s_xor_b32 m0, s82, 6144
v_mfma_f32_16x16x32_bf16 a[200:203], v[196:199], v[4:7], a[200:203]
buffer_load_dwordx4 v230, s[40:43], s79 offen lds
v_mfma_f32_16x16x32_bf16 a[204:207], v[204:207], v[4:7], a[204:207]
v_mfma_f32_16x16x32_bf16 a[204:207], v[200:203], v[0:3], a[204:207]
ds_read_b64_tr_b16 v[48:49], v242 offset:128
ds_read_b64_tr_b16 v[50:51], v242 offset:1152
v_mfma_f32_16x16x32_bf16 a[184:187], v[208:211], v[0:3], a[184:187]
s_xor_b32 m0, s82, 7168
v_mfma_f32_16x16x32_bf16 a[184:187], v[212:215], v[4:7], a[184:187]
buffer_load_dwordx4 v231, s[40:43], s79 offen lds
v_mfma_f32_16x16x32_bf16 a[188:191], v[220:223], v[4:7], a[188:191]
s_mov_b32 s79, 384
v_mfma_f32_16x16x32_bf16 a[188:191], v[216:219], v[0:3], a[188:191]
v_mfma_f32_16x16x32_bf16 a[12:15], v[216:219], v[8:11], a[12:15]
v_mfma_f32_16x16x32_bf16 a[12:15], v[220:223], v[12:15], a[12:15]
buffer_load_dword v254, v135, s[68:71], s96 offen
v_mfma_f32_16x16x32_bf16 a[8:11], v[212:215], v[12:15], a[8:11]
s_add_u32 s96, s96, 1048576
v_mfma_f32_16x16x32_bf16 a[8:11], v[208:211], v[8:11], a[8:11]
ds_read_b64_tr_b16 v[52:53], v242 offset:17024
ds_read_b64_tr_b16 v[54:55], v242 offset:18048
v_mfma_f32_16x16x32_bf16 a[76:79], v[200:203], v[8:11], a[76:79]
v_mfma_f32_16x16x32_bf16 a[76:79], v[204:207], v[12:15], a[76:79]
buffer_load_dword v254, v135, s[68:71], s96 offen
v_mfma_f32_16x16x32_bf16 a[72:75], v[196:199], v[12:15], a[72:75]
s_add_u32 s96, s96, 1048576
v_mfma_f32_16x16x32_bf16 a[72:75], v[192:195], v[8:11], a[72:75]
ds_read_b64_tr_b16 v[56:57], v243 offset:128
ds_read_b64_tr_b16 v[58:59], v243 offset:1152
v_mfma_f32_16x16x32_bf16 a[88:91], v[192:195], v[16:19], a[88:91]
v_mfma_f32_16x16x32_bf16 a[88:91], v[196:199], v[20:23], a[88:91]
v_mfma_f32_16x16x32_bf16 a[92:95], v[204:207], v[20:23], a[92:95]
v_mfma_f32_16x16x32_bf16 a[92:95], v[200:203], v[16:19], a[92:95]
ds_read_b64_tr_b16 v[60:61], v243 offset:17024
ds_read_b64_tr_b16 v[62:63], v243 offset:18048
v_mfma_f32_16x16x32_bf16 a[24:27], v[208:211], v[16:19], a[24:27]
v_mfma_f32_16x16x32_bf16 a[24:27], v[212:215], v[20:23], a[24:27]
v_mfma_f32_16x16x32_bf16 a[28:31], v[220:223], v[20:23], a[28:31]
v_mfma_f32_16x16x32_bf16 a[28:31], v[216:219], v[16:19], a[28:31]
v_mfma_f32_16x16x32_bf16 a[44:47], v[216:219], v[24:27], a[44:47]
v_mfma_f32_16x16x32_bf16 a[44:47], v[220:223], v[28:31], a[44:47]
v_mfma_f32_16x16x32_bf16 a[40:43], v[212:215], v[28:31], a[40:43]
v_mfma_f32_16x16x32_bf16 a[40:43], v[208:211], v[24:27], a[40:43]
v_mfma_f32_16x16x32_bf16 a[252:255], v[200:203], v[24:27], a[252:255]
v_mfma_f32_16x16x32_bf16 a[252:255], v[204:207], v[28:31], a[252:255]
v_mfma_f32_16x16x32_bf16 a[248:251], v[196:199], v[28:31], a[248:251]
s_waitcnt vmcnt(10) lgkmcnt(0)
s_barrier
v_mfma_f32_16x16x32_bf16 a[248:251], v[192:195], v[24:27], a[248:251]
v_mfma_f32_16x16x32_bf16 a[112:115], v[32:35], v[160:163], a[112:115]
v_mfma_f32_16x16x32_bf16 a[112:115], v[36:39], v[164:167], a[112:115]
v_mfma_f32_16x16x32_bf16 a[116:119], v[44:47], v[164:167], a[116:119]
ds_read_b64_tr_b16 v[192:193], v240 offset:33792
ds_read_b64_tr_b16 v[194:195], v240 offset:34816
v_mfma_f32_16x16x32_bf16 a[116:119], v[40:43], v[160:163], a[116:119]
ds_read_b64_tr_b16 v[196:197], v240 offset:50688
ds_read_b64_tr_b16 v[198:199], v240 offset:51712
v_mfma_f32_16x16x32_bf16 a[128:131], v[48:51], v[160:163], a[128:131]
v_mfma_f32_16x16x32_bf16 a[128:131], v[52:55], v[164:167], a[128:131]
v_mfma_f32_16x16x32_bf16 a[132:135], v[60:63], v[164:167], a[132:135]
v_mfma_f32_16x16x32_bf16 a[132:135], v[56:59], v[160:163], a[132:135]
ds_read_b128 v[160:163], v236 offset:57344
ds_read_b128 v[164:167], v237 offset:57344
v_mfma_f32_16x16x32_bf16 a[148:151], v[56:59], v[168:171], a[148:151]
v_mfma_f32_16x16x32_bf16 a[148:151], v[60:63], v[172:175], a[148:151]
v_mfma_f32_16x16x32_bf16 a[144:147], v[52:55], v[172:175], a[144:147]
v_mfma_f32_16x16x32_bf16 a[144:147], v[48:51], v[168:171], a[144:147]
ds_read_b64_tr_b16 v[200:201], v241 offset:33792
ds_read_b64_tr_b16 v[202:203], v241 offset:34816
v_mfma_f32_16x16x32_bf16 a[228:231], v[40:43], v[168:171], a[228:231]
v_mfma_f32_16x16x32_bf16 a[228:231], v[44:47], v[172:175], a[228:231]
v_mfma_f32_16x16x32_bf16 a[224:227], v[36:39], v[172:175], a[224:227]
v_mfma_f32_16x16x32_bf16 a[224:227], v[32:35], v[168:171], a[224:227]
ds_read_b128 v[168:171], v236 offset:59392
ds_read_b128 v[172:175], v237 offset:59392
v_mfma_f32_16x16x32_bf16 a[208:211], v[32:35], v[176:179], a[208:211]
v_mfma_f32_16x16x32_bf16 a[208:211], v[36:39], v[180:183], a[208:211]
v_mfma_f32_16x16x32_bf16 a[212:215], v[44:47], v[180:183], a[212:215]
v_mfma_f32_16x16x32_bf16 a[212:215], v[40:43], v[176:179], a[212:215]
ds_read_b64_tr_b16 v[204:205], v241 offset:50688
ds_read_b64_tr_b16 v[206:207], v241 offset:51712
v_mfma_f32_16x16x32_bf16 a[160:163], v[48:51], v[176:179], a[160:163]
v_mfma_f32_16x16x32_bf16 a[160:163], v[52:55], v[180:183], a[160:163]
v_mfma_f32_16x16x32_bf16 a[164:167], v[60:63], v[180:183], a[164:167]
v_mfma_f32_16x16x32_bf16 a[164:167], v[56:59], v[176:179], a[164:167]
ds_read_b128 v[176:179], v236 offset:49152
ds_read_b128 v[180:183], v237 offset:49152
v_mfma_f32_16x16x32_bf16 a[180:183], v[56:59], v[184:187], a[180:183]
v_mfma_f32_16x16x32_bf16 a[180:183], v[60:63], v[188:191], a[180:183]
v_mfma_f32_16x16x32_bf16 a[176:179], v[52:55], v[188:191], a[176:179]
v_mfma_f32_16x16x32_bf16 a[176:179], v[48:51], v[184:187], a[176:179]
ds_read_b64_tr_b16 v[208:209], v242 offset:33792
ds_read_b64_tr_b16 v[210:211], v242 offset:34816
v_mfma_f32_16x16x32_bf16 a[196:199], v[40:43], v[184:187], a[196:199]
s_add_u32 m0, s83, 0
v_mfma_f32_16x16x32_bf16 a[196:199], v[44:47], v[188:191], a[196:199]
buffer_load_dwordx4 v232, s[44:47], s80 offen lds
v_mfma_f32_16x16x32_bf16 a[192:195], v[36:39], v[188:191], a[192:195]
v_mfma_f32_16x16x32_bf16 a[192:195], v[32:35], v[184:187], a[192:195]
ds_read_b128 v[184:187], v236 offset:51200
ds_read_b128 v[188:191], v237 offset:51200
v_mfma_f32_16x16x32_bf16 a[64:67], v[32:35], v[0:3], a[64:67]
s_add_u32 m0, s83, 1024
v_mfma_f32_16x16x32_bf16 a[64:67], v[36:39], v[4:7], a[64:67]
buffer_load_dwordx4 v233, s[44:47], s80 offen lds
v_mfma_f32_16x16x32_bf16 a[68:71], v[44:47], v[4:7], a[68:71]
v_mfma_f32_16x16x32_bf16 a[68:71], v[40:43], v[0:3], a[68:71]
ds_read_b64_tr_b16 v[212:213], v242 offset:50688
ds_read_b64_tr_b16 v[214:215], v242 offset:51712
v_mfma_f32_16x16x32_bf16 a[0:3], v[48:51], v[0:3], a[0:3]
s_add_u32 m0, s83, 2176
v_mfma_f32_16x16x32_bf16 a[0:3], v[52:55], v[4:7], a[0:3]
buffer_load_dwordx4 v234, s[44:47], s80 offen lds
v_mfma_f32_16x16x32_bf16 a[4:7], v[60:63], v[4:7], a[4:7]
v_mfma_f32_16x16x32_bf16 a[4:7], v[56:59], v[0:3], a[4:7]
ds_read_b128 v[0:3], v236 offset:40960
ds_read_b128 v[4:7], v237 offset:40960
v_mfma_f32_16x16x32_bf16 a[20:23], v[56:59], v[8:11], a[20:23]
s_add_u32 m0, s83, 3200
v_mfma_f32_16x16x32_bf16 a[20:23], v[60:63], v[12:15], a[20:23]
buffer_load_dwordx4 v235, s[44:47], s80 offen lds
v_mfma_f32_16x16x32_bf16 a[16:19], v[52:55], v[12:15], a[16:19]
s_mov_b32 s80, 1310720
v_mfma_f32_16x16x32_bf16 a[16:19], v[48:51], v[8:11], a[16:19]
ds_read_b64_tr_b16 v[216:217], v243 offset:33792
ds_read_b64_tr_b16 v[218:219], v243 offset:34816
v_mfma_f32_16x16x32_bf16 a[84:87], v[40:43], v[8:11], a[84:87]
s_add_u32 m0, s83, 16896
v_mfma_f32_16x16x32_bf16 a[84:87], v[44:47], v[12:15], a[84:87]
buffer_load_dwordx4 v232, s[44:47], s80 offen lds
v_mfma_f32_16x16x32_bf16 a[80:83], v[36:39], v[12:15], a[80:83]
v_mfma_f32_16x16x32_bf16 a[80:83], v[32:35], v[8:11], a[80:83]
ds_read_b128 v[8:11], v236 offset:43008
ds_read_b128 v[12:15], v237 offset:43008
v_mfma_f32_16x16x32_bf16 a[240:243], v[32:35], v[16:19], a[240:243]
s_add_u32 m0, s83, 17920
v_mfma_f32_16x16x32_bf16 a[240:243], v[36:39], v[20:23], a[240:243]
buffer_load_dwordx4 v233, s[44:47], s80 offen lds
v_mfma_f32_16x16x32_bf16 a[244:247], v[44:47], v[20:23], a[244:247]
v_mfma_f32_16x16x32_bf16 a[244:247], v[40:43], v[16:19], a[244:247]
ds_read_b64_tr_b16 v[220:221], v243 offset:50688
ds_read_b64_tr_b16 v[222:223], v243 offset:51712
v_mfma_f32_16x16x32_bf16 a[32:35], v[48:51], v[16:19], a[32:35]
s_add_u32 m0, s83, 19072
v_mfma_f32_16x16x32_bf16 a[32:35], v[52:55], v[20:23], a[32:35]
buffer_load_dwordx4 v234, s[44:47], s80 offen lds
v_mfma_f32_16x16x32_bf16 a[36:39], v[60:63], v[20:23], a[36:39]
.La5_LW1_2:
s_cbranch_execz .La5_LW1_6
v_mfma_f32_16x16x32_bf16 a[36:39], v[56:59], v[16:19], a[36:39]
ds_read_b128 v[16:19], v236 offset:32768
ds_read_b128 v[20:23], v237 offset:32768
v_mfma_f32_16x16x32_bf16 a[60:63], v[56:59], v[24:27], a[60:63]
s_add_u32 m0, s83, 20096
v_mfma_f32_16x16x32_bf16 a[60:63], v[60:63], v[28:31], a[60:63]
buffer_load_dwordx4 v235, s[44:47], s80 offen lds
v_mfma_f32_16x16x32_bf16 a[56:59], v[52:55], v[28:31], a[56:59]
s_mov_b32 s80, 1572864
v_mfma_f32_16x16x32_bf16 a[56:59], v[48:51], v[24:27], a[56:59]
v_mfma_f32_16x16x32_bf16 a[52:55], v[40:43], v[24:27], a[52:55]
v_mfma_f32_16x16x32_bf16 a[52:55], v[44:47], v[28:31], a[52:55]
v_mfma_f32_16x16x32_bf16 a[48:51], v[36:39], v[28:31], a[48:51]
s_waitcnt lgkmcnt(2)
v_mfma_f32_16x16x32_bf16 a[48:51], v[32:35], v[24:27], a[48:51]
ds_read_b128 v[24:27], v236 offset:34816
ds_read_b128 v[28:31], v237 offset:34816
v_mfma_f32_16x16x32_bf16 a[96:99], v[192:195], v[160:163], a[96:99]
v_mfma_f32_16x16x32_bf16 a[96:99], v[196:199], v[164:167], a[96:99]
v_mfma_f32_16x16x32_bf16 a[100:103], v[204:207], v[164:167], a[100:103]
v_mfma_f32_16x16x32_bf16 a[100:103], v[200:203], v[160:163], a[100:103]
ds_read_b64_tr_b16 v[32:33], v240 offset:33920
ds_read_b64_tr_b16 v[34:35], v240 offset:34944
v_mfma_f32_16x16x32_bf16 a[104:107], v[208:211], v[160:163], a[104:107]
v_mfma_f32_16x16x32_bf16 a[104:107], v[212:215], v[164:167], a[104:107]
v_mfma_f32_16x16x32_bf16 a[108:111], v[220:223], v[164:167], a[108:111]
s_waitcnt lgkmcnt(2)
s_barrier
v_mfma_f32_16x16x32_bf16 a[108:111], v[216:219], v[160:163], a[108:111]
v_mfma_f32_16x16x32_bf16 a[140:143], v[216:219], v[168:171], a[140:143]
s_xor_b32 m0, s82, 32768
v_mfma_f32_16x16x32_bf16 a[140:143], v[220:223], v[172:175], a[140:143]
buffer_load_dwordx4 v224, s[40:43], s79 offen lds
v_mfma_f32_16x16x32_bf16 a[136:139], v[212:215], v[172:175], a[136:139]
v_mfma_f32_16x16x32_bf16 a[136:139], v[208:211], v[168:171], a[136:139]
ds_read_b64_tr_b16 v[36:37], v240 offset:50816
ds_read_b64_tr_b16 v[38:39], v240 offset:51840
v_mfma_f32_16x16x32_bf16 a[124:127], v[200:203], v[168:171], a[124:127]
s_xor_b32 m0, s82, 33792
v_mfma_f32_16x16x32_bf16 a[124:127], v[204:207], v[172:175], a[124:127]
buffer_load_dwordx4 v225, s[40:43], s79 offen lds
v_mfma_f32_16x16x32_bf16 a[120:123], v[196:199], v[172:175], a[120:123]
v_mfma_f32_16x16x32_bf16 a[120:123], v[192:195], v[168:171], a[120:123]
v_mfma_f32_16x16x32_bf16 a[232:235], v[192:195], v[176:179], a[232:235]
s_xor_b32 m0, s82, 34816
v_mfma_f32_16x16x32_bf16 a[232:235], v[196:199], v[180:183], a[232:235]
buffer_load_dwordx4 v226, s[40:43], s79 offen lds
v_mfma_f32_16x16x32_bf16 a[236:239], v[204:207], v[180:183], a[236:239]
v_mfma_f32_16x16x32_bf16 a[236:239], v[200:203], v[176:179], a[236:239]
ds_read_b64_tr_b16 v[40:41], v241 offset:33920
ds_read_b64_tr_b16 v[42:43], v241 offset:34944
v_mfma_f32_16x16x32_bf16 a[152:155], v[208:211], v[176:179], a[152:155]
s_xor_b32 m0, s82, 35840
v_mfma_f32_16x16x32_bf16 a[152:155], v[212:215], v[180:183], a[152:155]
buffer_load_dwordx4 v227, s[40:43], s79 offen lds
v_mfma_f32_16x16x32_bf16 a[156:159], v[220:223], v[180:183], a[156:159]
v_mfma_f32_16x16x32_bf16 a[156:159], v[216:219], v[176:179], a[156:159]
v_mfma_f32_16x16x32_bf16 a[172:175], v[216:219], v[184:187], a[172:175]
s_xor_b32 m0, s82, 36864
v_mfma_f32_16x16x32_bf16 a[172:175], v[220:223], v[188:191], a[172:175]
buffer_load_dwordx4 v228, s[40:43], s79 offen lds
v_mfma_f32_16x16x32_bf16 a[168:171], v[212:215], v[188:191], a[168:171]
v_mfma_f32_16x16x32_bf16 a[168:171], v[208:211], v[184:187], a[168:171]
ds_read_b64_tr_b16 v[44:45], v241 offset:50816
ds_read_b64_tr_b16 v[46:47], v241 offset:51840
v_mfma_f32_16x16x32_bf16 a[220:223], v[200:203], v[184:187], a[220:223]
s_xor_b32 m0, s82, 37888
v_mfma_f32_16x16x32_bf16 a[220:223], v[204:207], v[188:191], a[220:223]
buffer_load_dwordx4 v229, s[40:43], s79 offen lds
v_mfma_f32_16x16x32_bf16 a[216:219], v[196:199], v[188:191], a[216:219]
v_mfma_f32_16x16x32_bf16 a[216:219], v[192:195], v[184:187], a[216:219]
v_mfma_f32_16x16x32_bf16 a[200:203], v[192:195], v[0:3], a[200:203]
s_xor_b32 m0, s82, 38912
v_mfma_f32_16x16x32_bf16 a[200:203], v[196:199], v[4:7], a[200:203]
buffer_load_dwordx4 v230, s[40:43], s79 offen lds
v_mfma_f32_16x16x32_bf16 a[204:207], v[204:207], v[4:7], a[204:207]
v_mfma_f32_16x16x32_bf16 a[204:207], v[200:203], v[0:3], a[204:207]
ds_read_b64_tr_b16 v[48:49], v242 offset:33920
ds_read_b64_tr_b16 v[50:51], v242 offset:34944
v_mfma_f32_16x16x32_bf16 a[184:187], v[208:211], v[0:3], a[184:187]
s_xor_b32 m0, s82, 39936
v_mfma_f32_16x16x32_bf16 a[184:187], v[212:215], v[4:7], a[184:187]
buffer_load_dwordx4 v231, s[40:43], s79 offen lds
v_mfma_f32_16x16x32_bf16 a[188:191], v[220:223], v[4:7], a[188:191]
s_mov_b32 s79, 512
v_mfma_f32_16x16x32_bf16 a[188:191], v[216:219], v[0:3], a[188:191]
v_mfma_f32_16x16x32_bf16 a[12:15], v[216:219], v[8:11], a[12:15]
v_mfma_f32_16x16x32_bf16 a[12:15], v[220:223], v[12:15], a[12:15]
buffer_load_dword v254, v135, s[68:71], s96 offen
v_mfma_f32_16x16x32_bf16 a[8:11], v[212:215], v[12:15], a[8:11]
s_add_u32 s96, s96, 1048576
v_mfma_f32_16x16x32_bf16 a[8:11], v[208:211], v[8:11], a[8:11]
ds_read_b64_tr_b16 v[52:53], v242 offset:50816
ds_read_b64_tr_b16 v[54:55], v242 offset:51840
v_mfma_f32_16x16x32_bf16 a[76:79], v[200:203], v[8:11], a[76:79]
v_mfma_f32_16x16x32_bf16 a[76:79], v[204:207], v[12:15], a[76:79]
buffer_load_dword v254, v135, s[68:71], s96 offen
v_mfma_f32_16x16x32_bf16 a[72:75], v[196:199], v[12:15], a[72:75]
s_add_u32 s96, s96, 1048576
v_mfma_f32_16x16x32_bf16 a[72:75], v[192:195], v[8:11], a[72:75]
ds_read_b64_tr_b16 v[56:57], v243 offset:33920
ds_read_b64_tr_b16 v[58:59], v243 offset:34944
v_mfma_f32_16x16x32_bf16 a[88:91], v[192:195], v[16:19], a[88:91]
v_mfma_f32_16x16x32_bf16 a[88:91], v[196:199], v[20:23], a[88:91]
v_mfma_f32_16x16x32_bf16 a[92:95], v[204:207], v[20:23], a[92:95]
v_mfma_f32_16x16x32_bf16 a[92:95], v[200:203], v[16:19], a[92:95]
ds_read_b64_tr_b16 v[60:61], v243 offset:50816
ds_read_b64_tr_b16 v[62:63], v243 offset:51840
v_mfma_f32_16x16x32_bf16 a[24:27], v[208:211], v[16:19], a[24:27]
v_mfma_f32_16x16x32_bf16 a[24:27], v[212:215], v[20:23], a[24:27]
v_mfma_f32_16x16x32_bf16 a[28:31], v[220:223], v[20:23], a[28:31]
v_mfma_f32_16x16x32_bf16 a[28:31], v[216:219], v[16:19], a[28:31]
v_mfma_f32_16x16x32_bf16 a[44:47], v[216:219], v[24:27], a[44:47]
v_mfma_f32_16x16x32_bf16 a[44:47], v[220:223], v[28:31], a[44:47]
v_mfma_f32_16x16x32_bf16 a[40:43], v[212:215], v[28:31], a[40:43]
v_mfma_f32_16x16x32_bf16 a[40:43], v[208:211], v[24:27], a[40:43]
v_mfma_f32_16x16x32_bf16 a[252:255], v[200:203], v[24:27], a[252:255]
v_mfma_f32_16x16x32_bf16 a[252:255], v[204:207], v[28:31], a[252:255]
v_mfma_f32_16x16x32_bf16 a[248:251], v[196:199], v[28:31], a[248:251]
s_waitcnt vmcnt(10) lgkmcnt(0)
s_barrier
v_mfma_f32_16x16x32_bf16 a[248:251], v[192:195], v[24:27], a[248:251]
v_mfma_f32_16x16x32_bf16 a[112:115], v[32:35], v[160:163], a[112:115]
v_mfma_f32_16x16x32_bf16 a[112:115], v[36:39], v[164:167], a[112:115]
v_mfma_f32_16x16x32_bf16 a[116:119], v[44:47], v[164:167], a[116:119]
ds_read_b64_tr_b16 v[192:193], v240 offset:0
ds_read_b64_tr_b16 v[194:195], v240 offset:1024
v_mfma_f32_16x16x32_bf16 a[116:119], v[40:43], v[160:163], a[116:119]
ds_read_b64_tr_b16 v[196:197], v240 offset:16896
ds_read_b64_tr_b16 v[198:199], v240 offset:17920
v_mfma_f32_16x16x32_bf16 a[128:131], v[48:51], v[160:163], a[128:131]
v_mfma_f32_16x16x32_bf16 a[128:131], v[52:55], v[164:167], a[128:131]
v_mfma_f32_16x16x32_bf16 a[132:135], v[60:63], v[164:167], a[132:135]
v_mfma_f32_16x16x32_bf16 a[132:135], v[56:59], v[160:163], a[132:135]
ds_read_b128 v[160:163], v236 offset:24576
ds_read_b128 v[164:167], v237 offset:24576
v_mfma_f32_16x16x32_bf16 a[148:151], v[56:59], v[168:171], a[148:151]
v_mfma_f32_16x16x32_bf16 a[148:151], v[60:63], v[172:175], a[148:151]
v_mfma_f32_16x16x32_bf16 a[144:147], v[52:55], v[172:175], a[144:147]
v_mfma_f32_16x16x32_bf16 a[144:147], v[48:51], v[168:171], a[144:147]
ds_read_b64_tr_b16 v[200:201], v241 offset:0
ds_read_b64_tr_b16 v[202:203], v241 offset:1024
v_mfma_f32_16x16x32_bf16 a[228:231], v[40:43], v[168:171], a[228:231]
v_mfma_f32_16x16x32_bf16 a[228:231], v[44:47], v[172:175], a[228:231]
v_mfma_f32_16x16x32_bf16 a[224:227], v[36:39], v[172:175], a[224:227]
v_mfma_f32_16x16x32_bf16 a[224:227], v[32:35], v[168:171], a[224:227]
ds_read_b128 v[168:171], v236 offset:26624
ds_read_b128 v[172:175], v237 offset:26624
v_mfma_f32_16x16x32_bf16 a[208:211], v[32:35], v[176:179], a[208:211]
v_mfma_f32_16x16x32_bf16 a[208:211], v[36:39], v[180:183], a[208:211]
v_mfma_f32_16x16x32_bf16 a[212:215], v[44:47], v[180:183], a[212:215]
v_mfma_f32_16x16x32_bf16 a[212:215], v[40:43], v[176:179], a[212:215]
ds_read_b64_tr_b16 v[204:205], v241 offset:16896
ds_read_b64_tr_b16 v[206:207], v241 offset:17920
v_mfma_f32_16x16x32_bf16 a[160:163], v[48:51], v[176:179], a[160:163]
v_mfma_f32_16x16x32_bf16 a[160:163], v[52:55], v[180:183], a[160:163]
v_mfma_f32_16x16x32_bf16 a[164:167], v[60:63], v[180:183], a[164:167]
v_mfma_f32_16x16x32_bf16 a[164:167], v[56:59], v[176:179], a[164:167]
ds_read_b128 v[176:179], v236 offset:16384
ds_read_b128 v[180:183], v237 offset:16384
v_mfma_f32_16x16x32_bf16 a[180:183], v[56:59], v[184:187], a[180:183]
v_mfma_f32_16x16x32_bf16 a[180:183], v[60:63], v[188:191], a[180:183]
v_mfma_f32_16x16x32_bf16 a[176:179], v[52:55], v[188:191], a[176:179]
v_mfma_f32_16x16x32_bf16 a[176:179], v[48:51], v[184:187], a[176:179]
ds_read_b64_tr_b16 v[208:209], v242 offset:0
ds_read_b64_tr_b16 v[210:211], v242 offset:1024
v_mfma_f32_16x16x32_bf16 a[196:199], v[40:43], v[184:187], a[196:199]
s_add_u32 m0, s83, 33792
v_mfma_f32_16x16x32_bf16 a[196:199], v[44:47], v[188:191], a[196:199]
buffer_load_dwordx4 v232, s[44:47], s80 offen lds
v_mfma_f32_16x16x32_bf16 a[192:195], v[36:39], v[188:191], a[192:195]
v_mfma_f32_16x16x32_bf16 a[192:195], v[32:35], v[184:187], a[192:195]
ds_read_b128 v[184:187], v236 offset:18432
ds_read_b128 v[188:191], v237 offset:18432
v_mfma_f32_16x16x32_bf16 a[64:67], v[32:35], v[0:3], a[64:67]
s_add_u32 m0, s83, 34816
v_mfma_f32_16x16x32_bf16 a[64:67], v[36:39], v[4:7], a[64:67]
buffer_load_dwordx4 v233, s[44:47], s80 offen lds
v_mfma_f32_16x16x32_bf16 a[68:71], v[44:47], v[4:7], a[68:71]
v_mfma_f32_16x16x32_bf16 a[68:71], v[40:43], v[0:3], a[68:71]
ds_read_b64_tr_b16 v[212:213], v242 offset:16896
ds_read_b64_tr_b16 v[214:215], v242 offset:17920
v_mfma_f32_16x16x32_bf16 a[0:3], v[48:51], v[0:3], a[0:3]
s_add_u32 m0, s83, 35968
v_mfma_f32_16x16x32_bf16 a[0:3], v[52:55], v[4:7], a[0:3]
buffer_load_dwordx4 v234, s[44:47], s80 offen lds
v_mfma_f32_16x16x32_bf16 a[4:7], v[60:63], v[4:7], a[4:7]
v_mfma_f32_16x16x32_bf16 a[4:7], v[56:59], v[0:3], a[4:7]
ds_read_b128 v[0:3], v236 offset:8192
ds_read_b128 v[4:7], v237 offset:8192
v_mfma_f32_16x16x32_bf16 a[20:23], v[56:59], v[8:11], a[20:23]
s_add_u32 m0, s83, 36992
v_mfma_f32_16x16x32_bf16 a[20:23], v[60:63], v[12:15], a[20:23]
buffer_load_dwordx4 v235, s[44:47], s80 offen lds
v_mfma_f32_16x16x32_bf16 a[16:19], v[52:55], v[12:15], a[16:19]
s_mov_b32 s80, 1835008
v_mfma_f32_16x16x32_bf16 a[16:19], v[48:51], v[8:11], a[16:19]
ds_read_b64_tr_b16 v[216:217], v243 offset:0
ds_read_b64_tr_b16 v[218:219], v243 offset:1024
v_mfma_f32_16x16x32_bf16 a[84:87], v[40:43], v[8:11], a[84:87]
s_add_u32 m0, s83, 50688
v_mfma_f32_16x16x32_bf16 a[84:87], v[44:47], v[12:15], a[84:87]
buffer_load_dwordx4 v232, s[44:47], s80 offen lds
v_mfma_f32_16x16x32_bf16 a[80:83], v[36:39], v[12:15], a[80:83]
v_mfma_f32_16x16x32_bf16 a[80:83], v[32:35], v[8:11], a[80:83]
ds_read_b128 v[8:11], v236 offset:10240
ds_read_b128 v[12:15], v237 offset:10240
v_mfma_f32_16x16x32_bf16 a[240:243], v[32:35], v[16:19], a[240:243]
s_add_u32 m0, s83, 51712
v_mfma_f32_16x16x32_bf16 a[240:243], v[36:39], v[20:23], a[240:243]
buffer_load_dwordx4 v233, s[44:47], s80 offen lds
v_mfma_f32_16x16x32_bf16 a[244:247], v[44:47], v[20:23], a[244:247]
v_mfma_f32_16x16x32_bf16 a[244:247], v[40:43], v[16:19], a[244:247]
ds_read_b64_tr_b16 v[220:221], v243 offset:16896
ds_read_b64_tr_b16 v[222:223], v243 offset:17920
v_mfma_f32_16x16x32_bf16 a[32:35], v[48:51], v[16:19], a[32:35]
s_add_u32 m0, s83, 52864
v_mfma_f32_16x16x32_bf16 a[32:35], v[52:55], v[20:23], a[32:35]
buffer_load_dwordx4 v234, s[44:47], s80 offen lds
v_mfma_f32_16x16x32_bf16 a[36:39], v[60:63], v[20:23], a[36:39]
v_mfma_f32_16x16x32_bf16 a[36:39], v[56:59], v[16:19], a[36:39]
ds_read_b128 v[16:19], v236 offset:0
ds_read_b128 v[20:23], v237 offset:0
v_mfma_f32_16x16x32_bf16 a[60:63], v[56:59], v[24:27], a[60:63]
s_add_u32 m0, s83, 53888
v_mfma_f32_16x16x32_bf16 a[60:63], v[60:63], v[28:31], a[60:63]
buffer_load_dwordx4 v235, s[44:47], s80 offen lds
v_mfma_f32_16x16x32_bf16 a[56:59], v[52:55], v[28:31], a[56:59]
s_mov_b64 s[44:45], s[68:69]
s_mov_b32 s46, s70
v_mfma_f32_16x16x32_bf16 a[56:59], v[48:51], v[24:27], a[56:59]
v_mfma_f32_16x16x32_bf16 a[52:55], v[40:43], v[24:27], a[52:55]
v_mfma_f32_16x16x32_bf16 a[52:55], v[44:47], v[28:31], a[52:55]
v_mfma_f32_16x16x32_bf16 a[48:51], v[36:39], v[28:31], a[48:51]
s_waitcnt lgkmcnt(2)
s_lshl_b32 s80, s77, 18
v_mfma_f32_16x16x32_bf16 a[48:51], v[32:35], v[24:27], a[48:51]
ds_read_b128 v[24:27], v236 offset:2048
ds_read_b128 v[28:31], v237 offset:2048
v_mfma_f32_16x16x32_bf16 a[96:99], v[192:195], v[160:163], a[96:99]
v_mfma_f32_16x16x32_bf16 a[96:99], v[196:199], v[164:167], a[96:99]
v_mfma_f32_16x16x32_bf16 a[100:103], v[204:207], v[164:167], a[100:103]
v_mfma_f32_16x16x32_bf16 a[100:103], v[200:203], v[160:163], a[100:103]
ds_read_b64_tr_b16 v[32:33], v240 offset:128
ds_read_b64_tr_b16 v[34:35], v240 offset:1152
v_mfma_f32_16x16x32_bf16 a[104:107], v[208:211], v[160:163], a[104:107]
v_mfma_f32_16x16x32_bf16 a[104:107], v[212:215], v[164:167], a[104:107]
v_mfma_f32_16x16x32_bf16 a[108:111], v[220:223], v[164:167], a[108:111]
s_waitcnt lgkmcnt(2)
s_barrier
v_mfma_f32_16x16x32_bf16 a[108:111], v[216:219], v[160:163], a[108:111]
v_mfma_f32_16x16x32_bf16 a[140:143], v[216:219], v[168:171], a[140:143]
v_mfma_f32_16x16x32_bf16 a[140:143], v[220:223], v[172:175], a[140:143]
v_mfma_f32_16x16x32_bf16 a[136:139], v[212:215], v[172:175], a[136:139]
v_mfma_f32_16x16x32_bf16 a[136:139], v[208:211], v[168:171], a[136:139]
ds_read_b64_tr_b16 v[36:37], v240 offset:17024
ds_read_b64_tr_b16 v[38:39], v240 offset:18048
v_mfma_f32_16x16x32_bf16 a[124:127], v[200:203], v[168:171], a[124:127]
v_mfma_f32_16x16x32_bf16 a[124:127], v[204:207], v[172:175], a[124:127]
v_mfma_f32_16x16x32_bf16 a[120:123], v[196:199], v[172:175], a[120:123]
v_mfma_f32_16x16x32_bf16 a[120:123], v[192:195], v[168:171], a[120:123]
v_mfma_f32_16x16x32_bf16 a[232:235], v[192:195], v[176:179], a[232:235]
v_mfma_f32_16x16x32_bf16 a[232:235], v[196:199], v[180:183], a[232:235]
v_mfma_f32_16x16x32_bf16 a[236:239], v[204:207], v[180:183], a[236:239]
v_mfma_f32_16x16x32_bf16 a[236:239], v[200:203], v[176:179], a[236:239]
ds_read_b64_tr_b16 v[40:41], v241 offset:128
ds_read_b64_tr_b16 v[42:43], v241 offset:1152
v_mfma_f32_16x16x32_bf16 a[152:155], v[208:211], v[176:179], a[152:155]
v_mfma_f32_16x16x32_bf16 a[152:155], v[212:215], v[180:183], a[152:155]
v_mfma_f32_16x16x32_bf16 a[156:159], v[220:223], v[180:183], a[156:159]
v_mfma_f32_16x16x32_bf16 a[156:159], v[216:219], v[176:179], a[156:159]
v_mfma_f32_16x16x32_bf16 a[172:175], v[216:219], v[184:187], a[172:175]
v_mfma_f32_16x16x32_bf16 a[172:175], v[220:223], v[188:191], a[172:175]
v_mfma_f32_16x16x32_bf16 a[168:171], v[212:215], v[188:191], a[168:171]
v_mfma_f32_16x16x32_bf16 a[168:171], v[208:211], v[184:187], a[168:171]
ds_read_b64_tr_b16 v[44:45], v241 offset:17024
ds_read_b64_tr_b16 v[46:47], v241 offset:18048
v_mfma_f32_16x16x32_bf16 a[220:223], v[200:203], v[184:187], a[220:223]
v_mfma_f32_16x16x32_bf16 a[220:223], v[204:207], v[188:191], a[220:223]
v_mfma_f32_16x16x32_bf16 a[216:219], v[196:199], v[188:191], a[216:219]
v_mfma_f32_16x16x32_bf16 a[216:219], v[192:195], v[184:187], a[216:219]
v_mfma_f32_16x16x32_bf16 a[200:203], v[192:195], v[0:3], a[200:203]
v_mfma_f32_16x16x32_bf16 a[200:203], v[196:199], v[4:7], a[200:203]
v_mfma_f32_16x16x32_bf16 a[204:207], v[204:207], v[4:7], a[204:207]
v_mfma_f32_16x16x32_bf16 a[204:207], v[200:203], v[0:3], a[204:207]
ds_read_b64_tr_b16 v[48:49], v242 offset:128
ds_read_b64_tr_b16 v[50:51], v242 offset:1152
v_mfma_f32_16x16x32_bf16 a[184:187], v[208:211], v[0:3], a[184:187]
v_mfma_f32_16x16x32_bf16 a[184:187], v[212:215], v[4:7], a[184:187]
v_mfma_f32_16x16x32_bf16 a[188:191], v[220:223], v[4:7], a[188:191]
v_mfma_f32_16x16x32_bf16 a[188:191], v[216:219], v[0:3], a[188:191]
v_mfma_f32_16x16x32_bf16 a[12:15], v[216:219], v[8:11], a[12:15]
v_mfma_f32_16x16x32_bf16 a[12:15], v[220:223], v[12:15], a[12:15]
buffer_load_dword v254, v135, s[68:71], s96 offen
v_mfma_f32_16x16x32_bf16 a[8:11], v[212:215], v[12:15], a[8:11]
s_add_u32 s96, s96, 1048576
v_mfma_f32_16x16x32_bf16 a[8:11], v[208:211], v[8:11], a[8:11]
ds_read_b64_tr_b16 v[52:53], v242 offset:17024
ds_read_b64_tr_b16 v[54:55], v242 offset:18048
v_mfma_f32_16x16x32_bf16 a[76:79], v[200:203], v[8:11], a[76:79]
v_mfma_f32_16x16x32_bf16 a[76:79], v[204:207], v[12:15], a[76:79]
buffer_load_dword v254, v135, s[68:71], s96 offen
v_mfma_f32_16x16x32_bf16 a[72:75], v[196:199], v[12:15], a[72:75]
s_add_u32 s96, s96, 1048576
v_mfma_f32_16x16x32_bf16 a[72:75], v[192:195], v[8:11], a[72:75]
ds_read_b64_tr_b16 v[56:57], v243 offset:128
ds_read_b64_tr_b16 v[58:59], v243 offset:1152
v_mfma_f32_16x16x32_bf16 a[88:91], v[192:195], v[16:19], a[88:91]
v_mfma_f32_16x16x32_bf16 a[88:91], v[196:199], v[20:23], a[88:91]
v_mfma_f32_16x16x32_bf16 a[92:95], v[204:207], v[20:23], a[92:95]
v_mfma_f32_16x16x32_bf16 a[92:95], v[200:203], v[16:19], a[92:95]
ds_read_b64_tr_b16 v[60:61], v243 offset:17024
ds_read_b64_tr_b16 v[62:63], v243 offset:18048
v_mfma_f32_16x16x32_bf16 a[24:27], v[208:211], v[16:19], a[24:27]
v_mfma_f32_16x16x32_bf16 a[24:27], v[212:215], v[20:23], a[24:27]
v_mfma_f32_16x16x32_bf16 a[28:31], v[220:223], v[20:23], a[28:31]
v_mfma_f32_16x16x32_bf16 a[28:31], v[216:219], v[16:19], a[28:31]
v_mfma_f32_16x16x32_bf16 a[44:47], v[216:219], v[24:27], a[44:47]
v_mfma_f32_16x16x32_bf16 a[44:47], v[220:223], v[28:31], a[44:47]
buffer_load_dwordx4 v[72:75], v255, s[64:67], s89 offen offset:1024 sc1
v_mfma_f32_16x16x32_bf16 a[40:43], v[212:215], v[28:31], a[40:43]
v_mfma_f32_16x16x32_bf16 a[40:43], v[208:211], v[24:27], a[40:43]
v_mfma_f32_16x16x32_bf16 a[252:255], v[200:203], v[24:27], a[252:255]
v_mfma_f32_16x16x32_bf16 a[252:255], v[204:207], v[28:31], a[252:255]
v_mfma_f32_16x16x32_bf16 a[248:251], v[196:199], v[28:31], a[248:251]
s_waitcnt vmcnt(3) lgkmcnt(0)
s_barrier
v_mfma_f32_16x16x32_bf16 a[248:251], v[192:195], v[24:27], a[248:251]
v_mfma_f32_16x16x32_bf16 a[112:115], v[32:35], v[160:163], a[112:115]
v_mfma_f32_16x16x32_bf16 a[112:115], v[36:39], v[164:167], a[112:115]
v_mfma_f32_16x16x32_bf16 a[116:119], v[44:47], v[164:167], a[116:119]
ds_read_b64_tr_b16 v[192:193], v240 offset:33792
ds_read_b64_tr_b16 v[194:195], v240 offset:34816
v_mfma_f32_16x16x32_bf16 a[116:119], v[40:43], v[160:163], a[116:119]
ds_read_b64_tr_b16 v[196:197], v240 offset:50688
ds_read_b64_tr_b16 v[198:199], v240 offset:51712
v_mfma_f32_16x16x32_bf16 a[128:131], v[48:51], v[160:163], a[128:131]
v_mfma_f32_16x16x32_bf16 a[128:131], v[52:55], v[164:167], a[128:131]
v_mfma_f32_16x16x32_bf16 a[132:135], v[60:63], v[164:167], a[132:135]
v_mfma_f32_16x16x32_bf16 a[132:135], v[56:59], v[160:163], a[132:135]
ds_read_b128 v[160:163], v236 offset:57344
ds_read_b128 v[164:167], v237 offset:57344
v_mfma_f32_16x16x32_bf16 a[148:151], v[56:59], v[168:171], a[148:151]
v_mfma_f32_16x16x32_bf16 a[148:151], v[60:63], v[172:175], a[148:151]
v_mfma_f32_16x16x32_bf16 a[144:147], v[52:55], v[172:175], a[144:147]
v_mfma_f32_16x16x32_bf16 a[144:147], v[48:51], v[168:171], a[144:147]
ds_read_b64_tr_b16 v[200:201], v241 offset:33792
ds_read_b64_tr_b16 v[202:203], v241 offset:34816
v_mfma_f32_16x16x32_bf16 a[228:231], v[40:43], v[168:171], a[228:231]
v_mfma_f32_16x16x32_bf16 a[228:231], v[44:47], v[172:175], a[228:231]
v_mfma_f32_16x16x32_bf16 a[224:227], v[36:39], v[172:175], a[224:227]
v_mfma_f32_16x16x32_bf16 a[224:227], v[32:35], v[168:171], a[224:227]
ds_read_b128 v[168:171], v236 offset:59392
ds_read_b128 v[172:175], v237 offset:59392
v_mfma_f32_16x16x32_bf16 a[208:211], v[32:35], v[176:179], a[208:211]
v_mfma_f32_16x16x32_bf16 a[208:211], v[36:39], v[180:183], a[208:211]
v_mfma_f32_16x16x32_bf16 a[212:215], v[44:47], v[180:183], a[212:215]
v_mfma_f32_16x16x32_bf16 a[212:215], v[40:43], v[176:179], a[212:215]
ds_read_b64_tr_b16 v[204:205], v241 offset:50688
ds_read_b64_tr_b16 v[206:207], v241 offset:51712
v_mfma_f32_16x16x32_bf16 a[160:163], v[48:51], v[176:179], a[160:163]
v_mfma_f32_16x16x32_bf16 a[160:163], v[52:55], v[180:183], a[160:163]
v_mfma_f32_16x16x32_bf16 a[164:167], v[60:63], v[180:183], a[164:167]
v_mfma_f32_16x16x32_bf16 a[164:167], v[56:59], v[176:179], a[164:167]
ds_read_b128 v[176:179], v236 offset:49152
ds_read_b128 v[180:183], v237 offset:49152
v_mfma_f32_16x16x32_bf16 a[180:183], v[56:59], v[184:187], a[180:183]
v_mfma_f32_16x16x32_bf16 a[180:183], v[60:63], v[188:191], a[180:183]
v_mfma_f32_16x16x32_bf16 a[176:179], v[52:55], v[188:191], a[176:179]
v_mfma_f32_16x16x32_bf16 a[176:179], v[48:51], v[184:187], a[176:179]
ds_read_b64_tr_b16 v[208:209], v242 offset:33792
ds_read_b64_tr_b16 v[210:211], v242 offset:34816
v_mfma_f32_16x16x32_bf16 a[196:199], v[40:43], v[184:187], a[196:199]
s_add_u32 m0, s83, 0
v_mfma_f32_16x16x32_bf16 a[196:199], v[44:47], v[188:191], a[196:199]
buffer_load_dwordx4 v232, s[44:47], s80 offen lds
v_mfma_f32_16x16x32_bf16 a[192:195], v[36:39], v[188:191], a[192:195]
v_mfma_f32_16x16x32_bf16 a[192:195], v[32:35], v[184:187], a[192:195]
ds_read_b128 v[184:187], v236 offset:51200
ds_read_b128 v[188:191], v237 offset:51200
v_mfma_f32_16x16x32_bf16 a[64:67], v[32:35], v[0:3], a[64:67]
s_add_u32 m0, s83, 1024
v_mfma_f32_16x16x32_bf16 a[64:67], v[36:39], v[4:7], a[64:67]
buffer_load_dwordx4 v233, s[44:47], s80 offen lds
v_mfma_f32_16x16x32_bf16 a[68:71], v[44:47], v[4:7], a[68:71]
v_mfma_f32_16x16x32_bf16 a[68:71], v[40:43], v[0:3], a[68:71]
ds_read_b64_tr_b16 v[212:213], v242 offset:50688
ds_read_b64_tr_b16 v[214:215], v242 offset:51712
v_mfma_f32_16x16x32_bf16 a[0:3], v[48:51], v[0:3], a[0:3]
s_add_u32 m0, s83, 2176
v_mfma_f32_16x16x32_bf16 a[0:3], v[52:55], v[4:7], a[0:3]
buffer_load_dwordx4 v234, s[44:47], s80 offen lds
v_mfma_f32_16x16x32_bf16 a[4:7], v[60:63], v[4:7], a[4:7]
v_mfma_f32_16x16x32_bf16 a[4:7], v[56:59], v[0:3], a[4:7]
ds_read_b128 v[0:3], v236 offset:40960
ds_read_b128 v[4:7], v237 offset:40960
v_mfma_f32_16x16x32_bf16 a[20:23], v[56:59], v[8:11], a[20:23]
s_add_u32 m0, s83, 3200
v_mfma_f32_16x16x32_bf16 a[20:23], v[60:63], v[12:15], a[20:23]
buffer_load_dwordx4 v235, s[44:47], s80 offen lds
v_mfma_f32_16x16x32_bf16 a[16:19], v[52:55], v[12:15], a[16:19]
s_add_u32 s80, s80, 524288
v_mfma_f32_16x16x32_bf16 a[16:19], v[48:51], v[8:11], a[16:19]
ds_read_b64_tr_b16 v[216:217], v243 offset:33792
ds_read_b64_tr_b16 v[218:219], v243 offset:34816
v_mfma_f32_16x16x32_bf16 a[84:87], v[40:43], v[8:11], a[84:87]
s_add_u32 m0, s83, 16896
v_mfma_f32_16x16x32_bf16 a[84:87], v[44:47], v[12:15], a[84:87]
buffer_load_dwordx4 v232, s[44:47], s80 offen lds
v_mfma_f32_16x16x32_bf16 a[80:83], v[36:39], v[12:15], a[80:83]
v_mfma_f32_16x16x32_bf16 a[80:83], v[32:35], v[8:11], a[80:83]
ds_read_b128 v[8:11], v236 offset:43008
ds_read_b128 v[12:15], v237 offset:43008
v_mfma_f32_16x16x32_bf16 a[240:243], v[32:35], v[16:19], a[240:243]
s_add_u32 m0, s83, 17920
v_mfma_f32_16x16x32_bf16 a[240:243], v[36:39], v[20:23], a[240:243]
buffer_load_dwordx4 v233, s[44:47], s80 offen lds
v_mfma_f32_16x16x32_bf16 a[244:247], v[44:47], v[20:23], a[244:247]
v_mfma_f32_16x16x32_bf16 a[244:247], v[40:43], v[16:19], a[244:247]
ds_read_b64_tr_b16 v[220:221], v243 offset:50688
ds_read_b64_tr_b16 v[222:223], v243 offset:51712
v_mfma_f32_16x16x32_bf16 a[32:35], v[48:51], v[16:19], a[32:35]
s_add_u32 m0, s83, 19072
v_mfma_f32_16x16x32_bf16 a[32:35], v[52:55], v[20:23], a[32:35]
buffer_load_dwordx4 v234, s[44:47], s80 offen lds
v_mfma_f32_16x16x32_bf16 a[36:39], v[60:63], v[20:23], a[36:39]
v_mfma_f32_16x16x32_bf16 a[36:39], v[56:59], v[16:19], a[36:39]
ds_read_b128 v[16:19], v236 offset:32768
ds_read_b128 v[20:23], v237 offset:32768
v_mfma_f32_16x16x32_bf16 a[60:63], v[56:59], v[24:27], a[60:63]
s_add_u32 m0, s83, 20096
v_mfma_f32_16x16x32_bf16 a[60:63], v[60:63], v[28:31], a[60:63]
buffer_load_dwordx4 v235, s[44:47], s80 offen lds
v_mfma_f32_16x16x32_bf16 a[56:59], v[52:55], v[28:31], a[56:59]
s_add_u32 s80, s80, 524288
v_mfma_f32_16x16x32_bf16 a[56:59], v[48:51], v[24:27], a[56:59]
v_mfma_f32_16x16x32_bf16 a[52:55], v[40:43], v[24:27], a[52:55]
v_mfma_f32_16x16x32_bf16 a[52:55], v[44:47], v[28:31], a[52:55]
v_mfma_f32_16x16x32_bf16 a[48:51], v[36:39], v[28:31], a[48:51]
s_waitcnt lgkmcnt(2)
v_mfma_f32_16x16x32_bf16 a[48:51], v[32:35], v[24:27], a[48:51]
ds_read_b128 v[24:27], v236 offset:34816
ds_read_b128 v[28:31], v237 offset:34816
v_mfma_f32_16x16x32_bf16 a[96:99], v[192:195], v[160:163], a[96:99]
v_mfma_f32_16x16x32_bf16 a[96:99], v[196:199], v[164:167], a[96:99]
v_mfma_f32_16x16x32_bf16 a[100:103], v[204:207], v[164:167], a[100:103]
v_mfma_f32_16x16x32_bf16 a[100:103], v[200:203], v[160:163], a[100:103]
ds_read_b64_tr_b16 v[32:33], v240 offset:33920
ds_read_b64_tr_b16 v[34:35], v240 offset:34944
v_mfma_f32_16x16x32_bf16 a[104:107], v[208:211], v[160:163], a[104:107]
v_mfma_f32_16x16x32_bf16 a[104:107], v[212:215], v[164:167], a[104:107]
v_mfma_f32_16x16x32_bf16 a[108:111], v[220:223], v[164:167], a[108:111]
s_waitcnt lgkmcnt(2)
s_barrier
v_mfma_f32_16x16x32_bf16 a[108:111], v[216:219], v[160:163], a[108:111]
v_mfma_f32_16x16x32_bf16 a[140:143], v[216:219], v[168:171], a[140:143]
s_waitcnt vmcnt(8)
v_cmp_eq_u64 vcc, v[72:73], v[252:253]
v_mfma_f32_16x16x32_bf16 a[140:143], v[220:223], v[172:175], a[140:143]
v_cmp_eq_u64 s[96:97], v[74:75], v[252:253]
v_mfma_f32_16x16x32_bf16 a[136:139], v[212:215], v[172:175], a[136:139]
s_and_b64 vcc, vcc, s[96:97]
s_cmp_eq_u64 vcc, -1
s_cbranch_scc0 .La5_LK_XSPIN
.La5_LK_XOK:
v_mfma_f32_16x16x32_bf16 a[136:139], v[208:211], v[168:171], a[136:139]
ds_read_b64_tr_b16 v[36:37], v240 offset:50816
ds_read_b64_tr_b16 v[38:39], v240 offset:51840
v_mfma_f32_16x16x32_bf16 a[124:127], v[200:203], v[168:171], a[124:127]
s_mov_b32 s89, 49152
v_mfma_f32_16x16x32_bf16 a[124:127], v[204:207], v[172:175], a[124:127]
v_mfma_f32_16x16x32_bf16 a[120:123], v[196:199], v[172:175], a[120:123]
v_accvgpr_read_b32 v224, a96
v_accvgpr_read_b32 v225, a97
v_mfma_f32_16x16x32_bf16 a[120:123], v[192:195], v[168:171], a[120:123]
v_accvgpr_read_b32 v226, a98
v_accvgpr_read_b32 v227, a99
v_mfma_f32_16x16x32_bf16 a[232:235], v[192:195], v[176:179], a[232:235]
v_accvgpr_read_b32 v228, a100
v_accvgpr_read_b32 v229, a101
v_mfma_f32_16x16x32_bf16 a[232:235], v[196:199], v[180:183], a[232:235]
buffer_load_dwordx4 v[128:131], v244, s[52:55], s91 offen offset:0 sc1
v_mfma_f32_16x16x32_bf16 a[236:239], v[204:207], v[180:183], a[236:239]
v_accvgpr_read_b32 v230, a102
v_accvgpr_read_b32 v231, a103
v_mfma_f32_16x16x32_bf16 a[236:239], v[200:203], v[176:179], a[236:239]
ds_read_b64_tr_b16 v[40:41], v241 offset:33920
ds_read_b64_tr_b16 v[42:43], v241 offset:34944
v_mfma_f32_16x16x32_bf16 a[152:155], v[208:211], v[176:179], a[152:155]
v_cvt_pk_bf16_f32 v248, v224, v225
v_cvt_pk_bf16_f32 v249, v226, v227
v_mfma_f32_16x16x32_bf16 a[152:155], v[212:215], v[180:183], a[152:155]
buffer_load_dwordx4 v[132:135], v245, s[52:55], s91 offen offset:0 sc1
v_mfma_f32_16x16x32_bf16 a[156:159], v[220:223], v[180:183], a[156:159]
v_cvt_pk_bf16_f32 v250, v228, v229
v_cvt_pk_bf16_f32 v251, v230, v231
v_mfma_f32_16x16x32_bf16 a[156:159], v[216:219], v[176:179], a[156:159]
buffer_store_dwordx4 v[248:251], v255, s[48:51], s89 offen sc1
v_mfma_f32_16x16x32_bf16 a[172:175], v[216:219], v[184:187], a[172:175]
s_mov_b32 s89, 50176
ds_write_b128 v238, v[248:251] offset:0
v_mfma_f32_16x16x32_bf16 a[172:175], v[220:223], v[188:191], a[172:175]
buffer_load_dwordx4 v[136:139], v246, s[52:55], s91 offen offset:0 sc1
v_mfma_f32_16x16x32_bf16 a[168:171], v[212:215], v[188:191], a[168:171]
v_accvgpr_read_b32 v224, a104
v_accvgpr_read_b32 v225, a105
v_mfma_f32_16x16x32_bf16 a[168:171], v[208:211], v[184:187], a[168:171]
ds_read_b64_tr_b16 v[44:45], v241 offset:50816
ds_read_b64_tr_b16 v[46:47], v241 offset:51840
v_mfma_f32_16x16x32_bf16 a[220:223], v[200:203], v[184:187], a[220:223]
v_accvgpr_read_b32 v226, a106
v_accvgpr_read_b32 v227, a107
v_mfma_f32_16x16x32_bf16 a[220:223], v[204:207], v[188:191], a[220:223]
buffer_load_dwordx4 v[140:143], v247, s[52:55], s91 offen offset:0 sc1
v_mfma_f32_16x16x32_bf16 a[216:219], v[196:199], v[188:191], a[216:219]
v_accvgpr_read_b32 v228, a108
v_accvgpr_read_b32 v229, a109
v_mfma_f32_16x16x32_bf16 a[216:219], v[192:195], v[184:187], a[216:219]
v_accvgpr_read_b32 v230, a110
v_accvgpr_read_b32 v231, a111
v_mfma_f32_16x16x32_bf16 a[200:203], v[192:195], v[0:3], a[200:203]
v_cvt_pk_bf16_f32 v248, v224, v225
v_cvt_pk_bf16_f32 v249, v226, v227
v_mfma_f32_16x16x32_bf16 a[200:203], v[196:199], v[4:7], a[200:203]
.La5_LW1_3:
s_cbranch_execz .La5_LW1_7
buffer_load_dwordx4 v[144:147], v244, s[52:55], s91 offen offset:1024 sc1
v_mfma_f32_16x16x32_bf16 a[204:207], v[204:207], v[4:7], a[204:207]
v_cvt_pk_bf16_f32 v250, v228, v229
v_cvt_pk_bf16_f32 v251, v230, v231
v_mfma_f32_16x16x32_bf16 a[204:207], v[200:203], v[0:3], a[204:207]
ds_read_b64_tr_b16 v[48:49], v242 offset:33920
ds_read_b64_tr_b16 v[50:51], v242 offset:34944
v_mfma_f32_16x16x32_bf16 a[184:187], v[208:211], v[0:3], a[184:187]
buffer_store_dwordx4 v[248:251], v255, s[48:51], s89 offen sc1
v_mfma_f32_16x16x32_bf16 a[184:187], v[212:215], v[4:7], a[184:187]
buffer_load_dwordx4 v[148:151], v245, s[52:55], s91 offen offset:1024 sc1
v_mfma_f32_16x16x32_bf16 a[188:191], v[220:223], v[4:7], a[188:191]
s_mov_b32 s89, 51200
ds_write_b128 v238, v[248:251] offset:1024
v_mfma_f32_16x16x32_bf16 a[188:191], v[216:219], v[0:3], a[188:191]
v_mfma_f32_16x16x32_bf16 a[12:15], v[216:219], v[8:11], a[12:15]
v_mfma_f32_16x16x32_bf16 a[12:15], v[220:223], v[12:15], a[12:15]
buffer_load_dwordx4 v[152:155], v246, s[52:55], s91 offen offset:1024 sc1
v_mfma_f32_16x16x32_bf16 a[8:11], v[212:215], v[12:15], a[8:11]
v_mfma_f32_16x16x32_bf16 a[8:11], v[208:211], v[8:11], a[8:11]
ds_read_b64_tr_b16 v[52:53], v242 offset:50816
ds_read_b64_tr_b16 v[54:55], v242 offset:51840
v_mfma_f32_16x16x32_bf16 a[76:79], v[200:203], v[8:11], a[76:79]
v_mfma_f32_16x16x32_bf16 a[76:79], v[204:207], v[12:15], a[76:79]
buffer_load_dwordx4 v[156:159], v247, s[52:55], s91 offen offset:1024 sc1
v_mfma_f32_16x16x32_bf16 a[72:75], v[196:199], v[12:15], a[72:75]
v_mfma_f32_16x16x32_bf16 a[72:75], v[192:195], v[8:11], a[72:75]
ds_read_b64_tr_b16 v[56:57], v243 offset:33920
ds_read_b64_tr_b16 v[58:59], v243 offset:34944
v_mfma_f32_16x16x32_bf16 a[88:91], v[192:195], v[16:19], a[88:91]
v_mfma_f32_16x16x32_bf16 a[88:91], v[196:199], v[20:23], a[88:91]
v_mfma_f32_16x16x32_bf16 a[92:95], v[204:207], v[20:23], a[92:95]
v_mfma_f32_16x16x32_bf16 a[92:95], v[200:203], v[16:19], a[92:95]
ds_read_b64_tr_b16 v[60:61], v243 offset:50816
ds_read_b64_tr_b16 v[62:63], v243 offset:51840
v_mfma_f32_16x16x32_bf16 a[24:27], v[208:211], v[16:19], a[24:27]
v_mfma_f32_16x16x32_bf16 a[24:27], v[212:215], v[20:23], a[24:27]
v_mfma_f32_16x16x32_bf16 a[28:31], v[220:223], v[20:23], a[28:31]
v_mfma_f32_16x16x32_bf16 a[28:31], v[216:219], v[16:19], a[28:31]
v_mfma_f32_16x16x32_bf16 a[44:47], v[216:219], v[24:27], a[44:47]
v_mfma_f32_16x16x32_bf16 a[44:47], v[220:223], v[28:31], a[44:47]
v_mfma_f32_16x16x32_bf16 a[40:43], v[212:215], v[28:31], a[40:43]
v_mfma_f32_16x16x32_bf16 a[40:43], v[208:211], v[24:27], a[40:43]
v_mfma_f32_16x16x32_bf16 a[252:255], v[200:203], v[24:27], a[252:255]
v_mfma_f32_16x16x32_bf16 a[252:255], v[204:207], v[28:31], a[252:255]
v_mfma_f32_16x16x32_bf16 a[248:251], v[196:199], v[28:31], a[248:251]
s_waitcnt vmcnt(10) lgkmcnt(0)
s_barrier
v_mfma_f32_16x16x32_bf16 a[248:251], v[192:195], v[24:27], a[248:251]
v_mfma_f32_16x16x32_bf16 a[112:115], v[32:35], v[160:163], a[112:115]
s_add_u32 m0, s83, 33792
v_mfma_f32_16x16x32_bf16 a[112:115], v[36:39], v[164:167], a[112:115]
buffer_load_dwordx4 v232, s[44:47], s80 offen lds
v_mfma_f32_16x16x32_bf16 a[116:119], v[44:47], v[164:167], a[116:119]
v_mfma_f32_16x16x32_bf16 a[116:119], v[40:43], v[160:163], a[116:119]
ds_read_b64_tr_b16 v[192:193], v240 offset:0
ds_read_b64_tr_b16 v[194:195], v240 offset:1024
v_mfma_f32_16x16x32_bf16 a[128:131], v[48:51], v[160:163], a[128:131]
s_add_u32 m0, s83, 34816
v_mfma_f32_16x16x32_bf16 a[128:131], v[52:55], v[164:167], a[128:131]
buffer_load_dwordx4 v233, s[44:47], s80 offen lds
v_mfma_f32_16x16x32_bf16 a[132:135], v[60:63], v[164:167], a[132:135]
v_mfma_f32_16x16x32_bf16 a[132:135], v[56:59], v[160:163], a[132:135]
ds_read_b64_tr_b16 v[196:197], v240 offset:8448
ds_read_b64_tr_b16 v[198:199], v240 offset:9472
v_mfma_f32_16x16x32_bf16 a[148:151], v[56:59], v[168:171], a[148:151]
s_add_u32 m0, s83, 35968
v_mfma_f32_16x16x32_bf16 a[148:151], v[60:63], v[172:175], a[148:151]
buffer_load_dwordx4 v234, s[44:47], s80 offen lds
v_mfma_f32_16x16x32_bf16 a[144:147], v[52:55], v[172:175], a[144:147]
v_accvgpr_read_b32 v224, a112
v_accvgpr_read_b32 v225, a113
v_mfma_f32_16x16x32_bf16 a[144:147], v[48:51], v[168:171], a[144:147]
ds_read_b64_tr_b16 v[200:201], v241 offset:0
ds_read_b64_tr_b16 v[202:203], v241 offset:1024
v_mfma_f32_16x16x32_bf16 a[228:231], v[40:43], v[168:171], a[228:231]
s_add_u32 m0, s83, 36992
v_accvgpr_read_b32 v226, a114
v_mfma_f32_16x16x32_bf16 a[228:231], v[44:47], v[172:175], a[228:231]
buffer_load_dwordx4 v235, s[44:47], s80 offen lds
v_mfma_f32_16x16x32_bf16 a[224:227], v[36:39], v[172:175], a[224:227]
s_add_u32 s80, s80, 524288
v_accvgpr_read_b32 v227, a115
v_mfma_f32_16x16x32_bf16 a[224:227], v[32:35], v[168:171], a[224:227]
ds_read_b64_tr_b16 v[204:205], v241 offset:8448
ds_read_b64_tr_b16 v[206:207], v241 offset:9472
v_mfma_f32_16x16x32_bf16 a[208:211], v[32:35], v[176:179], a[208:211]
s_add_u32 m0, s83, 50688
v_accvgpr_read_b32 v228, a116
v_mfma_f32_16x16x32_bf16 a[208:211], v[36:39], v[180:183], a[208:211]
buffer_load_dwordx4 v232, s[44:47], s80 offen lds
v_mfma_f32_16x16x32_bf16 a[212:215], v[44:47], v[180:183], a[212:215]
v_accvgpr_read_b32 v229, a117
v_accvgpr_read_b32 v230, a118
v_mfma_f32_16x16x32_bf16 a[212:215], v[40:43], v[176:179], a[212:215]
ds_read_b64_tr_b16 v[208:209], v242 offset:0
ds_read_b64_tr_b16 v[210:211], v242 offset:1024
v_mfma_f32_16x16x32_bf16 a[160:163], v[48:51], v[176:179], a[160:163]
s_add_u32 m0, s83, 51712
v_accvgpr_read_b32 v231, a119
v_mfma_f32_16x16x32_bf16 a[160:163], v[52:55], v[180:183], a[160:163]
buffer_load_dwordx4 v233, s[44:47], s80 offen lds
v_mfma_f32_16x16x32_bf16 a[164:167], v[60:63], v[180:183], a[164:167]
v_cvt_pk_bf16_f32 v248, v224, v225
v_cvt_pk_bf16_f32 v249, v226, v227
v_mfma_f32_16x16x32_bf16 a[164:167], v[56:59], v[176:179], a[164:167]
ds_read_b64_tr_b16 v[212:213], v242 offset:8448
ds_read_b64_tr_b16 v[214:215], v242 offset:9472
v_mfma_f32_16x16x32_bf16 a[180:183], v[56:59], v[184:187], a[180:183]
s_add_u32 m0, s83, 52864
v_cvt_pk_bf16_f32 v250, v228, v229
v_mfma_f32_16x16x32_bf16 a[180:183], v[60:63], v[188:191], a[180:183]
buffer_load_dwordx4 v234, s[44:47], s80 offen lds
v_mfma_f32_16x16x32_bf16 a[176:179], v[52:55], v[188:191], a[176:179]
v_cvt_pk_bf16_f32 v251, v230, v231
buffer_store_dwordx4 v[248:251], v255, s[48:51], s89 offen sc1
v_mfma_f32_16x16x32_bf16 a[176:179], v[48:51], v[184:187], a[176:179]
ds_read_b64_tr_b16 v[216:217], v243 offset:0
ds_read_b64_tr_b16 v[218:219], v243 offset:1024
v_mfma_f32_16x16x32_bf16 a[196:199], v[40:43], v[184:187], a[196:199]
s_add_u32 m0, s83, 53888
s_mov_b32 s89, 52224
v_mfma_f32_16x16x32_bf16 a[196:199], v[44:47], v[188:191], a[196:199]
buffer_load_dwordx4 v235, s[44:47], s80 offen lds
v_mfma_f32_16x16x32_bf16 a[192:195], v[36:39], v[188:191], a[192:195]
s_add_u32 s80, s80, 524288
ds_write_b128 v238, v[248:251] offset:2048
v_mfma_f32_16x16x32_bf16 a[192:195], v[32:35], v[184:187], a[192:195]
ds_read_b64_tr_b16 v[220:221], v243 offset:8448
ds_read_b64_tr_b16 v[222:223], v243 offset:9472
v_mfma_f32_16x16x32_bf16 a[64:67], v[32:35], v[0:3], a[64:67]
v_mfma_f32_16x16x32_bf16 a[64:67], v[36:39], v[4:7], a[64:67]
buffer_load_dwordx4 v[160:163], v244, s[52:55], s91 offen offset:2048 sc1
v_mfma_f32_16x16x32_bf16 a[68:71], v[44:47], v[4:7], a[68:71]
v_mfma_f32_16x16x32_bf16 a[68:71], v[40:43], v[0:3], a[68:71]
v_mfma_f32_16x16x32_bf16 a[0:3], v[48:51], v[0:3], a[0:3]
v_mfma_f32_16x16x32_bf16 a[0:3], v[52:55], v[4:7], a[0:3]
buffer_load_dwordx4 v[164:167], v245, s[52:55], s91 offen offset:2048 sc1
v_mfma_f32_16x16x32_bf16 a[4:7], v[60:63], v[4:7], a[4:7]
v_mfma_f32_16x16x32_bf16 a[4:7], v[56:59], v[0:3], a[4:7]
v_mfma_f32_16x16x32_bf16 a[20:23], v[56:59], v[8:11], a[20:23]
v_mfma_f32_16x16x32_bf16 a[20:23], v[60:63], v[12:15], a[20:23]
buffer_load_dwordx4 v[168:171], v246, s[52:55], s91 offen offset:2048 sc1
v_mfma_f32_16x16x32_bf16 a[16:19], v[52:55], v[12:15], a[16:19]
v_mfma_f32_16x16x32_bf16 a[16:19], v[48:51], v[8:11], a[16:19]
v_mfma_f32_16x16x32_bf16 a[84:87], v[40:43], v[8:11], a[84:87]
v_mfma_f32_16x16x32_bf16 a[84:87], v[44:47], v[12:15], a[84:87]
buffer_load_dwordx4 v[172:175], v247, s[52:55], s91 offen offset:2048 sc1
v_mfma_f32_16x16x32_bf16 a[80:83], v[36:39], v[12:15], a[80:83]
v_mfma_f32_16x16x32_bf16 a[80:83], v[32:35], v[8:11], a[80:83]
v_mfma_f32_16x16x32_bf16 a[240:243], v[32:35], v[16:19], a[240:243]
v_mfma_f32_16x16x32_bf16 a[240:243], v[36:39], v[20:23], a[240:243]
v_mfma_f32_16x16x32_bf16 a[244:247], v[44:47], v[20:23], a[244:247]
v_mfma_f32_16x16x32_bf16 a[244:247], v[40:43], v[16:19], a[244:247]
v_mfma_f32_16x16x32_bf16 a[32:35], v[48:51], v[16:19], a[32:35]
v_mfma_f32_16x16x32_bf16 a[32:35], v[52:55], v[20:23], a[32:35]
v_mfma_f32_16x16x32_bf16 a[36:39], v[60:63], v[20:23], a[36:39]
v_mfma_f32_16x16x32_bf16 a[36:39], v[56:59], v[16:19], a[36:39]
v_mfma_f32_16x16x32_bf16 a[60:63], v[56:59], v[24:27], a[60:63]
v_mfma_f32_16x16x32_bf16 a[60:63], v[60:63], v[28:31], a[60:63]
v_mfma_f32_16x16x32_bf16 a[56:59], v[52:55], v[28:31], a[56:59]
v_mfma_f32_16x16x32_bf16 a[56:59], v[48:51], v[24:27], a[56:59]
v_mfma_f32_16x16x32_bf16 a[52:55], v[40:43], v[24:27], a[52:55]
v_mfma_f32_16x16x32_bf16 a[52:55], v[44:47], v[28:31], a[52:55]
v_mfma_f32_16x16x32_bf16 a[48:51], v[36:39], v[28:31], a[48:51]
v_mfma_f32_16x16x32_bf16 a[48:51], v[32:35], v[24:27], a[48:51]
s_branch .La5_LQ_START
; ---- V tile (waves 1, 3): the same with the operands exchanged (V comes out transposed)
.La5_LV_START:
.La5_LW1_4:
s_cbranch_execz .La5_LW1_8
v_mfma_f32_16x16x32_bf16 a[96:99], v[160:163], v[192:195], 0
s_add_u32 m0, s83, 33792
v_mfma_f32_16x16x32_bf16 a[96:99], v[164:167], v[196:199], a[96:99]
buffer_load_dwordx4 v232, s[44:47], s80 offen lds
v_mfma_f32_16x16x32_bf16 a[104:107], v[164:167], v[204:207], 0
ds_read_b128 v[0:3], v236 offset:8192
ds_read_b128 v[4:7], v237 offset:8192
s_add_u32 m0, s83, 34816
v_mfma_f32_16x16x32_bf16 a[104:107], v[160:163], v[200:203], a[104:107]
ds_read_b128 v[8:11], v236 offset:10240
ds_read_b128 v[12:15], v237 offset:10240
buffer_load_dwordx4 v233, s[44:47], s80 offen lds
v_mfma_f32_16x16x32_bf16 a[112:115], v[160:163], v[208:211], 0
s_add_u32 m0, s83, 35968
v_mfma_f32_16x16x32_bf16 a[112:115], v[164:167], v[212:215], a[112:115]
buffer_load_dwordx4 v234, s[44:47], s80 offen lds
v_mfma_f32_16x16x32_bf16 a[128:131], v[164:167], v[220:223], 0
ds_read_b128 v[16:19], v236 offset:0
ds_read_b128 v[20:23], v237 offset:0
s_add_u32 m0, s83, 36992
v_mfma_f32_16x16x32_bf16 a[128:131], v[160:163], v[216:219], a[128:131]
ds_read_b128 v[24:27], v236 offset:2048
ds_read_b128 v[28:31], v237 offset:2048
buffer_load_dwordx4 v235, s[44:47], s80 offen lds
v_mfma_f32_16x16x32_bf16 a[132:135], v[168:171], v[216:219], 0
s_mov_b32 s80, 2883584
v_mfma_f32_16x16x32_bf16 a[132:135], v[172:175], v[220:223], a[132:135]
v_mfma_f32_16x16x32_bf16 a[116:119], v[172:175], v[212:215], 0
s_xor_b32 m0, s82, 32768
v_mfma_f32_16x16x32_bf16 a[116:119], v[168:171], v[208:211], a[116:119]
ds_read_b64_tr_b16 v[32:33], v240 offset:128
ds_read_b64_tr_b16 v[34:35], v240 offset:1152
buffer_load_dwordx4 v224, s[40:43], s79 offen lds
v_mfma_f32_16x16x32_bf16 a[108:111], v[168:171], v[200:203], 0
s_xor_b32 m0, s82, 33792
v_mfma_f32_16x16x32_bf16 a[108:111], v[172:175], v[204:207], a[108:111]
buffer_load_dwordx4 v225, s[40:43], s79 offen lds
v_mfma_f32_16x16x32_bf16 a[100:103], v[172:175], v[196:199], 0
s_xor_b32 m0, s82, 34816
v_mfma_f32_16x16x32_bf16 a[100:103], v[168:171], v[192:195], a[100:103]
ds_read_b64_tr_b16 v[36:37], v240 offset:17024
ds_read_b64_tr_b16 v[38:39], v240 offset:18048
buffer_load_dwordx4 v226, s[40:43], s79 offen lds
v_mfma_f32_16x16x32_bf16 a[232:235], v[176:179], v[192:195], 0
s_xor_b32 m0, s82, 35840
v_mfma_f32_16x16x32_bf16 a[232:235], v[180:183], v[196:199], a[232:235]
buffer_load_dwordx4 v227, s[40:43], s79 offen lds
v_mfma_f32_16x16x32_bf16 a[152:155], v[180:183], v[204:207], 0
s_xor_b32 m0, s82, 36864
v_mfma_f32_16x16x32_bf16 a[152:155], v[176:179], v[200:203], a[152:155]
ds_read_b64_tr_b16 v[40:41], v241 offset:128
ds_read_b64_tr_b16 v[42:43], v241 offset:1152
buffer_load_dwordx4 v228, s[40:43], s79 offen lds
v_mfma_f32_16x16x32_bf16 a[208:211], v[176:179], v[208:211], 0
s_xor_b32 m0, s82, 37888
v_mfma_f32_16x16x32_bf16 a[208:211], v[180:183], v[212:215], a[208:211]
buffer_load_dwordx4 v229, s[40:43], s79 offen lds
v_mfma_f32_16x16x32_bf16 a[160:163], v[180:183], v[220:223], 0
s_xor_b32 m0, s82, 38912
v_mfma_f32_16x16x32_bf16 a[160:163], v[176:179], v[216:219], a[160:163]
buffer_load_dwordx4 v230, s[40:43], s79 offen lds
v_mfma_f32_16x16x32_bf16 a[164:167], v[184:187], v[216:219], 0
s_xor_b32 m0, s82, 39936
v_mfma_f32_16x16x32_bf16 a[164:167], v[188:191], v[220:223], a[164:167]
buffer_load_dwordx4 v231, s[40:43], s79 offen lds
v_mfma_f32_16x16x32_bf16 a[212:215], v[188:191], v[212:215], 0
s_mov_b32 s79, 768
v_mfma_f32_16x16x32_bf16 a[212:215], v[184:187], v[208:211], a[212:215]
ds_read_b64_tr_b16 v[44:45], v241 offset:17024
ds_read_b64_tr_b16 v[46:47], v241 offset:18048
v_mfma_f32_16x16x32_bf16 a[156:159], v[184:187], v[200:203], 0
s_add_u32 m0, s83, 50688
v_mfma_f32_16x16x32_bf16 a[156:159], v[188:191], v[204:207], a[156:159]
buffer_load_dwordx4 v232, s[44:47], s80 offen lds
v_mfma_f32_16x16x32_bf16 a[236:239], v[188:191], v[196:199], 0
s_waitcnt lgkmcnt(8)
s_add_u32 m0, s83, 51712
v_mfma_f32_16x16x32_bf16 a[236:239], v[184:187], v[192:195], a[236:239]
buffer_load_dwordx4 v233, s[44:47], s80 offen lds
v_mfma_f32_16x16x32_bf16 a[200:203], v[0:3], v[192:195], 0
s_add_u32 m0, s83, 52864
v_mfma_f32_16x16x32_bf16 a[200:203], v[4:7], v[196:199], a[200:203]
buffer_load_dwordx4 v234, s[44:47], s80 offen lds
v_mfma_f32_16x16x32_bf16 a[184:187], v[4:7], v[204:207], 0
s_add_u32 m0, s83, 53888
v_mfma_f32_16x16x32_bf16 a[184:187], v[0:3], v[200:203], a[184:187]
ds_read_b64_tr_b16 v[48:49], v242 offset:128
ds_read_b64_tr_b16 v[50:51], v242 offset:1152
buffer_load_dwordx4 v235, s[44:47], s80 offen lds
v_mfma_f32_16x16x32_bf16 a[64:67], v[0:3], v[208:211], 0
s_mov_b32 s80, 3145728
v_mfma_f32_16x16x32_bf16 a[64:67], v[4:7], v[212:215], a[64:67]
v_mfma_f32_16x16x32_bf16 a[0:3], v[4:7], v[220:223], 0
v_mfma_f32_16x16x32_bf16 a[0:3], v[0:3], v[216:219], a[0:3]
v_mfma_f32_16x16x32_bf16 a[4:7], v[8:11], v[216:219], 0
v_mfma_f32_16x16x32_bf16 a[4:7], v[12:15], v[220:223], a[4:7]
buffer_load_dword v70, v118, s[60:63], 0 offen
v_mfma_f32_16x16x32_bf16 a[68:71], v[12:15], v[212:215], 0
v_mfma_f32_16x16x32_bf16 a[68:71], v[8:11], v[208:211], a[68:71]
ds_read_b64_tr_b16 v[52:53], v242 offset:17024
ds_read_b64_tr_b16 v[54:55], v242 offset:18048
v_mfma_f32_16x16x32_bf16 a[188:191], v[8:11], v[200:203], 0
v_mfma_f32_16x16x32_bf16 a[188:191], v[12:15], v[204:207], a[188:191]
buffer_load_dwordx4 v[72:75], v119, s[60:63], 0 offen offset:0
v_mfma_f32_16x16x32_bf16 a[204:207], v[12:15], v[196:199], 0
v_mfma_f32_16x16x32_bf16 a[204:207], v[8:11], v[192:195], a[204:207]
ds_read_b64_tr_b16 v[56:57], v243 offset:128
ds_read_b64_tr_b16 v[58:59], v243 offset:1152
v_mfma_f32_16x16x32_bf16 a[88:91], v[16:19], v[192:195], 0
v_mfma_f32_16x16x32_bf16 a[88:91], v[20:23], v[196:199], a[88:91]
buffer_load_dwordx4 v[76:79], v119, s[60:63], 0 offen offset:128
v_mfma_f32_16x16x32_bf16 a[24:27], v[20:23], v[204:207], 0
v_mfma_f32_16x16x32_bf16 a[24:27], v[16:19], v[200:203], a[24:27]
ds_read_b64_tr_b16 v[60:61], v243 offset:17024
ds_read_b64_tr_b16 v[62:63], v243 offset:18048
v_mfma_f32_16x16x32_bf16 a[240:243], v[16:19], v[208:211], 0
v_mfma_f32_16x16x32_bf16 a[240:243], v[20:23], v[212:215], a[240:243]
buffer_load_dwordx4 v[80:83], v119, s[60:63], 0 offen offset:256
v_mfma_f32_16x16x32_bf16 a[32:35], v[20:23], v[220:223], 0
v_mfma_f32_16x16x32_bf16 a[32:35], v[16:19], v[216:219], a[32:35]
v_mfma_f32_16x16x32_bf16 a[36:39], v[24:27], v[216:219], 0
v_mfma_f32_16x16x32_bf16 a[36:39], v[28:31], v[220:223], a[36:39]
buffer_load_dwordx4 v[84:87], v119, s[60:63], 0 offen offset:384
v_mfma_f32_16x16x32_bf16 a[244:247], v[28:31], v[212:215], 0
v_mfma_f32_16x16x32_bf16 a[244:247], v[24:27], v[208:211], a[244:247]
v_mfma_f32_16x16x32_bf16 a[28:31], v[24:27], v[200:203], 0
v_mfma_f32_16x16x32_bf16 a[28:31], v[28:31], v[204:207], a[28:31]
v_mfma_f32_16x16x32_bf16 a[92:95], v[28:31], v[196:199], 0
v_mfma_f32_16x16x32_bf16 a[92:95], v[24:27], v[192:195], a[92:95]
s_mov_b64 exec, 0
s_cmp_eq_u32 s3, 1
s_cbranch_scc1 .La5_LW2_1
s_branch .La5_LW2_R1
.La5_LW2_R1:
s_mov_b64 exec, -1
s_waitcnt vmcnt(5) lgkmcnt(0)
s_barrier
v_mfma_f32_16x16x32_bf16 a[120:123], v[160:163], v[32:35], 0
v_mfma_f32_16x16x32_bf16 a[120:123], v[164:167], v[36:39], a[120:123]
v_mfma_f32_16x16x32_bf16 a[136:139], v[164:167], v[44:47], 0
ds_read_b64_tr_b16 v[192:193], v240 offset:33792
ds_read_b64_tr_b16 v[194:195], v240 offset:34816
v_mfma_f32_16x16x32_bf16 a[136:139], v[160:163], v[40:43], a[136:139]
ds_read_b64_tr_b16 v[196:197], v240 offset:50688
ds_read_b64_tr_b16 v[198:199], v240 offset:51712
v_mfma_f32_16x16x32_bf16 a[224:227], v[160:163], v[48:51], 0
v_mfma_f32_16x16x32_bf16 a[224:227], v[164:167], v[52:55], a[224:227]
v_mfma_f32_16x16x32_bf16 a[144:147], v[164:167], v[60:63], 0
v_mfma_f32_16x16x32_bf16 a[144:147], v[160:163], v[56:59], a[144:147]
ds_read_b128 v[160:163], v236 offset:57344
ds_read_b128 v[164:167], v237 offset:57344
v_mfma_f32_16x16x32_bf16 a[148:151], v[168:171], v[56:59], 0
v_mfma_f32_16x16x32_bf16 a[148:151], v[172:175], v[60:63], a[148:151]
v_mfma_f32_16x16x32_bf16 a[228:231], v[172:175], v[52:55], 0
v_mfma_f32_16x16x32_bf16 a[228:231], v[168:171], v[48:51], a[228:231]
ds_read_b64_tr_b16 v[200:201], v241 offset:33792
ds_read_b64_tr_b16 v[202:203], v241 offset:34816
v_mfma_f32_16x16x32_bf16 a[140:143], v[168:171], v[40:43], 0
v_mfma_f32_16x16x32_bf16 a[140:143], v[172:175], v[44:47], a[140:143]
v_mfma_f32_16x16x32_bf16 a[124:127], v[172:175], v[36:39], 0
v_mfma_f32_16x16x32_bf16 a[124:127], v[168:171], v[32:35], a[124:127]
ds_read_b128 v[168:171], v236 offset:59392
ds_read_b128 v[172:175], v237 offset:59392
v_mfma_f32_16x16x32_bf16 a[216:219], v[176:179], v[32:35], 0
s_xor_b32 m0, s82, 0
v_mfma_f32_16x16x32_bf16 a[216:219], v[180:183], v[36:39], a[216:219]
buffer_load_dwordx4 v224, s[40:43], s79 offen lds
v_mfma_f32_16x16x32_bf16 a[168:171], v[180:183], v[44:47], 0
v_mfma_f32_16x16x32_bf16 a[168:171], v[176:179], v[40:43], a[168:171]
ds_read_b64_tr_b16 v[204:205], v241 offset:50688
ds_read_b64_tr_b16 v[206:207], v241 offset:51712
s_xor_b32 m0, s82, 1024
v_mfma_f32_16x16x32_bf16 a[192:195], v[176:179], v[48:51], 0
buffer_load_dwordx4 v225, s[40:43], s79 offen lds
v_mfma_f32_16x16x32_bf16 a[192:195], v[180:183], v[52:55], a[192:195]
v_mfma_f32_16x16x32_bf16 a[176:179], v[180:183], v[60:63], 0
s_xor_b32 m0, s82, 2048
v_mfma_f32_16x16x32_bf16 a[176:179], v[176:179], v[56:59], a[176:179]
ds_read_b128 v[176:179], v236 offset:49152
ds_read_b128 v[180:183], v237 offset:49152
buffer_load_dwordx4 v226, s[40:43], s79 offen lds
v_mfma_f32_16x16x32_bf16 a[180:183], v[184:187], v[56:59], 0
v_mfma_f32_16x16x32_bf16 a[180:183], v[188:191], v[60:63], a[180:183]
s_xor_b32 m0, s82, 3072
v_mfma_f32_16x16x32_bf16 a[196:199], v[188:191], v[52:55], 0
buffer_load_dwordx4 v227, s[40:43], s79 offen lds
v_mfma_f32_16x16x32_bf16 a[196:199], v[184:187], v[48:51], a[196:199]
ds_read_b64_tr_b16 v[208:209], v242 offset:33792
ds_read_b64_tr_b16 v[210:211], v242 offset:34816
v_mfma_f32_16x16x32_bf16 a[172:175], v[184:187], v[40:43], 0
s_xor_b32 m0, s82, 4096
v_mfma_f32_16x16x32_bf16 a[172:175], v[188:191], v[44:47], a[172:175]
buffer_load_dwordx4 v228, s[40:43], s79 offen lds
v_mfma_f32_16x16x32_bf16 a[220:223], v[188:191], v[36:39], 0
v_mfma_f32_16x16x32_bf16 a[220:223], v[184:187], v[32:35], a[220:223]
ds_read_b128 v[184:187], v236 offset:51200
ds_read_b128 v[188:191], v237 offset:51200
s_xor_b32 m0, s82, 5120
v_mfma_f32_16x16x32_bf16 a[72:75], v[0:3], v[32:35], 0
buffer_load_dwordx4 v229, s[40:43], s79 offen lds
v_mfma_f32_16x16x32_bf16 a[72:75], v[4:7], v[36:39], a[72:75]
v_mfma_f32_16x16x32_bf16 a[8:11], v[4:7], v[44:47], 0
s_xor_b32 m0, s82, 6144
v_mfma_f32_16x16x32_bf16 a[8:11], v[0:3], v[40:43], a[8:11]
ds_read_b64_tr_b16 v[212:213], v242 offset:50688
ds_read_b64_tr_b16 v[214:215], v242 offset:51712
buffer_load_dwordx4 v230, s[40:43], s79 offen lds
v_mfma_f32_16x16x32_bf16 a[80:83], v[0:3], v[48:51], 0
v_mfma_f32_16x16x32_bf16 a[80:83], v[4:7], v[52:55], a[80:83]
s_xor_b32 m0, s82, 7168
v_mfma_f32_16x16x32_bf16 a[16:19], v[4:7], v[60:63], 0
buffer_load_dwordx4 v231, s[40:43], s79 offen lds
v_mfma_f32_16x16x32_bf16 a[16:19], v[0:3], v[56:59], a[16:19]
ds_read_b128 v[0:3], v236 offset:40960
ds_read_b128 v[4:7], v237 offset:40960
s_mov_b32 s79, 896
v_mfma_f32_16x16x32_bf16 a[20:23], v[8:11], v[56:59], 0
v_mfma_f32_16x16x32_bf16 a[20:23], v[12:15], v[60:63], a[20:23]
s_add_u32 m0, s83, 0
v_mfma_f32_16x16x32_bf16 a[84:87], v[12:15], v[52:55], 0
buffer_load_dwordx4 v232, s[44:47], s80 offen lds
v_mfma_f32_16x16x32_bf16 a[84:87], v[8:11], v[48:51], a[84:87]
ds_read_b64_tr_b16 v[216:217], v243 offset:33792
ds_read_b64_tr_b16 v[218:219], v243 offset:34816
v_mfma_f32_16x16x32_bf16 a[12:15], v[8:11], v[40:43], 0
s_add_u32 m0, s83, 1024
v_mfma_f32_16x16x32_bf16 a[12:15], v[12:15], v[44:47], a[12:15]
buffer_load_dwordx4 v233, s[44:47], s80 offen lds
v_mfma_f32_16x16x32_bf16 a[76:79], v[12:15], v[36:39], 0
s_waitcnt vmcnt(10)
v_lshlrev_b32 v70, 16, v70
v_mfma_f32_16x16x32_bf16 a[76:79], v[8:11], v[32:35], a[76:79]
ds_read_b128 v[8:11], v236 offset:43008
ds_read_b128 v[12:15], v237 offset:43008
s_add_u32 m0, s83, 2176
v_mfma_f32_16x16x32_bf16 a[248:251], v[16:19], v[32:35], 0
buffer_load_dwordx4 v234, s[44:47], s80 offen lds
v_mfma_f32_16x16x32_bf16 a[248:251], v[20:23], v[36:39], a[248:251]
v_lshlrev_b32 v88, 16, v72
v_and_b32 v89, 0xffff0000, v72
v_mfma_f32_16x16x32_bf16 a[40:43], v[20:23], v[44:47], 0
s_add_u32 m0, s83, 3200
v_sub_f32 v88, v88, v70
v_mfma_f32_16x16x32_bf16 a[40:43], v[16:19], v[40:43], a[40:43]
ds_read_b64_tr_b16 v[220:221], v243 offset:50688
ds_read_b64_tr_b16 v[222:223], v243 offset:51712
buffer_load_dwordx4 v235, s[44:47], s80 offen lds
v_mfma_f32_16x16x32_bf16 a[48:51], v[16:19], v[48:51], 0
s_mov_b32 s80, 3407872
v_sub_f32 v89, v89, v70
v_mfma_f32_16x16x32_bf16 a[48:51], v[20:23], v[52:55], a[48:51]
v_add_f32 v120, v88, v89
v_mul_f32 v122, v88, v88
v_mfma_f32_16x16x32_bf16 a[56:59], v[20:23], v[60:63], 0
s_add_u32 m0, s83, 16896
v_mul_f32 v123, v89, v89
v_mfma_f32_16x16x32_bf16 a[56:59], v[16:19], v[56:59], a[56:59]
ds_read_b128 v[16:19], v236 offset:32768
ds_read_b128 v[20:23], v237 offset:32768
buffer_load_dwordx4 v232, s[44:47], s80 offen lds
v_mfma_f32_16x16x32_bf16 a[60:63], v[24:27], v[56:59], 0
v_lshlrev_b32 v90, 16, v73
v_and_b32 v91, 0xffff0000, v73
v_mfma_f32_16x16x32_bf16 a[60:63], v[28:31], v[60:63], a[60:63]
s_add_u32 m0, s83, 17920
v_sub_f32 v90, v90, v70
v_mfma_f32_16x16x32_bf16 a[52:55], v[28:31], v[52:55], 0
buffer_load_dwordx4 v233, s[44:47], s80 offen lds
v_mfma_f32_16x16x32_bf16 a[52:55], v[24:27], v[48:51], a[52:55]
v_sub_f32 v91, v91, v70
v_add_f32 v124, v90, v91
v_mfma_f32_16x16x32_bf16 a[44:47], v[24:27], v[40:43], 0
s_add_u32 m0, s83, 19072
v_fmac_f32 v122, v90, v90
v_mfma_f32_16x16x32_bf16 a[44:47], v[28:31], v[44:47], a[44:47]
buffer_load_dwordx4 v234, s[44:47], s80 offen lds
v_mfma_f32_16x16x32_bf16 a[252:255], v[28:31], v[36:39], 0
s_waitcnt lgkmcnt(2)
v_add_f32 v120, v120, v124
v_mfma_f32_16x16x32_bf16 a[252:255], v[24:27], v[32:35], a[252:255]
ds_read_b128 v[24:27], v236 offset:34816
ds_read_b128 v[28:31], v237 offset:34816
s_add_u32 m0, s83, 20096
v_mfma_f32_16x16x32_bf16 a[96:99], v[160:163], v[192:195], a[96:99]
buffer_load_dwordx4 v235, s[44:47], s80 offen lds
v_mfma_f32_16x16x32_bf16 a[96:99], v[164:167], v[196:199], a[96:99]
s_mov_b32 s80, 3670016
v_fmac_f32 v123, v91, v91
v_mfma_f32_16x16x32_bf16 a[104:107], v[164:167], v[204:207], a[104:107]
v_lshlrev_b32 v92, 16, v74
v_and_b32 v93, 0xffff0000, v74
v_mfma_f32_16x16x32_bf16 a[104:107], v[160:163], v[200:203], a[104:107]
ds_read_b64_tr_b16 v[32:33], v240 offset:33920
ds_read_b64_tr_b16 v[34:35], v240 offset:34944
v_mfma_f32_16x16x32_bf16 a[112:115], v[160:163], v[208:211], a[112:115]
v_sub_f32 v92, v92, v70
v_sub_f32 v93, v93, v70
v_mfma_f32_16x16x32_bf16 a[112:115], v[164:167], v[212:215], a[112:115]
v_add_f32 v124, v92, v93
v_fmac_f32 v122, v92, v92
v_mfma_f32_16x16x32_bf16 a[128:131], v[164:167], v[220:223], a[128:131]
s_waitcnt lgkmcnt(2)
s_barrier
v_mfma_f32_16x16x32_bf16 a[128:131], v[160:163], v[216:219], a[128:131]
v_add_f32 v120, v120, v124
v_fmac_f32 v123, v93, v93
v_mfma_f32_16x16x32_bf16 a[132:135], v[168:171], v[216:219], a[132:135]
s_xor_b32 m0, s82, 32768
v_lshlrev_b32 v94, 16, v75
v_mfma_f32_16x16x32_bf16 a[132:135], v[172:175], v[220:223], a[132:135]
buffer_load_dwordx4 v224, s[40:43], s79 offen lds
v_mfma_f32_16x16x32_bf16 a[116:119], v[172:175], v[212:215], a[116:119]
v_and_b32 v95, 0xffff0000, v75
v_sub_f32 v94, v94, v70
v_mfma_f32_16x16x32_bf16 a[116:119], v[168:171], v[208:211], a[116:119]
ds_read_b64_tr_b16 v[36:37], v240 offset:50816
ds_read_b64_tr_b16 v[38:39], v240 offset:51840
v_mfma_f32_16x16x32_bf16 a[108:111], v[168:171], v[200:203], a[108:111]
s_xor_b32 m0, s82, 33792
v_sub_f32 v95, v95, v70
v_mfma_f32_16x16x32_bf16 a[108:111], v[172:175], v[204:207], a[108:111]
buffer_load_dwordx4 v225, s[40:43], s79 offen lds
v_mfma_f32_16x16x32_bf16 a[100:103], v[172:175], v[196:199], a[100:103]
v_add_f32 v124, v94, v95
v_fmac_f32 v122, v94, v94
v_mfma_f32_16x16x32_bf16 a[100:103], v[168:171], v[192:195], a[100:103]
v_add_f32 v120, v120, v124
v_fmac_f32 v123, v95, v95
v_mfma_f32_16x16x32_bf16 a[232:235], v[176:179], v[192:195], a[232:235]
s_xor_b32 m0, s82, 34816
v_lshlrev_b32 v96, 16, v76
v_mfma_f32_16x16x32_bf16 a[232:235], v[180:183], v[196:199], a[232:235]
buffer_load_dwordx4 v226, s[40:43], s79 offen lds
v_mfma_f32_16x16x32_bf16 a[152:155], v[180:183], v[204:207], a[152:155]
v_and_b32 v97, 0xffff0000, v76
v_sub_f32 v96, v96, v70
v_mfma_f32_16x16x32_bf16 a[152:155], v[176:179], v[200:203], a[152:155]
ds_read_b64_tr_b16 v[40:41], v241 offset:33920
ds_read_b64_tr_b16 v[42:43], v241 offset:34944
v_mfma_f32_16x16x32_bf16 a[208:211], v[176:179], v[208:211], a[208:211]
s_xor_b32 m0, s82, 35840
v_sub_f32 v97, v97, v70
v_mfma_f32_16x16x32_bf16 a[208:211], v[180:183], v[212:215], a[208:211]
buffer_load_dwordx4 v227, s[40:43], s79 offen lds
v_mfma_f32_16x16x32_bf16 a[160:163], v[180:183], v[220:223], a[160:163]
v_add_f32 v124, v96, v97
v_fmac_f32 v122, v96, v96
v_mfma_f32_16x16x32_bf16 a[160:163], v[176:179], v[216:219], a[160:163]
v_add_f32 v120, v120, v124
v_fmac_f32 v123, v97, v97
v_mfma_f32_16x16x32_bf16 a[164:167], v[184:187], v[216:219], a[164:167]
s_xor_b32 m0, s82, 36864
v_lshlrev_b32 v98, 16, v77
v_mfma_f32_16x16x32_bf16 a[164:167], v[188:191], v[220:223], a[164:167]
buffer_load_dwordx4 v228, s[40:43], s79 offen lds
v_mfma_f32_16x16x32_bf16 a[212:215], v[188:191], v[212:215], a[212:215]
v_and_b32 v99, 0xffff0000, v77
v_sub_f32 v98, v98, v70
v_mfma_f32_16x16x32_bf16 a[212:215], v[184:187], v[208:211], a[212:215]
ds_read_b64_tr_b16 v[44:45], v241 offset:50816
ds_read_b64_tr_b16 v[46:47], v241 offset:51840
v_mfma_f32_16x16x32_bf16 a[156:159], v[184:187], v[200:203], a[156:159]
s_xor_b32 m0, s82, 37888
v_sub_f32 v99, v99, v70
v_mfma_f32_16x16x32_bf16 a[156:159], v[188:191], v[204:207], a[156:159]
buffer_load_dwordx4 v229, s[40:43], s79 offen lds
v_mfma_f32_16x16x32_bf16 a[236:239], v[188:191], v[196:199], a[236:239]
v_add_f32 v124, v98, v99
v_fmac_f32 v122, v98, v98
v_mfma_f32_16x16x32_bf16 a[236:239], v[184:187], v[192:195], a[236:239]
v_add_f32 v120, v120, v124
v_fmac_f32 v123, v99, v99
v_mfma_f32_16x16x32_bf16 a[200:203], v[0:3], v[192:195], a[200:203]
s_xor_b32 m0, s82, 38912
.La5_LW1_5:
s_cbranch_execz .La5_LW1_9
v_lshlrev_b32 v100, 16, v78
v_mfma_f32_16x16x32_bf16 a[200:203], v[4:7], v[196:199], a[200:203]
buffer_load_dwordx4 v230, s[40:43], s79 offen lds
v_mfma_f32_16x16x32_bf16 a[184:187], v[4:7], v[204:207], a[184:187]
v_and_b32 v101, 0xffff0000, v78
v_sub_f32 v100, v100, v70
v_mfma_f32_16x16x32_bf16 a[184:187], v[0:3], v[200:203], a[184:187]
ds_read_b64_tr_b16 v[48:49], v242 offset:33920
ds_read_b64_tr_b16 v[50:51], v242 offset:34944
v_mfma_f32_16x16x32_bf16 a[64:67], v[0:3], v[208:211], a[64:67]
s_xor_b32 m0, s82, 39936
v_sub_f32 v101, v101, v70
v_mfma_f32_16x16x32_bf16 a[64:67], v[4:7], v[212:215], a[64:67]
buffer_load_dwordx4 v231, s[40:43], s79 offen lds
v_mfma_f32_16x16x32_bf16 a[0:3], v[4:7], v[220:223], a[0:3]
s_mov_b32 s79, 0
v_add_f32 v124, v100, v101
v_mfma_f32_16x16x32_bf16 a[0:3], v[0:3], v[216:219], a[0:3]
v_fmac_f32 v122, v100, v100
v_add_f32 v120, v120, v124
v_mfma_f32_16x16x32_bf16 a[4:7], v[8:11], v[216:219], a[4:7]
v_fmac_f32 v123, v101, v101
v_lshlrev_b32 v102, 16, v79
v_mfma_f32_16x16x32_bf16 a[4:7], v[12:15], v[220:223], a[4:7]
buffer_load_dword v254, v132, s[44:47], s86 offen
v_mfma_f32_16x16x32_bf16 a[68:71], v[12:15], v[212:215], a[68:71]
s_sub_u32 s86, s86, 2097152
v_and_b32 v103, 0xffff0000, v79
v_mfma_f32_16x16x32_bf16 a[68:71], v[8:11], v[208:211], a[68:71]
ds_read_b64_tr_b16 v[52:53], v242 offset:50816
ds_read_b64_tr_b16 v[54:55], v242 offset:51840
v_mfma_f32_16x16x32_bf16 a[188:191], v[8:11], v[200:203], a[188:191]
v_sub_f32 v102, v102, v70
v_sub_f32 v103, v103, v70
v_mfma_f32_16x16x32_bf16 a[188:191], v[12:15], v[204:207], a[188:191]
v_add_f32 v124, v102, v103
v_fmac_f32 v122, v102, v102
v_mfma_f32_16x16x32_bf16 a[204:207], v[12:15], v[196:199], a[204:207]
v_add_f32 v120, v120, v124
v_fmac_f32 v123, v103, v103
v_mfma_f32_16x16x32_bf16 a[204:207], v[8:11], v[192:195], a[204:207]
ds_read_b64_tr_b16 v[56:57], v243 offset:33920
ds_read_b64_tr_b16 v[58:59], v243 offset:34944
v_mfma_f32_16x16x32_bf16 a[88:91], v[16:19], v[192:195], a[88:91]
v_lshlrev_b32 v104, 16, v80
v_and_b32 v105, 0xffff0000, v80
v_mfma_f32_16x16x32_bf16 a[88:91], v[20:23], v[196:199], a[88:91]
v_sub_f32 v104, v104, v70
v_sub_f32 v105, v105, v70
v_mfma_f32_16x16x32_bf16 a[24:27], v[20:23], v[204:207], a[24:27]
v_add_f32 v124, v104, v105
v_fmac_f32 v122, v104, v104
v_mfma_f32_16x16x32_bf16 a[24:27], v[16:19], v[200:203], a[24:27]
ds_read_b64_tr_b16 v[60:61], v243 offset:50816
ds_read_b64_tr_b16 v[62:63], v243 offset:51840
v_mfma_f32_16x16x32_bf16 a[240:243], v[16:19], v[208:211], a[240:243]
v_add_f32 v120, v120, v124
v_fmac_f32 v123, v105, v105
v_mfma_f32_16x16x32_bf16 a[240:243], v[20:23], v[212:215], a[240:243]
v_lshlrev_b32 v106, 16, v81
v_and_b32 v107, 0xffff0000, v81
v_mfma_f32_16x16x32_bf16 a[32:35], v[20:23], v[220:223], a[32:35]
v_sub_f32 v106, v106, v70
v_sub_f32 v107, v107, v70
v_mfma_f32_16x16x32_bf16 a[32:35], v[16:19], v[216:219], a[32:35]
v_add_f32 v124, v106, v107
v_fmac_f32 v122, v106, v106
v_mfma_f32_16x16x32_bf16 a[36:39], v[24:27], v[216:219], a[36:39]
v_add_f32 v120, v120, v124
v_fmac_f32 v123, v107, v107
v_mfma_f32_16x16x32_bf16 a[36:39], v[28:31], v[220:223], a[36:39]
v_lshlrev_b32 v108, 16, v82
v_and_b32 v109, 0xffff0000, v82
v_mfma_f32_16x16x32_bf16 a[244:247], v[28:31], v[212:215], a[244:247]
v_sub_f32 v108, v108, v70
v_sub_f32 v109, v109, v70
v_mfma_f32_16x16x32_bf16 a[244:247], v[24:27], v[208:211], a[244:247]
v_add_f32 v124, v108, v109
v_fmac_f32 v122, v108, v108
v_mfma_f32_16x16x32_bf16 a[28:31], v[24:27], v[200:203], a[28:31]
v_add_f32 v120, v120, v124
v_fmac_f32 v123, v109, v109
v_mfma_f32_16x16x32_bf16 a[28:31], v[28:31], v[204:207], a[28:31]
v_lshlrev_b32 v110, 16, v83
v_and_b32 v111, 0xffff0000, v83
v_mfma_f32_16x16x32_bf16 a[92:95], v[28:31], v[196:199], a[92:95]
s_waitcnt vmcnt(9) lgkmcnt(0)
s_barrier
v_mfma_f32_16x16x32_bf16 a[92:95], v[24:27], v[192:195], a[92:95]
v_sub_f32 v110, v110, v70
v_sub_f32 v111, v111, v70
v_mfma_f32_16x16x32_bf16 a[120:123], v[160:163], v[32:35], a[120:123]
v_add_f32 v124, v110, v111
v_fmac_f32 v122, v110, v110
v_mfma_f32_16x16x32_bf16 a[120:123], v[164:167], v[36:39], a[120:123]
v_add_f32 v120, v120, v124
v_fmac_f32 v123, v111, v111
v_mfma_f32_16x16x32_bf16 a[136:139], v[164:167], v[44:47], a[136:139]
ds_read_b64_tr_b16 v[192:193], v240 offset:0
ds_read_b64_tr_b16 v[194:195], v240 offset:1024
v_mfma_f32_16x16x32_bf16 a[136:139], v[160:163], v[40:43], a[136:139]
ds_read_b64_tr_b16 v[196:197], v240 offset:16896
ds_read_b64_tr_b16 v[198:199], v240 offset:17920
v_mfma_f32_16x16x32_bf16 a[224:227], v[160:163], v[48:51], a[224:227]
v_lshlrev_b32 v112, 16, v84
v_and_b32 v113, 0xffff0000, v84
v_mfma_f32_16x16x32_bf16 a[224:227], v[164:167], v[52:55], a[224:227]
v_sub_f32 v112, v112, v70
v_sub_f32 v113, v113, v70
v_mfma_f32_16x16x32_bf16 a[144:147], v[164:167], v[60:63], a[144:147]
v_add_f32 v124, v112, v113
v_fmac_f32 v122, v112, v112
v_mfma_f32_16x16x32_bf16 a[144:147], v[160:163], v[56:59], a[144:147]
ds_read_b128 v[160:163], v236 offset:24576
ds_read_b128 v[164:167], v237 offset:24576
v_mfma_f32_16x16x32_bf16 a[148:151], v[168:171], v[56:59], a[148:151]
v_add_f32 v120, v120, v124
v_fmac_f32 v123, v113, v113
v_mfma_f32_16x16x32_bf16 a[148:151], v[172:175], v[60:63], a[148:151]
v_lshlrev_b32 v114, 16, v85
v_and_b32 v115, 0xffff0000, v85
v_mfma_f32_16x16x32_bf16 a[228:231], v[172:175], v[52:55], a[228:231]
v_sub_f32 v114, v114, v70
v_sub_f32 v115, v115, v70
v_mfma_f32_16x16x32_bf16 a[228:231], v[168:171], v[48:51], a[228:231]
ds_read_b64_tr_b16 v[200:201], v241 offset:0
ds_read_b64_tr_b16 v[202:203], v241 offset:1024
v_mfma_f32_16x16x32_bf16 a[140:143], v[168:171], v[40:43], a[140:143]
v_add_f32 v124, v114, v115
v_fmac_f32 v122, v114, v114
v_mfma_f32_16x16x32_bf16 a[140:143], v[172:175], v[44:47], a[140:143]
v_add_f32 v120, v120, v124
v_fmac_f32 v123, v115, v115
v_mfma_f32_16x16x32_bf16 a[124:127], v[172:175], v[36:39], a[124:127]
v_lshlrev_b32 v116, 16, v86
v_and_b32 v117, 0xffff0000, v86
v_mfma_f32_16x16x32_bf16 a[124:127], v[168:171], v[32:35], a[124:127]
ds_read_b128 v[168:171], v236 offset:26624
ds_read_b128 v[172:175], v237 offset:26624
v_mfma_f32_16x16x32_bf16 a[216:219], v[176:179], v[32:35], a[216:219]
v_sub_f32 v116, v116, v70
v_sub_f32 v117, v117, v70
v_mfma_f32_16x16x32_bf16 a[216:219], v[180:183], v[36:39], a[216:219]
v_add_f32 v124, v116, v117
v_fmac_f32 v122, v116, v116
v_mfma_f32_16x16x32_bf16 a[168:171], v[180:183], v[44:47], a[168:171]
v_add_f32 v120, v120, v124
v_fmac_f32 v123, v117, v117
v_mfma_f32_16x16x32_bf16 a[168:171], v[176:179], v[40:43], a[168:171]
ds_read_b64_tr_b16 v[204:205], v241 offset:16896
ds_read_b64_tr_b16 v[206:207], v241 offset:17920
v_mfma_f32_16x16x32_bf16 a[192:195], v[176:179], v[48:51], a[192:195]
v_lshlrev_b32 v118, 16, v87
v_and_b32 v119, 0xffff0000, v87
v_mfma_f32_16x16x32_bf16 a[192:195], v[180:183], v[52:55], a[192:195]
v_sub_f32 v118, v118, v70
v_sub_f32 v119, v119, v70
v_mfma_f32_16x16x32_bf16 a[176:179], v[180:183], v[60:63], a[176:179]
v_add_f32 v124, v118, v119
v_fmac_f32 v122, v118, v118
v_mfma_f32_16x16x32_bf16 a[176:179], v[176:179], v[56:59], a[176:179]
ds_read_b128 v[176:179], v236 offset:16384
ds_read_b128 v[180:183], v237 offset:16384
v_mfma_f32_16x16x32_bf16 a[180:183], v[184:187], v[56:59], a[180:183]
v_add_f32 v120, v120, v124
v_fmac_f32 v123, v119, v119
v_mfma_f32_16x16x32_bf16 a[180:183], v[188:191], v[60:63], a[180:183]
v_add_f32 v121, v122, v123
v_mfma_f32_16x16x32_bf16 a[196:199], v[188:191], v[52:55], a[196:199]
v_add_f32_dpp v120, v120, v120 row_ror:8 row_mask:0xf bank_mask:0xf
v_mfma_f32_16x16x32_bf16 a[196:199], v[184:187], v[48:51], a[196:199]
ds_read_b64_tr_b16 v[208:209], v242 offset:0
ds_read_b64_tr_b16 v[210:211], v242 offset:1024
v_mfma_f32_16x16x32_bf16 a[172:175], v[184:187], v[40:43], a[172:175]
s_add_u32 m0, s83, 33792
v_add_f32_dpp v121, v121, v121 row_ror:8 row_mask:0xf bank_mask:0xf
v_mfma_f32_16x16x32_bf16 a[172:175], v[188:191], v[44:47], a[172:175]
buffer_load_dwordx4 v232, s[44:47], s80 offen lds
v_mfma_f32_16x16x32_bf16 a[220:223], v[188:191], v[36:39], a[220:223]
v_mov_b32 v124, v120
v_mov_b32 v125, v121
v_mfma_f32_16x16x32_bf16 a[220:223], v[184:187], v[32:35], a[220:223]
ds_read_b128 v[184:187], v236 offset:18432
ds_read_b128 v[188:191], v237 offset:18432
v_mfma_f32_16x16x32_bf16 a[72:75], v[0:3], v[32:35], a[72:75]
s_add_u32 m0, s83, 34816
v_permlane16_swap_b32 v124, v120
v_mfma_f32_16x16x32_bf16 a[72:75], v[4:7], v[36:39], a[72:75]
buffer_load_dwordx4 v233, s[44:47], s80 offen lds
v_mfma_f32_16x16x32_bf16 a[8:11], v[4:7], v[44:47], a[8:11]
v_permlane16_swap_b32 v125, v121
v_mfma_f32_16x16x32_bf16 a[8:11], v[0:3], v[40:43], a[8:11]
ds_read_b64_tr_b16 v[212:213], v242 offset:16896
ds_read_b64_tr_b16 v[214:215], v242 offset:17920
v_mfma_f32_16x16x32_bf16 a[80:83], v[0:3], v[48:51], a[80:83]
s_add_u32 m0, s83, 35968
v_add_f32 v120, v120, v124
v_mfma_f32_16x16x32_bf16 a[80:83], v[4:7], v[52:55], a[80:83]
buffer_load_dwordx4 v234, s[44:47], s80 offen lds
v_mfma_f32_16x16x32_bf16 a[16:19], v[4:7], v[60:63], a[16:19]
v_add_f32 v121, v121, v125
v_mov_b32 v124, v120
v_mfma_f32_16x16x32_bf16 a[16:19], v[0:3], v[56:59], a[16:19]
ds_read_b128 v[0:3], v236 offset:8192
ds_read_b128 v[4:7], v237 offset:8192
v_mfma_f32_16x16x32_bf16 a[20:23], v[8:11], v[56:59], a[20:23]
s_add_u32 m0, s83, 36992
v_mov_b32 v125, v121
v_mfma_f32_16x16x32_bf16 a[20:23], v[12:15], v[60:63], a[20:23]
buffer_load_dwordx4 v235, s[44:47], s80 offen lds
v_mfma_f32_16x16x32_bf16 a[84:87], v[12:15], v[52:55], a[84:87]
s_mov_b32 s80, 3932160
v_permlane32_swap_b32 v124, v120
v_mfma_f32_16x16x32_bf16 a[84:87], v[8:11], v[48:51], a[84:87]
ds_read_b64_tr_b16 v[216:217], v243 offset:0
ds_read_b64_tr_b16 v[218:219], v243 offset:1024
v_mfma_f32_16x16x32_bf16 a[12:15], v[8:11], v[40:43], a[12:15]
s_add_u32 m0, s83, 50688
v_permlane32_swap_b32 v125, v121
v_mfma_f32_16x16x32_bf16 a[12:15], v[12:15], v[44:47], a[12:15]
buffer_load_dwordx4 v232, s[44:47], s80 offen lds
v_mfma_f32_16x16x32_bf16 a[76:79], v[12:15], v[36:39], a[76:79]
v_add_f32 v120, v120, v124
v_add_f32 v121, v121, v125
v_mfma_f32_16x16x32_bf16 a[76:79], v[8:11], v[32:35], a[76:79]
ds_read_b128 v[8:11], v236 offset:10240
ds_read_b128 v[12:15], v237 offset:10240
v_mfma_f32_16x16x32_bf16 a[248:251], v[16:19], v[32:35], a[248:251]
s_add_u32 m0, s83, 51712
ds_write_b64 v127, v[120:121]
v_mfma_f32_16x16x32_bf16 a[248:251], v[20:23], v[36:39], a[248:251]
buffer_load_dwordx4 v233, s[44:47], s80 offen lds
v_mfma_f32_16x16x32_bf16 a[40:43], v[20:23], v[44:47], a[40:43]
s_nop 0
v_readfirstlane_b32 s92, v252
v_mfma_f32_16x16x32_bf16 a[40:43], v[16:19], v[40:43], a[40:43]
ds_read_b64_tr_b16 v[220:221], v243 offset:16896
ds_read_b64_tr_b16 v[222:223], v243 offset:17920
v_mfma_f32_16x16x32_bf16 a[48:51], v[16:19], v[48:51], a[48:51]
s_add_u32 m0, s83, 52864
v_readfirstlane_b32 s93, v253
v_mfma_f32_16x16x32_bf16 a[48:51], v[20:23], v[52:55], a[48:51]
buffer_load_dwordx4 v234, s[44:47], s80 offen lds
v_mfma_f32_16x16x32_bf16 a[56:59], v[20:23], v[60:63], a[56:59]
s_add_u32 s92, s92, 1
s_addc_u32 s93, s93, 0
v_mfma_f32_16x16x32_bf16 a[56:59], v[16:19], v[56:59], a[56:59]
ds_read_b128 v[16:19], v236 offset:0
ds_read_b128 v[20:23], v237 offset:0
v_mfma_f32_16x16x32_bf16 a[60:63], v[24:27], v[56:59], a[60:63]
s_add_u32 m0, s83, 53888
v_mov_b32 v252, s92
v_mfma_f32_16x16x32_bf16 a[60:63], v[28:31], v[60:63], a[60:63]
buffer_load_dwordx4 v235, s[44:47], s80 offen lds
v_mfma_f32_16x16x32_bf16 a[52:55], v[28:31], v[52:55], a[52:55]
s_mov_b32 s80, 0
v_mov_b32 v253, s93
v_mfma_f32_16x16x32_bf16 a[52:55], v[24:27], v[48:51], a[52:55]
v_mov_b32 v128, 0
v_mov_b32 v129, 0
v_mfma_f32_16x16x32_bf16 a[44:47], v[24:27], v[40:43], a[44:47]
v_mov_b32 v130, 0
v_mov_b32 v131, 0
v_mfma_f32_16x16x32_bf16 a[44:47], v[28:31], v[44:47], a[44:47]
buffer_store_dwordx4 v[128:131], v255, s[52:55], s95 offen sc1
v_mfma_f32_16x16x32_bf16 a[252:255], v[28:31], v[36:39], a[252:255]
s_waitcnt lgkmcnt(2)
v_mfma_f32_16x16x32_bf16 a[252:255], v[24:27], v[32:35], a[252:255]
ds_read_b128 v[24:27], v236 offset:2048
ds_read_b128 v[28:31], v237 offset:2048
v_mfma_f32_16x16x32_bf16 a[96:99], v[160:163], v[192:195], a[96:99]
v_mfma_f32_16x16x32_bf16 a[96:99], v[164:167], v[196:199], a[96:99]
v_mfma_f32_16x16x32_bf16 a[104:107], v[164:167], v[204:207], a[104:107]
v_mfma_f32_16x16x32_bf16 a[104:107], v[160:163], v[200:203], a[104:107]
ds_read_b64_tr_b16 v[32:33], v240 offset:128
ds_read_b64_tr_b16 v[34:35], v240 offset:1152
v_mfma_f32_16x16x32_bf16 a[112:115], v[160:163], v[208:211], a[112:115]
v_mfma_f32_16x16x32_bf16 a[112:115], v[164:167], v[212:215], a[112:115]
v_mfma_f32_16x16x32_bf16 a[128:131], v[164:167], v[220:223], a[128:131]
s_waitcnt lgkmcnt(2)
s_barrier
v_mfma_f32_16x16x32_bf16 a[128:131], v[160:163], v[216:219], a[128:131]
ds_read_b128 v[72:75], v126
ds_read_b128 v[76:79], v126 offset:16
v_mfma_f32_16x16x32_bf16 a[132:135], v[168:171], v[216:219], a[132:135]
s_xor_b32 m0, s82, 0
v_mfma_f32_16x16x32_bf16 a[132:135], v[172:175], v[220:223], a[132:135]
buffer_load_dwordx4 v224, s[40:43], s79 offen lds
v_mfma_f32_16x16x32_bf16 a[116:119], v[172:175], v[212:215], a[116:119]
v_mfma_f32_16x16x32_bf16 a[116:119], v[168:171], v[208:211], a[116:119]
ds_read_b64_tr_b16 v[36:37], v240 offset:17024
ds_read_b64_tr_b16 v[38:39], v240 offset:18048
v_mfma_f32_16x16x32_bf16 a[108:111], v[168:171], v[200:203], a[108:111]
s_xor_b32 m0, s82, 1024
v_mfma_f32_16x16x32_bf16 a[108:111], v[172:175], v[204:207], a[108:111]
buffer_load_dwordx4 v225, s[40:43], s79 offen lds
v_mfma_f32_16x16x32_bf16 a[100:103], v[172:175], v[196:199], a[100:103]
s_waitcnt lgkmcnt(2)
v_add_f32 v72, v72, v74
v_mfma_f32_16x16x32_bf16 a[100:103], v[168:171], v[192:195], a[100:103]
v_add_f32 v73, v73, v75
v_add_f32 v76, v76, v78
v_mfma_f32_16x16x32_bf16 a[232:235], v[176:179], v[192:195], a[232:235]
s_xor_b32 m0, s82, 2048
v_add_f32 v77, v77, v79
v_mfma_f32_16x16x32_bf16 a[232:235], v[180:183], v[196:199], a[232:235]
buffer_load_dwordx4 v226, s[40:43], s79 offen lds
v_mfma_f32_16x16x32_bf16 a[152:155], v[180:183], v[204:207], a[152:155]
v_add_f32 v72, v72, v76
v_add_f32 v73, v73, v77
v_mfma_f32_16x16x32_bf16 a[152:155], v[176:179], v[200:203], a[152:155]
ds_read_b64_tr_b16 v[40:41], v241 offset:128
ds_read_b64_tr_b16 v[42:43], v241 offset:1152
v_mfma_f32_16x16x32_bf16 a[208:211], v[176:179], v[208:211], a[208:211]
s_xor_b32 m0, s82, 3072
v_mul_f32 v72, 0x3a800000, v72
v_mfma_f32_16x16x32_bf16 a[208:211], v[180:183], v[212:215], a[208:211]
buffer_load_dwordx4 v227, s[40:43], s79 offen lds
v_mfma_f32_16x16x32_bf16 a[160:163], v[180:183], v[220:223], a[160:163]
v_mul_f32 v73, 0x3a800000, v73
v_add_f32 v124, v70, v72
v_mfma_f32_16x16x32_bf16 a[160:163], v[176:179], v[216:219], a[160:163]
v_fma_f32 v125, -v72, v72, v73
v_cvt_pk_bf16_f32 v124, v124, v124
v_mfma_f32_16x16x32_bf16 a[164:167], v[184:187], v[216:219], a[164:167]
s_xor_b32 m0, s82, 4096
v_lshlrev_b32 v124, 16, v124
v_mfma_f32_16x16x32_bf16 a[164:167], v[188:191], v[220:223], a[164:167]
buffer_load_dwordx4 v228, s[40:43], s79 offen lds
v_mfma_f32_16x16x32_bf16 a[212:215], v[188:191], v[212:215], a[212:215]
v_max_f32 v125, 0, v125
v_cvt_pk_bf16_f32 v125, v125, v125
v_mfma_f32_16x16x32_bf16 a[212:215], v[184:187], v[208:211], a[212:215]
ds_read_b64_tr_b16 v[44:45], v241 offset:17024
ds_read_b64_tr_b16 v[46:47], v241 offset:18048
v_mfma_f32_16x16x32_bf16 a[156:159], v[184:187], v[200:203], a[156:159]
s_xor_b32 m0, s82, 5120
v_lshlrev_b32 v125, 16, v125
v_mfma_f32_16x16x32_bf16 a[156:159], v[188:191], v[204:207], a[156:159]
buffer_load_dwordx4 v229, s[40:43], s79 offen lds
v_mfma_f32_16x16x32_bf16 a[236:239], v[188:191], v[196:199], a[236:239]
v_add_f32 v125, 0x3727c5ac, v125
v_cvt_pk_bf16_f32 v125, v125, v125
v_mfma_f32_16x16x32_bf16 a[236:239], v[184:187], v[192:195], a[236:239]
v_lshlrev_b32 v125, 16, v125
v_mfma_f32_16x16x32_bf16 a[200:203], v[0:3], v[192:195], a[200:203]
s_xor_b32 m0, s82, 6144
v_mfma_f32_16x16x32_bf16 a[200:203], v[4:7], v[196:199], a[200:203]
buffer_load_dwordx4 v230, s[40:43], s79 offen lds
v_mfma_f32_16x16x32_bf16 a[184:187], v[4:7], v[204:207], a[184:187]
v_sqrt_f32 v125, v125
v_mfma_f32_16x16x32_bf16 a[184:187], v[0:3], v[200:203], a[184:187]
ds_read_b64_tr_b16 v[48:49], v242 offset:128
ds_read_b64_tr_b16 v[50:51], v242 offset:1152
v_mfma_f32_16x16x32_bf16 a[64:67], v[0:3], v[208:211], a[64:67]
s_xor_b32 m0, s82, 7168
v_sub_f32 v124, v70, v124
v_mfma_f32_16x16x32_bf16 a[64:67], v[4:7], v[212:215], a[64:67]
buffer_load_dwordx4 v231, s[40:43], s79 offen lds
v_mfma_f32_16x16x32_bf16 a[0:3], v[4:7], v[220:223], a[0:3]
s_mov_b32 s79, 128
v_cvt_pk_bf16_f32 v125, v125, v125
v_mfma_f32_16x16x32_bf16 a[0:3], v[0:3], v[216:219], a[0:3]
v_lshlrev_b32 v125, 16, v125
v_mfma_f32_16x16x32_bf16 a[4:7], v[8:11], v[216:219], a[4:7]
v_rcp_f32 v125, v125
v_mfma_f32_16x16x32_bf16 a[4:7], v[12:15], v[220:223], a[4:7]
v_mul_f32 v125, 0x3e0293ee, v125
v_mul_f32 v124, v124, v125
v_mfma_f32_16x16x32_bf16 a[68:71], v[12:15], v[212:215], a[68:71]
v_fma_f32 v88, v88, v125, v124
v_fma_f32 v89, v89, v125, v124
v_mfma_f32_16x16x32_bf16 a[68:71], v[8:11], v[208:211], a[68:71]
ds_read_b64_tr_b16 v[52:53], v242 offset:17024
ds_read_b64_tr_b16 v[54:55], v242 offset:18048
v_mfma_f32_16x16x32_bf16 a[188:191], v[8:11], v[200:203], a[188:191]
v_cvt_pk_bf16_f32 v72, v88, v89
v_fma_f32 v90, v90, v125, v124
v_mfma_f32_16x16x32_bf16 a[188:191], v[12:15], v[204:207], a[188:191]
v_fma_f32 v91, v91, v125, v124
v_cvt_pk_bf16_f32 v73, v90, v91
v_mfma_f32_16x16x32_bf16 a[204:207], v[12:15], v[196:199], a[204:207]
v_fma_f32 v92, v92, v125, v124
v_fma_f32 v93, v93, v125, v124
v_mfma_f32_16x16x32_bf16 a[204:207], v[8:11], v[192:195], a[204:207]
ds_read_b64_tr_b16 v[56:57], v243 offset:128
ds_read_b64_tr_b16 v[58:59], v243 offset:1152
v_mfma_f32_16x16x32_bf16 a[88:91], v[16:19], v[192:195], a[88:91]
v_cvt_pk_bf16_f32 v74, v92, v93
v_fma_f32 v94, v94, v125, v124
v_mfma_f32_16x16x32_bf16 a[88:91], v[20:23], v[196:199], a[88:91]
v_fma_f32 v95, v95, v125, v124
v_cvt_pk_bf16_f32 v75, v94, v95
v_mfma_f32_16x16x32_bf16 a[24:27], v[20:23], v[204:207], a[24:27]
buffer_store_dwordx4 v[72:75], v71, s[56:59], 0 offen offset:0 sc1
v_mfma_f32_16x16x32_bf16 a[24:27], v[16:19], v[200:203], a[24:27]
ds_read_b64_tr_b16 v[60:61], v243 offset:17024
ds_read_b64_tr_b16 v[62:63], v243 offset:18048
v_mfma_f32_16x16x32_bf16 a[240:243], v[16:19], v[208:211], a[240:243]
v_fma_f32 v96, v96, v125, v124
v_fma_f32 v97, v97, v125, v124
v_mfma_f32_16x16x32_bf16 a[240:243], v[20:23], v[212:215], a[240:243]
v_cvt_pk_bf16_f32 v76, v96, v97
v_fma_f32 v98, v98, v125, v124
v_mfma_f32_16x16x32_bf16 a[32:35], v[20:23], v[220:223], a[32:35]
v_fma_f32 v99, v99, v125, v124
v_cvt_pk_bf16_f32 v77, v98, v99
v_mfma_f32_16x16x32_bf16 a[32:35], v[16:19], v[216:219], a[32:35]
v_fma_f32 v100, v100, v125, v124
v_fma_f32 v101, v101, v125, v124
v_mfma_f32_16x16x32_bf16 a[36:39], v[24:27], v[216:219], a[36:39]
v_cvt_pk_bf16_f32 v78, v100, v101
v_fma_f32 v102, v102, v125, v124
v_mfma_f32_16x16x32_bf16 a[36:39], v[28:31], v[220:223], a[36:39]
v_fma_f32 v103, v103, v125, v124
v_cvt_pk_bf16_f32 v79, v102, v103
v_mfma_f32_16x16x32_bf16 a[244:247], v[28:31], v[212:215], a[244:247]
buffer_store_dwordx4 v[76:79], v71, s[56:59], 0 offen offset:2048 sc1
v_mfma_f32_16x16x32_bf16 a[244:247], v[24:27], v[208:211], a[244:247]
v_fma_f32 v104, v104, v125, v124
v_fma_f32 v105, v105, v125, v124
v_mfma_f32_16x16x32_bf16 a[28:31], v[24:27], v[200:203], a[28:31]
v_cvt_pk_bf16_f32 v80, v104, v105
v_fma_f32 v106, v106, v125, v124
v_mfma_f32_16x16x32_bf16 a[28:31], v[28:31], v[204:207], a[28:31]
v_fma_f32 v107, v107, v125, v124
v_cvt_pk_bf16_f32 v81, v106, v107
v_mfma_f32_16x16x32_bf16 a[92:95], v[28:31], v[196:199], a[92:95]
s_waitcnt vmcnt(11) lgkmcnt(0)
s_barrier
v_mfma_f32_16x16x32_bf16 a[92:95], v[24:27], v[192:195], a[92:95]
v_fma_f32 v108, v108, v125, v124
v_fma_f32 v109, v109, v125, v124
v_mfma_f32_16x16x32_bf16 a[120:123], v[160:163], v[32:35], a[120:123]
v_cvt_pk_bf16_f32 v82, v108, v109
v_fma_f32 v110, v110, v125, v124
v_mfma_f32_16x16x32_bf16 a[120:123], v[164:167], v[36:39], a[120:123]
v_fma_f32 v111, v111, v125, v124
v_cvt_pk_bf16_f32 v83, v110, v111
v_mfma_f32_16x16x32_bf16 a[136:139], v[164:167], v[44:47], a[136:139]
ds_read_b64_tr_b16 v[192:193], v240 offset:33792
ds_read_b64_tr_b16 v[194:195], v240 offset:34816
v_mfma_f32_16x16x32_bf16 a[136:139], v[160:163], v[40:43], a[136:139]
ds_read_b64_tr_b16 v[196:197], v240 offset:50688
ds_read_b64_tr_b16 v[198:199], v240 offset:51712
v_mfma_f32_16x16x32_bf16 a[224:227], v[160:163], v[48:51], a[224:227]
buffer_store_dwordx4 v[80:83], v71, s[56:59], s88 offen offset:0 sc1
v_mfma_f32_16x16x32_bf16 a[224:227], v[164:167], v[52:55], a[224:227]
v_fma_f32 v112, v112, v125, v124
v_fma_f32 v113, v113, v125, v124
v_mfma_f32_16x16x32_bf16 a[144:147], v[164:167], v[60:63], a[144:147]
v_cvt_pk_bf16_f32 v84, v112, v113
v_fma_f32 v114, v114, v125, v124
v_mfma_f32_16x16x32_bf16 a[144:147], v[160:163], v[56:59], a[144:147]
ds_read_b128 v[160:163], v236 offset:57344
ds_read_b128 v[164:167], v237 offset:57344
v_mfma_f32_16x16x32_bf16 a[148:151], v[168:171], v[56:59], a[148:151]
v_fma_f32 v115, v115, v125, v124
v_cvt_pk_bf16_f32 v85, v114, v115
v_mfma_f32_16x16x32_bf16 a[148:151], v[172:175], v[60:63], a[148:151]
v_fma_f32 v116, v116, v125, v124
v_fma_f32 v117, v117, v125, v124
v_mfma_f32_16x16x32_bf16 a[228:231], v[172:175], v[52:55], a[228:231]
v_cvt_pk_bf16_f32 v86, v116, v117
v_fma_f32 v118, v118, v125, v124
v_mfma_f32_16x16x32_bf16 a[228:231], v[168:171], v[48:51], a[228:231]
ds_read_b64_tr_b16 v[200:201], v241 offset:33792
ds_read_b64_tr_b16 v[202:203], v241 offset:34816
v_mfma_f32_16x16x32_bf16 a[140:143], v[168:171], v[40:43], a[140:143]
v_fma_f32 v119, v119, v125, v124
v_cvt_pk_bf16_f32 v87, v118, v119
v_mfma_f32_16x16x32_bf16 a[140:143], v[172:175], v[44:47], a[140:143]
buffer_store_dwordx4 v[84:87], v71, s[56:59], s88 offen offset:2048 sc1
v_mfma_f32_16x16x32_bf16 a[124:127], v[172:175], v[36:39], a[124:127]
v_mfma_f32_16x16x32_bf16 a[124:127], v[168:171], v[32:35], a[124:127]
ds_read_b128 v[168:171], v236 offset:59392
ds_read_b128 v[172:175], v237 offset:59392
v_mfma_f32_16x16x32_bf16 a[216:219], v[176:179], v[32:35], a[216:219]
v_mfma_f32_16x16x32_bf16 a[216:219], v[180:183], v[36:39], a[216:219]
v_mfma_f32_16x16x32_bf16 a[168:171], v[180:183], v[44:47], a[168:171]
v_mfma_f32_16x16x32_bf16 a[168:171], v[176:179], v[40:43], a[168:171]
ds_read_b64_tr_b16 v[204:205], v241 offset:50688
ds_read_b64_tr_b16 v[206:207], v241 offset:51712
v_mfma_f32_16x16x32_bf16 a[192:195], v[176:179], v[48:51], a[192:195]
v_mfma_f32_16x16x32_bf16 a[192:195], v[180:183], v[52:55], a[192:195]
v_mfma_f32_16x16x32_bf16 a[176:179], v[180:183], v[60:63], a[176:179]
v_mfma_f32_16x16x32_bf16 a[176:179], v[176:179], v[56:59], a[176:179]
ds_read_b128 v[176:179], v236 offset:49152
ds_read_b128 v[180:183], v237 offset:49152
v_mfma_f32_16x16x32_bf16 a[180:183], v[184:187], v[56:59], a[180:183]
v_mfma_f32_16x16x32_bf16 a[180:183], v[188:191], v[60:63], a[180:183]
v_mfma_f32_16x16x32_bf16 a[196:199], v[188:191], v[52:55], a[196:199]
v_mfma_f32_16x16x32_bf16 a[196:199], v[184:187], v[48:51], a[196:199]
ds_read_b64_tr_b16 v[208:209], v242 offset:33792
ds_read_b64_tr_b16 v[210:211], v242 offset:34816
v_mfma_f32_16x16x32_bf16 a[172:175], v[184:187], v[40:43], a[172:175]
s_add_u32 m0, s83, 0
v_mfma_f32_16x16x32_bf16 a[172:175], v[188:191], v[44:47], a[172:175]
buffer_load_dwordx4 v232, s[44:47], s80 offen lds
v_mfma_f32_16x16x32_bf16 a[220:223], v[188:191], v[36:39], a[220:223]
v_mfma_f32_16x16x32_bf16 a[220:223], v[184:187], v[32:35], a[220:223]
ds_read_b128 v[184:187], v236 offset:51200
ds_read_b128 v[188:191], v237 offset:51200
v_mfma_f32_16x16x32_bf16 a[72:75], v[0:3], v[32:35], a[72:75]
s_add_u32 m0, s83, 1024
v_mfma_f32_16x16x32_bf16 a[72:75], v[4:7], v[36:39], a[72:75]
buffer_load_dwordx4 v233, s[44:47], s80 offen lds
v_mfma_f32_16x16x32_bf16 a[8:11], v[4:7], v[44:47], a[8:11]
v_mfma_f32_16x16x32_bf16 a[8:11], v[0:3], v[40:43], a[8:11]
ds_read_b64_tr_b16 v[212:213], v242 offset:50688
ds_read_b64_tr_b16 v[214:215], v242 offset:51712
v_mfma_f32_16x16x32_bf16 a[80:83], v[0:3], v[48:51], a[80:83]
s_add_u32 m0, s83, 2176
v_mfma_f32_16x16x32_bf16 a[80:83], v[4:7], v[52:55], a[80:83]
buffer_load_dwordx4 v234, s[44:47], s80 offen lds
v_mfma_f32_16x16x32_bf16 a[16:19], v[4:7], v[60:63], a[16:19]
v_mfma_f32_16x16x32_bf16 a[16:19], v[0:3], v[56:59], a[16:19]
ds_read_b128 v[0:3], v236 offset:40960
ds_read_b128 v[4:7], v237 offset:40960
v_mfma_f32_16x16x32_bf16 a[20:23], v[8:11], v[56:59], a[20:23]
s_add_u32 m0, s83, 3200
v_mfma_f32_16x16x32_bf16 a[20:23], v[12:15], v[60:63], a[20:23]
buffer_load_dwordx4 v235, s[44:47], s80 offen lds
v_mfma_f32_16x16x32_bf16 a[84:87], v[12:15], v[52:55], a[84:87]
s_mov_b32 s80, 262144
v_mfma_f32_16x16x32_bf16 a[84:87], v[8:11], v[48:51], a[84:87]
ds_read_b64_tr_b16 v[216:217], v243 offset:33792
ds_read_b64_tr_b16 v[218:219], v243 offset:34816
v_mfma_f32_16x16x32_bf16 a[12:15], v[8:11], v[40:43], a[12:15]
s_add_u32 m0, s83, 16896
v_mfma_f32_16x16x32_bf16 a[12:15], v[12:15], v[44:47], a[12:15]
buffer_load_dwordx4 v232, s[44:47], s80 offen lds
v_mfma_f32_16x16x32_bf16 a[76:79], v[12:15], v[36:39], a[76:79]
v_mfma_f32_16x16x32_bf16 a[76:79], v[8:11], v[32:35], a[76:79]
ds_read_b128 v[8:11], v236 offset:43008
ds_read_b128 v[12:15], v237 offset:43008
v_mfma_f32_16x16x32_bf16 a[248:251], v[16:19], v[32:35], a[248:251]
s_add_u32 m0, s83, 17920
v_mfma_f32_16x16x32_bf16 a[248:251], v[20:23], v[36:39], a[248:251]
buffer_load_dwordx4 v233, s[44:47], s80 offen lds
v_mfma_f32_16x16x32_bf16 a[40:43], v[20:23], v[44:47], a[40:43]
v_mfma_f32_16x16x32_bf16 a[40:43], v[16:19], v[40:43], a[40:43]
ds_read_b64_tr_b16 v[220:221], v243 offset:50688
ds_read_b64_tr_b16 v[222:223], v243 offset:51712
v_mfma_f32_16x16x32_bf16 a[48:51], v[16:19], v[48:51], a[48:51]
s_add_u32 m0, s83, 19072
v_mfma_f32_16x16x32_bf16 a[48:51], v[20:23], v[52:55], a[48:51]
buffer_load_dwordx4 v234, s[44:47], s80 offen lds
v_mfma_f32_16x16x32_bf16 a[56:59], v[20:23], v[60:63], a[56:59]
v_mfma_f32_16x16x32_bf16 a[56:59], v[16:19], v[56:59], a[56:59]
ds_read_b128 v[16:19], v236 offset:32768
ds_read_b128 v[20:23], v237 offset:32768
v_mfma_f32_16x16x32_bf16 a[60:63], v[24:27], v[56:59], a[60:63]
s_add_u32 m0, s83, 20096
v_mfma_f32_16x16x32_bf16 a[60:63], v[28:31], v[60:63], a[60:63]
buffer_load_dwordx4 v235, s[44:47], s80 offen lds
v_mfma_f32_16x16x32_bf16 a[52:55], v[28:31], v[52:55], a[52:55]
s_mov_b32 s80, 524288
v_mfma_f32_16x16x32_bf16 a[52:55], v[24:27], v[48:51], a[52:55]
v_mfma_f32_16x16x32_bf16 a[44:47], v[24:27], v[40:43], a[44:47]
v_mfma_f32_16x16x32_bf16 a[44:47], v[28:31], v[44:47], a[44:47]
v_mfma_f32_16x16x32_bf16 a[252:255], v[28:31], v[36:39], a[252:255]
s_waitcnt lgkmcnt(2)
v_mfma_f32_16x16x32_bf16 a[252:255], v[24:27], v[32:35], a[252:255]
ds_read_b128 v[24:27], v236 offset:34816
ds_read_b128 v[28:31], v237 offset:34816
v_mfma_f32_16x16x32_bf16 a[96:99], v[160:163], v[192:195], a[96:99]
v_mfma_f32_16x16x32_bf16 a[96:99], v[164:167], v[196:199], a[96:99]
v_mfma_f32_16x16x32_bf16 a[104:107], v[164:167], v[204:207], a[104:107]
v_mfma_f32_16x16x32_bf16 a[104:107], v[160:163], v[200:203], a[104:107]
ds_read_b64_tr_b16 v[32:33], v240 offset:33920
ds_read_b64_tr_b16 v[34:35], v240 offset:34944
v_mfma_f32_16x16x32_bf16 a[112:115], v[160:163], v[208:211], a[112:115]
.La5_LW1_6:
s_cbranch_execz .La5_LW1_10
v_mfma_f32_16x16x32_bf16 a[112:115], v[164:167], v[212:215], a[112:115]
v_mfma_f32_16x16x32_bf16 a[128:131], v[164:167], v[220:223], a[128:131]
s_waitcnt lgkmcnt(2)
s_barrier
v_mfma_f32_16x16x32_bf16 a[128:131], v[160:163], v[216:219], a[128:131]
v_mfma_f32_16x16x32_bf16 a[132:135], v[168:171], v[216:219], a[132:135]
s_xor_b32 m0, s82, 32768
v_mfma_f32_16x16x32_bf16 a[132:135], v[172:175], v[220:223], a[132:135]
buffer_load_dwordx4 v224, s[40:43], s79 offen lds
v_mfma_f32_16x16x32_bf16 a[116:119], v[172:175], v[212:215], a[116:119]
v_mfma_f32_16x16x32_bf16 a[116:119], v[168:171], v[208:211], a[116:119]
ds_read_b64_tr_b16 v[36:37], v240 offset:50816
ds_read_b64_tr_b16 v[38:39], v240 offset:51840
v_mfma_f32_16x16x32_bf16 a[108:111], v[168:171], v[200:203], a[108:111]
s_xor_b32 m0, s82, 33792
v_mfma_f32_16x16x32_bf16 a[108:111], v[172:175], v[204:207], a[108:111]
buffer_load_dwordx4 v225, s[40:43], s79 offen lds
v_mfma_f32_16x16x32_bf16 a[100:103], v[172:175], v[196:199], a[100:103]
v_mfma_f32_16x16x32_bf16 a[100:103], v[168:171], v[192:195], a[100:103]
v_mfma_f32_16x16x32_bf16 a[232:235], v[176:179], v[192:195], a[232:235]
s_xor_b32 m0, s82, 34816
v_mfma_f32_16x16x32_bf16 a[232:235], v[180:183], v[196:199], a[232:235]
buffer_load_dwordx4 v226, s[40:43], s79 offen lds
v_mfma_f32_16x16x32_bf16 a[152:155], v[180:183], v[204:207], a[152:155]
v_mfma_f32_16x16x32_bf16 a[152:155], v[176:179], v[200:203], a[152:155]
ds_read_b64_tr_b16 v[40:41], v241 offset:33920
ds_read_b64_tr_b16 v[42:43], v241 offset:34944
v_mfma_f32_16x16x32_bf16 a[208:211], v[176:179], v[208:211], a[208:211]
s_xor_b32 m0, s82, 35840
v_mfma_f32_16x16x32_bf16 a[208:211], v[180:183], v[212:215], a[208:211]
buffer_load_dwordx4 v227, s[40:43], s79 offen lds
v_mfma_f32_16x16x32_bf16 a[160:163], v[180:183], v[220:223], a[160:163]
v_mfma_f32_16x16x32_bf16 a[160:163], v[176:179], v[216:219], a[160:163]
v_mfma_f32_16x16x32_bf16 a[164:167], v[184:187], v[216:219], a[164:167]
s_xor_b32 m0, s82, 36864
v_mfma_f32_16x16x32_bf16 a[164:167], v[188:191], v[220:223], a[164:167]
buffer_load_dwordx4 v228, s[40:43], s79 offen lds
v_mfma_f32_16x16x32_bf16 a[212:215], v[188:191], v[212:215], a[212:215]
v_mfma_f32_16x16x32_bf16 a[212:215], v[184:187], v[208:211], a[212:215]
ds_read_b64_tr_b16 v[44:45], v241 offset:50816
ds_read_b64_tr_b16 v[46:47], v241 offset:51840
v_mfma_f32_16x16x32_bf16 a[156:159], v[184:187], v[200:203], a[156:159]
s_xor_b32 m0, s82, 37888
v_mfma_f32_16x16x32_bf16 a[156:159], v[188:191], v[204:207], a[156:159]
buffer_load_dwordx4 v229, s[40:43], s79 offen lds
v_mfma_f32_16x16x32_bf16 a[236:239], v[188:191], v[196:199], a[236:239]
v_mfma_f32_16x16x32_bf16 a[236:239], v[184:187], v[192:195], a[236:239]
v_mfma_f32_16x16x32_bf16 a[200:203], v[0:3], v[192:195], a[200:203]
s_xor_b32 m0, s82, 38912
v_mfma_f32_16x16x32_bf16 a[200:203], v[4:7], v[196:199], a[200:203]
buffer_load_dwordx4 v230, s[40:43], s79 offen lds
v_mfma_f32_16x16x32_bf16 a[184:187], v[4:7], v[204:207], a[184:187]
v_mfma_f32_16x16x32_bf16 a[184:187], v[0:3], v[200:203], a[184:187]
ds_read_b64_tr_b16 v[48:49], v242 offset:33920
ds_read_b64_tr_b16 v[50:51], v242 offset:34944
v_mfma_f32_16x16x32_bf16 a[64:67], v[0:3], v[208:211], a[64:67]
s_xor_b32 m0, s82, 39936
v_mfma_f32_16x16x32_bf16 a[64:67], v[4:7], v[212:215], a[64:67]
buffer_load_dwordx4 v231, s[40:43], s79 offen lds
v_mfma_f32_16x16x32_bf16 a[0:3], v[4:7], v[220:223], a[0:3]
s_mov_b32 s79, 256
v_mfma_f32_16x16x32_bf16 a[0:3], v[0:3], v[216:219], a[0:3]
v_mfma_f32_16x16x32_bf16 a[4:7], v[8:11], v[216:219], a[4:7]
v_mfma_f32_16x16x32_bf16 a[4:7], v[12:15], v[220:223], a[4:7]
buffer_load_dword v254, v135, s[68:71], s96 offen
v_mfma_f32_16x16x32_bf16 a[68:71], v[12:15], v[212:215], a[68:71]
s_add_u32 s96, s96, 1048576
v_mfma_f32_16x16x32_bf16 a[68:71], v[8:11], v[208:211], a[68:71]
ds_read_b64_tr_b16 v[52:53], v242 offset:50816
ds_read_b64_tr_b16 v[54:55], v242 offset:51840
v_mfma_f32_16x16x32_bf16 a[188:191], v[8:11], v[200:203], a[188:191]
v_mfma_f32_16x16x32_bf16 a[188:191], v[12:15], v[204:207], a[188:191]
buffer_load_dword v254, v135, s[68:71], s96 offen
v_mfma_f32_16x16x32_bf16 a[204:207], v[12:15], v[196:199], a[204:207]
s_add_u32 s96, s96, 1048576
v_mfma_f32_16x16x32_bf16 a[204:207], v[8:11], v[192:195], a[204:207]
ds_read_b64_tr_b16 v[56:57], v243 offset:33920
ds_read_b64_tr_b16 v[58:59], v243 offset:34944
v_mfma_f32_16x16x32_bf16 a[88:91], v[16:19], v[192:195], a[88:91]
v_mfma_f32_16x16x32_bf16 a[88:91], v[20:23], v[196:199], a[88:91]
s_waitcnt vmcnt(18)
buffer_store_dwordx2 v[252:253], off, s[64:67], s91 offset:1024 sc1
v_mfma_f32_16x16x32_bf16 a[24:27], v[20:23], v[204:207], a[24:27]
s_mov_b32 s91, 0
s_lshl_b32 s92, s76, 19
v_mfma_f32_16x16x32_bf16 a[24:27], v[16:19], v[200:203], a[24:27]
ds_read_b64_tr_b16 v[60:61], v243 offset:50816
ds_read_b64_tr_b16 v[62:63], v243 offset:51840
v_mfma_f32_16x16x32_bf16 a[240:243], v[16:19], v[208:211], a[240:243]
s_add_u32 s92, s92, 88080384
v_mfma_f32_16x16x32_bf16 a[240:243], v[20:23], v[212:215], a[240:243]
s_add_u32 s52, s26, s92
s_addc_u32 s53, s27, 0
v_mfma_f32_16x16x32_bf16 a[32:35], v[20:23], v[220:223], a[32:35]
s_and_b32 s53, s53, 0xffff
s_mov_b32 s54, 524672
v_mfma_f32_16x16x32_bf16 a[32:35], v[16:19], v[216:219], a[32:35]
s_mov_b32 s55, 0x20000
s_lshl_b32 s92, s3, 16
v_mfma_f32_16x16x32_bf16 a[36:39], v[24:27], v[216:219], a[36:39]
s_sub_u32 s93, 458752, s92
v_add_u32 v244, s92, v136
v_mfma_f32_16x16x32_bf16 a[36:39], v[28:31], v[220:223], a[36:39]
v_add_u32 v246, s93, v136
v_add_u32 v245, 0x8000, v244
v_mfma_f32_16x16x32_bf16 a[244:247], v[28:31], v[212:215], a[244:247]
v_add_u32 v247, 0x8000, v246
s_lshl_b32 s94, s72, 19
v_mfma_f32_16x16x32_bf16 a[244:247], v[24:27], v[208:211], a[244:247]
s_lshl_b32 s93, s76, 16
s_add_u32 s92, s94, s93
v_mfma_f32_16x16x32_bf16 a[28:31], v[24:27], v[200:203], a[28:31]
s_lshl_b32 s93, s77, 13
s_add_u32 s92, s92, s93
v_mfma_f32_16x16x32_bf16 a[28:31], v[28:31], v[204:207], a[28:31]
s_add_u32 s92, s92, 37748736
v_mfma_f32_16x16x32_bf16 a[92:95], v[28:31], v[196:199], a[92:95]
s_waitcnt vmcnt(11) lgkmcnt(0)
s_barrier
v_mfma_f32_16x16x32_bf16 a[92:95], v[24:27], v[192:195], a[92:95]
s_add_u32 s48, s26, s92
s_addc_u32 s49, s27, 0
v_mfma_f32_16x16x32_bf16 a[120:123], v[160:163], v[32:35], a[120:123]
s_and_b32 s49, s49, 0xffff
s_mov_b32 s50, 57344
v_mfma_f32_16x16x32_bf16 a[120:123], v[164:167], v[36:39], a[120:123]
s_mov_b32 s51, 0x20000
s_lshl_b32 s93, s77, 24
v_mfma_f32_16x16x32_bf16 a[136:139], v[164:167], v[44:47], a[136:139]
ds_read_b64_tr_b16 v[192:193], v240 offset:0
ds_read_b64_tr_b16 v[194:195], v240 offset:1024
v_mfma_f32_16x16x32_bf16 a[136:139], v[160:163], v[40:43], a[136:139]
ds_read_b64_tr_b16 v[196:197], v240 offset:16896
ds_read_b64_tr_b16 v[198:199], v240 offset:17920
v_mfma_f32_16x16x32_bf16 a[224:227], v[160:163], v[48:51], a[224:227]
s_add_u32 s92, s94, s93
s_add_u32 s92, s92, 20975616
v_mfma_f32_16x16x32_bf16 a[224:227], v[164:167], v[52:55], a[224:227]
s_add_u32 s60, s26, s92
s_addc_u32 s61, s27, 0
v_mfma_f32_16x16x32_bf16 a[144:147], v[164:167], v[60:63], a[144:147]
s_and_b32 s61, s61, 0xffff
s_mov_b32 s62, 520192
v_mfma_f32_16x16x32_bf16 a[144:147], v[160:163], v[56:59], a[144:147]
ds_read_b128 v[160:163], v236 offset:24576
ds_read_b128 v[164:167], v237 offset:24576
v_mfma_f32_16x16x32_bf16 a[148:151], v[168:171], v[56:59], a[148:151]
s_mov_b32 s63, 0x20000
s_lshl_b32 s92, s77, 14
v_mfma_f32_16x16x32_bf16 a[148:151], v[172:175], v[60:63], a[148:151]
s_sub_u32 s95, 24576, s92
v_add_u32 v238, s95, v255
v_mfma_f32_16x16x32_bf16 a[228:231], v[172:175], v[52:55], a[228:231]
v_mfma_f32_16x16x32_bf16 a[228:231], v[168:171], v[48:51], a[228:231]
ds_read_b64_tr_b16 v[200:201], v241 offset:0
ds_read_b64_tr_b16 v[202:203], v241 offset:1024
v_mfma_f32_16x16x32_bf16 a[140:143], v[168:171], v[40:43], a[140:143]
v_mfma_f32_16x16x32_bf16 a[140:143], v[172:175], v[44:47], a[140:143]
v_mfma_f32_16x16x32_bf16 a[124:127], v[172:175], v[36:39], a[124:127]
v_mfma_f32_16x16x32_bf16 a[124:127], v[168:171], v[32:35], a[124:127]
ds_read_b128 v[168:171], v236 offset:26624
ds_read_b128 v[172:175], v237 offset:26624
v_mfma_f32_16x16x32_bf16 a[216:219], v[176:179], v[32:35], a[216:219]
v_mfma_f32_16x16x32_bf16 a[216:219], v[180:183], v[36:39], a[216:219]
v_mfma_f32_16x16x32_bf16 a[168:171], v[180:183], v[44:47], a[168:171]
v_mfma_f32_16x16x32_bf16 a[168:171], v[176:179], v[40:43], a[168:171]
ds_read_b64_tr_b16 v[204:205], v241 offset:16896
ds_read_b64_tr_b16 v[206:207], v241 offset:17920
v_mfma_f32_16x16x32_bf16 a[192:195], v[176:179], v[48:51], a[192:195]
v_mfma_f32_16x16x32_bf16 a[192:195], v[180:183], v[52:55], a[192:195]
v_mfma_f32_16x16x32_bf16 a[176:179], v[180:183], v[60:63], a[176:179]
v_mfma_f32_16x16x32_bf16 a[176:179], v[176:179], v[56:59], a[176:179]
ds_read_b128 v[176:179], v236 offset:16384
ds_read_b128 v[180:183], v237 offset:16384
v_mfma_f32_16x16x32_bf16 a[180:183], v[184:187], v[56:59], a[180:183]
v_mfma_f32_16x16x32_bf16 a[180:183], v[188:191], v[60:63], a[180:183]
v_mfma_f32_16x16x32_bf16 a[196:199], v[188:191], v[52:55], a[196:199]
v_mfma_f32_16x16x32_bf16 a[196:199], v[184:187], v[48:51], a[196:199]
ds_read_b64_tr_b16 v[208:209], v242 offset:0
ds_read_b64_tr_b16 v[210:211], v242 offset:1024
v_mfma_f32_16x16x32_bf16 a[172:175], v[184:187], v[40:43], a[172:175]
s_add_u32 m0, s83, 33792
v_mfma_f32_16x16x32_bf16 a[172:175], v[188:191], v[44:47], a[172:175]
buffer_load_dwordx4 v232, s[44:47], s80 offen lds
v_mfma_f32_16x16x32_bf16 a[220:223], v[188:191], v[36:39], a[220:223]
v_mfma_f32_16x16x32_bf16 a[220:223], v[184:187], v[32:35], a[220:223]
ds_read_b128 v[184:187], v236 offset:18432
ds_read_b128 v[188:191], v237 offset:18432
v_mfma_f32_16x16x32_bf16 a[72:75], v[0:3], v[32:35], a[72:75]
s_add_u32 m0, s83, 34816
v_mfma_f32_16x16x32_bf16 a[72:75], v[4:7], v[36:39], a[72:75]
buffer_load_dwordx4 v233, s[44:47], s80 offen lds
v_mfma_f32_16x16x32_bf16 a[8:11], v[4:7], v[44:47], a[8:11]
v_mfma_f32_16x16x32_bf16 a[8:11], v[0:3], v[40:43], a[8:11]
ds_read_b64_tr_b16 v[212:213], v242 offset:16896
ds_read_b64_tr_b16 v[214:215], v242 offset:17920
v_mfma_f32_16x16x32_bf16 a[80:83], v[0:3], v[48:51], a[80:83]
s_add_u32 m0, s83, 35968
v_mfma_f32_16x16x32_bf16 a[80:83], v[4:7], v[52:55], a[80:83]
buffer_load_dwordx4 v234, s[44:47], s80 offen lds
v_mfma_f32_16x16x32_bf16 a[16:19], v[4:7], v[60:63], a[16:19]
v_mfma_f32_16x16x32_bf16 a[16:19], v[0:3], v[56:59], a[16:19]
ds_read_b128 v[0:3], v236 offset:8192
ds_read_b128 v[4:7], v237 offset:8192
v_mfma_f32_16x16x32_bf16 a[20:23], v[8:11], v[56:59], a[20:23]
s_add_u32 m0, s83, 36992
v_mfma_f32_16x16x32_bf16 a[20:23], v[12:15], v[60:63], a[20:23]
buffer_load_dwordx4 v235, s[44:47], s80 offen lds
v_mfma_f32_16x16x32_bf16 a[84:87], v[12:15], v[52:55], a[84:87]
s_mov_b32 s80, 786432
v_mfma_f32_16x16x32_bf16 a[84:87], v[8:11], v[48:51], a[84:87]
ds_read_b64_tr_b16 v[216:217], v243 offset:0
ds_read_b64_tr_b16 v[218:219], v243 offset:1024
v_mfma_f32_16x16x32_bf16 a[12:15], v[8:11], v[40:43], a[12:15]
s_add_u32 m0, s83, 50688
v_mfma_f32_16x16x32_bf16 a[12:15], v[12:15], v[44:47], a[12:15]
buffer_load_dwordx4 v232, s[44:47], s80 offen lds
v_mfma_f32_16x16x32_bf16 a[76:79], v[12:15], v[36:39], a[76:79]
v_mfma_f32_16x16x32_bf16 a[76:79], v[8:11], v[32:35], a[76:79]
ds_read_b128 v[8:11], v236 offset:10240
ds_read_b128 v[12:15], v237 offset:10240
v_mfma_f32_16x16x32_bf16 a[248:251], v[16:19], v[32:35], a[248:251]
s_add_u32 m0, s83, 51712
v_mfma_f32_16x16x32_bf16 a[248:251], v[20:23], v[36:39], a[248:251]
buffer_load_dwordx4 v233, s[44:47], s80 offen lds
v_mfma_f32_16x16x32_bf16 a[40:43], v[20:23], v[44:47], a[40:43]
v_mfma_f32_16x16x32_bf16 a[40:43], v[16:19], v[40:43], a[40:43]
ds_read_b64_tr_b16 v[220:221], v243 offset:16896
ds_read_b64_tr_b16 v[222:223], v243 offset:17920
v_mfma_f32_16x16x32_bf16 a[48:51], v[16:19], v[48:51], a[48:51]
s_add_u32 m0, s83, 52864
v_mfma_f32_16x16x32_bf16 a[48:51], v[20:23], v[52:55], a[48:51]
buffer_load_dwordx4 v234, s[44:47], s80 offen lds
v_mfma_f32_16x16x32_bf16 a[56:59], v[20:23], v[60:63], a[56:59]
v_mfma_f32_16x16x32_bf16 a[56:59], v[16:19], v[56:59], a[56:59]
ds_read_b128 v[16:19], v236 offset:0
ds_read_b128 v[20:23], v237 offset:0
v_mfma_f32_16x16x32_bf16 a[60:63], v[24:27], v[56:59], a[60:63]
s_add_u32 m0, s83, 53888
v_mfma_f32_16x16x32_bf16 a[60:63], v[28:31], v[60:63], a[60:63]
buffer_load_dwordx4 v235, s[44:47], s80 offen lds
v_mfma_f32_16x16x32_bf16 a[52:55], v[28:31], v[52:55], a[52:55]
s_mov_b32 s80, 1048576
v_mfma_f32_16x16x32_bf16 a[52:55], v[24:27], v[48:51], a[52:55]
v_mfma_f32_16x16x32_bf16 a[44:47], v[24:27], v[40:43], a[44:47]
v_mfma_f32_16x16x32_bf16 a[44:47], v[28:31], v[44:47], a[44:47]
v_mfma_f32_16x16x32_bf16 a[252:255], v[28:31], v[36:39], a[252:255]
s_waitcnt lgkmcnt(2)
v_mfma_f32_16x16x32_bf16 a[252:255], v[24:27], v[32:35], a[252:255]
ds_read_b128 v[24:27], v236 offset:2048
ds_read_b128 v[28:31], v237 offset:2048
v_mfma_f32_16x16x32_bf16 a[96:99], v[160:163], v[192:195], a[96:99]
v_mfma_f32_16x16x32_bf16 a[96:99], v[164:167], v[196:199], a[96:99]
v_mfma_f32_16x16x32_bf16 a[104:107], v[164:167], v[204:207], a[104:107]
v_mfma_f32_16x16x32_bf16 a[104:107], v[160:163], v[200:203], a[104:107]
ds_read_b64_tr_b16 v[32:33], v240 offset:128
ds_read_b64_tr_b16 v[34:35], v240 offset:1152
v_mfma_f32_16x16x32_bf16 a[112:115], v[160:163], v[208:211], a[112:115]
v_mfma_f32_16x16x32_bf16 a[112:115], v[164:167], v[212:215], a[112:115]
v_mfma_f32_16x16x32_bf16 a[128:131], v[164:167], v[220:223], a[128:131]
s_waitcnt lgkmcnt(2)
s_barrier
v_mfma_f32_16x16x32_bf16 a[128:131], v[160:163], v[216:219], a[128:131]
v_mfma_f32_16x16x32_bf16 a[132:135], v[168:171], v[216:219], a[132:135]
s_xor_b32 m0, s82, 0
v_mfma_f32_16x16x32_bf16 a[132:135], v[172:175], v[220:223], a[132:135]
buffer_load_dwordx4 v224, s[40:43], s79 offen lds
v_mfma_f32_16x16x32_bf16 a[116:119], v[172:175], v[212:215], a[116:119]
v_mfma_f32_16x16x32_bf16 a[116:119], v[168:171], v[208:211], a[116:119]
ds_read_b64_tr_b16 v[36:37], v240 offset:17024
ds_read_b64_tr_b16 v[38:39], v240 offset:18048
v_mfma_f32_16x16x32_bf16 a[108:111], v[168:171], v[200:203], a[108:111]
s_xor_b32 m0, s82, 1024
v_mfma_f32_16x16x32_bf16 a[108:111], v[172:175], v[204:207], a[108:111]
buffer_load_dwordx4 v225, s[40:43], s79 offen lds
v_mfma_f32_16x16x32_bf16 a[100:103], v[172:175], v[196:199], a[100:103]
v_mfma_f32_16x16x32_bf16 a[100:103], v[168:171], v[192:195], a[100:103]
v_mfma_f32_16x16x32_bf16 a[232:235], v[176:179], v[192:195], a[232:235]
s_xor_b32 m0, s82, 2048
v_mfma_f32_16x16x32_bf16 a[232:235], v[180:183], v[196:199], a[232:235]
buffer_load_dwordx4 v226, s[40:43], s79 offen lds
v_mfma_f32_16x16x32_bf16 a[152:155], v[180:183], v[204:207], a[152:155]
v_mfma_f32_16x16x32_bf16 a[152:155], v[176:179], v[200:203], a[152:155]
ds_read_b64_tr_b16 v[40:41], v241 offset:128
ds_read_b64_tr_b16 v[42:43], v241 offset:1152
v_mfma_f32_16x16x32_bf16 a[208:211], v[176:179], v[208:211], a[208:211]
s_xor_b32 m0, s82, 3072
v_mfma_f32_16x16x32_bf16 a[208:211], v[180:183], v[212:215], a[208:211]
buffer_load_dwordx4 v227, s[40:43], s79 offen lds
v_mfma_f32_16x16x32_bf16 a[160:163], v[180:183], v[220:223], a[160:163]
v_mfma_f32_16x16x32_bf16 a[160:163], v[176:179], v[216:219], a[160:163]
v_mfma_f32_16x16x32_bf16 a[164:167], v[184:187], v[216:219], a[164:167]
s_xor_b32 m0, s82, 4096
v_mfma_f32_16x16x32_bf16 a[164:167], v[188:191], v[220:223], a[164:167]
buffer_load_dwordx4 v228, s[40:43], s79 offen lds
v_mfma_f32_16x16x32_bf16 a[212:215], v[188:191], v[212:215], a[212:215]
v_mfma_f32_16x16x32_bf16 a[212:215], v[184:187], v[208:211], a[212:215]
ds_read_b64_tr_b16 v[44:45], v241 offset:17024
ds_read_b64_tr_b16 v[46:47], v241 offset:18048
v_mfma_f32_16x16x32_bf16 a[156:159], v[184:187], v[200:203], a[156:159]
s_xor_b32 m0, s82, 5120
v_mfma_f32_16x16x32_bf16 a[156:159], v[188:191], v[204:207], a[156:159]
buffer_load_dwordx4 v229, s[40:43], s79 offen lds
v_mfma_f32_16x16x32_bf16 a[236:239], v[188:191], v[196:199], a[236:239]
v_mfma_f32_16x16x32_bf16 a[236:239], v[184:187], v[192:195], a[236:239]
v_mfma_f32_16x16x32_bf16 a[200:203], v[0:3], v[192:195], a[200:203]
s_xor_b32 m0, s82, 6144
v_mfma_f32_16x16x32_bf16 a[200:203], v[4:7], v[196:199], a[200:203]
buffer_load_dwordx4 v230, s[40:43], s79 offen lds
v_mfma_f32_16x16x32_bf16 a[184:187], v[4:7], v[204:207], a[184:187]
v_mfma_f32_16x16x32_bf16 a[184:187], v[0:3], v[200:203], a[184:187]
ds_read_b64_tr_b16 v[48:49], v242 offset:128
ds_read_b64_tr_b16 v[50:51], v242 offset:1152
v_mfma_f32_16x16x32_bf16 a[64:67], v[0:3], v[208:211], a[64:67]
s_xor_b32 m0, s82, 7168
v_mfma_f32_16x16x32_bf16 a[64:67], v[4:7], v[212:215], a[64:67]
buffer_load_dwordx4 v231, s[40:43], s79 offen lds
v_mfma_f32_16x16x32_bf16 a[0:3], v[4:7], v[220:223], a[0:3]
s_mov_b32 s79, 384
v_mfma_f32_16x16x32_bf16 a[0:3], v[0:3], v[216:219], a[0:3]
v_mfma_f32_16x16x32_bf16 a[4:7], v[8:11], v[216:219], a[4:7]
v_mfma_f32_16x16x32_bf16 a[4:7], v[12:15], v[220:223], a[4:7]
buffer_load_dword v254, v135, s[68:71], s96 offen
v_mfma_f32_16x16x32_bf16 a[68:71], v[12:15], v[212:215], a[68:71]
s_add_u32 s96, s96, 1048576
v_mfma_f32_16x16x32_bf16 a[68:71], v[8:11], v[208:211], a[68:71]
ds_read_b64_tr_b16 v[52:53], v242 offset:17024
ds_read_b64_tr_b16 v[54:55], v242 offset:18048
v_mfma_f32_16x16x32_bf16 a[188:191], v[8:11], v[200:203], a[188:191]
v_mfma_f32_16x16x32_bf16 a[188:191], v[12:15], v[204:207], a[188:191]
buffer_load_dword v254, v135, s[68:71], s96 offen
v_mfma_f32_16x16x32_bf16 a[204:207], v[12:15], v[196:199], a[204:207]
s_add_u32 s96, s96, 1048576
v_mfma_f32_16x16x32_bf16 a[204:207], v[8:11], v[192:195], a[204:207]
ds_read_b64_tr_b16 v[56:57], v243 offset:128
ds_read_b64_tr_b16 v[58:59], v243 offset:1152
v_mfma_f32_16x16x32_bf16 a[88:91], v[16:19], v[192:195], a[88:91]
v_mfma_f32_16x16x32_bf16 a[88:91], v[20:23], v[196:199], a[88:91]
v_mfma_f32_16x16x32_bf16 a[24:27], v[20:23], v[204:207], a[24:27]
v_mfma_f32_16x16x32_bf16 a[24:27], v[16:19], v[200:203], a[24:27]
ds_read_b64_tr_b16 v[60:61], v243 offset:17024
ds_read_b64_tr_b16 v[62:63], v243 offset:18048
v_mfma_f32_16x16x32_bf16 a[240:243], v[16:19], v[208:211], a[240:243]
v_mfma_f32_16x16x32_bf16 a[240:243], v[20:23], v[212:215], a[240:243]
v_mfma_f32_16x16x32_bf16 a[32:35], v[20:23], v[220:223], a[32:35]
v_mfma_f32_16x16x32_bf16 a[32:35], v[16:19], v[216:219], a[32:35]
v_mfma_f32_16x16x32_bf16 a[36:39], v[24:27], v[216:219], a[36:39]
v_mfma_f32_16x16x32_bf16 a[36:39], v[28:31], v[220:223], a[36:39]
v_mfma_f32_16x16x32_bf16 a[244:247], v[28:31], v[212:215], a[244:247]
v_mfma_f32_16x16x32_bf16 a[244:247], v[24:27], v[208:211], a[244:247]
v_mfma_f32_16x16x32_bf16 a[28:31], v[24:27], v[200:203], a[28:31]
v_mfma_f32_16x16x32_bf16 a[28:31], v[28:31], v[204:207], a[28:31]
v_mfma_f32_16x16x32_bf16 a[92:95], v[28:31], v[196:199], a[92:95]
s_waitcnt vmcnt(10) lgkmcnt(0)
s_barrier
v_mfma_f32_16x16x32_bf16 a[92:95], v[24:27], v[192:195], a[92:95]
v_mfma_f32_16x16x32_bf16 a[120:123], v[160:163], v[32:35], a[120:123]
v_mfma_f32_16x16x32_bf16 a[120:123], v[164:167], v[36:39], a[120:123]
v_mfma_f32_16x16x32_bf16 a[136:139], v[164:167], v[44:47], a[136:139]
ds_read_b64_tr_b16 v[192:193], v240 offset:33792
ds_read_b64_tr_b16 v[194:195], v240 offset:34816
v_mfma_f32_16x16x32_bf16 a[136:139], v[160:163], v[40:43], a[136:139]
ds_read_b64_tr_b16 v[196:197], v240 offset:50688
ds_read_b64_tr_b16 v[198:199], v240 offset:51712
v_mfma_f32_16x16x32_bf16 a[224:227], v[160:163], v[48:51], a[224:227]
v_mfma_f32_16x16x32_bf16 a[224:227], v[164:167], v[52:55], a[224:227]
v_mfma_f32_16x16x32_bf16 a[144:147], v[164:167], v[60:63], a[144:147]
v_mfma_f32_16x16x32_bf16 a[144:147], v[160:163], v[56:59], a[144:147]
ds_read_b128 v[160:163], v236 offset:57344
ds_read_b128 v[164:167], v237 offset:57344
v_mfma_f32_16x16x32_bf16 a[148:151], v[168:171], v[56:59], a[148:151]
v_mfma_f32_16x16x32_bf16 a[148:151], v[172:175], v[60:63], a[148:151]
v_mfma_f32_16x16x32_bf16 a[228:231], v[172:175], v[52:55], a[228:231]
v_mfma_f32_16x16x32_bf16 a[228:231], v[168:171], v[48:51], a[228:231]
ds_read_b64_tr_b16 v[200:201], v241 offset:33792
ds_read_b64_tr_b16 v[202:203], v241 offset:34816
v_mfma_f32_16x16x32_bf16 a[140:143], v[168:171], v[40:43], a[140:143]
v_mfma_f32_16x16x32_bf16 a[140:143], v[172:175], v[44:47], a[140:143]
v_mfma_f32_16x16x32_bf16 a[124:127], v[172:175], v[36:39], a[124:127]
v_mfma_f32_16x16x32_bf16 a[124:127], v[168:171], v[32:35], a[124:127]
ds_read_b128 v[168:171], v236 offset:59392
ds_read_b128 v[172:175], v237 offset:59392
v_mfma_f32_16x16x32_bf16 a[216:219], v[176:179], v[32:35], a[216:219]
v_mfma_f32_16x16x32_bf16 a[216:219], v[180:183], v[36:39], a[216:219]
v_mfma_f32_16x16x32_bf16 a[168:171], v[180:183], v[44:47], a[168:171]
v_mfma_f32_16x16x32_bf16 a[168:171], v[176:179], v[40:43], a[168:171]
ds_read_b64_tr_b16 v[204:205], v241 offset:50688
ds_read_b64_tr_b16 v[206:207], v241 offset:51712
v_mfma_f32_16x16x32_bf16 a[192:195], v[176:179], v[48:51], a[192:195]
v_mfma_f32_16x16x32_bf16 a[192:195], v[180:183], v[52:55], a[192:195]
v_mfma_f32_16x16x32_bf16 a[176:179], v[180:183], v[60:63], a[176:179]
v_mfma_f32_16x16x32_bf16 a[176:179], v[176:179], v[56:59], a[176:179]
ds_read_b128 v[176:179], v236 offset:49152
ds_read_b128 v[180:183], v237 offset:49152
v_mfma_f32_16x16x32_bf16 a[180:183], v[184:187], v[56:59], a[180:183]
v_mfma_f32_16x16x32_bf16 a[180:183], v[188:191], v[60:63], a[180:183]
v_mfma_f32_16x16x32_bf16 a[196:199], v[188:191], v[52:55], a[196:199]
v_mfma_f32_16x16x32_bf16 a[196:199], v[184:187], v[48:51], a[196:199]
ds_read_b64_tr_b16 v[208:209], v242 offset:33792
ds_read_b64_tr_b16 v[210:211], v242 offset:34816
v_mfma_f32_16x16x32_bf16 a[172:175], v[184:187], v[40:43], a[172:175]
s_add_u32 m0, s83, 0
v_mfma_f32_16x16x32_bf16 a[172:175], v[188:191], v[44:47], a[172:175]
buffer_load_dwordx4 v232, s[44:47], s80 offen lds
v_mfma_f32_16x16x32_bf16 a[220:223], v[188:191], v[36:39], a[220:223]
v_mfma_f32_16x16x32_bf16 a[220:223], v[184:187], v[32:35], a[220:223]
ds_read_b128 v[184:187], v236 offset:51200
ds_read_b128 v[188:191], v237 offset:51200
v_mfma_f32_16x16x32_bf16 a[72:75], v[0:3], v[32:35], a[72:75]
s_add_u32 m0, s83, 1024
v_mfma_f32_16x16x32_bf16 a[72:75], v[4:7], v[36:39], a[72:75]
buffer_load_dwordx4 v233, s[44:47], s80 offen lds
v_mfma_f32_16x16x32_bf16 a[8:11], v[4:7], v[44:47], a[8:11]
v_mfma_f32_16x16x32_bf16 a[8:11], v[0:3], v[40:43], a[8:11]
ds_read_b64_tr_b16 v[212:213], v242 offset:50688
ds_read_b64_tr_b16 v[214:215], v242 offset:51712
v_mfma_f32_16x16x32_bf16 a[80:83], v[0:3], v[48:51], a[80:83]
s_add_u32 m0, s83, 2176
v_mfma_f32_16x16x32_bf16 a[80:83], v[4:7], v[52:55], a[80:83]
buffer_load_dwordx4 v234, s[44:47], s80 offen lds
v_mfma_f32_16x16x32_bf16 a[16:19], v[4:7], v[60:63], a[16:19]
v_mfma_f32_16x16x32_bf16 a[16:19], v[0:3], v[56:59], a[16:19]
ds_read_b128 v[0:3], v236 offset:40960
ds_read_b128 v[4:7], v237 offset:40960
v_mfma_f32_16x16x32_bf16 a[20:23], v[8:11], v[56:59], a[20:23]
s_add_u32 m0, s83, 3200
v_mfma_f32_16x16x32_bf16 a[20:23], v[12:15], v[60:63], a[20:23]
buffer_load_dwordx4 v235, s[44:47], s80 offen lds
v_mfma_f32_16x16x32_bf16 a[84:87], v[12:15], v[52:55], a[84:87]
s_mov_b32 s80, 1310720
v_mfma_f32_16x16x32_bf16 a[84:87], v[8:11], v[48:51], a[84:87]
ds_read_b64_tr_b16 v[216:217], v243 offset:33792
ds_read_b64_tr_b16 v[218:219], v243 offset:34816
v_mfma_f32_16x16x32_bf16 a[12:15], v[8:11], v[40:43], a[12:15]
s_add_u32 m0, s83, 16896
v_mfma_f32_16x16x32_bf16 a[12:15], v[12:15], v[44:47], a[12:15]
buffer_load_dwordx4 v232, s[44:47], s80 offen lds
v_mfma_f32_16x16x32_bf16 a[76:79], v[12:15], v[36:39], a[76:79]
v_mfma_f32_16x16x32_bf16 a[76:79], v[8:11], v[32:35], a[76:79]
ds_read_b128 v[8:11], v236 offset:43008
ds_read_b128 v[12:15], v237 offset:43008
v_mfma_f32_16x16x32_bf16 a[248:251], v[16:19], v[32:35], a[248:251]
s_add_u32 m0, s83, 17920
v_mfma_f32_16x16x32_bf16 a[248:251], v[20:23], v[36:39], a[248:251]
buffer_load_dwordx4 v233, s[44:47], s80 offen lds
v_mfma_f32_16x16x32_bf16 a[40:43], v[20:23], v[44:47], a[40:43]
v_mfma_f32_16x16x32_bf16 a[40:43], v[16:19], v[40:43], a[40:43]
ds_read_b64_tr_b16 v[220:221], v243 offset:50688
ds_read_b64_tr_b16 v[222:223], v243 offset:51712
v_mfma_f32_16x16x32_bf16 a[48:51], v[16:19], v[48:51], a[48:51]
s_add_u32 m0, s83, 19072
v_mfma_f32_16x16x32_bf16 a[48:51], v[20:23], v[52:55], a[48:51]
buffer_load_dwordx4 v234, s[44:47], s80 offen lds
v_mfma_f32_16x16x32_bf16 a[56:59], v[20:23], v[60:63], a[56:59]
v_mfma_f32_16x16x32_bf16 a[56:59], v[16:19], v[56:59], a[56:59]
ds_read_b128 v[16:19], v236 offset:32768
ds_read_b128 v[20:23], v237 offset:32768
v_mfma_f32_16x16x32_bf16 a[60:63], v[24:27], v[56:59], a[60:63]
s_add_u32 m0, s83, 20096
v_mfma_f32_16x16x32_bf16 a[60:63], v[28:31], v[60:63], a[60:63]
buffer_load_dwordx4 v235, s[44:47], s80 offen lds
v_mfma_f32_16x16x32_bf16 a[52:55], v[28:31], v[52:55], a[52:55]
s_mov_b32 s80, 1572864
v_mfma_f32_16x16x32_bf16 a[52:55], v[24:27], v[48:51], a[52:55]
v_mfma_f32_16x16x32_bf16 a[44:47], v[24:27], v[40:43], a[44:47]
v_mfma_f32_16x16x32_bf16 a[44:47], v[28:31], v[44:47], a[44:47]
v_mfma_f32_16x16x32_bf16 a[252:255], v[28:31], v[36:39], a[252:255]
s_waitcnt lgkmcnt(2)
v_mfma_f32_16x16x32_bf16 a[252:255], v[24:27], v[32:35], a[252:255]
ds_read_b128 v[24:27], v236 offset:34816
ds_read_b128 v[28:31], v237 offset:34816
v_mfma_f32_16x16x32_bf16 a[96:99], v[160:163], v[192:195], a[96:99]
v_mfma_f32_16x16x32_bf16 a[96:99], v[164:167], v[196:199], a[96:99]
v_mfma_f32_16x16x32_bf16 a[104:107], v[164:167], v[204:207], a[104:107]
v_mfma_f32_16x16x32_bf16 a[104:107], v[160:163], v[200:203], a[104:107]
ds_read_b64_tr_b16 v[32:33], v240 offset:33920
ds_read_b64_tr_b16 v[34:35], v240 offset:34944
v_mfma_f32_16x16x32_bf16 a[112:115], v[160:163], v[208:211], a[112:115]
v_mfma_f32_16x16x32_bf16 a[112:115], v[164:167], v[212:215], a[112:115]
v_mfma_f32_16x16x32_bf16 a[128:131], v[164:167], v[220:223], a[128:131]
s_waitcnt lgkmcnt(2)
s_barrier
v_mfma_f32_16x16x32_bf16 a[128:131], v[160:163], v[216:219], a[128:131]
v_mfma_f32_16x16x32_bf16 a[132:135], v[168:171], v[216:219], a[132:135]
s_xor_b32 m0, s82, 32768
v_mfma_f32_16x16x32_bf16 a[132:135], v[172:175], v[220:223], a[132:135]
buffer_load_dwordx4 v224, s[40:43], s79 offen lds
v_mfma_f32_16x16x32_bf16 a[116:119], v[172:175], v[212:215], a[116:119]
v_mfma_f32_16x16x32_bf16 a[116:119], v[168:171], v[208:211], a[116:119]
ds_read_b64_tr_b16 v[36:37], v240 offset:50816
ds_read_b64_tr_b16 v[38:39], v240 offset:51840
v_mfma_f32_16x16x32_bf16 a[108:111], v[168:171], v[200:203], a[108:111]
s_xor_b32 m0, s82, 33792
v_mfma_f32_16x16x32_bf16 a[108:111], v[172:175], v[204:207], a[108:111]
buffer_load_dwordx4 v225, s[40:43], s79 offen lds
v_mfma_f32_16x16x32_bf16 a[100:103], v[172:175], v[196:199], a[100:103]
v_mfma_f32_16x16x32_bf16 a[100:103], v[168:171], v[192:195], a[100:103]
v_mfma_f32_16x16x32_bf16 a[232:235], v[176:179], v[192:195], a[232:235]
s_xor_b32 m0, s82, 34816
v_mfma_f32_16x16x32_bf16 a[232:235], v[180:183], v[196:199], a[232:235]
buffer_load_dwordx4 v226, s[40:43], s79 offen lds
v_mfma_f32_16x16x32_bf16 a[152:155], v[180:183], v[204:207], a[152:155]
v_mfma_f32_16x16x32_bf16 a[152:155], v[176:179], v[200:203], a[152:155]
ds_read_b64_tr_b16 v[40:41], v241 offset:33920
ds_read_b64_tr_b16 v[42:43], v241 offset:34944
v_mfma_f32_16x16x32_bf16 a[208:211], v[176:179], v[208:211], a[208:211]
s_xor_b32 m0, s82, 35840
v_mfma_f32_16x16x32_bf16 a[208:211], v[180:183], v[212:215], a[208:211]
buffer_load_dwordx4 v227, s[40:43], s79 offen lds
v_mfma_f32_16x16x32_bf16 a[160:163], v[180:183], v[220:223], a[160:163]
v_mfma_f32_16x16x32_bf16 a[160:163], v[176:179], v[216:219], a[160:163]
v_mfma_f32_16x16x32_bf16 a[164:167], v[184:187], v[216:219], a[164:167]
s_xor_b32 m0, s82, 36864
v_mfma_f32_16x16x32_bf16 a[164:167], v[188:191], v[220:223], a[164:167]
buffer_load_dwordx4 v228, s[40:43], s79 offen lds
v_mfma_f32_16x16x32_bf16 a[212:215], v[188:191], v[212:215], a[212:215]
v_mfma_f32_16x16x32_bf16 a[212:215], v[184:187], v[208:211], a[212:215]
ds_read_b64_tr_b16 v[44:45], v241 offset:50816
ds_read_b64_tr_b16 v[46:47], v241 offset:51840
v_mfma_f32_16x16x32_bf16 a[156:159], v[184:187], v[200:203], a[156:159]
s_xor_b32 m0, s82, 37888
v_mfma_f32_16x16x32_bf16 a[156:159], v[188:191], v[204:207], a[156:159]
buffer_load_dwordx4 v229, s[40:43], s79 offen lds
v_mfma_f32_16x16x32_bf16 a[236:239], v[188:191], v[196:199], a[236:239]
v_mfma_f32_16x16x32_bf16 a[236:239], v[184:187], v[192:195], a[236:239]
v_mfma_f32_16x16x32_bf16 a[200:203], v[0:3], v[192:195], a[200:203]
s_xor_b32 m0, s82, 38912
v_mfma_f32_16x16x32_bf16 a[200:203], v[4:7], v[196:199], a[200:203]
buffer_load_dwordx4 v230, s[40:43], s79 offen lds
.La5_LW1_7:
s_cbranch_execz .La5_LW1_11
v_mfma_f32_16x16x32_bf16 a[184:187], v[4:7], v[204:207], a[184:187]
v_mfma_f32_16x16x32_bf16 a[184:187], v[0:3], v[200:203], a[184:187]
ds_read_b64_tr_b16 v[48:49], v242 offset:33920
ds_read_b64_tr_b16 v[50:51], v242 offset:34944
v_mfma_f32_16x16x32_bf16 a[64:67], v[0:3], v[208:211], a[64:67]
s_xor_b32 m0, s82, 39936
v_mfma_f32_16x16x32_bf16 a[64:67], v[4:7], v[212:215], a[64:67]
buffer_load_dwordx4 v231, s[40:43], s79 offen lds
v_mfma_f32_16x16x32_bf16 a[0:3], v[4:7], v[220:223], a[0:3]
s_mov_b32 s79, 512
v_mfma_f32_16x16x32_bf16 a[0:3], v[0:3], v[216:219], a[0:3]
v_mfma_f32_16x16x32_bf16 a[4:7], v[8:11], v[216:219], a[4:7]
v_mfma_f32_16x16x32_bf16 a[4:7], v[12:15], v[220:223], a[4:7]
buffer_load_dword v254, v135, s[68:71], s96 offen
v_mfma_f32_16x16x32_bf16 a[68:71], v[12:15], v[212:215], a[68:71]
s_add_u32 s96, s96, 1048576
v_mfma_f32_16x16x32_bf16 a[68:71], v[8:11], v[208:211], a[68:71]
ds_read_b64_tr_b16 v[52:53], v242 offset:50816
ds_read_b64_tr_b16 v[54:55], v242 offset:51840
v_mfma_f32_16x16x32_bf16 a[188:191], v[8:11], v[200:203], a[188:191]
v_mfma_f32_16x16x32_bf16 a[188:191], v[12:15], v[204:207], a[188:191]
buffer_load_dword v254, v135, s[68:71], s96 offen
v_mfma_f32_16x16x32_bf16 a[204:207], v[12:15], v[196:199], a[204:207]
s_add_u32 s96, s96, 1048576
v_mfma_f32_16x16x32_bf16 a[204:207], v[8:11], v[192:195], a[204:207]
ds_read_b64_tr_b16 v[56:57], v243 offset:33920
ds_read_b64_tr_b16 v[58:59], v243 offset:34944
v_mfma_f32_16x16x32_bf16 a[88:91], v[16:19], v[192:195], a[88:91]
v_mfma_f32_16x16x32_bf16 a[88:91], v[20:23], v[196:199], a[88:91]
v_mfma_f32_16x16x32_bf16 a[24:27], v[20:23], v[204:207], a[24:27]
v_mfma_f32_16x16x32_bf16 a[24:27], v[16:19], v[200:203], a[24:27]
ds_read_b64_tr_b16 v[60:61], v243 offset:50816
ds_read_b64_tr_b16 v[62:63], v243 offset:51840
v_mfma_f32_16x16x32_bf16 a[240:243], v[16:19], v[208:211], a[240:243]
v_mfma_f32_16x16x32_bf16 a[240:243], v[20:23], v[212:215], a[240:243]
v_mfma_f32_16x16x32_bf16 a[32:35], v[20:23], v[220:223], a[32:35]
v_mfma_f32_16x16x32_bf16 a[32:35], v[16:19], v[216:219], a[32:35]
v_mfma_f32_16x16x32_bf16 a[36:39], v[24:27], v[216:219], a[36:39]
v_mfma_f32_16x16x32_bf16 a[36:39], v[28:31], v[220:223], a[36:39]
v_mfma_f32_16x16x32_bf16 a[244:247], v[28:31], v[212:215], a[244:247]
v_mfma_f32_16x16x32_bf16 a[244:247], v[24:27], v[208:211], a[244:247]
v_mfma_f32_16x16x32_bf16 a[28:31], v[24:27], v[200:203], a[28:31]
v_mfma_f32_16x16x32_bf16 a[28:31], v[28:31], v[204:207], a[28:31]
v_mfma_f32_16x16x32_bf16 a[92:95], v[28:31], v[196:199], a[92:95]
s_waitcnt vmcnt(10) lgkmcnt(0)
s_barrier
v_mfma_f32_16x16x32_bf16 a[92:95], v[24:27], v[192:195], a[92:95]
v_mfma_f32_16x16x32_bf16 a[120:123], v[160:163], v[32:35], a[120:123]
v_mfma_f32_16x16x32_bf16 a[120:123], v[164:167], v[36:39], a[120:123]
v_mfma_f32_16x16x32_bf16 a[136:139], v[164:167], v[44:47], a[136:139]
ds_read_b64_tr_b16 v[192:193], v240 offset:0
ds_read_b64_tr_b16 v[194:195], v240 offset:1024
v_mfma_f32_16x16x32_bf16 a[136:139], v[160:163], v[40:43], a[136:139]
ds_read_b64_tr_b16 v[196:197], v240 offset:16896
ds_read_b64_tr_b16 v[198:199], v240 offset:17920
v_mfma_f32_16x16x32_bf16 a[224:227], v[160:163], v[48:51], a[224:227]
v_mfma_f32_16x16x32_bf16 a[224:227], v[164:167], v[52:55], a[224:227]
v_mfma_f32_16x16x32_bf16 a[144:147], v[164:167], v[60:63], a[144:147]
v_mfma_f32_16x16x32_bf16 a[144:147], v[160:163], v[56:59], a[144:147]
ds_read_b128 v[160:163], v236 offset:24576
ds_read_b128 v[164:167], v237 offset:24576
v_mfma_f32_16x16x32_bf16 a[148:151], v[168:171], v[56:59], a[148:151]
v_mfma_f32_16x16x32_bf16 a[148:151], v[172:175], v[60:63], a[148:151]
v_mfma_f32_16x16x32_bf16 a[228:231], v[172:175], v[52:55], a[228:231]
v_mfma_f32_16x16x32_bf16 a[228:231], v[168:171], v[48:51], a[228:231]
ds_read_b64_tr_b16 v[200:201], v241 offset:0
ds_read_b64_tr_b16 v[202:203], v241 offset:1024
v_mfma_f32_16x16x32_bf16 a[140:143], v[168:171], v[40:43], a[140:143]
v_mfma_f32_16x16x32_bf16 a[140:143], v[172:175], v[44:47], a[140:143]
v_mfma_f32_16x16x32_bf16 a[124:127], v[172:175], v[36:39], a[124:127]
v_mfma_f32_16x16x32_bf16 a[124:127], v[168:171], v[32:35], a[124:127]
ds_read_b128 v[168:171], v236 offset:26624
ds_read_b128 v[172:175], v237 offset:26624
v_mfma_f32_16x16x32_bf16 a[216:219], v[176:179], v[32:35], a[216:219]
v_mfma_f32_16x16x32_bf16 a[216:219], v[180:183], v[36:39], a[216:219]
v_mfma_f32_16x16x32_bf16 a[168:171], v[180:183], v[44:47], a[168:171]
v_mfma_f32_16x16x32_bf16 a[168:171], v[176:179], v[40:43], a[168:171]
ds_read_b64_tr_b16 v[204:205], v241 offset:16896
ds_read_b64_tr_b16 v[206:207], v241 offset:17920
v_mfma_f32_16x16x32_bf16 a[192:195], v[176:179], v[48:51], a[192:195]
v_mfma_f32_16x16x32_bf16 a[192:195], v[180:183], v[52:55], a[192:195]
v_mfma_f32_16x16x32_bf16 a[176:179], v[180:183], v[60:63], a[176:179]
v_mfma_f32_16x16x32_bf16 a[176:179], v[176:179], v[56:59], a[176:179]
ds_read_b128 v[176:179], v236 offset:16384
ds_read_b128 v[180:183], v237 offset:16384
v_mfma_f32_16x16x32_bf16 a[180:183], v[184:187], v[56:59], a[180:183]
v_mfma_f32_16x16x32_bf16 a[180:183], v[188:191], v[60:63], a[180:183]
v_mfma_f32_16x16x32_bf16 a[196:199], v[188:191], v[52:55], a[196:199]
v_mfma_f32_16x16x32_bf16 a[196:199], v[184:187], v[48:51], a[196:199]
ds_read_b64_tr_b16 v[208:209], v242 offset:0
ds_read_b64_tr_b16 v[210:211], v242 offset:1024
v_mfma_f32_16x16x32_bf16 a[172:175], v[184:187], v[40:43], a[172:175]
s_add_u32 m0, s83, 33792
v_mfma_f32_16x16x32_bf16 a[172:175], v[188:191], v[44:47], a[172:175]
buffer_load_dwordx4 v232, s[44:47], s80 offen lds
v_mfma_f32_16x16x32_bf16 a[220:223], v[188:191], v[36:39], a[220:223]
v_mfma_f32_16x16x32_bf16 a[220:223], v[184:187], v[32:35], a[220:223]
ds_read_b128 v[184:187], v236 offset:18432
ds_read_b128 v[188:191], v237 offset:18432
v_mfma_f32_16x16x32_bf16 a[72:75], v[0:3], v[32:35], a[72:75]
s_add_u32 m0, s83, 34816
v_mfma_f32_16x16x32_bf16 a[72:75], v[4:7], v[36:39], a[72:75]
buffer_load_dwordx4 v233, s[44:47], s80 offen lds
v_mfma_f32_16x16x32_bf16 a[8:11], v[4:7], v[44:47], a[8:11]
v_mfma_f32_16x16x32_bf16 a[8:11], v[0:3], v[40:43], a[8:11]
ds_read_b64_tr_b16 v[212:213], v242 offset:16896
ds_read_b64_tr_b16 v[214:215], v242 offset:17920
v_mfma_f32_16x16x32_bf16 a[80:83], v[0:3], v[48:51], a[80:83]
s_add_u32 m0, s83, 35968
v_mfma_f32_16x16x32_bf16 a[80:83], v[4:7], v[52:55], a[80:83]
buffer_load_dwordx4 v234, s[44:47], s80 offen lds
v_mfma_f32_16x16x32_bf16 a[16:19], v[4:7], v[60:63], a[16:19]
v_mfma_f32_16x16x32_bf16 a[16:19], v[0:3], v[56:59], a[16:19]
ds_read_b128 v[0:3], v236 offset:8192
ds_read_b128 v[4:7], v237 offset:8192
v_mfma_f32_16x16x32_bf16 a[20:23], v[8:11], v[56:59], a[20:23]
s_add_u32 m0, s83, 36992
v_mfma_f32_16x16x32_bf16 a[20:23], v[12:15], v[60:63], a[20:23]
buffer_load_dwordx4 v235, s[44:47], s80 offen lds
v_mfma_f32_16x16x32_bf16 a[84:87], v[12:15], v[52:55], a[84:87]
s_mov_b32 s80, 1835008
v_mfma_f32_16x16x32_bf16 a[84:87], v[8:11], v[48:51], a[84:87]
ds_read_b64_tr_b16 v[216:217], v243 offset:0
ds_read_b64_tr_b16 v[218:219], v243 offset:1024
v_mfma_f32_16x16x32_bf16 a[12:15], v[8:11], v[40:43], a[12:15]
s_add_u32 m0, s83, 50688
v_mfma_f32_16x16x32_bf16 a[12:15], v[12:15], v[44:47], a[12:15]
buffer_load_dwordx4 v232, s[44:47], s80 offen lds
v_mfma_f32_16x16x32_bf16 a[76:79], v[12:15], v[36:39], a[76:79]
v_mfma_f32_16x16x32_bf16 a[76:79], v[8:11], v[32:35], a[76:79]
ds_read_b128 v[8:11], v236 offset:10240
ds_read_b128 v[12:15], v237 offset:10240
v_mfma_f32_16x16x32_bf16 a[248:251], v[16:19], v[32:35], a[248:251]
s_add_u32 m0, s83, 51712
v_mfma_f32_16x16x32_bf16 a[248:251], v[20:23], v[36:39], a[248:251]
buffer_load_dwordx4 v233, s[44:47], s80 offen lds
v_mfma_f32_16x16x32_bf16 a[40:43], v[20:23], v[44:47], a[40:43]
v_mfma_f32_16x16x32_bf16 a[40:43], v[16:19], v[40:43], a[40:43]
ds_read_b64_tr_b16 v[220:221], v243 offset:16896
ds_read_b64_tr_b16 v[222:223], v243 offset:17920
v_mfma_f32_16x16x32_bf16 a[48:51], v[16:19], v[48:51], a[48:51]
s_add_u32 m0, s83, 52864
v_mfma_f32_16x16x32_bf16 a[48:51], v[20:23], v[52:55], a[48:51]
buffer_load_dwordx4 v234, s[44:47], s80 offen lds
v_mfma_f32_16x16x32_bf16 a[56:59], v[20:23], v[60:63], a[56:59]
v_mfma_f32_16x16x32_bf16 a[56:59], v[16:19], v[56:59], a[56:59]
ds_read_b128 v[16:19], v236 offset:0
ds_read_b128 v[20:23], v237 offset:0
v_mfma_f32_16x16x32_bf16 a[60:63], v[24:27], v[56:59], a[60:63]
s_add_u32 m0, s83, 53888
v_mfma_f32_16x16x32_bf16 a[60:63], v[28:31], v[60:63], a[60:63]
buffer_load_dwordx4 v235, s[44:47], s80 offen lds
v_mfma_f32_16x16x32_bf16 a[52:55], v[28:31], v[52:55], a[52:55]
s_mov_b64 s[44:45], s[68:69]
s_mov_b32 s46, s70
v_mfma_f32_16x16x32_bf16 a[52:55], v[24:27], v[48:51], a[52:55]
v_mfma_f32_16x16x32_bf16 a[44:47], v[24:27], v[40:43], a[44:47]
v_mfma_f32_16x16x32_bf16 a[44:47], v[28:31], v[44:47], a[44:47]
v_mfma_f32_16x16x32_bf16 a[252:255], v[28:31], v[36:39], a[252:255]
s_waitcnt lgkmcnt(2)
s_lshl_b32 s80, s77, 18
v_mfma_f32_16x16x32_bf16 a[252:255], v[24:27], v[32:35], a[252:255]
ds_read_b128 v[24:27], v236 offset:2048
ds_read_b128 v[28:31], v237 offset:2048
v_mfma_f32_16x16x32_bf16 a[96:99], v[160:163], v[192:195], a[96:99]
v_mfma_f32_16x16x32_bf16 a[96:99], v[164:167], v[196:199], a[96:99]
v_mfma_f32_16x16x32_bf16 a[104:107], v[164:167], v[204:207], a[104:107]
v_mfma_f32_16x16x32_bf16 a[104:107], v[160:163], v[200:203], a[104:107]
ds_read_b64_tr_b16 v[32:33], v240 offset:128
ds_read_b64_tr_b16 v[34:35], v240 offset:1152
v_mfma_f32_16x16x32_bf16 a[112:115], v[160:163], v[208:211], a[112:115]
v_mfma_f32_16x16x32_bf16 a[112:115], v[164:167], v[212:215], a[112:115]
v_mfma_f32_16x16x32_bf16 a[128:131], v[164:167], v[220:223], a[128:131]
s_waitcnt lgkmcnt(2)
s_barrier
v_mfma_f32_16x16x32_bf16 a[128:131], v[160:163], v[216:219], a[128:131]
v_mfma_f32_16x16x32_bf16 a[132:135], v[168:171], v[216:219], a[132:135]
v_mfma_f32_16x16x32_bf16 a[132:135], v[172:175], v[220:223], a[132:135]
v_mfma_f32_16x16x32_bf16 a[116:119], v[172:175], v[212:215], a[116:119]
v_mfma_f32_16x16x32_bf16 a[116:119], v[168:171], v[208:211], a[116:119]
ds_read_b64_tr_b16 v[36:37], v240 offset:17024
ds_read_b64_tr_b16 v[38:39], v240 offset:18048
v_mfma_f32_16x16x32_bf16 a[108:111], v[168:171], v[200:203], a[108:111]
v_mfma_f32_16x16x32_bf16 a[108:111], v[172:175], v[204:207], a[108:111]
v_mfma_f32_16x16x32_bf16 a[100:103], v[172:175], v[196:199], a[100:103]
v_mfma_f32_16x16x32_bf16 a[100:103], v[168:171], v[192:195], a[100:103]
v_mfma_f32_16x16x32_bf16 a[232:235], v[176:179], v[192:195], a[232:235]
v_mfma_f32_16x16x32_bf16 a[232:235], v[180:183], v[196:199], a[232:235]
v_mfma_f32_16x16x32_bf16 a[152:155], v[180:183], v[204:207], a[152:155]
v_mfma_f32_16x16x32_bf16 a[152:155], v[176:179], v[200:203], a[152:155]
ds_read_b64_tr_b16 v[40:41], v241 offset:128
ds_read_b64_tr_b16 v[42:43], v241 offset:1152
v_mfma_f32_16x16x32_bf16 a[208:211], v[176:179], v[208:211], a[208:211]
v_mfma_f32_16x16x32_bf16 a[208:211], v[180:183], v[212:215], a[208:211]
v_mfma_f32_16x16x32_bf16 a[160:163], v[180:183], v[220:223], a[160:163]
v_mfma_f32_16x16x32_bf16 a[160:163], v[176:179], v[216:219], a[160:163]
v_mfma_f32_16x16x32_bf16 a[164:167], v[184:187], v[216:219], a[164:167]
v_mfma_f32_16x16x32_bf16 a[164:167], v[188:191], v[220:223], a[164:167]
v_mfma_f32_16x16x32_bf16 a[212:215], v[188:191], v[212:215], a[212:215]
v_mfma_f32_16x16x32_bf16 a[212:215], v[184:187], v[208:211], a[212:215]
ds_read_b64_tr_b16 v[44:45], v241 offset:17024
ds_read_b64_tr_b16 v[46:47], v241 offset:18048
v_mfma_f32_16x16x32_bf16 a[156:159], v[184:187], v[200:203], a[156:159]
v_mfma_f32_16x16x32_bf16 a[156:159], v[188:191], v[204:207], a[156:159]
v_mfma_f32_16x16x32_bf16 a[236:239], v[188:191], v[196:199], a[236:239]
v_mfma_f32_16x16x32_bf16 a[236:239], v[184:187], v[192:195], a[236:239]
v_mfma_f32_16x16x32_bf16 a[200:203], v[0:3], v[192:195], a[200:203]
v_mfma_f32_16x16x32_bf16 a[200:203], v[4:7], v[196:199], a[200:203]
v_mfma_f32_16x16x32_bf16 a[184:187], v[4:7], v[204:207], a[184:187]
v_mfma_f32_16x16x32_bf16 a[184:187], v[0:3], v[200:203], a[184:187]
ds_read_b64_tr_b16 v[48:49], v242 offset:128
ds_read_b64_tr_b16 v[50:51], v242 offset:1152
v_mfma_f32_16x16x32_bf16 a[64:67], v[0:3], v[208:211], a[64:67]
v_mfma_f32_16x16x32_bf16 a[64:67], v[4:7], v[212:215], a[64:67]
v_mfma_f32_16x16x32_bf16 a[0:3], v[4:7], v[220:223], a[0:3]
v_mfma_f32_16x16x32_bf16 a[0:3], v[0:3], v[216:219], a[0:3]
v_mfma_f32_16x16x32_bf16 a[4:7], v[8:11], v[216:219], a[4:7]
v_mfma_f32_16x16x32_bf16 a[4:7], v[12:15], v[220:223], a[4:7]
buffer_load_dword v254, v135, s[68:71], s96 offen
v_mfma_f32_16x16x32_bf16 a[68:71], v[12:15], v[212:215], a[68:71]
s_add_u32 s96, s96, 1048576
v_mfma_f32_16x16x32_bf16 a[68:71], v[8:11], v[208:211], a[68:71]
ds_read_b64_tr_b16 v[52:53], v242 offset:17024
ds_read_b64_tr_b16 v[54:55], v242 offset:18048
v_mfma_f32_16x16x32_bf16 a[188:191], v[8:11], v[200:203], a[188:191]
v_mfma_f32_16x16x32_bf16 a[188:191], v[12:15], v[204:207], a[188:191]
buffer_load_dword v254, v135, s[68:71], s96 offen
v_mfma_f32_16x16x32_bf16 a[204:207], v[12:15], v[196:199], a[204:207]
s_add_u32 s96, s96, 1048576
v_mfma_f32_16x16x32_bf16 a[204:207], v[8:11], v[192:195], a[204:207]
ds_read_b64_tr_b16 v[56:57], v243 offset:128
ds_read_b64_tr_b16 v[58:59], v243 offset:1152
v_mfma_f32_16x16x32_bf16 a[88:91], v[16:19], v[192:195], a[88:91]
v_mfma_f32_16x16x32_bf16 a[88:91], v[20:23], v[196:199], a[88:91]
v_mfma_f32_16x16x32_bf16 a[24:27], v[20:23], v[204:207], a[24:27]
v_mfma_f32_16x16x32_bf16 a[24:27], v[16:19], v[200:203], a[24:27]
ds_read_b64_tr_b16 v[60:61], v243 offset:17024
ds_read_b64_tr_b16 v[62:63], v243 offset:18048
v_mfma_f32_16x16x32_bf16 a[240:243], v[16:19], v[208:211], a[240:243]
v_mfma_f32_16x16x32_bf16 a[240:243], v[20:23], v[212:215], a[240:243]
v_mfma_f32_16x16x32_bf16 a[32:35], v[20:23], v[220:223], a[32:35]
v_mfma_f32_16x16x32_bf16 a[32:35], v[16:19], v[216:219], a[32:35]
v_mfma_f32_16x16x32_bf16 a[36:39], v[24:27], v[216:219], a[36:39]
v_mfma_f32_16x16x32_bf16 a[36:39], v[28:31], v[220:223], a[36:39]
buffer_load_dwordx4 v[72:75], v255, s[64:67], s89 offen offset:1024 sc1
v_mfma_f32_16x16x32_bf16 a[244:247], v[28:31], v[212:215], a[244:247]
v_mfma_f32_16x16x32_bf16 a[244:247], v[24:27], v[208:211], a[244:247]
v_mfma_f32_16x16x32_bf16 a[28:31], v[24:27], v[200:203], a[28:31]
v_mfma_f32_16x16x32_bf16 a[28:31], v[28:31], v[204:207], a[28:31]
v_mfma_f32_16x16x32_bf16 a[92:95], v[28:31], v[196:199], a[92:95]
s_waitcnt vmcnt(3) lgkmcnt(0)
s_barrier
v_mfma_f32_16x16x32_bf16 a[92:95], v[24:27], v[192:195], a[92:95]
v_mfma_f32_16x16x32_bf16 a[120:123], v[160:163], v[32:35], a[120:123]
v_mfma_f32_16x16x32_bf16 a[120:123], v[164:167], v[36:39], a[120:123]
v_mfma_f32_16x16x32_bf16 a[136:139], v[164:167], v[44:47], a[136:139]
ds_read_b64_tr_b16 v[192:193], v240 offset:33792
ds_read_b64_tr_b16 v[194:195], v240 offset:34816
v_mfma_f32_16x16x32_bf16 a[136:139], v[160:163], v[40:43], a[136:139]
ds_read_b64_tr_b16 v[196:197], v240 offset:50688
ds_read_b64_tr_b16 v[198:199], v240 offset:51712
v_mfma_f32_16x16x32_bf16 a[224:227], v[160:163], v[48:51], a[224:227]
v_mfma_f32_16x16x32_bf16 a[224:227], v[164:167], v[52:55], a[224:227]
v_mfma_f32_16x16x32_bf16 a[144:147], v[164:167], v[60:63], a[144:147]
v_mfma_f32_16x16x32_bf16 a[144:147], v[160:163], v[56:59], a[144:147]
ds_read_b128 v[160:163], v236 offset:57344
ds_read_b128 v[164:167], v237 offset:57344
v_mfma_f32_16x16x32_bf16 a[148:151], v[168:171], v[56:59], a[148:151]
v_mfma_f32_16x16x32_bf16 a[148:151], v[172:175], v[60:63], a[148:151]
v_mfma_f32_16x16x32_bf16 a[228:231], v[172:175], v[52:55], a[228:231]
v_mfma_f32_16x16x32_bf16 a[228:231], v[168:171], v[48:51], a[228:231]
ds_read_b64_tr_b16 v[200:201], v241 offset:33792
ds_read_b64_tr_b16 v[202:203], v241 offset:34816
v_mfma_f32_16x16x32_bf16 a[140:143], v[168:171], v[40:43], a[140:143]
v_mfma_f32_16x16x32_bf16 a[140:143], v[172:175], v[44:47], a[140:143]
v_mfma_f32_16x16x32_bf16 a[124:127], v[172:175], v[36:39], a[124:127]
v_mfma_f32_16x16x32_bf16 a[124:127], v[168:171], v[32:35], a[124:127]
ds_read_b128 v[168:171], v236 offset:59392
ds_read_b128 v[172:175], v237 offset:59392
v_mfma_f32_16x16x32_bf16 a[216:219], v[176:179], v[32:35], a[216:219]
v_mfma_f32_16x16x32_bf16 a[216:219], v[180:183], v[36:39], a[216:219]
v_mfma_f32_16x16x32_bf16 a[168:171], v[180:183], v[44:47], a[168:171]
v_mfma_f32_16x16x32_bf16 a[168:171], v[176:179], v[40:43], a[168:171]
ds_read_b64_tr_b16 v[204:205], v241 offset:50688
ds_read_b64_tr_b16 v[206:207], v241 offset:51712
v_mfma_f32_16x16x32_bf16 a[192:195], v[176:179], v[48:51], a[192:195]
v_mfma_f32_16x16x32_bf16 a[192:195], v[180:183], v[52:55], a[192:195]
v_mfma_f32_16x16x32_bf16 a[176:179], v[180:183], v[60:63], a[176:179]
v_mfma_f32_16x16x32_bf16 a[176:179], v[176:179], v[56:59], a[176:179]
ds_read_b128 v[176:179], v236 offset:49152
ds_read_b128 v[180:183], v237 offset:49152
v_mfma_f32_16x16x32_bf16 a[180:183], v[184:187], v[56:59], a[180:183]
v_mfma_f32_16x16x32_bf16 a[180:183], v[188:191], v[60:63], a[180:183]
v_mfma_f32_16x16x32_bf16 a[196:199], v[188:191], v[52:55], a[196:199]
v_mfma_f32_16x16x32_bf16 a[196:199], v[184:187], v[48:51], a[196:199]
ds_read_b64_tr_b16 v[208:209], v242 offset:33792
ds_read_b64_tr_b16 v[210:211], v242 offset:34816
v_mfma_f32_16x16x32_bf16 a[172:175], v[184:187], v[40:43], a[172:175]
s_add_u32 m0, s83, 0
v_mfma_f32_16x16x32_bf16 a[172:175], v[188:191], v[44:47], a[172:175]
buffer_load_dwordx4 v232, s[44:47], s80 offen lds
v_mfma_f32_16x16x32_bf16 a[220:223], v[188:191], v[36:39], a[220:223]
v_mfma_f32_16x16x32_bf16 a[220:223], v[184:187], v[32:35], a[220:223]
ds_read_b128 v[184:187], v236 offset:51200
ds_read_b128 v[188:191], v237 offset:51200
v_mfma_f32_16x16x32_bf16 a[72:75], v[0:3], v[32:35], a[72:75]
s_add_u32 m0, s83, 1024
v_mfma_f32_16x16x32_bf16 a[72:75], v[4:7], v[36:39], a[72:75]
buffer_load_dwordx4 v233, s[44:47], s80 offen lds
v_mfma_f32_16x16x32_bf16 a[8:11], v[4:7], v[44:47], a[8:11]
v_mfma_f32_16x16x32_bf16 a[8:11], v[0:3], v[40:43], a[8:11]
ds_read_b64_tr_b16 v[212:213], v242 offset:50688
ds_read_b64_tr_b16 v[214:215], v242 offset:51712
v_mfma_f32_16x16x32_bf16 a[80:83], v[0:3], v[48:51], a[80:83]
s_add_u32 m0, s83, 2176
v_mfma_f32_16x16x32_bf16 a[80:83], v[4:7], v[52:55], a[80:83]
buffer_load_dwordx4 v234, s[44:47], s80 offen lds
v_mfma_f32_16x16x32_bf16 a[16:19], v[4:7], v[60:63], a[16:19]
v_mfma_f32_16x16x32_bf16 a[16:19], v[0:3], v[56:59], a[16:19]
ds_read_b128 v[0:3], v236 offset:40960
ds_read_b128 v[4:7], v237 offset:40960
v_mfma_f32_16x16x32_bf16 a[20:23], v[8:11], v[56:59], a[20:23]
s_add_u32 m0, s83, 3200
v_mfma_f32_16x16x32_bf16 a[20:23], v[12:15], v[60:63], a[20:23]
buffer_load_dwordx4 v235, s[44:47], s80 offen lds
v_mfma_f32_16x16x32_bf16 a[84:87], v[12:15], v[52:55], a[84:87]
s_add_u32 s80, s80, 524288
v_mfma_f32_16x16x32_bf16 a[84:87], v[8:11], v[48:51], a[84:87]
ds_read_b64_tr_b16 v[216:217], v243 offset:33792
ds_read_b64_tr_b16 v[218:219], v243 offset:34816
v_mfma_f32_16x16x32_bf16 a[12:15], v[8:11], v[40:43], a[12:15]
s_add_u32 m0, s83, 16896
v_mfma_f32_16x16x32_bf16 a[12:15], v[12:15], v[44:47], a[12:15]
buffer_load_dwordx4 v232, s[44:47], s80 offen lds
v_mfma_f32_16x16x32_bf16 a[76:79], v[12:15], v[36:39], a[76:79]
v_mfma_f32_16x16x32_bf16 a[76:79], v[8:11], v[32:35], a[76:79]
ds_read_b128 v[8:11], v236 offset:43008
ds_read_b128 v[12:15], v237 offset:43008
v_mfma_f32_16x16x32_bf16 a[248:251], v[16:19], v[32:35], a[248:251]
s_add_u32 m0, s83, 17920
v_mfma_f32_16x16x32_bf16 a[248:251], v[20:23], v[36:39], a[248:251]
buffer_load_dwordx4 v233, s[44:47], s80 offen lds
v_mfma_f32_16x16x32_bf16 a[40:43], v[20:23], v[44:47], a[40:43]
v_mfma_f32_16x16x32_bf16 a[40:43], v[16:19], v[40:43], a[40:43]
ds_read_b64_tr_b16 v[220:221], v243 offset:50688
ds_read_b64_tr_b16 v[222:223], v243 offset:51712
v_mfma_f32_16x16x32_bf16 a[48:51], v[16:19], v[48:51], a[48:51]
s_add_u32 m0, s83, 19072
v_mfma_f32_16x16x32_bf16 a[48:51], v[20:23], v[52:55], a[48:51]
buffer_load_dwordx4 v234, s[44:47], s80 offen lds
v_mfma_f32_16x16x32_bf16 a[56:59], v[20:23], v[60:63], a[56:59]
v_mfma_f32_16x16x32_bf16 a[56:59], v[16:19], v[56:59], a[56:59]
ds_read_b128 v[16:19], v236 offset:32768
ds_read_b128 v[20:23], v237 offset:32768
v_mfma_f32_16x16x32_bf16 a[60:63], v[24:27], v[56:59], a[60:63]
s_add_u32 m0, s83, 20096
v_mfma_f32_16x16x32_bf16 a[60:63], v[28:31], v[60:63], a[60:63]
buffer_load_dwordx4 v235, s[44:47], s80 offen lds
v_mfma_f32_16x16x32_bf16 a[52:55], v[28:31], v[52:55], a[52:55]
s_add_u32 s80, s80, 524288
v_mfma_f32_16x16x32_bf16 a[52:55], v[24:27], v[48:51], a[52:55]
v_mfma_f32_16x16x32_bf16 a[44:47], v[24:27], v[40:43], a[44:47]
v_mfma_f32_16x16x32_bf16 a[44:47], v[28:31], v[44:47], a[44:47]
v_mfma_f32_16x16x32_bf16 a[252:255], v[28:31], v[36:39], a[252:255]
s_waitcnt lgkmcnt(2)
v_mfma_f32_16x16x32_bf16 a[252:255], v[24:27], v[32:35], a[252:255]
ds_read_b128 v[24:27], v236 offset:34816
ds_read_b128 v[28:31], v237 offset:34816
v_mfma_f32_16x16x32_bf16 a[96:99], v[160:163], v[192:195], a[96:99]
v_mfma_f32_16x16x32_bf16 a[96:99], v[164:167], v[196:199], a[96:99]
v_mfma_f32_16x16x32_bf16 a[104:107], v[164:167], v[204:207], a[104:107]
v_mfma_f32_16x16x32_bf16 a[104:107], v[160:163], v[200:203], a[104:107]
ds_read_b64_tr_b16 v[32:33], v240 offset:33920
ds_read_b64_tr_b16 v[34:35], v240 offset:34944
v_mfma_f32_16x16x32_bf16 a[112:115], v[160:163], v[208:211], a[112:115]
v_mfma_f32_16x16x32_bf16 a[112:115], v[164:167], v[212:215], a[112:115]
v_mfma_f32_16x16x32_bf16 a[128:131], v[164:167], v[220:223], a[128:131]
s_waitcnt lgkmcnt(2)
s_barrier
v_mfma_f32_16x16x32_bf16 a[128:131], v[160:163], v[216:219], a[128:131]
v_mfma_f32_16x16x32_bf16 a[132:135], v[168:171], v[216:219], a[132:135]
s_waitcnt vmcnt(8)
v_cmp_eq_u64 vcc, v[72:73], v[252:253]
v_mfma_f32_16x16x32_bf16 a[132:135], v[172:175], v[220:223], a[132:135]
v_cmp_eq_u64 s[96:97], v[74:75], v[252:253]
v_mfma_f32_16x16x32_bf16 a[116:119], v[172:175], v[212:215], a[116:119]
s_and_b64 vcc, vcc, s[96:97]
s_cmp_eq_u64 vcc, -1
s_nop 0
s_cbranch_scc0 .La5_LV_XSPIN
.La5_LV_XOK:
v_mfma_f32_16x16x32_bf16 a[116:119], v[168:171], v[208:211], a[116:119]
ds_read_b64_tr_b16 v[36:37], v240 offset:50816
ds_read_b64_tr_b16 v[38:39], v240 offset:51840
v_mfma_f32_16x16x32_bf16 a[108:111], v[168:171], v[200:203], a[108:111]
s_mov_b32 s89, 49152
v_mfma_f32_16x16x32_bf16 a[108:111], v[172:175], v[204:207], a[108:111]
v_mfma_f32_16x16x32_bf16 a[100:103], v[172:175], v[196:199], a[100:103]
v_mfma_f32_16x16x32_bf16 a[100:103], v[168:171], v[192:195], a[100:103]
v_mfma_f32_16x16x32_bf16 a[232:235], v[176:179], v[192:195], a[232:235]
v_mfma_f32_16x16x32_bf16 a[232:235], v[180:183], v[196:199], a[232:235]
buffer_load_dwordx4 v[128:131], v244, s[52:55], s91 offen offset:0 sc1
v_mfma_f32_16x16x32_bf16 a[152:155], v[180:183], v[204:207], a[152:155]
v_accvgpr_read_b32 v224, a96
v_accvgpr_read_b32 v225, a97
v_mfma_f32_16x16x32_bf16 a[152:155], v[176:179], v[200:203], a[152:155]
ds_read_b64_tr_b16 v[40:41], v241 offset:33920
ds_read_b64_tr_b16 v[42:43], v241 offset:34944
v_mfma_f32_16x16x32_bf16 a[208:211], v[176:179], v[208:211], a[208:211]
v_accvgpr_read_b32 v226, a98
v_accvgpr_read_b32 v227, a99
v_mfma_f32_16x16x32_bf16 a[208:211], v[180:183], v[212:215], a[208:211]
buffer_load_dwordx4 v[132:135], v245, s[52:55], s91 offen offset:0 sc1
v_mfma_f32_16x16x32_bf16 a[160:163], v[180:183], v[220:223], a[160:163]
v_accvgpr_read_b32 v228, a100
v_accvgpr_read_b32 v229, a101
v_mfma_f32_16x16x32_bf16 a[160:163], v[176:179], v[216:219], a[160:163]
v_accvgpr_read_b32 v230, a102
v_accvgpr_read_b32 v231, a103
v_mfma_f32_16x16x32_bf16 a[164:167], v[184:187], v[216:219], a[164:167]
v_cvt_pk_bf16_f32 v248, v224, v225
v_cvt_pk_bf16_f32 v249, v226, v227
v_mfma_f32_16x16x32_bf16 a[164:167], v[188:191], v[220:223], a[164:167]
buffer_load_dwordx4 v[136:139], v246, s[52:55], s91 offen offset:0 sc1
v_mfma_f32_16x16x32_bf16 a[212:215], v[188:191], v[212:215], a[212:215]
v_cvt_pk_bf16_f32 v250, v228, v229
v_cvt_pk_bf16_f32 v251, v230, v231
v_mfma_f32_16x16x32_bf16 a[212:215], v[184:187], v[208:211], a[212:215]
ds_read_b64_tr_b16 v[44:45], v241 offset:50816
ds_read_b64_tr_b16 v[46:47], v241 offset:51840
v_mfma_f32_16x16x32_bf16 a[156:159], v[184:187], v[200:203], a[156:159]
buffer_store_dwordx4 v[248:251], v255, s[48:51], s89 offen sc1
v_mfma_f32_16x16x32_bf16 a[156:159], v[188:191], v[204:207], a[156:159]
buffer_load_dwordx4 v[140:143], v247, s[52:55], s91 offen offset:0 sc1
v_mfma_f32_16x16x32_bf16 a[236:239], v[188:191], v[196:199], a[236:239]
s_mov_b32 s89, 50176
ds_write_b128 v238, v[248:251] offset:0
v_mfma_f32_16x16x32_bf16 a[236:239], v[184:187], v[192:195], a[236:239]
v_accvgpr_read_b32 v224, a104
v_accvgpr_read_b32 v225, a105
v_mfma_f32_16x16x32_bf16 a[200:203], v[0:3], v[192:195], a[200:203]
v_accvgpr_read_b32 v226, a106
v_accvgpr_read_b32 v227, a107
v_mfma_f32_16x16x32_bf16 a[200:203], v[4:7], v[196:199], a[200:203]
buffer_load_dwordx4 v[144:147], v244, s[52:55], s91 offen offset:1024 sc1
v_mfma_f32_16x16x32_bf16 a[184:187], v[4:7], v[204:207], a[184:187]
v_accvgpr_read_b32 v228, a108
v_accvgpr_read_b32 v229, a109
v_mfma_f32_16x16x32_bf16 a[184:187], v[0:3], v[200:203], a[184:187]
ds_read_b64_tr_b16 v[48:49], v242 offset:33920
ds_read_b64_tr_b16 v[50:51], v242 offset:34944
v_mfma_f32_16x16x32_bf16 a[64:67], v[0:3], v[208:211], a[64:67]
v_accvgpr_read_b32 v230, a110
v_accvgpr_read_b32 v231, a111
v_mfma_f32_16x16x32_bf16 a[64:67], v[4:7], v[212:215], a[64:67]
buffer_load_dwordx4 v[148:151], v245, s[52:55], s91 offen offset:1024 sc1
v_mfma_f32_16x16x32_bf16 a[0:3], v[4:7], v[220:223], a[0:3]
v_cvt_pk_bf16_f32 v248, v224, v225
v_cvt_pk_bf16_f32 v249, v226, v227
v_mfma_f32_16x16x32_bf16 a[0:3], v[0:3], v[216:219], a[0:3]
v_cvt_pk_bf16_f32 v250, v228, v229
v_cvt_pk_bf16_f32 v251, v230, v231
v_mfma_f32_16x16x32_bf16 a[4:7], v[8:11], v[216:219], a[4:7]
buffer_store_dwordx4 v[248:251], v255, s[48:51], s89 offen sc1
v_mfma_f32_16x16x32_bf16 a[4:7], v[12:15], v[220:223], a[4:7]
buffer_load_dwordx4 v[152:155], v246, s[52:55], s91 offen offset:1024 sc1
v_mfma_f32_16x16x32_bf16 a[68:71], v[12:15], v[212:215], a[68:71]
s_mov_b32 s89, 51200
ds_write_b128 v238, v[248:251] offset:1024
v_mfma_f32_16x16x32_bf16 a[68:71], v[8:11], v[208:211], a[68:71]
ds_read_b64_tr_b16 v[52:53], v242 offset:50816
ds_read_b64_tr_b16 v[54:55], v242 offset:51840
v_mfma_f32_16x16x32_bf16 a[188:191], v[8:11], v[200:203], a[188:191]
v_accvgpr_read_b32 v224, a112
v_accvgpr_read_b32 v225, a113
v_mfma_f32_16x16x32_bf16 a[188:191], v[12:15], v[204:207], a[188:191]
buffer_load_dwordx4 v[156:159], v247, s[52:55], s91 offen offset:1024 sc1
v_mfma_f32_16x16x32_bf16 a[204:207], v[12:15], v[196:199], a[204:207]
v_accvgpr_read_b32 v226, a114
v_accvgpr_read_b32 v227, a115
v_mfma_f32_16x16x32_bf16 a[204:207], v[8:11], v[192:195], a[204:207]
ds_read_b64_tr_b16 v[56:57], v243 offset:33920
ds_read_b64_tr_b16 v[58:59], v243 offset:34944
v_mfma_f32_16x16x32_bf16 a[88:91], v[16:19], v[192:195], a[88:91]
v_accvgpr_read_b32 v228, a116
v_accvgpr_read_b32 v229, a117
v_mfma_f32_16x16x32_bf16 a[88:91], v[20:23], v[196:199], a[88:91]
v_accvgpr_read_b32 v230, a118
v_accvgpr_read_b32 v231, a119
v_mfma_f32_16x16x32_bf16 a[24:27], v[20:23], v[204:207], a[24:27]
v_cvt_pk_bf16_f32 v248, v224, v225
v_cvt_pk_bf16_f32 v249, v226, v227
v_mfma_f32_16x16x32_bf16 a[24:27], v[16:19], v[200:203], a[24:27]
ds_read_b64_tr_b16 v[60:61], v243 offset:50816
ds_read_b64_tr_b16 v[62:63], v243 offset:51840
v_mfma_f32_16x16x32_bf16 a[240:243], v[16:19], v[208:211], a[240:243]
v_subrev_u32 v240, 8448, v240
v_subrev_u32 v241, 8448, v241
v_mfma_f32_16x16x32_bf16 a[240:243], v[20:23], v[212:215], a[240:243]
v_subrev_u32 v242, 8448, v242
v_subrev_u32 v243, 8448, v243
v_mfma_f32_16x16x32_bf16 a[32:35], v[20:23], v[220:223], a[32:35]
v_cvt_pk_bf16_f32 v250, v228, v229
v_cvt_pk_bf16_f32 v251, v230, v231
v_mfma_f32_16x16x32_bf16 a[32:35], v[16:19], v[216:219], a[32:35]
buffer_store_dwordx4 v[248:251], v255, s[48:51], s89 offen sc1
v_mfma_f32_16x16x32_bf16 a[36:39], v[24:27], v[216:219], a[36:39]
s_mov_b32 s89, 52224
ds_write_b128 v238, v[248:251] offset:2048
v_mfma_f32_16x16x32_bf16 a[36:39], v[28:31], v[220:223], a[36:39]
v_mfma_f32_16x16x32_bf16 a[244:247], v[28:31], v[212:215], a[244:247]
v_mfma_f32_16x16x32_bf16 a[244:247], v[24:27], v[208:211], a[244:247]
v_mfma_f32_16x16x32_bf16 a[28:31], v[24:27], v[200:203], a[28:31]
v_mfma_f32_16x16x32_bf16 a[28:31], v[28:31], v[204:207], a[28:31]
v_mfma_f32_16x16x32_bf16 a[92:95], v[28:31], v[196:199], a[92:95]
s_waitcnt vmcnt(11) lgkmcnt(1)
s_barrier
v_mfma_f32_16x16x32_bf16 a[92:95], v[24:27], v[192:195], a[92:95]
v_mfma_f32_16x16x32_bf16 a[120:123], v[160:163], v[32:35], a[120:123]
s_add_u32 m0, s83, 33792
v_mfma_f32_16x16x32_bf16 a[120:123], v[164:167], v[36:39], a[120:123]
buffer_load_dwordx4 v232, s[44:47], s80 offen lds
v_mfma_f32_16x16x32_bf16 a[136:139], v[164:167], v[44:47], a[136:139]
v_mfma_f32_16x16x32_bf16 a[136:139], v[160:163], v[40:43], a[136:139]
.La5_LW1_8:
s_cbranch_execz .La5_LW1_12
ds_read_b64_tr_b16 v[192:193], v240 offset:0
ds_read_b64_tr_b16 v[194:195], v240 offset:1024
v_mfma_f32_16x16x32_bf16 a[224:227], v[160:163], v[48:51], a[224:227]
s_add_u32 m0, s83, 34816
v_mfma_f32_16x16x32_bf16 a[224:227], v[164:167], v[52:55], a[224:227]
buffer_load_dwordx4 v233, s[44:47], s80 offen lds
v_mfma_f32_16x16x32_bf16 a[144:147], v[164:167], v[60:63], a[144:147]
v_mfma_f32_16x16x32_bf16 a[144:147], v[160:163], v[56:59], a[144:147]
ds_read_b64_tr_b16 v[196:197], v240 offset:8448
ds_read_b64_tr_b16 v[198:199], v240 offset:9472
v_mfma_f32_16x16x32_bf16 a[148:151], v[168:171], v[56:59], a[148:151]
s_add_u32 m0, s83, 35968
v_mfma_f32_16x16x32_bf16 a[148:151], v[172:175], v[60:63], a[148:151]
buffer_load_dwordx4 v234, s[44:47], s80 offen lds
v_mfma_f32_16x16x32_bf16 a[228:231], v[172:175], v[52:55], a[228:231]
v_mfma_f32_16x16x32_bf16 a[228:231], v[168:171], v[48:51], a[228:231]
ds_read_b64_tr_b16 v[200:201], v241 offset:0
ds_read_b64_tr_b16 v[202:203], v241 offset:1024
v_mfma_f32_16x16x32_bf16 a[140:143], v[168:171], v[40:43], a[140:143]
s_add_u32 m0, s83, 36992
v_mfma_f32_16x16x32_bf16 a[140:143], v[172:175], v[44:47], a[140:143]
buffer_load_dwordx4 v235, s[44:47], s80 offen lds
v_mfma_f32_16x16x32_bf16 a[124:127], v[172:175], v[36:39], a[124:127]
s_add_u32 s80, s80, 524288
v_mfma_f32_16x16x32_bf16 a[124:127], v[168:171], v[32:35], a[124:127]
ds_read_b64_tr_b16 v[204:205], v241 offset:8448
ds_read_b64_tr_b16 v[206:207], v241 offset:9472
v_mfma_f32_16x16x32_bf16 a[216:219], v[176:179], v[32:35], a[216:219]
s_add_u32 m0, s83, 50688
v_mfma_f32_16x16x32_bf16 a[216:219], v[180:183], v[36:39], a[216:219]
buffer_load_dwordx4 v232, s[44:47], s80 offen lds
v_mfma_f32_16x16x32_bf16 a[168:171], v[180:183], v[44:47], a[168:171]
v_mfma_f32_16x16x32_bf16 a[168:171], v[176:179], v[40:43], a[168:171]
ds_read_b64_tr_b16 v[208:209], v242 offset:0
ds_read_b64_tr_b16 v[210:211], v242 offset:1024
v_mfma_f32_16x16x32_bf16 a[192:195], v[176:179], v[48:51], a[192:195]
s_add_u32 m0, s83, 51712
v_mfma_f32_16x16x32_bf16 a[192:195], v[180:183], v[52:55], a[192:195]
buffer_load_dwordx4 v233, s[44:47], s80 offen lds
v_mfma_f32_16x16x32_bf16 a[176:179], v[180:183], v[60:63], a[176:179]
v_mfma_f32_16x16x32_bf16 a[176:179], v[176:179], v[56:59], a[176:179]
ds_read_b64_tr_b16 v[212:213], v242 offset:8448
ds_read_b64_tr_b16 v[214:215], v242 offset:9472
v_mfma_f32_16x16x32_bf16 a[180:183], v[184:187], v[56:59], a[180:183]
s_add_u32 m0, s83, 52864
v_mfma_f32_16x16x32_bf16 a[180:183], v[188:191], v[60:63], a[180:183]
buffer_load_dwordx4 v234, s[44:47], s80 offen lds
v_mfma_f32_16x16x32_bf16 a[196:199], v[188:191], v[52:55], a[196:199]
v_mfma_f32_16x16x32_bf16 a[196:199], v[184:187], v[48:51], a[196:199]
ds_read_b64_tr_b16 v[216:217], v243 offset:0
ds_read_b64_tr_b16 v[218:219], v243 offset:1024
v_mfma_f32_16x16x32_bf16 a[172:175], v[184:187], v[40:43], a[172:175]
s_add_u32 m0, s83, 53888
v_mfma_f32_16x16x32_bf16 a[172:175], v[188:191], v[44:47], a[172:175]
buffer_load_dwordx4 v235, s[44:47], s80 offen lds
v_mfma_f32_16x16x32_bf16 a[220:223], v[188:191], v[36:39], a[220:223]
s_add_u32 s80, s80, 524288
v_mfma_f32_16x16x32_bf16 a[220:223], v[184:187], v[32:35], a[220:223]
ds_read_b64_tr_b16 v[220:221], v243 offset:8448
ds_read_b64_tr_b16 v[222:223], v243 offset:9472
v_mfma_f32_16x16x32_bf16 a[72:75], v[0:3], v[32:35], a[72:75]
v_mfma_f32_16x16x32_bf16 a[72:75], v[4:7], v[36:39], a[72:75]
buffer_load_dwordx4 v[160:163], v244, s[52:55], s91 offen offset:2048 sc1
v_mfma_f32_16x16x32_bf16 a[8:11], v[4:7], v[44:47], a[8:11]
v_mfma_f32_16x16x32_bf16 a[8:11], v[0:3], v[40:43], a[8:11]
v_mfma_f32_16x16x32_bf16 a[80:83], v[0:3], v[48:51], a[80:83]
v_mfma_f32_16x16x32_bf16 a[80:83], v[4:7], v[52:55], a[80:83]
buffer_load_dwordx4 v[164:167], v245, s[52:55], s91 offen offset:2048 sc1
v_mfma_f32_16x16x32_bf16 a[16:19], v[4:7], v[60:63], a[16:19]
v_mfma_f32_16x16x32_bf16 a[16:19], v[0:3], v[56:59], a[16:19]
v_mfma_f32_16x16x32_bf16 a[20:23], v[8:11], v[56:59], a[20:23]
v_mfma_f32_16x16x32_bf16 a[20:23], v[12:15], v[60:63], a[20:23]
buffer_load_dwordx4 v[168:171], v246, s[52:55], s91 offen offset:2048 sc1
v_mfma_f32_16x16x32_bf16 a[84:87], v[12:15], v[52:55], a[84:87]
v_mfma_f32_16x16x32_bf16 a[84:87], v[8:11], v[48:51], a[84:87]
v_mfma_f32_16x16x32_bf16 a[12:15], v[8:11], v[40:43], a[12:15]
v_mfma_f32_16x16x32_bf16 a[12:15], v[12:15], v[44:47], a[12:15]
buffer_load_dwordx4 v[172:175], v247, s[52:55], s91 offen offset:2048 sc1
v_mfma_f32_16x16x32_bf16 a[76:79], v[12:15], v[36:39], a[76:79]
v_mfma_f32_16x16x32_bf16 a[76:79], v[8:11], v[32:35], a[76:79]
v_mfma_f32_16x16x32_bf16 a[248:251], v[16:19], v[32:35], a[248:251]
v_mfma_f32_16x16x32_bf16 a[248:251], v[20:23], v[36:39], a[248:251]
v_mfma_f32_16x16x32_bf16 a[40:43], v[20:23], v[44:47], a[40:43]
v_mfma_f32_16x16x32_bf16 a[40:43], v[16:19], v[40:43], a[40:43]
v_mfma_f32_16x16x32_bf16 a[48:51], v[16:19], v[48:51], a[48:51]
v_mfma_f32_16x16x32_bf16 a[48:51], v[20:23], v[52:55], a[48:51]
v_mfma_f32_16x16x32_bf16 a[56:59], v[20:23], v[60:63], a[56:59]
v_mfma_f32_16x16x32_bf16 a[56:59], v[16:19], v[56:59], a[56:59]
v_mfma_f32_16x16x32_bf16 a[60:63], v[24:27], v[56:59], a[60:63]
v_mfma_f32_16x16x32_bf16 a[60:63], v[28:31], v[60:63], a[60:63]
v_mfma_f32_16x16x32_bf16 a[52:55], v[28:31], v[52:55], a[52:55]
v_mfma_f32_16x16x32_bf16 a[52:55], v[24:27], v[48:51], a[52:55]
v_mfma_f32_16x16x32_bf16 a[44:47], v[24:27], v[40:43], a[44:47]
v_mfma_f32_16x16x32_bf16 a[44:47], v[28:31], v[44:47], a[44:47]
v_mfma_f32_16x16x32_bf16 a[252:255], v[28:31], v[36:39], a[252:255]
v_mfma_f32_16x16x32_bf16 a[252:255], v[24:27], v[32:35], a[252:255]
; ---- Q tile: 256 rows x 128 columns, K = 1024, while K / V leave the accumulators (memory + .La5_LDS ring); behind it the first step on the diagonal
.La5_LQ_START:
.La5_LW1_9:
s_cbranch_execz .La5_LW1_13
s_waitcnt vmcnt(13) lgkmcnt(4)
v_mfma_f32_16x16x32_bf16 v[0:3], v[192:195], v[128:131], 0
v_accvgpr_read_b32 v224, a128
v_accvgpr_read_b32 v225, a129
s_mov_b32 s85, s95
v_mfma_f32_16x16x32_bf16 v[0:3], v[196:199], v[144:147], v[0:3]
buffer_load_dwordx4 v[176:179], v244, s[52:55], s91 offen offset:3072 sc1
v_mfma_f32_16x16x32_bf16 v[32:35], v[196:199], v[148:151], 0
v_accvgpr_read_b32 v226, a130
v_accvgpr_read_b32 v227, a131
v_mfma_f32_16x16x32_bf16 v[32:35], v[192:195], v[132:135], v[32:35]
v_accvgpr_read_b32 v228, a132
v_accvgpr_read_b32 v229, a133
v_mfma_f32_16x16x32_bf16 v[64:67], v[192:195], v[136:139], 0
v_accvgpr_read_b32 v230, a134
v_accvgpr_read_b32 v231, a135
v_mfma_f32_16x16x32_bf16 v[64:67], v[196:199], v[152:155], v[64:67]
buffer_load_dwordx4 v[180:183], v245, s[52:55], s91 offen offset:3072 sc1
v_mfma_f32_16x16x32_bf16 v[96:99], v[196:199], v[156:159], 0
v_cvt_pk_bf16_f32 v248, v224, v225
v_cvt_pk_bf16_f32 v249, v226, v227
v_mfma_f32_16x16x32_bf16 v[96:99], v[192:195], v[140:143], v[96:99]
ds_read_b64_tr_b16 v[196:197], v240 offset:128
ds_read_b64_tr_b16 v[198:199], v240 offset:1152
v_mfma_f32_16x16x32_bf16 v[100:103], v[200:203], v[140:143], 0
v_cvt_pk_bf16_f32 v250, v228, v229
v_cvt_pk_bf16_f32 v251, v230, v231
v_mfma_f32_16x16x32_bf16 v[100:103], v[204:207], v[156:159], v[100:103]
buffer_load_dwordx4 v[184:187], v246, s[52:55], s91 offen offset:3072 sc1
v_mfma_f32_16x16x32_bf16 v[68:71], v[204:207], v[152:155], 0
v_accvgpr_write_b32 a128, v248
v_accvgpr_write_b32 a129, v249
v_mfma_f32_16x16x32_bf16 v[68:71], v[200:203], v[136:139], v[68:71]
ds_read_b64_tr_b16 v[192:193], v240 offset:8576
ds_read_b64_tr_b16 v[194:195], v240 offset:9600
v_mfma_f32_16x16x32_bf16 v[36:39], v[200:203], v[132:135], 0
v_accvgpr_write_b32 a130, v250
v_accvgpr_write_b32 a131, v251
v_mfma_f32_16x16x32_bf16 v[36:39], v[204:207], v[148:151], v[36:39]
buffer_load_dwordx4 v[188:191], v247, s[52:55], s91 offen offset:3072 sc1
v_mfma_f32_16x16x32_bf16 v[4:7], v[204:207], v[144:147], 0
s_waitcnt lgkmcnt(4)
s_add_u32 s91, s91, 4096
v_mfma_f32_16x16x32_bf16 v[4:7], v[200:203], v[128:131], v[4:7]
ds_read_b64_tr_b16 v[204:205], v241 offset:128
ds_read_b64_tr_b16 v[206:207], v241 offset:1152
v_mfma_f32_16x16x32_bf16 v[8:11], v[208:211], v[128:131], 0
s_and_b32 s97, s2, 7
s_mul_i32 s95, s97, 7
v_mfma_f32_16x16x32_bf16 v[8:11], v[212:215], v[144:147], v[8:11]
buffer_load_dwordx4 a[96:99], v244, s[52:55], s91 offen offset:0 sc1
v_mfma_f32_16x16x32_bf16 v[40:43], v[212:215], v[148:151], 0
s_lshr_b32 s96, s30, 7
s_add_u32 s95, s95, s96
v_mfma_f32_16x16x32_bf16 v[40:43], v[208:211], v[132:135], v[40:43]
ds_read_b64_tr_b16 v[200:201], v241 offset:8576
ds_read_b64_tr_b16 v[202:203], v241 offset:9600
v_mfma_f32_16x16x32_bf16 v[72:75], v[208:211], v[136:139], 0
s_sub_u32 s95, 3, s95
s_and_b32 s95, s95, 7
v_mfma_f32_16x16x32_bf16 v[72:75], v[212:215], v[152:155], v[72:75]
buffer_load_dwordx4 a[100:103], v245, s[52:55], s91 offen offset:0 sc1
v_mfma_f32_16x16x32_bf16 v[104:107], v[212:215], v[156:159], 0
s_min_u32 s95, s95, 6
s_lshl_b32 s95, s95, 1
v_mfma_f32_16x16x32_bf16 v[104:107], v[208:211], v[140:143], v[104:107]
ds_read_b64_tr_b16 v[212:213], v242 offset:128
ds_read_b64_tr_b16 v[214:215], v242 offset:1152
v_mfma_f32_16x16x32_bf16 v[108:111], v[216:219], v[140:143], 0
s_lshr_b32 s92, s2, 6
s_mul_i32 s93, s92, 3
v_mfma_f32_16x16x32_bf16 v[108:111], v[220:223], v[156:159], v[108:111]
buffer_load_dwordx4 a[104:107], v246, s[52:55], s91 offen offset:0 sc1
v_mfma_f32_16x16x32_bf16 v[76:79], v[220:223], v[152:155], 0
s_add_u32 s93, s93, s74
v_mfma_f32_16x16x32_bf16 v[76:79], v[216:219], v[136:139], v[76:79]
ds_read_b64_tr_b16 v[208:209], v242 offset:8576
ds_read_b64_tr_b16 v[210:211], v242 offset:9600
v_mfma_f32_16x16x32_bf16 v[44:47], v[216:219], v[132:135], 0
ds_write_b128 v238, v[248:251] offset:3072
v_mfma_f32_16x16x32_bf16 v[44:47], v[220:223], v[148:151], v[44:47]
buffer_store_dwordx4 v[248:251], v255, s[48:51], s89 offen offset:0 sc1
v_mfma_f32_16x16x32_bf16 v[12:15], v[220:223], v[144:147], 0
s_waitcnt lgkmcnt(5)
v_mfma_f32_16x16x32_bf16 v[12:15], v[216:219], v[128:131], v[12:15]
ds_read_b64_tr_b16 v[220:221], v243 offset:128
ds_read_b64_tr_b16 v[222:223], v243 offset:1152
v_mfma_f32_16x16x32_bf16 v[16:19], v[196:199], v[128:131], 0
v_accvgpr_read_b32 v224, a120
v_accvgpr_read_b32 v225, a121
v_mfma_f32_16x16x32_bf16 v[16:19], v[192:195], v[144:147], v[16:19]
buffer_load_dwordx4 a[108:111], v247, s[52:55], s91 offen offset:0 sc1
v_mfma_f32_16x16x32_bf16 v[48:51], v[192:195], v[148:151], 0
v_accvgpr_read_b32 v226, a122
v_accvgpr_read_b32 v227, a123
v_mfma_f32_16x16x32_bf16 v[48:51], v[196:199], v[132:135], v[48:51]
ds_read_b64_tr_b16 v[216:217], v243 offset:8576
ds_read_b64_tr_b16 v[218:219], v243 offset:9600
v_mfma_f32_16x16x32_bf16 v[80:83], v[196:199], v[136:139], 0
v_accvgpr_read_b32 v228, a124
v_accvgpr_read_b32 v229, a125
v_mfma_f32_16x16x32_bf16 v[80:83], v[192:195], v[152:155], v[80:83]
buffer_load_dwordx4 a[112:115], v244, s[52:55], s91 offen offset:1024 sc1
v_mfma_f32_16x16x32_bf16 v[112:115], v[192:195], v[156:159], 0
v_accvgpr_read_b32 v230, a126
v_accvgpr_read_b32 v231, a127
v_mfma_f32_16x16x32_bf16 v[112:115], v[196:199], v[140:143], v[112:115]
v_cvt_pk_bf16_f32 v248, v224, v225
v_cvt_pk_bf16_f32 v249, v226, v227
v_mfma_f32_16x16x32_bf16 v[116:119], v[204:207], v[140:143], 0
s_waitcnt lgkmcnt(0)
s_barrier
v_mfma_f32_16x16x32_bf16 v[116:119], v[200:203], v[156:159], v[116:119]
buffer_load_dwordx4 a[116:119], v245, s[52:55], s91 offen offset:1024 sc1
v_mfma_f32_16x16x32_bf16 v[84:87], v[200:203], v[152:155], 0
ds_read_b64_tr_b16 v[192:193], v240 offset:16896
ds_read_b64_tr_b16 v[194:195], v240 offset:17920
v_mfma_f32_16x16x32_bf16 v[84:87], v[204:207], v[136:139], v[84:87]
ds_read_b64_tr_b16 v[196:197], v240 offset:25344
ds_read_b64_tr_b16 v[198:199], v240 offset:26368
v_mfma_f32_16x16x32_bf16 v[52:55], v[204:207], v[132:135], 0
s_add_u32 m0, s83, 0
v_cvt_pk_bf16_f32 v250, v228, v229
v_mfma_f32_16x16x32_bf16 v[52:55], v[200:203], v[148:151], v[52:55]
buffer_load_dwordx4 v232, s[44:47], s80 offen lds
v_mfma_f32_16x16x32_bf16 v[20:23], v[200:203], v[144:147], 0
v_cvt_pk_bf16_f32 v251, v230, v231
v_accvgpr_write_b32 a132, v248
v_mfma_f32_16x16x32_bf16 v[20:23], v[204:207], v[128:131], v[20:23]
ds_read_b64_tr_b16 v[200:201], v241 offset:16896
ds_read_b64_tr_b16 v[202:203], v241 offset:17920
v_mfma_f32_16x16x32_bf16 v[24:27], v[212:215], v[128:131], 0
v_accvgpr_write_b32 a133, v249
v_accvgpr_write_b32 a134, v250
v_mfma_f32_16x16x32_bf16 v[24:27], v[208:211], v[144:147], v[24:27]
v_accvgpr_write_b32 a135, v251
v_mfma_f32_16x16x32_bf16 v[56:59], v[208:211], v[148:151], 0
s_cmp_ge_u32 s93, s95
s_cselect_b32 s96, 2, 0
v_mfma_f32_16x16x32_bf16 v[56:59], v[212:215], v[132:135], v[56:59]
ds_read_b64_tr_b16 v[204:205], v241 offset:25344
ds_read_b64_tr_b16 v[206:207], v241 offset:26368
v_mfma_f32_16x16x32_bf16 v[88:91], v[212:215], v[136:139], 0
s_add_u32 m0, s83, 1024
s_add_u32 s93, s93, s96
v_mfma_f32_16x16x32_bf16 v[88:91], v[208:211], v[152:155], v[88:91]
buffer_load_dwordx4 v233, s[44:47], s80 offen lds
v_mfma_f32_16x16x32_bf16 v[120:123], v[208:211], v[156:159], 0
s_add_u32 s95, s95, s92
v_mfma_f32_16x16x32_bf16 v[120:123], v[212:215], v[140:143], v[120:123]
ds_read_b64_tr_b16 v[208:209], v242 offset:16896
ds_read_b64_tr_b16 v[210:211], v242 offset:17920
v_mfma_f32_16x16x32_bf16 v[124:127], v[220:223], v[140:143], 0
s_cmp_lt_u32 s92, 2
s_cselect_b32 s94, s75, 0
v_mfma_f32_16x16x32_bf16 v[124:127], v[216:219], v[156:159], v[124:127]
buffer_load_dwordx4 v[140:143], v246, s[52:55], s91 offen offset:1024 sc1
v_mfma_f32_16x16x32_bf16 v[92:95], v[216:219], v[152:155], 0
s_cmp_eq_u32 s74, 3
s_cselect_b32 s92, s95, s93
s_cselect_b32 s94, s94, s75
v_mfma_f32_16x16x32_bf16 v[92:95], v[220:223], v[136:139], v[92:95]
ds_read_b64_tr_b16 v[212:213], v242 offset:25344
ds_read_b64_tr_b16 v[214:215], v242 offset:26368
v_mfma_f32_16x16x32_bf16 v[60:63], v[220:223], v[132:135], 0
ds_write_b128 v238, v[248:251] offset:4096
v_mfma_f32_16x16x32_bf16 v[60:63], v[216:219], v[148:151], v[60:63]
s_mul_i32 s93, s97, 14
s_add_u32 s92, s92, s93
v_mfma_f32_16x16x32_bf16 v[28:31], v[216:219], v[144:147], 0
s_waitcnt vmcnt(10) lgkmcnt(5)
s_mul_i32 s96, s92, s94
v_mfma_f32_16x16x32_bf16 v[28:31], v[220:223], v[128:131], v[28:31]
ds_read_b64_tr_b16 v[216:217], v243 offset:16896
ds_read_b64_tr_b16 v[218:219], v243 offset:17920
v_mfma_f32_16x16x32_bf16 v[0:3], v[192:195], v[160:163], v[0:3]
v_accvgpr_read_b32 v224, a136
v_accvgpr_read_b32 v225, a137
v_mfma_f32_16x16x32_bf16 v[0:3], v[196:199], v[176:179], v[0:3]
buffer_load_dwordx4 v[128:131], v247, s[52:55], s91 offen offset:1024 sc1
v_mfma_f32_16x16x32_bf16 v[32:35], v[196:199], v[180:183], v[32:35]
v_accvgpr_read_b32 v226, a138
v_accvgpr_read_b32 v227, a139
v_mfma_f32_16x16x32_bf16 v[32:35], v[192:195], v[164:167], v[32:35]
ds_read_b64_tr_b16 v[220:221], v243 offset:25344
ds_read_b64_tr_b16 v[222:223], v243 offset:26368
v_mfma_f32_16x16x32_bf16 v[64:67], v[192:195], v[168:171], v[64:67]
v_accvgpr_read_b32 v228, a140
v_accvgpr_read_b32 v229, a141
v_mfma_f32_16x16x32_bf16 v[64:67], v[196:199], v[184:187], v[64:67]
buffer_load_dwordx4 v[132:135], v244, s[52:55], s91 offen offset:2048 sc1
v_mfma_f32_16x16x32_bf16 v[96:99], v[196:199], v[188:191], v[96:99]
v_accvgpr_read_b32 v230, a142
v_accvgpr_read_b32 v231, a143
v_mfma_f32_16x16x32_bf16 v[96:99], v[192:195], v[172:175], v[96:99]
ds_read_b64_tr_b16 v[196:197], v240 offset:17024
ds_read_b64_tr_b16 v[198:199], v240 offset:18048
v_mfma_f32_16x16x32_bf16 v[100:103], v[200:203], v[172:175], v[100:103]
s_add_u32 m0, s83, 2176
v_cvt_pk_bf16_f32 v248, v224, v225
v_mfma_f32_16x16x32_bf16 v[100:103], v[204:207], v[188:191], v[100:103]
buffer_load_dwordx4 v234, s[44:47], s80 offen lds
v_mfma_f32_16x16x32_bf16 v[68:71], v[204:207], v[184:187], v[68:71]
v_cvt_pk_bf16_f32 v249, v226, v227
v_cvt_pk_bf16_f32 v250, v228, v229
v_mfma_f32_16x16x32_bf16 v[68:71], v[200:203], v[168:171], v[68:71]
ds_read_b64_tr_b16 v[192:193], v240 offset:25472
ds_read_b64_tr_b16 v[194:195], v240 offset:26496
v_mfma_f32_16x16x32_bf16 v[36:39], v[200:203], v[164:167], v[36:39]
v_cvt_pk_bf16_f32 v251, v230, v231
v_accvgpr_write_b32 a136, v248
v_mfma_f32_16x16x32_bf16 v[36:39], v[204:207], v[180:183], v[36:39]
buffer_load_dwordx4 v[136:139], v245, s[52:55], s91 offen offset:2048 sc1
v_mfma_f32_16x16x32_bf16 v[4:7], v[204:207], v[176:179], v[4:7]
s_waitcnt lgkmcnt(4)
v_accvgpr_write_b32 a137, v249
v_mfma_f32_16x16x32_bf16 v[4:7], v[200:203], v[160:163], v[4:7]
ds_read_b64_tr_b16 v[204:205], v241 offset:17024
ds_read_b64_tr_b16 v[206:207], v241 offset:18048
v_mfma_f32_16x16x32_bf16 v[8:11], v[208:211], v[160:163], v[8:11]
s_add_u32 m0, s83, 3200
v_accvgpr_write_b32 a138, v250
v_mfma_f32_16x16x32_bf16 v[8:11], v[212:215], v[176:179], v[8:11]
buffer_load_dwordx4 v235, s[44:47], s80 offen lds
v_mfma_f32_16x16x32_bf16 v[40:43], v[212:215], v[180:183], v[40:43]
s_add_u32 s80, s80, 524288
v_accvgpr_write_b32 a139, v251
v_mfma_f32_16x16x32_bf16 v[40:43], v[208:211], v[164:167], v[40:43]
ds_read_b64_tr_b16 v[200:201], v241 offset:25472
ds_read_b64_tr_b16 v[202:203], v241 offset:26496
v_mfma_f32_16x16x32_bf16 v[72:75], v[208:211], v[168:171], v[72:75]
s_mov_b32 s100, s96
s_lshr_b32 s92, s2, 3
v_mfma_f32_16x16x32_bf16 v[72:75], v[212:215], v[184:187], v[72:75]
buffer_load_dwordx4 v[144:147], v246, s[52:55], s91 offen offset:2048 sc1
v_mfma_f32_16x16x32_bf16 v[104:107], v[212:215], v[188:191], v[104:107]
s_andn2_b32 s92, s92, 1
s_and_b32 s93, s2, 7
v_mfma_f32_16x16x32_bf16 v[104:107], v[208:211], v[172:175], v[104:107]
ds_read_b64_tr_b16 v[212:213], v242 offset:17024
ds_read_b64_tr_b16 v[214:215], v242 offset:18048
v_mfma_f32_16x16x32_bf16 v[108:111], v[216:219], v[172:175], v[108:111]
s_lshl_b32 s95, s92, 16
s_lshr_b32 s97, s93, 1
v_mfma_f32_16x16x32_bf16 v[108:111], v[220:223], v[188:191], v[108:111]
buffer_load_dwordx4 v[148:151], v247, s[52:55], s91 offen offset:2048 sc1
v_mfma_f32_16x16x32_bf16 v[76:79], v[220:223], v[184:187], v[76:79]
s_mov_b32 s18, 53248
s_lshl_b32 s97, s97, 21
v_mfma_f32_16x16x32_bf16 v[76:79], v[216:219], v[168:171], v[76:79]
ds_read_b64_tr_b16 v[208:209], v242 offset:25472
ds_read_b64_tr_b16 v[210:211], v242 offset:26496
v_mfma_f32_16x16x32_bf16 v[44:47], v[216:219], v[164:167], v[44:47]
ds_write_b128 v238, v[248:251] offset:5120
v_mfma_f32_16x16x32_bf16 v[44:47], v[220:223], v[180:183], v[44:47]
buffer_store_dwordx4 a[132:135], v255, s[48:51], s18 offen offset:0 sc1
v_mfma_f32_16x16x32_bf16 v[12:15], v[220:223], v[176:179], v[12:15]
s_waitcnt lgkmcnt(5)
s_add_u32 s95, s95, s97
v_mfma_f32_16x16x32_bf16 v[12:15], v[216:219], v[160:163], v[12:15]
ds_read_b64_tr_b16 v[220:221], v243 offset:17024
ds_read_b64_tr_b16 v[222:223], v243 offset:18048
v_mfma_f32_16x16x32_bf16 v[16:19], v[196:199], v[160:163], v[16:19]
v_accvgpr_read_b32 v224, a224
v_accvgpr_read_b32 v225, a225
v_mfma_f32_16x16x32_bf16 v[16:19], v[192:195], v[176:179], v[16:19]
buffer_load_dwordx4 v[152:155], v244, s[52:55], s91 offen offset:3072 sc1
v_mfma_f32_16x16x32_bf16 v[48:51], v[192:195], v[180:183], v[48:51]
v_accvgpr_read_b32 v226, a226
v_accvgpr_read_b32 v227, a227
v_mfma_f32_16x16x32_bf16 v[48:51], v[196:199], v[164:167], v[48:51]
ds_read_b64_tr_b16 v[216:217], v243 offset:25472
ds_read_b64_tr_b16 v[218:219], v243 offset:26496
v_mfma_f32_16x16x32_bf16 v[80:83], v[196:199], v[168:171], v[80:83]
v_accvgpr_read_b32 v228, a228
v_accvgpr_read_b32 v229, a229
v_mfma_f32_16x16x32_bf16 v[80:83], v[192:195], v[184:187], v[80:83]
buffer_load_dwordx4 v[156:159], v245, s[52:55], s91 offen offset:3072 sc1
v_mfma_f32_16x16x32_bf16 v[112:115], v[192:195], v[188:191], v[112:115]
v_accvgpr_read_b32 v230, a230
v_accvgpr_read_b32 v231, a231
v_mfma_f32_16x16x32_bf16 v[112:115], v[196:199], v[172:175], v[112:115]
v_cvt_pk_bf16_f32 v248, v224, v225
v_cvt_pk_bf16_f32 v249, v226, v227
v_mfma_f32_16x16x32_bf16 v[116:119], v[204:207], v[172:175], v[116:119]
s_waitcnt lgkmcnt(0)
s_barrier
v_mfma_f32_16x16x32_bf16 v[116:119], v[200:203], v[188:191], v[116:119]
buffer_load_dwordx4 a[120:123], v246, s[52:55], s91 offen offset:3072 sc1
v_mfma_f32_16x16x32_bf16 v[84:87], v[200:203], v[184:187], v[84:87]
ds_read_b64_tr_b16 v[192:193], v240 offset:33792
ds_read_b64_tr_b16 v[194:195], v240 offset:34816
v_mfma_f32_16x16x32_bf16 v[84:87], v[204:207], v[168:171], v[84:87]
ds_read_b64_tr_b16 v[196:197], v240 offset:42240
ds_read_b64_tr_b16 v[198:199], v240 offset:43264
v_mfma_f32_16x16x32_bf16 v[52:55], v[204:207], v[164:167], v[52:55]
s_add_u32 m0, s83, 16896
v_cvt_pk_bf16_f32 v250, v228, v229
v_mfma_f32_16x16x32_bf16 v[52:55], v[200:203], v[180:183], v[52:55]
buffer_load_dwordx4 v232, s[44:47], s80 offen lds
v_mfma_f32_16x16x32_bf16 v[20:23], v[200:203], v[176:179], v[20:23]
v_cvt_pk_bf16_f32 v251, v230, v231
v_accvgpr_write_b32 a140, v248
v_mfma_f32_16x16x32_bf16 v[20:23], v[204:207], v[160:163], v[20:23]
ds_read_b64_tr_b16 v[200:201], v241 offset:33792
ds_read_b64_tr_b16 v[202:203], v241 offset:34816
v_mfma_f32_16x16x32_bf16 v[24:27], v[212:215], v[160:163], v[24:27]
v_accvgpr_write_b32 a141, v249
v_accvgpr_write_b32 a142, v250
v_mfma_f32_16x16x32_bf16 v[24:27], v[208:211], v[176:179], v[24:27]
buffer_load_dwordx4 a[124:127], v247, s[52:55], s91 offen offset:3072 sc1
v_mfma_f32_16x16x32_bf16 v[56:59], v[208:211], v[180:183], v[56:59]
s_add_u32 s91, s91, 4096
v_accvgpr_write_b32 a143, v251
v_mfma_f32_16x16x32_bf16 v[56:59], v[212:215], v[164:167], v[56:59]
ds_read_b64_tr_b16 v[204:205], v241 offset:42240
ds_read_b64_tr_b16 v[206:207], v241 offset:43264
v_mfma_f32_16x16x32_bf16 v[88:91], v[212:215], v[168:171], v[88:91]
s_add_u32 m0, s83, 17920
s_and_b32 s97, s93, 1
v_mfma_f32_16x16x32_bf16 v[88:91], v[208:211], v[184:187], v[88:91]
buffer_load_dwordx4 v233, s[44:47], s80 offen lds
v_mfma_f32_16x16x32_bf16 v[120:123], v[208:211], v[188:191], v[120:123]
s_lshl_b32 s97, s97, 10
s_add_u32 s95, s95, s97
v_mfma_f32_16x16x32_bf16 v[120:123], v[212:215], v[172:175], v[120:123]
ds_read_b64_tr_b16 v[208:209], v242 offset:33792
ds_read_b64_tr_b16 v[210:211], v242 offset:34816
v_mfma_f32_16x16x32_bf16 v[124:127], v[220:223], v[172:175], v[124:127]
s_mul_i32 s97, s92, 229376
s_mul_i32 s38, s93, 896
v_mfma_f32_16x16x32_bf16 v[124:127], v[216:219], v[188:191], v[124:127]
s_add_u32 s97, s97, s38
v_mfma_f32_16x16x32_bf16 v[92:95], v[216:219], v[184:187], v[92:95]
s_cmp_eq_u32 s3, 0
s_cselect_b32 s95, s95, s97
s_cselect_b64 s[36:37], s[28:29], s[30:31]
s_cselect_b32 s21, 2, 7
v_mfma_f32_16x16x32_bf16 v[92:95], v[220:223], v[168:171], v[92:95]
ds_read_b64_tr_b16 v[212:213], v242 offset:42240
ds_read_b64_tr_b16 v[214:215], v242 offset:43264
v_mfma_f32_16x16x32_bf16 v[60:63], v[220:223], v[164:167], v[60:63]
ds_write_b128 v238, v[248:251] offset:6144
v_mfma_f32_16x16x32_bf16 v[60:63], v[216:219], v[180:183], v[60:63]
buffer_store_dwordx4 a[136:139], v255, s[48:51], s18 offen offset:1024 sc1
v_mfma_f32_16x16x32_bf16 v[28:31], v[216:219], v[176:179], v[28:31]
s_waitcnt vmcnt(14) lgkmcnt(5)
v_mfma_f32_16x16x32_bf16 v[28:31], v[220:223], v[160:163], v[28:31]
ds_read_b64_tr_b16 v[216:217], v243 offset:33792
ds_read_b64_tr_b16 v[218:219], v243 offset:34816
v_mfma_f32_16x16x32_bf16 v[0:3], v[192:195], a[96:99], v[0:3]
v_accvgpr_read_b32 v224, a144
v_accvgpr_read_b32 v225, a145
v_mfma_f32_16x16x32_bf16 v[0:3], v[196:199], a[112:115], v[0:3]
buffer_load_dwordx4 v[160:163], v244, s[52:55], s91 offen offset:0 sc1
v_mfma_f32_16x16x32_bf16 v[32:35], v[196:199], a[116:119], v[32:35]
v_accvgpr_read_b32 v226, a146
v_accvgpr_read_b32 v227, a147
v_mfma_f32_16x16x32_bf16 v[32:35], v[192:195], a[100:103], v[32:35]
ds_read_b64_tr_b16 v[220:221], v243 offset:42240
ds_read_b64_tr_b16 v[222:223], v243 offset:43264
v_mfma_f32_16x16x32_bf16 v[64:67], v[192:195], a[104:107], v[64:67]
v_accvgpr_read_b32 v228, a148
v_accvgpr_read_b32 v229, a149
v_mfma_f32_16x16x32_bf16 v[64:67], v[196:199], v[140:143], v[64:67]
buffer_load_dwordx4 v[164:167], v245, s[52:55], s91 offen offset:0 sc1
v_mfma_f32_16x16x32_bf16 v[96:99], v[196:199], v[128:131], v[96:99]
v_accvgpr_read_b32 v230, a150
v_accvgpr_read_b32 v231, a151
v_mfma_f32_16x16x32_bf16 v[96:99], v[192:195], a[108:111], v[96:99]
ds_read_b64_tr_b16 v[196:197], v240 offset:33920
ds_read_b64_tr_b16 v[198:199], v240 offset:34944
v_mfma_f32_16x16x32_bf16 v[100:103], v[200:203], a[108:111], v[100:103]
s_add_u32 m0, s83, 19072
v_cvt_pk_bf16_f32 v248, v224, v225
v_mfma_f32_16x16x32_bf16 v[100:103], v[204:207], v[128:131], v[100:103]
buffer_load_dwordx4 v234, s[44:47], s80 offen lds
v_mfma_f32_16x16x32_bf16 v[68:71], v[204:207], v[140:143], v[68:71]
v_cvt_pk_bf16_f32 v249, v226, v227
v_cvt_pk_bf16_f32 v250, v228, v229
v_mfma_f32_16x16x32_bf16 v[68:71], v[200:203], a[104:107], v[68:71]
ds_read_b64_tr_b16 v[192:193], v240 offset:42368
ds_read_b64_tr_b16 v[194:195], v240 offset:43392
v_mfma_f32_16x16x32_bf16 v[36:39], v[200:203], a[100:103], v[36:39]
v_cvt_pk_bf16_f32 v251, v230, v231
v_accvgpr_write_b32 a144, v248
v_mfma_f32_16x16x32_bf16 v[36:39], v[204:207], a[116:119], v[36:39]
buffer_load_dwordx4 v[168:171], v246, s[52:55], s91 offen offset:0 sc1
v_mfma_f32_16x16x32_bf16 v[4:7], v[204:207], a[112:115], v[4:7]
s_waitcnt lgkmcnt(4)
v_accvgpr_write_b32 a145, v249
v_mfma_f32_16x16x32_bf16 v[4:7], v[200:203], a[96:99], v[4:7]
ds_read_b64_tr_b16 v[204:205], v241 offset:33920
ds_read_b64_tr_b16 v[206:207], v241 offset:34944
v_mfma_f32_16x16x32_bf16 v[8:11], v[208:211], a[96:99], v[8:11]
s_add_u32 m0, s83, 20096
v_accvgpr_write_b32 a146, v250
v_mfma_f32_16x16x32_bf16 v[8:11], v[212:215], a[112:115], v[8:11]
buffer_load_dwordx4 v235, s[44:47], s80 offen lds
v_mfma_f32_16x16x32_bf16 v[40:43], v[212:215], a[116:119], v[40:43]
s_add_u32 s80, s80, 524288
v_accvgpr_write_b32 a147, v251
v_mfma_f32_16x16x32_bf16 v[40:43], v[208:211], a[100:103], v[40:43]
ds_read_b64_tr_b16 v[200:201], v241 offset:42368
ds_read_b64_tr_b16 v[202:203], v241 offset:43392
v_mfma_f32_16x16x32_bf16 v[72:75], v[208:211], a[104:107], v[72:75]
s_cmp_eq_u32 s3, 2
s_cselect_b64 s[36:37], s[32:33], s[36:37]
v_mfma_f32_16x16x32_bf16 v[72:75], v[212:215], v[140:143], v[72:75]
buffer_load_dwordx4 v[172:175], v247, s[52:55], s91 offen offset:0 sc1
v_mfma_f32_16x16x32_bf16 v[104:107], v[212:215], v[128:131], v[104:107]
s_lshl_b32 s93, s93, 5
s_add_u32 s92, s92, s93
v_mfma_f32_16x16x32_bf16 v[104:107], v[208:211], a[108:111], v[104:107]
ds_read_b64_tr_b16 v[212:213], v242 offset:33920
ds_read_b64_tr_b16 v[214:215], v242 offset:34944
v_mfma_f32_16x16x32_bf16 v[108:111], v[216:219], a[108:111], v[108:111]
s_mul_i32 s97, s92, 28672
v_mfma_f32_16x16x32_bf16 v[108:111], v[220:223], v[128:131], v[108:111]
s_cmp_eq_u32 s3, 3
s_cselect_b32 s95, s97, s95
s_cselect_b64 s[36:37], s[34:35], s[36:37]
s_cselect_b32 s21, 1, s21
s_cselect_b32 s97, 31, 39
v_mfma_f32_16x16x32_bf16 v[76:79], v[220:223], v[140:143], v[76:79]
s_lshl_b32 s21, s21, 10
v_mfma_f32_16x16x32_bf16 v[76:79], v[216:219], a[104:107], v[76:79]
ds_read_b64_tr_b16 v[208:209], v242 offset:42368
ds_read_b64_tr_b16 v[210:211], v242 offset:43392
v_mfma_f32_16x16x32_bf16 v[44:47], v[216:219], a[100:103], v[44:47]
ds_write_b128 v238, v[248:251] offset:7168
v_mfma_f32_16x16x32_bf16 v[44:47], v[220:223], a[116:119], v[44:47]
buffer_store_dwordx4 a[140:143], v255, s[48:51], s18 offen offset:2048 sc1
v_mfma_f32_16x16x32_bf16 v[12:15], v[220:223], a[112:115], v[12:15]
s_waitcnt lgkmcnt(5)
v_mfma_f32_16x16x32_bf16 v[12:15], v[216:219], a[96:99], v[12:15]
ds_read_b64_tr_b16 v[220:221], v243 offset:33920
ds_read_b64_tr_b16 v[222:223], v243 offset:34944
v_mfma_f32_16x16x32_bf16 v[16:19], v[196:199], a[96:99], v[16:19]
v_accvgpr_read_b32 v224, a232
v_accvgpr_read_b32 v225, a233
v_mfma_f32_16x16x32_bf16 v[16:19], v[192:195], a[112:115], v[16:19]
buffer_load_dwordx4 v[176:179], v244, s[52:55], s91 offen offset:1024 sc1
v_mfma_f32_16x16x32_bf16 v[48:51], v[192:195], a[116:119], v[48:51]
v_accvgpr_read_b32 v226, a234
v_accvgpr_read_b32 v227, a235
v_mfma_f32_16x16x32_bf16 v[48:51], v[196:199], a[100:103], v[48:51]
.La5_LW1_10:
s_cbranch_execz .La5_LW1_14
ds_read_b64_tr_b16 v[216:217], v243 offset:42368
ds_read_b64_tr_b16 v[218:219], v243 offset:43392
v_mfma_f32_16x16x32_bf16 v[80:83], v[196:199], a[104:107], v[80:83]
v_accvgpr_read_b32 v228, a236
v_accvgpr_read_b32 v229, a237
v_mfma_f32_16x16x32_bf16 v[80:83], v[192:195], v[140:143], v[80:83]
buffer_load_dwordx4 v[180:183], v245, s[52:55], s91 offen offset:1024 sc1
v_mfma_f32_16x16x32_bf16 v[112:115], v[192:195], v[128:131], v[112:115]
v_accvgpr_read_b32 v230, a238
v_accvgpr_read_b32 v231, a239
v_mfma_f32_16x16x32_bf16 v[112:115], v[196:199], a[108:111], v[112:115]
v_cvt_pk_bf16_f32 v248, v224, v225
v_cvt_pk_bf16_f32 v249, v226, v227
v_mfma_f32_16x16x32_bf16 v[116:119], v[204:207], a[108:111], v[116:119]
s_waitcnt lgkmcnt(0)
s_barrier
v_mfma_f32_16x16x32_bf16 v[116:119], v[200:203], v[128:131], v[116:119]
buffer_load_dwordx4 v[184:187], v246, s[52:55], s91 offen offset:1024 sc1
v_mfma_f32_16x16x32_bf16 v[84:87], v[200:203], v[140:143], v[84:87]
ds_read_b64_tr_b16 v[192:193], v240 offset:50688
ds_read_b64_tr_b16 v[194:195], v240 offset:51712
v_mfma_f32_16x16x32_bf16 v[84:87], v[204:207], a[104:107], v[84:87]
ds_read_b64_tr_b16 v[196:197], v240 offset:59136
ds_read_b64_tr_b16 v[198:199], v240 offset:60160
v_mfma_f32_16x16x32_bf16 v[52:55], v[204:207], a[100:103], v[52:55]
s_add_u32 m0, s83, 33792
v_cvt_pk_bf16_f32 v250, v228, v229
v_mfma_f32_16x16x32_bf16 v[52:55], v[200:203], a[116:119], v[52:55]
buffer_load_dwordx4 v232, s[44:47], s80 offen lds
v_mfma_f32_16x16x32_bf16 v[20:23], v[200:203], a[112:115], v[20:23]
v_cvt_pk_bf16_f32 v251, v230, v231
v_accvgpr_write_b32 a148, v248
v_mfma_f32_16x16x32_bf16 v[20:23], v[204:207], a[96:99], v[20:23]
ds_read_b64_tr_b16 v[200:201], v241 offset:50688
ds_read_b64_tr_b16 v[202:203], v241 offset:51712
v_mfma_f32_16x16x32_bf16 v[24:27], v[212:215], a[96:99], v[24:27]
v_accvgpr_write_b32 a149, v249
v_accvgpr_write_b32 a150, v250
v_mfma_f32_16x16x32_bf16 v[24:27], v[208:211], a[112:115], v[24:27]
buffer_load_dwordx4 v[188:191], v247, s[52:55], s91 offen offset:1024 sc1
v_mfma_f32_16x16x32_bf16 v[56:59], v[208:211], a[116:119], v[56:59]
v_accvgpr_write_b32 a151, v251
v_mfma_f32_16x16x32_bf16 v[56:59], v[212:215], a[100:103], v[56:59]
ds_read_b64_tr_b16 v[204:205], v241 offset:59136
ds_read_b64_tr_b16 v[206:207], v241 offset:60160
v_mfma_f32_16x16x32_bf16 v[88:91], v[212:215], a[104:107], v[88:91]
s_add_u32 m0, s83, 34816
v_mfma_f32_16x16x32_bf16 v[88:91], v[208:211], v[140:143], v[88:91]
buffer_load_dwordx4 v233, s[44:47], s80 offen lds
v_mfma_f32_16x16x32_bf16 v[120:123], v[208:211], v[128:131], v[120:123]
s_cmp_eq_u32 s75, 1
s_cselect_b32 s97, 23, s97
s_cselect_b32 s92, 0, 24
v_mfma_f32_16x16x32_bf16 v[120:123], v[212:215], a[108:111], v[120:123]
ds_read_b64_tr_b16 v[208:209], v242 offset:50688
ds_read_b64_tr_b16 v[210:211], v242 offset:51712
v_mfma_f32_16x16x32_bf16 v[124:127], v[220:223], a[108:111], v[124:127]
s_mul_i32 s92, s92, s21
s_add_u32 s95, s95, s92
v_mfma_f32_16x16x32_bf16 v[124:127], v[216:219], v[128:131], v[124:127]
s_mul_i32 s97, s97, s21
s_add_u32 s97, s97, 896
v_mfma_f32_16x16x32_bf16 v[92:95], v[216:219], v[140:143], v[92:95]
s_add_u32 s36, s36, s95
s_addc_u32 s37, s37, 0
v_mfma_f32_16x16x32_bf16 v[92:95], v[220:223], a[104:107], v[92:95]
ds_read_b64_tr_b16 v[212:213], v242 offset:59136
ds_read_b64_tr_b16 v[214:215], v242 offset:60160
v_mfma_f32_16x16x32_bf16 v[60:63], v[220:223], a[100:103], v[60:63]
ds_write_b128 v238, v[248:251] offset:32768
v_mfma_f32_16x16x32_bf16 v[60:63], v[216:219], a[116:119], v[60:63]
s_and_b32 s37, s37, 0xffff
s_mov_b32 s38, s97
v_mfma_f32_16x16x32_bf16 v[28:31], v[216:219], a[112:115], v[28:31]
s_waitcnt vmcnt(15) lgkmcnt(5)
s_mov_b32 s39, 0x20000
v_mfma_f32_16x16x32_bf16 v[28:31], v[220:223], a[96:99], v[28:31]
ds_read_b64_tr_b16 v[216:217], v243 offset:50688
ds_read_b64_tr_b16 v[218:219], v243 offset:51712
v_mfma_f32_16x16x32_bf16 v[0:3], v[192:195], v[132:135], v[0:3]
v_accvgpr_read_b32 v224, a152
v_accvgpr_read_b32 v225, a153
v_mfma_f32_16x16x32_bf16 v[0:3], v[196:199], v[152:155], v[0:3]
buffer_load_dwordx4 v[128:131], v244, s[52:55], s91 offen offset:2048 sc1
v_mfma_f32_16x16x32_bf16 v[32:35], v[196:199], v[156:159], v[32:35]
v_accvgpr_read_b32 v226, a154
v_accvgpr_read_b32 v227, a155
v_mfma_f32_16x16x32_bf16 v[32:35], v[192:195], v[136:139], v[32:35]
ds_read_b64_tr_b16 v[220:221], v243 offset:59136
ds_read_b64_tr_b16 v[222:223], v243 offset:60160
v_mfma_f32_16x16x32_bf16 v[64:67], v[192:195], v[144:147], v[64:67]
v_accvgpr_read_b32 v228, a156
v_accvgpr_read_b32 v229, a157
v_mfma_f32_16x16x32_bf16 v[64:67], v[196:199], a[120:123], v[64:67]
buffer_load_dwordx4 v[140:143], v245, s[52:55], s91 offen offset:2048 sc1
v_mfma_f32_16x16x32_bf16 v[96:99], v[196:199], a[124:127], v[96:99]
v_accvgpr_read_b32 v230, a158
v_accvgpr_read_b32 v231, a159
v_mfma_f32_16x16x32_bf16 v[96:99], v[192:195], v[148:151], v[96:99]
ds_read_b64_tr_b16 v[196:197], v240 offset:50816
ds_read_b64_tr_b16 v[198:199], v240 offset:51840
v_mfma_f32_16x16x32_bf16 v[100:103], v[200:203], v[148:151], v[100:103]
s_add_u32 m0, s83, 35968
v_cvt_pk_bf16_f32 v248, v224, v225
v_mfma_f32_16x16x32_bf16 v[100:103], v[204:207], a[124:127], v[100:103]
buffer_load_dwordx4 v234, s[44:47], s80 offen lds
v_mfma_f32_16x16x32_bf16 v[68:71], v[204:207], a[120:123], v[68:71]
v_cvt_pk_bf16_f32 v249, v226, v227
v_cvt_pk_bf16_f32 v250, v228, v229
v_mfma_f32_16x16x32_bf16 v[68:71], v[200:203], v[144:147], v[68:71]
ds_read_b64_tr_b16 v[192:193], v240 offset:59264
ds_read_b64_tr_b16 v[194:195], v240 offset:60288
v_mfma_f32_16x16x32_bf16 v[36:39], v[200:203], v[136:139], v[36:39]
v_cvt_pk_bf16_f32 v251, v230, v231
v_accvgpr_write_b32 a152, v248
v_mfma_f32_16x16x32_bf16 v[36:39], v[204:207], v[156:159], v[36:39]
buffer_load_dwordx4 a[96:99], v246, s[52:55], s91 offen offset:2048 sc1
v_mfma_f32_16x16x32_bf16 v[4:7], v[204:207], v[152:155], v[4:7]
s_waitcnt lgkmcnt(4)
v_accvgpr_write_b32 a153, v249
v_mfma_f32_16x16x32_bf16 v[4:7], v[200:203], v[132:135], v[4:7]
ds_read_b64_tr_b16 v[204:205], v241 offset:50816
ds_read_b64_tr_b16 v[206:207], v241 offset:51840
v_mfma_f32_16x16x32_bf16 v[8:11], v[208:211], v[132:135], v[8:11]
s_add_u32 m0, s83, 36992
v_accvgpr_write_b32 a154, v250
v_mfma_f32_16x16x32_bf16 v[8:11], v[212:215], v[152:155], v[8:11]
buffer_load_dwordx4 v235, s[44:47], s80 offen lds
v_mfma_f32_16x16x32_bf16 v[40:43], v[212:215], v[156:159], v[40:43]
s_add_u32 s80, s80, 524288
v_accvgpr_write_b32 a155, v251
v_mfma_f32_16x16x32_bf16 v[40:43], v[208:211], v[136:139], v[40:43]
ds_read_b64_tr_b16 v[200:201], v241 offset:59264
ds_read_b64_tr_b16 v[202:203], v241 offset:60288
v_mfma_f32_16x16x32_bf16 v[72:75], v[208:211], v[144:147], v[72:75]
s_lshl_b32 s93, s96, 6
s_mul_i32 s95, s94, 7332928
v_mfma_f32_16x16x32_bf16 v[72:75], v[212:215], a[120:123], v[72:75]
buffer_load_dwordx4 a[100:103], v247, s[52:55], s91 offen offset:2048 sc1
v_mfma_f32_16x16x32_bf16 v[104:107], v[212:215], a[124:127], v[104:107]
s_cmp_eq_u32 s77, 0
s_cselect_b64 s[40:41], s[30:31], s[32:33]
v_mfma_f32_16x16x32_bf16 v[104:107], v[208:211], v[148:151], v[104:107]
ds_read_b64_tr_b16 v[212:213], v242 offset:50816
ds_read_b64_tr_b16 v[214:215], v242 offset:51840
v_mfma_f32_16x16x32_bf16 v[108:111], v[216:219], v[148:151], v[108:111]
s_add_u32 s40, s40, s93
s_addc_u32 s41, s41, 0
v_mfma_f32_16x16x32_bf16 v[108:111], v[220:223], a[124:127], v[108:111]
s_and_b32 s41, s41, 0xffff
s_mov_b32 s42, s95
v_mfma_f32_16x16x32_bf16 v[76:79], v[220:223], a[120:123], v[76:79]
s_mov_b32 s43, 0x20000
s_mov_b32 s92, s72
v_mfma_f32_16x16x32_bf16 v[76:79], v[216:219], v[144:147], v[76:79]
ds_read_b64_tr_b16 v[208:209], v242 offset:59264
ds_read_b64_tr_b16 v[210:211], v242 offset:60288
v_mfma_f32_16x16x32_bf16 v[44:47], v[216:219], v[136:139], v[44:47]
ds_write_b128 v238, v[248:251] offset:33792
v_mfma_f32_16x16x32_bf16 v[44:47], v[220:223], v[156:159], v[44:47]
buffer_store_dwordx4 a[144:147], v255, s[48:51], s18 offen offset:3072 sc1
v_mfma_f32_16x16x32_bf16 v[12:15], v[220:223], v[152:155], v[12:15]
s_waitcnt lgkmcnt(5)
s_xor_b32 s92, s92, 1
v_mfma_f32_16x16x32_bf16 v[12:15], v[216:219], v[132:135], v[12:15]
ds_read_b64_tr_b16 v[220:221], v243 offset:50816
ds_read_b64_tr_b16 v[222:223], v243 offset:51840
v_mfma_f32_16x16x32_bf16 v[16:19], v[196:199], v[132:135], v[16:19]
v_accvgpr_read_b32 v224, a208
v_accvgpr_read_b32 v225, a209
v_mfma_f32_16x16x32_bf16 v[16:19], v[192:195], v[152:155], v[16:19]
buffer_load_dwordx4 a[104:107], v244, s[52:55], s91 offen offset:3072 sc1
v_mfma_f32_16x16x32_bf16 v[48:51], v[192:195], v[156:159], v[48:51]
v_accvgpr_read_b32 v226, a210
v_accvgpr_read_b32 v227, a211
v_mfma_f32_16x16x32_bf16 v[48:51], v[196:199], v[136:139], v[48:51]
ds_read_b64_tr_b16 v[216:217], v243 offset:59264
ds_read_b64_tr_b16 v[218:219], v243 offset:60288
v_mfma_f32_16x16x32_bf16 v[80:83], v[196:199], v[144:147], v[80:83]
v_accvgpr_read_b32 v228, a212
v_accvgpr_read_b32 v229, a213
v_mfma_f32_16x16x32_bf16 v[80:83], v[192:195], a[120:123], v[80:83]
buffer_load_dwordx4 a[108:111], v245, s[52:55], s91 offen offset:3072 sc1
v_mfma_f32_16x16x32_bf16 v[112:115], v[192:195], a[124:127], v[112:115]
v_accvgpr_read_b32 v230, a214
v_accvgpr_read_b32 v231, a215
v_mfma_f32_16x16x32_bf16 v[112:115], v[196:199], v[148:151], v[112:115]
v_cvt_pk_bf16_f32 v248, v224, v225
v_cvt_pk_bf16_f32 v249, v226, v227
v_mfma_f32_16x16x32_bf16 v[116:119], v[204:207], v[148:151], v[116:119]
s_waitcnt lgkmcnt(0)
s_barrier
v_mfma_f32_16x16x32_bf16 v[116:119], v[200:203], a[124:127], v[116:119]
buffer_load_dwordx4 a[112:115], v246, s[52:55], s91 offen offset:3072 sc1
v_mfma_f32_16x16x32_bf16 v[84:87], v[200:203], a[120:123], v[84:87]
ds_read_b64_tr_b16 v[192:193], v240 offset:0
ds_read_b64_tr_b16 v[194:195], v240 offset:1024
v_mfma_f32_16x16x32_bf16 v[84:87], v[204:207], v[144:147], v[84:87]
ds_read_b64_tr_b16 v[196:197], v240 offset:8448
ds_read_b64_tr_b16 v[198:199], v240 offset:9472
v_mfma_f32_16x16x32_bf16 v[52:55], v[204:207], v[136:139], v[52:55]
s_add_u32 m0, s83, 50688
v_cvt_pk_bf16_f32 v250, v228, v229
v_mfma_f32_16x16x32_bf16 v[52:55], v[200:203], v[156:159], v[52:55]
buffer_load_dwordx4 v232, s[44:47], s80 offen lds
v_mfma_f32_16x16x32_bf16 v[20:23], v[200:203], v[152:155], v[20:23]
v_cvt_pk_bf16_f32 v251, v230, v231
v_accvgpr_write_b32 a156, v248
v_mfma_f32_16x16x32_bf16 v[20:23], v[204:207], v[132:135], v[20:23]
ds_read_b64_tr_b16 v[200:201], v241 offset:0
ds_read_b64_tr_b16 v[202:203], v241 offset:1024
v_mfma_f32_16x16x32_bf16 v[24:27], v[212:215], v[132:135], v[24:27]
v_accvgpr_write_b32 a157, v249
v_accvgpr_write_b32 a158, v250
v_mfma_f32_16x16x32_bf16 v[24:27], v[208:211], v[152:155], v[24:27]
buffer_load_dwordx4 a[116:119], v247, s[52:55], s91 offen offset:3072 sc1
v_mfma_f32_16x16x32_bf16 v[56:59], v[208:211], v[156:159], v[56:59]
s_add_u32 s91, s91, 4096
v_accvgpr_write_b32 a159, v251
v_mfma_f32_16x16x32_bf16 v[56:59], v[212:215], v[136:139], v[56:59]
ds_read_b64_tr_b16 v[204:205], v241 offset:8448
ds_read_b64_tr_b16 v[206:207], v241 offset:9472
v_mfma_f32_16x16x32_bf16 v[88:91], v[212:215], v[144:147], v[88:91]
s_add_u32 m0, s83, 51712
s_and_b32 s101, s92, 7
v_mfma_f32_16x16x32_bf16 v[88:91], v[208:211], a[120:123], v[88:91]
buffer_load_dwordx4 v233, s[44:47], s80 offen lds
v_mfma_f32_16x16x32_bf16 v[120:123], v[208:211], a[124:127], v[120:123]
s_lshl_b32 s92, s101, 8
s_sub_u32 s93, 8388608, s92
v_mfma_f32_16x16x32_bf16 v[120:123], v[212:215], v[148:151], v[120:123]
ds_read_b64_tr_b16 v[208:209], v242 offset:0
ds_read_b64_tr_b16 v[210:211], v242 offset:1024
v_mfma_f32_16x16x32_bf16 v[124:127], v[220:223], v[148:151], v[124:127]
s_add_u32 s56, s28, s92
s_addc_u32 s57, s29, 0
v_mfma_f32_16x16x32_bf16 v[124:127], v[216:219], a[124:127], v[124:127]
s_and_b32 s57, s57, 0xffff
s_mov_b32 s58, s93
v_mfma_f32_16x16x32_bf16 v[92:95], v[216:219], a[120:123], v[92:95]
s_mov_b32 s18, 32768
s_mov_b32 s59, 0x20000
v_mfma_f32_16x16x32_bf16 v[92:95], v[220:223], v[144:147], v[92:95]
ds_read_b64_tr_b16 v[212:213], v242 offset:8448
ds_read_b64_tr_b16 v[214:215], v242 offset:9472
v_mfma_f32_16x16x32_bf16 v[60:63], v[220:223], v[136:139], v[60:63]
ds_write_b128 v238, v[248:251] offset:34816
v_mfma_f32_16x16x32_bf16 v[60:63], v[216:219], v[156:159], v[60:63]
buffer_store_dwordx4 a[148:151], v255, s[48:51], s18 offen offset:0 sc1
v_mfma_f32_16x16x32_bf16 v[28:31], v[216:219], v[152:155], v[28:31]
s_waitcnt vmcnt(15) lgkmcnt(5)
s_lshl_b32 s93, s96, 16
v_mfma_f32_16x16x32_bf16 v[28:31], v[220:223], v[132:135], v[28:31]
ds_read_b64_tr_b16 v[216:217], v243 offset:0
ds_read_b64_tr_b16 v[218:219], v243 offset:1024
v_mfma_f32_16x16x32_bf16 v[0:3], v[192:195], v[160:163], v[0:3]
v_accvgpr_read_b32 v224, a160
v_accvgpr_read_b32 v225, a161
v_mfma_f32_16x16x32_bf16 v[0:3], v[196:199], v[176:179], v[0:3]
buffer_load_dwordx4 v[132:135], v244, s[52:55], s91 offen offset:0 sc1
v_mfma_f32_16x16x32_bf16 v[32:35], v[196:199], v[180:183], v[32:35]
v_accvgpr_read_b32 v226, a162
v_accvgpr_read_b32 v227, a163
v_mfma_f32_16x16x32_bf16 v[32:35], v[192:195], v[164:167], v[32:35]
ds_read_b64_tr_b16 v[220:221], v243 offset:8448
ds_read_b64_tr_b16 v[222:223], v243 offset:9472
v_mfma_f32_16x16x32_bf16 v[64:67], v[192:195], v[168:171], v[64:67]
v_accvgpr_read_b32 v228, a164
v_accvgpr_read_b32 v229, a165
v_mfma_f32_16x16x32_bf16 v[64:67], v[196:199], v[184:187], v[64:67]
buffer_load_dwordx4 v[136:139], v245, s[52:55], s91 offen offset:0 sc1
v_mfma_f32_16x16x32_bf16 v[96:99], v[196:199], v[188:191], v[96:99]
v_accvgpr_read_b32 v230, a166
v_accvgpr_read_b32 v231, a167
v_mfma_f32_16x16x32_bf16 v[96:99], v[192:195], v[172:175], v[96:99]
ds_read_b64_tr_b16 v[196:197], v240 offset:128
ds_read_b64_tr_b16 v[198:199], v240 offset:1152
v_mfma_f32_16x16x32_bf16 v[100:103], v[200:203], v[172:175], v[100:103]
s_add_u32 m0, s83, 52864
v_cvt_pk_bf16_f32 v248, v224, v225
v_mfma_f32_16x16x32_bf16 v[100:103], v[204:207], v[188:191], v[100:103]
buffer_load_dwordx4 v234, s[44:47], s80 offen lds
v_mfma_f32_16x16x32_bf16 v[68:71], v[204:207], v[184:187], v[68:71]
v_cvt_pk_bf16_f32 v249, v226, v227
v_cvt_pk_bf16_f32 v250, v228, v229
v_mfma_f32_16x16x32_bf16 v[68:71], v[200:203], v[168:171], v[68:71]
ds_read_b64_tr_b16 v[192:193], v240 offset:8576
ds_read_b64_tr_b16 v[194:195], v240 offset:9600
v_mfma_f32_16x16x32_bf16 v[36:39], v[200:203], v[164:167], v[36:39]
v_cvt_pk_bf16_f32 v251, v230, v231
v_accvgpr_write_b32 a160, v248
v_mfma_f32_16x16x32_bf16 v[36:39], v[204:207], v[180:183], v[36:39]
buffer_load_dwordx4 v[144:147], v246, s[52:55], s91 offen offset:0 sc1
v_mfma_f32_16x16x32_bf16 v[4:7], v[204:207], v[176:179], v[4:7]
s_waitcnt lgkmcnt(4)
v_accvgpr_write_b32 a161, v249
v_mfma_f32_16x16x32_bf16 v[4:7], v[200:203], v[160:163], v[4:7]
ds_read_b64_tr_b16 v[204:205], v241 offset:128
ds_read_b64_tr_b16 v[206:207], v241 offset:1152
v_mfma_f32_16x16x32_bf16 v[8:11], v[208:211], v[160:163], v[8:11]
s_add_u32 m0, s83, 53888
v_accvgpr_write_b32 a162, v250
v_mfma_f32_16x16x32_bf16 v[8:11], v[212:215], v[176:179], v[8:11]
buffer_load_dwordx4 v235, s[44:47], s80 offen lds
v_mfma_f32_16x16x32_bf16 v[40:43], v[212:215], v[180:183], v[40:43]
s_add_u32 s80, s80, 524288
v_accvgpr_write_b32 a163, v251
v_mfma_f32_16x16x32_bf16 v[40:43], v[208:211], v[164:167], v[40:43]
ds_read_b64_tr_b16 v[200:201], v241 offset:8576
ds_read_b64_tr_b16 v[202:203], v241 offset:9600
v_mfma_f32_16x16x32_bf16 v[72:75], v[208:211], v[168:171], v[72:75]
s_lshl_b32 s95, s94, 16
v_mfma_f32_16x16x32_bf16 v[72:75], v[212:215], v[184:187], v[72:75]
buffer_load_dwordx4 v[148:151], v247, s[52:55], s91 offen offset:0 sc1
v_mfma_f32_16x16x32_bf16 v[104:107], v[212:215], v[188:191], v[104:107]
s_add_u32 s28, s34, s93
s_addc_u32 s29, s35, 0
v_mfma_f32_16x16x32_bf16 v[104:107], v[208:211], v[172:175], v[104:107]
ds_read_b64_tr_b16 v[212:213], v242 offset:128
ds_read_b64_tr_b16 v[214:215], v242 offset:1152
v_mfma_f32_16x16x32_bf16 v[108:111], v[216:219], v[172:175], v[108:111]
s_and_b32 s29, s29, 0xffff
s_mov_b32 s30, s95
v_mfma_f32_16x16x32_bf16 v[108:111], v[220:223], v[188:191], v[108:111]
s_mov_b32 s31, 0x20000
s_lshl_b32 s93, s101, 12
v_mfma_f32_16x16x32_bf16 v[76:79], v[220:223], v[184:187], v[76:79]
s_add_u32 s92, s93, 71303168
s_sub_u32 s93, 25165824, s93
v_mfma_f32_16x16x32_bf16 v[76:79], v[216:219], v[168:171], v[76:79]
ds_read_b64_tr_b16 v[208:209], v242 offset:8576
ds_read_b64_tr_b16 v[210:211], v242 offset:9600
v_mfma_f32_16x16x32_bf16 v[44:47], v[216:219], v[164:167], v[44:47]
ds_write_b128 v238, v[248:251] offset:35840
v_mfma_f32_16x16x32_bf16 v[44:47], v[220:223], v[180:183], v[44:47]
buffer_store_dwordx4 a[152:155], v255, s[48:51], s18 offen offset:1024 sc1
v_mfma_f32_16x16x32_bf16 v[12:15], v[220:223], v[176:179], v[12:15]
s_waitcnt lgkmcnt(5)
v_mfma_f32_16x16x32_bf16 v[12:15], v[216:219], v[160:163], v[12:15]
ds_read_b64_tr_b16 v[220:221], v243 offset:128
ds_read_b64_tr_b16 v[222:223], v243 offset:1152
v_mfma_f32_16x16x32_bf16 v[16:19], v[196:199], v[160:163], v[16:19]
v_accvgpr_read_b32 v224, a216
v_accvgpr_read_b32 v225, a217
v_mfma_f32_16x16x32_bf16 v[16:19], v[192:195], v[176:179], v[16:19]
buffer_load_dwordx4 v[152:155], v244, s[52:55], s91 offen offset:1024 sc1
v_mfma_f32_16x16x32_bf16 v[48:51], v[192:195], v[180:183], v[48:51]
v_accvgpr_read_b32 v226, a218
v_accvgpr_read_b32 v227, a219
v_mfma_f32_16x16x32_bf16 v[48:51], v[196:199], v[164:167], v[48:51]
ds_read_b64_tr_b16 v[216:217], v243 offset:8576
ds_read_b64_tr_b16 v[218:219], v243 offset:9600
v_mfma_f32_16x16x32_bf16 v[80:83], v[196:199], v[168:171], v[80:83]
v_accvgpr_read_b32 v228, a220
v_accvgpr_read_b32 v229, a221
v_mfma_f32_16x16x32_bf16 v[80:83], v[192:195], v[184:187], v[80:83]
buffer_load_dwordx4 v[156:159], v245, s[52:55], s91 offen offset:1024 sc1
v_mfma_f32_16x16x32_bf16 v[112:115], v[192:195], v[188:191], v[112:115]
v_accvgpr_read_b32 v230, a222
v_accvgpr_read_b32 v231, a223
v_mfma_f32_16x16x32_bf16 v[112:115], v[196:199], v[172:175], v[112:115]
v_cvt_pk_bf16_f32 v248, v224, v225
v_cvt_pk_bf16_f32 v249, v226, v227
v_mfma_f32_16x16x32_bf16 v[116:119], v[204:207], v[172:175], v[116:119]
s_waitcnt lgkmcnt(0)
s_barrier
v_mfma_f32_16x16x32_bf16 v[116:119], v[200:203], v[188:191], v[116:119]
buffer_load_dwordx4 a[120:123], v246, s[52:55], s91 offen offset:1024 sc1
v_mfma_f32_16x16x32_bf16 v[84:87], v[200:203], v[184:187], v[84:87]
ds_read_b64_tr_b16 v[192:193], v240 offset:16896
ds_read_b64_tr_b16 v[194:195], v240 offset:17920
v_mfma_f32_16x16x32_bf16 v[84:87], v[204:207], v[168:171], v[84:87]
ds_read_b64_tr_b16 v[196:197], v240 offset:25344
ds_read_b64_tr_b16 v[198:199], v240 offset:26368
v_mfma_f32_16x16x32_bf16 v[52:55], v[204:207], v[164:167], v[52:55]
s_add_u32 m0, s83, 0
v_cvt_pk_bf16_f32 v250, v228, v229
v_mfma_f32_16x16x32_bf16 v[52:55], v[200:203], v[180:183], v[52:55]
buffer_load_dwordx4 v232, s[44:47], s80 offen lds
v_mfma_f32_16x16x32_bf16 v[20:23], v[200:203], v[176:179], v[20:23]
v_cvt_pk_bf16_f32 v251, v230, v231
v_accvgpr_write_b32 a164, v248
v_mfma_f32_16x16x32_bf16 v[20:23], v[204:207], v[160:163], v[20:23]
ds_read_b64_tr_b16 v[200:201], v241 offset:16896
ds_read_b64_tr_b16 v[202:203], v241 offset:17920
v_mfma_f32_16x16x32_bf16 v[24:27], v[212:215], v[160:163], v[24:27]
v_accvgpr_write_b32 a165, v249
v_accvgpr_write_b32 a166, v250
v_mfma_f32_16x16x32_bf16 v[24:27], v[208:211], v[176:179], v[24:27]
buffer_load_dwordx4 a[124:127], v247, s[52:55], s91 offen offset:1024 sc1
v_mfma_f32_16x16x32_bf16 v[56:59], v[208:211], v[180:183], v[56:59]
v_accvgpr_write_b32 a167, v251
v_mfma_f32_16x16x32_bf16 v[56:59], v[212:215], v[164:167], v[56:59]
ds_read_b64_tr_b16 v[204:205], v241 offset:25344
ds_read_b64_tr_b16 v[206:207], v241 offset:26368
v_mfma_f32_16x16x32_bf16 v[88:91], v[212:215], v[168:171], v[88:91]
s_add_u32 m0, s83, 1024
v_mfma_f32_16x16x32_bf16 v[88:91], v[208:211], v[184:187], v[88:91]
buffer_load_dwordx4 v233, s[44:47], s80 offen lds
v_mfma_f32_16x16x32_bf16 v[120:123], v[208:211], v[188:191], v[120:123]
s_add_u32 s32, s26, s92
s_addc_u32 s33, s27, 0
v_mfma_f32_16x16x32_bf16 v[120:123], v[212:215], v[172:175], v[120:123]
ds_read_b64_tr_b16 v[208:209], v242 offset:16896
ds_read_b64_tr_b16 v[210:211], v242 offset:17920
v_mfma_f32_16x16x32_bf16 v[124:127], v[220:223], v[172:175], v[124:127]
s_and_b32 s33, s33, 0xffff
s_mov_b32 s34, s93
v_mfma_f32_16x16x32_bf16 v[124:127], v[216:219], v[188:191], v[124:127]
s_mov_b32 s35, 0x20000
s_lshl2_add_u32 s92, s72, s74
v_mfma_f32_16x16x32_bf16 v[92:95], v[216:219], v[184:187], v[92:95]
s_lshl2_add_u32 s92, s92, s3
s_lshl_b32 s88, s92, 8
v_mfma_f32_16x16x32_bf16 v[92:95], v[220:223], v[168:171], v[92:95]
ds_read_b64_tr_b16 v[212:213], v242 offset:25344
ds_read_b64_tr_b16 v[214:215], v242 offset:26368
v_mfma_f32_16x16x32_bf16 v[60:63], v[220:223], v[164:167], v[60:63]
ds_write_b128 v238, v[248:251] offset:36864
v_mfma_f32_16x16x32_bf16 v[60:63], v[216:219], v[180:183], v[60:63]
buffer_store_dwordx4 a[156:159], v255, s[48:51], s18 offen offset:2048 sc1
v_mfma_f32_16x16x32_bf16 v[28:31], v[216:219], v[176:179], v[28:31]
s_waitcnt vmcnt(16) lgkmcnt(5)
s_mul_i32 s93, s92, 17408
v_mfma_f32_16x16x32_bf16 v[28:31], v[220:223], v[160:163], v[28:31]
ds_read_b64_tr_b16 v[216:217], v243 offset:16896
ds_read_b64_tr_b16 v[218:219], v243 offset:17920
v_mfma_f32_16x16x32_bf16 v[0:3], v[192:195], v[128:131], v[0:3]
v_accvgpr_read_b32 v224, a168
v_accvgpr_read_b32 v225, a169
v_mfma_f32_16x16x32_bf16 v[0:3], v[196:199], a[104:107], v[0:3]
buffer_load_dwordx4 v[160:163], v244, s[52:55], s91 offen offset:2048 sc1
v_mfma_f32_16x16x32_bf16 v[32:35], v[196:199], a[108:111], v[32:35]
v_accvgpr_read_b32 v226, a170
v_accvgpr_read_b32 v227, a171
v_mfma_f32_16x16x32_bf16 v[32:35], v[192:195], v[140:143], v[32:35]
ds_read_b64_tr_b16 v[220:221], v243 offset:25344
ds_read_b64_tr_b16 v[222:223], v243 offset:26368
v_mfma_f32_16x16x32_bf16 v[64:67], v[192:195], a[96:99], v[64:67]
v_accvgpr_read_b32 v228, a172
v_accvgpr_read_b32 v229, a173
v_mfma_f32_16x16x32_bf16 v[64:67], v[196:199], a[112:115], v[64:67]
buffer_load_dwordx4 v[164:167], v245, s[52:55], s91 offen offset:2048 sc1
v_mfma_f32_16x16x32_bf16 v[96:99], v[196:199], a[116:119], v[96:99]
v_accvgpr_read_b32 v230, a174
v_accvgpr_read_b32 v231, a175
v_mfma_f32_16x16x32_bf16 v[96:99], v[192:195], a[100:103], v[96:99]
ds_read_b64_tr_b16 v[196:197], v240 offset:17024
ds_read_b64_tr_b16 v[198:199], v240 offset:18048
v_mfma_f32_16x16x32_bf16 v[100:103], v[200:203], a[100:103], v[100:103]
s_add_u32 m0, s83, 2176
v_cvt_pk_bf16_f32 v248, v224, v225
v_mfma_f32_16x16x32_bf16 v[100:103], v[204:207], a[116:119], v[100:103]
buffer_load_dwordx4 v234, s[44:47], s80 offen lds
v_mfma_f32_16x16x32_bf16 v[68:71], v[204:207], a[112:115], v[68:71]
v_cvt_pk_bf16_f32 v249, v226, v227
v_cvt_pk_bf16_f32 v250, v228, v229
v_mfma_f32_16x16x32_bf16 v[68:71], v[200:203], a[96:99], v[68:71]
ds_read_b64_tr_b16 v[192:193], v240 offset:25472
ds_read_b64_tr_b16 v[194:195], v240 offset:26496
v_mfma_f32_16x16x32_bf16 v[36:39], v[200:203], v[140:143], v[36:39]
v_cvt_pk_bf16_f32 v251, v230, v231
v_accvgpr_write_b32 a168, v248
v_mfma_f32_16x16x32_bf16 v[36:39], v[204:207], a[108:111], v[36:39]
buffer_load_dwordx4 v[168:171], v246, s[52:55], s91 offen offset:2048 sc1
v_mfma_f32_16x16x32_bf16 v[4:7], v[204:207], a[104:107], v[4:7]
s_waitcnt lgkmcnt(4)
v_accvgpr_write_b32 a169, v249
v_mfma_f32_16x16x32_bf16 v[4:7], v[200:203], v[128:131], v[4:7]
ds_read_b64_tr_b16 v[204:205], v241 offset:17024
ds_read_b64_tr_b16 v[206:207], v241 offset:18048
v_mfma_f32_16x16x32_bf16 v[8:11], v[208:211], v[128:131], v[8:11]
s_add_u32 m0, s83, 3200
v_accvgpr_write_b32 a170, v250
v_mfma_f32_16x16x32_bf16 v[8:11], v[212:215], a[104:107], v[8:11]
buffer_load_dwordx4 v235, s[44:47], s80 offen lds
v_mfma_f32_16x16x32_bf16 v[40:43], v[212:215], a[108:111], v[40:43]
s_add_u32 s80, s80, 524288
v_accvgpr_write_b32 a171, v251
v_mfma_f32_16x16x32_bf16 v[40:43], v[208:211], v[140:143], v[40:43]
ds_read_b64_tr_b16 v[200:201], v241 offset:25472
ds_read_b64_tr_b16 v[202:203], v241 offset:26496
v_mfma_f32_16x16x32_bf16 v[72:75], v[208:211], a[96:99], v[72:75]
s_lshl_b32 s92, s92, 14
s_add_u32 s92, s92, 4194304
v_mfma_f32_16x16x32_bf16 v[72:75], v[212:215], a[112:115], v[72:75]
buffer_load_dwordx4 v[172:175], v247, s[52:55], s91 offen offset:2048 sc1
v_mfma_f32_16x16x32_bf16 v[104:107], v[212:215], a[116:119], v[104:107]
s_add_u32 s86, s26, s92
s_addc_u32 s87, s27, 0
v_mfma_f32_16x16x32_bf16 v[104:107], v[208:211], a[100:103], v[104:107]
ds_read_b64_tr_b16 v[212:213], v242 offset:17024
ds_read_b64_tr_b16 v[214:215], v242 offset:18048
v_mfma_f32_16x16x32_bf16 v[108:111], v[216:219], a[100:103], v[108:111]
s_and_b32 s87, s87, 0xffff
s_add_u32 s93, s93, 150994944
v_mfma_f32_16x16x32_bf16 v[108:111], v[220:223], a[116:119], v[108:111]
s_add_u32 s68, s26, s93
s_addc_u32 s69, s27, 0
v_mfma_f32_16x16x32_bf16 v[76:79], v[220:223], a[112:115], v[76:79]
s_and_b32 s69, s69, 0xffff
s_mov_b32 s70, 17408
v_mfma_f32_16x16x32_bf16 v[76:79], v[216:219], a[96:99], v[76:79]
ds_read_b64_tr_b16 v[208:209], v242 offset:25472
ds_read_b64_tr_b16 v[210:211], v242 offset:26496
v_mfma_f32_16x16x32_bf16 v[44:47], v[216:219], v[140:143], v[44:47]
ds_write_b128 v238, v[248:251] offset:37888
v_mfma_f32_16x16x32_bf16 v[44:47], v[220:223], a[108:111], v[44:47]
buffer_store_dwordx4 a[160:163], v255, s[48:51], s18 offen offset:3072 sc1
v_mfma_f32_16x16x32_bf16 v[12:15], v[220:223], a[104:107], v[12:15]
s_waitcnt lgkmcnt(5)
s_mov_b32 s96, 152962078
v_mfma_f32_16x16x32_bf16 v[12:15], v[216:219], v[128:131], v[12:15]
ds_read_b64_tr_b16 v[220:221], v243 offset:17024
ds_read_b64_tr_b16 v[222:223], v243 offset:18048
v_mfma_f32_16x16x32_bf16 v[16:19], v[196:199], v[128:131], v[16:19]
v_accvgpr_read_b32 v224, a192
v_accvgpr_read_b32 v225, a193
v_mfma_f32_16x16x32_bf16 v[16:19], v[192:195], a[104:107], v[16:19]
buffer_load_dwordx4 v[176:179], v244, s[52:55], s91 offen offset:3072 sc1
v_mfma_f32_16x16x32_bf16 v[48:51], v[192:195], a[108:111], v[48:51]
v_accvgpr_read_b32 v226, a194
v_accvgpr_read_b32 v227, a195
v_mfma_f32_16x16x32_bf16 v[48:51], v[196:199], v[140:143], v[48:51]
ds_read_b64_tr_b16 v[216:217], v243 offset:25472
ds_read_b64_tr_b16 v[218:219], v243 offset:26496
v_mfma_f32_16x16x32_bf16 v[80:83], v[196:199], a[96:99], v[80:83]
v_accvgpr_read_b32 v228, a196
v_accvgpr_read_b32 v229, a197
v_mfma_f32_16x16x32_bf16 v[80:83], v[192:195], a[112:115], v[80:83]
buffer_load_dwordx4 v[180:183], v245, s[52:55], s91 offen offset:3072 sc1
v_mfma_f32_16x16x32_bf16 v[112:115], v[192:195], a[116:119], v[112:115]
v_accvgpr_read_b32 v230, a198
v_accvgpr_read_b32 v231, a199
v_mfma_f32_16x16x32_bf16 v[112:115], v[196:199], a[100:103], v[112:115]
v_cvt_pk_bf16_f32 v248, v224, v225
v_cvt_pk_bf16_f32 v249, v226, v227
v_mfma_f32_16x16x32_bf16 v[116:119], v[204:207], a[100:103], v[116:119]
s_waitcnt lgkmcnt(0)
.La5_LW1_11:
s_cbranch_execz .La5_LW1_15
s_barrier
v_mfma_f32_16x16x32_bf16 v[116:119], v[200:203], a[116:119], v[116:119]
buffer_load_dwordx4 v[184:187], v246, s[52:55], s91 offen offset:3072 sc1
v_mfma_f32_16x16x32_bf16 v[84:87], v[200:203], a[112:115], v[84:87]
ds_read_b64_tr_b16 v[192:193], v240 offset:33792
ds_read_b64_tr_b16 v[194:195], v240 offset:34816
v_mfma_f32_16x16x32_bf16 v[84:87], v[204:207], a[96:99], v[84:87]
ds_read_b64_tr_b16 v[196:197], v240 offset:42240
ds_read_b64_tr_b16 v[198:199], v240 offset:43264
v_mfma_f32_16x16x32_bf16 v[52:55], v[204:207], v[140:143], v[52:55]
s_add_u32 m0, s83, 16896
v_cvt_pk_bf16_f32 v250, v228, v229
v_mfma_f32_16x16x32_bf16 v[52:55], v[200:203], a[108:111], v[52:55]
buffer_load_dwordx4 v232, s[44:47], s80 offen lds
v_mfma_f32_16x16x32_bf16 v[20:23], v[200:203], a[104:107], v[20:23]
v_cvt_pk_bf16_f32 v251, v230, v231
v_accvgpr_write_b32 a172, v248
v_mfma_f32_16x16x32_bf16 v[20:23], v[204:207], v[128:131], v[20:23]
ds_read_b64_tr_b16 v[200:201], v241 offset:33792
ds_read_b64_tr_b16 v[202:203], v241 offset:34816
v_mfma_f32_16x16x32_bf16 v[24:27], v[212:215], v[128:131], v[24:27]
v_accvgpr_write_b32 a173, v249
v_accvgpr_write_b32 a174, v250
v_mfma_f32_16x16x32_bf16 v[24:27], v[208:211], a[104:107], v[24:27]
buffer_load_dwordx4 v[188:191], v247, s[52:55], s91 offen offset:3072 sc1
v_mfma_f32_16x16x32_bf16 v[56:59], v[208:211], a[108:111], v[56:59]
s_add_u32 s91, s91, 4096
v_accvgpr_write_b32 a175, v251
v_mfma_f32_16x16x32_bf16 v[56:59], v[212:215], v[140:143], v[56:59]
ds_read_b64_tr_b16 v[204:205], v241 offset:42240
ds_read_b64_tr_b16 v[206:207], v241 offset:43264
v_mfma_f32_16x16x32_bf16 v[88:91], v[212:215], a[96:99], v[88:91]
s_add_u32 m0, s83, 17920
s_mov_b32 s97, 371069214
v_mfma_f32_16x16x32_bf16 v[88:91], v[208:211], a[112:115], v[88:91]
buffer_load_dwordx4 v233, s[44:47], s80 offen lds
v_mfma_f32_16x16x32_bf16 v[120:123], v[208:211], a[116:119], v[120:123]
s_lshl_b32 s95, s73, 3
s_lshr_b64 s[96:97], s[96:97], s95
v_mfma_f32_16x16x32_bf16 v[120:123], v[212:215], a[100:103], v[120:123]
ds_read_b64_tr_b16 v[208:209], v242 offset:33792
ds_read_b64_tr_b16 v[210:211], v242 offset:34816
v_mfma_f32_16x16x32_bf16 v[124:127], v[220:223], a[100:103], v[124:127]
s_and_b32 s82, s96, 255
s_lshl_b32 s95, 1, s82
v_mfma_f32_16x16x32_bf16 v[124:127], v[216:219], a[116:119], v[124:127]
s_lshl_b32 s96, s75, 22
s_or_b32 s95, s95, s96
v_mfma_f32_16x16x32_bf16 v[92:95], v[216:219], a[112:115], v[92:95]
s_cmp_eq_u32 s73, 1
s_cselect_b32 s96, 12, 4
s_cselect_b32 s79, 0, 0xff000000
v_mfma_f32_16x16x32_bf16 v[92:95], v[220:223], a[96:99], v[92:95]
ds_read_b64_tr_b16 v[212:213], v242 offset:42240
ds_read_b64_tr_b16 v[214:215], v242 offset:43264
v_mfma_f32_16x16x32_bf16 v[60:63], v[220:223], v[140:143], v[60:63]
ds_write_b128 v238, v[248:251] offset:38912
v_mfma_f32_16x16x32_bf16 v[60:63], v[216:219], a[108:111], v[60:63]
s_or_b32 s98, s95, s96
s_mov_b32 s99, 0
v_mfma_f32_16x16x32_bf16 v[28:31], v[216:219], a[104:107], v[28:31]
s_waitcnt vmcnt(15) lgkmcnt(5)
v_mfma_f32_16x16x32_bf16 v[28:31], v[220:223], v[128:131], v[28:31]
ds_read_b64_tr_b16 v[216:217], v243 offset:33792
ds_read_b64_tr_b16 v[218:219], v243 offset:34816
v_mfma_f32_16x16x32_bf16 v[0:3], v[192:195], v[132:135], v[0:3]
v_accvgpr_read_b32 v224, a176
v_accvgpr_read_b32 v225, a177
v_mfma_f32_16x16x32_bf16 v[0:3], v[196:199], v[152:155], v[0:3]
buffer_load_dwordx4 v[128:131], v244, s[52:55], s91 offen offset:0 sc1
v_mfma_f32_16x16x32_bf16 v[32:35], v[196:199], v[156:159], v[32:35]
v_accvgpr_read_b32 v226, a178
v_accvgpr_read_b32 v227, a179
v_mfma_f32_16x16x32_bf16 v[32:35], v[192:195], v[136:139], v[32:35]
ds_read_b64_tr_b16 v[220:221], v243 offset:42240
ds_read_b64_tr_b16 v[222:223], v243 offset:43264
v_mfma_f32_16x16x32_bf16 v[64:67], v[192:195], v[144:147], v[64:67]
v_accvgpr_read_b32 v228, a180
v_accvgpr_read_b32 v229, a181
v_mfma_f32_16x16x32_bf16 v[64:67], v[196:199], a[120:123], v[64:67]
buffer_load_dwordx4 v[140:143], v245, s[52:55], s91 offen offset:0 sc1
v_mfma_f32_16x16x32_bf16 v[96:99], v[196:199], a[124:127], v[96:99]
v_accvgpr_read_b32 v230, a182
v_accvgpr_read_b32 v231, a183
v_mfma_f32_16x16x32_bf16 v[96:99], v[192:195], v[148:151], v[96:99]
ds_read_b64_tr_b16 v[196:197], v240 offset:33920
ds_read_b64_tr_b16 v[198:199], v240 offset:34944
v_mfma_f32_16x16x32_bf16 v[100:103], v[200:203], v[148:151], v[100:103]
s_add_u32 m0, s83, 19072
v_cvt_pk_bf16_f32 v248, v224, v225
v_mfma_f32_16x16x32_bf16 v[100:103], v[204:207], a[124:127], v[100:103]
buffer_load_dwordx4 v234, s[44:47], s80 offen lds
v_mfma_f32_16x16x32_bf16 v[68:71], v[204:207], a[120:123], v[68:71]
v_cvt_pk_bf16_f32 v249, v226, v227
v_cvt_pk_bf16_f32 v250, v228, v229
v_mfma_f32_16x16x32_bf16 v[68:71], v[200:203], v[144:147], v[68:71]
ds_read_b64_tr_b16 v[192:193], v240 offset:42368
ds_read_b64_tr_b16 v[194:195], v240 offset:43392
v_mfma_f32_16x16x32_bf16 v[36:39], v[200:203], v[136:139], v[36:39]
v_cvt_pk_bf16_f32 v251, v230, v231
v_accvgpr_write_b32 a176, v248
v_mfma_f32_16x16x32_bf16 v[36:39], v[204:207], v[156:159], v[36:39]
buffer_load_dwordx4 a[96:99], v246, s[52:55], s91 offen offset:0 sc1
v_mfma_f32_16x16x32_bf16 v[4:7], v[204:207], v[152:155], v[4:7]
s_waitcnt lgkmcnt(4)
v_accvgpr_write_b32 a177, v249
v_mfma_f32_16x16x32_bf16 v[4:7], v[200:203], v[132:135], v[4:7]
ds_read_b64_tr_b16 v[204:205], v241 offset:33920
ds_read_b64_tr_b16 v[206:207], v241 offset:34944
v_mfma_f32_16x16x32_bf16 v[8:11], v[208:211], v[132:135], v[8:11]
s_add_u32 m0, s83, 20096
v_accvgpr_write_b32 a178, v250
v_mfma_f32_16x16x32_bf16 v[8:11], v[212:215], v[152:155], v[8:11]
buffer_load_dwordx4 v235, s[44:47], s80 offen lds
v_mfma_f32_16x16x32_bf16 v[40:43], v[212:215], v[156:159], v[40:43]
s_add_u32 s80, s80, 524288
v_accvgpr_write_b32 a179, v251
v_mfma_f32_16x16x32_bf16 v[40:43], v[208:211], v[136:139], v[40:43]
ds_read_b64_tr_b16 v[200:201], v241 offset:42368
ds_read_b64_tr_b16 v[202:203], v241 offset:43392
v_mfma_f32_16x16x32_bf16 v[72:75], v[208:211], v[144:147], v[72:75]
s_cmp_eq_u32 s74, 3
s_cselect_b32 s23, 3, 0
s_cselect_b32 s22, 0, s75
v_mfma_f32_16x16x32_bf16 v[72:75], v[212:215], a[120:123], v[72:75]
buffer_load_dwordx4 a[100:103], v247, s[52:55], s91 offen offset:0 sc1
v_mfma_f32_16x16x32_bf16 v[104:107], v[212:215], a[124:127], v[104:107]
s_lshl_b32 s95, s76, 5
s_add_u32 s95, s95, s3
v_mfma_f32_16x16x32_bf16 v[104:107], v[208:211], v[148:151], v[104:107]
ds_read_b64_tr_b16 v[212:213], v242 offset:33920
ds_read_b64_tr_b16 v[214:215], v242 offset:34944
v_mfma_f32_16x16x32_bf16 v[108:111], v[216:219], v[148:151], v[108:111]
s_lshl_b32 s95, s95, 5
s_add_u32 s95, s95, s72
v_mfma_f32_16x16x32_bf16 v[108:111], v[220:223], a[124:127], v[108:111]
s_lshl_b32 s95, s95, 3
s_add_u32 s6, s95, 1048576
v_mfma_f32_16x16x32_bf16 v[76:79], v[220:223], a[120:123], v[76:79]
s_mov_b32 s18, 36864
v_and_b32 v239, 0x70, v255
v_mfma_f32_16x16x32_bf16 v[76:79], v[216:219], v[144:147], v[76:79]
ds_read_b64_tr_b16 v[208:209], v242 offset:42368
ds_read_b64_tr_b16 v[210:211], v242 offset:43392
v_mfma_f32_16x16x32_bf16 v[44:47], v[216:219], v[136:139], v[44:47]
ds_write_b128 v238, v[248:251] offset:39936
v_mfma_f32_16x16x32_bf16 v[44:47], v[220:223], v[156:159], v[44:47]
buffer_store_dwordx4 a[164:167], v255, s[48:51], s18 offen offset:0 sc1
v_mfma_f32_16x16x32_bf16 v[12:15], v[220:223], v[152:155], v[12:15]
s_waitcnt lgkmcnt(5)
v_lshlrev_b32 v239, 6, v239
v_mfma_f32_16x16x32_bf16 v[12:15], v[216:219], v[132:135], v[12:15]
ds_read_b64_tr_b16 v[220:221], v243 offset:33920
ds_read_b64_tr_b16 v[222:223], v243 offset:34944
v_mfma_f32_16x16x32_bf16 v[16:19], v[196:199], v[132:135], v[16:19]
v_accvgpr_read_b32 v224, a200
v_accvgpr_read_b32 v225, a201
v_mfma_f32_16x16x32_bf16 v[16:19], v[192:195], v[152:155], v[16:19]
buffer_load_dwordx4 a[104:107], v244, s[52:55], s91 offen offset:1024 sc1
v_mfma_f32_16x16x32_bf16 v[48:51], v[192:195], v[156:159], v[48:51]
v_accvgpr_read_b32 v226, a202
v_accvgpr_read_b32 v227, a203
v_mfma_f32_16x16x32_bf16 v[48:51], v[196:199], v[136:139], v[48:51]
ds_read_b64_tr_b16 v[216:217], v243 offset:42368
ds_read_b64_tr_b16 v[218:219], v243 offset:43392
v_mfma_f32_16x16x32_bf16 v[80:83], v[196:199], v[144:147], v[80:83]
v_accvgpr_read_b32 v228, a204
v_accvgpr_read_b32 v229, a205
v_mfma_f32_16x16x32_bf16 v[80:83], v[192:195], a[120:123], v[80:83]
buffer_load_dwordx4 a[108:111], v245, s[52:55], s91 offen offset:1024 sc1
v_mfma_f32_16x16x32_bf16 v[112:115], v[192:195], a[124:127], v[112:115]
v_accvgpr_read_b32 v230, a206
v_accvgpr_read_b32 v231, a207
v_mfma_f32_16x16x32_bf16 v[112:115], v[196:199], v[148:151], v[112:115]
v_cvt_pk_bf16_f32 v248, v224, v225
v_cvt_pk_bf16_f32 v249, v226, v227
v_mfma_f32_16x16x32_bf16 v[116:119], v[204:207], v[148:151], v[116:119]
s_waitcnt lgkmcnt(0)
s_barrier
v_mfma_f32_16x16x32_bf16 v[116:119], v[200:203], a[124:127], v[116:119]
buffer_load_dwordx4 a[112:115], v246, s[52:55], s91 offen offset:1024 sc1
v_mfma_f32_16x16x32_bf16 v[84:87], v[200:203], a[120:123], v[84:87]
ds_read_b64_tr_b16 v[192:193], v240 offset:50688
ds_read_b64_tr_b16 v[194:195], v240 offset:51712
v_mfma_f32_16x16x32_bf16 v[84:87], v[204:207], v[144:147], v[84:87]
ds_read_b64_tr_b16 v[196:197], v240 offset:59136
ds_read_b64_tr_b16 v[198:199], v240 offset:60160
v_mfma_f32_16x16x32_bf16 v[52:55], v[204:207], v[136:139], v[52:55]
s_add_u32 m0, s83, 33792
v_cvt_pk_bf16_f32 v250, v228, v229
v_mfma_f32_16x16x32_bf16 v[52:55], v[200:203], v[156:159], v[52:55]
buffer_load_dwordx4 v232, s[44:47], s80 offen lds
v_mfma_f32_16x16x32_bf16 v[20:23], v[200:203], v[152:155], v[20:23]
v_cvt_pk_bf16_f32 v251, v230, v231
v_accvgpr_write_b32 a180, v248
v_mfma_f32_16x16x32_bf16 v[20:23], v[204:207], v[132:135], v[20:23]
ds_read_b64_tr_b16 v[200:201], v241 offset:50688
ds_read_b64_tr_b16 v[202:203], v241 offset:51712
v_mfma_f32_16x16x32_bf16 v[24:27], v[212:215], v[132:135], v[24:27]
v_accvgpr_write_b32 a181, v249
v_accvgpr_write_b32 a182, v250
v_mfma_f32_16x16x32_bf16 v[24:27], v[208:211], v[152:155], v[24:27]
buffer_load_dwordx4 a[116:119], v247, s[52:55], s91 offen offset:1024 sc1
v_mfma_f32_16x16x32_bf16 v[56:59], v[208:211], v[156:159], v[56:59]
v_accvgpr_write_b32 a183, v251
s_add_u32 s85, s85, 65536
v_mfma_f32_16x16x32_bf16 v[56:59], v[212:215], v[136:139], v[56:59]
ds_read_b64_tr_b16 v[204:205], v241 offset:59136
ds_read_b64_tr_b16 v[206:207], v241 offset:60160
v_mfma_f32_16x16x32_bf16 v[88:91], v[212:215], v[144:147], v[88:91]
s_add_u32 m0, s83, 34816
v_add_u32 v238, s85, v255
v_mfma_f32_16x16x32_bf16 v[88:91], v[208:211], a[120:123], v[88:91]
buffer_load_dwordx4 v233, s[44:47], s80 offen lds
v_mfma_f32_16x16x32_bf16 v[120:123], v[208:211], a[124:127], v[120:123]
s_mov_b32 s85, 0
v_accvgpr_write_b32 a231, v239
v_mfma_f32_16x16x32_bf16 v[120:123], v[212:215], v[148:151], v[120:123]
ds_read_b64_tr_b16 v[208:209], v242 offset:50688
ds_read_b64_tr_b16 v[210:211], v242 offset:51712
v_mfma_f32_16x16x32_bf16 v[124:127], v[220:223], v[148:151], v[124:127]
s_mul_i32 s95, s22, 0
s_sub_u32 s96, 2, s95
v_mfma_f32_16x16x32_bf16 v[124:127], v[216:219], a[124:127], v[124:127]
s_add_u32 s95, s95, 2
v_lshrrev_b32 v239, 7, v255
v_mfma_f32_16x16x32_bf16 v[92:95], v[216:219], a[120:123], v[92:95]
v_mul_u32_u24 v254, s21, v239
v_add_u32 v239, s96, v239
v_mfma_f32_16x16x32_bf16 v[92:95], v[220:223], v[144:147], v[92:95]
ds_read_b64_tr_b16 v[212:213], v242 offset:59136
ds_read_b64_tr_b16 v[214:215], v242 offset:60160
v_mfma_f32_16x16x32_bf16 v[60:63], v[220:223], v[136:139], v[60:63]
ds_write_b128 v238, v[248:251] offset:0
v_mfma_f32_16x16x32_bf16 v[60:63], v[216:219], v[156:159], v[60:63]
buffer_store_dwordx4 a[168:171], v255, s[48:51], s18 offen offset:1024 sc1
v_mfma_f32_16x16x32_bf16 v[28:31], v[216:219], v[152:155], v[28:31]
s_waitcnt vmcnt(15) lgkmcnt(5)
v_lshrrev_b32 v239, 2, v239
v_mfma_f32_16x16x32_bf16 v[28:31], v[220:223], v[132:135], v[28:31]
ds_read_b64_tr_b16 v[216:217], v243 offset:50688
ds_read_b64_tr_b16 v[218:219], v243 offset:51712
v_mfma_f32_16x16x32_bf16 v[0:3], v[192:195], v[160:163], v[0:3]
v_accvgpr_read_b32 v224, a184
v_accvgpr_read_b32 v225, a185
v_mfma_f32_16x16x32_bf16 v[0:3], v[196:199], v[176:179], v[0:3]
buffer_load_dwordx4 v[132:135], v244, s[52:55], s91 offen offset:2048 sc1
v_mfma_f32_16x16x32_bf16 v[32:35], v[196:199], v[180:183], v[32:35]
v_accvgpr_read_b32 v226, a186
v_accvgpr_read_b32 v227, a187
v_mfma_f32_16x16x32_bf16 v[32:35], v[192:195], v[164:167], v[32:35]
ds_read_b64_tr_b16 v[220:221], v243 offset:59136
ds_read_b64_tr_b16 v[222:223], v243 offset:60160
v_mfma_f32_16x16x32_bf16 v[64:67], v[192:195], v[168:171], v[64:67]
v_accvgpr_read_b32 v228, a188
v_accvgpr_read_b32 v229, a189
v_mfma_f32_16x16x32_bf16 v[64:67], v[196:199], v[184:187], v[64:67]
buffer_load_dwordx4 v[136:139], v245, s[52:55], s91 offen offset:2048 sc1
v_mfma_f32_16x16x32_bf16 v[96:99], v[196:199], v[188:191], v[96:99]
v_accvgpr_read_b32 v230, a190
v_accvgpr_read_b32 v231, a191
v_mfma_f32_16x16x32_bf16 v[96:99], v[192:195], v[172:175], v[96:99]
ds_read_b64_tr_b16 v[196:197], v240 offset:50816
ds_read_b64_tr_b16 v[198:199], v240 offset:51840
v_mfma_f32_16x16x32_bf16 v[100:103], v[200:203], v[172:175], v[100:103]
s_add_u32 m0, s83, 35968
v_cvt_pk_bf16_f32 v248, v224, v225
v_mfma_f32_16x16x32_bf16 v[100:103], v[204:207], v[188:191], v[100:103]
buffer_load_dwordx4 v234, s[44:47], s80 offen lds
v_mfma_f32_16x16x32_bf16 v[68:71], v[204:207], v[184:187], v[68:71]
v_cvt_pk_bf16_f32 v249, v226, v227
v_cvt_pk_bf16_f32 v250, v228, v229
v_mfma_f32_16x16x32_bf16 v[68:71], v[200:203], v[168:171], v[68:71]
ds_read_b64_tr_b16 v[192:193], v240 offset:59264
ds_read_b64_tr_b16 v[194:195], v240 offset:60288
v_mfma_f32_16x16x32_bf16 v[36:39], v[200:203], v[164:167], v[36:39]
v_cvt_pk_bf16_f32 v251, v230, v231
v_accvgpr_write_b32 a184, v248
v_mfma_f32_16x16x32_bf16 v[36:39], v[204:207], v[180:183], v[36:39]
buffer_load_dwordx4 v[144:147], v246, s[52:55], s91 offen offset:2048 sc1
v_mfma_f32_16x16x32_bf16 v[4:7], v[204:207], v[176:179], v[4:7]
s_waitcnt lgkmcnt(4)
v_accvgpr_write_b32 a185, v249
v_mfma_f32_16x16x32_bf16 v[4:7], v[200:203], v[160:163], v[4:7]
ds_read_b64_tr_b16 v[204:205], v241 offset:50816
ds_read_b64_tr_b16 v[206:207], v241 offset:51840
v_mfma_f32_16x16x32_bf16 v[8:11], v[208:211], v[160:163], v[8:11]
s_add_u32 m0, s83, 36992
v_accvgpr_write_b32 a186, v250
v_mfma_f32_16x16x32_bf16 v[8:11], v[212:215], v[176:179], v[8:11]
buffer_load_dwordx4 v235, s[44:47], s80 offen lds
v_mfma_f32_16x16x32_bf16 v[40:43], v[212:215], v[180:183], v[40:43]
s_add_u32 s80, s80, 524288
v_accvgpr_write_b32 a187, v251
v_mfma_f32_16x16x32_bf16 v[40:43], v[208:211], v[164:167], v[40:43]
ds_read_b64_tr_b16 v[200:201], v241 offset:59264
ds_read_b64_tr_b16 v[202:203], v241 offset:60288
v_mfma_f32_16x16x32_bf16 v[72:75], v[208:211], v[168:171], v[72:75]
v_lshl_add_u32 v254, v239, 28, v254
v_and_b32 v239, 0x70, v255
v_mfma_f32_16x16x32_bf16 v[72:75], v[212:215], v[184:187], v[72:75]
buffer_load_dwordx4 v[148:151], v247, s[52:55], s91 offen offset:2048 sc1
v_mfma_f32_16x16x32_bf16 v[104:107], v[212:215], v[188:191], v[104:107]
v_lshl_add_u32 v254, v239, 3, v254
s_mul_i32 s21, s21, s95
v_mfma_f32_16x16x32_bf16 v[104:107], v[208:211], v[172:175], v[104:107]
ds_read_b64_tr_b16 v[212:213], v242 offset:50816
ds_read_b64_tr_b16 v[214:215], v242 offset:51840
v_mfma_f32_16x16x32_bf16 v[108:111], v[216:219], v[172:175], v[108:111]
v_lshrrev_b32 v236, 8, v255
v_lshlrev_b32 v239, 5, v236
v_mfma_f32_16x16x32_bf16 v[108:111], v[220:223], v[188:191], v[108:111]
v_xor_b32 v239, v239, v255
v_and_b32 v239, 0xf0, v239
v_mfma_f32_16x16x32_bf16 v[76:79], v[220:223], v[184:187], v[76:79]
v_lshl_add_u32 v239, v236, 11, v239
s_lshl_b32 s92, s77, 16
v_mfma_f32_16x16x32_bf16 v[76:79], v[216:219], v[168:171], v[76:79]
ds_read_b64_tr_b16 v[208:209], v242 offset:59264
ds_read_b64_tr_b16 v[210:211], v242 offset:60288
v_mfma_f32_16x16x32_bf16 v[44:47], v[216:219], v[164:167], v[44:47]
ds_write_b128 v238, v[248:251] offset:1024
v_mfma_f32_16x16x32_bf16 v[44:47], v[220:223], v[180:183], v[44:47]
buffer_store_dwordx4 a[172:175], v255, s[48:51], s18 offen offset:2048 sc1
v_mfma_f32_16x16x32_bf16 v[12:15], v[220:223], v[176:179], v[12:15]
s_waitcnt lgkmcnt(5)
s_lshl_b32 s93, s78, 14
v_mfma_f32_16x16x32_bf16 v[12:15], v[216:219], v[160:163], v[12:15]
ds_read_b64_tr_b16 v[220:221], v243 offset:50816
ds_read_b64_tr_b16 v[222:223], v243 offset:51840
v_mfma_f32_16x16x32_bf16 v[16:19], v[196:199], v[160:163], v[16:19]
v_accvgpr_read_b32 v224, a64
v_accvgpr_read_b32 v225, a65
v_mfma_f32_16x16x32_bf16 v[16:19], v[192:195], v[176:179], v[16:19]
buffer_load_dwordx4 v[152:155], v244, s[52:55], s91 offen offset:3072 sc1
v_mfma_f32_16x16x32_bf16 v[48:51], v[192:195], v[180:183], v[48:51]
v_accvgpr_read_b32 v226, a66
v_accvgpr_read_b32 v227, a67
v_mfma_f32_16x16x32_bf16 v[48:51], v[196:199], v[164:167], v[48:51]
ds_read_b64_tr_b16 v[216:217], v243 offset:59264
ds_read_b64_tr_b16 v[218:219], v243 offset:60288
v_mfma_f32_16x16x32_bf16 v[80:83], v[196:199], v[168:171], v[80:83]
v_accvgpr_read_b32 v228, a68
v_accvgpr_read_b32 v229, a69
v_mfma_f32_16x16x32_bf16 v[80:83], v[192:195], v[184:187], v[80:83]
buffer_load_dwordx4 v[156:159], v245, s[52:55], s91 offen offset:3072 sc1
v_mfma_f32_16x16x32_bf16 v[112:115], v[192:195], v[188:191], v[112:115]
v_accvgpr_read_b32 v230, a70
v_accvgpr_read_b32 v231, a71
v_mfma_f32_16x16x32_bf16 v[112:115], v[196:199], v[172:175], v[112:115]
v_cvt_pk_bf16_f32 v248, v224, v225
v_cvt_pk_bf16_f32 v249, v226, v227
v_mfma_f32_16x16x32_bf16 v[116:119], v[204:207], v[172:175], v[116:119]
s_waitcnt lgkmcnt(0)
s_barrier
v_mfma_f32_16x16x32_bf16 v[116:119], v[200:203], v[188:191], v[116:119]
buffer_load_dwordx4 a[120:123], v246, s[52:55], s91 offen offset:3072 sc1
v_mfma_f32_16x16x32_bf16 v[84:87], v[200:203], v[184:187], v[84:87]
ds_read_b64_tr_b16 v[192:193], v240 offset:0
ds_read_b64_tr_b16 v[194:195], v240 offset:1024
v_mfma_f32_16x16x32_bf16 v[84:87], v[204:207], v[168:171], v[84:87]
ds_read_b64_tr_b16 v[196:197], v240 offset:8448
ds_read_b64_tr_b16 v[198:199], v240 offset:9472
v_mfma_f32_16x16x32_bf16 v[52:55], v[204:207], v[164:167], v[52:55]
s_add_u32 m0, s83, 50688
v_cvt_pk_bf16_f32 v250, v228, v229
v_mfma_f32_16x16x32_bf16 v[52:55], v[200:203], v[180:183], v[52:55]
buffer_load_dwordx4 v232, s[44:47], s80 offen lds
v_mfma_f32_16x16x32_bf16 v[20:23], v[200:203], v[176:179], v[20:23]
v_cvt_pk_bf16_f32 v251, v230, v231
v_accvgpr_write_b32 a188, v248
v_mfma_f32_16x16x32_bf16 v[20:23], v[204:207], v[160:163], v[20:23]
ds_read_b64_tr_b16 v[200:201], v241 offset:0
ds_read_b64_tr_b16 v[202:203], v241 offset:1024
v_mfma_f32_16x16x32_bf16 v[24:27], v[212:215], v[160:163], v[24:27]
v_accvgpr_write_b32 a189, v249
v_accvgpr_write_b32 a190, v250
v_mfma_f32_16x16x32_bf16 v[24:27], v[208:211], v[176:179], v[24:27]
buffer_load_dwordx4 a[124:127], v247, s[52:55], s91 offen offset:3072 sc1
v_mfma_f32_16x16x32_bf16 v[56:59], v[208:211], v[180:183], v[56:59]
s_add_u32 s91, s91, 4096
v_accvgpr_write_b32 a191, v251
v_mfma_f32_16x16x32_bf16 v[56:59], v[212:215], v[164:167], v[56:59]
ds_read_b64_tr_b16 v[204:205], v241 offset:8448
ds_read_b64_tr_b16 v[206:207], v241 offset:9472
v_mfma_f32_16x16x32_bf16 v[88:91], v[212:215], v[168:171], v[88:91]
s_add_u32 m0, s83, 51712
s_add_u32 s92, s92, s93
v_mfma_f32_16x16x32_bf16 v[88:91], v[208:211], v[184:187], v[88:91]
buffer_load_dwordx4 v233, s[44:47], s80 offen lds
v_mfma_f32_16x16x32_bf16 v[120:123], v[208:211], v[188:191], v[120:123]
v_add_u32 v239, s92, v239
v_accvgpr_write_b32 a228, v239
v_mfma_f32_16x16x32_bf16 v[120:123], v[212:215], v[172:175], v[120:123]
ds_read_b64_tr_b16 v[208:209], v242 offset:0
ds_read_b64_tr_b16 v[210:211], v242 offset:1024
v_mfma_f32_16x16x32_bf16 v[124:127], v[220:223], v[172:175], v[124:127]
v_bfe_u32 v239, v255, 6, 2
v_lshlrev_b32 v239, 7, v239
v_mfma_f32_16x16x32_bf16 v[124:127], v[216:219], v[188:191], v[124:127]
v_lshl_add_u32 v239, v236, 5, v239
s_lshl_b32 s92, s78, 9
v_mfma_f32_16x16x32_bf16 v[92:95], v[216:219], v[184:187], v[92:95]
v_add_u32 v239, s92, v239
v_mul_u32_u24 v239, 7168, v239
v_mfma_f32_16x16x32_bf16 v[92:95], v[220:223], v[168:171], v[92:95]
ds_read_b64_tr_b16 v[212:213], v242 offset:8448
ds_read_b64_tr_b16 v[214:215], v242 offset:9472
v_mfma_f32_16x16x32_bf16 v[60:63], v[220:223], v[164:167], v[60:63]
ds_write_b128 v238, v[248:251] offset:2048
v_mfma_f32_16x16x32_bf16 v[60:63], v[216:219], v[180:183], v[60:63]
buffer_store_dwordx4 a[176:179], v255, s[48:51], s18 offen offset:3072 sc1
v_mfma_f32_16x16x32_bf16 v[28:31], v[216:219], v[176:179], v[28:31]
s_waitcnt vmcnt(16) lgkmcnt(5)
v_and_or_b32 v239, v255, 48, v239
v_mfma_f32_16x16x32_bf16 v[28:31], v[220:223], v[160:163], v[28:31]
ds_read_b64_tr_b16 v[216:217], v243 offset:0
ds_read_b64_tr_b16 v[218:219], v243 offset:1024
v_mfma_f32_16x16x32_bf16 v[0:3], v[192:195], v[128:131], v[0:3]
v_accvgpr_read_b32 v224, a0
v_accvgpr_read_b32 v225, a1
v_mfma_f32_16x16x32_bf16 v[0:3], v[196:199], a[104:107], v[0:3]
buffer_load_dwordx4 v[160:163], v244, s[52:55], s91 offen offset:0 sc1
v_mfma_f32_16x16x32_bf16 v[32:35], v[196:199], a[108:111], v[32:35]
v_accvgpr_read_b32 v226, a2
v_accvgpr_read_b32 v227, a3
v_mfma_f32_16x16x32_bf16 v[32:35], v[192:195], v[140:143], v[32:35]
ds_read_b64_tr_b16 v[220:221], v243 offset:8448
ds_read_b64_tr_b16 v[222:223], v243 offset:9472
v_mfma_f32_16x16x32_bf16 v[64:67], v[192:195], a[96:99], v[64:67]
v_accvgpr_read_b32 v228, a4
v_accvgpr_read_b32 v229, a5
v_mfma_f32_16x16x32_bf16 v[64:67], v[196:199], a[112:115], v[64:67]
buffer_load_dwordx4 v[164:167], v245, s[52:55], s91 offen offset:0 sc1
v_mfma_f32_16x16x32_bf16 v[96:99], v[196:199], a[116:119], v[96:99]
v_accvgpr_read_b32 v230, a6
v_accvgpr_read_b32 v231, a7
v_mfma_f32_16x16x32_bf16 v[96:99], v[192:195], a[100:103], v[96:99]
ds_read_b64_tr_b16 v[196:197], v240 offset:128
ds_read_b64_tr_b16 v[198:199], v240 offset:1152
v_mfma_f32_16x16x32_bf16 v[100:103], v[200:203], a[100:103], v[100:103]
s_add_u32 m0, s83, 52864
v_cvt_pk_bf16_f32 v248, v224, v225
v_mfma_f32_16x16x32_bf16 v[100:103], v[204:207], a[116:119], v[100:103]
buffer_load_dwordx4 v234, s[44:47], s80 offen lds
v_mfma_f32_16x16x32_bf16 v[68:71], v[204:207], a[112:115], v[68:71]
v_cvt_pk_bf16_f32 v249, v226, v227
v_cvt_pk_bf16_f32 v250, v228, v229
v_mfma_f32_16x16x32_bf16 v[68:71], v[200:203], a[96:99], v[68:71]
ds_read_b64_tr_b16 v[192:193], v240 offset:8576
ds_read_b64_tr_b16 v[194:195], v240 offset:9600
v_mfma_f32_16x16x32_bf16 v[36:39], v[200:203], v[140:143], v[36:39]
v_cvt_pk_bf16_f32 v251, v230, v231
v_accvgpr_write_b32 a0, v248
v_mfma_f32_16x16x32_bf16 v[36:39], v[204:207], a[108:111], v[36:39]
buffer_load_dwordx4 v[168:171], v246, s[52:55], s91 offen offset:0 sc1
v_mfma_f32_16x16x32_bf16 v[4:7], v[204:207], a[104:107], v[4:7]
s_waitcnt lgkmcnt(4)
v_accvgpr_write_b32 a1, v249
v_mfma_f32_16x16x32_bf16 v[4:7], v[200:203], v[128:131], v[4:7]
ds_read_b64_tr_b16 v[204:205], v241 offset:128
ds_read_b64_tr_b16 v[206:207], v241 offset:1152
v_mfma_f32_16x16x32_bf16 v[8:11], v[208:211], v[128:131], v[8:11]
s_add_u32 m0, s83, 53888
v_accvgpr_write_b32 a2, v250
v_mfma_f32_16x16x32_bf16 v[8:11], v[212:215], a[104:107], v[8:11]
buffer_load_dwordx4 v235, s[44:47], s80 offen lds
v_mfma_f32_16x16x32_bf16 v[40:43], v[212:215], a[108:111], v[40:43]
s_add_u32 s80, s80, 524288
v_accvgpr_write_b32 a3, v251
v_mfma_f32_16x16x32_bf16 v[40:43], v[208:211], v[140:143], v[40:43]
ds_read_b64_tr_b16 v[200:201], v241 offset:8576
ds_read_b64_tr_b16 v[202:203], v241 offset:9600
v_mfma_f32_16x16x32_bf16 v[72:75], v[208:211], a[96:99], v[72:75]
v_accvgpr_write_b32 a229, v239
v_and_b32 v239, 0xf0, v255
v_mfma_f32_16x16x32_bf16 v[72:75], v[212:215], a[112:115], v[72:75]
buffer_load_dwordx4 v[172:175], v247, s[52:55], s91 offen offset:0 sc1
v_mfma_f32_16x16x32_bf16 v[104:107], v[212:215], a[116:119], v[104:107]
v_lshl_add_u32 v239, v236, 14, v239
s_lshl_b32 s92, s3, 9
v_mfma_f32_16x16x32_bf16 v[104:107], v[208:211], a[100:103], v[104:107]
ds_read_b64_tr_b16 v[212:213], v242 offset:128
ds_read_b64_tr_b16 v[214:215], v242 offset:1152
v_mfma_f32_16x16x32_bf16 v[108:111], v[216:219], a[100:103], v[108:111]
v_add_u32 v239, s92, v239
v_accvgpr_write_b32 a230, v239
v_mfma_f32_16x16x32_bf16 v[108:111], v[220:223], a[116:119], v[108:111]
v_add_u32 v239, 4294886912, v240
v_accvgpr_write_b32 a224, v239
v_mfma_f32_16x16x32_bf16 v[76:79], v[220:223], a[112:115], v[76:79]
s_mov_b32 s18, 16384
v_bfe_u32 v239, v255, 4, 4
v_mfma_f32_16x16x32_bf16 v[76:79], v[216:219], a[96:99], v[76:79]
ds_read_b64_tr_b16 v[208:209], v242 offset:8576
ds_read_b64_tr_b16 v[210:211], v242 offset:9600
v_mfma_f32_16x16x32_bf16 v[44:47], v[216:219], v[140:143], v[44:47]
ds_write_b128 v238, v[248:251] offset:3072
v_mfma_f32_16x16x32_bf16 v[44:47], v[220:223], a[108:111], v[44:47]
buffer_store_dwordx4 a[180:183], v255, s[48:51], s18 offen offset:0 sc1
v_mfma_f32_16x16x32_bf16 v[12:15], v[220:223], a[104:107], v[12:15]
s_waitcnt lgkmcnt(5)
v_lshlrev_b32 v236, 2, v236
v_mfma_f32_16x16x32_bf16 v[12:15], v[216:219], v[128:131], v[12:15]
ds_read_b64_tr_b16 v[220:221], v243 offset:128
ds_read_b64_tr_b16 v[222:223], v243 offset:1152
v_mfma_f32_16x16x32_bf16 v[16:19], v[196:199], v[128:131], v[16:19]
v_accvgpr_read_b32 v224, a72
v_accvgpr_read_b32 v225, a73
v_mfma_f32_16x16x32_bf16 v[16:19], v[192:195], a[104:107], v[16:19]
buffer_load_dwordx4 v[176:179], v244, s[52:55], s91 offen offset:1024 sc1
v_mfma_f32_16x16x32_bf16 v[48:51], v[192:195], a[108:111], v[48:51]
v_accvgpr_read_b32 v226, a74
v_accvgpr_read_b32 v227, a75
v_mfma_f32_16x16x32_bf16 v[48:51], v[196:199], v[140:143], v[48:51]
ds_read_b64_tr_b16 v[216:217], v243 offset:8576
ds_read_b64_tr_b16 v[218:219], v243 offset:9600
v_mfma_f32_16x16x32_bf16 v[80:83], v[196:199], a[96:99], v[80:83]
v_accvgpr_read_b32 v228, a76
v_accvgpr_read_b32 v229, a77
v_mfma_f32_16x16x32_bf16 v[80:83], v[192:195], a[112:115], v[80:83]
buffer_load_dwordx4 v[180:183], v245, s[52:55], s91 offen offset:1024 sc1
v_mfma_f32_16x16x32_bf16 v[112:115], v[192:195], a[116:119], v[112:115]
v_accvgpr_read_b32 v230, a78
v_accvgpr_read_b32 v231, a79
v_mfma_f32_16x16x32_bf16 v[112:115], v[196:199], a[100:103], v[112:115]
v_cvt_pk_bf16_f32 v248, v224, v225
v_cvt_pk_bf16_f32 v249, v226, v227
v_mfma_f32_16x16x32_bf16 v[116:119], v[204:207], a[100:103], v[116:119]
s_waitcnt lgkmcnt(0)
s_barrier
v_mfma_f32_16x16x32_bf16 v[116:119], v[200:203], a[116:119], v[116:119]
buffer_load_dwordx4 v[184:187], v246, s[52:55], s91 offen offset:1024 sc1
v_mfma_f32_16x16x32_bf16 v[84:87], v[200:203], a[112:115], v[84:87]
ds_read_b64_tr_b16 v[192:193], v240 offset:16896
ds_read_b64_tr_b16 v[194:195], v240 offset:17920
v_mfma_f32_16x16x32_bf16 v[84:87], v[204:207], a[96:99], v[84:87]
ds_read_b64_tr_b16 v[196:197], v240 offset:25344
ds_read_b64_tr_b16 v[198:199], v240 offset:26368
v_mfma_f32_16x16x32_bf16 v[52:55], v[204:207], v[140:143], v[52:55]
.La5_LW1_12:
s_cbranch_execz .La5_LW1_R0
s_add_u32 m0, s83, 0
v_cvt_pk_bf16_f32 v250, v228, v229
v_mfma_f32_16x16x32_bf16 v[52:55], v[200:203], a[108:111], v[52:55]
buffer_load_dwordx4 v232, s[44:47], s80 offen lds
v_mfma_f32_16x16x32_bf16 v[20:23], v[200:203], a[104:107], v[20:23]
v_cvt_pk_bf16_f32 v251, v230, v231
v_accvgpr_write_b32 a4, v248
v_mfma_f32_16x16x32_bf16 v[20:23], v[204:207], v[128:131], v[20:23]
ds_read_b64_tr_b16 v[200:201], v241 offset:16896
ds_read_b64_tr_b16 v[202:203], v241 offset:17920
v_mfma_f32_16x16x32_bf16 v[24:27], v[212:215], v[128:131], v[24:27]
v_accvgpr_write_b32 a5, v249
v_accvgpr_write_b32 a6, v250
v_mfma_f32_16x16x32_bf16 v[24:27], v[208:211], a[104:107], v[24:27]
buffer_load_dwordx4 v[188:191], v247, s[52:55], s91 offen offset:1024 sc1
v_mfma_f32_16x16x32_bf16 v[56:59], v[208:211], a[108:111], v[56:59]
v_accvgpr_write_b32 a7, v251
v_sub_u32 v239, v239, v236
v_mfma_f32_16x16x32_bf16 v[56:59], v[212:215], v[140:143], v[56:59]
ds_read_b64_tr_b16 v[204:205], v241 offset:25344
ds_read_b64_tr_b16 v[206:207], v241 offset:26368
v_mfma_f32_16x16x32_bf16 v[88:91], v[212:215], a[96:99], v[88:91]
s_add_u32 m0, s83, 1024
v_subrev_u32 v236, 0, v239
v_mfma_f32_16x16x32_bf16 v[88:91], v[208:211], a[112:115], v[88:91]
buffer_load_dwordx4 v233, s[44:47], s80 offen lds
v_mfma_f32_16x16x32_bf16 v[120:123], v[208:211], a[116:119], v[120:123]
v_ashrrev_i32 v236, 31, v236
v_and_b32 v236, 0xff800000, v236
v_mfma_f32_16x16x32_bf16 v[120:123], v[212:215], a[100:103], v[120:123]
ds_read_b64_tr_b16 v[208:209], v242 offset:16896
ds_read_b64_tr_b16 v[210:211], v242 offset:17920
v_mfma_f32_16x16x32_bf16 v[124:127], v[220:223], a[100:103], v[124:127]
v_accvgpr_write_b32 a236, v236
v_subrev_u32 v236, 1, v239
v_mfma_f32_16x16x32_bf16 v[124:127], v[216:219], a[116:119], v[124:127]
v_ashrrev_i32 v236, 31, v236
v_and_b32 v236, 0xff800000, v236
v_mfma_f32_16x16x32_bf16 v[92:95], v[216:219], a[112:115], v[92:95]
v_accvgpr_write_b32 a237, v236
v_subrev_u32 v236, 2, v239
v_mfma_f32_16x16x32_bf16 v[92:95], v[220:223], a[96:99], v[92:95]
ds_read_b64_tr_b16 v[212:213], v242 offset:25344
ds_read_b64_tr_b16 v[214:215], v242 offset:26368
v_mfma_f32_16x16x32_bf16 v[60:63], v[220:223], v[140:143], v[60:63]
ds_write_b128 v238, v[248:251] offset:4096
v_mfma_f32_16x16x32_bf16 v[60:63], v[216:219], a[108:111], v[60:63]
v_ashrrev_i32 v236, 31, v236
v_and_b32 v236, 0xff800000, v236
v_mfma_f32_16x16x32_bf16 v[28:31], v[216:219], a[104:107], v[28:31]
s_waitcnt vmcnt(15) lgkmcnt(5)
v_accvgpr_write_b32 a238, v236
v_mfma_f32_16x16x32_bf16 v[28:31], v[220:223], v[128:131], v[28:31]
ds_read_b64_tr_b16 v[216:217], v243 offset:16896
ds_read_b64_tr_b16 v[218:219], v243 offset:17920
v_mfma_f32_16x16x32_bf16 v[0:3], v[192:195], v[132:135], v[0:3]
v_accvgpr_read_b32 v224, a8
v_accvgpr_read_b32 v225, a9
v_mfma_f32_16x16x32_bf16 v[0:3], v[196:199], v[152:155], v[0:3]
buffer_load_dwordx4 v[128:131], v244, s[52:55], s91 offen offset:2048 sc1
v_mfma_f32_16x16x32_bf16 v[32:35], v[196:199], v[156:159], v[32:35]
v_accvgpr_read_b32 v226, a10
v_accvgpr_read_b32 v227, a11
v_mfma_f32_16x16x32_bf16 v[32:35], v[192:195], v[136:139], v[32:35]
ds_read_b64_tr_b16 v[220:221], v243 offset:25344
ds_read_b64_tr_b16 v[222:223], v243 offset:26368
v_mfma_f32_16x16x32_bf16 v[64:67], v[192:195], v[144:147], v[64:67]
v_accvgpr_read_b32 v228, a12
v_accvgpr_read_b32 v229, a13
v_mfma_f32_16x16x32_bf16 v[64:67], v[196:199], a[120:123], v[64:67]
buffer_load_dwordx4 v[140:143], v245, s[52:55], s91 offen offset:2048 sc1
v_mfma_f32_16x16x32_bf16 v[96:99], v[196:199], a[124:127], v[96:99]
v_accvgpr_read_b32 v230, a14
v_accvgpr_read_b32 v231, a15
v_mfma_f32_16x16x32_bf16 v[96:99], v[192:195], v[148:151], v[96:99]
ds_read_b64_tr_b16 v[196:197], v240 offset:17024
ds_read_b64_tr_b16 v[198:199], v240 offset:18048
v_mfma_f32_16x16x32_bf16 v[100:103], v[200:203], v[148:151], v[100:103]
s_add_u32 m0, s83, 2176
v_cvt_pk_bf16_f32 v248, v224, v225
v_mfma_f32_16x16x32_bf16 v[100:103], v[204:207], a[124:127], v[100:103]
buffer_load_dwordx4 v234, s[44:47], s80 offen lds
v_mfma_f32_16x16x32_bf16 v[68:71], v[204:207], a[120:123], v[68:71]
v_cvt_pk_bf16_f32 v249, v226, v227
v_cvt_pk_bf16_f32 v250, v228, v229
v_mfma_f32_16x16x32_bf16 v[68:71], v[200:203], v[144:147], v[68:71]
ds_read_b64_tr_b16 v[192:193], v240 offset:25472
ds_read_b64_tr_b16 v[194:195], v240 offset:26496
v_mfma_f32_16x16x32_bf16 v[36:39], v[200:203], v[136:139], v[36:39]
v_cvt_pk_bf16_f32 v251, v230, v231
v_accvgpr_write_b32 a8, v248
v_mfma_f32_16x16x32_bf16 v[36:39], v[204:207], v[156:159], v[36:39]
buffer_load_dwordx4 a[96:99], v246, s[52:55], s91 offen offset:2048 sc1
v_mfma_f32_16x16x32_bf16 v[4:7], v[204:207], v[152:155], v[4:7]
s_waitcnt lgkmcnt(4)
v_accvgpr_write_b32 a9, v249
v_mfma_f32_16x16x32_bf16 v[4:7], v[200:203], v[132:135], v[4:7]
ds_read_b64_tr_b16 v[204:205], v241 offset:17024
ds_read_b64_tr_b16 v[206:207], v241 offset:18048
v_mfma_f32_16x16x32_bf16 v[8:11], v[208:211], v[132:135], v[8:11]
s_add_u32 m0, s83, 3200
v_accvgpr_write_b32 a10, v250
v_mfma_f32_16x16x32_bf16 v[8:11], v[212:215], v[152:155], v[8:11]
buffer_load_dwordx4 v235, s[44:47], s80 offen lds
v_mfma_f32_16x16x32_bf16 v[40:43], v[212:215], v[156:159], v[40:43]
s_add_u32 s80, s80, 524288
v_accvgpr_write_b32 a11, v251
v_mfma_f32_16x16x32_bf16 v[40:43], v[208:211], v[136:139], v[40:43]
ds_read_b64_tr_b16 v[200:201], v241 offset:25472
ds_read_b64_tr_b16 v[202:203], v241 offset:26496
v_mfma_f32_16x16x32_bf16 v[72:75], v[208:211], v[144:147], v[72:75]
v_subrev_u32 v236, 3, v239
v_ashrrev_i32 v236, 31, v236
v_mfma_f32_16x16x32_bf16 v[72:75], v[212:215], a[120:123], v[72:75]
buffer_load_dwordx4 a[100:103], v247, s[52:55], s91 offen offset:2048 sc1
v_mfma_f32_16x16x32_bf16 v[104:107], v[212:215], a[124:127], v[104:107]
v_and_b32 v236, 0xff800000, v236
v_accvgpr_write_b32 a239, v236
v_mfma_f32_16x16x32_bf16 v[104:107], v[208:211], v[148:151], v[104:107]
ds_read_b64_tr_b16 v[212:213], v242 offset:17024
ds_read_b64_tr_b16 v[214:215], v242 offset:18048
v_mfma_f32_16x16x32_bf16 v[108:111], v[216:219], v[148:151], v[108:111]
v_lshlrev_b32 v236, 4, v239
s_mov_b32 s92, 0x3f80
v_mfma_f32_16x16x32_bf16 v[108:111], v[220:223], a[124:127], v[108:111]
v_lshlrev_b32 v236, v236, s92
v_cmp_gt_u32 vcc, 2, v239
v_mfma_f32_16x16x32_bf16 v[76:79], v[220:223], a[120:123], v[76:79]
v_subrev_u32 v239, 2, v239
v_cmp_gt_u32 s[92:93], 2, v239
v_mfma_f32_16x16x32_bf16 v[76:79], v[216:219], v[144:147], v[76:79]
ds_read_b64_tr_b16 v[208:209], v242 offset:25472
ds_read_b64_tr_b16 v[210:211], v242 offset:26496
v_mfma_f32_16x16x32_bf16 v[44:47], v[216:219], v[136:139], v[44:47]
ds_write_b128 v238, v[248:251] offset:5120
v_mfma_f32_16x16x32_bf16 v[44:47], v[220:223], v[156:159], v[44:47]
buffer_store_dwordx4 a[184:187], v255, s[48:51], s18 offen offset:1024 sc1
v_mfma_f32_16x16x32_bf16 v[12:15], v[220:223], v[152:155], v[12:15]
s_waitcnt lgkmcnt(5)
v_cndmask_b32 v239, 0, v236, vcc
v_mfma_f32_16x16x32_bf16 v[12:15], v[216:219], v[132:135], v[12:15]
ds_read_b64_tr_b16 v[220:221], v243 offset:17024
ds_read_b64_tr_b16 v[222:223], v243 offset:18048
v_mfma_f32_16x16x32_bf16 v[16:19], v[196:199], v[132:135], v[16:19]
v_accvgpr_read_b32 v224, a80
v_accvgpr_read_b32 v225, a81
v_mfma_f32_16x16x32_bf16 v[16:19], v[192:195], v[152:155], v[16:19]
buffer_load_dwordx4 a[104:107], v244, s[52:55], s91 offen offset:3072 sc1
v_mfma_f32_16x16x32_bf16 v[48:51], v[192:195], v[156:159], v[48:51]
v_accvgpr_read_b32 v226, a82
v_accvgpr_read_b32 v227, a83
v_mfma_f32_16x16x32_bf16 v[48:51], v[196:199], v[136:139], v[48:51]
ds_read_b64_tr_b16 v[216:217], v243 offset:25472
ds_read_b64_tr_b16 v[218:219], v243 offset:26496
v_mfma_f32_16x16x32_bf16 v[80:83], v[196:199], v[144:147], v[80:83]
v_accvgpr_read_b32 v228, a84
v_accvgpr_read_b32 v229, a85
v_mfma_f32_16x16x32_bf16 v[80:83], v[192:195], a[120:123], v[80:83]
buffer_load_dwordx4 a[108:111], v245, s[52:55], s91 offen offset:3072 sc1
v_mfma_f32_16x16x32_bf16 v[112:115], v[192:195], a[124:127], v[112:115]
v_accvgpr_read_b32 v230, a86
v_accvgpr_read_b32 v231, a87
v_mfma_f32_16x16x32_bf16 v[112:115], v[196:199], v[148:151], v[112:115]
v_cvt_pk_bf16_f32 v248, v224, v225
v_cvt_pk_bf16_f32 v249, v226, v227
v_mfma_f32_16x16x32_bf16 v[116:119], v[204:207], v[148:151], v[116:119]
s_waitcnt lgkmcnt(0)
s_barrier
v_mfma_f32_16x16x32_bf16 v[116:119], v[200:203], a[124:127], v[116:119]
buffer_load_dwordx4 a[112:115], v246, s[52:55], s91 offen offset:3072 sc1
v_mfma_f32_16x16x32_bf16 v[84:87], v[200:203], a[120:123], v[84:87]
ds_read_b64_tr_b16 v[192:193], v240 offset:33792
ds_read_b64_tr_b16 v[194:195], v240 offset:34816
v_mfma_f32_16x16x32_bf16 v[84:87], v[204:207], v[144:147], v[84:87]
ds_read_b64_tr_b16 v[196:197], v240 offset:42240
ds_read_b64_tr_b16 v[198:199], v240 offset:43264
v_mfma_f32_16x16x32_bf16 v[52:55], v[204:207], v[136:139], v[52:55]
s_add_u32 m0, s83, 16896
v_cvt_pk_bf16_f32 v250, v228, v229
v_mfma_f32_16x16x32_bf16 v[52:55], v[200:203], v[156:159], v[52:55]
buffer_load_dwordx4 v232, s[44:47], s80 offen lds
v_mfma_f32_16x16x32_bf16 v[20:23], v[200:203], v[152:155], v[20:23]
v_cvt_pk_bf16_f32 v251, v230, v231
v_accvgpr_write_b32 a12, v248
v_mfma_f32_16x16x32_bf16 v[20:23], v[204:207], v[132:135], v[20:23]
ds_read_b64_tr_b16 v[200:201], v241 offset:33792
ds_read_b64_tr_b16 v[202:203], v241 offset:34816
v_mfma_f32_16x16x32_bf16 v[24:27], v[212:215], v[132:135], v[24:27]
v_accvgpr_write_b32 a13, v249
v_accvgpr_write_b32 a14, v250
v_mfma_f32_16x16x32_bf16 v[24:27], v[208:211], v[152:155], v[24:27]
buffer_load_dwordx4 a[116:119], v247, s[52:55], s91 offen offset:3072 sc1
v_mfma_f32_16x16x32_bf16 v[56:59], v[208:211], v[156:159], v[56:59]
s_add_u32 s91, s91, 4096
v_accvgpr_write_b32 a15, v251
v_mfma_f32_16x16x32_bf16 v[56:59], v[212:215], v[136:139], v[56:59]
ds_read_b64_tr_b16 v[204:205], v241 offset:42240
ds_read_b64_tr_b16 v[206:207], v241 offset:43264
v_mfma_f32_16x16x32_bf16 v[88:91], v[212:215], v[144:147], v[88:91]
s_add_u32 m0, s83, 17920
v_accvgpr_write_b32 a212, 0
v_mfma_f32_16x16x32_bf16 v[88:91], v[208:211], a[120:123], v[88:91]
buffer_load_dwordx4 v233, s[44:47], s80 offen lds
v_mfma_f32_16x16x32_bf16 v[120:123], v[208:211], a[124:127], v[120:123]
v_accvgpr_write_b32 a214, v239
v_cndmask_b32 v239, 0, v236, s[92:93]
v_mfma_f32_16x16x32_bf16 v[120:123], v[212:215], v[148:151], v[120:123]
ds_read_b64_tr_b16 v[208:209], v242 offset:33792
ds_read_b64_tr_b16 v[210:211], v242 offset:34816
v_mfma_f32_16x16x32_bf16 v[124:127], v[220:223], v[148:151], v[124:127]
v_mov_b32 v236, 0x3f803f80
v_accvgpr_write_b32 a215, v239
v_mfma_f32_16x16x32_bf16 v[124:127], v[216:219], a[124:127], v[124:127]
v_accvgpr_write_b32 a213, 0
v_accvgpr_write_b32 a216, 0
v_mfma_f32_16x16x32_bf16 v[92:95], v[216:219], a[120:123], v[92:95]
v_accvgpr_write_b32 a217, 0
v_lshrrev_b32 v239, 4, v255
v_mfma_f32_16x16x32_bf16 v[92:95], v[220:223], v[144:147], v[92:95]
ds_read_b64_tr_b16 v[212:213], v242 offset:42240
ds_read_b64_tr_b16 v[214:215], v242 offset:43264
v_mfma_f32_16x16x32_bf16 v[60:63], v[220:223], v[136:139], v[60:63]
v_xor_b32 v239, v239, v255
v_and_b32 v239, 32, v239
v_mfma_f32_16x16x32_bf16 v[60:63], v[216:219], v[156:159], v[60:63]
buffer_store_dwordx4 a[188:191], v255, s[48:51], s18 offen offset:2048 sc1
v_mfma_f32_16x16x32_bf16 v[28:31], v[216:219], v[152:155], v[28:31]
s_waitcnt vmcnt(15) lgkmcnt(4)
v_cmp_eq_u32 vcc, 0, v239
v_mfma_f32_16x16x32_bf16 v[28:31], v[220:223], v[132:135], v[28:31]
ds_read_b64_tr_b16 v[216:217], v243 offset:33792
ds_read_b64_tr_b16 v[218:219], v243 offset:34816
v_mfma_f32_16x16x32_bf16 v[0:3], v[192:195], v[160:163], v[0:3]
v_accvgpr_read_b32 v224, a16
v_accvgpr_read_b32 v225, a17
v_mfma_f32_16x16x32_bf16 v[0:3], v[196:199], v[176:179], v[0:3]
buffer_load_dwordx4 v[132:135], v244, s[52:55], s91 offen offset:0 sc1
v_mfma_f32_16x16x32_bf16 v[32:35], v[196:199], v[180:183], v[32:35]
v_accvgpr_read_b32 v226, a18
v_accvgpr_read_b32 v227, a19
v_mfma_f32_16x16x32_bf16 v[32:35], v[192:195], v[164:167], v[32:35]
ds_read_b64_tr_b16 v[220:221], v243 offset:42240
ds_read_b64_tr_b16 v[222:223], v243 offset:43264
v_mfma_f32_16x16x32_bf16 v[64:67], v[192:195], v[168:171], v[64:67]
v_accvgpr_read_b32 v228, a20
v_accvgpr_read_b32 v229, a21
v_mfma_f32_16x16x32_bf16 v[64:67], v[196:199], v[184:187], v[64:67]
buffer_load_dwordx4 v[136:139], v245, s[52:55], s91 offen offset:0 sc1
v_mfma_f32_16x16x32_bf16 v[96:99], v[196:199], v[188:191], v[96:99]
v_accvgpr_read_b32 v230, a22
v_accvgpr_read_b32 v231, a23
v_mfma_f32_16x16x32_bf16 v[96:99], v[192:195], v[172:175], v[96:99]
ds_read_b64_tr_b16 v[196:197], v240 offset:33920
ds_read_b64_tr_b16 v[198:199], v240 offset:34944
v_mfma_f32_16x16x32_bf16 v[100:103], v[200:203], v[172:175], v[100:103]
s_add_u32 m0, s83, 19072
v_cvt_pk_bf16_f32 v248, v224, v225
v_mfma_f32_16x16x32_bf16 v[100:103], v[204:207], v[188:191], v[100:103]
buffer_load_dwordx4 v234, s[44:47], s80 offen lds
v_mfma_f32_16x16x32_bf16 v[68:71], v[204:207], v[184:187], v[68:71]
v_cvt_pk_bf16_f32 v249, v226, v227
v_cvt_pk_bf16_f32 v250, v228, v229
v_mfma_f32_16x16x32_bf16 v[68:71], v[200:203], v[168:171], v[68:71]
ds_read_b64_tr_b16 v[192:193], v240 offset:42368
ds_read_b64_tr_b16 v[194:195], v240 offset:43392
v_mfma_f32_16x16x32_bf16 v[36:39], v[200:203], v[164:167], v[36:39]
v_cvt_pk_bf16_f32 v251, v230, v231
v_accvgpr_write_b32 a16, v248
v_mfma_f32_16x16x32_bf16 v[36:39], v[204:207], v[180:183], v[36:39]
buffer_load_dwordx4 v[144:147], v246, s[52:55], s91 offen offset:0 sc1
v_mfma_f32_16x16x32_bf16 v[4:7], v[204:207], v[176:179], v[4:7]
s_waitcnt lgkmcnt(4)
v_accvgpr_write_b32 a17, v249
v_mfma_f32_16x16x32_bf16 v[4:7], v[200:203], v[160:163], v[4:7]
ds_read_b64_tr_b16 v[204:205], v241 offset:33920
ds_read_b64_tr_b16 v[206:207], v241 offset:34944
v_mfma_f32_16x16x32_bf16 v[8:11], v[208:211], v[160:163], v[8:11]
s_add_u32 m0, s83, 20096
v_accvgpr_write_b32 a18, v250
v_mfma_f32_16x16x32_bf16 v[8:11], v[212:215], v[176:179], v[8:11]
buffer_load_dwordx4 v235, s[44:47], s80 offen lds
v_mfma_f32_16x16x32_bf16 v[40:43], v[212:215], v[180:183], v[40:43]
s_add_u32 s80, s80, 524288
v_accvgpr_write_b32 a19, v251
v_mfma_f32_16x16x32_bf16 v[40:43], v[208:211], v[164:167], v[40:43]
ds_read_b64_tr_b16 v[200:201], v241 offset:42368
ds_read_b64_tr_b16 v[202:203], v241 offset:43392
v_mfma_f32_16x16x32_bf16 v[72:75], v[208:211], v[168:171], v[72:75]
s_mov_b32 s78, 4294950911
s_mul_i32 s70, s70, s22
v_mfma_f32_16x16x32_bf16 v[72:75], v[212:215], v[184:187], v[72:75]
buffer_load_dwordx4 v[148:151], v247, s[52:55], s91 offen offset:0 sc1
v_mfma_f32_16x16x32_bf16 v[104:107], v[212:215], v[188:191], v[104:107]
v_cndmask_b32 v236, 0, v236, vcc
s_sub_u32 s92, 4294967295, s3
v_mfma_f32_16x16x32_bf16 v[104:107], v[208:211], v[172:175], v[104:107]
ds_read_b64_tr_b16 v[212:213], v242 offset:33920
ds_read_b64_tr_b16 v[214:215], v242 offset:34944
v_mfma_f32_16x16x32_bf16 v[108:111], v[216:219], v[172:175], v[108:111]
s_mul_i32 s90, s92, s21
s_mov_b32 s17, 1073741824
v_mfma_f32_16x16x32_bf16 v[108:111], v[220:223], v[188:191], v[108:111]
s_lshl_b32 s92, s73, 2
s_lshr_b32 s92, 66051, s92
v_mfma_f32_16x16x32_bf16 v[76:79], v[220:223], v[184:187], v[76:79]
s_and_b32 s92, s92, 3
s_lshl_b32 s16, s92, 16
v_mfma_f32_16x16x32_bf16 v[76:79], v[216:219], v[168:171], v[76:79]
ds_read_b64_tr_b16 v[208:209], v242 offset:42368
ds_read_b64_tr_b16 v[210:211], v242 offset:43392
v_mfma_f32_16x16x32_bf16 v[44:47], v[216:219], v[164:167], v[44:47]
s_mul_i32 s92, s75, 4294836224
s_add_u32 s92, s92, s88
v_mfma_f32_16x16x32_bf16 v[44:47], v[220:223], v[180:183], v[44:47]
buffer_store_dwordx4 a[0:3], v255, s[48:51], s18 offen offset:3072 sc1
v_mfma_f32_16x16x32_bf16 v[12:15], v[220:223], v[176:179], v[12:15]
s_waitcnt lgkmcnt(4)
s_add_u32 s19, s92, 393216
v_mfma_f32_16x16x32_bf16 v[12:15], v[216:219], v[160:163], v[12:15]
ds_read_b64_tr_b16 v[220:221], v243 offset:33920
ds_read_b64_tr_b16 v[222:223], v243 offset:34944
v_mfma_f32_16x16x32_bf16 v[16:19], v[196:199], v[160:163], v[16:19]
v_accvgpr_read_b32 v224, a88
v_accvgpr_read_b32 v225, a89
v_mfma_f32_16x16x32_bf16 v[16:19], v[192:195], v[176:179], v[16:19]
buffer_load_dwordx4 v[152:155], v244, s[52:55], s91 offen offset:1024 sc1
v_mfma_f32_16x16x32_bf16 v[48:51], v[192:195], v[180:183], v[48:51]
v_accvgpr_read_b32 v226, a90
v_accvgpr_read_b32 v227, a91
v_mfma_f32_16x16x32_bf16 v[48:51], v[196:199], v[164:167], v[48:51]
ds_read_b64_tr_b16 v[216:217], v243 offset:42368
ds_read_b64_tr_b16 v[218:219], v243 offset:43392
v_mfma_f32_16x16x32_bf16 v[80:83], v[196:199], v[168:171], v[80:83]
v_accvgpr_read_b32 v228, a92
v_accvgpr_read_b32 v229, a93
v_mfma_f32_16x16x32_bf16 v[80:83], v[192:195], v[184:187], v[80:83]
buffer_load_dwordx4 v[156:159], v245, s[52:55], s91 offen offset:1024 sc1
v_mfma_f32_16x16x32_bf16 v[112:115], v[192:195], v[188:191], v[112:115]
v_accvgpr_read_b32 v230, a94
v_accvgpr_read_b32 v231, a95
v_mfma_f32_16x16x32_bf16 v[112:115], v[196:199], v[172:175], v[112:115]
v_cvt_pk_bf16_f32 v248, v224, v225
v_cvt_pk_bf16_f32 v249, v226, v227
v_mfma_f32_16x16x32_bf16 v[116:119], v[204:207], v[172:175], v[116:119]
s_waitcnt lgkmcnt(0)
s_barrier
v_mfma_f32_16x16x32_bf16 v[116:119], v[200:203], v[188:191], v[116:119]
buffer_load_dwordx4 a[120:123], v246, s[52:55], s91 offen offset:1024 sc1
v_mfma_f32_16x16x32_bf16 v[84:87], v[200:203], v[184:187], v[84:87]
ds_read_b64_tr_b16 v[192:193], v240 offset:50688
ds_read_b64_tr_b16 v[194:195], v240 offset:51712
v_mfma_f32_16x16x32_bf16 v[84:87], v[204:207], v[168:171], v[84:87]
ds_read_b64_tr_b16 v[196:197], v240 offset:59136
ds_read_b64_tr_b16 v[198:199], v240 offset:60160
v_mfma_f32_16x16x32_bf16 v[52:55], v[204:207], v[164:167], v[52:55]
s_add_u32 m0, s83, 33792
v_cvt_pk_bf16_f32 v250, v228, v229
v_mfma_f32_16x16x32_bf16 v[52:55], v[200:203], v[180:183], v[52:55]
buffer_load_dwordx4 v232, s[44:47], s80 offen lds
v_mfma_f32_16x16x32_bf16 v[20:23], v[200:203], v[176:179], v[20:23]
v_cvt_pk_bf16_f32 v251, v230, v231
v_accvgpr_write_b32 a20, v248
v_mfma_f32_16x16x32_bf16 v[20:23], v[204:207], v[160:163], v[20:23]
ds_read_b64_tr_b16 v[200:201], v241 offset:50688
ds_read_b64_tr_b16 v[202:203], v241 offset:51712
v_mfma_f32_16x16x32_bf16 v[24:27], v[212:215], v[160:163], v[24:27]
v_accvgpr_write_b32 a21, v249
v_accvgpr_write_b32 a22, v250
v_mfma_f32_16x16x32_bf16 v[24:27], v[208:211], v[176:179], v[24:27]
buffer_load_dwordx4 a[124:127], v247, s[52:55], s91 offen offset:1024 sc1
v_mfma_f32_16x16x32_bf16 v[56:59], v[208:211], v[180:183], v[56:59]
v_accvgpr_write_b32 a23, v251
v_accvgpr_write_b32 a208, v236
v_mfma_f32_16x16x32_bf16 v[56:59], v[212:215], v[164:167], v[56:59]
ds_read_b64_tr_b16 v[204:205], v241 offset:59136
ds_read_b64_tr_b16 v[206:207], v241 offset:60160
v_mfma_f32_16x16x32_bf16 v[88:91], v[212:215], v[168:171], v[88:91]
s_add_u32 m0, s83, 34816
v_accvgpr_write_b32 a209, v236
v_mfma_f32_16x16x32_bf16 v[88:91], v[208:211], v[184:187], v[88:91]
buffer_load_dwordx4 v233, s[44:47], s80 offen lds
v_mfma_f32_16x16x32_bf16 v[120:123], v[208:211], v[188:191], v[120:123]
v_accvgpr_write_b32 a210, v236
v_accvgpr_write_b32 a211, v236
v_mfma_f32_16x16x32_bf16 v[120:123], v[212:215], v[172:175], v[120:123]
ds_read_b64_tr_b16 v[208:209], v242 offset:50688
ds_read_b64_tr_b16 v[210:211], v242 offset:51712
v_mfma_f32_16x16x32_bf16 v[124:127], v[220:223], v[172:175], v[124:127]
v_accvgpr_write_b32 a232, 0
v_accvgpr_write_b32 a233, 0
v_mfma_f32_16x16x32_bf16 v[124:127], v[216:219], v[188:191], v[124:127]
v_accvgpr_write_b32 a234, 0
v_accvgpr_write_b32 a235, 0
v_mfma_f32_16x16x32_bf16 v[92:95], v[216:219], v[184:187], v[92:95]
s_mov_b32 s18, 20480
s_lshl_b32 s20, s72, 10
v_mfma_f32_16x16x32_bf16 v[92:95], v[220:223], v[168:171], v[92:95]
ds_read_b64_tr_b16 v[212:213], v242 offset:59136
ds_read_b64_tr_b16 v[214:215], v242 offset:60160
v_mfma_f32_16x16x32_bf16 v[60:63], v[220:223], v[164:167], v[60:63]
s_add_u32 s20, s20, 16384
s_mov_b32 s4, 1985221408
v_mfma_f32_16x16x32_bf16 v[60:63], v[216:219], v[180:183], v[60:63]
buffer_store_dwordx4 a[4:7], v255, s[48:51], s18 offen offset:0 sc1
v_mfma_f32_16x16x32_bf16 v[28:31], v[216:219], v[176:179], v[28:31]
s_waitcnt vmcnt(16) lgkmcnt(4)
v_mfma_f32_16x16x32_bf16 v[28:31], v[220:223], v[160:163], v[28:31]
ds_read_b64_tr_b16 v[216:217], v243 offset:50688
ds_read_b64_tr_b16 v[218:219], v243 offset:51712
v_mfma_f32_16x16x32_bf16 v[0:3], v[192:195], v[128:131], v[0:3]
v_accvgpr_read_b32 v224, a24
v_accvgpr_read_b32 v225, a25
v_mfma_f32_16x16x32_bf16 v[0:3], v[196:199], a[104:107], v[0:3]
buffer_load_dwordx4 v[160:163], v244, s[52:55], s91 offen offset:2048 sc1
v_mfma_f32_16x16x32_bf16 v[32:35], v[196:199], a[108:111], v[32:35]
v_accvgpr_read_b32 v226, a26
v_accvgpr_read_b32 v227, a27
v_mfma_f32_16x16x32_bf16 v[32:35], v[192:195], v[140:143], v[32:35]
ds_read_b64_tr_b16 v[220:221], v243 offset:59136
ds_read_b64_tr_b16 v[222:223], v243 offset:60160
v_mfma_f32_16x16x32_bf16 v[64:67], v[192:195], a[96:99], v[64:67]
v_accvgpr_read_b32 v228, a28
v_accvgpr_read_b32 v229, a29
v_mfma_f32_16x16x32_bf16 v[64:67], v[196:199], a[112:115], v[64:67]
buffer_load_dwordx4 v[164:167], v245, s[52:55], s91 offen offset:2048 sc1
v_mfma_f32_16x16x32_bf16 v[96:99], v[196:199], a[116:119], v[96:99]
v_accvgpr_read_b32 v230, a30
v_accvgpr_read_b32 v231, a31
v_mfma_f32_16x16x32_bf16 v[96:99], v[192:195], a[100:103], v[96:99]
ds_read_b64_tr_b16 v[196:197], v240 offset:50816
ds_read_b64_tr_b16 v[198:199], v240 offset:51840
v_mfma_f32_16x16x32_bf16 v[100:103], v[200:203], a[100:103], v[100:103]
s_add_u32 m0, s83, 35968
v_cvt_pk_bf16_f32 v248, v224, v225
v_mfma_f32_16x16x32_bf16 v[100:103], v[204:207], a[116:119], v[100:103]
buffer_load_dwordx4 v234, s[44:47], s80 offen lds
v_mfma_f32_16x16x32_bf16 v[68:71], v[204:207], a[112:115], v[68:71]
v_cvt_pk_bf16_f32 v249, v226, v227
v_cvt_pk_bf16_f32 v250, v228, v229
v_mfma_f32_16x16x32_bf16 v[68:71], v[200:203], a[96:99], v[68:71]
ds_read_b64_tr_b16 v[192:193], v240 offset:59264
ds_read_b64_tr_b16 v[194:195], v240 offset:60288
v_mfma_f32_16x16x32_bf16 v[36:39], v[200:203], v[140:143], v[36:39]
v_cvt_pk_bf16_f32 v251, v230, v231
v_accvgpr_write_b32 a24, v248
v_mfma_f32_16x16x32_bf16 v[36:39], v[204:207], a[108:111], v[36:39]
buffer_load_dwordx4 v[168:171], v246, s[52:55], s91 offen offset:2048 sc1
v_mfma_f32_16x16x32_bf16 v[4:7], v[204:207], a[104:107], v[4:7]
s_waitcnt lgkmcnt(4)
v_accvgpr_write_b32 a25, v249
v_mfma_f32_16x16x32_bf16 v[4:7], v[200:203], v[128:131], v[4:7]
ds_read_b64_tr_b16 v[204:205], v241 offset:50816
ds_read_b64_tr_b16 v[206:207], v241 offset:51840
v_mfma_f32_16x16x32_bf16 v[8:11], v[208:211], v[128:131], v[8:11]
s_add_u32 m0, s83, 36992
v_accvgpr_write_b32 a26, v250
v_mfma_f32_16x16x32_bf16 v[8:11], v[212:215], a[104:107], v[8:11]
buffer_load_dwordx4 v235, s[44:47], s80 offen lds
v_mfma_f32_16x16x32_bf16 v[40:43], v[212:215], a[108:111], v[40:43]
s_add_u32 s80, s80, 524288
v_accvgpr_write_b32 a27, v251
v_mfma_f32_16x16x32_bf16 v[40:43], v[208:211], v[140:143], v[40:43]
ds_read_b64_tr_b16 v[200:201], v241 offset:59264
ds_read_b64_tr_b16 v[202:203], v241 offset:60288
v_mfma_f32_16x16x32_bf16 v[72:75], v[208:211], a[96:99], v[72:75]
s_cmp_ge_u32 s2, 64
s_cselect_b32 s4, 1985225488, s4
v_mfma_f32_16x16x32_bf16 v[72:75], v[212:215], a[112:115], v[72:75]
buffer_load_dwordx4 v[172:175], v247, s[52:55], s91 offen offset:2048 sc1
v_mfma_f32_16x16x32_bf16 v[104:107], v[212:215], a[116:119], v[104:107]
s_cmp_ge_u32 s2, 128
s_cselect_b32 s4, 1985229328, s4
v_mfma_f32_16x16x32_bf16 v[104:107], v[208:211], a[100:103], v[104:107]
ds_read_b64_tr_b16 v[212:213], v242 offset:50816
ds_read_b64_tr_b16 v[214:215], v242 offset:51840
v_mfma_f32_16x16x32_bf16 v[108:111], v[216:219], a[100:103], v[108:111]
s_cmp_ge_u32 s2, 192
s_cselect_b32 s4, 1985225008, s4
v_mfma_f32_16x16x32_bf16 v[108:111], v[220:223], a[116:119], v[108:111]
v_bfe_u32 v236, v255, 4, 3
v_lshlrev_b32 v236, 2, v236
v_mfma_f32_16x16x32_bf16 v[76:79], v[220:223], a[112:115], v[76:79]
v_lshrrev_b32 v236, v236, s4
v_and_b32 v236, 15, v236
v_mfma_f32_16x16x32_bf16 v[76:79], v[216:219], a[96:99], v[76:79]
ds_read_b64_tr_b16 v[208:209], v242 offset:59264
ds_read_b64_tr_b16 v[210:211], v242 offset:60288
v_mfma_f32_16x16x32_bf16 v[44:47], v[216:219], v[140:143], v[44:47]
v_xor_b32 v236, s72, v236
v_lshlrev_b32 v236, 3, v236
v_mfma_f32_16x16x32_bf16 v[44:47], v[220:223], a[108:111], v[44:47]
buffer_store_dwordx4 a[8:11], v255, s[48:51], s18 offen offset:1024 sc1
v_mfma_f32_16x16x32_bf16 v[12:15], v[220:223], a[104:107], v[12:15]
s_waitcnt lgkmcnt(4)
v_accvgpr_write_b32 a225, v236
v_mfma_f32_16x16x32_bf16 v[12:15], v[216:219], v[128:131], v[12:15]
ds_read_b64_tr_b16 v[220:221], v243 offset:50816
ds_read_b64_tr_b16 v[222:223], v243 offset:51840
v_mfma_f32_16x16x32_bf16 v[16:19], v[196:199], v[128:131], v[16:19]
v_accvgpr_read_b32 v224, a240
v_accvgpr_read_b32 v225, a241
v_mfma_f32_16x16x32_bf16 v[16:19], v[192:195], a[104:107], v[16:19]
buffer_load_dwordx4 v[176:179], v244, s[52:55], s91 offen offset:3072 sc1
v_mfma_f32_16x16x32_bf16 v[48:51], v[192:195], a[108:111], v[48:51]
v_accvgpr_read_b32 v226, a242
v_accvgpr_read_b32 v227, a243
v_mfma_f32_16x16x32_bf16 v[48:51], v[196:199], v[140:143], v[48:51]
ds_read_b64_tr_b16 v[216:217], v243 offset:59264
ds_read_b64_tr_b16 v[218:219], v243 offset:60288
v_mfma_f32_16x16x32_bf16 v[80:83], v[196:199], a[96:99], v[80:83]
v_accvgpr_read_b32 v228, a244
v_accvgpr_read_b32 v229, a245
v_mfma_f32_16x16x32_bf16 v[80:83], v[192:195], a[112:115], v[80:83]
buffer_load_dwordx4 v[180:183], v245, s[52:55], s91 offen offset:3072 sc1
v_mfma_f32_16x16x32_bf16 v[112:115], v[192:195], a[116:119], v[112:115]
v_accvgpr_read_b32 v230, a246
v_accvgpr_read_b32 v231, a247
v_mfma_f32_16x16x32_bf16 v[112:115], v[196:199], a[100:103], v[112:115]
v_cvt_pk_bf16_f32 v248, v224, v225
v_cvt_pk_bf16_f32 v249, v226, v227
v_mfma_f32_16x16x32_bf16 v[116:119], v[204:207], a[100:103], v[116:119]
s_waitcnt lgkmcnt(0)
s_barrier
v_mfma_f32_16x16x32_bf16 v[116:119], v[200:203], a[116:119], v[116:119]
buffer_load_dwordx4 v[184:187], v246, s[52:55], s91 offen offset:3072 sc1
v_mfma_f32_16x16x32_bf16 v[84:87], v[200:203], a[112:115], v[84:87]
ds_read_b64_tr_b16 v[192:193], v240 offset:0
ds_read_b64_tr_b16 v[194:195], v240 offset:1024
v_mfma_f32_16x16x32_bf16 v[84:87], v[204:207], a[96:99], v[84:87]
ds_read_b64_tr_b16 v[196:197], v240 offset:8448
ds_read_b64_tr_b16 v[198:199], v240 offset:9472
v_mfma_f32_16x16x32_bf16 v[52:55], v[204:207], v[140:143], v[52:55]
s_add_u32 m0, s83, 50688
v_cvt_pk_bf16_f32 v250, v228, v229
v_mfma_f32_16x16x32_bf16 v[52:55], v[200:203], a[108:111], v[52:55]
buffer_load_dwordx4 v232, s[44:47], s80 offen lds
.La5_LW1_13:
s_cbranch_execz .La5_LW1_R0
v_mfma_f32_16x16x32_bf16 v[20:23], v[200:203], a[104:107], v[20:23]
v_cvt_pk_bf16_f32 v251, v230, v231
v_accvgpr_write_b32 a28, v248
v_mfma_f32_16x16x32_bf16 v[20:23], v[204:207], v[128:131], v[20:23]
ds_read_b64_tr_b16 v[200:201], v241 offset:0
ds_read_b64_tr_b16 v[202:203], v241 offset:1024
v_mfma_f32_16x16x32_bf16 v[24:27], v[212:215], v[128:131], v[24:27]
v_accvgpr_write_b32 a29, v249
v_accvgpr_write_b32 a30, v250
v_mfma_f32_16x16x32_bf16 v[24:27], v[208:211], a[104:107], v[24:27]
buffer_load_dwordx4 v[188:191], v247, s[52:55], s91 offen offset:3072 sc1
v_mfma_f32_16x16x32_bf16 v[56:59], v[208:211], a[108:111], v[56:59]
s_add_u32 s91, s91, 4096
v_accvgpr_write_b32 a31, v251
v_mfma_f32_16x16x32_bf16 v[56:59], v[212:215], v[140:143], v[56:59]
ds_read_b64_tr_b16 v[204:205], v241 offset:8448
ds_read_b64_tr_b16 v[206:207], v241 offset:9472
v_mfma_f32_16x16x32_bf16 v[88:91], v[212:215], a[96:99], v[88:91]
s_add_u32 m0, s83, 51712
s_lshl3_add_u32 s5, s76, s101
v_mfma_f32_16x16x32_bf16 v[88:91], v[208:211], a[112:115], v[88:91]
buffer_load_dwordx4 v233, s[44:47], s80 offen lds
v_mfma_f32_16x16x32_bf16 v[120:123], v[208:211], a[116:119], v[120:123]
s_lshl2_add_u32 s5, s5, s3
s_lshl_b32 s5, s5, 8
v_mfma_f32_16x16x32_bf16 v[120:123], v[212:215], a[100:103], v[120:123]
ds_read_b64_tr_b16 v[208:209], v242 offset:0
ds_read_b64_tr_b16 v[210:211], v242 offset:1024
v_mfma_f32_16x16x32_bf16 v[124:127], v[220:223], a[100:103], v[124:127]
s_add_u32 s5, s5, 1048576
v_mfma_f32_16x16x32_bf16 v[124:127], v[216:219], a[116:119], v[124:127]
v_mfma_f32_16x16x32_bf16 v[92:95], v[216:219], a[112:115], v[92:95]
v_mfma_f32_16x16x32_bf16 v[92:95], v[220:223], a[96:99], v[92:95]
ds_read_b64_tr_b16 v[212:213], v242 offset:8448
ds_read_b64_tr_b16 v[214:215], v242 offset:9472
v_mfma_f32_16x16x32_bf16 v[60:63], v[220:223], v[140:143], v[60:63]
v_mfma_f32_16x16x32_bf16 v[60:63], v[216:219], a[108:111], v[60:63]
v_mfma_f32_16x16x32_bf16 v[28:31], v[216:219], a[104:107], v[28:31]
s_waitcnt vmcnt(15) lgkmcnt(4)
v_mfma_f32_16x16x32_bf16 v[28:31], v[220:223], v[128:131], v[28:31]
ds_read_b64_tr_b16 v[216:217], v243 offset:0
ds_read_b64_tr_b16 v[218:219], v243 offset:1024
v_mfma_f32_16x16x32_bf16 v[0:3], v[192:195], v[132:135], v[0:3]
v_accvgpr_read_b32 v224, a32
v_accvgpr_read_b32 v225, a33
v_mfma_f32_16x16x32_bf16 v[0:3], v[196:199], v[152:155], v[0:3]
buffer_load_dwordx4 v[128:131], v244, s[52:55], s91 offen offset:0 sc1
v_mfma_f32_16x16x32_bf16 v[32:35], v[196:199], v[156:159], v[32:35]
v_accvgpr_read_b32 v226, a34
v_accvgpr_read_b32 v227, a35
v_mfma_f32_16x16x32_bf16 v[32:35], v[192:195], v[136:139], v[32:35]
ds_read_b64_tr_b16 v[220:221], v243 offset:8448
ds_read_b64_tr_b16 v[222:223], v243 offset:9472
v_mfma_f32_16x16x32_bf16 v[64:67], v[192:195], v[144:147], v[64:67]
v_accvgpr_read_b32 v228, a36
v_accvgpr_read_b32 v229, a37
v_mfma_f32_16x16x32_bf16 v[64:67], v[196:199], a[120:123], v[64:67]
buffer_load_dwordx4 v[140:143], v245, s[52:55], s91 offen offset:0 sc1
v_mfma_f32_16x16x32_bf16 v[96:99], v[196:199], a[124:127], v[96:99]
v_accvgpr_read_b32 v230, a38
v_accvgpr_read_b32 v231, a39
v_mfma_f32_16x16x32_bf16 v[96:99], v[192:195], v[148:151], v[96:99]
ds_read_b64_tr_b16 v[196:197], v240 offset:128
ds_read_b64_tr_b16 v[198:199], v240 offset:1152
v_mfma_f32_16x16x32_bf16 v[100:103], v[200:203], v[148:151], v[100:103]
s_add_u32 m0, s83, 52864
v_cvt_pk_bf16_f32 v248, v224, v225
v_mfma_f32_16x16x32_bf16 v[100:103], v[204:207], a[124:127], v[100:103]
buffer_load_dwordx4 v234, s[44:47], s80 offen lds
v_mfma_f32_16x16x32_bf16 v[68:71], v[204:207], a[120:123], v[68:71]
v_cvt_pk_bf16_f32 v249, v226, v227
v_cvt_pk_bf16_f32 v250, v228, v229
v_mfma_f32_16x16x32_bf16 v[68:71], v[200:203], v[144:147], v[68:71]
ds_read_b64_tr_b16 v[192:193], v240 offset:8576
ds_read_b64_tr_b16 v[194:195], v240 offset:9600
v_mfma_f32_16x16x32_bf16 v[36:39], v[200:203], v[136:139], v[36:39]
v_cvt_pk_bf16_f32 v251, v230, v231
v_accvgpr_write_b32 a32, v248
v_mfma_f32_16x16x32_bf16 v[36:39], v[204:207], v[156:159], v[36:39]
buffer_load_dwordx4 a[192:195], v246, s[52:55], s91 offen offset:0 sc1
v_mfma_f32_16x16x32_bf16 v[4:7], v[204:207], v[152:155], v[4:7]
s_waitcnt lgkmcnt(4)
v_accvgpr_write_b32 a33, v249
v_mfma_f32_16x16x32_bf16 v[4:7], v[200:203], v[132:135], v[4:7]
ds_read_b64_tr_b16 v[204:205], v241 offset:128
ds_read_b64_tr_b16 v[206:207], v241 offset:1152
v_mfma_f32_16x16x32_bf16 v[8:11], v[208:211], v[132:135], v[8:11]
s_add_u32 m0, s83, 53888
v_accvgpr_write_b32 a34, v250
v_mfma_f32_16x16x32_bf16 v[8:11], v[212:215], v[152:155], v[8:11]
buffer_load_dwordx4 v235, s[44:47], s80 offen lds
v_mfma_f32_16x16x32_bf16 v[40:43], v[212:215], v[156:159], v[40:43]
v_accvgpr_write_b32 a35, v251
v_mfma_f32_16x16x32_bf16 v[40:43], v[208:211], v[136:139], v[40:43]
ds_read_b64_tr_b16 v[200:201], v241 offset:8576
ds_read_b64_tr_b16 v[202:203], v241 offset:9600
v_mfma_f32_16x16x32_bf16 v[72:75], v[208:211], v[144:147], v[72:75]
v_mfma_f32_16x16x32_bf16 v[72:75], v[212:215], a[120:123], v[72:75]
buffer_load_dwordx4 a[196:199], v247, s[52:55], s91 offen offset:0 sc1
v_mfma_f32_16x16x32_bf16 v[104:107], v[212:215], a[124:127], v[104:107]
v_mfma_f32_16x16x32_bf16 v[104:107], v[208:211], v[148:151], v[104:107]
ds_read_b64_tr_b16 v[212:213], v242 offset:128
ds_read_b64_tr_b16 v[214:215], v242 offset:1152
v_mfma_f32_16x16x32_bf16 v[108:111], v[216:219], v[148:151], v[108:111]
v_mfma_f32_16x16x32_bf16 v[108:111], v[220:223], a[124:127], v[108:111]
v_mfma_f32_16x16x32_bf16 v[76:79], v[220:223], a[120:123], v[76:79]
v_mfma_f32_16x16x32_bf16 v[76:79], v[216:219], v[144:147], v[76:79]
ds_read_b64_tr_b16 v[208:209], v242 offset:8576
ds_read_b64_tr_b16 v[210:211], v242 offset:9600
v_mfma_f32_16x16x32_bf16 v[44:47], v[216:219], v[136:139], v[44:47]
v_mfma_f32_16x16x32_bf16 v[44:47], v[220:223], v[156:159], v[44:47]
buffer_store_dwordx4 a[12:15], v255, s[48:51], s18 offen offset:2048 sc1
v_mfma_f32_16x16x32_bf16 v[12:15], v[220:223], v[152:155], v[12:15]
s_waitcnt lgkmcnt(4)
v_mfma_f32_16x16x32_bf16 v[12:15], v[216:219], v[132:135], v[12:15]
ds_read_b64_tr_b16 v[220:221], v243 offset:128
ds_read_b64_tr_b16 v[222:223], v243 offset:1152
v_mfma_f32_16x16x32_bf16 v[16:19], v[196:199], v[132:135], v[16:19]
v_accvgpr_read_b32 v224, a248
v_accvgpr_read_b32 v225, a249
v_mfma_f32_16x16x32_bf16 v[16:19], v[192:195], v[152:155], v[16:19]
buffer_load_dwordx4 a[200:203], v244, s[52:55], s91 offen offset:1024 sc1
v_mfma_f32_16x16x32_bf16 v[48:51], v[192:195], v[156:159], v[48:51]
v_accvgpr_read_b32 v226, a250
v_accvgpr_read_b32 v227, a251
v_mfma_f32_16x16x32_bf16 v[48:51], v[196:199], v[136:139], v[48:51]
ds_read_b64_tr_b16 v[216:217], v243 offset:8576
ds_read_b64_tr_b16 v[218:219], v243 offset:9600
v_mfma_f32_16x16x32_bf16 v[80:83], v[196:199], v[144:147], v[80:83]
v_accvgpr_read_b32 v228, a252
v_accvgpr_read_b32 v229, a253
v_mfma_f32_16x16x32_bf16 v[80:83], v[192:195], a[120:123], v[80:83]
buffer_load_dwordx4 a[204:207], v245, s[52:55], s91 offen offset:1024 sc1
v_mfma_f32_16x16x32_bf16 v[112:115], v[192:195], a[124:127], v[112:115]
v_accvgpr_read_b32 v230, a254
v_accvgpr_read_b32 v231, a255
v_mfma_f32_16x16x32_bf16 v[112:115], v[196:199], v[148:151], v[112:115]
v_cvt_pk_bf16_f32 v248, v224, v225
v_cvt_pk_bf16_f32 v249, v226, v227
v_mfma_f32_16x16x32_bf16 v[116:119], v[204:207], v[148:151], v[116:119]
s_waitcnt lgkmcnt(0)
s_barrier
v_mfma_f32_16x16x32_bf16 v[116:119], v[200:203], a[124:127], v[116:119]
v_cvt_pk_bf16_f32 v250, v228, v229
v_cvt_pk_bf16_f32 v251, v230, v231
v_mfma_f32_16x16x32_bf16 v[84:87], v[200:203], a[120:123], v[84:87]
ds_read_b64_tr_b16 v[192:193], v240 offset:16896
ds_read_b64_tr_b16 v[194:195], v240 offset:17920
v_mfma_f32_16x16x32_bf16 v[84:87], v[204:207], v[144:147], v[84:87]
ds_read_b64_tr_b16 v[196:197], v240 offset:25344
ds_read_b64_tr_b16 v[198:199], v240 offset:26368
v_mfma_f32_16x16x32_bf16 v[52:55], v[204:207], v[136:139], v[52:55]
v_accvgpr_write_b32 a36, v248
v_accvgpr_write_b32 a37, v249
v_mfma_f32_16x16x32_bf16 v[52:55], v[200:203], v[156:159], v[52:55]
v_accvgpr_write_b32 a38, v250
v_accvgpr_write_b32 a39, v251
v_mfma_f32_16x16x32_bf16 v[20:23], v[200:203], v[152:155], v[20:23]
s_getpc_b64 s[92:93]
.La5_LCT2:
s_sub_u32 s92, s92, .La5_LCT2-.La5_LSTART
s_subb_u32 s93, s93, 0
v_mfma_f32_16x16x32_bf16 v[20:23], v[204:207], v[132:135], v[20:23]
ds_read_b64_tr_b16 v[200:201], v241 offset:16896
ds_read_b64_tr_b16 v[202:203], v241 offset:17920
v_mfma_f32_16x16x32_bf16 v[24:27], v[212:215], v[132:135], v[24:27]
s_and_b32 s93, s93, 0xffff
s_lshr_b32 s94, s2, 3
v_mfma_f32_16x16x32_bf16 v[24:27], v[208:211], v[152:155], v[24:27]
s_lshl2_add_u32 s94, s94, s3
s_lshl_b32 s94, s94, 10
v_mfma_f32_16x16x32_bf16 v[56:59], v[208:211], v[156:159], v[56:59]
v_lshrrev_b32 v233, 7, v255
v_lshlrev_b32 v232, 3, v255
v_mfma_f32_16x16x32_bf16 v[56:59], v[212:215], v[136:139], v[56:59]
ds_read_b64_tr_b16 v[204:205], v241 offset:25344
ds_read_b64_tr_b16 v[206:207], v241 offset:26368
v_mfma_f32_16x16x32_bf16 v[88:91], v[212:215], v[144:147], v[88:91]
v_lshl_add_u32 v232, v233, 28, v232
v_add_u32 v232, s94, v232
v_mfma_f32_16x16x32_bf16 v[88:91], v[208:211], a[120:123], v[88:91]
s_mov_b32 s94, .La5_LCODEEND-.La5_LSTART
s_mov_b32 s95, 0x20000
v_mfma_f32_16x16x32_bf16 v[120:123], v[208:211], a[124:127], v[120:123]
buffer_load_dword a253, v232, s[92:95], 0 offen
v_mfma_f32_16x16x32_bf16 v[120:123], v[212:215], v[148:151], v[120:123]
ds_read_b64_tr_b16 v[208:209], v242 offset:16896
ds_read_b64_tr_b16 v[210:211], v242 offset:17920
v_mfma_f32_16x16x32_bf16 v[124:127], v[220:223], v[148:151], v[124:127]
s_mov_b32 s80, 8192
s_add_u32 s83, s83, 4294886912
v_mfma_f32_16x16x32_bf16 v[124:127], v[216:219], a[124:127], v[124:127]
buffer_load_dwordx4 v[148:151], v246, s[52:55], s91 offen offset:1024 sc1
v_mfma_f32_16x16x32_bf16 v[92:95], v[216:219], a[120:123], v[92:95]
s_lshl_b32 s92, s76, 21
s_add_u32 s92, s92, 54525952
v_mfma_f32_16x16x32_bf16 v[92:95], v[220:223], v[144:147], v[92:95]
ds_read_b64_tr_b16 v[212:213], v242 offset:25344
ds_read_b64_tr_b16 v[214:215], v242 offset:26368
v_mfma_f32_16x16x32_bf16 v[60:63], v[220:223], v[136:139], v[60:63]
ds_write_b128 v238, a[12:15] offset:6144
v_mfma_f32_16x16x32_bf16 v[60:63], v[216:219], v[156:159], v[60:63]
buffer_store_dwordx4 a[16:19], v255, s[48:51], s18 offen offset:3072 sc1
v_mfma_f32_16x16x32_bf16 v[28:31], v[216:219], v[152:155], v[28:31]
s_waitcnt vmcnt(13) lgkmcnt(5)
v_mfma_f32_16x16x32_bf16 v[28:31], v[220:223], v[132:135], v[28:31]
ds_read_b64_tr_b16 v[216:217], v243 offset:16896
ds_read_b64_tr_b16 v[218:219], v243 offset:17920
v_mfma_f32_16x16x32_bf16 v[0:3], v[192:195], v[160:163], v[0:3]
v_accvgpr_read_b32 v224, a40
v_accvgpr_read_b32 v225, a41
v_mfma_f32_16x16x32_bf16 v[0:3], v[196:199], v[176:179], v[0:3]
buffer_load_dwordx4 v[132:135], v247, s[52:55], s91 offen offset:1024 sc1
v_mfma_f32_16x16x32_bf16 v[32:35], v[196:199], v[180:183], v[32:35]
v_accvgpr_read_b32 v226, a42
v_accvgpr_read_b32 v227, a43
v_mfma_f32_16x16x32_bf16 v[32:35], v[192:195], v[164:167], v[32:35]
ds_read_b64_tr_b16 v[220:221], v243 offset:25344
ds_read_b64_tr_b16 v[222:223], v243 offset:26368
v_mfma_f32_16x16x32_bf16 v[64:67], v[192:195], v[168:171], v[64:67]
v_accvgpr_read_b32 v228, a44
v_accvgpr_read_b32 v229, a45
v_mfma_f32_16x16x32_bf16 v[64:67], v[196:199], v[184:187], v[64:67]
buffer_load_dwordx4 a[96:99], v244, s[52:55], s91 offen offset:2048 sc1
v_mfma_f32_16x16x32_bf16 v[96:99], v[196:199], v[188:191], v[96:99]
v_accvgpr_read_b32 v230, a46
v_accvgpr_read_b32 v231, a47
v_mfma_f32_16x16x32_bf16 v[96:99], v[192:195], v[172:175], v[96:99]
ds_read_b64_tr_b16 v[196:197], v240 offset:17024
ds_read_b64_tr_b16 v[198:199], v240 offset:18048
v_mfma_f32_16x16x32_bf16 v[100:103], v[200:203], v[172:175], v[100:103]
v_cvt_pk_bf16_f32 v248, v224, v225
v_cvt_pk_bf16_f32 v249, v226, v227
v_mfma_f32_16x16x32_bf16 v[100:103], v[204:207], v[188:191], v[100:103]
buffer_load_dwordx4 a[100:103], v245, s[52:55], s91 offen offset:2048 sc1
v_mfma_f32_16x16x32_bf16 v[68:71], v[204:207], v[184:187], v[68:71]
v_cvt_pk_bf16_f32 v250, v228, v229
v_cvt_pk_bf16_f32 v251, v230, v231
v_mfma_f32_16x16x32_bf16 v[68:71], v[200:203], v[168:171], v[68:71]
ds_read_b64_tr_b16 v[192:193], v240 offset:25472
ds_read_b64_tr_b16 v[194:195], v240 offset:26496
v_mfma_f32_16x16x32_bf16 v[36:39], v[200:203], v[164:167], v[36:39]
v_accvgpr_write_b32 a40, v248
v_accvgpr_write_b32 a41, v249
v_mfma_f32_16x16x32_bf16 v[36:39], v[204:207], v[180:183], v[36:39]
buffer_load_dwordx4 a[104:107], v246, s[52:55], s91 offen offset:2048 sc1
v_mfma_f32_16x16x32_bf16 v[4:7], v[204:207], v[176:179], v[4:7]
s_waitcnt lgkmcnt(4)
v_accvgpr_write_b32 a42, v250
v_mfma_f32_16x16x32_bf16 v[4:7], v[200:203], v[160:163], v[4:7]
ds_read_b64_tr_b16 v[204:205], v241 offset:17024
ds_read_b64_tr_b16 v[206:207], v241 offset:18048
v_mfma_f32_16x16x32_bf16 v[8:11], v[208:211], v[160:163], v[8:11]
v_accvgpr_write_b32 a43, v251
v_mfma_f32_16x16x32_bf16 v[8:11], v[212:215], v[176:179], v[8:11]
buffer_load_dwordx4 a[108:111], v247, s[52:55], s91 offen offset:2048 sc1
v_mfma_f32_16x16x32_bf16 v[40:43], v[212:215], v[180:183], v[40:43]
s_add_u32 s44, s26, s92
s_addc_u32 s45, s27, 0
v_mfma_f32_16x16x32_bf16 v[40:43], v[208:211], v[164:167], v[40:43]
ds_read_b64_tr_b16 v[200:201], v241 offset:25472
ds_read_b64_tr_b16 v[202:203], v241 offset:26496
v_mfma_f32_16x16x32_bf16 v[72:75], v[208:211], v[168:171], v[72:75]
s_and_b32 s45, s45, 0xffff
s_mov_b32 s46, 2097536
v_mfma_f32_16x16x32_bf16 v[72:75], v[212:215], v[184:187], v[72:75]
s_mov_b32 s95, 4096
s_mov_b32 s96, 8192
v_mfma_f32_16x16x32_bf16 v[104:107], v[212:215], v[188:191], v[104:107]
s_mov_b32 s18, 0
s_mov_b32 s97, 12288
v_mfma_f32_16x16x32_bf16 v[104:107], v[208:211], v[172:175], v[104:107]
ds_read_b64_tr_b16 v[212:213], v242 offset:17024
ds_read_b64_tr_b16 v[214:215], v242 offset:18048
v_mfma_f32_16x16x32_bf16 v[108:111], v[216:219], v[172:175], v[108:111]
s_mul_i32 s92, s3, 196608
s_sub_u32 s93, 1376256, s92
v_mfma_f32_16x16x32_bf16 v[108:111], v[220:223], v[188:191], v[108:111]
buffer_store_dwordx4 a[20:23], v255, s[48:51], s18 offen offset:0 sc1
v_mfma_f32_16x16x32_bf16 v[76:79], v[220:223], v[184:187], v[76:79]
v_mfma_f32_16x16x32_bf16 v[76:79], v[216:219], v[168:171], v[76:79]
ds_read_b64_tr_b16 v[208:209], v242 offset:25472
ds_read_b64_tr_b16 v[210:211], v242 offset:26496
v_mfma_f32_16x16x32_bf16 v[44:47], v[216:219], v[164:167], v[44:47]
ds_write_b128 v238, a[16:19] offset:7168
v_mfma_f32_16x16x32_bf16 v[44:47], v[220:223], v[180:183], v[44:47]
buffer_store_dwordx4 a[24:27], v255, s[48:51], s18 offen offset:1024 sc1
v_mfma_f32_16x16x32_bf16 v[12:15], v[220:223], v[176:179], v[12:15]
s_waitcnt lgkmcnt(5)
v_mfma_f32_16x16x32_bf16 v[12:15], v[216:219], v[160:163], v[12:15]
ds_read_b64_tr_b16 v[220:221], v243 offset:17024
ds_read_b64_tr_b16 v[222:223], v243 offset:18048
v_mfma_f32_16x16x32_bf16 v[16:19], v[196:199], v[160:163], v[16:19]
v_accvgpr_read_b32 v224, a48
v_accvgpr_read_b32 v225, a49
v_mfma_f32_16x16x32_bf16 v[16:19], v[192:195], v[176:179], v[16:19]
buffer_load_dwordx4 a[112:115], v244, s[52:55], s91 offen offset:3072 sc1
v_mfma_f32_16x16x32_bf16 v[48:51], v[192:195], v[180:183], v[48:51]
v_accvgpr_read_b32 v226, a50
v_accvgpr_read_b32 v227, a51
v_mfma_f32_16x16x32_bf16 v[48:51], v[196:199], v[164:167], v[48:51]
ds_read_b64_tr_b16 v[216:217], v243 offset:25472
ds_read_b64_tr_b16 v[218:219], v243 offset:26496
v_mfma_f32_16x16x32_bf16 v[80:83], v[196:199], v[168:171], v[80:83]
v_accvgpr_read_b32 v228, a52
v_accvgpr_read_b32 v229, a53
v_mfma_f32_16x16x32_bf16 v[80:83], v[192:195], v[184:187], v[80:83]
buffer_load_dwordx4 a[116:119], v245, s[52:55], s91 offen offset:3072 sc1
v_mfma_f32_16x16x32_bf16 v[112:115], v[192:195], v[188:191], v[112:115]
v_accvgpr_read_b32 v230, a54
v_accvgpr_read_b32 v231, a55
v_mfma_f32_16x16x32_bf16 v[112:115], v[196:199], v[172:175], v[112:115]
v_cvt_pk_bf16_f32 v248, v224, v225
v_cvt_pk_bf16_f32 v249, v226, v227
v_mfma_f32_16x16x32_bf16 v[116:119], v[204:207], v[172:175], v[116:119]
s_waitcnt lgkmcnt(0)
s_barrier
v_mfma_f32_16x16x32_bf16 v[116:119], v[200:203], v[188:191], v[116:119]
buffer_load_dwordx4 a[120:123], v246, s[52:55], s91 offen offset:3072 sc1
v_mfma_f32_16x16x32_bf16 v[84:87], v[200:203], v[184:187], v[84:87]
ds_read_b64_tr_b16 v[192:193], v240 offset:33792
ds_read_b64_tr_b16 v[194:195], v240 offset:34816
v_mfma_f32_16x16x32_bf16 v[84:87], v[204:207], v[168:171], v[84:87]
ds_read_b64_tr_b16 v[196:197], v240 offset:42240
ds_read_b64_tr_b16 v[198:199], v240 offset:43264
v_mfma_f32_16x16x32_bf16 v[52:55], v[204:207], v[164:167], v[52:55]
ds_write_b128 v238, a[20:23] offset:32768
v_mfma_f32_16x16x32_bf16 v[52:55], v[200:203], v[180:183], v[52:55]
buffer_load_dwordx4 a[124:127], v247, s[52:55], s91 offen offset:3072 sc1
v_mfma_f32_16x16x32_bf16 v[20:23], v[200:203], v[176:179], v[20:23]
v_cvt_pk_bf16_f32 v250, v228, v229
v_cvt_pk_bf16_f32 v251, v230, v231
v_mfma_f32_16x16x32_bf16 v[20:23], v[204:207], v[160:163], v[20:23]
ds_read_b64_tr_b16 v[200:201], v241 offset:33792
ds_read_b64_tr_b16 v[202:203], v241 offset:34816
v_mfma_f32_16x16x32_bf16 v[24:27], v[212:215], v[160:163], v[24:27]
v_accvgpr_write_b32 a44, v248
v_accvgpr_write_b32 a45, v249
v_mfma_f32_16x16x32_bf16 v[24:27], v[208:211], v[176:179], v[24:27]
v_accvgpr_write_b32 a46, v250
v_accvgpr_write_b32 a47, v251
v_mfma_f32_16x16x32_bf16 v[56:59], v[208:211], v[180:183], v[56:59]
v_mfma_f32_16x16x32_bf16 v[56:59], v[212:215], v[164:167], v[56:59]
ds_read_b64_tr_b16 v[204:205], v241 offset:42240
ds_read_b64_tr_b16 v[206:207], v241 offset:43264
v_mfma_f32_16x16x32_bf16 v[88:91], v[212:215], v[168:171], v[88:91]
v_mfma_f32_16x16x32_bf16 v[88:91], v[208:211], v[184:187], v[88:91]
v_mfma_f32_16x16x32_bf16 v[120:123], v[208:211], v[188:191], v[120:123]
v_mfma_f32_16x16x32_bf16 v[120:123], v[212:215], v[172:175], v[120:123]
ds_read_b64_tr_b16 v[208:209], v242 offset:33792
ds_read_b64_tr_b16 v[210:211], v242 offset:34816
v_mfma_f32_16x16x32_bf16 v[124:127], v[220:223], v[172:175], v[124:127]
v_mfma_f32_16x16x32_bf16 v[124:127], v[216:219], v[188:191], v[124:127]
buffer_store_dwordx4 a[28:31], v255, s[48:51], s18 offen offset:2048 sc1
v_mfma_f32_16x16x32_bf16 v[92:95], v[216:219], v[184:187], v[92:95]
v_mfma_f32_16x16x32_bf16 v[92:95], v[220:223], v[168:171], v[92:95]
ds_read_b64_tr_b16 v[212:213], v242 offset:42240
ds_read_b64_tr_b16 v[214:215], v242 offset:43264
v_mfma_f32_16x16x32_bf16 v[60:63], v[220:223], v[164:167], v[60:63]
ds_write_b128 v238, a[24:27] offset:33792
v_mfma_f32_16x16x32_bf16 v[60:63], v[216:219], v[180:183], v[60:63]
buffer_store_dwordx4 a[32:35], v255, s[48:51], s18 offen offset:3072 sc1
v_mfma_f32_16x16x32_bf16 v[28:31], v[216:219], v[176:179], v[28:31]
s_waitcnt vmcnt(12) lgkmcnt(5)
v_mfma_f32_16x16x32_bf16 v[28:31], v[220:223], v[160:163], v[28:31]
ds_read_b64_tr_b16 v[216:217], v243 offset:33792
ds_read_b64_tr_b16 v[218:219], v243 offset:34816
v_mfma_f32_16x16x32_bf16 v[0:3], v[192:195], v[128:131], v[0:3]
v_accvgpr_read_b32 v224, a56
v_accvgpr_read_b32 v225, a57
v_mfma_f32_16x16x32_bf16 v[0:3], v[196:199], a[200:203], v[0:3]
v_accvgpr_read_b32 v226, a58
v_accvgpr_read_b32 v227, a59
v_mfma_f32_16x16x32_bf16 v[32:35], v[196:199], a[204:207], v[32:35]
v_accvgpr_read_b32 v228, a60
v_accvgpr_read_b32 v229, a61
v_mfma_f32_16x16x32_bf16 v[32:35], v[192:195], v[140:143], v[32:35]
ds_read_b64_tr_b16 v[220:221], v243 offset:42240
ds_read_b64_tr_b16 v[222:223], v243 offset:43264
v_mfma_f32_16x16x32_bf16 v[64:67], v[192:195], a[192:195], v[64:67]
v_accvgpr_read_b32 v230, a62
v_accvgpr_read_b32 v231, a63
v_mfma_f32_16x16x32_bf16 v[64:67], v[196:199], v[148:151], v[64:67]
v_cvt_pk_bf16_f32 v248, v224, v225
v_cvt_pk_bf16_f32 v249, v226, v227
v_mfma_f32_16x16x32_bf16 v[96:99], v[196:199], v[132:135], v[96:99]
v_cvt_pk_bf16_f32 v250, v228, v229
v_cvt_pk_bf16_f32 v251, v230, v231
v_mfma_f32_16x16x32_bf16 v[96:99], v[192:195], a[196:199], v[96:99]
ds_read_b64_tr_b16 v[196:197], v240 offset:33920
ds_read_b64_tr_b16 v[198:199], v240 offset:34944
v_mfma_f32_16x16x32_bf16 v[100:103], v[200:203], a[196:199], v[100:103]
v_accvgpr_write_b32 a48, v248
v_accvgpr_write_b32 a49, v249
v_mfma_f32_16x16x32_bf16 v[100:103], v[204:207], v[132:135], v[100:103]
v_accvgpr_write_b32 a50, v250
v_accvgpr_write_b32 a51, v251
v_mfma_f32_16x16x32_bf16 v[68:71], v[204:207], v[148:151], v[68:71]
v_add_u32 v244, s92, v244
v_add_u32 v246, s93, v246
v_mfma_f32_16x16x32_bf16 v[68:71], v[200:203], a[192:195], v[68:71]
ds_read_b64_tr_b16 v[192:193], v240 offset:42368
ds_read_b64_tr_b16 v[194:195], v240 offset:43392
v_mfma_f32_16x16x32_bf16 v[36:39], v[200:203], v[140:143], v[36:39]
ds_write_b128 v238, a[28:31] offset:34816
v_mfma_f32_16x16x32_bf16 v[36:39], v[204:207], a[204:207], v[36:39]
v_add_u32 v245, 0x1000, v244
v_add_u32 v247, 0x1000, v246
v_mfma_f32_16x16x32_bf16 v[4:7], v[204:207], a[200:203], v[4:7]
s_waitcnt lgkmcnt(5)
s_lshl_b32 s91, s72, 13
v_mfma_f32_16x16x32_bf16 v[4:7], v[200:203], v[128:131], v[4:7]
ds_read_b64_tr_b16 v[204:205], v241 offset:33920
ds_read_b64_tr_b16 v[206:207], v241 offset:34944
v_mfma_f32_16x16x32_bf16 v[8:11], v[208:211], v[128:131], v[8:11]
s_lshl4_add_u32 s92, s76, s3
s_lshl3_add_u32 s92, s92, s20
v_mfma_f32_16x16x32_bf16 v[8:11], v[212:215], a[200:203], v[8:11]
s_bitset1_b32 s20, 30
v_mfma_f32_16x16x32_bf16 v[40:43], v[212:215], a[204:207], v[40:43]
v_mfma_f32_16x16x32_bf16 v[40:43], v[208:211], v[140:143], v[40:43]
ds_read_b64_tr_b16 v[200:201], v241 offset:42368
ds_read_b64_tr_b16 v[202:203], v241 offset:43392
v_mfma_f32_16x16x32_bf16 v[72:75], v[208:211], a[192:195], v[72:75]
v_mfma_f32_16x16x32_bf16 v[72:75], v[212:215], v[148:151], v[72:75]
v_mfma_f32_16x16x32_bf16 v[104:107], v[212:215], v[132:135], v[104:107]
s_mov_b32 s18, 4096
v_mfma_f32_16x16x32_bf16 v[104:107], v[208:211], a[196:199], v[104:107]
ds_read_b64_tr_b16 v[212:213], v242 offset:33920
ds_read_b64_tr_b16 v[214:215], v242 offset:34944
v_mfma_f32_16x16x32_bf16 v[108:111], v[216:219], a[196:199], v[108:111]
v_mfma_f32_16x16x32_bf16 v[108:111], v[220:223], v[132:135], v[108:111]
buffer_store_dwordx4 a[36:39], v255, s[48:51], s18 offen offset:0 sc1
v_mfma_f32_16x16x32_bf16 v[76:79], v[220:223], v[148:151], v[76:79]
v_mfma_f32_16x16x32_bf16 v[76:79], v[216:219], a[192:195], v[76:79]
ds_read_b64_tr_b16 v[208:209], v242 offset:42368
ds_read_b64_tr_b16 v[210:211], v242 offset:43392
v_mfma_f32_16x16x32_bf16 v[44:47], v[216:219], v[140:143], v[44:47]
ds_write_b128 v238, a[32:35] offset:35840
v_mfma_f32_16x16x32_bf16 v[44:47], v[220:223], a[204:207], v[44:47]
buffer_store_dwordx4 a[40:43], v255, s[48:51], s18 offen offset:1024 sc1
v_mfma_f32_16x16x32_bf16 v[12:15], v[220:223], a[200:203], v[12:15]
s_waitcnt lgkmcnt(5)
v_mfma_f32_16x16x32_bf16 v[12:15], v[216:219], v[128:131], v[12:15]
ds_read_b64_tr_b16 v[220:221], v243 offset:33920
ds_read_b64_tr_b16 v[222:223], v243 offset:34944
v_mfma_f32_16x16x32_bf16 v[16:19], v[196:199], v[128:131], v[16:19]
s_mov_b64 s[52:53], s[86:87]
v_mfma_f32_16x16x32_bf16 v[16:19], v[192:195], a[200:203], v[16:19]
s_bitcmp1_b32 21, s73
s_cselect_b32 s54, 16384, 0
s_cselect_b32 s86, s88, 1073741824
v_mfma_f32_16x16x32_bf16 v[48:51], v[192:195], a[204:207], v[48:51]
s_lshl_b32 s89, s3, 14
v_add_u32 v248, s89, v255
v_mfma_f32_16x16x32_bf16 v[48:51], v[196:199], v[140:143], v[48:51]
ds_read_b64_tr_b16 v[216:217], v243 offset:42368
ds_read_b64_tr_b16 v[218:219], v243 offset:43392
v_mfma_f32_16x16x32_bf16 v[80:83], v[196:199], a[192:195], v[80:83]
ds_write_b128 v238, a[36:39] offset:36864
v_mfma_f32_16x16x32_bf16 v[80:83], v[192:195], v[148:151], v[80:83]
s_add_u32 s86, s86, 262144
v_mfma_f32_16x16x32_bf16 v[112:115], v[192:195], v[132:135], v[112:115]
s_cmp_eq_u32 s73, 0
s_cselect_b32 s86, s92, s86
s_cselect_b32 s92, 1073741824, s92
v_mfma_f32_16x16x32_bf16 v[112:115], v[196:199], a[196:199], v[112:115]
s_mov_b32 s87, s92
s_add_u32 s89, s89, 16384
v_mfma_f32_16x16x32_bf16 v[116:119], v[204:207], a[196:199], v[116:119]
s_waitcnt lgkmcnt(1)
s_barrier
v_mfma_f32_16x16x32_bf16 v[116:119], v[200:203], v[132:135], v[116:119]
s_lshl_b32 m0, s3, 12
s_add_u32 m0, m0, 131072
v_mfma_f32_16x16x32_bf16 v[84:87], v[200:203], v[148:151], v[84:87]
ds_read_b64_tr_b16 v[192:193], v240 offset:50688
ds_read_b64_tr_b16 v[194:195], v240 offset:51712
v_mfma_f32_16x16x32_bf16 v[84:87], v[204:207], a[192:195], v[84:87]
ds_read_b64_tr_b16 v[196:197], v240 offset:59136
ds_read_b64_tr_b16 v[198:199], v240 offset:60160
v_mfma_f32_16x16x32_bf16 v[52:55], v[204:207], v[140:143], v[52:55]
ds_write_b128 v238, a[40:43] offset:37888
v_mfma_f32_16x16x32_bf16 v[52:55], v[200:203], a[204:207], v[52:55]
v_bfe_u32 v251, v255, 2, 5
v_and_b32 v224, 0x380, v255
v_mfma_f32_16x16x32_bf16 v[20:23], v[200:203], a[200:203], v[20:23]
v_add_u32 v251, v251, v224
v_and_b32 v224, 16, v255
v_mfma_f32_16x16x32_bf16 v[20:23], v[204:207], v[128:131], v[20:23]
ds_read_b64_tr_b16 v[200:201], v241 offset:50688
ds_read_b64_tr_b16 v[202:203], v241 offset:51712
v_mfma_f32_16x16x32_bf16 v[24:27], v[212:215], v[128:131], v[24:27]
v_cmp_eq_u32 vcc, 0, v224
v_mfma_f32_16x16x32_bf16 v[24:27], v[208:211], a[200:203], v[24:27]
v_cndmask_b32 v249, v253, v252, vcc
v_mfma_f32_16x16x32_bf16 v[56:59], v[208:211], a[204:207], v[56:59]
v_mfma_f32_16x16x32_bf16 v[56:59], v[212:215], v[140:143], v[56:59]
ds_read_b64_tr_b16 v[204:205], v241 offset:59136
ds_read_b64_tr_b16 v[206:207], v241 offset:60160
v_mfma_f32_16x16x32_bf16 v[88:91], v[212:215], a[192:195], v[88:91]
ds_write_b128 v238, a[44:47] offset:38912
v_mfma_f32_16x16x32_bf16 v[88:91], v[208:211], v[148:151], v[88:91]
buffer_store_dwordx4 a[44:47], v255, s[48:51], s18 offen offset:2048 sc1
v_mfma_f32_16x16x32_bf16 v[120:123], v[208:211], v[132:135], v[120:123]
v_mfma_f32_16x16x32_bf16 v[120:123], v[212:215], a[196:199], v[120:123]
ds_read_b64_tr_b16 v[208:209], v242 offset:50688
ds_read_b64_tr_b16 v[210:211], v242 offset:51712
v_mfma_f32_16x16x32_bf16 v[124:127], v[220:223], a[196:199], v[124:127]
v_mfma_f32_16x16x32_bf16 v[124:127], v[216:219], v[132:135], v[124:127]
v_mfma_f32_16x16x32_bf16 v[92:95], v[216:219], v[148:151], v[92:95]
v_mfma_f32_16x16x32_bf16 v[92:95], v[220:223], a[192:195], v[92:95]
ds_read_b64_tr_b16 v[212:213], v242 offset:59136
ds_read_b64_tr_b16 v[214:215], v242 offset:60160
v_mfma_f32_16x16x32_bf16 v[60:63], v[220:223], v[140:143], v[60:63]
v_mfma_f32_16x16x32_bf16 v[60:63], v[216:219], a[204:207], v[60:63]
buffer_store_dwordx4 a[48:51], v255, s[48:51], s18 offen offset:3072 sc1
v_mfma_f32_16x16x32_bf16 v[28:31], v[216:219], a[200:203], v[28:31]
s_waitcnt vmcnt(6) lgkmcnt(5)
v_mfma_f32_16x16x32_bf16 v[28:31], v[220:223], v[128:131], v[28:31]
ds_read_b64_tr_b16 v[216:217], v243 offset:50688
ds_read_b64_tr_b16 v[218:219], v243 offset:51712
v_mfma_f32_16x16x32_bf16 v[0:3], v[192:195], a[96:99], v[0:3]
v_mfma_f32_16x16x32_bf16 v[0:3], v[196:199], a[112:115], v[0:3]
v_mfma_f32_16x16x32_bf16 v[32:35], v[196:199], a[116:119], v[32:35]
v_mfma_f32_16x16x32_bf16 v[32:35], v[192:195], a[100:103], v[32:35]
ds_read_b64_tr_b16 v[220:221], v243 offset:59136
ds_read_b64_tr_b16 v[222:223], v243 offset:60160
v_mfma_f32_16x16x32_bf16 v[64:67], v[192:195], a[104:107], v[64:67]
v_mfma_f32_16x16x32_bf16 v[64:67], v[196:199], a[120:123], v[64:67]
v_mfma_f32_16x16x32_bf16 v[96:99], v[196:199], a[124:127], v[96:99]
v_mfma_f32_16x16x32_bf16 v[96:99], v[192:195], a[108:111], v[96:99]
ds_read_b64_tr_b16 v[196:197], v240 offset:50816
ds_read_b64_tr_b16 v[198:199], v240 offset:51840
v_mfma_f32_16x16x32_bf16 v[100:103], v[200:203], a[108:111], v[100:103]
v_mfma_f32_16x16x32_bf16 v[100:103], v[204:207], a[124:127], v[100:103]
v_mfma_f32_16x16x32_bf16 v[68:71], v[204:207], a[120:123], v[68:71]
v_mfma_f32_16x16x32_bf16 v[68:71], v[200:203], a[104:107], v[68:71]
.La5_LW1_14:
s_cbranch_execz .La5_LW1_R0
ds_read_b64_tr_b16 v[192:193], v240 offset:59264
ds_read_b64_tr_b16 v[194:195], v240 offset:60288
v_mfma_f32_16x16x32_bf16 v[36:39], v[200:203], a[100:103], v[36:39]
v_mfma_f32_16x16x32_bf16 v[36:39], v[204:207], a[116:119], v[36:39]
v_mfma_f32_16x16x32_bf16 v[4:7], v[204:207], a[112:115], v[4:7]
s_waitcnt lgkmcnt(4)
v_mfma_f32_16x16x32_bf16 v[4:7], v[200:203], a[96:99], v[4:7]
ds_read_b64_tr_b16 v[204:205], v241 offset:50816
ds_read_b64_tr_b16 v[206:207], v241 offset:51840
v_mfma_f32_16x16x32_bf16 v[8:11], v[208:211], a[96:99], v[8:11]
v_mfma_f32_16x16x32_bf16 v[8:11], v[212:215], a[112:115], v[8:11]
v_mfma_f32_16x16x32_bf16 v[40:43], v[212:215], a[116:119], v[40:43]
v_cvt_pk_bf16_f32 v128, v0, v1
v_cvt_pk_bf16_f32 v129, v2, v3
v_mfma_f32_16x16x32_bf16 v[40:43], v[208:211], a[100:103], v[40:43]
ds_read_b64_tr_b16 v[200:201], v241 offset:59264
ds_read_b64_tr_b16 v[202:203], v241 offset:60288
v_mfma_f32_16x16x32_bf16 v[72:75], v[208:211], a[104:107], v[72:75]
v_cvt_pk_bf16_f32 v130, v4, v5
v_cvt_pk_bf16_f32 v131, v6, v7
v_mfma_f32_16x16x32_bf16 v[72:75], v[212:215], a[120:123], v[72:75]
v_cvt_pk_bf16_f32 v144, v32, v33
v_cvt_pk_bf16_f32 v145, v34, v35
v_mfma_f32_16x16x32_bf16 v[104:107], v[212:215], a[124:127], v[104:107]
v_cvt_pk_bf16_f32 v146, v36, v37
v_cvt_pk_bf16_f32 v147, v38, v39
v_mfma_f32_16x16x32_bf16 v[104:107], v[208:211], a[108:111], v[104:107]
ds_read_b64_tr_b16 v[212:213], v242 offset:50816
ds_read_b64_tr_b16 v[214:215], v242 offset:51840
v_mfma_f32_16x16x32_bf16 v[108:111], v[216:219], a[108:111], v[108:111]
v_cvt_pk_bf16_f32 v160, v64, v65
v_cvt_pk_bf16_f32 v161, v66, v67
v_mfma_f32_16x16x32_bf16 v[108:111], v[220:223], a[124:127], v[108:111]
v_cvt_pk_bf16_f32 v162, v68, v69
v_cvt_pk_bf16_f32 v163, v70, v71
v_mfma_f32_16x16x32_bf16 v[76:79], v[220:223], a[120:123], v[76:79]
v_cvt_pk_bf16_f32 v176, v96, v97
v_cvt_pk_bf16_f32 v177, v98, v99
v_mfma_f32_16x16x32_bf16 v[76:79], v[216:219], a[104:107], v[76:79]
ds_read_b64_tr_b16 v[208:209], v242 offset:59264
ds_read_b64_tr_b16 v[210:211], v242 offset:60288
v_mfma_f32_16x16x32_bf16 v[44:47], v[216:219], a[100:103], v[44:47]
v_cvt_pk_bf16_f32 v178, v100, v101
v_cvt_pk_bf16_f32 v179, v102, v103
v_mfma_f32_16x16x32_bf16 v[44:47], v[220:223], a[116:119], v[44:47]
v_mfma_f32_16x16x32_bf16 v[12:15], v[220:223], a[112:115], v[12:15]
s_waitcnt lgkmcnt(4)
v_mfma_f32_16x16x32_bf16 v[12:15], v[216:219], a[96:99], v[12:15]
ds_read_b64_tr_b16 v[220:221], v243 offset:50816
ds_read_b64_tr_b16 v[222:223], v243 offset:51840
v_mfma_f32_16x16x32_bf16 v[16:19], v[196:199], a[96:99], v[16:19]
v_mul_u32_u24 v241, 0x5555, v224
v_lshrrev_b32 v241, 4, v241
v_add_u32 v241, 0x8888, v241
v_mfma_f32_16x16x32_bf16 v[16:19], v[192:195], a[112:115], v[16:19]
buffer_store_dwordx4 v[128:131], v255, s[52:55], 0 offen offset:0 sc1
v_mfma_f32_16x16x32_bf16 v[48:51], v[192:195], a[116:119], v[48:51]
v_cvt_pk_bf16_f32 v132, v8, v9
v_cvt_pk_bf16_f32 v133, v10, v11
v_cvt_pk_bf16_f32 v181, v106, v107
v_mfma_f32_16x16x32_bf16 v[48:51], v[196:199], a[100:103], v[48:51]
ds_read_b64_tr_b16 v[216:217], v243 offset:59264
ds_read_b64_tr_b16 v[218:219], v243 offset:60288
v_cvt_pk_bf16_f32 v182, v108, v109
v_mfma_f32_16x16x32_bf16 v[80:83], v[196:199], a[104:107], v[80:83]
v_cvt_pk_bf16_f32 v134, v12, v13
v_cvt_pk_bf16_f32 v135, v14, v15
v_cvt_pk_bf16_f32 v183, v110, v111
v_mfma_f32_16x16x32_bf16 v[80:83], v[192:195], a[120:123], v[80:83]
buffer_store_dwordx4 v[144:147], v255, s[52:55], s95 offen offset:0 sc1
v_accvgpr_read_b32 v34, a238
v_mfma_f32_16x16x32_bf16 v[112:115], v[192:195], a[124:127], v[112:115]
v_cvt_pk_bf16_f32 v148, v40, v41
v_cvt_pk_bf16_f32 v149, v42, v43
v_accvgpr_read_b32 v35, a239
v_mfma_f32_16x16x32_bf16 v[112:115], v[196:199], a[108:111], v[112:115]
v_cvt_pk_bf16_f32 v150, v44, v45
v_cvt_pk_bf16_f32 v151, v46, v47
v_mov_b32 v36, 0xff800000
v_mfma_f32_16x16x32_bf16 v[116:119], v[204:207], a[108:111], v[116:119]
s_waitcnt lgkmcnt(0)
s_barrier
v_mov_b32 v37, 0xff800000
v_mfma_f32_16x16x32_bf16 v[116:119], v[200:203], a[124:127], v[116:119]
buffer_store_dwordx4 v[160:163], v255, s[52:55], s96 offen offset:0 sc1
v_mov_b32 v38, 0xff800000
v_mfma_f32_16x16x32_bf16 v[84:87], v[200:203], a[120:123], v[84:87]
v_cvt_pk_bf16_f32 v164, v72, v73
v_cvt_pk_bf16_f32 v165, v74, v75
v_mov_b32 v39, 0xff800000
v_mfma_f32_16x16x32_bf16 v[84:87], v[204:207], a[104:107], v[84:87]
ds_read_b128 a[64:67], v248 offset:0
ds_read_b128 a[68:71], v248 offset:1024
v_mfma_f32_16x16x32_bf16 v[52:55], v[204:207], a[100:103], v[52:55]
v_cvt_pk_bf16_f32 v166, v76, v77
v_cvt_pk_bf16_f32 v167, v78, v79
v_mfma_f32_16x16x32_bf16 v[52:55], v[200:203], a[116:119], v[52:55]
buffer_store_dwordx4 v[176:179], v255, s[52:55], s97 offen offset:0 sc1
v_mfma_f32_16x16x32_bf16 v[20:23], v[200:203], a[112:115], v[20:23]
s_nop 0
v_cvt_pk_bf16_f32 v180, v104, v105
v_mfma_f32_16x16x32_bf16 v[20:23], v[204:207], a[96:99], v[20:23]
ds_read_b128 a[72:75], v248 offset:2048
ds_read_b128 a[76:79], v248 offset:3072
v_mfma_f32_16x16x32_bf16 v[24:27], v[212:215], a[96:99], v[24:27]
v_accvgpr_read_b32 v32, a236
v_accvgpr_read_b32 v33, a237
v_mfma_f32_16x16x32_bf16 v[24:27], v[208:211], a[112:115], v[24:27]
buffer_store_dwordx4 v[132:135], v255, s[52:55], 0 offen offset:1024 sc1
v_mfma_f32_16x16x32_bf16 v[56:59], v[208:211], a[116:119], v[56:59]
v_cvt_pk_bf16_f32 v136, v16, v17
v_cvt_pk_bf16_f32 v137, v18, v19
v_mfma_f32_16x16x32_bf16 v[56:59], v[212:215], a[100:103], v[56:59]
ds_read_b128 a[80:83], v248 offset:4096
ds_read_b128 a[84:87], v248 offset:5120
v_mfma_f32_16x16x32_bf16 v[88:91], v[212:215], a[104:107], v[88:91]
v_cvt_pk_bf16_f32 v138, v20, v21
v_cvt_pk_bf16_f32 v139, v22, v23
v_mfma_f32_16x16x32_bf16 v[88:91], v[208:211], a[120:123], v[88:91]
buffer_store_dwordx4 v[148:151], v255, s[52:55], s95 offen offset:1024 sc1
v_mfma_f32_16x16x32_bf16 v[120:123], v[208:211], a[124:127], v[120:123]
v_cvt_pk_bf16_f32 v152, v48, v49
v_cvt_pk_bf16_f32 v153, v50, v51
v_mfma_f32_16x16x32_bf16 v[120:123], v[212:215], a[108:111], v[120:123]
ds_read_b128 a[88:91], v248 offset:6144
ds_read_b128 a[92:95], v248 offset:7168
v_mfma_f32_16x16x32_bf16 v[124:127], v[220:223], a[108:111], v[124:127]
ds_write_b128 v238, a[48:51] offset:39936
v_mfma_f32_16x16x32_bf16 v[124:127], v[216:219], a[124:127], v[124:127]
buffer_store_dwordx4 v[164:167], v255, s[52:55], s96 offen offset:1024 sc1
v_mfma_f32_16x16x32_bf16 v[92:95], v[216:219], a[120:123], v[92:95]
v_cvt_pk_bf16_f32 v154, v52, v53
v_cvt_pk_bf16_f32 v155, v54, v55
v_mfma_f32_16x16x32_bf16 v[92:95], v[220:223], a[104:107], v[92:95]
v_cvt_pk_bf16_f32 v168, v80, v81
v_cvt_pk_bf16_f32 v169, v82, v83
v_mfma_f32_16x16x32_bf16 v[60:63], v[220:223], a[100:103], v[60:63]
v_cvt_pk_bf16_f32 v170, v84, v85
v_cvt_pk_bf16_f32 v171, v86, v87
v_mfma_f32_16x16x32_bf16 v[60:63], v[216:219], a[116:119], v[60:63]
buffer_store_dwordx4 v[180:183], v255, s[52:55], s97 offen offset:1024 sc1
v_mfma_f32_16x16x32_bf16 v[28:31], v[216:219], a[112:115], v[28:31]
v_cvt_pk_bf16_f32 v184, v112, v113
v_cvt_pk_bf16_f32 v185, v114, v115
v_mfma_f32_16x16x32_bf16 v[28:31], v[220:223], a[96:99], v[28:31]
v_cvt_pk_bf16_f32 v186, v116, v117
v_cvt_pk_bf16_f32 v187, v118, v119
s_waitcnt lgkmcnt(1)
v_mfma_f32_16x16x32_bf16 v[208:211], a[64:67], v[160:163], v[32:35]
v_mfma_f32_16x16x32_bf16 v[216:219], a[64:67], v[176:179], 0
buffer_store_dwordx4 v[136:139], v255, s[52:55], 0 offen offset:2048 sc1
v_mfma_f32_16x16x32_bf16 v[212:215], a[80:83], v[160:163], v[36:39]
v_cvt_pk_bf16_f32 v172, v88, v89
v_cvt_pk_bf16_f32 v173, v90, v91
v_cvt_pk_bf16_f32 v174, v92, v93
v_mfma_f32_16x16x32_bf16 v[220:223], a[80:83], v[176:179], v[32:35]
v_cvt_pk_bf16_f32 v175, v94, v95
v_cvt_pk_bf16_f32 v188, v120, v121
v_cvt_pk_bf16_f32 v189, v122, v123
v_mfma_f32_16x16x32_bf16 v[208:211], a[68:71], v[164:167], v[208:211]
v_cvt_pk_bf16_f32 v190, v124, v125
v_cvt_pk_bf16_f32 v191, v126, v127
v_cvt_pk_bf16_f32 v140, v24, v25
v_mfma_f32_16x16x32_bf16 v[216:219], a[68:71], v[180:183], v[216:219]
buffer_store_dwordx4 v[152:155], v255, s[52:55], s95 offen offset:2048 sc1
v_cvt_pk_bf16_f32 v141, v26, v27
v_mfma_f32_16x16x32_bf16 v[212:215], a[84:87], v[164:167], v[212:215]
v_cvt_pk_bf16_f32 v142, v28, v29
v_cvt_pk_bf16_f32 v143, v30, v31
v_cvt_pk_bf16_f32 v156, v56, v57
v_mfma_f32_16x16x32_bf16 v[220:223], a[84:87], v[180:183], v[220:223]
v_cvt_pk_bf16_f32 v157, v58, v59
v_cvt_pk_bf16_f32 v158, v60, v61
v_cvt_pk_bf16_f32 v159, v62, v63
v_mfma_f32_16x16x32_bf16 v[208:211], a[72:75], v[168:171], v[208:211]
v_mfma_f32_16x16x32_bf16 v[216:219], a[72:75], v[184:187], v[216:219]
buffer_store_dwordx4 v[168:171], v255, s[52:55], s96 offen offset:2048 sc1
v_mfma_f32_16x16x32_bf16 v[212:215], a[88:91], v[168:171], v[212:215]
v_mfma_f32_16x16x32_bf16 v[220:223], a[88:91], v[184:187], v[220:223]
v_mfma_f32_16x16x32_bf16 v[208:211], a[76:79], v[172:175], v[208:211]
v_mfma_f32_16x16x32_bf16 v[216:219], a[76:79], v[188:191], v[216:219]
buffer_store_dwordx4 v[184:187], v255, s[52:55], s97 offen offset:2048 sc1
v_mfma_f32_16x16x32_bf16 v[212:215], a[92:95], v[172:175], v[212:215]
ds_read_b128 a[64:67], v248 offset:16384
v_mfma_f32_16x16x32_bf16 v[220:223], a[92:95], v[188:191], v[220:223]
ds_read_b128 a[96:99], v248 offset:8192
v_exp_f32 v0, v208
s_waitcnt vmcnt(12)
buffer_store_dwordx2 v[252:253], off, s[64:67], s87 sc1
v_mfma_f32_16x16x32_bf16 v[64:67], a[232:235], a[232:235], 0
ds_read_b128 a[68:71], v248 offset:17408
v_exp_f32 v1, v209
v_mfma_f32_16x16x32_bf16 v[68:71], a[232:235], a[232:235], 0
ds_read_b128 a[100:103], v248 offset:9216
buffer_store_dwordx4 v[140:143], v255, s[52:55], 0 offen offset:3072 sc1
v_mfma_f32_16x16x32_bf16 v[72:75], a[232:235], a[232:235], 0
ds_read_b128 a[72:75], v248 offset:18432
v_exp_f32 v2, v210
v_mfma_f32_16x16x32_bf16 v[76:79], a[232:235], a[232:235], 0
ds_read_b128 a[104:107], v248 offset:10240
v_exp_f32 v3, v211
v_mfma_f32_16x16x32_bf16 v[80:83], a[232:235], a[232:235], 0
ds_read_b128 a[76:79], v248 offset:19456
v_exp_f32 v8, v216
v_mfma_f32_16x16x32_bf16 v[84:87], a[232:235], a[232:235], 0
ds_read_b128 a[108:111], v248 offset:11264
buffer_store_dwordx4 v[156:159], v255, s[52:55], s95 offen offset:3072 sc1
v_mfma_f32_16x16x32_bf16 v[88:91], a[232:235], a[232:235], 0
ds_read_b128 a[80:83], v248 offset:20480
v_exp_f32 v9, v217
v_mfma_f32_16x16x32_bf16 v[92:95], a[232:235], a[232:235], 0
ds_read_b128 a[112:115], v248 offset:12288
v_exp_f32 v10, v218
v_mfma_f32_16x16x32_bf16 v[96:99], a[232:235], a[232:235], 0
ds_read_b128 a[84:87], v248 offset:21504
v_exp_f32 v11, v219
v_mfma_f32_16x16x32_bf16 v[100:103], a[232:235], a[232:235], 0
ds_read_b128 a[116:119], v248 offset:13312
buffer_store_dwordx4 v[172:175], v255, s[52:55], s96 offen offset:3072 sc1
v_mfma_f32_16x16x32_bf16 v[104:107], a[232:235], a[232:235], 0
ds_read_b128 a[88:91], v248 offset:22528
v_exp_f32 v4, v212
v_mfma_f32_16x16x32_bf16 v[108:111], a[232:235], a[232:235], 0
ds_read_b128 a[120:123], v248 offset:14336
v_exp_f32 v5, v213
v_mfma_f32_16x16x32_bf16 v[112:115], a[232:235], a[232:235], 0
ds_read_b128 a[92:95], v248 offset:23552
v_exp_f32 v6, v214
v_mfma_f32_16x16x32_bf16 v[116:119], a[232:235], a[232:235], 0
ds_read_b128 a[124:127], v248 offset:15360
buffer_store_dwordx4 v[188:191], v255, s[52:55], s97 offen offset:3072 sc1
v_exp_f32_e64 v7, v215
v_mfma_f32_16x16x32_bf16 v[120:123], a[232:235], a[232:235], 0
v_exp_f32_e64 v12, v220
v_mfma_f32_16x16x32_bf16 v[124:127], a[232:235], a[232:235], 0
v_accvgpr_write_b32 a192, 0
v_accvgpr_write_b32 a193, 0
v_accvgpr_write_b32 a194, 0
v_accvgpr_write_b32 a195, 0
v_accvgpr_write_b32 a196, 0
v_accvgpr_write_b32 a197, 0
v_accvgpr_write_b32 a198, 0
v_accvgpr_write_b32 a199, 0
v_add_u32 v248, 0x4000, v248
s_cmp_eq_u32 s3, 3
s_cbranch_scc1 .La5_LA_FH
.p2align 6
; ---- attention, 32 keys per window: S = K Q^T (causal mask through SrcC), P = exp2(S), O += V^T P, row sums l += 1 P; here the later 32 rows only
.La5_LA_HH:
s_waitcnt lgkmcnt(1)
v_mfma_f32_16x16x32_bf16 v[208:211], a[64:67], v[160:163], 0
v_exp_f32 v13, v221
v_mfma_f32_16x16x32_bf16 v[208:211], a[68:71], v[164:167], v[208:211]
v_cvt_pk_bf16_f32 v232, v0, v1
v_cvt_pk_bf16_f32 v233, v2, v3
v_mfma_f32_16x16x32_bf16 v[208:211], a[72:75], v[168:171], v[208:211]
v_exp_f32 v14, v222
v_mfma_f32_16x16x32_bf16 v[208:211], a[76:79], v[172:175], v[208:211]
v_cvt_pk_bf16_f32 v236, v8, v9
v_cvt_pk_bf16_f32 v237, v10, v11
v_mfma_f32_16x16x32_bf16 v[216:219], a[76:79], v[188:191], 0
v_exp_f32 v15, v223
v_mfma_f32_16x16x32_bf16 v[216:219], a[72:75], v[184:187], v[216:219]
v_cvt_pk_bf16_f32 v234, v4, v5
v_cvt_pk_bf16_f32 v235, v6, v7
v_mfma_f32_16x16x32_bf16 v[216:219], a[68:71], v[180:183], v[216:219]
v_exp_f32 v0, v208
v_mfma_f32_16x16x32_bf16 v[216:219], a[64:67], v[176:179], v[216:219]
v_cvt_pk_bf16_f32 v238, v12, v13
v_cvt_pk_bf16_f32 v239, v14, v15
v_mfma_f32_16x16x32_bf16 v[212:215], a[80:83], v[160:163], 0
v_exp_f32 v1, v209
v_mfma_f32_16x16x32_bf16 v[212:215], a[84:87], v[164:167], v[212:215]
ds_read_b128 a[64:67], v248 offset:16384
ds_read_b128 a[68:71], v248 offset:17408
v_mfma_f32_16x16x32_bf16 v[212:215], a[88:91], v[168:171], v[212:215]
v_exp_f32 v2, v210
v_mfma_f32_16x16x32_bf16 v[212:215], a[92:95], v[172:175], v[212:215]
ds_read_b128 a[72:75], v248 offset:18432
ds_read_b128 a[76:79], v248 offset:19456
v_mfma_f32_16x16x32_bf16 v[220:223], a[92:95], v[188:191], 0
v_exp_f32 v3, v211
v_mfma_f32_16x16x32_bf16 v[220:223], a[88:91], v[184:187], v[220:223]
ds_read_b128 a[92:95], v248 offset:23552
v_mfma_f32_16x16x32_bf16 v[220:223], a[84:87], v[180:183], v[220:223]
v_exp_f32 v8, v216
v_mfma_f32_16x16x32_bf16 v[220:223], a[80:83], v[176:179], v[220:223]
s_waitcnt lgkmcnt(5)
ds_read_b128 a[88:91], v248 offset:22528
v_mfma_f32_16x16x32_bf16 v[64:67], a[96:99], v[232:235], v[64:67]
v_exp_f32 v9, v217
v_mfma_f32_16x16x32_bf16 v[96:99], a[96:99], v[236:239], v[96:99]
ds_read_b128 a[96:99], v248 offset:8192
ds_read_b128 a[84:87], v248 offset:21504
v_mfma_f32_16x16x32_bf16 v[100:103], a[100:103], v[236:239], v[100:103]
v_exp_f32 v10, v218
v_mfma_f32_16x16x32_bf16 v[68:71], a[100:103], v[232:235], v[68:71]
ds_read_b128 a[100:103], v248 offset:9216
ds_read_b128 a[80:83], v248 offset:20480
v_mfma_f32_16x16x32_bf16 v[72:75], a[104:107], v[232:235], v[72:75]
v_exp_f32 v11, v219
v_mfma_f32_16x16x32_bf16 v[104:107], a[104:107], v[236:239], v[104:107]
ds_read_b128 a[104:107], v248 offset:10240
v_mfma_f32_16x16x32_bf16 v[108:111], a[108:111], v[236:239], v[108:111]
v_exp_f32 v4, v212
v_mfma_f32_16x16x32_bf16 v[76:79], a[108:111], v[232:235], v[76:79]
ds_read_b128 a[108:111], v248 offset:11264
s_add_u32 s85, s85, 1
v_mfma_f32_16x16x32_bf16 v[80:83], a[112:115], v[232:235], v[80:83]
v_exp_f32 v5, v213
v_mfma_f32_16x16x32_bf16 v[112:115], a[112:115], v[236:239], v[112:115]
ds_read_b128 a[112:115], v248 offset:12288
s_add_u32 s89, s89, 32768
v_mfma_f32_16x16x32_bf16 v[116:119], a[116:119], v[236:239], v[116:119]
v_exp_f32 v6, v214
v_mfma_f32_16x16x32_bf16 v[84:87], a[116:119], v[232:235], v[84:87]
ds_read_b128 a[116:119], v248 offset:13312
s_add_u32 s92, s85, s3
v_mfma_f32_16x16x32_bf16 v[88:91], a[120:123], v[232:235], v[88:91]
v_exp_f32 v7, v215
v_mfma_f32_16x16x32_bf16 v[120:123], a[120:123], v[236:239], v[120:123]
ds_read_b128 a[120:123], v248 offset:14336
buffer_load_dword v250, v251, s[64:67], s20 offen sc1
v_mfma_f32_16x16x32_bf16 v[124:127], a[124:127], v[236:239], v[124:127]
v_exp_f32 v12, v220
v_mfma_f32_16x16x32_bf16 v[92:95], a[124:127], v[232:235], v[92:95]
ds_read_b128 a[124:127], v248 offset:15360
s_barrier
v_smfmac_f32_16x16x64_bf16 a[196:199], a[208:211], v[232:239], v241
s_waitcnt lgkmcnt(6)
v_mfma_f32_16x16x32_bf16 v[208:211], a[64:67], v[160:163], 0
v_exp_f32 v13, v221
v_mfma_f32_16x16x32_bf16 v[208:211], a[68:71], v[164:167], v[208:211]
v_cvt_pk_bf16_f32 v232, v0, v1
v_cvt_pk_bf16_f32 v233, v2, v3
v_mfma_f32_16x16x32_bf16 v[208:211], a[72:75], v[168:171], v[208:211]
v_exp_f32 v14, v222
v_mfma_f32_16x16x32_bf16 v[208:211], a[76:79], v[172:175], v[208:211]
v_cvt_pk_bf16_f32 v236, v8, v9
v_cvt_pk_bf16_f32 v237, v10, v11
v_mfma_f32_16x16x32_bf16 v[216:219], a[76:79], v[188:191], 0
v_exp_f32 v15, v223
v_mfma_f32_16x16x32_bf16 v[216:219], a[72:75], v[184:187], v[216:219]
v_cvt_pk_bf16_f32 v234, v4, v5
v_cvt_pk_bf16_f32 v235, v6, v7
v_mfma_f32_16x16x32_bf16 v[216:219], a[68:71], v[180:183], v[216:219]
v_exp_f32 v0, v208
v_mfma_f32_16x16x32_bf16 v[216:219], a[64:67], v[176:179], v[216:219]
v_cvt_pk_bf16_f32 v238, v12, v13
v_cvt_pk_bf16_f32 v239, v14, v15
v_mfma_f32_16x16x32_bf16 v[212:215], a[80:83], v[160:163], 0
v_exp_f32 v1, v209
v_mfma_f32_16x16x32_bf16 v[212:215], a[84:87], v[164:167], v[212:215]
ds_read_b128 a[64:67], v248 offset:32768
ds_read_b128 a[68:71], v248 offset:33792
v_mfma_f32_16x16x32_bf16 v[212:215], a[88:91], v[168:171], v[212:215]
v_exp_f32 v2, v210
v_mfma_f32_16x16x32_bf16 v[212:215], a[92:95], v[172:175], v[212:215]
ds_read_b128 a[72:75], v248 offset:34816
ds_read_b128 a[76:79], v248 offset:35840
v_mfma_f32_16x16x32_bf16 v[220:223], a[92:95], v[188:191], 0
v_exp_f32 v3, v211
v_mfma_f32_16x16x32_bf16 v[220:223], a[88:91], v[184:187], v[220:223]
ds_read_b128 a[92:95], v248 offset:39936
s_bitcmp1_b64 s[98:99], s85
v_mfma_f32_16x16x32_bf16 v[220:223], a[84:87], v[180:183], v[220:223]
v_exp_f32 v8, v216
v_mfma_f32_16x16x32_bf16 v[220:223], a[80:83], v[176:179], v[220:223]
s_waitcnt lgkmcnt(5)
ds_read_b128 a[88:91], v248 offset:38912
v_mfma_f32_16x16x32_bf16 v[64:67], a[96:99], v[232:235], v[64:67]
v_exp_f32 v9, v217
v_mfma_f32_16x16x32_bf16 v[96:99], a[96:99], v[236:239], v[96:99]
ds_read_b128 a[96:99], v248 offset:24576
ds_read_b128 a[84:87], v248 offset:37888
v_mfma_f32_16x16x32_bf16 v[100:103], a[100:103], v[236:239], v[100:103]
v_exp_f32 v10, v218
v_mfma_f32_16x16x32_bf16 v[68:71], a[100:103], v[232:235], v[68:71]
ds_read_b128 a[100:103], v248 offset:25600
ds_read_b128 a[80:83], v248 offset:36864
v_mfma_f32_16x16x32_bf16 v[72:75], a[104:107], v[232:235], v[72:75]
v_exp_f32 v11, v219
v_mfma_f32_16x16x32_bf16 v[104:107], a[104:107], v[236:239], v[104:107]
ds_read_b128 a[104:107], v248 offset:26624
v_mfma_f32_16x16x32_bf16 v[108:111], a[108:111], v[236:239], v[108:111]
v_exp_f32 v4, v212
v_mfma_f32_16x16x32_bf16 v[76:79], a[108:111], v[232:235], v[76:79]
ds_read_b128 a[108:111], v248 offset:27648
v_mfma_f32_16x16x32_bf16 v[80:83], a[112:115], v[232:235], v[80:83]
v_exp_f32 v5, v213
v_mfma_f32_16x16x32_bf16 v[112:115], a[112:115], v[236:239], v[112:115]
ds_read_b128 a[112:115], v248 offset:28672
v_mfma_f32_16x16x32_bf16 v[116:119], a[116:119], v[236:239], v[116:119]
v_exp_f32 v6, v214
v_mfma_f32_16x16x32_bf16 v[84:87], a[116:119], v[232:235], v[84:87]
ds_read_b128 a[116:119], v248 offset:29696
s_waitcnt vmcnt(1)
buffer_store_dwordx2 v[252:253], off, s[64:67], s86 sc1
s_mov_b32 s86, 1073741824
s_bitset0_b32 s20, 30
v_mfma_f32_16x16x32_bf16 v[88:91], a[120:123], v[232:235], v[88:91]
v_exp_f32 v7, v215
v_mfma_f32_16x16x32_bf16 v[120:123], a[120:123], v[236:239], v[120:123]
ds_read_b128 a[120:123], v248 offset:30720
v_mfma_f32_16x16x32_bf16 v[124:127], a[124:127], v[236:239], v[124:127]
v_exp_f32_e64 v12, v220
v_mfma_f32_16x16x32_bf16 v[92:95], a[124:127], v[232:235], v[92:95]
ds_read_b128 a[124:127], v248 offset:31744
v_smfmac_f32_16x16x64_bf16 a[196:199], a[208:211], v[232:239], v241
v_add_u32 v248, 0x8000, v248
s_cbranch_scc0 .La5_LA_HH1
s_call_b64 s[24:25], .La5_LA_EVENT
s_add_u32 s92, s85, s3
.La5_LA_HH1:
s_cmp_lt_u32 s92, 3
s_cbranch_scc1 .La5_LA_HH
; ---- first window with both row groups (diagonal of the earlier group)
.La5_LA_FH:
.La5_LW1_15:
s_cbranch_execz .La5_LW1_R0
s_waitcnt lgkmcnt(9)
v_mfma_f32_16x16x32_bf16 v[192:195], a[64:67], v[128:131], v[32:35]
v_exp_f32 v13, v221
v_mfma_f32_16x16x32_bf16 v[192:195], a[68:71], v[132:135], v[192:195]
v_cvt_pk_bf16_f32 v232, v0, v1
v_cvt_pk_bf16_f32 v233, v2, v3
v_mfma_f32_16x16x32_bf16 v[192:195], a[72:75], v[136:139], v[192:195]
v_exp_f32 v14, v222
v_mfma_f32_16x16x32_bf16 v[192:195], a[76:79], v[140:143], v[192:195]
buffer_load_dwordx4 v255, s[60:63], s17 offen offset:0 sc1 lds
v_mfma_f32_16x16x32_bf16 v[200:203], a[76:79], v[156:159], 0
v_exp_f32 v15, v223
v_mfma_f32_16x16x32_bf16 v[200:203], a[72:75], v[152:155], v[200:203]
v_cvt_pk_bf16_f32 v236, v8, v9
v_cvt_pk_bf16_f32 v237, v10, v11
v_mfma_f32_16x16x32_bf16 v[200:203], a[68:71], v[148:151], v[200:203]
v_exp_f32 v192, v192
v_mfma_f32_16x16x32_bf16 v[200:203], a[64:67], v[144:147], v[200:203]
s_waitcnt lgkmcnt(1)
v_cvt_pk_bf16_f32 v234, v4, v5
v_mfma_f32_16x16x32_bf16 v[196:199], a[80:83], v[128:131], v[36:39]
v_exp_f32 v193, v193
v_mfma_f32_16x16x32_bf16 v[196:199], a[84:87], v[132:135], v[196:199]
v_cvt_pk_bf16_f32 v224, v192, v193
s_add_u32 s85, s85, 1
v_mfma_f32_16x16x32_bf16 v[196:199], a[88:91], v[136:139], v[196:199]
v_exp_f32 v194, v194
v_mfma_f32_16x16x32_bf16 v[196:199], a[92:95], v[140:143], v[196:199]
buffer_load_dwordx4 v255, s[60:63], s17 offen offset:1024 sc1 lds
v_mfma_f32_16x16x32_bf16 v[204:207], a[92:95], v[156:159], v[32:35]
v_exp_f32 v195, v195
v_mfma_f32_16x16x32_bf16 v[204:207], a[88:91], v[152:155], v[204:207]
v_cvt_pk_bf16_f32 v225, v194, v195
s_add_u32 s89, s89, 16384
v_mfma_f32_16x16x32_bf16 v[204:207], a[84:87], v[148:151], v[204:207]
v_exp_f32 v200, v200
v_mfma_f32_16x16x32_bf16 v[204:207], a[80:83], v[144:147], v[204:207]
buffer_load_dwordx4 v255, s[60:63], s17 offen offset:2048 sc1 lds
v_mfma_f32_16x16x32_bf16 v[208:211], a[64:67], v[160:163], 0
v_exp_f32 v201, v201
v_mfma_f32_16x16x32_bf16 v[208:211], a[68:71], v[164:167], v[208:211]
v_cvt_pk_bf16_f32 v228, v200, v201
s_and_b32 s89, s89, 0x1ffff
v_mfma_f32_16x16x32_bf16 v[208:211], a[72:75], v[168:171], v[208:211]
v_exp_f32 v202, v202
v_mfma_f32_16x16x32_bf16 v[208:211], a[76:79], v[172:175], v[208:211]
buffer_load_dwordx4 v255, s[60:63], s17 offen offset:3072 sc1 lds
v_mfma_f32_16x16x32_bf16 v[216:219], a[76:79], v[188:191], 0
v_exp_f32 v203, v203
v_mfma_f32_16x16x32_bf16 v[216:219], a[72:75], v[184:187], v[216:219]
v_cvt_pk_bf16_f32 v229, v202, v203
v_mfma_f32_16x16x32_bf16 v[216:219], a[68:71], v[180:183], v[216:219]
v_exp_f32 v196, v196
v_mfma_f32_16x16x32_bf16 v[216:219], a[64:67], v[176:179], v[216:219]
v_cvt_pk_bf16_f32 v235, v6, v7
v_cvt_pk_bf16_f32 v238, v12, v13
v_mfma_f32_16x16x32_bf16 v[212:215], a[80:83], v[160:163], 0
v_exp_f32 v197, v197
s_add_u32 s17, s17, 8192
v_mfma_f32_16x16x32_bf16 v[212:215], a[84:87], v[164:167], v[212:215]
v_cvt_pk_bf16_f32 v226, v196, v197
s_add_u32 m0, m0, 16384
s_and_b32 m0, m0, s78
v_mfma_f32_16x16x32_bf16 v[212:215], a[88:91], v[168:171], v[212:215]
v_exp_f32 v198, v198
v_mfma_f32_16x16x32_bf16 v[212:215], a[92:95], v[172:175], v[212:215]
v_cvt_pk_bf16_f32 v239, v14, v15
s_waitcnt lgkmcnt(0)
v_mfma_f32_16x16x32_bf16 v[220:223], a[92:95], v[188:191], 0
v_exp_f32 v199, v199
v_mfma_f32_16x16x32_bf16 v[220:223], a[88:91], v[184:187], v[220:223]
v_cvt_pk_bf16_f32 v227, v198, v199
v_mfma_f32_16x16x32_bf16 v[220:223], a[84:87], v[180:183], v[220:223]
v_exp_f32 v204, v204
v_mfma_f32_16x16x32_bf16 v[220:223], a[80:83], v[176:179], v[220:223]
s_waitcnt vmcnt(10)
s_barrier
v_mfma_f32_16x16x32_bf16 v[64:67], a[96:99], v[232:235], v[64:67]
v_exp_f32 v205, v205
v_mfma_f32_16x16x32_bf16 v[96:99], a[96:99], v[236:239], v[96:99]
ds_read_b128 a[96:99], v248 offset:8192
v_cvt_pk_bf16_f32 v230, v204, v205
v_mfma_f32_16x16x32_bf16 v[100:103], a[100:103], v[236:239], v[100:103]
v_exp_f32 v206, v206
v_mfma_f32_16x16x32_bf16 v[68:71], a[100:103], v[232:235], v[68:71]
ds_read_b128 a[100:103], v248 offset:9216
buffer_load_dword v250, v251, s[64:67], s20 offen sc1
v_mfma_f32_16x16x32_bf16 v[72:75], a[104:107], v[232:235], v[72:75]
v_exp_f32 v207, v207
v_mfma_f32_16x16x32_bf16 v[104:107], a[104:107], v[236:239], v[104:107]
ds_read_b128 a[104:107], v248 offset:10240
v_cvt_pk_bf16_f32 v231, v206, v207
v_mfma_f32_16x16x32_bf16 v[108:111], a[108:111], v[236:239], v[108:111]
v_exp_f32 v208, v208
v_mfma_f32_16x16x32_bf16 v[76:79], a[108:111], v[232:235], v[76:79]
ds_read_b128 a[108:111], v248 offset:11264
v_mfma_f32_16x16x32_bf16 v[80:83], a[112:115], v[232:235], v[80:83]
v_exp_f32 v209, v209
v_mfma_f32_16x16x32_bf16 v[112:115], a[112:115], v[236:239], v[112:115]
ds_read_b128 a[112:115], v248 offset:12288
v_mfma_f32_16x16x32_bf16 v[116:119], a[116:119], v[236:239], v[116:119]
v_exp_f32 v210, v210
v_mfma_f32_16x16x32_bf16 v[84:87], a[116:119], v[232:235], v[84:87]
ds_read_b128 a[116:119], v248 offset:13312
v_mfma_f32_16x16x32_bf16 v[88:91], a[120:123], v[232:235], v[88:91]
v_exp_f32 v211, v211
v_mfma_f32_16x16x32_bf16 v[120:123], a[120:123], v[236:239], v[120:123]
ds_read_b128 a[120:123], v248 offset:14336
s_bitcmp1_b64 s[98:99], s85
v_mfma_f32_16x16x32_bf16 v[124:127], a[124:127], v[236:239], v[124:127]
v_exp_f32 v216, v216
v_mfma_f32_16x16x32_bf16 v[92:95], a[124:127], v[232:235], v[92:95]
ds_read_b128 a[124:127], v248 offset:15360
v_or_b32 v248, s89, v255
v_smfmac_f32_16x16x64_bf16 a[196:199], a[208:211], v[232:239], v241
s_waitcnt lgkmcnt(4)
v_smfmac_f32_16x16x64_bf16 a[192:195], a[208:211], v[224:231], v241
ds_read_b128 a[64:67], v248 offset:0
v_mfma_f32_16x16x32_bf16 v[0:3], a[96:99], v[224:227], 0
v_exp_f32 v217, v217
v_mfma_f32_16x16x32_bf16 v[32:35], a[96:99], v[228:231], 0
ds_read_b128 a[68:71], v248 offset:1024
v_cvt_pk_bf16_f32 v232, v208, v209
v_mfma_f32_16x16x32_bf16 v[36:39], a[100:103], v[228:231], 0
v_exp_f32 v218, v218
v_mfma_f32_16x16x32_bf16 v[4:7], a[100:103], v[224:227], 0
ds_read_b128 a[72:75], v248 offset:2048
v_cvt_pk_bf16_f32 v233, v210, v211
v_mfma_f32_16x16x32_bf16 v[8:11], a[104:107], v[224:227], 0
v_exp_f32 v219, v219
v_mfma_f32_16x16x32_bf16 v[40:43], a[104:107], v[228:231], 0
ds_read_b128 a[76:79], v248 offset:3072
v_cvt_pk_bf16_f32 v236, v216, v217
v_mfma_f32_16x16x32_bf16 v[44:47], a[108:111], v[228:231], 0
v_exp_f32 v212, v212
v_mfma_f32_16x16x32_bf16 v[12:15], a[108:111], v[224:227], 0
ds_read_b128 a[80:83], v248 offset:4096
s_waitcnt lgkmcnt(5)
v_mfma_f32_16x16x32_bf16 v[16:19], a[112:115], v[224:227], 0
v_exp_f32 v213, v213
v_mfma_f32_16x16x32_bf16 v[48:51], a[112:115], v[228:231], 0
ds_read_b128 a[84:87], v248 offset:5120
v_cvt_pk_bf16_f32 v237, v218, v219
v_mfma_f32_16x16x32_bf16 v[52:55], a[116:119], v[228:231], 0
v_exp_f32 v214, v214
v_mfma_f32_16x16x32_bf16 v[20:23], a[116:119], v[224:227], 0
ds_read_b128 a[88:91], v248 offset:6144
v_cvt_pk_bf16_f32 v234, v212, v213
v_mfma_f32_16x16x32_bf16 v[24:27], a[120:123], v[224:227], 0
v_exp_f32 v215, v215
.La5_LW2_0:
s_cbranch_execz .La5_LW2_R0
v_mfma_f32_16x16x32_bf16 v[56:59], a[120:123], v[228:231], 0
ds_read_b128 a[92:95], v248 offset:7168
v_cvt_pk_bf16_f32 v235, v214, v215
v_mfma_f32_16x16x32_bf16 v[60:63], a[124:127], v[228:231], 0
v_exp_f32 v220, v220
v_mfma_f32_16x16x32_bf16 v[28:31], a[124:127], v[224:227], 0
s_waitcnt vmcnt(5)
buffer_store_dwordx2 v[252:253], off, s[64:67], s86 sc1
s_mov_b32 s86, 1073741824
s_bitset0_b32 s20, 30
s_cbranch_scc1 .La5_LA_FE0
.La5_LA_FS0:
s_waitcnt lgkmcnt(4)
v_mfma_f32_16x16x32_bf16 v[192:195], a[64:67], v[128:131], 0
v_exp_f32 v221, v221
v_mfma_f32_16x16x32_bf16 v[192:195], a[68:71], v[132:135], v[192:195]
v_cvt_pk_bf16_f32 v238, v220, v221
s_add_u32 s85, s85, 1
v_mfma_f32_16x16x32_bf16 v[192:195], a[72:75], v[136:139], v[192:195]
v_exp_f32 v222, v222
v_mfma_f32_16x16x32_bf16 v[192:195], a[76:79], v[140:143], v[192:195]
buffer_load_dwordx4 v255, s[60:63], s17 offen offset:0 sc1 lds
v_mfma_f32_16x16x32_bf16 v[200:203], a[76:79], v[156:159], 0
v_exp_f32 v223, v223
v_mfma_f32_16x16x32_bf16 v[200:203], a[72:75], v[152:155], v[200:203]
v_cvt_pk_bf16_f32 v239, v222, v223
s_add_u32 s89, s89, 16384
v_mfma_f32_16x16x32_bf16 v[200:203], a[68:71], v[148:151], v[200:203]
v_exp_f32 v192, v192
v_mfma_f32_16x16x32_bf16 v[200:203], a[64:67], v[144:147], v[200:203]
s_waitcnt lgkmcnt(0)
s_and_b32 s89, s89, 0x1ffff
v_mfma_f32_16x16x32_bf16 v[196:199], a[80:83], v[128:131], 0
v_exp_f32 v193, v193
v_mfma_f32_16x16x32_bf16 v[196:199], a[84:87], v[132:135], v[196:199]
v_cvt_pk_bf16_f32 v224, v192, v193
v_mfma_f32_16x16x32_bf16 v[196:199], a[88:91], v[136:139], v[196:199]
v_exp_f32 v194, v194
v_mfma_f32_16x16x32_bf16 v[196:199], a[92:95], v[140:143], v[196:199]
buffer_load_dwordx4 v255, s[60:63], s17 offen offset:1024 sc1 lds
v_mfma_f32_16x16x32_bf16 v[204:207], a[92:95], v[156:159], 0
v_exp_f32 v195, v195
v_mfma_f32_16x16x32_bf16 v[204:207], a[88:91], v[152:155], v[204:207]
v_cvt_pk_bf16_f32 v225, v194, v195
v_mfma_f32_16x16x32_bf16 v[204:207], a[84:87], v[148:151], v[204:207]
v_exp_f32 v200, v200
v_mfma_f32_16x16x32_bf16 v[204:207], a[80:83], v[144:147], v[204:207]
buffer_load_dwordx4 v255, s[60:63], s17 offen offset:2048 sc1 lds
v_mfma_f32_16x16x32_bf16 v[208:211], a[64:67], v[160:163], 0
v_exp_f32 v201, v201
v_mfma_f32_16x16x32_bf16 v[208:211], a[68:71], v[164:167], v[208:211]
v_cvt_pk_bf16_f32 v228, v200, v201
v_mfma_f32_16x16x32_bf16 v[208:211], a[72:75], v[168:171], v[208:211]
v_exp_f32 v202, v202
v_mfma_f32_16x16x32_bf16 v[208:211], a[76:79], v[172:175], v[208:211]
buffer_load_dwordx4 v255, s[60:63], s17 offen offset:3072 sc1 lds
v_mfma_f32_16x16x32_bf16 v[216:219], a[76:79], v[188:191], 0
v_exp_f32 v203, v203
v_mfma_f32_16x16x32_bf16 v[216:219], a[72:75], v[184:187], v[216:219]
v_cvt_pk_bf16_f32 v229, v202, v203
v_mfma_f32_16x16x32_bf16 v[216:219], a[68:71], v[180:183], v[216:219]
v_exp_f32 v196, v196
v_mfma_f32_16x16x32_bf16 v[216:219], a[64:67], v[176:179], v[216:219]
buffer_load_dword a253, v254, s[36:39], s90 offen
v_mfma_f32_16x16x32_bf16 v[212:215], a[80:83], v[160:163], 0
v_exp_f32 v197, v197
v_mfma_f32_16x16x32_bf16 v[212:215], a[84:87], v[164:167], v[212:215]
v_cvt_pk_bf16_f32 v226, v196, v197
s_add_u32 s17, s17, 8192
v_mfma_f32_16x16x32_bf16 v[212:215], a[88:91], v[168:171], v[212:215]
v_exp_f32 v198, v198
v_mfma_f32_16x16x32_bf16 v[212:215], a[92:95], v[172:175], v[212:215]
s_add_u32 m0, m0, 16384
s_and_b32 m0, m0, s78
v_mfma_f32_16x16x32_bf16 v[220:223], a[92:95], v[188:191], 0
v_exp_f32 v199, v199
v_mfma_f32_16x16x32_bf16 v[220:223], a[88:91], v[184:187], v[220:223]
v_cvt_pk_bf16_f32 v227, v198, v199
s_add_u32 s90, s90, s21
v_mfma_f32_16x16x32_bf16 v[220:223], a[84:87], v[180:183], v[220:223]
v_exp_f32 v204, v204
v_mfma_f32_16x16x32_bf16 v[220:223], a[80:83], v[176:179], v[220:223]
s_waitcnt vmcnt(11)
s_barrier
v_mfma_f32_16x16x32_bf16 v[64:67], a[96:99], v[232:235], v[64:67]
v_exp_f32 v205, v205
v_mfma_f32_16x16x32_bf16 v[96:99], a[96:99], v[236:239], v[96:99]
ds_read_b128 a[96:99], v248 offset:8192
v_cvt_pk_bf16_f32 v230, v204, v205
v_mfma_f32_16x16x32_bf16 v[100:103], a[100:103], v[236:239], v[100:103]
v_exp_f32 v206, v206
v_mfma_f32_16x16x32_bf16 v[68:71], a[100:103], v[232:235], v[68:71]
ds_read_b128 a[100:103], v248 offset:9216
buffer_load_dword v250, v251, s[64:67], s20 offen sc1
v_mfma_f32_16x16x32_bf16 v[72:75], a[104:107], v[232:235], v[72:75]
v_exp_f32 v207, v207
v_mfma_f32_16x16x32_bf16 v[104:107], a[104:107], v[236:239], v[104:107]
ds_read_b128 a[104:107], v248 offset:10240
v_cvt_pk_bf16_f32 v231, v206, v207
v_mfma_f32_16x16x32_bf16 v[108:111], a[108:111], v[236:239], v[108:111]
v_exp_f32 v208, v208
v_mfma_f32_16x16x32_bf16 v[76:79], a[108:111], v[232:235], v[76:79]
ds_read_b128 a[108:111], v248 offset:11264
v_mfma_f32_16x16x32_bf16 v[80:83], a[112:115], v[232:235], v[80:83]
v_exp_f32 v209, v209
v_mfma_f32_16x16x32_bf16 v[112:115], a[112:115], v[236:239], v[112:115]
ds_read_b128 a[112:115], v248 offset:12288
v_mfma_f32_16x16x32_bf16 v[116:119], a[116:119], v[236:239], v[116:119]
v_exp_f32 v210, v210
v_mfma_f32_16x16x32_bf16 v[84:87], a[116:119], v[232:235], v[84:87]
ds_read_b128 a[116:119], v248 offset:13312
v_mfma_f32_16x16x32_bf16 v[88:91], a[120:123], v[232:235], v[88:91]
v_exp_f32 v211, v211
v_mfma_f32_16x16x32_bf16 v[120:123], a[120:123], v[236:239], v[120:123]
ds_read_b128 a[120:123], v248 offset:14336
s_bitcmp1_b64 s[98:99], s85
v_mfma_f32_16x16x32_bf16 v[124:127], a[124:127], v[236:239], v[124:127]
v_exp_f32 v216, v216
v_mfma_f32_16x16x32_bf16 v[92:95], a[124:127], v[232:235], v[92:95]
ds_read_b128 a[124:127], v248 offset:15360
v_or_b32 v248, s89, v255
v_smfmac_f32_16x16x64_bf16 a[196:199], a[208:211], v[232:239], v241
s_waitcnt lgkmcnt(4)
v_smfmac_f32_16x16x64_bf16 a[192:195], a[208:211], v[224:231], v241
ds_read_b128 a[64:67], v248 offset:0
v_mfma_f32_16x16x32_bf16 v[0:3], a[96:99], v[224:227], v[0:3]
v_exp_f32 v217, v217
v_mfma_f32_16x16x32_bf16 v[32:35], a[96:99], v[228:231], v[32:35]
ds_read_b128 a[68:71], v248 offset:1024
v_cvt_pk_bf16_f32 v232, v208, v209
v_mfma_f32_16x16x32_bf16 v[36:39], a[100:103], v[228:231], v[36:39]
v_exp_f32 v218, v218
v_mfma_f32_16x16x32_bf16 v[4:7], a[100:103], v[224:227], v[4:7]
ds_read_b128 a[72:75], v248 offset:2048
v_cvt_pk_bf16_f32 v233, v210, v211
v_mfma_f32_16x16x32_bf16 v[8:11], a[104:107], v[224:227], v[8:11]
v_exp_f32 v219, v219
v_mfma_f32_16x16x32_bf16 v[40:43], a[104:107], v[228:231], v[40:43]
ds_read_b128 a[76:79], v248 offset:3072
v_cvt_pk_bf16_f32 v236, v216, v217
v_mfma_f32_16x16x32_bf16 v[44:47], a[108:111], v[228:231], v[44:47]
v_exp_f32 v212, v212
v_mfma_f32_16x16x32_bf16 v[12:15], a[108:111], v[224:227], v[12:15]
ds_read_b128 a[80:83], v248 offset:4096
s_waitcnt lgkmcnt(5)
v_mfma_f32_16x16x32_bf16 v[16:19], a[112:115], v[224:227], v[16:19]
v_exp_f32 v213, v213
v_mfma_f32_16x16x32_bf16 v[48:51], a[112:115], v[228:231], v[48:51]
ds_read_b128 a[84:87], v248 offset:5120
v_cvt_pk_bf16_f32 v237, v218, v219
v_mfma_f32_16x16x32_bf16 v[52:55], a[116:119], v[228:231], v[52:55]
v_exp_f32 v214, v214
v_mfma_f32_16x16x32_bf16 v[20:23], a[116:119], v[224:227], v[20:23]
ds_read_b128 a[88:91], v248 offset:6144
v_cvt_pk_bf16_f32 v234, v212, v213
v_mfma_f32_16x16x32_bf16 v[24:27], a[120:123], v[224:227], v[24:27]
v_exp_f32 v215, v215
v_mfma_f32_16x16x32_bf16 v[56:59], a[120:123], v[228:231], v[56:59]
ds_read_b128 a[92:95], v248 offset:7168
v_cvt_pk_bf16_f32 v235, v214, v215
v_mfma_f32_16x16x32_bf16 v[60:63], a[124:127], v[228:231], v[60:63]
v_exp_f32 v220, v220
v_mfma_f32_16x16x32_bf16 v[28:31], a[124:127], v[224:227], v[28:31]
s_cbranch_scc1 .La5_LA_F1E2
.p2align 6
; ---- steady windows
.La5_LA_F1:
s_waitcnt lgkmcnt(4)
v_mfma_f32_16x16x32_bf16 v[192:195], a[64:67], v[128:131], 0
v_exp_f32 v221, v221
v_mfma_f32_16x16x32_bf16 v[192:195], a[68:71], v[132:135], v[192:195]
v_cvt_pk_bf16_f32 v238, v220, v221
s_add_u32 s85, s85, 1
v_mfma_f32_16x16x32_bf16 v[192:195], a[72:75], v[136:139], v[192:195]
v_exp_f32 v222, v222
v_mfma_f32_16x16x32_bf16 v[192:195], a[76:79], v[140:143], v[192:195]
buffer_load_dwordx4 v255, s[60:63], s17 offen offset:0 sc1 lds
v_mfma_f32_16x16x32_bf16 v[200:203], a[76:79], v[156:159], 0
v_exp_f32 v223, v223
v_mfma_f32_16x16x32_bf16 v[200:203], a[72:75], v[152:155], v[200:203]
v_cvt_pk_bf16_f32 v239, v222, v223
s_add_u32 s89, s89, 16384
v_mfma_f32_16x16x32_bf16 v[200:203], a[68:71], v[148:151], v[200:203]
v_exp_f32 v192, v192
v_mfma_f32_16x16x32_bf16 v[200:203], a[64:67], v[144:147], v[200:203]
s_waitcnt lgkmcnt(0)
s_and_b32 s89, s89, 0x1ffff
v_mfma_f32_16x16x32_bf16 v[196:199], a[80:83], v[128:131], 0
v_exp_f32 v193, v193
v_mfma_f32_16x16x32_bf16 v[196:199], a[84:87], v[132:135], v[196:199]
v_cvt_pk_bf16_f32 v224, v192, v193
v_mfma_f32_16x16x32_bf16 v[196:199], a[88:91], v[136:139], v[196:199]
v_exp_f32 v194, v194
v_mfma_f32_16x16x32_bf16 v[196:199], a[92:95], v[140:143], v[196:199]
buffer_load_dwordx4 v255, s[60:63], s17 offen offset:1024 sc1 lds
v_mfma_f32_16x16x32_bf16 v[204:207], a[92:95], v[156:159], 0
v_exp_f32 v195, v195
v_mfma_f32_16x16x32_bf16 v[204:207], a[88:91], v[152:155], v[204:207]
v_cvt_pk_bf16_f32 v225, v194, v195
v_mfma_f32_16x16x32_bf16 v[204:207], a[84:87], v[148:151], v[204:207]
v_exp_f32 v200, v200
v_mfma_f32_16x16x32_bf16 v[204:207], a[80:83], v[144:147], v[204:207]
buffer_load_dwordx4 v255, s[60:63], s17 offen offset:2048 sc1 lds
v_mfma_f32_16x16x32_bf16 v[208:211], a[64:67], v[160:163], 0
v_exp_f32 v201, v201
v_mfma_f32_16x16x32_bf16 v[208:211], a[68:71], v[164:167], v[208:211]
v_cvt_pk_bf16_f32 v228, v200, v201
v_mfma_f32_16x16x32_bf16 v[208:211], a[72:75], v[168:171], v[208:211]
v_exp_f32 v202, v202
v_mfma_f32_16x16x32_bf16 v[208:211], a[76:79], v[172:175], v[208:211]
buffer_load_dwordx4 v255, s[60:63], s17 offen offset:3072 sc1 lds
v_mfma_f32_16x16x32_bf16 v[216:219], a[76:79], v[188:191], 0
v_exp_f32 v203, v203
v_mfma_f32_16x16x32_bf16 v[216:219], a[72:75], v[184:187], v[216:219]
v_cvt_pk_bf16_f32 v229, v202, v203
v_mfma_f32_16x16x32_bf16 v[216:219], a[68:71], v[180:183], v[216:219]
v_exp_f32 v196, v196
v_mfma_f32_16x16x32_bf16 v[216:219], a[64:67], v[176:179], v[216:219]
buffer_load_dword a253, v254, s[36:39], s90 offen
v_mfma_f32_16x16x32_bf16 v[212:215], a[80:83], v[160:163], 0
v_exp_f32 v197, v197
v_mfma_f32_16x16x32_bf16 v[212:215], a[84:87], v[164:167], v[212:215]
v_cvt_pk_bf16_f32 v226, v196, v197
s_add_u32 s17, s17, 8192
v_mfma_f32_16x16x32_bf16 v[212:215], a[88:91], v[168:171], v[212:215]
v_exp_f32 v198, v198
v_mfma_f32_16x16x32_bf16 v[212:215], a[92:95], v[172:175], v[212:215]
s_add_u32 m0, m0, 16384
s_and_b32 m0, m0, s78
v_mfma_f32_16x16x32_bf16 v[220:223], a[92:95], v[188:191], 0
v_exp_f32 v199, v199
v_mfma_f32_16x16x32_bf16 v[220:223], a[88:91], v[184:187], v[220:223]
v_cvt_pk_bf16_f32 v227, v198, v199
s_add_u32 s90, s90, s21
v_mfma_f32_16x16x32_bf16 v[220:223], a[84:87], v[180:183], v[220:223]
v_exp_f32 v204, v204
v_mfma_f32_16x16x32_bf16 v[220:223], a[80:83], v[176:179], v[220:223]
s_waitcnt vmcnt(11)
s_barrier
v_mfma_f32_16x16x32_bf16 v[64:67], a[96:99], v[232:235], v[64:67]
v_exp_f32 v205, v205
v_mfma_f32_16x16x32_bf16 v[96:99], a[96:99], v[236:239], v[96:99]
ds_read_b128 a[96:99], v248 offset:8192
v_cvt_pk_bf16_f32 v230, v204, v205
v_mfma_f32_16x16x32_bf16 v[100:103], a[100:103], v[236:239], v[100:103]
v_exp_f32 v206, v206
v_mfma_f32_16x16x32_bf16 v[68:71], a[100:103], v[232:235], v[68:71]
ds_read_b128 a[100:103], v248 offset:9216
v_mfma_f32_16x16x32_bf16 v[72:75], a[104:107], v[232:235], v[72:75]
v_exp_f32 v207, v207
v_mfma_f32_16x16x32_bf16 v[104:107], a[104:107], v[236:239], v[104:107]
ds_read_b128 a[104:107], v248 offset:10240
v_cvt_pk_bf16_f32 v231, v206, v207
v_mfma_f32_16x16x32_bf16 v[108:111], a[108:111], v[236:239], v[108:111]
v_exp_f32 v208, v208
v_mfma_f32_16x16x32_bf16 v[76:79], a[108:111], v[232:235], v[76:79]
ds_read_b128 a[108:111], v248 offset:11264
v_mfma_f32_16x16x32_bf16 v[80:83], a[112:115], v[232:235], v[80:83]
v_exp_f32 v209, v209
v_mfma_f32_16x16x32_bf16 v[112:115], a[112:115], v[236:239], v[112:115]
ds_read_b128 a[112:115], v248 offset:12288
v_mfma_f32_16x16x32_bf16 v[116:119], a[116:119], v[236:239], v[116:119]
v_exp_f32 v210, v210
v_mfma_f32_16x16x32_bf16 v[84:87], a[116:119], v[232:235], v[84:87]
ds_read_b128 a[116:119], v248 offset:13312
v_mfma_f32_16x16x32_bf16 v[88:91], a[120:123], v[232:235], v[88:91]
v_exp_f32 v211, v211
v_mfma_f32_16x16x32_bf16 v[120:123], a[120:123], v[236:239], v[120:123]
ds_read_b128 a[120:123], v248 offset:14336
s_bitcmp1_b64 s[98:99], s85
v_mfma_f32_16x16x32_bf16 v[124:127], a[124:127], v[236:239], v[124:127]
v_exp_f32 v216, v216
v_mfma_f32_16x16x32_bf16 v[92:95], a[124:127], v[232:235], v[92:95]
ds_read_b128 a[124:127], v248 offset:15360
v_or_b32 v248, s89, v255
v_smfmac_f32_16x16x64_bf16 a[196:199], a[208:211], v[232:239], v241
s_waitcnt lgkmcnt(4)
v_smfmac_f32_16x16x64_bf16 a[192:195], a[208:211], v[224:231], v241
ds_read_b128 a[64:67], v248 offset:0
v_mfma_f32_16x16x32_bf16 v[0:3], a[96:99], v[224:227], v[0:3]
v_exp_f32 v217, v217
v_mfma_f32_16x16x32_bf16 v[32:35], a[96:99], v[228:231], v[32:35]
ds_read_b128 a[68:71], v248 offset:1024
v_cvt_pk_bf16_f32 v232, v208, v209
v_mfma_f32_16x16x32_bf16 v[36:39], a[100:103], v[228:231], v[36:39]
v_exp_f32 v218, v218
v_mfma_f32_16x16x32_bf16 v[4:7], a[100:103], v[224:227], v[4:7]
ds_read_b128 a[72:75], v248 offset:2048
v_cvt_pk_bf16_f32 v233, v210, v211
v_mfma_f32_16x16x32_bf16 v[8:11], a[104:107], v[224:227], v[8:11]
v_exp_f32 v219, v219
v_mfma_f32_16x16x32_bf16 v[40:43], a[104:107], v[228:231], v[40:43]
ds_read_b128 a[76:79], v248 offset:3072
v_cvt_pk_bf16_f32 v236, v216, v217
v_mfma_f32_16x16x32_bf16 v[44:47], a[108:111], v[228:231], v[44:47]
v_exp_f32 v212, v212
v_mfma_f32_16x16x32_bf16 v[12:15], a[108:111], v[224:227], v[12:15]
ds_read_b128 a[80:83], v248 offset:4096
s_waitcnt lgkmcnt(5)
v_mfma_f32_16x16x32_bf16 v[16:19], a[112:115], v[224:227], v[16:19]
v_exp_f32 v213, v213
v_mfma_f32_16x16x32_bf16 v[48:51], a[112:115], v[228:231], v[48:51]
ds_read_b128 a[84:87], v248 offset:5120
v_cvt_pk_bf16_f32 v237, v218, v219
v_mfma_f32_16x16x32_bf16 v[52:55], a[116:119], v[228:231], v[52:55]
v_exp_f32 v214, v214
v_mfma_f32_16x16x32_bf16 v[20:23], a[116:119], v[224:227], v[20:23]
ds_read_b128 a[88:91], v248 offset:6144
v_cvt_pk_bf16_f32 v234, v212, v213
v_mfma_f32_16x16x32_bf16 v[24:27], a[120:123], v[224:227], v[24:27]
v_exp_f32 v215, v215
v_mfma_f32_16x16x32_bf16 v[56:59], a[120:123], v[228:231], v[56:59]
ds_read_b128 a[92:95], v248 offset:7168
v_cvt_pk_bf16_f32 v235, v214, v215
v_mfma_f32_16x16x32_bf16 v[60:63], a[124:127], v[228:231], v[60:63]
v_exp_f32 v220, v220
v_mfma_f32_16x16x32_bf16 v[28:31], a[124:127], v[224:227], v[28:31]
s_cbranch_scc1 .La5_LA_F1E2
s_waitcnt lgkmcnt(4)
v_mfma_f32_16x16x32_bf16 v[192:195], a[64:67], v[128:131], 0
v_exp_f32 v221, v221
v_mfma_f32_16x16x32_bf16 v[192:195], a[68:71], v[132:135], v[192:195]
v_cvt_pk_bf16_f32 v238, v220, v221
s_add_u32 s85, s85, 1
v_mfma_f32_16x16x32_bf16 v[192:195], a[72:75], v[136:139], v[192:195]
v_exp_f32 v222, v222
v_mfma_f32_16x16x32_bf16 v[192:195], a[76:79], v[140:143], v[192:195]
buffer_load_dwordx4 v255, s[60:63], s17 offen offset:0 sc1 lds
v_mfma_f32_16x16x32_bf16 v[200:203], a[76:79], v[156:159], 0
v_exp_f32 v223, v223
v_mfma_f32_16x16x32_bf16 v[200:203], a[72:75], v[152:155], v[200:203]
v_cvt_pk_bf16_f32 v239, v222, v223
s_add_u32 s89, s89, 16384
v_mfma_f32_16x16x32_bf16 v[200:203], a[68:71], v[148:151], v[200:203]
v_exp_f32 v192, v192
v_mfma_f32_16x16x32_bf16 v[200:203], a[64:67], v[144:147], v[200:203]
s_waitcnt lgkmcnt(0)
s_and_b32 s89, s89, 0x1ffff
v_mfma_f32_16x16x32_bf16 v[196:199], a[80:83], v[128:131], 0
v_exp_f32 v193, v193
v_mfma_f32_16x16x32_bf16 v[196:199], a[84:87], v[132:135], v[196:199]
v_cvt_pk_bf16_f32 v224, v192, v193
v_mfma_f32_16x16x32_bf16 v[196:199], a[88:91], v[136:139], v[196:199]
v_exp_f32 v194, v194
v_mfma_f32_16x16x32_bf16 v[196:199], a[92:95], v[140:143], v[196:199]
buffer_load_dwordx4 v255, s[60:63], s17 offen offset:1024 sc1 lds
v_mfma_f32_16x16x32_bf16 v[204:207], a[92:95], v[156:159], 0
v_exp_f32 v195, v195
v_mfma_f32_16x16x32_bf16 v[204:207], a[88:91], v[152:155], v[204:207]
v_cvt_pk_bf16_f32 v225, v194, v195
v_mfma_f32_16x16x32_bf16 v[204:207], a[84:87], v[148:151], v[204:207]
v_exp_f32 v200, v200
v_mfma_f32_16x16x32_bf16 v[204:207], a[80:83], v[144:147], v[204:207]
buffer_load_dwordx4 v255, s[60:63], s17 offen offset:2048 sc1 lds
v_mfma_f32_16x16x32_bf16 v[208:211], a[64:67], v[160:163], 0
v_exp_f32 v201, v201
v_mfma_f32_16x16x32_bf16 v[208:211], a[68:71], v[164:167], v[208:211]
v_cvt_pk_bf16_f32 v228, v200, v201
v_mfma_f32_16x16x32_bf16 v[208:211], a[72:75], v[168:171], v[208:211]
v_exp_f32 v202, v202
v_mfma_f32_16x16x32_bf16 v[208:211], a[76:79], v[172:175], v[208:211]
buffer_load_dwordx4 v255, s[60:63], s17 offen offset:3072 sc1 lds
v_mfma_f32_16x16x32_bf16 v[216:219], a[76:79], v[188:191], 0
v_exp_f32 v203, v203
v_mfma_f32_16x16x32_bf16 v[216:219], a[72:75], v[184:187], v[216:219]
v_cvt_pk_bf16_f32 v229, v202, v203
v_mfma_f32_16x16x32_bf16 v[216:219], a[68:71], v[180:183], v[216:219]
v_exp_f32 v196, v196
v_mfma_f32_16x16x32_bf16 v[216:219], a[64:67], v[176:179], v[216:219]
buffer_load_dword a253, v254, s[36:39], s90 offen
v_mfma_f32_16x16x32_bf16 v[212:215], a[80:83], v[160:163], 0
v_exp_f32 v197, v197
v_mfma_f32_16x16x32_bf16 v[212:215], a[84:87], v[164:167], v[212:215]
v_cvt_pk_bf16_f32 v226, v196, v197
s_add_u32 s17, s17, 8192
v_mfma_f32_16x16x32_bf16 v[212:215], a[88:91], v[168:171], v[212:215]
v_exp_f32 v198, v198
v_mfma_f32_16x16x32_bf16 v[212:215], a[92:95], v[172:175], v[212:215]
s_add_u32 m0, m0, 16384
s_and_b32 m0, m0, s78
v_mfma_f32_16x16x32_bf16 v[220:223], a[92:95], v[188:191], 0
v_exp_f32 v199, v199
v_mfma_f32_16x16x32_bf16 v[220:223], a[88:91], v[184:187], v[220:223]
v_cvt_pk_bf16_f32 v227, v198, v199
s_add_u32 s90, s90, s21
v_mfma_f32_16x16x32_bf16 v[220:223], a[84:87], v[180:183], v[220:223]
v_exp_f32 v204, v204
v_mfma_f32_16x16x32_bf16 v[220:223], a[80:83], v[176:179], v[220:223]
s_waitcnt vmcnt(10)
s_barrier
v_mfma_f32_16x16x32_bf16 v[64:67], a[96:99], v[232:235], v[64:67]
v_exp_f32 v205, v205
v_mfma_f32_16x16x32_bf16 v[96:99], a[96:99], v[236:239], v[96:99]
ds_read_b128 a[96:99], v248 offset:8192
v_cvt_pk_bf16_f32 v230, v204, v205
v_mfma_f32_16x16x32_bf16 v[100:103], a[100:103], v[236:239], v[100:103]
v_exp_f32 v206, v206
v_mfma_f32_16x16x32_bf16 v[68:71], a[100:103], v[232:235], v[68:71]
ds_read_b128 a[100:103], v248 offset:9216
v_mfma_f32_16x16x32_bf16 v[72:75], a[104:107], v[232:235], v[72:75]
v_exp_f32 v207, v207
v_mfma_f32_16x16x32_bf16 v[104:107], a[104:107], v[236:239], v[104:107]
ds_read_b128 a[104:107], v248 offset:10240
v_cvt_pk_bf16_f32 v231, v206, v207
v_mfma_f32_16x16x32_bf16 v[108:111], a[108:111], v[236:239], v[108:111]
v_exp_f32 v208, v208
v_mfma_f32_16x16x32_bf16 v[76:79], a[108:111], v[232:235], v[76:79]
ds_read_b128 a[108:111], v248 offset:11264
v_mfma_f32_16x16x32_bf16 v[80:83], a[112:115], v[232:235], v[80:83]
v_exp_f32 v209, v209
v_mfma_f32_16x16x32_bf16 v[112:115], a[112:115], v[236:239], v[112:115]
ds_read_b128 a[112:115], v248 offset:12288
v_mfma_f32_16x16x32_bf16 v[116:119], a[116:119], v[236:239], v[116:119]
v_exp_f32 v210, v210
v_mfma_f32_16x16x32_bf16 v[84:87], a[116:119], v[232:235], v[84:87]
ds_read_b128 a[116:119], v248 offset:13312
v_mfma_f32_16x16x32_bf16 v[88:91], a[120:123], v[232:235], v[88:91]
v_exp_f32 v211, v211
v_mfma_f32_16x16x32_bf16 v[120:123], a[120:123], v[236:239], v[120:123]
ds_read_b128 a[120:123], v248 offset:14336
s_bitcmp1_b64 s[98:99], s85
v_mfma_f32_16x16x32_bf16 v[124:127], a[124:127], v[236:239], v[124:127]
v_exp_f32 v216, v216
v_mfma_f32_16x16x32_bf16 v[92:95], a[124:127], v[232:235], v[92:95]
ds_read_b128 a[124:127], v248 offset:15360
v_or_b32 v248, s89, v255
v_smfmac_f32_16x16x64_bf16 a[196:199], a[208:211], v[232:239], v241
s_waitcnt lgkmcnt(4)
v_smfmac_f32_16x16x64_bf16 a[192:195], a[208:211], v[224:231], v241
ds_read_b128 a[64:67], v248 offset:0
v_mfma_f32_16x16x32_bf16 v[0:3], a[96:99], v[224:227], v[0:3]
v_exp_f32 v217, v217
v_mfma_f32_16x16x32_bf16 v[32:35], a[96:99], v[228:231], v[32:35]
ds_read_b128 a[68:71], v248 offset:1024
v_cvt_pk_bf16_f32 v232, v208, v209
v_mfma_f32_16x16x32_bf16 v[36:39], a[100:103], v[228:231], v[36:39]
v_exp_f32 v218, v218
v_mfma_f32_16x16x32_bf16 v[4:7], a[100:103], v[224:227], v[4:7]
ds_read_b128 a[72:75], v248 offset:2048
v_cvt_pk_bf16_f32 v233, v210, v211
v_mfma_f32_16x16x32_bf16 v[8:11], a[104:107], v[224:227], v[8:11]
v_exp_f32 v219, v219
v_mfma_f32_16x16x32_bf16 v[40:43], a[104:107], v[228:231], v[40:43]
ds_read_b128 a[76:79], v248 offset:3072
v_cvt_pk_bf16_f32 v236, v216, v217
v_mfma_f32_16x16x32_bf16 v[44:47], a[108:111], v[228:231], v[44:47]
v_exp_f32 v212, v212
v_mfma_f32_16x16x32_bf16 v[12:15], a[108:111], v[224:227], v[12:15]
ds_read_b128 a[80:83], v248 offset:4096
s_waitcnt lgkmcnt(5)
v_mfma_f32_16x16x32_bf16 v[16:19], a[112:115], v[224:227], v[16:19]
v_exp_f32 v213, v213
v_mfma_f32_16x16x32_bf16 v[48:51], a[112:115], v[228:231], v[48:51]
ds_read_b128 a[84:87], v248 offset:5120
v_cvt_pk_bf16_f32 v237, v218, v219
v_mfma_f32_16x16x32_bf16 v[52:55], a[116:119], v[228:231], v[52:55]
v_exp_f32 v214, v214
v_mfma_f32_16x16x32_bf16 v[20:23], a[116:119], v[224:227], v[20:23]
ds_read_b128 a[88:91], v248 offset:6144
v_cvt_pk_bf16_f32 v234, v212, v213
v_mfma_f32_16x16x32_bf16 v[24:27], a[120:123], v[224:227], v[24:27]
v_exp_f32 v215, v215
v_mfma_f32_16x16x32_bf16 v[56:59], a[120:123], v[228:231], v[56:59]
ds_read_b128 a[92:95], v248 offset:7168
v_cvt_pk_bf16_f32 v235, v214, v215
v_mfma_f32_16x16x32_bf16 v[60:63], a[124:127], v[228:231], v[60:63]
v_exp_f32 v220, v220
v_mfma_f32_16x16x32_bf16 v[28:31], a[124:127], v[224:227], v[28:31]
.La5_LA_F1E:
s_cbranch_scc0 .La5_LA_F1
.La5_LA_F1E2:
s_cmp_eq_u32 s85, s82
s_cbranch_scc1 .La5_LA_F1X
s_nop 0
s_nop 0
s_call_b64 s[24:25], .La5_LA_EVENT
s_branch .La5_LA_F1
.La5_LA_FE0:
.La5_LW2_1:
s_cbranch_execz .La5_LW2_R1
s_cmp_eq_u32 s85, s82
s_cbranch_scc1 .La5_LA_F1X
s_call_b64 s[24:25], .La5_LA_EVENT
s_branch .La5_LA_FS0
; ---- hand-offs between windows
.La5_LA_EVENT:
s_cmp_lg_u32 s85, 2
s_cbranch_scc1 .La5_LA_E1
s_waitcnt vmcnt(0)
v_cmp_eq_u32 vcc, v250, v249
s_mov_b32 s17, s16
s_mov_b32 s78, 0x1ffff
s_lshl_b32 m0, s3, 12
s_or_b32 vcc_hi, vcc_hi, s79
s_cmp_eq_u64 vcc, -1
s_cbranch_scc0 .La5_LA_KSPIN
.La5_LA_KOK:
buffer_load_dwordx4 v255, s[60:63], s17 offen offset:0 sc1 lds
buffer_load_dwordx4 v255, s[60:63], s17 offen offset:1024 sc1 lds
buffer_load_dwordx4 v255, s[60:63], s17 offen offset:2048 sc1 lds
buffer_load_dwordx4 v255, s[60:63], s17 offen offset:3072 sc1 lds
s_add_u32 s17, s17, 8192
s_add_u32 m0, m0, 16384
s_mov_b32 s20, s19
v_and_b32 v251, 4, v251
s_cmp_lg_u32 s3, 0
s_cbranch_scc1 .La5_LA_POLL
s_nop 0
buffer_load_dwordx4 v255, s[60:63], s17 offen offset:0 sc1 lds
buffer_load_dwordx4 v255, s[60:63], s17 offen offset:1024 sc1 lds
buffer_load_dwordx4 v255, s[60:63], s17 offen offset:2048 sc1 lds
buffer_load_dwordx4 v255, s[60:63], s17 offen offset:3072 sc1 lds
s_add_u32 s17, s17, 8192
s_add_u32 m0, m0, 16384
.La5_LA_POLL:
s_cmp_lg_u32 s22, 0
s_cbranch_scc1 .La5_LA_WP
s_mov_b64 exec, 0
s_cmp_eq_u32 s3, 0
s_cbranch_scc1 .La5_LW5_0
s_cmp_eq_u32 s3, 1
s_cbranch_scc1 .La5_LW5_1
s_cmp_eq_u32 s3, 2
s_cbranch_scc1 .La5_LW5_2
s_branch .La5_LW5_3
.La5_LW5_R0:
s_mov_b64 exec, -1
s_setpc_b64 s[24:25]
.La5_LA_WP:
s_mov_b64 exec, 0
s_cmp_eq_u32 s3, 0
s_cbranch_scc1 .La5_LW6_0
s_cmp_eq_u32 s3, 1
s_cbranch_scc1 .La5_LW6_1
s_cmp_eq_u32 s3, 2
s_cbranch_scc1 .La5_LW6_2
s_branch .La5_LW6_3
.La5_LW6_R0:
s_mov_b64 exec, -1
s_cmp_eq_u32 s73, 1
s_cbranch_scc1 .La5_LA_E0
buffer_load_dword v250, v251, s[64:67], s20 offen sc1
s_setpc_b64 s[24:25]
.La5_LA_E0:
s_mov_b32 s54, 16384
buffer_load_dwordx4 a[0:3], v255, s[52:55], 0 offen offset:0 sc1
buffer_load_dwordx4 a[4:7], v255, s[52:55], 0 offen offset:1024 sc1
buffer_load_dwordx4 a[8:11], v255, s[52:55], 0 offen offset:2048 sc1
buffer_load_dwordx4 a[12:15], v255, s[52:55], 0 offen offset:3072 sc1
buffer_load_dwordx4 a[16:19], v255, s[52:55], s95 offen offset:0 sc1
buffer_load_dwordx4 a[20:23], v255, s[52:55], s95 offen offset:1024 sc1
s_setpc_b64 s[24:25]
.La5_LA_E1:
buffer_load_dwordx4 a[24:27], v255, s[52:55], s95 offen offset:2048 sc1
buffer_load_dwordx4 a[28:31], v255, s[52:55], s95 offen offset:3072 sc1
buffer_load_dwordx4 a[32:35], v255, s[52:55], s96 offen offset:0 sc1
buffer_load_dwordx4 a[36:39], v255, s[52:55], s96 offen offset:1024 sc1
buffer_load_dwordx4 a[40:43], v255, s[52:55], s96 offen offset:2048 sc1
buffer_load_dwordx4 a[44:47], v255, s[52:55], s96 offen offset:3072 sc1
buffer_load_dwordx4 a[48:51], v255, s[52:55], s97 offen offset:0 sc1
buffer_load_dwordx4 a[52:55], v255, s[52:55], s97 offen offset:1024 sc1
buffer_load_dwordx4 a[56:59], v255, s[52:55], s97 offen offset:2048 sc1
buffer_load_dwordx4 a[60:63], v255, s[52:55], s97 offen offset:3072 sc1
s_setpc_b64 s[24:25]
; ---- helper: own block finished: l is checked (.La5_LA_SLOW if outside [2^-60, 2^60] or NaN), O / l leaves; the partner-s rows start
.La5_LA_F1X:
s_cmp_eq_u32 s22, 0
s_nop 0
s_nop 0
s_cbranch_scc1 .La5_LA_DC
s_cmp_eq_u32 s73, 1
s_cbranch_scc1 .La5_LA_F1Y
s_nop 0
v_cmp_eq_u32 vcc, v250, v249
s_mov_b32 s17, 0
s_mov_b32 s54, 16384
s_and_b32 s92, vcc_lo, 3
s_cmp_eq_u32 s92, 3
s_cbranch_scc0 .La5_LA_QSPIN
.La5_LA_QOK:
s_waitcnt lgkmcnt(4)
v_mfma_f32_16x16x32_bf16 v[192:195], a[64:67], v[128:131], 0
v_exp_f32 v221, v221
v_mfma_f32_16x16x32_bf16 v[192:195], a[68:71], v[132:135], v[192:195]
v_cvt_pk_bf16_f32 v238, v220, v221
s_add_u32 s85, s85, 1
v_mfma_f32_16x16x32_bf16 v[192:195], a[72:75], v[136:139], v[192:195]
v_exp_f32 v222, v222
v_mfma_f32_16x16x32_bf16 v[192:195], a[76:79], v[140:143], v[192:195]
buffer_load_dwordx4 v255, s[60:63], s17 offen offset:0 sc1 lds
v_mfma_f32_16x16x32_bf16 v[200:203], a[76:79], v[156:159], 0
v_exp_f32 v223, v223
v_mfma_f32_16x16x32_bf16 v[200:203], a[72:75], v[152:155], v[200:203]
v_cvt_pk_bf16_f32 v239, v222, v223
s_add_u32 s89, s89, 16384
v_mfma_f32_16x16x32_bf16 v[200:203], a[68:71], v[148:151], v[200:203]
v_exp_f32 v192, v192
v_mfma_f32_16x16x32_bf16 v[200:203], a[64:67], v[144:147], v[200:203]
s_waitcnt lgkmcnt(0)
s_and_b32 s89, s89, 0x1ffff
v_mfma_f32_16x16x32_bf16 v[196:199], a[80:83], v[128:131], 0
v_exp_f32 v193, v193
v_mfma_f32_16x16x32_bf16 v[196:199], a[84:87], v[132:135], v[196:199]
v_cvt_pk_bf16_f32 v224, v192, v193
v_mfma_f32_16x16x32_bf16 v[196:199], a[88:91], v[136:139], v[196:199]
v_exp_f32 v194, v194
v_mfma_f32_16x16x32_bf16 v[196:199], a[92:95], v[140:143], v[196:199]
buffer_load_dwordx4 v255, s[60:63], s17 offen offset:1024 sc1 lds
v_mfma_f32_16x16x32_bf16 v[204:207], a[92:95], v[156:159], 0
v_exp_f32 v195, v195
v_mfma_f32_16x16x32_bf16 v[204:207], a[88:91], v[152:155], v[204:207]
v_cvt_pk_bf16_f32 v225, v194, v195
v_mfma_f32_16x16x32_bf16 v[204:207], a[84:87], v[148:151], v[204:207]
v_exp_f32 v200, v200
v_mfma_f32_16x16x32_bf16 v[204:207], a[80:83], v[144:147], v[204:207]
buffer_load_dwordx4 v255, s[60:63], s17 offen offset:2048 sc1 lds
v_mfma_f32_16x16x32_bf16 v[208:211], a[64:67], v[160:163], 0
v_exp_f32 v201, v201
v_mfma_f32_16x16x32_bf16 v[208:211], a[68:71], v[164:167], v[208:211]
v_cvt_pk_bf16_f32 v228, v200, v201
v_mfma_f32_16x16x32_bf16 v[208:211], a[72:75], v[168:171], v[208:211]
v_exp_f32 v202, v202
v_mfma_f32_16x16x32_bf16 v[208:211], a[76:79], v[172:175], v[208:211]
buffer_load_dwordx4 v255, s[60:63], s17 offen offset:3072 sc1 lds
v_mfma_f32_16x16x32_bf16 v[216:219], a[76:79], v[188:191], 0
v_exp_f32 v203, v203
v_mfma_f32_16x16x32_bf16 v[216:219], a[72:75], v[184:187], v[216:219]
v_cvt_pk_bf16_f32 v229, v202, v203
v_mfma_f32_16x16x32_bf16 v[216:219], a[68:71], v[180:183], v[216:219]
v_exp_f32 v196, v196
v_mfma_f32_16x16x32_bf16 v[216:219], a[64:67], v[176:179], v[216:219]
buffer_load_dwordx4 a[0:3], v255, s[52:55], 0 offen offset:0 sc1
v_mfma_f32_16x16x32_bf16 v[212:215], a[80:83], v[160:163], 0
v_exp_f32 v197, v197
v_mfma_f32_16x16x32_bf16 v[212:215], a[84:87], v[164:167], v[212:215]
.La5_LW6_0:
s_cbranch_execz .La5_LW6_4
v_cvt_pk_bf16_f32 v226, v196, v197
s_add_u32 s17, s17, 8192
v_mfma_f32_16x16x32_bf16 v[212:215], a[88:91], v[168:171], v[212:215]
v_exp_f32 v198, v198
v_mfma_f32_16x16x32_bf16 v[212:215], a[92:95], v[172:175], v[212:215]
s_add_u32 m0, m0, 16384
s_and_b32 m0, m0, s78
buffer_load_dwordx4 a[4:7], v255, s[52:55], 0 offen offset:1024 sc1
v_mfma_f32_16x16x32_bf16 v[220:223], a[92:95], v[188:191], 0
v_exp_f32 v199, v199
v_mfma_f32_16x16x32_bf16 v[220:223], a[88:91], v[184:187], v[220:223]
v_cvt_pk_bf16_f32 v227, v198, v199
v_mfma_f32_16x16x32_bf16 v[220:223], a[84:87], v[180:183], v[220:223]
v_exp_f32 v204, v204
v_mfma_f32_16x16x32_bf16 v[220:223], a[80:83], v[176:179], v[220:223]
s_waitcnt vmcnt(12)
s_barrier
v_mfma_f32_16x16x32_bf16 v[64:67], a[96:99], v[232:235], v[64:67]
v_exp_f32 v205, v205
v_mfma_f32_16x16x32_bf16 v[96:99], a[96:99], v[236:239], v[96:99]
ds_read_b128 a[96:99], v248 offset:8192
v_cvt_pk_bf16_f32 v230, v204, v205
v_mfma_f32_16x16x32_bf16 v[100:103], a[100:103], v[236:239], v[100:103]
v_exp_f32 v206, v206
v_mfma_f32_16x16x32_bf16 v[68:71], a[100:103], v[232:235], v[68:71]
ds_read_b128 a[100:103], v248 offset:9216
buffer_load_dwordx4 a[8:11], v255, s[52:55], 0 offen offset:2048 sc1
v_mfma_f32_16x16x32_bf16 v[72:75], a[104:107], v[232:235], v[72:75]
v_exp_f32 v207, v207
v_mfma_f32_16x16x32_bf16 v[104:107], a[104:107], v[236:239], v[104:107]
ds_read_b128 a[104:107], v248 offset:10240
v_cvt_pk_bf16_f32 v231, v206, v207
v_mfma_f32_16x16x32_bf16 v[108:111], a[108:111], v[236:239], v[108:111]
v_exp_f32 v208, v208
v_mfma_f32_16x16x32_bf16 v[76:79], a[108:111], v[232:235], v[76:79]
ds_read_b128 a[108:111], v248 offset:11264
buffer_load_dwordx4 a[12:15], v255, s[52:55], 0 offen offset:3072 sc1
v_mfma_f32_16x16x32_bf16 v[80:83], a[112:115], v[232:235], v[80:83]
v_exp_f32 v209, v209
v_mfma_f32_16x16x32_bf16 v[112:115], a[112:115], v[236:239], v[112:115]
ds_read_b128 a[112:115], v248 offset:12288
v_mfma_f32_16x16x32_bf16 v[116:119], a[116:119], v[236:239], v[116:119]
v_exp_f32 v210, v210
v_mfma_f32_16x16x32_bf16 v[84:87], a[116:119], v[232:235], v[84:87]
ds_read_b128 a[116:119], v248 offset:13312
buffer_load_dwordx4 a[16:19], v255, s[52:55], s95 offen offset:0 sc1
v_mfma_f32_16x16x32_bf16 v[88:91], a[120:123], v[232:235], v[88:91]
v_exp_f32 v211, v211
v_mfma_f32_16x16x32_bf16 v[120:123], a[120:123], v[236:239], v[120:123]
ds_read_b128 a[120:123], v248 offset:14336
s_bitcmp1_b64 s[98:99], s85
v_mfma_f32_16x16x32_bf16 v[124:127], a[124:127], v[236:239], v[124:127]
v_exp_f32 v216, v216
v_mfma_f32_16x16x32_bf16 v[92:95], a[124:127], v[232:235], v[92:95]
ds_read_b128 a[124:127], v248 offset:15360
v_or_b32 v248, s89, v255
v_smfmac_f32_16x16x64_bf16 a[196:199], a[208:211], v[232:239], v241
s_waitcnt lgkmcnt(4)
v_smfmac_f32_16x16x64_bf16 a[192:195], a[208:211], v[224:231], v241
ds_read_b128 a[64:67], v248 offset:0
v_mfma_f32_16x16x32_bf16 v[0:3], a[96:99], v[224:227], v[0:3]
v_exp_f32 v217, v217
v_mfma_f32_16x16x32_bf16 v[32:35], a[96:99], v[228:231], v[32:35]
ds_read_b128 a[68:71], v248 offset:1024
v_cvt_pk_bf16_f32 v232, v208, v209
v_mfma_f32_16x16x32_bf16 v[36:39], a[100:103], v[228:231], v[36:39]
v_exp_f32 v218, v218
v_mfma_f32_16x16x32_bf16 v[4:7], a[100:103], v[224:227], v[4:7]
ds_read_b128 a[72:75], v248 offset:2048
v_cvt_pk_bf16_f32 v233, v210, v211
v_mfma_f32_16x16x32_bf16 v[8:11], a[104:107], v[224:227], v[8:11]
v_exp_f32 v219, v219
v_mfma_f32_16x16x32_bf16 v[40:43], a[104:107], v[228:231], v[40:43]
ds_read_b128 a[76:79], v248 offset:3072
v_cvt_pk_bf16_f32 v236, v216, v217
v_mfma_f32_16x16x32_bf16 v[44:47], a[108:111], v[228:231], v[44:47]
v_exp_f32 v212, v212
v_mfma_f32_16x16x32_bf16 v[12:15], a[108:111], v[224:227], v[12:15]
ds_read_b128 a[80:83], v248 offset:4096
s_waitcnt lgkmcnt(5)
v_mfma_f32_16x16x32_bf16 v[16:19], a[112:115], v[224:227], v[16:19]
v_exp_f32 v213, v213
v_mfma_f32_16x16x32_bf16 v[48:51], a[112:115], v[228:231], v[48:51]
ds_read_b128 a[84:87], v248 offset:5120
v_cvt_pk_bf16_f32 v237, v218, v219
v_mfma_f32_16x16x32_bf16 v[52:55], a[116:119], v[228:231], v[52:55]
v_exp_f32 v214, v214
v_mfma_f32_16x16x32_bf16 v[20:23], a[116:119], v[224:227], v[20:23]
ds_read_b128 a[88:91], v248 offset:6144
v_cvt_pk_bf16_f32 v234, v212, v213
v_mfma_f32_16x16x32_bf16 v[24:27], a[120:123], v[224:227], v[24:27]
v_exp_f32 v215, v215
v_mfma_f32_16x16x32_bf16 v[56:59], a[120:123], v[228:231], v[56:59]
ds_read_b128 a[92:95], v248 offset:7168
v_cvt_pk_bf16_f32 v235, v214, v215
v_mfma_f32_16x16x32_bf16 v[60:63], a[124:127], v[228:231], v[60:63]
v_exp_f32 v220, v220
v_mfma_f32_16x16x32_bf16 v[28:31], a[124:127], v[224:227], v[28:31]
buffer_load_dwordx4 a[20:23], v255, s[52:55], s95 offen offset:1024 sc1
s_waitcnt lgkmcnt(4)
v_mfma_f32_16x16x32_bf16 v[192:195], a[64:67], v[128:131], 0
v_exp_f32 v221, v221
v_mfma_f32_16x16x32_bf16 v[192:195], a[68:71], v[132:135], v[192:195]
v_cvt_pk_bf16_f32 v238, v220, v221
s_add_u32 s85, s85, 1
v_mfma_f32_16x16x32_bf16 v[192:195], a[72:75], v[136:139], v[192:195]
v_exp_f32 v222, v222
v_mfma_f32_16x16x32_bf16 v[192:195], a[76:79], v[140:143], v[192:195]
buffer_load_dwordx4 v255, s[60:63], s17 offen offset:0 sc1 lds
v_mfma_f32_16x16x32_bf16 v[200:203], a[76:79], v[156:159], 0
v_exp_f32 v223, v223
v_mfma_f32_16x16x32_bf16 v[200:203], a[72:75], v[152:155], v[200:203]
v_cvt_pk_bf16_f32 v239, v222, v223
s_add_u32 s89, s89, 16384
v_mfma_f32_16x16x32_bf16 v[200:203], a[68:71], v[148:151], v[200:203]
v_exp_f32 v192, v192
v_mfma_f32_16x16x32_bf16 v[200:203], a[64:67], v[144:147], v[200:203]
s_waitcnt lgkmcnt(0)
s_and_b32 s89, s89, 0x1ffff
buffer_load_dwordx4 a[24:27], v255, s[52:55], s95 offen offset:2048 sc1
v_mfma_f32_16x16x32_bf16 v[196:199], a[80:83], v[128:131], 0
v_exp_f32 v193, v193
v_mfma_f32_16x16x32_bf16 v[196:199], a[84:87], v[132:135], v[196:199]
v_cvt_pk_bf16_f32 v224, v192, v193
v_mfma_f32_16x16x32_bf16 v[196:199], a[88:91], v[136:139], v[196:199]
v_exp_f32 v194, v194
v_mfma_f32_16x16x32_bf16 v[196:199], a[92:95], v[140:143], v[196:199]
buffer_load_dwordx4 v255, s[60:63], s17 offen offset:1024 sc1 lds
v_mfma_f32_16x16x32_bf16 v[204:207], a[92:95], v[156:159], 0
v_exp_f32 v195, v195
v_mfma_f32_16x16x32_bf16 v[204:207], a[88:91], v[152:155], v[204:207]
v_cvt_pk_bf16_f32 v225, v194, v195
v_mfma_f32_16x16x32_bf16 v[204:207], a[84:87], v[148:151], v[204:207]
v_exp_f32 v200, v200
v_mfma_f32_16x16x32_bf16 v[204:207], a[80:83], v[144:147], v[204:207]
buffer_load_dwordx4 v255, s[60:63], s17 offen offset:2048 sc1 lds
v_mfma_f32_16x16x32_bf16 v[208:211], a[64:67], v[160:163], 0
v_exp_f32 v201, v201
v_mfma_f32_16x16x32_bf16 v[208:211], a[68:71], v[164:167], v[208:211]
v_cvt_pk_bf16_f32 v228, v200, v201
v_mfma_f32_16x16x32_bf16 v[208:211], a[72:75], v[168:171], v[208:211]
v_exp_f32 v202, v202
v_mfma_f32_16x16x32_bf16 v[208:211], a[76:79], v[172:175], v[208:211]
buffer_load_dwordx4 v255, s[60:63], s17 offen offset:3072 sc1 lds
v_mfma_f32_16x16x32_bf16 v[216:219], a[76:79], v[188:191], 0
v_exp_f32 v203, v203
v_mfma_f32_16x16x32_bf16 v[216:219], a[72:75], v[184:187], v[216:219]
v_cvt_pk_bf16_f32 v229, v202, v203
v_mfma_f32_16x16x32_bf16 v[216:219], a[68:71], v[180:183], v[216:219]
v_exp_f32 v196, v196
v_mfma_f32_16x16x32_bf16 v[216:219], a[64:67], v[176:179], v[216:219]
buffer_load_dwordx4 a[28:31], v255, s[52:55], s95 offen offset:3072 sc1
v_mfma_f32_16x16x32_bf16 v[212:215], a[80:83], v[160:163], 0
v_exp_f32 v197, v197
v_mfma_f32_16x16x32_bf16 v[212:215], a[84:87], v[164:167], v[212:215]
v_cvt_pk_bf16_f32 v226, v196, v197
s_add_u32 s17, s17, 8192
v_mfma_f32_16x16x32_bf16 v[212:215], a[88:91], v[168:171], v[212:215]
v_exp_f32 v198, v198
v_mfma_f32_16x16x32_bf16 v[212:215], a[92:95], v[172:175], v[212:215]
s_add_u32 m0, m0, 16384
s_and_b32 m0, m0, s78
buffer_load_dwordx4 a[32:35], v255, s[52:55], s96 offen offset:0 sc1
v_mfma_f32_16x16x32_bf16 v[220:223], a[92:95], v[188:191], 0
v_exp_f32 v199, v199
v_mfma_f32_16x16x32_bf16 v[220:223], a[88:91], v[184:187], v[220:223]
v_cvt_pk_bf16_f32 v227, v198, v199
v_mfma_f32_16x16x32_bf16 v[220:223], a[84:87], v[180:183], v[220:223]
v_exp_f32 v204, v204
v_mfma_f32_16x16x32_bf16 v[220:223], a[80:83], v[176:179], v[220:223]
s_waitcnt vmcnt(18)
s_barrier
v_mfma_f32_16x16x32_bf16 v[64:67], a[96:99], v[232:235], v[64:67]
v_exp_f32 v205, v205
v_mfma_f32_16x16x32_bf16 v[96:99], a[96:99], v[236:239], v[96:99]
ds_read_b128 a[96:99], v248 offset:8192
v_cvt_pk_bf16_f32 v230, v204, v205
v_mfma_f32_16x16x32_bf16 v[100:103], a[100:103], v[236:239], v[100:103]
v_exp_f32 v206, v206
v_mfma_f32_16x16x32_bf16 v[68:71], a[100:103], v[232:235], v[68:71]
ds_read_b128 a[100:103], v248 offset:9216
buffer_load_dwordx4 a[36:39], v255, s[52:55], s96 offen offset:1024 sc1
v_mfma_f32_16x16x32_bf16 v[72:75], a[104:107], v[232:235], v[72:75]
v_exp_f32 v207, v207
v_mfma_f32_16x16x32_bf16 v[104:107], a[104:107], v[236:239], v[104:107]
ds_read_b128 a[104:107], v248 offset:10240
v_cvt_pk_bf16_f32 v231, v206, v207
v_mfma_f32_16x16x32_bf16 v[108:111], a[108:111], v[236:239], v[108:111]
v_exp_f32 v208, v208
v_mfma_f32_16x16x32_bf16 v[76:79], a[108:111], v[232:235], v[76:79]
ds_read_b128 a[108:111], v248 offset:11264
buffer_load_dwordx4 a[40:43], v255, s[52:55], s96 offen offset:2048 sc1
v_mfma_f32_16x16x32_bf16 v[80:83], a[112:115], v[232:235], v[80:83]
v_exp_f32 v209, v209
v_mfma_f32_16x16x32_bf16 v[112:115], a[112:115], v[236:239], v[112:115]
ds_read_b128 a[112:115], v248 offset:12288
v_mfma_f32_16x16x32_bf16 v[116:119], a[116:119], v[236:239], v[116:119]
v_exp_f32 v210, v210
v_mfma_f32_16x16x32_bf16 v[84:87], a[116:119], v[232:235], v[84:87]
ds_read_b128 a[116:119], v248 offset:13312
buffer_load_dwordx4 a[44:47], v255, s[52:55], s96 offen offset:3072 sc1
v_mfma_f32_16x16x32_bf16 v[88:91], a[120:123], v[232:235], v[88:91]
v_exp_f32 v211, v211
v_mfma_f32_16x16x32_bf16 v[120:123], a[120:123], v[236:239], v[120:123]
ds_read_b128 a[120:123], v248 offset:14336
s_bitcmp1_b64 s[98:99], s85
v_mfma_f32_16x16x32_bf16 v[124:127], a[124:127], v[236:239], v[124:127]
v_exp_f32 v216, v216
v_mfma_f32_16x16x32_bf16 v[92:95], a[124:127], v[232:235], v[92:95]
ds_read_b128 a[124:127], v248 offset:15360
v_or_b32 v248, s89, v255
v_smfmac_f32_16x16x64_bf16 a[196:199], a[208:211], v[232:239], v241
s_waitcnt lgkmcnt(4)
v_smfmac_f32_16x16x64_bf16 a[192:195], a[208:211], v[224:231], v241
ds_read_b128 a[64:67], v248 offset:0
v_mfma_f32_16x16x32_bf16 v[0:3], a[96:99], v[224:227], v[0:3]
v_exp_f32 v217, v217
v_mfma_f32_16x16x32_bf16 v[32:35], a[96:99], v[228:231], v[32:35]
ds_read_b128 a[68:71], v248 offset:1024
v_cvt_pk_bf16_f32 v232, v208, v209
v_mfma_f32_16x16x32_bf16 v[36:39], a[100:103], v[228:231], v[36:39]
v_exp_f32 v218, v218
v_mfma_f32_16x16x32_bf16 v[4:7], a[100:103], v[224:227], v[4:7]
ds_read_b128 a[72:75], v248 offset:2048
v_cvt_pk_bf16_f32 v233, v210, v211
v_mfma_f32_16x16x32_bf16 v[8:11], a[104:107], v[224:227], v[8:11]
v_exp_f32 v219, v219
v_mfma_f32_16x16x32_bf16 v[40:43], a[104:107], v[228:231], v[40:43]
ds_read_b128 a[76:79], v248 offset:3072
v_cvt_pk_bf16_f32 v236, v216, v217
v_mfma_f32_16x16x32_bf16 v[44:47], a[108:111], v[228:231], v[44:47]
v_exp_f32 v212, v212
v_mfma_f32_16x16x32_bf16 v[12:15], a[108:111], v[224:227], v[12:15]
ds_read_b128 a[80:83], v248 offset:4096
s_waitcnt lgkmcnt(5)
v_mfma_f32_16x16x32_bf16 v[16:19], a[112:115], v[224:227], v[16:19]
v_exp_f32 v213, v213
v_mfma_f32_16x16x32_bf16 v[48:51], a[112:115], v[228:231], v[48:51]
ds_read_b128 a[84:87], v248 offset:5120
v_cvt_pk_bf16_f32 v237, v218, v219
v_mfma_f32_16x16x32_bf16 v[52:55], a[116:119], v[228:231], v[52:55]
v_exp_f32 v214, v214
v_mfma_f32_16x16x32_bf16 v[20:23], a[116:119], v[224:227], v[20:23]
ds_read_b128 a[88:91], v248 offset:6144
v_cvt_pk_bf16_f32 v234, v212, v213
v_mfma_f32_16x16x32_bf16 v[24:27], a[120:123], v[224:227], v[24:27]
v_exp_f32 v215, v215
v_mfma_f32_16x16x32_bf16 v[56:59], a[120:123], v[228:231], v[56:59]
ds_read_b128 a[92:95], v248 offset:7168
v_cvt_pk_bf16_f32 v235, v214, v215
v_mfma_f32_16x16x32_bf16 v[60:63], a[124:127], v[228:231], v[60:63]
v_exp_f32 v220, v220
v_mfma_f32_16x16x32_bf16 v[28:31], a[124:127], v[224:227], v[28:31]
buffer_load_dwordx4 a[48:51], v255, s[52:55], s97 offen offset:0 sc1
s_waitcnt lgkmcnt(4)
v_mfma_f32_16x16x32_bf16 v[192:195], a[64:67], v[128:131], 0
v_exp_f32 v221, v221
v_mfma_f32_16x16x32_bf16 v[192:195], a[68:71], v[132:135], v[192:195]
v_cvt_pk_bf16_f32 v238, v220, v221
s_add_u32 s85, s85, 1
v_mfma_f32_16x16x32_bf16 v[192:195], a[72:75], v[136:139], v[192:195]
v_exp_f32 v222, v222
v_mfma_f32_16x16x32_bf16 v[192:195], a[76:79], v[140:143], v[192:195]
buffer_load_dwordx4 v255, s[60:63], s17 offen offset:0 sc1 lds
v_mfma_f32_16x16x32_bf16 v[200:203], a[76:79], v[156:159], 0
v_exp_f32 v223, v223
v_mfma_f32_16x16x32_bf16 v[200:203], a[72:75], v[152:155], v[200:203]
v_cvt_pk_bf16_f32 v239, v222, v223
s_add_u32 s89, s89, 16384
v_mfma_f32_16x16x32_bf16 v[200:203], a[68:71], v[148:151], v[200:203]
v_exp_f32 v192, v192
v_mfma_f32_16x16x32_bf16 v[200:203], a[64:67], v[144:147], v[200:203]
s_waitcnt lgkmcnt(0)
s_and_b32 s89, s89, 0x1ffff
buffer_load_dwordx4 a[52:55], v255, s[52:55], s97 offen offset:1024 sc1
v_mfma_f32_16x16x32_bf16 v[196:199], a[80:83], v[128:131], 0
v_exp_f32 v193, v193
v_mfma_f32_16x16x32_bf16 v[196:199], a[84:87], v[132:135], v[196:199]
v_cvt_pk_bf16_f32 v224, v192, v193
v_mfma_f32_16x16x32_bf16 v[196:199], a[88:91], v[136:139], v[196:199]
v_exp_f32 v194, v194
v_mfma_f32_16x16x32_bf16 v[196:199], a[92:95], v[140:143], v[196:199]
buffer_load_dwordx4 v255, s[60:63], s17 offen offset:1024 sc1 lds
v_mfma_f32_16x16x32_bf16 v[204:207], a[92:95], v[156:159], 0
v_exp_f32 v195, v195
v_mfma_f32_16x16x32_bf16 v[204:207], a[88:91], v[152:155], v[204:207]
v_cvt_pk_bf16_f32 v225, v194, v195
v_mfma_f32_16x16x32_bf16 v[204:207], a[84:87], v[148:151], v[204:207]
v_exp_f32 v200, v200
v_mfma_f32_16x16x32_bf16 v[204:207], a[80:83], v[144:147], v[204:207]
buffer_load_dwordx4 v255, s[60:63], s17 offen offset:2048 sc1 lds
v_mfma_f32_16x16x32_bf16 v[208:211], a[64:67], v[160:163], 0
v_exp_f32 v201, v201
v_mfma_f32_16x16x32_bf16 v[208:211], a[68:71], v[164:167], v[208:211]
v_cvt_pk_bf16_f32 v228, v200, v201
v_mfma_f32_16x16x32_bf16 v[208:211], a[72:75], v[168:171], v[208:211]
v_exp_f32 v202, v202
v_mfma_f32_16x16x32_bf16 v[208:211], a[76:79], v[172:175], v[208:211]
buffer_load_dwordx4 v255, s[60:63], s17 offen offset:3072 sc1 lds
v_mfma_f32_16x16x32_bf16 v[216:219], a[76:79], v[188:191], 0
v_exp_f32 v203, v203
v_mfma_f32_16x16x32_bf16 v[216:219], a[72:75], v[184:187], v[216:219]
v_cvt_pk_bf16_f32 v229, v202, v203
v_mfma_f32_16x16x32_bf16 v[216:219], a[68:71], v[180:183], v[216:219]
v_exp_f32 v196, v196
v_mfma_f32_16x16x32_bf16 v[216:219], a[64:67], v[176:179], v[216:219]
buffer_load_dwordx4 a[56:59], v255, s[52:55], s97 offen offset:2048 sc1
v_mfma_f32_16x16x32_bf16 v[212:215], a[80:83], v[160:163], 0
v_exp_f32 v197, v197
v_mfma_f32_16x16x32_bf16 v[212:215], a[84:87], v[164:167], v[212:215]
v_cvt_pk_bf16_f32 v226, v196, v197
s_add_u32 s17, s17, 8192
v_mfma_f32_16x16x32_bf16 v[212:215], a[88:91], v[168:171], v[212:215]
v_exp_f32 v198, v198
v_mfma_f32_16x16x32_bf16 v[212:215], a[92:95], v[172:175], v[212:215]
s_add_u32 m0, m0, 16384
s_and_b32 m0, m0, s78
buffer_load_dwordx4 a[60:63], v255, s[52:55], s97 offen offset:3072 sc1
v_mfma_f32_16x16x32_bf16 v[220:223], a[92:95], v[188:191], 0
v_exp_f32 v199, v199
v_mfma_f32_16x16x32_bf16 v[220:223], a[88:91], v[184:187], v[220:223]
v_cvt_pk_bf16_f32 v227, v198, v199
v_mfma_f32_16x16x32_bf16 v[220:223], a[84:87], v[180:183], v[220:223]
v_exp_f32 v204, v204
v_mfma_f32_16x16x32_bf16 v[220:223], a[80:83], v[176:179], v[220:223]
s_waitcnt vmcnt(24)
s_barrier
v_mfma_f32_16x16x32_bf16 v[64:67], a[96:99], v[232:235], v[64:67]
v_exp_f32 v205, v205
v_mfma_f32_16x16x32_bf16 v[96:99], a[96:99], v[236:239], v[96:99]
ds_read_b128 a[96:99], v248 offset:8192
v_cvt_pk_bf16_f32 v230, v204, v205
v_mfma_f32_16x16x32_bf16 v[100:103], a[100:103], v[236:239], v[100:103]
v_exp_f32 v206, v206
v_mfma_f32_16x16x32_bf16 v[68:71], a[100:103], v[232:235], v[68:71]
ds_read_b128 a[100:103], v248 offset:9216
v_mfma_f32_16x16x32_bf16 v[72:75], a[104:107], v[232:235], v[72:75]
v_exp_f32 v207, v207
v_mfma_f32_16x16x32_bf16 v[104:107], a[104:107], v[236:239], v[104:107]
ds_read_b128 a[104:107], v248 offset:10240
v_cvt_pk_bf16_f32 v231, v206, v207
v_mfma_f32_16x16x32_bf16 v[108:111], a[108:111], v[236:239], v[108:111]
v_exp_f32 v208, v208
v_mfma_f32_16x16x32_bf16 v[76:79], a[108:111], v[232:235], v[76:79]
ds_read_b128 a[108:111], v248 offset:11264
v_mfma_f32_16x16x32_bf16 v[80:83], a[112:115], v[232:235], v[80:83]
v_exp_f32 v209, v209
v_mfma_f32_16x16x32_bf16 v[112:115], a[112:115], v[236:239], v[112:115]
ds_read_b128 a[112:115], v248 offset:12288
v_mfma_f32_16x16x32_bf16 v[116:119], a[116:119], v[236:239], v[116:119]
v_exp_f32 v210, v210
v_mfma_f32_16x16x32_bf16 v[84:87], a[116:119], v[232:235], v[84:87]
ds_read_b128 a[116:119], v248 offset:13312
v_mfma_f32_16x16x32_bf16 v[88:91], a[120:123], v[232:235], v[88:91]
v_exp_f32 v211, v211
v_mfma_f32_16x16x32_bf16 v[120:123], a[120:123], v[236:239], v[120:123]
ds_read_b128 a[120:123], v248 offset:14336
s_bitcmp1_b64 s[98:99], s85
v_mfma_f32_16x16x32_bf16 v[124:127], a[124:127], v[236:239], v[124:127]
v_exp_f32 v216, v216
v_mfma_f32_16x16x32_bf16 v[92:95], a[124:127], v[232:235], v[92:95]
ds_read_b128 a[124:127], v248 offset:15360
v_or_b32 v248, s89, v255
v_smfmac_f32_16x16x64_bf16 a[196:199], a[208:211], v[232:239], v241
s_waitcnt lgkmcnt(4)
v_smfmac_f32_16x16x64_bf16 a[192:195], a[208:211], v[224:231], v241
ds_read_b128 a[64:67], v248 offset:0
v_mfma_f32_16x16x32_bf16 v[0:3], a[96:99], v[224:227], v[0:3]
v_exp_f32 v217, v217
v_mfma_f32_16x16x32_bf16 v[32:35], a[96:99], v[228:231], v[32:35]
ds_read_b128 a[68:71], v248 offset:1024
v_cvt_pk_bf16_f32 v232, v208, v209
v_mfma_f32_16x16x32_bf16 v[36:39], a[100:103], v[228:231], v[36:39]
v_exp_f32 v218, v218
v_mfma_f32_16x16x32_bf16 v[4:7], a[100:103], v[224:227], v[4:7]
ds_read_b128 a[72:75], v248 offset:2048
v_cvt_pk_bf16_f32 v233, v210, v211
v_mfma_f32_16x16x32_bf16 v[8:11], a[104:107], v[224:227], v[8:11]
v_exp_f32 v219, v219
v_mfma_f32_16x16x32_bf16 v[40:43], a[104:107], v[228:231], v[40:43]
ds_read_b128 a[76:79], v248 offset:3072
v_cvt_pk_bf16_f32 v236, v216, v217
v_mfma_f32_16x16x32_bf16 v[44:47], a[108:111], v[228:231], v[44:47]
v_exp_f32 v212, v212
v_mfma_f32_16x16x32_bf16 v[12:15], a[108:111], v[224:227], v[12:15]
ds_read_b128 a[80:83], v248 offset:4096
s_waitcnt lgkmcnt(5)
v_mfma_f32_16x16x32_bf16 v[16:19], a[112:115], v[224:227], v[16:19]
v_exp_f32 v213, v213
v_mfma_f32_16x16x32_bf16 v[48:51], a[112:115], v[228:231], v[48:51]
ds_read_b128 a[84:87], v248 offset:5120
v_cvt_pk_bf16_f32 v237, v218, v219
v_mfma_f32_16x16x32_bf16 v[52:55], a[116:119], v[228:231], v[52:55]
v_exp_f32 v214, v214
v_mfma_f32_16x16x32_bf16 v[20:23], a[116:119], v[224:227], v[20:23]
ds_read_b128 a[88:91], v248 offset:6144
v_cvt_pk_bf16_f32 v234, v212, v213
v_mfma_f32_16x16x32_bf16 v[24:27], a[120:123], v[224:227], v[24:27]
v_exp_f32_e64 v215, v215
v_mfma_f32_16x16x32_bf16 v[56:59], a[120:123], v[228:231], v[56:59]
ds_read_b128 a[92:95], v248 offset:7168
v_cvt_pk_bf16_f32 v235, v214, v215
v_mfma_f32_16x16x32_bf16 v[60:63], a[124:127], v[228:231], v[60:63]
v_exp_f32_e64 v220, v220
v_mfma_f32_16x16x32_bf16 v[28:31], a[124:127], v[224:227], v[28:31]
.La5_LA_F1Y:
.La5_LW6_1:
s_cbranch_execz .La5_LW6_5
s_waitcnt lgkmcnt(4)
s_waitcnt vmcnt(0)
v_mfma_f32_16x16x32_bf16 v[192:195], a[64:67], a[0:3], 0
v_exp_f32 v221, v221
v_mfma_f32_16x16x32_bf16 v[192:195], a[68:71], a[4:7], v[192:195]
v_cvt_pk_bf16_f32 v238, v220, v221
s_add_u32 s89, s89, 16384
v_mfma_f32_16x16x32_bf16 v[192:195], a[72:75], a[8:11], v[192:195]
v_exp_f32 v222, v222
v_mfma_f32_16x16x32_bf16 v[192:195], a[76:79], a[12:15], v[192:195]
buffer_load_dwordx4 v255, s[60:63], s17 offen offset:0 sc1 lds
v_accvgpr_read_b32 v242, a192
v_accvgpr_read_b32 v240, a193
v_add_f32 v242, v242, v240
v_mfma_f32_16x16x32_bf16 v[200:203], a[76:79], a[28:31], 0
v_exp_f32 v223, v223
v_accvgpr_read_b32 v243, a194
v_accvgpr_read_b32 v240, a195
v_add_f32 v243, v243, v240
v_mfma_f32_16x16x32_bf16 v[200:203], a[72:75], a[24:27], v[200:203]
v_cvt_pk_bf16_f32 v239, v222, v223
s_and_b32 s89, s89, 0x1ffff
v_accvgpr_write_b32 a192, 0
v_accvgpr_write_b32 a193, 0
v_accvgpr_write_b32 a194, 0
v_mfma_f32_16x16x32_bf16 v[200:203], a[68:71], a[20:23], v[200:203]
v_exp_f32 v192, v192
v_accvgpr_write_b32 a195, 0
v_subrev_u32 v240, 562036736, v242
v_mfma_f32_16x16x32_bf16 v[200:203], a[64:67], a[16:19], v[200:203]
s_waitcnt lgkmcnt(0)
v_rcp_f32 v242, v242
v_cmp_le_u32 vcc, 1006632960, v240
v_subrev_u32 v240, 562036736, v243
v_mfma_f32_16x16x32_bf16 v[196:199], a[80:83], a[0:3], 0
v_exp_f32 v193, v193
s_mov_b64 s[92:93], vcc
v_cmp_le_u32 vcc, 1006632960, v240
v_mfma_f32_16x16x32_bf16 v[196:199], a[84:87], a[4:7], v[196:199]
v_cvt_pk_bf16_f32 v224, v192, v193
v_rcp_f32 v243, v243
v_mfma_f32_16x16x32_bf16 v[196:199], a[88:91], a[8:11], v[196:199]
v_exp_f32 v194, v194
s_or_b64 vcc, vcc, s[92:93]
s_cbranch_scc1 .La5_LA_SC1
.La5_LA_SR1:
v_mul_f32 v0, v0, v242
v_mfma_f32_16x16x32_bf16 v[196:199], a[92:95], a[12:15], v[196:199]
buffer_load_dwordx4 v255, s[60:63], s17 offen offset:1024 sc1 lds
v_mul_f32 v1, v1, v242
v_mul_f32 v2, v2, v242
v_mul_f32 v3, v3, v242
v_mfma_f32_16x16x32_bf16 v[204:207], a[92:95], a[28:31], 0
v_exp_f32 v195, v195
v_mul_f32 v4, v4, v242
v_mul_f32 v5, v5, v242
v_mul_f32 v6, v6, v242
v_mfma_f32_16x16x32_bf16 v[204:207], a[88:91], a[24:27], v[204:207]
v_cvt_pk_bf16_f32 v225, v194, v195
v_mul_f32 v7, v7, v242
v_cvt_pk_bf16_f32 v128, v0, v1
v_cvt_pk_bf16_f32 v129, v2, v3
v_cvt_pk_bf16_f32 v130, v4, v5
v_mfma_f32_16x16x32_bf16 v[204:207], a[84:87], a[20:23], v[204:207]
v_exp_f32 v200, v200
v_cvt_pk_bf16_f32 v131, v6, v7
v_mul_f32 v8, v8, v242
v_mul_f32 v9, v9, v242
v_mfma_f32_16x16x32_bf16 v[204:207], a[80:83], a[16:19], v[204:207]
buffer_load_dwordx4 v255, s[60:63], s17 offen offset:2048 sc1 lds
v_mul_f32 v10, v10, v242
v_mul_f32 v11, v11, v242
v_mul_f32 v12, v12, v242
v_mfma_f32_16x16x32_bf16 v[208:211], a[64:67], a[32:35], 0
v_exp_f32 v201, v201
v_mul_f32 v13, v13, v242
v_mul_f32 v14, v14, v242
v_mul_f32 v15, v15, v242
v_mfma_f32_16x16x32_bf16 v[208:211], a[68:71], a[36:39], v[208:211]
v_cvt_pk_bf16_f32 v228, v200, v201
v_cvt_pk_bf16_f32 v132, v8, v9
v_cvt_pk_bf16_f32 v133, v10, v11
v_cvt_pk_bf16_f32 v134, v12, v13
v_cvt_pk_bf16_f32 v135, v14, v15
v_mfma_f32_16x16x32_bf16 v[208:211], a[72:75], a[40:43], v[208:211]
v_exp_f32 v202, v202
v_mul_f32 v16, v16, v242
v_mul_f32 v17, v17, v242
v_mul_f32 v18, v18, v242
v_mfma_f32_16x16x32_bf16 v[208:211], a[76:79], a[44:47], v[208:211]
buffer_load_dwordx4 v255, s[60:63], s17 offen offset:3072 sc1 lds
v_mul_f32 v19, v19, v242
v_mul_f32 v20, v20, v242
v_mul_f32 v21, v21, v242
v_mfma_f32_16x16x32_bf16 v[216:219], a[76:79], a[60:63], 0
v_exp_f32 v203, v203
v_mul_f32 v22, v22, v242
v_mul_f32 v23, v23, v242
v_cvt_pk_bf16_f32 v136, v16, v17
v_mfma_f32_16x16x32_bf16 v[216:219], a[72:75], a[56:59], v[216:219]
v_cvt_pk_bf16_f32 v229, v202, v203
v_cvt_pk_bf16_f32 v137, v18, v19
v_cvt_pk_bf16_f32 v138, v20, v21
v_cvt_pk_bf16_f32 v139, v22, v23
v_mul_f32 v24, v24, v242
v_mfma_f32_16x16x32_bf16 v[216:219], a[68:71], a[52:55], v[216:219]
v_exp_f32 v196, v196
v_mul_f32 v25, v25, v242
v_mul_f32 v26, v26, v242
v_mul_f32 v27, v27, v242
v_mfma_f32_16x16x32_bf16 v[216:219], a[64:67], a[48:51], v[216:219]
v_mul_f32 v28, v28, v242
v_mul_f32 v29, v29, v242
v_mul_f32 v30, v30, v242
v_mul_f32 v31, v31, v242
v_cvt_pk_bf16_f32 v140, v24, v25
v_mfma_f32_16x16x32_bf16 v[212:215], a[80:83], a[32:35], 0
v_exp_f32 v197, v197
s_add_u32 s17, s17, 8192
v_cvt_pk_bf16_f32 v141, v26, v27
v_cvt_pk_bf16_f32 v142, v28, v29
v_mfma_f32_16x16x32_bf16 v[212:215], a[84:87], a[36:39], v[212:215]
v_cvt_pk_bf16_f32 v226, v196, v197
s_add_u32 m0, m0, 16384
s_and_b32 m0, m0, s78
v_cvt_pk_bf16_f32 v143, v30, v31
v_mul_f32 v32, v32, v243
v_mfma_f32_16x16x32_bf16 v[212:215], a[88:91], a[40:43], v[212:215]
v_exp_f32 v198, v198
v_mul_f32 v33, v33, v243
v_mul_f32 v34, v34, v243
v_mul_f32 v35, v35, v243
v_mfma_f32_16x16x32_bf16 v[212:215], a[92:95], a[44:47], v[212:215]
buffer_store_dwordx4 v[128:131], v244, s[44:47], s91 offen offset:0 sc1
v_mul_f32 v36, v36, v243
v_mul_f32 v37, v37, v243
v_mul_f32 v38, v38, v243
v_mfma_f32_16x16x32_bf16 v[220:223], a[92:95], a[60:63], 0
v_exp_f32 v199, v199
v_mul_f32 v39, v39, v243
v_cvt_pk_bf16_f32 v144, v32, v33
v_cvt_pk_bf16_f32 v145, v34, v35
v_mfma_f32_16x16x32_bf16 v[220:223], a[88:91], a[56:59], v[220:223]
v_cvt_pk_bf16_f32 v227, v198, v199
v_cvt_pk_bf16_f32 v146, v36, v37
v_cvt_pk_bf16_f32 v147, v38, v39
v_mul_f32 v40, v40, v243
v_mul_f32 v41, v41, v243
v_mfma_f32_16x16x32_bf16 v[220:223], a[84:87], a[52:55], v[220:223]
v_exp_f32 v204, v204
v_mul_f32 v42, v42, v243
v_mul_f32 v43, v43, v243
v_mul_f32 v44, v44, v243
v_mfma_f32_16x16x32_bf16 v[220:223], a[80:83], a[48:51], v[220:223]
s_nop 0
s_barrier
v_mul_f32 v45, v45, v243
v_mul_f32 v46, v46, v243
v_mul_f32 v47, v47, v243
v_mfma_f32_16x16x32_bf16 v[64:67], a[96:99], v[232:235], v[64:67]
v_exp_f32 v205, v205
v_cvt_pk_bf16_f32 v148, v40, v41
v_cvt_pk_bf16_f32 v149, v42, v43
v_cvt_pk_bf16_f32 v150, v44, v45
v_mfma_f32_16x16x32_bf16 v[96:99], a[96:99], v[236:239], v[96:99]
ds_read_b128 a[96:99], v248 offset:8192
v_cvt_pk_bf16_f32 v230, v204, v205
v_cvt_pk_bf16_f32 v151, v46, v47
v_mul_f32 v48, v48, v243
v_mul_f32 v49, v49, v243
v_mfma_f32_16x16x32_bf16 v[100:103], a[100:103], v[236:239], v[100:103]
v_exp_f32 v206, v206
v_mul_f32 v50, v50, v243
v_mul_f32 v51, v51, v243
v_mul_f32 v52, v52, v243
v_mfma_f32_16x16x32_bf16 v[68:71], a[100:103], v[232:235], v[68:71]
ds_read_b128 a[100:103], v248 offset:9216
buffer_store_dwordx4 v[132:135], v244, s[44:47], s91 offen offset:1024 sc1
v_mul_f32 v53, v53, v243
v_mul_f32 v54, v54, v243
v_mfma_f32_16x16x32_bf16 v[72:75], a[104:107], v[232:235], v[72:75]
v_exp_f32 v207, v207
v_mul_f32 v55, v55, v243
v_cvt_pk_bf16_f32 v152, v48, v49
v_cvt_pk_bf16_f32 v153, v50, v51
v_mfma_f32_16x16x32_bf16 v[104:107], a[104:107], v[236:239], v[104:107]
ds_read_b128 a[104:107], v248 offset:10240
v_cvt_pk_bf16_f32 v231, v206, v207
v_cvt_pk_bf16_f32 v154, v52, v53
v_cvt_pk_bf16_f32 v155, v54, v55
v_mul_f32 v56, v56, v243
v_mfma_f32_16x16x32_bf16 v[108:111], a[108:111], v[236:239], v[108:111]
v_exp_f32 v208, v208
v_mul_f32 v57, v57, v243
v_mul_f32 v58, v58, v243
v_mul_f32 v59, v59, v243
v_mfma_f32_16x16x32_bf16 v[76:79], a[108:111], v[232:235], v[76:79]
ds_read_b128 a[108:111], v248 offset:11264
v_mul_f32 v60, v60, v243
v_mul_f32 v61, v61, v243
v_mul_f32 v62, v62, v243
v_mul_f32 v63, v63, v243
v_mfma_f32_16x16x32_bf16 v[80:83], a[112:115], v[232:235], v[80:83]
v_exp_f32 v209, v209
v_cvt_pk_bf16_f32 v156, v56, v57
v_cvt_pk_bf16_f32 v157, v58, v59
v_cvt_pk_bf16_f32 v158, v60, v61
v_mfma_f32_16x16x32_bf16 v[112:115], a[112:115], v[236:239], v[112:115]
ds_read_b128 a[112:115], v248 offset:12288
buffer_store_dwordx4 v[136:139], v244, s[44:47], s91 offen offset:2048 sc1
.La5_LW6_2:
s_cbranch_execz .La5_LW6_6
v_cvt_pk_bf16_f32 v159, v62, v63
v_mfma_f32_16x16x32_bf16 v[116:119], a[116:119], v[236:239], v[116:119]
v_exp_f32 v210, v210
v_mfma_f32_16x16x32_bf16 v[84:87], a[116:119], v[232:235], v[84:87]
ds_read_b128 a[116:119], v248 offset:13312
v_mfma_f32_16x16x32_bf16 v[88:91], a[120:123], v[232:235], v[88:91]
v_exp_f32 v211, v211
v_mfma_f32_16x16x32_bf16 v[120:123], a[120:123], v[236:239], v[120:123]
ds_read_b128 a[120:123], v248 offset:14336
buffer_store_dwordx4 v[140:143], v244, s[44:47], s91 offen offset:3072 sc1
v_mfma_f32_16x16x32_bf16 v[124:127], a[124:127], v[236:239], v[124:127]
v_exp_f32 v216, v216
v_mfma_f32_16x16x32_bf16 v[92:95], a[124:127], v[232:235], v[92:95]
ds_read_b128 a[124:127], v248 offset:15360
v_or_b32 v248, s89, v255
v_smfmac_f32_16x16x64_bf16 a[196:199], a[208:211], v[232:239], v241
s_waitcnt lgkmcnt(4)
v_smfmac_f32_16x16x64_bf16 a[192:195], a[208:211], v[224:231], v241
ds_read_b128 a[64:67], v248 offset:0
v_mfma_f32_16x16x32_bf16 v[0:3], a[96:99], v[224:227], 0
v_exp_f32 v217, v217
v_mfma_f32_16x16x32_bf16 v[32:35], a[96:99], v[228:231], 0
ds_read_b128 a[68:71], v248 offset:1024
v_cvt_pk_bf16_f32 v232, v208, v209
v_accvgpr_read_b32 v242, a196
v_accvgpr_read_b32 v240, a197
v_add_f32 v242, v242, v240
v_mfma_f32_16x16x32_bf16 v[36:39], a[100:103], v[228:231], 0
v_exp_f32 v218, v218
v_accvgpr_read_b32 v243, a198
v_accvgpr_read_b32 v240, a199
v_add_f32 v243, v243, v240
v_mfma_f32_16x16x32_bf16 v[4:7], a[100:103], v[224:227], 0
ds_read_b128 a[72:75], v248 offset:2048
v_cvt_pk_bf16_f32 v233, v210, v211
buffer_store_dwordx4 v[144:147], v245, s[44:47], s91 offen offset:0 sc1
v_accvgpr_write_b32 a196, 0
v_mfma_f32_16x16x32_bf16 v[8:11], a[104:107], v[224:227], 0
v_exp_f32 v219, v219
v_accvgpr_write_b32 a197, 0
v_accvgpr_write_b32 a198, 0
v_accvgpr_write_b32 a199, 0
v_mfma_f32_16x16x32_bf16 v[40:43], a[104:107], v[228:231], 0
ds_read_b128 a[76:79], v248 offset:3072
v_cvt_pk_bf16_f32 v236, v216, v217
v_subrev_u32 v240, 562036736, v242
v_rcp_f32 v242, v242
v_mfma_f32_16x16x32_bf16 v[44:47], a[108:111], v[228:231], 0
v_exp_f32 v212, v212
v_cmp_le_u32 vcc, 1006632960, v240
v_subrev_u32 v240, 562036736, v243
s_mov_b64 s[92:93], vcc
v_mfma_f32_16x16x32_bf16 v[12:15], a[108:111], v[224:227], 0
ds_read_b128 a[80:83], v248 offset:4096
s_waitcnt lgkmcnt(5)
v_cmp_le_u32 vcc, 1006632960, v240
v_rcp_f32 v243, v243
v_mfma_f32_16x16x32_bf16 v[16:19], a[112:115], v[224:227], 0
v_exp_f32 v213, v213
s_or_b64 vcc, vcc, s[92:93]
s_cbranch_scc1 .La5_LA_SC2
.La5_LA_SR2:
v_mul_f32 v64, v64, v242
v_mfma_f32_16x16x32_bf16 v[48:51], a[112:115], v[228:231], 0
ds_read_b128 a[84:87], v248 offset:5120
v_cvt_pk_bf16_f32 v237, v218, v219
v_mul_f32 v65, v65, v242
v_mul_f32 v66, v66, v242
v_mul_f32 v67, v67, v242
v_mfma_f32_16x16x32_bf16 v[52:55], a[116:119], v[228:231], 0
v_exp_f32 v214, v214
v_mul_f32 v68, v68, v242
v_mul_f32 v69, v69, v242
v_mul_f32 v70, v70, v242
v_mfma_f32_16x16x32_bf16 v[20:23], a[116:119], v[224:227], 0
ds_read_b128 a[88:91], v248 offset:6144
v_cvt_pk_bf16_f32 v234, v212, v213
buffer_store_dwordx4 v[148:151], v245, s[44:47], s91 offen offset:1024 sc1
v_mul_f32 v71, v71, v242
v_mfma_f32_16x16x32_bf16 v[24:27], a[120:123], v[224:227], 0
v_exp_f32 v215, v215
v_cvt_pk_bf16_f32 v160, v64, v65
v_cvt_pk_bf16_f32 v161, v66, v67
v_cvt_pk_bf16_f32 v162, v68, v69
v_mfma_f32_16x16x32_bf16 v[56:59], a[120:123], v[228:231], 0
ds_read_b128 a[92:95], v248 offset:7168
v_cvt_pk_bf16_f32 v235, v214, v215
v_cvt_pk_bf16_f32 v163, v70, v71
v_mul_f32 v72, v72, v242
v_mul_f32 v73, v73, v242
v_mfma_f32_16x16x32_bf16 v[60:63], a[124:127], v[228:231], 0
v_exp_f32 v220, v220
v_mul_f32 v74, v74, v242
v_mul_f32 v75, v75, v242
v_mul_f32 v76, v76, v242
v_mfma_f32_16x16x32_bf16 v[28:31], a[124:127], v[224:227], 0
s_add_u32 s85, s85, 1
v_mul_f32 v77, v77, v242
v_mul_f32 v78, v78, v242
v_mul_f32 v79, v79, v242
v_cvt_pk_bf16_f32 v164, v72, v73
s_waitcnt lgkmcnt(4)
v_mfma_f32_16x16x32_bf16 v[192:195], a[64:67], a[0:3], 0
v_exp_f32 v221, v221
v_cvt_pk_bf16_f32 v165, v74, v75
v_cvt_pk_bf16_f32 v166, v76, v77
v_cvt_pk_bf16_f32 v167, v78, v79
v_mfma_f32_16x16x32_bf16 v[192:195], a[68:71], a[4:7], v[192:195]
v_cvt_pk_bf16_f32 v238, v220, v221
s_add_u32 s89, s89, 16384
v_mul_f32 v80, v80, v242
v_mul_f32 v81, v81, v242
v_mul_f32 v82, v82, v242
v_mfma_f32_16x16x32_bf16 v[192:195], a[72:75], a[8:11], v[192:195]
v_exp_f32 v222, v222
v_mul_f32 v83, v83, v242
v_mul_f32 v84, v84, v242
v_mul_f32 v85, v85, v242
v_mfma_f32_16x16x32_bf16 v[192:195], a[76:79], a[12:15], v[192:195]
buffer_load_dwordx4 v255, s[60:63], s17 offen offset:0 sc1 lds
v_mul_f32 v86, v86, v242
v_mul_f32 v87, v87, v242
v_cvt_pk_bf16_f32 v168, v80, v81
v_mfma_f32_16x16x32_bf16 v[200:203], a[76:79], a[28:31], 0
v_exp_f32 v223, v223
v_cvt_pk_bf16_f32 v169, v82, v83
v_cvt_pk_bf16_f32 v170, v84, v85
v_cvt_pk_bf16_f32 v171, v86, v87
v_mfma_f32_16x16x32_bf16 v[200:203], a[72:75], a[24:27], v[200:203]
v_cvt_pk_bf16_f32 v239, v222, v223
s_and_b32 s89, s89, 0x1ffff
v_mul_f32 v88, v88, v242
v_mul_f32 v89, v89, v242
v_mul_f32 v90, v90, v242
v_mfma_f32_16x16x32_bf16 v[200:203], a[68:71], a[20:23], v[200:203]
v_exp_f32 v192, v192
v_mul_f32 v91, v91, v242
v_mul_f32 v92, v92, v242
v_mul_f32 v93, v93, v242
v_mfma_f32_16x16x32_bf16 v[200:203], a[64:67], a[16:19], v[200:203]
s_waitcnt lgkmcnt(0)
buffer_store_dwordx4 v[152:155], v245, s[44:47], s91 offen offset:2048 sc1
v_mul_f32 v94, v94, v242
v_mul_f32 v95, v95, v242
v_mfma_f32_16x16x32_bf16 v[196:199], a[80:83], a[0:3], 0
v_exp_f32 v193, v193
v_cvt_pk_bf16_f32 v172, v88, v89
v_cvt_pk_bf16_f32 v173, v90, v91
v_cvt_pk_bf16_f32 v174, v92, v93
v_mfma_f32_16x16x32_bf16 v[196:199], a[84:87], a[4:7], v[196:199]
v_cvt_pk_bf16_f32 v224, v192, v193
v_cvt_pk_bf16_f32 v175, v94, v95
v_mul_f32 v96, v96, v243
v_mul_f32 v97, v97, v243
v_mul_f32 v98, v98, v243
v_mfma_f32_16x16x32_bf16 v[196:199], a[88:91], a[8:11], v[196:199]
v_exp_f32 v194, v194
v_mul_f32 v99, v99, v243
v_mul_f32 v100, v100, v243
v_mul_f32 v101, v101, v243
v_mfma_f32_16x16x32_bf16 v[196:199], a[92:95], a[12:15], v[196:199]
buffer_load_dwordx4 v255, s[60:63], s17 offen offset:1024 sc1 lds
v_mul_f32 v102, v102, v243
v_mul_f32 v103, v103, v243
v_cvt_pk_bf16_f32 v176, v96, v97
v_mfma_f32_16x16x32_bf16 v[204:207], a[92:95], a[28:31], 0
v_exp_f32 v195, v195
v_cvt_pk_bf16_f32 v177, v98, v99
v_cvt_pk_bf16_f32 v178, v100, v101
v_cvt_pk_bf16_f32 v179, v102, v103
v_mfma_f32_16x16x32_bf16 v[204:207], a[88:91], a[24:27], v[204:207]
v_cvt_pk_bf16_f32 v225, v194, v195
buffer_store_dwordx4 v[156:159], v245, s[44:47], s91 offen offset:3072 sc1
v_mul_f32 v104, v104, v243
v_mul_f32 v105, v105, v243
v_mfma_f32_16x16x32_bf16 v[204:207], a[84:87], a[20:23], v[204:207]
v_exp_f32 v200, v200
v_mul_f32 v106, v106, v243
v_mul_f32 v107, v107, v243
v_mul_f32 v108, v108, v243
v_mfma_f32_16x16x32_bf16 v[204:207], a[80:83], a[16:19], v[204:207]
buffer_load_dwordx4 v255, s[60:63], s17 offen offset:2048 sc1 lds
v_mul_f32 v109, v109, v243
v_mul_f32 v110, v110, v243
v_mul_f32 v111, v111, v243
v_mfma_f32_16x16x32_bf16 v[208:211], a[64:67], a[32:35], 0
v_exp_f32 v201, v201
v_cvt_pk_bf16_f32 v180, v104, v105
v_cvt_pk_bf16_f32 v181, v106, v107
v_cvt_pk_bf16_f32 v182, v108, v109
v_mfma_f32_16x16x32_bf16 v[208:211], a[68:71], a[36:39], v[208:211]
v_cvt_pk_bf16_f32 v228, v200, v201
v_cvt_pk_bf16_f32 v183, v110, v111
v_mul_f32 v112, v112, v243
v_mul_f32 v113, v113, v243
v_mul_f32 v114, v114, v243
v_mfma_f32_16x16x32_bf16 v[208:211], a[72:75], a[40:43], v[208:211]
v_exp_f32 v202, v202
v_mul_f32 v115, v115, v243
v_mul_f32 v116, v116, v243
v_mul_f32 v117, v117, v243
v_mfma_f32_16x16x32_bf16 v[208:211], a[76:79], a[44:47], v[208:211]
buffer_load_dwordx4 v255, s[60:63], s17 offen offset:3072 sc1 lds
v_mul_f32 v118, v118, v243
v_mul_f32 v119, v119, v243
v_cvt_pk_bf16_f32 v184, v112, v113
v_mfma_f32_16x16x32_bf16 v[216:219], a[76:79], a[60:63], 0
v_exp_f32 v203, v203
v_cvt_pk_bf16_f32 v185, v114, v115
v_cvt_pk_bf16_f32 v186, v116, v117
v_cvt_pk_bf16_f32 v187, v118, v119
v_mfma_f32_16x16x32_bf16 v[216:219], a[72:75], a[56:59], v[216:219]
v_cvt_pk_bf16_f32 v229, v202, v203
v_mul_f32 v120, v120, v243
v_mul_f32 v121, v121, v243
v_mul_f32 v122, v122, v243
v_mul_f32 v123, v123, v243
v_mfma_f32_16x16x32_bf16 v[216:219], a[68:71], a[52:55], v[216:219]
v_exp_f32 v196, v196
v_mul_f32 v124, v124, v243
v_mul_f32 v125, v125, v243
v_mul_f32 v126, v126, v243
v_mfma_f32_16x16x32_bf16 v[216:219], a[64:67], a[48:51], v[216:219]
v_mul_f32 v127, v127, v243
v_cvt_pk_bf16_f32 v188, v120, v121
v_cvt_pk_bf16_f32 v189, v122, v123
v_cvt_pk_bf16_f32 v190, v124, v125
v_cvt_pk_bf16_f32 v191, v126, v127
v_mfma_f32_16x16x32_bf16 v[212:215], a[80:83], a[32:35], 0
v_exp_f32 v197, v197
s_add_u32 s17, s17, 8192
v_mfma_f32_16x16x32_bf16 v[212:215], a[84:87], a[36:39], v[212:215]
v_cvt_pk_bf16_f32 v226, v196, v197
s_add_u32 m0, m0, 16384
s_and_b32 m0, m0, s78
v_mfma_f32_16x16x32_bf16 v[212:215], a[88:91], a[40:43], v[212:215]
v_exp_f32 v198, v198
v_mfma_f32_16x16x32_bf16 v[212:215], a[92:95], a[44:47], v[212:215]
buffer_store_dwordx4 v[160:163], v246, s[44:47], s91 offen offset:0 sc1
v_mfma_f32_16x16x32_bf16 v[220:223], a[92:95], a[60:63], 0
v_exp_f32 v199, v199
v_mfma_f32_16x16x32_bf16 v[220:223], a[88:91], a[56:59], v[220:223]
v_cvt_pk_bf16_f32 v227, v198, v199
v_mfma_f32_16x16x32_bf16 v[220:223], a[84:87], a[52:55], v[220:223]
v_exp_f32 v204, v204
v_mfma_f32_16x16x32_bf16 v[220:223], a[80:83], a[48:51], v[220:223]
s_waitcnt vmcnt(18)
s_barrier
v_mfma_f32_16x16x32_bf16 v[64:67], a[96:99], v[232:235], 0
v_exp_f32 v205, v205
v_mfma_f32_16x16x32_bf16 v[96:99], a[96:99], v[236:239], 0
ds_read_b128 a[96:99], v248 offset:8192
v_cvt_pk_bf16_f32 v230, v204, v205
v_mfma_f32_16x16x32_bf16 v[100:103], a[100:103], v[236:239], 0
v_exp_f32 v206, v206
v_mfma_f32_16x16x32_bf16 v[68:71], a[100:103], v[232:235], 0
ds_read_b128 a[100:103], v248 offset:9216
buffer_store_dwordx4 v[164:167], v246, s[44:47], s91 offen offset:1024 sc1
v_mfma_f32_16x16x32_bf16 v[72:75], a[104:107], v[232:235], 0
v_exp_f32 v207, v207
v_mfma_f32_16x16x32_bf16 v[104:107], a[104:107], v[236:239], 0
ds_read_b128 a[104:107], v248 offset:10240
v_cvt_pk_bf16_f32 v231, v206, v207
v_mfma_f32_16x16x32_bf16 v[108:111], a[108:111], v[236:239], 0
v_exp_f32 v208, v208
v_mfma_f32_16x16x32_bf16 v[76:79], a[108:111], v[232:235], 0
ds_read_b128 a[108:111], v248 offset:11264
buffer_store_dwordx4 v[168:171], v246, s[44:47], s91 offen offset:2048 sc1
v_mfma_f32_16x16x32_bf16 v[80:83], a[112:115], v[232:235], 0
v_exp_f32 v209, v209
v_mfma_f32_16x16x32_bf16 v[112:115], a[112:115], v[236:239], 0
ds_read_b128 a[112:115], v248 offset:12288
v_mfma_f32_16x16x32_bf16 v[116:119], a[116:119], v[236:239], 0
v_exp_f32 v210, v210
v_mfma_f32_16x16x32_bf16 v[84:87], a[116:119], v[232:235], 0
ds_read_b128 a[116:119], v248 offset:13312
buffer_store_dwordx4 v[172:175], v246, s[44:47], s91 offen offset:3072 sc1
v_mfma_f32_16x16x32_bf16 v[88:91], a[120:123], v[232:235], 0
v_exp_f32 v211, v211
v_mfma_f32_16x16x32_bf16 v[120:123], a[120:123], v[236:239], 0
ds_read_b128 a[120:123], v248 offset:14336
v_mfma_f32_16x16x32_bf16 v[124:127], a[124:127], v[236:239], 0
v_exp_f32 v216, v216
v_mfma_f32_16x16x32_bf16 v[92:95], a[124:127], v[232:235], 0
ds_read_b128 a[124:127], v248 offset:15360
v_or_b32 v248, s89, v255
buffer_store_dwordx4 v[176:179], v247, s[44:47], s91 offen offset:0 sc1
v_smfmac_f32_16x16x64_bf16 a[196:199], a[208:211], v[232:239], v241
s_waitcnt lgkmcnt(4)
v_smfmac_f32_16x16x64_bf16 a[192:195], a[208:211], v[224:231], v241
ds_read_b128 a[64:67], v248 offset:0
v_mfma_f32_16x16x32_bf16 v[0:3], a[96:99], v[224:227], v[0:3]
v_exp_f32 v217, v217
v_mfma_f32_16x16x32_bf16 v[32:35], a[96:99], v[228:231], v[32:35]
ds_read_b128 a[68:71], v248 offset:1024
v_cvt_pk_bf16_f32 v232, v208, v209
v_mfma_f32_16x16x32_bf16 v[36:39], a[100:103], v[228:231], v[36:39]
v_exp_f32 v218, v218
v_mfma_f32_16x16x32_bf16 v[4:7], a[100:103], v[224:227], v[4:7]
ds_read_b128 a[72:75], v248 offset:2048
v_cvt_pk_bf16_f32 v233, v210, v211
buffer_store_dwordx4 v[180:183], v247, s[44:47], s91 offen offset:1024 sc1
v_mfma_f32_16x16x32_bf16 v[8:11], a[104:107], v[224:227], v[8:11]
v_exp_f32 v219, v219
v_mfma_f32_16x16x32_bf16 v[40:43], a[104:107], v[228:231], v[40:43]
ds_read_b128 a[76:79], v248 offset:3072
v_cvt_pk_bf16_f32 v236, v216, v217
v_mfma_f32_16x16x32_bf16 v[44:47], a[108:111], v[228:231], v[44:47]
v_exp_f32 v212, v212
v_mfma_f32_16x16x32_bf16 v[12:15], a[108:111], v[224:227], v[12:15]
ds_read_b128 a[80:83], v248 offset:4096
s_waitcnt lgkmcnt(5)
v_mfma_f32_16x16x32_bf16 v[16:19], a[112:115], v[224:227], v[16:19]
v_exp_f32 v213, v213
v_mfma_f32_16x16x32_bf16 v[48:51], a[112:115], v[228:231], v[48:51]
ds_read_b128 a[84:87], v248 offset:5120
v_cvt_pk_bf16_f32 v237, v218, v219
buffer_store_dwordx4 v[184:187], v247, s[44:47], s91 offen offset:2048 sc1
v_mfma_f32_16x16x32_bf16 v[52:55], a[116:119], v[228:231], v[52:55]
v_exp_f32 v214, v214
v_mfma_f32_16x16x32_bf16 v[20:23], a[116:119], v[224:227], v[20:23]
ds_read_b128 a[88:91], v248 offset:6144
v_cvt_pk_bf16_f32 v234, v212, v213
v_mfma_f32_16x16x32_bf16 v[24:27], a[120:123], v[224:227], v[24:27]
v_exp_f32 v215, v215
v_mfma_f32_16x16x32_bf16 v[56:59], a[120:123], v[228:231], v[56:59]
ds_read_b128 a[92:95], v248 offset:7168
v_cvt_pk_bf16_f32 v235, v214, v215
buffer_store_dwordx4 v[188:191], v247, s[44:47], s91 offen offset:3072 sc1
v_mfma_f32_16x16x32_bf16 v[60:63], a[124:127], v[228:231], v[60:63]
v_exp_f32 v220, v220
v_mfma_f32_16x16x32_bf16 v[28:31], a[124:127], v[224:227], v[28:31]
s_add_u32 s85, s85, 1
s_cmp_eq_u32 s85, 22
s_cbranch_scc1 .La5_LA_F3
.p2align 6
.La5_LA_F2:
s_waitcnt lgkmcnt(4)
v_mfma_f32_16x16x32_bf16 v[192:195], a[64:67], a[0:3], 0
v_exp_f32 v221, v221
v_mfma_f32_16x16x32_bf16 v[192:195], a[68:71], a[4:7], v[192:195]
v_cvt_pk_bf16_f32 v238, v220, v221
s_add_u32 s85, s85, 1
v_mfma_f32_16x16x32_bf16 v[192:195], a[72:75], a[8:11], v[192:195]
v_exp_f32 v222, v222
v_mfma_f32_16x16x32_bf16 v[192:195], a[76:79], a[12:15], v[192:195]
buffer_load_dwordx4 v255, s[60:63], s17 offen offset:0 sc1 lds
v_mfma_f32_16x16x32_bf16 v[200:203], a[76:79], a[28:31], 0
v_exp_f32 v223, v223
v_mfma_f32_16x16x32_bf16 v[200:203], a[72:75], a[24:27], v[200:203]
v_cvt_pk_bf16_f32 v239, v222, v223
s_add_u32 s89, s89, 16384
v_mfma_f32_16x16x32_bf16 v[200:203], a[68:71], a[20:23], v[200:203]
v_exp_f32 v192, v192
v_mfma_f32_16x16x32_bf16 v[200:203], a[64:67], a[16:19], v[200:203]
s_waitcnt lgkmcnt(0)
s_and_b32 s89, s89, 0x1ffff
v_mfma_f32_16x16x32_bf16 v[196:199], a[80:83], a[0:3], 0
v_exp_f32 v193, v193
v_mfma_f32_16x16x32_bf16 v[196:199], a[84:87], a[4:7], v[196:199]
v_cvt_pk_bf16_f32 v224, v192, v193
v_mfma_f32_16x16x32_bf16 v[196:199], a[88:91], a[8:11], v[196:199]
v_exp_f32 v194, v194
v_mfma_f32_16x16x32_bf16 v[196:199], a[92:95], a[12:15], v[196:199]
buffer_load_dwordx4 v255, s[60:63], s17 offen offset:1024 sc1 lds
v_mfma_f32_16x16x32_bf16 v[204:207], a[92:95], a[28:31], 0
v_exp_f32 v195, v195
v_mfma_f32_16x16x32_bf16 v[204:207], a[88:91], a[24:27], v[204:207]
v_cvt_pk_bf16_f32 v225, v194, v195
v_mfma_f32_16x16x32_bf16 v[204:207], a[84:87], a[20:23], v[204:207]
v_exp_f32 v200, v200
v_mfma_f32_16x16x32_bf16 v[204:207], a[80:83], a[16:19], v[204:207]
buffer_load_dwordx4 v255, s[60:63], s17 offen offset:2048 sc1 lds
v_mfma_f32_16x16x32_bf16 v[208:211], a[64:67], a[32:35], 0
v_exp_f32 v201, v201
v_mfma_f32_16x16x32_bf16 v[208:211], a[68:71], a[36:39], v[208:211]
v_cvt_pk_bf16_f32 v228, v200, v201
v_mfma_f32_16x16x32_bf16 v[208:211], a[72:75], a[40:43], v[208:211]
v_exp_f32 v202, v202
v_mfma_f32_16x16x32_bf16 v[208:211], a[76:79], a[44:47], v[208:211]
buffer_load_dwordx4 v255, s[60:63], s17 offen offset:3072 sc1 lds
v_mfma_f32_16x16x32_bf16 v[216:219], a[76:79], a[60:63], 0
v_exp_f32 v203, v203
v_mfma_f32_16x16x32_bf16 v[216:219], a[72:75], a[56:59], v[216:219]
v_cvt_pk_bf16_f32 v229, v202, v203
v_mfma_f32_16x16x32_bf16 v[216:219], a[68:71], a[52:55], v[216:219]
v_exp_f32 v196, v196
v_mfma_f32_16x16x32_bf16 v[216:219], a[64:67], a[48:51], v[216:219]
buffer_load_dword a253, v254, s[36:39], s90 offen
v_mfma_f32_16x16x32_bf16 v[212:215], a[80:83], a[32:35], 0
v_exp_f32 v197, v197
v_mfma_f32_16x16x32_bf16 v[212:215], a[84:87], a[36:39], v[212:215]
v_cvt_pk_bf16_f32 v226, v196, v197
s_add_u32 s17, s17, 8192
v_mfma_f32_16x16x32_bf16 v[212:215], a[88:91], a[40:43], v[212:215]
v_exp_f32 v198, v198
v_mfma_f32_16x16x32_bf16 v[212:215], a[92:95], a[44:47], v[212:215]
s_add_u32 m0, m0, 16384
s_and_b32 m0, m0, s78
v_mfma_f32_16x16x32_bf16 v[220:223], a[92:95], a[60:63], 0
v_exp_f32 v199, v199
v_mfma_f32_16x16x32_bf16 v[220:223], a[88:91], a[56:59], v[220:223]
v_cvt_pk_bf16_f32 v227, v198, v199
s_add_u32 s90, s90, s21
v_mfma_f32_16x16x32_bf16 v[220:223], a[84:87], a[52:55], v[220:223]
v_exp_f32 v204, v204
v_mfma_f32_16x16x32_bf16 v[220:223], a[80:83], a[48:51], v[220:223]
s_waitcnt vmcnt(11)
s_barrier
v_mfma_f32_16x16x32_bf16 v[64:67], a[96:99], v[232:235], v[64:67]
v_exp_f32 v205, v205
v_mfma_f32_16x16x32_bf16 v[96:99], a[96:99], v[236:239], v[96:99]
ds_read_b128 a[96:99], v248 offset:8192
v_cvt_pk_bf16_f32 v230, v204, v205
v_mfma_f32_16x16x32_bf16 v[100:103], a[100:103], v[236:239], v[100:103]
v_exp_f32 v206, v206
v_mfma_f32_16x16x32_bf16 v[68:71], a[100:103], v[232:235], v[68:71]
ds_read_b128 a[100:103], v248 offset:9216
v_mfma_f32_16x16x32_bf16 v[72:75], a[104:107], v[232:235], v[72:75]
v_exp_f32 v207, v207
v_mfma_f32_16x16x32_bf16 v[104:107], a[104:107], v[236:239], v[104:107]
ds_read_b128 a[104:107], v248 offset:10240
v_cvt_pk_bf16_f32 v231, v206, v207
v_mfma_f32_16x16x32_bf16 v[108:111], a[108:111], v[236:239], v[108:111]
v_exp_f32 v208, v208
v_mfma_f32_16x16x32_bf16 v[76:79], a[108:111], v[232:235], v[76:79]
ds_read_b128 a[108:111], v248 offset:11264
v_mfma_f32_16x16x32_bf16 v[80:83], a[112:115], v[232:235], v[80:83]
v_exp_f32 v209, v209
v_mfma_f32_16x16x32_bf16 v[112:115], a[112:115], v[236:239], v[112:115]
ds_read_b128 a[112:115], v248 offset:12288
v_mfma_f32_16x16x32_bf16 v[116:119], a[116:119], v[236:239], v[116:119]
v_exp_f32 v210, v210
v_mfma_f32_16x16x32_bf16 v[84:87], a[116:119], v[232:235], v[84:87]
ds_read_b128 a[116:119], v248 offset:13312
v_mfma_f32_16x16x32_bf16 v[88:91], a[120:123], v[232:235], v[88:91]
v_exp_f32 v211, v211
v_mfma_f32_16x16x32_bf16 v[120:123], a[120:123], v[236:239], v[120:123]
ds_read_b128 a[120:123], v248 offset:14336
s_bitcmp1_b64 s[98:99], s85
v_mfma_f32_16x16x32_bf16 v[124:127], a[124:127], v[236:239], v[124:127]
v_exp_f32 v216, v216
v_mfma_f32_16x16x32_bf16 v[92:95], a[124:127], v[232:235], v[92:95]
ds_read_b128 a[124:127], v248 offset:15360
v_or_b32 v248, s89, v255
v_smfmac_f32_16x16x64_bf16 a[196:199], a[208:211], v[232:239], v241
s_waitcnt lgkmcnt(4)
v_smfmac_f32_16x16x64_bf16 a[192:195], a[208:211], v[224:231], v241
ds_read_b128 a[64:67], v248 offset:0
v_mfma_f32_16x16x32_bf16 v[0:3], a[96:99], v[224:227], v[0:3]
v_exp_f32 v217, v217
v_mfma_f32_16x16x32_bf16 v[32:35], a[96:99], v[228:231], v[32:35]
ds_read_b128 a[68:71], v248 offset:1024
v_cvt_pk_bf16_f32 v232, v208, v209
v_mfma_f32_16x16x32_bf16 v[36:39], a[100:103], v[228:231], v[36:39]
v_exp_f32 v218, v218
v_mfma_f32_16x16x32_bf16 v[4:7], a[100:103], v[224:227], v[4:7]
ds_read_b128 a[72:75], v248 offset:2048
v_cvt_pk_bf16_f32 v233, v210, v211
v_mfma_f32_16x16x32_bf16 v[8:11], a[104:107], v[224:227], v[8:11]
v_exp_f32 v219, v219
v_mfma_f32_16x16x32_bf16 v[40:43], a[104:107], v[228:231], v[40:43]
ds_read_b128 a[76:79], v248 offset:3072
v_cvt_pk_bf16_f32 v236, v216, v217
v_mfma_f32_16x16x32_bf16 v[44:47], a[108:111], v[228:231], v[44:47]
v_exp_f32 v212, v212
v_mfma_f32_16x16x32_bf16 v[12:15], a[108:111], v[224:227], v[12:15]
ds_read_b128 a[80:83], v248 offset:4096
s_waitcnt lgkmcnt(5)
v_mfma_f32_16x16x32_bf16 v[16:19], a[112:115], v[224:227], v[16:19]
v_exp_f32 v213, v213
v_mfma_f32_16x16x32_bf16 v[48:51], a[112:115], v[228:231], v[48:51]
ds_read_b128 a[84:87], v248 offset:5120
v_cvt_pk_bf16_f32 v237, v218, v219
v_mfma_f32_16x16x32_bf16 v[52:55], a[116:119], v[228:231], v[52:55]
v_exp_f32 v214, v214
v_mfma_f32_16x16x32_bf16 v[20:23], a[116:119], v[224:227], v[20:23]
ds_read_b128 a[88:91], v248 offset:6144
v_cvt_pk_bf16_f32 v234, v212, v213
v_mfma_f32_16x16x32_bf16 v[24:27], a[120:123], v[224:227], v[24:27]
v_exp_f32 v215, v215
v_mfma_f32_16x16x32_bf16 v[56:59], a[120:123], v[228:231], v[56:59]
ds_read_b128 a[92:95], v248 offset:7168
v_cvt_pk_bf16_f32 v235, v214, v215
v_mfma_f32_16x16x32_bf16 v[60:63], a[124:127], v[228:231], v[60:63]
v_exp_f32 v220, v220
v_mfma_f32_16x16x32_bf16 v[28:31], a[124:127], v[224:227], v[28:31]
s_cbranch_scc1 .La5_LA_F2E2
s_waitcnt lgkmcnt(4)
v_mfma_f32_16x16x32_bf16 v[192:195], a[64:67], a[0:3], 0
v_exp_f32 v221, v221
v_mfma_f32_16x16x32_bf16 v[192:195], a[68:71], a[4:7], v[192:195]
v_cvt_pk_bf16_f32 v238, v220, v221
s_add_u32 s85, s85, 1
v_mfma_f32_16x16x32_bf16 v[192:195], a[72:75], a[8:11], v[192:195]
v_exp_f32 v222, v222
v_mfma_f32_16x16x32_bf16 v[192:195], a[76:79], a[12:15], v[192:195]
buffer_load_dwordx4 v255, s[60:63], s17 offen offset:0 sc1 lds
v_mfma_f32_16x16x32_bf16 v[200:203], a[76:79], a[28:31], 0
v_exp_f32 v223, v223
v_mfma_f32_16x16x32_bf16 v[200:203], a[72:75], a[24:27], v[200:203]
v_cvt_pk_bf16_f32 v239, v222, v223
s_add_u32 s89, s89, 16384
v_mfma_f32_16x16x32_bf16 v[200:203], a[68:71], a[20:23], v[200:203]
v_exp_f32 v192, v192
v_mfma_f32_16x16x32_bf16 v[200:203], a[64:67], a[16:19], v[200:203]
s_waitcnt lgkmcnt(0)
s_and_b32 s89, s89, 0x1ffff
v_mfma_f32_16x16x32_bf16 v[196:199], a[80:83], a[0:3], 0
v_exp_f32 v193, v193
v_mfma_f32_16x16x32_bf16 v[196:199], a[84:87], a[4:7], v[196:199]
v_cvt_pk_bf16_f32 v224, v192, v193
v_mfma_f32_16x16x32_bf16 v[196:199], a[88:91], a[8:11], v[196:199]
v_exp_f32 v194, v194
v_mfma_f32_16x16x32_bf16 v[196:199], a[92:95], a[12:15], v[196:199]
buffer_load_dwordx4 v255, s[60:63], s17 offen offset:1024 sc1 lds
v_mfma_f32_16x16x32_bf16 v[204:207], a[92:95], a[28:31], 0
v_exp_f32 v195, v195
v_mfma_f32_16x16x32_bf16 v[204:207], a[88:91], a[24:27], v[204:207]
v_cvt_pk_bf16_f32 v225, v194, v195
v_mfma_f32_16x16x32_bf16 v[204:207], a[84:87], a[20:23], v[204:207]
v_exp_f32 v200, v200
v_mfma_f32_16x16x32_bf16 v[204:207], a[80:83], a[16:19], v[204:207]
buffer_load_dwordx4 v255, s[60:63], s17 offen offset:2048 sc1 lds
v_mfma_f32_16x16x32_bf16 v[208:211], a[64:67], a[32:35], 0
v_exp_f32 v201, v201
v_mfma_f32_16x16x32_bf16 v[208:211], a[68:71], a[36:39], v[208:211]
v_cvt_pk_bf16_f32 v228, v200, v201
v_mfma_f32_16x16x32_bf16 v[208:211], a[72:75], a[40:43], v[208:211]
v_exp_f32 v202, v202
v_mfma_f32_16x16x32_bf16 v[208:211], a[76:79], a[44:47], v[208:211]
buffer_load_dwordx4 v255, s[60:63], s17 offen offset:3072 sc1 lds
v_mfma_f32_16x16x32_bf16 v[216:219], a[76:79], a[60:63], 0
v_exp_f32 v203, v203
v_mfma_f32_16x16x32_bf16 v[216:219], a[72:75], a[56:59], v[216:219]
v_cvt_pk_bf16_f32 v229, v202, v203
v_mfma_f32_16x16x32_bf16 v[216:219], a[68:71], a[52:55], v[216:219]
v_exp_f32 v196, v196
v_mfma_f32_16x16x32_bf16 v[216:219], a[64:67], a[48:51], v[216:219]
buffer_load_dword a253, v254, s[36:39], s90 offen
v_mfma_f32_16x16x32_bf16 v[212:215], a[80:83], a[32:35], 0
v_exp_f32 v197, v197
v_mfma_f32_16x16x32_bf16 v[212:215], a[84:87], a[36:39], v[212:215]
v_cvt_pk_bf16_f32 v226, v196, v197
s_add_u32 s17, s17, 8192
v_mfma_f32_16x16x32_bf16 v[212:215], a[88:91], a[40:43], v[212:215]
v_exp_f32 v198, v198
v_mfma_f32_16x16x32_bf16 v[212:215], a[92:95], a[44:47], v[212:215]
s_add_u32 m0, m0, 16384
s_and_b32 m0, m0, s78
v_mfma_f32_16x16x32_bf16 v[220:223], a[92:95], a[60:63], 0
v_exp_f32 v199, v199
v_mfma_f32_16x16x32_bf16 v[220:223], a[88:91], a[56:59], v[220:223]
v_cvt_pk_bf16_f32 v227, v198, v199
s_add_u32 s90, s90, s21
v_mfma_f32_16x16x32_bf16 v[220:223], a[84:87], a[52:55], v[220:223]
v_exp_f32 v204, v204
v_mfma_f32_16x16x32_bf16 v[220:223], a[80:83], a[48:51], v[220:223]
s_waitcnt vmcnt(11)
s_barrier
v_mfma_f32_16x16x32_bf16 v[64:67], a[96:99], v[232:235], v[64:67]
v_exp_f32 v205, v205
v_mfma_f32_16x16x32_bf16 v[96:99], a[96:99], v[236:239], v[96:99]
ds_read_b128 a[96:99], v248 offset:8192
v_cvt_pk_bf16_f32 v230, v204, v205
v_mfma_f32_16x16x32_bf16 v[100:103], a[100:103], v[236:239], v[100:103]
v_exp_f32 v206, v206
v_mfma_f32_16x16x32_bf16 v[68:71], a[100:103], v[232:235], v[68:71]
ds_read_b128 a[100:103], v248 offset:9216
v_mfma_f32_16x16x32_bf16 v[72:75], a[104:107], v[232:235], v[72:75]
v_exp_f32 v207, v207
v_mfma_f32_16x16x32_bf16 v[104:107], a[104:107], v[236:239], v[104:107]
ds_read_b128 a[104:107], v248 offset:10240
v_cvt_pk_bf16_f32 v231, v206, v207
v_mfma_f32_16x16x32_bf16 v[108:111], a[108:111], v[236:239], v[108:111]
v_exp_f32 v208, v208
v_mfma_f32_16x16x32_bf16 v[76:79], a[108:111], v[232:235], v[76:79]
ds_read_b128 a[108:111], v248 offset:11264
v_mfma_f32_16x16x32_bf16 v[80:83], a[112:115], v[232:235], v[80:83]
v_exp_f32 v209, v209
v_mfma_f32_16x16x32_bf16 v[112:115], a[112:115], v[236:239], v[112:115]
ds_read_b128 a[112:115], v248 offset:12288
v_mfma_f32_16x16x32_bf16 v[116:119], a[116:119], v[236:239], v[116:119]
v_exp_f32 v210, v210
v_mfma_f32_16x16x32_bf16 v[84:87], a[116:119], v[232:235], v[84:87]
ds_read_b128 a[116:119], v248 offset:13312
v_mfma_f32_16x16x32_bf16 v[88:91], a[120:123], v[232:235], v[88:91]
v_exp_f32 v211, v211
v_mfma_f32_16x16x32_bf16 v[120:123], a[120:123], v[236:239], v[120:123]
ds_read_b128 a[120:123], v248 offset:14336
s_bitcmp1_b64 s[98:99], s85
v_mfma_f32_16x16x32_bf16 v[124:127], a[124:127], v[236:239], v[124:127]
v_exp_f32 v216, v216
v_mfma_f32_16x16x32_bf16 v[92:95], a[124:127], v[232:235], v[92:95]
ds_read_b128 a[124:127], v248 offset:15360
v_or_b32 v248, s89, v255
v_smfmac_f32_16x16x64_bf16 a[196:199], a[208:211], v[232:239], v241
s_waitcnt lgkmcnt(4)
v_smfmac_f32_16x16x64_bf16 a[192:195], a[208:211], v[224:231], v241
ds_read_b128 a[64:67], v248 offset:0
v_mfma_f32_16x16x32_bf16 v[0:3], a[96:99], v[224:227], v[0:3]
v_exp_f32 v217, v217
v_mfma_f32_16x16x32_bf16 v[32:35], a[96:99], v[228:231], v[32:35]
ds_read_b128 a[68:71], v248 offset:1024
v_cvt_pk_bf16_f32 v232, v208, v209
v_mfma_f32_16x16x32_bf16 v[36:39], a[100:103], v[228:231], v[36:39]
v_exp_f32 v218, v218
v_mfma_f32_16x16x32_bf16 v[4:7], a[100:103], v[224:227], v[4:7]
ds_read_b128 a[72:75], v248 offset:2048
v_cvt_pk_bf16_f32 v233, v210, v211
v_mfma_f32_16x16x32_bf16 v[8:11], a[104:107], v[224:227], v[8:11]
v_exp_f32 v219, v219
v_mfma_f32_16x16x32_bf16 v[40:43], a[104:107], v[228:231], v[40:43]
ds_read_b128 a[76:79], v248 offset:3072
v_cvt_pk_bf16_f32 v236, v216, v217
v_mfma_f32_16x16x32_bf16 v[44:47], a[108:111], v[228:231], v[44:47]
v_exp_f32 v212, v212
v_mfma_f32_16x16x32_bf16 v[12:15], a[108:111], v[224:227], v[12:15]
.La5_LW6_3:
s_cbranch_execz .La5_LW6_7
ds_read_b128 a[80:83], v248 offset:4096
s_waitcnt lgkmcnt(5)
v_mfma_f32_16x16x32_bf16 v[16:19], a[112:115], v[224:227], v[16:19]
v_exp_f32 v213, v213
v_mfma_f32_16x16x32_bf16 v[48:51], a[112:115], v[228:231], v[48:51]
ds_read_b128 a[84:87], v248 offset:5120
v_cvt_pk_bf16_f32 v237, v218, v219
v_mfma_f32_16x16x32_bf16 v[52:55], a[116:119], v[228:231], v[52:55]
v_exp_f32 v214, v214
v_mfma_f32_16x16x32_bf16 v[20:23], a[116:119], v[224:227], v[20:23]
ds_read_b128 a[88:91], v248 offset:6144
v_cvt_pk_bf16_f32 v234, v212, v213
v_mfma_f32_16x16x32_bf16 v[24:27], a[120:123], v[224:227], v[24:27]
v_exp_f32 v215, v215
v_mfma_f32_16x16x32_bf16 v[56:59], a[120:123], v[228:231], v[56:59]
ds_read_b128 a[92:95], v248 offset:7168
v_cvt_pk_bf16_f32 v235, v214, v215
v_mfma_f32_16x16x32_bf16 v[60:63], a[124:127], v[228:231], v[60:63]
v_exp_f32 v220, v220
v_mfma_f32_16x16x32_bf16 v[28:31], a[124:127], v[224:227], v[28:31]
s_cbranch_scc0 .La5_LA_F2
.La5_LA_F2E2:
.La5_LA_F3:
.La5_LW6_4:
s_cbranch_execz .La5_LW6_R0
s_waitcnt lgkmcnt(4)
v_mfma_f32_16x16x32_bf16 v[192:195], a[64:67], a[0:3], 0
v_exp_f32 v221, v221
v_mfma_f32_16x16x32_bf16 v[192:195], a[68:71], a[4:7], v[192:195]
v_cvt_pk_bf16_f32 v238, v220, v221
s_add_u32 s89, s89, 16384
v_mfma_f32_16x16x32_bf16 v[192:195], a[72:75], a[8:11], v[192:195]
v_exp_f32 v222, v222
v_mfma_f32_16x16x32_bf16 v[192:195], a[76:79], a[12:15], v[192:195]
buffer_load_dwordx4 v255, s[60:63], s17 offen offset:0 sc1 lds
v_mfma_f32_16x16x32_bf16 v[200:203], a[76:79], a[28:31], 0
v_exp_f32 v223, v223
v_mfma_f32_16x16x32_bf16 v[200:203], a[72:75], a[24:27], v[200:203]
v_cvt_pk_bf16_f32 v239, v222, v223
s_and_b32 s89, s89, 0x1ffff
v_mfma_f32_16x16x32_bf16 v[200:203], a[68:71], a[20:23], v[200:203]
v_exp_f32 v192, v192
v_mfma_f32_16x16x32_bf16 v[200:203], a[64:67], a[16:19], v[200:203]
s_waitcnt lgkmcnt(0)
v_mfma_f32_16x16x32_bf16 v[196:199], a[80:83], a[0:3], 0
v_exp_f32 v193, v193
v_mfma_f32_16x16x32_bf16 v[196:199], a[84:87], a[4:7], v[196:199]
v_cvt_pk_bf16_f32 v224, v192, v193
v_accvgpr_read_b32 v254, a229
v_mfma_f32_16x16x32_bf16 v[196:199], a[88:91], a[8:11], v[196:199]
v_exp_f32 v194, v194
v_mfma_f32_16x16x32_bf16 v[196:199], a[92:95], a[12:15], v[196:199]
buffer_load_dwordx4 v255, s[60:63], s17 offen offset:1024 sc1 lds
v_mfma_f32_16x16x32_bf16 v[204:207], a[92:95], a[28:31], 0
v_exp_f32 v195, v195
v_mfma_f32_16x16x32_bf16 v[204:207], a[88:91], a[24:27], v[204:207]
v_cvt_pk_bf16_f32 v225, v194, v195
v_mfma_f32_16x16x32_bf16 v[204:207], a[84:87], a[20:23], v[204:207]
v_exp_f32 v200, v200
v_mfma_f32_16x16x32_bf16 v[204:207], a[80:83], a[16:19], v[204:207]
buffer_load_dwordx4 v255, s[60:63], s17 offen offset:2048 sc1 lds
v_mfma_f32_16x16x32_bf16 v[208:211], a[64:67], a[32:35], 0
v_exp_f32 v201, v201
v_mfma_f32_16x16x32_bf16 v[208:211], a[68:71], a[36:39], v[208:211]
v_cvt_pk_bf16_f32 v228, v200, v201
v_mfma_f32_16x16x32_bf16 v[208:211], a[72:75], a[40:43], v[208:211]
v_exp_f32 v202, v202
v_mfma_f32_16x16x32_bf16 v[208:211], a[76:79], a[44:47], v[208:211]
buffer_load_dwordx4 v255, s[60:63], s17 offen offset:3072 sc1 lds
v_mfma_f32_16x16x32_bf16 v[216:219], a[76:79], a[60:63], 0
v_exp_f32 v203, v203
v_mfma_f32_16x16x32_bf16 v[216:219], a[72:75], a[56:59], v[216:219]
v_cvt_pk_bf16_f32 v229, v202, v203
s_mov_b32 s93, 0
v_mfma_f32_16x16x32_bf16 v[216:219], a[68:71], a[52:55], v[216:219]
v_exp_f32 v196, v196
v_mfma_f32_16x16x32_bf16 v[216:219], a[64:67], a[48:51], v[216:219]
buffer_load_dwordx4 a[128:131], v254, s[40:43], s93 offen
v_mfma_f32_16x16x32_bf16 v[212:215], a[80:83], a[32:35], 0
v_exp_f32 v197, v197
s_add_u32 s17, s17, 8192
v_mfma_f32_16x16x32_bf16 v[212:215], a[84:87], a[36:39], v[212:215]
v_cvt_pk_bf16_f32 v226, v196, v197
s_mov_b32 s93, 7168
s_add_u32 m0, m0, 16384
v_mfma_f32_16x16x32_bf16 v[212:215], a[88:91], a[40:43], v[212:215]
v_exp_f32 v198, v198
s_and_b32 m0, m0, s78
v_mfma_f32_16x16x32_bf16 v[212:215], a[92:95], a[44:47], v[212:215]
buffer_load_dwordx4 a[132:135], v254, s[40:43], s93 offen
v_mfma_f32_16x16x32_bf16 v[220:223], a[92:95], a[60:63], 0
v_exp_f32 v199, v199
v_mfma_f32_16x16x32_bf16 v[220:223], a[88:91], a[56:59], v[220:223]
v_cvt_pk_bf16_f32 v227, v198, v199
v_mfma_f32_16x16x32_bf16 v[220:223], a[84:87], a[52:55], v[220:223]
v_exp_f32 v204, v204
v_mfma_f32_16x16x32_bf16 v[220:223], a[80:83], a[48:51], v[220:223]
s_waitcnt vmcnt(12)
s_barrier
v_mfma_f32_16x16x32_bf16 v[64:67], a[96:99], v[232:235], v[64:67]
v_exp_f32 v205, v205
v_mfma_f32_16x16x32_bf16 v[96:99], a[96:99], v[236:239], v[96:99]
ds_read_b128 a[96:99], v248 offset:8192
v_cvt_pk_bf16_f32 v230, v204, v205
v_mfma_f32_16x16x32_bf16 v[100:103], a[100:103], v[236:239], v[100:103]
v_exp_f32 v206, v206
v_mfma_f32_16x16x32_bf16 v[68:71], a[100:103], v[232:235], v[68:71]
ds_read_b128 a[100:103], v248 offset:9216
v_mfma_f32_16x16x32_bf16 v[72:75], a[104:107], v[232:235], v[72:75]
v_exp_f32 v207, v207
v_mfma_f32_16x16x32_bf16 v[104:107], a[104:107], v[236:239], v[104:107]
ds_read_b128 a[104:107], v248 offset:10240
v_cvt_pk_bf16_f32 v231, v206, v207
s_mov_b32 s93, 14336
v_mfma_f32_16x16x32_bf16 v[108:111], a[108:111], v[236:239], v[108:111]
v_exp_f32 v208, v208
v_mfma_f32_16x16x32_bf16 v[76:79], a[108:111], v[232:235], v[76:79]
ds_read_b128 a[108:111], v248 offset:11264
buffer_load_dwordx4 a[136:139], v254, s[40:43], s93 offen
v_mfma_f32_16x16x32_bf16 v[80:83], a[112:115], v[232:235], v[80:83]
v_exp_f32 v209, v209
v_mfma_f32_16x16x32_bf16 v[112:115], a[112:115], v[236:239], v[112:115]
ds_read_b128 a[112:115], v248 offset:12288
v_mfma_f32_16x16x32_bf16 v[116:119], a[116:119], v[236:239], v[116:119]
v_exp_f32 v210, v210
v_mfma_f32_16x16x32_bf16 v[84:87], a[116:119], v[232:235], v[84:87]
ds_read_b128 a[116:119], v248 offset:13312
v_mfma_f32_16x16x32_bf16 v[88:91], a[120:123], v[232:235], v[88:91]
v_exp_f32 v211, v211
v_mfma_f32_16x16x32_bf16 v[120:123], a[120:123], v[236:239], v[120:123]
ds_read_b128 a[120:123], v248 offset:14336
v_mfma_f32_16x16x32_bf16 v[124:127], a[124:127], v[236:239], v[124:127]
v_exp_f32 v216, v216
v_mfma_f32_16x16x32_bf16 v[92:95], a[124:127], v[232:235], v[92:95]
ds_read_b128 a[124:127], v248 offset:15360
v_or_b32 v248, s89, v255
v_smfmac_f32_16x16x64_bf16 a[196:199], a[208:211], v[232:239], v241
s_waitcnt lgkmcnt(4)
v_smfmac_f32_16x16x64_bf16 a[192:195], a[208:211], v[224:231], v241
ds_read_b128 a[64:67], v248 offset:0
v_mfma_f32_16x16x32_bf16 v[0:3], a[96:99], v[224:227], v[0:3]
v_exp_f32 v217, v217
v_mfma_f32_16x16x32_bf16 v[32:35], a[96:99], v[228:231], v[32:35]
ds_read_b128 a[68:71], v248 offset:1024
v_cvt_pk_bf16_f32 v232, v208, v209
v_mfma_f32_16x16x32_bf16 v[36:39], a[100:103], v[228:231], v[36:39]
v_exp_f32 v218, v218
v_mfma_f32_16x16x32_bf16 v[4:7], a[100:103], v[224:227], v[4:7]
ds_read_b128 a[72:75], v248 offset:2048
v_cvt_pk_bf16_f32 v233, v210, v211
v_mfma_f32_16x16x32_bf16 v[8:11], a[104:107], v[224:227], v[8:11]
v_exp_f32 v219, v219
v_mfma_f32_16x16x32_bf16 v[40:43], a[104:107], v[228:231], v[40:43]
ds_read_b128 a[76:79], v248 offset:3072
v_cvt_pk_bf16_f32 v236, v216, v217
v_mfma_f32_16x16x32_bf16 v[44:47], a[108:111], v[228:231], v[44:47]
v_exp_f32 v212, v212
v_mfma_f32_16x16x32_bf16 v[12:15], a[108:111], v[224:227], v[12:15]
ds_read_b128 a[80:83], v248 offset:4096
s_waitcnt lgkmcnt(5)
v_mfma_f32_16x16x32_bf16 v[16:19], a[112:115], v[224:227], v[16:19]
v_exp_f32 v213, v213
v_mfma_f32_16x16x32_bf16 v[48:51], a[112:115], v[228:231], v[48:51]
ds_read_b128 a[84:87], v248 offset:5120
v_cvt_pk_bf16_f32 v237, v218, v219
v_mfma_f32_16x16x32_bf16 v[52:55], a[116:119], v[228:231], v[52:55]
v_exp_f32 v214, v214
v_mfma_f32_16x16x32_bf16 v[20:23], a[116:119], v[224:227], v[20:23]
ds_read_b128 a[88:91], v248 offset:6144
v_cvt_pk_bf16_f32 v234, v212, v213
v_mfma_f32_16x16x32_bf16 v[24:27], a[120:123], v[224:227], v[24:27]
v_exp_f32 v215, v215
v_mfma_f32_16x16x32_bf16 v[56:59], a[120:123], v[228:231], v[56:59]
ds_read_b128 a[92:95], v248 offset:7168
v_cvt_pk_bf16_f32 v235, v214, v215
v_mfma_f32_16x16x32_bf16 v[60:63], a[124:127], v[228:231], v[60:63]
v_exp_f32 v220, v220
v_mfma_f32_16x16x32_bf16 v[28:31], a[124:127], v[224:227], v[28:31]
s_waitcnt lgkmcnt(4)
v_mfma_f32_16x16x32_bf16 v[192:195], a[64:67], a[0:3], 0
v_exp_f32 v221, v221
v_mfma_f32_16x16x32_bf16 v[192:195], a[68:71], a[4:7], v[192:195]
v_cvt_pk_bf16_f32 v238, v220, v221
s_add_u32 s89, s89, 16384
v_mfma_f32_16x16x32_bf16 v[192:195], a[72:75], a[8:11], v[192:195]
v_exp_f32 v222, v222
v_mfma_f32_16x16x32_bf16 v[192:195], a[76:79], a[12:15], v[192:195]
buffer_load_dwordx4 v255, s[60:63], s17 offen offset:0 sc1 lds
v_mfma_f32_16x16x32_bf16 v[200:203], a[76:79], a[28:31], 0
v_exp_f32 v223, v223
v_mfma_f32_16x16x32_bf16 v[200:203], a[72:75], a[24:27], v[200:203]
v_cvt_pk_bf16_f32 v239, v222, v223
s_and_b32 s89, s89, 0x1ffff
v_mfma_f32_16x16x32_bf16 v[200:203], a[68:71], a[20:23], v[200:203]
v_exp_f32 v192, v192
v_mfma_f32_16x16x32_bf16 v[200:203], a[64:67], a[16:19], v[200:203]
s_waitcnt lgkmcnt(0)
v_mfma_f32_16x16x32_bf16 v[196:199], a[80:83], a[0:3], 0
v_exp_f32 v193, v193
v_mfma_f32_16x16x32_bf16 v[196:199], a[84:87], a[4:7], v[196:199]
v_cvt_pk_bf16_f32 v224, v192, v193
v_mfma_f32_16x16x32_bf16 v[196:199], a[88:91], a[8:11], v[196:199]
v_exp_f32 v194, v194
v_mfma_f32_16x16x32_bf16 v[196:199], a[92:95], a[12:15], v[196:199]
buffer_load_dwordx4 v255, s[60:63], s17 offen offset:1024 sc1 lds
v_mfma_f32_16x16x32_bf16 v[204:207], a[92:95], a[28:31], 0
v_exp_f32 v195, v195
v_mfma_f32_16x16x32_bf16 v[204:207], a[88:91], a[24:27], v[204:207]
v_cvt_pk_bf16_f32 v225, v194, v195
v_mfma_f32_16x16x32_bf16 v[204:207], a[84:87], a[20:23], v[204:207]
v_exp_f32 v200, v200
v_mfma_f32_16x16x32_bf16 v[204:207], a[80:83], a[16:19], v[204:207]
buffer_load_dwordx4 v255, s[60:63], s17 offen offset:2048 sc1 lds
v_mfma_f32_16x16x32_bf16 v[208:211], a[64:67], a[32:35], 0
v_exp_f32 v201, v201
v_mfma_f32_16x16x32_bf16 v[208:211], a[68:71], a[36:39], v[208:211]
v_cvt_pk_bf16_f32 v228, v200, v201
v_mfma_f32_16x16x32_bf16 v[208:211], a[72:75], a[40:43], v[208:211]
v_exp_f32 v202, v202
v_mfma_f32_16x16x32_bf16 v[208:211], a[76:79], a[44:47], v[208:211]
buffer_load_dwordx4 v255, s[60:63], s17 offen offset:3072 sc1 lds
v_mfma_f32_16x16x32_bf16 v[216:219], a[76:79], a[60:63], 0
v_exp_f32 v203, v203
v_mfma_f32_16x16x32_bf16 v[216:219], a[72:75], a[56:59], v[216:219]
v_cvt_pk_bf16_f32 v229, v202, v203
s_mov_b32 s93, 21504
v_mfma_f32_16x16x32_bf16 v[216:219], a[68:71], a[52:55], v[216:219]
v_exp_f32 v196, v196
v_mfma_f32_16x16x32_bf16 v[216:219], a[64:67], a[48:51], v[216:219]
buffer_load_dwordx4 a[140:143], v254, s[40:43], s93 offen
v_mfma_f32_16x16x32_bf16 v[212:215], a[80:83], a[32:35], 0
v_exp_f32 v197, v197
s_add_u32 s17, s17, 8192
v_mfma_f32_16x16x32_bf16 v[212:215], a[84:87], a[36:39], v[212:215]
v_cvt_pk_bf16_f32 v226, v196, v197
s_mov_b32 s93, 28672
s_add_u32 m0, m0, 16384
v_mfma_f32_16x16x32_bf16 v[212:215], a[88:91], a[40:43], v[212:215]
v_exp_f32 v198, v198
s_and_b32 m0, m0, s78
v_mfma_f32_16x16x32_bf16 v[212:215], a[92:95], a[44:47], v[212:215]
buffer_load_dwordx4 a[144:147], v254, s[40:43], s93 offen
v_mfma_f32_16x16x32_bf16 v[220:223], a[92:95], a[60:63], 0
v_exp_f32 v199, v199
v_mfma_f32_16x16x32_bf16 v[220:223], a[88:91], a[56:59], v[220:223]
v_cvt_pk_bf16_f32 v227, v198, v199
v_mfma_f32_16x16x32_bf16 v[220:223], a[84:87], a[52:55], v[220:223]
v_exp_f32 v204, v204
v_mfma_f32_16x16x32_bf16 v[220:223], a[80:83], a[48:51], v[220:223]
s_waitcnt vmcnt(14)
s_barrier
v_mfma_f32_16x16x32_bf16 v[64:67], a[96:99], v[232:235], v[64:67]
v_exp_f32 v205, v205
v_mfma_f32_16x16x32_bf16 v[96:99], a[96:99], v[236:239], v[96:99]
ds_read_b128 a[96:99], v248 offset:8192
v_cvt_pk_bf16_f32 v230, v204, v205
v_mfma_f32_16x16x32_bf16 v[100:103], a[100:103], v[236:239], v[100:103]
v_exp_f32 v206, v206
v_mfma_f32_16x16x32_bf16 v[68:71], a[100:103], v[232:235], v[68:71]
ds_read_b128 a[100:103], v248 offset:9216
v_mfma_f32_16x16x32_bf16 v[72:75], a[104:107], v[232:235], v[72:75]
v_exp_f32 v207, v207
v_mfma_f32_16x16x32_bf16 v[104:107], a[104:107], v[236:239], v[104:107]
ds_read_b128 a[104:107], v248 offset:10240
v_cvt_pk_bf16_f32 v231, v206, v207
s_mov_b32 s93, 35840
v_mfma_f32_16x16x32_bf16 v[108:111], a[108:111], v[236:239], v[108:111]
v_exp_f32 v208, v208
v_mfma_f32_16x16x32_bf16 v[76:79], a[108:111], v[232:235], v[76:79]
ds_read_b128 a[108:111], v248 offset:11264
buffer_load_dwordx4 a[148:151], v254, s[40:43], s93 offen
v_mfma_f32_16x16x32_bf16 v[80:83], a[112:115], v[232:235], v[80:83]
v_exp_f32 v209, v209
v_mfma_f32_16x16x32_bf16 v[112:115], a[112:115], v[236:239], v[112:115]
ds_read_b128 a[112:115], v248 offset:12288
v_mfma_f32_16x16x32_bf16 v[116:119], a[116:119], v[236:239], v[116:119]
v_exp_f32 v210, v210
v_mfma_f32_16x16x32_bf16 v[84:87], a[116:119], v[232:235], v[84:87]
ds_read_b128 a[116:119], v248 offset:13312
v_accvgpr_read_b32 v240, a231
s_waitcnt vmcnt(14)
v_mfma_f32_16x16x32_bf16 v[88:91], a[120:123], v[232:235], v[88:91]
v_exp_f32 v211, v211
v_mfma_f32_16x16x32_bf16 v[120:123], a[120:123], v[236:239], v[120:123]
ds_read_b128 a[120:123], v248 offset:14336
v_mfma_f32_16x16x32_bf16 v[124:127], a[124:127], v[236:239], v[124:127]
v_exp_f32 v216, v216
v_mfma_f32_16x16x32_bf16 v[92:95], a[124:127], v[232:235], v[92:95]
ds_read_b128 a[124:127], v248 offset:15360
v_or_b32 v248, s89, v255
v_smfmac_f32_16x16x64_bf16 a[196:199], a[208:211], v[232:239], v241
s_waitcnt lgkmcnt(4)
v_smfmac_f32_16x16x64_bf16 a[192:195], a[208:211], v[224:231], v241
ds_read_b128 a[64:67], v248 offset:0
v_mfma_f32_16x16x32_bf16 v[0:3], a[96:99], v[224:227], v[0:3]
v_exp_f32 v217, v217
v_mfma_f32_16x16x32_bf16 v[32:35], a[96:99], v[228:231], v[32:35]
ds_read_b128 a[68:71], v248 offset:1024
v_cvt_pk_bf16_f32 v232, v208, v209
v_mfma_f32_16x16x32_bf16 v[36:39], a[100:103], v[228:231], v[36:39]
v_exp_f32 v218, v218
v_mfma_f32_16x16x32_bf16 v[4:7], a[100:103], v[224:227], v[4:7]
ds_read_b128 a[72:75], v248 offset:2048
v_cvt_pk_bf16_f32 v233, v210, v211
v_mfma_f32_16x16x32_bf16 v[8:11], a[104:107], v[224:227], v[8:11]
v_exp_f32 v219, v219
v_mfma_f32_16x16x32_bf16 v[40:43], a[104:107], v[228:231], v[40:43]
ds_read_b128 a[76:79], v248 offset:3072
v_cvt_pk_bf16_f32 v236, v216, v217
v_mfma_f32_16x16x32_bf16 v[44:47], a[108:111], v[228:231], v[44:47]
v_exp_f32 v212, v212
v_mfma_f32_16x16x32_bf16 v[12:15], a[108:111], v[224:227], v[12:15]
ds_read_b128 a[80:83], v248 offset:4096
s_waitcnt lgkmcnt(5)
v_mfma_f32_16x16x32_bf16 v[16:19], a[112:115], v[224:227], v[16:19]
v_exp_f32 v213, v213
v_mfma_f32_16x16x32_bf16 v[48:51], a[112:115], v[228:231], v[48:51]
ds_read_b128 a[84:87], v248 offset:5120
v_cvt_pk_bf16_f32 v237, v218, v219
v_mfma_f32_16x16x32_bf16 v[52:55], a[116:119], v[228:231], v[52:55]
v_exp_f32 v214, v214
v_mfma_f32_16x16x32_bf16 v[20:23], a[116:119], v[224:227], v[20:23]
ds_read_b128 a[88:91], v248 offset:6144
v_cvt_pk_bf16_f32 v234, v212, v213
v_mfma_f32_16x16x32_bf16 v[24:27], a[120:123], v[224:227], v[24:27]
v_exp_f32 v215, v215
v_mfma_f32_16x16x32_bf16 v[56:59], a[120:123], v[228:231], v[56:59]
ds_read_b128 a[92:95], v248 offset:7168
v_cvt_pk_bf16_f32 v235, v214, v215
v_mfma_f32_16x16x32_bf16 v[60:63], a[124:127], v[228:231], v[60:63]
v_exp_f32 v220, v220
v_mfma_f32_16x16x32_bf16 v[28:31], a[124:127], v[224:227], v[28:31]
buffer_store_dwordx2 v[252:253], v240, s[64:67], s6 offen offset:0 sc1
s_waitcnt lgkmcnt(4)
v_mfma_f32_16x16x32_bf16 v[192:195], a[64:67], a[0:3], 0
v_exp_f32 v221, v221
v_mfma_f32_16x16x32_bf16 v[192:195], a[68:71], a[4:7], v[192:195]
v_cvt_pk_bf16_f32 v238, v220, v221
s_add_u32 s89, s89, 16384
v_mfma_f32_16x16x32_bf16 v[192:195], a[72:75], a[8:11], v[192:195]
v_exp_f32 v222, v222
v_mfma_f32_16x16x32_bf16 v[192:195], a[76:79], a[12:15], v[192:195]
buffer_load_dwordx4 v255, s[60:63], s17 offen offset:0 sc1 lds
v_mfma_f32_16x16x32_bf16 v[200:203], a[76:79], a[28:31], 0
v_exp_f32 v223, v223
v_mfma_f32_16x16x32_bf16 v[200:203], a[72:75], a[24:27], v[200:203]
v_cvt_pk_bf16_f32 v239, v222, v223
s_and_b32 s89, s89, 0x1ffff
v_mfma_f32_16x16x32_bf16 v[200:203], a[68:71], a[20:23], v[200:203]
v_exp_f32 v192, v192
v_mfma_f32_16x16x32_bf16 v[200:203], a[64:67], a[16:19], v[200:203]
s_waitcnt lgkmcnt(0)
v_mfma_f32_16x16x32_bf16 v[196:199], a[80:83], a[0:3], 0
v_exp_f32 v193, v193
v_mfma_f32_16x16x32_bf16 v[196:199], a[84:87], a[4:7], v[196:199]
v_cvt_pk_bf16_f32 v224, v192, v193
v_mfma_f32_16x16x32_bf16 v[196:199], a[88:91], a[8:11], v[196:199]
v_exp_f32 v194, v194
v_mfma_f32_16x16x32_bf16 v[196:199], a[92:95], a[12:15], v[196:199]
buffer_load_dwordx4 v255, s[60:63], s17 offen offset:1024 sc1 lds
v_mfma_f32_16x16x32_bf16 v[204:207], a[92:95], a[28:31], 0
v_exp_f32 v195, v195
v_mfma_f32_16x16x32_bf16 v[204:207], a[88:91], a[24:27], v[204:207]
v_cvt_pk_bf16_f32 v225, v194, v195
v_mfma_f32_16x16x32_bf16 v[204:207], a[84:87], a[20:23], v[204:207]
v_exp_f32 v200, v200
v_mfma_f32_16x16x32_bf16 v[204:207], a[80:83], a[16:19], v[204:207]
buffer_load_dwordx4 v255, s[60:63], s17 offen offset:2048 sc1 lds
v_mfma_f32_16x16x32_bf16 v[208:211], a[64:67], a[32:35], 0
v_exp_f32 v201, v201
v_mfma_f32_16x16x32_bf16 v[208:211], a[68:71], a[36:39], v[208:211]
v_cvt_pk_bf16_f32 v228, v200, v201
v_mfma_f32_16x16x32_bf16 v[208:211], a[72:75], a[40:43], v[208:211]
v_exp_f32 v202, v202
v_mfma_f32_16x16x32_bf16 v[208:211], a[76:79], a[44:47], v[208:211]
buffer_load_dwordx4 v255, s[60:63], s17 offen offset:3072 sc1 lds
v_mfma_f32_16x16x32_bf16 v[216:219], a[76:79], a[60:63], 0
v_exp_f32 v203, v203
v_mfma_f32_16x16x32_bf16 v[216:219], a[72:75], a[56:59], v[216:219]
v_cvt_pk_bf16_f32 v229, v202, v203
s_mov_b32 s93, 43008
v_mfma_f32_16x16x32_bf16 v[216:219], a[68:71], a[52:55], v[216:219]
v_exp_f32 v196, v196
v_mfma_f32_16x16x32_bf16 v[216:219], a[64:67], a[48:51], v[216:219]
buffer_load_dwordx4 a[152:155], v254, s[40:43], s93 offen
v_mfma_f32_16x16x32_bf16 v[212:215], a[80:83], a[32:35], 0
v_exp_f32 v197, v197
s_add_u32 s17, s17, 8192
v_mfma_f32_16x16x32_bf16 v[212:215], a[84:87], a[36:39], v[212:215]
v_cvt_pk_bf16_f32 v226, v196, v197
s_mov_b32 s93, 50176
s_add_u32 m0, m0, 16384
v_mfma_f32_16x16x32_bf16 v[212:215], a[88:91], a[40:43], v[212:215]
v_exp_f32 v198, v198
s_and_b32 m0, m0, s78
v_mfma_f32_16x16x32_bf16 v[212:215], a[92:95], a[44:47], v[212:215]
buffer_load_dwordx4 a[156:159], v254, s[40:43], s93 offen
v_mfma_f32_16x16x32_bf16 v[220:223], a[92:95], a[60:63], 0
v_exp_f32 v199, v199
v_mfma_f32_16x16x32_bf16 v[220:223], a[88:91], a[56:59], v[220:223]
v_cvt_pk_bf16_f32 v227, v198, v199
v_mfma_f32_16x16x32_bf16 v[220:223], a[84:87], a[52:55], v[220:223]
v_exp_f32 v204, v204
v_mfma_f32_16x16x32_bf16 v[220:223], a[80:83], a[48:51], v[220:223]
s_waitcnt vmcnt(17)
s_barrier
v_mfma_f32_16x16x32_bf16 v[64:67], a[96:99], v[232:235], v[64:67]
v_exp_f32 v205, v205
v_mfma_f32_16x16x32_bf16 v[96:99], a[96:99], v[236:239], v[96:99]
ds_read_b128 a[96:99], v248 offset:8192
v_cvt_pk_bf16_f32 v230, v204, v205
v_mfma_f32_16x16x32_bf16 v[100:103], a[100:103], v[236:239], v[100:103]
v_exp_f32 v206, v206
v_mfma_f32_16x16x32_bf16 v[68:71], a[100:103], v[232:235], v[68:71]
ds_read_b128 a[100:103], v248 offset:9216
v_mfma_f32_16x16x32_bf16 v[72:75], a[104:107], v[232:235], v[72:75]
v_exp_f32 v207, v207
v_mfma_f32_16x16x32_bf16 v[104:107], a[104:107], v[236:239], v[104:107]
ds_read_b128 a[104:107], v248 offset:10240
v_cvt_pk_bf16_f32 v231, v206, v207
s_mov_b32 s93, 57344
v_mfma_f32_16x16x32_bf16 v[108:111], a[108:111], v[236:239], v[108:111]
v_exp_f32 v208, v208
v_mfma_f32_16x16x32_bf16 v[76:79], a[108:111], v[232:235], v[76:79]
ds_read_b128 a[108:111], v248 offset:11264
buffer_load_dwordx4 a[160:163], v254, s[40:43], s93 offen
v_mfma_f32_16x16x32_bf16 v[80:83], a[112:115], v[232:235], v[80:83]
v_exp_f32 v209, v209
v_mfma_f32_16x16x32_bf16 v[112:115], a[112:115], v[236:239], v[112:115]
ds_read_b128 a[112:115], v248 offset:12288
v_mfma_f32_16x16x32_bf16 v[116:119], a[116:119], v[236:239], v[116:119]
v_exp_f32 v210, v210
v_mfma_f32_16x16x32_bf16 v[84:87], a[116:119], v[232:235], v[84:87]
ds_read_b128 a[116:119], v248 offset:13312
v_mfma_f32_16x16x32_bf16 v[88:91], a[120:123], v[232:235], v[88:91]
v_exp_f32 v211, v211
v_mfma_f32_16x16x32_bf16 v[120:123], a[120:123], v[236:239], v[120:123]
ds_read_b128 a[120:123], v248 offset:14336
v_mfma_f32_16x16x32_bf16 v[124:127], a[124:127], v[236:239], v[124:127]
v_exp_f32 v216, v216
v_mfma_f32_16x16x32_bf16 v[92:95], a[124:127], v[232:235], v[92:95]
ds_read_b128 a[124:127], v248 offset:15360
v_or_b32 v248, s89, v255
v_smfmac_f32_16x16x64_bf16 a[196:199], a[208:211], v[232:239], v241
s_waitcnt lgkmcnt(4)
v_smfmac_f32_16x16x64_bf16 a[192:195], a[208:211], v[224:231], v241
ds_read_b128 a[64:67], v248 offset:0
v_mfma_f32_16x16x32_bf16 v[0:3], a[96:99], v[224:227], v[0:3]
v_exp_f32 v217, v217
v_mfma_f32_16x16x32_bf16 v[32:35], a[96:99], v[228:231], v[32:35]
ds_read_b128 a[68:71], v248 offset:1024
v_cvt_pk_bf16_f32 v232, v208, v209
v_mfma_f32_16x16x32_bf16 v[36:39], a[100:103], v[228:231], v[36:39]
v_exp_f32 v218, v218
v_mfma_f32_16x16x32_bf16 v[4:7], a[100:103], v[224:227], v[4:7]
ds_read_b128 a[72:75], v248 offset:2048
v_cvt_pk_bf16_f32 v233, v210, v211
v_mfma_f32_16x16x32_bf16 v[8:11], a[104:107], v[224:227], v[8:11]
v_exp_f32 v219, v219
v_mfma_f32_16x16x32_bf16 v[40:43], a[104:107], v[228:231], v[40:43]
ds_read_b128 a[76:79], v248 offset:3072
v_cvt_pk_bf16_f32 v236, v216, v217
v_mfma_f32_16x16x32_bf16 v[44:47], a[108:111], v[228:231], v[44:47]
v_exp_f32 v212, v212
v_mfma_f32_16x16x32_bf16 v[12:15], a[108:111], v[224:227], v[12:15]
ds_read_b128 a[80:83], v248 offset:4096
s_waitcnt lgkmcnt(5)
v_mfma_f32_16x16x32_bf16 v[16:19], a[112:115], v[224:227], v[16:19]
v_exp_f32 v213, v213
v_mfma_f32_16x16x32_bf16 v[48:51], a[112:115], v[228:231], v[48:51]
ds_read_b128 a[84:87], v248 offset:5120
v_cvt_pk_bf16_f32 v237, v218, v219
v_mfma_f32_16x16x32_bf16 v[52:55], a[116:119], v[228:231], v[52:55]
v_exp_f32 v214, v214
v_mfma_f32_16x16x32_bf16 v[20:23], a[116:119], v[224:227], v[20:23]
ds_read_b128 a[88:91], v248 offset:6144
v_cvt_pk_bf16_f32 v234, v212, v213
v_mfma_f32_16x16x32_bf16 v[24:27], a[120:123], v[224:227], v[24:27]
v_exp_f32 v215, v215
v_mfma_f32_16x16x32_bf16 v[56:59], a[120:123], v[228:231], v[56:59]
ds_read_b128 a[92:95], v248 offset:7168
v_cvt_pk_bf16_f32 v235, v214, v215
v_mfma_f32_16x16x32_bf16 v[60:63], a[124:127], v[228:231], v[60:63]
v_exp_f32 v220, v220
v_mfma_f32_16x16x32_bf16 v[28:31], a[124:127], v[224:227], v[28:31]
s_waitcnt lgkmcnt(4)
v_mfma_f32_16x16x32_bf16 v[192:195], a[64:67], a[0:3], 0
v_exp_f32 v221, v221
v_mfma_f32_16x16x32_bf16 v[192:195], a[68:71], a[4:7], v[192:195]
v_cvt_pk_bf16_f32 v238, v220, v221
v_accvgpr_read_b32 v240, a228
v_mfma_f32_16x16x32_bf16 v[192:195], a[72:75], a[8:11], v[192:195]
v_exp_f32 v222, v222
v_mfma_f32_16x16x32_bf16 v[192:195], a[76:79], a[12:15], v[192:195]
s_add_u32 m0, s83, 16896
s_lshl_b32 s17, s72, 18
v_mfma_f32_16x16x32_bf16 v[200:203], a[76:79], a[28:31], 0
v_exp_f32 v223, v223
v_mfma_f32_16x16x32_bf16 v[200:203], a[72:75], a[24:27], v[200:203]
v_cvt_pk_bf16_f32 v239, v222, v223
s_add_u32 s89, s89, 16384
v_mfma_f32_16x16x32_bf16 v[200:203], a[68:71], a[20:23], v[200:203]
v_exp_f32 v192, v192
v_mfma_f32_16x16x32_bf16 v[200:203], a[64:67], a[16:19], v[200:203]
s_waitcnt lgkmcnt(0)
s_and_b32 s89, s89, 0x1ffff
v_mfma_f32_16x16x32_bf16 v[196:199], a[80:83], a[0:3], 0
v_exp_f32 v193, v193
v_mfma_f32_16x16x32_bf16 v[196:199], a[84:87], a[4:7], v[196:199]
v_cvt_pk_bf16_f32 v224, v192, v193
v_mfma_f32_16x16x32_bf16 v[196:199], a[88:91], a[8:11], v[196:199]
v_exp_f32 v194, v194
v_mfma_f32_16x16x32_bf16 v[196:199], a[92:95], a[12:15], v[196:199]
buffer_load_dwordx4 v240, s[56:59], s17 offen lds
v_mfma_f32_16x16x32_bf16 v[204:207], a[92:95], a[28:31], 0
v_exp_f32 v195, v195
s_add_u32 s17, s17, 32768
v_mfma_f32_16x16x32_bf16 v[204:207], a[88:91], a[24:27], v[204:207]
v_cvt_pk_bf16_f32 v225, v194, v195
s_add_u32 m0, m0, 1024
v_mfma_f32_16x16x32_bf16 v[204:207], a[84:87], a[20:23], v[204:207]
v_exp_f32 v200, v200
v_mfma_f32_16x16x32_bf16 v[204:207], a[80:83], a[16:19], v[204:207]
buffer_load_dwordx4 v240, s[56:59], s17 offen lds
v_mfma_f32_16x16x32_bf16 v[208:211], a[64:67], a[32:35], 0
v_exp_f32 v201, v201
s_add_u32 s17, s17, 4294942720
v_mfma_f32_16x16x32_bf16 v[208:211], a[68:71], a[36:39], v[208:211]
v_cvt_pk_bf16_f32 v228, v200, v201
s_add_u32 m0, m0, 1152
v_mfma_f32_16x16x32_bf16 v[208:211], a[72:75], a[40:43], v[208:211]
v_exp_f32 v202, v202
v_mfma_f32_16x16x32_bf16 v[208:211], a[76:79], a[44:47], v[208:211]
buffer_load_dwordx4 v240, s[56:59], s17 offen lds
v_mfma_f32_16x16x32_bf16 v[216:219], a[76:79], a[60:63], 0
v_exp_f32 v203, v203
s_add_u32 s17, s17, 32768
v_mfma_f32_16x16x32_bf16 v[216:219], a[72:75], a[56:59], v[216:219]
v_cvt_pk_bf16_f32 v229, v202, v203
s_add_u32 m0, m0, 1024
v_mfma_f32_16x16x32_bf16 v[216:219], a[68:71], a[52:55], v[216:219]
v_exp_f32 v196, v196
v_mfma_f32_16x16x32_bf16 v[216:219], a[64:67], a[48:51], v[216:219]
buffer_load_dwordx4 v240, s[56:59], s17 offen lds
v_mfma_f32_16x16x32_bf16 v[212:215], a[80:83], a[32:35], 0
v_exp_f32 v197, v197
s_add_u32 s17, s17, 90112
v_mfma_f32_16x16x32_bf16 v[212:215], a[84:87], a[36:39], v[212:215]
v_cvt_pk_bf16_f32 v226, v196, v197
s_mov_b32 s93, 64512
s_add_u32 m0, s83, 33792
v_mfma_f32_16x16x32_bf16 v[212:215], a[88:91], a[40:43], v[212:215]
v_exp_f32 v198, v198
v_mfma_f32_16x16x32_bf16 v[212:215], a[92:95], a[44:47], v[212:215]
buffer_load_dwordx4 a[164:167], v254, s[40:43], s93 offen
v_mfma_f32_16x16x32_bf16 v[220:223], a[92:95], a[60:63], 0
v_exp_f32 v199, v199
v_mfma_f32_16x16x32_bf16 v[220:223], a[88:91], a[56:59], v[220:223]
v_cvt_pk_bf16_f32 v227, v198, v199
v_mfma_f32_16x16x32_bf16 v[220:223], a[84:87], a[52:55], v[220:223]
v_exp_f32 v204, v204
v_mfma_f32_16x16x32_bf16 v[220:223], a[80:83], a[48:51], v[220:223]
s_waitcnt vmcnt(16)
s_barrier
v_mfma_f32_16x16x32_bf16 v[64:67], a[96:99], v[232:235], v[64:67]
v_exp_f32 v205, v205
v_mfma_f32_16x16x32_bf16 v[96:99], a[96:99], v[236:239], v[96:99]
ds_read_b128 a[96:99], v248 offset:8192
v_cvt_pk_bf16_f32 v230, v204, v205
v_mfma_f32_16x16x32_bf16 v[100:103], a[100:103], v[236:239], v[100:103]
v_exp_f32 v206, v206
v_mfma_f32_16x16x32_bf16 v[68:71], a[100:103], v[232:235], v[68:71]
ds_read_b128 a[100:103], v248 offset:9216
v_mfma_f32_16x16x32_bf16 v[72:75], a[104:107], v[232:235], v[72:75]
v_exp_f32 v207, v207
v_mfma_f32_16x16x32_bf16 v[104:107], a[104:107], v[236:239], v[104:107]
ds_read_b128 a[104:107], v248 offset:10240
v_cvt_pk_bf16_f32 v231, v206, v207
s_mov_b32 s93, 71680
v_mfma_f32_16x16x32_bf16 v[108:111], a[108:111], v[236:239], v[108:111]
v_exp_f32 v208, v208
v_mfma_f32_16x16x32_bf16 v[76:79], a[108:111], v[232:235], v[76:79]
ds_read_b128 a[108:111], v248 offset:11264
buffer_load_dwordx4 a[168:171], v254, s[40:43], s93 offen
v_mfma_f32_16x16x32_bf16 v[80:83], a[112:115], v[232:235], v[80:83]
v_exp_f32 v209, v209
v_mfma_f32_16x16x32_bf16 v[112:115], a[112:115], v[236:239], v[112:115]
ds_read_b128 a[112:115], v248 offset:12288
s_mov_b32 s93, 78848
v_mfma_f32_16x16x32_bf16 v[116:119], a[116:119], v[236:239], v[116:119]
v_exp_f32 v210, v210
v_mfma_f32_16x16x32_bf16 v[84:87], a[116:119], v[232:235], v[84:87]
ds_read_b128 a[116:119], v248 offset:13312
.La5_LW6_5:
s_cbranch_execz .La5_LW6_R0
buffer_load_dwordx4 a[172:175], v254, s[40:43], s93 offen
v_mfma_f32_16x16x32_bf16 v[88:91], a[120:123], v[232:235], v[88:91]
v_exp_f32 v211, v211
v_mfma_f32_16x16x32_bf16 v[120:123], a[120:123], v[236:239], v[120:123]
ds_read_b128 a[120:123], v248 offset:14336
v_mfma_f32_16x16x32_bf16 v[124:127], a[124:127], v[236:239], v[124:127]
v_exp_f32 v216, v216
v_mfma_f32_16x16x32_bf16 v[92:95], a[124:127], v[232:235], v[92:95]
ds_read_b128 a[124:127], v248 offset:15360
v_or_b32 v248, s89, v255
v_smfmac_f32_16x16x64_bf16 a[196:199], a[208:211], v[232:239], v241
s_waitcnt lgkmcnt(4)
v_smfmac_f32_16x16x64_bf16 a[192:195], a[208:211], v[224:231], v241
ds_read_b128 a[64:67], v248 offset:0
v_mfma_f32_16x16x32_bf16 v[0:3], a[96:99], v[224:227], v[0:3]
v_exp_f32 v217, v217
v_mfma_f32_16x16x32_bf16 v[32:35], a[96:99], v[228:231], v[32:35]
ds_read_b128 a[68:71], v248 offset:1024
v_cvt_pk_bf16_f32 v232, v208, v209
v_mfma_f32_16x16x32_bf16 v[36:39], a[100:103], v[228:231], v[36:39]
v_exp_f32 v218, v218
v_mfma_f32_16x16x32_bf16 v[4:7], a[100:103], v[224:227], v[4:7]
ds_read_b128 a[72:75], v248 offset:2048
v_cvt_pk_bf16_f32 v233, v210, v211
v_mfma_f32_16x16x32_bf16 v[8:11], a[104:107], v[224:227], v[8:11]
v_exp_f32 v219, v219
v_mfma_f32_16x16x32_bf16 v[40:43], a[104:107], v[228:231], v[40:43]
ds_read_b128 a[76:79], v248 offset:3072
v_cvt_pk_bf16_f32 v236, v216, v217
v_mfma_f32_16x16x32_bf16 v[44:47], a[108:111], v[228:231], v[44:47]
v_exp_f32 v212, v212
v_mfma_f32_16x16x32_bf16 v[12:15], a[108:111], v[224:227], v[12:15]
ds_read_b128 a[80:83], v248 offset:4096
s_waitcnt lgkmcnt(5)
v_mfma_f32_16x16x32_bf16 v[16:19], a[112:115], v[224:227], v[16:19]
v_exp_f32 v213, v213
v_mfma_f32_16x16x32_bf16 v[48:51], a[112:115], v[228:231], v[48:51]
ds_read_b128 a[84:87], v248 offset:5120
v_cvt_pk_bf16_f32 v237, v218, v219
v_mfma_f32_16x16x32_bf16 v[52:55], a[116:119], v[228:231], v[52:55]
v_exp_f32 v214, v214
v_mfma_f32_16x16x32_bf16 v[20:23], a[116:119], v[224:227], v[20:23]
ds_read_b128 a[88:91], v248 offset:6144
v_cvt_pk_bf16_f32 v234, v212, v213
v_mfma_f32_16x16x32_bf16 v[24:27], a[120:123], v[224:227], v[24:27]
v_exp_f32 v215, v215
v_mfma_f32_16x16x32_bf16 v[56:59], a[120:123], v[228:231], v[56:59]
ds_read_b128 a[92:95], v248 offset:7168
v_cvt_pk_bf16_f32 v235, v214, v215
v_mfma_f32_16x16x32_bf16 v[60:63], a[124:127], v[228:231], v[60:63]
v_exp_f32 v220, v220
v_mfma_f32_16x16x32_bf16 v[28:31], a[124:127], v[224:227], v[28:31]
s_waitcnt lgkmcnt(4)
v_mfma_f32_16x16x32_bf16 v[192:195], a[64:67], a[0:3], 0
v_exp_f32 v221, v221
v_mfma_f32_16x16x32_bf16 v[192:195], a[68:71], a[4:7], v[192:195]
v_cvt_pk_bf16_f32 v238, v220, v221
s_add_u32 s89, s89, 16384
v_mfma_f32_16x16x32_bf16 v[192:195], a[72:75], a[8:11], v[192:195]
v_exp_f32 v222, v222
v_mfma_f32_16x16x32_bf16 v[192:195], a[76:79], a[12:15], v[192:195]
buffer_load_dwordx4 v240, s[56:59], s17 offen lds
v_mfma_f32_16x16x32_bf16 v[200:203], a[76:79], a[28:31], 0
v_exp_f32 v223, v223
s_add_u32 s17, s17, 32768
v_mfma_f32_16x16x32_bf16 v[200:203], a[72:75], a[24:27], v[200:203]
v_cvt_pk_bf16_f32 v239, v222, v223
s_add_u32 m0, m0, 1024
v_mfma_f32_16x16x32_bf16 v[200:203], a[68:71], a[20:23], v[200:203]
v_exp_f32 v192, v192
v_mfma_f32_16x16x32_bf16 v[200:203], a[64:67], a[16:19], v[200:203]
s_waitcnt lgkmcnt(0)
s_and_b32 s89, s89, 0x1ffff
v_mfma_f32_16x16x32_bf16 v[196:199], a[80:83], a[0:3], 0
v_exp_f32 v193, v193
v_mfma_f32_16x16x32_bf16 v[196:199], a[84:87], a[4:7], v[196:199]
v_cvt_pk_bf16_f32 v224, v192, v193
v_mfma_f32_16x16x32_bf16 v[196:199], a[88:91], a[8:11], v[196:199]
v_exp_f32 v194, v194
v_mfma_f32_16x16x32_bf16 v[196:199], a[92:95], a[12:15], v[196:199]
buffer_load_dwordx4 v240, s[56:59], s17 offen lds
v_mfma_f32_16x16x32_bf16 v[204:207], a[92:95], a[28:31], 0
v_exp_f32 v195, v195
s_add_u32 s17, s17, 4294942720
v_mfma_f32_16x16x32_bf16 v[204:207], a[88:91], a[24:27], v[204:207]
v_cvt_pk_bf16_f32 v225, v194, v195
s_add_u32 m0, m0, 1152
v_mfma_f32_16x16x32_bf16 v[204:207], a[84:87], a[20:23], v[204:207]
v_exp_f32 v200, v200
v_mfma_f32_16x16x32_bf16 v[204:207], a[80:83], a[16:19], v[204:207]
buffer_load_dwordx4 v240, s[56:59], s17 offen lds
v_mfma_f32_16x16x32_bf16 v[208:211], a[64:67], a[32:35], 0
v_exp_f32 v201, v201
s_add_u32 s17, s17, 32768
v_mfma_f32_16x16x32_bf16 v[208:211], a[68:71], a[36:39], v[208:211]
v_cvt_pk_bf16_f32 v228, v200, v201
s_add_u32 m0, m0, 1024
v_mfma_f32_16x16x32_bf16 v[208:211], a[72:75], a[40:43], v[208:211]
v_exp_f32 v202, v202
v_mfma_f32_16x16x32_bf16 v[208:211], a[76:79], a[44:47], v[208:211]
buffer_load_dwordx4 v240, s[56:59], s17 offen lds
v_mfma_f32_16x16x32_bf16 v[216:219], a[76:79], a[60:63], 0
v_exp_f32 v203, v203
v_mfma_f32_16x16x32_bf16 v[216:219], a[72:75], a[56:59], v[216:219]
v_cvt_pk_bf16_f32 v229, v202, v203
s_mov_b32 s93, 86016
v_mfma_f32_16x16x32_bf16 v[216:219], a[68:71], a[52:55], v[216:219]
v_exp_f32 v196, v196
v_mfma_f32_16x16x32_bf16 v[216:219], a[64:67], a[48:51], v[216:219]
buffer_load_dwordx4 a[176:179], v254, s[40:43], s93 offen
v_mfma_f32_16x16x32_bf16 v[212:215], a[80:83], a[32:35], 0
v_exp_f32 v197, v197
s_bfe_u32 s17, s4, 262148
v_mfma_f32_16x16x32_bf16 v[212:215], a[84:87], a[36:39], v[212:215]
v_cvt_pk_bf16_f32 v226, v196, v197
s_mov_b32 s93, 93184
s_xor_b32 s17, s17, s72
v_mfma_f32_16x16x32_bf16 v[212:215], a[88:91], a[40:43], v[212:215]
v_exp_f32 v198, v198
s_lshl_b32 s17, s17, 18
v_mfma_f32_16x16x32_bf16 v[212:215], a[92:95], a[44:47], v[212:215]
buffer_load_dwordx4 a[180:183], v254, s[40:43], s93 offen
s_add_u32 m0, s83, 50688
v_mfma_f32_16x16x32_bf16 v[220:223], a[92:95], a[60:63], 0
v_exp_f32 v199, v199
v_mfma_f32_16x16x32_bf16 v[220:223], a[88:91], a[56:59], v[220:223]
v_cvt_pk_bf16_f32 v227, v198, v199
v_mfma_f32_16x16x32_bf16 v[220:223], a[84:87], a[52:55], v[220:223]
v_exp_f32 v204, v204
v_mfma_f32_16x16x32_bf16 v[220:223], a[80:83], a[48:51], v[220:223]
s_waitcnt vmcnt(16)
s_barrier
v_mfma_f32_16x16x32_bf16 v[64:67], a[96:99], v[232:235], v[64:67]
v_exp_f32 v205, v205
v_mfma_f32_16x16x32_bf16 v[96:99], a[96:99], v[236:239], v[96:99]
ds_read_b128 a[96:99], v248 offset:8192
v_cvt_pk_bf16_f32 v230, v204, v205
v_mfma_f32_16x16x32_bf16 v[100:103], a[100:103], v[236:239], v[100:103]
v_exp_f32 v206, v206
v_mfma_f32_16x16x32_bf16 v[68:71], a[100:103], v[232:235], v[68:71]
ds_read_b128 a[100:103], v248 offset:9216
v_mfma_f32_16x16x32_bf16 v[72:75], a[104:107], v[232:235], v[72:75]
v_exp_f32 v207, v207
v_mfma_f32_16x16x32_bf16 v[104:107], a[104:107], v[236:239], v[104:107]
ds_read_b128 a[104:107], v248 offset:10240
v_cvt_pk_bf16_f32 v231, v206, v207
s_mov_b32 s93, 100352
v_mfma_f32_16x16x32_bf16 v[108:111], a[108:111], v[236:239], v[108:111]
v_exp_f32 v208, v208
v_mfma_f32_16x16x32_bf16 v[76:79], a[108:111], v[232:235], v[76:79]
ds_read_b128 a[108:111], v248 offset:11264
buffer_load_dwordx4 a[184:187], v254, s[40:43], s93 offen
v_mfma_f32_16x16x32_bf16 v[80:83], a[112:115], v[232:235], v[80:83]
v_exp_f32 v209, v209
v_mfma_f32_16x16x32_bf16 v[112:115], a[112:115], v[236:239], v[112:115]
ds_read_b128 a[112:115], v248 offset:12288
v_mfma_f32_16x16x32_bf16 v[116:119], a[116:119], v[236:239], v[116:119]
v_exp_f32 v210, v210
v_mfma_f32_16x16x32_bf16 v[84:87], a[116:119], v[232:235], v[84:87]
ds_read_b128 a[116:119], v248 offset:13312
v_mfma_f32_16x16x32_bf16 v[88:91], a[120:123], v[232:235], v[88:91]
v_exp_f32 v211, v211
v_mfma_f32_16x16x32_bf16 v[120:123], a[120:123], v[236:239], v[120:123]
ds_read_b128 a[120:123], v248 offset:14336
v_mfma_f32_16x16x32_bf16 v[124:127], a[124:127], v[236:239], v[124:127]
v_exp_f32 v216, v216
v_mfma_f32_16x16x32_bf16 v[92:95], a[124:127], v[232:235], v[92:95]
ds_read_b128 a[124:127], v248 offset:15360
v_or_b32 v248, s89, v255
v_smfmac_f32_16x16x64_bf16 a[196:199], a[208:211], v[232:239], v241
s_waitcnt lgkmcnt(4)
v_smfmac_f32_16x16x64_bf16 a[192:195], a[208:211], v[224:231], v241
ds_read_b128 a[64:67], v248 offset:0
v_mfma_f32_16x16x32_bf16 v[0:3], a[96:99], v[224:227], v[0:3]
v_exp_f32 v217, v217
v_mfma_f32_16x16x32_bf16 v[32:35], a[96:99], v[228:231], v[32:35]
ds_read_b128 a[68:71], v248 offset:1024
v_cvt_pk_bf16_f32 v232, v208, v209
v_mfma_f32_16x16x32_bf16 v[36:39], a[100:103], v[228:231], v[36:39]
v_exp_f32 v218, v218
v_mfma_f32_16x16x32_bf16 v[4:7], a[100:103], v[224:227], v[4:7]
ds_read_b128 a[72:75], v248 offset:2048
v_cvt_pk_bf16_f32 v233, v210, v211
v_mfma_f32_16x16x32_bf16 v[8:11], a[104:107], v[224:227], v[8:11]
v_exp_f32 v219, v219
v_mfma_f32_16x16x32_bf16 v[40:43], a[104:107], v[228:231], v[40:43]
ds_read_b128 a[76:79], v248 offset:3072
v_cvt_pk_bf16_f32 v236, v216, v217
v_mfma_f32_16x16x32_bf16 v[44:47], a[108:111], v[228:231], v[44:47]
v_exp_f32 v212, v212
v_mfma_f32_16x16x32_bf16 v[12:15], a[108:111], v[224:227], v[12:15]
ds_read_b128 a[80:83], v248 offset:4096
s_waitcnt lgkmcnt(5)
v_mfma_f32_16x16x32_bf16 v[16:19], a[112:115], v[224:227], v[16:19]
v_exp_f32 v213, v213
v_mfma_f32_16x16x32_bf16 v[48:51], a[112:115], v[228:231], v[48:51]
ds_read_b128 a[84:87], v248 offset:5120
v_cvt_pk_bf16_f32 v237, v218, v219
v_mfma_f32_16x16x32_bf16 v[52:55], a[116:119], v[228:231], v[52:55]
v_exp_f32 v214, v214
v_mfma_f32_16x16x32_bf16 v[20:23], a[116:119], v[224:227], v[20:23]
ds_read_b128 a[88:91], v248 offset:6144
v_cvt_pk_bf16_f32 v234, v212, v213
v_mfma_f32_16x16x32_bf16 v[24:27], a[120:123], v[224:227], v[24:27]
v_exp_f32 v215, v215
v_mfma_f32_16x16x32_bf16 v[56:59], a[120:123], v[228:231], v[56:59]
ds_read_b128 a[92:95], v248 offset:7168
v_cvt_pk_bf16_f32 v235, v214, v215
v_mfma_f32_16x16x32_bf16 v[60:63], a[124:127], v[228:231], v[60:63]
v_exp_f32 v220, v220
v_mfma_f32_16x16x32_bf16 v[28:31], a[124:127], v[224:227], v[28:31]
s_waitcnt lgkmcnt(4)
v_mfma_f32_16x16x32_bf16 v[192:195], a[64:67], a[0:3], 0
v_exp_f32 v221, v221
v_mfma_f32_16x16x32_bf16 v[192:195], a[68:71], a[4:7], v[192:195]
v_cvt_pk_bf16_f32 v238, v220, v221
v_mfma_f32_16x16x32_bf16 v[192:195], a[72:75], a[8:11], v[192:195]
v_exp_f32 v222, v222
v_mfma_f32_16x16x32_bf16 v[192:195], a[76:79], a[12:15], v[192:195]
buffer_load_dwordx4 v240, s[56:59], s17 offen lds
v_mfma_f32_16x16x32_bf16 v[200:203], a[76:79], a[28:31], 0
v_exp_f32 v223, v223
s_add_u32 s17, s17, 32768
v_mfma_f32_16x16x32_bf16 v[200:203], a[72:75], a[24:27], v[200:203]
v_cvt_pk_bf16_f32 v239, v222, v223
s_add_u32 m0, m0, 1024
v_mfma_f32_16x16x32_bf16 v[200:203], a[68:71], a[20:23], v[200:203]
v_exp_f32 v192, v192
v_mfma_f32_16x16x32_bf16 v[200:203], a[64:67], a[16:19], v[200:203]
s_waitcnt lgkmcnt(0)
v_mfma_f32_16x16x32_bf16 v[196:199], a[80:83], a[0:3], 0
v_exp_f32 v193, v193
v_mfma_f32_16x16x32_bf16 v[196:199], a[84:87], a[4:7], v[196:199]
v_cvt_pk_bf16_f32 v224, v192, v193
v_mfma_f32_16x16x32_bf16 v[196:199], a[88:91], a[8:11], v[196:199]
v_exp_f32 v194, v194
v_mfma_f32_16x16x32_bf16 v[196:199], a[92:95], a[12:15], v[196:199]
buffer_load_dwordx4 v240, s[56:59], s17 offen lds
v_mfma_f32_16x16x32_bf16 v[204:207], a[92:95], a[28:31], 0
v_exp_f32 v195, v195
s_add_u32 s17, s17, 4294942720
v_mfma_f32_16x16x32_bf16 v[204:207], a[88:91], a[24:27], v[204:207]
v_cvt_pk_bf16_f32 v225, v194, v195
s_add_u32 m0, m0, 1152
v_mfma_f32_16x16x32_bf16 v[204:207], a[84:87], a[20:23], v[204:207]
v_exp_f32 v200, v200
v_mfma_f32_16x16x32_bf16 v[204:207], a[80:83], a[16:19], v[204:207]
buffer_load_dwordx4 v240, s[56:59], s17 offen lds
v_mfma_f32_16x16x32_bf16 v[208:211], a[64:67], a[32:35], 0
v_exp_f32 v201, v201
s_add_u32 s17, s17, 32768
v_mfma_f32_16x16x32_bf16 v[208:211], a[68:71], a[36:39], v[208:211]
v_cvt_pk_bf16_f32 v228, v200, v201
s_add_u32 m0, m0, 1024
v_mfma_f32_16x16x32_bf16 v[208:211], a[72:75], a[40:43], v[208:211]
v_exp_f32 v202, v202
v_mfma_f32_16x16x32_bf16 v[208:211], a[76:79], a[44:47], v[208:211]
buffer_load_dwordx4 v240, s[56:59], s17 offen lds
v_mfma_f32_16x16x32_bf16 v[216:219], a[76:79], a[60:63], 0
v_exp_f32 v203, v203
v_mfma_f32_16x16x32_bf16 v[216:219], a[72:75], a[56:59], v[216:219]
v_cvt_pk_bf16_f32 v229, v202, v203
s_mov_b32 s93, 107520
v_mfma_f32_16x16x32_bf16 v[216:219], a[68:71], a[52:55], v[216:219]
v_exp_f32 v196, v196
v_mfma_f32_16x16x32_bf16 v[216:219], a[64:67], a[48:51], v[216:219]
buffer_load_dwordx4 a[188:191], v254, s[40:43], s93 offen
v_mfma_f32_16x16x32_bf16 v[212:215], a[80:83], a[32:35], 0
v_exp_f32 v197, v197
v_mfma_f32_16x16x32_bf16 v[212:215], a[84:87], a[36:39], v[212:215]
v_cvt_pk_bf16_f32 v226, v196, v197
v_mfma_f32_16x16x32_bf16 v[212:215], a[88:91], a[40:43], v[212:215]
v_exp_f32 v198, v198
v_mfma_f32_16x16x32_bf16 v[212:215], a[92:95], a[44:47], v[212:215]
v_mfma_f32_16x16x32_bf16 v[220:223], a[92:95], a[60:63], 0
v_exp_f32 v199, v199
v_mfma_f32_16x16x32_bf16 v[220:223], a[88:91], a[56:59], v[220:223]
v_cvt_pk_bf16_f32 v227, v198, v199
v_mfma_f32_16x16x32_bf16 v[220:223], a[84:87], a[52:55], v[220:223]
v_exp_f32 v204, v204
v_mfma_f32_16x16x32_bf16 v[220:223], a[80:83], a[48:51], v[220:223]
v_mfma_f32_16x16x32_bf16 v[64:67], a[96:99], v[232:235], v[64:67]
v_exp_f32 v205, v205
v_mfma_f32_16x16x32_bf16 v[96:99], a[96:99], v[236:239], v[96:99]
ds_read_b128 a[96:99], v248 offset:8192
v_cvt_pk_bf16_f32 v230, v204, v205
v_mfma_f32_16x16x32_bf16 v[100:103], a[100:103], v[236:239], v[100:103]
v_exp_f32 v206, v206
v_mfma_f32_16x16x32_bf16 v[68:71], a[100:103], v[232:235], v[68:71]
ds_read_b128 a[100:103], v248 offset:9216
v_mfma_f32_16x16x32_bf16 v[72:75], a[104:107], v[232:235], v[72:75]
v_exp_f32 v207, v207
v_mfma_f32_16x16x32_bf16 v[104:107], a[104:107], v[236:239], v[104:107]
ds_read_b128 a[104:107], v248 offset:10240
v_cvt_pk_bf16_f32 v231, v206, v207
v_mfma_f32_16x16x32_bf16 v[108:111], a[108:111], v[236:239], v[108:111]
v_exp_f32 v208, v208
v_mfma_f32_16x16x32_bf16 v[76:79], a[108:111], v[232:235], v[76:79]
ds_read_b128 a[108:111], v248 offset:11264
v_mfma_f32_16x16x32_bf16 v[80:83], a[112:115], v[232:235], v[80:83]
v_exp_f32 v209, v209
v_mfma_f32_16x16x32_bf16 v[112:115], a[112:115], v[236:239], v[112:115]
ds_read_b128 a[112:115], v248 offset:12288
v_mfma_f32_16x16x32_bf16 v[116:119], a[116:119], v[236:239], v[116:119]
v_exp_f32 v210, v210
v_mfma_f32_16x16x32_bf16 v[84:87], a[116:119], v[232:235], v[84:87]
ds_read_b128 a[116:119], v248 offset:13312
v_mfma_f32_16x16x32_bf16 v[88:91], a[120:123], v[232:235], v[88:91]
v_exp_f32 v211, v211
v_mfma_f32_16x16x32_bf16 v[120:123], a[120:123], v[236:239], v[120:123]
ds_read_b128 a[120:123], v248 offset:14336
v_mfma_f32_16x16x32_bf16 v[124:127], a[124:127], v[236:239], v[124:127]
v_exp_f32 v216, v216
v_mfma_f32_16x16x32_bf16 v[92:95], a[124:127], v[232:235], v[92:95]
ds_read_b128 a[124:127], v248 offset:15360
v_smfmac_f32_16x16x64_bf16 a[196:199], a[208:211], v[232:239], v241
s_waitcnt lgkmcnt(4)
v_smfmac_f32_16x16x64_bf16 a[192:195], a[208:211], v[224:231], v241
v_mfma_f32_16x16x32_bf16 v[0:3], a[96:99], v[224:227], v[0:3]
v_exp_f32 v217, v217
v_mfma_f32_16x16x32_bf16 v[32:35], a[96:99], v[228:231], v[32:35]
v_cvt_pk_bf16_f32 v232, v208, v209
v_mfma_f32_16x16x32_bf16 v[36:39], a[100:103], v[228:231], v[36:39]
v_exp_f32 v218, v218
v_mfma_f32_16x16x32_bf16 v[4:7], a[100:103], v[224:227], v[4:7]
v_cvt_pk_bf16_f32 v233, v210, v211
v_mfma_f32_16x16x32_bf16 v[8:11], a[104:107], v[224:227], v[8:11]
v_exp_f32 v219, v219
v_mfma_f32_16x16x32_bf16 v[40:43], a[104:107], v[228:231], v[40:43]
v_cvt_pk_bf16_f32 v236, v216, v217
v_mfma_f32_16x16x32_bf16 v[44:47], a[108:111], v[228:231], v[44:47]
v_exp_f32 v212, v212
v_mfma_f32_16x16x32_bf16 v[12:15], a[108:111], v[224:227], v[12:15]
s_waitcnt lgkmcnt(0)
v_mfma_f32_16x16x32_bf16 v[16:19], a[112:115], v[224:227], v[16:19]
v_exp_f32 v213, v213
v_mfma_f32_16x16x32_bf16 v[48:51], a[112:115], v[228:231], v[48:51]
v_cvt_pk_bf16_f32 v237, v218, v219
v_mfma_f32_16x16x32_bf16 v[52:55], a[116:119], v[228:231], v[52:55]
v_exp_f32 v214, v214
v_mfma_f32_16x16x32_bf16 v[20:23], a[116:119], v[224:227], v[20:23]
v_cvt_pk_bf16_f32 v234, v212, v213
v_mfma_f32_16x16x32_bf16 v[24:27], a[120:123], v[224:227], v[24:27]
v_exp_f32 v215, v215
v_mfma_f32_16x16x32_bf16 v[56:59], a[120:123], v[228:231], v[56:59]
v_cvt_pk_bf16_f32 v235, v214, v215
v_mfma_f32_16x16x32_bf16 v[60:63], a[124:127], v[228:231], v[60:63]
v_exp_f32 v220, v220
v_mfma_f32_16x16x32_bf16 v[28:31], a[124:127], v[224:227], v[28:31]
; ---- helper: partial O and l go to the partner
.La5_LA_DP:
v_exp_f32 v221, v221
v_exp_f32 v222, v222
v_exp_f32 v223, v223
v_cvt_pk_bf16_f32 v238, v220, v221
s_nop 0
v_cvt_pk_bf16_f32 v239, v222, v223
s_nop 1
v_smfmac_f32_16x16x64_bf16 a[196:199], a[208:211], v[232:239], v241
v_cvt_pk_bf16_f32 v0, v0, v1
v_cvt_pk_bf16_f32 v1, v2, v3
v_cvt_pk_bf16_f32 v2, v4, v5
v_cvt_pk_bf16_f32 v3, v6, v7
buffer_store_dwordx4 v[0:3], v255, s[68:71], 0 offen offset:0 sc1
v_mfma_f32_16x16x32_bf16 v[64:67], a[96:99], v[232:235], v[64:67]
v_cvt_pk_bf16_f32 v8, v8, v9
v_cvt_pk_bf16_f32 v9, v10, v11
v_cvt_pk_bf16_f32 v10, v12, v13
v_cvt_pk_bf16_f32 v11, v14, v15
buffer_store_dwordx4 v[8:11], v255, s[68:71], 0 offen offset:1024 sc1
v_mfma_f32_16x16x32_bf16 v[96:99], a[96:99], v[236:239], v[96:99]
v_accvgpr_read_b32 v240, a224
v_xor_b32 v241, 32, v240
v_cvt_pk_bf16_f32 v16, v16, v17
v_cvt_pk_bf16_f32 v17, v18, v19
v_cvt_pk_bf16_f32 v18, v20, v21
v_cvt_pk_bf16_f32 v19, v22, v23
v_mfma_f32_16x16x32_bf16 v[100:103], a[100:103], v[236:239], v[100:103]
v_xor_b32 v242, 64, v240
v_xor_b32 v243, 96, v240
buffer_store_dwordx4 v[16:19], v255, s[68:71], 0 offen offset:2048 sc1
v_cvt_pk_bf16_f32 v24, v24, v25
v_cvt_pk_bf16_f32 v25, v26, v27
v_mfma_f32_16x16x32_bf16 v[68:71], a[100:103], v[232:235], v[68:71]
v_cvt_pk_bf16_f32 v26, v28, v29
v_cvt_pk_bf16_f32 v27, v30, v31
buffer_store_dwordx4 v[24:27], v255, s[68:71], 0 offen offset:3072 sc1
v_cvt_pk_bf16_f32 v32, v32, v33
v_cvt_pk_bf16_f32 v33, v34, v35
v_mfma_f32_16x16x32_bf16 v[72:75], a[104:107], v[232:235], v[72:75]
v_cvt_pk_bf16_f32 v34, v36, v37
v_cvt_pk_bf16_f32 v35, v38, v39
buffer_store_dwordx4 v[32:35], v255, s[68:71], s95 offen offset:0 sc1
v_cvt_pk_bf16_f32 v40, v40, v41
v_cvt_pk_bf16_f32 v41, v42, v43
v_mfma_f32_16x16x32_bf16 v[104:107], a[104:107], v[236:239], v[104:107]
s_waitcnt vmcnt(13)
s_barrier
v_cvt_pk_bf16_f32 v42, v44, v45
v_cvt_pk_bf16_f32 v43, v46, v47
buffer_store_dwordx4 v[40:43], v255, s[68:71], s95 offen offset:1024 sc1
v_mfma_f32_16x16x32_bf16 v[108:111], a[108:111], v[236:239], v[108:111]
ds_read_b64_tr_b16 v[192:193], v240 offset:16896
ds_read_b64_tr_b16 v[194:195], v240 offset:17920
v_cvt_pk_bf16_f32 v48, v48, v49
v_cvt_pk_bf16_f32 v49, v50, v51
v_cvt_pk_bf16_f32 v50, v52, v53
v_cvt_pk_bf16_f32 v51, v54, v55
v_mfma_f32_16x16x32_bf16 v[76:79], a[108:111], v[232:235], v[76:79]
ds_read_b64_tr_b16 v[196:197], v240 offset:25344
ds_read_b64_tr_b16 v[198:199], v240 offset:26368
buffer_store_dwordx4 v[48:51], v255, s[68:71], s95 offen offset:2048 sc1
v_cvt_pk_bf16_f32 v56, v56, v57
v_cvt_pk_bf16_f32 v57, v58, v59
v_mfma_f32_16x16x32_bf16 v[80:83], a[112:115], v[232:235], v[80:83]
ds_read_b64_tr_b16 v[200:201], v241 offset:16896
ds_read_b64_tr_b16 v[202:203], v241 offset:17920
v_cvt_pk_bf16_f32 v58, v60, v61
v_cvt_pk_bf16_f32 v59, v62, v63
buffer_store_dwordx4 v[56:59], v255, s[68:71], s95 offen offset:3072 sc1
v_mfma_f32_16x16x32_bf16 v[112:115], a[112:115], v[236:239], v[112:115]
ds_read_b64_tr_b16 v[204:205], v241 offset:25344
ds_read_b64_tr_b16 v[206:207], v241 offset:26368
v_mfma_f32_16x16x32_bf16 v[116:119], a[116:119], v[236:239], v[116:119]
ds_read_b64_tr_b16 v[208:209], v242 offset:16896
ds_read_b64_tr_b16 v[210:211], v242 offset:17920
v_mfma_f32_16x16x32_bf16 v[84:87], a[116:119], v[232:235], v[84:87]
ds_read_b64_tr_b16 v[212:213], v242 offset:25344
ds_read_b64_tr_b16 v[214:215], v242 offset:26368
v_mfma_f32_16x16x32_bf16 v[88:91], a[120:123], v[232:235], v[88:91]
ds_read_b64_tr_b16 v[216:217], v243 offset:16896
ds_read_b64_tr_b16 v[218:219], v243 offset:17920
v_mfma_f32_16x16x32_bf16 v[120:123], a[120:123], v[236:239], v[120:123]
ds_read_b64_tr_b16 v[220:221], v243 offset:25344
ds_read_b64_tr_b16 v[222:223], v243 offset:26368
v_mfma_f32_16x16x32_bf16 v[124:127], a[124:127], v[236:239], v[124:127]
v_mfma_f32_16x16x32_bf16 v[92:95], a[124:127], v[232:235], v[92:95]
s_waitcnt lgkmcnt(12)
v_mfma_f32_16x16x32_bf16 v[0:3], v[192:195], v[128:131], 0
v_mfma_f32_16x16x32_bf16 v[0:3], v[196:199], v[132:135], v[0:3]
v_mfma_f32_16x16x32_bf16 v[32:35], v[196:199], v[148:151], 0
s_waitcnt lgkmcnt(8)
v_accvgpr_read_b32 v224, a192
v_accvgpr_read_b32 v228, a193
v_accvgpr_read_b32 v225, a194
v_accvgpr_read_b32 v229, a195
v_accvgpr_read_b32 v226, a196
v_mfma_f32_16x16x32_bf16 v[32:35], v[192:195], v[144:147], v[32:35]
ds_read_b64_tr_b16 v[196:197], v240 offset:17024
ds_read_b64_tr_b16 v[198:199], v240 offset:18048
v_accvgpr_read_b32 v230, a197
v_accvgpr_read_b32 v227, a198
v_accvgpr_read_b32 v231, a199
v_add_f32 v224, v224, v228
v_mfma_f32_16x16x32_bf16 v[36:39], v[200:203], v[144:147], 0
v_add_f32 v225, v225, v229
v_add_f32 v226, v226, v230
v_add_f32 v227, v227, v231
s_mov_b32 s92, 16384
buffer_store_dwordx4 v[224:227], v255, s[68:71], s92 offen sc1
v_mfma_f32_16x16x32_bf16 v[36:39], v[204:207], v[148:151], v[36:39]
ds_read_b64_tr_b16 v[192:193], v240 offset:25472
ds_read_b64_tr_b16 v[194:195], v240 offset:26496
v_cvt_pk_bf16_f32 v64, v64, v65
v_cvt_pk_bf16_f32 v65, v66, v67
v_cvt_pk_bf16_f32 v66, v68, v69
v_cvt_pk_bf16_f32 v67, v70, v71
v_mfma_f32_16x16x32_bf16 v[4:7], v[204:207], v[132:135], 0
s_waitcnt lgkmcnt(4)
buffer_store_dwordx4 v[64:67], v255, s[68:71], s96 offen offset:0 sc1
v_cvt_pk_bf16_f32 v72, v72, v73
v_cvt_pk_bf16_f32 v73, v74, v75
v_cvt_pk_bf16_f32 v74, v76, v77
v_mfma_f32_16x16x32_bf16 v[4:7], v[200:203], v[128:131], v[4:7]
ds_read_b64_tr_b16 v[204:205], v241 offset:17024
ds_read_b64_tr_b16 v[206:207], v241 offset:18048
v_cvt_pk_bf16_f32 v75, v78, v79
buffer_store_dwordx4 v[72:75], v255, s[68:71], s96 offen offset:1024 sc1
v_cvt_pk_bf16_f32 v80, v80, v81
v_mfma_f32_16x16x32_bf16 v[8:11], v[208:211], v[128:131], 0
v_cvt_pk_bf16_f32 v81, v82, v83
v_cvt_pk_bf16_f32 v82, v84, v85
v_cvt_pk_bf16_f32 v83, v86, v87
buffer_store_dwordx4 v[80:83], v255, s[68:71], s96 offen offset:2048 sc1
v_cvt_pk_bf16_f32 v88, v88, v89
v_mfma_f32_16x16x32_bf16 v[8:11], v[212:215], v[132:135], v[8:11]
ds_read_b64_tr_b16 v[200:201], v241 offset:25472
ds_read_b64_tr_b16 v[202:203], v241 offset:26496
v_cvt_pk_bf16_f32 v89, v90, v91
v_cvt_pk_bf16_f32 v90, v92, v93
v_cvt_pk_bf16_f32 v91, v94, v95
buffer_store_dwordx4 v[88:91], v255, s[68:71], s96 offen offset:3072 sc1
v_mfma_f32_16x16x32_bf16 v[40:43], v[212:215], v[148:151], 0
v_cvt_pk_bf16_f32 v96, v96, v97
v_cvt_pk_bf16_f32 v97, v98, v99
v_cvt_pk_bf16_f32 v98, v100, v101
v_cvt_pk_bf16_f32 v99, v102, v103
buffer_store_dwordx4 v[96:99], v255, s[68:71], s97 offen offset:0 sc1
v_mfma_f32_16x16x32_bf16 v[40:43], v[208:211], v[144:147], v[40:43]
ds_read_b64_tr_b16 v[212:213], v242 offset:17024
ds_read_b64_tr_b16 v[214:215], v242 offset:18048
v_cvt_pk_bf16_f32 v104, v104, v105
v_cvt_pk_bf16_f32 v105, v106, v107
v_cvt_pk_bf16_f32 v106, v108, v109
v_cvt_pk_bf16_f32 v107, v110, v111
v_mfma_f32_16x16x32_bf16 v[44:47], v[216:219], v[144:147], 0
buffer_store_dwordx4 v[104:107], v255, s[68:71], s97 offen offset:1024 sc1
v_cvt_pk_bf16_f32 v112, v112, v113
v_cvt_pk_bf16_f32 v113, v114, v115
v_cvt_pk_bf16_f32 v114, v116, v117
v_cvt_pk_bf16_f32 v115, v118, v119
v_mfma_f32_16x16x32_bf16 v[44:47], v[220:223], v[148:151], v[44:47]
ds_read_b64_tr_b16 v[208:209], v242 offset:25472
ds_read_b64_tr_b16 v[210:211], v242 offset:26496
buffer_store_dwordx4 v[112:115], v255, s[68:71], s97 offen offset:2048 sc1
v_cvt_pk_bf16_f32 v120, v120, v121
v_cvt_pk_bf16_f32 v121, v122, v123
v_mfma_f32_16x16x32_bf16 v[12:15], v[220:223], v[132:135], 0
s_waitcnt lgkmcnt(4)
v_cvt_pk_bf16_f32 v122, v124, v125
v_cvt_pk_bf16_f32 v123, v126, v127
buffer_store_dwordx4 v[120:123], v255, s[68:71], s97 offen offset:3072 sc1
v_mov_b32 v249, 0
v_mfma_f32_16x16x32_bf16 v[12:15], v[216:219], v[128:131], v[12:15]
ds_read_b64_tr_b16 v[220:221], v243 offset:17024
ds_read_b64_tr_b16 v[222:223], v243 offset:18048
s_add_u32 s6, s88, 393216
v_mfma_f32_16x16x32_bf16 v[16:19], v[196:199], v[128:131], 0
v_accvgpr_read_b32 v224, a228
v_add_u32 v225, 0x8000, v224
v_add_u32 v226, 0x2000, v224
v_add_u32 v227, 0xa000, v224
v_mfma_f32_16x16x32_bf16 v[16:19], v[192:195], v[132:135], v[16:19]
ds_read_b64_tr_b16 v[216:217], v243 offset:25472
ds_read_b64_tr_b16 v[218:219], v243 offset:26496
v_accvgpr_read_b32 v248, a225
v_accvgpr_read_b32 v228, a229
v_mfma_f32_16x16x32_bf16 v[48:51], v[192:195], v[148:151], 0
v_accvgpr_read_b32 v229, a230
v_mfma_f32_16x16x32_bf16 v[48:51], v[196:199], v[144:147], v[48:51]
ds_read_b64_tr_b16 v[192:193], v240 offset:33792
ds_read_b64_tr_b16 v[194:195], v240 offset:34816
v_mfma_f32_16x16x32_bf16 v[52:55], v[204:207], v[144:147], 0
v_mfma_f32_16x16x32_bf16 v[52:55], v[200:203], v[148:151], v[52:55]
ds_read_b64_tr_b16 v[196:197], v240 offset:42240
ds_read_b64_tr_b16 v[198:199], v240 offset:43264
v_mfma_f32_16x16x32_bf16 v[20:23], v[200:203], v[132:135], 0
s_waitcnt lgkmcnt(4)
v_mfma_f32_16x16x32_bf16 v[20:23], v[204:207], v[128:131], v[20:23]
ds_read_b64_tr_b16 v[200:201], v241 offset:33792
ds_read_b64_tr_b16 v[202:203], v241 offset:34816
v_mfma_f32_16x16x32_bf16 v[24:27], v[212:215], v[128:131], 0
s_waitcnt vmcnt(0)
buffer_store_dwordx2 v[252:253], v249, s[64:67], s6 offen offset:0 sc1
v_mfma_f32_16x16x32_bf16 v[24:27], v[208:211], v[132:135], v[24:27]
ds_read_b64_tr_b16 v[204:205], v241 offset:42240
ds_read_b64_tr_b16 v[206:207], v241 offset:43264
v_mfma_f32_16x16x32_bf16 v[56:59], v[208:211], v[148:151], 0
s_mov_b32 s93, 114688
buffer_load_dwordx4 a[192:195], v228, s[40:43], s93 offen
v_mfma_f32_16x16x32_bf16 v[56:59], v[212:215], v[144:147], v[56:59]
ds_read_b64_tr_b16 v[208:209], v242 offset:33792
ds_read_b64_tr_b16 v[210:211], v242 offset:34816
v_mfma_f32_16x16x32_bf16 v[60:63], v[220:223], v[144:147], 0
s_mov_b32 s93, 121856
buffer_load_dwordx4 a[196:199], v228, s[40:43], s93 offen
v_mfma_f32_16x16x32_bf16 v[60:63], v[216:219], v[148:151], v[60:63]
ds_read_b64_tr_b16 v[212:213], v242 offset:42240
.La5_LW6_6:
s_cbranch_execz .La5_LW6_R0
ds_read_b64_tr_b16 v[214:215], v242 offset:43264
v_mfma_f32_16x16x32_bf16 v[28:31], v[216:219], v[132:135], 0
s_waitcnt lgkmcnt(4)
s_mov_b32 s93, 129024
buffer_load_dwordx4 a[200:203], v228, s[40:43], s93 offen
v_mfma_f32_16x16x32_bf16 v[28:31], v[220:223], v[128:131], v[28:31]
ds_read_b64_tr_b16 v[216:217], v243 offset:33792
ds_read_b64_tr_b16 v[218:219], v243 offset:34816
v_mfma_f32_16x16x32_bf16 v[0:3], v[192:195], v[136:139], v[0:3]
s_mov_b32 s93, 136192
buffer_load_dwordx4 a[204:207], v228, s[40:43], s93 offen
v_mfma_f32_16x16x32_bf16 v[0:3], v[196:199], v[140:143], v[0:3]
ds_read_b64_tr_b16 v[220:221], v243 offset:42240
ds_read_b64_tr_b16 v[222:223], v243 offset:43264
v_mfma_f32_16x16x32_bf16 v[32:35], v[196:199], v[156:159], v[32:35]
s_mov_b32 s93, 143360
buffer_load_dwordx4 a[208:211], v228, s[40:43], s93 offen
v_mfma_f32_16x16x32_bf16 v[32:35], v[192:195], v[152:155], v[32:35]
ds_read_b64_tr_b16 v[196:197], v240 offset:33920
ds_read_b64_tr_b16 v[198:199], v240 offset:34944
v_mfma_f32_16x16x32_bf16 v[36:39], v[200:203], v[152:155], v[36:39]
s_mov_b32 s93, 150528
buffer_load_dwordx4 a[212:215], v228, s[40:43], s93 offen
v_mfma_f32_16x16x32_bf16 v[36:39], v[204:207], v[156:159], v[36:39]
ds_read_b64_tr_b16 v[192:193], v240 offset:42368
ds_read_b64_tr_b16 v[194:195], v240 offset:43392
v_mfma_f32_16x16x32_bf16 v[4:7], v[204:207], v[140:143], v[4:7]
s_waitcnt lgkmcnt(4)
s_mov_b32 s93, 157696
buffer_load_dwordx4 a[216:219], v228, s[40:43], s93 offen
v_mfma_f32_16x16x32_bf16 v[4:7], v[200:203], v[136:139], v[4:7]
ds_read_b64_tr_b16 v[204:205], v241 offset:33920
ds_read_b64_tr_b16 v[206:207], v241 offset:34944
v_mfma_f32_16x16x32_bf16 v[8:11], v[208:211], v[136:139], v[8:11]
s_mov_b32 s93, 164864
buffer_load_dwordx4 a[220:223], v228, s[40:43], s93 offen
v_mfma_f32_16x16x32_bf16 v[8:11], v[212:215], v[140:143], v[8:11]
ds_read_b64_tr_b16 v[200:201], v241 offset:42368
ds_read_b64_tr_b16 v[202:203], v241 offset:43392
v_mfma_f32_16x16x32_bf16 v[40:43], v[212:215], v[156:159], v[40:43]
s_mov_b32 s93, 172032
buffer_load_dwordx4 a[224:227], v228, s[40:43], s93 offen
v_mfma_f32_16x16x32_bf16 v[40:43], v[208:211], v[152:155], v[40:43]
ds_read_b64_tr_b16 v[212:213], v242 offset:33920
ds_read_b64_tr_b16 v[214:215], v242 offset:34944
v_mfma_f32_16x16x32_bf16 v[44:47], v[216:219], v[152:155], v[44:47]
s_mov_b32 s93, 179200
buffer_load_dwordx4 a[228:231], v228, s[40:43], s93 offen
v_mfma_f32_16x16x32_bf16 v[44:47], v[220:223], v[156:159], v[44:47]
ds_read_b64_tr_b16 v[208:209], v242 offset:42368
ds_read_b64_tr_b16 v[210:211], v242 offset:43392
v_mfma_f32_16x16x32_bf16 v[12:15], v[220:223], v[140:143], v[12:15]
s_waitcnt lgkmcnt(4)
s_mov_b32 s93, 186368
buffer_load_dwordx4 a[232:235], v228, s[40:43], s93 offen
v_mfma_f32_16x16x32_bf16 v[12:15], v[216:219], v[136:139], v[12:15]
ds_read_b64_tr_b16 v[220:221], v243 offset:33920
ds_read_b64_tr_b16 v[222:223], v243 offset:34944
v_mfma_f32_16x16x32_bf16 v[16:19], v[196:199], v[136:139], v[16:19]
s_mov_b32 s93, 193536
buffer_load_dwordx4 a[236:239], v228, s[40:43], s93 offen
v_mfma_f32_16x16x32_bf16 v[16:19], v[192:195], v[140:143], v[16:19]
ds_read_b64_tr_b16 v[216:217], v243 offset:42368
ds_read_b64_tr_b16 v[218:219], v243 offset:43392
v_mfma_f32_16x16x32_bf16 v[48:51], v[192:195], v[156:159], v[48:51]
s_mov_b32 s93, 200704
buffer_load_dwordx4 a[240:243], v228, s[40:43], s93 offen
v_mfma_f32_16x16x32_bf16 v[48:51], v[196:199], v[152:155], v[48:51]
ds_read_b64_tr_b16 v[192:193], v240 offset:16896
ds_read_b64_tr_b16 v[194:195], v240 offset:17920
v_mfma_f32_16x16x32_bf16 v[52:55], v[204:207], v[152:155], v[52:55]
s_mov_b32 s93, 207872
buffer_load_dwordx4 a[244:247], v228, s[40:43], s93 offen
v_mfma_f32_16x16x32_bf16 v[52:55], v[200:203], v[156:159], v[52:55]
ds_read_b64_tr_b16 v[196:197], v240 offset:25344
ds_read_b64_tr_b16 v[198:199], v240 offset:26368
v_mfma_f32_16x16x32_bf16 v[20:23], v[200:203], v[140:143], v[20:23]
s_waitcnt lgkmcnt(4)
s_mov_b32 s93, 215040
buffer_load_dwordx4 a[248:251], v228, s[40:43], s93 offen
v_mfma_f32_16x16x32_bf16 v[20:23], v[204:207], v[136:139], v[20:23]
ds_read_b64_tr_b16 v[200:201], v241 offset:16896
ds_read_b64_tr_b16 v[202:203], v241 offset:17920
v_mfma_f32_16x16x32_bf16 v[24:27], v[212:215], v[136:139], v[24:27]
s_mov_b32 s93, 222208
buffer_load_dwordx4 a[252:255], v228, s[40:43], s93 offen
v_mfma_f32_16x16x32_bf16 v[24:27], v[208:211], v[140:143], v[24:27]
ds_read_b64_tr_b16 v[204:205], v241 offset:25344
ds_read_b64_tr_b16 v[206:207], v241 offset:26368
v_mfma_f32_16x16x32_bf16 v[56:59], v[208:211], v[156:159], v[56:59]
v_mfma_f32_16x16x32_bf16 v[56:59], v[212:215], v[152:155], v[56:59]
ds_read_b64_tr_b16 v[208:209], v242 offset:16896
ds_read_b64_tr_b16 v[210:211], v242 offset:17920
v_mfma_f32_16x16x32_bf16 v[60:63], v[220:223], v[152:155], v[60:63]
v_mfma_f32_16x16x32_bf16 v[60:63], v[216:219], v[156:159], v[60:63]
ds_read_b64_tr_b16 v[212:213], v242 offset:25344
ds_read_b64_tr_b16 v[214:215], v242 offset:26368
v_mfma_f32_16x16x32_bf16 v[28:31], v[216:219], v[140:143], v[28:31]
s_waitcnt lgkmcnt(4)
v_mfma_f32_16x16x32_bf16 v[28:31], v[220:223], v[136:139], v[28:31]
ds_read_b64_tr_b16 v[216:217], v243 offset:16896
ds_read_b64_tr_b16 v[218:219], v243 offset:17920
s_branch .La5_LO_START
; ---- owner: last window, partner-s part added, l checked, O / l leaves
.La5_LA_DC:
.La5_LW5_0:
s_cbranch_execz .La5_LW5_R0
s_waitcnt lgkmcnt(4)
v_mfma_f32_16x16x32_bf16 v[192:195], a[64:67], v[128:131], 0
v_exp_f32 v221, v221
v_mfma_f32_16x16x32_bf16 v[192:195], a[68:71], v[132:135], v[192:195]
v_cvt_pk_bf16_f32 v238, v220, v221
s_add_u32 s89, s89, 16384
v_mfma_f32_16x16x32_bf16 v[192:195], a[72:75], v[136:139], v[192:195]
v_exp_f32 v222, v222
v_mfma_f32_16x16x32_bf16 v[192:195], a[76:79], v[140:143], v[192:195]
buffer_load_dwordx4 v255, s[60:63], s17 offen offset:0 sc1 lds
v_mfma_f32_16x16x32_bf16 v[200:203], a[76:79], v[156:159], 0
v_exp_f32 v223, v223
v_mfma_f32_16x16x32_bf16 v[200:203], a[72:75], v[152:155], v[200:203]
v_cvt_pk_bf16_f32 v239, v222, v223
s_and_b32 s89, s89, 0x1ffff
v_mfma_f32_16x16x32_bf16 v[200:203], a[68:71], v[148:151], v[200:203]
v_exp_f32 v192, v192
v_mfma_f32_16x16x32_bf16 v[200:203], a[64:67], v[144:147], v[200:203]
s_waitcnt lgkmcnt(0)
v_mfma_f32_16x16x32_bf16 v[196:199], a[80:83], v[128:131], 0
v_exp_f32 v193, v193
v_mfma_f32_16x16x32_bf16 v[196:199], a[84:87], v[132:135], v[196:199]
v_cvt_pk_bf16_f32 v224, v192, v193
v_accvgpr_read_b32 v254, a229
v_mfma_f32_16x16x32_bf16 v[196:199], a[88:91], v[136:139], v[196:199]
v_exp_f32 v194, v194
v_mfma_f32_16x16x32_bf16 v[196:199], a[92:95], v[140:143], v[196:199]
buffer_load_dwordx4 v255, s[60:63], s17 offen offset:1024 sc1 lds
v_mfma_f32_16x16x32_bf16 v[204:207], a[92:95], v[156:159], 0
v_exp_f32 v195, v195
v_mfma_f32_16x16x32_bf16 v[204:207], a[88:91], v[152:155], v[204:207]
v_cvt_pk_bf16_f32 v225, v194, v195
v_mfma_f32_16x16x32_bf16 v[204:207], a[84:87], v[148:151], v[204:207]
v_exp_f32 v200, v200
v_mfma_f32_16x16x32_bf16 v[204:207], a[80:83], v[144:147], v[204:207]
buffer_load_dwordx4 v255, s[60:63], s17 offen offset:2048 sc1 lds
v_mfma_f32_16x16x32_bf16 v[208:211], a[64:67], v[160:163], 0
v_exp_f32 v201, v201
v_mfma_f32_16x16x32_bf16 v[208:211], a[68:71], v[164:167], v[208:211]
v_cvt_pk_bf16_f32 v228, v200, v201
v_mfma_f32_16x16x32_bf16 v[208:211], a[72:75], v[168:171], v[208:211]
v_exp_f32 v202, v202
v_mfma_f32_16x16x32_bf16 v[208:211], a[76:79], v[172:175], v[208:211]
buffer_load_dwordx4 v255, s[60:63], s17 offen offset:3072 sc1 lds
v_mfma_f32_16x16x32_bf16 v[216:219], a[76:79], v[188:191], 0
v_exp_f32 v203, v203
v_mfma_f32_16x16x32_bf16 v[216:219], a[72:75], v[184:187], v[216:219]
v_cvt_pk_bf16_f32 v229, v202, v203
s_mov_b32 s93, 0
v_mfma_f32_16x16x32_bf16 v[216:219], a[68:71], v[180:183], v[216:219]
v_exp_f32 v196, v196
v_mfma_f32_16x16x32_bf16 v[216:219], a[64:67], v[176:179], v[216:219]
buffer_load_dwordx4 a[128:131], v254, s[40:43], s93 offen
v_mfma_f32_16x16x32_bf16 v[212:215], a[80:83], v[160:163], 0
v_exp_f32 v197, v197
s_add_u32 s17, s17, 8192
v_mfma_f32_16x16x32_bf16 v[212:215], a[84:87], v[164:167], v[212:215]
v_cvt_pk_bf16_f32 v226, v196, v197
s_mov_b32 s93, 7168
s_add_u32 m0, m0, 16384
v_mfma_f32_16x16x32_bf16 v[212:215], a[88:91], v[168:171], v[212:215]
v_exp_f32 v198, v198
s_and_b32 m0, m0, s78
v_mfma_f32_16x16x32_bf16 v[212:215], a[92:95], v[172:175], v[212:215]
buffer_load_dwordx4 a[132:135], v254, s[40:43], s93 offen
v_mfma_f32_16x16x32_bf16 v[220:223], a[92:95], v[188:191], 0
v_exp_f32 v199, v199
v_mfma_f32_16x16x32_bf16 v[220:223], a[88:91], v[184:187], v[220:223]
v_cvt_pk_bf16_f32 v227, v198, v199
v_mfma_f32_16x16x32_bf16 v[220:223], a[84:87], v[180:183], v[220:223]
v_exp_f32 v204, v204
v_mfma_f32_16x16x32_bf16 v[220:223], a[80:83], v[176:179], v[220:223]
s_waitcnt vmcnt(12)
s_barrier
v_mfma_f32_16x16x32_bf16 v[64:67], a[96:99], v[232:235], v[64:67]
v_exp_f32 v205, v205
v_mfma_f32_16x16x32_bf16 v[96:99], a[96:99], v[236:239], v[96:99]
ds_read_b128 a[96:99], v248 offset:8192
v_cvt_pk_bf16_f32 v230, v204, v205
v_mfma_f32_16x16x32_bf16 v[100:103], a[100:103], v[236:239], v[100:103]
v_exp_f32 v206, v206
v_mfma_f32_16x16x32_bf16 v[68:71], a[100:103], v[232:235], v[68:71]
ds_read_b128 a[100:103], v248 offset:9216
v_mfma_f32_16x16x32_bf16 v[72:75], a[104:107], v[232:235], v[72:75]
v_exp_f32 v207, v207
v_mfma_f32_16x16x32_bf16 v[104:107], a[104:107], v[236:239], v[104:107]
ds_read_b128 a[104:107], v248 offset:10240
v_cvt_pk_bf16_f32 v231, v206, v207
s_mov_b32 s93, 14336
v_mfma_f32_16x16x32_bf16 v[108:111], a[108:111], v[236:239], v[108:111]
v_exp_f32 v208, v208
v_mfma_f32_16x16x32_bf16 v[76:79], a[108:111], v[232:235], v[76:79]
ds_read_b128 a[108:111], v248 offset:11264
buffer_load_dwordx4 a[136:139], v254, s[40:43], s93 offen
v_mfma_f32_16x16x32_bf16 v[80:83], a[112:115], v[232:235], v[80:83]
v_exp_f32 v209, v209
v_mfma_f32_16x16x32_bf16 v[112:115], a[112:115], v[236:239], v[112:115]
ds_read_b128 a[112:115], v248 offset:12288
s_mov_b32 s93, 21504
v_mfma_f32_16x16x32_bf16 v[116:119], a[116:119], v[236:239], v[116:119]
v_exp_f32 v210, v210
v_mfma_f32_16x16x32_bf16 v[84:87], a[116:119], v[232:235], v[84:87]
ds_read_b128 a[116:119], v248 offset:13312
buffer_load_dwordx4 a[140:143], v254, s[40:43], s93 offen
v_mfma_f32_16x16x32_bf16 v[88:91], a[120:123], v[232:235], v[88:91]
v_exp_f32 v211, v211
v_mfma_f32_16x16x32_bf16 v[120:123], a[120:123], v[236:239], v[120:123]
ds_read_b128 a[120:123], v248 offset:14336
v_mfma_f32_16x16x32_bf16 v[124:127], a[124:127], v[236:239], v[124:127]
v_exp_f32 v216, v216
v_mfma_f32_16x16x32_bf16 v[92:95], a[124:127], v[232:235], v[92:95]
ds_read_b128 a[124:127], v248 offset:15360
v_or_b32 v248, s89, v255
v_smfmac_f32_16x16x64_bf16 a[196:199], a[208:211], v[232:239], v241
s_waitcnt lgkmcnt(4)
v_smfmac_f32_16x16x64_bf16 a[192:195], a[208:211], v[224:231], v241
ds_read_b128 a[64:67], v248 offset:0
v_mfma_f32_16x16x32_bf16 v[0:3], a[96:99], v[224:227], v[0:3]
v_exp_f32 v217, v217
v_mfma_f32_16x16x32_bf16 v[32:35], a[96:99], v[228:231], v[32:35]
ds_read_b128 a[68:71], v248 offset:1024
v_cvt_pk_bf16_f32 v232, v208, v209
v_mfma_f32_16x16x32_bf16 v[36:39], a[100:103], v[228:231], v[36:39]
v_exp_f32 v218, v218
v_mfma_f32_16x16x32_bf16 v[4:7], a[100:103], v[224:227], v[4:7]
ds_read_b128 a[72:75], v248 offset:2048
v_cvt_pk_bf16_f32 v233, v210, v211
v_mfma_f32_16x16x32_bf16 v[8:11], a[104:107], v[224:227], v[8:11]
v_exp_f32 v219, v219
v_mfma_f32_16x16x32_bf16 v[40:43], a[104:107], v[228:231], v[40:43]
ds_read_b128 a[76:79], v248 offset:3072
v_cvt_pk_bf16_f32 v236, v216, v217
v_mfma_f32_16x16x32_bf16 v[44:47], a[108:111], v[228:231], v[44:47]
v_exp_f32 v212, v212
v_mfma_f32_16x16x32_bf16 v[12:15], a[108:111], v[224:227], v[12:15]
ds_read_b128 a[80:83], v248 offset:4096
s_waitcnt lgkmcnt(5)
v_mfma_f32_16x16x32_bf16 v[16:19], a[112:115], v[224:227], v[16:19]
v_exp_f32 v213, v213
v_mfma_f32_16x16x32_bf16 v[48:51], a[112:115], v[228:231], v[48:51]
ds_read_b128 a[84:87], v248 offset:5120
v_cvt_pk_bf16_f32 v237, v218, v219
v_mfma_f32_16x16x32_bf16 v[52:55], a[116:119], v[228:231], v[52:55]
v_exp_f32 v214, v214
v_mfma_f32_16x16x32_bf16 v[20:23], a[116:119], v[224:227], v[20:23]
ds_read_b128 a[88:91], v248 offset:6144
v_cvt_pk_bf16_f32 v234, v212, v213
v_mfma_f32_16x16x32_bf16 v[24:27], a[120:123], v[224:227], v[24:27]
v_exp_f32 v215, v215
v_mfma_f32_16x16x32_bf16 v[56:59], a[120:123], v[228:231], v[56:59]
ds_read_b128 a[92:95], v248 offset:7168
v_cvt_pk_bf16_f32 v235, v214, v215
v_mfma_f32_16x16x32_bf16 v[60:63], a[124:127], v[228:231], v[60:63]
v_exp_f32 v220, v220
v_mfma_f32_16x16x32_bf16 v[28:31], a[124:127], v[224:227], v[28:31]
s_waitcnt lgkmcnt(4)
v_mfma_f32_16x16x32_bf16 v[192:195], a[64:67], v[128:131], 0
v_exp_f32 v221, v221
v_mfma_f32_16x16x32_bf16 v[192:195], a[68:71], v[132:135], v[192:195]
v_cvt_pk_bf16_f32 v238, v220, v221
s_add_u32 s89, s89, 16384
v_mfma_f32_16x16x32_bf16 v[192:195], a[72:75], v[136:139], v[192:195]
v_exp_f32 v222, v222
v_mfma_f32_16x16x32_bf16 v[192:195], a[76:79], v[140:143], v[192:195]
buffer_load_dwordx4 v255, s[60:63], s17 offen offset:0 sc1 lds
v_mfma_f32_16x16x32_bf16 v[200:203], a[76:79], v[156:159], 0
v_exp_f32 v223, v223
v_mfma_f32_16x16x32_bf16 v[200:203], a[72:75], v[152:155], v[200:203]
v_cvt_pk_bf16_f32 v239, v222, v223
s_and_b32 s89, s89, 0x1ffff
v_mfma_f32_16x16x32_bf16 v[200:203], a[68:71], v[148:151], v[200:203]
v_exp_f32 v192, v192
v_mfma_f32_16x16x32_bf16 v[200:203], a[64:67], v[144:147], v[200:203]
s_waitcnt lgkmcnt(0)
v_mfma_f32_16x16x32_bf16 v[196:199], a[80:83], v[128:131], 0
v_exp_f32 v193, v193
v_mfma_f32_16x16x32_bf16 v[196:199], a[84:87], v[132:135], v[196:199]
v_cvt_pk_bf16_f32 v224, v192, v193
v_mfma_f32_16x16x32_bf16 v[196:199], a[88:91], v[136:139], v[196:199]
v_exp_f32 v194, v194
v_mfma_f32_16x16x32_bf16 v[196:199], a[92:95], v[140:143], v[196:199]
buffer_load_dwordx4 v255, s[60:63], s17 offen offset:1024 sc1 lds
v_mfma_f32_16x16x32_bf16 v[204:207], a[92:95], v[156:159], 0
v_exp_f32 v195, v195
v_mfma_f32_16x16x32_bf16 v[204:207], a[88:91], v[152:155], v[204:207]
v_cvt_pk_bf16_f32 v225, v194, v195
v_mfma_f32_16x16x32_bf16 v[204:207], a[84:87], v[148:151], v[204:207]
v_exp_f32 v200, v200
v_mfma_f32_16x16x32_bf16 v[204:207], a[80:83], v[144:147], v[204:207]
buffer_load_dwordx4 v255, s[60:63], s17 offen offset:2048 sc1 lds
v_mfma_f32_16x16x32_bf16 v[208:211], a[64:67], v[160:163], 0
v_exp_f32 v201, v201
v_mfma_f32_16x16x32_bf16 v[208:211], a[68:71], v[164:167], v[208:211]
v_cvt_pk_bf16_f32 v228, v200, v201
v_mfma_f32_16x16x32_bf16 v[208:211], a[72:75], v[168:171], v[208:211]
v_exp_f32 v202, v202
v_mfma_f32_16x16x32_bf16 v[208:211], a[76:79], v[172:175], v[208:211]
buffer_load_dwordx4 v255, s[60:63], s17 offen offset:3072 sc1 lds
v_mfma_f32_16x16x32_bf16 v[216:219], a[76:79], v[188:191], 0
v_exp_f32 v203, v203
v_mfma_f32_16x16x32_bf16 v[216:219], a[72:75], v[184:187], v[216:219]
v_cvt_pk_bf16_f32 v229, v202, v203
s_mov_b32 s93, 28672
v_mfma_f32_16x16x32_bf16 v[216:219], a[68:71], v[180:183], v[216:219]
v_exp_f32 v196, v196
v_mfma_f32_16x16x32_bf16 v[216:219], a[64:67], v[176:179], v[216:219]
buffer_load_dwordx4 a[144:147], v254, s[40:43], s93 offen
v_mfma_f32_16x16x32_bf16 v[212:215], a[80:83], v[160:163], 0
v_exp_f32 v197, v197
s_add_u32 s17, s17, 8192
v_mfma_f32_16x16x32_bf16 v[212:215], a[84:87], v[164:167], v[212:215]
v_cvt_pk_bf16_f32 v226, v196, v197
s_mov_b32 s93, 35840
s_add_u32 m0, m0, 16384
v_mfma_f32_16x16x32_bf16 v[212:215], a[88:91], v[168:171], v[212:215]
v_exp_f32 v198, v198
s_and_b32 m0, m0, s78
v_mfma_f32_16x16x32_bf16 v[212:215], a[92:95], v[172:175], v[212:215]
buffer_load_dwordx4 a[148:151], v254, s[40:43], s93 offen
v_mfma_f32_16x16x32_bf16 v[220:223], a[92:95], v[188:191], 0
v_exp_f32 v199, v199
v_mfma_f32_16x16x32_bf16 v[220:223], a[88:91], v[184:187], v[220:223]
v_cvt_pk_bf16_f32 v227, v198, v199
v_mfma_f32_16x16x32_bf16 v[220:223], a[84:87], v[180:183], v[220:223]
v_exp_f32 v204, v204
v_mfma_f32_16x16x32_bf16 v[220:223], a[80:83], v[176:179], v[220:223]
s_waitcnt vmcnt(15)
s_barrier
v_mfma_f32_16x16x32_bf16 v[64:67], a[96:99], v[232:235], v[64:67]
v_exp_f32 v205, v205
v_mfma_f32_16x16x32_bf16 v[96:99], a[96:99], v[236:239], v[96:99]
ds_read_b128 a[96:99], v248 offset:8192
v_cvt_pk_bf16_f32 v230, v204, v205
v_mfma_f32_16x16x32_bf16 v[100:103], a[100:103], v[236:239], v[100:103]
v_exp_f32 v206, v206
v_mfma_f32_16x16x32_bf16 v[68:71], a[100:103], v[232:235], v[68:71]
ds_read_b128 a[100:103], v248 offset:9216
v_mfma_f32_16x16x32_bf16 v[72:75], a[104:107], v[232:235], v[72:75]
v_exp_f32 v207, v207
v_mfma_f32_16x16x32_bf16 v[104:107], a[104:107], v[236:239], v[104:107]
ds_read_b128 a[104:107], v248 offset:10240
v_cvt_pk_bf16_f32 v231, v206, v207
s_mov_b32 s93, 43008
v_mfma_f32_16x16x32_bf16 v[108:111], a[108:111], v[236:239], v[108:111]
v_exp_f32 v208, v208
v_mfma_f32_16x16x32_bf16 v[76:79], a[108:111], v[232:235], v[76:79]
ds_read_b128 a[108:111], v248 offset:11264
buffer_load_dwordx4 a[152:155], v254, s[40:43], s93 offen
v_mfma_f32_16x16x32_bf16 v[80:83], a[112:115], v[232:235], v[80:83]
v_exp_f32 v209, v209
v_mfma_f32_16x16x32_bf16 v[112:115], a[112:115], v[236:239], v[112:115]
ds_read_b128 a[112:115], v248 offset:12288
s_mov_b32 s93, 50176
v_mfma_f32_16x16x32_bf16 v[116:119], a[116:119], v[236:239], v[116:119]
v_exp_f32 v210, v210
v_mfma_f32_16x16x32_bf16 v[84:87], a[116:119], v[232:235], v[84:87]
ds_read_b128 a[116:119], v248 offset:13312
buffer_load_dwordx4 a[156:159], v254, s[40:43], s93 offen
v_mfma_f32_16x16x32_bf16 v[88:91], a[120:123], v[232:235], v[88:91]
v_exp_f32 v211, v211
v_mfma_f32_16x16x32_bf16 v[120:123], a[120:123], v[236:239], v[120:123]
ds_read_b128 a[120:123], v248 offset:14336
v_mfma_f32_16x16x32_bf16 v[124:127], a[124:127], v[236:239], v[124:127]
v_exp_f32 v216, v216
v_mfma_f32_16x16x32_bf16 v[92:95], a[124:127], v[232:235], v[92:95]
ds_read_b128 a[124:127], v248 offset:15360
v_or_b32 v248, s89, v255
v_smfmac_f32_16x16x64_bf16 a[196:199], a[208:211], v[232:239], v241
s_waitcnt lgkmcnt(4)
v_smfmac_f32_16x16x64_bf16 a[192:195], a[208:211], v[224:231], v241
ds_read_b128 a[64:67], v248 offset:0
v_mfma_f32_16x16x32_bf16 v[0:3], a[96:99], v[224:227], v[0:3]
v_exp_f32 v217, v217
v_mfma_f32_16x16x32_bf16 v[32:35], a[96:99], v[228:231], v[32:35]
ds_read_b128 a[68:71], v248 offset:1024
v_cvt_pk_bf16_f32 v232, v208, v209
v_mfma_f32_16x16x32_bf16 v[36:39], a[100:103], v[228:231], v[36:39]
v_exp_f32 v218, v218
v_mfma_f32_16x16x32_bf16 v[4:7], a[100:103], v[224:227], v[4:7]
ds_read_b128 a[72:75], v248 offset:2048
v_cvt_pk_bf16_f32 v233, v210, v211
v_mfma_f32_16x16x32_bf16 v[8:11], a[104:107], v[224:227], v[8:11]
v_exp_f32 v219, v219
v_mfma_f32_16x16x32_bf16 v[40:43], a[104:107], v[228:231], v[40:43]
ds_read_b128 a[76:79], v248 offset:3072
v_cvt_pk_bf16_f32 v236, v216, v217
v_mfma_f32_16x16x32_bf16 v[44:47], a[108:111], v[228:231], v[44:47]
v_exp_f32 v212, v212
v_mfma_f32_16x16x32_bf16 v[12:15], a[108:111], v[224:227], v[12:15]
ds_read_b128 a[80:83], v248 offset:4096
s_waitcnt lgkmcnt(5)
v_mfma_f32_16x16x32_bf16 v[16:19], a[112:115], v[224:227], v[16:19]
v_exp_f32 v213, v213
v_mfma_f32_16x16x32_bf16 v[48:51], a[112:115], v[228:231], v[48:51]
ds_read_b128 a[84:87], v248 offset:5120
v_cvt_pk_bf16_f32 v237, v218, v219
v_mfma_f32_16x16x32_bf16 v[52:55], a[116:119], v[228:231], v[52:55]
v_exp_f32 v214, v214
v_mfma_f32_16x16x32_bf16 v[20:23], a[116:119], v[224:227], v[20:23]
ds_read_b128 a[88:91], v248 offset:6144
v_cvt_pk_bf16_f32 v234, v212, v213
v_mfma_f32_16x16x32_bf16 v[24:27], a[120:123], v[224:227], v[24:27]
v_exp_f32 v215, v215
v_mfma_f32_16x16x32_bf16 v[56:59], a[120:123], v[228:231], v[56:59]
ds_read_b128 a[92:95], v248 offset:7168
v_cvt_pk_bf16_f32 v235, v214, v215
v_mfma_f32_16x16x32_bf16 v[60:63], a[124:127], v[228:231], v[60:63]
v_exp_f32 v220, v220
v_mfma_f32_16x16x32_bf16 v[28:31], a[124:127], v[224:227], v[28:31]
s_waitcnt lgkmcnt(4)
v_mfma_f32_16x16x32_bf16 v[192:195], a[64:67], v[128:131], 0
v_exp_f32 v221, v221
v_mfma_f32_16x16x32_bf16 v[192:195], a[68:71], v[132:135], v[192:195]
v_cvt_pk_bf16_f32 v238, v220, v221
s_add_u32 s89, s89, 16384
v_mfma_f32_16x16x32_bf16 v[192:195], a[72:75], v[136:139], v[192:195]
v_exp_f32 v222, v222
v_mfma_f32_16x16x32_bf16 v[192:195], a[76:79], v[140:143], v[192:195]
buffer_load_dwordx4 v255, s[60:63], s17 offen offset:0 sc1 lds
v_mfma_f32_16x16x32_bf16 v[200:203], a[76:79], v[156:159], 0
v_exp_f32 v223, v223
v_mfma_f32_16x16x32_bf16 v[200:203], a[72:75], v[152:155], v[200:203]
v_cvt_pk_bf16_f32 v239, v222, v223
s_and_b32 s89, s89, 0x1ffff
v_mfma_f32_16x16x32_bf16 v[200:203], a[68:71], v[148:151], v[200:203]
v_exp_f32 v192, v192
v_mfma_f32_16x16x32_bf16 v[200:203], a[64:67], v[144:147], v[200:203]
s_waitcnt lgkmcnt(0)
v_mfma_f32_16x16x32_bf16 v[196:199], a[80:83], v[128:131], 0
v_exp_f32 v193, v193
v_mfma_f32_16x16x32_bf16 v[196:199], a[84:87], v[132:135], v[196:199]
v_cvt_pk_bf16_f32 v224, v192, v193
v_mfma_f32_16x16x32_bf16 v[196:199], a[88:91], v[136:139], v[196:199]
v_exp_f32 v194, v194
v_mfma_f32_16x16x32_bf16 v[196:199], a[92:95], v[140:143], v[196:199]
buffer_load_dwordx4 v255, s[60:63], s17 offen offset:1024 sc1 lds
v_mfma_f32_16x16x32_bf16 v[204:207], a[92:95], v[156:159], 0
v_exp_f32 v195, v195
v_mfma_f32_16x16x32_bf16 v[204:207], a[88:91], v[152:155], v[204:207]
v_cvt_pk_bf16_f32 v225, v194, v195
v_mfma_f32_16x16x32_bf16 v[204:207], a[84:87], v[148:151], v[204:207]
v_exp_f32 v200, v200
v_mfma_f32_16x16x32_bf16 v[204:207], a[80:83], v[144:147], v[204:207]
buffer_load_dwordx4 v255, s[60:63], s17 offen offset:2048 sc1 lds
v_mfma_f32_16x16x32_bf16 v[208:211], a[64:67], v[160:163], 0
v_exp_f32 v201, v201
v_mfma_f32_16x16x32_bf16 v[208:211], a[68:71], v[164:167], v[208:211]
v_cvt_pk_bf16_f32 v228, v200, v201
v_mfma_f32_16x16x32_bf16 v[208:211], a[72:75], v[168:171], v[208:211]
v_exp_f32 v202, v202
v_mfma_f32_16x16x32_bf16 v[208:211], a[76:79], v[172:175], v[208:211]
buffer_load_dwordx4 v255, s[60:63], s17 offen offset:3072 sc1 lds
v_mfma_f32_16x16x32_bf16 v[216:219], a[76:79], v[188:191], 0
v_exp_f32 v203, v203
v_mfma_f32_16x16x32_bf16 v[216:219], a[72:75], v[184:187], v[216:219]
v_cvt_pk_bf16_f32 v229, v202, v203
s_mov_b32 s93, 57344
v_mfma_f32_16x16x32_bf16 v[216:219], a[68:71], v[180:183], v[216:219]
v_exp_f32 v196, v196
v_mfma_f32_16x16x32_bf16 v[216:219], a[64:67], v[176:179], v[216:219]
buffer_load_dwordx4 a[160:163], v254, s[40:43], s93 offen
v_mfma_f32_16x16x32_bf16 v[212:215], a[80:83], v[160:163], 0
v_exp_f32 v197, v197
s_add_u32 s17, s17, 8192
v_mfma_f32_16x16x32_bf16 v[212:215], a[84:87], v[164:167], v[212:215]
v_cvt_pk_bf16_f32 v226, v196, v197
s_mov_b32 s93, 64512
s_add_u32 m0, m0, 16384
v_mfma_f32_16x16x32_bf16 v[212:215], a[88:91], v[168:171], v[212:215]
v_exp_f32 v198, v198
s_and_b32 m0, m0, s78
v_mfma_f32_16x16x32_bf16 v[212:215], a[92:95], v[172:175], v[212:215]
buffer_load_dwordx4 a[164:167], v254, s[40:43], s93 offen
v_mfma_f32_16x16x32_bf16 v[220:223], a[92:95], v[188:191], 0
v_exp_f32 v199, v199
v_mfma_f32_16x16x32_bf16 v[220:223], a[88:91], v[184:187], v[220:223]
v_cvt_pk_bf16_f32 v227, v198, v199
v_mfma_f32_16x16x32_bf16 v[220:223], a[84:87], v[180:183], v[220:223]
v_exp_f32 v204, v204
v_mfma_f32_16x16x32_bf16 v[220:223], a[80:83], v[176:179], v[220:223]
s_waitcnt vmcnt(18)
s_barrier
v_mfma_f32_16x16x32_bf16 v[64:67], a[96:99], v[232:235], v[64:67]
v_exp_f32 v205, v205
v_mfma_f32_16x16x32_bf16 v[96:99], a[96:99], v[236:239], v[96:99]
ds_read_b128 a[96:99], v248 offset:8192
v_cvt_pk_bf16_f32 v230, v204, v205
v_mfma_f32_16x16x32_bf16 v[100:103], a[100:103], v[236:239], v[100:103]
v_exp_f32 v206, v206
v_mfma_f32_16x16x32_bf16 v[68:71], a[100:103], v[232:235], v[68:71]
ds_read_b128 a[100:103], v248 offset:9216
v_mfma_f32_16x16x32_bf16 v[72:75], a[104:107], v[232:235], v[72:75]
v_exp_f32 v207, v207
v_mfma_f32_16x16x32_bf16 v[104:107], a[104:107], v[236:239], v[104:107]
ds_read_b128 a[104:107], v248 offset:10240
v_cvt_pk_bf16_f32 v231, v206, v207
s_mov_b32 s93, 71680
v_mfma_f32_16x16x32_bf16 v[108:111], a[108:111], v[236:239], v[108:111]
v_exp_f32 v208, v208
v_mfma_f32_16x16x32_bf16 v[76:79], a[108:111], v[232:235], v[76:79]
ds_read_b128 a[108:111], v248 offset:11264
buffer_load_dwordx4 a[168:171], v254, s[40:43], s93 offen
v_mfma_f32_16x16x32_bf16 v[80:83], a[112:115], v[232:235], v[80:83]
v_exp_f32 v209, v209
v_mfma_f32_16x16x32_bf16 v[112:115], a[112:115], v[236:239], v[112:115]
ds_read_b128 a[112:115], v248 offset:12288
s_mov_b32 s93, 78848
v_mfma_f32_16x16x32_bf16 v[116:119], a[116:119], v[236:239], v[116:119]
v_exp_f32 v210, v210
v_mfma_f32_16x16x32_bf16 v[84:87], a[116:119], v[232:235], v[84:87]
ds_read_b128 a[116:119], v248 offset:13312
buffer_load_dwordx4 a[172:175], v254, s[40:43], s93 offen
v_mfma_f32_16x16x32_bf16 v[88:91], a[120:123], v[232:235], v[88:91]
v_exp_f32 v211, v211
v_mfma_f32_16x16x32_bf16 v[120:123], a[120:123], v[236:239], v[120:123]
ds_read_b128 a[120:123], v248 offset:14336
v_mfma_f32_16x16x32_bf16 v[124:127], a[124:127], v[236:239], v[124:127]
v_exp_f32 v216, v216
v_mfma_f32_16x16x32_bf16 v[92:95], a[124:127], v[232:235], v[92:95]
ds_read_b128 a[124:127], v248 offset:15360
v_or_b32 v248, s89, v255
v_smfmac_f32_16x16x64_bf16 a[196:199], a[208:211], v[232:239], v241
s_waitcnt lgkmcnt(4)
v_smfmac_f32_16x16x64_bf16 a[192:195], a[208:211], v[224:231], v241
ds_read_b128 a[64:67], v248 offset:0
v_mfma_f32_16x16x32_bf16 v[0:3], a[96:99], v[224:227], v[0:3]
v_exp_f32 v217, v217
v_mfma_f32_16x16x32_bf16 v[32:35], a[96:99], v[228:231], v[32:35]
ds_read_b128 a[68:71], v248 offset:1024
v_cvt_pk_bf16_f32 v232, v208, v209
v_mfma_f32_16x16x32_bf16 v[36:39], a[100:103], v[228:231], v[36:39]
v_exp_f32 v218, v218
v_mfma_f32_16x16x32_bf16 v[4:7], a[100:103], v[224:227], v[4:7]
ds_read_b128 a[72:75], v248 offset:2048
v_cvt_pk_bf16_f32 v233, v210, v211
v_mfma_f32_16x16x32_bf16 v[8:11], a[104:107], v[224:227], v[8:11]
v_exp_f32 v219, v219
v_mfma_f32_16x16x32_bf16 v[40:43], a[104:107], v[228:231], v[40:43]
ds_read_b128 a[76:79], v248 offset:3072
v_cvt_pk_bf16_f32 v236, v216, v217
v_mfma_f32_16x16x32_bf16 v[44:47], a[108:111], v[228:231], v[44:47]
v_exp_f32 v212, v212
v_mfma_f32_16x16x32_bf16 v[12:15], a[108:111], v[224:227], v[12:15]
ds_read_b128 a[80:83], v248 offset:4096
s_waitcnt lgkmcnt(5)
v_mfma_f32_16x16x32_bf16 v[16:19], a[112:115], v[224:227], v[16:19]
v_exp_f32 v213, v213
v_mfma_f32_16x16x32_bf16 v[48:51], a[112:115], v[228:231], v[48:51]
ds_read_b128 a[84:87], v248 offset:5120
v_cvt_pk_bf16_f32 v237, v218, v219
v_mfma_f32_16x16x32_bf16 v[52:55], a[116:119], v[228:231], v[52:55]
v_exp_f32 v214, v214
v_mfma_f32_16x16x32_bf16 v[20:23], a[116:119], v[224:227], v[20:23]
ds_read_b128 a[88:91], v248 offset:6144
v_cvt_pk_bf16_f32 v234, v212, v213
v_mfma_f32_16x16x32_bf16 v[24:27], a[120:123], v[224:227], v[24:27]
v_exp_f32 v215, v215
v_mfma_f32_16x16x32_bf16 v[56:59], a[120:123], v[228:231], v[56:59]
ds_read_b128 a[92:95], v248 offset:7168
v_cvt_pk_bf16_f32 v235, v214, v215
v_mfma_f32_16x16x32_bf16 v[60:63], a[124:127], v[228:231], v[60:63]
v_exp_f32 v220, v220
v_mfma_f32_16x16x32_bf16 v[28:31], a[124:127], v[224:227], v[28:31]
s_waitcnt lgkmcnt(4)
v_mfma_f32_16x16x32_bf16 v[192:195], a[64:67], v[128:131], 0
v_exp_f32 v221, v221
v_mfma_f32_16x16x32_bf16 v[192:195], a[68:71], v[132:135], v[192:195]
v_cvt_pk_bf16_f32 v238, v220, v221
v_accvgpr_read_b32 v240, a228
v_mfma_f32_16x16x32_bf16 v[192:195], a[72:75], v[136:139], v[192:195]
v_exp_f32 v222, v222
v_mfma_f32_16x16x32_bf16 v[192:195], a[76:79], v[140:143], v[192:195]
s_add_u32 m0, s83, 16896
.La5_LW5_1:
s_cbranch_execz .La5_LW5_R0
s_lshl_b32 s17, s72, 18
v_mfma_f32_16x16x32_bf16 v[200:203], a[76:79], v[156:159], 0
v_exp_f32 v223, v223
v_mfma_f32_16x16x32_bf16 v[200:203], a[72:75], v[152:155], v[200:203]
v_cvt_pk_bf16_f32 v239, v222, v223
s_add_u32 s89, s89, 16384
v_mfma_f32_16x16x32_bf16 v[200:203], a[68:71], v[148:151], v[200:203]
v_exp_f32 v192, v192
v_mfma_f32_16x16x32_bf16 v[200:203], a[64:67], v[144:147], v[200:203]
s_waitcnt lgkmcnt(0)
s_and_b32 s89, s89, 0x1ffff
v_mfma_f32_16x16x32_bf16 v[196:199], a[80:83], v[128:131], 0
v_exp_f32 v193, v193
v_mfma_f32_16x16x32_bf16 v[196:199], a[84:87], v[132:135], v[196:199]
v_cvt_pk_bf16_f32 v224, v192, v193
v_mfma_f32_16x16x32_bf16 v[196:199], a[88:91], v[136:139], v[196:199]
v_exp_f32 v194, v194
v_mfma_f32_16x16x32_bf16 v[196:199], a[92:95], v[140:143], v[196:199]
buffer_load_dwordx4 v240, s[56:59], s17 offen lds
v_mfma_f32_16x16x32_bf16 v[204:207], a[92:95], v[156:159], 0
v_exp_f32 v195, v195
s_add_u32 s17, s17, 32768
v_mfma_f32_16x16x32_bf16 v[204:207], a[88:91], v[152:155], v[204:207]
v_cvt_pk_bf16_f32 v225, v194, v195
s_add_u32 m0, m0, 1024
v_mfma_f32_16x16x32_bf16 v[204:207], a[84:87], v[148:151], v[204:207]
v_exp_f32 v200, v200
v_mfma_f32_16x16x32_bf16 v[204:207], a[80:83], v[144:147], v[204:207]
buffer_load_dwordx4 v240, s[56:59], s17 offen lds
v_mfma_f32_16x16x32_bf16 v[208:211], a[64:67], v[160:163], 0
v_exp_f32 v201, v201
s_add_u32 s17, s17, 4294942720
v_mfma_f32_16x16x32_bf16 v[208:211], a[68:71], v[164:167], v[208:211]
v_cvt_pk_bf16_f32 v228, v200, v201
s_add_u32 m0, m0, 1152
v_mfma_f32_16x16x32_bf16 v[208:211], a[72:75], v[168:171], v[208:211]
v_exp_f32 v202, v202
v_mfma_f32_16x16x32_bf16 v[208:211], a[76:79], v[172:175], v[208:211]
buffer_load_dwordx4 v240, s[56:59], s17 offen lds
v_mfma_f32_16x16x32_bf16 v[216:219], a[76:79], v[188:191], 0
v_exp_f32 v203, v203
s_add_u32 s17, s17, 32768
v_mfma_f32_16x16x32_bf16 v[216:219], a[72:75], v[184:187], v[216:219]
v_cvt_pk_bf16_f32 v229, v202, v203
s_add_u32 m0, m0, 1024
v_mfma_f32_16x16x32_bf16 v[216:219], a[68:71], v[180:183], v[216:219]
v_exp_f32 v196, v196
v_mfma_f32_16x16x32_bf16 v[216:219], a[64:67], v[176:179], v[216:219]
buffer_load_dwordx4 v240, s[56:59], s17 offen lds
v_mfma_f32_16x16x32_bf16 v[212:215], a[80:83], v[160:163], 0
v_exp_f32 v197, v197
s_add_u32 s17, s17, 90112
v_mfma_f32_16x16x32_bf16 v[212:215], a[84:87], v[164:167], v[212:215]
v_cvt_pk_bf16_f32 v226, v196, v197
s_mov_b32 s93, 86016
s_add_u32 m0, s83, 33792
v_mfma_f32_16x16x32_bf16 v[212:215], a[88:91], v[168:171], v[212:215]
v_exp_f32 v198, v198
v_mfma_f32_16x16x32_bf16 v[212:215], a[92:95], v[172:175], v[212:215]
buffer_load_dwordx4 a[176:179], v254, s[40:43], s93 offen
v_mfma_f32_16x16x32_bf16 v[220:223], a[92:95], v[188:191], 0
v_exp_f32 v199, v199
v_mfma_f32_16x16x32_bf16 v[220:223], a[88:91], v[184:187], v[220:223]
v_cvt_pk_bf16_f32 v227, v198, v199
v_mfma_f32_16x16x32_bf16 v[220:223], a[84:87], v[180:183], v[220:223]
v_exp_f32 v204, v204
v_mfma_f32_16x16x32_bf16 v[220:223], a[80:83], v[176:179], v[220:223]
s_waitcnt vmcnt(17)
s_barrier
v_mfma_f32_16x16x32_bf16 v[64:67], a[96:99], v[232:235], v[64:67]
v_exp_f32 v205, v205
v_mfma_f32_16x16x32_bf16 v[96:99], a[96:99], v[236:239], v[96:99]
ds_read_b128 a[96:99], v248 offset:8192
v_cvt_pk_bf16_f32 v230, v204, v205
v_mfma_f32_16x16x32_bf16 v[100:103], a[100:103], v[236:239], v[100:103]
v_exp_f32 v206, v206
v_mfma_f32_16x16x32_bf16 v[68:71], a[100:103], v[232:235], v[68:71]
ds_read_b128 a[100:103], v248 offset:9216
v_mfma_f32_16x16x32_bf16 v[72:75], a[104:107], v[232:235], v[72:75]
v_exp_f32 v207, v207
v_mfma_f32_16x16x32_bf16 v[104:107], a[104:107], v[236:239], v[104:107]
ds_read_b128 a[104:107], v248 offset:10240
v_cvt_pk_bf16_f32 v231, v206, v207
s_mov_b32 s93, 93184
v_mfma_f32_16x16x32_bf16 v[108:111], a[108:111], v[236:239], v[108:111]
v_exp_f32 v208, v208
v_mfma_f32_16x16x32_bf16 v[76:79], a[108:111], v[232:235], v[76:79]
ds_read_b128 a[108:111], v248 offset:11264
buffer_load_dwordx4 a[180:183], v254, s[40:43], s93 offen
v_mfma_f32_16x16x32_bf16 v[80:83], a[112:115], v[232:235], v[80:83]
v_exp_f32 v209, v209
v_mfma_f32_16x16x32_bf16 v[112:115], a[112:115], v[236:239], v[112:115]
ds_read_b128 a[112:115], v248 offset:12288
s_mov_b32 s93, 100352
v_mfma_f32_16x16x32_bf16 v[116:119], a[116:119], v[236:239], v[116:119]
v_exp_f32 v210, v210
v_mfma_f32_16x16x32_bf16 v[84:87], a[116:119], v[232:235], v[84:87]
ds_read_b128 a[116:119], v248 offset:13312
buffer_load_dwordx4 a[184:187], v254, s[40:43], s93 offen
v_mfma_f32_16x16x32_bf16 v[88:91], a[120:123], v[232:235], v[88:91]
v_exp_f32 v211, v211
v_mfma_f32_16x16x32_bf16 v[120:123], a[120:123], v[236:239], v[120:123]
ds_read_b128 a[120:123], v248 offset:14336
v_mfma_f32_16x16x32_bf16 v[124:127], a[124:127], v[236:239], v[124:127]
v_exp_f32 v216, v216
v_mfma_f32_16x16x32_bf16 v[92:95], a[124:127], v[232:235], v[92:95]
ds_read_b128 a[124:127], v248 offset:15360
v_or_b32 v248, s89, v255
v_smfmac_f32_16x16x64_bf16 a[196:199], a[208:211], v[232:239], v241
s_waitcnt lgkmcnt(4)
v_smfmac_f32_16x16x64_bf16 a[192:195], a[208:211], v[224:231], v241
ds_read_b128 a[64:67], v248 offset:0
v_mfma_f32_16x16x32_bf16 v[0:3], a[96:99], v[224:227], v[0:3]
v_exp_f32 v217, v217
v_mfma_f32_16x16x32_bf16 v[32:35], a[96:99], v[228:231], v[32:35]
ds_read_b128 a[68:71], v248 offset:1024
v_cvt_pk_bf16_f32 v232, v208, v209
v_mfma_f32_16x16x32_bf16 v[36:39], a[100:103], v[228:231], v[36:39]
v_exp_f32 v218, v218
v_mfma_f32_16x16x32_bf16 v[4:7], a[100:103], v[224:227], v[4:7]
ds_read_b128 a[72:75], v248 offset:2048
v_cvt_pk_bf16_f32 v233, v210, v211
v_mfma_f32_16x16x32_bf16 v[8:11], a[104:107], v[224:227], v[8:11]
v_exp_f32 v219, v219
v_mfma_f32_16x16x32_bf16 v[40:43], a[104:107], v[228:231], v[40:43]
ds_read_b128 a[76:79], v248 offset:3072
v_cvt_pk_bf16_f32 v236, v216, v217
v_mfma_f32_16x16x32_bf16 v[44:47], a[108:111], v[228:231], v[44:47]
v_exp_f32 v212, v212
v_mfma_f32_16x16x32_bf16 v[12:15], a[108:111], v[224:227], v[12:15]
ds_read_b128 a[80:83], v248 offset:4096
s_waitcnt lgkmcnt(5)
v_mfma_f32_16x16x32_bf16 v[16:19], a[112:115], v[224:227], v[16:19]
v_exp_f32 v213, v213
v_mfma_f32_16x16x32_bf16 v[48:51], a[112:115], v[228:231], v[48:51]
ds_read_b128 a[84:87], v248 offset:5120
v_cvt_pk_bf16_f32 v237, v218, v219
v_mfma_f32_16x16x32_bf16 v[52:55], a[116:119], v[228:231], v[52:55]
v_exp_f32 v214, v214
v_mfma_f32_16x16x32_bf16 v[20:23], a[116:119], v[224:227], v[20:23]
ds_read_b128 a[88:91], v248 offset:6144
v_cvt_pk_bf16_f32 v234, v212, v213
v_mfma_f32_16x16x32_bf16 v[24:27], a[120:123], v[224:227], v[24:27]
v_exp_f32 v215, v215
v_mfma_f32_16x16x32_bf16 v[56:59], a[120:123], v[228:231], v[56:59]
ds_read_b128 a[92:95], v248 offset:7168
v_cvt_pk_bf16_f32 v235, v214, v215
v_mfma_f32_16x16x32_bf16 v[60:63], a[124:127], v[228:231], v[60:63]
v_exp_f32 v220, v220
v_mfma_f32_16x16x32_bf16 v[28:31], a[124:127], v[224:227], v[28:31]
buffer_load_dword v250, v251, s[64:67], s20 offen sc1
s_waitcnt lgkmcnt(4)
v_mfma_f32_16x16x32_bf16 v[192:195], a[64:67], v[128:131], 0
v_exp_f32 v221, v221
v_mfma_f32_16x16x32_bf16 v[192:195], a[68:71], v[132:135], v[192:195]
v_cvt_pk_bf16_f32 v238, v220, v221
s_add_u32 s89, s89, 16384
v_mfma_f32_16x16x32_bf16 v[192:195], a[72:75], v[136:139], v[192:195]
v_exp_f32 v222, v222
v_mfma_f32_16x16x32_bf16 v[192:195], a[76:79], v[140:143], v[192:195]
buffer_load_dwordx4 v240, s[56:59], s17 offen lds
v_mfma_f32_16x16x32_bf16 v[200:203], a[76:79], v[156:159], 0
v_exp_f32 v223, v223
s_add_u32 s17, s17, 32768
v_mfma_f32_16x16x32_bf16 v[200:203], a[72:75], v[152:155], v[200:203]
v_cvt_pk_bf16_f32 v239, v222, v223
s_add_u32 m0, m0, 1024
v_mfma_f32_16x16x32_bf16 v[200:203], a[68:71], v[148:151], v[200:203]
v_exp_f32 v192, v192
v_mfma_f32_16x16x32_bf16 v[200:203], a[64:67], v[144:147], v[200:203]
s_waitcnt lgkmcnt(0)
s_and_b32 s89, s89, 0x1ffff
v_mfma_f32_16x16x32_bf16 v[196:199], a[80:83], v[128:131], 0
v_exp_f32 v193, v193
v_mfma_f32_16x16x32_bf16 v[196:199], a[84:87], v[132:135], v[196:199]
v_cvt_pk_bf16_f32 v224, v192, v193
v_mfma_f32_16x16x32_bf16 v[196:199], a[88:91], v[136:139], v[196:199]
v_exp_f32 v194, v194
v_mfma_f32_16x16x32_bf16 v[196:199], a[92:95], v[140:143], v[196:199]
buffer_load_dwordx4 v240, s[56:59], s17 offen lds
v_mfma_f32_16x16x32_bf16 v[204:207], a[92:95], v[156:159], 0
v_exp_f32 v195, v195
s_add_u32 s17, s17, 4294942720
v_mfma_f32_16x16x32_bf16 v[204:207], a[88:91], v[152:155], v[204:207]
v_cvt_pk_bf16_f32 v225, v194, v195
s_add_u32 m0, m0, 1152
v_mfma_f32_16x16x32_bf16 v[204:207], a[84:87], v[148:151], v[204:207]
v_exp_f32 v200, v200
v_mfma_f32_16x16x32_bf16 v[204:207], a[80:83], v[144:147], v[204:207]
buffer_load_dwordx4 v240, s[56:59], s17 offen lds
v_mfma_f32_16x16x32_bf16 v[208:211], a[64:67], v[160:163], 0
v_exp_f32 v201, v201
s_add_u32 s17, s17, 32768
v_mfma_f32_16x16x32_bf16 v[208:211], a[68:71], v[164:167], v[208:211]
v_cvt_pk_bf16_f32 v228, v200, v201
s_add_u32 m0, m0, 1024
v_mfma_f32_16x16x32_bf16 v[208:211], a[72:75], v[168:171], v[208:211]
v_exp_f32 v202, v202
v_mfma_f32_16x16x32_bf16 v[208:211], a[76:79], v[172:175], v[208:211]
buffer_load_dwordx4 v240, s[56:59], s17 offen lds
v_mfma_f32_16x16x32_bf16 v[216:219], a[76:79], v[188:191], 0
v_exp_f32 v203, v203
v_mfma_f32_16x16x32_bf16 v[216:219], a[72:75], v[184:187], v[216:219]
v_cvt_pk_bf16_f32 v229, v202, v203
s_mov_b32 s93, 107520
v_mfma_f32_16x16x32_bf16 v[216:219], a[68:71], v[180:183], v[216:219]
v_exp_f32 v196, v196
v_mfma_f32_16x16x32_bf16 v[216:219], a[64:67], v[176:179], v[216:219]
buffer_load_dwordx4 a[188:191], v254, s[40:43], s93 offen
v_mfma_f32_16x16x32_bf16 v[212:215], a[80:83], v[160:163], 0
v_exp_f32 v197, v197
s_bfe_u32 s17, s4, 262148
v_mfma_f32_16x16x32_bf16 v[212:215], a[84:87], v[164:167], v[212:215]
v_cvt_pk_bf16_f32 v226, v196, v197
s_xor_b32 s17, s17, s72
s_lshl_b32 s17, s17, 18
v_mfma_f32_16x16x32_bf16 v[212:215], a[88:91], v[168:171], v[212:215]
v_exp_f32 v198, v198
s_add_u32 m0, s83, 50688
v_mfma_f32_16x16x32_bf16 v[212:215], a[92:95], v[172:175], v[212:215]
v_mfma_f32_16x16x32_bf16 v[220:223], a[92:95], v[188:191], 0
v_exp_f32 v199, v199
v_mfma_f32_16x16x32_bf16 v[220:223], a[88:91], v[184:187], v[220:223]
v_cvt_pk_bf16_f32 v227, v198, v199
s_waitcnt vmcnt(5)
v_cmp_eq_u32 s[92:93], v250, v249
s_mov_b32 s94, 16384
v_mfma_f32_16x16x32_bf16 v[220:223], a[84:87], v[180:183], v[220:223]
v_exp_f32 v204, v204
v_mfma_f32_16x16x32_bf16 v[220:223], a[80:83], v[176:179], v[220:223]
s_nop 0
s_barrier
v_mfma_f32_16x16x32_bf16 v[64:67], a[96:99], v[232:235], v[64:67]
v_exp_f32 v205, v205
v_mfma_f32_16x16x32_bf16 v[96:99], a[96:99], v[236:239], v[96:99]
ds_read_b128 a[96:99], v248 offset:8192
v_cvt_pk_bf16_f32 v230, v204, v205
s_and_b32 s92, s92, 3
s_andn2_b32 s92, s92, s23
s_cmp_eq_u32 s92, 3
s_cselect_b32 s70, 17408, 0
s_cselect_b32 s84, 3, s23
v_mfma_f32_16x16x32_bf16 v[100:103], a[100:103], v[236:239], v[100:103]
v_exp_f32 v206, v206
v_mfma_f32_16x16x32_bf16 v[68:71], a[100:103], v[232:235], v[68:71]
ds_read_b128 a[100:103], v248 offset:9216
buffer_load_dwordx4 a[220:223], v255, s[68:71], s94 offen sc1
v_mfma_f32_16x16x32_bf16 v[72:75], a[104:107], v[232:235], v[72:75]
v_exp_f32 v207, v207
v_mfma_f32_16x16x32_bf16 v[104:107], a[104:107], v[236:239], v[104:107]
ds_read_b128 a[104:107], v248 offset:10240
v_cvt_pk_bf16_f32 v231, v206, v207
v_mfma_f32_16x16x32_bf16 v[108:111], a[108:111], v[236:239], v[108:111]
v_exp_f32 v208, v208
v_mfma_f32_16x16x32_bf16 v[76:79], a[108:111], v[232:235], v[76:79]
ds_read_b128 a[108:111], v248 offset:11264
buffer_load_dwordx4 a[0:3], v255, s[68:71], 0 offen offset:0 sc1
v_mfma_f32_16x16x32_bf16 v[80:83], a[112:115], v[232:235], v[80:83]
v_exp_f32 v209, v209
v_mfma_f32_16x16x32_bf16 v[112:115], a[112:115], v[236:239], v[112:115]
ds_read_b128 a[112:115], v248 offset:12288
v_mfma_f32_16x16x32_bf16 v[116:119], a[116:119], v[236:239], v[116:119]
v_exp_f32 v210, v210
v_mfma_f32_16x16x32_bf16 v[84:87], a[116:119], v[232:235], v[84:87]
ds_read_b128 a[116:119], v248 offset:13312
buffer_load_dwordx4 a[4:7], v255, s[68:71], 0 offen offset:1024 sc1
v_mfma_f32_16x16x32_bf16 v[88:91], a[120:123], v[232:235], v[88:91]
v_exp_f32 v211, v211
v_mfma_f32_16x16x32_bf16 v[120:123], a[120:123], v[236:239], v[120:123]
ds_read_b128 a[120:123], v248 offset:14336
v_mfma_f32_16x16x32_bf16 v[124:127], a[124:127], v[236:239], v[124:127]
v_exp_f32 v216, v216
v_mfma_f32_16x16x32_bf16 v[92:95], a[124:127], v[232:235], v[92:95]
ds_read_b128 a[124:127], v248 offset:15360
v_or_b32 v248, s89, v255
buffer_load_dword v250, v251, s[64:67], s20 offen sc1
v_smfmac_f32_16x16x64_bf16 a[196:199], a[208:211], v[232:239], v241
s_waitcnt lgkmcnt(4)
v_smfmac_f32_16x16x64_bf16 a[192:195], a[208:211], v[224:231], v241
ds_read_b128 a[64:67], v248 offset:0
v_mfma_f32_16x16x32_bf16 v[0:3], a[96:99], v[224:227], v[0:3]
v_exp_f32 v217, v217
v_mfma_f32_16x16x32_bf16 v[32:35], a[96:99], v[228:231], v[32:35]
ds_read_b128 a[68:71], v248 offset:1024
v_cvt_pk_bf16_f32 v232, v208, v209
buffer_load_dwordx4 a[8:11], v255, s[68:71], 0 offen offset:2048 sc1
v_mfma_f32_16x16x32_bf16 v[36:39], a[100:103], v[228:231], v[36:39]
v_exp_f32 v218, v218
v_mfma_f32_16x16x32_bf16 v[4:7], a[100:103], v[224:227], v[4:7]
ds_read_b128 a[72:75], v248 offset:2048
v_cvt_pk_bf16_f32 v233, v210, v211
v_mfma_f32_16x16x32_bf16 v[8:11], a[104:107], v[224:227], v[8:11]
v_exp_f32 v219, v219
v_mfma_f32_16x16x32_bf16 v[40:43], a[104:107], v[228:231], v[40:43]
ds_read_b128 a[76:79], v248 offset:3072
v_cvt_pk_bf16_f32 v236, v216, v217
buffer_load_dwordx4 a[12:15], v255, s[68:71], 0 offen offset:3072 sc1
v_mfma_f32_16x16x32_bf16 v[44:47], a[108:111], v[228:231], v[44:47]
v_exp_f32 v212, v212
v_mfma_f32_16x16x32_bf16 v[12:15], a[108:111], v[224:227], v[12:15]
ds_read_b128 a[80:83], v248 offset:4096
s_waitcnt lgkmcnt(5)
v_mfma_f32_16x16x32_bf16 v[16:19], a[112:115], v[224:227], v[16:19]
v_exp_f32 v213, v213
v_mfma_f32_16x16x32_bf16 v[48:51], a[112:115], v[228:231], v[48:51]
ds_read_b128 a[84:87], v248 offset:5120
v_cvt_pk_bf16_f32 v237, v218, v219
buffer_load_dwordx4 a[16:19], v255, s[68:71], s95 offen offset:0 sc1
v_mfma_f32_16x16x32_bf16 v[52:55], a[116:119], v[228:231], v[52:55]
v_exp_f32 v214, v214
v_mfma_f32_16x16x32_bf16 v[20:23], a[116:119], v[224:227], v[20:23]
ds_read_b128 a[88:91], v248 offset:6144
v_cvt_pk_bf16_f32 v234, v212, v213
v_mfma_f32_16x16x32_bf16 v[24:27], a[120:123], v[224:227], v[24:27]
v_exp_f32 v215, v215
v_mfma_f32_16x16x32_bf16 v[56:59], a[120:123], v[228:231], v[56:59]
ds_read_b128 a[92:95], v248 offset:7168
v_cvt_pk_bf16_f32 v235, v214, v215
buffer_load_dwordx4 a[20:23], v255, s[68:71], s95 offen offset:1024 sc1
v_mfma_f32_16x16x32_bf16 v[60:63], a[124:127], v[228:231], v[60:63]
v_exp_f32 v220, v220
v_mfma_f32_16x16x32_bf16 v[28:31], a[124:127], v[224:227], v[28:31]
buffer_load_dwordx4 a[24:27], v255, s[68:71], s95 offen offset:2048 sc1
s_waitcnt lgkmcnt(4)
v_mfma_f32_16x16x32_bf16 v[192:195], a[64:67], v[128:131], 0
v_exp_f32 v221, v221
v_mfma_f32_16x16x32_bf16 v[192:195], a[68:71], v[132:135], v[192:195]
v_cvt_pk_bf16_f32 v238, v220, v221
v_mfma_f32_16x16x32_bf16 v[192:195], a[72:75], v[136:139], v[192:195]
v_exp_f32 v222, v222
v_mfma_f32_16x16x32_bf16 v[192:195], a[76:79], v[140:143], v[192:195]
buffer_load_dwordx4 v240, s[56:59], s17 offen lds
v_mfma_f32_16x16x32_bf16 v[200:203], a[76:79], v[156:159], 0
v_exp_f32 v223, v223
s_add_u32 s17, s17, 32768
v_mfma_f32_16x16x32_bf16 v[200:203], a[72:75], v[152:155], v[200:203]
v_cvt_pk_bf16_f32 v239, v222, v223
s_add_u32 m0, m0, 1024
v_mfma_f32_16x16x32_bf16 v[200:203], a[68:71], v[148:151], v[200:203]
v_exp_f32 v192, v192
v_mfma_f32_16x16x32_bf16 v[200:203], a[64:67], v[144:147], v[200:203]
s_waitcnt lgkmcnt(0)
v_mfma_f32_16x16x32_bf16 v[196:199], a[80:83], v[128:131], 0
v_exp_f32 v193, v193
v_mfma_f32_16x16x32_bf16 v[196:199], a[84:87], v[132:135], v[196:199]
v_cvt_pk_bf16_f32 v224, v192, v193
s_waitcnt vmcnt(6)
v_cmp_eq_u32 s[92:93], v250, v249
v_mfma_f32_16x16x32_bf16 v[196:199], a[88:91], v[136:139], v[196:199]
v_exp_f32 v194, v194
v_mfma_f32_16x16x32_bf16 v[196:199], a[92:95], v[140:143], v[196:199]
buffer_load_dwordx4 v240, s[56:59], s17 offen lds
v_mfma_f32_16x16x32_bf16 v[204:207], a[92:95], v[156:159], 0
v_exp_f32 v195, v195
s_add_u32 s17, s17, 4294942720
v_mfma_f32_16x16x32_bf16 v[204:207], a[88:91], v[152:155], v[204:207]
v_cvt_pk_bf16_f32 v225, v194, v195
s_add_u32 m0, m0, 1152
s_and_b32 s92, s92, 3
s_sub_u32 s92, s92, s84
s_cmp_eq_u32 s92, 3
s_cbranch_scc1 .La5_LA_PS0
.La5_LA_PS1:
v_mfma_f32_16x16x32_bf16 v[204:207], a[84:87], v[148:151], v[204:207]
v_exp_f32 v200, v200
v_mfma_f32_16x16x32_bf16 v[204:207], a[80:83], v[144:147], v[204:207]
buffer_load_dwordx4 v240, s[56:59], s17 offen lds
v_mfma_f32_16x16x32_bf16 v[208:211], a[64:67], v[160:163], 0
v_exp_f32 v201, v201
s_add_u32 s17, s17, 32768
v_mfma_f32_16x16x32_bf16 v[208:211], a[68:71], v[164:167], v[208:211]
v_cvt_pk_bf16_f32 v228, v200, v201
s_add_u32 m0, m0, 1024
buffer_load_dwordx4 a[28:31], v255, s[68:71], s95 offen offset:3072 sc1
v_mfma_f32_16x16x32_bf16 v[208:211], a[72:75], v[168:171], v[208:211]
v_exp_f32 v202, v202
v_mfma_f32_16x16x32_bf16 v[208:211], a[76:79], v[172:175], v[208:211]
buffer_load_dwordx4 v240, s[56:59], s17 offen lds
v_mfma_f32_16x16x32_bf16 v[216:219], a[76:79], v[188:191], 0
v_exp_f32 v203, v203
v_mfma_f32_16x16x32_bf16 v[216:219], a[72:75], v[184:187], v[216:219]
v_cvt_pk_bf16_f32 v229, v202, v203
buffer_load_dwordx4 a[32:35], v255, s[68:71], s96 offen offset:0 sc1
v_mfma_f32_16x16x32_bf16 v[216:219], a[68:71], v[180:183], v[216:219]
v_exp_f32 v196, v196
v_mfma_f32_16x16x32_bf16 v[216:219], a[64:67], v[176:179], v[216:219]
v_mfma_f32_16x16x32_bf16 v[212:215], a[80:83], v[160:163], 0
v_exp_f32 v197, v197
v_mfma_f32_16x16x32_bf16 v[212:215], a[84:87], v[164:167], v[212:215]
v_cvt_pk_bf16_f32 v226, v196, v197
buffer_load_dword v250, v251, s[64:67], s20 offen sc1
v_mfma_f32_16x16x32_bf16 v[212:215], a[88:91], v[168:171], v[212:215]
v_exp_f32 v198, v198
v_mfma_f32_16x16x32_bf16 v[212:215], a[92:95], v[172:175], v[212:215]
buffer_load_dwordx4 a[36:39], v255, s[68:71], s96 offen offset:1024 sc1
v_mfma_f32_16x16x32_bf16 v[220:223], a[92:95], v[188:191], 0
v_exp_f32 v199, v199
v_mfma_f32_16x16x32_bf16 v[220:223], a[88:91], v[184:187], v[220:223]
v_cvt_pk_bf16_f32 v227, v198, v199
v_mfma_f32_16x16x32_bf16 v[220:223], a[84:87], v[180:183], v[220:223]
v_exp_f32 v204, v204
v_mfma_f32_16x16x32_bf16 v[220:223], a[80:83], v[176:179], v[220:223]
buffer_load_dwordx4 a[40:43], v255, s[68:71], s96 offen offset:2048 sc1
v_mfma_f32_16x16x32_bf16 v[64:67], a[96:99], v[232:235], v[64:67]
v_exp_f32 v205, v205
v_mfma_f32_16x16x32_bf16 v[96:99], a[96:99], v[236:239], v[96:99]
ds_read_b128 a[96:99], v248 offset:8192
v_cvt_pk_bf16_f32 v230, v204, v205
v_mfma_f32_16x16x32_bf16 v[100:103], a[100:103], v[236:239], v[100:103]
v_exp_f32 v206, v206
v_mfma_f32_16x16x32_bf16 v[68:71], a[100:103], v[232:235], v[68:71]
ds_read_b128 a[100:103], v248 offset:9216
buffer_load_dwordx4 a[44:47], v255, s[68:71], s96 offen offset:3072 sc1
v_mfma_f32_16x16x32_bf16 v[72:75], a[104:107], v[232:235], v[72:75]
v_exp_f32 v207, v207
v_mfma_f32_16x16x32_bf16 v[104:107], a[104:107], v[236:239], v[104:107]
ds_read_b128 a[104:107], v248 offset:10240
v_cvt_pk_bf16_f32 v231, v206, v207
v_mfma_f32_16x16x32_bf16 v[108:111], a[108:111], v[236:239], v[108:111]
v_exp_f32 v208, v208
v_mfma_f32_16x16x32_bf16 v[76:79], a[108:111], v[232:235], v[76:79]
ds_read_b128 a[108:111], v248 offset:11264
buffer_load_dwordx4 a[48:51], v255, s[68:71], s97 offen offset:0 sc1
v_mfma_f32_16x16x32_bf16 v[80:83], a[112:115], v[232:235], v[80:83]
v_exp_f32 v209, v209
v_mfma_f32_16x16x32_bf16 v[112:115], a[112:115], v[236:239], v[112:115]
ds_read_b128 a[112:115], v248 offset:12288
v_mfma_f32_16x16x32_bf16 v[116:119], a[116:119], v[236:239], v[116:119]
v_exp_f32 v210, v210
v_mfma_f32_16x16x32_bf16 v[84:87], a[116:119], v[232:235], v[84:87]
ds_read_b128 a[116:119], v248 offset:13312
buffer_load_dwordx4 a[52:55], v255, s[68:71], s97 offen offset:1024 sc1
v_mfma_f32_16x16x32_bf16 v[88:91], a[120:123], v[232:235], v[88:91]
v_exp_f32 v211, v211
v_mfma_f32_16x16x32_bf16 v[120:123], a[120:123], v[236:239], v[120:123]
ds_read_b128 a[120:123], v248 offset:14336
v_mfma_f32_16x16x32_bf16 v[124:127], a[124:127], v[236:239], v[124:127]
v_exp_f32 v216, v216
v_mfma_f32_16x16x32_bf16 v[92:95], a[124:127], v[232:235], v[92:95]
ds_read_b128 a[124:127], v248 offset:15360
buffer_load_dwordx4 a[56:59], v255, s[68:71], s97 offen offset:2048 sc1
v_smfmac_f32_16x16x64_bf16 a[196:199], a[208:211], v[232:239], v241
s_waitcnt lgkmcnt(4)
v_smfmac_f32_16x16x64_bf16 a[192:195], a[208:211], v[224:231], v241
buffer_load_dwordx4 a[60:63], v255, s[68:71], s97 offen offset:3072 sc1
v_mfma_f32_16x16x32_bf16 v[0:3], a[96:99], v[224:227], v[0:3]
v_exp_f32 v217, v217
v_mfma_f32_16x16x32_bf16 v[32:35], a[96:99], v[228:231], v[32:35]
v_cvt_pk_bf16_f32 v232, v208, v209
v_mfma_f32_16x16x32_bf16 v[36:39], a[100:103], v[228:231], v[36:39]
v_exp_f32 v218, v218
v_mfma_f32_16x16x32_bf16 v[4:7], a[100:103], v[224:227], v[4:7]
v_cvt_pk_bf16_f32 v233, v210, v211
v_mfma_f32_16x16x32_bf16 v[8:11], a[104:107], v[224:227], v[8:11]
v_exp_f32 v219, v219
v_mfma_f32_16x16x32_bf16 v[40:43], a[104:107], v[228:231], v[40:43]
v_cvt_pk_bf16_f32 v236, v216, v217
v_mfma_f32_16x16x32_bf16 v[44:47], a[108:111], v[228:231], v[44:47]
v_exp_f32 v212, v212
s_waitcnt vmcnt(7)
v_cmp_eq_u32 s[92:93], v250, v249
v_mfma_f32_16x16x32_bf16 v[12:15], a[108:111], v[224:227], v[12:15]
s_waitcnt lgkmcnt(0)
v_mfma_f32_16x16x32_bf16 v[16:19], a[112:115], v[224:227], v[16:19]
v_exp_f32 v213, v213
v_mfma_f32_16x16x32_bf16 v[48:51], a[112:115], v[228:231], v[48:51]
v_cvt_pk_bf16_f32 v237, v218, v219
s_and_b32 s92, s92, 3
s_sub_u32 s92, s92, s84
v_mfma_f32_16x16x32_bf16 v[52:55], a[116:119], v[228:231], v[52:55]
v_exp_f32 v214, v214
v_mfma_f32_16x16x32_bf16 v[20:23], a[116:119], v[224:227], v[20:23]
v_cvt_pk_bf16_f32 v234, v212, v213
v_mfma_f32_16x16x32_bf16 v[24:27], a[120:123], v[224:227], v[24:27]
v_exp_f32 v215, v215
v_mfma_f32_16x16x32_bf16 v[56:59], a[120:123], v[228:231], v[56:59]
v_cvt_pk_bf16_f32 v235, v214, v215
v_mfma_f32_16x16x32_bf16 v[60:63], a[124:127], v[228:231], v[60:63]
v_exp_f32 v220, v220
v_mfma_f32_16x16x32_bf16 v[28:31], a[124:127], v[224:227], v[28:31]
s_cmp_eq_u32 s92, 3
s_cbranch_scc1 .La5_LA_PS2
.La5_LA_PS3:
s_cmp_lg_u32 s84, 3
s_cbranch_scc1 .La5_LA_PSPIN
.La5_LA_DC1:
s_waitcnt vmcnt(8)
v_mfma_f32_16x16x32_bf16 v[0:3], a[214:217], a[0:3], v[0:3]
v_accvgpr_read_b32 v250, a192
v_accvgpr_read_b32 v251, a193
v_add_f32 v250, v250, v251
v_accvgpr_read_b32 v251, a220
v_add_f32 v250, v250, v251
v_accvgpr_read_b32 v249, a194
v_mfma_f32_16x16x32_bf16 v[4:7], a[212:215], a[0:3], v[4:7]
v_accvgpr_read_b32 v251, a195
v_add_f32 v249, v249, v251
v_accvgpr_read_b32 v251, a221
v_add_f32 v249, v249, v251
v_subrev_u32 v251, 562036736, v250
v_mfma_f32_16x16x32_bf16 v[8:11], a[214:217], a[4:7], v[8:11]
v_rcp_f32 v250, v250
v_cmp_le_u32 vcc, 1006632960, v251
v_subrev_u32 v251, 562036736, v249
s_mov_b64 s[92:93], vcc
v_cmp_le_u32 vcc, 1006632960, v251
v_mfma_f32_16x16x32_bf16 v[12:15], a[212:215], a[4:7], v[12:15]
v_rcp_f32 v249, v249
v_mfma_f32_16x16x32_bf16 v[16:19], a[214:217], a[8:11], v[16:19]
s_or_b64 vcc, vcc, s[92:93]
s_cbranch_scc1 .La5_LA_SC3
.La5_LA_SR3:
v_exp_f32 v221, v221
v_exp_f32 v222, v222
v_mfma_f32_16x16x32_bf16 v[20:23], a[212:215], a[8:11], v[20:23]
v_exp_f32 v223, v223
v_cvt_pk_bf16_f32 v238, v220, v221
v_mul_f32 v0, v0, v250
v_mul_f32 v1, v1, v250
v_mul_f32 v2, v2, v250
v_mfma_f32_16x16x32_bf16 v[24:27], a[214:217], a[12:15], v[24:27]
v_mul_f32 v3, v3, v250
v_mul_f32 v4, v4, v250
v_mul_f32 v5, v5, v250
v_mul_f32 v6, v6, v250
v_mul_f32 v7, v7, v250
v_cvt_pk_bf16_f32 v128, v0, v1
v_mfma_f32_16x16x32_bf16 v[28:31], a[212:215], a[12:15], v[28:31]
v_cvt_pk_bf16_f32 v129, v2, v3
v_cvt_pk_bf16_f32 v130, v4, v5
v_cvt_pk_bf16_f32 v131, v6, v7
buffer_store_dwordx4 v[128:131], v244, s[44:47], s91 offen offset:0 sc1
v_cvt_pk_bf16_f32 v239, v222, v223
v_mfma_f32_16x16x32_bf16 v[32:35], a[214:217], a[16:19], v[32:35]
v_mul_f32 v8, v8, v250
v_mul_f32 v9, v9, v250
v_mul_f32 v10, v10, v250
v_mul_f32 v11, v11, v250
v_mul_f32 v12, v12, v250
v_mul_f32 v13, v13, v250
v_mfma_f32_16x16x32_bf16 v[36:39], a[212:215], a[16:19], v[36:39]
v_mul_f32 v14, v14, v250
v_mul_f32 v15, v15, v250
v_cvt_pk_bf16_f32 v132, v8, v9
v_cvt_pk_bf16_f32 v133, v10, v11
v_cvt_pk_bf16_f32 v134, v12, v13
v_cvt_pk_bf16_f32 v135, v14, v15
v_mfma_f32_16x16x32_bf16 v[40:43], a[214:217], a[20:23], v[40:43]
buffer_store_dwordx4 v[132:135], v244, s[44:47], s91 offen offset:1024 sc1
v_mul_f32 v16, v16, v250
v_mul_f32 v17, v17, v250
v_mul_f32 v18, v18, v250
v_mul_f32 v19, v19, v250
v_mfma_f32_16x16x32_bf16 v[44:47], a[212:215], a[20:23], v[44:47]
v_mul_f32 v20, v20, v250
v_mul_f32 v21, v21, v250
v_mul_f32 v22, v22, v250
v_mul_f32 v23, v23, v250
v_cvt_pk_bf16_f32 v136, v16, v17
v_cvt_pk_bf16_f32 v137, v18, v19
v_mfma_f32_16x16x32_bf16 v[48:51], a[214:217], a[24:27], v[48:51]
v_cvt_pk_bf16_f32 v138, v20, v21
v_cvt_pk_bf16_f32 v139, v22, v23
buffer_store_dwordx4 v[136:139], v244, s[44:47], s91 offen offset:2048 sc1
v_mul_f32 v24, v24, v250
v_mul_f32 v25, v25, v250
v_mfma_f32_16x16x32_bf16 v[52:55], a[212:215], a[24:27], v[52:55]
v_mul_f32 v26, v26, v250
v_mul_f32 v27, v27, v250
v_mul_f32 v28, v28, v250
v_mul_f32 v29, v29, v250
v_mul_f32 v30, v30, v250
v_mul_f32 v31, v31, v250
v_mfma_f32_16x16x32_bf16 v[56:59], a[214:217], a[28:31], v[56:59]
v_cvt_pk_bf16_f32 v140, v24, v25
v_cvt_pk_bf16_f32 v141, v26, v27
v_cvt_pk_bf16_f32 v142, v28, v29
v_cvt_pk_bf16_f32 v143, v30, v31
buffer_store_dwordx4 v[140:143], v244, s[44:47], s91 offen offset:3072 sc1
v_mfma_f32_16x16x32_bf16 v[60:63], a[212:215], a[28:31], v[60:63]
v_mul_f32 v32, v32, v249
v_mul_f32 v33, v33, v249
v_mul_f32 v34, v34, v249
v_mul_f32 v35, v35, v249
v_mul_f32 v36, v36, v249
v_mul_f32 v37, v37, v249
v_smfmac_f32_16x16x64_bf16 a[196:199], a[208:211], v[232:239], v241
v_mul_f32 v38, v38, v249
v_mul_f32 v39, v39, v249
v_cvt_pk_bf16_f32 v144, v32, v33
v_cvt_pk_bf16_f32 v145, v34, v35
v_cvt_pk_bf16_f32 v146, v36, v37
v_mfma_f32_16x16x32_bf16 v[64:67], a[96:99], v[232:235], v[64:67]
v_cvt_pk_bf16_f32 v147, v38, v39
buffer_store_dwordx4 v[144:147], v245, s[44:47], s91 offen offset:0 sc1
v_mul_f32 v40, v40, v249
v_mul_f32 v41, v41, v249
v_mul_f32 v42, v42, v249
v_mfma_f32_16x16x32_bf16 v[96:99], a[96:99], v[236:239], v[96:99]
v_mul_f32 v43, v43, v249
v_mul_f32 v44, v44, v249
v_mul_f32 v45, v45, v249
v_mul_f32 v46, v46, v249
v_mul_f32 v47, v47, v249
v_cvt_pk_bf16_f32 v148, v40, v41
v_mfma_f32_16x16x32_bf16 v[100:103], a[100:103], v[236:239], v[100:103]
v_cvt_pk_bf16_f32 v149, v42, v43
v_cvt_pk_bf16_f32 v150, v44, v45
v_cvt_pk_bf16_f32 v151, v46, v47
buffer_store_dwordx4 v[148:151], v245, s[44:47], s91 offen offset:1024 sc1
v_mul_f32 v48, v48, v249
v_mfma_f32_16x16x32_bf16 v[68:71], a[100:103], v[232:235], v[68:71]
.La5_LW5_2:
s_cbranch_execz .La5_LW5_R0
v_mul_f32 v49, v49, v249
v_mul_f32 v50, v50, v249
v_mul_f32 v51, v51, v249
v_mul_f32 v52, v52, v249
v_mul_f32 v53, v53, v249
v_mul_f32 v54, v54, v249
v_mfma_f32_16x16x32_bf16 v[72:75], a[104:107], v[232:235], v[72:75]
v_mul_f32 v55, v55, v249
v_cvt_pk_bf16_f32 v152, v48, v49
v_cvt_pk_bf16_f32 v153, v50, v51
v_cvt_pk_bf16_f32 v154, v52, v53
v_cvt_pk_bf16_f32 v155, v54, v55
buffer_store_dwordx4 v[152:155], v245, s[44:47], s91 offen offset:2048 sc1
v_mfma_f32_16x16x32_bf16 v[104:107], a[104:107], v[236:239], v[104:107]
v_mul_f32 v56, v56, v249
v_mul_f32 v57, v57, v249
v_mul_f32 v58, v58, v249
v_mul_f32 v59, v59, v249
v_mul_f32 v60, v60, v249
v_mul_f32 v61, v61, v249
v_mfma_f32_16x16x32_bf16 v[108:111], a[108:111], v[236:239], v[108:111]
v_mul_f32 v62, v62, v249
v_mul_f32 v63, v63, v249
v_cvt_pk_bf16_f32 v156, v56, v57
v_cvt_pk_bf16_f32 v157, v58, v59
v_cvt_pk_bf16_f32 v158, v60, v61
v_cvt_pk_bf16_f32 v159, v62, v63
v_mfma_f32_16x16x32_bf16 v[76:79], a[108:111], v[232:235], v[76:79]
buffer_store_dwordx4 v[156:159], v245, s[44:47], s91 offen offset:3072 sc1
v_accvgpr_read_b32 v240, a224
v_xor_b32 v241, 32, v240
v_xor_b32 v242, 64, v240
v_xor_b32 v243, 96, v240
v_mfma_f32_16x16x32_bf16 v[80:83], a[112:115], v[232:235], v[80:83]
v_mfma_f32_16x16x32_bf16 v[112:115], a[112:115], v[236:239], v[112:115]
v_mfma_f32_16x16x32_bf16 v[116:119], a[116:119], v[236:239], v[116:119]
v_mfma_f32_16x16x32_bf16 v[84:87], a[116:119], v[232:235], v[84:87]
v_mfma_f32_16x16x32_bf16 v[88:91], a[120:123], v[232:235], v[88:91]
v_mfma_f32_16x16x32_bf16 v[120:123], a[120:123], v[236:239], v[120:123]
v_mfma_f32_16x16x32_bf16 v[124:127], a[124:127], v[236:239], v[124:127]
v_mfma_f32_16x16x32_bf16 v[92:95], a[124:127], v[232:235], v[92:95]
s_nop 0
s_barrier
s_waitcnt vmcnt(8)
v_mfma_f32_16x16x32_bf16 v[64:67], a[214:217], a[32:35], v[64:67]
ds_read_b64_tr_b16 v[192:193], v240 offset:16896
ds_read_b64_tr_b16 v[194:195], v240 offset:17920
v_mfma_f32_16x16x32_bf16 v[68:71], a[212:215], a[32:35], v[68:71]
ds_read_b64_tr_b16 v[196:197], v240 offset:25344
ds_read_b64_tr_b16 v[198:199], v240 offset:26368
v_mfma_f32_16x16x32_bf16 v[72:75], a[214:217], a[36:39], v[72:75]
ds_read_b64_tr_b16 v[200:201], v241 offset:16896
ds_read_b64_tr_b16 v[202:203], v241 offset:17920
v_accvgpr_read_b32 v250, a196
v_accvgpr_read_b32 v251, a197
v_add_f32 v250, v250, v251
v_accvgpr_read_b32 v251, a222
v_mfma_f32_16x16x32_bf16 v[76:79], a[212:215], a[36:39], v[76:79]
ds_read_b64_tr_b16 v[204:205], v241 offset:25344
ds_read_b64_tr_b16 v[206:207], v241 offset:26368
v_add_f32 v250, v250, v251
v_accvgpr_read_b32 v249, a198
v_accvgpr_read_b32 v251, a199
v_add_f32 v249, v249, v251
v_mfma_f32_16x16x32_bf16 v[80:83], a[214:217], a[40:43], v[80:83]
ds_read_b64_tr_b16 v[208:209], v242 offset:16896
ds_read_b64_tr_b16 v[210:211], v242 offset:17920
v_accvgpr_read_b32 v251, a223
v_add_f32 v249, v249, v251
v_subrev_u32 v251, 562036736, v250
v_mfma_f32_16x16x32_bf16 v[84:87], a[212:215], a[40:43], v[84:87]
ds_read_b64_tr_b16 v[212:213], v242 offset:25344
ds_read_b64_tr_b16 v[214:215], v242 offset:26368
v_rcp_f32 v250, v250
v_cmp_le_u32 vcc, 1006632960, v251
v_subrev_u32 v251, 562036736, v249
v_mfma_f32_16x16x32_bf16 v[88:91], a[214:217], a[44:47], v[88:91]
ds_read_b64_tr_b16 v[216:217], v243 offset:16896
ds_read_b64_tr_b16 v[218:219], v243 offset:17920
s_mov_b64 s[92:93], vcc
v_cmp_le_u32 vcc, 1006632960, v251
v_rcp_f32 v249, v249
v_mfma_f32_16x16x32_bf16 v[92:95], a[212:215], a[44:47], v[92:95]
ds_read_b64_tr_b16 v[220:221], v243 offset:25344
ds_read_b64_tr_b16 v[222:223], v243 offset:26368
s_or_b64 vcc, vcc, s[92:93]
s_cbranch_scc1 .La5_LA_SC4
.La5_LA_SR4:
v_mul_f32 v64, v64, v250
v_mul_f32 v65, v65, v250
v_mfma_f32_16x16x32_bf16 v[96:99], a[214:217], a[48:51], v[96:99]
v_mul_f32 v66, v66, v250
v_mul_f32 v67, v67, v250
v_mul_f32 v68, v68, v250
v_mul_f32 v69, v69, v250
v_mul_f32 v70, v70, v250
v_mul_f32 v71, v71, v250
v_mfma_f32_16x16x32_bf16 v[100:103], a[212:215], a[48:51], v[100:103]
v_cvt_pk_bf16_f32 v160, v64, v65
v_cvt_pk_bf16_f32 v161, v66, v67
v_cvt_pk_bf16_f32 v162, v68, v69
v_cvt_pk_bf16_f32 v163, v70, v71
buffer_store_dwordx4 v[160:163], v246, s[44:47], s91 offen offset:0 sc1
v_mfma_f32_16x16x32_bf16 v[104:107], a[214:217], a[52:55], v[104:107]
v_mul_f32 v72, v72, v250
v_mul_f32 v73, v73, v250
v_mul_f32 v74, v74, v250
v_mul_f32 v75, v75, v250
v_mul_f32 v76, v76, v250
v_mul_f32 v77, v77, v250
v_mfma_f32_16x16x32_bf16 v[108:111], a[212:215], a[52:55], v[108:111]
v_mul_f32 v78, v78, v250
v_mul_f32 v79, v79, v250
v_cvt_pk_bf16_f32 v164, v72, v73
v_cvt_pk_bf16_f32 v165, v74, v75
v_cvt_pk_bf16_f32 v166, v76, v77
v_cvt_pk_bf16_f32 v167, v78, v79
v_mfma_f32_16x16x32_bf16 v[112:115], a[214:217], a[56:59], v[112:115]
buffer_store_dwordx4 v[164:167], v246, s[44:47], s91 offen offset:1024 sc1
v_mul_f32 v80, v80, v250
v_mul_f32 v81, v81, v250
v_mul_f32 v82, v82, v250
v_mul_f32 v83, v83, v250
v_mfma_f32_16x16x32_bf16 v[116:119], a[212:215], a[56:59], v[116:119]
v_mul_f32 v84, v84, v250
v_mul_f32 v85, v85, v250
v_mul_f32 v86, v86, v250
v_mul_f32 v87, v87, v250
v_cvt_pk_bf16_f32 v168, v80, v81
v_cvt_pk_bf16_f32 v169, v82, v83
v_mfma_f32_16x16x32_bf16 v[120:123], a[214:217], a[60:63], v[120:123]
v_cvt_pk_bf16_f32 v170, v84, v85
v_cvt_pk_bf16_f32 v171, v86, v87
buffer_store_dwordx4 v[168:171], v246, s[44:47], s91 offen offset:2048 sc1
v_mul_f32 v88, v88, v250
v_mul_f32 v89, v89, v250
v_mfma_f32_16x16x32_bf16 v[124:127], a[212:215], a[60:63], v[124:127]
s_waitcnt lgkmcnt(12)
v_mul_f32 v90, v90, v250
v_mul_f32 v91, v91, v250
v_mul_f32 v92, v92, v250
v_mul_f32 v93, v93, v250
v_mul_f32 v94, v94, v250
v_mfma_f32_16x16x32_bf16 v[0:3], v[192:195], v[128:131], 0
v_mul_f32 v95, v95, v250
v_cvt_pk_bf16_f32 v172, v88, v89
v_cvt_pk_bf16_f32 v173, v90, v91
v_cvt_pk_bf16_f32 v174, v92, v93
v_cvt_pk_bf16_f32 v175, v94, v95
buffer_store_dwordx4 v[172:175], v246, s[44:47], s91 offen offset:3072 sc1
v_mfma_f32_16x16x32_bf16 v[0:3], v[196:199], v[132:135], v[0:3]
v_mul_f32 v96, v96, v249
v_mul_f32 v97, v97, v249
v_mul_f32 v98, v98, v249
v_mul_f32 v99, v99, v249
v_mul_f32 v100, v100, v249
v_mul_f32 v101, v101, v249
v_mfma_f32_16x16x32_bf16 v[32:35], v[196:199], v[148:151], 0
s_waitcnt lgkmcnt(8)
v_mul_f32 v102, v102, v249
v_mul_f32 v103, v103, v249
v_cvt_pk_bf16_f32 v176, v96, v97
v_cvt_pk_bf16_f32 v177, v98, v99
v_cvt_pk_bf16_f32 v178, v100, v101
v_mfma_f32_16x16x32_bf16 v[32:35], v[192:195], v[144:147], v[32:35]
ds_read_b64_tr_b16 v[196:197], v240 offset:17024
ds_read_b64_tr_b16 v[198:199], v240 offset:18048
v_cvt_pk_bf16_f32 v179, v102, v103
buffer_store_dwordx4 v[176:179], v247, s[44:47], s91 offen offset:0 sc1
v_mul_f32 v104, v104, v249
v_mfma_f32_16x16x32_bf16 v[36:39], v[200:203], v[144:147], 0
v_mul_f32 v105, v105, v249
v_mul_f32 v106, v106, v249
v_mul_f32 v107, v107, v249
v_mul_f32 v108, v108, v249
v_mul_f32 v109, v109, v249
v_mul_f32 v110, v110, v249
v_mfma_f32_16x16x32_bf16 v[36:39], v[204:207], v[148:151], v[36:39]
ds_read_b64_tr_b16 v[192:193], v240 offset:25472
ds_read_b64_tr_b16 v[194:195], v240 offset:26496
v_mul_f32 v111, v111, v249
v_cvt_pk_bf16_f32 v180, v104, v105
v_cvt_pk_bf16_f32 v181, v106, v107
v_cvt_pk_bf16_f32 v182, v108, v109
v_mfma_f32_16x16x32_bf16 v[4:7], v[204:207], v[132:135], 0
s_waitcnt lgkmcnt(4)
v_cvt_pk_bf16_f32 v183, v110, v111
buffer_store_dwordx4 v[180:183], v247, s[44:47], s91 offen offset:1024 sc1
v_mul_f32 v112, v112, v249
v_mul_f32 v113, v113, v249
v_mfma_f32_16x16x32_bf16 v[4:7], v[200:203], v[128:131], v[4:7]
ds_read_b64_tr_b16 v[204:205], v241 offset:17024
ds_read_b64_tr_b16 v[206:207], v241 offset:18048
v_mul_f32 v114, v114, v249
v_mul_f32 v115, v115, v249
v_mul_f32 v116, v116, v249
v_mul_f32 v117, v117, v249
v_mfma_f32_16x16x32_bf16 v[8:11], v[208:211], v[128:131], 0
v_mul_f32 v118, v118, v249
v_mul_f32 v119, v119, v249
v_cvt_pk_bf16_f32 v184, v112, v113
v_cvt_pk_bf16_f32 v185, v114, v115
v_cvt_pk_bf16_f32 v186, v116, v117
v_cvt_pk_bf16_f32 v187, v118, v119
v_mfma_f32_16x16x32_bf16 v[8:11], v[212:215], v[132:135], v[8:11]
ds_read_b64_tr_b16 v[200:201], v241 offset:25472
ds_read_b64_tr_b16 v[202:203], v241 offset:26496
buffer_store_dwordx4 v[184:187], v247, s[44:47], s91 offen offset:2048 sc1
v_mul_f32 v120, v120, v249
v_mul_f32 v121, v121, v249
v_mfma_f32_16x16x32_bf16 v[40:43], v[212:215], v[148:151], 0
v_mul_f32 v122, v122, v249
v_mul_f32 v123, v123, v249
v_mul_f32 v124, v124, v249
v_mul_f32 v125, v125, v249
v_mul_f32 v126, v126, v249
v_mul_f32 v127, v127, v249
v_mfma_f32_16x16x32_bf16 v[40:43], v[208:211], v[144:147], v[40:43]
ds_read_b64_tr_b16 v[212:213], v242 offset:17024
ds_read_b64_tr_b16 v[214:215], v242 offset:18048
v_cvt_pk_bf16_f32 v188, v120, v121
v_cvt_pk_bf16_f32 v189, v122, v123
v_cvt_pk_bf16_f32 v190, v124, v125
v_cvt_pk_bf16_f32 v191, v126, v127
v_mfma_f32_16x16x32_bf16 v[44:47], v[216:219], v[144:147], 0
buffer_store_dwordx4 v[188:191], v247, s[44:47], s91 offen offset:3072 sc1
v_mfma_f32_16x16x32_bf16 v[44:47], v[220:223], v[148:151], v[44:47]
ds_read_b64_tr_b16 v[208:209], v242 offset:25472
ds_read_b64_tr_b16 v[210:211], v242 offset:26496
v_mfma_f32_16x16x32_bf16 v[12:15], v[220:223], v[132:135], 0
s_waitcnt lgkmcnt(4)
v_mfma_f32_16x16x32_bf16 v[12:15], v[216:219], v[128:131], v[12:15]
ds_read_b64_tr_b16 v[220:221], v243 offset:17024
ds_read_b64_tr_b16 v[222:223], v243 offset:18048
v_mfma_f32_16x16x32_bf16 v[16:19], v[196:199], v[128:131], 0
v_accvgpr_read_b32 v224, a228
v_add_u32 v225, 0x8000, v224
v_add_u32 v226, 0x2000, v224
v_add_u32 v227, 0xa000, v224
v_mfma_f32_16x16x32_bf16 v[16:19], v[192:195], v[132:135], v[16:19]
ds_read_b64_tr_b16 v[216:217], v243 offset:25472
ds_read_b64_tr_b16 v[218:219], v243 offset:26496
v_accvgpr_read_b32 v248, a225
v_accvgpr_read_b32 v228, a229
v_mfma_f32_16x16x32_bf16 v[48:51], v[192:195], v[148:151], 0
v_accvgpr_read_b32 v229, a230
v_mfma_f32_16x16x32_bf16 v[48:51], v[196:199], v[144:147], v[48:51]
ds_read_b64_tr_b16 v[192:193], v240 offset:33792
ds_read_b64_tr_b16 v[194:195], v240 offset:34816
v_mfma_f32_16x16x32_bf16 v[52:55], v[204:207], v[144:147], 0
v_mfma_f32_16x16x32_bf16 v[52:55], v[200:203], v[148:151], v[52:55]
ds_read_b64_tr_b16 v[196:197], v240 offset:42240
ds_read_b64_tr_b16 v[198:199], v240 offset:43264
v_mfma_f32_16x16x32_bf16 v[20:23], v[200:203], v[132:135], 0
s_waitcnt lgkmcnt(4)
v_mfma_f32_16x16x32_bf16 v[20:23], v[204:207], v[128:131], v[20:23]
ds_read_b64_tr_b16 v[200:201], v241 offset:33792
ds_read_b64_tr_b16 v[202:203], v241 offset:34816
v_mfma_f32_16x16x32_bf16 v[24:27], v[212:215], v[128:131], 0
v_mfma_f32_16x16x32_bf16 v[24:27], v[208:211], v[132:135], v[24:27]
ds_read_b64_tr_b16 v[204:205], v241 offset:42240
ds_read_b64_tr_b16 v[206:207], v241 offset:43264
v_mfma_f32_16x16x32_bf16 v[56:59], v[208:211], v[148:151], 0
v_mfma_f32_16x16x32_bf16 v[56:59], v[212:215], v[144:147], v[56:59]
ds_read_b64_tr_b16 v[208:209], v242 offset:33792
ds_read_b64_tr_b16 v[210:211], v242 offset:34816
v_mfma_f32_16x16x32_bf16 v[60:63], v[220:223], v[144:147], 0
v_mfma_f32_16x16x32_bf16 v[60:63], v[216:219], v[148:151], v[60:63]
ds_read_b64_tr_b16 v[212:213], v242 offset:42240
ds_read_b64_tr_b16 v[214:215], v242 offset:43264
v_mfma_f32_16x16x32_bf16 v[28:31], v[216:219], v[132:135], 0
s_waitcnt lgkmcnt(4)
v_mfma_f32_16x16x32_bf16 v[28:31], v[220:223], v[128:131], v[28:31]
ds_read_b64_tr_b16 v[216:217], v243 offset:33792
ds_read_b64_tr_b16 v[218:219], v243 offset:34816
v_mfma_f32_16x16x32_bf16 v[0:3], v[192:195], v[136:139], v[0:3]
v_mfma_f32_16x16x32_bf16 v[0:3], v[196:199], v[140:143], v[0:3]
ds_read_b64_tr_b16 v[220:221], v243 offset:42240
ds_read_b64_tr_b16 v[222:223], v243 offset:43264
v_mfma_f32_16x16x32_bf16 v[32:35], v[196:199], v[156:159], v[32:35]
v_mfma_f32_16x16x32_bf16 v[32:35], v[192:195], v[152:155], v[32:35]
ds_read_b64_tr_b16 v[196:197], v240 offset:33920
ds_read_b64_tr_b16 v[198:199], v240 offset:34944
v_mfma_f32_16x16x32_bf16 v[36:39], v[200:203], v[152:155], v[36:39]
v_mfma_f32_16x16x32_bf16 v[36:39], v[204:207], v[156:159], v[36:39]
ds_read_b64_tr_b16 v[192:193], v240 offset:42368
ds_read_b64_tr_b16 v[194:195], v240 offset:43392
v_mfma_f32_16x16x32_bf16 v[4:7], v[204:207], v[140:143], v[4:7]
s_waitcnt lgkmcnt(4)
v_mfma_f32_16x16x32_bf16 v[4:7], v[200:203], v[136:139], v[4:7]
ds_read_b64_tr_b16 v[204:205], v241 offset:33920
ds_read_b64_tr_b16 v[206:207], v241 offset:34944
v_mfma_f32_16x16x32_bf16 v[8:11], v[208:211], v[136:139], v[8:11]
v_mfma_f32_16x16x32_bf16 v[8:11], v[212:215], v[140:143], v[8:11]
ds_read_b64_tr_b16 v[200:201], v241 offset:42368
ds_read_b64_tr_b16 v[202:203], v241 offset:43392
v_mfma_f32_16x16x32_bf16 v[40:43], v[212:215], v[156:159], v[40:43]
v_accvgpr_read_b32 v249, a231
s_waitcnt vmcnt(0)
buffer_store_dwordx2 v[252:253], v249, s[64:67], s6 offen offset:0 sc1
v_mfma_f32_16x16x32_bf16 v[40:43], v[208:211], v[152:155], v[40:43]
ds_read_b64_tr_b16 v[212:213], v242 offset:33920
ds_read_b64_tr_b16 v[214:215], v242 offset:34944
v_mfma_f32_16x16x32_bf16 v[44:47], v[216:219], v[152:155], v[44:47]
v_mfma_f32_16x16x32_bf16 v[44:47], v[220:223], v[156:159], v[44:47]
ds_read_b64_tr_b16 v[208:209], v242 offset:42368
ds_read_b64_tr_b16 v[210:211], v242 offset:43392
v_mfma_f32_16x16x32_bf16 v[12:15], v[220:223], v[140:143], v[12:15]
s_waitcnt lgkmcnt(4)
v_mfma_f32_16x16x32_bf16 v[12:15], v[216:219], v[136:139], v[12:15]
ds_read_b64_tr_b16 v[220:221], v243 offset:33920
ds_read_b64_tr_b16 v[222:223], v243 offset:34944
v_mfma_f32_16x16x32_bf16 v[16:19], v[196:199], v[136:139], v[16:19]
v_mfma_f32_16x16x32_bf16 v[16:19], v[192:195], v[140:143], v[16:19]
ds_read_b64_tr_b16 v[216:217], v243 offset:42368
ds_read_b64_tr_b16 v[218:219], v243 offset:43392
v_mfma_f32_16x16x32_bf16 v[48:51], v[192:195], v[156:159], v[48:51]
v_mfma_f32_16x16x32_bf16 v[48:51], v[196:199], v[152:155], v[48:51]
ds_read_b64_tr_b16 v[192:193], v240 offset:16896
ds_read_b64_tr_b16 v[194:195], v240 offset:17920
v_mfma_f32_16x16x32_bf16 v[52:55], v[204:207], v[152:155], v[52:55]
v_mfma_f32_16x16x32_bf16 v[52:55], v[200:203], v[156:159], v[52:55]
ds_read_b64_tr_b16 v[196:197], v240 offset:25344
ds_read_b64_tr_b16 v[198:199], v240 offset:26368
v_mfma_f32_16x16x32_bf16 v[20:23], v[200:203], v[140:143], v[20:23]
s_waitcnt lgkmcnt(4)
v_mfma_f32_16x16x32_bf16 v[20:23], v[204:207], v[136:139], v[20:23]
ds_read_b64_tr_b16 v[200:201], v241 offset:16896
ds_read_b64_tr_b16 v[202:203], v241 offset:17920
v_mfma_f32_16x16x32_bf16 v[24:27], v[212:215], v[136:139], v[24:27]
v_mfma_f32_16x16x32_bf16 v[24:27], v[208:211], v[140:143], v[24:27]
ds_read_b64_tr_b16 v[204:205], v241 offset:25344
ds_read_b64_tr_b16 v[206:207], v241 offset:26368
v_mfma_f32_16x16x32_bf16 v[56:59], v[208:211], v[156:159], v[56:59]
v_mfma_f32_16x16x32_bf16 v[56:59], v[212:215], v[152:155], v[56:59]
ds_read_b64_tr_b16 v[208:209], v242 offset:16896
ds_read_b64_tr_b16 v[210:211], v242 offset:17920
v_mfma_f32_16x16x32_bf16 v[60:63], v[220:223], v[152:155], v[60:63]
v_mfma_f32_16x16x32_bf16 v[60:63], v[216:219], v[156:159], v[60:63]
ds_read_b64_tr_b16 v[212:213], v242 offset:25344
ds_read_b64_tr_b16 v[214:215], v242 offset:26368
v_mfma_f32_16x16x32_bf16 v[28:31], v[216:219], v[140:143], v[28:31]
s_waitcnt lgkmcnt(4)
v_mfma_f32_16x16x32_bf16 v[28:31], v[220:223], v[136:139], v[28:31]
ds_read_b64_tr_b16 v[216:217], v243 offset:16896
ds_read_b64_tr_b16 v[218:219], v243 offset:17920
s_cmp_lg_u32 s30, 0
s_cbranch_scc1 .La5_LO_W
; ---- O projection: 256 x 128 tile over eight heads
.La5_LO_START:
.La5_LW6_7:
s_cbranch_execz .La5_LW6_R0
v_mfma_f32_16x16x32_bf16 v[64:67], v[192:195], v[160:163], 0
buffer_load_dwordx2 v[250:251], v248, s[64:67], s5 offen sc1
v_mfma_f32_16x16x32_bf16 v[64:67], v[196:199], v[164:167], v[64:67]
ds_read_b64_tr_b16 v[220:221], v243 offset:25344
ds_read_b64_tr_b16 v[222:223], v243 offset:26368
v_mfma_f32_16x16x32_bf16 v[96:99], v[196:199], v[180:183], 0
s_add_u32 m0, s83, 0
s_bfe_u32 s80, s4, 262148
v_mfma_f32_16x16x32_bf16 v[96:99], v[192:195], v[176:179], v[96:99]
ds_read_b64_tr_b16 v[196:197], v240 offset:17024
ds_read_b64_tr_b16 v[198:199], v240 offset:18048
s_xor_b32 s80, s80, s72
s_lshl_b32 s80, s80, 18
s_add_u32 s80, s80, 131072
v_mfma_f32_16x16x32_bf16 v[100:103], v[200:203], v[176:179], 0
buffer_load_dwordx4 v224, s[56:59], s80 offen lds
v_mfma_f32_16x16x32_bf16 v[100:103], v[204:207], v[180:183], v[100:103]
ds_read_b64_tr_b16 v[192:193], v240 offset:25472
ds_read_b64_tr_b16 v[194:195], v240 offset:26496
v_mfma_f32_16x16x32_bf16 v[68:71], v[204:207], v[164:167], 0
s_add_u32 m0, s83, 1024
s_waitcnt lgkmcnt(4)
v_mfma_f32_16x16x32_bf16 v[68:71], v[200:203], v[160:163], v[68:71]
ds_read_b64_tr_b16 v[204:205], v241 offset:17024
ds_read_b64_tr_b16 v[206:207], v241 offset:18048
v_mfma_f32_16x16x32_bf16 v[72:75], v[208:211], v[160:163], 0
buffer_load_dwordx4 v225, s[56:59], s80 offen lds
v_mfma_f32_16x16x32_bf16 v[72:75], v[212:215], v[164:167], v[72:75]
ds_read_b64_tr_b16 v[200:201], v241 offset:25472
ds_read_b64_tr_b16 v[202:203], v241 offset:26496
v_mfma_f32_16x16x32_bf16 v[104:107], v[212:215], v[180:183], 0
s_add_u32 m0, s83, 2176
s_bfe_u32 s53, s4, 262148
v_mfma_f32_16x16x32_bf16 v[104:107], v[208:211], v[176:179], v[104:107]
ds_read_b64_tr_b16 v[212:213], v242 offset:17024
ds_read_b64_tr_b16 v[214:215], v242 offset:18048
v_mfma_f32_16x16x32_bf16 v[108:111], v[216:219], v[176:179], 0
buffer_load_dwordx4 v226, s[56:59], s80 offen lds
v_mfma_f32_16x16x32_bf16 v[108:111], v[220:223], v[180:183], v[108:111]
ds_read_b64_tr_b16 v[208:209], v242 offset:25472
ds_read_b64_tr_b16 v[210:211], v242 offset:26496
v_mfma_f32_16x16x32_bf16 v[76:79], v[220:223], v[164:167], 0
s_add_u32 m0, s83, 3200
s_waitcnt lgkmcnt(4)
v_mfma_f32_16x16x32_bf16 v[76:79], v[216:219], v[160:163], v[76:79]
ds_read_b64_tr_b16 v[220:221], v243 offset:17024
ds_read_b64_tr_b16 v[222:223], v243 offset:18048
v_mfma_f32_16x16x32_bf16 v[80:83], v[196:199], v[160:163], 0
buffer_load_dwordx4 v227, s[56:59], s80 offen lds
v_mfma_f32_16x16x32_bf16 v[80:83], v[192:195], v[164:167], v[80:83]
ds_read_b64_tr_b16 v[216:217], v243 offset:25472
ds_read_b64_tr_b16 v[218:219], v243 offset:26496
v_mfma_f32_16x16x32_bf16 v[112:115], v[192:195], v[180:183], 0
s_add_u32 m0, s83, 118272
s_bfe_u32 s80, s4, 262152
v_mfma_f32_16x16x32_bf16 v[112:115], v[196:199], v[176:179], v[112:115]
ds_read_b64_tr_b16 v[192:193], v240 offset:33792
ds_read_b64_tr_b16 v[194:195], v240 offset:34816
s_xor_b32 s80, s80, s72
s_lshl_b32 s80, s80, 18
v_mfma_f32_16x16x32_bf16 v[116:119], v[204:207], v[176:179], 0
buffer_load_dwordx4 v224, s[56:59], s80 offen lds
v_mfma_f32_16x16x32_bf16 v[116:119], v[200:203], v[180:183], v[116:119]
s_mov_b64 exec, 0
s_cmp_eq_u32 s3, 0
s_cbranch_scc1 .La5_LW3_0
s_cmp_eq_u32 s3, 1
s_cbranch_scc1 .La5_LW3_1
s_cmp_eq_u32 s3, 2
s_cbranch_scc1 .La5_LW3_2
s_branch .La5_LW3_3
.La5_LW3_R0:
s_mov_b64 exec, -1
ds_read_b64_tr_b16 v[196:197], v240 offset:42240
ds_read_b64_tr_b16 v[198:199], v240 offset:43264
v_mfma_f32_16x16x32_bf16 v[84:87], v[200:203], v[164:167], 0
s_waitcnt vmcnt(5)
v_cmp_eq_u64 vcc, v[250:251], v[252:253]
s_nop 0
s_bitcmp1_b32 vcc_lo, 1
s_cbranch_scc0 .La5_LO_N1
.La5_LO_K1:
s_waitcnt lgkmcnt(4)
v_mfma_f32_16x16x32_bf16 v[84:87], v[204:207], v[160:163], v[84:87]
ds_read_b64_tr_b16 v[200:201], v241 offset:33792
ds_read_b64_tr_b16 v[202:203], v241 offset:34816
s_xor_b32 s53, s53, s72
s_lshl_b32 s53, s53, 13
v_mfma_f32_16x16x32_bf16 v[88:91], v[212:215], v[160:163], 0
buffer_load_dwordx4 a[0:3], v244, s[44:47], s53 offen offset:0 sc1
v_mfma_f32_16x16x32_bf16 v[88:91], v[208:211], v[164:167], v[88:91]
ds_read_b64_tr_b16 v[204:205], v241 offset:42240
ds_read_b64_tr_b16 v[206:207], v241 offset:43264
v_mfma_f32_16x16x32_bf16 v[120:123], v[208:211], v[180:183], 0
buffer_load_dwordx4 a[4:7], v245, s[44:47], s53 offen offset:0 sc1
v_mfma_f32_16x16x32_bf16 v[120:123], v[212:215], v[176:179], v[120:123]
ds_read_b64_tr_b16 v[208:209], v242 offset:33792
ds_read_b64_tr_b16 v[210:211], v242 offset:34816
v_mfma_f32_16x16x32_bf16 v[124:127], v[220:223], v[176:179], 0
buffer_load_dwordx4 a[8:11], v246, s[44:47], s53 offen offset:0 sc1
v_mfma_f32_16x16x32_bf16 v[124:127], v[216:219], v[180:183], v[124:127]
ds_read_b64_tr_b16 v[212:213], v242 offset:42240
ds_read_b64_tr_b16 v[214:215], v242 offset:43264
v_mfma_f32_16x16x32_bf16 v[92:95], v[216:219], v[164:167], 0
s_waitcnt lgkmcnt(4)
s_bfe_u32 s54, s4, 262152
v_mfma_f32_16x16x32_bf16 v[92:95], v[220:223], v[160:163], v[92:95]
ds_read_b64_tr_b16 v[216:217], v243 offset:33792
ds_read_b64_tr_b16 v[218:219], v243 offset:34816
v_mfma_f32_16x16x32_bf16 v[64:67], v[192:195], v[168:171], v[64:67]
buffer_load_dwordx4 a[12:15], v247, s[44:47], s53 offen offset:0 sc1
v_mfma_f32_16x16x32_bf16 v[64:67], v[196:199], v[172:175], v[64:67]
ds_read_b64_tr_b16 v[220:221], v243 offset:42240
ds_read_b64_tr_b16 v[222:223], v243 offset:43264
v_mfma_f32_16x16x32_bf16 v[96:99], v[196:199], v[188:191], v[96:99]
buffer_load_dwordx4 a[16:19], v244, s[44:47], s53 offen offset:1024 sc1
v_mfma_f32_16x16x32_bf16 v[96:99], v[192:195], v[184:187], v[96:99]
ds_read_b64_tr_b16 v[196:197], v240 offset:33920
ds_read_b64_tr_b16 v[198:199], v240 offset:34944
v_mfma_f32_16x16x32_bf16 v[100:103], v[200:203], v[184:187], v[100:103]
buffer_load_dwordx4 a[20:23], v245, s[44:47], s53 offen offset:1024 sc1
v_mfma_f32_16x16x32_bf16 v[100:103], v[204:207], v[188:191], v[100:103]
ds_read_b64_tr_b16 v[192:193], v240 offset:42368
ds_read_b64_tr_b16 v[194:195], v240 offset:43392
v_mfma_f32_16x16x32_bf16 v[68:71], v[204:207], v[172:175], v[68:71]
s_waitcnt lgkmcnt(4)
s_xor_b32 s54, s54, s72
v_mfma_f32_16x16x32_bf16 v[68:71], v[200:203], v[168:171], v[68:71]
ds_read_b64_tr_b16 v[204:205], v241 offset:33920
ds_read_b64_tr_b16 v[206:207], v241 offset:34944
v_mfma_f32_16x16x32_bf16 v[72:75], v[208:211], v[168:171], v[72:75]
buffer_load_dwordx4 a[24:27], v246, s[44:47], s53 offen offset:1024 sc1
v_mfma_f32_16x16x32_bf16 v[72:75], v[212:215], v[172:175], v[72:75]
ds_read_b64_tr_b16 v[200:201], v241 offset:42368
ds_read_b64_tr_b16 v[202:203], v241 offset:43392
v_mfma_f32_16x16x32_bf16 v[104:107], v[212:215], v[188:191], v[104:107]
buffer_load_dwordx4 a[28:31], v247, s[44:47], s53 offen offset:1024 sc1
s_add_u32 m0, s83, 119296
v_mfma_f32_16x16x32_bf16 v[104:107], v[208:211], v[184:187], v[104:107]
ds_read_b64_tr_b16 v[212:213], v242 offset:33920
ds_read_b64_tr_b16 v[214:215], v242 offset:34944
v_mfma_f32_16x16x32_bf16 v[108:111], v[216:219], v[184:187], v[108:111]
buffer_load_dwordx4 v225, s[56:59], s80 offen lds
v_mfma_f32_16x16x32_bf16 v[108:111], v[220:223], v[188:191], v[108:111]
ds_read_b64_tr_b16 v[208:209], v242 offset:42368
ds_read_b64_tr_b16 v[210:211], v242 offset:43392
v_mfma_f32_16x16x32_bf16 v[76:79], v[220:223], v[172:175], v[76:79]
s_add_u32 m0, s83, 120448
s_nop 0
s_barrier
s_waitcnt lgkmcnt(4)
v_mfma_f32_16x16x32_bf16 v[76:79], v[216:219], v[168:171], v[76:79]
ds_read_b64_tr_b16 v[220:221], v243 offset:33920
ds_read_b64_tr_b16 v[222:223], v243 offset:34944
v_mfma_f32_16x16x32_bf16 v[80:83], v[196:199], v[168:171], v[80:83]
buffer_load_dwordx4 v226, s[56:59], s80 offen lds
v_mfma_f32_16x16x32_bf16 v[80:83], v[192:195], v[172:175], v[80:83]
ds_read_b64_tr_b16 v[216:217], v243 offset:42368
ds_read_b64_tr_b16 v[218:219], v243 offset:43392
v_mfma_f32_16x16x32_bf16 v[112:115], v[192:195], v[188:191], v[112:115]
buffer_load_dwordx4 a[32:35], v244, s[44:47], s53 offen offset:2048 sc1
v_mfma_f32_16x16x32_bf16 v[112:115], v[196:199], v[184:187], v[112:115]
ds_read_b64_tr_b16 v[192:193], v240 offset:50688
ds_read_b64_tr_b16 v[194:195], v240 offset:51712
v_mfma_f32_16x16x32_bf16 v[116:119], v[204:207], v[184:187], v[116:119]
buffer_load_dwordx4 a[36:39], v245, s[44:47], s53 offen offset:2048 sc1
v_mfma_f32_16x16x32_bf16 v[116:119], v[200:203], v[188:191], v[116:119]
ds_read_b64_tr_b16 v[196:197], v240 offset:59136
ds_read_b64_tr_b16 v[198:199], v240 offset:60160
v_mfma_f32_16x16x32_bf16 v[84:87], v[200:203], v[172:175], v[84:87]
s_waitcnt lgkmcnt(4)
s_lshl_b32 s54, s54, 13
v_mfma_f32_16x16x32_bf16 v[84:87], v[204:207], v[168:171], v[84:87]
ds_read_b64_tr_b16 v[200:201], v241 offset:50688
ds_read_b64_tr_b16 v[202:203], v241 offset:51712
v_mfma_f32_16x16x32_bf16 v[88:91], v[212:215], v[168:171], v[88:91]
buffer_load_dwordx4 a[40:43], v246, s[44:47], s53 offen offset:2048 sc1
v_mfma_f32_16x16x32_bf16 v[88:91], v[208:211], v[172:175], v[88:91]
ds_read_b64_tr_b16 v[204:205], v241 offset:59136
ds_read_b64_tr_b16 v[206:207], v241 offset:60160
v_mfma_f32_16x16x32_bf16 v[120:123], v[208:211], v[188:191], v[120:123]
buffer_load_dwordx4 a[44:47], v247, s[44:47], s53 offen offset:2048 sc1
v_mfma_f32_16x16x32_bf16 v[120:123], v[212:215], v[184:187], v[120:123]
ds_read_b64_tr_b16 v[208:209], v242 offset:50688
ds_read_b64_tr_b16 v[210:211], v242 offset:51712
v_mfma_f32_16x16x32_bf16 v[124:127], v[220:223], v[184:187], v[124:127]
buffer_load_dwordx4 a[48:51], v244, s[44:47], s53 offen offset:3072 sc1
v_mfma_f32_16x16x32_bf16 v[124:127], v[216:219], v[188:191], v[124:127]
ds_read_b64_tr_b16 v[212:213], v242 offset:59136
ds_read_b64_tr_b16 v[214:215], v242 offset:60160
v_mfma_f32_16x16x32_bf16 v[92:95], v[216:219], v[172:175], v[92:95]
s_waitcnt vmcnt(7) lgkmcnt(4)
s_bfe_u32 s55, s4, 262156
v_mfma_f32_16x16x32_bf16 v[92:95], v[220:223], v[168:171], v[92:95]
ds_read_b64_tr_b16 v[216:217], v243 offset:50688
ds_read_b64_tr_b16 v[218:219], v243 offset:51712
v_mfma_f32_16x16x32_bf16 v[0:3], v[192:195], a[0:3], v[0:3]
s_xor_b32 s55, s55, s72
s_lshl_b32 s55, s55, 13
v_mfma_f32_16x16x32_bf16 v[0:3], v[196:199], a[16:19], v[0:3]
buffer_load_dwordx4 a[52:55], v245, s[44:47], s53 offen offset:3072 sc1
v_mfma_f32_16x16x32_bf16 v[32:35], v[196:199], a[20:23], v[32:35]
s_bfe_u32 s52, s4, 262160
s_xor_b32 s52, s52, s72
v_mfma_f32_16x16x32_bf16 v[32:35], v[192:195], a[4:7], v[32:35]
ds_read_b64_tr_b16 v[220:221], v243 offset:59136
ds_read_b64_tr_b16 v[222:223], v243 offset:60160
v_mfma_f32_16x16x32_bf16 v[64:67], v[192:195], a[8:11], v[64:67]
s_lshl_b32 s52, s52, 13
v_mfma_f32_16x16x32_bf16 v[64:67], v[196:199], a[24:27], v[64:67]
buffer_load_dwordx4 a[56:59], v246, s[44:47], s53 offen offset:3072 sc1
v_mfma_f32_16x16x32_bf16 v[96:99], v[196:199], a[28:31], v[96:99]
v_mfma_f32_16x16x32_bf16 v[96:99], v[192:195], a[12:15], v[96:99]
ds_read_b64_tr_b16 v[196:197], v240 offset:50816
ds_read_b64_tr_b16 v[198:199], v240 offset:51840
v_mfma_f32_16x16x32_bf16 v[100:103], v[200:203], a[12:15], v[100:103]
v_mfma_f32_16x16x32_bf16 v[100:103], v[204:207], a[28:31], v[100:103]
buffer_load_dwordx2 v[250:251], v248, s[64:67], s5 offen sc1
v_mfma_f32_16x16x32_bf16 v[68:71], v[204:207], a[24:27], v[68:71]
v_mfma_f32_16x16x32_bf16 v[68:71], v[200:203], a[8:11], v[68:71]
ds_read_b64_tr_b16 v[192:193], v240 offset:59264
ds_read_b64_tr_b16 v[194:195], v240 offset:60288
v_mfma_f32_16x16x32_bf16 v[36:39], v[200:203], a[4:7], v[36:39]
v_mfma_f32_16x16x32_bf16 v[36:39], v[204:207], a[20:23], v[36:39]
buffer_load_dwordx4 a[60:63], v247, s[44:47], s53 offen offset:3072 sc1
v_mfma_f32_16x16x32_bf16 v[4:7], v[204:207], a[16:19], v[4:7]
s_waitcnt lgkmcnt(4)
s_bfe_u32 s53, s4, 262164
v_mfma_f32_16x16x32_bf16 v[4:7], v[200:203], a[0:3], v[4:7]
ds_read_b64_tr_b16 v[204:205], v241 offset:50816
ds_read_b64_tr_b16 v[206:207], v241 offset:51840
v_mfma_f32_16x16x32_bf16 v[8:11], v[208:211], a[0:3], v[8:11]
s_add_u32 m0, s83, 121472
s_xor_b32 s53, s53, s72
v_mfma_f32_16x16x32_bf16 v[8:11], v[212:215], a[16:19], v[8:11]
buffer_load_dwordx4 v227, s[56:59], s80 offen lds
v_mfma_f32_16x16x32_bf16 v[40:43], v[212:215], a[20:23], v[40:43]
s_add_u32 s80, s80, 131072
s_lshl_b32 s53, s53, 13
.La5_LW3_0:
s_cbranch_execz .La5_LW3_4
v_mfma_f32_16x16x32_bf16 v[40:43], v[208:211], a[4:7], v[40:43]
ds_read_b64_tr_b16 v[200:201], v241 offset:59264
ds_read_b64_tr_b16 v[202:203], v241 offset:60288
v_mfma_f32_16x16x32_bf16 v[72:75], v[208:211], a[8:11], v[72:75]
s_add_u32 m0, s83, 67584
v_mfma_f32_16x16x32_bf16 v[72:75], v[212:215], a[24:27], v[72:75]
buffer_load_dwordx4 v224, s[56:59], s80 offen lds
v_mfma_f32_16x16x32_bf16 v[104:107], v[212:215], a[28:31], v[104:107]
v_mfma_f32_16x16x32_bf16 v[104:107], v[208:211], a[12:15], v[104:107]
ds_read_b64_tr_b16 v[212:213], v242 offset:50816
ds_read_b64_tr_b16 v[214:215], v242 offset:51840
v_mfma_f32_16x16x32_bf16 v[108:111], v[216:219], a[12:15], v[108:111]
s_add_u32 m0, s83, 68608
v_mfma_f32_16x16x32_bf16 v[108:111], v[220:223], a[28:31], v[108:111]
buffer_load_dwordx4 v225, s[56:59], s80 offen lds
v_mfma_f32_16x16x32_bf16 v[76:79], v[220:223], a[24:27], v[76:79]
v_mfma_f32_16x16x32_bf16 v[76:79], v[216:219], a[8:11], v[76:79]
ds_read_b64_tr_b16 v[208:209], v242 offset:59264
ds_read_b64_tr_b16 v[210:211], v242 offset:60288
v_mfma_f32_16x16x32_bf16 v[44:47], v[216:219], a[4:7], v[44:47]
s_add_u32 m0, s83, 69760
v_mfma_f32_16x16x32_bf16 v[44:47], v[220:223], a[20:23], v[44:47]
buffer_load_dwordx4 v226, s[56:59], s80 offen lds
v_mfma_f32_16x16x32_bf16 v[12:15], v[220:223], a[16:19], v[12:15]
s_waitcnt lgkmcnt(4)
v_mfma_f32_16x16x32_bf16 v[12:15], v[216:219], a[0:3], v[12:15]
ds_read_b64_tr_b16 v[220:221], v243 offset:50816
ds_read_b64_tr_b16 v[222:223], v243 offset:51840
v_mfma_f32_16x16x32_bf16 v[16:19], v[196:199], a[0:3], v[16:19]
s_add_u32 m0, s83, 70784
v_mfma_f32_16x16x32_bf16 v[16:19], v[192:195], a[16:19], v[16:19]
buffer_load_dwordx4 v227, s[56:59], s80 offen lds
v_mfma_f32_16x16x32_bf16 v[48:51], v[192:195], a[20:23], v[48:51]
s_bfe_u32 s80, s4, 262156
s_xor_b32 s80, s80, s72
v_mfma_f32_16x16x32_bf16 v[48:51], v[196:199], a[4:7], v[48:51]
ds_read_b64_tr_b16 v[216:217], v243 offset:59264
ds_read_b64_tr_b16 v[218:219], v243 offset:60288
v_mfma_f32_16x16x32_bf16 v[80:83], v[196:199], a[8:11], v[80:83]
s_add_u32 m0, s83, 84480
s_nop 0
s_barrier
s_lshl_b32 s80, s80, 18
v_mfma_f32_16x16x32_bf16 v[80:83], v[192:195], a[24:27], v[80:83]
buffer_load_dwordx4 v224, s[56:59], s80 offen lds
v_mfma_f32_16x16x32_bf16 v[112:115], v[192:195], a[28:31], v[112:115]
v_mfma_f32_16x16x32_bf16 v[112:115], v[196:199], a[12:15], v[112:115]
ds_read_b64_tr_b16 v[192:193], v240 offset:0
ds_read_b64_tr_b16 v[194:195], v240 offset:1024
v_mfma_f32_16x16x32_bf16 v[116:119], v[204:207], a[12:15], v[116:119]
s_add_u32 m0, s83, 85504
v_mfma_f32_16x16x32_bf16 v[116:119], v[200:203], a[28:31], v[116:119]
buffer_load_dwordx4 v225, s[56:59], s80 offen lds
v_mfma_f32_16x16x32_bf16 v[84:87], v[200:203], a[24:27], v[84:87]
v_mfma_f32_16x16x32_bf16 v[84:87], v[204:207], a[8:11], v[84:87]
ds_read_b64_tr_b16 v[196:197], v240 offset:8448
ds_read_b64_tr_b16 v[198:199], v240 offset:9472
v_mfma_f32_16x16x32_bf16 v[52:55], v[204:207], a[4:7], v[52:55]
s_add_u32 m0, s83, 86656
v_mfma_f32_16x16x32_bf16 v[52:55], v[200:203], a[20:23], v[52:55]
buffer_load_dwordx4 v226, s[56:59], s80 offen lds
v_mfma_f32_16x16x32_bf16 v[20:23], v[200:203], a[16:19], v[20:23]
s_waitcnt lgkmcnt(4)
v_mfma_f32_16x16x32_bf16 v[20:23], v[204:207], a[0:3], v[20:23]
ds_read_b64_tr_b16 v[200:201], v241 offset:0
ds_read_b64_tr_b16 v[202:203], v241 offset:1024
v_mfma_f32_16x16x32_bf16 v[24:27], v[212:215], a[0:3], v[24:27]
s_add_u32 m0, s83, 87680
v_mfma_f32_16x16x32_bf16 v[24:27], v[208:211], a[16:19], v[24:27]
buffer_load_dwordx4 v227, s[56:59], s80 offen lds
v_mfma_f32_16x16x32_bf16 v[56:59], v[208:211], a[20:23], v[56:59]
s_add_u32 s80, s80, 131072
s_waitcnt vmcnt(10)
v_mfma_f32_16x16x32_bf16 v[56:59], v[212:215], a[4:7], v[56:59]
ds_read_b64_tr_b16 v[204:205], v241 offset:8448
ds_read_b64_tr_b16 v[206:207], v241 offset:9472
v_mfma_f32_16x16x32_bf16 v[88:91], v[212:215], a[8:11], v[88:91]
s_add_u32 m0, s83, 101376
v_cmp_eq_u64 vcc, v[250:251], v[252:253]
v_mfma_f32_16x16x32_bf16 v[88:91], v[208:211], a[24:27], v[88:91]
buffer_load_dwordx4 v224, s[56:59], s80 offen lds
v_mfma_f32_16x16x32_bf16 v[120:123], v[208:211], a[28:31], v[120:123]
s_bitcmp1_b32 vcc_lo, 2
s_nop 0
s_nop 0
s_cbranch_scc0 .La5_LO_N2
.La5_LO_K2:
v_mfma_f32_16x16x32_bf16 v[120:123], v[212:215], a[12:15], v[120:123]
ds_read_b64_tr_b16 v[208:209], v242 offset:0
ds_read_b64_tr_b16 v[210:211], v242 offset:1024
v_mfma_f32_16x16x32_bf16 v[124:127], v[220:223], a[12:15], v[124:127]
v_mfma_f32_16x16x32_bf16 v[124:127], v[216:219], a[28:31], v[124:127]
buffer_load_dwordx4 a[12:15], v244, s[44:47], s54 offen offset:0 sc1
v_mfma_f32_16x16x32_bf16 v[92:95], v[216:219], a[24:27], v[92:95]
v_mfma_f32_16x16x32_bf16 v[92:95], v[220:223], a[8:11], v[92:95]
ds_read_b64_tr_b16 v[212:213], v242 offset:8448
ds_read_b64_tr_b16 v[214:215], v242 offset:9472
v_mfma_f32_16x16x32_bf16 v[60:63], v[220:223], a[4:7], v[60:63]
v_mfma_f32_16x16x32_bf16 v[60:63], v[216:219], a[20:23], v[60:63]
v_mfma_f32_16x16x32_bf16 v[28:31], v[216:219], a[16:19], v[28:31]
s_waitcnt vmcnt(11) lgkmcnt(4)
v_mfma_f32_16x16x32_bf16 v[28:31], v[220:223], a[0:3], v[28:31]
ds_read_b64_tr_b16 v[216:217], v243 offset:0
ds_read_b64_tr_b16 v[218:219], v243 offset:1024
v_mfma_f32_16x16x32_bf16 v[0:3], v[192:195], a[32:35], v[0:3]
v_mfma_f32_16x16x32_bf16 v[0:3], v[196:199], a[48:51], v[0:3]
buffer_load_dwordx4 a[0:3], v245, s[44:47], s54 offen offset:0 sc1
v_mfma_f32_16x16x32_bf16 v[32:35], v[196:199], a[52:55], v[32:35]
v_mfma_f32_16x16x32_bf16 v[32:35], v[192:195], a[36:39], v[32:35]
ds_read_b64_tr_b16 v[220:221], v243 offset:8448
ds_read_b64_tr_b16 v[222:223], v243 offset:9472
v_mfma_f32_16x16x32_bf16 v[64:67], v[192:195], a[40:43], v[64:67]
v_mfma_f32_16x16x32_bf16 v[64:67], v[196:199], a[56:59], v[64:67]
buffer_load_dwordx4 a[4:7], v246, s[44:47], s54 offen offset:0 sc1
v_mfma_f32_16x16x32_bf16 v[96:99], v[196:199], a[60:63], v[96:99]
v_mfma_f32_16x16x32_bf16 v[96:99], v[192:195], a[44:47], v[96:99]
ds_read_b64_tr_b16 v[196:197], v240 offset:128
ds_read_b64_tr_b16 v[198:199], v240 offset:1152
v_mfma_f32_16x16x32_bf16 v[100:103], v[200:203], a[44:47], v[100:103]
s_mov_b32 s86, 0x05040100
s_mov_b32 s87, 0x07060302
v_mfma_f32_16x16x32_bf16 v[100:103], v[204:207], a[60:63], v[100:103]
buffer_load_dwordx4 a[8:11], v247, s[44:47], s54 offen offset:0 sc1
v_mfma_f32_16x16x32_bf16 v[68:71], v[204:207], a[56:59], v[68:71]
s_nop 0
v_accvgpr_read_b32 v230, a128
v_mfma_f32_16x16x32_bf16 v[68:71], v[200:203], a[40:43], v[68:71]
ds_read_b64_tr_b16 v[192:193], v240 offset:8576
ds_read_b64_tr_b16 v[194:195], v240 offset:9600
v_mfma_f32_16x16x32_bf16 v[36:39], v[200:203], a[36:39], v[36:39]
v_add_u32 v240, 67584, v240
v_accvgpr_read_b32 v231, a132
v_mfma_f32_16x16x32_bf16 v[36:39], v[204:207], a[52:55], v[36:39]
buffer_load_dwordx4 a[16:19], v244, s[44:47], s54 offen offset:1024 sc1
v_mfma_f32_16x16x32_bf16 v[4:7], v[204:207], a[48:51], v[4:7]
s_waitcnt lgkmcnt(4)
v_perm_b32 v232, v231, v230, s86
v_mfma_f32_16x16x32_bf16 v[4:7], v[200:203], a[32:35], v[4:7]
ds_read_b64_tr_b16 v[204:205], v241 offset:128
ds_read_b64_tr_b16 v[206:207], v241 offset:1152
v_mfma_f32_16x16x32_bf16 v[8:11], v[208:211], a[32:35], v[8:11]
v_perm_b32 v233, v231, v230, s87
v_accvgpr_write_b32 a96, v232
v_mfma_f32_16x16x32_bf16 v[8:11], v[212:215], a[48:51], v[8:11]
buffer_load_dwordx4 a[20:23], v245, s[44:47], s54 offen offset:1024 sc1
v_mfma_f32_16x16x32_bf16 v[40:43], v[212:215], a[52:55], v[40:43]
v_accvgpr_write_b32 a100, v233
v_accvgpr_read_b32 v234, a136
v_mfma_f32_16x16x32_bf16 v[40:43], v[208:211], a[36:39], v[40:43]
ds_read_b64_tr_b16 v[200:201], v241 offset:8576
ds_read_b64_tr_b16 v[202:203], v241 offset:9600
v_mfma_f32_16x16x32_bf16 v[72:75], v[208:211], a[40:43], v[72:75]
v_add_u32 v241, 67584, v241
v_accvgpr_read_b32 v235, a140
v_mfma_f32_16x16x32_bf16 v[72:75], v[212:215], a[56:59], v[72:75]
buffer_load_dwordx4 a[24:27], v246, s[44:47], s54 offen offset:1024 sc1
v_mfma_f32_16x16x32_bf16 v[104:107], v[212:215], a[60:63], v[104:107]
v_perm_b32 v236, v235, v234, s86
v_perm_b32 v237, v235, v234, s87
v_mfma_f32_16x16x32_bf16 v[104:107], v[208:211], a[44:47], v[104:107]
ds_read_b64_tr_b16 v[212:213], v242 offset:128
ds_read_b64_tr_b16 v[214:215], v242 offset:1152
v_mfma_f32_16x16x32_bf16 v[108:111], v[216:219], a[44:47], v[108:111]
v_accvgpr_write_b32 a97, v236
v_accvgpr_write_b32 a101, v237
v_mfma_f32_16x16x32_bf16 v[108:111], v[220:223], a[60:63], v[108:111]
buffer_load_dwordx4 a[28:31], v247, s[44:47], s54 offen offset:1024 sc1
v_mfma_f32_16x16x32_bf16 v[76:79], v[220:223], a[56:59], v[76:79]
v_accvgpr_read_b32 v230, a144
v_accvgpr_read_b32 v231, a148
v_mfma_f32_16x16x32_bf16 v[76:79], v[216:219], a[40:43], v[76:79]
ds_read_b64_tr_b16 v[208:209], v242 offset:8576
ds_read_b64_tr_b16 v[210:211], v242 offset:9600
v_mfma_f32_16x16x32_bf16 v[44:47], v[216:219], a[36:39], v[44:47]
v_add_u32 v242, 67584, v242
v_perm_b32 v232, v231, v230, s86
v_mfma_f32_16x16x32_bf16 v[44:47], v[220:223], a[52:55], v[44:47]
v_perm_b32 v233, v231, v230, s87
v_accvgpr_write_b32 a98, v232
v_mfma_f32_16x16x32_bf16 v[12:15], v[220:223], a[48:51], v[12:15]
s_waitcnt lgkmcnt(4)
v_accvgpr_write_b32 a102, v233
v_mfma_f32_16x16x32_bf16 v[12:15], v[216:219], a[32:35], v[12:15]
ds_read_b64_tr_b16 v[220:221], v243 offset:128
ds_read_b64_tr_b16 v[222:223], v243 offset:1152
v_mfma_f32_16x16x32_bf16 v[16:19], v[196:199], a[32:35], v[16:19]
s_add_u32 m0, s83, 102400
v_accvgpr_read_b32 v234, a152
v_mfma_f32_16x16x32_bf16 v[16:19], v[192:195], a[48:51], v[16:19]
buffer_load_dwordx4 v225, s[56:59], s80 offen lds
v_mfma_f32_16x16x32_bf16 v[48:51], v[192:195], a[52:55], v[48:51]
v_accvgpr_read_b32 v235, a156
v_perm_b32 v236, v235, v234, s86
v_mfma_f32_16x16x32_bf16 v[48:51], v[196:199], a[36:39], v[48:51]
ds_read_b64_tr_b16 v[216:217], v243 offset:8576
ds_read_b64_tr_b16 v[218:219], v243 offset:9600
v_mfma_f32_16x16x32_bf16 v[80:83], v[196:199], a[40:43], v[80:83]
s_add_u32 m0, s83, 103552
v_add_u32 v243, 67584, v243
v_mfma_f32_16x16x32_bf16 v[80:83], v[192:195], a[56:59], v[80:83]
buffer_load_dwordx4 v226, s[56:59], s80 offen lds
v_mfma_f32_16x16x32_bf16 v[112:115], v[192:195], a[60:63], v[112:115]
s_waitcnt vmcnt(15)
s_barrier
v_mfma_f32_16x16x32_bf16 v[112:115], v[196:199], a[44:47], v[112:115]
ds_read_b64_tr_b16 v[192:193], v240 offset:50688
ds_read_b64_tr_b16 v[194:195], v240 offset:51712
v_mfma_f32_16x16x32_bf16 v[116:119], v[204:207], a[44:47], v[116:119]
v_perm_b32 v237, v235, v234, s87
v_accvgpr_write_b32 a99, v236
v_mfma_f32_16x16x32_bf16 v[116:119], v[200:203], a[60:63], v[116:119]
buffer_load_dwordx4 a[64:67], v244, s[44:47], s54 offen offset:2048 sc1
v_mfma_f32_16x16x32_bf16 v[84:87], v[200:203], a[56:59], v[84:87]
v_accvgpr_write_b32 a103, v237
v_accvgpr_read_b32 v230, a129
v_mfma_f32_16x16x32_bf16 v[84:87], v[204:207], a[40:43], v[84:87]
ds_read_b64_tr_b16 v[196:197], v240 offset:59136
ds_read_b64_tr_b16 v[198:199], v240 offset:60160
v_mfma_f32_16x16x32_bf16 v[52:55], v[204:207], a[36:39], v[52:55]
v_accvgpr_read_b32 v231, a133
v_perm_b32 v232, v231, v230, s86
v_mfma_f32_16x16x32_bf16 v[52:55], v[200:203], a[52:55], v[52:55]
buffer_load_dwordx4 a[68:71], v245, s[44:47], s54 offen offset:2048 sc1
v_mfma_f32_16x16x32_bf16 v[20:23], v[200:203], a[48:51], v[20:23]
s_waitcnt lgkmcnt(4)
v_perm_b32 v233, v231, v230, s87
v_mfma_f32_16x16x32_bf16 v[20:23], v[204:207], a[32:35], v[20:23]
ds_read_b64_tr_b16 v[200:201], v241 offset:50688
ds_read_b64_tr_b16 v[202:203], v241 offset:51712
v_mfma_f32_16x16x32_bf16 v[24:27], v[212:215], a[32:35], v[24:27]
v_accvgpr_write_b32 a104, v232
v_accvgpr_write_b32 a108, v233
v_mfma_f32_16x16x32_bf16 v[24:27], v[208:211], a[48:51], v[24:27]
buffer_load_dwordx4 a[72:75], v246, s[44:47], s54 offen offset:2048 sc1
v_mfma_f32_16x16x32_bf16 v[56:59], v[208:211], a[52:55], v[56:59]
v_accvgpr_read_b32 v234, a137
v_accvgpr_read_b32 v235, a141
v_mfma_f32_16x16x32_bf16 v[56:59], v[212:215], a[36:39], v[56:59]
ds_read_b64_tr_b16 v[204:205], v241 offset:59136
ds_read_b64_tr_b16 v[206:207], v241 offset:60160
v_mfma_f32_16x16x32_bf16 v[88:91], v[212:215], a[40:43], v[88:91]
v_perm_b32 v236, v235, v234, s86
v_perm_b32 v237, v235, v234, s87
v_mfma_f32_16x16x32_bf16 v[88:91], v[208:211], a[56:59], v[88:91]
buffer_load_dwordx4 a[76:79], v247, s[44:47], s54 offen offset:2048 sc1
v_mfma_f32_16x16x32_bf16 v[120:123], v[208:211], a[60:63], v[120:123]
v_accvgpr_write_b32 a105, v236
v_accvgpr_write_b32 a109, v237
v_mfma_f32_16x16x32_bf16 v[120:123], v[212:215], a[44:47], v[120:123]
ds_read_b64_tr_b16 v[208:209], v242 offset:50688
ds_read_b64_tr_b16 v[210:211], v242 offset:51712
v_mfma_f32_16x16x32_bf16 v[124:127], v[220:223], a[44:47], v[124:127]
v_accvgpr_read_b32 v230, a145
v_accvgpr_read_b32 v231, a149
v_mfma_f32_16x16x32_bf16 v[124:127], v[216:219], a[60:63], v[124:127]
buffer_load_dwordx2 v[250:251], v248, s[64:67], s5 offen sc1
v_mfma_f32_16x16x32_bf16 v[92:95], v[216:219], a[56:59], v[92:95]
v_perm_b32 v232, v231, v230, s86
v_perm_b32 v233, v231, v230, s87
v_mfma_f32_16x16x32_bf16 v[92:95], v[220:223], a[40:43], v[92:95]
ds_read_b64_tr_b16 v[212:213], v242 offset:59136
ds_read_b64_tr_b16 v[214:215], v242 offset:60160
v_mfma_f32_16x16x32_bf16 v[60:63], v[220:223], a[36:39], v[60:63]
v_accvgpr_write_b32 a106, v232
v_accvgpr_write_b32 a110, v233
v_mfma_f32_16x16x32_bf16 v[60:63], v[216:219], a[52:55], v[60:63]
buffer_load_dwordx4 a[36:39], v244, s[44:47], s54 offen offset:3072 sc1
v_mfma_f32_16x16x32_bf16 v[28:31], v[216:219], a[48:51], v[28:31]
s_waitcnt vmcnt(8) lgkmcnt(4)
v_accvgpr_read_b32 v234, a153
v_mfma_f32_16x16x32_bf16 v[28:31], v[220:223], a[32:35], v[28:31]
ds_read_b64_tr_b16 v[216:217], v243 offset:50688
ds_read_b64_tr_b16 v[218:219], v243 offset:51712
v_mfma_f32_16x16x32_bf16 v[0:3], v[192:195], a[12:15], v[0:3]
v_accvgpr_read_b32 v235, a157
v_perm_b32 v236, v235, v234, s86
v_mfma_f32_16x16x32_bf16 v[0:3], v[196:199], a[16:19], v[0:3]
buffer_load_dwordx4 a[32:35], v245, s[44:47], s54 offen offset:3072 sc1
v_mfma_f32_16x16x32_bf16 v[32:35], v[196:199], a[20:23], v[32:35]
v_perm_b32 v237, v235, v234, s87
v_accvgpr_write_b32 a107, v236
v_mfma_f32_16x16x32_bf16 v[32:35], v[192:195], a[0:3], v[32:35]
ds_read_b64_tr_b16 v[220:221], v243 offset:59136
ds_read_b64_tr_b16 v[222:223], v243 offset:60160
v_mfma_f32_16x16x32_bf16 v[64:67], v[192:195], a[4:7], v[64:67]
v_accvgpr_write_b32 a111, v237
v_accvgpr_read_b32 v230, a130
v_mfma_f32_16x16x32_bf16 v[64:67], v[196:199], a[24:27], v[64:67]
buffer_load_dwordx4 a[40:43], v246, s[44:47], s54 offen offset:3072 sc1
v_mfma_f32_16x16x32_bf16 v[96:99], v[196:199], a[28:31], v[96:99]
v_accvgpr_read_b32 v231, a134
v_perm_b32 v232, v231, v230, s86
v_mfma_f32_16x16x32_bf16 v[96:99], v[192:195], a[8:11], v[96:99]
ds_read_b64_tr_b16 v[196:197], v240 offset:50816
ds_read_b64_tr_b16 v[198:199], v240 offset:51840
v_mfma_f32_16x16x32_bf16 v[100:103], v[200:203], a[8:11], v[100:103]
v_perm_b32 v233, v231, v230, s87
v_accvgpr_write_b32 a112, v232
v_mfma_f32_16x16x32_bf16 v[100:103], v[204:207], a[28:31], v[100:103]
buffer_load_dwordx4 a[44:47], v247, s[44:47], s54 offen offset:3072 sc1
v_mfma_f32_16x16x32_bf16 v[68:71], v[204:207], a[24:27], v[68:71]
s_bfe_u32 s54, s4, 262168
s_xor_b32 s54, s54, s72
v_mfma_f32_16x16x32_bf16 v[68:71], v[200:203], a[4:7], v[68:71]
ds_read_b64_tr_b16 v[192:193], v240 offset:59264
ds_read_b64_tr_b16 v[194:195], v240 offset:60288
v_mfma_f32_16x16x32_bf16 v[36:39], v[200:203], a[0:3], v[36:39]
s_add_u32 m0, s83, 104576
s_lshl_b32 s54, s54, 13
v_mfma_f32_16x16x32_bf16 v[36:39], v[204:207], a[20:23], v[36:39]
buffer_load_dwordx4 v227, s[56:59], s80 offen lds
v_mfma_f32_16x16x32_bf16 v[4:7], v[204:207], a[16:19], v[4:7]
s_waitcnt lgkmcnt(4)
s_bfe_u32 s80, s4, 262160
v_mfma_f32_16x16x32_bf16 v[4:7], v[200:203], a[12:15], v[4:7]
ds_read_b64_tr_b16 v[204:205], v241 offset:50816
ds_read_b64_tr_b16 v[206:207], v241 offset:51840
v_mfma_f32_16x16x32_bf16 v[8:11], v[208:211], a[12:15], v[8:11]
s_bitcmp1_b32 vcc_lo, 3
s_cbranch_scc0 .La5_LO_N3
.La5_LO_K3:
v_mfma_f32_16x16x32_bf16 v[8:11], v[212:215], a[16:19], v[8:11]
buffer_load_dwordx4 a[48:51], v244, s[44:47], s55 offen offset:0 sc1
v_mfma_f32_16x16x32_bf16 v[40:43], v[212:215], a[20:23], v[40:43]
s_xor_b32 s80, s80, s72
s_lshl_b32 s80, s80, 18
v_mfma_f32_16x16x32_bf16 v[40:43], v[208:211], a[0:3], v[40:43]
ds_read_b64_tr_b16 v[200:201], v241 offset:59264
ds_read_b64_tr_b16 v[202:203], v241 offset:60288
v_mfma_f32_16x16x32_bf16 v[72:75], v[208:211], a[4:7], v[72:75]
v_accvgpr_write_b32 a116, v233
v_accvgpr_read_b32 v234, a138
v_mfma_f32_16x16x32_bf16 v[72:75], v[212:215], a[24:27], v[72:75]
buffer_load_dwordx4 a[52:55], v245, s[44:47], s55 offen offset:0 sc1
v_mfma_f32_16x16x32_bf16 v[104:107], v[212:215], a[28:31], v[104:107]
v_accvgpr_read_b32 v235, a142
v_perm_b32 v236, v235, v234, s86
v_mfma_f32_16x16x32_bf16 v[104:107], v[208:211], a[8:11], v[104:107]
ds_read_b64_tr_b16 v[212:213], v242 offset:50816
ds_read_b64_tr_b16 v[214:215], v242 offset:51840
v_mfma_f32_16x16x32_bf16 v[108:111], v[216:219], a[8:11], v[108:111]
v_perm_b32 v237, v235, v234, s87
v_accvgpr_write_b32 a113, v236
v_mfma_f32_16x16x32_bf16 v[108:111], v[220:223], a[28:31], v[108:111]
buffer_load_dwordx4 a[56:59], v246, s[44:47], s55 offen offset:0 sc1
v_mfma_f32_16x16x32_bf16 v[76:79], v[220:223], a[24:27], v[76:79]
v_accvgpr_write_b32 a117, v237
v_accvgpr_read_b32 v230, a146
v_mfma_f32_16x16x32_bf16 v[76:79], v[216:219], a[4:7], v[76:79]
ds_read_b64_tr_b16 v[208:209], v242 offset:59264
ds_read_b64_tr_b16 v[210:211], v242 offset:60288
v_mfma_f32_16x16x32_bf16 v[44:47], v[216:219], a[0:3], v[44:47]
v_accvgpr_read_b32 v231, a150
v_perm_b32 v232, v231, v230, s86
v_mfma_f32_16x16x32_bf16 v[44:47], v[220:223], a[20:23], v[44:47]
v_perm_b32 v233, v231, v230, s87
v_accvgpr_write_b32 a114, v232
v_mfma_f32_16x16x32_bf16 v[12:15], v[220:223], a[16:19], v[12:15]
s_waitcnt lgkmcnt(4)
v_accvgpr_write_b32 a118, v233
v_mfma_f32_16x16x32_bf16 v[12:15], v[216:219], a[12:15], v[12:15]
ds_read_b64_tr_b16 v[220:221], v243 offset:50816
ds_read_b64_tr_b16 v[222:223], v243 offset:51840
v_mfma_f32_16x16x32_bf16 v[16:19], v[196:199], a[12:15], v[16:19]
v_accvgpr_read_b32 v234, a154
v_accvgpr_read_b32 v235, a158
v_mfma_f32_16x16x32_bf16 v[16:19], v[192:195], a[16:19], v[16:19]
buffer_load_dwordx4 a[60:63], v247, s[44:47], s55 offen offset:0 sc1
v_mfma_f32_16x16x32_bf16 v[48:51], v[192:195], a[20:23], v[48:51]
v_perm_b32 v236, v235, v234, s86
v_perm_b32 v237, v235, v234, s87
v_mfma_f32_16x16x32_bf16 v[48:51], v[196:199], a[0:3], v[48:51]
ds_read_b64_tr_b16 v[216:217], v243 offset:59264
ds_read_b64_tr_b16 v[218:219], v243 offset:60288
v_mfma_f32_16x16x32_bf16 v[80:83], v[196:199], a[4:7], v[80:83]
v_accvgpr_write_b32 a115, v236
v_accvgpr_write_b32 a119, v237
v_mfma_f32_16x16x32_bf16 v[80:83], v[192:195], a[24:27], v[80:83]
buffer_load_dwordx4 a[80:83], v244, s[44:47], s55 offen offset:1024 sc1
v_mfma_f32_16x16x32_bf16 v[112:115], v[192:195], a[28:31], v[112:115]
v_accvgpr_read_b32 v230, a131
v_accvgpr_read_b32 v231, a135
v_mfma_f32_16x16x32_bf16 v[112:115], v[196:199], a[8:11], v[112:115]
ds_read_b64_tr_b16 v[192:193], v240 offset:0
ds_read_b64_tr_b16 v[194:195], v240 offset:1024
v_mfma_f32_16x16x32_bf16 v[116:119], v[204:207], a[8:11], v[116:119]
v_perm_b32 v232, v231, v230, s86
v_perm_b32 v233, v231, v230, s87
v_mfma_f32_16x16x32_bf16 v[116:119], v[200:203], a[28:31], v[116:119]
buffer_load_dwordx4 a[84:87], v245, s[44:47], s55 offen offset:1024 sc1
v_mfma_f32_16x16x32_bf16 v[84:87], v[200:203], a[24:27], v[84:87]
v_accvgpr_write_b32 a120, v232
v_accvgpr_write_b32 a124, v233
v_mfma_f32_16x16x32_bf16 v[84:87], v[204:207], a[4:7], v[84:87]
ds_read_b64_tr_b16 v[196:197], v240 offset:8448
ds_read_b64_tr_b16 v[198:199], v240 offset:9472
v_mfma_f32_16x16x32_bf16 v[52:55], v[204:207], a[0:3], v[52:55]
s_load_dwordx2 s[16:17], s[0:1], 0
s_load_dwordx4 s[36:39], s[0:1], 80
v_mfma_f32_16x16x32_bf16 v[52:55], v[200:203], a[20:23], v[52:55]
buffer_load_dwordx4 a[88:91], v246, s[44:47], s55 offen offset:1024 sc1
v_mfma_f32_16x16x32_bf16 v[20:23], v[200:203], a[16:19], v[20:23]
s_waitcnt lgkmcnt(4)
v_accvgpr_read_b32 v234, a139
v_mfma_f32_16x16x32_bf16 v[20:23], v[204:207], a[12:15], v[20:23]
ds_read_b64_tr_b16 v[200:201], v241 offset:0
ds_read_b64_tr_b16 v[202:203], v241 offset:1024
v_mfma_f32_16x16x32_bf16 v[24:27], v[212:215], a[12:15], v[24:27]
v_accvgpr_read_b32 v235, a143
v_perm_b32 v236, v235, v234, s86
v_mfma_f32_16x16x32_bf16 v[24:27], v[208:211], a[16:19], v[24:27]
buffer_load_dwordx4 a[92:95], v247, s[44:47], s55 offen offset:1024 sc1
v_mfma_f32_16x16x32_bf16 v[56:59], v[208:211], a[20:23], v[56:59]
v_perm_b32 v237, v235, v234, s87
v_accvgpr_write_b32 a121, v236
v_mfma_f32_16x16x32_bf16 v[56:59], v[212:215], a[0:3], v[56:59]
ds_read_b64_tr_b16 v[204:205], v241 offset:8448
ds_read_b64_tr_b16 v[206:207], v241 offset:9472
v_mfma_f32_16x16x32_bf16 v[88:91], v[212:215], a[4:7], v[88:91]
s_add_u32 m0, s83, 16896
v_accvgpr_write_b32 a125, v237
v_mfma_f32_16x16x32_bf16 v[88:91], v[208:211], a[24:27], v[88:91]
buffer_load_dwordx4 v224, s[56:59], s80 offen lds
v_mfma_f32_16x16x32_bf16 v[120:123], v[208:211], a[28:31], v[120:123]
v_accvgpr_read_b32 v230, a147
v_accvgpr_read_b32 v231, a151
v_mfma_f32_16x16x32_bf16 v[120:123], v[212:215], a[8:11], v[120:123]
ds_read_b64_tr_b16 v[208:209], v242 offset:0
ds_read_b64_tr_b16 v[210:211], v242 offset:1024
v_mfma_f32_16x16x32_bf16 v[124:127], v[220:223], a[8:11], v[124:127]
s_add_u32 m0, s83, 17920
v_perm_b32 v232, v231, v230, s86
v_mfma_f32_16x16x32_bf16 v[124:127], v[216:219], a[28:31], v[124:127]
buffer_load_dwordx4 v225, s[56:59], s80 offen lds
v_mfma_f32_16x16x32_bf16 v[92:95], v[216:219], a[24:27], v[92:95]
v_perm_b32 v233, v231, v230, s87
v_accvgpr_write_b32 a122, v232
v_mfma_f32_16x16x32_bf16 v[92:95], v[220:223], a[4:7], v[92:95]
ds_read_b64_tr_b16 v[212:213], v242 offset:8448
ds_read_b64_tr_b16 v[214:215], v242 offset:9472
v_mfma_f32_16x16x32_bf16 v[60:63], v[220:223], a[0:3], v[60:63]
v_accvgpr_write_b32 a126, v233
v_accvgpr_read_b32 v234, a155
v_mfma_f32_16x16x32_bf16 v[60:63], v[216:219], a[20:23], v[60:63]
v_accvgpr_read_b32 v235, a159
v_perm_b32 v236, v235, v234, s86
v_mfma_f32_16x16x32_bf16 v[28:31], v[216:219], a[16:19], v[28:31]
s_waitcnt vmcnt(11) lgkmcnt(4)
v_perm_b32 v237, v235, v234, s87
v_mfma_f32_16x16x32_bf16 v[28:31], v[220:223], a[12:15], v[28:31]
ds_read_b64_tr_b16 v[216:217], v243 offset:0
ds_read_b64_tr_b16 v[218:219], v243 offset:1024
v_mfma_f32_16x16x32_bf16 v[0:3], v[192:195], a[64:67], v[0:3]
s_add_u32 m0, s83, 19072
v_accvgpr_write_b32 a123, v236
v_mfma_f32_16x16x32_bf16 v[0:3], v[196:199], a[36:39], v[0:3]
buffer_load_dwordx4 v226, s[56:59], s80 offen lds
v_mfma_f32_16x16x32_bf16 v[32:35], v[196:199], a[32:35], v[32:35]
v_accvgpr_write_b32 a127, v237
v_accvgpr_read_b32 v230, a160
v_mfma_f32_16x16x32_bf16 v[32:35], v[192:195], a[68:71], v[32:35]
ds_read_b64_tr_b16 v[220:221], v243 offset:8448
ds_read_b64_tr_b16 v[222:223], v243 offset:9472
v_mfma_f32_16x16x32_bf16 v[64:67], v[192:195], a[72:75], v[64:67]
v_accvgpr_read_b32 v231, a164
v_perm_b32 v232, v231, v230, s86
v_mfma_f32_16x16x32_bf16 v[64:67], v[196:199], a[40:43], v[64:67]
buffer_load_dwordx4 a[0:3], v244, s[44:47], s55 offen offset:2048 sc1
v_mfma_f32_16x16x32_bf16 v[96:99], v[196:199], a[44:47], v[96:99]
v_perm_b32 v233, v231, v230, s87
v_accvgpr_write_b32 a128, v232
v_mfma_f32_16x16x32_bf16 v[96:99], v[192:195], a[76:79], v[96:99]
ds_read_b64_tr_b16 v[196:197], v240 offset:128
ds_read_b64_tr_b16 v[198:199], v240 offset:1152
v_mfma_f32_16x16x32_bf16 v[100:103], v[200:203], a[76:79], v[100:103]
v_accvgpr_write_b32 a132, v233
v_accvgpr_read_b32 v234, a168
v_mfma_f32_16x16x32_bf16 v[100:103], v[204:207], a[44:47], v[100:103]
buffer_load_dwordx4 a[4:7], v245, s[44:47], s55 offen offset:2048 sc1
v_mfma_f32_16x16x32_bf16 v[68:71], v[204:207], a[40:43], v[68:71]
v_accvgpr_read_b32 v235, a172
v_perm_b32 v236, v235, v234, s86
v_mfma_f32_16x16x32_bf16 v[68:71], v[200:203], a[72:75], v[68:71]
ds_read_b64_tr_b16 v[192:193], v240 offset:8576
ds_read_b64_tr_b16 v[194:195], v240 offset:9600
v_mfma_f32_16x16x32_bf16 v[36:39], v[200:203], a[68:71], v[36:39]
v_perm_b32 v237, v235, v234, s87
v_accvgpr_write_b32 a129, v236
v_mfma_f32_16x16x32_bf16 v[36:39], v[204:207], a[32:35], v[36:39]
buffer_load_dwordx4 a[8:11], v246, s[44:47], s55 offen offset:2048 sc1
v_mfma_f32_16x16x32_bf16 v[4:7], v[204:207], a[36:39], v[4:7]
s_waitcnt lgkmcnt(4)
v_accvgpr_write_b32 a133, v237
v_mfma_f32_16x16x32_bf16 v[4:7], v[200:203], a[64:67], v[4:7]
ds_read_b64_tr_b16 v[204:205], v241 offset:128
ds_read_b64_tr_b16 v[206:207], v241 offset:1152
v_mfma_f32_16x16x32_bf16 v[8:11], v[208:211], a[64:67], v[8:11]
v_accvgpr_read_b32 v230, a176
v_accvgpr_read_b32 v231, a180
v_mfma_f32_16x16x32_bf16 v[8:11], v[212:215], a[36:39], v[8:11]
buffer_load_dwordx4 a[12:15], v247, s[44:47], s55 offen offset:2048 sc1
v_mfma_f32_16x16x32_bf16 v[40:43], v[212:215], a[32:35], v[40:43]
v_perm_b32 v232, v231, v230, s86
v_perm_b32 v233, v231, v230, s87
v_mfma_f32_16x16x32_bf16 v[40:43], v[208:211], a[68:71], v[40:43]
ds_read_b64_tr_b16 v[200:201], v241 offset:8576
ds_read_b64_tr_b16 v[202:203], v241 offset:9600
v_mfma_f32_16x16x32_bf16 v[72:75], v[208:211], a[72:75], v[72:75]
v_accvgpr_write_b32 a130, v232
v_accvgpr_write_b32 a134, v233
v_mfma_f32_16x16x32_bf16 v[72:75], v[212:215], a[40:43], v[72:75]
buffer_load_dwordx4 a[16:19], v244, s[44:47], s55 offen offset:3072 sc1
v_mfma_f32_16x16x32_bf16 v[104:107], v[212:215], a[44:47], v[104:107]
v_accvgpr_read_b32 v234, a184
v_accvgpr_read_b32 v235, a188
v_mfma_f32_16x16x32_bf16 v[104:107], v[208:211], a[76:79], v[104:107]
ds_read_b64_tr_b16 v[212:213], v242 offset:128
ds_read_b64_tr_b16 v[214:215], v242 offset:1152
v_mfma_f32_16x16x32_bf16 v[108:111], v[216:219], a[76:79], v[108:111]
v_perm_b32 v236, v235, v234, s86
v_perm_b32 v237, v235, v234, s87
v_mfma_f32_16x16x32_bf16 v[108:111], v[220:223], a[44:47], v[108:111]
buffer_load_dwordx4 a[20:23], v245, s[44:47], s55 offen offset:3072 sc1
v_mfma_f32_16x16x32_bf16 v[76:79], v[220:223], a[40:43], v[76:79]
v_accvgpr_write_b32 a131, v236
v_accvgpr_write_b32 a135, v237
v_mfma_f32_16x16x32_bf16 v[76:79], v[216:219], a[72:75], v[76:79]
ds_read_b64_tr_b16 v[208:209], v242 offset:8576
ds_read_b64_tr_b16 v[210:211], v242 offset:9600
.La5_LW3_1:
s_cbranch_execz .La5_LW3_5
v_mfma_f32_16x16x32_bf16 v[44:47], v[216:219], a[68:71], v[44:47]
v_accvgpr_read_b32 v230, a161
v_accvgpr_read_b32 v231, a165
v_mfma_f32_16x16x32_bf16 v[44:47], v[220:223], a[32:35], v[44:47]
s_nop 0
v_cmp_eq_u64 vcc, v[250:251], v[252:253]
v_mfma_f32_16x16x32_bf16 v[12:15], v[220:223], a[36:39], v[12:15]
s_waitcnt lgkmcnt(4)
v_perm_b32 v232, v231, v230, s86
v_mfma_f32_16x16x32_bf16 v[12:15], v[216:219], a[64:67], v[12:15]
ds_read_b64_tr_b16 v[220:221], v243 offset:128
ds_read_b64_tr_b16 v[222:223], v243 offset:1152
v_mfma_f32_16x16x32_bf16 v[16:19], v[196:199], a[64:67], v[16:19]
v_perm_b32 v233, v231, v230, s87
v_accvgpr_write_b32 a136, v232
v_mfma_f32_16x16x32_bf16 v[16:19], v[192:195], a[36:39], v[16:19]
buffer_load_dwordx4 a[24:27], v246, s[44:47], s55 offen offset:3072 sc1
v_mfma_f32_16x16x32_bf16 v[48:51], v[192:195], a[32:35], v[48:51]
v_accvgpr_write_b32 a140, v233
v_accvgpr_read_b32 v234, a169
v_mfma_f32_16x16x32_bf16 v[48:51], v[196:199], a[68:71], v[48:51]
ds_read_b64_tr_b16 v[216:217], v243 offset:8576
ds_read_b64_tr_b16 v[218:219], v243 offset:9600
v_mfma_f32_16x16x32_bf16 v[80:83], v[196:199], a[72:75], v[80:83]
v_accvgpr_read_b32 v235, a173
v_perm_b32 v236, v235, v234, s86
v_mfma_f32_16x16x32_bf16 v[80:83], v[192:195], a[40:43], v[80:83]
buffer_load_dwordx4 a[28:31], v247, s[44:47], s55 offen offset:3072 sc1
v_mfma_f32_16x16x32_bf16 v[112:115], v[192:195], a[44:47], v[112:115]
s_waitcnt vmcnt(19) lgkmcnt(0)
s_barrier
v_mfma_f32_16x16x32_bf16 v[112:115], v[196:199], a[76:79], v[112:115]
ds_read_b64_tr_b16 v[192:193], v240 offset:16896
ds_read_b64_tr_b16 v[194:195], v240 offset:17920
v_mfma_f32_16x16x32_bf16 v[116:119], v[204:207], a[76:79], v[116:119]
s_bfe_u32 s55, s4, 262172
s_xor_b32 s55, s55, s72
v_mfma_f32_16x16x32_bf16 v[116:119], v[200:203], a[44:47], v[116:119]
buffer_load_dwordx2 v[250:251], v248, s[64:67], s5 offen sc1
v_mfma_f32_16x16x32_bf16 v[84:87], v[200:203], a[40:43], v[84:87]
s_lshl_b32 s55, s55, 13
s_lshl_b32 s92, s76, 5
v_mfma_f32_16x16x32_bf16 v[84:87], v[204:207], a[72:75], v[84:87]
ds_read_b64_tr_b16 v[196:197], v240 offset:25344
ds_read_b64_tr_b16 v[198:199], v240 offset:26368
v_mfma_f32_16x16x32_bf16 v[52:55], v[204:207], a[68:71], v[52:55]
s_add_u32 m0, s83, 20096
s_add_u32 s92, s92, s72
v_mfma_f32_16x16x32_bf16 v[52:55], v[200:203], a[32:35], v[52:55]
buffer_load_dwordx4 v227, s[56:59], s80 offen lds
v_mfma_f32_16x16x32_bf16 v[20:23], v[200:203], a[36:39], v[20:23]
s_nop 0
s_add_u32 s80, s80, 131072
v_mfma_f32_16x16x32_bf16 v[20:23], v[204:207], a[64:67], v[20:23]
ds_read_b64_tr_b16 v[200:201], v241 offset:16896
ds_read_b64_tr_b16 v[202:203], v241 offset:17920
v_mfma_f32_16x16x32_bf16 v[24:27], v[212:215], a[64:67], v[24:27]
s_add_u32 m0, s83, 33792
s_lshl2_add_u32 s92, s92, s3
v_mfma_f32_16x16x32_bf16 v[24:27], v[208:211], a[36:39], v[24:27]
buffer_load_dwordx4 v224, s[56:59], s80 offen lds
v_mfma_f32_16x16x32_bf16 v[56:59], v[208:211], a[32:35], v[56:59]
s_lshl_b32 s92, s92, 12
v_perm_b32 v237, v235, v234, s87
v_mfma_f32_16x16x32_bf16 v[56:59], v[212:215], a[68:71], v[56:59]
ds_read_b64_tr_b16 v[204:205], v241 offset:25344
ds_read_b64_tr_b16 v[206:207], v241 offset:26368
v_mfma_f32_16x16x32_bf16 v[88:91], v[212:215], a[72:75], v[88:91]
s_add_u32 m0, s83, 34816
v_accvgpr_write_b32 a137, v236
v_mfma_f32_16x16x32_bf16 v[88:91], v[208:211], a[40:43], v[88:91]
buffer_load_dwordx4 v225, s[56:59], s80 offen lds
v_mfma_f32_16x16x32_bf16 v[120:123], v[208:211], a[44:47], v[120:123]
s_add_u32 s68, s16, s92
s_addc_u32 s69, s17, 0
v_mfma_f32_16x16x32_bf16 v[120:123], v[212:215], a[76:79], v[120:123]
ds_read_b64_tr_b16 v[208:209], v242 offset:16896
ds_read_b64_tr_b16 v[210:211], v242 offset:17920
v_mfma_f32_16x16x32_bf16 v[124:127], v[220:223], a[76:79], v[124:127]
s_add_u32 m0, s83, 35968
s_and_b32 s69, s69, 0xffff
v_mfma_f32_16x16x32_bf16 v[124:127], v[216:219], a[44:47], v[124:127]
buffer_load_dwordx4 v226, s[56:59], s80 offen lds
v_mfma_f32_16x16x32_bf16 v[92:95], v[216:219], a[40:43], v[92:95]
s_mov_b32 s70, 4096
s_mov_b32 s71, 0x20000
v_mfma_f32_16x16x32_bf16 v[92:95], v[220:223], a[72:75], v[92:95]
ds_read_b64_tr_b16 v[212:213], v242 offset:25344
ds_read_b64_tr_b16 v[214:215], v242 offset:26368
v_mfma_f32_16x16x32_bf16 v[60:63], v[220:223], a[68:71], v[60:63]
s_add_u32 m0, s83, 36992
v_accvgpr_write_b32 a141, v237
v_mfma_f32_16x16x32_bf16 v[60:63], v[216:219], a[32:35], v[60:63]
buffer_load_dwordx4 v227, s[56:59], s80 offen lds
v_mfma_f32_16x16x32_bf16 v[28:31], v[216:219], a[36:39], v[28:31]
s_waitcnt vmcnt(17) lgkmcnt(4)
s_bfe_u32 s80, s4, 262164
v_mfma_f32_16x16x32_bf16 v[28:31], v[220:223], a[64:67], v[28:31]
ds_read_b64_tr_b16 v[216:217], v243 offset:16896
ds_read_b64_tr_b16 v[218:219], v243 offset:17920
v_mfma_f32_16x16x32_bf16 v[0:3], v[192:195], a[48:51], v[0:3]
s_bitcmp1_b32 vcc_lo, 4
s_cbranch_scc0 .La5_LO_N4
.La5_LO_K4:
v_mfma_f32_16x16x32_bf16 v[0:3], v[196:199], a[80:83], v[0:3]
buffer_load_dwordx4 v[136:139], v244, s[44:47], s52 offen offset:0 sc1
v_mfma_f32_16x16x32_bf16 v[32:35], v[196:199], a[84:87], v[32:35]
s_xor_b32 s80, s80, s72
s_lshl_b32 s80, s80, 18
v_mfma_f32_16x16x32_bf16 v[32:35], v[192:195], a[52:55], v[32:35]
ds_read_b64_tr_b16 v[220:221], v243 offset:25344
ds_read_b64_tr_b16 v[222:223], v243 offset:26368
v_mfma_f32_16x16x32_bf16 v[64:67], v[192:195], a[56:59], v[64:67]
s_add_u32 s48, s36, 0
s_addc_u32 s49, s37, 0
v_mfma_f32_16x16x32_bf16 v[64:67], v[196:199], a[88:91], v[64:67]
buffer_load_dwordx4 v[140:143], v245, s[44:47], s52 offen offset:0 sc1
v_mfma_f32_16x16x32_bf16 v[96:99], v[196:199], a[92:95], v[96:99]
s_and_b32 s49, s49, 0xffff
s_mov_b32 s50, 2048
v_mfma_f32_16x16x32_bf16 v[96:99], v[192:195], a[60:63], v[96:99]
ds_read_b64_tr_b16 v[196:197], v240 offset:17024
ds_read_b64_tr_b16 v[198:199], v240 offset:18048
v_mfma_f32_16x16x32_bf16 v[100:103], v[200:203], a[60:63], v[100:103]
s_mov_b32 s51, 0x20000
v_and_b32 v238, 0xf0, v255
v_mfma_f32_16x16x32_bf16 v[100:103], v[204:207], a[92:95], v[100:103]
buffer_load_dwordx4 v[152:155], v246, s[44:47], s52 offen offset:0 sc1
v_mfma_f32_16x16x32_bf16 v[68:71], v[204:207], a[88:91], v[68:71]
v_lshlrev_b32 v238, 2, v238
v_bfe_u32 v249, v255, 8, 1
v_mfma_f32_16x16x32_bf16 v[68:71], v[200:203], a[56:59], v[68:71]
ds_read_b64_tr_b16 v[192:193], v240 offset:25472
ds_read_b64_tr_b16 v[194:195], v240 offset:26496
v_mfma_f32_16x16x32_bf16 v[36:39], v[200:203], a[52:55], v[36:39]
v_lshl_add_u32 v238, v249, 5, v238
v_lshrrev_b32 v249, 9, v255
v_mfma_f32_16x16x32_bf16 v[36:39], v[204:207], a[84:87], v[36:39]
buffer_load_dwordx4 v[156:159], v247, s[44:47], s52 offen offset:0 sc1
v_mfma_f32_16x16x32_bf16 v[4:7], v[204:207], a[80:83], v[4:7]
s_waitcnt lgkmcnt(4)
v_lshl_add_u32 v238, v249, 4, v238
v_mfma_f32_16x16x32_bf16 v[4:7], v[200:203], a[48:51], v[4:7]
ds_read_b64_tr_b16 v[204:205], v241 offset:17024
ds_read_b64_tr_b16 v[206:207], v241 offset:18048
v_mfma_f32_16x16x32_bf16 v[8:11], v[208:211], a[48:51], v[8:11]
s_lshr_b32 s23, s72, 3
s_lshl3_add_u32 s92, s23, s76
v_mfma_f32_16x16x32_bf16 v[8:11], v[212:215], a[80:83], v[8:11]
buffer_load_dwordx4 v[168:171], v244, s[44:47], s52 offen offset:1024 sc1
v_mfma_f32_16x16x32_bf16 v[40:43], v[212:215], a[84:87], v[40:43]
s_lshl_b32 s92, s92, 19
s_lshl_b32 s97, s3, 16
v_mfma_f32_16x16x32_bf16 v[40:43], v[208:211], a[52:55], v[40:43]
ds_read_b64_tr_b16 v[200:201], v241 offset:25472
ds_read_b64_tr_b16 v[202:203], v241 offset:26496
v_mfma_f32_16x16x32_bf16 v[72:75], v[208:211], a[56:59], v[72:75]
s_add_u32 s94, s92, s97
s_add_u32 s95, s94, 32768
v_mfma_f32_16x16x32_bf16 v[72:75], v[212:215], a[88:91], v[72:75]
buffer_load_dwordx4 v[172:175], v245, s[44:47], s52 offen offset:1024 sc1
v_mfma_f32_16x16x32_bf16 v[104:107], v[212:215], a[92:95], v[104:107]
s_sub_u32 s96, s92, s97
s_add_u32 s96, s96, 458752
v_mfma_f32_16x16x32_bf16 v[104:107], v[208:211], a[60:63], v[104:107]
ds_read_b64_tr_b16 v[212:213], v242 offset:17024
ds_read_b64_tr_b16 v[214:215], v242 offset:18048
v_mfma_f32_16x16x32_bf16 v[108:111], v[216:219], a[60:63], v[108:111]
s_add_u32 s97, s96, 32768
s_lshl3_add_u32 s92, s23, s101
v_mfma_f32_16x16x32_bf16 v[108:111], v[220:223], a[92:95], v[108:111]
buffer_load_dwordx4 v[184:187], v246, s[44:47], s52 offen offset:1024 sc1
v_mfma_f32_16x16x32_bf16 v[76:79], v[220:223], a[88:91], v[76:79]
s_lshl_b32 s85, s76, 14
s_lshl_b32 s92, s92, 3
v_mfma_f32_16x16x32_bf16 v[76:79], v[216:219], a[56:59], v[76:79]
ds_read_b64_tr_b16 v[208:209], v242 offset:25472
ds_read_b64_tr_b16 v[210:211], v242 offset:26496
v_mfma_f32_16x16x32_bf16 v[44:47], v[216:219], a[52:55], v[44:47]
s_add_u32 s92, s92, s85
s_add_u32 s92, s92, 2097152
v_mfma_f32_16x16x32_bf16 v[44:47], v[220:223], a[84:87], v[44:47]
v_accvgpr_read_b32 v230, a177
v_accvgpr_read_b32 v231, a181
v_mfma_f32_16x16x32_bf16 v[12:15], v[220:223], a[80:83], v[12:15]
s_waitcnt lgkmcnt(4)
v_perm_b32 v232, v231, v230, s86
v_mfma_f32_16x16x32_bf16 v[12:15], v[216:219], a[48:51], v[12:15]
ds_read_b64_tr_b16 v[220:221], v243 offset:17024
ds_read_b64_tr_b16 v[222:223], v243 offset:18048
v_mfma_f32_16x16x32_bf16 v[16:19], v[196:199], a[48:51], v[16:19]
v_perm_b32 v233, v231, v230, s87
v_accvgpr_write_b32 a138, v232
v_mfma_f32_16x16x32_bf16 v[16:19], v[192:195], a[80:83], v[16:19]
buffer_load_dwordx4 v[188:191], v247, s[44:47], s52 offen offset:1024 sc1
v_mfma_f32_16x16x32_bf16 v[48:51], v[192:195], a[84:87], v[48:51]
v_accvgpr_write_b32 a142, v233
v_accvgpr_read_b32 v234, a185
v_mfma_f32_16x16x32_bf16 v[48:51], v[196:199], a[52:55], v[48:51]
ds_read_b64_tr_b16 v[216:217], v243 offset:25472
ds_read_b64_tr_b16 v[218:219], v243 offset:26496
v_mfma_f32_16x16x32_bf16 v[80:83], v[196:199], a[56:59], v[80:83]
s_add_u32 m0, s83, 50688
v_accvgpr_read_b32 v235, a189
v_mfma_f32_16x16x32_bf16 v[80:83], v[192:195], a[88:91], v[80:83]
buffer_load_dwordx4 v224, s[56:59], s80 offen lds
v_mfma_f32_16x16x32_bf16 v[112:115], v[192:195], a[92:95], v[112:115]
v_perm_b32 v236, v235, v234, s86
v_perm_b32 v237, v235, v234, s87
v_mfma_f32_16x16x32_bf16 v[112:115], v[196:199], a[60:63], v[112:115]
ds_read_b64_tr_b16 v[192:193], v240 offset:33792
ds_read_b64_tr_b16 v[194:195], v240 offset:34816
v_mfma_f32_16x16x32_bf16 v[116:119], v[204:207], a[60:63], v[116:119]
s_add_u32 m0, s83, 51712
v_accvgpr_write_b32 a139, v236
v_mfma_f32_16x16x32_bf16 v[116:119], v[200:203], a[92:95], v[116:119]
buffer_load_dwordx4 v225, s[56:59], s80 offen lds
v_mfma_f32_16x16x32_bf16 v[84:87], v[200:203], a[88:91], v[84:87]
v_accvgpr_write_b32 a143, v237
v_accvgpr_read_b32 v230, a162
v_mfma_f32_16x16x32_bf16 v[84:87], v[204:207], a[56:59], v[84:87]
ds_read_b64_tr_b16 v[196:197], v240 offset:42240
ds_read_b64_tr_b16 v[198:199], v240 offset:43264
v_mfma_f32_16x16x32_bf16 v[52:55], v[204:207], a[52:55], v[52:55]
s_add_u32 m0, s83, 52864
v_accvgpr_read_b32 v231, a166
v_mfma_f32_16x16x32_bf16 v[52:55], v[200:203], a[84:87], v[52:55]
buffer_load_dwordx4 v226, s[56:59], s80 offen lds
v_mfma_f32_16x16x32_bf16 v[20:23], v[200:203], a[80:83], v[20:23]
s_waitcnt lgkmcnt(4)
v_perm_b32 v232, v231, v230, s86
v_mfma_f32_16x16x32_bf16 v[20:23], v[204:207], a[48:51], v[20:23]
ds_read_b64_tr_b16 v[200:201], v241 offset:33792
ds_read_b64_tr_b16 v[202:203], v241 offset:34816
v_mfma_f32_16x16x32_bf16 v[24:27], v[212:215], a[48:51], v[24:27]
v_perm_b32 v233, v231, v230, s87
v_accvgpr_write_b32 a144, v232
v_mfma_f32_16x16x32_bf16 v[24:27], v[208:211], a[80:83], v[24:27]
buffer_load_dwordx4 a[32:35], v244, s[44:47], s52 offen offset:2048 sc1
v_mfma_f32_16x16x32_bf16 v[56:59], v[208:211], a[84:87], v[56:59]
v_accvgpr_write_b32 a148, v233
v_accvgpr_read_b32 v234, a170
v_mfma_f32_16x16x32_bf16 v[56:59], v[212:215], a[52:55], v[56:59]
ds_read_b64_tr_b16 v[204:205], v241 offset:42240
ds_read_b64_tr_b16 v[206:207], v241 offset:43264
v_mfma_f32_16x16x32_bf16 v[88:91], v[212:215], a[56:59], v[88:91]
v_accvgpr_read_b32 v235, a174
v_perm_b32 v236, v235, v234, s86
v_mfma_f32_16x16x32_bf16 v[88:91], v[208:211], a[88:91], v[88:91]
buffer_load_dwordx4 a[36:39], v245, s[44:47], s52 offen offset:2048 sc1
v_mfma_f32_16x16x32_bf16 v[120:123], v[208:211], a[92:95], v[120:123]
v_perm_b32 v237, v235, v234, s87
v_accvgpr_write_b32 a145, v236
v_mfma_f32_16x16x32_bf16 v[120:123], v[212:215], a[60:63], v[120:123]
ds_read_b64_tr_b16 v[208:209], v242 offset:33792
ds_read_b64_tr_b16 v[210:211], v242 offset:34816
v_mfma_f32_16x16x32_bf16 v[124:127], v[220:223], a[60:63], v[124:127]
v_accvgpr_write_b32 a149, v237
v_accvgpr_read_b32 v230, a178
v_mfma_f32_16x16x32_bf16 v[124:127], v[216:219], a[92:95], v[124:127]
buffer_load_dwordx4 a[40:43], v246, s[44:47], s52 offen offset:2048 sc1
v_mfma_f32_16x16x32_bf16 v[92:95], v[216:219], a[88:91], v[92:95]
v_accvgpr_read_b32 v231, a182
v_perm_b32 v232, v231, v230, s86
v_mfma_f32_16x16x32_bf16 v[92:95], v[220:223], a[56:59], v[92:95]
ds_read_b64_tr_b16 v[212:213], v242 offset:42240
ds_read_b64_tr_b16 v[214:215], v242 offset:43264
v_mfma_f32_16x16x32_bf16 v[60:63], v[220:223], a[52:55], v[60:63]
v_perm_b32 v233, v231, v230, s87
v_accvgpr_write_b32 a146, v232
v_mfma_f32_16x16x32_bf16 v[60:63], v[216:219], a[84:87], v[60:63]
v_accvgpr_write_b32 a150, v233
v_accvgpr_read_b32 v234, a186
v_mfma_f32_16x16x32_bf16 v[28:31], v[216:219], a[80:83], v[28:31]
s_waitcnt vmcnt(20) lgkmcnt(4)
v_accvgpr_read_b32 v235, a190
v_mfma_f32_16x16x32_bf16 v[28:31], v[220:223], a[48:51], v[28:31]
ds_read_b64_tr_b16 v[216:217], v243 offset:33792
ds_read_b64_tr_b16 v[218:219], v243 offset:34816
v_mfma_f32_16x16x32_bf16 v[0:3], v[192:195], a[0:3], v[0:3]
v_perm_b32 v236, v235, v234, s86
v_perm_b32 v237, v235, v234, s87
v_mfma_f32_16x16x32_bf16 v[0:3], v[196:199], a[16:19], v[0:3]
buffer_load_dwordx4 a[44:47], v247, s[44:47], s52 offen offset:2048 sc1
v_mfma_f32_16x16x32_bf16 v[32:35], v[196:199], a[20:23], v[32:35]
v_accvgpr_write_b32 a147, v236
v_accvgpr_write_b32 a151, v237
v_mfma_f32_16x16x32_bf16 v[32:35], v[192:195], a[4:7], v[32:35]
ds_read_b64_tr_b16 v[220:221], v243 offset:42240
ds_read_b64_tr_b16 v[222:223], v243 offset:43264
v_mfma_f32_16x16x32_bf16 v[64:67], v[192:195], a[8:11], v[64:67]
v_accvgpr_read_b32 v230, a163
v_accvgpr_read_b32 v231, a167
v_mfma_f32_16x16x32_bf16 v[64:67], v[196:199], a[24:27], v[64:67]
buffer_load_dwordx4 a[48:51], v244, s[44:47], s52 offen offset:3072 sc1
v_mfma_f32_16x16x32_bf16 v[96:99], v[196:199], a[28:31], v[96:99]
v_perm_b32 v232, v231, v230, s86
v_perm_b32 v233, v231, v230, s87
v_mfma_f32_16x16x32_bf16 v[96:99], v[192:195], a[12:15], v[96:99]
ds_read_b64_tr_b16 v[196:197], v240 offset:33920
ds_read_b64_tr_b16 v[198:199], v240 offset:34944
v_mfma_f32_16x16x32_bf16 v[100:103], v[200:203], a[12:15], v[100:103]
v_accvgpr_write_b32 a152, v232
v_accvgpr_write_b32 a156, v233
v_mfma_f32_16x16x32_bf16 v[100:103], v[204:207], a[28:31], v[100:103]
buffer_load_dwordx4 a[52:55], v245, s[44:47], s52 offen offset:3072 sc1
v_mfma_f32_16x16x32_bf16 v[68:71], v[204:207], a[24:27], v[68:71]
v_accvgpr_read_b32 v234, a171
v_accvgpr_read_b32 v235, a175
v_mfma_f32_16x16x32_bf16 v[68:71], v[200:203], a[8:11], v[68:71]
ds_read_b64_tr_b16 v[192:193], v240 offset:42368
ds_read_b64_tr_b16 v[194:195], v240 offset:43392
v_mfma_f32_16x16x32_bf16 v[36:39], v[200:203], a[4:7], v[36:39]
v_add_u32 v240, 4294899712, v240
v_perm_b32 v236, v235, v234, s86
v_mfma_f32_16x16x32_bf16 v[36:39], v[204:207], a[20:23], v[36:39]
buffer_load_dwordx4 a[56:59], v246, s[44:47], s52 offen offset:3072 sc1
v_mfma_f32_16x16x32_bf16 v[4:7], v[204:207], a[16:19], v[4:7]
s_waitcnt lgkmcnt(4)
s_waitcnt vmcnt(23)
v_mfma_f32_16x16x32_bf16 v[4:7], v[200:203], a[0:3], v[4:7]
ds_read_b64_tr_b16 v[204:205], v241 offset:33920
ds_read_b64_tr_b16 v[206:207], v241 offset:34944
v_mfma_f32_16x16x32_bf16 v[8:11], v[208:211], a[0:3], v[8:11]
v_cmp_eq_u64 vcc, v[250:251], v[252:253]
v_perm_b32 v237, v235, v234, s87
v_mfma_f32_16x16x32_bf16 v[8:11], v[212:215], a[16:19], v[8:11]
buffer_load_dwordx4 a[60:63], v247, s[44:47], s52 offen offset:3072 sc1
v_mfma_f32_16x16x32_bf16 v[40:43], v[212:215], a[20:23], v[40:43]
v_accvgpr_write_b32 a153, v236
v_accvgpr_write_b32 a157, v237
v_mfma_f32_16x16x32_bf16 v[40:43], v[208:211], a[4:7], v[40:43]
ds_read_b64_tr_b16 v[200:201], v241 offset:42368
ds_read_b64_tr_b16 v[202:203], v241 offset:43392
v_mfma_f32_16x16x32_bf16 v[72:75], v[208:211], a[8:11], v[72:75]
s_add_u32 m0, s83, 53888
v_add_u32 v241, 4294899712, v241
v_mfma_f32_16x16x32_bf16 v[72:75], v[212:215], a[24:27], v[72:75]
buffer_load_dwordx4 v227, s[56:59], s80 offen lds
v_mfma_f32_16x16x32_bf16 v[104:107], v[212:215], a[28:31], v[104:107]
s_add_u32 s80, s80, 131072
v_accvgpr_read_b32 v230, a179
v_mfma_f32_16x16x32_bf16 v[104:107], v[208:211], a[12:15], v[104:107]
ds_read_b64_tr_b16 v[212:213], v242 offset:33920
ds_read_b64_tr_b16 v[214:215], v242 offset:34944
v_mfma_f32_16x16x32_bf16 v[108:111], v[216:219], a[12:15], v[108:111]
v_accvgpr_read_b32 v231, a183
v_perm_b32 v232, v231, v230, s86
v_mfma_f32_16x16x32_bf16 v[108:111], v[220:223], a[28:31], v[108:111]
buffer_load_dwordx2 v[250:251], v248, s[64:67], s5 offen sc1
v_mfma_f32_16x16x32_bf16 v[76:79], v[220:223], a[24:27], v[76:79]
v_perm_b32 v233, v231, v230, s87
v_accvgpr_write_b32 a154, v232
v_mfma_f32_16x16x32_bf16 v[76:79], v[216:219], a[8:11], v[76:79]
ds_read_b64_tr_b16 v[208:209], v242 offset:42368
ds_read_b64_tr_b16 v[210:211], v242 offset:43392
v_mfma_f32_16x16x32_bf16 v[44:47], v[216:219], a[4:7], v[44:47]
s_add_u32 m0, s83, 0
v_add_u32 v242, 4294899712, v242
v_mfma_f32_16x16x32_bf16 v[44:47], v[220:223], a[20:23], v[44:47]
buffer_load_dwordx4 v224, s[56:59], s80 offen lds
v_mfma_f32_16x16x32_bf16 v[12:15], v[220:223], a[16:19], v[12:15]
s_waitcnt lgkmcnt(4)
v_accvgpr_write_b32 a158, v233
v_mfma_f32_16x16x32_bf16 v[12:15], v[216:219], a[0:3], v[12:15]
ds_read_b64_tr_b16 v[220:221], v243 offset:33920
ds_read_b64_tr_b16 v[222:223], v243 offset:34944
v_mfma_f32_16x16x32_bf16 v[16:19], v[196:199], a[0:3], v[16:19]
s_add_u32 m0, s83, 1024
v_accvgpr_read_b32 v234, a187
v_mfma_f32_16x16x32_bf16 v[16:19], v[192:195], a[16:19], v[16:19]
buffer_load_dwordx4 v225, s[56:59], s80 offen lds
v_mfma_f32_16x16x32_bf16 v[48:51], v[192:195], a[20:23], v[48:51]
v_accvgpr_read_b32 v235, a191
v_perm_b32 v236, v235, v234, s86
v_mfma_f32_16x16x32_bf16 v[48:51], v[196:199], a[4:7], v[48:51]
ds_read_b64_tr_b16 v[216:217], v243 offset:42368
ds_read_b64_tr_b16 v[218:219], v243 offset:43392
v_mfma_f32_16x16x32_bf16 v[80:83], v[196:199], a[8:11], v[80:83]
s_add_u32 m0, s83, 2176
v_add_u32 v243, 4294899712, v243
v_mfma_f32_16x16x32_bf16 v[80:83], v[192:195], a[24:27], v[80:83]
buffer_load_dwordx4 v226, s[56:59], s80 offen lds
v_mfma_f32_16x16x32_bf16 v[112:115], v[192:195], a[28:31], v[112:115]
s_waitcnt vmcnt(24) lgkmcnt(0)
s_barrier
v_mfma_f32_16x16x32_bf16 v[112:115], v[196:199], a[12:15], v[112:115]
ds_read_b64_tr_b16 v[192:193], v240 offset:16896
ds_read_b64_tr_b16 v[194:195], v240 offset:17920
v_mfma_f32_16x16x32_bf16 v[116:119], v[204:207], a[12:15], v[116:119]
s_add_u32 m0, s83, 3200
v_perm_b32 v237, v235, v234, s87
v_mfma_f32_16x16x32_bf16 v[116:119], v[200:203], a[28:31], v[116:119]
buffer_load_dwordx4 v227, s[56:59], s80 offen lds
v_mfma_f32_16x16x32_bf16 v[84:87], v[200:203], a[24:27], v[84:87]
s_bitcmp1_b32 vcc_lo, 5
s_cbranch_scc0 .La5_LO_N5
.La5_LO_K5:
v_mfma_f32_16x16x32_bf16 v[84:87], v[204:207], a[8:11], v[84:87]
ds_read_b64_tr_b16 v[196:197], v240 offset:25344
ds_read_b64_tr_b16 v[198:199], v240 offset:26368
v_mfma_f32_16x16x32_bf16 v[52:55], v[204:207], a[4:7], v[52:55]
s_bfe_u32 s80, s4, 262168
s_xor_b32 s80, s80, s72
v_mfma_f32_16x16x32_bf16 v[52:55], v[200:203], a[20:23], v[52:55]
buffer_load_dwordx4 v[128:131], v244, s[44:47], s53 offen offset:0 sc1
v_mfma_f32_16x16x32_bf16 v[20:23], v[200:203], a[16:19], v[20:23]
s_nop 0
s_lshl_b32 s80, s80, 18
v_mfma_f32_16x16x32_bf16 v[20:23], v[204:207], a[0:3], v[20:23]
ds_read_b64_tr_b16 v[200:201], v241 offset:16896
ds_read_b64_tr_b16 v[202:203], v241 offset:17920
v_mfma_f32_16x16x32_bf16 v[24:27], v[212:215], a[0:3], v[24:27]
v_accvgpr_write_b32 a155, v236
v_accvgpr_write_b32 a159, v237
v_mfma_f32_16x16x32_bf16 v[24:27], v[208:211], a[16:19], v[24:27]
buffer_load_dwordx4 v[132:135], v245, s[44:47], s53 offen offset:0 sc1
v_mfma_f32_16x16x32_bf16 v[56:59], v[208:211], a[20:23], v[56:59]
v_accvgpr_read_b32 v230, a192
v_accvgpr_read_b32 v231, a196
v_mfma_f32_16x16x32_bf16 v[56:59], v[212:215], a[4:7], v[56:59]
ds_read_b64_tr_b16 v[204:205], v241 offset:25344
ds_read_b64_tr_b16 v[206:207], v241 offset:26368
v_mfma_f32_16x16x32_bf16 v[88:91], v[212:215], a[8:11], v[88:91]
v_perm_b32 v232, v231, v230, s86
v_perm_b32 v233, v231, v230, s87
v_mfma_f32_16x16x32_bf16 v[88:91], v[208:211], a[24:27], v[88:91]
buffer_load_dwordx4 v[144:147], v246, s[44:47], s53 offen offset:0 sc1
v_mfma_f32_16x16x32_bf16 v[120:123], v[208:211], a[28:31], v[120:123]
v_accvgpr_write_b32 a160, v232
v_accvgpr_write_b32 a164, v233
v_mfma_f32_16x16x32_bf16 v[120:123], v[212:215], a[12:15], v[120:123]
ds_read_b64_tr_b16 v[208:209], v242 offset:16896
ds_read_b64_tr_b16 v[210:211], v242 offset:17920
v_mfma_f32_16x16x32_bf16 v[124:127], v[220:223], a[12:15], v[124:127]
v_accvgpr_read_b32 v234, a200
v_accvgpr_read_b32 v235, a204
v_mfma_f32_16x16x32_bf16 v[124:127], v[216:219], a[28:31], v[124:127]
buffer_load_dwordx4 a[12:15], v247, s[44:47], s53 offen offset:0 sc1
v_mfma_f32_16x16x32_bf16 v[92:95], v[216:219], a[24:27], v[92:95]
v_perm_b32 v236, v235, v234, s86
v_perm_b32 v237, v235, v234, s87
v_mfma_f32_16x16x32_bf16 v[92:95], v[220:223], a[8:11], v[92:95]
ds_read_b64_tr_b16 v[212:213], v242 offset:25344
ds_read_b64_tr_b16 v[214:215], v242 offset:26368
v_mfma_f32_16x16x32_bf16 v[60:63], v[220:223], a[4:7], v[60:63]
v_accvgpr_write_b32 a161, v236
v_accvgpr_write_b32 a165, v237
v_mfma_f32_16x16x32_bf16 v[60:63], v[216:219], a[20:23], v[60:63]
v_accvgpr_read_b32 v230, a208
v_accvgpr_read_b32 v231, a212
v_mfma_f32_16x16x32_bf16 v[28:31], v[216:219], a[16:19], v[28:31]
s_waitcnt vmcnt(21) lgkmcnt(4)
v_perm_b32 v232, v231, v230, s86
v_mfma_f32_16x16x32_bf16 v[28:31], v[220:223], a[0:3], v[28:31]
ds_read_b64_tr_b16 v[216:217], v243 offset:16896
ds_read_b64_tr_b16 v[218:219], v243 offset:17920
v_mfma_f32_16x16x32_bf16 v[0:3], v[192:195], v[136:139], v[0:3]
v_perm_b32 v233, v231, v230, s87
v_accvgpr_write_b32 a162, v232
v_mfma_f32_16x16x32_bf16 v[0:3], v[196:199], v[168:171], v[0:3]
buffer_load_dwordx4 a[0:3], v244, s[44:47], s53 offen offset:1024 sc1
v_mfma_f32_16x16x32_bf16 v[32:35], v[196:199], v[172:175], v[32:35]
v_accvgpr_write_b32 a166, v233
v_accvgpr_read_b32 v234, a216
v_mfma_f32_16x16x32_bf16 v[32:35], v[192:195], v[140:143], v[32:35]
ds_read_b64_tr_b16 v[220:221], v243 offset:25344
ds_read_b64_tr_b16 v[222:223], v243 offset:26368
v_mfma_f32_16x16x32_bf16 v[64:67], v[192:195], v[152:155], v[64:67]
v_accvgpr_read_b32 v235, a220
v_perm_b32 v236, v235, v234, s86
v_mfma_f32_16x16x32_bf16 v[64:67], v[196:199], v[184:187], v[64:67]
buffer_load_dwordx4 a[4:7], v245, s[44:47], s53 offen offset:1024 sc1
v_mfma_f32_16x16x32_bf16 v[96:99], v[196:199], v[188:191], v[96:99]
v_perm_b32 v237, v235, v234, s87
v_accvgpr_write_b32 a163, v236
v_mfma_f32_16x16x32_bf16 v[96:99], v[192:195], v[156:159], v[96:99]
ds_read_b64_tr_b16 v[196:197], v240 offset:17024
ds_read_b64_tr_b16 v[198:199], v240 offset:18048
v_mfma_f32_16x16x32_bf16 v[100:103], v[200:203], v[156:159], v[100:103]
v_accvgpr_write_b32 a167, v237
v_accvgpr_read_b32 v230, a193
v_mfma_f32_16x16x32_bf16 v[100:103], v[204:207], v[188:191], v[100:103]
buffer_load_dwordx4 a[8:11], v246, s[44:47], s53 offen offset:1024 sc1
v_mfma_f32_16x16x32_bf16 v[68:71], v[204:207], v[184:187], v[68:71]
v_accvgpr_read_b32 v231, a197
v_perm_b32 v232, v231, v230, s86
v_mfma_f32_16x16x32_bf16 v[68:71], v[200:203], v[152:155], v[68:71]
ds_read_b64_tr_b16 v[192:193], v240 offset:25472
ds_read_b64_tr_b16 v[194:195], v240 offset:26496
v_mfma_f32_16x16x32_bf16 v[36:39], v[200:203], v[140:143], v[36:39]
v_perm_b32 v233, v231, v230, s87
v_accvgpr_write_b32 a168, v232
v_mfma_f32_16x16x32_bf16 v[36:39], v[204:207], v[172:175], v[36:39]
buffer_load_dwordx4 a[16:19], v247, s[44:47], s53 offen offset:1024 sc1
v_mfma_f32_16x16x32_bf16 v[4:7], v[204:207], v[168:171], v[4:7]
s_waitcnt lgkmcnt(4)
v_accvgpr_write_b32 a172, v233
v_mfma_f32_16x16x32_bf16 v[4:7], v[200:203], v[136:139], v[4:7]
ds_read_b64_tr_b16 v[204:205], v241 offset:17024
ds_read_b64_tr_b16 v[206:207], v241 offset:18048
v_mfma_f32_16x16x32_bf16 v[8:11], v[208:211], v[136:139], v[8:11]
s_add_u32 m0, s83, 118272
v_accvgpr_read_b32 v234, a201
v_mfma_f32_16x16x32_bf16 v[8:11], v[212:215], v[168:171], v[8:11]
buffer_load_dwordx4 v224, s[56:59], s80 offen lds
v_mfma_f32_16x16x32_bf16 v[40:43], v[212:215], v[172:175], v[40:43]
v_accvgpr_read_b32 v235, a205
v_perm_b32 v236, v235, v234, s86
v_mfma_f32_16x16x32_bf16 v[40:43], v[208:211], v[140:143], v[40:43]
ds_read_b64_tr_b16 v[200:201], v241 offset:25472
ds_read_b64_tr_b16 v[202:203], v241 offset:26496
v_mfma_f32_16x16x32_bf16 v[72:75], v[208:211], v[152:155], v[72:75]
s_add_u32 m0, s83, 119296
v_perm_b32 v237, v235, v234, s87
v_mfma_f32_16x16x32_bf16 v[72:75], v[212:215], v[184:187], v[72:75]
buffer_load_dwordx4 v225, s[56:59], s80 offen lds
v_mfma_f32_16x16x32_bf16 v[104:107], v[212:215], v[188:191], v[104:107]
v_accvgpr_write_b32 a169, v236
v_accvgpr_write_b32 a173, v237
v_mfma_f32_16x16x32_bf16 v[104:107], v[208:211], v[156:159], v[104:107]
ds_read_b64_tr_b16 v[212:213], v242 offset:17024
ds_read_b64_tr_b16 v[214:215], v242 offset:18048
v_mfma_f32_16x16x32_bf16 v[108:111], v[216:219], v[156:159], v[108:111]
s_add_u32 m0, s83, 120448
v_accvgpr_read_b32 v230, a209
v_mfma_f32_16x16x32_bf16 v[108:111], v[220:223], v[188:191], v[108:111]
buffer_load_dwordx4 v226, s[56:59], s80 offen lds
v_mfma_f32_16x16x32_bf16 v[76:79], v[220:223], v[184:187], v[76:79]
v_accvgpr_read_b32 v231, a213
v_perm_b32 v232, v231, v230, s86
v_mfma_f32_16x16x32_bf16 v[76:79], v[216:219], v[152:155], v[76:79]
ds_read_b64_tr_b16 v[208:209], v242 offset:25472
ds_read_b64_tr_b16 v[210:211], v242 offset:26496
v_mfma_f32_16x16x32_bf16 v[44:47], v[216:219], v[140:143], v[44:47]
v_perm_b32 v233, v231, v230, s87
v_accvgpr_write_b32 a170, v232
v_mfma_f32_16x16x32_bf16 v[44:47], v[220:223], v[172:175], v[44:47]
v_accvgpr_write_b32 a174, v233
.La5_LW3_2:
s_cbranch_execz .La5_LW3_R0
v_accvgpr_read_b32 v234, a217
v_mfma_f32_16x16x32_bf16 v[12:15], v[220:223], v[168:171], v[12:15]
s_waitcnt lgkmcnt(4)
v_accvgpr_read_b32 v235, a221
v_mfma_f32_16x16x32_bf16 v[12:15], v[216:219], v[136:139], v[12:15]
ds_read_b64_tr_b16 v[220:221], v243 offset:17024
ds_read_b64_tr_b16 v[222:223], v243 offset:18048
v_mfma_f32_16x16x32_bf16 v[16:19], v[196:199], v[136:139], v[16:19]
v_perm_b32 v236, v235, v234, s86
v_perm_b32 v237, v235, v234, s87
v_mfma_f32_16x16x32_bf16 v[16:19], v[192:195], v[168:171], v[16:19]
buffer_load_dwordx4 a[20:23], v244, s[44:47], s53 offen offset:2048 sc1
v_mfma_f32_16x16x32_bf16 v[48:51], v[192:195], v[172:175], v[48:51]
v_accvgpr_write_b32 a171, v236
v_accvgpr_write_b32 a175, v237
v_mfma_f32_16x16x32_bf16 v[48:51], v[196:199], v[140:143], v[48:51]
ds_read_b64_tr_b16 v[216:217], v243 offset:25472
ds_read_b64_tr_b16 v[218:219], v243 offset:26496
v_mfma_f32_16x16x32_bf16 v[80:83], v[196:199], v[152:155], v[80:83]
v_accvgpr_read_b32 v230, a194
v_accvgpr_read_b32 v231, a198
v_mfma_f32_16x16x32_bf16 v[80:83], v[192:195], v[184:187], v[80:83]
buffer_load_dwordx4 a[24:27], v245, s[44:47], s53 offen offset:2048 sc1
v_mfma_f32_16x16x32_bf16 v[112:115], v[192:195], v[188:191], v[112:115]
v_perm_b32 v232, v231, v230, s86
v_perm_b32 v233, v231, v230, s87
v_mfma_f32_16x16x32_bf16 v[112:115], v[196:199], v[156:159], v[112:115]
ds_read_b64_tr_b16 v[192:193], v240 offset:33792
ds_read_b64_tr_b16 v[194:195], v240 offset:34816
v_mfma_f32_16x16x32_bf16 v[116:119], v[204:207], v[156:159], v[116:119]
v_accvgpr_write_b32 a176, v232
v_accvgpr_write_b32 a180, v233
v_mfma_f32_16x16x32_bf16 v[116:119], v[200:203], v[188:191], v[116:119]
buffer_load_dwordx4 a[28:31], v246, s[44:47], s53 offen offset:2048 sc1
v_mfma_f32_16x16x32_bf16 v[84:87], v[200:203], v[184:187], v[84:87]
v_accvgpr_read_b32 v234, a202
v_accvgpr_read_b32 v235, a206
v_mfma_f32_16x16x32_bf16 v[84:87], v[204:207], v[152:155], v[84:87]
ds_read_b64_tr_b16 v[196:197], v240 offset:42240
ds_read_b64_tr_b16 v[198:199], v240 offset:43264
v_mfma_f32_16x16x32_bf16 v[52:55], v[204:207], v[140:143], v[52:55]
v_perm_b32 v236, v235, v234, s86
v_perm_b32 v237, v235, v234, s87
v_mfma_f32_16x16x32_bf16 v[52:55], v[200:203], v[172:175], v[52:55]
buffer_load_dwordx4 v[148:151], v247, s[44:47], s53 offen offset:2048 sc1
v_mfma_f32_16x16x32_bf16 v[20:23], v[200:203], v[168:171], v[20:23]
s_waitcnt lgkmcnt(4)
v_accvgpr_write_b32 a177, v236
v_mfma_f32_16x16x32_bf16 v[20:23], v[204:207], v[136:139], v[20:23]
ds_read_b64_tr_b16 v[200:201], v241 offset:33792
ds_read_b64_tr_b16 v[202:203], v241 offset:34816
v_mfma_f32_16x16x32_bf16 v[24:27], v[212:215], v[136:139], v[24:27]
v_accvgpr_write_b32 a181, v237
v_accvgpr_read_b32 v230, a210
v_mfma_f32_16x16x32_bf16 v[24:27], v[208:211], v[168:171], v[24:27]
buffer_load_dwordx4 v[160:163], v244, s[44:47], s53 offen offset:3072 sc1
v_mfma_f32_16x16x32_bf16 v[56:59], v[208:211], v[172:175], v[56:59]
v_accvgpr_read_b32 v231, a214
v_perm_b32 v232, v231, v230, s86
v_mfma_f32_16x16x32_bf16 v[56:59], v[212:215], v[140:143], v[56:59]
ds_read_b64_tr_b16 v[204:205], v241 offset:42240
ds_read_b64_tr_b16 v[206:207], v241 offset:43264
v_mfma_f32_16x16x32_bf16 v[88:91], v[212:215], v[152:155], v[88:91]
v_perm_b32 v233, v231, v230, s87
v_accvgpr_write_b32 a178, v232
v_mfma_f32_16x16x32_bf16 v[88:91], v[208:211], v[184:187], v[88:91]
buffer_load_dwordx4 v[164:167], v245, s[44:47], s53 offen offset:3072 sc1
v_mfma_f32_16x16x32_bf16 v[120:123], v[208:211], v[188:191], v[120:123]
v_accvgpr_write_b32 a182, v233
v_accvgpr_read_b32 v234, a218
v_mfma_f32_16x16x32_bf16 v[120:123], v[212:215], v[156:159], v[120:123]
ds_read_b64_tr_b16 v[208:209], v242 offset:33792
ds_read_b64_tr_b16 v[210:211], v242 offset:34816
v_mfma_f32_16x16x32_bf16 v[124:127], v[220:223], v[156:159], v[124:127]
v_accvgpr_read_b32 v235, a222
v_perm_b32 v236, v235, v234, s86
v_mfma_f32_16x16x32_bf16 v[124:127], v[216:219], v[188:191], v[124:127]
buffer_load_dwordx4 v[156:159], v246, s[44:47], s53 offen offset:3072 sc1
v_mfma_f32_16x16x32_bf16 v[92:95], v[216:219], v[184:187], v[92:95]
v_perm_b32 v237, v235, v234, s87
v_accvgpr_write_b32 a179, v236
v_mfma_f32_16x16x32_bf16 v[92:95], v[220:223], v[152:155], v[92:95]
ds_read_b64_tr_b16 v[212:213], v242 offset:42240
ds_read_b64_tr_b16 v[214:215], v242 offset:43264
v_mfma_f32_16x16x32_bf16 v[60:63], v[220:223], v[140:143], v[60:63]
v_accvgpr_write_b32 a183, v237
v_accvgpr_read_b32 v230, a195
v_mfma_f32_16x16x32_bf16 v[60:63], v[216:219], v[172:175], v[60:63]
s_waitcnt vmcnt(22)
v_cmp_eq_u64 vcc, v[250:251], v[252:253]
v_mfma_f32_16x16x32_bf16 v[28:31], v[216:219], v[168:171], v[28:31]
s_waitcnt lgkmcnt(4)
v_accvgpr_read_b32 v231, a199
v_mfma_f32_16x16x32_bf16 v[28:31], v[220:223], v[136:139], v[28:31]
ds_read_b64_tr_b16 v[216:217], v243 offset:33792
ds_read_b64_tr_b16 v[218:219], v243 offset:34816
v_mfma_f32_16x16x32_bf16 v[0:3], v[192:195], a[32:35], v[0:3]
v_perm_b32 v232, v231, v230, s86
v_perm_b32 v233, v231, v230, s87
v_mfma_f32_16x16x32_bf16 v[0:3], v[196:199], a[48:51], v[0:3]
buffer_load_dwordx4 v[136:139], v247, s[44:47], s53 offen offset:3072 sc1
v_mfma_f32_16x16x32_bf16 v[32:35], v[196:199], a[52:55], v[32:35]
v_accvgpr_write_b32 a184, v232
v_accvgpr_write_b32 a188, v233
v_mfma_f32_16x16x32_bf16 v[32:35], v[192:195], a[36:39], v[32:35]
ds_read_b64_tr_b16 v[220:221], v243 offset:42240
ds_read_b64_tr_b16 v[222:223], v243 offset:43264
v_mfma_f32_16x16x32_bf16 v[64:67], v[192:195], a[40:43], v[64:67]
s_add_u32 m0, s83, 121472
v_accvgpr_read_b32 v234, a203
v_mfma_f32_16x16x32_bf16 v[64:67], v[196:199], a[56:59], v[64:67]
buffer_load_dwordx4 v227, s[56:59], s80 offen lds
v_mfma_f32_16x16x32_bf16 v[96:99], v[196:199], a[60:63], v[96:99]
s_add_u32 s80, s80, 131072
v_accvgpr_read_b32 v235, a207
v_mfma_f32_16x16x32_bf16 v[96:99], v[192:195], a[44:47], v[96:99]
ds_read_b64_tr_b16 v[196:197], v240 offset:33920
ds_read_b64_tr_b16 v[198:199], v240 offset:34944
v_mfma_f32_16x16x32_bf16 v[100:103], v[200:203], a[44:47], v[100:103]
v_perm_b32 v236, v235, v234, s86
v_perm_b32 v237, v235, v234, s87
v_mfma_f32_16x16x32_bf16 v[100:103], v[204:207], a[60:63], v[100:103]
buffer_load_dwordx2 v[250:251], v248, s[64:67], s5 offen sc1
v_mfma_f32_16x16x32_bf16 v[68:71], v[204:207], a[56:59], v[68:71]
v_accvgpr_write_b32 a185, v236
v_accvgpr_write_b32 a189, v237
v_mfma_f32_16x16x32_bf16 v[68:71], v[200:203], a[40:43], v[68:71]
ds_read_b64_tr_b16 v[192:193], v240 offset:42368
ds_read_b64_tr_b16 v[194:195], v240 offset:43392
v_mfma_f32_16x16x32_bf16 v[36:39], v[200:203], a[36:39], v[36:39]
s_add_u32 m0, s83, 67584
v_accvgpr_read_b32 v230, a211
v_mfma_f32_16x16x32_bf16 v[36:39], v[204:207], a[52:55], v[36:39]
buffer_load_dwordx4 v224, s[56:59], s80 offen lds
v_mfma_f32_16x16x32_bf16 v[4:7], v[204:207], a[48:51], v[4:7]
s_waitcnt lgkmcnt(4)
v_accvgpr_read_b32 v231, a215
v_mfma_f32_16x16x32_bf16 v[4:7], v[200:203], a[32:35], v[4:7]
ds_read_b64_tr_b16 v[204:205], v241 offset:33920
ds_read_b64_tr_b16 v[206:207], v241 offset:34944
v_mfma_f32_16x16x32_bf16 v[8:11], v[208:211], a[32:35], v[8:11]
s_add_u32 m0, s83, 68608
v_perm_b32 v232, v231, v230, s86
v_mfma_f32_16x16x32_bf16 v[8:11], v[212:215], a[48:51], v[8:11]
buffer_load_dwordx4 v225, s[56:59], s80 offen lds
v_mfma_f32_16x16x32_bf16 v[40:43], v[212:215], a[52:55], v[40:43]
v_perm_b32 v233, v231, v230, s87
v_accvgpr_write_b32 a186, v232
v_mfma_f32_16x16x32_bf16 v[40:43], v[208:211], a[36:39], v[40:43]
ds_read_b64_tr_b16 v[200:201], v241 offset:42368
ds_read_b64_tr_b16 v[202:203], v241 offset:43392
v_mfma_f32_16x16x32_bf16 v[72:75], v[208:211], a[40:43], v[72:75]
s_add_u32 m0, s83, 69760
v_accvgpr_write_b32 a190, v233
v_mfma_f32_16x16x32_bf16 v[72:75], v[212:215], a[56:59], v[72:75]
buffer_load_dwordx4 v226, s[56:59], s80 offen lds
v_mfma_f32_16x16x32_bf16 v[104:107], v[212:215], a[60:63], v[104:107]
v_accvgpr_read_b32 v234, a219
v_accvgpr_read_b32 v235, a223
v_mfma_f32_16x16x32_bf16 v[104:107], v[208:211], a[44:47], v[104:107]
ds_read_b64_tr_b16 v[212:213], v242 offset:33920
ds_read_b64_tr_b16 v[214:215], v242 offset:34944
v_mfma_f32_16x16x32_bf16 v[108:111], v[216:219], a[44:47], v[108:111]
s_add_u32 m0, s83, 70784
v_perm_b32 v236, v235, v234, s86
v_mfma_f32_16x16x32_bf16 v[108:111], v[220:223], a[60:63], v[108:111]
buffer_load_dwordx4 v227, s[56:59], s80 offen lds
v_mfma_f32_16x16x32_bf16 v[76:79], v[220:223], a[56:59], v[76:79]
s_bfe_u32 s80, s4, 262172
s_xor_b32 s80, s80, s72
v_mfma_f32_16x16x32_bf16 v[76:79], v[216:219], a[40:43], v[76:79]
ds_read_b64_tr_b16 v[208:209], v242 offset:42368
ds_read_b64_tr_b16 v[210:211], v242 offset:43392
v_mfma_f32_16x16x32_bf16 v[44:47], v[216:219], a[36:39], v[44:47]
s_add_u32 m0, s83, 84480
s_lshl_b32 s80, s80, 18
v_mfma_f32_16x16x32_bf16 v[44:47], v[220:223], a[52:55], v[44:47]
buffer_load_dwordx4 v224, s[56:59], s80 offen lds
v_mfma_f32_16x16x32_bf16 v[12:15], v[220:223], a[48:51], v[12:15]
s_waitcnt lgkmcnt(4)
v_perm_b32 v237, v235, v234, s87
v_mfma_f32_16x16x32_bf16 v[12:15], v[216:219], a[32:35], v[12:15]
ds_read_b64_tr_b16 v[220:221], v243 offset:33920
ds_read_b64_tr_b16 v[222:223], v243 offset:34944
v_mfma_f32_16x16x32_bf16 v[16:19], v[196:199], a[32:35], v[16:19]
s_add_u32 m0, s83, 85504
v_accvgpr_write_b32 a187, v236
v_mfma_f32_16x16x32_bf16 v[16:19], v[192:195], a[48:51], v[16:19]
buffer_load_dwordx4 v225, s[56:59], s80 offen lds
v_mfma_f32_16x16x32_bf16 v[48:51], v[192:195], a[52:55], v[48:51]
v_accvgpr_write_b32 a191, v237
v_accvgpr_read_b32 v230, a224
v_mfma_f32_16x16x32_bf16 v[48:51], v[196:199], a[36:39], v[48:51]
ds_read_b64_tr_b16 v[216:217], v243 offset:42368
ds_read_b64_tr_b16 v[218:219], v243 offset:43392
v_mfma_f32_16x16x32_bf16 v[80:83], v[196:199], a[40:43], v[80:83]
s_add_u32 m0, s83, 86656
v_accvgpr_read_b32 v231, a228
v_mfma_f32_16x16x32_bf16 v[80:83], v[192:195], a[56:59], v[80:83]
buffer_load_dwordx4 v226, s[56:59], s80 offen lds
v_mfma_f32_16x16x32_bf16 v[112:115], v[192:195], a[60:63], v[112:115]
s_waitcnt vmcnt(28)
s_barrier
v_mfma_f32_16x16x32_bf16 v[112:115], v[196:199], a[44:47], v[112:115]
ds_read_b64_tr_b16 v[192:193], v240 offset:50688
ds_read_b64_tr_b16 v[194:195], v240 offset:51712
v_mfma_f32_16x16x32_bf16 v[116:119], v[204:207], a[44:47], v[116:119]
s_add_u32 m0, s83, 87680
v_perm_b32 v232, v231, v230, s86
v_mfma_f32_16x16x32_bf16 v[116:119], v[200:203], a[60:63], v[116:119]
buffer_load_dwordx4 v227, s[56:59], s80 offen lds
v_mfma_f32_16x16x32_bf16 v[84:87], v[200:203], a[56:59], v[84:87]
s_add_u32 s80, s80, 131072
v_perm_b32 v233, v231, v230, s87
v_mfma_f32_16x16x32_bf16 v[84:87], v[204:207], a[40:43], v[84:87]
ds_read_b64_tr_b16 v[196:197], v240 offset:59136
ds_read_b64_tr_b16 v[198:199], v240 offset:60160
v_mfma_f32_16x16x32_bf16 v[52:55], v[204:207], a[36:39], v[52:55]
s_add_u32 m0, s83, 101376
v_accvgpr_write_b32 a192, v232
v_mfma_f32_16x16x32_bf16 v[52:55], v[200:203], a[52:55], v[52:55]
buffer_load_dwordx4 v224, s[56:59], s80 offen lds
v_mfma_f32_16x16x32_bf16 v[20:23], v[200:203], a[48:51], v[20:23]
s_waitcnt lgkmcnt(4)
v_accvgpr_write_b32 a196, v233
v_mfma_f32_16x16x32_bf16 v[20:23], v[204:207], a[32:35], v[20:23]
ds_read_b64_tr_b16 v[200:201], v241 offset:50688
ds_read_b64_tr_b16 v[202:203], v241 offset:51712
v_mfma_f32_16x16x32_bf16 v[24:27], v[212:215], a[32:35], v[24:27]
s_add_u32 m0, s83, 102400
v_accvgpr_read_b32 v234, a232
v_mfma_f32_16x16x32_bf16 v[24:27], v[208:211], a[48:51], v[24:27]
buffer_load_dwordx4 v225, s[56:59], s80 offen lds
v_mfma_f32_16x16x32_bf16 v[56:59], v[208:211], a[52:55], v[56:59]
v_accvgpr_read_b32 v235, a236
v_perm_b32 v236, v235, v234, s86
v_mfma_f32_16x16x32_bf16 v[56:59], v[212:215], a[36:39], v[56:59]
ds_read_b64_tr_b16 v[204:205], v241 offset:59136
ds_read_b64_tr_b16 v[206:207], v241 offset:60160
v_mfma_f32_16x16x32_bf16 v[88:91], v[212:215], a[40:43], v[88:91]
s_add_u32 m0, s83, 103552
v_perm_b32 v237, v235, v234, s87
v_mfma_f32_16x16x32_bf16 v[88:91], v[208:211], a[56:59], v[88:91]
buffer_load_dwordx4 v226, s[56:59], s80 offen lds
v_mfma_f32_16x16x32_bf16 v[120:123], v[208:211], a[60:63], v[120:123]
v_accvgpr_write_b32 a193, v236
v_accvgpr_write_b32 a197, v237
v_mfma_f32_16x16x32_bf16 v[120:123], v[212:215], a[44:47], v[120:123]
ds_read_b64_tr_b16 v[208:209], v242 offset:50688
ds_read_b64_tr_b16 v[210:211], v242 offset:51712
v_mfma_f32_16x16x32_bf16 v[124:127], v[220:223], a[44:47], v[124:127]
s_add_u32 m0, s83, 104576
v_accvgpr_read_b32 v230, a240
v_mfma_f32_16x16x32_bf16 v[124:127], v[216:219], a[60:63], v[124:127]
buffer_load_dwordx4 v227, s[56:59], s80 offen lds
v_mfma_f32_16x16x32_bf16 v[92:95], v[216:219], a[56:59], v[92:95]
v_bfe_u32 v224, v255, 6, 1
v_lshrrev_b32 v225, 7, v255
v_mfma_f32_16x16x32_bf16 v[92:95], v[220:223], a[40:43], v[92:95]
ds_read_b64_tr_b16 v[212:213], v242 offset:59136
ds_read_b64_tr_b16 v[214:215], v242 offset:60160
v_mfma_f32_16x16x32_bf16 v[60:63], v[220:223], a[36:39], v[60:63]
s_bitcmp1_b32 vcc_lo, 6
s_cbranch_scc0 .La5_LO_N6
.La5_LO_K6:
v_mfma_f32_16x16x32_bf16 v[60:63], v[216:219], a[52:55], v[60:63]
v_and_b32 v226, 48, v255
v_lshl_add_u32 v225, v225, 6, v226
v_mfma_f32_16x16x32_bf16 v[28:31], v[216:219], a[48:51], v[28:31]
s_waitcnt vmcnt(25) lgkmcnt(4)
v_lshl_add_u32 v224, v224, 11, v225
v_mfma_f32_16x16x32_bf16 v[28:31], v[220:223], a[32:35], v[28:31]
ds_read_b64_tr_b16 v[216:217], v243 offset:50688
ds_read_b64_tr_b16 v[218:219], v243 offset:51712
v_mfma_f32_16x16x32_bf16 v[0:3], v[192:195], v[128:131], v[0:3]
v_accvgpr_read_b32 v231, a244
v_perm_b32 v232, v231, v230, s86
v_mfma_f32_16x16x32_bf16 v[0:3], v[196:199], a[0:3], v[0:3]
buffer_load_dwordx4 a[64:67], v244, s[44:47], s54 offen offset:0 sc1
v_mfma_f32_16x16x32_bf16 v[32:35], v[196:199], a[4:7], v[32:35]
v_perm_b32 v233, v231, v230, s87
v_accvgpr_write_b32 a194, v232
v_mfma_f32_16x16x32_bf16 v[32:35], v[192:195], v[132:135], v[32:35]
ds_read_b64_tr_b16 v[220:221], v243 offset:59136
ds_read_b64_tr_b16 v[222:223], v243 offset:60160
v_mfma_f32_16x16x32_bf16 v[64:67], v[192:195], v[144:147], v[64:67]
v_accvgpr_write_b32 a198, v233
v_accvgpr_read_b32 v234, a248
v_mfma_f32_16x16x32_bf16 v[64:67], v[196:199], a[8:11], v[64:67]
buffer_load_dwordx4 a[68:71], v245, s[44:47], s54 offen offset:0 sc1
v_mfma_f32_16x16x32_bf16 v[96:99], v[196:199], a[16:19], v[96:99]
v_accvgpr_read_b32 v235, a252
v_perm_b32 v236, v235, v234, s86
v_mfma_f32_16x16x32_bf16 v[96:99], v[192:195], a[12:15], v[96:99]
ds_read_b64_tr_b16 v[196:197], v240 offset:50816
ds_read_b64_tr_b16 v[198:199], v240 offset:51840
v_mfma_f32_16x16x32_bf16 v[100:103], v[200:203], a[12:15], v[100:103]
v_perm_b32 v237, v235, v234, s87
v_accvgpr_write_b32 a195, v236
v_mfma_f32_16x16x32_bf16 v[100:103], v[204:207], a[16:19], v[100:103]
buffer_load_dwordx4 a[72:75], v244, s[44:47], s54 offen offset:1024 sc1
v_mfma_f32_16x16x32_bf16 v[68:71], v[204:207], a[8:11], v[68:71]
v_accvgpr_write_b32 a199, v237
v_accvgpr_read_b32 v230, a225
v_mfma_f32_16x16x32_bf16 v[68:71], v[200:203], v[144:147], v[68:71]
ds_read_b64_tr_b16 v[192:193], v240 offset:59264
ds_read_b64_tr_b16 v[194:195], v240 offset:60288
v_mfma_f32_16x16x32_bf16 v[36:39], v[200:203], v[132:135], v[36:39]
v_accvgpr_read_b32 v231, a229
v_perm_b32 v232, v231, v230, s86
v_mfma_f32_16x16x32_bf16 v[36:39], v[204:207], a[4:7], v[36:39]
buffer_load_dwordx4 a[76:79], v245, s[44:47], s54 offen offset:1024 sc1
v_mfma_f32_16x16x32_bf16 v[4:7], v[204:207], a[0:3], v[4:7]
s_waitcnt lgkmcnt(4)
v_perm_b32 v233, v231, v230, s87
v_mfma_f32_16x16x32_bf16 v[4:7], v[200:203], v[128:131], v[4:7]
ds_read_b64_tr_b16 v[204:205], v241 offset:50816
ds_read_b64_tr_b16 v[206:207], v241 offset:51840
v_mfma_f32_16x16x32_bf16 v[8:11], v[208:211], v[128:131], v[8:11]
v_accvgpr_write_b32 a200, v232
v_accvgpr_write_b32 a204, v233
v_mfma_f32_16x16x32_bf16 v[8:11], v[212:215], a[0:3], v[8:11]
buffer_load_dwordx4 a[80:83], v244, s[44:47], s54 offen offset:2048 sc1
v_mfma_f32_16x16x32_bf16 v[40:43], v[212:215], a[4:7], v[40:43]
v_accvgpr_read_b32 v234, a233
v_accvgpr_read_b32 v235, a237
v_mfma_f32_16x16x32_bf16 v[40:43], v[208:211], v[132:135], v[40:43]
ds_read_b64_tr_b16 v[200:201], v241 offset:59264
ds_read_b64_tr_b16 v[202:203], v241 offset:60288
v_mfma_f32_16x16x32_bf16 v[72:75], v[208:211], v[144:147], v[72:75]
v_perm_b32 v236, v235, v234, s86
v_perm_b32 v237, v235, v234, s87
v_mfma_f32_16x16x32_bf16 v[72:75], v[212:215], a[8:11], v[72:75]
buffer_load_dwordx4 a[84:87], v245, s[44:47], s54 offen offset:2048 sc1
v_mfma_f32_16x16x32_bf16 v[104:107], v[212:215], a[16:19], v[104:107]
v_accvgpr_write_b32 a201, v236
v_accvgpr_write_b32 a205, v237
v_mfma_f32_16x16x32_bf16 v[104:107], v[208:211], a[12:15], v[104:107]
ds_read_b64_tr_b16 v[212:213], v242 offset:50816
ds_read_b64_tr_b16 v[214:215], v242 offset:51840
v_mfma_f32_16x16x32_bf16 v[108:111], v[216:219], a[12:15], v[108:111]
v_accvgpr_read_b32 v230, a241
v_accvgpr_read_b32 v231, a245
v_mfma_f32_16x16x32_bf16 v[108:111], v[220:223], a[16:19], v[108:111]
buffer_load_dwordx4 a[88:91], v244, s[44:47], s54 offen offset:3072 sc1
v_mfma_f32_16x16x32_bf16 v[76:79], v[220:223], a[8:11], v[76:79]
v_perm_b32 v232, v231, v230, s86
v_perm_b32 v233, v231, v230, s87
v_mfma_f32_16x16x32_bf16 v[76:79], v[216:219], v[144:147], v[76:79]
ds_read_b64_tr_b16 v[208:209], v242 offset:59264
ds_read_b64_tr_b16 v[210:211], v242 offset:60288
v_mfma_f32_16x16x32_bf16 v[44:47], v[216:219], v[132:135], v[44:47]
v_accvgpr_write_b32 a202, v232
v_accvgpr_write_b32 a206, v233
v_mfma_f32_16x16x32_bf16 v[44:47], v[220:223], a[4:7], v[44:47]
v_accvgpr_read_b32 v234, a249
v_accvgpr_read_b32 v235, a253
v_mfma_f32_16x16x32_bf16 v[12:15], v[220:223], a[0:3], v[12:15]
s_waitcnt lgkmcnt(4)
v_perm_b32 v236, v235, v234, s86
v_mfma_f32_16x16x32_bf16 v[12:15], v[216:219], v[128:131], v[12:15]
ds_read_b64_tr_b16 v[220:221], v243 offset:50816
ds_read_b64_tr_b16 v[222:223], v243 offset:51840
v_mfma_f32_16x16x32_bf16 v[16:19], v[196:199], v[128:131], v[16:19]
s_bitcmp1_b32 vcc_lo, 7
s_cbranch_scc0 .La5_LO_N7
.La5_LO_K7:
v_mfma_f32_16x16x32_bf16 v[16:19], v[192:195], a[0:3], v[16:19]
buffer_load_dwordx4 a[92:95], v245, s[44:47], s54 offen offset:3072 sc1
v_mfma_f32_16x16x32_bf16 v[48:51], v[192:195], a[4:7], v[48:51]
v_perm_b32 v237, v235, v234, s87
v_accvgpr_write_b32 a203, v236
v_mfma_f32_16x16x32_bf16 v[48:51], v[196:199], v[132:135], v[48:51]
ds_read_b64_tr_b16 v[216:217], v243 offset:59264
ds_read_b64_tr_b16 v[218:219], v243 offset:60288
v_mfma_f32_16x16x32_bf16 v[80:83], v[196:199], v[144:147], v[80:83]
v_accvgpr_write_b32 a207, v237
v_accvgpr_read_b32 v230, a226
v_mfma_f32_16x16x32_bf16 v[80:83], v[192:195], a[8:11], v[80:83]
buffer_load_dwordx4 a[32:35], v244, s[44:47], s55 offen offset:0 sc1
v_mfma_f32_16x16x32_bf16 v[112:115], v[192:195], a[16:19], v[112:115]
v_accvgpr_read_b32 v231, a230
v_perm_b32 v232, v231, v230, s86
v_mfma_f32_16x16x32_bf16 v[112:115], v[196:199], a[12:15], v[112:115]
ds_read_b64_tr_b16 v[192:193], v240 offset:0
ds_read_b64_tr_b16 v[194:195], v240 offset:1024
v_mfma_f32_16x16x32_bf16 v[116:119], v[204:207], a[12:15], v[116:119]
v_perm_b32 v233, v231, v230, s87
v_accvgpr_write_b32 a208, v232
v_mfma_f32_16x16x32_bf16 v[116:119], v[200:203], a[16:19], v[116:119]
buffer_load_dwordx4 a[36:39], v245, s[44:47], s55 offen offset:0 sc1
v_mfma_f32_16x16x32_bf16 v[84:87], v[200:203], a[8:11], v[84:87]
v_accvgpr_write_b32 a212, v233
v_accvgpr_read_b32 v234, a234
v_mfma_f32_16x16x32_bf16 v[84:87], v[204:207], v[144:147], v[84:87]
ds_read_b64_tr_b16 v[196:197], v240 offset:8448
ds_read_b64_tr_b16 v[198:199], v240 offset:9472
v_mfma_f32_16x16x32_bf16 v[52:55], v[204:207], v[132:135], v[52:55]
v_accvgpr_read_b32 v235, a238
v_perm_b32 v236, v235, v234, s86
v_mfma_f32_16x16x32_bf16 v[52:55], v[200:203], a[4:7], v[52:55]
buffer_load_dwordx4 a[40:43], v244, s[44:47], s55 offen offset:1024 sc1
v_mfma_f32_16x16x32_bf16 v[20:23], v[200:203], a[0:3], v[20:23]
s_waitcnt lgkmcnt(4)
s_waitcnt vmcnt(23)
v_mfma_f32_16x16x32_bf16 v[20:23], v[204:207], v[128:131], v[20:23]
ds_read_b64_tr_b16 v[200:201], v241 offset:0
ds_read_b64_tr_b16 v[202:203], v241 offset:1024
v_mfma_f32_16x16x32_bf16 v[24:27], v[212:215], v[128:131], v[24:27]
v_cmp_eq_u64 vcc, v[250:251], v[252:253]
v_perm_b32 v237, v235, v234, s87
v_mfma_f32_16x16x32_bf16 v[24:27], v[208:211], a[0:3], v[24:27]
buffer_load_dwordx4 a[44:47], v245, s[44:47], s55 offen offset:1024 sc1
v_mfma_f32_16x16x32_bf16 v[56:59], v[208:211], a[4:7], v[56:59]
v_accvgpr_write_b32 a209, v236
v_accvgpr_write_b32 a213, v237
v_mfma_f32_16x16x32_bf16 v[56:59], v[212:215], v[132:135], v[56:59]
ds_read_b64_tr_b16 v[204:205], v241 offset:8448
ds_read_b64_tr_b16 v[206:207], v241 offset:9472
v_mfma_f32_16x16x32_bf16 v[88:91], v[212:215], v[144:147], v[88:91]
v_accvgpr_read_b32 v230, a242
v_accvgpr_read_b32 v231, a246
v_mfma_f32_16x16x32_bf16 v[88:91], v[208:211], a[8:11], v[88:91]
buffer_load_dwordx4 a[48:51], v244, s[44:47], s55 offen offset:2048 sc1
v_mfma_f32_16x16x32_bf16 v[120:123], v[208:211], a[16:19], v[120:123]
v_perm_b32 v232, v231, v230, s86
v_perm_b32 v233, v231, v230, s87
v_mfma_f32_16x16x32_bf16 v[120:123], v[212:215], a[12:15], v[120:123]
ds_read_b64_tr_b16 v[208:209], v242 offset:0
ds_read_b64_tr_b16 v[210:211], v242 offset:1024
v_mfma_f32_16x16x32_bf16 v[124:127], v[220:223], a[12:15], v[124:127]
v_accvgpr_write_b32 a210, v232
v_accvgpr_write_b32 a214, v233
v_mfma_f32_16x16x32_bf16 v[124:127], v[216:219], a[16:19], v[124:127]
buffer_load_dwordx4 a[12:15], v245, s[44:47], s55 offen offset:2048 sc1
v_mfma_f32_16x16x32_bf16 v[92:95], v[216:219], a[8:11], v[92:95]
v_accvgpr_read_b32 v234, a250
v_accvgpr_read_b32 v235, a254
v_mfma_f32_16x16x32_bf16 v[92:95], v[220:223], v[144:147], v[92:95]
ds_read_b64_tr_b16 v[212:213], v242 offset:8448
ds_read_b64_tr_b16 v[214:215], v242 offset:9472
v_mfma_f32_16x16x32_bf16 v[60:63], v[220:223], v[132:135], v[60:63]
v_perm_b32 v236, v235, v234, s86
v_perm_b32 v237, v235, v234, s87
v_mfma_f32_16x16x32_bf16 v[60:63], v[216:219], a[4:7], v[60:63]
v_accvgpr_write_b32 a211, v236
v_accvgpr_write_b32 a215, v237
v_mfma_f32_16x16x32_bf16 v[28:31], v[216:219], a[0:3], v[28:31]
s_waitcnt lgkmcnt(4)
v_accvgpr_read_b32 v230, a227
v_mfma_f32_16x16x32_bf16 v[28:31], v[220:223], v[128:131], v[28:31]
ds_read_b64_tr_b16 v[216:217], v243 offset:0
ds_read_b64_tr_b16 v[218:219], v243 offset:1024
v_mfma_f32_16x16x32_bf16 v[0:3], v[192:195], a[20:23], v[0:3]
v_accvgpr_read_b32 v231, a231
v_perm_b32 v232, v231, v230, s86
v_mfma_f32_16x16x32_bf16 v[0:3], v[196:199], v[160:163], v[0:3]
buffer_load_dwordx4 a[0:3], v244, s[44:47], s55 offen offset:3072 sc1
v_mfma_f32_16x16x32_bf16 v[32:35], v[196:199], v[164:167], v[32:35]
v_perm_b32 v233, v231, v230, s87
v_accvgpr_write_b32 a216, v232
v_mfma_f32_16x16x32_bf16 v[32:35], v[192:195], a[24:27], v[32:35]
ds_read_b64_tr_b16 v[220:221], v243 offset:8448
ds_read_b64_tr_b16 v[222:223], v243 offset:9472
v_mfma_f32_16x16x32_bf16 v[64:67], v[192:195], a[28:31], v[64:67]
v_accvgpr_write_b32 a220, v233
v_accvgpr_read_b32 v234, a235
v_mfma_f32_16x16x32_bf16 v[64:67], v[196:199], v[156:159], v[64:67]
buffer_load_dwordx4 a[4:7], v245, s[44:47], s55 offen offset:3072 sc1
v_mfma_f32_16x16x32_bf16 v[96:99], v[196:199], v[136:139], v[96:99]
v_accvgpr_read_b32 v235, a239
v_perm_b32 v236, v235, v234, s86
v_mfma_f32_16x16x32_bf16 v[96:99], v[192:195], v[148:151], v[96:99]
ds_read_b64_tr_b16 v[196:197], v240 offset:128
ds_read_b64_tr_b16 v[198:199], v240 offset:1152
v_mfma_f32_16x16x32_bf16 v[100:103], v[200:203], v[148:151], v[100:103]
v_perm_b32 v237, v235, v234, s87
v_accvgpr_write_b32 a217, v236
v_mfma_f32_16x16x32_bf16 v[100:103], v[204:207], v[136:139], v[100:103]
v_accvgpr_write_b32 a221, v237
v_accvgpr_read_b32 v230, a243
v_mfma_f32_16x16x32_bf16 v[68:71], v[204:207], v[156:159], v[68:71]
v_accvgpr_read_b32 v231, a247
v_perm_b32 v232, v231, v230, s86
v_mfma_f32_16x16x32_bf16 v[68:71], v[200:203], a[28:31], v[68:71]
ds_read_b64_tr_b16 v[192:193], v240 offset:8576
ds_read_b64_tr_b16 v[194:195], v240 offset:9600
v_mfma_f32_16x16x32_bf16 v[36:39], v[200:203], a[24:27], v[36:39]
v_add_u32 v240, 67584, v240
v_perm_b32 v233, v231, v230, s87
v_mfma_f32_16x16x32_bf16 v[36:39], v[204:207], v[164:167], v[36:39]
v_accvgpr_write_b32 a218, v232
v_accvgpr_write_b32 a222, v233
v_mfma_f32_16x16x32_bf16 v[4:7], v[204:207], v[160:163], v[4:7]
s_waitcnt lgkmcnt(4)
v_accvgpr_read_b32 v234, a251
v_mfma_f32_16x16x32_bf16 v[4:7], v[200:203], a[20:23], v[4:7]
ds_read_b64_tr_b16 v[204:205], v241 offset:128
ds_read_b64_tr_b16 v[206:207], v241 offset:1152
v_mfma_f32_16x16x32_bf16 v[8:11], v[208:211], a[20:23], v[8:11]
v_accvgpr_read_b32 v235, a255
v_perm_b32 v236, v235, v234, s86
v_mfma_f32_16x16x32_bf16 v[8:11], v[212:215], v[160:163], v[8:11]
v_perm_b32 v237, v235, v234, s87
v_accvgpr_write_b32 a219, v236
v_mfma_f32_16x16x32_bf16 v[40:43], v[212:215], v[164:167], v[40:43]
v_accvgpr_write_b32 a223, v237
v_mfma_f32_16x16x32_bf16 v[40:43], v[208:211], a[24:27], v[40:43]
ds_read_b64_tr_b16 v[200:201], v241 offset:8576
ds_read_b64_tr_b16 v[202:203], v241 offset:9600
v_mfma_f32_16x16x32_bf16 v[72:75], v[208:211], a[28:31], v[72:75]
v_add_u32 v241, 67584, v241
v_mfma_f32_16x16x32_bf16 v[72:75], v[212:215], v[156:159], v[72:75]
v_mfma_f32_16x16x32_bf16 v[104:107], v[212:215], v[136:139], v[104:107]
v_mfma_f32_16x16x32_bf16 v[104:107], v[208:211], v[148:151], v[104:107]
ds_read_b64_tr_b16 v[212:213], v242 offset:128
ds_read_b64_tr_b16 v[214:215], v242 offset:1152
v_mfma_f32_16x16x32_bf16 v[108:111], v[216:219], v[148:151], v[108:111]
v_mfma_f32_16x16x32_bf16 v[108:111], v[220:223], v[136:139], v[108:111]
v_mfma_f32_16x16x32_bf16 v[76:79], v[220:223], v[156:159], v[76:79]
v_mfma_f32_16x16x32_bf16 v[76:79], v[216:219], a[28:31], v[76:79]
ds_read_b64_tr_b16 v[208:209], v242 offset:8576
ds_read_b64_tr_b16 v[210:211], v242 offset:9600
v_mfma_f32_16x16x32_bf16 v[44:47], v[216:219], a[24:27], v[44:47]
v_add_u32 v242, 67584, v242
v_mfma_f32_16x16x32_bf16 v[44:47], v[220:223], v[164:167], v[44:47]
v_mfma_f32_16x16x32_bf16 v[12:15], v[220:223], v[160:163], v[12:15]
.La5_LW3_3:
s_cbranch_execz .La5_LW3_R0
s_waitcnt lgkmcnt(4)
v_mfma_f32_16x16x32_bf16 v[12:15], v[216:219], a[20:23], v[12:15]
ds_read_b64_tr_b16 v[220:221], v243 offset:128
ds_read_b64_tr_b16 v[222:223], v243 offset:1152
v_mfma_f32_16x16x32_bf16 v[16:19], v[196:199], a[20:23], v[16:19]
v_mfma_f32_16x16x32_bf16 v[16:19], v[192:195], v[160:163], v[16:19]
buffer_load_dwordx4 a[8:11], v246, s[44:47], s54 offen offset:0 sc1
v_mfma_f32_16x16x32_bf16 v[48:51], v[192:195], v[164:167], v[48:51]
v_mfma_f32_16x16x32_bf16 v[48:51], v[196:199], a[24:27], v[48:51]
ds_read_b64_tr_b16 v[216:217], v243 offset:8576
ds_read_b64_tr_b16 v[218:219], v243 offset:9600
v_mfma_f32_16x16x32_bf16 v[80:83], v[196:199], a[28:31], v[80:83]
v_add_u32 v243, 67584, v243
v_mfma_f32_16x16x32_bf16 v[80:83], v[192:195], v[156:159], v[80:83]
buffer_load_dwordx4 a[16:19], v247, s[44:47], s54 offen offset:0 sc1
v_mfma_f32_16x16x32_bf16 v[112:115], v[192:195], v[136:139], v[112:115]
s_waitcnt vmcnt(18)
s_barrier
v_mfma_f32_16x16x32_bf16 v[112:115], v[196:199], v[148:151], v[112:115]
ds_read_b64_tr_b16 v[192:193], v240 offset:50688
ds_read_b64_tr_b16 v[194:195], v240 offset:51712
v_mfma_f32_16x16x32_bf16 v[116:119], v[204:207], v[148:151], v[116:119]
v_mfma_f32_16x16x32_bf16 v[116:119], v[200:203], v[136:139], v[116:119]
buffer_load_dwordx4 a[52:55], v246, s[44:47], s54 offen offset:1024 sc1
v_mfma_f32_16x16x32_bf16 v[84:87], v[200:203], v[156:159], v[84:87]
v_mfma_f32_16x16x32_bf16 v[84:87], v[204:207], a[28:31], v[84:87]
ds_read_b64_tr_b16 v[196:197], v240 offset:59136
ds_read_b64_tr_b16 v[198:199], v240 offset:60160
v_mfma_f32_16x16x32_bf16 v[52:55], v[204:207], a[24:27], v[52:55]
v_mfma_f32_16x16x32_bf16 v[52:55], v[200:203], v[164:167], v[52:55]
buffer_load_dwordx4 a[56:59], v247, s[44:47], s54 offen offset:1024 sc1
v_mfma_f32_16x16x32_bf16 v[20:23], v[200:203], v[160:163], v[20:23]
s_waitcnt lgkmcnt(4)
v_mfma_f32_16x16x32_bf16 v[20:23], v[204:207], a[20:23], v[20:23]
ds_read_b64_tr_b16 v[200:201], v240 offset:0
ds_read_b64_tr_b16 v[202:203], v240 offset:1024
v_mfma_f32_16x16x32_bf16 v[24:27], v[212:215], a[20:23], v[24:27]
v_mfma_f32_16x16x32_bf16 v[24:27], v[208:211], v[160:163], v[24:27]
buffer_load_dwordx4 a[60:63], v246, s[44:47], s54 offen offset:2048 sc1
v_mfma_f32_16x16x32_bf16 v[56:59], v[208:211], v[164:167], v[56:59]
v_mfma_f32_16x16x32_bf16 v[56:59], v[212:215], a[24:27], v[56:59]
ds_read_b64_tr_b16 v[204:205], v240 offset:8448
ds_read_b64_tr_b16 v[206:207], v240 offset:9472
v_mfma_f32_16x16x32_bf16 v[88:91], v[212:215], a[28:31], v[88:91]
v_mfma_f32_16x16x32_bf16 v[88:91], v[208:211], v[156:159], v[88:91]
buffer_load_dwordx4 v[128:131], v247, s[44:47], s54 offen offset:2048 sc1
v_mfma_f32_16x16x32_bf16 v[120:123], v[208:211], v[136:139], v[120:123]
v_mfma_f32_16x16x32_bf16 v[120:123], v[212:215], v[148:151], v[120:123]
ds_read_b64_tr_b16 v[208:209], v240 offset:16896
ds_read_b64_tr_b16 v[210:211], v240 offset:17920
v_mfma_f32_16x16x32_bf16 v[124:127], v[220:223], v[148:151], v[124:127]
v_mfma_f32_16x16x32_bf16 v[124:127], v[216:219], v[136:139], v[124:127]
buffer_load_dwordx4 v[132:135], v246, s[44:47], s54 offen offset:3072 sc1
v_mfma_f32_16x16x32_bf16 v[92:95], v[216:219], v[156:159], v[92:95]
v_mfma_f32_16x16x32_bf16 v[92:95], v[220:223], a[28:31], v[92:95]
ds_read_b64_tr_b16 v[212:213], v240 offset:25344
ds_read_b64_tr_b16 v[214:215], v240 offset:26368
v_mfma_f32_16x16x32_bf16 v[60:63], v[220:223], a[24:27], v[60:63]
v_mfma_f32_16x16x32_bf16 v[60:63], v[216:219], v[164:167], v[60:63]
v_mfma_f32_16x16x32_bf16 v[28:31], v[216:219], v[160:163], v[28:31]
s_waitcnt vmcnt(15) lgkmcnt(4)
v_mfma_f32_16x16x32_bf16 v[28:31], v[220:223], a[20:23], v[28:31]
ds_read_b64_tr_b16 v[216:217], v240 offset:33792
ds_read_b64_tr_b16 v[218:219], v240 offset:34816
v_mfma_f32_16x16x32_bf16 v[0:3], v[192:195], a[64:67], v[0:3]
buffer_load_dwordx4 a[20:23], v247, s[44:47], s54 offen offset:3072 sc1
v_mfma_f32_16x16x32_bf16 v[0:3], v[196:199], a[72:75], v[0:3]
ds_read_b64_tr_b16 v[220:221], v240 offset:42240
ds_read_b64_tr_b16 v[222:223], v240 offset:43264
v_mfma_f32_16x16x32_bf16 v[32:35], v[196:199], a[76:79], v[32:35]
v_mfma_f32_16x16x32_bf16 v[32:35], v[192:195], a[68:71], v[32:35]
ds_read_b64_tr_b16 v[196:197], v241 offset:50688
ds_read_b64_tr_b16 v[198:199], v241 offset:51712
v_mfma_f32_16x16x32_bf16 v[32:35], v[200:203], a[84:87], v[32:35]
buffer_load_dwordx4 a[24:27], v246, s[44:47], s55 offen offset:0 sc1
v_mfma_f32_16x16x32_bf16 v[32:35], v[204:207], a[92:95], v[32:35]
ds_read_b64_tr_b16 v[192:193], v241 offset:59136
ds_read_b64_tr_b16 v[194:195], v241 offset:60160
v_mfma_f32_16x16x32_bf16 v[0:3], v[204:207], a[88:91], v[0:3]
s_waitcnt vmcnt(9) lgkmcnt(4)
v_mfma_f32_16x16x32_bf16 v[0:3], v[200:203], a[80:83], v[0:3]
ds_read_b64_tr_b16 v[204:205], v241 offset:0
ds_read_b64_tr_b16 v[206:207], v241 offset:1024
v_mfma_f32_16x16x32_bf16 v[0:3], v[208:211], a[32:35], v[0:3]
buffer_load_dwordx4 a[28:31], v247, s[44:47], s55 offen offset:0 sc1
v_mfma_f32_16x16x32_bf16 v[0:3], v[212:215], a[40:43], v[0:3]
ds_read_b64_tr_b16 v[200:201], v241 offset:8448
ds_read_b64_tr_b16 v[202:203], v241 offset:9472
v_mfma_f32_16x16x32_bf16 v[32:35], v[212:215], a[44:47], v[32:35]
v_mfma_f32_16x16x32_bf16 v[32:35], v[208:211], a[36:39], v[32:35]
ds_read_b64_tr_b16 v[212:213], v241 offset:16896
ds_read_b64_tr_b16 v[214:215], v241 offset:17920
v_mfma_f32_16x16x32_bf16 v[32:35], v[216:219], a[12:15], v[32:35]
buffer_load_dwordx4 v[144:147], v246, s[44:47], s55 offen offset:1024 sc1
v_mfma_f32_16x16x32_bf16 v[32:35], v[220:223], a[4:7], v[32:35]
ds_read_b64_tr_b16 v[208:209], v241 offset:25344
ds_read_b64_tr_b16 v[210:211], v241 offset:26368
v_mfma_f32_16x16x32_bf16 v[0:3], v[220:223], a[0:3], v[0:3]
s_waitcnt lgkmcnt(4)
v_mfma_f32_16x16x32_bf16 v[0:3], v[216:219], a[48:51], v[0:3]
ds_read_b64_tr_b16 v[220:221], v241 offset:33792
ds_read_b64_tr_b16 v[222:223], v241 offset:34816
v_mfma_f32_16x16x32_bf16 v[4:7], v[196:199], a[64:67], v[4:7]
buffer_load_dwordx4 v[148:151], v247, s[44:47], s55 offen offset:1024 sc1
v_mfma_f32_16x16x32_bf16 v[4:7], v[192:195], a[72:75], v[4:7]
ds_read_b64_tr_b16 v[216:217], v241 offset:42240
ds_read_b64_tr_b16 v[218:219], v241 offset:43264
v_mfma_f32_16x16x32_bf16 v[36:39], v[192:195], a[76:79], v[36:39]
v_mfma_f32_16x16x32_bf16 v[36:39], v[196:199], a[68:71], v[36:39]
ds_read_b64_tr_b16 v[192:193], v242 offset:50688
ds_read_b64_tr_b16 v[194:195], v242 offset:51712
v_mfma_f32_16x16x32_bf16 v[36:39], v[204:207], a[84:87], v[36:39]
buffer_load_dwordx4 v[160:163], v246, s[44:47], s55 offen offset:2048 sc1
v_mfma_f32_16x16x32_bf16 v[36:39], v[200:203], a[92:95], v[36:39]
ds_read_b64_tr_b16 v[196:197], v242 offset:59136
ds_read_b64_tr_b16 v[198:199], v242 offset:60160
v_mfma_f32_16x16x32_bf16 v[4:7], v[200:203], a[88:91], v[4:7]
s_waitcnt lgkmcnt(4)
v_mfma_f32_16x16x32_bf16 v[4:7], v[204:207], a[80:83], v[4:7]
ds_read_b64_tr_b16 v[200:201], v242 offset:0
ds_read_b64_tr_b16 v[202:203], v242 offset:1024
v_mfma_f32_16x16x32_bf16 v[4:7], v[212:215], a[32:35], v[4:7]
buffer_load_dwordx4 v[164:167], v247, s[44:47], s55 offen offset:2048 sc1
v_mfma_f32_16x16x32_bf16 v[4:7], v[208:211], a[40:43], v[4:7]
ds_read_b64_tr_b16 v[204:205], v242 offset:8448
ds_read_b64_tr_b16 v[206:207], v242 offset:9472
v_mfma_f32_16x16x32_bf16 v[36:39], v[208:211], a[44:47], v[36:39]
v_mfma_f32_16x16x32_bf16 v[36:39], v[212:215], a[36:39], v[36:39]
ds_read_b64_tr_b16 v[208:209], v242 offset:16896
ds_read_b64_tr_b16 v[210:211], v242 offset:17920
v_mfma_f32_16x16x32_bf16 v[36:39], v[220:223], a[12:15], v[36:39]
v_mfma_f32_16x16x32_bf16 v[36:39], v[216:219], a[4:7], v[36:39]
ds_read_b64_tr_b16 v[212:213], v242 offset:25344
ds_read_b64_tr_b16 v[214:215], v242 offset:26368
v_mfma_f32_16x16x32_bf16 v[4:7], v[216:219], a[0:3], v[4:7]
s_waitcnt lgkmcnt(4)
v_mfma_f32_16x16x32_bf16 v[4:7], v[220:223], a[48:51], v[4:7]
ds_read_b64_tr_b16 v[216:217], v242 offset:33792
ds_read_b64_tr_b16 v[218:219], v242 offset:34816
v_mfma_f32_16x16x32_bf16 v[8:11], v[192:195], a[64:67], v[8:11]
buffer_load_dwordx4 v[176:179], v246, s[44:47], s55 offen offset:3072 sc1
v_mfma_f32_16x16x32_bf16 v[8:11], v[196:199], a[72:75], v[8:11]
ds_read_b64_tr_b16 v[220:221], v242 offset:42240
ds_read_b64_tr_b16 v[222:223], v242 offset:43264
v_mfma_f32_16x16x32_bf16 v[40:43], v[196:199], a[76:79], v[40:43]
v_cvt_pk_bf16_f32 v234, v32, v33
v_cvt_pk_bf16_f32 v236, v36, v37
v_mfma_f32_16x16x32_bf16 v[40:43], v[192:195], a[68:71], v[40:43]
ds_read_b64_tr_b16 v[196:197], v243 offset:50688
ds_read_b64_tr_b16 v[198:199], v243 offset:51712
v_mfma_f32_16x16x32_bf16 v[40:43], v[200:203], a[84:87], v[40:43]
buffer_load_dwordx4 v[180:183], v247, s[44:47], s55 offen offset:3072 sc1
v_mfma_f32_16x16x32_bf16 v[40:43], v[204:207], a[92:95], v[40:43]
ds_read_b64_tr_b16 v[192:193], v243 offset:59136
ds_read_b64_tr_b16 v[194:195], v243 offset:60160
v_mfma_f32_16x16x32_bf16 v[8:11], v[204:207], a[88:91], v[8:11]
s_waitcnt lgkmcnt(4)
v_bfe_u32 v250, v255, 6, 1
v_mfma_f32_16x16x32_bf16 v[8:11], v[200:203], a[80:83], v[8:11]
ds_read_b64_tr_b16 v[204:205], v243 offset:0
ds_read_b64_tr_b16 v[206:207], v243 offset:1024
v_mfma_f32_16x16x32_bf16 v[8:11], v[208:211], a[32:35], v[8:11]
v_cvt_pk_bf16_f32 v235, v34, v35
v_cvt_pk_bf16_f32 v237, v38, v39
v_mfma_f32_16x16x32_bf16 v[8:11], v[212:215], a[40:43], v[8:11]
ds_read_b64_tr_b16 v[200:201], v243 offset:8448
ds_read_b64_tr_b16 v[202:203], v243 offset:9472
v_mfma_f32_16x16x32_bf16 v[40:43], v[212:215], a[44:47], v[40:43]
v_permlane16_swap_b32 v234, v236
v_permlane16_swap_b32 v235, v237
v_mfma_f32_16x16x32_bf16 v[40:43], v[208:211], a[36:39], v[40:43]
ds_read_b64_tr_b16 v[212:213], v243 offset:16896
ds_read_b64_tr_b16 v[214:215], v243 offset:17920
v_mfma_f32_16x16x32_bf16 v[40:43], v[216:219], a[12:15], v[40:43]
buffer_store_dwordx4 v[234:237], v238, s[32:35], s95 offen offset:0 sc1
v_mfma_f32_16x16x32_bf16 v[40:43], v[220:223], a[4:7], v[40:43]
ds_read_b64_tr_b16 v[208:209], v243 offset:25344
ds_read_b64_tr_b16 v[210:211], v243 offset:26368
v_mfma_f32_16x16x32_bf16 v[8:11], v[220:223], a[0:3], v[8:11]
s_waitcnt lgkmcnt(4)
v_mul_u32_u24 v250, 7, v250
v_mfma_f32_16x16x32_bf16 v[8:11], v[216:219], a[48:51], v[8:11]
ds_read_b64_tr_b16 v[220:221], v243 offset:33792
ds_read_b64_tr_b16 v[222:223], v243 offset:34816
v_mfma_f32_16x16x32_bf16 v[12:15], v[196:199], a[64:67], v[12:15]
v_cvt_pk_bf16_f32 v230, v0, v1
v_cvt_pk_bf16_f32 v232, v4, v5
v_mfma_f32_16x16x32_bf16 v[12:15], v[192:195], a[72:75], v[12:15]
ds_read_b64_tr_b16 v[216:217], v243 offset:42240
ds_read_b64_tr_b16 v[218:219], v243 offset:43264
v_mfma_f32_16x16x32_bf16 v[44:47], v[192:195], a[76:79], v[44:47]
v_cvt_pk_bf16_f32 v231, v2, v3
v_cvt_pk_bf16_f32 v233, v6, v7
v_mfma_f32_16x16x32_bf16 v[44:47], v[196:199], a[68:71], v[44:47]
ds_read_b64_tr_b16 v[192:193], v240 offset:50816
ds_read_b64_tr_b16 v[194:195], v240 offset:51840
v_mfma_f32_16x16x32_bf16 v[44:47], v[204:207], a[84:87], v[44:47]
v_permlane16_swap_b32 v230, v232
v_permlane16_swap_b32 v231, v233
v_mfma_f32_16x16x32_bf16 v[44:47], v[200:203], a[92:95], v[44:47]
ds_read_b64_tr_b16 v[196:197], v240 offset:59264
ds_read_b64_tr_b16 v[198:199], v240 offset:60288
v_mfma_f32_16x16x32_bf16 v[12:15], v[200:203], a[88:91], v[12:15]
s_waitcnt lgkmcnt(4)
v_xor_b32 v250, s3, v250
v_mfma_f32_16x16x32_bf16 v[12:15], v[204:207], a[80:83], v[12:15]
ds_read_b64_tr_b16 v[200:201], v240 offset:128
ds_read_b64_tr_b16 v[202:203], v240 offset:1152
v_mfma_f32_16x16x32_bf16 v[12:15], v[212:215], a[32:35], v[12:15]
buffer_store_dwordx4 v[230:233], v238, s[32:35], s94 offen offset:0 sc1
v_mfma_f32_16x16x32_bf16 v[12:15], v[208:211], a[40:43], v[12:15]
ds_read_b64_tr_b16 v[204:205], v240 offset:8576
ds_read_b64_tr_b16 v[206:207], v240 offset:9600
v_mfma_f32_16x16x32_bf16 v[44:47], v[208:211], a[44:47], v[44:47]
v_bfe_u32 v251, v255, 4, 2
v_lshl_add_u32 v250, v250, 2, v251
v_mfma_f32_16x16x32_bf16 v[44:47], v[212:215], a[36:39], v[44:47]
ds_read_b64_tr_b16 v[208:209], v240 offset:17024
ds_read_b64_tr_b16 v[210:211], v240 offset:18048
v_mfma_f32_16x16x32_bf16 v[44:47], v[220:223], a[12:15], v[44:47]
buffer_load_dwordx4 v[136:139], v224, s[68:71], 0 offen offset:0
v_mfma_f32_16x16x32_bf16 v[44:47], v[216:219], a[4:7], v[44:47]
ds_read_b64_tr_b16 v[212:213], v240 offset:25472
ds_read_b64_tr_b16 v[214:215], v240 offset:26496
v_mfma_f32_16x16x32_bf16 v[12:15], v[216:219], a[0:3], v[12:15]
s_waitcnt lgkmcnt(4)
v_lshlrev_b32 v250, 9, v250
v_mfma_f32_16x16x32_bf16 v[12:15], v[220:223], a[48:51], v[12:15]
ds_read_b64_tr_b16 v[216:217], v240 offset:33920
ds_read_b64_tr_b16 v[218:219], v240 offset:34944
v_mfma_f32_16x16x32_bf16 v[16:19], v[192:195], a[64:67], v[16:19]
v_cvt_pk_bf16_f32 v234, v40, v41
v_cvt_pk_bf16_f32 v236, v44, v45
v_mfma_f32_16x16x32_bf16 v[16:19], v[196:199], a[72:75], v[16:19]
ds_read_b64_tr_b16 v[220:221], v240 offset:42368
ds_read_b64_tr_b16 v[222:223], v240 offset:43392
v_mfma_f32_16x16x32_bf16 v[48:51], v[196:199], a[76:79], v[48:51]
v_cvt_pk_bf16_f32 v235, v42, v43
v_cvt_pk_bf16_f32 v237, v46, v47
v_mfma_f32_16x16x32_bf16 v[48:51], v[192:195], a[68:71], v[48:51]
ds_read_b64_tr_b16 v[196:197], v241 offset:50816
ds_read_b64_tr_b16 v[198:199], v241 offset:51840
v_mfma_f32_16x16x32_bf16 v[48:51], v[200:203], a[84:87], v[48:51]
v_permlane16_swap_b32 v234, v236
v_permlane16_swap_b32 v235, v237
v_mfma_f32_16x16x32_bf16 v[48:51], v[204:207], a[92:95], v[48:51]
ds_read_b64_tr_b16 v[192:193], v241 offset:59264
ds_read_b64_tr_b16 v[194:195], v241 offset:60288
v_mfma_f32_16x16x32_bf16 v[16:19], v[204:207], a[88:91], v[16:19]
s_waitcnt lgkmcnt(4)
v_and_b32 v248, 0x1f0, v255
v_mfma_f32_16x16x32_bf16 v[16:19], v[200:203], a[80:83], v[16:19]
ds_read_b64_tr_b16 v[204:205], v241 offset:128
ds_read_b64_tr_b16 v[206:207], v241 offset:1152
v_mfma_f32_16x16x32_bf16 v[16:19], v[208:211], a[32:35], v[16:19]
buffer_store_dwordx4 v[234:237], v238, s[32:35], s95 offen offset:1024 sc1
v_mfma_f32_16x16x32_bf16 v[16:19], v[212:215], a[40:43], v[16:19]
ds_read_b64_tr_b16 v[200:201], v241 offset:8576
ds_read_b64_tr_b16 v[202:203], v241 offset:9600
v_mfma_f32_16x16x32_bf16 v[48:51], v[212:215], a[44:47], v[48:51]
v_cvt_pk_bf16_f32 v230, v8, v9
v_cvt_pk_bf16_f32 v232, v12, v13
v_mfma_f32_16x16x32_bf16 v[48:51], v[208:211], a[36:39], v[48:51]
ds_read_b64_tr_b16 v[212:213], v241 offset:17024
ds_read_b64_tr_b16 v[214:215], v241 offset:18048
v_mfma_f32_16x16x32_bf16 v[48:51], v[216:219], a[12:15], v[48:51]
v_cvt_pk_bf16_f32 v231, v10, v11
v_cvt_pk_bf16_f32 v233, v14, v15
v_mfma_f32_16x16x32_bf16 v[48:51], v[220:223], a[4:7], v[48:51]
ds_read_b64_tr_b16 v[208:209], v241 offset:25472
ds_read_b64_tr_b16 v[210:211], v241 offset:26496
v_mfma_f32_16x16x32_bf16 v[16:19], v[220:223], a[0:3], v[16:19]
s_waitcnt lgkmcnt(4)
v_lshrrev_b32 v248, 1, v248
v_mfma_f32_16x16x32_bf16 v[16:19], v[216:219], a[48:51], v[16:19]
ds_read_b64_tr_b16 v[220:221], v241 offset:33920
ds_read_b64_tr_b16 v[222:223], v241 offset:34944
v_mfma_f32_16x16x32_bf16 v[20:23], v[196:199], a[64:67], v[20:23]
v_permlane16_swap_b32 v230, v232
v_permlane16_swap_b32 v231, v233
v_mfma_f32_16x16x32_bf16 v[20:23], v[192:195], a[72:75], v[20:23]
ds_read_b64_tr_b16 v[216:217], v241 offset:42368
ds_read_b64_tr_b16 v[218:219], v241 offset:43392
v_mfma_f32_16x16x32_bf16 v[52:55], v[192:195], a[76:79], v[52:55]
v_mfma_f32_16x16x32_bf16 v[52:55], v[196:199], a[68:71], v[52:55]
ds_read_b64_tr_b16 v[192:193], v242 offset:50816
ds_read_b64_tr_b16 v[194:195], v242 offset:51840
v_mfma_f32_16x16x32_bf16 v[52:55], v[204:207], a[84:87], v[52:55]
buffer_store_dwordx4 v[230:233], v238, s[32:35], s94 offen offset:1024 sc1
v_mfma_f32_16x16x32_bf16 v[52:55], v[200:203], a[92:95], v[52:55]
ds_read_b64_tr_b16 v[196:197], v242 offset:59264
ds_read_b64_tr_b16 v[198:199], v242 offset:60288
v_mfma_f32_16x16x32_bf16 v[20:23], v[200:203], a[88:91], v[20:23]
s_waitcnt lgkmcnt(4)
v_mfma_f32_16x16x32_bf16 v[20:23], v[204:207], a[80:83], v[20:23]
ds_read_b64_tr_b16 v[200:201], v242 offset:128
ds_read_b64_tr_b16 v[202:203], v242 offset:1152
v_mfma_f32_16x16x32_bf16 v[20:23], v[212:215], a[32:35], v[20:23]
buffer_load_dwordx4 v[140:143], v225, s[48:51], 0 offen offset:0
v_mfma_f32_16x16x32_bf16 v[20:23], v[208:211], a[40:43], v[20:23]
ds_read_b64_tr_b16 v[204:205], v242 offset:8576
ds_read_b64_tr_b16 v[206:207], v242 offset:9600
v_mfma_f32_16x16x32_bf16 v[52:55], v[208:211], a[44:47], v[52:55]
v_mfma_f32_16x16x32_bf16 v[52:55], v[212:215], a[36:39], v[52:55]
ds_read_b64_tr_b16 v[208:209], v242 offset:17024
ds_read_b64_tr_b16 v[210:211], v242 offset:18048
v_mfma_f32_16x16x32_bf16 v[52:55], v[220:223], a[12:15], v[52:55]
buffer_load_dwordx4 v[152:155], v224, s[68:71], 0 offen offset:512
v_mfma_f32_16x16x32_bf16 v[52:55], v[216:219], a[4:7], v[52:55]
ds_read_b64_tr_b16 v[212:213], v242 offset:25472
ds_read_b64_tr_b16 v[214:215], v242 offset:26496
v_mfma_f32_16x16x32_bf16 v[20:23], v[216:219], a[0:3], v[20:23]
s_waitcnt lgkmcnt(4)
s_mov_b32 s93, 0
buffer_load_dwordx4 a[224:227], v229, s[28:31], s93 offen offset:0
v_mfma_f32_16x16x32_bf16 v[20:23], v[220:223], a[48:51], v[20:23]
ds_read_b64_tr_b16 v[216:217], v242 offset:33920
ds_read_b64_tr_b16 v[218:219], v242 offset:34944
v_mfma_f32_16x16x32_bf16 v[24:27], v[192:195], a[64:67], v[24:27]
v_cvt_pk_bf16_f32 v234, v48, v49
v_cvt_pk_bf16_f32 v236, v52, v53
v_mfma_f32_16x16x32_bf16 v[24:27], v[196:199], a[72:75], v[24:27]
ds_read_b64_tr_b16 v[220:221], v242 offset:42368
ds_read_b64_tr_b16 v[222:223], v242 offset:43392
v_mfma_f32_16x16x32_bf16 v[56:59], v[196:199], a[76:79], v[56:59]
v_cvt_pk_bf16_f32 v235, v50, v51
v_cvt_pk_bf16_f32 v237, v54, v55
v_mfma_f32_16x16x32_bf16 v[56:59], v[192:195], a[68:71], v[56:59]
ds_read_b64_tr_b16 v[196:197], v243 offset:50816
ds_read_b64_tr_b16 v[198:199], v243 offset:51840
v_mfma_f32_16x16x32_bf16 v[56:59], v[200:203], a[84:87], v[56:59]
v_permlane16_swap_b32 v234, v236
v_permlane16_swap_b32 v235, v237
v_mfma_f32_16x16x32_bf16 v[56:59], v[204:207], a[92:95], v[56:59]
ds_read_b64_tr_b16 v[192:193], v243 offset:59264
ds_read_b64_tr_b16 v[194:195], v243 offset:60288
v_mfma_f32_16x16x32_bf16 v[24:27], v[204:207], a[88:91], v[24:27]
s_waitcnt lgkmcnt(4)
buffer_load_dwordx4 a[228:231], v229, s[28:31], s93 offen offset:2048
v_mfma_f32_16x16x32_bf16 v[24:27], v[200:203], a[80:83], v[24:27]
ds_read_b64_tr_b16 v[204:205], v243 offset:128
ds_read_b64_tr_b16 v[206:207], v243 offset:1152
v_mfma_f32_16x16x32_bf16 v[24:27], v[208:211], a[32:35], v[24:27]
buffer_store_dwordx4 v[234:237], v238, s[32:35], s95 offen offset:2048 sc1
v_mfma_f32_16x16x32_bf16 v[24:27], v[212:215], a[40:43], v[24:27]
ds_read_b64_tr_b16 v[200:201], v243 offset:8576
ds_read_b64_tr_b16 v[202:203], v243 offset:9600
v_mfma_f32_16x16x32_bf16 v[56:59], v[212:215], a[44:47], v[56:59]
v_cvt_pk_bf16_f32 v230, v16, v17
v_cvt_pk_bf16_f32 v232, v20, v21
v_mfma_f32_16x16x32_bf16 v[56:59], v[208:211], a[36:39], v[56:59]
ds_read_b64_tr_b16 v[212:213], v243 offset:17024
ds_read_b64_tr_b16 v[214:215], v243 offset:18048
v_mfma_f32_16x16x32_bf16 v[56:59], v[216:219], a[12:15], v[56:59]
v_cvt_pk_bf16_f32 v231, v18, v19
v_cvt_pk_bf16_f32 v233, v22, v23
v_mfma_f32_16x16x32_bf16 v[56:59], v[220:223], a[4:7], v[56:59]
ds_read_b64_tr_b16 v[208:209], v243 offset:25472
ds_read_b64_tr_b16 v[210:211], v243 offset:26496
v_mfma_f32_16x16x32_bf16 v[24:27], v[220:223], a[0:3], v[24:27]
s_waitcnt lgkmcnt(4)
s_mov_b32 s93, 4096
buffer_load_dwordx4 a[232:235], v229, s[28:31], s93 offen offset:0
v_mfma_f32_16x16x32_bf16 v[24:27], v[216:219], a[48:51], v[24:27]
ds_read_b64_tr_b16 v[220:221], v243 offset:33920
ds_read_b64_tr_b16 v[222:223], v243 offset:34944
v_mfma_f32_16x16x32_bf16 v[28:31], v[196:199], a[64:67], v[28:31]
v_permlane16_swap_b32 v230, v232
v_permlane16_swap_b32 v231, v233
v_mfma_f32_16x16x32_bf16 v[28:31], v[192:195], a[72:75], v[28:31]
ds_read_b64_tr_b16 v[216:217], v243 offset:42368
ds_read_b64_tr_b16 v[218:219], v243 offset:43392
v_mfma_f32_16x16x32_bf16 v[60:63], v[192:195], a[76:79], v[60:63]
buffer_load_dwordx4 a[236:239], v229, s[28:31], s93 offen offset:2048
v_mfma_f32_16x16x32_bf16 v[60:63], v[196:199], a[68:71], v[60:63]
ds_read_b64_tr_b16 v[192:193], v240 offset:50688
ds_read_b64_tr_b16 v[194:195], v240 offset:51712
v_mfma_f32_16x16x32_bf16 v[60:63], v[204:207], a[84:87], v[60:63]
buffer_store_dwordx4 v[230:233], v238, s[32:35], s94 offen offset:2048 sc1
v_mfma_f32_16x16x32_bf16 v[60:63], v[200:203], a[92:95], v[60:63]
ds_read_b64_tr_b16 v[196:197], v240 offset:59136
ds_read_b64_tr_b16 v[198:199], v240 offset:60160
v_mfma_f32_16x16x32_bf16 v[28:31], v[200:203], a[88:91], v[28:31]
s_waitcnt lgkmcnt(4)
s_mov_b32 s93, 8192
buffer_load_dwordx4 a[240:243], v229, s[28:31], s93 offen offset:0
v_mfma_f32_16x16x32_bf16 v[28:31], v[204:207], a[80:83], v[28:31]
ds_read_b64_tr_b16 v[200:201], v240 offset:0
ds_read_b64_tr_b16 v[202:203], v240 offset:1024
v_mfma_f32_16x16x32_bf16 v[28:31], v[212:215], a[32:35], v[28:31]
buffer_load_dwordx4 v[156:159], v225, s[48:51], 0 offen offset:512
v_mfma_f32_16x16x32_bf16 v[28:31], v[208:211], a[40:43], v[28:31]
ds_read_b64_tr_b16 v[204:205], v240 offset:8448
ds_read_b64_tr_b16 v[206:207], v240 offset:9472
v_mfma_f32_16x16x32_bf16 v[60:63], v[208:211], a[44:47], v[60:63]
buffer_load_dwordx4 a[244:247], v229, s[28:31], s93 offen offset:2048
v_mfma_f32_16x16x32_bf16 v[60:63], v[212:215], a[36:39], v[60:63]
ds_read_b64_tr_b16 v[208:209], v240 offset:16896
ds_read_b64_tr_b16 v[210:211], v240 offset:17920
v_mfma_f32_16x16x32_bf16 v[60:63], v[220:223], a[12:15], v[60:63]
buffer_load_dwordx4 v[168:171], v224, s[68:71], 0 offen offset:1024
v_mfma_f32_16x16x32_bf16 v[60:63], v[216:219], a[4:7], v[60:63]
ds_read_b64_tr_b16 v[212:213], v240 offset:25344
ds_read_b64_tr_b16 v[214:215], v240 offset:26368
v_mfma_f32_16x16x32_bf16 v[28:31], v[216:219], a[0:3], v[28:31]
s_waitcnt vmcnt(25) lgkmcnt(4)
s_mov_b32 s93, 12288
buffer_load_dwordx4 a[248:251], v229, s[28:31], s93 offen offset:0
v_mfma_f32_16x16x32_bf16 v[28:31], v[220:223], a[48:51], v[28:31]
ds_read_b64_tr_b16 v[216:217], v240 offset:33792
ds_read_b64_tr_b16 v[218:219], v240 offset:34816
v_mfma_f32_16x16x32_bf16 v[64:67], v[192:195], a[8:11], v[64:67]
v_cvt_pk_bf16_f32 v234, v56, v57
v_cvt_pk_bf16_f32 v236, v60, v61
v_mfma_f32_16x16x32_bf16 v[64:67], v[196:199], a[52:55], v[64:67]
ds_read_b64_tr_b16 v[220:221], v240 offset:42240
ds_read_b64_tr_b16 v[222:223], v240 offset:43264
v_mfma_f32_16x16x32_bf16 v[96:99], v[196:199], a[56:59], v[96:99]
v_cvt_pk_bf16_f32 v235, v58, v59
v_cvt_pk_bf16_f32 v237, v62, v63
v_mfma_f32_16x16x32_bf16 v[96:99], v[192:195], a[16:19], v[96:99]
ds_read_b64_tr_b16 v[196:197], v241 offset:50688
ds_read_b64_tr_b16 v[198:199], v241 offset:51712
v_mfma_f32_16x16x32_bf16 v[96:99], v[200:203], v[128:131], v[96:99]
v_permlane16_swap_b32 v234, v236
v_permlane16_swap_b32 v235, v237
v_mfma_f32_16x16x32_bf16 v[96:99], v[204:207], a[20:23], v[96:99]
ds_read_b64_tr_b16 v[192:193], v241 offset:59136
ds_read_b64_tr_b16 v[194:195], v241 offset:60160
v_mfma_f32_16x16x32_bf16 v[64:67], v[204:207], v[132:135], v[64:67]
s_waitcnt vmcnt(18) lgkmcnt(4)
buffer_load_dwordx4 a[252:255], v229, s[28:31], s93 offen offset:2048
v_mfma_f32_16x16x32_bf16 v[64:67], v[200:203], a[60:63], v[64:67]
ds_read_b64_tr_b16 v[204:205], v241 offset:0
ds_read_b64_tr_b16 v[206:207], v241 offset:1024
v_mfma_f32_16x16x32_bf16 v[64:67], v[208:211], a[24:27], v[64:67]
buffer_store_dwordx4 v[234:237], v238, s[32:35], s95 offen offset:3072 sc1
v_mfma_f32_16x16x32_bf16 v[64:67], v[212:215], v[144:147], v[64:67]
ds_read_b64_tr_b16 v[200:201], v241 offset:8448
ds_read_b64_tr_b16 v[202:203], v241 offset:9472
v_mfma_f32_16x16x32_bf16 v[96:99], v[212:215], v[148:151], v[96:99]
v_cvt_pk_bf16_f32 v230, v24, v25
v_cvt_pk_bf16_f32 v232, v28, v29
v_mfma_f32_16x16x32_bf16 v[96:99], v[208:211], a[28:31], v[96:99]
ds_read_b64_tr_b16 v[212:213], v241 offset:16896
ds_read_b64_tr_b16 v[214:215], v241 offset:17920
v_mfma_f32_16x16x32_bf16 v[96:99], v[216:219], v[164:167], v[96:99]
v_cvt_pk_bf16_f32 v231, v26, v27
v_cvt_pk_bf16_f32 v233, v30, v31
v_mfma_f32_16x16x32_bf16 v[96:99], v[220:223], v[180:183], v[96:99]
ds_read_b64_tr_b16 v[208:209], v241 offset:25344
ds_read_b64_tr_b16 v[210:211], v241 offset:26368
v_mfma_f32_16x16x32_bf16 v[64:67], v[220:223], v[176:179], v[64:67]
s_waitcnt lgkmcnt(4)
s_mov_b32 s93, 0
buffer_load_dwordx4 a[64:67], v229, s[28:31], s93 offen offset:256
v_mfma_f32_16x16x32_bf16 v[64:67], v[216:219], v[160:163], v[64:67]
ds_read_b64_tr_b16 v[220:221], v241 offset:33792
ds_read_b64_tr_b16 v[222:223], v241 offset:34816
v_mfma_f32_16x16x32_bf16 v[68:71], v[196:199], a[8:11], v[68:71]
v_permlane16_swap_b32 v230, v232
v_permlane16_swap_b32 v231, v233
v_mfma_f32_16x16x32_bf16 v[68:71], v[192:195], a[52:55], v[68:71]
ds_read_b64_tr_b16 v[216:217], v241 offset:42240
ds_read_b64_tr_b16 v[218:219], v241 offset:43264
v_mfma_f32_16x16x32_bf16 v[100:103], v[192:195], a[56:59], v[100:103]
buffer_load_dwordx4 a[68:71], v229, s[28:31], s93 offen offset:2304
v_mfma_f32_16x16x32_bf16 v[100:103], v[196:199], a[16:19], v[100:103]
ds_read_b64_tr_b16 v[192:193], v242 offset:50688
ds_read_b64_tr_b16 v[194:195], v242 offset:51712
v_mfma_f32_16x16x32_bf16 v[100:103], v[204:207], v[128:131], v[100:103]
buffer_store_dwordx4 v[230:233], v238, s[32:35], s94 offen offset:3072 sc1
v_mfma_f32_16x16x32_bf16 v[100:103], v[200:203], a[20:23], v[100:103]
ds_read_b64_tr_b16 v[196:197], v242 offset:59136
ds_read_b64_tr_b16 v[198:199], v242 offset:60160
v_mfma_f32_16x16x32_bf16 v[68:71], v[200:203], v[132:135], v[68:71]
s_waitcnt lgkmcnt(4)
s_mov_b32 s93, 4096
buffer_load_dwordx4 a[72:75], v229, s[28:31], s93 offen offset:256
v_mfma_f32_16x16x32_bf16 v[68:71], v[204:207], a[60:63], v[68:71]
ds_read_b64_tr_b16 v[200:201], v242 offset:0
ds_read_b64_tr_b16 v[202:203], v242 offset:1024
v_mfma_f32_16x16x32_bf16 v[68:71], v[212:215], a[24:27], v[68:71]
buffer_load_dwordx4 v[172:175], v225, s[48:51], 0 offen offset:1024
v_mfma_f32_16x16x32_bf16 v[68:71], v[208:211], v[144:147], v[68:71]
ds_read_b64_tr_b16 v[204:205], v242 offset:8448
ds_read_b64_tr_b16 v[206:207], v242 offset:9472
v_mfma_f32_16x16x32_bf16 v[100:103], v[208:211], v[148:151], v[100:103]
buffer_load_dwordx4 a[76:79], v229, s[28:31], s93 offen offset:2304
v_mfma_f32_16x16x32_bf16 v[100:103], v[212:215], a[28:31], v[100:103]
ds_read_b64_tr_b16 v[208:209], v242 offset:16896
ds_read_b64_tr_b16 v[210:211], v242 offset:17920
v_mfma_f32_16x16x32_bf16 v[100:103], v[220:223], v[164:167], v[100:103]
buffer_load_dwordx4 v[184:187], v224, s[68:71], 0 offen offset:1536
v_mfma_f32_16x16x32_bf16 v[100:103], v[216:219], v[180:183], v[100:103]
ds_read_b64_tr_b16 v[212:213], v242 offset:25344
ds_read_b64_tr_b16 v[214:215], v242 offset:26368
v_mfma_f32_16x16x32_bf16 v[68:71], v[216:219], v[176:179], v[68:71]
s_waitcnt lgkmcnt(4)
s_mov_b32 s93, 8192
buffer_load_dwordx4 a[80:83], v229, s[28:31], s93 offen offset:256
v_mfma_f32_16x16x32_bf16 v[68:71], v[220:223], v[160:163], v[68:71]
ds_read_b64_tr_b16 v[216:217], v242 offset:33792
ds_read_b64_tr_b16 v[218:219], v242 offset:34816
v_mfma_f32_16x16x32_bf16 v[72:75], v[192:195], a[8:11], v[72:75]
v_cvt_pk_bf16_f32 v234, v96, v97
v_cvt_pk_bf16_f32 v236, v100, v101
v_mfma_f32_16x16x32_bf16 v[72:75], v[196:199], a[52:55], v[72:75]
ds_read_b64_tr_b16 v[220:221], v242 offset:42240
ds_read_b64_tr_b16 v[222:223], v242 offset:43264
v_mfma_f32_16x16x32_bf16 v[104:107], v[196:199], a[56:59], v[104:107]
v_cvt_pk_bf16_f32 v235, v98, v99
v_cvt_pk_bf16_f32 v237, v102, v103
v_mfma_f32_16x16x32_bf16 v[104:107], v[192:195], a[16:19], v[104:107]
ds_read_b64_tr_b16 v[196:197], v243 offset:50688
ds_read_b64_tr_b16 v[198:199], v243 offset:51712
v_mfma_f32_16x16x32_bf16 v[104:107], v[200:203], v[128:131], v[104:107]
v_permlane16_swap_b32 v234, v236
v_permlane16_swap_b32 v235, v237
v_mfma_f32_16x16x32_bf16 v[104:107], v[204:207], a[20:23], v[104:107]
ds_read_b64_tr_b16 v[192:193], v243 offset:59136
ds_read_b64_tr_b16 v[194:195], v243 offset:60160
v_mfma_f32_16x16x32_bf16 v[72:75], v[204:207], v[132:135], v[72:75]
s_waitcnt lgkmcnt(4)
buffer_load_dwordx4 a[84:87], v229, s[28:31], s93 offen offset:2304
v_mfma_f32_16x16x32_bf16 v[72:75], v[200:203], a[60:63], v[72:75]
ds_read_b64_tr_b16 v[204:205], v243 offset:0
ds_read_b64_tr_b16 v[206:207], v243 offset:1024
v_mfma_f32_16x16x32_bf16 v[72:75], v[208:211], a[24:27], v[72:75]
buffer_store_dwordx4 v[234:237], v238, s[32:35], s97 offen offset:0 sc1
v_mfma_f32_16x16x32_bf16 v[72:75], v[212:215], v[144:147], v[72:75]
ds_read_b64_tr_b16 v[200:201], v243 offset:8448
ds_read_b64_tr_b16 v[202:203], v243 offset:9472
v_mfma_f32_16x16x32_bf16 v[104:107], v[212:215], v[148:151], v[104:107]
v_cvt_pk_bf16_f32 v230, v64, v65
v_cvt_pk_bf16_f32 v232, v68, v69
.La5_LW3_4:
s_cbranch_execz .La5_LW3_R0
v_mfma_f32_16x16x32_bf16 v[104:107], v[208:211], a[28:31], v[104:107]
ds_read_b64_tr_b16 v[212:213], v243 offset:16896
ds_read_b64_tr_b16 v[214:215], v243 offset:17920
v_mfma_f32_16x16x32_bf16 v[104:107], v[216:219], v[164:167], v[104:107]
v_cvt_pk_bf16_f32 v231, v66, v67
v_cvt_pk_bf16_f32 v233, v70, v71
v_mfma_f32_16x16x32_bf16 v[104:107], v[220:223], v[180:183], v[104:107]
ds_read_b64_tr_b16 v[208:209], v243 offset:25344
ds_read_b64_tr_b16 v[210:211], v243 offset:26368
v_mfma_f32_16x16x32_bf16 v[72:75], v[220:223], v[176:179], v[72:75]
s_waitcnt lgkmcnt(4)
s_mov_b32 s93, 12288
buffer_load_dwordx4 a[88:91], v229, s[28:31], s93 offen offset:256
v_mfma_f32_16x16x32_bf16 v[72:75], v[216:219], v[160:163], v[72:75]
ds_read_b64_tr_b16 v[220:221], v243 offset:33792
ds_read_b64_tr_b16 v[222:223], v243 offset:34816
v_mfma_f32_16x16x32_bf16 v[76:79], v[196:199], a[8:11], v[76:79]
v_permlane16_swap_b32 v230, v232
v_permlane16_swap_b32 v231, v233
v_mfma_f32_16x16x32_bf16 v[76:79], v[192:195], a[52:55], v[76:79]
ds_read_b64_tr_b16 v[216:217], v243 offset:42240
ds_read_b64_tr_b16 v[218:219], v243 offset:43264
v_mfma_f32_16x16x32_bf16 v[108:111], v[192:195], a[56:59], v[108:111]
buffer_load_dwordx4 a[92:95], v229, s[28:31], s93 offen offset:2304
v_mfma_f32_16x16x32_bf16 v[108:111], v[196:199], a[16:19], v[108:111]
ds_read_b64_tr_b16 v[192:193], v240 offset:50816
ds_read_b64_tr_b16 v[194:195], v240 offset:51840
v_mfma_f32_16x16x32_bf16 v[108:111], v[204:207], v[128:131], v[108:111]
buffer_store_dwordx4 v[230:233], v238, s[32:35], s96 offen offset:0 sc1
v_mfma_f32_16x16x32_bf16 v[108:111], v[200:203], a[20:23], v[108:111]
ds_read_b64_tr_b16 v[196:197], v240 offset:59264
ds_read_b64_tr_b16 v[198:199], v240 offset:60288
v_mfma_f32_16x16x32_bf16 v[76:79], v[200:203], v[132:135], v[76:79]
s_waitcnt lgkmcnt(4)
v_mfma_f32_16x16x32_bf16 v[76:79], v[204:207], a[60:63], v[76:79]
ds_read_b64_tr_b16 v[200:201], v240 offset:128
ds_read_b64_tr_b16 v[202:203], v240 offset:1152
v_mfma_f32_16x16x32_bf16 v[76:79], v[212:215], a[24:27], v[76:79]
buffer_load_dwordx4 v[188:191], v225, s[48:51], 0 offen offset:1536
v_mfma_f32_16x16x32_bf16 v[76:79], v[208:211], v[144:147], v[76:79]
ds_read_b64_tr_b16 v[204:205], v240 offset:8576
ds_read_b64_tr_b16 v[206:207], v240 offset:9600
v_mfma_f32_16x16x32_bf16 v[108:111], v[208:211], v[148:151], v[108:111]
v_mfma_f32_16x16x32_bf16 v[108:111], v[212:215], a[28:31], v[108:111]
ds_read_b64_tr_b16 v[208:209], v240 offset:17024
ds_read_b64_tr_b16 v[210:211], v240 offset:18048
v_mfma_f32_16x16x32_bf16 v[108:111], v[220:223], v[164:167], v[108:111]
v_mfma_f32_16x16x32_bf16 v[108:111], v[216:219], v[180:183], v[108:111]
ds_read_b64_tr_b16 v[212:213], v240 offset:25472
ds_read_b64_tr_b16 v[214:215], v240 offset:26496
v_mfma_f32_16x16x32_bf16 v[76:79], v[216:219], v[176:179], v[76:79]
s_waitcnt lgkmcnt(4)
v_mfma_f32_16x16x32_bf16 v[76:79], v[220:223], v[160:163], v[76:79]
ds_read_b64_tr_b16 v[216:217], v240 offset:33920
ds_read_b64_tr_b16 v[218:219], v240 offset:34944
v_mfma_f32_16x16x32_bf16 v[80:83], v[192:195], a[8:11], v[80:83]
v_cvt_pk_bf16_f32 v234, v104, v105
v_cvt_pk_bf16_f32 v236, v108, v109
v_mfma_f32_16x16x32_bf16 v[80:83], v[196:199], a[52:55], v[80:83]
ds_read_b64_tr_b16 v[220:221], v240 offset:42368
ds_read_b64_tr_b16 v[222:223], v240 offset:43392
v_mfma_f32_16x16x32_bf16 v[112:115], v[196:199], a[56:59], v[112:115]
v_cvt_pk_bf16_f32 v235, v106, v107
v_cvt_pk_bf16_f32 v237, v110, v111
v_mfma_f32_16x16x32_bf16 v[112:115], v[192:195], a[16:19], v[112:115]
ds_read_b64_tr_b16 v[196:197], v241 offset:50816
ds_read_b64_tr_b16 v[198:199], v241 offset:51840
v_mfma_f32_16x16x32_bf16 v[112:115], v[200:203], v[128:131], v[112:115]
v_permlane16_swap_b32 v234, v236
v_permlane16_swap_b32 v235, v237
v_mfma_f32_16x16x32_bf16 v[112:115], v[204:207], a[20:23], v[112:115]
ds_read_b64_tr_b16 v[192:193], v241 offset:59264
ds_read_b64_tr_b16 v[194:195], v241 offset:60288
v_mfma_f32_16x16x32_bf16 v[80:83], v[204:207], v[132:135], v[80:83]
s_waitcnt lgkmcnt(4)
v_mfma_f32_16x16x32_bf16 v[80:83], v[200:203], a[60:63], v[80:83]
ds_read_b64_tr_b16 v[204:205], v241 offset:128
ds_read_b64_tr_b16 v[206:207], v241 offset:1152
v_mfma_f32_16x16x32_bf16 v[80:83], v[208:211], a[24:27], v[80:83]
buffer_store_dwordx4 v[234:237], v238, s[32:35], s97 offen offset:1024 sc1
v_mfma_f32_16x16x32_bf16 v[80:83], v[212:215], v[144:147], v[80:83]
ds_read_b64_tr_b16 v[200:201], v241 offset:8576
ds_read_b64_tr_b16 v[202:203], v241 offset:9600
v_mfma_f32_16x16x32_bf16 v[112:115], v[212:215], v[148:151], v[112:115]
v_cvt_pk_bf16_f32 v230, v72, v73
v_cvt_pk_bf16_f32 v232, v76, v77
v_mfma_f32_16x16x32_bf16 v[112:115], v[208:211], a[28:31], v[112:115]
ds_read_b64_tr_b16 v[212:213], v241 offset:17024
ds_read_b64_tr_b16 v[214:215], v241 offset:18048
v_mfma_f32_16x16x32_bf16 v[112:115], v[216:219], v[164:167], v[112:115]
v_cvt_pk_bf16_f32 v231, v74, v75
v_cvt_pk_bf16_f32 v233, v78, v79
v_mfma_f32_16x16x32_bf16 v[112:115], v[220:223], v[180:183], v[112:115]
ds_read_b64_tr_b16 v[208:209], v241 offset:25472
ds_read_b64_tr_b16 v[210:211], v241 offset:26496
v_mfma_f32_16x16x32_bf16 v[80:83], v[220:223], v[176:179], v[80:83]
s_waitcnt lgkmcnt(4)
v_mfma_f32_16x16x32_bf16 v[80:83], v[216:219], v[160:163], v[80:83]
ds_read_b64_tr_b16 v[220:221], v241 offset:33920
ds_read_b64_tr_b16 v[222:223], v241 offset:34944
v_mfma_f32_16x16x32_bf16 v[84:87], v[196:199], a[8:11], v[84:87]
v_permlane16_swap_b32 v230, v232
v_permlane16_swap_b32 v231, v233
v_mfma_f32_16x16x32_bf16 v[84:87], v[192:195], a[52:55], v[84:87]
ds_read_b64_tr_b16 v[216:217], v241 offset:42368
ds_read_b64_tr_b16 v[218:219], v241 offset:43392
v_mfma_f32_16x16x32_bf16 v[116:119], v[192:195], a[56:59], v[116:119]
v_mfma_f32_16x16x32_bf16 v[116:119], v[196:199], a[16:19], v[116:119]
ds_read_b64_tr_b16 v[192:193], v242 offset:50816
ds_read_b64_tr_b16 v[194:195], v242 offset:51840
v_mfma_f32_16x16x32_bf16 v[116:119], v[204:207], v[128:131], v[116:119]
buffer_store_dwordx4 v[230:233], v238, s[32:35], s96 offen offset:1024 sc1
v_mfma_f32_16x16x32_bf16 v[116:119], v[200:203], a[20:23], v[116:119]
ds_read_b64_tr_b16 v[196:197], v242 offset:59264
ds_read_b64_tr_b16 v[198:199], v242 offset:60288
v_mfma_f32_16x16x32_bf16 v[84:87], v[200:203], v[132:135], v[84:87]
s_waitcnt lgkmcnt(4)
v_mfma_f32_16x16x32_bf16 v[84:87], v[204:207], a[60:63], v[84:87]
ds_read_b64_tr_b16 v[200:201], v242 offset:128
ds_read_b64_tr_b16 v[202:203], v242 offset:1152
v_mfma_f32_16x16x32_bf16 v[84:87], v[212:215], a[24:27], v[84:87]
v_mfma_f32_16x16x32_bf16 v[84:87], v[208:211], v[144:147], v[84:87]
ds_read_b64_tr_b16 v[204:205], v242 offset:8576
ds_read_b64_tr_b16 v[206:207], v242 offset:9600
v_mfma_f32_16x16x32_bf16 v[116:119], v[208:211], v[148:151], v[116:119]
v_mfma_f32_16x16x32_bf16 v[116:119], v[212:215], a[28:31], v[116:119]
ds_read_b64_tr_b16 v[208:209], v242 offset:17024
ds_read_b64_tr_b16 v[210:211], v242 offset:18048
v_mfma_f32_16x16x32_bf16 v[116:119], v[220:223], v[164:167], v[116:119]
v_mfma_f32_16x16x32_bf16 v[116:119], v[216:219], v[180:183], v[116:119]
ds_read_b64_tr_b16 v[212:213], v242 offset:25472
ds_read_b64_tr_b16 v[214:215], v242 offset:26496
v_mfma_f32_16x16x32_bf16 v[84:87], v[216:219], v[176:179], v[84:87]
s_waitcnt lgkmcnt(4)
v_mfma_f32_16x16x32_bf16 v[84:87], v[220:223], v[160:163], v[84:87]
ds_read_b64_tr_b16 v[216:217], v242 offset:33920
ds_read_b64_tr_b16 v[218:219], v242 offset:34944
v_mfma_f32_16x16x32_bf16 v[88:91], v[192:195], a[8:11], v[88:91]
v_cvt_pk_bf16_f32 v234, v112, v113
v_cvt_pk_bf16_f32 v236, v116, v117
v_mfma_f32_16x16x32_bf16 v[88:91], v[196:199], a[52:55], v[88:91]
ds_read_b64_tr_b16 v[220:221], v242 offset:42368
ds_read_b64_tr_b16 v[222:223], v242 offset:43392
v_mfma_f32_16x16x32_bf16 v[120:123], v[196:199], a[56:59], v[120:123]
v_cvt_pk_bf16_f32 v235, v114, v115
v_cvt_pk_bf16_f32 v237, v118, v119
v_mfma_f32_16x16x32_bf16 v[120:123], v[192:195], a[16:19], v[120:123]
ds_read_b64_tr_b16 v[196:197], v243 offset:50816
ds_read_b64_tr_b16 v[198:199], v243 offset:51840
v_mfma_f32_16x16x32_bf16 v[120:123], v[200:203], v[128:131], v[120:123]
v_permlane16_swap_b32 v234, v236
v_permlane16_swap_b32 v235, v237
v_mfma_f32_16x16x32_bf16 v[120:123], v[204:207], a[20:23], v[120:123]
ds_read_b64_tr_b16 v[192:193], v243 offset:59264
ds_read_b64_tr_b16 v[194:195], v243 offset:60288
v_mfma_f32_16x16x32_bf16 v[88:91], v[204:207], v[132:135], v[88:91]
s_waitcnt lgkmcnt(4)
v_mfma_f32_16x16x32_bf16 v[88:91], v[200:203], a[60:63], v[88:91]
ds_read_b64_tr_b16 v[204:205], v243 offset:128
ds_read_b64_tr_b16 v[206:207], v243 offset:1152
v_mfma_f32_16x16x32_bf16 v[88:91], v[208:211], a[24:27], v[88:91]
buffer_store_dwordx4 v[234:237], v238, s[32:35], s97 offen offset:2048 sc1
v_mfma_f32_16x16x32_bf16 v[88:91], v[212:215], v[144:147], v[88:91]
ds_read_b64_tr_b16 v[200:201], v243 offset:8576
ds_read_b64_tr_b16 v[202:203], v243 offset:9600
v_mfma_f32_16x16x32_bf16 v[120:123], v[212:215], v[148:151], v[120:123]
v_cvt_pk_bf16_f32 v230, v80, v81
v_cvt_pk_bf16_f32 v232, v84, v85
v_mfma_f32_16x16x32_bf16 v[120:123], v[208:211], a[28:31], v[120:123]
ds_read_b64_tr_b16 v[212:213], v243 offset:17024
ds_read_b64_tr_b16 v[214:215], v243 offset:18048
v_mfma_f32_16x16x32_bf16 v[120:123], v[216:219], v[164:167], v[120:123]
v_cvt_pk_bf16_f32 v231, v82, v83
v_cvt_pk_bf16_f32 v233, v86, v87
v_mfma_f32_16x16x32_bf16 v[120:123], v[220:223], v[180:183], v[120:123]
ds_read_b64_tr_b16 v[208:209], v243 offset:25472
ds_read_b64_tr_b16 v[210:211], v243 offset:26496
v_mfma_f32_16x16x32_bf16 v[88:91], v[220:223], v[176:179], v[88:91]
s_waitcnt lgkmcnt(4)
v_mfma_f32_16x16x32_bf16 v[88:91], v[216:219], v[160:163], v[88:91]
ds_read_b64_tr_b16 v[220:221], v243 offset:33920
ds_read_b64_tr_b16 v[222:223], v243 offset:34944
v_mfma_f32_16x16x32_bf16 v[92:95], v[196:199], a[8:11], v[92:95]
v_permlane16_swap_b32 v230, v232
v_permlane16_swap_b32 v231, v233
v_mfma_f32_16x16x32_bf16 v[92:95], v[192:195], a[52:55], v[92:95]
ds_read_b64_tr_b16 v[216:217], v243 offset:42368
ds_read_b64_tr_b16 v[218:219], v243 offset:43392
v_mfma_f32_16x16x32_bf16 v[124:127], v[192:195], a[56:59], v[124:127]
v_mfma_f32_16x16x32_bf16 v[124:127], v[196:199], a[16:19], v[124:127]
v_mfma_f32_16x16x32_bf16 v[124:127], v[204:207], v[128:131], v[124:127]
buffer_store_dwordx4 v[230:233], v238, s[32:35], s96 offen offset:2048 sc1
v_mfma_f32_16x16x32_bf16 v[124:127], v[200:203], a[20:23], v[124:127]
v_mfma_f32_16x16x32_bf16 v[92:95], v[200:203], v[132:135], v[92:95]
s_waitcnt lgkmcnt(0)
v_mfma_f32_16x16x32_bf16 v[92:95], v[204:207], a[60:63], v[92:95]
v_mfma_f32_16x16x32_bf16 v[92:95], v[212:215], a[24:27], v[92:95]
v_mfma_f32_16x16x32_bf16 v[92:95], v[208:211], v[144:147], v[92:95]
v_mfma_f32_16x16x32_bf16 v[124:127], v[208:211], v[148:151], v[124:127]
v_mfma_f32_16x16x32_bf16 v[124:127], v[212:215], a[28:31], v[124:127]
v_mfma_f32_16x16x32_bf16 v[124:127], v[220:223], v[164:167], v[124:127]
v_mfma_f32_16x16x32_bf16 v[124:127], v[216:219], v[180:183], v[124:127]
v_mfma_f32_16x16x32_bf16 v[92:95], v[216:219], v[176:179], v[92:95]
v_mfma_f32_16x16x32_bf16 v[92:95], v[220:223], v[160:163], v[92:95]
s_nop 7
v_cvt_pk_bf16_f32 v234, v120, v121
v_cvt_pk_bf16_f32 v236, v124, v125
v_cvt_pk_bf16_f32 v235, v122, v123
v_cvt_pk_bf16_f32 v237, v126, v127
v_cvt_pk_bf16_f32 v230, v88, v89
v_cvt_pk_bf16_f32 v232, v92, v93
v_cvt_pk_bf16_f32 v231, v90, v91
v_cvt_pk_bf16_f32 v233, v94, v95
v_permlane16_swap_b32 v234, v236
v_permlane16_swap_b32 v235, v237
buffer_store_dwordx4 v[234:237], v238, s[32:35], s97 offen offset:3072 sc1
v_permlane16_swap_b32 v230, v232
v_permlane16_swap_b32 v231, v233
buffer_store_dwordx4 v[230:233], v238, s[32:35], s96 offen offset:3072 sc1
s_lshl_b32 s91, s76, 5
s_add_u32 s91, s91, s72
s_lshl_b32 s20, s91, 9
s_add_u32 s20, s20, 2097152
s_lshr_b32 s93, s91, 1
s_lshl_b32 s93, s93, 15
s_and_b32 s94, s91, 1
s_lshl2_add_u32 s94, s94, s3
s_lshl_b32 s94, s94, 7
s_add_u32 s97, s93, s94
s_add_u32 s32, s26, 71303168
s_addc_u32 s33, s27, 0
s_and_b32 s33, s33, 0xffff
s_mov_b32 s34, 16777216
s_mov_b32 s35, 0x20000
s_add_u32 s44, s26, 171966464
s_addc_u32 s45, s27, 0
s_and_b32 s45, s45, 0xffff
s_mov_b32 s46, 950272
s_mov_b32 s47, 0x20000
s_add_u32 s56, s38, 0
s_addc_u32 s57, s39, 0
s_and_b32 s57, s57, 0xffff
s_mov_b32 s58, 4194304
s_mov_b32 s59, 0x20000
s_add_u32 s68, s16, 0
s_addc_u32 s69, s17, 0
s_and_b32 s69, s69, 0xffff
s_mov_b32 s70, 4194304
s_mov_b32 s71, 0x20000
v_and_b32 v230, 0x70, v255
v_lshrrev_b32 v231, 7, v255
v_lshl_add_u32 v230, v231, 10, v230
v_add_u32 v240, s97, v230
v_add_u32 v241, 8192, v240
v_add_u32 v242, 16384, v240
v_add_u32 v243, 24576, v240
v_mov_b32 v237, 0x3f803f80
v_mov_b32 v236, 0
s_waitcnt vmcnt(6)
v_dot2_f32_bf16 v236, v140, v237, v236
v_dot2_f32_bf16 v236, v141, v237, v236
v_dot2_f32_bf16 v236, v142, v237, v236
v_dot2_f32_bf16 v236, v143, v237, v236
v_dot2_f32_bf16 v236, v156, v237, v236
v_dot2_f32_bf16 v236, v157, v237, v236
v_dot2_f32_bf16 v236, v158, v237, v236
v_dot2_f32_bf16 v236, v159, v237, v236
v_dot2_f32_bf16 v236, v172, v237, v236
v_dot2_f32_bf16 v236, v173, v237, v236
v_dot2_f32_bf16 v236, v174, v237, v236
v_dot2_f32_bf16 v236, v175, v237, v236
v_dot2_f32_bf16 v236, v188, v237, v236
v_dot2_f32_bf16 v236, v189, v237, v236
v_dot2_f32_bf16 v236, v190, v237, v236
v_dot2_f32_bf16 v236, v191, v237, v236
v_lshlrev_b32 v0, 16, v136
v_and_b32 v1, 0xffff0000, v136
v_lshlrev_b32 v2, 16, v137
v_and_b32 v3, 0xffff0000, v137
v_lshlrev_b32 v4, 16, v138
v_and_b32 v5, 0xffff0000, v138
v_lshlrev_b32 v6, 16, v139
v_and_b32 v7, 0xffff0000, v139
v_lshlrev_b32 v8, 16, v152
v_and_b32 v9, 0xffff0000, v152
v_lshlrev_b32 v10, 16, v153
v_and_b32 v11, 0xffff0000, v153
v_lshlrev_b32 v12, 16, v154
v_and_b32 v13, 0xffff0000, v154
v_lshlrev_b32 v14, 16, v155
v_and_b32 v15, 0xffff0000, v155
v_lshlrev_b32 v16, 16, v168
v_and_b32 v17, 0xffff0000, v168
v_lshlrev_b32 v18, 16, v169
v_and_b32 v19, 0xffff0000, v169
v_lshlrev_b32 v20, 16, v170
v_and_b32 v21, 0xffff0000, v170
v_lshlrev_b32 v22, 16, v171
v_and_b32 v23, 0xffff0000, v171
v_lshlrev_b32 v24, 16, v184
v_and_b32 v25, 0xffff0000, v184
v_lshlrev_b32 v26, 16, v185
v_and_b32 v27, 0xffff0000, v185
v_lshlrev_b32 v28, 16, v186
v_and_b32 v29, 0xffff0000, v186
v_lshlrev_b32 v30, 16, v187
v_and_b32 v31, 0xffff0000, v187
s_nop 1
v_add_f32_dpp v236, v236, v236 quad_perm:[1,0,3,2] row_mask:0xf bank_mask:0xf
s_nop 1
v_add_f32_dpp v236, v236, v236 quad_perm:[2,3,0,1] row_mask:0xf bank_mask:0xf
s_nop 1
v_add_f32_dpp v236, v236, v236 row_ror:8 row_mask:0xf bank_mask:0xf
v_mov_b32 v230, v236
s_nop 1
v_permlane16_swap_b32 v230, v236
s_nop 1
v_add_f32 v236, v236, v230
v_mov_b32 v230, v236
s_nop 1
v_permlane32_swap_b32 v230, v236
s_nop 1
v_add_f32 v236, v236, v230
s_mov_b64 exec, 0
s_cmp_eq_u32 s3, 0
s_cbranch_scc1 .La5_LW4_0
s_cmp_eq_u32 s3, 1
s_cbranch_scc1 .La5_LW4_1
s_cmp_eq_u32 s3, 2
s_cbranch_scc1 .La5_LW4_2
s_branch .La5_LW4_3
.La5_LW4_R0:
s_mov_b64 exec, -1
s_waitcnt vmcnt(0)
buffer_store_dwordx2 v[252:253], v250, s[64:67], s92 offen sc1
s_mov_b32 s85, 99
s_cmp_eq_u32 s30, 0
s_cbranch_scc1 .La5_LT_ROWS
; ---- h = x + attn, LayerNorm statistics, router score, two best of eight rows
.La5_LT_ROWS:
s_nop 0
s_nop 0
.La5_LW3_5:
s_cbranch_execz .La5_LW3_R0
s_mov_b32 s96, 0
.La5_LT_P3:
buffer_load_dwordx2 v[250:251], v248, s[64:67], s20 offen sc1
s_waitcnt vmcnt(0)
.La5_LT_P2:
v_cmp_eq_u64 vcc, v[250:251], v[252:253]
s_add_u32 s96, s96, 1
s_lshr_b32 s92, vcc_lo, 1
s_and_b32 s92, s92, vcc_lo
s_lshr_b32 s93, s92, 2
s_and_b32 s92, s92, s93
s_lshr_b32 s93, s92, 4
s_and_b32 s92, s92, s93
s_lshr_b32 s93, s96, 20
s_mul_i32 s93, s93, 0x01010101
s_or_b32 s92, s92, s93
s_andn2_b32 s92, 0x01010101, s92
s_bcnt1_i32_b32 s93, s92
s_cmp_gt_u32 s93, 1
s_cbranch_scc1 .La5_LT_P3
s_or_b32 s93, s92, 0x01000000
s_ff1_i32_b32 s93, s93
s_lshl_b32 s80, s93, 19
s_xor_b32 s93, s80, 8388608
s_xor_b32 s94, s80, 12582912
s_xor_b32 s95, s80, 4194304
buffer_load_dwordx4 v[208:211], v240, s[32:35], s93 offen sc1
buffer_load_dwordx4 v[212:215], v241, s[32:35], s93 offen sc1
buffer_load_dwordx4 v[216:219], v242, s[32:35], s93 offen sc1
buffer_load_dwordx4 v[220:223], v243, s[32:35], s93 offen sc1
buffer_load_dwordx4 v[112:115], v240, s[32:35], s94 offen sc1
buffer_load_dwordx4 v[116:119], v241, s[32:35], s94 offen sc1
buffer_load_dwordx4 v[120:123], v242, s[32:35], s94 offen sc1
buffer_load_dwordx4 v[124:127], v243, s[32:35], s94 offen sc1
buffer_load_dwordx4 v[96:99], v240, s[32:35], s95 offen sc1
buffer_load_dwordx4 v[100:103], v241, s[32:35], s95 offen sc1
buffer_load_dwordx4 v[104:107], v242, s[32:35], s95 offen sc1
buffer_load_dwordx4 v[108:111], v243, s[32:35], s95 offen sc1
s_cmp_lg_u32 s92, 0
s_cbranch_scc1 .La5_LT_P4
buffer_load_dwordx4 v[192:195], v240, s[32:35], s80 offen sc1
buffer_load_dwordx4 v[196:199], v241, s[32:35], s80 offen sc1
buffer_load_dwordx4 v[200:203], v242, s[32:35], s80 offen sc1
buffer_load_dwordx4 v[204:207], v243, s[32:35], s80 offen sc1
.La5_LT_P4:
buffer_load_dwordx2 v[250:251], v248, s[64:67], s20 offen sc1
s_waitcnt vmcnt(8)
v_lshlrev_b32 v32, 16, v208
v_and_b32 v33, 0xffff0000, v208
v_lshlrev_b32 v230, 16, v112
v_and_b32 v231, 0xffff0000, v112
v_pk_add_f32 v[32:33], v[32:33], v[230:231]
v_lshlrev_b32 v34, 16, v209
v_and_b32 v35, 0xffff0000, v209
v_lshlrev_b32 v230, 16, v113
v_and_b32 v231, 0xffff0000, v113
v_pk_add_f32 v[34:35], v[34:35], v[230:231]
v_lshlrev_b32 v36, 16, v210
v_and_b32 v37, 0xffff0000, v210
v_lshlrev_b32 v230, 16, v114
v_and_b32 v231, 0xffff0000, v114
v_pk_add_f32 v[36:37], v[36:37], v[230:231]
v_lshlrev_b32 v38, 16, v211
v_and_b32 v39, 0xffff0000, v211
v_lshlrev_b32 v230, 16, v115
v_and_b32 v231, 0xffff0000, v115
v_pk_add_f32 v[38:39], v[38:39], v[230:231]
s_waitcnt vmcnt(7)
v_lshlrev_b32 v40, 16, v212
v_and_b32 v41, 0xffff0000, v212
v_lshlrev_b32 v230, 16, v116
v_and_b32 v231, 0xffff0000, v116
v_pk_add_f32 v[40:41], v[40:41], v[230:231]
v_lshlrev_b32 v42, 16, v213
v_and_b32 v43, 0xffff0000, v213
v_lshlrev_b32 v230, 16, v117
v_and_b32 v231, 0xffff0000, v117
v_pk_add_f32 v[42:43], v[42:43], v[230:231]
v_lshlrev_b32 v44, 16, v214
v_and_b32 v45, 0xffff0000, v214
v_lshlrev_b32 v230, 16, v118
v_and_b32 v231, 0xffff0000, v118
v_pk_add_f32 v[44:45], v[44:45], v[230:231]
v_lshlrev_b32 v46, 16, v215
v_and_b32 v47, 0xffff0000, v215
v_lshlrev_b32 v230, 16, v119
v_and_b32 v231, 0xffff0000, v119
v_pk_add_f32 v[46:47], v[46:47], v[230:231]
s_waitcnt vmcnt(6)
v_lshlrev_b32 v48, 16, v216
v_and_b32 v49, 0xffff0000, v216
v_lshlrev_b32 v230, 16, v120
v_and_b32 v231, 0xffff0000, v120
v_pk_add_f32 v[48:49], v[48:49], v[230:231]
v_lshlrev_b32 v50, 16, v217
v_and_b32 v51, 0xffff0000, v217
v_lshlrev_b32 v230, 16, v121
v_and_b32 v231, 0xffff0000, v121
v_pk_add_f32 v[50:51], v[50:51], v[230:231]
v_lshlrev_b32 v52, 16, v218
v_and_b32 v53, 0xffff0000, v218
v_lshlrev_b32 v230, 16, v122
v_and_b32 v231, 0xffff0000, v122
v_pk_add_f32 v[52:53], v[52:53], v[230:231]
v_lshlrev_b32 v54, 16, v219
v_and_b32 v55, 0xffff0000, v219
v_lshlrev_b32 v230, 16, v123
v_and_b32 v231, 0xffff0000, v123
v_pk_add_f32 v[54:55], v[54:55], v[230:231]
s_waitcnt vmcnt(5)
v_lshlrev_b32 v56, 16, v220
v_and_b32 v57, 0xffff0000, v220
v_lshlrev_b32 v230, 16, v124
v_and_b32 v231, 0xffff0000, v124
v_pk_add_f32 v[56:57], v[56:57], v[230:231]
v_lshlrev_b32 v58, 16, v221
v_and_b32 v59, 0xffff0000, v221
v_lshlrev_b32 v230, 16, v125
v_and_b32 v231, 0xffff0000, v125
v_pk_add_f32 v[58:59], v[58:59], v[230:231]
v_lshlrev_b32 v60, 16, v222
v_and_b32 v61, 0xffff0000, v222
v_lshlrev_b32 v230, 16, v126
v_and_b32 v231, 0xffff0000, v126
v_pk_add_f32 v[60:61], v[60:61], v[230:231]
v_lshlrev_b32 v62, 16, v223
v_and_b32 v63, 0xffff0000, v223
v_lshlrev_b32 v230, 16, v127
v_and_b32 v231, 0xffff0000, v127
v_pk_add_f32 v[62:63], v[62:63], v[230:231]
s_waitcnt vmcnt(1)
v_lshlrev_b32 v64, 16, v96
v_and_b32 v65, 0xffff0000, v96
v_lshlrev_b32 v66, 16, v97
v_and_b32 v67, 0xffff0000, v97
v_lshlrev_b32 v68, 16, v98
v_and_b32 v69, 0xffff0000, v98
v_lshlrev_b32 v70, 16, v99
v_and_b32 v71, 0xffff0000, v99
v_lshlrev_b32 v72, 16, v100
v_and_b32 v73, 0xffff0000, v100
v_lshlrev_b32 v74, 16, v101
v_and_b32 v75, 0xffff0000, v101
v_lshlrev_b32 v76, 16, v102
v_and_b32 v77, 0xffff0000, v102
v_lshlrev_b32 v78, 16, v103
v_and_b32 v79, 0xffff0000, v103
v_lshlrev_b32 v80, 16, v104
v_and_b32 v81, 0xffff0000, v104
v_lshlrev_b32 v82, 16, v105
v_and_b32 v83, 0xffff0000, v105
v_lshlrev_b32 v84, 16, v106
v_and_b32 v85, 0xffff0000, v106
v_lshlrev_b32 v86, 16, v107
v_and_b32 v87, 0xffff0000, v107
v_lshlrev_b32 v88, 16, v108
v_and_b32 v89, 0xffff0000, v108
v_lshlrev_b32 v90, 16, v109
v_and_b32 v91, 0xffff0000, v109
v_lshlrev_b32 v92, 16, v110
v_and_b32 v93, 0xffff0000, v110
v_lshlrev_b32 v94, 16, v111
v_and_b32 v95, 0xffff0000, v111
v_mov_b32 v232, 0
v_mov_b32 v233, 0
v_mov_b32 v234, 0
s_cmp_eq_u32 s92, 0
s_cbranch_scc1 .La5_LT_SUM
.La5_LT_P5:
s_waitcnt vmcnt(0)
v_cmp_eq_u64 vcc, v[250:251], v[252:253]
s_add_u32 s96, s96, 1
s_lshr_b32 s92, vcc_lo, 1
s_and_b32 s92, s92, vcc_lo
s_lshr_b32 s93, s92, 2
s_and_b32 s92, s92, s93
s_lshr_b32 s93, s92, 4
s_and_b32 s92, s92, s93
s_lshr_b32 s93, s96, 20
s_mul_i32 s93, s93, 0x01010101
s_or_b32 s92, s92, s93
s_andn2_b32 s92, 0x01010101, s92
s_cbranch_scc0 .La5_LT_P6
buffer_load_dwordx2 v[250:251], v248, s[64:67], s20 offen sc1
s_branch .La5_LT_P5
.La5_LT_P6:
buffer_load_dwordx4 v[192:195], v240, s[32:35], s80 offen sc1
buffer_load_dwordx4 v[196:199], v241, s[32:35], s80 offen sc1
buffer_load_dwordx4 v[200:203], v242, s[32:35], s80 offen sc1
buffer_load_dwordx4 v[204:207], v243, s[32:35], s80 offen sc1
.La5_LT_SUM:
s_waitcnt vmcnt(3)
v_lshlrev_b32 v208, 16, v192
v_and_b32 v209, 0xffff0000, v192
v_lshlrev_b32 v210, 16, v193
v_and_b32 v211, 0xffff0000, v193
v_lshlrev_b32 v212, 16, v194
v_and_b32 v213, 0xffff0000, v194
v_lshlrev_b32 v214, 16, v195
v_and_b32 v215, 0xffff0000, v195
v_pk_add_f32 v[208:209], v[208:209], v[64:65]
v_pk_add_f32 v[210:211], v[210:211], v[66:67]
v_pk_add_f32 v[212:213], v[212:213], v[68:69]
v_pk_add_f32 v[214:215], v[214:215], v[70:71]
v_pk_add_f32 v[208:209], v[208:209], v[32:33]
v_pk_add_f32 v[210:211], v[210:211], v[34:35]
v_pk_add_f32 v[212:213], v[212:213], v[36:37]
v_pk_add_f32 v[214:215], v[214:215], v[38:39]
v_pk_add_f32 v[208:209], v[208:209], v[0:1]
v_pk_add_f32 v[210:211], v[210:211], v[2:3]
v_pk_add_f32 v[212:213], v[212:213], v[4:5]
v_pk_add_f32 v[214:215], v[214:215], v[6:7]
v_cvt_pk_bf16_f32 v96, v208, v209
v_cvt_pk_bf16_f32 v97, v210, v211
v_cvt_pk_bf16_f32 v98, v212, v213
v_cvt_pk_bf16_f32 v99, v214, v215
v_dot2_f32_bf16 v232, v96, v237, v232
v_dot2_f32_bf16 v233, v96, v96, v233
v_dot2_f32_bf16 v234, v96, v140, v234
v_dot2_f32_bf16 v232, v97, v237, v232
v_dot2_f32_bf16 v233, v97, v97, v233
v_dot2_f32_bf16 v234, v97, v141, v234
v_dot2_f32_bf16 v232, v98, v237, v232
v_dot2_f32_bf16 v233, v98, v98, v233
v_dot2_f32_bf16 v234, v98, v142, v234
v_dot2_f32_bf16 v232, v99, v237, v232
v_dot2_f32_bf16 v233, v99, v99, v233
v_dot2_f32_bf16 v234, v99, v143, v234
s_waitcnt vmcnt(2)
v_lshlrev_b32 v208, 16, v196
v_and_b32 v209, 0xffff0000, v196
v_lshlrev_b32 v210, 16, v197
v_and_b32 v211, 0xffff0000, v197
v_lshlrev_b32 v212, 16, v198
v_and_b32 v213, 0xffff0000, v198
v_lshlrev_b32 v214, 16, v199
v_and_b32 v215, 0xffff0000, v199
v_pk_add_f32 v[208:209], v[208:209], v[72:73]
v_pk_add_f32 v[210:211], v[210:211], v[74:75]
v_pk_add_f32 v[212:213], v[212:213], v[76:77]
v_pk_add_f32 v[214:215], v[214:215], v[78:79]
v_pk_add_f32 v[208:209], v[208:209], v[40:41]
v_pk_add_f32 v[210:211], v[210:211], v[42:43]
v_pk_add_f32 v[212:213], v[212:213], v[44:45]
v_pk_add_f32 v[214:215], v[214:215], v[46:47]
v_pk_add_f32 v[208:209], v[208:209], v[8:9]
v_pk_add_f32 v[210:211], v[210:211], v[10:11]
v_pk_add_f32 v[212:213], v[212:213], v[12:13]
v_pk_add_f32 v[214:215], v[214:215], v[14:15]
v_cvt_pk_bf16_f32 v100, v208, v209
v_cvt_pk_bf16_f32 v101, v210, v211
v_cvt_pk_bf16_f32 v102, v212, v213
v_cvt_pk_bf16_f32 v103, v214, v215
v_dot2_f32_bf16 v232, v100, v237, v232
v_dot2_f32_bf16 v233, v100, v100, v233
v_dot2_f32_bf16 v234, v100, v156, v234
v_dot2_f32_bf16 v232, v101, v237, v232
v_dot2_f32_bf16 v233, v101, v101, v233
v_dot2_f32_bf16 v234, v101, v157, v234
v_dot2_f32_bf16 v232, v102, v237, v232
v_dot2_f32_bf16 v233, v102, v102, v233
v_dot2_f32_bf16 v234, v102, v158, v234
v_dot2_f32_bf16 v232, v103, v237, v232
v_dot2_f32_bf16 v233, v103, v103, v233
v_dot2_f32_bf16 v234, v103, v159, v234
s_waitcnt vmcnt(1)
v_lshlrev_b32 v208, 16, v200
v_and_b32 v209, 0xffff0000, v200
v_lshlrev_b32 v210, 16, v201
v_and_b32 v211, 0xffff0000, v201
v_lshlrev_b32 v212, 16, v202
v_and_b32 v213, 0xffff0000, v202
v_lshlrev_b32 v214, 16, v203
v_and_b32 v215, 0xffff0000, v203
.La5_LW4_0:
s_cbranch_execz .La5_LW4_4
v_pk_add_f32 v[208:209], v[208:209], v[80:81]
v_pk_add_f32 v[210:211], v[210:211], v[82:83]
v_pk_add_f32 v[212:213], v[212:213], v[84:85]
v_pk_add_f32 v[214:215], v[214:215], v[86:87]
v_pk_add_f32 v[208:209], v[208:209], v[48:49]
v_pk_add_f32 v[210:211], v[210:211], v[50:51]
v_pk_add_f32 v[212:213], v[212:213], v[52:53]
v_pk_add_f32 v[214:215], v[214:215], v[54:55]
v_pk_add_f32 v[208:209], v[208:209], v[16:17]
v_pk_add_f32 v[210:211], v[210:211], v[18:19]
v_pk_add_f32 v[212:213], v[212:213], v[20:21]
v_pk_add_f32 v[214:215], v[214:215], v[22:23]
v_cvt_pk_bf16_f32 v104, v208, v209
v_cvt_pk_bf16_f32 v105, v210, v211
v_cvt_pk_bf16_f32 v106, v212, v213
v_cvt_pk_bf16_f32 v107, v214, v215
v_dot2_f32_bf16 v232, v104, v237, v232
v_dot2_f32_bf16 v233, v104, v104, v233
v_dot2_f32_bf16 v234, v104, v172, v234
v_dot2_f32_bf16 v232, v105, v237, v232
v_dot2_f32_bf16 v233, v105, v105, v233
v_dot2_f32_bf16 v234, v105, v173, v234
v_dot2_f32_bf16 v232, v106, v237, v232
v_dot2_f32_bf16 v233, v106, v106, v233
v_dot2_f32_bf16 v234, v106, v174, v234
v_dot2_f32_bf16 v232, v107, v237, v232
v_dot2_f32_bf16 v233, v107, v107, v233
v_dot2_f32_bf16 v234, v107, v175, v234
s_waitcnt vmcnt(0)
v_lshlrev_b32 v208, 16, v204
v_and_b32 v209, 0xffff0000, v204
v_lshlrev_b32 v210, 16, v205
v_and_b32 v211, 0xffff0000, v205
v_lshlrev_b32 v212, 16, v206
v_and_b32 v213, 0xffff0000, v206
v_lshlrev_b32 v214, 16, v207
v_and_b32 v215, 0xffff0000, v207
v_pk_add_f32 v[208:209], v[208:209], v[88:89]
v_pk_add_f32 v[210:211], v[210:211], v[90:91]
v_pk_add_f32 v[212:213], v[212:213], v[92:93]
v_pk_add_f32 v[214:215], v[214:215], v[94:95]
v_pk_add_f32 v[208:209], v[208:209], v[56:57]
v_pk_add_f32 v[210:211], v[210:211], v[58:59]
v_pk_add_f32 v[212:213], v[212:213], v[60:61]
v_pk_add_f32 v[214:215], v[214:215], v[62:63]
v_pk_add_f32 v[208:209], v[208:209], v[24:25]
v_pk_add_f32 v[210:211], v[210:211], v[26:27]
v_pk_add_f32 v[212:213], v[212:213], v[28:29]
v_pk_add_f32 v[214:215], v[214:215], v[30:31]
v_cvt_pk_bf16_f32 v108, v208, v209
v_cvt_pk_bf16_f32 v109, v210, v211
v_cvt_pk_bf16_f32 v110, v212, v213
v_cvt_pk_bf16_f32 v111, v214, v215
v_dot2_f32_bf16 v232, v108, v237, v232
v_dot2_f32_bf16 v233, v108, v108, v233
v_dot2_f32_bf16 v234, v108, v188, v234
v_dot2_f32_bf16 v232, v109, v237, v232
v_dot2_f32_bf16 v233, v109, v109, v233
v_dot2_f32_bf16 v234, v109, v189, v234
v_dot2_f32_bf16 v232, v110, v237, v232
v_dot2_f32_bf16 v233, v110, v110, v233
v_dot2_f32_bf16 v234, v110, v190, v234
v_dot2_f32_bf16 v232, v111, v237, v232
v_dot2_f32_bf16 v233, v111, v111, v233
v_dot2_f32_bf16 v234, v111, v191, v234
s_waitcnt vmcnt(0)
s_lshl2_add_u32 s92, s91, s3
s_lshl_b32 s92, s92, 1
s_sub_u32 s92, 0xffff, s92
v_add_f32_dpp v232, v232, v232 quad_perm:[1,0,3,2] row_mask:0xf bank_mask:0xf
v_add_f32_dpp v233, v233, v233 quad_perm:[1,0,3,2] row_mask:0xf bank_mask:0xf
v_add_f32_dpp v234, v234, v234 quad_perm:[1,0,3,2] row_mask:0xf bank_mask:0xf
v_add_f32_dpp v232, v232, v232 quad_perm:[2,3,0,1] row_mask:0xf bank_mask:0xf
v_add_f32_dpp v233, v233, v233 quad_perm:[2,3,0,1] row_mask:0xf bank_mask:0xf
v_add_f32_dpp v234, v234, v234 quad_perm:[2,3,0,1] row_mask:0xf bank_mask:0xf
v_add_f32_dpp v232, v232, v232 row_ror:8 row_mask:0xf bank_mask:0xf
v_add_f32_dpp v233, v233, v233 row_ror:8 row_mask:0xf bank_mask:0xf
v_add_f32_dpp v234, v234, v234 row_ror:8 row_mask:0xf bank_mask:0xf
v_mov_b32 v230, v232
v_mov_b32 v231, v233
v_mov_b32 v235, v234
v_permlane16_swap_b32 v230, v232
v_permlane16_swap_b32 v231, v233
v_permlane16_swap_b32 v235, v234
v_add_f32 v232, v232, v230
v_add_f32 v233, v233, v231
v_add_f32 v234, v234, v235
v_mov_b32 v230, v232
v_mov_b32 v231, v233
v_mov_b32 v235, v234
v_permlane32_swap_b32 v230, v232
v_permlane32_swap_b32 v231, v233
v_permlane32_swap_b32 v235, v234
v_add_f32 v232, v232, v230
v_add_f32 v233, v233, v231
v_add_f32 v234, v234, v235
v_mul_f32 v244, 0x3a800000, v232
v_mul_f32 v233, 0x3a800000, v233
v_fma_f32 v230, -v244, v244, v233
v_mul_f32 v233, 0x3a800000, v233
v_cmp_nge_f32 vcc, v230, v233
v_bfe_u32 v235, v255, 6, 1
v_sub_u32 v235, s92, v235
s_cbranch_vccnz .La5_LT_VAR
.La5_LT_VOK:
v_cvt_pk_bf16_f32 v244, v244, v244
v_lshlrev_b32 v244, 16, v244
v_cvt_pk_bf16_f32 v230, v230, v230
v_lshlrev_b32 v230, 16, v230
v_fma_f32 v234, -v244, v236, v234
v_add_f32 v230, 0x3727c5ac, v230
v_cvt_pk_bf16_f32 v230, v230, v230
v_lshlrev_b32 v230, 16, v230
v_sqrt_f32 v230, v230
v_lshrrev_b32 v232, 3, v255
v_cvt_pk_bf16_f32 v230, v230, v230
v_lshlrev_b32 v231, 16, v230
v_rcp_f32 v246, v231
v_lshrrev_b32 v230, 16, v230
v_or_b32 v233, v244, v230
v_mul_f32 v234, v234, v246
s_lshl_b32 s93, s3, 4
v_cvt_pk_bf16_f32 v234, v234, v234
v_and_b32 v230, 8, v232
v_ashrrev_i32 v232, 31, v234
v_or_b32 v232, 0x80000000, v232
v_xor_b32 v232, v232, v234
v_and_b32 v232, 0xffff0000, v232
v_add_u32 v230, s93, v230
v_or_b32 v232, v232, v235
ds_write_b64 v230, v[232:233] offset:0
v_lshrrev_b32 v230, 1, v255
v_and_b32 v230, 56, v230
s_waitcnt lgkmcnt(0)
s_barrier
s_cmp_lg_u32 s3, 0
s_cbranch_scc1 .La5_LT_OUT
ds_read_b64 v[232:233], v230 offset:0
s_lshl3_add_u32 s92, s72, s76
s_lshl_b32 s92, s92, 4
v_lshrrev_b32 v235, 7, v255
v_lshlrev_b32 v235, 12, v235
s_waitcnt lgkmcnt(0)
v_mov_b32 v234, v232
s_nop 1
v_max_u32_dpp v234, v234, v234 quad_perm:[1,0,3,2] row_mask:0xf bank_mask:0xf
s_nop 1
v_max_u32_dpp v234, v234, v234 quad_perm:[2,3,0,1] row_mask:0xf bank_mask:0xf
s_nop 1
v_max_u32_dpp v234, v234, v234 row_half_mirror row_mask:0xf bank_mask:0xf
s_nop 1
v_cmp_eq_u32 vcc, v232, v234
v_add_u32 v231, 8, v235
s_nop 0
v_cndmask_b32 v234, v232, 0, vcc
v_cndmask_b32 v235, v231, v235, vcc
s_nop 0
v_max_u32_dpp v234, v234, v234 quad_perm:[1,0,3,2] row_mask:0xf bank_mask:0xf
s_nop 1
v_max_u32_dpp v234, v234, v234 quad_perm:[2,3,0,1] row_mask:0xf bank_mask:0xf
s_nop 1
v_max_u32_dpp v234, v234, v234 row_half_mirror row_mask:0xf bank_mask:0xf
s_nop 1
v_cmp_ge_u32 vcc, v232, v234
s_nop 1
s_mov_b64 exec, vcc
buffer_store_dwordx2 v[232:233], v235, s[44:47], s92 offen sc1
s_mov_b64 exec, -1
; ---- out = h + .La5_LN(h) of the own rows; FFN weights into MFMA order
.La5_LT_OUT:
v_mul_f32 v244, v244, v246
v_mov_b32 v247, v246
v_xor_b32 v244, 0x80000000, v244
v_mov_b32 v245, v244
v_lshlrev_b32 v230, 16, v96
v_and_b32 v231, 0xffff0000, v96
v_pk_fma_f32 v[232:233], v[230:231], v[246:247], v[244:245]
v_cvt_pk_bf16_f32 v232, v232, v233
v_lshlrev_b32 v234, 16, v232
v_and_b32 v235, 0xffff0000, v232
v_pk_add_f32 v[230:231], v[230:231], v[234:235]
v_cvt_pk_bf16_f32 v192, v230, v231
v_lshlrev_b32 v230, 16, v97
v_and_b32 v231, 0xffff0000, v97
v_pk_fma_f32 v[232:233], v[230:231], v[246:247], v[244:245]
v_cvt_pk_bf16_f32 v232, v232, v233
v_lshlrev_b32 v234, 16, v232
v_and_b32 v235, 0xffff0000, v232
v_pk_add_f32 v[230:231], v[230:231], v[234:235]
v_cvt_pk_bf16_f32 v193, v230, v231
v_lshlrev_b32 v230, 16, v98
v_and_b32 v231, 0xffff0000, v98
v_pk_fma_f32 v[232:233], v[230:231], v[246:247], v[244:245]
v_cvt_pk_bf16_f32 v232, v232, v233
v_lshlrev_b32 v234, 16, v232
v_and_b32 v235, 0xffff0000, v232
v_pk_add_f32 v[230:231], v[230:231], v[234:235]
v_cvt_pk_bf16_f32 v194, v230, v231
v_lshlrev_b32 v230, 16, v99
v_and_b32 v231, 0xffff0000, v99
v_pk_fma_f32 v[232:233], v[230:231], v[246:247], v[244:245]
v_cvt_pk_bf16_f32 v232, v232, v233
v_lshlrev_b32 v234, 16, v232
v_and_b32 v235, 0xffff0000, v232
v_pk_add_f32 v[230:231], v[230:231], v[234:235]
v_cvt_pk_bf16_f32 v195, v230, v231
v_lshlrev_b32 v230, 16, v100
v_and_b32 v231, 0xffff0000, v100
v_pk_fma_f32 v[232:233], v[230:231], v[246:247], v[244:245]
v_cvt_pk_bf16_f32 v232, v232, v233
v_lshlrev_b32 v234, 16, v232
v_and_b32 v235, 0xffff0000, v232
v_pk_add_f32 v[230:231], v[230:231], v[234:235]
v_cvt_pk_bf16_f32 v196, v230, v231
v_lshlrev_b32 v230, 16, v101
v_and_b32 v231, 0xffff0000, v101
v_pk_fma_f32 v[232:233], v[230:231], v[246:247], v[244:245]
v_cvt_pk_bf16_f32 v232, v232, v233
v_lshlrev_b32 v234, 16, v232
v_and_b32 v235, 0xffff0000, v232
v_pk_add_f32 v[230:231], v[230:231], v[234:235]
v_cvt_pk_bf16_f32 v197, v230, v231
v_lshlrev_b32 v230, 16, v102
v_and_b32 v231, 0xffff0000, v102
v_pk_fma_f32 v[232:233], v[230:231], v[246:247], v[244:245]
v_cvt_pk_bf16_f32 v232, v232, v233
v_lshlrev_b32 v234, 16, v232
v_and_b32 v235, 0xffff0000, v232
v_pk_add_f32 v[230:231], v[230:231], v[234:235]
v_cvt_pk_bf16_f32 v198, v230, v231
v_lshlrev_b32 v230, 16, v103
v_and_b32 v231, 0xffff0000, v103
v_pk_fma_f32 v[232:233], v[230:231], v[246:247], v[244:245]
v_cvt_pk_bf16_f32 v232, v232, v233
v_lshlrev_b32 v234, 16, v232
v_and_b32 v235, 0xffff0000, v232
v_pk_add_f32 v[230:231], v[230:231], v[234:235]
v_cvt_pk_bf16_f32 v199, v230, v231
v_lshlrev_b32 v230, 16, v104
v_and_b32 v231, 0xffff0000, v104
v_pk_fma_f32 v[232:233], v[230:231], v[246:247], v[244:245]
v_cvt_pk_bf16_f32 v232, v232, v233
v_lshlrev_b32 v234, 16, v232
v_and_b32 v235, 0xffff0000, v232
v_pk_add_f32 v[230:231], v[230:231], v[234:235]
v_cvt_pk_bf16_f32 v200, v230, v231
v_lshlrev_b32 v230, 16, v105
v_and_b32 v231, 0xffff0000, v105
v_pk_fma_f32 v[232:233], v[230:231], v[246:247], v[244:245]
v_cvt_pk_bf16_f32 v232, v232, v233
v_lshlrev_b32 v234, 16, v232
v_and_b32 v235, 0xffff0000, v232
v_pk_add_f32 v[230:231], v[230:231], v[234:235]
v_cvt_pk_bf16_f32 v201, v230, v231
v_lshlrev_b32 v230, 16, v106
v_and_b32 v231, 0xffff0000, v106
v_pk_fma_f32 v[232:233], v[230:231], v[246:247], v[244:245]
v_cvt_pk_bf16_f32 v232, v232, v233
v_lshlrev_b32 v234, 16, v232
v_and_b32 v235, 0xffff0000, v232
v_pk_add_f32 v[230:231], v[230:231], v[234:235]
v_cvt_pk_bf16_f32 v202, v230, v231
v_lshlrev_b32 v230, 16, v107
v_and_b32 v231, 0xffff0000, v107
v_pk_fma_f32 v[232:233], v[230:231], v[246:247], v[244:245]
v_cvt_pk_bf16_f32 v232, v232, v233
v_lshlrev_b32 v234, 16, v232
v_and_b32 v235, 0xffff0000, v232
v_pk_add_f32 v[230:231], v[230:231], v[234:235]
v_cvt_pk_bf16_f32 v203, v230, v231
v_lshlrev_b32 v230, 16, v108
v_and_b32 v231, 0xffff0000, v108
v_pk_fma_f32 v[232:233], v[230:231], v[246:247], v[244:245]
v_cvt_pk_bf16_f32 v232, v232, v233
v_lshlrev_b32 v234, 16, v232
v_and_b32 v235, 0xffff0000, v232
v_pk_add_f32 v[230:231], v[230:231], v[234:235]
v_cvt_pk_bf16_f32 v204, v230, v231
v_lshlrev_b32 v230, 16, v109
v_and_b32 v231, 0xffff0000, v109
v_pk_fma_f32 v[232:233], v[230:231], v[246:247], v[244:245]
v_cvt_pk_bf16_f32 v232, v232, v233
v_lshlrev_b32 v234, 16, v232
v_and_b32 v235, 0xffff0000, v232
v_pk_add_f32 v[230:231], v[230:231], v[234:235]
v_cvt_pk_bf16_f32 v205, v230, v231
v_lshlrev_b32 v230, 16, v110
v_and_b32 v231, 0xffff0000, v110
v_pk_fma_f32 v[232:233], v[230:231], v[246:247], v[244:245]
v_cvt_pk_bf16_f32 v232, v232, v233
v_lshlrev_b32 v234, 16, v232
v_and_b32 v235, 0xffff0000, v232
v_pk_add_f32 v[230:231], v[230:231], v[234:235]
v_cvt_pk_bf16_f32 v206, v230, v231
v_lshlrev_b32 v230, 16, v111
v_and_b32 v231, 0xffff0000, v111
v_pk_fma_f32 v[232:233], v[230:231], v[246:247], v[244:245]
v_cvt_pk_bf16_f32 v232, v232, v233
v_lshlrev_b32 v234, 16, v232
v_and_b32 v235, 0xffff0000, v232
v_pk_add_f32 v[230:231], v[230:231], v[234:235]
v_cvt_pk_bf16_f32 v207, v230, v231
s_cmp_eq_u32 s30, 0
s_cbranch_scc1 .La5_LT_SEL
s_mov_b32 s94, 0x05040100
s_mov_b32 s95, 0x07060302
s_waitcnt vmcnt(0)
v_accvgpr_read_b32 v230, a224
v_accvgpr_read_b32 v231, a228
v_perm_b32 v128, v231, v230, s94
v_perm_b32 v132, v231, v230, s95
v_accvgpr_read_b32 v230, a232
v_accvgpr_read_b32 v231, a236
v_perm_b32 v129, v231, v230, s94
v_perm_b32 v133, v231, v230, s95
v_accvgpr_read_b32 v230, a240
v_accvgpr_read_b32 v231, a244
v_perm_b32 v130, v231, v230, s94
v_perm_b32 v134, v231, v230, s95
v_accvgpr_read_b32 v230, a248
v_accvgpr_read_b32 v231, a252
v_perm_b32 v131, v231, v230, s94
v_perm_b32 v135, v231, v230, s95
v_accvgpr_read_b32 v230, a225
v_accvgpr_read_b32 v231, a229
v_perm_b32 v136, v231, v230, s94
v_perm_b32 v140, v231, v230, s95
v_accvgpr_read_b32 v230, a233
v_accvgpr_read_b32 v231, a237
v_perm_b32 v137, v231, v230, s94
v_perm_b32 v141, v231, v230, s95
v_accvgpr_read_b32 v230, a241
v_accvgpr_read_b32 v231, a245
v_perm_b32 v138, v231, v230, s94
v_perm_b32 v142, v231, v230, s95
v_accvgpr_read_b32 v230, a249
v_accvgpr_read_b32 v231, a253
v_perm_b32 v139, v231, v230, s94
v_perm_b32 v143, v231, v230, s95
v_accvgpr_read_b32 v230, a226
v_accvgpr_read_b32 v231, a230
v_perm_b32 v144, v231, v230, s94
v_perm_b32 v148, v231, v230, s95
v_accvgpr_read_b32 v230, a234
v_accvgpr_read_b32 v231, a238
v_perm_b32 v145, v231, v230, s94
v_perm_b32 v149, v231, v230, s95
v_accvgpr_read_b32 v230, a242
v_accvgpr_read_b32 v231, a246
v_perm_b32 v146, v231, v230, s94
v_perm_b32 v150, v231, v230, s95
v_accvgpr_read_b32 v230, a250
v_accvgpr_read_b32 v231, a254
v_perm_b32 v147, v231, v230, s94
v_perm_b32 v151, v231, v230, s95
v_accvgpr_read_b32 v230, a227
v_accvgpr_read_b32 v231, a231
v_perm_b32 v152, v231, v230, s94
v_perm_b32 v156, v231, v230, s95
v_accvgpr_read_b32 v230, a235
v_accvgpr_read_b32 v231, a239
v_perm_b32 v153, v231, v230, s94
v_perm_b32 v157, v231, v230, s95
v_accvgpr_read_b32 v230, a243
v_accvgpr_read_b32 v231, a247
v_perm_b32 v154, v231, v230, s94
v_perm_b32 v158, v231, v230, s95
v_accvgpr_read_b32 v230, a251
v_accvgpr_read_b32 v231, a255
v_perm_b32 v155, v231, v230, s94
v_perm_b32 v159, v231, v230, s95
v_accvgpr_read_b32 v230, a64
v_accvgpr_read_b32 v231, a68
v_perm_b32 v160, v231, v230, s94
v_perm_b32 v164, v231, v230, s95
v_accvgpr_read_b32 v230, a72
v_accvgpr_read_b32 v231, a76
v_perm_b32 v161, v231, v230, s94
v_perm_b32 v165, v231, v230, s95
v_accvgpr_read_b32 v230, a80
v_accvgpr_read_b32 v231, a84
v_perm_b32 v162, v231, v230, s94
v_perm_b32 v166, v231, v230, s95
v_accvgpr_read_b32 v230, a88
v_accvgpr_read_b32 v231, a92
v_perm_b32 v163, v231, v230, s94
v_perm_b32 v167, v231, v230, s95
v_accvgpr_read_b32 v230, a65
v_accvgpr_read_b32 v231, a69
v_perm_b32 v168, v231, v230, s94
v_perm_b32 v172, v231, v230, s95
v_accvgpr_read_b32 v230, a73
v_accvgpr_read_b32 v231, a77
v_perm_b32 v169, v231, v230, s94
v_perm_b32 v173, v231, v230, s95
v_accvgpr_read_b32 v230, a81
v_accvgpr_read_b32 v231, a85
v_perm_b32 v170, v231, v230, s94
v_perm_b32 v174, v231, v230, s95
v_accvgpr_read_b32 v230, a89
v_accvgpr_read_b32 v231, a93
v_perm_b32 v171, v231, v230, s94
v_perm_b32 v175, v231, v230, s95
v_accvgpr_read_b32 v230, a66
v_accvgpr_read_b32 v231, a70
v_perm_b32 v176, v231, v230, s94
v_perm_b32 v180, v231, v230, s95
v_accvgpr_read_b32 v230, a74
v_accvgpr_read_b32 v231, a78
v_perm_b32 v177, v231, v230, s94
v_perm_b32 v181, v231, v230, s95
v_accvgpr_read_b32 v230, a82
v_accvgpr_read_b32 v231, a86
v_perm_b32 v178, v231, v230, s94
v_perm_b32 v182, v231, v230, s95
v_accvgpr_read_b32 v230, a90
v_accvgpr_read_b32 v231, a94
v_perm_b32 v179, v231, v230, s94
v_perm_b32 v183, v231, v230, s95
v_accvgpr_read_b32 v230, a67
v_accvgpr_read_b32 v231, a71
v_perm_b32 v184, v231, v230, s94
v_perm_b32 v188, v231, v230, s95
v_accvgpr_read_b32 v230, a75
v_accvgpr_read_b32 v231, a79
v_perm_b32 v185, v231, v230, s94
v_perm_b32 v189, v231, v230, s95
v_accvgpr_read_b32 v230, a83
v_accvgpr_read_b32 v231, a87
v_perm_b32 v186, v231, v230, s94
v_perm_b32 v190, v231, v230, s95
v_accvgpr_read_b32 v230, a91
v_accvgpr_read_b32 v231, a95
v_perm_b32 v187, v231, v230, s94
v_perm_b32 v191, v231, v230, s95
s_and_b32 s92, s3, 1
s_lshr_b32 s93, s3, 1
v_lshrrev_b32 v230, 6, v255
v_and_b32 v231, 48, v255
v_lshl_add_u32 v240, v230, 10, v231
s_lshl_b32 s96, s92, 14
v_add_u32 v240, s96, v240
s_lshl_b32 s96, s92, 10
v_add_u32 v241, s96, v255
s_lshl_b32 s97, s3, 10
v_add_u32 v208, s97, v255
v_bfe_u32 v230, v255, 4, 1
v_bfe_u32 v231, v255, 6, 2
v_lshrrev_b32 v232, 8, v255
v_lshlrev_b32 v209, 11, v230
v_lshl_add_u32 v209, v231, 8, v209
v_lshl_add_u32 v209, v232, 6, v209
v_add_u32 v209, s96, v209
v_bfe_u32 v233, v255, 4, 2
v_mul_u32_u24 v210, 640, v233
v_lshl_add_u32 v210, v231, 3, v210
s_mul_i32 s96, s93, 2560
s_lshl_b32 s92, s92, 5
s_add_u32 s96, s96, s92
v_add_u32 v210, s96, v210
v_cmp_eq_u32 s[98:99], v231, v232
v_mul_u32_u24 v211, 5, v255
s_lshl_b32 s96, s3, 7
v_lshrrev_b32 v212, 3, v255
v_and_b32 v212, 62, v212
v_add_u32 v212, s96, v212
v_lshlrev_b32 v234, 6, v230
v_lshl_add_u32 v234, v232, 4, v234
v_add_u32 v234, s96, v234
v_mov_b32 v235, 1024
v_cmp_eq_u32 vcc, 0, v231
s_nop 1
v_cndmask_b32 v216, v235, v234, vcc
v_cmp_eq_u32 vcc, 1, v231
s_nop 1
v_cndmask_b32 v217, v235, v234, vcc
v_cmp_eq_u32 vcc, 2, v231
s_nop 1
v_cndmask_b32 v218, v235, v234, vcc
v_cmp_eq_u32 vcc, 3, v231
s_nop 1
v_cndmask_b32 v219, v235, v234, vcc
v_mov_b32 v220, 0
v_mov_b32 v221, 0
v_mov_b32 v222, 0
v_mov_b32 v223, 0
ds_write_b128 v235, v[220:223] offset:10240
s_lshl_b32 s92, s100, 13
s_add_u32 s80, s92, 32768
s_add_u32 s20, s80, 4096
; ---- top two rows of the sequence; SwiGLU slice of 32 hidden units
.La5_LT_SEL:
.La5_LW4_1:
s_cbranch_execz .La5_LW4_R0
s_and_b32 s97, s2, 7
s_lshl_b32 s97, s97, 12
s_cmp_lg_u32 s3, 0
s_cbranch_scc1 .La5_LT_S2
s_mov_b32 s96, 0
.La5_LT_S1:
s_cmp_lg_u32 s30, 0
s_nop 0
s_cbranch_scc1 .La5_LT_S0
s_sleep 24
.La5_LT_S0:
buffer_load_dwordx4 v[224:227], v255, s[44:47], s97 offen offset:0 sc1
buffer_load_dwordx4 v[232:235], v255, s[44:47], s97 offen offset:1024 sc1
buffer_load_dwordx4 v[244:247], v255, s[44:47], s97 offen offset:2048 sc1
buffer_load_dwordx4 v[248:251], v255, s[44:47], s97 offen offset:3072 sc1
s_add_u32 s96, s96, 1
s_lshr_b32 s92, s96, 20
s_waitcnt vmcnt(3)
v_min3_u32 v228, v224, v225, v226
v_min_u32 v228, v227, v228
s_waitcnt vmcnt(2)
v_min3_u32 v228, v232, v233, v228
v_min3_u32 v228, v234, v235, v228
s_waitcnt vmcnt(1)
v_min3_u32 v228, v244, v245, v228
v_min3_u32 v228, v246, v247, v228
s_waitcnt vmcnt(0)
v_min3_u32 v228, v248, v249, v228
v_min3_u32 v228, v250, v251, v228
v_cmp_ne_u32 vcc, 0, v228
v_max_u32 v229, v224, v232
v_max_u32 v230, v244, v248
s_cbranch_scc1 .La5_LT_S3
s_cmp_eq_u64 vcc, -1
s_cbranch_scc0 .La5_LT_S1
.La5_LT_S3:
v_max_u32 v228, v229, v230
v_min_u32 v229, v229, v230
v_mov_b32 v231, v228
v_min_u32 v224, v224, v232
v_min_u32 v244, v244, v248
v_max3_u32 v229, v229, v224, v244
v_max3_u32 v230, v226, v234, v246
v_max3_u32 v229, v229, v230, v250
s_nop 1
v_max_u32_dpp v231, v231, v231 row_shr:1 row_mask:0xf bank_mask:0xf
s_nop 1
v_max_u32_dpp v231, v231, v231 row_shr:2 row_mask:0xf bank_mask:0xf
s_nop 1
v_max_u32_dpp v231, v231, v231 row_shr:4 row_mask:0xf bank_mask:0xf
s_nop 1
v_max_u32_dpp v231, v231, v231 row_shr:8 row_mask:0xf bank_mask:0xf
s_nop 1
v_max_u32_dpp v231, v231, v231 row_bcast:15 row_mask:0xa bank_mask:0xf
s_nop 1
v_max_u32_dpp v231, v231, v231 row_bcast:31 row_mask:0xc bank_mask:0xf
s_nop 0
v_readlane_b32 s82, v231, 63
s_nop 1
v_cmp_eq_u32 vcc, s82, v228
v_mov_b32 v230, s82
v_mov_b32 v224, 0
v_cndmask_b32 v228, v228, v229, vcc
s_nop 1
v_max_u32_dpp v228, v228, v228 row_shr:1 row_mask:0xf bank_mask:0xf
s_nop 1
v_max_u32_dpp v228, v228, v228 row_shr:2 row_mask:0xf bank_mask:0xf
s_nop 1
v_max_u32_dpp v228, v228, v228 row_shr:4 row_mask:0xf bank_mask:0xf
s_nop 1
v_max_u32_dpp v228, v228, v228 row_shr:8 row_mask:0xf bank_mask:0xf
s_nop 1
v_max_u32_dpp v228, v228, v228 row_bcast:15 row_mask:0xa bank_mask:0xf
s_nop 1
v_max_u32_dpp v228, v228, v228 row_bcast:31 row_mask:0xc bank_mask:0xf
s_nop 0
v_readlane_b32 s81, v228, 63
s_nop 1
v_mov_b32 v231, s81
ds_write_b64 v224, v[230:231] offset:64
.La5_LT_S2:
v_mov_b32 v224, 0
s_waitcnt lgkmcnt(0)
s_barrier
ds_read_b64 v[228:229], v224 offset:64
s_waitcnt lgkmcnt(0)
v_readfirstlane_b32 s82, v228
v_readfirstlane_b32 s81, v229
s_nop 0
s_andn2_b32 s84, 0x7ff, s82
s_andn2_b32 s21, 0x7ff, s81
s_cmp_eq_u32 s30, 0
s_cbranch_scc1 .La5_LT_ST
s_cmp_lt_u32 s3, 2
s_cselect_b32 s91, s84, s21
s_cselect_b32 s85, s82, s81
s_lshr_b32 s92, s91, 4
s_lshl_b32 s92, s92, 15
s_and_b32 s93, s91, 15
s_lshl_b32 s93, s93, 6
s_add_u32 s92, s92, s93
s_add_u32 s93, s92, 4194304
s_add_u32 s94, s92, 8388608
s_add_u32 s95, s92, 12582912
buffer_load_dwordx4 v[224:227], v240, s[32:35], s92 offen sc1
buffer_load_dwordx4 v[228:231], v240, s[32:35], s93 offen sc1
buffer_load_dwordx4 v[232:235], v240, s[32:35], s94 offen sc1
buffer_load_dwordx4 v[244:247], v240, s[32:35], s95 offen sc1
s_lshl_b32 s96, s91, 11
s_and_b32 s92, s91, 0xf8
s_lshl4_add_u32 s97, s92, s97
s_lshr_b32 s92, s91, 8
s_lshl4_add_u32 s97, s92, s97
buffer_load_dwordx4 v[248:251], v241, s[68:71], s96 offen
buffer_load_dwordx4 v[220:223], off, s[44:47], s97 sc1
s_waitcnt vmcnt(4)
v_lshlrev_b32 v236, 16, v224
v_and_b32 v237, 0xffff0000, v224
v_lshlrev_b32 v238, 16, v228
v_and_b32 v239, 0xffff0000, v228
v_pk_add_f32 v[236:237], v[236:237], v[238:239]
v_lshlrev_b32 v242, 16, v225
v_and_b32 v243, 0xffff0000, v225
v_lshlrev_b32 v238, 16, v229
v_and_b32 v239, 0xffff0000, v229
v_pk_add_f32 v[242:243], v[242:243], v[238:239]
v_lshlrev_b32 v252, 16, v226
v_and_b32 v253, 0xffff0000, v226
v_lshlrev_b32 v238, 16, v230
v_and_b32 v239, 0xffff0000, v230
v_pk_add_f32 v[252:253], v[252:253], v[238:239]
v_lshlrev_b32 v214, 16, v227
v_and_b32 v215, 0xffff0000, v227
v_lshlrev_b32 v238, 16, v231
v_and_b32 v239, 0xffff0000, v231
v_pk_add_f32 v[214:215], v[214:215], v[238:239]
s_waitcnt vmcnt(2)
v_lshlrev_b32 v224, 16, v232
v_and_b32 v225, 0xffff0000, v232
v_lshlrev_b32 v226, 16, v244
v_and_b32 v227, 0xffff0000, v244
v_pk_add_f32 v[224:225], v[224:225], v[226:227]
v_pk_add_f32 v[236:237], v[236:237], v[224:225]
v_lshlrev_b32 v224, 16, v233
v_and_b32 v225, 0xffff0000, v233
.La5_LW4_2:
s_cbranch_execz .La5_LW4_R0
v_lshlrev_b32 v226, 16, v245
v_and_b32 v227, 0xffff0000, v245
v_pk_add_f32 v[224:225], v[224:225], v[226:227]
v_pk_add_f32 v[242:243], v[242:243], v[224:225]
v_lshlrev_b32 v224, 16, v234
v_and_b32 v225, 0xffff0000, v234
v_lshlrev_b32 v226, 16, v246
v_and_b32 v227, 0xffff0000, v246
v_pk_add_f32 v[224:225], v[224:225], v[226:227]
v_pk_add_f32 v[252:253], v[252:253], v[224:225]
v_lshlrev_b32 v224, 16, v235
v_and_b32 v225, 0xffff0000, v235
v_lshlrev_b32 v226, 16, v247
v_and_b32 v227, 0xffff0000, v247
v_pk_add_f32 v[224:225], v[224:225], v[226:227]
v_pk_add_f32 v[214:215], v[214:215], v[224:225]
s_waitcnt vmcnt(0)
v_cmp_eq_u32 vcc, s85, v220
s_nop 1
v_cndmask_b32 v220, v223, v221, vcc
v_lshlrev_b32 v222, 16, v220
v_rcp_f32 v222, v222
v_and_b32 v220, 0xffff0000, v220
v_mul_f32 v220, v220, v222
v_mov_b32 v223, v222
v_xor_b32 v220, 0x80000000, v220
v_mov_b32 v221, v220
v_lshlrev_b32 v224, 16, v248
v_and_b32 v225, 0xffff0000, v248
v_pk_add_f32 v[236:237], v[236:237], v[224:225]
v_cvt_pk_bf16_f32 v224, v236, v237
v_lshlrev_b32 v226, 16, v224
v_and_b32 v227, 0xffff0000, v224
v_pk_fma_f32 v[226:227], v[226:227], v[222:223], v[220:221]
v_cvt_pk_bf16_f32 v228, v226, v227
v_lshlrev_b32 v224, 16, v249
v_and_b32 v225, 0xffff0000, v249
v_pk_add_f32 v[242:243], v[242:243], v[224:225]
v_cvt_pk_bf16_f32 v224, v242, v243
v_lshlrev_b32 v226, 16, v224
v_and_b32 v227, 0xffff0000, v224
v_pk_fma_f32 v[226:227], v[226:227], v[222:223], v[220:221]
v_cvt_pk_bf16_f32 v229, v226, v227
v_lshlrev_b32 v224, 16, v250
v_and_b32 v225, 0xffff0000, v250
v_pk_add_f32 v[252:253], v[252:253], v[224:225]
v_cvt_pk_bf16_f32 v224, v252, v253
v_lshlrev_b32 v226, 16, v224
v_and_b32 v227, 0xffff0000, v224
v_pk_fma_f32 v[226:227], v[226:227], v[222:223], v[220:221]
v_cvt_pk_bf16_f32 v230, v226, v227
v_lshlrev_b32 v224, 16, v251
v_and_b32 v225, 0xffff0000, v251
v_pk_add_f32 v[214:215], v[214:215], v[224:225]
v_cvt_pk_bf16_f32 v224, v214, v215
v_lshlrev_b32 v226, 16, v224
v_and_b32 v227, 0xffff0000, v224
v_pk_fma_f32 v[226:227], v[226:227], v[222:223], v[220:221]
v_cvt_pk_bf16_f32 v231, v226, v227
ds_write_b128 v208, v[228:231] offset:1024
s_waitcnt lgkmcnt(0)
s_barrier
ds_read_b128 v[224:227], v209 offset:1024
ds_read_b128 v[232:235], v209 offset:1040
ds_read_b128 v[244:247], v209 offset:1056
ds_read_b128 v[248:251], v209 offset:1072
s_waitcnt lgkmcnt(3)
v_mfma_f32_16x16x32_bf16 a[0:3], v[224:227], a[96:99], 0
v_mfma_f32_16x16x32_bf16 a[4:7], v[224:227], a[100:103], 0
v_mfma_f32_16x16x32_bf16 a[8:11], v[224:227], a[104:107], 0
v_mfma_f32_16x16x32_bf16 a[12:15], v[224:227], a[108:111], 0
v_mfma_f32_16x16x32_bf16 a[16:19], v[224:227], a[112:115], 0
v_mfma_f32_16x16x32_bf16 a[20:23], v[224:227], a[116:119], 0
v_mfma_f32_16x16x32_bf16 a[24:27], v[224:227], a[120:123], 0
v_mfma_f32_16x16x32_bf16 a[28:31], v[224:227], a[124:127], 0
s_waitcnt lgkmcnt(2)
v_mfma_f32_16x16x32_bf16 a[0:3], v[232:235], a[128:131], a[0:3]
v_mfma_f32_16x16x32_bf16 a[4:7], v[232:235], a[132:135], a[4:7]
v_mfma_f32_16x16x32_bf16 a[8:11], v[232:235], a[136:139], a[8:11]
v_mfma_f32_16x16x32_bf16 a[12:15], v[232:235], a[140:143], a[12:15]
v_mfma_f32_16x16x32_bf16 a[16:19], v[232:235], a[144:147], a[16:19]
v_mfma_f32_16x16x32_bf16 a[20:23], v[232:235], a[148:151], a[20:23]
v_mfma_f32_16x16x32_bf16 a[24:27], v[232:235], a[152:155], a[24:27]
v_mfma_f32_16x16x32_bf16 a[28:31], v[232:235], a[156:159], a[28:31]
s_waitcnt lgkmcnt(1)
v_mfma_f32_16x16x32_bf16 a[0:3], v[244:247], a[160:163], a[0:3]
v_mfma_f32_16x16x32_bf16 a[4:7], v[244:247], a[164:167], a[4:7]
v_mfma_f32_16x16x32_bf16 a[8:11], v[244:247], a[168:171], a[8:11]
v_mfma_f32_16x16x32_bf16 a[12:15], v[244:247], a[172:175], a[12:15]
v_mfma_f32_16x16x32_bf16 a[16:19], v[244:247], a[176:179], a[16:19]
v_mfma_f32_16x16x32_bf16 a[20:23], v[244:247], a[180:183], a[20:23]
v_mfma_f32_16x16x32_bf16 a[24:27], v[244:247], a[184:187], a[24:27]
v_mfma_f32_16x16x32_bf16 a[28:31], v[244:247], a[188:191], a[28:31]
s_waitcnt lgkmcnt(0)
v_mfma_f32_16x16x32_bf16 a[0:3], v[248:251], a[192:195], a[0:3]
v_mfma_f32_16x16x32_bf16 a[4:7], v[248:251], a[196:199], a[4:7]
v_mfma_f32_16x16x32_bf16 a[8:11], v[248:251], a[200:203], a[8:11]
v_mfma_f32_16x16x32_bf16 a[12:15], v[248:251], a[204:207], a[12:15]
v_mfma_f32_16x16x32_bf16 a[16:19], v[248:251], a[208:211], a[16:19]
v_mfma_f32_16x16x32_bf16 a[20:23], v[248:251], a[212:215], a[20:23]
v_mfma_f32_16x16x32_bf16 a[24:27], v[248:251], a[216:219], a[24:27]
v_mfma_f32_16x16x32_bf16 a[28:31], v[248:251], a[220:223], a[28:31]
s_mov_b64 exec, s[98:99]
ds_write_b64 v210, a[0:1] offset:5120
ds_write_b64 v210, a[4:5] offset:5200
ds_write_b64 v210, a[8:9] offset:5280
ds_write_b64 v210, a[12:13] offset:5360
ds_write_b64 v210, a[16:17] offset:5440
s_nop 3
ds_write_b64 v210, a[20:21] offset:5520
s_nop 3
ds_write_b64 v210, a[24:25] offset:5600
s_nop 3
ds_write_b64 v210, a[28:29] offset:5680
s_mov_b64 exec, -1
s_waitcnt lgkmcnt(0)
s_barrier
ds_read_b128 v[224:227], v211 offset:5120
ds_read_b128 v[232:235], v211 offset:5136
ds_read_b128 v[244:247], v211 offset:5152
ds_read_b128 v[248:251], v211 offset:5168
s_waitcnt lgkmcnt(2)
v_pk_add_f32 v[224:225], v[224:225], v[226:227]
v_pk_add_f32 v[232:233], v[232:233], v[234:235]
s_waitcnt lgkmcnt(0)
v_pk_add_f32 v[244:245], v[244:245], v[246:247]
v_pk_add_f32 v[248:249], v[248:249], v[250:251]
v_pk_add_f32 v[224:225], v[224:225], v[232:233]
v_pk_add_f32 v[244:245], v[244:245], v[248:249]
v_pk_add_f32 v[224:225], v[224:225], v[244:245]
v_cvt_pk_bf16_f32 v224, v224, v225
v_mov_b32 v226, v224
s_nop 1
v_permlane32_swap_b32 v224, v226
s_nop 1
v_lshlrev_b32 v228, 16, v224
v_and_b32 v229, 0xffff0000, v224
v_lshlrev_b32 v232, 16, v226
v_and_b32 v233, 0xffff0000, v226
v_mul_f32 v230, 0xbfb8aa3b, v228
v_mul_f32 v231, 0xbfb8aa3b, v229
v_exp_f32 v230, v230
v_exp_f32 v231, v231
v_add_f32 v230, 1.0, v230
v_add_f32 v231, 1.0, v231
v_rcp_f32 v230, v230
v_rcp_f32 v231, v231
s_nop 0
v_pk_mul_f32 v[228:229], v[228:229], v[230:231]
v_cvt_pk_bf16_f32 v228, v228, v229
v_lshlrev_b32 v230, 16, v228
v_and_b32 v231, 0xffff0000, v228
v_pk_mul_f32 v[230:231], v[230:231], v[232:233]
v_cvt_pk_bf16_f32 v228, v230, v231
ds_write_b16 v212, v228 offset:10240
ds_write_b16_d16_hi v212, v228 offset:10304
s_waitcnt lgkmcnt(0)
ds_read_b128 v[224:227], v216 offset:10240
ds_read_b128 v[232:235], v217 offset:10240
ds_read_b128 v[244:247], v218 offset:10240
ds_read_b128 v[248:251], v219 offset:10240
s_waitcnt lgkmcnt(3)
v_mfma_f32_16x16x32_bf16 v[0:3], v[224:227], v[128:131], 0
v_mfma_f32_16x16x32_bf16 v[4:7], v[224:227], v[132:135], 0
v_mfma_f32_16x16x32_bf16 v[8:11], v[224:227], v[136:139], 0
v_mfma_f32_16x16x32_bf16 v[12:15], v[224:227], v[140:143], 0
s_waitcnt lgkmcnt(2)
v_mfma_f32_16x16x32_bf16 v[0:3], v[232:235], v[144:147], v[0:3]
v_mfma_f32_16x16x32_bf16 v[4:7], v[232:235], v[148:151], v[4:7]
v_mfma_f32_16x16x32_bf16 v[8:11], v[232:235], v[152:155], v[8:11]
v_mfma_f32_16x16x32_bf16 v[12:15], v[232:235], v[156:159], v[12:15]
s_waitcnt lgkmcnt(1)
v_mfma_f32_16x16x32_bf16 v[0:3], v[244:247], v[160:163], v[0:3]
v_mfma_f32_16x16x32_bf16 v[4:7], v[244:247], v[164:167], v[4:7]
v_mfma_f32_16x16x32_bf16 v[8:11], v[244:247], v[168:171], v[8:11]
v_mfma_f32_16x16x32_bf16 v[12:15], v[244:247], v[172:175], v[12:15]
s_waitcnt lgkmcnt(0)
v_mfma_f32_16x16x32_bf16 v[0:3], v[248:251], v[176:179], v[0:3]
v_mfma_f32_16x16x32_bf16 v[4:7], v[248:251], v[180:183], v[4:7]
v_mfma_f32_16x16x32_bf16 v[8:11], v[248:251], v[184:187], v[8:11]
v_mfma_f32_16x16x32_bf16 v[12:15], v[248:251], v[188:191], v[12:15]
s_nop 7
v_or_b32 v16, 1, v0
v_or_b32 v20, 1, v1
v_or_b32 v17, 1, v4
v_or_b32 v21, 1, v5
v_or_b32 v18, 1, v8
v_or_b32 v22, 1, v9
v_or_b32 v19, 1, v12
v_or_b32 v23, 1, v13
buffer_store_dwordx4 v[16:19], v208, s[44:47], s80 offen sc1
buffer_store_dwordx4 v[20:23], v208, s[44:47], s20 offen sc1
; ---- rows that were not routed leave; final sums of the routed rows
.La5_LT_ST:
.La5_LW4_3:
s_cbranch_execz .La5_LW4_R0
s_lshl_b32 s92, s76, 5
s_add_u32 s92, s92, s72
s_lshl2_add_u32 s92, s92, s3
s_lshl_b32 s93, s92, 1
v_bfe_u32 v230, v255, 6, 1
v_add_u32 v231, s93, v230
v_cmp_ne_u32 vcc, s84, v231
v_cmp_ne_u32 s[94:95], s21, v231
v_lshrrev_b32 v232, 7, v255
v_and_b32 v233, 48, v255
v_lshl_add_u32 v232, v232, 6, v233
v_lshl_add_u32 v232, v230, 11, v232
s_lshl_b32 s92, s92, 12
s_and_b64 exec, vcc, s[94:95]
s_cbranch_scc0 .La5_LT_F0
buffer_store_dwordx4 v[192:195], v232, s[56:59], s92 offen offset:0 sc1
buffer_store_dwordx4 v[196:199], v232, s[56:59], s92 offen offset:512 sc1
buffer_store_dwordx4 v[200:203], v232, s[56:59], s92 offen offset:1024 sc1
buffer_store_dwordx4 v[204:207], v232, s[56:59], s92 offen offset:1536 sc1
.La5_LT_F0:
s_mov_b64 exec, -1
s_cmp_eq_u32 s75, 1
s_nop 0
s_cbranch_scc1 .La5_LT_END
s_lshr_b32 s92, s2, 6
s_lshl2_add_u32 s92, s92, s74
s_and_b32 s93, s2, 7
s_lshl3_add_u32 s91, s92, s93
s_cmp_lt_u32 s91, 64
s_cselect_b32 s84, s84, s21
s_cselect_b32 s82, s82, s81
s_bfe_u32 s92, s91, 0x30003
s_and_b32 s93, s91, 3
s_lshl2_add_u32 s96, s92, s93
s_and_b32 s85, s91, 4
s_lshl_b32 s85, s85, 1
v_bfe_u32 v0, v255, 6, 2
v_bfe_u32 v1, v255, 4, 2
v_lshlrev_b32 v1, 1, v1
v_lshl_add_u32 v0, v0, 4, v1
v_add_u32 v0, s85, v0
s_lshl_b32 s85, s96, 10
v_add_u32 v1, s85, v0
s_lshl_b32 s85, s96, 6
v_add_u32 v0, s85, v0
s_lshr_b32 s92, s84, 4
s_lshl_b32 s92, s92, 15
s_and_b32 s93, s84, 15
s_lshl_b32 s93, s93, 6
s_add_u32 s92, s92, s93
s_add_u32 s93, s92, 4194304
s_add_u32 s94, s92, 8388608
s_add_u32 s95, s92, 12582912
buffer_load_ushort v4, v1, s[32:35], s92 offen sc1
buffer_load_ushort v5, v1, s[32:35], s93 offen sc1
buffer_load_ushort v6, v1, s[32:35], s94 offen sc1
buffer_load_ushort v7, v1, s[32:35], s95 offen sc1
s_lshl_b32 s21, s84, 11
s_and_b32 s92, s84, 0xf8
s_lshl4_add_u32 s97, s92, s97
s_lshr_b32 s92, s84, 8
s_lshl4_add_u32 s97, s92, s97
buffer_load_ushort v8, v0, s[68:71], s21 offen
buffer_load_dwordx4 v[12:15], off, s[44:47], s97 sc1
v_lshrrev_b32 v2, 6, v255
v_lshlrev_b32 v2, 13, v2
v_and_b32 v3, 48, v255
v_add_u32 v2, v2, v3
s_lshl_b32 s92, s3, 17
s_lshl_b32 s93, s91, 6
s_add_u32 s92, s92, s93
s_add_u32 s94, s92, 32768
s_cmp_eq_u32 s3, 3
s_cselect_b32 s95, 0, 524288
s_cselect_b32 s96, 0, -1
s_add_u32 s95, s95, s94
s_ashr_i32 s92, s82, 31
s_orn2_b32 s92, 0x80000000, s92
s_xor_b32 s92, s92, s82
s_and_b32 s92, s92, 0xffff0000
v_mov_b32 v9, 0xbfb8aa3b
v_mul_f32 v9, s92, v9
v_exp_f32 v9, v9
v_lshrrev_b32 v3, 8, v255
s_lshl_b32 s92, s3, 4
v_add_f32 v9, 1.0, v9
v_lshl_add_u32 v3, v3, 2, s92
v_rcp_f32 v9, v9
v_and_b32 v10, 48, v255
v_mul_u32_u24 v10, 20, v10
v_add_u32 v3, v3, v10
v_cvt_pk_bf16_f32 v9, v9, v9
v_lshlrev_b32 v9, 16, v9
v_and_b32 v10, 0xf0, v255
v_mul_u32_u24 v10, 5, v10
s_waitcnt vmcnt(0)
v_cmp_eq_u32 vcc, s82, v12
s_nop 1
v_cndmask_b32 v12, v15, v13, vcc
v_lshlrev_b32 v14, 16, v12
v_rcp_f32 v14, v14
v_and_b32 v12, 0xffff0000, v12
v_mul_f32 v12, v12, v14
v_mov_b32 v15, v14
v_xor_b32 v12, 0x80000000, v12
v_mov_b32 v13, v12
v_lshlrev_b32 v4, 16, v4
v_lshlrev_b32 v5, 16, v5
v_lshlrev_b32 v6, 16, v6
v_lshlrev_b32 v7, 16, v7
v_lshlrev_b32 v8, 16, v8
v_add_f32 v4, v4, v5
v_add_f32 v6, v6, v7
v_add_f32 v4, v4, v6
v_add_f32 v4, v4, v8
v_cvt_pk_bf16_f32 v4, v4, v4
v_lshlrev_b32 v4, 16, v4
v_fma_f32 v5, v4, v14, v12
v_cvt_pk_bf16_f32 v5, v5, v5
v_lshlrev_b32 v5, 16, v5
s_sleep 40
s_mov_b32 s93, 0
.La5_LT_Y:
buffer_load_dwordx4 v[16:19], v2, s[44:47], s94 offen sc1
buffer_load_dwordx4 v[20:23], v2, s[44:47], s95 offen sc1
s_add_u32 s93, s93, 1
s_lshr_b32 s92, s93, 20
s_waitcnt vmcnt(0)
v_min3_u32 v24, v16, v17, v18
v_min3_u32 v25, v20, v21, v22
v_min3_u32 v24, v24, v19, v23
v_min_u32 v24, v24, v25
v_cmp_ne_u32 vcc, 0, v24
v_and_b32 v20, s96, v20
v_and_b32 v21, s96, v21
v_and_b32 v22, s96, v22
v_and_b32 v23, s96, v23
s_cbranch_scc1 .La5_LT_Y1
s_cmp_eq_u64 vcc, -1
s_cbranch_scc0 .La5_LT_Y
.La5_LT_Y1:
v_pk_add_f32 v[16:17], v[16:17], v[20:21]
v_pk_add_f32 v[18:19], v[18:19], v[22:23]
s_nop 0
v_add_f32_dpp v16, v16, v16 row_ror:4 row_mask:0xf bank_mask:0xf
v_add_f32_dpp v17, v17, v17 row_ror:4 row_mask:0xf bank_mask:0xf
v_add_f32_dpp v18, v18, v18 row_ror:4 row_mask:0xf bank_mask:0xf
v_add_f32_dpp v19, v19, v19 row_ror:4 row_mask:0xf bank_mask:0xf
v_add_f32_dpp v16, v16, v16 row_ror:8 row_mask:0xf bank_mask:0xf
v_add_f32_dpp v17, v17, v17 row_ror:8 row_mask:0xf bank_mask:0xf
v_add_f32_dpp v18, v18, v18 row_ror:8 row_mask:0xf bank_mask:0xf
v_add_f32_dpp v19, v19, v19 row_ror:8 row_mask:0xf bank_mask:0xf
ds_write_b32 v3, v16 offset:5120
ds_write_b32 v3, v17 offset:5200
ds_write_b32 v3, v18 offset:5280
ds_write_b32 v3, v19 offset:5360
s_waitcnt lgkmcnt(0)
s_barrier
ds_read_b128 v[16:19], v10 offset:5120
ds_read_b128 v[20:23], v10 offset:5136
ds_read_b128 v[24:27], v10 offset:5152
ds_read_b128 v[28:31], v10 offset:5168
s_waitcnt lgkmcnt(2)
v_pk_add_f32 v[16:17], v[16:17], v[18:19]
v_pk_add_f32 v[20:21], v[20:21], v[22:23]
s_waitcnt lgkmcnt(0)
v_pk_add_f32 v[24:25], v[24:25], v[26:27]
v_pk_add_f32 v[28:29], v[28:29], v[30:31]
v_pk_add_f32 v[16:17], v[16:17], v[20:21]
v_pk_add_f32 v[24:25], v[24:25], v[28:29]
v_pk_add_f32 v[16:17], v[16:17], v[24:25]
v_add_f32 v16, v16, v17
v_cvt_pk_bf16_f32 v16, v16, v16
v_lshlrev_b32 v16, 16, v16
v_mul_f32 v16, v16, v9
v_cvt_pk_bf16_f32 v16, v16, v16
v_lshlrev_b32 v16, 16, v16
v_add_f32 v16, v16, v5
v_cvt_pk_bf16_f32 v16, v16, v16
v_lshlrev_b32 v16, 16, v16
v_add_f32 v16, v16, v4
v_cvt_pk_bf16_f32 v16, v16, v16
s_cmp_lg_u32 s3, 0
s_cbranch_scc1 .La5_LT_END
s_mov_b64 exec, 0xffff
buffer_store_short v16, v0, s[56:59], s21 offen
s_mov_b64 exec, -1
.La5_LT_END:
.La5_LW4_4:
s_cbranch_execz .La5_LW4_R0
s_lshl2_add_u32 s92, s2, s3
s_cmp_lg_u32 s92, 992
s_cbranch_scc1 .La5_LT_E1
buffer_store_dwordx2 v[252:253], off, s[64:67], 0 sc1
.La5_LT_E1:
s_branch .La5_LEND
; ---- fallback: the earlier 32 rows against all their keys with the true running maximum (online softmax)
.La5_LA_SLOW0:
s_waitcnt lgkmcnt(0)
s_nop 7
s_nop 7
v_accvgpr_write_b32 a160, v192
v_accvgpr_write_b32 a161, v193
v_accvgpr_write_b32 a162, v194
v_accvgpr_write_b32 a163, v195
v_accvgpr_write_b32 a164, v196
v_accvgpr_write_b32 a165, v197
v_accvgpr_write_b32 a166, v198
v_accvgpr_write_b32 a167, v199
v_accvgpr_write_b32 a168, v200
v_accvgpr_write_b32 a169, v201
v_accvgpr_write_b32 a170, v202
v_accvgpr_write_b32 a171, v203
v_accvgpr_write_b32 a172, v204
v_accvgpr_write_b32 a173, v205
v_accvgpr_write_b32 a174, v206
v_accvgpr_write_b32 a175, v207
v_accvgpr_write_b32 a176, v208
v_accvgpr_write_b32 a177, v209
v_accvgpr_write_b32 a178, v210
v_accvgpr_write_b32 a179, v211
v_accvgpr_write_b32 a180, v212
v_accvgpr_write_b32 a181, v213
v_accvgpr_write_b32 a182, v214
v_accvgpr_write_b32 a183, v215
v_accvgpr_write_b32 a184, v216
v_accvgpr_write_b32 a185, v217
v_accvgpr_write_b32 a186, v218
v_accvgpr_write_b32 a187, v219
v_accvgpr_write_b32 a188, v220
v_accvgpr_write_b32 a189, v221
v_accvgpr_write_b32 a190, v222
v_accvgpr_write_b32 a191, v223
s_lshl_b32 s96, s72, 19
s_add_u32 s96, s96, 20971520
s_add_u32 s92, s26, s96
s_addc_u32 s93, s27, 0
s_and_b32 s93, s93, 0xffff
s_mov_b32 s94, 17301504
s_mov_b32 s95, 0x20000
v_mov_b32 v0, 0
v_mov_b32 v1, 0
v_mov_b32 v2, 0
v_mov_b32 v3, 0
v_mov_b32 v4, 0
v_mov_b32 v5, 0
v_mov_b32 v6, 0
v_mov_b32 v7, 0
v_mov_b32 v8, 0
v_mov_b32 v9, 0
v_mov_b32 v10, 0
v_mov_b32 v11, 0
v_mov_b32 v12, 0
v_mov_b32 v13, 0
v_mov_b32 v14, 0
v_mov_b32 v15, 0
v_mov_b32 v16, 0
v_mov_b32 v17, 0
v_mov_b32 v18, 0
v_mov_b32 v19, 0
v_mov_b32 v20, 0
v_mov_b32 v21, 0
v_mov_b32 v22, 0
v_mov_b32 v23, 0
v_mov_b32 v24, 0
v_mov_b32 v25, 0
v_mov_b32 v26, 0
v_mov_b32 v27, 0
v_mov_b32 v28, 0
v_mov_b32 v29, 0
v_mov_b32 v30, 0
v_mov_b32 v31, 0
v_mov_b32 v32, 0
v_mov_b32 v33, 0
v_mov_b32 v34, 0
v_mov_b32 v35, 0
v_mov_b32 v36, 0
v_mov_b32 v37, 0
v_mov_b32 v38, 0
v_mov_b32 v39, 0
v_mov_b32 v40, 0
v_mov_b32 v41, 0
v_mov_b32 v42, 0
v_mov_b32 v43, 0
v_mov_b32 v44, 0
v_mov_b32 v45, 0
v_mov_b32 v46, 0
v_mov_b32 v47, 0
v_mov_b32 v48, 0
v_mov_b32 v49, 0
v_mov_b32 v50, 0
v_mov_b32 v51, 0
v_mov_b32 v52, 0
v_mov_b32 v53, 0
v_mov_b32 v54, 0
v_mov_b32 v55, 0
v_mov_b32 v56, 0
v_mov_b32 v57, 0
v_mov_b32 v58, 0
v_mov_b32 v59, 0
v_mov_b32 v60, 0
v_mov_b32 v61, 0
v_mov_b32 v62, 0
v_mov_b32 v63, 0
v_mov_b32 v216, 0xff61b1e6
v_mov_b32 v217, 0xff61b1e6
v_mov_b32 v218, 0
v_mov_b32 v219, 0
s_mov_b32 s7, 0
.La5_LA_SLOW0_L:
s_lshl_b32 s96, s7, 13
s_add_u32 s97, s96, 4096
buffer_load_dwordx4 a[128:131], v255, s[92:95], s96 offen offset:0 sc1
buffer_load_dwordx4 a[132:135], v255, s[92:95], s96 offen offset:1024 sc1
buffer_load_dwordx4 a[136:139], v255, s[92:95], s96 offen offset:2048 sc1
buffer_load_dwordx4 a[140:143], v255, s[92:95], s96 offen offset:3072 sc1
buffer_load_dwordx4 a[144:147], v255, s[92:95], s97 offen offset:0 sc1
buffer_load_dwordx4 a[148:151], v255, s[92:95], s97 offen offset:1024 sc1
buffer_load_dwordx4 a[152:155], v255, s[92:95], s97 offen offset:2048 sc1
buffer_load_dwordx4 a[156:159], v255, s[92:95], s97 offen offset:3072 sc1
s_lshl_b32 s96, s76, 3
s_sub_u32 s96, s96, s7
s_add_u32 s96, s96, s3
s_lshl_b32 s96, s96, 5
v_bfe_u32 v220, v255, 4, 4
v_lshrrev_b32 v221, 8, v255
v_lshlrev_b32 v221, 2, v221
v_sub_u32 v220, v220, v221
v_add_u32 v220, s96, v220
v_mov_b32 v222, 0xceabfb8f
s_waitcnt vmcnt(0)
v_mfma_f32_16x16x32_bf16 v[192:195], a[128:131], v[128:131], 0
v_mfma_f32_16x16x32_bf16 v[192:195], a[132:135], v[132:135], v[192:195]
v_mfma_f32_16x16x32_bf16 v[192:195], a[136:139], v[136:139], v[192:195]
v_mfma_f32_16x16x32_bf16 v[192:195], a[140:143], v[140:143], v[192:195]
v_mfma_f32_16x16x32_bf16 v[196:199], a[144:147], v[128:131], 0
v_mfma_f32_16x16x32_bf16 v[196:199], a[148:151], v[132:135], v[196:199]
v_mfma_f32_16x16x32_bf16 v[196:199], a[152:155], v[136:139], v[196:199]
v_mfma_f32_16x16x32_bf16 v[196:199], a[156:159], v[140:143], v[196:199]
v_mfma_f32_16x16x32_bf16 v[200:203], a[128:131], v[144:147], 0
v_mfma_f32_16x16x32_bf16 v[200:203], a[132:135], v[148:151], v[200:203]
v_mfma_f32_16x16x32_bf16 v[200:203], a[136:139], v[152:155], v[200:203]
v_mfma_f32_16x16x32_bf16 v[200:203], a[140:143], v[156:159], v[200:203]
v_mfma_f32_16x16x32_bf16 v[204:207], a[144:147], v[144:147], 0
v_mfma_f32_16x16x32_bf16 v[204:207], a[148:151], v[148:151], v[204:207]
v_mfma_f32_16x16x32_bf16 v[204:207], a[152:155], v[152:155], v[204:207]
v_mfma_f32_16x16x32_bf16 v[204:207], a[156:159], v[156:159], v[204:207]
s_nop 7
s_nop 7
s_lshl_b32 s96, s7, 13
s_add_u32 s96, s96, 16777216
s_add_u32 s97, s96, 4096
buffer_load_dwordx4 a[128:131], v255, s[92:95], s96 offen offset:0 sc1
buffer_load_dwordx4 a[132:135], v255, s[92:95], s96 offen offset:1024 sc1
buffer_load_dwordx4 a[136:139], v255, s[92:95], s96 offen offset:2048 sc1
buffer_load_dwordx4 a[140:143], v255, s[92:95], s96 offen offset:3072 sc1
buffer_load_dwordx4 a[144:147], v255, s[92:95], s97 offen offset:0 sc1
buffer_load_dwordx4 a[148:151], v255, s[92:95], s97 offen offset:1024 sc1
buffer_load_dwordx4 a[152:155], v255, s[92:95], s97 offen offset:2048 sc1
buffer_load_dwordx4 a[156:159], v255, s[92:95], s97 offen offset:3072 sc1
v_cmp_gt_i32 vcc, 0, v220
s_nop 1
v_cndmask_b32 v192, v192, v222, vcc
v_cmp_gt_i32 vcc, 1, v220
s_nop 1
v_cndmask_b32 v193, v193, v222, vcc
v_cmp_gt_i32 vcc, 2, v220
s_nop 1
v_cndmask_b32 v194, v194, v222, vcc
v_cmp_gt_i32 vcc, 3, v220
s_nop 1
v_cndmask_b32 v195, v195, v222, vcc
v_cmp_gt_i32 vcc, 16, v220
s_nop 1
v_cndmask_b32 v196, v196, v222, vcc
v_cmp_gt_i32 vcc, 17, v220
s_nop 1
v_cndmask_b32 v197, v197, v222, vcc
v_cmp_gt_i32 vcc, 18, v220
s_nop 1
v_cndmask_b32 v198, v198, v222, vcc
v_cmp_gt_i32 vcc, 19, v220
s_nop 1
v_cndmask_b32 v199, v199, v222, vcc
v_cmp_gt_i32 vcc, -16, v220
s_nop 1
v_cndmask_b32 v200, v200, v222, vcc
v_cmp_gt_i32 vcc, -15, v220
s_nop 1
v_cndmask_b32 v201, v201, v222, vcc
v_cmp_gt_i32 vcc, -14, v220
s_nop 1
v_cndmask_b32 v202, v202, v222, vcc
v_cmp_gt_i32 vcc, -13, v220
s_nop 1
v_cndmask_b32 v203, v203, v222, vcc
v_cmp_gt_i32 vcc, 0, v220
s_nop 1
v_cndmask_b32 v204, v204, v222, vcc
v_cmp_gt_i32 vcc, 1, v220
s_nop 1
v_cndmask_b32 v205, v205, v222, vcc
v_cmp_gt_i32 vcc, 2, v220
s_nop 1
v_cndmask_b32 v206, v206, v222, vcc
v_cmp_gt_i32 vcc, 3, v220
s_nop 1
v_cndmask_b32 v207, v207, v222, vcc
v_max3_f32 v222, v192, v193, v216
v_max3_f32 v222, v194, v195, v222
v_max3_f32 v222, v196, v197, v222
v_max3_f32 v222, v198, v199, v222
v_mov_b32 v221, v222
s_nop 1
v_permlane16_swap_b32 v221, v222
s_nop 1
v_max_f32 v222, v222, v221
v_mov_b32 v221, v222
s_nop 1
v_permlane32_swap_b32 v221, v222
s_nop 1
v_max_f32 v222, v222, v221
v_sub_f32 v223, v216, v222
v_mov_b32 v216, v222
v_exp_f32 v223, v223
v_sub_f32 v192, v192, v222
v_sub_f32 v193, v193, v222
v_sub_f32 v194, v194, v222
v_sub_f32 v195, v195, v222
v_sub_f32 v196, v196, v222
v_sub_f32 v197, v197, v222
v_sub_f32 v198, v198, v222
v_sub_f32 v199, v199, v222
v_exp_f32 v192, v192
v_exp_f32 v193, v193
v_exp_f32 v194, v194
v_exp_f32 v195, v195
v_exp_f32 v196, v196
v_exp_f32 v197, v197
v_exp_f32 v198, v198
v_exp_f32 v199, v199
v_mul_f32 v218, v218, v223
v_add_f32 v218, v218, v192
v_add_f32 v218, v218, v193
v_add_f32 v218, v218, v194
v_add_f32 v218, v218, v195
v_add_f32 v218, v218, v196
v_add_f32 v218, v218, v197
v_add_f32 v218, v218, v198
v_add_f32 v218, v218, v199
v_cvt_pk_bf16_f32 v208, v192, v193
v_cvt_pk_bf16_f32 v209, v194, v195
v_cvt_pk_bf16_f32 v210, v196, v197
v_cvt_pk_bf16_f32 v211, v198, v199
v_mul_f32 v0, v0, v223
v_mul_f32 v1, v1, v223
v_mul_f32 v2, v2, v223
v_mul_f32 v3, v3, v223
v_mul_f32 v4, v4, v223
v_mul_f32 v5, v5, v223
v_mul_f32 v6, v6, v223
v_mul_f32 v7, v7, v223
v_mul_f32 v8, v8, v223
v_mul_f32 v9, v9, v223
v_mul_f32 v10, v10, v223
v_mul_f32 v11, v11, v223
v_mul_f32 v12, v12, v223
v_mul_f32 v13, v13, v223
v_mul_f32 v14, v14, v223
v_mul_f32 v15, v15, v223
v_mul_f32 v16, v16, v223
v_mul_f32 v17, v17, v223
v_mul_f32 v18, v18, v223
v_mul_f32 v19, v19, v223
v_mul_f32 v20, v20, v223
v_mul_f32 v21, v21, v223
v_mul_f32 v22, v22, v223
v_mul_f32 v23, v23, v223
v_mul_f32 v24, v24, v223
v_mul_f32 v25, v25, v223
v_mul_f32 v26, v26, v223
v_mul_f32 v27, v27, v223
v_mul_f32 v28, v28, v223
v_mul_f32 v29, v29, v223
v_mul_f32 v30, v30, v223
v_mul_f32 v31, v31, v223
v_max3_f32 v222, v200, v201, v217
v_max3_f32 v222, v202, v203, v222
v_max3_f32 v222, v204, v205, v222
v_max3_f32 v222, v206, v207, v222
v_mov_b32 v221, v222
s_nop 1
v_permlane16_swap_b32 v221, v222
s_nop 1
v_max_f32 v222, v222, v221
v_mov_b32 v221, v222
s_nop 1
v_permlane32_swap_b32 v221, v222
s_nop 1
v_max_f32 v222, v222, v221
v_sub_f32 v223, v217, v222
v_mov_b32 v217, v222
v_exp_f32 v223, v223
v_sub_f32 v200, v200, v222
v_sub_f32 v201, v201, v222
v_sub_f32 v202, v202, v222
v_sub_f32 v203, v203, v222
v_sub_f32 v204, v204, v222
v_sub_f32 v205, v205, v222
v_sub_f32 v206, v206, v222
v_sub_f32 v207, v207, v222
v_exp_f32 v200, v200
v_exp_f32 v201, v201
v_exp_f32 v202, v202
v_exp_f32 v203, v203
v_exp_f32 v204, v204
v_exp_f32 v205, v205
v_exp_f32 v206, v206
v_exp_f32 v207, v207
v_mul_f32 v219, v219, v223
v_add_f32 v219, v219, v200
v_add_f32 v219, v219, v201
v_add_f32 v219, v219, v202
v_add_f32 v219, v219, v203
v_add_f32 v219, v219, v204
v_add_f32 v219, v219, v205
v_add_f32 v219, v219, v206
v_add_f32 v219, v219, v207
v_cvt_pk_bf16_f32 v212, v200, v201
v_cvt_pk_bf16_f32 v213, v202, v203
v_cvt_pk_bf16_f32 v214, v204, v205
v_cvt_pk_bf16_f32 v215, v206, v207
v_mul_f32 v32, v32, v223
v_mul_f32 v33, v33, v223
v_mul_f32 v34, v34, v223
v_mul_f32 v35, v35, v223
v_mul_f32 v36, v36, v223
v_mul_f32 v37, v37, v223
v_mul_f32 v38, v38, v223
v_mul_f32 v39, v39, v223
v_mul_f32 v40, v40, v223
v_mul_f32 v41, v41, v223
v_mul_f32 v42, v42, v223
v_mul_f32 v43, v43, v223
v_mul_f32 v44, v44, v223
v_mul_f32 v45, v45, v223
v_mul_f32 v46, v46, v223
v_mul_f32 v47, v47, v223
v_mul_f32 v48, v48, v223
v_mul_f32 v49, v49, v223
v_mul_f32 v50, v50, v223
v_mul_f32 v51, v51, v223
v_mul_f32 v52, v52, v223
v_mul_f32 v53, v53, v223
v_mul_f32 v54, v54, v223
v_mul_f32 v55, v55, v223
v_mul_f32 v56, v56, v223
v_mul_f32 v57, v57, v223
v_mul_f32 v58, v58, v223
v_mul_f32 v59, v59, v223
v_mul_f32 v60, v60, v223
v_mul_f32 v61, v61, v223
v_mul_f32 v62, v62, v223
v_mul_f32_e64 v63, v63, v223
s_waitcnt vmcnt(0)
v_mfma_f32_16x16x32_bf16 v[0:3], a[128:131], v[208:211], v[0:3]
v_mfma_f32_16x16x32_bf16 v[32:35], a[128:131], v[212:215], v[32:35]
v_mfma_f32_16x16x32_bf16 v[4:7], a[132:135], v[208:211], v[4:7]
v_mfma_f32_16x16x32_bf16 v[36:39], a[132:135], v[212:215], v[36:39]
v_mfma_f32_16x16x32_bf16 v[8:11], a[136:139], v[208:211], v[8:11]
v_mfma_f32_16x16x32_bf16 v[40:43], a[136:139], v[212:215], v[40:43]
v_mfma_f32_16x16x32_bf16 v[12:15], a[140:143], v[208:211], v[12:15]
v_mfma_f32_16x16x32_bf16 v[44:47], a[140:143], v[212:215], v[44:47]
v_mfma_f32_16x16x32_bf16 v[16:19], a[144:147], v[208:211], v[16:19]
v_mfma_f32_16x16x32_bf16 v[48:51], a[144:147], v[212:215], v[48:51]
v_mfma_f32_16x16x32_bf16 v[20:23], a[148:151], v[208:211], v[20:23]
v_mfma_f32_16x16x32_bf16 v[52:55], a[148:151], v[212:215], v[52:55]
v_mfma_f32_16x16x32_bf16 v[24:27], a[152:155], v[208:211], v[24:27]
v_mfma_f32_16x16x32_bf16 v[56:59], a[152:155], v[212:215], v[56:59]
v_mfma_f32_16x16x32_bf16 v[28:31], a[156:159], v[208:211], v[28:31]
v_mfma_f32_16x16x32_bf16 v[60:63], a[156:159], v[212:215], v[60:63]
s_nop 7
s_nop 7
s_add_u32 s7, s7, 1
s_lshr_b32 s96, s7, 3
s_cmp_le_u32 s96, s76
s_cbranch_scc1 .La5_LA_SLOW0_L
v_mov_b32 v221, v218
s_nop 1
v_permlane16_swap_b32 v221, v218
s_nop 1
v_add_f32 v218, v218, v221
v_mov_b32 v221, v218
s_nop 1
v_permlane32_swap_b32 v221, v218
s_nop 1
v_add_f32 v218, v218, v221
v_mov_b32 v221, v219
s_nop 1
v_permlane16_swap_b32 v221, v219
s_nop 1
v_add_f32 v219, v219, v221
v_mov_b32 v221, v219
s_nop 1
v_permlane32_swap_b32 v221, v219
s_nop 1
v_add_f32 v219, v219, v221
v_rcp_f32 v242, v218
v_rcp_f32 v243, v219
v_accvgpr_read_b32 v192, a160
v_accvgpr_read_b32 v193, a161
v_accvgpr_read_b32 v194, a162
v_accvgpr_read_b32 v195, a163
v_accvgpr_read_b32 v196, a164
v_accvgpr_read_b32 v197, a165
v_accvgpr_read_b32 v198, a166
v_accvgpr_read_b32 v199, a167
v_accvgpr_read_b32 v200, a168
v_accvgpr_read_b32 v201, a169
v_accvgpr_read_b32 v202, a170
v_accvgpr_read_b32 v203, a171
v_accvgpr_read_b32 v204, a172
v_accvgpr_read_b32 v205, a173
v_accvgpr_read_b32 v206, a174
v_accvgpr_read_b32 v207, a175
v_accvgpr_read_b32 v208, a176
v_accvgpr_read_b32 v209, a177
v_accvgpr_read_b32 v210, a178
v_accvgpr_read_b32 v211, a179
v_accvgpr_read_b32 v212, a180
v_accvgpr_read_b32 v213, a181
v_accvgpr_read_b32 v214, a182
v_accvgpr_read_b32 v215, a183
v_accvgpr_read_b32 v216, a184
v_accvgpr_read_b32 v217, a185
v_accvgpr_read_b32 v218, a186
v_accvgpr_read_b32 v219, a187
v_accvgpr_read_b32 v220, a188
v_accvgpr_read_b32 v221, a189
v_accvgpr_read_b32 v222, a190
v_accvgpr_read_b32 v223, a191
s_mov_b32 s95, 4096
s_mov_b32 s96, 8192
s_mov_b32 s97, 12288
s_mov_b64 vcc, 0
s_nop 1
s_setpc_b64 s[24:25]
; ---- fallback: the later 32 rows
.La5_LA_SLOW1:
s_waitcnt lgkmcnt(0)
s_nop 7
s_nop 7
v_accvgpr_write_b32 a160, v192
v_accvgpr_write_b32 a161, v193
v_accvgpr_write_b32 a162, v194
v_accvgpr_write_b32 a163, v195
v_accvgpr_write_b32 a164, v196
v_accvgpr_write_b32 a165, v197
v_accvgpr_write_b32 a166, v198
v_accvgpr_write_b32 a167, v199
v_accvgpr_write_b32 a168, v200
v_accvgpr_write_b32 a169, v201
v_accvgpr_write_b32 a170, v202
v_accvgpr_write_b32 a171, v203
v_accvgpr_write_b32 a172, v204
v_accvgpr_write_b32 a173, v205
v_accvgpr_write_b32 a174, v206
v_accvgpr_write_b32 a175, v207
v_accvgpr_write_b32 a176, v208
v_accvgpr_write_b32 a177, v209
v_accvgpr_write_b32 a178, v210
v_accvgpr_write_b32 a179, v211
v_accvgpr_write_b32 a180, v212
v_accvgpr_write_b32 a181, v213
v_accvgpr_write_b32 a182, v214
v_accvgpr_write_b32 a183, v215
v_accvgpr_write_b32 a184, v216
v_accvgpr_write_b32 a185, v217
v_accvgpr_write_b32 a186, v218
v_accvgpr_write_b32 a187, v219
v_accvgpr_write_b32 a188, v220
v_accvgpr_write_b32 a189, v221
v_accvgpr_write_b32 a190, v222
v_accvgpr_write_b32 a191, v223
s_lshl_b32 s96, s72, 19
s_add_u32 s96, s96, 20971520
s_add_u32 s92, s26, s96
s_addc_u32 s93, s27, 0
s_and_b32 s93, s93, 0xffff
s_mov_b32 s94, 17301504
s_mov_b32 s95, 0x20000
v_mov_b32 v64, 0
v_mov_b32 v65, 0
v_mov_b32 v66, 0
v_mov_b32 v67, 0
v_mov_b32 v68, 0
v_mov_b32 v69, 0
v_mov_b32 v70, 0
v_mov_b32 v71, 0
v_mov_b32 v72, 0
v_mov_b32 v73, 0
v_mov_b32 v74, 0
v_mov_b32 v75, 0
v_mov_b32 v76, 0
v_mov_b32 v77, 0
v_mov_b32 v78, 0
v_mov_b32 v79, 0
v_mov_b32 v80, 0
v_mov_b32 v81, 0
v_mov_b32 v82, 0
v_mov_b32 v83, 0
v_mov_b32 v84, 0
v_mov_b32 v85, 0
v_mov_b32 v86, 0
v_mov_b32 v87, 0
v_mov_b32 v88, 0
v_mov_b32 v89, 0
v_mov_b32 v90, 0
v_mov_b32 v91, 0
v_mov_b32 v92, 0
v_mov_b32 v93, 0
v_mov_b32 v94, 0
v_mov_b32 v95, 0
v_mov_b32 v96, 0
v_mov_b32 v97, 0
v_mov_b32 v98, 0
v_mov_b32 v99, 0
v_mov_b32 v100, 0
v_mov_b32 v101, 0
v_mov_b32 v102, 0
v_mov_b32 v103, 0
v_mov_b32 v104, 0
v_mov_b32 v105, 0
v_mov_b32 v106, 0
v_mov_b32 v107, 0
v_mov_b32 v108, 0
v_mov_b32 v109, 0
v_mov_b32 v110, 0
v_mov_b32 v111, 0
v_mov_b32 v112, 0
v_mov_b32 v113, 0
v_mov_b32 v114, 0
v_mov_b32 v115, 0
v_mov_b32 v116, 0
v_mov_b32 v117, 0
v_mov_b32 v118, 0
v_mov_b32 v119, 0
v_mov_b32 v120, 0
v_mov_b32 v121, 0
v_mov_b32 v122, 0
v_mov_b32 v123, 0
v_mov_b32 v124, 0
v_mov_b32 v125, 0
v_mov_b32 v126, 0
v_mov_b32 v127, 0
v_mov_b32 v216, 0xff61b1e6
v_mov_b32 v217, 0xff61b1e6
v_mov_b32 v218, 0
v_mov_b32 v219, 0
s_mov_b32 s7, 0
.La5_LA_SLOW1_L:
s_lshl_b32 s96, s7, 13
s_add_u32 s97, s96, 4096
buffer_load_dwordx4 a[128:131], v255, s[92:95], s96 offen offset:0 sc1
buffer_load_dwordx4 a[132:135], v255, s[92:95], s96 offen offset:1024 sc1
buffer_load_dwordx4 a[136:139], v255, s[92:95], s96 offen offset:2048 sc1
buffer_load_dwordx4 a[140:143], v255, s[92:95], s96 offen offset:3072 sc1
buffer_load_dwordx4 a[144:147], v255, s[92:95], s97 offen offset:0 sc1
buffer_load_dwordx4 a[148:151], v255, s[92:95], s97 offen offset:1024 sc1
buffer_load_dwordx4 a[152:155], v255, s[92:95], s97 offen offset:2048 sc1
buffer_load_dwordx4 a[156:159], v255, s[92:95], s97 offen offset:3072 sc1
s_lshl_b32 s96, s76, 3
s_sub_u32 s96, s96, s7
s_sub_u32 s96, s96, s3
s_add_u32 s96, s96, 7
s_lshl_b32 s96, s96, 5
v_bfe_u32 v220, v255, 4, 4
v_lshrrev_b32 v221, 8, v255
v_lshlrev_b32 v221, 2, v221
v_sub_u32 v220, v220, v221
v_add_u32 v220, s96, v220
v_mov_b32 v222, 0xceabfb8f
s_waitcnt vmcnt(0)
v_mfma_f32_16x16x32_bf16 v[192:195], a[128:131], v[160:163], 0
v_mfma_f32_16x16x32_bf16 v[192:195], a[132:135], v[164:167], v[192:195]
v_mfma_f32_16x16x32_bf16 v[192:195], a[136:139], v[168:171], v[192:195]
v_mfma_f32_16x16x32_bf16 v[192:195], a[140:143], v[172:175], v[192:195]
v_mfma_f32_16x16x32_bf16 v[196:199], a[144:147], v[160:163], 0
v_mfma_f32_16x16x32_bf16 v[196:199], a[148:151], v[164:167], v[196:199]
v_mfma_f32_16x16x32_bf16 v[196:199], a[152:155], v[168:171], v[196:199]
v_mfma_f32_16x16x32_bf16 v[196:199], a[156:159], v[172:175], v[196:199]
v_mfma_f32_16x16x32_bf16 v[200:203], a[128:131], v[176:179], 0
v_mfma_f32_16x16x32_bf16 v[200:203], a[132:135], v[180:183], v[200:203]
v_mfma_f32_16x16x32_bf16 v[200:203], a[136:139], v[184:187], v[200:203]
v_mfma_f32_16x16x32_bf16 v[200:203], a[140:143], v[188:191], v[200:203]
v_mfma_f32_16x16x32_bf16 v[204:207], a[144:147], v[176:179], 0
v_mfma_f32_16x16x32_bf16 v[204:207], a[148:151], v[180:183], v[204:207]
v_mfma_f32_16x16x32_bf16 v[204:207], a[152:155], v[184:187], v[204:207]
v_mfma_f32_16x16x32_bf16 v[204:207], a[156:159], v[188:191], v[204:207]
s_nop 7
s_nop 7
s_lshl_b32 s96, s7, 13
s_add_u32 s96, s96, 16777216
s_add_u32 s97, s96, 4096
buffer_load_dwordx4 a[128:131], v255, s[92:95], s96 offen offset:0 sc1
buffer_load_dwordx4 a[132:135], v255, s[92:95], s96 offen offset:1024 sc1
buffer_load_dwordx4 a[136:139], v255, s[92:95], s96 offen offset:2048 sc1
buffer_load_dwordx4 a[140:143], v255, s[92:95], s96 offen offset:3072 sc1
buffer_load_dwordx4 a[144:147], v255, s[92:95], s97 offen offset:0 sc1
buffer_load_dwordx4 a[148:151], v255, s[92:95], s97 offen offset:1024 sc1
buffer_load_dwordx4 a[152:155], v255, s[92:95], s97 offen offset:2048 sc1
buffer_load_dwordx4 a[156:159], v255, s[92:95], s97 offen offset:3072 sc1
v_cmp_gt_i32 vcc, 0, v220
s_nop 1
v_cndmask_b32 v192, v192, v222, vcc
v_cmp_gt_i32 vcc, 1, v220
s_nop 1
v_cndmask_b32 v193, v193, v222, vcc
v_cmp_gt_i32 vcc, 2, v220
s_nop 1
v_cndmask_b32 v194, v194, v222, vcc
v_cmp_gt_i32 vcc, 3, v220
s_nop 1
v_cndmask_b32 v195, v195, v222, vcc
v_cmp_gt_i32 vcc, 16, v220
s_nop 1
v_cndmask_b32 v196, v196, v222, vcc
v_cmp_gt_i32 vcc, 17, v220
s_nop 1
v_cndmask_b32 v197, v197, v222, vcc
v_cmp_gt_i32 vcc, 18, v220
s_nop 1
v_cndmask_b32 v198, v198, v222, vcc
v_cmp_gt_i32 vcc, 19, v220
s_nop 1
v_cndmask_b32 v199, v199, v222, vcc
v_cmp_gt_i32 vcc, -16, v220
s_nop 1
v_cndmask_b32 v200, v200, v222, vcc
v_cmp_gt_i32 vcc, -15, v220
s_nop 1
v_cndmask_b32 v201, v201, v222, vcc
v_cmp_gt_i32 vcc, -14, v220
s_nop 1
v_cndmask_b32 v202, v202, v222, vcc
v_cmp_gt_i32 vcc, -13, v220
s_nop 1
v_cndmask_b32 v203, v203, v222, vcc
v_cmp_gt_i32 vcc, 0, v220
s_nop 1
v_cndmask_b32 v204, v204, v222, vcc
v_cmp_gt_i32 vcc, 1, v220
s_nop 1
v_cndmask_b32 v205, v205, v222, vcc
v_cmp_gt_i32 vcc, 2, v220
s_nop 1
v_cndmask_b32 v206, v206, v222, vcc
v_cmp_gt_i32 vcc, 3, v220
s_nop 1
v_cndmask_b32 v207, v207, v222, vcc
v_max3_f32 v222, v192, v193, v216
v_max3_f32 v222, v194, v195, v222
v_max3_f32 v222, v196, v197, v222
v_max3_f32 v222, v198, v199, v222
v_mov_b32 v221, v222
s_nop 1
v_permlane16_swap_b32 v221, v222
s_nop 1
v_max_f32 v222, v222, v221
v_mov_b32 v221, v222
s_nop 1
v_permlane32_swap_b32 v221, v222
s_nop 1
v_max_f32 v222, v222, v221
v_sub_f32 v223, v216, v222
v_mov_b32 v216, v222
v_exp_f32 v223, v223
v_sub_f32 v192, v192, v222
v_sub_f32 v193, v193, v222
v_sub_f32 v194, v194, v222
v_sub_f32 v195, v195, v222
v_sub_f32 v196, v196, v222
v_sub_f32 v197, v197, v222
v_sub_f32 v198, v198, v222
v_sub_f32 v199, v199, v222
v_exp_f32 v192, v192
v_exp_f32 v193, v193
v_exp_f32 v194, v194
v_exp_f32 v195, v195
v_exp_f32 v196, v196
v_exp_f32 v197, v197
v_exp_f32 v198, v198
v_exp_f32 v199, v199
v_mul_f32 v218, v218, v223
v_add_f32 v218, v218, v192
v_add_f32 v218, v218, v193
v_add_f32 v218, v218, v194
v_add_f32 v218, v218, v195
v_add_f32 v218, v218, v196
v_add_f32 v218, v218, v197
v_add_f32 v218, v218, v198
v_add_f32 v218, v218, v199
v_cvt_pk_bf16_f32 v208, v192, v193
v_cvt_pk_bf16_f32 v209, v194, v195
v_cvt_pk_bf16_f32 v210, v196, v197
v_cvt_pk_bf16_f32 v211, v198, v199
v_mul_f32 v64, v64, v223
v_mul_f32 v65, v65, v223
v_mul_f32 v66, v66, v223
v_mul_f32 v67, v67, v223
v_mul_f32 v68, v68, v223
v_mul_f32 v69, v69, v223
v_mul_f32 v70, v70, v223
v_mul_f32 v71, v71, v223
v_mul_f32 v72, v72, v223
v_mul_f32 v73, v73, v223
v_mul_f32 v74, v74, v223
v_mul_f32 v75, v75, v223
v_mul_f32 v76, v76, v223
v_mul_f32 v77, v77, v223
v_mul_f32 v78, v78, v223
v_mul_f32 v79, v79, v223
v_mul_f32 v80, v80, v223
v_mul_f32 v81, v81, v223
v_mul_f32 v82, v82, v223
v_mul_f32 v83, v83, v223
v_mul_f32 v84, v84, v223
v_mul_f32 v85, v85, v223
v_mul_f32 v86, v86, v223
v_mul_f32 v87, v87, v223
v_mul_f32 v88, v88, v223
v_mul_f32 v89, v89, v223
v_mul_f32 v90, v90, v223
v_mul_f32 v91, v91, v223
v_mul_f32 v92, v92, v223
v_mul_f32 v93, v93, v223
v_mul_f32 v94, v94, v223
v_mul_f32 v95, v95, v223
v_max3_f32 v222, v200, v201, v217
v_max3_f32 v222, v202, v203, v222
v_max3_f32 v222, v204, v205, v222
v_max3_f32 v222, v206, v207, v222
v_mov_b32 v221, v222
s_nop 1
v_permlane16_swap_b32 v221, v222
s_nop 1
v_max_f32 v222, v222, v221
v_mov_b32 v221, v222
s_nop 1
v_permlane32_swap_b32 v221, v222
s_nop 1
v_max_f32 v222, v222, v221
v_sub_f32 v223, v217, v222
v_mov_b32 v217, v222
v_exp_f32 v223, v223
v_sub_f32 v200, v200, v222
v_sub_f32 v201, v201, v222
v_sub_f32 v202, v202, v222
v_sub_f32 v203, v203, v222
v_sub_f32 v204, v204, v222
v_sub_f32 v205, v205, v222
v_sub_f32 v206, v206, v222
v_sub_f32 v207, v207, v222
v_exp_f32 v200, v200
v_exp_f32 v201, v201
v_exp_f32 v202, v202
v_exp_f32 v203, v203
v_exp_f32 v204, v204
v_exp_f32 v205, v205
v_exp_f32 v206, v206
v_exp_f32 v207, v207
v_mul_f32 v219, v219, v223
v_add_f32 v219, v219, v200
v_add_f32 v219, v219, v201
v_add_f32 v219, v219, v202
v_add_f32 v219, v219, v203
v_add_f32 v219, v219, v204
v_add_f32 v219, v219, v205
v_add_f32 v219, v219, v206
v_add_f32 v219, v219, v207
v_cvt_pk_bf16_f32 v212, v200, v201
v_cvt_pk_bf16_f32 v213, v202, v203
v_cvt_pk_bf16_f32 v214, v204, v205
v_cvt_pk_bf16_f32 v215, v206, v207
v_mul_f32 v96, v96, v223
v_mul_f32 v97, v97, v223
v_mul_f32 v98, v98, v223
v_mul_f32 v99, v99, v223
v_mul_f32 v100, v100, v223
v_mul_f32 v101, v101, v223
v_mul_f32 v102, v102, v223
v_mul_f32 v103, v103, v223
v_mul_f32 v104, v104, v223
v_mul_f32 v105, v105, v223
v_mul_f32 v106, v106, v223
v_mul_f32 v107, v107, v223
v_mul_f32 v108, v108, v223
v_mul_f32 v109, v109, v223
v_mul_f32 v110, v110, v223
v_mul_f32 v111, v111, v223
v_mul_f32 v112, v112, v223
v_mul_f32 v113, v113, v223
v_mul_f32 v114, v114, v223
v_mul_f32 v115, v115, v223
v_mul_f32 v116, v116, v223
v_mul_f32 v117, v117, v223
v_mul_f32 v118, v118, v223
v_mul_f32 v119, v119, v223
v_mul_f32 v120, v120, v223
v_mul_f32 v121, v121, v223
v_mul_f32 v122, v122, v223
v_mul_f32 v123, v123, v223
v_mul_f32 v124, v124, v223
v_mul_f32 v125, v125, v223
v_mul_f32 v126, v126, v223
v_mul_f32_e64 v127, v127, v223
s_waitcnt vmcnt(0)
v_mfma_f32_16x16x32_bf16 v[64:67], a[128:131], v[208:211], v[64:67]
v_mfma_f32_16x16x32_bf16 v[96:99], a[128:131], v[212:215], v[96:99]
v_mfma_f32_16x16x32_bf16 v[68:71], a[132:135], v[208:211], v[68:71]
v_mfma_f32_16x16x32_bf16 v[100:103], a[132:135], v[212:215], v[100:103]
v_mfma_f32_16x16x32_bf16 v[72:75], a[136:139], v[208:211], v[72:75]
v_mfma_f32_16x16x32_bf16 v[104:107], a[136:139], v[212:215], v[104:107]
v_mfma_f32_16x16x32_bf16 v[76:79], a[140:143], v[208:211], v[76:79]
v_mfma_f32_16x16x32_bf16 v[108:111], a[140:143], v[212:215], v[108:111]
v_mfma_f32_16x16x32_bf16 v[80:83], a[144:147], v[208:211], v[80:83]
v_mfma_f32_16x16x32_bf16 v[112:115], a[144:147], v[212:215], v[112:115]
v_mfma_f32_16x16x32_bf16 v[84:87], a[148:151], v[208:211], v[84:87]
v_mfma_f32_16x16x32_bf16 v[116:119], a[148:151], v[212:215], v[116:119]
v_mfma_f32_16x16x32_bf16 v[88:91], a[152:155], v[208:211], v[88:91]
v_mfma_f32_16x16x32_bf16 v[120:123], a[152:155], v[212:215], v[120:123]
v_mfma_f32_16x16x32_bf16 v[92:95], a[156:159], v[208:211], v[92:95]
v_mfma_f32_16x16x32_bf16 v[124:127], a[156:159], v[212:215], v[124:127]
s_nop 7
s_nop 7
s_add_u32 s7, s7, 1
s_lshr_b32 s96, s7, 3
s_cmp_le_u32 s96, s76
s_cbranch_scc1 .La5_LA_SLOW1_L
v_mov_b32 v221, v218
s_nop 1
v_permlane16_swap_b32 v221, v218
s_nop 1
v_add_f32 v218, v218, v221
v_mov_b32 v221, v218
s_nop 1
v_permlane32_swap_b32 v221, v218
s_nop 1
v_add_f32 v218, v218, v221
v_mov_b32 v221, v219
s_nop 1
v_permlane16_swap_b32 v221, v219
s_nop 1
v_add_f32 v219, v219, v221
v_mov_b32 v221, v219
s_nop 1
v_permlane32_swap_b32 v221, v219
s_nop 1
v_add_f32 v219, v219, v221
v_rcp_f32 v242, v218
v_rcp_f32 v243, v219
v_accvgpr_read_b32 v192, a160
v_accvgpr_read_b32 v193, a161
v_accvgpr_read_b32 v194, a162
v_accvgpr_read_b32 v195, a163
v_accvgpr_read_b32 v196, a164
v_accvgpr_read_b32 v197, a165
v_accvgpr_read_b32 v198, a166
v_accvgpr_read_b32 v199, a167
v_accvgpr_read_b32 v200, a168
v_accvgpr_read_b32 v201, a169
v_accvgpr_read_b32 v202, a170
v_accvgpr_read_b32 v203, a171
v_accvgpr_read_b32 v204, a172
v_accvgpr_read_b32 v205, a173
v_accvgpr_read_b32 v206, a174
v_accvgpr_read_b32 v207, a175
v_accvgpr_read_b32 v208, a176
v_accvgpr_read_b32 v209, a177
v_accvgpr_read_b32 v210, a178
v_accvgpr_read_b32 v211, a179
v_accvgpr_read_b32 v212, a180
v_accvgpr_read_b32 v213, a181
v_accvgpr_read_b32 v214, a182
v_accvgpr_read_b32 v215, a183
v_accvgpr_read_b32 v216, a184
v_accvgpr_read_b32 v217, a185
v_accvgpr_read_b32 v218, a186
v_accvgpr_read_b32 v219, a187
v_accvgpr_read_b32 v220, a188
v_accvgpr_read_b32 v221, a189
v_accvgpr_read_b32 v222, a190
v_accvgpr_read_b32 v223, a191
s_mov_b32 s95, 4096
s_mov_b32 s96, 8192
s_mov_b32 s97, 12288
s_mov_b64 vcc, 0
s_nop 1
s_setpc_b64 s[24:25]
.La5_LK_XSPIN:
s_mov_b32 s93, 0
.La5_LK_XSPIN_L:
s_add_u32 s93, s93, 1
s_cmp_gt_u32 s93, 2097152
s_cbranch_scc1 .La5_LK_XOK
s_sleep 8
buffer_load_dwordx4 v[72:75], v255, s[64:67], s89 offen offset:1024 sc1
s_waitcnt vmcnt(0)
v_cmp_eq_u64 vcc, v[72:73], v[252:253]
v_cmp_eq_u64 s[96:97], v[74:75], v[252:253]
s_nop 1
s_and_b64 vcc, vcc, s[96:97]
s_cmp_eq_u64 vcc, -1
s_nop 0
s_cbranch_scc0 .La5_LK_XSPIN_L
s_branch .La5_LK_XOK
.La5_LV_XSPIN:
s_mov_b32 s93, 0
.La5_LV_XSPIN_L:
s_add_u32 s93, s93, 1
s_cmp_gt_u32 s93, 2097152
s_nop 0
s_nop 0
s_cbranch_scc1 .La5_LV_XOK
s_sleep 8
buffer_load_dwordx4 v[72:75], v255, s[64:67], s89 offen offset:1024 sc1
s_waitcnt vmcnt(0)
v_cmp_eq_u64 vcc, v[72:73], v[252:253]
v_cmp_eq_u64 s[96:97], v[74:75], v[252:253]
s_nop 1
s_and_b64 vcc, vcc, s[96:97]
s_cmp_eq_u64 vcc, -1
s_cbranch_scc0 .La5_LV_XSPIN_L
s_branch .La5_LV_XOK
.La5_LA_SC1:
s_call_b64 s[24:25], .La5_LA_SLOW0
s_branch .La5_LA_SR1
.La5_LA_SC2:
s_nop 0
s_call_b64 s[24:25], .La5_LA_SLOW1
s_branch .La5_LA_SR2
.La5_LA_PS0:
s_mov_b32 s84, 3
s_mov_b32 s70, 17408
s_waitcnt vmcnt(2)
buffer_load_dwordx4 a[220:223], v255, s[68:71], s94 offen sc1
buffer_load_dwordx4 a[0:3], v255, s[68:71], 0 offen offset:0 sc1
buffer_load_dwordx4 a[4:7], v255, s[68:71], 0 offen offset:1024 sc1
buffer_load_dwordx4 a[8:11], v255, s[68:71], 0 offen offset:2048 sc1
buffer_load_dwordx4 a[12:15], v255, s[68:71], 0 offen offset:3072 sc1
buffer_load_dwordx4 a[16:19], v255, s[68:71], s95 offen offset:0 sc1
buffer_load_dwordx4 a[20:23], v255, s[68:71], s95 offen offset:1024 sc1
buffer_load_dwordx4 a[24:27], v255, s[68:71], s95 offen offset:2048 sc1
s_branch .La5_LA_PS1
.La5_LA_PS2:
s_mov_b32 s84, 3
s_mov_b32 s70, 17408
s_waitcnt vmcnt(0)
buffer_load_dwordx4 a[220:223], v255, s[68:71], s94 offen sc1
buffer_load_dwordx4 a[0:3], v255, s[68:71], 0 offen offset:0 sc1
buffer_load_dwordx4 a[4:7], v255, s[68:71], 0 offen offset:1024 sc1
buffer_load_dwordx4 a[8:11], v255, s[68:71], 0 offen offset:2048 sc1
buffer_load_dwordx4 a[12:15], v255, s[68:71], 0 offen offset:3072 sc1
buffer_load_dwordx4 a[16:19], v255, s[68:71], s95 offen offset:0 sc1
buffer_load_dwordx4 a[20:23], v255, s[68:71], s95 offen offset:1024 sc1
buffer_load_dwordx4 a[24:27], v255, s[68:71], s95 offen offset:2048 sc1
buffer_load_dwordx4 a[28:31], v255, s[68:71], s95 offen offset:3072 sc1
buffer_load_dwordx4 a[32:35], v255, s[68:71], s96 offen offset:0 sc1
buffer_load_dwordx4 a[36:39], v255, s[68:71], s96 offen offset:1024 sc1
buffer_load_dwordx4 a[40:43], v255, s[68:71], s96 offen offset:2048 sc1
buffer_load_dwordx4 a[44:47], v255, s[68:71], s96 offen offset:3072 sc1
buffer_load_dwordx4 a[48:51], v255, s[68:71], s97 offen offset:0 sc1
buffer_load_dwordx4 a[52:55], v255, s[68:71], s97 offen offset:1024 sc1
buffer_load_dwordx4 a[56:59], v255, s[68:71], s97 offen offset:2048 sc1
buffer_load_dwordx4 a[60:63], v255, s[68:71], s97 offen offset:3072 sc1
s_branch .La5_LA_PS3
.La5_LA_PSPIN:
s_mov_b32 s93, 0
.La5_LA_PSPIN_L:
s_add_u32 s93, s93, 1
s_cmp_gt_u32 s93, 2097152
s_cbranch_scc1 .La5_LA_PSOK
s_sleep 8
buffer_load_dword v250, v251, s[64:67], s20 offen sc1
s_waitcnt vmcnt(0)
v_cmp_eq_u32 vcc, v250, v249
s_nop 1
s_and_b32 s92, vcc_lo, 3
s_cmp_eq_u32 s92, 3
s_cbranch_scc0 .La5_LA_PSPIN_L
s_branch .La5_LA_PSOK
.La5_LA_PSOK:
s_mov_b32 s70, 17408
buffer_load_dwordx4 a[220:223], v255, s[68:71], s94 offen sc1
buffer_load_dwordx4 a[0:3], v255, s[68:71], 0 offen offset:0 sc1
buffer_load_dwordx4 a[4:7], v255, s[68:71], 0 offen offset:1024 sc1
buffer_load_dwordx4 a[8:11], v255, s[68:71], 0 offen offset:2048 sc1
buffer_load_dwordx4 a[12:15], v255, s[68:71], 0 offen offset:3072 sc1
buffer_load_dwordx4 a[16:19], v255, s[68:71], s95 offen offset:0 sc1
buffer_load_dwordx4 a[20:23], v255, s[68:71], s95 offen offset:1024 sc1
buffer_load_dwordx4 a[24:27], v255, s[68:71], s95 offen offset:2048 sc1
buffer_load_dwordx4 a[28:31], v255, s[68:71], s95 offen offset:3072 sc1
buffer_load_dwordx4 a[32:35], v255, s[68:71], s96 offen offset:0 sc1
buffer_load_dwordx4 a[36:39], v255, s[68:71], s96 offen offset:1024 sc1
buffer_load_dwordx4 a[40:43], v255, s[68:71], s96 offen offset:2048 sc1
buffer_load_dwordx4 a[44:47], v255, s[68:71], s96 offen offset:3072 sc1
buffer_load_dwordx4 a[48:51], v255, s[68:71], s97 offen offset:0 sc1
buffer_load_dwordx4 a[52:55], v255, s[68:71], s97 offen offset:1024 sc1
buffer_load_dwordx4 a[56:59], v255, s[68:71], s97 offen offset:2048 sc1
buffer_load_dwordx4 a[60:63], v255, s[68:71], s97 offen offset:3072 sc1
s_branch .La5_LA_DC1
.La5_LO_N1:
.La5_LW5_3:
s_cbranch_execz .La5_LW5_R0
s_mov_b32 s93, 0
.La5_LO_N1_L:
s_add_u32 s93, s93, 1
s_cmp_gt_u32 s93, 2097152
s_nop 0
s_cbranch_scc1 .La5_LO_K1
s_sleep 8
buffer_load_dwordx2 v[250:251], v248, s[64:67], s5 offen sc1
s_waitcnt vmcnt(0)
v_cmp_eq_u64 vcc, v[250:251], v[252:253]
s_nop 1
s_bitcmp1_b32 vcc_lo, 1
s_cbranch_scc0 .La5_LO_N1_L
s_branch .La5_LO_K1
.La5_LO_N2:
s_mov_b32 s93, 0
.La5_LO_N2_L:
s_add_u32 s93, s93, 1
s_cmp_gt_u32 s93, 2097152
s_nop 0
s_nop 0
s_cbranch_scc1 .La5_LO_K2
s_sleep 8
buffer_load_dwordx2 v[250:251], v248, s[64:67], s5 offen sc1
s_waitcnt vmcnt(0)
v_cmp_eq_u64 vcc, v[250:251], v[252:253]
s_nop 1
s_bitcmp1_b32 vcc_lo, 2
s_cbranch_scc0 .La5_LO_N2_L
s_branch .La5_LO_K2
.La5_LO_N3:
s_mov_b32 s93, 0
.La5_LO_N3_L:
s_add_u32 s93, s93, 1
s_cmp_gt_u32 s93, 2097152
s_nop 0
s_nop 0
s_cbranch_scc1 .La5_LO_K3
s_sleep 8
buffer_load_dwordx2 v[250:251], v248, s[64:67], s5 offen sc1
s_waitcnt vmcnt(0)
v_cmp_eq_u64 vcc, v[250:251], v[252:253]
s_nop 1
s_bitcmp1_b32 vcc_lo, 3
s_cbranch_scc0 .La5_LO_N3_L
s_branch .La5_LO_K3
.La5_LO_N4:
s_mov_b32 s93, 0
.La5_LO_N4_L:
s_add_u32 s93, s93, 1
s_cmp_gt_u32 s93, 2097152
s_nop 0
s_nop 0
s_cbranch_scc1 .La5_LO_K4
s_sleep 8
buffer_load_dwordx2 v[250:251], v248, s[64:67], s5 offen sc1
s_waitcnt vmcnt(0)
v_cmp_eq_u64 vcc, v[250:251], v[252:253]
s_nop 1
s_bitcmp1_b32 vcc_lo, 4
s_cbranch_scc0 .La5_LO_N4_L
s_branch .La5_LO_K4
.La5_LO_N5:
s_mov_b32 s93, 0
.La5_LO_N5_L:
s_add_u32 s93, s93, 1
s_cmp_gt_u32 s93, 2097152
s_nop 0
s_nop 0
s_cbranch_scc1 .La5_LO_K5
s_sleep 8
buffer_load_dwordx2 v[250:251], v248, s[64:67], s5 offen sc1
s_waitcnt vmcnt(0)
v_cmp_eq_u64 vcc, v[250:251], v[252:253]
s_nop 1
s_bitcmp1_b32 vcc_lo, 5
s_cbranch_scc0 .La5_LO_N5_L
s_branch .La5_LO_K5
.La5_LO_N6:
s_mov_b32 s93, 0
.La5_LO_N6_L:
s_add_u32 s93, s93, 1
s_cmp_gt_u32 s93, 2097152
s_nop 0
s_nop 0
s_cbranch_scc1 .La5_LO_K6
s_sleep 8
buffer_load_dwordx2 v[250:251], v248, s[64:67], s5 offen sc1
s_waitcnt vmcnt(0)
v_cmp_eq_u64 vcc, v[250:251], v[252:253]
s_nop 1
s_bitcmp1_b32 vcc_lo, 6
s_cbranch_scc0 .La5_LO_N6_L
s_branch .La5_LO_K6
.La5_LO_N7:
s_mov_b32 s93, 0
.La5_LO_N7_L:
s_add_u32 s93, s93, 1
s_cmp_gt_u32 s93, 2097152
s_nop 0
s_nop 0
s_cbranch_scc1 .La5_LO_K7
s_sleep 8
buffer_load_dwordx2 v[250:251], v248, s[64:67], s5 offen sc1
s_waitcnt vmcnt(0)
v_cmp_eq_u64 vcc, v[250:251], v[252:253]
s_nop 1
s_bitcmp1_b32 vcc_lo, 7
s_cbranch_scc0 .La5_LO_N7_L
s_branch .La5_LO_K7
.La5_LA_SC3:
s_call_b64 s[24:25], .La5_LA_SLOW0
v_accvgpr_write_b32 a0, 0
v_accvgpr_write_b32 a1, 0
v_accvgpr_write_b32 a2, 0
v_accvgpr_write_b32 a3, 0
v_accvgpr_write_b32 a4, 0
v_accvgpr_write_b32 a5, 0
v_accvgpr_write_b32 a6, 0
v_accvgpr_write_b32 a7, 0
v_accvgpr_write_b32 a8, 0
v_accvgpr_write_b32 a9, 0
v_accvgpr_write_b32 a10, 0
v_accvgpr_write_b32 a11, 0
v_accvgpr_write_b32 a12, 0
v_accvgpr_write_b32 a13, 0
v_accvgpr_write_b32 a14, 0
v_accvgpr_write_b32 a15, 0
v_accvgpr_write_b32 a16, 0
v_accvgpr_write_b32 a17, 0
v_accvgpr_write_b32 a18, 0
v_accvgpr_write_b32 a19, 0
v_accvgpr_write_b32 a20, 0
v_accvgpr_write_b32 a21, 0
v_accvgpr_write_b32 a22, 0
v_accvgpr_write_b32 a23, 0
v_accvgpr_write_b32 a24, 0
v_accvgpr_write_b32 a25, 0
v_accvgpr_write_b32 a26, 0
v_accvgpr_write_b32 a27, 0
v_accvgpr_write_b32 a28, 0
v_accvgpr_write_b32 a29, 0
v_accvgpr_write_b32 a30, 0
v_accvgpr_write_b32 a31, 0
s_nop 1
v_mov_b32 v250, v242
v_mov_b32 v249, v243
v_xor_b32 v242, 64, v240
v_xor_b32 v243, 96, v240
s_mov_b32 s93, 0
buffer_load_dwordx4 a[128:131], v254, s[40:43], s93 offen
s_mov_b32 s93, 7168
buffer_load_dwordx4 a[132:135], v254, s[40:43], s93 offen
s_mov_b32 s93, 14336
buffer_load_dwordx4 a[136:139], v254, s[40:43], s93 offen
s_mov_b32 s93, 21504
buffer_load_dwordx4 a[140:143], v254, s[40:43], s93 offen
s_mov_b32 s93, 28672
buffer_load_dwordx4 a[144:147], v254, s[40:43], s93 offen
s_mov_b32 s93, 35840
buffer_load_dwordx4 a[148:151], v254, s[40:43], s93 offen
s_mov_b32 s93, 43008
buffer_load_dwordx4 a[152:155], v254, s[40:43], s93 offen
s_mov_b32 s93, 50176
buffer_load_dwordx4 a[156:159], v254, s[40:43], s93 offen
s_mov_b32 s93, 57344
buffer_load_dwordx4 a[160:163], v254, s[40:43], s93 offen
s_mov_b32 s93, 64512
buffer_load_dwordx4 a[164:167], v254, s[40:43], s93 offen
s_mov_b32 s93, 71680
buffer_load_dwordx4 a[168:171], v254, s[40:43], s93 offen
s_mov_b32 s93, 78848
buffer_load_dwordx4 a[172:175], v254, s[40:43], s93 offen
s_mov_b32 s93, 86016
buffer_load_dwordx4 a[176:179], v254, s[40:43], s93 offen
s_mov_b32 s93, 93184
buffer_load_dwordx4 a[180:183], v254, s[40:43], s93 offen
s_mov_b32 s93, 100352
buffer_load_dwordx4 a[184:187], v254, s[40:43], s93 offen
s_mov_b32 s93, 107520
buffer_load_dwordx4 a[188:191], v254, s[40:43], s93 offen
s_branch .La5_LA_SR3
.La5_LA_SC4:
s_call_b64 s[24:25], .La5_LA_SLOW1
v_accvgpr_write_b32 a32, 0
v_accvgpr_write_b32 a33, 0
v_accvgpr_write_b32 a34, 0
v_accvgpr_write_b32 a35, 0
v_accvgpr_write_b32 a36, 0
v_accvgpr_write_b32 a37, 0
v_accvgpr_write_b32 a38, 0
v_accvgpr_write_b32 a39, 0
v_accvgpr_write_b32 a40, 0
v_accvgpr_write_b32 a41, 0
v_accvgpr_write_b32 a42, 0
v_accvgpr_write_b32 a43, 0
v_accvgpr_write_b32 a44, 0
v_accvgpr_write_b32 a45, 0
v_accvgpr_write_b32 a46, 0
v_accvgpr_write_b32 a47, 0
v_accvgpr_write_b32 a48, 0
v_accvgpr_write_b32 a49, 0
v_accvgpr_write_b32 a50, 0
v_accvgpr_write_b32 a51, 0
v_accvgpr_write_b32 a52, 0
v_accvgpr_write_b32 a53, 0
v_accvgpr_write_b32 a54, 0
v_accvgpr_write_b32 a55, 0
v_accvgpr_write_b32 a56, 0
v_accvgpr_write_b32 a57, 0
v_accvgpr_write_b32 a58, 0
v_accvgpr_write_b32 a59, 0
v_accvgpr_write_b32 a60, 0
v_accvgpr_write_b32 a61, 0
v_accvgpr_write_b32 a62, 0
v_accvgpr_write_b32 a63, 0
s_nop 1
v_mov_b32 v250, v242
v_mov_b32 v249, v243
v_xor_b32 v242, 64, v240
v_xor_b32 v243, 96, v240
s_mov_b32 s93, 0
buffer_load_dwordx4 a[128:131], v254, s[40:43], s93 offen
s_mov_b32 s93, 7168
buffer_load_dwordx4 a[132:135], v254, s[40:43], s93 offen
s_mov_b32 s93, 14336
buffer_load_dwordx4 a[136:139], v254, s[40:43], s93 offen
s_mov_b32 s93, 21504
buffer_load_dwordx4 a[140:143], v254, s[40:43], s93 offen
s_mov_b32 s93, 28672
buffer_load_dwordx4 a[144:147], v254, s[40:43], s93 offen
s_mov_b32 s93, 35840
buffer_load_dwordx4 a[148:151], v254, s[40:43], s93 offen
s_mov_b32 s93, 43008
buffer_load_dwordx4 a[152:155], v254, s[40:43], s93 offen
s_mov_b32 s93, 50176
buffer_load_dwordx4 a[156:159], v254, s[40:43], s93 offen
s_mov_b32 s93, 57344
buffer_load_dwordx4 a[160:163], v254, s[40:43], s93 offen
s_mov_b32 s93, 64512
buffer_load_dwordx4 a[164:167], v254, s[40:43], s93 offen
s_mov_b32 s93, 71680
buffer_load_dwordx4 a[168:171], v254, s[40:43], s93 offen
s_mov_b32 s93, 78848
buffer_load_dwordx4 a[172:175], v254, s[40:43], s93 offen
s_mov_b32 s93, 86016
buffer_load_dwordx4 a[176:179], v254, s[40:43], s93 offen
s_mov_b32 s93, 93184
buffer_load_dwordx4 a[180:183], v254, s[40:43], s93 offen
s_mov_b32 s93, 100352
buffer_load_dwordx4 a[184:187], v254, s[40:43], s93 offen
s_mov_b32 s93, 107520
buffer_load_dwordx4 a[188:191], v254, s[40:43], s93 offen
s_branch .La5_LA_SR4
.La5_LO_W:
s_mov_b32 s93, 114688
buffer_load_dwordx4 a[192:195], v228, s[40:43], s93 offen
s_mov_b32 s93, 121856
buffer_load_dwordx4 a[196:199], v228, s[40:43], s93 offen
s_mov_b32 s93, 129024
buffer_load_dwordx4 a[200:203], v228, s[40:43], s93 offen
s_mov_b32 s93, 136192
buffer_load_dwordx4 a[204:207], v228, s[40:43], s93 offen
s_mov_b32 s93, 143360
buffer_load_dwordx4 a[208:211], v228, s[40:43], s93 offen
s_mov_b32 s93, 150528
buffer_load_dwordx4 a[212:215], v228, s[40:43], s93 offen
s_mov_b32 s93, 157696
buffer_load_dwordx4 a[216:219], v228, s[40:43], s93 offen
s_mov_b32 s93, 164864
buffer_load_dwordx4 a[220:223], v228, s[40:43], s93 offen
s_mov_b32 s93, 172032
buffer_load_dwordx4 a[224:227], v228, s[40:43], s93 offen
s_mov_b32 s93, 179200
buffer_load_dwordx4 a[228:231], v228, s[40:43], s93 offen
s_mov_b32 s93, 186368
buffer_load_dwordx4 a[232:235], v228, s[40:43], s93 offen
s_mov_b32 s93, 193536
buffer_load_dwordx4 a[236:239], v228, s[40:43], s93 offen
s_mov_b32 s93, 200704
buffer_load_dwordx4 a[240:243], v228, s[40:43], s93 offen
s_mov_b32 s93, 207872
buffer_load_dwordx4 a[244:247], v228, s[40:43], s93 offen
s_mov_b32 s93, 215040
buffer_load_dwordx4 a[248:251], v228, s[40:43], s93 offen
s_mov_b32 s93, 222208
buffer_load_dwordx4 a[252:255], v228, s[40:43], s93 offen
s_branch .La5_LO_START
.La5_LT_VAR:
v_mov_b32 v230, 0
v_lshlrev_b32 v232, 16, v96
v_and_b32 v233, 0xffff0000, v96
v_sub_f32 v232, v232, v244
v_sub_f32 v233, v233, v244
v_fmac_f32 v230, v232, v232
v_fmac_f32 v230, v233, v233
v_lshlrev_b32 v232, 16, v97
v_and_b32 v233, 0xffff0000, v97
v_sub_f32 v232, v232, v244
v_sub_f32 v233, v233, v244
v_fmac_f32 v230, v232, v232
v_fmac_f32 v230, v233, v233
v_lshlrev_b32 v232, 16, v98
v_and_b32 v233, 0xffff0000, v98
v_sub_f32 v232, v232, v244
v_sub_f32 v233, v233, v244
v_fmac_f32 v230, v232, v232
v_fmac_f32 v230, v233, v233
v_lshlrev_b32 v232, 16, v99
v_and_b32 v233, 0xffff0000, v99
v_sub_f32 v232, v232, v244
v_sub_f32 v233, v233, v244
v_fmac_f32 v230, v232, v232
v_fmac_f32 v230, v233, v233
v_lshlrev_b32 v232, 16, v100
v_and_b32 v233, 0xffff0000, v100
v_sub_f32 v232, v232, v244
v_sub_f32 v233, v233, v244
v_fmac_f32 v230, v232, v232
v_fmac_f32 v230, v233, v233
v_lshlrev_b32 v232, 16, v101
v_and_b32 v233, 0xffff0000, v101
v_sub_f32 v232, v232, v244
v_sub_f32 v233, v233, v244
v_fmac_f32 v230, v232, v232
v_fmac_f32 v230, v233, v233
v_lshlrev_b32 v232, 16, v102
v_and_b32 v233, 0xffff0000, v102
v_sub_f32 v232, v232, v244
v_sub_f32 v233, v233, v244
v_fmac_f32 v230, v232, v232
v_fmac_f32 v230, v233, v233
v_lshlrev_b32 v232, 16, v103
v_and_b32 v233, 0xffff0000, v103
v_sub_f32 v232, v232, v244
v_sub_f32 v233, v233, v244
v_fmac_f32 v230, v232, v232
v_fmac_f32 v230, v233, v233
v_lshlrev_b32 v232, 16, v104
v_and_b32 v233, 0xffff0000, v104
v_sub_f32 v232, v232, v244
v_sub_f32 v233, v233, v244
v_fmac_f32 v230, v232, v232
v_fmac_f32 v230, v233, v233
v_lshlrev_b32 v232, 16, v105
v_and_b32 v233, 0xffff0000, v105
v_sub_f32 v232, v232, v244
v_sub_f32 v233, v233, v244
v_fmac_f32 v230, v232, v232
v_fmac_f32 v230, v233, v233
v_lshlrev_b32 v232, 16, v106
v_and_b32 v233, 0xffff0000, v106
v_sub_f32 v232, v232, v244
v_sub_f32 v233, v233, v244
v_fmac_f32 v230, v232, v232
v_fmac_f32 v230, v233, v233
v_lshlrev_b32 v232, 16, v107
v_and_b32 v233, 0xffff0000, v107
v_sub_f32 v232, v232, v244
v_sub_f32 v233, v233, v244
v_fmac_f32 v230, v232, v232
v_fmac_f32 v230, v233, v233
v_lshlrev_b32 v232, 16, v108
v_and_b32 v233, 0xffff0000, v108
v_sub_f32 v232, v232, v244
v_sub_f32 v233, v233, v244
v_fmac_f32 v230, v232, v232
v_fmac_f32 v230, v233, v233
v_lshlrev_b32 v232, 16, v109
v_and_b32 v233, 0xffff0000, v109
v_sub_f32 v232, v232, v244
v_sub_f32 v233, v233, v244
v_fmac_f32 v230, v232, v232
v_fmac_f32 v230, v233, v233
v_lshlrev_b32 v232, 16, v110
v_and_b32 v233, 0xffff0000, v110
v_sub_f32 v232, v232, v244
v_sub_f32 v233, v233, v244
v_fmac_f32 v230, v232, v232
v_fmac_f32 v230, v233, v233
v_lshlrev_b32 v232, 16, v111
v_and_b32 v233, 0xffff0000, v111
v_sub_f32 v232, v232, v244
v_sub_f32 v233, v233, v244
v_fmac_f32 v230, v232, v232
v_fmac_f32 v230, v233, v233
s_nop 1
v_add_f32_dpp v230, v230, v230 quad_perm:[1,0,3,2] row_mask:0xf bank_mask:0xf
s_nop 1
v_add_f32_dpp v230, v230, v230 quad_perm:[2,3,0,1] row_mask:0xf bank_mask:0xf
s_nop 1
v_add_f32_dpp v230, v230, v230 row_ror:8 row_mask:0xf bank_mask:0xf
v_mov_b32 v231, v230
s_nop 1
v_permlane16_swap_b32 v231, v230
s_nop 1
v_add_f32 v230, v230, v231
v_mov_b32 v231, v230
s_nop 1
v_permlane32_swap_b32 v231, v230
s_nop 1
v_add_f32 v230, v230, v231
v_mul_f32 v230, 0x3a800000, v230
s_branch .La5_LT_VOK
.La5_LA_KSPIN:
s_mov_b32 s93, 0
.La5_LA_KSPIN_L:
s_add_u32 s93, s93, 1
s_cmp_gt_u32 s93, 2097152
s_cbranch_scc1 .La5_LA_KOK
s_sleep 8
buffer_load_dword v250, v251, s[64:67], s20 offen sc1
s_waitcnt vmcnt(0)
v_cmp_eq_u32 vcc, v250, v249
s_nop 1
s_or_b32 vcc_hi, vcc_hi, s79
s_cmp_eq_u64 vcc, -1
s_cbranch_scc0 .La5_LA_KSPIN_L
s_branch .La5_LA_KOK
.La5_LA_QSPIN:
s_mov_b32 s93, 0
.La5_LA_QSPIN_L:
s_add_u32 s93, s93, 1
s_cmp_gt_u32 s93, 2097152
s_cbranch_scc1 .La5_LA_QOK
s_sleep 8
buffer_load_dword v250, v251, s[64:67], s20 offen sc1
s_waitcnt vmcnt(0)
v_cmp_eq_u32 vcc, v250, v249
s_nop 1
s_and_b32 s92, vcc_lo, 3
s_cmp_eq_u32 s92, 3
s_cbranch_scc0 .La5_LA_QSPIN_L
s_branch .La5_LA_QOK
.La5_LEND:
.La5_LCODEEND:
)ASM"
        :: "{s[0:1]}"(__builtin_amdgcn_kernarg_segment_ptr()), "{s2}"(blockIdx.x), "{s3}"(__builtin_amdgcn_readfirstlane(threadIdx.x >> 6)), "{s[4:5]}"(DBGP)
        : "v0","v1","v2","v3","v4","v5","v6","v7","v8","v9","v10","v11","v12","v13","v14","v15","v16","v17","v18","v19","v20","v21","v22","v23","v24","v25","v26","v27","v28","v29","v30","v31","v32","v33","v34","v35","v36","v37","v38","v39","v40","v41","v42","v43","v44","v45","v46","v47","v48","v49","v50","v51","v52","v53","v54","v55","v56","v57","v58","v59","v60","v61","v62","v63","v64","v65","v66","v67","v68","v69","v70","v71","v72","v73","v74","v75","v76","v77","v78","v79","v80","v81","v82","v83","v84","v85","v86","v87","v88","v89","v90","v91","v92","v93","v94","v95","v96","v97","v98","v99","v100","v101","v102","v103","v104","v105","v106","v107","v108","v109","v110","v111","v112","v113","v114","v115","v116","v117","v118","v119","v120","v121","v122","v123","v124","v125","v126","v127","v128","v129","v130","v131","v132","v133","v134","v135","v136","v137","v138","v139","v140","v141","v142","v143","v144","v145","v146","v147","v148","v149","v150","v151","v152","v153","v154","v155","v156","v157","v158","v159","v160","v161","v162","v163","v164","v165","v166","v167","v168","v169","v170","v171","v172","v173","v174","v175","v176","v177","v178","v179","v180","v181","v182","v183","v184","v185","v186","v187","v188","v189","v190","v191","v192","v193","v194","v195","v196","v197","v198","v199","v200","v201","v202","v203","v204","v205","v206","v207","v208","v209","v210","v211","v212","v213","v214","v215","v216","v217","v218","v219","v220","v221","v222","v223","v224","v225","v226","v227","v228","v229","v230","v231","v232","v233","v234","v235","v236","v237","v238","v239","v240","v241","v242","v243","v244","v245","v246","v247","v248","v249","v250","v251","v252","v253","v254","v255","a0","a1","a2","a3","a4","a5","a6","a7","a8","a9","a10","a11","a12","a13","a14","a15","a16","a17","a18","a19","a20","a21","a22","a23","a24","a25","a26","a27","a28","a29","a30","a31","a32","a33","a34","a35","a36","a37","a38","a39","a40","a41","a42","a43","a44","a45","a46","a47","a48","a49","a50","a51","a52","a53","a54","a55","a56","a57","a58","a59","a60","a61","a62","a63","a64","a65","a66","a67","a68","a69","a70","a71","a72","a73","a74","a75","a76","a77","a78","a79","a80","a81","a82","a83","a84","a85","a86","a87","a88","a89","a90","a91","a92","a93","a94","a95","a96","a97","a98","a99","a100","a101","a102","a103","a104","a105","a106","a107","a108","a109","a110","a111","a112","a113","a114","a115","a116","a117","a118","a119","a120","a121","a122","a123","a124","a125","a126","a127","a128","a129","a130","a131","a132","a133","a134","a135","a136","a137","a138","a139","a140","a141","a142","a143","a144","a145","a146","a147","a148","a149","a150","a151","a152","a153","a154","a155","a156","a157","a158","a159","a160","a161","a162","a163","a164","a165","a166","a167","a168","a169","a170","a171","a172","a173","a174","a175","a176","a177","a178","a179","a180","a181","a182","a183","a184","a185","a186","a187","a188","a189","a190","a191","a192","a193","a194","a195","a196","a197","a198","a199","a200","a201","a202","a203","a204","a205","a206","a207","a208","a209","a210","a211","a212","a213","a214","a215","a216","a217","a218","a219","a220","a221","a222","a223","a224","a225","a226","a227","a228","a229","a230","a231","a232","a233","a234","a235","a236","a237","a238","a239","a240","a241","a242","a243","a244","a245","a246","a247","a248","a249","a250","a251","a252","a253","a254","a255","s6","s7","s8","s9","s10","s11","s12","s13","s14","s15","s16","s17","s18","s19","s20","s21","s22","s23","s24","s25","s26","s27","s28","s29","s30","s31","s32","s33","s34","s35","s36","s37","s38","s39","s40","s41","s42","s43","s44","s45","s46","s47","s48","s49","s50","s51","s52","s53","s54","s55","s56","s57","s58","s59","s60","s61","s62","s63","s64","s65","s66","s67","s68","s69","s70","s71","s72","s73","s74","s75","s76","s77","s78","s79","s80","s81","s82","s83","s84","s85","s86","s87","s88","s89","s90","s91","s92","s93","s94","s95","s96","s97","s98","s99","s100","s101","vcc","scc","m0","memory");
}

extern "C" void kernel_launch(void* const* d_in, const int* in_sizes, int n_in, void* d_out, int out_size, void* d_ws, size_t ws_size,
                              hipStream_t stream) {
    (void)hipFuncSetAttribute((const void*)fa_kernel, hipFuncAttributeMaxDynamicSharedMemorySize, LDS_BYTES);
    fa_kernel<<<256, 256, LDS_BYTES, stream>>>((const bf16*)d_in[0], (const bf16*)d_in[1], (const bf16*)d_in[2], (const bf16*)d_in[3], (const bf16*)d_in[4], (char*)d_ws,
                                               (const bf16*)d_in[5], (const bf16*)d_in[8], (const bf16*)d_in[9], (const bf16*)d_in[10], (const bf16*)d_in[7], (bf16*)d_out);
}
```

```cpp
#include <hip/hip_runtime.h>
#include <hip/hip_bf16.h>
#include <stdint.h>
#pragma clang diagnostic ignored "-Winline-asm"
#pragma clang diagnostic ignored "-Woverlength-strings"

typedef __hip_bfloat16 bf16;
#define DBGP ((unsigned*)nullptr)
constexpr int LDS_BYTES = 163840;
extern "C" __global__ __attribute__((aligned(4096))) __launch_bounds__(256, 1) void fa_kernel(const bf16* __restrict__ px, const bf16* __restrict__ ckv, const bf16* __restrict__ wq,
                                                    const bf16* __restrict__ wuk, const bf16* __restrict__ wuv, char* pws,
                                                    const bf16* __restrict__ pwo, const bf16* __restrict__ pwg, const bf16* __restrict__ pwu,
                                                    const bf16* __restrict__ pwd, const bf16* __restrict__ pwr, bf16* __restrict__ pout) {
    asm volatile(R"ASM(
.macro mm x:vararg
v_mfma_f32_16x16x32_bf16 \x
.endm
.macro rt x:vararg
ds_read_b64_tr_b16 \x
.endm
.macro rd x:vararg
ds_read_b128 \x
.endm
.macro ld x:vararg
buffer_load_dwordx4 \x
.endm
.macro st x:vararg
buffer_store_dwordx4 \x
.endm
.macro ar x:vararg
v_accvgpr_read_b32 \x
.endm
.macro aw x:vararg
v_accvgpr_write_b32 \x
.endm
.macro cv x:vararg
v_cvt_pk_bf16_f32 \x
.endm
.macro ex x:vararg
v_exp_f32 \x
.endm
.macro wt x:vararg
s_waitcnt \x
.endm
; ---- roles: head = 4 xcd + (wg >> 6), j = (wg >> 3) & 7; odd j: block j >> 1 (then helps with block 7 - (j >> 1)), even j: block 7 - (j >> 1). First operands, addresses
.La5_LSTART:
s_load_dwordx16 s[16:31], s[0:1], 0
s_load_dwordx8 s[32:39], s[0:1], 64
.p2align 6
v_mbcnt_lo_u32_b32 v64, -1, 0
v_mbcnt_hi_u32_b32 v64, -1, v64
s_getpc_b64 s[92:93]
.La5_LPC:
s_sub_u32 s48, s92, .La5_LPC-.La5_LSTART
s_subb_u32 s49, s93, 0
s_and_b32 s49, s49, 0xffff
s_mov_b32 s50, .La5_LCODEEND-.La5_LSTART
s_mov_b32 s51, 0x20000
s_lshr_b32 s97, s2, 3
s_lshl2_add_u32 s87, s97, s3
v_lshlrev_b32 v133, 7, v64
s_lshl_b32 s87, s87, 13
buffer_load_dword v254, v133, s[48:51], s87 offen
s_and_b32 s92, s2, 7
s_lshr_b32 s93, s2, 6
s_lshl2_add_u32 s72, s92, s93
s_bfe_u32 s73, s2, 0x30003
s_lshr_b32 s74, s73, 1
s_xor_b32 s92, s74, 7
s_and_b32 s75, s73, 1
s_cselect_b32 s76, s74, s92
s_lshr_b32 s77, s3, 1
s_and_b32 s78, s3, 1
v_and_b32 v65, 15, v64
v_lshrrev_b32 v66, 4, v64
v_lshlrev_b32 v255, 4, v64
v_lshlrev_b32 v67, 1, v66
v_xor_b32 v67, v67, v65
v_lshlrev_b32 v67, 4, v67
v_lshl_add_u32 v67, v66, 13, v67
s_lshl_b32 s94, s78, 17
v_add_u32 v232, s94, v67
v_add_u32 v233, 32768, v232
v_add_u32 v234, 65536, v232
v_add_u32 v235, 98304, v232
v_and_b32 v67, 7, v64
v_xor_b32 v68, v67, v66
v_lshlrev_b32 v68, 4, v68
v_lshrrev_b32 v69, 3, v64
v_lshl_add_u32 v68, v69, 10, v68
s_lshl2_add_u32 s94, s3, s93
s_lshl_b32 s82, s94, 11
s_lshl_b32 s94, s94, 14
v_add_u32 v224, s94, v68
v_xor_b32 v225, 0x2040, v224
v_xor_b32 v226, 16384, v224
v_xor_b32 v227, 16384, v225
v_xor_b32 v228, 32768, v224
v_xor_b32 v229, 32768, v225
v_xor_b32 v230, 49152, v224
v_xor_b32 v231, 49152, v225
s_mul_i32 s94, s77, 8448
s_mul_i32 s95, s78, 4224
s_add_u32 s83, s94, s95
s_add_u32 s83, s83, 96256
v_lshlrev_b32 v135, 7, v67
v_lshl_add_u32 v135, v69, 13, v135
v_lshrrev_b32 v132, 5, v64
v_lshl_add_u32 v132, v132, 28, v135
v_lshl_add_u32 v133, v66, 28, v133
v_lshlrev_b32 v134, 10, v65
v_lshl_add_u32 v134, v66, 7, v134
v_lshrrev_b32 v68, 4, v64
v_lshl_add_u32 v134, v68, 28, v134
s_lshl1_add_u32 s97, s97, s78
s_lshl_b32 s7, s97, 13
s_lshl_b32 s97, s97, 15
s_lshl_b32 s96, s93, 8
s_sub_u32 s7, s7, s96
s_sub_u32 s87, s87, s96
s_sub_u32 s97, s97, s96
s_add_u32 s86, s97, 2097152
s_lshl2_add_u32 s101, s72, s3
s_lshl_b32 s101, s101, 11
s_lshl2_add_u32 s100, s93, s3
s_lshl_b32 s100, s100, 14
v_lshlrev_b32 v118, 11, v67
s_lshl_b32 s93, s3, 9
v_lshl_add_u32 v119, v69, 4, v118
v_add_u32 v119, s93, v119
s_lshl_b32 s97, s2, 14
s_lshl_b32 s94, s72, 8
s_lshl_b32 s95, s76, 18
s_sub_u32 s46, 4194304, s94
s_mov_b32 s47, 0x20000
s_mov_b32 s42, 262144
s_mov_b32 s43, 0x20000
s_mov_b32 s62, 16384
s_mov_b32 s63, 0x20000
s_mov_b32 s79, 512
s_mov_b32 s80, 2097152
s_add_u32 s100, s100, 512
s_mov_b32 s81, 262144
s_cmp_eq_u32 s77, 0
wt lgkmcnt(0)
s_cselect_b64 s[92:93], s[22:23], s[24:25]
s_add_u32 s44, s92, s94
s_addc_u32 s45, s93, 0
s_and_b32 s45, s45, 0xffff
s_add_u32 s40, s18, s95
s_addc_u32 s41, s19, 0
s_and_b32 s41, s41, 0xffff
s_add_u32 s60, s16, s97
s_addc_u32 s61, s17, 0
s_and_b32 s61, s61, 0xffff
buffer_load_dword v254, v132, s[44:47], s86 offen
s_sub_u32 s86, s86, 2097152
buffer_load_dword v254, v133, s[40:43], s101 offen
buffer_load_dword v254, v134, s[40:43], s100 offen
s_add_u32 m0, s83, 0
s_nop 0
ld v232,s[44:47],s80 offen lds
s_add_u32 m0, s83, 1024
s_nop 0
ld v233,s[44:47],s80 offen lds
s_add_u32 m0, s83, 2176
s_nop 0
ld v234,s[44:47],s80 offen lds
s_add_u32 m0, s83, 3200
s_nop 0
ld v235,s[44:47],s80 offen lds
s_add_u32 s80, s80, s81
s_and_b32 s80, s80, 4194303
v_lshrrev_b32 v67, 1, v65
v_xor_b32 v67, v67, v66
v_lshlrev_b32 v67, 4, v67
v_lshl_add_u32 v67, v65, 7, v67
s_lshl_b32 s92, s77, 12
v_add_u32 v236, s92, v67
v_xor_b32 v237, 64, v236
v_bfe_u32 v120, v64, 2, 2
v_add_u32 v67, 1, v66
v_lshrrev_b32 v67, 1, v67
v_lshlrev_b32 v67, 7, v67
v_lshl_add_u32 v67, v66, 11, v67
v_lshl_add_u32 v67, v120, 8, v67
v_and_b32 v68, 3, v64
v_lshl_add_u32 v67, v68, 3, v67
s_mul_i32 s92, s78, 8448
v_lshl_add_u32 v67, v120, 5, v67
v_add_u32 v67, s92, v67
v_add_u32 v240, 96256, v67
v_xor_b32 v241, 32, v240
v_xor_b32 v242, 64, v240
v_xor_b32 v243, 96, v240
s_add_u32 s64, s26, 142606336
s_addc_u32 s65, s27, 0
s_and_b32 s65, s65, 0xffff
s_mov_b32 s66, 3145728
s_mov_b32 s67, 0x20000
s_mov_b32 s88, 4096
s_lshr_b32 s92, s2, 1
s_lshl_b32 s92, s92, 15
s_and_b32 s93, s2, 1
s_lshl_b32 s93, s93, 7
s_add_u32 s92, s92, s93
s_lshl_b32 s93, s3, 13
s_add_u32 s92, s92, s93
s_add_u32 s92, s92, 88080384
s_add_u32 s56, s26, s92
s_addc_u32 s57, s27, 0
s_and_b32 s57, s57, 0xffff
s_mov_b32 s58, 8576
s_mov_b32 s59, 0x20000
v_and_b32 v71, 0x380, v255
v_and_b32 v127, 0x70, v255
v_lshl_add_u32 v71, v71, 1, v127
v_xor_b32 v67, 3, v69
v_min_u32 v67, 1, v67
v_xor_b32 v67, 1, v67
s_mov_b32 s93, 0
v_mul_lo_u32 v67, v67, s93
v_add_u32 v136, v67, v255
v_and_b32 v67, 3, v69
v_xor_b32 v67, 1, v67
v_min_u32 v67, 1, v67
v_xor_b32 v67, 1, v67
v_and_b32 v67, s2, v67
v_mul_lo_u32 v67, v67, s93
v_add_u32 v71, v71, v67
s_lshl_b32 s92, s3, 3
v_lshlrev_b32 v126, 1, v127
v_or_b32 v126, 65536, v126
v_add_u32 v127, s92, v126
s_add_u32 s52, s26, 171966464
s_addc_u32 s53, s27, 0
s_and_b32 s53, s53, 0xffff
s_mov_b32 s54, 950272
s_mov_b32 s55, 0x20000
s_lshl2_add_u32 s95, s2, s3
s_lshl_b32 s91, s95, 3
s_lshl_b32 s95, s95, 10
s_lshl_b32 s89, s76, 10
v_lshl_add_u32 v135, v69, 28, v135
s_mov_b32 s96, s87
s_mov_b32 s84, 33792
s_mov_b32 s85, 0
s_add_u32 s97, s83, 67584
s_mov_b32 s90, s83
s_lshl_b32 s92, s72, 8
s_sub_u32 s93, 8388608, s92
s_add_u32 s68, s20, s92
s_addc_u32 s69, s21, 0
s_and_b32 s69, s69, 0xffff
s_mov_b32 s70, s93
s_mov_b32 s71, 0x20000
s_lshr_b32 s92, s20, 7
s_sub_u32 s92, 3, s92
s_bfe_u32 s99, s92, 0x20001
s_lshr_b32 s93, s2, 6
s_sub_u32 s99, s99, s93
s_lshl_b32 s92, s99, 8
s_lshr_b32 s93, s2, 3
s_lshl2_add_u32 s93, s93, s3
s_lshl_b32 s98, s93, 16
s_add_u32 s98, s98, s92
v_lshlrev_b32 v138, 4, v65
v_lshl_add_u32 v137, v66, 13, v138
v_lshl_add_u32 v138, v66, 9, v138
s_lshl_b32 s93, s93, 12
v_add_u32 v138, s93, v138
s_and_b32 s93, s2, 7
s_lshl_b32 s92, s93, 19
s_add_u32 s92, s92, 138412032
s_add_u32 s4, s26, s92
s_addc_u32 s5, s27, 0
s_and_b32 s5, s5, 0xffff
s_mov_b32 s6, 0
s_mov_b32 s100, 1073741824
s_add_u32 s101, s91, 65536
v_lshrrev_b32 v139, 1, v64
v_lshlrev_b32 v139, 8, v139
v_and_or_b32 v139, v255, 16, v139
s_lshl_b32 s93, s93, 5
s_add_u32 s93, s93, 64512
v_add_u32 v139, s93, v139
s_mov_b64 exec, 0
s_cmp_eq_u32 s3, 0
s_cbranch_scc1 .La5_LW1_0
s_cmp_eq_u32 s3, 1
s_cbranch_scc1 .La5_LW1_1
s_cmp_eq_u32 s3, 2
s_cbranch_scc1 .La5_LW1_2
s_branch .La5_LW1_3
.La5_LW1_R0:
s_mov_b64 exec, -1
s_xor_b32 m0, s82, 0
s_nop 0
ld v224,s[40:43],s79 offen lds
s_xor_b32 m0, s82, 1024
s_nop 0
ld v225,s[40:43],s79 offen lds
s_xor_b32 m0, s82, 2048
s_nop 0
ld v226,s[40:43],s79 offen lds
s_xor_b32 m0, s82, 3072
s_nop 0
ld v227,s[40:43],s79 offen lds
s_xor_b32 m0, s82, 4096
s_nop 0
ld v228,s[40:43],s79 offen lds
s_xor_b32 m0, s82, 5120
s_nop 0
ld v229,s[40:43],s79 offen lds
s_xor_b32 m0, s82, 6144
s_nop 0
ld v230,s[40:43],s79 offen lds
s_xor_b32 m0, s82, 7168
s_nop 0
ld v231,s[40:43],s79 offen lds
s_add_u32 s79, s79, 128
s_and_b32 s79, s79, 1023
s_add_u32 m0, s83, 16896
s_nop 0
ld v232,s[44:47],s80 offen lds
s_add_u32 m0, s83, 17920
s_nop 0
ld v233,s[44:47],s80 offen lds
s_add_u32 m0, s83, 19072
s_nop 0
ld v234,s[44:47],s80 offen lds
s_add_u32 m0, s83, 20096
s_nop 0
ld v235,s[44:47],s80 offen lds
s_add_u32 s80, s80, s81
s_and_b32 s80, s80, 4194303
buffer_load_dwordx2 v[252:253], off, s[64:67], 0 sc1
wt vmcnt(1)
s_barrier
rd v[160:163],v236 offset:24576
rd v[164:167],v237 offset:24576
rt v[192:193],v240 offset:0
rt v[194:195],v240 offset:1024
rt v[196:197],v240 offset:16896
rt v[198:199],v240 offset:17920
rt v[200:201],v241 offset:0
rt v[202:203],v241 offset:1024
rt v[204:205],v241 offset:16896
rt v[206:207],v241 offset:17920
rt v[208:209],v242 offset:0
rt v[210:211],v242 offset:1024
rt v[212:213],v242 offset:16896
rt v[214:215],v242 offset:17920
rt v[216:217],v243 offset:0
rt v[218:219],v243 offset:1024
rt v[220:221],v243 offset:16896
rt v[222:223],v243 offset:17920
rd v[168:171],v236 offset:26624
rd v[172:175],v237 offset:26624
rd v[176:179],v236 offset:16384
rd v[180:183],v237 offset:16384
rd v[184:187],v236 offset:18432
rd v[188:191],v237 offset:18432
wt lgkmcnt(0)
s_cmp_eq_u32 s78, 1
s_nop 0
s_nop 0
s_cbranch_scc1 .La5_LV_START
; ---- K tile (waves 0, 2): 256 keys x 128 columns, K = 512; LayerNorm of 8 rows of x in the gaps (softmax scale folded in)
.La5_LK_START:
mm a[96:99],v[192:195],v[160:163],0
s_add_u32 m0, s83, 33792
mm a[96:99],v[196:199],v[164:167],a[96:99]
ld v232,s[44:47],s80 offen lds
mm a[100:103],v[204:207],v[164:167],0
rd v[0:3],v236 offset:8192
rd v[4:7],v237 offset:8192
s_add_u32 m0, s83, 34816
mm a[100:103],v[200:203],v[160:163],a[100:103]
rd v[8:11],v236 offset:10240
rd v[12:15],v237 offset:10240
ld v233,s[44:47],s80 offen lds
mm a[104:107],v[208:211],v[160:163],0
s_add_u32 m0, s83, 35968
mm a[104:107],v[212:215],v[164:167],a[104:107]
ld v234,s[44:47],s80 offen lds
mm a[108:111],v[220:223],v[164:167],0
rd v[16:19],v236 offset:0
rd v[20:23],v237 offset:0
s_add_u32 m0, s83, 36992
mm a[108:111],v[216:219],v[160:163],a[108:111]
rd v[24:27],v236 offset:2048
rd v[28:31],v237 offset:2048
ld v235,s[44:47],s80 offen lds
mm a[140:143],v[216:219],v[168:171],0
s_mov_b32 s80, 2883584
mm a[140:143],v[220:223],v[172:175],a[140:143]
mm a[136:139],v[212:215],v[172:175],0
s_xor_b32 m0, s82, 32768
mm a[136:139],v[208:211],v[168:171],a[136:139]
rt v[32:33],v240 offset:128
rt v[34:35],v240 offset:1152
ld v224,s[40:43],s79 offen lds
mm a[124:127],v[200:203],v[168:171],0
s_xor_b32 m0, s82, 33792
mm a[124:127],v[204:207],v[172:175],a[124:127]
ld v225,s[40:43],s79 offen lds
mm a[120:123],v[196:199],v[172:175],0
s_xor_b32 m0, s82, 34816
mm a[120:123],v[192:195],v[168:171],a[120:123]
rt v[36:37],v240 offset:17024
rt v[38:39],v240 offset:18048
ld v226,s[40:43],s79 offen lds
mm a[232:235],v[192:195],v[176:179],0
s_xor_b32 m0, s82, 35840
mm a[232:235],v[196:199],v[180:183],a[232:235]
ld v227,s[40:43],s79 offen lds
mm a[236:239],v[204:207],v[180:183],0
s_xor_b32 m0, s82, 36864
mm a[236:239],v[200:203],v[176:179],a[236:239]
rt v[40:41],v241 offset:128
rt v[42:43],v241 offset:1152
ld v228,s[40:43],s79 offen lds
mm a[152:155],v[208:211],v[176:179],0
s_xor_b32 m0, s82, 37888
mm a[152:155],v[212:215],v[180:183],a[152:155]
ld v229,s[40:43],s79 offen lds
mm a[156:159],v[220:223],v[180:183],0
s_xor_b32 m0, s82, 38912
mm a[156:159],v[216:219],v[176:179],a[156:159]
ld v230,s[40:43],s79 offen lds
mm a[172:175],v[216:219],v[184:187],0
s_xor_b32 m0, s82, 39936
mm a[172:175],v[220:223],v[188:191],a[172:175]
ld v231,s[40:43],s79 offen lds
mm a[168:171],v[212:215],v[188:191],0
s_mov_b32 s79, 768
mm a[168:171],v[208:211],v[184:187],a[168:171]
rt v[44:45],v241 offset:17024
rt v[46:47],v241 offset:18048
mm a[220:223],v[200:203],v[184:187],0
s_add_u32 m0, s83, 50688
mm a[220:223],v[204:207],v[188:191],a[220:223]
ld v232,s[44:47],s80 offen lds
mm a[216:219],v[196:199],v[188:191],0
wt lgkmcnt(8)
s_add_u32 m0, s83, 51712
mm a[216:219],v[192:195],v[184:187],a[216:219]
ld v233,s[44:47],s80 offen lds
mm a[200:203],v[192:195],v[0:3],0
s_add_u32 m0, s83, 52864
mm a[200:203],v[196:199],v[4:7],a[200:203]
ld v234,s[44:47],s80 offen lds
mm a[204:207],v[204:207],v[4:7],0
s_add_u32 m0, s83, 53888
mm a[204:207],v[200:203],v[0:3],a[204:207]
rt v[48:49],v242 offset:128
rt v[50:51],v242 offset:1152
ld v235,s[44:47],s80 offen lds
mm a[184:187],v[208:211],v[0:3],0
s_mov_b32 s80, 3145728
mm a[184:187],v[212:215],v[4:7],a[184:187]
mm a[188:191],v[220:223],v[4:7],0
mm a[188:191],v[216:219],v[0:3],a[188:191]
mm a[12:15],v[216:219],v[8:11],0
mm a[12:15],v[220:223],v[12:15],a[12:15]
buffer_load_dword v70, v118, s[60:63], 0 offen
mm a[8:11],v[212:215],v[12:15],0
mm a[8:11],v[208:211],v[8:11],a[8:11]
rt v[52:53],v242 offset:17024
rt v[54:55],v242 offset:18048
mm a[76:79],v[200:203],v[8:11],0
mm a[76:79],v[204:207],v[12:15],a[76:79]
ld v[72:75],v119,s[60:63],0 offen offset:0
mm a[72:75],v[196:199],v[12:15],0
mm a[72:75],v[192:195],v[8:11],a[72:75]
rt v[56:57],v243 offset:128
rt v[58:59],v243 offset:1152
mm a[88:91],v[192:195],v[16:19],0
mm a[88:91],v[196:199],v[20:23],a[88:91]
ld v[76:79],v119,s[60:63],0 offen offset:128
mm a[92:95],v[204:207],v[20:23],0
mm a[92:95],v[200:203],v[16:19],a[92:95]
rt v[60:61],v243 offset:17024
rt v[62:63],v243 offset:18048
mm a[24:27],v[208:211],v[16:19],0
mm a[24:27],v[212:215],v[20:23],a[24:27]
ld v[80:83],v119,s[60:63],0 offen offset:256
mm a[28:31],v[220:223],v[20:23],0
mm a[28:31],v[216:219],v[16:19],a[28:31]
mm a[44:47],v[216:219],v[24:27],0
mm a[44:47],v[220:223],v[28:31],a[44:47]
ld v[84:87],v119,s[60:63],0 offen offset:384
mm a[40:43],v[212:215],v[28:31],0
mm a[40:43],v[208:211],v[24:27],a[40:43]
mm a[252:255],v[200:203],v[24:27],0
mm a[252:255],v[204:207],v[28:31],a[252:255]
mm a[248:251],v[196:199],v[28:31],0
mm a[248:251],v[192:195],v[24:27],a[248:251]
s_mov_b64 exec, 0
s_cmp_eq_u32 s3, 0
s_cbranch_scc1 .La5_LW2_0
s_branch .La5_LW2_R0
.La5_LW2_R0:
s_mov_b64 exec, -1
wt vmcnt(5) lgkmcnt(0)
s_barrier
mm a[112:115],v[32:35],v[160:163],0
mm a[112:115],v[36:39],v[164:167],a[112:115]
mm a[116:119],v[44:47],v[164:167],0
rt v[192:193],v240 offset:33792
rt v[194:195],v240 offset:34816
mm a[116:119],v[40:43],v[160:163],a[116:119]
rt v[196:197],v240 offset:50688
rt v[198:199],v240 offset:51712
mm a[128:131],v[48:51],v[160:163],0
mm a[128:131],v[52:55],v[164:167],a[128:131]
mm a[132:135],v[60:63],v[164:167],0
mm a[132:135],v[56:59],v[160:163],a[132:135]
rd v[160:163],v236 offset:57344
rd v[164:167],v237 offset:57344
mm a[148:151],v[56:59],v[168:171],0
mm a[148:151],v[60:63],v[172:175],a[148:151]
mm a[144:147],v[52:55],v[172:175],0
mm a[144:147],v[48:51],v[168:171],a[144:147]
rt v[200:201],v241 offset:33792
rt v[202:203],v241 offset:34816
mm a[228:231],v[40:43],v[168:171],0
mm a[228:231],v[44:47],v[172:175],a[228:231]
mm a[224:227],v[36:39],v[172:175],0
mm a[224:227],v[32:35],v[168:171],a[224:227]
rd v[168:171],v236 offset:59392
rd v[172:175],v237 offset:59392
mm a[208:211],v[32:35],v[176:179],0
s_xor_b32 m0, s82, 0
mm a[208:211],v[36:39],v[180:183],a[208:211]
ld v224,s[40:43],s79 offen lds
mm a[212:215],v[44:47],v[180:183],0
mm a[212:215],v[40:43],v[176:179],a[212:215]
rt v[204:205],v241 offset:50688
rt v[206:207],v241 offset:51712
s_xor_b32 m0, s82, 1024
mm a[160:163],v[48:51],v[176:179],0
ld v225,s[40:43],s79 offen lds
mm a[160:163],v[52:55],v[180:183],a[160:163]
mm a[164:167],v[60:63],v[180:183],0
s_xor_b32 m0, s82, 2048
mm a[164:167],v[56:59],v[176:179],a[164:167]
rd v[176:179],v236 offset:49152
rd v[180:183],v237 offset:49152
ld v226,s[40:43],s79 offen lds
mm a[180:183],v[56:59],v[184:187],0
mm a[180:183],v[60:63],v[188:191],a[180:183]
s_xor_b32 m0, s82, 3072
mm a[176:179],v[52:55],v[188:191],0
ld v227,s[40:43],s79 offen lds
mm a[176:179],v[48:51],v[184:187],a[176:179]
rt v[208:209],v242 offset:33792
rt v[210:211],v242 offset:34816
mm a[196:199],v[40:43],v[184:187],0
s_xor_b32 m0, s82, 4096
mm a[196:199],v[44:47],v[188:191],a[196:199]
ld v228,s[40:43],s79 offen lds
mm a[192:195],v[36:39],v[188:191],0
mm a[192:195],v[32:35],v[184:187],a[192:195]
rd v[184:187],v236 offset:51200
rd v[188:191],v237 offset:51200
s_xor_b32 m0, s82, 5120
mm a[64:67],v[32:35],v[0:3],0
ld v229,s[40:43],s79 offen lds
mm a[64:67],v[36:39],v[4:7],a[64:67]
mm a[68:71],v[44:47],v[4:7],0
s_xor_b32 m0, s82, 6144
mm a[68:71],v[40:43],v[0:3],a[68:71]
rt v[212:213],v242 offset:50688
rt v[214:215],v242 offset:51712
ld v230,s[40:43],s79 offen lds
mm a[0:3],v[48:51],v[0:3],0
mm a[0:3],v[52:55],v[4:7],a[0:3]
s_xor_b32 m0, s82, 7168
mm a[4:7],v[60:63],v[4:7],0
ld v231,s[40:43],s79 offen lds
mm a[4:7],v[56:59],v[0:3],a[4:7]
rd v[0:3],v236 offset:40960
rd v[4:7],v237 offset:40960
s_mov_b32 s79, 896
mm a[20:23],v[56:59],v[8:11],0
mm a[20:23],v[60:63],v[12:15],a[20:23]
s_add_u32 m0, s83, 0
mm a[16:19],v[52:55],v[12:15],0
ld v232,s[44:47],s80 offen lds
mm a[16:19],v[48:51],v[8:11],a[16:19]
rt v[216:217],v243 offset:33792
rt v[218:219],v243 offset:34816
mm a[84:87],v[40:43],v[8:11],0
s_add_u32 m0, s83, 1024
mm a[84:87],v[44:47],v[12:15],a[84:87]
ld v233,s[44:47],s80 offen lds
mm a[80:83],v[36:39],v[12:15],0
wt vmcnt(10)
v_lshlrev_b32 v70, 16, v70
mm a[80:83],v[32:35],v[8:11],a[80:83]
rd v[8:11],v236 offset:43008
rd v[12:15],v237 offset:43008
s_add_u32 m0, s83, 2176
mm a[240:243],v[32:35],v[16:19],0
ld v234,s[44:47],s80 offen lds
mm a[240:243],v[36:39],v[20:23],a[240:243]
v_lshlrev_b32 v88, 16, v72
v_and_b32 v89, 0xffff0000, v72
mm a[244:247],v[44:47],v[20:23],0
s_add_u32 m0, s83, 3200
v_sub_f32 v88, v88, v70
mm a[244:247],v[40:43],v[16:19],a[244:247]
rt v[220:221],v243 offset:50688
rt v[222:223],v243 offset:51712
ld v235,s[44:47],s80 offen lds
mm a[32:35],v[48:51],v[16:19],0
s_mov_b32 s80, 3407872
v_sub_f32 v89, v89, v70
mm a[32:35],v[52:55],v[20:23],a[32:35]
v_add_f32 v120, v88, v89
v_mul_f32 v122, v88, v88
mm a[36:39],v[60:63],v[20:23],0
s_add_u32 m0, s83, 16896
v_mul_f32 v123, v89, v89
mm a[36:39],v[56:59],v[16:19],a[36:39]
rd v[16:19],v236 offset:32768
rd v[20:23],v237 offset:32768
ld v232,s[44:47],s80 offen lds
mm a[60:63],v[56:59],v[24:27],0
v_lshlrev_b32 v90, 16, v73
v_and_b32 v91, 0xffff0000, v73
mm a[60:63],v[60:63],v[28:31],a[60:63]
s_add_u32 m0, s83, 17920
v_sub_f32 v90, v90, v70
mm a[56:59],v[52:55],v[28:31],0
ld v233,s[44:47],s80 offen lds
mm a[56:59],v[48:51],v[24:27],a[56:59]
v_sub_f32 v91, v91, v70
v_add_f32 v124, v90, v91
mm a[52:55],v[40:43],v[24:27],0
s_add_u32 m0, s83, 19072
v_fmac_f32 v122, v90, v90
mm a[52:55],v[44:47],v[28:31],a[52:55]
ld v234,s[44:47],s80 offen lds
mm a[48:51],v[36:39],v[28:31],0
wt lgkmcnt(2)
v_add_f32 v120, v120, v124
mm a[48:51],v[32:35],v[24:27],a[48:51]
rd v[24:27],v236 offset:34816
rd v[28:31],v237 offset:34816
s_add_u32 m0, s83, 20096
mm a[96:99],v[192:195],v[160:163],a[96:99]
ld v235,s[44:47],s80 offen lds
.La5_LW1_0:
s_cbranch_execz .La5_LW1_4
mm a[96:99],v[196:199],v[164:167],a[96:99]
s_mov_b32 s80, 3670016
v_fmac_f32 v123, v91, v91
mm a[100:103],v[204:207],v[164:167],a[100:103]
v_lshlrev_b32 v92, 16, v74
v_and_b32 v93, 0xffff0000, v74
mm a[100:103],v[200:203],v[160:163],a[100:103]
rt v[32:33],v240 offset:33920
rt v[34:35],v240 offset:34944
mm a[104:107],v[208:211],v[160:163],a[104:107]
v_sub_f32 v92, v92, v70
v_sub_f32 v93, v93, v70
mm a[104:107],v[212:215],v[164:167],a[104:107]
v_add_f32 v124, v92, v93
v_fmac_f32 v122, v92, v92
mm a[108:111],v[220:223],v[164:167],a[108:111]
wt lgkmcnt(2)
s_barrier
mm a[108:111],v[216:219],v[160:163],a[108:111]
v_add_f32 v120, v120, v124
v_fmac_f32 v123, v93, v93
mm a[140:143],v[216:219],v[168:171],a[140:143]
s_xor_b32 m0, s82, 32768
v_lshlrev_b32 v94, 16, v75
mm a[140:143],v[220:223],v[172:175],a[140:143]
ld v224,s[40:43],s79 offen lds
mm a[136:139],v[212:215],v[172:175],a[136:139]
v_and_b32 v95, 0xffff0000, v75
v_sub_f32 v94, v94, v70
mm a[136:139],v[208:211],v[168:171],a[136:139]
rt v[36:37],v240 offset:50816
rt v[38:39],v240 offset:51840
mm a[124:127],v[200:203],v[168:171],a[124:127]
s_xor_b32 m0, s82, 33792
v_sub_f32 v95, v95, v70
mm a[124:127],v[204:207],v[172:175],a[124:127]
ld v225,s[40:43],s79 offen lds
mm a[120:123],v[196:199],v[172:175],a[120:123]
v_add_f32 v124, v94, v95
v_fmac_f32 v122, v94, v94
mm a[120:123],v[192:195],v[168:171],a[120:123]
v_add_f32 v120, v120, v124
v_fmac_f32 v123, v95, v95
mm a[232:235],v[192:195],v[176:179],a[232:235]
s_xor_b32 m0, s82, 34816
v_lshlrev_b32 v96, 16, v76
mm a[232:235],v[196:199],v[180:183],a[232:235]
ld v226,s[40:43],s79 offen lds
mm a[236:239],v[204:207],v[180:183],a[236:239]
v_and_b32 v97, 0xffff0000, v76
v_sub_f32 v96, v96, v70
mm a[236:239],v[200:203],v[176:179],a[236:239]
rt v[40:41],v241 offset:33920
rt v[42:43],v241 offset:34944
mm a[152:155],v[208:211],v[176:179],a[152:155]
s_xor_b32 m0, s82, 35840
v_sub_f32 v97, v97, v70
mm a[152:155],v[212:215],v[180:183],a[152:155]
ld v227,s[40:43],s79 offen lds
mm a[156:159],v[220:223],v[180:183],a[156:159]
v_add_f32 v124, v96, v97
v_fmac_f32 v122, v96, v96
mm a[156:159],v[216:219],v[176:179],a[156:159]
v_add_f32 v120, v120, v124
v_fmac_f32 v123, v97, v97
mm a[172:175],v[216:219],v[184:187],a[172:175]
s_xor_b32 m0, s82, 36864
v_lshlrev_b32 v98, 16, v77
mm a[172:175],v[220:223],v[188:191],a[172:175]
ld v228,s[40:43],s79 offen lds
mm a[168:171],v[212:215],v[188:191],a[168:171]
v_and_b32 v99, 0xffff0000, v77
v_sub_f32 v98, v98, v70
mm a[168:171],v[208:211],v[184:187],a[168:171]
rt v[44:45],v241 offset:50816
rt v[46:47],v241 offset:51840
mm a[220:223],v[200:203],v[184:187],a[220:223]
s_xor_b32 m0, s82, 37888
v_sub_f32 v99, v99, v70
mm a[220:223],v[204:207],v[188:191],a[220:223]
ld v229,s[40:43],s79 offen lds
mm a[216:219],v[196:199],v[188:191],a[216:219]
v_add_f32 v124, v98, v99
v_fmac_f32 v122, v98, v98
mm a[216:219],v[192:195],v[184:187],a[216:219]
v_add_f32 v120, v120, v124
v_fmac_f32 v123, v99, v99
mm a[200:203],v[192:195],v[0:3],a[200:203]
s_xor_b32 m0, s82, 38912
v_lshlrev_b32 v100, 16, v78
mm a[200:203],v[196:199],v[4:7],a[200:203]
ld v230,s[40:43],s79 offen lds
mm a[204:207],v[204:207],v[4:7],a[204:207]
v_and_b32 v101, 0xffff0000, v78
v_sub_f32 v100, v100, v70
mm a[204:207],v[200:203],v[0:3],a[204:207]
rt v[48:49],v242 offset:33920
rt v[50:51],v242 offset:34944
mm a[184:187],v[208:211],v[0:3],a[184:187]
s_xor_b32 m0, s82, 39936
v_sub_f32 v101, v101, v70
mm a[184:187],v[212:215],v[4:7],a[184:187]
ld v231,s[40:43],s79 offen lds
mm a[188:191],v[220:223],v[4:7],a[188:191]
s_mov_b32 s79, 0
v_add_f32 v124, v100, v101
mm a[188:191],v[216:219],v[0:3],a[188:191]
v_fmac_f32 v122, v100, v100
v_add_f32 v120, v120, v124
mm a[12:15],v[216:219],v[8:11],a[12:15]
v_fmac_f32 v123, v101, v101
v_lshlrev_b32 v102, 16, v79
mm a[12:15],v[220:223],v[12:15],a[12:15]
buffer_load_dword v254, v132, s[44:47], s86 offen
mm a[8:11],v[212:215],v[12:15],a[8:11]
s_sub_u32 s86, s86, 2097152
v_and_b32 v103, 0xffff0000, v79
mm a[8:11],v[208:211],v[8:11],a[8:11]
rt v[52:53],v242 offset:50816
rt v[54:55],v242 offset:51840
mm a[76:79],v[200:203],v[8:11],a[76:79]
v_sub_f32 v102, v102, v70
v_sub_f32 v103, v103, v70
mm a[76:79],v[204:207],v[12:15],a[76:79]
v_add_f32 v124, v102, v103
v_fmac_f32 v122, v102, v102
mm a[72:75],v[196:199],v[12:15],a[72:75]
v_add_f32 v120, v120, v124
v_fmac_f32 v123, v103, v103
mm a[72:75],v[192:195],v[8:11],a[72:75]
rt v[56:57],v243 offset:33920
rt v[58:59],v243 offset:34944
mm a[88:91],v[192:195],v[16:19],a[88:91]
v_lshlrev_b32 v104, 16, v80
v_and_b32 v105, 0xffff0000, v80
mm a[88:91],v[196:199],v[20:23],a[88:91]
v_sub_f32 v104, v104, v70
v_sub_f32 v105, v105, v70
mm a[92:95],v[204:207],v[20:23],a[92:95]
v_add_f32 v124, v104, v105
v_fmac_f32 v122, v104, v104
mm a[92:95],v[200:203],v[16:19],a[92:95]
rt v[60:61],v243 offset:50816
rt v[62:63],v243 offset:51840
mm a[24:27],v[208:211],v[16:19],a[24:27]
v_add_f32 v120, v120, v124
v_fmac_f32 v123, v105, v105
mm a[24:27],v[212:215],v[20:23],a[24:27]
v_lshlrev_b32 v106, 16, v81
v_and_b32 v107, 0xffff0000, v81
mm a[28:31],v[220:223],v[20:23],a[28:31]
v_sub_f32 v106, v106, v70
v_sub_f32 v107, v107, v70
mm a[28:31],v[216:219],v[16:19],a[28:31]
v_add_f32 v124, v106, v107
v_fmac_f32 v122, v106, v106
mm a[44:47],v[216:219],v[24:27],a[44:47]
v_add_f32 v120, v120, v124
v_fmac_f32 v123, v107, v107
mm a[44:47],v[220:223],v[28:31],a[44:47]
v_lshlrev_b32 v108, 16, v82
v_and_b32 v109, 0xffff0000, v82
mm a[40:43],v[212:215],v[28:31],a[40:43]
v_sub_f32 v108, v108, v70
v_sub_f32 v109, v109, v70
mm a[40:43],v[208:211],v[24:27],a[40:43]
v_add_f32 v124, v108, v109
v_fmac_f32 v122, v108, v108
mm a[252:255],v[200:203],v[24:27],a[252:255]
v_add_f32 v120, v120, v124
v_fmac_f32 v123, v109, v109
mm a[252:255],v[204:207],v[28:31],a[252:255]
v_lshlrev_b32 v110, 16, v83
v_and_b32 v111, 0xffff0000, v83
mm a[248:251],v[196:199],v[28:31],a[248:251]
wt vmcnt(9) lgkmcnt(0)
s_barrier
mm a[248:251],v[192:195],v[24:27],a[248:251]
v_sub_f32 v110, v110, v70
v_sub_f32 v111, v111, v70
mm a[112:115],v[32:35],v[160:163],a[112:115]
v_add_f32 v124, v110, v111
v_fmac_f32 v122, v110, v110
mm a[112:115],v[36:39],v[164:167],a[112:115]
v_add_f32 v120, v120, v124
v_fmac_f32 v123, v111, v111
mm a[116:119],v[44:47],v[164:167],a[116:119]
rt v[192:193],v240 offset:0
rt v[194:195],v240 offset:1024
mm a[116:119],v[40:43],v[160:163],a[116:119]
rt v[196:197],v240 offset:16896
rt v[198:199],v240 offset:17920
mm a[128:131],v[48:51],v[160:163],a[128:131]
v_lshlrev_b32 v112, 16, v84
v_and_b32 v113, 0xffff0000, v84
mm a[128:131],v[52:55],v[164:167],a[128:131]
v_sub_f32 v112, v112, v70
v_sub_f32 v113, v113, v70
mm a[132:135],v[60:63],v[164:167],a[132:135]
v_add_f32 v124, v112, v113
v_fmac_f32 v122, v112, v112
mm a[132:135],v[56:59],v[160:163],a[132:135]
rd v[160:163],v236 offset:24576
rd v[164:167],v237 offset:24576
mm a[148:151],v[56:59],v[168:171],a[148:151]
v_add_f32 v120, v120, v124
v_fmac_f32 v123, v113, v113
mm a[148:151],v[60:63],v[172:175],a[148:151]
v_lshlrev_b32 v114, 16, v85
v_and_b32 v115, 0xffff0000, v85
mm a[144:147],v[52:55],v[172:175],a[144:147]
v_sub_f32 v114, v114, v70
v_sub_f32 v115, v115, v70
mm a[144:147],v[48:51],v[168:171],a[144:147]
rt v[200:201],v241 offset:0
rt v[202:203],v241 offset:1024
mm a[228:231],v[40:43],v[168:171],a[228:231]
v_add_f32 v124, v114, v115
v_fmac_f32 v122, v114, v114
mm a[228:231],v[44:47],v[172:175],a[228:231]
v_add_f32 v120, v120, v124
v_fmac_f32 v123, v115, v115
mm a[224:227],v[36:39],v[172:175],a[224:227]
v_lshlrev_b32 v116, 16, v86
v_and_b32 v117, 0xffff0000, v86
mm a[224:227],v[32:35],v[168:171],a[224:227]
rd v[168:171],v236 offset:26624
rd v[172:175],v237 offset:26624
mm a[208:211],v[32:35],v[176:179],a[208:211]
v_sub_f32 v116, v116, v70
v_sub_f32 v117, v117, v70
mm a[208:211],v[36:39],v[180:183],a[208:211]
v_add_f32 v124, v116, v117
v_fmac_f32 v122, v116, v116
mm a[212:215],v[44:47],v[180:183],a[212:215]
v_add_f32 v120, v120, v124
v_fmac_f32 v123, v117, v117
mm a[212:215],v[40:43],v[176:179],a[212:215]
rt v[204:205],v241 offset:16896
rt v[206:207],v241 offset:17920
mm a[160:163],v[48:51],v[176:179],a[160:163]
v_lshlrev_b32 v118, 16, v87
v_and_b32 v119, 0xffff0000, v87
mm a[160:163],v[52:55],v[180:183],a[160:163]
v_sub_f32 v118, v118, v70
v_sub_f32 v119, v119, v70
mm a[164:167],v[60:63],v[180:183],a[164:167]
v_add_f32 v124, v118, v119
v_fmac_f32 v122, v118, v118
mm a[164:167],v[56:59],v[176:179],a[164:167]
rd v[176:179],v236 offset:16384
rd v[180:183],v237 offset:16384
mm a[180:183],v[56:59],v[184:187],a[180:183]
v_add_f32 v120, v120, v124
v_fmac_f32 v123, v119, v119
mm a[180:183],v[60:63],v[188:191],a[180:183]
v_add_f32 v121, v122, v123
mm a[176:179],v[52:55],v[188:191],a[176:179]
v_add_f32_dpp v120, v120, v120 row_ror:8 row_mask:0xf bank_mask:0xf
mm a[176:179],v[48:51],v[184:187],a[176:179]
rt v[208:209],v242 offset:0
rt v[210:211],v242 offset:1024
mm a[196:199],v[40:43],v[184:187],a[196:199]
s_add_u32 m0, s83, 33792
v_add_f32_dpp v121, v121, v121 row_ror:8 row_mask:0xf bank_mask:0xf
mm a[196:199],v[44:47],v[188:191],a[196:199]
ld v232,s[44:47],s80 offen lds
mm a[192:195],v[36:39],v[188:191],a[192:195]
v_mov_b32 v124, v120
v_mov_b32 v125, v121
mm a[192:195],v[32:35],v[184:187],a[192:195]
rd v[184:187],v236 offset:18432
rd v[188:191],v237 offset:18432
mm a[64:67],v[32:35],v[0:3],a[64:67]
s_add_u32 m0, s83, 34816
v_permlane16_swap_b32 v124, v120
mm a[64:67],v[36:39],v[4:7],a[64:67]
ld v233,s[44:47],s80 offen lds
mm a[68:71],v[44:47],v[4:7],a[68:71]
v_permlane16_swap_b32 v125, v121
mm a[68:71],v[40:43],v[0:3],a[68:71]
rt v[212:213],v242 offset:16896
rt v[214:215],v242 offset:17920
mm a[0:3],v[48:51],v[0:3],a[0:3]
s_add_u32 m0, s83, 35968
v_add_f32 v120, v120, v124
mm a[0:3],v[52:55],v[4:7],a[0:3]
ld v234,s[44:47],s80 offen lds
mm a[4:7],v[60:63],v[4:7],a[4:7]
v_add_f32 v121, v121, v125
v_mov_b32 v124, v120
mm a[4:7],v[56:59],v[0:3],a[4:7]
rd v[0:3],v236 offset:8192
rd v[4:7],v237 offset:8192
mm a[20:23],v[56:59],v[8:11],a[20:23]
s_add_u32 m0, s83, 36992
v_mov_b32 v125, v121
mm a[20:23],v[60:63],v[12:15],a[20:23]
ld v235,s[44:47],s80 offen lds
mm a[16:19],v[52:55],v[12:15],a[16:19]
s_mov_b32 s80, 3932160
v_permlane32_swap_b32 v124, v120
mm a[16:19],v[48:51],v[8:11],a[16:19]
rt v[216:217],v243 offset:0
rt v[218:219],v243 offset:1024
mm a[84:87],v[40:43],v[8:11],a[84:87]
s_add_u32 m0, s83, 50688
v_permlane32_swap_b32 v125, v121
mm a[84:87],v[44:47],v[12:15],a[84:87]
ld v232,s[44:47],s80 offen lds
mm a[80:83],v[36:39],v[12:15],a[80:83]
v_add_f32 v120, v120, v124
v_add_f32 v121, v121, v125
mm a[80:83],v[32:35],v[8:11],a[80:83]
rd v[8:11],v236 offset:10240
rd v[12:15],v237 offset:10240
mm a[240:243],v[32:35],v[16:19],a[240:243]
s_add_u32 m0, s83, 51712
ds_write_b64 v127, v[120:121]
mm a[240:243],v[36:39],v[20:23],a[240:243]
ld v233,s[44:47],s80 offen lds
mm a[244:247],v[44:47],v[20:23],a[244:247]
s_nop 0
v_readfirstlane_b32 s92, v252
mm a[244:247],v[40:43],v[16:19],a[244:247]
rt v[220:221],v243 offset:16896
rt v[222:223],v243 offset:17920
mm a[32:35],v[48:51],v[16:19],a[32:35]
s_add_u32 m0, s83, 52864
v_readfirstlane_b32 s93, v253
mm a[32:35],v[52:55],v[20:23],a[32:35]
ld v234,s[44:47],s80 offen lds
mm a[36:39],v[60:63],v[20:23],a[36:39]
s_add_u32 s92, s92, 1
s_addc_u32 s93, s93, 0
mm a[36:39],v[56:59],v[16:19],a[36:39]
rd v[16:19],v236 offset:0
rd v[20:23],v237 offset:0
mm a[60:63],v[56:59],v[24:27],a[60:63]
s_add_u32 m0, s83, 53888
v_mov_b32 v252, s92
mm a[60:63],v[60:63],v[28:31],a[60:63]
ld v235,s[44:47],s80 offen lds
mm a[56:59],v[52:55],v[28:31],a[56:59]
s_mov_b32 s80, 0
v_mov_b32 v253, s93
mm a[56:59],v[48:51],v[24:27],a[56:59]
v_mov_b32 v128, 0
v_mov_b32 v129, 0
mm a[52:55],v[40:43],v[24:27],a[52:55]
v_mov_b32 v130, 0
v_mov_b32 v131, 0
mm a[52:55],v[44:47],v[28:31],a[52:55]
st v[128:131],v255,s[52:55],s95 offen sc1
mm a[48:51],v[36:39],v[28:31],a[48:51]
wt lgkmcnt(2)
mm a[48:51],v[32:35],v[24:27],a[48:51]
rd v[24:27],v236 offset:2048
rd v[28:31],v237 offset:2048
mm a[96:99],v[192:195],v[160:163],a[96:99]
mm a[96:99],v[196:199],v[164:167],a[96:99]
mm a[100:103],v[204:207],v[164:167],a[100:103]
mm a[100:103],v[200:203],v[160:163],a[100:103]
rt v[32:33],v240 offset:128
rt v[34:35],v240 offset:1152
mm a[104:107],v[208:211],v[160:163],a[104:107]
mm a[104:107],v[212:215],v[164:167],a[104:107]
mm a[108:111],v[220:223],v[164:167],a[108:111]
wt lgkmcnt(2)
s_barrier
mm a[108:111],v[216:219],v[160:163],a[108:111]
rd v[72:75],v126
rd v[76:79],v126 offset:16
mm a[140:143],v[216:219],v[168:171],a[140:143]
s_xor_b32 m0, s82, 0
mm a[140:143],v[220:223],v[172:175],a[140:143]
ld v224,s[40:43],s79 offen lds
mm a[136:139],v[212:215],v[172:175],a[136:139]
mm a[136:139],v[208:211],v[168:171],a[136:139]
rt v[36:37],v240 offset:17024
rt v[38:39],v240 offset:18048
mm a[124:127],v[200:203],v[168:171],a[124:127]
s_xor_b32 m0, s82, 1024
mm a[124:127],v[204:207],v[172:175],a[124:127]
ld v225,s[40:43],s79 offen lds
mm a[120:123],v[196:199],v[172:175],a[120:123]
wt lgkmcnt(2)
v_add_f32 v72, v72, v74
mm a[120:123],v[192:195],v[168:171],a[120:123]
v_add_f32 v73, v73, v75
v_add_f32 v76, v76, v78
mm a[232:235],v[192:195],v[176:179],a[232:235]
s_xor_b32 m0, s82, 2048
v_add_f32 v77, v77, v79
mm a[232:235],v[196:199],v[180:183],a[232:235]
ld v226,s[40:43],s79 offen lds
mm a[236:239],v[204:207],v[180:183],a[236:239]
v_add_f32 v72, v72, v76
v_add_f32 v73, v73, v77
mm a[236:239],v[200:203],v[176:179],a[236:239]
rt v[40:41],v241 offset:128
rt v[42:43],v241 offset:1152
mm a[152:155],v[208:211],v[176:179],a[152:155]
s_xor_b32 m0, s82, 3072
v_mul_f32 v72, 0x3a800000, v72
mm a[152:155],v[212:215],v[180:183],a[152:155]
ld v227,s[40:43],s79 offen lds
mm a[156:159],v[220:223],v[180:183],a[156:159]
v_mul_f32 v73, 0x3a800000, v73
v_add_f32 v124, v70, v72
mm a[156:159],v[216:219],v[176:179],a[156:159]
v_fma_f32 v125, -v72, v72, v73
cv v124,v124,v124
mm a[172:175],v[216:219],v[184:187],a[172:175]
s_xor_b32 m0, s82, 4096
v_lshlrev_b32 v124, 16, v124
mm a[172:175],v[220:223],v[188:191],a[172:175]
ld v228,s[40:43],s79 offen lds
mm a[168:171],v[212:215],v[188:191],a[168:171]
v_max_f32 v125, 0, v125
cv v125,v125,v125
mm a[168:171],v[208:211],v[184:187],a[168:171]
rt v[44:45],v241 offset:17024
rt v[46:47],v241 offset:18048
mm a[220:223],v[200:203],v[184:187],a[220:223]
s_xor_b32 m0, s82, 5120
v_lshlrev_b32 v125, 16, v125
mm a[220:223],v[204:207],v[188:191],a[220:223]
ld v229,s[40:43],s79 offen lds
mm a[216:219],v[196:199],v[188:191],a[216:219]
v_add_f32 v125, 0x3727c5ac, v125
cv v125,v125,v125
mm a[216:219],v[192:195],v[184:187],a[216:219]
v_lshlrev_b32 v125, 16, v125
mm a[200:203],v[192:195],v[0:3],a[200:203]
s_xor_b32 m0, s82, 6144
mm a[200:203],v[196:199],v[4:7],a[200:203]
ld v230,s[40:43],s79 offen lds
mm a[204:207],v[204:207],v[4:7],a[204:207]
v_sqrt_f32 v125, v125
mm a[204:207],v[200:203],v[0:3],a[204:207]
rt v[48:49],v242 offset:128
rt v[50:51],v242 offset:1152
mm a[184:187],v[208:211],v[0:3],a[184:187]
s_xor_b32 m0, s82, 7168
v_sub_f32 v124, v70, v124
mm a[184:187],v[212:215],v[4:7],a[184:187]
ld v231,s[40:43],s79 offen lds
mm a[188:191],v[220:223],v[4:7],a[188:191]
s_mov_b32 s79, 128
cv v125,v125,v125
mm a[188:191],v[216:219],v[0:3],a[188:191]
v_lshlrev_b32 v125, 16, v125
mm a[12:15],v[216:219],v[8:11],a[12:15]
v_rcp_f32 v125, v125
mm a[12:15],v[220:223],v[12:15],a[12:15]
v_mul_f32 v125, 0x3e0293ee, v125
v_mul_f32 v124, v124, v125
mm a[8:11],v[212:215],v[12:15],a[8:11]
v_fma_f32 v88, v88, v125, v124
v_fma_f32 v89, v89, v125, v124
mm a[8:11],v[208:211],v[8:11],a[8:11]
rt v[52:53],v242 offset:17024
rt v[54:55],v242 offset:18048
mm a[76:79],v[200:203],v[8:11],a[76:79]
cv v72,v88,v89
v_fma_f32 v90, v90, v125, v124
mm a[76:79],v[204:207],v[12:15],a[76:79]
v_fma_f32 v91, v91, v125, v124
cv v73,v90,v91
mm a[72:75],v[196:199],v[12:15],a[72:75]
v_fma_f32 v92, v92, v125, v124
v_fma_f32 v93, v93, v125, v124
mm a[72:75],v[192:195],v[8:11],a[72:75]
rt v[56:57],v243 offset:128
rt v[58:59],v243 offset:1152
mm a[88:91],v[192:195],v[16:19],a[88:91]
cv v74,v92,v93
v_fma_f32 v94, v94, v125, v124
mm a[88:91],v[196:199],v[20:23],a[88:91]
v_fma_f32 v95, v95, v125, v124
cv v75,v94,v95
mm a[92:95],v[204:207],v[20:23],a[92:95]
st v[72:75],v71,s[56:59],0 offen offset:0 sc1
mm a[92:95],v[200:203],v[16:19],a[92:95]
rt v[60:61],v243 offset:17024
rt v[62:63],v243 offset:18048
mm a[24:27],v[208:211],v[16:19],a[24:27]
v_fma_f32 v96, v96, v125, v124
v_fma_f32 v97, v97, v125, v124
mm a[24:27],v[212:215],v[20:23],a[24:27]
cv v76,v96,v97
v_fma_f32 v98, v98, v125, v124
mm a[28:31],v[220:223],v[20:23],a[28:31]
v_fma_f32 v99, v99, v125, v124
cv v77,v98,v99
mm a[28:31],v[216:219],v[16:19],a[28:31]
v_fma_f32 v100, v100, v125, v124
v_fma_f32 v101, v101, v125, v124
mm a[44:47],v[216:219],v[24:27],a[44:47]
cv v78,v100,v101
v_fma_f32 v102, v102, v125, v124
mm a[44:47],v[220:223],v[28:31],a[44:47]
v_fma_f32 v103, v103, v125, v124
cv v79,v102,v103
mm a[40:43],v[212:215],v[28:31],a[40:43]
st v[76:79],v71,s[56:59],0 offen offset:2048 sc1
mm a[40:43],v[208:211],v[24:27],a[40:43]
v_fma_f32 v104, v104, v125, v124
v_fma_f32 v105, v105, v125, v124
mm a[252:255],v[200:203],v[24:27],a[252:255]
cv v80,v104,v105
v_fma_f32 v106, v106, v125, v124
mm a[252:255],v[204:207],v[28:31],a[252:255]
v_fma_f32 v107, v107, v125, v124
cv v81,v106,v107
mm a[248:251],v[196:199],v[28:31],a[248:251]
wt vmcnt(11) lgkmcnt(0)
s_barrier
mm a[248:251],v[192:195],v[24:27],a[248:251]
v_fma_f32 v108, v108, v125, v124
v_fma_f32 v109, v109, v125, v124
mm a[112:115],v[32:35],v[160:163],a[112:115]
cv v82,v108,v109
v_fma_f32 v110, v110, v125, v124
mm a[112:115],v[36:39],v[164:167],a[112:115]
v_fma_f32 v111, v111, v125, v124
cv v83,v110,v111
mm a[116:119],v[44:47],v[164:167],a[116:119]
rt v[192:193],v240 offset:33792
rt v[194:195],v240 offset:34816
mm a[116:119],v[40:43],v[160:163],a[116:119]
rt v[196:197],v240 offset:50688
rt v[198:199],v240 offset:51712
mm a[128:131],v[48:51],v[160:163],a[128:131]
st v[80:83],v71,s[56:59],s88 offen offset:0 sc1
mm a[128:131],v[52:55],v[164:167],a[128:131]
v_fma_f32 v112, v112, v125, v124
v_fma_f32 v113, v113, v125, v124
mm a[132:135],v[60:63],v[164:167],a[132:135]
cv v84,v112,v113
v_fma_f32 v114, v114, v125, v124
mm a[132:135],v[56:59],v[160:163],a[132:135]
rd v[160:163],v236 offset:57344
rd v[164:167],v237 offset:57344
mm a[148:151],v[56:59],v[168:171],a[148:151]
v_fma_f32 v115, v115, v125, v124
cv v85,v114,v115
mm a[148:151],v[60:63],v[172:175],a[148:151]
v_fma_f32 v116, v116, v125, v124
v_fma_f32 v117, v117, v125, v124
mm a[144:147],v[52:55],v[172:175],a[144:147]
cv v86,v116,v117
v_fma_f32 v118, v118, v125, v124
mm a[144:147],v[48:51],v[168:171],a[144:147]
rt v[200:201],v241 offset:33792
rt v[202:203],v241 offset:34816
mm a[228:231],v[40:43],v[168:171],a[228:231]
v_fma_f32 v119, v119, v125, v124
cv v87,v118,v119
mm a[228:231],v[44:47],v[172:175],a[228:231]
st v[84:87],v71,s[56:59],s88 offen offset:2048 sc1
mm a[224:227],v[36:39],v[172:175],a[224:227]
mm a[224:227],v[32:35],v[168:171],a[224:227]
rd v[168:171],v236 offset:59392
rd v[172:175],v237 offset:59392
mm a[208:211],v[32:35],v[176:179],a[208:211]
mm a[208:211],v[36:39],v[180:183],a[208:211]
mm a[212:215],v[44:47],v[180:183],a[212:215]
mm a[212:215],v[40:43],v[176:179],a[212:215]
rt v[204:205],v241 offset:50688
rt v[206:207],v241 offset:51712
mm a[160:163],v[48:51],v[176:179],a[160:163]
mm a[160:163],v[52:55],v[180:183],a[160:163]
mm a[164:167],v[60:63],v[180:183],a[164:167]
mm a[164:167],v[56:59],v[176:179],a[164:167]
rd v[176:179],v236 offset:49152
rd v[180:183],v237 offset:49152
mm a[180:183],v[56:59],v[184:187],a[180:183]
mm a[180:183],v[60:63],v[188:191],a[180:183]
mm a[176:179],v[52:55],v[188:191],a[176:179]
mm a[176:179],v[48:51],v[184:187],a[176:179]
rt v[208:209],v242 offset:33792
rt v[210:211],v242 offset:34816
mm a[196:199],v[40:43],v[184:187],a[196:199]
s_add_u32 m0, s83, 0
mm a[196:199],v[44:47],v[188:191],a[196:199]
ld v232,s[44:47],s80 offen lds
.La5_LW1_1:
s_cbranch_execz .La5_LW1_5
mm a[192:195],v[36:39],v[188:191],a[192:195]
mm a[192:195],v[32:35],v[184:187],a[192:195]
rd v[184:187],v236 offset:51200
rd v[188:191],v237 offset:51200
mm a[64:67],v[32:35],v[0:3],a[64:67]
s_add_u32 m0, s83, 1024
mm a[64:67],v[36:39],v[4:7],a[64:67]
ld v233,s[44:47],s80 offen lds
mm a[68:71],v[44:47],v[4:7],a[68:71]
mm a[68:71],v[40:43],v[0:3],a[68:71]
rt v[212:213],v242 offset:50688
rt v[214:215],v242 offset:51712
mm a[0:3],v[48:51],v[0:3],a[0:3]
s_add_u32 m0, s83, 2176
mm a[0:3],v[52:55],v[4:7],a[0:3]
ld v234,s[44:47],s80 offen lds
mm a[4:7],v[60:63],v[4:7],a[4:7]
mm a[4:7],v[56:59],v[0:3],a[4:7]
rd v[0:3],v236 offset:40960
rd v[4:7],v237 offset:40960
mm a[20:23],v[56:59],v[8:11],a[20:23]
s_add_u32 m0, s83, 3200
mm a[20:23],v[60:63],v[12:15],a[20:23]
ld v235,s[44:47],s80 offen lds
mm a[16:19],v[52:55],v[12:15],a[16:19]
s_mov_b32 s80, 262144
mm a[16:19],v[48:51],v[8:11],a[16:19]
rt v[216:217],v243 offset:33792
rt v[218:219],v243 offset:34816
mm a[84:87],v[40:43],v[8:11],a[84:87]
s_add_u32 m0, s83, 16896
mm a[84:87],v[44:47],v[12:15],a[84:87]
ld v232,s[44:47],s80 offen lds
mm a[80:83],v[36:39],v[12:15],a[80:83]
mm a[80:83],v[32:35],v[8:11],a[80:83]
rd v[8:11],v236 offset:43008
rd v[12:15],v237 offset:43008
mm a[240:243],v[32:35],v[16:19],a[240:243]
s_add_u32 m0, s83, 17920
mm a[240:243],v[36:39],v[20:23],a[240:243]
ld v233,s[44:47],s80 offen lds
mm a[244:247],v[44:47],v[20:23],a[244:247]
mm a[244:247],v[40:43],v[16:19],a[244:247]
rt v[220:221],v243 offset:50688
rt v[222:223],v243 offset:51712
mm a[32:35],v[48:51],v[16:19],a[32:35]
s_add_u32 m0, s83, 19072
mm a[32:35],v[52:55],v[20:23],a[32:35]
ld v234,s[44:47],s80 offen lds
mm a[36:39],v[60:63],v[20:23],a[36:39]
mm a[36:39],v[56:59],v[16:19],a[36:39]
rd v[16:19],v236 offset:32768
rd v[20:23],v237 offset:32768
mm a[60:63],v[56:59],v[24:27],a[60:63]
s_add_u32 m0, s83, 20096
mm a[60:63],v[60:63],v[28:31],a[60:63]
ld v235,s[44:47],s80 offen lds
mm a[56:59],v[52:55],v[28:31],a[56:59]
s_mov_b32 s80, 524288
mm a[56:59],v[48:51],v[24:27],a[56:59]
mm a[52:55],v[40:43],v[24:27],a[52:55]
mm a[52:55],v[44:47],v[28:31],a[52:55]
mm a[48:51],v[36:39],v[28:31],a[48:51]
wt lgkmcnt(2)
mm a[48:51],v[32:35],v[24:27],a[48:51]
rd v[24:27],v236 offset:34816
rd v[28:31],v237 offset:34816
mm a[96:99],v[192:195],v[160:163],a[96:99]
mm a[96:99],v[196:199],v[164:167],a[96:99]
mm a[100:103],v[204:207],v[164:167],a[100:103]
mm a[100:103],v[200:203],v[160:163],a[100:103]
rt v[32:33],v240 offset:33920
rt v[34:35],v240 offset:34944
mm a[104:107],v[208:211],v[160:163],a[104:107]
mm a[104:107],v[212:215],v[164:167],a[104:107]
mm a[108:111],v[220:223],v[164:167],a[108:111]
wt lgkmcnt(2)
s_barrier
mm a[108:111],v[216:219],v[160:163],a[108:111]
mm a[140:143],v[216:219],v[168:171],a[140:143]
s_xor_b32 m0, s82, 32768
mm a[140:143],v[220:223],v[172:175],a[140:143]
ld v224,s[40:43],s79 offen lds
mm a[136:139],v[212:215],v[172:175],a[136:139]
mm a[136:139],v[208:211],v[168:171],a[136:139]
rt v[36:37],v240 offset:50816
rt v[38:39],v240 offset:51840
mm a[124:127],v[200:203],v[168:171],a[124:127]
s_xor_b32 m0, s82, 33792
mm a[124:127],v[204:207],v[172:175],a[124:127]
ld v225,s[40:43],s79 offen lds
mm a[120:123],v[196:199],v[172:175],a[120:123]
mm a[120:123],v[192:195],v[168:171],a[120:123]
mm a[232:235],v[192:195],v[176:179],a[232:235]
s_xor_b32 m0, s82, 34816
mm a[232:235],v[196:199],v[180:183],a[232:235]
ld v226,s[40:43],s79 offen lds
mm a[236:239],v[204:207],v[180:183],a[236:239]
mm a[236:239],v[200:203],v[176:179],a[236:239]
rt v[40:41],v241 offset:33920
rt v[42:43],v241 offset:34944
mm a[152:155],v[208:211],v[176:179],a[152:155]
s_xor_b32 m0, s82, 35840
mm a[152:155],v[212:215],v[180:183],a[152:155]
ld v227,s[40:43],s79 offen lds
mm a[156:159],v[220:223],v[180:183],a[156:159]
mm a[156:159],v[216:219],v[176:179],a[156:159]
mm a[172:175],v[216:219],v[184:187],a[172:175]
s_xor_b32 m0, s82, 36864
mm a[172:175],v[220:223],v[188:191],a[172:175]
ld v228,s[40:43],s79 offen lds
mm a[168:171],v[212:215],v[188:191],a[168:171]
mm a[168:171],v[208:211],v[184:187],a[168:171]
rt v[44:45],v241 offset:50816
rt v[46:47],v241 offset:51840
mm a[220:223],v[200:203],v[184:187],a[220:223]
s_xor_b32 m0, s82, 37888
mm a[220:223],v[204:207],v[188:191],a[220:223]
ld v229,s[40:43],s79 offen lds
mm a[216:219],v[196:199],v[188:191],a[216:219]
mm a[216:219],v[192:195],v[184:187],a[216:219]
mm a[200:203],v[192:195],v[0:3],a[200:203]
s_xor_b32 m0, s82, 38912
mm a[200:203],v[196:199],v[4:7],a[200:203]
ld v230,s[40:43],s79 offen lds
mm a[204:207],v[204:207],v[4:7],a[204:207]
mm a[204:207],v[200:203],v[0:3],a[204:207]
rt v[48:49],v242 offset:33920
rt v[50:51],v242 offset:34944
mm a[184:187],v[208:211],v[0:3],a[184:187]
s_xor_b32 m0, s82, 39936
mm a[184:187],v[212:215],v[4:7],a[184:187]
ld v231,s[40:43],s79 offen lds
mm a[188:191],v[220:223],v[4:7],a[188:191]
s_mov_b32 s79, 256
mm a[188:191],v[216:219],v[0:3],a[188:191]
mm a[12:15],v[216:219],v[8:11],a[12:15]
mm a[12:15],v[220:223],v[12:15],a[12:15]
buffer_load_dword v254, v135, s[68:71], s96 offen
mm a[8:11],v[212:215],v[12:15],a[8:11]
s_add_u32 s96, s96, 1048576
mm a[8:11],v[208:211],v[8:11],a[8:11]
rt v[52:53],v242 offset:50816
rt v[54:55],v242 offset:51840
mm a[76:79],v[200:203],v[8:11],a[76:79]
mm a[76:79],v[204:207],v[12:15],a[76:79]
buffer_load_dword v254, v135, s[68:71], s96 offen
mm a[72:75],v[196:199],v[12:15],a[72:75]
s_add_u32 s96, s96, 1048576
mm a[72:75],v[192:195],v[8:11],a[72:75]
rt v[56:57],v243 offset:33920
rt v[58:59],v243 offset:34944
mm a[88:91],v[192:195],v[16:19],a[88:91]
mm a[88:91],v[196:199],v[20:23],a[88:91]
wt vmcnt(18)
buffer_store_dwordx2 v[252:253], off, s[64:67], s91 offset:1024 sc1
mm a[92:95],v[204:207],v[20:23],a[92:95]
s_mov_b32 s91, 0
s_lshl_b32 s92, s76, 19
mm a[92:95],v[200:203],v[16:19],a[92:95]
rt v[60:61],v243 offset:50816
rt v[62:63],v243 offset:51840
mm a[24:27],v[208:211],v[16:19],a[24:27]
s_add_u32 s92, s92, 88080384
mm a[24:27],v[212:215],v[20:23],a[24:27]
s_add_u32 s52, s26, s92
s_addc_u32 s53, s27, 0
mm a[28:31],v[220:223],v[20:23],a[28:31]
s_and_b32 s53, s53, 0xffff
s_mov_b32 s54, 524672
mm a[28:31],v[216:219],v[16:19],a[28:31]
s_mov_b32 s55, 0x20000
s_lshl_b32 s92, s3, 16
mm a[44:47],v[216:219],v[24:27],a[44:47]
s_sub_u32 s93, 458752, s92
v_add_u32 v244, s92, v136
mm a[44:47],v[220:223],v[28:31],a[44:47]
v_add_u32 v246, s93, v136
v_add_u32 v245, 0x8000, v244
mm a[40:43],v[212:215],v[28:31],a[40:43]
v_add_u32 v247, 0x8000, v246
s_lshl_b32 s94, s72, 19
mm a[40:43],v[208:211],v[24:27],a[40:43]
s_lshl_b32 s93, s76, 16
s_add_u32 s92, s94, s93
mm a[252:255],v[200:203],v[24:27],a[252:255]
s_lshl_b32 s93, s77, 13
s_add_u32 s92, s92, s93
mm a[252:255],v[204:207],v[28:31],a[252:255]
s_add_u32 s92, s92, 20971520
mm a[248:251],v[196:199],v[28:31],a[248:251]
wt vmcnt(11) lgkmcnt(0)
s_barrier
mm a[248:251],v[192:195],v[24:27],a[248:251]
s_add_u32 s48, s26, s92
s_addc_u32 s49, s27, 0
mm a[112:115],v[32:35],v[160:163],a[112:115]
s_and_b32 s49, s49, 0xffff
s_mov_b32 s50, 57344
mm a[112:115],v[36:39],v[164:167],a[112:115]
s_mov_b32 s51, 0x20000
s_lshl_b32 s93, s77, 24
mm a[116:119],v[44:47],v[164:167],a[116:119]
rt v[192:193],v240 offset:0
rt v[194:195],v240 offset:1024
mm a[116:119],v[40:43],v[160:163],a[116:119]
rt v[196:197],v240 offset:16896
rt v[198:199],v240 offset:17920
mm a[128:131],v[48:51],v[160:163],a[128:131]
s_add_u32 s92, s94, s93
s_add_u32 s92, s92, 20971520
mm a[128:131],v[52:55],v[164:167],a[128:131]
s_add_u32 s60, s26, s92
s_addc_u32 s61, s27, 0
mm a[132:135],v[60:63],v[164:167],a[132:135]
s_and_b32 s61, s61, 0xffff
s_mov_b32 s62, 524288
mm a[132:135],v[56:59],v[160:163],a[132:135]
rd v[160:163],v236 offset:24576
rd v[164:167],v237 offset:24576
mm a[148:151],v[56:59],v[168:171],a[148:151]
s_mov_b32 s63, 0x20000
s_lshl_b32 s92, s77, 14
mm a[148:151],v[60:63],v[172:175],a[148:151]
s_sub_u32 s95, 16384, s92
v_add_u32 v238, s95, v255
mm a[144:147],v[52:55],v[172:175],a[144:147]
mm a[144:147],v[48:51],v[168:171],a[144:147]
rt v[200:201],v241 offset:0
rt v[202:203],v241 offset:1024
mm a[228:231],v[40:43],v[168:171],a[228:231]
mm a[228:231],v[44:47],v[172:175],a[228:231]
mm a[224:227],v[36:39],v[172:175],a[224:227]
mm a[224:227],v[32:35],v[168:171],a[224:227]
rd v[168:171],v236 offset:26624
rd v[172:175],v237 offset:26624
mm a[208:211],v[32:35],v[176:179],a[208:211]
mm a[208:211],v[36:39],v[180:183],a[208:211]
mm a[212:215],v[44:47],v[180:183],a[212:215]
mm a[212:215],v[40:43],v[176:179],a[212:215]
rt v[204:205],v241 offset:16896
rt v[206:207],v241 offset:17920
mm a[160:163],v[48:51],v[176:179],a[160:163]
mm a[160:163],v[52:55],v[180:183],a[160:163]
mm a[164:167],v[60:63],v[180:183],a[164:167]
mm a[164:167],v[56:59],v[176:179],a[164:167]
rd v[176:179],v236 offset:16384
rd v[180:183],v237 offset:16384
mm a[180:183],v[56:59],v[184:187],a[180:183]
mm a[180:183],v[60:63],v[188:191],a[180:183]
mm a[176:179],v[52:55],v[188:191],a[176:179]
mm a[176:179],v[48:51],v[184:187],a[176:179]
rt v[208:209],v242 offset:0
rt v[210:211],v242 offset:1024
mm a[196:199],v[40:43],v[184:187],a[196:199]
s_add_u32 m0, s83, 33792
mm a[196:199],v[44:47],v[188:191],a[196:199]
ld v232,s[44:47],s80 offen lds
mm a[192:195],v[36:39],v[188:191],a[192:195]
mm a[192:195],v[32:35],v[184:187],a[192:195]
rd v[184:187],v236 offset:18432
rd v[188:191],v237 offset:18432
mm a[64:67],v[32:35],v[0:3],a[64:67]
s_add_u32 m0, s83, 34816
mm a[64:67],v[36:39],v[4:7],a[64:67]
ld v233,s[44:47],s80 offen lds
mm a[68:71],v[44:47],v[4:7],a[68:71]
mm a[68:71],v[40:43],v[0:3],a[68:71]
rt v[212:213],v242 offset:16896
rt v[214:215],v242 offset:17920
mm a[0:3],v[48:51],v[0:3],a[0:3]
s_add_u32 m0, s83, 35968
mm a[0:3],v[52:55],v[4:7],a[0:3]
ld v234,s[44:47],s80 offen lds
mm a[4:7],v[60:63],v[4:7],a[4:7]
mm a[4:7],v[56:59],v[0:3],a[4:7]
rd v[0:3],v236 offset:8192
rd v[4:7],v237 offset:8192
mm a[20:23],v[56:59],v[8:11],a[20:23]
s_add_u32 m0, s83, 36992
mm a[20:23],v[60:63],v[12:15],a[20:23]
ld v235,s[44:47],s80 offen lds
mm a[16:19],v[52:55],v[12:15],a[16:19]
s_mov_b32 s80, 786432
mm a[16:19],v[48:51],v[8:11],a[16:19]
rt v[216:217],v243 offset:0
rt v[218:219],v243 offset:1024
mm a[84:87],v[40:43],v[8:11],a[84:87]
s_add_u32 m0, s83, 50688
mm a[84:87],v[44:47],v[12:15],a[84:87]
ld v232,s[44:47],s80 offen lds
mm a[80:83],v[36:39],v[12:15],a[80:83]
mm a[80:83],v[32:35],v[8:11],a[80:83]
rd v[8:11],v236 offset:10240
rd v[12:15],v237 offset:10240
mm a[240:243],v[32:35],v[16:19],a[240:243]
s_add_u32 m0, s83, 51712
mm a[240:243],v[36:39],v[20:23],a[240:243]
ld v233,s[44:47],s80 offen lds
mm a[244:247],v[44:47],v[20:23],a[244:247]
mm a[244:247],v[40:43],v[16:19],a[244:247]
rt v[220:221],v243 offset:16896
rt v[222:223],v243 offset:17920
mm a[32:35],v[48:51],v[16:19],a[32:35]
s_add_u32 m0, s83, 52864
mm a[32:35],v[52:55],v[20:23],a[32:35]
ld v234,s[44:47],s80 offen lds
mm a[36:39],v[60:63],v[20:23],a[36:39]
mm a[36:39],v[56:59],v[16:19],a[36:39]
rd v[16:19],v236 offset:0
rd v[20:23],v237 offset:0
mm a[60:63],v[56:59],v[24:27],a[60:63]
s_add_u32 m0, s83, 53888
mm a[60:63],v[60:63],v[28:31],a[60:63]
ld v235,s[44:47],s80 offen lds
mm a[56:59],v[52:55],v[28:31],a[56:59]
s_mov_b32 s80, 1048576
mm a[56:59],v[48:51],v[24:27],a[56:59]
mm a[52:55],v[40:43],v[24:27],a[52:55]
mm a[52:55],v[44:47],v[28:31],a[52:55]
mm a[48:51],v[36:39],v[28:31],a[48:51]
wt lgkmcnt(2)
mm a[48:51],v[32:35],v[24:27],a[48:51]
rd v[24:27],v236 offset:2048
rd v[28:31],v237 offset:2048
mm a[96:99],v[192:195],v[160:163],a[96:99]
mm a[96:99],v[196:199],v[164:167],a[96:99]
mm a[100:103],v[204:207],v[164:167],a[100:103]
mm a[100:103],v[200:203],v[160:163],a[100:103]
rt v[32:33],v240 offset:128
rt v[34:35],v240 offset:1152
mm a[104:107],v[208:211],v[160:163],a[104:107]
mm a[104:107],v[212:215],v[164:167],a[104:107]
mm a[108:111],v[220:223],v[164:167],a[108:111]
wt lgkmcnt(2)
s_barrier
mm a[108:111],v[216:219],v[160:163],a[108:111]
mm a[140:143],v[216:219],v[168:171],a[140:143]
s_xor_b32 m0, s82, 0
mm a[140:143],v[220:223],v[172:175],a[140:143]
ld v224,s[40:43],s79 offen lds
mm a[136:139],v[212:215],v[172:175],a[136:139]
mm a[136:139],v[208:211],v[168:171],a[136:139]
rt v[36:37],v240 offset:17024
rt v[38:39],v240 offset:18048
mm a[124:127],v[200:203],v[168:171],a[124:127]
s_xor_b32 m0, s82, 1024
mm a[124:127],v[204:207],v[172:175],a[124:127]
ld v225,s[40:43],s79 offen lds
mm a[120:123],v[196:199],v[172:175],a[120:123]
mm a[120:123],v[192:195],v[168:171],a[120:123]
mm a[232:235],v[192:195],v[176:179],a[232:235]
s_xor_b32 m0, s82, 2048
mm a[232:235],v[196:199],v[180:183],a[232:235]
ld v226,s[40:43],s79 offen lds
mm a[236:239],v[204:207],v[180:183],a[236:239]
mm a[236:239],v[200:203],v[176:179],a[236:239]
rt v[40:41],v241 offset:128
rt v[42:43],v241 offset:1152
mm a[152:155],v[208:211],v[176:179],a[152:155]
s_xor_b32 m0, s82, 3072
mm a[152:155],v[212:215],v[180:183],a[152:155]
ld v227,s[40:43],s79 offen lds
mm a[156:159],v[220:223],v[180:183],a[156:159]
mm a[156:159],v[216:219],v[176:179],a[156:159]
mm a[172:175],v[216:219],v[184:187],a[172:175]
s_xor_b32 m0, s82, 4096
mm a[172:175],v[220:223],v[188:191],a[172:175]
ld v228,s[40:43],s79 offen lds
mm a[168:171],v[212:215],v[188:191],a[168:171]
mm a[168:171],v[208:211],v[184:187],a[168:171]
rt v[44:45],v241 offset:17024
rt v[46:47],v241 offset:18048
mm a[220:223],v[200:203],v[184:187],a[220:223]
s_xor_b32 m0, s82, 5120
mm a[220:223],v[204:207],v[188:191],a[220:223]
ld v229,s[40:43],s79 offen lds
mm a[216:219],v[196:199],v[188:191],a[216:219]
mm a[216:219],v[192:195],v[184:187],a[216:219]
mm a[200:203],v[192:195],v[0:3],a[200:203]
s_xor_b32 m0, s82, 6144
mm a[200:203],v[196:199],v[4:7],a[200:203]
ld v230,s[40:43],s79 offen lds
mm a[204:207],v[204:207],v[4:7],a[204:207]
mm a[204:207],v[200:203],v[0:3],a[204:207]
rt v[48:49],v242 offset:128
rt v[50:51],v242 offset:1152
mm a[184:187],v[208:211],v[0:3],a[184:187]
s_xor_b32 m0, s82, 7168
mm a[184:187],v[212:215],v[4:7],a[184:187]
ld v231,s[40:43],s79 offen lds
mm a[188:191],v[220:223],v[4:7],a[188:191]
s_mov_b32 s79, 384
mm a[188:191],v[216:219],v[0:3],a[188:191]
mm a[12:15],v[216:219],v[8:11],a[12:15]
mm a[12:15],v[220:223],v[12:15],a[12:15]
buffer_load_dword v254, v135, s[68:71], s96 offen
mm a[8:11],v[212:215],v[12:15],a[8:11]
s_add_u32 s96, s96, 1048576
mm a[8:11],v[208:211],v[8:11],a[8:11]
rt v[52:53],v242 offset:17024
rt v[54:55],v242 offset:18048
mm a[76:79],v[200:203],v[8:11],a[76:79]
mm a[76:79],v[204:207],v[12:15],a[76:79]
buffer_load_dword v254, v135, s[68:71], s96 offen
mm a[72:75],v[196:199],v[12:15],a[72:75]
s_add_u32 s96, s96, 1048576
mm a[72:75],v[192:195],v[8:11],a[72:75]
rt v[56:57],v243 offset:128
rt v[58:59],v243 offset:1152
mm a[88:91],v[192:195],v[16:19],a[88:91]
mm a[88:91],v[196:199],v[20:23],a[88:91]
mm a[92:95],v[204:207],v[20:23],a[92:95]
mm a[92:95],v[200:203],v[16:19],a[92:95]
rt v[60:61],v243 offset:17024
rt v[62:63],v243 offset:18048
mm a[24:27],v[208:211],v[16:19],a[24:27]
mm a[24:27],v[212:215],v[20:23],a[24:27]
mm a[28:31],v[220:223],v[20:23],a[28:31]
mm a[28:31],v[216:219],v[16:19],a[28:31]
mm a[44:47],v[216:219],v[24:27],a[44:47]
mm a[44:47],v[220:223],v[28:31],a[44:47]
mm a[40:43],v[212:215],v[28:31],a[40:43]
mm a[40:43],v[208:211],v[24:27],a[40:43]
mm a[252:255],v[200:203],v[24:27],a[252:255]
mm a[252:255],v[204:207],v[28:31],a[252:255]
mm a[248:251],v[196:199],v[28:31],a[248:251]
wt vmcnt(10) lgkmcnt(0)
s_barrier
mm a[248:251],v[192:195],v[24:27],a[248:251]
mm a[112:115],v[32:35],v[160:163],a[112:115]
mm a[112:115],v[36:39],v[164:167],a[112:115]
mm a[116:119],v[44:47],v[164:167],a[116:119]
rt v[192:193],v240 offset:33792
rt v[194:195],v240 offset:34816
mm a[116:119],v[40:43],v[160:163],a[116:119]
rt v[196:197],v240 offset:50688
rt v[198:199],v240 offset:51712
mm a[128:131],v[48:51],v[160:163],a[128:131]
mm a[128:131],v[52:55],v[164:167],a[128:131]
mm a[132:135],v[60:63],v[164:167],a[132:135]
mm a[132:135],v[56:59],v[160:163],a[132:135]
rd v[160:163],v236 offset:57344
rd v[164:167],v237 offset:57344
mm a[148:151],v[56:59],v[168:171],a[148:151]
mm a[148:151],v[60:63],v[172:175],a[148:151]
mm a[144:147],v[52:55],v[172:175],a[144:147]
mm a[144:147],v[48:51],v[168:171],a[144:147]
rt v[200:201],v241 offset:33792
rt v[202:203],v241 offset:34816
mm a[228:231],v[40:43],v[168:171],a[228:231]
mm a[228:231],v[44:47],v[172:175],a[228:231]
mm a[224:227],v[36:39],v[172:175],a[224:227]
mm a[224:227],v[32:35],v[168:171],a[224:227]
rd v[168:171],v236 offset:59392
rd v[172:175],v237 offset:59392
mm a[208:211],v[32:35],v[176:179],a[208:211]
mm a[208:211],v[36:39],v[180:183],a[208:211]
mm a[212:215],v[44:47],v[180:183],a[212:215]
mm a[212:215],v[40:43],v[176:179],a[212:215]
rt v[204:205],v241 offset:50688
rt v[206:207],v241 offset:51712
mm a[160:163],v[48:51],v[176:179],a[160:163]
mm a[160:163],v[52:55],v[180:183],a[160:163]
mm a[164:167],v[60:63],v[180:183],a[164:167]
mm a[164:167],v[56:59],v[176:179],a[164:167]
rd v[176:179],v236 offset:49152
rd v[180:183],v237 offset:49152
mm a[180:183],v[56:59],v[184:187],a[180:183]
mm a[180:183],v[60:63],v[188:191],a[180:183]
mm a[176:179],v[52:55],v[188:191],a[176:179]
mm a[176:179],v[48:51],v[184:187],a[176:179]
rt v[208:209],v242 offset:33792
rt v[210:211],v242 offset:34816
mm a[196:199],v[40:43],v[184:187],a[196:199]
s_add_u32 m0, s83, 0
mm a[196:199],v[44:47],v[188:191],a[196:199]
ld v232,s[44:47],s80 offen lds
mm a[192:195],v[36:39],v[188:191],a[192:195]
mm a[192:195],v[32:35],v[184:187],a[192:195]
rd v[184:187],v236 offset:51200
rd v[188:191],v237 offset:51200
mm a[64:67],v[32:35],v[0:3],a[64:67]
s_add_u32 m0, s83, 1024
mm a[64:67],v[36:39],v[4:7],a[64:67]
ld v233,s[44:47],s80 offen lds
mm a[68:71],v[44:47],v[4:7],a[68:71]
mm a[68:71],v[40:43],v[0:3],a[68:71]
rt v[212:213],v242 offset:50688
rt v[214:215],v242 offset:51712
mm a[0:3],v[48:51],v[0:3],a[0:3]
s_add_u32 m0, s83, 2176
mm a[0:3],v[52:55],v[4:7],a[0:3]
ld v234,s[44:47],s80 offen lds
mm a[4:7],v[60:63],v[4:7],a[4:7]
mm a[4:7],v[56:59],v[0:3],a[4:7]
rd v[0:3],v236 offset:40960
rd v[4:7],v237 offset:40960
mm a[20:23],v[56:59],v[8:11],a[20:23]
s_add_u32 m0, s83, 3200
mm a[20:23],v[60:63],v[12:15],a[20:23]
ld v235,s[44:47],s80 offen lds
mm a[16:19],v[52:55],v[12:15],a[16:19]
s_mov_b32 s80, 1310720
mm a[16:19],v[48:51],v[8:11],a[16:19]
rt v[216:217],v243 offset:33792
rt v[218:219],v243 offset:34816
mm a[84:87],v[40:43],v[8:11],a[84:87]
s_add_u32 m0, s83, 16896
mm a[84:87],v[44:47],v[12:15],a[84:87]
ld v232,s[44:47],s80 offen lds
mm a[80:83],v[36:39],v[12:15],a[80:83]
mm a[80:83],v[32:35],v[8:11],a[80:83]
rd v[8:11],v236 offset:43008
rd v[12:15],v237 offset:43008
mm a[240:243],v[32:35],v[16:19],a[240:243]
s_add_u32 m0, s83, 17920
mm a[240:243],v[36:39],v[20:23],a[240:243]
ld v233,s[44:47],s80 offen lds
mm a[244:247],v[44:47],v[20:23],a[244:247]
mm a[244:247],v[40:43],v[16:19],a[244:247]
rt v[220:221],v243 offset:50688
rt v[222:223],v243 offset:51712
mm a[32:35],v[48:51],v[16:19],a[32:35]
s_add_u32 m0, s83, 19072
mm a[32:35],v[52:55],v[20:23],a[32:35]
ld v234,s[44:47],s80 offen lds
mm a[36:39],v[60:63],v[20:23],a[36:39]
.La5_LW1_2:
s_cbranch_execz .La5_LW1_6
mm a[36:39],v[56:59],v[16:19],a[36:39]
rd v[16:19],v236 offset:32768
rd v[20:23],v237 offset:32768
mm a[60:63],v[56:59],v[24:27],a[60:63]
s_add_u32 m0, s83, 20096
mm a[60:63],v[60:63],v[28:31],a[60:63]
ld v235,s[44:47],s80 offen lds
mm a[56:59],v[52:55],v[28:31],a[56:59]
s_mov_b32 s80, 1572864
mm a[56:59],v[48:51],v[24:27],a[56:59]
mm a[52:55],v[40:43],v[24:27],a[52:55]
mm a[52:55],v[44:47],v[28:31],a[52:55]
mm a[48:51],v[36:39],v[28:31],a[48:51]
wt lgkmcnt(2)
mm a[48:51],v[32:35],v[24:27],a[48:51]
rd v[24:27],v236 offset:34816
rd v[28:31],v237 offset:34816
mm a[96:99],v[192:195],v[160:163],a[96:99]
mm a[96:99],v[196:199],v[164:167],a[96:99]
mm a[100:103],v[204:207],v[164:167],a[100:103]
mm a[100:103],v[200:203],v[160:163],a[100:103]
rt v[32:33],v240 offset:33920
rt v[34:35],v240 offset:34944
mm a[104:107],v[208:211],v[160:163],a[104:107]
mm a[104:107],v[212:215],v[164:167],a[104:107]
mm a[108:111],v[220:223],v[164:167],a[108:111]
wt lgkmcnt(2)
s_barrier
mm a[108:111],v[216:219],v[160:163],a[108:111]
mm a[140:143],v[216:219],v[168:171],a[140:143]
s_xor_b32 m0, s82, 32768
mm a[140:143],v[220:223],v[172:175],a[140:143]
ld v224,s[40:43],s79 offen lds
mm a[136:139],v[212:215],v[172:175],a[136:139]
mm a[136:139],v[208:211],v[168:171],a[136:139]
rt v[36:37],v240 offset:50816
rt v[38:39],v240 offset:51840
mm a[124:127],v[200:203],v[168:171],a[124:127]
s_xor_b32 m0, s82, 33792
mm a[124:127],v[204:207],v[172:175],a[124:127]
ld v225,s[40:43],s79 offen lds
mm a[120:123],v[196:199],v[172:175],a[120:123]
mm a[120:123],v[192:195],v[168:171],a[120:123]
mm a[232:235],v[192:195],v[176:179],a[232:235]
s_xor_b32 m0, s82, 34816
mm a[232:235],v[196:199],v[180:183],a[232:235]
ld v226,s[40:43],s79 offen lds
mm a[236:239],v[204:207],v[180:183],a[236:239]
mm a[236:239],v[200:203],v[176:179],a[236:239]
rt v[40:41],v241 offset:33920
rt v[42:43],v241 offset:34944
mm a[152:155],v[208:211],v[176:179],a[152:155]
s_xor_b32 m0, s82, 35840
mm a[152:155],v[212:215],v[180:183],a[152:155]
ld v227,s[40:43],s79 offen lds
mm a[156:159],v[220:223],v[180:183],a[156:159]
mm a[156:159],v[216:219],v[176:179],a[156:159]
mm a[172:175],v[216:219],v[184:187],a[172:175]
s_xor_b32 m0, s82, 36864
mm a[172:175],v[220:223],v[188:191],a[172:175]
ld v228,s[40:43],s79 offen lds
mm a[168:171],v[212:215],v[188:191],a[168:171]
mm a[168:171],v[208:211],v[184:187],a[168:171]
rt v[44:45],v241 offset:50816
rt v[46:47],v241 offset:51840
mm a[220:223],v[200:203],v[184:187],a[220:223]
s_xor_b32 m0, s82, 37888
mm a[220:223],v[204:207],v[188:191],a[220:223]
ld v229,s[40:43],s79 offen lds
mm a[216:219],v[196:199],v[188:191],a[216:219]
mm a[216:219],v[192:195],v[184:187],a[216:219]
mm a[200:203],v[192:195],v[0:3],a[200:203]
s_xor_b32 m0, s82, 38912
mm a[200:203],v[196:199],v[4:7],a[200:203]
ld v230,s[40:43],s79 offen lds
mm a[204:207],v[204:207],v[4:7],a[204:207]
mm a[204:207],v[200:203],v[0:3],a[204:207]
rt v[48:49],v242 offset:33920
rt v[50:51],v242 offset:34944
mm a[184:187],v[208:211],v[0:3],a[184:187]
s_xor_b32 m0, s82, 39936
mm a[184:187],v[212:215],v[4:7],a[184:187]
ld v231,s[40:43],s79 offen lds
mm a[188:191],v[220:223],v[4:7],a[188:191]
s_mov_b32 s79, 512
mm a[188:191],v[216:219],v[0:3],a[188:191]
mm a[12:15],v[216:219],v[8:11],a[12:15]
mm a[12:15],v[220:223],v[12:15],a[12:15]
buffer_load_dword v254, v135, s[68:71], s96 offen
mm a[8:11],v[212:215],v[12:15],a[8:11]
s_add_u32 s96, s96, 1048576
mm a[8:11],v[208:211],v[8:11],a[8:11]
rt v[52:53],v242 offset:50816
rt v[54:55],v242 offset:51840
mm a[76:79],v[200:203],v[8:11],a[76:79]
mm a[76:79],v[204:207],v[12:15],a[76:79]
buffer_load_dword v254, v135, s[68:71], s96 offen
mm a[72:75],v[196:199],v[12:15],a[72:75]
s_add_u32 s96, s96, 1048576
mm a[72:75],v[192:195],v[8:11],a[72:75]
rt v[56:57],v243 offset:33920
rt v[58:59],v243 offset:34944
mm a[88:91],v[192:195],v[16:19],a[88:91]
mm a[88:91],v[196:199],v[20:23],a[88:91]
mm a[92:95],v[204:207],v[20:23],a[92:95]
mm a[92:95],v[200:203],v[16:19],a[92:95]
rt v[60:61],v243 offset:50816
rt v[62:63],v243 offset:51840
mm a[24:27],v[208:211],v[16:19],a[24:27]
mm a[24:27],v[212:215],v[20:23],a[24:27]
mm a[28:31],v[220:223],v[20:23],a[28:31]
mm a[28:31],v[216:219],v[16:19],a[28:31]
mm a[44:47],v[216:219],v[24:27],a[44:47]
mm a[44:47],v[220:223],v[28:31],a[44:47]
mm a[40:43],v[212:215],v[28:31],a[40:43]
mm a[40:43],v[208:211],v[24:27],a[40:43]
mm a[252:255],v[200:203],v[24:27],a[252:255]
mm a[252:255],v[204:207],v[28:31],a[252:255]
mm a[248:251],v[196:199],v[28:31],a[248:251]
wt vmcnt(10) lgkmcnt(0)
s_barrier
mm a[248:251],v[192:195],v[24:27],a[248:251]
mm a[112:115],v[32:35],v[160:163],a[112:115]
mm a[112:115],v[36:39],v[164:167],a[112:115]
mm a[116:119],v[44:47],v[164:167],a[116:119]
rt v[192:193],v240 offset:0
rt v[194:195],v240 offset:1024
mm a[116:119],v[40:43],v[160:163],a[116:119]
rt v[196:197],v240 offset:16896
rt v[198:199],v240 offset:17920
mm a[128:131],v[48:51],v[160:163],a[128:131]
mm a[128:131],v[52:55],v[164:167],a[128:131]
mm a[132:135],v[60:63],v[164:167],a[132:135]
mm a[132:135],v[56:59],v[160:163],a[132:135]
rd v[160:163],v236 offset:24576
rd v[164:167],v237 offset:24576
mm a[148:151],v[56:59],v[168:171],a[148:151]
mm a[148:151],v[60:63],v[172:175],a[148:151]
mm a[144:147],v[52:55],v[172:175],a[144:147]
mm a[144:147],v[48:51],v[168:171],a[144:147]
rt v[200:201],v241 offset:0
rt v[202:203],v241 offset:1024
mm a[228:231],v[40:43],v[168:171],a[228:231]
mm a[228:231],v[44:47],v[172:175],a[228:231]
mm a[224:227],v[36:39],v[172:175],a[224:227]
mm a[224:227],v[32:35],v[168:171],a[224:227]
rd v[168:171],v236 offset:26624
rd v[172:175],v237 offset:26624
mm a[208:211],v[32:35],v[176:179],a[208:211]
mm a[208:211],v[36:39],v[180:183],a[208:211]
mm a[212:215],v[44:47],v[180:183],a[212:215]
mm a[212:215],v[40:43],v[176:179],a[212:215]
rt v[204:205],v241 offset:16896
rt v[206:207],v241 offset:17920
mm a[160:163],v[48:51],v[176:179],a[160:163]
mm a[160:163],v[52:55],v[180:183],a[160:163]
mm a[164:167],v[60:63],v[180:183],a[164:167]
mm a[164:167],v[56:59],v[176:179],a[164:167]
rd v[176:179],v236 offset:16384
rd v[180:183],v237 offset:16384
mm a[180:183],v[56:59],v[184:187],a[180:183]
mm a[180:183],v[60:63],v[188:191],a[180:183]
mm a[176:179],v[52:55],v[188:191],a[176:179]
mm a[176:179],v[48:51],v[184:187],a[176:179]
rt v[208:209],v242 offset:0
rt v[210:211],v242 offset:1024
mm a[196:199],v[40:43],v[184:187],a[196:199]
s_add_u32 m0, s83, 33792
mm a[196:199],v[44:47],v[188:191],a[196:199]
ld v232,s[44:47],s80 offen lds
mm a[192:195],v[36:39],v[188:191],a[192:195]
mm a[192:195],v[32:35],v[184:187],a[192:195]
rd v[184:187],v236 offset:18432
rd v[188:191],v237 offset:18432
mm a[64:67],v[32:35],v[0:3],a[64:67]
s_add_u32 m0, s83, 34816
mm a[64:67],v[36:39],v[4:7],a[64:67]
ld v233,s[44:47],s80 offen lds
mm a[68:71],v[44:47],v[4:7],a[68:71]
mm a[68:71],v[40:43],v[0:3],a[68:71]
rt v[212:213],v242 offset:16896
rt v[214:215],v242 offset:17920
mm a[0:3],v[48:51],v[0:3],a[0:3]
s_add_u32 m0, s83, 35968
mm a[0:3],v[52:55],v[4:7],a[0:3]
ld v234,s[44:47],s80 offen lds
mm a[4:7],v[60:63],v[4:7],a[4:7]
mm a[4:7],v[56:59],v[0:3],a[4:7]
rd v[0:3],v236 offset:8192
rd v[4:7],v237 offset:8192
mm a[20:23],v[56:59],v[8:11],a[20:23]
s_add_u32 m0, s83, 36992
mm a[20:23],v[60:63],v[12:15],a[20:23]
ld v235,s[44:47],s80 offen lds
mm a[16:19],v[52:55],v[12:15],a[16:19]
s_mov_b32 s80, 1835008
mm a[16:19],v[48:51],v[8:11],a[16:19]
rt v[216:217],v243 offset:0
rt v[218:219],v243 offset:1024
mm a[84:87],v[40:43],v[8:11],a[84:87]
s_add_u32 m0, s83, 50688
mm a[84:87],v[44:47],v[12:15],a[84:87]
ld v232,s[44:47],s80 offen lds
mm a[80:83],v[36:39],v[12:15],a[80:83]
mm a[80:83],v[32:35],v[8:11],a[80:83]
rd v[8:11],v236 offset:10240
rd v[12:15],v237 offset:10240
mm a[240:243],v[32:35],v[16:19],a[240:243]
s_add_u32 m0, s83, 51712
mm a[240:243],v[36:39],v[20:23],a[240:243]
ld v233,s[44:47],s80 offen lds
mm a[244:247],v[44:47],v[20:23],a[244:247]
mm a[244:247],v[40:43],v[16:19],a[244:247]
rt v[220:221],v243 offset:16896
rt v[222:223],v243 offset:17920
mm a[32:35],v[48:51],v[16:19],a[32:35]
s_add_u32 m0, s83, 52864
mm a[32:35],v[52:55],v[20:23],a[32:35]
ld v234,s[44:47],s80 offen lds
mm a[36:39],v[60:63],v[20:23],a[36:39]
mm a[36:39],v[56:59],v[16:19],a[36:39]
rd v[16:19],v236 offset:0
rd v[20:23],v237 offset:0
mm a[60:63],v[56:59],v[24:27],a[60:63]
s_add_u32 m0, s83, 53888
mm a[60:63],v[60:63],v[28:31],a[60:63]
ld v235,s[44:47],s80 offen lds
mm a[56:59],v[52:55],v[28:31],a[56:59]
s_mov_b64 s[44:45], s[68:69]
s_mov_b32 s46, s70
mm a[56:59],v[48:51],v[24:27],a[56:59]
mm a[52:55],v[40:43],v[24:27],a[52:55]
mm a[52:55],v[44:47],v[28:31],a[52:55]
mm a[48:51],v[36:39],v[28:31],a[48:51]
wt lgkmcnt(2)
s_lshl_b32 s80, s77, 18
mm a[48:51],v[32:35],v[24:27],a[48:51]
rd v[24:27],v236 offset:2048
rd v[28:31],v237 offset:2048
mm a[96:99],v[192:195],v[160:163],a[96:99]
mm a[96:99],v[196:199],v[164:167],a[96:99]
mm a[100:103],v[204:207],v[164:167],a[100:103]
mm a[100:103],v[200:203],v[160:163],a[100:103]
rt v[32:33],v240 offset:128
rt v[34:35],v240 offset:1152
mm a[104:107],v[208:211],v[160:163],a[104:107]
mm a[104:107],v[212:215],v[164:167],a[104:107]
mm a[108:111],v[220:223],v[164:167],a[108:111]
wt lgkmcnt(2)
s_barrier
mm a[108:111],v[216:219],v[160:163],a[108:111]
mm a[140:143],v[216:219],v[168:171],a[140:143]
mm a[140:143],v[220:223],v[172:175],a[140:143]
mm a[136:139],v[212:215],v[172:175],a[136:139]
mm a[136:139],v[208:211],v[168:171],a[136:139]
rt v[36:37],v240 offset:17024
rt v[38:39],v240 offset:18048
mm a[124:127],v[200:203],v[168:171],a[124:127]
mm a[124:127],v[204:207],v[172:175],a[124:127]
mm a[120:123],v[196:199],v[172:175],a[120:123]
mm a[120:123],v[192:195],v[168:171],a[120:123]
mm a[232:235],v[192:195],v[176:179],a[232:235]
mm a[232:235],v[196:199],v[180:183],a[232:235]
mm a[236:239],v[204:207],v[180:183],a[236:239]
mm a[236:239],v[200:203],v[176:179],a[236:239]
rt v[40:41],v241 offset:128
rt v[42:43],v241 offset:1152
mm a[152:155],v[208:211],v[176:179],a[152:155]
mm a[152:155],v[212:215],v[180:183],a[152:155]
mm a[156:159],v[220:223],v[180:183],a[156:159]
mm a[156:159],v[216:219],v[176:179],a[156:159]
mm a[172:175],v[216:219],v[184:187],a[172:175]
mm a[172:175],v[220:223],v[188:191],a[172:175]
mm a[168:171],v[212:215],v[188:191],a[168:171]
mm a[168:171],v[208:211],v[184:187],a[168:171]
rt v[44:45],v241 offset:17024
rt v[46:47],v241 offset:18048
mm a[220:223],v[200:203],v[184:187],a[220:223]
mm a[220:223],v[204:207],v[188:191],a[220:223]
mm a[216:219],v[196:199],v[188:191],a[216:219]
mm a[216:219],v[192:195],v[184:187],a[216:219]
mm a[200:203],v[192:195],v[0:3],a[200:203]
mm a[200:203],v[196:199],v[4:7],a[200:203]
mm a[204:207],v[204:207],v[4:7],a[204:207]
mm a[204:207],v[200:203],v[0:3],a[204:207]
rt v[48:49],v242 offset:128
rt v[50:51],v242 offset:1152
mm a[184:187],v[208:211],v[0:3],a[184:187]
mm a[184:187],v[212:215],v[4:7],a[184:187]
mm a[188:191],v[220:223],v[4:7],a[188:191]
mm a[188:191],v[216:219],v[0:3],a[188:191]
mm a[12:15],v[216:219],v[8:11],a[12:15]
mm a[12:15],v[220:223],v[12:15],a[12:15]
buffer_load_dword v254, v135, s[68:71], s96 offen
mm a[8:11],v[212:215],v[12:15],a[8:11]
s_add_u32 s96, s96, 1048576
mm a[8:11],v[208:211],v[8:11],a[8:11]
rt v[52:53],v242 offset:17024
rt v[54:55],v242 offset:18048
mm a[76:79],v[200:203],v[8:11],a[76:79]
mm a[76:79],v[204:207],v[12:15],a[76:79]
buffer_load_dword v254, v135, s[68:71], s96 offen
mm a[72:75],v[196:199],v[12:15],a[72:75]
s_add_u32 s96, s96, 1048576
mm a[72:75],v[192:195],v[8:11],a[72:75]
rt v[56:57],v243 offset:128
rt v[58:59],v243 offset:1152
mm a[88:91],v[192:195],v[16:19],a[88:91]
mm a[88:91],v[196:199],v[20:23],a[88:91]
mm a[92:95],v[204:207],v[20:23],a[92:95]
mm a[92:95],v[200:203],v[16:19],a[92:95]
rt v[60:61],v243 offset:17024
rt v[62:63],v243 offset:18048
mm a[24:27],v[208:211],v[16:19],a[24:27]
mm a[24:27],v[212:215],v[20:23],a[24:27]
mm a[28:31],v[220:223],v[20:23],a[28:31]
mm a[28:31],v[216:219],v[16:19],a[28:31]
mm a[44:47],v[216:219],v[24:27],a[44:47]
mm a[44:47],v[220:223],v[28:31],a[44:47]
ld v[72:75],v255,s[64:67],s89 offen offset:1024 sc1
mm a[40:43],v[212:215],v[28:31],a[40:43]
mm a[40:43],v[208:211],v[24:27],a[40:43]
mm a[252:255],v[200:203],v[24:27],a[252:255]
mm a[252:255],v[204:207],v[28:31],a[252:255]
mm a[248:251],v[196:199],v[28:31],a[248:251]
wt vmcnt(3) lgkmcnt(0)
s_barrier
mm a[248:251],v[192:195],v[24:27],a[248:251]
mm a[112:115],v[32:35],v[160:163],a[112:115]
mm a[112:115],v[36:39],v[164:167],a[112:115]
mm a[116:119],v[44:47],v[164:167],a[116:119]
rt v[192:193],v240 offset:33792
rt v[194:195],v240 offset:34816
mm a[116:119],v[40:43],v[160:163],a[116:119]
rt v[196:197],v240 offset:50688
rt v[198:199],v240 offset:51712
mm a[128:131],v[48:51],v[160:163],a[128:131]
mm a[128:131],v[52:55],v[164:167],a[128:131]
mm a[132:135],v[60:63],v[164:167],a[132:135]
mm a[132:135],v[56:59],v[160:163],a[132:135]
rd v[160:163],v236 offset:57344
rd v[164:167],v237 offset:57344
mm a[148:151],v[56:59],v[168:171],a[148:151]
mm a[148:151],v[60:63],v[172:175],a[148:151]
mm a[144:147],v[52:55],v[172:175],a[144:147]
mm a[144:147],v[48:51],v[168:171],a[144:147]
rt v[200:201],v241 offset:33792
rt v[202:203],v241 offset:34816
mm a[228:231],v[40:43],v[168:171],a[228:231]
mm a[228:231],v[44:47],v[172:175],a[228:231]
mm a[224:227],v[36:39],v[172:175],a[224:227]
mm a[224:227],v[32:35],v[168:171],a[224:227]
rd v[168:171],v236 offset:59392
rd v[172:175],v237 offset:59392
mm a[208:211],v[32:35],v[176:179],a[208:211]
mm a[208:211],v[36:39],v[180:183],a[208:211]
mm a[212:215],v[44:47],v[180:183],a[212:215]
mm a[212:215],v[40:43],v[176:179],a[212:215]
rt v[204:205],v241 offset:50688
rt v[206:207],v241 offset:51712
mm a[160:163],v[48:51],v[176:179],a[160:163]
mm a[160:163],v[52:55],v[180:183],a[160:163]
mm a[164:167],v[60:63],v[180:183],a[164:167]
mm a[164:167],v[56:59],v[176:179],a[164:167]
rd v[176:179],v236 offset:49152
rd v[180:183],v237 offset:49152
mm a[180:183],v[56:59],v[184:187],a[180:183]
mm a[180:183],v[60:63],v[188:191],a[180:183]
mm a[176:179],v[52:55],v[188:191],a[176:179]
mm a[176:179],v[48:51],v[184:187],a[176:179]
rt v[208:209],v242 offset:33792
rt v[210:211],v242 offset:34816
mm a[196:199],v[40:43],v[184:187],a[196:199]
s_add_u32 m0, s83, 0
mm a[196:199],v[44:47],v[188:191],a[196:199]
ld v232,s[44:47],s80 offen lds
mm a[192:195],v[36:39],v[188:191],a[192:195]
mm a[192:195],v[32:35],v[184:187],a[192:195]
rd v[184:187],v236 offset:51200
rd v[188:191],v237 offset:51200
mm a[64:67],v[32:35],v[0:3],a[64:67]
s_add_u32 m0, s83, 1024
mm a[64:67],v[36:39],v[4:7],a[64:67]
ld v233,s[44:47],s80 offen lds
mm a[68:71],v[44:47],v[4:7],a[68:71]
mm a[68:71],v[40:43],v[0:3],a[68:71]
rt v[212:213],v242 offset:50688
rt v[214:215],v242 offset:51712
mm a[0:3],v[48:51],v[0:3],a[0:3]
s_add_u32 m0, s83, 2176
mm a[0:3],v[52:55],v[4:7],a[0:3]
ld v234,s[44:47],s80 offen lds
mm a[4:7],v[60:63],v[4:7],a[4:7]
mm a[4:7],v[56:59],v[0:3],a[4:7]
rd v[0:3],v236 offset:40960
rd v[4:7],v237 offset:40960
mm a[20:23],v[56:59],v[8:11],a[20:23]
s_add_u32 m0, s83, 3200
mm a[20:23],v[60:63],v[12:15],a[20:23]
ld v235,s[44:47],s80 offen lds
mm a[16:19],v[52:55],v[12:15],a[16:19]
s_add_u32 s80, s80, 524288
mm a[16:19],v[48:51],v[8:11],a[16:19]
rt v[216:217],v243 offset:33792
rt v[218:219],v243 offset:34816
mm a[84:87],v[40:43],v[8:11],a[84:87]
s_add_u32 m0, s83, 16896
mm a[84:87],v[44:47],v[12:15],a[84:87]
ld v232,s[44:47],s80 offen lds
mm a[80:83],v[36:39],v[12:15],a[80:83]
mm a[80:83],v[32:35],v[8:11],a[80:83]
rd v[8:11],v236 offset:43008
rd v[12:15],v237 offset:43008
mm a[240:243],v[32:35],v[16:19],a[240:243]
s_add_u32 m0, s83, 17920
mm a[240:243],v[36:39],v[20:23],a[240:243]
ld v233,s[44:47],s80 offen lds
mm a[244:247],v[44:47],v[20:23],a[244:247]
mm a[244:247],v[40:43],v[16:19],a[244:247]
rt v[220:221],v243 offset:50688
rt v[222:223],v243 offset:51712
mm a[32:35],v[48:51],v[16:19],a[32:35]
s_add_u32 m0, s83, 19072
mm a[32:35],v[52:55],v[20:23],a[32:35]
ld v234,s[44:47],s80 offen lds
mm a[36:39],v[60:63],v[20:23],a[36:39]
mm a[36:39],v[56:59],v[16:19],a[36:39]
rd v[16:19],v236 offset:32768
rd v[20:23],v237 offset:32768
mm a[60:63],v[56:59],v[24:27],a[60:63]
s_add_u32 m0, s83, 20096
mm a[60:63],v[60:63],v[28:31],a[60:63]
ld v235,s[44:47],s80 offen lds
mm a[56:59],v[52:55],v[28:31],a[56:59]
s_add_u32 s80, s80, 524288
mm a[56:59],v[48:51],v[24:27],a[56:59]
mm a[52:55],v[40:43],v[24:27],a[52:55]
mm a[52:55],v[44:47],v[28:31],a[52:55]
mm a[48:51],v[36:39],v[28:31],a[48:51]
wt lgkmcnt(2)
mm a[48:51],v[32:35],v[24:27],a[48:51]
rd v[24:27],v236 offset:34816
rd v[28:31],v237 offset:34816
mm a[96:99],v[192:195],v[160:163],a[96:99]
mm a[96:99],v[196:199],v[164:167],a[96:99]
mm a[100:103],v[204:207],v[164:167],a[100:103]
mm a[100:103],v[200:203],v[160:163],a[100:103]
rt v[32:33],v240 offset:33920
rt v[34:35],v240 offset:34944
mm a[104:107],v[208:211],v[160:163],a[104:107]
mm a[104:107],v[212:215],v[164:167],a[104:107]
mm a[108:111],v[220:223],v[164:167],a[108:111]
wt lgkmcnt(2)
s_barrier
mm a[108:111],v[216:219],v[160:163],a[108:111]
mm a[140:143],v[216:219],v[168:171],a[140:143]
wt vmcnt(8)
v_cmp_eq_u64 vcc, v[72:73], v[252:253]
mm a[140:143],v[220:223],v[172:175],a[140:143]
v_cmp_eq_u64 s[96:97], v[74:75], v[252:253]
mm a[136:139],v[212:215],v[172:175],a[136:139]
s_and_b64 vcc, vcc, s[96:97]
s_cmp_eq_u64 vcc, -1
s_cbranch_scc0 .La5_LK_XSPIN
.La5_LK_XOK:
mm a[136:139],v[208:211],v[168:171],a[136:139]
rt v[36:37],v240 offset:50816
rt v[38:39],v240 offset:51840
mm a[124:127],v[200:203],v[168:171],a[124:127]
s_mov_b32 s89, 49152
mm a[124:127],v[204:207],v[172:175],a[124:127]
mm a[120:123],v[196:199],v[172:175],a[120:123]
ar v224,a96
ar v225,a97
mm a[120:123],v[192:195],v[168:171],a[120:123]
ar v226,a98
ar v227,a99
mm a[232:235],v[192:195],v[176:179],a[232:235]
ar v228,a100
ar v229,a101
mm a[232:235],v[196:199],v[180:183],a[232:235]
ld v[128:131],v244,s[52:55],s91 offen offset:0 sc1
mm a[236:239],v[204:207],v[180:183],a[236:239]
ar v230,a102
ar v231,a103
mm a[236:239],v[200:203],v[176:179],a[236:239]
rt v[40:41],v241 offset:33920
rt v[42:43],v241 offset:34944
mm a[152:155],v[208:211],v[176:179],a[152:155]
cv v248,v224,v225
cv v249,v226,v227
mm a[152:155],v[212:215],v[180:183],a[152:155]
ld v[132:135],v245,s[52:55],s91 offen offset:0 sc1
mm a[156:159],v[220:223],v[180:183],a[156:159]
cv v250,v228,v229
cv v251,v230,v231
mm a[156:159],v[216:219],v[176:179],a[156:159]
st v[248:251],v255,s[48:51],s89 offen sc1
mm a[172:175],v[216:219],v[184:187],a[172:175]
s_mov_b32 s89, 50176
ds_write_b128 v238, v[248:251] offset:0
mm a[172:175],v[220:223],v[188:191],a[172:175]
ld v[136:139],v246,s[52:55],s91 offen offset:0 sc1
mm a[168:171],v[212:215],v[188:191],a[168:171]
ar v224,a104
ar v225,a105
mm a[168:171],v[208:211],v[184:187],a[168:171]
rt v[44:45],v241 offset:50816
rt v[46:47],v241 offset:51840
mm a[220:223],v[200:203],v[184:187],a[220:223]
ar v226,a106
ar v227,a107
mm a[220:223],v[204:207],v[188:191],a[220:223]
ld v[140:143],v247,s[52:55],s91 offen offset:0 sc1
mm a[216:219],v[196:199],v[188:191],a[216:219]
ar v228,a108
ar v229,a109
mm a[216:219],v[192:195],v[184:187],a[216:219]
ar v230,a110
ar v231,a111
mm a[200:203],v[192:195],v[0:3],a[200:203]
cv v248,v224,v225
cv v249,v226,v227
mm a[200:203],v[196:199],v[4:7],a[200:203]
.La5_LW1_3:
s_cbranch_execz .La5_LW1_7
ld v[144:147],v244,s[52:55],s91 offen offset:1024 sc1
mm a[204:207],v[204:207],v[4:7],a[204:207]
cv v250,v228,v229
cv v251,v230,v231
mm a[204:207],v[200:203],v[0:3],a[204:207]
rt v[48:49],v242 offset:33920
rt v[50:51],v242 offset:34944
mm a[184:187],v[208:211],v[0:3],a[184:187]
st v[248:251],v255,s[48:51],s89 offen sc1
mm a[184:187],v[212:215],v[4:7],a[184:187]
ld v[148:151],v245,s[52:55],s91 offen offset:1024 sc1
mm a[188:191],v[220:223],v[4:7],a[188:191]
s_mov_b32 s89, 51200
ds_write_b128 v238, v[248:251] offset:1024
mm a[188:191],v[216:219],v[0:3],a[188:191]
mm a[12:15],v[216:219],v[8:11],a[12:15]
mm a[12:15],v[220:223],v[12:15],a[12:15]
ld v[152:155],v246,s[52:55],s91 offen offset:1024 sc1
mm a[8:11],v[212:215],v[12:15],a[8:11]
mm a[8:11],v[208:211],v[8:11],a[8:11]
rt v[52:53],v242 offset:50816
rt v[54:55],v242 offset:51840
mm a[76:79],v[200:203],v[8:11],a[76:79]
mm a[76:79],v[204:207],v[12:15],a[76:79]
ld v[156:159],v247,s[52:55],s91 offen offset:1024 sc1
mm a[72:75],v[196:199],v[12:15],a[72:75]
mm a[72:75],v[192:195],v[8:11],a[72:75]
rt v[56:57],v243 offset:33920
rt v[58:59],v243 offset:34944
mm a[88:91],v[192:195],v[16:19],a[88:91]
mm a[88:91],v[196:199],v[20:23],a[88:91]
mm a[92:95],v[204:207],v[20:23],a[92:95]
mm a[92:95],v[200:203],v[16:19],a[92:95]
rt v[60:61],v243 offset:50816
rt v[62:63],v243 offset:51840
mm a[24:27],v[208:211],v[16:19],a[24:27]
mm a[24:27],v[212:215],v[20:23],a[24:27]
mm a[28:31],v[220:223],v[20:23],a[28:31]
mm a[28:31],v[216:219],v[16:19],a[28:31]
mm a[44:47],v[216:219],v[24:27],a[44:47]
mm a[44:47],v[220:223],v[28:31],a[44:47]
mm a[40:43],v[212:215],v[28:31],a[40:43]
mm a[40:43],v[208:211],v[24:27],a[40:43]
mm a[252:255],v[200:203],v[24:27],a[252:255]
mm a[252:255],v[204:207],v[28:31],a[252:255]
mm a[248:251],v[196:199],v[28:31],a[248:251]
wt vmcnt(10) lgkmcnt(0)
s_barrier
mm a[248:251],v[192:195],v[24:27],a[248:251]
mm a[112:115],v[32:35],v[160:163],a[112:115]
s_add_u32 m0, s83, 33792
mm a[112:115],v[36:39],v[164:167],a[112:115]
ld v232,s[44:47],s80 offen lds
mm a[116:119],v[44:47],v[164:167],a[116:119]
mm a[116:119],v[40:43],v[160:163],a[116:119]
rt v[192:193],v240 offset:0
rt v[194:195],v240 offset:1024
mm a[128:131],v[48:51],v[160:163],a[128:131]
s_add_u32 m0, s83, 34816
mm a[128:131],v[52:55],v[164:167],a[128:131]
ld v233,s[44:47],s80 offen lds
mm a[132:135],v[60:63],v[164:167],a[132:135]
mm a[132:135],v[56:59],v[160:163],a[132:135]
rt v[196:197],v240 offset:8448
rt v[198:199],v240 offset:9472
mm a[148:151],v[56:59],v[168:171],a[148:151]
s_add_u32 m0, s83, 35968
mm a[148:151],v[60:63],v[172:175],a[148:151]
ld v234,s[44:47],s80 offen lds
mm a[144:147],v[52:55],v[172:175],a[144:147]
ar v224,a112
ar v225,a113
mm a[144:147],v[48:51],v[168:171],a[144:147]
rt v[200:201],v241 offset:0
rt v[202:203],v241 offset:1024
mm a[228:231],v[40:43],v[168:171],a[228:231]
s_add_u32 m0, s83, 36992
ar v226,a114
mm a[228:231],v[44:47],v[172:175],a[228:231]
ld v235,s[44:47],s80 offen lds
mm a[224:227],v[36:39],v[172:175],a[224:227]
s_add_u32 s80, s80, 524288
ar v227,a115
mm a[224:227],v[32:35],v[168:171],a[224:227]
rt v[204:205],v241 offset:8448
rt v[206:207],v241 offset:9472
mm a[208:211],v[32:35],v[176:179],a[208:211]
s_add_u32 m0, s83, 50688
ar v228,a116
mm a[208:211],v[36:39],v[180:183],a[208:211]
ld v232,s[44:47],s80 offen lds
mm a[212:215],v[44:47],v[180:183],a[212:215]
ar v229,a117
ar v230,a118
mm a[212:215],v[40:43],v[176:179],a[212:215]
rt v[208:209],v242 offset:0
rt v[210:211],v242 offset:1024
mm a[160:163],v[48:51],v[176:179],a[160:163]
s_add_u32 m0, s83, 51712
ar v231,a119
mm a[160:163],v[52:55],v[180:183],a[160:163]
ld v233,s[44:47],s80 offen lds
mm a[164:167],v[60:63],v[180:183],a[164:167]
cv v248,v224,v225
cv v249,v226,v227
mm a[164:167],v[56:59],v[176:179],a[164:167]
rt v[212:213],v242 offset:8448
rt v[214:215],v242 offset:9472
mm a[180:183],v[56:59],v[184:187],a[180:183]
s_add_u32 m0, s83, 52864
cv v250,v228,v229
mm a[180:183],v[60:63],v[188:191],a[180:183]
ld v234,s[44:47],s80 offen lds
mm a[176:179],v[52:55],v[188:191],a[176:179]
cv v251,v230,v231
st v[248:251],v255,s[48:51],s89 offen sc1
mm a[176:179],v[48:51],v[184:187],a[176:179]
rt v[216:217],v243 offset:0
rt v[218:219],v243 offset:1024
mm a[196:199],v[40:43],v[184:187],a[196:199]
s_add_u32 m0, s83, 53888
s_mov_b32 s89, 52224
mm a[196:199],v[44:47],v[188:191],a[196:199]
ld v235,s[44:47],s80 offen lds
mm a[192:195],v[36:39],v[188:191],a[192:195]
s_add_u32 s80, s80, 524288
ds_write_b128 v238, v[248:251] offset:2048
mm a[192:195],v[32:35],v[184:187],a[192:195]
rt v[220:221],v243 offset:8448
rt v[222:223],v243 offset:9472
mm a[64:67],v[32:35],v[0:3],a[64:67]
mm a[64:67],v[36:39],v[4:7],a[64:67]
ld v[160:163],v244,s[52:55],s91 offen offset:2048 sc1
mm a[68:71],v[44:47],v[4:7],a[68:71]
mm a[68:71],v[40:43],v[0:3],a[68:71]
mm a[0:3],v[48:51],v[0:3],a[0:3]
mm a[0:3],v[52:55],v[4:7],a[0:3]
ld v[164:167],v245,s[52:55],s91 offen offset:2048 sc1
mm a[4:7],v[60:63],v[4:7],a[4:7]
mm a[4:7],v[56:59],v[0:3],a[4:7]
mm a[20:23],v[56:59],v[8:11],a[20:23]
mm a[20:23],v[60:63],v[12:15],a[20:23]
ld v[168:171],v246,s[52:55],s91 offen offset:2048 sc1
mm a[16:19],v[52:55],v[12:15],a[16:19]
mm a[16:19],v[48:51],v[8:11],a[16:19]
mm a[84:87],v[40:43],v[8:11],a[84:87]
mm a[84:87],v[44:47],v[12:15],a[84:87]
ld v[172:175],v247,s[52:55],s91 offen offset:2048 sc1
mm a[80:83],v[36:39],v[12:15],a[80:83]
mm a[80:83],v[32:35],v[8:11],a[80:83]
mm a[240:243],v[32:35],v[16:19],a[240:243]
mm a[240:243],v[36:39],v[20:23],a[240:243]
mm a[244:247],v[44:47],v[20:23],a[244:247]
mm a[244:247],v[40:43],v[16:19],a[244:247]
mm a[32:35],v[48:51],v[16:19],a[32:35]
mm a[32:35],v[52:55],v[20:23],a[32:35]
mm a[36:39],v[60:63],v[20:23],a[36:39]
mm a[36:39],v[56:59],v[16:19],a[36:39]
mm a[60:63],v[56:59],v[24:27],a[60:63]
mm a[60:63],v[60:63],v[28:31],a[60:63]
mm a[56:59],v[52:55],v[28:31],a[56:59]
mm a[56:59],v[48:51],v[24:27],a[56:59]
mm a[52:55],v[40:43],v[24:27],a[52:55]
mm a[52:55],v[44:47],v[28:31],a[52:55]
mm a[48:51],v[36:39],v[28:31],a[48:51]
mm a[48:51],v[32:35],v[24:27],a[48:51]
s_branch .La5_LQ_START
.La5_LV_XSPIN:
s_mov_b32 s93, 0
.La5_LV_XSPIN_L:
s_add_u32 s93, s93, 1
s_cmp_gt_u32 s93, 2097152
s_cbranch_scc1 .La5_LV_XOK
s_sleep 8
ld v[72:75],v255,s[64:67],s89 offen offset:1024 sc1
wt vmcnt(0)
v_cmp_eq_u64 vcc, v[72:73], v[252:253]
v_cmp_eq_u64 s[96:97], v[74:75], v[252:253]
s_nop 1
s_and_b64 vcc, vcc, s[96:97]
s_cmp_eq_u64 vcc, -1
s_cbranch_scc0 .La5_LV_XSPIN_L
s_branch .La5_LV_XOK
.La5_LK_XSPIN:
s_mov_b32 s93, 0
.La5_LK_XSPIN_L:
s_add_u32 s93, s93, 1
s_cmp_gt_u32 s93, 2097152
s_cbranch_scc1 .La5_LK_XOK
s_sleep 8
ld v[72:75],v255,s[64:67],s89 offen offset:1024 sc1
wt vmcnt(0)
v_cmp_eq_u64 vcc, v[72:73], v[252:253]
v_cmp_eq_u64 s[96:97], v[74:75], v[252:253]
s_nop 1
s_and_b64 vcc, vcc, s[96:97]
s_cmp_eq_u64 vcc, -1
s_cbranch_scc0 .La5_LK_XSPIN_L
s_branch .La5_LK_XOK
; ---- V tile (waves 1, 3): the same with the operands exchanged (V comes out transposed)
.La5_LV_START:
s_nop 0
.La5_LW1_4:
s_cbranch_execz .La5_LW1_8
mm a[96:99],v[160:163],v[192:195],0
s_add_u32 m0, s83, 33792
mm a[96:99],v[164:167],v[196:199],a[96:99]
ld v232,s[44:47],s80 offen lds
mm a[104:107],v[164:167],v[204:207],0
rd v[0:3],v236 offset:8192
rd v[4:7],v237 offset:8192
s_add_u32 m0, s83, 34816
mm a[104:107],v[160:163],v[200:203],a[104:107]
rd v[8:11],v236 offset:10240
rd v[12:15],v237 offset:10240
ld v233,s[44:47],s80 offen lds
mm a[112:115],v[160:163],v[208:211],0
s_add_u32 m0, s83, 35968
mm a[112:115],v[164:167],v[212:215],a[112:115]
ld v234,s[44:47],s80 offen lds
mm a[128:131],v[164:167],v[220:223],0
rd v[16:19],v236 offset:0
rd v[20:23],v237 offset:0
s_add_u32 m0, s83, 36992
mm a[128:131],v[160:163],v[216:219],a[128:131]
rd v[24:27],v236 offset:2048
rd v[28:31],v237 offset:2048
ld v235,s[44:47],s80 offen lds
mm a[132:135],v[168:171],v[216:219],0
s_mov_b32 s80, 2883584
mm a[132:135],v[172:175],v[220:223],a[132:135]
mm a[116:119],v[172:175],v[212:215],0
s_xor_b32 m0, s82, 32768
mm a[116:119],v[168:171],v[208:211],a[116:119]
rt v[32:33],v240 offset:128
rt v[34:35],v240 offset:1152
ld v224,s[40:43],s79 offen lds
mm a[108:111],v[168:171],v[200:203],0
s_xor_b32 m0, s82, 33792
mm a[108:111],v[172:175],v[204:207],a[108:111]
ld v225,s[40:43],s79 offen lds
mm a[100:103],v[172:175],v[196:199],0
s_xor_b32 m0, s82, 34816
mm a[100:103],v[168:171],v[192:195],a[100:103]
rt v[36:37],v240 offset:17024
rt v[38:39],v240 offset:18048
ld v226,s[40:43],s79 offen lds
mm a[232:235],v[176:179],v[192:195],0
s_xor_b32 m0, s82, 35840
mm a[232:235],v[180:183],v[196:199],a[232:235]
ld v227,s[40:43],s79 offen lds
mm a[152:155],v[180:183],v[204:207],0
s_xor_b32 m0, s82, 36864
mm a[152:155],v[176:179],v[200:203],a[152:155]
rt v[40:41],v241 offset:128
rt v[42:43],v241 offset:1152
ld v228,s[40:43],s79 offen lds
mm a[208:211],v[176:179],v[208:211],0
s_xor_b32 m0, s82, 37888
mm a[208:211],v[180:183],v[212:215],a[208:211]
ld v229,s[40:43],s79 offen lds
mm a[160:163],v[180:183],v[220:223],0
s_xor_b32 m0, s82, 38912
mm a[160:163],v[176:179],v[216:219],a[160:163]
ld v230,s[40:43],s79 offen lds
mm a[164:167],v[184:187],v[216:219],0
s_xor_b32 m0, s82, 39936
mm a[164:167],v[188:191],v[220:223],a[164:167]
ld v231,s[40:43],s79 offen lds
mm a[212:215],v[188:191],v[212:215],0
s_mov_b32 s79, 768
mm a[212:215],v[184:187],v[208:211],a[212:215]
rt v[44:45],v241 offset:17024
rt v[46:47],v241 offset:18048
mm a[156:159],v[184:187],v[200:203],0
s_add_u32 m0, s83, 50688
mm a[156:159],v[188:191],v[204:207],a[156:159]
ld v232,s[44:47],s80 offen lds
mm a[236:239],v[188:191],v[196:199],0
wt lgkmcnt(8)
s_add_u32 m0, s83, 51712
mm a[236:239],v[184:187],v[192:195],a[236:239]
ld v233,s[44:47],s80 offen lds
mm a[200:203],v[0:3],v[192:195],0
s_add_u32 m0, s83, 52864
mm a[200:203],v[4:7],v[196:199],a[200:203]
ld v234,s[44:47],s80 offen lds
mm a[184:187],v[4:7],v[204:207],0
s_add_u32 m0, s83, 53888
mm a[184:187],v[0:3],v[200:203],a[184:187]
rt v[48:49],v242 offset:128
rt v[50:51],v242 offset:1152
ld v235,s[44:47],s80 offen lds
mm a[64:67],v[0:3],v[208:211],0
s_mov_b32 s80, 3145728
mm a[64:67],v[4:7],v[212:215],a[64:67]
mm a[0:3],v[4:7],v[220:223],0
mm a[0:3],v[0:3],v[216:219],a[0:3]
mm a[4:7],v[8:11],v[216:219],0
mm a[4:7],v[12:15],v[220:223],a[4:7]
buffer_load_dword v70, v118, s[60:63], 0 offen
mm a[68:71],v[12:15],v[212:215],0
mm a[68:71],v[8:11],v[208:211],a[68:71]
rt v[52:53],v242 offset:17024
rt v[54:55],v242 offset:18048
mm a[188:191],v[8:11],v[200:203],0
mm a[188:191],v[12:15],v[204:207],a[188:191]
ld v[72:75],v119,s[60:63],0 offen offset:0
mm a[204:207],v[12:15],v[196:199],0
mm a[204:207],v[8:11],v[192:195],a[204:207]
rt v[56:57],v243 offset:128
rt v[58:59],v243 offset:1152
mm a[88:91],v[16:19],v[192:195],0
mm a[88:91],v[20:23],v[196:199],a[88:91]
ld v[76:79],v119,s[60:63],0 offen offset:128
mm a[24:27],v[20:23],v[204:207],0
mm a[24:27],v[16:19],v[200:203],a[24:27]
rt v[60:61],v243 offset:17024
rt v[62:63],v243 offset:18048
mm a[240:243],v[16:19],v[208:211],0
mm a[240:243],v[20:23],v[212:215],a[240:243]
ld v[80:83],v119,s[60:63],0 offen offset:256
mm a[32:35],v[20:23],v[220:223],0
mm a[32:35],v[16:19],v[216:219],a[32:35]
mm a[36:39],v[24:27],v[216:219],0
mm a[36:39],v[28:31],v[220:223],a[36:39]
ld v[84:87],v119,s[60:63],0 offen offset:384
mm a[244:247],v[28:31],v[212:215],0
mm a[244:247],v[24:27],v[208:211],a[244:247]
mm a[28:31],v[24:27],v[200:203],0
mm a[28:31],v[28:31],v[204:207],a[28:31]
mm a[92:95],v[28:31],v[196:199],0
mm a[92:95],v[24:27],v[192:195],a[92:95]
s_mov_b64 exec, 0
s_cmp_eq_u32 s3, 1
s_cbranch_scc1 .La5_LW2_1
s_branch .La5_LW2_R1
.La5_LW2_R1:
s_mov_b64 exec, -1
wt vmcnt(5) lgkmcnt(0)
s_barrier
mm a[120:123],v[160:163],v[32:35],0
mm a[120:123],v[164:167],v[36:39],a[120:123]
mm a[136:139],v[164:167],v[44:47],0
rt v[192:193],v240 offset:33792
rt v[194:195],v240 offset:34816
mm a[136:139],v[160:163],v[40:43],a[136:139]
rt v[196:197],v240 offset:50688
rt v[198:199],v240 offset:51712
mm a[224:227],v[160:163],v[48:51],0
mm a[224:227],v[164:167],v[52:55],a[224:227]
mm a[144:147],v[164:167],v[60:63],0
mm a[144:147],v[160:163],v[56:59],a[144:147]
rd v[160:163],v236 offset:57344
rd v[164:167],v237 offset:57344
mm a[148:151],v[168:171],v[56:59],0
mm a[148:151],v[172:175],v[60:63],a[148:151]
mm a[228:231],v[172:175],v[52:55],0
mm a[228:231],v[168:171],v[48:51],a[228:231]
rt v[200:201],v241 offset:33792
rt v[202:203],v241 offset:34816
mm a[140:143],v[168:171],v[40:43],0
mm a[140:143],v[172:175],v[44:47],a[140:143]
mm a[124:127],v[172:175],v[36:39],0
mm a[124:127],v[168:171],v[32:35],a[124:127]
rd v[168:171],v236 offset:59392
rd v[172:175],v237 offset:59392
mm a[216:219],v[176:179],v[32:35],0
s_xor_b32 m0, s82, 0
mm a[216:219],v[180:183],v[36:39],a[216:219]
ld v224,s[40:43],s79 offen lds
mm a[168:171],v[180:183],v[44:47],0
mm a[168:171],v[176:179],v[40:43],a[168:171]
rt v[204:205],v241 offset:50688
rt v[206:207],v241 offset:51712
s_xor_b32 m0, s82, 1024
mm a[192:195],v[176:179],v[48:51],0
ld v225,s[40:43],s79 offen lds
mm a[192:195],v[180:183],v[52:55],a[192:195]
mm a[176:179],v[180:183],v[60:63],0
s_xor_b32 m0, s82, 2048
mm a[176:179],v[176:179],v[56:59],a[176:179]
rd v[176:179],v236 offset:49152
rd v[180:183],v237 offset:49152
ld v226,s[40:43],s79 offen lds
mm a[180:183],v[184:187],v[56:59],0
mm a[180:183],v[188:191],v[60:63],a[180:183]
s_xor_b32 m0, s82, 3072
mm a[196:199],v[188:191],v[52:55],0
ld v227,s[40:43],s79 offen lds
mm a[196:199],v[184:187],v[48:51],a[196:199]
rt v[208:209],v242 offset:33792
rt v[210:211],v242 offset:34816
mm a[172:175],v[184:187],v[40:43],0
s_xor_b32 m0, s82, 4096
mm a[172:175],v[188:191],v[44:47],a[172:175]
ld v228,s[40:43],s79 offen lds
mm a[220:223],v[188:191],v[36:39],0
mm a[220:223],v[184:187],v[32:35],a[220:223]
rd v[184:187],v236 offset:51200
rd v[188:191],v237 offset:51200
s_xor_b32 m0, s82, 5120
mm a[72:75],v[0:3],v[32:35],0
ld v229,s[40:43],s79 offen lds
mm a[72:75],v[4:7],v[36:39],a[72:75]
mm a[8:11],v[4:7],v[44:47],0
s_xor_b32 m0, s82, 6144
mm a[8:11],v[0:3],v[40:43],a[8:11]
rt v[212:213],v242 offset:50688
rt v[214:215],v242 offset:51712
ld v230,s[40:43],s79 offen lds
mm a[80:83],v[0:3],v[48:51],0
mm a[80:83],v[4:7],v[52:55],a[80:83]
s_xor_b32 m0, s82, 7168
mm a[16:19],v[4:7],v[60:63],0
ld v231,s[40:43],s79 offen lds
mm a[16:19],v[0:3],v[56:59],a[16:19]
rd v[0:3],v236 offset:40960
rd v[4:7],v237 offset:40960
s_mov_b32 s79, 896
mm a[20:23],v[8:11],v[56:59],0
mm a[20:23],v[12:15],v[60:63],a[20:23]
s_add_u32 m0, s83, 0
mm a[84:87],v[12:15],v[52:55],0
ld v232,s[44:47],s80 offen lds
mm a[84:87],v[8:11],v[48:51],a[84:87]
rt v[216:217],v243 offset:33792
rt v[218:219],v243 offset:34816
mm a[12:15],v[8:11],v[40:43],0
s_add_u32 m0, s83, 1024
mm a[12:15],v[12:15],v[44:47],a[12:15]
ld v233,s[44:47],s80 offen lds
mm a[76:79],v[12:15],v[36:39],0
wt vmcnt(10)
v_lshlrev_b32 v70, 16, v70
mm a[76:79],v[8:11],v[32:35],a[76:79]
rd v[8:11],v236 offset:43008
rd v[12:15],v237 offset:43008
s_add_u32 m0, s83, 2176
mm a[248:251],v[16:19],v[32:35],0
ld v234,s[44:47],s80 offen lds
mm a[248:251],v[20:23],v[36:39],a[248:251]
v_lshlrev_b32 v88, 16, v72
v_and_b32 v89, 0xffff0000, v72
mm a[40:43],v[20:23],v[44:47],0
s_add_u32 m0, s83, 3200
v_sub_f32 v88, v88, v70
mm a[40:43],v[16:19],v[40:43],a[40:43]
rt v[220:221],v243 offset:50688
rt v[222:223],v243 offset:51712
ld v235,s[44:47],s80 offen lds
mm a[48:51],v[16:19],v[48:51],0
s_mov_b32 s80, 3407872
v_sub_f32 v89, v89, v70
mm a[48:51],v[20:23],v[52:55],a[48:51]
v_add_f32 v120, v88, v89
v_mul_f32 v122, v88, v88
mm a[56:59],v[20:23],v[60:63],0
s_add_u32 m0, s83, 16896
v_mul_f32 v123, v89, v89
mm a[56:59],v[16:19],v[56:59],a[56:59]
rd v[16:19],v236 offset:32768
rd v[20:23],v237 offset:32768
ld v232,s[44:47],s80 offen lds
mm a[60:63],v[24:27],v[56:59],0
v_lshlrev_b32 v90, 16, v73
v_and_b32 v91, 0xffff0000, v73
mm a[60:63],v[28:31],v[60:63],a[60:63]
s_add_u32 m0, s83, 17920
v_sub_f32 v90, v90, v70
mm a[52:55],v[28:31],v[52:55],0
ld v233,s[44:47],s80 offen lds
mm a[52:55],v[24:27],v[48:51],a[52:55]
v_sub_f32 v91, v91, v70
v_add_f32 v124, v90, v91
mm a[44:47],v[24:27],v[40:43],0
s_add_u32 m0, s83, 19072
v_fmac_f32 v122, v90, v90
mm a[44:47],v[28:31],v[44:47],a[44:47]
ld v234,s[44:47],s80 offen lds
mm a[252:255],v[28:31],v[36:39],0
wt lgkmcnt(2)
v_add_f32 v120, v120, v124
mm a[252:255],v[24:27],v[32:35],a[252:255]
rd v[24:27],v236 offset:34816
rd v[28:31],v237 offset:34816
s_add_u32 m0, s83, 20096
mm a[96:99],v[160:163],v[192:195],a[96:99]
ld v235,s[44:47],s80 offen lds
mm a[96:99],v[164:167],v[196:199],a[96:99]
s_mov_b32 s80, 3670016
v_fmac_f32 v123, v91, v91
mm a[104:107],v[164:167],v[204:207],a[104:107]
v_lshlrev_b32 v92, 16, v74
v_and_b32 v93, 0xffff0000, v74
mm a[104:107],v[160:163],v[200:203],a[104:107]
rt v[32:33],v240 offset:33920
rt v[34:35],v240 offset:34944
mm a[112:115],v[160:163],v[208:211],a[112:115]
v_sub_f32 v92, v92, v70
v_sub_f32 v93, v93, v70
mm a[112:115],v[164:167],v[212:215],a[112:115]
v_add_f32 v124, v92, v93
v_fmac_f32 v122, v92, v92
mm a[128:131],v[164:167],v[220:223],a[128:131]
wt lgkmcnt(2)
s_barrier
mm a[128:131],v[160:163],v[216:219],a[128:131]
v_add_f32 v120, v120, v124
v_fmac_f32 v123, v93, v93
mm a[132:135],v[168:171],v[216:219],a[132:135]
s_xor_b32 m0, s82, 32768
v_lshlrev_b32 v94, 16, v75
mm a[132:135],v[172:175],v[220:223],a[132:135]
ld v224,s[40:43],s79 offen lds
mm a[116:119],v[172:175],v[212:215],a[116:119]
v_and_b32 v95, 0xffff0000, v75
v_sub_f32 v94, v94, v70
mm a[116:119],v[168:171],v[208:211],a[116:119]
rt v[36:37],v240 offset:50816
rt v[38:39],v240 offset:51840
mm a[108:111],v[168:171],v[200:203],a[108:111]
s_xor_b32 m0, s82, 33792
v_sub_f32 v95, v95, v70
mm a[108:111],v[172:175],v[204:207],a[108:111]
ld v225,s[40:43],s79 offen lds
mm a[100:103],v[172:175],v[196:199],a[100:103]
v_add_f32 v124, v94, v95
v_fmac_f32 v122, v94, v94
mm a[100:103],v[168:171],v[192:195],a[100:103]
v_add_f32 v120, v120, v124
v_fmac_f32 v123, v95, v95
mm a[232:235],v[176:179],v[192:195],a[232:235]
s_xor_b32 m0, s82, 34816
v_lshlrev_b32 v96, 16, v76
mm a[232:235],v[180:183],v[196:199],a[232:235]
ld v226,s[40:43],s79 offen lds
mm a[152:155],v[180:183],v[204:207],a[152:155]
v_and_b32 v97, 0xffff0000, v76
v_sub_f32 v96, v96, v70
mm a[152:155],v[176:179],v[200:203],a[152:155]
rt v[40:41],v241 offset:33920
rt v[42:43],v241 offset:34944
mm a[208:211],v[176:179],v[208:211],a[208:211]
s_xor_b32 m0, s82, 35840
v_sub_f32 v97, v97, v70
mm a[208:211],v[180:183],v[212:215],a[208:211]
ld v227,s[40:43],s79 offen lds
mm a[160:163],v[180:183],v[220:223],a[160:163]
v_add_f32 v124, v96, v97
v_fmac_f32 v122, v96, v96
mm a[160:163],v[176:179],v[216:219],a[160:163]
v_add_f32 v120, v120, v124
v_fmac_f32 v123, v97, v97
mm a[164:167],v[184:187],v[216:219],a[164:167]
s_xor_b32 m0, s82, 36864
v_lshlrev_b32 v98, 16, v77
mm a[164:167],v[188:191],v[220:223],a[164:167]
.La5_LW1_5:
s_cbranch_execz .La5_LW1_9
ld v228,s[40:43],s79 offen lds
mm a[212:215],v[188:191],v[212:215],a[212:215]
v_and_b32 v99, 0xffff0000, v77
v_sub_f32 v98, v98, v70
mm a[212:215],v[184:187],v[208:211],a[212:215]
rt v[44:45],v241 offset:50816
rt v[46:47],v241 offset:51840
mm a[156:159],v[184:187],v[200:203],a[156:159]
s_xor_b32 m0, s82, 37888
v_sub_f32 v99, v99, v70
mm a[156:159],v[188:191],v[204:207],a[156:159]
ld v229,s[40:43],s79 offen lds
mm a[236:239],v[188:191],v[196:199],a[236:239]
v_add_f32 v124, v98, v99
v_fmac_f32 v122, v98, v98
mm a[236:239],v[184:187],v[192:195],a[236:239]
v_add_f32 v120, v120, v124
v_fmac_f32 v123, v99, v99
mm a[200:203],v[0:3],v[192:195],a[200:203]
s_xor_b32 m0, s82, 38912
v_lshlrev_b32 v100, 16, v78
mm a[200:203],v[4:7],v[196:199],a[200:203]
ld v230,s[40:43],s79 offen lds
mm a[184:187],v[4:7],v[204:207],a[184:187]
v_and_b32 v101, 0xffff0000, v78
v_sub_f32 v100, v100, v70
mm a[184:187],v[0:3],v[200:203],a[184:187]
rt v[48:49],v242 offset:33920
rt v[50:51],v242 offset:34944
mm a[64:67],v[0:3],v[208:211],a[64:67]
s_xor_b32 m0, s82, 39936
v_sub_f32 v101, v101, v70
mm a[64:67],v[4:7],v[212:215],a[64:67]
ld v231,s[40:43],s79 offen lds
mm a[0:3],v[4:7],v[220:223],a[0:3]
s_mov_b32 s79, 0
v_add_f32 v124, v100, v101
mm a[0:3],v[0:3],v[216:219],a[0:3]
v_fmac_f32 v122, v100, v100
v_add_f32 v120, v120, v124
mm a[4:7],v[8:11],v[216:219],a[4:7]
v_fmac_f32 v123, v101, v101
v_lshlrev_b32 v102, 16, v79
mm a[4:7],v[12:15],v[220:223],a[4:7]
buffer_load_dword v254, v132, s[44:47], s86 offen
mm a[68:71],v[12:15],v[212:215],a[68:71]
s_sub_u32 s86, s86, 2097152
v_and_b32 v103, 0xffff0000, v79
mm a[68:71],v[8:11],v[208:211],a[68:71]
rt v[52:53],v242 offset:50816
rt v[54:55],v242 offset:51840
mm a[188:191],v[8:11],v[200:203],a[188:191]
v_sub_f32 v102, v102, v70
v_sub_f32 v103, v103, v70
mm a[188:191],v[12:15],v[204:207],a[188:191]
v_add_f32 v124, v102, v103
v_fmac_f32 v122, v102, v102
mm a[204:207],v[12:15],v[196:199],a[204:207]
v_add_f32 v120, v120, v124
v_fmac_f32 v123, v103, v103
mm a[204:207],v[8:11],v[192:195],a[204:207]
rt v[56:57],v243 offset:33920
rt v[58:59],v243 offset:34944
mm a[88:91],v[16:19],v[192:195],a[88:91]
v_lshlrev_b32 v104, 16, v80
v_and_b32 v105, 0xffff0000, v80
mm a[88:91],v[20:23],v[196:199],a[88:91]
v_sub_f32 v104, v104, v70
v_sub_f32 v105, v105, v70
mm a[24:27],v[20:23],v[204:207],a[24:27]
v_add_f32 v124, v104, v105
v_fmac_f32 v122, v104, v104
mm a[24:27],v[16:19],v[200:203],a[24:27]
rt v[60:61],v243 offset:50816
rt v[62:63],v243 offset:51840
mm a[240:243],v[16:19],v[208:211],a[240:243]
v_add_f32 v120, v120, v124
v_fmac_f32 v123, v105, v105
mm a[240:243],v[20:23],v[212:215],a[240:243]
v_lshlrev_b32 v106, 16, v81
v_and_b32 v107, 0xffff0000, v81
mm a[32:35],v[20:23],v[220:223],a[32:35]
v_sub_f32 v106, v106, v70
v_sub_f32 v107, v107, v70
mm a[32:35],v[16:19],v[216:219],a[32:35]
v_add_f32 v124, v106, v107
v_fmac_f32 v122, v106, v106
mm a[36:39],v[24:27],v[216:219],a[36:39]
v_add_f32 v120, v120, v124
v_fmac_f32 v123, v107, v107
mm a[36:39],v[28:31],v[220:223],a[36:39]
v_lshlrev_b32 v108, 16, v82
v_and_b32 v109, 0xffff0000, v82
mm a[244:247],v[28:31],v[212:215],a[244:247]
v_sub_f32 v108, v108, v70
v_sub_f32 v109, v109, v70
mm a[244:247],v[24:27],v[208:211],a[244:247]
v_add_f32 v124, v108, v109
v_fmac_f32 v122, v108, v108
mm a[28:31],v[24:27],v[200:203],a[28:31]
v_add_f32 v120, v120, v124
v_fmac_f32 v123, v109, v109
mm a[28:31],v[28:31],v[204:207],a[28:31]
v_lshlrev_b32 v110, 16, v83
v_and_b32 v111, 0xffff0000, v83
mm a[92:95],v[28:31],v[196:199],a[92:95]
wt vmcnt(9) lgkmcnt(0)
s_barrier
mm a[92:95],v[24:27],v[192:195],a[92:95]
v_sub_f32 v110, v110, v70
v_sub_f32 v111, v111, v70
mm a[120:123],v[160:163],v[32:35],a[120:123]
v_add_f32 v124, v110, v111
v_fmac_f32 v122, v110, v110
mm a[120:123],v[164:167],v[36:39],a[120:123]
v_add_f32 v120, v120, v124
v_fmac_f32 v123, v111, v111
mm a[136:139],v[164:167],v[44:47],a[136:139]
rt v[192:193],v240 offset:0
rt v[194:195],v240 offset:1024
mm a[136:139],v[160:163],v[40:43],a[136:139]
rt v[196:197],v240 offset:16896
rt v[198:199],v240 offset:17920
mm a[224:227],v[160:163],v[48:51],a[224:227]
v_lshlrev_b32 v112, 16, v84
v_and_b32 v113, 0xffff0000, v84
mm a[224:227],v[164:167],v[52:55],a[224:227]
v_sub_f32 v112, v112, v70
v_sub_f32 v113, v113, v70
mm a[144:147],v[164:167],v[60:63],a[144:147]
v_add_f32 v124, v112, v113
v_fmac_f32 v122, v112, v112
mm a[144:147],v[160:163],v[56:59],a[144:147]
rd v[160:163],v236 offset:24576
rd v[164:167],v237 offset:24576
mm a[148:151],v[168:171],v[56:59],a[148:151]
v_add_f32 v120, v120, v124
v_fmac_f32 v123, v113, v113
mm a[148:151],v[172:175],v[60:63],a[148:151]
v_lshlrev_b32 v114, 16, v85
v_and_b32 v115, 0xffff0000, v85
mm a[228:231],v[172:175],v[52:55],a[228:231]
v_sub_f32 v114, v114, v70
v_sub_f32 v115, v115, v70
mm a[228:231],v[168:171],v[48:51],a[228:231]
rt v[200:201],v241 offset:0
rt v[202:203],v241 offset:1024
mm a[140:143],v[168:171],v[40:43],a[140:143]
v_add_f32 v124, v114, v115
v_fmac_f32 v122, v114, v114
mm a[140:143],v[172:175],v[44:47],a[140:143]
v_add_f32 v120, v120, v124
v_fmac_f32 v123, v115, v115
mm a[124:127],v[172:175],v[36:39],a[124:127]
v_lshlrev_b32 v116, 16, v86
v_and_b32 v117, 0xffff0000, v86
mm a[124:127],v[168:171],v[32:35],a[124:127]
rd v[168:171],v236 offset:26624
rd v[172:175],v237 offset:26624
mm a[216:219],v[176:179],v[32:35],a[216:219]
v_sub_f32 v116, v116, v70
v_sub_f32 v117, v117, v70
mm a[216:219],v[180:183],v[36:39],a[216:219]
v_add_f32 v124, v116, v117
v_fmac_f32 v122, v116, v116
mm a[168:171],v[180:183],v[44:47],a[168:171]
v_add_f32 v120, v120, v124
v_fmac_f32 v123, v117, v117
mm a[168:171],v[176:179],v[40:43],a[168:171]
rt v[204:205],v241 offset:16896
rt v[206:207],v241 offset:17920
mm a[192:195],v[176:179],v[48:51],a[192:195]
v_lshlrev_b32 v118, 16, v87
v_and_b32 v119, 0xffff0000, v87
mm a[192:195],v[180:183],v[52:55],a[192:195]
v_sub_f32 v118, v118, v70
v_sub_f32 v119, v119, v70
mm a[176:179],v[180:183],v[60:63],a[176:179]
v_add_f32 v124, v118, v119
v_fmac_f32 v122, v118, v118
mm a[176:179],v[176:179],v[56:59],a[176:179]
rd v[176:179],v236 offset:16384
rd v[180:183],v237 offset:16384
mm a[180:183],v[184:187],v[56:59],a[180:183]
v_add_f32 v120, v120, v124
v_fmac_f32 v123, v119, v119
mm a[180:183],v[188:191],v[60:63],a[180:183]
v_add_f32 v121, v122, v123
mm a[196:199],v[188:191],v[52:55],a[196:199]
v_add_f32_dpp v120, v120, v120 row_ror:8 row_mask:0xf bank_mask:0xf
mm a[196:199],v[184:187],v[48:51],a[196:199]
rt v[208:209],v242 offset:0
rt v[210:211],v242 offset:1024
mm a[172:175],v[184:187],v[40:43],a[172:175]
s_add_u32 m0, s83, 33792
v_add_f32_dpp v121, v121, v121 row_ror:8 row_mask:0xf bank_mask:0xf
mm a[172:175],v[188:191],v[44:47],a[172:175]
ld v232,s[44:47],s80 offen lds
mm a[220:223],v[188:191],v[36:39],a[220:223]
v_mov_b32 v124, v120
v_mov_b32 v125, v121
mm a[220:223],v[184:187],v[32:35],a[220:223]
rd v[184:187],v236 offset:18432
rd v[188:191],v237 offset:18432
mm a[72:75],v[0:3],v[32:35],a[72:75]
s_add_u32 m0, s83, 34816
v_permlane16_swap_b32 v124, v120
mm a[72:75],v[4:7],v[36:39],a[72:75]
ld v233,s[44:47],s80 offen lds
mm a[8:11],v[4:7],v[44:47],a[8:11]
v_permlane16_swap_b32 v125, v121
mm a[8:11],v[0:3],v[40:43],a[8:11]
rt v[212:213],v242 offset:16896
rt v[214:215],v242 offset:17920
mm a[80:83],v[0:3],v[48:51],a[80:83]
s_add_u32 m0, s83, 35968
v_add_f32 v120, v120, v124
mm a[80:83],v[4:7],v[52:55],a[80:83]
ld v234,s[44:47],s80 offen lds
mm a[16:19],v[4:7],v[60:63],a[16:19]
v_add_f32 v121, v121, v125
v_mov_b32 v124, v120
mm a[16:19],v[0:3],v[56:59],a[16:19]
rd v[0:3],v236 offset:8192
rd v[4:7],v237 offset:8192
mm a[20:23],v[8:11],v[56:59],a[20:23]
s_add_u32 m0, s83, 36992
v_mov_b32 v125, v121
mm a[20:23],v[12:15],v[60:63],a[20:23]
ld v235,s[44:47],s80 offen lds
mm a[84:87],v[12:15],v[52:55],a[84:87]
s_mov_b32 s80, 3932160
v_permlane32_swap_b32 v124, v120
mm a[84:87],v[8:11],v[48:51],a[84:87]
rt v[216:217],v243 offset:0
rt v[218:219],v243 offset:1024
mm a[12:15],v[8:11],v[40:43],a[12:15]
s_add_u32 m0, s83, 50688
v_permlane32_swap_b32 v125, v121
mm a[12:15],v[12:15],v[44:47],a[12:15]
ld v232,s[44:47],s80 offen lds
mm a[76:79],v[12:15],v[36:39],a[76:79]
v_add_f32 v120, v120, v124
v_add_f32 v121, v121, v125
mm a[76:79],v[8:11],v[32:35],a[76:79]
rd v[8:11],v236 offset:10240
rd v[12:15],v237 offset:10240
mm a[248:251],v[16:19],v[32:35],a[248:251]
s_add_u32 m0, s83, 51712
ds_write_b64 v127, v[120:121]
mm a[248:251],v[20:23],v[36:39],a[248:251]
ld v233,s[44:47],s80 offen lds
mm a[40:43],v[20:23],v[44:47],a[40:43]
s_nop 0
v_readfirstlane_b32 s92, v252
mm a[40:43],v[16:19],v[40:43],a[40:43]
rt v[220:221],v243 offset:16896
rt v[222:223],v243 offset:17920
mm a[48:51],v[16:19],v[48:51],a[48:51]
s_add_u32 m0, s83, 52864
v_readfirstlane_b32 s93, v253
mm a[48:51],v[20:23],v[52:55],a[48:51]
ld v234,s[44:47],s80 offen lds
mm a[56:59],v[20:23],v[60:63],a[56:59]
s_add_u32 s92, s92, 1
s_addc_u32 s93, s93, 0
mm a[56:59],v[16:19],v[56:59],a[56:59]
rd v[16:19],v236 offset:0
rd v[20:23],v237 offset:0
mm a[60:63],v[24:27],v[56:59],a[60:63]
s_add_u32 m0, s83, 53888
v_mov_b32 v252, s92
mm a[60:63],v[28:31],v[60:63],a[60:63]
ld v235,s[44:47],s80 offen lds
mm a[52:55],v[28:31],v[52:55],a[52:55]
s_mov_b32 s80, 0
v_mov_b32 v253, s93
mm a[52:55],v[24:27],v[48:51],a[52:55]
v_mov_b32 v128, 0
v_mov_b32 v129, 0
mm a[44:47],v[24:27],v[40:43],a[44:47]
v_mov_b32 v130, 0
v_mov_b32 v131, 0
mm a[44:47],v[28:31],v[44:47],a[44:47]
st v[128:131],v255,s[52:55],s95 offen sc1
mm a[252:255],v[28:31],v[36:39],a[252:255]
wt lgkmcnt(2)
mm a[252:255],v[24:27],v[32:35],a[252:255]
rd v[24:27],v236 offset:2048
rd v[28:31],v237 offset:2048
mm a[96:99],v[160:163],v[192:195],a[96:99]
mm a[96:99],v[164:167],v[196:199],a[96:99]
mm a[104:107],v[164:167],v[204:207],a[104:107]
mm a[104:107],v[160:163],v[200:203],a[104:107]
rt v[32:33],v240 offset:128
rt v[34:35],v240 offset:1152
mm a[112:115],v[160:163],v[208:211],a[112:115]
mm a[112:115],v[164:167],v[212:215],a[112:115]
mm a[128:131],v[164:167],v[220:223],a[128:131]
wt lgkmcnt(2)
s_barrier
mm a[128:131],v[160:163],v[216:219],a[128:131]
rd v[72:75],v126
rd v[76:79],v126 offset:16
mm a[132:135],v[168:171],v[216:219],a[132:135]
s_xor_b32 m0, s82, 0
mm a[132:135],v[172:175],v[220:223],a[132:135]
ld v224,s[40:43],s79 offen lds
mm a[116:119],v[172:175],v[212:215],a[116:119]
mm a[116:119],v[168:171],v[208:211],a[116:119]
rt v[36:37],v240 offset:17024
rt v[38:39],v240 offset:18048
mm a[108:111],v[168:171],v[200:203],a[108:111]
s_xor_b32 m0, s82, 1024
mm a[108:111],v[172:175],v[204:207],a[108:111]
ld v225,s[40:43],s79 offen lds
mm a[100:103],v[172:175],v[196:199],a[100:103]
wt lgkmcnt(2)
v_add_f32 v72, v72, v74
mm a[100:103],v[168:171],v[192:195],a[100:103]
v_add_f32 v73, v73, v75
v_add_f32 v76, v76, v78
mm a[232:235],v[176:179],v[192:195],a[232:235]
s_xor_b32 m0, s82, 2048
v_add_f32 v77, v77, v79
mm a[232:235],v[180:183],v[196:199],a[232:235]
ld v226,s[40:43],s79 offen lds
mm a[152:155],v[180:183],v[204:207],a[152:155]
v_add_f32 v72, v72, v76
v_add_f32 v73, v73, v77
mm a[152:155],v[176:179],v[200:203],a[152:155]
rt v[40:41],v241 offset:128
rt v[42:43],v241 offset:1152
mm a[208:211],v[176:179],v[208:211],a[208:211]
s_xor_b32 m0, s82, 3072
v_mul_f32 v72, 0x3a800000, v72
mm a[208:211],v[180:183],v[212:215],a[208:211]
ld v227,s[40:43],s79 offen lds
mm a[160:163],v[180:183],v[220:223],a[160:163]
v_mul_f32 v73, 0x3a800000, v73
v_add_f32 v124, v70, v72
mm a[160:163],v[176:179],v[216:219],a[160:163]
v_fma_f32 v125, -v72, v72, v73
cv v124,v124,v124
mm a[164:167],v[184:187],v[216:219],a[164:167]
s_xor_b32 m0, s82, 4096
v_lshlrev_b32 v124, 16, v124
mm a[164:167],v[188:191],v[220:223],a[164:167]
ld v228,s[40:43],s79 offen lds
mm a[212:215],v[188:191],v[212:215],a[212:215]
v_max_f32 v125, 0, v125
cv v125,v125,v125
mm a[212:215],v[184:187],v[208:211],a[212:215]
rt v[44:45],v241 offset:17024
rt v[46:47],v241 offset:18048
mm a[156:159],v[184:187],v[200:203],a[156:159]
s_xor_b32 m0, s82, 5120
v_lshlrev_b32 v125, 16, v125
mm a[156:159],v[188:191],v[204:207],a[156:159]
ld v229,s[40:43],s79 offen lds
mm a[236:239],v[188:191],v[196:199],a[236:239]
v_add_f32 v125, 0x3727c5ac, v125
cv v125,v125,v125
mm a[236:239],v[184:187],v[192:195],a[236:239]
v_lshlrev_b32 v125, 16, v125
mm a[200:203],v[0:3],v[192:195],a[200:203]
s_xor_b32 m0, s82, 6144
mm a[200:203],v[4:7],v[196:199],a[200:203]
ld v230,s[40:43],s79 offen lds
mm a[184:187],v[4:7],v[204:207],a[184:187]
v_sqrt_f32 v125, v125
mm a[184:187],v[0:3],v[200:203],a[184:187]
rt v[48:49],v242 offset:128
rt v[50:51],v242 offset:1152
mm a[64:67],v[0:3],v[208:211],a[64:67]
s_xor_b32 m0, s82, 7168
v_sub_f32 v124, v70, v124
mm a[64:67],v[4:7],v[212:215],a[64:67]
ld v231,s[40:43],s79 offen lds
mm a[0:3],v[4:7],v[220:223],a[0:3]
s_mov_b32 s79, 128
cv v125,v125,v125
mm a[0:3],v[0:3],v[216:219],a[0:3]
v_lshlrev_b32 v125, 16, v125
mm a[4:7],v[8:11],v[216:219],a[4:7]
v_rcp_f32 v125, v125
mm a[4:7],v[12:15],v[220:223],a[4:7]
v_mul_f32 v125, 0x3e0293ee, v125
v_mul_f32 v124, v124, v125
mm a[68:71],v[12:15],v[212:215],a[68:71]
v_fma_f32 v88, v88, v125, v124
v_fma_f32 v89, v89, v125, v124
mm a[68:71],v[8:11],v[208:211],a[68:71]
rt v[52:53],v242 offset:17024
rt v[54:55],v242 offset:18048
mm a[188:191],v[8:11],v[200:203],a[188:191]
cv v72,v88,v89
v_fma_f32 v90, v90, v125, v124
mm a[188:191],v[12:15],v[204:207],a[188:191]
v_fma_f32 v91, v91, v125, v124
cv v73,v90,v91
mm a[204:207],v[12:15],v[196:199],a[204:207]
v_fma_f32 v92, v92, v125, v124
v_fma_f32 v93, v93, v125, v124
mm a[204:207],v[8:11],v[192:195],a[204:207]
rt v[56:57],v243 offset:128
rt v[58:59],v243 offset:1152
mm a[88:91],v[16:19],v[192:195],a[88:91]
cv v74,v92,v93
v_fma_f32 v94, v94, v125, v124
mm a[88:91],v[20:23],v[196:199],a[88:91]
v_fma_f32 v95, v95, v125, v124
cv v75,v94,v95
mm a[24:27],v[20:23],v[204:207],a[24:27]
st v[72:75],v71,s[56:59],0 offen offset:0 sc1
mm a[24:27],v[16:19],v[200:203],a[24:27]
rt v[60:61],v243 offset:17024
rt v[62:63],v243 offset:18048
mm a[240:243],v[16:19],v[208:211],a[240:243]
v_fma_f32 v96, v96, v125, v124
v_fma_f32 v97, v97, v125, v124
mm a[240:243],v[20:23],v[212:215],a[240:243]
cv v76,v96,v97
v_fma_f32 v98, v98, v125, v124
mm a[32:35],v[20:23],v[220:223],a[32:35]
v_fma_f32 v99, v99, v125, v124
cv v77,v98,v99
mm a[32:35],v[16:19],v[216:219],a[32:35]
v_fma_f32 v100, v100, v125, v124
v_fma_f32 v101, v101, v125, v124
mm a[36:39],v[24:27],v[216:219],a[36:39]
cv v78,v100,v101
v_fma_f32 v102, v102, v125, v124
mm a[36:39],v[28:31],v[220:223],a[36:39]
v_fma_f32 v103, v103, v125, v124
cv v79,v102,v103
mm a[244:247],v[28:31],v[212:215],a[244:247]
st v[76:79],v71,s[56:59],0 offen offset:2048 sc1
mm a[244:247],v[24:27],v[208:211],a[244:247]
v_fma_f32 v104, v104, v125, v124
v_fma_f32 v105, v105, v125, v124
mm a[28:31],v[24:27],v[200:203],a[28:31]
cv v80,v104,v105
v_fma_f32 v106, v106, v125, v124
mm a[28:31],v[28:31],v[204:207],a[28:31]
v_fma_f32 v107, v107, v125, v124
cv v81,v106,v107
mm a[92:95],v[28:31],v[196:199],a[92:95]
wt vmcnt(11) lgkmcnt(0)
s_barrier
mm a[92:95],v[24:27],v[192:195],a[92:95]
v_fma_f32 v108, v108, v125, v124
v_fma_f32 v109, v109, v125, v124
mm a[120:123],v[160:163],v[32:35],a[120:123]
cv v82,v108,v109
v_fma_f32 v110, v110, v125, v124
mm a[120:123],v[164:167],v[36:39],a[120:123]
v_fma_f32 v111, v111, v125, v124
cv v83,v110,v111
mm a[136:139],v[164:167],v[44:47],a[136:139]
rt v[192:193],v240 offset:33792
rt v[194:195],v240 offset:34816
mm a[136:139],v[160:163],v[40:43],a[136:139]
rt v[196:197],v240 offset:50688
rt v[198:199],v240 offset:51712
mm a[224:227],v[160:163],v[48:51],a[224:227]
st v[80:83],v71,s[56:59],s88 offen offset:0 sc1
mm a[224:227],v[164:167],v[52:55],a[224:227]
v_fma_f32 v112, v112, v125, v124
v_fma_f32 v113, v113, v125, v124
mm a[144:147],v[164:167],v[60:63],a[144:147]
cv v84,v112,v113
v_fma_f32 v114, v114, v125, v124
mm a[144:147],v[160:163],v[56:59],a[144:147]
rd v[160:163],v236 offset:57344
rd v[164:167],v237 offset:57344
mm a[148:151],v[168:171],v[56:59],a[148:151]
v_fma_f32 v115, v115, v125, v124
cv v85,v114,v115
mm a[148:151],v[172:175],v[60:63],a[148:151]
v_fma_f32 v116, v116, v125, v124
v_fma_f32 v117, v117, v125, v124
mm a[228:231],v[172:175],v[52:55],a[228:231]
cv v86,v116,v117
v_fma_f32 v118, v118, v125, v124
mm a[228:231],v[168:171],v[48:51],a[228:231]
rt v[200:201],v241 offset:33792
rt v[202:203],v241 offset:34816
mm a[140:143],v[168:171],v[40:43],a[140:143]
v_fma_f32 v119, v119, v125, v124
cv v87,v118,v119
mm a[140:143],v[172:175],v[44:47],a[140:143]
st v[84:87],v71,s[56:59],s88 offen offset:2048 sc1
mm a[124:127],v[172:175],v[36:39],a[124:127]
mm a[124:127],v[168:171],v[32:35],a[124:127]
rd v[168:171],v236 offset:59392
rd v[172:175],v237 offset:59392
mm a[216:219],v[176:179],v[32:35],a[216:219]
mm a[216:219],v[180:183],v[36:39],a[216:219]
mm a[168:171],v[180:183],v[44:47],a[168:171]
mm a[168:171],v[176:179],v[40:43],a[168:171]
rt v[204:205],v241 offset:50688
rt v[206:207],v241 offset:51712
mm a[192:195],v[176:179],v[48:51],a[192:195]
mm a[192:195],v[180:183],v[52:55],a[192:195]
mm a[176:179],v[180:183],v[60:63],a[176:179]
mm a[176:179],v[176:179],v[56:59],a[176:179]
rd v[176:179],v236 offset:49152
rd v[180:183],v237 offset:49152
mm a[180:183],v[184:187],v[56:59],a[180:183]
mm a[180:183],v[188:191],v[60:63],a[180:183]
mm a[196:199],v[188:191],v[52:55],a[196:199]
mm a[196:199],v[184:187],v[48:51],a[196:199]
rt v[208:209],v242 offset:33792
rt v[210:211],v242 offset:34816
mm a[172:175],v[184:187],v[40:43],a[172:175]
s_add_u32 m0, s83, 0
mm a[172:175],v[188:191],v[44:47],a[172:175]
ld v232,s[44:47],s80 offen lds
mm a[220:223],v[188:191],v[36:39],a[220:223]
mm a[220:223],v[184:187],v[32:35],a[220:223]
rd v[184:187],v236 offset:51200
rd v[188:191],v237 offset:51200
mm a[72:75],v[0:3],v[32:35],a[72:75]
s_add_u32 m0, s83, 1024
mm a[72:75],v[4:7],v[36:39],a[72:75]
ld v233,s[44:47],s80 offen lds
mm a[8:11],v[4:7],v[44:47],a[8:11]
mm a[8:11],v[0:3],v[40:43],a[8:11]
rt v[212:213],v242 offset:50688
rt v[214:215],v242 offset:51712
mm a[80:83],v[0:3],v[48:51],a[80:83]
s_add_u32 m0, s83, 2176
mm a[80:83],v[4:7],v[52:55],a[80:83]
ld v234,s[44:47],s80 offen lds
mm a[16:19],v[4:7],v[60:63],a[16:19]
mm a[16:19],v[0:3],v[56:59],a[16:19]
rd v[0:3],v236 offset:40960
rd v[4:7],v237 offset:40960
mm a[20:23],v[8:11],v[56:59],a[20:23]
s_add_u32 m0, s83, 3200
mm a[20:23],v[12:15],v[60:63],a[20:23]
ld v235,s[44:47],s80 offen lds
mm a[84:87],v[12:15],v[52:55],a[84:87]
s_mov_b32 s80, 262144
mm a[84:87],v[8:11],v[48:51],a[84:87]
rt v[216:217],v243 offset:33792
rt v[218:219],v243 offset:34816
mm a[12:15],v[8:11],v[40:43],a[12:15]
s_add_u32 m0, s83, 16896
mm a[12:15],v[12:15],v[44:47],a[12:15]
ld v232,s[44:47],s80 offen lds
mm a[76:79],v[12:15],v[36:39],a[76:79]
mm a[76:79],v[8:11],v[32:35],a[76:79]
rd v[8:11],v236 offset:43008
rd v[12:15],v237 offset:43008
mm a[248:251],v[16:19],v[32:35],a[248:251]
s_add_u32 m0, s83, 17920
mm a[248:251],v[20:23],v[36:39],a[248:251]
ld v233,s[44:47],s80 offen lds
mm a[40:43],v[20:23],v[44:47],a[40:43]
mm a[40:43],v[16:19],v[40:43],a[40:43]
rt v[220:221],v243 offset:50688
rt v[222:223],v243 offset:51712
mm a[48:51],v[16:19],v[48:51],a[48:51]
s_add_u32 m0, s83, 19072
mm a[48:51],v[20:23],v[52:55],a[48:51]
ld v234,s[44:47],s80 offen lds
mm a[56:59],v[20:23],v[60:63],a[56:59]
mm a[56:59],v[16:19],v[56:59],a[56:59]
rd v[16:19],v236 offset:32768
rd v[20:23],v237 offset:32768
mm a[60:63],v[24:27],v[56:59],a[60:63]
s_add_u32 m0, s83, 20096
mm a[60:63],v[28:31],v[60:63],a[60:63]
.La5_LW1_6:
s_cbranch_execz .La5_LW1_10
ld v235,s[44:47],s80 offen lds
mm a[52:55],v[28:31],v[52:55],a[52:55]
s_mov_b32 s80, 524288
mm a[52:55],v[24:27],v[48:51],a[52:55]
mm a[44:47],v[24:27],v[40:43],a[44:47]
mm a[44:47],v[28:31],v[44:47],a[44:47]
mm a[252:255],v[28:31],v[36:39],a[252:255]
wt lgkmcnt(2)
mm a[252:255],v[24:27],v[32:35],a[252:255]
rd v[24:27],v236 offset:34816
rd v[28:31],v237 offset:34816
mm a[96:99],v[160:163],v[192:195],a[96:99]
mm a[96:99],v[164:167],v[196:199],a[96:99]
mm a[104:107],v[164:167],v[204:207],a[104:107]
mm a[104:107],v[160:163],v[200:203],a[104:107]
rt v[32:33],v240 offset:33920
rt v[34:35],v240 offset:34944
mm a[112:115],v[160:163],v[208:211],a[112:115]
mm a[112:115],v[164:167],v[212:215],a[112:115]
mm a[128:131],v[164:167],v[220:223],a[128:131]
wt lgkmcnt(2)
s_barrier
mm a[128:131],v[160:163],v[216:219],a[128:131]
mm a[132:135],v[168:171],v[216:219],a[132:135]
s_xor_b32 m0, s82, 32768
mm a[132:135],v[172:175],v[220:223],a[132:135]
ld v224,s[40:43],s79 offen lds
mm a[116:119],v[172:175],v[212:215],a[116:119]
mm a[116:119],v[168:171],v[208:211],a[116:119]
rt v[36:37],v240 offset:50816
rt v[38:39],v240 offset:51840
mm a[108:111],v[168:171],v[200:203],a[108:111]
s_xor_b32 m0, s82, 33792
mm a[108:111],v[172:175],v[204:207],a[108:111]
ld v225,s[40:43],s79 offen lds
mm a[100:103],v[172:175],v[196:199],a[100:103]
mm a[100:103],v[168:171],v[192:195],a[100:103]
mm a[232:235],v[176:179],v[192:195],a[232:235]
s_xor_b32 m0, s82, 34816
mm a[232:235],v[180:183],v[196:199],a[232:235]
ld v226,s[40:43],s79 offen lds
mm a[152:155],v[180:183],v[204:207],a[152:155]
mm a[152:155],v[176:179],v[200:203],a[152:155]
rt v[40:41],v241 offset:33920
rt v[42:43],v241 offset:34944
mm a[208:211],v[176:179],v[208:211],a[208:211]
s_xor_b32 m0, s82, 35840
mm a[208:211],v[180:183],v[212:215],a[208:211]
ld v227,s[40:43],s79 offen lds
mm a[160:163],v[180:183],v[220:223],a[160:163]
mm a[160:163],v[176:179],v[216:219],a[160:163]
mm a[164:167],v[184:187],v[216:219],a[164:167]
s_xor_b32 m0, s82, 36864
mm a[164:167],v[188:191],v[220:223],a[164:167]
ld v228,s[40:43],s79 offen lds
mm a[212:215],v[188:191],v[212:215],a[212:215]
mm a[212:215],v[184:187],v[208:211],a[212:215]
rt v[44:45],v241 offset:50816
rt v[46:47],v241 offset:51840
mm a[156:159],v[184:187],v[200:203],a[156:159]
s_xor_b32 m0, s82, 37888
mm a[156:159],v[188:191],v[204:207],a[156:159]
ld v229,s[40:43],s79 offen lds
mm a[236:239],v[188:191],v[196:199],a[236:239]
mm a[236:239],v[184:187],v[192:195],a[236:239]
mm a[200:203],v[0:3],v[192:195],a[200:203]
s_xor_b32 m0, s82, 38912
mm a[200:203],v[4:7],v[196:199],a[200:203]
ld v230,s[40:43],s79 offen lds
mm a[184:187],v[4:7],v[204:207],a[184:187]
mm a[184:187],v[0:3],v[200:203],a[184:187]
rt v[48:49],v242 offset:33920
rt v[50:51],v242 offset:34944
mm a[64:67],v[0:3],v[208:211],a[64:67]
s_xor_b32 m0, s82, 39936
mm a[64:67],v[4:7],v[212:215],a[64:67]
ld v231,s[40:43],s79 offen lds
mm a[0:3],v[4:7],v[220:223],a[0:3]
s_mov_b32 s79, 256
mm a[0:3],v[0:3],v[216:219],a[0:3]
mm a[4:7],v[8:11],v[216:219],a[4:7]
mm a[4:7],v[12:15],v[220:223],a[4:7]
buffer_load_dword v254, v135, s[68:71], s96 offen
mm a[68:71],v[12:15],v[212:215],a[68:71]
s_add_u32 s96, s96, 1048576
mm a[68:71],v[8:11],v[208:211],a[68:71]
rt v[52:53],v242 offset:50816
rt v[54:55],v242 offset:51840
mm a[188:191],v[8:11],v[200:203],a[188:191]
mm a[188:191],v[12:15],v[204:207],a[188:191]
buffer_load_dword v254, v135, s[68:71], s96 offen
mm a[204:207],v[12:15],v[196:199],a[204:207]
s_add_u32 s96, s96, 1048576
mm a[204:207],v[8:11],v[192:195],a[204:207]
rt v[56:57],v243 offset:33920
rt v[58:59],v243 offset:34944
mm a[88:91],v[16:19],v[192:195],a[88:91]
mm a[88:91],v[20:23],v[196:199],a[88:91]
wt vmcnt(18)
buffer_store_dwordx2 v[252:253], off, s[64:67], s91 offset:1024 sc1
mm a[24:27],v[20:23],v[204:207],a[24:27]
s_mov_b32 s91, 0
s_lshl_b32 s92, s76, 19
mm a[24:27],v[16:19],v[200:203],a[24:27]
rt v[60:61],v243 offset:50816
rt v[62:63],v243 offset:51840
mm a[240:243],v[16:19],v[208:211],a[240:243]
s_add_u32 s92, s92, 88080384
mm a[240:243],v[20:23],v[212:215],a[240:243]
s_add_u32 s52, s26, s92
s_addc_u32 s53, s27, 0
mm a[32:35],v[20:23],v[220:223],a[32:35]
s_and_b32 s53, s53, 0xffff
s_mov_b32 s54, 524672
mm a[32:35],v[16:19],v[216:219],a[32:35]
s_mov_b32 s55, 0x20000
s_lshl_b32 s92, s3, 16
mm a[36:39],v[24:27],v[216:219],a[36:39]
s_sub_u32 s93, 458752, s92
v_add_u32 v244, s92, v136
mm a[36:39],v[28:31],v[220:223],a[36:39]
v_add_u32 v246, s93, v136
v_add_u32 v245, 0x8000, v244
mm a[244:247],v[28:31],v[212:215],a[244:247]
v_add_u32 v247, 0x8000, v246
s_lshl_b32 s94, s72, 19
mm a[244:247],v[24:27],v[208:211],a[244:247]
s_lshl_b32 s93, s76, 16
s_add_u32 s92, s94, s93
mm a[28:31],v[24:27],v[200:203],a[28:31]
s_lshl_b32 s93, s77, 13
s_add_u32 s92, s92, s93
mm a[28:31],v[28:31],v[204:207],a[28:31]
s_add_u32 s92, s92, 37748736
mm a[92:95],v[28:31],v[196:199],a[92:95]
wt vmcnt(11) lgkmcnt(0)
s_barrier
mm a[92:95],v[24:27],v[192:195],a[92:95]
s_add_u32 s48, s26, s92
s_addc_u32 s49, s27, 0
mm a[120:123],v[160:163],v[32:35],a[120:123]
s_and_b32 s49, s49, 0xffff
s_mov_b32 s50, 57344
mm a[120:123],v[164:167],v[36:39],a[120:123]
s_mov_b32 s51, 0x20000
s_lshl_b32 s93, s77, 24
mm a[136:139],v[164:167],v[44:47],a[136:139]
rt v[192:193],v240 offset:0
rt v[194:195],v240 offset:1024
mm a[136:139],v[160:163],v[40:43],a[136:139]
rt v[196:197],v240 offset:16896
rt v[198:199],v240 offset:17920
mm a[224:227],v[160:163],v[48:51],a[224:227]
s_add_u32 s92, s94, s93
s_add_u32 s92, s92, 20975616
mm a[224:227],v[164:167],v[52:55],a[224:227]
s_add_u32 s60, s26, s92
s_addc_u32 s61, s27, 0
mm a[144:147],v[164:167],v[60:63],a[144:147]
s_and_b32 s61, s61, 0xffff
s_mov_b32 s62, 520192
mm a[144:147],v[160:163],v[56:59],a[144:147]
rd v[160:163],v236 offset:24576
rd v[164:167],v237 offset:24576
mm a[148:151],v[168:171],v[56:59],a[148:151]
s_mov_b32 s63, 0x20000
s_lshl_b32 s92, s77, 14
mm a[148:151],v[172:175],v[60:63],a[148:151]
s_sub_u32 s95, 24576, s92
v_add_u32 v238, s95, v255
mm a[228:231],v[172:175],v[52:55],a[228:231]
mm a[228:231],v[168:171],v[48:51],a[228:231]
rt v[200:201],v241 offset:0
rt v[202:203],v241 offset:1024
mm a[140:143],v[168:171],v[40:43],a[140:143]
mm a[140:143],v[172:175],v[44:47],a[140:143]
mm a[124:127],v[172:175],v[36:39],a[124:127]
mm a[124:127],v[168:171],v[32:35],a[124:127]
rd v[168:171],v236 offset:26624
rd v[172:175],v237 offset:26624
mm a[216:219],v[176:179],v[32:35],a[216:219]
mm a[216:219],v[180:183],v[36:39],a[216:219]
mm a[168:171],v[180:183],v[44:47],a[168:171]
mm a[168:171],v[176:179],v[40:43],a[168:171]
rt v[204:205],v241 offset:16896
rt v[206:207],v241 offset:17920
mm a[192:195],v[176:179],v[48:51],a[192:195]
mm a[192:195],v[180:183],v[52:55],a[192:195]
mm a[176:179],v[180:183],v[60:63],a[176:179]
mm a[176:179],v[176:179],v[56:59],a[176:179]
rd v[176:179],v236 offset:16384
rd v[180:183],v237 offset:16384
mm a[180:183],v[184:187],v[56:59],a[180:183]
mm a[180:183],v[188:191],v[60:63],a[180:183]
mm a[196:199],v[188:191],v[52:55],a[196:199]
mm a[196:199],v[184:187],v[48:51],a[196:199]
rt v[208:209],v242 offset:0
rt v[210:211],v242 offset:1024
mm a[172:175],v[184:187],v[40:43],a[172:175]
s_add_u32 m0, s83, 33792
mm a[172:175],v[188:191],v[44:47],a[172:175]
ld v232,s[44:47],s80 offen lds
mm a[220:223],v[188:191],v[36:39],a[220:223]
mm a[220:223],v[184:187],v[32:35],a[220:223]
rd v[184:187],v236 offset:18432
rd v[188:191],v237 offset:18432
mm a[72:75],v[0:3],v[32:35],a[72:75]
s_add_u32 m0, s83, 34816
mm a[72:75],v[4:7],v[36:39],a[72:75]
ld v233,s[44:47],s80 offen lds
mm a[8:11],v[4:7],v[44:47],a[8:11]
mm a[8:11],v[0:3],v[40:43],a[8:11]
rt v[212:213],v242 offset:16896
rt v[214:215],v242 offset:17920
mm a[80:83],v[0:3],v[48:51],a[80:83]
s_add_u32 m0, s83, 35968
mm a[80:83],v[4:7],v[52:55],a[80:83]
ld v234,s[44:47],s80 offen lds
mm a[16:19],v[4:7],v[60:63],a[16:19]
mm a[16:19],v[0:3],v[56:59],a[16:19]
rd v[0:3],v236 offset:8192
rd v[4:7],v237 offset:8192
mm a[20:23],v[8:11],v[56:59],a[20:23]
s_add_u32 m0, s83, 36992
mm a[20:23],v[12:15],v[60:63],a[20:23]
ld v235,s[44:47],s80 offen lds
mm a[84:87],v[12:15],v[52:55],a[84:87]
s_mov_b32 s80, 786432
mm a[84:87],v[8:11],v[48:51],a[84:87]
rt v[216:217],v243 offset:0
rt v[218:219],v243 offset:1024
mm a[12:15],v[8:11],v[40:43],a[12:15]
s_add_u32 m0, s83, 50688
mm a[12:15],v[12:15],v[44:47],a[12:15]
ld v232,s[44:47],s80 offen lds
mm a[76:79],v[12:15],v[36:39],a[76:79]
mm a[76:79],v[8:11],v[32:35],a[76:79]
rd v[8:11],v236 offset:10240
rd v[12:15],v237 offset:10240
mm a[248:251],v[16:19],v[32:35],a[248:251]
s_add_u32 m0, s83, 51712
mm a[248:251],v[20:23],v[36:39],a[248:251]
ld v233,s[44:47],s80 offen lds
mm a[40:43],v[20:23],v[44:47],a[40:43]
mm a[40:43],v[16:19],v[40:43],a[40:43]
rt v[220:221],v243 offset:16896
rt v[222:223],v243 offset:17920
mm a[48:51],v[16:19],v[48:51],a[48:51]
s_add_u32 m0, s83, 52864
mm a[48:51],v[20:23],v[52:55],a[48:51]
ld v234,s[44:47],s80 offen lds
mm a[56:59],v[20:23],v[60:63],a[56:59]
mm a[56:59],v[16:19],v[56:59],a[56:59]
rd v[16:19],v236 offset:0
rd v[20:23],v237 offset:0
mm a[60:63],v[24:27],v[56:59],a[60:63]
s_add_u32 m0, s83, 53888
mm a[60:63],v[28:31],v[60:63],a[60:63]
ld v235,s[44:47],s80 offen lds
mm a[52:55],v[28:31],v[52:55],a[52:55]
s_mov_b32 s80, 1048576
mm a[52:55],v[24:27],v[48:51],a[52:55]
mm a[44:47],v[24:27],v[40:43],a[44:47]
mm a[44:47],v[28:31],v[44:47],a[44:47]
mm a[252:255],v[28:31],v[36:39],a[252:255]
wt lgkmcnt(2)
mm a[252:255],v[24:27],v[32:35],a[252:255]
rd v[24:27],v236 offset:2048
rd v[28:31],v237 offset:2048
mm a[96:99],v[160:163],v[192:195],a[96:99]
mm a[96:99],v[164:167],v[196:199],a[96:99]
mm a[104:107],v[164:167],v[204:207],a[104:107]
mm a[104:107],v[160:163],v[200:203],a[104:107]
rt v[32:33],v240 offset:128
rt v[34:35],v240 offset:1152
mm a[112:115],v[160:163],v[208:211],a[112:115]
mm a[112:115],v[164:167],v[212:215],a[112:115]
mm a[128:131],v[164:167],v[220:223],a[128:131]
wt lgkmcnt(2)
s_barrier
mm a[128:131],v[160:163],v[216:219],a[128:131]
mm a[132:135],v[168:171],v[216:219],a[132:135]
s_xor_b32 m0, s82, 0
mm a[132:135],v[172:175],v[220:223],a[132:135]
ld v224,s[40:43],s79 offen lds
mm a[116:119],v[172:175],v[212:215],a[116:119]
mm a[116:119],v[168:171],v[208:211],a[116:119]
rt v[36:37],v240 offset:17024
rt v[38:39],v240 offset:18048
mm a[108:111],v[168:171],v[200:203],a[108:111]
s_xor_b32 m0, s82, 1024
mm a[108:111],v[172:175],v[204:207],a[108:111]
ld v225,s[40:43],s79 offen lds
mm a[100:103],v[172:175],v[196:199],a[100:103]
mm a[100:103],v[168:171],v[192:195],a[100:103]
mm a[232:235],v[176:179],v[192:195],a[232:235]
s_xor_b32 m0, s82, 2048
mm a[232:235],v[180:183],v[196:199],a[232:235]
ld v226,s[40:43],s79 offen lds
mm a[152:155],v[180:183],v[204:207],a[152:155]
mm a[152:155],v[176:179],v[200:203],a[152:155]
rt v[40:41],v241 offset:128
rt v[42:43],v241 offset:1152
mm a[208:211],v[176:179],v[208:211],a[208:211]
s_xor_b32 m0, s82, 3072
mm a[208:211],v[180:183],v[212:215],a[208:211]
ld v227,s[40:43],s79 offen lds
mm a[160:163],v[180:183],v[220:223],a[160:163]
mm a[160:163],v[176:179],v[216:219],a[160:163]
mm a[164:167],v[184:187],v[216:219],a[164:167]
s_xor_b32 m0, s82, 4096
mm a[164:167],v[188:191],v[220:223],a[164:167]
ld v228,s[40:43],s79 offen lds
mm a[212:215],v[188:191],v[212:215],a[212:215]
mm a[212:215],v[184:187],v[208:211],a[212:215]
rt v[44:45],v241 offset:17024
rt v[46:47],v241 offset:18048
mm a[156:159],v[184:187],v[200:203],a[156:159]
s_xor_b32 m0, s82, 5120
mm a[156:159],v[188:191],v[204:207],a[156:159]
ld v229,s[40:43],s79 offen lds
mm a[236:239],v[188:191],v[196:199],a[236:239]
mm a[236:239],v[184:187],v[192:195],a[236:239]
mm a[200:203],v[0:3],v[192:195],a[200:203]
s_xor_b32 m0, s82, 6144
mm a[200:203],v[4:7],v[196:199],a[200:203]
ld v230,s[40:43],s79 offen lds
mm a[184:187],v[4:7],v[204:207],a[184:187]
mm a[184:187],v[0:3],v[200:203],a[184:187]
rt v[48:49],v242 offset:128
rt v[50:51],v242 offset:1152
mm a[64:67],v[0:3],v[208:211],a[64:67]
s_xor_b32 m0, s82, 7168
mm a[64:67],v[4:7],v[212:215],a[64:67]
ld v231,s[40:43],s79 offen lds
mm a[0:3],v[4:7],v[220:223],a[0:3]
s_mov_b32 s79, 384
mm a[0:3],v[0:3],v[216:219],a[0:3]
mm a[4:7],v[8:11],v[216:219],a[4:7]
mm a[4:7],v[12:15],v[220:223],a[4:7]
buffer_load_dword v254, v135, s[68:71], s96 offen
mm a[68:71],v[12:15],v[212:215],a[68:71]
s_add_u32 s96, s96, 1048576
mm a[68:71],v[8:11],v[208:211],a[68:71]
rt v[52:53],v242 offset:17024
rt v[54:55],v242 offset:18048
mm a[188:191],v[8:11],v[200:203],a[188:191]
mm a[188:191],v[12:15],v[204:207],a[188:191]
buffer_load_dword v254, v135, s[68:71], s96 offen
mm a[204:207],v[12:15],v[196:199],a[204:207]
s_add_u32 s96, s96, 1048576
mm a[204:207],v[8:11],v[192:195],a[204:207]
rt v[56:57],v243 offset:128
rt v[58:59],v243 offset:1152
mm a[88:91],v[16:19],v[192:195],a[88:91]
mm a[88:91],v[20:23],v[196:199],a[88:91]
mm a[24:27],v[20:23],v[204:207],a[24:27]
mm a[24:27],v[16:19],v[200:203],a[24:27]
rt v[60:61],v243 offset:17024
rt v[62:63],v243 offset:18048
mm a[240:243],v[16:19],v[208:211],a[240:243]
mm a[240:243],v[20:23],v[212:215],a[240:243]
mm a[32:35],v[20:23],v[220:223],a[32:35]
mm a[32:35],v[16:19],v[216:219],a[32:35]
mm a[36:39],v[24:27],v[216:219],a[36:39]
mm a[36:39],v[28:31],v[220:223],a[36:39]
mm a[244:247],v[28:31],v[212:215],a[244:247]
mm a[244:247],v[24:27],v[208:211],a[244:247]
mm a[28:31],v[24:27],v[200:203],a[28:31]
mm a[28:31],v[28:31],v[204:207],a[28:31]
mm a[92:95],v[28:31],v[196:199],a[92:95]
wt vmcnt(10) lgkmcnt(0)
s_barrier
mm a[92:95],v[24:27],v[192:195],a[92:95]
mm a[120:123],v[160:163],v[32:35],a[120:123]
mm a[120:123],v[164:167],v[36:39],a[120:123]
mm a[136:139],v[164:167],v[44:47],a[136:139]
rt v[192:193],v240 offset:33792
rt v[194:195],v240 offset:34816
mm a[136:139],v[160:163],v[40:43],a[136:139]
rt v[196:197],v240 offset:50688
rt v[198:199],v240 offset:51712
mm a[224:227],v[160:163],v[48:51],a[224:227]
mm a[224:227],v[164:167],v[52:55],a[224:227]
mm a[144:147],v[164:167],v[60:63],a[144:147]
mm a[144:147],v[160:163],v[56:59],a[144:147]
rd v[160:163],v236 offset:57344
rd v[164:167],v237 offset:57344
mm a[148:151],v[168:171],v[56:59],a[148:151]
mm a[148:151],v[172:175],v[60:63],a[148:151]
mm a[228:231],v[172:175],v[52:55],a[228:231]
mm a[228:231],v[168:171],v[48:51],a[228:231]
rt v[200:201],v241 offset:33792
rt v[202:203],v241 offset:34816
mm a[140:143],v[168:171],v[40:43],a[140:143]
mm a[140:143],v[172:175],v[44:47],a[140:143]
mm a[124:127],v[172:175],v[36:39],a[124:127]
mm a[124:127],v[168:171],v[32:35],a[124:127]
rd v[168:171],v236 offset:59392
rd v[172:175],v237 offset:59392
mm a[216:219],v[176:179],v[32:35],a[216:219]
mm a[216:219],v[180:183],v[36:39],a[216:219]
mm a[168:171],v[180:183],v[44:47],a[168:171]
mm a[168:171],v[176:179],v[40:43],a[168:171]
rt v[204:205],v241 offset:50688
rt v[206:207],v241 offset:51712
mm a[192:195],v[176:179],v[48:51],a[192:195]
mm a[192:195],v[180:183],v[52:55],a[192:195]
mm a[176:179],v[180:183],v[60:63],a[176:179]
mm a[176:179],v[176:179],v[56:59],a[176:179]
rd v[176:179],v236 offset:49152
rd v[180:183],v237 offset:49152
mm a[180:183],v[184:187],v[56:59],a[180:183]
mm a[180:183],v[188:191],v[60:63],a[180:183]
mm a[196:199],v[188:191],v[52:55],a[196:199]
mm a[196:199],v[184:187],v[48:51],a[196:199]
rt v[208:209],v242 offset:33792
rt v[210:211],v242 offset:34816
mm a[172:175],v[184:187],v[40:43],a[172:175]
s_add_u32 m0, s83, 0
mm a[172:175],v[188:191],v[44:47],a[172:175]
ld v232,s[44:47],s80 offen lds
mm a[220:223],v[188:191],v[36:39],a[220:223]
mm a[220:223],v[184:187],v[32:35],a[220:223]
rd v[184:187],v236 offset:51200
rd v[188:191],v237 offset:51200
mm a[72:75],v[0:3],v[32:35],a[72:75]
s_add_u32 m0, s83, 1024
mm a[72:75],v[4:7],v[36:39],a[72:75]
ld v233,s[44:47],s80 offen lds
mm a[8:11],v[4:7],v[44:47],a[8:11]
mm a[8:11],v[0:3],v[40:43],a[8:11]
rt v[212:213],v242 offset:50688
rt v[214:215],v242 offset:51712
mm a[80:83],v[0:3],v[48:51],a[80:83]
s_add_u32 m0, s83, 2176
mm a[80:83],v[4:7],v[52:55],a[80:83]
ld v234,s[44:47],s80 offen lds
mm a[16:19],v[4:7],v[60:63],a[16:19]
mm a[16:19],v[0:3],v[56:59],a[16:19]
rd v[0:3],v236 offset:40960
rd v[4:7],v237 offset:40960
mm a[20:23],v[8:11],v[56:59],a[20:23]
s_add_u32 m0, s83, 3200
mm a[20:23],v[12:15],v[60:63],a[20:23]
ld v235,s[44:47],s80 offen lds
mm a[84:87],v[12:15],v[52:55],a[84:87]
s_mov_b32 s80, 1310720
mm a[84:87],v[8:11],v[48:51],a[84:87]
rt v[216:217],v243 offset:33792
rt v[218:219],v243 offset:34816
mm a[12:15],v[8:11],v[40:43],a[12:15]
s_add_u32 m0, s83, 16896
mm a[12:15],v[12:15],v[44:47],a[12:15]
ld v232,s[44:47],s80 offen lds
mm a[76:79],v[12:15],v[36:39],a[76:79]
mm a[76:79],v[8:11],v[32:35],a[76:79]
rd v[8:11],v236 offset:43008
rd v[12:15],v237 offset:43008
mm a[248:251],v[16:19],v[32:35],a[248:251]
s_add_u32 m0, s83, 17920
mm a[248:251],v[20:23],v[36:39],a[248:251]
ld v233,s[44:47],s80 offen lds
mm a[40:43],v[20:23],v[44:47],a[40:43]
mm a[40:43],v[16:19],v[40:43],a[40:43]
rt v[220:221],v243 offset:50688
rt v[222:223],v243 offset:51712
mm a[48:51],v[16:19],v[48:51],a[48:51]
s_add_u32 m0, s83, 19072
mm a[48:51],v[20:23],v[52:55],a[48:51]
ld v234,s[44:47],s80 offen lds
mm a[56:59],v[20:23],v[60:63],a[56:59]
mm a[56:59],v[16:19],v[56:59],a[56:59]
rd v[16:19],v236 offset:32768
rd v[20:23],v237 offset:32768
mm a[60:63],v[24:27],v[56:59],a[60:63]
s_add_u32 m0, s83, 20096
mm a[60:63],v[28:31],v[60:63],a[60:63]
ld v235,s[44:47],s80 offen lds
mm a[52:55],v[28:31],v[52:55],a[52:55]
s_mov_b32 s80, 1572864
mm a[52:55],v[24:27],v[48:51],a[52:55]
mm a[44:47],v[24:27],v[40:43],a[44:47]
mm a[44:47],v[28:31],v[44:47],a[44:47]
mm a[252:255],v[28:31],v[36:39],a[252:255]
wt lgkmcnt(2)
mm a[252:255],v[24:27],v[32:35],a[252:255]
rd v[24:27],v236 offset:34816
rd v[28:31],v237 offset:34816
mm a[96:99],v[160:163],v[192:195],a[96:99]
mm a[96:99],v[164:167],v[196:199],a[96:99]
mm a[104:107],v[164:167],v[204:207],a[104:107]
mm a[104:107],v[160:163],v[200:203],a[104:107]
rt v[32:33],v240 offset:33920
rt v[34:35],v240 offset:34944
mm a[112:115],v[160:163],v[208:211],a[112:115]
mm a[112:115],v[164:167],v[212:215],a[112:115]
mm a[128:131],v[164:167],v[220:223],a[128:131]
wt lgkmcnt(2)
s_barrier
mm a[128:131],v[160:163],v[216:219],a[128:131]
mm a[132:135],v[168:171],v[216:219],a[132:135]
s_xor_b32 m0, s82, 32768
mm a[132:135],v[172:175],v[220:223],a[132:135]
ld v224,s[40:43],s79 offen lds
mm a[116:119],v[172:175],v[212:215],a[116:119]
mm a[116:119],v[168:171],v[208:211],a[116:119]
rt v[36:37],v240 offset:50816
rt v[38:39],v240 offset:51840
mm a[108:111],v[168:171],v[200:203],a[108:111]
s_xor_b32 m0, s82, 33792
mm a[108:111],v[172:175],v[204:207],a[108:111]
ld v225,s[40:43],s79 offen lds
mm a[100:103],v[172:175],v[196:199],a[100:103]
mm a[100:103],v[168:171],v[192:195],a[100:103]
mm a[232:235],v[176:179],v[192:195],a[232:235]
s_xor_b32 m0, s82, 34816
mm a[232:235],v[180:183],v[196:199],a[232:235]
ld v226,s[40:43],s79 offen lds
mm a[152:155],v[180:183],v[204:207],a[152:155]
mm a[152:155],v[176:179],v[200:203],a[152:155]
rt v[40:41],v241 offset:33920
rt v[42:43],v241 offset:34944
mm a[208:211],v[176:179],v[208:211],a[208:211]
s_xor_b32 m0, s82, 35840
mm a[208:211],v[180:183],v[212:215],a[208:211]
ld v227,s[40:43],s79 offen lds
mm a[160:163],v[180:183],v[220:223],a[160:163]
mm a[160:163],v[176:179],v[216:219],a[160:163]
mm a[164:167],v[184:187],v[216:219],a[164:167]
.La5_LW1_7:
s_cbranch_execz .La5_LW1_11
s_xor_b32 m0, s82, 36864
mm a[164:167],v[188:191],v[220:223],a[164:167]
ld v228,s[40:43],s79 offen lds
mm a[212:215],v[188:191],v[212:215],a[212:215]
mm a[212:215],v[184:187],v[208:211],a[212:215]
rt v[44:45],v241 offset:50816
rt v[46:47],v241 offset:51840
mm a[156:159],v[184:187],v[200:203],a[156:159]
s_xor_b32 m0, s82, 37888
mm a[156:159],v[188:191],v[204:207],a[156:159]
ld v229,s[40:43],s79 offen lds
mm a[236:239],v[188:191],v[196:199],a[236:239]
mm a[236:239],v[184:187],v[192:195],a[236:239]
mm a[200:203],v[0:3],v[192:195],a[200:203]
s_xor_b32 m0, s82, 38912
mm a[200:203],v[4:7],v[196:199],a[200:203]
ld v230,s[40:43],s79 offen lds
mm a[184:187],v[4:7],v[204:207],a[184:187]
mm a[184:187],v[0:3],v[200:203],a[184:187]
rt v[48:49],v242 offset:33920
rt v[50:51],v242 offset:34944
mm a[64:67],v[0:3],v[208:211],a[64:67]
s_xor_b32 m0, s82, 39936
mm a[64:67],v[4:7],v[212:215],a[64:67]
ld v231,s[40:43],s79 offen lds
mm a[0:3],v[4:7],v[220:223],a[0:3]
s_mov_b32 s79, 512
mm a[0:3],v[0:3],v[216:219],a[0:3]
mm a[4:7],v[8:11],v[216:219],a[4:7]
mm a[4:7],v[12:15],v[220:223],a[4:7]
buffer_load_dword v254, v135, s[68:71], s96 offen
mm a[68:71],v[12:15],v[212:215],a[68:71]
s_add_u32 s96, s96, 1048576
mm a[68:71],v[8:11],v[208:211],a[68:71]
rt v[52:53],v242 offset:50816
rt v[54:55],v242 offset:51840
mm a[188:191],v[8:11],v[200:203],a[188:191]
mm a[188:191],v[12:15],v[204:207],a[188:191]
buffer_load_dword v254, v135, s[68:71], s96 offen
mm a[204:207],v[12:15],v[196:199],a[204:207]
s_add_u32 s96, s96, 1048576
mm a[204:207],v[8:11],v[192:195],a[204:207]
rt v[56:57],v243 offset:33920
rt v[58:59],v243 offset:34944
mm a[88:91],v[16:19],v[192:195],a[88:91]
mm a[88:91],v[20:23],v[196:199],a[88:91]
mm a[24:27],v[20:23],v[204:207],a[24:27]
mm a[24:27],v[16:19],v[200:203],a[24:27]
rt v[60:61],v243 offset:50816
rt v[62:63],v243 offset:51840
mm a[240:243],v[16:19],v[208:211],a[240:243]
mm a[240:243],v[20:23],v[212:215],a[240:243]
mm a[32:35],v[20:23],v[220:223],a[32:35]
mm a[32:35],v[16:19],v[216:219],a[32:35]
mm a[36:39],v[24:27],v[216:219],a[36:39]
mm a[36:39],v[28:31],v[220:223],a[36:39]
mm a[244:247],v[28:31],v[212:215],a[244:247]
mm a[244:247],v[24:27],v[208:211],a[244:247]
mm a[28:31],v[24:27],v[200:203],a[28:31]
mm a[28:31],v[28:31],v[204:207],a[28:31]
mm a[92:95],v[28:31],v[196:199],a[92:95]
wt vmcnt(10) lgkmcnt(0)
s_barrier
mm a[92:95],v[24:27],v[192:195],a[92:95]
mm a[120:123],v[160:163],v[32:35],a[120:123]
mm a[120:123],v[164:167],v[36:39],a[120:123]
mm a[136:139],v[164:167],v[44:47],a[136:139]
rt v[192:193],v240 offset:0
rt v[194:195],v240 offset:1024
mm a[136:139],v[160:163],v[40:43],a[136:139]
rt v[196:197],v240 offset:16896
rt v[198:199],v240 offset:17920
mm a[224:227],v[160:163],v[48:51],a[224:227]
mm a[224:227],v[164:167],v[52:55],a[224:227]
mm a[144:147],v[164:167],v[60:63],a[144:147]
mm a[144:147],v[160:163],v[56:59],a[144:147]
rd v[160:163],v236 offset:24576
rd v[164:167],v237 offset:24576
mm a[148:151],v[168:171],v[56:59],a[148:151]
mm a[148:151],v[172:175],v[60:63],a[148:151]
mm a[228:231],v[172:175],v[52:55],a[228:231]
mm a[228:231],v[168:171],v[48:51],a[228:231]
rt v[200:201],v241 offset:0
rt v[202:203],v241 offset:1024
mm a[140:143],v[168:171],v[40:43],a[140:143]
mm a[140:143],v[172:175],v[44:47],a[140:143]
mm a[124:127],v[172:175],v[36:39],a[124:127]
mm a[124:127],v[168:171],v[32:35],a[124:127]
rd v[168:171],v236 offset:26624
rd v[172:175],v237 offset:26624
mm a[216:219],v[176:179],v[32:35],a[216:219]
mm a[216:219],v[180:183],v[36:39],a[216:219]
mm a[168:171],v[180:183],v[44:47],a[168:171]
mm a[168:171],v[176:179],v[40:43],a[168:171]
rt v[204:205],v241 offset:16896
rt v[206:207],v241 offset:17920
mm a[192:195],v[176:179],v[48:51],a[192:195]
mm a[192:195],v[180:183],v[52:55],a[192:195]
mm a[176:179],v[180:183],v[60:63],a[176:179]
mm a[176:179],v[176:179],v[56:59],a[176:179]
rd v[176:179],v236 offset:16384
rd v[180:183],v237 offset:16384
mm a[180:183],v[184:187],v[56:59],a[180:183]
mm a[180:183],v[188:191],v[60:63],a[180:183]
mm a[196:199],v[188:191],v[52:55],a[196:199]
mm a[196:199],v[184:187],v[48:51],a[196:199]
rt v[208:209],v242 offset:0
rt v[210:211],v242 offset:1024
mm a[172:175],v[184:187],v[40:43],a[172:175]
s_add_u32 m0, s83, 33792
mm a[172:175],v[188:191],v[44:47],a[172:175]
ld v232,s[44:47],s80 offen lds
mm a[220:223],v[188:191],v[36:39],a[220:223]
mm a[220:223],v[184:187],v[32:35],a[220:223]
rd v[184:187],v236 offset:18432
rd v[188:191],v237 offset:18432
mm a[72:75],v[0:3],v[32:35],a[72:75]
s_add_u32 m0, s83, 34816
mm a[72:75],v[4:7],v[36:39],a[72:75]
ld v233,s[44:47],s80 offen lds
mm a[8:11],v[4:7],v[44:47],a[8:11]
mm a[8:11],v[0:3],v[40:43],a[8:11]
rt v[212:213],v242 offset:16896
rt v[214:215],v242 offset:17920
mm a[80:83],v[0:3],v[48:51],a[80:83]
s_add_u32 m0, s83, 35968
mm a[80:83],v[4:7],v[52:55],a[80:83]
ld v234,s[44:47],s80 offen lds
mm a[16:19],v[4:7],v[60:63],a[16:19]
mm a[16:19],v[0:3],v[56:59],a[16:19]
rd v[0:3],v236 offset:8192
rd v[4:7],v237 offset:8192
mm a[20:23],v[8:11],v[56:59],a[20:23]
s_add_u32 m0, s83, 36992
mm a[20:23],v[12:15],v[60:63],a[20:23]
ld v235,s[44:47],s80 offen lds
mm a[84:87],v[12:15],v[52:55],a[84:87]
s_mov_b32 s80, 1835008
mm a[84:87],v[8:11],v[48:51],a[84:87]
rt v[216:217],v243 offset:0
rt v[218:219],v243 offset:1024
mm a[12:15],v[8:11],v[40:43],a[12:15]
s_add_u32 m0, s83, 50688
mm a[12:15],v[12:15],v[44:47],a[12:15]
ld v232,s[44:47],s80 offen lds
mm a[76:79],v[12:15],v[36:39],a[76:79]
mm a[76:79],v[8:11],v[32:35],a[76:79]
rd v[8:11],v236 offset:10240
rd v[12:15],v237 offset:10240
mm a[248:251],v[16:19],v[32:35],a[248:251]
s_add_u32 m0, s83, 51712
mm a[248:251],v[20:23],v[36:39],a[248:251]
ld v233,s[44:47],s80 offen lds
mm a[40:43],v[20:23],v[44:47],a[40:43]
mm a[40:43],v[16:19],v[40:43],a[40:43]
rt v[220:221],v243 offset:16896
rt v[222:223],v243 offset:17920
mm a[48:51],v[16:19],v[48:51],a[48:51]
s_add_u32 m0, s83, 52864
mm a[48:51],v[20:23],v[52:55],a[48:51]
ld v234,s[44:47],s80 offen lds
mm a[56:59],v[20:23],v[60:63],a[56:59]
mm a[56:59],v[16:19],v[56:59],a[56:59]
rd v[16:19],v236 offset:0
rd v[20:23],v237 offset:0
mm a[60:63],v[24:27],v[56:59],a[60:63]
s_add_u32 m0, s83, 53888
mm a[60:63],v[28:31],v[60:63],a[60:63]
ld v235,s[44:47],s80 offen lds
mm a[52:55],v[28:31],v[52:55],a[52:55]
s_mov_b64 s[44:45], s[68:69]
s_mov_b32 s46, s70
mm a[52:55],v[24:27],v[48:51],a[52:55]
mm a[44:47],v[24:27],v[40:43],a[44:47]
mm a[44:47],v[28:31],v[44:47],a[44:47]
mm a[252:255],v[28:31],v[36:39],a[252:255]
wt lgkmcnt(2)
s_lshl_b32 s80, s77, 18
mm a[252:255],v[24:27],v[32:35],a[252:255]
rd v[24:27],v236 offset:2048
rd v[28:31],v237 offset:2048
mm a[96:99],v[160:163],v[192:195],a[96:99]
mm a[96:99],v[164:167],v[196:199],a[96:99]
mm a[104:107],v[164:167],v[204:207],a[104:107]
mm a[104:107],v[160:163],v[200:203],a[104:107]
rt v[32:33],v240 offset:128
rt v[34:35],v240 offset:1152
mm a[112:115],v[160:163],v[208:211],a[112:115]
mm a[112:115],v[164:167],v[212:215],a[112:115]
mm a[128:131],v[164:167],v[220:223],a[128:131]
wt lgkmcnt(2)
s_barrier
mm a[128:131],v[160:163],v[216:219],a[128:131]
mm a[132:135],v[168:171],v[216:219],a[132:135]
mm a[132:135],v[172:175],v[220:223],a[132:135]
mm a[116:119],v[172:175],v[212:215],a[116:119]
mm a[116:119],v[168:171],v[208:211],a[116:119]
rt v[36:37],v240 offset:17024
rt v[38:39],v240 offset:18048
mm a[108:111],v[168:171],v[200:203],a[108:111]
mm a[108:111],v[172:175],v[204:207],a[108:111]
mm a[100:103],v[172:175],v[196:199],a[100:103]
mm a[100:103],v[168:171],v[192:195],a[100:103]
mm a[232:235],v[176:179],v[192:195],a[232:235]
mm a[232:235],v[180:183],v[196:199],a[232:235]
mm a[152:155],v[180:183],v[204:207],a[152:155]
mm a[152:155],v[176:179],v[200:203],a[152:155]
rt v[40:41],v241 offset:128
rt v[42:43],v241 offset:1152
mm a[208:211],v[176:179],v[208:211],a[208:211]
mm a[208:211],v[180:183],v[212:215],a[208:211]
mm a[160:163],v[180:183],v[220:223],a[160:163]
mm a[160:163],v[176:179],v[216:219],a[160:163]
mm a[164:167],v[184:187],v[216:219],a[164:167]
mm a[164:167],v[188:191],v[220:223],a[164:167]
mm a[212:215],v[188:191],v[212:215],a[212:215]
mm a[212:215],v[184:187],v[208:211],a[212:215]
rt v[44:45],v241 offset:17024
rt v[46:47],v241 offset:18048
mm a[156:159],v[184:187],v[200:203],a[156:159]
mm a[156:159],v[188:191],v[204:207],a[156:159]
mm a[236:239],v[188:191],v[196:199],a[236:239]
mm a[236:239],v[184:187],v[192:195],a[236:239]
mm a[200:203],v[0:3],v[192:195],a[200:203]
mm a[200:203],v[4:7],v[196:199],a[200:203]
mm a[184:187],v[4:7],v[204:207],a[184:187]
mm a[184:187],v[0:3],v[200:203],a[184:187]
rt v[48:49],v242 offset:128
rt v[50:51],v242 offset:1152
mm a[64:67],v[0:3],v[208:211],a[64:67]
mm a[64:67],v[4:7],v[212:215],a[64:67]
mm a[0:3],v[4:7],v[220:223],a[0:3]
mm a[0:3],v[0:3],v[216:219],a[0:3]
mm a[4:7],v[8:11],v[216:219],a[4:7]
mm a[4:7],v[12:15],v[220:223],a[4:7]
buffer_load_dword v254, v135, s[68:71], s96 offen
mm a[68:71],v[12:15],v[212:215],a[68:71]
s_add_u32 s96, s96, 1048576
mm a[68:71],v[8:11],v[208:211],a[68:71]
rt v[52:53],v242 offset:17024
rt v[54:55],v242 offset:18048
mm a[188:191],v[8:11],v[200:203],a[188:191]
mm a[188:191],v[12:15],v[204:207],a[188:191]
buffer_load_dword v254, v135, s[68:71], s96 offen
mm a[204:207],v[12:15],v[196:199],a[204:207]
s_add_u32 s96, s96, 1048576
mm a[204:207],v[8:11],v[192:195],a[204:207]
rt v[56:57],v243 offset:128
rt v[58:59],v243 offset:1152
mm a[88:91],v[16:19],v[192:195],a[88:91]
mm a[88:91],v[20:23],v[196:199],a[88:91]
mm a[24:27],v[20:23],v[204:207],a[24:27]
mm a[24:27],v[16:19],v[200:203],a[24:27]
rt v[60:61],v243 offset:17024
rt v[62:63],v243 offset:18048
mm a[240:243],v[16:19],v[208:211],a[240:243]
mm a[240:243],v[20:23],v[212:215],a[240:243]
mm a[32:35],v[20:23],v[220:223],a[32:35]
mm a[32:35],v[16:19],v[216:219],a[32:35]
mm a[36:39],v[24:27],v[216:219],a[36:39]
mm a[36:39],v[28:31],v[220:223],a[36:39]
ld v[72:75],v255,s[64:67],s89 offen offset:1024 sc1
mm a[244:247],v[28:31],v[212:215],a[244:247]
mm a[244:247],v[24:27],v[208:211],a[244:247]
mm a[28:31],v[24:27],v[200:203],a[28:31]
mm a[28:31],v[28:31],v[204:207],a[28:31]
mm a[92:95],v[28:31],v[196:199],a[92:95]
wt vmcnt(3) lgkmcnt(0)
s_barrier
mm a[92:95],v[24:27],v[192:195],a[92:95]
mm a[120:123],v[160:163],v[32:35],a[120:123]
mm a[120:123],v[164:167],v[36:39],a[120:123]
mm a[136:139],v[164:167],v[44:47],a[136:139]
rt v[192:193],v240 offset:33792
rt v[194:195],v240 offset:34816
mm a[136:139],v[160:163],v[40:43],a[136:139]
rt v[196:197],v240 offset:50688
rt v[198:199],v240 offset:51712
mm a[224:227],v[160:163],v[48:51],a[224:227]
mm a[224:227],v[164:167],v[52:55],a[224:227]
mm a[144:147],v[164:167],v[60:63],a[144:147]
mm a[144:147],v[160:163],v[56:59],a[144:147]
rd v[160:163],v236 offset:57344
rd v[164:167],v237 offset:57344
mm a[148:151],v[168:171],v[56:59],a[148:151]
mm a[148:151],v[172:175],v[60:63],a[148:151]
mm a[228:231],v[172:175],v[52:55],a[228:231]
mm a[228:231],v[168:171],v[48:51],a[228:231]
rt v[200:201],v241 offset:33792
rt v[202:203],v241 offset:34816
mm a[140:143],v[168:171],v[40:43],a[140:143]
mm a[140:143],v[172:175],v[44:47],a[140:143]
mm a[124:127],v[172:175],v[36:39],a[124:127]
mm a[124:127],v[168:171],v[32:35],a[124:127]
rd v[168:171],v236 offset:59392
rd v[172:175],v237 offset:59392
mm a[216:219],v[176:179],v[32:35],a[216:219]
mm a[216:219],v[180:183],v[36:39],a[216:219]
mm a[168:171],v[180:183],v[44:47],a[168:171]
mm a[168:171],v[176:179],v[40:43],a[168:171]
rt v[204:205],v241 offset:50688
rt v[206:207],v241 offset:51712
mm a[192:195],v[176:179],v[48:51],a[192:195]
mm a[192:195],v[180:183],v[52:55],a[192:195]
mm a[176:179],v[180:183],v[60:63],a[176:179]
mm a[176:179],v[176:179],v[56:59],a[176:179]
rd v[176:179],v236 offset:49152
rd v[180:183],v237 offset:49152
mm a[180:183],v[184:187],v[56:59],a[180:183]
mm a[180:183],v[188:191],v[60:63],a[180:183]
mm a[196:199],v[188:191],v[52:55],a[196:199]
mm a[196:199],v[184:187],v[48:51],a[196:199]
rt v[208:209],v242 offset:33792
rt v[210:211],v242 offset:34816
mm a[172:175],v[184:187],v[40:43],a[172:175]
s_add_u32 m0, s83, 0
mm a[172:175],v[188:191],v[44:47],a[172:175]
ld v232,s[44:47],s80 offen lds
mm a[220:223],v[188:191],v[36:39],a[220:223]
mm a[220:223],v[184:187],v[32:35],a[220:223]
rd v[184:187],v236 offset:51200
rd v[188:191],v237 offset:51200
mm a[72:75],v[0:3],v[32:35],a[72:75]
s_add_u32 m0, s83, 1024
mm a[72:75],v[4:7],v[36:39],a[72:75]
ld v233,s[44:47],s80 offen lds
mm a[8:11],v[4:7],v[44:47],a[8:11]
mm a[8:11],v[0:3],v[40:43],a[8:11]
rt v[212:213],v242 offset:50688
rt v[214:215],v242 offset:51712
mm a[80:83],v[0:3],v[48:51],a[80:83]
s_add_u32 m0, s83, 2176
mm a[80:83],v[4:7],v[52:55],a[80:83]
ld v234,s[44:47],s80 offen lds
mm a[16:19],v[4:7],v[60:63],a[16:19]
mm a[16:19],v[0:3],v[56:59],a[16:19]
rd v[0:3],v236 offset:40960
rd v[4:7],v237 offset:40960
mm a[20:23],v[8:11],v[56:59],a[20:23]
s_add_u32 m0, s83, 3200
mm a[20:23],v[12:15],v[60:63],a[20:23]
ld v235,s[44:47],s80 offen lds
mm a[84:87],v[12:15],v[52:55],a[84:87]
s_add_u32 s80, s80, 524288
mm a[84:87],v[8:11],v[48:51],a[84:87]
rt v[216:217],v243 offset:33792
rt v[218:219],v243 offset:34816
mm a[12:15],v[8:11],v[40:43],a[12:15]
s_add_u32 m0, s83, 16896
mm a[12:15],v[12:15],v[44:47],a[12:15]
ld v232,s[44:47],s80 offen lds
mm a[76:79],v[12:15],v[36:39],a[76:79]
mm a[76:79],v[8:11],v[32:35],a[76:79]
rd v[8:11],v236 offset:43008
rd v[12:15],v237 offset:43008
mm a[248:251],v[16:19],v[32:35],a[248:251]
s_add_u32 m0, s83, 17920
mm a[248:251],v[20:23],v[36:39],a[248:251]
ld v233,s[44:47],s80 offen lds
mm a[40:43],v[20:23],v[44:47],a[40:43]
mm a[40:43],v[16:19],v[40:43],a[40:43]
rt v[220:221],v243 offset:50688
rt v[222:223],v243 offset:51712
mm a[48:51],v[16:19],v[48:51],a[48:51]
s_add_u32 m0, s83, 19072
mm a[48:51],v[20:23],v[52:55],a[48:51]
ld v234,s[44:47],s80 offen lds
mm a[56:59],v[20:23],v[60:63],a[56:59]
mm a[56:59],v[16:19],v[56:59],a[56:59]
rd v[16:19],v236 offset:32768
rd v[20:23],v237 offset:32768
mm a[60:63],v[24:27],v[56:59],a[60:63]
s_add_u32 m0, s83, 20096
mm a[60:63],v[28:31],v[60:63],a[60:63]
ld v235,s[44:47],s80 offen lds
mm a[52:55],v[28:31],v[52:55],a[52:55]
s_add_u32 s80, s80, 524288
mm a[52:55],v[24:27],v[48:51],a[52:55]
mm a[44:47],v[24:27],v[40:43],a[44:47]
mm a[44:47],v[28:31],v[44:47],a[44:47]
mm a[252:255],v[28:31],v[36:39],a[252:255]
wt lgkmcnt(2)
mm a[252:255],v[24:27],v[32:35],a[252:255]
rd v[24:27],v236 offset:34816
rd v[28:31],v237 offset:34816
mm a[96:99],v[160:163],v[192:195],a[96:99]
mm a[96:99],v[164:167],v[196:199],a[96:99]
mm a[104:107],v[164:167],v[204:207],a[104:107]
mm a[104:107],v[160:163],v[200:203],a[104:107]
rt v[32:33],v240 offset:33920
rt v[34:35],v240 offset:34944
mm a[112:115],v[160:163],v[208:211],a[112:115]
mm a[112:115],v[164:167],v[212:215],a[112:115]
mm a[128:131],v[164:167],v[220:223],a[128:131]
wt lgkmcnt(2)
s_barrier
mm a[128:131],v[160:163],v[216:219],a[128:131]
mm a[132:135],v[168:171],v[216:219],a[132:135]
wt vmcnt(8)
v_cmp_eq_u64 vcc, v[72:73], v[252:253]
mm a[132:135],v[172:175],v[220:223],a[132:135]
v_cmp_eq_u64 s[96:97], v[74:75], v[252:253]
mm a[116:119],v[172:175],v[212:215],a[116:119]
s_and_b64 vcc, vcc, s[96:97]
s_cmp_eq_u64 vcc, -1
s_cbranch_scc0 .La5_LV_XSPIN
.La5_LV_XOK:
mm a[116:119],v[168:171],v[208:211],a[116:119]
rt v[36:37],v240 offset:50816
rt v[38:39],v240 offset:51840
mm a[108:111],v[168:171],v[200:203],a[108:111]
s_mov_b32 s89, 49152
mm a[108:111],v[172:175],v[204:207],a[108:111]
mm a[100:103],v[172:175],v[196:199],a[100:103]
mm a[100:103],v[168:171],v[192:195],a[100:103]
mm a[232:235],v[176:179],v[192:195],a[232:235]
mm a[232:235],v[180:183],v[196:199],a[232:235]
ld v[128:131],v244,s[52:55],s91 offen offset:0 sc1
mm a[152:155],v[180:183],v[204:207],a[152:155]
ar v224,a96
ar v225,a97
mm a[152:155],v[176:179],v[200:203],a[152:155]
rt v[40:41],v241 offset:33920
rt v[42:43],v241 offset:34944
mm a[208:211],v[176:179],v[208:211],a[208:211]
ar v226,a98
ar v227,a99
mm a[208:211],v[180:183],v[212:215],a[208:211]
ld v[132:135],v245,s[52:55],s91 offen offset:0 sc1
mm a[160:163],v[180:183],v[220:223],a[160:163]
ar v228,a100
ar v229,a101
mm a[160:163],v[176:179],v[216:219],a[160:163]
ar v230,a102
ar v231,a103
mm a[164:167],v[184:187],v[216:219],a[164:167]
cv v248,v224,v225
cv v249,v226,v227
mm a[164:167],v[188:191],v[220:223],a[164:167]
ld v[136:139],v246,s[52:55],s91 offen offset:0 sc1
mm a[212:215],v[188:191],v[212:215],a[212:215]
cv v250,v228,v229
cv v251,v230,v231
mm a[212:215],v[184:187],v[208:211],a[212:215]
rt v[44:45],v241 offset:50816
rt v[46:47],v241 offset:51840
mm a[156:159],v[184:187],v[200:203],a[156:159]
st v[248:251],v255,s[48:51],s89 offen sc1
mm a[156:159],v[188:191],v[204:207],a[156:159]
ld v[140:143],v247,s[52:55],s91 offen offset:0 sc1
mm a[236:239],v[188:191],v[196:199],a[236:239]
s_mov_b32 s89, 50176
ds_write_b128 v238, v[248:251] offset:0
mm a[236:239],v[184:187],v[192:195],a[236:239]
ar v224,a104
ar v225,a105
mm a[200:203],v[0:3],v[192:195],a[200:203]
ar v226,a106
ar v227,a107
mm a[200:203],v[4:7],v[196:199],a[200:203]
ld v[144:147],v244,s[52:55],s91 offen offset:1024 sc1
mm a[184:187],v[4:7],v[204:207],a[184:187]
ar v228,a108
ar v229,a109
mm a[184:187],v[0:3],v[200:203],a[184:187]
rt v[48:49],v242 offset:33920
rt v[50:51],v242 offset:34944
mm a[64:67],v[0:3],v[208:211],a[64:67]
ar v230,a110
ar v231,a111
mm a[64:67],v[4:7],v[212:215],a[64:67]
ld v[148:151],v245,s[52:55],s91 offen offset:1024 sc1
mm a[0:3],v[4:7],v[220:223],a[0:3]
cv v248,v224,v225
cv v249,v226,v227
mm a[0:3],v[0:3],v[216:219],a[0:3]
cv v250,v228,v229
cv v251,v230,v231
mm a[4:7],v[8:11],v[216:219],a[4:7]
st v[248:251],v255,s[48:51],s89 offen sc1
mm a[4:7],v[12:15],v[220:223],a[4:7]
ld v[152:155],v246,s[52:55],s91 offen offset:1024 sc1
mm a[68:71],v[12:15],v[212:215],a[68:71]
s_mov_b32 s89, 51200
ds_write_b128 v238, v[248:251] offset:1024
mm a[68:71],v[8:11],v[208:211],a[68:71]
rt v[52:53],v242 offset:50816
rt v[54:55],v242 offset:51840
mm a[188:191],v[8:11],v[200:203],a[188:191]
ar v224,a112
ar v225,a113
mm a[188:191],v[12:15],v[204:207],a[188:191]
ld v[156:159],v247,s[52:55],s91 offen offset:1024 sc1
mm a[204:207],v[12:15],v[196:199],a[204:207]
ar v226,a114
ar v227,a115
mm a[204:207],v[8:11],v[192:195],a[204:207]
rt v[56:57],v243 offset:33920
rt v[58:59],v243 offset:34944
mm a[88:91],v[16:19],v[192:195],a[88:91]
ar v228,a116
ar v229,a117
mm a[88:91],v[20:23],v[196:199],a[88:91]
ar v230,a118
ar v231,a119
mm a[24:27],v[20:23],v[204:207],a[24:27]
cv v248,v224,v225
cv v249,v226,v227
mm a[24:27],v[16:19],v[200:203],a[24:27]
rt v[60:61],v243 offset:50816
rt v[62:63],v243 offset:51840
mm a[240:243],v[16:19],v[208:211],a[240:243]
v_subrev_u32 v240, 8448, v240
v_subrev_u32 v241, 8448, v241
mm a[240:243],v[20:23],v[212:215],a[240:243]
v_subrev_u32 v242, 8448, v242
v_subrev_u32 v243, 8448, v243
mm a[32:35],v[20:23],v[220:223],a[32:35]
cv v250,v228,v229
cv v251,v230,v231
mm a[32:35],v[16:19],v[216:219],a[32:35]
st v[248:251],v255,s[48:51],s89 offen sc1
.La5_LW1_8:
s_cbranch_execz .La5_LW1_12
mm a[36:39],v[24:27],v[216:219],a[36:39]
s_mov_b32 s89, 52224
ds_write_b128 v238, v[248:251] offset:2048
mm a[36:39],v[28:31],v[220:223],a[36:39]
mm a[244:247],v[28:31],v[212:215],a[244:247]
mm a[244:247],v[24:27],v[208:211],a[244:247]
mm a[28:31],v[24:27],v[200:203],a[28:31]
mm a[28:31],v[28:31],v[204:207],a[28:31]
mm a[92:95],v[28:31],v[196:199],a[92:95]
wt vmcnt(11) lgkmcnt(1)
s_barrier
mm a[92:95],v[24:27],v[192:195],a[92:95]
mm a[120:123],v[160:163],v[32:35],a[120:123]
s_add_u32 m0, s83, 33792
mm a[120:123],v[164:167],v[36:39],a[120:123]
ld v232,s[44:47],s80 offen lds
mm a[136:139],v[164:167],v[44:47],a[136:139]
mm a[136:139],v[160:163],v[40:43],a[136:139]
rt v[192:193],v240 offset:0
rt v[194:195],v240 offset:1024
mm a[224:227],v[160:163],v[48:51],a[224:227]
s_add_u32 m0, s83, 34816
mm a[224:227],v[164:167],v[52:55],a[224:227]
ld v233,s[44:47],s80 offen lds
mm a[144:147],v[164:167],v[60:63],a[144:147]
mm a[144:147],v[160:163],v[56:59],a[144:147]
rt v[196:197],v240 offset:8448
rt v[198:199],v240 offset:9472
mm a[148:151],v[168:171],v[56:59],a[148:151]
s_add_u32 m0, s83, 35968
mm a[148:151],v[172:175],v[60:63],a[148:151]
ld v234,s[44:47],s80 offen lds
mm a[228:231],v[172:175],v[52:55],a[228:231]
mm a[228:231],v[168:171],v[48:51],a[228:231]
rt v[200:201],v241 offset:0
rt v[202:203],v241 offset:1024
mm a[140:143],v[168:171],v[40:43],a[140:143]
s_add_u32 m0, s83, 36992
mm a[140:143],v[172:175],v[44:47],a[140:143]
ld v235,s[44:47],s80 offen lds
mm a[124:127],v[172:175],v[36:39],a[124:127]
s_add_u32 s80, s80, 524288
mm a[124:127],v[168:171],v[32:35],a[124:127]
rt v[204:205],v241 offset:8448
rt v[206:207],v241 offset:9472
mm a[216:219],v[176:179],v[32:35],a[216:219]
s_add_u32 m0, s83, 50688
mm a[216:219],v[180:183],v[36:39],a[216:219]
ld v232,s[44:47],s80 offen lds
mm a[168:171],v[180:183],v[44:47],a[168:171]
mm a[168:171],v[176:179],v[40:43],a[168:171]
rt v[208:209],v242 offset:0
rt v[210:211],v242 offset:1024
mm a[192:195],v[176:179],v[48:51],a[192:195]
s_add_u32 m0, s83, 51712
mm a[192:195],v[180:183],v[52:55],a[192:195]
ld v233,s[44:47],s80 offen lds
mm a[176:179],v[180:183],v[60:63],a[176:179]
mm a[176:179],v[176:179],v[56:59],a[176:179]
rt v[212:213],v242 offset:8448
rt v[214:215],v242 offset:9472
mm a[180:183],v[184:187],v[56:59],a[180:183]
s_add_u32 m0, s83, 52864
mm a[180:183],v[188:191],v[60:63],a[180:183]
ld v234,s[44:47],s80 offen lds
mm a[196:199],v[188:191],v[52:55],a[196:199]
mm a[196:199],v[184:187],v[48:51],a[196:199]
rt v[216:217],v243 offset:0
rt v[218:219],v243 offset:1024
mm a[172:175],v[184:187],v[40:43],a[172:175]
s_add_u32 m0, s83, 53888
mm a[172:175],v[188:191],v[44:47],a[172:175]
ld v235,s[44:47],s80 offen lds
mm a[220:223],v[188:191],v[36:39],a[220:223]
s_add_u32 s80, s80, 524288
mm a[220:223],v[184:187],v[32:35],a[220:223]
rt v[220:221],v243 offset:8448
rt v[222:223],v243 offset:9472
mm a[72:75],v[0:3],v[32:35],a[72:75]
mm a[72:75],v[4:7],v[36:39],a[72:75]
ld v[160:163],v244,s[52:55],s91 offen offset:2048 sc1
mm a[8:11],v[4:7],v[44:47],a[8:11]
mm a[8:11],v[0:3],v[40:43],a[8:11]
mm a[80:83],v[0:3],v[48:51],a[80:83]
mm a[80:83],v[4:7],v[52:55],a[80:83]
ld v[164:167],v245,s[52:55],s91 offen offset:2048 sc1
mm a[16:19],v[4:7],v[60:63],a[16:19]
mm a[16:19],v[0:3],v[56:59],a[16:19]
mm a[20:23],v[8:11],v[56:59],a[20:23]
mm a[20:23],v[12:15],v[60:63],a[20:23]
ld v[168:171],v246,s[52:55],s91 offen offset:2048 sc1
mm a[84:87],v[12:15],v[52:55],a[84:87]
mm a[84:87],v[8:11],v[48:51],a[84:87]
mm a[12:15],v[8:11],v[40:43],a[12:15]
mm a[12:15],v[12:15],v[44:47],a[12:15]
ld v[172:175],v247,s[52:55],s91 offen offset:2048 sc1
mm a[76:79],v[12:15],v[36:39],a[76:79]
mm a[76:79],v[8:11],v[32:35],a[76:79]
mm a[248:251],v[16:19],v[32:35],a[248:251]
mm a[248:251],v[20:23],v[36:39],a[248:251]
mm a[40:43],v[20:23],v[44:47],a[40:43]
mm a[40:43],v[16:19],v[40:43],a[40:43]
mm a[48:51],v[16:19],v[48:51],a[48:51]
mm a[48:51],v[20:23],v[52:55],a[48:51]
mm a[56:59],v[20:23],v[60:63],a[56:59]
mm a[56:59],v[16:19],v[56:59],a[56:59]
mm a[60:63],v[24:27],v[56:59],a[60:63]
mm a[60:63],v[28:31],v[60:63],a[60:63]
mm a[52:55],v[28:31],v[52:55],a[52:55]
mm a[52:55],v[24:27],v[48:51],a[52:55]
mm a[44:47],v[24:27],v[40:43],a[44:47]
mm a[44:47],v[28:31],v[44:47],a[44:47]
mm a[252:255],v[28:31],v[36:39],a[252:255]
mm a[252:255],v[24:27],v[32:35],a[252:255]
; ---- Q tile: 256 rows x 128 columns, K = 1024, while K / V leave the accumulators (memory + .La5_LDS ring); behind it the first step on the diagonal
.La5_LQ_START:
.La5_LW1_9:
s_cbranch_execz .La5_LW1_13
wt vmcnt(13) lgkmcnt(4)
mm v[0:3],v[192:195],v[128:131],0
ar v224,a128
ar v225,a129
s_mov_b32 s85, s95
mm v[0:3],v[196:199],v[144:147],v[0:3]
ld v[176:179],v244,s[52:55],s91 offen offset:3072 sc1
mm v[32:35],v[196:199],v[148:151],0
ar v226,a130
ar v227,a131
mm v[32:35],v[192:195],v[132:135],v[32:35]
ar v228,a132
ar v229,a133
mm v[64:67],v[192:195],v[136:139],0
ar v230,a134
ar v231,a135
mm v[64:67],v[196:199],v[152:155],v[64:67]
ld v[180:183],v245,s[52:55],s91 offen offset:3072 sc1
mm v[96:99],v[196:199],v[156:159],0
cv v248,v224,v225
cv v249,v226,v227
mm v[96:99],v[192:195],v[140:143],v[96:99]
rt v[196:197],v240 offset:128
rt v[198:199],v240 offset:1152
mm v[100:103],v[200:203],v[140:143],0
cv v250,v228,v229
cv v251,v230,v231
mm v[100:103],v[204:207],v[156:159],v[100:103]
ld v[184:187],v246,s[52:55],s91 offen offset:3072 sc1
mm v[68:71],v[204:207],v[152:155],0
aw a128,v248
aw a129,v249
mm v[68:71],v[200:203],v[136:139],v[68:71]
rt v[192:193],v240 offset:8576
rt v[194:195],v240 offset:9600
mm v[36:39],v[200:203],v[132:135],0
aw a130,v250
aw a131,v251
mm v[36:39],v[204:207],v[148:151],v[36:39]
ld v[188:191],v247,s[52:55],s91 offen offset:3072 sc1
mm v[4:7],v[204:207],v[144:147],0
wt lgkmcnt(4)
s_add_u32 s91, s91, 4096
mm v[4:7],v[200:203],v[128:131],v[4:7]
rt v[204:205],v241 offset:128
rt v[206:207],v241 offset:1152
mm v[8:11],v[208:211],v[128:131],0
s_and_b32 s97, s2, 7
s_mul_i32 s95, s97, 7
mm v[8:11],v[212:215],v[144:147],v[8:11]
ld a[96:99],v244,s[52:55],s91 offen offset:0 sc1
mm v[40:43],v[212:215],v[148:151],0
s_lshr_b32 s96, s30, 7
s_add_u32 s95, s95, s96
mm v[40:43],v[208:211],v[132:135],v[40:43]
rt v[200:201],v241 offset:8576
rt v[202:203],v241 offset:9600
mm v[72:75],v[208:211],v[136:139],0
s_sub_u32 s95, 3, s95
s_and_b32 s95, s95, 7
mm v[72:75],v[212:215],v[152:155],v[72:75]
ld a[100:103],v245,s[52:55],s91 offen offset:0 sc1
mm v[104:107],v[212:215],v[156:159],0
s_min_u32 s95, s95, 6
s_lshl_b32 s95, s95, 1
mm v[104:107],v[208:211],v[140:143],v[104:107]
rt v[212:213],v242 offset:128
rt v[214:215],v242 offset:1152
mm v[108:111],v[216:219],v[140:143],0
s_lshr_b32 s92, s2, 6
s_mul_i32 s93, s92, 3
mm v[108:111],v[220:223],v[156:159],v[108:111]
ld a[104:107],v246,s[52:55],s91 offen offset:0 sc1
mm v[76:79],v[220:223],v[152:155],0
s_add_u32 s93, s93, s74
mm v[76:79],v[216:219],v[136:139],v[76:79]
rt v[208:209],v242 offset:8576
rt v[210:211],v242 offset:9600
mm v[44:47],v[216:219],v[132:135],0
ds_write_b128 v238, v[248:251] offset:3072
mm v[44:47],v[220:223],v[148:151],v[44:47]
st v[248:251],v255,s[48:51],s89 offen offset:0 sc1
mm v[12:15],v[220:223],v[144:147],0
wt lgkmcnt(5)
mm v[12:15],v[216:219],v[128:131],v[12:15]
rt v[220:221],v243 offset:128
rt v[222:223],v243 offset:1152
mm v[16:19],v[196:199],v[128:131],0
ar v224,a120
ar v225,a121
mm v[16:19],v[192:195],v[144:147],v[16:19]
ld a[108:111],v247,s[52:55],s91 offen offset:0 sc1
mm v[48:51],v[192:195],v[148:151],0
ar v226,a122
ar v227,a123
mm v[48:51],v[196:199],v[132:135],v[48:51]
rt v[216:217],v243 offset:8576
rt v[218:219],v243 offset:9600
mm v[80:83],v[196:199],v[136:139],0
ar v228,a124
ar v229,a125
mm v[80:83],v[192:195],v[152:155],v[80:83]
ld a[112:115],v244,s[52:55],s91 offen offset:1024 sc1
mm v[112:115],v[192:195],v[156:159],0
ar v230,a126
ar v231,a127
mm v[112:115],v[196:199],v[140:143],v[112:115]
cv v248,v224,v225
cv v249,v226,v227
mm v[116:119],v[204:207],v[140:143],0
wt lgkmcnt(0)
s_barrier
mm v[116:119],v[200:203],v[156:159],v[116:119]
ld a[116:119],v245,s[52:55],s91 offen offset:1024 sc1
mm v[84:87],v[200:203],v[152:155],0
rt v[192:193],v240 offset:16896
rt v[194:195],v240 offset:17920
mm v[84:87],v[204:207],v[136:139],v[84:87]
rt v[196:197],v240 offset:25344
rt v[198:199],v240 offset:26368
mm v[52:55],v[204:207],v[132:135],0
s_add_u32 m0, s83, 0
cv v250,v228,v229
mm v[52:55],v[200:203],v[148:151],v[52:55]
ld v232,s[44:47],s80 offen lds
mm v[20:23],v[200:203],v[144:147],0
cv v251,v230,v231
aw a132,v248
mm v[20:23],v[204:207],v[128:131],v[20:23]
rt v[200:201],v241 offset:16896
rt v[202:203],v241 offset:17920
mm v[24:27],v[212:215],v[128:131],0
aw a133,v249
aw a134,v250
mm v[24:27],v[208:211],v[144:147],v[24:27]
aw a135,v251
mm v[56:59],v[208:211],v[148:151],0
s_cmp_ge_u32 s93, s95
s_cselect_b32 s96, 2, 0
mm v[56:59],v[212:215],v[132:135],v[56:59]
rt v[204:205],v241 offset:25344
rt v[206:207],v241 offset:26368
mm v[88:91],v[212:215],v[136:139],0
s_add_u32 m0, s83, 1024
s_add_u32 s93, s93, s96
mm v[88:91],v[208:211],v[152:155],v[88:91]
ld v233,s[44:47],s80 offen lds
mm v[120:123],v[208:211],v[156:159],0
s_add_u32 s95, s95, s92
mm v[120:123],v[212:215],v[140:143],v[120:123]
rt v[208:209],v242 offset:16896
rt v[210:211],v242 offset:17920
mm v[124:127],v[220:223],v[140:143],0
s_cmp_lt_u32 s92, 2
s_cselect_b32 s94, s75, 0
mm v[124:127],v[216:219],v[156:159],v[124:127]
ld v[140:143],v246,s[52:55],s91 offen offset:1024 sc1
mm v[92:95],v[216:219],v[152:155],0
s_cmp_eq_u32 s74, 3
s_cselect_b32 s92, s95, s93
s_cselect_b32 s94, s94, s75
mm v[92:95],v[220:223],v[136:139],v[92:95]
rt v[212:213],v242 offset:25344
rt v[214:215],v242 offset:26368
mm v[60:63],v[220:223],v[132:135],0
ds_write_b128 v238, v[248:251] offset:4096
mm v[60:63],v[216:219],v[148:151],v[60:63]
s_mul_i32 s93, s97, 14
s_add_u32 s92, s92, s93
mm v[28:31],v[216:219],v[144:147],0
wt vmcnt(10) lgkmcnt(5)
s_mul_i32 s96, s92, s94
mm v[28:31],v[220:223],v[128:131],v[28:31]
rt v[216:217],v243 offset:16896
rt v[218:219],v243 offset:17920
mm v[0:3],v[192:195],v[160:163],v[0:3]
ar v224,a136
ar v225,a137
mm v[0:3],v[196:199],v[176:179],v[0:3]
ld v[128:131],v247,s[52:55],s91 offen offset:1024 sc1
mm v[32:35],v[196:199],v[180:183],v[32:35]
ar v226,a138
ar v227,a139
mm v[32:35],v[192:195],v[164:167],v[32:35]
rt v[220:221],v243 offset:25344
rt v[222:223],v243 offset:26368
mm v[64:67],v[192:195],v[168:171],v[64:67]
ar v228,a140
ar v229,a141
mm v[64:67],v[196:199],v[184:187],v[64:67]
ld v[132:135],v244,s[52:55],s91 offen offset:2048 sc1
mm v[96:99],v[196:199],v[188:191],v[96:99]
ar v230,a142
ar v231,a143
mm v[96:99],v[192:195],v[172:175],v[96:99]
rt v[196:197],v240 offset:17024
rt v[198:199],v240 offset:18048
mm v[100:103],v[200:203],v[172:175],v[100:103]
s_add_u32 m0, s83, 2176
cv v248,v224,v225
mm v[100:103],v[204:207],v[188:191],v[100:103]
ld v234,s[44:47],s80 offen lds
mm v[68:71],v[204:207],v[184:187],v[68:71]
cv v249,v226,v227
cv v250,v228,v229
mm v[68:71],v[200:203],v[168:171],v[68:71]
rt v[192:193],v240 offset:25472
rt v[194:195],v240 offset:26496
mm v[36:39],v[200:203],v[164:167],v[36:39]
cv v251,v230,v231
aw a136,v248
mm v[36:39],v[204:207],v[180:183],v[36:39]
ld v[136:139],v245,s[52:55],s91 offen offset:2048 sc1
mm v[4:7],v[204:207],v[176:179],v[4:7]
wt lgkmcnt(4)
aw a137,v249
mm v[4:7],v[200:203],v[160:163],v[4:7]
rt v[204:205],v241 offset:17024
rt v[206:207],v241 offset:18048
mm v[8:11],v[208:211],v[160:163],v[8:11]
s_add_u32 m0, s83, 3200
aw a138,v250
mm v[8:11],v[212:215],v[176:179],v[8:11]
ld v235,s[44:47],s80 offen lds
mm v[40:43],v[212:215],v[180:183],v[40:43]
s_add_u32 s80, s80, 524288
aw a139,v251
mm v[40:43],v[208:211],v[164:167],v[40:43]
rt v[200:201],v241 offset:25472
rt v[202:203],v241 offset:26496
mm v[72:75],v[208:211],v[168:171],v[72:75]
s_mov_b32 s100, s96
s_and_b32 s93, s2, 7
mm v[72:75],v[212:215],v[184:187],v[72:75]
ld v[144:147],v246,s[52:55],s91 offen offset:2048 sc1
mm v[104:107],v[212:215],v[188:191],v[104:107]
s_lshr_b32 s95, s93, 1
s_xor_b32 s97, 0, s93
mm v[104:107],v[208:211],v[172:175],v[104:107]
rt v[212:213],v242 offset:17024
rt v[214:215],v242 offset:18048
mm v[108:111],v[216:219],v[172:175],v[108:111]
s_lshl_b32 s95, s95, 21
s_and_b32 s97, s97, 1
mm v[108:111],v[220:223],v[188:191],v[108:111]
ld v[148:151],v247,s[52:55],s91 offen offset:2048 sc1
mm v[76:79],v[220:223],v[184:187],v[76:79]
s_mov_b32 s18, 53248
s_lshl_b32 s97, s97, 10
mm v[76:79],v[216:219],v[168:171],v[76:79]
rt v[208:209],v242 offset:25472
rt v[210:211],v242 offset:26496
mm v[44:47],v[216:219],v[164:167],v[44:47]
ds_write_b128 v238, v[248:251] offset:5120
mm v[44:47],v[220:223],v[180:183],v[44:47]
st a[132:135],v255,s[48:51],s18 offen offset:0 sc1
mm v[12:15],v[220:223],v[176:179],v[12:15]
wt lgkmcnt(5)
s_add_u32 s95, s95, s97
mm v[12:15],v[216:219],v[160:163],v[12:15]
rt v[220:221],v243 offset:17024
rt v[222:223],v243 offset:18048
mm v[16:19],v[196:199],v[160:163],v[16:19]
ar v224,a224
ar v225,a225
mm v[16:19],v[192:195],v[176:179],v[16:19]
ld v[152:155],v244,s[52:55],s91 offen offset:3072 sc1
mm v[48:51],v[192:195],v[180:183],v[48:51]
ar v226,a226
ar v227,a227
mm v[48:51],v[196:199],v[164:167],v[48:51]
rt v[216:217],v243 offset:25472
rt v[218:219],v243 offset:26496
mm v[80:83],v[196:199],v[168:171],v[80:83]
ar v228,a228
ar v229,a229
mm v[80:83],v[192:195],v[184:187],v[80:83]
ld v[156:159],v245,s[52:55],s91 offen offset:3072 sc1
mm v[112:115],v[192:195],v[188:191],v[112:115]
ar v230,a230
ar v231,a231
mm v[112:115],v[196:199],v[172:175],v[112:115]
cv v248,v224,v225
cv v249,v226,v227
mm v[116:119],v[204:207],v[172:175],v[116:119]
wt lgkmcnt(0)
s_barrier
mm v[116:119],v[200:203],v[188:191],v[116:119]
ld a[120:123],v246,s[52:55],s91 offen offset:3072 sc1
mm v[84:87],v[200:203],v[184:187],v[84:87]
rt v[192:193],v240 offset:33792
rt v[194:195],v240 offset:34816
mm v[84:87],v[204:207],v[168:171],v[84:87]
rt v[196:197],v240 offset:42240
rt v[198:199],v240 offset:43264
mm v[52:55],v[204:207],v[164:167],v[52:55]
s_add_u32 m0, s83, 16896
cv v250,v228,v229
mm v[52:55],v[200:203],v[180:183],v[52:55]
ld v232,s[44:47],s80 offen lds
mm v[20:23],v[200:203],v[176:179],v[20:23]
cv v251,v230,v231
aw a140,v248
mm v[20:23],v[204:207],v[160:163],v[20:23]
rt v[200:201],v241 offset:33792
rt v[202:203],v241 offset:34816
mm v[24:27],v[212:215],v[160:163],v[24:27]
aw a141,v249
aw a142,v250
mm v[24:27],v[208:211],v[176:179],v[24:27]
ld a[124:127],v247,s[52:55],s91 offen offset:3072 sc1
mm v[56:59],v[208:211],v[180:183],v[56:59]
s_add_u32 s91, s91, 4096
aw a143,v251
mm v[56:59],v[212:215],v[164:167],v[56:59]
rt v[204:205],v241 offset:42240
rt v[206:207],v241 offset:43264
mm v[88:91],v[212:215],v[168:171],v[88:91]
s_add_u32 m0, s83, 17920
s_mul_i32 s97, s93, 896
mm v[88:91],v[208:211],v[184:187],v[88:91]
ld v233,s[44:47],s80 offen lds
mm v[120:123],v[208:211],v[188:191],v[120:123]
s_cmp_eq_u32 s3, 0
s_cselect_b32 s95, s95, s97
s_cselect_b64 s[36:37], s[28:29], s[30:31]
s_cselect_b32 s21, 2, 7
s_cselect_b32 s93, s93, 0
mm v[120:123],v[212:215],v[172:175],v[120:123]
rt v[208:209],v242 offset:33792
rt v[210:211],v242 offset:34816
mm v[124:127],v[220:223],v[172:175],v[124:127]
s_cmp_eq_u32 s3, 2
s_cselect_b64 s[36:37], s[32:33], s[36:37]
mm v[124:127],v[216:219],v[188:191],v[124:127]
s_lshl_b32 s97, s97, 10
s_mov_b32 s92, 704
mm v[92:95],v[216:219],v[184:187],v[92:95]
s_mov_b32 s90, 320
mm v[92:95],v[220:223],v[168:171],v[92:95]
rt v[212:213],v242 offset:42240
rt v[214:215],v242 offset:43264
mm v[60:63],v[220:223],v[164:167],v[60:63]
ds_write_b128 v238, v[248:251] offset:6144
mm v[60:63],v[216:219],v[180:183],v[60:63]
st a[136:139],v255,s[48:51],s18 offen offset:1024 sc1
mm v[28:31],v[216:219],v[176:179],v[28:31]
wt vmcnt(14) lgkmcnt(5)
mm v[28:31],v[220:223],v[160:163],v[28:31]
rt v[216:217],v243 offset:33792
rt v[218:219],v243 offset:34816
mm v[0:3],v[192:195],a[96:99],v[0:3]
ar v224,a144
ar v225,a145
mm v[0:3],v[196:199],a[112:115],v[0:3]
ld v[160:163],v244,s[52:55],s91 offen offset:0 sc1
mm v[32:35],v[196:199],a[116:119],v[32:35]
ar v226,a146
ar v227,a147
mm v[32:35],v[192:195],a[100:103],v[32:35]
rt v[220:221],v243 offset:42240
rt v[222:223],v243 offset:43264
mm v[64:67],v[192:195],a[104:107],v[64:67]
ar v228,a148
ar v229,a149
mm v[64:67],v[196:199],v[140:143],v[64:67]
ld v[164:167],v245,s[52:55],s91 offen offset:0 sc1
mm v[96:99],v[196:199],v[128:131],v[96:99]
ar v230,a150
ar v231,a151
mm v[96:99],v[192:195],a[108:111],v[96:99]
rt v[196:197],v240 offset:33920
rt v[198:199],v240 offset:34944
mm v[100:103],v[200:203],a[108:111],v[100:103]
s_add_u32 m0, s83, 19072
cv v248,v224,v225
mm v[100:103],v[204:207],v[128:131],v[100:103]
ld v234,s[44:47],s80 offen lds
mm v[68:71],v[204:207],v[140:143],v[68:71]
cv v249,v226,v227
cv v250,v228,v229
mm v[68:71],v[200:203],a[104:107],v[68:71]
rt v[192:193],v240 offset:42368
rt v[194:195],v240 offset:43392
mm v[36:39],v[200:203],a[100:103],v[36:39]
cv v251,v230,v231
aw a144,v248
mm v[36:39],v[204:207],a[116:119],v[36:39]
ld v[168:171],v246,s[52:55],s91 offen offset:0 sc1
mm v[4:7],v[204:207],a[112:115],v[4:7]
wt lgkmcnt(4)
aw a145,v249
mm v[4:7],v[200:203],a[96:99],v[4:7]
rt v[204:205],v241 offset:33920
rt v[206:207],v241 offset:34944
mm v[8:11],v[208:211],a[96:99],v[8:11]
s_add_u32 m0, s83, 20096
aw a146,v250
mm v[8:11],v[212:215],a[112:115],v[8:11]
ld v235,s[44:47],s80 offen lds
mm v[40:43],v[212:215],a[116:119],v[40:43]
s_add_u32 s80, s80, 524288
aw a147,v251
mm v[40:43],v[208:211],a[100:103],v[40:43]
rt v[200:201],v241 offset:42368
rt v[202:203],v241 offset:43392
mm v[72:75],v[208:211],a[104:107],v[72:75]
s_cmp_eq_u32 s3, 3
s_cselect_b32 s95, s97, s95
s_cselect_b64 s[36:37], s[34:35], s[36:37]
s_cselect_b32 s21, 1, s21
s_cselect_b32 s92, 576, s92
mm v[72:75],v[212:215],v[140:143],v[72:75]
ld v[172:175],v247,s[52:55],s91 offen offset:0 sc1
mm v[104:107],v[212:215],v[128:131],v[104:107]
s_lshl_b32 s21, s21, 10
s_and_b32 s7, s93, 1
mm v[104:107],v[208:211],a[108:111],v[104:107]
rt v[212:213],v242 offset:33920
rt v[214:215],v242 offset:34944
mm v[108:111],v[216:219],a[108:111],v[108:111]
s_cmp_eq_u32 s75, 1
s_cselect_b32 s97, s90, s92
s_cselect_b32 s92, s92, s90
mm v[108:111],v[220:223],v[128:131],v[108:111]
s_cmp_eq_u32 s75, s7
s_cselect_b32 s92, s92, 0
mm v[76:79],v[220:223],v[140:143],v[76:79]
s_mul_i32 s92, s92, s21
s_add_u32 s95, s95, s92
mm v[76:79],v[216:219],a[104:107],v[76:79]
.La5_LW1_10:
s_cbranch_execz .La5_LW1_14
rt v[208:209],v242 offset:42368
rt v[210:211],v242 offset:43392
mm v[44:47],v[216:219],a[100:103],v[44:47]
ds_write_b128 v238, v[248:251] offset:7168
mm v[44:47],v[220:223],a[116:119],v[44:47]
st a[140:143],v255,s[48:51],s18 offen offset:2048 sc1
mm v[12:15],v[220:223],a[112:115],v[12:15]
wt lgkmcnt(5)
s_mul_i32 s97, s97, s21
mm v[12:15],v[216:219],a[96:99],v[12:15]
rt v[220:221],v243 offset:33920
rt v[222:223],v243 offset:34944
mm v[16:19],v[196:199],a[96:99],v[16:19]
ar v224,a232
ar v225,a233
mm v[16:19],v[192:195],a[112:115],v[16:19]
ld v[176:179],v244,s[52:55],s91 offen offset:1024 sc1
mm v[48:51],v[192:195],a[116:119],v[48:51]
ar v226,a234
ar v227,a235
mm v[48:51],v[196:199],a[100:103],v[48:51]
rt v[216:217],v243 offset:42368
rt v[218:219],v243 offset:43392
mm v[80:83],v[196:199],a[104:107],v[80:83]
ar v228,a236
ar v229,a237
mm v[80:83],v[192:195],v[140:143],v[80:83]
ld v[180:183],v245,s[52:55],s91 offen offset:1024 sc1
mm v[112:115],v[192:195],v[128:131],v[112:115]
ar v230,a238
ar v231,a239
mm v[112:115],v[196:199],a[108:111],v[112:115]
cv v248,v224,v225
cv v249,v226,v227
mm v[116:119],v[204:207],a[108:111],v[116:119]
wt lgkmcnt(0)
s_barrier
mm v[116:119],v[200:203],v[128:131],v[116:119]
ld v[184:187],v246,s[52:55],s91 offen offset:1024 sc1
mm v[84:87],v[200:203],v[140:143],v[84:87]
rt v[192:193],v240 offset:50688
rt v[194:195],v240 offset:51712
mm v[84:87],v[204:207],a[104:107],v[84:87]
rt v[196:197],v240 offset:59136
rt v[198:199],v240 offset:60160
mm v[52:55],v[204:207],a[100:103],v[52:55]
s_add_u32 m0, s83, 33792
cv v250,v228,v229
mm v[52:55],v[200:203],a[116:119],v[52:55]
ld v232,s[44:47],s80 offen lds
mm v[20:23],v[200:203],a[112:115],v[20:23]
cv v251,v230,v231
aw a148,v248
mm v[20:23],v[204:207],a[96:99],v[20:23]
rt v[200:201],v241 offset:50688
rt v[202:203],v241 offset:51712
mm v[24:27],v[212:215],a[96:99],v[24:27]
aw a149,v249
aw a150,v250
mm v[24:27],v[208:211],a[112:115],v[24:27]
ld v[188:191],v247,s[52:55],s91 offen offset:1024 sc1
mm v[56:59],v[208:211],a[116:119],v[56:59]
aw a151,v251
s_mul_i32 s90, s97, s7
mm v[56:59],v[212:215],a[100:103],v[56:59]
rt v[204:205],v241 offset:59136
rt v[206:207],v241 offset:60160
mm v[88:91],v[212:215],a[104:107],v[88:91]
s_add_u32 m0, s83, 34816
s_sub_u32 s97, s97, s21
mm v[88:91],v[208:211],v[140:143],v[88:91]
ld v233,s[44:47],s80 offen lds
mm v[120:123],v[208:211],v[128:131],v[120:123]
s_add_u32 s97, s97, 896
mm v[120:123],v[212:215],a[108:111],v[120:123]
rt v[208:209],v242 offset:50688
rt v[210:211],v242 offset:51712
mm v[124:127],v[220:223],a[108:111],v[124:127]
s_add_u32 s36, s36, s95
s_addc_u32 s37, s37, 0
mm v[124:127],v[216:219],v[128:131],v[124:127]
s_and_b32 s37, s37, 0xffff
s_mov_b32 s38, s97
mm v[92:95],v[216:219],v[140:143],v[92:95]
s_mov_b32 s39, 0x20000
s_lshl_b32 s93, s96, 6
mm v[92:95],v[220:223],a[104:107],v[92:95]
rt v[212:213],v242 offset:59136
rt v[214:215],v242 offset:60160
mm v[60:63],v[220:223],a[100:103],v[60:63]
ds_write_b128 v238, v[248:251] offset:32768
mm v[60:63],v[216:219],a[116:119],v[60:63]
s_mul_i32 s95, s94, 7332928
mm v[28:31],v[216:219],a[112:115],v[28:31]
wt vmcnt(15) lgkmcnt(5)
mm v[28:31],v[220:223],a[96:99],v[28:31]
rt v[216:217],v243 offset:50688
rt v[218:219],v243 offset:51712
mm v[0:3],v[192:195],v[132:135],v[0:3]
ar v224,a152
ar v225,a153
mm v[0:3],v[196:199],v[152:155],v[0:3]
ld v[128:131],v244,s[52:55],s91 offen offset:2048 sc1
mm v[32:35],v[196:199],v[156:159],v[32:35]
ar v226,a154
ar v227,a155
mm v[32:35],v[192:195],v[136:139],v[32:35]
rt v[220:221],v243 offset:59136
rt v[222:223],v243 offset:60160
mm v[64:67],v[192:195],v[144:147],v[64:67]
ar v228,a156
ar v229,a157
mm v[64:67],v[196:199],a[120:123],v[64:67]
ld v[140:143],v245,s[52:55],s91 offen offset:2048 sc1
mm v[96:99],v[196:199],a[124:127],v[96:99]
ar v230,a158
ar v231,a159
mm v[96:99],v[192:195],v[148:151],v[96:99]
rt v[196:197],v240 offset:50816
rt v[198:199],v240 offset:51840
mm v[100:103],v[200:203],v[148:151],v[100:103]
s_add_u32 m0, s83, 35968
cv v248,v224,v225
mm v[100:103],v[204:207],a[124:127],v[100:103]
ld v234,s[44:47],s80 offen lds
mm v[68:71],v[204:207],a[120:123],v[68:71]
cv v249,v226,v227
cv v250,v228,v229
mm v[68:71],v[200:203],v[144:147],v[68:71]
rt v[192:193],v240 offset:59264
rt v[194:195],v240 offset:60288
mm v[36:39],v[200:203],v[136:139],v[36:39]
cv v251,v230,v231
aw a152,v248
mm v[36:39],v[204:207],v[156:159],v[36:39]
ld a[96:99],v246,s[52:55],s91 offen offset:2048 sc1
mm v[4:7],v[204:207],v[152:155],v[4:7]
wt lgkmcnt(4)
aw a153,v249
mm v[4:7],v[200:203],v[132:135],v[4:7]
rt v[204:205],v241 offset:50816
rt v[206:207],v241 offset:51840
mm v[8:11],v[208:211],v[132:135],v[8:11]
s_add_u32 m0, s83, 36992
aw a154,v250
mm v[8:11],v[212:215],v[152:155],v[8:11]
ld v235,s[44:47],s80 offen lds
mm v[40:43],v[212:215],v[156:159],v[40:43]
s_add_u32 s80, s80, 524288
aw a155,v251
mm v[40:43],v[208:211],v[136:139],v[40:43]
rt v[200:201],v241 offset:59264
rt v[202:203],v241 offset:60288
mm v[72:75],v[208:211],v[144:147],v[72:75]
s_cmp_eq_u32 s77, 0
s_cselect_b64 s[40:41], s[30:31], s[32:33]
mm v[72:75],v[212:215],a[120:123],v[72:75]
ld a[100:103],v247,s[52:55],s91 offen offset:2048 sc1
mm v[104:107],v[212:215],a[124:127],v[104:107]
s_add_u32 s40, s40, s93
s_addc_u32 s41, s41, 0
mm v[104:107],v[208:211],v[148:151],v[104:107]
rt v[212:213],v242 offset:50816
rt v[214:215],v242 offset:51840
mm v[108:111],v[216:219],v[148:151],v[108:111]
s_and_b32 s41, s41, 0xffff
s_mov_b32 s42, s95
mm v[108:111],v[220:223],a[124:127],v[108:111]
s_mov_b32 s43, 0x20000
s_mov_b32 s92, s72
mm v[76:79],v[220:223],a[120:123],v[76:79]
s_xor_b32 s92, s92, 1
s_and_b32 s101, s92, 7
mm v[76:79],v[216:219],v[144:147],v[76:79]
rt v[208:209],v242 offset:59264
rt v[210:211],v242 offset:60288
mm v[44:47],v[216:219],v[136:139],v[44:47]
ds_write_b128 v238, v[248:251] offset:33792
mm v[44:47],v[220:223],v[156:159],v[44:47]
st a[144:147],v255,s[48:51],s18 offen offset:3072 sc1
mm v[12:15],v[220:223],v[152:155],v[12:15]
wt lgkmcnt(5)
s_lshl_b32 s92, s101, 8
mm v[12:15],v[216:219],v[132:135],v[12:15]
rt v[220:221],v243 offset:50816
rt v[222:223],v243 offset:51840
mm v[16:19],v[196:199],v[132:135],v[16:19]
ar v224,a208
ar v225,a209
mm v[16:19],v[192:195],v[152:155],v[16:19]
ld a[104:107],v244,s[52:55],s91 offen offset:3072 sc1
mm v[48:51],v[192:195],v[156:159],v[48:51]
ar v226,a210
ar v227,a211
mm v[48:51],v[196:199],v[136:139],v[48:51]
rt v[216:217],v243 offset:59264
rt v[218:219],v243 offset:60288
mm v[80:83],v[196:199],v[144:147],v[80:83]
ar v228,a212
ar v229,a213
mm v[80:83],v[192:195],a[120:123],v[80:83]
ld a[108:111],v245,s[52:55],s91 offen offset:3072 sc1
mm v[112:115],v[192:195],a[124:127],v[112:115]
ar v230,a214
ar v231,a215
mm v[112:115],v[196:199],v[148:151],v[112:115]
cv v248,v224,v225
cv v249,v226,v227
mm v[116:119],v[204:207],v[148:151],v[116:119]
wt lgkmcnt(0)
s_barrier
mm v[116:119],v[200:203],a[124:127],v[116:119]
ld a[112:115],v246,s[52:55],s91 offen offset:3072 sc1
mm v[84:87],v[200:203],a[120:123],v[84:87]
rt v[192:193],v240 offset:0
rt v[194:195],v240 offset:1024
mm v[84:87],v[204:207],v[144:147],v[84:87]
rt v[196:197],v240 offset:8448
rt v[198:199],v240 offset:9472
mm v[52:55],v[204:207],v[136:139],v[52:55]
s_add_u32 m0, s83, 50688
cv v250,v228,v229
mm v[52:55],v[200:203],v[156:159],v[52:55]
ld v232,s[44:47],s80 offen lds
mm v[20:23],v[200:203],v[152:155],v[20:23]
cv v251,v230,v231
aw a156,v248
mm v[20:23],v[204:207],v[132:135],v[20:23]
rt v[200:201],v241 offset:0
rt v[202:203],v241 offset:1024
mm v[24:27],v[212:215],v[132:135],v[24:27]
aw a157,v249
aw a158,v250
mm v[24:27],v[208:211],v[152:155],v[24:27]
ld a[116:119],v247,s[52:55],s91 offen offset:3072 sc1
mm v[56:59],v[208:211],v[156:159],v[56:59]
s_add_u32 s91, s91, 4096
aw a159,v251
mm v[56:59],v[212:215],v[136:139],v[56:59]
rt v[204:205],v241 offset:8448
rt v[206:207],v241 offset:9472
mm v[88:91],v[212:215],v[144:147],v[88:91]
s_add_u32 m0, s83, 51712
s_sub_u32 s93, 8388608, s92
mm v[88:91],v[208:211],a[120:123],v[88:91]
ld v233,s[44:47],s80 offen lds
mm v[120:123],v[208:211],a[124:127],v[120:123]
s_add_u32 s56, s28, s92
s_addc_u32 s57, s29, 0
mm v[120:123],v[212:215],v[148:151],v[120:123]
rt v[208:209],v242 offset:0
rt v[210:211],v242 offset:1024
mm v[124:127],v[220:223],v[148:151],v[124:127]
s_and_b32 s57, s57, 0xffff
s_mov_b32 s58, s93
mm v[124:127],v[216:219],a[124:127],v[124:127]
s_mov_b32 s59, 0x20000
s_lshl_b32 s93, s96, 16
mm v[92:95],v[216:219],a[120:123],v[92:95]
s_mov_b32 s18, 32768
s_lshl_b32 s95, s94, 16
mm v[92:95],v[220:223],v[144:147],v[92:95]
rt v[212:213],v242 offset:8448
rt v[214:215],v242 offset:9472
mm v[60:63],v[220:223],v[136:139],v[60:63]
ds_write_b128 v238, v[248:251] offset:34816
mm v[60:63],v[216:219],v[156:159],v[60:63]
st a[148:151],v255,s[48:51],s18 offen offset:0 sc1
mm v[28:31],v[216:219],v[152:155],v[28:31]
wt vmcnt(15) lgkmcnt(5)
mm v[28:31],v[220:223],v[132:135],v[28:31]
rt v[216:217],v243 offset:0
rt v[218:219],v243 offset:1024
mm v[0:3],v[192:195],v[160:163],v[0:3]
ar v224,a160
ar v225,a161
mm v[0:3],v[196:199],v[176:179],v[0:3]
ld v[132:135],v244,s[52:55],s91 offen offset:0 sc1
mm v[32:35],v[196:199],v[180:183],v[32:35]
ar v226,a162
ar v227,a163
mm v[32:35],v[192:195],v[164:167],v[32:35]
rt v[220:221],v243 offset:8448
rt v[222:223],v243 offset:9472
mm v[64:67],v[192:195],v[168:171],v[64:67]
ar v228,a164
ar v229,a165
mm v[64:67],v[196:199],v[184:187],v[64:67]
ld v[136:139],v245,s[52:55],s91 offen offset:0 sc1
mm v[96:99],v[196:199],v[188:191],v[96:99]
ar v230,a166
ar v231,a167
mm v[96:99],v[192:195],v[172:175],v[96:99]
rt v[196:197],v240 offset:128
rt v[198:199],v240 offset:1152
mm v[100:103],v[200:203],v[172:175],v[100:103]
s_add_u32 m0, s83, 52864
cv v248,v224,v225
mm v[100:103],v[204:207],v[188:191],v[100:103]
ld v234,s[44:47],s80 offen lds
mm v[68:71],v[204:207],v[184:187],v[68:71]
cv v249,v226,v227
cv v250,v228,v229
mm v[68:71],v[200:203],v[168:171],v[68:71]
rt v[192:193],v240 offset:8576
rt v[194:195],v240 offset:9600
mm v[36:39],v[200:203],v[164:167],v[36:39]
cv v251,v230,v231
aw a160,v248
mm v[36:39],v[204:207],v[180:183],v[36:39]
ld v[144:147],v246,s[52:55],s91 offen offset:0 sc1
mm v[4:7],v[204:207],v[176:179],v[4:7]
wt lgkmcnt(4)
aw a161,v249
mm v[4:7],v[200:203],v[160:163],v[4:7]
rt v[204:205],v241 offset:128
rt v[206:207],v241 offset:1152
mm v[8:11],v[208:211],v[160:163],v[8:11]
s_add_u32 m0, s83, 53888
aw a162,v250
mm v[8:11],v[212:215],v[176:179],v[8:11]
ld v235,s[44:47],s80 offen lds
mm v[40:43],v[212:215],v[180:183],v[40:43]
s_add_u32 s80, s80, 524288
aw a163,v251
mm v[40:43],v[208:211],v[164:167],v[40:43]
rt v[200:201],v241 offset:8576
rt v[202:203],v241 offset:9600
mm v[72:75],v[208:211],v[168:171],v[72:75]
s_add_u32 s28, s34, s93
s_addc_u32 s29, s35, 0
mm v[72:75],v[212:215],v[184:187],v[72:75]
ld v[148:151],v247,s[52:55],s91 offen offset:0 sc1
mm v[104:107],v[212:215],v[188:191],v[104:107]
s_and_b32 s29, s29, 0xffff
s_mov_b32 s30, s95
mm v[104:107],v[208:211],v[172:175],v[104:107]
rt v[212:213],v242 offset:128
rt v[214:215],v242 offset:1152
mm v[108:111],v[216:219],v[172:175],v[108:111]
s_mov_b32 s31, 0x20000
s_lshl_b32 s93, s101, 12
mm v[108:111],v[220:223],v[188:191],v[108:111]
s_add_u32 s92, s93, 71303168
s_sub_u32 s93, 16777216, s93
mm v[76:79],v[220:223],v[184:187],v[76:79]
s_add_u32 s32, s26, s92
s_addc_u32 s33, s27, 0
mm v[76:79],v[216:219],v[168:171],v[76:79]
rt v[208:209],v242 offset:8576
rt v[210:211],v242 offset:9600
mm v[44:47],v[216:219],v[164:167],v[44:47]
ds_write_b128 v238, v[248:251] offset:35840
mm v[44:47],v[220:223],v[180:183],v[44:47]
st a[152:155],v255,s[48:51],s18 offen offset:1024 sc1
mm v[12:15],v[220:223],v[176:179],v[12:15]
wt lgkmcnt(5)
s_and_b32 s33, s33, 0xffff
mm v[12:15],v[216:219],v[160:163],v[12:15]
rt v[220:221],v243 offset:128
rt v[222:223],v243 offset:1152
mm v[16:19],v[196:199],v[160:163],v[16:19]
ar v224,a216
ar v225,a217
mm v[16:19],v[192:195],v[176:179],v[16:19]
ld v[152:155],v244,s[52:55],s91 offen offset:1024 sc1
mm v[48:51],v[192:195],v[180:183],v[48:51]
ar v226,a218
ar v227,a219
mm v[48:51],v[196:199],v[164:167],v[48:51]
rt v[216:217],v243 offset:8576
rt v[218:219],v243 offset:9600
mm v[80:83],v[196:199],v[168:171],v[80:83]
ar v228,a220
ar v229,a221
mm v[80:83],v[192:195],v[184:187],v[80:83]
ld v[156:159],v245,s[52:55],s91 offen offset:1024 sc1
mm v[112:115],v[192:195],v[188:191],v[112:115]
ar v230,a222
ar v231,a223
mm v[112:115],v[196:199],v[172:175],v[112:115]
cv v248,v224,v225
cv v249,v226,v227
mm v[116:119],v[204:207],v[172:175],v[116:119]
wt lgkmcnt(0)
s_barrier
mm v[116:119],v[200:203],v[188:191],v[116:119]
ld a[120:123],v246,s[52:55],s91 offen offset:1024 sc1
mm v[84:87],v[200:203],v[184:187],v[84:87]
rt v[192:193],v240 offset:16896
rt v[194:195],v240 offset:17920
mm v[84:87],v[204:207],v[168:171],v[84:87]
rt v[196:197],v240 offset:25344
rt v[198:199],v240 offset:26368
mm v[52:55],v[204:207],v[164:167],v[52:55]
s_add_u32 m0, s83, 0
cv v250,v228,v229
mm v[52:55],v[200:203],v[180:183],v[52:55]
ld v232,s[44:47],s80 offen lds
mm v[20:23],v[200:203],v[176:179],v[20:23]
cv v251,v230,v231
aw a164,v248
mm v[20:23],v[204:207],v[160:163],v[20:23]
rt v[200:201],v241 offset:16896
rt v[202:203],v241 offset:17920
mm v[24:27],v[212:215],v[160:163],v[24:27]
aw a165,v249
aw a166,v250
mm v[24:27],v[208:211],v[176:179],v[24:27]
ld a[124:127],v247,s[52:55],s91 offen offset:1024 sc1
mm v[56:59],v[208:211],v[180:183],v[56:59]
aw a167,v251
s_mov_b32 s34, s93
mm v[56:59],v[212:215],v[164:167],v[56:59]
rt v[204:205],v241 offset:25344
rt v[206:207],v241 offset:26368
mm v[88:91],v[212:215],v[168:171],v[88:91]
s_add_u32 m0, s83, 1024
s_mov_b32 s35, 0x20000
mm v[88:91],v[208:211],v[184:187],v[88:91]
ld v233,s[44:47],s80 offen lds
mm v[120:123],v[208:211],v[188:191],v[120:123]
s_lshl2_add_u32 s92, s72, s74
s_lshl2_add_u32 s92, s92, s3
mm v[120:123],v[212:215],v[172:175],v[120:123]
rt v[208:209],v242 offset:16896
rt v[210:211],v242 offset:17920
mm v[124:127],v[220:223],v[172:175],v[124:127]
s_lshl_b32 s88, s92, 8
s_mul_i32 s93, s92, 17408
mm v[124:127],v[216:219],v[188:191],v[124:127]
s_lshl_b32 s92, s92, 14
s_add_u32 s92, s92, 4194304
mm v[92:95],v[216:219],v[184:187],v[92:95]
s_add_u32 s86, s26, s92
s_addc_u32 s87, s27, 0
mm v[92:95],v[220:223],v[168:171],v[92:95]
rt v[212:213],v242 offset:25344
rt v[214:215],v242 offset:26368
mm v[60:63],v[220:223],v[164:167],v[60:63]
ds_write_b128 v238, v[248:251] offset:36864
mm v[60:63],v[216:219],v[180:183],v[60:63]
st a[156:159],v255,s[48:51],s18 offen offset:2048 sc1
mm v[28:31],v[216:219],v[176:179],v[28:31]
wt vmcnt(16) lgkmcnt(5)
s_and_b32 s87, s87, 0xffff
mm v[28:31],v[220:223],v[160:163],v[28:31]
rt v[216:217],v243 offset:16896
rt v[218:219],v243 offset:17920
mm v[0:3],v[192:195],v[128:131],v[0:3]
ar v224,a168
ar v225,a169
mm v[0:3],v[196:199],a[104:107],v[0:3]
ld v[160:163],v244,s[52:55],s91 offen offset:2048 sc1
mm v[32:35],v[196:199],a[108:111],v[32:35]
ar v226,a170
ar v227,a171
mm v[32:35],v[192:195],v[140:143],v[32:35]
rt v[220:221],v243 offset:25344
rt v[222:223],v243 offset:26368
mm v[64:67],v[192:195],a[96:99],v[64:67]
ar v228,a172
ar v229,a173
mm v[64:67],v[196:199],a[112:115],v[64:67]
ld v[164:167],v245,s[52:55],s91 offen offset:2048 sc1
mm v[96:99],v[196:199],a[116:119],v[96:99]
ar v230,a174
ar v231,a175
mm v[96:99],v[192:195],a[100:103],v[96:99]
rt v[196:197],v240 offset:17024
rt v[198:199],v240 offset:18048
mm v[100:103],v[200:203],a[100:103],v[100:103]
s_add_u32 m0, s83, 2176
cv v248,v224,v225
mm v[100:103],v[204:207],a[116:119],v[100:103]
ld v234,s[44:47],s80 offen lds
mm v[68:71],v[204:207],a[112:115],v[68:71]
cv v249,v226,v227
cv v250,v228,v229
mm v[68:71],v[200:203],a[96:99],v[68:71]
rt v[192:193],v240 offset:25472
rt v[194:195],v240 offset:26496
mm v[36:39],v[200:203],v[140:143],v[36:39]
cv v251,v230,v231
aw a168,v248
mm v[36:39],v[204:207],a[108:111],v[36:39]
ld v[168:171],v246,s[52:55],s91 offen offset:2048 sc1
mm v[4:7],v[204:207],a[104:107],v[4:7]
wt lgkmcnt(4)
aw a169,v249
mm v[4:7],v[200:203],v[128:131],v[4:7]
rt v[204:205],v241 offset:17024
rt v[206:207],v241 offset:18048
mm v[8:11],v[208:211],v[128:131],v[8:11]
s_add_u32 m0, s83, 3200
aw a170,v250
mm v[8:11],v[212:215],a[104:107],v[8:11]
ld v235,s[44:47],s80 offen lds
mm v[40:43],v[212:215],a[108:111],v[40:43]
s_add_u32 s80, s80, 524288
aw a171,v251
mm v[40:43],v[208:211],v[140:143],v[40:43]
rt v[200:201],v241 offset:25472
rt v[202:203],v241 offset:26496
mm v[72:75],v[208:211],a[96:99],v[72:75]
s_add_u32 s93, s93, 150994944
mm v[72:75],v[212:215],a[112:115],v[72:75]
ld v[172:175],v247,s[52:55],s91 offen offset:2048 sc1
mm v[104:107],v[212:215],a[116:119],v[104:107]
s_add_u32 s68, s26, s93
s_addc_u32 s69, s27, 0
mm v[104:107],v[208:211],a[100:103],v[104:107]
rt v[212:213],v242 offset:17024
rt v[214:215],v242 offset:18048
mm v[108:111],v[216:219],a[100:103],v[108:111]
s_and_b32 s69, s69, 0xffff
s_mov_b32 s70, 17408
mm v[108:111],v[220:223],a[116:119],v[108:111]
s_mov_b32 s96, 152962078
s_mov_b32 s97, 371069214
mm v[76:79],v[220:223],a[112:115],v[76:79]
s_lshl_b32 s95, s73, 3
s_lshr_b64 s[96:97], s[96:97], s95
mm v[76:79],v[216:219],a[96:99],v[76:79]
rt v[208:209],v242 offset:25472
rt v[210:211],v242 offset:26496
mm v[44:47],v[216:219],v[140:143],v[44:47]
ds_write_b128 v238, v[248:251] offset:37888
mm v[44:47],v[220:223],a[108:111],v[44:47]
st a[160:163],v255,s[48:51],s18 offen offset:3072 sc1
mm v[12:15],v[220:223],a[104:107],v[12:15]
wt lgkmcnt(5)
s_and_b32 s82, s96, 255
mm v[12:15],v[216:219],v[128:131],v[12:15]
rt v[220:221],v243 offset:17024
rt v[222:223],v243 offset:18048
mm v[16:19],v[196:199],v[128:131],v[16:19]
ar v224,a192
ar v225,a193
.La5_LW1_11:
s_cbranch_execz .La5_LW1_15
mm v[16:19],v[192:195],a[104:107],v[16:19]
ld v[176:179],v244,s[52:55],s91 offen offset:3072 sc1
mm v[48:51],v[192:195],a[108:111],v[48:51]
ar v226,a194
ar v227,a195
mm v[48:51],v[196:199],v[140:143],v[48:51]
rt v[216:217],v243 offset:25472
rt v[218:219],v243 offset:26496
mm v[80:83],v[196:199],a[96:99],v[80:83]
ar v228,a196
ar v229,a197
mm v[80:83],v[192:195],a[112:115],v[80:83]
ld v[180:183],v245,s[52:55],s91 offen offset:3072 sc1
mm v[112:115],v[192:195],a[116:119],v[112:115]
ar v230,a198
ar v231,a199
mm v[112:115],v[196:199],a[100:103],v[112:115]
cv v248,v224,v225
cv v249,v226,v227
mm v[116:119],v[204:207],a[100:103],v[116:119]
wt lgkmcnt(0)
s_barrier
mm v[116:119],v[200:203],a[116:119],v[116:119]
ld v[184:187],v246,s[52:55],s91 offen offset:3072 sc1
mm v[84:87],v[200:203],a[112:115],v[84:87]
rt v[192:193],v240 offset:33792
rt v[194:195],v240 offset:34816
mm v[84:87],v[204:207],a[96:99],v[84:87]
rt v[196:197],v240 offset:42240
rt v[198:199],v240 offset:43264
mm v[52:55],v[204:207],v[140:143],v[52:55]
s_add_u32 m0, s83, 16896
cv v250,v228,v229
mm v[52:55],v[200:203],a[108:111],v[52:55]
ld v232,s[44:47],s80 offen lds
mm v[20:23],v[200:203],a[104:107],v[20:23]
cv v251,v230,v231
aw a172,v248
mm v[20:23],v[204:207],v[128:131],v[20:23]
rt v[200:201],v241 offset:33792
rt v[202:203],v241 offset:34816
mm v[24:27],v[212:215],v[128:131],v[24:27]
aw a173,v249
aw a174,v250
mm v[24:27],v[208:211],a[104:107],v[24:27]
ld v[188:191],v247,s[52:55],s91 offen offset:3072 sc1
mm v[56:59],v[208:211],a[108:111],v[56:59]
s_add_u32 s91, s91, 4096
aw a175,v251
mm v[56:59],v[212:215],v[140:143],v[56:59]
rt v[204:205],v241 offset:42240
rt v[206:207],v241 offset:43264
mm v[88:91],v[212:215],a[96:99],v[88:91]
s_add_u32 m0, s83, 17920
s_lshl_b32 s95, 1, s82
mm v[88:91],v[208:211],a[112:115],v[88:91]
ld v233,s[44:47],s80 offen lds
mm v[120:123],v[208:211],a[116:119],v[120:123]
s_lshl_b32 s96, s75, 22
s_or_b32 s95, s95, s96
mm v[120:123],v[212:215],a[100:103],v[120:123]
rt v[208:209],v242 offset:33792
rt v[210:211],v242 offset:34816
mm v[124:127],v[220:223],a[100:103],v[124:127]
s_cmp_eq_u32 s73, 1
s_cselect_b32 s96, 12, 4
s_cselect_b32 s79, 0, 0xff000000
mm v[124:127],v[216:219],a[116:119],v[124:127]
s_or_b32 s98, s95, s96
s_mov_b32 s99, 0
mm v[92:95],v[216:219],a[112:115],v[92:95]
s_cmp_eq_u32 s74, 3
s_cselect_b32 s23, 3, 0
s_cselect_b32 s22, 0, s75
mm v[92:95],v[220:223],a[96:99],v[92:95]
rt v[212:213],v242 offset:42240
rt v[214:215],v242 offset:43264
mm v[60:63],v[220:223],v[140:143],v[60:63]
ds_write_b128 v238, v[248:251] offset:38912
mm v[60:63],v[216:219],a[108:111],v[60:63]
s_lshl_b32 s95, s76, 5
s_add_u32 s95, s95, s3
mm v[28:31],v[216:219],a[104:107],v[28:31]
wt vmcnt(15) lgkmcnt(5)
s_lshl_b32 s95, s95, 5
mm v[28:31],v[220:223],v[128:131],v[28:31]
rt v[216:217],v243 offset:33792
rt v[218:219],v243 offset:34816
mm v[0:3],v[192:195],v[132:135],v[0:3]
ar v224,a176
ar v225,a177
mm v[0:3],v[196:199],v[152:155],v[0:3]
ld v[128:131],v244,s[52:55],s91 offen offset:0 sc1
mm v[32:35],v[196:199],v[156:159],v[32:35]
ar v226,a178
ar v227,a179
mm v[32:35],v[192:195],v[136:139],v[32:35]
rt v[220:221],v243 offset:42240
rt v[222:223],v243 offset:43264
mm v[64:67],v[192:195],v[144:147],v[64:67]
ar v228,a180
ar v229,a181
mm v[64:67],v[196:199],a[120:123],v[64:67]
ld v[140:143],v245,s[52:55],s91 offen offset:0 sc1
mm v[96:99],v[196:199],a[124:127],v[96:99]
ar v230,a182
ar v231,a183
mm v[96:99],v[192:195],v[148:151],v[96:99]
rt v[196:197],v240 offset:33920
rt v[198:199],v240 offset:34944
mm v[100:103],v[200:203],v[148:151],v[100:103]
s_add_u32 m0, s83, 19072
cv v248,v224,v225
mm v[100:103],v[204:207],a[124:127],v[100:103]
ld v234,s[44:47],s80 offen lds
mm v[68:71],v[204:207],a[120:123],v[68:71]
cv v249,v226,v227
cv v250,v228,v229
mm v[68:71],v[200:203],v[144:147],v[68:71]
rt v[192:193],v240 offset:42368
rt v[194:195],v240 offset:43392
mm v[36:39],v[200:203],v[136:139],v[36:39]
cv v251,v230,v231
aw a176,v248
mm v[36:39],v[204:207],v[156:159],v[36:39]
ld a[96:99],v246,s[52:55],s91 offen offset:0 sc1
mm v[4:7],v[204:207],v[152:155],v[4:7]
wt lgkmcnt(4)
aw a177,v249
mm v[4:7],v[200:203],v[132:135],v[4:7]
rt v[204:205],v241 offset:33920
rt v[206:207],v241 offset:34944
mm v[8:11],v[208:211],v[132:135],v[8:11]
s_add_u32 m0, s83, 20096
aw a178,v250
mm v[8:11],v[212:215],v[152:155],v[8:11]
ld v235,s[44:47],s80 offen lds
mm v[40:43],v[212:215],v[156:159],v[40:43]
s_add_u32 s80, s80, 524288
aw a179,v251
mm v[40:43],v[208:211],v[136:139],v[40:43]
rt v[200:201],v241 offset:42368
rt v[202:203],v241 offset:43392
mm v[72:75],v[208:211],v[144:147],v[72:75]
s_add_u32 s95, s95, s72
s_lshl_b32 s95, s95, 3
mm v[72:75],v[212:215],a[120:123],v[72:75]
ld a[100:103],v247,s[52:55],s91 offen offset:0 sc1
mm v[104:107],v[212:215],a[124:127],v[104:107]
s_add_u32 s6, s95, 1048576
v_and_b32 v239, 0x70, v255
mm v[104:107],v[208:211],v[148:151],v[104:107]
rt v[212:213],v242 offset:33920
rt v[214:215],v242 offset:34944
mm v[108:111],v[216:219],v[148:151],v[108:111]
v_lshlrev_b32 v239, 6, v239
aw a231,v239
mm v[108:111],v[220:223],a[124:127],v[108:111]
s_mul_i32 s95, s75, 0
s_add_u32 s95, s95, 2
mm v[76:79],v[220:223],a[120:123],v[76:79]
s_mov_b32 s18, 36864
s_sub_u32 s96, 4, s95
mm v[76:79],v[216:219],v[144:147],v[76:79]
rt v[208:209],v242 offset:42368
rt v[210:211],v242 offset:43392
mm v[44:47],v[216:219],v[136:139],v[44:47]
ds_write_b128 v238, v[248:251] offset:39936
mm v[44:47],v[220:223],v[156:159],v[44:47]
st a[164:167],v255,s[48:51],s18 offen offset:0 sc1
mm v[12:15],v[220:223],v[152:155],v[12:15]
wt lgkmcnt(5)
s_lshr_b32 s97, s2, 4
mm v[12:15],v[216:219],v[132:135],v[12:15]
rt v[220:221],v243 offset:33920
rt v[222:223],v243 offset:34944
mm v[16:19],v[196:199],v[132:135],v[16:19]
ar v224,a200
ar v225,a201
mm v[16:19],v[192:195],v[152:155],v[16:19]
ld a[104:107],v244,s[52:55],s91 offen offset:1024 sc1
mm v[48:51],v[192:195],v[156:159],v[48:51]
ar v226,a202
ar v227,a203
mm v[48:51],v[196:199],v[136:139],v[48:51]
rt v[216:217],v243 offset:42368
rt v[218:219],v243 offset:43392
mm v[80:83],v[196:199],v[144:147],v[80:83]
ar v228,a204
ar v229,a205
mm v[80:83],v[192:195],a[120:123],v[80:83]
ld a[108:111],v245,s[52:55],s91 offen offset:1024 sc1
mm v[112:115],v[192:195],a[124:127],v[112:115]
ar v230,a206
ar v231,a207
mm v[112:115],v[196:199],v[148:151],v[112:115]
cv v248,v224,v225
cv v249,v226,v227
mm v[116:119],v[204:207],v[148:151],v[116:119]
wt lgkmcnt(0)
s_barrier
mm v[116:119],v[200:203],a[124:127],v[116:119]
ld a[112:115],v246,s[52:55],s91 offen offset:1024 sc1
mm v[84:87],v[200:203],a[120:123],v[84:87]
rt v[192:193],v240 offset:50688
rt v[194:195],v240 offset:51712
mm v[84:87],v[204:207],v[144:147],v[84:87]
rt v[196:197],v240 offset:59136
rt v[198:199],v240 offset:60160
mm v[52:55],v[204:207],v[136:139],v[52:55]
s_add_u32 m0, s83, 33792
cv v250,v228,v229
mm v[52:55],v[200:203],v[156:159],v[52:55]
ld v232,s[44:47],s80 offen lds
mm v[20:23],v[200:203],v[152:155],v[20:23]
cv v251,v230,v231
aw a180,v248
mm v[20:23],v[204:207],v[132:135],v[20:23]
rt v[200:201],v241 offset:50688
rt v[202:203],v241 offset:51712
mm v[24:27],v[212:215],v[132:135],v[24:27]
aw a181,v249
aw a182,v250
mm v[24:27],v[208:211],v[152:155],v[24:27]
ld a[116:119],v247,s[52:55],s91 offen offset:1024 sc1
mm v[56:59],v[208:211],v[156:159],v[56:59]
aw a183,v251
s_add_u32 s85, s85, 65536
mm v[56:59],v[212:215],v[136:139],v[56:59]
rt v[204:205],v241 offset:59136
rt v[206:207],v241 offset:60160
mm v[88:91],v[212:215],v[144:147],v[88:91]
s_add_u32 m0, s83, 34816
v_add_u32 v238, s85, v255
mm v[88:91],v[208:211],a[120:123],v[88:91]
ld v233,s[44:47],s80 offen lds
mm v[120:123],v[208:211],a[124:127],v[120:123]
s_mov_b32 s85, 0
s_mul_i32 s97, s97, s95
mm v[120:123],v[212:215],v[148:151],v[120:123]
rt v[208:209],v242 offset:50688
rt v[210:211],v242 offset:51712
mm v[124:127],v[220:223],v[148:151],v[124:127]
v_lshrrev_b32 v239, 7, v255
v_add_u32 v254, s97, v239
mm v[124:127],v[216:219],a[124:127],v[124:127]
v_mul_u32_u24 v254, s21, v254
v_add_u32 v239, s96, v239
mm v[92:95],v[216:219],a[120:123],v[92:95]
v_lshrrev_b32 v239, 2, v239
v_lshl_add_u32 v254, v239, 28, v254
mm v[92:95],v[220:223],v[144:147],v[92:95]
rt v[212:213],v242 offset:59136
rt v[214:215],v242 offset:60160
mm v[60:63],v[220:223],v[136:139],v[60:63]
ds_write_b128 v238, v[248:251] offset:0
mm v[60:63],v[216:219],v[156:159],v[60:63]
st a[168:171],v255,s[48:51],s18 offen offset:1024 sc1
mm v[28:31],v[216:219],v[152:155],v[28:31]
wt vmcnt(15) lgkmcnt(5)
v_and_b32 v239, 0x70, v255
mm v[28:31],v[220:223],v[132:135],v[28:31]
rt v[216:217],v243 offset:50688
rt v[218:219],v243 offset:51712
mm v[0:3],v[192:195],v[160:163],v[0:3]
ar v224,a184
ar v225,a185
mm v[0:3],v[196:199],v[176:179],v[0:3]
ld v[132:135],v244,s[52:55],s91 offen offset:2048 sc1
mm v[32:35],v[196:199],v[180:183],v[32:35]
ar v226,a186
ar v227,a187
mm v[32:35],v[192:195],v[164:167],v[32:35]
rt v[220:221],v243 offset:59136
rt v[222:223],v243 offset:60160
mm v[64:67],v[192:195],v[168:171],v[64:67]
ar v228,a188
ar v229,a189
mm v[64:67],v[196:199],v[184:187],v[64:67]
ld v[136:139],v245,s[52:55],s91 offen offset:2048 sc1
mm v[96:99],v[196:199],v[188:191],v[96:99]
ar v230,a190
ar v231,a191
mm v[96:99],v[192:195],v[172:175],v[96:99]
rt v[196:197],v240 offset:50816
rt v[198:199],v240 offset:51840
mm v[100:103],v[200:203],v[172:175],v[100:103]
s_add_u32 m0, s83, 35968
cv v248,v224,v225
mm v[100:103],v[204:207],v[188:191],v[100:103]
ld v234,s[44:47],s80 offen lds
mm v[68:71],v[204:207],v[184:187],v[68:71]
cv v249,v226,v227
cv v250,v228,v229
mm v[68:71],v[200:203],v[168:171],v[68:71]
rt v[192:193],v240 offset:59264
rt v[194:195],v240 offset:60288
mm v[36:39],v[200:203],v[164:167],v[36:39]
cv v251,v230,v231
aw a184,v248
mm v[36:39],v[204:207],v[180:183],v[36:39]
ld v[144:147],v246,s[52:55],s91 offen offset:2048 sc1
mm v[4:7],v[204:207],v[176:179],v[4:7]
wt lgkmcnt(4)
aw a185,v249
mm v[4:7],v[200:203],v[160:163],v[4:7]
rt v[204:205],v241 offset:50816
rt v[206:207],v241 offset:51840
mm v[8:11],v[208:211],v[160:163],v[8:11]
s_add_u32 m0, s83, 36992
aw a186,v250
mm v[8:11],v[212:215],v[176:179],v[8:11]
ld v235,s[44:47],s80 offen lds
mm v[40:43],v[212:215],v[180:183],v[40:43]
s_add_u32 s80, s80, 524288
aw a187,v251
mm v[40:43],v[208:211],v[164:167],v[40:43]
rt v[200:201],v241 offset:59264
rt v[202:203],v241 offset:60288
mm v[72:75],v[208:211],v[168:171],v[72:75]
v_lshl_add_u32 v254, v239, 3, v254
s_lshl_b32 s96, s7, 5
mm v[72:75],v[212:215],v[184:187],v[72:75]
ld v[148:151],v247,s[52:55],s91 offen offset:2048 sc1
mm v[104:107],v[212:215],v[188:191],v[104:107]
s_sub_u32 s96, 16, s96
s_mul_i32 s95, s95, s96
mm v[104:107],v[208:211],v[172:175],v[104:107]
rt v[212:213],v242 offset:50816
rt v[214:215],v242 offset:51840
mm v[108:111],v[216:219],v[172:175],v[108:111]
s_mul_i32 s21, s21, s95
v_lshrrev_b32 v236, 8, v255
mm v[108:111],v[220:223],v[188:191],v[108:111]
v_lshlrev_b32 v239, 5, v236
v_xor_b32 v239, v239, v255
mm v[76:79],v[220:223],v[184:187],v[76:79]
v_and_b32 v239, 0xf0, v239
v_lshl_add_u32 v239, v236, 11, v239
mm v[76:79],v[216:219],v[168:171],v[76:79]
rt v[208:209],v242 offset:59264
rt v[210:211],v242 offset:60288
mm v[44:47],v[216:219],v[164:167],v[44:47]
ds_write_b128 v238, v[248:251] offset:1024
mm v[44:47],v[220:223],v[180:183],v[44:47]
st a[172:175],v255,s[48:51],s18 offen offset:2048 sc1
mm v[12:15],v[220:223],v[176:179],v[12:15]
wt lgkmcnt(5)
s_lshl_b32 s92, s77, 16
mm v[12:15],v[216:219],v[160:163],v[12:15]
rt v[220:221],v243 offset:50816
rt v[222:223],v243 offset:51840
mm v[16:19],v[196:199],v[160:163],v[16:19]
ar v224,a64
ar v225,a65
mm v[16:19],v[192:195],v[176:179],v[16:19]
ld v[152:155],v244,s[52:55],s91 offen offset:3072 sc1
mm v[48:51],v[192:195],v[180:183],v[48:51]
ar v226,a66
ar v227,a67
mm v[48:51],v[196:199],v[164:167],v[48:51]
rt v[216:217],v243 offset:59264
rt v[218:219],v243 offset:60288
mm v[80:83],v[196:199],v[168:171],v[80:83]
ar v228,a68
ar v229,a69
mm v[80:83],v[192:195],v[184:187],v[80:83]
ld v[156:159],v245,s[52:55],s91 offen offset:3072 sc1
mm v[112:115],v[192:195],v[188:191],v[112:115]
ar v230,a70
ar v231,a71
mm v[112:115],v[196:199],v[172:175],v[112:115]
cv v248,v224,v225
cv v249,v226,v227
mm v[116:119],v[204:207],v[172:175],v[116:119]
wt lgkmcnt(0)
s_barrier
mm v[116:119],v[200:203],v[188:191],v[116:119]
ld a[120:123],v246,s[52:55],s91 offen offset:3072 sc1
mm v[84:87],v[200:203],v[184:187],v[84:87]
rt v[192:193],v240 offset:0
rt v[194:195],v240 offset:1024
mm v[84:87],v[204:207],v[168:171],v[84:87]
rt v[196:197],v240 offset:8448
rt v[198:199],v240 offset:9472
mm v[52:55],v[204:207],v[164:167],v[52:55]
s_add_u32 m0, s83, 50688
cv v250,v228,v229
mm v[52:55],v[200:203],v[180:183],v[52:55]
ld v232,s[44:47],s80 offen lds
mm v[20:23],v[200:203],v[176:179],v[20:23]
cv v251,v230,v231
aw a188,v248
mm v[20:23],v[204:207],v[160:163],v[20:23]
rt v[200:201],v241 offset:0
rt v[202:203],v241 offset:1024
mm v[24:27],v[212:215],v[160:163],v[24:27]
aw a189,v249
aw a190,v250
mm v[24:27],v[208:211],v[176:179],v[24:27]
ld a[124:127],v247,s[52:55],s91 offen offset:3072 sc1
mm v[56:59],v[208:211],v[180:183],v[56:59]
s_add_u32 s91, s91, 4096
aw a191,v251
mm v[56:59],v[212:215],v[164:167],v[56:59]
rt v[204:205],v241 offset:8448
rt v[206:207],v241 offset:9472
mm v[88:91],v[212:215],v[168:171],v[88:91]
s_add_u32 m0, s83, 51712
s_lshl_b32 s93, s78, 14
mm v[88:91],v[208:211],v[184:187],v[88:91]
ld v233,s[44:47],s80 offen lds
mm v[120:123],v[208:211],v[188:191],v[120:123]
s_add_u32 s92, s92, s93
v_add_u32 v239, s92, v239
mm v[120:123],v[212:215],v[172:175],v[120:123]
rt v[208:209],v242 offset:0
rt v[210:211],v242 offset:1024
mm v[124:127],v[220:223],v[172:175],v[124:127]
aw a228,v239
v_bfe_u32 v239, v255, 6, 2
mm v[124:127],v[216:219],v[188:191],v[124:127]
v_lshlrev_b32 v239, 6, v239
v_lshl_add_u32 v239, v236, 4, v239
mm v[92:95],v[216:219],v[184:187],v[92:95]
s_lshl_b32 s92, s78, 8
v_add_u32 v239, s92, v239
mm v[92:95],v[220:223],v[168:171],v[92:95]
rt v[212:213],v242 offset:8448
rt v[214:215],v242 offset:9472
mm v[60:63],v[220:223],v[164:167],v[60:63]
ds_write_b128 v238, v[248:251] offset:2048
mm v[60:63],v[216:219],v[180:183],v[60:63]
st a[176:179],v255,s[48:51],s18 offen offset:3072 sc1
mm v[28:31],v[216:219],v[176:179],v[28:31]
wt vmcnt(16) lgkmcnt(5)
v_mul_u32_u24 v239, 7168, v239
mm v[28:31],v[220:223],v[160:163],v[28:31]
rt v[216:217],v243 offset:0
rt v[218:219],v243 offset:1024
mm v[0:3],v[192:195],v[128:131],v[0:3]
ar v224,a0
ar v225,a1
mm v[0:3],v[196:199],a[104:107],v[0:3]
ld v[160:163],v244,s[52:55],s91 offen offset:0 sc1
mm v[32:35],v[196:199],a[108:111],v[32:35]
ar v226,a2
ar v227,a3
mm v[32:35],v[192:195],v[140:143],v[32:35]
rt v[220:221],v243 offset:8448
rt v[222:223],v243 offset:9472
mm v[64:67],v[192:195],a[96:99],v[64:67]
ar v228,a4
ar v229,a5
mm v[64:67],v[196:199],a[112:115],v[64:67]
ld v[164:167],v245,s[52:55],s91 offen offset:0 sc1
mm v[96:99],v[196:199],a[116:119],v[96:99]
ar v230,a6
ar v231,a7
mm v[96:99],v[192:195],a[100:103],v[96:99]
rt v[196:197],v240 offset:128
rt v[198:199],v240 offset:1152
mm v[100:103],v[200:203],a[100:103],v[100:103]
s_add_u32 m0, s83, 52864
cv v248,v224,v225
mm v[100:103],v[204:207],a[116:119],v[100:103]
ld v234,s[44:47],s80 offen lds
mm v[68:71],v[204:207],a[112:115],v[68:71]
cv v249,v226,v227
cv v250,v228,v229
mm v[68:71],v[200:203],a[96:99],v[68:71]
rt v[192:193],v240 offset:8576
rt v[194:195],v240 offset:9600
mm v[36:39],v[200:203],v[140:143],v[36:39]
cv v251,v230,v231
aw a0,v248
mm v[36:39],v[204:207],a[108:111],v[36:39]
ld v[168:171],v246,s[52:55],s91 offen offset:0 sc1
mm v[4:7],v[204:207],a[104:107],v[4:7]
wt lgkmcnt(4)
aw a1,v249
mm v[4:7],v[200:203],v[128:131],v[4:7]
rt v[204:205],v241 offset:128
rt v[206:207],v241 offset:1152
mm v[8:11],v[208:211],v[128:131],v[8:11]
s_add_u32 m0, s83, 53888
aw a2,v250
mm v[8:11],v[212:215],a[104:107],v[8:11]
ld v235,s[44:47],s80 offen lds
mm v[40:43],v[212:215],a[108:111],v[40:43]
s_add_u32 s80, s80, 524288
aw a3,v251
mm v[40:43],v[208:211],v[140:143],v[40:43]
rt v[200:201],v241 offset:8576
rt v[202:203],v241 offset:9600
mm v[72:75],v[208:211],a[96:99],v[72:75]
v_and_or_b32 v239, v255, 48, v239
aw a229,v239
mm v[72:75],v[212:215],a[112:115],v[72:75]
ld v[172:175],v247,s[52:55],s91 offen offset:0 sc1
mm v[104:107],v[212:215],a[116:119],v[104:107]
v_and_b32 v239, 0xf0, v255
v_lshl_add_u32 v239, v236, 14, v239
mm v[104:107],v[208:211],a[100:103],v[104:107]
rt v[212:213],v242 offset:128
rt v[214:215],v242 offset:1152
mm v[108:111],v[216:219],a[100:103],v[108:111]
s_lshl_b32 s92, s3, 9
v_add_u32 v239, s92, v239
mm v[108:111],v[220:223],a[116:119],v[108:111]
aw a230,v239
v_add_u32 v239, 4294886912, v240
mm v[76:79],v[220:223],a[112:115],v[76:79]
s_mov_b32 s18, 16384
aw a224,v239
mm v[76:79],v[216:219],a[96:99],v[76:79]
rt v[208:209],v242 offset:8576
rt v[210:211],v242 offset:9600
mm v[44:47],v[216:219],v[140:143],v[44:47]
ds_write_b128 v238, v[248:251] offset:3072
mm v[44:47],v[220:223],a[108:111],v[44:47]
st a[180:183],v255,s[48:51],s18 offen offset:0 sc1
mm v[12:15],v[220:223],a[104:107],v[12:15]
wt lgkmcnt(5)
v_bfe_u32 v239, v255, 4, 4
mm v[12:15],v[216:219],v[128:131],v[12:15]
rt v[220:221],v243 offset:128
rt v[222:223],v243 offset:1152
mm v[16:19],v[196:199],v[128:131],v[16:19]
ar v224,a72
ar v225,a73
mm v[16:19],v[192:195],a[104:107],v[16:19]
ld v[176:179],v244,s[52:55],s91 offen offset:1024 sc1
mm v[48:51],v[192:195],a[108:111],v[48:51]
ar v226,a74
ar v227,a75
mm v[48:51],v[196:199],v[140:143],v[48:51]
rt v[216:217],v243 offset:8576
rt v[218:219],v243 offset:9600
mm v[80:83],v[196:199],a[96:99],v[80:83]
ar v228,a76
.La5_LW1_12:
s_cbranch_execz .La5_LW1_R0
ar v229,a77
mm v[80:83],v[192:195],a[112:115],v[80:83]
ld v[180:183],v245,s[52:55],s91 offen offset:1024 sc1
mm v[112:115],v[192:195],a[116:119],v[112:115]
ar v230,a78
ar v231,a79
mm v[112:115],v[196:199],a[100:103],v[112:115]
cv v248,v224,v225
cv v249,v226,v227
mm v[116:119],v[204:207],a[100:103],v[116:119]
wt lgkmcnt(0)
s_barrier
mm v[116:119],v[200:203],a[116:119],v[116:119]
ld v[184:187],v246,s[52:55],s91 offen offset:1024 sc1
mm v[84:87],v[200:203],a[112:115],v[84:87]
rt v[192:193],v240 offset:16896
rt v[194:195],v240 offset:17920
mm v[84:87],v[204:207],a[96:99],v[84:87]
rt v[196:197],v240 offset:25344
rt v[198:199],v240 offset:26368
mm v[52:55],v[204:207],v[140:143],v[52:55]
s_add_u32 m0, s83, 0
cv v250,v228,v229
mm v[52:55],v[200:203],a[108:111],v[52:55]
ld v232,s[44:47],s80 offen lds
mm v[20:23],v[200:203],a[104:107],v[20:23]
cv v251,v230,v231
aw a4,v248
mm v[20:23],v[204:207],v[128:131],v[20:23]
rt v[200:201],v241 offset:16896
rt v[202:203],v241 offset:17920
mm v[24:27],v[212:215],v[128:131],v[24:27]
aw a5,v249
aw a6,v250
mm v[24:27],v[208:211],a[104:107],v[24:27]
ld v[188:191],v247,s[52:55],s91 offen offset:1024 sc1
mm v[56:59],v[208:211],a[108:111],v[56:59]
aw a7,v251
v_lshlrev_b32 v236, 2, v236
mm v[56:59],v[212:215],v[140:143],v[56:59]
rt v[204:205],v241 offset:25344
rt v[206:207],v241 offset:26368
mm v[88:91],v[212:215],a[96:99],v[88:91]
s_add_u32 m0, s83, 1024
v_sub_u32 v239, v239, v236
mm v[88:91],v[208:211],a[112:115],v[88:91]
ld v233,s[44:47],s80 offen lds
mm v[120:123],v[208:211],a[116:119],v[120:123]
v_subrev_u32 v236, 0, v239
v_ashrrev_i32 v236, 31, v236
mm v[120:123],v[212:215],a[100:103],v[120:123]
rt v[208:209],v242 offset:16896
rt v[210:211],v242 offset:17920
mm v[124:127],v[220:223],a[100:103],v[124:127]
v_and_b32 v236, 0xff800000, v236
aw a236,v236
mm v[124:127],v[216:219],a[116:119],v[124:127]
v_subrev_u32 v236, 1, v239
v_ashrrev_i32 v236, 31, v236
mm v[92:95],v[216:219],a[112:115],v[92:95]
v_and_b32 v236, 0xff800000, v236
aw a237,v236
mm v[92:95],v[220:223],a[96:99],v[92:95]
rt v[212:213],v242 offset:25344
rt v[214:215],v242 offset:26368
mm v[60:63],v[220:223],v[140:143],v[60:63]
ds_write_b128 v238, v[248:251] offset:4096
mm v[60:63],v[216:219],a[108:111],v[60:63]
v_subrev_u32 v236, 2, v239
v_ashrrev_i32 v236, 31, v236
mm v[28:31],v[216:219],a[104:107],v[28:31]
wt vmcnt(15) lgkmcnt(5)
v_and_b32 v236, 0xff800000, v236
mm v[28:31],v[220:223],v[128:131],v[28:31]
rt v[216:217],v243 offset:16896
rt v[218:219],v243 offset:17920
mm v[0:3],v[192:195],v[132:135],v[0:3]
ar v224,a8
ar v225,a9
mm v[0:3],v[196:199],v[152:155],v[0:3]
ld v[128:131],v244,s[52:55],s91 offen offset:2048 sc1
mm v[32:35],v[196:199],v[156:159],v[32:35]
ar v226,a10
ar v227,a11
mm v[32:35],v[192:195],v[136:139],v[32:35]
rt v[220:221],v243 offset:25344
rt v[222:223],v243 offset:26368
mm v[64:67],v[192:195],v[144:147],v[64:67]
ar v228,a12
ar v229,a13
mm v[64:67],v[196:199],a[120:123],v[64:67]
ld v[140:143],v245,s[52:55],s91 offen offset:2048 sc1
mm v[96:99],v[196:199],a[124:127],v[96:99]
ar v230,a14
ar v231,a15
mm v[96:99],v[192:195],v[148:151],v[96:99]
rt v[196:197],v240 offset:17024
rt v[198:199],v240 offset:18048
mm v[100:103],v[200:203],v[148:151],v[100:103]
s_add_u32 m0, s83, 2176
cv v248,v224,v225
mm v[100:103],v[204:207],a[124:127],v[100:103]
ld v234,s[44:47],s80 offen lds
mm v[68:71],v[204:207],a[120:123],v[68:71]
cv v249,v226,v227
cv v250,v228,v229
mm v[68:71],v[200:203],v[144:147],v[68:71]
rt v[192:193],v240 offset:25472
rt v[194:195],v240 offset:26496
mm v[36:39],v[200:203],v[136:139],v[36:39]
cv v251,v230,v231
aw a8,v248
mm v[36:39],v[204:207],v[156:159],v[36:39]
ld a[96:99],v246,s[52:55],s91 offen offset:2048 sc1
mm v[4:7],v[204:207],v[152:155],v[4:7]
wt lgkmcnt(4)
aw a9,v249
mm v[4:7],v[200:203],v[132:135],v[4:7]
rt v[204:205],v241 offset:17024
rt v[206:207],v241 offset:18048
mm v[8:11],v[208:211],v[132:135],v[8:11]
s_add_u32 m0, s83, 3200
aw a10,v250
mm v[8:11],v[212:215],v[152:155],v[8:11]
ld v235,s[44:47],s80 offen lds
mm v[40:43],v[212:215],v[156:159],v[40:43]
s_add_u32 s80, s80, 524288
aw a11,v251
mm v[40:43],v[208:211],v[136:139],v[40:43]
rt v[200:201],v241 offset:25472
rt v[202:203],v241 offset:26496
mm v[72:75],v[208:211],v[144:147],v[72:75]
aw a238,v236
v_subrev_u32 v236, 3, v239
mm v[72:75],v[212:215],a[120:123],v[72:75]
ld a[100:103],v247,s[52:55],s91 offen offset:2048 sc1
mm v[104:107],v[212:215],a[124:127],v[104:107]
v_ashrrev_i32 v236, 31, v236
v_and_b32 v236, 0xff800000, v236
mm v[104:107],v[208:211],v[148:151],v[104:107]
rt v[212:213],v242 offset:17024
rt v[214:215],v242 offset:18048
mm v[108:111],v[216:219],v[148:151],v[108:111]
aw a239,v236
v_lshlrev_b32 v236, 4, v239
mm v[108:111],v[220:223],a[124:127],v[108:111]
s_mov_b32 s92, 0x3f80
v_lshlrev_b32 v236, v236, s92
mm v[76:79],v[220:223],a[120:123],v[76:79]
v_cmp_gt_u32 vcc, 2, v239
v_subrev_u32 v239, 2, v239
mm v[76:79],v[216:219],v[144:147],v[76:79]
rt v[208:209],v242 offset:25472
rt v[210:211],v242 offset:26496
mm v[44:47],v[216:219],v[136:139],v[44:47]
ds_write_b128 v238, v[248:251] offset:5120
mm v[44:47],v[220:223],v[156:159],v[44:47]
st a[184:187],v255,s[48:51],s18 offen offset:1024 sc1
mm v[12:15],v[220:223],v[152:155],v[12:15]
wt lgkmcnt(5)
v_cmp_gt_u32 s[92:93], 2, v239
mm v[12:15],v[216:219],v[132:135],v[12:15]
rt v[220:221],v243 offset:17024
rt v[222:223],v243 offset:18048
mm v[16:19],v[196:199],v[132:135],v[16:19]
ar v224,a80
ar v225,a81
mm v[16:19],v[192:195],v[152:155],v[16:19]
ld a[104:107],v244,s[52:55],s91 offen offset:3072 sc1
mm v[48:51],v[192:195],v[156:159],v[48:51]
ar v226,a82
ar v227,a83
mm v[48:51],v[196:199],v[136:139],v[48:51]
rt v[216:217],v243 offset:25472
rt v[218:219],v243 offset:26496
mm v[80:83],v[196:199],v[144:147],v[80:83]
ar v228,a84
ar v229,a85
mm v[80:83],v[192:195],a[120:123],v[80:83]
ld a[108:111],v245,s[52:55],s91 offen offset:3072 sc1
mm v[112:115],v[192:195],a[124:127],v[112:115]
ar v230,a86
ar v231,a87
mm v[112:115],v[196:199],v[148:151],v[112:115]
cv v248,v224,v225
cv v249,v226,v227
mm v[116:119],v[204:207],v[148:151],v[116:119]
wt lgkmcnt(0)
s_barrier
mm v[116:119],v[200:203],a[124:127],v[116:119]
ld a[112:115],v246,s[52:55],s91 offen offset:3072 sc1
mm v[84:87],v[200:203],a[120:123],v[84:87]
rt v[192:193],v240 offset:33792
rt v[194:195],v240 offset:34816
mm v[84:87],v[204:207],v[144:147],v[84:87]
rt v[196:197],v240 offset:42240
rt v[198:199],v240 offset:43264
mm v[52:55],v[204:207],v[136:139],v[52:55]
s_add_u32 m0, s83, 16896
cv v250,v228,v229
mm v[52:55],v[200:203],v[156:159],v[52:55]
ld v232,s[44:47],s80 offen lds
mm v[20:23],v[200:203],v[152:155],v[20:23]
cv v251,v230,v231
aw a12,v248
mm v[20:23],v[204:207],v[132:135],v[20:23]
rt v[200:201],v241 offset:33792
rt v[202:203],v241 offset:34816
mm v[24:27],v[212:215],v[132:135],v[24:27]
aw a13,v249
aw a14,v250
mm v[24:27],v[208:211],v[152:155],v[24:27]
ld a[116:119],v247,s[52:55],s91 offen offset:3072 sc1
mm v[56:59],v[208:211],v[156:159],v[56:59]
s_add_u32 s91, s91, 4096
aw a15,v251
mm v[56:59],v[212:215],v[136:139],v[56:59]
rt v[204:205],v241 offset:42240
rt v[206:207],v241 offset:43264
mm v[88:91],v[212:215],v[144:147],v[88:91]
s_add_u32 m0, s83, 17920
v_cndmask_b32 v239, 0, v236, vcc
mm v[88:91],v[208:211],a[120:123],v[88:91]
ld v233,s[44:47],s80 offen lds
mm v[120:123],v[208:211],a[124:127],v[120:123]
aw a212,0
aw a214,v239
mm v[120:123],v[212:215],v[148:151],v[120:123]
rt v[208:209],v242 offset:33792
rt v[210:211],v242 offset:34816
mm v[124:127],v[220:223],v[148:151],v[124:127]
v_cndmask_b32 v239, 0, v236, s[92:93]
v_mov_b32 v236, 0x3f803f80
mm v[124:127],v[216:219],a[124:127],v[124:127]
aw a215,v239
aw a213,0
mm v[92:95],v[216:219],a[120:123],v[92:95]
aw a216,0
aw a217,0
mm v[92:95],v[220:223],v[144:147],v[92:95]
rt v[212:213],v242 offset:42240
rt v[214:215],v242 offset:43264
mm v[60:63],v[220:223],v[136:139],v[60:63]
v_lshrrev_b32 v239, 4, v255
v_xor_b32 v239, v239, v255
mm v[60:63],v[216:219],v[156:159],v[60:63]
st a[188:191],v255,s[48:51],s18 offen offset:2048 sc1
mm v[28:31],v[216:219],v[152:155],v[28:31]
wt vmcnt(15) lgkmcnt(4)
v_and_b32 v239, 32, v239
mm v[28:31],v[220:223],v[132:135],v[28:31]
rt v[216:217],v243 offset:33792
rt v[218:219],v243 offset:34816
mm v[0:3],v[192:195],v[160:163],v[0:3]
ar v224,a16
ar v225,a17
mm v[0:3],v[196:199],v[176:179],v[0:3]
ld v[132:135],v244,s[52:55],s91 offen offset:0 sc1
mm v[32:35],v[196:199],v[180:183],v[32:35]
ar v226,a18
ar v227,a19
mm v[32:35],v[192:195],v[164:167],v[32:35]
rt v[220:221],v243 offset:42240
rt v[222:223],v243 offset:43264
mm v[64:67],v[192:195],v[168:171],v[64:67]
ar v228,a20
ar v229,a21
mm v[64:67],v[196:199],v[184:187],v[64:67]
ld v[136:139],v245,s[52:55],s91 offen offset:0 sc1
mm v[96:99],v[196:199],v[188:191],v[96:99]
ar v230,a22
ar v231,a23
mm v[96:99],v[192:195],v[172:175],v[96:99]
rt v[196:197],v240 offset:33920
rt v[198:199],v240 offset:34944
mm v[100:103],v[200:203],v[172:175],v[100:103]
s_add_u32 m0, s83, 19072
cv v248,v224,v225
mm v[100:103],v[204:207],v[188:191],v[100:103]
ld v234,s[44:47],s80 offen lds
mm v[68:71],v[204:207],v[184:187],v[68:71]
cv v249,v226,v227
cv v250,v228,v229
mm v[68:71],v[200:203],v[168:171],v[68:71]
rt v[192:193],v240 offset:42368
rt v[194:195],v240 offset:43392
mm v[36:39],v[200:203],v[164:167],v[36:39]
cv v251,v230,v231
aw a16,v248
mm v[36:39],v[204:207],v[180:183],v[36:39]
ld v[144:147],v246,s[52:55],s91 offen offset:0 sc1
mm v[4:7],v[204:207],v[176:179],v[4:7]
wt lgkmcnt(4)
aw a17,v249
mm v[4:7],v[200:203],v[160:163],v[4:7]
rt v[204:205],v241 offset:33920
rt v[206:207],v241 offset:34944
mm v[8:11],v[208:211],v[160:163],v[8:11]
s_add_u32 m0, s83, 20096
aw a18,v250
mm v[8:11],v[212:215],v[176:179],v[8:11]
ld v235,s[44:47],s80 offen lds
mm v[40:43],v[212:215],v[180:183],v[40:43]
s_add_u32 s80, s80, 524288
aw a19,v251
mm v[40:43],v[208:211],v[164:167],v[40:43]
rt v[200:201],v241 offset:42368
rt v[202:203],v241 offset:43392
mm v[72:75],v[208:211],v[168:171],v[72:75]
v_cmp_eq_u32 vcc, 0, v239
s_mov_b32 s78, 4294950911
mm v[72:75],v[212:215],v[184:187],v[72:75]
ld v[148:151],v247,s[52:55],s91 offen offset:0 sc1
mm v[104:107],v[212:215],v[188:191],v[104:107]
s_mul_i32 s70, s70, s22
v_cndmask_b32 v236, 0, v236, vcc
mm v[104:107],v[208:211],v[172:175],v[104:107]
rt v[212:213],v242 offset:33920
rt v[214:215],v242 offset:34944
mm v[108:111],v[216:219],v[172:175],v[108:111]
s_sub_u32 s92, 4294967293, s3
s_add_u32 s92, s92, s7
mm v[108:111],v[220:223],v[188:191],v[108:111]
s_mul_i32 s92, s92, s21
s_add_u32 s90, s90, s92
mm v[76:79],v[220:223],v[184:187],v[76:79]
s_mov_b32 s17, 1073741824
s_lshl_b32 s92, s73, 2
mm v[76:79],v[216:219],v[168:171],v[76:79]
rt v[208:209],v242 offset:42368
rt v[210:211],v242 offset:43392
mm v[44:47],v[216:219],v[164:167],v[44:47]
s_lshr_b32 s92, 66051, s92
s_and_b32 s92, s92, 3
mm v[44:47],v[220:223],v[180:183],v[44:47]
st a[0:3],v255,s[48:51],s18 offen offset:3072 sc1
mm v[12:15],v[220:223],v[176:179],v[12:15]
wt lgkmcnt(4)
s_lshl_b32 s16, s92, 16
mm v[12:15],v[216:219],v[160:163],v[12:15]
rt v[220:221],v243 offset:33920
rt v[222:223],v243 offset:34944
mm v[16:19],v[196:199],v[160:163],v[16:19]
ar v224,a88
ar v225,a89
mm v[16:19],v[192:195],v[176:179],v[16:19]
ld v[152:155],v244,s[52:55],s91 offen offset:1024 sc1
mm v[48:51],v[192:195],v[180:183],v[48:51]
ar v226,a90
ar v227,a91
mm v[48:51],v[196:199],v[164:167],v[48:51]
rt v[216:217],v243 offset:42368
rt v[218:219],v243 offset:43392
mm v[80:83],v[196:199],v[168:171],v[80:83]
ar v228,a92
ar v229,a93
mm v[80:83],v[192:195],v[184:187],v[80:83]
ld v[156:159],v245,s[52:55],s91 offen offset:1024 sc1
mm v[112:115],v[192:195],v[188:191],v[112:115]
ar v230,a94
ar v231,a95
mm v[112:115],v[196:199],v[172:175],v[112:115]
cv v248,v224,v225
cv v249,v226,v227
mm v[116:119],v[204:207],v[172:175],v[116:119]
wt lgkmcnt(0)
s_barrier
mm v[116:119],v[200:203],v[188:191],v[116:119]
ld a[120:123],v246,s[52:55],s91 offen offset:1024 sc1
mm v[84:87],v[200:203],v[184:187],v[84:87]
rt v[192:193],v240 offset:50688
rt v[194:195],v240 offset:51712
mm v[84:87],v[204:207],v[168:171],v[84:87]
rt v[196:197],v240 offset:59136
rt v[198:199],v240 offset:60160
mm v[52:55],v[204:207],v[164:167],v[52:55]
s_add_u32 m0, s83, 33792
cv v250,v228,v229
mm v[52:55],v[200:203],v[180:183],v[52:55]
ld v232,s[44:47],s80 offen lds
mm v[20:23],v[200:203],v[176:179],v[20:23]
cv v251,v230,v231
aw a20,v248
mm v[20:23],v[204:207],v[160:163],v[20:23]
rt v[200:201],v241 offset:50688
rt v[202:203],v241 offset:51712
mm v[24:27],v[212:215],v[160:163],v[24:27]
aw a21,v249
aw a22,v250
mm v[24:27],v[208:211],v[176:179],v[24:27]
ld a[124:127],v247,s[52:55],s91 offen offset:1024 sc1
mm v[56:59],v[208:211],v[180:183],v[56:59]
aw a23,v251
s_mul_i32 s92, s75, 4294836224
mm v[56:59],v[212:215],v[164:167],v[56:59]
rt v[204:205],v241 offset:59136
rt v[206:207],v241 offset:60160
mm v[88:91],v[212:215],v[168:171],v[88:91]
s_add_u32 m0, s83, 34816
s_add_u32 s92, s92, s88
mm v[88:91],v[208:211],v[184:187],v[88:91]
ld v233,s[44:47],s80 offen lds
mm v[120:123],v[208:211],v[188:191],v[120:123]
s_add_u32 s19, s92, 393216
aw a208,v236
mm v[120:123],v[212:215],v[172:175],v[120:123]
rt v[208:209],v242 offset:50688
rt v[210:211],v242 offset:51712
mm v[124:127],v[220:223],v[172:175],v[124:127]
aw a209,v236
aw a210,v236
mm v[124:127],v[216:219],v[188:191],v[124:127]
aw a211,v236
aw a232,0
mm v[92:95],v[216:219],v[184:187],v[92:95]
s_mov_b32 s18, 20480
aw a233,0
mm v[92:95],v[220:223],v[168:171],v[92:95]
rt v[212:213],v242 offset:59136
rt v[214:215],v242 offset:60160
mm v[60:63],v[220:223],v[164:167],v[60:63]
aw a234,0
aw a235,0
mm v[60:63],v[216:219],v[180:183],v[60:63]
st a[4:7],v255,s[48:51],s18 offen offset:0 sc1
mm v[28:31],v[216:219],v[176:179],v[28:31]
wt vmcnt(16) lgkmcnt(4)
s_lshl_b32 s20, s72, 10
mm v[28:31],v[220:223],v[160:163],v[28:31]
rt v[216:217],v243 offset:50688
rt v[218:219],v243 offset:51712
mm v[0:3],v[192:195],v[128:131],v[0:3]
ar v224,a24
ar v225,a25
mm v[0:3],v[196:199],a[104:107],v[0:3]
ld v[160:163],v244,s[52:55],s91 offen offset:2048 sc1
mm v[32:35],v[196:199],a[108:111],v[32:35]
ar v226,a26
ar v227,a27
mm v[32:35],v[192:195],v[140:143],v[32:35]
rt v[220:221],v243 offset:59136
rt v[222:223],v243 offset:60160
mm v[64:67],v[192:195],a[96:99],v[64:67]
ar v228,a28
ar v229,a29
mm v[64:67],v[196:199],a[112:115],v[64:67]
ld v[164:167],v245,s[52:55],s91 offen offset:2048 sc1
mm v[96:99],v[196:199],a[116:119],v[96:99]
ar v230,a30
ar v231,a31
mm v[96:99],v[192:195],a[100:103],v[96:99]
rt v[196:197],v240 offset:50816
rt v[198:199],v240 offset:51840
mm v[100:103],v[200:203],a[100:103],v[100:103]
s_add_u32 m0, s83, 35968
cv v248,v224,v225
mm v[100:103],v[204:207],a[116:119],v[100:103]
ld v234,s[44:47],s80 offen lds
mm v[68:71],v[204:207],a[112:115],v[68:71]
cv v249,v226,v227
cv v250,v228,v229
mm v[68:71],v[200:203],a[96:99],v[68:71]
rt v[192:193],v240 offset:59264
rt v[194:195],v240 offset:60288
mm v[36:39],v[200:203],v[140:143],v[36:39]
cv v251,v230,v231
aw a24,v248
mm v[36:39],v[204:207],a[108:111],v[36:39]
ld v[168:171],v246,s[52:55],s91 offen offset:2048 sc1
mm v[4:7],v[204:207],a[104:107],v[4:7]
wt lgkmcnt(4)
aw a25,v249
mm v[4:7],v[200:203],v[128:131],v[4:7]
rt v[204:205],v241 offset:50816
rt v[206:207],v241 offset:51840
mm v[8:11],v[208:211],v[128:131],v[8:11]
s_add_u32 m0, s83, 36992
aw a26,v250
mm v[8:11],v[212:215],a[104:107],v[8:11]
ld v235,s[44:47],s80 offen lds
mm v[40:43],v[212:215],a[108:111],v[40:43]
s_add_u32 s80, s80, 524288
aw a27,v251
mm v[40:43],v[208:211],v[140:143],v[40:43]
rt v[200:201],v241 offset:59264
rt v[202:203],v241 offset:60288
mm v[72:75],v[208:211],a[96:99],v[72:75]
s_add_u32 s20, s20, 16384
s_mov_b32 s4, 1985221408
mm v[72:75],v[212:215],a[112:115],v[72:75]
ld v[172:175],v247,s[52:55],s91 offen offset:2048 sc1
mm v[104:107],v[212:215],a[116:119],v[104:107]
s_cmp_ge_u32 s2, 64
s_cselect_b32 s4, 1985225488, s4
mm v[104:107],v[208:211],a[100:103],v[104:107]
rt v[212:213],v242 offset:50816
rt v[214:215],v242 offset:51840
mm v[108:111],v[216:219],a[100:103],v[108:111]
s_cmp_ge_u32 s2, 128
s_cselect_b32 s4, 1985229328, s4
mm v[108:111],v[220:223],a[116:119],v[108:111]
s_cmp_ge_u32 s2, 192
s_cselect_b32 s4, 1985225008, s4
mm v[76:79],v[220:223],a[112:115],v[76:79]
v_bfe_u32 v236, v255, 4, 3
v_lshlrev_b32 v236, 2, v236
mm v[76:79],v[216:219],a[96:99],v[76:79]
rt v[208:209],v242 offset:59264
rt v[210:211],v242 offset:60288
mm v[44:47],v[216:219],v[140:143],v[44:47]
v_lshrrev_b32 v236, v236, s4
v_and_b32 v236, 15, v236
mm v[44:47],v[220:223],a[108:111],v[44:47]
st a[8:11],v255,s[48:51],s18 offen offset:1024 sc1
mm v[12:15],v[220:223],a[104:107],v[12:15]
wt lgkmcnt(4)
v_bfe_u32 v239, v255, 7, 1
mm v[12:15],v[216:219],v[128:131],v[12:15]
rt v[220:221],v243 offset:50816
rt v[222:223],v243 offset:51840
mm v[16:19],v[196:199],v[128:131],v[16:19]
ar v224,a240
ar v225,a241
mm v[16:19],v[192:195],a[104:107],v[16:19]
ld v[176:179],v244,s[52:55],s91 offen offset:3072 sc1
mm v[48:51],v[192:195],a[108:111],v[48:51]
ar v226,a242
ar v227,a243
mm v[48:51],v[196:199],v[140:143],v[48:51]
rt v[216:217],v243 offset:59264
rt v[218:219],v243 offset:60288
mm v[80:83],v[196:199],a[96:99],v[80:83]
ar v228,a244
ar v229,a245
mm v[80:83],v[192:195],a[112:115],v[80:83]
ld v[180:183],v245,s[52:55],s91 offen offset:3072 sc1
.La5_LW1_13:
s_cbranch_execz .La5_LW1_R0
mm v[112:115],v[192:195],a[116:119],v[112:115]
ar v230,a246
ar v231,a247
mm v[112:115],v[196:199],a[100:103],v[112:115]
cv v248,v224,v225
cv v249,v226,v227
mm v[116:119],v[204:207],a[100:103],v[116:119]
wt lgkmcnt(0)
s_barrier
mm v[116:119],v[200:203],a[116:119],v[116:119]
ld v[184:187],v246,s[52:55],s91 offen offset:3072 sc1
mm v[84:87],v[200:203],a[112:115],v[84:87]
rt v[192:193],v240 offset:0
rt v[194:195],v240 offset:1024
mm v[84:87],v[204:207],a[96:99],v[84:87]
rt v[196:197],v240 offset:8448
rt v[198:199],v240 offset:9472
mm v[52:55],v[204:207],v[140:143],v[52:55]
s_add_u32 m0, s83, 50688
cv v250,v228,v229
mm v[52:55],v[200:203],a[108:111],v[52:55]
ld v232,s[44:47],s80 offen lds
mm v[20:23],v[200:203],a[104:107],v[20:23]
cv v251,v230,v231
aw a28,v248
mm v[20:23],v[204:207],v[128:131],v[20:23]
rt v[200:201],v241 offset:0
rt v[202:203],v241 offset:1024
mm v[24:27],v[212:215],v[128:131],v[24:27]
aw a29,v249
aw a30,v250
mm v[24:27],v[208:211],a[104:107],v[24:27]
ld v[188:191],v247,s[52:55],s91 offen offset:3072 sc1
mm v[56:59],v[208:211],a[108:111],v[56:59]
s_add_u32 s91, s91, 4096
aw a31,v251
mm v[56:59],v[212:215],v[140:143],v[56:59]
rt v[204:205],v241 offset:8448
rt v[206:207],v241 offset:9472
mm v[88:91],v[212:215],a[96:99],v[88:91]
s_add_u32 m0, s83, 51712
v_mul_u32_u24 v239, 25, v239
mm v[88:91],v[208:211],a[112:115],v[88:91]
ld v233,s[44:47],s80 offen lds
mm v[120:123],v[208:211],a[116:119],v[120:123]
v_xor_b32 v236, v236, v239
v_xor_b32 v236, s72, v236
mm v[120:123],v[212:215],a[100:103],v[120:123]
rt v[208:209],v242 offset:0
rt v[210:211],v242 offset:1024
mm v[124:127],v[220:223],a[100:103],v[124:127]
v_lshlrev_b32 v236, 3, v236
aw a225,v236
mm v[124:127],v[216:219],a[116:119],v[124:127]
s_lshl3_add_u32 s5, s76, s101
s_lshl2_add_u32 s5, s5, s3
mm v[92:95],v[216:219],a[112:115],v[92:95]
s_lshl_b32 s5, s5, 8
s_add_u32 s5, s5, 1048576
mm v[92:95],v[220:223],a[96:99],v[92:95]
rt v[212:213],v242 offset:8448
rt v[214:215],v242 offset:9472
mm v[60:63],v[220:223],v[140:143],v[60:63]
mm v[60:63],v[216:219],a[108:111],v[60:63]
mm v[28:31],v[216:219],a[104:107],v[28:31]
wt vmcnt(15) lgkmcnt(4)
mm v[28:31],v[220:223],v[128:131],v[28:31]
rt v[216:217],v243 offset:0
rt v[218:219],v243 offset:1024
mm v[0:3],v[192:195],v[132:135],v[0:3]
ar v224,a32
ar v225,a33
mm v[0:3],v[196:199],v[152:155],v[0:3]
ld v[128:131],v244,s[52:55],s91 offen offset:0 sc1
mm v[32:35],v[196:199],v[156:159],v[32:35]
ar v226,a34
ar v227,a35
mm v[32:35],v[192:195],v[136:139],v[32:35]
rt v[220:221],v243 offset:8448
rt v[222:223],v243 offset:9472
mm v[64:67],v[192:195],v[144:147],v[64:67]
ar v228,a36
ar v229,a37
mm v[64:67],v[196:199],a[120:123],v[64:67]
ld v[140:143],v245,s[52:55],s91 offen offset:0 sc1
mm v[96:99],v[196:199],a[124:127],v[96:99]
ar v230,a38
ar v231,a39
mm v[96:99],v[192:195],v[148:151],v[96:99]
rt v[196:197],v240 offset:128
rt v[198:199],v240 offset:1152
mm v[100:103],v[200:203],v[148:151],v[100:103]
s_add_u32 m0, s83, 52864
cv v248,v224,v225
mm v[100:103],v[204:207],a[124:127],v[100:103]
ld v234,s[44:47],s80 offen lds
mm v[68:71],v[204:207],a[120:123],v[68:71]
cv v249,v226,v227
cv v250,v228,v229
mm v[68:71],v[200:203],v[144:147],v[68:71]
rt v[192:193],v240 offset:8576
rt v[194:195],v240 offset:9600
mm v[36:39],v[200:203],v[136:139],v[36:39]
cv v251,v230,v231
aw a32,v248
mm v[36:39],v[204:207],v[156:159],v[36:39]
ld a[192:195],v246,s[52:55],s91 offen offset:0 sc1
mm v[4:7],v[204:207],v[152:155],v[4:7]
wt lgkmcnt(4)
aw a33,v249
mm v[4:7],v[200:203],v[132:135],v[4:7]
rt v[204:205],v241 offset:128
rt v[206:207],v241 offset:1152
mm v[8:11],v[208:211],v[132:135],v[8:11]
s_add_u32 m0, s83, 53888
aw a34,v250
mm v[8:11],v[212:215],v[152:155],v[8:11]
ld v235,s[44:47],s80 offen lds
mm v[40:43],v[212:215],v[156:159],v[40:43]
aw a35,v251
mm v[40:43],v[208:211],v[136:139],v[40:43]
rt v[200:201],v241 offset:8576
rt v[202:203],v241 offset:9600
mm v[72:75],v[208:211],v[144:147],v[72:75]
mm v[72:75],v[212:215],a[120:123],v[72:75]
ld a[196:199],v247,s[52:55],s91 offen offset:0 sc1
mm v[104:107],v[212:215],a[124:127],v[104:107]
mm v[104:107],v[208:211],v[148:151],v[104:107]
rt v[212:213],v242 offset:128
rt v[214:215],v242 offset:1152
mm v[108:111],v[216:219],v[148:151],v[108:111]
mm v[108:111],v[220:223],a[124:127],v[108:111]
mm v[76:79],v[220:223],a[120:123],v[76:79]
mm v[76:79],v[216:219],v[144:147],v[76:79]
rt v[208:209],v242 offset:8576
rt v[210:211],v242 offset:9600
mm v[44:47],v[216:219],v[136:139],v[44:47]
mm v[44:47],v[220:223],v[156:159],v[44:47]
st a[12:15],v255,s[48:51],s18 offen offset:2048 sc1
mm v[12:15],v[220:223],v[152:155],v[12:15]
wt lgkmcnt(4)
mm v[12:15],v[216:219],v[132:135],v[12:15]
rt v[220:221],v243 offset:128
rt v[222:223],v243 offset:1152
mm v[16:19],v[196:199],v[132:135],v[16:19]
ar v224,a248
ar v225,a249
mm v[16:19],v[192:195],v[152:155],v[16:19]
ld a[200:203],v244,s[52:55],s91 offen offset:1024 sc1
mm v[48:51],v[192:195],v[156:159],v[48:51]
ar v226,a250
ar v227,a251
mm v[48:51],v[196:199],v[136:139],v[48:51]
rt v[216:217],v243 offset:8576
rt v[218:219],v243 offset:9600
mm v[80:83],v[196:199],v[144:147],v[80:83]
ar v228,a252
ar v229,a253
mm v[80:83],v[192:195],a[120:123],v[80:83]
ld a[204:207],v245,s[52:55],s91 offen offset:1024 sc1
mm v[112:115],v[192:195],a[124:127],v[112:115]
ar v230,a254
ar v231,a255
mm v[112:115],v[196:199],v[148:151],v[112:115]
cv v248,v224,v225
cv v249,v226,v227
mm v[116:119],v[204:207],v[148:151],v[116:119]
wt lgkmcnt(0)
s_barrier
mm v[116:119],v[200:203],a[124:127],v[116:119]
cv v250,v228,v229
cv v251,v230,v231
mm v[84:87],v[200:203],a[120:123],v[84:87]
rt v[192:193],v240 offset:16896
rt v[194:195],v240 offset:17920
mm v[84:87],v[204:207],v[144:147],v[84:87]
rt v[196:197],v240 offset:25344
rt v[198:199],v240 offset:26368
mm v[52:55],v[204:207],v[136:139],v[52:55]
aw a36,v248
aw a37,v249
mm v[52:55],v[200:203],v[156:159],v[52:55]
aw a38,v250
aw a39,v251
mm v[20:23],v[200:203],v[152:155],v[20:23]
s_getpc_b64 s[92:93]
.La5_LCT2:
s_and_b32 s93, s93, 0xffff
mm v[20:23],v[204:207],v[132:135],v[20:23]
rt v[200:201],v241 offset:16896
rt v[202:203],v241 offset:17920
mm v[24:27],v[212:215],v[132:135],v[24:27]
s_lshr_b32 s94, s2, 3
s_lshl2_add_u32 s94, s94, s3
mm v[24:27],v[208:211],v[152:155],v[24:27]
s_lshl_b32 s94, s94, 10
v_lshrrev_b32 v233, 7, v255
mm v[56:59],v[208:211],v[156:159],v[56:59]
v_lshlrev_b32 v232, 3, v255
v_lshl_add_u32 v232, v233, 28, v232
mm v[56:59],v[212:215],v[136:139],v[56:59]
rt v[204:205],v241 offset:25344
rt v[206:207],v241 offset:26368
mm v[88:91],v[212:215],v[144:147],v[88:91]
v_add_u32 v232, s94, v232
s_mov_b32 s94, .La5_LCODEEND-.La5_LCT2
mm v[88:91],v[208:211],a[120:123],v[88:91]
s_mov_b32 s95, 0x20000
buffer_load_dword a253, v232, s[92:95], 0 offen
mm v[120:123],v[208:211],a[124:127],v[120:123]
s_mov_b32 s80, 8192
s_add_u32 s83, s83, 4294886912
mm v[120:123],v[212:215],v[148:151],v[120:123]
rt v[208:209],v242 offset:16896
rt v[210:211],v242 offset:17920
mm v[124:127],v[220:223],v[148:151],v[124:127]
s_lshl_b32 s92, s76, 21
s_add_u32 s92, s92, 54525952
mm v[124:127],v[216:219],a[124:127],v[124:127]
ld v[148:151],v246,s[52:55],s91 offen offset:1024 sc1
mm v[92:95],v[216:219],a[120:123],v[92:95]
s_add_u32 s44, s26, s92
s_addc_u32 s45, s27, 0
mm v[92:95],v[220:223],v[144:147],v[92:95]
rt v[212:213],v242 offset:25344
rt v[214:215],v242 offset:26368
mm v[60:63],v[220:223],v[136:139],v[60:63]
ds_write_b128 v238, a[12:15] offset:6144
mm v[60:63],v[216:219],v[156:159],v[60:63]
st a[16:19],v255,s[48:51],s18 offen offset:3072 sc1
mm v[28:31],v[216:219],v[152:155],v[28:31]
wt vmcnt(13) lgkmcnt(5)
s_and_b32 s45, s45, 0xffff
mm v[28:31],v[220:223],v[132:135],v[28:31]
rt v[216:217],v243 offset:16896
rt v[218:219],v243 offset:17920
mm v[0:3],v[192:195],v[160:163],v[0:3]
ar v224,a40
ar v225,a41
mm v[0:3],v[196:199],v[176:179],v[0:3]
ld v[132:135],v247,s[52:55],s91 offen offset:1024 sc1
mm v[32:35],v[196:199],v[180:183],v[32:35]
ar v226,a42
ar v227,a43
mm v[32:35],v[192:195],v[164:167],v[32:35]
rt v[220:221],v243 offset:25344
rt v[222:223],v243 offset:26368
mm v[64:67],v[192:195],v[168:171],v[64:67]
ar v228,a44
ar v229,a45
mm v[64:67],v[196:199],v[184:187],v[64:67]
ld a[96:99],v244,s[52:55],s91 offen offset:2048 sc1
mm v[96:99],v[196:199],v[188:191],v[96:99]
ar v230,a46
ar v231,a47
mm v[96:99],v[192:195],v[172:175],v[96:99]
rt v[196:197],v240 offset:17024
rt v[198:199],v240 offset:18048
mm v[100:103],v[200:203],v[172:175],v[100:103]
cv v248,v224,v225
cv v249,v226,v227
mm v[100:103],v[204:207],v[188:191],v[100:103]
ld a[100:103],v245,s[52:55],s91 offen offset:2048 sc1
mm v[68:71],v[204:207],v[184:187],v[68:71]
cv v250,v228,v229
cv v251,v230,v231
mm v[68:71],v[200:203],v[168:171],v[68:71]
rt v[192:193],v240 offset:25472
rt v[194:195],v240 offset:26496
mm v[36:39],v[200:203],v[164:167],v[36:39]
aw a40,v248
aw a41,v249
mm v[36:39],v[204:207],v[180:183],v[36:39]
ld a[104:107],v246,s[52:55],s91 offen offset:2048 sc1
mm v[4:7],v[204:207],v[176:179],v[4:7]
wt lgkmcnt(4)
aw a42,v250
mm v[4:7],v[200:203],v[160:163],v[4:7]
rt v[204:205],v241 offset:17024
rt v[206:207],v241 offset:18048
mm v[8:11],v[208:211],v[160:163],v[8:11]
aw a43,v251
s_mov_b32 s46, 2097536
mm v[8:11],v[212:215],v[176:179],v[8:11]
ld a[108:111],v247,s[52:55],s91 offen offset:2048 sc1
mm v[40:43],v[212:215],v[180:183],v[40:43]
s_mov_b32 s95, 4096
s_mov_b32 s96, 8192
mm v[40:43],v[208:211],v[164:167],v[40:43]
rt v[200:201],v241 offset:25472
rt v[202:203],v241 offset:26496
mm v[72:75],v[208:211],v[168:171],v[72:75]
s_mov_b32 s97, 12288
s_mul_i32 s92, s3, 196608
mm v[72:75],v[212:215],v[184:187],v[72:75]
s_sub_u32 s93, 1376256, s92
mm v[104:107],v[212:215],v[188:191],v[104:107]
s_mov_b32 s18, 0
mm v[104:107],v[208:211],v[172:175],v[104:107]
rt v[212:213],v242 offset:17024
rt v[214:215],v242 offset:18048
mm v[108:111],v[216:219],v[172:175],v[108:111]
mm v[108:111],v[220:223],v[188:191],v[108:111]
st a[20:23],v255,s[48:51],s18 offen offset:0 sc1
mm v[76:79],v[220:223],v[184:187],v[76:79]
mm v[76:79],v[216:219],v[168:171],v[76:79]
rt v[208:209],v242 offset:25472
rt v[210:211],v242 offset:26496
mm v[44:47],v[216:219],v[164:167],v[44:47]
ds_write_b128 v238, a[16:19] offset:7168
mm v[44:47],v[220:223],v[180:183],v[44:47]
st a[24:27],v255,s[48:51],s18 offen offset:1024 sc1
mm v[12:15],v[220:223],v[176:179],v[12:15]
wt lgkmcnt(5)
mm v[12:15],v[216:219],v[160:163],v[12:15]
rt v[220:221],v243 offset:17024
rt v[222:223],v243 offset:18048
mm v[16:19],v[196:199],v[160:163],v[16:19]
ar v224,a48
ar v225,a49
mm v[16:19],v[192:195],v[176:179],v[16:19]
ld a[112:115],v244,s[52:55],s91 offen offset:3072 sc1
mm v[48:51],v[192:195],v[180:183],v[48:51]
ar v226,a50
ar v227,a51
mm v[48:51],v[196:199],v[164:167],v[48:51]
rt v[216:217],v243 offset:25472
rt v[218:219],v243 offset:26496
mm v[80:83],v[196:199],v[168:171],v[80:83]
ar v228,a52
ar v229,a53
mm v[80:83],v[192:195],v[184:187],v[80:83]
ld a[116:119],v245,s[52:55],s91 offen offset:3072 sc1
mm v[112:115],v[192:195],v[188:191],v[112:115]
ar v230,a54
ar v231,a55
mm v[112:115],v[196:199],v[172:175],v[112:115]
cv v248,v224,v225
cv v249,v226,v227
mm v[116:119],v[204:207],v[172:175],v[116:119]
wt lgkmcnt(0)
s_barrier
mm v[116:119],v[200:203],v[188:191],v[116:119]
ld a[120:123],v246,s[52:55],s91 offen offset:3072 sc1
mm v[84:87],v[200:203],v[184:187],v[84:87]
rt v[192:193],v240 offset:33792
rt v[194:195],v240 offset:34816
mm v[84:87],v[204:207],v[168:171],v[84:87]
rt v[196:197],v240 offset:42240
rt v[198:199],v240 offset:43264
mm v[52:55],v[204:207],v[164:167],v[52:55]
ds_write_b128 v238, a[20:23] offset:32768
mm v[52:55],v[200:203],v[180:183],v[52:55]
ld a[124:127],v247,s[52:55],s91 offen offset:3072 sc1
mm v[20:23],v[200:203],v[176:179],v[20:23]
cv v250,v228,v229
cv v251,v230,v231
mm v[20:23],v[204:207],v[160:163],v[20:23]
rt v[200:201],v241 offset:33792
rt v[202:203],v241 offset:34816
mm v[24:27],v[212:215],v[160:163],v[24:27]
aw a44,v248
aw a45,v249
mm v[24:27],v[208:211],v[176:179],v[24:27]
aw a46,v250
aw a47,v251
mm v[56:59],v[208:211],v[180:183],v[56:59]
mm v[56:59],v[212:215],v[164:167],v[56:59]
rt v[204:205],v241 offset:42240
rt v[206:207],v241 offset:43264
mm v[88:91],v[212:215],v[168:171],v[88:91]
mm v[88:91],v[208:211],v[184:187],v[88:91]
mm v[120:123],v[208:211],v[188:191],v[120:123]
mm v[120:123],v[212:215],v[172:175],v[120:123]
rt v[208:209],v242 offset:33792
rt v[210:211],v242 offset:34816
mm v[124:127],v[220:223],v[172:175],v[124:127]
mm v[124:127],v[216:219],v[188:191],v[124:127]
st a[28:31],v255,s[48:51],s18 offen offset:2048 sc1
mm v[92:95],v[216:219],v[184:187],v[92:95]
mm v[92:95],v[220:223],v[168:171],v[92:95]
rt v[212:213],v242 offset:42240
rt v[214:215],v242 offset:43264
mm v[60:63],v[220:223],v[164:167],v[60:63]
ds_write_b128 v238, a[24:27] offset:33792
mm v[60:63],v[216:219],v[180:183],v[60:63]
st a[32:35],v255,s[48:51],s18 offen offset:3072 sc1
mm v[28:31],v[216:219],v[176:179],v[28:31]
wt vmcnt(12) lgkmcnt(5)
mm v[28:31],v[220:223],v[160:163],v[28:31]
rt v[216:217],v243 offset:33792
rt v[218:219],v243 offset:34816
mm v[0:3],v[192:195],v[128:131],v[0:3]
ar v224,a56
ar v225,a57
mm v[0:3],v[196:199],a[200:203],v[0:3]
ar v226,a58
ar v227,a59
mm v[32:35],v[196:199],a[204:207],v[32:35]
ar v228,a60
ar v229,a61
mm v[32:35],v[192:195],v[140:143],v[32:35]
rt v[220:221],v243 offset:42240
rt v[222:223],v243 offset:43264
mm v[64:67],v[192:195],a[192:195],v[64:67]
ar v230,a62
ar v231,a63
mm v[64:67],v[196:199],v[148:151],v[64:67]
cv v248,v224,v225
cv v249,v226,v227
mm v[96:99],v[196:199],v[132:135],v[96:99]
cv v250,v228,v229
cv v251,v230,v231
mm v[96:99],v[192:195],a[196:199],v[96:99]
rt v[196:197],v240 offset:33920
rt v[198:199],v240 offset:34944
mm v[100:103],v[200:203],a[196:199],v[100:103]
aw a48,v248
aw a49,v249
mm v[100:103],v[204:207],v[132:135],v[100:103]
aw a50,v250
aw a51,v251
mm v[68:71],v[204:207],v[148:151],v[68:71]
v_add_u32 v244, s92, v244
v_add_u32 v246, s93, v246
mm v[68:71],v[200:203],a[192:195],v[68:71]
rt v[192:193],v240 offset:42368
rt v[194:195],v240 offset:43392
mm v[36:39],v[200:203],v[140:143],v[36:39]
ds_write_b128 v238, a[28:31] offset:34816
mm v[36:39],v[204:207],a[204:207],v[36:39]
v_add_u32 v245, 0x1000, v244
v_add_u32 v247, 0x1000, v246
mm v[4:7],v[204:207],a[200:203],v[4:7]
wt lgkmcnt(5)
s_lshl_b32 s91, s72, 13
mm v[4:7],v[200:203],v[128:131],v[4:7]
rt v[204:205],v241 offset:33920
rt v[206:207],v241 offset:34944
mm v[8:11],v[208:211],v[128:131],v[8:11]
s_lshl4_add_u32 s92, s76, s3
s_lshl3_add_u32 s92, s92, s20
mm v[8:11],v[212:215],a[200:203],v[8:11]
s_bitset1_b32 s20, 30
mm v[40:43],v[212:215],a[204:207],v[40:43]
mm v[40:43],v[208:211],v[140:143],v[40:43]
rt v[200:201],v241 offset:42368
rt v[202:203],v241 offset:43392
mm v[72:75],v[208:211],a[192:195],v[72:75]
mm v[72:75],v[212:215],v[148:151],v[72:75]
mm v[104:107],v[212:215],v[132:135],v[104:107]
s_mov_b32 s18, 4096
mm v[104:107],v[208:211],a[196:199],v[104:107]
rt v[212:213],v242 offset:33920
rt v[214:215],v242 offset:34944
mm v[108:111],v[216:219],a[196:199],v[108:111]
mm v[108:111],v[220:223],v[132:135],v[108:111]
st a[36:39],v255,s[48:51],s18 offen offset:0 sc1
mm v[76:79],v[220:223],v[148:151],v[76:79]
mm v[76:79],v[216:219],a[192:195],v[76:79]
rt v[208:209],v242 offset:42368
rt v[210:211],v242 offset:43392
mm v[44:47],v[216:219],v[140:143],v[44:47]
ds_write_b128 v238, a[32:35] offset:35840
mm v[44:47],v[220:223],a[204:207],v[44:47]
st a[40:43],v255,s[48:51],s18 offen offset:1024 sc1
mm v[12:15],v[220:223],a[200:203],v[12:15]
wt lgkmcnt(5)
mm v[12:15],v[216:219],v[128:131],v[12:15]
rt v[220:221],v243 offset:33920
rt v[222:223],v243 offset:34944
mm v[16:19],v[196:199],v[128:131],v[16:19]
s_mov_b64 s[52:53], s[86:87]
mm v[16:19],v[192:195],a[200:203],v[16:19]
s_bitcmp1_b32 21, s73
s_cselect_b32 s54, 16384, 0
s_cselect_b32 s86, s88, 1073741824
mm v[48:51],v[192:195],a[204:207],v[48:51]
s_lshl_b32 s89, s3, 14
v_add_u32 v248, s89, v255
mm v[48:51],v[196:199],v[140:143],v[48:51]
rt v[216:217],v243 offset:42368
rt v[218:219],v243 offset:43392
mm v[80:83],v[196:199],a[192:195],v[80:83]
ds_write_b128 v238, a[36:39] offset:36864
mm v[80:83],v[192:195],v[148:151],v[80:83]
s_add_u32 s86, s86, 262144
mm v[112:115],v[192:195],v[132:135],v[112:115]
s_cmp_eq_u32 s73, 0
s_cselect_b32 s86, s92, s86
s_cselect_b32 s92, 1073741824, s92
mm v[112:115],v[196:199],a[196:199],v[112:115]
s_mov_b32 s87, s92
s_add_u32 s89, s89, 16384
mm v[116:119],v[204:207],a[196:199],v[116:119]
wt lgkmcnt(1)
s_barrier
mm v[116:119],v[200:203],v[132:135],v[116:119]
s_lshl_b32 m0, s3, 12
s_add_u32 m0, m0, 131072
mm v[84:87],v[200:203],v[148:151],v[84:87]
rt v[192:193],v240 offset:50688
rt v[194:195],v240 offset:51712
mm v[84:87],v[204:207],a[192:195],v[84:87]
rt v[196:197],v240 offset:59136
rt v[198:199],v240 offset:60160
mm v[52:55],v[204:207],v[140:143],v[52:55]
ds_write_b128 v238, a[40:43] offset:37888
mm v[52:55],v[200:203],a[204:207],v[52:55]
v_bfe_u32 v251, v255, 2, 5
v_and_b32 v224, 0x380, v255
mm v[20:23],v[200:203],a[200:203],v[20:23]
v_add_u32 v251, v251, v224
v_and_b32 v224, 16, v255
mm v[20:23],v[204:207],v[128:131],v[20:23]
rt v[200:201],v241 offset:50688
rt v[202:203],v241 offset:51712
mm v[24:27],v[212:215],v[128:131],v[24:27]
v_cmp_eq_u32 vcc, 0, v224
mm v[24:27],v[208:211],a[200:203],v[24:27]
v_cndmask_b32 v249, v253, v252, vcc
mm v[56:59],v[208:211],a[204:207],v[56:59]
mm v[56:59],v[212:215],v[140:143],v[56:59]
rt v[204:205],v241 offset:59136
rt v[206:207],v241 offset:60160
mm v[88:91],v[212:215],a[192:195],v[88:91]
ds_write_b128 v238, a[44:47] offset:38912
mm v[88:91],v[208:211],v[148:151],v[88:91]
st a[44:47],v255,s[48:51],s18 offen offset:2048 sc1
mm v[120:123],v[208:211],v[132:135],v[120:123]
mm v[120:123],v[212:215],a[196:199],v[120:123]
rt v[208:209],v242 offset:50688
rt v[210:211],v242 offset:51712
mm v[124:127],v[220:223],a[196:199],v[124:127]
mm v[124:127],v[216:219],v[132:135],v[124:127]
mm v[92:95],v[216:219],v[148:151],v[92:95]
mm v[92:95],v[220:223],a[192:195],v[92:95]
rt v[212:213],v242 offset:59136
rt v[214:215],v242 offset:60160
mm v[60:63],v[220:223],v[140:143],v[60:63]
.La5_LW1_14:
s_cbranch_execz .La5_LW1_R0
mm v[60:63],v[216:219],a[204:207],v[60:63]
st a[48:51],v255,s[48:51],s18 offen offset:3072 sc1
mm v[28:31],v[216:219],a[200:203],v[28:31]
wt vmcnt(6) lgkmcnt(5)
mm v[28:31],v[220:223],v[128:131],v[28:31]
rt v[216:217],v243 offset:50688
rt v[218:219],v243 offset:51712
mm v[0:3],v[192:195],a[96:99],v[0:3]
mm v[0:3],v[196:199],a[112:115],v[0:3]
mm v[32:35],v[196:199],a[116:119],v[32:35]
mm v[32:35],v[192:195],a[100:103],v[32:35]
rt v[220:221],v243 offset:59136
rt v[222:223],v243 offset:60160
mm v[64:67],v[192:195],a[104:107],v[64:67]
mm v[64:67],v[196:199],a[120:123],v[64:67]
mm v[96:99],v[196:199],a[124:127],v[96:99]
mm v[96:99],v[192:195],a[108:111],v[96:99]
rt v[196:197],v240 offset:50816
rt v[198:199],v240 offset:51840
mm v[100:103],v[200:203],a[108:111],v[100:103]
mm v[100:103],v[204:207],a[124:127],v[100:103]
mm v[68:71],v[204:207],a[120:123],v[68:71]
mm v[68:71],v[200:203],a[104:107],v[68:71]
rt v[192:193],v240 offset:59264
rt v[194:195],v240 offset:60288
mm v[36:39],v[200:203],a[100:103],v[36:39]
mm v[36:39],v[204:207],a[116:119],v[36:39]
mm v[4:7],v[204:207],a[112:115],v[4:7]
wt lgkmcnt(4)
mm v[4:7],v[200:203],a[96:99],v[4:7]
rt v[204:205],v241 offset:50816
rt v[206:207],v241 offset:51840
mm v[8:11],v[208:211],a[96:99],v[8:11]
mm v[8:11],v[212:215],a[112:115],v[8:11]
mm v[40:43],v[212:215],a[116:119],v[40:43]
cv v128,v0,v1
cv v129,v2,v3
mm v[40:43],v[208:211],a[100:103],v[40:43]
rt v[200:201],v241 offset:59264
rt v[202:203],v241 offset:60288
mm v[72:75],v[208:211],a[104:107],v[72:75]
cv v130,v4,v5
cv v131,v6,v7
mm v[72:75],v[212:215],a[120:123],v[72:75]
cv v144,v32,v33
cv v145,v34,v35
mm v[104:107],v[212:215],a[124:127],v[104:107]
cv v146,v36,v37
cv v147,v38,v39
mm v[104:107],v[208:211],a[108:111],v[104:107]
rt v[212:213],v242 offset:50816
rt v[214:215],v242 offset:51840
mm v[108:111],v[216:219],a[108:111],v[108:111]
cv v160,v64,v65
cv v161,v66,v67
mm v[108:111],v[220:223],a[124:127],v[108:111]
cv v162,v68,v69
cv v163,v70,v71
mm v[76:79],v[220:223],a[120:123],v[76:79]
cv v176,v96,v97
cv v177,v98,v99
mm v[76:79],v[216:219],a[104:107],v[76:79]
rt v[208:209],v242 offset:59264
rt v[210:211],v242 offset:60288
mm v[44:47],v[216:219],a[100:103],v[44:47]
cv v178,v100,v101
cv v179,v102,v103
mm v[44:47],v[220:223],a[116:119],v[44:47]
mm v[12:15],v[220:223],a[112:115],v[12:15]
wt lgkmcnt(4)
mm v[12:15],v[216:219],a[96:99],v[12:15]
rt v[220:221],v243 offset:50816
rt v[222:223],v243 offset:51840
mm v[16:19],v[196:199],a[96:99],v[16:19]
v_mul_u32_u24 v241, 0x5555, v224
v_lshrrev_b32 v241, 4, v241
v_add_u32 v241, 0x8888, v241
mm v[16:19],v[192:195],a[112:115],v[16:19]
st v[128:131],v255,s[52:55],0 offen offset:0 sc1
mm v[48:51],v[192:195],a[116:119],v[48:51]
cv v132,v8,v9
cv v133,v10,v11
cv v181,v106,v107
mm v[48:51],v[196:199],a[100:103],v[48:51]
rt v[216:217],v243 offset:59264
rt v[218:219],v243 offset:60288
cv v182,v108,v109
mm v[80:83],v[196:199],a[104:107],v[80:83]
cv v134,v12,v13
cv v135,v14,v15
cv v183,v110,v111
mm v[80:83],v[192:195],a[120:123],v[80:83]
st v[144:147],v255,s[52:55],s95 offen offset:0 sc1
ar v34,a238
mm v[112:115],v[192:195],a[124:127],v[112:115]
cv v148,v40,v41
cv v149,v42,v43
ar v35,a239
mm v[112:115],v[196:199],a[108:111],v[112:115]
cv v150,v44,v45
cv v151,v46,v47
v_mov_b32 v36, 0xff800000
mm v[116:119],v[204:207],a[108:111],v[116:119]
wt lgkmcnt(0)
s_barrier
v_mov_b32 v37, 0xff800000
mm v[116:119],v[200:203],a[124:127],v[116:119]
st v[160:163],v255,s[52:55],s96 offen offset:0 sc1
v_mov_b32 v38, 0xff800000
mm v[84:87],v[200:203],a[120:123],v[84:87]
cv v164,v72,v73
cv v165,v74,v75
v_mov_b32 v39, 0xff800000
mm v[84:87],v[204:207],a[104:107],v[84:87]
rd a[64:67],v248 offset:0
rd a[68:71],v248 offset:1024
mm v[52:55],v[204:207],a[100:103],v[52:55]
cv v166,v76,v77
cv v167,v78,v79
mm v[52:55],v[200:203],a[116:119],v[52:55]
st v[176:179],v255,s[52:55],s97 offen offset:0 sc1
mm v[20:23],v[200:203],a[112:115],v[20:23]
s_nop 0
cv v180,v104,v105
mm v[20:23],v[204:207],a[96:99],v[20:23]
rd a[72:75],v248 offset:2048
rd a[76:79],v248 offset:3072
mm v[24:27],v[212:215],a[96:99],v[24:27]
ar v32,a236
ar v33,a237
mm v[24:27],v[208:211],a[112:115],v[24:27]
st v[132:135],v255,s[52:55],0 offen offset:1024 sc1
mm v[56:59],v[208:211],a[116:119],v[56:59]
cv v136,v16,v17
cv v137,v18,v19
mm v[56:59],v[212:215],a[100:103],v[56:59]
rd a[80:83],v248 offset:4096
rd a[84:87],v248 offset:5120
mm v[88:91],v[212:215],a[104:107],v[88:91]
cv v138,v20,v21
cv v139,v22,v23
mm v[88:91],v[208:211],a[120:123],v[88:91]
st v[148:151],v255,s[52:55],s95 offen offset:1024 sc1
mm v[120:123],v[208:211],a[124:127],v[120:123]
cv v152,v48,v49
cv v153,v50,v51
mm v[120:123],v[212:215],a[108:111],v[120:123]
rd a[88:91],v248 offset:6144
rd a[92:95],v248 offset:7168
mm v[124:127],v[220:223],a[108:111],v[124:127]
ds_write_b128 v238, a[48:51] offset:39936
mm v[124:127],v[216:219],a[124:127],v[124:127]
st v[164:167],v255,s[52:55],s96 offen offset:1024 sc1
mm v[92:95],v[216:219],a[120:123],v[92:95]
cv v154,v52,v53
cv v155,v54,v55
mm v[92:95],v[220:223],a[104:107],v[92:95]
cv v168,v80,v81
cv v169,v82,v83
mm v[60:63],v[220:223],a[100:103],v[60:63]
cv v170,v84,v85
cv v171,v86,v87
mm v[60:63],v[216:219],a[116:119],v[60:63]
st v[180:183],v255,s[52:55],s97 offen offset:1024 sc1
mm v[28:31],v[216:219],a[112:115],v[28:31]
cv v184,v112,v113
cv v185,v114,v115
mm v[28:31],v[220:223],a[96:99],v[28:31]
cv v186,v116,v117
cv v187,v118,v119
wt lgkmcnt(1)
mm v[208:211],a[64:67],v[160:163],v[32:35]
mm v[216:219],a[64:67],v[176:179],0
st v[136:139],v255,s[52:55],0 offen offset:2048 sc1
mm v[212:215],a[80:83],v[160:163],v[36:39]
cv v172,v88,v89
cv v173,v90,v91
cv v174,v92,v93
mm v[220:223],a[80:83],v[176:179],v[32:35]
cv v175,v94,v95
cv v188,v120,v121
cv v189,v122,v123
mm v[208:211],a[68:71],v[164:167],v[208:211]
cv v190,v124,v125
cv v191,v126,v127
cv v140,v24,v25
mm v[216:219],a[68:71],v[180:183],v[216:219]
st v[152:155],v255,s[52:55],s95 offen offset:2048 sc1
cv v141,v26,v27
mm v[212:215],a[84:87],v[164:167],v[212:215]
cv v142,v28,v29
cv v143,v30,v31
cv v156,v56,v57
mm v[220:223],a[84:87],v[180:183],v[220:223]
cv v157,v58,v59
cv v158,v60,v61
cv v159,v62,v63
mm v[208:211],a[72:75],v[168:171],v[208:211]
mm v[216:219],a[72:75],v[184:187],v[216:219]
st v[168:171],v255,s[52:55],s96 offen offset:2048 sc1
mm v[212:215],a[88:91],v[168:171],v[212:215]
mm v[220:223],a[88:91],v[184:187],v[220:223]
mm v[208:211],a[76:79],v[172:175],v[208:211]
mm v[216:219],a[76:79],v[188:191],v[216:219]
st v[184:187],v255,s[52:55],s97 offen offset:2048 sc1
mm v[212:215],a[92:95],v[172:175],v[212:215]
rd a[64:67],v248 offset:16384
mm v[220:223],a[92:95],v[188:191],v[220:223]
rd a[96:99],v248 offset:8192
ex v0,v208
wt vmcnt(12)
buffer_store_dwordx2 v[252:253], off, s[64:67], s87 sc1
mm v[64:67],a[232:235],a[232:235],0
rd a[68:71],v248 offset:17408
ex v1,v209
mm v[68:71],a[232:235],a[232:235],0
rd a[100:103],v248 offset:9216
st v[140:143],v255,s[52:55],0 offen offset:3072 sc1
mm v[72:75],a[232:235],a[232:235],0
rd a[72:75],v248 offset:18432
ex v2,v210
mm v[76:79],a[232:235],a[232:235],0
rd a[104:107],v248 offset:10240
ex v3,v211
mm v[80:83],a[232:235],a[232:235],0
rd a[76:79],v248 offset:19456
ex v8,v216
mm v[84:87],a[232:235],a[232:235],0
rd a[108:111],v248 offset:11264
st v[156:159],v255,s[52:55],s95 offen offset:3072 sc1
mm v[88:91],a[232:235],a[232:235],0
rd a[80:83],v248 offset:20480
ex v9,v217
mm v[92:95],a[232:235],a[232:235],0
rd a[112:115],v248 offset:12288
ex v10,v218
mm v[96:99],a[232:235],a[232:235],0
rd a[84:87],v248 offset:21504
ex v11,v219
mm v[100:103],a[232:235],a[232:235],0
rd a[116:119],v248 offset:13312
st v[172:175],v255,s[52:55],s96 offen offset:3072 sc1
mm v[104:107],a[232:235],a[232:235],0
rd a[88:91],v248 offset:22528
ex v4,v212
mm v[108:111],a[232:235],a[232:235],0
rd a[120:123],v248 offset:14336
ex v5,v213
mm v[112:115],a[232:235],a[232:235],0
rd a[92:95],v248 offset:23552
ex v6,v214
mm v[116:119],a[232:235],a[232:235],0
rd a[124:127],v248 offset:15360
st v[188:191],v255,s[52:55],s97 offen offset:3072 sc1
ex v7,v215
mm v[120:123],a[232:235],a[232:235],0
ex v12,v220
mm v[124:127],a[232:235],a[232:235],0
aw a192,0
aw a193,0
aw a194,0
aw a195,0
aw a196,0
aw a197,0
aw a198,0
aw a199,0
v_add_u32 v248, 0x4000, v248
s_cmp_eq_u32 s3, 3
s_cbranch_scc1 .La5_LA_FH
.p2align 6
; ---- attention, 32 keys per window: S = K Q^T (causal mask through SrcC), P = exp2(S), O += V^T P, row sums l += 1 P; here the later 32 rows only
.La5_LA_HH:
wt lgkmcnt(1)
mm v[208:211],a[64:67],v[160:163],0
ex v13,v221
mm v[208:211],a[68:71],v[164:167],v[208:211]
cv v232,v0,v1
cv v233,v2,v3
mm v[208:211],a[72:75],v[168:171],v[208:211]
ex v14,v222
mm v[208:211],a[76:79],v[172:175],v[208:211]
cv v236,v8,v9
cv v237,v10,v11
mm v[216:219],a[76:79],v[188:191],0
ex v15,v223
mm v[216:219],a[72:75],v[184:187],v[216:219]
cv v234,v4,v5
cv v235,v6,v7
mm v[216:219],a[68:71],v[180:183],v[216:219]
ex v0,v208
mm v[216:219],a[64:67],v[176:179],v[216:219]
cv v238,v12,v13
cv v239,v14,v15
mm v[212:215],a[80:83],v[160:163],0
ex v1,v209
mm v[212:215],a[84:87],v[164:167],v[212:215]
rd a[64:67],v248 offset:16384
rd a[68:71],v248 offset:17408
mm v[212:215],a[88:91],v[168:171],v[212:215]
ex v2,v210
mm v[212:215],a[92:95],v[172:175],v[212:215]
rd a[72:75],v248 offset:18432
rd a[76:79],v248 offset:19456
mm v[220:223],a[92:95],v[188:191],0
ex v3,v211
mm v[220:223],a[88:91],v[184:187],v[220:223]
rd a[92:95],v248 offset:23552
mm v[220:223],a[84:87],v[180:183],v[220:223]
ex v8,v216
mm v[220:223],a[80:83],v[176:179],v[220:223]
wt lgkmcnt(5)
rd a[88:91],v248 offset:22528
mm v[64:67],a[96:99],v[232:235],v[64:67]
ex v9,v217
mm v[96:99],a[96:99],v[236:239],v[96:99]
rd a[96:99],v248 offset:8192
rd a[84:87],v248 offset:21504
mm v[100:103],a[100:103],v[236:239],v[100:103]
ex v10,v218
mm v[68:71],a[100:103],v[232:235],v[68:71]
rd a[100:103],v248 offset:9216
rd a[80:83],v248 offset:20480
mm v[72:75],a[104:107],v[232:235],v[72:75]
ex v11,v219
mm v[104:107],a[104:107],v[236:239],v[104:107]
rd a[104:107],v248 offset:10240
mm v[108:111],a[108:111],v[236:239],v[108:111]
ex v4,v212
mm v[76:79],a[108:111],v[232:235],v[76:79]
rd a[108:111],v248 offset:11264
s_add_u32 s85, s85, 1
mm v[80:83],a[112:115],v[232:235],v[80:83]
ex v5,v213
mm v[112:115],a[112:115],v[236:239],v[112:115]
rd a[112:115],v248 offset:12288
s_add_u32 s89, s89, 32768
mm v[116:119],a[116:119],v[236:239],v[116:119]
ex v6,v214
mm v[84:87],a[116:119],v[232:235],v[84:87]
rd a[116:119],v248 offset:13312
s_add_u32 s92, s85, s3
mm v[88:91],a[120:123],v[232:235],v[88:91]
ex v7,v215
mm v[120:123],a[120:123],v[236:239],v[120:123]
rd a[120:123],v248 offset:14336
buffer_load_dword v250, v251, s[64:67], s20 offen sc1
mm v[124:127],a[124:127],v[236:239],v[124:127]
ex v12,v220
mm v[92:95],a[124:127],v[232:235],v[92:95]
rd a[124:127],v248 offset:15360
s_barrier
v_smfmac_f32_16x16x64_bf16 a[196:199], a[208:211], v[232:239], v241
wt lgkmcnt(6)
mm v[208:211],a[64:67],v[160:163],0
ex v13,v221
mm v[208:211],a[68:71],v[164:167],v[208:211]
cv v232,v0,v1
cv v233,v2,v3
mm v[208:211],a[72:75],v[168:171],v[208:211]
ex v14,v222
mm v[208:211],a[76:79],v[172:175],v[208:211]
cv v236,v8,v9
cv v237,v10,v11
mm v[216:219],a[76:79],v[188:191],0
ex v15,v223
mm v[216:219],a[72:75],v[184:187],v[216:219]
cv v234,v4,v5
cv v235,v6,v7
mm v[216:219],a[68:71],v[180:183],v[216:219]
ex v0,v208
mm v[216:219],a[64:67],v[176:179],v[216:219]
cv v238,v12,v13
cv v239,v14,v15
mm v[212:215],a[80:83],v[160:163],0
ex v1,v209
mm v[212:215],a[84:87],v[164:167],v[212:215]
rd a[64:67],v248 offset:32768
rd a[68:71],v248 offset:33792
mm v[212:215],a[88:91],v[168:171],v[212:215]
ex v2,v210
mm v[212:215],a[92:95],v[172:175],v[212:215]
rd a[72:75],v248 offset:34816
rd a[76:79],v248 offset:35840
mm v[220:223],a[92:95],v[188:191],0
ex v3,v211
mm v[220:223],a[88:91],v[184:187],v[220:223]
rd a[92:95],v248 offset:39936
s_bitcmp1_b64 s[98:99], s85
mm v[220:223],a[84:87],v[180:183],v[220:223]
ex v8,v216
mm v[220:223],a[80:83],v[176:179],v[220:223]
wt lgkmcnt(5)
rd a[88:91],v248 offset:38912
mm v[64:67],a[96:99],v[232:235],v[64:67]
ex v9,v217
mm v[96:99],a[96:99],v[236:239],v[96:99]
rd a[96:99],v248 offset:24576
rd a[84:87],v248 offset:37888
mm v[100:103],a[100:103],v[236:239],v[100:103]
ex v10,v218
mm v[68:71],a[100:103],v[232:235],v[68:71]
rd a[100:103],v248 offset:25600
rd a[80:83],v248 offset:36864
mm v[72:75],a[104:107],v[232:235],v[72:75]
ex v11,v219
mm v[104:107],a[104:107],v[236:239],v[104:107]
rd a[104:107],v248 offset:26624
mm v[108:111],a[108:111],v[236:239],v[108:111]
ex v4,v212
mm v[76:79],a[108:111],v[232:235],v[76:79]
rd a[108:111],v248 offset:27648
mm v[80:83],a[112:115],v[232:235],v[80:83]
ex v5,v213
mm v[112:115],a[112:115],v[236:239],v[112:115]
rd a[112:115],v248 offset:28672
mm v[116:119],a[116:119],v[236:239],v[116:119]
ex v6,v214
mm v[84:87],a[116:119],v[232:235],v[84:87]
rd a[116:119],v248 offset:29696
wt vmcnt(1)
buffer_store_dwordx2 v[252:253], off, s[64:67], s86 sc1
s_mov_b32 s86, 1073741824
s_bitset0_b32 s20, 30
mm v[88:91],a[120:123],v[232:235],v[88:91]
ex v7,v215
mm v[120:123],a[120:123],v[236:239],v[120:123]
rd a[120:123],v248 offset:30720
mm v[124:127],a[124:127],v[236:239],v[124:127]
v_exp_f32_e64 v12, v220
mm v[92:95],a[124:127],v[232:235],v[92:95]
rd a[124:127],v248 offset:31744
v_smfmac_f32_16x16x64_bf16 a[196:199], a[208:211], v[232:239], v241
v_add_u32 v248, 0x8000, v248
s_cbranch_scc0 .La5_LA_HH1
s_call_b64 s[24:25], .La5_LA_EVENT
s_add_u32 s92, s85, s3
.La5_LA_HH1:
s_cmp_lt_u32 s92, 3
s_cbranch_scc1 .La5_LA_HH
; ---- first window with both row groups (diagonal of the earlier group)
.La5_LA_FH:
.La5_LW1_15:
s_cbranch_execz .La5_LW1_R0
wt lgkmcnt(9)
mm v[192:195],a[64:67],v[128:131],v[32:35]
ex v13,v221
mm v[192:195],a[68:71],v[132:135],v[192:195]
cv v232,v0,v1
cv v233,v2,v3
mm v[192:195],a[72:75],v[136:139],v[192:195]
ex v14,v222
mm v[192:195],a[76:79],v[140:143],v[192:195]
ld v255,s[60:63],s17 offen offset:0 sc1 lds
mm v[200:203],a[76:79],v[156:159],0
ex v15,v223
mm v[200:203],a[72:75],v[152:155],v[200:203]
cv v236,v8,v9
cv v237,v10,v11
mm v[200:203],a[68:71],v[148:151],v[200:203]
ex v192,v192
mm v[200:203],a[64:67],v[144:147],v[200:203]
wt lgkmcnt(1)
cv v234,v4,v5
mm v[196:199],a[80:83],v[128:131],v[36:39]
ex v193,v193
mm v[196:199],a[84:87],v[132:135],v[196:199]
cv v224,v192,v193
s_add_u32 s85, s85, 1
mm v[196:199],a[88:91],v[136:139],v[196:199]
ex v194,v194
mm v[196:199],a[92:95],v[140:143],v[196:199]
ld v255,s[60:63],s17 offen offset:1024 sc1 lds
mm v[204:207],a[92:95],v[156:159],v[32:35]
ex v195,v195
mm v[204:207],a[88:91],v[152:155],v[204:207]
cv v225,v194,v195
s_add_u32 s89, s89, 16384
mm v[204:207],a[84:87],v[148:151],v[204:207]
ex v200,v200
mm v[204:207],a[80:83],v[144:147],v[204:207]
ld v255,s[60:63],s17 offen offset:2048 sc1 lds
mm v[208:211],a[64:67],v[160:163],0
ex v201,v201
mm v[208:211],a[68:71],v[164:167],v[208:211]
cv v228,v200,v201
s_and_b32 s89, s89, 0x1ffff
mm v[208:211],a[72:75],v[168:171],v[208:211]
ex v202,v202
mm v[208:211],a[76:79],v[172:175],v[208:211]
ld v255,s[60:63],s17 offen offset:3072 sc1 lds
mm v[216:219],a[76:79],v[188:191],0
ex v203,v203
mm v[216:219],a[72:75],v[184:187],v[216:219]
cv v229,v202,v203
mm v[216:219],a[68:71],v[180:183],v[216:219]
ex v196,v196
mm v[216:219],a[64:67],v[176:179],v[216:219]
cv v235,v6,v7
cv v238,v12,v13
mm v[212:215],a[80:83],v[160:163],0
ex v197,v197
s_add_u32 s17, s17, 8192
mm v[212:215],a[84:87],v[164:167],v[212:215]
cv v226,v196,v197
s_add_u32 m0, m0, 16384
s_and_b32 m0, m0, s78
mm v[212:215],a[88:91],v[168:171],v[212:215]
ex v198,v198
mm v[212:215],a[92:95],v[172:175],v[212:215]
cv v239,v14,v15
wt lgkmcnt(0)
mm v[220:223],a[92:95],v[188:191],0
ex v199,v199
mm v[220:223],a[88:91],v[184:187],v[220:223]
cv v227,v198,v199
mm v[220:223],a[84:87],v[180:183],v[220:223]
ex v204,v204
mm v[220:223],a[80:83],v[176:179],v[220:223]
wt vmcnt(10)
s_barrier
mm v[64:67],a[96:99],v[232:235],v[64:67]
ex v205,v205
mm v[96:99],a[96:99],v[236:239],v[96:99]
rd a[96:99],v248 offset:8192
cv v230,v204,v205
mm v[100:103],a[100:103],v[236:239],v[100:103]
ex v206,v206
mm v[68:71],a[100:103],v[232:235],v[68:71]
rd a[100:103],v248 offset:9216
buffer_load_dword v250, v251, s[64:67], s20 offen sc1
mm v[72:75],a[104:107],v[232:235],v[72:75]
ex v207,v207
mm v[104:107],a[104:107],v[236:239],v[104:107]
rd a[104:107],v248 offset:10240
cv v231,v206,v207
mm v[108:111],a[108:111],v[236:239],v[108:111]
ex v208,v208
mm v[76:79],a[108:111],v[232:235],v[76:79]
rd a[108:111],v248 offset:11264
mm v[80:83],a[112:115],v[232:235],v[80:83]
ex v209,v209
mm v[112:115],a[112:115],v[236:239],v[112:115]
rd a[112:115],v248 offset:12288
mm v[116:119],a[116:119],v[236:239],v[116:119]
ex v210,v210
mm v[84:87],a[116:119],v[232:235],v[84:87]
rd a[116:119],v248 offset:13312
mm v[88:91],a[120:123],v[232:235],v[88:91]
ex v211,v211
mm v[120:123],a[120:123],v[236:239],v[120:123]
rd a[120:123],v248 offset:14336
s_bitcmp1_b64 s[98:99], s85
mm v[124:127],a[124:127],v[236:239],v[124:127]
ex v216,v216
mm v[92:95],a[124:127],v[232:235],v[92:95]
rd a[124:127],v248 offset:15360
v_or_b32 v248, s89, v255
v_smfmac_f32_16x16x64_bf16 a[196:199], a[208:211], v[232:239], v241
wt lgkmcnt(4)
v_smfmac_f32_16x16x64_bf16 a[192:195], a[208:211], v[224:231], v241
rd a[64:67],v248 offset:0
mm v[0:3],a[96:99],v[224:227],0
ex v217,v217
mm v[32:35],a[96:99],v[228:231],0
rd a[68:71],v248 offset:1024
cv v232,v208,v209
.La5_LW2_0:
s_cbranch_execz .La5_LW2_R0
mm v[36:39],a[100:103],v[228:231],0
ex v218,v218
mm v[4:7],a[100:103],v[224:227],0
rd a[72:75],v248 offset:2048
cv v233,v210,v211
mm v[8:11],a[104:107],v[224:227],0
ex v219,v219
mm v[40:43],a[104:107],v[228:231],0
rd a[76:79],v248 offset:3072
cv v236,v216,v217
mm v[44:47],a[108:111],v[228:231],0
ex v212,v212
mm v[12:15],a[108:111],v[224:227],0
rd a[80:83],v248 offset:4096
wt lgkmcnt(5)
mm v[16:19],a[112:115],v[224:227],0
ex v213,v213
mm v[48:51],a[112:115],v[228:231],0
rd a[84:87],v248 offset:5120
cv v237,v218,v219
mm v[52:55],a[116:119],v[228:231],0
ex v214,v214
mm v[20:23],a[116:119],v[224:227],0
rd a[88:91],v248 offset:6144
cv v234,v212,v213
mm v[24:27],a[120:123],v[224:227],0
ex v215,v215
mm v[56:59],a[120:123],v[228:231],0
rd a[92:95],v248 offset:7168
cv v235,v214,v215
mm v[60:63],a[124:127],v[228:231],0
ex v220,v220
mm v[28:31],a[124:127],v[224:227],0
wt vmcnt(5)
buffer_store_dwordx2 v[252:253], off, s[64:67], s86 sc1
s_mov_b32 s86, 1073741824
s_bitset0_b32 s20, 30
s_cbranch_scc1 .La5_LA_FE0
.La5_LA_FS0:
wt lgkmcnt(4)
mm v[192:195],a[64:67],v[128:131],0
ex v221,v221
mm v[192:195],a[68:71],v[132:135],v[192:195]
cv v238,v220,v221
s_add_u32 s85, s85, 1
mm v[192:195],a[72:75],v[136:139],v[192:195]
ex v222,v222
mm v[192:195],a[76:79],v[140:143],v[192:195]
ld v255,s[60:63],s17 offen offset:0 sc1 lds
mm v[200:203],a[76:79],v[156:159],0
ex v223,v223
mm v[200:203],a[72:75],v[152:155],v[200:203]
cv v239,v222,v223
s_add_u32 s89, s89, 16384
mm v[200:203],a[68:71],v[148:151],v[200:203]
ex v192,v192
mm v[200:203],a[64:67],v[144:147],v[200:203]
wt lgkmcnt(0)
s_and_b32 s89, s89, 0x1ffff
mm v[196:199],a[80:83],v[128:131],0
ex v193,v193
mm v[196:199],a[84:87],v[132:135],v[196:199]
cv v224,v192,v193
mm v[196:199],a[88:91],v[136:139],v[196:199]
ex v194,v194
mm v[196:199],a[92:95],v[140:143],v[196:199]
ld v255,s[60:63],s17 offen offset:1024 sc1 lds
mm v[204:207],a[92:95],v[156:159],0
ex v195,v195
mm v[204:207],a[88:91],v[152:155],v[204:207]
cv v225,v194,v195
mm v[204:207],a[84:87],v[148:151],v[204:207]
ex v200,v200
mm v[204:207],a[80:83],v[144:147],v[204:207]
ld v255,s[60:63],s17 offen offset:2048 sc1 lds
mm v[208:211],a[64:67],v[160:163],0
ex v201,v201
mm v[208:211],a[68:71],v[164:167],v[208:211]
cv v228,v200,v201
mm v[208:211],a[72:75],v[168:171],v[208:211]
ex v202,v202
mm v[208:211],a[76:79],v[172:175],v[208:211]
ld v255,s[60:63],s17 offen offset:3072 sc1 lds
mm v[216:219],a[76:79],v[188:191],0
ex v203,v203
mm v[216:219],a[72:75],v[184:187],v[216:219]
cv v229,v202,v203
mm v[216:219],a[68:71],v[180:183],v[216:219]
ex v196,v196
mm v[216:219],a[64:67],v[176:179],v[216:219]
buffer_load_dword a253, v254, s[36:39], s90 offen
mm v[212:215],a[80:83],v[160:163],0
ex v197,v197
mm v[212:215],a[84:87],v[164:167],v[212:215]
cv v226,v196,v197
s_add_u32 s17, s17, 8192
mm v[212:215],a[88:91],v[168:171],v[212:215]
ex v198,v198
mm v[212:215],a[92:95],v[172:175],v[212:215]
s_add_u32 m0, m0, 16384
s_and_b32 m0, m0, s78
mm v[220:223],a[92:95],v[188:191],0
ex v199,v199
mm v[220:223],a[88:91],v[184:187],v[220:223]
cv v227,v198,v199
s_add_u32 s90, s90, s21
mm v[220:223],a[84:87],v[180:183],v[220:223]
ex v204,v204
mm v[220:223],a[80:83],v[176:179],v[220:223]
wt vmcnt(11)
s_barrier
mm v[64:67],a[96:99],v[232:235],v[64:67]
ex v205,v205
mm v[96:99],a[96:99],v[236:239],v[96:99]
rd a[96:99],v248 offset:8192
cv v230,v204,v205
mm v[100:103],a[100:103],v[236:239],v[100:103]
ex v206,v206
mm v[68:71],a[100:103],v[232:235],v[68:71]
rd a[100:103],v248 offset:9216
buffer_load_dword v250, v251, s[64:67], s20 offen sc1
mm v[72:75],a[104:107],v[232:235],v[72:75]
ex v207,v207
mm v[104:107],a[104:107],v[236:239],v[104:107]
rd a[104:107],v248 offset:10240
cv v231,v206,v207
mm v[108:111],a[108:111],v[236:239],v[108:111]
ex v208,v208
mm v[76:79],a[108:111],v[232:235],v[76:79]
rd a[108:111],v248 offset:11264
mm v[80:83],a[112:115],v[232:235],v[80:83]
ex v209,v209
mm v[112:115],a[112:115],v[236:239],v[112:115]
rd a[112:115],v248 offset:12288
mm v[116:119],a[116:119],v[236:239],v[116:119]
ex v210,v210
mm v[84:87],a[116:119],v[232:235],v[84:87]
rd a[116:119],v248 offset:13312
mm v[88:91],a[120:123],v[232:235],v[88:91]
ex v211,v211
mm v[120:123],a[120:123],v[236:239],v[120:123]
rd a[120:123],v248 offset:14336
s_bitcmp1_b64 s[98:99], s85
mm v[124:127],a[124:127],v[236:239],v[124:127]
ex v216,v216
mm v[92:95],a[124:127],v[232:235],v[92:95]
rd a[124:127],v248 offset:15360
v_or_b32 v248, s89, v255
v_smfmac_f32_16x16x64_bf16 a[196:199], a[208:211], v[232:239], v241
wt lgkmcnt(4)
v_smfmac_f32_16x16x64_bf16 a[192:195], a[208:211], v[224:231], v241
rd a[64:67],v248 offset:0
mm v[0:3],a[96:99],v[224:227],v[0:3]
ex v217,v217
mm v[32:35],a[96:99],v[228:231],v[32:35]
rd a[68:71],v248 offset:1024
cv v232,v208,v209
mm v[36:39],a[100:103],v[228:231],v[36:39]
ex v218,v218
mm v[4:7],a[100:103],v[224:227],v[4:7]
rd a[72:75],v248 offset:2048
cv v233,v210,v211
mm v[8:11],a[104:107],v[224:227],v[8:11]
ex v219,v219
mm v[40:43],a[104:107],v[228:231],v[40:43]
rd a[76:79],v248 offset:3072
cv v236,v216,v217
mm v[44:47],a[108:111],v[228:231],v[44:47]
ex v212,v212
mm v[12:15],a[108:111],v[224:227],v[12:15]
rd a[80:83],v248 offset:4096
wt lgkmcnt(5)
mm v[16:19],a[112:115],v[224:227],v[16:19]
ex v213,v213
mm v[48:51],a[112:115],v[228:231],v[48:51]
rd a[84:87],v248 offset:5120
cv v237,v218,v219
mm v[52:55],a[116:119],v[228:231],v[52:55]
ex v214,v214
mm v[20:23],a[116:119],v[224:227],v[20:23]
rd a[88:91],v248 offset:6144
cv v234,v212,v213
mm v[24:27],a[120:123],v[224:227],v[24:27]
ex v215,v215
mm v[56:59],a[120:123],v[228:231],v[56:59]
rd a[92:95],v248 offset:7168
cv v235,v214,v215
mm v[60:63],a[124:127],v[228:231],v[60:63]
ex v220,v220
mm v[28:31],a[124:127],v[224:227],v[28:31]
s_cbranch_scc1 .La5_LA_F1E2
.p2align 6
; ---- steady windows
.La5_LA_F1:
wt lgkmcnt(4)
mm v[192:195],a[64:67],v[128:131],0
ex v221,v221
mm v[192:195],a[68:71],v[132:135],v[192:195]
cv v238,v220,v221
s_add_u32 s85, s85, 1
mm v[192:195],a[72:75],v[136:139],v[192:195]
ex v222,v222
mm v[192:195],a[76:79],v[140:143],v[192:195]
ld v255,s[60:63],s17 offen offset:0 sc1 lds
mm v[200:203],a[76:79],v[156:159],0
ex v223,v223
mm v[200:203],a[72:75],v[152:155],v[200:203]
cv v239,v222,v223
s_add_u32 s89, s89, 16384
mm v[200:203],a[68:71],v[148:151],v[200:203]
ex v192,v192
mm v[200:203],a[64:67],v[144:147],v[200:203]
wt lgkmcnt(0)
s_and_b32 s89, s89, 0x1ffff
mm v[196:199],a[80:83],v[128:131],0
ex v193,v193
mm v[196:199],a[84:87],v[132:135],v[196:199]
cv v224,v192,v193
mm v[196:199],a[88:91],v[136:139],v[196:199]
ex v194,v194
mm v[196:199],a[92:95],v[140:143],v[196:199]
ld v255,s[60:63],s17 offen offset:1024 sc1 lds
mm v[204:207],a[92:95],v[156:159],0
ex v195,v195
mm v[204:207],a[88:91],v[152:155],v[204:207]
cv v225,v194,v195
mm v[204:207],a[84:87],v[148:151],v[204:207]
ex v200,v200
mm v[204:207],a[80:83],v[144:147],v[204:207]
ld v255,s[60:63],s17 offen offset:2048 sc1 lds
mm v[208:211],a[64:67],v[160:163],0
ex v201,v201
mm v[208:211],a[68:71],v[164:167],v[208:211]
cv v228,v200,v201
mm v[208:211],a[72:75],v[168:171],v[208:211]
ex v202,v202
mm v[208:211],a[76:79],v[172:175],v[208:211]
ld v255,s[60:63],s17 offen offset:3072 sc1 lds
mm v[216:219],a[76:79],v[188:191],0
ex v203,v203
mm v[216:219],a[72:75],v[184:187],v[216:219]
cv v229,v202,v203
mm v[216:219],a[68:71],v[180:183],v[216:219]
ex v196,v196
mm v[216:219],a[64:67],v[176:179],v[216:219]
buffer_load_dword a253, v254, s[36:39], s90 offen
mm v[212:215],a[80:83],v[160:163],0
ex v197,v197
mm v[212:215],a[84:87],v[164:167],v[212:215]
cv v226,v196,v197
s_add_u32 s17, s17, 8192
mm v[212:215],a[88:91],v[168:171],v[212:215]
ex v198,v198
mm v[212:215],a[92:95],v[172:175],v[212:215]
s_add_u32 m0, m0, 16384
s_and_b32 m0, m0, s78
mm v[220:223],a[92:95],v[188:191],0
ex v199,v199
mm v[220:223],a[88:91],v[184:187],v[220:223]
cv v227,v198,v199
s_add_u32 s90, s90, s21
mm v[220:223],a[84:87],v[180:183],v[220:223]
ex v204,v204
mm v[220:223],a[80:83],v[176:179],v[220:223]
wt vmcnt(11)
s_barrier
mm v[64:67],a[96:99],v[232:235],v[64:67]
ex v205,v205
mm v[96:99],a[96:99],v[236:239],v[96:99]
rd a[96:99],v248 offset:8192
cv v230,v204,v205
mm v[100:103],a[100:103],v[236:239],v[100:103]
ex v206,v206
mm v[68:71],a[100:103],v[232:235],v[68:71]
rd a[100:103],v248 offset:9216
mm v[72:75],a[104:107],v[232:235],v[72:75]
ex v207,v207
mm v[104:107],a[104:107],v[236:239],v[104:107]
rd a[104:107],v248 offset:10240
cv v231,v206,v207
mm v[108:111],a[108:111],v[236:239],v[108:111]
ex v208,v208
mm v[76:79],a[108:111],v[232:235],v[76:79]
rd a[108:111],v248 offset:11264
mm v[80:83],a[112:115],v[232:235],v[80:83]
ex v209,v209
mm v[112:115],a[112:115],v[236:239],v[112:115]
rd a[112:115],v248 offset:12288
mm v[116:119],a[116:119],v[236:239],v[116:119]
ex v210,v210
mm v[84:87],a[116:119],v[232:235],v[84:87]
rd a[116:119],v248 offset:13312
mm v[88:91],a[120:123],v[232:235],v[88:91]
ex v211,v211
mm v[120:123],a[120:123],v[236:239],v[120:123]
rd a[120:123],v248 offset:14336
s_bitcmp1_b64 s[98:99], s85
mm v[124:127],a[124:127],v[236:239],v[124:127]
ex v216,v216
mm v[92:95],a[124:127],v[232:235],v[92:95]
rd a[124:127],v248 offset:15360
v_or_b32 v248, s89, v255
v_smfmac_f32_16x16x64_bf16 a[196:199], a[208:211], v[232:239], v241
wt lgkmcnt(4)
v_smfmac_f32_16x16x64_bf16 a[192:195], a[208:211], v[224:231], v241
rd a[64:67],v248 offset:0
mm v[0:3],a[96:99],v[224:227],v[0:3]
ex v217,v217
mm v[32:35],a[96:99],v[228:231],v[32:35]
rd a[68:71],v248 offset:1024
cv v232,v208,v209
mm v[36:39],a[100:103],v[228:231],v[36:39]
ex v218,v218
mm v[4:7],a[100:103],v[224:227],v[4:7]
rd a[72:75],v248 offset:2048
cv v233,v210,v211
mm v[8:11],a[104:107],v[224:227],v[8:11]
ex v219,v219
mm v[40:43],a[104:107],v[228:231],v[40:43]
rd a[76:79],v248 offset:3072
cv v236,v216,v217
mm v[44:47],a[108:111],v[228:231],v[44:47]
ex v212,v212
mm v[12:15],a[108:111],v[224:227],v[12:15]
rd a[80:83],v248 offset:4096
wt lgkmcnt(5)
mm v[16:19],a[112:115],v[224:227],v[16:19]
ex v213,v213
mm v[48:51],a[112:115],v[228:231],v[48:51]
rd a[84:87],v248 offset:5120
cv v237,v218,v219
mm v[52:55],a[116:119],v[228:231],v[52:55]
ex v214,v214
mm v[20:23],a[116:119],v[224:227],v[20:23]
rd a[88:91],v248 offset:6144
cv v234,v212,v213
mm v[24:27],a[120:123],v[224:227],v[24:27]
ex v215,v215
mm v[56:59],a[120:123],v[228:231],v[56:59]
rd a[92:95],v248 offset:7168
cv v235,v214,v215
mm v[60:63],a[124:127],v[228:231],v[60:63]
ex v220,v220
mm v[28:31],a[124:127],v[224:227],v[28:31]
s_cbranch_scc1 .La5_LA_F1E2
wt lgkmcnt(4)
mm v[192:195],a[64:67],v[128:131],0
ex v221,v221
mm v[192:195],a[68:71],v[132:135],v[192:195]
cv v238,v220,v221
s_add_u32 s85, s85, 1
mm v[192:195],a[72:75],v[136:139],v[192:195]
ex v222,v222
mm v[192:195],a[76:79],v[140:143],v[192:195]
ld v255,s[60:63],s17 offen offset:0 sc1 lds
mm v[200:203],a[76:79],v[156:159],0
ex v223,v223
mm v[200:203],a[72:75],v[152:155],v[200:203]
cv v239,v222,v223
s_add_u32 s89, s89, 16384
mm v[200:203],a[68:71],v[148:151],v[200:203]
ex v192,v192
mm v[200:203],a[64:67],v[144:147],v[200:203]
wt lgkmcnt(0)
s_and_b32 s89, s89, 0x1ffff
mm v[196:199],a[80:83],v[128:131],0
ex v193,v193
mm v[196:199],a[84:87],v[132:135],v[196:199]
cv v224,v192,v193
mm v[196:199],a[88:91],v[136:139],v[196:199]
ex v194,v194
mm v[196:199],a[92:95],v[140:143],v[196:199]
ld v255,s[60:63],s17 offen offset:1024 sc1 lds
mm v[204:207],a[92:95],v[156:159],0
ex v195,v195
mm v[204:207],a[88:91],v[152:155],v[204:207]
cv v225,v194,v195
mm v[204:207],a[84:87],v[148:151],v[204:207]
ex v200,v200
mm v[204:207],a[80:83],v[144:147],v[204:207]
ld v255,s[60:63],s17 offen offset:2048 sc1 lds
mm v[208:211],a[64:67],v[160:163],0
ex v201,v201
mm v[208:211],a[68:71],v[164:167],v[208:211]
cv v228,v200,v201
mm v[208:211],a[72:75],v[168:171],v[208:211]
ex v202,v202
mm v[208:211],a[76:79],v[172:175],v[208:211]
ld v255,s[60:63],s17 offen offset:3072 sc1 lds
mm v[216:219],a[76:79],v[188:191],0
ex v203,v203
mm v[216:219],a[72:75],v[184:187],v[216:219]
cv v229,v202,v203
mm v[216:219],a[68:71],v[180:183],v[216:219]
ex v196,v196
mm v[216:219],a[64:67],v[176:179],v[216:219]
buffer_load_dword a253, v254, s[36:39], s90 offen
mm v[212:215],a[80:83],v[160:163],0
ex v197,v197
mm v[212:215],a[84:87],v[164:167],v[212:215]
cv v226,v196,v197
s_add_u32 s17, s17, 8192
mm v[212:215],a[88:91],v[168:171],v[212:215]
ex v198,v198
mm v[212:215],a[92:95],v[172:175],v[212:215]
s_add_u32 m0, m0, 16384
s_and_b32 m0, m0, s78
mm v[220:223],a[92:95],v[188:191],0
ex v199,v199
mm v[220:223],a[88:91],v[184:187],v[220:223]
cv v227,v198,v199
s_add_u32 s90, s90, s21
mm v[220:223],a[84:87],v[180:183],v[220:223]
ex v204,v204
mm v[220:223],a[80:83],v[176:179],v[220:223]
wt vmcnt(10)
s_barrier
mm v[64:67],a[96:99],v[232:235],v[64:67]
ex v205,v205
mm v[96:99],a[96:99],v[236:239],v[96:99]
rd a[96:99],v248 offset:8192
cv v230,v204,v205
mm v[100:103],a[100:103],v[236:239],v[100:103]
ex v206,v206
mm v[68:71],a[100:103],v[232:235],v[68:71]
rd a[100:103],v248 offset:9216
mm v[72:75],a[104:107],v[232:235],v[72:75]
ex v207,v207
mm v[104:107],a[104:107],v[236:239],v[104:107]
rd a[104:107],v248 offset:10240
cv v231,v206,v207
mm v[108:111],a[108:111],v[236:239],v[108:111]
ex v208,v208
mm v[76:79],a[108:111],v[232:235],v[76:79]
rd a[108:111],v248 offset:11264
mm v[80:83],a[112:115],v[232:235],v[80:83]
ex v209,v209
mm v[112:115],a[112:115],v[236:239],v[112:115]
rd a[112:115],v248 offset:12288
mm v[116:119],a[116:119],v[236:239],v[116:119]
ex v210,v210
mm v[84:87],a[116:119],v[232:235],v[84:87]
rd a[116:119],v248 offset:13312
mm v[88:91],a[120:123],v[232:235],v[88:91]
ex v211,v211
mm v[120:123],a[120:123],v[236:239],v[120:123]
rd a[120:123],v248 offset:14336
s_bitcmp1_b64 s[98:99], s85
mm v[124:127],a[124:127],v[236:239],v[124:127]
ex v216,v216
mm v[92:95],a[124:127],v[232:235],v[92:95]
rd a[124:127],v248 offset:15360
v_or_b32 v248, s89, v255
v_smfmac_f32_16x16x64_bf16 a[196:199], a[208:211], v[232:239], v241
wt lgkmcnt(4)
v_smfmac_f32_16x16x64_bf16 a[192:195], a[208:211], v[224:231], v241
rd a[64:67],v248 offset:0
mm v[0:3],a[96:99],v[224:227],v[0:3]
ex v217,v217
mm v[32:35],a[96:99],v[228:231],v[32:35]
rd a[68:71],v248 offset:1024
cv v232,v208,v209
mm v[36:39],a[100:103],v[228:231],v[36:39]
ex v218,v218
mm v[4:7],a[100:103],v[224:227],v[4:7]
rd a[72:75],v248 offset:2048
cv v233,v210,v211
mm v[8:11],a[104:107],v[224:227],v[8:11]
ex v219,v219
mm v[40:43],a[104:107],v[228:231],v[40:43]
rd a[76:79],v248 offset:3072
cv v236,v216,v217
mm v[44:47],a[108:111],v[228:231],v[44:47]
ex v212,v212
mm v[12:15],a[108:111],v[224:227],v[12:15]
rd a[80:83],v248 offset:4096
wt lgkmcnt(5)
mm v[16:19],a[112:115],v[224:227],v[16:19]
ex v213,v213
mm v[48:51],a[112:115],v[228:231],v[48:51]
rd a[84:87],v248 offset:5120
cv v237,v218,v219
mm v[52:55],a[116:119],v[228:231],v[52:55]
ex v214,v214
mm v[20:23],a[116:119],v[224:227],v[20:23]
rd a[88:91],v248 offset:6144
cv v234,v212,v213
mm v[24:27],a[120:123],v[224:227],v[24:27]
ex v215,v215
mm v[56:59],a[120:123],v[228:231],v[56:59]
rd a[92:95],v248 offset:7168
cv v235,v214,v215
mm v[60:63],a[124:127],v[228:231],v[60:63]
ex v220,v220
mm v[28:31],a[124:127],v[224:227],v[28:31]
.La5_LA_F1E:
s_cbranch_scc0 .La5_LA_F1
.La5_LA_F1E2:
s_cmp_eq_u32 s85, s82
s_cbranch_scc1 .La5_LA_F1X
s_nop 0
s_nop 0
s_call_b64 s[24:25], .La5_LA_EVENT
s_branch .La5_LA_F1
.La5_LA_FE0:
.La5_LW2_1:
s_cbranch_execz .La5_LW2_R1
s_cmp_eq_u32 s85, s82
s_cbranch_scc1 .La5_LA_F1X
s_call_b64 s[24:25], .La5_LA_EVENT
s_branch .La5_LA_FS0
; ---- hand-offs between windows
.La5_LA_EVENT:
s_cmp_lg_u32 s85, 2
s_cbranch_scc1 .La5_LA_E1
wt vmcnt(0)
v_cmp_eq_u32 vcc, v250, v249
s_mov_b32 s17, s16
s_mov_b32 s78, 0x1ffff
s_lshl_b32 m0, s3, 12
s_or_b32 vcc_hi, vcc_hi, s79
s_cmp_eq_u64 vcc, -1
s_cbranch_scc0 .La5_LA_KSPIN
.La5_LA_KOK:
ld v255,s[60:63],s17 offen offset:0 sc1 lds
ld v255,s[60:63],s17 offen offset:1024 sc1 lds
ld v255,s[60:63],s17 offen offset:2048 sc1 lds
ld v255,s[60:63],s17 offen offset:3072 sc1 lds
s_add_u32 s17, s17, 8192
s_add_u32 m0, m0, 16384
s_mov_b32 s20, s19
v_and_b32 v251, 4, v251
s_cmp_lg_u32 s3, 0
s_cbranch_scc1 .La5_LA_POLL
s_nop 0
ld v255,s[60:63],s17 offen offset:0 sc1 lds
ld v255,s[60:63],s17 offen offset:1024 sc1 lds
ld v255,s[60:63],s17 offen offset:2048 sc1 lds
ld v255,s[60:63],s17 offen offset:3072 sc1 lds
s_add_u32 s17, s17, 8192
s_add_u32 m0, m0, 16384
.La5_LA_POLL:
s_cmp_lg_u32 s22, 0
s_cbranch_scc1 .La5_LA_WP
s_mov_b64 exec, 0
s_cmp_eq_u32 s3, 0
s_cbranch_scc1 .La5_LW5_0
s_cmp_eq_u32 s3, 1
s_cbranch_scc1 .La5_LW5_1
s_cmp_eq_u32 s3, 2
s_cbranch_scc1 .La5_LW5_2
s_branch .La5_LW5_R0
.La5_LW5_R0:
s_mov_b64 exec, -1
.La5_LA_RETURN:
s_setpc_b64 s[24:25]
.La5_LA_WP:
s_mov_b64 exec, 0
s_cmp_eq_u32 s3, 0
s_cbranch_scc1 .La5_LW6_0
s_cmp_eq_u32 s3, 1
s_cbranch_scc1 .La5_LW6_1
s_cmp_eq_u32 s3, 2
s_cbranch_scc1 .La5_LW6_2
s_branch .La5_LW6_3
.La5_LW6_R0:
s_mov_b64 exec, -1
s_cmp_eq_u32 s73, 1
s_cbranch_scc1 .La5_LA_E0
buffer_load_dword v250, v251, s[64:67], s20 offen sc1
s_setpc_b64 s[24:25]
.La5_LA_E0:
s_mov_b32 s54, 16384
ld a[0:3],v255,s[52:55],0 offen offset:0 sc1
ld a[4:7],v255,s[52:55],0 offen offset:1024 sc1
ld a[8:11],v255,s[52:55],0 offen offset:2048 sc1
ld a[12:15],v255,s[52:55],0 offen offset:3072 sc1
ld a[16:19],v255,s[52:55],s95 offen offset:0 sc1
ld a[20:23],v255,s[52:55],s95 offen offset:1024 sc1
s_setpc_b64 s[24:25]
.La5_LA_E1:
ld a[24:27],v255,s[52:55],s95 offen offset:2048 sc1
ld a[28:31],v255,s[52:55],s95 offen offset:3072 sc1
ld a[32:35],v255,s[52:55],s96 offen offset:0 sc1
ld a[36:39],v255,s[52:55],s96 offen offset:1024 sc1
ld a[40:43],v255,s[52:55],s96 offen offset:2048 sc1
ld a[44:47],v255,s[52:55],s96 offen offset:3072 sc1
ld a[48:51],v255,s[52:55],s97 offen offset:0 sc1
ld a[52:55],v255,s[52:55],s97 offen offset:1024 sc1
ld a[56:59],v255,s[52:55],s97 offen offset:2048 sc1
ld a[60:63],v255,s[52:55],s97 offen offset:3072 sc1
s_setpc_b64 s[24:25]
; ---- helper: own block finished: l is checked (.La5_LA_SLOW if outside [2^-60, 2^60] or NaN), O / l leaves; the partner-s rows start
.La5_LA_F1X:
s_cmp_eq_u32 s22, 0
s_nop 0
s_nop 0
s_cbranch_scc1 .La5_LA_DC
s_cmp_eq_u32 s73, 1
s_cbranch_scc1 .La5_LA_F1Y
s_nop 0
v_cmp_eq_u32 vcc, v250, v249
s_mov_b32 s17, 0
s_mov_b32 s54, 16384
s_and_b32 s92, vcc_lo, 3
s_cmp_eq_u32 s92, 3
s_cbranch_scc0 .La5_LA_QSPIN
.La5_LA_QOK:
wt lgkmcnt(4)
mm v[192:195],a[64:67],v[128:131],0
ex v221,v221
mm v[192:195],a[68:71],v[132:135],v[192:195]
cv v238,v220,v221
s_add_u32 s85, s85, 1
mm v[192:195],a[72:75],v[136:139],v[192:195]
ex v222,v222
mm v[192:195],a[76:79],v[140:143],v[192:195]
ld v255,s[60:63],s17 offen offset:0 sc1 lds
mm v[200:203],a[76:79],v[156:159],0
ex v223,v223
mm v[200:203],a[72:75],v[152:155],v[200:203]
cv v239,v222,v223
s_add_u32 s89, s89, 16384
mm v[200:203],a[68:71],v[148:151],v[200:203]
ex v192,v192
mm v[200:203],a[64:67],v[144:147],v[200:203]
wt lgkmcnt(0)
s_and_b32 s89, s89, 0x1ffff
mm v[196:199],a[80:83],v[128:131],0
ex v193,v193
mm v[196:199],a[84:87],v[132:135],v[196:199]
cv v224,v192,v193
mm v[196:199],a[88:91],v[136:139],v[196:199]
ex v194,v194
mm v[196:199],a[92:95],v[140:143],v[196:199]
.La5_LW6_0:
s_cbranch_execz .La5_LW6_4
ld v255,s[60:63],s17 offen offset:1024 sc1 lds
mm v[204:207],a[92:95],v[156:159],0
ex v195,v195
mm v[204:207],a[88:91],v[152:155],v[204:207]
cv v225,v194,v195
mm v[204:207],a[84:87],v[148:151],v[204:207]
ex v200,v200
mm v[204:207],a[80:83],v[144:147],v[204:207]
ld v255,s[60:63],s17 offen offset:2048 sc1 lds
mm v[208:211],a[64:67],v[160:163],0
ex v201,v201
mm v[208:211],a[68:71],v[164:167],v[208:211]
cv v228,v200,v201
mm v[208:211],a[72:75],v[168:171],v[208:211]
ex v202,v202
mm v[208:211],a[76:79],v[172:175],v[208:211]
ld v255,s[60:63],s17 offen offset:3072 sc1 lds
mm v[216:219],a[76:79],v[188:191],0
ex v203,v203
mm v[216:219],a[72:75],v[184:187],v[216:219]
cv v229,v202,v203
mm v[216:219],a[68:71],v[180:183],v[216:219]
ex v196,v196
mm v[216:219],a[64:67],v[176:179],v[216:219]
ld a[0:3],v255,s[52:55],0 offen offset:0 sc1
mm v[212:215],a[80:83],v[160:163],0
ex v197,v197
mm v[212:215],a[84:87],v[164:167],v[212:215]
cv v226,v196,v197
s_add_u32 s17, s17, 8192
mm v[212:215],a[88:91],v[168:171],v[212:215]
ex v198,v198
mm v[212:215],a[92:95],v[172:175],v[212:215]
s_add_u32 m0, m0, 16384
s_and_b32 m0, m0, s78
ld a[4:7],v255,s[52:55],0 offen offset:1024 sc1
mm v[220:223],a[92:95],v[188:191],0
ex v199,v199
mm v[220:223],a[88:91],v[184:187],v[220:223]
cv v227,v198,v199
mm v[220:223],a[84:87],v[180:183],v[220:223]
ex v204,v204
mm v[220:223],a[80:83],v[176:179],v[220:223]
wt vmcnt(12)
s_barrier
mm v[64:67],a[96:99],v[232:235],v[64:67]
ex v205,v205
mm v[96:99],a[96:99],v[236:239],v[96:99]
rd a[96:99],v248 offset:8192
cv v230,v204,v205
mm v[100:103],a[100:103],v[236:239],v[100:103]
ex v206,v206
mm v[68:71],a[100:103],v[232:235],v[68:71]
rd a[100:103],v248 offset:9216
ld a[8:11],v255,s[52:55],0 offen offset:2048 sc1
mm v[72:75],a[104:107],v[232:235],v[72:75]
ex v207,v207
mm v[104:107],a[104:107],v[236:239],v[104:107]
rd a[104:107],v248 offset:10240
cv v231,v206,v207
mm v[108:111],a[108:111],v[236:239],v[108:111]
ex v208,v208
mm v[76:79],a[108:111],v[232:235],v[76:79]
rd a[108:111],v248 offset:11264
ld a[12:15],v255,s[52:55],0 offen offset:3072 sc1
mm v[80:83],a[112:115],v[232:235],v[80:83]
ex v209,v209
mm v[112:115],a[112:115],v[236:239],v[112:115]
rd a[112:115],v248 offset:12288
mm v[116:119],a[116:119],v[236:239],v[116:119]
ex v210,v210
mm v[84:87],a[116:119],v[232:235],v[84:87]
rd a[116:119],v248 offset:13312
ld a[16:19],v255,s[52:55],s95 offen offset:0 sc1
mm v[88:91],a[120:123],v[232:235],v[88:91]
ex v211,v211
mm v[120:123],a[120:123],v[236:239],v[120:123]
rd a[120:123],v248 offset:14336
s_bitcmp1_b64 s[98:99], s85
mm v[124:127],a[124:127],v[236:239],v[124:127]
ex v216,v216
mm v[92:95],a[124:127],v[232:235],v[92:95]
rd a[124:127],v248 offset:15360
v_or_b32 v248, s89, v255
v_smfmac_f32_16x16x64_bf16 a[196:199], a[208:211], v[232:239], v241
wt lgkmcnt(4)
v_smfmac_f32_16x16x64_bf16 a[192:195], a[208:211], v[224:231], v241
rd a[64:67],v248 offset:0
mm v[0:3],a[96:99],v[224:227],v[0:3]
ex v217,v217
mm v[32:35],a[96:99],v[228:231],v[32:35]
rd a[68:71],v248 offset:1024
cv v232,v208,v209
mm v[36:39],a[100:103],v[228:231],v[36:39]
ex v218,v218
mm v[4:7],a[100:103],v[224:227],v[4:7]
rd a[72:75],v248 offset:2048
cv v233,v210,v211
mm v[8:11],a[104:107],v[224:227],v[8:11]
ex v219,v219
mm v[40:43],a[104:107],v[228:231],v[40:43]
rd a[76:79],v248 offset:3072
cv v236,v216,v217
mm v[44:47],a[108:111],v[228:231],v[44:47]
ex v212,v212
mm v[12:15],a[108:111],v[224:227],v[12:15]
rd a[80:83],v248 offset:4096
wt lgkmcnt(5)
mm v[16:19],a[112:115],v[224:227],v[16:19]
ex v213,v213
mm v[48:51],a[112:115],v[228:231],v[48:51]
rd a[84:87],v248 offset:5120
cv v237,v218,v219
mm v[52:55],a[116:119],v[228:231],v[52:55]
ex v214,v214
mm v[20:23],a[116:119],v[224:227],v[20:23]
rd a[88:91],v248 offset:6144
cv v234,v212,v213
mm v[24:27],a[120:123],v[224:227],v[24:27]
ex v215,v215
mm v[56:59],a[120:123],v[228:231],v[56:59]
rd a[92:95],v248 offset:7168
cv v235,v214,v215
mm v[60:63],a[124:127],v[228:231],v[60:63]
ex v220,v220
mm v[28:31],a[124:127],v[224:227],v[28:31]
ld a[20:23],v255,s[52:55],s95 offen offset:1024 sc1
wt lgkmcnt(4)
mm v[192:195],a[64:67],v[128:131],0
ex v221,v221
mm v[192:195],a[68:71],v[132:135],v[192:195]
cv v238,v220,v221
s_add_u32 s85, s85, 1
mm v[192:195],a[72:75],v[136:139],v[192:195]
ex v222,v222
mm v[192:195],a[76:79],v[140:143],v[192:195]
ld v255,s[60:63],s17 offen offset:0 sc1 lds
mm v[200:203],a[76:79],v[156:159],0
ex v223,v223
mm v[200:203],a[72:75],v[152:155],v[200:203]
cv v239,v222,v223
s_add_u32 s89, s89, 16384
mm v[200:203],a[68:71],v[148:151],v[200:203]
ex v192,v192
mm v[200:203],a[64:67],v[144:147],v[200:203]
wt lgkmcnt(0)
s_and_b32 s89, s89, 0x1ffff
ld a[24:27],v255,s[52:55],s95 offen offset:2048 sc1
mm v[196:199],a[80:83],v[128:131],0
ex v193,v193
mm v[196:199],a[84:87],v[132:135],v[196:199]
cv v224,v192,v193
mm v[196:199],a[88:91],v[136:139],v[196:199]
ex v194,v194
mm v[196:199],a[92:95],v[140:143],v[196:199]
ld v255,s[60:63],s17 offen offset:1024 sc1 lds
mm v[204:207],a[92:95],v[156:159],0
ex v195,v195
mm v[204:207],a[88:91],v[152:155],v[204:207]
cv v225,v194,v195
mm v[204:207],a[84:87],v[148:151],v[204:207]
ex v200,v200
mm v[204:207],a[80:83],v[144:147],v[204:207]
ld v255,s[60:63],s17 offen offset:2048 sc1 lds
mm v[208:211],a[64:67],v[160:163],0
ex v201,v201
mm v[208:211],a[68:71],v[164:167],v[208:211]
cv v228,v200,v201
mm v[208:211],a[72:75],v[168:171],v[208:211]
ex v202,v202
mm v[208:211],a[76:79],v[172:175],v[208:211]
ld v255,s[60:63],s17 offen offset:3072 sc1 lds
mm v[216:219],a[76:79],v[188:191],0
ex v203,v203
mm v[216:219],a[72:75],v[184:187],v[216:219]
cv v229,v202,v203
mm v[216:219],a[68:71],v[180:183],v[216:219]
ex v196,v196
mm v[216:219],a[64:67],v[176:179],v[216:219]
ld a[28:31],v255,s[52:55],s95 offen offset:3072 sc1
mm v[212:215],a[80:83],v[160:163],0
ex v197,v197
mm v[212:215],a[84:87],v[164:167],v[212:215]
cv v226,v196,v197
s_add_u32 s17, s17, 8192
mm v[212:215],a[88:91],v[168:171],v[212:215]
ex v198,v198
mm v[212:215],a[92:95],v[172:175],v[212:215]
s_add_u32 m0, m0, 16384
s_and_b32 m0, m0, s78
ld a[32:35],v255,s[52:55],s96 offen offset:0 sc1
mm v[220:223],a[92:95],v[188:191],0
ex v199,v199
mm v[220:223],a[88:91],v[184:187],v[220:223]
cv v227,v198,v199
mm v[220:223],a[84:87],v[180:183],v[220:223]
ex v204,v204
mm v[220:223],a[80:83],v[176:179],v[220:223]
wt vmcnt(18)
s_barrier
mm v[64:67],a[96:99],v[232:235],v[64:67]
ex v205,v205
mm v[96:99],a[96:99],v[236:239],v[96:99]
rd a[96:99],v248 offset:8192
cv v230,v204,v205
mm v[100:103],a[100:103],v[236:239],v[100:103]
ex v206,v206
mm v[68:71],a[100:103],v[232:235],v[68:71]
rd a[100:103],v248 offset:9216
ld a[36:39],v255,s[52:55],s96 offen offset:1024 sc1
mm v[72:75],a[104:107],v[232:235],v[72:75]
ex v207,v207
mm v[104:107],a[104:107],v[236:239],v[104:107]
rd a[104:107],v248 offset:10240
cv v231,v206,v207
mm v[108:111],a[108:111],v[236:239],v[108:111]
ex v208,v208
mm v[76:79],a[108:111],v[232:235],v[76:79]
rd a[108:111],v248 offset:11264
ld a[40:43],v255,s[52:55],s96 offen offset:2048 sc1
mm v[80:83],a[112:115],v[232:235],v[80:83]
ex v209,v209
mm v[112:115],a[112:115],v[236:239],v[112:115]
rd a[112:115],v248 offset:12288
mm v[116:119],a[116:119],v[236:239],v[116:119]
ex v210,v210
mm v[84:87],a[116:119],v[232:235],v[84:87]
rd a[116:119],v248 offset:13312
ld a[44:47],v255,s[52:55],s96 offen offset:3072 sc1
mm v[88:91],a[120:123],v[232:235],v[88:91]
ex v211,v211
mm v[120:123],a[120:123],v[236:239],v[120:123]
rd a[120:123],v248 offset:14336
s_bitcmp1_b64 s[98:99], s85
mm v[124:127],a[124:127],v[236:239],v[124:127]
ex v216,v216
mm v[92:95],a[124:127],v[232:235],v[92:95]
rd a[124:127],v248 offset:15360
v_or_b32 v248, s89, v255
v_smfmac_f32_16x16x64_bf16 a[196:199], a[208:211], v[232:239], v241
wt lgkmcnt(4)
v_smfmac_f32_16x16x64_bf16 a[192:195], a[208:211], v[224:231], v241
rd a[64:67],v248 offset:0
mm v[0:3],a[96:99],v[224:227],v[0:3]
ex v217,v217
mm v[32:35],a[96:99],v[228:231],v[32:35]
rd a[68:71],v248 offset:1024
cv v232,v208,v209
mm v[36:39],a[100:103],v[228:231],v[36:39]
ex v218,v218
mm v[4:7],a[100:103],v[224:227],v[4:7]
rd a[72:75],v248 offset:2048
cv v233,v210,v211
mm v[8:11],a[104:107],v[224:227],v[8:11]
ex v219,v219
mm v[40:43],a[104:107],v[228:231],v[40:43]
rd a[76:79],v248 offset:3072
cv v236,v216,v217
mm v[44:47],a[108:111],v[228:231],v[44:47]
ex v212,v212
mm v[12:15],a[108:111],v[224:227],v[12:15]
rd a[80:83],v248 offset:4096
wt lgkmcnt(5)
mm v[16:19],a[112:115],v[224:227],v[16:19]
ex v213,v213
mm v[48:51],a[112:115],v[228:231],v[48:51]
rd a[84:87],v248 offset:5120
cv v237,v218,v219
mm v[52:55],a[116:119],v[228:231],v[52:55]
ex v214,v214
mm v[20:23],a[116:119],v[224:227],v[20:23]
rd a[88:91],v248 offset:6144
cv v234,v212,v213
mm v[24:27],a[120:123],v[224:227],v[24:27]
ex v215,v215
mm v[56:59],a[120:123],v[228:231],v[56:59]
rd a[92:95],v248 offset:7168
cv v235,v214,v215
mm v[60:63],a[124:127],v[228:231],v[60:63]
ex v220,v220
mm v[28:31],a[124:127],v[224:227],v[28:31]
ld a[48:51],v255,s[52:55],s97 offen offset:0 sc1
wt lgkmcnt(4)
mm v[192:195],a[64:67],v[128:131],0
ex v221,v221
mm v[192:195],a[68:71],v[132:135],v[192:195]
cv v238,v220,v221
s_add_u32 s85, s85, 1
mm v[192:195],a[72:75],v[136:139],v[192:195]
ex v222,v222
mm v[192:195],a[76:79],v[140:143],v[192:195]
ld v255,s[60:63],s17 offen offset:0 sc1 lds
mm v[200:203],a[76:79],v[156:159],0
ex v223,v223
mm v[200:203],a[72:75],v[152:155],v[200:203]
cv v239,v222,v223
s_add_u32 s89, s89, 16384
mm v[200:203],a[68:71],v[148:151],v[200:203]
ex v192,v192
mm v[200:203],a[64:67],v[144:147],v[200:203]
wt lgkmcnt(0)
s_and_b32 s89, s89, 0x1ffff
ld a[52:55],v255,s[52:55],s97 offen offset:1024 sc1
mm v[196:199],a[80:83],v[128:131],0
ex v193,v193
mm v[196:199],a[84:87],v[132:135],v[196:199]
cv v224,v192,v193
mm v[196:199],a[88:91],v[136:139],v[196:199]
ex v194,v194
mm v[196:199],a[92:95],v[140:143],v[196:199]
ld v255,s[60:63],s17 offen offset:1024 sc1 lds
mm v[204:207],a[92:95],v[156:159],0
ex v195,v195
mm v[204:207],a[88:91],v[152:155],v[204:207]
cv v225,v194,v195
mm v[204:207],a[84:87],v[148:151],v[204:207]
ex v200,v200
mm v[204:207],a[80:83],v[144:147],v[204:207]
ld v255,s[60:63],s17 offen offset:2048 sc1 lds
mm v[208:211],a[64:67],v[160:163],0
ex v201,v201
mm v[208:211],a[68:71],v[164:167],v[208:211]
cv v228,v200,v201
mm v[208:211],a[72:75],v[168:171],v[208:211]
ex v202,v202
mm v[208:211],a[76:79],v[172:175],v[208:211]
ld v255,s[60:63],s17 offen offset:3072 sc1 lds
mm v[216:219],a[76:79],v[188:191],0
ex v203,v203
mm v[216:219],a[72:75],v[184:187],v[216:219]
cv v229,v202,v203
mm v[216:219],a[68:71],v[180:183],v[216:219]
ex v196,v196
mm v[216:219],a[64:67],v[176:179],v[216:219]
ld a[56:59],v255,s[52:55],s97 offen offset:2048 sc1
mm v[212:215],a[80:83],v[160:163],0
ex v197,v197
mm v[212:215],a[84:87],v[164:167],v[212:215]
cv v226,v196,v197
s_add_u32 s17, s17, 8192
mm v[212:215],a[88:91],v[168:171],v[212:215]
ex v198,v198
mm v[212:215],a[92:95],v[172:175],v[212:215]
s_add_u32 m0, m0, 16384
s_and_b32 m0, m0, s78
ld a[60:63],v255,s[52:55],s97 offen offset:3072 sc1
mm v[220:223],a[92:95],v[188:191],0
ex v199,v199
mm v[220:223],a[88:91],v[184:187],v[220:223]
cv v227,v198,v199
mm v[220:223],a[84:87],v[180:183],v[220:223]
ex v204,v204
mm v[220:223],a[80:83],v[176:179],v[220:223]
wt vmcnt(24)
s_barrier
mm v[64:67],a[96:99],v[232:235],v[64:67]
ex v205,v205
mm v[96:99],a[96:99],v[236:239],v[96:99]
rd a[96:99],v248 offset:8192
cv v230,v204,v205
mm v[100:103],a[100:103],v[236:239],v[100:103]
ex v206,v206
mm v[68:71],a[100:103],v[232:235],v[68:71]
rd a[100:103],v248 offset:9216
mm v[72:75],a[104:107],v[232:235],v[72:75]
ex v207,v207
mm v[104:107],a[104:107],v[236:239],v[104:107]
rd a[104:107],v248 offset:10240
cv v231,v206,v207
mm v[108:111],a[108:111],v[236:239],v[108:111]
ex v208,v208
mm v[76:79],a[108:111],v[232:235],v[76:79]
rd a[108:111],v248 offset:11264
mm v[80:83],a[112:115],v[232:235],v[80:83]
ex v209,v209
mm v[112:115],a[112:115],v[236:239],v[112:115]
rd a[112:115],v248 offset:12288
mm v[116:119],a[116:119],v[236:239],v[116:119]
ex v210,v210
mm v[84:87],a[116:119],v[232:235],v[84:87]
rd a[116:119],v248 offset:13312
mm v[88:91],a[120:123],v[232:235],v[88:91]
ex v211,v211
mm v[120:123],a[120:123],v[236:239],v[120:123]
rd a[120:123],v248 offset:14336
s_bitcmp1_b64 s[98:99], s85
mm v[124:127],a[124:127],v[236:239],v[124:127]
ex v216,v216
mm v[92:95],a[124:127],v[232:235],v[92:95]
rd a[124:127],v248 offset:15360
v_or_b32 v248, s89, v255
v_smfmac_f32_16x16x64_bf16 a[196:199], a[208:211], v[232:239], v241
wt lgkmcnt(4)
v_smfmac_f32_16x16x64_bf16 a[192:195], a[208:211], v[224:231], v241
rd a[64:67],v248 offset:0
mm v[0:3],a[96:99],v[224:227],v[0:3]
ex v217,v217
mm v[32:35],a[96:99],v[228:231],v[32:35]
rd a[68:71],v248 offset:1024
cv v232,v208,v209
mm v[36:39],a[100:103],v[228:231],v[36:39]
ex v218,v218
mm v[4:7],a[100:103],v[224:227],v[4:7]
rd a[72:75],v248 offset:2048
cv v233,v210,v211
mm v[8:11],a[104:107],v[224:227],v[8:11]
ex v219,v219
mm v[40:43],a[104:107],v[228:231],v[40:43]
rd a[76:79],v248 offset:3072
cv v236,v216,v217
mm v[44:47],a[108:111],v[228:231],v[44:47]
ex v212,v212
mm v[12:15],a[108:111],v[224:227],v[12:15]
rd a[80:83],v248 offset:4096
wt lgkmcnt(5)
mm v[16:19],a[112:115],v[224:227],v[16:19]
ex v213,v213
mm v[48:51],a[112:115],v[228:231],v[48:51]
rd a[84:87],v248 offset:5120
cv v237,v218,v219
mm v[52:55],a[116:119],v[228:231],v[52:55]
ex v214,v214
mm v[20:23],a[116:119],v[224:227],v[20:23]
rd a[88:91],v248 offset:6144
cv v234,v212,v213
mm v[24:27],a[120:123],v[224:227],v[24:27]
v_exp_f32_e64 v215, v215
mm v[56:59],a[120:123],v[228:231],v[56:59]
rd a[92:95],v248 offset:7168
cv v235,v214,v215
mm v[60:63],a[124:127],v[228:231],v[60:63]
v_exp_f32_e64 v220, v220
mm v[28:31],a[124:127],v[224:227],v[28:31]
.La5_LA_F1Y:
.La5_LW6_1:
s_cbranch_execz .La5_LW6_5
wt lgkmcnt(4)
wt vmcnt(0)
mm v[192:195],a[64:67],a[0:3],0
ex v221,v221
mm v[192:195],a[68:71],a[4:7],v[192:195]
cv v238,v220,v221
s_add_u32 s89, s89, 16384
mm v[192:195],a[72:75],a[8:11],v[192:195]
ex v222,v222
mm v[192:195],a[76:79],a[12:15],v[192:195]
ld v255,s[60:63],s17 offen offset:0 sc1 lds
ar v242,a192
ar v240,a193
v_add_f32 v242, v242, v240
mm v[200:203],a[76:79],a[28:31],0
ex v223,v223
ar v243,a194
ar v240,a195
v_add_f32 v243, v243, v240
mm v[200:203],a[72:75],a[24:27],v[200:203]
cv v239,v222,v223
s_and_b32 s89, s89, 0x1ffff
aw a192,0
aw a193,0
aw a194,0
mm v[200:203],a[68:71],a[20:23],v[200:203]
ex v192,v192
aw a195,0
v_subrev_u32 v240, 562036736, v242
mm v[200:203],a[64:67],a[16:19],v[200:203]
wt lgkmcnt(0)
v_rcp_f32 v242, v242
v_cmp_le_u32 vcc, 1006632960, v240
v_subrev_u32 v240, 562036736, v243
mm v[196:199],a[80:83],a[0:3],0
ex v193,v193
s_mov_b64 s[92:93], vcc
v_cmp_le_u32 vcc, 1006632960, v240
mm v[196:199],a[84:87],a[4:7],v[196:199]
cv v224,v192,v193
v_rcp_f32 v243, v243
mm v[196:199],a[88:91],a[8:11],v[196:199]
ex v194,v194
s_or_b64 vcc, vcc, s[92:93]
s_cbranch_scc1 .La5_LA_SC1
.La5_LA_SR1:
v_mul_f32 v0, v0, v242
mm v[196:199],a[92:95],a[12:15],v[196:199]
ld v255,s[60:63],s17 offen offset:1024 sc1 lds
v_mul_f32 v1, v1, v242
v_mul_f32 v2, v2, v242
v_mul_f32 v3, v3, v242
mm v[204:207],a[92:95],a[28:31],0
ex v195,v195
v_mul_f32 v4, v4, v242
v_mul_f32 v5, v5, v242
v_mul_f32 v6, v6, v242
mm v[204:207],a[88:91],a[24:27],v[204:207]
cv v225,v194,v195
v_mul_f32 v7, v7, v242
cv v128,v0,v1
cv v129,v2,v3
cv v130,v4,v5
mm v[204:207],a[84:87],a[20:23],v[204:207]
ex v200,v200
cv v131,v6,v7
v_mul_f32 v8, v8, v242
v_mul_f32 v9, v9, v242
mm v[204:207],a[80:83],a[16:19],v[204:207]
ld v255,s[60:63],s17 offen offset:2048 sc1 lds
v_mul_f32 v10, v10, v242
v_mul_f32 v11, v11, v242
v_mul_f32 v12, v12, v242
mm v[208:211],a[64:67],a[32:35],0
ex v201,v201
v_mul_f32 v13, v13, v242
v_mul_f32 v14, v14, v242
v_mul_f32 v15, v15, v242
mm v[208:211],a[68:71],a[36:39],v[208:211]
cv v228,v200,v201
cv v132,v8,v9
cv v133,v10,v11
cv v134,v12,v13
cv v135,v14,v15
mm v[208:211],a[72:75],a[40:43],v[208:211]
ex v202,v202
v_mul_f32 v16, v16, v242
v_mul_f32 v17, v17, v242
v_mul_f32 v18, v18, v242
mm v[208:211],a[76:79],a[44:47],v[208:211]
ld v255,s[60:63],s17 offen offset:3072 sc1 lds
v_mul_f32 v19, v19, v242
v_mul_f32 v20, v20, v242
v_mul_f32 v21, v21, v242
mm v[216:219],a[76:79],a[60:63],0
ex v203,v203
v_mul_f32 v22, v22, v242
v_mul_f32 v23, v23, v242
cv v136,v16,v17
mm v[216:219],a[72:75],a[56:59],v[216:219]
cv v229,v202,v203
cv v137,v18,v19
cv v138,v20,v21
cv v139,v22,v23
v_mul_f32 v24, v24, v242
mm v[216:219],a[68:71],a[52:55],v[216:219]
ex v196,v196
v_mul_f32 v25, v25, v242
v_mul_f32 v26, v26, v242
v_mul_f32 v27, v27, v242
mm v[216:219],a[64:67],a[48:51],v[216:219]
v_mul_f32 v28, v28, v242
v_mul_f32 v29, v29, v242
v_mul_f32 v30, v30, v242
v_mul_f32 v31, v31, v242
cv v140,v24,v25
mm v[212:215],a[80:83],a[32:35],0
ex v197,v197
s_add_u32 s17, s17, 8192
cv v141,v26,v27
cv v142,v28,v29
mm v[212:215],a[84:87],a[36:39],v[212:215]
cv v226,v196,v197
s_add_u32 m0, m0, 16384
s_and_b32 m0, m0, s78
cv v143,v30,v31
v_mul_f32 v32, v32, v243
mm v[212:215],a[88:91],a[40:43],v[212:215]
ex v198,v198
v_mul_f32 v33, v33, v243
v_mul_f32 v34, v34, v243
v_mul_f32 v35, v35, v243
mm v[212:215],a[92:95],a[44:47],v[212:215]
st v[128:131],v244,s[44:47],s91 offen offset:0 sc1
v_mul_f32 v36, v36, v243
v_mul_f32 v37, v37, v243
v_mul_f32 v38, v38, v243
mm v[220:223],a[92:95],a[60:63],0
ex v199,v199
v_mul_f32 v39, v39, v243
cv v144,v32,v33
cv v145,v34,v35
mm v[220:223],a[88:91],a[56:59],v[220:223]
cv v227,v198,v199
cv v146,v36,v37
cv v147,v38,v39
v_mul_f32 v40, v40, v243
v_mul_f32 v41, v41, v243
mm v[220:223],a[84:87],a[52:55],v[220:223]
ex v204,v204
v_mul_f32 v42, v42, v243
v_mul_f32 v43, v43, v243
v_mul_f32 v44, v44, v243
mm v[220:223],a[80:83],a[48:51],v[220:223]
s_nop 0
s_barrier
v_mul_f32 v45, v45, v243
v_mul_f32 v46, v46, v243
v_mul_f32 v47, v47, v243
mm v[64:67],a[96:99],v[232:235],v[64:67]
ex v205,v205
cv v148,v40,v41
cv v149,v42,v43
cv v150,v44,v45
mm v[96:99],a[96:99],v[236:239],v[96:99]
rd a[96:99],v248 offset:8192
cv v230,v204,v205
cv v151,v46,v47
v_mul_f32 v48, v48, v243
v_mul_f32 v49, v49, v243
mm v[100:103],a[100:103],v[236:239],v[100:103]
ex v206,v206
v_mul_f32 v50, v50, v243
v_mul_f32 v51, v51, v243
v_mul_f32 v52, v52, v243
mm v[68:71],a[100:103],v[232:235],v[68:71]
rd a[100:103],v248 offset:9216
st v[132:135],v244,s[44:47],s91 offen offset:1024 sc1
v_mul_f32 v53, v53, v243
.La5_LW6_2:
s_cbranch_execz .La5_LW6_6
v_mul_f32 v54, v54, v243
mm v[72:75],a[104:107],v[232:235],v[72:75]
ex v207,v207
v_mul_f32 v55, v55, v243
cv v152,v48,v49
cv v153,v50,v51
mm v[104:107],a[104:107],v[236:239],v[104:107]
rd a[104:107],v248 offset:10240
cv v231,v206,v207
cv v154,v52,v53
cv v155,v54,v55
v_mul_f32 v56, v56, v243
mm v[108:111],a[108:111],v[236:239],v[108:111]
ex v208,v208
v_mul_f32 v57, v57, v243
v_mul_f32 v58, v58, v243
v_mul_f32 v59, v59, v243
mm v[76:79],a[108:111],v[232:235],v[76:79]
rd a[108:111],v248 offset:11264
v_mul_f32 v60, v60, v243
v_mul_f32 v61, v61, v243
v_mul_f32 v62, v62, v243
v_mul_f32 v63, v63, v243
mm v[80:83],a[112:115],v[232:235],v[80:83]
ex v209,v209
cv v156,v56,v57
cv v157,v58,v59
cv v158,v60,v61
mm v[112:115],a[112:115],v[236:239],v[112:115]
rd a[112:115],v248 offset:12288
st v[136:139],v244,s[44:47],s91 offen offset:2048 sc1
cv v159,v62,v63
mm v[116:119],a[116:119],v[236:239],v[116:119]
ex v210,v210
mm v[84:87],a[116:119],v[232:235],v[84:87]
rd a[116:119],v248 offset:13312
mm v[88:91],a[120:123],v[232:235],v[88:91]
ex v211,v211
mm v[120:123],a[120:123],v[236:239],v[120:123]
rd a[120:123],v248 offset:14336
st v[140:143],v244,s[44:47],s91 offen offset:3072 sc1
mm v[124:127],a[124:127],v[236:239],v[124:127]
ex v216,v216
mm v[92:95],a[124:127],v[232:235],v[92:95]
rd a[124:127],v248 offset:15360
v_or_b32 v248, s89, v255
v_smfmac_f32_16x16x64_bf16 a[196:199], a[208:211], v[232:239], v241
wt lgkmcnt(4)
v_smfmac_f32_16x16x64_bf16 a[192:195], a[208:211], v[224:231], v241
rd a[64:67],v248 offset:0
mm v[0:3],a[96:99],v[224:227],0
ex v217,v217
mm v[32:35],a[96:99],v[228:231],0
rd a[68:71],v248 offset:1024
cv v232,v208,v209
ar v242,a196
ar v240,a197
v_add_f32 v242, v242, v240
mm v[36:39],a[100:103],v[228:231],0
ex v218,v218
ar v243,a198
ar v240,a199
v_add_f32 v243, v243, v240
mm v[4:7],a[100:103],v[224:227],0
rd a[72:75],v248 offset:2048
cv v233,v210,v211
st v[144:147],v245,s[44:47],s91 offen offset:0 sc1
aw a196,0
mm v[8:11],a[104:107],v[224:227],0
ex v219,v219
aw a197,0
aw a198,0
aw a199,0
mm v[40:43],a[104:107],v[228:231],0
rd a[76:79],v248 offset:3072
cv v236,v216,v217
v_subrev_u32 v240, 562036736, v242
v_rcp_f32 v242, v242
mm v[44:47],a[108:111],v[228:231],0
ex v212,v212
v_cmp_le_u32 vcc, 1006632960, v240
v_subrev_u32 v240, 562036736, v243
s_mov_b64 s[92:93], vcc
mm v[12:15],a[108:111],v[224:227],0
rd a[80:83],v248 offset:4096
wt lgkmcnt(5)
v_cmp_le_u32 vcc, 1006632960, v240
v_rcp_f32 v243, v243
mm v[16:19],a[112:115],v[224:227],0
ex v213,v213
s_or_b64 vcc, vcc, s[92:93]
s_cbranch_scc1 .La5_LA_SC2
.La5_LA_SR2:
v_mul_f32 v64, v64, v242
mm v[48:51],a[112:115],v[228:231],0
rd a[84:87],v248 offset:5120
cv v237,v218,v219
v_mul_f32 v65, v65, v242
v_mul_f32 v66, v66, v242
v_mul_f32 v67, v67, v242
mm v[52:55],a[116:119],v[228:231],0
ex v214,v214
v_mul_f32 v68, v68, v242
v_mul_f32 v69, v69, v242
v_mul_f32 v70, v70, v242
mm v[20:23],a[116:119],v[224:227],0
rd a[88:91],v248 offset:6144
cv v234,v212,v213
st v[148:151],v245,s[44:47],s91 offen offset:1024 sc1
v_mul_f32 v71, v71, v242
mm v[24:27],a[120:123],v[224:227],0
ex v215,v215
cv v160,v64,v65
cv v161,v66,v67
cv v162,v68,v69
mm v[56:59],a[120:123],v[228:231],0
rd a[92:95],v248 offset:7168
cv v235,v214,v215
cv v163,v70,v71
v_mul_f32 v72, v72, v242
v_mul_f32 v73, v73, v242
mm v[60:63],a[124:127],v[228:231],0
ex v220,v220
v_mul_f32 v74, v74, v242
v_mul_f32 v75, v75, v242
v_mul_f32 v76, v76, v242
mm v[28:31],a[124:127],v[224:227],0
s_add_u32 s85, s85, 1
v_mul_f32 v77, v77, v242
v_mul_f32 v78, v78, v242
v_mul_f32 v79, v79, v242
cv v164,v72,v73
wt lgkmcnt(4)
mm v[192:195],a[64:67],a[0:3],0
ex v221,v221
cv v165,v74,v75
cv v166,v76,v77
cv v167,v78,v79
mm v[192:195],a[68:71],a[4:7],v[192:195]
cv v238,v220,v221
s_add_u32 s89, s89, 16384
v_mul_f32 v80, v80, v242
v_mul_f32 v81, v81, v242
v_mul_f32 v82, v82, v242
mm v[192:195],a[72:75],a[8:11],v[192:195]
ex v222,v222
v_mul_f32 v83, v83, v242
v_mul_f32 v84, v84, v242
v_mul_f32 v85, v85, v242
mm v[192:195],a[76:79],a[12:15],v[192:195]
ld v255,s[60:63],s17 offen offset:0 sc1 lds
v_mul_f32 v86, v86, v242
v_mul_f32 v87, v87, v242
cv v168,v80,v81
mm v[200:203],a[76:79],a[28:31],0
ex v223,v223
cv v169,v82,v83
cv v170,v84,v85
cv v171,v86,v87
mm v[200:203],a[72:75],a[24:27],v[200:203]
cv v239,v222,v223
s_and_b32 s89, s89, 0x1ffff
v_mul_f32 v88, v88, v242
v_mul_f32 v89, v89, v242
v_mul_f32 v90, v90, v242
mm v[200:203],a[68:71],a[20:23],v[200:203]
ex v192,v192
v_mul_f32 v91, v91, v242
v_mul_f32 v92, v92, v242
v_mul_f32 v93, v93, v242
mm v[200:203],a[64:67],a[16:19],v[200:203]
wt lgkmcnt(0)
st v[152:155],v245,s[44:47],s91 offen offset:2048 sc1
v_mul_f32 v94, v94, v242
v_mul_f32 v95, v95, v242
mm v[196:199],a[80:83],a[0:3],0
ex v193,v193
cv v172,v88,v89
cv v173,v90,v91
cv v174,v92,v93
mm v[196:199],a[84:87],a[4:7],v[196:199]
cv v224,v192,v193
cv v175,v94,v95
v_mul_f32 v96, v96, v243
v_mul_f32 v97, v97, v243
v_mul_f32 v98, v98, v243
mm v[196:199],a[88:91],a[8:11],v[196:199]
ex v194,v194
v_mul_f32 v99, v99, v243
v_mul_f32 v100, v100, v243
v_mul_f32 v101, v101, v243
mm v[196:199],a[92:95],a[12:15],v[196:199]
ld v255,s[60:63],s17 offen offset:1024 sc1 lds
v_mul_f32 v102, v102, v243
v_mul_f32 v103, v103, v243
cv v176,v96,v97
mm v[204:207],a[92:95],a[28:31],0
ex v195,v195
cv v177,v98,v99
cv v178,v100,v101
cv v179,v102,v103
mm v[204:207],a[88:91],a[24:27],v[204:207]
cv v225,v194,v195
st v[156:159],v245,s[44:47],s91 offen offset:3072 sc1
v_mul_f32 v104, v104, v243
v_mul_f32 v105, v105, v243
mm v[204:207],a[84:87],a[20:23],v[204:207]
ex v200,v200
v_mul_f32 v106, v106, v243
v_mul_f32 v107, v107, v243
v_mul_f32 v108, v108, v243
mm v[204:207],a[80:83],a[16:19],v[204:207]
ld v255,s[60:63],s17 offen offset:2048 sc1 lds
v_mul_f32 v109, v109, v243
v_mul_f32 v110, v110, v243
v_mul_f32 v111, v111, v243
mm v[208:211],a[64:67],a[32:35],0
ex v201,v201
cv v180,v104,v105
cv v181,v106,v107
cv v182,v108,v109
mm v[208:211],a[68:71],a[36:39],v[208:211]
cv v228,v200,v201
cv v183,v110,v111
v_mul_f32 v112, v112, v243
v_mul_f32 v113, v113, v243
v_mul_f32 v114, v114, v243
mm v[208:211],a[72:75],a[40:43],v[208:211]
ex v202,v202
v_mul_f32 v115, v115, v243
v_mul_f32 v116, v116, v243
v_mul_f32 v117, v117, v243
mm v[208:211],a[76:79],a[44:47],v[208:211]
ld v255,s[60:63],s17 offen offset:3072 sc1 lds
v_mul_f32 v118, v118, v243
v_mul_f32 v119, v119, v243
cv v184,v112,v113
mm v[216:219],a[76:79],a[60:63],0
ex v203,v203
cv v185,v114,v115
cv v186,v116,v117
cv v187,v118,v119
mm v[216:219],a[72:75],a[56:59],v[216:219]
cv v229,v202,v203
v_mul_f32 v120, v120, v243
v_mul_f32 v121, v121, v243
v_mul_f32 v122, v122, v243
v_mul_f32 v123, v123, v243
mm v[216:219],a[68:71],a[52:55],v[216:219]
ex v196,v196
v_mul_f32 v124, v124, v243
v_mul_f32 v125, v125, v243
v_mul_f32 v126, v126, v243
mm v[216:219],a[64:67],a[48:51],v[216:219]
v_mul_f32 v127, v127, v243
cv v188,v120,v121
cv v189,v122,v123
cv v190,v124,v125
cv v191,v126,v127
mm v[212:215],a[80:83],a[32:35],0
ex v197,v197
s_add_u32 s17, s17, 8192
mm v[212:215],a[84:87],a[36:39],v[212:215]
cv v226,v196,v197
s_add_u32 m0, m0, 16384
s_and_b32 m0, m0, s78
mm v[212:215],a[88:91],a[40:43],v[212:215]
ex v198,v198
mm v[212:215],a[92:95],a[44:47],v[212:215]
st v[160:163],v246,s[44:47],s91 offen offset:0 sc1
mm v[220:223],a[92:95],a[60:63],0
ex v199,v199
mm v[220:223],a[88:91],a[56:59],v[220:223]
cv v227,v198,v199
mm v[220:223],a[84:87],a[52:55],v[220:223]
ex v204,v204
mm v[220:223],a[80:83],a[48:51],v[220:223]
wt vmcnt(18)
s_barrier
mm v[64:67],a[96:99],v[232:235],0
ex v205,v205
mm v[96:99],a[96:99],v[236:239],0
rd a[96:99],v248 offset:8192
cv v230,v204,v205
mm v[100:103],a[100:103],v[236:239],0
ex v206,v206
mm v[68:71],a[100:103],v[232:235],0
rd a[100:103],v248 offset:9216
st v[164:167],v246,s[44:47],s91 offen offset:1024 sc1
mm v[72:75],a[104:107],v[232:235],0
ex v207,v207
mm v[104:107],a[104:107],v[236:239],0
rd a[104:107],v248 offset:10240
cv v231,v206,v207
mm v[108:111],a[108:111],v[236:239],0
ex v208,v208
mm v[76:79],a[108:111],v[232:235],0
rd a[108:111],v248 offset:11264
st v[168:171],v246,s[44:47],s91 offen offset:2048 sc1
mm v[80:83],a[112:115],v[232:235],0
ex v209,v209
mm v[112:115],a[112:115],v[236:239],0
rd a[112:115],v248 offset:12288
mm v[116:119],a[116:119],v[236:239],0
ex v210,v210
mm v[84:87],a[116:119],v[232:235],0
rd a[116:119],v248 offset:13312
st v[172:175],v246,s[44:47],s91 offen offset:3072 sc1
mm v[88:91],a[120:123],v[232:235],0
ex v211,v211
mm v[120:123],a[120:123],v[236:239],0
rd a[120:123],v248 offset:14336
mm v[124:127],a[124:127],v[236:239],0
ex v216,v216
mm v[92:95],a[124:127],v[232:235],0
rd a[124:127],v248 offset:15360
v_or_b32 v248, s89, v255
st v[176:179],v247,s[44:47],s91 offen offset:0 sc1
v_smfmac_f32_16x16x64_bf16 a[196:199], a[208:211], v[232:239], v241
wt lgkmcnt(4)
v_smfmac_f32_16x16x64_bf16 a[192:195], a[208:211], v[224:231], v241
rd a[64:67],v248 offset:0
mm v[0:3],a[96:99],v[224:227],v[0:3]
ex v217,v217
mm v[32:35],a[96:99],v[228:231],v[32:35]
rd a[68:71],v248 offset:1024
cv v232,v208,v209
mm v[36:39],a[100:103],v[228:231],v[36:39]
ex v218,v218
mm v[4:7],a[100:103],v[224:227],v[4:7]
rd a[72:75],v248 offset:2048
cv v233,v210,v211
st v[180:183],v247,s[44:47],s91 offen offset:1024 sc1
mm v[8:11],a[104:107],v[224:227],v[8:11]
ex v219,v219
mm v[40:43],a[104:107],v[228:231],v[40:43]
rd a[76:79],v248 offset:3072
cv v236,v216,v217
mm v[44:47],a[108:111],v[228:231],v[44:47]
ex v212,v212
mm v[12:15],a[108:111],v[224:227],v[12:15]
rd a[80:83],v248 offset:4096
wt lgkmcnt(5)
mm v[16:19],a[112:115],v[224:227],v[16:19]
ex v213,v213
mm v[48:51],a[112:115],v[228:231],v[48:51]
rd a[84:87],v248 offset:5120
cv v237,v218,v219
st v[184:187],v247,s[44:47],s91 offen offset:2048 sc1
mm v[52:55],a[116:119],v[228:231],v[52:55]
ex v214,v214
mm v[20:23],a[116:119],v[224:227],v[20:23]
rd a[88:91],v248 offset:6144
cv v234,v212,v213
mm v[24:27],a[120:123],v[224:227],v[24:27]
ex v215,v215
mm v[56:59],a[120:123],v[228:231],v[56:59]
rd a[92:95],v248 offset:7168
cv v235,v214,v215
st v[188:191],v247,s[44:47],s91 offen offset:3072 sc1
mm v[60:63],a[124:127],v[228:231],v[60:63]
ex v220,v220
mm v[28:31],a[124:127],v[224:227],v[28:31]
s_add_u32 s85, s85, 1
s_cmp_eq_u32 s85, 22
s_cbranch_scc1 .La5_LA_F3
.p2align 6
.La5_LA_F2:
wt lgkmcnt(4)
mm v[192:195],a[64:67],a[0:3],0
ex v221,v221
mm v[192:195],a[68:71],a[4:7],v[192:195]
cv v238,v220,v221
s_add_u32 s85, s85, 1
mm v[192:195],a[72:75],a[8:11],v[192:195]
ex v222,v222
mm v[192:195],a[76:79],a[12:15],v[192:195]
ld v255,s[60:63],s17 offen offset:0 sc1 lds
mm v[200:203],a[76:79],a[28:31],0
ex v223,v223
mm v[200:203],a[72:75],a[24:27],v[200:203]
cv v239,v222,v223
s_add_u32 s89, s89, 16384
mm v[200:203],a[68:71],a[20:23],v[200:203]
ex v192,v192
mm v[200:203],a[64:67],a[16:19],v[200:203]
wt lgkmcnt(0)
s_and_b32 s89, s89, 0x1ffff
mm v[196:199],a[80:83],a[0:3],0
ex v193,v193
mm v[196:199],a[84:87],a[4:7],v[196:199]
cv v224,v192,v193
mm v[196:199],a[88:91],a[8:11],v[196:199]
ex v194,v194
mm v[196:199],a[92:95],a[12:15],v[196:199]
ld v255,s[60:63],s17 offen offset:1024 sc1 lds
mm v[204:207],a[92:95],a[28:31],0
ex v195,v195
mm v[204:207],a[88:91],a[24:27],v[204:207]
cv v225,v194,v195
mm v[204:207],a[84:87],a[20:23],v[204:207]
ex v200,v200
mm v[204:207],a[80:83],a[16:19],v[204:207]
ld v255,s[60:63],s17 offen offset:2048 sc1 lds
mm v[208:211],a[64:67],a[32:35],0
ex v201,v201
mm v[208:211],a[68:71],a[36:39],v[208:211]
cv v228,v200,v201
mm v[208:211],a[72:75],a[40:43],v[208:211]
ex v202,v202
mm v[208:211],a[76:79],a[44:47],v[208:211]
ld v255,s[60:63],s17 offen offset:3072 sc1 lds
mm v[216:219],a[76:79],a[60:63],0
ex v203,v203
mm v[216:219],a[72:75],a[56:59],v[216:219]
cv v229,v202,v203
mm v[216:219],a[68:71],a[52:55],v[216:219]
ex v196,v196
mm v[216:219],a[64:67],a[48:51],v[216:219]
buffer_load_dword a253, v254, s[36:39], s90 offen
mm v[212:215],a[80:83],a[32:35],0
ex v197,v197
mm v[212:215],a[84:87],a[36:39],v[212:215]
cv v226,v196,v197
s_add_u32 s17, s17, 8192
mm v[212:215],a[88:91],a[40:43],v[212:215]
ex v198,v198
mm v[212:215],a[92:95],a[44:47],v[212:215]
s_add_u32 m0, m0, 16384
s_and_b32 m0, m0, s78
mm v[220:223],a[92:95],a[60:63],0
ex v199,v199
mm v[220:223],a[88:91],a[56:59],v[220:223]
cv v227,v198,v199
s_add_u32 s90, s90, s21
mm v[220:223],a[84:87],a[52:55],v[220:223]
ex v204,v204
mm v[220:223],a[80:83],a[48:51],v[220:223]
wt vmcnt(11)
s_barrier
mm v[64:67],a[96:99],v[232:235],v[64:67]
ex v205,v205
mm v[96:99],a[96:99],v[236:239],v[96:99]
rd a[96:99],v248 offset:8192
cv v230,v204,v205
mm v[100:103],a[100:103],v[236:239],v[100:103]
ex v206,v206
mm v[68:71],a[100:103],v[232:235],v[68:71]
rd a[100:103],v248 offset:9216
mm v[72:75],a[104:107],v[232:235],v[72:75]
ex v207,v207
mm v[104:107],a[104:107],v[236:239],v[104:107]
rd a[104:107],v248 offset:10240
cv v231,v206,v207
mm v[108:111],a[108:111],v[236:239],v[108:111]
ex v208,v208
mm v[76:79],a[108:111],v[232:235],v[76:79]
rd a[108:111],v248 offset:11264
mm v[80:83],a[112:115],v[232:235],v[80:83]
ex v209,v209
mm v[112:115],a[112:115],v[236:239],v[112:115]
rd a[112:115],v248 offset:12288
mm v[116:119],a[116:119],v[236:239],v[116:119]
ex v210,v210
mm v[84:87],a[116:119],v[232:235],v[84:87]
rd a[116:119],v248 offset:13312
mm v[88:91],a[120:123],v[232:235],v[88:91]
ex v211,v211
mm v[120:123],a[120:123],v[236:239],v[120:123]
rd a[120:123],v248 offset:14336
s_bitcmp1_b64 s[98:99], s85
mm v[124:127],a[124:127],v[236:239],v[124:127]
ex v216,v216
mm v[92:95],a[124:127],v[232:235],v[92:95]
rd a[124:127],v248 offset:15360
v_or_b32 v248, s89, v255
v_smfmac_f32_16x16x64_bf16 a[196:199], a[208:211], v[232:239], v241
wt lgkmcnt(4)
v_smfmac_f32_16x16x64_bf16 a[192:195], a[208:211], v[224:231], v241
rd a[64:67],v248 offset:0
mm v[0:3],a[96:99],v[224:227],v[0:3]
ex v217,v217
mm v[32:35],a[96:99],v[228:231],v[32:35]
rd a[68:71],v248 offset:1024
cv v232,v208,v209
mm v[36:39],a[100:103],v[228:231],v[36:39]
ex v218,v218
mm v[4:7],a[100:103],v[224:227],v[4:7]
rd a[72:75],v248 offset:2048
cv v233,v210,v211
mm v[8:11],a[104:107],v[224:227],v[8:11]
ex v219,v219
mm v[40:43],a[104:107],v[228:231],v[40:43]
rd a[76:79],v248 offset:3072
cv v236,v216,v217
mm v[44:47],a[108:111],v[228:231],v[44:47]
ex v212,v212
mm v[12:15],a[108:111],v[224:227],v[12:15]
rd a[80:83],v248 offset:4096
wt lgkmcnt(5)
mm v[16:19],a[112:115],v[224:227],v[16:19]
ex v213,v213
mm v[48:51],a[112:115],v[228:231],v[48:51]
rd a[84:87],v248 offset:5120
cv v237,v218,v219
mm v[52:55],a[116:119],v[228:231],v[52:55]
ex v214,v214
mm v[20:23],a[116:119],v[224:227],v[20:23]
rd a[88:91],v248 offset:6144
cv v234,v212,v213
mm v[24:27],a[120:123],v[224:227],v[24:27]
ex v215,v215
mm v[56:59],a[120:123],v[228:231],v[56:59]
rd a[92:95],v248 offset:7168
cv v235,v214,v215
mm v[60:63],a[124:127],v[228:231],v[60:63]
ex v220,v220
mm v[28:31],a[124:127],v[224:227],v[28:31]
s_cbranch_scc1 .La5_LA_F2E2
wt lgkmcnt(4)
mm v[192:195],a[64:67],a[0:3],0
ex v221,v221
mm v[192:195],a[68:71],a[4:7],v[192:195]
cv v238,v220,v221
s_add_u32 s85, s85, 1
mm v[192:195],a[72:75],a[8:11],v[192:195]
ex v222,v222
mm v[192:195],a[76:79],a[12:15],v[192:195]
ld v255,s[60:63],s17 offen offset:0 sc1 lds
mm v[200:203],a[76:79],a[28:31],0
ex v223,v223
mm v[200:203],a[72:75],a[24:27],v[200:203]
cv v239,v222,v223
s_add_u32 s89, s89, 16384
mm v[200:203],a[68:71],a[20:23],v[200:203]
ex v192,v192
mm v[200:203],a[64:67],a[16:19],v[200:203]
wt lgkmcnt(0)
s_and_b32 s89, s89, 0x1ffff
mm v[196:199],a[80:83],a[0:3],0
ex v193,v193
mm v[196:199],a[84:87],a[4:7],v[196:199]
cv v224,v192,v193
mm v[196:199],a[88:91],a[8:11],v[196:199]
ex v194,v194
mm v[196:199],a[92:95],a[12:15],v[196:199]
ld v255,s[60:63],s17 offen offset:1024 sc1 lds
mm v[204:207],a[92:95],a[28:31],0
ex v195,v195
mm v[204:207],a[88:91],a[24:27],v[204:207]
cv v225,v194,v195
mm v[204:207],a[84:87],a[20:23],v[204:207]
ex v200,v200
mm v[204:207],a[80:83],a[16:19],v[204:207]
ld v255,s[60:63],s17 offen offset:2048 sc1 lds
mm v[208:211],a[64:67],a[32:35],0
ex v201,v201
mm v[208:211],a[68:71],a[36:39],v[208:211]
cv v228,v200,v201
mm v[208:211],a[72:75],a[40:43],v[208:211]
ex v202,v202
mm v[208:211],a[76:79],a[44:47],v[208:211]
ld v255,s[60:63],s17 offen offset:3072 sc1 lds
mm v[216:219],a[76:79],a[60:63],0
ex v203,v203
mm v[216:219],a[72:75],a[56:59],v[216:219]
cv v229,v202,v203
mm v[216:219],a[68:71],a[52:55],v[216:219]
ex v196,v196
mm v[216:219],a[64:67],a[48:51],v[216:219]
buffer_load_dword a253, v254, s[36:39], s90 offen
mm v[212:215],a[80:83],a[32:35],0
ex v197,v197
mm v[212:215],a[84:87],a[36:39],v[212:215]
cv v226,v196,v197
s_add_u32 s17, s17, 8192
mm v[212:215],a[88:91],a[40:43],v[212:215]
ex v198,v198
mm v[212:215],a[92:95],a[44:47],v[212:215]
s_add_u32 m0, m0, 16384
s_and_b32 m0, m0, s78
mm v[220:223],a[92:95],a[60:63],0
ex v199,v199
mm v[220:223],a[88:91],a[56:59],v[220:223]
cv v227,v198,v199
s_add_u32 s90, s90, s21
mm v[220:223],a[84:87],a[52:55],v[220:223]
ex v204,v204
mm v[220:223],a[80:83],a[48:51],v[220:223]
wt vmcnt(11)
s_barrier
mm v[64:67],a[96:99],v[232:235],v[64:67]
ex v205,v205
mm v[96:99],a[96:99],v[236:239],v[96:99]
rd a[96:99],v248 offset:8192
cv v230,v204,v205
mm v[100:103],a[100:103],v[236:239],v[100:103]
ex v206,v206
mm v[68:71],a[100:103],v[232:235],v[68:71]
rd a[100:103],v248 offset:9216
mm v[72:75],a[104:107],v[232:235],v[72:75]
ex v207,v207
mm v[104:107],a[104:107],v[236:239],v[104:107]
rd a[104:107],v248 offset:10240
cv v231,v206,v207
mm v[108:111],a[108:111],v[236:239],v[108:111]
ex v208,v208
mm v[76:79],a[108:111],v[232:235],v[76:79]
rd a[108:111],v248 offset:11264
mm v[80:83],a[112:115],v[232:235],v[80:83]
ex v209,v209
mm v[112:115],a[112:115],v[236:239],v[112:115]
rd a[112:115],v248 offset:12288
mm v[116:119],a[116:119],v[236:239],v[116:119]
ex v210,v210
mm v[84:87],a[116:119],v[232:235],v[84:87]
rd a[116:119],v248 offset:13312
mm v[88:91],a[120:123],v[232:235],v[88:91]
ex v211,v211
mm v[120:123],a[120:123],v[236:239],v[120:123]
rd a[120:123],v248 offset:14336
.La5_LW6_3:
s_cbranch_execz .La5_LW6_7
s_bitcmp1_b64 s[98:99], s85
mm v[124:127],a[124:127],v[236:239],v[124:127]
ex v216,v216
mm v[92:95],a[124:127],v[232:235],v[92:95]
rd a[124:127],v248 offset:15360
v_or_b32 v248, s89, v255
v_smfmac_f32_16x16x64_bf16 a[196:199], a[208:211], v[232:239], v241
wt lgkmcnt(4)
v_smfmac_f32_16x16x64_bf16 a[192:195], a[208:211], v[224:231], v241
rd a[64:67],v248 offset:0
mm v[0:3],a[96:99],v[224:227],v[0:3]
ex v217,v217
mm v[32:35],a[96:99],v[228:231],v[32:35]
rd a[68:71],v248 offset:1024
cv v232,v208,v209
mm v[36:39],a[100:103],v[228:231],v[36:39]
ex v218,v218
mm v[4:7],a[100:103],v[224:227],v[4:7]
rd a[72:75],v248 offset:2048
cv v233,v210,v211
mm v[8:11],a[104:107],v[224:227],v[8:11]
ex v219,v219
mm v[40:43],a[104:107],v[228:231],v[40:43]
rd a[76:79],v248 offset:3072
cv v236,v216,v217
mm v[44:47],a[108:111],v[228:231],v[44:47]
ex v212,v212
mm v[12:15],a[108:111],v[224:227],v[12:15]
rd a[80:83],v248 offset:4096
wt lgkmcnt(5)
mm v[16:19],a[112:115],v[224:227],v[16:19]
ex v213,v213
mm v[48:51],a[112:115],v[228:231],v[48:51]
rd a[84:87],v248 offset:5120
cv v237,v218,v219
mm v[52:55],a[116:119],v[228:231],v[52:55]
ex v214,v214
mm v[20:23],a[116:119],v[224:227],v[20:23]
rd a[88:91],v248 offset:6144
cv v234,v212,v213
mm v[24:27],a[120:123],v[224:227],v[24:27]
ex v215,v215
mm v[56:59],a[120:123],v[228:231],v[56:59]
rd a[92:95],v248 offset:7168
cv v235,v214,v215
mm v[60:63],a[124:127],v[228:231],v[60:63]
ex v220,v220
mm v[28:31],a[124:127],v[224:227],v[28:31]
s_cbranch_scc0 .La5_LA_F2
.La5_LA_F2E2:
.La5_LA_F3:
.La5_LW6_4:
s_cbranch_execz .La5_LW6_R0
wt lgkmcnt(4)
mm v[192:195],a[64:67],a[0:3],0
ex v221,v221
mm v[192:195],a[68:71],a[4:7],v[192:195]
cv v238,v220,v221
s_add_u32 s89, s89, 16384
mm v[192:195],a[72:75],a[8:11],v[192:195]
ex v222,v222
mm v[192:195],a[76:79],a[12:15],v[192:195]
ld v255,s[60:63],s17 offen offset:0 sc1 lds
mm v[200:203],a[76:79],a[28:31],0
ex v223,v223
mm v[200:203],a[72:75],a[24:27],v[200:203]
cv v239,v222,v223
s_and_b32 s89, s89, 0x1ffff
mm v[200:203],a[68:71],a[20:23],v[200:203]
ex v192,v192
mm v[200:203],a[64:67],a[16:19],v[200:203]
wt lgkmcnt(0)
mm v[196:199],a[80:83],a[0:3],0
ex v193,v193
mm v[196:199],a[84:87],a[4:7],v[196:199]
cv v224,v192,v193
ar v243,a229
mm v[196:199],a[88:91],a[8:11],v[196:199]
ex v194,v194
mm v[196:199],a[92:95],a[12:15],v[196:199]
ld v255,s[60:63],s17 offen offset:1024 sc1 lds
mm v[204:207],a[92:95],a[28:31],0
ex v195,v195
mm v[204:207],a[88:91],a[24:27],v[204:207]
cv v225,v194,v195
mm v[204:207],a[84:87],a[20:23],v[204:207]
ex v200,v200
mm v[204:207],a[80:83],a[16:19],v[204:207]
ld v255,s[60:63],s17 offen offset:2048 sc1 lds
mm v[208:211],a[64:67],a[32:35],0
ex v201,v201
mm v[208:211],a[68:71],a[36:39],v[208:211]
cv v228,v200,v201
mm v[208:211],a[72:75],a[40:43],v[208:211]
ex v202,v202
mm v[208:211],a[76:79],a[44:47],v[208:211]
ld v255,s[60:63],s17 offen offset:3072 sc1 lds
mm v[216:219],a[76:79],a[60:63],0
ex v203,v203
mm v[216:219],a[72:75],a[56:59],v[216:219]
cv v229,v202,v203
s_mov_b32 s93, 0
mm v[216:219],a[68:71],a[52:55],v[216:219]
ex v196,v196
mm v[216:219],a[64:67],a[48:51],v[216:219]
ld a[128:131],v243,s[40:43],s93 offen
mm v[212:215],a[80:83],a[32:35],0
ex v197,v197
s_add_u32 s17, s17, 8192
mm v[212:215],a[84:87],a[36:39],v[212:215]
cv v226,v196,v197
s_mov_b32 s93, 7168
s_add_u32 m0, m0, 16384
mm v[212:215],a[88:91],a[40:43],v[212:215]
ex v198,v198
s_and_b32 m0, m0, s78
mm v[212:215],a[92:95],a[44:47],v[212:215]
ld a[132:135],v243,s[40:43],s93 offen
mm v[220:223],a[92:95],a[60:63],0
ex v199,v199
mm v[220:223],a[88:91],a[56:59],v[220:223]
cv v227,v198,v199
mm v[220:223],a[84:87],a[52:55],v[220:223]
ex v204,v204
mm v[220:223],a[80:83],a[48:51],v[220:223]
wt vmcnt(12)
s_barrier
mm v[64:67],a[96:99],v[232:235],v[64:67]
ex v205,v205
mm v[96:99],a[96:99],v[236:239],v[96:99]
rd a[96:99],v248 offset:8192
cv v230,v204,v205
mm v[100:103],a[100:103],v[236:239],v[100:103]
ex v206,v206
mm v[68:71],a[100:103],v[232:235],v[68:71]
rd a[100:103],v248 offset:9216
mm v[72:75],a[104:107],v[232:235],v[72:75]
ex v207,v207
mm v[104:107],a[104:107],v[236:239],v[104:107]
rd a[104:107],v248 offset:10240
cv v231,v206,v207
s_mov_b32 s93, 14336
mm v[108:111],a[108:111],v[236:239],v[108:111]
ex v208,v208
mm v[76:79],a[108:111],v[232:235],v[76:79]
rd a[108:111],v248 offset:11264
ld a[136:139],v243,s[40:43],s93 offen
mm v[80:83],a[112:115],v[232:235],v[80:83]
ex v209,v209
mm v[112:115],a[112:115],v[236:239],v[112:115]
rd a[112:115],v248 offset:12288
mm v[116:119],a[116:119],v[236:239],v[116:119]
ex v210,v210
mm v[84:87],a[116:119],v[232:235],v[84:87]
rd a[116:119],v248 offset:13312
mm v[88:91],a[120:123],v[232:235],v[88:91]
ex v211,v211
mm v[120:123],a[120:123],v[236:239],v[120:123]
rd a[120:123],v248 offset:14336
mm v[124:127],a[124:127],v[236:239],v[124:127]
ex v216,v216
mm v[92:95],a[124:127],v[232:235],v[92:95]
rd a[124:127],v248 offset:15360
v_or_b32 v248, s89, v255
v_smfmac_f32_16x16x64_bf16 a[196:199], a[208:211], v[232:239], v241
wt lgkmcnt(4)
v_smfmac_f32_16x16x64_bf16 a[192:195], a[208:211], v[224:231], v241
rd a[64:67],v248 offset:0
mm v[0:3],a[96:99],v[224:227],v[0:3]
ex v217,v217
mm v[32:35],a[96:99],v[228:231],v[32:35]
rd a[68:71],v248 offset:1024
cv v232,v208,v209
mm v[36:39],a[100:103],v[228:231],v[36:39]
ex v218,v218
mm v[4:7],a[100:103],v[224:227],v[4:7]
rd a[72:75],v248 offset:2048
cv v233,v210,v211
mm v[8:11],a[104:107],v[224:227],v[8:11]
ex v219,v219
mm v[40:43],a[104:107],v[228:231],v[40:43]
rd a[76:79],v248 offset:3072
cv v236,v216,v217
mm v[44:47],a[108:111],v[228:231],v[44:47]
ex v212,v212
mm v[12:15],a[108:111],v[224:227],v[12:15]
rd a[80:83],v248 offset:4096
wt lgkmcnt(5)
mm v[16:19],a[112:115],v[224:227],v[16:19]
ex v213,v213
mm v[48:51],a[112:115],v[228:231],v[48:51]
rd a[84:87],v248 offset:5120
cv v237,v218,v219
mm v[52:55],a[116:119],v[228:231],v[52:55]
ex v214,v214
mm v[20:23],a[116:119],v[224:227],v[20:23]
rd a[88:91],v248 offset:6144
cv v234,v212,v213
mm v[24:27],a[120:123],v[224:227],v[24:27]
ex v215,v215
mm v[56:59],a[120:123],v[228:231],v[56:59]
rd a[92:95],v248 offset:7168
cv v235,v214,v215
mm v[60:63],a[124:127],v[228:231],v[60:63]
ex v220,v220
mm v[28:31],a[124:127],v[224:227],v[28:31]
wt lgkmcnt(4)
mm v[192:195],a[64:67],a[0:3],0
ex v221,v221
mm v[192:195],a[68:71],a[4:7],v[192:195]
cv v238,v220,v221
s_add_u32 s89, s89, 16384
mm v[192:195],a[72:75],a[8:11],v[192:195]
ex v222,v222
mm v[192:195],a[76:79],a[12:15],v[192:195]
ld v255,s[60:63],s17 offen offset:0 sc1 lds
mm v[200:203],a[76:79],a[28:31],0
ex v223,v223
mm v[200:203],a[72:75],a[24:27],v[200:203]
cv v239,v222,v223
s_and_b32 s89, s89, 0x1ffff
mm v[200:203],a[68:71],a[20:23],v[200:203]
ex v192,v192
mm v[200:203],a[64:67],a[16:19],v[200:203]
wt lgkmcnt(0)
mm v[196:199],a[80:83],a[0:3],0
ex v193,v193
mm v[196:199],a[84:87],a[4:7],v[196:199]
cv v224,v192,v193
mm v[196:199],a[88:91],a[8:11],v[196:199]
ex v194,v194
mm v[196:199],a[92:95],a[12:15],v[196:199]
ld v255,s[60:63],s17 offen offset:1024 sc1 lds
mm v[204:207],a[92:95],a[28:31],0
ex v195,v195
mm v[204:207],a[88:91],a[24:27],v[204:207]
cv v225,v194,v195
mm v[204:207],a[84:87],a[20:23],v[204:207]
ex v200,v200
mm v[204:207],a[80:83],a[16:19],v[204:207]
ld v255,s[60:63],s17 offen offset:2048 sc1 lds
mm v[208:211],a[64:67],a[32:35],0
ex v201,v201
mm v[208:211],a[68:71],a[36:39],v[208:211]
cv v228,v200,v201
mm v[208:211],a[72:75],a[40:43],v[208:211]
ex v202,v202
mm v[208:211],a[76:79],a[44:47],v[208:211]
ld v255,s[60:63],s17 offen offset:3072 sc1 lds
mm v[216:219],a[76:79],a[60:63],0
ex v203,v203
mm v[216:219],a[72:75],a[56:59],v[216:219]
cv v229,v202,v203
s_mov_b32 s93, 21504
mm v[216:219],a[68:71],a[52:55],v[216:219]
ex v196,v196
mm v[216:219],a[64:67],a[48:51],v[216:219]
ld a[140:143],v243,s[40:43],s93 offen
mm v[212:215],a[80:83],a[32:35],0
ex v197,v197
s_add_u32 s17, s17, 8192
mm v[212:215],a[84:87],a[36:39],v[212:215]
cv v226,v196,v197
s_mov_b32 s93, 28672
s_add_u32 m0, m0, 16384
mm v[212:215],a[88:91],a[40:43],v[212:215]
ex v198,v198
s_and_b32 m0, m0, s78
mm v[212:215],a[92:95],a[44:47],v[212:215]
ld a[144:147],v243,s[40:43],s93 offen
mm v[220:223],a[92:95],a[60:63],0
ex v199,v199
mm v[220:223],a[88:91],a[56:59],v[220:223]
cv v227,v198,v199
mm v[220:223],a[84:87],a[52:55],v[220:223]
ex v204,v204
mm v[220:223],a[80:83],a[48:51],v[220:223]
wt vmcnt(14)
s_barrier
mm v[64:67],a[96:99],v[232:235],v[64:67]
ex v205,v205
mm v[96:99],a[96:99],v[236:239],v[96:99]
rd a[96:99],v248 offset:8192
cv v230,v204,v205
mm v[100:103],a[100:103],v[236:239],v[100:103]
ex v206,v206
mm v[68:71],a[100:103],v[232:235],v[68:71]
rd a[100:103],v248 offset:9216
mm v[72:75],a[104:107],v[232:235],v[72:75]
ex v207,v207
mm v[104:107],a[104:107],v[236:239],v[104:107]
rd a[104:107],v248 offset:10240
cv v231,v206,v207
s_mov_b32 s93, 35840
mm v[108:111],a[108:111],v[236:239],v[108:111]
ex v208,v208
mm v[76:79],a[108:111],v[232:235],v[76:79]
rd a[108:111],v248 offset:11264
ld a[148:151],v243,s[40:43],s93 offen
mm v[80:83],a[112:115],v[232:235],v[80:83]
ex v209,v209
mm v[112:115],a[112:115],v[236:239],v[112:115]
rd a[112:115],v248 offset:12288
mm v[116:119],a[116:119],v[236:239],v[116:119]
ex v210,v210
mm v[84:87],a[116:119],v[232:235],v[84:87]
rd a[116:119],v248 offset:13312
mm v[88:91],a[120:123],v[232:235],v[88:91]
ex v211,v211
mm v[120:123],a[120:123],v[236:239],v[120:123]
rd a[120:123],v248 offset:14336
ar v242,a231
wt vmcnt(14)
mm v[124:127],a[124:127],v[236:239],v[124:127]
ex v216,v216
mm v[92:95],a[124:127],v[232:235],v[92:95]
rd a[124:127],v248 offset:15360
v_or_b32 v248, s89, v255
v_smfmac_f32_16x16x64_bf16 a[196:199], a[208:211], v[232:239], v241
wt lgkmcnt(4)
v_smfmac_f32_16x16x64_bf16 a[192:195], a[208:211], v[224:231], v241
rd a[64:67],v248 offset:0
mm v[0:3],a[96:99],v[224:227],v[0:3]
ex v217,v217
mm v[32:35],a[96:99],v[228:231],v[32:35]
rd a[68:71],v248 offset:1024
cv v232,v208,v209
mm v[36:39],a[100:103],v[228:231],v[36:39]
ex v218,v218
mm v[4:7],a[100:103],v[224:227],v[4:7]
rd a[72:75],v248 offset:2048
cv v233,v210,v211
mm v[8:11],a[104:107],v[224:227],v[8:11]
ex v219,v219
mm v[40:43],a[104:107],v[228:231],v[40:43]
rd a[76:79],v248 offset:3072
cv v236,v216,v217
mm v[44:47],a[108:111],v[228:231],v[44:47]
ex v212,v212
mm v[12:15],a[108:111],v[224:227],v[12:15]
rd a[80:83],v248 offset:4096
wt lgkmcnt(5)
mm v[16:19],a[112:115],v[224:227],v[16:19]
ex v213,v213
mm v[48:51],a[112:115],v[228:231],v[48:51]
rd a[84:87],v248 offset:5120
cv v237,v218,v219
mm v[52:55],a[116:119],v[228:231],v[52:55]
ex v214,v214
mm v[20:23],a[116:119],v[224:227],v[20:23]
rd a[88:91],v248 offset:6144
cv v234,v212,v213
mm v[24:27],a[120:123],v[224:227],v[24:27]
ex v215,v215
mm v[56:59],a[120:123],v[228:231],v[56:59]
rd a[92:95],v248 offset:7168
cv v235,v214,v215
mm v[60:63],a[124:127],v[228:231],v[60:63]
ex v220,v220
mm v[28:31],a[124:127],v[224:227],v[28:31]
buffer_store_dwordx2 v[252:253], v242, s[64:67], s6 offen offset:0 sc1
wt lgkmcnt(4)
mm v[192:195],a[64:67],a[0:3],0
ex v221,v221
mm v[192:195],a[68:71],a[4:7],v[192:195]
cv v238,v220,v221
s_add_u32 s89, s89, 16384
mm v[192:195],a[72:75],a[8:11],v[192:195]
ex v222,v222
mm v[192:195],a[76:79],a[12:15],v[192:195]
ld v255,s[60:63],s17 offen offset:0 sc1 lds
mm v[200:203],a[76:79],a[28:31],0
ex v223,v223
mm v[200:203],a[72:75],a[24:27],v[200:203]
cv v239,v222,v223
s_and_b32 s89, s89, 0x1ffff
mm v[200:203],a[68:71],a[20:23],v[200:203]
ex v192,v192
mm v[200:203],a[64:67],a[16:19],v[200:203]
wt lgkmcnt(0)
mm v[196:199],a[80:83],a[0:3],0
ex v193,v193
mm v[196:199],a[84:87],a[4:7],v[196:199]
cv v224,v192,v193
mm v[196:199],a[88:91],a[8:11],v[196:199]
ex v194,v194
mm v[196:199],a[92:95],a[12:15],v[196:199]
ld v255,s[60:63],s17 offen offset:1024 sc1 lds
mm v[204:207],a[92:95],a[28:31],0
ex v195,v195
mm v[204:207],a[88:91],a[24:27],v[204:207]
cv v225,v194,v195
mm v[204:207],a[84:87],a[20:23],v[204:207]
ex v200,v200
mm v[204:207],a[80:83],a[16:19],v[204:207]
ld v255,s[60:63],s17 offen offset:2048 sc1 lds
mm v[208:211],a[64:67],a[32:35],0
ex v201,v201
mm v[208:211],a[68:71],a[36:39],v[208:211]
cv v228,v200,v201
mm v[208:211],a[72:75],a[40:43],v[208:211]
ex v202,v202
mm v[208:211],a[76:79],a[44:47],v[208:211]
ld v255,s[60:63],s17 offen offset:3072 sc1 lds
mm v[216:219],a[76:79],a[60:63],0
ex v203,v203
mm v[216:219],a[72:75],a[56:59],v[216:219]
cv v229,v202,v203
s_mov_b32 s93, 43008
mm v[216:219],a[68:71],a[52:55],v[216:219]
ex v196,v196
mm v[216:219],a[64:67],a[48:51],v[216:219]
ld a[152:155],v243,s[40:43],s93 offen
mm v[212:215],a[80:83],a[32:35],0
ex v197,v197
s_add_u32 s17, s17, 8192
mm v[212:215],a[84:87],a[36:39],v[212:215]
cv v226,v196,v197
s_mov_b32 s93, 50176
s_add_u32 m0, m0, 16384
mm v[212:215],a[88:91],a[40:43],v[212:215]
ex v198,v198
s_and_b32 m0, m0, s78
mm v[212:215],a[92:95],a[44:47],v[212:215]
ld a[156:159],v243,s[40:43],s93 offen
mm v[220:223],a[92:95],a[60:63],0
ex v199,v199
mm v[220:223],a[88:91],a[56:59],v[220:223]
cv v227,v198,v199
mm v[220:223],a[84:87],a[52:55],v[220:223]
ex v204,v204
mm v[220:223],a[80:83],a[48:51],v[220:223]
wt vmcnt(17)
s_barrier
mm v[64:67],a[96:99],v[232:235],v[64:67]
ex v205,v205
mm v[96:99],a[96:99],v[236:239],v[96:99]
rd a[96:99],v248 offset:8192
cv v230,v204,v205
mm v[100:103],a[100:103],v[236:239],v[100:103]
ex v206,v206
mm v[68:71],a[100:103],v[232:235],v[68:71]
rd a[100:103],v248 offset:9216
mm v[72:75],a[104:107],v[232:235],v[72:75]
ex v207,v207
mm v[104:107],a[104:107],v[236:239],v[104:107]
rd a[104:107],v248 offset:10240
cv v231,v206,v207
s_mov_b32 s93, 57344
mm v[108:111],a[108:111],v[236:239],v[108:111]
ex v208,v208
mm v[76:79],a[108:111],v[232:235],v[76:79]
rd a[108:111],v248 offset:11264
ld a[160:163],v243,s[40:43],s93 offen
mm v[80:83],a[112:115],v[232:235],v[80:83]
ex v209,v209
mm v[112:115],a[112:115],v[236:239],v[112:115]
rd a[112:115],v248 offset:12288
mm v[116:119],a[116:119],v[236:239],v[116:119]
ex v210,v210
mm v[84:87],a[116:119],v[232:235],v[84:87]
rd a[116:119],v248 offset:13312
mm v[88:91],a[120:123],v[232:235],v[88:91]
ex v211,v211
mm v[120:123],a[120:123],v[236:239],v[120:123]
rd a[120:123],v248 offset:14336
mm v[124:127],a[124:127],v[236:239],v[124:127]
ex v216,v216
mm v[92:95],a[124:127],v[232:235],v[92:95]
rd a[124:127],v248 offset:15360
v_or_b32 v248, s89, v255
v_smfmac_f32_16x16x64_bf16 a[196:199], a[208:211], v[232:239], v241
wt lgkmcnt(4)
v_smfmac_f32_16x16x64_bf16 a[192:195], a[208:211], v[224:231], v241
rd a[64:67],v248 offset:0
mm v[0:3],a[96:99],v[224:227],v[0:3]
ex v217,v217
mm v[32:35],a[96:99],v[228:231],v[32:35]
rd a[68:71],v248 offset:1024
cv v232,v208,v209
mm v[36:39],a[100:103],v[228:231],v[36:39]
ex v218,v218
mm v[4:7],a[100:103],v[224:227],v[4:7]
rd a[72:75],v248 offset:2048
cv v233,v210,v211
mm v[8:11],a[104:107],v[224:227],v[8:11]
ex v219,v219
mm v[40:43],a[104:107],v[228:231],v[40:43]
rd a[76:79],v248 offset:3072
cv v236,v216,v217
mm v[44:47],a[108:111],v[228:231],v[44:47]
ex v212,v212
mm v[12:15],a[108:111],v[224:227],v[12:15]
rd a[80:83],v248 offset:4096
wt lgkmcnt(5)
mm v[16:19],a[112:115],v[224:227],v[16:19]
ex v213,v213
mm v[48:51],a[112:115],v[228:231],v[48:51]
rd a[84:87],v248 offset:5120
cv v237,v218,v219
mm v[52:55],a[116:119],v[228:231],v[52:55]
ex v214,v214
mm v[20:23],a[116:119],v[224:227],v[20:23]
rd a[88:91],v248 offset:6144
cv v234,v212,v213
mm v[24:27],a[120:123],v[224:227],v[24:27]
ex v215,v215
mm v[56:59],a[120:123],v[228:231],v[56:59]
rd a[92:95],v248 offset:7168
cv v235,v214,v215
mm v[60:63],a[124:127],v[228:231],v[60:63]
ex v220,v220
mm v[28:31],a[124:127],v[224:227],v[28:31]
wt lgkmcnt(4)
mm v[192:195],a[64:67],a[0:3],0
ex v221,v221
mm v[192:195],a[68:71],a[4:7],v[192:195]
cv v238,v220,v221
ar v240,a228
mm v[192:195],a[72:75],a[8:11],v[192:195]
ex v222,v222
mm v[192:195],a[76:79],a[12:15],v[192:195]
s_add_u32 m0, s83, 16896
s_lshl_b32 s17, s72, 18
mm v[200:203],a[76:79],a[28:31],0
ex v223,v223
mm v[200:203],a[72:75],a[24:27],v[200:203]
cv v239,v222,v223
s_add_u32 s89, s89, 16384
mm v[200:203],a[68:71],a[20:23],v[200:203]
ex v192,v192
mm v[200:203],a[64:67],a[16:19],v[200:203]
wt lgkmcnt(0)
s_and_b32 s89, s89, 0x1ffff
mm v[196:199],a[80:83],a[0:3],0
ex v193,v193
mm v[196:199],a[84:87],a[4:7],v[196:199]
cv v224,v192,v193
mm v[196:199],a[88:91],a[8:11],v[196:199]
ex v194,v194
mm v[196:199],a[92:95],a[12:15],v[196:199]
ld v240,s[56:59],s17 offen lds
mm v[204:207],a[92:95],a[28:31],0
ex v195,v195
s_add_u32 s17, s17, 32768
mm v[204:207],a[88:91],a[24:27],v[204:207]
cv v225,v194,v195
s_add_u32 m0, m0, 1024
mm v[204:207],a[84:87],a[20:23],v[204:207]
ex v200,v200
mm v[204:207],a[80:83],a[16:19],v[204:207]
ld v240,s[56:59],s17 offen lds
mm v[208:211],a[64:67],a[32:35],0
ex v201,v201
s_add_u32 s17, s17, 4294942720
mm v[208:211],a[68:71],a[36:39],v[208:211]
cv v228,v200,v201
s_add_u32 m0, m0, 1152
mm v[208:211],a[72:75],a[40:43],v[208:211]
ex v202,v202
mm v[208:211],a[76:79],a[44:47],v[208:211]
ld v240,s[56:59],s17 offen lds
mm v[216:219],a[76:79],a[60:63],0
ex v203,v203
s_add_u32 s17, s17, 32768
mm v[216:219],a[72:75],a[56:59],v[216:219]
cv v229,v202,v203
s_add_u32 m0, m0, 1024
mm v[216:219],a[68:71],a[52:55],v[216:219]
ex v196,v196
mm v[216:219],a[64:67],a[48:51],v[216:219]
ld v240,s[56:59],s17 offen lds
mm v[212:215],a[80:83],a[32:35],0
ex v197,v197
s_add_u32 s17, s17, 90112
mm v[212:215],a[84:87],a[36:39],v[212:215]
cv v226,v196,v197
s_mov_b32 s93, 64512
s_add_u32 m0, s83, 33792
mm v[212:215],a[88:91],a[40:43],v[212:215]
ex v198,v198
mm v[212:215],a[92:95],a[44:47],v[212:215]
ld a[164:167],v243,s[40:43],s93 offen
mm v[220:223],a[92:95],a[60:63],0
ex v199,v199
mm v[220:223],a[88:91],a[56:59],v[220:223]
cv v227,v198,v199
mm v[220:223],a[84:87],a[52:55],v[220:223]
ex v204,v204
mm v[220:223],a[80:83],a[48:51],v[220:223]
wt vmcnt(16)
s_barrier
mm v[64:67],a[96:99],v[232:235],v[64:67]
ex v205,v205
.La5_LW6_5:
s_cbranch_execz .La5_LW6_R0
mm v[96:99],a[96:99],v[236:239],v[96:99]
rd a[96:99],v248 offset:8192
cv v230,v204,v205
mm v[100:103],a[100:103],v[236:239],v[100:103]
ex v206,v206
mm v[68:71],a[100:103],v[232:235],v[68:71]
rd a[100:103],v248 offset:9216
mm v[72:75],a[104:107],v[232:235],v[72:75]
ex v207,v207
mm v[104:107],a[104:107],v[236:239],v[104:107]
rd a[104:107],v248 offset:10240
cv v231,v206,v207
s_mov_b32 s93, 71680
mm v[108:111],a[108:111],v[236:239],v[108:111]
ex v208,v208
mm v[76:79],a[108:111],v[232:235],v[76:79]
rd a[108:111],v248 offset:11264
ld a[168:171],v243,s[40:43],s93 offen
mm v[80:83],a[112:115],v[232:235],v[80:83]
ex v209,v209
mm v[112:115],a[112:115],v[236:239],v[112:115]
rd a[112:115],v248 offset:12288
s_mov_b32 s93, 78848
mm v[116:119],a[116:119],v[236:239],v[116:119]
ex v210,v210
mm v[84:87],a[116:119],v[232:235],v[84:87]
rd a[116:119],v248 offset:13312
ld a[172:175],v243,s[40:43],s93 offen
mm v[88:91],a[120:123],v[232:235],v[88:91]
ex v211,v211
mm v[120:123],a[120:123],v[236:239],v[120:123]
rd a[120:123],v248 offset:14336
mm v[124:127],a[124:127],v[236:239],v[124:127]
ex v216,v216
mm v[92:95],a[124:127],v[232:235],v[92:95]
rd a[124:127],v248 offset:15360
v_or_b32 v248, s89, v255
v_smfmac_f32_16x16x64_bf16 a[196:199], a[208:211], v[232:239], v241
wt lgkmcnt(4)
v_smfmac_f32_16x16x64_bf16 a[192:195], a[208:211], v[224:231], v241
rd a[64:67],v248 offset:0
mm v[0:3],a[96:99],v[224:227],v[0:3]
ex v217,v217
mm v[32:35],a[96:99],v[228:231],v[32:35]
rd a[68:71],v248 offset:1024
cv v232,v208,v209
mm v[36:39],a[100:103],v[228:231],v[36:39]
ex v218,v218
mm v[4:7],a[100:103],v[224:227],v[4:7]
rd a[72:75],v248 offset:2048
cv v233,v210,v211
mm v[8:11],a[104:107],v[224:227],v[8:11]
ex v219,v219
mm v[40:43],a[104:107],v[228:231],v[40:43]
rd a[76:79],v248 offset:3072
cv v236,v216,v217
mm v[44:47],a[108:111],v[228:231],v[44:47]
ex v212,v212
mm v[12:15],a[108:111],v[224:227],v[12:15]
rd a[80:83],v248 offset:4096
wt lgkmcnt(5)
mm v[16:19],a[112:115],v[224:227],v[16:19]
ex v213,v213
mm v[48:51],a[112:115],v[228:231],v[48:51]
rd a[84:87],v248 offset:5120
cv v237,v218,v219
mm v[52:55],a[116:119],v[228:231],v[52:55]
ex v214,v214
mm v[20:23],a[116:119],v[224:227],v[20:23]
rd a[88:91],v248 offset:6144
cv v234,v212,v213
mm v[24:27],a[120:123],v[224:227],v[24:27]
ex v215,v215
mm v[56:59],a[120:123],v[228:231],v[56:59]
rd a[92:95],v248 offset:7168
cv v235,v214,v215
mm v[60:63],a[124:127],v[228:231],v[60:63]
ex v220,v220
mm v[28:31],a[124:127],v[224:227],v[28:31]
wt lgkmcnt(4)
mm v[192:195],a[64:67],a[0:3],0
ex v221,v221
mm v[192:195],a[68:71],a[4:7],v[192:195]
cv v238,v220,v221
s_add_u32 s89, s89, 16384
mm v[192:195],a[72:75],a[8:11],v[192:195]
ex v222,v222
mm v[192:195],a[76:79],a[12:15],v[192:195]
ld v240,s[56:59],s17 offen lds
mm v[200:203],a[76:79],a[28:31],0
ex v223,v223
s_add_u32 s17, s17, 32768
mm v[200:203],a[72:75],a[24:27],v[200:203]
cv v239,v222,v223
s_add_u32 m0, m0, 1024
mm v[200:203],a[68:71],a[20:23],v[200:203]
ex v192,v192
mm v[200:203],a[64:67],a[16:19],v[200:203]
wt lgkmcnt(0)
s_and_b32 s89, s89, 0x1ffff
mm v[196:199],a[80:83],a[0:3],0
ex v193,v193
mm v[196:199],a[84:87],a[4:7],v[196:199]
cv v224,v192,v193
mm v[196:199],a[88:91],a[8:11],v[196:199]
ex v194,v194
mm v[196:199],a[92:95],a[12:15],v[196:199]
ld v240,s[56:59],s17 offen lds
mm v[204:207],a[92:95],a[28:31],0
ex v195,v195
s_add_u32 s17, s17, 4294942720
mm v[204:207],a[88:91],a[24:27],v[204:207]
cv v225,v194,v195
s_add_u32 m0, m0, 1152
mm v[204:207],a[84:87],a[20:23],v[204:207]
ex v200,v200
mm v[204:207],a[80:83],a[16:19],v[204:207]
ld v240,s[56:59],s17 offen lds
mm v[208:211],a[64:67],a[32:35],0
ex v201,v201
s_add_u32 s17, s17, 32768
mm v[208:211],a[68:71],a[36:39],v[208:211]
cv v228,v200,v201
s_add_u32 m0, m0, 1024
mm v[208:211],a[72:75],a[40:43],v[208:211]
ex v202,v202
mm v[208:211],a[76:79],a[44:47],v[208:211]
ld v240,s[56:59],s17 offen lds
mm v[216:219],a[76:79],a[60:63],0
ex v203,v203
mm v[216:219],a[72:75],a[56:59],v[216:219]
cv v229,v202,v203
s_mov_b32 s93, 86016
mm v[216:219],a[68:71],a[52:55],v[216:219]
ex v196,v196
mm v[216:219],a[64:67],a[48:51],v[216:219]
ld a[176:179],v243,s[40:43],s93 offen
mm v[212:215],a[80:83],a[32:35],0
ex v197,v197
s_bfe_u32 s17, s4, 262148
mm v[212:215],a[84:87],a[36:39],v[212:215]
cv v226,v196,v197
s_mov_b32 s93, 93184
s_xor_b32 s17, s17, s72
mm v[212:215],a[88:91],a[40:43],v[212:215]
ex v198,v198
s_lshl_b32 s17, s17, 18
mm v[212:215],a[92:95],a[44:47],v[212:215]
ld a[180:183],v243,s[40:43],s93 offen
s_add_u32 m0, s83, 50688
mm v[220:223],a[92:95],a[60:63],0
ex v199,v199
mm v[220:223],a[88:91],a[56:59],v[220:223]
cv v227,v198,v199
mm v[220:223],a[84:87],a[52:55],v[220:223]
ex v204,v204
mm v[220:223],a[80:83],a[48:51],v[220:223]
wt vmcnt(16)
s_barrier
mm v[64:67],a[96:99],v[232:235],v[64:67]
ex v205,v205
mm v[96:99],a[96:99],v[236:239],v[96:99]
rd a[96:99],v248 offset:8192
cv v230,v204,v205
mm v[100:103],a[100:103],v[236:239],v[100:103]
ex v206,v206
mm v[68:71],a[100:103],v[232:235],v[68:71]
rd a[100:103],v248 offset:9216
mm v[72:75],a[104:107],v[232:235],v[72:75]
ex v207,v207
mm v[104:107],a[104:107],v[236:239],v[104:107]
rd a[104:107],v248 offset:10240
cv v231,v206,v207
s_mov_b32 s93, 100352
mm v[108:111],a[108:111],v[236:239],v[108:111]
ex v208,v208
mm v[76:79],a[108:111],v[232:235],v[76:79]
rd a[108:111],v248 offset:11264
ld a[184:187],v243,s[40:43],s93 offen
mm v[80:83],a[112:115],v[232:235],v[80:83]
ex v209,v209
mm v[112:115],a[112:115],v[236:239],v[112:115]
rd a[112:115],v248 offset:12288
mm v[116:119],a[116:119],v[236:239],v[116:119]
ex v210,v210
mm v[84:87],a[116:119],v[232:235],v[84:87]
rd a[116:119],v248 offset:13312
mm v[88:91],a[120:123],v[232:235],v[88:91]
ex v211,v211
mm v[120:123],a[120:123],v[236:239],v[120:123]
rd a[120:123],v248 offset:14336
mm v[124:127],a[124:127],v[236:239],v[124:127]
ex v216,v216
mm v[92:95],a[124:127],v[232:235],v[92:95]
rd a[124:127],v248 offset:15360
v_or_b32 v248, s89, v255
v_smfmac_f32_16x16x64_bf16 a[196:199], a[208:211], v[232:239], v241
wt lgkmcnt(4)
v_smfmac_f32_16x16x64_bf16 a[192:195], a[208:211], v[224:231], v241
rd a[64:67],v248 offset:0
mm v[0:3],a[96:99],v[224:227],v[0:3]
ex v217,v217
mm v[32:35],a[96:99],v[228:231],v[32:35]
rd a[68:71],v248 offset:1024
cv v232,v208,v209
mm v[36:39],a[100:103],v[228:231],v[36:39]
ex v218,v218
mm v[4:7],a[100:103],v[224:227],v[4:7]
rd a[72:75],v248 offset:2048
cv v233,v210,v211
mm v[8:11],a[104:107],v[224:227],v[8:11]
ex v219,v219
mm v[40:43],a[104:107],v[228:231],v[40:43]
rd a[76:79],v248 offset:3072
cv v236,v216,v217
mm v[44:47],a[108:111],v[228:231],v[44:47]
ex v212,v212
mm v[12:15],a[108:111],v[224:227],v[12:15]
rd a[80:83],v248 offset:4096
wt lgkmcnt(5)
mm v[16:19],a[112:115],v[224:227],v[16:19]
ex v213,v213
mm v[48:51],a[112:115],v[228:231],v[48:51]
rd a[84:87],v248 offset:5120
cv v237,v218,v219
mm v[52:55],a[116:119],v[228:231],v[52:55]
ex v214,v214
mm v[20:23],a[116:119],v[224:227],v[20:23]
rd a[88:91],v248 offset:6144
cv v234,v212,v213
mm v[24:27],a[120:123],v[224:227],v[24:27]
ex v215,v215
mm v[56:59],a[120:123],v[228:231],v[56:59]
rd a[92:95],v248 offset:7168
cv v235,v214,v215
mm v[60:63],a[124:127],v[228:231],v[60:63]
ex v220,v220
mm v[28:31],a[124:127],v[224:227],v[28:31]
wt lgkmcnt(4)
mm v[192:195],a[64:67],a[0:3],0
ex v221,v221
mm v[192:195],a[68:71],a[4:7],v[192:195]
cv v238,v220,v221
mm v[192:195],a[72:75],a[8:11],v[192:195]
ex v222,v222
mm v[192:195],a[76:79],a[12:15],v[192:195]
ld v240,s[56:59],s17 offen lds
mm v[200:203],a[76:79],a[28:31],0
ex v223,v223
s_add_u32 s17, s17, 32768
mm v[200:203],a[72:75],a[24:27],v[200:203]
cv v239,v222,v223
s_add_u32 m0, m0, 1024
mm v[200:203],a[68:71],a[20:23],v[200:203]
ex v192,v192
mm v[200:203],a[64:67],a[16:19],v[200:203]
wt lgkmcnt(0)
mm v[196:199],a[80:83],a[0:3],0
ex v193,v193
mm v[196:199],a[84:87],a[4:7],v[196:199]
cv v224,v192,v193
mm v[196:199],a[88:91],a[8:11],v[196:199]
ex v194,v194
mm v[196:199],a[92:95],a[12:15],v[196:199]
ld v240,s[56:59],s17 offen lds
mm v[204:207],a[92:95],a[28:31],0
ex v195,v195
s_add_u32 s17, s17, 4294942720
mm v[204:207],a[88:91],a[24:27],v[204:207]
cv v225,v194,v195
s_add_u32 m0, m0, 1152
mm v[204:207],a[84:87],a[20:23],v[204:207]
ex v200,v200
mm v[204:207],a[80:83],a[16:19],v[204:207]
ld v240,s[56:59],s17 offen lds
mm v[208:211],a[64:67],a[32:35],0
ex v201,v201
s_add_u32 s17, s17, 32768
mm v[208:211],a[68:71],a[36:39],v[208:211]
cv v228,v200,v201
s_add_u32 m0, m0, 1024
mm v[208:211],a[72:75],a[40:43],v[208:211]
ex v202,v202
mm v[208:211],a[76:79],a[44:47],v[208:211]
ld v240,s[56:59],s17 offen lds
mm v[216:219],a[76:79],a[60:63],0
ex v203,v203
mm v[216:219],a[72:75],a[56:59],v[216:219]
cv v229,v202,v203
s_mov_b32 s93, 107520
mm v[216:219],a[68:71],a[52:55],v[216:219]
ex v196,v196
mm v[216:219],a[64:67],a[48:51],v[216:219]
ld a[188:191],v243,s[40:43],s93 offen
mm v[212:215],a[80:83],a[32:35],0
ex v197,v197
mm v[212:215],a[84:87],a[36:39],v[212:215]
cv v226,v196,v197
mm v[212:215],a[88:91],a[40:43],v[212:215]
ex v198,v198
mm v[212:215],a[92:95],a[44:47],v[212:215]
mm v[220:223],a[92:95],a[60:63],0
ex v199,v199
mm v[220:223],a[88:91],a[56:59],v[220:223]
cv v227,v198,v199
mm v[220:223],a[84:87],a[52:55],v[220:223]
ex v204,v204
mm v[220:223],a[80:83],a[48:51],v[220:223]
mm v[64:67],a[96:99],v[232:235],v[64:67]
ex v205,v205
mm v[96:99],a[96:99],v[236:239],v[96:99]
rd a[96:99],v248 offset:8192
cv v230,v204,v205
mm v[100:103],a[100:103],v[236:239],v[100:103]
ex v206,v206
mm v[68:71],a[100:103],v[232:235],v[68:71]
rd a[100:103],v248 offset:9216
mm v[72:75],a[104:107],v[232:235],v[72:75]
ex v207,v207
mm v[104:107],a[104:107],v[236:239],v[104:107]
rd a[104:107],v248 offset:10240
cv v231,v206,v207
mm v[108:111],a[108:111],v[236:239],v[108:111]
ex v208,v208
mm v[76:79],a[108:111],v[232:235],v[76:79]
rd a[108:111],v248 offset:11264
mm v[80:83],a[112:115],v[232:235],v[80:83]
ex v209,v209
mm v[112:115],a[112:115],v[236:239],v[112:115]
rd a[112:115],v248 offset:12288
mm v[116:119],a[116:119],v[236:239],v[116:119]
ex v210,v210
mm v[84:87],a[116:119],v[232:235],v[84:87]
rd a[116:119],v248 offset:13312
mm v[88:91],a[120:123],v[232:235],v[88:91]
ex v211,v211
mm v[120:123],a[120:123],v[236:239],v[120:123]
rd a[120:123],v248 offset:14336
mm v[124:127],a[124:127],v[236:239],v[124:127]
ex v216,v216
mm v[92:95],a[124:127],v[232:235],v[92:95]
rd a[124:127],v248 offset:15360
v_smfmac_f32_16x16x64_bf16 a[196:199], a[208:211], v[232:239], v241
wt lgkmcnt(4)
v_smfmac_f32_16x16x64_bf16 a[192:195], a[208:211], v[224:231], v241
mm v[0:3],a[96:99],v[224:227],v[0:3]
ex v217,v217
mm v[32:35],a[96:99],v[228:231],v[32:35]
cv v232,v208,v209
mm v[36:39],a[100:103],v[228:231],v[36:39]
ex v218,v218
mm v[4:7],a[100:103],v[224:227],v[4:7]
cv v233,v210,v211
mm v[8:11],a[104:107],v[224:227],v[8:11]
ex v219,v219
mm v[40:43],a[104:107],v[228:231],v[40:43]
cv v236,v216,v217
mm v[44:47],a[108:111],v[228:231],v[44:47]
ex v212,v212
mm v[12:15],a[108:111],v[224:227],v[12:15]
wt lgkmcnt(0)
mm v[16:19],a[112:115],v[224:227],v[16:19]
ex v213,v213
mm v[48:51],a[112:115],v[228:231],v[48:51]
cv v237,v218,v219
mm v[52:55],a[116:119],v[228:231],v[52:55]
ex v214,v214
mm v[20:23],a[116:119],v[224:227],v[20:23]
cv v234,v212,v213
mm v[24:27],a[120:123],v[224:227],v[24:27]
ex v215,v215
mm v[56:59],a[120:123],v[228:231],v[56:59]
cv v235,v214,v215
mm v[60:63],a[124:127],v[228:231],v[60:63]
ex v220,v220
mm v[28:31],a[124:127],v[224:227],v[28:31]
; ---- helper: partial O and l go to the partner
.La5_LA_DP:
ex v221,v221
ex v222,v222
ex v223,v223
cv v238,v220,v221
s_nop 0
cv v239,v222,v223
s_nop 1
v_smfmac_f32_16x16x64_bf16 a[196:199], a[208:211], v[232:239], v241
cv v0,v0,v1
cv v1,v2,v3
cv v2,v4,v5
cv v3,v6,v7
st v[0:3],v255,s[68:71],0 offen offset:0 sc1
mm v[64:67],a[96:99],v[232:235],v[64:67]
cv v8,v8,v9
cv v9,v10,v11
cv v10,v12,v13
cv v11,v14,v15
st v[8:11],v255,s[68:71],0 offen offset:1024 sc1
mm v[96:99],a[96:99],v[236:239],v[96:99]
ar v240,a224
v_xor_b32 v241, 32, v240
cv v16,v16,v17
cv v17,v18,v19
cv v18,v20,v21
cv v19,v22,v23
mm v[100:103],a[100:103],v[236:239],v[100:103]
v_xor_b32 v242, 64, v240
v_xor_b32 v243, 96, v240
st v[16:19],v255,s[68:71],0 offen offset:2048 sc1
cv v24,v24,v25
cv v25,v26,v27
mm v[68:71],a[100:103],v[232:235],v[68:71]
cv v26,v28,v29
cv v27,v30,v31
st v[24:27],v255,s[68:71],0 offen offset:3072 sc1
cv v32,v32,v33
cv v33,v34,v35
mm v[72:75],a[104:107],v[232:235],v[72:75]
cv v34,v36,v37
cv v35,v38,v39
st v[32:35],v255,s[68:71],s95 offen offset:0 sc1
cv v40,v40,v41
cv v41,v42,v43
mm v[104:107],a[104:107],v[236:239],v[104:107]
wt vmcnt(13)
s_barrier
cv v42,v44,v45
cv v43,v46,v47
st v[40:43],v255,s[68:71],s95 offen offset:1024 sc1
mm v[108:111],a[108:111],v[236:239],v[108:111]
rt v[192:193],v240 offset:16896
rt v[194:195],v240 offset:17920
cv v48,v48,v49
cv v49,v50,v51
cv v50,v52,v53
cv v51,v54,v55
mm v[76:79],a[108:111],v[232:235],v[76:79]
rt v[196:197],v240 offset:25344
rt v[198:199],v240 offset:26368
st v[48:51],v255,s[68:71],s95 offen offset:2048 sc1
cv v56,v56,v57
cv v57,v58,v59
mm v[80:83],a[112:115],v[232:235],v[80:83]
rt v[200:201],v241 offset:16896
rt v[202:203],v241 offset:17920
cv v58,v60,v61
cv v59,v62,v63
st v[56:59],v255,s[68:71],s95 offen offset:3072 sc1
mm v[112:115],a[112:115],v[236:239],v[112:115]
rt v[204:205],v241 offset:25344
rt v[206:207],v241 offset:26368
mm v[116:119],a[116:119],v[236:239],v[116:119]
rt v[208:209],v242 offset:16896
rt v[210:211],v242 offset:17920
mm v[84:87],a[116:119],v[232:235],v[84:87]
rt v[212:213],v242 offset:25344
rt v[214:215],v242 offset:26368
mm v[88:91],a[120:123],v[232:235],v[88:91]
rt v[216:217],v243 offset:16896
rt v[218:219],v243 offset:17920
mm v[120:123],a[120:123],v[236:239],v[120:123]
rt v[220:221],v243 offset:25344
rt v[222:223],v243 offset:26368
mm v[124:127],a[124:127],v[236:239],v[124:127]
mm v[92:95],a[124:127],v[232:235],v[92:95]
wt lgkmcnt(12)
mm v[0:3],v[192:195],v[128:131],0
mm v[0:3],v[196:199],v[132:135],v[0:3]
mm v[32:35],v[196:199],v[148:151],0
wt lgkmcnt(8)
ar v224,a192
ar v228,a193
ar v225,a194
ar v229,a195
ar v226,a196
mm v[32:35],v[192:195],v[144:147],v[32:35]
rt v[196:197],v240 offset:17024
rt v[198:199],v240 offset:18048
ar v230,a197
ar v227,a198
ar v231,a199
v_add_f32 v224, v224, v228
mm v[36:39],v[200:203],v[144:147],0
v_add_f32 v225, v225, v229
v_add_f32 v226, v226, v230
v_add_f32 v227, v227, v231
s_mov_b32 s92, 16384
st v[224:227],v255,s[68:71],s92 offen sc1
mm v[36:39],v[204:207],v[148:151],v[36:39]
rt v[192:193],v240 offset:25472
rt v[194:195],v240 offset:26496
cv v64,v64,v65
cv v65,v66,v67
cv v66,v68,v69
cv v67,v70,v71
mm v[4:7],v[204:207],v[132:135],0
wt lgkmcnt(4)
st v[64:67],v255,s[68:71],s96 offen offset:0 sc1
cv v72,v72,v73
cv v73,v74,v75
cv v74,v76,v77
mm v[4:7],v[200:203],v[128:131],v[4:7]
rt v[204:205],v241 offset:17024
rt v[206:207],v241 offset:18048
cv v75,v78,v79
st v[72:75],v255,s[68:71],s96 offen offset:1024 sc1
cv v80,v80,v81
mm v[8:11],v[208:211],v[128:131],0
cv v81,v82,v83
cv v82,v84,v85
cv v83,v86,v87
st v[80:83],v255,s[68:71],s96 offen offset:2048 sc1
cv v88,v88,v89
mm v[8:11],v[212:215],v[132:135],v[8:11]
rt v[200:201],v241 offset:25472
rt v[202:203],v241 offset:26496
cv v89,v90,v91
cv v90,v92,v93
cv v91,v94,v95
st v[88:91],v255,s[68:71],s96 offen offset:3072 sc1
mm v[40:43],v[212:215],v[148:151],0
cv v96,v96,v97
cv v97,v98,v99
cv v98,v100,v101
cv v99,v102,v103
st v[96:99],v255,s[68:71],s97 offen offset:0 sc1
mm v[40:43],v[208:211],v[144:147],v[40:43]
rt v[212:213],v242 offset:17024
rt v[214:215],v242 offset:18048
cv v104,v104,v105
cv v105,v106,v107
cv v106,v108,v109
cv v107,v110,v111
mm v[44:47],v[216:219],v[144:147],0
st v[104:107],v255,s[68:71],s97 offen offset:1024 sc1
cv v112,v112,v113
cv v113,v114,v115
cv v114,v116,v117
cv v115,v118,v119
mm v[44:47],v[220:223],v[148:151],v[44:47]
rt v[208:209],v242 offset:25472
rt v[210:211],v242 offset:26496
st v[112:115],v255,s[68:71],s97 offen offset:2048 sc1
cv v120,v120,v121
cv v121,v122,v123
mm v[12:15],v[220:223],v[132:135],0
wt lgkmcnt(4)
cv v122,v124,v125
cv v123,v126,v127
st v[120:123],v255,s[68:71],s97 offen offset:3072 sc1
v_mov_b32 v249, 0
mm v[12:15],v[216:219],v[128:131],v[12:15]
rt v[220:221],v243 offset:17024
rt v[222:223],v243 offset:18048
s_add_u32 s6, s88, 393216
mm v[16:19],v[196:199],v[128:131],0
ar v224,a228
v_add_u32 v225, 0x8000, v224
v_add_u32 v226, 0x2000, v224
v_add_u32 v227, 0xa000, v224
mm v[16:19],v[192:195],v[132:135],v[16:19]
rt v[216:217],v243 offset:25472
rt v[218:219],v243 offset:26496
ar v248,a225
ar v228,a229
mm v[48:51],v[192:195],v[148:151],0
ar v229,a230
mm v[48:51],v[196:199],v[144:147],v[48:51]
rt v[192:193],v240 offset:33792
rt v[194:195],v240 offset:34816
mm v[52:55],v[204:207],v[144:147],0
.La5_LW6_6:
s_cbranch_execz .La5_LW6_R0
mm v[52:55],v[200:203],v[148:151],v[52:55]
rt v[196:197],v240 offset:42240
rt v[198:199],v240 offset:43264
mm v[20:23],v[200:203],v[132:135],0
wt lgkmcnt(4)
mm v[20:23],v[204:207],v[128:131],v[20:23]
rt v[200:201],v241 offset:33792
rt v[202:203],v241 offset:34816
mm v[24:27],v[212:215],v[128:131],0
s_getpc_b64 s[24:25]
.La5_LW3_P0:
s_add_u32 s24, s24, .La5_LW3_R0-.La5_LW3_P0
s_addc_u32 s25, s25, 0
s_mov_b64 exec, 0
s_cmp_eq_u32 s3, 0
s_cbranch_scc1 .La5_LW3_0
s_cmp_eq_u32 s3, 1
s_cbranch_scc1 .La5_LW3_1
s_cmp_eq_u32 s3, 2
s_cbranch_scc1 .La5_LW3_2
s_branch .La5_LW3_3
.La5_LW3_R0:
s_mov_b64 exec, -1
.La5_LO_VE2:
wt vmcnt(0)
buffer_store_dwordx2 v[252:253], v249, s[64:67], s6 offen offset:0 sc1
mm v[24:27],v[208:211],v[132:135],v[24:27]
rt v[204:205],v241 offset:42240
rt v[206:207],v241 offset:43264
mm v[56:59],v[208:211],v[148:151],0
s_mov_b32 s93, 3670016
ld a[192:195],v228,s[40:43],s93 offen
mm v[56:59],v[212:215],v[144:147],v[56:59]
rt v[208:209],v242 offset:33792
rt v[210:211],v242 offset:34816
mm v[60:63],v[220:223],v[144:147],0
s_mov_b32 s93, 3677184
ld a[196:199],v228,s[40:43],s93 offen
mm v[60:63],v[216:219],v[148:151],v[60:63]
rt v[212:213],v242 offset:42240
rt v[214:215],v242 offset:43264
mm v[28:31],v[216:219],v[132:135],0
wt lgkmcnt(4)
s_mov_b32 s93, 3684352
ld a[200:203],v228,s[40:43],s93 offen
mm v[28:31],v[220:223],v[128:131],v[28:31]
rt v[216:217],v243 offset:33792
rt v[218:219],v243 offset:34816
mm v[0:3],v[192:195],v[136:139],v[0:3]
s_mov_b32 s93, 3691520
ld a[204:207],v228,s[40:43],s93 offen
mm v[0:3],v[196:199],v[140:143],v[0:3]
rt v[220:221],v243 offset:42240
rt v[222:223],v243 offset:43264
mm v[32:35],v[196:199],v[156:159],v[32:35]
s_mov_b32 s93, 3698688
ld a[208:211],v228,s[40:43],s93 offen
mm v[32:35],v[192:195],v[152:155],v[32:35]
rt v[196:197],v240 offset:33920
rt v[198:199],v240 offset:34944
mm v[36:39],v[200:203],v[152:155],v[36:39]
s_mov_b32 s93, 3705856
ld a[212:215],v228,s[40:43],s93 offen
mm v[36:39],v[204:207],v[156:159],v[36:39]
rt v[192:193],v240 offset:42368
rt v[194:195],v240 offset:43392
mm v[4:7],v[204:207],v[140:143],v[4:7]
wt lgkmcnt(4)
s_mov_b32 s93, 3713024
ld a[216:219],v228,s[40:43],s93 offen
mm v[4:7],v[200:203],v[136:139],v[4:7]
rt v[204:205],v241 offset:33920
rt v[206:207],v241 offset:34944
mm v[8:11],v[208:211],v[136:139],v[8:11]
s_mov_b32 s93, 3720192
ld a[220:223],v228,s[40:43],s93 offen
mm v[8:11],v[212:215],v[140:143],v[8:11]
rt v[200:201],v241 offset:42368
rt v[202:203],v241 offset:43392
mm v[40:43],v[212:215],v[156:159],v[40:43]
s_mov_b32 s93, 3727360
ld a[224:227],v228,s[40:43],s93 offen
mm v[40:43],v[208:211],v[152:155],v[40:43]
rt v[212:213],v242 offset:33920
rt v[214:215],v242 offset:34944
mm v[44:47],v[216:219],v[152:155],v[44:47]
s_mov_b32 s93, 3734528
ld a[228:231],v228,s[40:43],s93 offen
mm v[44:47],v[220:223],v[156:159],v[44:47]
rt v[208:209],v242 offset:42368
rt v[210:211],v242 offset:43392
mm v[12:15],v[220:223],v[140:143],v[12:15]
wt lgkmcnt(4)
s_mov_b32 s93, 3741696
ld a[232:235],v228,s[40:43],s93 offen
mm v[12:15],v[216:219],v[136:139],v[12:15]
rt v[220:221],v243 offset:33920
rt v[222:223],v243 offset:34944
mm v[16:19],v[196:199],v[136:139],v[16:19]
s_mov_b32 s93, 3748864
ld a[236:239],v228,s[40:43],s93 offen
mm v[16:19],v[192:195],v[140:143],v[16:19]
rt v[216:217],v243 offset:42368
rt v[218:219],v243 offset:43392
mm v[48:51],v[192:195],v[156:159],v[48:51]
s_mov_b32 s93, 3756032
ld a[240:243],v228,s[40:43],s93 offen
mm v[48:51],v[196:199],v[152:155],v[48:51]
rt v[192:193],v240 offset:16896
rt v[194:195],v240 offset:17920
mm v[52:55],v[204:207],v[152:155],v[52:55]
s_mov_b32 s93, 3763200
ld a[244:247],v228,s[40:43],s93 offen
mm v[52:55],v[200:203],v[156:159],v[52:55]
rt v[196:197],v240 offset:25344
rt v[198:199],v240 offset:26368
mm v[20:23],v[200:203],v[140:143],v[20:23]
wt lgkmcnt(4)
s_mov_b32 s93, 3770368
ld a[248:251],v228,s[40:43],s93 offen
mm v[20:23],v[204:207],v[136:139],v[20:23]
rt v[200:201],v241 offset:16896
rt v[202:203],v241 offset:17920
mm v[24:27],v[212:215],v[136:139],v[24:27]
s_mov_b32 s93, 3777536
ld a[252:255],v228,s[40:43],s93 offen
mm v[24:27],v[208:211],v[140:143],v[24:27]
rt v[204:205],v241 offset:25344
rt v[206:207],v241 offset:26368
mm v[56:59],v[208:211],v[156:159],v[56:59]
mm v[56:59],v[212:215],v[152:155],v[56:59]
rt v[208:209],v242 offset:16896
rt v[210:211],v242 offset:17920
mm v[60:63],v[220:223],v[152:155],v[60:63]
mm v[60:63],v[216:219],v[156:159],v[60:63]
rt v[212:213],v242 offset:25344
rt v[214:215],v242 offset:26368
mm v[28:31],v[216:219],v[140:143],v[28:31]
wt lgkmcnt(4)
mm v[28:31],v[220:223],v[136:139],v[28:31]
rt v[216:217],v243 offset:16896
rt v[218:219],v243 offset:17920
s_branch .La5_LO_START
; ---- owner: last window, partner-s part added, l checked, O / l leaves
.La5_LA_DC:
s_nop 0
s_nop 0
.La5_LW5_0:
s_cbranch_execz .La5_LW5_R0
wt lgkmcnt(4)
mm v[192:195],a[64:67],v[128:131],0
ex v221,v221
mm v[192:195],a[68:71],v[132:135],v[192:195]
cv v238,v220,v221
s_add_u32 s89, s89, 16384
mm v[192:195],a[72:75],v[136:139],v[192:195]
ex v222,v222
mm v[192:195],a[76:79],v[140:143],v[192:195]
ld v255,s[60:63],s17 offen offset:0 sc1 lds
mm v[200:203],a[76:79],v[156:159],0
ex v223,v223
mm v[200:203],a[72:75],v[152:155],v[200:203]
cv v239,v222,v223
s_and_b32 s89, s89, 0x1ffff
mm v[200:203],a[68:71],v[148:151],v[200:203]
ex v192,v192
mm v[200:203],a[64:67],v[144:147],v[200:203]
wt lgkmcnt(0)
mm v[196:199],a[80:83],v[128:131],0
ex v193,v193
mm v[196:199],a[84:87],v[132:135],v[196:199]
cv v224,v192,v193
ar v243,a229
mm v[196:199],a[88:91],v[136:139],v[196:199]
ex v194,v194
mm v[196:199],a[92:95],v[140:143],v[196:199]
ld v255,s[60:63],s17 offen offset:1024 sc1 lds
mm v[204:207],a[92:95],v[156:159],0
ex v195,v195
mm v[204:207],a[88:91],v[152:155],v[204:207]
cv v225,v194,v195
mm v[204:207],a[84:87],v[148:151],v[204:207]
ex v200,v200
mm v[204:207],a[80:83],v[144:147],v[204:207]
ld v255,s[60:63],s17 offen offset:2048 sc1 lds
mm v[208:211],a[64:67],v[160:163],0
ex v201,v201
mm v[208:211],a[68:71],v[164:167],v[208:211]
cv v228,v200,v201
mm v[208:211],a[72:75],v[168:171],v[208:211]
ex v202,v202
mm v[208:211],a[76:79],v[172:175],v[208:211]
ld v255,s[60:63],s17 offen offset:3072 sc1 lds
mm v[216:219],a[76:79],v[188:191],0
ex v203,v203
mm v[216:219],a[72:75],v[184:187],v[216:219]
cv v229,v202,v203
s_mov_b32 s93, 0
mm v[216:219],a[68:71],v[180:183],v[216:219]
ex v196,v196
mm v[216:219],a[64:67],v[176:179],v[216:219]
ld a[128:131],v243,s[40:43],s93 offen
mm v[212:215],a[80:83],v[160:163],0
ex v197,v197
s_add_u32 s17, s17, 8192
mm v[212:215],a[84:87],v[164:167],v[212:215]
cv v226,v196,v197
s_mov_b32 s93, 7168
s_add_u32 m0, m0, 16384
mm v[212:215],a[88:91],v[168:171],v[212:215]
ex v198,v198
s_and_b32 m0, m0, s78
mm v[212:215],a[92:95],v[172:175],v[212:215]
ld a[132:135],v243,s[40:43],s93 offen
mm v[220:223],a[92:95],v[188:191],0
ex v199,v199
mm v[220:223],a[88:91],v[184:187],v[220:223]
cv v227,v198,v199
mm v[220:223],a[84:87],v[180:183],v[220:223]
ex v204,v204
mm v[220:223],a[80:83],v[176:179],v[220:223]
wt vmcnt(12)
s_barrier
mm v[64:67],a[96:99],v[232:235],v[64:67]
ex v205,v205
mm v[96:99],a[96:99],v[236:239],v[96:99]
rd a[96:99],v248 offset:8192
cv v230,v204,v205
mm v[100:103],a[100:103],v[236:239],v[100:103]
ex v206,v206
mm v[68:71],a[100:103],v[232:235],v[68:71]
rd a[100:103],v248 offset:9216
mm v[72:75],a[104:107],v[232:235],v[72:75]
ex v207,v207
mm v[104:107],a[104:107],v[236:239],v[104:107]
rd a[104:107],v248 offset:10240
cv v231,v206,v207
s_mov_b32 s93, 14336
mm v[108:111],a[108:111],v[236:239],v[108:111]
ex v208,v208
mm v[76:79],a[108:111],v[232:235],v[76:79]
rd a[108:111],v248 offset:11264
ld a[136:139],v243,s[40:43],s93 offen
mm v[80:83],a[112:115],v[232:235],v[80:83]
ex v209,v209
mm v[112:115],a[112:115],v[236:239],v[112:115]
rd a[112:115],v248 offset:12288
s_mov_b32 s93, 21504
mm v[116:119],a[116:119],v[236:239],v[116:119]
ex v210,v210
mm v[84:87],a[116:119],v[232:235],v[84:87]
rd a[116:119],v248 offset:13312
ld a[140:143],v243,s[40:43],s93 offen
mm v[88:91],a[120:123],v[232:235],v[88:91]
ex v211,v211
mm v[120:123],a[120:123],v[236:239],v[120:123]
rd a[120:123],v248 offset:14336
mm v[124:127],a[124:127],v[236:239],v[124:127]
ex v216,v216
mm v[92:95],a[124:127],v[232:235],v[92:95]
rd a[124:127],v248 offset:15360
v_or_b32 v248, s89, v255
v_smfmac_f32_16x16x64_bf16 a[196:199], a[208:211], v[232:239], v241
wt lgkmcnt(4)
v_smfmac_f32_16x16x64_bf16 a[192:195], a[208:211], v[224:231], v241
rd a[64:67],v248 offset:0
mm v[0:3],a[96:99],v[224:227],v[0:3]
ex v217,v217
mm v[32:35],a[96:99],v[228:231],v[32:35]
rd a[68:71],v248 offset:1024
cv v232,v208,v209
mm v[36:39],a[100:103],v[228:231],v[36:39]
ex v218,v218
mm v[4:7],a[100:103],v[224:227],v[4:7]
rd a[72:75],v248 offset:2048
cv v233,v210,v211
mm v[8:11],a[104:107],v[224:227],v[8:11]
ex v219,v219
mm v[40:43],a[104:107],v[228:231],v[40:43]
rd a[76:79],v248 offset:3072
cv v236,v216,v217
mm v[44:47],a[108:111],v[228:231],v[44:47]
ex v212,v212
mm v[12:15],a[108:111],v[224:227],v[12:15]
rd a[80:83],v248 offset:4096
wt lgkmcnt(5)
mm v[16:19],a[112:115],v[224:227],v[16:19]
ex v213,v213
mm v[48:51],a[112:115],v[228:231],v[48:51]
rd a[84:87],v248 offset:5120
cv v237,v218,v219
mm v[52:55],a[116:119],v[228:231],v[52:55]
ex v214,v214
mm v[20:23],a[116:119],v[224:227],v[20:23]
rd a[88:91],v248 offset:6144
cv v234,v212,v213
mm v[24:27],a[120:123],v[224:227],v[24:27]
ex v215,v215
mm v[56:59],a[120:123],v[228:231],v[56:59]
rd a[92:95],v248 offset:7168
cv v235,v214,v215
mm v[60:63],a[124:127],v[228:231],v[60:63]
ex v220,v220
mm v[28:31],a[124:127],v[224:227],v[28:31]
wt lgkmcnt(4)
mm v[192:195],a[64:67],v[128:131],0
ex v221,v221
mm v[192:195],a[68:71],v[132:135],v[192:195]
cv v238,v220,v221
s_add_u32 s89, s89, 16384
mm v[192:195],a[72:75],v[136:139],v[192:195]
ex v222,v222
mm v[192:195],a[76:79],v[140:143],v[192:195]
ld v255,s[60:63],s17 offen offset:0 sc1 lds
mm v[200:203],a[76:79],v[156:159],0
ex v223,v223
mm v[200:203],a[72:75],v[152:155],v[200:203]
cv v239,v222,v223
s_and_b32 s89, s89, 0x1ffff
mm v[200:203],a[68:71],v[148:151],v[200:203]
ex v192,v192
mm v[200:203],a[64:67],v[144:147],v[200:203]
wt lgkmcnt(0)
mm v[196:199],a[80:83],v[128:131],0
ex v193,v193
mm v[196:199],a[84:87],v[132:135],v[196:199]
cv v224,v192,v193
mm v[196:199],a[88:91],v[136:139],v[196:199]
ex v194,v194
mm v[196:199],a[92:95],v[140:143],v[196:199]
ld v255,s[60:63],s17 offen offset:1024 sc1 lds
mm v[204:207],a[92:95],v[156:159],0
ex v195,v195
mm v[204:207],a[88:91],v[152:155],v[204:207]
cv v225,v194,v195
mm v[204:207],a[84:87],v[148:151],v[204:207]
ex v200,v200
mm v[204:207],a[80:83],v[144:147],v[204:207]
ld v255,s[60:63],s17 offen offset:2048 sc1 lds
mm v[208:211],a[64:67],v[160:163],0
ex v201,v201
mm v[208:211],a[68:71],v[164:167],v[208:211]
cv v228,v200,v201
mm v[208:211],a[72:75],v[168:171],v[208:211]
ex v202,v202
mm v[208:211],a[76:79],v[172:175],v[208:211]
ld v255,s[60:63],s17 offen offset:3072 sc1 lds
mm v[216:219],a[76:79],v[188:191],0
ex v203,v203
mm v[216:219],a[72:75],v[184:187],v[216:219]
cv v229,v202,v203
s_mov_b32 s93, 28672
mm v[216:219],a[68:71],v[180:183],v[216:219]
ex v196,v196
mm v[216:219],a[64:67],v[176:179],v[216:219]
ld a[144:147],v243,s[40:43],s93 offen
mm v[212:215],a[80:83],v[160:163],0
ex v197,v197
s_add_u32 s17, s17, 8192
mm v[212:215],a[84:87],v[164:167],v[212:215]
cv v226,v196,v197
s_mov_b32 s93, 35840
s_add_u32 m0, m0, 16384
mm v[212:215],a[88:91],v[168:171],v[212:215]
ex v198,v198
s_and_b32 m0, m0, s78
mm v[212:215],a[92:95],v[172:175],v[212:215]
ld a[148:151],v243,s[40:43],s93 offen
mm v[220:223],a[92:95],v[188:191],0
ex v199,v199
mm v[220:223],a[88:91],v[184:187],v[220:223]
cv v227,v198,v199
mm v[220:223],a[84:87],v[180:183],v[220:223]
ex v204,v204
mm v[220:223],a[80:83],v[176:179],v[220:223]
wt vmcnt(15)
s_barrier
mm v[64:67],a[96:99],v[232:235],v[64:67]
ex v205,v205
mm v[96:99],a[96:99],v[236:239],v[96:99]
rd a[96:99],v248 offset:8192
cv v230,v204,v205
mm v[100:103],a[100:103],v[236:239],v[100:103]
ex v206,v206
mm v[68:71],a[100:103],v[232:235],v[68:71]
rd a[100:103],v248 offset:9216
mm v[72:75],a[104:107],v[232:235],v[72:75]
ex v207,v207
mm v[104:107],a[104:107],v[236:239],v[104:107]
rd a[104:107],v248 offset:10240
cv v231,v206,v207
s_mov_b32 s93, 43008
mm v[108:111],a[108:111],v[236:239],v[108:111]
ex v208,v208
mm v[76:79],a[108:111],v[232:235],v[76:79]
rd a[108:111],v248 offset:11264
ld a[152:155],v243,s[40:43],s93 offen
mm v[80:83],a[112:115],v[232:235],v[80:83]
ex v209,v209
mm v[112:115],a[112:115],v[236:239],v[112:115]
rd a[112:115],v248 offset:12288
s_mov_b32 s93, 50176
mm v[116:119],a[116:119],v[236:239],v[116:119]
ex v210,v210
mm v[84:87],a[116:119],v[232:235],v[84:87]
rd a[116:119],v248 offset:13312
ld a[156:159],v243,s[40:43],s93 offen
mm v[88:91],a[120:123],v[232:235],v[88:91]
ex v211,v211
mm v[120:123],a[120:123],v[236:239],v[120:123]
rd a[120:123],v248 offset:14336
mm v[124:127],a[124:127],v[236:239],v[124:127]
ex v216,v216
mm v[92:95],a[124:127],v[232:235],v[92:95]
rd a[124:127],v248 offset:15360
v_or_b32 v248, s89, v255
v_smfmac_f32_16x16x64_bf16 a[196:199], a[208:211], v[232:239], v241
wt lgkmcnt(4)
v_smfmac_f32_16x16x64_bf16 a[192:195], a[208:211], v[224:231], v241
rd a[64:67],v248 offset:0
mm v[0:3],a[96:99],v[224:227],v[0:3]
ex v217,v217
mm v[32:35],a[96:99],v[228:231],v[32:35]
rd a[68:71],v248 offset:1024
cv v232,v208,v209
mm v[36:39],a[100:103],v[228:231],v[36:39]
ex v218,v218
mm v[4:7],a[100:103],v[224:227],v[4:7]
rd a[72:75],v248 offset:2048
cv v233,v210,v211
mm v[8:11],a[104:107],v[224:227],v[8:11]
ex v219,v219
mm v[40:43],a[104:107],v[228:231],v[40:43]
rd a[76:79],v248 offset:3072
cv v236,v216,v217
mm v[44:47],a[108:111],v[228:231],v[44:47]
ex v212,v212
mm v[12:15],a[108:111],v[224:227],v[12:15]
rd a[80:83],v248 offset:4096
wt lgkmcnt(5)
mm v[16:19],a[112:115],v[224:227],v[16:19]
ex v213,v213
mm v[48:51],a[112:115],v[228:231],v[48:51]
rd a[84:87],v248 offset:5120
cv v237,v218,v219
mm v[52:55],a[116:119],v[228:231],v[52:55]
ex v214,v214
mm v[20:23],a[116:119],v[224:227],v[20:23]
rd a[88:91],v248 offset:6144
cv v234,v212,v213
mm v[24:27],a[120:123],v[224:227],v[24:27]
ex v215,v215
mm v[56:59],a[120:123],v[228:231],v[56:59]
rd a[92:95],v248 offset:7168
cv v235,v214,v215
mm v[60:63],a[124:127],v[228:231],v[60:63]
ex v220,v220
mm v[28:31],a[124:127],v[224:227],v[28:31]
wt lgkmcnt(4)
mm v[192:195],a[64:67],v[128:131],0
ex v221,v221
mm v[192:195],a[68:71],v[132:135],v[192:195]
cv v238,v220,v221
s_add_u32 s89, s89, 16384
mm v[192:195],a[72:75],v[136:139],v[192:195]
ex v222,v222
mm v[192:195],a[76:79],v[140:143],v[192:195]
ld v255,s[60:63],s17 offen offset:0 sc1 lds
mm v[200:203],a[76:79],v[156:159],0
ex v223,v223
mm v[200:203],a[72:75],v[152:155],v[200:203]
cv v239,v222,v223
s_and_b32 s89, s89, 0x1ffff
mm v[200:203],a[68:71],v[148:151],v[200:203]
ex v192,v192
mm v[200:203],a[64:67],v[144:147],v[200:203]
wt lgkmcnt(0)
mm v[196:199],a[80:83],v[128:131],0
ex v193,v193
mm v[196:199],a[84:87],v[132:135],v[196:199]
cv v224,v192,v193
mm v[196:199],a[88:91],v[136:139],v[196:199]
ex v194,v194
mm v[196:199],a[92:95],v[140:143],v[196:199]
ld v255,s[60:63],s17 offen offset:1024 sc1 lds
mm v[204:207],a[92:95],v[156:159],0
ex v195,v195
mm v[204:207],a[88:91],v[152:155],v[204:207]
cv v225,v194,v195
mm v[204:207],a[84:87],v[148:151],v[204:207]
ex v200,v200
mm v[204:207],a[80:83],v[144:147],v[204:207]
ld v255,s[60:63],s17 offen offset:2048 sc1 lds
mm v[208:211],a[64:67],v[160:163],0
ex v201,v201
mm v[208:211],a[68:71],v[164:167],v[208:211]
cv v228,v200,v201
mm v[208:211],a[72:75],v[168:171],v[208:211]
ex v202,v202
mm v[208:211],a[76:79],v[172:175],v[208:211]
ld v255,s[60:63],s17 offen offset:3072 sc1 lds
mm v[216:219],a[76:79],v[188:191],0
ex v203,v203
mm v[216:219],a[72:75],v[184:187],v[216:219]
cv v229,v202,v203
s_mov_b32 s93, 57344
mm v[216:219],a[68:71],v[180:183],v[216:219]
ex v196,v196
mm v[216:219],a[64:67],v[176:179],v[216:219]
ld a[160:163],v243,s[40:43],s93 offen
mm v[212:215],a[80:83],v[160:163],0
ex v197,v197
s_add_u32 s17, s17, 8192
mm v[212:215],a[84:87],v[164:167],v[212:215]
cv v226,v196,v197
s_mov_b32 s93, 64512
s_add_u32 m0, m0, 16384
mm v[212:215],a[88:91],v[168:171],v[212:215]
ex v198,v198
s_and_b32 m0, m0, s78
mm v[212:215],a[92:95],v[172:175],v[212:215]
ld a[164:167],v243,s[40:43],s93 offen
mm v[220:223],a[92:95],v[188:191],0
ex v199,v199
mm v[220:223],a[88:91],v[184:187],v[220:223]
cv v227,v198,v199
mm v[220:223],a[84:87],v[180:183],v[220:223]
ex v204,v204
mm v[220:223],a[80:83],v[176:179],v[220:223]
wt vmcnt(18)
s_barrier
mm v[64:67],a[96:99],v[232:235],v[64:67]
ex v205,v205
mm v[96:99],a[96:99],v[236:239],v[96:99]
rd a[96:99],v248 offset:8192
cv v230,v204,v205
mm v[100:103],a[100:103],v[236:239],v[100:103]
ex v206,v206
mm v[68:71],a[100:103],v[232:235],v[68:71]
rd a[100:103],v248 offset:9216
mm v[72:75],a[104:107],v[232:235],v[72:75]
ex v207,v207
mm v[104:107],a[104:107],v[236:239],v[104:107]
rd a[104:107],v248 offset:10240
cv v231,v206,v207
s_mov_b32 s93, 71680
mm v[108:111],a[108:111],v[236:239],v[108:111]
ex v208,v208
mm v[76:79],a[108:111],v[232:235],v[76:79]
rd a[108:111],v248 offset:11264
ld a[168:171],v243,s[40:43],s93 offen
mm v[80:83],a[112:115],v[232:235],v[80:83]
ex v209,v209
mm v[112:115],a[112:115],v[236:239],v[112:115]
rd a[112:115],v248 offset:12288
s_mov_b32 s93, 78848
mm v[116:119],a[116:119],v[236:239],v[116:119]
ex v210,v210
mm v[84:87],a[116:119],v[232:235],v[84:87]
rd a[116:119],v248 offset:13312
ld a[172:175],v243,s[40:43],s93 offen
mm v[88:91],a[120:123],v[232:235],v[88:91]
ex v211,v211
mm v[120:123],a[120:123],v[236:239],v[120:123]
rd a[120:123],v248 offset:14336
mm v[124:127],a[124:127],v[236:239],v[124:127]
ex v216,v216
mm v[92:95],a[124:127],v[232:235],v[92:95]
rd a[124:127],v248 offset:15360
v_or_b32 v248, s89, v255
v_smfmac_f32_16x16x64_bf16 a[196:199], a[208:211], v[232:239], v241
wt lgkmcnt(4)
v_smfmac_f32_16x16x64_bf16 a[192:195], a[208:211], v[224:231], v241
rd a[64:67],v248 offset:0
mm v[0:3],a[96:99],v[224:227],v[0:3]
ex v217,v217
mm v[32:35],a[96:99],v[228:231],v[32:35]
rd a[68:71],v248 offset:1024
cv v232,v208,v209
mm v[36:39],a[100:103],v[228:231],v[36:39]
ex v218,v218
mm v[4:7],a[100:103],v[224:227],v[4:7]
rd a[72:75],v248 offset:2048
cv v233,v210,v211
mm v[8:11],a[104:107],v[224:227],v[8:11]
ex v219,v219
.La5_LW5_1:
s_cbranch_execz .La5_LW5_R0
mm v[40:43],a[104:107],v[228:231],v[40:43]
rd a[76:79],v248 offset:3072
cv v236,v216,v217
mm v[44:47],a[108:111],v[228:231],v[44:47]
ex v212,v212
mm v[12:15],a[108:111],v[224:227],v[12:15]
rd a[80:83],v248 offset:4096
wt lgkmcnt(5)
mm v[16:19],a[112:115],v[224:227],v[16:19]
ex v213,v213
mm v[48:51],a[112:115],v[228:231],v[48:51]
rd a[84:87],v248 offset:5120
cv v237,v218,v219
mm v[52:55],a[116:119],v[228:231],v[52:55]
ex v214,v214
mm v[20:23],a[116:119],v[224:227],v[20:23]
rd a[88:91],v248 offset:6144
cv v234,v212,v213
mm v[24:27],a[120:123],v[224:227],v[24:27]
ex v215,v215
mm v[56:59],a[120:123],v[228:231],v[56:59]
rd a[92:95],v248 offset:7168
cv v235,v214,v215
mm v[60:63],a[124:127],v[228:231],v[60:63]
ex v220,v220
mm v[28:31],a[124:127],v[224:227],v[28:31]
wt lgkmcnt(4)
mm v[192:195],a[64:67],v[128:131],0
ex v221,v221
mm v[192:195],a[68:71],v[132:135],v[192:195]
cv v238,v220,v221
ar v240,a228
mm v[192:195],a[72:75],v[136:139],v[192:195]
ex v222,v222
mm v[192:195],a[76:79],v[140:143],v[192:195]
s_add_u32 m0, s83, 16896
s_lshl_b32 s17, s72, 18
mm v[200:203],a[76:79],v[156:159],0
ex v223,v223
mm v[200:203],a[72:75],v[152:155],v[200:203]
cv v239,v222,v223
s_add_u32 s89, s89, 16384
mm v[200:203],a[68:71],v[148:151],v[200:203]
ex v192,v192
mm v[200:203],a[64:67],v[144:147],v[200:203]
wt lgkmcnt(0)
s_and_b32 s89, s89, 0x1ffff
mm v[196:199],a[80:83],v[128:131],0
ex v193,v193
mm v[196:199],a[84:87],v[132:135],v[196:199]
cv v224,v192,v193
mm v[196:199],a[88:91],v[136:139],v[196:199]
ex v194,v194
mm v[196:199],a[92:95],v[140:143],v[196:199]
ld v240,s[56:59],s17 offen lds
mm v[204:207],a[92:95],v[156:159],0
ex v195,v195
s_add_u32 s17, s17, 32768
mm v[204:207],a[88:91],v[152:155],v[204:207]
cv v225,v194,v195
s_add_u32 m0, m0, 1024
mm v[204:207],a[84:87],v[148:151],v[204:207]
ex v200,v200
mm v[204:207],a[80:83],v[144:147],v[204:207]
ld v240,s[56:59],s17 offen lds
mm v[208:211],a[64:67],v[160:163],0
ex v201,v201
s_add_u32 s17, s17, 4294942720
mm v[208:211],a[68:71],v[164:167],v[208:211]
cv v228,v200,v201
s_add_u32 m0, m0, 1152
mm v[208:211],a[72:75],v[168:171],v[208:211]
ex v202,v202
mm v[208:211],a[76:79],v[172:175],v[208:211]
ld v240,s[56:59],s17 offen lds
mm v[216:219],a[76:79],v[188:191],0
ex v203,v203
s_add_u32 s17, s17, 32768
mm v[216:219],a[72:75],v[184:187],v[216:219]
cv v229,v202,v203
s_add_u32 m0, m0, 1024
mm v[216:219],a[68:71],v[180:183],v[216:219]
ex v196,v196
mm v[216:219],a[64:67],v[176:179],v[216:219]
ld v240,s[56:59],s17 offen lds
mm v[212:215],a[80:83],v[160:163],0
ex v197,v197
s_add_u32 s17, s17, 90112
mm v[212:215],a[84:87],v[164:167],v[212:215]
cv v226,v196,v197
s_mov_b32 s93, 86016
s_add_u32 m0, s83, 33792
mm v[212:215],a[88:91],v[168:171],v[212:215]
ex v198,v198
mm v[212:215],a[92:95],v[172:175],v[212:215]
ld a[176:179],v243,s[40:43],s93 offen
mm v[220:223],a[92:95],v[188:191],0
ex v199,v199
mm v[220:223],a[88:91],v[184:187],v[220:223]
cv v227,v198,v199
mm v[220:223],a[84:87],v[180:183],v[220:223]
ex v204,v204
mm v[220:223],a[80:83],v[176:179],v[220:223]
wt vmcnt(17)
s_barrier
mm v[64:67],a[96:99],v[232:235],v[64:67]
ex v205,v205
mm v[96:99],a[96:99],v[236:239],v[96:99]
rd a[96:99],v248 offset:8192
cv v230,v204,v205
mm v[100:103],a[100:103],v[236:239],v[100:103]
ex v206,v206
mm v[68:71],a[100:103],v[232:235],v[68:71]
rd a[100:103],v248 offset:9216
mm v[72:75],a[104:107],v[232:235],v[72:75]
ex v207,v207
mm v[104:107],a[104:107],v[236:239],v[104:107]
rd a[104:107],v248 offset:10240
cv v231,v206,v207
s_mov_b32 s93, 93184
mm v[108:111],a[108:111],v[236:239],v[108:111]
ex v208,v208
mm v[76:79],a[108:111],v[232:235],v[76:79]
rd a[108:111],v248 offset:11264
ld a[180:183],v243,s[40:43],s93 offen
mm v[80:83],a[112:115],v[232:235],v[80:83]
ex v209,v209
mm v[112:115],a[112:115],v[236:239],v[112:115]
rd a[112:115],v248 offset:12288
s_mov_b32 s93, 100352
mm v[116:119],a[116:119],v[236:239],v[116:119]
ex v210,v210
mm v[84:87],a[116:119],v[232:235],v[84:87]
rd a[116:119],v248 offset:13312
ld a[184:187],v243,s[40:43],s93 offen
mm v[88:91],a[120:123],v[232:235],v[88:91]
ex v211,v211
mm v[120:123],a[120:123],v[236:239],v[120:123]
rd a[120:123],v248 offset:14336
mm v[124:127],a[124:127],v[236:239],v[124:127]
ex v216,v216
mm v[92:95],a[124:127],v[232:235],v[92:95]
rd a[124:127],v248 offset:15360
v_or_b32 v248, s89, v255
v_smfmac_f32_16x16x64_bf16 a[196:199], a[208:211], v[232:239], v241
wt lgkmcnt(4)
v_smfmac_f32_16x16x64_bf16 a[192:195], a[208:211], v[224:231], v241
rd a[64:67],v248 offset:0
mm v[0:3],a[96:99],v[224:227],v[0:3]
ex v217,v217
mm v[32:35],a[96:99],v[228:231],v[32:35]
rd a[68:71],v248 offset:1024
cv v232,v208,v209
mm v[36:39],a[100:103],v[228:231],v[36:39]
ex v218,v218
mm v[4:7],a[100:103],v[224:227],v[4:7]
rd a[72:75],v248 offset:2048
cv v233,v210,v211
mm v[8:11],a[104:107],v[224:227],v[8:11]
ex v219,v219
mm v[40:43],a[104:107],v[228:231],v[40:43]
rd a[76:79],v248 offset:3072
cv v236,v216,v217
mm v[44:47],a[108:111],v[228:231],v[44:47]
ex v212,v212
mm v[12:15],a[108:111],v[224:227],v[12:15]
rd a[80:83],v248 offset:4096
wt lgkmcnt(5)
mm v[16:19],a[112:115],v[224:227],v[16:19]
ex v213,v213
mm v[48:51],a[112:115],v[228:231],v[48:51]
rd a[84:87],v248 offset:5120
cv v237,v218,v219
mm v[52:55],a[116:119],v[228:231],v[52:55]
ex v214,v214
mm v[20:23],a[116:119],v[224:227],v[20:23]
rd a[88:91],v248 offset:6144
cv v234,v212,v213
mm v[24:27],a[120:123],v[224:227],v[24:27]
ex v215,v215
mm v[56:59],a[120:123],v[228:231],v[56:59]
rd a[92:95],v248 offset:7168
cv v235,v214,v215
mm v[60:63],a[124:127],v[228:231],v[60:63]
ex v220,v220
mm v[28:31],a[124:127],v[224:227],v[28:31]
buffer_load_dword v250, v251, s[64:67], s20 offen sc1
wt lgkmcnt(4)
mm v[192:195],a[64:67],v[128:131],0
ex v221,v221
mm v[192:195],a[68:71],v[132:135],v[192:195]
cv v238,v220,v221
s_add_u32 s89, s89, 16384
mm v[192:195],a[72:75],v[136:139],v[192:195]
ex v222,v222
mm v[192:195],a[76:79],v[140:143],v[192:195]
ld v240,s[56:59],s17 offen lds
mm v[200:203],a[76:79],v[156:159],0
ex v223,v223
s_add_u32 s17, s17, 32768
mm v[200:203],a[72:75],v[152:155],v[200:203]
cv v239,v222,v223
s_add_u32 m0, m0, 1024
mm v[200:203],a[68:71],v[148:151],v[200:203]
ex v192,v192
mm v[200:203],a[64:67],v[144:147],v[200:203]
wt lgkmcnt(0)
s_and_b32 s89, s89, 0x1ffff
mm v[196:199],a[80:83],v[128:131],0
ex v193,v193
mm v[196:199],a[84:87],v[132:135],v[196:199]
cv v224,v192,v193
mm v[196:199],a[88:91],v[136:139],v[196:199]
ex v194,v194
mm v[196:199],a[92:95],v[140:143],v[196:199]
ld v240,s[56:59],s17 offen lds
mm v[204:207],a[92:95],v[156:159],0
ex v195,v195
s_add_u32 s17, s17, 4294942720
mm v[204:207],a[88:91],v[152:155],v[204:207]
cv v225,v194,v195
s_add_u32 m0, m0, 1152
mm v[204:207],a[84:87],v[148:151],v[204:207]
ex v200,v200
mm v[204:207],a[80:83],v[144:147],v[204:207]
ld v240,s[56:59],s17 offen lds
mm v[208:211],a[64:67],v[160:163],0
ex v201,v201
s_add_u32 s17, s17, 32768
mm v[208:211],a[68:71],v[164:167],v[208:211]
cv v228,v200,v201
s_add_u32 m0, m0, 1024
mm v[208:211],a[72:75],v[168:171],v[208:211]
ex v202,v202
mm v[208:211],a[76:79],v[172:175],v[208:211]
ld v240,s[56:59],s17 offen lds
mm v[216:219],a[76:79],v[188:191],0
ex v203,v203
mm v[216:219],a[72:75],v[184:187],v[216:219]
cv v229,v202,v203
s_mov_b32 s93, 107520
mm v[216:219],a[68:71],v[180:183],v[216:219]
ex v196,v196
mm v[216:219],a[64:67],v[176:179],v[216:219]
ld a[188:191],v243,s[40:43],s93 offen
mm v[212:215],a[80:83],v[160:163],0
ex v197,v197
s_bfe_u32 s17, s4, 262148
mm v[212:215],a[84:87],v[164:167],v[212:215]
cv v226,v196,v197
s_xor_b32 s17, s17, s72
s_lshl_b32 s17, s17, 18
mm v[212:215],a[88:91],v[168:171],v[212:215]
ex v198,v198
s_add_u32 m0, s83, 50688
mm v[212:215],a[92:95],v[172:175],v[212:215]
mm v[220:223],a[92:95],v[188:191],0
ex v199,v199
mm v[220:223],a[88:91],v[184:187],v[220:223]
cv v227,v198,v199
wt vmcnt(5)
v_cmp_eq_u32 s[92:93], v250, v249
s_mov_b32 s94, 16384
mm v[220:223],a[84:87],v[180:183],v[220:223]
ex v204,v204
mm v[220:223],a[80:83],v[176:179],v[220:223]
s_nop 0
s_barrier
mm v[64:67],a[96:99],v[232:235],v[64:67]
ex v205,v205
mm v[96:99],a[96:99],v[236:239],v[96:99]
rd a[96:99],v248 offset:8192
cv v230,v204,v205
s_and_b32 s92, s92, 3
s_andn2_b32 s92, s92, s23
s_cmp_eq_u32 s92, 3
s_cselect_b32 s70, 17408, 0
s_cselect_b32 s84, 3, s23
mm v[100:103],a[100:103],v[236:239],v[100:103]
ex v206,v206
mm v[68:71],a[100:103],v[232:235],v[68:71]
rd a[100:103],v248 offset:9216
ld a[220:223],v255,s[68:71],s94 offen sc1
mm v[72:75],a[104:107],v[232:235],v[72:75]
ex v207,v207
mm v[104:107],a[104:107],v[236:239],v[104:107]
rd a[104:107],v248 offset:10240
cv v231,v206,v207
mm v[108:111],a[108:111],v[236:239],v[108:111]
ex v208,v208
mm v[76:79],a[108:111],v[232:235],v[76:79]
rd a[108:111],v248 offset:11264
ld a[0:3],v255,s[68:71],0 offen offset:0 sc1
mm v[80:83],a[112:115],v[232:235],v[80:83]
ex v209,v209
mm v[112:115],a[112:115],v[236:239],v[112:115]
rd a[112:115],v248 offset:12288
mm v[116:119],a[116:119],v[236:239],v[116:119]
ex v210,v210
mm v[84:87],a[116:119],v[232:235],v[84:87]
rd a[116:119],v248 offset:13312
ld a[4:7],v255,s[68:71],0 offen offset:1024 sc1
mm v[88:91],a[120:123],v[232:235],v[88:91]
ex v211,v211
mm v[120:123],a[120:123],v[236:239],v[120:123]
rd a[120:123],v248 offset:14336
mm v[124:127],a[124:127],v[236:239],v[124:127]
ex v216,v216
mm v[92:95],a[124:127],v[232:235],v[92:95]
rd a[124:127],v248 offset:15360
v_or_b32 v248, s89, v255
buffer_load_dword v250, v251, s[64:67], s20 offen sc1
v_smfmac_f32_16x16x64_bf16 a[196:199], a[208:211], v[232:239], v241
wt lgkmcnt(4)
v_smfmac_f32_16x16x64_bf16 a[192:195], a[208:211], v[224:231], v241
rd a[64:67],v248 offset:0
mm v[0:3],a[96:99],v[224:227],v[0:3]
ex v217,v217
mm v[32:35],a[96:99],v[228:231],v[32:35]
rd a[68:71],v248 offset:1024
cv v232,v208,v209
ld a[8:11],v255,s[68:71],0 offen offset:2048 sc1
mm v[36:39],a[100:103],v[228:231],v[36:39]
ex v218,v218
mm v[4:7],a[100:103],v[224:227],v[4:7]
rd a[72:75],v248 offset:2048
cv v233,v210,v211
mm v[8:11],a[104:107],v[224:227],v[8:11]
ex v219,v219
mm v[40:43],a[104:107],v[228:231],v[40:43]
rd a[76:79],v248 offset:3072
cv v236,v216,v217
ld a[12:15],v255,s[68:71],0 offen offset:3072 sc1
mm v[44:47],a[108:111],v[228:231],v[44:47]
ex v212,v212
mm v[12:15],a[108:111],v[224:227],v[12:15]
rd a[80:83],v248 offset:4096
wt lgkmcnt(5)
mm v[16:19],a[112:115],v[224:227],v[16:19]
ex v213,v213
mm v[48:51],a[112:115],v[228:231],v[48:51]
rd a[84:87],v248 offset:5120
cv v237,v218,v219
ld a[16:19],v255,s[68:71],s95 offen offset:0 sc1
mm v[52:55],a[116:119],v[228:231],v[52:55]
ex v214,v214
mm v[20:23],a[116:119],v[224:227],v[20:23]
rd a[88:91],v248 offset:6144
cv v234,v212,v213
mm v[24:27],a[120:123],v[224:227],v[24:27]
ex v215,v215
mm v[56:59],a[120:123],v[228:231],v[56:59]
rd a[92:95],v248 offset:7168
cv v235,v214,v215
ld a[20:23],v255,s[68:71],s95 offen offset:1024 sc1
mm v[60:63],a[124:127],v[228:231],v[60:63]
ex v220,v220
mm v[28:31],a[124:127],v[224:227],v[28:31]
ld a[24:27],v255,s[68:71],s95 offen offset:2048 sc1
wt lgkmcnt(4)
mm v[192:195],a[64:67],v[128:131],0
ex v221,v221
mm v[192:195],a[68:71],v[132:135],v[192:195]
cv v238,v220,v221
mm v[192:195],a[72:75],v[136:139],v[192:195]
ex v222,v222
mm v[192:195],a[76:79],v[140:143],v[192:195]
ld v240,s[56:59],s17 offen lds
mm v[200:203],a[76:79],v[156:159],0
ex v223,v223
s_add_u32 s17, s17, 32768
mm v[200:203],a[72:75],v[152:155],v[200:203]
cv v239,v222,v223
s_add_u32 m0, m0, 1024
mm v[200:203],a[68:71],v[148:151],v[200:203]
ex v192,v192
mm v[200:203],a[64:67],v[144:147],v[200:203]
wt lgkmcnt(0)
mm v[196:199],a[80:83],v[128:131],0
ex v193,v193
mm v[196:199],a[84:87],v[132:135],v[196:199]
cv v224,v192,v193
wt vmcnt(6)
v_cmp_eq_u32 s[92:93], v250, v249
mm v[196:199],a[88:91],v[136:139],v[196:199]
ex v194,v194
mm v[196:199],a[92:95],v[140:143],v[196:199]
ld v240,s[56:59],s17 offen lds
mm v[204:207],a[92:95],v[156:159],0
ex v195,v195
s_add_u32 s17, s17, 4294942720
mm v[204:207],a[88:91],v[152:155],v[204:207]
cv v225,v194,v195
s_add_u32 m0, m0, 1152
s_and_b32 s92, s92, 3
s_sub_u32 s92, s92, s84
s_cmp_eq_u32 s92, 3
s_cbranch_scc1 .La5_LA_PS0
.La5_LA_PS1:
mm v[204:207],a[84:87],v[148:151],v[204:207]
ex v200,v200
mm v[204:207],a[80:83],v[144:147],v[204:207]
ld v240,s[56:59],s17 offen lds
mm v[208:211],a[64:67],v[160:163],0
ex v201,v201
s_add_u32 s17, s17, 32768
mm v[208:211],a[68:71],v[164:167],v[208:211]
cv v228,v200,v201
s_add_u32 m0, m0, 1024
ld a[28:31],v255,s[68:71],s95 offen offset:3072 sc1
mm v[208:211],a[72:75],v[168:171],v[208:211]
ex v202,v202
mm v[208:211],a[76:79],v[172:175],v[208:211]
ld v240,s[56:59],s17 offen lds
mm v[216:219],a[76:79],v[188:191],0
ex v203,v203
mm v[216:219],a[72:75],v[184:187],v[216:219]
cv v229,v202,v203
ld a[32:35],v255,s[68:71],s96 offen offset:0 sc1
mm v[216:219],a[68:71],v[180:183],v[216:219]
ex v196,v196
mm v[216:219],a[64:67],v[176:179],v[216:219]
mm v[212:215],a[80:83],v[160:163],0
ex v197,v197
mm v[212:215],a[84:87],v[164:167],v[212:215]
cv v226,v196,v197
buffer_load_dword v250, v251, s[64:67], s20 offen sc1
mm v[212:215],a[88:91],v[168:171],v[212:215]
ex v198,v198
mm v[212:215],a[92:95],v[172:175],v[212:215]
ld a[36:39],v255,s[68:71],s96 offen offset:1024 sc1
mm v[220:223],a[92:95],v[188:191],0
ex v199,v199
mm v[220:223],a[88:91],v[184:187],v[220:223]
cv v227,v198,v199
mm v[220:223],a[84:87],v[180:183],v[220:223]
ex v204,v204
mm v[220:223],a[80:83],v[176:179],v[220:223]
ld a[40:43],v255,s[68:71],s96 offen offset:2048 sc1
mm v[64:67],a[96:99],v[232:235],v[64:67]
ex v205,v205
mm v[96:99],a[96:99],v[236:239],v[96:99]
rd a[96:99],v248 offset:8192
cv v230,v204,v205
mm v[100:103],a[100:103],v[236:239],v[100:103]
ex v206,v206
mm v[68:71],a[100:103],v[232:235],v[68:71]
rd a[100:103],v248 offset:9216
ld a[44:47],v255,s[68:71],s96 offen offset:3072 sc1
mm v[72:75],a[104:107],v[232:235],v[72:75]
ex v207,v207
mm v[104:107],a[104:107],v[236:239],v[104:107]
rd a[104:107],v248 offset:10240
cv v231,v206,v207
mm v[108:111],a[108:111],v[236:239],v[108:111]
ex v208,v208
mm v[76:79],a[108:111],v[232:235],v[76:79]
rd a[108:111],v248 offset:11264
ld a[48:51],v255,s[68:71],s97 offen offset:0 sc1
mm v[80:83],a[112:115],v[232:235],v[80:83]
ex v209,v209
mm v[112:115],a[112:115],v[236:239],v[112:115]
rd a[112:115],v248 offset:12288
mm v[116:119],a[116:119],v[236:239],v[116:119]
ex v210,v210
mm v[84:87],a[116:119],v[232:235],v[84:87]
rd a[116:119],v248 offset:13312
ld a[52:55],v255,s[68:71],s97 offen offset:1024 sc1
mm v[88:91],a[120:123],v[232:235],v[88:91]
ex v211,v211
mm v[120:123],a[120:123],v[236:239],v[120:123]
rd a[120:123],v248 offset:14336
mm v[124:127],a[124:127],v[236:239],v[124:127]
ex v216,v216
mm v[92:95],a[124:127],v[232:235],v[92:95]
rd a[124:127],v248 offset:15360
ld a[56:59],v255,s[68:71],s97 offen offset:2048 sc1
v_smfmac_f32_16x16x64_bf16 a[196:199], a[208:211], v[232:239], v241
wt lgkmcnt(4)
v_smfmac_f32_16x16x64_bf16 a[192:195], a[208:211], v[224:231], v241
ld a[60:63],v255,s[68:71],s97 offen offset:3072 sc1
mm v[0:3],a[96:99],v[224:227],v[0:3]
ex v217,v217
mm v[32:35],a[96:99],v[228:231],v[32:35]
cv v232,v208,v209
mm v[36:39],a[100:103],v[228:231],v[36:39]
ex v218,v218
mm v[4:7],a[100:103],v[224:227],v[4:7]
cv v233,v210,v211
mm v[8:11],a[104:107],v[224:227],v[8:11]
ex v219,v219
mm v[40:43],a[104:107],v[228:231],v[40:43]
cv v236,v216,v217
mm v[44:47],a[108:111],v[228:231],v[44:47]
ex v212,v212
wt vmcnt(7)
v_cmp_eq_u32 s[92:93], v250, v249
mm v[12:15],a[108:111],v[224:227],v[12:15]
wt lgkmcnt(0)
mm v[16:19],a[112:115],v[224:227],v[16:19]
ex v213,v213
mm v[48:51],a[112:115],v[228:231],v[48:51]
cv v237,v218,v219
s_and_b32 s92, s92, 3
s_sub_u32 s92, s92, s84
mm v[52:55],a[116:119],v[228:231],v[52:55]
ex v214,v214
mm v[20:23],a[116:119],v[224:227],v[20:23]
cv v234,v212,v213
mm v[24:27],a[120:123],v[224:227],v[24:27]
ex v215,v215
mm v[56:59],a[120:123],v[228:231],v[56:59]
cv v235,v214,v215
mm v[60:63],a[124:127],v[228:231],v[60:63]
ex v220,v220
mm v[28:31],a[124:127],v[224:227],v[28:31]
s_cmp_eq_u32 s92, 3
s_cbranch_scc1 .La5_LA_PS2
.La5_LA_PS3:
s_cmp_lg_u32 s84, 3
s_cbranch_scc1 .La5_LA_PSPIN
.La5_LA_DC1:
wt vmcnt(8)
mm v[0:3],a[214:217],a[0:3],v[0:3]
ar v250,a192
ar v251,a193
v_add_f32 v250, v250, v251
ar v251,a220
v_add_f32 v250, v250, v251
ar v249,a194
mm v[4:7],a[212:215],a[0:3],v[4:7]
ar v251,a195
v_add_f32 v249, v249, v251
ar v251,a221
v_add_f32 v249, v249, v251
v_subrev_u32 v251, 562036736, v250
mm v[8:11],a[214:217],a[4:7],v[8:11]
v_rcp_f32 v250, v250
v_cmp_le_u32 vcc, 1006632960, v251
v_subrev_u32 v251, 562036736, v249
s_mov_b64 s[92:93], vcc
v_cmp_le_u32 vcc, 1006632960, v251
mm v[12:15],a[212:215],a[4:7],v[12:15]
v_rcp_f32 v249, v249
mm v[16:19],a[214:217],a[8:11],v[16:19]
s_or_b64 vcc, vcc, s[92:93]
s_cbranch_scc1 .La5_LA_SC3
.La5_LA_SR3:
ex v221,v221
ex v222,v222
mm v[20:23],a[212:215],a[8:11],v[20:23]
ex v223,v223
cv v238,v220,v221
v_mul_f32 v0, v0, v250
v_mul_f32 v1, v1, v250
v_mul_f32 v2, v2, v250
mm v[24:27],a[214:217],a[12:15],v[24:27]
v_mul_f32 v3, v3, v250
v_mul_f32 v4, v4, v250
v_mul_f32 v5, v5, v250
v_mul_f32 v6, v6, v250
v_mul_f32 v7, v7, v250
cv v128,v0,v1
mm v[28:31],a[212:215],a[12:15],v[28:31]
cv v129,v2,v3
cv v130,v4,v5
cv v131,v6,v7
st v[128:131],v244,s[44:47],s91 offen offset:0 sc1
cv v239,v222,v223
mm v[32:35],a[214:217],a[16:19],v[32:35]
v_mul_f32 v8, v8, v250
v_mul_f32 v9, v9, v250
v_mul_f32 v10, v10, v250
v_mul_f32 v11, v11, v250
v_mul_f32 v12, v12, v250
v_mul_f32 v13, v13, v250
mm v[36:39],a[212:215],a[16:19],v[36:39]
v_mul_f32 v14, v14, v250
v_mul_f32 v15, v15, v250
cv v132,v8,v9
cv v133,v10,v11
cv v134,v12,v13
cv v135,v14,v15
mm v[40:43],a[214:217],a[20:23],v[40:43]
st v[132:135],v244,s[44:47],s91 offen offset:1024 sc1
v_mul_f32 v16, v16, v250
v_mul_f32 v17, v17, v250
v_mul_f32 v18, v18, v250
v_mul_f32 v19, v19, v250
mm v[44:47],a[212:215],a[20:23],v[44:47]
v_mul_f32 v20, v20, v250
v_mul_f32 v21, v21, v250
v_mul_f32 v22, v22, v250
v_mul_f32 v23, v23, v250
cv v136,v16,v17
cv v137,v18,v19
mm v[48:51],a[214:217],a[24:27],v[48:51]
cv v138,v20,v21
cv v139,v22,v23
st v[136:139],v244,s[44:47],s91 offen offset:2048 sc1
v_mul_f32 v24, v24, v250
v_mul_f32 v25, v25, v250
mm v[52:55],a[212:215],a[24:27],v[52:55]
v_mul_f32 v26, v26, v250
v_mul_f32 v27, v27, v250
v_mul_f32 v28, v28, v250
v_mul_f32 v29, v29, v250
.La5_LW5_2:
s_cbranch_execz .La5_LW5_R0
v_mul_f32 v30, v30, v250
v_mul_f32 v31, v31, v250
mm v[56:59],a[214:217],a[28:31],v[56:59]
cv v140,v24,v25
cv v141,v26,v27
cv v142,v28,v29
cv v143,v30,v31
st v[140:143],v244,s[44:47],s91 offen offset:3072 sc1
mm v[60:63],a[212:215],a[28:31],v[60:63]
v_mul_f32 v32, v32, v249
v_mul_f32 v33, v33, v249
v_mul_f32 v34, v34, v249
v_mul_f32 v35, v35, v249
v_mul_f32 v36, v36, v249
v_mul_f32 v37, v37, v249
v_smfmac_f32_16x16x64_bf16 a[196:199], a[208:211], v[232:239], v241
v_mul_f32 v38, v38, v249
v_mul_f32 v39, v39, v249
cv v144,v32,v33
cv v145,v34,v35
cv v146,v36,v37
mm v[64:67],a[96:99],v[232:235],v[64:67]
cv v147,v38,v39
st v[144:147],v245,s[44:47],s91 offen offset:0 sc1
v_mul_f32 v40, v40, v249
v_mul_f32 v41, v41, v249
v_mul_f32 v42, v42, v249
mm v[96:99],a[96:99],v[236:239],v[96:99]
v_mul_f32 v43, v43, v249
v_mul_f32 v44, v44, v249
v_mul_f32 v45, v45, v249
v_mul_f32 v46, v46, v249
v_mul_f32 v47, v47, v249
cv v148,v40,v41
mm v[100:103],a[100:103],v[236:239],v[100:103]
cv v149,v42,v43
cv v150,v44,v45
cv v151,v46,v47
st v[148:151],v245,s[44:47],s91 offen offset:1024 sc1
v_mul_f32 v48, v48, v249
mm v[68:71],a[100:103],v[232:235],v[68:71]
v_mul_f32 v49, v49, v249
v_mul_f32 v50, v50, v249
v_mul_f32 v51, v51, v249
v_mul_f32 v52, v52, v249
v_mul_f32 v53, v53, v249
v_mul_f32 v54, v54, v249
mm v[72:75],a[104:107],v[232:235],v[72:75]
v_mul_f32 v55, v55, v249
cv v152,v48,v49
cv v153,v50,v51
cv v154,v52,v53
cv v155,v54,v55
st v[152:155],v245,s[44:47],s91 offen offset:2048 sc1
mm v[104:107],a[104:107],v[236:239],v[104:107]
v_mul_f32 v56, v56, v249
v_mul_f32 v57, v57, v249
v_mul_f32 v58, v58, v249
v_mul_f32 v59, v59, v249
v_mul_f32 v60, v60, v249
v_mul_f32 v61, v61, v249
mm v[108:111],a[108:111],v[236:239],v[108:111]
v_mul_f32 v62, v62, v249
v_mul_f32 v63, v63, v249
cv v156,v56,v57
cv v157,v58,v59
cv v158,v60,v61
cv v159,v62,v63
mm v[76:79],a[108:111],v[232:235],v[76:79]
st v[156:159],v245,s[44:47],s91 offen offset:3072 sc1
ar v240,a224
v_xor_b32 v241, 32, v240
v_xor_b32 v242, 64, v240
v_xor_b32 v243, 96, v240
mm v[80:83],a[112:115],v[232:235],v[80:83]
mm v[112:115],a[112:115],v[236:239],v[112:115]
mm v[116:119],a[116:119],v[236:239],v[116:119]
mm v[84:87],a[116:119],v[232:235],v[84:87]
mm v[88:91],a[120:123],v[232:235],v[88:91]
mm v[120:123],a[120:123],v[236:239],v[120:123]
mm v[124:127],a[124:127],v[236:239],v[124:127]
mm v[92:95],a[124:127],v[232:235],v[92:95]
s_nop 0
s_barrier
wt vmcnt(8)
mm v[64:67],a[214:217],a[32:35],v[64:67]
rt v[192:193],v240 offset:16896
rt v[194:195],v240 offset:17920
mm v[68:71],a[212:215],a[32:35],v[68:71]
rt v[196:197],v240 offset:25344
rt v[198:199],v240 offset:26368
mm v[72:75],a[214:217],a[36:39],v[72:75]
rt v[200:201],v241 offset:16896
rt v[202:203],v241 offset:17920
ar v250,a196
ar v251,a197
v_add_f32 v250, v250, v251
ar v251,a222
mm v[76:79],a[212:215],a[36:39],v[76:79]
rt v[204:205],v241 offset:25344
rt v[206:207],v241 offset:26368
v_add_f32 v250, v250, v251
ar v249,a198
ar v251,a199
v_add_f32 v249, v249, v251
mm v[80:83],a[214:217],a[40:43],v[80:83]
rt v[208:209],v242 offset:16896
rt v[210:211],v242 offset:17920
ar v251,a223
v_add_f32 v249, v249, v251
v_subrev_u32 v251, 562036736, v250
mm v[84:87],a[212:215],a[40:43],v[84:87]
rt v[212:213],v242 offset:25344
rt v[214:215],v242 offset:26368
v_rcp_f32 v250, v250
v_cmp_le_u32 vcc, 1006632960, v251
v_subrev_u32 v251, 562036736, v249
mm v[88:91],a[214:217],a[44:47],v[88:91]
rt v[216:217],v243 offset:16896
rt v[218:219],v243 offset:17920
s_mov_b64 s[92:93], vcc
v_cmp_le_u32 vcc, 1006632960, v251
v_rcp_f32 v249, v249
mm v[92:95],a[212:215],a[44:47],v[92:95]
rt v[220:221],v243 offset:25344
rt v[222:223],v243 offset:26368
s_or_b64 vcc, vcc, s[92:93]
s_cbranch_scc1 .La5_LA_SC4
.La5_LA_SR4:
v_mul_f32 v64, v64, v250
v_mul_f32 v65, v65, v250
mm v[96:99],a[214:217],a[48:51],v[96:99]
v_mul_f32 v66, v66, v250
v_mul_f32 v67, v67, v250
v_mul_f32 v68, v68, v250
v_mul_f32 v69, v69, v250
v_mul_f32 v70, v70, v250
v_mul_f32 v71, v71, v250
mm v[100:103],a[212:215],a[48:51],v[100:103]
cv v160,v64,v65
cv v161,v66,v67
cv v162,v68,v69
cv v163,v70,v71
st v[160:163],v246,s[44:47],s91 offen offset:0 sc1
mm v[104:107],a[214:217],a[52:55],v[104:107]
v_mul_f32 v72, v72, v250
v_mul_f32 v73, v73, v250
v_mul_f32 v74, v74, v250
v_mul_f32 v75, v75, v250
v_mul_f32 v76, v76, v250
v_mul_f32 v77, v77, v250
mm v[108:111],a[212:215],a[52:55],v[108:111]
v_mul_f32 v78, v78, v250
v_mul_f32 v79, v79, v250
cv v164,v72,v73
cv v165,v74,v75
cv v166,v76,v77
cv v167,v78,v79
mm v[112:115],a[214:217],a[56:59],v[112:115]
st v[164:167],v246,s[44:47],s91 offen offset:1024 sc1
v_mul_f32 v80, v80, v250
v_mul_f32 v81, v81, v250
v_mul_f32 v82, v82, v250
v_mul_f32 v83, v83, v250
mm v[116:119],a[212:215],a[56:59],v[116:119]
v_mul_f32 v84, v84, v250
v_mul_f32 v85, v85, v250
v_mul_f32 v86, v86, v250
v_mul_f32 v87, v87, v250
cv v168,v80,v81
cv v169,v82,v83
mm v[120:123],a[214:217],a[60:63],v[120:123]
cv v170,v84,v85
cv v171,v86,v87
st v[168:171],v246,s[44:47],s91 offen offset:2048 sc1
v_mul_f32 v88, v88, v250
v_mul_f32 v89, v89, v250
mm v[124:127],a[212:215],a[60:63],v[124:127]
wt lgkmcnt(12)
v_mul_f32 v90, v90, v250
v_mul_f32 v91, v91, v250
v_mul_f32 v92, v92, v250
v_mul_f32 v93, v93, v250
v_mul_f32 v94, v94, v250
mm v[0:3],v[192:195],v[128:131],0
v_mul_f32 v95, v95, v250
cv v172,v88,v89
cv v173,v90,v91
cv v174,v92,v93
cv v175,v94,v95
st v[172:175],v246,s[44:47],s91 offen offset:3072 sc1
mm v[0:3],v[196:199],v[132:135],v[0:3]
v_mul_f32 v96, v96, v249
v_mul_f32 v97, v97, v249
v_mul_f32 v98, v98, v249
v_mul_f32 v99, v99, v249
v_mul_f32 v100, v100, v249
v_mul_f32 v101, v101, v249
mm v[32:35],v[196:199],v[148:151],0
wt lgkmcnt(8)
v_mul_f32 v102, v102, v249
v_mul_f32 v103, v103, v249
cv v176,v96,v97
cv v177,v98,v99
cv v178,v100,v101
mm v[32:35],v[192:195],v[144:147],v[32:35]
rt v[196:197],v240 offset:17024
rt v[198:199],v240 offset:18048
cv v179,v102,v103
st v[176:179],v247,s[44:47],s91 offen offset:0 sc1
v_mul_f32 v104, v104, v249
mm v[36:39],v[200:203],v[144:147],0
v_mul_f32 v105, v105, v249
v_mul_f32 v106, v106, v249
v_mul_f32 v107, v107, v249
v_mul_f32 v108, v108, v249
v_mul_f32 v109, v109, v249
v_mul_f32 v110, v110, v249
mm v[36:39],v[204:207],v[148:151],v[36:39]
rt v[192:193],v240 offset:25472
rt v[194:195],v240 offset:26496
v_mul_f32 v111, v111, v249
cv v180,v104,v105
cv v181,v106,v107
cv v182,v108,v109
mm v[4:7],v[204:207],v[132:135],0
wt lgkmcnt(4)
cv v183,v110,v111
st v[180:183],v247,s[44:47],s91 offen offset:1024 sc1
v_mul_f32 v112, v112, v249
v_mul_f32 v113, v113, v249
mm v[4:7],v[200:203],v[128:131],v[4:7]
rt v[204:205],v241 offset:17024
rt v[206:207],v241 offset:18048
v_mul_f32 v114, v114, v249
v_mul_f32 v115, v115, v249
v_mul_f32 v116, v116, v249
v_mul_f32 v117, v117, v249
mm v[8:11],v[208:211],v[128:131],0
v_mul_f32 v118, v118, v249
v_mul_f32 v119, v119, v249
cv v184,v112,v113
cv v185,v114,v115
cv v186,v116,v117
cv v187,v118,v119
mm v[8:11],v[212:215],v[132:135],v[8:11]
rt v[200:201],v241 offset:25472
rt v[202:203],v241 offset:26496
st v[184:187],v247,s[44:47],s91 offen offset:2048 sc1
v_mul_f32 v120, v120, v249
v_mul_f32 v121, v121, v249
mm v[40:43],v[212:215],v[148:151],0
v_mul_f32 v122, v122, v249
v_mul_f32 v123, v123, v249
v_mul_f32 v124, v124, v249
v_mul_f32 v125, v125, v249
v_mul_f32 v126, v126, v249
v_mul_f32 v127, v127, v249
mm v[40:43],v[208:211],v[144:147],v[40:43]
rt v[212:213],v242 offset:17024
rt v[214:215],v242 offset:18048
cv v188,v120,v121
cv v189,v122,v123
cv v190,v124,v125
cv v191,v126,v127
mm v[44:47],v[216:219],v[144:147],0
st v[188:191],v247,s[44:47],s91 offen offset:3072 sc1
mm v[44:47],v[220:223],v[148:151],v[44:47]
rt v[208:209],v242 offset:25472
rt v[210:211],v242 offset:26496
mm v[12:15],v[220:223],v[132:135],0
wt lgkmcnt(4)
mm v[12:15],v[216:219],v[128:131],v[12:15]
rt v[220:221],v243 offset:17024
rt v[222:223],v243 offset:18048
mm v[16:19],v[196:199],v[128:131],0
ar v224,a228
v_add_u32 v225, 0x8000, v224
v_add_u32 v226, 0x2000, v224
v_add_u32 v227, 0xa000, v224
mm v[16:19],v[192:195],v[132:135],v[16:19]
rt v[216:217],v243 offset:25472
rt v[218:219],v243 offset:26496
ar v248,a225
ar v228,a229
mm v[48:51],v[192:195],v[148:151],0
ar v229,a230
mm v[48:51],v[196:199],v[144:147],v[48:51]
rt v[192:193],v240 offset:33792
rt v[194:195],v240 offset:34816
mm v[52:55],v[204:207],v[144:147],0
mm v[52:55],v[200:203],v[148:151],v[52:55]
rt v[196:197],v240 offset:42240
rt v[198:199],v240 offset:43264
mm v[20:23],v[200:203],v[132:135],0
wt lgkmcnt(4)
mm v[20:23],v[204:207],v[128:131],v[20:23]
rt v[200:201],v241 offset:33792
rt v[202:203],v241 offset:34816
mm v[24:27],v[212:215],v[128:131],0
mm v[24:27],v[208:211],v[132:135],v[24:27]
rt v[204:205],v241 offset:42240
rt v[206:207],v241 offset:43264
mm v[56:59],v[208:211],v[148:151],0
mm v[56:59],v[212:215],v[144:147],v[56:59]
rt v[208:209],v242 offset:33792
rt v[210:211],v242 offset:34816
mm v[60:63],v[220:223],v[144:147],0
mm v[60:63],v[216:219],v[148:151],v[60:63]
rt v[212:213],v242 offset:42240
rt v[214:215],v242 offset:43264
mm v[28:31],v[216:219],v[132:135],0
wt lgkmcnt(4)
mm v[28:31],v[220:223],v[128:131],v[28:31]
rt v[216:217],v243 offset:33792
rt v[218:219],v243 offset:34816
mm v[0:3],v[192:195],v[136:139],v[0:3]
ar v249,a231
s_getpc_b64 s[24:25]
.La5_LW3_P1:
s_add_u32 s24, s24, .La5_LW3_R1-.La5_LW3_P1
s_addc_u32 s25, s25, 0
s_mov_b64 exec, 0
s_cmp_eq_u32 s3, 0
s_cbranch_scc1 .La5_LW3_0
s_cmp_eq_u32 s3, 1
s_cbranch_scc1 .La5_LW3_1
s_cmp_eq_u32 s3, 2
s_cbranch_scc1 .La5_LW3_2
s_branch .La5_LW3_3
.La5_LW3_R1:
s_mov_b64 exec, -1
.La5_LO_VE1:
wt vmcnt(0)
buffer_store_dwordx2 v[252:253], v249, s[64:67], s6 offen offset:0 sc1
mm v[0:3],v[196:199],v[140:143],v[0:3]
rt v[220:221],v243 offset:42240
rt v[222:223],v243 offset:43264
mm v[32:35],v[196:199],v[156:159],v[32:35]
mm v[32:35],v[192:195],v[152:155],v[32:35]
rt v[196:197],v240 offset:33920
rt v[198:199],v240 offset:34944
mm v[36:39],v[200:203],v[152:155],v[36:39]
mm v[36:39],v[204:207],v[156:159],v[36:39]
rt v[192:193],v240 offset:42368
rt v[194:195],v240 offset:43392
mm v[4:7],v[204:207],v[140:143],v[4:7]
wt lgkmcnt(4)
mm v[4:7],v[200:203],v[136:139],v[4:7]
rt v[204:205],v241 offset:33920
rt v[206:207],v241 offset:34944
mm v[8:11],v[208:211],v[136:139],v[8:11]
mm v[8:11],v[212:215],v[140:143],v[8:11]
rt v[200:201],v241 offset:42368
rt v[202:203],v241 offset:43392
mm v[40:43],v[212:215],v[156:159],v[40:43]
mm v[40:43],v[208:211],v[152:155],v[40:43]
rt v[212:213],v242 offset:33920
rt v[214:215],v242 offset:34944
mm v[44:47],v[216:219],v[152:155],v[44:47]
mm v[44:47],v[220:223],v[156:159],v[44:47]
rt v[208:209],v242 offset:42368
rt v[210:211],v242 offset:43392
mm v[12:15],v[220:223],v[140:143],v[12:15]
wt lgkmcnt(4)
mm v[12:15],v[216:219],v[136:139],v[12:15]
rt v[220:221],v243 offset:33920
rt v[222:223],v243 offset:34944
mm v[16:19],v[196:199],v[136:139],v[16:19]
mm v[16:19],v[192:195],v[140:143],v[16:19]
rt v[216:217],v243 offset:42368
rt v[218:219],v243 offset:43392
mm v[48:51],v[192:195],v[156:159],v[48:51]
mm v[48:51],v[196:199],v[152:155],v[48:51]
rt v[192:193],v240 offset:16896
rt v[194:195],v240 offset:17920
mm v[52:55],v[204:207],v[152:155],v[52:55]
mm v[52:55],v[200:203],v[156:159],v[52:55]
rt v[196:197],v240 offset:25344
rt v[198:199],v240 offset:26368
mm v[20:23],v[200:203],v[140:143],v[20:23]
wt lgkmcnt(4)
mm v[20:23],v[204:207],v[136:139],v[20:23]
rt v[200:201],v241 offset:16896
rt v[202:203],v241 offset:17920
mm v[24:27],v[212:215],v[136:139],v[24:27]
mm v[24:27],v[208:211],v[140:143],v[24:27]
rt v[204:205],v241 offset:25344
rt v[206:207],v241 offset:26368
mm v[56:59],v[208:211],v[156:159],v[56:59]
mm v[56:59],v[212:215],v[152:155],v[56:59]
rt v[208:209],v242 offset:16896
rt v[210:211],v242 offset:17920
mm v[60:63],v[220:223],v[152:155],v[60:63]
mm v[60:63],v[216:219],v[156:159],v[60:63]
rt v[212:213],v242 offset:25344
rt v[214:215],v242 offset:26368
mm v[28:31],v[216:219],v[140:143],v[28:31]
wt lgkmcnt(4)
mm v[28:31],v[220:223],v[136:139],v[28:31]
rt v[216:217],v243 offset:16896
rt v[218:219],v243 offset:17920
s_cmp_lg_u32 s30, 0
s_cbranch_scc1 .La5_LO_W
; ---- O projection: 256 x 128 tile over eight heads
.La5_LO_START:
.La5_LW6_7:
s_cbranch_execz .La5_LW6_R0
.La5_LO_SA:
mm v[64:67],v[192:195],v[160:163],0
buffer_load_dwordx2 v[250:251], v248, s[64:67], s5 offen sc1
mm v[64:67],v[196:199],v[164:167],v[64:67]
rt v[220:221],v243 offset:25344
rt v[222:223],v243 offset:26368
mm v[96:99],v[196:199],v[180:183],0
s_add_u32 m0, s83, 0
s_bfe_u32 s80, s4, 262148
mm v[96:99],v[192:195],v[176:179],v[96:99]
rt v[196:197],v240 offset:17024
rt v[198:199],v240 offset:18048
s_xor_b32 s80, s80, s72
s_lshl_b32 s80, s80, 18
s_add_u32 s80, s80, 131072
mm v[100:103],v[200:203],v[176:179],0
ld v224,s[56:59],s80 offen lds
mm v[100:103],v[204:207],v[180:183],v[100:103]
rt v[192:193],v240 offset:25472
rt v[194:195],v240 offset:26496
mm v[68:71],v[204:207],v[164:167],0
s_add_u32 m0, s83, 1024
wt lgkmcnt(4)
mm v[68:71],v[200:203],v[160:163],v[68:71]
rt v[204:205],v241 offset:17024
rt v[206:207],v241 offset:18048
mm v[72:75],v[208:211],v[160:163],0
ld v225,s[56:59],s80 offen lds
mm v[72:75],v[212:215],v[164:167],v[72:75]
rt v[200:201],v241 offset:25472
rt v[202:203],v241 offset:26496
mm v[104:107],v[212:215],v[180:183],0
s_add_u32 m0, s83, 2176
s_bfe_u32 s53, s4, 262148
mm v[104:107],v[208:211],v[176:179],v[104:107]
rt v[212:213],v242 offset:17024
rt v[214:215],v242 offset:18048
mm v[108:111],v[216:219],v[176:179],0
ld v226,s[56:59],s80 offen lds
mm v[108:111],v[220:223],v[180:183],v[108:111]
rt v[208:209],v242 offset:25472
rt v[210:211],v242 offset:26496
mm v[76:79],v[220:223],v[164:167],0
s_add_u32 m0, s83, 3200
wt lgkmcnt(4)
mm v[76:79],v[216:219],v[160:163],v[76:79]
rt v[220:221],v243 offset:17024
rt v[222:223],v243 offset:18048
mm v[80:83],v[196:199],v[160:163],0
ld v227,s[56:59],s80 offen lds
mm v[80:83],v[192:195],v[164:167],v[80:83]
rt v[216:217],v243 offset:25472
rt v[218:219],v243 offset:26496
mm v[112:115],v[192:195],v[180:183],0
s_add_u32 m0, s83, 118272
s_bfe_u32 s80, s4, 262152
mm v[112:115],v[196:199],v[176:179],v[112:115]
rt v[192:193],v240 offset:33792
rt v[194:195],v240 offset:34816
s_xor_b32 s80, s80, s72
s_lshl_b32 s80, s80, 18
mm v[116:119],v[204:207],v[176:179],0
ld v224,s[56:59],s80 offen lds
mm v[116:119],v[200:203],v[180:183],v[116:119]
rt v[196:197],v240 offset:42240
rt v[198:199],v240 offset:43264
mm v[84:87],v[200:203],v[164:167],0
wt vmcnt(5)
v_cmp_eq_u64 vcc, v[250:251], v[252:253]
s_nop 0
s_bitcmp1_b32 vcc_lo, 1
s_cbranch_scc0 .La5_LO_NA1
.La5_LO_KA1:
wt lgkmcnt(4)
mm v[84:87],v[204:207],v[160:163],v[84:87]
rt v[200:201],v241 offset:33792
rt v[202:203],v241 offset:34816
s_xor_b32 s53, s53, s72
s_lshl_b32 s53, s53, 13
mm v[88:91],v[212:215],v[160:163],0
ld a[0:3],v244,s[44:47],s53 offen offset:0 sc1
mm v[88:91],v[208:211],v[164:167],v[88:91]
rt v[204:205],v241 offset:42240
rt v[206:207],v241 offset:43264
mm v[120:123],v[208:211],v[180:183],0
ld a[4:7],v245,s[44:47],s53 offen offset:0 sc1
mm v[120:123],v[212:215],v[176:179],v[120:123]
rt v[208:209],v242 offset:33792
rt v[210:211],v242 offset:34816
mm v[124:127],v[220:223],v[176:179],0
ld a[8:11],v246,s[44:47],s53 offen offset:0 sc1
mm v[124:127],v[216:219],v[180:183],v[124:127]
rt v[212:213],v242 offset:42240
rt v[214:215],v242 offset:43264
mm v[92:95],v[216:219],v[164:167],0
wt lgkmcnt(4)
s_bfe_u32 s54, s4, 262152
mm v[92:95],v[220:223],v[160:163],v[92:95]
rt v[216:217],v243 offset:33792
rt v[218:219],v243 offset:34816
mm v[64:67],v[192:195],v[168:171],v[64:67]
ld a[12:15],v247,s[44:47],s53 offen offset:0 sc1
mm v[64:67],v[196:199],v[172:175],v[64:67]
rt v[220:221],v243 offset:42240
rt v[222:223],v243 offset:43264
mm v[96:99],v[196:199],v[188:191],v[96:99]
ld a[16:19],v244,s[44:47],s53 offen offset:1024 sc1
mm v[96:99],v[192:195],v[184:187],v[96:99]
rt v[196:197],v240 offset:33920
rt v[198:199],v240 offset:34944
mm v[100:103],v[200:203],v[184:187],v[100:103]
ld a[20:23],v245,s[44:47],s53 offen offset:1024 sc1
mm v[100:103],v[204:207],v[188:191],v[100:103]
rt v[192:193],v240 offset:42368
rt v[194:195],v240 offset:43392
mm v[68:71],v[204:207],v[172:175],v[68:71]
wt lgkmcnt(4)
s_xor_b32 s54, s54, s72
mm v[68:71],v[200:203],v[168:171],v[68:71]
rt v[204:205],v241 offset:33920
rt v[206:207],v241 offset:34944
mm v[72:75],v[208:211],v[168:171],v[72:75]
ld a[24:27],v246,s[44:47],s53 offen offset:1024 sc1
mm v[72:75],v[212:215],v[172:175],v[72:75]
rt v[200:201],v241 offset:42368
rt v[202:203],v241 offset:43392
mm v[104:107],v[212:215],v[188:191],v[104:107]
ld a[28:31],v247,s[44:47],s53 offen offset:1024 sc1
s_add_u32 m0, s83, 119296
mm v[104:107],v[208:211],v[184:187],v[104:107]
rt v[212:213],v242 offset:33920
rt v[214:215],v242 offset:34944
mm v[108:111],v[216:219],v[184:187],v[108:111]
ld v225,s[56:59],s80 offen lds
mm v[108:111],v[220:223],v[188:191],v[108:111]
rt v[208:209],v242 offset:42368
rt v[210:211],v242 offset:43392
mm v[76:79],v[220:223],v[172:175],v[76:79]
s_add_u32 m0, s83, 120448
s_nop 0
s_barrier
wt lgkmcnt(4)
mm v[76:79],v[216:219],v[168:171],v[76:79]
rt v[220:221],v243 offset:33920
rt v[222:223],v243 offset:34944
mm v[80:83],v[196:199],v[168:171],v[80:83]
ld v226,s[56:59],s80 offen lds
mm v[80:83],v[192:195],v[172:175],v[80:83]
rt v[216:217],v243 offset:42368
rt v[218:219],v243 offset:43392
mm v[112:115],v[192:195],v[188:191],v[112:115]
ld a[32:35],v244,s[44:47],s53 offen offset:2048 sc1
mm v[112:115],v[196:199],v[184:187],v[112:115]
rt v[192:193],v240 offset:50688
rt v[194:195],v240 offset:51712
mm v[116:119],v[204:207],v[184:187],v[116:119]
ld a[36:39],v245,s[44:47],s53 offen offset:2048 sc1
mm v[116:119],v[200:203],v[188:191],v[116:119]
rt v[196:197],v240 offset:59136
rt v[198:199],v240 offset:60160
mm v[84:87],v[200:203],v[172:175],v[84:87]
wt lgkmcnt(4)
s_lshl_b32 s54, s54, 13
mm v[84:87],v[204:207],v[168:171],v[84:87]
rt v[200:201],v241 offset:50688
rt v[202:203],v241 offset:51712
mm v[88:91],v[212:215],v[168:171],v[88:91]
ld a[40:43],v246,s[44:47],s53 offen offset:2048 sc1
mm v[88:91],v[208:211],v[172:175],v[88:91]
rt v[204:205],v241 offset:59136
rt v[206:207],v241 offset:60160
mm v[120:123],v[208:211],v[188:191],v[120:123]
ld a[44:47],v247,s[44:47],s53 offen offset:2048 sc1
mm v[120:123],v[212:215],v[184:187],v[120:123]
rt v[208:209],v242 offset:50688
rt v[210:211],v242 offset:51712
mm v[124:127],v[220:223],v[184:187],v[124:127]
ld a[48:51],v244,s[44:47],s53 offen offset:3072 sc1
mm v[124:127],v[216:219],v[188:191],v[124:127]
rt v[212:213],v242 offset:59136
rt v[214:215],v242 offset:60160
mm v[92:95],v[216:219],v[172:175],v[92:95]
wt vmcnt(7) lgkmcnt(4)
s_bfe_u32 s55, s4, 262156
mm v[92:95],v[220:223],v[168:171],v[92:95]
rt v[216:217],v243 offset:50688
rt v[218:219],v243 offset:51712
mm v[0:3],v[192:195],a[0:3],v[0:3]
s_xor_b32 s55, s55, s72
s_lshl_b32 s55, s55, 13
mm v[0:3],v[196:199],a[16:19],v[0:3]
ld a[52:55],v245,s[44:47],s53 offen offset:3072 sc1
mm v[32:35],v[196:199],a[20:23],v[32:35]
s_bfe_u32 s52, s4, 262160
.La5_LW3_0:
s_cbranch_execz .La5_LW3_4
s_xor_b32 s52, s52, s72
mm v[32:35],v[192:195],a[4:7],v[32:35]
rt v[220:221],v243 offset:59136
rt v[222:223],v243 offset:60160
mm v[64:67],v[192:195],a[8:11],v[64:67]
s_lshl_b32 s52, s52, 13
mm v[64:67],v[196:199],a[24:27],v[64:67]
ld a[56:59],v246,s[44:47],s53 offen offset:3072 sc1
mm v[96:99],v[196:199],a[28:31],v[96:99]
mm v[96:99],v[192:195],a[12:15],v[96:99]
rt v[196:197],v240 offset:50816
rt v[198:199],v240 offset:51840
mm v[100:103],v[200:203],a[12:15],v[100:103]
mm v[100:103],v[204:207],a[28:31],v[100:103]
buffer_load_dwordx2 v[250:251], v248, s[64:67], s5 offen sc1
mm v[68:71],v[204:207],a[24:27],v[68:71]
mm v[68:71],v[200:203],a[8:11],v[68:71]
rt v[192:193],v240 offset:59264
rt v[194:195],v240 offset:60288
mm v[36:39],v[200:203],a[4:7],v[36:39]
mm v[36:39],v[204:207],a[20:23],v[36:39]
ld a[60:63],v247,s[44:47],s53 offen offset:3072 sc1
mm v[4:7],v[204:207],a[16:19],v[4:7]
wt lgkmcnt(4)
s_bfe_u32 s53, s4, 262164
mm v[4:7],v[200:203],a[0:3],v[4:7]
rt v[204:205],v241 offset:50816
rt v[206:207],v241 offset:51840
mm v[8:11],v[208:211],a[0:3],v[8:11]
s_add_u32 m0, s83, 121472
s_xor_b32 s53, s53, s72
mm v[8:11],v[212:215],a[16:19],v[8:11]
ld v227,s[56:59],s80 offen lds
mm v[40:43],v[212:215],a[20:23],v[40:43]
s_add_u32 s80, s80, 131072
s_lshl_b32 s53, s53, 13
mm v[40:43],v[208:211],a[4:7],v[40:43]
rt v[200:201],v241 offset:59264
rt v[202:203],v241 offset:60288
mm v[72:75],v[208:211],a[8:11],v[72:75]
s_add_u32 m0, s83, 67584
mm v[72:75],v[212:215],a[24:27],v[72:75]
ld v224,s[56:59],s80 offen lds
mm v[104:107],v[212:215],a[28:31],v[104:107]
mm v[104:107],v[208:211],a[12:15],v[104:107]
rt v[212:213],v242 offset:50816
rt v[214:215],v242 offset:51840
mm v[108:111],v[216:219],a[12:15],v[108:111]
s_add_u32 m0, s83, 68608
mm v[108:111],v[220:223],a[28:31],v[108:111]
ld v225,s[56:59],s80 offen lds
mm v[76:79],v[220:223],a[24:27],v[76:79]
mm v[76:79],v[216:219],a[8:11],v[76:79]
rt v[208:209],v242 offset:59264
rt v[210:211],v242 offset:60288
mm v[44:47],v[216:219],a[4:7],v[44:47]
s_add_u32 m0, s83, 69760
mm v[44:47],v[220:223],a[20:23],v[44:47]
ld v226,s[56:59],s80 offen lds
mm v[12:15],v[220:223],a[16:19],v[12:15]
wt lgkmcnt(4)
mm v[12:15],v[216:219],a[0:3],v[12:15]
rt v[220:221],v243 offset:50816
rt v[222:223],v243 offset:51840
mm v[16:19],v[196:199],a[0:3],v[16:19]
s_add_u32 m0, s83, 70784
mm v[16:19],v[192:195],a[16:19],v[16:19]
ld v227,s[56:59],s80 offen lds
mm v[48:51],v[192:195],a[20:23],v[48:51]
s_bfe_u32 s80, s4, 262156
s_xor_b32 s80, s80, s72
mm v[48:51],v[196:199],a[4:7],v[48:51]
rt v[216:217],v243 offset:59264
rt v[218:219],v243 offset:60288
mm v[80:83],v[196:199],a[8:11],v[80:83]
s_add_u32 m0, s83, 84480
s_nop 0
s_barrier
s_lshl_b32 s80, s80, 18
mm v[80:83],v[192:195],a[24:27],v[80:83]
ld v224,s[56:59],s80 offen lds
mm v[112:115],v[192:195],a[28:31],v[112:115]
mm v[112:115],v[196:199],a[12:15],v[112:115]
rt v[192:193],v240 offset:0
rt v[194:195],v240 offset:1024
mm v[116:119],v[204:207],a[12:15],v[116:119]
s_add_u32 m0, s83, 85504
mm v[116:119],v[200:203],a[28:31],v[116:119]
ld v225,s[56:59],s80 offen lds
mm v[84:87],v[200:203],a[24:27],v[84:87]
mm v[84:87],v[204:207],a[8:11],v[84:87]
rt v[196:197],v240 offset:8448
rt v[198:199],v240 offset:9472
mm v[52:55],v[204:207],a[4:7],v[52:55]
s_add_u32 m0, s83, 86656
mm v[52:55],v[200:203],a[20:23],v[52:55]
ld v226,s[56:59],s80 offen lds
mm v[20:23],v[200:203],a[16:19],v[20:23]
wt lgkmcnt(4)
mm v[20:23],v[204:207],a[0:3],v[20:23]
rt v[200:201],v241 offset:0
rt v[202:203],v241 offset:1024
mm v[24:27],v[212:215],a[0:3],v[24:27]
s_add_u32 m0, s83, 87680
mm v[24:27],v[208:211],a[16:19],v[24:27]
ld v227,s[56:59],s80 offen lds
mm v[56:59],v[208:211],a[20:23],v[56:59]
s_add_u32 s80, s80, 131072
mm v[56:59],v[212:215],a[4:7],v[56:59]
rt v[204:205],v241 offset:8448
rt v[206:207],v241 offset:9472
mm v[88:91],v[212:215],a[8:11],v[88:91]
s_add_u32 m0, s83, 101376
mm v[88:91],v[208:211],a[24:27],v[88:91]
ld v224,s[56:59],s80 offen lds
wt vmcnt(11)
v_cmp_eq_u64 vcc, v[250:251], v[252:253]
mm v[120:123],v[208:211],a[28:31],v[120:123]
s_bitcmp1_b32 vcc_lo, 2
s_cbranch_scc0 .La5_LO_NA2
.La5_LO_KA2:
mm v[120:123],v[212:215],a[12:15],v[120:123]
rt v[208:209],v242 offset:0
rt v[210:211],v242 offset:1024
mm v[124:127],v[220:223],a[12:15],v[124:127]
mm v[124:127],v[216:219],a[28:31],v[124:127]
ld a[12:15],v244,s[44:47],s54 offen offset:0 sc1
mm v[92:95],v[216:219],a[24:27],v[92:95]
mm v[92:95],v[220:223],a[8:11],v[92:95]
rt v[212:213],v242 offset:8448
rt v[214:215],v242 offset:9472
mm v[60:63],v[220:223],a[4:7],v[60:63]
mm v[60:63],v[216:219],a[20:23],v[60:63]
mm v[28:31],v[216:219],a[16:19],v[28:31]
wt vmcnt(11) lgkmcnt(4)
mm v[28:31],v[220:223],a[0:3],v[28:31]
rt v[216:217],v243 offset:0
rt v[218:219],v243 offset:1024
mm v[0:3],v[192:195],a[32:35],v[0:3]
mm v[0:3],v[196:199],a[48:51],v[0:3]
ld a[0:3],v245,s[44:47],s54 offen offset:0 sc1
mm v[32:35],v[196:199],a[52:55],v[32:35]
mm v[32:35],v[192:195],a[36:39],v[32:35]
rt v[220:221],v243 offset:8448
rt v[222:223],v243 offset:9472
mm v[64:67],v[192:195],a[40:43],v[64:67]
mm v[64:67],v[196:199],a[56:59],v[64:67]
ld a[4:7],v246,s[44:47],s54 offen offset:0 sc1
mm v[96:99],v[196:199],a[60:63],v[96:99]
mm v[96:99],v[192:195],a[44:47],v[96:99]
rt v[196:197],v240 offset:128
rt v[198:199],v240 offset:1152
mm v[100:103],v[200:203],a[44:47],v[100:103]
s_mov_b32 s86, 0x05040100
s_mov_b32 s87, 0x07060302
mm v[100:103],v[204:207],a[60:63],v[100:103]
ld a[8:11],v247,s[44:47],s54 offen offset:0 sc1
mm v[68:71],v[204:207],a[56:59],v[68:71]
s_nop 0
ar v230,a128
mm v[68:71],v[200:203],a[40:43],v[68:71]
rt v[192:193],v240 offset:8576
rt v[194:195],v240 offset:9600
mm v[36:39],v[200:203],a[36:39],v[36:39]
v_add_u32 v240, 67584, v240
ar v231,a132
mm v[36:39],v[204:207],a[52:55],v[36:39]
ld a[16:19],v244,s[44:47],s54 offen offset:1024 sc1
mm v[4:7],v[204:207],a[48:51],v[4:7]
wt lgkmcnt(4)
v_perm_b32 v232, v231, v230, s86
mm v[4:7],v[200:203],a[32:35],v[4:7]
rt v[204:205],v241 offset:128
rt v[206:207],v241 offset:1152
mm v[8:11],v[208:211],a[32:35],v[8:11]
v_perm_b32 v233, v231, v230, s87
aw a96,v232
mm v[8:11],v[212:215],a[48:51],v[8:11]
ld a[20:23],v245,s[44:47],s54 offen offset:1024 sc1
mm v[40:43],v[212:215],a[52:55],v[40:43]
aw a100,v233
ar v234,a136
mm v[40:43],v[208:211],a[36:39],v[40:43]
rt v[200:201],v241 offset:8576
rt v[202:203],v241 offset:9600
mm v[72:75],v[208:211],a[40:43],v[72:75]
v_add_u32 v241, 67584, v241
ar v235,a140
mm v[72:75],v[212:215],a[56:59],v[72:75]
ld a[24:27],v246,s[44:47],s54 offen offset:1024 sc1
mm v[104:107],v[212:215],a[60:63],v[104:107]
v_perm_b32 v236, v235, v234, s86
v_perm_b32 v237, v235, v234, s87
mm v[104:107],v[208:211],a[44:47],v[104:107]
rt v[212:213],v242 offset:128
rt v[214:215],v242 offset:1152
mm v[108:111],v[216:219],a[44:47],v[108:111]
aw a97,v236
aw a101,v237
mm v[108:111],v[220:223],a[60:63],v[108:111]
ld a[28:31],v247,s[44:47],s54 offen offset:1024 sc1
mm v[76:79],v[220:223],a[56:59],v[76:79]
ar v230,a144
ar v231,a148
mm v[76:79],v[216:219],a[40:43],v[76:79]
rt v[208:209],v242 offset:8576
rt v[210:211],v242 offset:9600
mm v[44:47],v[216:219],a[36:39],v[44:47]
v_add_u32 v242, 67584, v242
v_perm_b32 v232, v231, v230, s86
mm v[44:47],v[220:223],a[52:55],v[44:47]
v_perm_b32 v233, v231, v230, s87
aw a98,v232
mm v[12:15],v[220:223],a[48:51],v[12:15]
wt lgkmcnt(4)
aw a102,v233
mm v[12:15],v[216:219],a[32:35],v[12:15]
rt v[220:221],v243 offset:128
rt v[222:223],v243 offset:1152
mm v[16:19],v[196:199],a[32:35],v[16:19]
s_add_u32 m0, s83, 102400
ar v234,a152
mm v[16:19],v[192:195],a[48:51],v[16:19]
ld v225,s[56:59],s80 offen lds
mm v[48:51],v[192:195],a[52:55],v[48:51]
ar v235,a156
v_perm_b32 v236, v235, v234, s86
mm v[48:51],v[196:199],a[36:39],v[48:51]
rt v[216:217],v243 offset:8576
rt v[218:219],v243 offset:9600
mm v[80:83],v[196:199],a[40:43],v[80:83]
s_add_u32 m0, s83, 103552
v_add_u32 v243, 67584, v243
mm v[80:83],v[192:195],a[56:59],v[80:83]
ld v226,s[56:59],s80 offen lds
mm v[112:115],v[192:195],a[60:63],v[112:115]
wt vmcnt(15)
s_barrier
mm v[112:115],v[196:199],a[44:47],v[112:115]
rt v[192:193],v240 offset:50688
rt v[194:195],v240 offset:51712
mm v[116:119],v[204:207],a[44:47],v[116:119]
v_perm_b32 v237, v235, v234, s87
aw a99,v236
mm v[116:119],v[200:203],a[60:63],v[116:119]
ld a[64:67],v244,s[44:47],s54 offen offset:2048 sc1
mm v[84:87],v[200:203],a[56:59],v[84:87]
aw a103,v237
ar v230,a129
mm v[84:87],v[204:207],a[40:43],v[84:87]
rt v[196:197],v240 offset:59136
rt v[198:199],v240 offset:60160
mm v[52:55],v[204:207],a[36:39],v[52:55]
ar v231,a133
v_perm_b32 v232, v231, v230, s86
mm v[52:55],v[200:203],a[52:55],v[52:55]
ld a[68:71],v245,s[44:47],s54 offen offset:2048 sc1
mm v[20:23],v[200:203],a[48:51],v[20:23]
wt lgkmcnt(4)
v_perm_b32 v233, v231, v230, s87
mm v[20:23],v[204:207],a[32:35],v[20:23]
rt v[200:201],v241 offset:50688
rt v[202:203],v241 offset:51712
mm v[24:27],v[212:215],a[32:35],v[24:27]
aw a104,v232
aw a108,v233
mm v[24:27],v[208:211],a[48:51],v[24:27]
ld a[72:75],v246,s[44:47],s54 offen offset:2048 sc1
mm v[56:59],v[208:211],a[52:55],v[56:59]
ar v234,a137
ar v235,a141
mm v[56:59],v[212:215],a[36:39],v[56:59]
rt v[204:205],v241 offset:59136
rt v[206:207],v241 offset:60160
mm v[88:91],v[212:215],a[40:43],v[88:91]
v_perm_b32 v236, v235, v234, s86
v_perm_b32 v237, v235, v234, s87
mm v[88:91],v[208:211],a[56:59],v[88:91]
ld a[76:79],v247,s[44:47],s54 offen offset:2048 sc1
mm v[120:123],v[208:211],a[60:63],v[120:123]
aw a105,v236
aw a109,v237
mm v[120:123],v[212:215],a[44:47],v[120:123]
rt v[208:209],v242 offset:50688
rt v[210:211],v242 offset:51712
mm v[124:127],v[220:223],a[44:47],v[124:127]
ar v230,a145
ar v231,a149
mm v[124:127],v[216:219],a[60:63],v[124:127]
buffer_load_dwordx2 v[250:251], v248, s[64:67], s5 offen sc1
mm v[92:95],v[216:219],a[56:59],v[92:95]
v_perm_b32 v232, v231, v230, s86
v_perm_b32 v233, v231, v230, s87
mm v[92:95],v[220:223],a[40:43],v[92:95]
rt v[212:213],v242 offset:59136
rt v[214:215],v242 offset:60160
mm v[60:63],v[220:223],a[36:39],v[60:63]
aw a106,v232
aw a110,v233
mm v[60:63],v[216:219],a[52:55],v[60:63]
ld a[36:39],v244,s[44:47],s54 offen offset:3072 sc1
mm v[28:31],v[216:219],a[48:51],v[28:31]
wt vmcnt(8) lgkmcnt(4)
ar v234,a153
mm v[28:31],v[220:223],a[32:35],v[28:31]
rt v[216:217],v243 offset:50688
rt v[218:219],v243 offset:51712
mm v[0:3],v[192:195],a[12:15],v[0:3]
ar v235,a157
v_perm_b32 v236, v235, v234, s86
mm v[0:3],v[196:199],a[16:19],v[0:3]
ld a[32:35],v245,s[44:47],s54 offen offset:3072 sc1
mm v[32:35],v[196:199],a[20:23],v[32:35]
v_perm_b32 v237, v235, v234, s87
aw a107,v236
mm v[32:35],v[192:195],a[0:3],v[32:35]
rt v[220:221],v243 offset:59136
rt v[222:223],v243 offset:60160
mm v[64:67],v[192:195],a[4:7],v[64:67]
aw a111,v237
ar v230,a130
mm v[64:67],v[196:199],a[24:27],v[64:67]
ld a[40:43],v246,s[44:47],s54 offen offset:3072 sc1
mm v[96:99],v[196:199],a[28:31],v[96:99]
ar v231,a134
v_perm_b32 v232, v231, v230, s86
mm v[96:99],v[192:195],a[8:11],v[96:99]
rt v[196:197],v240 offset:50816
rt v[198:199],v240 offset:51840
mm v[100:103],v[200:203],a[8:11],v[100:103]
v_perm_b32 v233, v231, v230, s87
aw a112,v232
mm v[100:103],v[204:207],a[28:31],v[100:103]
ld a[44:47],v247,s[44:47],s54 offen offset:3072 sc1
mm v[68:71],v[204:207],a[24:27],v[68:71]
s_bfe_u32 s54, s4, 262168
s_xor_b32 s54, s54, s72
mm v[68:71],v[200:203],a[4:7],v[68:71]
rt v[192:193],v240 offset:59264
rt v[194:195],v240 offset:60288
mm v[36:39],v[200:203],a[0:3],v[36:39]
s_add_u32 m0, s83, 104576
s_lshl_b32 s54, s54, 13
mm v[36:39],v[204:207],a[20:23],v[36:39]
ld v227,s[56:59],s80 offen lds
mm v[4:7],v[204:207],a[16:19],v[4:7]
wt lgkmcnt(4)
s_bfe_u32 s80, s4, 262160
mm v[4:7],v[200:203],a[12:15],v[4:7]
rt v[204:205],v241 offset:50816
rt v[206:207],v241 offset:51840
mm v[8:11],v[208:211],a[12:15],v[8:11]
s_bitcmp1_b32 vcc_lo, 3
s_nop 0
s_nop 0
s_cbranch_scc0 .La5_LO_NA3
.La5_LO_KA3:
mm v[8:11],v[212:215],a[16:19],v[8:11]
ld a[48:51],v244,s[44:47],s55 offen offset:0 sc1
mm v[40:43],v[212:215],a[20:23],v[40:43]
s_xor_b32 s80, s80, s72
s_lshl_b32 s80, s80, 18
mm v[40:43],v[208:211],a[0:3],v[40:43]
rt v[200:201],v241 offset:59264
rt v[202:203],v241 offset:60288
mm v[72:75],v[208:211],a[4:7],v[72:75]
aw a116,v233
ar v234,a138
mm v[72:75],v[212:215],a[24:27],v[72:75]
ld a[52:55],v245,s[44:47],s55 offen offset:0 sc1
mm v[104:107],v[212:215],a[28:31],v[104:107]
ar v235,a142
v_perm_b32 v236, v235, v234, s86
mm v[104:107],v[208:211],a[8:11],v[104:107]
rt v[212:213],v242 offset:50816
rt v[214:215],v242 offset:51840
mm v[108:111],v[216:219],a[8:11],v[108:111]
v_perm_b32 v237, v235, v234, s87
aw a113,v236
mm v[108:111],v[220:223],a[28:31],v[108:111]
ld a[56:59],v246,s[44:47],s55 offen offset:0 sc1
mm v[76:79],v[220:223],a[24:27],v[76:79]
aw a117,v237
ar v230,a146
mm v[76:79],v[216:219],a[4:7],v[76:79]
rt v[208:209],v242 offset:59264
rt v[210:211],v242 offset:60288
mm v[44:47],v[216:219],a[0:3],v[44:47]
ar v231,a150
v_perm_b32 v232, v231, v230, s86
mm v[44:47],v[220:223],a[20:23],v[44:47]
v_perm_b32 v233, v231, v230, s87
aw a114,v232
mm v[12:15],v[220:223],a[16:19],v[12:15]
wt lgkmcnt(4)
aw a118,v233
mm v[12:15],v[216:219],a[12:15],v[12:15]
rt v[220:221],v243 offset:50816
rt v[222:223],v243 offset:51840
mm v[16:19],v[196:199],a[12:15],v[16:19]
ar v234,a154
ar v235,a158
mm v[16:19],v[192:195],a[16:19],v[16:19]
ld a[60:63],v247,s[44:47],s55 offen offset:0 sc1
mm v[48:51],v[192:195],a[20:23],v[48:51]
v_perm_b32 v236, v235, v234, s86
v_perm_b32 v237, v235, v234, s87
mm v[48:51],v[196:199],a[0:3],v[48:51]
rt v[216:217],v243 offset:59264
rt v[218:219],v243 offset:60288
mm v[80:83],v[196:199],a[4:7],v[80:83]
aw a115,v236
aw a119,v237
mm v[80:83],v[192:195],a[24:27],v[80:83]
ld a[80:83],v244,s[44:47],s55 offen offset:1024 sc1
mm v[112:115],v[192:195],a[28:31],v[112:115]
ar v230,a131
ar v231,a135
mm v[112:115],v[196:199],a[8:11],v[112:115]
rt v[192:193],v240 offset:0
rt v[194:195],v240 offset:1024
mm v[116:119],v[204:207],a[8:11],v[116:119]
v_perm_b32 v232, v231, v230, s86
v_perm_b32 v233, v231, v230, s87
mm v[116:119],v[200:203],a[28:31],v[116:119]
ld a[84:87],v245,s[44:47],s55 offen offset:1024 sc1
mm v[84:87],v[200:203],a[24:27],v[84:87]
aw a120,v232
aw a124,v233
mm v[84:87],v[204:207],a[4:7],v[84:87]
rt v[196:197],v240 offset:8448
rt v[198:199],v240 offset:9472
mm v[52:55],v[204:207],a[0:3],v[52:55]
s_load_dwordx2 s[16:17], s[0:1], 0
s_load_dwordx4 s[36:39], s[0:1], 80
mm v[52:55],v[200:203],a[20:23],v[52:55]
ld a[88:91],v246,s[44:47],s55 offen offset:1024 sc1
mm v[20:23],v[200:203],a[16:19],v[20:23]
wt lgkmcnt(4)
ar v234,a139
mm v[20:23],v[204:207],a[12:15],v[20:23]
rt v[200:201],v241 offset:0
rt v[202:203],v241 offset:1024
mm v[24:27],v[212:215],a[12:15],v[24:27]
ar v235,a143
v_perm_b32 v236, v235, v234, s86
mm v[24:27],v[208:211],a[16:19],v[24:27]
ld a[92:95],v247,s[44:47],s55 offen offset:1024 sc1
mm v[56:59],v[208:211],a[20:23],v[56:59]
v_perm_b32 v237, v235, v234, s87
aw a121,v236
mm v[56:59],v[212:215],a[0:3],v[56:59]
rt v[204:205],v241 offset:8448
rt v[206:207],v241 offset:9472
mm v[88:91],v[212:215],a[4:7],v[88:91]
s_add_u32 m0, s83, 16896
aw a125,v237
mm v[88:91],v[208:211],a[24:27],v[88:91]
ld v224,s[56:59],s80 offen lds
mm v[120:123],v[208:211],a[28:31],v[120:123]
ar v230,a147
ar v231,a151
mm v[120:123],v[212:215],a[8:11],v[120:123]
rt v[208:209],v242 offset:0
rt v[210:211],v242 offset:1024
mm v[124:127],v[220:223],a[8:11],v[124:127]
s_add_u32 m0, s83, 17920
v_perm_b32 v232, v231, v230, s86
mm v[124:127],v[216:219],a[28:31],v[124:127]
ld v225,s[56:59],s80 offen lds
mm v[92:95],v[216:219],a[24:27],v[92:95]
v_perm_b32 v233, v231, v230, s87
aw a122,v232
mm v[92:95],v[220:223],a[4:7],v[92:95]
rt v[212:213],v242 offset:8448
rt v[214:215],v242 offset:9472
mm v[60:63],v[220:223],a[0:3],v[60:63]
aw a126,v233
ar v234,a155
mm v[60:63],v[216:219],a[20:23],v[60:63]
ar v235,a159
v_perm_b32 v236, v235, v234, s86
mm v[28:31],v[216:219],a[16:19],v[28:31]
wt vmcnt(11) lgkmcnt(4)
v_perm_b32 v237, v235, v234, s87
mm v[28:31],v[220:223],a[12:15],v[28:31]
rt v[216:217],v243 offset:0
rt v[218:219],v243 offset:1024
mm v[0:3],v[192:195],a[64:67],v[0:3]
s_add_u32 m0, s83, 19072
aw a123,v236
mm v[0:3],v[196:199],a[36:39],v[0:3]
ld v226,s[56:59],s80 offen lds
mm v[32:35],v[196:199],a[32:35],v[32:35]
aw a127,v237
ar v230,a160
mm v[32:35],v[192:195],a[68:71],v[32:35]
rt v[220:221],v243 offset:8448
rt v[222:223],v243 offset:9472
mm v[64:67],v[192:195],a[72:75],v[64:67]
ar v231,a164
v_perm_b32 v232, v231, v230, s86
mm v[64:67],v[196:199],a[40:43],v[64:67]
ld a[0:3],v244,s[44:47],s55 offen offset:2048 sc1
mm v[96:99],v[196:199],a[44:47],v[96:99]
v_perm_b32 v233, v231, v230, s87
aw a128,v232
mm v[96:99],v[192:195],a[76:79],v[96:99]
rt v[196:197],v240 offset:128
rt v[198:199],v240 offset:1152
mm v[100:103],v[200:203],a[76:79],v[100:103]
aw a132,v233
ar v234,a168
mm v[100:103],v[204:207],a[44:47],v[100:103]
ld a[4:7],v245,s[44:47],s55 offen offset:2048 sc1
mm v[68:71],v[204:207],a[40:43],v[68:71]
ar v235,a172
v_perm_b32 v236, v235, v234, s86
mm v[68:71],v[200:203],a[72:75],v[68:71]
rt v[192:193],v240 offset:8576
rt v[194:195],v240 offset:9600
mm v[36:39],v[200:203],a[68:71],v[36:39]
v_perm_b32 v237, v235, v234, s87
aw a129,v236
mm v[36:39],v[204:207],a[32:35],v[36:39]
ld a[8:11],v246,s[44:47],s55 offen offset:2048 sc1
mm v[4:7],v[204:207],a[36:39],v[4:7]
wt lgkmcnt(4)
aw a133,v237
mm v[4:7],v[200:203],a[64:67],v[4:7]
rt v[204:205],v241 offset:128
rt v[206:207],v241 offset:1152
.La5_LW3_1:
s_cbranch_execz .La5_LW3_5
mm v[8:11],v[208:211],a[64:67],v[8:11]
ar v230,a176
ar v231,a180
mm v[8:11],v[212:215],a[36:39],v[8:11]
ld a[12:15],v247,s[44:47],s55 offen offset:2048 sc1
mm v[40:43],v[212:215],a[32:35],v[40:43]
v_perm_b32 v232, v231, v230, s86
v_perm_b32 v233, v231, v230, s87
mm v[40:43],v[208:211],a[68:71],v[40:43]
rt v[200:201],v241 offset:8576
rt v[202:203],v241 offset:9600
mm v[72:75],v[208:211],a[72:75],v[72:75]
aw a130,v232
aw a134,v233
mm v[72:75],v[212:215],a[40:43],v[72:75]
ld a[16:19],v244,s[44:47],s55 offen offset:3072 sc1
mm v[104:107],v[212:215],a[44:47],v[104:107]
ar v234,a184
ar v235,a188
mm v[104:107],v[208:211],a[76:79],v[104:107]
rt v[212:213],v242 offset:128
rt v[214:215],v242 offset:1152
mm v[108:111],v[216:219],a[76:79],v[108:111]
v_perm_b32 v236, v235, v234, s86
v_perm_b32 v237, v235, v234, s87
mm v[108:111],v[220:223],a[44:47],v[108:111]
ld a[20:23],v245,s[44:47],s55 offen offset:3072 sc1
mm v[76:79],v[220:223],a[40:43],v[76:79]
aw a131,v236
aw a135,v237
mm v[76:79],v[216:219],a[72:75],v[76:79]
rt v[208:209],v242 offset:8576
rt v[210:211],v242 offset:9600
mm v[44:47],v[216:219],a[68:71],v[44:47]
ar v230,a161
ar v231,a165
mm v[44:47],v[220:223],a[32:35],v[44:47]
s_nop 0
v_cmp_eq_u64 vcc, v[250:251], v[252:253]
mm v[12:15],v[220:223],a[36:39],v[12:15]
wt lgkmcnt(4)
v_perm_b32 v232, v231, v230, s86
mm v[12:15],v[216:219],a[64:67],v[12:15]
rt v[220:221],v243 offset:128
rt v[222:223],v243 offset:1152
mm v[16:19],v[196:199],a[64:67],v[16:19]
v_perm_b32 v233, v231, v230, s87
aw a136,v232
mm v[16:19],v[192:195],a[36:39],v[16:19]
ld a[24:27],v246,s[44:47],s55 offen offset:3072 sc1
mm v[48:51],v[192:195],a[32:35],v[48:51]
aw a140,v233
ar v234,a169
mm v[48:51],v[196:199],a[68:71],v[48:51]
rt v[216:217],v243 offset:8576
rt v[218:219],v243 offset:9600
mm v[80:83],v[196:199],a[72:75],v[80:83]
ar v235,a173
v_perm_b32 v236, v235, v234, s86
mm v[80:83],v[192:195],a[40:43],v[80:83]
ld a[28:31],v247,s[44:47],s55 offen offset:3072 sc1
mm v[112:115],v[192:195],a[44:47],v[112:115]
wt vmcnt(19) lgkmcnt(0)
s_barrier
mm v[112:115],v[196:199],a[76:79],v[112:115]
rt v[192:193],v240 offset:16896
rt v[194:195],v240 offset:17920
mm v[116:119],v[204:207],a[76:79],v[116:119]
s_bfe_u32 s55, s4, 262172
s_xor_b32 s55, s55, s72
mm v[116:119],v[200:203],a[44:47],v[116:119]
buffer_load_dwordx2 v[250:251], v248, s[64:67], s5 offen sc1
mm v[84:87],v[200:203],a[40:43],v[84:87]
s_lshl_b32 s55, s55, 13
s_lshl_b32 s92, s76, 5
mm v[84:87],v[204:207],a[72:75],v[84:87]
rt v[196:197],v240 offset:25344
rt v[198:199],v240 offset:26368
mm v[52:55],v[204:207],a[68:71],v[52:55]
s_add_u32 m0, s83, 20096
s_add_u32 s92, s92, s72
mm v[52:55],v[200:203],a[32:35],v[52:55]
ld v227,s[56:59],s80 offen lds
mm v[20:23],v[200:203],a[36:39],v[20:23]
s_nop 0
s_add_u32 s80, s80, 131072
mm v[20:23],v[204:207],a[64:67],v[20:23]
rt v[200:201],v241 offset:16896
rt v[202:203],v241 offset:17920
mm v[24:27],v[212:215],a[64:67],v[24:27]
s_add_u32 m0, s83, 33792
s_lshl2_add_u32 s92, s92, s3
mm v[24:27],v[208:211],a[36:39],v[24:27]
ld v224,s[56:59],s80 offen lds
mm v[56:59],v[208:211],a[32:35],v[56:59]
s_lshl_b32 s92, s92, 12
v_perm_b32 v237, v235, v234, s87
mm v[56:59],v[212:215],a[68:71],v[56:59]
rt v[204:205],v241 offset:25344
rt v[206:207],v241 offset:26368
mm v[88:91],v[212:215],a[72:75],v[88:91]
s_add_u32 m0, s83, 34816
aw a137,v236
mm v[88:91],v[208:211],a[40:43],v[88:91]
ld v225,s[56:59],s80 offen lds
mm v[120:123],v[208:211],a[44:47],v[120:123]
s_add_u32 s68, s16, s92
s_addc_u32 s69, s17, 0
mm v[120:123],v[212:215],a[76:79],v[120:123]
rt v[208:209],v242 offset:16896
rt v[210:211],v242 offset:17920
mm v[124:127],v[220:223],a[76:79],v[124:127]
s_add_u32 m0, s83, 35968
s_and_b32 s69, s69, 0xffff
mm v[124:127],v[216:219],a[44:47],v[124:127]
ld v226,s[56:59],s80 offen lds
mm v[92:95],v[216:219],a[40:43],v[92:95]
s_mov_b32 s70, 4096
s_mov_b32 s71, 0x20000
mm v[92:95],v[220:223],a[72:75],v[92:95]
rt v[212:213],v242 offset:25344
rt v[214:215],v242 offset:26368
mm v[60:63],v[220:223],a[68:71],v[60:63]
s_add_u32 m0, s83, 36992
aw a141,v237
mm v[60:63],v[216:219],a[32:35],v[60:63]
ld v227,s[56:59],s80 offen lds
mm v[28:31],v[216:219],a[36:39],v[28:31]
wt vmcnt(17) lgkmcnt(4)
s_bfe_u32 s80, s4, 262164
mm v[28:31],v[220:223],a[64:67],v[28:31]
rt v[216:217],v243 offset:16896
rt v[218:219],v243 offset:17920
mm v[0:3],v[192:195],a[48:51],v[0:3]
s_bitcmp1_b32 vcc_lo, 4
s_cbranch_scc0 .La5_LO_NA4
.La5_LO_KA4:
mm v[0:3],v[196:199],a[80:83],v[0:3]
ld v[136:139],v244,s[44:47],s52 offen offset:0 sc1
mm v[32:35],v[196:199],a[84:87],v[32:35]
s_xor_b32 s80, s80, s72
s_lshl_b32 s80, s80, 18
mm v[32:35],v[192:195],a[52:55],v[32:35]
rt v[220:221],v243 offset:25344
rt v[222:223],v243 offset:26368
mm v[64:67],v[192:195],a[56:59],v[64:67]
s_add_u32 s48, s36, 0
s_addc_u32 s49, s37, 0
mm v[64:67],v[196:199],a[88:91],v[64:67]
ld v[140:143],v245,s[44:47],s52 offen offset:0 sc1
mm v[96:99],v[196:199],a[92:95],v[96:99]
s_and_b32 s49, s49, 0xffff
s_mov_b32 s50, 2048
mm v[96:99],v[192:195],a[60:63],v[96:99]
rt v[196:197],v240 offset:17024
rt v[198:199],v240 offset:18048
mm v[100:103],v[200:203],a[60:63],v[100:103]
s_mov_b32 s51, 0x20000
v_and_b32 v238, 0xf0, v255
mm v[100:103],v[204:207],a[92:95],v[100:103]
ld v[152:155],v246,s[44:47],s52 offen offset:0 sc1
mm v[68:71],v[204:207],a[88:91],v[68:71]
v_lshlrev_b32_e64 v238, 2, v238
v_bfe_u32 v249, v255, 8, 1
mm v[68:71],v[200:203],a[56:59],v[68:71]
rt v[192:193],v240 offset:25472
rt v[194:195],v240 offset:26496
mm v[36:39],v[200:203],a[52:55],v[36:39]
v_lshl_add_u32 v238, v249, 5, v238
v_lshrrev_b32_e64 v249, 9, v255
mm v[36:39],v[204:207],a[84:87],v[36:39]
ld v[156:159],v247,s[44:47],s52 offen offset:0 sc1
mm v[4:7],v[204:207],a[80:83],v[4:7]
wt lgkmcnt(4)
v_lshl_add_u32 v238, v249, 4, v238
mm v[4:7],v[200:203],a[48:51],v[4:7]
rt v[204:205],v241 offset:17024
rt v[206:207],v241 offset:18048
mm v[8:11],v[208:211],a[48:51],v[8:11]
s_lshr_b32 s23, s72, 3
s_lshl3_add_u32 s92, s23, s76
mm v[8:11],v[212:215],a[80:83],v[8:11]
ld v[168:171],v244,s[44:47],s52 offen offset:1024 sc1
mm v[40:43],v[212:215],a[84:87],v[40:43]
s_lshl_b32 s92, s92, 19
s_lshl_b32 s97, s3, 16
mm v[40:43],v[208:211],a[52:55],v[40:43]
rt v[200:201],v241 offset:25472
rt v[202:203],v241 offset:26496
mm v[72:75],v[208:211],a[56:59],v[72:75]
s_add_u32 s94, s92, s97
s_add_u32 s95, s94, 32768
mm v[72:75],v[212:215],a[88:91],v[72:75]
ld v[172:175],v245,s[44:47],s52 offen offset:1024 sc1
mm v[104:107],v[212:215],a[92:95],v[104:107]
s_sub_u32 s96, s92, s97
s_add_u32 s96, s96, 458752
mm v[104:107],v[208:211],a[60:63],v[104:107]
rt v[212:213],v242 offset:17024
rt v[214:215],v242 offset:18048
mm v[108:111],v[216:219],a[60:63],v[108:111]
s_add_u32 s97, s96, 32768
s_lshl3_add_u32 s92, s23, s101
mm v[108:111],v[220:223],a[92:95],v[108:111]
ld v[184:187],v246,s[44:47],s52 offen offset:1024 sc1
mm v[76:79],v[220:223],a[88:91],v[76:79]
s_lshl_b32 s85, s76, 14
s_lshl3_add_u32 s92, s92, s85
mm v[76:79],v[216:219],a[56:59],v[76:79]
rt v[208:209],v242 offset:25472
rt v[210:211],v242 offset:26496
mm v[44:47],v[216:219],a[52:55],v[44:47]
s_add_u32 s92, s92, 2097152
ar v230,a177
mm v[44:47],v[220:223],a[84:87],v[44:47]
ar v231,a181
v_perm_b32 v232, v231, v230, s86
mm v[12:15],v[220:223],a[80:83],v[12:15]
wt lgkmcnt(4)
v_perm_b32 v233, v231, v230, s87
mm v[12:15],v[216:219],a[48:51],v[12:15]
rt v[220:221],v243 offset:17024
rt v[222:223],v243 offset:18048
mm v[16:19],v[196:199],a[48:51],v[16:19]
aw a138,v232
aw a142,v233
mm v[16:19],v[192:195],a[80:83],v[16:19]
ld v[188:191],v247,s[44:47],s52 offen offset:1024 sc1
mm v[48:51],v[192:195],a[84:87],v[48:51]
ar v234,a185
ar v235,a189
mm v[48:51],v[196:199],a[52:55],v[48:51]
rt v[216:217],v243 offset:25472
rt v[218:219],v243 offset:26496
mm v[80:83],v[196:199],a[56:59],v[80:83]
s_add_u32 m0, s83, 50688
v_perm_b32 v236, v235, v234, s86
mm v[80:83],v[192:195],a[88:91],v[80:83]
ld v224,s[56:59],s80 offen lds
mm v[112:115],v[192:195],a[92:95],v[112:115]
v_perm_b32 v237, v235, v234, s87
aw a139,v236
mm v[112:115],v[196:199],a[60:63],v[112:115]
rt v[192:193],v240 offset:33792
rt v[194:195],v240 offset:34816
mm v[116:119],v[204:207],a[60:63],v[116:119]
s_add_u32 m0, s83, 51712
aw a143,v237
mm v[116:119],v[200:203],a[92:95],v[116:119]
ld v225,s[56:59],s80 offen lds
mm v[84:87],v[200:203],a[88:91],v[84:87]
ar v230,a162
ar v231,a166
mm v[84:87],v[204:207],a[56:59],v[84:87]
rt v[196:197],v240 offset:42240
rt v[198:199],v240 offset:43264
mm v[52:55],v[204:207],a[52:55],v[52:55]
s_add_u32 m0, s83, 52864
v_perm_b32 v232, v231, v230, s86
mm v[52:55],v[200:203],a[84:87],v[52:55]
ld v226,s[56:59],s80 offen lds
mm v[20:23],v[200:203],a[80:83],v[20:23]
wt lgkmcnt(4)
v_perm_b32 v233, v231, v230, s87
mm v[20:23],v[204:207],a[48:51],v[20:23]
rt v[200:201],v241 offset:33792
rt v[202:203],v241 offset:34816
mm v[24:27],v[212:215],a[48:51],v[24:27]
aw a144,v232
aw a148,v233
mm v[24:27],v[208:211],a[80:83],v[24:27]
ld a[32:35],v244,s[44:47],s52 offen offset:2048 sc1
mm v[56:59],v[208:211],a[84:87],v[56:59]
ar v234,a170
ar v235,a174
mm v[56:59],v[212:215],a[52:55],v[56:59]
rt v[204:205],v241 offset:42240
rt v[206:207],v241 offset:43264
mm v[88:91],v[212:215],a[56:59],v[88:91]
v_perm_b32 v236, v235, v234, s86
v_perm_b32 v237, v235, v234, s87
mm v[88:91],v[208:211],a[88:91],v[88:91]
ld a[36:39],v245,s[44:47],s52 offen offset:2048 sc1
mm v[120:123],v[208:211],a[92:95],v[120:123]
aw a145,v236
aw a149,v237
mm v[120:123],v[212:215],a[60:63],v[120:123]
rt v[208:209],v242 offset:33792
rt v[210:211],v242 offset:34816
mm v[124:127],v[220:223],a[60:63],v[124:127]
ar v230,a178
ar v231,a182
mm v[124:127],v[216:219],a[92:95],v[124:127]
ld a[40:43],v246,s[44:47],s52 offen offset:2048 sc1
mm v[92:95],v[216:219],a[88:91],v[92:95]
v_perm_b32 v232, v231, v230, s86
v_perm_b32 v233, v231, v230, s87
mm v[92:95],v[220:223],a[56:59],v[92:95]
rt v[212:213],v242 offset:42240
rt v[214:215],v242 offset:43264
mm v[60:63],v[220:223],a[52:55],v[60:63]
aw a146,v232
aw a150,v233
mm v[60:63],v[216:219],a[84:87],v[60:63]
ar v234,a186
ar v235,a190
mm v[28:31],v[216:219],a[80:83],v[28:31]
wt vmcnt(20) lgkmcnt(4)
v_perm_b32 v236, v235, v234, s86
mm v[28:31],v[220:223],a[48:51],v[28:31]
rt v[216:217],v243 offset:33792
rt v[218:219],v243 offset:34816
mm v[0:3],v[192:195],a[0:3],v[0:3]
v_perm_b32 v237, v235, v234, s87
aw a147,v236
mm v[0:3],v[196:199],a[16:19],v[0:3]
ld a[44:47],v247,s[44:47],s52 offen offset:2048 sc1
mm v[32:35],v[196:199],a[20:23],v[32:35]
aw a151,v237
ar v230,a163
mm v[32:35],v[192:195],a[4:7],v[32:35]
rt v[220:221],v243 offset:42240
rt v[222:223],v243 offset:43264
mm v[64:67],v[192:195],a[8:11],v[64:67]
ar v231,a167
v_perm_b32 v232, v231, v230, s86
mm v[64:67],v[196:199],a[24:27],v[64:67]
ld a[48:51],v244,s[44:47],s52 offen offset:3072 sc1
mm v[96:99],v[196:199],a[28:31],v[96:99]
v_perm_b32 v233, v231, v230, s87
aw a152,v232
mm v[96:99],v[192:195],a[12:15],v[96:99]
rt v[196:197],v240 offset:33920
rt v[198:199],v240 offset:34944
mm v[100:103],v[200:203],a[12:15],v[100:103]
aw a156,v233
ar v234,a171
mm v[100:103],v[204:207],a[28:31],v[100:103]
ld a[52:55],v245,s[44:47],s52 offen offset:3072 sc1
mm v[68:71],v[204:207],a[24:27],v[68:71]
ar v235,a175
v_perm_b32 v236, v235, v234, s86
mm v[68:71],v[200:203],a[8:11],v[68:71]
rt v[192:193],v240 offset:42368
rt v[194:195],v240 offset:43392
mm v[36:39],v[200:203],a[4:7],v[36:39]
v_add_u32 v240, 4294899712, v240
v_perm_b32 v237, v235, v234, s87
mm v[36:39],v[204:207],a[20:23],v[36:39]
ld a[56:59],v246,s[44:47],s52 offen offset:3072 sc1
mm v[4:7],v[204:207],a[16:19],v[4:7]
wt lgkmcnt(4)
aw a153,v236
mm v[4:7],v[200:203],a[0:3],v[4:7]
rt v[204:205],v241 offset:33920
rt v[206:207],v241 offset:34944
mm v[8:11],v[208:211],a[0:3],v[8:11]
wt vmcnt(23)
v_cmp_eq_u64 vcc, v[250:251], v[252:253]
mm v[8:11],v[212:215],a[16:19],v[8:11]
ld a[60:63],v247,s[44:47],s52 offen offset:3072 sc1
mm v[40:43],v[212:215],a[20:23],v[40:43]
aw a157,v237
ar v230,a179
mm v[40:43],v[208:211],a[4:7],v[40:43]
rt v[200:201],v241 offset:42368
rt v[202:203],v241 offset:43392
mm v[72:75],v[208:211],a[8:11],v[72:75]
s_add_u32 m0, s83, 53888
v_add_u32 v241, 4294899712, v241
mm v[72:75],v[212:215],a[24:27],v[72:75]
ld v227,s[56:59],s80 offen lds
mm v[104:107],v[212:215],a[28:31],v[104:107]
s_add_u32 s80, s80, 131072
ar v231,a183
mm v[104:107],v[208:211],a[12:15],v[104:107]
rt v[212:213],v242 offset:33920
rt v[214:215],v242 offset:34944
mm v[108:111],v[216:219],a[12:15],v[108:111]
v_perm_b32 v232, v231, v230, s86
v_perm_b32 v233, v231, v230, s87
mm v[108:111],v[220:223],a[28:31],v[108:111]
buffer_load_dwordx2 v[250:251], v248, s[64:67], s5 offen sc1
mm v[76:79],v[220:223],a[24:27],v[76:79]
aw a154,v232
aw a158,v233
mm v[76:79],v[216:219],a[8:11],v[76:79]
rt v[208:209],v242 offset:42368
rt v[210:211],v242 offset:43392
mm v[44:47],v[216:219],a[4:7],v[44:47]
s_add_u32 m0, s83, 0
v_add_u32 v242, 4294899712, v242
mm v[44:47],v[220:223],a[20:23],v[44:47]
ld v224,s[56:59],s80 offen lds
mm v[12:15],v[220:223],a[16:19],v[12:15]
wt lgkmcnt(4)
ar v234,a187
mm v[12:15],v[216:219],a[0:3],v[12:15]
rt v[220:221],v243 offset:33920
rt v[222:223],v243 offset:34944
mm v[16:19],v[196:199],a[0:3],v[16:19]
s_add_u32 m0, s83, 1024
ar v235,a191
mm v[16:19],v[192:195],a[16:19],v[16:19]
ld v225,s[56:59],s80 offen lds
mm v[48:51],v[192:195],a[20:23],v[48:51]
v_perm_b32 v236, v235, v234, s86
v_perm_b32 v237, v235, v234, s87
mm v[48:51],v[196:199],a[4:7],v[48:51]
rt v[216:217],v243 offset:42368
rt v[218:219],v243 offset:43392
mm v[80:83],v[196:199],a[8:11],v[80:83]
s_add_u32 m0, s83, 2176
v_add_u32 v243, 4294899712, v243
mm v[80:83],v[192:195],a[24:27],v[80:83]
ld v226,s[56:59],s80 offen lds
mm v[112:115],v[192:195],a[28:31],v[112:115]
wt vmcnt(24) lgkmcnt(0)
s_barrier
mm v[112:115],v[196:199],a[12:15],v[112:115]
rt v[192:193],v240 offset:16896
rt v[194:195],v240 offset:17920
mm v[116:119],v[204:207],a[12:15],v[116:119]
s_add_u32 m0, s83, 3200
aw a155,v236
mm v[116:119],v[200:203],a[28:31],v[116:119]
ld v227,s[56:59],s80 offen lds
mm v[84:87],v[200:203],a[24:27],v[84:87]
s_bitcmp1_b32 vcc_lo, 5
s_cbranch_scc0 .La5_LO_NA5
.La5_LO_KA5:
mm v[84:87],v[204:207],a[8:11],v[84:87]
rt v[196:197],v240 offset:25344
rt v[198:199],v240 offset:26368
mm v[52:55],v[204:207],a[4:7],v[52:55]
s_bfe_u32 s80, s4, 262168
s_xor_b32 s80, s80, s72
mm v[52:55],v[200:203],a[20:23],v[52:55]
ld v[128:131],v244,s[44:47],s53 offen offset:0 sc1
mm v[20:23],v[200:203],a[16:19],v[20:23]
s_nop 0
s_lshl_b32 s80, s80, 18
mm v[20:23],v[204:207],a[0:3],v[20:23]
rt v[200:201],v241 offset:16896
rt v[202:203],v241 offset:17920
mm v[24:27],v[212:215],a[0:3],v[24:27]
aw a159,v237
ar v230,a192
mm v[24:27],v[208:211],a[16:19],v[24:27]
ld v[132:135],v245,s[44:47],s53 offen offset:0 sc1
mm v[56:59],v[208:211],a[20:23],v[56:59]
ar v231,a196
v_perm_b32 v232, v231, v230, s86
mm v[56:59],v[212:215],a[4:7],v[56:59]
rt v[204:205],v241 offset:25344
rt v[206:207],v241 offset:26368
mm v[88:91],v[212:215],a[8:11],v[88:91]
v_perm_b32 v233, v231, v230, s87
aw a160,v232
mm v[88:91],v[208:211],a[24:27],v[88:91]
ld v[144:147],v246,s[44:47],s53 offen offset:0 sc1
mm v[120:123],v[208:211],a[28:31],v[120:123]
aw a164,v233
ar v234,a200
mm v[120:123],v[212:215],a[12:15],v[120:123]
rt v[208:209],v242 offset:16896
rt v[210:211],v242 offset:17920
mm v[124:127],v[220:223],a[12:15],v[124:127]
ar v235,a204
v_perm_b32 v236, v235, v234, s86
mm v[124:127],v[216:219],a[28:31],v[124:127]
ld a[12:15],v247,s[44:47],s53 offen offset:0 sc1
mm v[92:95],v[216:219],a[24:27],v[92:95]
v_perm_b32 v237, v235, v234, s87
aw a161,v236
mm v[92:95],v[220:223],a[8:11],v[92:95]
rt v[212:213],v242 offset:25344
rt v[214:215],v242 offset:26368
mm v[60:63],v[220:223],a[4:7],v[60:63]
aw a165,v237
ar v230,a208
mm v[60:63],v[216:219],a[20:23],v[60:63]
ar v231,a212
v_perm_b32 v232, v231, v230, s86
mm v[28:31],v[216:219],a[16:19],v[28:31]
wt vmcnt(21) lgkmcnt(4)
v_perm_b32 v233, v231, v230, s87
mm v[28:31],v[220:223],a[0:3],v[28:31]
rt v[216:217],v243 offset:16896
rt v[218:219],v243 offset:17920
mm v[0:3],v[192:195],v[136:139],v[0:3]
aw a162,v232
aw a166,v233
mm v[0:3],v[196:199],v[168:171],v[0:3]
ld a[0:3],v244,s[44:47],s53 offen offset:1024 sc1
mm v[32:35],v[196:199],v[172:175],v[32:35]
ar v234,a216
ar v235,a220
mm v[32:35],v[192:195],v[140:143],v[32:35]
rt v[220:221],v243 offset:25344
rt v[222:223],v243 offset:26368
mm v[64:67],v[192:195],v[152:155],v[64:67]
v_perm_b32 v236, v235, v234, s86
v_perm_b32 v237, v235, v234, s87
mm v[64:67],v[196:199],v[184:187],v[64:67]
ld a[4:7],v245,s[44:47],s53 offen offset:1024 sc1
mm v[96:99],v[196:199],v[188:191],v[96:99]
aw a163,v236
aw a167,v237
mm v[96:99],v[192:195],v[156:159],v[96:99]
rt v[196:197],v240 offset:17024
rt v[198:199],v240 offset:18048
mm v[100:103],v[200:203],v[156:159],v[100:103]
ar v230,a193
ar v231,a197
mm v[100:103],v[204:207],v[188:191],v[100:103]
ld a[8:11],v246,s[44:47],s53 offen offset:1024 sc1
mm v[68:71],v[204:207],v[184:187],v[68:71]
v_perm_b32 v232, v231, v230, s86
v_perm_b32 v233, v231, v230, s87
mm v[68:71],v[200:203],v[152:155],v[68:71]
rt v[192:193],v240 offset:25472
rt v[194:195],v240 offset:26496
mm v[36:39],v[200:203],v[140:143],v[36:39]
aw a168,v232
aw a172,v233
mm v[36:39],v[204:207],v[172:175],v[36:39]
ld a[16:19],v247,s[44:47],s53 offen offset:1024 sc1
mm v[4:7],v[204:207],v[168:171],v[4:7]
wt lgkmcnt(4)
ar v234,a201
mm v[4:7],v[200:203],v[136:139],v[4:7]
rt v[204:205],v241 offset:17024
rt v[206:207],v241 offset:18048
mm v[8:11],v[208:211],v[136:139],v[8:11]
s_add_u32 m0, s83, 118272
ar v235,a205
.La5_LW3_2:
s_cbranch_execz .La5_LW3_6
mm v[8:11],v[212:215],v[168:171],v[8:11]
ld v224,s[56:59],s80 offen lds
mm v[40:43],v[212:215],v[172:175],v[40:43]
v_perm_b32 v236, v235, v234, s86
v_perm_b32 v237, v235, v234, s87
mm v[40:43],v[208:211],v[140:143],v[40:43]
rt v[200:201],v241 offset:25472
rt v[202:203],v241 offset:26496
mm v[72:75],v[208:211],v[152:155],v[72:75]
s_add_u32 m0, s83, 119296
aw a169,v236
mm v[72:75],v[212:215],v[184:187],v[72:75]
ld v225,s[56:59],s80 offen lds
mm v[104:107],v[212:215],v[188:191],v[104:107]
aw a173,v237
ar v230,a209
mm v[104:107],v[208:211],v[156:159],v[104:107]
rt v[212:213],v242 offset:17024
rt v[214:215],v242 offset:18048
mm v[108:111],v[216:219],v[156:159],v[108:111]
s_add_u32 m0, s83, 120448
ar v231,a213
mm v[108:111],v[220:223],v[188:191],v[108:111]
ld v226,s[56:59],s80 offen lds
mm v[76:79],v[220:223],v[184:187],v[76:79]
v_perm_b32 v232, v231, v230, s86
v_perm_b32 v233, v231, v230, s87
mm v[76:79],v[216:219],v[152:155],v[76:79]
rt v[208:209],v242 offset:25472
rt v[210:211],v242 offset:26496
mm v[44:47],v[216:219],v[140:143],v[44:47]
aw a170,v232
aw a174,v233
mm v[44:47],v[220:223],v[172:175],v[44:47]
ar v234,a217
ar v235,a221
mm v[12:15],v[220:223],v[168:171],v[12:15]
wt lgkmcnt(4)
v_perm_b32 v236, v235, v234, s86
mm v[12:15],v[216:219],v[136:139],v[12:15]
rt v[220:221],v243 offset:17024
rt v[222:223],v243 offset:18048
mm v[16:19],v[196:199],v[136:139],v[16:19]
v_perm_b32 v237, v235, v234, s87
aw a171,v236
mm v[16:19],v[192:195],v[168:171],v[16:19]
ld a[20:23],v244,s[44:47],s53 offen offset:2048 sc1
mm v[48:51],v[192:195],v[172:175],v[48:51]
aw a175,v237
ar v230,a194
mm v[48:51],v[196:199],v[140:143],v[48:51]
rt v[216:217],v243 offset:25472
rt v[218:219],v243 offset:26496
mm v[80:83],v[196:199],v[152:155],v[80:83]
ar v231,a198
v_perm_b32 v232, v231, v230, s86
mm v[80:83],v[192:195],v[184:187],v[80:83]
ld a[24:27],v245,s[44:47],s53 offen offset:2048 sc1
mm v[112:115],v[192:195],v[188:191],v[112:115]
v_perm_b32 v233, v231, v230, s87
aw a176,v232
mm v[112:115],v[196:199],v[156:159],v[112:115]
rt v[192:193],v240 offset:33792
rt v[194:195],v240 offset:34816
mm v[116:119],v[204:207],v[156:159],v[116:119]
aw a180,v233
ar v234,a202
mm v[116:119],v[200:203],v[188:191],v[116:119]
ld a[28:31],v246,s[44:47],s53 offen offset:2048 sc1
mm v[84:87],v[200:203],v[184:187],v[84:87]
ar v235,a206
v_perm_b32 v236, v235, v234, s86
mm v[84:87],v[204:207],v[152:155],v[84:87]
rt v[196:197],v240 offset:42240
rt v[198:199],v240 offset:43264
mm v[52:55],v[204:207],v[140:143],v[52:55]
v_perm_b32 v237, v235, v234, s87
aw a177,v236
mm v[52:55],v[200:203],v[172:175],v[52:55]
ld v[148:151],v247,s[44:47],s53 offen offset:2048 sc1
mm v[20:23],v[200:203],v[168:171],v[20:23]
wt lgkmcnt(4)
aw a181,v237
mm v[20:23],v[204:207],v[136:139],v[20:23]
rt v[200:201],v241 offset:33792
rt v[202:203],v241 offset:34816
mm v[24:27],v[212:215],v[136:139],v[24:27]
ar v230,a210
ar v231,a214
mm v[24:27],v[208:211],v[168:171],v[24:27]
ld v[160:163],v244,s[44:47],s53 offen offset:3072 sc1
mm v[56:59],v[208:211],v[172:175],v[56:59]
v_perm_b32 v232, v231, v230, s86
v_perm_b32 v233, v231, v230, s87
mm v[56:59],v[212:215],v[140:143],v[56:59]
rt v[204:205],v241 offset:42240
rt v[206:207],v241 offset:43264
mm v[88:91],v[212:215],v[152:155],v[88:91]
aw a178,v232
aw a182,v233
mm v[88:91],v[208:211],v[184:187],v[88:91]
ld v[164:167],v245,s[44:47],s53 offen offset:3072 sc1
mm v[120:123],v[208:211],v[188:191],v[120:123]
ar v234,a218
ar v235,a222
mm v[120:123],v[212:215],v[156:159],v[120:123]
rt v[208:209],v242 offset:33792
rt v[210:211],v242 offset:34816
mm v[124:127],v[220:223],v[156:159],v[124:127]
v_perm_b32 v236, v235, v234, s86
v_perm_b32 v237, v235, v234, s87
mm v[124:127],v[216:219],v[188:191],v[124:127]
ld v[156:159],v246,s[44:47],s53 offen offset:3072 sc1
mm v[92:95],v[216:219],v[184:187],v[92:95]
aw a179,v236
aw a183,v237
mm v[92:95],v[220:223],v[152:155],v[92:95]
rt v[212:213],v242 offset:42240
rt v[214:215],v242 offset:43264
mm v[60:63],v[220:223],v[140:143],v[60:63]
ar v230,a195
ar v231,a199
mm v[60:63],v[216:219],v[172:175],v[60:63]
wt vmcnt(22)
v_cmp_eq_u64 vcc, v[250:251], v[252:253]
mm v[28:31],v[216:219],v[168:171],v[28:31]
wt lgkmcnt(4)
v_perm_b32 v232, v231, v230, s86
mm v[28:31],v[220:223],v[136:139],v[28:31]
rt v[216:217],v243 offset:33792
rt v[218:219],v243 offset:34816
mm v[0:3],v[192:195],a[32:35],v[0:3]
v_perm_b32 v233, v231, v230, s87
aw a184,v232
mm v[0:3],v[196:199],a[48:51],v[0:3]
ld v[136:139],v247,s[44:47],s53 offen offset:3072 sc1
mm v[32:35],v[196:199],a[52:55],v[32:35]
aw a188,v233
ar v234,a203
mm v[32:35],v[192:195],a[36:39],v[32:35]
rt v[220:221],v243 offset:42240
rt v[222:223],v243 offset:43264
mm v[64:67],v[192:195],a[40:43],v[64:67]
s_add_u32 m0, s83, 121472
ar v235,a207
mm v[64:67],v[196:199],a[56:59],v[64:67]
ld v227,s[56:59],s80 offen lds
mm v[96:99],v[196:199],a[60:63],v[96:99]
s_add_u32 s80, s80, 131072
v_perm_b32 v236, v235, v234, s86
mm v[96:99],v[192:195],a[44:47],v[96:99]
rt v[196:197],v240 offset:33920
rt v[198:199],v240 offset:34944
mm v[100:103],v[200:203],a[44:47],v[100:103]
v_perm_b32 v237, v235, v234, s87
aw a185,v236
mm v[100:103],v[204:207],a[60:63],v[100:103]
buffer_load_dwordx2 v[250:251], v248, s[64:67], s5 offen sc1
mm v[68:71],v[204:207],a[56:59],v[68:71]
aw a189,v237
ar v230,a211
mm v[68:71],v[200:203],a[40:43],v[68:71]
rt v[192:193],v240 offset:42368
rt v[194:195],v240 offset:43392
mm v[36:39],v[200:203],a[36:39],v[36:39]
s_add_u32 m0, s83, 67584
ar v231,a215
mm v[36:39],v[204:207],a[52:55],v[36:39]
ld v224,s[56:59],s80 offen lds
mm v[4:7],v[204:207],a[48:51],v[4:7]
wt lgkmcnt(4)
v_perm_b32 v232, v231, v230, s86
mm v[4:7],v[200:203],a[32:35],v[4:7]
rt v[204:205],v241 offset:33920
rt v[206:207],v241 offset:34944
mm v[8:11],v[208:211],a[32:35],v[8:11]
s_add_u32 m0, s83, 68608
v_perm_b32 v233, v231, v230, s87
mm v[8:11],v[212:215],a[48:51],v[8:11]
ld v225,s[56:59],s80 offen lds
mm v[40:43],v[212:215],a[52:55],v[40:43]
aw a186,v232
aw a190,v233
mm v[40:43],v[208:211],a[36:39],v[40:43]
rt v[200:201],v241 offset:42368
rt v[202:203],v241 offset:43392
mm v[72:75],v[208:211],a[40:43],v[72:75]
s_add_u32 m0, s83, 69760
ar v234,a219
mm v[72:75],v[212:215],a[56:59],v[72:75]
ld v226,s[56:59],s80 offen lds
mm v[104:107],v[212:215],a[60:63],v[104:107]
ar v235,a223
v_perm_b32 v236, v235, v234, s86
mm v[104:107],v[208:211],a[44:47],v[104:107]
rt v[212:213],v242 offset:33920
rt v[214:215],v242 offset:34944
mm v[108:111],v[216:219],a[44:47],v[108:111]
s_add_u32 m0, s83, 70784
v_perm_b32 v237, v235, v234, s87
mm v[108:111],v[220:223],a[60:63],v[108:111]
ld v227,s[56:59],s80 offen lds
mm v[76:79],v[220:223],a[56:59],v[76:79]
s_bfe_u32 s80, s4, 262172
s_xor_b32 s80, s80, s72
mm v[76:79],v[216:219],a[40:43],v[76:79]
rt v[208:209],v242 offset:42368
rt v[210:211],v242 offset:43392
mm v[44:47],v[216:219],a[36:39],v[44:47]
s_add_u32 m0, s83, 84480
s_lshl_b32 s80, s80, 18
mm v[44:47],v[220:223],a[52:55],v[44:47]
ld v224,s[56:59],s80 offen lds
mm v[12:15],v[220:223],a[48:51],v[12:15]
wt lgkmcnt(4)
aw a187,v236
mm v[12:15],v[216:219],a[32:35],v[12:15]
rt v[220:221],v243 offset:33920
rt v[222:223],v243 offset:34944
mm v[16:19],v[196:199],a[32:35],v[16:19]
s_add_u32 m0, s83, 85504
aw a191,v237
mm v[16:19],v[192:195],a[48:51],v[16:19]
ld v225,s[56:59],s80 offen lds
mm v[48:51],v[192:195],a[52:55],v[48:51]
ar v230,a224
ar v231,a228
mm v[48:51],v[196:199],a[36:39],v[48:51]
rt v[216:217],v243 offset:42368
rt v[218:219],v243 offset:43392
mm v[80:83],v[196:199],a[40:43],v[80:83]
s_add_u32 m0, s83, 86656
v_perm_b32 v232, v231, v230, s86
mm v[80:83],v[192:195],a[56:59],v[80:83]
ld v226,s[56:59],s80 offen lds
mm v[112:115],v[192:195],a[60:63],v[112:115]
wt vmcnt(28)
s_barrier
mm v[112:115],v[196:199],a[44:47],v[112:115]
rt v[192:193],v240 offset:50688
rt v[194:195],v240 offset:51712
mm v[116:119],v[204:207],a[44:47],v[116:119]
s_add_u32 m0, s83, 87680
v_perm_b32 v233, v231, v230, s87
mm v[116:119],v[200:203],a[60:63],v[116:119]
ld v227,s[56:59],s80 offen lds
mm v[84:87],v[200:203],a[56:59],v[84:87]
s_add_u32 s80, s80, 131072
aw a192,v232
mm v[84:87],v[204:207],a[40:43],v[84:87]
rt v[196:197],v240 offset:59136
rt v[198:199],v240 offset:60160
mm v[52:55],v[204:207],a[36:39],v[52:55]
s_add_u32 m0, s83, 101376
aw a196,v233
mm v[52:55],v[200:203],a[52:55],v[52:55]
ld v224,s[56:59],s80 offen lds
mm v[20:23],v[200:203],a[48:51],v[20:23]
wt lgkmcnt(4)
ar v234,a232
mm v[20:23],v[204:207],a[32:35],v[20:23]
rt v[200:201],v241 offset:50688
rt v[202:203],v241 offset:51712
mm v[24:27],v[212:215],a[32:35],v[24:27]
s_add_u32 m0, s83, 102400
ar v235,a236
mm v[24:27],v[208:211],a[48:51],v[24:27]
ld v225,s[56:59],s80 offen lds
mm v[56:59],v[208:211],a[52:55],v[56:59]
v_perm_b32 v236, v235, v234, s86
v_perm_b32 v237, v235, v234, s87
mm v[56:59],v[212:215],a[36:39],v[56:59]
rt v[204:205],v241 offset:59136
rt v[206:207],v241 offset:60160
mm v[88:91],v[212:215],a[40:43],v[88:91]
s_add_u32 m0, s83, 103552
aw a193,v236
mm v[88:91],v[208:211],a[56:59],v[88:91]
ld v226,s[56:59],s80 offen lds
mm v[120:123],v[208:211],a[60:63],v[120:123]
aw a197,v237
ar v230,a240
mm v[120:123],v[212:215],a[44:47],v[120:123]
rt v[208:209],v242 offset:50688
rt v[210:211],v242 offset:51712
mm v[124:127],v[220:223],a[44:47],v[124:127]
s_add_u32 m0, s83, 104576
ar v231,a244
mm v[124:127],v[216:219],a[60:63],v[124:127]
ld v227,s[56:59],s80 offen lds
mm v[92:95],v[216:219],a[56:59],v[92:95]
v_bfe_u32 v224, v255, 6, 1
v_lshrrev_b32_e64 v225, 7, v255
mm v[92:95],v[220:223],a[40:43],v[92:95]
rt v[212:213],v242 offset:59136
rt v[214:215],v242 offset:60160
mm v[60:63],v[220:223],a[36:39],v[60:63]
s_bitcmp1_b32 vcc_lo, 6
s_cbranch_scc0 .La5_LO_NA6
.La5_LO_KA6:
mm v[60:63],v[216:219],a[52:55],v[60:63]
v_and_b32 v226, 48, v255
v_lshl_add_u32 v225, v225, 6, v226
mm v[28:31],v[216:219],a[48:51],v[28:31]
wt vmcnt(25) lgkmcnt(4)
v_lshl_add_u32 v224, v224, 11, v225
mm v[28:31],v[220:223],a[32:35],v[28:31]
rt v[216:217],v243 offset:50688
rt v[218:219],v243 offset:51712
mm v[0:3],v[192:195],v[128:131],v[0:3]
v_perm_b32 v232, v231, v230, s86
v_perm_b32 v233, v231, v230, s87
mm v[0:3],v[196:199],a[0:3],v[0:3]
ld a[64:67],v244,s[44:47],s54 offen offset:0 sc1
mm v[32:35],v[196:199],a[4:7],v[32:35]
aw a194,v232
aw a198,v233
mm v[32:35],v[192:195],v[132:135],v[32:35]
rt v[220:221],v243 offset:59136
rt v[222:223],v243 offset:60160
mm v[64:67],v[192:195],v[144:147],v[64:67]
ar v234,a248
ar v235,a252
mm v[64:67],v[196:199],a[8:11],v[64:67]
ld a[68:71],v245,s[44:47],s54 offen offset:0 sc1
mm v[96:99],v[196:199],a[16:19],v[96:99]
v_perm_b32 v236, v235, v234, s86
v_perm_b32 v237, v235, v234, s87
mm v[96:99],v[192:195],a[12:15],v[96:99]
rt v[196:197],v240 offset:50816
rt v[198:199],v240 offset:51840
mm v[100:103],v[200:203],a[12:15],v[100:103]
aw a195,v236
aw a199,v237
mm v[100:103],v[204:207],a[16:19],v[100:103]
ld a[72:75],v244,s[44:47],s54 offen offset:1024 sc1
mm v[68:71],v[204:207],a[8:11],v[68:71]
ar v230,a225
ar v231,a229
mm v[68:71],v[200:203],v[144:147],v[68:71]
rt v[192:193],v240 offset:59264
rt v[194:195],v240 offset:60288
mm v[36:39],v[200:203],v[132:135],v[36:39]
v_perm_b32 v232, v231, v230, s86
v_perm_b32 v233, v231, v230, s87
mm v[36:39],v[204:207],a[4:7],v[36:39]
ld a[76:79],v245,s[44:47],s54 offen offset:1024 sc1
mm v[4:7],v[204:207],a[0:3],v[4:7]
wt lgkmcnt(4)
aw a200,v232
mm v[4:7],v[200:203],v[128:131],v[4:7]
rt v[204:205],v241 offset:50816
rt v[206:207],v241 offset:51840
mm v[8:11],v[208:211],v[128:131],v[8:11]
aw a204,v233
ar v234,a233
mm v[8:11],v[212:215],a[0:3],v[8:11]
ld a[80:83],v244,s[44:47],s54 offen offset:2048 sc1
mm v[40:43],v[212:215],a[4:7],v[40:43]
ar v235,a237
v_perm_b32 v236, v235, v234, s86
mm v[40:43],v[208:211],v[132:135],v[40:43]
rt v[200:201],v241 offset:59264
rt v[202:203],v241 offset:60288
mm v[72:75],v[208:211],v[144:147],v[72:75]
v_perm_b32 v237, v235, v234, s87
aw a201,v236
mm v[72:75],v[212:215],a[8:11],v[72:75]
ld a[84:87],v245,s[44:47],s54 offen offset:2048 sc1
mm v[104:107],v[212:215],a[16:19],v[104:107]
aw a205,v237
ar v230,a241
mm v[104:107],v[208:211],a[12:15],v[104:107]
rt v[212:213],v242 offset:50816
rt v[214:215],v242 offset:51840
mm v[108:111],v[216:219],a[12:15],v[108:111]
ar v231,a245
v_perm_b32 v232, v231, v230, s86
mm v[108:111],v[220:223],a[16:19],v[108:111]
ld a[88:91],v244,s[44:47],s54 offen offset:3072 sc1
mm v[76:79],v[220:223],a[8:11],v[76:79]
v_perm_b32 v233, v231, v230, s87
aw a202,v232
mm v[76:79],v[216:219],v[144:147],v[76:79]
rt v[208:209],v242 offset:59264
rt v[210:211],v242 offset:60288
mm v[44:47],v[216:219],v[132:135],v[44:47]
aw a206,v233
ar v234,a249
mm v[44:47],v[220:223],a[4:7],v[44:47]
ar v235,a253
v_perm_b32 v236, v235, v234, s86
mm v[12:15],v[220:223],a[0:3],v[12:15]
wt lgkmcnt(4)
v_perm_b32 v237, v235, v234, s87
mm v[12:15],v[216:219],v[128:131],v[12:15]
rt v[220:221],v243 offset:50816
rt v[222:223],v243 offset:51840
mm v[16:19],v[196:199],v[128:131],v[16:19]
s_bitcmp1_b32 vcc_lo, 7
s_cbranch_scc0 .La5_LO_NA7
.La5_LO_KA7:
mm v[16:19],v[192:195],a[0:3],v[16:19]
ld a[92:95],v245,s[44:47],s54 offen offset:3072 sc1
mm v[48:51],v[192:195],a[4:7],v[48:51]
aw a203,v236
aw a207,v237
mm v[48:51],v[196:199],v[132:135],v[48:51]
rt v[216:217],v243 offset:59264
rt v[218:219],v243 offset:60288
mm v[80:83],v[196:199],v[144:147],v[80:83]
ar v230,a226
ar v231,a230
mm v[80:83],v[192:195],a[8:11],v[80:83]
ld a[32:35],v244,s[44:47],s55 offen offset:0 sc1
mm v[112:115],v[192:195],a[16:19],v[112:115]
v_perm_b32 v232, v231, v230, s86
v_perm_b32 v233, v231, v230, s87
mm v[112:115],v[196:199],a[12:15],v[112:115]
rt v[192:193],v240 offset:0
rt v[194:195],v240 offset:1024
mm v[116:119],v[204:207],a[12:15],v[116:119]
aw a208,v232
aw a212,v233
mm v[116:119],v[200:203],a[16:19],v[116:119]
ld a[36:39],v245,s[44:47],s55 offen offset:0 sc1
mm v[84:87],v[200:203],a[8:11],v[84:87]
ar v234,a234
ar v235,a238
mm v[84:87],v[204:207],v[144:147],v[84:87]
rt v[196:197],v240 offset:8448
rt v[198:199],v240 offset:9472
mm v[52:55],v[204:207],v[132:135],v[52:55]
v_perm_b32 v236, v235, v234, s86
v_perm_b32 v237, v235, v234, s87
mm v[52:55],v[200:203],a[4:7],v[52:55]
ld a[40:43],v244,s[44:47],s55 offen offset:1024 sc1
mm v[20:23],v[200:203],a[0:3],v[20:23]
wt lgkmcnt(4)
aw a209,v236
mm v[20:23],v[204:207],v[128:131],v[20:23]
rt v[200:201],v241 offset:0
rt v[202:203],v241 offset:1024
mm v[24:27],v[212:215],v[128:131],v[24:27]
wt vmcnt(23)
v_cmp_eq_u64 vcc, v[250:251], v[252:253]
mm v[24:27],v[208:211],a[0:3],v[24:27]
ld a[44:47],v245,s[44:47],s55 offen offset:1024 sc1
mm v[56:59],v[208:211],a[4:7],v[56:59]
aw a213,v237
ar v230,a242
mm v[56:59],v[212:215],v[132:135],v[56:59]
rt v[204:205],v241 offset:8448
rt v[206:207],v241 offset:9472
mm v[88:91],v[212:215],v[144:147],v[88:91]
ar v231,a246
v_perm_b32 v232, v231, v230, s86
mm v[88:91],v[208:211],a[8:11],v[88:91]
ld a[48:51],v244,s[44:47],s55 offen offset:2048 sc1
mm v[120:123],v[208:211],a[16:19],v[120:123]
v_perm_b32 v233, v231, v230, s87
aw a210,v232
mm v[120:123],v[212:215],a[12:15],v[120:123]
rt v[208:209],v242 offset:0
rt v[210:211],v242 offset:1024
mm v[124:127],v[220:223],a[12:15],v[124:127]
aw a214,v233
ar v234,a250
mm v[124:127],v[216:219],a[16:19],v[124:127]
ld a[12:15],v245,s[44:47],s55 offen offset:2048 sc1
mm v[92:95],v[216:219],a[8:11],v[92:95]
ar v235,a254
v_perm_b32 v236, v235, v234, s86
mm v[92:95],v[220:223],v[144:147],v[92:95]
rt v[212:213],v242 offset:8448
rt v[214:215],v242 offset:9472
mm v[60:63],v[220:223],v[132:135],v[60:63]
v_perm_b32 v237, v235, v234, s87
aw a211,v236
mm v[60:63],v[216:219],a[4:7],v[60:63]
aw a215,v237
ar v230,a227
mm v[28:31],v[216:219],a[0:3],v[28:31]
wt lgkmcnt(4)
ar v231,a231
mm v[28:31],v[220:223],v[128:131],v[28:31]
rt v[216:217],v243 offset:0
rt v[218:219],v243 offset:1024
mm v[0:3],v[192:195],a[20:23],v[0:3]
v_perm_b32 v232, v231, v230, s86
v_perm_b32 v233, v231, v230, s87
mm v[0:3],v[196:199],v[160:163],v[0:3]
ld a[0:3],v244,s[44:47],s55 offen offset:3072 sc1
mm v[32:35],v[196:199],v[164:167],v[32:35]
aw a216,v232
aw a220,v233
mm v[32:35],v[192:195],a[24:27],v[32:35]
rt v[220:221],v243 offset:8448
rt v[222:223],v243 offset:9472
mm v[64:67],v[192:195],a[28:31],v[64:67]
ar v234,a235
ar v235,a239
mm v[64:67],v[196:199],v[156:159],v[64:67]
ld a[4:7],v245,s[44:47],s55 offen offset:3072 sc1
mm v[96:99],v[196:199],v[136:139],v[96:99]
v_perm_b32 v236, v235, v234, s86
v_perm_b32 v237, v235, v234, s87
mm v[96:99],v[192:195],v[148:151],v[96:99]
rt v[196:197],v240 offset:128
rt v[198:199],v240 offset:1152
mm v[100:103],v[200:203],v[148:151],v[100:103]
aw a217,v236
aw a221,v237
mm v[100:103],v[204:207],v[136:139],v[100:103]
ar v230,a243
ar v231,a247
mm v[68:71],v[204:207],v[156:159],v[68:71]
v_perm_b32 v232, v231, v230, s86
v_perm_b32 v233, v231, v230, s87
mm v[68:71],v[200:203],a[28:31],v[68:71]
rt v[192:193],v240 offset:8576
rt v[194:195],v240 offset:9600
mm v[36:39],v[200:203],a[24:27],v[36:39]
v_add_u32 v240, 67584, v240
aw a218,v232
mm v[36:39],v[204:207],v[164:167],v[36:39]
.La5_LW3_3:
s_cbranch_execz .La5_LA_RETURN
aw a222,v233
ar v234,a251
mm v[4:7],v[204:207],v[160:163],v[4:7]
wt lgkmcnt(4)
ar v235,a255
mm v[4:7],v[200:203],a[20:23],v[4:7]
rt v[204:205],v241 offset:128
rt v[206:207],v241 offset:1152
mm v[8:11],v[208:211],a[20:23],v[8:11]
v_perm_b32 v236, v235, v234, s86
v_perm_b32 v237, v235, v234, s87
mm v[8:11],v[212:215],v[160:163],v[8:11]
aw a219,v236
aw a223,v237
mm v[40:43],v[212:215],v[164:167],v[40:43]
mm v[40:43],v[208:211],a[24:27],v[40:43]
rt v[200:201],v241 offset:8576
rt v[202:203],v241 offset:9600
mm v[72:75],v[208:211],a[28:31],v[72:75]
v_add_u32 v241, 67584, v241
mm v[72:75],v[212:215],v[156:159],v[72:75]
mm v[104:107],v[212:215],v[136:139],v[104:107]
mm v[104:107],v[208:211],v[148:151],v[104:107]
rt v[212:213],v242 offset:128
rt v[214:215],v242 offset:1152
mm v[108:111],v[216:219],v[148:151],v[108:111]
mm v[108:111],v[220:223],v[136:139],v[108:111]
mm v[76:79],v[220:223],v[156:159],v[76:79]
mm v[76:79],v[216:219],a[28:31],v[76:79]
rt v[208:209],v242 offset:8576
rt v[210:211],v242 offset:9600
mm v[44:47],v[216:219],a[24:27],v[44:47]
v_add_u32 v242, 67584, v242
mm v[44:47],v[220:223],v[164:167],v[44:47]
mm v[12:15],v[220:223],v[160:163],v[12:15]
wt lgkmcnt(4)
mm v[12:15],v[216:219],a[20:23],v[12:15]
rt v[220:221],v243 offset:128
rt v[222:223],v243 offset:1152
mm v[16:19],v[196:199],a[20:23],v[16:19]
mm v[16:19],v[192:195],v[160:163],v[16:19]
ld a[8:11],v246,s[44:47],s54 offen offset:0 sc1
mm v[48:51],v[192:195],v[164:167],v[48:51]
mm v[48:51],v[196:199],a[24:27],v[48:51]
rt v[216:217],v243 offset:8576
rt v[218:219],v243 offset:9600
mm v[80:83],v[196:199],a[28:31],v[80:83]
v_add_u32 v243, 67584, v243
mm v[80:83],v[192:195],v[156:159],v[80:83]
ld a[16:19],v247,s[44:47],s54 offen offset:0 sc1
mm v[112:115],v[192:195],v[136:139],v[112:115]
wt vmcnt(18)
s_barrier
mm v[112:115],v[196:199],v[148:151],v[112:115]
rt v[192:193],v240 offset:50688
rt v[194:195],v240 offset:51712
mm v[116:119],v[204:207],v[148:151],v[116:119]
mm v[116:119],v[200:203],v[136:139],v[116:119]
ld a[52:55],v246,s[44:47],s54 offen offset:1024 sc1
mm v[84:87],v[200:203],v[156:159],v[84:87]
mm v[84:87],v[204:207],a[28:31],v[84:87]
rt v[196:197],v240 offset:59136
rt v[198:199],v240 offset:60160
mm v[52:55],v[204:207],a[24:27],v[52:55]
mm v[52:55],v[200:203],v[164:167],v[52:55]
ld a[56:59],v247,s[44:47],s54 offen offset:1024 sc1
mm v[20:23],v[200:203],v[160:163],v[20:23]
wt lgkmcnt(4)
mm v[20:23],v[204:207],a[20:23],v[20:23]
rt v[200:201],v240 offset:0
rt v[202:203],v240 offset:1024
mm v[24:27],v[212:215],a[20:23],v[24:27]
mm v[24:27],v[208:211],v[160:163],v[24:27]
ld a[60:63],v246,s[44:47],s54 offen offset:2048 sc1
mm v[56:59],v[208:211],v[164:167],v[56:59]
mm v[56:59],v[212:215],a[24:27],v[56:59]
rt v[204:205],v240 offset:8448
rt v[206:207],v240 offset:9472
mm v[88:91],v[212:215],a[28:31],v[88:91]
mm v[88:91],v[208:211],v[156:159],v[88:91]
ld v[128:131],v247,s[44:47],s54 offen offset:2048 sc1
mm v[120:123],v[208:211],v[136:139],v[120:123]
mm v[120:123],v[212:215],v[148:151],v[120:123]
rt v[208:209],v240 offset:16896
rt v[210:211],v240 offset:17920
mm v[124:127],v[220:223],v[148:151],v[124:127]
mm v[124:127],v[216:219],v[136:139],v[124:127]
ld v[132:135],v246,s[44:47],s54 offen offset:3072 sc1
mm v[92:95],v[216:219],v[156:159],v[92:95]
mm v[92:95],v[220:223],a[28:31],v[92:95]
rt v[212:213],v240 offset:25344
rt v[214:215],v240 offset:26368
mm v[60:63],v[220:223],a[24:27],v[60:63]
mm v[60:63],v[216:219],v[164:167],v[60:63]
mm v[28:31],v[216:219],v[160:163],v[28:31]
wt vmcnt(15) lgkmcnt(4)
mm v[28:31],v[220:223],a[20:23],v[28:31]
rt v[216:217],v240 offset:33792
rt v[218:219],v240 offset:34816
mm v[0:3],v[192:195],a[64:67],v[0:3]
ld a[20:23],v247,s[44:47],s54 offen offset:3072 sc1
mm v[0:3],v[196:199],a[72:75],v[0:3]
rt v[220:221],v240 offset:42240
rt v[222:223],v240 offset:43264
mm v[32:35],v[196:199],a[76:79],v[32:35]
mm v[32:35],v[192:195],a[68:71],v[32:35]
rt v[196:197],v241 offset:50688
rt v[198:199],v241 offset:51712
mm v[32:35],v[200:203],a[84:87],v[32:35]
ld a[24:27],v246,s[44:47],s55 offen offset:0 sc1
mm v[32:35],v[204:207],a[92:95],v[32:35]
rt v[192:193],v241 offset:59136
rt v[194:195],v241 offset:60160
mm v[0:3],v[204:207],a[88:91],v[0:3]
wt vmcnt(9) lgkmcnt(4)
mm v[0:3],v[200:203],a[80:83],v[0:3]
rt v[204:205],v241 offset:0
rt v[206:207],v241 offset:1024
mm v[0:3],v[208:211],a[32:35],v[0:3]
ld a[28:31],v247,s[44:47],s55 offen offset:0 sc1
mm v[0:3],v[212:215],a[40:43],v[0:3]
rt v[200:201],v241 offset:8448
rt v[202:203],v241 offset:9472
mm v[32:35],v[212:215],a[44:47],v[32:35]
mm v[32:35],v[208:211],a[36:39],v[32:35]
rt v[212:213],v241 offset:16896
rt v[214:215],v241 offset:17920
mm v[32:35],v[216:219],a[12:15],v[32:35]
ld v[144:147],v246,s[44:47],s55 offen offset:1024 sc1
mm v[32:35],v[220:223],a[4:7],v[32:35]
rt v[208:209],v241 offset:25344
rt v[210:211],v241 offset:26368
mm v[0:3],v[220:223],a[0:3],v[0:3]
wt lgkmcnt(4)
mm v[0:3],v[216:219],a[48:51],v[0:3]
rt v[220:221],v241 offset:33792
rt v[222:223],v241 offset:34816
mm v[4:7],v[196:199],a[64:67],v[4:7]
ld v[148:151],v247,s[44:47],s55 offen offset:1024 sc1
mm v[4:7],v[192:195],a[72:75],v[4:7]
rt v[216:217],v241 offset:42240
rt v[218:219],v241 offset:43264
mm v[36:39],v[192:195],a[76:79],v[36:39]
mm v[36:39],v[196:199],a[68:71],v[36:39]
rt v[192:193],v242 offset:50688
rt v[194:195],v242 offset:51712
mm v[36:39],v[204:207],a[84:87],v[36:39]
ld v[160:163],v246,s[44:47],s55 offen offset:2048 sc1
mm v[36:39],v[200:203],a[92:95],v[36:39]
rt v[196:197],v242 offset:59136
rt v[198:199],v242 offset:60160
mm v[4:7],v[200:203],a[88:91],v[4:7]
wt lgkmcnt(4)
mm v[4:7],v[204:207],a[80:83],v[4:7]
rt v[200:201],v242 offset:0
rt v[202:203],v242 offset:1024
mm v[4:7],v[212:215],a[32:35],v[4:7]
ld v[164:167],v247,s[44:47],s55 offen offset:2048 sc1
mm v[4:7],v[208:211],a[40:43],v[4:7]
rt v[204:205],v242 offset:8448
rt v[206:207],v242 offset:9472
mm v[36:39],v[208:211],a[44:47],v[36:39]
mm v[36:39],v[212:215],a[36:39],v[36:39]
rt v[208:209],v242 offset:16896
rt v[210:211],v242 offset:17920
mm v[36:39],v[220:223],a[12:15],v[36:39]
mm v[36:39],v[216:219],a[4:7],v[36:39]
rt v[212:213],v242 offset:25344
rt v[214:215],v242 offset:26368
mm v[4:7],v[216:219],a[0:3],v[4:7]
wt lgkmcnt(4)
mm v[4:7],v[220:223],a[48:51],v[4:7]
rt v[216:217],v242 offset:33792
rt v[218:219],v242 offset:34816
mm v[8:11],v[192:195],a[64:67],v[8:11]
ld v[176:179],v246,s[44:47],s55 offen offset:3072 sc1
mm v[8:11],v[196:199],a[72:75],v[8:11]
rt v[220:221],v242 offset:42240
rt v[222:223],v242 offset:43264
mm v[40:43],v[196:199],a[76:79],v[40:43]
cv v234,v32,v33
cv v236,v36,v37
mm v[40:43],v[192:195],a[68:71],v[40:43]
rt v[196:197],v243 offset:50688
rt v[198:199],v243 offset:51712
mm v[40:43],v[200:203],a[84:87],v[40:43]
ld v[180:183],v247,s[44:47],s55 offen offset:3072 sc1
mm v[40:43],v[204:207],a[92:95],v[40:43]
rt v[192:193],v243 offset:59136
rt v[194:195],v243 offset:60160
mm v[8:11],v[204:207],a[88:91],v[8:11]
wt lgkmcnt(4)
v_bfe_u32 v250, v255, 6, 1
mm v[8:11],v[200:203],a[80:83],v[8:11]
rt v[204:205],v243 offset:0
rt v[206:207],v243 offset:1024
mm v[8:11],v[208:211],a[32:35],v[8:11]
cv v235,v34,v35
cv v237,v38,v39
mm v[8:11],v[212:215],a[40:43],v[8:11]
rt v[200:201],v243 offset:8448
rt v[202:203],v243 offset:9472
mm v[40:43],v[212:215],a[44:47],v[40:43]
v_permlane16_swap_b32 v234, v236
v_permlane16_swap_b32 v235, v237
mm v[40:43],v[208:211],a[36:39],v[40:43]
rt v[212:213],v243 offset:16896
rt v[214:215],v243 offset:17920
mm v[40:43],v[216:219],a[12:15],v[40:43]
st v[234:237],v238,s[32:35],s95 offen offset:0 sc1
mm v[40:43],v[220:223],a[4:7],v[40:43]
rt v[208:209],v243 offset:25344
rt v[210:211],v243 offset:26368
mm v[8:11],v[220:223],a[0:3],v[8:11]
wt lgkmcnt(4)
v_mul_u32_u24 v250, 7, v250
mm v[8:11],v[216:219],a[48:51],v[8:11]
rt v[220:221],v243 offset:33792
rt v[222:223],v243 offset:34816
mm v[12:15],v[196:199],a[64:67],v[12:15]
cv v230,v0,v1
cv v232,v4,v5
mm v[12:15],v[192:195],a[72:75],v[12:15]
rt v[216:217],v243 offset:42240
rt v[218:219],v243 offset:43264
mm v[44:47],v[192:195],a[76:79],v[44:47]
cv v231,v2,v3
cv v233,v6,v7
mm v[44:47],v[196:199],a[68:71],v[44:47]
rt v[192:193],v240 offset:50816
rt v[194:195],v240 offset:51840
mm v[44:47],v[204:207],a[84:87],v[44:47]
v_permlane16_swap_b32 v230, v232
v_permlane16_swap_b32 v231, v233
mm v[44:47],v[200:203],a[92:95],v[44:47]
rt v[196:197],v240 offset:59264
rt v[198:199],v240 offset:60288
mm v[12:15],v[200:203],a[88:91],v[12:15]
wt lgkmcnt(4)
v_xor_b32 v250, s3, v250
mm v[12:15],v[204:207],a[80:83],v[12:15]
rt v[200:201],v240 offset:128
rt v[202:203],v240 offset:1152
mm v[12:15],v[212:215],a[32:35],v[12:15]
st v[230:233],v238,s[32:35],s94 offen offset:0 sc1
mm v[12:15],v[208:211],a[40:43],v[12:15]
rt v[204:205],v240 offset:8576
rt v[206:207],v240 offset:9600
mm v[44:47],v[208:211],a[44:47],v[44:47]
v_bfe_u32 v251, v255, 4, 2
v_lshl_add_u32 v250, v250, 2, v251
mm v[44:47],v[212:215],a[36:39],v[44:47]
rt v[208:209],v240 offset:17024
rt v[210:211],v240 offset:18048
mm v[44:47],v[220:223],a[12:15],v[44:47]
ld v[136:139],v224,s[68:71],0 offen offset:0
mm v[44:47],v[216:219],a[4:7],v[44:47]
rt v[212:213],v240 offset:25472
rt v[214:215],v240 offset:26496
mm v[12:15],v[216:219],a[0:3],v[12:15]
wt lgkmcnt(4)
v_lshlrev_b32 v250, 9, v250
mm v[12:15],v[220:223],a[48:51],v[12:15]
rt v[216:217],v240 offset:33920
rt v[218:219],v240 offset:34944
mm v[16:19],v[192:195],a[64:67],v[16:19]
cv v234,v40,v41
cv v236,v44,v45
mm v[16:19],v[196:199],a[72:75],v[16:19]
rt v[220:221],v240 offset:42368
rt v[222:223],v240 offset:43392
mm v[48:51],v[196:199],a[76:79],v[48:51]
cv v235,v42,v43
cv v237,v46,v47
mm v[48:51],v[192:195],a[68:71],v[48:51]
rt v[196:197],v241 offset:50816
rt v[198:199],v241 offset:51840
mm v[48:51],v[200:203],a[84:87],v[48:51]
v_permlane16_swap_b32 v234, v236
v_permlane16_swap_b32 v235, v237
mm v[48:51],v[204:207],a[92:95],v[48:51]
rt v[192:193],v241 offset:59264
rt v[194:195],v241 offset:60288
mm v[16:19],v[204:207],a[88:91],v[16:19]
wt lgkmcnt(4)
v_and_b32 v248, 0x1f0, v255
mm v[16:19],v[200:203],a[80:83],v[16:19]
rt v[204:205],v241 offset:128
rt v[206:207],v241 offset:1152
mm v[16:19],v[208:211],a[32:35],v[16:19]
st v[234:237],v238,s[32:35],s95 offen offset:1024 sc1
mm v[16:19],v[212:215],a[40:43],v[16:19]
rt v[200:201],v241 offset:8576
rt v[202:203],v241 offset:9600
mm v[48:51],v[212:215],a[44:47],v[48:51]
cv v230,v8,v9
cv v232,v12,v13
mm v[48:51],v[208:211],a[36:39],v[48:51]
rt v[212:213],v241 offset:17024
rt v[214:215],v241 offset:18048
mm v[48:51],v[216:219],a[12:15],v[48:51]
cv v231,v10,v11
cv v233,v14,v15
mm v[48:51],v[220:223],a[4:7],v[48:51]
rt v[208:209],v241 offset:25472
rt v[210:211],v241 offset:26496
mm v[16:19],v[220:223],a[0:3],v[16:19]
wt lgkmcnt(4)
v_lshrrev_b32 v248, 1, v248
mm v[16:19],v[216:219],a[48:51],v[16:19]
rt v[220:221],v241 offset:33920
rt v[222:223],v241 offset:34944
mm v[20:23],v[196:199],a[64:67],v[20:23]
v_permlane16_swap_b32 v230, v232
v_permlane16_swap_b32 v231, v233
mm v[20:23],v[192:195],a[72:75],v[20:23]
rt v[216:217],v241 offset:42368
rt v[218:219],v241 offset:43392
mm v[52:55],v[192:195],a[76:79],v[52:55]
mm v[52:55],v[196:199],a[68:71],v[52:55]
rt v[192:193],v242 offset:50816
rt v[194:195],v242 offset:51840
mm v[52:55],v[204:207],a[84:87],v[52:55]
st v[230:233],v238,s[32:35],s94 offen offset:1024 sc1
mm v[52:55],v[200:203],a[92:95],v[52:55]
rt v[196:197],v242 offset:59264
rt v[198:199],v242 offset:60288
mm v[20:23],v[200:203],a[88:91],v[20:23]
wt lgkmcnt(4)
mm v[20:23],v[204:207],a[80:83],v[20:23]
rt v[200:201],v242 offset:128
rt v[202:203],v242 offset:1152
mm v[20:23],v[212:215],a[32:35],v[20:23]
ld v[140:143],v225,s[48:51],0 offen offset:0
mm v[20:23],v[208:211],a[40:43],v[20:23]
rt v[204:205],v242 offset:8576
rt v[206:207],v242 offset:9600
mm v[52:55],v[208:211],a[44:47],v[52:55]
mm v[52:55],v[212:215],a[36:39],v[52:55]
rt v[208:209],v242 offset:17024
rt v[210:211],v242 offset:18048
mm v[52:55],v[220:223],a[12:15],v[52:55]
ld v[152:155],v224,s[68:71],0 offen offset:512
mm v[52:55],v[216:219],a[4:7],v[52:55]
rt v[212:213],v242 offset:25472
rt v[214:215],v242 offset:26496
mm v[20:23],v[216:219],a[0:3],v[20:23]
wt lgkmcnt(4)
s_mov_b32 s93, 0
ld a[224:227],v229,s[28:31],s93 offen offset:0
mm v[20:23],v[220:223],a[48:51],v[20:23]
rt v[216:217],v242 offset:33920
rt v[218:219],v242 offset:34944
mm v[24:27],v[192:195],a[64:67],v[24:27]
cv v234,v48,v49
cv v236,v52,v53
mm v[24:27],v[196:199],a[72:75],v[24:27]
rt v[220:221],v242 offset:42368
rt v[222:223],v242 offset:43392
mm v[56:59],v[196:199],a[76:79],v[56:59]
cv v235,v50,v51
cv v237,v54,v55
mm v[56:59],v[192:195],a[68:71],v[56:59]
rt v[196:197],v243 offset:50816
rt v[198:199],v243 offset:51840
mm v[56:59],v[200:203],a[84:87],v[56:59]
v_permlane16_swap_b32 v234, v236
v_permlane16_swap_b32 v235, v237
mm v[56:59],v[204:207],a[92:95],v[56:59]
rt v[192:193],v243 offset:59264
rt v[194:195],v243 offset:60288
mm v[24:27],v[204:207],a[88:91],v[24:27]
wt lgkmcnt(4)
ld a[228:231],v229,s[28:31],s93 offen offset:2048
mm v[24:27],v[200:203],a[80:83],v[24:27]
rt v[204:205],v243 offset:128
rt v[206:207],v243 offset:1152
mm v[24:27],v[208:211],a[32:35],v[24:27]
st v[234:237],v238,s[32:35],s95 offen offset:2048 sc1
mm v[24:27],v[212:215],a[40:43],v[24:27]
rt v[200:201],v243 offset:8576
rt v[202:203],v243 offset:9600
mm v[56:59],v[212:215],a[44:47],v[56:59]
cv v230,v16,v17
cv v232,v20,v21
mm v[56:59],v[208:211],a[36:39],v[56:59]
rt v[212:213],v243 offset:17024
rt v[214:215],v243 offset:18048
mm v[56:59],v[216:219],a[12:15],v[56:59]
cv v231,v18,v19
cv v233,v22,v23
mm v[56:59],v[220:223],a[4:7],v[56:59]
rt v[208:209],v243 offset:25472
rt v[210:211],v243 offset:26496
mm v[24:27],v[220:223],a[0:3],v[24:27]
wt lgkmcnt(4)
s_mov_b32 s93, 4096
ld a[232:235],v229,s[28:31],s93 offen offset:0
mm v[24:27],v[216:219],a[48:51],v[24:27]
rt v[220:221],v243 offset:33920
rt v[222:223],v243 offset:34944
mm v[28:31],v[196:199],a[64:67],v[28:31]
v_permlane16_swap_b32 v230, v232
v_permlane16_swap_b32 v231, v233
mm v[28:31],v[192:195],a[72:75],v[28:31]
rt v[216:217],v243 offset:42368
rt v[218:219],v243 offset:43392
mm v[60:63],v[192:195],a[76:79],v[60:63]
ld a[236:239],v229,s[28:31],s93 offen offset:2048
mm v[60:63],v[196:199],a[68:71],v[60:63]
rt v[192:193],v240 offset:50688
rt v[194:195],v240 offset:51712
mm v[60:63],v[204:207],a[84:87],v[60:63]
st v[230:233],v238,s[32:35],s94 offen offset:2048 sc1
mm v[60:63],v[200:203],a[92:95],v[60:63]
rt v[196:197],v240 offset:59136
rt v[198:199],v240 offset:60160
mm v[28:31],v[200:203],a[88:91],v[28:31]
wt lgkmcnt(4)
s_mov_b32 s93, 8192
ld a[240:243],v229,s[28:31],s93 offen offset:0
mm v[28:31],v[204:207],a[80:83],v[28:31]
rt v[200:201],v240 offset:0
rt v[202:203],v240 offset:1024
mm v[28:31],v[212:215],a[32:35],v[28:31]
ld v[156:159],v225,s[48:51],0 offen offset:512
mm v[28:31],v[208:211],a[40:43],v[28:31]
rt v[204:205],v240 offset:8448
rt v[206:207],v240 offset:9472
mm v[60:63],v[208:211],a[44:47],v[60:63]
ld a[244:247],v229,s[28:31],s93 offen offset:2048
mm v[60:63],v[212:215],a[36:39],v[60:63]
rt v[208:209],v240 offset:16896
rt v[210:211],v240 offset:17920
mm v[60:63],v[220:223],a[12:15],v[60:63]
ld v[168:171],v224,s[68:71],0 offen offset:1024
mm v[60:63],v[216:219],a[4:7],v[60:63]
rt v[212:213],v240 offset:25344
rt v[214:215],v240 offset:26368
mm v[28:31],v[216:219],a[0:3],v[28:31]
wt vmcnt(25) lgkmcnt(4)
s_mov_b32 s93, 12288
ld a[248:251],v229,s[28:31],s93 offen offset:0
mm v[28:31],v[220:223],a[48:51],v[28:31]
rt v[216:217],v240 offset:33792
rt v[218:219],v240 offset:34816
mm v[64:67],v[192:195],a[8:11],v[64:67]
cv v234,v56,v57
cv v236,v60,v61
mm v[64:67],v[196:199],a[52:55],v[64:67]
rt v[220:221],v240 offset:42240
rt v[222:223],v240 offset:43264
mm v[96:99],v[196:199],a[56:59],v[96:99]
cv v235,v58,v59
cv v237,v62,v63
mm v[96:99],v[192:195],a[16:19],v[96:99]
rt v[196:197],v241 offset:50688
rt v[198:199],v241 offset:51712
mm v[96:99],v[200:203],v[128:131],v[96:99]
v_permlane16_swap_b32 v234, v236
v_permlane16_swap_b32 v235, v237
mm v[96:99],v[204:207],a[20:23],v[96:99]
rt v[192:193],v241 offset:59136
rt v[194:195],v241 offset:60160
mm v[64:67],v[204:207],v[132:135],v[64:67]
wt vmcnt(18) lgkmcnt(4)
ld a[252:255],v229,s[28:31],s93 offen offset:2048
mm v[64:67],v[200:203],a[60:63],v[64:67]
rt v[204:205],v241 offset:0
rt v[206:207],v241 offset:1024
mm v[64:67],v[208:211],a[24:27],v[64:67]
st v[234:237],v238,s[32:35],s95 offen offset:3072 sc1
mm v[64:67],v[212:215],v[144:147],v[64:67]
rt v[200:201],v241 offset:8448
rt v[202:203],v241 offset:9472
mm v[96:99],v[212:215],v[148:151],v[96:99]
cv v230,v24,v25
cv v232,v28,v29
mm v[96:99],v[208:211],a[28:31],v[96:99]
rt v[212:213],v241 offset:16896
rt v[214:215],v241 offset:17920
mm v[96:99],v[216:219],v[164:167],v[96:99]
cv v231,v26,v27
cv v233,v30,v31
mm v[96:99],v[220:223],v[180:183],v[96:99]
rt v[208:209],v241 offset:25344
rt v[210:211],v241 offset:26368
mm v[64:67],v[220:223],v[176:179],v[64:67]
wt lgkmcnt(4)
s_mov_b32 s93, 0
ld a[64:67],v229,s[28:31],s93 offen offset:256
mm v[64:67],v[216:219],v[160:163],v[64:67]
rt v[220:221],v241 offset:33792
rt v[222:223],v241 offset:34816
mm v[68:71],v[196:199],a[8:11],v[68:71]
v_permlane16_swap_b32 v230, v232
v_permlane16_swap_b32 v231, v233
mm v[68:71],v[192:195],a[52:55],v[68:71]
rt v[216:217],v241 offset:42240
rt v[218:219],v241 offset:43264
mm v[100:103],v[192:195],a[56:59],v[100:103]
ld a[68:71],v229,s[28:31],s93 offen offset:2304
mm v[100:103],v[196:199],a[16:19],v[100:103]
rt v[192:193],v242 offset:50688
rt v[194:195],v242 offset:51712
mm v[100:103],v[204:207],v[128:131],v[100:103]
st v[230:233],v238,s[32:35],s94 offen offset:3072 sc1
mm v[100:103],v[200:203],a[20:23],v[100:103]
rt v[196:197],v242 offset:59136
rt v[198:199],v242 offset:60160
mm v[68:71],v[200:203],v[132:135],v[68:71]
wt lgkmcnt(4)
s_mov_b32 s93, 4096
ld a[72:75],v229,s[28:31],s93 offen offset:256
mm v[68:71],v[204:207],a[60:63],v[68:71]
rt v[200:201],v242 offset:0
rt v[202:203],v242 offset:1024
mm v[68:71],v[212:215],a[24:27],v[68:71]
ld v[172:175],v225,s[48:51],0 offen offset:1024
mm v[68:71],v[208:211],v[144:147],v[68:71]
rt v[204:205],v242 offset:8448
rt v[206:207],v242 offset:9472
mm v[100:103],v[208:211],v[148:151],v[100:103]
ld a[76:79],v229,s[28:31],s93 offen offset:2304
mm v[100:103],v[212:215],a[28:31],v[100:103]
rt v[208:209],v242 offset:16896
rt v[210:211],v242 offset:17920
mm v[100:103],v[220:223],v[164:167],v[100:103]
ld v[184:187],v224,s[68:71],0 offen offset:1536
mm v[100:103],v[216:219],v[180:183],v[100:103]
rt v[212:213],v242 offset:25344
rt v[214:215],v242 offset:26368
mm v[68:71],v[216:219],v[176:179],v[68:71]
wt lgkmcnt(4)
s_mov_b32 s93, 8192
.La5_LW3_4:
s_cbranch_execz .La5_LA_RETURN
ld a[80:83],v229,s[28:31],s93 offen offset:256
mm v[68:71],v[220:223],v[160:163],v[68:71]
rt v[216:217],v242 offset:33792
rt v[218:219],v242 offset:34816
mm v[72:75],v[192:195],a[8:11],v[72:75]
cv v234,v96,v97
cv v236,v100,v101
mm v[72:75],v[196:199],a[52:55],v[72:75]
rt v[220:221],v242 offset:42240
rt v[222:223],v242 offset:43264
mm v[104:107],v[196:199],a[56:59],v[104:107]
cv v235,v98,v99
cv v237,v102,v103
mm v[104:107],v[192:195],a[16:19],v[104:107]
rt v[196:197],v243 offset:50688
rt v[198:199],v243 offset:51712
mm v[104:107],v[200:203],v[128:131],v[104:107]
v_permlane16_swap_b32 v234, v236
v_permlane16_swap_b32 v235, v237
mm v[104:107],v[204:207],a[20:23],v[104:107]
rt v[192:193],v243 offset:59136
rt v[194:195],v243 offset:60160
mm v[72:75],v[204:207],v[132:135],v[72:75]
wt lgkmcnt(4)
ld a[84:87],v229,s[28:31],s93 offen offset:2304
mm v[72:75],v[200:203],a[60:63],v[72:75]
rt v[204:205],v243 offset:0
rt v[206:207],v243 offset:1024
mm v[72:75],v[208:211],a[24:27],v[72:75]
st v[234:237],v238,s[32:35],s97 offen offset:0 sc1
mm v[72:75],v[212:215],v[144:147],v[72:75]
rt v[200:201],v243 offset:8448
rt v[202:203],v243 offset:9472
mm v[104:107],v[212:215],v[148:151],v[104:107]
cv v230,v64,v65
cv v232,v68,v69
mm v[104:107],v[208:211],a[28:31],v[104:107]
rt v[212:213],v243 offset:16896
rt v[214:215],v243 offset:17920
mm v[104:107],v[216:219],v[164:167],v[104:107]
cv v231,v66,v67
cv v233,v70,v71
mm v[104:107],v[220:223],v[180:183],v[104:107]
rt v[208:209],v243 offset:25344
rt v[210:211],v243 offset:26368
mm v[72:75],v[220:223],v[176:179],v[72:75]
wt lgkmcnt(4)
s_mov_b32 s93, 12288
ld a[88:91],v229,s[28:31],s93 offen offset:256
mm v[72:75],v[216:219],v[160:163],v[72:75]
rt v[220:221],v243 offset:33792
rt v[222:223],v243 offset:34816
mm v[76:79],v[196:199],a[8:11],v[76:79]
v_permlane16_swap_b32 v230, v232
v_permlane16_swap_b32 v231, v233
mm v[76:79],v[192:195],a[52:55],v[76:79]
rt v[216:217],v243 offset:42240
rt v[218:219],v243 offset:43264
mm v[108:111],v[192:195],a[56:59],v[108:111]
ld a[92:95],v229,s[28:31],s93 offen offset:2304
mm v[108:111],v[196:199],a[16:19],v[108:111]
rt v[192:193],v240 offset:50816
rt v[194:195],v240 offset:51840
mm v[108:111],v[204:207],v[128:131],v[108:111]
st v[230:233],v238,s[32:35],s96 offen offset:0 sc1
mm v[108:111],v[200:203],a[20:23],v[108:111]
rt v[196:197],v240 offset:59264
rt v[198:199],v240 offset:60288
mm v[76:79],v[200:203],v[132:135],v[76:79]
wt lgkmcnt(4)
mm v[76:79],v[204:207],a[60:63],v[76:79]
rt v[200:201],v240 offset:128
rt v[202:203],v240 offset:1152
mm v[76:79],v[212:215],a[24:27],v[76:79]
ld v[188:191],v225,s[48:51],0 offen offset:1536
mm v[76:79],v[208:211],v[144:147],v[76:79]
rt v[204:205],v240 offset:8576
rt v[206:207],v240 offset:9600
mm v[108:111],v[208:211],v[148:151],v[108:111]
mm v[108:111],v[212:215],a[28:31],v[108:111]
rt v[208:209],v240 offset:17024
rt v[210:211],v240 offset:18048
mm v[108:111],v[220:223],v[164:167],v[108:111]
mm v[108:111],v[216:219],v[180:183],v[108:111]
rt v[212:213],v240 offset:25472
rt v[214:215],v240 offset:26496
mm v[76:79],v[216:219],v[176:179],v[76:79]
wt lgkmcnt(4)
mm v[76:79],v[220:223],v[160:163],v[76:79]
rt v[216:217],v240 offset:33920
rt v[218:219],v240 offset:34944
mm v[80:83],v[192:195],a[8:11],v[80:83]
cv v234,v104,v105
cv v236,v108,v109
mm v[80:83],v[196:199],a[52:55],v[80:83]
rt v[220:221],v240 offset:42368
rt v[222:223],v240 offset:43392
mm v[112:115],v[196:199],a[56:59],v[112:115]
cv v235,v106,v107
cv v237,v110,v111
mm v[112:115],v[192:195],a[16:19],v[112:115]
rt v[196:197],v241 offset:50816
rt v[198:199],v241 offset:51840
mm v[112:115],v[200:203],v[128:131],v[112:115]
v_permlane16_swap_b32 v234, v236
v_permlane16_swap_b32 v235, v237
mm v[112:115],v[204:207],a[20:23],v[112:115]
rt v[192:193],v241 offset:59264
rt v[194:195],v241 offset:60288
mm v[80:83],v[204:207],v[132:135],v[80:83]
wt lgkmcnt(4)
mm v[80:83],v[200:203],a[60:63],v[80:83]
rt v[204:205],v241 offset:128
rt v[206:207],v241 offset:1152
mm v[80:83],v[208:211],a[24:27],v[80:83]
st v[234:237],v238,s[32:35],s97 offen offset:1024 sc1
mm v[80:83],v[212:215],v[144:147],v[80:83]
rt v[200:201],v241 offset:8576
rt v[202:203],v241 offset:9600
mm v[112:115],v[212:215],v[148:151],v[112:115]
cv v230,v72,v73
cv v232,v76,v77
mm v[112:115],v[208:211],a[28:31],v[112:115]
rt v[212:213],v241 offset:17024
rt v[214:215],v241 offset:18048
mm v[112:115],v[216:219],v[164:167],v[112:115]
cv v231,v74,v75
cv v233,v78,v79
mm v[112:115],v[220:223],v[180:183],v[112:115]
rt v[208:209],v241 offset:25472
rt v[210:211],v241 offset:26496
mm v[80:83],v[220:223],v[176:179],v[80:83]
wt lgkmcnt(4)
mm v[80:83],v[216:219],v[160:163],v[80:83]
rt v[220:221],v241 offset:33920
rt v[222:223],v241 offset:34944
mm v[84:87],v[196:199],a[8:11],v[84:87]
v_permlane16_swap_b32 v230, v232
v_permlane16_swap_b32 v231, v233
mm v[84:87],v[192:195],a[52:55],v[84:87]
rt v[216:217],v241 offset:42368
rt v[218:219],v241 offset:43392
mm v[116:119],v[192:195],a[56:59],v[116:119]
mm v[116:119],v[196:199],a[16:19],v[116:119]
rt v[192:193],v242 offset:50816
rt v[194:195],v242 offset:51840
mm v[116:119],v[204:207],v[128:131],v[116:119]
st v[230:233],v238,s[32:35],s96 offen offset:1024 sc1
mm v[116:119],v[200:203],a[20:23],v[116:119]
rt v[196:197],v242 offset:59264
rt v[198:199],v242 offset:60288
mm v[84:87],v[200:203],v[132:135],v[84:87]
wt lgkmcnt(4)
mm v[84:87],v[204:207],a[60:63],v[84:87]
rt v[200:201],v242 offset:128
rt v[202:203],v242 offset:1152
mm v[84:87],v[212:215],a[24:27],v[84:87]
mm v[84:87],v[208:211],v[144:147],v[84:87]
rt v[204:205],v242 offset:8576
rt v[206:207],v242 offset:9600
mm v[116:119],v[208:211],v[148:151],v[116:119]
mm v[116:119],v[212:215],a[28:31],v[116:119]
rt v[208:209],v242 offset:17024
rt v[210:211],v242 offset:18048
mm v[116:119],v[220:223],v[164:167],v[116:119]
mm v[116:119],v[216:219],v[180:183],v[116:119]
rt v[212:213],v242 offset:25472
rt v[214:215],v242 offset:26496
mm v[84:87],v[216:219],v[176:179],v[84:87]
wt lgkmcnt(4)
mm v[84:87],v[220:223],v[160:163],v[84:87]
rt v[216:217],v242 offset:33920
rt v[218:219],v242 offset:34944
mm v[88:91],v[192:195],a[8:11],v[88:91]
cv v234,v112,v113
cv v236,v116,v117
mm v[88:91],v[196:199],a[52:55],v[88:91]
rt v[220:221],v242 offset:42368
rt v[222:223],v242 offset:43392
mm v[120:123],v[196:199],a[56:59],v[120:123]
cv v235,v114,v115
cv v237,v118,v119
mm v[120:123],v[192:195],a[16:19],v[120:123]
rt v[196:197],v243 offset:50816
rt v[198:199],v243 offset:51840
mm v[120:123],v[200:203],v[128:131],v[120:123]
v_permlane16_swap_b32 v234, v236
v_permlane16_swap_b32 v235, v237
mm v[120:123],v[204:207],a[20:23],v[120:123]
rt v[192:193],v243 offset:59264
rt v[194:195],v243 offset:60288
mm v[88:91],v[204:207],v[132:135],v[88:91]
wt lgkmcnt(4)
mm v[88:91],v[200:203],a[60:63],v[88:91]
rt v[204:205],v243 offset:128
rt v[206:207],v243 offset:1152
mm v[88:91],v[208:211],a[24:27],v[88:91]
st v[234:237],v238,s[32:35],s97 offen offset:2048 sc1
mm v[88:91],v[212:215],v[144:147],v[88:91]
rt v[200:201],v243 offset:8576
rt v[202:203],v243 offset:9600
mm v[120:123],v[212:215],v[148:151],v[120:123]
cv v230,v80,v81
cv v232,v84,v85
mm v[120:123],v[208:211],a[28:31],v[120:123]
rt v[212:213],v243 offset:17024
rt v[214:215],v243 offset:18048
mm v[120:123],v[216:219],v[164:167],v[120:123]
cv v231,v82,v83
cv v233,v86,v87
mm v[120:123],v[220:223],v[180:183],v[120:123]
rt v[208:209],v243 offset:25472
rt v[210:211],v243 offset:26496
mm v[88:91],v[220:223],v[176:179],v[88:91]
wt lgkmcnt(4)
mm v[88:91],v[216:219],v[160:163],v[88:91]
rt v[220:221],v243 offset:33920
rt v[222:223],v243 offset:34944
mm v[92:95],v[196:199],a[8:11],v[92:95]
v_permlane16_swap_b32 v230, v232
v_permlane16_swap_b32 v231, v233
mm v[92:95],v[192:195],a[52:55],v[92:95]
rt v[216:217],v243 offset:42368
rt v[218:219],v243 offset:43392
mm v[124:127],v[192:195],a[56:59],v[124:127]
mm v[124:127],v[196:199],a[16:19],v[124:127]
mm v[124:127],v[204:207],v[128:131],v[124:127]
st v[230:233],v238,s[32:35],s96 offen offset:2048 sc1
mm v[124:127],v[200:203],a[20:23],v[124:127]
mm v[92:95],v[200:203],v[132:135],v[92:95]
wt lgkmcnt(0)
mm v[92:95],v[204:207],a[60:63],v[92:95]
mm v[92:95],v[212:215],a[24:27],v[92:95]
mm v[92:95],v[208:211],v[144:147],v[92:95]
mm v[124:127],v[208:211],v[148:151],v[124:127]
mm v[124:127],v[212:215],a[28:31],v[124:127]
mm v[124:127],v[220:223],v[164:167],v[124:127]
mm v[124:127],v[216:219],v[180:183],v[124:127]
mm v[92:95],v[216:219],v[176:179],v[92:95]
mm v[92:95],v[220:223],v[160:163],v[92:95]
s_nop 7
cv v234,v120,v121
cv v236,v124,v125
cv v235,v122,v123
cv v237,v126,v127
cv v230,v88,v89
cv v232,v92,v93
cv v231,v90,v91
cv v233,v94,v95
v_permlane16_swap_b32 v234, v236
v_permlane16_swap_b32 v235, v237
st v[234:237],v238,s[32:35],s97 offen offset:3072 sc1
v_permlane16_swap_b32 v230, v232
v_permlane16_swap_b32 v231, v233
st v[230:233],v238,s[32:35],s96 offen offset:3072 sc1
.La5_LT_0:
.La5_LW3_5:
s_cbranch_execz .La5_LA_RETURN
s_lshl_b32 s91, s76, 5
s_add_u32 s91, s91, s72
s_lshl_b32 s20, s91, 9
s_add_u32 s20, s20, 2097152
s_lshr_b32 s93, s91, 1
s_lshl_b32 s93, s93, 15
s_and_b32 s94, s91, 1
s_lshl2_add_u32 s94, s94, s3
s_lshl_b32 s94, s94, 7
s_add_u32 s97, s93, s94
s_add_u32 s32, s26, 71303168
s_addc_u32 s33, s27, 0
s_and_b32 s33, s33, 0xffff
s_mov_b32 s34, 16777216
s_mov_b32 s35, 0x20000
s_add_u32 s44, s26, 171966464
s_addc_u32 s45, s27, 0
s_and_b32 s45, s45, 0xffff
s_mov_b32 s46, 950272
s_mov_b32 s47, 0x20000
s_add_u32 s56, s38, 0
s_addc_u32 s57, s39, 0
s_and_b32 s57, s57, 0xffff
s_mov_b32 s58, 4194304
s_mov_b32 s59, 0x20000
s_add_u32 s68, s16, 0
s_addc_u32 s69, s17, 0
s_and_b32 s69, s69, 0xffff
s_mov_b32 s70, 4194304
s_mov_b32 s71, 0x20000
v_and_b32 v230, 0x70, v255
v_lshrrev_b32 v231, 7, v255
v_lshl_add_u32 v230, v231, 10, v230
v_add_u32 v240, s97, v230
v_add_u32 v241, 8192, v240
v_add_u32 v242, 16384, v240
v_add_u32 v243, 24576, v240
v_mov_b32 v237, 0x3f803f80
v_mov_b32 v236, 0
wt vmcnt(6)
v_dot2_f32_bf16 v236, v140, v237, v236
v_dot2_f32_bf16 v236, v141, v237, v236
v_dot2_f32_bf16 v236, v142, v237, v236
v_dot2_f32_bf16 v236, v143, v237, v236
v_dot2_f32_bf16 v236, v156, v237, v236
v_dot2_f32_bf16 v236, v157, v237, v236
v_dot2_f32_bf16 v236, v158, v237, v236
v_dot2_f32_bf16 v236, v159, v237, v236
v_dot2_f32_bf16 v236, v172, v237, v236
v_dot2_f32_bf16 v236, v173, v237, v236
v_dot2_f32_bf16 v236, v174, v237, v236
v_dot2_f32_bf16 v236, v175, v237, v236
v_dot2_f32_bf16 v236, v188, v237, v236
v_dot2_f32_bf16 v236, v189, v237, v236
v_dot2_f32_bf16 v236, v190, v237, v236
v_dot2_f32_bf16 v236, v191, v237, v236
v_lshlrev_b32 v0, 16, v136
v_and_b32 v1, 0xffff0000, v136
v_lshlrev_b32 v2, 16, v137
v_and_b32 v3, 0xffff0000, v137
v_lshlrev_b32 v4, 16, v138
v_and_b32 v5, 0xffff0000, v138
v_lshlrev_b32 v6, 16, v139
v_and_b32 v7, 0xffff0000, v139
v_lshlrev_b32 v8, 16, v152
v_and_b32 v9, 0xffff0000, v152
v_lshlrev_b32 v10, 16, v153
v_and_b32 v11, 0xffff0000, v153
v_lshlrev_b32 v12, 16, v154
v_and_b32 v13, 0xffff0000, v154
v_lshlrev_b32 v14, 16, v155
v_and_b32 v15, 0xffff0000, v155
v_lshlrev_b32 v16, 16, v168
v_and_b32 v17, 0xffff0000, v168
v_lshlrev_b32 v18, 16, v169
v_and_b32 v19, 0xffff0000, v169
v_lshlrev_b32 v20, 16, v170
v_and_b32 v21, 0xffff0000, v170
v_lshlrev_b32 v22, 16, v171
v_and_b32 v23, 0xffff0000, v171
v_lshlrev_b32 v24, 16, v184
v_and_b32 v25, 0xffff0000, v184
v_lshlrev_b32 v26, 16, v185
v_and_b32 v27, 0xffff0000, v185
v_lshlrev_b32 v28, 16, v186
v_and_b32 v29, 0xffff0000, v186
v_lshlrev_b32 v30, 16, v187
v_and_b32 v31, 0xffff0000, v187
s_nop 1
v_add_f32_dpp v236, v236, v236 quad_perm:[1,0,3,2] row_mask:0xf bank_mask:0xf
s_nop 1
v_add_f32_dpp v236, v236, v236 quad_perm:[2,3,0,1] row_mask:0xf bank_mask:0xf
s_nop 1
v_add_f32_dpp v236, v236, v236 row_ror:8 row_mask:0xf bank_mask:0xf
v_mov_b32 v230, v236
s_nop 1
v_permlane16_swap_b32 v230, v236
s_nop 1
v_add_f32 v236, v236, v230
v_mov_b32 v230, v236
s_nop 1
v_permlane32_swap_b32 v230, v236
s_nop 1
v_add_f32 v236, v236, v230
s_mov_b64 exec, 0
s_cmp_eq_u32 s3, 0
s_cbranch_scc1 .La5_LW4_0
s_cmp_eq_u32 s3, 1
s_cbranch_scc1 .La5_LW4_1
s_cmp_eq_u32 s3, 2
s_cbranch_scc1 .La5_LW4_2
s_branch .La5_LW4_3
.La5_LW4_R0:
s_mov_b64 exec, -1
wt vmcnt(0)
buffer_store_dwordx2 v[252:253], v250, s[64:67], s92 offen sc1
s_mov_b32 s85, 99
s_cmp_eq_u32 s30, 0
s_cbranch_scc1 .La5_LT_ROWS
; ---- h = x + attn, LayerNorm statistics, router score, two best of eight rows
.La5_LT_ROWS:
s_mov_b32 s96, 0
.La5_LT_P3:
buffer_load_dwordx2 v[250:251], v248, s[64:67], s20 offen sc1
wt vmcnt(0)
.La5_LT_P2:
v_cmp_eq_u64 vcc, v[250:251], v[252:253]
s_add_u32 s96, s96, 1
s_lshr_b32 s92, vcc_lo, 1
s_and_b32 s92, s92, vcc_lo
s_lshr_b32 s93, s92, 2
s_and_b32 s92, s92, s93
s_lshr_b32 s93, s92, 4
s_and_b32 s92, s92, s93
s_lshr_b32 s93, s96, 20
s_mul_i32 s93, s93, 0x01010101
s_or_b32 s92, s92, s93
s_andn2_b32 s92, 0x01010101, s92
s_bcnt1_i32_b32 s93, s92
s_cmp_gt_u32 s93, 1
s_cbranch_scc1 .La5_LT_P3
s_or_b32 s93, s92, 0x01000000
s_ff1_i32_b32 s93, s93
s_lshl_b32 s80, s93, 19
s_xor_b32 s93, s80, 8388608
s_xor_b32 s94, s80, 12582912
s_xor_b32 s95, s80, 4194304
ld v[208:211],v240,s[32:35],s93 offen sc1
ld v[212:215],v241,s[32:35],s93 offen sc1
ld v[216:219],v242,s[32:35],s93 offen sc1
ld v[220:223],v243,s[32:35],s93 offen sc1
ld v[112:115],v240,s[32:35],s94 offen sc1
ld v[116:119],v241,s[32:35],s94 offen sc1
ld v[120:123],v242,s[32:35],s94 offen sc1
ld v[124:127],v243,s[32:35],s94 offen sc1
ld v[96:99],v240,s[32:35],s95 offen sc1
ld v[100:103],v241,s[32:35],s95 offen sc1
ld v[104:107],v242,s[32:35],s95 offen sc1
ld v[108:111],v243,s[32:35],s95 offen sc1
s_cmp_lg_u32 s92, 0
s_cbranch_scc1 .La5_LT_P4
ld v[192:195],v240,s[32:35],s80 offen sc1
ld v[196:199],v241,s[32:35],s80 offen sc1
ld v[200:203],v242,s[32:35],s80 offen sc1
ld v[204:207],v243,s[32:35],s80 offen sc1
.La5_LT_P4:
buffer_load_dwordx2 v[250:251], v248, s[64:67], s20 offen sc1
wt vmcnt(8)
v_lshlrev_b32 v32, 16, v208
v_and_b32 v33, 0xffff0000, v208
v_lshlrev_b32 v230, 16, v112
v_and_b32 v231, 0xffff0000, v112
v_pk_add_f32 v[32:33], v[32:33], v[230:231]
v_lshlrev_b32 v34, 16, v209
v_and_b32 v35, 0xffff0000, v209
v_lshlrev_b32 v230, 16, v113
v_and_b32 v231, 0xffff0000, v113
v_pk_add_f32 v[34:35], v[34:35], v[230:231]
v_lshlrev_b32 v36, 16, v210
v_and_b32 v37, 0xffff0000, v210
v_lshlrev_b32 v230, 16, v114
v_and_b32 v231, 0xffff0000, v114
v_pk_add_f32 v[36:37], v[36:37], v[230:231]
v_lshlrev_b32 v38, 16, v211
v_and_b32 v39, 0xffff0000, v211
v_lshlrev_b32 v230, 16, v115
v_and_b32 v231, 0xffff0000, v115
v_pk_add_f32 v[38:39], v[38:39], v[230:231]
wt vmcnt(7)
v_lshlrev_b32 v40, 16, v212
v_and_b32 v41, 0xffff0000, v212
v_lshlrev_b32 v230, 16, v116
v_and_b32 v231, 0xffff0000, v116
v_pk_add_f32 v[40:41], v[40:41], v[230:231]
v_lshlrev_b32 v42, 16, v213
v_and_b32 v43, 0xffff0000, v213
v_lshlrev_b32 v230, 16, v117
v_and_b32 v231, 0xffff0000, v117
v_pk_add_f32 v[42:43], v[42:43], v[230:231]
v_lshlrev_b32 v44, 16, v214
v_and_b32 v45, 0xffff0000, v214
v_lshlrev_b32 v230, 16, v118
v_and_b32 v231, 0xffff0000, v118
v_pk_add_f32 v[44:45], v[44:45], v[230:231]
v_lshlrev_b32 v46, 16, v215
v_and_b32 v47, 0xffff0000, v215
v_lshlrev_b32 v230, 16, v119
v_and_b32 v231, 0xffff0000, v119
v_pk_add_f32 v[46:47], v[46:47], v[230:231]
wt vmcnt(6)
v_lshlrev_b32 v48, 16, v216
v_and_b32 v49, 0xffff0000, v216
v_lshlrev_b32 v230, 16, v120
v_and_b32 v231, 0xffff0000, v120
v_pk_add_f32 v[48:49], v[48:49], v[230:231]
v_lshlrev_b32 v50, 16, v217
v_and_b32 v51, 0xffff0000, v217
v_lshlrev_b32 v230, 16, v121
v_and_b32 v231, 0xffff0000, v121
v_pk_add_f32 v[50:51], v[50:51], v[230:231]
v_lshlrev_b32 v52, 16, v218
v_and_b32 v53, 0xffff0000, v218
v_lshlrev_b32 v230, 16, v122
v_and_b32 v231, 0xffff0000, v122
v_pk_add_f32 v[52:53], v[52:53], v[230:231]
v_lshlrev_b32 v54, 16, v219
v_and_b32 v55, 0xffff0000, v219
v_lshlrev_b32 v230, 16, v123
v_and_b32 v231, 0xffff0000, v123
v_pk_add_f32 v[54:55], v[54:55], v[230:231]
wt vmcnt(5)
v_lshlrev_b32 v56, 16, v220
v_and_b32 v57, 0xffff0000, v220
v_lshlrev_b32 v230, 16, v124
v_and_b32 v231, 0xffff0000, v124
v_pk_add_f32 v[56:57], v[56:57], v[230:231]
v_lshlrev_b32 v58, 16, v221
v_and_b32 v59, 0xffff0000, v221
v_lshlrev_b32 v230, 16, v125
v_and_b32 v231, 0xffff0000, v125
v_pk_add_f32 v[58:59], v[58:59], v[230:231]
v_lshlrev_b32 v60, 16, v222
v_and_b32 v61, 0xffff0000, v222
v_lshlrev_b32 v230, 16, v126
v_and_b32 v231, 0xffff0000, v126
v_pk_add_f32 v[60:61], v[60:61], v[230:231]
v_lshlrev_b32 v62, 16, v223
v_and_b32 v63, 0xffff0000, v223
v_lshlrev_b32 v230, 16, v127
v_and_b32 v231, 0xffff0000, v127
v_pk_add_f32 v[62:63], v[62:63], v[230:231]
wt vmcnt(1)
v_lshlrev_b32 v64, 16, v96
v_and_b32 v65, 0xffff0000, v96
v_lshlrev_b32 v66, 16, v97
v_and_b32 v67, 0xffff0000, v97
v_lshlrev_b32 v68, 16, v98
v_and_b32 v69, 0xffff0000, v98
v_lshlrev_b32 v70, 16, v99
v_and_b32 v71, 0xffff0000, v99
v_lshlrev_b32 v72, 16, v100
v_and_b32 v73, 0xffff0000, v100
v_lshlrev_b32 v74, 16, v101
v_and_b32 v75, 0xffff0000, v101
v_lshlrev_b32 v76, 16, v102
v_and_b32 v77, 0xffff0000, v102
v_lshlrev_b32 v78, 16, v103
v_and_b32 v79, 0xffff0000, v103
v_lshlrev_b32 v80, 16, v104
v_and_b32 v81, 0xffff0000, v104
v_lshlrev_b32 v82, 16, v105
v_and_b32 v83, 0xffff0000, v105
v_lshlrev_b32 v84, 16, v106
v_and_b32 v85, 0xffff0000, v106
v_lshlrev_b32 v86, 16, v107
v_and_b32 v87, 0xffff0000, v107
v_lshlrev_b32 v88, 16, v108
v_and_b32 v89, 0xffff0000, v108
v_lshlrev_b32 v90, 16, v109
v_and_b32 v91, 0xffff0000, v109
v_lshlrev_b32 v92, 16, v110
v_and_b32 v93, 0xffff0000, v110
v_lshlrev_b32 v94, 16, v111
v_and_b32 v95, 0xffff0000, v111
v_mov_b32 v232, 0
v_mov_b32 v233, 0
v_mov_b32 v234, 0
s_cmp_eq_u32 s92, 0
s_cbranch_scc1 .La5_LT_SUM
.La5_LT_P5:
wt vmcnt(0)
v_cmp_eq_u64 vcc, v[250:251], v[252:253]
s_add_u32 s96, s96, 1
s_lshr_b32 s92, vcc_lo, 1
s_and_b32 s92, s92, vcc_lo
s_lshr_b32 s93, s92, 2
s_and_b32 s92, s92, s93
s_lshr_b32 s93, s92, 4
s_and_b32 s92, s92, s93
s_lshr_b32 s93, s96, 20
s_mul_i32 s93, s93, 0x01010101
s_or_b32 s92, s92, s93
s_andn2_b32 s92, 0x01010101, s92
s_cbranch_scc0 .La5_LT_P6
buffer_load_dwordx2 v[250:251], v248, s[64:67], s20 offen sc1
s_branch .La5_LT_P5
.La5_LT_P6:
ld v[192:195],v240,s[32:35],s80 offen sc1
ld v[196:199],v241,s[32:35],s80 offen sc1
ld v[200:203],v242,s[32:35],s80 offen sc1
ld v[204:207],v243,s[32:35],s80 offen sc1
.La5_LT_SUM:
wt vmcnt(3)
v_lshlrev_b32 v208, 16, v192
v_and_b32 v209, 0xffff0000, v192
v_lshlrev_b32 v210, 16, v193
v_and_b32 v211, 0xffff0000, v193
v_lshlrev_b32 v212, 16, v194
v_and_b32 v213, 0xffff0000, v194
v_lshlrev_b32 v214, 16, v195
v_and_b32 v215, 0xffff0000, v195
v_pk_add_f32 v[208:209], v[208:209], v[64:65]
v_pk_add_f32 v[210:211], v[210:211], v[66:67]
v_pk_add_f32 v[212:213], v[212:213], v[68:69]
v_pk_add_f32 v[214:215], v[214:215], v[70:71]
v_pk_add_f32 v[208:209], v[208:209], v[32:33]
v_pk_add_f32 v[210:211], v[210:211], v[34:35]
v_pk_add_f32 v[212:213], v[212:213], v[36:37]
v_pk_add_f32 v[214:215], v[214:215], v[38:39]
v_pk_add_f32 v[208:209], v[208:209], v[0:1]
v_pk_add_f32 v[210:211], v[210:211], v[2:3]
v_pk_add_f32 v[212:213], v[212:213], v[4:5]
v_pk_add_f32 v[214:215], v[214:215], v[6:7]
cv v96,v208,v209
cv v97,v210,v211
cv v98,v212,v213
cv v99,v214,v215
v_dot2_f32_bf16 v232, v96, v237, v232
v_dot2_f32_bf16 v233, v96, v96, v233
v_dot2_f32_bf16 v234, v96, v140, v234
v_dot2_f32_bf16 v232, v97, v237, v232
v_dot2_f32_bf16 v233, v97, v97, v233
v_dot2_f32_bf16 v234, v97, v141, v234
v_dot2_f32_bf16 v232, v98, v237, v232
v_dot2_f32_bf16 v233, v98, v98, v233
v_dot2_f32_bf16 v234, v98, v142, v234
v_dot2_f32_bf16 v232, v99, v237, v232
v_dot2_f32_bf16 v233, v99, v99, v233
v_dot2_f32_bf16 v234, v99, v143, v234
wt vmcnt(2)
v_lshlrev_b32 v208, 16, v196
v_and_b32 v209, 0xffff0000, v196
v_lshlrev_b32 v210, 16, v197
v_and_b32 v211, 0xffff0000, v197
v_lshlrev_b32 v212, 16, v198
v_and_b32 v213, 0xffff0000, v198
v_lshlrev_b32 v214, 16, v199
v_and_b32 v215, 0xffff0000, v199
v_pk_add_f32 v[208:209], v[208:209], v[72:73]
.La5_LW4_0:
s_cbranch_execz .La5_LW4_4
v_pk_add_f32 v[210:211], v[210:211], v[74:75]
v_pk_add_f32 v[212:213], v[212:213], v[76:77]
v_pk_add_f32 v[214:215], v[214:215], v[78:79]
v_pk_add_f32 v[208:209], v[208:209], v[40:41]
v_pk_add_f32 v[210:211], v[210:211], v[42:43]
v_pk_add_f32 v[212:213], v[212:213], v[44:45]
v_pk_add_f32 v[214:215], v[214:215], v[46:47]
v_pk_add_f32 v[208:209], v[208:209], v[8:9]
v_pk_add_f32 v[210:211], v[210:211], v[10:11]
v_pk_add_f32 v[212:213], v[212:213], v[12:13]
v_pk_add_f32 v[214:215], v[214:215], v[14:15]
cv v100,v208,v209
cv v101,v210,v211
cv v102,v212,v213
cv v103,v214,v215
v_dot2_f32_bf16 v232, v100, v237, v232
v_dot2_f32_bf16 v233, v100, v100, v233
v_dot2_f32_bf16 v234, v100, v156, v234
v_dot2_f32_bf16 v232, v101, v237, v232
v_dot2_f32_bf16 v233, v101, v101, v233
v_dot2_f32_bf16 v234, v101, v157, v234
v_dot2_f32_bf16 v232, v102, v237, v232
v_dot2_f32_bf16 v233, v102, v102, v233
v_dot2_f32_bf16 v234, v102, v158, v234
v_dot2_f32_bf16 v232, v103, v237, v232
v_dot2_f32_bf16 v233, v103, v103, v233
v_dot2_f32_bf16 v234, v103, v159, v234
wt vmcnt(1)
v_lshlrev_b32 v208, 16, v200
v_and_b32 v209, 0xffff0000, v200
v_lshlrev_b32 v210, 16, v201
v_and_b32 v211, 0xffff0000, v201
v_lshlrev_b32 v212, 16, v202
v_and_b32 v213, 0xffff0000, v202
v_lshlrev_b32 v214, 16, v203
v_and_b32 v215, 0xffff0000, v203
v_pk_add_f32 v[208:209], v[208:209], v[80:81]
v_pk_add_f32 v[210:211], v[210:211], v[82:83]
v_pk_add_f32 v[212:213], v[212:213], v[84:85]
v_pk_add_f32 v[214:215], v[214:215], v[86:87]
v_pk_add_f32 v[208:209], v[208:209], v[48:49]
v_pk_add_f32 v[210:211], v[210:211], v[50:51]
v_pk_add_f32 v[212:213], v[212:213], v[52:53]
v_pk_add_f32 v[214:215], v[214:215], v[54:55]
v_pk_add_f32 v[208:209], v[208:209], v[16:17]
v_pk_add_f32 v[210:211], v[210:211], v[18:19]
v_pk_add_f32 v[212:213], v[212:213], v[20:21]
v_pk_add_f32 v[214:215], v[214:215], v[22:23]
cv v104,v208,v209
cv v105,v210,v211
cv v106,v212,v213
cv v107,v214,v215
v_dot2_f32_bf16 v232, v104, v237, v232
v_dot2_f32_bf16 v233, v104, v104, v233
v_dot2_f32_bf16 v234, v104, v172, v234
v_dot2_f32_bf16 v232, v105, v237, v232
v_dot2_f32_bf16 v233, v105, v105, v233
v_dot2_f32_bf16 v234, v105, v173, v234
v_dot2_f32_bf16 v232, v106, v237, v232
v_dot2_f32_bf16 v233, v106, v106, v233
v_dot2_f32_bf16 v234, v106, v174, v234
v_dot2_f32_bf16 v232, v107, v237, v232
v_dot2_f32_bf16 v233, v107, v107, v233
v_dot2_f32_bf16 v234, v107, v175, v234
wt vmcnt(0)
v_lshlrev_b32 v208, 16, v204
v_and_b32 v209, 0xffff0000, v204
v_lshlrev_b32 v210, 16, v205
v_and_b32 v211, 0xffff0000, v205
v_lshlrev_b32 v212, 16, v206
v_and_b32 v213, 0xffff0000, v206
v_lshlrev_b32 v214, 16, v207
v_and_b32 v215, 0xffff0000, v207
v_pk_add_f32 v[208:209], v[208:209], v[88:89]
v_pk_add_f32 v[210:211], v[210:211], v[90:91]
v_pk_add_f32 v[212:213], v[212:213], v[92:93]
v_pk_add_f32 v[214:215], v[214:215], v[94:95]
v_pk_add_f32 v[208:209], v[208:209], v[56:57]
v_pk_add_f32 v[210:211], v[210:211], v[58:59]
v_pk_add_f32 v[212:213], v[212:213], v[60:61]
v_pk_add_f32 v[214:215], v[214:215], v[62:63]
v_pk_add_f32 v[208:209], v[208:209], v[24:25]
v_pk_add_f32 v[210:211], v[210:211], v[26:27]
v_pk_add_f32 v[212:213], v[212:213], v[28:29]
v_pk_add_f32 v[214:215], v[214:215], v[30:31]
cv v108,v208,v209
cv v109,v210,v211
cv v110,v212,v213
cv v111,v214,v215
v_dot2_f32_bf16 v232, v108, v237, v232
v_dot2_f32_bf16 v233, v108, v108, v233
v_dot2_f32_bf16 v234, v108, v188, v234
v_dot2_f32_bf16 v232, v109, v237, v232
v_dot2_f32_bf16 v233, v109, v109, v233
v_dot2_f32_bf16 v234, v109, v189, v234
v_dot2_f32_bf16 v232, v110, v237, v232
v_dot2_f32_bf16 v233, v110, v110, v233
v_dot2_f32_bf16 v234, v110, v190, v234
v_dot2_f32_bf16 v232, v111, v237, v232
v_dot2_f32_bf16 v233, v111, v111, v233
v_dot2_f32_bf16 v234, v111, v191, v234
wt vmcnt(0)
s_lshl2_add_u32 s92, s91, s3
s_lshl_b32 s92, s92, 1
s_sub_u32 s92, 0xffff, s92
v_add_f32_dpp v232, v232, v232 quad_perm:[1,0,3,2] row_mask:0xf bank_mask:0xf
v_add_f32_dpp v233, v233, v233 quad_perm:[1,0,3,2] row_mask:0xf bank_mask:0xf
v_add_f32_dpp v234, v234, v234 quad_perm:[1,0,3,2] row_mask:0xf bank_mask:0xf
v_add_f32_dpp v232, v232, v232 quad_perm:[2,3,0,1] row_mask:0xf bank_mask:0xf
v_add_f32_dpp v233, v233, v233 quad_perm:[2,3,0,1] row_mask:0xf bank_mask:0xf
v_add_f32_dpp v234, v234, v234 quad_perm:[2,3,0,1] row_mask:0xf bank_mask:0xf
v_add_f32_dpp v232, v232, v232 row_ror:8 row_mask:0xf bank_mask:0xf
v_add_f32_dpp v233, v233, v233 row_ror:8 row_mask:0xf bank_mask:0xf
v_add_f32_dpp v234, v234, v234 row_ror:8 row_mask:0xf bank_mask:0xf
v_mov_b32 v230, v232
v_mov_b32 v231, v233
v_mov_b32 v235, v234
v_permlane16_swap_b32 v230, v232
v_permlane16_swap_b32 v231, v233
v_permlane16_swap_b32 v235, v234
v_add_f32 v232, v232, v230
v_add_f32 v233, v233, v231
v_add_f32 v234, v234, v235
v_mov_b32 v230, v232
v_mov_b32 v231, v233
v_mov_b32 v235, v234
v_permlane32_swap_b32 v230, v232
v_permlane32_swap_b32 v231, v233
v_permlane32_swap_b32 v235, v234
v_add_f32 v232, v232, v230
v_add_f32 v233, v233, v231
v_add_f32_e64 v234, v234, v235
v_mul_f32 v244, 0x3a800000, v232
v_mul_f32 v233, 0x3a800000, v233
v_fma_f32 v230, -v244, v244, v233
v_mul_f32 v233, 0x3a800000, v233
v_cmp_nge_f32 vcc, v230, v233
v_bfe_u32 v235, v255, 6, 1
v_sub_u32_e64 v235, s92, v235
s_cbranch_vccnz .La5_LT_VAR
.La5_LT_VOK:
cv v244,v244,v244
v_lshlrev_b32 v244, 16, v244
cv v230,v230,v230
v_lshlrev_b32 v230, 16, v230
v_fma_f32 v234, -v244, v236, v234
v_add_f32 v230, 0x3727c5ac, v230
cv v230,v230,v230
v_lshlrev_b32 v230, 16, v230
v_sqrt_f32 v230, v230
v_lshrrev_b32 v232, 3, v255
cv v230,v230,v230
v_lshlrev_b32 v231, 16, v230
v_rcp_f32 v246, v231
v_lshrrev_b32 v230, 16, v230
v_or_b32 v233, v244, v230
v_mul_f32 v234, v234, v246
s_lshl_b32 s93, s3, 4
cv v234,v234,v234
v_and_b32 v230, 8, v232
v_ashrrev_i32 v232, 31, v234
v_or_b32 v232, 0x80000000, v232
v_xor_b32 v232, v232, v234
v_and_b32 v232, 0xffff0000, v232
v_add_u32 v230, s93, v230
v_or_b32 v232, v232, v235
ds_write_b64 v230, v[232:233] offset:0
v_lshrrev_b32 v230, 1, v255
v_and_b32 v230, 56, v230
wt lgkmcnt(0)
s_barrier
s_cmp_lg_u32 s3, 0
s_cbranch_scc1 .La5_LT_OUT
ds_read_b64 v[232:233], v230 offset:0
s_lshl3_add_u32 s92, s72, s76
s_lshl_b32 s92, s92, 4
v_lshrrev_b32 v235, 7, v255
v_lshlrev_b32 v235, 12, v235
wt lgkmcnt(0)
v_mov_b32 v234, v232
s_nop 1
v_max_u32_dpp v234, v234, v234 quad_perm:[1,0,3,2] row_mask:0xf bank_mask:0xf
s_nop 1
v_max_u32_dpp v234, v234, v234 quad_perm:[2,3,0,1] row_mask:0xf bank_mask:0xf
s_nop 1
v_max_u32_dpp v234, v234, v234 row_half_mirror row_mask:0xf bank_mask:0xf
s_nop 1
v_cmp_eq_u32 vcc, v232, v234
v_add_u32 v231, 8, v235
s_nop 0
v_cndmask_b32 v234, v232, 0, vcc
v_cndmask_b32 v235, v231, v235, vcc
s_nop 0
v_max_u32_dpp v234, v234, v234 quad_perm:[1,0,3,2] row_mask:0xf bank_mask:0xf
s_nop 1
v_max_u32_dpp v234, v234, v234 quad_perm:[2,3,0,1] row_mask:0xf bank_mask:0xf
s_nop 1
v_max_u32_dpp v234, v234, v234 row_half_mirror row_mask:0xf bank_mask:0xf
s_nop 1
v_cmp_ge_u32 vcc, v232, v234
s_nop 1
s_mov_b64 exec, vcc
buffer_store_dwordx2 v[232:233], v235, s[44:47], s92 offen sc1
s_mov_b64 exec, -1
; ---- out = h + .La5_LN(h) of the own rows; FFN weights into MFMA order
.La5_LT_OUT:
v_mul_f32 v244, v244, v246
v_mov_b32 v247, v246
v_xor_b32 v244, 0x80000000, v244
v_mov_b32 v245, v244
v_lshlrev_b32 v230, 16, v96
v_and_b32 v231, 0xffff0000, v96
v_pk_fma_f32 v[232:233], v[230:231], v[246:247], v[244:245]
cv v232,v232,v233
v_lshlrev_b32 v234, 16, v232
v_and_b32 v235, 0xffff0000, v232
v_pk_add_f32 v[230:231], v[230:231], v[234:235]
cv v192,v230,v231
v_lshlrev_b32 v230, 16, v97
v_and_b32 v231, 0xffff0000, v97
v_pk_fma_f32 v[232:233], v[230:231], v[246:247], v[244:245]
cv v232,v232,v233
v_lshlrev_b32 v234, 16, v232
v_and_b32 v235, 0xffff0000, v232
v_pk_add_f32 v[230:231], v[230:231], v[234:235]
cv v193,v230,v231
v_lshlrev_b32 v230, 16, v98
v_and_b32 v231, 0xffff0000, v98
v_pk_fma_f32 v[232:233], v[230:231], v[246:247], v[244:245]
cv v232,v232,v233
v_lshlrev_b32 v234, 16, v232
v_and_b32 v235, 0xffff0000, v232
v_pk_add_f32 v[230:231], v[230:231], v[234:235]
cv v194,v230,v231
v_lshlrev_b32 v230, 16, v99
v_and_b32 v231, 0xffff0000, v99
v_pk_fma_f32 v[232:233], v[230:231], v[246:247], v[244:245]
cv v232,v232,v233
v_lshlrev_b32 v234, 16, v232
v_and_b32 v235, 0xffff0000, v232
v_pk_add_f32 v[230:231], v[230:231], v[234:235]
cv v195,v230,v231
v_lshlrev_b32 v230, 16, v100
v_and_b32 v231, 0xffff0000, v100
v_pk_fma_f32 v[232:233], v[230:231], v[246:247], v[244:245]
cv v232,v232,v233
v_lshlrev_b32 v234, 16, v232
v_and_b32 v235, 0xffff0000, v232
v_pk_add_f32 v[230:231], v[230:231], v[234:235]
cv v196,v230,v231
v_lshlrev_b32 v230, 16, v101
v_and_b32 v231, 0xffff0000, v101
v_pk_fma_f32 v[232:233], v[230:231], v[246:247], v[244:245]
cv v232,v232,v233
v_lshlrev_b32 v234, 16, v232
v_and_b32 v235, 0xffff0000, v232
v_pk_add_f32 v[230:231], v[230:231], v[234:235]
cv v197,v230,v231
v_lshlrev_b32 v230, 16, v102
v_and_b32 v231, 0xffff0000, v102
v_pk_fma_f32 v[232:233], v[230:231], v[246:247], v[244:245]
cv v232,v232,v233
v_lshlrev_b32 v234, 16, v232
v_and_b32 v235, 0xffff0000, v232
v_pk_add_f32 v[230:231], v[230:231], v[234:235]
cv v198,v230,v231
v_lshlrev_b32 v230, 16, v103
v_and_b32 v231, 0xffff0000, v103
v_pk_fma_f32 v[232:233], v[230:231], v[246:247], v[244:245]
cv v232,v232,v233
v_lshlrev_b32 v234, 16, v232
v_and_b32 v235, 0xffff0000, v232
v_pk_add_f32 v[230:231], v[230:231], v[234:235]
cv v199,v230,v231
v_lshlrev_b32 v230, 16, v104
v_and_b32 v231, 0xffff0000, v104
v_pk_fma_f32 v[232:233], v[230:231], v[246:247], v[244:245]
cv v232,v232,v233
v_lshlrev_b32 v234, 16, v232
v_and_b32 v235, 0xffff0000, v232
v_pk_add_f32 v[230:231], v[230:231], v[234:235]
cv v200,v230,v231
v_lshlrev_b32 v230, 16, v105
v_and_b32 v231, 0xffff0000, v105
v_pk_fma_f32 v[232:233], v[230:231], v[246:247], v[244:245]
cv v232,v232,v233
v_lshlrev_b32 v234, 16, v232
v_and_b32 v235, 0xffff0000, v232
v_pk_add_f32 v[230:231], v[230:231], v[234:235]
cv v201,v230,v231
v_lshlrev_b32 v230, 16, v106
v_and_b32 v231, 0xffff0000, v106
v_pk_fma_f32 v[232:233], v[230:231], v[246:247], v[244:245]
cv v232,v232,v233
v_lshlrev_b32 v234, 16, v232
v_and_b32 v235, 0xffff0000, v232
v_pk_add_f32 v[230:231], v[230:231], v[234:235]
cv v202,v230,v231
v_lshlrev_b32 v230, 16, v107
v_and_b32 v231, 0xffff0000, v107
v_pk_fma_f32 v[232:233], v[230:231], v[246:247], v[244:245]
cv v232,v232,v233
v_lshlrev_b32 v234, 16, v232
v_and_b32 v235, 0xffff0000, v232
v_pk_add_f32 v[230:231], v[230:231], v[234:235]
cv v203,v230,v231
v_lshlrev_b32 v230, 16, v108
v_and_b32 v231, 0xffff0000, v108
v_pk_fma_f32 v[232:233], v[230:231], v[246:247], v[244:245]
cv v232,v232,v233
v_lshlrev_b32 v234, 16, v232
v_and_b32 v235, 0xffff0000, v232
v_pk_add_f32 v[230:231], v[230:231], v[234:235]
cv v204,v230,v231
v_lshlrev_b32 v230, 16, v109
v_and_b32 v231, 0xffff0000, v109
v_pk_fma_f32 v[232:233], v[230:231], v[246:247], v[244:245]
cv v232,v232,v233
v_lshlrev_b32 v234, 16, v232
v_and_b32 v235, 0xffff0000, v232
v_pk_add_f32 v[230:231], v[230:231], v[234:235]
cv v205,v230,v231
v_lshlrev_b32 v230, 16, v110
v_and_b32 v231, 0xffff0000, v110
v_pk_fma_f32 v[232:233], v[230:231], v[246:247], v[244:245]
cv v232,v232,v233
v_lshlrev_b32 v234, 16, v232
v_and_b32 v235, 0xffff0000, v232
v_pk_add_f32 v[230:231], v[230:231], v[234:235]
cv v206,v230,v231
v_lshlrev_b32 v230, 16, v111
v_and_b32 v231, 0xffff0000, v111
v_pk_fma_f32 v[232:233], v[230:231], v[246:247], v[244:245]
cv v232,v232,v233
v_lshlrev_b32 v234, 16, v232
v_and_b32 v235, 0xffff0000, v232
v_pk_add_f32 v[230:231], v[230:231], v[234:235]
cv v207,v230,v231
s_cmp_eq_u32 s30, 0
s_cbranch_scc1 .La5_LT_SEL
s_mov_b32 s94, 0x05040100
s_mov_b32 s95, 0x07060302
wt vmcnt(0)
ar v230,a224
ar v231,a228
v_perm_b32 v128, v231, v230, s94
v_perm_b32 v132, v231, v230, s95
ar v230,a232
ar v231,a236
v_perm_b32 v129, v231, v230, s94
v_perm_b32 v133, v231, v230, s95
ar v230,a240
ar v231,a244
v_perm_b32 v130, v231, v230, s94
v_perm_b32 v134, v231, v230, s95
ar v230,a248
ar v231,a252
v_perm_b32 v131, v231, v230, s94
v_perm_b32 v135, v231, v230, s95
ar v230,a225
ar v231,a229
v_perm_b32 v136, v231, v230, s94
v_perm_b32 v140, v231, v230, s95
ar v230,a233
ar v231,a237
v_perm_b32 v137, v231, v230, s94
v_perm_b32 v141, v231, v230, s95
ar v230,a241
ar v231,a245
v_perm_b32 v138, v231, v230, s94
v_perm_b32 v142, v231, v230, s95
ar v230,a249
ar v231,a253
v_perm_b32 v139, v231, v230, s94
v_perm_b32 v143, v231, v230, s95
ar v230,a226
ar v231,a230
v_perm_b32 v144, v231, v230, s94
v_perm_b32 v148, v231, v230, s95
ar v230,a234
ar v231,a238
v_perm_b32 v145, v231, v230, s94
v_perm_b32 v149, v231, v230, s95
ar v230,a242
ar v231,a246
v_perm_b32 v146, v231, v230, s94
v_perm_b32 v150, v231, v230, s95
ar v230,a250
ar v231,a254
v_perm_b32 v147, v231, v230, s94
v_perm_b32 v151, v231, v230, s95
ar v230,a227
ar v231,a231
v_perm_b32 v152, v231, v230, s94
v_perm_b32 v156, v231, v230, s95
ar v230,a235
ar v231,a239
v_perm_b32 v153, v231, v230, s94
v_perm_b32 v157, v231, v230, s95
ar v230,a243
ar v231,a247
v_perm_b32 v154, v231, v230, s94
v_perm_b32 v158, v231, v230, s95
ar v230,a251
ar v231,a255
v_perm_b32 v155, v231, v230, s94
v_perm_b32 v159, v231, v230, s95
ar v230,a64
ar v231,a68
v_perm_b32 v160, v231, v230, s94
v_perm_b32 v164, v231, v230, s95
ar v230,a72
ar v231,a76
v_perm_b32 v161, v231, v230, s94
v_perm_b32 v165, v231, v230, s95
ar v230,a80
ar v231,a84
v_perm_b32 v162, v231, v230, s94
v_perm_b32 v166, v231, v230, s95
ar v230,a88
ar v231,a92
v_perm_b32 v163, v231, v230, s94
v_perm_b32 v167, v231, v230, s95
ar v230,a65
ar v231,a69
v_perm_b32 v168, v231, v230, s94
v_perm_b32 v172, v231, v230, s95
ar v230,a73
ar v231,a77
v_perm_b32 v169, v231, v230, s94
v_perm_b32 v173, v231, v230, s95
ar v230,a81
ar v231,a85
v_perm_b32 v170, v231, v230, s94
v_perm_b32 v174, v231, v230, s95
ar v230,a89
ar v231,a93
v_perm_b32 v171, v231, v230, s94
v_perm_b32 v175, v231, v230, s95
ar v230,a66
ar v231,a70
v_perm_b32 v176, v231, v230, s94
v_perm_b32 v180, v231, v230, s95
ar v230,a74
ar v231,a78
v_perm_b32 v177, v231, v230, s94
v_perm_b32 v181, v231, v230, s95
ar v230,a82
ar v231,a86
v_perm_b32 v178, v231, v230, s94
v_perm_b32 v182, v231, v230, s95
ar v230,a90
ar v231,a94
v_perm_b32 v179, v231, v230, s94
v_perm_b32 v183, v231, v230, s95
ar v230,a67
ar v231,a71
v_perm_b32 v184, v231, v230, s94
v_perm_b32 v188, v231, v230, s95
ar v230,a75
ar v231,a79
v_perm_b32 v185, v231, v230, s94
v_perm_b32 v189, v231, v230, s95
ar v230,a83
ar v231,a87
v_perm_b32 v186, v231, v230, s94
v_perm_b32 v190, v231, v230, s95
ar v230,a91
ar v231,a95
v_perm_b32 v187, v231, v230, s94
v_perm_b32 v191, v231, v230, s95
s_and_b32 s92, s3, 1
s_lshr_b32 s93, s3, 1
v_lshrrev_b32 v230, 6, v255
v_and_b32 v231, 48, v255
v_lshl_add_u32 v240, v230, 10, v231
s_lshl_b32 s96, s92, 14
v_add_u32 v240, s96, v240
s_lshl_b32 s96, s92, 10
v_add_u32 v241, s96, v255
s_lshl_b32 s97, s3, 10
v_add_u32 v208, s97, v255
v_bfe_u32 v230, v255, 4, 1
v_bfe_u32 v231, v255, 6, 2
v_lshrrev_b32 v232, 8, v255
v_lshlrev_b32 v209, 11, v230
v_lshl_add_u32 v209, v231, 7, v209
v_lshl_add_u32 v209, v232, 5, v209
s_lshr_b32 s96, s96, 1
v_add_u32 v209, s96, v209
v_bfe_u32 v233, v255, 4, 2
v_mul_u32_u24 v210, 640, v233
v_lshl_add_u32 v210, v231, 3, v210
s_mul_i32 s96, s93, 2560
s_lshl_b32 s92, s92, 5
s_add_u32 s96, s96, s92
v_add_u32 v210, s96, v210
v_cmp_eq_u32 s[98:99], v231, v232
v_mul_u32_u24 v211, 5, v255
s_lshl_b32 s96, s3, 7
v_lshrrev_b32 v212, 3, v255
v_and_b32 v212, 62, v212
v_add_u32 v212, s96, v212
v_lshlrev_b32 v234, 6, v230
v_lshl_add_u32 v234, v232, 4, v234
v_add_u32 v234, s96, v234
v_mov_b32 v235, 1024
v_cmp_eq_u32 vcc, 0, v231
s_nop 1
v_cndmask_b32 v216, v235, v234, vcc
v_cmp_eq_u32 vcc, 1, v231
s_nop 1
v_cndmask_b32 v217, v235, v234, vcc
v_cmp_eq_u32 vcc, 2, v231
s_nop 1
v_cndmask_b32 v218, v235, v234, vcc
v_cmp_eq_u32 vcc, 3, v231
s_nop 1
v_cndmask_b32 v219, v235, v234, vcc
v_mov_b32 v220, 0
v_mov_b32 v221, 0
v_mov_b32_e64 v222, 0
v_mov_b32_e64 v223, 0
ds_write_b128 v235, v[220:223] offset:10240
s_lshl_b32 s92, s100, 13
s_add_u32 s80, s92, 32768
s_add_u32 s20, s80, 4096
; ---- top two rows of the sequence; SwiGLU slice of 32 hidden units
.La5_LT_SEL:
.La5_LW4_1:
s_cbranch_execz .La5_LW4_R0
s_and_b32 s97, s2, 7
s_lshl_b32 s97, s97, 12
s_cmp_lg_u32 s3, 0
s_cbranch_scc1 .La5_LT_S2
s_mov_b32 s96, 0
.La5_LT_S1:
s_cmp_lg_u32 s30, 0
s_nop 0
s_cbranch_scc1 .La5_LT_S0
s_sleep 24
.La5_LT_S0:
ld v[224:227],v255,s[44:47],s97 offen offset:0 sc1
ld v[232:235],v255,s[44:47],s97 offen offset:1024 sc1
ld v[244:247],v255,s[44:47],s97 offen offset:2048 sc1
ld v[248:251],v255,s[44:47],s97 offen offset:3072 sc1
s_add_u32 s96, s96, 1
s_lshr_b32 s92, s96, 20
wt vmcnt(3)
v_min3_u32 v228, v224, v225, v226
v_min_u32 v228, v227, v228
wt vmcnt(2)
v_min3_u32 v228, v232, v233, v228
v_min3_u32 v228, v234, v235, v228
wt vmcnt(1)
v_min3_u32 v228, v244, v245, v228
v_min3_u32 v228, v246, v247, v228
wt vmcnt(0)
v_min3_u32 v228, v248, v249, v228
v_min3_u32 v228, v250, v251, v228
v_cmp_ne_u32 vcc, 0, v228
v_max_u32 v229, v224, v232
v_max_u32 v230, v244, v248
s_cbranch_scc1 .La5_LT_S3
s_cmp_eq_u64 vcc, -1
s_cbranch_scc0 .La5_LT_S1
.La5_LT_S3:
v_max_u32 v228, v229, v230
v_min_u32 v229, v229, v230
v_mov_b32 v231, v228
v_min_u32 v224, v224, v232
v_min_u32 v244, v244, v248
v_max3_u32 v229, v229, v224, v244
v_max3_u32 v230, v226, v234, v246
v_max3_u32 v229, v229, v230, v250
s_nop 1
v_max_u32_dpp v231, v231, v231 row_shr:1 row_mask:0xf bank_mask:0xf
s_nop 1
v_max_u32_dpp v231, v231, v231 row_shr:2 row_mask:0xf bank_mask:0xf
s_nop 1
v_max_u32_dpp v231, v231, v231 row_shr:4 row_mask:0xf bank_mask:0xf
s_nop 1
v_max_u32_dpp v231, v231, v231 row_shr:8 row_mask:0xf bank_mask:0xf
s_nop 1
v_max_u32_dpp v231, v231, v231 row_bcast:15 row_mask:0xa bank_mask:0xf
s_nop 1
v_max_u32_dpp v231, v231, v231 row_bcast:31 row_mask:0xc bank_mask:0xf
s_nop 0
v_readlane_b32 s82, v231, 63
s_nop 1
v_cmp_eq_u32 vcc, s82, v228
v_mov_b32 v230, s82
v_mov_b32 v224, 0
v_cndmask_b32 v228, v228, v229, vcc
s_nop 1
v_max_u32_dpp v228, v228, v228 row_shr:1 row_mask:0xf bank_mask:0xf
s_nop 1
v_max_u32_dpp v228, v228, v228 row_shr:2 row_mask:0xf bank_mask:0xf
s_nop 1
v_max_u32_dpp v228, v228, v228 row_shr:4 row_mask:0xf bank_mask:0xf
s_nop 1
v_max_u32_dpp v228, v228, v228 row_shr:8 row_mask:0xf bank_mask:0xf
s_nop 1
v_max_u32_dpp v228, v228, v228 row_bcast:15 row_mask:0xa bank_mask:0xf
s_nop 1
v_max_u32_dpp v228, v228, v228 row_bcast:31 row_mask:0xc bank_mask:0xf
s_nop 0
v_readlane_b32 s81, v228, 63
s_nop 1
v_mov_b32 v231, s81
ds_write_b64 v224, v[230:231] offset:64
.La5_LT_S2:
v_mov_b32 v224, 0
wt lgkmcnt(0)
s_barrier
ds_read_b64 v[228:229], v224 offset:64
wt lgkmcnt(0)
v_readfirstlane_b32 s82, v228
v_readfirstlane_b32 s81, v229
s_nop 0
s_andn2_b32 s84, 0x7ff, s82
s_andn2_b32 s21, 0x7ff, s81
s_cmp_eq_u32 s30, 0
s_cbranch_scc1 .La5_LT_ST
s_cmp_lt_u32 s3, 2
s_cselect_b32 s91, s84, s21
s_cselect_b32 s85, s82, s81
s_lshr_b32 s92, s91, 4
s_lshl_b32 s92, s92, 15
s_and_b32 s93, s91, 15
.La5_LW4_2:
s_cbranch_execz .La5_LW4_R0
s_lshl_b32 s93, s93, 6
s_add_u32 s92, s92, s93
s_add_u32 s93, s92, 4194304
s_add_u32 s94, s92, 8388608
s_add_u32 s95, s92, 12582912
ld v[224:227],v240,s[32:35],s92 offen sc1
ld v[228:231],v240,s[32:35],s93 offen sc1
ld v[232:235],v240,s[32:35],s94 offen sc1
ld v[244:247],v240,s[32:35],s95 offen sc1
s_lshl_b32 s96, s91, 11
s_and_b32 s92, s91, 0xf8
s_lshl4_add_u32 s97, s92, s97
s_lshr_b32 s92, s91, 8
s_lshl4_add_u32 s97, s92, s97
ld v[248:251],v241,s[68:71],s96 offen
ld v[220:223],off,s[44:47],s97 sc1
wt vmcnt(4)
v_lshlrev_b32 v236, 16, v224
v_and_b32 v237, 0xffff0000, v224
v_lshlrev_b32 v238, 16, v228
v_and_b32 v239, 0xffff0000, v228
v_pk_add_f32 v[236:237], v[236:237], v[238:239]
v_lshlrev_b32 v242, 16, v225
v_and_b32 v243, 0xffff0000, v225
v_lshlrev_b32 v238, 16, v229
v_and_b32 v239, 0xffff0000, v229
v_pk_add_f32 v[242:243], v[242:243], v[238:239]
v_lshlrev_b32 v252, 16, v226
v_and_b32 v253, 0xffff0000, v226
v_lshlrev_b32 v238, 16, v230
v_and_b32 v239, 0xffff0000, v230
v_pk_add_f32 v[252:253], v[252:253], v[238:239]
v_lshlrev_b32 v214, 16, v227
v_and_b32 v215, 0xffff0000, v227
v_lshlrev_b32 v238, 16, v231
v_and_b32 v239, 0xffff0000, v231
v_pk_add_f32 v[214:215], v[214:215], v[238:239]
wt vmcnt(2)
v_lshlrev_b32 v224, 16, v232
v_and_b32 v225, 0xffff0000, v232
v_lshlrev_b32 v226, 16, v244
v_and_b32 v227, 0xffff0000, v244
v_pk_add_f32 v[224:225], v[224:225], v[226:227]
v_pk_add_f32 v[236:237], v[236:237], v[224:225]
v_lshlrev_b32 v224, 16, v233
v_and_b32 v225, 0xffff0000, v233
v_lshlrev_b32 v226, 16, v245
v_and_b32 v227, 0xffff0000, v245
v_pk_add_f32 v[224:225], v[224:225], v[226:227]
v_pk_add_f32 v[242:243], v[242:243], v[224:225]
v_lshlrev_b32 v224, 16, v234
v_and_b32 v225, 0xffff0000, v234
v_lshlrev_b32 v226, 16, v246
v_and_b32 v227, 0xffff0000, v246
v_pk_add_f32 v[224:225], v[224:225], v[226:227]
v_pk_add_f32 v[252:253], v[252:253], v[224:225]
v_lshlrev_b32 v224, 16, v235
v_and_b32 v225, 0xffff0000, v235
v_lshlrev_b32 v226, 16, v247
v_and_b32 v227, 0xffff0000, v247
v_pk_add_f32 v[224:225], v[224:225], v[226:227]
v_pk_add_f32 v[214:215], v[214:215], v[224:225]
wt vmcnt(0)
v_cmp_eq_u32 vcc, s85, v220
s_nop 1
v_cndmask_b32 v220, v223, v221, vcc
v_lshlrev_b32 v222, 16, v220
v_rcp_f32 v222, v222
v_and_b32 v220, 0xffff0000, v220
v_mul_f32 v220, v220, v222
v_mov_b32 v223, v222
v_xor_b32 v220, 0x80000000, v220
v_mov_b32 v221, v220
v_lshlrev_b32 v224, 16, v248
v_and_b32 v225, 0xffff0000, v248
v_pk_add_f32 v[236:237], v[236:237], v[224:225]
cv v224,v236,v237
v_lshlrev_b32 v226, 16, v224
v_and_b32 v227, 0xffff0000, v224
v_pk_fma_f32 v[226:227], v[226:227], v[222:223], v[220:221]
cv v228,v226,v227
v_lshlrev_b32 v224, 16, v249
v_and_b32 v225, 0xffff0000, v249
v_pk_add_f32 v[242:243], v[242:243], v[224:225]
cv v224,v242,v243
v_lshlrev_b32 v226, 16, v224
v_and_b32 v227, 0xffff0000, v224
v_pk_fma_f32 v[226:227], v[226:227], v[222:223], v[220:221]
cv v229,v226,v227
v_lshlrev_b32 v224, 16, v250
v_and_b32 v225, 0xffff0000, v250
v_pk_add_f32 v[252:253], v[252:253], v[224:225]
cv v224,v252,v253
v_lshlrev_b32 v226, 16, v224
v_and_b32 v227, 0xffff0000, v224
v_pk_fma_f32 v[226:227], v[226:227], v[222:223], v[220:221]
cv v230,v226,v227
v_lshlrev_b32 v224, 16, v251
v_and_b32 v225, 0xffff0000, v251
v_pk_add_f32 v[214:215], v[214:215], v[224:225]
cv v224,v214,v215
v_lshlrev_b32 v226, 16, v224
v_and_b32 v227, 0xffff0000, v224
v_pk_fma_f32 v[226:227], v[226:227], v[222:223], v[220:221]
cv v231,v226,v227
ds_write_b128 v208, v[228:231] offset:1024
wt lgkmcnt(0)
s_barrier
rd v[224:227],v209 offset:1024
rd v[232:235],v209 offset:1040
rd v[244:247],v209 offset:2048
rd v[248:251],v209 offset:2064
wt lgkmcnt(3)
mm a[0:3],v[224:227],a[96:99],0
mm a[4:7],v[224:227],a[100:103],0
mm a[8:11],v[224:227],a[104:107],0
mm a[12:15],v[224:227],a[108:111],0
mm a[16:19],v[224:227],a[112:115],0
mm a[20:23],v[224:227],a[116:119],0
mm a[24:27],v[224:227],a[120:123],0
mm a[28:31],v[224:227],a[124:127],0
wt lgkmcnt(2)
mm a[0:3],v[232:235],a[128:131],a[0:3]
mm a[4:7],v[232:235],a[132:135],a[4:7]
mm a[8:11],v[232:235],a[136:139],a[8:11]
mm a[12:15],v[232:235],a[140:143],a[12:15]
mm a[16:19],v[232:235],a[144:147],a[16:19]
mm a[20:23],v[232:235],a[148:151],a[20:23]
mm a[24:27],v[232:235],a[152:155],a[24:27]
mm a[28:31],v[232:235],a[156:159],a[28:31]
wt lgkmcnt(1)
mm a[0:3],v[244:247],a[160:163],a[0:3]
mm a[4:7],v[244:247],a[164:167],a[4:7]
mm a[8:11],v[244:247],a[168:171],a[8:11]
mm a[12:15],v[244:247],a[172:175],a[12:15]
mm a[16:19],v[244:247],a[176:179],a[16:19]
mm a[20:23],v[244:247],a[180:183],a[20:23]
mm a[24:27],v[244:247],a[184:187],a[24:27]
mm a[28:31],v[244:247],a[188:191],a[28:31]
wt lgkmcnt(0)
mm a[0:3],v[248:251],a[192:195],a[0:3]
mm a[4:7],v[248:251],a[196:199],a[4:7]
mm a[8:11],v[248:251],a[200:203],a[8:11]
mm a[12:15],v[248:251],a[204:207],a[12:15]
mm a[16:19],v[248:251],a[208:211],a[16:19]
mm a[20:23],v[248:251],a[212:215],a[20:23]
mm a[24:27],v[248:251],a[216:219],a[24:27]
mm a[28:31],v[248:251],a[220:223],a[28:31]
s_mov_b64 exec, s[98:99]
ds_write_b64 v210, a[0:1] offset:5120
ds_write_b64 v210, a[4:5] offset:5200
ds_write_b64 v210, a[8:9] offset:5280
ds_write_b64 v210, a[12:13] offset:5360
ds_write_b64 v210, a[16:17] offset:5440
s_nop 3
ds_write_b64 v210, a[20:21] offset:5520
s_nop 3
ds_write_b64 v210, a[24:25] offset:5600
s_nop 3
ds_write_b64 v210, a[28:29] offset:5680
s_mov_b64 exec, -1
wt lgkmcnt(0)
s_barrier
rd v[224:227],v211 offset:5120
rd v[232:235],v211 offset:5136
rd v[244:247],v211 offset:5152
rd v[248:251],v211 offset:5168
wt lgkmcnt(2)
v_pk_add_f32 v[224:225], v[224:225], v[226:227]
v_pk_add_f32 v[232:233], v[232:233], v[234:235]
wt lgkmcnt(0)
v_pk_add_f32 v[244:245], v[244:245], v[246:247]
v_pk_add_f32 v[248:249], v[248:249], v[250:251]
v_pk_add_f32 v[224:225], v[224:225], v[232:233]
v_pk_add_f32 v[244:245], v[244:245], v[248:249]
v_pk_add_f32 v[224:225], v[224:225], v[244:245]
cv v224,v224,v225
v_mov_b32 v226, v224
s_nop 1
v_permlane32_swap_b32 v224, v226
s_nop 1
v_lshlrev_b32 v228, 16, v224
v_and_b32 v229, 0xffff0000, v224
v_lshlrev_b32 v232, 16, v226
v_and_b32 v233, 0xffff0000, v226
v_mul_f32 v230, 0xbfb8aa3b, v228
v_mul_f32 v231, 0xbfb8aa3b, v229
ex v230,v230
ex v231,v231
v_add_f32 v230, 1.0, v230
v_add_f32 v231, 1.0, v231
v_rcp_f32 v230, v230
v_rcp_f32 v231, v231
s_nop 0
v_pk_mul_f32 v[228:229], v[228:229], v[230:231]
cv v228,v228,v229
v_lshlrev_b32 v230, 16, v228
v_and_b32 v231, 0xffff0000, v228
v_pk_mul_f32 v[230:231], v[230:231], v[232:233]
cv v228,v230,v231
ds_write_b16 v212, v228 offset:10240
ds_write_b16_d16_hi v212, v228 offset:10304
wt lgkmcnt(0)
rd v[224:227],v216 offset:10240
rd v[232:235],v217 offset:10240
rd v[244:247],v218 offset:10240
rd v[248:251],v219 offset:10240
wt lgkmcnt(3)
mm v[0:3],v[224:227],v[128:131],0
mm v[4:7],v[224:227],v[132:135],0
mm v[8:11],v[224:227],v[136:139],0
mm v[12:15],v[224:227],v[140:143],0
wt lgkmcnt(2)
mm v[0:3],v[232:235],v[144:147],v[0:3]
mm v[4:7],v[232:235],v[148:151],v[4:7]
mm v[8:11],v[232:235],v[152:155],v[8:11]
mm v[12:15],v[232:235],v[156:159],v[12:15]
wt lgkmcnt(1)
mm v[0:3],v[244:247],v[160:163],v[0:3]
mm v[4:7],v[244:247],v[164:167],v[4:7]
mm v[8:11],v[244:247],v[168:171],v[8:11]
mm v[12:15],v[244:247],v[172:175],v[12:15]
wt lgkmcnt(0)
mm v[0:3],v[248:251],v[176:179],v[0:3]
mm v[4:7],v[248:251],v[180:183],v[4:7]
mm v[8:11],v[248:251],v[184:187],v[8:11]
mm v[12:15],v[248:251],v[188:191],v[12:15]
s_nop 7
v_or_b32 v16, 1, v0
v_or_b32 v20, 1, v1
v_or_b32 v17, 1, v4
v_or_b32 v21, 1, v5
v_or_b32 v18, 1, v8
v_or_b32 v22, 1, v9
v_or_b32 v19, 1, v12
v_or_b32 v23, 1, v13
st v[16:19],v208,s[44:47],s80 offen sc1
st v[20:23],v208,s[44:47],s20 offen sc1
; ---- rows that were not routed leave; final sums of the routed rows
.La5_LT_ST:
.La5_LW4_3:
s_cbranch_execz .La5_LW4_R0
s_lshl_b32 s92, s76, 5
s_add_u32 s92, s92, s72
s_lshl2_add_u32 s92, s92, s3
s_lshl_b32 s93, s92, 1
v_bfe_u32 v230, v255, 6, 1
v_add_u32 v231, s93, v230
v_cmp_ne_u32 vcc, s84, v231
v_cmp_ne_u32 s[94:95], s21, v231
v_lshrrev_b32 v232, 7, v255
v_and_b32 v233, 48, v255
v_lshl_add_u32 v232, v232, 6, v233
v_lshl_add_u32 v232, v230, 11, v232
s_lshl_b32 s92, s92, 12
s_and_b64 exec, vcc, s[94:95]
s_cbranch_scc0 .La5_LT_F0
st v[192:195],v232,s[56:59],s92 offen offset:0 sc1
st v[196:199],v232,s[56:59],s92 offen offset:512 sc1
st v[200:203],v232,s[56:59],s92 offen offset:1024 sc1
st v[204:207],v232,s[56:59],s92 offen offset:1536 sc1
.La5_LT_F0:
s_mov_b64 exec, -1
s_cmp_eq_u32 s75, 1
s_nop 0
s_cbranch_scc1 .La5_LT_END
s_lshr_b32 s92, s2, 6
s_lshl2_add_u32 s92, s92, s74
s_and_b32 s93, s2, 7
s_lshl3_add_u32 s91, s92, s93
s_cmp_lt_u32 s91, 64
s_cselect_b32 s84, s84, s21
s_cselect_b32 s82, s82, s81
s_bfe_u32 s92, s91, 0x30003
s_and_b32 s93, s91, 3
s_lshl2_add_u32 s96, s92, s93
s_and_b32 s85, s91, 4
s_lshl_b32 s85, s85, 1
v_bfe_u32 v0, v255, 6, 2
v_bfe_u32 v1, v255, 4, 2
v_lshlrev_b32 v1, 1, v1
v_lshl_add_u32 v0, v0, 4, v1
v_add_u32 v0, s85, v0
s_lshl_b32 s85, s96, 10
v_add_u32 v1, s85, v0
s_lshl_b32 s85, s96, 6
v_add_u32 v0, s85, v0
s_lshr_b32 s92, s84, 4
s_lshl_b32 s92, s92, 15
s_and_b32 s93, s84, 15
s_lshl_b32 s93, s93, 6
s_add_u32 s92, s92, s93
s_add_u32 s93, s92, 4194304
s_add_u32 s94, s92, 8388608
s_add_u32 s95, s92, 12582912
buffer_load_ushort v4, v1, s[32:35], s92 offen sc1
buffer_load_ushort v5, v1, s[32:35], s93 offen sc1
buffer_load_ushort v6, v1, s[32:35], s94 offen sc1
buffer_load_ushort v7, v1, s[32:35], s95 offen sc1
s_lshl_b32 s21, s84, 11
s_and_b32 s92, s84, 0xf8
s_lshl4_add_u32 s97, s92, s97
s_lshr_b32 s92, s84, 8
s_lshl4_add_u32 s97, s92, s97
buffer_load_ushort v8, v0, s[68:71], s21 offen
ld v[12:15],off,s[44:47],s97 sc1
v_lshrrev_b32 v2, 6, v255
v_lshlrev_b32 v2, 13, v2
v_and_b32 v3, 48, v255
v_add_u32 v2, v2, v3
s_lshl_b32 s92, s3, 17
s_lshl_b32 s93, s91, 6
s_add_u32 s92, s92, s93
s_add_u32 s94, s92, 32768
s_cmp_eq_u32 s3, 3
s_cselect_b32 s95, 0, 524288
s_cselect_b32 s96, 0, -1
s_add_u32 s95, s95, s94
s_ashr_i32 s92, s82, 31
s_orn2_b32 s92, 0x80000000, s92
s_xor_b32 s92, s92, s82
s_and_b32 s92, s92, 0xffff0000
v_mov_b32 v9, 0xbfb8aa3b
v_mul_f32 v9, s92, v9
ex v9,v9
v_lshrrev_b32 v3, 8, v255
s_lshl_b32 s92, s3, 4
v_add_f32 v9, 1.0, v9
v_lshl_add_u32 v3, v3, 2, s92
v_rcp_f32 v9, v9
v_and_b32 v10, 48, v255
v_mul_u32_u24 v10, 20, v10
v_add_u32 v3, v3, v10
cv v9,v9,v9
v_lshlrev_b32 v9, 16, v9
v_and_b32 v10, 0xf0, v255
v_mul_u32_u24 v10, 5, v10
wt vmcnt(0)
v_cmp_eq_u32 vcc, s82, v12
s_nop 1
v_cndmask_b32 v12, v15, v13, vcc
v_lshlrev_b32 v14, 16, v12
v_rcp_f32 v14, v14
v_and_b32 v12, 0xffff0000, v12
v_mul_f32 v12, v12, v14
v_mov_b32 v15, v14
v_xor_b32 v12, 0x80000000, v12
v_mov_b32 v13, v12
v_lshlrev_b32 v4, 16, v4
v_lshlrev_b32 v5, 16, v5
v_lshlrev_b32 v6, 16, v6
v_lshlrev_b32 v7, 16, v7
v_lshlrev_b32 v8, 16, v8
v_add_f32 v4, v4, v5
v_add_f32 v6, v6, v7
v_add_f32 v4, v4, v6
v_add_f32 v4, v4, v8
cv v4,v4,v4
v_lshlrev_b32 v4, 16, v4
v_fma_f32 v5, v4, v14, v12
cv v5,v5,v5
v_lshlrev_b32 v5, 16, v5
s_sleep 40
s_mov_b32 s93, 0
.La5_LT_Y:
ld v[16:19],v2,s[44:47],s94 offen sc1
ld v[20:23],v2,s[44:47],s95 offen sc1
s_add_u32 s93, s93, 1
s_lshr_b32 s92, s93, 20
wt vmcnt(0)
v_min3_u32 v24, v16, v17, v18
v_min3_u32 v25, v20, v21, v22
v_min3_u32 v24, v24, v19, v23
v_min_u32 v24, v24, v25
v_cmp_ne_u32 vcc, 0, v24
v_and_b32 v20, s96, v20
v_and_b32 v21, s96, v21
v_and_b32 v22, s96, v22
v_and_b32 v23, s96, v23
s_cbranch_scc1 .La5_LT_Y1
s_cmp_eq_u64 vcc, -1
s_cbranch_scc0 .La5_LT_Y
.La5_LT_Y1:
v_pk_add_f32 v[16:17], v[16:17], v[20:21]
v_pk_add_f32 v[18:19], v[18:19], v[22:23]
s_nop 0
v_add_f32_dpp v16, v16, v16 row_ror:4 row_mask:0xf bank_mask:0xf
v_add_f32_dpp v17, v17, v17 row_ror:4 row_mask:0xf bank_mask:0xf
v_add_f32_dpp v18, v18, v18 row_ror:4 row_mask:0xf bank_mask:0xf
v_add_f32_dpp v19, v19, v19 row_ror:4 row_mask:0xf bank_mask:0xf
v_add_f32_dpp v16, v16, v16 row_ror:8 row_mask:0xf bank_mask:0xf
v_add_f32_dpp v17, v17, v17 row_ror:8 row_mask:0xf bank_mask:0xf
v_add_f32_dpp v18, v18, v18 row_ror:8 row_mask:0xf bank_mask:0xf
v_add_f32_dpp v19, v19, v19 row_ror:8 row_mask:0xf bank_mask:0xf
ds_write_b32 v3, v16 offset:5120
ds_write_b32 v3, v17 offset:5200
ds_write_b32 v3, v18 offset:5280
ds_write_b32 v3, v19 offset:5360
wt lgkmcnt(0)
s_barrier
rd v[16:19],v10 offset:5120
rd v[20:23],v10 offset:5136
rd v[24:27],v10 offset:5152
rd v[28:31],v10 offset:5168
wt lgkmcnt(2)
v_pk_add_f32 v[16:17], v[16:17], v[18:19]
v_pk_add_f32 v[20:21], v[20:21], v[22:23]
wt lgkmcnt(0)
v_pk_add_f32 v[24:25], v[24:25], v[26:27]
v_pk_add_f32 v[28:29], v[28:29], v[30:31]
v_pk_add_f32 v[16:17], v[16:17], v[20:21]
v_pk_add_f32 v[24:25], v[24:25], v[28:29]
v_pk_add_f32 v[16:17], v[16:17], v[24:25]
v_add_f32 v16, v16, v17
cv v16,v16,v16
v_lshlrev_b32 v16, 16, v16
v_mul_f32 v16, v16, v9
cv v16,v16,v16
v_lshlrev_b32 v16, 16, v16
v_add_f32 v16, v16, v5
cv v16,v16,v16
v_lshlrev_b32 v16, 16, v16
v_add_f32 v16, v16, v4
cv v16,v16,v16
s_cmp_lg_u32 s3, 0
s_cbranch_scc1 .La5_LT_END
s_mov_b64 exec, 0xffff
buffer_store_short v16, v0, s[56:59], s21 offen
s_mov_b64 exec, -1
.La5_LT_END:
.La5_LW4_4:
s_cbranch_execz .La5_LW4_R0
s_lshl2_add_u32 s92, s2, s3
s_cmp_lg_u32 s92, 992
s_cbranch_scc1 .La5_LT_E1
buffer_store_dwordx2 v[252:253], off, s[64:67], 0 sc1
.La5_LT_E1:
s_branch .La5_LEND
; ---- fallback: the earlier 32 rows against all their keys with the true running maximum (online softmax)
.La5_LA_SLOW0:
wt lgkmcnt(0)
s_nop 7
s_nop 7
aw a160,v192
aw a161,v193
aw a162,v194
aw a163,v195
aw a164,v196
aw a165,v197
aw a166,v198
aw a167,v199
aw a168,v200
aw a169,v201
aw a170,v202
aw a171,v203
aw a172,v204
aw a173,v205
aw a174,v206
aw a175,v207
aw a176,v208
aw a177,v209
aw a178,v210
aw a179,v211
aw a180,v212
aw a181,v213
aw a182,v214
aw a183,v215
aw a184,v216
aw a185,v217
aw a186,v218
aw a187,v219
aw a188,v220
aw a189,v221
aw a190,v222
aw a191,v223
s_lshl_b32 s96, s72, 19
s_add_u32 s96, s96, 20971520
s_add_u32 s92, s26, s96
s_addc_u32 s93, s27, 0
s_and_b32 s93, s93, 0xffff
s_mov_b32 s94, 17301504
s_mov_b32 s95, 0x20000
v_mov_b32 v0, 0
v_mov_b32 v1, 0
v_mov_b32 v2, 0
v_mov_b32 v3, 0
v_mov_b32 v4, 0
v_mov_b32 v5, 0
v_mov_b32 v6, 0
v_mov_b32 v7, 0
v_mov_b32 v8, 0
v_mov_b32 v9, 0
v_mov_b32 v10, 0
v_mov_b32 v11, 0
v_mov_b32 v12, 0
v_mov_b32 v13, 0
v_mov_b32 v14, 0
v_mov_b32 v15, 0
v_mov_b32 v16, 0
v_mov_b32 v17, 0
v_mov_b32 v18, 0
v_mov_b32 v19, 0
v_mov_b32 v20, 0
v_mov_b32 v21, 0
v_mov_b32 v22, 0
v_mov_b32 v23, 0
v_mov_b32 v24, 0
v_mov_b32 v25, 0
v_mov_b32 v26, 0
v_mov_b32 v27, 0
v_mov_b32 v28, 0
v_mov_b32 v29, 0
v_mov_b32 v30, 0
v_mov_b32 v31, 0
v_mov_b32 v32, 0
v_mov_b32 v33, 0
v_mov_b32 v34, 0
v_mov_b32 v35, 0
v_mov_b32 v36, 0
v_mov_b32 v37, 0
v_mov_b32 v38, 0
v_mov_b32 v39, 0
v_mov_b32 v40, 0
v_mov_b32 v41, 0
v_mov_b32 v42, 0
v_mov_b32 v43, 0
v_mov_b32 v44, 0
v_mov_b32 v45, 0
v_mov_b32 v46, 0
v_mov_b32 v47, 0
v_mov_b32 v48, 0
v_mov_b32 v49, 0
v_mov_b32 v50, 0
v_mov_b32 v51, 0
v_mov_b32 v52, 0
v_mov_b32 v53, 0
v_mov_b32 v54, 0
v_mov_b32 v55, 0
v_mov_b32 v56, 0
v_mov_b32 v57, 0
v_mov_b32 v58, 0
v_mov_b32 v59, 0
v_mov_b32 v60, 0
v_mov_b32 v61, 0
v_mov_b32 v62, 0
v_mov_b32 v63, 0
v_mov_b32 v216, 0xff61b1e6
v_mov_b32 v217, 0xff61b1e6
v_mov_b32 v218, 0
v_mov_b32 v219, 0
s_mov_b32 s7, 0
.La5_LA_SLOW0_L:
s_lshl_b32 s96, s7, 13
s_add_u32 s97, s96, 4096
ld a[128:131],v255,s[92:95],s96 offen offset:0 sc1
ld a[132:135],v255,s[92:95],s96 offen offset:1024 sc1
ld a[136:139],v255,s[92:95],s96 offen offset:2048 sc1
ld a[140:143],v255,s[92:95],s96 offen offset:3072 sc1
ld a[144:147],v255,s[92:95],s97 offen offset:0 sc1
ld a[148:151],v255,s[92:95],s97 offen offset:1024 sc1
ld a[152:155],v255,s[92:95],s97 offen offset:2048 sc1
ld a[156:159],v255,s[92:95],s97 offen offset:3072 sc1
s_lshl_b32 s96, s76, 3
s_sub_u32 s96, s96, s7
s_add_u32 s96, s96, s3
s_lshl_b32 s96, s96, 5
v_bfe_u32 v220, v255, 4, 4
v_lshrrev_b32 v221, 8, v255
v_lshlrev_b32 v221, 2, v221
v_sub_u32 v220, v220, v221
v_add_u32 v220, s96, v220
v_mov_b32 v222, 0xceabfb8f
wt vmcnt(0)
mm v[192:195],a[128:131],v[128:131],0
mm v[192:195],a[132:135],v[132:135],v[192:195]
mm v[192:195],a[136:139],v[136:139],v[192:195]
mm v[192:195],a[140:143],v[140:143],v[192:195]
mm v[196:199],a[144:147],v[128:131],0
mm v[196:199],a[148:151],v[132:135],v[196:199]
mm v[196:199],a[152:155],v[136:139],v[196:199]
mm v[196:199],a[156:159],v[140:143],v[196:199]
mm v[200:203],a[128:131],v[144:147],0
mm v[200:203],a[132:135],v[148:151],v[200:203]
mm v[200:203],a[136:139],v[152:155],v[200:203]
mm v[200:203],a[140:143],v[156:159],v[200:203]
mm v[204:207],a[144:147],v[144:147],0
mm v[204:207],a[148:151],v[148:151],v[204:207]
mm v[204:207],a[152:155],v[152:155],v[204:207]
mm v[204:207],a[156:159],v[156:159],v[204:207]
s_nop 7
s_nop 7
s_lshl_b32 s96, s7, 13
s_add_u32 s96, s96, 16777216
s_add_u32 s97, s96, 4096
ld a[128:131],v255,s[92:95],s96 offen offset:0 sc1
ld a[132:135],v255,s[92:95],s96 offen offset:1024 sc1
ld a[136:139],v255,s[92:95],s96 offen offset:2048 sc1
ld a[140:143],v255,s[92:95],s96 offen offset:3072 sc1
ld a[144:147],v255,s[92:95],s97 offen offset:0 sc1
ld a[148:151],v255,s[92:95],s97 offen offset:1024 sc1
ld a[152:155],v255,s[92:95],s97 offen offset:2048 sc1
ld a[156:159],v255,s[92:95],s97 offen offset:3072 sc1
v_cmp_gt_i32 vcc, 0, v220
s_nop 1
v_cndmask_b32 v192, v192, v222, vcc
v_cmp_gt_i32 vcc, 1, v220
s_nop 1
v_cndmask_b32 v193, v193, v222, vcc
v_cmp_gt_i32 vcc, 2, v220
s_nop 1
v_cndmask_b32 v194, v194, v222, vcc
v_cmp_gt_i32 vcc, 3, v220
s_nop 1
v_cndmask_b32 v195, v195, v222, vcc
v_cmp_gt_i32 vcc, 16, v220
s_nop 1
v_cndmask_b32 v196, v196, v222, vcc
v_cmp_gt_i32 vcc, 17, v220
s_nop 1
v_cndmask_b32 v197, v197, v222, vcc
v_cmp_gt_i32 vcc, 18, v220
s_nop 1
v_cndmask_b32 v198, v198, v222, vcc
v_cmp_gt_i32 vcc, 19, v220
s_nop 1
v_cndmask_b32 v199, v199, v222, vcc
v_cmp_gt_i32 vcc, -16, v220
s_nop 1
v_cndmask_b32 v200, v200, v222, vcc
v_cmp_gt_i32 vcc, -15, v220
s_nop 1
v_cndmask_b32 v201, v201, v222, vcc
v_cmp_gt_i32 vcc, -14, v220
s_nop 1
v_cndmask_b32 v202, v202, v222, vcc
v_cmp_gt_i32 vcc, -13, v220
s_nop 1
v_cndmask_b32 v203, v203, v222, vcc
v_cmp_gt_i32 vcc, 0, v220
s_nop 1
v_cndmask_b32 v204, v204, v222, vcc
v_cmp_gt_i32 vcc, 1, v220
s_nop 1
v_cndmask_b32 v205, v205, v222, vcc
v_cmp_gt_i32 vcc, 2, v220
s_nop 1
v_cndmask_b32 v206, v206, v222, vcc
v_cmp_gt_i32 vcc, 3, v220
s_nop 1
v_cndmask_b32 v207, v207, v222, vcc
v_max3_f32 v222, v192, v193, v216
v_max3_f32 v222, v194, v195, v222
v_max3_f32 v222, v196, v197, v222
v_max3_f32 v222, v198, v199, v222
v_mov_b32 v221, v222
s_nop 1
v_permlane16_swap_b32 v221, v222
s_nop 1
v_max_f32 v222, v222, v221
v_mov_b32 v221, v222
s_nop 1
v_permlane32_swap_b32 v221, v222
s_nop 1
v_max_f32 v222, v222, v221
v_sub_f32 v223, v216, v222
v_mov_b32 v216, v222
ex v223,v223
v_sub_f32 v192, v192, v222
v_sub_f32 v193, v193, v222
v_sub_f32 v194, v194, v222
v_sub_f32 v195, v195, v222
v_sub_f32 v196, v196, v222
v_sub_f32 v197, v197, v222
v_sub_f32 v198, v198, v222
v_sub_f32 v199, v199, v222
ex v192,v192
ex v193,v193
ex v194,v194
ex v195,v195
ex v196,v196
ex v197,v197
ex v198,v198
ex v199,v199
v_mul_f32 v218, v218, v223
v_add_f32 v218, v218, v192
v_add_f32 v218, v218, v193
v_add_f32 v218, v218, v194
v_add_f32 v218, v218, v195
v_add_f32 v218, v218, v196
v_add_f32 v218, v218, v197
v_add_f32 v218, v218, v198
v_add_f32 v218, v218, v199
cv v208,v192,v193
cv v209,v194,v195
cv v210,v196,v197
cv v211,v198,v199
v_mul_f32 v0, v0, v223
v_mul_f32 v1, v1, v223
v_mul_f32 v2, v2, v223
v_mul_f32 v3, v3, v223
v_mul_f32 v4, v4, v223
v_mul_f32 v5, v5, v223
v_mul_f32 v6, v6, v223
v_mul_f32 v7, v7, v223
v_mul_f32 v8, v8, v223
v_mul_f32 v9, v9, v223
v_mul_f32 v10, v10, v223
v_mul_f32 v11, v11, v223
v_mul_f32 v12, v12, v223
v_mul_f32 v13, v13, v223
v_mul_f32 v14, v14, v223
v_mul_f32 v15, v15, v223
v_mul_f32 v16, v16, v223
v_mul_f32 v17, v17, v223
v_mul_f32 v18, v18, v223
v_mul_f32 v19, v19, v223
v_mul_f32 v20, v20, v223
v_mul_f32 v21, v21, v223
v_mul_f32 v22, v22, v223
v_mul_f32 v23, v23, v223
v_mul_f32 v24, v24, v223
v_mul_f32 v25, v25, v223
v_mul_f32 v26, v26, v223
v_mul_f32 v27, v27, v223
v_mul_f32 v28, v28, v223
v_mul_f32 v29, v29, v223
v_mul_f32 v30, v30, v223
v_mul_f32 v31, v31, v223
v_max3_f32 v222, v200, v201, v217
v_max3_f32 v222, v202, v203, v222
v_max3_f32 v222, v204, v205, v222
v_max3_f32 v222, v206, v207, v222
v_mov_b32 v221, v222
s_nop 1
v_permlane16_swap_b32 v221, v222
s_nop 1
v_max_f32 v222, v222, v221
v_mov_b32 v221, v222
s_nop 1
v_permlane32_swap_b32 v221, v222
s_nop 1
v_max_f32 v222, v222, v221
v_sub_f32 v223, v217, v222
v_mov_b32 v217, v222
ex v223,v223
v_sub_f32 v200, v200, v222
v_sub_f32 v201, v201, v222
v_sub_f32 v202, v202, v222
v_sub_f32 v203, v203, v222
v_sub_f32 v204, v204, v222
v_sub_f32 v205, v205, v222
v_sub_f32 v206, v206, v222
v_sub_f32 v207, v207, v222
ex v200,v200
ex v201,v201
ex v202,v202
ex v203,v203
ex v204,v204
ex v205,v205
ex v206,v206
ex v207,v207
v_mul_f32 v219, v219, v223
v_add_f32 v219, v219, v200
v_add_f32 v219, v219, v201
v_add_f32 v219, v219, v202
v_add_f32 v219, v219, v203
v_add_f32 v219, v219, v204
v_add_f32 v219, v219, v205
v_add_f32 v219, v219, v206
v_add_f32 v219, v219, v207
cv v212,v200,v201
cv v213,v202,v203
cv v214,v204,v205
cv v215,v206,v207
v_mul_f32 v32, v32, v223
v_mul_f32 v33, v33, v223
v_mul_f32 v34, v34, v223
v_mul_f32 v35, v35, v223
v_mul_f32 v36, v36, v223
v_mul_f32 v37, v37, v223
v_mul_f32 v38, v38, v223
v_mul_f32 v39, v39, v223
v_mul_f32 v40, v40, v223
v_mul_f32 v41, v41, v223
v_mul_f32 v42, v42, v223
v_mul_f32 v43, v43, v223
v_mul_f32 v44, v44, v223
v_mul_f32 v45, v45, v223
v_mul_f32 v46, v46, v223
v_mul_f32 v47, v47, v223
v_mul_f32 v48, v48, v223
v_mul_f32 v49, v49, v223
v_mul_f32 v50, v50, v223
v_mul_f32 v51, v51, v223
v_mul_f32 v52, v52, v223
v_mul_f32 v53, v53, v223
v_mul_f32 v54, v54, v223
v_mul_f32 v55, v55, v223
v_mul_f32 v56, v56, v223
v_mul_f32 v57, v57, v223
v_mul_f32 v58, v58, v223
v_mul_f32 v59, v59, v223
v_mul_f32 v60, v60, v223
v_mul_f32 v61, v61, v223
v_mul_f32 v62, v62, v223
v_mul_f32_e64 v63, v63, v223
wt vmcnt(0)
mm v[0:3],a[128:131],v[208:211],v[0:3]
mm v[32:35],a[128:131],v[212:215],v[32:35]
mm v[4:7],a[132:135],v[208:211],v[4:7]
mm v[36:39],a[132:135],v[212:215],v[36:39]
mm v[8:11],a[136:139],v[208:211],v[8:11]
mm v[40:43],a[136:139],v[212:215],v[40:43]
mm v[12:15],a[140:143],v[208:211],v[12:15]
mm v[44:47],a[140:143],v[212:215],v[44:47]
mm v[16:19],a[144:147],v[208:211],v[16:19]
mm v[48:51],a[144:147],v[212:215],v[48:51]
mm v[20:23],a[148:151],v[208:211],v[20:23]
mm v[52:55],a[148:151],v[212:215],v[52:55]
mm v[24:27],a[152:155],v[208:211],v[24:27]
mm v[56:59],a[152:155],v[212:215],v[56:59]
mm v[28:31],a[156:159],v[208:211],v[28:31]
mm v[60:63],a[156:159],v[212:215],v[60:63]
s_nop 7
s_nop 7
s_add_u32 s7, s7, 1
s_lshr_b32 s96, s7, 3
s_cmp_le_u32 s96, s76
s_cbranch_scc1 .La5_LA_SLOW0_L
v_mov_b32 v221, v218
s_nop 1
v_permlane16_swap_b32 v221, v218
s_nop 1
v_add_f32 v218, v218, v221
v_mov_b32 v221, v218
s_nop 1
v_permlane32_swap_b32 v221, v218
s_nop 1
v_add_f32 v218, v218, v221
v_mov_b32 v221, v219
s_nop 1
v_permlane16_swap_b32 v221, v219
s_nop 1
v_add_f32 v219, v219, v221
v_mov_b32 v221, v219
s_nop 1
v_permlane32_swap_b32 v221, v219
s_nop 1
v_add_f32 v219, v219, v221
v_rcp_f32 v242, v218
v_rcp_f32 v243, v219
ar v192,a160
ar v193,a161
ar v194,a162
ar v195,a163
ar v196,a164
ar v197,a165
ar v198,a166
ar v199,a167
ar v200,a168
ar v201,a169
ar v202,a170
ar v203,a171
ar v204,a172
ar v205,a173
ar v206,a174
ar v207,a175
ar v208,a176
ar v209,a177
ar v210,a178
ar v211,a179
ar v212,a180
ar v213,a181
ar v214,a182
ar v215,a183
ar v216,a184
ar v217,a185
ar v218,a186
ar v219,a187
ar v220,a188
ar v221,a189
ar v222,a190
ar v223,a191
s_mov_b32 s95, 4096
s_mov_b32 s96, 8192
s_mov_b32 s97, 12288
s_mov_b64 vcc, 0
s_nop 1
s_setpc_b64 s[24:25]
; ---- fallback: the later 32 rows
.La5_LA_SLOW1:
wt lgkmcnt(0)
s_nop 7
s_nop 7
aw a160,v192
aw a161,v193
aw a162,v194
aw a163,v195
aw a164,v196
aw a165,v197
aw a166,v198
aw a167,v199
aw a168,v200
aw a169,v201
aw a170,v202
aw a171,v203
aw a172,v204
aw a173,v205
aw a174,v206
aw a175,v207
aw a176,v208
aw a177,v209
aw a178,v210
aw a179,v211
aw a180,v212
aw a181,v213
aw a182,v214
aw a183,v215
aw a184,v216
aw a185,v217
aw a186,v218
aw a187,v219
aw a188,v220
aw a189,v221
aw a190,v222
aw a191,v223
s_lshl_b32 s96, s72, 19
s_add_u32 s96, s96, 20971520
s_add_u32 s92, s26, s96
s_addc_u32 s93, s27, 0
s_and_b32 s93, s93, 0xffff
s_mov_b32 s94, 17301504
s_mov_b32 s95, 0x20000
v_mov_b32 v64, 0
v_mov_b32 v65, 0
v_mov_b32 v66, 0
v_mov_b32 v67, 0
v_mov_b32 v68, 0
v_mov_b32 v69, 0
v_mov_b32 v70, 0
v_mov_b32 v71, 0
v_mov_b32 v72, 0
v_mov_b32 v73, 0
v_mov_b32 v74, 0
v_mov_b32 v75, 0
v_mov_b32 v76, 0
v_mov_b32 v77, 0
v_mov_b32 v78, 0
v_mov_b32 v79, 0
v_mov_b32 v80, 0
v_mov_b32 v81, 0
v_mov_b32 v82, 0
v_mov_b32 v83, 0
v_mov_b32 v84, 0
v_mov_b32 v85, 0
v_mov_b32 v86, 0
v_mov_b32 v87, 0
v_mov_b32 v88, 0
v_mov_b32 v89, 0
v_mov_b32 v90, 0
v_mov_b32 v91, 0
v_mov_b32 v92, 0
v_mov_b32 v93, 0
v_mov_b32 v94, 0
v_mov_b32 v95, 0
v_mov_b32 v96, 0
v_mov_b32 v97, 0
v_mov_b32 v98, 0
v_mov_b32 v99, 0
v_mov_b32 v100, 0
v_mov_b32 v101, 0
v_mov_b32 v102, 0
v_mov_b32 v103, 0
v_mov_b32 v104, 0
v_mov_b32 v105, 0
v_mov_b32 v106, 0
v_mov_b32 v107, 0
v_mov_b32 v108, 0
v_mov_b32 v109, 0
v_mov_b32 v110, 0
v_mov_b32 v111, 0
v_mov_b32 v112, 0
v_mov_b32 v113, 0
v_mov_b32 v114, 0
v_mov_b32 v115, 0
v_mov_b32 v116, 0
v_mov_b32 v117, 0
v_mov_b32 v118, 0
v_mov_b32 v119, 0
v_mov_b32 v120, 0
v_mov_b32 v121, 0
v_mov_b32 v122, 0
v_mov_b32 v123, 0
v_mov_b32 v124, 0
v_mov_b32 v125, 0
v_mov_b32 v126, 0
v_mov_b32 v127, 0
v_mov_b32 v216, 0xff61b1e6
v_mov_b32 v217, 0xff61b1e6
v_mov_b32 v218, 0
v_mov_b32 v219, 0
s_mov_b32 s7, 0
.La5_LA_SLOW1_L:
s_lshl_b32 s96, s7, 13
s_add_u32 s97, s96, 4096
ld a[128:131],v255,s[92:95],s96 offen offset:0 sc1
ld a[132:135],v255,s[92:95],s96 offen offset:1024 sc1
ld a[136:139],v255,s[92:95],s96 offen offset:2048 sc1
ld a[140:143],v255,s[92:95],s96 offen offset:3072 sc1
ld a[144:147],v255,s[92:95],s97 offen offset:0 sc1
ld a[148:151],v255,s[92:95],s97 offen offset:1024 sc1
ld a[152:155],v255,s[92:95],s97 offen offset:2048 sc1
ld a[156:159],v255,s[92:95],s97 offen offset:3072 sc1
s_lshl_b32 s96, s76, 3
s_sub_u32 s96, s96, s7
s_sub_u32 s96, s96, s3
s_add_u32 s96, s96, 7
s_lshl_b32 s96, s96, 5
v_bfe_u32 v220, v255, 4, 4
v_lshrrev_b32 v221, 8, v255
v_lshlrev_b32 v221, 2, v221
v_sub_u32 v220, v220, v221
v_add_u32 v220, s96, v220
v_mov_b32 v222, 0xceabfb8f
wt vmcnt(0)
mm v[192:195],a[128:131],v[160:163],0
mm v[192:195],a[132:135],v[164:167],v[192:195]
mm v[192:195],a[136:139],v[168:171],v[192:195]
mm v[192:195],a[140:143],v[172:175],v[192:195]
mm v[196:199],a[144:147],v[160:163],0
mm v[196:199],a[148:151],v[164:167],v[196:199]
mm v[196:199],a[152:155],v[168:171],v[196:199]
mm v[196:199],a[156:159],v[172:175],v[196:199]
mm v[200:203],a[128:131],v[176:179],0
mm v[200:203],a[132:135],v[180:183],v[200:203]
mm v[200:203],a[136:139],v[184:187],v[200:203]
mm v[200:203],a[140:143],v[188:191],v[200:203]
mm v[204:207],a[144:147],v[176:179],0
mm v[204:207],a[148:151],v[180:183],v[204:207]
mm v[204:207],a[152:155],v[184:187],v[204:207]
mm v[204:207],a[156:159],v[188:191],v[204:207]
s_nop 7
s_nop 7
s_lshl_b32 s96, s7, 13
s_add_u32 s96, s96, 16777216
s_add_u32 s97, s96, 4096
ld a[128:131],v255,s[92:95],s96 offen offset:0 sc1
ld a[132:135],v255,s[92:95],s96 offen offset:1024 sc1
ld a[136:139],v255,s[92:95],s96 offen offset:2048 sc1
ld a[140:143],v255,s[92:95],s96 offen offset:3072 sc1
ld a[144:147],v255,s[92:95],s97 offen offset:0 sc1
ld a[148:151],v255,s[92:95],s97 offen offset:1024 sc1
ld a[152:155],v255,s[92:95],s97 offen offset:2048 sc1
ld a[156:159],v255,s[92:95],s97 offen offset:3072 sc1
v_cmp_gt_i32 vcc, 0, v220
s_nop 1
v_cndmask_b32 v192, v192, v222, vcc
v_cmp_gt_i32 vcc, 1, v220
s_nop 1
v_cndmask_b32 v193, v193, v222, vcc
v_cmp_gt_i32 vcc, 2, v220
s_nop 1
v_cndmask_b32 v194, v194, v222, vcc
v_cmp_gt_i32 vcc, 3, v220
s_nop 1
v_cndmask_b32 v195, v195, v222, vcc
v_cmp_gt_i32 vcc, 16, v220
s_nop 1
v_cndmask_b32 v196, v196, v222, vcc
v_cmp_gt_i32 vcc, 17, v220
s_nop 1
v_cndmask_b32 v197, v197, v222, vcc
v_cmp_gt_i32 vcc, 18, v220
s_nop 1
v_cndmask_b32 v198, v198, v222, vcc
v_cmp_gt_i32 vcc, 19, v220
s_nop 1
v_cndmask_b32 v199, v199, v222, vcc
v_cmp_gt_i32 vcc, -16, v220
s_nop 1
v_cndmask_b32 v200, v200, v222, vcc
v_cmp_gt_i32 vcc, -15, v220
s_nop 1
v_cndmask_b32 v201, v201, v222, vcc
v_cmp_gt_i32 vcc, -14, v220
s_nop 1
v_cndmask_b32 v202, v202, v222, vcc
v_cmp_gt_i32 vcc, -13, v220
s_nop 1
v_cndmask_b32 v203, v203, v222, vcc
v_cmp_gt_i32 vcc, 0, v220
s_nop 1
v_cndmask_b32 v204, v204, v222, vcc
v_cmp_gt_i32 vcc, 1, v220
s_nop 1
v_cndmask_b32 v205, v205, v222, vcc
v_cmp_gt_i32 vcc, 2, v220
s_nop 1
v_cndmask_b32 v206, v206, v222, vcc
v_cmp_gt_i32 vcc, 3, v220
s_nop 1
v_cndmask_b32 v207, v207, v222, vcc
v_max3_f32 v222, v192, v193, v216
v_max3_f32 v222, v194, v195, v222
v_max3_f32 v222, v196, v197, v222
v_max3_f32 v222, v198, v199, v222
v_mov_b32 v221, v222
s_nop 1
v_permlane16_swap_b32 v221, v222
s_nop 1
v_max_f32 v222, v222, v221
v_mov_b32 v221, v222
s_nop 1
v_permlane32_swap_b32 v221, v222
s_nop 1
v_max_f32 v222, v222, v221
v_sub_f32 v223, v216, v222
v_mov_b32 v216, v222
ex v223,v223
v_sub_f32 v192, v192, v222
v_sub_f32 v193, v193, v222
v_sub_f32 v194, v194, v222
v_sub_f32 v195, v195, v222
v_sub_f32 v196, v196, v222
v_sub_f32 v197, v197, v222
v_sub_f32 v198, v198, v222
v_sub_f32 v199, v199, v222
ex v192,v192
ex v193,v193
ex v194,v194
ex v195,v195
ex v196,v196
ex v197,v197
ex v198,v198
ex v199,v199
v_mul_f32 v218, v218, v223
v_add_f32 v218, v218, v192
v_add_f32 v218, v218, v193
v_add_f32 v218, v218, v194
v_add_f32 v218, v218, v195
v_add_f32 v218, v218, v196
v_add_f32 v218, v218, v197
v_add_f32 v218, v218, v198
v_add_f32 v218, v218, v199
cv v208,v192,v193
cv v209,v194,v195
cv v210,v196,v197
cv v211,v198,v199
v_mul_f32 v64, v64, v223
v_mul_f32 v65, v65, v223
v_mul_f32 v66, v66, v223
v_mul_f32 v67, v67, v223
v_mul_f32 v68, v68, v223
v_mul_f32 v69, v69, v223
v_mul_f32 v70, v70, v223
v_mul_f32 v71, v71, v223
v_mul_f32 v72, v72, v223
v_mul_f32 v73, v73, v223
v_mul_f32 v74, v74, v223
v_mul_f32 v75, v75, v223
v_mul_f32 v76, v76, v223
v_mul_f32 v77, v77, v223
v_mul_f32 v78, v78, v223
v_mul_f32 v79, v79, v223
v_mul_f32 v80, v80, v223
v_mul_f32 v81, v81, v223
v_mul_f32 v82, v82, v223
v_mul_f32 v83, v83, v223
v_mul_f32 v84, v84, v223
v_mul_f32 v85, v85, v223
v_mul_f32 v86, v86, v223
v_mul_f32 v87, v87, v223
v_mul_f32 v88, v88, v223
v_mul_f32 v89, v89, v223
v_mul_f32 v90, v90, v223
v_mul_f32 v91, v91, v223
v_mul_f32 v92, v92, v223
v_mul_f32 v93, v93, v223
v_mul_f32 v94, v94, v223
v_mul_f32 v95, v95, v223
v_max3_f32 v222, v200, v201, v217
v_max3_f32 v222, v202, v203, v222
v_max3_f32 v222, v204, v205, v222
v_max3_f32 v222, v206, v207, v222
v_mov_b32 v221, v222
s_nop 1
v_permlane16_swap_b32 v221, v222
s_nop 1
v_max_f32 v222, v222, v221
v_mov_b32 v221, v222
s_nop 1
v_permlane32_swap_b32 v221, v222
s_nop 1
v_max_f32 v222, v222, v221
v_sub_f32 v223, v217, v222
v_mov_b32 v217, v222
ex v223,v223
v_sub_f32 v200, v200, v222
v_sub_f32 v201, v201, v222
v_sub_f32 v202, v202, v222
v_sub_f32 v203, v203, v222
v_sub_f32 v204, v204, v222
v_sub_f32 v205, v205, v222
v_sub_f32 v206, v206, v222
v_sub_f32 v207, v207, v222
ex v200,v200
ex v201,v201
ex v202,v202
ex v203,v203
ex v204,v204
ex v205,v205
ex v206,v206
ex v207,v207
v_mul_f32 v219, v219, v223
v_add_f32 v219, v219, v200
v_add_f32 v219, v219, v201
v_add_f32 v219, v219, v202
v_add_f32 v219, v219, v203
v_add_f32 v219, v219, v204
v_add_f32 v219, v219, v205
v_add_f32 v219, v219, v206
v_add_f32 v219, v219, v207
cv v212,v200,v201
cv v213,v202,v203
cv v214,v204,v205
cv v215,v206,v207
v_mul_f32 v96, v96, v223
v_mul_f32 v97, v97, v223
v_mul_f32 v98, v98, v223
v_mul_f32 v99, v99, v223
v_mul_f32 v100, v100, v223
v_mul_f32 v101, v101, v223
v_mul_f32 v102, v102, v223
v_mul_f32 v103, v103, v223
v_mul_f32 v104, v104, v223
v_mul_f32 v105, v105, v223
v_mul_f32 v106, v106, v223
v_mul_f32 v107, v107, v223
v_mul_f32 v108, v108, v223
v_mul_f32 v109, v109, v223
v_mul_f32 v110, v110, v223
v_mul_f32 v111, v111, v223
v_mul_f32 v112, v112, v223
v_mul_f32 v113, v113, v223
v_mul_f32 v114, v114, v223
v_mul_f32 v115, v115, v223
v_mul_f32 v116, v116, v223
v_mul_f32 v117, v117, v223
v_mul_f32 v118, v118, v223
v_mul_f32 v119, v119, v223
v_mul_f32 v120, v120, v223
v_mul_f32 v121, v121, v223
v_mul_f32 v122, v122, v223
v_mul_f32 v123, v123, v223
v_mul_f32 v124, v124, v223
v_mul_f32 v125, v125, v223
v_mul_f32 v126, v126, v223
v_mul_f32_e64 v127, v127, v223
wt vmcnt(0)
mm v[64:67],a[128:131],v[208:211],v[64:67]
mm v[96:99],a[128:131],v[212:215],v[96:99]
mm v[68:71],a[132:135],v[208:211],v[68:71]
mm v[100:103],a[132:135],v[212:215],v[100:103]
mm v[72:75],a[136:139],v[208:211],v[72:75]
mm v[104:107],a[136:139],v[212:215],v[104:107]
mm v[76:79],a[140:143],v[208:211],v[76:79]
mm v[108:111],a[140:143],v[212:215],v[108:111]
mm v[80:83],a[144:147],v[208:211],v[80:83]
mm v[112:115],a[144:147],v[212:215],v[112:115]
mm v[84:87],a[148:151],v[208:211],v[84:87]
mm v[116:119],a[148:151],v[212:215],v[116:119]
mm v[88:91],a[152:155],v[208:211],v[88:91]
mm v[120:123],a[152:155],v[212:215],v[120:123]
mm v[92:95],a[156:159],v[208:211],v[92:95]
mm v[124:127],a[156:159],v[212:215],v[124:127]
s_nop 7
s_nop 7
s_add_u32 s7, s7, 1
s_lshr_b32 s96, s7, 3
s_cmp_le_u32 s96, s76
s_cbranch_scc1 .La5_LA_SLOW1_L
v_mov_b32 v221, v218
s_nop 1
v_permlane16_swap_b32 v221, v218
s_nop 1
v_add_f32 v218, v218, v221
v_mov_b32 v221, v218
s_nop 1
v_permlane32_swap_b32 v221, v218
s_nop 1
v_add_f32 v218, v218, v221
v_mov_b32 v221, v219
s_nop 1
v_permlane16_swap_b32 v221, v219
s_nop 1
v_add_f32 v219, v219, v221
v_mov_b32 v221, v219
s_nop 1
v_permlane32_swap_b32 v221, v219
s_nop 1
v_add_f32 v219, v219, v221
v_rcp_f32 v242, v218
v_rcp_f32 v243, v219
ar v192,a160
ar v193,a161
ar v194,a162
ar v195,a163
ar v196,a164
ar v197,a165
ar v198,a166
ar v199,a167
ar v200,a168
ar v201,a169
ar v202,a170
ar v203,a171
ar v204,a172
ar v205,a173
ar v206,a174
ar v207,a175
ar v208,a176
ar v209,a177
ar v210,a178
ar v211,a179
ar v212,a180
ar v213,a181
ar v214,a182
ar v215,a183
ar v216,a184
ar v217,a185
ar v218,a186
ar v219,a187
ar v220,a188
ar v221,a189
ar v222,a190
ar v223,a191
s_mov_b32 s95, 4096
s_mov_b32 s96, 8192
s_mov_b32 s97, 12288
s_mov_b64 vcc, 0
s_nop 1
s_setpc_b64 s[24:25]
.La5_LA_SC1:
s_call_b64 s[24:25], .La5_LA_SLOW0
s_branch .La5_LA_SR1
.La5_LA_SC2:
s_call_b64 s[24:25], .La5_LA_SLOW1
s_branch .La5_LA_SR2
.La5_LA_PS0:
s_mov_b32 s84, 3
s_mov_b32 s70, 17408
wt vmcnt(2)
ld a[220:223],v255,s[68:71],s94 offen sc1
ld a[0:3],v255,s[68:71],0 offen offset:0 sc1
ld a[4:7],v255,s[68:71],0 offen offset:1024 sc1
ld a[8:11],v255,s[68:71],0 offen offset:2048 sc1
ld a[12:15],v255,s[68:71],0 offen offset:3072 sc1
ld a[16:19],v255,s[68:71],s95 offen offset:0 sc1
ld a[20:23],v255,s[68:71],s95 offen offset:1024 sc1
ld a[24:27],v255,s[68:71],s95 offen offset:2048 sc1
s_branch .La5_LA_PS1
.La5_LA_PS2:
s_mov_b32 s84, 3
s_mov_b32 s70, 17408
wt vmcnt(0)
ld a[220:223],v255,s[68:71],s94 offen sc1
ld a[0:3],v255,s[68:71],0 offen offset:0 sc1
ld a[4:7],v255,s[68:71],0 offen offset:1024 sc1
ld a[8:11],v255,s[68:71],0 offen offset:2048 sc1
ld a[12:15],v255,s[68:71],0 offen offset:3072 sc1
ld a[16:19],v255,s[68:71],s95 offen offset:0 sc1
ld a[20:23],v255,s[68:71],s95 offen offset:1024 sc1
ld a[24:27],v255,s[68:71],s95 offen offset:2048 sc1
ld a[28:31],v255,s[68:71],s95 offen offset:3072 sc1
ld a[32:35],v255,s[68:71],s96 offen offset:0 sc1
ld a[36:39],v255,s[68:71],s96 offen offset:1024 sc1
ld a[40:43],v255,s[68:71],s96 offen offset:2048 sc1
ld a[44:47],v255,s[68:71],s96 offen offset:3072 sc1
ld a[48:51],v255,s[68:71],s97 offen offset:0 sc1
ld a[52:55],v255,s[68:71],s97 offen offset:1024 sc1
ld a[56:59],v255,s[68:71],s97 offen offset:2048 sc1
ld a[60:63],v255,s[68:71],s97 offen offset:3072 sc1
s_branch .La5_LA_PS3
.La5_LA_PSPIN:
s_mov_b32 s93, 0
.La5_LA_PSPIN_L:
s_add_u32 s93, s93, 1
s_cmp_gt_u32 s93, 2097152
s_cbranch_scc1 .La5_LA_PSOK
s_sleep 8
buffer_load_dword v250, v251, s[64:67], s20 offen sc1
wt vmcnt(0)
v_cmp_eq_u32 vcc, v250, v249
s_nop 1
s_and_b32 s92, vcc_lo, 3
s_cmp_eq_u32 s92, 3
s_cbranch_scc0 .La5_LA_PSPIN_L
s_branch .La5_LA_PSOK
.La5_LA_PSOK:
s_mov_b32 s70, 17408
ld a[220:223],v255,s[68:71],s94 offen sc1
ld a[0:3],v255,s[68:71],0 offen offset:0 sc1
ld a[4:7],v255,s[68:71],0 offen offset:1024 sc1
ld a[8:11],v255,s[68:71],0 offen offset:2048 sc1
ld a[12:15],v255,s[68:71],0 offen offset:3072 sc1
ld a[16:19],v255,s[68:71],s95 offen offset:0 sc1
ld a[20:23],v255,s[68:71],s95 offen offset:1024 sc1
ld a[24:27],v255,s[68:71],s95 offen offset:2048 sc1
ld a[28:31],v255,s[68:71],s95 offen offset:3072 sc1
ld a[32:35],v255,s[68:71],s96 offen offset:0 sc1
ld a[36:39],v255,s[68:71],s96 offen offset:1024 sc1
ld a[40:43],v255,s[68:71],s96 offen offset:2048 sc1
ld a[44:47],v255,s[68:71],s96 offen offset:3072 sc1
ld a[48:51],v255,s[68:71],s97 offen offset:0 sc1
ld a[52:55],v255,s[68:71],s97 offen offset:1024 sc1
ld a[56:59],v255,s[68:71],s97 offen offset:2048 sc1
ld a[60:63],v255,s[68:71],s97 offen offset:3072 sc1
s_branch .La5_LA_DC1
.La5_LO_NA1:
.La5_LW3_6:
s_cbranch_execz .La5_LA_RETURN
s_mov_b32 s93, 0
.La5_LO_NA1_L:
s_add_u32 s93, s93, 1
s_cmp_gt_u32 s93, 2097152
s_cbranch_scc1 .La5_LO_KA1
s_sleep 8
buffer_load_dwordx2 v[250:251], v248, s[64:67], s5 offen sc1
wt vmcnt(0)
v_cmp_eq_u64 vcc, v[250:251], v[252:253]
s_nop 1
s_bitcmp1_b32 vcc_lo, 1
s_cbranch_scc0 .La5_LO_NA1_L
s_branch .La5_LO_KA1
.La5_LO_NA2:
s_mov_b32 s93, 0
.La5_LO_NA2_L:
s_add_u32 s93, s93, 1
s_cmp_gt_u32 s93, 2097152
s_nop 0
s_cbranch_scc1 .La5_LO_KA2
s_sleep 8
buffer_load_dwordx2 v[250:251], v248, s[64:67], s5 offen sc1
wt vmcnt(0)
v_cmp_eq_u64 vcc, v[250:251], v[252:253]
s_nop 1
s_bitcmp1_b32 vcc_lo, 2
s_cbranch_scc0 .La5_LO_NA2_L
s_branch .La5_LO_KA2
.La5_LO_NA3:
s_mov_b32 s93, 0
.La5_LO_NA3_L:
s_add_u32 s93, s93, 1
s_cmp_gt_u32 s93, 2097152
s_nop 0
s_nop 0
s_cbranch_scc1 .La5_LO_KA3
s_sleep 8
buffer_load_dwordx2 v[250:251], v248, s[64:67], s5 offen sc1
wt vmcnt(0)
v_cmp_eq_u64 vcc, v[250:251], v[252:253]
s_nop 1
s_bitcmp1_b32 vcc_lo, 3
s_cbranch_scc0 .La5_LO_NA3_L
s_branch .La5_LO_KA3
.La5_LO_NA4:
s_mov_b32 s93, 0
.La5_LO_NA4_L:
s_add_u32 s93, s93, 1
s_cmp_gt_u32 s93, 2097152
s_nop 0
s_nop 0
s_cbranch_scc1 .La5_LO_KA4
s_sleep 8
buffer_load_dwordx2 v[250:251], v248, s[64:67], s5 offen sc1
wt vmcnt(0)
v_cmp_eq_u64 vcc, v[250:251], v[252:253]
s_nop 1
s_bitcmp1_b32 vcc_lo, 4
s_cbranch_scc0 .La5_LO_NA4_L
s_branch .La5_LO_KA4
.La5_LO_NA5:
s_mov_b32 s93, 0
.La5_LO_NA5_L:
s_add_u32 s93, s93, 1
s_cmp_gt_u32 s93, 2097152
s_nop 0
s_nop 0
s_cbranch_scc1 .La5_LO_KA5
s_sleep 8
buffer_load_dwordx2 v[250:251], v248, s[64:67], s5 offen sc1
wt vmcnt(0)
v_cmp_eq_u64 vcc, v[250:251], v[252:253]
s_nop 1
s_bitcmp1_b32 vcc_lo, 5
s_cbranch_scc0 .La5_LO_NA5_L
s_branch .La5_LO_KA5
.La5_LO_NA6:
s_mov_b32 s93, 0
.La5_LO_NA6_L:
s_add_u32 s93, s93, 1
s_cmp_gt_u32 s93, 2097152
s_nop 0
s_nop 0
s_cbranch_scc1 .La5_LO_KA6
s_sleep 8
buffer_load_dwordx2 v[250:251], v248, s[64:67], s5 offen sc1
wt vmcnt(0)
v_cmp_eq_u64 vcc, v[250:251], v[252:253]
s_nop 1
s_bitcmp1_b32 vcc_lo, 6
s_cbranch_scc0 .La5_LO_NA6_L
s_branch .La5_LO_KA6
.La5_LO_NA7:
s_mov_b32 s93, 0
.La5_LO_NA7_L:
s_add_u32 s93, s93, 1
s_cmp_gt_u32 s93, 2097152
s_nop 0
s_nop 0
s_cbranch_scc1 .La5_LO_KA7
s_sleep 8
buffer_load_dwordx2 v[250:251], v248, s[64:67], s5 offen sc1
wt vmcnt(0)
v_cmp_eq_u64 vcc, v[250:251], v[252:253]
s_nop 1
s_bitcmp1_b32 vcc_lo, 7
s_cbranch_scc0 .La5_LO_NA7_L
s_branch .La5_LO_KA7
.La5_LA_SC3:
s_call_b64 s[24:25], .La5_LA_SLOW0
aw a0,0
aw a1,0
aw a2,0
aw a3,0
aw a4,0
aw a5,0
aw a6,0
aw a7,0
aw a8,0
aw a9,0
aw a10,0
aw a11,0
aw a12,0
aw a13,0
aw a14,0
aw a15,0
aw a16,0
aw a17,0
aw a18,0
aw a19,0
aw a20,0
aw a21,0
aw a22,0
aw a23,0
aw a24,0
aw a25,0
aw a26,0
aw a27,0
aw a28,0
aw a29,0
aw a30,0
aw a31,0
s_nop 1
v_mov_b32 v250, v242
v_mov_b32 v249, v243
v_xor_b32 v242, 64, v240
v_xor_b32 v243, 96, v240
ar v254,a229
s_mov_b32 s93, 0
ld a[128:131],v254,s[40:43],s93 offen
s_mov_b32 s93, 7168
ld a[132:135],v254,s[40:43],s93 offen
s_mov_b32 s93, 14336
ld a[136:139],v254,s[40:43],s93 offen
s_mov_b32 s93, 21504
ld a[140:143],v254,s[40:43],s93 offen
s_mov_b32 s93, 28672
ld a[144:147],v254,s[40:43],s93 offen
s_mov_b32 s93, 35840
ld a[148:151],v254,s[40:43],s93 offen
s_mov_b32 s93, 43008
ld a[152:155],v254,s[40:43],s93 offen
s_mov_b32 s93, 50176
ld a[156:159],v254,s[40:43],s93 offen
s_mov_b32 s93, 57344
ld a[160:163],v254,s[40:43],s93 offen
s_mov_b32 s93, 64512
ld a[164:167],v254,s[40:43],s93 offen
s_mov_b32 s93, 71680
ld a[168:171],v254,s[40:43],s93 offen
s_mov_b32 s93, 78848
ld a[172:175],v254,s[40:43],s93 offen
s_mov_b32 s93, 86016
ld a[176:179],v254,s[40:43],s93 offen
s_mov_b32 s93, 93184
ld a[180:183],v254,s[40:43],s93 offen
s_mov_b32 s93, 100352
ld a[184:187],v254,s[40:43],s93 offen
s_mov_b32 s93, 107520
ld a[188:191],v254,s[40:43],s93 offen
s_branch .La5_LA_SR3
.La5_LA_SC4:
s_call_b64 s[24:25], .La5_LA_SLOW1
aw a32,0
aw a33,0
aw a34,0
aw a35,0
aw a36,0
aw a37,0
aw a38,0
aw a39,0
aw a40,0
aw a41,0
aw a42,0
aw a43,0
aw a44,0
aw a45,0
aw a46,0
aw a47,0
aw a48,0
aw a49,0
aw a50,0
aw a51,0
aw a52,0
aw a53,0
aw a54,0
aw a55,0
aw a56,0
aw a57,0
aw a58,0
aw a59,0
aw a60,0
aw a61,0
aw a62,0
aw a63,0
s_nop 1
v_mov_b32 v250, v242
v_mov_b32 v249, v243
v_xor_b32 v242, 64, v240
v_xor_b32 v243, 96, v240
ar v254,a229
s_mov_b32 s93, 0
ld a[128:131],v254,s[40:43],s93 offen
s_mov_b32 s93, 7168
ld a[132:135],v254,s[40:43],s93 offen
s_mov_b32 s93, 14336
ld a[136:139],v254,s[40:43],s93 offen
s_mov_b32 s93, 21504
ld a[140:143],v254,s[40:43],s93 offen
s_mov_b32 s93, 28672
ld a[144:147],v254,s[40:43],s93 offen
s_mov_b32 s93, 35840
ld a[148:151],v254,s[40:43],s93 offen
s_mov_b32 s93, 43008
ld a[152:155],v254,s[40:43],s93 offen
s_mov_b32 s93, 50176
ld a[156:159],v254,s[40:43],s93 offen
s_mov_b32 s93, 57344
ld a[160:163],v254,s[40:43],s93 offen
s_mov_b32 s93, 64512
ld a[164:167],v254,s[40:43],s93 offen
s_mov_b32 s93, 71680
ld a[168:171],v254,s[40:43],s93 offen
s_mov_b32 s93, 78848
ld a[172:175],v254,s[40:43],s93 offen
s_mov_b32 s93, 86016
ld a[176:179],v254,s[40:43],s93 offen
s_mov_b32 s93, 93184
ld a[180:183],v254,s[40:43],s93 offen
s_mov_b32 s93, 100352
ld a[184:187],v254,s[40:43],s93 offen
s_mov_b32 s93, 107520
ld a[188:191],v254,s[40:43],s93 offen
s_branch .La5_LA_SR4
.La5_LO_W:
s_mov_b32 s93, 3670016
ld a[192:195],v228,s[40:43],s93 offen
s_mov_b32 s93, 3677184
ld a[196:199],v228,s[40:43],s93 offen
s_mov_b32 s93, 3684352
ld a[200:203],v228,s[40:43],s93 offen
s_mov_b32 s93, 3691520
ld a[204:207],v228,s[40:43],s93 offen
s_mov_b32 s93, 3698688
ld a[208:211],v228,s[40:43],s93 offen
s_mov_b32 s93, 3705856
ld a[212:215],v228,s[40:43],s93 offen
s_mov_b32 s93, 3713024
ld a[216:219],v228,s[40:43],s93 offen
s_mov_b32 s93, 3720192
ld a[220:223],v228,s[40:43],s93 offen
s_mov_b32 s93, 3727360
ld a[224:227],v228,s[40:43],s93 offen
s_mov_b32 s93, 3734528
ld a[228:231],v228,s[40:43],s93 offen
s_mov_b32 s93, 3741696
ld a[232:235],v228,s[40:43],s93 offen
s_mov_b32 s93, 3748864
ld a[236:239],v228,s[40:43],s93 offen
s_mov_b32 s93, 3756032
ld a[240:243],v228,s[40:43],s93 offen
s_mov_b32 s93, 3763200
ld a[244:247],v228,s[40:43],s93 offen
s_mov_b32 s93, 3770368
ld a[248:251],v228,s[40:43],s93 offen
s_mov_b32 s93, 3777536
ld a[252:255],v228,s[40:43],s93 offen
s_branch .La5_LO_START
.La5_LT_VAR:
v_mov_b32 v230, 0
v_lshlrev_b32 v232, 16, v96
v_and_b32 v233, 0xffff0000, v96
v_sub_f32 v232, v232, v244
v_sub_f32 v233, v233, v244
v_fmac_f32 v230, v232, v232
v_fmac_f32 v230, v233, v233
v_lshlrev_b32 v232, 16, v97
v_and_b32 v233, 0xffff0000, v97
v_sub_f32 v232, v232, v244
v_sub_f32 v233, v233, v244
v_fmac_f32 v230, v232, v232
v_fmac_f32 v230, v233, v233
v_lshlrev_b32 v232, 16, v98
v_and_b32 v233, 0xffff0000, v98
v_sub_f32 v232, v232, v244
v_sub_f32 v233, v233, v244
v_fmac_f32 v230, v232, v232
v_fmac_f32 v230, v233, v233
v_lshlrev_b32 v232, 16, v99
v_and_b32 v233, 0xffff0000, v99
v_sub_f32 v232, v232, v244
v_sub_f32 v233, v233, v244
v_fmac_f32 v230, v232, v232
v_fmac_f32 v230, v233, v233
v_lshlrev_b32 v232, 16, v100
v_and_b32 v233, 0xffff0000, v100
v_sub_f32 v232, v232, v244
v_sub_f32 v233, v233, v244
v_fmac_f32 v230, v232, v232
v_fmac_f32 v230, v233, v233
v_lshlrev_b32 v232, 16, v101
v_and_b32 v233, 0xffff0000, v101
v_sub_f32 v232, v232, v244
v_sub_f32 v233, v233, v244
v_fmac_f32 v230, v232, v232
v_fmac_f32 v230, v233, v233
v_lshlrev_b32 v232, 16, v102
v_and_b32 v233, 0xffff0000, v102
v_sub_f32 v232, v232, v244
v_sub_f32 v233, v233, v244
v_fmac_f32 v230, v232, v232
v_fmac_f32 v230, v233, v233
v_lshlrev_b32 v232, 16, v103
v_and_b32 v233, 0xffff0000, v103
v_sub_f32 v232, v232, v244
v_sub_f32 v233, v233, v244
v_fmac_f32 v230, v232, v232
v_fmac_f32 v230, v233, v233
v_lshlrev_b32 v232, 16, v104
v_and_b32 v233, 0xffff0000, v104
v_sub_f32 v232, v232, v244
v_sub_f32 v233, v233, v244
v_fmac_f32 v230, v232, v232
v_fmac_f32 v230, v233, v233
v_lshlrev_b32 v232, 16, v105
v_and_b32 v233, 0xffff0000, v105
v_sub_f32 v232, v232, v244
v_sub_f32 v233, v233, v244
v_fmac_f32 v230, v232, v232
v_fmac_f32 v230, v233, v233
v_lshlrev_b32 v232, 16, v106
v_and_b32 v233, 0xffff0000, v106
v_sub_f32 v232, v232, v244
v_sub_f32 v233, v233, v244
v_fmac_f32 v230, v232, v232
v_fmac_f32 v230, v233, v233
v_lshlrev_b32 v232, 16, v107
v_and_b32 v233, 0xffff0000, v107
v_sub_f32 v232, v232, v244
v_sub_f32 v233, v233, v244
v_fmac_f32 v230, v232, v232
v_fmac_f32 v230, v233, v233
v_lshlrev_b32 v232, 16, v108
v_and_b32 v233, 0xffff0000, v108
v_sub_f32 v232, v232, v244
v_sub_f32 v233, v233, v244
v_fmac_f32 v230, v232, v232
v_fmac_f32 v230, v233, v233
v_lshlrev_b32 v232, 16, v109
v_and_b32 v233, 0xffff0000, v109
v_sub_f32 v232, v232, v244
v_sub_f32 v233, v233, v244
v_fmac_f32 v230, v232, v232
v_fmac_f32 v230, v233, v233
v_lshlrev_b32 v232, 16, v110
v_and_b32 v233, 0xffff0000, v110
v_sub_f32 v232, v232, v244
v_sub_f32 v233, v233, v244
v_fmac_f32 v230, v232, v232
v_fmac_f32 v230, v233, v233
v_lshlrev_b32 v232, 16, v111
v_and_b32 v233, 0xffff0000, v111
v_sub_f32 v232, v232, v244
v_sub_f32 v233, v233, v244
v_fmac_f32 v230, v232, v232
v_fmac_f32 v230, v233, v233
s_nop 1
v_add_f32_dpp v230, v230, v230 quad_perm:[1,0,3,2] row_mask:0xf bank_mask:0xf
s_nop 1
v_add_f32_dpp v230, v230, v230 quad_perm:[2,3,0,1] row_mask:0xf bank_mask:0xf
s_nop 1
v_add_f32_dpp v230, v230, v230 row_ror:8 row_mask:0xf bank_mask:0xf
v_mov_b32 v231, v230
s_nop 1
v_permlane16_swap_b32 v231, v230
s_nop 1
v_add_f32 v230, v230, v231
v_mov_b32 v231, v230
s_nop 1
v_permlane32_swap_b32 v231, v230
s_nop 1
v_add_f32 v230, v230, v231
v_mul_f32 v230, 0x3a800000, v230
s_branch .La5_LT_VOK
.La5_LA_KSPIN:
s_mov_b32 s93, 0
.La5_LA_KSPIN_L:
s_add_u32 s93, s93, 1
s_cmp_gt_u32 s93, 2097152
s_cbranch_scc1 .La5_LA_KOK
s_sleep 8
buffer_load_dword v250, v251, s[64:67], s20 offen sc1
wt vmcnt(0)
v_cmp_eq_u32 vcc, v250, v249
s_nop 1
s_or_b32 vcc_hi, vcc_hi, s79
s_cmp_eq_u64 vcc, -1
s_cbranch_scc0 .La5_LA_KSPIN_L
s_branch .La5_LA_KOK
.La5_LA_QSPIN:
s_mov_b32 s93, 0
.La5_LA_QSPIN_L:
s_add_u32 s93, s93, 1
s_cmp_gt_u32 s93, 2097152
s_nop 0
s_cbranch_scc1 .La5_LA_QOK
s_sleep 8
buffer_load_dword v250, v251, s[64:67], s20 offen sc1
wt vmcnt(0)
v_cmp_eq_u32 vcc, v250, v249
s_nop 1
s_and_b32 s92, vcc_lo, 3
s_cmp_eq_u32 s92, 3
s_cbranch_scc0 .La5_LA_QSPIN_L
s_branch .La5_LA_QOK
.La5_LEND:
.La5_LCODEEND:
)ASM"
        :: "{s[0:1]}"(__builtin_amdgcn_kernarg_segment_ptr()), "{s2}"(blockIdx.x), "{s3}"(__builtin_amdgcn_readfirstlane(threadIdx.x >> 6)), "{s[4:5]}"(DBGP)
        : "v0","v1","v2","v3","v4","v5","v6","v7","v8","v9","v10","v11","v12","v13","v14","v15","v16","v17","v18","v19","v20","v21","v22","v23","v24","v25","v26","v27","v28","v29","v30","v31","v32","v33","v34","v35","v36","v37","v38","v39","v40","v41","v42","v43","v44","v45","v46","v47","v48","v49","v50","v51","v52","v53","v54","v55","v56","v57","v58","v59","v60","v61","v62","v63","v64","v65","v66","v67","v68","v69","v70","v71","v72","v73","v74","v75","v76","v77","v78","v79","v80","v81","v82","v83","v84","v85","v86","v87","v88","v89","v90","v91","v92","v93","v94","v95","v96","v97","v98","v99","v100","v101","v102","v103","v104","v105","v106","v107","v108","v109","v110","v111","v112","v113","v114","v115","v116","v117","v118","v119","v120","v121","v122","v123","v124","v125","v126","v127","v128","v129","v130","v131","v132","v133","v134","v135","v136","v137","v138","v139","v140","v141","v142","v143","v144","v145","v146","v147","v148","v149","v150","v151","v152","v153","v154","v155","v156","v157","v158","v159","v160","v161","v162","v163","v164","v165","v166","v167","v168","v169","v170","v171","v172","v173","v174","v175","v176","v177","v178","v179","v180","v181","v182","v183","v184","v185","v186","v187","v188","v189","v190","v191","v192","v193","v194","v195","v196","v197","v198","v199","v200","v201","v202","v203","v204","v205","v206","v207","v208","v209","v210","v211","v212","v213","v214","v215","v216","v217","v218","v219","v220","v221","v222","v223","v224","v225","v226","v227","v228","v229","v230","v231","v232","v233","v234","v235","v236","v237","v238","v239","v240","v241","v242","v243","v244","v245","v246","v247","v248","v249","v250","v251","v252","v253","v254","v255","a0","a1","a2","a3","a4","a5","a6","a7","a8","a9","a10","a11","a12","a13","a14","a15","a16","a17","a18","a19","a20","a21","a22","a23","a24","a25","a26","a27","a28","a29","a30","a31","a32","a33","a34","a35","a36","a37","a38","a39","a40","a41","a42","a43","a44","a45","a46","a47","a48","a49","a50","a51","a52","a53","a54","a55","a56","a57","a58","a59","a60","a61","a62","a63","a64","a65","a66","a67","a68","a69","a70","a71","a72","a73","a74","a75","a76","a77","a78","a79","a80","a81","a82","a83","a84","a85","a86","a87","a88","a89","a90","a91","a92","a93","a94","a95","a96","a97","a98","a99","a100","a101","a102","a103","a104","a105","a106","a107","a108","a109","a110","a111","a112","a113","a114","a115","a116","a117","a118","a119","a120","a121","a122","a123","a124","a125","a126","a127","a128","a129","a130","a131","a132","a133","a134","a135","a136","a137","a138","a139","a140","a141","a142","a143","a144","a145","a146","a147","a148","a149","a150","a151","a152","a153","a154","a155","a156","a157","a158","a159","a160","a161","a162","a163","a164","a165","a166","a167","a168","a169","a170","a171","a172","a173","a174","a175","a176","a177","a178","a179","a180","a181","a182","a183","a184","a185","a186","a187","a188","a189","a190","a191","a192","a193","a194","a195","a196","a197","a198","a199","a200","a201","a202","a203","a204","a205","a206","a207","a208","a209","a210","a211","a212","a213","a214","a215","a216","a217","a218","a219","a220","a221","a222","a223","a224","a225","a226","a227","a228","a229","a230","a231","a232","a233","a234","a235","a236","a237","a238","a239","a240","a241","a242","a243","a244","a245","a246","a247","a248","a249","a250","a251","a252","a253","a254","a255","s6","s7","s8","s9","s10","s11","s12","s13","s14","s15","s16","s17","s18","s19","s20","s21","s22","s23","s24","s25","s26","s27","s28","s29","s30","s31","s32","s33","s34","s35","s36","s37","s38","s39","s40","s41","s42","s43","s44","s45","s46","s47","s48","s49","s50","s51","s52","s53","s54","s55","s56","s57","s58","s59","s60","s61","s62","s63","s64","s65","s66","s67","s68","s69","s70","s71","s72","s73","s74","s75","s76","s77","s78","s79","s80","s81","s82","s83","s84","s85","s86","s87","s88","s89","s90","s91","s92","s93","s94","s95","s96","s97","s98","s99","s100","s101","vcc","scc","m0","memory");
}

extern "C" void kernel_launch(void* const* d_in, const int* in_sizes, int n_in, void* d_out, int out_size, void* d_ws, size_t ws_size,
                              hipStream_t stream) {
    (void)hipFuncSetAttribute((const void*)fa_kernel, hipFuncAttributeMaxDynamicSharedMemorySize, LDS_BYTES);
    fa_kernel<<<256, 256, LDS_BYTES, stream>>>((const bf16*)d_in[0], (const bf16*)d_in[1], (const bf16*)d_in[2], (const bf16*)d_in[3], (const bf16*)d_in[4], (char*)d_ws,
                                               (const bf16*)d_in[5], (const bf16*)d_in[8], (const bf16*)d_in[9], (const bf16*)d_in[10], (const bf16*)d_in[7], (bf16*)d_out);
}
```

```cpp
#include <hip/hip_runtime.h>
#include <hip/hip_bf16.h>
#include <stdint.h>
#pragma clang diagnostic ignored "-Winline-asm"
#pragma clang diagnostic ignored "-Woverlength-strings"

typedef __hip_bfloat16 bf16;
#define DBGP ((unsigned*)nullptr)
constexpr int LDS_BYTES = 163840;
extern "C" __global__ __attribute__((aligned(4096))) __launch_bounds__(256, 1) void fa_kernel(const bf16* __restrict__ px, const bf16* __restrict__ ckv, const bf16* __restrict__ wq,
                                                    const bf16* __restrict__ wuk, const bf16* __restrict__ wuv, char* pws,
                                                    const bf16* __restrict__ pwo, const bf16* __restrict__ pwg, const bf16* __restrict__ pwu,
                                                    const bf16* __restrict__ pwd, const bf16* __restrict__ pwr, bf16* __restrict__ pout) {
    asm volatile(R"ASM(
.macro mm x:vararg
v_mfma_f32_16x16x32_bf16 \x
.endm
.macro rt x:vararg
ds_read_b64_tr_b16 \x
.endm
.macro rd x:vararg
ds_read_b128 \x
.endm
.macro ld x:vararg
buffer_load_dwordx4 \x
.endm
.macro st x:vararg
buffer_store_dwordx4 \x
.endm
.macro ar x:vararg
v_accvgpr_read_b32 \x
.endm
.macro aw x:vararg
v_accvgpr_write_b32 \x
.endm
.macro cv x:vararg
v_cvt_pk_bf16_f32 \x
.endm
.macro ex x:vararg
v_exp_f32 \x
.endm
.macro wt x:vararg
s_waitcnt \x
.endm
; ---- roles: head = 4 xcd + (wg >> 6), j = (wg >> 3) & 7; odd j: block j >> 1 (then helps with block 7 - (j >> 1)), even j: block 7 - (j >> 1). First operands, addresses
.La5_LSTART:
s_load_dwordx16 s[16:31], s[0:1], 0
s_load_dwordx8 s[32:39], s[0:1], 64
.p2align 6
v_mbcnt_lo_u32_b32 v64, -1, 0
v_mbcnt_hi_u32_b32 v64, -1, v64
s_getpc_b64 s[92:93]
.La5_LPC:
s_sub_u32 s48, s92, .La5_LPC-.La5_LSTART
s_subb_u32 s49, s93, 0
s_and_b32 s49, s49, 0xffff
s_mov_b32 s50, .La5_LCODEEND-.La5_LSTART
s_mov_b32 s51, 0x20000
s_lshr_b32 s97, s2, 3
s_lshl2_add_u32 s87, s97, s3
v_lshlrev_b32 v133, 7, v64
s_lshl_b32 s87, s87, 13
buffer_load_dword v254, v133, s[48:51], s87 offen
s_and_b32 s92, s2, 7
s_lshr_b32 s93, s2, 6
s_lshl2_add_u32 s72, s92, s93
s_bfe_u32 s73, s2, 0x30003
s_lshr_b32 s74, s73, 1
s_xor_b32 s92, s74, 7
s_and_b32 s75, s73, 1
s_cselect_b32 s76, s74, s92
s_lshr_b32 s77, s3, 1
s_and_b32 s78, s3, 1
v_and_b32 v65, 15, v64
v_lshrrev_b32 v66, 4, v64
v_lshlrev_b32 v255, 4, v64
v_lshlrev_b32 v67, 1, v66
v_xor_b32 v67, v67, v65
v_lshlrev_b32 v67, 4, v67
v_lshl_add_u32 v67, v66, 13, v67
s_lshl_b32 s94, s78, 17
v_add_u32 v232, s94, v67
v_add_u32 v233, 32768, v232
v_add_u32 v234, 65536, v232
v_add_u32 v235, 98304, v232
v_and_b32 v67, 7, v64
v_xor_b32 v68, v67, v66
v_lshlrev_b32 v68, 4, v68
v_lshrrev_b32 v69, 3, v64
v_lshl_add_u32 v68, v69, 10, v68
s_lshl2_add_u32 s94, s3, s93
s_lshl_b32 s82, s94, 11
s_lshl_b32 s94, s94, 14
v_add_u32 v224, s94, v68
v_xor_b32 v225, 0x2040, v224
v_xor_b32 v226, 16384, v224
v_xor_b32 v227, 16384, v225
v_xor_b32 v228, 32768, v224
v_xor_b32 v229, 32768, v225
v_xor_b32 v230, 49152, v224
v_xor_b32 v231, 49152, v225
s_mul_i32 s94, s77, 8448
s_mul_i32 s95, s78, 4224
s_add_u32 s83, s94, s95
s_add_u32 s83, s83, 96256
v_lshlrev_b32 v135, 7, v67
v_lshl_add_u32 v135, v69, 13, v135
v_lshrrev_b32 v132, 5, v64
v_lshl_add_u32 v132, v132, 28, v135
v_lshl_add_u32 v133, v66, 28, v133
v_lshlrev_b32 v134, 10, v65
v_lshl_add_u32 v134, v66, 7, v134
v_lshrrev_b32 v68, 4, v64
v_lshl_add_u32 v134, v68, 28, v134
s_lshl1_add_u32 s97, s97, s78
s_lshl_b32 s7, s97, 13
s_lshl_b32 s97, s97, 15
s_lshl_b32 s96, s93, 8
s_sub_u32 s7, s7, s96
s_sub_u32 s87, s87, s96
s_sub_u32 s97, s97, s96
s_add_u32 s86, s97, 2097152
s_lshl2_add_u32 s101, s72, s3
s_lshl_b32 s101, s101, 11
s_lshl2_add_u32 s100, s93, s3
s_lshl_b32 s100, s100, 14
v_lshlrev_b32 v118, 11, v67
s_lshl_b32 s93, s3, 9
v_lshl_add_u32 v119, v69, 4, v118
v_add_u32 v119, s93, v119
s_lshl_b32 s97, s2, 14
s_lshl_b32 s94, s72, 8
s_lshl_b32 s95, s76, 18
s_sub_u32 s46, 4194304, s94
s_mov_b32 s47, 0x20000
s_mov_b32 s42, 262144
s_mov_b32 s43, 0x20000
s_mov_b32 s62, 16384
s_mov_b32 s63, 0x20000
s_mov_b32 s79, 512
s_mov_b32 s80, 2097152
s_add_u32 s100, s100, 512
s_mov_b32 s81, 262144
s_cmp_eq_u32 s77, 0
wt lgkmcnt(0)
s_cselect_b64 s[92:93], s[22:23], s[24:25]
s_add_u32 s44, s92, s94
s_addc_u32 s45, s93, 0
s_and_b32 s45, s45, 0xffff
s_add_u32 s40, s18, s95
s_addc_u32 s41, s19, 0
s_and_b32 s41, s41, 0xffff
s_add_u32 s60, s16, s97
s_addc_u32 s61, s17, 0
s_and_b32 s61, s61, 0xffff
buffer_load_dword v254, v132, s[44:47], s86 offen
s_add_u32 s86, s86, 2097152
s_and_b32 s86, s86, 4194303
buffer_load_dword v254, v133, s[40:43], s101 offen
buffer_load_dword v254, v134, s[40:43], s100 offen
s_add_u32 m0, s83, 0
s_nop 0
ld v232,s[44:47],s80 offen lds
s_add_u32 m0, s83, 1024
s_nop 0
ld v233,s[44:47],s80 offen lds
s_add_u32 m0, s83, 2176
s_nop 0
ld v234,s[44:47],s80 offen lds
s_add_u32 m0, s83, 3200
s_nop 0
ld v235,s[44:47],s80 offen lds
s_add_u32 s80, s80, s81
s_and_b32 s80, s80, 4194303
v_lshrrev_b32 v67, 1, v65
v_xor_b32 v67, v67, v66
v_lshlrev_b32 v67, 4, v67
v_lshl_add_u32 v67, v65, 7, v67
s_lshl_b32 s92, s77, 12
v_add_u32 v236, s92, v67
v_xor_b32 v237, 64, v236
v_bfe_u32 v120, v64, 2, 2
v_add_u32 v67, 1, v66
v_lshrrev_b32 v67, 1, v67
v_lshlrev_b32 v67, 7, v67
v_lshl_add_u32 v67, v66, 11, v67
v_lshl_add_u32 v67, v120, 8, v67
v_and_b32 v68, 3, v64
v_lshl_add_u32 v67, v68, 3, v67
s_mul_i32 s92, s78, 8448
v_lshl_add_u32 v67, v120, 5, v67
v_add_u32 v67, s92, v67
v_add_u32 v240, 96256, v67
v_xor_b32 v241, 32, v240
v_xor_b32 v242, 64, v240
v_xor_b32 v243, 96, v240
s_add_u32 s64, s26, 142606336
s_addc_u32 s65, s27, 0
s_and_b32 s65, s65, 0xffff
s_mov_b32 s66, 3145728
s_mov_b32 s67, 0x20000
s_mov_b32 s88, 4096
s_lshr_b32 s92, s2, 1
s_lshl_b32 s92, s92, 15
s_and_b32 s93, s2, 1
s_lshl_b32 s93, s93, 7
s_add_u32 s92, s92, s93
s_lshl_b32 s93, s3, 13
s_add_u32 s92, s92, s93
s_add_u32 s92, s92, 88080384
s_add_u32 s56, s26, s92
s_addc_u32 s57, s27, 0
s_and_b32 s57, s57, 0xffff
s_mov_b32 s58, 8576
s_mov_b32 s59, 0x20000
v_and_b32 v71, 0x380, v255
v_and_b32 v127, 0x70, v255
v_lshl_add_u32 v71, v71, 1, v127
v_xor_b32 v67, 3, v69
v_min_u32 v67, 1, v67
v_xor_b32 v67, 1, v67
s_mov_b32 s93, 0
v_mul_lo_u32 v67, v67, s93
v_add_u32 v136, v67, v255
v_and_b32 v67, 3, v69
v_xor_b32 v67, 1, v67
v_min_u32 v67, 1, v67
v_xor_b32 v67, 1, v67
v_and_b32 v67, s2, v67
v_mul_lo_u32 v67, v67, s93
v_add_u32 v71, v71, v67
s_lshl_b32 s92, s3, 3
v_lshlrev_b32 v126, 1, v127
v_or_b32 v126, 65536, v126
v_add_u32 v127, s92, v126
s_add_u32 s52, s26, 171966464
s_addc_u32 s53, s27, 0
s_and_b32 s53, s53, 0xffff
s_mov_b32 s54, 950272
s_mov_b32 s55, 0x20000
s_lshl2_add_u32 s95, s2, s3
s_lshl_b32 s91, s95, 3
s_lshl_b32 s95, s95, 10
s_lshl_b32 s89, s76, 10
v_lshl_add_u32 v135, v69, 28, v135
s_mov_b32 s96, s87
s_mov_b32 s84, 33792
s_mov_b32 s85, 0
s_add_u32 s97, s83, 67584
s_mov_b32 s90, s83
s_lshl_b32 s92, s72, 8
s_sub_u32 s93, 8388608, s92
s_add_u32 s68, s20, s92
s_addc_u32 s69, s21, 0
s_and_b32 s69, s69, 0xffff
s_mov_b32 s70, s93
s_mov_b32 s71, 0x20000
s_lshr_b32 s92, s20, 7
s_sub_u32 s92, 3, s92
s_bfe_u32 s99, s92, 0x20001
s_lshr_b32 s93, s2, 6
s_sub_u32 s99, s99, s93
s_lshl_b32 s92, s99, 8
s_lshr_b32 s93, s2, 3
s_lshl2_add_u32 s93, s93, s3
s_lshl_b32 s98, s93, 16
s_add_u32 s98, s98, s92
v_lshlrev_b32 v138, 4, v65
v_lshl_add_u32 v137, v66, 13, v138
v_lshl_add_u32 v138, v66, 9, v138
s_lshl_b32 s93, s93, 12
v_add_u32 v138, s93, v138
s_and_b32 s93, s2, 7
s_lshl_b32 s92, s93, 19
s_add_u32 s92, s92, 138412032
s_add_u32 s4, s26, s92
s_addc_u32 s5, s27, 0
s_and_b32 s5, s5, 0xffff
s_mov_b32 s6, 0
s_mov_b32 s100, 1073741824
s_add_u32 s101, s91, 65536
v_lshrrev_b32 v139, 1, v64
v_lshlrev_b32 v139, 8, v139
v_and_or_b32 v139, v255, 16, v139
s_lshl_b32 s93, s93, 5
s_add_u32 s93, s93, 64512
v_add_u32 v139, s93, v139
s_mov_b64 exec, 0
s_cmp_eq_u32 s3, 0
s_cbranch_scc1 .La5_LW1_0
s_cmp_eq_u32 s3, 1
s_cbranch_scc1 .La5_LW1_1
s_cmp_eq_u32 s3, 2
s_cbranch_scc1 .La5_LW1_2
s_branch .La5_LW1_3
.La5_LW1_R0:
s_mov_b64 exec, -1
s_xor_b32 m0, s82, 0
s_nop 0
ld v224,s[40:43],s79 offen lds
s_xor_b32 m0, s82, 1024
s_nop 0
ld v225,s[40:43],s79 offen lds
s_xor_b32 m0, s82, 2048
s_nop 0
ld v226,s[40:43],s79 offen lds
s_xor_b32 m0, s82, 3072
s_nop 0
ld v227,s[40:43],s79 offen lds
s_xor_b32 m0, s82, 4096
s_nop 0
ld v228,s[40:43],s79 offen lds
s_xor_b32 m0, s82, 5120
s_nop 0
ld v229,s[40:43],s79 offen lds
s_xor_b32 m0, s82, 6144
s_nop 0
ld v230,s[40:43],s79 offen lds
s_xor_b32 m0, s82, 7168
s_nop 0
ld v231,s[40:43],s79 offen lds
s_add_u32 s79, s79, 128
s_and_b32 s79, s79, 1023
s_add_u32 m0, s83, 16896
s_nop 0
ld v232,s[44:47],s80 offen lds
s_add_u32 m0, s83, 17920
s_nop 0
ld v233,s[44:47],s80 offen lds
s_add_u32 m0, s83, 19072
s_nop 0
ld v234,s[44:47],s80 offen lds
s_add_u32 m0, s83, 20096
s_nop 0
ld v235,s[44:47],s80 offen lds
s_add_u32 s80, s80, s81
s_and_b32 s80, s80, 4194303
buffer_load_dwordx2 v[252:253], off, s[64:67], 0 sc1
wt vmcnt(1)
s_barrier
rd v[160:163],v236 offset:24576
rd v[164:167],v237 offset:24576
rt v[192:193],v240 offset:0
rt v[194:195],v240 offset:1024
rt v[196:197],v240 offset:16896
rt v[198:199],v240 offset:17920
rt v[200:201],v241 offset:0
rt v[202:203],v241 offset:1024
rt v[204:205],v241 offset:16896
rt v[206:207],v241 offset:17920
rt v[208:209],v242 offset:0
rt v[210:211],v242 offset:1024
rt v[212:213],v242 offset:16896
rt v[214:215],v242 offset:17920
rt v[216:217],v243 offset:0
rt v[218:219],v243 offset:1024
rt v[220:221],v243 offset:16896
rt v[222:223],v243 offset:17920
rd v[168:171],v236 offset:26624
rd v[172:175],v237 offset:26624
rd v[176:179],v236 offset:16384
rd v[180:183],v237 offset:16384
rd v[184:187],v236 offset:18432
rd v[188:191],v237 offset:18432
wt lgkmcnt(0)
s_cmp_eq_u32 s78, 1
s_cbranch_scc1 .La5_LV_START
; ---- K tile (waves 0, 2): 256 keys x 128 columns, K = 512; LayerNorm of 8 rows of x in the gaps (softmax scale folded in)
.La5_LK_START:
mm a[96:99],v[192:195],v[160:163],0
s_add_u32 m0, s83, 33792
mm a[96:99],v[196:199],v[164:167],a[96:99]
ld v232,s[44:47],s80 offen lds
mm a[100:103],v[204:207],v[164:167],0
rd v[0:3],v236 offset:8192
rd v[4:7],v237 offset:8192
s_add_u32 m0, s83, 34816
mm a[100:103],v[200:203],v[160:163],a[100:103]
rd v[8:11],v236 offset:10240
rd v[12:15],v237 offset:10240
ld v233,s[44:47],s80 offen lds
mm a[104:107],v[208:211],v[160:163],0
s_add_u32 m0, s83, 35968
mm a[104:107],v[212:215],v[164:167],a[104:107]
ld v234,s[44:47],s80 offen lds
mm a[108:111],v[220:223],v[164:167],0
rd v[16:19],v236 offset:0
rd v[20:23],v237 offset:0
s_add_u32 m0, s83, 36992
mm a[108:111],v[216:219],v[160:163],a[108:111]
rd v[24:27],v236 offset:2048
rd v[28:31],v237 offset:2048
ld v235,s[44:47],s80 offen lds
mm a[140:143],v[216:219],v[168:171],0
s_mov_b32 s80, 2883584
mm a[140:143],v[220:223],v[172:175],a[140:143]
mm a[136:139],v[212:215],v[172:175],0
s_xor_b32 m0, s82, 32768
mm a[136:139],v[208:211],v[168:171],a[136:139]
rt v[32:33],v240 offset:128
rt v[34:35],v240 offset:1152
ld v224,s[40:43],s79 offen lds
mm a[124:127],v[200:203],v[168:171],0
s_xor_b32 m0, s82, 33792
mm a[124:127],v[204:207],v[172:175],a[124:127]
ld v225,s[40:43],s79 offen lds
mm a[120:123],v[196:199],v[172:175],0
s_xor_b32 m0, s82, 34816
mm a[120:123],v[192:195],v[168:171],a[120:123]
rt v[36:37],v240 offset:17024
rt v[38:39],v240 offset:18048
ld v226,s[40:43],s79 offen lds
mm a[232:235],v[192:195],v[176:179],0
s_xor_b32 m0, s82, 35840
mm a[232:235],v[196:199],v[180:183],a[232:235]
ld v227,s[40:43],s79 offen lds
mm a[236:239],v[204:207],v[180:183],0
s_xor_b32 m0, s82, 36864
mm a[236:239],v[200:203],v[176:179],a[236:239]
rt v[40:41],v241 offset:128
rt v[42:43],v241 offset:1152
ld v228,s[40:43],s79 offen lds
mm a[152:155],v[208:211],v[176:179],0
s_xor_b32 m0, s82, 37888
mm a[152:155],v[212:215],v[180:183],a[152:155]
ld v229,s[40:43],s79 offen lds
mm a[156:159],v[220:223],v[180:183],0
s_xor_b32 m0, s82, 38912
mm a[156:159],v[216:219],v[176:179],a[156:159]
ld v230,s[40:43],s79 offen lds
mm a[172:175],v[216:219],v[184:187],0
s_xor_b32 m0, s82, 39936
mm a[172:175],v[220:223],v[188:191],a[172:175]
ld v231,s[40:43],s79 offen lds
mm a[168:171],v[212:215],v[188:191],0
s_mov_b32 s79, 768
mm a[168:171],v[208:211],v[184:187],a[168:171]
rt v[44:45],v241 offset:17024
rt v[46:47],v241 offset:18048
mm a[220:223],v[200:203],v[184:187],0
s_add_u32 m0, s83, 50688
mm a[220:223],v[204:207],v[188:191],a[220:223]
ld v232,s[44:47],s80 offen lds
mm a[216:219],v[196:199],v[188:191],0
wt lgkmcnt(8)
s_add_u32 m0, s83, 51712
mm a[216:219],v[192:195],v[184:187],a[216:219]
ld v233,s[44:47],s80 offen lds
mm a[200:203],v[192:195],v[0:3],0
s_add_u32 m0, s83, 52864
mm a[200:203],v[196:199],v[4:7],a[200:203]
ld v234,s[44:47],s80 offen lds
mm a[204:207],v[204:207],v[4:7],0
s_add_u32 m0, s83, 53888
mm a[204:207],v[200:203],v[0:3],a[204:207]
rt v[48:49],v242 offset:128
rt v[50:51],v242 offset:1152
ld v235,s[44:47],s80 offen lds
mm a[184:187],v[208:211],v[0:3],0
s_mov_b32 s80, 3145728
mm a[184:187],v[212:215],v[4:7],a[184:187]
mm a[188:191],v[220:223],v[4:7],0
mm a[188:191],v[216:219],v[0:3],a[188:191]
mm a[12:15],v[216:219],v[8:11],0
mm a[12:15],v[220:223],v[12:15],a[12:15]
buffer_load_dword v70, v118, s[60:63], 0 offen
mm a[8:11],v[212:215],v[12:15],0
mm a[8:11],v[208:211],v[8:11],a[8:11]
rt v[52:53],v242 offset:17024
rt v[54:55],v242 offset:18048
mm a[76:79],v[200:203],v[8:11],0
mm a[76:79],v[204:207],v[12:15],a[76:79]
ld v[72:75],v119,s[60:63],0 offen offset:0
mm a[72:75],v[196:199],v[12:15],0
mm a[72:75],v[192:195],v[8:11],a[72:75]
rt v[56:57],v243 offset:128
rt v[58:59],v243 offset:1152
mm a[88:91],v[192:195],v[16:19],0
mm a[88:91],v[196:199],v[20:23],a[88:91]
ld v[76:79],v119,s[60:63],0 offen offset:128
mm a[92:95],v[204:207],v[20:23],0
mm a[92:95],v[200:203],v[16:19],a[92:95]
rt v[60:61],v243 offset:17024
rt v[62:63],v243 offset:18048
mm a[24:27],v[208:211],v[16:19],0
mm a[24:27],v[212:215],v[20:23],a[24:27]
ld v[80:83],v119,s[60:63],0 offen offset:256
mm a[28:31],v[220:223],v[20:23],0
mm a[28:31],v[216:219],v[16:19],a[28:31]
mm a[44:47],v[216:219],v[24:27],0
mm a[44:47],v[220:223],v[28:31],a[44:47]
ld v[84:87],v119,s[60:63],0 offen offset:384
mm a[40:43],v[212:215],v[28:31],0
mm a[40:43],v[208:211],v[24:27],a[40:43]
mm a[252:255],v[200:203],v[24:27],0
mm a[252:255],v[204:207],v[28:31],a[252:255]
mm a[248:251],v[196:199],v[28:31],0
mm a[248:251],v[192:195],v[24:27],a[248:251]
s_mov_b64 exec, 0
s_cmp_eq_u32 s3, 0
s_cbranch_scc1 .La5_LW2_0
s_branch .La5_LW2_R0
.La5_LW2_R0:
s_mov_b64 exec, -1
wt vmcnt(5) lgkmcnt(0)
s_barrier
mm a[112:115],v[32:35],v[160:163],0
mm a[112:115],v[36:39],v[164:167],a[112:115]
mm a[116:119],v[44:47],v[164:167],0
rt v[192:193],v240 offset:33792
rt v[194:195],v240 offset:34816
mm a[116:119],v[40:43],v[160:163],a[116:119]
rt v[196:197],v240 offset:50688
rt v[198:199],v240 offset:51712
mm a[128:131],v[48:51],v[160:163],0
mm a[128:131],v[52:55],v[164:167],a[128:131]
mm a[132:135],v[60:63],v[164:167],0
mm a[132:135],v[56:59],v[160:163],a[132:135]
rd v[160:163],v236 offset:57344
rd v[164:167],v237 offset:57344
mm a[148:151],v[56:59],v[168:171],0
mm a[148:151],v[60:63],v[172:175],a[148:151]
mm a[144:147],v[52:55],v[172:175],0
mm a[144:147],v[48:51],v[168:171],a[144:147]
rt v[200:201],v241 offset:33792
rt v[202:203],v241 offset:34816
mm a[228:231],v[40:43],v[168:171],0
mm a[228:231],v[44:47],v[172:175],a[228:231]
mm a[224:227],v[36:39],v[172:175],0
mm a[224:227],v[32:35],v[168:171],a[224:227]
rd v[168:171],v236 offset:59392
rd v[172:175],v237 offset:59392
mm a[208:211],v[32:35],v[176:179],0
s_xor_b32 m0, s82, 0
mm a[208:211],v[36:39],v[180:183],a[208:211]
ld v224,s[40:43],s79 offen lds
mm a[212:215],v[44:47],v[180:183],0
mm a[212:215],v[40:43],v[176:179],a[212:215]
rt v[204:205],v241 offset:50688
rt v[206:207],v241 offset:51712
s_xor_b32 m0, s82, 1024
mm a[160:163],v[48:51],v[176:179],0
ld v225,s[40:43],s79 offen lds
mm a[160:163],v[52:55],v[180:183],a[160:163]
mm a[164:167],v[60:63],v[180:183],0
s_xor_b32 m0, s82, 2048
mm a[164:167],v[56:59],v[176:179],a[164:167]
rd v[176:179],v236 offset:49152
rd v[180:183],v237 offset:49152
ld v226,s[40:43],s79 offen lds
mm a[180:183],v[56:59],v[184:187],0
mm a[180:183],v[60:63],v[188:191],a[180:183]
s_xor_b32 m0, s82, 3072
mm a[176:179],v[52:55],v[188:191],0
ld v227,s[40:43],s79 offen lds
mm a[176:179],v[48:51],v[184:187],a[176:179]
rt v[208:209],v242 offset:33792
rt v[210:211],v242 offset:34816
mm a[196:199],v[40:43],v[184:187],0
s_xor_b32 m0, s82, 4096
mm a[196:199],v[44:47],v[188:191],a[196:199]
ld v228,s[40:43],s79 offen lds
mm a[192:195],v[36:39],v[188:191],0
mm a[192:195],v[32:35],v[184:187],a[192:195]
rd v[184:187],v236 offset:51200
rd v[188:191],v237 offset:51200
s_xor_b32 m0, s82, 5120
mm a[64:67],v[32:35],v[0:3],0
ld v229,s[40:43],s79 offen lds
mm a[64:67],v[36:39],v[4:7],a[64:67]
mm a[68:71],v[44:47],v[4:7],0
s_xor_b32 m0, s82, 6144
mm a[68:71],v[40:43],v[0:3],a[68:71]
rt v[212:213],v242 offset:50688
rt v[214:215],v242 offset:51712
ld v230,s[40:43],s79 offen lds
mm a[0:3],v[48:51],v[0:3],0
mm a[0:3],v[52:55],v[4:7],a[0:3]
s_xor_b32 m0, s82, 7168
mm a[4:7],v[60:63],v[4:7],0
ld v231,s[40:43],s79 offen lds
mm a[4:7],v[56:59],v[0:3],a[4:7]
rd v[0:3],v236 offset:40960
rd v[4:7],v237 offset:40960
s_mov_b32 s79, 896
mm a[20:23],v[56:59],v[8:11],0
mm a[20:23],v[60:63],v[12:15],a[20:23]
s_add_u32 m0, s83, 0
mm a[16:19],v[52:55],v[12:15],0
ld v232,s[44:47],s80 offen lds
mm a[16:19],v[48:51],v[8:11],a[16:19]
rt v[216:217],v243 offset:33792
rt v[218:219],v243 offset:34816
mm a[84:87],v[40:43],v[8:11],0
s_add_u32 m0, s83, 1024
mm a[84:87],v[44:47],v[12:15],a[84:87]
ld v233,s[44:47],s80 offen lds
mm a[80:83],v[36:39],v[12:15],0
wt vmcnt(10)
v_lshlrev_b32 v70, 16, v70
mm a[80:83],v[32:35],v[8:11],a[80:83]
rd v[8:11],v236 offset:43008
rd v[12:15],v237 offset:43008
s_add_u32 m0, s83, 2176
mm a[240:243],v[32:35],v[16:19],0
ld v234,s[44:47],s80 offen lds
mm a[240:243],v[36:39],v[20:23],a[240:243]
v_lshlrev_b32 v88, 16, v72
v_and_b32 v89, 0xffff0000, v72
mm a[244:247],v[44:47],v[20:23],0
s_add_u32 m0, s83, 3200
v_sub_f32 v88, v88, v70
mm a[244:247],v[40:43],v[16:19],a[244:247]
rt v[220:221],v243 offset:50688
rt v[222:223],v243 offset:51712
ld v235,s[44:47],s80 offen lds
mm a[32:35],v[48:51],v[16:19],0
s_mov_b32 s80, 3407872
v_sub_f32 v89, v89, v70
mm a[32:35],v[52:55],v[20:23],a[32:35]
v_add_f32 v120, v88, v89
v_mul_f32 v122, v88, v88
mm a[36:39],v[60:63],v[20:23],0
s_add_u32 m0, s83, 16896
v_mul_f32 v123, v89, v89
mm a[36:39],v[56:59],v[16:19],a[36:39]
rd v[16:19],v236 offset:32768
rd v[20:23],v237 offset:32768
ld v232,s[44:47],s80 offen lds
mm a[60:63],v[56:59],v[24:27],0
v_lshlrev_b32 v90, 16, v73
v_and_b32 v91, 0xffff0000, v73
mm a[60:63],v[60:63],v[28:31],a[60:63]
s_add_u32 m0, s83, 17920
v_sub_f32 v90, v90, v70
mm a[56:59],v[52:55],v[28:31],0
ld v233,s[44:47],s80 offen lds
mm a[56:59],v[48:51],v[24:27],a[56:59]
v_sub_f32 v91, v91, v70
v_add_f32 v124, v90, v91
mm a[52:55],v[40:43],v[24:27],0
s_add_u32 m0, s83, 19072
v_fmac_f32 v122, v90, v90
mm a[52:55],v[44:47],v[28:31],a[52:55]
ld v234,s[44:47],s80 offen lds
mm a[48:51],v[36:39],v[28:31],0
wt lgkmcnt(2)
v_add_f32 v120, v120, v124
mm a[48:51],v[32:35],v[24:27],a[48:51]
rd v[24:27],v236 offset:34816
rd v[28:31],v237 offset:34816
s_add_u32 m0, s83, 20096
mm a[96:99],v[192:195],v[160:163],a[96:99]
ld v235,s[44:47],s80 offen lds
.La5_LW1_0:
s_cbranch_execz .La5_LW1_4
mm a[96:99],v[196:199],v[164:167],a[96:99]
s_mov_b32 s80, 3670016
v_fmac_f32 v123, v91, v91
mm a[100:103],v[204:207],v[164:167],a[100:103]
v_lshlrev_b32 v92, 16, v74
v_and_b32 v93, 0xffff0000, v74
mm a[100:103],v[200:203],v[160:163],a[100:103]
rt v[32:33],v240 offset:33920
rt v[34:35],v240 offset:34944
mm a[104:107],v[208:211],v[160:163],a[104:107]
v_sub_f32 v92, v92, v70
v_sub_f32 v93, v93, v70
mm a[104:107],v[212:215],v[164:167],a[104:107]
v_add_f32 v124, v92, v93
v_fmac_f32 v122, v92, v92
mm a[108:111],v[220:223],v[164:167],a[108:111]
wt lgkmcnt(2)
s_barrier
mm a[108:111],v[216:219],v[160:163],a[108:111]
v_add_f32 v120, v120, v124
v_fmac_f32 v123, v93, v93
mm a[140:143],v[216:219],v[168:171],a[140:143]
s_xor_b32 m0, s82, 32768
v_lshlrev_b32 v94, 16, v75
mm a[140:143],v[220:223],v[172:175],a[140:143]
ld v224,s[40:43],s79 offen lds
mm a[136:139],v[212:215],v[172:175],a[136:139]
v_and_b32 v95, 0xffff0000, v75
v_sub_f32 v94, v94, v70
mm a[136:139],v[208:211],v[168:171],a[136:139]
rt v[36:37],v240 offset:50816
rt v[38:39],v240 offset:51840
mm a[124:127],v[200:203],v[168:171],a[124:127]
s_xor_b32 m0, s82, 33792
v_sub_f32 v95, v95, v70
mm a[124:127],v[204:207],v[172:175],a[124:127]
ld v225,s[40:43],s79 offen lds
mm a[120:123],v[196:199],v[172:175],a[120:123]
v_add_f32 v124, v94, v95
v_fmac_f32 v122, v94, v94
mm a[120:123],v[192:195],v[168:171],a[120:123]
v_add_f32 v120, v120, v124
v_fmac_f32 v123, v95, v95
mm a[232:235],v[192:195],v[176:179],a[232:235]
s_xor_b32 m0, s82, 34816
v_lshlrev_b32 v96, 16, v76
mm a[232:235],v[196:199],v[180:183],a[232:235]
ld v226,s[40:43],s79 offen lds
mm a[236:239],v[204:207],v[180:183],a[236:239]
v_and_b32 v97, 0xffff0000, v76
v_sub_f32 v96, v96, v70
mm a[236:239],v[200:203],v[176:179],a[236:239]
rt v[40:41],v241 offset:33920
rt v[42:43],v241 offset:34944
mm a[152:155],v[208:211],v[176:179],a[152:155]
s_xor_b32 m0, s82, 35840
v_sub_f32 v97, v97, v70
mm a[152:155],v[212:215],v[180:183],a[152:155]
ld v227,s[40:43],s79 offen lds
mm a[156:159],v[220:223],v[180:183],a[156:159]
v_add_f32 v124, v96, v97
v_fmac_f32 v122, v96, v96
mm a[156:159],v[216:219],v[176:179],a[156:159]
v_add_f32 v120, v120, v124
v_fmac_f32 v123, v97, v97
mm a[172:175],v[216:219],v[184:187],a[172:175]
s_xor_b32 m0, s82, 36864
v_lshlrev_b32 v98, 16, v77
mm a[172:175],v[220:223],v[188:191],a[172:175]
ld v228,s[40:43],s79 offen lds
mm a[168:171],v[212:215],v[188:191],a[168:171]
v_and_b32 v99, 0xffff0000, v77
v_sub_f32 v98, v98, v70
mm a[168:171],v[208:211],v[184:187],a[168:171]
rt v[44:45],v241 offset:50816
rt v[46:47],v241 offset:51840
mm a[220:223],v[200:203],v[184:187],a[220:223]
s_xor_b32 m0, s82, 37888
v_sub_f32 v99, v99, v70
mm a[220:223],v[204:207],v[188:191],a[220:223]
ld v229,s[40:43],s79 offen lds
mm a[216:219],v[196:199],v[188:191],a[216:219]
v_add_f32 v124, v98, v99
v_fmac_f32 v122, v98, v98
mm a[216:219],v[192:195],v[184:187],a[216:219]
v_add_f32 v120, v120, v124
v_fmac_f32 v123, v99, v99
mm a[200:203],v[192:195],v[0:3],a[200:203]
s_xor_b32 m0, s82, 38912
v_lshlrev_b32 v100, 16, v78
mm a[200:203],v[196:199],v[4:7],a[200:203]
ld v230,s[40:43],s79 offen lds
mm a[204:207],v[204:207],v[4:7],a[204:207]
v_and_b32 v101, 0xffff0000, v78
v_sub_f32 v100, v100, v70
mm a[204:207],v[200:203],v[0:3],a[204:207]
rt v[48:49],v242 offset:33920
rt v[50:51],v242 offset:34944
mm a[184:187],v[208:211],v[0:3],a[184:187]
s_xor_b32 m0, s82, 39936
v_sub_f32 v101, v101, v70
mm a[184:187],v[212:215],v[4:7],a[184:187]
ld v231,s[40:43],s79 offen lds
mm a[188:191],v[220:223],v[4:7],a[188:191]
s_mov_b32 s79, 0
v_add_f32 v124, v100, v101
mm a[188:191],v[216:219],v[0:3],a[188:191]
v_fmac_f32 v122, v100, v100
v_add_f32 v120, v120, v124
mm a[12:15],v[216:219],v[8:11],a[12:15]
v_fmac_f32 v123, v101, v101
v_lshlrev_b32 v102, 16, v79
mm a[12:15],v[220:223],v[12:15],a[12:15]
buffer_load_dword v254, v132, s[44:47], s86 offen
mm a[8:11],v[212:215],v[12:15],a[8:11]
s_add_u32 s86, s86, 2097152
s_and_b32 s86, s86, 4194303
mm a[8:11],v[208:211],v[8:11],a[8:11]
rt v[52:53],v242 offset:50816
rt v[54:55],v242 offset:51840
mm a[76:79],v[200:203],v[8:11],a[76:79]
v_and_b32 v103, 0xffff0000, v79
v_sub_f32 v102, v102, v70
mm a[76:79],v[204:207],v[12:15],a[76:79]
v_sub_f32 v103, v103, v70
v_add_f32 v124, v102, v103
mm a[72:75],v[196:199],v[12:15],a[72:75]
v_fmac_f32 v122, v102, v102
v_add_f32 v120, v120, v124
mm a[72:75],v[192:195],v[8:11],a[72:75]
rt v[56:57],v243 offset:33920
rt v[58:59],v243 offset:34944
mm a[88:91],v[192:195],v[16:19],a[88:91]
v_fmac_f32 v123, v103, v103
v_lshlrev_b32 v104, 16, v80
mm a[88:91],v[196:199],v[20:23],a[88:91]
v_and_b32 v105, 0xffff0000, v80
v_sub_f32 v104, v104, v70
mm a[92:95],v[204:207],v[20:23],a[92:95]
v_sub_f32 v105, v105, v70
v_add_f32 v124, v104, v105
mm a[92:95],v[200:203],v[16:19],a[92:95]
rt v[60:61],v243 offset:50816
rt v[62:63],v243 offset:51840
mm a[24:27],v[208:211],v[16:19],a[24:27]
v_fmac_f32 v122, v104, v104
v_add_f32 v120, v120, v124
mm a[24:27],v[212:215],v[20:23],a[24:27]
v_fmac_f32 v123, v105, v105
v_lshlrev_b32 v106, 16, v81
mm a[28:31],v[220:223],v[20:23],a[28:31]
v_and_b32 v107, 0xffff0000, v81
v_sub_f32 v106, v106, v70
mm a[28:31],v[216:219],v[16:19],a[28:31]
v_sub_f32 v107, v107, v70
v_add_f32 v124, v106, v107
mm a[44:47],v[216:219],v[24:27],a[44:47]
v_fmac_f32 v122, v106, v106
v_add_f32 v120, v120, v124
mm a[44:47],v[220:223],v[28:31],a[44:47]
v_fmac_f32 v123, v107, v107
v_lshlrev_b32 v108, 16, v82
mm a[40:43],v[212:215],v[28:31],a[40:43]
v_and_b32 v109, 0xffff0000, v82
v_sub_f32 v108, v108, v70
mm a[40:43],v[208:211],v[24:27],a[40:43]
v_sub_f32 v109, v109, v70
v_add_f32 v124, v108, v109
mm a[252:255],v[200:203],v[24:27],a[252:255]
v_fmac_f32 v122, v108, v108
v_add_f32 v120, v120, v124
mm a[252:255],v[204:207],v[28:31],a[252:255]
v_fmac_f32 v123, v109, v109
v_lshlrev_b32 v110, 16, v83
mm a[248:251],v[196:199],v[28:31],a[248:251]
wt vmcnt(9) lgkmcnt(0)
s_barrier
mm a[248:251],v[192:195],v[24:27],a[248:251]
v_and_b32 v111, 0xffff0000, v83
v_sub_f32 v110, v110, v70
mm a[112:115],v[32:35],v[160:163],a[112:115]
v_sub_f32 v111, v111, v70
v_add_f32 v124, v110, v111
mm a[112:115],v[36:39],v[164:167],a[112:115]
v_fmac_f32 v122, v110, v110
v_add_f32 v120, v120, v124
mm a[116:119],v[44:47],v[164:167],a[116:119]
rt v[192:193],v240 offset:0
rt v[194:195],v240 offset:1024
mm a[116:119],v[40:43],v[160:163],a[116:119]
rt v[196:197],v240 offset:16896
rt v[198:199],v240 offset:17920
mm a[128:131],v[48:51],v[160:163],a[128:131]
v_fmac_f32 v123, v111, v111
v_lshlrev_b32 v112, 16, v84
mm a[128:131],v[52:55],v[164:167],a[128:131]
v_and_b32 v113, 0xffff0000, v84
v_sub_f32 v112, v112, v70
mm a[132:135],v[60:63],v[164:167],a[132:135]
v_sub_f32 v113, v113, v70
v_add_f32 v124, v112, v113
mm a[132:135],v[56:59],v[160:163],a[132:135]
rd v[160:163],v236 offset:24576
rd v[164:167],v237 offset:24576
mm a[148:151],v[56:59],v[168:171],a[148:151]
v_fmac_f32 v122, v112, v112
v_add_f32 v120, v120, v124
mm a[148:151],v[60:63],v[172:175],a[148:151]
v_fmac_f32 v123, v113, v113
v_lshlrev_b32 v114, 16, v85
mm a[144:147],v[52:55],v[172:175],a[144:147]
v_and_b32 v115, 0xffff0000, v85
v_sub_f32 v114, v114, v70
mm a[144:147],v[48:51],v[168:171],a[144:147]
rt v[200:201],v241 offset:0
rt v[202:203],v241 offset:1024
mm a[228:231],v[40:43],v[168:171],a[228:231]
v_sub_f32 v115, v115, v70
v_add_f32 v124, v114, v115
mm a[228:231],v[44:47],v[172:175],a[228:231]
v_fmac_f32 v122, v114, v114
v_add_f32 v120, v120, v124
mm a[224:227],v[36:39],v[172:175],a[224:227]
v_fmac_f32 v123, v115, v115
v_lshlrev_b32 v116, 16, v86
mm a[224:227],v[32:35],v[168:171],a[224:227]
rd v[168:171],v236 offset:26624
rd v[172:175],v237 offset:26624
mm a[208:211],v[32:35],v[176:179],a[208:211]
v_and_b32 v117, 0xffff0000, v86
v_sub_f32 v116, v116, v70
mm a[208:211],v[36:39],v[180:183],a[208:211]
v_sub_f32 v117, v117, v70
v_add_f32 v124, v116, v117
mm a[212:215],v[44:47],v[180:183],a[212:215]
v_fmac_f32 v122, v116, v116
v_add_f32 v120, v120, v124
mm a[212:215],v[40:43],v[176:179],a[212:215]
rt v[204:205],v241 offset:16896
rt v[206:207],v241 offset:17920
mm a[160:163],v[48:51],v[176:179],a[160:163]
v_fmac_f32 v123, v117, v117
v_lshlrev_b32 v118, 16, v87
mm a[160:163],v[52:55],v[180:183],a[160:163]
v_and_b32 v119, 0xffff0000, v87
v_sub_f32 v118, v118, v70
mm a[164:167],v[60:63],v[180:183],a[164:167]
v_sub_f32 v119, v119, v70
v_add_f32 v124, v118, v119
mm a[164:167],v[56:59],v[176:179],a[164:167]
rd v[176:179],v236 offset:16384
rd v[180:183],v237 offset:16384
mm a[180:183],v[56:59],v[184:187],a[180:183]
v_fmac_f32 v122, v118, v118
v_add_f32 v120, v120, v124
mm a[180:183],v[60:63],v[188:191],a[180:183]
v_fmac_f32 v123, v119, v119
v_add_f32 v121, v122, v123
mm a[176:179],v[52:55],v[188:191],a[176:179]
v_add_f32_dpp v120, v120, v120 row_ror:8 row_mask:0xf bank_mask:0xf
mm a[176:179],v[48:51],v[184:187],a[176:179]
rt v[208:209],v242 offset:0
rt v[210:211],v242 offset:1024
mm a[196:199],v[40:43],v[184:187],a[196:199]
s_add_u32 m0, s83, 33792
v_add_f32_dpp v121, v121, v121 row_ror:8 row_mask:0xf bank_mask:0xf
mm a[196:199],v[44:47],v[188:191],a[196:199]
ld v232,s[44:47],s80 offen lds
mm a[192:195],v[36:39],v[188:191],a[192:195]
v_mov_b32 v124, v120
v_mov_b32 v125, v121
mm a[192:195],v[32:35],v[184:187],a[192:195]
rd v[184:187],v236 offset:18432
rd v[188:191],v237 offset:18432
mm a[64:67],v[32:35],v[0:3],a[64:67]
s_add_u32 m0, s83, 34816
v_permlane16_swap_b32 v124, v120
mm a[64:67],v[36:39],v[4:7],a[64:67]
ld v233,s[44:47],s80 offen lds
mm a[68:71],v[44:47],v[4:7],a[68:71]
v_permlane16_swap_b32 v125, v121
mm a[68:71],v[40:43],v[0:3],a[68:71]
rt v[212:213],v242 offset:16896
rt v[214:215],v242 offset:17920
mm a[0:3],v[48:51],v[0:3],a[0:3]
s_add_u32 m0, s83, 35968
v_add_f32 v120, v120, v124
mm a[0:3],v[52:55],v[4:7],a[0:3]
ld v234,s[44:47],s80 offen lds
mm a[4:7],v[60:63],v[4:7],a[4:7]
v_add_f32 v121, v121, v125
v_mov_b32 v124, v120
mm a[4:7],v[56:59],v[0:3],a[4:7]
rd v[0:3],v236 offset:8192
rd v[4:7],v237 offset:8192
mm a[20:23],v[56:59],v[8:11],a[20:23]
s_add_u32 m0, s83, 36992
v_mov_b32 v125, v121
mm a[20:23],v[60:63],v[12:15],a[20:23]
ld v235,s[44:47],s80 offen lds
mm a[16:19],v[52:55],v[12:15],a[16:19]
s_mov_b32 s80, 3932160
v_permlane32_swap_b32 v124, v120
mm a[16:19],v[48:51],v[8:11],a[16:19]
rt v[216:217],v243 offset:0
rt v[218:219],v243 offset:1024
mm a[84:87],v[40:43],v[8:11],a[84:87]
s_add_u32 m0, s83, 50688
v_permlane32_swap_b32 v125, v121
mm a[84:87],v[44:47],v[12:15],a[84:87]
ld v232,s[44:47],s80 offen lds
mm a[80:83],v[36:39],v[12:15],a[80:83]
v_add_f32 v120, v120, v124
v_add_f32 v121, v121, v125
mm a[80:83],v[32:35],v[8:11],a[80:83]
rd v[8:11],v236 offset:10240
rd v[12:15],v237 offset:10240
mm a[240:243],v[32:35],v[16:19],a[240:243]
s_add_u32 m0, s83, 51712
ds_write_b64 v127, v[120:121]
mm a[240:243],v[36:39],v[20:23],a[240:243]
ld v233,s[44:47],s80 offen lds
mm a[244:247],v[44:47],v[20:23],a[244:247]
s_nop 0
v_readfirstlane_b32 s92, v252
mm a[244:247],v[40:43],v[16:19],a[244:247]
rt v[220:221],v243 offset:16896
rt v[222:223],v243 offset:17920
mm a[32:35],v[48:51],v[16:19],a[32:35]
s_add_u32 m0, s83, 52864
v_readfirstlane_b32 s93, v253
mm a[32:35],v[52:55],v[20:23],a[32:35]
ld v234,s[44:47],s80 offen lds
mm a[36:39],v[60:63],v[20:23],a[36:39]
s_add_u32 s92, s92, 1
s_addc_u32 s93, s93, 0
mm a[36:39],v[56:59],v[16:19],a[36:39]
rd v[16:19],v236 offset:0
rd v[20:23],v237 offset:0
mm a[60:63],v[56:59],v[24:27],a[60:63]
s_add_u32 m0, s83, 53888
v_mov_b32 v252, s92
mm a[60:63],v[60:63],v[28:31],a[60:63]
ld v235,s[44:47],s80 offen lds
mm a[56:59],v[52:55],v[28:31],a[56:59]
s_mov_b32 s80, 0
v_mov_b32 v253, s93
mm a[56:59],v[48:51],v[24:27],a[56:59]
v_mov_b32 v128, 0
v_mov_b32 v129, 0
mm a[52:55],v[40:43],v[24:27],a[52:55]
v_mov_b32 v130, 0
v_mov_b32 v131, 0
mm a[52:55],v[44:47],v[28:31],a[52:55]
st v[128:131],v255,s[52:55],s95 offen sc1
mm a[48:51],v[36:39],v[28:31],a[48:51]
wt lgkmcnt(2)
mm a[48:51],v[32:35],v[24:27],a[48:51]
rd v[24:27],v236 offset:2048
rd v[28:31],v237 offset:2048
mm a[96:99],v[192:195],v[160:163],a[96:99]
mm a[96:99],v[196:199],v[164:167],a[96:99]
mm a[100:103],v[204:207],v[164:167],a[100:103]
mm a[100:103],v[200:203],v[160:163],a[100:103]
rt v[32:33],v240 offset:128
rt v[34:35],v240 offset:1152
mm a[104:107],v[208:211],v[160:163],a[104:107]
mm a[104:107],v[212:215],v[164:167],a[104:107]
mm a[108:111],v[220:223],v[164:167],a[108:111]
wt lgkmcnt(2)
s_barrier
mm a[108:111],v[216:219],v[160:163],a[108:111]
rd v[72:75],v126
rd v[76:79],v126 offset:16
mm a[140:143],v[216:219],v[168:171],a[140:143]
s_xor_b32 m0, s82, 0
mm a[140:143],v[220:223],v[172:175],a[140:143]
ld v224,s[40:43],s79 offen lds
mm a[136:139],v[212:215],v[172:175],a[136:139]
mm a[136:139],v[208:211],v[168:171],a[136:139]
rt v[36:37],v240 offset:17024
rt v[38:39],v240 offset:18048
mm a[124:127],v[200:203],v[168:171],a[124:127]
s_xor_b32 m0, s82, 1024
mm a[124:127],v[204:207],v[172:175],a[124:127]
ld v225,s[40:43],s79 offen lds
mm a[120:123],v[196:199],v[172:175],a[120:123]
wt lgkmcnt(2)
v_add_f32 v72, v72, v74
mm a[120:123],v[192:195],v[168:171],a[120:123]
v_add_f32 v73, v73, v75
v_add_f32 v76, v76, v78
mm a[232:235],v[192:195],v[176:179],a[232:235]
s_xor_b32 m0, s82, 2048
v_add_f32 v77, v77, v79
mm a[232:235],v[196:199],v[180:183],a[232:235]
ld v226,s[40:43],s79 offen lds
mm a[236:239],v[204:207],v[180:183],a[236:239]
v_add_f32 v72, v72, v76
v_add_f32 v73, v73, v77
mm a[236:239],v[200:203],v[176:179],a[236:239]
rt v[40:41],v241 offset:128
rt v[42:43],v241 offset:1152
mm a[152:155],v[208:211],v[176:179],a[152:155]
s_xor_b32 m0, s82, 3072
v_mul_f32 v72, 0x3a800000, v72
mm a[152:155],v[212:215],v[180:183],a[152:155]
ld v227,s[40:43],s79 offen lds
mm a[156:159],v[220:223],v[180:183],a[156:159]
v_mul_f32 v73, 0x3a800000, v73
v_add_f32 v124, v70, v72
mm a[156:159],v[216:219],v[176:179],a[156:159]
v_fma_f32 v125, -v72, v72, v73
cv v124,v124,v124
mm a[172:175],v[216:219],v[184:187],a[172:175]
s_xor_b32 m0, s82, 4096
v_lshlrev_b32 v124, 16, v124
mm a[172:175],v[220:223],v[188:191],a[172:175]
ld v228,s[40:43],s79 offen lds
mm a[168:171],v[212:215],v[188:191],a[168:171]
v_max_f32 v125, 0, v125
cv v125,v125,v125
mm a[168:171],v[208:211],v[184:187],a[168:171]
rt v[44:45],v241 offset:17024
rt v[46:47],v241 offset:18048
mm a[220:223],v[200:203],v[184:187],a[220:223]
s_xor_b32 m0, s82, 5120
v_lshlrev_b32 v125, 16, v125
mm a[220:223],v[204:207],v[188:191],a[220:223]
ld v229,s[40:43],s79 offen lds
mm a[216:219],v[196:199],v[188:191],a[216:219]
v_add_f32 v125, 0x3727c5ac, v125
cv v125,v125,v125
mm a[216:219],v[192:195],v[184:187],a[216:219]
v_lshlrev_b32 v125, 16, v125
mm a[200:203],v[192:195],v[0:3],a[200:203]
s_xor_b32 m0, s82, 6144
mm a[200:203],v[196:199],v[4:7],a[200:203]
ld v230,s[40:43],s79 offen lds
mm a[204:207],v[204:207],v[4:7],a[204:207]
v_sqrt_f32 v125, v125
mm a[204:207],v[200:203],v[0:3],a[204:207]
rt v[48:49],v242 offset:128
rt v[50:51],v242 offset:1152
mm a[184:187],v[208:211],v[0:3],a[184:187]
s_xor_b32 m0, s82, 7168
v_sub_f32 v124, v70, v124
mm a[184:187],v[212:215],v[4:7],a[184:187]
ld v231,s[40:43],s79 offen lds
mm a[188:191],v[220:223],v[4:7],a[188:191]
s_mov_b32 s79, 128
cv v125,v125,v125
mm a[188:191],v[216:219],v[0:3],a[188:191]
v_lshlrev_b32 v125, 16, v125
mm a[12:15],v[216:219],v[8:11],a[12:15]
v_rcp_f32 v125, v125
mm a[12:15],v[220:223],v[12:15],a[12:15]
v_mul_f32 v125, 0x3e0293ee, v125
v_mul_f32 v124, v124, v125
mm a[8:11],v[212:215],v[12:15],a[8:11]
v_fma_f32 v88, v88, v125, v124
v_fma_f32 v89, v89, v125, v124
mm a[8:11],v[208:211],v[8:11],a[8:11]
rt v[52:53],v242 offset:17024
rt v[54:55],v242 offset:18048
mm a[76:79],v[200:203],v[8:11],a[76:79]
cv v72,v88,v89
v_fma_f32 v90, v90, v125, v124
mm a[76:79],v[204:207],v[12:15],a[76:79]
v_fma_f32 v91, v91, v125, v124
cv v73,v90,v91
mm a[72:75],v[196:199],v[12:15],a[72:75]
v_fma_f32 v92, v92, v125, v124
v_fma_f32 v93, v93, v125, v124
mm a[72:75],v[192:195],v[8:11],a[72:75]
rt v[56:57],v243 offset:128
rt v[58:59],v243 offset:1152
mm a[88:91],v[192:195],v[16:19],a[88:91]
cv v74,v92,v93
v_fma_f32 v94, v94, v125, v124
mm a[88:91],v[196:199],v[20:23],a[88:91]
v_fma_f32 v95, v95, v125, v124
cv v75,v94,v95
mm a[92:95],v[204:207],v[20:23],a[92:95]
st v[72:75],v71,s[56:59],0 offen offset:0 sc1
mm a[92:95],v[200:203],v[16:19],a[92:95]
rt v[60:61],v243 offset:17024
rt v[62:63],v243 offset:18048
mm a[24:27],v[208:211],v[16:19],a[24:27]
v_fma_f32 v96, v96, v125, v124
v_fma_f32 v97, v97, v125, v124
mm a[24:27],v[212:215],v[20:23],a[24:27]
cv v76,v96,v97
v_fma_f32 v98, v98, v125, v124
mm a[28:31],v[220:223],v[20:23],a[28:31]
v_fma_f32 v99, v99, v125, v124
cv v77,v98,v99
mm a[28:31],v[216:219],v[16:19],a[28:31]
v_fma_f32 v100, v100, v125, v124
v_fma_f32 v101, v101, v125, v124
mm a[44:47],v[216:219],v[24:27],a[44:47]
cv v78,v100,v101
v_fma_f32 v102, v102, v125, v124
mm a[44:47],v[220:223],v[28:31],a[44:47]
v_fma_f32 v103, v103, v125, v124
cv v79,v102,v103
mm a[40:43],v[212:215],v[28:31],a[40:43]
st v[76:79],v71,s[56:59],0 offen offset:2048 sc1
mm a[40:43],v[208:211],v[24:27],a[40:43]
v_fma_f32 v104, v104, v125, v124
v_fma_f32 v105, v105, v125, v124
mm a[252:255],v[200:203],v[24:27],a[252:255]
cv v80,v104,v105
v_fma_f32 v106, v106, v125, v124
mm a[252:255],v[204:207],v[28:31],a[252:255]
v_fma_f32 v107, v107, v125, v124
cv v81,v106,v107
mm a[248:251],v[196:199],v[28:31],a[248:251]
wt vmcnt(11) lgkmcnt(0)
s_barrier
mm a[248:251],v[192:195],v[24:27],a[248:251]
v_fma_f32 v108, v108, v125, v124
v_fma_f32 v109, v109, v125, v124
mm a[112:115],v[32:35],v[160:163],a[112:115]
cv v82,v108,v109
v_fma_f32 v110, v110, v125, v124
mm a[112:115],v[36:39],v[164:167],a[112:115]
v_fma_f32 v111, v111, v125, v124
cv v83,v110,v111
mm a[116:119],v[44:47],v[164:167],a[116:119]
rt v[192:193],v240 offset:33792
rt v[194:195],v240 offset:34816
mm a[116:119],v[40:43],v[160:163],a[116:119]
rt v[196:197],v240 offset:50688
rt v[198:199],v240 offset:51712
mm a[128:131],v[48:51],v[160:163],a[128:131]
st v[80:83],v71,s[56:59],s88 offen offset:0 sc1
mm a[128:131],v[52:55],v[164:167],a[128:131]
v_fma_f32 v112, v112, v125, v124
v_fma_f32 v113, v113, v125, v124
mm a[132:135],v[60:63],v[164:167],a[132:135]
cv v84,v112,v113
v_fma_f32 v114, v114, v125, v124
mm a[132:135],v[56:59],v[160:163],a[132:135]
rd v[160:163],v236 offset:57344
rd v[164:167],v237 offset:57344
mm a[148:151],v[56:59],v[168:171],a[148:151]
v_fma_f32 v115, v115, v125, v124
cv v85,v114,v115
mm a[148:151],v[60:63],v[172:175],a[148:151]
v_fma_f32 v116, v116, v125, v124
v_fma_f32 v117, v117, v125, v124
mm a[144:147],v[52:55],v[172:175],a[144:147]
cv v86,v116,v117
v_fma_f32 v118, v118, v125, v124
mm a[144:147],v[48:51],v[168:171],a[144:147]
rt v[200:201],v241 offset:33792
rt v[202:203],v241 offset:34816
mm a[228:231],v[40:43],v[168:171],a[228:231]
v_fma_f32 v119, v119, v125, v124
cv v87,v118,v119
mm a[228:231],v[44:47],v[172:175],a[228:231]
st v[84:87],v71,s[56:59],s88 offen offset:2048 sc1
mm a[224:227],v[36:39],v[172:175],a[224:227]
mm a[224:227],v[32:35],v[168:171],a[224:227]
rd v[168:171],v236 offset:59392
rd v[172:175],v237 offset:59392
mm a[208:211],v[32:35],v[176:179],a[208:211]
mm a[208:211],v[36:39],v[180:183],a[208:211]
mm a[212:215],v[44:47],v[180:183],a[212:215]
mm a[212:215],v[40:43],v[176:179],a[212:215]
rt v[204:205],v241 offset:50688
rt v[206:207],v241 offset:51712
mm a[160:163],v[48:51],v[176:179],a[160:163]
mm a[160:163],v[52:55],v[180:183],a[160:163]
mm a[164:167],v[60:63],v[180:183],a[164:167]
mm a[164:167],v[56:59],v[176:179],a[164:167]
rd v[176:179],v236 offset:49152
rd v[180:183],v237 offset:49152
mm a[180:183],v[56:59],v[184:187],a[180:183]
mm a[180:183],v[60:63],v[188:191],a[180:183]
mm a[176:179],v[52:55],v[188:191],a[176:179]
mm a[176:179],v[48:51],v[184:187],a[176:179]
rt v[208:209],v242 offset:33792
rt v[210:211],v242 offset:34816
mm a[196:199],v[40:43],v[184:187],a[196:199]
s_add_u32 m0, s83, 0
mm a[196:199],v[44:47],v[188:191],a[196:199]
.La5_LW1_1:
s_cbranch_execz .La5_LW1_5
ld v232,s[44:47],s80 offen lds
mm a[192:195],v[36:39],v[188:191],a[192:195]
mm a[192:195],v[32:35],v[184:187],a[192:195]
rd v[184:187],v236 offset:51200
rd v[188:191],v237 offset:51200
mm a[64:67],v[32:35],v[0:3],a[64:67]
s_add_u32 m0, s83, 1024
mm a[64:67],v[36:39],v[4:7],a[64:67]
ld v233,s[44:47],s80 offen lds
mm a[68:71],v[44:47],v[4:7],a[68:71]
mm a[68:71],v[40:43],v[0:3],a[68:71]
rt v[212:213],v242 offset:50688
rt v[214:215],v242 offset:51712
mm a[0:3],v[48:51],v[0:3],a[0:3]
s_add_u32 m0, s83, 2176
mm a[0:3],v[52:55],v[4:7],a[0:3]
ld v234,s[44:47],s80 offen lds
mm a[4:7],v[60:63],v[4:7],a[4:7]
mm a[4:7],v[56:59],v[0:3],a[4:7]
rd v[0:3],v236 offset:40960
rd v[4:7],v237 offset:40960
mm a[20:23],v[56:59],v[8:11],a[20:23]
s_add_u32 m0, s83, 3200
mm a[20:23],v[60:63],v[12:15],a[20:23]
ld v235,s[44:47],s80 offen lds
mm a[16:19],v[52:55],v[12:15],a[16:19]
s_mov_b32 s80, 262144
mm a[16:19],v[48:51],v[8:11],a[16:19]
rt v[216:217],v243 offset:33792
rt v[218:219],v243 offset:34816
mm a[84:87],v[40:43],v[8:11],a[84:87]
s_add_u32 m0, s83, 16896
mm a[84:87],v[44:47],v[12:15],a[84:87]
ld v232,s[44:47],s80 offen lds
mm a[80:83],v[36:39],v[12:15],a[80:83]
mm a[80:83],v[32:35],v[8:11],a[80:83]
rd v[8:11],v236 offset:43008
rd v[12:15],v237 offset:43008
mm a[240:243],v[32:35],v[16:19],a[240:243]
s_add_u32 m0, s83, 17920
mm a[240:243],v[36:39],v[20:23],a[240:243]
ld v233,s[44:47],s80 offen lds
mm a[244:247],v[44:47],v[20:23],a[244:247]
mm a[244:247],v[40:43],v[16:19],a[244:247]
rt v[220:221],v243 offset:50688
rt v[222:223],v243 offset:51712
mm a[32:35],v[48:51],v[16:19],a[32:35]
s_add_u32 m0, s83, 19072
mm a[32:35],v[52:55],v[20:23],a[32:35]
ld v234,s[44:47],s80 offen lds
mm a[36:39],v[60:63],v[20:23],a[36:39]
mm a[36:39],v[56:59],v[16:19],a[36:39]
rd v[16:19],v236 offset:32768
rd v[20:23],v237 offset:32768
mm a[60:63],v[56:59],v[24:27],a[60:63]
s_add_u32 m0, s83, 20096
mm a[60:63],v[60:63],v[28:31],a[60:63]
ld v235,s[44:47],s80 offen lds
mm a[56:59],v[52:55],v[28:31],a[56:59]
s_mov_b32 s80, 524288
mm a[56:59],v[48:51],v[24:27],a[56:59]
mm a[52:55],v[40:43],v[24:27],a[52:55]
mm a[52:55],v[44:47],v[28:31],a[52:55]
mm a[48:51],v[36:39],v[28:31],a[48:51]
wt lgkmcnt(2)
mm a[48:51],v[32:35],v[24:27],a[48:51]
rd v[24:27],v236 offset:34816
rd v[28:31],v237 offset:34816
mm a[96:99],v[192:195],v[160:163],a[96:99]
mm a[96:99],v[196:199],v[164:167],a[96:99]
mm a[100:103],v[204:207],v[164:167],a[100:103]
mm a[100:103],v[200:203],v[160:163],a[100:103]
rt v[32:33],v240 offset:33920
rt v[34:35],v240 offset:34944
mm a[104:107],v[208:211],v[160:163],a[104:107]
mm a[104:107],v[212:215],v[164:167],a[104:107]
mm a[108:111],v[220:223],v[164:167],a[108:111]
wt lgkmcnt(2)
s_barrier
mm a[108:111],v[216:219],v[160:163],a[108:111]
mm a[140:143],v[216:219],v[168:171],a[140:143]
s_xor_b32 m0, s82, 32768
mm a[140:143],v[220:223],v[172:175],a[140:143]
ld v224,s[40:43],s79 offen lds
mm a[136:139],v[212:215],v[172:175],a[136:139]
mm a[136:139],v[208:211],v[168:171],a[136:139]
rt v[36:37],v240 offset:50816
rt v[38:39],v240 offset:51840
mm a[124:127],v[200:203],v[168:171],a[124:127]
s_xor_b32 m0, s82, 33792
mm a[124:127],v[204:207],v[172:175],a[124:127]
ld v225,s[40:43],s79 offen lds
mm a[120:123],v[196:199],v[172:175],a[120:123]
mm a[120:123],v[192:195],v[168:171],a[120:123]
mm a[232:235],v[192:195],v[176:179],a[232:235]
s_xor_b32 m0, s82, 34816
mm a[232:235],v[196:199],v[180:183],a[232:235]
ld v226,s[40:43],s79 offen lds
mm a[236:239],v[204:207],v[180:183],a[236:239]
mm a[236:239],v[200:203],v[176:179],a[236:239]
rt v[40:41],v241 offset:33920
rt v[42:43],v241 offset:34944
mm a[152:155],v[208:211],v[176:179],a[152:155]
s_xor_b32 m0, s82, 35840
mm a[152:155],v[212:215],v[180:183],a[152:155]
ld v227,s[40:43],s79 offen lds
mm a[156:159],v[220:223],v[180:183],a[156:159]
mm a[156:159],v[216:219],v[176:179],a[156:159]
mm a[172:175],v[216:219],v[184:187],a[172:175]
s_xor_b32 m0, s82, 36864
mm a[172:175],v[220:223],v[188:191],a[172:175]
ld v228,s[40:43],s79 offen lds
mm a[168:171],v[212:215],v[188:191],a[168:171]
mm a[168:171],v[208:211],v[184:187],a[168:171]
rt v[44:45],v241 offset:50816
rt v[46:47],v241 offset:51840
mm a[220:223],v[200:203],v[184:187],a[220:223]
s_xor_b32 m0, s82, 37888
mm a[220:223],v[204:207],v[188:191],a[220:223]
ld v229,s[40:43],s79 offen lds
mm a[216:219],v[196:199],v[188:191],a[216:219]
mm a[216:219],v[192:195],v[184:187],a[216:219]
mm a[200:203],v[192:195],v[0:3],a[200:203]
s_xor_b32 m0, s82, 38912
mm a[200:203],v[196:199],v[4:7],a[200:203]
ld v230,s[40:43],s79 offen lds
mm a[204:207],v[204:207],v[4:7],a[204:207]
mm a[204:207],v[200:203],v[0:3],a[204:207]
rt v[48:49],v242 offset:33920
rt v[50:51],v242 offset:34944
mm a[184:187],v[208:211],v[0:3],a[184:187]
s_xor_b32 m0, s82, 39936
mm a[184:187],v[212:215],v[4:7],a[184:187]
ld v231,s[40:43],s79 offen lds
mm a[188:191],v[220:223],v[4:7],a[188:191]
s_mov_b32 s79, 256
mm a[188:191],v[216:219],v[0:3],a[188:191]
mm a[12:15],v[216:219],v[8:11],a[12:15]
mm a[12:15],v[220:223],v[12:15],a[12:15]
buffer_load_dword v254, v135, s[68:71], s96 offen
mm a[8:11],v[212:215],v[12:15],a[8:11]
s_add_u32 s96, s96, 1048576
mm a[8:11],v[208:211],v[8:11],a[8:11]
rt v[52:53],v242 offset:50816
rt v[54:55],v242 offset:51840
mm a[76:79],v[200:203],v[8:11],a[76:79]
mm a[76:79],v[204:207],v[12:15],a[76:79]
buffer_load_dword v254, v135, s[68:71], s96 offen
mm a[72:75],v[196:199],v[12:15],a[72:75]
s_add_u32 s96, s96, 1048576
mm a[72:75],v[192:195],v[8:11],a[72:75]
rt v[56:57],v243 offset:33920
rt v[58:59],v243 offset:34944
mm a[88:91],v[192:195],v[16:19],a[88:91]
mm a[88:91],v[196:199],v[20:23],a[88:91]
wt vmcnt(18)
buffer_store_dwordx2 v[252:253], off, s[64:67], s91 offset:1024 sc1
mm a[92:95],v[204:207],v[20:23],a[92:95]
s_mov_b32 s91, 0
s_lshl_b32 s92, s76, 19
mm a[92:95],v[200:203],v[16:19],a[92:95]
rt v[60:61],v243 offset:50816
rt v[62:63],v243 offset:51840
mm a[24:27],v[208:211],v[16:19],a[24:27]
s_add_u32 s92, s92, 88080384
mm a[24:27],v[212:215],v[20:23],a[24:27]
s_add_u32 s52, s26, s92
s_addc_u32 s53, s27, 0
mm a[28:31],v[220:223],v[20:23],a[28:31]
s_and_b32 s53, s53, 0xffff
s_mov_b32 s54, 524672
mm a[28:31],v[216:219],v[16:19],a[28:31]
s_mov_b32 s55, 0x20000
s_lshl_b32 s92, s3, 16
mm a[44:47],v[216:219],v[24:27],a[44:47]
s_sub_u32 s93, 458752, s92
v_add_u32 v244, s92, v136
mm a[44:47],v[220:223],v[28:31],a[44:47]
v_add_u32 v246, s93, v136
v_add_u32 v245, 0x8000, v244
mm a[40:43],v[212:215],v[28:31],a[40:43]
v_add_u32 v247, 0x8000, v246
s_lshl_b32 s94, s72, 19
mm a[40:43],v[208:211],v[24:27],a[40:43]
s_lshl_b32 s93, s76, 16
s_add_u32 s92, s94, s93
mm a[252:255],v[200:203],v[24:27],a[252:255]
s_lshl_b32 s93, s77, 13
s_add_u32 s92, s92, s93
mm a[252:255],v[204:207],v[28:31],a[252:255]
s_add_u32 s92, s92, 20971520
mm a[248:251],v[196:199],v[28:31],a[248:251]
wt vmcnt(11) lgkmcnt(0)
s_barrier
mm a[248:251],v[192:195],v[24:27],a[248:251]
s_add_u32 s48, s26, s92
s_addc_u32 s49, s27, 0
mm a[112:115],v[32:35],v[160:163],a[112:115]
s_and_b32 s49, s49, 0xffff
s_mov_b32 s50, 57344
mm a[112:115],v[36:39],v[164:167],a[112:115]
s_mov_b32 s51, 0x20000
s_lshl_b32 s93, s77, 24
mm a[116:119],v[44:47],v[164:167],a[116:119]
rt v[192:193],v240 offset:0
rt v[194:195],v240 offset:1024
mm a[116:119],v[40:43],v[160:163],a[116:119]
rt v[196:197],v240 offset:16896
rt v[198:199],v240 offset:17920
mm a[128:131],v[48:51],v[160:163],a[128:131]
s_add_u32 s92, s94, s93
s_add_u32 s92, s92, 20971520
mm a[128:131],v[52:55],v[164:167],a[128:131]
s_add_u32 s60, s26, s92
s_addc_u32 s61, s27, 0
mm a[132:135],v[60:63],v[164:167],a[132:135]
s_and_b32 s61, s61, 0xffff
s_mov_b32 s62, 524288
mm a[132:135],v[56:59],v[160:163],a[132:135]
rd v[160:163],v236 offset:24576
rd v[164:167],v237 offset:24576
mm a[148:151],v[56:59],v[168:171],a[148:151]
s_mov_b32 s63, 0x20000
s_lshl_b32 s92, s77, 14
mm a[148:151],v[60:63],v[172:175],a[148:151]
s_sub_u32 s95, 16384, s92
v_add_u32 v238, s95, v255
mm a[144:147],v[52:55],v[172:175],a[144:147]
mm a[144:147],v[48:51],v[168:171],a[144:147]
rt v[200:201],v241 offset:0
rt v[202:203],v241 offset:1024
mm a[228:231],v[40:43],v[168:171],a[228:231]
mm a[228:231],v[44:47],v[172:175],a[228:231]
mm a[224:227],v[36:39],v[172:175],a[224:227]
mm a[224:227],v[32:35],v[168:171],a[224:227]
rd v[168:171],v236 offset:26624
rd v[172:175],v237 offset:26624
mm a[208:211],v[32:35],v[176:179],a[208:211]
mm a[208:211],v[36:39],v[180:183],a[208:211]
mm a[212:215],v[44:47],v[180:183],a[212:215]
mm a[212:215],v[40:43],v[176:179],a[212:215]
rt v[204:205],v241 offset:16896
rt v[206:207],v241 offset:17920
mm a[160:163],v[48:51],v[176:179],a[160:163]
mm a[160:163],v[52:55],v[180:183],a[160:163]
mm a[164:167],v[60:63],v[180:183],a[164:167]
mm a[164:167],v[56:59],v[176:179],a[164:167]
rd v[176:179],v236 offset:16384
rd v[180:183],v237 offset:16384
mm a[180:183],v[56:59],v[184:187],a[180:183]
mm a[180:183],v[60:63],v[188:191],a[180:183]
mm a[176:179],v[52:55],v[188:191],a[176:179]
mm a[176:179],v[48:51],v[184:187],a[176:179]
rt v[208:209],v242 offset:0
rt v[210:211],v242 offset:1024
mm a[196:199],v[40:43],v[184:187],a[196:199]
s_add_u32 m0, s83, 33792
mm a[196:199],v[44:47],v[188:191],a[196:199]
ld v232,s[44:47],s80 offen lds
mm a[192:195],v[36:39],v[188:191],a[192:195]
mm a[192:195],v[32:35],v[184:187],a[192:195]
rd v[184:187],v236 offset:18432
rd v[188:191],v237 offset:18432
mm a[64:67],v[32:35],v[0:3],a[64:67]
s_add_u32 m0, s83, 34816
mm a[64:67],v[36:39],v[4:7],a[64:67]
ld v233,s[44:47],s80 offen lds
mm a[68:71],v[44:47],v[4:7],a[68:71]
mm a[68:71],v[40:43],v[0:3],a[68:71]
rt v[212:213],v242 offset:16896
rt v[214:215],v242 offset:17920
mm a[0:3],v[48:51],v[0:3],a[0:3]
s_add_u32 m0, s83, 35968
mm a[0:3],v[52:55],v[4:7],a[0:3]
ld v234,s[44:47],s80 offen lds
mm a[4:7],v[60:63],v[4:7],a[4:7]
mm a[4:7],v[56:59],v[0:3],a[4:7]
rd v[0:3],v236 offset:8192
rd v[4:7],v237 offset:8192
mm a[20:23],v[56:59],v[8:11],a[20:23]
s_add_u32 m0, s83, 36992
mm a[20:23],v[60:63],v[12:15],a[20:23]
ld v235,s[44:47],s80 offen lds
mm a[16:19],v[52:55],v[12:15],a[16:19]
s_mov_b32 s80, 786432
mm a[16:19],v[48:51],v[8:11],a[16:19]
rt v[216:217],v243 offset:0
rt v[218:219],v243 offset:1024
mm a[84:87],v[40:43],v[8:11],a[84:87]
s_add_u32 m0, s83, 50688
mm a[84:87],v[44:47],v[12:15],a[84:87]
ld v232,s[44:47],s80 offen lds
mm a[80:83],v[36:39],v[12:15],a[80:83]
mm a[80:83],v[32:35],v[8:11],a[80:83]
rd v[8:11],v236 offset:10240
rd v[12:15],v237 offset:10240
mm a[240:243],v[32:35],v[16:19],a[240:243]
s_add_u32 m0, s83, 51712
mm a[240:243],v[36:39],v[20:23],a[240:243]
ld v233,s[44:47],s80 offen lds
mm a[244:247],v[44:47],v[20:23],a[244:247]
mm a[244:247],v[40:43],v[16:19],a[244:247]
rt v[220:221],v243 offset:16896
rt v[222:223],v243 offset:17920
mm a[32:35],v[48:51],v[16:19],a[32:35]
s_add_u32 m0, s83, 52864
mm a[32:35],v[52:55],v[20:23],a[32:35]
ld v234,s[44:47],s80 offen lds
mm a[36:39],v[60:63],v[20:23],a[36:39]
mm a[36:39],v[56:59],v[16:19],a[36:39]
rd v[16:19],v236 offset:0
rd v[20:23],v237 offset:0
mm a[60:63],v[56:59],v[24:27],a[60:63]
s_add_u32 m0, s83, 53888
mm a[60:63],v[60:63],v[28:31],a[60:63]
ld v235,s[44:47],s80 offen lds
mm a[56:59],v[52:55],v[28:31],a[56:59]
s_mov_b32 s80, 1048576
mm a[56:59],v[48:51],v[24:27],a[56:59]
mm a[52:55],v[40:43],v[24:27],a[52:55]
mm a[52:55],v[44:47],v[28:31],a[52:55]
mm a[48:51],v[36:39],v[28:31],a[48:51]
wt lgkmcnt(2)
mm a[48:51],v[32:35],v[24:27],a[48:51]
rd v[24:27],v236 offset:2048
rd v[28:31],v237 offset:2048
mm a[96:99],v[192:195],v[160:163],a[96:99]
mm a[96:99],v[196:199],v[164:167],a[96:99]
mm a[100:103],v[204:207],v[164:167],a[100:103]
mm a[100:103],v[200:203],v[160:163],a[100:103]
rt v[32:33],v240 offset:128
rt v[34:35],v240 offset:1152
mm a[104:107],v[208:211],v[160:163],a[104:107]
mm a[104:107],v[212:215],v[164:167],a[104:107]
mm a[108:111],v[220:223],v[164:167],a[108:111]
wt lgkmcnt(2)
s_barrier
mm a[108:111],v[216:219],v[160:163],a[108:111]
mm a[140:143],v[216:219],v[168:171],a[140:143]
s_xor_b32 m0, s82, 0
mm a[140:143],v[220:223],v[172:175],a[140:143]
ld v224,s[40:43],s79 offen lds
mm a[136:139],v[212:215],v[172:175],a[136:139]
mm a[136:139],v[208:211],v[168:171],a[136:139]
rt v[36:37],v240 offset:17024
rt v[38:39],v240 offset:18048
mm a[124:127],v[200:203],v[168:171],a[124:127]
s_xor_b32 m0, s82, 1024
mm a[124:127],v[204:207],v[172:175],a[124:127]
ld v225,s[40:43],s79 offen lds
mm a[120:123],v[196:199],v[172:175],a[120:123]
mm a[120:123],v[192:195],v[168:171],a[120:123]
mm a[232:235],v[192:195],v[176:179],a[232:235]
s_xor_b32 m0, s82, 2048
mm a[232:235],v[196:199],v[180:183],a[232:235]
ld v226,s[40:43],s79 offen lds
mm a[236:239],v[204:207],v[180:183],a[236:239]
mm a[236:239],v[200:203],v[176:179],a[236:239]
rt v[40:41],v241 offset:128
rt v[42:43],v241 offset:1152
mm a[152:155],v[208:211],v[176:179],a[152:155]
s_xor_b32 m0, s82, 3072
mm a[152:155],v[212:215],v[180:183],a[152:155]
ld v227,s[40:43],s79 offen lds
mm a[156:159],v[220:223],v[180:183],a[156:159]
mm a[156:159],v[216:219],v[176:179],a[156:159]
mm a[172:175],v[216:219],v[184:187],a[172:175]
s_xor_b32 m0, s82, 4096
mm a[172:175],v[220:223],v[188:191],a[172:175]
ld v228,s[40:43],s79 offen lds
mm a[168:171],v[212:215],v[188:191],a[168:171]
mm a[168:171],v[208:211],v[184:187],a[168:171]
rt v[44:45],v241 offset:17024
rt v[46:47],v241 offset:18048
mm a[220:223],v[200:203],v[184:187],a[220:223]
s_xor_b32 m0, s82, 5120
mm a[220:223],v[204:207],v[188:191],a[220:223]
ld v229,s[40:43],s79 offen lds
mm a[216:219],v[196:199],v[188:191],a[216:219]
mm a[216:219],v[192:195],v[184:187],a[216:219]
mm a[200:203],v[192:195],v[0:3],a[200:203]
s_xor_b32 m0, s82, 6144
mm a[200:203],v[196:199],v[4:7],a[200:203]
ld v230,s[40:43],s79 offen lds
mm a[204:207],v[204:207],v[4:7],a[204:207]
mm a[204:207],v[200:203],v[0:3],a[204:207]
rt v[48:49],v242 offset:128
rt v[50:51],v242 offset:1152
mm a[184:187],v[208:211],v[0:3],a[184:187]
s_xor_b32 m0, s82, 7168
mm a[184:187],v[212:215],v[4:7],a[184:187]
ld v231,s[40:43],s79 offen lds
mm a[188:191],v[220:223],v[4:7],a[188:191]
s_mov_b32 s79, 384
mm a[188:191],v[216:219],v[0:3],a[188:191]
mm a[12:15],v[216:219],v[8:11],a[12:15]
mm a[12:15],v[220:223],v[12:15],a[12:15]
buffer_load_dword v254, v135, s[68:71], s96 offen
mm a[8:11],v[212:215],v[12:15],a[8:11]
s_add_u32 s96, s96, 1048576
mm a[8:11],v[208:211],v[8:11],a[8:11]
rt v[52:53],v242 offset:17024
rt v[54:55],v242 offset:18048
mm a[76:79],v[200:203],v[8:11],a[76:79]
mm a[76:79],v[204:207],v[12:15],a[76:79]
buffer_load_dword v254, v135, s[68:71], s96 offen
mm a[72:75],v[196:199],v[12:15],a[72:75]
s_add_u32 s96, s96, 1048576
mm a[72:75],v[192:195],v[8:11],a[72:75]
rt v[56:57],v243 offset:128
rt v[58:59],v243 offset:1152
mm a[88:91],v[192:195],v[16:19],a[88:91]
mm a[88:91],v[196:199],v[20:23],a[88:91]
mm a[92:95],v[204:207],v[20:23],a[92:95]
mm a[92:95],v[200:203],v[16:19],a[92:95]
rt v[60:61],v243 offset:17024
rt v[62:63],v243 offset:18048
mm a[24:27],v[208:211],v[16:19],a[24:27]
mm a[24:27],v[212:215],v[20:23],a[24:27]
mm a[28:31],v[220:223],v[20:23],a[28:31]
mm a[28:31],v[216:219],v[16:19],a[28:31]
mm a[44:47],v[216:219],v[24:27],a[44:47]
mm a[44:47],v[220:223],v[28:31],a[44:47]
mm a[40:43],v[212:215],v[28:31],a[40:43]
mm a[40:43],v[208:211],v[24:27],a[40:43]
mm a[252:255],v[200:203],v[24:27],a[252:255]
mm a[252:255],v[204:207],v[28:31],a[252:255]
mm a[248:251],v[196:199],v[28:31],a[248:251]
wt vmcnt(10) lgkmcnt(0)
s_barrier
mm a[248:251],v[192:195],v[24:27],a[248:251]
mm a[112:115],v[32:35],v[160:163],a[112:115]
mm a[112:115],v[36:39],v[164:167],a[112:115]
mm a[116:119],v[44:47],v[164:167],a[116:119]
rt v[192:193],v240 offset:33792
rt v[194:195],v240 offset:34816
mm a[116:119],v[40:43],v[160:163],a[116:119]
rt v[196:197],v240 offset:50688
rt v[198:199],v240 offset:51712
mm a[128:131],v[48:51],v[160:163],a[128:131]
mm a[128:131],v[52:55],v[164:167],a[128:131]
mm a[132:135],v[60:63],v[164:167],a[132:135]
mm a[132:135],v[56:59],v[160:163],a[132:135]
rd v[160:163],v236 offset:57344
rd v[164:167],v237 offset:57344
mm a[148:151],v[56:59],v[168:171],a[148:151]
mm a[148:151],v[60:63],v[172:175],a[148:151]
mm a[144:147],v[52:55],v[172:175],a[144:147]
mm a[144:147],v[48:51],v[168:171],a[144:147]
rt v[200:201],v241 offset:33792
rt v[202:203],v241 offset:34816
mm a[228:231],v[40:43],v[168:171],a[228:231]
mm a[228:231],v[44:47],v[172:175],a[228:231]
mm a[224:227],v[36:39],v[172:175],a[224:227]
mm a[224:227],v[32:35],v[168:171],a[224:227]
rd v[168:171],v236 offset:59392
rd v[172:175],v237 offset:59392
mm a[208:211],v[32:35],v[176:179],a[208:211]
mm a[208:211],v[36:39],v[180:183],a[208:211]
mm a[212:215],v[44:47],v[180:183],a[212:215]
mm a[212:215],v[40:43],v[176:179],a[212:215]
rt v[204:205],v241 offset:50688
rt v[206:207],v241 offset:51712
mm a[160:163],v[48:51],v[176:179],a[160:163]
mm a[160:163],v[52:55],v[180:183],a[160:163]
mm a[164:167],v[60:63],v[180:183],a[164:167]
mm a[164:167],v[56:59],v[176:179],a[164:167]
rd v[176:179],v236 offset:49152
rd v[180:183],v237 offset:49152
mm a[180:183],v[56:59],v[184:187],a[180:183]
mm a[180:183],v[60:63],v[188:191],a[180:183]
mm a[176:179],v[52:55],v[188:191],a[176:179]
mm a[176:179],v[48:51],v[184:187],a[176:179]
rt v[208:209],v242 offset:33792
rt v[210:211],v242 offset:34816
mm a[196:199],v[40:43],v[184:187],a[196:199]
s_add_u32 m0, s83, 0
mm a[196:199],v[44:47],v[188:191],a[196:199]
ld v232,s[44:47],s80 offen lds
mm a[192:195],v[36:39],v[188:191],a[192:195]
mm a[192:195],v[32:35],v[184:187],a[192:195]
rd v[184:187],v236 offset:51200
rd v[188:191],v237 offset:51200
mm a[64:67],v[32:35],v[0:3],a[64:67]
s_add_u32 m0, s83, 1024
mm a[64:67],v[36:39],v[4:7],a[64:67]
ld v233,s[44:47],s80 offen lds
mm a[68:71],v[44:47],v[4:7],a[68:71]
mm a[68:71],v[40:43],v[0:3],a[68:71]
rt v[212:213],v242 offset:50688
rt v[214:215],v242 offset:51712
mm a[0:3],v[48:51],v[0:3],a[0:3]
s_add_u32 m0, s83, 2176
mm a[0:3],v[52:55],v[4:7],a[0:3]
ld v234,s[44:47],s80 offen lds
mm a[4:7],v[60:63],v[4:7],a[4:7]
mm a[4:7],v[56:59],v[0:3],a[4:7]
rd v[0:3],v236 offset:40960
rd v[4:7],v237 offset:40960
mm a[20:23],v[56:59],v[8:11],a[20:23]
s_add_u32 m0, s83, 3200
mm a[20:23],v[60:63],v[12:15],a[20:23]
ld v235,s[44:47],s80 offen lds
mm a[16:19],v[52:55],v[12:15],a[16:19]
s_mov_b32 s80, 1310720
mm a[16:19],v[48:51],v[8:11],a[16:19]
rt v[216:217],v243 offset:33792
rt v[218:219],v243 offset:34816
mm a[84:87],v[40:43],v[8:11],a[84:87]
s_add_u32 m0, s83, 16896
mm a[84:87],v[44:47],v[12:15],a[84:87]
ld v232,s[44:47],s80 offen lds
mm a[80:83],v[36:39],v[12:15],a[80:83]
mm a[80:83],v[32:35],v[8:11],a[80:83]
rd v[8:11],v236 offset:43008
rd v[12:15],v237 offset:43008
mm a[240:243],v[32:35],v[16:19],a[240:243]
s_add_u32 m0, s83, 17920
mm a[240:243],v[36:39],v[20:23],a[240:243]
ld v233,s[44:47],s80 offen lds
mm a[244:247],v[44:47],v[20:23],a[244:247]
mm a[244:247],v[40:43],v[16:19],a[244:247]
rt v[220:221],v243 offset:50688
rt v[222:223],v243 offset:51712
mm a[32:35],v[48:51],v[16:19],a[32:35]
s_add_u32 m0, s83, 19072
mm a[32:35],v[52:55],v[20:23],a[32:35]
ld v234,s[44:47],s80 offen lds
.La5_LW1_2:
s_cbranch_execz .La5_LW1_6
mm a[36:39],v[60:63],v[20:23],a[36:39]
mm a[36:39],v[56:59],v[16:19],a[36:39]
rd v[16:19],v236 offset:32768
rd v[20:23],v237 offset:32768
mm a[60:63],v[56:59],v[24:27],a[60:63]
s_add_u32 m0, s83, 20096
mm a[60:63],v[60:63],v[28:31],a[60:63]
ld v235,s[44:47],s80 offen lds
mm a[56:59],v[52:55],v[28:31],a[56:59]
s_mov_b32 s80, 1572864
mm a[56:59],v[48:51],v[24:27],a[56:59]
mm a[52:55],v[40:43],v[24:27],a[52:55]
mm a[52:55],v[44:47],v[28:31],a[52:55]
mm a[48:51],v[36:39],v[28:31],a[48:51]
wt lgkmcnt(2)
mm a[48:51],v[32:35],v[24:27],a[48:51]
rd v[24:27],v236 offset:34816
rd v[28:31],v237 offset:34816
mm a[96:99],v[192:195],v[160:163],a[96:99]
mm a[96:99],v[196:199],v[164:167],a[96:99]
mm a[100:103],v[204:207],v[164:167],a[100:103]
mm a[100:103],v[200:203],v[160:163],a[100:103]
rt v[32:33],v240 offset:33920
rt v[34:35],v240 offset:34944
mm a[104:107],v[208:211],v[160:163],a[104:107]
mm a[104:107],v[212:215],v[164:167],a[104:107]
mm a[108:111],v[220:223],v[164:167],a[108:111]
wt lgkmcnt(2)
s_barrier
mm a[108:111],v[216:219],v[160:163],a[108:111]
mm a[140:143],v[216:219],v[168:171],a[140:143]
s_xor_b32 m0, s82, 32768
mm a[140:143],v[220:223],v[172:175],a[140:143]
ld v224,s[40:43],s79 offen lds
mm a[136:139],v[212:215],v[172:175],a[136:139]
mm a[136:139],v[208:211],v[168:171],a[136:139]
rt v[36:37],v240 offset:50816
rt v[38:39],v240 offset:51840
mm a[124:127],v[200:203],v[168:171],a[124:127]
s_xor_b32 m0, s82, 33792
mm a[124:127],v[204:207],v[172:175],a[124:127]
ld v225,s[40:43],s79 offen lds
mm a[120:123],v[196:199],v[172:175],a[120:123]
mm a[120:123],v[192:195],v[168:171],a[120:123]
mm a[232:235],v[192:195],v[176:179],a[232:235]
s_xor_b32 m0, s82, 34816
mm a[232:235],v[196:199],v[180:183],a[232:235]
ld v226,s[40:43],s79 offen lds
mm a[236:239],v[204:207],v[180:183],a[236:239]
mm a[236:239],v[200:203],v[176:179],a[236:239]
rt v[40:41],v241 offset:33920
rt v[42:43],v241 offset:34944
mm a[152:155],v[208:211],v[176:179],a[152:155]
s_xor_b32 m0, s82, 35840
mm a[152:155],v[212:215],v[180:183],a[152:155]
ld v227,s[40:43],s79 offen lds
mm a[156:159],v[220:223],v[180:183],a[156:159]
mm a[156:159],v[216:219],v[176:179],a[156:159]
mm a[172:175],v[216:219],v[184:187],a[172:175]
s_xor_b32 m0, s82, 36864
mm a[172:175],v[220:223],v[188:191],a[172:175]
ld v228,s[40:43],s79 offen lds
mm a[168:171],v[212:215],v[188:191],a[168:171]
mm a[168:171],v[208:211],v[184:187],a[168:171]
rt v[44:45],v241 offset:50816
rt v[46:47],v241 offset:51840
mm a[220:223],v[200:203],v[184:187],a[220:223]
s_xor_b32 m0, s82, 37888
mm a[220:223],v[204:207],v[188:191],a[220:223]
ld v229,s[40:43],s79 offen lds
mm a[216:219],v[196:199],v[188:191],a[216:219]
mm a[216:219],v[192:195],v[184:187],a[216:219]
mm a[200:203],v[192:195],v[0:3],a[200:203]
s_xor_b32 m0, s82, 38912
mm a[200:203],v[196:199],v[4:7],a[200:203]
ld v230,s[40:43],s79 offen lds
mm a[204:207],v[204:207],v[4:7],a[204:207]
mm a[204:207],v[200:203],v[0:3],a[204:207]
rt v[48:49],v242 offset:33920
rt v[50:51],v242 offset:34944
mm a[184:187],v[208:211],v[0:3],a[184:187]
s_xor_b32 m0, s82, 39936
mm a[184:187],v[212:215],v[4:7],a[184:187]
ld v231,s[40:43],s79 offen lds
mm a[188:191],v[220:223],v[4:7],a[188:191]
s_mov_b32 s79, 512
mm a[188:191],v[216:219],v[0:3],a[188:191]
mm a[12:15],v[216:219],v[8:11],a[12:15]
mm a[12:15],v[220:223],v[12:15],a[12:15]
buffer_load_dword v254, v135, s[68:71], s96 offen
mm a[8:11],v[212:215],v[12:15],a[8:11]
s_add_u32 s96, s96, 1048576
mm a[8:11],v[208:211],v[8:11],a[8:11]
rt v[52:53],v242 offset:50816
rt v[54:55],v242 offset:51840
mm a[76:79],v[200:203],v[8:11],a[76:79]
mm a[76:79],v[204:207],v[12:15],a[76:79]
buffer_load_dword v254, v135, s[68:71], s96 offen
mm a[72:75],v[196:199],v[12:15],a[72:75]
s_add_u32 s96, s96, 1048576
mm a[72:75],v[192:195],v[8:11],a[72:75]
rt v[56:57],v243 offset:33920
rt v[58:59],v243 offset:34944
mm a[88:91],v[192:195],v[16:19],a[88:91]
mm a[88:91],v[196:199],v[20:23],a[88:91]
mm a[92:95],v[204:207],v[20:23],a[92:95]
mm a[92:95],v[200:203],v[16:19],a[92:95]
rt v[60:61],v243 offset:50816
rt v[62:63],v243 offset:51840
mm a[24:27],v[208:211],v[16:19],a[24:27]
mm a[24:27],v[212:215],v[20:23],a[24:27]
mm a[28:31],v[220:223],v[20:23],a[28:31]
mm a[28:31],v[216:219],v[16:19],a[28:31]
mm a[44:47],v[216:219],v[24:27],a[44:47]
mm a[44:47],v[220:223],v[28:31],a[44:47]
mm a[40:43],v[212:215],v[28:31],a[40:43]
mm a[40:43],v[208:211],v[24:27],a[40:43]
mm a[252:255],v[200:203],v[24:27],a[252:255]
mm a[252:255],v[204:207],v[28:31],a[252:255]
mm a[248:251],v[196:199],v[28:31],a[248:251]
wt vmcnt(10) lgkmcnt(0)
s_barrier
mm a[248:251],v[192:195],v[24:27],a[248:251]
mm a[112:115],v[32:35],v[160:163],a[112:115]
mm a[112:115],v[36:39],v[164:167],a[112:115]
mm a[116:119],v[44:47],v[164:167],a[116:119]
rt v[192:193],v240 offset:0
rt v[194:195],v240 offset:1024
mm a[116:119],v[40:43],v[160:163],a[116:119]
rt v[196:197],v240 offset:16896
rt v[198:199],v240 offset:17920
mm a[128:131],v[48:51],v[160:163],a[128:131]
mm a[128:131],v[52:55],v[164:167],a[128:131]
mm a[132:135],v[60:63],v[164:167],a[132:135]
mm a[132:135],v[56:59],v[160:163],a[132:135]
rd v[160:163],v236 offset:24576
rd v[164:167],v237 offset:24576
mm a[148:151],v[56:59],v[168:171],a[148:151]
mm a[148:151],v[60:63],v[172:175],a[148:151]
mm a[144:147],v[52:55],v[172:175],a[144:147]
mm a[144:147],v[48:51],v[168:171],a[144:147]
rt v[200:201],v241 offset:0
rt v[202:203],v241 offset:1024
mm a[228:231],v[40:43],v[168:171],a[228:231]
mm a[228:231],v[44:47],v[172:175],a[228:231]
mm a[224:227],v[36:39],v[172:175],a[224:227]
mm a[224:227],v[32:35],v[168:171],a[224:227]
rd v[168:171],v236 offset:26624
rd v[172:175],v237 offset:26624
mm a[208:211],v[32:35],v[176:179],a[208:211]
mm a[208:211],v[36:39],v[180:183],a[208:211]
mm a[212:215],v[44:47],v[180:183],a[212:215]
mm a[212:215],v[40:43],v[176:179],a[212:215]
rt v[204:205],v241 offset:16896
rt v[206:207],v241 offset:17920
mm a[160:163],v[48:51],v[176:179],a[160:163]
mm a[160:163],v[52:55],v[180:183],a[160:163]
mm a[164:167],v[60:63],v[180:183],a[164:167]
mm a[164:167],v[56:59],v[176:179],a[164:167]
rd v[176:179],v236 offset:16384
rd v[180:183],v237 offset:16384
mm a[180:183],v[56:59],v[184:187],a[180:183]
mm a[180:183],v[60:63],v[188:191],a[180:183]
mm a[176:179],v[52:55],v[188:191],a[176:179]
mm a[176:179],v[48:51],v[184:187],a[176:179]
rt v[208:209],v242 offset:0
rt v[210:211],v242 offset:1024
mm a[196:199],v[40:43],v[184:187],a[196:199]
s_add_u32 m0, s83, 33792
mm a[196:199],v[44:47],v[188:191],a[196:199]
ld v232,s[44:47],s80 offen lds
mm a[192:195],v[36:39],v[188:191],a[192:195]
mm a[192:195],v[32:35],v[184:187],a[192:195]
rd v[184:187],v236 offset:18432
rd v[188:191],v237 offset:18432
mm a[64:67],v[32:35],v[0:3],a[64:67]
s_add_u32 m0, s83, 34816
mm a[64:67],v[36:39],v[4:7],a[64:67]
ld v233,s[44:47],s80 offen lds
mm a[68:71],v[44:47],v[4:7],a[68:71]
mm a[68:71],v[40:43],v[0:3],a[68:71]
rt v[212:213],v242 offset:16896
rt v[214:215],v242 offset:17920
mm a[0:3],v[48:51],v[0:3],a[0:3]
s_add_u32 m0, s83, 35968
mm a[0:3],v[52:55],v[4:7],a[0:3]
ld v234,s[44:47],s80 offen lds
mm a[4:7],v[60:63],v[4:7],a[4:7]
mm a[4:7],v[56:59],v[0:3],a[4:7]
rd v[0:3],v236 offset:8192
rd v[4:7],v237 offset:8192
mm a[20:23],v[56:59],v[8:11],a[20:23]
s_add_u32 m0, s83, 36992
mm a[20:23],v[60:63],v[12:15],a[20:23]
ld v235,s[44:47],s80 offen lds
mm a[16:19],v[52:55],v[12:15],a[16:19]
s_mov_b32 s80, 1835008
mm a[16:19],v[48:51],v[8:11],a[16:19]
rt v[216:217],v243 offset:0
rt v[218:219],v243 offset:1024
mm a[84:87],v[40:43],v[8:11],a[84:87]
s_add_u32 m0, s83, 50688
mm a[84:87],v[44:47],v[12:15],a[84:87]
ld v232,s[44:47],s80 offen lds
mm a[80:83],v[36:39],v[12:15],a[80:83]
mm a[80:83],v[32:35],v[8:11],a[80:83]
rd v[8:11],v236 offset:10240
rd v[12:15],v237 offset:10240
mm a[240:243],v[32:35],v[16:19],a[240:243]
s_add_u32 m0, s83, 51712
mm a[240:243],v[36:39],v[20:23],a[240:243]
ld v233,s[44:47],s80 offen lds
mm a[244:247],v[44:47],v[20:23],a[244:247]
mm a[244:247],v[40:43],v[16:19],a[244:247]
rt v[220:221],v243 offset:16896
rt v[222:223],v243 offset:17920
mm a[32:35],v[48:51],v[16:19],a[32:35]
s_add_u32 m0, s83, 52864
mm a[32:35],v[52:55],v[20:23],a[32:35]
ld v234,s[44:47],s80 offen lds
mm a[36:39],v[60:63],v[20:23],a[36:39]
mm a[36:39],v[56:59],v[16:19],a[36:39]
rd v[16:19],v236 offset:0
rd v[20:23],v237 offset:0
mm a[60:63],v[56:59],v[24:27],a[60:63]
s_add_u32 m0, s83, 53888
mm a[60:63],v[60:63],v[28:31],a[60:63]
ld v235,s[44:47],s80 offen lds
mm a[56:59],v[52:55],v[28:31],a[56:59]
s_mov_b64 s[44:45], s[68:69]
s_mov_b32 s46, s70
mm a[56:59],v[48:51],v[24:27],a[56:59]
mm a[52:55],v[40:43],v[24:27],a[52:55]
mm a[52:55],v[44:47],v[28:31],a[52:55]
mm a[48:51],v[36:39],v[28:31],a[48:51]
wt lgkmcnt(2)
s_lshl_b32 s80, s77, 18
mm a[48:51],v[32:35],v[24:27],a[48:51]
rd v[24:27],v236 offset:2048
rd v[28:31],v237 offset:2048
mm a[96:99],v[192:195],v[160:163],a[96:99]
mm a[96:99],v[196:199],v[164:167],a[96:99]
mm a[100:103],v[204:207],v[164:167],a[100:103]
mm a[100:103],v[200:203],v[160:163],a[100:103]
rt v[32:33],v240 offset:128
rt v[34:35],v240 offset:1152
mm a[104:107],v[208:211],v[160:163],a[104:107]
mm a[104:107],v[212:215],v[164:167],a[104:107]
mm a[108:111],v[220:223],v[164:167],a[108:111]
wt lgkmcnt(2)
s_barrier
mm a[108:111],v[216:219],v[160:163],a[108:111]
mm a[140:143],v[216:219],v[168:171],a[140:143]
mm a[140:143],v[220:223],v[172:175],a[140:143]
mm a[136:139],v[212:215],v[172:175],a[136:139]
mm a[136:139],v[208:211],v[168:171],a[136:139]
rt v[36:37],v240 offset:17024
rt v[38:39],v240 offset:18048
mm a[124:127],v[200:203],v[168:171],a[124:127]
mm a[124:127],v[204:207],v[172:175],a[124:127]
mm a[120:123],v[196:199],v[172:175],a[120:123]
mm a[120:123],v[192:195],v[168:171],a[120:123]
mm a[232:235],v[192:195],v[176:179],a[232:235]
mm a[232:235],v[196:199],v[180:183],a[232:235]
mm a[236:239],v[204:207],v[180:183],a[236:239]
mm a[236:239],v[200:203],v[176:179],a[236:239]
rt v[40:41],v241 offset:128
rt v[42:43],v241 offset:1152
mm a[152:155],v[208:211],v[176:179],a[152:155]
mm a[152:155],v[212:215],v[180:183],a[152:155]
mm a[156:159],v[220:223],v[180:183],a[156:159]
mm a[156:159],v[216:219],v[176:179],a[156:159]
mm a[172:175],v[216:219],v[184:187],a[172:175]
mm a[172:175],v[220:223],v[188:191],a[172:175]
mm a[168:171],v[212:215],v[188:191],a[168:171]
mm a[168:171],v[208:211],v[184:187],a[168:171]
rt v[44:45],v241 offset:17024
rt v[46:47],v241 offset:18048
mm a[220:223],v[200:203],v[184:187],a[220:223]
mm a[220:223],v[204:207],v[188:191],a[220:223]
mm a[216:219],v[196:199],v[188:191],a[216:219]
mm a[216:219],v[192:195],v[184:187],a[216:219]
mm a[200:203],v[192:195],v[0:3],a[200:203]
mm a[200:203],v[196:199],v[4:7],a[200:203]
mm a[204:207],v[204:207],v[4:7],a[204:207]
mm a[204:207],v[200:203],v[0:3],a[204:207]
rt v[48:49],v242 offset:128
rt v[50:51],v242 offset:1152
mm a[184:187],v[208:211],v[0:3],a[184:187]
mm a[184:187],v[212:215],v[4:7],a[184:187]
mm a[188:191],v[220:223],v[4:7],a[188:191]
mm a[188:191],v[216:219],v[0:3],a[188:191]
mm a[12:15],v[216:219],v[8:11],a[12:15]
mm a[12:15],v[220:223],v[12:15],a[12:15]
buffer_load_dword v254, v135, s[68:71], s96 offen
mm a[8:11],v[212:215],v[12:15],a[8:11]
s_add_u32 s96, s96, 1048576
mm a[8:11],v[208:211],v[8:11],a[8:11]
rt v[52:53],v242 offset:17024
rt v[54:55],v242 offset:18048
mm a[76:79],v[200:203],v[8:11],a[76:79]
mm a[76:79],v[204:207],v[12:15],a[76:79]
buffer_load_dword v254, v135, s[68:71], s96 offen
mm a[72:75],v[196:199],v[12:15],a[72:75]
s_add_u32 s96, s96, 1048576
mm a[72:75],v[192:195],v[8:11],a[72:75]
rt v[56:57],v243 offset:128
rt v[58:59],v243 offset:1152
mm a[88:91],v[192:195],v[16:19],a[88:91]
mm a[88:91],v[196:199],v[20:23],a[88:91]
mm a[92:95],v[204:207],v[20:23],a[92:95]
mm a[92:95],v[200:203],v[16:19],a[92:95]
rt v[60:61],v243 offset:17024
rt v[62:63],v243 offset:18048
mm a[24:27],v[208:211],v[16:19],a[24:27]
mm a[24:27],v[212:215],v[20:23],a[24:27]
mm a[28:31],v[220:223],v[20:23],a[28:31]
mm a[28:31],v[216:219],v[16:19],a[28:31]
mm a[44:47],v[216:219],v[24:27],a[44:47]
mm a[44:47],v[220:223],v[28:31],a[44:47]
ld v[72:75],v255,s[64:67],s89 offen offset:1024 sc1
mm a[40:43],v[212:215],v[28:31],a[40:43]
mm a[40:43],v[208:211],v[24:27],a[40:43]
mm a[252:255],v[200:203],v[24:27],a[252:255]
mm a[252:255],v[204:207],v[28:31],a[252:255]
mm a[248:251],v[196:199],v[28:31],a[248:251]
wt vmcnt(3) lgkmcnt(0)
s_barrier
mm a[248:251],v[192:195],v[24:27],a[248:251]
mm a[112:115],v[32:35],v[160:163],a[112:115]
mm a[112:115],v[36:39],v[164:167],a[112:115]
mm a[116:119],v[44:47],v[164:167],a[116:119]
rt v[192:193],v240 offset:33792
rt v[194:195],v240 offset:34816
mm a[116:119],v[40:43],v[160:163],a[116:119]
rt v[196:197],v240 offset:50688
rt v[198:199],v240 offset:51712
mm a[128:131],v[48:51],v[160:163],a[128:131]
mm a[128:131],v[52:55],v[164:167],a[128:131]
mm a[132:135],v[60:63],v[164:167],a[132:135]
mm a[132:135],v[56:59],v[160:163],a[132:135]
rd v[160:163],v236 offset:57344
rd v[164:167],v237 offset:57344
mm a[148:151],v[56:59],v[168:171],a[148:151]
mm a[148:151],v[60:63],v[172:175],a[148:151]
mm a[144:147],v[52:55],v[172:175],a[144:147]
mm a[144:147],v[48:51],v[168:171],a[144:147]
rt v[200:201],v241 offset:33792
rt v[202:203],v241 offset:34816
mm a[228:231],v[40:43],v[168:171],a[228:231]
mm a[228:231],v[44:47],v[172:175],a[228:231]
mm a[224:227],v[36:39],v[172:175],a[224:227]
mm a[224:227],v[32:35],v[168:171],a[224:227]
rd v[168:171],v236 offset:59392
rd v[172:175],v237 offset:59392
mm a[208:211],v[32:35],v[176:179],a[208:211]
mm a[208:211],v[36:39],v[180:183],a[208:211]
mm a[212:215],v[44:47],v[180:183],a[212:215]
mm a[212:215],v[40:43],v[176:179],a[212:215]
rt v[204:205],v241 offset:50688
rt v[206:207],v241 offset:51712
mm a[160:163],v[48:51],v[176:179],a[160:163]
mm a[160:163],v[52:55],v[180:183],a[160:163]
mm a[164:167],v[60:63],v[180:183],a[164:167]
mm a[164:167],v[56:59],v[176:179],a[164:167]
rd v[176:179],v236 offset:49152
rd v[180:183],v237 offset:49152
mm a[180:183],v[56:59],v[184:187],a[180:183]
mm a[180:183],v[60:63],v[188:191],a[180:183]
mm a[176:179],v[52:55],v[188:191],a[176:179]
mm a[176:179],v[48:51],v[184:187],a[176:179]
rt v[208:209],v242 offset:33792
rt v[210:211],v242 offset:34816
mm a[196:199],v[40:43],v[184:187],a[196:199]
s_add_u32 m0, s83, 0
mm a[196:199],v[44:47],v[188:191],a[196:199]
ld v232,s[44:47],s80 offen lds
mm a[192:195],v[36:39],v[188:191],a[192:195]
mm a[192:195],v[32:35],v[184:187],a[192:195]
rd v[184:187],v236 offset:51200
rd v[188:191],v237 offset:51200
mm a[64:67],v[32:35],v[0:3],a[64:67]
s_add_u32 m0, s83, 1024
mm a[64:67],v[36:39],v[4:7],a[64:67]
ld v233,s[44:47],s80 offen lds
mm a[68:71],v[44:47],v[4:7],a[68:71]
mm a[68:71],v[40:43],v[0:3],a[68:71]
rt v[212:213],v242 offset:50688
rt v[214:215],v242 offset:51712
mm a[0:3],v[48:51],v[0:3],a[0:3]
s_add_u32 m0, s83, 2176
mm a[0:3],v[52:55],v[4:7],a[0:3]
ld v234,s[44:47],s80 offen lds
mm a[4:7],v[60:63],v[4:7],a[4:7]
mm a[4:7],v[56:59],v[0:3],a[4:7]
rd v[0:3],v236 offset:40960
rd v[4:7],v237 offset:40960
mm a[20:23],v[56:59],v[8:11],a[20:23]
s_add_u32 m0, s83, 3200
mm a[20:23],v[60:63],v[12:15],a[20:23]
ld v235,s[44:47],s80 offen lds
mm a[16:19],v[52:55],v[12:15],a[16:19]
s_add_u32 s80, s80, 524288
mm a[16:19],v[48:51],v[8:11],a[16:19]
rt v[216:217],v243 offset:33792
rt v[218:219],v243 offset:34816
mm a[84:87],v[40:43],v[8:11],a[84:87]
s_add_u32 m0, s83, 16896
mm a[84:87],v[44:47],v[12:15],a[84:87]
ld v232,s[44:47],s80 offen lds
mm a[80:83],v[36:39],v[12:15],a[80:83]
mm a[80:83],v[32:35],v[8:11],a[80:83]
rd v[8:11],v236 offset:43008
rd v[12:15],v237 offset:43008
mm a[240:243],v[32:35],v[16:19],a[240:243]
s_add_u32 m0, s83, 17920
mm a[240:243],v[36:39],v[20:23],a[240:243]
ld v233,s[44:47],s80 offen lds
mm a[244:247],v[44:47],v[20:23],a[244:247]
mm a[244:247],v[40:43],v[16:19],a[244:247]
rt v[220:221],v243 offset:50688
rt v[222:223],v243 offset:51712
mm a[32:35],v[48:51],v[16:19],a[32:35]
s_add_u32 m0, s83, 19072
mm a[32:35],v[52:55],v[20:23],a[32:35]
ld v234,s[44:47],s80 offen lds
mm a[36:39],v[60:63],v[20:23],a[36:39]
mm a[36:39],v[56:59],v[16:19],a[36:39]
rd v[16:19],v236 offset:32768
rd v[20:23],v237 offset:32768
mm a[60:63],v[56:59],v[24:27],a[60:63]
s_add_u32 m0, s83, 20096
mm a[60:63],v[60:63],v[28:31],a[60:63]
ld v235,s[44:47],s80 offen lds
mm a[56:59],v[52:55],v[28:31],a[56:59]
s_add_u32 s80, s80, 524288
mm a[56:59],v[48:51],v[24:27],a[56:59]
mm a[52:55],v[40:43],v[24:27],a[52:55]
mm a[52:55],v[44:47],v[28:31],a[52:55]
mm a[48:51],v[36:39],v[28:31],a[48:51]
wt lgkmcnt(2)
mm a[48:51],v[32:35],v[24:27],a[48:51]
rd v[24:27],v236 offset:34816
rd v[28:31],v237 offset:34816
mm a[96:99],v[192:195],v[160:163],a[96:99]
mm a[96:99],v[196:199],v[164:167],a[96:99]
mm a[100:103],v[204:207],v[164:167],a[100:103]
mm a[100:103],v[200:203],v[160:163],a[100:103]
rt v[32:33],v240 offset:33920
rt v[34:35],v240 offset:34944
mm a[104:107],v[208:211],v[160:163],a[104:107]
mm a[104:107],v[212:215],v[164:167],a[104:107]
mm a[108:111],v[220:223],v[164:167],a[108:111]
wt lgkmcnt(2)
s_barrier
mm a[108:111],v[216:219],v[160:163],a[108:111]
mm a[140:143],v[216:219],v[168:171],a[140:143]
wt vmcnt(8)
v_cmp_eq_u64 vcc, v[72:73], v[252:253]
mm a[140:143],v[220:223],v[172:175],a[140:143]
v_cmp_eq_u64 s[96:97], v[74:75], v[252:253]
mm a[136:139],v[212:215],v[172:175],a[136:139]
s_and_b64 vcc, vcc, s[96:97]
s_cmp_eq_u64 vcc, -1
s_cbranch_scc0 .La5_LK_XSPIN
.La5_LK_XOK:
mm a[136:139],v[208:211],v[168:171],a[136:139]
rt v[36:37],v240 offset:50816
rt v[38:39],v240 offset:51840
mm a[124:127],v[200:203],v[168:171],a[124:127]
s_mov_b32 s89, 49152
mm a[124:127],v[204:207],v[172:175],a[124:127]
mm a[120:123],v[196:199],v[172:175],a[120:123]
ar v224,a96
ar v225,a97
mm a[120:123],v[192:195],v[168:171],a[120:123]
ar v226,a98
ar v227,a99
mm a[232:235],v[192:195],v[176:179],a[232:235]
ar v228,a100
ar v229,a101
mm a[232:235],v[196:199],v[180:183],a[232:235]
ld v[128:131],v244,s[52:55],s91 offen offset:0 sc1
mm a[236:239],v[204:207],v[180:183],a[236:239]
ar v230,a102
ar v231,a103
mm a[236:239],v[200:203],v[176:179],a[236:239]
rt v[40:41],v241 offset:33920
rt v[42:43],v241 offset:34944
mm a[152:155],v[208:211],v[176:179],a[152:155]
cv v248,v224,v225
cv v249,v226,v227
mm a[152:155],v[212:215],v[180:183],a[152:155]
ld v[132:135],v245,s[52:55],s91 offen offset:0 sc1
mm a[156:159],v[220:223],v[180:183],a[156:159]
cv v250,v228,v229
cv v251,v230,v231
mm a[156:159],v[216:219],v[176:179],a[156:159]
st v[248:251],v255,s[48:51],s89 offen sc1
mm a[172:175],v[216:219],v[184:187],a[172:175]
s_mov_b32 s89, 50176
ds_write_b128 v238, v[248:251] offset:0
mm a[172:175],v[220:223],v[188:191],a[172:175]
ld v[136:139],v246,s[52:55],s91 offen offset:0 sc1
mm a[168:171],v[212:215],v[188:191],a[168:171]
ar v224,a104
ar v225,a105
mm a[168:171],v[208:211],v[184:187],a[168:171]
rt v[44:45],v241 offset:50816
rt v[46:47],v241 offset:51840
mm a[220:223],v[200:203],v[184:187],a[220:223]
ar v226,a106
ar v227,a107
mm a[220:223],v[204:207],v[188:191],a[220:223]
ld v[140:143],v247,s[52:55],s91 offen offset:0 sc1
mm a[216:219],v[196:199],v[188:191],a[216:219]
ar v228,a108
ar v229,a109
mm a[216:219],v[192:195],v[184:187],a[216:219]
ar v230,a110
ar v231,a111
mm a[200:203],v[192:195],v[0:3],a[200:203]
cv v248,v224,v225
cv v249,v226,v227
.La5_LW1_3:
s_cbranch_execz .La5_LW1_7
mm a[200:203],v[196:199],v[4:7],a[200:203]
ld v[144:147],v244,s[52:55],s91 offen offset:1024 sc1
mm a[204:207],v[204:207],v[4:7],a[204:207]
cv v250,v228,v229
cv v251,v230,v231
mm a[204:207],v[200:203],v[0:3],a[204:207]
rt v[48:49],v242 offset:33920
rt v[50:51],v242 offset:34944
mm a[184:187],v[208:211],v[0:3],a[184:187]
st v[248:251],v255,s[48:51],s89 offen sc1
mm a[184:187],v[212:215],v[4:7],a[184:187]
ld v[148:151],v245,s[52:55],s91 offen offset:1024 sc1
mm a[188:191],v[220:223],v[4:7],a[188:191]
s_mov_b32 s89, 51200
ds_write_b128 v238, v[248:251] offset:1024
mm a[188:191],v[216:219],v[0:3],a[188:191]
mm a[12:15],v[216:219],v[8:11],a[12:15]
mm a[12:15],v[220:223],v[12:15],a[12:15]
ld v[152:155],v246,s[52:55],s91 offen offset:1024 sc1
mm a[8:11],v[212:215],v[12:15],a[8:11]
mm a[8:11],v[208:211],v[8:11],a[8:11]
rt v[52:53],v242 offset:50816
rt v[54:55],v242 offset:51840
mm a[76:79],v[200:203],v[8:11],a[76:79]
mm a[76:79],v[204:207],v[12:15],a[76:79]
ld v[156:159],v247,s[52:55],s91 offen offset:1024 sc1
mm a[72:75],v[196:199],v[12:15],a[72:75]
mm a[72:75],v[192:195],v[8:11],a[72:75]
rt v[56:57],v243 offset:33920
rt v[58:59],v243 offset:34944
mm a[88:91],v[192:195],v[16:19],a[88:91]
mm a[88:91],v[196:199],v[20:23],a[88:91]
mm a[92:95],v[204:207],v[20:23],a[92:95]
mm a[92:95],v[200:203],v[16:19],a[92:95]
rt v[60:61],v243 offset:50816
rt v[62:63],v243 offset:51840
mm a[24:27],v[208:211],v[16:19],a[24:27]
mm a[24:27],v[212:215],v[20:23],a[24:27]
mm a[28:31],v[220:223],v[20:23],a[28:31]
mm a[28:31],v[216:219],v[16:19],a[28:31]
mm a[44:47],v[216:219],v[24:27],a[44:47]
mm a[44:47],v[220:223],v[28:31],a[44:47]
mm a[40:43],v[212:215],v[28:31],a[40:43]
mm a[40:43],v[208:211],v[24:27],a[40:43]
mm a[252:255],v[200:203],v[24:27],a[252:255]
mm a[252:255],v[204:207],v[28:31],a[252:255]
mm a[248:251],v[196:199],v[28:31],a[248:251]
wt vmcnt(10) lgkmcnt(0)
s_barrier
mm a[248:251],v[192:195],v[24:27],a[248:251]
mm a[112:115],v[32:35],v[160:163],a[112:115]
s_add_u32 m0, s83, 33792
mm a[112:115],v[36:39],v[164:167],a[112:115]
ld v232,s[44:47],s80 offen lds
mm a[116:119],v[44:47],v[164:167],a[116:119]
mm a[116:119],v[40:43],v[160:163],a[116:119]
rt v[192:193],v240 offset:0
rt v[194:195],v240 offset:1024
mm a[128:131],v[48:51],v[160:163],a[128:131]
s_add_u32 m0, s83, 34816
mm a[128:131],v[52:55],v[164:167],a[128:131]
ld v233,s[44:47],s80 offen lds
mm a[132:135],v[60:63],v[164:167],a[132:135]
mm a[132:135],v[56:59],v[160:163],a[132:135]
rt v[196:197],v240 offset:8448
rt v[198:199],v240 offset:9472
mm a[148:151],v[56:59],v[168:171],a[148:151]
s_add_u32 m0, s83, 35968
mm a[148:151],v[60:63],v[172:175],a[148:151]
ld v234,s[44:47],s80 offen lds
mm a[144:147],v[52:55],v[172:175],a[144:147]
ar v224,a112
ar v225,a113
mm a[144:147],v[48:51],v[168:171],a[144:147]
rt v[200:201],v241 offset:0
rt v[202:203],v241 offset:1024
mm a[228:231],v[40:43],v[168:171],a[228:231]
s_add_u32 m0, s83, 36992
ar v226,a114
mm a[228:231],v[44:47],v[172:175],a[228:231]
ld v235,s[44:47],s80 offen lds
mm a[224:227],v[36:39],v[172:175],a[224:227]
s_add_u32 s80, s80, 524288
ar v227,a115
mm a[224:227],v[32:35],v[168:171],a[224:227]
rt v[204:205],v241 offset:8448
rt v[206:207],v241 offset:9472
mm a[208:211],v[32:35],v[176:179],a[208:211]
s_add_u32 m0, s83, 50688
ar v228,a116
mm a[208:211],v[36:39],v[180:183],a[208:211]
ld v232,s[44:47],s80 offen lds
mm a[212:215],v[44:47],v[180:183],a[212:215]
ar v229,a117
ar v230,a118
mm a[212:215],v[40:43],v[176:179],a[212:215]
rt v[208:209],v242 offset:0
rt v[210:211],v242 offset:1024
mm a[160:163],v[48:51],v[176:179],a[160:163]
s_add_u32 m0, s83, 51712
ar v231,a119
mm a[160:163],v[52:55],v[180:183],a[160:163]
ld v233,s[44:47],s80 offen lds
mm a[164:167],v[60:63],v[180:183],a[164:167]
cv v248,v224,v225
cv v249,v226,v227
mm a[164:167],v[56:59],v[176:179],a[164:167]
rt v[212:213],v242 offset:8448
rt v[214:215],v242 offset:9472
mm a[180:183],v[56:59],v[184:187],a[180:183]
s_add_u32 m0, s83, 52864
cv v250,v228,v229
mm a[180:183],v[60:63],v[188:191],a[180:183]
ld v234,s[44:47],s80 offen lds
mm a[176:179],v[52:55],v[188:191],a[176:179]
cv v251,v230,v231
st v[248:251],v255,s[48:51],s89 offen sc1
mm a[176:179],v[48:51],v[184:187],a[176:179]
rt v[216:217],v243 offset:0
rt v[218:219],v243 offset:1024
mm a[196:199],v[40:43],v[184:187],a[196:199]
s_add_u32 m0, s83, 53888
s_mov_b32 s89, 52224
mm a[196:199],v[44:47],v[188:191],a[196:199]
ld v235,s[44:47],s80 offen lds
mm a[192:195],v[36:39],v[188:191],a[192:195]
s_add_u32 s80, s80, 524288
ds_write_b128 v238, v[248:251] offset:2048
mm a[192:195],v[32:35],v[184:187],a[192:195]
rt v[220:221],v243 offset:8448
rt v[222:223],v243 offset:9472
mm a[64:67],v[32:35],v[0:3],a[64:67]
mm a[64:67],v[36:39],v[4:7],a[64:67]
ld v[160:163],v244,s[52:55],s91 offen offset:2048 sc1
mm a[68:71],v[44:47],v[4:7],a[68:71]
mm a[68:71],v[40:43],v[0:3],a[68:71]
mm a[0:3],v[48:51],v[0:3],a[0:3]
mm a[0:3],v[52:55],v[4:7],a[0:3]
ld v[164:167],v245,s[52:55],s91 offen offset:2048 sc1
mm a[4:7],v[60:63],v[4:7],a[4:7]
mm a[4:7],v[56:59],v[0:3],a[4:7]
mm a[20:23],v[56:59],v[8:11],a[20:23]
mm a[20:23],v[60:63],v[12:15],a[20:23]
ld v[168:171],v246,s[52:55],s91 offen offset:2048 sc1
mm a[16:19],v[52:55],v[12:15],a[16:19]
mm a[16:19],v[48:51],v[8:11],a[16:19]
mm a[84:87],v[40:43],v[8:11],a[84:87]
mm a[84:87],v[44:47],v[12:15],a[84:87]
ld v[172:175],v247,s[52:55],s91 offen offset:2048 sc1
mm a[80:83],v[36:39],v[12:15],a[80:83]
mm a[80:83],v[32:35],v[8:11],a[80:83]
mm a[240:243],v[32:35],v[16:19],a[240:243]
mm a[240:243],v[36:39],v[20:23],a[240:243]
mm a[244:247],v[44:47],v[20:23],a[244:247]
mm a[244:247],v[40:43],v[16:19],a[244:247]
mm a[32:35],v[48:51],v[16:19],a[32:35]
mm a[32:35],v[52:55],v[20:23],a[32:35]
mm a[36:39],v[60:63],v[20:23],a[36:39]
mm a[36:39],v[56:59],v[16:19],a[36:39]
mm a[60:63],v[56:59],v[24:27],a[60:63]
mm a[60:63],v[60:63],v[28:31],a[60:63]
mm a[56:59],v[52:55],v[28:31],a[56:59]
mm a[56:59],v[48:51],v[24:27],a[56:59]
mm a[52:55],v[40:43],v[24:27],a[52:55]
mm a[52:55],v[44:47],v[28:31],a[52:55]
mm a[48:51],v[36:39],v[28:31],a[48:51]
mm a[48:51],v[32:35],v[24:27],a[48:51]
s_branch .La5_LQ_START
.La5_LV_XSPIN:
s_mov_b32 s93, 0
.La5_LV_XSPIN_L:
s_add_u32 s93, s93, 1
s_cmp_gt_u32 s93, 2097152
s_cbranch_scc1 .La5_LV_XOK
s_sleep 8
ld v[72:75],v255,s[64:67],s89 offen offset:1024 sc1
wt vmcnt(0)
v_cmp_eq_u64 vcc, v[72:73], v[252:253]
v_cmp_eq_u64 s[96:97], v[74:75], v[252:253]
s_nop 1
s_and_b64 vcc, vcc, s[96:97]
s_cmp_eq_u64 vcc, -1
s_nop 0
s_nop 0
s_cbranch_scc0 .La5_LV_XSPIN_L
s_branch .La5_LV_XOK
.La5_LK_XSPIN:
s_mov_b32 s93, 0
.La5_LK_XSPIN_L:
s_add_u32 s93, s93, 1
s_cmp_gt_u32 s93, 2097152
s_nop 0
s_nop 0
s_cbranch_scc1 .La5_LK_XOK
s_sleep 8
ld v[72:75],v255,s[64:67],s89 offen offset:1024 sc1
wt vmcnt(0)
v_cmp_eq_u64 vcc, v[72:73], v[252:253]
v_cmp_eq_u64 s[96:97], v[74:75], v[252:253]
s_nop 1
s_and_b64 vcc, vcc, s[96:97]
s_cmp_eq_u64 vcc, -1
s_cbranch_scc0 .La5_LK_XSPIN_L
s_branch .La5_LK_XOK
; ---- V tile (waves 1, 3): the same with the operands exchanged (V comes out transposed)
.La5_LV_START:
.La5_LW1_4:
s_cbranch_execz .La5_LW1_8
mm a[96:99],v[160:163],v[192:195],0
s_add_u32 m0, s83, 33792
mm a[96:99],v[164:167],v[196:199],a[96:99]
ld v232,s[44:47],s80 offen lds
mm a[104:107],v[164:167],v[204:207],0
rd v[0:3],v236 offset:8192
rd v[4:7],v237 offset:8192
s_add_u32 m0, s83, 34816
mm a[104:107],v[160:163],v[200:203],a[104:107]
rd v[8:11],v236 offset:10240
rd v[12:15],v237 offset:10240
ld v233,s[44:47],s80 offen lds
mm a[112:115],v[160:163],v[208:211],0
s_add_u32 m0, s83, 35968
mm a[112:115],v[164:167],v[212:215],a[112:115]
ld v234,s[44:47],s80 offen lds
mm a[128:131],v[164:167],v[220:223],0
rd v[16:19],v236 offset:0
rd v[20:23],v237 offset:0
s_add_u32 m0, s83, 36992
mm a[128:131],v[160:163],v[216:219],a[128:131]
rd v[24:27],v236 offset:2048
rd v[28:31],v237 offset:2048
ld v235,s[44:47],s80 offen lds
mm a[132:135],v[168:171],v[216:219],0
s_mov_b32 s80, 2883584
mm a[132:135],v[172:175],v[220:223],a[132:135]
mm a[116:119],v[172:175],v[212:215],0
s_xor_b32 m0, s82, 32768
mm a[116:119],v[168:171],v[208:211],a[116:119]
rt v[32:33],v240 offset:128
rt v[34:35],v240 offset:1152
ld v224,s[40:43],s79 offen lds
mm a[108:111],v[168:171],v[200:203],0
s_xor_b32 m0, s82, 33792
mm a[108:111],v[172:175],v[204:207],a[108:111]
ld v225,s[40:43],s79 offen lds
mm a[100:103],v[172:175],v[196:199],0
s_xor_b32 m0, s82, 34816
mm a[100:103],v[168:171],v[192:195],a[100:103]
rt v[36:37],v240 offset:17024
rt v[38:39],v240 offset:18048
ld v226,s[40:43],s79 offen lds
mm a[232:235],v[176:179],v[192:195],0
s_xor_b32 m0, s82, 35840
mm a[232:235],v[180:183],v[196:199],a[232:235]
ld v227,s[40:43],s79 offen lds
mm a[152:155],v[180:183],v[204:207],0
s_xor_b32 m0, s82, 36864
mm a[152:155],v[176:179],v[200:203],a[152:155]
rt v[40:41],v241 offset:128
rt v[42:43],v241 offset:1152
ld v228,s[40:43],s79 offen lds
mm a[208:211],v[176:179],v[208:211],0
s_xor_b32 m0, s82, 37888
mm a[208:211],v[180:183],v[212:215],a[208:211]
ld v229,s[40:43],s79 offen lds
mm a[160:163],v[180:183],v[220:223],0
s_xor_b32 m0, s82, 38912
mm a[160:163],v[176:179],v[216:219],a[160:163]
ld v230,s[40:43],s79 offen lds
mm a[164:167],v[184:187],v[216:219],0
s_xor_b32 m0, s82, 39936
mm a[164:167],v[188:191],v[220:223],a[164:167]
ld v231,s[40:43],s79 offen lds
mm a[212:215],v[188:191],v[212:215],0
s_mov_b32 s79, 768
mm a[212:215],v[184:187],v[208:211],a[212:215]
rt v[44:45],v241 offset:17024
rt v[46:47],v241 offset:18048
mm a[156:159],v[184:187],v[200:203],0
s_add_u32 m0, s83, 50688
mm a[156:159],v[188:191],v[204:207],a[156:159]
ld v232,s[44:47],s80 offen lds
mm a[236:239],v[188:191],v[196:199],0
wt lgkmcnt(8)
s_add_u32 m0, s83, 51712
mm a[236:239],v[184:187],v[192:195],a[236:239]
ld v233,s[44:47],s80 offen lds
mm a[200:203],v[0:3],v[192:195],0
s_add_u32 m0, s83, 52864
mm a[200:203],v[4:7],v[196:199],a[200:203]
ld v234,s[44:47],s80 offen lds
mm a[184:187],v[4:7],v[204:207],0
s_add_u32 m0, s83, 53888
mm a[184:187],v[0:3],v[200:203],a[184:187]
rt v[48:49],v242 offset:128
rt v[50:51],v242 offset:1152
ld v235,s[44:47],s80 offen lds
mm a[64:67],v[0:3],v[208:211],0
s_mov_b32 s80, 3145728
mm a[64:67],v[4:7],v[212:215],a[64:67]
mm a[0:3],v[4:7],v[220:223],0
mm a[0:3],v[0:3],v[216:219],a[0:3]
mm a[4:7],v[8:11],v[216:219],0
mm a[4:7],v[12:15],v[220:223],a[4:7]
buffer_load_dword v70, v118, s[60:63], 0 offen
mm a[68:71],v[12:15],v[212:215],0
mm a[68:71],v[8:11],v[208:211],a[68:71]
rt v[52:53],v242 offset:17024
rt v[54:55],v242 offset:18048
mm a[188:191],v[8:11],v[200:203],0
mm a[188:191],v[12:15],v[204:207],a[188:191]
ld v[72:75],v119,s[60:63],0 offen offset:0
mm a[204:207],v[12:15],v[196:199],0
mm a[204:207],v[8:11],v[192:195],a[204:207]
rt v[56:57],v243 offset:128
rt v[58:59],v243 offset:1152
mm a[88:91],v[16:19],v[192:195],0
mm a[88:91],v[20:23],v[196:199],a[88:91]
ld v[76:79],v119,s[60:63],0 offen offset:128
mm a[24:27],v[20:23],v[204:207],0
mm a[24:27],v[16:19],v[200:203],a[24:27]
rt v[60:61],v243 offset:17024
rt v[62:63],v243 offset:18048
mm a[240:243],v[16:19],v[208:211],0
mm a[240:243],v[20:23],v[212:215],a[240:243]
ld v[80:83],v119,s[60:63],0 offen offset:256
mm a[32:35],v[20:23],v[220:223],0
mm a[32:35],v[16:19],v[216:219],a[32:35]
mm a[36:39],v[24:27],v[216:219],0
mm a[36:39],v[28:31],v[220:223],a[36:39]
ld v[84:87],v119,s[60:63],0 offen offset:384
mm a[244:247],v[28:31],v[212:215],0
mm a[244:247],v[24:27],v[208:211],a[244:247]
mm a[28:31],v[24:27],v[200:203],0
mm a[28:31],v[28:31],v[204:207],a[28:31]
mm a[92:95],v[28:31],v[196:199],0
mm a[92:95],v[24:27],v[192:195],a[92:95]
s_mov_b64 exec, 0
s_cmp_eq_u32 s3, 1
s_cbranch_scc1 .La5_LW2_1
s_branch .La5_LW2_R1
.La5_LW2_R1:
s_mov_b64 exec, -1
wt vmcnt(5) lgkmcnt(0)
s_barrier
mm a[120:123],v[160:163],v[32:35],0
mm a[120:123],v[164:167],v[36:39],a[120:123]
mm a[136:139],v[164:167],v[44:47],0
rt v[192:193],v240 offset:33792
rt v[194:195],v240 offset:34816
mm a[136:139],v[160:163],v[40:43],a[136:139]
rt v[196:197],v240 offset:50688
rt v[198:199],v240 offset:51712
mm a[224:227],v[160:163],v[48:51],0
mm a[224:227],v[164:167],v[52:55],a[224:227]
mm a[144:147],v[164:167],v[60:63],0
mm a[144:147],v[160:163],v[56:59],a[144:147]
rd v[160:163],v236 offset:57344
rd v[164:167],v237 offset:57344
mm a[148:151],v[168:171],v[56:59],0
mm a[148:151],v[172:175],v[60:63],a[148:151]
mm a[228:231],v[172:175],v[52:55],0
mm a[228:231],v[168:171],v[48:51],a[228:231]
rt v[200:201],v241 offset:33792
rt v[202:203],v241 offset:34816
mm a[140:143],v[168:171],v[40:43],0
mm a[140:143],v[172:175],v[44:47],a[140:143]
mm a[124:127],v[172:175],v[36:39],0
mm a[124:127],v[168:171],v[32:35],a[124:127]
rd v[168:171],v236 offset:59392
rd v[172:175],v237 offset:59392
mm a[216:219],v[176:179],v[32:35],0
s_xor_b32 m0, s82, 0
mm a[216:219],v[180:183],v[36:39],a[216:219]
ld v224,s[40:43],s79 offen lds
mm a[168:171],v[180:183],v[44:47],0
mm a[168:171],v[176:179],v[40:43],a[168:171]
rt v[204:205],v241 offset:50688
rt v[206:207],v241 offset:51712
s_xor_b32 m0, s82, 1024
mm a[192:195],v[176:179],v[48:51],0
ld v225,s[40:43],s79 offen lds
mm a[192:195],v[180:183],v[52:55],a[192:195]
mm a[176:179],v[180:183],v[60:63],0
s_xor_b32 m0, s82, 2048
mm a[176:179],v[176:179],v[56:59],a[176:179]
rd v[176:179],v236 offset:49152
rd v[180:183],v237 offset:49152
ld v226,s[40:43],s79 offen lds
mm a[180:183],v[184:187],v[56:59],0
mm a[180:183],v[188:191],v[60:63],a[180:183]
s_xor_b32 m0, s82, 3072
mm a[196:199],v[188:191],v[52:55],0
ld v227,s[40:43],s79 offen lds
mm a[196:199],v[184:187],v[48:51],a[196:199]
rt v[208:209],v242 offset:33792
rt v[210:211],v242 offset:34816
mm a[172:175],v[184:187],v[40:43],0
s_xor_b32 m0, s82, 4096
mm a[172:175],v[188:191],v[44:47],a[172:175]
ld v228,s[40:43],s79 offen lds
mm a[220:223],v[188:191],v[36:39],0
mm a[220:223],v[184:187],v[32:35],a[220:223]
rd v[184:187],v236 offset:51200
rd v[188:191],v237 offset:51200
s_xor_b32 m0, s82, 5120
mm a[72:75],v[0:3],v[32:35],0
ld v229,s[40:43],s79 offen lds
mm a[72:75],v[4:7],v[36:39],a[72:75]
mm a[8:11],v[4:7],v[44:47],0
s_xor_b32 m0, s82, 6144
mm a[8:11],v[0:3],v[40:43],a[8:11]
rt v[212:213],v242 offset:50688
rt v[214:215],v242 offset:51712
ld v230,s[40:43],s79 offen lds
mm a[80:83],v[0:3],v[48:51],0
mm a[80:83],v[4:7],v[52:55],a[80:83]
s_xor_b32 m0, s82, 7168
mm a[16:19],v[4:7],v[60:63],0
ld v231,s[40:43],s79 offen lds
mm a[16:19],v[0:3],v[56:59],a[16:19]
rd v[0:3],v236 offset:40960
rd v[4:7],v237 offset:40960
s_mov_b32 s79, 896
mm a[20:23],v[8:11],v[56:59],0
mm a[20:23],v[12:15],v[60:63],a[20:23]
s_add_u32 m0, s83, 0
mm a[84:87],v[12:15],v[52:55],0
ld v232,s[44:47],s80 offen lds
mm a[84:87],v[8:11],v[48:51],a[84:87]
rt v[216:217],v243 offset:33792
rt v[218:219],v243 offset:34816
mm a[12:15],v[8:11],v[40:43],0
s_add_u32 m0, s83, 1024
mm a[12:15],v[12:15],v[44:47],a[12:15]
ld v233,s[44:47],s80 offen lds
mm a[76:79],v[12:15],v[36:39],0
wt vmcnt(10)
v_lshlrev_b32 v70, 16, v70
mm a[76:79],v[8:11],v[32:35],a[76:79]
rd v[8:11],v236 offset:43008
rd v[12:15],v237 offset:43008
s_add_u32 m0, s83, 2176
mm a[248:251],v[16:19],v[32:35],0
ld v234,s[44:47],s80 offen lds
mm a[248:251],v[20:23],v[36:39],a[248:251]
v_lshlrev_b32 v88, 16, v72
v_and_b32 v89, 0xffff0000, v72
mm a[40:43],v[20:23],v[44:47],0
s_add_u32 m0, s83, 3200
v_sub_f32 v88, v88, v70
mm a[40:43],v[16:19],v[40:43],a[40:43]
rt v[220:221],v243 offset:50688
rt v[222:223],v243 offset:51712
ld v235,s[44:47],s80 offen lds
mm a[48:51],v[16:19],v[48:51],0
s_mov_b32 s80, 3407872
v_sub_f32 v89, v89, v70
mm a[48:51],v[20:23],v[52:55],a[48:51]
v_add_f32 v120, v88, v89
v_mul_f32 v122, v88, v88
mm a[56:59],v[20:23],v[60:63],0
s_add_u32 m0, s83, 16896
v_mul_f32 v123, v89, v89
mm a[56:59],v[16:19],v[56:59],a[56:59]
rd v[16:19],v236 offset:32768
rd v[20:23],v237 offset:32768
ld v232,s[44:47],s80 offen lds
mm a[60:63],v[24:27],v[56:59],0
v_lshlrev_b32 v90, 16, v73
v_and_b32 v91, 0xffff0000, v73
mm a[60:63],v[28:31],v[60:63],a[60:63]
s_add_u32 m0, s83, 17920
v_sub_f32 v90, v90, v70
mm a[52:55],v[28:31],v[52:55],0
ld v233,s[44:47],s80 offen lds
mm a[52:55],v[24:27],v[48:51],a[52:55]
v_sub_f32 v91, v91, v70
v_add_f32 v124, v90, v91
mm a[44:47],v[24:27],v[40:43],0
s_add_u32 m0, s83, 19072
v_fmac_f32 v122, v90, v90
mm a[44:47],v[28:31],v[44:47],a[44:47]
ld v234,s[44:47],s80 offen lds
mm a[252:255],v[28:31],v[36:39],0
wt lgkmcnt(2)
v_add_f32 v120, v120, v124
mm a[252:255],v[24:27],v[32:35],a[252:255]
rd v[24:27],v236 offset:34816
rd v[28:31],v237 offset:34816
s_add_u32 m0, s83, 20096
mm a[96:99],v[160:163],v[192:195],a[96:99]
ld v235,s[44:47],s80 offen lds
mm a[96:99],v[164:167],v[196:199],a[96:99]
s_mov_b32 s80, 3670016
v_fmac_f32 v123, v91, v91
mm a[104:107],v[164:167],v[204:207],a[104:107]
v_lshlrev_b32 v92, 16, v74
v_and_b32 v93, 0xffff0000, v74
mm a[104:107],v[160:163],v[200:203],a[104:107]
rt v[32:33],v240 offset:33920
rt v[34:35],v240 offset:34944
mm a[112:115],v[160:163],v[208:211],a[112:115]
v_sub_f32 v92, v92, v70
v_sub_f32 v93, v93, v70
mm a[112:115],v[164:167],v[212:215],a[112:115]
v_add_f32 v124, v92, v93
v_fmac_f32 v122, v92, v92
mm a[128:131],v[164:167],v[220:223],a[128:131]
wt lgkmcnt(2)
s_barrier
mm a[128:131],v[160:163],v[216:219],a[128:131]
v_add_f32 v120, v120, v124
v_fmac_f32 v123, v93, v93
mm a[132:135],v[168:171],v[216:219],a[132:135]
s_xor_b32 m0, s82, 32768
v_lshlrev_b32 v94, 16, v75
mm a[132:135],v[172:175],v[220:223],a[132:135]
ld v224,s[40:43],s79 offen lds
mm a[116:119],v[172:175],v[212:215],a[116:119]
v_and_b32 v95, 0xffff0000, v75
v_sub_f32 v94, v94, v70
mm a[116:119],v[168:171],v[208:211],a[116:119]
rt v[36:37],v240 offset:50816
rt v[38:39],v240 offset:51840
mm a[108:111],v[168:171],v[200:203],a[108:111]
s_xor_b32 m0, s82, 33792
v_sub_f32 v95, v95, v70
mm a[108:111],v[172:175],v[204:207],a[108:111]
ld v225,s[40:43],s79 offen lds
mm a[100:103],v[172:175],v[196:199],a[100:103]
v_add_f32 v124, v94, v95
v_fmac_f32 v122, v94, v94
mm a[100:103],v[168:171],v[192:195],a[100:103]
v_add_f32 v120, v120, v124
v_fmac_f32 v123, v95, v95
mm a[232:235],v[176:179],v[192:195],a[232:235]
s_xor_b32 m0, s82, 34816
v_lshlrev_b32 v96, 16, v76
mm a[232:235],v[180:183],v[196:199],a[232:235]
ld v226,s[40:43],s79 offen lds
mm a[152:155],v[180:183],v[204:207],a[152:155]
v_and_b32 v97, 0xffff0000, v76
v_sub_f32 v96, v96, v70
mm a[152:155],v[176:179],v[200:203],a[152:155]
rt v[40:41],v241 offset:33920
rt v[42:43],v241 offset:34944
mm a[208:211],v[176:179],v[208:211],a[208:211]
s_xor_b32 m0, s82, 35840
v_sub_f32 v97, v97, v70
mm a[208:211],v[180:183],v[212:215],a[208:211]
ld v227,s[40:43],s79 offen lds
mm a[160:163],v[180:183],v[220:223],a[160:163]
v_add_f32 v124, v96, v97
v_fmac_f32 v122, v96, v96
mm a[160:163],v[176:179],v[216:219],a[160:163]
v_add_f32 v120, v120, v124
v_fmac_f32 v123, v97, v97
mm a[164:167],v[184:187],v[216:219],a[164:167]
.La5_LW1_5:
s_cbranch_execz .La5_LW1_9
s_xor_b32 m0, s82, 36864
v_lshlrev_b32 v98, 16, v77
mm a[164:167],v[188:191],v[220:223],a[164:167]
ld v228,s[40:43],s79 offen lds
mm a[212:215],v[188:191],v[212:215],a[212:215]
v_and_b32 v99, 0xffff0000, v77
v_sub_f32 v98, v98, v70
mm a[212:215],v[184:187],v[208:211],a[212:215]
rt v[44:45],v241 offset:50816
rt v[46:47],v241 offset:51840
mm a[156:159],v[184:187],v[200:203],a[156:159]
s_xor_b32 m0, s82, 37888
v_sub_f32 v99, v99, v70
mm a[156:159],v[188:191],v[204:207],a[156:159]
ld v229,s[40:43],s79 offen lds
mm a[236:239],v[188:191],v[196:199],a[236:239]
v_add_f32 v124, v98, v99
v_fmac_f32 v122, v98, v98
mm a[236:239],v[184:187],v[192:195],a[236:239]
v_add_f32 v120, v120, v124
v_fmac_f32 v123, v99, v99
mm a[200:203],v[0:3],v[192:195],a[200:203]
s_xor_b32 m0, s82, 38912
v_lshlrev_b32 v100, 16, v78
mm a[200:203],v[4:7],v[196:199],a[200:203]
ld v230,s[40:43],s79 offen lds
mm a[184:187],v[4:7],v[204:207],a[184:187]
v_and_b32 v101, 0xffff0000, v78
v_sub_f32 v100, v100, v70
mm a[184:187],v[0:3],v[200:203],a[184:187]
rt v[48:49],v242 offset:33920
rt v[50:51],v242 offset:34944
mm a[64:67],v[0:3],v[208:211],a[64:67]
s_xor_b32 m0, s82, 39936
v_sub_f32 v101, v101, v70
mm a[64:67],v[4:7],v[212:215],a[64:67]
ld v231,s[40:43],s79 offen lds
mm a[0:3],v[4:7],v[220:223],a[0:3]
s_mov_b32 s79, 0
v_add_f32 v124, v100, v101
mm a[0:3],v[0:3],v[216:219],a[0:3]
v_fmac_f32 v122, v100, v100
v_add_f32 v120, v120, v124
mm a[4:7],v[8:11],v[216:219],a[4:7]
v_fmac_f32 v123, v101, v101
v_lshlrev_b32 v102, 16, v79
mm a[4:7],v[12:15],v[220:223],a[4:7]
buffer_load_dword v254, v132, s[44:47], s86 offen
mm a[68:71],v[12:15],v[212:215],a[68:71]
s_add_u32 s86, s86, 2097152
s_and_b32 s86, s86, 4194303
mm a[68:71],v[8:11],v[208:211],a[68:71]
rt v[52:53],v242 offset:50816
rt v[54:55],v242 offset:51840
mm a[188:191],v[8:11],v[200:203],a[188:191]
v_and_b32 v103, 0xffff0000, v79
v_sub_f32 v102, v102, v70
mm a[188:191],v[12:15],v[204:207],a[188:191]
v_sub_f32 v103, v103, v70
v_add_f32 v124, v102, v103
mm a[204:207],v[12:15],v[196:199],a[204:207]
v_fmac_f32 v122, v102, v102
v_add_f32 v120, v120, v124
mm a[204:207],v[8:11],v[192:195],a[204:207]
rt v[56:57],v243 offset:33920
rt v[58:59],v243 offset:34944
mm a[88:91],v[16:19],v[192:195],a[88:91]
v_fmac_f32 v123, v103, v103
v_lshlrev_b32 v104, 16, v80
mm a[88:91],v[20:23],v[196:199],a[88:91]
v_and_b32 v105, 0xffff0000, v80
v_sub_f32 v104, v104, v70
mm a[24:27],v[20:23],v[204:207],a[24:27]
v_sub_f32 v105, v105, v70
v_add_f32 v124, v104, v105
mm a[24:27],v[16:19],v[200:203],a[24:27]
rt v[60:61],v243 offset:50816
rt v[62:63],v243 offset:51840
mm a[240:243],v[16:19],v[208:211],a[240:243]
v_fmac_f32 v122, v104, v104
v_add_f32 v120, v120, v124
mm a[240:243],v[20:23],v[212:215],a[240:243]
v_fmac_f32 v123, v105, v105
v_lshlrev_b32 v106, 16, v81
mm a[32:35],v[20:23],v[220:223],a[32:35]
v_and_b32 v107, 0xffff0000, v81
v_sub_f32 v106, v106, v70
mm a[32:35],v[16:19],v[216:219],a[32:35]
v_sub_f32 v107, v107, v70
v_add_f32 v124, v106, v107
mm a[36:39],v[24:27],v[216:219],a[36:39]
v_fmac_f32 v122, v106, v106
v_add_f32 v120, v120, v124
mm a[36:39],v[28:31],v[220:223],a[36:39]
v_fmac_f32 v123, v107, v107
v_lshlrev_b32 v108, 16, v82
mm a[244:247],v[28:31],v[212:215],a[244:247]
v_and_b32 v109, 0xffff0000, v82
v_sub_f32 v108, v108, v70
mm a[244:247],v[24:27],v[208:211],a[244:247]
v_sub_f32 v109, v109, v70
v_add_f32 v124, v108, v109
mm a[28:31],v[24:27],v[200:203],a[28:31]
v_fmac_f32 v122, v108, v108
v_add_f32 v120, v120, v124
mm a[28:31],v[28:31],v[204:207],a[28:31]
v_fmac_f32 v123, v109, v109
v_lshlrev_b32 v110, 16, v83
mm a[92:95],v[28:31],v[196:199],a[92:95]
wt vmcnt(9) lgkmcnt(0)
s_barrier
mm a[92:95],v[24:27],v[192:195],a[92:95]
v_and_b32 v111, 0xffff0000, v83
v_sub_f32 v110, v110, v70
mm a[120:123],v[160:163],v[32:35],a[120:123]
v_sub_f32 v111, v111, v70
v_add_f32 v124, v110, v111
mm a[120:123],v[164:167],v[36:39],a[120:123]
v_fmac_f32 v122, v110, v110
v_add_f32 v120, v120, v124
mm a[136:139],v[164:167],v[44:47],a[136:139]
rt v[192:193],v240 offset:0
rt v[194:195],v240 offset:1024
mm a[136:139],v[160:163],v[40:43],a[136:139]
rt v[196:197],v240 offset:16896
rt v[198:199],v240 offset:17920
mm a[224:227],v[160:163],v[48:51],a[224:227]
v_fmac_f32 v123, v111, v111
v_lshlrev_b32 v112, 16, v84
mm a[224:227],v[164:167],v[52:55],a[224:227]
v_and_b32 v113, 0xffff0000, v84
v_sub_f32 v112, v112, v70
mm a[144:147],v[164:167],v[60:63],a[144:147]
v_sub_f32 v113, v113, v70
v_add_f32 v124, v112, v113
mm a[144:147],v[160:163],v[56:59],a[144:147]
rd v[160:163],v236 offset:24576
rd v[164:167],v237 offset:24576
mm a[148:151],v[168:171],v[56:59],a[148:151]
v_fmac_f32 v122, v112, v112
v_add_f32 v120, v120, v124
mm a[148:151],v[172:175],v[60:63],a[148:151]
v_fmac_f32 v123, v113, v113
v_lshlrev_b32 v114, 16, v85
mm a[228:231],v[172:175],v[52:55],a[228:231]
v_and_b32 v115, 0xffff0000, v85
v_sub_f32 v114, v114, v70
mm a[228:231],v[168:171],v[48:51],a[228:231]
rt v[200:201],v241 offset:0
rt v[202:203],v241 offset:1024
mm a[140:143],v[168:171],v[40:43],a[140:143]
v_sub_f32 v115, v115, v70
v_add_f32 v124, v114, v115
mm a[140:143],v[172:175],v[44:47],a[140:143]
v_fmac_f32 v122, v114, v114
v_add_f32 v120, v120, v124
mm a[124:127],v[172:175],v[36:39],a[124:127]
v_fmac_f32 v123, v115, v115
v_lshlrev_b32 v116, 16, v86
mm a[124:127],v[168:171],v[32:35],a[124:127]
rd v[168:171],v236 offset:26624
rd v[172:175],v237 offset:26624
mm a[216:219],v[176:179],v[32:35],a[216:219]
v_and_b32 v117, 0xffff0000, v86
v_sub_f32 v116, v116, v70
mm a[216:219],v[180:183],v[36:39],a[216:219]
v_sub_f32 v117, v117, v70
v_add_f32 v124, v116, v117
mm a[168:171],v[180:183],v[44:47],a[168:171]
v_fmac_f32 v122, v116, v116
v_add_f32 v120, v120, v124
mm a[168:171],v[176:179],v[40:43],a[168:171]
rt v[204:205],v241 offset:16896
rt v[206:207],v241 offset:17920
mm a[192:195],v[176:179],v[48:51],a[192:195]
v_fmac_f32 v123, v117, v117
v_lshlrev_b32 v118, 16, v87
mm a[192:195],v[180:183],v[52:55],a[192:195]
v_and_b32 v119, 0xffff0000, v87
v_sub_f32 v118, v118, v70
mm a[176:179],v[180:183],v[60:63],a[176:179]
v_sub_f32 v119, v119, v70
v_add_f32 v124, v118, v119
mm a[176:179],v[176:179],v[56:59],a[176:179]
rd v[176:179],v236 offset:16384
rd v[180:183],v237 offset:16384
mm a[180:183],v[184:187],v[56:59],a[180:183]
v_fmac_f32 v122, v118, v118
v_add_f32 v120, v120, v124
mm a[180:183],v[188:191],v[60:63],a[180:183]
v_fmac_f32 v123, v119, v119
v_add_f32 v121, v122, v123
mm a[196:199],v[188:191],v[52:55],a[196:199]
v_add_f32_dpp v120, v120, v120 row_ror:8 row_mask:0xf bank_mask:0xf
mm a[196:199],v[184:187],v[48:51],a[196:199]
rt v[208:209],v242 offset:0
rt v[210:211],v242 offset:1024
mm a[172:175],v[184:187],v[40:43],a[172:175]
s_add_u32 m0, s83, 33792
v_add_f32_dpp v121, v121, v121 row_ror:8 row_mask:0xf bank_mask:0xf
mm a[172:175],v[188:191],v[44:47],a[172:175]
ld v232,s[44:47],s80 offen lds
mm a[220:223],v[188:191],v[36:39],a[220:223]
v_mov_b32 v124, v120
v_mov_b32 v125, v121
mm a[220:223],v[184:187],v[32:35],a[220:223]
rd v[184:187],v236 offset:18432
rd v[188:191],v237 offset:18432
mm a[72:75],v[0:3],v[32:35],a[72:75]
s_add_u32 m0, s83, 34816
v_permlane16_swap_b32 v124, v120
mm a[72:75],v[4:7],v[36:39],a[72:75]
ld v233,s[44:47],s80 offen lds
mm a[8:11],v[4:7],v[44:47],a[8:11]
v_permlane16_swap_b32 v125, v121
mm a[8:11],v[0:3],v[40:43],a[8:11]
rt v[212:213],v242 offset:16896
rt v[214:215],v242 offset:17920
mm a[80:83],v[0:3],v[48:51],a[80:83]
s_add_u32 m0, s83, 35968
v_add_f32 v120, v120, v124
mm a[80:83],v[4:7],v[52:55],a[80:83]
ld v234,s[44:47],s80 offen lds
mm a[16:19],v[4:7],v[60:63],a[16:19]
v_add_f32 v121, v121, v125
v_mov_b32 v124, v120
mm a[16:19],v[0:3],v[56:59],a[16:19]
rd v[0:3],v236 offset:8192
rd v[4:7],v237 offset:8192
mm a[20:23],v[8:11],v[56:59],a[20:23]
s_add_u32 m0, s83, 36992
v_mov_b32 v125, v121
mm a[20:23],v[12:15],v[60:63],a[20:23]
ld v235,s[44:47],s80 offen lds
mm a[84:87],v[12:15],v[52:55],a[84:87]
s_mov_b32 s80, 3932160
v_permlane32_swap_b32 v124, v120
mm a[84:87],v[8:11],v[48:51],a[84:87]
rt v[216:217],v243 offset:0
rt v[218:219],v243 offset:1024
mm a[12:15],v[8:11],v[40:43],a[12:15]
s_add_u32 m0, s83, 50688
v_permlane32_swap_b32 v125, v121
mm a[12:15],v[12:15],v[44:47],a[12:15]
ld v232,s[44:47],s80 offen lds
mm a[76:79],v[12:15],v[36:39],a[76:79]
v_add_f32 v120, v120, v124
v_add_f32 v121, v121, v125
mm a[76:79],v[8:11],v[32:35],a[76:79]
rd v[8:11],v236 offset:10240
rd v[12:15],v237 offset:10240
mm a[248:251],v[16:19],v[32:35],a[248:251]
s_add_u32 m0, s83, 51712
ds_write_b64 v127, v[120:121]
mm a[248:251],v[20:23],v[36:39],a[248:251]
ld v233,s[44:47],s80 offen lds
mm a[40:43],v[20:23],v[44:47],a[40:43]
s_nop 0
v_readfirstlane_b32 s92, v252
mm a[40:43],v[16:19],v[40:43],a[40:43]
rt v[220:221],v243 offset:16896
rt v[222:223],v243 offset:17920
mm a[48:51],v[16:19],v[48:51],a[48:51]
s_add_u32 m0, s83, 52864
v_readfirstlane_b32 s93, v253
mm a[48:51],v[20:23],v[52:55],a[48:51]
ld v234,s[44:47],s80 offen lds
mm a[56:59],v[20:23],v[60:63],a[56:59]
s_add_u32 s92, s92, 1
s_addc_u32 s93, s93, 0
mm a[56:59],v[16:19],v[56:59],a[56:59]
rd v[16:19],v236 offset:0
rd v[20:23],v237 offset:0
mm a[60:63],v[24:27],v[56:59],a[60:63]
s_add_u32 m0, s83, 53888
v_mov_b32 v252, s92
mm a[60:63],v[28:31],v[60:63],a[60:63]
ld v235,s[44:47],s80 offen lds
mm a[52:55],v[28:31],v[52:55],a[52:55]
s_mov_b32 s80, 0
v_mov_b32 v253, s93
mm a[52:55],v[24:27],v[48:51],a[52:55]
v_mov_b32 v128, 0
v_mov_b32 v129, 0
mm a[44:47],v[24:27],v[40:43],a[44:47]
v_mov_b32 v130, 0
v_mov_b32 v131, 0
mm a[44:47],v[28:31],v[44:47],a[44:47]
st v[128:131],v255,s[52:55],s95 offen sc1
mm a[252:255],v[28:31],v[36:39],a[252:255]
wt lgkmcnt(2)
mm a[252:255],v[24:27],v[32:35],a[252:255]
rd v[24:27],v236 offset:2048
rd v[28:31],v237 offset:2048
mm a[96:99],v[160:163],v[192:195],a[96:99]
mm a[96:99],v[164:167],v[196:199],a[96:99]
mm a[104:107],v[164:167],v[204:207],a[104:107]
mm a[104:107],v[160:163],v[200:203],a[104:107]
rt v[32:33],v240 offset:128
rt v[34:35],v240 offset:1152
mm a[112:115],v[160:163],v[208:211],a[112:115]
mm a[112:115],v[164:167],v[212:215],a[112:115]
mm a[128:131],v[164:167],v[220:223],a[128:131]
wt lgkmcnt(2)
s_barrier
mm a[128:131],v[160:163],v[216:219],a[128:131]
rd v[72:75],v126
rd v[76:79],v126 offset:16
mm a[132:135],v[168:171],v[216:219],a[132:135]
s_xor_b32 m0, s82, 0
mm a[132:135],v[172:175],v[220:223],a[132:135]
ld v224,s[40:43],s79 offen lds
mm a[116:119],v[172:175],v[212:215],a[116:119]
mm a[116:119],v[168:171],v[208:211],a[116:119]
rt v[36:37],v240 offset:17024
rt v[38:39],v240 offset:18048
mm a[108:111],v[168:171],v[200:203],a[108:111]
s_xor_b32 m0, s82, 1024
mm a[108:111],v[172:175],v[204:207],a[108:111]
ld v225,s[40:43],s79 offen lds
mm a[100:103],v[172:175],v[196:199],a[100:103]
wt lgkmcnt(2)
v_add_f32 v72, v72, v74
mm a[100:103],v[168:171],v[192:195],a[100:103]
v_add_f32 v73, v73, v75
v_add_f32 v76, v76, v78
mm a[232:235],v[176:179],v[192:195],a[232:235]
s_xor_b32 m0, s82, 2048
v_add_f32 v77, v77, v79
mm a[232:235],v[180:183],v[196:199],a[232:235]
ld v226,s[40:43],s79 offen lds
mm a[152:155],v[180:183],v[204:207],a[152:155]
v_add_f32 v72, v72, v76
v_add_f32 v73, v73, v77
mm a[152:155],v[176:179],v[200:203],a[152:155]
rt v[40:41],v241 offset:128
rt v[42:43],v241 offset:1152
mm a[208:211],v[176:179],v[208:211],a[208:211]
s_xor_b32 m0, s82, 3072
v_mul_f32 v72, 0x3a800000, v72
mm a[208:211],v[180:183],v[212:215],a[208:211]
ld v227,s[40:43],s79 offen lds
mm a[160:163],v[180:183],v[220:223],a[160:163]
v_mul_f32 v73, 0x3a800000, v73
v_add_f32 v124, v70, v72
mm a[160:163],v[176:179],v[216:219],a[160:163]
v_fma_f32 v125, -v72, v72, v73
cv v124,v124,v124
mm a[164:167],v[184:187],v[216:219],a[164:167]
s_xor_b32 m0, s82, 4096
v_lshlrev_b32 v124, 16, v124
mm a[164:167],v[188:191],v[220:223],a[164:167]
ld v228,s[40:43],s79 offen lds
mm a[212:215],v[188:191],v[212:215],a[212:215]
v_max_f32 v125, 0, v125
cv v125,v125,v125
mm a[212:215],v[184:187],v[208:211],a[212:215]
rt v[44:45],v241 offset:17024
rt v[46:47],v241 offset:18048
mm a[156:159],v[184:187],v[200:203],a[156:159]
s_xor_b32 m0, s82, 5120
v_lshlrev_b32 v125, 16, v125
mm a[156:159],v[188:191],v[204:207],a[156:159]
ld v229,s[40:43],s79 offen lds
mm a[236:239],v[188:191],v[196:199],a[236:239]
v_add_f32 v125, 0x3727c5ac, v125
cv v125,v125,v125
mm a[236:239],v[184:187],v[192:195],a[236:239]
v_lshlrev_b32 v125, 16, v125
mm a[200:203],v[0:3],v[192:195],a[200:203]
s_xor_b32 m0, s82, 6144
mm a[200:203],v[4:7],v[196:199],a[200:203]
ld v230,s[40:43],s79 offen lds
mm a[184:187],v[4:7],v[204:207],a[184:187]
v_sqrt_f32 v125, v125
mm a[184:187],v[0:3],v[200:203],a[184:187]
rt v[48:49],v242 offset:128
rt v[50:51],v242 offset:1152
mm a[64:67],v[0:3],v[208:211],a[64:67]
s_xor_b32 m0, s82, 7168
v_sub_f32 v124, v70, v124
mm a[64:67],v[4:7],v[212:215],a[64:67]
ld v231,s[40:43],s79 offen lds
mm a[0:3],v[4:7],v[220:223],a[0:3]
s_mov_b32 s79, 128
cv v125,v125,v125
mm a[0:3],v[0:3],v[216:219],a[0:3]
v_lshlrev_b32 v125, 16, v125
mm a[4:7],v[8:11],v[216:219],a[4:7]
v_rcp_f32 v125, v125
mm a[4:7],v[12:15],v[220:223],a[4:7]
v_mul_f32 v125, 0x3e0293ee, v125
v_mul_f32 v124, v124, v125
mm a[68:71],v[12:15],v[212:215],a[68:71]
v_fma_f32 v88, v88, v125, v124
v_fma_f32 v89, v89, v125, v124
mm a[68:71],v[8:11],v[208:211],a[68:71]
rt v[52:53],v242 offset:17024
rt v[54:55],v242 offset:18048
mm a[188:191],v[8:11],v[200:203],a[188:191]
cv v72,v88,v89
v_fma_f32 v90, v90, v125, v124
mm a[188:191],v[12:15],v[204:207],a[188:191]
v_fma_f32 v91, v91, v125, v124
cv v73,v90,v91
mm a[204:207],v[12:15],v[196:199],a[204:207]
v_fma_f32 v92, v92, v125, v124
v_fma_f32 v93, v93, v125, v124
mm a[204:207],v[8:11],v[192:195],a[204:207]
rt v[56:57],v243 offset:128
rt v[58:59],v243 offset:1152
mm a[88:91],v[16:19],v[192:195],a[88:91]
cv v74,v92,v93
v_fma_f32 v94, v94, v125, v124
mm a[88:91],v[20:23],v[196:199],a[88:91]
v_fma_f32 v95, v95, v125, v124
cv v75,v94,v95
mm a[24:27],v[20:23],v[204:207],a[24:27]
st v[72:75],v71,s[56:59],0 offen offset:0 sc1
mm a[24:27],v[16:19],v[200:203],a[24:27]
rt v[60:61],v243 offset:17024
rt v[62:63],v243 offset:18048
mm a[240:243],v[16:19],v[208:211],a[240:243]
v_fma_f32 v96, v96, v125, v124
v_fma_f32 v97, v97, v125, v124
mm a[240:243],v[20:23],v[212:215],a[240:243]
cv v76,v96,v97
v_fma_f32 v98, v98, v125, v124
mm a[32:35],v[20:23],v[220:223],a[32:35]
v_fma_f32 v99, v99, v125, v124
cv v77,v98,v99
mm a[32:35],v[16:19],v[216:219],a[32:35]
v_fma_f32 v100, v100, v125, v124
v_fma_f32 v101, v101, v125, v124
mm a[36:39],v[24:27],v[216:219],a[36:39]
cv v78,v100,v101
v_fma_f32 v102, v102, v125, v124
mm a[36:39],v[28:31],v[220:223],a[36:39]
v_fma_f32 v103, v103, v125, v124
cv v79,v102,v103
mm a[244:247],v[28:31],v[212:215],a[244:247]
st v[76:79],v71,s[56:59],0 offen offset:2048 sc1
mm a[244:247],v[24:27],v[208:211],a[244:247]
v_fma_f32 v104, v104, v125, v124
v_fma_f32 v105, v105, v125, v124
mm a[28:31],v[24:27],v[200:203],a[28:31]
cv v80,v104,v105
v_fma_f32 v106, v106, v125, v124
mm a[28:31],v[28:31],v[204:207],a[28:31]
v_fma_f32 v107, v107, v125, v124
cv v81,v106,v107
mm a[92:95],v[28:31],v[196:199],a[92:95]
wt vmcnt(11) lgkmcnt(0)
s_barrier
mm a[92:95],v[24:27],v[192:195],a[92:95]
v_fma_f32 v108, v108, v125, v124
v_fma_f32 v109, v109, v125, v124
mm a[120:123],v[160:163],v[32:35],a[120:123]
cv v82,v108,v109
v_fma_f32 v110, v110, v125, v124
mm a[120:123],v[164:167],v[36:39],a[120:123]
v_fma_f32 v111, v111, v125, v124
cv v83,v110,v111
mm a[136:139],v[164:167],v[44:47],a[136:139]
rt v[192:193],v240 offset:33792
rt v[194:195],v240 offset:34816
mm a[136:139],v[160:163],v[40:43],a[136:139]
rt v[196:197],v240 offset:50688
rt v[198:199],v240 offset:51712
mm a[224:227],v[160:163],v[48:51],a[224:227]
st v[80:83],v71,s[56:59],s88 offen offset:0 sc1
mm a[224:227],v[164:167],v[52:55],a[224:227]
v_fma_f32 v112, v112, v125, v124
v_fma_f32 v113, v113, v125, v124
mm a[144:147],v[164:167],v[60:63],a[144:147]
cv v84,v112,v113
v_fma_f32 v114, v114, v125, v124
mm a[144:147],v[160:163],v[56:59],a[144:147]
rd v[160:163],v236 offset:57344
rd v[164:167],v237 offset:57344
mm a[148:151],v[168:171],v[56:59],a[148:151]
v_fma_f32 v115, v115, v125, v124
cv v85,v114,v115
mm a[148:151],v[172:175],v[60:63],a[148:151]
v_fma_f32 v116, v116, v125, v124
v_fma_f32 v117, v117, v125, v124
mm a[228:231],v[172:175],v[52:55],a[228:231]
cv v86,v116,v117
v_fma_f32 v118, v118, v125, v124
mm a[228:231],v[168:171],v[48:51],a[228:231]
rt v[200:201],v241 offset:33792
rt v[202:203],v241 offset:34816
mm a[140:143],v[168:171],v[40:43],a[140:143]
v_fma_f32 v119, v119, v125, v124
cv v87,v118,v119
mm a[140:143],v[172:175],v[44:47],a[140:143]
st v[84:87],v71,s[56:59],s88 offen offset:2048 sc1
mm a[124:127],v[172:175],v[36:39],a[124:127]
mm a[124:127],v[168:171],v[32:35],a[124:127]
rd v[168:171],v236 offset:59392
rd v[172:175],v237 offset:59392
mm a[216:219],v[176:179],v[32:35],a[216:219]
mm a[216:219],v[180:183],v[36:39],a[216:219]
mm a[168:171],v[180:183],v[44:47],a[168:171]
mm a[168:171],v[176:179],v[40:43],a[168:171]
rt v[204:205],v241 offset:50688
rt v[206:207],v241 offset:51712
mm a[192:195],v[176:179],v[48:51],a[192:195]
mm a[192:195],v[180:183],v[52:55],a[192:195]
mm a[176:179],v[180:183],v[60:63],a[176:179]
mm a[176:179],v[176:179],v[56:59],a[176:179]
rd v[176:179],v236 offset:49152
rd v[180:183],v237 offset:49152
mm a[180:183],v[184:187],v[56:59],a[180:183]
mm a[180:183],v[188:191],v[60:63],a[180:183]
mm a[196:199],v[188:191],v[52:55],a[196:199]
mm a[196:199],v[184:187],v[48:51],a[196:199]
rt v[208:209],v242 offset:33792
rt v[210:211],v242 offset:34816
mm a[172:175],v[184:187],v[40:43],a[172:175]
s_add_u32 m0, s83, 0
mm a[172:175],v[188:191],v[44:47],a[172:175]
ld v232,s[44:47],s80 offen lds
mm a[220:223],v[188:191],v[36:39],a[220:223]
mm a[220:223],v[184:187],v[32:35],a[220:223]
rd v[184:187],v236 offset:51200
rd v[188:191],v237 offset:51200
mm a[72:75],v[0:3],v[32:35],a[72:75]
s_add_u32 m0, s83, 1024
mm a[72:75],v[4:7],v[36:39],a[72:75]
ld v233,s[44:47],s80 offen lds
mm a[8:11],v[4:7],v[44:47],a[8:11]
mm a[8:11],v[0:3],v[40:43],a[8:11]
rt v[212:213],v242 offset:50688
rt v[214:215],v242 offset:51712
mm a[80:83],v[0:3],v[48:51],a[80:83]
s_add_u32 m0, s83, 2176
mm a[80:83],v[4:7],v[52:55],a[80:83]
ld v234,s[44:47],s80 offen lds
mm a[16:19],v[4:7],v[60:63],a[16:19]
mm a[16:19],v[0:3],v[56:59],a[16:19]
rd v[0:3],v236 offset:40960
rd v[4:7],v237 offset:40960
mm a[20:23],v[8:11],v[56:59],a[20:23]
s_add_u32 m0, s83, 3200
mm a[20:23],v[12:15],v[60:63],a[20:23]
ld v235,s[44:47],s80 offen lds
mm a[84:87],v[12:15],v[52:55],a[84:87]
s_mov_b32 s80, 262144
mm a[84:87],v[8:11],v[48:51],a[84:87]
rt v[216:217],v243 offset:33792
rt v[218:219],v243 offset:34816
mm a[12:15],v[8:11],v[40:43],a[12:15]
s_add_u32 m0, s83, 16896
mm a[12:15],v[12:15],v[44:47],a[12:15]
ld v232,s[44:47],s80 offen lds
mm a[76:79],v[12:15],v[36:39],a[76:79]
mm a[76:79],v[8:11],v[32:35],a[76:79]
rd v[8:11],v236 offset:43008
rd v[12:15],v237 offset:43008
mm a[248:251],v[16:19],v[32:35],a[248:251]
s_add_u32 m0, s83, 17920
mm a[248:251],v[20:23],v[36:39],a[248:251]
ld v233,s[44:47],s80 offen lds
mm a[40:43],v[20:23],v[44:47],a[40:43]
mm a[40:43],v[16:19],v[40:43],a[40:43]
rt v[220:221],v243 offset:50688
rt v[222:223],v243 offset:51712
mm a[48:51],v[16:19],v[48:51],a[48:51]
s_add_u32 m0, s83, 19072
mm a[48:51],v[20:23],v[52:55],a[48:51]
ld v234,s[44:47],s80 offen lds
mm a[56:59],v[20:23],v[60:63],a[56:59]
mm a[56:59],v[16:19],v[56:59],a[56:59]
rd v[16:19],v236 offset:32768
rd v[20:23],v237 offset:32768
.La5_LW1_6:
s_cbranch_execz .La5_LW1_10
mm a[60:63],v[24:27],v[56:59],a[60:63]
s_add_u32 m0, s83, 20096
mm a[60:63],v[28:31],v[60:63],a[60:63]
ld v235,s[44:47],s80 offen lds
mm a[52:55],v[28:31],v[52:55],a[52:55]
s_mov_b32 s80, 524288
mm a[52:55],v[24:27],v[48:51],a[52:55]
mm a[44:47],v[24:27],v[40:43],a[44:47]
mm a[44:47],v[28:31],v[44:47],a[44:47]
mm a[252:255],v[28:31],v[36:39],a[252:255]
wt lgkmcnt(2)
mm a[252:255],v[24:27],v[32:35],a[252:255]
rd v[24:27],v236 offset:34816
rd v[28:31],v237 offset:34816
mm a[96:99],v[160:163],v[192:195],a[96:99]
mm a[96:99],v[164:167],v[196:199],a[96:99]
mm a[104:107],v[164:167],v[204:207],a[104:107]
mm a[104:107],v[160:163],v[200:203],a[104:107]
rt v[32:33],v240 offset:33920
rt v[34:35],v240 offset:34944
mm a[112:115],v[160:163],v[208:211],a[112:115]
mm a[112:115],v[164:167],v[212:215],a[112:115]
mm a[128:131],v[164:167],v[220:223],a[128:131]
wt lgkmcnt(2)
s_barrier
mm a[128:131],v[160:163],v[216:219],a[128:131]
mm a[132:135],v[168:171],v[216:219],a[132:135]
s_xor_b32 m0, s82, 32768
mm a[132:135],v[172:175],v[220:223],a[132:135]
ld v224,s[40:43],s79 offen lds
mm a[116:119],v[172:175],v[212:215],a[116:119]
mm a[116:119],v[168:171],v[208:211],a[116:119]
rt v[36:37],v240 offset:50816
rt v[38:39],v240 offset:51840
mm a[108:111],v[168:171],v[200:203],a[108:111]
s_xor_b32 m0, s82, 33792
mm a[108:111],v[172:175],v[204:207],a[108:111]
ld v225,s[40:43],s79 offen lds
mm a[100:103],v[172:175],v[196:199],a[100:103]
mm a[100:103],v[168:171],v[192:195],a[100:103]
mm a[232:235],v[176:179],v[192:195],a[232:235]
s_xor_b32 m0, s82, 34816
mm a[232:235],v[180:183],v[196:199],a[232:235]
ld v226,s[40:43],s79 offen lds
mm a[152:155],v[180:183],v[204:207],a[152:155]
mm a[152:155],v[176:179],v[200:203],a[152:155]
rt v[40:41],v241 offset:33920
rt v[42:43],v241 offset:34944
mm a[208:211],v[176:179],v[208:211],a[208:211]
s_xor_b32 m0, s82, 35840
mm a[208:211],v[180:183],v[212:215],a[208:211]
ld v227,s[40:43],s79 offen lds
mm a[160:163],v[180:183],v[220:223],a[160:163]
mm a[160:163],v[176:179],v[216:219],a[160:163]
mm a[164:167],v[184:187],v[216:219],a[164:167]
s_xor_b32 m0, s82, 36864
mm a[164:167],v[188:191],v[220:223],a[164:167]
ld v228,s[40:43],s79 offen lds
mm a[212:215],v[188:191],v[212:215],a[212:215]
mm a[212:215],v[184:187],v[208:211],a[212:215]
rt v[44:45],v241 offset:50816
rt v[46:47],v241 offset:51840
mm a[156:159],v[184:187],v[200:203],a[156:159]
s_xor_b32 m0, s82, 37888
mm a[156:159],v[188:191],v[204:207],a[156:159]
ld v229,s[40:43],s79 offen lds
mm a[236:239],v[188:191],v[196:199],a[236:239]
mm a[236:239],v[184:187],v[192:195],a[236:239]
mm a[200:203],v[0:3],v[192:195],a[200:203]
s_xor_b32 m0, s82, 38912
mm a[200:203],v[4:7],v[196:199],a[200:203]
ld v230,s[40:43],s79 offen lds
mm a[184:187],v[4:7],v[204:207],a[184:187]
mm a[184:187],v[0:3],v[200:203],a[184:187]
rt v[48:49],v242 offset:33920
rt v[50:51],v242 offset:34944
mm a[64:67],v[0:3],v[208:211],a[64:67]
s_xor_b32 m0, s82, 39936
mm a[64:67],v[4:7],v[212:215],a[64:67]
ld v231,s[40:43],s79 offen lds
mm a[0:3],v[4:7],v[220:223],a[0:3]
s_mov_b32 s79, 256
mm a[0:3],v[0:3],v[216:219],a[0:3]
mm a[4:7],v[8:11],v[216:219],a[4:7]
mm a[4:7],v[12:15],v[220:223],a[4:7]
buffer_load_dword v254, v135, s[68:71], s96 offen
mm a[68:71],v[12:15],v[212:215],a[68:71]
s_add_u32 s96, s96, 1048576
mm a[68:71],v[8:11],v[208:211],a[68:71]
rt v[52:53],v242 offset:50816
rt v[54:55],v242 offset:51840
mm a[188:191],v[8:11],v[200:203],a[188:191]
mm a[188:191],v[12:15],v[204:207],a[188:191]
buffer_load_dword v254, v135, s[68:71], s96 offen
mm a[204:207],v[12:15],v[196:199],a[204:207]
s_add_u32 s96, s96, 1048576
mm a[204:207],v[8:11],v[192:195],a[204:207]
rt v[56:57],v243 offset:33920
rt v[58:59],v243 offset:34944
mm a[88:91],v[16:19],v[192:195],a[88:91]
mm a[88:91],v[20:23],v[196:199],a[88:91]
wt vmcnt(18)
buffer_store_dwordx2 v[252:253], off, s[64:67], s91 offset:1024 sc1
mm a[24:27],v[20:23],v[204:207],a[24:27]
s_mov_b32 s91, 0
s_lshl_b32 s92, s76, 19
mm a[24:27],v[16:19],v[200:203],a[24:27]
rt v[60:61],v243 offset:50816
rt v[62:63],v243 offset:51840
mm a[240:243],v[16:19],v[208:211],a[240:243]
s_add_u32 s92, s92, 88080384
mm a[240:243],v[20:23],v[212:215],a[240:243]
s_add_u32 s52, s26, s92
s_addc_u32 s53, s27, 0
mm a[32:35],v[20:23],v[220:223],a[32:35]
s_and_b32 s53, s53, 0xffff
s_mov_b32 s54, 524672
mm a[32:35],v[16:19],v[216:219],a[32:35]
s_mov_b32 s55, 0x20000
s_lshl_b32 s92, s3, 16
mm a[36:39],v[24:27],v[216:219],a[36:39]
s_sub_u32 s93, 458752, s92
v_add_u32 v244, s92, v136
mm a[36:39],v[28:31],v[220:223],a[36:39]
v_add_u32 v246, s93, v136
v_add_u32 v245, 0x8000, v244
mm a[244:247],v[28:31],v[212:215],a[244:247]
v_add_u32 v247, 0x8000, v246
s_lshl_b32 s94, s72, 19
mm a[244:247],v[24:27],v[208:211],a[244:247]
s_lshl_b32 s93, s76, 16
s_add_u32 s92, s94, s93
mm a[28:31],v[24:27],v[200:203],a[28:31]
s_lshl_b32 s93, s77, 13
s_add_u32 s92, s92, s93
mm a[28:31],v[28:31],v[204:207],a[28:31]
s_add_u32 s92, s92, 37748736
mm a[92:95],v[28:31],v[196:199],a[92:95]
wt vmcnt(11) lgkmcnt(0)
s_barrier
mm a[92:95],v[24:27],v[192:195],a[92:95]
s_add_u32 s48, s26, s92
s_addc_u32 s49, s27, 0
mm a[120:123],v[160:163],v[32:35],a[120:123]
s_and_b32 s49, s49, 0xffff
s_mov_b32 s50, 57344
mm a[120:123],v[164:167],v[36:39],a[120:123]
s_mov_b32 s51, 0x20000
s_lshl_b32 s93, s77, 24
mm a[136:139],v[164:167],v[44:47],a[136:139]
rt v[192:193],v240 offset:0
rt v[194:195],v240 offset:1024
mm a[136:139],v[160:163],v[40:43],a[136:139]
rt v[196:197],v240 offset:16896
rt v[198:199],v240 offset:17920
mm a[224:227],v[160:163],v[48:51],a[224:227]
s_add_u32 s92, s94, s93
s_add_u32 s92, s92, 20975616
mm a[224:227],v[164:167],v[52:55],a[224:227]
s_add_u32 s60, s26, s92
s_addc_u32 s61, s27, 0
mm a[144:147],v[164:167],v[60:63],a[144:147]
s_and_b32 s61, s61, 0xffff
s_mov_b32 s62, 520192
mm a[144:147],v[160:163],v[56:59],a[144:147]
rd v[160:163],v236 offset:24576
rd v[164:167],v237 offset:24576
mm a[148:151],v[168:171],v[56:59],a[148:151]
s_mov_b32 s63, 0x20000
s_lshl_b32 s92, s77, 14
mm a[148:151],v[172:175],v[60:63],a[148:151]
s_sub_u32 s95, 24576, s92
v_add_u32 v238, s95, v255
mm a[228:231],v[172:175],v[52:55],a[228:231]
mm a[228:231],v[168:171],v[48:51],a[228:231]
rt v[200:201],v241 offset:0
rt v[202:203],v241 offset:1024
mm a[140:143],v[168:171],v[40:43],a[140:143]
mm a[140:143],v[172:175],v[44:47],a[140:143]
mm a[124:127],v[172:175],v[36:39],a[124:127]
mm a[124:127],v[168:171],v[32:35],a[124:127]
rd v[168:171],v236 offset:26624
rd v[172:175],v237 offset:26624
mm a[216:219],v[176:179],v[32:35],a[216:219]
mm a[216:219],v[180:183],v[36:39],a[216:219]
mm a[168:171],v[180:183],v[44:47],a[168:171]
mm a[168:171],v[176:179],v[40:43],a[168:171]
rt v[204:205],v241 offset:16896
rt v[206:207],v241 offset:17920
mm a[192:195],v[176:179],v[48:51],a[192:195]
mm a[192:195],v[180:183],v[52:55],a[192:195]
mm a[176:179],v[180:183],v[60:63],a[176:179]
mm a[176:179],v[176:179],v[56:59],a[176:179]
rd v[176:179],v236 offset:16384
rd v[180:183],v237 offset:16384
mm a[180:183],v[184:187],v[56:59],a[180:183]
mm a[180:183],v[188:191],v[60:63],a[180:183]
mm a[196:199],v[188:191],v[52:55],a[196:199]
mm a[196:199],v[184:187],v[48:51],a[196:199]
rt v[208:209],v242 offset:0
rt v[210:211],v242 offset:1024
mm a[172:175],v[184:187],v[40:43],a[172:175]
s_add_u32 m0, s83, 33792
mm a[172:175],v[188:191],v[44:47],a[172:175]
ld v232,s[44:47],s80 offen lds
mm a[220:223],v[188:191],v[36:39],a[220:223]
mm a[220:223],v[184:187],v[32:35],a[220:223]
rd v[184:187],v236 offset:18432
rd v[188:191],v237 offset:18432
mm a[72:75],v[0:3],v[32:35],a[72:75]
s_add_u32 m0, s83, 34816
mm a[72:75],v[4:7],v[36:39],a[72:75]
ld v233,s[44:47],s80 offen lds
mm a[8:11],v[4:7],v[44:47],a[8:11]
mm a[8:11],v[0:3],v[40:43],a[8:11]
rt v[212:213],v242 offset:16896
rt v[214:215],v242 offset:17920
mm a[80:83],v[0:3],v[48:51],a[80:83]
s_add_u32 m0, s83, 35968
mm a[80:83],v[4:7],v[52:55],a[80:83]
ld v234,s[44:47],s80 offen lds
mm a[16:19],v[4:7],v[60:63],a[16:19]
mm a[16:19],v[0:3],v[56:59],a[16:19]
rd v[0:3],v236 offset:8192
rd v[4:7],v237 offset:8192
mm a[20:23],v[8:11],v[56:59],a[20:23]
s_add_u32 m0, s83, 36992
mm a[20:23],v[12:15],v[60:63],a[20:23]
ld v235,s[44:47],s80 offen lds
mm a[84:87],v[12:15],v[52:55],a[84:87]
s_mov_b32 s80, 786432
mm a[84:87],v[8:11],v[48:51],a[84:87]
rt v[216:217],v243 offset:0
rt v[218:219],v243 offset:1024
mm a[12:15],v[8:11],v[40:43],a[12:15]
s_add_u32 m0, s83, 50688
mm a[12:15],v[12:15],v[44:47],a[12:15]
ld v232,s[44:47],s80 offen lds
mm a[76:79],v[12:15],v[36:39],a[76:79]
mm a[76:79],v[8:11],v[32:35],a[76:79]
rd v[8:11],v236 offset:10240
rd v[12:15],v237 offset:10240
mm a[248:251],v[16:19],v[32:35],a[248:251]
s_add_u32 m0, s83, 51712
mm a[248:251],v[20:23],v[36:39],a[248:251]
ld v233,s[44:47],s80 offen lds
mm a[40:43],v[20:23],v[44:47],a[40:43]
mm a[40:43],v[16:19],v[40:43],a[40:43]
rt v[220:221],v243 offset:16896
rt v[222:223],v243 offset:17920
mm a[48:51],v[16:19],v[48:51],a[48:51]
s_add_u32 m0, s83, 52864
mm a[48:51],v[20:23],v[52:55],a[48:51]
ld v234,s[44:47],s80 offen lds
mm a[56:59],v[20:23],v[60:63],a[56:59]
mm a[56:59],v[16:19],v[56:59],a[56:59]
rd v[16:19],v236 offset:0
rd v[20:23],v237 offset:0
mm a[60:63],v[24:27],v[56:59],a[60:63]
s_add_u32 m0, s83, 53888
mm a[60:63],v[28:31],v[60:63],a[60:63]
ld v235,s[44:47],s80 offen lds
mm a[52:55],v[28:31],v[52:55],a[52:55]
s_mov_b32 s80, 1048576
mm a[52:55],v[24:27],v[48:51],a[52:55]
mm a[44:47],v[24:27],v[40:43],a[44:47]
mm a[44:47],v[28:31],v[44:47],a[44:47]
mm a[252:255],v[28:31],v[36:39],a[252:255]
wt lgkmcnt(2)
mm a[252:255],v[24:27],v[32:35],a[252:255]
rd v[24:27],v236 offset:2048
rd v[28:31],v237 offset:2048
mm a[96:99],v[160:163],v[192:195],a[96:99]
mm a[96:99],v[164:167],v[196:199],a[96:99]
mm a[104:107],v[164:167],v[204:207],a[104:107]
mm a[104:107],v[160:163],v[200:203],a[104:107]
rt v[32:33],v240 offset:128
rt v[34:35],v240 offset:1152
mm a[112:115],v[160:163],v[208:211],a[112:115]
mm a[112:115],v[164:167],v[212:215],a[112:115]
mm a[128:131],v[164:167],v[220:223],a[128:131]
wt lgkmcnt(2)
s_barrier
mm a[128:131],v[160:163],v[216:219],a[128:131]
mm a[132:135],v[168:171],v[216:219],a[132:135]
s_xor_b32 m0, s82, 0
mm a[132:135],v[172:175],v[220:223],a[132:135]
ld v224,s[40:43],s79 offen lds
mm a[116:119],v[172:175],v[212:215],a[116:119]
mm a[116:119],v[168:171],v[208:211],a[116:119]
rt v[36:37],v240 offset:17024
rt v[38:39],v240 offset:18048
mm a[108:111],v[168:171],v[200:203],a[108:111]
s_xor_b32 m0, s82, 1024
mm a[108:111],v[172:175],v[204:207],a[108:111]
ld v225,s[40:43],s79 offen lds
mm a[100:103],v[172:175],v[196:199],a[100:103]
mm a[100:103],v[168:171],v[192:195],a[100:103]
mm a[232:235],v[176:179],v[192:195],a[232:235]
s_xor_b32 m0, s82, 2048
mm a[232:235],v[180:183],v[196:199],a[232:235]
ld v226,s[40:43],s79 offen lds
mm a[152:155],v[180:183],v[204:207],a[152:155]
mm a[152:155],v[176:179],v[200:203],a[152:155]
rt v[40:41],v241 offset:128
rt v[42:43],v241 offset:1152
mm a[208:211],v[176:179],v[208:211],a[208:211]
s_xor_b32 m0, s82, 3072
mm a[208:211],v[180:183],v[212:215],a[208:211]
ld v227,s[40:43],s79 offen lds
mm a[160:163],v[180:183],v[220:223],a[160:163]
mm a[160:163],v[176:179],v[216:219],a[160:163]
mm a[164:167],v[184:187],v[216:219],a[164:167]
s_xor_b32 m0, s82, 4096
mm a[164:167],v[188:191],v[220:223],a[164:167]
ld v228,s[40:43],s79 offen lds
mm a[212:215],v[188:191],v[212:215],a[212:215]
mm a[212:215],v[184:187],v[208:211],a[212:215]
rt v[44:45],v241 offset:17024
rt v[46:47],v241 offset:18048
mm a[156:159],v[184:187],v[200:203],a[156:159]
s_xor_b32 m0, s82, 5120
mm a[156:159],v[188:191],v[204:207],a[156:159]
ld v229,s[40:43],s79 offen lds
mm a[236:239],v[188:191],v[196:199],a[236:239]
mm a[236:239],v[184:187],v[192:195],a[236:239]
mm a[200:203],v[0:3],v[192:195],a[200:203]
s_xor_b32 m0, s82, 6144
mm a[200:203],v[4:7],v[196:199],a[200:203]
ld v230,s[40:43],s79 offen lds
mm a[184:187],v[4:7],v[204:207],a[184:187]
mm a[184:187],v[0:3],v[200:203],a[184:187]
rt v[48:49],v242 offset:128
rt v[50:51],v242 offset:1152
mm a[64:67],v[0:3],v[208:211],a[64:67]
s_xor_b32 m0, s82, 7168
mm a[64:67],v[4:7],v[212:215],a[64:67]
ld v231,s[40:43],s79 offen lds
mm a[0:3],v[4:7],v[220:223],a[0:3]
s_mov_b32 s79, 384
mm a[0:3],v[0:3],v[216:219],a[0:3]
mm a[4:7],v[8:11],v[216:219],a[4:7]
mm a[4:7],v[12:15],v[220:223],a[4:7]
buffer_load_dword v254, v135, s[68:71], s96 offen
mm a[68:71],v[12:15],v[212:215],a[68:71]
s_add_u32 s96, s96, 1048576
mm a[68:71],v[8:11],v[208:211],a[68:71]
rt v[52:53],v242 offset:17024
rt v[54:55],v242 offset:18048
mm a[188:191],v[8:11],v[200:203],a[188:191]
mm a[188:191],v[12:15],v[204:207],a[188:191]
buffer_load_dword v254, v135, s[68:71], s96 offen
mm a[204:207],v[12:15],v[196:199],a[204:207]
s_add_u32 s96, s96, 1048576
mm a[204:207],v[8:11],v[192:195],a[204:207]
rt v[56:57],v243 offset:128
rt v[58:59],v243 offset:1152
mm a[88:91],v[16:19],v[192:195],a[88:91]
mm a[88:91],v[20:23],v[196:199],a[88:91]
mm a[24:27],v[20:23],v[204:207],a[24:27]
mm a[24:27],v[16:19],v[200:203],a[24:27]
rt v[60:61],v243 offset:17024
rt v[62:63],v243 offset:18048
mm a[240:243],v[16:19],v[208:211],a[240:243]
mm a[240:243],v[20:23],v[212:215],a[240:243]
mm a[32:35],v[20:23],v[220:223],a[32:35]
mm a[32:35],v[16:19],v[216:219],a[32:35]
mm a[36:39],v[24:27],v[216:219],a[36:39]
mm a[36:39],v[28:31],v[220:223],a[36:39]
mm a[244:247],v[28:31],v[212:215],a[244:247]
mm a[244:247],v[24:27],v[208:211],a[244:247]
mm a[28:31],v[24:27],v[200:203],a[28:31]
mm a[28:31],v[28:31],v[204:207],a[28:31]
mm a[92:95],v[28:31],v[196:199],a[92:95]
wt vmcnt(10) lgkmcnt(0)
s_barrier
mm a[92:95],v[24:27],v[192:195],a[92:95]
mm a[120:123],v[160:163],v[32:35],a[120:123]
mm a[120:123],v[164:167],v[36:39],a[120:123]
mm a[136:139],v[164:167],v[44:47],a[136:139]
rt v[192:193],v240 offset:33792
rt v[194:195],v240 offset:34816
mm a[136:139],v[160:163],v[40:43],a[136:139]
rt v[196:197],v240 offset:50688
rt v[198:199],v240 offset:51712
mm a[224:227],v[160:163],v[48:51],a[224:227]
mm a[224:227],v[164:167],v[52:55],a[224:227]
mm a[144:147],v[164:167],v[60:63],a[144:147]
mm a[144:147],v[160:163],v[56:59],a[144:147]
rd v[160:163],v236 offset:57344
rd v[164:167],v237 offset:57344
mm a[148:151],v[168:171],v[56:59],a[148:151]
mm a[148:151],v[172:175],v[60:63],a[148:151]
mm a[228:231],v[172:175],v[52:55],a[228:231]
mm a[228:231],v[168:171],v[48:51],a[228:231]
rt v[200:201],v241 offset:33792
rt v[202:203],v241 offset:34816
mm a[140:143],v[168:171],v[40:43],a[140:143]
mm a[140:143],v[172:175],v[44:47],a[140:143]
mm a[124:127],v[172:175],v[36:39],a[124:127]
mm a[124:127],v[168:171],v[32:35],a[124:127]
rd v[168:171],v236 offset:59392
rd v[172:175],v237 offset:59392
mm a[216:219],v[176:179],v[32:35],a[216:219]
mm a[216:219],v[180:183],v[36:39],a[216:219]
mm a[168:171],v[180:183],v[44:47],a[168:171]
mm a[168:171],v[176:179],v[40:43],a[168:171]
rt v[204:205],v241 offset:50688
rt v[206:207],v241 offset:51712
mm a[192:195],v[176:179],v[48:51],a[192:195]
mm a[192:195],v[180:183],v[52:55],a[192:195]
mm a[176:179],v[180:183],v[60:63],a[176:179]
mm a[176:179],v[176:179],v[56:59],a[176:179]
rd v[176:179],v236 offset:49152
rd v[180:183],v237 offset:49152
mm a[180:183],v[184:187],v[56:59],a[180:183]
mm a[180:183],v[188:191],v[60:63],a[180:183]
mm a[196:199],v[188:191],v[52:55],a[196:199]
mm a[196:199],v[184:187],v[48:51],a[196:199]
rt v[208:209],v242 offset:33792
rt v[210:211],v242 offset:34816
mm a[172:175],v[184:187],v[40:43],a[172:175]
s_add_u32 m0, s83, 0
mm a[172:175],v[188:191],v[44:47],a[172:175]
ld v232,s[44:47],s80 offen lds
mm a[220:223],v[188:191],v[36:39],a[220:223]
mm a[220:223],v[184:187],v[32:35],a[220:223]
rd v[184:187],v236 offset:51200
rd v[188:191],v237 offset:51200
mm a[72:75],v[0:3],v[32:35],a[72:75]
s_add_u32 m0, s83, 1024
mm a[72:75],v[4:7],v[36:39],a[72:75]
ld v233,s[44:47],s80 offen lds
mm a[8:11],v[4:7],v[44:47],a[8:11]
mm a[8:11],v[0:3],v[40:43],a[8:11]
rt v[212:213],v242 offset:50688
rt v[214:215],v242 offset:51712
mm a[80:83],v[0:3],v[48:51],a[80:83]
s_add_u32 m0, s83, 2176
mm a[80:83],v[4:7],v[52:55],a[80:83]
ld v234,s[44:47],s80 offen lds
mm a[16:19],v[4:7],v[60:63],a[16:19]
mm a[16:19],v[0:3],v[56:59],a[16:19]
rd v[0:3],v236 offset:40960
rd v[4:7],v237 offset:40960
mm a[20:23],v[8:11],v[56:59],a[20:23]
s_add_u32 m0, s83, 3200
mm a[20:23],v[12:15],v[60:63],a[20:23]
ld v235,s[44:47],s80 offen lds
mm a[84:87],v[12:15],v[52:55],a[84:87]
s_mov_b32 s80, 1310720
mm a[84:87],v[8:11],v[48:51],a[84:87]
rt v[216:217],v243 offset:33792
rt v[218:219],v243 offset:34816
mm a[12:15],v[8:11],v[40:43],a[12:15]
s_add_u32 m0, s83, 16896
mm a[12:15],v[12:15],v[44:47],a[12:15]
ld v232,s[44:47],s80 offen lds
mm a[76:79],v[12:15],v[36:39],a[76:79]
mm a[76:79],v[8:11],v[32:35],a[76:79]
rd v[8:11],v236 offset:43008
rd v[12:15],v237 offset:43008
mm a[248:251],v[16:19],v[32:35],a[248:251]
s_add_u32 m0, s83, 17920
mm a[248:251],v[20:23],v[36:39],a[248:251]
ld v233,s[44:47],s80 offen lds
mm a[40:43],v[20:23],v[44:47],a[40:43]
mm a[40:43],v[16:19],v[40:43],a[40:43]
rt v[220:221],v243 offset:50688
rt v[222:223],v243 offset:51712
mm a[48:51],v[16:19],v[48:51],a[48:51]
s_add_u32 m0, s83, 19072
mm a[48:51],v[20:23],v[52:55],a[48:51]
ld v234,s[44:47],s80 offen lds
mm a[56:59],v[20:23],v[60:63],a[56:59]
mm a[56:59],v[16:19],v[56:59],a[56:59]
rd v[16:19],v236 offset:32768
rd v[20:23],v237 offset:32768
mm a[60:63],v[24:27],v[56:59],a[60:63]
s_add_u32 m0, s83, 20096
mm a[60:63],v[28:31],v[60:63],a[60:63]
ld v235,s[44:47],s80 offen lds
mm a[52:55],v[28:31],v[52:55],a[52:55]
s_mov_b32 s80, 1572864
mm a[52:55],v[24:27],v[48:51],a[52:55]
mm a[44:47],v[24:27],v[40:43],a[44:47]
mm a[44:47],v[28:31],v[44:47],a[44:47]
mm a[252:255],v[28:31],v[36:39],a[252:255]
wt lgkmcnt(2)
mm a[252:255],v[24:27],v[32:35],a[252:255]
rd v[24:27],v236 offset:34816
rd v[28:31],v237 offset:34816
mm a[96:99],v[160:163],v[192:195],a[96:99]
mm a[96:99],v[164:167],v[196:199],a[96:99]
mm a[104:107],v[164:167],v[204:207],a[104:107]
mm a[104:107],v[160:163],v[200:203],a[104:107]
rt v[32:33],v240 offset:33920
rt v[34:35],v240 offset:34944
mm a[112:115],v[160:163],v[208:211],a[112:115]
mm a[112:115],v[164:167],v[212:215],a[112:115]
mm a[128:131],v[164:167],v[220:223],a[128:131]
wt lgkmcnt(2)
s_barrier
mm a[128:131],v[160:163],v[216:219],a[128:131]
mm a[132:135],v[168:171],v[216:219],a[132:135]
s_xor_b32 m0, s82, 32768
mm a[132:135],v[172:175],v[220:223],a[132:135]
ld v224,s[40:43],s79 offen lds
mm a[116:119],v[172:175],v[212:215],a[116:119]
mm a[116:119],v[168:171],v[208:211],a[116:119]
rt v[36:37],v240 offset:50816
rt v[38:39],v240 offset:51840
mm a[108:111],v[168:171],v[200:203],a[108:111]
s_xor_b32 m0, s82, 33792
mm a[108:111],v[172:175],v[204:207],a[108:111]
ld v225,s[40:43],s79 offen lds
mm a[100:103],v[172:175],v[196:199],a[100:103]
mm a[100:103],v[168:171],v[192:195],a[100:103]
mm a[232:235],v[176:179],v[192:195],a[232:235]
s_xor_b32 m0, s82, 34816
mm a[232:235],v[180:183],v[196:199],a[232:235]
ld v226,s[40:43],s79 offen lds
mm a[152:155],v[180:183],v[204:207],a[152:155]
mm a[152:155],v[176:179],v[200:203],a[152:155]
rt v[40:41],v241 offset:33920
rt v[42:43],v241 offset:34944
mm a[208:211],v[176:179],v[208:211],a[208:211]
s_xor_b32 m0, s82, 35840
mm a[208:211],v[180:183],v[212:215],a[208:211]
.La5_LW1_7:
s_cbranch_execz .La5_LW1_11
ld v227,s[40:43],s79 offen lds
mm a[160:163],v[180:183],v[220:223],a[160:163]
mm a[160:163],v[176:179],v[216:219],a[160:163]
mm a[164:167],v[184:187],v[216:219],a[164:167]
s_xor_b32 m0, s82, 36864
mm a[164:167],v[188:191],v[220:223],a[164:167]
ld v228,s[40:43],s79 offen lds
mm a[212:215],v[188:191],v[212:215],a[212:215]
mm a[212:215],v[184:187],v[208:211],a[212:215]
rt v[44:45],v241 offset:50816
rt v[46:47],v241 offset:51840
mm a[156:159],v[184:187],v[200:203],a[156:159]
s_xor_b32 m0, s82, 37888
mm a[156:159],v[188:191],v[204:207],a[156:159]
ld v229,s[40:43],s79 offen lds
mm a[236:239],v[188:191],v[196:199],a[236:239]
mm a[236:239],v[184:187],v[192:195],a[236:239]
mm a[200:203],v[0:3],v[192:195],a[200:203]
s_xor_b32 m0, s82, 38912
mm a[200:203],v[4:7],v[196:199],a[200:203]
ld v230,s[40:43],s79 offen lds
mm a[184:187],v[4:7],v[204:207],a[184:187]
mm a[184:187],v[0:3],v[200:203],a[184:187]
rt v[48:49],v242 offset:33920
rt v[50:51],v242 offset:34944
mm a[64:67],v[0:3],v[208:211],a[64:67]
s_xor_b32 m0, s82, 39936
mm a[64:67],v[4:7],v[212:215],a[64:67]
ld v231,s[40:43],s79 offen lds
mm a[0:3],v[4:7],v[220:223],a[0:3]
s_mov_b32 s79, 512
mm a[0:3],v[0:3],v[216:219],a[0:3]
mm a[4:7],v[8:11],v[216:219],a[4:7]
mm a[4:7],v[12:15],v[220:223],a[4:7]
buffer_load_dword v254, v135, s[68:71], s96 offen
mm a[68:71],v[12:15],v[212:215],a[68:71]
s_add_u32 s96, s96, 1048576
mm a[68:71],v[8:11],v[208:211],a[68:71]
rt v[52:53],v242 offset:50816
rt v[54:55],v242 offset:51840
mm a[188:191],v[8:11],v[200:203],a[188:191]
mm a[188:191],v[12:15],v[204:207],a[188:191]
buffer_load_dword v254, v135, s[68:71], s96 offen
mm a[204:207],v[12:15],v[196:199],a[204:207]
s_add_u32 s96, s96, 1048576
mm a[204:207],v[8:11],v[192:195],a[204:207]
rt v[56:57],v243 offset:33920
rt v[58:59],v243 offset:34944
mm a[88:91],v[16:19],v[192:195],a[88:91]
mm a[88:91],v[20:23],v[196:199],a[88:91]
mm a[24:27],v[20:23],v[204:207],a[24:27]
mm a[24:27],v[16:19],v[200:203],a[24:27]
rt v[60:61],v243 offset:50816
rt v[62:63],v243 offset:51840
mm a[240:243],v[16:19],v[208:211],a[240:243]
mm a[240:243],v[20:23],v[212:215],a[240:243]
mm a[32:35],v[20:23],v[220:223],a[32:35]
mm a[32:35],v[16:19],v[216:219],a[32:35]
mm a[36:39],v[24:27],v[216:219],a[36:39]
mm a[36:39],v[28:31],v[220:223],a[36:39]
mm a[244:247],v[28:31],v[212:215],a[244:247]
mm a[244:247],v[24:27],v[208:211],a[244:247]
mm a[28:31],v[24:27],v[200:203],a[28:31]
mm a[28:31],v[28:31],v[204:207],a[28:31]
mm a[92:95],v[28:31],v[196:199],a[92:95]
wt vmcnt(10) lgkmcnt(0)
s_barrier
mm a[92:95],v[24:27],v[192:195],a[92:95]
mm a[120:123],v[160:163],v[32:35],a[120:123]
mm a[120:123],v[164:167],v[36:39],a[120:123]
mm a[136:139],v[164:167],v[44:47],a[136:139]
rt v[192:193],v240 offset:0
rt v[194:195],v240 offset:1024
mm a[136:139],v[160:163],v[40:43],a[136:139]
rt v[196:197],v240 offset:16896
rt v[198:199],v240 offset:17920
mm a[224:227],v[160:163],v[48:51],a[224:227]
mm a[224:227],v[164:167],v[52:55],a[224:227]
mm a[144:147],v[164:167],v[60:63],a[144:147]
mm a[144:147],v[160:163],v[56:59],a[144:147]
rd v[160:163],v236 offset:24576
rd v[164:167],v237 offset:24576
mm a[148:151],v[168:171],v[56:59],a[148:151]
mm a[148:151],v[172:175],v[60:63],a[148:151]
mm a[228:231],v[172:175],v[52:55],a[228:231]
mm a[228:231],v[168:171],v[48:51],a[228:231]
rt v[200:201],v241 offset:0
rt v[202:203],v241 offset:1024
mm a[140:143],v[168:171],v[40:43],a[140:143]
mm a[140:143],v[172:175],v[44:47],a[140:143]
mm a[124:127],v[172:175],v[36:39],a[124:127]
mm a[124:127],v[168:171],v[32:35],a[124:127]
rd v[168:171],v236 offset:26624
rd v[172:175],v237 offset:26624
mm a[216:219],v[176:179],v[32:35],a[216:219]
mm a[216:219],v[180:183],v[36:39],a[216:219]
mm a[168:171],v[180:183],v[44:47],a[168:171]
mm a[168:171],v[176:179],v[40:43],a[168:171]
rt v[204:205],v241 offset:16896
rt v[206:207],v241 offset:17920
mm a[192:195],v[176:179],v[48:51],a[192:195]
mm a[192:195],v[180:183],v[52:55],a[192:195]
mm a[176:179],v[180:183],v[60:63],a[176:179]
mm a[176:179],v[176:179],v[56:59],a[176:179]
rd v[176:179],v236 offset:16384
rd v[180:183],v237 offset:16384
mm a[180:183],v[184:187],v[56:59],a[180:183]
mm a[180:183],v[188:191],v[60:63],a[180:183]
mm a[196:199],v[188:191],v[52:55],a[196:199]
mm a[196:199],v[184:187],v[48:51],a[196:199]
rt v[208:209],v242 offset:0
rt v[210:211],v242 offset:1024
mm a[172:175],v[184:187],v[40:43],a[172:175]
s_add_u32 m0, s83, 33792
mm a[172:175],v[188:191],v[44:47],a[172:175]
ld v232,s[44:47],s80 offen lds
mm a[220:223],v[188:191],v[36:39],a[220:223]
mm a[220:223],v[184:187],v[32:35],a[220:223]
rd v[184:187],v236 offset:18432
rd v[188:191],v237 offset:18432
mm a[72:75],v[0:3],v[32:35],a[72:75]
s_add_u32 m0, s83, 34816
mm a[72:75],v[4:7],v[36:39],a[72:75]
ld v233,s[44:47],s80 offen lds
mm a[8:11],v[4:7],v[44:47],a[8:11]
mm a[8:11],v[0:3],v[40:43],a[8:11]
rt v[212:213],v242 offset:16896
rt v[214:215],v242 offset:17920
mm a[80:83],v[0:3],v[48:51],a[80:83]
s_add_u32 m0, s83, 35968
mm a[80:83],v[4:7],v[52:55],a[80:83]
ld v234,s[44:47],s80 offen lds
mm a[16:19],v[4:7],v[60:63],a[16:19]
mm a[16:19],v[0:3],v[56:59],a[16:19]
rd v[0:3],v236 offset:8192
rd v[4:7],v237 offset:8192
mm a[20:23],v[8:11],v[56:59],a[20:23]
s_add_u32 m0, s83, 36992
mm a[20:23],v[12:15],v[60:63],a[20:23]
ld v235,s[44:47],s80 offen lds
mm a[84:87],v[12:15],v[52:55],a[84:87]
s_mov_b32 s80, 1835008
mm a[84:87],v[8:11],v[48:51],a[84:87]
rt v[216:217],v243 offset:0
rt v[218:219],v243 offset:1024
mm a[12:15],v[8:11],v[40:43],a[12:15]
s_add_u32 m0, s83, 50688
mm a[12:15],v[12:15],v[44:47],a[12:15]
ld v232,s[44:47],s80 offen lds
mm a[76:79],v[12:15],v[36:39],a[76:79]
mm a[76:79],v[8:11],v[32:35],a[76:79]
rd v[8:11],v236 offset:10240
rd v[12:15],v237 offset:10240
mm a[248:251],v[16:19],v[32:35],a[248:251]
s_add_u32 m0, s83, 51712
mm a[248:251],v[20:23],v[36:39],a[248:251]
ld v233,s[44:47],s80 offen lds
mm a[40:43],v[20:23],v[44:47],a[40:43]
mm a[40:43],v[16:19],v[40:43],a[40:43]
rt v[220:221],v243 offset:16896
rt v[222:223],v243 offset:17920
mm a[48:51],v[16:19],v[48:51],a[48:51]
s_add_u32 m0, s83, 52864
mm a[48:51],v[20:23],v[52:55],a[48:51]
ld v234,s[44:47],s80 offen lds
mm a[56:59],v[20:23],v[60:63],a[56:59]
mm a[56:59],v[16:19],v[56:59],a[56:59]
rd v[16:19],v236 offset:0
rd v[20:23],v237 offset:0
mm a[60:63],v[24:27],v[56:59],a[60:63]
s_add_u32 m0, s83, 53888
mm a[60:63],v[28:31],v[60:63],a[60:63]
ld v235,s[44:47],s80 offen lds
mm a[52:55],v[28:31],v[52:55],a[52:55]
s_mov_b64 s[44:45], s[68:69]
s_mov_b32 s46, s70
mm a[52:55],v[24:27],v[48:51],a[52:55]
mm a[44:47],v[24:27],v[40:43],a[44:47]
mm a[44:47],v[28:31],v[44:47],a[44:47]
mm a[252:255],v[28:31],v[36:39],a[252:255]
wt lgkmcnt(2)
s_lshl_b32 s80, s77, 18
mm a[252:255],v[24:27],v[32:35],a[252:255]
rd v[24:27],v236 offset:2048
rd v[28:31],v237 offset:2048
mm a[96:99],v[160:163],v[192:195],a[96:99]
mm a[96:99],v[164:167],v[196:199],a[96:99]
mm a[104:107],v[164:167],v[204:207],a[104:107]
mm a[104:107],v[160:163],v[200:203],a[104:107]
rt v[32:33],v240 offset:128
rt v[34:35],v240 offset:1152
mm a[112:115],v[160:163],v[208:211],a[112:115]
mm a[112:115],v[164:167],v[212:215],a[112:115]
mm a[128:131],v[164:167],v[220:223],a[128:131]
wt lgkmcnt(2)
s_barrier
mm a[128:131],v[160:163],v[216:219],a[128:131]
mm a[132:135],v[168:171],v[216:219],a[132:135]
mm a[132:135],v[172:175],v[220:223],a[132:135]
mm a[116:119],v[172:175],v[212:215],a[116:119]
mm a[116:119],v[168:171],v[208:211],a[116:119]
rt v[36:37],v240 offset:17024
rt v[38:39],v240 offset:18048
mm a[108:111],v[168:171],v[200:203],a[108:111]
mm a[108:111],v[172:175],v[204:207],a[108:111]
mm a[100:103],v[172:175],v[196:199],a[100:103]
mm a[100:103],v[168:171],v[192:195],a[100:103]
mm a[232:235],v[176:179],v[192:195],a[232:235]
mm a[232:235],v[180:183],v[196:199],a[232:235]
mm a[152:155],v[180:183],v[204:207],a[152:155]
mm a[152:155],v[176:179],v[200:203],a[152:155]
rt v[40:41],v241 offset:128
rt v[42:43],v241 offset:1152
mm a[208:211],v[176:179],v[208:211],a[208:211]
mm a[208:211],v[180:183],v[212:215],a[208:211]
mm a[160:163],v[180:183],v[220:223],a[160:163]
mm a[160:163],v[176:179],v[216:219],a[160:163]
mm a[164:167],v[184:187],v[216:219],a[164:167]
mm a[164:167],v[188:191],v[220:223],a[164:167]
mm a[212:215],v[188:191],v[212:215],a[212:215]
mm a[212:215],v[184:187],v[208:211],a[212:215]
rt v[44:45],v241 offset:17024
rt v[46:47],v241 offset:18048
mm a[156:159],v[184:187],v[200:203],a[156:159]
mm a[156:159],v[188:191],v[204:207],a[156:159]
mm a[236:239],v[188:191],v[196:199],a[236:239]
mm a[236:239],v[184:187],v[192:195],a[236:239]
mm a[200:203],v[0:3],v[192:195],a[200:203]
mm a[200:203],v[4:7],v[196:199],a[200:203]
mm a[184:187],v[4:7],v[204:207],a[184:187]
mm a[184:187],v[0:3],v[200:203],a[184:187]
rt v[48:49],v242 offset:128
rt v[50:51],v242 offset:1152
mm a[64:67],v[0:3],v[208:211],a[64:67]
mm a[64:67],v[4:7],v[212:215],a[64:67]
mm a[0:3],v[4:7],v[220:223],a[0:3]
mm a[0:3],v[0:3],v[216:219],a[0:3]
mm a[4:7],v[8:11],v[216:219],a[4:7]
mm a[4:7],v[12:15],v[220:223],a[4:7]
buffer_load_dword v254, v135, s[68:71], s96 offen
mm a[68:71],v[12:15],v[212:215],a[68:71]
s_add_u32 s96, s96, 1048576
mm a[68:71],v[8:11],v[208:211],a[68:71]
rt v[52:53],v242 offset:17024
rt v[54:55],v242 offset:18048
mm a[188:191],v[8:11],v[200:203],a[188:191]
mm a[188:191],v[12:15],v[204:207],a[188:191]
buffer_load_dword v254, v135, s[68:71], s96 offen
mm a[204:207],v[12:15],v[196:199],a[204:207]
s_add_u32 s96, s96, 1048576
mm a[204:207],v[8:11],v[192:195],a[204:207]
rt v[56:57],v243 offset:128
rt v[58:59],v243 offset:1152
mm a[88:91],v[16:19],v[192:195],a[88:91]
mm a[88:91],v[20:23],v[196:199],a[88:91]
mm a[24:27],v[20:23],v[204:207],a[24:27]
mm a[24:27],v[16:19],v[200:203],a[24:27]
rt v[60:61],v243 offset:17024
rt v[62:63],v243 offset:18048
mm a[240:243],v[16:19],v[208:211],a[240:243]
mm a[240:243],v[20:23],v[212:215],a[240:243]
mm a[32:35],v[20:23],v[220:223],a[32:35]
mm a[32:35],v[16:19],v[216:219],a[32:35]
mm a[36:39],v[24:27],v[216:219],a[36:39]
mm a[36:39],v[28:31],v[220:223],a[36:39]
ld v[72:75],v255,s[64:67],s89 offen offset:1024 sc1
mm a[244:247],v[28:31],v[212:215],a[244:247]
mm a[244:247],v[24:27],v[208:211],a[244:247]
mm a[28:31],v[24:27],v[200:203],a[28:31]
mm a[28:31],v[28:31],v[204:207],a[28:31]
mm a[92:95],v[28:31],v[196:199],a[92:95]
wt vmcnt(3) lgkmcnt(0)
s_barrier
mm a[92:95],v[24:27],v[192:195],a[92:95]
mm a[120:123],v[160:163],v[32:35],a[120:123]
mm a[120:123],v[164:167],v[36:39],a[120:123]
mm a[136:139],v[164:167],v[44:47],a[136:139]
rt v[192:193],v240 offset:33792
rt v[194:195],v240 offset:34816
mm a[136:139],v[160:163],v[40:43],a[136:139]
rt v[196:197],v240 offset:50688
rt v[198:199],v240 offset:51712
mm a[224:227],v[160:163],v[48:51],a[224:227]
mm a[224:227],v[164:167],v[52:55],a[224:227]
mm a[144:147],v[164:167],v[60:63],a[144:147]
mm a[144:147],v[160:163],v[56:59],a[144:147]
rd v[160:163],v236 offset:57344
rd v[164:167],v237 offset:57344
mm a[148:151],v[168:171],v[56:59],a[148:151]
mm a[148:151],v[172:175],v[60:63],a[148:151]
mm a[228:231],v[172:175],v[52:55],a[228:231]
mm a[228:231],v[168:171],v[48:51],a[228:231]
rt v[200:201],v241 offset:33792
rt v[202:203],v241 offset:34816
mm a[140:143],v[168:171],v[40:43],a[140:143]
mm a[140:143],v[172:175],v[44:47],a[140:143]
mm a[124:127],v[172:175],v[36:39],a[124:127]
mm a[124:127],v[168:171],v[32:35],a[124:127]
rd v[168:171],v236 offset:59392
rd v[172:175],v237 offset:59392
mm a[216:219],v[176:179],v[32:35],a[216:219]
mm a[216:219],v[180:183],v[36:39],a[216:219]
mm a[168:171],v[180:183],v[44:47],a[168:171]
mm a[168:171],v[176:179],v[40:43],a[168:171]
rt v[204:205],v241 offset:50688
rt v[206:207],v241 offset:51712
mm a[192:195],v[176:179],v[48:51],a[192:195]
mm a[192:195],v[180:183],v[52:55],a[192:195]
mm a[176:179],v[180:183],v[60:63],a[176:179]
mm a[176:179],v[176:179],v[56:59],a[176:179]
rd v[176:179],v236 offset:49152
rd v[180:183],v237 offset:49152
mm a[180:183],v[184:187],v[56:59],a[180:183]
mm a[180:183],v[188:191],v[60:63],a[180:183]
mm a[196:199],v[188:191],v[52:55],a[196:199]
mm a[196:199],v[184:187],v[48:51],a[196:199]
rt v[208:209],v242 offset:33792
rt v[210:211],v242 offset:34816
mm a[172:175],v[184:187],v[40:43],a[172:175]
s_add_u32 m0, s83, 0
mm a[172:175],v[188:191],v[44:47],a[172:175]
ld v232,s[44:47],s80 offen lds
mm a[220:223],v[188:191],v[36:39],a[220:223]
mm a[220:223],v[184:187],v[32:35],a[220:223]
rd v[184:187],v236 offset:51200
rd v[188:191],v237 offset:51200
mm a[72:75],v[0:3],v[32:35],a[72:75]
s_add_u32 m0, s83, 1024
mm a[72:75],v[4:7],v[36:39],a[72:75]
ld v233,s[44:47],s80 offen lds
mm a[8:11],v[4:7],v[44:47],a[8:11]
mm a[8:11],v[0:3],v[40:43],a[8:11]
rt v[212:213],v242 offset:50688
rt v[214:215],v242 offset:51712
mm a[80:83],v[0:3],v[48:51],a[80:83]
s_add_u32 m0, s83, 2176
mm a[80:83],v[4:7],v[52:55],a[80:83]
ld v234,s[44:47],s80 offen lds
mm a[16:19],v[4:7],v[60:63],a[16:19]
mm a[16:19],v[0:3],v[56:59],a[16:19]
rd v[0:3],v236 offset:40960
rd v[4:7],v237 offset:40960
mm a[20:23],v[8:11],v[56:59],a[20:23]
s_add_u32 m0, s83, 3200
mm a[20:23],v[12:15],v[60:63],a[20:23]
ld v235,s[44:47],s80 offen lds
mm a[84:87],v[12:15],v[52:55],a[84:87]
s_add_u32 s80, s80, 524288
mm a[84:87],v[8:11],v[48:51],a[84:87]
rt v[216:217],v243 offset:33792
rt v[218:219],v243 offset:34816
mm a[12:15],v[8:11],v[40:43],a[12:15]
s_add_u32 m0, s83, 16896
mm a[12:15],v[12:15],v[44:47],a[12:15]
ld v232,s[44:47],s80 offen lds
mm a[76:79],v[12:15],v[36:39],a[76:79]
mm a[76:79],v[8:11],v[32:35],a[76:79]
rd v[8:11],v236 offset:43008
rd v[12:15],v237 offset:43008
mm a[248:251],v[16:19],v[32:35],a[248:251]
s_add_u32 m0, s83, 17920
mm a[248:251],v[20:23],v[36:39],a[248:251]
ld v233,s[44:47],s80 offen lds
mm a[40:43],v[20:23],v[44:47],a[40:43]
mm a[40:43],v[16:19],v[40:43],a[40:43]
rt v[220:221],v243 offset:50688
rt v[222:223],v243 offset:51712
mm a[48:51],v[16:19],v[48:51],a[48:51]
s_add_u32 m0, s83, 19072
mm a[48:51],v[20:23],v[52:55],a[48:51]
ld v234,s[44:47],s80 offen lds
mm a[56:59],v[20:23],v[60:63],a[56:59]
mm a[56:59],v[16:19],v[56:59],a[56:59]
rd v[16:19],v236 offset:32768
rd v[20:23],v237 offset:32768
mm a[60:63],v[24:27],v[56:59],a[60:63]
s_add_u32 m0, s83, 20096
mm a[60:63],v[28:31],v[60:63],a[60:63]
ld v235,s[44:47],s80 offen lds
mm a[52:55],v[28:31],v[52:55],a[52:55]
s_add_u32 s80, s80, 524288
mm a[52:55],v[24:27],v[48:51],a[52:55]
mm a[44:47],v[24:27],v[40:43],a[44:47]
mm a[44:47],v[28:31],v[44:47],a[44:47]
mm a[252:255],v[28:31],v[36:39],a[252:255]
wt lgkmcnt(2)
mm a[252:255],v[24:27],v[32:35],a[252:255]
rd v[24:27],v236 offset:34816
rd v[28:31],v237 offset:34816
mm a[96:99],v[160:163],v[192:195],a[96:99]
mm a[96:99],v[164:167],v[196:199],a[96:99]
mm a[104:107],v[164:167],v[204:207],a[104:107]
mm a[104:107],v[160:163],v[200:203],a[104:107]
rt v[32:33],v240 offset:33920
rt v[34:35],v240 offset:34944
mm a[112:115],v[160:163],v[208:211],a[112:115]
mm a[112:115],v[164:167],v[212:215],a[112:115]
mm a[128:131],v[164:167],v[220:223],a[128:131]
wt lgkmcnt(2)
s_barrier
mm a[128:131],v[160:163],v[216:219],a[128:131]
mm a[132:135],v[168:171],v[216:219],a[132:135]
wt vmcnt(8)
v_cmp_eq_u64 vcc, v[72:73], v[252:253]
mm a[132:135],v[172:175],v[220:223],a[132:135]
v_cmp_eq_u64 s[96:97], v[74:75], v[252:253]
mm a[116:119],v[172:175],v[212:215],a[116:119]
s_and_b64 vcc, vcc, s[96:97]
s_cmp_eq_u64 vcc, -1
s_cbranch_scc0 .La5_LV_XSPIN
.La5_LV_XOK:
mm a[116:119],v[168:171],v[208:211],a[116:119]
rt v[36:37],v240 offset:50816
rt v[38:39],v240 offset:51840
mm a[108:111],v[168:171],v[200:203],a[108:111]
s_mov_b32 s89, 49152
mm a[108:111],v[172:175],v[204:207],a[108:111]
mm a[100:103],v[172:175],v[196:199],a[100:103]
mm a[100:103],v[168:171],v[192:195],a[100:103]
mm a[232:235],v[176:179],v[192:195],a[232:235]
mm a[232:235],v[180:183],v[196:199],a[232:235]
ld v[128:131],v244,s[52:55],s91 offen offset:0 sc1
mm a[152:155],v[180:183],v[204:207],a[152:155]
ar v224,a96
ar v225,a97
mm a[152:155],v[176:179],v[200:203],a[152:155]
rt v[40:41],v241 offset:33920
rt v[42:43],v241 offset:34944
mm a[208:211],v[176:179],v[208:211],a[208:211]
ar v226,a98
ar v227,a99
mm a[208:211],v[180:183],v[212:215],a[208:211]
ld v[132:135],v245,s[52:55],s91 offen offset:0 sc1
mm a[160:163],v[180:183],v[220:223],a[160:163]
ar v228,a100
ar v229,a101
mm a[160:163],v[176:179],v[216:219],a[160:163]
ar v230,a102
ar v231,a103
mm a[164:167],v[184:187],v[216:219],a[164:167]
cv v248,v224,v225
cv v249,v226,v227
mm a[164:167],v[188:191],v[220:223],a[164:167]
ld v[136:139],v246,s[52:55],s91 offen offset:0 sc1
mm a[212:215],v[188:191],v[212:215],a[212:215]
cv v250,v228,v229
cv v251,v230,v231
mm a[212:215],v[184:187],v[208:211],a[212:215]
rt v[44:45],v241 offset:50816
rt v[46:47],v241 offset:51840
mm a[156:159],v[184:187],v[200:203],a[156:159]
st v[248:251],v255,s[48:51],s89 offen sc1
mm a[156:159],v[188:191],v[204:207],a[156:159]
ld v[140:143],v247,s[52:55],s91 offen offset:0 sc1
mm a[236:239],v[188:191],v[196:199],a[236:239]
s_mov_b32 s89, 50176
ds_write_b128 v238, v[248:251] offset:0
mm a[236:239],v[184:187],v[192:195],a[236:239]
ar v224,a104
ar v225,a105
mm a[200:203],v[0:3],v[192:195],a[200:203]
ar v226,a106
ar v227,a107
mm a[200:203],v[4:7],v[196:199],a[200:203]
ld v[144:147],v244,s[52:55],s91 offen offset:1024 sc1
mm a[184:187],v[4:7],v[204:207],a[184:187]
ar v228,a108
ar v229,a109
mm a[184:187],v[0:3],v[200:203],a[184:187]
rt v[48:49],v242 offset:33920
rt v[50:51],v242 offset:34944
mm a[64:67],v[0:3],v[208:211],a[64:67]
ar v230,a110
ar v231,a111
mm a[64:67],v[4:7],v[212:215],a[64:67]
ld v[148:151],v245,s[52:55],s91 offen offset:1024 sc1
mm a[0:3],v[4:7],v[220:223],a[0:3]
cv v248,v224,v225
cv v249,v226,v227
mm a[0:3],v[0:3],v[216:219],a[0:3]
cv v250,v228,v229
cv v251,v230,v231
mm a[4:7],v[8:11],v[216:219],a[4:7]
st v[248:251],v255,s[48:51],s89 offen sc1
mm a[4:7],v[12:15],v[220:223],a[4:7]
ld v[152:155],v246,s[52:55],s91 offen offset:1024 sc1
mm a[68:71],v[12:15],v[212:215],a[68:71]
s_mov_b32 s89, 51200
ds_write_b128 v238, v[248:251] offset:1024
mm a[68:71],v[8:11],v[208:211],a[68:71]
rt v[52:53],v242 offset:50816
rt v[54:55],v242 offset:51840
mm a[188:191],v[8:11],v[200:203],a[188:191]
ar v224,a112
ar v225,a113
mm a[188:191],v[12:15],v[204:207],a[188:191]
ld v[156:159],v247,s[52:55],s91 offen offset:1024 sc1
mm a[204:207],v[12:15],v[196:199],a[204:207]
ar v226,a114
ar v227,a115
mm a[204:207],v[8:11],v[192:195],a[204:207]
rt v[56:57],v243 offset:33920
rt v[58:59],v243 offset:34944
mm a[88:91],v[16:19],v[192:195],a[88:91]
ar v228,a116
ar v229,a117
mm a[88:91],v[20:23],v[196:199],a[88:91]
ar v230,a118
ar v231,a119
mm a[24:27],v[20:23],v[204:207],a[24:27]
cv v248,v224,v225
cv v249,v226,v227
mm a[24:27],v[16:19],v[200:203],a[24:27]
rt v[60:61],v243 offset:50816
rt v[62:63],v243 offset:51840
mm a[240:243],v[16:19],v[208:211],a[240:243]
v_subrev_u32 v240, 8448, v240
v_subrev_u32 v241, 8448, v241
mm a[240:243],v[20:23],v[212:215],a[240:243]
v_subrev_u32 v242, 8448, v242
v_subrev_u32 v243, 8448, v243
mm a[32:35],v[20:23],v[220:223],a[32:35]
.La5_LW1_8:
s_cbranch_execz .La5_LW1_12
cv v250,v228,v229
cv v251,v230,v231
mm a[32:35],v[16:19],v[216:219],a[32:35]
st v[248:251],v255,s[48:51],s89 offen sc1
mm a[36:39],v[24:27],v[216:219],a[36:39]
s_mov_b32 s89, 52224
ds_write_b128 v238, v[248:251] offset:2048
mm a[36:39],v[28:31],v[220:223],a[36:39]
mm a[244:247],v[28:31],v[212:215],a[244:247]
mm a[244:247],v[24:27],v[208:211],a[244:247]
mm a[28:31],v[24:27],v[200:203],a[28:31]
mm a[28:31],v[28:31],v[204:207],a[28:31]
mm a[92:95],v[28:31],v[196:199],a[92:95]
wt vmcnt(11) lgkmcnt(1)
s_barrier
mm a[92:95],v[24:27],v[192:195],a[92:95]
mm a[120:123],v[160:163],v[32:35],a[120:123]
s_add_u32 m0, s83, 33792
mm a[120:123],v[164:167],v[36:39],a[120:123]
ld v232,s[44:47],s80 offen lds
mm a[136:139],v[164:167],v[44:47],a[136:139]
mm a[136:139],v[160:163],v[40:43],a[136:139]
rt v[192:193],v240 offset:0
rt v[194:195],v240 offset:1024
mm a[224:227],v[160:163],v[48:51],a[224:227]
s_add_u32 m0, s83, 34816
mm a[224:227],v[164:167],v[52:55],a[224:227]
ld v233,s[44:47],s80 offen lds
mm a[144:147],v[164:167],v[60:63],a[144:147]
mm a[144:147],v[160:163],v[56:59],a[144:147]
rt v[196:197],v240 offset:8448
rt v[198:199],v240 offset:9472
mm a[148:151],v[168:171],v[56:59],a[148:151]
s_add_u32 m0, s83, 35968
mm a[148:151],v[172:175],v[60:63],a[148:151]
ld v234,s[44:47],s80 offen lds
mm a[228:231],v[172:175],v[52:55],a[228:231]
mm a[228:231],v[168:171],v[48:51],a[228:231]
rt v[200:201],v241 offset:0
rt v[202:203],v241 offset:1024
mm a[140:143],v[168:171],v[40:43],a[140:143]
s_add_u32 m0, s83, 36992
mm a[140:143],v[172:175],v[44:47],a[140:143]
ld v235,s[44:47],s80 offen lds
mm a[124:127],v[172:175],v[36:39],a[124:127]
s_add_u32 s80, s80, 524288
mm a[124:127],v[168:171],v[32:35],a[124:127]
rt v[204:205],v241 offset:8448
rt v[206:207],v241 offset:9472
mm a[216:219],v[176:179],v[32:35],a[216:219]
s_add_u32 m0, s83, 50688
mm a[216:219],v[180:183],v[36:39],a[216:219]
ld v232,s[44:47],s80 offen lds
mm a[168:171],v[180:183],v[44:47],a[168:171]
mm a[168:171],v[176:179],v[40:43],a[168:171]
rt v[208:209],v242 offset:0
rt v[210:211],v242 offset:1024
mm a[192:195],v[176:179],v[48:51],a[192:195]
s_add_u32 m0, s83, 51712
mm a[192:195],v[180:183],v[52:55],a[192:195]
ld v233,s[44:47],s80 offen lds
mm a[176:179],v[180:183],v[60:63],a[176:179]
mm a[176:179],v[176:179],v[56:59],a[176:179]
rt v[212:213],v242 offset:8448
rt v[214:215],v242 offset:9472
mm a[180:183],v[184:187],v[56:59],a[180:183]
s_add_u32 m0, s83, 52864
mm a[180:183],v[188:191],v[60:63],a[180:183]
ld v234,s[44:47],s80 offen lds
mm a[196:199],v[188:191],v[52:55],a[196:199]
mm a[196:199],v[184:187],v[48:51],a[196:199]
rt v[216:217],v243 offset:0
rt v[218:219],v243 offset:1024
mm a[172:175],v[184:187],v[40:43],a[172:175]
s_add_u32 m0, s83, 53888
mm a[172:175],v[188:191],v[44:47],a[172:175]
ld v235,s[44:47],s80 offen lds
mm a[220:223],v[188:191],v[36:39],a[220:223]
s_add_u32 s80, s80, 524288
mm a[220:223],v[184:187],v[32:35],a[220:223]
rt v[220:221],v243 offset:8448
rt v[222:223],v243 offset:9472
mm a[72:75],v[0:3],v[32:35],a[72:75]
mm a[72:75],v[4:7],v[36:39],a[72:75]
ld v[160:163],v244,s[52:55],s91 offen offset:2048 sc1
mm a[8:11],v[4:7],v[44:47],a[8:11]
mm a[8:11],v[0:3],v[40:43],a[8:11]
mm a[80:83],v[0:3],v[48:51],a[80:83]
mm a[80:83],v[4:7],v[52:55],a[80:83]
ld v[164:167],v245,s[52:55],s91 offen offset:2048 sc1
mm a[16:19],v[4:7],v[60:63],a[16:19]
mm a[16:19],v[0:3],v[56:59],a[16:19]
mm a[20:23],v[8:11],v[56:59],a[20:23]
mm a[20:23],v[12:15],v[60:63],a[20:23]
ld v[168:171],v246,s[52:55],s91 offen offset:2048 sc1
mm a[84:87],v[12:15],v[52:55],a[84:87]
mm a[84:87],v[8:11],v[48:51],a[84:87]
mm a[12:15],v[8:11],v[40:43],a[12:15]
mm a[12:15],v[12:15],v[44:47],a[12:15]
ld v[172:175],v247,s[52:55],s91 offen offset:2048 sc1
mm a[76:79],v[12:15],v[36:39],a[76:79]
mm a[76:79],v[8:11],v[32:35],a[76:79]
mm a[248:251],v[16:19],v[32:35],a[248:251]
mm a[248:251],v[20:23],v[36:39],a[248:251]
mm a[40:43],v[20:23],v[44:47],a[40:43]
mm a[40:43],v[16:19],v[40:43],a[40:43]
mm a[48:51],v[16:19],v[48:51],a[48:51]
mm a[48:51],v[20:23],v[52:55],a[48:51]
mm a[56:59],v[20:23],v[60:63],a[56:59]
mm a[56:59],v[16:19],v[56:59],a[56:59]
mm a[60:63],v[24:27],v[56:59],a[60:63]
mm a[60:63],v[28:31],v[60:63],a[60:63]
mm a[52:55],v[28:31],v[52:55],a[52:55]
mm a[52:55],v[24:27],v[48:51],a[52:55]
mm a[44:47],v[24:27],v[40:43],a[44:47]
mm a[44:47],v[28:31],v[44:47],a[44:47]
mm a[252:255],v[28:31],v[36:39],a[252:255]
mm a[252:255],v[24:27],v[32:35],a[252:255]
; ---- Q tile: 256 rows x 128 columns, K = 1024, while K / V leave the accumulators (memory + .La5_LDS ring); behind it the first step on the diagonal
.La5_LQ_START:
.La5_LW1_9:
s_cbranch_execz .La5_LW1_13
wt vmcnt(13) lgkmcnt(4)
mm v[0:3],v[192:195],v[128:131],0
ar v224,a128
ar v225,a129
s_mov_b32 s85, s95
mm v[0:3],v[196:199],v[144:147],v[0:3]
ld v[176:179],v244,s[52:55],s91 offen offset:3072 sc1
mm v[32:35],v[196:199],v[148:151],0
ar v226,a130
ar v227,a131
mm v[32:35],v[192:195],v[132:135],v[32:35]
ar v228,a132
ar v229,a133
mm v[64:67],v[192:195],v[136:139],0
ar v230,a134
ar v231,a135
mm v[64:67],v[196:199],v[152:155],v[64:67]
ld v[180:183],v245,s[52:55],s91 offen offset:3072 sc1
mm v[96:99],v[196:199],v[156:159],0
cv v248,v224,v225
cv v249,v226,v227
mm v[96:99],v[192:195],v[140:143],v[96:99]
rt v[196:197],v240 offset:128
rt v[198:199],v240 offset:1152
mm v[100:103],v[200:203],v[140:143],0
cv v250,v228,v229
cv v251,v230,v231
mm v[100:103],v[204:207],v[156:159],v[100:103]
ld v[184:187],v246,s[52:55],s91 offen offset:3072 sc1
mm v[68:71],v[204:207],v[152:155],0
aw a128,v248
aw a129,v249
mm v[68:71],v[200:203],v[136:139],v[68:71]
rt v[192:193],v240 offset:8576
rt v[194:195],v240 offset:9600
mm v[36:39],v[200:203],v[132:135],0
aw a130,v250
aw a131,v251
mm v[36:39],v[204:207],v[148:151],v[36:39]
ld v[188:191],v247,s[52:55],s91 offen offset:3072 sc1
mm v[4:7],v[204:207],v[144:147],0
wt lgkmcnt(4)
s_add_u32 s91, s91, 4096
mm v[4:7],v[200:203],v[128:131],v[4:7]
rt v[204:205],v241 offset:128
rt v[206:207],v241 offset:1152
mm v[8:11],v[208:211],v[128:131],0
s_and_b32 s97, s2, 7
s_mul_i32 s95, s97, 7
mm v[8:11],v[212:215],v[144:147],v[8:11]
ld a[96:99],v244,s[52:55],s91 offen offset:0 sc1
mm v[40:43],v[212:215],v[148:151],0
s_lshr_b32 s96, s30, 7
s_add_u32 s95, s95, s96
mm v[40:43],v[208:211],v[132:135],v[40:43]
rt v[200:201],v241 offset:8576
rt v[202:203],v241 offset:9600
mm v[72:75],v[208:211],v[136:139],0
s_sub_u32 s95, 3, s95
s_and_b32 s95, s95, 7
mm v[72:75],v[212:215],v[152:155],v[72:75]
ld a[100:103],v245,s[52:55],s91 offen offset:0 sc1
mm v[104:107],v[212:215],v[156:159],0
s_min_u32 s95, s95, 6
s_lshl_b32 s95, s95, 1
mm v[104:107],v[208:211],v[140:143],v[104:107]
rt v[212:213],v242 offset:128
rt v[214:215],v242 offset:1152
mm v[108:111],v[216:219],v[140:143],0
s_lshr_b32 s92, s2, 6
s_mul_i32 s93, s92, 3
mm v[108:111],v[220:223],v[156:159],v[108:111]
ld a[104:107],v246,s[52:55],s91 offen offset:0 sc1
mm v[76:79],v[220:223],v[152:155],0
s_add_u32 s93, s93, s74
mm v[76:79],v[216:219],v[136:139],v[76:79]
rt v[208:209],v242 offset:8576
rt v[210:211],v242 offset:9600
mm v[44:47],v[216:219],v[132:135],0
ds_write_b128 v238, v[248:251] offset:3072
mm v[44:47],v[220:223],v[148:151],v[44:47]
st v[248:251],v255,s[48:51],s89 offen offset:0 sc1
mm v[12:15],v[220:223],v[144:147],0
wt lgkmcnt(5)
mm v[12:15],v[216:219],v[128:131],v[12:15]
rt v[220:221],v243 offset:128
rt v[222:223],v243 offset:1152
mm v[16:19],v[196:199],v[128:131],0
ar v224,a120
ar v225,a121
mm v[16:19],v[192:195],v[144:147],v[16:19]
ld a[108:111],v247,s[52:55],s91 offen offset:0 sc1
mm v[48:51],v[192:195],v[148:151],0
ar v226,a122
ar v227,a123
mm v[48:51],v[196:199],v[132:135],v[48:51]
rt v[216:217],v243 offset:8576
rt v[218:219],v243 offset:9600
mm v[80:83],v[196:199],v[136:139],0
ar v228,a124
ar v229,a125
mm v[80:83],v[192:195],v[152:155],v[80:83]
ld a[112:115],v244,s[52:55],s91 offen offset:1024 sc1
mm v[112:115],v[192:195],v[156:159],0
ar v230,a126
ar v231,a127
mm v[112:115],v[196:199],v[140:143],v[112:115]
cv v248,v224,v225
cv v249,v226,v227
mm v[116:119],v[204:207],v[140:143],0
wt lgkmcnt(0)
s_barrier
mm v[116:119],v[200:203],v[156:159],v[116:119]
ld a[116:119],v245,s[52:55],s91 offen offset:1024 sc1
mm v[84:87],v[200:203],v[152:155],0
rt v[192:193],v240 offset:16896
rt v[194:195],v240 offset:17920
mm v[84:87],v[204:207],v[136:139],v[84:87]
rt v[196:197],v240 offset:25344
rt v[198:199],v240 offset:26368
mm v[52:55],v[204:207],v[132:135],0
s_add_u32 m0, s83, 0
cv v250,v228,v229
mm v[52:55],v[200:203],v[148:151],v[52:55]
ld v232,s[44:47],s80 offen lds
mm v[20:23],v[200:203],v[144:147],0
cv v251,v230,v231
aw a132,v248
mm v[20:23],v[204:207],v[128:131],v[20:23]
rt v[200:201],v241 offset:16896
rt v[202:203],v241 offset:17920
mm v[24:27],v[212:215],v[128:131],0
aw a133,v249
aw a134,v250
mm v[24:27],v[208:211],v[144:147],v[24:27]
aw a135,v251
mm v[56:59],v[208:211],v[148:151],0
s_cmp_ge_u32 s93, s95
s_cselect_b32 s96, 2, 0
mm v[56:59],v[212:215],v[132:135],v[56:59]
rt v[204:205],v241 offset:25344
rt v[206:207],v241 offset:26368
mm v[88:91],v[212:215],v[136:139],0
s_add_u32 m0, s83, 1024
s_add_u32 s93, s93, s96
mm v[88:91],v[208:211],v[152:155],v[88:91]
ld v233,s[44:47],s80 offen lds
mm v[120:123],v[208:211],v[156:159],0
s_add_u32 s95, s95, s92
mm v[120:123],v[212:215],v[140:143],v[120:123]
rt v[208:209],v242 offset:16896
rt v[210:211],v242 offset:17920
mm v[124:127],v[220:223],v[140:143],0
s_cmp_lt_u32 s92, 2
s_cselect_b32 s94, s75, 0
mm v[124:127],v[216:219],v[156:159],v[124:127]
ld v[140:143],v246,s[52:55],s91 offen offset:1024 sc1
mm v[92:95],v[216:219],v[152:155],0
s_cmp_eq_u32 s74, 3
s_cselect_b32 s92, s95, s93
s_cselect_b32 s94, s94, s75
mm v[92:95],v[220:223],v[136:139],v[92:95]
rt v[212:213],v242 offset:25344
rt v[214:215],v242 offset:26368
mm v[60:63],v[220:223],v[132:135],0
ds_write_b128 v238, v[248:251] offset:4096
mm v[60:63],v[216:219],v[148:151],v[60:63]
s_mul_i32 s93, s97, 14
s_add_u32 s92, s92, s93
mm v[28:31],v[216:219],v[144:147],0
wt vmcnt(10) lgkmcnt(5)
s_mul_i32 s96, s92, s94
mm v[28:31],v[220:223],v[128:131],v[28:31]
rt v[216:217],v243 offset:16896
rt v[218:219],v243 offset:17920
mm v[0:3],v[192:195],v[160:163],v[0:3]
ar v224,a136
ar v225,a137
mm v[0:3],v[196:199],v[176:179],v[0:3]
ld v[128:131],v247,s[52:55],s91 offen offset:1024 sc1
mm v[32:35],v[196:199],v[180:183],v[32:35]
ar v226,a138
ar v227,a139
mm v[32:35],v[192:195],v[164:167],v[32:35]
rt v[220:221],v243 offset:25344
rt v[222:223],v243 offset:26368
mm v[64:67],v[192:195],v[168:171],v[64:67]
ar v228,a140
ar v229,a141
mm v[64:67],v[196:199],v[184:187],v[64:67]
ld v[132:135],v244,s[52:55],s91 offen offset:2048 sc1
mm v[96:99],v[196:199],v[188:191],v[96:99]
ar v230,a142
ar v231,a143
mm v[96:99],v[192:195],v[172:175],v[96:99]
rt v[196:197],v240 offset:17024
rt v[198:199],v240 offset:18048
mm v[100:103],v[200:203],v[172:175],v[100:103]
s_add_u32 m0, s83, 2176
cv v248,v224,v225
mm v[100:103],v[204:207],v[188:191],v[100:103]
ld v234,s[44:47],s80 offen lds
mm v[68:71],v[204:207],v[184:187],v[68:71]
cv v249,v226,v227
cv v250,v228,v229
mm v[68:71],v[200:203],v[168:171],v[68:71]
rt v[192:193],v240 offset:25472
rt v[194:195],v240 offset:26496
mm v[36:39],v[200:203],v[164:167],v[36:39]
cv v251,v230,v231
aw a136,v248
mm v[36:39],v[204:207],v[180:183],v[36:39]
ld v[136:139],v245,s[52:55],s91 offen offset:2048 sc1
mm v[4:7],v[204:207],v[176:179],v[4:7]
wt lgkmcnt(4)
aw a137,v249
mm v[4:7],v[200:203],v[160:163],v[4:7]
rt v[204:205],v241 offset:17024
rt v[206:207],v241 offset:18048
mm v[8:11],v[208:211],v[160:163],v[8:11]
s_add_u32 m0, s83, 3200
aw a138,v250
mm v[8:11],v[212:215],v[176:179],v[8:11]
ld v235,s[44:47],s80 offen lds
mm v[40:43],v[212:215],v[180:183],v[40:43]
s_add_u32 s80, s80, 524288
aw a139,v251
mm v[40:43],v[208:211],v[164:167],v[40:43]
rt v[200:201],v241 offset:25472
rt v[202:203],v241 offset:26496
mm v[72:75],v[208:211],v[168:171],v[72:75]
s_mov_b32 s100, s96
s_and_b32 s93, s2, 7
mm v[72:75],v[212:215],v[184:187],v[72:75]
ld v[144:147],v246,s[52:55],s91 offen offset:2048 sc1
mm v[104:107],v[212:215],v[188:191],v[104:107]
s_lshr_b32 s95, s93, 1
s_xor_b32 s97, s95, s93
mm v[104:107],v[208:211],v[172:175],v[104:107]
rt v[212:213],v242 offset:17024
rt v[214:215],v242 offset:18048
mm v[108:111],v[216:219],v[172:175],v[108:111]
s_lshl_b32 s95, s95, 21
s_and_b32 s97, s97, 1
mm v[108:111],v[220:223],v[188:191],v[108:111]
ld v[148:151],v247,s[52:55],s91 offen offset:2048 sc1
mm v[76:79],v[220:223],v[184:187],v[76:79]
s_mov_b32 s18, 53248
s_lshl_b32 s97, s97, 10
mm v[76:79],v[216:219],v[168:171],v[76:79]
rt v[208:209],v242 offset:25472
rt v[210:211],v242 offset:26496
mm v[44:47],v[216:219],v[164:167],v[44:47]
ds_write_b128 v238, v[248:251] offset:5120
mm v[44:47],v[220:223],v[180:183],v[44:47]
st a[132:135],v255,s[48:51],s18 offen offset:0 sc1
mm v[12:15],v[220:223],v[176:179],v[12:15]
wt lgkmcnt(5)
s_add_u32 s95, s95, s97
mm v[12:15],v[216:219],v[160:163],v[12:15]
rt v[220:221],v243 offset:17024
rt v[222:223],v243 offset:18048
mm v[16:19],v[196:199],v[160:163],v[16:19]
ar v224,a224
ar v225,a225
mm v[16:19],v[192:195],v[176:179],v[16:19]
ld v[152:155],v244,s[52:55],s91 offen offset:3072 sc1
mm v[48:51],v[192:195],v[180:183],v[48:51]
ar v226,a226
ar v227,a227
mm v[48:51],v[196:199],v[164:167],v[48:51]
rt v[216:217],v243 offset:25472
rt v[218:219],v243 offset:26496
mm v[80:83],v[196:199],v[168:171],v[80:83]
ar v228,a228
ar v229,a229
mm v[80:83],v[192:195],v[184:187],v[80:83]
ld v[156:159],v245,s[52:55],s91 offen offset:3072 sc1
mm v[112:115],v[192:195],v[188:191],v[112:115]
ar v230,a230
ar v231,a231
mm v[112:115],v[196:199],v[172:175],v[112:115]
cv v248,v224,v225
cv v249,v226,v227
mm v[116:119],v[204:207],v[172:175],v[116:119]
wt lgkmcnt(0)
s_barrier
mm v[116:119],v[200:203],v[188:191],v[116:119]
ld a[120:123],v246,s[52:55],s91 offen offset:3072 sc1
mm v[84:87],v[200:203],v[184:187],v[84:87]
rt v[192:193],v240 offset:33792
rt v[194:195],v240 offset:34816
mm v[84:87],v[204:207],v[168:171],v[84:87]
rt v[196:197],v240 offset:42240
rt v[198:199],v240 offset:43264
mm v[52:55],v[204:207],v[164:167],v[52:55]
s_add_u32 m0, s83, 16896
cv v250,v228,v229
mm v[52:55],v[200:203],v[180:183],v[52:55]
ld v232,s[44:47],s80 offen lds
mm v[20:23],v[200:203],v[176:179],v[20:23]
cv v251,v230,v231
aw a140,v248
mm v[20:23],v[204:207],v[160:163],v[20:23]
rt v[200:201],v241 offset:33792
rt v[202:203],v241 offset:34816
mm v[24:27],v[212:215],v[160:163],v[24:27]
aw a141,v249
aw a142,v250
mm v[24:27],v[208:211],v[176:179],v[24:27]
ld a[124:127],v247,s[52:55],s91 offen offset:3072 sc1
mm v[56:59],v[208:211],v[180:183],v[56:59]
s_add_u32 s91, s91, 4096
aw a143,v251
mm v[56:59],v[212:215],v[164:167],v[56:59]
rt v[204:205],v241 offset:42240
rt v[206:207],v241 offset:43264
mm v[88:91],v[212:215],v[168:171],v[88:91]
s_add_u32 m0, s83, 17920
s_mul_i32 s97, s93, 896
mm v[88:91],v[208:211],v[184:187],v[88:91]
ld v233,s[44:47],s80 offen lds
mm v[120:123],v[208:211],v[188:191],v[120:123]
s_cmp_eq_u32 s3, 0
s_cselect_b32 s95, s95, s97
s_cselect_b64 s[36:37], s[28:29], s[30:31]
s_cselect_b32 s21, 2, 7
s_cselect_b32 s93, s93, 0
mm v[120:123],v[212:215],v[172:175],v[120:123]
rt v[208:209],v242 offset:33792
rt v[210:211],v242 offset:34816
mm v[124:127],v[220:223],v[172:175],v[124:127]
s_cmp_eq_u32 s3, 2
s_cselect_b64 s[36:37], s[32:33], s[36:37]
mm v[124:127],v[216:219],v[188:191],v[124:127]
s_lshl_b32 s97, s97, 10
s_mov_b32 s92, 704
mm v[92:95],v[216:219],v[184:187],v[92:95]
s_mov_b32 s90, 320
mm v[92:95],v[220:223],v[168:171],v[92:95]
rt v[212:213],v242 offset:42240
rt v[214:215],v242 offset:43264
mm v[60:63],v[220:223],v[164:167],v[60:63]
ds_write_b128 v238, v[248:251] offset:6144
mm v[60:63],v[216:219],v[180:183],v[60:63]
st a[136:139],v255,s[48:51],s18 offen offset:1024 sc1
mm v[28:31],v[216:219],v[176:179],v[28:31]
wt vmcnt(14) lgkmcnt(5)
mm v[28:31],v[220:223],v[160:163],v[28:31]
rt v[216:217],v243 offset:33792
rt v[218:219],v243 offset:34816
mm v[0:3],v[192:195],a[96:99],v[0:3]
ar v224,a144
ar v225,a145
mm v[0:3],v[196:199],a[112:115],v[0:3]
ld v[160:163],v244,s[52:55],s91 offen offset:0 sc1
mm v[32:35],v[196:199],a[116:119],v[32:35]
ar v226,a146
ar v227,a147
mm v[32:35],v[192:195],a[100:103],v[32:35]
rt v[220:221],v243 offset:42240
rt v[222:223],v243 offset:43264
mm v[64:67],v[192:195],a[104:107],v[64:67]
ar v228,a148
ar v229,a149
mm v[64:67],v[196:199],v[140:143],v[64:67]
ld v[164:167],v245,s[52:55],s91 offen offset:0 sc1
mm v[96:99],v[196:199],v[128:131],v[96:99]
ar v230,a150
ar v231,a151
mm v[96:99],v[192:195],a[108:111],v[96:99]
rt v[196:197],v240 offset:33920
rt v[198:199],v240 offset:34944
mm v[100:103],v[200:203],a[108:111],v[100:103]
s_add_u32 m0, s83, 19072
cv v248,v224,v225
mm v[100:103],v[204:207],v[128:131],v[100:103]
ld v234,s[44:47],s80 offen lds
mm v[68:71],v[204:207],v[140:143],v[68:71]
cv v249,v226,v227
cv v250,v228,v229
mm v[68:71],v[200:203],a[104:107],v[68:71]
rt v[192:193],v240 offset:42368
rt v[194:195],v240 offset:43392
mm v[36:39],v[200:203],a[100:103],v[36:39]
cv v251,v230,v231
aw a144,v248
mm v[36:39],v[204:207],a[116:119],v[36:39]
ld v[168:171],v246,s[52:55],s91 offen offset:0 sc1
mm v[4:7],v[204:207],a[112:115],v[4:7]
wt lgkmcnt(4)
aw a145,v249
mm v[4:7],v[200:203],a[96:99],v[4:7]
rt v[204:205],v241 offset:33920
rt v[206:207],v241 offset:34944
mm v[8:11],v[208:211],a[96:99],v[8:11]
s_add_u32 m0, s83, 20096
aw a146,v250
mm v[8:11],v[212:215],a[112:115],v[8:11]
ld v235,s[44:47],s80 offen lds
mm v[40:43],v[212:215],a[116:119],v[40:43]
s_add_u32 s80, s80, 524288
aw a147,v251
mm v[40:43],v[208:211],a[100:103],v[40:43]
rt v[200:201],v241 offset:42368
rt v[202:203],v241 offset:43392
mm v[72:75],v[208:211],a[104:107],v[72:75]
s_cmp_eq_u32 s3, 3
s_cselect_b32 s95, s97, s95
s_cselect_b64 s[36:37], s[34:35], s[36:37]
s_cselect_b32 s21, 1, s21
s_cselect_b32 s92, 576, s92
mm v[72:75],v[212:215],v[140:143],v[72:75]
ld v[172:175],v247,s[52:55],s91 offen offset:0 sc1
mm v[104:107],v[212:215],v[128:131],v[104:107]
s_lshl_b32 s21, s21, 10
s_and_b32 s7, s93, 1
mm v[104:107],v[208:211],a[108:111],v[104:107]
rt v[212:213],v242 offset:33920
rt v[214:215],v242 offset:34944
mm v[108:111],v[216:219],a[108:111],v[108:111]
s_cmp_eq_u32 s75, 1
s_cselect_b32 s97, s90, s92
s_cselect_b32 s92, s92, s90
mm v[108:111],v[220:223],v[128:131],v[108:111]
.La5_LW1_10:
s_cbranch_execz .La5_LW1_14
s_cmp_eq_u32 s75, s7
s_cselect_b32 s92, s92, 0
mm v[76:79],v[220:223],v[140:143],v[76:79]
s_mul_i32 s92, s92, s21
s_add_u32 s95, s95, s92
mm v[76:79],v[216:219],a[104:107],v[76:79]
rt v[208:209],v242 offset:42368
rt v[210:211],v242 offset:43392
mm v[44:47],v[216:219],a[100:103],v[44:47]
ds_write_b128 v238, v[248:251] offset:7168
mm v[44:47],v[220:223],a[116:119],v[44:47]
st a[140:143],v255,s[48:51],s18 offen offset:2048 sc1
mm v[12:15],v[220:223],a[112:115],v[12:15]
wt lgkmcnt(5)
s_mul_i32 s97, s97, s21
mm v[12:15],v[216:219],a[96:99],v[12:15]
rt v[220:221],v243 offset:33920
rt v[222:223],v243 offset:34944
mm v[16:19],v[196:199],a[96:99],v[16:19]
ar v224,a232
ar v225,a233
mm v[16:19],v[192:195],a[112:115],v[16:19]
ld v[176:179],v244,s[52:55],s91 offen offset:1024 sc1
mm v[48:51],v[192:195],a[116:119],v[48:51]
ar v226,a234
ar v227,a235
mm v[48:51],v[196:199],a[100:103],v[48:51]
rt v[216:217],v243 offset:42368
rt v[218:219],v243 offset:43392
mm v[80:83],v[196:199],a[104:107],v[80:83]
ar v228,a236
ar v229,a237
mm v[80:83],v[192:195],v[140:143],v[80:83]
ld v[180:183],v245,s[52:55],s91 offen offset:1024 sc1
mm v[112:115],v[192:195],v[128:131],v[112:115]
ar v230,a238
ar v231,a239
mm v[112:115],v[196:199],a[108:111],v[112:115]
cv v248,v224,v225
cv v249,v226,v227
mm v[116:119],v[204:207],a[108:111],v[116:119]
wt lgkmcnt(0)
s_barrier
mm v[116:119],v[200:203],v[128:131],v[116:119]
ld v[184:187],v246,s[52:55],s91 offen offset:1024 sc1
mm v[84:87],v[200:203],v[140:143],v[84:87]
rt v[192:193],v240 offset:50688
rt v[194:195],v240 offset:51712
mm v[84:87],v[204:207],a[104:107],v[84:87]
rt v[196:197],v240 offset:59136
rt v[198:199],v240 offset:60160
mm v[52:55],v[204:207],a[100:103],v[52:55]
s_add_u32 m0, s83, 33792
cv v250,v228,v229
mm v[52:55],v[200:203],a[116:119],v[52:55]
ld v232,s[44:47],s80 offen lds
mm v[20:23],v[200:203],a[112:115],v[20:23]
cv v251,v230,v231
aw a148,v248
mm v[20:23],v[204:207],a[96:99],v[20:23]
rt v[200:201],v241 offset:50688
rt v[202:203],v241 offset:51712
mm v[24:27],v[212:215],a[96:99],v[24:27]
aw a149,v249
aw a150,v250
mm v[24:27],v[208:211],a[112:115],v[24:27]
ld v[188:191],v247,s[52:55],s91 offen offset:1024 sc1
mm v[56:59],v[208:211],a[116:119],v[56:59]
aw a151,v251
s_mul_i32 s90, s97, s7
mm v[56:59],v[212:215],a[100:103],v[56:59]
rt v[204:205],v241 offset:59136
rt v[206:207],v241 offset:60160
mm v[88:91],v[212:215],a[104:107],v[88:91]
s_add_u32 m0, s83, 34816
s_sub_u32 s97, s97, s21
mm v[88:91],v[208:211],v[140:143],v[88:91]
ld v233,s[44:47],s80 offen lds
mm v[120:123],v[208:211],v[128:131],v[120:123]
s_add_u32 s97, s97, 896
mm v[120:123],v[212:215],a[108:111],v[120:123]
rt v[208:209],v242 offset:50688
rt v[210:211],v242 offset:51712
mm v[124:127],v[220:223],a[108:111],v[124:127]
s_add_u32 s36, s36, s95
s_addc_u32 s37, s37, 0
mm v[124:127],v[216:219],v[128:131],v[124:127]
s_and_b32 s37, s37, 0xffff
s_mov_b32 s38, s97
mm v[92:95],v[216:219],v[140:143],v[92:95]
s_mov_b32 s39, 0x20000
s_lshl_b32 s93, s96, 6
mm v[92:95],v[220:223],a[104:107],v[92:95]
rt v[212:213],v242 offset:59136
rt v[214:215],v242 offset:60160
mm v[60:63],v[220:223],a[100:103],v[60:63]
ds_write_b128 v238, v[248:251] offset:32768
mm v[60:63],v[216:219],a[116:119],v[60:63]
s_mul_i32 s95, s94, 7332928
mm v[28:31],v[216:219],a[112:115],v[28:31]
wt vmcnt(15) lgkmcnt(5)
mm v[28:31],v[220:223],a[96:99],v[28:31]
rt v[216:217],v243 offset:50688
rt v[218:219],v243 offset:51712
mm v[0:3],v[192:195],v[132:135],v[0:3]
ar v224,a152
ar v225,a153
mm v[0:3],v[196:199],v[152:155],v[0:3]
ld v[128:131],v244,s[52:55],s91 offen offset:2048 sc1
mm v[32:35],v[196:199],v[156:159],v[32:35]
ar v226,a154
ar v227,a155
mm v[32:35],v[192:195],v[136:139],v[32:35]
rt v[220:221],v243 offset:59136
rt v[222:223],v243 offset:60160
mm v[64:67],v[192:195],v[144:147],v[64:67]
ar v228,a156
ar v229,a157
mm v[64:67],v[196:199],a[120:123],v[64:67]
ld v[140:143],v245,s[52:55],s91 offen offset:2048 sc1
mm v[96:99],v[196:199],a[124:127],v[96:99]
ar v230,a158
ar v231,a159
mm v[96:99],v[192:195],v[148:151],v[96:99]
rt v[196:197],v240 offset:50816
rt v[198:199],v240 offset:51840
mm v[100:103],v[200:203],v[148:151],v[100:103]
s_add_u32 m0, s83, 35968
cv v248,v224,v225
mm v[100:103],v[204:207],a[124:127],v[100:103]
ld v234,s[44:47],s80 offen lds
mm v[68:71],v[204:207],a[120:123],v[68:71]
cv v249,v226,v227
cv v250,v228,v229
mm v[68:71],v[200:203],v[144:147],v[68:71]
rt v[192:193],v240 offset:59264
rt v[194:195],v240 offset:60288
mm v[36:39],v[200:203],v[136:139],v[36:39]
cv v251,v230,v231
aw a152,v248
mm v[36:39],v[204:207],v[156:159],v[36:39]
ld a[96:99],v246,s[52:55],s91 offen offset:2048 sc1
mm v[4:7],v[204:207],v[152:155],v[4:7]
wt lgkmcnt(4)
aw a153,v249
mm v[4:7],v[200:203],v[132:135],v[4:7]
rt v[204:205],v241 offset:50816
rt v[206:207],v241 offset:51840
mm v[8:11],v[208:211],v[132:135],v[8:11]
s_add_u32 m0, s83, 36992
aw a154,v250
mm v[8:11],v[212:215],v[152:155],v[8:11]
ld v235,s[44:47],s80 offen lds
mm v[40:43],v[212:215],v[156:159],v[40:43]
s_add_u32 s80, s80, 524288
aw a155,v251
mm v[40:43],v[208:211],v[136:139],v[40:43]
rt v[200:201],v241 offset:59264
rt v[202:203],v241 offset:60288
mm v[72:75],v[208:211],v[144:147],v[72:75]
s_cmp_eq_u32 s77, 0
s_cselect_b64 s[40:41], s[30:31], s[32:33]
mm v[72:75],v[212:215],a[120:123],v[72:75]
ld a[100:103],v247,s[52:55],s91 offen offset:2048 sc1
mm v[104:107],v[212:215],a[124:127],v[104:107]
s_add_u32 s40, s40, s93
s_addc_u32 s41, s41, 0
mm v[104:107],v[208:211],v[148:151],v[104:107]
rt v[212:213],v242 offset:50816
rt v[214:215],v242 offset:51840
mm v[108:111],v[216:219],v[148:151],v[108:111]
s_and_b32 s41, s41, 0xffff
s_mov_b32 s42, s95
mm v[108:111],v[220:223],a[124:127],v[108:111]
s_mov_b32 s43, 0x20000
s_lshr_b32 s92, s72, 1
mm v[76:79],v[220:223],a[120:123],v[76:79]
s_and_b32 s92, s92, 4
s_xor_b32 s92, s92, s72
mm v[76:79],v[216:219],v[144:147],v[76:79]
rt v[208:209],v242 offset:59264
rt v[210:211],v242 offset:60288
mm v[44:47],v[216:219],v[136:139],v[44:47]
ds_write_b128 v238, v[248:251] offset:33792
mm v[44:47],v[220:223],v[156:159],v[44:47]
st a[144:147],v255,s[48:51],s18 offen offset:3072 sc1
mm v[12:15],v[220:223],v[152:155],v[12:15]
wt lgkmcnt(5)
s_xor_b32 s92, s92, 1
mm v[12:15],v[216:219],v[132:135],v[12:15]
rt v[220:221],v243 offset:50816
rt v[222:223],v243 offset:51840
mm v[16:19],v[196:199],v[132:135],v[16:19]
ar v224,a208
ar v225,a209
mm v[16:19],v[192:195],v[152:155],v[16:19]
ld a[104:107],v244,s[52:55],s91 offen offset:3072 sc1
mm v[48:51],v[192:195],v[156:159],v[48:51]
ar v226,a210
ar v227,a211
mm v[48:51],v[196:199],v[136:139],v[48:51]
rt v[216:217],v243 offset:59264
rt v[218:219],v243 offset:60288
mm v[80:83],v[196:199],v[144:147],v[80:83]
ar v228,a212
ar v229,a213
mm v[80:83],v[192:195],a[120:123],v[80:83]
ld a[108:111],v245,s[52:55],s91 offen offset:3072 sc1
mm v[112:115],v[192:195],a[124:127],v[112:115]
ar v230,a214
ar v231,a215
mm v[112:115],v[196:199],v[148:151],v[112:115]
cv v248,v224,v225
cv v249,v226,v227
mm v[116:119],v[204:207],v[148:151],v[116:119]
wt lgkmcnt(0)
s_barrier
mm v[116:119],v[200:203],a[124:127],v[116:119]
ld a[112:115],v246,s[52:55],s91 offen offset:3072 sc1
mm v[84:87],v[200:203],a[120:123],v[84:87]
rt v[192:193],v240 offset:0
rt v[194:195],v240 offset:1024
mm v[84:87],v[204:207],v[144:147],v[84:87]
rt v[196:197],v240 offset:8448
rt v[198:199],v240 offset:9472
mm v[52:55],v[204:207],v[136:139],v[52:55]
s_add_u32 m0, s83, 50688
cv v250,v228,v229
mm v[52:55],v[200:203],v[156:159],v[52:55]
ld v232,s[44:47],s80 offen lds
mm v[20:23],v[200:203],v[152:155],v[20:23]
cv v251,v230,v231
aw a156,v248
mm v[20:23],v[204:207],v[132:135],v[20:23]
rt v[200:201],v241 offset:0
rt v[202:203],v241 offset:1024
mm v[24:27],v[212:215],v[132:135],v[24:27]
aw a157,v249
aw a158,v250
mm v[24:27],v[208:211],v[152:155],v[24:27]
ld a[116:119],v247,s[52:55],s91 offen offset:3072 sc1
mm v[56:59],v[208:211],v[156:159],v[56:59]
s_add_u32 s91, s91, 4096
aw a159,v251
mm v[56:59],v[212:215],v[136:139],v[56:59]
rt v[204:205],v241 offset:8448
rt v[206:207],v241 offset:9472
mm v[88:91],v[212:215],v[144:147],v[88:91]
s_add_u32 m0, s83, 51712
s_and_b32 s101, s92, 7
mm v[88:91],v[208:211],a[120:123],v[88:91]
ld v233,s[44:47],s80 offen lds
mm v[120:123],v[208:211],a[124:127],v[120:123]
s_lshl_b32 s92, s101, 8
s_sub_u32 s93, 8388608, s92
mm v[120:123],v[212:215],v[148:151],v[120:123]
rt v[208:209],v242 offset:0
rt v[210:211],v242 offset:1024
mm v[124:127],v[220:223],v[148:151],v[124:127]
s_add_u32 s56, s28, s92
s_addc_u32 s57, s29, 0
mm v[124:127],v[216:219],a[124:127],v[124:127]
s_and_b32 s57, s57, 0xffff
s_mov_b32 s58, s93
mm v[92:95],v[216:219],a[120:123],v[92:95]
s_mov_b32 s18, 32768
s_mov_b32 s59, 0x20000
mm v[92:95],v[220:223],v[144:147],v[92:95]
rt v[212:213],v242 offset:8448
rt v[214:215],v242 offset:9472
mm v[60:63],v[220:223],v[136:139],v[60:63]
ds_write_b128 v238, v[248:251] offset:34816
mm v[60:63],v[216:219],v[156:159],v[60:63]
st a[148:151],v255,s[48:51],s18 offen offset:0 sc1
mm v[28:31],v[216:219],v[152:155],v[28:31]
wt vmcnt(15) lgkmcnt(5)
s_lshl_b32 s93, s96, 16
mm v[28:31],v[220:223],v[132:135],v[28:31]
rt v[216:217],v243 offset:0
rt v[218:219],v243 offset:1024
mm v[0:3],v[192:195],v[160:163],v[0:3]
ar v224,a160
ar v225,a161
mm v[0:3],v[196:199],v[176:179],v[0:3]
ld v[132:135],v244,s[52:55],s91 offen offset:0 sc1
mm v[32:35],v[196:199],v[180:183],v[32:35]
ar v226,a162
ar v227,a163
mm v[32:35],v[192:195],v[164:167],v[32:35]
rt v[220:221],v243 offset:8448
rt v[222:223],v243 offset:9472
mm v[64:67],v[192:195],v[168:171],v[64:67]
ar v228,a164
ar v229,a165
mm v[64:67],v[196:199],v[184:187],v[64:67]
ld v[136:139],v245,s[52:55],s91 offen offset:0 sc1
mm v[96:99],v[196:199],v[188:191],v[96:99]
ar v230,a166
ar v231,a167
mm v[96:99],v[192:195],v[172:175],v[96:99]
rt v[196:197],v240 offset:128
rt v[198:199],v240 offset:1152
mm v[100:103],v[200:203],v[172:175],v[100:103]
s_add_u32 m0, s83, 52864
cv v248,v224,v225
mm v[100:103],v[204:207],v[188:191],v[100:103]
ld v234,s[44:47],s80 offen lds
mm v[68:71],v[204:207],v[184:187],v[68:71]
cv v249,v226,v227
cv v250,v228,v229
mm v[68:71],v[200:203],v[168:171],v[68:71]
rt v[192:193],v240 offset:8576
rt v[194:195],v240 offset:9600
mm v[36:39],v[200:203],v[164:167],v[36:39]
cv v251,v230,v231
aw a160,v248
mm v[36:39],v[204:207],v[180:183],v[36:39]
ld v[144:147],v246,s[52:55],s91 offen offset:0 sc1
mm v[4:7],v[204:207],v[176:179],v[4:7]
wt lgkmcnt(4)
aw a161,v249
mm v[4:7],v[200:203],v[160:163],v[4:7]
rt v[204:205],v241 offset:128
rt v[206:207],v241 offset:1152
mm v[8:11],v[208:211],v[160:163],v[8:11]
s_add_u32 m0, s83, 53888
aw a162,v250
mm v[8:11],v[212:215],v[176:179],v[8:11]
ld v235,s[44:47],s80 offen lds
mm v[40:43],v[212:215],v[180:183],v[40:43]
s_add_u32 s80, s80, 524288
aw a163,v251
mm v[40:43],v[208:211],v[164:167],v[40:43]
rt v[200:201],v241 offset:8576
rt v[202:203],v241 offset:9600
mm v[72:75],v[208:211],v[168:171],v[72:75]
s_lshl_b32 s95, s94, 16
mm v[72:75],v[212:215],v[184:187],v[72:75]
ld v[148:151],v247,s[52:55],s91 offen offset:0 sc1
mm v[104:107],v[212:215],v[188:191],v[104:107]
s_add_u32 s28, s34, s93
s_addc_u32 s29, s35, 0
mm v[104:107],v[208:211],v[172:175],v[104:107]
rt v[212:213],v242 offset:128
rt v[214:215],v242 offset:1152
mm v[108:111],v[216:219],v[172:175],v[108:111]
s_and_b32 s29, s29, 0xffff
s_mov_b32 s30, s95
mm v[108:111],v[220:223],v[188:191],v[108:111]
s_mov_b32 s31, 0x20000
s_lshl_b32 s93, s101, 12
mm v[76:79],v[220:223],v[184:187],v[76:79]
s_add_u32 s92, s93, 71303168
s_sub_u32 s93, 16777216, s93
mm v[76:79],v[216:219],v[168:171],v[76:79]
rt v[208:209],v242 offset:8576
rt v[210:211],v242 offset:9600
mm v[44:47],v[216:219],v[164:167],v[44:47]
ds_write_b128 v238, v[248:251] offset:35840
mm v[44:47],v[220:223],v[180:183],v[44:47]
st a[152:155],v255,s[48:51],s18 offen offset:1024 sc1
mm v[12:15],v[220:223],v[176:179],v[12:15]
wt lgkmcnt(5)
mm v[12:15],v[216:219],v[160:163],v[12:15]
rt v[220:221],v243 offset:128
rt v[222:223],v243 offset:1152
mm v[16:19],v[196:199],v[160:163],v[16:19]
ar v224,a216
ar v225,a217
mm v[16:19],v[192:195],v[176:179],v[16:19]
ld v[152:155],v244,s[52:55],s91 offen offset:1024 sc1
mm v[48:51],v[192:195],v[180:183],v[48:51]
ar v226,a218
ar v227,a219
mm v[48:51],v[196:199],v[164:167],v[48:51]
rt v[216:217],v243 offset:8576
rt v[218:219],v243 offset:9600
mm v[80:83],v[196:199],v[168:171],v[80:83]
ar v228,a220
ar v229,a221
mm v[80:83],v[192:195],v[184:187],v[80:83]
ld v[156:159],v245,s[52:55],s91 offen offset:1024 sc1
mm v[112:115],v[192:195],v[188:191],v[112:115]
ar v230,a222
ar v231,a223
mm v[112:115],v[196:199],v[172:175],v[112:115]
cv v248,v224,v225
cv v249,v226,v227
mm v[116:119],v[204:207],v[172:175],v[116:119]
wt lgkmcnt(0)
s_barrier
mm v[116:119],v[200:203],v[188:191],v[116:119]
ld a[120:123],v246,s[52:55],s91 offen offset:1024 sc1
mm v[84:87],v[200:203],v[184:187],v[84:87]
rt v[192:193],v240 offset:16896
rt v[194:195],v240 offset:17920
mm v[84:87],v[204:207],v[168:171],v[84:87]
rt v[196:197],v240 offset:25344
rt v[198:199],v240 offset:26368
mm v[52:55],v[204:207],v[164:167],v[52:55]
s_add_u32 m0, s83, 0
cv v250,v228,v229
mm v[52:55],v[200:203],v[180:183],v[52:55]
ld v232,s[44:47],s80 offen lds
mm v[20:23],v[200:203],v[176:179],v[20:23]
cv v251,v230,v231
aw a164,v248
mm v[20:23],v[204:207],v[160:163],v[20:23]
rt v[200:201],v241 offset:16896
rt v[202:203],v241 offset:17920
mm v[24:27],v[212:215],v[160:163],v[24:27]
aw a165,v249
aw a166,v250
mm v[24:27],v[208:211],v[176:179],v[24:27]
ld a[124:127],v247,s[52:55],s91 offen offset:1024 sc1
mm v[56:59],v[208:211],v[180:183],v[56:59]
aw a167,v251
mm v[56:59],v[212:215],v[164:167],v[56:59]
rt v[204:205],v241 offset:25344
rt v[206:207],v241 offset:26368
mm v[88:91],v[212:215],v[168:171],v[88:91]
s_add_u32 m0, s83, 1024
mm v[88:91],v[208:211],v[184:187],v[88:91]
ld v233,s[44:47],s80 offen lds
mm v[120:123],v[208:211],v[188:191],v[120:123]
s_add_u32 s32, s26, s92
s_addc_u32 s33, s27, 0
mm v[120:123],v[212:215],v[172:175],v[120:123]
rt v[208:209],v242 offset:16896
rt v[210:211],v242 offset:17920
mm v[124:127],v[220:223],v[172:175],v[124:127]
s_and_b32 s33, s33, 0xffff
s_mov_b32 s34, s93
mm v[124:127],v[216:219],v[188:191],v[124:127]
s_mov_b32 s35, 0x20000
s_lshl2_add_u32 s92, s72, s74
mm v[92:95],v[216:219],v[184:187],v[92:95]
s_lshl2_add_u32 s92, s92, s3
s_lshl_b32 s88, s92, 8
mm v[92:95],v[220:223],v[168:171],v[92:95]
rt v[212:213],v242 offset:25344
rt v[214:215],v242 offset:26368
mm v[60:63],v[220:223],v[164:167],v[60:63]
ds_write_b128 v238, v[248:251] offset:36864
mm v[60:63],v[216:219],v[180:183],v[60:63]
st a[156:159],v255,s[48:51],s18 offen offset:2048 sc1
mm v[28:31],v[216:219],v[176:179],v[28:31]
wt vmcnt(16) lgkmcnt(5)
s_mul_i32 s93, s92, 17408
mm v[28:31],v[220:223],v[160:163],v[28:31]
rt v[216:217],v243 offset:16896
rt v[218:219],v243 offset:17920
mm v[0:3],v[192:195],v[128:131],v[0:3]
ar v224,a168
ar v225,a169
mm v[0:3],v[196:199],a[104:107],v[0:3]
ld v[160:163],v244,s[52:55],s91 offen offset:2048 sc1
mm v[32:35],v[196:199],a[108:111],v[32:35]
ar v226,a170
ar v227,a171
mm v[32:35],v[192:195],v[140:143],v[32:35]
rt v[220:221],v243 offset:25344
rt v[222:223],v243 offset:26368
mm v[64:67],v[192:195],a[96:99],v[64:67]
ar v228,a172
ar v229,a173
mm v[64:67],v[196:199],a[112:115],v[64:67]
ld v[164:167],v245,s[52:55],s91 offen offset:2048 sc1
mm v[96:99],v[196:199],a[116:119],v[96:99]
ar v230,a174
ar v231,a175
mm v[96:99],v[192:195],a[100:103],v[96:99]
rt v[196:197],v240 offset:17024
rt v[198:199],v240 offset:18048
mm v[100:103],v[200:203],a[100:103],v[100:103]
s_add_u32 m0, s83, 2176
cv v248,v224,v225
mm v[100:103],v[204:207],a[116:119],v[100:103]
ld v234,s[44:47],s80 offen lds
mm v[68:71],v[204:207],a[112:115],v[68:71]
cv v249,v226,v227
cv v250,v228,v229
mm v[68:71],v[200:203],a[96:99],v[68:71]
rt v[192:193],v240 offset:25472
rt v[194:195],v240 offset:26496
mm v[36:39],v[200:203],v[140:143],v[36:39]
cv v251,v230,v231
aw a168,v248
mm v[36:39],v[204:207],a[108:111],v[36:39]
ld v[168:171],v246,s[52:55],s91 offen offset:2048 sc1
mm v[4:7],v[204:207],a[104:107],v[4:7]
wt lgkmcnt(4)
aw a169,v249
mm v[4:7],v[200:203],v[128:131],v[4:7]
rt v[204:205],v241 offset:17024
rt v[206:207],v241 offset:18048
mm v[8:11],v[208:211],v[128:131],v[8:11]
s_add_u32 m0, s83, 3200
aw a170,v250
mm v[8:11],v[212:215],a[104:107],v[8:11]
ld v235,s[44:47],s80 offen lds
mm v[40:43],v[212:215],a[108:111],v[40:43]
s_add_u32 s80, s80, 524288
aw a171,v251
mm v[40:43],v[208:211],v[140:143],v[40:43]
rt v[200:201],v241 offset:25472
rt v[202:203],v241 offset:26496
mm v[72:75],v[208:211],a[96:99],v[72:75]
s_lshl_b32 s92, s92, 14
s_add_u32 s92, s92, 4194304
mm v[72:75],v[212:215],a[112:115],v[72:75]
ld v[172:175],v247,s[52:55],s91 offen offset:2048 sc1
mm v[104:107],v[212:215],a[116:119],v[104:107]
s_add_u32 s86, s26, s92
s_addc_u32 s87, s27, 0
mm v[104:107],v[208:211],a[100:103],v[104:107]
rt v[212:213],v242 offset:17024
rt v[214:215],v242 offset:18048
mm v[108:111],v[216:219],a[100:103],v[108:111]
s_and_b32 s87, s87, 0xffff
s_add_u32 s93, s93, 150994944
mm v[108:111],v[220:223],a[116:119],v[108:111]
s_add_u32 s68, s26, s93
s_addc_u32 s69, s27, 0
mm v[76:79],v[220:223],a[112:115],v[76:79]
s_and_b32 s69, s69, 0xffff
s_mov_b32 s70, 17408
mm v[76:79],v[216:219],a[96:99],v[76:79]
rt v[208:209],v242 offset:25472
rt v[210:211],v242 offset:26496
mm v[44:47],v[216:219],v[140:143],v[44:47]
ds_write_b128 v238, v[248:251] offset:37888
mm v[44:47],v[220:223],a[108:111],v[44:47]
st a[160:163],v255,s[48:51],s18 offen offset:3072 sc1
mm v[12:15],v[220:223],a[104:107],v[12:15]
wt lgkmcnt(5)
s_mov_b32 s96, 153158689
mm v[12:15],v[216:219],v[128:131],v[12:15]
rt v[220:221],v243 offset:17024
rt v[222:223],v243 offset:18048
mm v[16:19],v[196:199],v[128:131],v[16:19]
.La5_LW1_11:
s_cbranch_execz .La5_LW1_15
ar v224,a192
ar v225,a193
mm v[16:19],v[192:195],a[104:107],v[16:19]
ld v[176:179],v244,s[52:55],s91 offen offset:3072 sc1
mm v[48:51],v[192:195],a[108:111],v[48:51]
ar v226,a194
ar v227,a195
mm v[48:51],v[196:199],v[140:143],v[48:51]
rt v[216:217],v243 offset:25472
rt v[218:219],v243 offset:26496
mm v[80:83],v[196:199],a[96:99],v[80:83]
ar v228,a196
ar v229,a197
mm v[80:83],v[192:195],a[112:115],v[80:83]
ld v[180:183],v245,s[52:55],s91 offen offset:3072 sc1
mm v[112:115],v[192:195],a[116:119],v[112:115]
ar v230,a198
ar v231,a199
mm v[112:115],v[196:199],a[100:103],v[112:115]
cv v248,v224,v225
cv v249,v226,v227
mm v[116:119],v[204:207],a[100:103],v[116:119]
wt lgkmcnt(0)
s_barrier
mm v[116:119],v[200:203],a[116:119],v[116:119]
ld v[184:187],v246,s[52:55],s91 offen offset:3072 sc1
mm v[84:87],v[200:203],a[112:115],v[84:87]
rt v[192:193],v240 offset:33792
rt v[194:195],v240 offset:34816
mm v[84:87],v[204:207],a[96:99],v[84:87]
rt v[196:197],v240 offset:42240
rt v[198:199],v240 offset:43264
mm v[52:55],v[204:207],v[140:143],v[52:55]
s_add_u32 m0, s83, 16896
cv v250,v228,v229
mm v[52:55],v[200:203],a[108:111],v[52:55]
ld v232,s[44:47],s80 offen lds
mm v[20:23],v[200:203],a[104:107],v[20:23]
cv v251,v230,v231
aw a172,v248
mm v[20:23],v[204:207],v[128:131],v[20:23]
rt v[200:201],v241 offset:33792
rt v[202:203],v241 offset:34816
mm v[24:27],v[212:215],v[128:131],v[24:27]
aw a173,v249
aw a174,v250
mm v[24:27],v[208:211],a[104:107],v[24:27]
ld v[188:191],v247,s[52:55],s91 offen offset:3072 sc1
mm v[56:59],v[208:211],a[108:111],v[56:59]
s_add_u32 s91, s91, 4096
aw a175,v251
mm v[56:59],v[212:215],v[140:143],v[56:59]
rt v[204:205],v241 offset:42240
rt v[206:207],v241 offset:43264
mm v[88:91],v[212:215],a[96:99],v[88:91]
s_add_u32 m0, s83, 17920
s_mov_b32 s97, 371265825
mm v[88:91],v[208:211],a[112:115],v[88:91]
ld v233,s[44:47],s80 offen lds
mm v[120:123],v[208:211],a[116:119],v[120:123]
s_lshl_b32 s95, s73, 3
s_lshr_b64 s[96:97], s[96:97], s95
mm v[120:123],v[212:215],a[100:103],v[120:123]
rt v[208:209],v242 offset:33792
rt v[210:211],v242 offset:34816
mm v[124:127],v[220:223],a[100:103],v[124:127]
s_and_b32 s82, s96, 255
s_lshl_b64 s[98:99], 1, s82
mm v[124:127],v[216:219],a[116:119],v[124:127]
s_lshl_b32 s96, s75, 22
s_or_b32 s95, s98, s96
mm v[92:95],v[216:219],a[112:115],v[92:95]
s_cmp_eq_u32 s73, 1
s_cselect_b32 s96, 12, 4
s_cselect_b32 s79, 0, 0xff000000
mm v[92:95],v[220:223],a[96:99],v[92:95]
rt v[212:213],v242 offset:42240
rt v[214:215],v242 offset:43264
mm v[60:63],v[220:223],v[140:143],v[60:63]
ds_write_b128 v238, v[248:251] offset:38912
mm v[60:63],v[216:219],a[108:111],v[60:63]
s_or_b32 s98, s95, s96
mm v[28:31],v[216:219],a[104:107],v[28:31]
wt vmcnt(15) lgkmcnt(5)
mm v[28:31],v[220:223],v[128:131],v[28:31]
rt v[216:217],v243 offset:33792
rt v[218:219],v243 offset:34816
mm v[0:3],v[192:195],v[132:135],v[0:3]
ar v224,a176
ar v225,a177
mm v[0:3],v[196:199],v[152:155],v[0:3]
ld v[128:131],v244,s[52:55],s91 offen offset:0 sc1
mm v[32:35],v[196:199],v[156:159],v[32:35]
ar v226,a178
ar v227,a179
mm v[32:35],v[192:195],v[136:139],v[32:35]
rt v[220:221],v243 offset:42240
rt v[222:223],v243 offset:43264
mm v[64:67],v[192:195],v[144:147],v[64:67]
ar v228,a180
ar v229,a181
mm v[64:67],v[196:199],a[120:123],v[64:67]
ld v[140:143],v245,s[52:55],s91 offen offset:0 sc1
mm v[96:99],v[196:199],a[124:127],v[96:99]
ar v230,a182
ar v231,a183
mm v[96:99],v[192:195],v[148:151],v[96:99]
rt v[196:197],v240 offset:33920
rt v[198:199],v240 offset:34944
mm v[100:103],v[200:203],v[148:151],v[100:103]
s_add_u32 m0, s83, 19072
cv v248,v224,v225
mm v[100:103],v[204:207],a[124:127],v[100:103]
ld v234,s[44:47],s80 offen lds
mm v[68:71],v[204:207],a[120:123],v[68:71]
cv v249,v226,v227
cv v250,v228,v229
mm v[68:71],v[200:203],v[144:147],v[68:71]
rt v[192:193],v240 offset:42368
rt v[194:195],v240 offset:43392
mm v[36:39],v[200:203],v[136:139],v[36:39]
cv v251,v230,v231
aw a176,v248
mm v[36:39],v[204:207],v[156:159],v[36:39]
ld a[96:99],v246,s[52:55],s91 offen offset:0 sc1
mm v[4:7],v[204:207],v[152:155],v[4:7]
wt lgkmcnt(4)
aw a177,v249
mm v[4:7],v[200:203],v[132:135],v[4:7]
rt v[204:205],v241 offset:33920
rt v[206:207],v241 offset:34944
mm v[8:11],v[208:211],v[132:135],v[8:11]
s_add_u32 m0, s83, 20096
aw a178,v250
mm v[8:11],v[212:215],v[152:155],v[8:11]
ld v235,s[44:47],s80 offen lds
mm v[40:43],v[212:215],v[156:159],v[40:43]
s_add_u32 s80, s80, 524288
aw a179,v251
mm v[40:43],v[208:211],v[136:139],v[40:43]
rt v[200:201],v241 offset:42368
rt v[202:203],v241 offset:43392
mm v[72:75],v[208:211],v[144:147],v[72:75]
s_cmp_eq_u32 s74, 3
s_cselect_b32 s23, 3, 0
s_cselect_b32 s22, 0, s75
mm v[72:75],v[212:215],a[120:123],v[72:75]
ld a[100:103],v247,s[52:55],s91 offen offset:0 sc1
mm v[104:107],v[212:215],a[124:127],v[104:107]
s_lshl_b32 s95, s76, 5
s_add_u32 s95, s95, s3
mm v[104:107],v[208:211],v[148:151],v[104:107]
rt v[212:213],v242 offset:33920
rt v[214:215],v242 offset:34944
mm v[108:111],v[216:219],v[148:151],v[108:111]
s_lshl_b32 s95, s95, 5
s_add_u32 s95, s95, s72
mm v[108:111],v[220:223],a[124:127],v[108:111]
s_lshl_b32 s95, s95, 3
s_add_u32 s6, s95, 1048576
mm v[76:79],v[220:223],a[120:123],v[76:79]
s_mov_b32 s18, 36864
v_and_b32 v239, 0x70, v255
mm v[76:79],v[216:219],v[144:147],v[76:79]
rt v[208:209],v242 offset:42368
rt v[210:211],v242 offset:43392
mm v[44:47],v[216:219],v[136:139],v[44:47]
ds_write_b128 v238, v[248:251] offset:39936
mm v[44:47],v[220:223],v[156:159],v[44:47]
st a[164:167],v255,s[48:51],s18 offen offset:0 sc1
mm v[12:15],v[220:223],v[152:155],v[12:15]
wt lgkmcnt(5)
v_lshlrev_b32 v239, 6, v239
mm v[12:15],v[216:219],v[132:135],v[12:15]
rt v[220:221],v243 offset:33920
rt v[222:223],v243 offset:34944
mm v[16:19],v[196:199],v[132:135],v[16:19]
ar v224,a200
ar v225,a201
mm v[16:19],v[192:195],v[152:155],v[16:19]
ld a[104:107],v244,s[52:55],s91 offen offset:1024 sc1
mm v[48:51],v[192:195],v[156:159],v[48:51]
ar v226,a202
ar v227,a203
mm v[48:51],v[196:199],v[136:139],v[48:51]
rt v[216:217],v243 offset:42368
rt v[218:219],v243 offset:43392
mm v[80:83],v[196:199],v[144:147],v[80:83]
ar v228,a204
ar v229,a205
mm v[80:83],v[192:195],a[120:123],v[80:83]
ld a[108:111],v245,s[52:55],s91 offen offset:1024 sc1
mm v[112:115],v[192:195],a[124:127],v[112:115]
ar v230,a206
ar v231,a207
mm v[112:115],v[196:199],v[148:151],v[112:115]
cv v248,v224,v225
cv v249,v226,v227
mm v[116:119],v[204:207],v[148:151],v[116:119]
wt lgkmcnt(0)
s_barrier
mm v[116:119],v[200:203],a[124:127],v[116:119]
ld a[112:115],v246,s[52:55],s91 offen offset:1024 sc1
mm v[84:87],v[200:203],a[120:123],v[84:87]
rt v[192:193],v240 offset:50688
rt v[194:195],v240 offset:51712
mm v[84:87],v[204:207],v[144:147],v[84:87]
rt v[196:197],v240 offset:59136
rt v[198:199],v240 offset:60160
mm v[52:55],v[204:207],v[136:139],v[52:55]
s_add_u32 m0, s83, 33792
cv v250,v228,v229
mm v[52:55],v[200:203],v[156:159],v[52:55]
ld v232,s[44:47],s80 offen lds
mm v[20:23],v[200:203],v[152:155],v[20:23]
cv v251,v230,v231
aw a180,v248
mm v[20:23],v[204:207],v[132:135],v[20:23]
rt v[200:201],v241 offset:50688
rt v[202:203],v241 offset:51712
mm v[24:27],v[212:215],v[132:135],v[24:27]
aw a181,v249
aw a182,v250
mm v[24:27],v[208:211],v[152:155],v[24:27]
ld a[116:119],v247,s[52:55],s91 offen offset:1024 sc1
mm v[56:59],v[208:211],v[156:159],v[56:59]
aw a183,v251
s_add_u32 s85, s85, 65536
mm v[56:59],v[212:215],v[136:139],v[56:59]
rt v[204:205],v241 offset:59136
rt v[206:207],v241 offset:60160
mm v[88:91],v[212:215],v[144:147],v[88:91]
s_add_u32 m0, s83, 34816
v_add_u32 v238, s85, v255
mm v[88:91],v[208:211],a[120:123],v[88:91]
ld v233,s[44:47],s80 offen lds
mm v[120:123],v[208:211],a[124:127],v[120:123]
s_mov_b32 s85, 0
aw a231,v239
mm v[120:123],v[212:215],v[148:151],v[120:123]
rt v[208:209],v242 offset:50688
rt v[210:211],v242 offset:51712
mm v[124:127],v[220:223],v[148:151],v[124:127]
s_mul_i32 s95, s75, 0
s_add_u32 s95, s95, 2
mm v[124:127],v[216:219],a[124:127],v[124:127]
s_sub_u32 s96, 4, s95
s_lshr_b32 s97, s2, 4
mm v[92:95],v[216:219],a[120:123],v[92:95]
s_mul_i32 s97, s97, s95
v_lshrrev_b32 v239, 7, v255
mm v[92:95],v[220:223],v[144:147],v[92:95]
rt v[212:213],v242 offset:59136
rt v[214:215],v242 offset:60160
mm v[60:63],v[220:223],v[136:139],v[60:63]
ds_write_b128 v238, v[248:251] offset:0
mm v[60:63],v[216:219],v[156:159],v[60:63]
st a[168:171],v255,s[48:51],s18 offen offset:1024 sc1
mm v[28:31],v[216:219],v[152:155],v[28:31]
wt vmcnt(15) lgkmcnt(5)
v_add_u32 v254, s97, v239
mm v[28:31],v[220:223],v[132:135],v[28:31]
rt v[216:217],v243 offset:50688
rt v[218:219],v243 offset:51712
mm v[0:3],v[192:195],v[160:163],v[0:3]
ar v224,a184
ar v225,a185
mm v[0:3],v[196:199],v[176:179],v[0:3]
ld v[132:135],v244,s[52:55],s91 offen offset:2048 sc1
mm v[32:35],v[196:199],v[180:183],v[32:35]
ar v226,a186
ar v227,a187
mm v[32:35],v[192:195],v[164:167],v[32:35]
rt v[220:221],v243 offset:59136
rt v[222:223],v243 offset:60160
mm v[64:67],v[192:195],v[168:171],v[64:67]
ar v228,a188
ar v229,a189
mm v[64:67],v[196:199],v[184:187],v[64:67]
ld v[136:139],v245,s[52:55],s91 offen offset:2048 sc1
mm v[96:99],v[196:199],v[188:191],v[96:99]
ar v230,a190
ar v231,a191
mm v[96:99],v[192:195],v[172:175],v[96:99]
rt v[196:197],v240 offset:50816
rt v[198:199],v240 offset:51840
mm v[100:103],v[200:203],v[172:175],v[100:103]
s_add_u32 m0, s83, 35968
cv v248,v224,v225
mm v[100:103],v[204:207],v[188:191],v[100:103]
ld v234,s[44:47],s80 offen lds
mm v[68:71],v[204:207],v[184:187],v[68:71]
cv v249,v226,v227
cv v250,v228,v229
mm v[68:71],v[200:203],v[168:171],v[68:71]
rt v[192:193],v240 offset:59264
rt v[194:195],v240 offset:60288
mm v[36:39],v[200:203],v[164:167],v[36:39]
cv v251,v230,v231
aw a184,v248
mm v[36:39],v[204:207],v[180:183],v[36:39]
ld v[144:147],v246,s[52:55],s91 offen offset:2048 sc1
mm v[4:7],v[204:207],v[176:179],v[4:7]
wt lgkmcnt(4)
aw a185,v249
mm v[4:7],v[200:203],v[160:163],v[4:7]
rt v[204:205],v241 offset:50816
rt v[206:207],v241 offset:51840
mm v[8:11],v[208:211],v[160:163],v[8:11]
s_add_u32 m0, s83, 36992
aw a186,v250
mm v[8:11],v[212:215],v[176:179],v[8:11]
ld v235,s[44:47],s80 offen lds
mm v[40:43],v[212:215],v[180:183],v[40:43]
s_add_u32 s80, s80, 524288
aw a187,v251
mm v[40:43],v[208:211],v[164:167],v[40:43]
rt v[200:201],v241 offset:59264
rt v[202:203],v241 offset:60288
mm v[72:75],v[208:211],v[168:171],v[72:75]
v_mul_u32_u24 v254, s21, v254
v_add_u32 v239, s96, v239
mm v[72:75],v[212:215],v[184:187],v[72:75]
ld v[148:151],v247,s[52:55],s91 offen offset:2048 sc1
mm v[104:107],v[212:215],v[188:191],v[104:107]
v_lshrrev_b32 v239, 2, v239
v_lshl_add_u32 v254, v239, 28, v254
mm v[104:107],v[208:211],v[172:175],v[104:107]
rt v[212:213],v242 offset:50816
rt v[214:215],v242 offset:51840
mm v[108:111],v[216:219],v[172:175],v[108:111]
v_and_b32 v239, 0x70, v255
v_lshl_add_u32 v254, v239, 3, v254
mm v[108:111],v[220:223],v[188:191],v[108:111]
s_lshl_b32 s96, s7, 5
s_sub_u32 s96, 16, s96
mm v[76:79],v[220:223],v[184:187],v[76:79]
s_mul_i32 s95, s95, s96
s_mul_i32 s21, s21, s95
mm v[76:79],v[216:219],v[168:171],v[76:79]
rt v[208:209],v242 offset:59264
rt v[210:211],v242 offset:60288
mm v[44:47],v[216:219],v[164:167],v[44:47]
ds_write_b128 v238, v[248:251] offset:1024
mm v[44:47],v[220:223],v[180:183],v[44:47]
st a[172:175],v255,s[48:51],s18 offen offset:2048 sc1
mm v[12:15],v[220:223],v[176:179],v[12:15]
wt lgkmcnt(5)
v_lshrrev_b32 v236, 8, v255
mm v[12:15],v[216:219],v[160:163],v[12:15]
rt v[220:221],v243 offset:50816
rt v[222:223],v243 offset:51840
mm v[16:19],v[196:199],v[160:163],v[16:19]
ar v224,a64
ar v225,a65
mm v[16:19],v[192:195],v[176:179],v[16:19]
ld v[152:155],v244,s[52:55],s91 offen offset:3072 sc1
mm v[48:51],v[192:195],v[180:183],v[48:51]
ar v226,a66
ar v227,a67
mm v[48:51],v[196:199],v[164:167],v[48:51]
rt v[216:217],v243 offset:59264
rt v[218:219],v243 offset:60288
mm v[80:83],v[196:199],v[168:171],v[80:83]
ar v228,a68
ar v229,a69
mm v[80:83],v[192:195],v[184:187],v[80:83]
ld v[156:159],v245,s[52:55],s91 offen offset:3072 sc1
mm v[112:115],v[192:195],v[188:191],v[112:115]
ar v230,a70
ar v231,a71
mm v[112:115],v[196:199],v[172:175],v[112:115]
cv v248,v224,v225
cv v249,v226,v227
mm v[116:119],v[204:207],v[172:175],v[116:119]
wt lgkmcnt(0)
s_barrier
mm v[116:119],v[200:203],v[188:191],v[116:119]
ld a[120:123],v246,s[52:55],s91 offen offset:3072 sc1
mm v[84:87],v[200:203],v[184:187],v[84:87]
rt v[192:193],v240 offset:0
rt v[194:195],v240 offset:1024
mm v[84:87],v[204:207],v[168:171],v[84:87]
rt v[196:197],v240 offset:8448
rt v[198:199],v240 offset:9472
mm v[52:55],v[204:207],v[164:167],v[52:55]
s_add_u32 m0, s83, 50688
cv v250,v228,v229
mm v[52:55],v[200:203],v[180:183],v[52:55]
ld v232,s[44:47],s80 offen lds
mm v[20:23],v[200:203],v[176:179],v[20:23]
cv v251,v230,v231
aw a188,v248
mm v[20:23],v[204:207],v[160:163],v[20:23]
rt v[200:201],v241 offset:0
rt v[202:203],v241 offset:1024
mm v[24:27],v[212:215],v[160:163],v[24:27]
aw a189,v249
aw a190,v250
mm v[24:27],v[208:211],v[176:179],v[24:27]
ld a[124:127],v247,s[52:55],s91 offen offset:3072 sc1
mm v[56:59],v[208:211],v[180:183],v[56:59]
s_add_u32 s91, s91, 4096
aw a191,v251
mm v[56:59],v[212:215],v[164:167],v[56:59]
rt v[204:205],v241 offset:8448
rt v[206:207],v241 offset:9472
mm v[88:91],v[212:215],v[168:171],v[88:91]
s_add_u32 m0, s83, 51712
v_lshlrev_b32 v239, 5, v236
mm v[88:91],v[208:211],v[184:187],v[88:91]
ld v233,s[44:47],s80 offen lds
mm v[120:123],v[208:211],v[188:191],v[120:123]
v_xor_b32 v239, v239, v255
v_and_b32 v239, 0xf0, v239
mm v[120:123],v[212:215],v[172:175],v[120:123]
rt v[208:209],v242 offset:0
rt v[210:211],v242 offset:1024
mm v[124:127],v[220:223],v[172:175],v[124:127]
v_lshl_add_u32 v239, v236, 11, v239
s_lshl_b32 s92, s77, 16
mm v[124:127],v[216:219],v[188:191],v[124:127]
s_lshl_b32 s93, s78, 14
s_add_u32 s92, s92, s93
mm v[92:95],v[216:219],v[184:187],v[92:95]
v_add_u32 v239, s92, v239
aw a228,v239
mm v[92:95],v[220:223],v[168:171],v[92:95]
rt v[212:213],v242 offset:8448
rt v[214:215],v242 offset:9472
mm v[60:63],v[220:223],v[164:167],v[60:63]
ds_write_b128 v238, v[248:251] offset:2048
mm v[60:63],v[216:219],v[180:183],v[60:63]
st a[176:179],v255,s[48:51],s18 offen offset:3072 sc1
mm v[28:31],v[216:219],v[176:179],v[28:31]
wt vmcnt(16) lgkmcnt(5)
v_bfe_u32 v239, v255, 6, 2
mm v[28:31],v[220:223],v[160:163],v[28:31]
rt v[216:217],v243 offset:0
rt v[218:219],v243 offset:1024
mm v[0:3],v[192:195],v[128:131],v[0:3]
ar v224,a0
ar v225,a1
mm v[0:3],v[196:199],a[104:107],v[0:3]
ld v[160:163],v244,s[52:55],s91 offen offset:0 sc1
mm v[32:35],v[196:199],a[108:111],v[32:35]
ar v226,a2
ar v227,a3
mm v[32:35],v[192:195],v[140:143],v[32:35]
rt v[220:221],v243 offset:8448
rt v[222:223],v243 offset:9472
mm v[64:67],v[192:195],a[96:99],v[64:67]
ar v228,a4
ar v229,a5
mm v[64:67],v[196:199],a[112:115],v[64:67]
ld v[164:167],v245,s[52:55],s91 offen offset:0 sc1
mm v[96:99],v[196:199],a[116:119],v[96:99]
ar v230,a6
ar v231,a7
mm v[96:99],v[192:195],a[100:103],v[96:99]
rt v[196:197],v240 offset:128
rt v[198:199],v240 offset:1152
mm v[100:103],v[200:203],a[100:103],v[100:103]
s_add_u32 m0, s83, 52864
cv v248,v224,v225
mm v[100:103],v[204:207],a[116:119],v[100:103]
ld v234,s[44:47],s80 offen lds
mm v[68:71],v[204:207],a[112:115],v[68:71]
cv v249,v226,v227
cv v250,v228,v229
mm v[68:71],v[200:203],a[96:99],v[68:71]
rt v[192:193],v240 offset:8576
rt v[194:195],v240 offset:9600
mm v[36:39],v[200:203],v[140:143],v[36:39]
cv v251,v230,v231
aw a0,v248
mm v[36:39],v[204:207],a[108:111],v[36:39]
ld v[168:171],v246,s[52:55],s91 offen offset:0 sc1
mm v[4:7],v[204:207],a[104:107],v[4:7]
wt lgkmcnt(4)
aw a1,v249
mm v[4:7],v[200:203],v[128:131],v[4:7]
rt v[204:205],v241 offset:128
rt v[206:207],v241 offset:1152
mm v[8:11],v[208:211],v[128:131],v[8:11]
s_add_u32 m0, s83, 53888
aw a2,v250
mm v[8:11],v[212:215],a[104:107],v[8:11]
ld v235,s[44:47],s80 offen lds
mm v[40:43],v[212:215],a[108:111],v[40:43]
s_add_u32 s80, s80, 524288
aw a3,v251
mm v[40:43],v[208:211],v[140:143],v[40:43]
rt v[200:201],v241 offset:8576
rt v[202:203],v241 offset:9600
mm v[72:75],v[208:211],a[96:99],v[72:75]
v_lshlrev_b32 v239, 6, v239
v_lshl_add_u32 v239, v236, 4, v239
mm v[72:75],v[212:215],a[112:115],v[72:75]
ld v[172:175],v247,s[52:55],s91 offen offset:0 sc1
mm v[104:107],v[212:215],a[116:119],v[104:107]
s_lshl_b32 s92, s78, 8
v_add_u32 v239, s92, v239
mm v[104:107],v[208:211],a[100:103],v[104:107]
rt v[212:213],v242 offset:128
rt v[214:215],v242 offset:1152
mm v[108:111],v[216:219],a[100:103],v[108:111]
v_mul_u32_u24 v239, 7168, v239
v_and_or_b32 v239, v255, 48, v239
mm v[108:111],v[220:223],a[116:119],v[108:111]
aw a229,v239
v_and_b32 v239, 0xf0, v255
mm v[76:79],v[220:223],a[112:115],v[76:79]
s_mov_b32 s18, 16384
v_lshl_add_u32 v239, v236, 14, v239
mm v[76:79],v[216:219],a[96:99],v[76:79]
rt v[208:209],v242 offset:8576
rt v[210:211],v242 offset:9600
mm v[44:47],v[216:219],v[140:143],v[44:47]
ds_write_b128 v238, v[248:251] offset:3072
mm v[44:47],v[220:223],a[108:111],v[44:47]
st a[180:183],v255,s[48:51],s18 offen offset:0 sc1
mm v[12:15],v[220:223],a[104:107],v[12:15]
wt lgkmcnt(5)
s_lshl_b32 s92, s3, 9
mm v[12:15],v[216:219],v[128:131],v[12:15]
rt v[220:221],v243 offset:128
rt v[222:223],v243 offset:1152
mm v[16:19],v[196:199],v[128:131],v[16:19]
ar v224,a72
ar v225,a73
mm v[16:19],v[192:195],a[104:107],v[16:19]
ld v[176:179],v244,s[52:55],s91 offen offset:1024 sc1
mm v[48:51],v[192:195],a[108:111],v[48:51]
ar v226,a74
ar v227,a75
mm v[48:51],v[196:199],v[140:143],v[48:51]
rt v[216:217],v243 offset:8576
rt v[218:219],v243 offset:9600
mm v[80:83],v[196:199],a[96:99],v[80:83]
ar v228,a76
.La5_LW1_12:
s_cbranch_execz .La5_LW1_R0
ar v229,a77
mm v[80:83],v[192:195],a[112:115],v[80:83]
ld v[180:183],v245,s[52:55],s91 offen offset:1024 sc1
mm v[112:115],v[192:195],a[116:119],v[112:115]
ar v230,a78
ar v231,a79
mm v[112:115],v[196:199],a[100:103],v[112:115]
cv v248,v224,v225
cv v249,v226,v227
mm v[116:119],v[204:207],a[100:103],v[116:119]
wt lgkmcnt(0)
s_barrier
mm v[116:119],v[200:203],a[116:119],v[116:119]
ld v[184:187],v246,s[52:55],s91 offen offset:1024 sc1
mm v[84:87],v[200:203],a[112:115],v[84:87]
rt v[192:193],v240 offset:16896
rt v[194:195],v240 offset:17920
mm v[84:87],v[204:207],a[96:99],v[84:87]
rt v[196:197],v240 offset:25344
rt v[198:199],v240 offset:26368
mm v[52:55],v[204:207],v[140:143],v[52:55]
s_add_u32 m0, s83, 0
cv v250,v228,v229
mm v[52:55],v[200:203],a[108:111],v[52:55]
ld v232,s[44:47],s80 offen lds
mm v[20:23],v[200:203],a[104:107],v[20:23]
cv v251,v230,v231
aw a4,v248
mm v[20:23],v[204:207],v[128:131],v[20:23]
rt v[200:201],v241 offset:16896
rt v[202:203],v241 offset:17920
mm v[24:27],v[212:215],v[128:131],v[24:27]
aw a5,v249
aw a6,v250
mm v[24:27],v[208:211],a[104:107],v[24:27]
ld v[188:191],v247,s[52:55],s91 offen offset:1024 sc1
mm v[56:59],v[208:211],a[108:111],v[56:59]
aw a7,v251
v_add_u32 v239, s92, v239
mm v[56:59],v[212:215],v[140:143],v[56:59]
rt v[204:205],v241 offset:25344
rt v[206:207],v241 offset:26368
mm v[88:91],v[212:215],a[96:99],v[88:91]
s_add_u32 m0, s83, 1024
aw a230,v239
mm v[88:91],v[208:211],a[112:115],v[88:91]
ld v233,s[44:47],s80 offen lds
mm v[120:123],v[208:211],a[116:119],v[120:123]
v_add_u32 v239, 4294886912, v240
aw a224,v239
mm v[120:123],v[212:215],a[100:103],v[120:123]
rt v[208:209],v242 offset:16896
rt v[210:211],v242 offset:17920
mm v[124:127],v[220:223],a[100:103],v[124:127]
v_bfe_u32 v239, v255, 4, 4
v_lshlrev_b32 v236, 2, v236
mm v[124:127],v[216:219],a[116:119],v[124:127]
v_sub_u32 v239, v239, v236
v_subrev_u32 v236, 0, v239
mm v[92:95],v[216:219],a[112:115],v[92:95]
v_ashrrev_i32 v236, 31, v236
v_and_b32 v236, 0xff800000, v236
mm v[92:95],v[220:223],a[96:99],v[92:95]
rt v[212:213],v242 offset:25344
rt v[214:215],v242 offset:26368
mm v[60:63],v[220:223],v[140:143],v[60:63]
ds_write_b128 v238, v[248:251] offset:4096
mm v[60:63],v[216:219],a[108:111],v[60:63]
aw a236,v236
v_subrev_u32 v236, 1, v239
mm v[28:31],v[216:219],a[104:107],v[28:31]
wt vmcnt(15) lgkmcnt(5)
v_ashrrev_i32 v236, 31, v236
mm v[28:31],v[220:223],v[128:131],v[28:31]
rt v[216:217],v243 offset:16896
rt v[218:219],v243 offset:17920
mm v[0:3],v[192:195],v[132:135],v[0:3]
ar v224,a8
ar v225,a9
mm v[0:3],v[196:199],v[152:155],v[0:3]
ld v[128:131],v244,s[52:55],s91 offen offset:2048 sc1
mm v[32:35],v[196:199],v[156:159],v[32:35]
ar v226,a10
ar v227,a11
mm v[32:35],v[192:195],v[136:139],v[32:35]
rt v[220:221],v243 offset:25344
rt v[222:223],v243 offset:26368
mm v[64:67],v[192:195],v[144:147],v[64:67]
ar v228,a12
ar v229,a13
mm v[64:67],v[196:199],a[120:123],v[64:67]
ld v[140:143],v245,s[52:55],s91 offen offset:2048 sc1
mm v[96:99],v[196:199],a[124:127],v[96:99]
ar v230,a14
ar v231,a15
mm v[96:99],v[192:195],v[148:151],v[96:99]
rt v[196:197],v240 offset:17024
rt v[198:199],v240 offset:18048
mm v[100:103],v[200:203],v[148:151],v[100:103]
s_add_u32 m0, s83, 2176
cv v248,v224,v225
mm v[100:103],v[204:207],a[124:127],v[100:103]
ld v234,s[44:47],s80 offen lds
mm v[68:71],v[204:207],a[120:123],v[68:71]
cv v249,v226,v227
cv v250,v228,v229
mm v[68:71],v[200:203],v[144:147],v[68:71]
rt v[192:193],v240 offset:25472
rt v[194:195],v240 offset:26496
mm v[36:39],v[200:203],v[136:139],v[36:39]
cv v251,v230,v231
aw a8,v248
mm v[36:39],v[204:207],v[156:159],v[36:39]
ld a[96:99],v246,s[52:55],s91 offen offset:2048 sc1
mm v[4:7],v[204:207],v[152:155],v[4:7]
wt lgkmcnt(4)
aw a9,v249
mm v[4:7],v[200:203],v[132:135],v[4:7]
rt v[204:205],v241 offset:17024
rt v[206:207],v241 offset:18048
mm v[8:11],v[208:211],v[132:135],v[8:11]
s_add_u32 m0, s83, 3200
aw a10,v250
mm v[8:11],v[212:215],v[152:155],v[8:11]
ld v235,s[44:47],s80 offen lds
mm v[40:43],v[212:215],v[156:159],v[40:43]
s_add_u32 s80, s80, 524288
aw a11,v251
mm v[40:43],v[208:211],v[136:139],v[40:43]
rt v[200:201],v241 offset:25472
rt v[202:203],v241 offset:26496
mm v[72:75],v[208:211],v[144:147],v[72:75]
v_and_b32 v236, 0xff800000, v236
aw a237,v236
mm v[72:75],v[212:215],a[120:123],v[72:75]
ld a[100:103],v247,s[52:55],s91 offen offset:2048 sc1
mm v[104:107],v[212:215],a[124:127],v[104:107]
v_subrev_u32 v236, 2, v239
v_ashrrev_i32 v236, 31, v236
mm v[104:107],v[208:211],v[148:151],v[104:107]
rt v[212:213],v242 offset:17024
rt v[214:215],v242 offset:18048
mm v[108:111],v[216:219],v[148:151],v[108:111]
v_and_b32 v236, 0xff800000, v236
aw a238,v236
mm v[108:111],v[220:223],a[124:127],v[108:111]
v_subrev_u32 v236, 3, v239
v_ashrrev_i32 v236, 31, v236
mm v[76:79],v[220:223],a[120:123],v[76:79]
v_and_b32 v236, 0xff800000, v236
aw a239,v236
mm v[76:79],v[216:219],v[144:147],v[76:79]
rt v[208:209],v242 offset:25472
rt v[210:211],v242 offset:26496
mm v[44:47],v[216:219],v[136:139],v[44:47]
ds_write_b128 v238, v[248:251] offset:5120
mm v[44:47],v[220:223],v[156:159],v[44:47]
st a[184:187],v255,s[48:51],s18 offen offset:1024 sc1
mm v[12:15],v[220:223],v[152:155],v[12:15]
wt lgkmcnt(5)
v_lshlrev_b32 v236, 4, v239
mm v[12:15],v[216:219],v[132:135],v[12:15]
rt v[220:221],v243 offset:17024
rt v[222:223],v243 offset:18048
mm v[16:19],v[196:199],v[132:135],v[16:19]
ar v224,a80
ar v225,a81
mm v[16:19],v[192:195],v[152:155],v[16:19]
ld a[104:107],v244,s[52:55],s91 offen offset:3072 sc1
mm v[48:51],v[192:195],v[156:159],v[48:51]
ar v226,a82
ar v227,a83
mm v[48:51],v[196:199],v[136:139],v[48:51]
rt v[216:217],v243 offset:25472
rt v[218:219],v243 offset:26496
mm v[80:83],v[196:199],v[144:147],v[80:83]
ar v228,a84
ar v229,a85
mm v[80:83],v[192:195],a[120:123],v[80:83]
ld a[108:111],v245,s[52:55],s91 offen offset:3072 sc1
mm v[112:115],v[192:195],a[124:127],v[112:115]
ar v230,a86
ar v231,a87
mm v[112:115],v[196:199],v[148:151],v[112:115]
cv v248,v224,v225
cv v249,v226,v227
mm v[116:119],v[204:207],v[148:151],v[116:119]
wt lgkmcnt(0)
s_barrier
mm v[116:119],v[200:203],a[124:127],v[116:119]
ld a[112:115],v246,s[52:55],s91 offen offset:3072 sc1
mm v[84:87],v[200:203],a[120:123],v[84:87]
rt v[192:193],v240 offset:33792
rt v[194:195],v240 offset:34816
mm v[84:87],v[204:207],v[144:147],v[84:87]
rt v[196:197],v240 offset:42240
rt v[198:199],v240 offset:43264
mm v[52:55],v[204:207],v[136:139],v[52:55]
s_add_u32 m0, s83, 16896
cv v250,v228,v229
mm v[52:55],v[200:203],v[156:159],v[52:55]
ld v232,s[44:47],s80 offen lds
mm v[20:23],v[200:203],v[152:155],v[20:23]
cv v251,v230,v231
aw a12,v248
mm v[20:23],v[204:207],v[132:135],v[20:23]
rt v[200:201],v241 offset:33792
rt v[202:203],v241 offset:34816
mm v[24:27],v[212:215],v[132:135],v[24:27]
aw a13,v249
aw a14,v250
mm v[24:27],v[208:211],v[152:155],v[24:27]
ld a[116:119],v247,s[52:55],s91 offen offset:3072 sc1
mm v[56:59],v[208:211],v[156:159],v[56:59]
s_add_u32 s91, s91, 4096
aw a15,v251
mm v[56:59],v[212:215],v[136:139],v[56:59]
rt v[204:205],v241 offset:42240
rt v[206:207],v241 offset:43264
mm v[88:91],v[212:215],v[144:147],v[88:91]
s_add_u32 m0, s83, 17920
s_mov_b32 s92, 0x3f80
mm v[88:91],v[208:211],a[120:123],v[88:91]
ld v233,s[44:47],s80 offen lds
mm v[120:123],v[208:211],a[124:127],v[120:123]
v_lshlrev_b32 v236, v236, s92
v_cmp_gt_u32 vcc, 2, v239
mm v[120:123],v[212:215],v[148:151],v[120:123]
rt v[208:209],v242 offset:33792
rt v[210:211],v242 offset:34816
mm v[124:127],v[220:223],v[148:151],v[124:127]
v_subrev_u32 v239, 2, v239
v_cmp_gt_u32 s[92:93], 2, v239
mm v[124:127],v[216:219],a[124:127],v[124:127]
v_cndmask_b32 v239, 0, v236, vcc
aw a212,0
mm v[92:95],v[216:219],a[120:123],v[92:95]
aw a214,v239
v_cndmask_b32 v239, 0, v236, s[92:93]
mm v[92:95],v[220:223],v[144:147],v[92:95]
rt v[212:213],v242 offset:42240
rt v[214:215],v242 offset:43264
mm v[60:63],v[220:223],v[136:139],v[60:63]
v_mov_b32 v236, 0x3f803f80
aw a215,v239
mm v[60:63],v[216:219],v[156:159],v[60:63]
st a[188:191],v255,s[48:51],s18 offen offset:2048 sc1
mm v[28:31],v[216:219],v[152:155],v[28:31]
wt vmcnt(15) lgkmcnt(4)
aw a213,0
mm v[28:31],v[220:223],v[132:135],v[28:31]
rt v[216:217],v243 offset:33792
rt v[218:219],v243 offset:34816
mm v[0:3],v[192:195],v[160:163],v[0:3]
ar v224,a16
ar v225,a17
mm v[0:3],v[196:199],v[176:179],v[0:3]
ld v[132:135],v244,s[52:55],s91 offen offset:0 sc1
mm v[32:35],v[196:199],v[180:183],v[32:35]
ar v226,a18
ar v227,a19
mm v[32:35],v[192:195],v[164:167],v[32:35]
rt v[220:221],v243 offset:42240
rt v[222:223],v243 offset:43264
mm v[64:67],v[192:195],v[168:171],v[64:67]
ar v228,a20
ar v229,a21
mm v[64:67],v[196:199],v[184:187],v[64:67]
ld v[136:139],v245,s[52:55],s91 offen offset:0 sc1
mm v[96:99],v[196:199],v[188:191],v[96:99]
ar v230,a22
ar v231,a23
mm v[96:99],v[192:195],v[172:175],v[96:99]
rt v[196:197],v240 offset:33920
rt v[198:199],v240 offset:34944
mm v[100:103],v[200:203],v[172:175],v[100:103]
s_add_u32 m0, s83, 19072
cv v248,v224,v225
mm v[100:103],v[204:207],v[188:191],v[100:103]
ld v234,s[44:47],s80 offen lds
mm v[68:71],v[204:207],v[184:187],v[68:71]
cv v249,v226,v227
cv v250,v228,v229
mm v[68:71],v[200:203],v[168:171],v[68:71]
rt v[192:193],v240 offset:42368
rt v[194:195],v240 offset:43392
mm v[36:39],v[200:203],v[164:167],v[36:39]
cv v251,v230,v231
aw a16,v248
mm v[36:39],v[204:207],v[180:183],v[36:39]
ld v[144:147],v246,s[52:55],s91 offen offset:0 sc1
mm v[4:7],v[204:207],v[176:179],v[4:7]
wt lgkmcnt(4)
aw a17,v249
mm v[4:7],v[200:203],v[160:163],v[4:7]
rt v[204:205],v241 offset:33920
rt v[206:207],v241 offset:34944
mm v[8:11],v[208:211],v[160:163],v[8:11]
s_add_u32 m0, s83, 20096
aw a18,v250
mm v[8:11],v[212:215],v[176:179],v[8:11]
ld v235,s[44:47],s80 offen lds
mm v[40:43],v[212:215],v[180:183],v[40:43]
s_add_u32 s80, s80, 524288
aw a19,v251
mm v[40:43],v[208:211],v[164:167],v[40:43]
rt v[200:201],v241 offset:42368
rt v[202:203],v241 offset:43392
mm v[72:75],v[208:211],v[168:171],v[72:75]
aw a216,0
aw a217,0
mm v[72:75],v[212:215],v[184:187],v[72:75]
ld v[148:151],v247,s[52:55],s91 offen offset:0 sc1
mm v[104:107],v[212:215],v[188:191],v[104:107]
v_lshrrev_b32 v239, 4, v255
v_xor_b32 v239, v239, v255
mm v[104:107],v[208:211],v[172:175],v[104:107]
rt v[212:213],v242 offset:33920
rt v[214:215],v242 offset:34944
mm v[108:111],v[216:219],v[172:175],v[108:111]
v_and_b32 v239, 32, v239
v_cmp_eq_u32 vcc, 0, v239
mm v[108:111],v[220:223],v[188:191],v[108:111]
s_mov_b32 s78, 4294950911
s_mul_i32 s70, s70, s22
mm v[76:79],v[220:223],v[184:187],v[76:79]
v_cndmask_b32 v236, 0, v236, vcc
s_sub_u32 s92, 4294967293, s3
mm v[76:79],v[216:219],v[168:171],v[76:79]
rt v[208:209],v242 offset:42368
rt v[210:211],v242 offset:43392
mm v[44:47],v[216:219],v[164:167],v[44:47]
s_add_u32 s92, s92, s7
s_mul_i32 s92, s92, s21
mm v[44:47],v[220:223],v[180:183],v[44:47]
st a[0:3],v255,s[48:51],s18 offen offset:3072 sc1
mm v[12:15],v[220:223],v[176:179],v[12:15]
wt lgkmcnt(4)
s_add_u32 s90, s90, s92
mm v[12:15],v[216:219],v[160:163],v[12:15]
rt v[220:221],v243 offset:33920
rt v[222:223],v243 offset:34944
mm v[16:19],v[196:199],v[160:163],v[16:19]
ar v224,a88
ar v225,a89
mm v[16:19],v[192:195],v[176:179],v[16:19]
ld v[152:155],v244,s[52:55],s91 offen offset:1024 sc1
mm v[48:51],v[192:195],v[180:183],v[48:51]
ar v226,a90
ar v227,a91
mm v[48:51],v[196:199],v[164:167],v[48:51]
rt v[216:217],v243 offset:42368
rt v[218:219],v243 offset:43392
mm v[80:83],v[196:199],v[168:171],v[80:83]
ar v228,a92
ar v229,a93
mm v[80:83],v[192:195],v[184:187],v[80:83]
ld v[156:159],v245,s[52:55],s91 offen offset:1024 sc1
mm v[112:115],v[192:195],v[188:191],v[112:115]
ar v230,a94
ar v231,a95
mm v[112:115],v[196:199],v[172:175],v[112:115]
cv v248,v224,v225
cv v249,v226,v227
mm v[116:119],v[204:207],v[172:175],v[116:119]
wt lgkmcnt(0)
s_barrier
mm v[116:119],v[200:203],v[188:191],v[116:119]
ld a[120:123],v246,s[52:55],s91 offen offset:1024 sc1
mm v[84:87],v[200:203],v[184:187],v[84:87]
rt v[192:193],v240 offset:50688
rt v[194:195],v240 offset:51712
mm v[84:87],v[204:207],v[168:171],v[84:87]
rt v[196:197],v240 offset:59136
rt v[198:199],v240 offset:60160
mm v[52:55],v[204:207],v[164:167],v[52:55]
s_add_u32 m0, s83, 33792
cv v250,v228,v229
mm v[52:55],v[200:203],v[180:183],v[52:55]
ld v232,s[44:47],s80 offen lds
mm v[20:23],v[200:203],v[176:179],v[20:23]
cv v251,v230,v231
aw a20,v248
mm v[20:23],v[204:207],v[160:163],v[20:23]
rt v[200:201],v241 offset:50688
rt v[202:203],v241 offset:51712
mm v[24:27],v[212:215],v[160:163],v[24:27]
aw a21,v249
aw a22,v250
mm v[24:27],v[208:211],v[176:179],v[24:27]
ld a[124:127],v247,s[52:55],s91 offen offset:1024 sc1
mm v[56:59],v[208:211],v[180:183],v[56:59]
aw a23,v251
s_mov_b32 s17, 1073741824
mm v[56:59],v[212:215],v[164:167],v[56:59]
rt v[204:205],v241 offset:59136
rt v[206:207],v241 offset:60160
mm v[88:91],v[212:215],v[168:171],v[88:91]
s_add_u32 m0, s83, 34816
s_lshl_b32 s92, s73, 2
mm v[88:91],v[208:211],v[184:187],v[88:91]
ld v233,s[44:47],s80 offen lds
mm v[120:123],v[208:211],v[188:191],v[120:123]
s_lshr_b32 s92, 66051, s92
s_and_b32 s92, s92, 3
mm v[120:123],v[212:215],v[172:175],v[120:123]
rt v[208:209],v242 offset:50688
rt v[210:211],v242 offset:51712
mm v[124:127],v[220:223],v[172:175],v[124:127]
s_lshl_b32 s16, s92, 16
s_mul_i32 s92, s75, 4294836224
mm v[124:127],v[216:219],v[188:191],v[124:127]
s_add_u32 s92, s92, s88
s_add_u32 s19, s92, 393216
mm v[92:95],v[216:219],v[184:187],v[92:95]
s_mov_b32 s18, 20480
aw a208,v236
mm v[92:95],v[220:223],v[168:171],v[92:95]
rt v[212:213],v242 offset:59136
rt v[214:215],v242 offset:60160
mm v[60:63],v[220:223],v[164:167],v[60:63]
aw a209,v236
aw a210,v236
mm v[60:63],v[216:219],v[180:183],v[60:63]
st a[4:7],v255,s[48:51],s18 offen offset:0 sc1
mm v[28:31],v[216:219],v[176:179],v[28:31]
wt vmcnt(16) lgkmcnt(4)
aw a211,v236
mm v[28:31],v[220:223],v[160:163],v[28:31]
rt v[216:217],v243 offset:50688
rt v[218:219],v243 offset:51712
mm v[0:3],v[192:195],v[128:131],v[0:3]
ar v224,a24
ar v225,a25
mm v[0:3],v[196:199],a[104:107],v[0:3]
ld v[160:163],v244,s[52:55],s91 offen offset:2048 sc1
mm v[32:35],v[196:199],a[108:111],v[32:35]
ar v226,a26
ar v227,a27
mm v[32:35],v[192:195],v[140:143],v[32:35]
rt v[220:221],v243 offset:59136
rt v[222:223],v243 offset:60160
mm v[64:67],v[192:195],a[96:99],v[64:67]
ar v228,a28
ar v229,a29
mm v[64:67],v[196:199],a[112:115],v[64:67]
ld v[164:167],v245,s[52:55],s91 offen offset:2048 sc1
mm v[96:99],v[196:199],a[116:119],v[96:99]
ar v230,a30
ar v231,a31
mm v[96:99],v[192:195],a[100:103],v[96:99]
rt v[196:197],v240 offset:50816
rt v[198:199],v240 offset:51840
mm v[100:103],v[200:203],a[100:103],v[100:103]
s_add_u32 m0, s83, 35968
cv v248,v224,v225
mm v[100:103],v[204:207],a[116:119],v[100:103]
ld v234,s[44:47],s80 offen lds
mm v[68:71],v[204:207],a[112:115],v[68:71]
cv v249,v226,v227
cv v250,v228,v229
mm v[68:71],v[200:203],a[96:99],v[68:71]
rt v[192:193],v240 offset:59264
rt v[194:195],v240 offset:60288
mm v[36:39],v[200:203],v[140:143],v[36:39]
cv v251,v230,v231
aw a24,v248
mm v[36:39],v[204:207],a[108:111],v[36:39]
ld v[168:171],v246,s[52:55],s91 offen offset:2048 sc1
mm v[4:7],v[204:207],a[104:107],v[4:7]
wt lgkmcnt(4)
aw a25,v249
mm v[4:7],v[200:203],v[128:131],v[4:7]
rt v[204:205],v241 offset:50816
rt v[206:207],v241 offset:51840
mm v[8:11],v[208:211],v[128:131],v[8:11]
s_add_u32 m0, s83, 36992
aw a26,v250
mm v[8:11],v[212:215],a[104:107],v[8:11]
ld v235,s[44:47],s80 offen lds
mm v[40:43],v[212:215],a[108:111],v[40:43]
s_add_u32 s80, s80, 524288
aw a27,v251
mm v[40:43],v[208:211],v[140:143],v[40:43]
rt v[200:201],v241 offset:59264
rt v[202:203],v241 offset:60288
mm v[72:75],v[208:211],a[96:99],v[72:75]
aw a232,0
aw a233,0
mm v[72:75],v[212:215],a[112:115],v[72:75]
ld v[172:175],v247,s[52:55],s91 offen offset:2048 sc1
mm v[104:107],v[212:215],a[116:119],v[104:107]
aw a234,0
aw a235,0
mm v[104:107],v[208:211],a[100:103],v[104:107]
rt v[212:213],v242 offset:50816
rt v[214:215],v242 offset:51840
mm v[108:111],v[216:219],a[100:103],v[108:111]
s_lshl_b32 s20, s72, 10
s_add_u32 s20, s20, 16384
mm v[108:111],v[220:223],a[116:119],v[108:111]
s_mov_b32 s4, 1985221408
mm v[76:79],v[220:223],a[112:115],v[76:79]
s_cmp_ge_u32 s2, 64
s_cselect_b32 s4, 1985225488, s4
mm v[76:79],v[216:219],a[96:99],v[76:79]
rt v[208:209],v242 offset:59264
rt v[210:211],v242 offset:60288
mm v[44:47],v[216:219],v[140:143],v[44:47]
s_cmp_ge_u32 s2, 128
s_cselect_b32 s4, 1985229328, s4
mm v[44:47],v[220:223],a[108:111],v[44:47]
st a[8:11],v255,s[48:51],s18 offen offset:1024 sc1
mm v[12:15],v[220:223],a[104:107],v[12:15]
wt lgkmcnt(4)
mm v[12:15],v[216:219],v[128:131],v[12:15]
rt v[220:221],v243 offset:50816
rt v[222:223],v243 offset:51840
mm v[16:19],v[196:199],v[128:131],v[16:19]
ar v224,a240
ar v225,a241
mm v[16:19],v[192:195],a[104:107],v[16:19]
ld v[176:179],v244,s[52:55],s91 offen offset:3072 sc1
mm v[48:51],v[192:195],a[108:111],v[48:51]
ar v226,a242
ar v227,a243
mm v[48:51],v[196:199],v[140:143],v[48:51]
rt v[216:217],v243 offset:59264
rt v[218:219],v243 offset:60288
mm v[80:83],v[196:199],a[96:99],v[80:83]
ar v228,a244
ar v229,a245
mm v[80:83],v[192:195],a[112:115],v[80:83]
ld v[180:183],v245,s[52:55],s91 offen offset:3072 sc1
mm v[112:115],v[192:195],a[116:119],v[112:115]
ar v230,a246
.La5_LW1_13:
s_cbranch_execz .La5_LW1_R0
ar v231,a247
mm v[112:115],v[196:199],a[100:103],v[112:115]
cv v248,v224,v225
cv v249,v226,v227
mm v[116:119],v[204:207],a[100:103],v[116:119]
wt lgkmcnt(0)
s_barrier
mm v[116:119],v[200:203],a[116:119],v[116:119]
ld v[184:187],v246,s[52:55],s91 offen offset:3072 sc1
mm v[84:87],v[200:203],a[112:115],v[84:87]
rt v[192:193],v240 offset:0
rt v[194:195],v240 offset:1024
mm v[84:87],v[204:207],a[96:99],v[84:87]
rt v[196:197],v240 offset:8448
rt v[198:199],v240 offset:9472
mm v[52:55],v[204:207],v[140:143],v[52:55]
s_add_u32 m0, s83, 50688
cv v250,v228,v229
mm v[52:55],v[200:203],a[108:111],v[52:55]
ld v232,s[44:47],s80 offen lds
mm v[20:23],v[200:203],a[104:107],v[20:23]
cv v251,v230,v231
aw a28,v248
mm v[20:23],v[204:207],v[128:131],v[20:23]
rt v[200:201],v241 offset:0
rt v[202:203],v241 offset:1024
mm v[24:27],v[212:215],v[128:131],v[24:27]
aw a29,v249
aw a30,v250
mm v[24:27],v[208:211],a[104:107],v[24:27]
ld v[188:191],v247,s[52:55],s91 offen offset:3072 sc1
mm v[56:59],v[208:211],a[108:111],v[56:59]
s_add_u32 s91, s91, 4096
aw a31,v251
mm v[56:59],v[212:215],v[140:143],v[56:59]
rt v[204:205],v241 offset:8448
rt v[206:207],v241 offset:9472
mm v[88:91],v[212:215],a[96:99],v[88:91]
s_add_u32 m0, s83, 51712
mm v[88:91],v[208:211],a[112:115],v[88:91]
ld v233,s[44:47],s80 offen lds
mm v[120:123],v[208:211],a[116:119],v[120:123]
s_cmp_ge_u32 s2, 192
s_cselect_b32 s4, 1985225008, s4
mm v[120:123],v[212:215],a[100:103],v[120:123]
rt v[208:209],v242 offset:0
rt v[210:211],v242 offset:1024
mm v[124:127],v[220:223],a[100:103],v[124:127]
v_bfe_u32 v236, v255, 4, 3
v_lshlrev_b32 v236, 2, v236
mm v[124:127],v[216:219],a[116:119],v[124:127]
v_lshrrev_b32 v236, v236, s4
v_and_b32 v236, 15, v236
mm v[92:95],v[216:219],a[112:115],v[92:95]
v_bfe_u32 v239, v255, 7, 1
v_mul_u32_u24 v239, 25, v239
mm v[92:95],v[220:223],a[96:99],v[92:95]
rt v[212:213],v242 offset:8448
rt v[214:215],v242 offset:9472
mm v[60:63],v[220:223],v[140:143],v[60:63]
v_xor_b32 v236, v236, v239
v_xor_b32 v236, s72, v236
mm v[60:63],v[216:219],a[108:111],v[60:63]
v_lshlrev_b32 v236, 3, v236
aw a225,v236
mm v[28:31],v[216:219],a[104:107],v[28:31]
wt vmcnt(15) lgkmcnt(4)
s_lshl3_add_u32 s5, s76, s101
mm v[28:31],v[220:223],v[128:131],v[28:31]
rt v[216:217],v243 offset:0
rt v[218:219],v243 offset:1024
mm v[0:3],v[192:195],v[132:135],v[0:3]
ar v224,a32
ar v225,a33
s_lshl2_add_u32 s5, s5, s3
mm v[0:3],v[196:199],v[152:155],v[0:3]
ld v[128:131],v244,s[52:55],s91 offen offset:0 sc1
s_lshl_b32 s5, s5, 8
mm v[32:35],v[196:199],v[156:159],v[32:35]
ar v226,a34
ar v227,a35
s_add_u32 s5, s5, 1048576
mm v[32:35],v[192:195],v[136:139],v[32:35]
rt v[220:221],v243 offset:8448
rt v[222:223],v243 offset:9472
mm v[64:67],v[192:195],v[144:147],v[64:67]
ar v228,a36
ar v229,a37
mm v[64:67],v[196:199],a[120:123],v[64:67]
ld v[140:143],v245,s[52:55],s91 offen offset:0 sc1
mm v[96:99],v[196:199],a[124:127],v[96:99]
ar v230,a38
ar v231,a39
mm v[96:99],v[192:195],v[148:151],v[96:99]
rt v[196:197],v240 offset:128
rt v[198:199],v240 offset:1152
mm v[100:103],v[200:203],v[148:151],v[100:103]
s_add_u32 m0, s83, 52864
cv v248,v224,v225
mm v[100:103],v[204:207],a[124:127],v[100:103]
ld v234,s[44:47],s80 offen lds
mm v[68:71],v[204:207],a[120:123],v[68:71]
cv v249,v226,v227
cv v250,v228,v229
mm v[68:71],v[200:203],v[144:147],v[68:71]
rt v[192:193],v240 offset:8576
rt v[194:195],v240 offset:9600
mm v[36:39],v[200:203],v[136:139],v[36:39]
cv v251,v230,v231
aw a32,v248
mm v[36:39],v[204:207],v[156:159],v[36:39]
ld a[192:195],v246,s[52:55],s91 offen offset:0 sc1
mm v[4:7],v[204:207],v[152:155],v[4:7]
wt lgkmcnt(4)
aw a33,v249
mm v[4:7],v[200:203],v[132:135],v[4:7]
rt v[204:205],v241 offset:128
rt v[206:207],v241 offset:1152
mm v[8:11],v[208:211],v[132:135],v[8:11]
s_add_u32 m0, s83, 53888
aw a34,v250
mm v[8:11],v[212:215],v[152:155],v[8:11]
ld v235,s[44:47],s80 offen lds
mm v[40:43],v[212:215],v[156:159],v[40:43]
aw a35,v251
mm v[40:43],v[208:211],v[136:139],v[40:43]
rt v[200:201],v241 offset:8576
rt v[202:203],v241 offset:9600
mm v[72:75],v[208:211],v[144:147],v[72:75]
mm v[72:75],v[212:215],a[120:123],v[72:75]
ld a[196:199],v247,s[52:55],s91 offen offset:0 sc1
mm v[104:107],v[212:215],a[124:127],v[104:107]
mm v[104:107],v[208:211],v[148:151],v[104:107]
rt v[212:213],v242 offset:128
rt v[214:215],v242 offset:1152
mm v[108:111],v[216:219],v[148:151],v[108:111]
mm v[108:111],v[220:223],a[124:127],v[108:111]
mm v[76:79],v[220:223],a[120:123],v[76:79]
mm v[76:79],v[216:219],v[144:147],v[76:79]
rt v[208:209],v242 offset:8576
rt v[210:211],v242 offset:9600
mm v[44:47],v[216:219],v[136:139],v[44:47]
mm v[44:47],v[220:223],v[156:159],v[44:47]
st a[12:15],v255,s[48:51],s18 offen offset:2048 sc1
mm v[12:15],v[220:223],v[152:155],v[12:15]
wt lgkmcnt(4)
mm v[12:15],v[216:219],v[132:135],v[12:15]
rt v[220:221],v243 offset:128
rt v[222:223],v243 offset:1152
mm v[16:19],v[196:199],v[132:135],v[16:19]
ar v224,a248
ar v225,a249
mm v[16:19],v[192:195],v[152:155],v[16:19]
ld a[200:203],v244,s[52:55],s91 offen offset:1024 sc1
mm v[48:51],v[192:195],v[156:159],v[48:51]
ar v226,a250
ar v227,a251
mm v[48:51],v[196:199],v[136:139],v[48:51]
rt v[216:217],v243 offset:8576
rt v[218:219],v243 offset:9600
mm v[80:83],v[196:199],v[144:147],v[80:83]
ar v228,a252
ar v229,a253
mm v[80:83],v[192:195],a[120:123],v[80:83]
ld a[204:207],v245,s[52:55],s91 offen offset:1024 sc1
mm v[112:115],v[192:195],a[124:127],v[112:115]
ar v230,a254
ar v231,a255
mm v[112:115],v[196:199],v[148:151],v[112:115]
cv v248,v224,v225
cv v249,v226,v227
mm v[116:119],v[204:207],v[148:151],v[116:119]
wt lgkmcnt(0)
s_barrier
mm v[116:119],v[200:203],a[124:127],v[116:119]
cv v250,v228,v229
cv v251,v230,v231
mm v[84:87],v[200:203],a[120:123],v[84:87]
rt v[192:193],v240 offset:16896
rt v[194:195],v240 offset:17920
mm v[84:87],v[204:207],v[144:147],v[84:87]
rt v[196:197],v240 offset:25344
rt v[198:199],v240 offset:26368
mm v[52:55],v[204:207],v[136:139],v[52:55]
aw a36,v248
aw a37,v249
mm v[52:55],v[200:203],v[156:159],v[52:55]
aw a38,v250
aw a39,v251
mm v[20:23],v[200:203],v[152:155],v[20:23]
s_getpc_b64 s[92:93]
.La5_LCT2:
s_and_b32 s93, s93, 0xffff
mm v[20:23],v[204:207],v[132:135],v[20:23]
rt v[200:201],v241 offset:16896
rt v[202:203],v241 offset:17920
mm v[24:27],v[212:215],v[132:135],v[24:27]
s_lshr_b32 s94, s2, 3
s_lshl2_add_u32 s94, s94, s3
mm v[24:27],v[208:211],v[152:155],v[24:27]
s_lshl_b32 s94, s94, 10
v_lshrrev_b32 v233, 7, v255
mm v[56:59],v[208:211],v[156:159],v[56:59]
v_lshlrev_b32 v232, 3, v255
v_lshl_add_u32 v232, v233, 28, v232
mm v[56:59],v[212:215],v[136:139],v[56:59]
rt v[204:205],v241 offset:25344
rt v[206:207],v241 offset:26368
mm v[88:91],v[212:215],v[144:147],v[88:91]
v_add_u32 v232, s94, v232
s_mov_b32 s94, .La5_LCODEEND-.La5_LCT2
mm v[88:91],v[208:211],a[120:123],v[88:91]
s_mov_b32 s95, 0x20000
buffer_load_dword a253, v232, s[92:95], 0 offen
mm v[120:123],v[208:211],a[124:127],v[120:123]
s_mov_b32 s80, 8192
s_add_u32 s83, s83, 4294886912
mm v[120:123],v[212:215],v[148:151],v[120:123]
rt v[208:209],v242 offset:16896
rt v[210:211],v242 offset:17920
mm v[124:127],v[220:223],v[148:151],v[124:127]
s_lshl_b32 s92, s76, 21
s_add_u32 s92, s92, 54525952
mm v[124:127],v[216:219],a[124:127],v[124:127]
ld v[148:151],v246,s[52:55],s91 offen offset:1024 sc1
mm v[92:95],v[216:219],a[120:123],v[92:95]
s_add_u32 s44, s26, s92
s_addc_u32 s45, s27, 0
mm v[92:95],v[220:223],v[144:147],v[92:95]
rt v[212:213],v242 offset:25344
rt v[214:215],v242 offset:26368
mm v[60:63],v[220:223],v[136:139],v[60:63]
ds_write_b128 v238, a[12:15] offset:6144
mm v[60:63],v[216:219],v[156:159],v[60:63]
st a[16:19],v255,s[48:51],s18 offen offset:3072 sc1
mm v[28:31],v[216:219],v[152:155],v[28:31]
wt vmcnt(13) lgkmcnt(5)
s_and_b32 s45, s45, 0xffff
mm v[28:31],v[220:223],v[132:135],v[28:31]
rt v[216:217],v243 offset:16896
rt v[218:219],v243 offset:17920
mm v[0:3],v[192:195],v[160:163],v[0:3]
ar v224,a40
ar v225,a41
mm v[0:3],v[196:199],v[176:179],v[0:3]
ld v[132:135],v247,s[52:55],s91 offen offset:1024 sc1
mm v[32:35],v[196:199],v[180:183],v[32:35]
ar v226,a42
ar v227,a43
mm v[32:35],v[192:195],v[164:167],v[32:35]
rt v[220:221],v243 offset:25344
rt v[222:223],v243 offset:26368
mm v[64:67],v[192:195],v[168:171],v[64:67]
ar v228,a44
ar v229,a45
mm v[64:67],v[196:199],v[184:187],v[64:67]
ld a[96:99],v244,s[52:55],s91 offen offset:2048 sc1
mm v[96:99],v[196:199],v[188:191],v[96:99]
ar v230,a46
ar v231,a47
mm v[96:99],v[192:195],v[172:175],v[96:99]
rt v[196:197],v240 offset:17024
rt v[198:199],v240 offset:18048
mm v[100:103],v[200:203],v[172:175],v[100:103]
cv v248,v224,v225
cv v249,v226,v227
mm v[100:103],v[204:207],v[188:191],v[100:103]
ld a[100:103],v245,s[52:55],s91 offen offset:2048 sc1
mm v[68:71],v[204:207],v[184:187],v[68:71]
cv v250,v228,v229
cv v251,v230,v231
mm v[68:71],v[200:203],v[168:171],v[68:71]
rt v[192:193],v240 offset:25472
rt v[194:195],v240 offset:26496
mm v[36:39],v[200:203],v[164:167],v[36:39]
aw a40,v248
aw a41,v249
mm v[36:39],v[204:207],v[180:183],v[36:39]
ld a[104:107],v246,s[52:55],s91 offen offset:2048 sc1
mm v[4:7],v[204:207],v[176:179],v[4:7]
wt lgkmcnt(4)
aw a42,v250
mm v[4:7],v[200:203],v[160:163],v[4:7]
rt v[204:205],v241 offset:17024
rt v[206:207],v241 offset:18048
mm v[8:11],v[208:211],v[160:163],v[8:11]
aw a43,v251
s_mov_b32 s46, 2097536
mm v[8:11],v[212:215],v[176:179],v[8:11]
ld a[108:111],v247,s[52:55],s91 offen offset:2048 sc1
mm v[40:43],v[212:215],v[180:183],v[40:43]
s_mov_b32 s95, 4096
s_mov_b32 s96, 8192
mm v[40:43],v[208:211],v[164:167],v[40:43]
rt v[200:201],v241 offset:25472
rt v[202:203],v241 offset:26496
mm v[72:75],v[208:211],v[168:171],v[72:75]
s_mov_b32 s97, 12288
s_mul_i32 s92, s3, 196608
mm v[72:75],v[212:215],v[184:187],v[72:75]
s_sub_u32 s93, 1376256, s92
mm v[104:107],v[212:215],v[188:191],v[104:107]
s_mov_b32 s18, 0
mm v[104:107],v[208:211],v[172:175],v[104:107]
rt v[212:213],v242 offset:17024
rt v[214:215],v242 offset:18048
mm v[108:111],v[216:219],v[172:175],v[108:111]
mm v[108:111],v[220:223],v[188:191],v[108:111]
st a[20:23],v255,s[48:51],s18 offen offset:0 sc1
mm v[76:79],v[220:223],v[184:187],v[76:79]
mm v[76:79],v[216:219],v[168:171],v[76:79]
rt v[208:209],v242 offset:25472
rt v[210:211],v242 offset:26496
mm v[44:47],v[216:219],v[164:167],v[44:47]
ds_write_b128 v238, a[16:19] offset:7168
mm v[44:47],v[220:223],v[180:183],v[44:47]
st a[24:27],v255,s[48:51],s18 offen offset:1024 sc1
mm v[12:15],v[220:223],v[176:179],v[12:15]
wt lgkmcnt(5)
mm v[12:15],v[216:219],v[160:163],v[12:15]
rt v[220:221],v243 offset:17024
rt v[222:223],v243 offset:18048
mm v[16:19],v[196:199],v[160:163],v[16:19]
ar v224,a48
ar v225,a49
mm v[16:19],v[192:195],v[176:179],v[16:19]
ld a[112:115],v244,s[52:55],s91 offen offset:3072 sc1
mm v[48:51],v[192:195],v[180:183],v[48:51]
ar v226,a50
ar v227,a51
mm v[48:51],v[196:199],v[164:167],v[48:51]
rt v[216:217],v243 offset:25472
rt v[218:219],v243 offset:26496
mm v[80:83],v[196:199],v[168:171],v[80:83]
ar v228,a52
ar v229,a53
mm v[80:83],v[192:195],v[184:187],v[80:83]
ld a[116:119],v245,s[52:55],s91 offen offset:3072 sc1
mm v[112:115],v[192:195],v[188:191],v[112:115]
ar v230,a54
ar v231,a55
mm v[112:115],v[196:199],v[172:175],v[112:115]
cv v248,v224,v225
cv v249,v226,v227
mm v[116:119],v[204:207],v[172:175],v[116:119]
wt lgkmcnt(0)
s_barrier
mm v[116:119],v[200:203],v[188:191],v[116:119]
ld a[120:123],v246,s[52:55],s91 offen offset:3072 sc1
mm v[84:87],v[200:203],v[184:187],v[84:87]
rt v[192:193],v240 offset:33792
rt v[194:195],v240 offset:34816
mm v[84:87],v[204:207],v[168:171],v[84:87]
rt v[196:197],v240 offset:42240
rt v[198:199],v240 offset:43264
mm v[52:55],v[204:207],v[164:167],v[52:55]
ds_write_b128 v238, a[20:23] offset:32768
mm v[52:55],v[200:203],v[180:183],v[52:55]
ld a[124:127],v247,s[52:55],s91 offen offset:3072 sc1
mm v[20:23],v[200:203],v[176:179],v[20:23]
cv v250,v228,v229
cv v251,v230,v231
mm v[20:23],v[204:207],v[160:163],v[20:23]
rt v[200:201],v241 offset:33792
rt v[202:203],v241 offset:34816
mm v[24:27],v[212:215],v[160:163],v[24:27]
aw a44,v248
aw a45,v249
mm v[24:27],v[208:211],v[176:179],v[24:27]
aw a46,v250
aw a47,v251
mm v[56:59],v[208:211],v[180:183],v[56:59]
mm v[56:59],v[212:215],v[164:167],v[56:59]
rt v[204:205],v241 offset:42240
rt v[206:207],v241 offset:43264
mm v[88:91],v[212:215],v[168:171],v[88:91]
mm v[88:91],v[208:211],v[184:187],v[88:91]
mm v[120:123],v[208:211],v[188:191],v[120:123]
mm v[120:123],v[212:215],v[172:175],v[120:123]
rt v[208:209],v242 offset:33792
rt v[210:211],v242 offset:34816
mm v[124:127],v[220:223],v[172:175],v[124:127]
mm v[124:127],v[216:219],v[188:191],v[124:127]
st a[28:31],v255,s[48:51],s18 offen offset:2048 sc1
mm v[92:95],v[216:219],v[184:187],v[92:95]
mm v[92:95],v[220:223],v[168:171],v[92:95]
rt v[212:213],v242 offset:42240
rt v[214:215],v242 offset:43264
mm v[60:63],v[220:223],v[164:167],v[60:63]
ds_write_b128 v238, a[24:27] offset:33792
mm v[60:63],v[216:219],v[180:183],v[60:63]
st a[32:35],v255,s[48:51],s18 offen offset:3072 sc1
mm v[28:31],v[216:219],v[176:179],v[28:31]
wt vmcnt(12) lgkmcnt(5)
mm v[28:31],v[220:223],v[160:163],v[28:31]
rt v[216:217],v243 offset:33792
rt v[218:219],v243 offset:34816
mm v[0:3],v[192:195],v[128:131],v[0:3]
ar v224,a56
ar v225,a57
mm v[0:3],v[196:199],a[200:203],v[0:3]
ar v226,a58
ar v227,a59
mm v[32:35],v[196:199],a[204:207],v[32:35]
ar v228,a60
ar v229,a61
mm v[32:35],v[192:195],v[140:143],v[32:35]
rt v[220:221],v243 offset:42240
rt v[222:223],v243 offset:43264
mm v[64:67],v[192:195],a[192:195],v[64:67]
ar v230,a62
ar v231,a63
mm v[64:67],v[196:199],v[148:151],v[64:67]
cv v248,v224,v225
cv v249,v226,v227
mm v[96:99],v[196:199],v[132:135],v[96:99]
cv v250,v228,v229
cv v251,v230,v231
mm v[96:99],v[192:195],a[196:199],v[96:99]
rt v[196:197],v240 offset:33920
rt v[198:199],v240 offset:34944
mm v[100:103],v[200:203],a[196:199],v[100:103]
aw a48,v248
aw a49,v249
mm v[100:103],v[204:207],v[132:135],v[100:103]
aw a50,v250
aw a51,v251
mm v[68:71],v[204:207],v[148:151],v[68:71]
v_add_u32 v244, s92, v244
v_add_u32 v246, s93, v246
mm v[68:71],v[200:203],a[192:195],v[68:71]
rt v[192:193],v240 offset:42368
rt v[194:195],v240 offset:43392
mm v[36:39],v[200:203],v[140:143],v[36:39]
ds_write_b128 v238, a[28:31] offset:34816
mm v[36:39],v[204:207],a[204:207],v[36:39]
v_add_u32 v245, 0x1000, v244
v_add_u32 v247, 0x1000, v246
mm v[4:7],v[204:207],a[200:203],v[4:7]
wt lgkmcnt(5)
s_lshl_b32 s91, s72, 13
mm v[4:7],v[200:203],v[128:131],v[4:7]
rt v[204:205],v241 offset:33920
rt v[206:207],v241 offset:34944
mm v[8:11],v[208:211],v[128:131],v[8:11]
s_lshl4_add_u32 s92, s76, s3
s_lshl3_add_u32 s92, s92, s20
mm v[8:11],v[212:215],a[200:203],v[8:11]
s_bitset1_b32 s20, 30
mm v[40:43],v[212:215],a[204:207],v[40:43]
mm v[40:43],v[208:211],v[140:143],v[40:43]
rt v[200:201],v241 offset:42368
rt v[202:203],v241 offset:43392
mm v[72:75],v[208:211],a[192:195],v[72:75]
mm v[72:75],v[212:215],v[148:151],v[72:75]
mm v[104:107],v[212:215],v[132:135],v[104:107]
s_mov_b32 s18, 4096
mm v[104:107],v[208:211],a[196:199],v[104:107]
rt v[212:213],v242 offset:33920
rt v[214:215],v242 offset:34944
mm v[108:111],v[216:219],a[196:199],v[108:111]
mm v[108:111],v[220:223],v[132:135],v[108:111]
st a[36:39],v255,s[48:51],s18 offen offset:0 sc1
mm v[76:79],v[220:223],v[148:151],v[76:79]
mm v[76:79],v[216:219],a[192:195],v[76:79]
rt v[208:209],v242 offset:42368
rt v[210:211],v242 offset:43392
mm v[44:47],v[216:219],v[140:143],v[44:47]
ds_write_b128 v238, a[32:35] offset:35840
mm v[44:47],v[220:223],a[204:207],v[44:47]
st a[40:43],v255,s[48:51],s18 offen offset:1024 sc1
mm v[12:15],v[220:223],a[200:203],v[12:15]
wt lgkmcnt(5)
mm v[12:15],v[216:219],v[128:131],v[12:15]
rt v[220:221],v243 offset:33920
rt v[222:223],v243 offset:34944
mm v[16:19],v[196:199],v[128:131],v[16:19]
s_mov_b64 s[52:53], s[86:87]
mm v[16:19],v[192:195],a[200:203],v[16:19]
s_bitcmp1_b32 21, s73
s_cselect_b32 s54, 16384, 0
s_cselect_b32 s86, s88, 1073741824
mm v[48:51],v[192:195],a[204:207],v[48:51]
s_lshl_b32 s89, s3, 14
v_add_u32 v248, s89, v255
mm v[48:51],v[196:199],v[140:143],v[48:51]
rt v[216:217],v243 offset:42368
rt v[218:219],v243 offset:43392
mm v[80:83],v[196:199],a[192:195],v[80:83]
ds_write_b128 v238, a[36:39] offset:36864
mm v[80:83],v[192:195],v[148:151],v[80:83]
s_add_u32 s86, s86, 262144
mm v[112:115],v[192:195],v[132:135],v[112:115]
s_cmp_eq_u32 s73, 0
s_cselect_b32 s86, s92, s86
s_cselect_b32 s92, 1073741824, s92
mm v[112:115],v[196:199],a[196:199],v[112:115]
s_mov_b32 s87, s92
s_add_u32 s89, s89, 16384
mm v[116:119],v[204:207],a[196:199],v[116:119]
wt lgkmcnt(1)
s_barrier
mm v[116:119],v[200:203],v[132:135],v[116:119]
s_lshl_b32 m0, s3, 12
s_add_u32 m0, m0, 131072
mm v[84:87],v[200:203],v[148:151],v[84:87]
rt v[192:193],v240 offset:50688
rt v[194:195],v240 offset:51712
mm v[84:87],v[204:207],a[192:195],v[84:87]
rt v[196:197],v240 offset:59136
rt v[198:199],v240 offset:60160
mm v[52:55],v[204:207],v[140:143],v[52:55]
ds_write_b128 v238, a[40:43] offset:37888
mm v[52:55],v[200:203],a[204:207],v[52:55]
v_bfe_u32 v251, v255, 2, 5
v_and_b32 v224, 0x380, v255
mm v[20:23],v[200:203],a[200:203],v[20:23]
v_add_u32 v251, v251, v224
v_and_b32 v224, 16, v255
mm v[20:23],v[204:207],v[128:131],v[20:23]
rt v[200:201],v241 offset:50688
rt v[202:203],v241 offset:51712
mm v[24:27],v[212:215],v[128:131],v[24:27]
v_cmp_eq_u32 vcc, 0, v224
mm v[24:27],v[208:211],a[200:203],v[24:27]
v_cndmask_b32 v249, v253, v252, vcc
mm v[56:59],v[208:211],a[204:207],v[56:59]
mm v[56:59],v[212:215],v[140:143],v[56:59]
rt v[204:205],v241 offset:59136
rt v[206:207],v241 offset:60160
mm v[88:91],v[212:215],a[192:195],v[88:91]
ds_write_b128 v238, a[44:47] offset:38912
mm v[88:91],v[208:211],v[148:151],v[88:91]
st a[44:47],v255,s[48:51],s18 offen offset:2048 sc1
mm v[120:123],v[208:211],v[132:135],v[120:123]
mm v[120:123],v[212:215],a[196:199],v[120:123]
rt v[208:209],v242 offset:50688
rt v[210:211],v242 offset:51712
mm v[124:127],v[220:223],a[196:199],v[124:127]
mm v[124:127],v[216:219],v[132:135],v[124:127]
mm v[92:95],v[216:219],v[148:151],v[92:95]
.La5_LW1_14:
s_cbranch_execz .La5_LW1_R0
mm v[92:95],v[220:223],a[192:195],v[92:95]
rt v[212:213],v242 offset:59136
rt v[214:215],v242 offset:60160
mm v[60:63],v[220:223],v[140:143],v[60:63]
mm v[60:63],v[216:219],a[204:207],v[60:63]
st a[48:51],v255,s[48:51],s18 offen offset:3072 sc1
mm v[28:31],v[216:219],a[200:203],v[28:31]
wt vmcnt(6) lgkmcnt(5)
mm v[28:31],v[220:223],v[128:131],v[28:31]
rt v[216:217],v243 offset:50688
rt v[218:219],v243 offset:51712
mm v[0:3],v[192:195],a[96:99],v[0:3]
mm v[0:3],v[196:199],a[112:115],v[0:3]
mm v[32:35],v[196:199],a[116:119],v[32:35]
mm v[32:35],v[192:195],a[100:103],v[32:35]
rt v[220:221],v243 offset:59136
rt v[222:223],v243 offset:60160
mm v[64:67],v[192:195],a[104:107],v[64:67]
mm v[64:67],v[196:199],a[120:123],v[64:67]
mm v[96:99],v[196:199],a[124:127],v[96:99]
mm v[96:99],v[192:195],a[108:111],v[96:99]
rt v[196:197],v240 offset:50816
rt v[198:199],v240 offset:51840
mm v[100:103],v[200:203],a[108:111],v[100:103]
mm v[100:103],v[204:207],a[124:127],v[100:103]
mm v[68:71],v[204:207],a[120:123],v[68:71]
mm v[68:71],v[200:203],a[104:107],v[68:71]
rt v[192:193],v240 offset:59264
rt v[194:195],v240 offset:60288
mm v[36:39],v[200:203],a[100:103],v[36:39]
mm v[36:39],v[204:207],a[116:119],v[36:39]
mm v[4:7],v[204:207],a[112:115],v[4:7]
wt lgkmcnt(4)
mm v[4:7],v[200:203],a[96:99],v[4:7]
rt v[204:205],v241 offset:50816
rt v[206:207],v241 offset:51840
mm v[8:11],v[208:211],a[96:99],v[8:11]
mm v[8:11],v[212:215],a[112:115],v[8:11]
mm v[40:43],v[212:215],a[116:119],v[40:43]
cv v128,v0,v1
cv v129,v2,v3
mm v[40:43],v[208:211],a[100:103],v[40:43]
rt v[200:201],v241 offset:59264
rt v[202:203],v241 offset:60288
mm v[72:75],v[208:211],a[104:107],v[72:75]
cv v130,v4,v5
cv v131,v6,v7
mm v[72:75],v[212:215],a[120:123],v[72:75]
cv v144,v32,v33
cv v145,v34,v35
mm v[104:107],v[212:215],a[124:127],v[104:107]
cv v146,v36,v37
cv v147,v38,v39
mm v[104:107],v[208:211],a[108:111],v[104:107]
rt v[212:213],v242 offset:50816
rt v[214:215],v242 offset:51840
mm v[108:111],v[216:219],a[108:111],v[108:111]
cv v160,v64,v65
cv v161,v66,v67
mm v[108:111],v[220:223],a[124:127],v[108:111]
cv v162,v68,v69
cv v163,v70,v71
mm v[76:79],v[220:223],a[120:123],v[76:79]
cv v176,v96,v97
cv v177,v98,v99
mm v[76:79],v[216:219],a[104:107],v[76:79]
rt v[208:209],v242 offset:59264
rt v[210:211],v242 offset:60288
mm v[44:47],v[216:219],a[100:103],v[44:47]
cv v178,v100,v101
cv v179,v102,v103
mm v[44:47],v[220:223],a[116:119],v[44:47]
mm v[12:15],v[220:223],a[112:115],v[12:15]
wt lgkmcnt(4)
mm v[12:15],v[216:219],a[96:99],v[12:15]
rt v[220:221],v243 offset:50816
rt v[222:223],v243 offset:51840
mm v[16:19],v[196:199],a[96:99],v[16:19]
v_mul_u32_u24 v241, 0x5555, v224
v_lshrrev_b32 v241, 4, v241
v_add_u32 v241, 0x8888, v241
mm v[16:19],v[192:195],a[112:115],v[16:19]
st v[128:131],v255,s[52:55],0 offen offset:0 sc1
mm v[48:51],v[192:195],a[116:119],v[48:51]
cv v132,v8,v9
cv v133,v10,v11
cv v181,v106,v107
mm v[48:51],v[196:199],a[100:103],v[48:51]
rt v[216:217],v243 offset:59264
rt v[218:219],v243 offset:60288
cv v182,v108,v109
mm v[80:83],v[196:199],a[104:107],v[80:83]
cv v134,v12,v13
cv v135,v14,v15
cv v183,v110,v111
mm v[80:83],v[192:195],a[120:123],v[80:83]
st v[144:147],v255,s[52:55],s95 offen offset:0 sc1
ar v34,a238
mm v[112:115],v[192:195],a[124:127],v[112:115]
cv v148,v40,v41
cv v149,v42,v43
ar v35,a239
mm v[112:115],v[196:199],a[108:111],v[112:115]
cv v150,v44,v45
cv v151,v46,v47
v_mov_b32 v36, 0xff800000
mm v[116:119],v[204:207],a[108:111],v[116:119]
wt lgkmcnt(0)
s_barrier
v_mov_b32 v37, 0xff800000
mm v[116:119],v[200:203],a[124:127],v[116:119]
st v[160:163],v255,s[52:55],s96 offen offset:0 sc1
v_mov_b32 v38, 0xff800000
mm v[84:87],v[200:203],a[120:123],v[84:87]
cv v164,v72,v73
cv v165,v74,v75
v_mov_b32 v39, 0xff800000
mm v[84:87],v[204:207],a[104:107],v[84:87]
rd a[64:67],v248 offset:0
rd a[68:71],v248 offset:1024
mm v[52:55],v[204:207],a[100:103],v[52:55]
cv v166,v76,v77
cv v167,v78,v79
mm v[52:55],v[200:203],a[116:119],v[52:55]
st v[176:179],v255,s[52:55],s97 offen offset:0 sc1
mm v[20:23],v[200:203],a[112:115],v[20:23]
s_nop 0
cv v180,v104,v105
mm v[20:23],v[204:207],a[96:99],v[20:23]
rd a[72:75],v248 offset:2048
rd a[76:79],v248 offset:3072
mm v[24:27],v[212:215],a[96:99],v[24:27]
ar v32,a236
ar v33,a237
mm v[24:27],v[208:211],a[112:115],v[24:27]
st v[132:135],v255,s[52:55],0 offen offset:1024 sc1
mm v[56:59],v[208:211],a[116:119],v[56:59]
cv v136,v16,v17
cv v137,v18,v19
mm v[56:59],v[212:215],a[100:103],v[56:59]
rd a[80:83],v248 offset:4096
rd a[84:87],v248 offset:5120
mm v[88:91],v[212:215],a[104:107],v[88:91]
cv v138,v20,v21
cv v139,v22,v23
mm v[88:91],v[208:211],a[120:123],v[88:91]
st v[148:151],v255,s[52:55],s95 offen offset:1024 sc1
mm v[120:123],v[208:211],a[124:127],v[120:123]
cv v152,v48,v49
cv v153,v50,v51
mm v[120:123],v[212:215],a[108:111],v[120:123]
rd a[88:91],v248 offset:6144
rd a[92:95],v248 offset:7168
mm v[124:127],v[220:223],a[108:111],v[124:127]
ds_write_b128 v238, a[48:51] offset:39936
mm v[124:127],v[216:219],a[124:127],v[124:127]
st v[164:167],v255,s[52:55],s96 offen offset:1024 sc1
mm v[92:95],v[216:219],a[120:123],v[92:95]
cv v154,v52,v53
cv v155,v54,v55
mm v[92:95],v[220:223],a[104:107],v[92:95]
cv v168,v80,v81
cv v169,v82,v83
mm v[60:63],v[220:223],a[100:103],v[60:63]
cv v170,v84,v85
cv v171,v86,v87
mm v[60:63],v[216:219],a[116:119],v[60:63]
st v[180:183],v255,s[52:55],s97 offen offset:1024 sc1
mm v[28:31],v[216:219],a[112:115],v[28:31]
cv v184,v112,v113
cv v185,v114,v115
mm v[28:31],v[220:223],a[96:99],v[28:31]
cv v186,v116,v117
cv v187,v118,v119
wt lgkmcnt(1)
mm v[208:211],a[64:67],v[160:163],v[32:35]
mm v[216:219],a[64:67],v[176:179],0
st v[136:139],v255,s[52:55],0 offen offset:2048 sc1
mm v[212:215],a[80:83],v[160:163],v[36:39]
cv v172,v88,v89
cv v173,v90,v91
cv v174,v92,v93
mm v[220:223],a[80:83],v[176:179],v[32:35]
cv v175,v94,v95
cv v188,v120,v121
cv v189,v122,v123
mm v[208:211],a[68:71],v[164:167],v[208:211]
cv v190,v124,v125
cv v191,v126,v127
cv v140,v24,v25
mm v[216:219],a[68:71],v[180:183],v[216:219]
st v[152:155],v255,s[52:55],s95 offen offset:2048 sc1
cv v141,v26,v27
mm v[212:215],a[84:87],v[164:167],v[212:215]
cv v142,v28,v29
cv v143,v30,v31
cv v156,v56,v57
mm v[220:223],a[84:87],v[180:183],v[220:223]
cv v157,v58,v59
cv v158,v60,v61
cv v159,v62,v63
mm v[208:211],a[72:75],v[168:171],v[208:211]
mm v[216:219],a[72:75],v[184:187],v[216:219]
st v[168:171],v255,s[52:55],s96 offen offset:2048 sc1
mm v[212:215],a[88:91],v[168:171],v[212:215]
mm v[220:223],a[88:91],v[184:187],v[220:223]
mm v[208:211],a[76:79],v[172:175],v[208:211]
mm v[216:219],a[76:79],v[188:191],v[216:219]
st v[184:187],v255,s[52:55],s97 offen offset:2048 sc1
mm v[212:215],a[92:95],v[172:175],v[212:215]
rd a[64:67],v248 offset:16384
mm v[220:223],a[92:95],v[188:191],v[220:223]
rd a[96:99],v248 offset:8192
ex v0,v208
wt vmcnt(12)
buffer_store_dwordx2 v[252:253], off, s[64:67], s87 sc1
mm v[64:67],a[232:235],a[232:235],0
rd a[68:71],v248 offset:17408
ex v1,v209
mm v[68:71],a[232:235],a[232:235],0
rd a[100:103],v248 offset:9216
st v[140:143],v255,s[52:55],0 offen offset:3072 sc1
mm v[72:75],a[232:235],a[232:235],0
rd a[72:75],v248 offset:18432
ex v2,v210
mm v[76:79],a[232:235],a[232:235],0
rd a[104:107],v248 offset:10240
ex v3,v211
mm v[80:83],a[232:235],a[232:235],0
rd a[76:79],v248 offset:19456
ex v8,v216
mm v[84:87],a[232:235],a[232:235],0
rd a[108:111],v248 offset:11264
st v[156:159],v255,s[52:55],s95 offen offset:3072 sc1
mm v[88:91],a[232:235],a[232:235],0
rd a[80:83],v248 offset:20480
ex v9,v217
mm v[92:95],a[232:235],a[232:235],0
rd a[112:115],v248 offset:12288
ex v10,v218
mm v[96:99],a[232:235],a[232:235],0
rd a[84:87],v248 offset:21504
ex v11,v219
mm v[100:103],a[232:235],a[232:235],0
rd a[116:119],v248 offset:13312
st v[172:175],v255,s[52:55],s96 offen offset:3072 sc1
mm v[104:107],a[232:235],a[232:235],0
rd a[88:91],v248 offset:22528
ex v4,v212
mm v[108:111],a[232:235],a[232:235],0
rd a[120:123],v248 offset:14336
ex v5,v213
mm v[112:115],a[232:235],a[232:235],0
rd a[92:95],v248 offset:23552
ex v6,v214
mm v[116:119],a[232:235],a[232:235],0
rd a[124:127],v248 offset:15360
st v[188:191],v255,s[52:55],s97 offen offset:3072 sc1
ex v7,v215
mm v[120:123],a[232:235],a[232:235],0
ex v12,v220
mm v[124:127],a[232:235],a[232:235],0
aw a192,0
aw a193,0
aw a194,0
aw a195,0
aw a196,0
aw a197,0
aw a198,0
aw a199,0
v_add_u32 v248, 0x4000, v248
s_cmp_eq_u32 s3, 3
s_cbranch_scc1 .La5_LA_FH
.p2align 6
; ---- attention, 32 keys per window: S = K Q^T (causal mask through SrcC), P = exp2(S), O += V^T P, row sums l += 1 P; here the later 32 rows only
.La5_LA_HH:
wt lgkmcnt(1)
mm v[208:211],a[64:67],v[160:163],0
ex v13,v221
mm v[208:211],a[68:71],v[164:167],v[208:211]
cv v232,v0,v1
cv v233,v2,v3
mm v[208:211],a[72:75],v[168:171],v[208:211]
ex v14,v222
mm v[208:211],a[76:79],v[172:175],v[208:211]
cv v236,v8,v9
cv v237,v10,v11
mm v[216:219],a[76:79],v[188:191],0
ex v15,v223
mm v[216:219],a[72:75],v[184:187],v[216:219]
cv v234,v4,v5
cv v235,v6,v7
mm v[216:219],a[68:71],v[180:183],v[216:219]
ex v0,v208
mm v[216:219],a[64:67],v[176:179],v[216:219]
cv v238,v12,v13
cv v239,v14,v15
mm v[212:215],a[80:83],v[160:163],0
ex v1,v209
mm v[212:215],a[84:87],v[164:167],v[212:215]
rd a[64:67],v248 offset:16384
rd a[68:71],v248 offset:17408
mm v[212:215],a[88:91],v[168:171],v[212:215]
ex v2,v210
mm v[212:215],a[92:95],v[172:175],v[212:215]
rd a[72:75],v248 offset:18432
rd a[76:79],v248 offset:19456
mm v[220:223],a[92:95],v[188:191],0
ex v3,v211
mm v[220:223],a[88:91],v[184:187],v[220:223]
rd a[92:95],v248 offset:23552
mm v[220:223],a[84:87],v[180:183],v[220:223]
ex v8,v216
mm v[220:223],a[80:83],v[176:179],v[220:223]
wt lgkmcnt(5)
rd a[88:91],v248 offset:22528
mm v[64:67],a[96:99],v[232:235],v[64:67]
ex v9,v217
mm v[96:99],a[96:99],v[236:239],v[96:99]
rd a[96:99],v248 offset:8192
rd a[84:87],v248 offset:21504
mm v[100:103],a[100:103],v[236:239],v[100:103]
ex v10,v218
mm v[68:71],a[100:103],v[232:235],v[68:71]
rd a[100:103],v248 offset:9216
rd a[80:83],v248 offset:20480
mm v[72:75],a[104:107],v[232:235],v[72:75]
ex v11,v219
mm v[104:107],a[104:107],v[236:239],v[104:107]
rd a[104:107],v248 offset:10240
mm v[108:111],a[108:111],v[236:239],v[108:111]
ex v4,v212
mm v[76:79],a[108:111],v[232:235],v[76:79]
rd a[108:111],v248 offset:11264
s_add_u32 s85, s85, 1
mm v[80:83],a[112:115],v[232:235],v[80:83]
ex v5,v213
mm v[112:115],a[112:115],v[236:239],v[112:115]
rd a[112:115],v248 offset:12288
s_add_u32 s89, s89, 32768
mm v[116:119],a[116:119],v[236:239],v[116:119]
ex v6,v214
mm v[84:87],a[116:119],v[232:235],v[84:87]
rd a[116:119],v248 offset:13312
s_add_u32 s92, s85, s3
mm v[88:91],a[120:123],v[232:235],v[88:91]
ex v7,v215
mm v[120:123],a[120:123],v[236:239],v[120:123]
rd a[120:123],v248 offset:14336
buffer_load_dword v250, v251, s[64:67], s20 offen sc1
mm v[124:127],a[124:127],v[236:239],v[124:127]
ex v12,v220
mm v[92:95],a[124:127],v[232:235],v[92:95]
rd a[124:127],v248 offset:15360
s_barrier
v_smfmac_f32_16x16x64_bf16 a[196:199], a[208:211], v[232:239], v241
wt lgkmcnt(6)
mm v[208:211],a[64:67],v[160:163],0
ex v13,v221
mm v[208:211],a[68:71],v[164:167],v[208:211]
cv v232,v0,v1
cv v233,v2,v3
mm v[208:211],a[72:75],v[168:171],v[208:211]
ex v14,v222
mm v[208:211],a[76:79],v[172:175],v[208:211]
cv v236,v8,v9
cv v237,v10,v11
mm v[216:219],a[76:79],v[188:191],0
ex v15,v223
mm v[216:219],a[72:75],v[184:187],v[216:219]
cv v234,v4,v5
cv v235,v6,v7
mm v[216:219],a[68:71],v[180:183],v[216:219]
ex v0,v208
mm v[216:219],a[64:67],v[176:179],v[216:219]
cv v238,v12,v13
cv v239,v14,v15
mm v[212:215],a[80:83],v[160:163],0
ex v1,v209
mm v[212:215],a[84:87],v[164:167],v[212:215]
rd a[64:67],v248 offset:32768
rd a[68:71],v248 offset:33792
mm v[212:215],a[88:91],v[168:171],v[212:215]
ex v2,v210
mm v[212:215],a[92:95],v[172:175],v[212:215]
rd a[72:75],v248 offset:34816
rd a[76:79],v248 offset:35840
mm v[220:223],a[92:95],v[188:191],0
ex v3,v211
mm v[220:223],a[88:91],v[184:187],v[220:223]
rd a[92:95],v248 offset:39936
s_bitcmp1_b64 s[98:99], s85
mm v[220:223],a[84:87],v[180:183],v[220:223]
ex v8,v216
mm v[220:223],a[80:83],v[176:179],v[220:223]
wt lgkmcnt(5)
rd a[88:91],v248 offset:38912
mm v[64:67],a[96:99],v[232:235],v[64:67]
ex v9,v217
mm v[96:99],a[96:99],v[236:239],v[96:99]
rd a[96:99],v248 offset:24576
rd a[84:87],v248 offset:37888
mm v[100:103],a[100:103],v[236:239],v[100:103]
ex v10,v218
mm v[68:71],a[100:103],v[232:235],v[68:71]
rd a[100:103],v248 offset:25600
rd a[80:83],v248 offset:36864
mm v[72:75],a[104:107],v[232:235],v[72:75]
ex v11,v219
mm v[104:107],a[104:107],v[236:239],v[104:107]
rd a[104:107],v248 offset:26624
mm v[108:111],a[108:111],v[236:239],v[108:111]
ex v4,v212
mm v[76:79],a[108:111],v[232:235],v[76:79]
rd a[108:111],v248 offset:27648
mm v[80:83],a[112:115],v[232:235],v[80:83]
ex v5,v213
mm v[112:115],a[112:115],v[236:239],v[112:115]
rd a[112:115],v248 offset:28672
mm v[116:119],a[116:119],v[236:239],v[116:119]
ex v6,v214
mm v[84:87],a[116:119],v[232:235],v[84:87]
rd a[116:119],v248 offset:29696
wt vmcnt(1)
buffer_store_dwordx2 v[252:253], off, s[64:67], s86 sc1
s_mov_b32 s86, 1073741824
s_bitset0_b32 s20, 30
mm v[88:91],a[120:123],v[232:235],v[88:91]
ex v7,v215
mm v[120:123],a[120:123],v[236:239],v[120:123]
rd a[120:123],v248 offset:30720
mm v[124:127],a[124:127],v[236:239],v[124:127]
v_exp_f32_e64 v12, v220
mm v[92:95],a[124:127],v[232:235],v[92:95]
rd a[124:127],v248 offset:31744
v_smfmac_f32_16x16x64_bf16 a[196:199], a[208:211], v[232:239], v241
v_add_u32 v248, 0x8000, v248
s_cbranch_scc0 .La5_LA_HH1
s_call_b64 s[24:25], .La5_LA_EVENT
s_add_u32 s92, s85, s3
.La5_LA_HH1:
s_cmp_lt_u32 s92, 3
s_cbranch_scc1 .La5_LA_HH
; ---- first window with both row groups (diagonal of the earlier group)
.La5_LA_FH:
.La5_LW1_15:
s_cbranch_execz .La5_LW1_R0
wt lgkmcnt(9)
mm v[192:195],a[64:67],v[128:131],v[32:35]
ex v13,v221
mm v[192:195],a[68:71],v[132:135],v[192:195]
cv v232,v0,v1
cv v233,v2,v3
mm v[192:195],a[72:75],v[136:139],v[192:195]
ex v14,v222
mm v[192:195],a[76:79],v[140:143],v[192:195]
ld v255,s[60:63],s17 offen offset:0 sc1 lds
mm v[200:203],a[76:79],v[156:159],0
ex v15,v223
mm v[200:203],a[72:75],v[152:155],v[200:203]
cv v236,v8,v9
cv v237,v10,v11
mm v[200:203],a[68:71],v[148:151],v[200:203]
ex v192,v192
mm v[200:203],a[64:67],v[144:147],v[200:203]
wt lgkmcnt(1)
cv v234,v4,v5
mm v[196:199],a[80:83],v[128:131],v[36:39]
ex v193,v193
mm v[196:199],a[84:87],v[132:135],v[196:199]
cv v224,v192,v193
s_add_u32 s85, s85, 1
mm v[196:199],a[88:91],v[136:139],v[196:199]
ex v194,v194
mm v[196:199],a[92:95],v[140:143],v[196:199]
ld v255,s[60:63],s17 offen offset:1024 sc1 lds
mm v[204:207],a[92:95],v[156:159],v[32:35]
ex v195,v195
mm v[204:207],a[88:91],v[152:155],v[204:207]
cv v225,v194,v195
s_add_u32 s89, s89, 16384
mm v[204:207],a[84:87],v[148:151],v[204:207]
ex v200,v200
mm v[204:207],a[80:83],v[144:147],v[204:207]
ld v255,s[60:63],s17 offen offset:2048 sc1 lds
mm v[208:211],a[64:67],v[160:163],0
ex v201,v201
mm v[208:211],a[68:71],v[164:167],v[208:211]
cv v228,v200,v201
s_and_b32 s89, s89, 0x1ffff
mm v[208:211],a[72:75],v[168:171],v[208:211]
ex v202,v202
mm v[208:211],a[76:79],v[172:175],v[208:211]
ld v255,s[60:63],s17 offen offset:3072 sc1 lds
mm v[216:219],a[76:79],v[188:191],0
ex v203,v203
mm v[216:219],a[72:75],v[184:187],v[216:219]
cv v229,v202,v203
mm v[216:219],a[68:71],v[180:183],v[216:219]
ex v196,v196
mm v[216:219],a[64:67],v[176:179],v[216:219]
cv v235,v6,v7
cv v238,v12,v13
mm v[212:215],a[80:83],v[160:163],0
ex v197,v197
s_add_u32 s17, s17, 8192
mm v[212:215],a[84:87],v[164:167],v[212:215]
cv v226,v196,v197
s_add_u32 m0, m0, 16384
s_and_b32 m0, m0, s78
mm v[212:215],a[88:91],v[168:171],v[212:215]
ex v198,v198
mm v[212:215],a[92:95],v[172:175],v[212:215]
cv v239,v14,v15
wt lgkmcnt(0)
mm v[220:223],a[92:95],v[188:191],0
ex v199,v199
mm v[220:223],a[88:91],v[184:187],v[220:223]
cv v227,v198,v199
mm v[220:223],a[84:87],v[180:183],v[220:223]
ex v204,v204
mm v[220:223],a[80:83],v[176:179],v[220:223]
wt vmcnt(10)
s_barrier
mm v[64:67],a[96:99],v[232:235],v[64:67]
ex v205,v205
mm v[96:99],a[96:99],v[236:239],v[96:99]
rd a[96:99],v248 offset:8192
cv v230,v204,v205
mm v[100:103],a[100:103],v[236:239],v[100:103]
ex v206,v206
mm v[68:71],a[100:103],v[232:235],v[68:71]
rd a[100:103],v248 offset:9216
buffer_load_dword v250, v251, s[64:67], s20 offen sc1
mm v[72:75],a[104:107],v[232:235],v[72:75]
ex v207,v207
mm v[104:107],a[104:107],v[236:239],v[104:107]
rd a[104:107],v248 offset:10240
cv v231,v206,v207
mm v[108:111],a[108:111],v[236:239],v[108:111]
ex v208,v208
mm v[76:79],a[108:111],v[232:235],v[76:79]
rd a[108:111],v248 offset:11264
mm v[80:83],a[112:115],v[232:235],v[80:83]
ex v209,v209
mm v[112:115],a[112:115],v[236:239],v[112:115]
rd a[112:115],v248 offset:12288
mm v[116:119],a[116:119],v[236:239],v[116:119]
ex v210,v210
mm v[84:87],a[116:119],v[232:235],v[84:87]
rd a[116:119],v248 offset:13312
mm v[88:91],a[120:123],v[232:235],v[88:91]
ex v211,v211
mm v[120:123],a[120:123],v[236:239],v[120:123]
rd a[120:123],v248 offset:14336
s_bitcmp1_b64 s[98:99], s85
mm v[124:127],a[124:127],v[236:239],v[124:127]
ex v216,v216
mm v[92:95],a[124:127],v[232:235],v[92:95]
rd a[124:127],v248 offset:15360
v_or_b32 v248, s89, v255
v_smfmac_f32_16x16x64_bf16 a[196:199], a[208:211], v[232:239], v241
wt lgkmcnt(4)
v_smfmac_f32_16x16x64_bf16 a[192:195], a[208:211], v[224:231], v241
rd a[64:67],v248 offset:0
mm v[0:3],a[96:99],v[224:227],0
ex v217,v217
mm v[32:35],a[96:99],v[228:231],0
rd a[68:71],v248 offset:1024
cv v232,v208,v209
.La5_LW2_0:
s_cbranch_execz .La5_LW2_R0
mm v[36:39],a[100:103],v[228:231],0
ex v218,v218
mm v[4:7],a[100:103],v[224:227],0
rd a[72:75],v248 offset:2048
cv v233,v210,v211
mm v[8:11],a[104:107],v[224:227],0
ex v219,v219
mm v[40:43],a[104:107],v[228:231],0
rd a[76:79],v248 offset:3072
cv v236,v216,v217
mm v[44:47],a[108:111],v[228:231],0
ex v212,v212
mm v[12:15],a[108:111],v[224:227],0
rd a[80:83],v248 offset:4096
wt lgkmcnt(5)
mm v[16:19],a[112:115],v[224:227],0
ex v213,v213
mm v[48:51],a[112:115],v[228:231],0
rd a[84:87],v248 offset:5120
cv v237,v218,v219
mm v[52:55],a[116:119],v[228:231],0
ex v214,v214
mm v[20:23],a[116:119],v[224:227],0
rd a[88:91],v248 offset:6144
cv v234,v212,v213
mm v[24:27],a[120:123],v[224:227],0
ex v215,v215
mm v[56:59],a[120:123],v[228:231],0
rd a[92:95],v248 offset:7168
cv v235,v214,v215
mm v[60:63],a[124:127],v[228:231],0
ex v220,v220
mm v[28:31],a[124:127],v[224:227],0
wt vmcnt(5)
buffer_store_dwordx2 v[252:253], off, s[64:67], s86 sc1
s_mov_b32 s86, 1073741824
s_bitset0_b32 s20, 30
s_cbranch_scc1 .La5_LA_FE0
.La5_LA_FS0:
wt lgkmcnt(4)
mm v[192:195],a[64:67],v[128:131],0
ex v221,v221
mm v[192:195],a[68:71],v[132:135],v[192:195]
cv v238,v220,v221
s_add_u32 s85, s85, 1
mm v[192:195],a[72:75],v[136:139],v[192:195]
ex v222,v222
mm v[192:195],a[76:79],v[140:143],v[192:195]
ld v255,s[60:63],s17 offen offset:0 sc1 lds
mm v[200:203],a[76:79],v[156:159],0
ex v223,v223
mm v[200:203],a[72:75],v[152:155],v[200:203]
cv v239,v222,v223
s_add_u32 s89, s89, 16384
mm v[200:203],a[68:71],v[148:151],v[200:203]
ex v192,v192
mm v[200:203],a[64:67],v[144:147],v[200:203]
wt lgkmcnt(0)
s_and_b32 s89, s89, 0x1ffff
mm v[196:199],a[80:83],v[128:131],0
ex v193,v193
mm v[196:199],a[84:87],v[132:135],v[196:199]
cv v224,v192,v193
mm v[196:199],a[88:91],v[136:139],v[196:199]
ex v194,v194
mm v[196:199],a[92:95],v[140:143],v[196:199]
ld v255,s[60:63],s17 offen offset:1024 sc1 lds
mm v[204:207],a[92:95],v[156:159],0
ex v195,v195
mm v[204:207],a[88:91],v[152:155],v[204:207]
cv v225,v194,v195
mm v[204:207],a[84:87],v[148:151],v[204:207]
ex v200,v200
mm v[204:207],a[80:83],v[144:147],v[204:207]
ld v255,s[60:63],s17 offen offset:2048 sc1 lds
mm v[208:211],a[64:67],v[160:163],0
ex v201,v201
mm v[208:211],a[68:71],v[164:167],v[208:211]
cv v228,v200,v201
mm v[208:211],a[72:75],v[168:171],v[208:211]
ex v202,v202
mm v[208:211],a[76:79],v[172:175],v[208:211]
ld v255,s[60:63],s17 offen offset:3072 sc1 lds
mm v[216:219],a[76:79],v[188:191],0
ex v203,v203
mm v[216:219],a[72:75],v[184:187],v[216:219]
cv v229,v202,v203
mm v[216:219],a[68:71],v[180:183],v[216:219]
ex v196,v196
mm v[216:219],a[64:67],v[176:179],v[216:219]
buffer_load_dword a253, v254, s[36:39], s90 offen
mm v[212:215],a[80:83],v[160:163],0
ex v197,v197
mm v[212:215],a[84:87],v[164:167],v[212:215]
cv v226,v196,v197
s_add_u32 s17, s17, 8192
mm v[212:215],a[88:91],v[168:171],v[212:215]
ex v198,v198
mm v[212:215],a[92:95],v[172:175],v[212:215]
s_add_u32 m0, m0, 16384
s_and_b32 m0, m0, s78
mm v[220:223],a[92:95],v[188:191],0
ex v199,v199
mm v[220:223],a[88:91],v[184:187],v[220:223]
cv v227,v198,v199
s_add_u32 s90, s90, s21
mm v[220:223],a[84:87],v[180:183],v[220:223]
ex v204,v204
mm v[220:223],a[80:83],v[176:179],v[220:223]
wt vmcnt(11)
s_barrier
mm v[64:67],a[96:99],v[232:235],v[64:67]
ex v205,v205
mm v[96:99],a[96:99],v[236:239],v[96:99]
rd a[96:99],v248 offset:8192
cv v230,v204,v205
mm v[100:103],a[100:103],v[236:239],v[100:103]
ex v206,v206
mm v[68:71],a[100:103],v[232:235],v[68:71]
rd a[100:103],v248 offset:9216
buffer_load_dword v250, v251, s[64:67], s20 offen sc1
mm v[72:75],a[104:107],v[232:235],v[72:75]
ex v207,v207
mm v[104:107],a[104:107],v[236:239],v[104:107]
rd a[104:107],v248 offset:10240
cv v231,v206,v207
mm v[108:111],a[108:111],v[236:239],v[108:111]
ex v208,v208
mm v[76:79],a[108:111],v[232:235],v[76:79]
rd a[108:111],v248 offset:11264
mm v[80:83],a[112:115],v[232:235],v[80:83]
ex v209,v209
mm v[112:115],a[112:115],v[236:239],v[112:115]
rd a[112:115],v248 offset:12288
mm v[116:119],a[116:119],v[236:239],v[116:119]
ex v210,v210
mm v[84:87],a[116:119],v[232:235],v[84:87]
rd a[116:119],v248 offset:13312
mm v[88:91],a[120:123],v[232:235],v[88:91]
ex v211,v211
mm v[120:123],a[120:123],v[236:239],v[120:123]
rd a[120:123],v248 offset:14336
s_bitcmp1_b64 s[98:99], s85
mm v[124:127],a[124:127],v[236:239],v[124:127]
ex v216,v216
mm v[92:95],a[124:127],v[232:235],v[92:95]
rd a[124:127],v248 offset:15360
v_or_b32 v248, s89, v255
v_smfmac_f32_16x16x64_bf16 a[196:199], a[208:211], v[232:239], v241
wt lgkmcnt(4)
v_smfmac_f32_16x16x64_bf16 a[192:195], a[208:211], v[224:231], v241
rd a[64:67],v248 offset:0
mm v[0:3],a[96:99],v[224:227],v[0:3]
ex v217,v217
mm v[32:35],a[96:99],v[228:231],v[32:35]
rd a[68:71],v248 offset:1024
cv v232,v208,v209
mm v[36:39],a[100:103],v[228:231],v[36:39]
ex v218,v218
mm v[4:7],a[100:103],v[224:227],v[4:7]
rd a[72:75],v248 offset:2048
cv v233,v210,v211
mm v[8:11],a[104:107],v[224:227],v[8:11]
ex v219,v219
mm v[40:43],a[104:107],v[228:231],v[40:43]
rd a[76:79],v248 offset:3072
cv v236,v216,v217
mm v[44:47],a[108:111],v[228:231],v[44:47]
ex v212,v212
mm v[12:15],a[108:111],v[224:227],v[12:15]
rd a[80:83],v248 offset:4096
wt lgkmcnt(5)
mm v[16:19],a[112:115],v[224:227],v[16:19]
ex v213,v213
mm v[48:51],a[112:115],v[228:231],v[48:51]
rd a[84:87],v248 offset:5120
cv v237,v218,v219
mm v[52:55],a[116:119],v[228:231],v[52:55]
ex v214,v214
mm v[20:23],a[116:119],v[224:227],v[20:23]
rd a[88:91],v248 offset:6144
cv v234,v212,v213
mm v[24:27],a[120:123],v[224:227],v[24:27]
ex v215,v215
mm v[56:59],a[120:123],v[228:231],v[56:59]
rd a[92:95],v248 offset:7168
cv v235,v214,v215
mm v[60:63],a[124:127],v[228:231],v[60:63]
ex v220,v220
mm v[28:31],a[124:127],v[224:227],v[28:31]
s_cbranch_scc1 .La5_LA_F1E2
.p2align 6
; ---- steady windows
.La5_LA_F1:
wt lgkmcnt(4)
mm v[192:195],a[64:67],v[128:131],0
ex v221,v221
mm v[192:195],a[68:71],v[132:135],v[192:195]
cv v238,v220,v221
s_add_u32 s85, s85, 1
mm v[192:195],a[72:75],v[136:139],v[192:195]
ex v222,v222
mm v[192:195],a[76:79],v[140:143],v[192:195]
ld v255,s[60:63],s17 offen offset:0 sc1 lds
mm v[200:203],a[76:79],v[156:159],0
ex v223,v223
mm v[200:203],a[72:75],v[152:155],v[200:203]
cv v239,v222,v223
s_add_u32 s89, s89, 16384
mm v[200:203],a[68:71],v[148:151],v[200:203]
ex v192,v192
mm v[200:203],a[64:67],v[144:147],v[200:203]
wt lgkmcnt(0)
s_and_b32 s89, s89, 0x1ffff
mm v[196:199],a[80:83],v[128:131],0
ex v193,v193
mm v[196:199],a[84:87],v[132:135],v[196:199]
cv v224,v192,v193
mm v[196:199],a[88:91],v[136:139],v[196:199]
ex v194,v194
mm v[196:199],a[92:95],v[140:143],v[196:199]
ld v255,s[60:63],s17 offen offset:1024 sc1 lds
mm v[204:207],a[92:95],v[156:159],0
ex v195,v195
mm v[204:207],a[88:91],v[152:155],v[204:207]
cv v225,v194,v195
mm v[204:207],a[84:87],v[148:151],v[204:207]
ex v200,v200
mm v[204:207],a[80:83],v[144:147],v[204:207]
ld v255,s[60:63],s17 offen offset:2048 sc1 lds
mm v[208:211],a[64:67],v[160:163],0
ex v201,v201
mm v[208:211],a[68:71],v[164:167],v[208:211]
cv v228,v200,v201
mm v[208:211],a[72:75],v[168:171],v[208:211]
ex v202,v202
mm v[208:211],a[76:79],v[172:175],v[208:211]
ld v255,s[60:63],s17 offen offset:3072 sc1 lds
mm v[216:219],a[76:79],v[188:191],0
ex v203,v203
mm v[216:219],a[72:75],v[184:187],v[216:219]
cv v229,v202,v203
mm v[216:219],a[68:71],v[180:183],v[216:219]
ex v196,v196
mm v[216:219],a[64:67],v[176:179],v[216:219]
buffer_load_dword a253, v254, s[36:39], s90 offen
mm v[212:215],a[80:83],v[160:163],0
ex v197,v197
mm v[212:215],a[84:87],v[164:167],v[212:215]
cv v226,v196,v197
s_add_u32 s17, s17, 8192
mm v[212:215],a[88:91],v[168:171],v[212:215]
ex v198,v198
mm v[212:215],a[92:95],v[172:175],v[212:215]
s_add_u32 m0, m0, 16384
s_and_b32 m0, m0, s78
mm v[220:223],a[92:95],v[188:191],0
ex v199,v199
mm v[220:223],a[88:91],v[184:187],v[220:223]
cv v227,v198,v199
s_add_u32 s90, s90, s21
mm v[220:223],a[84:87],v[180:183],v[220:223]
ex v204,v204
mm v[220:223],a[80:83],v[176:179],v[220:223]
wt vmcnt(11)
s_barrier
mm v[64:67],a[96:99],v[232:235],v[64:67]
ex v205,v205
mm v[96:99],a[96:99],v[236:239],v[96:99]
rd a[96:99],v248 offset:8192
cv v230,v204,v205
mm v[100:103],a[100:103],v[236:239],v[100:103]
ex v206,v206
mm v[68:71],a[100:103],v[232:235],v[68:71]
rd a[100:103],v248 offset:9216
mm v[72:75],a[104:107],v[232:235],v[72:75]
ex v207,v207
mm v[104:107],a[104:107],v[236:239],v[104:107]
rd a[104:107],v248 offset:10240
cv v231,v206,v207
mm v[108:111],a[108:111],v[236:239],v[108:111]
ex v208,v208
mm v[76:79],a[108:111],v[232:235],v[76:79]
rd a[108:111],v248 offset:11264
mm v[80:83],a[112:115],v[232:235],v[80:83]
ex v209,v209
mm v[112:115],a[112:115],v[236:239],v[112:115]
rd a[112:115],v248 offset:12288
mm v[116:119],a[116:119],v[236:239],v[116:119]
ex v210,v210
mm v[84:87],a[116:119],v[232:235],v[84:87]
rd a[116:119],v248 offset:13312
mm v[88:91],a[120:123],v[232:235],v[88:91]
ex v211,v211
mm v[120:123],a[120:123],v[236:239],v[120:123]
rd a[120:123],v248 offset:14336
s_bitcmp1_b64 s[98:99], s85
mm v[124:127],a[124:127],v[236:239],v[124:127]
ex v216,v216
mm v[92:95],a[124:127],v[232:235],v[92:95]
rd a[124:127],v248 offset:15360
v_or_b32 v248, s89, v255
v_smfmac_f32_16x16x64_bf16 a[196:199], a[208:211], v[232:239], v241
wt lgkmcnt(4)
v_smfmac_f32_16x16x64_bf16 a[192:195], a[208:211], v[224:231], v241
rd a[64:67],v248 offset:0
mm v[0:3],a[96:99],v[224:227],v[0:3]
ex v217,v217
mm v[32:35],a[96:99],v[228:231],v[32:35]
rd a[68:71],v248 offset:1024
cv v232,v208,v209
mm v[36:39],a[100:103],v[228:231],v[36:39]
ex v218,v218
mm v[4:7],a[100:103],v[224:227],v[4:7]
rd a[72:75],v248 offset:2048
cv v233,v210,v211
mm v[8:11],a[104:107],v[224:227],v[8:11]
ex v219,v219
mm v[40:43],a[104:107],v[228:231],v[40:43]
rd a[76:79],v248 offset:3072
cv v236,v216,v217
mm v[44:47],a[108:111],v[228:231],v[44:47]
ex v212,v212
mm v[12:15],a[108:111],v[224:227],v[12:15]
rd a[80:83],v248 offset:4096
wt lgkmcnt(5)
mm v[16:19],a[112:115],v[224:227],v[16:19]
ex v213,v213
mm v[48:51],a[112:115],v[228:231],v[48:51]
rd a[84:87],v248 offset:5120
cv v237,v218,v219
mm v[52:55],a[116:119],v[228:231],v[52:55]
ex v214,v214
mm v[20:23],a[116:119],v[224:227],v[20:23]
rd a[88:91],v248 offset:6144
cv v234,v212,v213
mm v[24:27],a[120:123],v[224:227],v[24:27]
ex v215,v215
mm v[56:59],a[120:123],v[228:231],v[56:59]
rd a[92:95],v248 offset:7168
cv v235,v214,v215
mm v[60:63],a[124:127],v[228:231],v[60:63]
ex v220,v220
mm v[28:31],a[124:127],v[224:227],v[28:31]
s_cbranch_scc1 .La5_LA_F1E2
wt lgkmcnt(4)
mm v[192:195],a[64:67],v[128:131],0
ex v221,v221
mm v[192:195],a[68:71],v[132:135],v[192:195]
cv v238,v220,v221
s_add_u32 s85, s85, 1
mm v[192:195],a[72:75],v[136:139],v[192:195]
ex v222,v222
mm v[192:195],a[76:79],v[140:143],v[192:195]
ld v255,s[60:63],s17 offen offset:0 sc1 lds
mm v[200:203],a[76:79],v[156:159],0
ex v223,v223
mm v[200:203],a[72:75],v[152:155],v[200:203]
cv v239,v222,v223
s_add_u32 s89, s89, 16384
mm v[200:203],a[68:71],v[148:151],v[200:203]
ex v192,v192
mm v[200:203],a[64:67],v[144:147],v[200:203]
wt lgkmcnt(0)
s_and_b32 s89, s89, 0x1ffff
mm v[196:199],a[80:83],v[128:131],0
ex v193,v193
mm v[196:199],a[84:87],v[132:135],v[196:199]
cv v224,v192,v193
mm v[196:199],a[88:91],v[136:139],v[196:199]
ex v194,v194
mm v[196:199],a[92:95],v[140:143],v[196:199]
ld v255,s[60:63],s17 offen offset:1024 sc1 lds
mm v[204:207],a[92:95],v[156:159],0
ex v195,v195
mm v[204:207],a[88:91],v[152:155],v[204:207]
cv v225,v194,v195
mm v[204:207],a[84:87],v[148:151],v[204:207]
ex v200,v200
mm v[204:207],a[80:83],v[144:147],v[204:207]
ld v255,s[60:63],s17 offen offset:2048 sc1 lds
mm v[208:211],a[64:67],v[160:163],0
ex v201,v201
mm v[208:211],a[68:71],v[164:167],v[208:211]
cv v228,v200,v201
mm v[208:211],a[72:75],v[168:171],v[208:211]
ex v202,v202
mm v[208:211],a[76:79],v[172:175],v[208:211]
ld v255,s[60:63],s17 offen offset:3072 sc1 lds
mm v[216:219],a[76:79],v[188:191],0
ex v203,v203
mm v[216:219],a[72:75],v[184:187],v[216:219]
cv v229,v202,v203
mm v[216:219],a[68:71],v[180:183],v[216:219]
ex v196,v196
mm v[216:219],a[64:67],v[176:179],v[216:219]
buffer_load_dword a253, v254, s[36:39], s90 offen
mm v[212:215],a[80:83],v[160:163],0
ex v197,v197
mm v[212:215],a[84:87],v[164:167],v[212:215]
cv v226,v196,v197
s_add_u32 s17, s17, 8192
mm v[212:215],a[88:91],v[168:171],v[212:215]
ex v198,v198
mm v[212:215],a[92:95],v[172:175],v[212:215]
s_add_u32 m0, m0, 16384
s_and_b32 m0, m0, s78
mm v[220:223],a[92:95],v[188:191],0
ex v199,v199
mm v[220:223],a[88:91],v[184:187],v[220:223]
cv v227,v198,v199
s_add_u32 s90, s90, s21
mm v[220:223],a[84:87],v[180:183],v[220:223]
ex v204,v204
mm v[220:223],a[80:83],v[176:179],v[220:223]
wt vmcnt(10)
s_barrier
mm v[64:67],a[96:99],v[232:235],v[64:67]
ex v205,v205
mm v[96:99],a[96:99],v[236:239],v[96:99]
rd a[96:99],v248 offset:8192
cv v230,v204,v205
mm v[100:103],a[100:103],v[236:239],v[100:103]
ex v206,v206
mm v[68:71],a[100:103],v[232:235],v[68:71]
rd a[100:103],v248 offset:9216
mm v[72:75],a[104:107],v[232:235],v[72:75]
ex v207,v207
mm v[104:107],a[104:107],v[236:239],v[104:107]
rd a[104:107],v248 offset:10240
cv v231,v206,v207
mm v[108:111],a[108:111],v[236:239],v[108:111]
ex v208,v208
mm v[76:79],a[108:111],v[232:235],v[76:79]
rd a[108:111],v248 offset:11264
mm v[80:83],a[112:115],v[232:235],v[80:83]
ex v209,v209
mm v[112:115],a[112:115],v[236:239],v[112:115]
rd a[112:115],v248 offset:12288
mm v[116:119],a[116:119],v[236:239],v[116:119]
ex v210,v210
mm v[84:87],a[116:119],v[232:235],v[84:87]
rd a[116:119],v248 offset:13312
mm v[88:91],a[120:123],v[232:235],v[88:91]
ex v211,v211
mm v[120:123],a[120:123],v[236:239],v[120:123]
rd a[120:123],v248 offset:14336
s_bitcmp1_b64 s[98:99], s85
mm v[124:127],a[124:127],v[236:239],v[124:127]
ex v216,v216
mm v[92:95],a[124:127],v[232:235],v[92:95]
rd a[124:127],v248 offset:15360
v_or_b32 v248, s89, v255
v_smfmac_f32_16x16x64_bf16 a[196:199], a[208:211], v[232:239], v241
wt lgkmcnt(4)
v_smfmac_f32_16x16x64_bf16 a[192:195], a[208:211], v[224:231], v241
rd a[64:67],v248 offset:0
mm v[0:3],a[96:99],v[224:227],v[0:3]
ex v217,v217
mm v[32:35],a[96:99],v[228:231],v[32:35]
rd a[68:71],v248 offset:1024
cv v232,v208,v209
mm v[36:39],a[100:103],v[228:231],v[36:39]
ex v218,v218
mm v[4:7],a[100:103],v[224:227],v[4:7]
rd a[72:75],v248 offset:2048
cv v233,v210,v211
mm v[8:11],a[104:107],v[224:227],v[8:11]
ex v219,v219
mm v[40:43],a[104:107],v[228:231],v[40:43]
rd a[76:79],v248 offset:3072
cv v236,v216,v217
mm v[44:47],a[108:111],v[228:231],v[44:47]
ex v212,v212
mm v[12:15],a[108:111],v[224:227],v[12:15]
rd a[80:83],v248 offset:4096
wt lgkmcnt(5)
mm v[16:19],a[112:115],v[224:227],v[16:19]
ex v213,v213
mm v[48:51],a[112:115],v[228:231],v[48:51]
rd a[84:87],v248 offset:5120
cv v237,v218,v219
mm v[52:55],a[116:119],v[228:231],v[52:55]
ex v214,v214
mm v[20:23],a[116:119],v[224:227],v[20:23]
rd a[88:91],v248 offset:6144
cv v234,v212,v213
mm v[24:27],a[120:123],v[224:227],v[24:27]
ex v215,v215
mm v[56:59],a[120:123],v[228:231],v[56:59]
rd a[92:95],v248 offset:7168
cv v235,v214,v215
mm v[60:63],a[124:127],v[228:231],v[60:63]
ex v220,v220
mm v[28:31],a[124:127],v[224:227],v[28:31]
.La5_LA_F1E:
s_cbranch_scc0 .La5_LA_F1
.La5_LA_F1E2:
s_cmp_eq_u32 s85, s82
s_cbranch_scc1 .La5_LA_F1X
s_nop 0
s_nop 0
s_call_b64 s[24:25], .La5_LA_EVENT
s_branch .La5_LA_F1
.La5_LA_FE0:
.La5_LW2_1:
s_cbranch_execz .La5_LW2_R1
s_cmp_eq_u32 s85, s82
s_cbranch_scc1 .La5_LA_F1X
s_call_b64 s[24:25], .La5_LA_EVENT
s_branch .La5_LA_FS0
; ---- hand-offs between windows
.La5_LA_EVENT:
s_cmp_lg_u32 s85, 2
s_cbranch_scc1 .La5_LA_E1
wt vmcnt(0)
v_cmp_eq_u32 vcc, v250, v249
s_mov_b32 s17, s16
s_mov_b32 s78, 0x1ffff
s_lshl_b32 m0, s3, 12
s_or_b32 vcc_hi, vcc_hi, s79
s_cmp_eq_u64 vcc, -1
s_cbranch_scc0 .La5_LA_KSPIN
.La5_LA_KOK:
ld v255,s[60:63],s17 offen offset:0 sc1 lds
ld v255,s[60:63],s17 offen offset:1024 sc1 lds
ld v255,s[60:63],s17 offen offset:2048 sc1 lds
ld v255,s[60:63],s17 offen offset:3072 sc1 lds
s_add_u32 s17, s17, 8192
s_add_u32 m0, m0, 16384
s_mov_b32 s20, s19
v_and_b32 v251, 4, v251
s_cmp_lg_u32 s3, 0
s_cbranch_scc1 .La5_LA_POLL
s_nop 0
ld v255,s[60:63],s17 offen offset:0 sc1 lds
ld v255,s[60:63],s17 offen offset:1024 sc1 lds
ld v255,s[60:63],s17 offen offset:2048 sc1 lds
ld v255,s[60:63],s17 offen offset:3072 sc1 lds
s_add_u32 s17, s17, 8192
s_add_u32 m0, m0, 16384
.La5_LA_POLL:
s_cmp_lg_u32 s22, 0
s_cbranch_scc1 .La5_LA_WP
s_mov_b64 exec, 0
s_cmp_eq_u32 s3, 0
s_cbranch_scc1 .La5_LW5_0
s_cmp_eq_u32 s3, 1
s_cbranch_scc1 .La5_LW5_1
s_cmp_eq_u32 s3, 2
s_cbranch_scc1 .La5_LW5_2
s_branch .La5_LW5_3
.La5_LW5_R0:
s_mov_b64 exec, -1
.La5_LA_RETURN:
s_setpc_b64 s[24:25]
.La5_LA_WP:
s_mov_b64 exec, 0
s_cmp_eq_u32 s3, 0
s_cbranch_scc1 .La5_LW6_0
s_cmp_eq_u32 s3, 1
s_cbranch_scc1 .La5_LW6_1
s_cmp_eq_u32 s3, 2
s_cbranch_scc1 .La5_LW6_2
s_branch .La5_LW6_3
.La5_LW6_R0:
s_mov_b64 exec, -1
s_cmp_eq_u32 s73, 1
s_cbranch_scc1 .La5_LA_E0
buffer_load_dword v250, v251, s[64:67], s20 offen sc1
s_setpc_b64 s[24:25]
.La5_LA_E0:
s_mov_b32 s54, 16384
ld a[0:3],v255,s[52:55],0 offen offset:0 sc1
ld a[4:7],v255,s[52:55],0 offen offset:1024 sc1
ld a[8:11],v255,s[52:55],0 offen offset:2048 sc1
ld a[12:15],v255,s[52:55],0 offen offset:3072 sc1
ld a[16:19],v255,s[52:55],s95 offen offset:0 sc1
ld a[20:23],v255,s[52:55],s95 offen offset:1024 sc1
s_setpc_b64 s[24:25]
.La5_LA_E1:
ld a[24:27],v255,s[52:55],s95 offen offset:2048 sc1
ld a[28:31],v255,s[52:55],s95 offen offset:3072 sc1
ld a[32:35],v255,s[52:55],s96 offen offset:0 sc1
ld a[36:39],v255,s[52:55],s96 offen offset:1024 sc1
ld a[40:43],v255,s[52:55],s96 offen offset:2048 sc1
ld a[44:47],v255,s[52:55],s96 offen offset:3072 sc1
ld a[48:51],v255,s[52:55],s97 offen offset:0 sc1
ld a[52:55],v255,s[52:55],s97 offen offset:1024 sc1
ld a[56:59],v255,s[52:55],s97 offen offset:2048 sc1
ld a[60:63],v255,s[52:55],s97 offen offset:3072 sc1
s_setpc_b64 s[24:25]
; ---- helper: own block finished: l is checked (.La5_LA_SLOW if outside [2^-60, 2^60] or NaN), O / l leaves; the partner-s rows start
.La5_LA_F1X:
s_cmp_eq_u32 s75, 0
s_nop 0
s_nop 0
s_cbranch_scc1 .La5_LA_DC3
s_cmp_eq_u32 s22, 0
s_cbranch_scc1 .La5_LA_DC
s_cmp_eq_u32 s73, 1
s_cbranch_scc1 .La5_LA_F1Y
s_nop 0
v_cmp_eq_u32 vcc, v250, v249
s_mov_b32 s17, 0
s_mov_b32 s54, 16384
s_and_b32 s92, vcc_lo, 3
s_cmp_eq_u32 s92, 3
s_cbranch_scc0 .La5_LA_QSPIN
.La5_LA_QOK:
wt lgkmcnt(4)
mm v[192:195],a[64:67],v[128:131],0
ex v221,v221
mm v[192:195],a[68:71],v[132:135],v[192:195]
cv v238,v220,v221
s_add_u32 s85, s85, 1
mm v[192:195],a[72:75],v[136:139],v[192:195]
ex v222,v222
mm v[192:195],a[76:79],v[140:143],v[192:195]
ld v255,s[60:63],s17 offen offset:0 sc1 lds
mm v[200:203],a[76:79],v[156:159],0
ex v223,v223
mm v[200:203],a[72:75],v[152:155],v[200:203]
cv v239,v222,v223
s_add_u32 s89, s89, 16384
mm v[200:203],a[68:71],v[148:151],v[200:203]
ex v192,v192
mm v[200:203],a[64:67],v[144:147],v[200:203]
wt lgkmcnt(0)
s_and_b32 s89, s89, 0x1ffff
mm v[196:199],a[80:83],v[128:131],0
ex v193,v193
mm v[196:199],a[84:87],v[132:135],v[196:199]
cv v224,v192,v193
mm v[196:199],a[88:91],v[136:139],v[196:199]
ex v194,v194
.La5_LW6_0:
s_cbranch_execz .La5_LW6_4
mm v[196:199],a[92:95],v[140:143],v[196:199]
ld v255,s[60:63],s17 offen offset:1024 sc1 lds
mm v[204:207],a[92:95],v[156:159],0
ex v195,v195
mm v[204:207],a[88:91],v[152:155],v[204:207]
cv v225,v194,v195
mm v[204:207],a[84:87],v[148:151],v[204:207]
ex v200,v200
mm v[204:207],a[80:83],v[144:147],v[204:207]
ld v255,s[60:63],s17 offen offset:2048 sc1 lds
mm v[208:211],a[64:67],v[160:163],0
ex v201,v201
mm v[208:211],a[68:71],v[164:167],v[208:211]
cv v228,v200,v201
mm v[208:211],a[72:75],v[168:171],v[208:211]
ex v202,v202
mm v[208:211],a[76:79],v[172:175],v[208:211]
ld v255,s[60:63],s17 offen offset:3072 sc1 lds
mm v[216:219],a[76:79],v[188:191],0
ex v203,v203
mm v[216:219],a[72:75],v[184:187],v[216:219]
cv v229,v202,v203
mm v[216:219],a[68:71],v[180:183],v[216:219]
ex v196,v196
mm v[216:219],a[64:67],v[176:179],v[216:219]
ld a[0:3],v255,s[52:55],0 offen offset:0 sc1
mm v[212:215],a[80:83],v[160:163],0
ex v197,v197
mm v[212:215],a[84:87],v[164:167],v[212:215]
cv v226,v196,v197
s_add_u32 s17, s17, 8192
mm v[212:215],a[88:91],v[168:171],v[212:215]
ex v198,v198
mm v[212:215],a[92:95],v[172:175],v[212:215]
s_add_u32 m0, m0, 16384
s_and_b32 m0, m0, s78
ld a[4:7],v255,s[52:55],0 offen offset:1024 sc1
mm v[220:223],a[92:95],v[188:191],0
ex v199,v199
mm v[220:223],a[88:91],v[184:187],v[220:223]
cv v227,v198,v199
mm v[220:223],a[84:87],v[180:183],v[220:223]
ex v204,v204
mm v[220:223],a[80:83],v[176:179],v[220:223]
wt vmcnt(12)
s_barrier
mm v[64:67],a[96:99],v[232:235],v[64:67]
ex v205,v205
mm v[96:99],a[96:99],v[236:239],v[96:99]
rd a[96:99],v248 offset:8192
cv v230,v204,v205
mm v[100:103],a[100:103],v[236:239],v[100:103]
ex v206,v206
mm v[68:71],a[100:103],v[232:235],v[68:71]
rd a[100:103],v248 offset:9216
ld a[8:11],v255,s[52:55],0 offen offset:2048 sc1
mm v[72:75],a[104:107],v[232:235],v[72:75]
ex v207,v207
mm v[104:107],a[104:107],v[236:239],v[104:107]
rd a[104:107],v248 offset:10240
cv v231,v206,v207
mm v[108:111],a[108:111],v[236:239],v[108:111]
ex v208,v208
mm v[76:79],a[108:111],v[232:235],v[76:79]
rd a[108:111],v248 offset:11264
ld a[12:15],v255,s[52:55],0 offen offset:3072 sc1
mm v[80:83],a[112:115],v[232:235],v[80:83]
ex v209,v209
mm v[112:115],a[112:115],v[236:239],v[112:115]
rd a[112:115],v248 offset:12288
mm v[116:119],a[116:119],v[236:239],v[116:119]
ex v210,v210
mm v[84:87],a[116:119],v[232:235],v[84:87]
rd a[116:119],v248 offset:13312
ld a[16:19],v255,s[52:55],s95 offen offset:0 sc1
mm v[88:91],a[120:123],v[232:235],v[88:91]
ex v211,v211
mm v[120:123],a[120:123],v[236:239],v[120:123]
rd a[120:123],v248 offset:14336
s_bitcmp1_b64 s[98:99], s85
mm v[124:127],a[124:127],v[236:239],v[124:127]
ex v216,v216
mm v[92:95],a[124:127],v[232:235],v[92:95]
rd a[124:127],v248 offset:15360
v_or_b32 v248, s89, v255
v_smfmac_f32_16x16x64_bf16 a[196:199], a[208:211], v[232:239], v241
wt lgkmcnt(4)
v_smfmac_f32_16x16x64_bf16 a[192:195], a[208:211], v[224:231], v241
rd a[64:67],v248 offset:0
mm v[0:3],a[96:99],v[224:227],v[0:3]
ex v217,v217
mm v[32:35],a[96:99],v[228:231],v[32:35]
rd a[68:71],v248 offset:1024
cv v232,v208,v209
mm v[36:39],a[100:103],v[228:231],v[36:39]
ex v218,v218
mm v[4:7],a[100:103],v[224:227],v[4:7]
rd a[72:75],v248 offset:2048
cv v233,v210,v211
mm v[8:11],a[104:107],v[224:227],v[8:11]
ex v219,v219
mm v[40:43],a[104:107],v[228:231],v[40:43]
rd a[76:79],v248 offset:3072
cv v236,v216,v217
mm v[44:47],a[108:111],v[228:231],v[44:47]
ex v212,v212
mm v[12:15],a[108:111],v[224:227],v[12:15]
rd a[80:83],v248 offset:4096
wt lgkmcnt(5)
mm v[16:19],a[112:115],v[224:227],v[16:19]
ex v213,v213
mm v[48:51],a[112:115],v[228:231],v[48:51]
rd a[84:87],v248 offset:5120
cv v237,v218,v219
mm v[52:55],a[116:119],v[228:231],v[52:55]
ex v214,v214
mm v[20:23],a[116:119],v[224:227],v[20:23]
rd a[88:91],v248 offset:6144
cv v234,v212,v213
mm v[24:27],a[120:123],v[224:227],v[24:27]
ex v215,v215
mm v[56:59],a[120:123],v[228:231],v[56:59]
rd a[92:95],v248 offset:7168
cv v235,v214,v215
mm v[60:63],a[124:127],v[228:231],v[60:63]
ex v220,v220
mm v[28:31],a[124:127],v[224:227],v[28:31]
ld a[20:23],v255,s[52:55],s95 offen offset:1024 sc1
wt lgkmcnt(4)
mm v[192:195],a[64:67],v[128:131],0
ex v221,v221
mm v[192:195],a[68:71],v[132:135],v[192:195]
cv v238,v220,v221
s_add_u32 s85, s85, 1
mm v[192:195],a[72:75],v[136:139],v[192:195]
ex v222,v222
mm v[192:195],a[76:79],v[140:143],v[192:195]
ld v255,s[60:63],s17 offen offset:0 sc1 lds
mm v[200:203],a[76:79],v[156:159],0
ex v223,v223
mm v[200:203],a[72:75],v[152:155],v[200:203]
cv v239,v222,v223
s_add_u32 s89, s89, 16384
mm v[200:203],a[68:71],v[148:151],v[200:203]
ex v192,v192
mm v[200:203],a[64:67],v[144:147],v[200:203]
wt lgkmcnt(0)
s_and_b32 s89, s89, 0x1ffff
ld a[24:27],v255,s[52:55],s95 offen offset:2048 sc1
mm v[196:199],a[80:83],v[128:131],0
ex v193,v193
mm v[196:199],a[84:87],v[132:135],v[196:199]
cv v224,v192,v193
mm v[196:199],a[88:91],v[136:139],v[196:199]
ex v194,v194
mm v[196:199],a[92:95],v[140:143],v[196:199]
ld v255,s[60:63],s17 offen offset:1024 sc1 lds
mm v[204:207],a[92:95],v[156:159],0
ex v195,v195
mm v[204:207],a[88:91],v[152:155],v[204:207]
cv v225,v194,v195
mm v[204:207],a[84:87],v[148:151],v[204:207]
ex v200,v200
mm v[204:207],a[80:83],v[144:147],v[204:207]
ld v255,s[60:63],s17 offen offset:2048 sc1 lds
mm v[208:211],a[64:67],v[160:163],0
ex v201,v201
mm v[208:211],a[68:71],v[164:167],v[208:211]
cv v228,v200,v201
mm v[208:211],a[72:75],v[168:171],v[208:211]
ex v202,v202
mm v[208:211],a[76:79],v[172:175],v[208:211]
ld v255,s[60:63],s17 offen offset:3072 sc1 lds
mm v[216:219],a[76:79],v[188:191],0
ex v203,v203
mm v[216:219],a[72:75],v[184:187],v[216:219]
cv v229,v202,v203
mm v[216:219],a[68:71],v[180:183],v[216:219]
ex v196,v196
mm v[216:219],a[64:67],v[176:179],v[216:219]
ld a[28:31],v255,s[52:55],s95 offen offset:3072 sc1
mm v[212:215],a[80:83],v[160:163],0
ex v197,v197
mm v[212:215],a[84:87],v[164:167],v[212:215]
cv v226,v196,v197
s_add_u32 s17, s17, 8192
mm v[212:215],a[88:91],v[168:171],v[212:215]
ex v198,v198
mm v[212:215],a[92:95],v[172:175],v[212:215]
s_add_u32 m0, m0, 16384
s_and_b32 m0, m0, s78
ld a[32:35],v255,s[52:55],s96 offen offset:0 sc1
mm v[220:223],a[92:95],v[188:191],0
ex v199,v199
mm v[220:223],a[88:91],v[184:187],v[220:223]
cv v227,v198,v199
mm v[220:223],a[84:87],v[180:183],v[220:223]
ex v204,v204
mm v[220:223],a[80:83],v[176:179],v[220:223]
wt vmcnt(18)
s_barrier
mm v[64:67],a[96:99],v[232:235],v[64:67]
ex v205,v205
mm v[96:99],a[96:99],v[236:239],v[96:99]
rd a[96:99],v248 offset:8192
cv v230,v204,v205
mm v[100:103],a[100:103],v[236:239],v[100:103]
ex v206,v206
mm v[68:71],a[100:103],v[232:235],v[68:71]
rd a[100:103],v248 offset:9216
ld a[36:39],v255,s[52:55],s96 offen offset:1024 sc1
mm v[72:75],a[104:107],v[232:235],v[72:75]
ex v207,v207
mm v[104:107],a[104:107],v[236:239],v[104:107]
rd a[104:107],v248 offset:10240
cv v231,v206,v207
mm v[108:111],a[108:111],v[236:239],v[108:111]
ex v208,v208
mm v[76:79],a[108:111],v[232:235],v[76:79]
rd a[108:111],v248 offset:11264
ld a[40:43],v255,s[52:55],s96 offen offset:2048 sc1
mm v[80:83],a[112:115],v[232:235],v[80:83]
ex v209,v209
mm v[112:115],a[112:115],v[236:239],v[112:115]
rd a[112:115],v248 offset:12288
mm v[116:119],a[116:119],v[236:239],v[116:119]
ex v210,v210
mm v[84:87],a[116:119],v[232:235],v[84:87]
rd a[116:119],v248 offset:13312
ld a[44:47],v255,s[52:55],s96 offen offset:3072 sc1
mm v[88:91],a[120:123],v[232:235],v[88:91]
ex v211,v211
mm v[120:123],a[120:123],v[236:239],v[120:123]
rd a[120:123],v248 offset:14336
s_bitcmp1_b64 s[98:99], s85
mm v[124:127],a[124:127],v[236:239],v[124:127]
ex v216,v216
mm v[92:95],a[124:127],v[232:235],v[92:95]
rd a[124:127],v248 offset:15360
v_or_b32 v248, s89, v255
v_smfmac_f32_16x16x64_bf16 a[196:199], a[208:211], v[232:239], v241
wt lgkmcnt(4)
v_smfmac_f32_16x16x64_bf16 a[192:195], a[208:211], v[224:231], v241
rd a[64:67],v248 offset:0
mm v[0:3],a[96:99],v[224:227],v[0:3]
ex v217,v217
mm v[32:35],a[96:99],v[228:231],v[32:35]
rd a[68:71],v248 offset:1024
cv v232,v208,v209
mm v[36:39],a[100:103],v[228:231],v[36:39]
ex v218,v218
mm v[4:7],a[100:103],v[224:227],v[4:7]
rd a[72:75],v248 offset:2048
cv v233,v210,v211
mm v[8:11],a[104:107],v[224:227],v[8:11]
ex v219,v219
mm v[40:43],a[104:107],v[228:231],v[40:43]
rd a[76:79],v248 offset:3072
cv v236,v216,v217
mm v[44:47],a[108:111],v[228:231],v[44:47]
ex v212,v212
mm v[12:15],a[108:111],v[224:227],v[12:15]
rd a[80:83],v248 offset:4096
wt lgkmcnt(5)
mm v[16:19],a[112:115],v[224:227],v[16:19]
ex v213,v213
mm v[48:51],a[112:115],v[228:231],v[48:51]
rd a[84:87],v248 offset:5120
cv v237,v218,v219
mm v[52:55],a[116:119],v[228:231],v[52:55]
ex v214,v214
mm v[20:23],a[116:119],v[224:227],v[20:23]
rd a[88:91],v248 offset:6144
cv v234,v212,v213
mm v[24:27],a[120:123],v[224:227],v[24:27]
ex v215,v215
mm v[56:59],a[120:123],v[228:231],v[56:59]
rd a[92:95],v248 offset:7168
cv v235,v214,v215
mm v[60:63],a[124:127],v[228:231],v[60:63]
ex v220,v220
mm v[28:31],a[124:127],v[224:227],v[28:31]
ld a[48:51],v255,s[52:55],s97 offen offset:0 sc1
wt lgkmcnt(4)
mm v[192:195],a[64:67],v[128:131],0
ex v221,v221
mm v[192:195],a[68:71],v[132:135],v[192:195]
cv v238,v220,v221
s_add_u32 s85, s85, 1
mm v[192:195],a[72:75],v[136:139],v[192:195]
ex v222,v222
mm v[192:195],a[76:79],v[140:143],v[192:195]
ld v255,s[60:63],s17 offen offset:0 sc1 lds
mm v[200:203],a[76:79],v[156:159],0
ex v223,v223
mm v[200:203],a[72:75],v[152:155],v[200:203]
cv v239,v222,v223
s_add_u32 s89, s89, 16384
mm v[200:203],a[68:71],v[148:151],v[200:203]
ex v192,v192
mm v[200:203],a[64:67],v[144:147],v[200:203]
wt lgkmcnt(0)
s_and_b32 s89, s89, 0x1ffff
ld a[52:55],v255,s[52:55],s97 offen offset:1024 sc1
mm v[196:199],a[80:83],v[128:131],0
ex v193,v193
mm v[196:199],a[84:87],v[132:135],v[196:199]
cv v224,v192,v193
mm v[196:199],a[88:91],v[136:139],v[196:199]
ex v194,v194
mm v[196:199],a[92:95],v[140:143],v[196:199]
ld v255,s[60:63],s17 offen offset:1024 sc1 lds
mm v[204:207],a[92:95],v[156:159],0
ex v195,v195
mm v[204:207],a[88:91],v[152:155],v[204:207]
cv v225,v194,v195
mm v[204:207],a[84:87],v[148:151],v[204:207]
ex v200,v200
mm v[204:207],a[80:83],v[144:147],v[204:207]
ld v255,s[60:63],s17 offen offset:2048 sc1 lds
mm v[208:211],a[64:67],v[160:163],0
ex v201,v201
mm v[208:211],a[68:71],v[164:167],v[208:211]
cv v228,v200,v201
mm v[208:211],a[72:75],v[168:171],v[208:211]
ex v202,v202
mm v[208:211],a[76:79],v[172:175],v[208:211]
ld v255,s[60:63],s17 offen offset:3072 sc1 lds
mm v[216:219],a[76:79],v[188:191],0
ex v203,v203
mm v[216:219],a[72:75],v[184:187],v[216:219]
cv v229,v202,v203
mm v[216:219],a[68:71],v[180:183],v[216:219]
ex v196,v196
mm v[216:219],a[64:67],v[176:179],v[216:219]
ld a[56:59],v255,s[52:55],s97 offen offset:2048 sc1
mm v[212:215],a[80:83],v[160:163],0
ex v197,v197
mm v[212:215],a[84:87],v[164:167],v[212:215]
cv v226,v196,v197
s_add_u32 s17, s17, 8192
mm v[212:215],a[88:91],v[168:171],v[212:215]
ex v198,v198
mm v[212:215],a[92:95],v[172:175],v[212:215]
s_add_u32 m0, m0, 16384
s_and_b32 m0, m0, s78
ld a[60:63],v255,s[52:55],s97 offen offset:3072 sc1
mm v[220:223],a[92:95],v[188:191],0
ex v199,v199
mm v[220:223],a[88:91],v[184:187],v[220:223]
cv v227,v198,v199
mm v[220:223],a[84:87],v[180:183],v[220:223]
ex v204,v204
mm v[220:223],a[80:83],v[176:179],v[220:223]
wt vmcnt(24)
s_barrier
mm v[64:67],a[96:99],v[232:235],v[64:67]
ex v205,v205
mm v[96:99],a[96:99],v[236:239],v[96:99]
rd a[96:99],v248 offset:8192
cv v230,v204,v205
mm v[100:103],a[100:103],v[236:239],v[100:103]
ex v206,v206
mm v[68:71],a[100:103],v[232:235],v[68:71]
rd a[100:103],v248 offset:9216
mm v[72:75],a[104:107],v[232:235],v[72:75]
ex v207,v207
mm v[104:107],a[104:107],v[236:239],v[104:107]
rd a[104:107],v248 offset:10240
cv v231,v206,v207
mm v[108:111],a[108:111],v[236:239],v[108:111]
ex v208,v208
mm v[76:79],a[108:111],v[232:235],v[76:79]
rd a[108:111],v248 offset:11264
mm v[80:83],a[112:115],v[232:235],v[80:83]
ex v209,v209
mm v[112:115],a[112:115],v[236:239],v[112:115]
rd a[112:115],v248 offset:12288
mm v[116:119],a[116:119],v[236:239],v[116:119]
ex v210,v210
mm v[84:87],a[116:119],v[232:235],v[84:87]
rd a[116:119],v248 offset:13312
mm v[88:91],a[120:123],v[232:235],v[88:91]
ex v211,v211
mm v[120:123],a[120:123],v[236:239],v[120:123]
rd a[120:123],v248 offset:14336
s_bitcmp1_b64 s[98:99], s85
mm v[124:127],a[124:127],v[236:239],v[124:127]
ex v216,v216
mm v[92:95],a[124:127],v[232:235],v[92:95]
rd a[124:127],v248 offset:15360
v_or_b32 v248, s89, v255
v_smfmac_f32_16x16x64_bf16 a[196:199], a[208:211], v[232:239], v241
wt lgkmcnt(4)
v_smfmac_f32_16x16x64_bf16 a[192:195], a[208:211], v[224:231], v241
rd a[64:67],v248 offset:0
mm v[0:3],a[96:99],v[224:227],v[0:3]
ex v217,v217
mm v[32:35],a[96:99],v[228:231],v[32:35]
rd a[68:71],v248 offset:1024
cv v232,v208,v209
mm v[36:39],a[100:103],v[228:231],v[36:39]
ex v218,v218
mm v[4:7],a[100:103],v[224:227],v[4:7]
rd a[72:75],v248 offset:2048
cv v233,v210,v211
mm v[8:11],a[104:107],v[224:227],v[8:11]
ex v219,v219
mm v[40:43],a[104:107],v[228:231],v[40:43]
rd a[76:79],v248 offset:3072
cv v236,v216,v217
mm v[44:47],a[108:111],v[228:231],v[44:47]
ex v212,v212
mm v[12:15],a[108:111],v[224:227],v[12:15]
rd a[80:83],v248 offset:4096
wt lgkmcnt(5)
mm v[16:19],a[112:115],v[224:227],v[16:19]
ex v213,v213
mm v[48:51],a[112:115],v[228:231],v[48:51]
rd a[84:87],v248 offset:5120
cv v237,v218,v219
mm v[52:55],a[116:119],v[228:231],v[52:55]
ex v214,v214
mm v[20:23],a[116:119],v[224:227],v[20:23]
rd a[88:91],v248 offset:6144
cv v234,v212,v213
mm v[24:27],a[120:123],v[224:227],v[24:27]
ex v215,v215
mm v[56:59],a[120:123],v[228:231],v[56:59]
rd a[92:95],v248 offset:7168
cv v235,v214,v215
mm v[60:63],a[124:127],v[228:231],v[60:63]
ex v220,v220
mm v[28:31],a[124:127],v[224:227],v[28:31]
.La5_LA_F1Y:
.La5_LW6_1:
s_cbranch_execz .La5_LW6_5
wt lgkmcnt(4)
wt vmcnt(0)
mm v[192:195],a[64:67],a[0:3],0
ex v221,v221
mm v[192:195],a[68:71],a[4:7],v[192:195]
cv v238,v220,v221
s_add_u32 s89, s89, 16384
mm v[192:195],a[72:75],a[8:11],v[192:195]
ex v222,v222
mm v[192:195],a[76:79],a[12:15],v[192:195]
ld v255,s[60:63],s17 offen offset:0 sc1 lds
ar v242,a192
ar v240,a193
v_add_f32 v242, v242, v240
mm v[200:203],a[76:79],a[28:31],0
ex v223,v223
ar v243,a194
ar v240,a195
v_add_f32 v243, v243, v240
mm v[200:203],a[72:75],a[24:27],v[200:203]
cv v239,v222,v223
s_and_b32 s89, s89, 0x1ffff
aw a192,0
aw a193,0
aw a194,0
mm v[200:203],a[68:71],a[20:23],v[200:203]
ex v192,v192
aw a195,0
v_subrev_u32 v240, 562036736, v242
mm v[200:203],a[64:67],a[16:19],v[200:203]
wt lgkmcnt(0)
v_rcp_f32 v242, v242
v_cmp_le_u32 vcc, 1006632960, v240
v_subrev_u32 v240, 562036736, v243
mm v[196:199],a[80:83],a[0:3],0
ex v193,v193
s_mov_b64 s[92:93], vcc
v_cmp_le_u32 vcc, 1006632960, v240
mm v[196:199],a[84:87],a[4:7],v[196:199]
cv v224,v192,v193
v_rcp_f32 v243, v243
mm v[196:199],a[88:91],a[8:11],v[196:199]
ex v194,v194
s_or_b64 vcc, vcc, s[92:93]
s_cbranch_scc1 .La5_LA_SC1
.La5_LA_SR1:
v_mul_f32 v0, v0, v242
mm v[196:199],a[92:95],a[12:15],v[196:199]
ld v255,s[60:63],s17 offen offset:1024 sc1 lds
v_mul_f32 v1, v1, v242
v_mul_f32 v2, v2, v242
v_mul_f32 v3, v3, v242
mm v[204:207],a[92:95],a[28:31],0
ex v195,v195
v_mul_f32 v4, v4, v242
v_mul_f32 v5, v5, v242
v_mul_f32 v6, v6, v242
mm v[204:207],a[88:91],a[24:27],v[204:207]
cv v225,v194,v195
v_mul_f32 v7, v7, v242
cv v128,v0,v1
cv v129,v2,v3
cv v130,v4,v5
mm v[204:207],a[84:87],a[20:23],v[204:207]
ex v200,v200
cv v131,v6,v7
v_mul_f32 v8, v8, v242
v_mul_f32 v9, v9, v242
mm v[204:207],a[80:83],a[16:19],v[204:207]
ld v255,s[60:63],s17 offen offset:2048 sc1 lds
v_mul_f32 v10, v10, v242
v_mul_f32 v11, v11, v242
v_mul_f32 v12, v12, v242
mm v[208:211],a[64:67],a[32:35],0
ex v201,v201
v_mul_f32 v13, v13, v242
v_mul_f32 v14, v14, v242
v_mul_f32 v15, v15, v242
mm v[208:211],a[68:71],a[36:39],v[208:211]
cv v228,v200,v201
cv v132,v8,v9
cv v133,v10,v11
cv v134,v12,v13
cv v135,v14,v15
mm v[208:211],a[72:75],a[40:43],v[208:211]
ex v202,v202
v_mul_f32 v16, v16, v242
v_mul_f32 v17, v17, v242
v_mul_f32 v18, v18, v242
mm v[208:211],a[76:79],a[44:47],v[208:211]
ld v255,s[60:63],s17 offen offset:3072 sc1 lds
v_mul_f32 v19, v19, v242
v_mul_f32 v20, v20, v242
v_mul_f32 v21, v21, v242
mm v[216:219],a[76:79],a[60:63],0
ex v203,v203
v_mul_f32 v22, v22, v242
v_mul_f32 v23, v23, v242
cv v136,v16,v17
mm v[216:219],a[72:75],a[56:59],v[216:219]
cv v229,v202,v203
cv v137,v18,v19
cv v138,v20,v21
cv v139,v22,v23
v_mul_f32 v24, v24, v242
mm v[216:219],a[68:71],a[52:55],v[216:219]
ex v196,v196
v_mul_f32 v25, v25, v242
v_mul_f32 v26, v26, v242
v_mul_f32 v27, v27, v242
mm v[216:219],a[64:67],a[48:51],v[216:219]
v_mul_f32 v28, v28, v242
v_mul_f32 v29, v29, v242
v_mul_f32 v30, v30, v242
v_mul_f32 v31, v31, v242
cv v140,v24,v25
mm v[212:215],a[80:83],a[32:35],0
ex v197,v197
s_add_u32 s17, s17, 8192
cv v141,v26,v27
cv v142,v28,v29
mm v[212:215],a[84:87],a[36:39],v[212:215]
cv v226,v196,v197
s_add_u32 m0, m0, 16384
s_and_b32 m0, m0, s78
cv v143,v30,v31
v_mul_f32 v32, v32, v243
mm v[212:215],a[88:91],a[40:43],v[212:215]
ex v198,v198
v_mul_f32 v33, v33, v243
v_mul_f32 v34, v34, v243
v_mul_f32 v35, v35, v243
mm v[212:215],a[92:95],a[44:47],v[212:215]
st v[128:131],v244,s[44:47],s91 offen offset:0 sc1
v_mul_f32 v36, v36, v243
v_mul_f32 v37, v37, v243
v_mul_f32 v38, v38, v243
mm v[220:223],a[92:95],a[60:63],0
ex v199,v199
v_mul_f32 v39, v39, v243
cv v144,v32,v33
cv v145,v34,v35
mm v[220:223],a[88:91],a[56:59],v[220:223]
cv v227,v198,v199
cv v146,v36,v37
cv v147,v38,v39
v_mul_f32 v40, v40, v243
v_mul_f32 v41, v41, v243
mm v[220:223],a[84:87],a[52:55],v[220:223]
ex v204,v204
v_mul_f32 v42, v42, v243
v_mul_f32 v43, v43, v243
v_mul_f32 v44, v44, v243
mm v[220:223],a[80:83],a[48:51],v[220:223]
s_nop 0
s_barrier
v_mul_f32 v45, v45, v243
v_mul_f32 v46, v46, v243
v_mul_f32 v47, v47, v243
mm v[64:67],a[96:99],v[232:235],v[64:67]
ex v205,v205
cv v148,v40,v41
cv v149,v42,v43
cv v150,v44,v45
mm v[96:99],a[96:99],v[236:239],v[96:99]
rd a[96:99],v248 offset:8192
cv v230,v204,v205
cv v151,v46,v47
v_mul_f32 v48, v48, v243
v_mul_f32 v49, v49, v243
mm v[100:103],a[100:103],v[236:239],v[100:103]
ex v206,v206
v_mul_f32 v50, v50, v243
v_mul_f32 v51, v51, v243
v_mul_f32 v52, v52, v243
mm v[68:71],a[100:103],v[232:235],v[68:71]
rd a[100:103],v248 offset:9216
st v[132:135],v244,s[44:47],s91 offen offset:1024 sc1
v_mul_f32 v53, v53, v243
.La5_LW6_2:
s_cbranch_execz .La5_LW6_6
v_mul_f32 v54, v54, v243
mm v[72:75],a[104:107],v[232:235],v[72:75]
ex v207,v207
v_mul_f32 v55, v55, v243
cv v152,v48,v49
cv v153,v50,v51
mm v[104:107],a[104:107],v[236:239],v[104:107]
rd a[104:107],v248 offset:10240
cv v231,v206,v207
cv v154,v52,v53
cv v155,v54,v55
v_mul_f32 v56, v56, v243
mm v[108:111],a[108:111],v[236:239],v[108:111]
ex v208,v208
v_mul_f32 v57, v57, v243
v_mul_f32 v58, v58, v243
v_mul_f32 v59, v59, v243
mm v[76:79],a[108:111],v[232:235],v[76:79]
rd a[108:111],v248 offset:11264
v_mul_f32 v60, v60, v243
v_mul_f32 v61, v61, v243
v_mul_f32 v62, v62, v243
v_mul_f32 v63, v63, v243
mm v[80:83],a[112:115],v[232:235],v[80:83]
ex v209,v209
cv v156,v56,v57
cv v157,v58,v59
cv v158,v60,v61
mm v[112:115],a[112:115],v[236:239],v[112:115]
rd a[112:115],v248 offset:12288
st v[136:139],v244,s[44:47],s91 offen offset:2048 sc1
cv v159,v62,v63
mm v[116:119],a[116:119],v[236:239],v[116:119]
ex v210,v210
mm v[84:87],a[116:119],v[232:235],v[84:87]
rd a[116:119],v248 offset:13312
mm v[88:91],a[120:123],v[232:235],v[88:91]
ex v211,v211
mm v[120:123],a[120:123],v[236:239],v[120:123]
rd a[120:123],v248 offset:14336
st v[140:143],v244,s[44:47],s91 offen offset:3072 sc1
mm v[124:127],a[124:127],v[236:239],v[124:127]
ex v216,v216
mm v[92:95],a[124:127],v[232:235],v[92:95]
rd a[124:127],v248 offset:15360
v_or_b32 v248, s89, v255
v_smfmac_f32_16x16x64_bf16 a[196:199], a[208:211], v[232:239], v241
wt lgkmcnt(4)
v_smfmac_f32_16x16x64_bf16 a[192:195], a[208:211], v[224:231], v241
rd a[64:67],v248 offset:0
mm v[0:3],a[96:99],v[224:227],0
ex v217,v217
mm v[32:35],a[96:99],v[228:231],0
rd a[68:71],v248 offset:1024
cv v232,v208,v209
ar v242,a196
ar v240,a197
v_add_f32 v242, v242, v240
mm v[36:39],a[100:103],v[228:231],0
ex v218,v218
ar v243,a198
ar v240,a199
v_add_f32 v243, v243, v240
mm v[4:7],a[100:103],v[224:227],0
rd a[72:75],v248 offset:2048
cv v233,v210,v211
st v[144:147],v245,s[44:47],s91 offen offset:0 sc1
aw a196,0
mm v[8:11],a[104:107],v[224:227],0
ex v219,v219
aw a197,0
aw a198,0
aw a199,0
mm v[40:43],a[104:107],v[228:231],0
rd a[76:79],v248 offset:3072
cv v236,v216,v217
v_subrev_u32 v240, 562036736, v242
v_rcp_f32 v242, v242
mm v[44:47],a[108:111],v[228:231],0
ex v212,v212
v_cmp_le_u32 vcc, 1006632960, v240
v_subrev_u32 v240, 562036736, v243
s_mov_b64 s[92:93], vcc
mm v[12:15],a[108:111],v[224:227],0
rd a[80:83],v248 offset:4096
wt lgkmcnt(5)
v_cmp_le_u32 vcc, 1006632960, v240
v_rcp_f32 v243, v243
mm v[16:19],a[112:115],v[224:227],0
ex v213,v213
s_or_b64 vcc, vcc, s[92:93]
s_cbranch_scc1 .La5_LA_SC2
.La5_LA_SR2:
v_mul_f32 v64, v64, v242
mm v[48:51],a[112:115],v[228:231],0
rd a[84:87],v248 offset:5120
cv v237,v218,v219
v_mul_f32 v65, v65, v242
v_mul_f32 v66, v66, v242
v_mul_f32 v67, v67, v242
mm v[52:55],a[116:119],v[228:231],0
ex v214,v214
v_mul_f32 v68, v68, v242
v_mul_f32 v69, v69, v242
v_mul_f32 v70, v70, v242
mm v[20:23],a[116:119],v[224:227],0
rd a[88:91],v248 offset:6144
cv v234,v212,v213
st v[148:151],v245,s[44:47],s91 offen offset:1024 sc1
v_mul_f32 v71, v71, v242
mm v[24:27],a[120:123],v[224:227],0
ex v215,v215
cv v160,v64,v65
cv v161,v66,v67
cv v162,v68,v69
mm v[56:59],a[120:123],v[228:231],0
rd a[92:95],v248 offset:7168
cv v235,v214,v215
cv v163,v70,v71
v_mul_f32 v72, v72, v242
v_mul_f32 v73, v73, v242
mm v[60:63],a[124:127],v[228:231],0
ex v220,v220
v_mul_f32 v74, v74, v242
v_mul_f32 v75, v75, v242
v_mul_f32 v76, v76, v242
mm v[28:31],a[124:127],v[224:227],0
s_add_u32 s85, s85, 1
v_mul_f32 v77, v77, v242
v_mul_f32 v78, v78, v242
v_mul_f32 v79, v79, v242
cv v164,v72,v73
wt lgkmcnt(4)
mm v[192:195],a[64:67],a[0:3],0
ex v221,v221
cv v165,v74,v75
cv v166,v76,v77
cv v167,v78,v79
mm v[192:195],a[68:71],a[4:7],v[192:195]
cv v238,v220,v221
s_add_u32 s89, s89, 16384
v_mul_f32 v80, v80, v242
v_mul_f32 v81, v81, v242
v_mul_f32 v82, v82, v242
mm v[192:195],a[72:75],a[8:11],v[192:195]
ex v222,v222
v_mul_f32 v83, v83, v242
v_mul_f32 v84, v84, v242
v_mul_f32 v85, v85, v242
mm v[192:195],a[76:79],a[12:15],v[192:195]
ld v255,s[60:63],s17 offen offset:0 sc1 lds
v_mul_f32 v86, v86, v242
v_mul_f32 v87, v87, v242
cv v168,v80,v81
mm v[200:203],a[76:79],a[28:31],0
ex v223,v223
cv v169,v82,v83
cv v170,v84,v85
cv v171,v86,v87
mm v[200:203],a[72:75],a[24:27],v[200:203]
cv v239,v222,v223
s_and_b32 s89, s89, 0x1ffff
v_mul_f32 v88, v88, v242
v_mul_f32 v89, v89, v242
v_mul_f32 v90, v90, v242
mm v[200:203],a[68:71],a[20:23],v[200:203]
ex v192,v192
v_mul_f32 v91, v91, v242
v_mul_f32 v92, v92, v242
v_mul_f32 v93, v93, v242
mm v[200:203],a[64:67],a[16:19],v[200:203]
wt lgkmcnt(0)
st v[152:155],v245,s[44:47],s91 offen offset:2048 sc1
v_mul_f32 v94, v94, v242
v_mul_f32 v95, v95, v242
mm v[196:199],a[80:83],a[0:3],0
ex v193,v193
cv v172,v88,v89
cv v173,v90,v91
cv v174,v92,v93
mm v[196:199],a[84:87],a[4:7],v[196:199]
cv v224,v192,v193
cv v175,v94,v95
v_mul_f32 v96, v96, v243
v_mul_f32 v97, v97, v243
v_mul_f32 v98, v98, v243
mm v[196:199],a[88:91],a[8:11],v[196:199]
ex v194,v194
v_mul_f32 v99, v99, v243
v_mul_f32 v100, v100, v243
v_mul_f32 v101, v101, v243
mm v[196:199],a[92:95],a[12:15],v[196:199]
ld v255,s[60:63],s17 offen offset:1024 sc1 lds
v_mul_f32 v102, v102, v243
v_mul_f32 v103, v103, v243
cv v176,v96,v97
mm v[204:207],a[92:95],a[28:31],0
ex v195,v195
cv v177,v98,v99
cv v178,v100,v101
cv v179,v102,v103
mm v[204:207],a[88:91],a[24:27],v[204:207]
cv v225,v194,v195
st v[156:159],v245,s[44:47],s91 offen offset:3072 sc1
v_mul_f32 v104, v104, v243
v_mul_f32 v105, v105, v243
mm v[204:207],a[84:87],a[20:23],v[204:207]
ex v200,v200
v_mul_f32 v106, v106, v243
v_mul_f32 v107, v107, v243
v_mul_f32 v108, v108, v243
mm v[204:207],a[80:83],a[16:19],v[204:207]
ld v255,s[60:63],s17 offen offset:2048 sc1 lds
v_mul_f32 v109, v109, v243
v_mul_f32 v110, v110, v243
v_mul_f32 v111, v111, v243
mm v[208:211],a[64:67],a[32:35],0
ex v201,v201
cv v180,v104,v105
cv v181,v106,v107
cv v182,v108,v109
mm v[208:211],a[68:71],a[36:39],v[208:211]
cv v228,v200,v201
cv v183,v110,v111
v_mul_f32 v112, v112, v243
v_mul_f32 v113, v113, v243
v_mul_f32 v114, v114, v243
mm v[208:211],a[72:75],a[40:43],v[208:211]
ex v202,v202
v_mul_f32 v115, v115, v243
v_mul_f32 v116, v116, v243
v_mul_f32 v117, v117, v243
mm v[208:211],a[76:79],a[44:47],v[208:211]
ld v255,s[60:63],s17 offen offset:3072 sc1 lds
v_mul_f32 v118, v118, v243
v_mul_f32 v119, v119, v243
cv v184,v112,v113
mm v[216:219],a[76:79],a[60:63],0
ex v203,v203
cv v185,v114,v115
cv v186,v116,v117
cv v187,v118,v119
mm v[216:219],a[72:75],a[56:59],v[216:219]
cv v229,v202,v203
v_mul_f32 v120, v120, v243
v_mul_f32 v121, v121, v243
v_mul_f32 v122, v122, v243
v_mul_f32 v123, v123, v243
mm v[216:219],a[68:71],a[52:55],v[216:219]
ex v196,v196
v_mul_f32 v124, v124, v243
v_mul_f32 v125, v125, v243
v_mul_f32 v126, v126, v243
mm v[216:219],a[64:67],a[48:51],v[216:219]
v_mul_f32 v127, v127, v243
cv v188,v120,v121
cv v189,v122,v123
cv v190,v124,v125
cv v191,v126,v127
mm v[212:215],a[80:83],a[32:35],0
ex v197,v197
s_add_u32 s17, s17, 8192
mm v[212:215],a[84:87],a[36:39],v[212:215]
cv v226,v196,v197
s_add_u32 m0, m0, 16384
s_and_b32 m0, m0, s78
mm v[212:215],a[88:91],a[40:43],v[212:215]
ex v198,v198
mm v[212:215],a[92:95],a[44:47],v[212:215]
st v[160:163],v246,s[44:47],s91 offen offset:0 sc1
mm v[220:223],a[92:95],a[60:63],0
ex v199,v199
mm v[220:223],a[88:91],a[56:59],v[220:223]
cv v227,v198,v199
mm v[220:223],a[84:87],a[52:55],v[220:223]
ex v204,v204
mm v[220:223],a[80:83],a[48:51],v[220:223]
wt vmcnt(18)
s_barrier
mm v[64:67],a[96:99],v[232:235],0
ex v205,v205
mm v[96:99],a[96:99],v[236:239],0
rd a[96:99],v248 offset:8192
cv v230,v204,v205
mm v[100:103],a[100:103],v[236:239],0
ex v206,v206
mm v[68:71],a[100:103],v[232:235],0
rd a[100:103],v248 offset:9216
st v[164:167],v246,s[44:47],s91 offen offset:1024 sc1
mm v[72:75],a[104:107],v[232:235],0
ex v207,v207
mm v[104:107],a[104:107],v[236:239],0
rd a[104:107],v248 offset:10240
cv v231,v206,v207
mm v[108:111],a[108:111],v[236:239],0
ex v208,v208
mm v[76:79],a[108:111],v[232:235],0
rd a[108:111],v248 offset:11264
st v[168:171],v246,s[44:47],s91 offen offset:2048 sc1
mm v[80:83],a[112:115],v[232:235],0
ex v209,v209
mm v[112:115],a[112:115],v[236:239],0
rd a[112:115],v248 offset:12288
mm v[116:119],a[116:119],v[236:239],0
ex v210,v210
mm v[84:87],a[116:119],v[232:235],0
rd a[116:119],v248 offset:13312
st v[172:175],v246,s[44:47],s91 offen offset:3072 sc1
mm v[88:91],a[120:123],v[232:235],0
ex v211,v211
mm v[120:123],a[120:123],v[236:239],0
rd a[120:123],v248 offset:14336
mm v[124:127],a[124:127],v[236:239],0
ex v216,v216
mm v[92:95],a[124:127],v[232:235],0
rd a[124:127],v248 offset:15360
v_or_b32 v248, s89, v255
st v[176:179],v247,s[44:47],s91 offen offset:0 sc1
v_smfmac_f32_16x16x64_bf16 a[196:199], a[208:211], v[232:239], v241
wt lgkmcnt(4)
v_smfmac_f32_16x16x64_bf16 a[192:195], a[208:211], v[224:231], v241
rd a[64:67],v248 offset:0
mm v[0:3],a[96:99],v[224:227],v[0:3]
ex v217,v217
mm v[32:35],a[96:99],v[228:231],v[32:35]
rd a[68:71],v248 offset:1024
cv v232,v208,v209
mm v[36:39],a[100:103],v[228:231],v[36:39]
ex v218,v218
mm v[4:7],a[100:103],v[224:227],v[4:7]
rd a[72:75],v248 offset:2048
cv v233,v210,v211
st v[180:183],v247,s[44:47],s91 offen offset:1024 sc1
mm v[8:11],a[104:107],v[224:227],v[8:11]
ex v219,v219
mm v[40:43],a[104:107],v[228:231],v[40:43]
rd a[76:79],v248 offset:3072
cv v236,v216,v217
mm v[44:47],a[108:111],v[228:231],v[44:47]
ex v212,v212
mm v[12:15],a[108:111],v[224:227],v[12:15]
rd a[80:83],v248 offset:4096
wt lgkmcnt(5)
mm v[16:19],a[112:115],v[224:227],v[16:19]
ex v213,v213
mm v[48:51],a[112:115],v[228:231],v[48:51]
rd a[84:87],v248 offset:5120
cv v237,v218,v219
st v[184:187],v247,s[44:47],s91 offen offset:2048 sc1
mm v[52:55],a[116:119],v[228:231],v[52:55]
ex v214,v214
mm v[20:23],a[116:119],v[224:227],v[20:23]
rd a[88:91],v248 offset:6144
cv v234,v212,v213
mm v[24:27],a[120:123],v[224:227],v[24:27]
ex v215,v215
mm v[56:59],a[120:123],v[228:231],v[56:59]
rd a[92:95],v248 offset:7168
cv v235,v214,v215
st v[188:191],v247,s[44:47],s91 offen offset:3072 sc1
mm v[60:63],a[124:127],v[228:231],v[60:63]
ex v220,v220
mm v[28:31],a[124:127],v[224:227],v[28:31]
s_add_u32 s85, s85, 1
s_cmp_eq_u32 s85, 22
s_cbranch_scc1 .La5_LA_F3
.p2align 6
.La5_LA_F2:
wt lgkmcnt(4)
mm v[192:195],a[64:67],a[0:3],0
ex v221,v221
mm v[192:195],a[68:71],a[4:7],v[192:195]
cv v238,v220,v221
s_add_u32 s85, s85, 1
mm v[192:195],a[72:75],a[8:11],v[192:195]
ex v222,v222
mm v[192:195],a[76:79],a[12:15],v[192:195]
ld v255,s[60:63],s17 offen offset:0 sc1 lds
mm v[200:203],a[76:79],a[28:31],0
ex v223,v223
mm v[200:203],a[72:75],a[24:27],v[200:203]
cv v239,v222,v223
s_add_u32 s89, s89, 16384
mm v[200:203],a[68:71],a[20:23],v[200:203]
ex v192,v192
mm v[200:203],a[64:67],a[16:19],v[200:203]
wt lgkmcnt(0)
s_and_b32 s89, s89, 0x1ffff
mm v[196:199],a[80:83],a[0:3],0
ex v193,v193
mm v[196:199],a[84:87],a[4:7],v[196:199]
cv v224,v192,v193
mm v[196:199],a[88:91],a[8:11],v[196:199]
ex v194,v194
mm v[196:199],a[92:95],a[12:15],v[196:199]
ld v255,s[60:63],s17 offen offset:1024 sc1 lds
mm v[204:207],a[92:95],a[28:31],0
ex v195,v195
mm v[204:207],a[88:91],a[24:27],v[204:207]
cv v225,v194,v195
mm v[204:207],a[84:87],a[20:23],v[204:207]
ex v200,v200
mm v[204:207],a[80:83],a[16:19],v[204:207]
ld v255,s[60:63],s17 offen offset:2048 sc1 lds
mm v[208:211],a[64:67],a[32:35],0
ex v201,v201
mm v[208:211],a[68:71],a[36:39],v[208:211]
cv v228,v200,v201
mm v[208:211],a[72:75],a[40:43],v[208:211]
ex v202,v202
mm v[208:211],a[76:79],a[44:47],v[208:211]
ld v255,s[60:63],s17 offen offset:3072 sc1 lds
mm v[216:219],a[76:79],a[60:63],0
ex v203,v203
mm v[216:219],a[72:75],a[56:59],v[216:219]
cv v229,v202,v203
mm v[216:219],a[68:71],a[52:55],v[216:219]
ex v196,v196
mm v[216:219],a[64:67],a[48:51],v[216:219]
buffer_load_dword a253, v254, s[36:39], s90 offen
mm v[212:215],a[80:83],a[32:35],0
ex v197,v197
mm v[212:215],a[84:87],a[36:39],v[212:215]
cv v226,v196,v197
s_add_u32 s17, s17, 8192
mm v[212:215],a[88:91],a[40:43],v[212:215]
ex v198,v198
mm v[212:215],a[92:95],a[44:47],v[212:215]
s_add_u32 m0, m0, 16384
s_and_b32 m0, m0, s78
mm v[220:223],a[92:95],a[60:63],0
ex v199,v199
mm v[220:223],a[88:91],a[56:59],v[220:223]
cv v227,v198,v199
s_add_u32 s90, s90, s21
mm v[220:223],a[84:87],a[52:55],v[220:223]
ex v204,v204
mm v[220:223],a[80:83],a[48:51],v[220:223]
wt vmcnt(11)
s_barrier
mm v[64:67],a[96:99],v[232:235],v[64:67]
ex v205,v205
mm v[96:99],a[96:99],v[236:239],v[96:99]
rd a[96:99],v248 offset:8192
cv v230,v204,v205
mm v[100:103],a[100:103],v[236:239],v[100:103]
ex v206,v206
mm v[68:71],a[100:103],v[232:235],v[68:71]
rd a[100:103],v248 offset:9216
mm v[72:75],a[104:107],v[232:235],v[72:75]
ex v207,v207
mm v[104:107],a[104:107],v[236:239],v[104:107]
rd a[104:107],v248 offset:10240
cv v231,v206,v207
mm v[108:111],a[108:111],v[236:239],v[108:111]
ex v208,v208
mm v[76:79],a[108:111],v[232:235],v[76:79]
rd a[108:111],v248 offset:11264
mm v[80:83],a[112:115],v[232:235],v[80:83]
ex v209,v209
mm v[112:115],a[112:115],v[236:239],v[112:115]
rd a[112:115],v248 offset:12288
mm v[116:119],a[116:119],v[236:239],v[116:119]
ex v210,v210
mm v[84:87],a[116:119],v[232:235],v[84:87]
rd a[116:119],v248 offset:13312
mm v[88:91],a[120:123],v[232:235],v[88:91]
ex v211,v211
mm v[120:123],a[120:123],v[236:239],v[120:123]
rd a[120:123],v248 offset:14336
s_bitcmp1_b64 s[98:99], s85
mm v[124:127],a[124:127],v[236:239],v[124:127]
ex v216,v216
mm v[92:95],a[124:127],v[232:235],v[92:95]
rd a[124:127],v248 offset:15360
v_or_b32 v248, s89, v255
v_smfmac_f32_16x16x64_bf16 a[196:199], a[208:211], v[232:239], v241
wt lgkmcnt(4)
v_smfmac_f32_16x16x64_bf16 a[192:195], a[208:211], v[224:231], v241
rd a[64:67],v248 offset:0
mm v[0:3],a[96:99],v[224:227],v[0:3]
ex v217,v217
mm v[32:35],a[96:99],v[228:231],v[32:35]
rd a[68:71],v248 offset:1024
cv v232,v208,v209
mm v[36:39],a[100:103],v[228:231],v[36:39]
ex v218,v218
mm v[4:7],a[100:103],v[224:227],v[4:7]
rd a[72:75],v248 offset:2048
cv v233,v210,v211
mm v[8:11],a[104:107],v[224:227],v[8:11]
ex v219,v219
mm v[40:43],a[104:107],v[228:231],v[40:43]
rd a[76:79],v248 offset:3072
cv v236,v216,v217
mm v[44:47],a[108:111],v[228:231],v[44:47]
ex v212,v212
mm v[12:15],a[108:111],v[224:227],v[12:15]
rd a[80:83],v248 offset:4096
wt lgkmcnt(5)
mm v[16:19],a[112:115],v[224:227],v[16:19]
ex v213,v213
mm v[48:51],a[112:115],v[228:231],v[48:51]
rd a[84:87],v248 offset:5120
cv v237,v218,v219
mm v[52:55],a[116:119],v[228:231],v[52:55]
ex v214,v214
mm v[20:23],a[116:119],v[224:227],v[20:23]
rd a[88:91],v248 offset:6144
cv v234,v212,v213
mm v[24:27],a[120:123],v[224:227],v[24:27]
ex v215,v215
mm v[56:59],a[120:123],v[228:231],v[56:59]
rd a[92:95],v248 offset:7168
cv v235,v214,v215
mm v[60:63],a[124:127],v[228:231],v[60:63]
ex v220,v220
mm v[28:31],a[124:127],v[224:227],v[28:31]
s_cbranch_scc1 .La5_LA_F2E2
wt lgkmcnt(4)
mm v[192:195],a[64:67],a[0:3],0
ex v221,v221
mm v[192:195],a[68:71],a[4:7],v[192:195]
cv v238,v220,v221
s_add_u32 s85, s85, 1
mm v[192:195],a[72:75],a[8:11],v[192:195]
ex v222,v222
mm v[192:195],a[76:79],a[12:15],v[192:195]
ld v255,s[60:63],s17 offen offset:0 sc1 lds
mm v[200:203],a[76:79],a[28:31],0
ex v223,v223
mm v[200:203],a[72:75],a[24:27],v[200:203]
cv v239,v222,v223
s_add_u32 s89, s89, 16384
mm v[200:203],a[68:71],a[20:23],v[200:203]
ex v192,v192
mm v[200:203],a[64:67],a[16:19],v[200:203]
wt lgkmcnt(0)
s_and_b32 s89, s89, 0x1ffff
mm v[196:199],a[80:83],a[0:3],0
ex v193,v193
mm v[196:199],a[84:87],a[4:7],v[196:199]
cv v224,v192,v193
mm v[196:199],a[88:91],a[8:11],v[196:199]
ex v194,v194
mm v[196:199],a[92:95],a[12:15],v[196:199]
ld v255,s[60:63],s17 offen offset:1024 sc1 lds
mm v[204:207],a[92:95],a[28:31],0
ex v195,v195
mm v[204:207],a[88:91],a[24:27],v[204:207]
cv v225,v194,v195
mm v[204:207],a[84:87],a[20:23],v[204:207]
ex v200,v200
mm v[204:207],a[80:83],a[16:19],v[204:207]
ld v255,s[60:63],s17 offen offset:2048 sc1 lds
mm v[208:211],a[64:67],a[32:35],0
ex v201,v201
mm v[208:211],a[68:71],a[36:39],v[208:211]
cv v228,v200,v201
mm v[208:211],a[72:75],a[40:43],v[208:211]
ex v202,v202
mm v[208:211],a[76:79],a[44:47],v[208:211]
ld v255,s[60:63],s17 offen offset:3072 sc1 lds
mm v[216:219],a[76:79],a[60:63],0
ex v203,v203
mm v[216:219],a[72:75],a[56:59],v[216:219]
cv v229,v202,v203
mm v[216:219],a[68:71],a[52:55],v[216:219]
ex v196,v196
mm v[216:219],a[64:67],a[48:51],v[216:219]
buffer_load_dword a253, v254, s[36:39], s90 offen
mm v[212:215],a[80:83],a[32:35],0
ex v197,v197
mm v[212:215],a[84:87],a[36:39],v[212:215]
cv v226,v196,v197
s_add_u32 s17, s17, 8192
mm v[212:215],a[88:91],a[40:43],v[212:215]
ex v198,v198
mm v[212:215],a[92:95],a[44:47],v[212:215]
s_add_u32 m0, m0, 16384
s_and_b32 m0, m0, s78
mm v[220:223],a[92:95],a[60:63],0
ex v199,v199
mm v[220:223],a[88:91],a[56:59],v[220:223]
cv v227,v198,v199
s_add_u32 s90, s90, s21
mm v[220:223],a[84:87],a[52:55],v[220:223]
ex v204,v204
mm v[220:223],a[80:83],a[48:51],v[220:223]
wt vmcnt(11)
s_barrier
mm v[64:67],a[96:99],v[232:235],v[64:67]
ex v205,v205
mm v[96:99],a[96:99],v[236:239],v[96:99]
rd a[96:99],v248 offset:8192
cv v230,v204,v205
mm v[100:103],a[100:103],v[236:239],v[100:103]
ex v206,v206
mm v[68:71],a[100:103],v[232:235],v[68:71]
rd a[100:103],v248 offset:9216
mm v[72:75],a[104:107],v[232:235],v[72:75]
ex v207,v207
mm v[104:107],a[104:107],v[236:239],v[104:107]
rd a[104:107],v248 offset:10240
cv v231,v206,v207
mm v[108:111],a[108:111],v[236:239],v[108:111]
ex v208,v208
mm v[76:79],a[108:111],v[232:235],v[76:79]
rd a[108:111],v248 offset:11264
mm v[80:83],a[112:115],v[232:235],v[80:83]
ex v209,v209
mm v[112:115],a[112:115],v[236:239],v[112:115]
rd a[112:115],v248 offset:12288
mm v[116:119],a[116:119],v[236:239],v[116:119]
ex v210,v210
mm v[84:87],a[116:119],v[232:235],v[84:87]
rd a[116:119],v248 offset:13312
mm v[88:91],a[120:123],v[232:235],v[88:91]
ex v211,v211
mm v[120:123],a[120:123],v[236:239],v[120:123]
rd a[120:123],v248 offset:14336
.La5_LW6_3:
s_cbranch_execz .La5_LW6_7
s_bitcmp1_b64 s[98:99], s85
mm v[124:127],a[124:127],v[236:239],v[124:127]
ex v216,v216
mm v[92:95],a[124:127],v[232:235],v[92:95]
rd a[124:127],v248 offset:15360
v_or_b32 v248, s89, v255
v_smfmac_f32_16x16x64_bf16 a[196:199], a[208:211], v[232:239], v241
wt lgkmcnt(4)
v_smfmac_f32_16x16x64_bf16 a[192:195], a[208:211], v[224:231], v241
rd a[64:67],v248 offset:0
mm v[0:3],a[96:99],v[224:227],v[0:3]
ex v217,v217
mm v[32:35],a[96:99],v[228:231],v[32:35]
rd a[68:71],v248 offset:1024
cv v232,v208,v209
mm v[36:39],a[100:103],v[228:231],v[36:39]
ex v218,v218
mm v[4:7],a[100:103],v[224:227],v[4:7]
rd a[72:75],v248 offset:2048
cv v233,v210,v211
mm v[8:11],a[104:107],v[224:227],v[8:11]
ex v219,v219
mm v[40:43],a[104:107],v[228:231],v[40:43]
rd a[76:79],v248 offset:3072
cv v236,v216,v217
mm v[44:47],a[108:111],v[228:231],v[44:47]
ex v212,v212
mm v[12:15],a[108:111],v[224:227],v[12:15]
rd a[80:83],v248 offset:4096
wt lgkmcnt(5)
mm v[16:19],a[112:115],v[224:227],v[16:19]
ex v213,v213
mm v[48:51],a[112:115],v[228:231],v[48:51]
rd a[84:87],v248 offset:5120
cv v237,v218,v219
mm v[52:55],a[116:119],v[228:231],v[52:55]
ex v214,v214
mm v[20:23],a[116:119],v[224:227],v[20:23]
rd a[88:91],v248 offset:6144
cv v234,v212,v213
mm v[24:27],a[120:123],v[224:227],v[24:27]
ex v215,v215
mm v[56:59],a[120:123],v[228:231],v[56:59]
rd a[92:95],v248 offset:7168
cv v235,v214,v215
mm v[60:63],a[124:127],v[228:231],v[60:63]
ex v220,v220
mm v[28:31],a[124:127],v[224:227],v[28:31]
s_cbranch_scc0 .La5_LA_F2
.La5_LA_F2E2:
.La5_LA_F3:
.La5_LW6_4:
s_cbranch_execz .La5_LW6_8
wt lgkmcnt(4)
mm v[192:195],a[64:67],a[0:3],0
ex v221,v221
mm v[192:195],a[68:71],a[4:7],v[192:195]
cv v238,v220,v221
s_add_u32 s89, s89, 16384
mm v[192:195],a[72:75],a[8:11],v[192:195]
ex v222,v222
mm v[192:195],a[76:79],a[12:15],v[192:195]
ld v255,s[60:63],s17 offen offset:0 sc1 lds
mm v[200:203],a[76:79],a[28:31],0
ex v223,v223
mm v[200:203],a[72:75],a[24:27],v[200:203]
cv v239,v222,v223
s_and_b32 s89, s89, 0x1ffff
mm v[200:203],a[68:71],a[20:23],v[200:203]
ex v192,v192
mm v[200:203],a[64:67],a[16:19],v[200:203]
wt lgkmcnt(0)
mm v[196:199],a[80:83],a[0:3],0
ex v193,v193
mm v[196:199],a[84:87],a[4:7],v[196:199]
cv v224,v192,v193
ar v243,a229
mm v[196:199],a[88:91],a[8:11],v[196:199]
ex v194,v194
mm v[196:199],a[92:95],a[12:15],v[196:199]
ld v255,s[60:63],s17 offen offset:1024 sc1 lds
mm v[204:207],a[92:95],a[28:31],0
ex v195,v195
mm v[204:207],a[88:91],a[24:27],v[204:207]
cv v225,v194,v195
mm v[204:207],a[84:87],a[20:23],v[204:207]
ex v200,v200
mm v[204:207],a[80:83],a[16:19],v[204:207]
ld v255,s[60:63],s17 offen offset:2048 sc1 lds
mm v[208:211],a[64:67],a[32:35],0
ex v201,v201
mm v[208:211],a[68:71],a[36:39],v[208:211]
cv v228,v200,v201
mm v[208:211],a[72:75],a[40:43],v[208:211]
ex v202,v202
mm v[208:211],a[76:79],a[44:47],v[208:211]
ld v255,s[60:63],s17 offen offset:3072 sc1 lds
mm v[216:219],a[76:79],a[60:63],0
ex v203,v203
mm v[216:219],a[72:75],a[56:59],v[216:219]
cv v229,v202,v203
s_mov_b32 s93, 0
mm v[216:219],a[68:71],a[52:55],v[216:219]
ex v196,v196
mm v[216:219],a[64:67],a[48:51],v[216:219]
ld a[128:131],v243,s[40:43],s93 offen
mm v[212:215],a[80:83],a[32:35],0
ex v197,v197
s_add_u32 s17, s17, 8192
mm v[212:215],a[84:87],a[36:39],v[212:215]
cv v226,v196,v197
s_mov_b32 s93, 7168
s_add_u32 m0, m0, 16384
mm v[212:215],a[88:91],a[40:43],v[212:215]
ex v198,v198
s_and_b32 m0, m0, s78
mm v[212:215],a[92:95],a[44:47],v[212:215]
ld a[132:135],v243,s[40:43],s93 offen
mm v[220:223],a[92:95],a[60:63],0
ex v199,v199
mm v[220:223],a[88:91],a[56:59],v[220:223]
cv v227,v198,v199
mm v[220:223],a[84:87],a[52:55],v[220:223]
ex v204,v204
mm v[220:223],a[80:83],a[48:51],v[220:223]
wt vmcnt(12)
s_barrier
mm v[64:67],a[96:99],v[232:235],v[64:67]
ex v205,v205
mm v[96:99],a[96:99],v[236:239],v[96:99]
rd a[96:99],v248 offset:8192
cv v230,v204,v205
mm v[100:103],a[100:103],v[236:239],v[100:103]
ex v206,v206
mm v[68:71],a[100:103],v[232:235],v[68:71]
rd a[100:103],v248 offset:9216
mm v[72:75],a[104:107],v[232:235],v[72:75]
ex v207,v207
mm v[104:107],a[104:107],v[236:239],v[104:107]
rd a[104:107],v248 offset:10240
cv v231,v206,v207
s_mov_b32 s93, 14336
mm v[108:111],a[108:111],v[236:239],v[108:111]
ex v208,v208
mm v[76:79],a[108:111],v[232:235],v[76:79]
rd a[108:111],v248 offset:11264
ld a[136:139],v243,s[40:43],s93 offen
mm v[80:83],a[112:115],v[232:235],v[80:83]
ex v209,v209
mm v[112:115],a[112:115],v[236:239],v[112:115]
rd a[112:115],v248 offset:12288
mm v[116:119],a[116:119],v[236:239],v[116:119]
ex v210,v210
mm v[84:87],a[116:119],v[232:235],v[84:87]
rd a[116:119],v248 offset:13312
mm v[88:91],a[120:123],v[232:235],v[88:91]
ex v211,v211
mm v[120:123],a[120:123],v[236:239],v[120:123]
rd a[120:123],v248 offset:14336
mm v[124:127],a[124:127],v[236:239],v[124:127]
ex v216,v216
mm v[92:95],a[124:127],v[232:235],v[92:95]
rd a[124:127],v248 offset:15360
v_or_b32 v248, s89, v255
v_smfmac_f32_16x16x64_bf16 a[196:199], a[208:211], v[232:239], v241
wt lgkmcnt(4)
v_smfmac_f32_16x16x64_bf16 a[192:195], a[208:211], v[224:231], v241
rd a[64:67],v248 offset:0
mm v[0:3],a[96:99],v[224:227],v[0:3]
ex v217,v217
mm v[32:35],a[96:99],v[228:231],v[32:35]
rd a[68:71],v248 offset:1024
cv v232,v208,v209
mm v[36:39],a[100:103],v[228:231],v[36:39]
ex v218,v218
mm v[4:7],a[100:103],v[224:227],v[4:7]
rd a[72:75],v248 offset:2048
cv v233,v210,v211
mm v[8:11],a[104:107],v[224:227],v[8:11]
ex v219,v219
mm v[40:43],a[104:107],v[228:231],v[40:43]
rd a[76:79],v248 offset:3072
cv v236,v216,v217
mm v[44:47],a[108:111],v[228:231],v[44:47]
ex v212,v212
mm v[12:15],a[108:111],v[224:227],v[12:15]
rd a[80:83],v248 offset:4096
wt lgkmcnt(5)
mm v[16:19],a[112:115],v[224:227],v[16:19]
ex v213,v213
mm v[48:51],a[112:115],v[228:231],v[48:51]
rd a[84:87],v248 offset:5120
cv v237,v218,v219
mm v[52:55],a[116:119],v[228:231],v[52:55]
ex v214,v214
mm v[20:23],a[116:119],v[224:227],v[20:23]
rd a[88:91],v248 offset:6144
cv v234,v212,v213
mm v[24:27],a[120:123],v[224:227],v[24:27]
ex v215,v215
mm v[56:59],a[120:123],v[228:231],v[56:59]
rd a[92:95],v248 offset:7168
cv v235,v214,v215
mm v[60:63],a[124:127],v[228:231],v[60:63]
ex v220,v220
mm v[28:31],a[124:127],v[224:227],v[28:31]
wt lgkmcnt(4)
mm v[192:195],a[64:67],a[0:3],0
ex v221,v221
mm v[192:195],a[68:71],a[4:7],v[192:195]
cv v238,v220,v221
s_add_u32 s89, s89, 16384
mm v[192:195],a[72:75],a[8:11],v[192:195]
ex v222,v222
mm v[192:195],a[76:79],a[12:15],v[192:195]
ld v255,s[60:63],s17 offen offset:0 sc1 lds
mm v[200:203],a[76:79],a[28:31],0
ex v223,v223
mm v[200:203],a[72:75],a[24:27],v[200:203]
cv v239,v222,v223
s_and_b32 s89, s89, 0x1ffff
mm v[200:203],a[68:71],a[20:23],v[200:203]
ex v192,v192
mm v[200:203],a[64:67],a[16:19],v[200:203]
wt lgkmcnt(0)
mm v[196:199],a[80:83],a[0:3],0
ex v193,v193
mm v[196:199],a[84:87],a[4:7],v[196:199]
cv v224,v192,v193
mm v[196:199],a[88:91],a[8:11],v[196:199]
ex v194,v194
mm v[196:199],a[92:95],a[12:15],v[196:199]
ld v255,s[60:63],s17 offen offset:1024 sc1 lds
mm v[204:207],a[92:95],a[28:31],0
ex v195,v195
mm v[204:207],a[88:91],a[24:27],v[204:207]
cv v225,v194,v195
mm v[204:207],a[84:87],a[20:23],v[204:207]
ex v200,v200
mm v[204:207],a[80:83],a[16:19],v[204:207]
ld v255,s[60:63],s17 offen offset:2048 sc1 lds
mm v[208:211],a[64:67],a[32:35],0
ex v201,v201
mm v[208:211],a[68:71],a[36:39],v[208:211]
cv v228,v200,v201
mm v[208:211],a[72:75],a[40:43],v[208:211]
ex v202,v202
mm v[208:211],a[76:79],a[44:47],v[208:211]
ld v255,s[60:63],s17 offen offset:3072 sc1 lds
mm v[216:219],a[76:79],a[60:63],0
ex v203,v203
mm v[216:219],a[72:75],a[56:59],v[216:219]
cv v229,v202,v203
s_mov_b32 s93, 21504
mm v[216:219],a[68:71],a[52:55],v[216:219]
ex v196,v196
mm v[216:219],a[64:67],a[48:51],v[216:219]
ld a[140:143],v243,s[40:43],s93 offen
mm v[212:215],a[80:83],a[32:35],0
ex v197,v197
s_add_u32 s17, s17, 8192
mm v[212:215],a[84:87],a[36:39],v[212:215]
cv v226,v196,v197
s_mov_b32 s93, 28672
s_add_u32 m0, m0, 16384
mm v[212:215],a[88:91],a[40:43],v[212:215]
ex v198,v198
s_and_b32 m0, m0, s78
mm v[212:215],a[92:95],a[44:47],v[212:215]
ld a[144:147],v243,s[40:43],s93 offen
mm v[220:223],a[92:95],a[60:63],0
ex v199,v199
mm v[220:223],a[88:91],a[56:59],v[220:223]
cv v227,v198,v199
mm v[220:223],a[84:87],a[52:55],v[220:223]
ex v204,v204
mm v[220:223],a[80:83],a[48:51],v[220:223]
wt vmcnt(14)
s_barrier
mm v[64:67],a[96:99],v[232:235],v[64:67]
ex v205,v205
mm v[96:99],a[96:99],v[236:239],v[96:99]
rd a[96:99],v248 offset:8192
cv v230,v204,v205
mm v[100:103],a[100:103],v[236:239],v[100:103]
ex v206,v206
mm v[68:71],a[100:103],v[232:235],v[68:71]
rd a[100:103],v248 offset:9216
mm v[72:75],a[104:107],v[232:235],v[72:75]
ex v207,v207
mm v[104:107],a[104:107],v[236:239],v[104:107]
rd a[104:107],v248 offset:10240
cv v231,v206,v207
s_mov_b32 s93, 35840
mm v[108:111],a[108:111],v[236:239],v[108:111]
ex v208,v208
mm v[76:79],a[108:111],v[232:235],v[76:79]
rd a[108:111],v248 offset:11264
ld a[148:151],v243,s[40:43],s93 offen
mm v[80:83],a[112:115],v[232:235],v[80:83]
ex v209,v209
mm v[112:115],a[112:115],v[236:239],v[112:115]
rd a[112:115],v248 offset:12288
mm v[116:119],a[116:119],v[236:239],v[116:119]
ex v210,v210
mm v[84:87],a[116:119],v[232:235],v[84:87]
rd a[116:119],v248 offset:13312
mm v[88:91],a[120:123],v[232:235],v[88:91]
ex v211,v211
mm v[120:123],a[120:123],v[236:239],v[120:123]
rd a[120:123],v248 offset:14336
ar v242,a231
wt vmcnt(14)
mm v[124:127],a[124:127],v[236:239],v[124:127]
ex v216,v216
mm v[92:95],a[124:127],v[232:235],v[92:95]
rd a[124:127],v248 offset:15360
v_or_b32 v248, s89, v255
v_smfmac_f32_16x16x64_bf16 a[196:199], a[208:211], v[232:239], v241
wt lgkmcnt(4)
v_smfmac_f32_16x16x64_bf16 a[192:195], a[208:211], v[224:231], v241
rd a[64:67],v248 offset:0
mm v[0:3],a[96:99],v[224:227],v[0:3]
ex v217,v217
mm v[32:35],a[96:99],v[228:231],v[32:35]
rd a[68:71],v248 offset:1024
cv v232,v208,v209
mm v[36:39],a[100:103],v[228:231],v[36:39]
ex v218,v218
mm v[4:7],a[100:103],v[224:227],v[4:7]
rd a[72:75],v248 offset:2048
cv v233,v210,v211
mm v[8:11],a[104:107],v[224:227],v[8:11]
ex v219,v219
mm v[40:43],a[104:107],v[228:231],v[40:43]
rd a[76:79],v248 offset:3072
cv v236,v216,v217
mm v[44:47],a[108:111],v[228:231],v[44:47]
ex v212,v212
mm v[12:15],a[108:111],v[224:227],v[12:15]
rd a[80:83],v248 offset:4096
wt lgkmcnt(5)
mm v[16:19],a[112:115],v[224:227],v[16:19]
ex v213,v213
mm v[48:51],a[112:115],v[228:231],v[48:51]
rd a[84:87],v248 offset:5120
cv v237,v218,v219
mm v[52:55],a[116:119],v[228:231],v[52:55]
ex v214,v214
mm v[20:23],a[116:119],v[224:227],v[20:23]
rd a[88:91],v248 offset:6144
cv v234,v212,v213
mm v[24:27],a[120:123],v[224:227],v[24:27]
ex v215,v215
mm v[56:59],a[120:123],v[228:231],v[56:59]
rd a[92:95],v248 offset:7168
cv v235,v214,v215
mm v[60:63],a[124:127],v[228:231],v[60:63]
ex v220,v220
mm v[28:31],a[124:127],v[224:227],v[28:31]
buffer_store_dwordx2 v[252:253], v242, s[64:67], s6 offen offset:0 sc1
wt lgkmcnt(4)
mm v[192:195],a[64:67],a[0:3],0
ex v221,v221
mm v[192:195],a[68:71],a[4:7],v[192:195]
cv v238,v220,v221
s_add_u32 s89, s89, 16384
mm v[192:195],a[72:75],a[8:11],v[192:195]
ex v222,v222
mm v[192:195],a[76:79],a[12:15],v[192:195]
ld v255,s[60:63],s17 offen offset:0 sc1 lds
mm v[200:203],a[76:79],a[28:31],0
ex v223,v223
mm v[200:203],a[72:75],a[24:27],v[200:203]
cv v239,v222,v223
s_and_b32 s89, s89, 0x1ffff
mm v[200:203],a[68:71],a[20:23],v[200:203]
ex v192,v192
mm v[200:203],a[64:67],a[16:19],v[200:203]
wt lgkmcnt(0)
mm v[196:199],a[80:83],a[0:3],0
ex v193,v193
mm v[196:199],a[84:87],a[4:7],v[196:199]
cv v224,v192,v193
mm v[196:199],a[88:91],a[8:11],v[196:199]
ex v194,v194
mm v[196:199],a[92:95],a[12:15],v[196:199]
ld v255,s[60:63],s17 offen offset:1024 sc1 lds
mm v[204:207],a[92:95],a[28:31],0
ex v195,v195
mm v[204:207],a[88:91],a[24:27],v[204:207]
cv v225,v194,v195
mm v[204:207],a[84:87],a[20:23],v[204:207]
ex v200,v200
mm v[204:207],a[80:83],a[16:19],v[204:207]
ld v255,s[60:63],s17 offen offset:2048 sc1 lds
mm v[208:211],a[64:67],a[32:35],0
ex v201,v201
mm v[208:211],a[68:71],a[36:39],v[208:211]
cv v228,v200,v201
mm v[208:211],a[72:75],a[40:43],v[208:211]
ex v202,v202
mm v[208:211],a[76:79],a[44:47],v[208:211]
ld v255,s[60:63],s17 offen offset:3072 sc1 lds
mm v[216:219],a[76:79],a[60:63],0
ex v203,v203
mm v[216:219],a[72:75],a[56:59],v[216:219]
cv v229,v202,v203
s_mov_b32 s93, 43008
mm v[216:219],a[68:71],a[52:55],v[216:219]
ex v196,v196
mm v[216:219],a[64:67],a[48:51],v[216:219]
ld a[152:155],v243,s[40:43],s93 offen
mm v[212:215],a[80:83],a[32:35],0
ex v197,v197
s_add_u32 s17, s17, 8192
mm v[212:215],a[84:87],a[36:39],v[212:215]
cv v226,v196,v197
s_mov_b32 s93, 50176
s_add_u32 m0, m0, 16384
mm v[212:215],a[88:91],a[40:43],v[212:215]
ex v198,v198
s_and_b32 m0, m0, s78
mm v[212:215],a[92:95],a[44:47],v[212:215]
ld a[156:159],v243,s[40:43],s93 offen
mm v[220:223],a[92:95],a[60:63],0
ex v199,v199
mm v[220:223],a[88:91],a[56:59],v[220:223]
cv v227,v198,v199
mm v[220:223],a[84:87],a[52:55],v[220:223]
ex v204,v204
mm v[220:223],a[80:83],a[48:51],v[220:223]
wt vmcnt(17)
s_barrier
mm v[64:67],a[96:99],v[232:235],v[64:67]
ex v205,v205
mm v[96:99],a[96:99],v[236:239],v[96:99]
rd a[96:99],v248 offset:8192
cv v230,v204,v205
mm v[100:103],a[100:103],v[236:239],v[100:103]
ex v206,v206
mm v[68:71],a[100:103],v[232:235],v[68:71]
rd a[100:103],v248 offset:9216
mm v[72:75],a[104:107],v[232:235],v[72:75]
ex v207,v207
mm v[104:107],a[104:107],v[236:239],v[104:107]
rd a[104:107],v248 offset:10240
cv v231,v206,v207
s_mov_b32 s93, 57344
mm v[108:111],a[108:111],v[236:239],v[108:111]
ex v208,v208
mm v[76:79],a[108:111],v[232:235],v[76:79]
rd a[108:111],v248 offset:11264
ld a[160:163],v243,s[40:43],s93 offen
mm v[80:83],a[112:115],v[232:235],v[80:83]
ex v209,v209
mm v[112:115],a[112:115],v[236:239],v[112:115]
rd a[112:115],v248 offset:12288
mm v[116:119],a[116:119],v[236:239],v[116:119]
ex v210,v210
mm v[84:87],a[116:119],v[232:235],v[84:87]
rd a[116:119],v248 offset:13312
mm v[88:91],a[120:123],v[232:235],v[88:91]
ex v211,v211
mm v[120:123],a[120:123],v[236:239],v[120:123]
rd a[120:123],v248 offset:14336
mm v[124:127],a[124:127],v[236:239],v[124:127]
ex v216,v216
mm v[92:95],a[124:127],v[232:235],v[92:95]
rd a[124:127],v248 offset:15360
v_or_b32 v248, s89, v255
v_smfmac_f32_16x16x64_bf16 a[196:199], a[208:211], v[232:239], v241
wt lgkmcnt(4)
v_smfmac_f32_16x16x64_bf16 a[192:195], a[208:211], v[224:231], v241
rd a[64:67],v248 offset:0
mm v[0:3],a[96:99],v[224:227],v[0:3]
ex v217,v217
mm v[32:35],a[96:99],v[228:231],v[32:35]
rd a[68:71],v248 offset:1024
cv v232,v208,v209
mm v[36:39],a[100:103],v[228:231],v[36:39]
ex v218,v218
mm v[4:7],a[100:103],v[224:227],v[4:7]
rd a[72:75],v248 offset:2048
cv v233,v210,v211
mm v[8:11],a[104:107],v[224:227],v[8:11]
ex v219,v219
mm v[40:43],a[104:107],v[228:231],v[40:43]
rd a[76:79],v248 offset:3072
cv v236,v216,v217
mm v[44:47],a[108:111],v[228:231],v[44:47]
ex v212,v212
mm v[12:15],a[108:111],v[224:227],v[12:15]
rd a[80:83],v248 offset:4096
wt lgkmcnt(5)
mm v[16:19],a[112:115],v[224:227],v[16:19]
ex v213,v213
mm v[48:51],a[112:115],v[228:231],v[48:51]
rd a[84:87],v248 offset:5120
cv v237,v218,v219
mm v[52:55],a[116:119],v[228:231],v[52:55]
ex v214,v214
mm v[20:23],a[116:119],v[224:227],v[20:23]
rd a[88:91],v248 offset:6144
cv v234,v212,v213
mm v[24:27],a[120:123],v[224:227],v[24:27]
ex v215,v215
mm v[56:59],a[120:123],v[228:231],v[56:59]
rd a[92:95],v248 offset:7168
cv v235,v214,v215
mm v[60:63],a[124:127],v[228:231],v[60:63]
ex v220,v220
mm v[28:31],a[124:127],v[224:227],v[28:31]
wt lgkmcnt(4)
mm v[192:195],a[64:67],a[0:3],0
ex v221,v221
mm v[192:195],a[68:71],a[4:7],v[192:195]
cv v238,v220,v221
ar v240,a228
mm v[192:195],a[72:75],a[8:11],v[192:195]
ex v222,v222
mm v[192:195],a[76:79],a[12:15],v[192:195]
s_add_u32 m0, s83, 16896
s_lshl_b32 s17, s72, 18
mm v[200:203],a[76:79],a[28:31],0
ex v223,v223
mm v[200:203],a[72:75],a[24:27],v[200:203]
cv v239,v222,v223
s_add_u32 s89, s89, 16384
mm v[200:203],a[68:71],a[20:23],v[200:203]
ex v192,v192
mm v[200:203],a[64:67],a[16:19],v[200:203]
wt lgkmcnt(0)
s_and_b32 s89, s89, 0x1ffff
mm v[196:199],a[80:83],a[0:3],0
ex v193,v193
mm v[196:199],a[84:87],a[4:7],v[196:199]
cv v224,v192,v193
mm v[196:199],a[88:91],a[8:11],v[196:199]
ex v194,v194
mm v[196:199],a[92:95],a[12:15],v[196:199]
ld v240,s[56:59],s17 offen lds
mm v[204:207],a[92:95],a[28:31],0
ex v195,v195
s_add_u32 s17, s17, 32768
mm v[204:207],a[88:91],a[24:27],v[204:207]
cv v225,v194,v195
s_add_u32 m0, m0, 1024
mm v[204:207],a[84:87],a[20:23],v[204:207]
ex v200,v200
mm v[204:207],a[80:83],a[16:19],v[204:207]
ld v240,s[56:59],s17 offen lds
mm v[208:211],a[64:67],a[32:35],0
ex v201,v201
s_add_u32 s17, s17, 4294942720
mm v[208:211],a[68:71],a[36:39],v[208:211]
cv v228,v200,v201
s_add_u32 m0, m0, 1152
mm v[208:211],a[72:75],a[40:43],v[208:211]
ex v202,v202
mm v[208:211],a[76:79],a[44:47],v[208:211]
ld v240,s[56:59],s17 offen lds
mm v[216:219],a[76:79],a[60:63],0
ex v203,v203
s_add_u32 s17, s17, 32768
mm v[216:219],a[72:75],a[56:59],v[216:219]
cv v229,v202,v203
s_add_u32 m0, m0, 1024
mm v[216:219],a[68:71],a[52:55],v[216:219]
ex v196,v196
mm v[216:219],a[64:67],a[48:51],v[216:219]
ld v240,s[56:59],s17 offen lds
mm v[212:215],a[80:83],a[32:35],0
ex v197,v197
s_add_u32 s17, s17, 90112
mm v[212:215],a[84:87],a[36:39],v[212:215]
cv v226,v196,v197
s_mov_b32 s93, 64512
s_add_u32 m0, s83, 33792
mm v[212:215],a[88:91],a[40:43],v[212:215]
ex v198,v198
mm v[212:215],a[92:95],a[44:47],v[212:215]
ld a[164:167],v243,s[40:43],s93 offen
mm v[220:223],a[92:95],a[60:63],0
ex v199,v199
mm v[220:223],a[88:91],a[56:59],v[220:223]
cv v227,v198,v199
mm v[220:223],a[84:87],a[52:55],v[220:223]
ex v204,v204
mm v[220:223],a[80:83],a[48:51],v[220:223]
wt vmcnt(16)
s_barrier
mm v[64:67],a[96:99],v[232:235],v[64:67]
ex v205,v205
.La5_LW6_5:
s_cbranch_execz .La5_LW6_R0
mm v[96:99],a[96:99],v[236:239],v[96:99]
rd a[96:99],v248 offset:8192
cv v230,v204,v205
mm v[100:103],a[100:103],v[236:239],v[100:103]
ex v206,v206
mm v[68:71],a[100:103],v[232:235],v[68:71]
rd a[100:103],v248 offset:9216
mm v[72:75],a[104:107],v[232:235],v[72:75]
ex v207,v207
mm v[104:107],a[104:107],v[236:239],v[104:107]
rd a[104:107],v248 offset:10240
cv v231,v206,v207
s_mov_b32 s93, 71680
mm v[108:111],a[108:111],v[236:239],v[108:111]
ex v208,v208
mm v[76:79],a[108:111],v[232:235],v[76:79]
rd a[108:111],v248 offset:11264
ld a[168:171],v243,s[40:43],s93 offen
mm v[80:83],a[112:115],v[232:235],v[80:83]
ex v209,v209
mm v[112:115],a[112:115],v[236:239],v[112:115]
rd a[112:115],v248 offset:12288
s_mov_b32 s93, 78848
mm v[116:119],a[116:119],v[236:239],v[116:119]
ex v210,v210
mm v[84:87],a[116:119],v[232:235],v[84:87]
rd a[116:119],v248 offset:13312
ld a[172:175],v243,s[40:43],s93 offen
mm v[88:91],a[120:123],v[232:235],v[88:91]
ex v211,v211
mm v[120:123],a[120:123],v[236:239],v[120:123]
rd a[120:123],v248 offset:14336
mm v[124:127],a[124:127],v[236:239],v[124:127]
ex v216,v216
mm v[92:95],a[124:127],v[232:235],v[92:95]
rd a[124:127],v248 offset:15360
v_or_b32 v248, s89, v255
v_smfmac_f32_16x16x64_bf16 a[196:199], a[208:211], v[232:239], v241
wt lgkmcnt(4)
v_smfmac_f32_16x16x64_bf16 a[192:195], a[208:211], v[224:231], v241
rd a[64:67],v248 offset:0
mm v[0:3],a[96:99],v[224:227],v[0:3]
ex v217,v217
mm v[32:35],a[96:99],v[228:231],v[32:35]
rd a[68:71],v248 offset:1024
cv v232,v208,v209
mm v[36:39],a[100:103],v[228:231],v[36:39]
ex v218,v218
mm v[4:7],a[100:103],v[224:227],v[4:7]
rd a[72:75],v248 offset:2048
cv v233,v210,v211
mm v[8:11],a[104:107],v[224:227],v[8:11]
ex v219,v219
mm v[40:43],a[104:107],v[228:231],v[40:43]
rd a[76:79],v248 offset:3072
cv v236,v216,v217
mm v[44:47],a[108:111],v[228:231],v[44:47]
ex v212,v212
mm v[12:15],a[108:111],v[224:227],v[12:15]
rd a[80:83],v248 offset:4096
wt lgkmcnt(5)
mm v[16:19],a[112:115],v[224:227],v[16:19]
ex v213,v213
mm v[48:51],a[112:115],v[228:231],v[48:51]
rd a[84:87],v248 offset:5120
cv v237,v218,v219
mm v[52:55],a[116:119],v[228:231],v[52:55]
ex v214,v214
mm v[20:23],a[116:119],v[224:227],v[20:23]
rd a[88:91],v248 offset:6144
cv v234,v212,v213
mm v[24:27],a[120:123],v[224:227],v[24:27]
ex v215,v215
mm v[56:59],a[120:123],v[228:231],v[56:59]
rd a[92:95],v248 offset:7168
cv v235,v214,v215
mm v[60:63],a[124:127],v[228:231],v[60:63]
ex v220,v220
mm v[28:31],a[124:127],v[224:227],v[28:31]
wt lgkmcnt(4)
mm v[192:195],a[64:67],a[0:3],0
ex v221,v221
mm v[192:195],a[68:71],a[4:7],v[192:195]
cv v238,v220,v221
s_add_u32 s89, s89, 16384
mm v[192:195],a[72:75],a[8:11],v[192:195]
ex v222,v222
mm v[192:195],a[76:79],a[12:15],v[192:195]
ld v240,s[56:59],s17 offen lds
mm v[200:203],a[76:79],a[28:31],0
ex v223,v223
s_add_u32 s17, s17, 32768
mm v[200:203],a[72:75],a[24:27],v[200:203]
cv v239,v222,v223
s_add_u32 m0, m0, 1024
mm v[200:203],a[68:71],a[20:23],v[200:203]
ex v192,v192
mm v[200:203],a[64:67],a[16:19],v[200:203]
wt lgkmcnt(0)
s_and_b32 s89, s89, 0x1ffff
mm v[196:199],a[80:83],a[0:3],0
ex v193,v193
mm v[196:199],a[84:87],a[4:7],v[196:199]
cv v224,v192,v193
mm v[196:199],a[88:91],a[8:11],v[196:199]
ex v194,v194
mm v[196:199],a[92:95],a[12:15],v[196:199]
ld v240,s[56:59],s17 offen lds
mm v[204:207],a[92:95],a[28:31],0
ex v195,v195
s_add_u32 s17, s17, 4294942720
mm v[204:207],a[88:91],a[24:27],v[204:207]
cv v225,v194,v195
s_add_u32 m0, m0, 1152
mm v[204:207],a[84:87],a[20:23],v[204:207]
ex v200,v200
mm v[204:207],a[80:83],a[16:19],v[204:207]
ld v240,s[56:59],s17 offen lds
mm v[208:211],a[64:67],a[32:35],0
ex v201,v201
s_add_u32 s17, s17, 32768
mm v[208:211],a[68:71],a[36:39],v[208:211]
cv v228,v200,v201
s_add_u32 m0, m0, 1024
mm v[208:211],a[72:75],a[40:43],v[208:211]
ex v202,v202
mm v[208:211],a[76:79],a[44:47],v[208:211]
ld v240,s[56:59],s17 offen lds
mm v[216:219],a[76:79],a[60:63],0
ex v203,v203
mm v[216:219],a[72:75],a[56:59],v[216:219]
cv v229,v202,v203
s_mov_b32 s93, 86016
mm v[216:219],a[68:71],a[52:55],v[216:219]
ex v196,v196
mm v[216:219],a[64:67],a[48:51],v[216:219]
ld a[176:179],v243,s[40:43],s93 offen
mm v[212:215],a[80:83],a[32:35],0
ex v197,v197
s_bfe_u32 s17, s4, 262148
mm v[212:215],a[84:87],a[36:39],v[212:215]
cv v226,v196,v197
s_mov_b32 s93, 93184
s_xor_b32 s17, s17, s72
mm v[212:215],a[88:91],a[40:43],v[212:215]
ex v198,v198
s_lshl_b32 s17, s17, 18
mm v[212:215],a[92:95],a[44:47],v[212:215]
ld a[180:183],v243,s[40:43],s93 offen
s_add_u32 m0, s83, 50688
mm v[220:223],a[92:95],a[60:63],0
ex v199,v199
mm v[220:223],a[88:91],a[56:59],v[220:223]
cv v227,v198,v199
mm v[220:223],a[84:87],a[52:55],v[220:223]
ex v204,v204
mm v[220:223],a[80:83],a[48:51],v[220:223]
wt vmcnt(16)
s_barrier
mm v[64:67],a[96:99],v[232:235],v[64:67]
ex v205,v205
mm v[96:99],a[96:99],v[236:239],v[96:99]
rd a[96:99],v248 offset:8192
cv v230,v204,v205
mm v[100:103],a[100:103],v[236:239],v[100:103]
ex v206,v206
mm v[68:71],a[100:103],v[232:235],v[68:71]
rd a[100:103],v248 offset:9216
mm v[72:75],a[104:107],v[232:235],v[72:75]
ex v207,v207
mm v[104:107],a[104:107],v[236:239],v[104:107]
rd a[104:107],v248 offset:10240
cv v231,v206,v207
s_mov_b32 s93, 100352
mm v[108:111],a[108:111],v[236:239],v[108:111]
ex v208,v208
mm v[76:79],a[108:111],v[232:235],v[76:79]
rd a[108:111],v248 offset:11264
ld a[184:187],v243,s[40:43],s93 offen
mm v[80:83],a[112:115],v[232:235],v[80:83]
ex v209,v209
mm v[112:115],a[112:115],v[236:239],v[112:115]
rd a[112:115],v248 offset:12288
mm v[116:119],a[116:119],v[236:239],v[116:119]
ex v210,v210
mm v[84:87],a[116:119],v[232:235],v[84:87]
rd a[116:119],v248 offset:13312
mm v[88:91],a[120:123],v[232:235],v[88:91]
ex v211,v211
mm v[120:123],a[120:123],v[236:239],v[120:123]
rd a[120:123],v248 offset:14336
mm v[124:127],a[124:127],v[236:239],v[124:127]
ex v216,v216
mm v[92:95],a[124:127],v[232:235],v[92:95]
rd a[124:127],v248 offset:15360
v_or_b32 v248, s89, v255
v_smfmac_f32_16x16x64_bf16 a[196:199], a[208:211], v[232:239], v241
wt lgkmcnt(4)
v_smfmac_f32_16x16x64_bf16 a[192:195], a[208:211], v[224:231], v241
rd a[64:67],v248 offset:0
mm v[0:3],a[96:99],v[224:227],v[0:3]
ex v217,v217
mm v[32:35],a[96:99],v[228:231],v[32:35]
rd a[68:71],v248 offset:1024
cv v232,v208,v209
mm v[36:39],a[100:103],v[228:231],v[36:39]
ex v218,v218
mm v[4:7],a[100:103],v[224:227],v[4:7]
rd a[72:75],v248 offset:2048
cv v233,v210,v211
mm v[8:11],a[104:107],v[224:227],v[8:11]
ex v219,v219
mm v[40:43],a[104:107],v[228:231],v[40:43]
rd a[76:79],v248 offset:3072
cv v236,v216,v217
mm v[44:47],a[108:111],v[228:231],v[44:47]
ex v212,v212
mm v[12:15],a[108:111],v[224:227],v[12:15]
rd a[80:83],v248 offset:4096
wt lgkmcnt(5)
mm v[16:19],a[112:115],v[224:227],v[16:19]
ex v213,v213
mm v[48:51],a[112:115],v[228:231],v[48:51]
rd a[84:87],v248 offset:5120
cv v237,v218,v219
mm v[52:55],a[116:119],v[228:231],v[52:55]
ex v214,v214
mm v[20:23],a[116:119],v[224:227],v[20:23]
rd a[88:91],v248 offset:6144
cv v234,v212,v213
mm v[24:27],a[120:123],v[224:227],v[24:27]
ex v215,v215
mm v[56:59],a[120:123],v[228:231],v[56:59]
rd a[92:95],v248 offset:7168
cv v235,v214,v215
mm v[60:63],a[124:127],v[228:231],v[60:63]
ex v220,v220
mm v[28:31],a[124:127],v[224:227],v[28:31]
wt lgkmcnt(4)
mm v[192:195],a[64:67],a[0:3],0
ex v221,v221
mm v[192:195],a[68:71],a[4:7],v[192:195]
cv v238,v220,v221
mm v[192:195],a[72:75],a[8:11],v[192:195]
ex v222,v222
mm v[192:195],a[76:79],a[12:15],v[192:195]
ld v240,s[56:59],s17 offen lds
mm v[200:203],a[76:79],a[28:31],0
ex v223,v223
s_add_u32 s17, s17, 32768
mm v[200:203],a[72:75],a[24:27],v[200:203]
cv v239,v222,v223
s_add_u32 m0, m0, 1024
mm v[200:203],a[68:71],a[20:23],v[200:203]
ex v192,v192
mm v[200:203],a[64:67],a[16:19],v[200:203]
wt lgkmcnt(0)
mm v[196:199],a[80:83],a[0:3],0
ex v193,v193
mm v[196:199],a[84:87],a[4:7],v[196:199]
cv v224,v192,v193
mm v[196:199],a[88:91],a[8:11],v[196:199]
ex v194,v194
mm v[196:199],a[92:95],a[12:15],v[196:199]
ld v240,s[56:59],s17 offen lds
mm v[204:207],a[92:95],a[28:31],0
ex v195,v195
s_add_u32 s17, s17, 4294942720
mm v[204:207],a[88:91],a[24:27],v[204:207]
cv v225,v194,v195
s_add_u32 m0, m0, 1152
mm v[204:207],a[84:87],a[20:23],v[204:207]
ex v200,v200
mm v[204:207],a[80:83],a[16:19],v[204:207]
ld v240,s[56:59],s17 offen lds
mm v[208:211],a[64:67],a[32:35],0
ex v201,v201
s_add_u32 s17, s17, 32768
mm v[208:211],a[68:71],a[36:39],v[208:211]
cv v228,v200,v201
s_add_u32 m0, m0, 1024
mm v[208:211],a[72:75],a[40:43],v[208:211]
ex v202,v202
mm v[208:211],a[76:79],a[44:47],v[208:211]
ld v240,s[56:59],s17 offen lds
mm v[216:219],a[76:79],a[60:63],0
ex v203,v203
mm v[216:219],a[72:75],a[56:59],v[216:219]
cv v229,v202,v203
s_mov_b32 s93, 107520
mm v[216:219],a[68:71],a[52:55],v[216:219]
ex v196,v196
mm v[216:219],a[64:67],a[48:51],v[216:219]
ld a[188:191],v243,s[40:43],s93 offen
mm v[212:215],a[80:83],a[32:35],0
ex v197,v197
mm v[212:215],a[84:87],a[36:39],v[212:215]
cv v226,v196,v197
mm v[212:215],a[88:91],a[40:43],v[212:215]
ex v198,v198
mm v[212:215],a[92:95],a[44:47],v[212:215]
mm v[220:223],a[92:95],a[60:63],0
ex v199,v199
mm v[220:223],a[88:91],a[56:59],v[220:223]
cv v227,v198,v199
mm v[220:223],a[84:87],a[52:55],v[220:223]
ex v204,v204
mm v[220:223],a[80:83],a[48:51],v[220:223]
mm v[64:67],a[96:99],v[232:235],v[64:67]
ex v205,v205
mm v[96:99],a[96:99],v[236:239],v[96:99]
rd a[96:99],v248 offset:8192
cv v230,v204,v205
mm v[100:103],a[100:103],v[236:239],v[100:103]
ex v206,v206
mm v[68:71],a[100:103],v[232:235],v[68:71]
rd a[100:103],v248 offset:9216
mm v[72:75],a[104:107],v[232:235],v[72:75]
ex v207,v207
mm v[104:107],a[104:107],v[236:239],v[104:107]
rd a[104:107],v248 offset:10240
cv v231,v206,v207
mm v[108:111],a[108:111],v[236:239],v[108:111]
ex v208,v208
mm v[76:79],a[108:111],v[232:235],v[76:79]
rd a[108:111],v248 offset:11264
mm v[80:83],a[112:115],v[232:235],v[80:83]
ex v209,v209
mm v[112:115],a[112:115],v[236:239],v[112:115]
rd a[112:115],v248 offset:12288
mm v[116:119],a[116:119],v[236:239],v[116:119]
ex v210,v210
mm v[84:87],a[116:119],v[232:235],v[84:87]
rd a[116:119],v248 offset:13312
mm v[88:91],a[120:123],v[232:235],v[88:91]
ex v211,v211
mm v[120:123],a[120:123],v[236:239],v[120:123]
rd a[120:123],v248 offset:14336
mm v[124:127],a[124:127],v[236:239],v[124:127]
ex v216,v216
mm v[92:95],a[124:127],v[232:235],v[92:95]
rd a[124:127],v248 offset:15360
v_smfmac_f32_16x16x64_bf16 a[196:199], a[208:211], v[232:239], v241
wt lgkmcnt(4)
v_smfmac_f32_16x16x64_bf16 a[192:195], a[208:211], v[224:231], v241
mm v[0:3],a[96:99],v[224:227],v[0:3]
ex v217,v217
mm v[32:35],a[96:99],v[228:231],v[32:35]
cv v232,v208,v209
mm v[36:39],a[100:103],v[228:231],v[36:39]
ex v218,v218
mm v[4:7],a[100:103],v[224:227],v[4:7]
cv v233,v210,v211
mm v[8:11],a[104:107],v[224:227],v[8:11]
ex v219,v219
mm v[40:43],a[104:107],v[228:231],v[40:43]
cv v236,v216,v217
mm v[44:47],a[108:111],v[228:231],v[44:47]
ex v212,v212
mm v[12:15],a[108:111],v[224:227],v[12:15]
wt lgkmcnt(0)
mm v[16:19],a[112:115],v[224:227],v[16:19]
ex v213,v213
mm v[48:51],a[112:115],v[228:231],v[48:51]
cv v237,v218,v219
mm v[52:55],a[116:119],v[228:231],v[52:55]
ex v214,v214
mm v[20:23],a[116:119],v[224:227],v[20:23]
cv v234,v212,v213
mm v[24:27],a[120:123],v[224:227],v[24:27]
ex v215,v215
mm v[56:59],a[120:123],v[228:231],v[56:59]
cv v235,v214,v215
mm v[60:63],a[124:127],v[228:231],v[60:63]
ex v220,v220
mm v[28:31],a[124:127],v[224:227],v[28:31]
; ---- helper: partial O and l go to the partner
.La5_LA_DP:
ex v221,v221
ex v222,v222
ex v223,v223
cv v238,v220,v221
s_nop 0
cv v239,v222,v223
s_nop 1
v_smfmac_f32_16x16x64_bf16 a[196:199], a[208:211], v[232:239], v241
cv v0,v0,v1
cv v1,v2,v3
cv v2,v4,v5
cv v3,v6,v7
st v[0:3],v255,s[68:71],0 offen offset:0 sc1
mm v[64:67],a[96:99],v[232:235],v[64:67]
cv v8,v8,v9
cv v9,v10,v11
cv v10,v12,v13
cv v11,v14,v15
st v[8:11],v255,s[68:71],0 offen offset:1024 sc1
mm v[96:99],a[96:99],v[236:239],v[96:99]
ar v240,a224
v_xor_b32 v241, 32, v240
cv v16,v16,v17
cv v17,v18,v19
cv v18,v20,v21
cv v19,v22,v23
mm v[100:103],a[100:103],v[236:239],v[100:103]
v_xor_b32 v242, 64, v240
v_xor_b32 v243, 96, v240
st v[16:19],v255,s[68:71],0 offen offset:2048 sc1
cv v24,v24,v25
cv v25,v26,v27
mm v[68:71],a[100:103],v[232:235],v[68:71]
cv v26,v28,v29
cv v27,v30,v31
st v[24:27],v255,s[68:71],0 offen offset:3072 sc1
cv v32,v32,v33
cv v33,v34,v35
mm v[72:75],a[104:107],v[232:235],v[72:75]
cv v34,v36,v37
cv v35,v38,v39
st v[32:35],v255,s[68:71],s95 offen offset:0 sc1
cv v40,v40,v41
cv v41,v42,v43
mm v[104:107],a[104:107],v[236:239],v[104:107]
wt vmcnt(13)
s_barrier
cv v42,v44,v45
cv v43,v46,v47
st v[40:43],v255,s[68:71],s95 offen offset:1024 sc1
mm v[108:111],a[108:111],v[236:239],v[108:111]
rt v[192:193],v240 offset:16896
rt v[194:195],v240 offset:17920
cv v48,v48,v49
cv v49,v50,v51
cv v50,v52,v53
cv v51,v54,v55
mm v[76:79],a[108:111],v[232:235],v[76:79]
rt v[196:197],v240 offset:25344
rt v[198:199],v240 offset:26368
st v[48:51],v255,s[68:71],s95 offen offset:2048 sc1
cv v56,v56,v57
cv v57,v58,v59
mm v[80:83],a[112:115],v[232:235],v[80:83]
rt v[200:201],v241 offset:16896
rt v[202:203],v241 offset:17920
cv v58,v60,v61
cv v59,v62,v63
st v[56:59],v255,s[68:71],s95 offen offset:3072 sc1
mm v[112:115],a[112:115],v[236:239],v[112:115]
rt v[204:205],v241 offset:25344
rt v[206:207],v241 offset:26368
mm v[116:119],a[116:119],v[236:239],v[116:119]
rt v[208:209],v242 offset:16896
rt v[210:211],v242 offset:17920
mm v[84:87],a[116:119],v[232:235],v[84:87]
rt v[212:213],v242 offset:25344
rt v[214:215],v242 offset:26368
mm v[88:91],a[120:123],v[232:235],v[88:91]
rt v[216:217],v243 offset:16896
rt v[218:219],v243 offset:17920
mm v[120:123],a[120:123],v[236:239],v[120:123]
rt v[220:221],v243 offset:25344
rt v[222:223],v243 offset:26368
mm v[124:127],a[124:127],v[236:239],v[124:127]
mm v[92:95],a[124:127],v[232:235],v[92:95]
wt lgkmcnt(12)
mm v[0:3],v[192:195],v[128:131],0
mm v[0:3],v[196:199],v[132:135],v[0:3]
mm v[32:35],v[196:199],v[148:151],0
wt lgkmcnt(8)
ar v224,a192
ar v228,a193
ar v225,a194
ar v229,a195
ar v226,a196
mm v[32:35],v[192:195],v[144:147],v[32:35]
rt v[196:197],v240 offset:17024
rt v[198:199],v240 offset:18048
ar v230,a197
ar v227,a198
ar v231,a199
v_add_f32 v224, v224, v228
mm v[36:39],v[200:203],v[144:147],0
v_add_f32 v225, v225, v229
v_add_f32 v226, v226, v230
v_add_f32 v227, v227, v231
s_mov_b32 s92, 16384
st v[224:227],v255,s[68:71],s92 offen sc1
mm v[36:39],v[204:207],v[148:151],v[36:39]
rt v[192:193],v240 offset:25472
rt v[194:195],v240 offset:26496
cv v64,v64,v65
cv v65,v66,v67
cv v66,v68,v69
cv v67,v70,v71
mm v[4:7],v[204:207],v[132:135],0
wt lgkmcnt(4)
st v[64:67],v255,s[68:71],s96 offen offset:0 sc1
cv v72,v72,v73
cv v73,v74,v75
cv v74,v76,v77
mm v[4:7],v[200:203],v[128:131],v[4:7]
rt v[204:205],v241 offset:17024
rt v[206:207],v241 offset:18048
cv v75,v78,v79
st v[72:75],v255,s[68:71],s96 offen offset:1024 sc1
cv v80,v80,v81
mm v[8:11],v[208:211],v[128:131],0
cv v81,v82,v83
cv v82,v84,v85
cv v83,v86,v87
st v[80:83],v255,s[68:71],s96 offen offset:2048 sc1
cv v88,v88,v89
mm v[8:11],v[212:215],v[132:135],v[8:11]
rt v[200:201],v241 offset:25472
rt v[202:203],v241 offset:26496
cv v89,v90,v91
cv v90,v92,v93
cv v91,v94,v95
st v[88:91],v255,s[68:71],s96 offen offset:3072 sc1
mm v[40:43],v[212:215],v[148:151],0
cv v96,v96,v97
cv v97,v98,v99
cv v98,v100,v101
cv v99,v102,v103
st v[96:99],v255,s[68:71],s97 offen offset:0 sc1
mm v[40:43],v[208:211],v[144:147],v[40:43]
rt v[212:213],v242 offset:17024
rt v[214:215],v242 offset:18048
cv v104,v104,v105
cv v105,v106,v107
cv v106,v108,v109
cv v107,v110,v111
mm v[44:47],v[216:219],v[144:147],0
st v[104:107],v255,s[68:71],s97 offen offset:1024 sc1
cv v112,v112,v113
cv v113,v114,v115
cv v114,v116,v117
cv v115,v118,v119
mm v[44:47],v[220:223],v[148:151],v[44:47]
rt v[208:209],v242 offset:25472
rt v[210:211],v242 offset:26496
st v[112:115],v255,s[68:71],s97 offen offset:2048 sc1
cv v120,v120,v121
cv v121,v122,v123
mm v[12:15],v[220:223],v[132:135],0
wt lgkmcnt(4)
cv v122,v124,v125
cv v123,v126,v127
st v[120:123],v255,s[68:71],s97 offen offset:3072 sc1
v_mov_b32 v249, 0
mm v[12:15],v[216:219],v[128:131],v[12:15]
rt v[220:221],v243 offset:17024
rt v[222:223],v243 offset:18048
s_add_u32 s6, s88, 393216
mm v[16:19],v[196:199],v[128:131],0
ar v224,a228
v_add_u32 v225, 0x8000, v224
v_add_u32 v226, 0x2000, v224
v_add_u32 v227, 0xa000, v224
mm v[16:19],v[192:195],v[132:135],v[16:19]
rt v[216:217],v243 offset:25472
rt v[218:219],v243 offset:26496
ar v248,a225
ar v228,a229
mm v[48:51],v[192:195],v[148:151],0
ar v229,a230
mm v[48:51],v[196:199],v[144:147],v[48:51]
rt v[192:193],v240 offset:33792
rt v[194:195],v240 offset:34816
mm v[52:55],v[204:207],v[144:147],0
.La5_LW6_6:
s_cbranch_execz .La5_LW6_R0
mm v[52:55],v[200:203],v[148:151],v[52:55]
rt v[196:197],v240 offset:42240
rt v[198:199],v240 offset:43264
mm v[20:23],v[200:203],v[132:135],0
wt lgkmcnt(4)
mm v[20:23],v[204:207],v[128:131],v[20:23]
rt v[200:201],v241 offset:33792
rt v[202:203],v241 offset:34816
mm v[24:27],v[212:215],v[128:131],0
s_bitcmp1_b32 2863311530, s2
s_cbranch_scc1 .La5_LO_VC2
s_getpc_b64 s[24:25]
.La5_LW3_P0:
s_add_u32 s24, s24, .La5_LW3_R0-.La5_LW3_P0
s_addc_u32 s25, s25, 0
s_mov_b64 exec, 0
s_cmp_eq_u32 s3, 0
s_cbranch_scc1 .La5_LW3_0
s_cmp_eq_u32 s3, 1
s_cbranch_scc1 .La5_LW3_1
s_cmp_eq_u32 s3, 2
s_cbranch_scc1 .La5_LW3_2
s_branch .La5_LW3_3
.La5_LW3_R0:
s_mov_b64 exec, -1
.La5_LO_VE2:
wt vmcnt(0)
buffer_store_dwordx2 v[252:253], v249, s[64:67], s6 offen offset:0 sc1
mm v[24:27],v[208:211],v[132:135],v[24:27]
rt v[204:205],v241 offset:42240
rt v[206:207],v241 offset:43264
mm v[56:59],v[208:211],v[148:151],0
s_mov_b32 s93, 3670016
ld a[192:195],v228,s[40:43],s93 offen
mm v[56:59],v[212:215],v[144:147],v[56:59]
rt v[208:209],v242 offset:33792
rt v[210:211],v242 offset:34816
mm v[60:63],v[220:223],v[144:147],0
s_mov_b32 s93, 3677184
ld a[196:199],v228,s[40:43],s93 offen
mm v[60:63],v[216:219],v[148:151],v[60:63]
rt v[212:213],v242 offset:42240
rt v[214:215],v242 offset:43264
mm v[28:31],v[216:219],v[132:135],0
wt lgkmcnt(4)
s_mov_b32 s93, 3684352
ld a[200:203],v228,s[40:43],s93 offen
mm v[28:31],v[220:223],v[128:131],v[28:31]
rt v[216:217],v243 offset:33792
rt v[218:219],v243 offset:34816
mm v[0:3],v[192:195],v[136:139],v[0:3]
s_mov_b32 s93, 3691520
ld a[204:207],v228,s[40:43],s93 offen
mm v[0:3],v[196:199],v[140:143],v[0:3]
rt v[220:221],v243 offset:42240
rt v[222:223],v243 offset:43264
mm v[32:35],v[196:199],v[156:159],v[32:35]
s_mov_b32 s93, 3698688
ld a[208:211],v228,s[40:43],s93 offen
mm v[32:35],v[192:195],v[152:155],v[32:35]
rt v[196:197],v240 offset:33920
rt v[198:199],v240 offset:34944
mm v[36:39],v[200:203],v[152:155],v[36:39]
s_mov_b32 s93, 3705856
ld a[212:215],v228,s[40:43],s93 offen
mm v[36:39],v[204:207],v[156:159],v[36:39]
rt v[192:193],v240 offset:42368
rt v[194:195],v240 offset:43392
mm v[4:7],v[204:207],v[140:143],v[4:7]
wt lgkmcnt(4)
s_mov_b32 s93, 3713024
ld a[216:219],v228,s[40:43],s93 offen
mm v[4:7],v[200:203],v[136:139],v[4:7]
rt v[204:205],v241 offset:33920
rt v[206:207],v241 offset:34944
mm v[8:11],v[208:211],v[136:139],v[8:11]
s_mov_b32 s93, 3720192
ld a[220:223],v228,s[40:43],s93 offen
mm v[8:11],v[212:215],v[140:143],v[8:11]
rt v[200:201],v241 offset:42368
rt v[202:203],v241 offset:43392
mm v[40:43],v[212:215],v[156:159],v[40:43]
s_mov_b32 s93, 3727360
ld a[224:227],v228,s[40:43],s93 offen
mm v[40:43],v[208:211],v[152:155],v[40:43]
rt v[212:213],v242 offset:33920
rt v[214:215],v242 offset:34944
mm v[44:47],v[216:219],v[152:155],v[44:47]
s_mov_b32 s93, 3734528
ld a[228:231],v228,s[40:43],s93 offen
mm v[44:47],v[220:223],v[156:159],v[44:47]
rt v[208:209],v242 offset:42368
rt v[210:211],v242 offset:43392
mm v[12:15],v[220:223],v[140:143],v[12:15]
wt lgkmcnt(4)
s_mov_b32 s93, 3741696
ld a[232:235],v228,s[40:43],s93 offen
mm v[12:15],v[216:219],v[136:139],v[12:15]
rt v[220:221],v243 offset:33920
rt v[222:223],v243 offset:34944
mm v[16:19],v[196:199],v[136:139],v[16:19]
s_mov_b32 s93, 3748864
ld a[236:239],v228,s[40:43],s93 offen
mm v[16:19],v[192:195],v[140:143],v[16:19]
rt v[216:217],v243 offset:42368
rt v[218:219],v243 offset:43392
mm v[48:51],v[192:195],v[156:159],v[48:51]
s_mov_b32 s93, 3756032
ld a[240:243],v228,s[40:43],s93 offen
mm v[48:51],v[196:199],v[152:155],v[48:51]
rt v[192:193],v240 offset:16896
rt v[194:195],v240 offset:17920
mm v[52:55],v[204:207],v[152:155],v[52:55]
s_mov_b32 s93, 3763200
ld a[244:247],v228,s[40:43],s93 offen
mm v[52:55],v[200:203],v[156:159],v[52:55]
rt v[196:197],v240 offset:25344
rt v[198:199],v240 offset:26368
mm v[20:23],v[200:203],v[140:143],v[20:23]
wt lgkmcnt(4)
s_mov_b32 s93, 3770368
ld a[248:251],v228,s[40:43],s93 offen
mm v[20:23],v[204:207],v[136:139],v[20:23]
rt v[200:201],v241 offset:16896
rt v[202:203],v241 offset:17920
mm v[24:27],v[212:215],v[136:139],v[24:27]
s_mov_b32 s93, 3777536
ld a[252:255],v228,s[40:43],s93 offen
mm v[24:27],v[208:211],v[140:143],v[24:27]
rt v[204:205],v241 offset:25344
rt v[206:207],v241 offset:26368
mm v[56:59],v[208:211],v[156:159],v[56:59]
mm v[56:59],v[212:215],v[152:155],v[56:59]
rt v[208:209],v242 offset:16896
rt v[210:211],v242 offset:17920
mm v[60:63],v[220:223],v[152:155],v[60:63]
mm v[60:63],v[216:219],v[156:159],v[60:63]
rt v[212:213],v242 offset:25344
rt v[214:215],v242 offset:26368
mm v[28:31],v[216:219],v[140:143],v[28:31]
wt lgkmcnt(4)
mm v[28:31],v[220:223],v[136:139],v[28:31]
rt v[216:217],v243 offset:16896
rt v[218:219],v243 offset:17920
s_branch .La5_LO_START
; ---- owner: last window, partner-s part added, l checked, O / l leaves
.La5_LA_DC:
.La5_LW5_0:
s_cbranch_execz .La5_LW5_4
wt lgkmcnt(4)
mm v[192:195],a[64:67],v[128:131],0
ex v221,v221
mm v[192:195],a[68:71],v[132:135],v[192:195]
cv v238,v220,v221
s_add_u32 s89, s89, 16384
mm v[192:195],a[72:75],v[136:139],v[192:195]
ex v222,v222
mm v[192:195],a[76:79],v[140:143],v[192:195]
ld v255,s[60:63],s17 offen offset:0 sc1 lds
mm v[200:203],a[76:79],v[156:159],0
ex v223,v223
mm v[200:203],a[72:75],v[152:155],v[200:203]
cv v239,v222,v223
s_and_b32 s89, s89, 0x1ffff
mm v[200:203],a[68:71],v[148:151],v[200:203]
ex v192,v192
mm v[200:203],a[64:67],v[144:147],v[200:203]
wt lgkmcnt(0)
mm v[196:199],a[80:83],v[128:131],0
ex v193,v193
mm v[196:199],a[84:87],v[132:135],v[196:199]
cv v224,v192,v193
ar v243,a229
mm v[196:199],a[88:91],v[136:139],v[196:199]
ex v194,v194
mm v[196:199],a[92:95],v[140:143],v[196:199]
ld v255,s[60:63],s17 offen offset:1024 sc1 lds
mm v[204:207],a[92:95],v[156:159],0
ex v195,v195
mm v[204:207],a[88:91],v[152:155],v[204:207]
cv v225,v194,v195
mm v[204:207],a[84:87],v[148:151],v[204:207]
ex v200,v200
mm v[204:207],a[80:83],v[144:147],v[204:207]
ld v255,s[60:63],s17 offen offset:2048 sc1 lds
mm v[208:211],a[64:67],v[160:163],0
ex v201,v201
mm v[208:211],a[68:71],v[164:167],v[208:211]
cv v228,v200,v201
mm v[208:211],a[72:75],v[168:171],v[208:211]
ex v202,v202
mm v[208:211],a[76:79],v[172:175],v[208:211]
ld v255,s[60:63],s17 offen offset:3072 sc1 lds
mm v[216:219],a[76:79],v[188:191],0
ex v203,v203
mm v[216:219],a[72:75],v[184:187],v[216:219]
cv v229,v202,v203
s_mov_b32 s93, 0
mm v[216:219],a[68:71],v[180:183],v[216:219]
ex v196,v196
mm v[216:219],a[64:67],v[176:179],v[216:219]
ld a[128:131],v243,s[40:43],s93 offen
mm v[212:215],a[80:83],v[160:163],0
ex v197,v197
s_add_u32 s17, s17, 8192
mm v[212:215],a[84:87],v[164:167],v[212:215]
cv v226,v196,v197
s_mov_b32 s93, 7168
s_add_u32 m0, m0, 16384
mm v[212:215],a[88:91],v[168:171],v[212:215]
ex v198,v198
s_and_b32 m0, m0, s78
mm v[212:215],a[92:95],v[172:175],v[212:215]
ld a[132:135],v243,s[40:43],s93 offen
mm v[220:223],a[92:95],v[188:191],0
ex v199,v199
mm v[220:223],a[88:91],v[184:187],v[220:223]
cv v227,v198,v199
mm v[220:223],a[84:87],v[180:183],v[220:223]
ex v204,v204
mm v[220:223],a[80:83],v[176:179],v[220:223]
wt vmcnt(12)
s_barrier
mm v[64:67],a[96:99],v[232:235],v[64:67]
ex v205,v205
mm v[96:99],a[96:99],v[236:239],v[96:99]
rd a[96:99],v248 offset:8192
cv v230,v204,v205
s_mov_b32 s93, 28672
mm v[100:103],a[100:103],v[236:239],v[100:103]
ex v206,v206
mm v[68:71],a[100:103],v[232:235],v[68:71]
rd a[100:103],v248 offset:9216
ld a[144:147],v243,s[40:43],s93 offen
mm v[72:75],a[104:107],v[232:235],v[72:75]
ex v207,v207
mm v[104:107],a[104:107],v[236:239],v[104:107]
rd a[104:107],v248 offset:10240
cv v231,v206,v207
s_mov_b32 s93, 14336
mm v[108:111],a[108:111],v[236:239],v[108:111]
ex v208,v208
mm v[76:79],a[108:111],v[232:235],v[76:79]
rd a[108:111],v248 offset:11264
ld a[136:139],v243,s[40:43],s93 offen
mm v[80:83],a[112:115],v[232:235],v[80:83]
ex v209,v209
mm v[112:115],a[112:115],v[236:239],v[112:115]
rd a[112:115],v248 offset:12288
s_mov_b32 s93, 21504
mm v[116:119],a[116:119],v[236:239],v[116:119]
ex v210,v210
mm v[84:87],a[116:119],v[232:235],v[84:87]
rd a[116:119],v248 offset:13312
ld a[140:143],v243,s[40:43],s93 offen
mm v[88:91],a[120:123],v[232:235],v[88:91]
ex v211,v211
mm v[120:123],a[120:123],v[236:239],v[120:123]
rd a[120:123],v248 offset:14336
s_mov_b32 s93, 35840
mm v[124:127],a[124:127],v[236:239],v[124:127]
ex v216,v216
mm v[92:95],a[124:127],v[232:235],v[92:95]
rd a[124:127],v248 offset:15360
ld a[148:151],v243,s[40:43],s93 offen
v_or_b32 v248, s89, v255
v_smfmac_f32_16x16x64_bf16 a[196:199], a[208:211], v[232:239], v241
wt lgkmcnt(4)
v_smfmac_f32_16x16x64_bf16 a[192:195], a[208:211], v[224:231], v241
rd a[64:67],v248 offset:0
mm v[0:3],a[96:99],v[224:227],v[0:3]
ex v217,v217
mm v[32:35],a[96:99],v[228:231],v[32:35]
rd a[68:71],v248 offset:1024
cv v232,v208,v209
mm v[36:39],a[100:103],v[228:231],v[36:39]
ex v218,v218
mm v[4:7],a[100:103],v[224:227],v[4:7]
rd a[72:75],v248 offset:2048
cv v233,v210,v211
mm v[8:11],a[104:107],v[224:227],v[8:11]
ex v219,v219
mm v[40:43],a[104:107],v[228:231],v[40:43]
rd a[76:79],v248 offset:3072
cv v236,v216,v217
mm v[44:47],a[108:111],v[228:231],v[44:47]
ex v212,v212
mm v[12:15],a[108:111],v[224:227],v[12:15]
rd a[80:83],v248 offset:4096
wt lgkmcnt(5)
mm v[16:19],a[112:115],v[224:227],v[16:19]
ex v213,v213
mm v[48:51],a[112:115],v[228:231],v[48:51]
rd a[84:87],v248 offset:5120
cv v237,v218,v219
mm v[52:55],a[116:119],v[228:231],v[52:55]
ex v214,v214
mm v[20:23],a[116:119],v[224:227],v[20:23]
rd a[88:91],v248 offset:6144
cv v234,v212,v213
mm v[24:27],a[120:123],v[224:227],v[24:27]
ex v215,v215
mm v[56:59],a[120:123],v[228:231],v[56:59]
rd a[92:95],v248 offset:7168
cv v235,v214,v215
mm v[60:63],a[124:127],v[228:231],v[60:63]
ex v220,v220
mm v[28:31],a[124:127],v[224:227],v[28:31]
wt lgkmcnt(4)
mm v[192:195],a[64:67],v[128:131],0
ex v221,v221
mm v[192:195],a[68:71],v[132:135],v[192:195]
cv v238,v220,v221
s_add_u32 s89, s89, 16384
mm v[192:195],a[72:75],v[136:139],v[192:195]
ex v222,v222
mm v[192:195],a[76:79],v[140:143],v[192:195]
ld v255,s[60:63],s17 offen offset:0 sc1 lds
mm v[200:203],a[76:79],v[156:159],0
ex v223,v223
mm v[200:203],a[72:75],v[152:155],v[200:203]
cv v239,v222,v223
s_and_b32 s89, s89, 0x1ffff
mm v[200:203],a[68:71],v[148:151],v[200:203]
ex v192,v192
mm v[200:203],a[64:67],v[144:147],v[200:203]
wt lgkmcnt(0)
mm v[196:199],a[80:83],v[128:131],0
ex v193,v193
mm v[196:199],a[84:87],v[132:135],v[196:199]
cv v224,v192,v193
mm v[196:199],a[88:91],v[136:139],v[196:199]
ex v194,v194
mm v[196:199],a[92:95],v[140:143],v[196:199]
ld v255,s[60:63],s17 offen offset:1024 sc1 lds
mm v[204:207],a[92:95],v[156:159],0
ex v195,v195
mm v[204:207],a[88:91],v[152:155],v[204:207]
cv v225,v194,v195
mm v[204:207],a[84:87],v[148:151],v[204:207]
ex v200,v200
mm v[204:207],a[80:83],v[144:147],v[204:207]
ld v255,s[60:63],s17 offen offset:2048 sc1 lds
mm v[208:211],a[64:67],v[160:163],0
ex v201,v201
mm v[208:211],a[68:71],v[164:167],v[208:211]
cv v228,v200,v201
mm v[208:211],a[72:75],v[168:171],v[208:211]
ex v202,v202
mm v[208:211],a[76:79],v[172:175],v[208:211]
ld v255,s[60:63],s17 offen offset:3072 sc1 lds
mm v[216:219],a[76:79],v[188:191],0
ex v203,v203
mm v[216:219],a[72:75],v[184:187],v[216:219]
cv v229,v202,v203
s_mov_b32 s93, 43008
mm v[216:219],a[68:71],v[180:183],v[216:219]
ex v196,v196
mm v[216:219],a[64:67],v[176:179],v[216:219]
ld a[152:155],v243,s[40:43],s93 offen
mm v[212:215],a[80:83],v[160:163],0
ex v197,v197
s_add_u32 s17, s17, 8192
mm v[212:215],a[84:87],v[164:167],v[212:215]
cv v226,v196,v197
s_mov_b32 s93, 50176
s_add_u32 m0, m0, 16384
mm v[212:215],a[88:91],v[168:171],v[212:215]
ex v198,v198
s_and_b32 m0, m0, s78
mm v[212:215],a[92:95],v[172:175],v[212:215]
ld a[156:159],v243,s[40:43],s93 offen
mm v[220:223],a[92:95],v[188:191],0
ex v199,v199
mm v[220:223],a[88:91],v[184:187],v[220:223]
cv v227,v198,v199
mm v[220:223],a[84:87],v[180:183],v[220:223]
ex v204,v204
mm v[220:223],a[80:83],v[176:179],v[220:223]
wt vmcnt(17)
s_barrier
mm v[64:67],a[96:99],v[232:235],v[64:67]
ex v205,v205
mm v[96:99],a[96:99],v[236:239],v[96:99]
rd a[96:99],v248 offset:8192
cv v230,v204,v205
s_mov_b32 s93, 71680
mm v[100:103],a[100:103],v[236:239],v[100:103]
ex v206,v206
mm v[68:71],a[100:103],v[232:235],v[68:71]
rd a[100:103],v248 offset:9216
ld a[168:171],v243,s[40:43],s93 offen
mm v[72:75],a[104:107],v[232:235],v[72:75]
ex v207,v207
mm v[104:107],a[104:107],v[236:239],v[104:107]
rd a[104:107],v248 offset:10240
cv v231,v206,v207
s_mov_b32 s93, 57344
mm v[108:111],a[108:111],v[236:239],v[108:111]
ex v208,v208
mm v[76:79],a[108:111],v[232:235],v[76:79]
rd a[108:111],v248 offset:11264
ld a[160:163],v243,s[40:43],s93 offen
mm v[80:83],a[112:115],v[232:235],v[80:83]
ex v209,v209
mm v[112:115],a[112:115],v[236:239],v[112:115]
rd a[112:115],v248 offset:12288
s_mov_b32 s93, 64512
mm v[116:119],a[116:119],v[236:239],v[116:119]
ex v210,v210
mm v[84:87],a[116:119],v[232:235],v[84:87]
rd a[116:119],v248 offset:13312
ld a[164:167],v243,s[40:43],s93 offen
mm v[88:91],a[120:123],v[232:235],v[88:91]
ex v211,v211
mm v[120:123],a[120:123],v[236:239],v[120:123]
rd a[120:123],v248 offset:14336
mm v[124:127],a[124:127],v[236:239],v[124:127]
ex v216,v216
mm v[92:95],a[124:127],v[232:235],v[92:95]
rd a[124:127],v248 offset:15360
v_or_b32 v248, s89, v255
v_smfmac_f32_16x16x64_bf16 a[196:199], a[208:211], v[232:239], v241
wt lgkmcnt(4)
v_smfmac_f32_16x16x64_bf16 a[192:195], a[208:211], v[224:231], v241
rd a[64:67],v248 offset:0
mm v[0:3],a[96:99],v[224:227],v[0:3]
ex v217,v217
mm v[32:35],a[96:99],v[228:231],v[32:35]
rd a[68:71],v248 offset:1024
cv v232,v208,v209
mm v[36:39],a[100:103],v[228:231],v[36:39]
ex v218,v218
mm v[4:7],a[100:103],v[224:227],v[4:7]
rd a[72:75],v248 offset:2048
cv v233,v210,v211
mm v[8:11],a[104:107],v[224:227],v[8:11]
ex v219,v219
mm v[40:43],a[104:107],v[228:231],v[40:43]
rd a[76:79],v248 offset:3072
cv v236,v216,v217
mm v[44:47],a[108:111],v[228:231],v[44:47]
ex v212,v212
mm v[12:15],a[108:111],v[224:227],v[12:15]
rd a[80:83],v248 offset:4096
wt lgkmcnt(5)
mm v[16:19],a[112:115],v[224:227],v[16:19]
ex v213,v213
mm v[48:51],a[112:115],v[228:231],v[48:51]
rd a[84:87],v248 offset:5120
cv v237,v218,v219
mm v[52:55],a[116:119],v[228:231],v[52:55]
ex v214,v214
mm v[20:23],a[116:119],v[224:227],v[20:23]
rd a[88:91],v248 offset:6144
cv v234,v212,v213
mm v[24:27],a[120:123],v[224:227],v[24:27]
ex v215,v215
mm v[56:59],a[120:123],v[228:231],v[56:59]
rd a[92:95],v248 offset:7168
cv v235,v214,v215
mm v[60:63],a[124:127],v[228:231],v[60:63]
ex v220,v220
mm v[28:31],a[124:127],v[224:227],v[28:31]
wt lgkmcnt(4)
mm v[192:195],a[64:67],v[128:131],0
ex v221,v221
mm v[192:195],a[68:71],v[132:135],v[192:195]
cv v238,v220,v221
s_add_u32 s89, s89, 16384
mm v[192:195],a[72:75],v[136:139],v[192:195]
ex v222,v222
mm v[192:195],a[76:79],v[140:143],v[192:195]
ld v255,s[60:63],s17 offen offset:0 sc1 lds
mm v[200:203],a[76:79],v[156:159],0
ex v223,v223
mm v[200:203],a[72:75],v[152:155],v[200:203]
cv v239,v222,v223
s_and_b32 s89, s89, 0x1ffff
mm v[200:203],a[68:71],v[148:151],v[200:203]
ex v192,v192
mm v[200:203],a[64:67],v[144:147],v[200:203]
wt lgkmcnt(0)
mm v[196:199],a[80:83],v[128:131],0
ex v193,v193
mm v[196:199],a[84:87],v[132:135],v[196:199]
cv v224,v192,v193
mm v[196:199],a[88:91],v[136:139],v[196:199]
ex v194,v194
mm v[196:199],a[92:95],v[140:143],v[196:199]
ld v255,s[60:63],s17 offen offset:1024 sc1 lds
mm v[204:207],a[92:95],v[156:159],0
ex v195,v195
mm v[204:207],a[88:91],v[152:155],v[204:207]
cv v225,v194,v195
mm v[204:207],a[84:87],v[148:151],v[204:207]
ex v200,v200
mm v[204:207],a[80:83],v[144:147],v[204:207]
ld v255,s[60:63],s17 offen offset:2048 sc1 lds
mm v[208:211],a[64:67],v[160:163],0
ex v201,v201
mm v[208:211],a[68:71],v[164:167],v[208:211]
cv v228,v200,v201
mm v[208:211],a[72:75],v[168:171],v[208:211]
ex v202,v202
mm v[208:211],a[76:79],v[172:175],v[208:211]
ld v255,s[60:63],s17 offen offset:3072 sc1 lds
mm v[216:219],a[76:79],v[188:191],0
ex v203,v203
mm v[216:219],a[72:75],v[184:187],v[216:219]
cv v229,v202,v203
s_mov_b32 s93, 78848
mm v[216:219],a[68:71],v[180:183],v[216:219]
ex v196,v196
mm v[216:219],a[64:67],v[176:179],v[216:219]
ld a[172:175],v243,s[40:43],s93 offen
mm v[212:215],a[80:83],v[160:163],0
ex v197,v197
s_add_u32 s17, s17, 8192
mm v[212:215],a[84:87],v[164:167],v[212:215]
cv v226,v196,v197
s_mov_b32 s93, 86016
s_add_u32 m0, m0, 16384
mm v[212:215],a[88:91],v[168:171],v[212:215]
ex v198,v198
s_and_b32 m0, m0, s78
mm v[212:215],a[92:95],v[172:175],v[212:215]
ld a[176:179],v243,s[40:43],s93 offen
mm v[220:223],a[92:95],v[188:191],0
ex v199,v199
mm v[220:223],a[88:91],v[184:187],v[220:223]
cv v227,v198,v199
mm v[220:223],a[84:87],v[180:183],v[220:223]
ex v204,v204
mm v[220:223],a[80:83],v[176:179],v[220:223]
wt vmcnt(21)
s_barrier
mm v[64:67],a[96:99],v[232:235],v[64:67]
ex v205,v205
mm v[96:99],a[96:99],v[236:239],v[96:99]
rd a[96:99],v248 offset:8192
cv v230,v204,v205
s_mov_b32 s93, 107520
mm v[100:103],a[100:103],v[236:239],v[100:103]
ex v206,v206
mm v[68:71],a[100:103],v[232:235],v[68:71]
rd a[100:103],v248 offset:9216
ld a[188:191],v243,s[40:43],s93 offen
mm v[72:75],a[104:107],v[232:235],v[72:75]
ex v207,v207
mm v[104:107],a[104:107],v[236:239],v[104:107]
rd a[104:107],v248 offset:10240
cv v231,v206,v207
s_mov_b32 s93, 93184
mm v[108:111],a[108:111],v[236:239],v[108:111]
ex v208,v208
mm v[76:79],a[108:111],v[232:235],v[76:79]
rd a[108:111],v248 offset:11264
ld a[180:183],v243,s[40:43],s93 offen
mm v[80:83],a[112:115],v[232:235],v[80:83]
ex v209,v209
mm v[112:115],a[112:115],v[236:239],v[112:115]
rd a[112:115],v248 offset:12288
s_mov_b32 s93, 100352
mm v[116:119],a[116:119],v[236:239],v[116:119]
ex v210,v210
mm v[84:87],a[116:119],v[232:235],v[84:87]
rd a[116:119],v248 offset:13312
ld a[184:187],v243,s[40:43],s93 offen
mm v[88:91],a[120:123],v[232:235],v[88:91]
ex v211,v211
mm v[120:123],a[120:123],v[236:239],v[120:123]
rd a[120:123],v248 offset:14336
mm v[124:127],a[124:127],v[236:239],v[124:127]
ex v216,v216
mm v[92:95],a[124:127],v[232:235],v[92:95]
rd a[124:127],v248 offset:15360
v_or_b32 v248, s89, v255
v_smfmac_f32_16x16x64_bf16 a[196:199], a[208:211], v[232:239], v241
wt lgkmcnt(4)
v_smfmac_f32_16x16x64_bf16 a[192:195], a[208:211], v[224:231], v241
rd a[64:67],v248 offset:0
mm v[0:3],a[96:99],v[224:227],v[0:3]
ex v217,v217
mm v[32:35],a[96:99],v[228:231],v[32:35]
.La5_LW5_1:
s_cbranch_execz .La5_LW5_R0
rd a[68:71],v248 offset:1024
cv v232,v208,v209
mm v[36:39],a[100:103],v[228:231],v[36:39]
ex v218,v218
mm v[4:7],a[100:103],v[224:227],v[4:7]
rd a[72:75],v248 offset:2048
cv v233,v210,v211
mm v[8:11],a[104:107],v[224:227],v[8:11]
ex v219,v219
mm v[40:43],a[104:107],v[228:231],v[40:43]
rd a[76:79],v248 offset:3072
cv v236,v216,v217
mm v[44:47],a[108:111],v[228:231],v[44:47]
ex v212,v212
mm v[12:15],a[108:111],v[224:227],v[12:15]
rd a[80:83],v248 offset:4096
wt lgkmcnt(5)
mm v[16:19],a[112:115],v[224:227],v[16:19]
ex v213,v213
mm v[48:51],a[112:115],v[228:231],v[48:51]
rd a[84:87],v248 offset:5120
cv v237,v218,v219
mm v[52:55],a[116:119],v[228:231],v[52:55]
ex v214,v214
mm v[20:23],a[116:119],v[224:227],v[20:23]
rd a[88:91],v248 offset:6144
cv v234,v212,v213
mm v[24:27],a[120:123],v[224:227],v[24:27]
ex v215,v215
mm v[56:59],a[120:123],v[228:231],v[56:59]
rd a[92:95],v248 offset:7168
cv v235,v214,v215
mm v[60:63],a[124:127],v[228:231],v[60:63]
ex v220,v220
mm v[28:31],a[124:127],v[224:227],v[28:31]
.La5_LA_DC3:
.La5_LW5_2:
s_cbranch_execz .La5_LW5_R0
wt lgkmcnt(4)
mm v[192:195],a[64:67],v[128:131],0
ex v221,v221
mm v[192:195],a[68:71],v[132:135],v[192:195]
cv v238,v220,v221
ar v240,a228
mm v[192:195],a[72:75],v[136:139],v[192:195]
ex v222,v222
mm v[192:195],a[76:79],v[140:143],v[192:195]
s_add_u32 m0, s83, 16896
s_lshl_b32 s17, s72, 18
mm v[200:203],a[76:79],v[156:159],0
ex v223,v223
mm v[200:203],a[72:75],v[152:155],v[200:203]
cv v239,v222,v223
s_add_u32 s89, s89, 16384
mm v[200:203],a[68:71],v[148:151],v[200:203]
ex v192,v192
mm v[200:203],a[64:67],v[144:147],v[200:203]
wt lgkmcnt(0)
s_and_b32 s89, s89, 0x1ffff
mm v[196:199],a[80:83],v[128:131],0
ex v193,v193
mm v[196:199],a[84:87],v[132:135],v[196:199]
cv v224,v192,v193
mm v[196:199],a[88:91],v[136:139],v[196:199]
ex v194,v194
mm v[196:199],a[92:95],v[140:143],v[196:199]
ld v240,s[56:59],s17 offen lds
mm v[204:207],a[92:95],v[156:159],0
ex v195,v195
s_add_u32 s17, s17, 32768
mm v[204:207],a[88:91],v[152:155],v[204:207]
cv v225,v194,v195
s_add_u32 m0, m0, 1024
mm v[204:207],a[84:87],v[148:151],v[204:207]
ex v200,v200
mm v[204:207],a[80:83],v[144:147],v[204:207]
ld v240,s[56:59],s17 offen lds
mm v[208:211],a[64:67],v[160:163],0
ex v201,v201
s_add_u32 s17, s17, 4294942720
mm v[208:211],a[68:71],v[164:167],v[208:211]
cv v228,v200,v201
s_add_u32 m0, m0, 1152
mm v[208:211],a[72:75],v[168:171],v[208:211]
ex v202,v202
mm v[208:211],a[76:79],v[172:175],v[208:211]
ld v240,s[56:59],s17 offen lds
mm v[216:219],a[76:79],v[188:191],0
ex v203,v203
s_add_u32 s17, s17, 32768
mm v[216:219],a[72:75],v[184:187],v[216:219]
cv v229,v202,v203
s_add_u32 m0, m0, 1024
mm v[216:219],a[68:71],v[180:183],v[216:219]
ex v196,v196
mm v[216:219],a[64:67],v[176:179],v[216:219]
ld v240,s[56:59],s17 offen lds
mm v[212:215],a[80:83],v[160:163],0
ex v197,v197
s_add_u32 s17, s17, 90112
mm v[212:215],a[84:87],v[164:167],v[212:215]
cv v226,v196,v197
s_add_u32 m0, s83, 33792
mm v[212:215],a[88:91],v[168:171],v[212:215]
ex v198,v198
mm v[212:215],a[92:95],v[172:175],v[212:215]
mm v[220:223],a[92:95],v[188:191],0
ex v199,v199
mm v[220:223],a[88:91],v[184:187],v[220:223]
cv v227,v198,v199
mm v[220:223],a[84:87],v[180:183],v[220:223]
ex v204,v204
mm v[220:223],a[80:83],v[176:179],v[220:223]
wt vmcnt(10)
s_barrier
mm v[64:67],a[96:99],v[232:235],v[64:67]
ex v205,v205
mm v[96:99],a[96:99],v[236:239],v[96:99]
rd a[96:99],v248 offset:8192
cv v230,v204,v205
mm v[100:103],a[100:103],v[236:239],v[100:103]
ex v206,v206
mm v[68:71],a[100:103],v[232:235],v[68:71]
rd a[100:103],v248 offset:9216
mm v[72:75],a[104:107],v[232:235],v[72:75]
ex v207,v207
mm v[104:107],a[104:107],v[236:239],v[104:107]
rd a[104:107],v248 offset:10240
cv v231,v206,v207
mm v[108:111],a[108:111],v[236:239],v[108:111]
ex v208,v208
mm v[76:79],a[108:111],v[232:235],v[76:79]
rd a[108:111],v248 offset:11264
mm v[80:83],a[112:115],v[232:235],v[80:83]
ex v209,v209
mm v[112:115],a[112:115],v[236:239],v[112:115]
rd a[112:115],v248 offset:12288
mm v[116:119],a[116:119],v[236:239],v[116:119]
ex v210,v210
mm v[84:87],a[116:119],v[232:235],v[84:87]
rd a[116:119],v248 offset:13312
mm v[88:91],a[120:123],v[232:235],v[88:91]
ex v211,v211
mm v[120:123],a[120:123],v[236:239],v[120:123]
rd a[120:123],v248 offset:14336
mm v[124:127],a[124:127],v[236:239],v[124:127]
ex v216,v216
mm v[92:95],a[124:127],v[232:235],v[92:95]
rd a[124:127],v248 offset:15360
v_or_b32 v248, s89, v255
v_smfmac_f32_16x16x64_bf16 a[196:199], a[208:211], v[232:239], v241
wt lgkmcnt(4)
v_smfmac_f32_16x16x64_bf16 a[192:195], a[208:211], v[224:231], v241
rd a[64:67],v248 offset:0
mm v[0:3],a[96:99],v[224:227],v[0:3]
ex v217,v217
mm v[32:35],a[96:99],v[228:231],v[32:35]
rd a[68:71],v248 offset:1024
cv v232,v208,v209
mm v[36:39],a[100:103],v[228:231],v[36:39]
ex v218,v218
mm v[4:7],a[100:103],v[224:227],v[4:7]
rd a[72:75],v248 offset:2048
cv v233,v210,v211
mm v[8:11],a[104:107],v[224:227],v[8:11]
ex v219,v219
mm v[40:43],a[104:107],v[228:231],v[40:43]
rd a[76:79],v248 offset:3072
cv v236,v216,v217
mm v[44:47],a[108:111],v[228:231],v[44:47]
ex v212,v212
mm v[12:15],a[108:111],v[224:227],v[12:15]
rd a[80:83],v248 offset:4096
wt lgkmcnt(5)
mm v[16:19],a[112:115],v[224:227],v[16:19]
ex v213,v213
mm v[48:51],a[112:115],v[228:231],v[48:51]
rd a[84:87],v248 offset:5120
cv v237,v218,v219
mm v[52:55],a[116:119],v[228:231],v[52:55]
ex v214,v214
mm v[20:23],a[116:119],v[224:227],v[20:23]
rd a[88:91],v248 offset:6144
cv v234,v212,v213
mm v[24:27],a[120:123],v[224:227],v[24:27]
ex v215,v215
mm v[56:59],a[120:123],v[228:231],v[56:59]
rd a[92:95],v248 offset:7168
cv v235,v214,v215
mm v[60:63],a[124:127],v[228:231],v[60:63]
ex v220,v220
mm v[28:31],a[124:127],v[224:227],v[28:31]
buffer_load_dword v250, v251, s[64:67], s20 offen sc1
wt lgkmcnt(4)
mm v[192:195],a[64:67],v[128:131],0
ex v221,v221
mm v[192:195],a[68:71],v[132:135],v[192:195]
cv v238,v220,v221
s_add_u32 s89, s89, 16384
mm v[192:195],a[72:75],v[136:139],v[192:195]
ex v222,v222
mm v[192:195],a[76:79],v[140:143],v[192:195]
ld v240,s[56:59],s17 offen lds
mm v[200:203],a[76:79],v[156:159],0
ex v223,v223
s_add_u32 s17, s17, 32768
mm v[200:203],a[72:75],v[152:155],v[200:203]
cv v239,v222,v223
s_add_u32 m0, m0, 1024
mm v[200:203],a[68:71],v[148:151],v[200:203]
ex v192,v192
mm v[200:203],a[64:67],v[144:147],v[200:203]
wt lgkmcnt(0)
s_and_b32 s89, s89, 0x1ffff
mm v[196:199],a[80:83],v[128:131],0
ex v193,v193
mm v[196:199],a[84:87],v[132:135],v[196:199]
cv v224,v192,v193
mm v[196:199],a[88:91],v[136:139],v[196:199]
ex v194,v194
mm v[196:199],a[92:95],v[140:143],v[196:199]
ld v240,s[56:59],s17 offen lds
mm v[204:207],a[92:95],v[156:159],0
ex v195,v195
s_add_u32 s17, s17, 4294942720
mm v[204:207],a[88:91],v[152:155],v[204:207]
cv v225,v194,v195
s_add_u32 m0, m0, 1152
mm v[204:207],a[84:87],v[148:151],v[204:207]
ex v200,v200
mm v[204:207],a[80:83],v[144:147],v[204:207]
ld v240,s[56:59],s17 offen lds
mm v[208:211],a[64:67],v[160:163],0
ex v201,v201
s_add_u32 s17, s17, 32768
mm v[208:211],a[68:71],v[164:167],v[208:211]
cv v228,v200,v201
s_add_u32 m0, m0, 1024
mm v[208:211],a[72:75],v[168:171],v[208:211]
ex v202,v202
mm v[208:211],a[76:79],v[172:175],v[208:211]
ld v240,s[56:59],s17 offen lds
mm v[216:219],a[76:79],v[188:191],0
ex v203,v203
mm v[216:219],a[72:75],v[184:187],v[216:219]
cv v229,v202,v203
mm v[216:219],a[68:71],v[180:183],v[216:219]
ex v196,v196
mm v[216:219],a[64:67],v[176:179],v[216:219]
mm v[212:215],a[80:83],v[160:163],0
ex v197,v197
s_bfe_u32 s17, s4, 262148
mm v[212:215],a[84:87],v[164:167],v[212:215]
cv v226,v196,v197
s_xor_b32 s17, s17, s72
s_lshl_b32 s17, s17, 18
mm v[212:215],a[88:91],v[168:171],v[212:215]
ex v198,v198
s_add_u32 m0, s83, 50688
mm v[212:215],a[92:95],v[172:175],v[212:215]
mm v[220:223],a[92:95],v[188:191],0
ex v199,v199
mm v[220:223],a[88:91],v[184:187],v[220:223]
cv v227,v198,v199
wt vmcnt(4)
v_cmp_eq_u32 s[92:93], v250, v249
s_mov_b32 s94, 16384
mm v[220:223],a[84:87],v[180:183],v[220:223]
ex v204,v204
mm v[220:223],a[80:83],v[176:179],v[220:223]
s_nop 0
s_barrier
mm v[64:67],a[96:99],v[232:235],v[64:67]
ex v205,v205
mm v[96:99],a[96:99],v[236:239],v[96:99]
rd a[96:99],v248 offset:8192
cv v230,v204,v205
s_and_b32 s92, s92, 3
s_andn2_b32 s92, s92, s23
s_cmp_eq_u32 s92, 3
s_cselect_b32 s70, 17408, 0
s_cselect_b32 s84, 3, s23
mm v[100:103],a[100:103],v[236:239],v[100:103]
ex v206,v206
mm v[68:71],a[100:103],v[232:235],v[68:71]
rd a[100:103],v248 offset:9216
ld a[220:223],v255,s[68:71],s94 offen sc1
mm v[72:75],a[104:107],v[232:235],v[72:75]
ex v207,v207
mm v[104:107],a[104:107],v[236:239],v[104:107]
rd a[104:107],v248 offset:10240
cv v231,v206,v207
mm v[108:111],a[108:111],v[236:239],v[108:111]
ex v208,v208
mm v[76:79],a[108:111],v[232:235],v[76:79]
rd a[108:111],v248 offset:11264
ld a[0:3],v255,s[68:71],0 offen offset:0 sc1
mm v[80:83],a[112:115],v[232:235],v[80:83]
ex v209,v209
mm v[112:115],a[112:115],v[236:239],v[112:115]
rd a[112:115],v248 offset:12288
mm v[116:119],a[116:119],v[236:239],v[116:119]
ex v210,v210
mm v[84:87],a[116:119],v[232:235],v[84:87]
rd a[116:119],v248 offset:13312
ld a[4:7],v255,s[68:71],0 offen offset:1024 sc1
mm v[88:91],a[120:123],v[232:235],v[88:91]
ex v211,v211
mm v[120:123],a[120:123],v[236:239],v[120:123]
rd a[120:123],v248 offset:14336
mm v[124:127],a[124:127],v[236:239],v[124:127]
ex v216,v216
mm v[92:95],a[124:127],v[232:235],v[92:95]
rd a[124:127],v248 offset:15360
v_or_b32 v248, s89, v255
buffer_load_dword v250, v251, s[64:67], s20 offen sc1
v_smfmac_f32_16x16x64_bf16 a[196:199], a[208:211], v[232:239], v241
wt lgkmcnt(4)
v_smfmac_f32_16x16x64_bf16 a[192:195], a[208:211], v[224:231], v241
rd a[64:67],v248 offset:0
mm v[0:3],a[96:99],v[224:227],v[0:3]
ex v217,v217
mm v[32:35],a[96:99],v[228:231],v[32:35]
rd a[68:71],v248 offset:1024
cv v232,v208,v209
ld a[8:11],v255,s[68:71],0 offen offset:2048 sc1
mm v[36:39],a[100:103],v[228:231],v[36:39]
ex v218,v218
mm v[4:7],a[100:103],v[224:227],v[4:7]
rd a[72:75],v248 offset:2048
cv v233,v210,v211
mm v[8:11],a[104:107],v[224:227],v[8:11]
ex v219,v219
mm v[40:43],a[104:107],v[228:231],v[40:43]
rd a[76:79],v248 offset:3072
cv v236,v216,v217
ld a[12:15],v255,s[68:71],0 offen offset:3072 sc1
mm v[44:47],a[108:111],v[228:231],v[44:47]
ex v212,v212
mm v[12:15],a[108:111],v[224:227],v[12:15]
rd a[80:83],v248 offset:4096
wt lgkmcnt(5)
mm v[16:19],a[112:115],v[224:227],v[16:19]
ex v213,v213
mm v[48:51],a[112:115],v[228:231],v[48:51]
rd a[84:87],v248 offset:5120
cv v237,v218,v219
ld a[16:19],v255,s[68:71],s95 offen offset:0 sc1
mm v[52:55],a[116:119],v[228:231],v[52:55]
ex v214,v214
mm v[20:23],a[116:119],v[224:227],v[20:23]
rd a[88:91],v248 offset:6144
cv v234,v212,v213
mm v[24:27],a[120:123],v[224:227],v[24:27]
ex v215,v215
mm v[56:59],a[120:123],v[228:231],v[56:59]
rd a[92:95],v248 offset:7168
cv v235,v214,v215
ld a[20:23],v255,s[68:71],s95 offen offset:1024 sc1
mm v[60:63],a[124:127],v[228:231],v[60:63]
ex v220,v220
mm v[28:31],a[124:127],v[224:227],v[28:31]
ld a[24:27],v255,s[68:71],s95 offen offset:2048 sc1
wt lgkmcnt(4)
mm v[192:195],a[64:67],v[128:131],0
ex v221,v221
mm v[192:195],a[68:71],v[132:135],v[192:195]
cv v238,v220,v221
mm v[192:195],a[72:75],v[136:139],v[192:195]
ex v222,v222
mm v[192:195],a[76:79],v[140:143],v[192:195]
ld v240,s[56:59],s17 offen lds
mm v[200:203],a[76:79],v[156:159],0
ex v223,v223
s_add_u32 s17, s17, 32768
mm v[200:203],a[72:75],v[152:155],v[200:203]
cv v239,v222,v223
s_add_u32 m0, m0, 1024
mm v[200:203],a[68:71],v[148:151],v[200:203]
ex v192,v192
mm v[200:203],a[64:67],v[144:147],v[200:203]
wt lgkmcnt(0)
mm v[196:199],a[80:83],v[128:131],0
ex v193,v193
mm v[196:199],a[84:87],v[132:135],v[196:199]
cv v224,v192,v193
wt vmcnt(6)
v_cmp_eq_u32 s[92:93], v250, v249
mm v[196:199],a[88:91],v[136:139],v[196:199]
ex v194,v194
mm v[196:199],a[92:95],v[140:143],v[196:199]
ld v240,s[56:59],s17 offen lds
mm v[204:207],a[92:95],v[156:159],0
ex v195,v195
s_add_u32 s17, s17, 4294942720
mm v[204:207],a[88:91],v[152:155],v[204:207]
cv v225,v194,v195
s_add_u32 m0, m0, 1152
s_and_b32 s92, s92, 3
s_sub_u32 s92, s92, s84
s_cmp_eq_u32 s92, 3
s_cbranch_scc1 .La5_LA_PS0
.La5_LA_PS1:
mm v[204:207],a[84:87],v[148:151],v[204:207]
ex v200,v200
mm v[204:207],a[80:83],v[144:147],v[204:207]
ld v240,s[56:59],s17 offen lds
mm v[208:211],a[64:67],v[160:163],0
ex v201,v201
s_add_u32 s17, s17, 32768
mm v[208:211],a[68:71],v[164:167],v[208:211]
cv v228,v200,v201
s_add_u32 m0, m0, 1024
ld a[28:31],v255,s[68:71],s95 offen offset:3072 sc1
mm v[208:211],a[72:75],v[168:171],v[208:211]
ex v202,v202
mm v[208:211],a[76:79],v[172:175],v[208:211]
ld v240,s[56:59],s17 offen lds
mm v[216:219],a[76:79],v[188:191],0
ex v203,v203
mm v[216:219],a[72:75],v[184:187],v[216:219]
cv v229,v202,v203
ld a[32:35],v255,s[68:71],s96 offen offset:0 sc1
mm v[216:219],a[68:71],v[180:183],v[216:219]
ex v196,v196
mm v[216:219],a[64:67],v[176:179],v[216:219]
mm v[212:215],a[80:83],v[160:163],0
ex v197,v197
mm v[212:215],a[84:87],v[164:167],v[212:215]
cv v226,v196,v197
buffer_load_dword v250, v251, s[64:67], s20 offen sc1
mm v[212:215],a[88:91],v[168:171],v[212:215]
ex v198,v198
mm v[212:215],a[92:95],v[172:175],v[212:215]
ld a[36:39],v255,s[68:71],s96 offen offset:1024 sc1
mm v[220:223],a[92:95],v[188:191],0
ex v199,v199
mm v[220:223],a[88:91],v[184:187],v[220:223]
cv v227,v198,v199
mm v[220:223],a[84:87],v[180:183],v[220:223]
ex v204,v204
mm v[220:223],a[80:83],v[176:179],v[220:223]
ld a[40:43],v255,s[68:71],s96 offen offset:2048 sc1
mm v[64:67],a[96:99],v[232:235],v[64:67]
ex v205,v205
mm v[96:99],a[96:99],v[236:239],v[96:99]
rd a[96:99],v248 offset:8192
cv v230,v204,v205
mm v[100:103],a[100:103],v[236:239],v[100:103]
ex v206,v206
mm v[68:71],a[100:103],v[232:235],v[68:71]
rd a[100:103],v248 offset:9216
ld a[44:47],v255,s[68:71],s96 offen offset:3072 sc1
mm v[72:75],a[104:107],v[232:235],v[72:75]
ex v207,v207
mm v[104:107],a[104:107],v[236:239],v[104:107]
rd a[104:107],v248 offset:10240
cv v231,v206,v207
mm v[108:111],a[108:111],v[236:239],v[108:111]
ex v208,v208
mm v[76:79],a[108:111],v[232:235],v[76:79]
rd a[108:111],v248 offset:11264
ld a[48:51],v255,s[68:71],s97 offen offset:0 sc1
mm v[80:83],a[112:115],v[232:235],v[80:83]
ex v209,v209
mm v[112:115],a[112:115],v[236:239],v[112:115]
rd a[112:115],v248 offset:12288
mm v[116:119],a[116:119],v[236:239],v[116:119]
ex v210,v210
mm v[84:87],a[116:119],v[232:235],v[84:87]
rd a[116:119],v248 offset:13312
ld a[52:55],v255,s[68:71],s97 offen offset:1024 sc1
mm v[88:91],a[120:123],v[232:235],v[88:91]
ex v211,v211
mm v[120:123],a[120:123],v[236:239],v[120:123]
rd a[120:123],v248 offset:14336
mm v[124:127],a[124:127],v[236:239],v[124:127]
ex v216,v216
mm v[92:95],a[124:127],v[232:235],v[92:95]
rd a[124:127],v248 offset:15360
ld a[56:59],v255,s[68:71],s97 offen offset:2048 sc1
v_smfmac_f32_16x16x64_bf16 a[196:199], a[208:211], v[232:239], v241
wt lgkmcnt(4)
v_smfmac_f32_16x16x64_bf16 a[192:195], a[208:211], v[224:231], v241
ld a[60:63],v255,s[68:71],s97 offen offset:3072 sc1
mm v[0:3],a[96:99],v[224:227],v[0:3]
ex v217,v217
mm v[32:35],a[96:99],v[228:231],v[32:35]
cv v232,v208,v209
mm v[36:39],a[100:103],v[228:231],v[36:39]
ex v218,v218
mm v[4:7],a[100:103],v[224:227],v[4:7]
cv v233,v210,v211
mm v[8:11],a[104:107],v[224:227],v[8:11]
ex v219,v219
mm v[40:43],a[104:107],v[228:231],v[40:43]
cv v236,v216,v217
mm v[44:47],a[108:111],v[228:231],v[44:47]
ex v212,v212
wt vmcnt(7)
v_cmp_eq_u32 s[92:93], v250, v249
mm v[12:15],a[108:111],v[224:227],v[12:15]
wt lgkmcnt(0)
mm v[16:19],a[112:115],v[224:227],v[16:19]
ex v213,v213
mm v[48:51],a[112:115],v[228:231],v[48:51]
cv v237,v218,v219
s_and_b32 s92, s92, 3
s_sub_u32 s92, s92, s84
mm v[52:55],a[116:119],v[228:231],v[52:55]
ex v214,v214
mm v[20:23],a[116:119],v[224:227],v[20:23]
cv v234,v212,v213
mm v[24:27],a[120:123],v[224:227],v[24:27]
ex v215,v215
mm v[56:59],a[120:123],v[228:231],v[56:59]
cv v235,v214,v215
mm v[60:63],a[124:127],v[228:231],v[60:63]
ex v220,v220
mm v[28:31],a[124:127],v[224:227],v[28:31]
s_cmp_eq_u32 s92, 3
s_cbranch_scc1 .La5_LA_PS2
.La5_LA_PS3:
s_cmp_lg_u32 s84, 3
s_cbranch_scc1 .La5_LA_PSPIN
.La5_LA_DC1:
wt vmcnt(8)
mm v[0:3],a[214:217],a[0:3],v[0:3]
ar v250,a192
ar v251,a193
v_add_f32 v250, v250, v251
ar v251,a220
v_add_f32 v250, v250, v251
ar v249,a194
mm v[4:7],a[212:215],a[0:3],v[4:7]
ar v251,a195
v_add_f32 v249, v249, v251
ar v251,a221
v_add_f32 v249, v249, v251
v_subrev_u32 v251, 562036736, v250
mm v[8:11],a[214:217],a[4:7],v[8:11]
v_rcp_f32 v250, v250
v_cmp_le_u32 vcc, 1006632960, v251
v_subrev_u32 v251, 562036736, v249
s_mov_b64 s[92:93], vcc
v_cmp_le_u32 vcc, 1006632960, v251
mm v[12:15],a[212:215],a[4:7],v[12:15]
v_rcp_f32 v249, v249
mm v[16:19],a[214:217],a[8:11],v[16:19]
s_or_b64 vcc, vcc, s[92:93]
s_cbranch_scc1 .La5_LA_SC3
.La5_LA_SR3:
ex v221,v221
ex v222,v222
mm v[20:23],a[212:215],a[8:11],v[20:23]
ex v223,v223
cv v238,v220,v221
v_mul_f32 v0, v0, v250
v_mul_f32 v1, v1, v250
v_mul_f32 v2, v2, v250
mm v[24:27],a[214:217],a[12:15],v[24:27]
v_mul_f32 v3, v3, v250
v_mul_f32 v4, v4, v250
v_mul_f32 v5, v5, v250
v_mul_f32 v6, v6, v250
v_mul_f32 v7, v7, v250
cv v128,v0,v1
mm v[28:31],a[212:215],a[12:15],v[28:31]
cv v129,v2,v3
cv v130,v4,v5
cv v131,v6,v7
st v[128:131],v244,s[44:47],s91 offen offset:0 sc1
cv v239,v222,v223
mm v[32:35],a[214:217],a[16:19],v[32:35]
v_mul_f32 v8, v8, v250
v_mul_f32 v9, v9, v250
v_mul_f32 v10, v10, v250
v_mul_f32 v11, v11, v250
v_mul_f32 v12, v12, v250
v_mul_f32 v13, v13, v250
mm v[36:39],a[212:215],a[16:19],v[36:39]
v_mul_f32 v14, v14, v250
v_mul_f32 v15, v15, v250
cv v132,v8,v9
cv v133,v10,v11
cv v134,v12,v13
cv v135,v14,v15
mm v[40:43],a[214:217],a[20:23],v[40:43]
st v[132:135],v244,s[44:47],s91 offen offset:1024 sc1
v_mul_f32 v16, v16, v250
v_mul_f32 v17, v17, v250
v_mul_f32 v18, v18, v250
v_mul_f32 v19, v19, v250
mm v[44:47],a[212:215],a[20:23],v[44:47]
v_mul_f32 v20, v20, v250
v_mul_f32 v21, v21, v250
v_mul_f32 v22, v22, v250
v_mul_f32 v23, v23, v250
cv v136,v16,v17
cv v137,v18,v19
mm v[48:51],a[214:217],a[24:27],v[48:51]
cv v138,v20,v21
cv v139,v22,v23
st v[136:139],v244,s[44:47],s91 offen offset:2048 sc1
v_mul_f32 v24, v24, v250
v_mul_f32 v25, v25, v250
mm v[52:55],a[212:215],a[24:27],v[52:55]
v_mul_f32 v26, v26, v250
v_mul_f32 v27, v27, v250
.La5_LW5_3:
s_cbranch_execz .La5_LW5_R0
v_mul_f32 v28, v28, v250
v_mul_f32 v29, v29, v250
v_mul_f32 v30, v30, v250
v_mul_f32 v31, v31, v250
mm v[56:59],a[214:217],a[28:31],v[56:59]
cv v140,v24,v25
cv v141,v26,v27
cv v142,v28,v29
cv v143,v30,v31
st v[140:143],v244,s[44:47],s91 offen offset:3072 sc1
mm v[60:63],a[212:215],a[28:31],v[60:63]
v_mul_f32 v32, v32, v249
v_mul_f32 v33, v33, v249
v_mul_f32 v34, v34, v249
v_mul_f32 v35, v35, v249
v_mul_f32 v36, v36, v249
v_mul_f32 v37, v37, v249
v_smfmac_f32_16x16x64_bf16 a[196:199], a[208:211], v[232:239], v241
v_mul_f32 v38, v38, v249
v_mul_f32 v39, v39, v249
cv v144,v32,v33
cv v145,v34,v35
cv v146,v36,v37
mm v[64:67],a[96:99],v[232:235],v[64:67]
cv v147,v38,v39
st v[144:147],v245,s[44:47],s91 offen offset:0 sc1
v_mul_f32 v40, v40, v249
v_mul_f32 v41, v41, v249
v_mul_f32 v42, v42, v249
mm v[96:99],a[96:99],v[236:239],v[96:99]
v_mul_f32 v43, v43, v249
v_mul_f32 v44, v44, v249
v_mul_f32 v45, v45, v249
v_mul_f32 v46, v46, v249
v_mul_f32 v47, v47, v249
cv v148,v40,v41
mm v[100:103],a[100:103],v[236:239],v[100:103]
cv v149,v42,v43
cv v150,v44,v45
cv v151,v46,v47
st v[148:151],v245,s[44:47],s91 offen offset:1024 sc1
v_mul_f32 v48, v48, v249
mm v[68:71],a[100:103],v[232:235],v[68:71]
v_mul_f32 v49, v49, v249
v_mul_f32 v50, v50, v249
v_mul_f32 v51, v51, v249
v_mul_f32 v52, v52, v249
v_mul_f32 v53, v53, v249
v_mul_f32 v54, v54, v249
mm v[72:75],a[104:107],v[232:235],v[72:75]
v_mul_f32 v55, v55, v249
cv v152,v48,v49
cv v153,v50,v51
cv v154,v52,v53
cv v155,v54,v55
st v[152:155],v245,s[44:47],s91 offen offset:2048 sc1
mm v[104:107],a[104:107],v[236:239],v[104:107]
v_mul_f32 v56, v56, v249
v_mul_f32 v57, v57, v249
v_mul_f32 v58, v58, v249
v_mul_f32 v59, v59, v249
v_mul_f32 v60, v60, v249
v_mul_f32 v61, v61, v249
mm v[108:111],a[108:111],v[236:239],v[108:111]
v_mul_f32 v62, v62, v249
v_mul_f32 v63, v63, v249
cv v156,v56,v57
cv v157,v58,v59
cv v158,v60,v61
cv v159,v62,v63
mm v[76:79],a[108:111],v[232:235],v[76:79]
st v[156:159],v245,s[44:47],s91 offen offset:3072 sc1
ar v240,a224
v_xor_b32 v241, 32, v240
v_xor_b32 v242, 64, v240
v_xor_b32 v243, 96, v240
mm v[80:83],a[112:115],v[232:235],v[80:83]
mm v[112:115],a[112:115],v[236:239],v[112:115]
mm v[116:119],a[116:119],v[236:239],v[116:119]
mm v[84:87],a[116:119],v[232:235],v[84:87]
mm v[88:91],a[120:123],v[232:235],v[88:91]
mm v[120:123],a[120:123],v[236:239],v[120:123]
mm v[124:127],a[124:127],v[236:239],v[124:127]
mm v[92:95],a[124:127],v[232:235],v[92:95]
s_nop 0
s_barrier
wt vmcnt(8)
mm v[64:67],a[214:217],a[32:35],v[64:67]
rt v[192:193],v240 offset:16896
rt v[194:195],v240 offset:17920
mm v[68:71],a[212:215],a[32:35],v[68:71]
rt v[196:197],v240 offset:25344
rt v[198:199],v240 offset:26368
mm v[72:75],a[214:217],a[36:39],v[72:75]
rt v[200:201],v241 offset:16896
rt v[202:203],v241 offset:17920
ar v250,a196
ar v251,a197
v_add_f32 v250, v250, v251
ar v251,a222
mm v[76:79],a[212:215],a[36:39],v[76:79]
rt v[204:205],v241 offset:25344
rt v[206:207],v241 offset:26368
v_add_f32 v250, v250, v251
ar v249,a198
ar v251,a199
v_add_f32 v249, v249, v251
mm v[80:83],a[214:217],a[40:43],v[80:83]
rt v[208:209],v242 offset:16896
rt v[210:211],v242 offset:17920
ar v251,a223
v_add_f32 v249, v249, v251
v_subrev_u32 v251, 562036736, v250
mm v[84:87],a[212:215],a[40:43],v[84:87]
rt v[212:213],v242 offset:25344
rt v[214:215],v242 offset:26368
v_rcp_f32 v250, v250
v_cmp_le_u32 vcc, 1006632960, v251
v_subrev_u32 v251, 562036736, v249
mm v[88:91],a[214:217],a[44:47],v[88:91]
rt v[216:217],v243 offset:16896
rt v[218:219],v243 offset:17920
s_mov_b64 s[92:93], vcc
v_cmp_le_u32 vcc, 1006632960, v251
v_rcp_f32 v249, v249
mm v[92:95],a[212:215],a[44:47],v[92:95]
rt v[220:221],v243 offset:25344
rt v[222:223],v243 offset:26368
s_or_b64 vcc, vcc, s[92:93]
s_cbranch_scc1 .La5_LA_SC4
.La5_LA_SR4:
v_mul_f32 v64, v64, v250
v_mul_f32 v65, v65, v250
mm v[96:99],a[214:217],a[48:51],v[96:99]
v_mul_f32 v66, v66, v250
v_mul_f32 v67, v67, v250
v_mul_f32 v68, v68, v250
v_mul_f32 v69, v69, v250
v_mul_f32 v70, v70, v250
v_mul_f32 v71, v71, v250
mm v[100:103],a[212:215],a[48:51],v[100:103]
cv v160,v64,v65
cv v161,v66,v67
cv v162,v68,v69
cv v163,v70,v71
st v[160:163],v246,s[44:47],s91 offen offset:0 sc1
mm v[104:107],a[214:217],a[52:55],v[104:107]
v_mul_f32 v72, v72, v250
v_mul_f32 v73, v73, v250
v_mul_f32 v74, v74, v250
v_mul_f32 v75, v75, v250
v_mul_f32 v76, v76, v250
v_mul_f32 v77, v77, v250
mm v[108:111],a[212:215],a[52:55],v[108:111]
v_mul_f32 v78, v78, v250
v_mul_f32 v79, v79, v250
cv v164,v72,v73
cv v165,v74,v75
cv v166,v76,v77
cv v167,v78,v79
mm v[112:115],a[214:217],a[56:59],v[112:115]
st v[164:167],v246,s[44:47],s91 offen offset:1024 sc1
v_mul_f32 v80, v80, v250
v_mul_f32 v81, v81, v250
v_mul_f32 v82, v82, v250
v_mul_f32 v83, v83, v250
mm v[116:119],a[212:215],a[56:59],v[116:119]
v_mul_f32 v84, v84, v250
v_mul_f32 v85, v85, v250
v_mul_f32 v86, v86, v250
v_mul_f32 v87, v87, v250
cv v168,v80,v81
cv v169,v82,v83
mm v[120:123],a[214:217],a[60:63],v[120:123]
cv v170,v84,v85
cv v171,v86,v87
st v[168:171],v246,s[44:47],s91 offen offset:2048 sc1
v_mul_f32 v88, v88, v250
v_mul_f32 v89, v89, v250
mm v[124:127],a[212:215],a[60:63],v[124:127]
wt lgkmcnt(12)
v_mul_f32 v90, v90, v250
v_mul_f32 v91, v91, v250
v_mul_f32 v92, v92, v250
v_mul_f32 v93, v93, v250
v_mul_f32 v94, v94, v250
mm v[0:3],v[192:195],v[128:131],0
v_mul_f32 v95, v95, v250
cv v172,v88,v89
cv v173,v90,v91
cv v174,v92,v93
cv v175,v94,v95
st v[172:175],v246,s[44:47],s91 offen offset:3072 sc1
mm v[0:3],v[196:199],v[132:135],v[0:3]
v_mul_f32 v96, v96, v249
v_mul_f32 v97, v97, v249
v_mul_f32 v98, v98, v249
v_mul_f32 v99, v99, v249
v_mul_f32 v100, v100, v249
v_mul_f32 v101, v101, v249
mm v[32:35],v[196:199],v[148:151],0
wt lgkmcnt(8)
v_mul_f32 v102, v102, v249
v_mul_f32 v103, v103, v249
cv v176,v96,v97
cv v177,v98,v99
cv v178,v100,v101
mm v[32:35],v[192:195],v[144:147],v[32:35]
rt v[196:197],v240 offset:17024
rt v[198:199],v240 offset:18048
cv v179,v102,v103
st v[176:179],v247,s[44:47],s91 offen offset:0 sc1
v_mul_f32 v104, v104, v249
mm v[36:39],v[200:203],v[144:147],0
v_mul_f32 v105, v105, v249
v_mul_f32 v106, v106, v249
v_mul_f32 v107, v107, v249
v_mul_f32 v108, v108, v249
v_mul_f32 v109, v109, v249
v_mul_f32 v110, v110, v249
mm v[36:39],v[204:207],v[148:151],v[36:39]
rt v[192:193],v240 offset:25472
rt v[194:195],v240 offset:26496
v_mul_f32 v111, v111, v249
cv v180,v104,v105
cv v181,v106,v107
cv v182,v108,v109
mm v[4:7],v[204:207],v[132:135],0
wt lgkmcnt(4)
cv v183,v110,v111
st v[180:183],v247,s[44:47],s91 offen offset:1024 sc1
v_mul_f32 v112, v112, v249
v_mul_f32 v113, v113, v249
mm v[4:7],v[200:203],v[128:131],v[4:7]
rt v[204:205],v241 offset:17024
rt v[206:207],v241 offset:18048
v_mul_f32 v114, v114, v249
v_mul_f32 v115, v115, v249
v_mul_f32 v116, v116, v249
v_mul_f32 v117, v117, v249
mm v[8:11],v[208:211],v[128:131],0
v_mul_f32 v118, v118, v249
v_mul_f32 v119, v119, v249
cv v184,v112,v113
cv v185,v114,v115
cv v186,v116,v117
cv v187,v118,v119
mm v[8:11],v[212:215],v[132:135],v[8:11]
rt v[200:201],v241 offset:25472
rt v[202:203],v241 offset:26496
st v[184:187],v247,s[44:47],s91 offen offset:2048 sc1
v_mul_f32 v120, v120, v249
v_mul_f32 v121, v121, v249
mm v[40:43],v[212:215],v[148:151],0
v_mul_f32 v122, v122, v249
v_mul_f32 v123, v123, v249
v_mul_f32 v124, v124, v249
v_mul_f32 v125, v125, v249
v_mul_f32 v126, v126, v249
v_mul_f32 v127, v127, v249
mm v[40:43],v[208:211],v[144:147],v[40:43]
rt v[212:213],v242 offset:17024
rt v[214:215],v242 offset:18048
cv v188,v120,v121
cv v189,v122,v123
cv v190,v124,v125
cv v191,v126,v127
mm v[44:47],v[216:219],v[144:147],0
st v[188:191],v247,s[44:47],s91 offen offset:3072 sc1
mm v[44:47],v[220:223],v[148:151],v[44:47]
rt v[208:209],v242 offset:25472
rt v[210:211],v242 offset:26496
mm v[12:15],v[220:223],v[132:135],0
wt lgkmcnt(4)
mm v[12:15],v[216:219],v[128:131],v[12:15]
rt v[220:221],v243 offset:17024
rt v[222:223],v243 offset:18048
mm v[16:19],v[196:199],v[128:131],0
ar v224,a228
v_add_u32 v225, 0x8000, v224
v_add_u32 v226, 0x2000, v224
v_add_u32 v227, 0xa000, v224
mm v[16:19],v[192:195],v[132:135],v[16:19]
rt v[216:217],v243 offset:25472
rt v[218:219],v243 offset:26496
ar v248,a225
ar v228,a229
mm v[48:51],v[192:195],v[148:151],0
ar v229,a230
mm v[48:51],v[196:199],v[144:147],v[48:51]
rt v[192:193],v240 offset:33792
rt v[194:195],v240 offset:34816
mm v[52:55],v[204:207],v[144:147],0
mm v[52:55],v[200:203],v[148:151],v[52:55]
rt v[196:197],v240 offset:42240
rt v[198:199],v240 offset:43264
mm v[20:23],v[200:203],v[132:135],0
wt lgkmcnt(4)
mm v[20:23],v[204:207],v[128:131],v[20:23]
rt v[200:201],v241 offset:33792
rt v[202:203],v241 offset:34816
mm v[24:27],v[212:215],v[128:131],0
mm v[24:27],v[208:211],v[132:135],v[24:27]
rt v[204:205],v241 offset:42240
rt v[206:207],v241 offset:43264
mm v[56:59],v[208:211],v[148:151],0
mm v[56:59],v[212:215],v[144:147],v[56:59]
rt v[208:209],v242 offset:33792
rt v[210:211],v242 offset:34816
mm v[60:63],v[220:223],v[144:147],0
mm v[60:63],v[216:219],v[148:151],v[60:63]
rt v[212:213],v242 offset:42240
rt v[214:215],v242 offset:43264
mm v[28:31],v[216:219],v[132:135],0
wt lgkmcnt(4)
mm v[28:31],v[220:223],v[128:131],v[28:31]
rt v[216:217],v243 offset:33792
rt v[218:219],v243 offset:34816
mm v[0:3],v[192:195],v[136:139],v[0:3]
ar v249,a231
s_bitcmp1_b32 2863311530, s2
s_cbranch_scc1 .La5_LO_VC1
s_getpc_b64 s[24:25]
.La5_LW3_P1:
s_add_u32 s24, s24, .La5_LW3_R1-.La5_LW3_P1
s_addc_u32 s25, s25, 0
s_mov_b64 exec, 0
s_cmp_eq_u32 s3, 0
s_cbranch_scc1 .La5_LW3_0
s_cmp_eq_u32 s3, 1
s_cbranch_scc1 .La5_LW3_1
s_cmp_eq_u32 s3, 2
s_cbranch_scc1 .La5_LW3_2
s_branch .La5_LW3_3
.La5_LW3_R1:
s_mov_b64 exec, -1
.La5_LO_VE1:
wt vmcnt(0)
buffer_store_dwordx2 v[252:253], v249, s[64:67], s6 offen offset:0 sc1
mm v[0:3],v[196:199],v[140:143],v[0:3]
rt v[220:221],v243 offset:42240
rt v[222:223],v243 offset:43264
mm v[32:35],v[196:199],v[156:159],v[32:35]
mm v[32:35],v[192:195],v[152:155],v[32:35]
rt v[196:197],v240 offset:33920
rt v[198:199],v240 offset:34944
mm v[36:39],v[200:203],v[152:155],v[36:39]
mm v[36:39],v[204:207],v[156:159],v[36:39]
rt v[192:193],v240 offset:42368
rt v[194:195],v240 offset:43392
mm v[4:7],v[204:207],v[140:143],v[4:7]
wt lgkmcnt(4)
mm v[4:7],v[200:203],v[136:139],v[4:7]
rt v[204:205],v241 offset:33920
rt v[206:207],v241 offset:34944
mm v[8:11],v[208:211],v[136:139],v[8:11]
mm v[8:11],v[212:215],v[140:143],v[8:11]
rt v[200:201],v241 offset:42368
rt v[202:203],v241 offset:43392
mm v[40:43],v[212:215],v[156:159],v[40:43]
mm v[40:43],v[208:211],v[152:155],v[40:43]
rt v[212:213],v242 offset:33920
rt v[214:215],v242 offset:34944
mm v[44:47],v[216:219],v[152:155],v[44:47]
mm v[44:47],v[220:223],v[156:159],v[44:47]
rt v[208:209],v242 offset:42368
rt v[210:211],v242 offset:43392
mm v[12:15],v[220:223],v[140:143],v[12:15]
wt lgkmcnt(4)
mm v[12:15],v[216:219],v[136:139],v[12:15]
rt v[220:221],v243 offset:33920
rt v[222:223],v243 offset:34944
mm v[16:19],v[196:199],v[136:139],v[16:19]
mm v[16:19],v[192:195],v[140:143],v[16:19]
rt v[216:217],v243 offset:42368
rt v[218:219],v243 offset:43392
mm v[48:51],v[192:195],v[156:159],v[48:51]
mm v[48:51],v[196:199],v[152:155],v[48:51]
rt v[192:193],v240 offset:16896
rt v[194:195],v240 offset:17920
mm v[52:55],v[204:207],v[152:155],v[52:55]
mm v[52:55],v[200:203],v[156:159],v[52:55]
rt v[196:197],v240 offset:25344
rt v[198:199],v240 offset:26368
mm v[20:23],v[200:203],v[140:143],v[20:23]
wt lgkmcnt(4)
mm v[20:23],v[204:207],v[136:139],v[20:23]
rt v[200:201],v241 offset:16896
rt v[202:203],v241 offset:17920
mm v[24:27],v[212:215],v[136:139],v[24:27]
mm v[24:27],v[208:211],v[140:143],v[24:27]
rt v[204:205],v241 offset:25344
rt v[206:207],v241 offset:26368
mm v[56:59],v[208:211],v[156:159],v[56:59]
mm v[56:59],v[212:215],v[152:155],v[56:59]
rt v[208:209],v242 offset:16896
rt v[210:211],v242 offset:17920
mm v[60:63],v[220:223],v[152:155],v[60:63]
mm v[60:63],v[216:219],v[156:159],v[60:63]
rt v[212:213],v242 offset:25344
rt v[214:215],v242 offset:26368
mm v[28:31],v[216:219],v[140:143],v[28:31]
wt lgkmcnt(4)
mm v[28:31],v[220:223],v[136:139],v[28:31]
rt v[216:217],v243 offset:16896
rt v[218:219],v243 offset:17920
s_cmp_lg_u32 s30, 0
s_cbranch_scc1 .La5_LO_W
; ---- O projection: 256 x 128 tile over eight heads
.La5_LO_START:
s_nop 0
s_nop 0
.La5_LW6_7:
s_cbranch_execz .La5_LW6_R0
s_bitcmp1_b32 2863311530, s2
s_cbranch_scc1 .La5_LO_SC
.La5_LO_SB:
mm v[64:67],v[192:195],v[160:163],0
buffer_load_dwordx2 v[250:251], v248, s[64:67], s5 offen sc1
mm v[64:67],v[196:199],v[164:167],v[64:67]
rt v[220:221],v243 offset:25344
rt v[222:223],v243 offset:26368
mm v[96:99],v[196:199],v[180:183],0
s_add_u32 m0, s83, 0
s_bfe_u32 s80, s4, 262148
mm v[96:99],v[192:195],v[176:179],v[96:99]
rt v[196:197],v240 offset:17024
rt v[198:199],v240 offset:18048
s_xor_b32 s80, s80, s72
s_lshl_b32 s80, s80, 18
s_add_u32 s80, s80, 131072
mm v[100:103],v[200:203],v[176:179],0
ld v224,s[56:59],s80 offen lds
mm v[100:103],v[204:207],v[180:183],v[100:103]
rt v[192:193],v240 offset:25472
rt v[194:195],v240 offset:26496
mm v[68:71],v[204:207],v[164:167],0
s_add_u32 m0, s83, 1024
wt lgkmcnt(4)
mm v[68:71],v[200:203],v[160:163],v[68:71]
rt v[204:205],v241 offset:17024
rt v[206:207],v241 offset:18048
mm v[72:75],v[208:211],v[160:163],0
ld v225,s[56:59],s80 offen lds
mm v[72:75],v[212:215],v[164:167],v[72:75]
rt v[200:201],v241 offset:25472
rt v[202:203],v241 offset:26496
mm v[104:107],v[212:215],v[180:183],0
s_add_u32 m0, s83, 2176
s_bfe_u32 s53, s4, 262148
mm v[104:107],v[208:211],v[176:179],v[104:107]
rt v[212:213],v242 offset:17024
rt v[214:215],v242 offset:18048
mm v[108:111],v[216:219],v[176:179],0
ld v226,s[56:59],s80 offen lds
mm v[108:111],v[220:223],v[180:183],v[108:111]
rt v[208:209],v242 offset:25472
rt v[210:211],v242 offset:26496
mm v[76:79],v[220:223],v[164:167],0
s_add_u32 m0, s83, 3200
wt lgkmcnt(4)
mm v[76:79],v[216:219],v[160:163],v[76:79]
rt v[220:221],v243 offset:17024
rt v[222:223],v243 offset:18048
mm v[80:83],v[196:199],v[160:163],0
ld v227,s[56:59],s80 offen lds
mm v[80:83],v[192:195],v[164:167],v[80:83]
rt v[216:217],v243 offset:25472
rt v[218:219],v243 offset:26496
mm v[112:115],v[192:195],v[180:183],0
s_add_u32 m0, s83, 118272
s_bfe_u32 s80, s4, 262152
mm v[112:115],v[196:199],v[176:179],v[112:115]
rt v[192:193],v240 offset:33792
rt v[194:195],v240 offset:34816
s_xor_b32 s80, s80, s72
s_lshl_b32 s80, s80, 18
mm v[116:119],v[204:207],v[176:179],0
ld v224,s[56:59],s80 offen lds
mm v[116:119],v[200:203],v[180:183],v[116:119]
rt v[196:197],v240 offset:42240
rt v[198:199],v240 offset:43264
mm v[84:87],v[200:203],v[164:167],0
s_add_u32 m0, s83, 119296
wt lgkmcnt(4)
mm v[84:87],v[204:207],v[160:163],v[84:87]
rt v[200:201],v241 offset:33792
rt v[202:203],v241 offset:34816
mm v[88:91],v[212:215],v[160:163],0
ld v225,s[56:59],s80 offen lds
mm v[88:91],v[208:211],v[164:167],v[88:91]
rt v[204:205],v241 offset:42240
rt v[206:207],v241 offset:43264
mm v[120:123],v[208:211],v[180:183],0
s_add_u32 m0, s83, 120448
s_xor_b32 s53, s53, s72
mm v[120:123],v[212:215],v[176:179],v[120:123]
rt v[208:209],v242 offset:33792
rt v[210:211],v242 offset:34816
mm v[124:127],v[220:223],v[176:179],0
ld v226,s[56:59],s80 offen lds
mm v[124:127],v[216:219],v[180:183],v[124:127]
rt v[212:213],v242 offset:42240
rt v[214:215],v242 offset:43264
mm v[92:95],v[216:219],v[164:167],0
wt vmcnt(7)
v_cmp_eq_u64 vcc, v[250:251], v[252:253]
s_nop 0
s_bitcmp1_b32 vcc_lo, 1
s_nop 0
s_cbranch_scc0 .La5_LO_NB1
.La5_LO_KB1:
wt lgkmcnt(4)
mm v[92:95],v[220:223],v[160:163],v[92:95]
rt v[216:217],v243 offset:33792
rt v[218:219],v243 offset:34816
s_lshl_b32 s53, s53, 13
mm v[64:67],v[192:195],v[168:171],v[64:67]
ld a[0:3],v244,s[44:47],s53 offen offset:0 sc1
mm v[64:67],v[196:199],v[172:175],v[64:67]
rt v[220:221],v243 offset:42240
rt v[222:223],v243 offset:43264
mm v[96:99],v[196:199],v[188:191],v[96:99]
ld a[4:7],v245,s[44:47],s53 offen offset:0 sc1
mm v[96:99],v[192:195],v[184:187],v[96:99]
rt v[196:197],v240 offset:33920
rt v[198:199],v240 offset:34944
mm v[100:103],v[200:203],v[184:187],v[100:103]
ld a[8:11],v246,s[44:47],s53 offen offset:0 sc1
mm v[100:103],v[204:207],v[188:191],v[100:103]
rt v[192:193],v240 offset:42368
rt v[194:195],v240 offset:43392
mm v[68:71],v[204:207],v[172:175],v[68:71]
wt lgkmcnt(4)
s_bfe_u32 s54, s4, 262152
mm v[68:71],v[200:203],v[168:171],v[68:71]
rt v[204:205],v241 offset:33920
rt v[206:207],v241 offset:34944
mm v[72:75],v[208:211],v[168:171],v[72:75]
ld a[12:15],v247,s[44:47],s53 offen offset:0 sc1
mm v[72:75],v[212:215],v[172:175],v[72:75]
rt v[200:201],v241 offset:42368
rt v[202:203],v241 offset:43392
mm v[104:107],v[212:215],v[188:191],v[104:107]
ld a[16:19],v244,s[44:47],s53 offen offset:1024 sc1
mm v[104:107],v[208:211],v[184:187],v[104:107]
rt v[212:213],v242 offset:33920
rt v[214:215],v242 offset:34944
mm v[108:111],v[216:219],v[184:187],v[108:111]
ld a[20:23],v245,s[44:47],s53 offen offset:1024 sc1
mm v[108:111],v[220:223],v[188:191],v[108:111]
rt v[208:209],v242 offset:42368
rt v[210:211],v242 offset:43392
mm v[76:79],v[220:223],v[172:175],v[76:79]
s_nop 0
s_barrier
wt lgkmcnt(4)
mm v[76:79],v[216:219],v[168:171],v[76:79]
rt v[220:221],v243 offset:33920
rt v[222:223],v243 offset:34944
mm v[80:83],v[196:199],v[168:171],v[80:83]
ld a[24:27],v246,s[44:47],s53 offen offset:1024 sc1
mm v[80:83],v[192:195],v[172:175],v[80:83]
rt v[216:217],v243 offset:42368
rt v[218:219],v243 offset:43392
mm v[112:115],v[192:195],v[188:191],v[112:115]
ld a[28:31],v247,s[44:47],s53 offen offset:1024 sc1
mm v[112:115],v[196:199],v[184:187],v[112:115]
rt v[192:193],v240 offset:50688
rt v[194:195],v240 offset:51712
mm v[116:119],v[204:207],v[184:187],v[116:119]
ld a[32:35],v244,s[44:47],s53 offen offset:2048 sc1
mm v[116:119],v[200:203],v[188:191],v[116:119]
rt v[196:197],v240 offset:59136
rt v[198:199],v240 offset:60160
mm v[84:87],v[200:203],v[172:175],v[84:87]
wt lgkmcnt(4)
s_xor_b32 s54, s54, s72
mm v[84:87],v[204:207],v[168:171],v[84:87]
rt v[200:201],v241 offset:50688
rt v[202:203],v241 offset:51712
mm v[88:91],v[212:215],v[168:171],v[88:91]
ld a[36:39],v245,s[44:47],s53 offen offset:2048 sc1
mm v[88:91],v[208:211],v[172:175],v[88:91]
rt v[204:205],v241 offset:59136
rt v[206:207],v241 offset:60160
mm v[120:123],v[208:211],v[188:191],v[120:123]
ld a[40:43],v246,s[44:47],s53 offen offset:2048 sc1
mm v[120:123],v[212:215],v[184:187],v[120:123]
rt v[208:209],v242 offset:50688
rt v[210:211],v242 offset:51712
mm v[124:127],v[220:223],v[184:187],v[124:127]
ld a[44:47],v247,s[44:47],s53 offen offset:2048 sc1
mm v[124:127],v[216:219],v[188:191],v[124:127]
rt v[212:213],v242 offset:59136
rt v[214:215],v242 offset:60160
mm v[92:95],v[216:219],v[172:175],v[92:95]
wt vmcnt(4) lgkmcnt(4)
s_lshl_b32 s54, s54, 13
mm v[92:95],v[220:223],v[168:171],v[92:95]
rt v[216:217],v243 offset:50688
rt v[218:219],v243 offset:51712
mm v[0:3],v[192:195],a[0:3],v[0:3]
s_bfe_u32 s55, s4, 262156
s_xor_b32 s55, s55, s72
.La5_LW3_0:
s_cbranch_execz .La5_LW3_4
mm v[0:3],v[196:199],a[16:19],v[0:3]
ld a[48:51],v244,s[44:47],s53 offen offset:3072 sc1
mm v[32:35],v[196:199],a[20:23],v[32:35]
s_lshl_b32 s55, s55, 13
s_bfe_u32 s52, s4, 262160
mm v[32:35],v[192:195],a[4:7],v[32:35]
rt v[220:221],v243 offset:59136
rt v[222:223],v243 offset:60160
mm v[64:67],v[192:195],a[8:11],v[64:67]
s_xor_b32 s52, s52, s72
s_lshl_b32 s52, s52, 13
mm v[64:67],v[196:199],a[24:27],v[64:67]
ld a[52:55],v245,s[44:47],s53 offen offset:3072 sc1
mm v[96:99],v[196:199],a[28:31],v[96:99]
mm v[96:99],v[192:195],a[12:15],v[96:99]
rt v[196:197],v240 offset:50816
rt v[198:199],v240 offset:51840
mm v[100:103],v[200:203],a[12:15],v[100:103]
mm v[100:103],v[204:207],a[28:31],v[100:103]
buffer_load_dwordx2 v[250:251], v248, s[64:67], s5 offen sc1
mm v[68:71],v[204:207],a[24:27],v[68:71]
mm v[68:71],v[200:203],a[8:11],v[68:71]
rt v[192:193],v240 offset:59264
rt v[194:195],v240 offset:60288
mm v[36:39],v[200:203],a[4:7],v[36:39]
mm v[36:39],v[204:207],a[20:23],v[36:39]
ld a[56:59],v246,s[44:47],s53 offen offset:3072 sc1
mm v[4:7],v[204:207],a[16:19],v[4:7]
wt lgkmcnt(4)
mm v[4:7],v[200:203],a[0:3],v[4:7]
rt v[204:205],v241 offset:50816
rt v[206:207],v241 offset:51840
mm v[8:11],v[208:211],a[0:3],v[8:11]
mm v[8:11],v[212:215],a[16:19],v[8:11]
ld a[60:63],v247,s[44:47],s53 offen offset:3072 sc1
mm v[40:43],v[212:215],a[20:23],v[40:43]
s_bfe_u32 s53, s4, 262164
s_xor_b32 s53, s53, s72
mm v[40:43],v[208:211],a[4:7],v[40:43]
rt v[200:201],v241 offset:59264
rt v[202:203],v241 offset:60288
mm v[72:75],v[208:211],a[8:11],v[72:75]
s_add_u32 m0, s83, 121472
s_lshl_b32 s53, s53, 13
mm v[72:75],v[212:215],a[24:27],v[72:75]
ld v227,s[56:59],s80 offen lds
mm v[104:107],v[212:215],a[28:31],v[104:107]
s_add_u32 s80, s80, 131072
mm v[104:107],v[208:211],a[12:15],v[104:107]
rt v[212:213],v242 offset:50816
rt v[214:215],v242 offset:51840
mm v[108:111],v[216:219],a[12:15],v[108:111]
s_add_u32 m0, s83, 67584
mm v[108:111],v[220:223],a[28:31],v[108:111]
ld v224,s[56:59],s80 offen lds
mm v[76:79],v[220:223],a[24:27],v[76:79]
mm v[76:79],v[216:219],a[8:11],v[76:79]
rt v[208:209],v242 offset:59264
rt v[210:211],v242 offset:60288
mm v[44:47],v[216:219],a[4:7],v[44:47]
s_add_u32 m0, s83, 68608
mm v[44:47],v[220:223],a[20:23],v[44:47]
ld v225,s[56:59],s80 offen lds
mm v[12:15],v[220:223],a[16:19],v[12:15]
wt lgkmcnt(4)
mm v[12:15],v[216:219],a[0:3],v[12:15]
rt v[220:221],v243 offset:50816
rt v[222:223],v243 offset:51840
mm v[16:19],v[196:199],a[0:3],v[16:19]
s_add_u32 m0, s83, 69760
mm v[16:19],v[192:195],a[16:19],v[16:19]
ld v226,s[56:59],s80 offen lds
mm v[48:51],v[192:195],a[20:23],v[48:51]
mm v[48:51],v[196:199],a[4:7],v[48:51]
rt v[216:217],v243 offset:59264
rt v[218:219],v243 offset:60288
mm v[80:83],v[196:199],a[8:11],v[80:83]
s_add_u32 m0, s83, 70784
s_nop 0
s_barrier
mm v[80:83],v[192:195],a[24:27],v[80:83]
ld v227,s[56:59],s80 offen lds
mm v[112:115],v[192:195],a[28:31],v[112:115]
s_bfe_u32 s80, s4, 262156
s_xor_b32 s80, s80, s72
mm v[112:115],v[196:199],a[12:15],v[112:115]
rt v[192:193],v240 offset:0
rt v[194:195],v240 offset:1024
mm v[116:119],v[204:207],a[12:15],v[116:119]
s_add_u32 m0, s83, 84480
s_lshl_b32 s80, s80, 18
mm v[116:119],v[200:203],a[28:31],v[116:119]
ld v224,s[56:59],s80 offen lds
mm v[84:87],v[200:203],a[24:27],v[84:87]
mm v[84:87],v[204:207],a[8:11],v[84:87]
rt v[196:197],v240 offset:8448
rt v[198:199],v240 offset:9472
mm v[52:55],v[204:207],a[4:7],v[52:55]
s_add_u32 m0, s83, 85504
mm v[52:55],v[200:203],a[20:23],v[52:55]
ld v225,s[56:59],s80 offen lds
mm v[20:23],v[200:203],a[16:19],v[20:23]
wt lgkmcnt(4)
mm v[20:23],v[204:207],a[0:3],v[20:23]
rt v[200:201],v241 offset:0
rt v[202:203],v241 offset:1024
mm v[24:27],v[212:215],a[0:3],v[24:27]
s_add_u32 m0, s83, 86656
mm v[24:27],v[208:211],a[16:19],v[24:27]
ld v226,s[56:59],s80 offen lds
mm v[56:59],v[208:211],a[20:23],v[56:59]
wt vmcnt(10)
v_cmp_eq_u64 vcc, v[250:251], v[252:253]
mm v[56:59],v[212:215],a[4:7],v[56:59]
rt v[204:205],v241 offset:8448
rt v[206:207],v241 offset:9472
mm v[88:91],v[212:215],a[8:11],v[88:91]
s_add_u32 m0, s83, 87680
mm v[88:91],v[208:211],a[24:27],v[88:91]
ld v227,s[56:59],s80 offen lds
mm v[120:123],v[208:211],a[28:31],v[120:123]
s_bitcmp1_b32 vcc_lo, 2
s_nop 0
s_nop 0
s_cbranch_scc0 .La5_LO_NB2
.La5_LO_KB2:
mm v[120:123],v[212:215],a[12:15],v[120:123]
rt v[208:209],v242 offset:0
rt v[210:211],v242 offset:1024
mm v[124:127],v[220:223],a[12:15],v[124:127]
s_add_u32 s80, s80, 131072
mm v[124:127],v[216:219],a[28:31],v[124:127]
ld a[12:15],v244,s[44:47],s54 offen offset:0 sc1
mm v[92:95],v[216:219],a[24:27],v[92:95]
mm v[92:95],v[220:223],a[8:11],v[92:95]
rt v[212:213],v242 offset:8448
rt v[214:215],v242 offset:9472
mm v[60:63],v[220:223],a[4:7],v[60:63]
mm v[60:63],v[216:219],a[20:23],v[60:63]
mm v[28:31],v[216:219],a[16:19],v[28:31]
wt vmcnt(10) lgkmcnt(4)
mm v[28:31],v[220:223],a[0:3],v[28:31]
rt v[216:217],v243 offset:0
rt v[218:219],v243 offset:1024
mm v[0:3],v[192:195],a[32:35],v[0:3]
mm v[0:3],v[196:199],a[48:51],v[0:3]
ld a[0:3],v245,s[44:47],s54 offen offset:0 sc1
mm v[32:35],v[196:199],a[52:55],v[32:35]
mm v[32:35],v[192:195],a[36:39],v[32:35]
rt v[220:221],v243 offset:8448
rt v[222:223],v243 offset:9472
mm v[64:67],v[192:195],a[40:43],v[64:67]
mm v[64:67],v[196:199],a[56:59],v[64:67]
ld a[4:7],v246,s[44:47],s54 offen offset:0 sc1
mm v[96:99],v[196:199],a[60:63],v[96:99]
mm v[96:99],v[192:195],a[44:47],v[96:99]
rt v[196:197],v240 offset:128
rt v[198:199],v240 offset:1152
mm v[100:103],v[200:203],a[44:47],v[100:103]
s_mov_b32 s86, 0x05040100
s_mov_b32 s87, 0x07060302
mm v[100:103],v[204:207],a[60:63],v[100:103]
ld a[8:11],v247,s[44:47],s54 offen offset:0 sc1
mm v[68:71],v[204:207],a[56:59],v[68:71]
s_nop 0
ar v230,a128
mm v[68:71],v[200:203],a[40:43],v[68:71]
rt v[192:193],v240 offset:8576
rt v[194:195],v240 offset:9600
mm v[36:39],v[200:203],a[36:39],v[36:39]
v_add_u32 v240, 67584, v240
ar v231,a132
mm v[36:39],v[204:207],a[52:55],v[36:39]
ld a[16:19],v244,s[44:47],s54 offen offset:1024 sc1
mm v[4:7],v[204:207],a[48:51],v[4:7]
wt lgkmcnt(4)
v_perm_b32 v232, v231, v230, s86
mm v[4:7],v[200:203],a[32:35],v[4:7]
rt v[204:205],v241 offset:128
rt v[206:207],v241 offset:1152
mm v[8:11],v[208:211],a[32:35],v[8:11]
v_perm_b32 v233, v231, v230, s87
aw a96,v232
mm v[8:11],v[212:215],a[48:51],v[8:11]
ld a[20:23],v245,s[44:47],s54 offen offset:1024 sc1
mm v[40:43],v[212:215],a[52:55],v[40:43]
aw a100,v233
ar v234,a136
mm v[40:43],v[208:211],a[36:39],v[40:43]
rt v[200:201],v241 offset:8576
rt v[202:203],v241 offset:9600
mm v[72:75],v[208:211],a[40:43],v[72:75]
v_add_u32 v241, 67584, v241
ar v235,a140
mm v[72:75],v[212:215],a[56:59],v[72:75]
ld a[24:27],v246,s[44:47],s54 offen offset:1024 sc1
mm v[104:107],v[212:215],a[60:63],v[104:107]
v_perm_b32 v236, v235, v234, s86
v_perm_b32 v237, v235, v234, s87
mm v[104:107],v[208:211],a[44:47],v[104:107]
rt v[212:213],v242 offset:128
rt v[214:215],v242 offset:1152
mm v[108:111],v[216:219],a[44:47],v[108:111]
aw a97,v236
aw a101,v237
mm v[108:111],v[220:223],a[60:63],v[108:111]
ld a[28:31],v247,s[44:47],s54 offen offset:1024 sc1
mm v[76:79],v[220:223],a[56:59],v[76:79]
ar v230,a144
ar v231,a148
mm v[76:79],v[216:219],a[40:43],v[76:79]
rt v[208:209],v242 offset:8576
rt v[210:211],v242 offset:9600
mm v[44:47],v[216:219],a[36:39],v[44:47]
v_add_u32 v242, 67584, v242
v_perm_b32 v232, v231, v230, s86
mm v[44:47],v[220:223],a[52:55],v[44:47]
v_perm_b32 v233, v231, v230, s87
aw a98,v232
mm v[12:15],v[220:223],a[48:51],v[12:15]
wt lgkmcnt(4)
aw a102,v233
mm v[12:15],v[216:219],a[32:35],v[12:15]
rt v[220:221],v243 offset:128
rt v[222:223],v243 offset:1152
mm v[16:19],v[196:199],a[32:35],v[16:19]
s_add_u32 m0, s83, 101376
ar v234,a152
mm v[16:19],v[192:195],a[48:51],v[16:19]
ld v224,s[56:59],s80 offen lds
mm v[48:51],v[192:195],a[52:55],v[48:51]
ar v235,a156
v_perm_b32 v236, v235, v234, s86
mm v[48:51],v[196:199],a[36:39],v[48:51]
rt v[216:217],v243 offset:8576
rt v[218:219],v243 offset:9600
mm v[80:83],v[196:199],a[40:43],v[80:83]
s_add_u32 m0, s83, 102400
v_add_u32 v243, 67584, v243
mm v[80:83],v[192:195],a[56:59],v[80:83]
ld v225,s[56:59],s80 offen lds
mm v[112:115],v[192:195],a[60:63],v[112:115]
wt vmcnt(14)
s_barrier
mm v[112:115],v[196:199],a[44:47],v[112:115]
rt v[192:193],v240 offset:50688
rt v[194:195],v240 offset:51712
mm v[116:119],v[204:207],a[44:47],v[116:119]
s_add_u32 m0, s83, 103552
v_perm_b32 v237, v235, v234, s87
mm v[116:119],v[200:203],a[60:63],v[116:119]
ld v226,s[56:59],s80 offen lds
mm v[84:87],v[200:203],a[56:59],v[84:87]
aw a99,v236
aw a103,v237
mm v[84:87],v[204:207],a[40:43],v[84:87]
rt v[196:197],v240 offset:59136
rt v[198:199],v240 offset:60160
mm v[52:55],v[204:207],a[36:39],v[52:55]
ar v230,a129
ar v231,a133
mm v[52:55],v[200:203],a[52:55],v[52:55]
ld a[64:67],v244,s[44:47],s54 offen offset:2048 sc1
mm v[20:23],v[200:203],a[48:51],v[20:23]
wt lgkmcnt(4)
v_perm_b32 v232, v231, v230, s86
mm v[20:23],v[204:207],a[32:35],v[20:23]
rt v[200:201],v241 offset:50688
rt v[202:203],v241 offset:51712
mm v[24:27],v[212:215],a[32:35],v[24:27]
v_perm_b32 v233, v231, v230, s87
aw a104,v232
mm v[24:27],v[208:211],a[48:51],v[24:27]
ld a[68:71],v245,s[44:47],s54 offen offset:2048 sc1
mm v[56:59],v[208:211],a[52:55],v[56:59]
aw a108,v233
ar v234,a137
mm v[56:59],v[212:215],a[36:39],v[56:59]
rt v[204:205],v241 offset:59136
rt v[206:207],v241 offset:60160
mm v[88:91],v[212:215],a[40:43],v[88:91]
ar v235,a141
v_perm_b32 v236, v235, v234, s86
mm v[88:91],v[208:211],a[56:59],v[88:91]
ld a[72:75],v246,s[44:47],s54 offen offset:2048 sc1
mm v[120:123],v[208:211],a[60:63],v[120:123]
v_perm_b32 v237, v235, v234, s87
aw a105,v236
mm v[120:123],v[212:215],a[44:47],v[120:123]
rt v[208:209],v242 offset:50688
rt v[210:211],v242 offset:51712
mm v[124:127],v[220:223],a[44:47],v[124:127]
aw a109,v237
ar v230,a145
mm v[124:127],v[216:219],a[60:63],v[124:127]
buffer_load_dwordx2 v[250:251], v248, s[64:67], s5 offen sc1
mm v[92:95],v[216:219],a[56:59],v[92:95]
ar v231,a149
v_perm_b32 v232, v231, v230, s86
mm v[92:95],v[220:223],a[40:43],v[92:95]
rt v[212:213],v242 offset:59136
rt v[214:215],v242 offset:60160
mm v[60:63],v[220:223],a[36:39],v[60:63]
v_perm_b32 v233, v231, v230, s87
aw a106,v232
mm v[60:63],v[216:219],a[52:55],v[60:63]
ld a[36:39],v247,s[44:47],s54 offen offset:2048 sc1
mm v[28:31],v[216:219],a[48:51],v[28:31]
wt vmcnt(8) lgkmcnt(4)
aw a110,v233
mm v[28:31],v[220:223],a[32:35],v[28:31]
rt v[216:217],v243 offset:50688
rt v[218:219],v243 offset:51712
mm v[0:3],v[192:195],a[12:15],v[0:3]
ar v234,a153
ar v235,a157
mm v[0:3],v[196:199],a[16:19],v[0:3]
ld a[32:35],v244,s[44:47],s54 offen offset:3072 sc1
mm v[32:35],v[196:199],a[20:23],v[32:35]
v_perm_b32 v236, v235, v234, s86
v_perm_b32 v237, v235, v234, s87
mm v[32:35],v[192:195],a[0:3],v[32:35]
rt v[220:221],v243 offset:59136
rt v[222:223],v243 offset:60160
mm v[64:67],v[192:195],a[4:7],v[64:67]
aw a107,v236
aw a111,v237
mm v[64:67],v[196:199],a[24:27],v[64:67]
ld a[40:43],v245,s[44:47],s54 offen offset:3072 sc1
mm v[96:99],v[196:199],a[28:31],v[96:99]
ar v230,a130
ar v231,a134
mm v[96:99],v[192:195],a[8:11],v[96:99]
rt v[196:197],v240 offset:50816
rt v[198:199],v240 offset:51840
mm v[100:103],v[200:203],a[8:11],v[100:103]
v_perm_b32 v232, v231, v230, s86
v_perm_b32 v233, v231, v230, s87
mm v[100:103],v[204:207],a[28:31],v[100:103]
ld a[44:47],v246,s[44:47],s54 offen offset:3072 sc1
mm v[68:71],v[204:207],a[24:27],v[68:71]
aw a112,v232
aw a116,v233
mm v[68:71],v[200:203],a[4:7],v[68:71]
rt v[192:193],v240 offset:59264
rt v[194:195],v240 offset:60288
mm v[36:39],v[200:203],a[0:3],v[36:39]
ar v234,a138
ar v235,a142
mm v[36:39],v[204:207],a[20:23],v[36:39]
ld a[48:51],v247,s[44:47],s54 offen offset:3072 sc1
mm v[4:7],v[204:207],a[16:19],v[4:7]
wt lgkmcnt(4)
s_xor_b32 s54, s72, 25
mm v[4:7],v[200:203],a[12:15],v[4:7]
rt v[204:205],v241 offset:50816
rt v[206:207],v241 offset:51840
mm v[8:11],v[208:211],a[12:15],v[8:11]
s_add_u32 m0, s83, 104576
s_lshl_b32 s54, s54, 13
mm v[8:11],v[212:215],a[16:19],v[8:11]
ld v227,s[56:59],s80 offen lds
mm v[40:43],v[212:215],a[20:23],v[40:43]
s_bitcmp1_b32 vcc_lo, 3
s_cbranch_scc0 .La5_LO_NB3
.La5_LO_KB3:
mm v[40:43],v[208:211],a[0:3],v[40:43]
rt v[200:201],v241 offset:59264
rt v[202:203],v241 offset:60288
mm v[72:75],v[208:211],a[4:7],v[72:75]
s_bfe_u32 s80, s4, 262160
s_xor_b32 s80, s80, s72
mm v[72:75],v[212:215],a[24:27],v[72:75]
ld a[52:55],v244,s[44:47],s55 offen offset:0 sc1
mm v[104:107],v[212:215],a[28:31],v[104:107]
s_lshl_b32 s80, s80, 18
v_perm_b32 v236, v235, v234, s86
mm v[104:107],v[208:211],a[8:11],v[104:107]
rt v[212:213],v242 offset:50816
rt v[214:215],v242 offset:51840
mm v[108:111],v[216:219],a[8:11],v[108:111]
v_perm_b32 v237, v235, v234, s87
aw a113,v236
mm v[108:111],v[220:223],a[28:31],v[108:111]
ld a[56:59],v245,s[44:47],s55 offen offset:0 sc1
mm v[76:79],v[220:223],a[24:27],v[76:79]
aw a117,v237
ar v230,a146
mm v[76:79],v[216:219],a[4:7],v[76:79]
rt v[208:209],v242 offset:59264
rt v[210:211],v242 offset:60288
mm v[44:47],v[216:219],a[0:3],v[44:47]
ar v231,a150
v_perm_b32 v232, v231, v230, s86
mm v[44:47],v[220:223],a[20:23],v[44:47]
v_perm_b32 v233, v231, v230, s87
aw a114,v232
mm v[12:15],v[220:223],a[16:19],v[12:15]
wt lgkmcnt(4)
aw a118,v233
mm v[12:15],v[216:219],a[12:15],v[12:15]
rt v[220:221],v243 offset:50816
rt v[222:223],v243 offset:51840
mm v[16:19],v[196:199],a[12:15],v[16:19]
ar v234,a154
ar v235,a158
mm v[16:19],v[192:195],a[16:19],v[16:19]
ld a[60:63],v246,s[44:47],s55 offen offset:0 sc1
mm v[48:51],v[192:195],a[20:23],v[48:51]
v_perm_b32 v236, v235, v234, s86
v_perm_b32 v237, v235, v234, s87
mm v[48:51],v[196:199],a[0:3],v[48:51]
rt v[216:217],v243 offset:59264
rt v[218:219],v243 offset:60288
mm v[80:83],v[196:199],a[4:7],v[80:83]
aw a115,v236
aw a119,v237
mm v[80:83],v[192:195],a[24:27],v[80:83]
ld a[76:79],v247,s[44:47],s55 offen offset:0 sc1
mm v[112:115],v[192:195],a[28:31],v[112:115]
ar v230,a131
ar v231,a135
mm v[112:115],v[196:199],a[8:11],v[112:115]
rt v[192:193],v240 offset:0
rt v[194:195],v240 offset:1024
mm v[116:119],v[204:207],a[8:11],v[116:119]
v_perm_b32 v232, v231, v230, s86
v_perm_b32 v233, v231, v230, s87
mm v[116:119],v[200:203],a[28:31],v[116:119]
ld a[80:83],v244,s[44:47],s55 offen offset:1024 sc1
mm v[84:87],v[200:203],a[24:27],v[84:87]
aw a120,v232
aw a124,v233
mm v[84:87],v[204:207],a[4:7],v[84:87]
rt v[196:197],v240 offset:8448
rt v[198:199],v240 offset:9472
mm v[52:55],v[204:207],a[0:3],v[52:55]
s_load_dwordx2 s[16:17], s[0:1], 0
s_load_dwordx4 s[36:39], s[0:1], 80
mm v[52:55],v[200:203],a[20:23],v[52:55]
ld a[84:87],v245,s[44:47],s55 offen offset:1024 sc1
mm v[20:23],v[200:203],a[16:19],v[20:23]
wt lgkmcnt(4)
ar v234,a139
mm v[20:23],v[204:207],a[12:15],v[20:23]
rt v[200:201],v241 offset:0
rt v[202:203],v241 offset:1024
mm v[24:27],v[212:215],a[12:15],v[24:27]
ar v235,a143
v_perm_b32 v236, v235, v234, s86
mm v[24:27],v[208:211],a[16:19],v[24:27]
ld a[88:91],v246,s[44:47],s55 offen offset:1024 sc1
mm v[56:59],v[208:211],a[20:23],v[56:59]
v_perm_b32 v237, v235, v234, s87
aw a121,v236
mm v[56:59],v[212:215],a[0:3],v[56:59]
rt v[204:205],v241 offset:8448
rt v[206:207],v241 offset:9472
mm v[88:91],v[212:215],a[4:7],v[88:91]
aw a125,v237
ar v230,a147
mm v[88:91],v[208:211],a[24:27],v[88:91]
ld a[92:95],v247,s[44:47],s55 offen offset:1024 sc1
mm v[120:123],v[208:211],a[28:31],v[120:123]
ar v231,a151
v_perm_b32 v232, v231, v230, s86
mm v[120:123],v[212:215],a[8:11],v[120:123]
rt v[208:209],v242 offset:0
rt v[210:211],v242 offset:1024
mm v[124:127],v[220:223],a[8:11],v[124:127]
s_add_u32 m0, s83, 16896
v_perm_b32 v233, v231, v230, s87
mm v[124:127],v[216:219],a[28:31],v[124:127]
ld v224,s[56:59],s80 offen lds
mm v[92:95],v[216:219],a[24:27],v[92:95]
aw a122,v232
aw a126,v233
mm v[92:95],v[220:223],a[4:7],v[92:95]
rt v[212:213],v242 offset:8448
rt v[214:215],v242 offset:9472
mm v[60:63],v[220:223],a[0:3],v[60:63]
ar v234,a155
ar v235,a159
mm v[60:63],v[216:219],a[20:23],v[60:63]
v_perm_b32 v236, v235, v234, s86
v_perm_b32 v237, v235, v234, s87
mm v[28:31],v[216:219],a[16:19],v[28:31]
wt vmcnt(10) lgkmcnt(4)
aw a123,v236
mm v[28:31],v[220:223],a[12:15],v[28:31]
rt v[216:217],v243 offset:0
rt v[218:219],v243 offset:1024
mm v[0:3],v[192:195],a[64:67],v[0:3]
s_add_u32 m0, s83, 17920
aw a127,v237
mm v[0:3],v[196:199],a[32:35],v[0:3]
ld v225,s[56:59],s80 offen lds
mm v[32:35],v[196:199],a[40:43],v[32:35]
ar v230,a160
ar v231,a164
mm v[32:35],v[192:195],a[68:71],v[32:35]
rt v[220:221],v243 offset:8448
rt v[222:223],v243 offset:9472
mm v[64:67],v[192:195],a[72:75],v[64:67]
s_add_u32 m0, s83, 19072
v_perm_b32 v232, v231, v230, s86
mm v[64:67],v[196:199],a[44:47],v[64:67]
ld v226,s[56:59],s80 offen lds
mm v[96:99],v[196:199],a[48:51],v[96:99]
v_perm_b32 v233, v231, v230, s87
aw a128,v232
mm v[96:99],v[192:195],a[36:39],v[96:99]
rt v[196:197],v240 offset:128
rt v[198:199],v240 offset:1152
mm v[100:103],v[200:203],a[36:39],v[100:103]
aw a132,v233
ar v234,a168
mm v[100:103],v[204:207],a[48:51],v[100:103]
ld a[0:3],v244,s[44:47],s55 offen offset:2048 sc1
mm v[68:71],v[204:207],a[44:47],v[68:71]
ar v235,a172
v_perm_b32 v236, v235, v234, s86
mm v[68:71],v[200:203],a[72:75],v[68:71]
rt v[192:193],v240 offset:8576
rt v[194:195],v240 offset:9600
mm v[36:39],v[200:203],a[68:71],v[36:39]
v_perm_b32 v237, v235, v234, s87
aw a129,v236
mm v[36:39],v[204:207],a[40:43],v[36:39]
ld a[4:7],v245,s[44:47],s55 offen offset:2048 sc1
mm v[4:7],v[204:207],a[32:35],v[4:7]
wt lgkmcnt(4)
.La5_LW3_1:
s_cbranch_execz .La5_LW3_5
aw a133,v237
mm v[4:7],v[200:203],a[64:67],v[4:7]
rt v[204:205],v241 offset:128
rt v[206:207],v241 offset:1152
mm v[8:11],v[208:211],a[64:67],v[8:11]
ar v230,a176
ar v231,a180
mm v[8:11],v[212:215],a[32:35],v[8:11]
ld a[8:11],v246,s[44:47],s55 offen offset:2048 sc1
mm v[40:43],v[212:215],a[40:43],v[40:43]
v_perm_b32 v232, v231, v230, s86
v_perm_b32 v233, v231, v230, s87
mm v[40:43],v[208:211],a[68:71],v[40:43]
rt v[200:201],v241 offset:8576
rt v[202:203],v241 offset:9600
mm v[72:75],v[208:211],a[72:75],v[72:75]
aw a130,v232
aw a134,v233
mm v[72:75],v[212:215],a[44:47],v[72:75]
ld a[12:15],v247,s[44:47],s55 offen offset:2048 sc1
mm v[104:107],v[212:215],a[48:51],v[104:107]
ar v234,a184
ar v235,a188
mm v[104:107],v[208:211],a[36:39],v[104:107]
rt v[212:213],v242 offset:128
rt v[214:215],v242 offset:1152
mm v[108:111],v[216:219],a[36:39],v[108:111]
v_perm_b32 v236, v235, v234, s86
v_perm_b32 v237, v235, v234, s87
mm v[108:111],v[220:223],a[48:51],v[108:111]
ld a[16:19],v244,s[44:47],s55 offen offset:3072 sc1
mm v[76:79],v[220:223],a[44:47],v[76:79]
aw a131,v236
aw a135,v237
mm v[76:79],v[216:219],a[72:75],v[76:79]
rt v[208:209],v242 offset:8576
rt v[210:211],v242 offset:9600
mm v[44:47],v[216:219],a[68:71],v[44:47]
ar v230,a161
ar v231,a165
mm v[44:47],v[220:223],a[40:43],v[44:47]
s_nop 0
v_cmp_eq_u64 vcc, v[250:251], v[252:253]
mm v[12:15],v[220:223],a[32:35],v[12:15]
wt lgkmcnt(4)
v_perm_b32 v232, v231, v230, s86
mm v[12:15],v[216:219],a[64:67],v[12:15]
rt v[220:221],v243 offset:128
rt v[222:223],v243 offset:1152
mm v[16:19],v[196:199],a[64:67],v[16:19]
v_perm_b32 v233, v231, v230, s87
aw a136,v232
mm v[16:19],v[192:195],a[32:35],v[16:19]
ld a[20:23],v245,s[44:47],s55 offen offset:3072 sc1
mm v[48:51],v[192:195],a[40:43],v[48:51]
aw a140,v233
ar v234,a169
mm v[48:51],v[196:199],a[68:71],v[48:51]
rt v[216:217],v243 offset:8576
rt v[218:219],v243 offset:9600
mm v[80:83],v[196:199],a[72:75],v[80:83]
ar v235,a173
v_perm_b32 v236, v235, v234, s86
mm v[80:83],v[192:195],a[44:47],v[80:83]
ld a[24:27],v246,s[44:47],s55 offen offset:3072 sc1
mm v[112:115],v[192:195],a[48:51],v[112:115]
wt vmcnt(18) lgkmcnt(0)
s_barrier
mm v[112:115],v[196:199],a[36:39],v[112:115]
rt v[192:193],v240 offset:16896
rt v[194:195],v240 offset:17920
mm v[116:119],v[204:207],a[36:39],v[116:119]
s_xor_b32 s90, s72, 0
s_lshl_b32 s92, s76, 5
mm v[116:119],v[200:203],a[48:51],v[116:119]
buffer_load_dwordx2 v[250:251], v248, s[64:67], s5 offen sc1
mm v[84:87],v[200:203],a[44:47],v[84:87]
s_add_u32 s92, s92, s90
s_lshl2_add_u32 s92, s92, s3
mm v[84:87],v[204:207],a[72:75],v[84:87]
rt v[196:197],v240 offset:25344
rt v[198:199],v240 offset:26368
mm v[52:55],v[204:207],a[68:71],v[52:55]
s_lshl_b32 s92, s92, 12
v_perm_b32 v237, v235, v234, s87
mm v[52:55],v[200:203],a[40:43],v[52:55]
ld a[28:31],v247,s[44:47],s55 offen offset:3072 sc1
mm v[20:23],v[200:203],a[32:35],v[20:23]
s_nop 0
s_bfe_u32 s55, s4, 262168
mm v[20:23],v[204:207],a[64:67],v[20:23]
rt v[200:201],v241 offset:16896
rt v[202:203],v241 offset:17920
mm v[24:27],v[212:215],a[64:67],v[24:27]
s_add_u32 m0, s83, 20096
s_xor_b32 s55, s55, s72
mm v[24:27],v[208:211],a[32:35],v[24:27]
ld v227,s[56:59],s80 offen lds
mm v[56:59],v[208:211],a[40:43],v[56:59]
s_add_u32 s80, s80, 131072
s_lshl_b32 s55, s55, 13
mm v[56:59],v[212:215],a[68:71],v[56:59]
rt v[204:205],v241 offset:25344
rt v[206:207],v241 offset:26368
mm v[88:91],v[212:215],a[72:75],v[88:91]
s_add_u32 m0, s83, 33792
aw a137,v236
mm v[88:91],v[208:211],a[44:47],v[88:91]
ld v224,s[56:59],s80 offen lds
mm v[120:123],v[208:211],a[48:51],v[120:123]
s_add_u32 s68, s16, s92
s_addc_u32 s69, s17, 0
mm v[120:123],v[212:215],a[36:39],v[120:123]
rt v[208:209],v242 offset:16896
rt v[210:211],v242 offset:17920
mm v[124:127],v[220:223],a[36:39],v[124:127]
s_add_u32 m0, s83, 34816
s_and_b32 s69, s69, 0xffff
mm v[124:127],v[216:219],a[48:51],v[124:127]
ld v225,s[56:59],s80 offen lds
mm v[92:95],v[216:219],a[44:47],v[92:95]
s_mov_b32 s70, 4096
s_mov_b32 s71, 0x20000
mm v[92:95],v[220:223],a[72:75],v[92:95]
rt v[212:213],v242 offset:25344
rt v[214:215],v242 offset:26368
mm v[60:63],v[220:223],a[68:71],v[60:63]
s_add_u32 m0, s83, 35968
aw a141,v237
mm v[60:63],v[216:219],a[40:43],v[60:63]
ld v226,s[56:59],s80 offen lds
mm v[28:31],v[216:219],a[32:35],v[28:31]
wt vmcnt(16) lgkmcnt(4)
ar v230,a177
mm v[28:31],v[220:223],a[64:67],v[28:31]
rt v[216:217],v243 offset:16896
rt v[218:219],v243 offset:17920
mm v[0:3],v[192:195],a[52:55],v[0:3]
s_add_u32 m0, s83, 36992
ar v231,a181
mm v[0:3],v[196:199],a[80:83],v[0:3]
ld v227,s[56:59],s80 offen lds
mm v[32:35],v[196:199],a[84:87],v[32:35]
s_bitcmp1_b32 vcc_lo, 4
s_cbranch_scc0 .La5_LO_NB4
.La5_LO_KB4:
mm v[32:35],v[192:195],a[56:59],v[32:35]
rt v[220:221],v243 offset:25344
rt v[222:223],v243 offset:26368
mm v[64:67],v[192:195],a[60:63],v[64:67]
s_bfe_u32 s80, s4, 262164
s_xor_b32 s80, s80, s72
mm v[64:67],v[196:199],a[88:91],v[64:67]
ld a[32:35],v244,s[44:47],s52 offen offset:0 sc1
mm v[96:99],v[196:199],a[92:95],v[96:99]
s_lshl_b32 s80, s80, 18
v_perm_b32 v232, v231, v230, s86
mm v[96:99],v[192:195],a[76:79],v[96:99]
rt v[196:197],v240 offset:17024
rt v[198:199],v240 offset:18048
mm v[100:103],v[200:203],a[76:79],v[100:103]
s_add_u32 s48, s36, 0
s_addc_u32 s49, s37, 0
mm v[100:103],v[204:207],a[92:95],v[100:103]
ld a[36:39],v245,s[44:47],s52 offen offset:0 sc1
mm v[68:71],v[204:207],a[88:91],v[68:71]
s_and_b32 s49, s49, 0xffff
s_mov_b32 s50, 2048
mm v[68:71],v[200:203],a[60:63],v[68:71]
rt v[192:193],v240 offset:25472
rt v[194:195],v240 offset:26496
mm v[36:39],v[200:203],a[56:59],v[36:39]
s_mov_b32 s51, 0x20000
v_and_b32 v238, 0xf0, v255
mm v[36:39],v[204:207],a[84:87],v[36:39]
ld a[40:43],v246,s[44:47],s52 offen offset:0 sc1
mm v[4:7],v[204:207],a[80:83],v[4:7]
wt lgkmcnt(4)
v_lshlrev_b32 v238, 2, v238
mm v[4:7],v[200:203],a[52:55],v[4:7]
rt v[204:205],v241 offset:17024
rt v[206:207],v241 offset:18048
mm v[8:11],v[208:211],a[52:55],v[8:11]
v_bfe_u32 v249, v255, 8, 1
v_lshl_add_u32 v238, v249, 5, v238
mm v[8:11],v[212:215],a[80:83],v[8:11]
ld a[44:47],v247,s[44:47],s52 offen offset:0 sc1
mm v[40:43],v[212:215],a[84:87],v[40:43]
v_lshrrev_b32 v249, 9, v255
v_lshl_add_u32 v238, v249, 4, v238
mm v[40:43],v[208:211],a[56:59],v[40:43]
rt v[200:201],v241 offset:25472
rt v[202:203],v241 offset:26496
mm v[72:75],v[208:211],a[60:63],v[72:75]
s_lshr_b32 s23, s72, 3
s_lshl3_add_u32 s92, s23, s76
mm v[72:75],v[212:215],a[88:91],v[72:75]
ld a[48:51],v244,s[44:47],s52 offen offset:1024 sc1
mm v[104:107],v[212:215],a[92:95],v[104:107]
s_lshl_b32 s92, s92, 19
s_lshl_b32 s97, s3, 16
mm v[104:107],v[208:211],a[76:79],v[104:107]
rt v[212:213],v242 offset:17024
rt v[214:215],v242 offset:18048
mm v[108:111],v[216:219],a[76:79],v[108:111]
s_add_u32 s94, s92, s97
s_add_u32 s95, s94, 32768
mm v[108:111],v[220:223],a[92:95],v[108:111]
ld a[64:67],v245,s[44:47],s52 offen offset:1024 sc1
mm v[76:79],v[220:223],a[88:91],v[76:79]
s_sub_u32 s96, s92, s97
s_add_u32 s96, s96, 458752
mm v[76:79],v[216:219],a[60:63],v[76:79]
rt v[208:209],v242 offset:25472
rt v[210:211],v242 offset:26496
mm v[44:47],v[216:219],a[56:59],v[44:47]
s_add_u32 s97, s96, 32768
s_lshl3_add_u32 s92, s23, s101
mm v[44:47],v[220:223],a[84:87],v[44:47]
s_lshl_b32 s85, s76, 14
s_lshl3_add_u32 s92, s92, s85
mm v[12:15],v[220:223],a[80:83],v[12:15]
wt lgkmcnt(4)
s_add_u32 s92, s92, 2097152
mm v[12:15],v[216:219],a[52:55],v[12:15]
rt v[220:221],v243 offset:17024
rt v[222:223],v243 offset:18048
mm v[16:19],v[196:199],a[52:55],v[16:19]
v_perm_b32 v233, v231, v230, s87
aw a138,v232
mm v[16:19],v[192:195],a[80:83],v[16:19]
ld a[68:71],v246,s[44:47],s52 offen offset:1024 sc1
mm v[48:51],v[192:195],a[84:87],v[48:51]
aw a142,v233
ar v234,a185
mm v[48:51],v[196:199],a[56:59],v[48:51]
rt v[216:217],v243 offset:25472
rt v[218:219],v243 offset:26496
mm v[80:83],v[196:199],a[60:63],v[80:83]
ar v235,a189
v_perm_b32 v236, v235, v234, s86
mm v[80:83],v[192:195],a[88:91],v[80:83]
ld a[72:75],v247,s[44:47],s52 offen offset:1024 sc1
mm v[112:115],v[192:195],a[92:95],v[112:115]
v_perm_b32 v237, v235, v234, s87
aw a139,v236
mm v[112:115],v[196:199],a[76:79],v[112:115]
rt v[192:193],v240 offset:33792
rt v[194:195],v240 offset:34816
mm v[116:119],v[204:207],a[76:79],v[116:119]
s_add_u32 m0, s83, 50688
aw a143,v237
mm v[116:119],v[200:203],a[92:95],v[116:119]
ld v224,s[56:59],s80 offen lds
mm v[84:87],v[200:203],a[88:91],v[84:87]
ar v230,a162
ar v231,a166
mm v[84:87],v[204:207],a[60:63],v[84:87]
rt v[196:197],v240 offset:42240
rt v[198:199],v240 offset:43264
mm v[52:55],v[204:207],a[56:59],v[52:55]
s_add_u32 m0, s83, 51712
v_perm_b32 v232, v231, v230, s86
mm v[52:55],v[200:203],a[84:87],v[52:55]
ld v225,s[56:59],s80 offen lds
mm v[20:23],v[200:203],a[80:83],v[20:23]
wt lgkmcnt(4)
v_perm_b32 v233, v231, v230, s87
mm v[20:23],v[204:207],a[52:55],v[20:23]
rt v[200:201],v241 offset:33792
rt v[202:203],v241 offset:34816
mm v[24:27],v[212:215],a[52:55],v[24:27]
s_add_u32 m0, s83, 52864
aw a144,v232
mm v[24:27],v[208:211],a[80:83],v[24:27]
ld v226,s[56:59],s80 offen lds
mm v[56:59],v[208:211],a[84:87],v[56:59]
aw a148,v233
ar v234,a170
mm v[56:59],v[212:215],a[56:59],v[56:59]
rt v[204:205],v241 offset:42240
rt v[206:207],v241 offset:43264
mm v[88:91],v[212:215],a[60:63],v[88:91]
ar v235,a174
v_perm_b32 v236, v235, v234, s86
mm v[88:91],v[208:211],a[88:91],v[88:91]
ld v[136:139],v244,s[44:47],s52 offen offset:2048 sc1
mm v[120:123],v[208:211],a[92:95],v[120:123]
v_perm_b32 v237, v235, v234, s87
aw a145,v236
mm v[120:123],v[212:215],a[76:79],v[120:123]
rt v[208:209],v242 offset:33792
rt v[210:211],v242 offset:34816
mm v[124:127],v[220:223],a[76:79],v[124:127]
aw a149,v237
ar v230,a178
mm v[124:127],v[216:219],a[92:95],v[124:127]
ld v[140:143],v245,s[44:47],s52 offen offset:2048 sc1
mm v[92:95],v[216:219],a[88:91],v[92:95]
ar v231,a182
v_perm_b32 v232, v231, v230, s86
mm v[92:95],v[220:223],a[60:63],v[92:95]
rt v[212:213],v242 offset:42240
rt v[214:215],v242 offset:43264
mm v[60:63],v[220:223],a[56:59],v[60:63]
v_perm_b32 v233, v231, v230, s87
aw a146,v232
mm v[60:63],v[216:219],a[84:87],v[60:63]
aw a150,v233
ar v234,a186
mm v[28:31],v[216:219],a[80:83],v[28:31]
wt vmcnt(18) lgkmcnt(4)
ar v235,a190
mm v[28:31],v[220:223],a[52:55],v[28:31]
rt v[216:217],v243 offset:33792
rt v[218:219],v243 offset:34816
mm v[0:3],v[192:195],a[0:3],v[0:3]
v_perm_b32 v236, v235, v234, s86
v_perm_b32 v237, v235, v234, s87
mm v[0:3],v[196:199],a[16:19],v[0:3]
ld v[152:155],v246,s[44:47],s52 offen offset:2048 sc1
mm v[32:35],v[196:199],a[20:23],v[32:35]
aw a147,v236
aw a151,v237
mm v[32:35],v[192:195],a[4:7],v[32:35]
rt v[220:221],v243 offset:42240
rt v[222:223],v243 offset:43264
mm v[64:67],v[192:195],a[8:11],v[64:67]
ar v230,a163
ar v231,a167
mm v[64:67],v[196:199],a[24:27],v[64:67]
ld v[156:159],v247,s[44:47],s52 offen offset:2048 sc1
mm v[96:99],v[196:199],a[28:31],v[96:99]
v_perm_b32 v232, v231, v230, s86
v_perm_b32 v233, v231, v230, s87
mm v[96:99],v[192:195],a[12:15],v[96:99]
rt v[196:197],v240 offset:33920
rt v[198:199],v240 offset:34944
mm v[100:103],v[200:203],a[12:15],v[100:103]
aw a152,v232
aw a156,v233
mm v[100:103],v[204:207],a[28:31],v[100:103]
ld v[168:171],v244,s[44:47],s52 offen offset:3072 sc1
mm v[68:71],v[204:207],a[24:27],v[68:71]
ar v234,a171
ar v235,a175
mm v[68:71],v[200:203],a[8:11],v[68:71]
rt v[192:193],v240 offset:42368
rt v[194:195],v240 offset:43392
mm v[36:39],v[200:203],a[4:7],v[36:39]
v_add_u32 v240, 4294899712, v240
v_perm_b32 v236, v235, v234, s86
mm v[36:39],v[204:207],a[20:23],v[36:39]
ld v[172:175],v245,s[44:47],s52 offen offset:3072 sc1
mm v[4:7],v[204:207],a[16:19],v[4:7]
wt lgkmcnt(4)
v_perm_b32 v237, v235, v234, s87
mm v[4:7],v[200:203],a[0:3],v[4:7]
rt v[204:205],v241 offset:33920
rt v[206:207],v241 offset:34944
mm v[8:11],v[208:211],a[0:3],v[8:11]
s_nop 0
v_cmp_eq_u64 vcc, v[250:251], v[252:253]
mm v[8:11],v[212:215],a[16:19],v[8:11]
ld v[184:187],v246,s[44:47],s52 offen offset:3072 sc1
mm v[40:43],v[212:215],a[20:23],v[40:43]
aw a153,v236
aw a157,v237
mm v[40:43],v[208:211],a[4:7],v[40:43]
rt v[200:201],v241 offset:42368
rt v[202:203],v241 offset:43392
mm v[72:75],v[208:211],a[8:11],v[72:75]
v_add_u32 v241, 4294899712, v241
ar v230,a179
mm v[72:75],v[212:215],a[24:27],v[72:75]
ld v[188:191],v247,s[44:47],s52 offen offset:3072 sc1
mm v[104:107],v[212:215],a[28:31],v[104:107]
s_bfe_u32 s52, s4, 262172
s_xor_b32 s52, s52, s72
mm v[104:107],v[208:211],a[12:15],v[104:107]
rt v[212:213],v242 offset:33920
rt v[214:215],v242 offset:34944
mm v[108:111],v[216:219],a[12:15],v[108:111]
s_lshl_b32 s52, s52, 13
ar v231,a183
mm v[108:111],v[220:223],a[28:31],v[108:111]
buffer_load_dwordx2 v[250:251], v248, s[64:67], s5 offen sc1
mm v[76:79],v[220:223],a[24:27],v[76:79]
v_perm_b32 v232, v231, v230, s86
v_perm_b32 v233, v231, v230, s87
mm v[76:79],v[216:219],a[8:11],v[76:79]
rt v[208:209],v242 offset:42368
rt v[210:211],v242 offset:43392
mm v[44:47],v[216:219],a[4:7],v[44:47]
s_add_u32 m0, s83, 53888
v_add_u32 v242, 4294899712, v242
mm v[44:47],v[220:223],a[20:23],v[44:47]
ld v227,s[56:59],s80 offen lds
mm v[12:15],v[220:223],a[16:19],v[12:15]
wt lgkmcnt(4)
s_add_u32 s80, s80, 131072
mm v[12:15],v[216:219],a[0:3],v[12:15]
rt v[220:221],v243 offset:33920
rt v[222:223],v243 offset:34944
mm v[16:19],v[196:199],a[0:3],v[16:19]
s_add_u32 m0, s83, 0
aw a154,v232
mm v[16:19],v[192:195],a[16:19],v[16:19]
ld v224,s[56:59],s80 offen lds
mm v[48:51],v[192:195],a[20:23],v[48:51]
aw a158,v233
ar v234,a187
mm v[48:51],v[196:199],a[4:7],v[48:51]
rt v[216:217],v243 offset:42368
rt v[218:219],v243 offset:43392
mm v[80:83],v[196:199],a[8:11],v[80:83]
s_add_u32 m0, s83, 1024
v_add_u32 v243, 4294899712, v243
mm v[80:83],v[192:195],a[24:27],v[80:83]
ld v225,s[56:59],s80 offen lds
mm v[112:115],v[192:195],a[28:31],v[112:115]
wt vmcnt(23) lgkmcnt(0)
s_barrier
mm v[112:115],v[196:199],a[12:15],v[112:115]
rt v[192:193],v240 offset:16896
rt v[194:195],v240 offset:17920
mm v[116:119],v[204:207],a[12:15],v[116:119]
s_add_u32 m0, s83, 2176
ar v235,a191
mm v[116:119],v[200:203],a[28:31],v[116:119]
ld v226,s[56:59],s80 offen lds
mm v[84:87],v[200:203],a[24:27],v[84:87]
v_perm_b32 v236, v235, v234, s86
v_perm_b32 v237, v235, v234, s87
mm v[84:87],v[204:207],a[8:11],v[84:87]
rt v[196:197],v240 offset:25344
rt v[198:199],v240 offset:26368
mm v[52:55],v[204:207],a[4:7],v[52:55]
s_add_u32 m0, s83, 3200
aw a155,v236
mm v[52:55],v[200:203],a[20:23],v[52:55]
ld v227,s[56:59],s80 offen lds
mm v[20:23],v[200:203],a[16:19],v[20:23]
s_nop 0
s_xor_b32 s80, s72, 25
mm v[20:23],v[204:207],a[0:3],v[20:23]
rt v[200:201],v241 offset:16896
rt v[202:203],v241 offset:17920
mm v[24:27],v[212:215],a[0:3],v[24:27]
s_bitcmp1_b32 vcc_lo, 5
s_cbranch_scc0 .La5_LO_NB5
.La5_LO_KB5:
mm v[24:27],v[208:211],a[16:19],v[24:27]
ld a[52:55],v244,s[44:47],s53 offen offset:0 sc1
mm v[56:59],v[208:211],a[20:23],v[56:59]
s_lshl_b32 s80, s80, 18
s_add_u32 s80, s80, 131072
mm v[56:59],v[212:215],a[4:7],v[56:59]
rt v[204:205],v241 offset:25344
rt v[206:207],v241 offset:26368
mm v[88:91],v[212:215],a[8:11],v[88:91]
aw a159,v237
ar v230,a192
mm v[88:91],v[208:211],a[24:27],v[88:91]
ld a[56:59],v245,s[44:47],s53 offen offset:0 sc1
mm v[120:123],v[208:211],a[28:31],v[120:123]
ar v231,a196
v_perm_b32 v232, v231, v230, s86
mm v[120:123],v[212:215],a[12:15],v[120:123]
rt v[208:209],v242 offset:16896
rt v[210:211],v242 offset:17920
mm v[124:127],v[220:223],a[12:15],v[124:127]
v_perm_b32 v233, v231, v230, s87
aw a160,v232
mm v[124:127],v[216:219],a[28:31],v[124:127]
ld a[12:15],v246,s[44:47],s53 offen offset:0 sc1
mm v[92:95],v[216:219],a[24:27],v[92:95]
aw a164,v233
ar v234,a200
mm v[92:95],v[220:223],a[8:11],v[92:95]
rt v[212:213],v242 offset:25344
rt v[214:215],v242 offset:26368
mm v[60:63],v[220:223],a[4:7],v[60:63]
ar v235,a204
v_perm_b32 v236, v235, v234, s86
mm v[60:63],v[216:219],a[20:23],v[60:63]
v_perm_b32 v237, v235, v234, s87
aw a161,v236
mm v[28:31],v[216:219],a[16:19],v[28:31]
wt vmcnt(20) lgkmcnt(4)
aw a165,v237
mm v[28:31],v[220:223],a[0:3],v[28:31]
rt v[216:217],v243 offset:16896
rt v[218:219],v243 offset:17920
mm v[0:3],v[192:195],a[32:35],v[0:3]
ar v230,a208
ar v231,a212
mm v[0:3],v[196:199],a[48:51],v[0:3]
ld a[0:3],v247,s[44:47],s53 offen offset:0 sc1
mm v[32:35],v[196:199],a[64:67],v[32:35]
v_perm_b32 v232, v231, v230, s86
v_perm_b32 v233, v231, v230, s87
mm v[32:35],v[192:195],a[36:39],v[32:35]
rt v[220:221],v243 offset:25344
rt v[222:223],v243 offset:26368
mm v[64:67],v[192:195],a[40:43],v[64:67]
aw a162,v232
aw a166,v233
mm v[64:67],v[196:199],a[68:71],v[64:67]
ld a[4:7],v244,s[44:47],s53 offen offset:1024 sc1
mm v[96:99],v[196:199],a[72:75],v[96:99]
ar v234,a216
ar v235,a220
mm v[96:99],v[192:195],a[44:47],v[96:99]
rt v[196:197],v240 offset:17024
rt v[198:199],v240 offset:18048
mm v[100:103],v[200:203],a[44:47],v[100:103]
v_perm_b32 v236, v235, v234, s86
v_perm_b32 v237, v235, v234, s87
mm v[100:103],v[204:207],a[72:75],v[100:103]
ld a[8:11],v245,s[44:47],s53 offen offset:1024 sc1
mm v[68:71],v[204:207],a[68:71],v[68:71]
aw a163,v236
aw a167,v237
mm v[68:71],v[200:203],a[40:43],v[68:71]
rt v[192:193],v240 offset:25472
rt v[194:195],v240 offset:26496
mm v[36:39],v[200:203],a[36:39],v[36:39]
ar v230,a193
ar v231,a197
mm v[36:39],v[204:207],a[64:67],v[36:39]
ld a[16:19],v246,s[44:47],s53 offen offset:1024 sc1
mm v[4:7],v[204:207],a[48:51],v[4:7]
wt lgkmcnt(4)
v_perm_b32 v232, v231, v230, s86
mm v[4:7],v[200:203],a[32:35],v[4:7]
rt v[204:205],v241 offset:17024
rt v[206:207],v241 offset:18048
mm v[8:11],v[208:211],a[32:35],v[8:11]
v_perm_b32 v233, v231, v230, s87
.La5_LW3_2:
s_cbranch_execz .La5_LW3_6
aw a168,v232
mm v[8:11],v[212:215],a[48:51],v[8:11]
ld a[20:23],v247,s[44:47],s53 offen offset:1024 sc1
mm v[40:43],v[212:215],a[64:67],v[40:43]
aw a172,v233
ar v234,a201
mm v[40:43],v[208:211],a[36:39],v[40:43]
rt v[200:201],v241 offset:25472
rt v[202:203],v241 offset:26496
mm v[72:75],v[208:211],a[40:43],v[72:75]
ar v235,a205
v_perm_b32 v236, v235, v234, s86
mm v[72:75],v[212:215],a[68:71],v[72:75]
v_perm_b32 v237, v235, v234, s87
aw a169,v236
mm v[104:107],v[212:215],a[72:75],v[104:107]
aw a173,v237
ar v230,a209
mm v[104:107],v[208:211],a[44:47],v[104:107]
rt v[212:213],v242 offset:17024
rt v[214:215],v242 offset:18048
mm v[108:111],v[216:219],a[44:47],v[108:111]
ar v231,a213
v_perm_b32 v232, v231, v230, s86
mm v[108:111],v[220:223],a[72:75],v[108:111]
v_perm_b32 v233, v231, v230, s87
aw a170,v232
mm v[76:79],v[220:223],a[68:71],v[76:79]
aw a174,v233
ar v234,a217
mm v[76:79],v[216:219],a[40:43],v[76:79]
rt v[208:209],v242 offset:25472
rt v[210:211],v242 offset:26496
mm v[44:47],v[216:219],a[36:39],v[44:47]
ar v235,a221
v_perm_b32 v236, v235, v234, s86
mm v[44:47],v[220:223],a[64:67],v[44:47]
v_perm_b32 v237, v235, v234, s87
aw a171,v236
mm v[12:15],v[220:223],a[48:51],v[12:15]
wt lgkmcnt(4)
aw a175,v237
mm v[12:15],v[216:219],a[32:35],v[12:15]
rt v[220:221],v243 offset:17024
rt v[222:223],v243 offset:18048
mm v[16:19],v[196:199],a[32:35],v[16:19]
ar v230,a194
ar v231,a198
mm v[16:19],v[192:195],a[48:51],v[16:19]
ld a[24:27],v244,s[44:47],s53 offen offset:2048 sc1
mm v[48:51],v[192:195],a[64:67],v[48:51]
v_perm_b32 v232, v231, v230, s86
v_perm_b32 v233, v231, v230, s87
mm v[48:51],v[196:199],a[36:39],v[48:51]
rt v[216:217],v243 offset:25472
rt v[218:219],v243 offset:26496
mm v[80:83],v[196:199],a[40:43],v[80:83]
aw a176,v232
aw a180,v233
mm v[80:83],v[192:195],a[68:71],v[80:83]
ld a[28:31],v245,s[44:47],s53 offen offset:2048 sc1
mm v[112:115],v[192:195],a[72:75],v[112:115]
ar v234,a202
ar v235,a206
mm v[112:115],v[196:199],a[44:47],v[112:115]
rt v[192:193],v240 offset:33792
rt v[194:195],v240 offset:34816
mm v[116:119],v[204:207],a[44:47],v[116:119]
v_perm_b32 v236, v235, v234, s86
v_perm_b32 v237, v235, v234, s87
mm v[116:119],v[200:203],a[72:75],v[116:119]
ld a[60:63],v246,s[44:47],s53 offen offset:2048 sc1
mm v[84:87],v[200:203],a[68:71],v[84:87]
aw a177,v236
aw a181,v237
mm v[84:87],v[204:207],a[40:43],v[84:87]
rt v[196:197],v240 offset:42240
rt v[198:199],v240 offset:43264
mm v[52:55],v[204:207],a[36:39],v[52:55]
ar v230,a210
ar v231,a214
mm v[52:55],v[200:203],a[64:67],v[52:55]
ld v[128:131],v247,s[44:47],s53 offen offset:2048 sc1
mm v[20:23],v[200:203],a[48:51],v[20:23]
wt lgkmcnt(4)
v_perm_b32 v232, v231, v230, s86
mm v[20:23],v[204:207],a[32:35],v[20:23]
rt v[200:201],v241 offset:33792
rt v[202:203],v241 offset:34816
mm v[24:27],v[212:215],a[32:35],v[24:27]
v_perm_b32 v233, v231, v230, s87
aw a178,v232
mm v[24:27],v[208:211],a[48:51],v[24:27]
ld v[132:135],v244,s[44:47],s53 offen offset:3072 sc1
mm v[56:59],v[208:211],a[64:67],v[56:59]
aw a182,v233
ar v234,a218
mm v[56:59],v[212:215],a[36:39],v[56:59]
rt v[204:205],v241 offset:42240
rt v[206:207],v241 offset:43264
mm v[88:91],v[212:215],a[40:43],v[88:91]
ar v235,a222
v_perm_b32 v236, v235, v234, s86
mm v[88:91],v[208:211],a[68:71],v[88:91]
ld v[144:147],v245,s[44:47],s53 offen offset:3072 sc1
mm v[120:123],v[208:211],a[72:75],v[120:123]
v_perm_b32 v237, v235, v234, s87
aw a179,v236
mm v[120:123],v[212:215],a[44:47],v[120:123]
rt v[208:209],v242 offset:33792
rt v[210:211],v242 offset:34816
mm v[124:127],v[220:223],a[44:47],v[124:127]
aw a183,v237
ar v230,a195
mm v[124:127],v[216:219],a[72:75],v[124:127]
ld a[44:47],v246,s[44:47],s53 offen offset:3072 sc1
mm v[92:95],v[216:219],a[68:71],v[92:95]
ar v231,a199
v_perm_b32 v232, v231, v230, s86
mm v[92:95],v[220:223],a[40:43],v[92:95]
rt v[212:213],v242 offset:42240
rt v[214:215],v242 offset:43264
mm v[60:63],v[220:223],a[36:39],v[60:63]
v_perm_b32 v233, v231, v230, s87
aw a184,v232
mm v[60:63],v[216:219],a[64:67],v[60:63]
wt vmcnt(20)
v_cmp_eq_u64 vcc, v[250:251], v[252:253]
mm v[28:31],v[216:219],a[48:51],v[28:31]
wt lgkmcnt(4)
aw a188,v233
mm v[28:31],v[220:223],a[32:35],v[28:31]
rt v[216:217],v243 offset:33792
rt v[218:219],v243 offset:34816
mm v[0:3],v[192:195],v[136:139],v[0:3]
ar v234,a203
ar v235,a207
mm v[0:3],v[196:199],v[168:171],v[0:3]
ld a[32:35],v247,s[44:47],s53 offen offset:3072 sc1
mm v[32:35],v[196:199],v[172:175],v[32:35]
v_perm_b32 v236, v235, v234, s86
v_perm_b32 v237, v235, v234, s87
mm v[32:35],v[192:195],v[140:143],v[32:35]
rt v[220:221],v243 offset:42240
rt v[222:223],v243 offset:43264
mm v[64:67],v[192:195],v[152:155],v[64:67]
aw a185,v236
aw a189,v237
mm v[64:67],v[196:199],v[184:187],v[64:67]
ar v230,a211
ar v231,a215
mm v[96:99],v[196:199],v[188:191],v[96:99]
v_perm_b32 v232, v231, v230, s86
v_perm_b32 v233, v231, v230, s87
mm v[96:99],v[192:195],v[156:159],v[96:99]
rt v[196:197],v240 offset:33920
rt v[198:199],v240 offset:34944
mm v[100:103],v[200:203],v[156:159],v[100:103]
aw a186,v232
aw a190,v233
mm v[100:103],v[204:207],v[188:191],v[100:103]
buffer_load_dwordx2 v[250:251], v248, s[64:67], s5 offen sc1
mm v[68:71],v[204:207],v[184:187],v[68:71]
ar v234,a219
ar v235,a223
mm v[68:71],v[200:203],v[152:155],v[68:71]
rt v[192:193],v240 offset:42368
rt v[194:195],v240 offset:43392
mm v[36:39],v[200:203],v[140:143],v[36:39]
v_perm_b32 v236, v235, v234, s86
v_perm_b32 v237, v235, v234, s87
mm v[36:39],v[204:207],v[172:175],v[36:39]
aw a187,v236
aw a191,v237
mm v[4:7],v[204:207],v[168:171],v[4:7]
wt lgkmcnt(4)
ar v230,a224
mm v[4:7],v[200:203],v[136:139],v[4:7]
rt v[204:205],v241 offset:33920
rt v[206:207],v241 offset:34944
mm v[8:11],v[208:211],v[136:139],v[8:11]
ar v231,a228
v_perm_b32 v232, v231, v230, s86
mm v[8:11],v[212:215],v[168:171],v[8:11]
v_perm_b32 v233, v231, v230, s87
aw a192,v232
mm v[40:43],v[212:215],v[172:175],v[40:43]
aw a196,v233
ar v234,a232
mm v[40:43],v[208:211],v[140:143],v[40:43]
rt v[200:201],v241 offset:42368
rt v[202:203],v241 offset:43392
mm v[72:75],v[208:211],v[152:155],v[72:75]
ar v235,a236
v_perm_b32 v236, v235, v234, s86
mm v[72:75],v[212:215],v[184:187],v[72:75]
v_perm_b32 v237, v235, v234, s87
aw a193,v236
mm v[104:107],v[212:215],v[188:191],v[104:107]
aw a197,v237
ar v230,a240
mm v[104:107],v[208:211],v[156:159],v[104:107]
rt v[212:213],v242 offset:33920
rt v[214:215],v242 offset:34944
mm v[108:111],v[216:219],v[156:159],v[108:111]
ar v231,a244
v_perm_b32 v232, v231, v230, s86
mm v[108:111],v[220:223],v[188:191],v[108:111]
v_perm_b32 v233, v231, v230, s87
aw a194,v232
mm v[76:79],v[220:223],v[184:187],v[76:79]
aw a198,v233
ar v234,a248
mm v[76:79],v[216:219],v[152:155],v[76:79]
rt v[208:209],v242 offset:42368
rt v[210:211],v242 offset:43392
mm v[44:47],v[216:219],v[140:143],v[44:47]
s_bitcmp1_b32 vcc_lo, 8
s_nop 0
s_nop 0
s_cbranch_scc0 .La5_LO_NB8
.La5_LO_KB8:
mm v[44:47],v[220:223],v[172:175],v[44:47]
ar v235,a252
v_perm_b32 v236, v235, v234, s86
mm v[12:15],v[220:223],v[168:171],v[12:15]
wt lgkmcnt(4)
v_perm_b32 v237, v235, v234, s87
mm v[12:15],v[216:219],v[136:139],v[12:15]
rt v[220:221],v243 offset:33920
rt v[222:223],v243 offset:34944
mm v[16:19],v[196:199],v[136:139],v[16:19]
aw a195,v236
aw a199,v237
mm v[16:19],v[192:195],v[168:171],v[16:19]
ld a[36:39],v244,s[44:47],s54 offen offset:2048 sc1
mm v[48:51],v[192:195],v[172:175],v[48:51]
ar v230,a225
ar v231,a229
mm v[48:51],v[196:199],v[140:143],v[48:51]
rt v[216:217],v243 offset:42368
rt v[218:219],v243 offset:43392
mm v[80:83],v[196:199],v[152:155],v[80:83]
v_perm_b32 v232, v231, v230, s86
v_perm_b32 v233, v231, v230, s87
mm v[80:83],v[192:195],v[184:187],v[80:83]
ld a[40:43],v245,s[44:47],s54 offen offset:2048 sc1
mm v[112:115],v[192:195],v[188:191],v[112:115]
wt vmcnt(19)
s_barrier
mm v[112:115],v[196:199],v[156:159],v[112:115]
rt v[192:193],v240 offset:50688
rt v[194:195],v240 offset:51712
mm v[116:119],v[204:207],v[156:159],v[116:119]
s_add_u32 m0, s83, 16896
aw a200,v232
mm v[116:119],v[200:203],v[188:191],v[116:119]
ld v224,s[56:59],s80 offen lds
mm v[84:87],v[200:203],v[184:187],v[84:87]
aw a204,v233
ar v234,a233
mm v[84:87],v[204:207],v[152:155],v[84:87]
rt v[196:197],v240 offset:59136
rt v[198:199],v240 offset:60160
mm v[52:55],v[204:207],v[140:143],v[52:55]
s_add_u32 m0, s83, 17920
ar v235,a237
mm v[52:55],v[200:203],v[172:175],v[52:55]
ld v225,s[56:59],s80 offen lds
mm v[20:23],v[200:203],v[168:171],v[20:23]
wt lgkmcnt(4)
v_perm_b32 v236, v235, v234, s86
mm v[20:23],v[204:207],v[136:139],v[20:23]
rt v[200:201],v241 offset:50688
rt v[202:203],v241 offset:51712
mm v[24:27],v[212:215],v[136:139],v[24:27]
s_add_u32 m0, s83, 19072
v_perm_b32 v237, v235, v234, s87
mm v[24:27],v[208:211],v[168:171],v[24:27]
ld v226,s[56:59],s80 offen lds
mm v[56:59],v[208:211],v[172:175],v[56:59]
aw a201,v236
aw a205,v237
mm v[56:59],v[212:215],v[140:143],v[56:59]
rt v[204:205],v241 offset:59136
rt v[206:207],v241 offset:60160
mm v[88:91],v[212:215],v[152:155],v[88:91]
s_add_u32 m0, s83, 20096
ar v230,a241
mm v[88:91],v[208:211],v[184:187],v[88:91]
ld v227,s[56:59],s80 offen lds
mm v[120:123],v[208:211],v[188:191],v[120:123]
s_bfe_u32 s80, s4, 262168
s_xor_b32 s80, s80, s72
mm v[120:123],v[212:215],v[156:159],v[120:123]
rt v[208:209],v242 offset:50688
rt v[210:211],v242 offset:51712
mm v[124:127],v[220:223],v[156:159],v[124:127]
s_add_u32 m0, s83, 118272
s_lshl_b32 s80, s80, 18
mm v[124:127],v[216:219],v[188:191],v[124:127]
ld v224,s[56:59],s80 offen lds
mm v[92:95],v[216:219],v[184:187],v[92:95]
ar v231,a245
v_perm_b32 v232, v231, v230, s86
mm v[92:95],v[220:223],v[152:155],v[92:95]
rt v[212:213],v242 offset:59136
rt v[214:215],v242 offset:60160
mm v[60:63],v[220:223],v[140:143],v[60:63]
v_perm_b32 v233, v231, v230, s87
aw a202,v232
mm v[60:63],v[216:219],v[172:175],v[60:63]
aw a206,v233
ar v234,a249
mm v[28:31],v[216:219],v[168:171],v[28:31]
wt vmcnt(16) lgkmcnt(4)
ar v235,a253
mm v[28:31],v[220:223],v[136:139],v[28:31]
rt v[216:217],v243 offset:50688
rt v[218:219],v243 offset:51712
mm v[0:3],v[192:195],a[52:55],v[0:3]
s_add_u32 m0, s83, 119296
v_perm_b32 v236, v235, v234, s86
mm v[0:3],v[196:199],a[4:7],v[0:3]
ld v225,s[56:59],s80 offen lds
mm v[32:35],v[196:199],a[8:11],v[32:35]
v_perm_b32 v237, v235, v234, s87
aw a203,v236
mm v[32:35],v[192:195],a[56:59],v[32:35]
rt v[220:221],v243 offset:59136
rt v[222:223],v243 offset:60160
mm v[64:67],v[192:195],a[12:15],v[64:67]
s_add_u32 m0, s83, 120448
aw a207,v237
mm v[64:67],v[196:199],a[16:19],v[64:67]
ld v226,s[56:59],s80 offen lds
mm v[96:99],v[196:199],a[20:23],v[96:99]
ar v230,a226
ar v231,a230
mm v[96:99],v[192:195],a[0:3],v[96:99]
rt v[196:197],v240 offset:50816
rt v[198:199],v240 offset:51840
mm v[100:103],v[200:203],a[0:3],v[100:103]
v_perm_b32 v232, v231, v230, s86
v_perm_b32 v233, v231, v230, s87
mm v[100:103],v[204:207],a[20:23],v[100:103]
ld v[136:139],v246,s[44:47],s54 offen offset:2048 sc1
mm v[68:71],v[204:207],a[16:19],v[68:71]
aw a208,v232
aw a212,v233
mm v[68:71],v[200:203],a[12:15],v[68:71]
rt v[192:193],v240 offset:59264
rt v[194:195],v240 offset:60288
mm v[36:39],v[200:203],a[56:59],v[36:39]
ar v234,a234
ar v235,a238
mm v[36:39],v[204:207],a[8:11],v[36:39]
ld v[140:143],v247,s[44:47],s54 offen offset:2048 sc1
mm v[4:7],v[204:207],a[4:7],v[4:7]
wt lgkmcnt(4)
v_perm_b32 v236, v235, v234, s86
mm v[4:7],v[200:203],a[52:55],v[4:7]
rt v[204:205],v241 offset:50816
rt v[206:207],v241 offset:51840
mm v[8:11],v[208:211],a[52:55],v[8:11]
v_perm_b32 v237, v235, v234, s87
aw a209,v236
mm v[8:11],v[212:215],a[4:7],v[8:11]
ld v[152:155],v244,s[44:47],s54 offen offset:3072 sc1
mm v[40:43],v[212:215],a[8:11],v[40:43]
aw a213,v237
ar v230,a242
mm v[40:43],v[208:211],a[56:59],v[40:43]
rt v[200:201],v241 offset:59264
rt v[202:203],v241 offset:60288
mm v[72:75],v[208:211],a[12:15],v[72:75]
ar v231,a246
v_perm_b32 v232, v231, v230, s86
mm v[72:75],v[212:215],a[16:19],v[72:75]
ld v[156:159],v245,s[44:47],s54 offen offset:3072 sc1
mm v[104:107],v[212:215],a[20:23],v[104:107]
v_perm_b32 v233, v231, v230, s87
aw a210,v232
mm v[104:107],v[208:211],a[0:3],v[104:107]
rt v[212:213],v242 offset:50816
rt v[214:215],v242 offset:51840
mm v[108:111],v[216:219],a[0:3],v[108:111]
aw a214,v233
ar v234,a250
mm v[108:111],v[220:223],a[20:23],v[108:111]
ld v[168:171],v246,s[44:47],s54 offen offset:3072 sc1
mm v[76:79],v[220:223],a[16:19],v[76:79]
ar v235,a254
v_perm_b32 v236, v235, v234, s86
mm v[76:79],v[216:219],a[12:15],v[76:79]
rt v[208:209],v242 offset:59264
rt v[210:211],v242 offset:60288
mm v[44:47],v[216:219],a[56:59],v[44:47]
v_perm_b32 v237, v235, v234, s87
aw a211,v236
mm v[44:47],v[220:223],a[8:11],v[44:47]
aw a215,v237
ar v230,a227
mm v[12:15],v[220:223],a[4:7],v[12:15]
wt lgkmcnt(4)
ar v231,a231
mm v[12:15],v[216:219],a[52:55],v[12:15]
rt v[220:221],v243 offset:50816
rt v[222:223],v243 offset:51840
mm v[16:19],v[196:199],a[52:55],v[16:19]
v_perm_b32 v232, v231, v230, s86
v_perm_b32 v233, v231, v230, s87
mm v[16:19],v[192:195],a[4:7],v[16:19]
ld v[172:175],v247,s[44:47],s54 offen offset:3072 sc1
mm v[48:51],v[192:195],a[8:11],v[48:51]
aw a216,v232
aw a220,v233
mm v[48:51],v[196:199],a[56:59],v[48:51]
rt v[216:217],v243 offset:59264
rt v[218:219],v243 offset:60288
mm v[80:83],v[196:199],a[12:15],v[80:83]
s_add_u32 m0, s83, 121472
ar v234,a235
mm v[80:83],v[192:195],a[16:19],v[80:83]
ld v227,s[56:59],s80 offen lds
mm v[112:115],v[192:195],a[20:23],v[112:115]
s_add_u32 s80, s80, 131072
ar v235,a239
mm v[112:115],v[196:199],a[0:3],v[112:115]
rt v[192:193],v240 offset:0
rt v[194:195],v240 offset:1024
mm v[116:119],v[204:207],a[0:3],v[116:119]
s_add_u32 m0, s83, 67584
v_perm_b32 v236, v235, v234, s86
mm v[116:119],v[200:203],a[20:23],v[116:119]
ld v224,s[56:59],s80 offen lds
mm v[84:87],v[200:203],a[16:19],v[84:87]
v_perm_b32 v237, v235, v234, s87
aw a217,v236
mm v[84:87],v[204:207],a[12:15],v[84:87]
rt v[196:197],v240 offset:8448
rt v[198:199],v240 offset:9472
mm v[52:55],v[204:207],a[56:59],v[52:55]
s_add_u32 m0, s83, 68608
aw a221,v237
mm v[52:55],v[200:203],a[8:11],v[52:55]
ld v225,s[56:59],s80 offen lds
mm v[20:23],v[200:203],a[4:7],v[20:23]
wt lgkmcnt(4)
ar v230,a243
mm v[20:23],v[204:207],a[52:55],v[20:23]
rt v[200:201],v241 offset:0
rt v[202:203],v241 offset:1024
mm v[24:27],v[212:215],a[52:55],v[24:27]
s_add_u32 m0, s83, 69760
ar v231,a247
mm v[24:27],v[208:211],a[4:7],v[24:27]
ld v226,s[56:59],s80 offen lds
wt vmcnt(19)
v_cmp_eq_u64 vcc, v[250:251], v[252:253]
mm v[56:59],v[208:211],a[8:11],v[56:59]
v_perm_b32 v232, v231, v230, s86
v_perm_b32 v233, v231, v230, s87
mm v[56:59],v[212:215],a[56:59],v[56:59]
rt v[204:205],v241 offset:8448
rt v[206:207],v241 offset:9472
mm v[88:91],v[212:215],a[12:15],v[88:91]
s_add_u32 m0, s83, 70784
aw a218,v232
mm v[88:91],v[208:211],a[16:19],v[88:91]
ld v227,s[56:59],s80 offen lds
mm v[120:123],v[208:211],a[20:23],v[120:123]
s_bfe_u32 s80, s4, 262172
s_xor_b32 s80, s80, s72
mm v[120:123],v[212:215],a[0:3],v[120:123]
rt v[208:209],v242 offset:0
rt v[210:211],v242 offset:1024
mm v[124:127],v[220:223],a[0:3],v[124:127]
s_lshl_b32 s80, s80, 18
aw a222,v233
mm v[124:127],v[216:219],a[20:23],v[124:127]
buffer_load_dwordx2 v[250:251], v248, s[64:67], s5 offen sc1
mm v[92:95],v[216:219],a[16:19],v[92:95]
ar v234,a251
ar v235,a255
mm v[92:95],v[220:223],a[12:15],v[92:95]
rt v[212:213],v242 offset:8448
rt v[214:215],v242 offset:9472
mm v[60:63],v[220:223],a[56:59],v[60:63]
s_add_u32 m0, s83, 84480
v_perm_b32 v236, v235, v234, s86
mm v[60:63],v[216:219],a[8:11],v[60:63]
ld v224,s[56:59],s80 offen lds
mm v[28:31],v[216:219],a[4:7],v[28:31]
wt lgkmcnt(4)
v_perm_b32 v237, v235, v234, s87
mm v[28:31],v[220:223],a[52:55],v[28:31]
rt v[216:217],v243 offset:0
rt v[218:219],v243 offset:1024
mm v[0:3],v[192:195],a[24:27],v[0:3]
s_add_u32 m0, s83, 85504
aw a219,v236
mm v[0:3],v[196:199],v[132:135],v[0:3]
ld v225,s[56:59],s80 offen lds
mm v[32:35],v[196:199],v[144:147],v[32:35]
aw a223,v237
mm v[32:35],v[192:195],a[28:31],v[32:35]
rt v[220:221],v243 offset:8448
rt v[222:223],v243 offset:9472
mm v[64:67],v[192:195],a[60:63],v[64:67]
s_add_u32 m0, s83, 86656
mm v[64:67],v[196:199],a[44:47],v[64:67]
ld v226,s[56:59],s80 offen lds
mm v[96:99],v[196:199],a[32:35],v[96:99]
mm v[96:99],v[192:195],v[128:131],v[96:99]
rt v[196:197],v240 offset:128
rt v[198:199],v240 offset:1152
mm v[100:103],v[200:203],v[128:131],v[100:103]
s_add_u32 m0, s83, 87680
mm v[100:103],v[204:207],a[32:35],v[100:103]
ld v227,s[56:59],s80 offen lds
mm v[68:71],v[204:207],a[44:47],v[68:71]
s_add_u32 s80, s80, 131072
mm v[68:71],v[200:203],a[60:63],v[68:71]
rt v[192:193],v240 offset:8576
.La5_LW3_3:
s_cbranch_execz .La5_LA_RETURN
rt v[194:195],v240 offset:9600
mm v[36:39],v[200:203],a[28:31],v[36:39]
s_add_u32 m0, s83, 101376
mm v[36:39],v[204:207],v[144:147],v[36:39]
ld v224,s[56:59],s80 offen lds
mm v[4:7],v[204:207],v[132:135],v[4:7]
wt lgkmcnt(4)
mm v[4:7],v[200:203],a[24:27],v[4:7]
rt v[204:205],v241 offset:128
rt v[206:207],v241 offset:1152
mm v[8:11],v[208:211],a[24:27],v[8:11]
s_add_u32 m0, s83, 102400
mm v[8:11],v[212:215],v[132:135],v[8:11]
ld v225,s[56:59],s80 offen lds
mm v[40:43],v[212:215],v[144:147],v[40:43]
mm v[40:43],v[208:211],a[28:31],v[40:43]
rt v[200:201],v241 offset:8576
rt v[202:203],v241 offset:9600
mm v[72:75],v[208:211],a[60:63],v[72:75]
s_add_u32 m0, s83, 103552
mm v[72:75],v[212:215],a[44:47],v[72:75]
ld v226,s[56:59],s80 offen lds
mm v[104:107],v[212:215],a[32:35],v[104:107]
mm v[104:107],v[208:211],v[128:131],v[104:107]
rt v[212:213],v242 offset:128
rt v[214:215],v242 offset:1152
mm v[108:111],v[216:219],v[128:131],v[108:111]
s_add_u32 m0, s83, 104576
mm v[108:111],v[220:223],a[32:35],v[108:111]
ld v227,s[56:59],s80 offen lds
mm v[76:79],v[220:223],a[44:47],v[76:79]
v_bfe_u32 v224, v255, 6, 1
v_lshrrev_b32 v225, 7, v255
mm v[76:79],v[216:219],a[60:63],v[76:79]
rt v[208:209],v242 offset:8576
rt v[210:211],v242 offset:9600
mm v[44:47],v[216:219],a[28:31],v[44:47]
s_bitcmp1_b32 vcc_lo, 6
s_cbranch_scc0 .La5_LO_NB6
.La5_LO_KB6:
mm v[44:47],v[220:223],v[144:147],v[44:47]
v_and_b32 v226, 48, v255
v_lshl_add_u32 v225, v225, 6, v226
mm v[12:15],v[220:223],v[132:135],v[12:15]
wt lgkmcnt(4)
v_lshl_add_u32 v224, v224, 11, v225
mm v[12:15],v[216:219],a[24:27],v[12:15]
rt v[220:221],v243 offset:128
rt v[222:223],v243 offset:1152
mm v[16:19],v[196:199],a[24:27],v[16:19]
mm v[16:19],v[192:195],v[132:135],v[16:19]
ld a[64:67],v244,s[44:47],s55 offen offset:0 sc1
mm v[48:51],v[192:195],v[144:147],v[48:51]
mm v[48:51],v[196:199],a[28:31],v[48:51]
rt v[216:217],v243 offset:8576
rt v[218:219],v243 offset:9600
mm v[80:83],v[196:199],a[60:63],v[80:83]
mm v[80:83],v[192:195],a[44:47],v[80:83]
ld a[68:71],v245,s[44:47],s55 offen offset:0 sc1
mm v[112:115],v[192:195],a[32:35],v[112:115]
wt vmcnt(25)
s_barrier
mm v[112:115],v[196:199],v[128:131],v[112:115]
rt v[192:193],v240 offset:16896
rt v[194:195],v240 offset:17920
mm v[116:119],v[204:207],v[128:131],v[116:119]
mm v[116:119],v[200:203],a[32:35],v[116:119]
ld a[72:75],v244,s[44:47],s55 offen offset:1024 sc1
mm v[84:87],v[200:203],a[44:47],v[84:87]
mm v[84:87],v[204:207],a[60:63],v[84:87]
rt v[196:197],v240 offset:25344
rt v[198:199],v240 offset:26368
mm v[52:55],v[204:207],a[28:31],v[52:55]
mm v[52:55],v[200:203],v[144:147],v[52:55]
ld a[76:79],v245,s[44:47],s55 offen offset:1024 sc1
mm v[20:23],v[200:203],v[132:135],v[20:23]
wt lgkmcnt(4)
mm v[20:23],v[204:207],a[24:27],v[20:23]
rt v[200:201],v241 offset:16896
rt v[202:203],v241 offset:17920
mm v[24:27],v[212:215],a[24:27],v[24:27]
mm v[24:27],v[208:211],v[132:135],v[24:27]
ld a[80:83],v244,s[44:47],s55 offen offset:2048 sc1
mm v[56:59],v[208:211],v[144:147],v[56:59]
mm v[56:59],v[212:215],a[28:31],v[56:59]
rt v[204:205],v241 offset:25344
rt v[206:207],v241 offset:26368
mm v[88:91],v[212:215],a[60:63],v[88:91]
mm v[88:91],v[208:211],a[44:47],v[88:91]
ld a[84:87],v245,s[44:47],s55 offen offset:2048 sc1
mm v[120:123],v[208:211],a[32:35],v[120:123]
mm v[120:123],v[212:215],v[128:131],v[120:123]
rt v[208:209],v242 offset:16896
rt v[210:211],v242 offset:17920
mm v[124:127],v[220:223],v[128:131],v[124:127]
mm v[124:127],v[216:219],a[32:35],v[124:127]
ld a[88:91],v244,s[44:47],s55 offen offset:3072 sc1
mm v[92:95],v[216:219],a[44:47],v[92:95]
mm v[92:95],v[220:223],a[60:63],v[92:95]
rt v[212:213],v242 offset:25344
rt v[214:215],v242 offset:26368
mm v[60:63],v[220:223],a[28:31],v[60:63]
mm v[60:63],v[216:219],v[144:147],v[60:63]
mm v[28:31],v[216:219],v[132:135],v[28:31]
wt vmcnt(21) lgkmcnt(4)
mm v[28:31],v[220:223],a[24:27],v[28:31]
rt v[216:217],v243 offset:16896
rt v[218:219],v243 offset:17920
mm v[0:3],v[192:195],a[36:39],v[0:3]
s_bitcmp1_b32 vcc_lo, 7
s_cbranch_scc0 .La5_LO_NB7
.La5_LO_KB7:
mm v[0:3],v[196:199],v[152:155],v[0:3]
ld a[92:95],v245,s[44:47],s55 offen offset:3072 sc1
mm v[32:35],v[196:199],v[156:159],v[32:35]
mm v[32:35],v[192:195],a[40:43],v[32:35]
rt v[220:221],v243 offset:25344
rt v[222:223],v243 offset:26368
mm v[64:67],v[192:195],v[136:139],v[64:67]
mm v[64:67],v[196:199],v[168:171],v[64:67]
ld a[0:3],v244,s[44:47],s52 offen offset:0 sc1
mm v[96:99],v[196:199],v[172:175],v[96:99]
mm v[96:99],v[192:195],v[140:143],v[96:99]
rt v[196:197],v240 offset:17024
rt v[198:199],v240 offset:18048
mm v[100:103],v[200:203],v[140:143],v[100:103]
mm v[100:103],v[204:207],v[172:175],v[100:103]
ld a[4:7],v245,s[44:47],s52 offen offset:0 sc1
mm v[68:71],v[204:207],v[168:171],v[68:71]
mm v[68:71],v[200:203],v[136:139],v[68:71]
rt v[192:193],v240 offset:25472
rt v[194:195],v240 offset:26496
mm v[36:39],v[200:203],a[40:43],v[36:39]
v_add_u32 v240, 67584, v240
mm v[36:39],v[204:207],v[156:159],v[36:39]
ld a[8:11],v244,s[44:47],s52 offen offset:1024 sc1
mm v[4:7],v[204:207],v[152:155],v[4:7]
wt lgkmcnt(4)
mm v[4:7],v[200:203],a[36:39],v[4:7]
rt v[204:205],v241 offset:17024
rt v[206:207],v241 offset:18048
mm v[8:11],v[208:211],a[36:39],v[8:11]
mm v[8:11],v[212:215],v[152:155],v[8:11]
ld a[12:15],v245,s[44:47],s52 offen offset:1024 sc1
mm v[40:43],v[212:215],v[156:159],v[40:43]
mm v[40:43],v[208:211],a[40:43],v[40:43]
rt v[200:201],v241 offset:25472
rt v[202:203],v241 offset:26496
mm v[72:75],v[208:211],v[136:139],v[72:75]
v_add_u32 v241, 67584, v241
mm v[72:75],v[212:215],v[168:171],v[72:75]
ld a[16:19],v244,s[44:47],s52 offen offset:2048 sc1
mm v[104:107],v[212:215],v[172:175],v[104:107]
mm v[104:107],v[208:211],v[140:143],v[104:107]
rt v[212:213],v242 offset:17024
rt v[214:215],v242 offset:18048
mm v[108:111],v[216:219],v[140:143],v[108:111]
mm v[108:111],v[220:223],v[172:175],v[108:111]
ld a[20:23],v245,s[44:47],s52 offen offset:2048 sc1
mm v[76:79],v[220:223],v[168:171],v[76:79]
mm v[76:79],v[216:219],v[136:139],v[76:79]
rt v[208:209],v242 offset:25472
rt v[210:211],v242 offset:26496
mm v[44:47],v[216:219],a[40:43],v[44:47]
v_add_u32 v242, 67584, v242
mm v[44:47],v[220:223],v[156:159],v[44:47]
wt vmcnt(22)
v_cmp_eq_u64 vcc, v[250:251], v[252:253]
mm v[12:15],v[220:223],v[152:155],v[12:15]
wt lgkmcnt(4)
mm v[12:15],v[216:219],a[36:39],v[12:15]
rt v[220:221],v243 offset:17024
rt v[222:223],v243 offset:18048
mm v[16:19],v[196:199],a[36:39],v[16:19]
mm v[16:19],v[192:195],v[152:155],v[16:19]
ld a[24:27],v244,s[44:47],s52 offen offset:3072 sc1
mm v[48:51],v[192:195],v[156:159],v[48:51]
mm v[48:51],v[196:199],a[40:43],v[48:51]
rt v[216:217],v243 offset:25472
rt v[218:219],v243 offset:26496
mm v[80:83],v[196:199],v[136:139],v[80:83]
v_add_u32 v243, 67584, v243
mm v[80:83],v[192:195],v[168:171],v[80:83]
ld a[28:31],v245,s[44:47],s52 offen offset:3072 sc1
mm v[112:115],v[192:195],v[172:175],v[112:115]
wt vmcnt(16)
s_barrier
mm v[112:115],v[196:199],v[140:143],v[112:115]
rt v[192:193],v240 offset:50688
rt v[194:195],v240 offset:51712
mm v[116:119],v[204:207],v[140:143],v[116:119]
mm v[116:119],v[200:203],v[172:175],v[116:119]
ld a[32:35],v246,s[44:47],s55 offen offset:0 sc1
mm v[84:87],v[200:203],v[168:171],v[84:87]
mm v[84:87],v[204:207],v[136:139],v[84:87]
rt v[196:197],v240 offset:59136
rt v[198:199],v240 offset:60160
mm v[52:55],v[204:207],a[40:43],v[52:55]
mm v[52:55],v[200:203],v[156:159],v[52:55]
ld a[44:47],v247,s[44:47],s55 offen offset:0 sc1
mm v[20:23],v[200:203],v[152:155],v[20:23]
wt lgkmcnt(4)
mm v[20:23],v[204:207],a[36:39],v[20:23]
rt v[200:201],v240 offset:0
rt v[202:203],v240 offset:1024
mm v[24:27],v[212:215],a[36:39],v[24:27]
mm v[24:27],v[208:211],v[152:155],v[24:27]
ld a[48:51],v246,s[44:47],s55 offen offset:1024 sc1
mm v[56:59],v[208:211],v[156:159],v[56:59]
mm v[56:59],v[212:215],a[40:43],v[56:59]
rt v[204:205],v240 offset:8448
rt v[206:207],v240 offset:9472
mm v[88:91],v[212:215],v[136:139],v[88:91]
mm v[88:91],v[208:211],v[168:171],v[88:91]
ld a[52:55],v247,s[44:47],s55 offen offset:1024 sc1
mm v[120:123],v[208:211],v[172:175],v[120:123]
mm v[120:123],v[212:215],v[140:143],v[120:123]
rt v[208:209],v240 offset:16896
rt v[210:211],v240 offset:17920
mm v[124:127],v[220:223],v[140:143],v[124:127]
mm v[124:127],v[216:219],v[172:175],v[124:127]
ld a[56:59],v246,s[44:47],s55 offen offset:2048 sc1
mm v[92:95],v[216:219],v[168:171],v[92:95]
mm v[92:95],v[220:223],v[136:139],v[92:95]
rt v[212:213],v240 offset:25344
rt v[214:215],v240 offset:26368
mm v[60:63],v[220:223],a[40:43],v[60:63]
mm v[60:63],v[216:219],v[156:159],v[60:63]
mm v[28:31],v[216:219],v[152:155],v[28:31]
wt vmcnt(13) lgkmcnt(4)
mm v[28:31],v[220:223],a[36:39],v[28:31]
rt v[216:217],v240 offset:33792
rt v[218:219],v240 offset:34816
mm v[0:3],v[192:195],a[64:67],v[0:3]
ld a[36:39],v247,s[44:47],s55 offen offset:2048 sc1
mm v[0:3],v[196:199],a[72:75],v[0:3]
rt v[220:221],v240 offset:42240
rt v[222:223],v240 offset:43264
mm v[32:35],v[196:199],a[76:79],v[32:35]
mm v[32:35],v[192:195],a[68:71],v[32:35]
rt v[196:197],v241 offset:50688
rt v[198:199],v241 offset:51712
mm v[32:35],v[200:203],a[84:87],v[32:35]
ld a[40:43],v246,s[44:47],s55 offen offset:3072 sc1
mm v[32:35],v[204:207],a[92:95],v[32:35]
rt v[192:193],v241 offset:59136
rt v[194:195],v241 offset:60160
mm v[0:3],v[204:207],a[88:91],v[0:3]
wt vmcnt(7) lgkmcnt(4)
mm v[0:3],v[200:203],a[80:83],v[0:3]
rt v[204:205],v241 offset:0
rt v[206:207],v241 offset:1024
mm v[0:3],v[208:211],a[0:3],v[0:3]
ld a[60:63],v247,s[44:47],s55 offen offset:3072 sc1
mm v[0:3],v[212:215],a[8:11],v[0:3]
rt v[200:201],v241 offset:8448
rt v[202:203],v241 offset:9472
mm v[32:35],v[212:215],a[12:15],v[32:35]
mm v[32:35],v[208:211],a[4:7],v[32:35]
rt v[212:213],v241 offset:16896
rt v[214:215],v241 offset:17920
mm v[32:35],v[216:219],a[20:23],v[32:35]
ld v[128:131],v246,s[44:47],s52 offen offset:0 sc1
mm v[32:35],v[220:223],a[28:31],v[32:35]
rt v[208:209],v241 offset:25344
rt v[210:211],v241 offset:26368
mm v[0:3],v[220:223],a[24:27],v[0:3]
wt lgkmcnt(4)
mm v[0:3],v[216:219],a[16:19],v[0:3]
rt v[220:221],v241 offset:33792
rt v[222:223],v241 offset:34816
mm v[4:7],v[196:199],a[64:67],v[4:7]
ld v[132:135],v247,s[44:47],s52 offen offset:0 sc1
mm v[4:7],v[192:195],a[72:75],v[4:7]
rt v[216:217],v241 offset:42240
rt v[218:219],v241 offset:43264
mm v[36:39],v[192:195],a[76:79],v[36:39]
mm v[36:39],v[196:199],a[68:71],v[36:39]
rt v[192:193],v242 offset:50688
rt v[194:195],v242 offset:51712
mm v[36:39],v[204:207],a[84:87],v[36:39]
ld v[144:147],v246,s[44:47],s52 offen offset:1024 sc1
mm v[36:39],v[200:203],a[92:95],v[36:39]
rt v[196:197],v242 offset:59136
rt v[198:199],v242 offset:60160
mm v[4:7],v[200:203],a[88:91],v[4:7]
wt lgkmcnt(4)
mm v[4:7],v[204:207],a[80:83],v[4:7]
rt v[200:201],v242 offset:0
rt v[202:203],v242 offset:1024
mm v[4:7],v[212:215],a[0:3],v[4:7]
ld v[148:151],v247,s[44:47],s52 offen offset:1024 sc1
mm v[4:7],v[208:211],a[8:11],v[4:7]
rt v[204:205],v242 offset:8448
rt v[206:207],v242 offset:9472
mm v[36:39],v[208:211],a[12:15],v[36:39]
mm v[36:39],v[212:215],a[4:7],v[36:39]
rt v[208:209],v242 offset:16896
rt v[210:211],v242 offset:17920
mm v[36:39],v[220:223],a[20:23],v[36:39]
mm v[36:39],v[216:219],a[28:31],v[36:39]
rt v[212:213],v242 offset:25344
rt v[214:215],v242 offset:26368
mm v[4:7],v[216:219],a[24:27],v[4:7]
wt lgkmcnt(4)
mm v[4:7],v[220:223],a[16:19],v[4:7]
rt v[216:217],v242 offset:33792
rt v[218:219],v242 offset:34816
mm v[8:11],v[192:195],a[64:67],v[8:11]
ld v[160:163],v246,s[44:47],s52 offen offset:2048 sc1
mm v[8:11],v[196:199],a[72:75],v[8:11]
rt v[220:221],v242 offset:42240
rt v[222:223],v242 offset:43264
mm v[40:43],v[196:199],a[76:79],v[40:43]
cv v234,v32,v33
cv v236,v36,v37
mm v[40:43],v[192:195],a[68:71],v[40:43]
rt v[196:197],v243 offset:50688
rt v[198:199],v243 offset:51712
mm v[40:43],v[200:203],a[84:87],v[40:43]
ld v[164:167],v247,s[44:47],s52 offen offset:2048 sc1
mm v[40:43],v[204:207],a[92:95],v[40:43]
rt v[192:193],v243 offset:59136
rt v[194:195],v243 offset:60160
mm v[8:11],v[204:207],a[88:91],v[8:11]
wt lgkmcnt(4)
mm v[8:11],v[200:203],a[80:83],v[8:11]
rt v[204:205],v243 offset:0
rt v[206:207],v243 offset:1024
mm v[8:11],v[208:211],a[0:3],v[8:11]
ld v[176:179],v246,s[44:47],s52 offen offset:3072 sc1
mm v[8:11],v[212:215],a[8:11],v[8:11]
rt v[200:201],v243 offset:8448
rt v[202:203],v243 offset:9472
mm v[40:43],v[212:215],a[12:15],v[40:43]
cv v235,v34,v35
cv v237,v38,v39
mm v[40:43],v[208:211],a[4:7],v[40:43]
rt v[212:213],v243 offset:16896
rt v[214:215],v243 offset:17920
mm v[40:43],v[216:219],a[20:23],v[40:43]
ld v[180:183],v247,s[44:47],s52 offen offset:3072 sc1
mm v[40:43],v[220:223],a[28:31],v[40:43]
rt v[208:209],v243 offset:25344
rt v[210:211],v243 offset:26368
mm v[8:11],v[220:223],a[24:27],v[8:11]
wt lgkmcnt(4)
v_bfe_u32 v250, v255, 6, 1
mm v[8:11],v[216:219],a[16:19],v[8:11]
rt v[220:221],v243 offset:33792
rt v[222:223],v243 offset:34816
mm v[12:15],v[196:199],a[64:67],v[12:15]
v_permlane16_swap_b32 v234, v236
v_permlane16_swap_b32 v235, v237
mm v[12:15],v[192:195],a[72:75],v[12:15]
rt v[216:217],v243 offset:42240
rt v[218:219],v243 offset:43264
mm v[44:47],v[192:195],a[76:79],v[44:47]
cv v230,v0,v1
cv v232,v4,v5
mm v[44:47],v[196:199],a[68:71],v[44:47]
rt v[192:193],v240 offset:50816
rt v[194:195],v240 offset:51840
mm v[44:47],v[204:207],a[84:87],v[44:47]
st v[234:237],v238,s[32:35],s95 offen offset:0 sc1
mm v[44:47],v[200:203],a[92:95],v[44:47]
rt v[196:197],v240 offset:59264
rt v[198:199],v240 offset:60288
mm v[12:15],v[200:203],a[88:91],v[12:15]
wt lgkmcnt(4)
v_mul_u32_u24 v250, 7, v250
mm v[12:15],v[204:207],a[80:83],v[12:15]
rt v[200:201],v240 offset:128
rt v[202:203],v240 offset:1152
mm v[12:15],v[212:215],a[0:3],v[12:15]
cv v231,v2,v3
cv v233,v6,v7
mm v[12:15],v[208:211],a[8:11],v[12:15]
rt v[204:205],v240 offset:8576
rt v[206:207],v240 offset:9600
mm v[44:47],v[208:211],a[12:15],v[44:47]
v_permlane16_swap_b32 v230, v232
v_permlane16_swap_b32 v231, v233
mm v[44:47],v[212:215],a[4:7],v[44:47]
rt v[208:209],v240 offset:17024
rt v[210:211],v240 offset:18048
mm v[44:47],v[220:223],a[20:23],v[44:47]
st v[230:233],v238,s[32:35],s94 offen offset:0 sc1
mm v[44:47],v[216:219],a[28:31],v[44:47]
rt v[212:213],v240 offset:25472
rt v[214:215],v240 offset:26496
mm v[12:15],v[216:219],a[24:27],v[12:15]
wt lgkmcnt(4)
v_xor_b32 v250, s3, v250
mm v[12:15],v[220:223],a[16:19],v[12:15]
rt v[216:217],v240 offset:33920
rt v[218:219],v240 offset:34944
mm v[16:19],v[192:195],a[64:67],v[16:19]
cv v234,v40,v41
cv v236,v44,v45
mm v[16:19],v[196:199],a[72:75],v[16:19]
rt v[220:221],v240 offset:42368
rt v[222:223],v240 offset:43392
mm v[48:51],v[196:199],a[76:79],v[48:51]
cv v235,v42,v43
cv v237,v46,v47
mm v[48:51],v[192:195],a[68:71],v[48:51]
rt v[196:197],v241 offset:50816
rt v[198:199],v241 offset:51840
mm v[48:51],v[200:203],a[84:87],v[48:51]
v_permlane16_swap_b32 v234, v236
v_permlane16_swap_b32 v235, v237
mm v[48:51],v[204:207],a[92:95],v[48:51]
rt v[192:193],v241 offset:59264
rt v[194:195],v241 offset:60288
mm v[16:19],v[204:207],a[88:91],v[16:19]
wt lgkmcnt(4)
v_bfe_u32 v251, v255, 4, 2
mm v[16:19],v[200:203],a[80:83],v[16:19]
rt v[204:205],v241 offset:128
rt v[206:207],v241 offset:1152
mm v[16:19],v[208:211],a[0:3],v[16:19]
st v[234:237],v238,s[32:35],s95 offen offset:1024 sc1
mm v[16:19],v[212:215],a[8:11],v[16:19]
rt v[200:201],v241 offset:8576
rt v[202:203],v241 offset:9600
mm v[48:51],v[212:215],a[12:15],v[48:51]
cv v230,v8,v9
cv v232,v12,v13
mm v[48:51],v[208:211],a[4:7],v[48:51]
rt v[212:213],v241 offset:17024
rt v[214:215],v241 offset:18048
mm v[48:51],v[216:219],a[20:23],v[48:51]
cv v231,v10,v11
cv v233,v14,v15
mm v[48:51],v[220:223],a[28:31],v[48:51]
rt v[208:209],v241 offset:25472
rt v[210:211],v241 offset:26496
mm v[16:19],v[220:223],a[24:27],v[16:19]
wt lgkmcnt(4)
v_lshl_add_u32 v250, v250, 2, v251
mm v[16:19],v[216:219],a[16:19],v[16:19]
rt v[220:221],v241 offset:33920
rt v[222:223],v241 offset:34944
mm v[20:23],v[196:199],a[64:67],v[20:23]
v_permlane16_swap_b32 v230, v232
v_permlane16_swap_b32 v231, v233
mm v[20:23],v[192:195],a[72:75],v[20:23]
rt v[216:217],v241 offset:42368
rt v[218:219],v241 offset:43392
mm v[52:55],v[192:195],a[76:79],v[52:55]
v_lshlrev_b32 v250, 9, v250
v_and_b32 v251, 64, v255
mm v[52:55],v[196:199],a[68:71],v[52:55]
rt v[192:193],v242 offset:50816
rt v[194:195],v242 offset:51840
mm v[52:55],v[204:207],a[84:87],v[52:55]
st v[230:233],v238,s[32:35],s94 offen offset:1024 sc1
mm v[52:55],v[200:203],a[92:95],v[52:55]
rt v[196:197],v242 offset:59264
rt v[198:199],v242 offset:60288
mm v[20:23],v[200:203],a[88:91],v[20:23]
wt lgkmcnt(4)
v_lshl_add_u32 v251, v251, 24, v250
mm v[20:23],v[204:207],a[80:83],v[20:23]
rt v[200:201],v242 offset:128
rt v[202:203],v242 offset:1152
mm v[20:23],v[212:215],a[0:3],v[20:23]
ld v[136:139],v224,s[68:71],0 offen offset:0
mm v[20:23],v[208:211],a[8:11],v[20:23]
rt v[204:205],v242 offset:8576
rt v[206:207],v242 offset:9600
mm v[52:55],v[208:211],a[12:15],v[52:55]
v_and_b32 v248, 0x1f0, v255
v_lshrrev_b32 v248, 1, v248
mm v[52:55],v[212:215],a[4:7],v[52:55]
rt v[208:209],v242 offset:17024
rt v[210:211],v242 offset:18048
mm v[52:55],v[220:223],a[20:23],v[52:55]
ld v[140:143],v225,s[48:51],0 offen offset:0
mm v[52:55],v[216:219],a[28:31],v[52:55]
rt v[212:213],v242 offset:25472
rt v[214:215],v242 offset:26496
mm v[20:23],v[216:219],a[24:27],v[20:23]
wt lgkmcnt(4)
s_mov_b32 s93, 0
ld a[224:227],v229,s[28:31],s93 offen offset:0
mm v[20:23],v[220:223],a[16:19],v[20:23]
rt v[216:217],v242 offset:33920
rt v[218:219],v242 offset:34944
mm v[24:27],v[192:195],a[64:67],v[24:27]
cv v234,v48,v49
cv v236,v52,v53
mm v[24:27],v[196:199],a[72:75],v[24:27]
rt v[220:221],v242 offset:42368
rt v[222:223],v242 offset:43392
mm v[56:59],v[196:199],a[76:79],v[56:59]
cv v235,v50,v51
cv v237,v54,v55
mm v[56:59],v[192:195],a[68:71],v[56:59]
rt v[196:197],v243 offset:50816
rt v[198:199],v243 offset:51840
mm v[56:59],v[200:203],a[84:87],v[56:59]
v_permlane16_swap_b32 v234, v236
v_permlane16_swap_b32 v235, v237
mm v[56:59],v[204:207],a[92:95],v[56:59]
rt v[192:193],v243 offset:59264
rt v[194:195],v243 offset:60288
mm v[24:27],v[204:207],a[88:91],v[24:27]
wt lgkmcnt(4)
ld a[228:231],v229,s[28:31],s93 offen offset:2048
mm v[24:27],v[200:203],a[80:83],v[24:27]
rt v[204:205],v243 offset:128
rt v[206:207],v243 offset:1152
mm v[24:27],v[208:211],a[0:3],v[24:27]
st v[234:237],v238,s[32:35],s95 offen offset:2048 sc1
mm v[24:27],v[212:215],a[8:11],v[24:27]
rt v[200:201],v243 offset:8576
rt v[202:203],v243 offset:9600
mm v[56:59],v[212:215],a[12:15],v[56:59]
cv v230,v16,v17
cv v232,v20,v21
mm v[56:59],v[208:211],a[4:7],v[56:59]
rt v[212:213],v243 offset:17024
rt v[214:215],v243 offset:18048
mm v[56:59],v[216:219],a[20:23],v[56:59]
cv v231,v18,v19
cv v233,v22,v23
mm v[56:59],v[220:223],a[28:31],v[56:59]
rt v[208:209],v243 offset:25472
rt v[210:211],v243 offset:26496
mm v[24:27],v[220:223],a[24:27],v[24:27]
wt lgkmcnt(4)
s_mov_b32 s93, 4096
ld a[232:235],v229,s[28:31],s93 offen offset:0
mm v[24:27],v[216:219],a[16:19],v[24:27]
rt v[220:221],v243 offset:33920
rt v[222:223],v243 offset:34944
mm v[28:31],v[196:199],a[64:67],v[28:31]
.La5_LW3_4:
s_cbranch_execz .La5_LA_RETURN
v_permlane16_swap_b32 v230, v232
v_permlane16_swap_b32 v231, v233
mm v[28:31],v[192:195],a[72:75],v[28:31]
rt v[216:217],v243 offset:42368
rt v[218:219],v243 offset:43392
mm v[60:63],v[192:195],a[76:79],v[60:63]
ld a[236:239],v229,s[28:31],s93 offen offset:2048
mm v[60:63],v[196:199],a[68:71],v[60:63]
rt v[192:193],v240 offset:50688
rt v[194:195],v240 offset:51712
mm v[60:63],v[204:207],a[84:87],v[60:63]
st v[230:233],v238,s[32:35],s94 offen offset:2048 sc1
mm v[60:63],v[200:203],a[92:95],v[60:63]
rt v[196:197],v240 offset:59136
rt v[198:199],v240 offset:60160
mm v[28:31],v[200:203],a[88:91],v[28:31]
wt lgkmcnt(4)
s_mov_b32 s93, 8192
ld a[240:243],v229,s[28:31],s93 offen offset:0
mm v[28:31],v[204:207],a[80:83],v[28:31]
rt v[200:201],v240 offset:0
rt v[202:203],v240 offset:1024
mm v[28:31],v[212:215],a[0:3],v[28:31]
ld v[152:155],v224,s[68:71],0 offen offset:512
mm v[28:31],v[208:211],a[8:11],v[28:31]
rt v[204:205],v240 offset:8448
rt v[206:207],v240 offset:9472
mm v[60:63],v[208:211],a[12:15],v[60:63]
ld a[244:247],v229,s[28:31],s93 offen offset:2048
mm v[60:63],v[212:215],a[4:7],v[60:63]
rt v[208:209],v240 offset:16896
rt v[210:211],v240 offset:17920
mm v[60:63],v[220:223],a[20:23],v[60:63]
ld v[156:159],v225,s[48:51],0 offen offset:512
mm v[60:63],v[216:219],a[28:31],v[60:63]
rt v[212:213],v240 offset:25344
rt v[214:215],v240 offset:26368
mm v[28:31],v[216:219],a[24:27],v[28:31]
wt vmcnt(24) lgkmcnt(4)
s_mov_b32 s93, 12288
ld a[248:251],v229,s[28:31],s93 offen offset:0
mm v[28:31],v[220:223],a[16:19],v[28:31]
rt v[216:217],v240 offset:33792
rt v[218:219],v240 offset:34816
mm v[64:67],v[192:195],a[32:35],v[64:67]
cv v234,v56,v57
cv v236,v60,v61
mm v[64:67],v[196:199],a[48:51],v[64:67]
rt v[220:221],v240 offset:42240
rt v[222:223],v240 offset:43264
mm v[96:99],v[196:199],a[52:55],v[96:99]
cv v235,v58,v59
cv v237,v62,v63
mm v[96:99],v[192:195],a[44:47],v[96:99]
rt v[196:197],v241 offset:50688
rt v[198:199],v241 offset:51712
mm v[96:99],v[200:203],a[36:39],v[96:99]
v_permlane16_swap_b32 v234, v236
v_permlane16_swap_b32 v235, v237
mm v[96:99],v[204:207],a[60:63],v[96:99]
rt v[192:193],v241 offset:59136
rt v[194:195],v241 offset:60160
mm v[64:67],v[204:207],a[40:43],v[64:67]
wt vmcnt(17) lgkmcnt(4)
ld a[252:255],v229,s[28:31],s93 offen offset:2048
mm v[64:67],v[200:203],a[56:59],v[64:67]
rt v[204:205],v241 offset:0
rt v[206:207],v241 offset:1024
mm v[64:67],v[208:211],v[128:131],v[64:67]
st v[234:237],v238,s[32:35],s95 offen offset:3072 sc1
mm v[64:67],v[212:215],v[144:147],v[64:67]
rt v[200:201],v241 offset:8448
rt v[202:203],v241 offset:9472
mm v[96:99],v[212:215],v[148:151],v[96:99]
cv v230,v24,v25
cv v232,v28,v29
mm v[96:99],v[208:211],v[132:135],v[96:99]
rt v[212:213],v241 offset:16896
rt v[214:215],v241 offset:17920
mm v[96:99],v[216:219],v[164:167],v[96:99]
cv v231,v26,v27
cv v233,v30,v31
mm v[96:99],v[220:223],v[180:183],v[96:99]
rt v[208:209],v241 offset:25344
rt v[210:211],v241 offset:26368
mm v[64:67],v[220:223],v[176:179],v[64:67]
wt lgkmcnt(4)
s_mov_b32 s93, 0
ld a[64:67],v229,s[28:31],s93 offen offset:256
mm v[64:67],v[216:219],v[160:163],v[64:67]
rt v[220:221],v241 offset:33792
rt v[222:223],v241 offset:34816
mm v[68:71],v[196:199],a[32:35],v[68:71]
v_permlane16_swap_b32 v230, v232
v_permlane16_swap_b32 v231, v233
mm v[68:71],v[192:195],a[48:51],v[68:71]
rt v[216:217],v241 offset:42240
rt v[218:219],v241 offset:43264
mm v[100:103],v[192:195],a[52:55],v[100:103]
ld a[68:71],v229,s[28:31],s93 offen offset:2304
mm v[100:103],v[196:199],a[44:47],v[100:103]
rt v[192:193],v242 offset:50688
rt v[194:195],v242 offset:51712
mm v[100:103],v[204:207],a[36:39],v[100:103]
st v[230:233],v238,s[32:35],s94 offen offset:3072 sc1
mm v[100:103],v[200:203],a[60:63],v[100:103]
rt v[196:197],v242 offset:59136
rt v[198:199],v242 offset:60160
mm v[68:71],v[200:203],a[40:43],v[68:71]
wt lgkmcnt(4)
s_mov_b32 s93, 4096
ld a[72:75],v229,s[28:31],s93 offen offset:256
mm v[68:71],v[204:207],a[56:59],v[68:71]
rt v[200:201],v242 offset:0
rt v[202:203],v242 offset:1024
mm v[68:71],v[212:215],v[128:131],v[68:71]
ld v[168:171],v224,s[68:71],0 offen offset:1024
mm v[68:71],v[208:211],v[144:147],v[68:71]
rt v[204:205],v242 offset:8448
rt v[206:207],v242 offset:9472
mm v[100:103],v[208:211],v[148:151],v[100:103]
ld a[76:79],v229,s[28:31],s93 offen offset:2304
mm v[100:103],v[212:215],v[132:135],v[100:103]
rt v[208:209],v242 offset:16896
rt v[210:211],v242 offset:17920
mm v[100:103],v[220:223],v[164:167],v[100:103]
ld v[172:175],v225,s[48:51],0 offen offset:1024
mm v[100:103],v[216:219],v[180:183],v[100:103]
rt v[212:213],v242 offset:25344
rt v[214:215],v242 offset:26368
mm v[68:71],v[216:219],v[176:179],v[68:71]
wt lgkmcnt(4)
s_mov_b32 s93, 8192
ld a[80:83],v229,s[28:31],s93 offen offset:256
mm v[68:71],v[220:223],v[160:163],v[68:71]
rt v[216:217],v242 offset:33792
rt v[218:219],v242 offset:34816
mm v[72:75],v[192:195],a[32:35],v[72:75]
cv v234,v96,v97
cv v236,v100,v101
mm v[72:75],v[196:199],a[48:51],v[72:75]
rt v[220:221],v242 offset:42240
rt v[222:223],v242 offset:43264
mm v[104:107],v[196:199],a[52:55],v[104:107]
cv v235,v98,v99
cv v237,v102,v103
mm v[104:107],v[192:195],a[44:47],v[104:107]
rt v[196:197],v243 offset:50688
rt v[198:199],v243 offset:51712
mm v[104:107],v[200:203],a[36:39],v[104:107]
v_permlane16_swap_b32 v234, v236
v_permlane16_swap_b32 v235, v237
mm v[104:107],v[204:207],a[60:63],v[104:107]
rt v[192:193],v243 offset:59136
rt v[194:195],v243 offset:60160
mm v[72:75],v[204:207],a[40:43],v[72:75]
wt lgkmcnt(4)
ld a[84:87],v229,s[28:31],s93 offen offset:2304
mm v[72:75],v[200:203],a[56:59],v[72:75]
rt v[204:205],v243 offset:0
rt v[206:207],v243 offset:1024
mm v[72:75],v[208:211],v[128:131],v[72:75]
st v[234:237],v238,s[32:35],s97 offen offset:0 sc1
mm v[72:75],v[212:215],v[144:147],v[72:75]
rt v[200:201],v243 offset:8448
rt v[202:203],v243 offset:9472
mm v[104:107],v[212:215],v[148:151],v[104:107]
cv v230,v64,v65
cv v232,v68,v69
mm v[104:107],v[208:211],v[132:135],v[104:107]
rt v[212:213],v243 offset:16896
rt v[214:215],v243 offset:17920
mm v[104:107],v[216:219],v[164:167],v[104:107]
cv v231,v66,v67
cv v233,v70,v71
mm v[104:107],v[220:223],v[180:183],v[104:107]
rt v[208:209],v243 offset:25344
rt v[210:211],v243 offset:26368
mm v[72:75],v[220:223],v[176:179],v[72:75]
wt lgkmcnt(4)
s_mov_b32 s93, 12288
ld a[88:91],v229,s[28:31],s93 offen offset:256
mm v[72:75],v[216:219],v[160:163],v[72:75]
rt v[220:221],v243 offset:33792
rt v[222:223],v243 offset:34816
mm v[76:79],v[196:199],a[32:35],v[76:79]
v_permlane16_swap_b32 v230, v232
v_permlane16_swap_b32 v231, v233
mm v[76:79],v[192:195],a[48:51],v[76:79]
rt v[216:217],v243 offset:42240
rt v[218:219],v243 offset:43264
mm v[108:111],v[192:195],a[52:55],v[108:111]
ld a[92:95],v229,s[28:31],s93 offen offset:2304
mm v[108:111],v[196:199],a[44:47],v[108:111]
rt v[192:193],v240 offset:50816
rt v[194:195],v240 offset:51840
mm v[108:111],v[204:207],a[36:39],v[108:111]
st v[230:233],v238,s[32:35],s96 offen offset:0 sc1
mm v[108:111],v[200:203],a[60:63],v[108:111]
rt v[196:197],v240 offset:59264
rt v[198:199],v240 offset:60288
mm v[76:79],v[200:203],a[40:43],v[76:79]
wt lgkmcnt(4)
mm v[76:79],v[204:207],a[56:59],v[76:79]
rt v[200:201],v240 offset:128
rt v[202:203],v240 offset:1152
mm v[76:79],v[212:215],v[128:131],v[76:79]
ld v[184:187],v224,s[68:71],0 offen offset:1536
mm v[76:79],v[208:211],v[144:147],v[76:79]
rt v[204:205],v240 offset:8576
rt v[206:207],v240 offset:9600
mm v[108:111],v[208:211],v[148:151],v[108:111]
mm v[108:111],v[212:215],v[132:135],v[108:111]
rt v[208:209],v240 offset:17024
rt v[210:211],v240 offset:18048
mm v[108:111],v[220:223],v[164:167],v[108:111]
ld v[188:191],v225,s[48:51],0 offen offset:1536
mm v[108:111],v[216:219],v[180:183],v[108:111]
rt v[212:213],v240 offset:25472
rt v[214:215],v240 offset:26496
mm v[76:79],v[216:219],v[176:179],v[76:79]
wt lgkmcnt(4)
mm v[76:79],v[220:223],v[160:163],v[76:79]
rt v[216:217],v240 offset:33920
rt v[218:219],v240 offset:34944
mm v[80:83],v[192:195],a[32:35],v[80:83]
cv v234,v104,v105
cv v236,v108,v109
mm v[80:83],v[196:199],a[48:51],v[80:83]
rt v[220:221],v240 offset:42368
rt v[222:223],v240 offset:43392
mm v[112:115],v[196:199],a[52:55],v[112:115]
cv v235,v106,v107
cv v237,v110,v111
mm v[112:115],v[192:195],a[44:47],v[112:115]
rt v[196:197],v241 offset:50816
rt v[198:199],v241 offset:51840
mm v[112:115],v[200:203],a[36:39],v[112:115]
v_permlane16_swap_b32 v234, v236
v_permlane16_swap_b32 v235, v237
mm v[112:115],v[204:207],a[60:63],v[112:115]
rt v[192:193],v241 offset:59264
rt v[194:195],v241 offset:60288
mm v[80:83],v[204:207],a[40:43],v[80:83]
wt lgkmcnt(4)
mm v[80:83],v[200:203],a[56:59],v[80:83]
rt v[204:205],v241 offset:128
rt v[206:207],v241 offset:1152
mm v[80:83],v[208:211],v[128:131],v[80:83]
st v[234:237],v238,s[32:35],s97 offen offset:1024 sc1
mm v[80:83],v[212:215],v[144:147],v[80:83]
rt v[200:201],v241 offset:8576
rt v[202:203],v241 offset:9600
mm v[112:115],v[212:215],v[148:151],v[112:115]
cv v230,v72,v73
cv v232,v76,v77
mm v[112:115],v[208:211],v[132:135],v[112:115]
rt v[212:213],v241 offset:17024
rt v[214:215],v241 offset:18048
mm v[112:115],v[216:219],v[164:167],v[112:115]
cv v231,v74,v75
cv v233,v78,v79
mm v[112:115],v[220:223],v[180:183],v[112:115]
rt v[208:209],v241 offset:25472
rt v[210:211],v241 offset:26496
mm v[80:83],v[220:223],v[176:179],v[80:83]
wt lgkmcnt(4)
mm v[80:83],v[216:219],v[160:163],v[80:83]
rt v[220:221],v241 offset:33920
rt v[222:223],v241 offset:34944
mm v[84:87],v[196:199],a[32:35],v[84:87]
v_permlane16_swap_b32 v230, v232
v_permlane16_swap_b32 v231, v233
mm v[84:87],v[192:195],a[48:51],v[84:87]
rt v[216:217],v241 offset:42368
rt v[218:219],v241 offset:43392
mm v[116:119],v[192:195],a[52:55],v[116:119]
mm v[116:119],v[196:199],a[44:47],v[116:119]
rt v[192:193],v242 offset:50816
rt v[194:195],v242 offset:51840
mm v[116:119],v[204:207],a[36:39],v[116:119]
st v[230:233],v238,s[32:35],s96 offen offset:1024 sc1
mm v[116:119],v[200:203],a[60:63],v[116:119]
rt v[196:197],v242 offset:59264
rt v[198:199],v242 offset:60288
mm v[84:87],v[200:203],a[40:43],v[84:87]
wt lgkmcnt(4)
mm v[84:87],v[204:207],a[56:59],v[84:87]
rt v[200:201],v242 offset:128
rt v[202:203],v242 offset:1152
mm v[84:87],v[212:215],v[128:131],v[84:87]
mm v[84:87],v[208:211],v[144:147],v[84:87]
rt v[204:205],v242 offset:8576
rt v[206:207],v242 offset:9600
mm v[116:119],v[208:211],v[148:151],v[116:119]
mm v[116:119],v[212:215],v[132:135],v[116:119]
rt v[208:209],v242 offset:17024
rt v[210:211],v242 offset:18048
mm v[116:119],v[220:223],v[164:167],v[116:119]
mm v[116:119],v[216:219],v[180:183],v[116:119]
rt v[212:213],v242 offset:25472
rt v[214:215],v242 offset:26496
mm v[84:87],v[216:219],v[176:179],v[84:87]
wt lgkmcnt(4)
mm v[84:87],v[220:223],v[160:163],v[84:87]
rt v[216:217],v242 offset:33920
rt v[218:219],v242 offset:34944
mm v[88:91],v[192:195],a[32:35],v[88:91]
cv v234,v112,v113
cv v236,v116,v117
mm v[88:91],v[196:199],a[48:51],v[88:91]
rt v[220:221],v242 offset:42368
rt v[222:223],v242 offset:43392
mm v[120:123],v[196:199],a[52:55],v[120:123]
cv v235,v114,v115
cv v237,v118,v119
mm v[120:123],v[192:195],a[44:47],v[120:123]
rt v[196:197],v243 offset:50816
rt v[198:199],v243 offset:51840
mm v[120:123],v[200:203],a[36:39],v[120:123]
v_permlane16_swap_b32 v234, v236
v_permlane16_swap_b32 v235, v237
mm v[120:123],v[204:207],a[60:63],v[120:123]
rt v[192:193],v243 offset:59264
rt v[194:195],v243 offset:60288
mm v[88:91],v[204:207],a[40:43],v[88:91]
wt lgkmcnt(4)
mm v[88:91],v[200:203],a[56:59],v[88:91]
rt v[204:205],v243 offset:128
rt v[206:207],v243 offset:1152
mm v[88:91],v[208:211],v[128:131],v[88:91]
st v[234:237],v238,s[32:35],s97 offen offset:2048 sc1
mm v[88:91],v[212:215],v[144:147],v[88:91]
rt v[200:201],v243 offset:8576
rt v[202:203],v243 offset:9600
mm v[120:123],v[212:215],v[148:151],v[120:123]
cv v230,v80,v81
cv v232,v84,v85
mm v[120:123],v[208:211],v[132:135],v[120:123]
rt v[212:213],v243 offset:17024
rt v[214:215],v243 offset:18048
mm v[120:123],v[216:219],v[164:167],v[120:123]
cv v231,v82,v83
cv v233,v86,v87
wt vmcnt(15)
buffer_store_dwordx2 v[252:253], v251, s[64:67], s92 offen sc1
mm v[120:123],v[220:223],v[180:183],v[120:123]
rt v[208:209],v243 offset:25472
rt v[210:211],v243 offset:26496
mm v[88:91],v[220:223],v[176:179],v[88:91]
wt lgkmcnt(4)
mm v[88:91],v[216:219],v[160:163],v[88:91]
rt v[220:221],v243 offset:33920
rt v[222:223],v243 offset:34944
mm v[92:95],v[196:199],a[32:35],v[92:95]
v_permlane16_swap_b32 v230, v232
v_permlane16_swap_b32 v231, v233
mm v[92:95],v[192:195],a[48:51],v[92:95]
rt v[216:217],v243 offset:42368
rt v[218:219],v243 offset:43392
mm v[124:127],v[192:195],a[52:55],v[124:127]
mm v[124:127],v[196:199],a[44:47],v[124:127]
mm v[124:127],v[204:207],a[36:39],v[124:127]
st v[230:233],v238,s[32:35],s96 offen offset:2048 sc1
mm v[124:127],v[200:203],a[60:63],v[124:127]
mm v[92:95],v[200:203],a[40:43],v[92:95]
wt lgkmcnt(0)
mm v[92:95],v[204:207],a[56:59],v[92:95]
mm v[92:95],v[212:215],v[128:131],v[92:95]
mm v[92:95],v[208:211],v[144:147],v[92:95]
mm v[124:127],v[208:211],v[148:151],v[124:127]
mm v[124:127],v[212:215],v[132:135],v[124:127]
mm v[124:127],v[220:223],v[164:167],v[124:127]
mm v[124:127],v[216:219],v[180:183],v[124:127]
mm v[92:95],v[216:219],v[176:179],v[92:95]
mm v[92:95],v[220:223],v[160:163],v[92:95]
s_nop 7
cv v234,v120,v121
cv v236,v124,v125
cv v235,v122,v123
cv v237,v126,v127
cv v230,v88,v89
cv v232,v92,v93
cv v231,v90,v91
cv v233,v94,v95
v_permlane16_swap_b32 v234, v236
v_permlane16_swap_b32 v235, v237
st v[234:237],v238,s[32:35],s97 offen offset:3072 sc1
v_permlane16_swap_b32 v230, v232
v_permlane16_swap_b32 v231, v233
st v[230:233],v238,s[32:35],s96 offen offset:3072 sc1
s_branch .La5_LT_0
.La5_LO_SC:
s_nop 0
.La5_LW7_0:
s_cbranch_execz .La5_LW7_4
mm v[64:67],v[192:195],v[160:163],0
buffer_load_dwordx2 v[250:251], v248, s[64:67], s5 offen sc1
mm v[64:67],v[196:199],v[164:167],v[64:67]
rt v[220:221],v243 offset:25344
rt v[222:223],v243 offset:26368
mm v[96:99],v[196:199],v[180:183],0
s_add_u32 m0, s83, 0
s_bfe_u32 s80, s4, 262148
mm v[96:99],v[192:195],v[176:179],v[96:99]
rt v[196:197],v240 offset:17024
rt v[198:199],v240 offset:18048
s_xor_b32 s80, s80, s72
s_lshl_b32 s80, s80, 18
s_add_u32 s80, s80, 131072
mm v[100:103],v[200:203],v[176:179],0
ld v224,s[56:59],s80 offen lds
mm v[100:103],v[204:207],v[180:183],v[100:103]
rt v[192:193],v240 offset:25472
rt v[194:195],v240 offset:26496
mm v[68:71],v[204:207],v[164:167],0
s_add_u32 m0, s83, 1024
wt lgkmcnt(4)
mm v[68:71],v[200:203],v[160:163],v[68:71]
rt v[204:205],v241 offset:17024
rt v[206:207],v241 offset:18048
mm v[72:75],v[208:211],v[160:163],0
ld v225,s[56:59],s80 offen lds
mm v[72:75],v[212:215],v[164:167],v[72:75]
rt v[200:201],v241 offset:25472
rt v[202:203],v241 offset:26496
mm v[104:107],v[212:215],v[180:183],0
s_add_u32 m0, s83, 2176
s_bfe_u32 s53, s4, 262148
mm v[104:107],v[208:211],v[176:179],v[104:107]
rt v[212:213],v242 offset:17024
rt v[214:215],v242 offset:18048
mm v[108:111],v[216:219],v[176:179],0
ld v226,s[56:59],s80 offen lds
mm v[108:111],v[220:223],v[180:183],v[108:111]
rt v[208:209],v242 offset:25472
rt v[210:211],v242 offset:26496
mm v[76:79],v[220:223],v[164:167],0
s_add_u32 m0, s83, 3200
wt lgkmcnt(4)
mm v[76:79],v[216:219],v[160:163],v[76:79]
rt v[220:221],v243 offset:17024
rt v[222:223],v243 offset:18048
mm v[80:83],v[196:199],v[160:163],0
ld v227,s[56:59],s80 offen lds
mm v[80:83],v[192:195],v[164:167],v[80:83]
rt v[216:217],v243 offset:25472
rt v[218:219],v243 offset:26496
mm v[112:115],v[192:195],v[180:183],0
s_add_u32 m0, s83, 118272
s_bfe_u32 s80, s4, 262152
mm v[112:115],v[196:199],v[176:179],v[112:115]
rt v[192:193],v240 offset:33792
rt v[194:195],v240 offset:34816
s_xor_b32 s80, s80, s72
s_lshl_b32 s80, s80, 18
mm v[116:119],v[204:207],v[176:179],0
ld v224,s[56:59],s80 offen lds
mm v[116:119],v[200:203],v[180:183],v[116:119]
rt v[196:197],v240 offset:42240
rt v[198:199],v240 offset:43264
mm v[84:87],v[200:203],v[164:167],0
s_add_u32 m0, s83, 119296
wt lgkmcnt(4)
mm v[84:87],v[204:207],v[160:163],v[84:87]
rt v[200:201],v241 offset:33792
rt v[202:203],v241 offset:34816
mm v[88:91],v[212:215],v[160:163],0
ld v225,s[56:59],s80 offen lds
mm v[88:91],v[208:211],v[164:167],v[88:91]
rt v[204:205],v241 offset:42240
rt v[206:207],v241 offset:43264
mm v[120:123],v[208:211],v[180:183],0
s_add_u32 m0, s83, 120448
s_xor_b32 s53, s53, s72
mm v[120:123],v[212:215],v[176:179],v[120:123]
rt v[208:209],v242 offset:33792
rt v[210:211],v242 offset:34816
mm v[124:127],v[220:223],v[176:179],0
ld v226,s[56:59],s80 offen lds
mm v[124:127],v[216:219],v[180:183],v[124:127]
rt v[212:213],v242 offset:42240
rt v[214:215],v242 offset:43264
mm v[92:95],v[216:219],v[164:167],0
wt vmcnt(7)
v_cmp_eq_u64 vcc, v[250:251], v[252:253]
s_nop 0
s_bitcmp1_b32 vcc_lo, 1
s_cbranch_scc0 .La5_LO_NC1
.La5_LO_KC1:
wt lgkmcnt(4)
mm v[92:95],v[220:223],v[160:163],v[92:95]
rt v[216:217],v243 offset:33792
rt v[218:219],v243 offset:34816
s_lshl_b32 s53, s53, 13
mm v[64:67],v[192:195],v[168:171],v[64:67]
ld a[0:3],v244,s[44:47],s53 offen offset:0 sc1
mm v[64:67],v[196:199],v[172:175],v[64:67]
rt v[220:221],v243 offset:42240
rt v[222:223],v243 offset:43264
mm v[96:99],v[196:199],v[188:191],v[96:99]
ld a[4:7],v245,s[44:47],s53 offen offset:0 sc1
mm v[96:99],v[192:195],v[184:187],v[96:99]
rt v[196:197],v240 offset:33920
rt v[198:199],v240 offset:34944
mm v[100:103],v[200:203],v[184:187],v[100:103]
ld a[8:11],v246,s[44:47],s53 offen offset:0 sc1
mm v[100:103],v[204:207],v[188:191],v[100:103]
rt v[192:193],v240 offset:42368
rt v[194:195],v240 offset:43392
mm v[68:71],v[204:207],v[172:175],v[68:71]
wt lgkmcnt(4)
s_bfe_u32 s54, s4, 262152
mm v[68:71],v[200:203],v[168:171],v[68:71]
rt v[204:205],v241 offset:33920
rt v[206:207],v241 offset:34944
mm v[72:75],v[208:211],v[168:171],v[72:75]
ld a[12:15],v247,s[44:47],s53 offen offset:0 sc1
mm v[72:75],v[212:215],v[172:175],v[72:75]
rt v[200:201],v241 offset:42368
rt v[202:203],v241 offset:43392
mm v[104:107],v[212:215],v[188:191],v[104:107]
ld a[16:19],v244,s[44:47],s53 offen offset:1024 sc1
mm v[104:107],v[208:211],v[184:187],v[104:107]
rt v[212:213],v242 offset:33920
rt v[214:215],v242 offset:34944
mm v[108:111],v[216:219],v[184:187],v[108:111]
ld a[20:23],v245,s[44:47],s53 offen offset:1024 sc1
mm v[108:111],v[220:223],v[188:191],v[108:111]
rt v[208:209],v242 offset:42368
rt v[210:211],v242 offset:43392
mm v[76:79],v[220:223],v[172:175],v[76:79]
s_nop 0
s_barrier
wt lgkmcnt(4)
mm v[76:79],v[216:219],v[168:171],v[76:79]
rt v[220:221],v243 offset:33920
rt v[222:223],v243 offset:34944
mm v[80:83],v[196:199],v[168:171],v[80:83]
ld a[24:27],v246,s[44:47],s53 offen offset:1024 sc1
mm v[80:83],v[192:195],v[172:175],v[80:83]
rt v[216:217],v243 offset:42368
rt v[218:219],v243 offset:43392
mm v[112:115],v[192:195],v[188:191],v[112:115]
ld a[28:31],v247,s[44:47],s53 offen offset:1024 sc1
mm v[112:115],v[196:199],v[184:187],v[112:115]
rt v[192:193],v240 offset:50688
.La5_LW7_1:
s_cbranch_execz .La5_LW7_5
rt v[194:195],v240 offset:51712
mm v[116:119],v[204:207],v[184:187],v[116:119]
ld a[32:35],v244,s[44:47],s53 offen offset:2048 sc1
mm v[116:119],v[200:203],v[188:191],v[116:119]
rt v[196:197],v240 offset:59136
rt v[198:199],v240 offset:60160
mm v[84:87],v[200:203],v[172:175],v[84:87]
wt lgkmcnt(4)
s_xor_b32 s54, s54, s72
mm v[84:87],v[204:207],v[168:171],v[84:87]
rt v[200:201],v241 offset:50688
rt v[202:203],v241 offset:51712
mm v[88:91],v[212:215],v[168:171],v[88:91]
ld a[36:39],v245,s[44:47],s53 offen offset:2048 sc1
mm v[88:91],v[208:211],v[172:175],v[88:91]
rt v[204:205],v241 offset:59136
rt v[206:207],v241 offset:60160
mm v[120:123],v[208:211],v[188:191],v[120:123]
ld a[40:43],v246,s[44:47],s53 offen offset:2048 sc1
mm v[120:123],v[212:215],v[184:187],v[120:123]
rt v[208:209],v242 offset:50688
rt v[210:211],v242 offset:51712
mm v[124:127],v[220:223],v[184:187],v[124:127]
ld a[44:47],v247,s[44:47],s53 offen offset:2048 sc1
mm v[124:127],v[216:219],v[188:191],v[124:127]
rt v[212:213],v242 offset:59136
rt v[214:215],v242 offset:60160
mm v[92:95],v[216:219],v[172:175],v[92:95]
wt vmcnt(4) lgkmcnt(4)
s_lshl_b32 s54, s54, 13
mm v[92:95],v[220:223],v[168:171],v[92:95]
rt v[216:217],v243 offset:50688
rt v[218:219],v243 offset:51712
mm v[0:3],v[192:195],a[0:3],v[0:3]
s_bfe_u32 s55, s4, 262156
s_xor_b32 s55, s55, s72
mm v[0:3],v[196:199],a[16:19],v[0:3]
ld a[48:51],v244,s[44:47],s53 offen offset:3072 sc1
mm v[32:35],v[196:199],a[20:23],v[32:35]
s_lshl_b32 s55, s55, 13
s_bfe_u32 s52, s4, 262160
mm v[32:35],v[192:195],a[4:7],v[32:35]
rt v[220:221],v243 offset:59136
rt v[222:223],v243 offset:60160
mm v[64:67],v[192:195],a[8:11],v[64:67]
s_xor_b32 s52, s52, s72
s_lshl_b32 s52, s52, 13
mm v[64:67],v[196:199],a[24:27],v[64:67]
ld a[52:55],v245,s[44:47],s53 offen offset:3072 sc1
mm v[96:99],v[196:199],a[28:31],v[96:99]
mm v[96:99],v[192:195],a[12:15],v[96:99]
rt v[196:197],v240 offset:50816
rt v[198:199],v240 offset:51840
mm v[100:103],v[200:203],a[12:15],v[100:103]
mm v[100:103],v[204:207],a[28:31],v[100:103]
buffer_load_dwordx2 v[250:251], v248, s[64:67], s5 offen sc1
mm v[68:71],v[204:207],a[24:27],v[68:71]
mm v[68:71],v[200:203],a[8:11],v[68:71]
rt v[192:193],v240 offset:59264
rt v[194:195],v240 offset:60288
mm v[36:39],v[200:203],a[4:7],v[36:39]
mm v[36:39],v[204:207],a[20:23],v[36:39]
ld a[56:59],v246,s[44:47],s53 offen offset:3072 sc1
mm v[4:7],v[204:207],a[16:19],v[4:7]
wt lgkmcnt(4)
mm v[4:7],v[200:203],a[0:3],v[4:7]
rt v[204:205],v241 offset:50816
rt v[206:207],v241 offset:51840
mm v[8:11],v[208:211],a[0:3],v[8:11]
mm v[8:11],v[212:215],a[16:19],v[8:11]
ld a[60:63],v247,s[44:47],s53 offen offset:3072 sc1
mm v[40:43],v[212:215],a[20:23],v[40:43]
s_bfe_u32 s53, s4, 262164
s_xor_b32 s53, s53, s72
mm v[40:43],v[208:211],a[4:7],v[40:43]
rt v[200:201],v241 offset:59264
rt v[202:203],v241 offset:60288
mm v[72:75],v[208:211],a[8:11],v[72:75]
s_add_u32 m0, s83, 121472
s_lshl_b32 s53, s53, 13
mm v[72:75],v[212:215],a[24:27],v[72:75]
ld v227,s[56:59],s80 offen lds
mm v[104:107],v[212:215],a[28:31],v[104:107]
s_add_u32 s80, s80, 131072
mm v[104:107],v[208:211],a[12:15],v[104:107]
rt v[212:213],v242 offset:50816
rt v[214:215],v242 offset:51840
mm v[108:111],v[216:219],a[12:15],v[108:111]
s_add_u32 m0, s83, 67584
mm v[108:111],v[220:223],a[28:31],v[108:111]
ld v224,s[56:59],s80 offen lds
mm v[76:79],v[220:223],a[24:27],v[76:79]
mm v[76:79],v[216:219],a[8:11],v[76:79]
rt v[208:209],v242 offset:59264
rt v[210:211],v242 offset:60288
mm v[44:47],v[216:219],a[4:7],v[44:47]
s_add_u32 m0, s83, 68608
mm v[44:47],v[220:223],a[20:23],v[44:47]
ld v225,s[56:59],s80 offen lds
mm v[12:15],v[220:223],a[16:19],v[12:15]
wt lgkmcnt(4)
mm v[12:15],v[216:219],a[0:3],v[12:15]
rt v[220:221],v243 offset:50816
rt v[222:223],v243 offset:51840
mm v[16:19],v[196:199],a[0:3],v[16:19]
s_add_u32 m0, s83, 69760
mm v[16:19],v[192:195],a[16:19],v[16:19]
ld v226,s[56:59],s80 offen lds
mm v[48:51],v[192:195],a[20:23],v[48:51]
mm v[48:51],v[196:199],a[4:7],v[48:51]
rt v[216:217],v243 offset:59264
rt v[218:219],v243 offset:60288
mm v[80:83],v[196:199],a[8:11],v[80:83]
s_add_u32 m0, s83, 70784
s_nop 0
s_barrier
mm v[80:83],v[192:195],a[24:27],v[80:83]
ld v227,s[56:59],s80 offen lds
mm v[112:115],v[192:195],a[28:31],v[112:115]
s_bfe_u32 s80, s4, 262156
s_xor_b32 s80, s80, s72
mm v[112:115],v[196:199],a[12:15],v[112:115]
rt v[192:193],v240 offset:0
rt v[194:195],v240 offset:1024
mm v[116:119],v[204:207],a[12:15],v[116:119]
s_add_u32 m0, s83, 84480
s_lshl_b32 s80, s80, 18
mm v[116:119],v[200:203],a[28:31],v[116:119]
ld v224,s[56:59],s80 offen lds
mm v[84:87],v[200:203],a[24:27],v[84:87]
mm v[84:87],v[204:207],a[8:11],v[84:87]
rt v[196:197],v240 offset:8448
rt v[198:199],v240 offset:9472
mm v[52:55],v[204:207],a[4:7],v[52:55]
s_add_u32 m0, s83, 85504
mm v[52:55],v[200:203],a[20:23],v[52:55]
ld v225,s[56:59],s80 offen lds
mm v[20:23],v[200:203],a[16:19],v[20:23]
wt lgkmcnt(4)
mm v[20:23],v[204:207],a[0:3],v[20:23]
rt v[200:201],v241 offset:0
rt v[202:203],v241 offset:1024
mm v[24:27],v[212:215],a[0:3],v[24:27]
s_add_u32 m0, s83, 86656
mm v[24:27],v[208:211],a[16:19],v[24:27]
ld v226,s[56:59],s80 offen lds
mm v[56:59],v[208:211],a[20:23],v[56:59]
wt vmcnt(10)
v_cmp_eq_u64 vcc, v[250:251], v[252:253]
mm v[56:59],v[212:215],a[4:7],v[56:59]
rt v[204:205],v241 offset:8448
rt v[206:207],v241 offset:9472
mm v[88:91],v[212:215],a[8:11],v[88:91]
s_add_u32 m0, s83, 87680
mm v[88:91],v[208:211],a[24:27],v[88:91]
ld v227,s[56:59],s80 offen lds
mm v[120:123],v[208:211],a[28:31],v[120:123]
s_bitcmp1_b32 vcc_lo, 2
s_cbranch_scc0 .La5_LO_NC2
.La5_LO_KC2:
mm v[120:123],v[212:215],a[12:15],v[120:123]
rt v[208:209],v242 offset:0
rt v[210:211],v242 offset:1024
mm v[124:127],v[220:223],a[12:15],v[124:127]
s_add_u32 s80, s80, 131072
mm v[124:127],v[216:219],a[28:31],v[124:127]
ld a[12:15],v244,s[44:47],s54 offen offset:0 sc1
mm v[92:95],v[216:219],a[24:27],v[92:95]
mm v[92:95],v[220:223],a[8:11],v[92:95]
rt v[212:213],v242 offset:8448
rt v[214:215],v242 offset:9472
mm v[60:63],v[220:223],a[4:7],v[60:63]
mm v[60:63],v[216:219],a[20:23],v[60:63]
mm v[28:31],v[216:219],a[16:19],v[28:31]
wt vmcnt(10) lgkmcnt(4)
mm v[28:31],v[220:223],a[0:3],v[28:31]
rt v[216:217],v243 offset:0
rt v[218:219],v243 offset:1024
mm v[0:3],v[192:195],a[32:35],v[0:3]
mm v[0:3],v[196:199],a[48:51],v[0:3]
ld a[0:3],v245,s[44:47],s54 offen offset:0 sc1
mm v[32:35],v[196:199],a[52:55],v[32:35]
mm v[32:35],v[192:195],a[36:39],v[32:35]
rt v[220:221],v243 offset:8448
rt v[222:223],v243 offset:9472
mm v[64:67],v[192:195],a[40:43],v[64:67]
mm v[64:67],v[196:199],a[56:59],v[64:67]
ld a[4:7],v246,s[44:47],s54 offen offset:0 sc1
mm v[96:99],v[196:199],a[60:63],v[96:99]
mm v[96:99],v[192:195],a[44:47],v[96:99]
rt v[196:197],v240 offset:128
rt v[198:199],v240 offset:1152
mm v[100:103],v[200:203],a[44:47],v[100:103]
s_mov_b32 s86, 0x05040100
s_mov_b32 s87, 0x07060302
mm v[100:103],v[204:207],a[60:63],v[100:103]
ld a[8:11],v247,s[44:47],s54 offen offset:0 sc1
mm v[68:71],v[204:207],a[56:59],v[68:71]
s_nop 0
ar v230,a128
mm v[68:71],v[200:203],a[40:43],v[68:71]
rt v[192:193],v240 offset:8576
rt v[194:195],v240 offset:9600
mm v[36:39],v[200:203],a[36:39],v[36:39]
v_add_u32 v240, 67584, v240
ar v231,a132
mm v[36:39],v[204:207],a[52:55],v[36:39]
ld a[16:19],v244,s[44:47],s54 offen offset:1024 sc1
mm v[4:7],v[204:207],a[48:51],v[4:7]
wt lgkmcnt(4)
v_perm_b32 v232, v231, v230, s86
mm v[4:7],v[200:203],a[32:35],v[4:7]
rt v[204:205],v241 offset:128
rt v[206:207],v241 offset:1152
mm v[8:11],v[208:211],a[32:35],v[8:11]
v_perm_b32 v233, v231, v230, s87
aw a96,v232
mm v[8:11],v[212:215],a[48:51],v[8:11]
ld a[20:23],v245,s[44:47],s54 offen offset:1024 sc1
mm v[40:43],v[212:215],a[52:55],v[40:43]
aw a100,v233
ar v234,a136
mm v[40:43],v[208:211],a[36:39],v[40:43]
rt v[200:201],v241 offset:8576
rt v[202:203],v241 offset:9600
mm v[72:75],v[208:211],a[40:43],v[72:75]
v_add_u32 v241, 67584, v241
ar v235,a140
mm v[72:75],v[212:215],a[56:59],v[72:75]
ld a[24:27],v246,s[44:47],s54 offen offset:1024 sc1
mm v[104:107],v[212:215],a[60:63],v[104:107]
v_perm_b32 v236, v235, v234, s86
v_perm_b32 v237, v235, v234, s87
mm v[104:107],v[208:211],a[44:47],v[104:107]
rt v[212:213],v242 offset:128
rt v[214:215],v242 offset:1152
mm v[108:111],v[216:219],a[44:47],v[108:111]
aw a97,v236
aw a101,v237
mm v[108:111],v[220:223],a[60:63],v[108:111]
ld a[28:31],v247,s[44:47],s54 offen offset:1024 sc1
mm v[76:79],v[220:223],a[56:59],v[76:79]
ar v230,a144
ar v231,a148
mm v[76:79],v[216:219],a[40:43],v[76:79]
rt v[208:209],v242 offset:8576
rt v[210:211],v242 offset:9600
mm v[44:47],v[216:219],a[36:39],v[44:47]
v_add_u32 v242, 67584, v242
v_perm_b32 v232, v231, v230, s86
mm v[44:47],v[220:223],a[52:55],v[44:47]
v_perm_b32 v233, v231, v230, s87
aw a98,v232
mm v[12:15],v[220:223],a[48:51],v[12:15]
wt lgkmcnt(4)
aw a102,v233
mm v[12:15],v[216:219],a[32:35],v[12:15]
rt v[220:221],v243 offset:128
rt v[222:223],v243 offset:1152
mm v[16:19],v[196:199],a[32:35],v[16:19]
s_add_u32 m0, s83, 101376
ar v234,a152
mm v[16:19],v[192:195],a[48:51],v[16:19]
ld v224,s[56:59],s80 offen lds
mm v[48:51],v[192:195],a[52:55],v[48:51]
ar v235,a156
v_perm_b32 v236, v235, v234, s86
mm v[48:51],v[196:199],a[36:39],v[48:51]
rt v[216:217],v243 offset:8576
rt v[218:219],v243 offset:9600
mm v[80:83],v[196:199],a[40:43],v[80:83]
s_add_u32 m0, s83, 102400
v_add_u32 v243, 67584, v243
mm v[80:83],v[192:195],a[56:59],v[80:83]
ld v225,s[56:59],s80 offen lds
mm v[112:115],v[192:195],a[60:63],v[112:115]
wt vmcnt(14)
s_barrier
mm v[112:115],v[196:199],a[44:47],v[112:115]
rt v[192:193],v240 offset:50688
rt v[194:195],v240 offset:51712
mm v[116:119],v[204:207],a[44:47],v[116:119]
s_add_u32 m0, s83, 103552
v_perm_b32 v237, v235, v234, s87
mm v[116:119],v[200:203],a[60:63],v[116:119]
ld v226,s[56:59],s80 offen lds
mm v[84:87],v[200:203],a[56:59],v[84:87]
aw a99,v236
aw a103,v237
mm v[84:87],v[204:207],a[40:43],v[84:87]
rt v[196:197],v240 offset:59136
rt v[198:199],v240 offset:60160
mm v[52:55],v[204:207],a[36:39],v[52:55]
ar v230,a129
ar v231,a133
mm v[52:55],v[200:203],a[52:55],v[52:55]
ld a[64:67],v244,s[44:47],s54 offen offset:2048 sc1
mm v[20:23],v[200:203],a[48:51],v[20:23]
wt lgkmcnt(4)
v_perm_b32 v232, v231, v230, s86
mm v[20:23],v[204:207],a[32:35],v[20:23]
rt v[200:201],v241 offset:50688
rt v[202:203],v241 offset:51712
mm v[24:27],v[212:215],a[32:35],v[24:27]
v_perm_b32 v233, v231, v230, s87
aw a104,v232
mm v[24:27],v[208:211],a[48:51],v[24:27]
ld a[68:71],v245,s[44:47],s54 offen offset:2048 sc1
mm v[56:59],v[208:211],a[52:55],v[56:59]
aw a108,v233
ar v234,a137
mm v[56:59],v[212:215],a[36:39],v[56:59]
rt v[204:205],v241 offset:59136
rt v[206:207],v241 offset:60160
mm v[88:91],v[212:215],a[40:43],v[88:91]
ar v235,a141
v_perm_b32 v236, v235, v234, s86
mm v[88:91],v[208:211],a[56:59],v[88:91]
ld a[72:75],v246,s[44:47],s54 offen offset:2048 sc1
mm v[120:123],v[208:211],a[60:63],v[120:123]
v_perm_b32 v237, v235, v234, s87
aw a105,v236
mm v[120:123],v[212:215],a[44:47],v[120:123]
rt v[208:209],v242 offset:50688
rt v[210:211],v242 offset:51712
mm v[124:127],v[220:223],a[44:47],v[124:127]
aw a109,v237
ar v230,a145
mm v[124:127],v[216:219],a[60:63],v[124:127]
buffer_load_dwordx2 v[250:251], v248, s[64:67], s5 offen sc1
mm v[92:95],v[216:219],a[56:59],v[92:95]
ar v231,a149
v_perm_b32 v232, v231, v230, s86
mm v[92:95],v[220:223],a[40:43],v[92:95]
rt v[212:213],v242 offset:59136
rt v[214:215],v242 offset:60160
mm v[60:63],v[220:223],a[36:39],v[60:63]
v_perm_b32 v233, v231, v230, s87
aw a106,v232
mm v[60:63],v[216:219],a[52:55],v[60:63]
ld a[36:39],v247,s[44:47],s54 offen offset:2048 sc1
mm v[28:31],v[216:219],a[48:51],v[28:31]
wt vmcnt(8) lgkmcnt(4)
aw a110,v233
mm v[28:31],v[220:223],a[32:35],v[28:31]
rt v[216:217],v243 offset:50688
rt v[218:219],v243 offset:51712
mm v[0:3],v[192:195],a[12:15],v[0:3]
ar v234,a153
ar v235,a157
mm v[0:3],v[196:199],a[16:19],v[0:3]
ld a[32:35],v244,s[44:47],s54 offen offset:3072 sc1
mm v[32:35],v[196:199],a[20:23],v[32:35]
v_perm_b32 v236, v235, v234, s86
v_perm_b32 v237, v235, v234, s87
mm v[32:35],v[192:195],a[0:3],v[32:35]
rt v[220:221],v243 offset:59136
rt v[222:223],v243 offset:60160
mm v[64:67],v[192:195],a[4:7],v[64:67]
aw a107,v236
aw a111,v237
mm v[64:67],v[196:199],a[24:27],v[64:67]
ld a[40:43],v245,s[44:47],s54 offen offset:3072 sc1
mm v[96:99],v[196:199],a[28:31],v[96:99]
ar v230,a130
ar v231,a134
mm v[96:99],v[192:195],a[8:11],v[96:99]
rt v[196:197],v240 offset:50816
rt v[198:199],v240 offset:51840
mm v[100:103],v[200:203],a[8:11],v[100:103]
v_perm_b32 v232, v231, v230, s86
v_perm_b32 v233, v231, v230, s87
mm v[100:103],v[204:207],a[28:31],v[100:103]
ld a[44:47],v246,s[44:47],s54 offen offset:3072 sc1
mm v[68:71],v[204:207],a[24:27],v[68:71]
aw a112,v232
aw a116,v233
mm v[68:71],v[200:203],a[4:7],v[68:71]
rt v[192:193],v240 offset:59264
rt v[194:195],v240 offset:60288
mm v[36:39],v[200:203],a[0:3],v[36:39]
ar v234,a138
ar v235,a142
mm v[36:39],v[204:207],a[20:23],v[36:39]
ld a[48:51],v247,s[44:47],s54 offen offset:3072 sc1
mm v[4:7],v[204:207],a[16:19],v[4:7]
wt lgkmcnt(4)
s_bfe_u32 s54, s4, 262168
mm v[4:7],v[200:203],a[12:15],v[4:7]
rt v[204:205],v241 offset:50816
rt v[206:207],v241 offset:51840
mm v[8:11],v[208:211],a[12:15],v[8:11]
s_add_u32 m0, s83, 104576
s_xor_b32 s54, s54, s72
mm v[8:11],v[212:215],a[16:19],v[8:11]
ld v227,s[56:59],s80 offen lds
mm v[40:43],v[212:215],a[20:23],v[40:43]
s_bitcmp1_b32 vcc_lo, 3
s_cbranch_scc0 .La5_LO_NC3
.La5_LO_KC3:
mm v[40:43],v[208:211],a[0:3],v[40:43]
rt v[200:201],v241 offset:59264
rt v[202:203],v241 offset:60288
mm v[72:75],v[208:211],a[4:7],v[72:75]
s_bfe_u32 s80, s4, 262160
s_xor_b32 s80, s80, s72
mm v[72:75],v[212:215],a[24:27],v[72:75]
ld a[52:55],v244,s[44:47],s55 offen offset:0 sc1
mm v[104:107],v[212:215],a[28:31],v[104:107]
s_lshl_b32 s80, s80, 18
s_lshl_b32 s54, s54, 13
mm v[104:107],v[208:211],a[8:11],v[104:107]
rt v[212:213],v242 offset:50816
rt v[214:215],v242 offset:51840
mm v[108:111],v[216:219],a[8:11],v[108:111]
v_perm_b32 v236, v235, v234, s86
v_perm_b32 v237, v235, v234, s87
mm v[108:111],v[220:223],a[28:31],v[108:111]
ld a[56:59],v245,s[44:47],s55 offen offset:0 sc1
mm v[76:79],v[220:223],a[24:27],v[76:79]
aw a113,v236
aw a117,v237
mm v[76:79],v[216:219],a[4:7],v[76:79]
rt v[208:209],v242 offset:59264
rt v[210:211],v242 offset:60288
mm v[44:47],v[216:219],a[0:3],v[44:47]
ar v230,a146
ar v231,a150
mm v[44:47],v[220:223],a[20:23],v[44:47]
v_perm_b32 v232, v231, v230, s86
v_perm_b32 v233, v231, v230, s87
mm v[12:15],v[220:223],a[16:19],v[12:15]
wt lgkmcnt(4)
aw a114,v232
mm v[12:15],v[216:219],a[12:15],v[12:15]
rt v[220:221],v243 offset:50816
rt v[222:223],v243 offset:51840
mm v[16:19],v[196:199],a[12:15],v[16:19]
aw a118,v233
ar v234,a154
mm v[16:19],v[192:195],a[16:19],v[16:19]
ld a[60:63],v246,s[44:47],s55 offen offset:0 sc1
mm v[48:51],v[192:195],a[20:23],v[48:51]
ar v235,a158
v_perm_b32 v236, v235, v234, s86
mm v[48:51],v[196:199],a[0:3],v[48:51]
rt v[216:217],v243 offset:59264
rt v[218:219],v243 offset:60288
mm v[80:83],v[196:199],a[4:7],v[80:83]
v_perm_b32 v237, v235, v234, s87
aw a115,v236
mm v[80:83],v[192:195],a[24:27],v[80:83]
ld a[76:79],v247,s[44:47],s55 offen offset:0 sc1
mm v[112:115],v[192:195],a[28:31],v[112:115]
aw a119,v237
ar v230,a131
mm v[112:115],v[196:199],a[8:11],v[112:115]
rt v[192:193],v240 offset:0
rt v[194:195],v240 offset:1024
mm v[116:119],v[204:207],a[8:11],v[116:119]
ar v231,a135
v_perm_b32 v232, v231, v230, s86
mm v[116:119],v[200:203],a[28:31],v[116:119]
ld a[80:83],v244,s[44:47],s55 offen offset:1024 sc1
mm v[84:87],v[200:203],a[24:27],v[84:87]
v_perm_b32 v233, v231, v230, s87
aw a120,v232
mm v[84:87],v[204:207],a[4:7],v[84:87]
rt v[196:197],v240 offset:8448
rt v[198:199],v240 offset:9472
mm v[52:55],v[204:207],a[0:3],v[52:55]
s_load_dwordx2 s[16:17], s[0:1], 0
s_load_dwordx4 s[36:39], s[0:1], 80
mm v[52:55],v[200:203],a[20:23],v[52:55]
ld a[84:87],v245,s[44:47],s55 offen offset:1024 sc1
mm v[20:23],v[200:203],a[16:19],v[20:23]
wt lgkmcnt(4)
aw a124,v233
mm v[20:23],v[204:207],a[12:15],v[20:23]
rt v[200:201],v241 offset:0
rt v[202:203],v241 offset:1024
mm v[24:27],v[212:215],a[12:15],v[24:27]
ar v234,a139
ar v235,a143
mm v[24:27],v[208:211],a[16:19],v[24:27]
ld a[88:91],v246,s[44:47],s55 offen offset:1024 sc1
mm v[56:59],v[208:211],a[20:23],v[56:59]
v_perm_b32 v236, v235, v234, s86
v_perm_b32 v237, v235, v234, s87
mm v[56:59],v[212:215],a[0:3],v[56:59]
rt v[204:205],v241 offset:8448
rt v[206:207],v241 offset:9472
mm v[88:91],v[212:215],a[4:7],v[88:91]
aw a121,v236
aw a125,v237
mm v[88:91],v[208:211],a[24:27],v[88:91]
ld a[92:95],v247,s[44:47],s55 offen offset:1024 sc1
mm v[120:123],v[208:211],a[28:31],v[120:123]
ar v230,a147
ar v231,a151
mm v[120:123],v[212:215],a[8:11],v[120:123]
rt v[208:209],v242 offset:0
rt v[210:211],v242 offset:1024
mm v[124:127],v[220:223],a[8:11],v[124:127]
s_add_u32 m0, s83, 16896
v_perm_b32 v232, v231, v230, s86
mm v[124:127],v[216:219],a[28:31],v[124:127]
ld v224,s[56:59],s80 offen lds
mm v[92:95],v[216:219],a[24:27],v[92:95]
v_perm_b32 v233, v231, v230, s87
aw a122,v232
mm v[92:95],v[220:223],a[4:7],v[92:95]
rt v[212:213],v242 offset:8448
rt v[214:215],v242 offset:9472
mm v[60:63],v[220:223],a[0:3],v[60:63]
aw a126,v233
ar v234,a155
mm v[60:63],v[216:219],a[20:23],v[60:63]
ar v235,a159
v_perm_b32 v236, v235, v234, s86
mm v[28:31],v[216:219],a[16:19],v[28:31]
wt vmcnt(10) lgkmcnt(4)
v_perm_b32 v237, v235, v234, s87
mm v[28:31],v[220:223],a[12:15],v[28:31]
rt v[216:217],v243 offset:0
rt v[218:219],v243 offset:1024
mm v[0:3],v[192:195],a[64:67],v[0:3]
s_add_u32 m0, s83, 17920
aw a123,v236
mm v[0:3],v[196:199],a[32:35],v[0:3]
ld v225,s[56:59],s80 offen lds
mm v[32:35],v[196:199],a[40:43],v[32:35]
aw a127,v237
.La5_LW7_2:
s_cbranch_execz .La5_LW7_6
ar v230,a160
mm v[32:35],v[192:195],a[68:71],v[32:35]
rt v[220:221],v243 offset:8448
rt v[222:223],v243 offset:9472
mm v[64:67],v[192:195],a[72:75],v[64:67]
s_add_u32 m0, s83, 19072
ar v231,a164
mm v[64:67],v[196:199],a[44:47],v[64:67]
ld v226,s[56:59],s80 offen lds
mm v[96:99],v[196:199],a[48:51],v[96:99]
v_perm_b32 v232, v231, v230, s86
v_perm_b32 v233, v231, v230, s87
mm v[96:99],v[192:195],a[36:39],v[96:99]
rt v[196:197],v240 offset:128
rt v[198:199],v240 offset:1152
mm v[100:103],v[200:203],a[36:39],v[100:103]
aw a128,v232
aw a132,v233
mm v[100:103],v[204:207],a[48:51],v[100:103]
ld v[136:139],v244,s[44:47],s55 offen offset:2048 sc1
mm v[68:71],v[204:207],a[44:47],v[68:71]
ar v234,a168
ar v235,a172
mm v[68:71],v[200:203],a[72:75],v[68:71]
rt v[192:193],v240 offset:8576
rt v[194:195],v240 offset:9600
mm v[36:39],v[200:203],a[68:71],v[36:39]
v_perm_b32 v236, v235, v234, s86
v_perm_b32 v237, v235, v234, s87
mm v[36:39],v[204:207],a[40:43],v[36:39]
ld v[140:143],v245,s[44:47],s55 offen offset:2048 sc1
mm v[4:7],v[204:207],a[32:35],v[4:7]
wt lgkmcnt(4)
aw a129,v236
mm v[4:7],v[200:203],a[64:67],v[4:7]
rt v[204:205],v241 offset:128
rt v[206:207],v241 offset:1152
mm v[8:11],v[208:211],a[64:67],v[8:11]
aw a133,v237
ar v230,a176
mm v[8:11],v[212:215],a[32:35],v[8:11]
ld v[152:155],v246,s[44:47],s55 offen offset:2048 sc1
mm v[40:43],v[212:215],a[40:43],v[40:43]
ar v231,a180
v_perm_b32 v232, v231, v230, s86
mm v[40:43],v[208:211],a[68:71],v[40:43]
rt v[200:201],v241 offset:8576
rt v[202:203],v241 offset:9600
mm v[72:75],v[208:211],a[72:75],v[72:75]
v_perm_b32 v233, v231, v230, s87
aw a130,v232
mm v[72:75],v[212:215],a[44:47],v[72:75]
ld v[156:159],v247,s[44:47],s55 offen offset:2048 sc1
mm v[104:107],v[212:215],a[48:51],v[104:107]
aw a134,v233
ar v234,a184
mm v[104:107],v[208:211],a[36:39],v[104:107]
rt v[212:213],v242 offset:128
rt v[214:215],v242 offset:1152
mm v[108:111],v[216:219],a[36:39],v[108:111]
ar v235,a188
v_perm_b32 v236, v235, v234, s86
mm v[108:111],v[220:223],a[48:51],v[108:111]
ld v[168:171],v244,s[44:47],s55 offen offset:3072 sc1
mm v[76:79],v[220:223],a[44:47],v[76:79]
v_perm_b32 v237, v235, v234, s87
aw a131,v236
mm v[76:79],v[216:219],a[72:75],v[76:79]
rt v[208:209],v242 offset:8576
rt v[210:211],v242 offset:9600
mm v[44:47],v[216:219],a[68:71],v[44:47]
aw a135,v237
ar v230,a161
mm v[44:47],v[220:223],a[40:43],v[44:47]
s_nop 0
v_cmp_eq_u64 vcc, v[250:251], v[252:253]
mm v[12:15],v[220:223],a[32:35],v[12:15]
wt lgkmcnt(4)
ar v231,a165
mm v[12:15],v[216:219],a[64:67],v[12:15]
rt v[220:221],v243 offset:128
rt v[222:223],v243 offset:1152
mm v[16:19],v[196:199],a[64:67],v[16:19]
v_perm_b32 v232, v231, v230, s86
v_perm_b32 v233, v231, v230, s87
mm v[16:19],v[192:195],a[32:35],v[16:19]
ld v[172:175],v245,s[44:47],s55 offen offset:3072 sc1
mm v[48:51],v[192:195],a[40:43],v[48:51]
aw a136,v232
aw a140,v233
mm v[48:51],v[196:199],a[68:71],v[48:51]
rt v[216:217],v243 offset:8576
rt v[218:219],v243 offset:9600
mm v[80:83],v[196:199],a[72:75],v[80:83]
ar v234,a169
ar v235,a173
mm v[80:83],v[192:195],a[44:47],v[80:83]
ld v[184:187],v246,s[44:47],s55 offen offset:3072 sc1
mm v[112:115],v[192:195],a[48:51],v[112:115]
wt vmcnt(18) lgkmcnt(0)
s_barrier
mm v[112:115],v[196:199],a[36:39],v[112:115]
rt v[192:193],v240 offset:16896
rt v[194:195],v240 offset:17920
mm v[116:119],v[204:207],a[36:39],v[116:119]
s_xor_b32 s90, s72, 0
s_lshl_b32 s92, s76, 5
mm v[116:119],v[200:203],a[48:51],v[116:119]
buffer_load_dwordx2 v[250:251], v248, s[64:67], s5 offen sc1
mm v[84:87],v[200:203],a[44:47],v[84:87]
s_add_u32 s92, s92, s90
s_lshl2_add_u32 s92, s92, s3
mm v[84:87],v[204:207],a[72:75],v[84:87]
rt v[196:197],v240 offset:25344
rt v[198:199],v240 offset:26368
mm v[52:55],v[204:207],a[68:71],v[52:55]
s_lshl_b32 s92, s92, 12
v_perm_b32 v236, v235, v234, s86
mm v[52:55],v[200:203],a[40:43],v[52:55]
ld v[188:191],v247,s[44:47],s55 offen offset:3072 sc1
mm v[20:23],v[200:203],a[32:35],v[20:23]
s_nop 0
s_bfe_u32 s55, s4, 262172
mm v[20:23],v[204:207],a[64:67],v[20:23]
rt v[200:201],v241 offset:16896
rt v[202:203],v241 offset:17920
mm v[24:27],v[212:215],a[64:67],v[24:27]
s_add_u32 m0, s83, 20096
s_xor_b32 s55, s55, s72
mm v[24:27],v[208:211],a[32:35],v[24:27]
ld v227,s[56:59],s80 offen lds
mm v[56:59],v[208:211],a[40:43],v[56:59]
s_add_u32 s80, s80, 131072
s_lshl_b32 s55, s55, 13
mm v[56:59],v[212:215],a[68:71],v[56:59]
rt v[204:205],v241 offset:25344
rt v[206:207],v241 offset:26368
mm v[88:91],v[212:215],a[72:75],v[88:91]
s_add_u32 m0, s83, 33792
v_perm_b32 v237, v235, v234, s87
mm v[88:91],v[208:211],a[44:47],v[88:91]
ld v224,s[56:59],s80 offen lds
mm v[120:123],v[208:211],a[48:51],v[120:123]
s_add_u32 s68, s16, s92
s_addc_u32 s69, s17, 0
mm v[120:123],v[212:215],a[36:39],v[120:123]
rt v[208:209],v242 offset:16896
rt v[210:211],v242 offset:17920
mm v[124:127],v[220:223],a[36:39],v[124:127]
s_add_u32 m0, s83, 34816
s_and_b32 s69, s69, 0xffff
mm v[124:127],v[216:219],a[48:51],v[124:127]
ld v225,s[56:59],s80 offen lds
mm v[92:95],v[216:219],a[44:47],v[92:95]
s_mov_b32 s70, 4096
s_mov_b32 s71, 0x20000
mm v[92:95],v[220:223],a[72:75],v[92:95]
rt v[212:213],v242 offset:25344
rt v[214:215],v242 offset:26368
mm v[60:63],v[220:223],a[68:71],v[60:63]
s_add_u32 m0, s83, 35968
aw a137,v236
mm v[60:63],v[216:219],a[40:43],v[60:63]
ld v226,s[56:59],s80 offen lds
mm v[28:31],v[216:219],a[32:35],v[28:31]
wt vmcnt(16) lgkmcnt(4)
aw a141,v237
mm v[28:31],v[220:223],a[64:67],v[28:31]
rt v[216:217],v243 offset:16896
rt v[218:219],v243 offset:17920
mm v[0:3],v[192:195],a[52:55],v[0:3]
s_add_u32 m0, s83, 36992
ar v230,a177
mm v[0:3],v[196:199],a[80:83],v[0:3]
ld v227,s[56:59],s80 offen lds
mm v[32:35],v[196:199],a[84:87],v[32:35]
s_bitcmp1_b32 vcc_lo, 4
s_cbranch_scc0 .La5_LO_NC4
.La5_LO_KC4:
mm v[32:35],v[192:195],a[56:59],v[32:35]
rt v[220:221],v243 offset:25344
rt v[222:223],v243 offset:26368
mm v[64:67],v[192:195],a[60:63],v[64:67]
s_bfe_u32 s80, s4, 262164
s_xor_b32 s80, s80, s72
mm v[64:67],v[196:199],a[88:91],v[64:67]
ld a[0:3],v244,s[44:47],s52 offen offset:0 sc1
mm v[96:99],v[196:199],a[92:95],v[96:99]
s_lshl_b32 s80, s80, 18
ar v231,a181
mm v[96:99],v[192:195],a[76:79],v[96:99]
rt v[196:197],v240 offset:17024
rt v[198:199],v240 offset:18048
mm v[100:103],v[200:203],a[76:79],v[100:103]
s_add_u32 s48, s36, 0
s_addc_u32 s49, s37, 0
mm v[100:103],v[204:207],a[92:95],v[100:103]
ld a[4:7],v245,s[44:47],s52 offen offset:0 sc1
mm v[68:71],v[204:207],a[88:91],v[68:71]
s_and_b32 s49, s49, 0xffff
s_mov_b32 s50, 2048
mm v[68:71],v[200:203],a[60:63],v[68:71]
rt v[192:193],v240 offset:25472
rt v[194:195],v240 offset:26496
mm v[36:39],v[200:203],a[56:59],v[36:39]
s_mov_b32 s51, 0x20000
v_and_b32 v238, 0xf0, v255
mm v[36:39],v[204:207],a[84:87],v[36:39]
ld a[8:11],v246,s[44:47],s52 offen offset:0 sc1
mm v[4:7],v[204:207],a[80:83],v[4:7]
wt lgkmcnt(4)
v_lshlrev_b32 v238, 2, v238
mm v[4:7],v[200:203],a[52:55],v[4:7]
rt v[204:205],v241 offset:17024
rt v[206:207],v241 offset:18048
mm v[8:11],v[208:211],a[52:55],v[8:11]
v_bfe_u32 v249, v255, 8, 1
v_lshl_add_u32 v238, v249, 5, v238
mm v[8:11],v[212:215],a[80:83],v[8:11]
ld a[12:15],v247,s[44:47],s52 offen offset:0 sc1
mm v[40:43],v[212:215],a[84:87],v[40:43]
v_lshrrev_b32_e64 v249, 9, v255
v_lshl_add_u32 v238, v249, 4, v238
mm v[40:43],v[208:211],a[56:59],v[40:43]
rt v[200:201],v241 offset:25472
rt v[202:203],v241 offset:26496
mm v[72:75],v[208:211],a[60:63],v[72:75]
s_lshr_b32 s23, s72, 3
s_lshl3_add_u32 s92, s23, s76
mm v[72:75],v[212:215],a[88:91],v[72:75]
ld a[16:19],v244,s[44:47],s52 offen offset:1024 sc1
mm v[104:107],v[212:215],a[92:95],v[104:107]
s_lshl_b32 s92, s92, 19
s_lshl_b32 s97, s3, 16
mm v[104:107],v[208:211],a[76:79],v[104:107]
rt v[212:213],v242 offset:17024
rt v[214:215],v242 offset:18048
mm v[108:111],v[216:219],a[76:79],v[108:111]
s_add_u32 s94, s92, s97
s_add_u32 s95, s94, 32768
mm v[108:111],v[220:223],a[92:95],v[108:111]
ld a[20:23],v245,s[44:47],s52 offen offset:1024 sc1
mm v[76:79],v[220:223],a[88:91],v[76:79]
s_sub_u32 s96, s92, s97
s_add_u32 s96, s96, 458752
mm v[76:79],v[216:219],a[60:63],v[76:79]
rt v[208:209],v242 offset:25472
rt v[210:211],v242 offset:26496
mm v[44:47],v[216:219],a[56:59],v[44:47]
s_add_u32 s97, s96, 32768
s_lshl3_add_u32 s92, s23, s101
mm v[44:47],v[220:223],a[84:87],v[44:47]
s_lshl_b32 s85, s76, 14
s_lshl3_add_u32 s92, s92, s85
mm v[12:15],v[220:223],a[80:83],v[12:15]
wt lgkmcnt(4)
s_add_u32 s92, s92, 2097152
mm v[12:15],v[216:219],a[52:55],v[12:15]
rt v[220:221],v243 offset:17024
rt v[222:223],v243 offset:18048
mm v[16:19],v[196:199],a[52:55],v[16:19]
v_perm_b32 v232, v231, v230, s86
v_perm_b32 v233, v231, v230, s87
mm v[16:19],v[192:195],a[80:83],v[16:19]
ld a[24:27],v246,s[44:47],s52 offen offset:1024 sc1
mm v[48:51],v[192:195],a[84:87],v[48:51]
aw a138,v232
aw a142,v233
mm v[48:51],v[196:199],a[56:59],v[48:51]
rt v[216:217],v243 offset:25472
rt v[218:219],v243 offset:26496
mm v[80:83],v[196:199],a[60:63],v[80:83]
ar v234,a185
ar v235,a189
mm v[80:83],v[192:195],a[88:91],v[80:83]
ld a[28:31],v247,s[44:47],s52 offen offset:1024 sc1
mm v[112:115],v[192:195],a[92:95],v[112:115]
v_perm_b32 v236, v235, v234, s86
v_perm_b32 v237, v235, v234, s87
mm v[112:115],v[196:199],a[76:79],v[112:115]
rt v[192:193],v240 offset:33792
rt v[194:195],v240 offset:34816
mm v[116:119],v[204:207],a[76:79],v[116:119]
s_add_u32 m0, s83, 50688
aw a139,v236
mm v[116:119],v[200:203],a[92:95],v[116:119]
ld v224,s[56:59],s80 offen lds
mm v[84:87],v[200:203],a[88:91],v[84:87]
aw a143,v237
ar v230,a162
mm v[84:87],v[204:207],a[60:63],v[84:87]
rt v[196:197],v240 offset:42240
rt v[198:199],v240 offset:43264
mm v[52:55],v[204:207],a[56:59],v[52:55]
s_add_u32 m0, s83, 51712
ar v231,a166
mm v[52:55],v[200:203],a[84:87],v[52:55]
ld v225,s[56:59],s80 offen lds
mm v[20:23],v[200:203],a[80:83],v[20:23]
wt lgkmcnt(4)
v_perm_b32 v232, v231, v230, s86
mm v[20:23],v[204:207],a[52:55],v[20:23]
rt v[200:201],v241 offset:33792
rt v[202:203],v241 offset:34816
mm v[24:27],v[212:215],a[52:55],v[24:27]
s_add_u32 m0, s83, 52864
v_perm_b32 v233, v231, v230, s87
mm v[24:27],v[208:211],a[80:83],v[24:27]
ld v226,s[56:59],s80 offen lds
mm v[56:59],v[208:211],a[84:87],v[56:59]
aw a144,v232
aw a148,v233
mm v[56:59],v[212:215],a[56:59],v[56:59]
rt v[204:205],v241 offset:42240
rt v[206:207],v241 offset:43264
mm v[88:91],v[212:215],a[60:63],v[88:91]
ar v234,a170
ar v235,a174
mm v[88:91],v[208:211],a[88:91],v[88:91]
ld a[32:35],v244,s[44:47],s52 offen offset:2048 sc1
mm v[120:123],v[208:211],a[92:95],v[120:123]
v_perm_b32 v236, v235, v234, s86
v_perm_b32 v237, v235, v234, s87
mm v[120:123],v[212:215],a[76:79],v[120:123]
rt v[208:209],v242 offset:33792
rt v[210:211],v242 offset:34816
mm v[124:127],v[220:223],a[76:79],v[124:127]
aw a145,v236
aw a149,v237
mm v[124:127],v[216:219],a[92:95],v[124:127]
ld a[36:39],v245,s[44:47],s52 offen offset:2048 sc1
mm v[92:95],v[216:219],a[88:91],v[92:95]
ar v230,a178
ar v231,a182
mm v[92:95],v[220:223],a[60:63],v[92:95]
rt v[212:213],v242 offset:42240
rt v[214:215],v242 offset:43264
mm v[60:63],v[220:223],a[56:59],v[60:63]
v_perm_b32 v232, v231, v230, s86
v_perm_b32 v233, v231, v230, s87
mm v[60:63],v[216:219],a[84:87],v[60:63]
aw a146,v232
aw a150,v233
mm v[28:31],v[216:219],a[80:83],v[28:31]
wt vmcnt(18) lgkmcnt(4)
ar v234,a186
mm v[28:31],v[220:223],a[52:55],v[28:31]
rt v[216:217],v243 offset:33792
rt v[218:219],v243 offset:34816
mm v[0:3],v[192:195],v[136:139],v[0:3]
ar v235,a190
v_perm_b32 v236, v235, v234, s86
mm v[0:3],v[196:199],v[168:171],v[0:3]
ld a[40:43],v246,s[44:47],s52 offen offset:2048 sc1
mm v[32:35],v[196:199],v[172:175],v[32:35]
v_perm_b32 v237, v235, v234, s87
aw a147,v236
mm v[32:35],v[192:195],v[140:143],v[32:35]
rt v[220:221],v243 offset:42240
rt v[222:223],v243 offset:43264
mm v[64:67],v[192:195],v[152:155],v[64:67]
aw a151,v237
ar v230,a163
mm v[64:67],v[196:199],v[184:187],v[64:67]
ld a[44:47],v247,s[44:47],s52 offen offset:2048 sc1
mm v[96:99],v[196:199],v[188:191],v[96:99]
ar v231,a167
v_perm_b32 v232, v231, v230, s86
mm v[96:99],v[192:195],v[156:159],v[96:99]
rt v[196:197],v240 offset:33920
rt v[198:199],v240 offset:34944
mm v[100:103],v[200:203],v[156:159],v[100:103]
v_perm_b32 v233, v231, v230, s87
aw a152,v232
mm v[100:103],v[204:207],v[188:191],v[100:103]
ld a[48:51],v244,s[44:47],s52 offen offset:3072 sc1
mm v[68:71],v[204:207],v[184:187],v[68:71]
aw a156,v233
ar v234,a171
mm v[68:71],v[200:203],v[152:155],v[68:71]
rt v[192:193],v240 offset:42368
rt v[194:195],v240 offset:43392
mm v[36:39],v[200:203],v[140:143],v[36:39]
v_add_u32 v240, 4294899712, v240
ar v235,a175
mm v[36:39],v[204:207],v[172:175],v[36:39]
ld a[52:55],v245,s[44:47],s52 offen offset:3072 sc1
mm v[4:7],v[204:207],v[168:171],v[4:7]
wt lgkmcnt(4)
v_perm_b32 v236, v235, v234, s86
mm v[4:7],v[200:203],v[136:139],v[4:7]
rt v[204:205],v241 offset:33920
rt v[206:207],v241 offset:34944
mm v[8:11],v[208:211],v[136:139],v[8:11]
s_nop 0
v_cmp_eq_u64 vcc, v[250:251], v[252:253]
mm v[8:11],v[212:215],v[168:171],v[8:11]
ld a[56:59],v246,s[44:47],s52 offen offset:3072 sc1
mm v[40:43],v[212:215],v[172:175],v[40:43]
v_perm_b32 v237, v235, v234, s87
aw a153,v236
mm v[40:43],v[208:211],v[140:143],v[40:43]
rt v[200:201],v241 offset:42368
rt v[202:203],v241 offset:43392
mm v[72:75],v[208:211],v[152:155],v[72:75]
v_add_u32 v241, 4294899712, v241
aw a157,v237
mm v[72:75],v[212:215],v[184:187],v[72:75]
ld a[60:63],v247,s[44:47],s52 offen offset:3072 sc1
mm v[104:107],v[212:215],v[188:191],v[104:107]
ar v230,a179
ar v231,a183
mm v[104:107],v[208:211],v[156:159],v[104:107]
rt v[212:213],v242 offset:33920
rt v[214:215],v242 offset:34944
mm v[108:111],v[216:219],v[156:159],v[108:111]
v_perm_b32 v232, v231, v230, s86
v_perm_b32 v233, v231, v230, s87
mm v[108:111],v[220:223],v[188:191],v[108:111]
buffer_load_dwordx2 v[250:251], v248, s[64:67], s5 offen sc1
mm v[76:79],v[220:223],v[184:187],v[76:79]
aw a154,v232
aw a158,v233
mm v[76:79],v[216:219],v[152:155],v[76:79]
rt v[208:209],v242 offset:42368
rt v[210:211],v242 offset:43392
mm v[44:47],v[216:219],v[140:143],v[44:47]
s_add_u32 m0, s83, 53888
v_add_u32 v242, 4294899712, v242
mm v[44:47],v[220:223],v[172:175],v[44:47]
ld v227,s[56:59],s80 offen lds
mm v[12:15],v[220:223],v[168:171],v[12:15]
wt lgkmcnt(4)
s_bfe_u32 s80, s4, 262168
mm v[12:15],v[216:219],v[136:139],v[12:15]
rt v[220:221],v243 offset:33920
rt v[222:223],v243 offset:34944
mm v[16:19],v[196:199],v[136:139],v[16:19]
s_add_u32 m0, s83, 118272
s_xor_b32 s80, s80, s72
s_lshl_b32 s80, s80, 18
mm v[16:19],v[192:195],v[168:171],v[16:19]
ld v224,s[56:59],s80 offen lds
mm v[48:51],v[192:195],v[172:175],v[48:51]
ar v234,a187
ar v235,a191
mm v[48:51],v[196:199],v[140:143],v[48:51]
rt v[216:217],v243 offset:42368
rt v[218:219],v243 offset:43392
mm v[80:83],v[196:199],v[152:155],v[80:83]
s_add_u32 m0, s83, 119296
v_add_u32 v243, 4294899712, v243
mm v[80:83],v[192:195],v[184:187],v[80:83]
ld v225,s[56:59],s80 offen lds
mm v[112:115],v[192:195],v[188:191],v[112:115]
wt vmcnt(23) lgkmcnt(0)
s_barrier
mm v[112:115],v[196:199],v[156:159],v[112:115]
rt v[192:193],v240 offset:16896
rt v[194:195],v240 offset:17920
mm v[116:119],v[204:207],v[156:159],v[116:119]
s_add_u32 m0, s83, 120448
v_perm_b32 v236, v235, v234, s86
mm v[116:119],v[200:203],v[188:191],v[116:119]
ld v226,s[56:59],s80 offen lds
mm v[84:87],v[200:203],v[184:187],v[84:87]
s_bitcmp1_b32 vcc_lo, 5
s_cbranch_scc0 .La5_LO_NC5
.La5_LO_KC5:
mm v[84:87],v[204:207],v[152:155],v[84:87]
rt v[196:197],v240 offset:25344
rt v[198:199],v240 offset:26368
mm v[52:55],v[204:207],v[140:143],v[52:55]
v_perm_b32 v237, v235, v234, s87
aw a155,v236
mm v[52:55],v[200:203],v[172:175],v[52:55]
ld v[128:131],v244,s[44:47],s53 offen offset:0 sc1
mm v[20:23],v[200:203],v[168:171],v[20:23]
s_nop 0
aw a159,v237
mm v[20:23],v[204:207],v[136:139],v[20:23]
rt v[200:201],v241 offset:16896
rt v[202:203],v241 offset:17920
mm v[24:27],v[212:215],v[136:139],v[24:27]
ar v230,a192
ar v231,a196
mm v[24:27],v[208:211],v[168:171],v[24:27]
ld v[132:135],v245,s[44:47],s53 offen offset:0 sc1
mm v[56:59],v[208:211],v[172:175],v[56:59]
v_perm_b32 v232, v231, v230, s86
v_perm_b32 v233, v231, v230, s87
mm v[56:59],v[212:215],v[140:143],v[56:59]
rt v[204:205],v241 offset:25344
rt v[206:207],v241 offset:26368
mm v[88:91],v[212:215],v[152:155],v[88:91]
aw a160,v232
aw a164,v233
mm v[88:91],v[208:211],v[184:187],v[88:91]
ld v[144:147],v246,s[44:47],s53 offen offset:0 sc1
mm v[120:123],v[208:211],v[188:191],v[120:123]
ar v234,a200
ar v235,a204
mm v[120:123],v[212:215],v[156:159],v[120:123]
rt v[208:209],v242 offset:16896
rt v[210:211],v242 offset:17920
mm v[124:127],v[220:223],v[156:159],v[124:127]
v_perm_b32 v236, v235, v234, s86
v_perm_b32 v237, v235, v234, s87
mm v[124:127],v[216:219],v[188:191],v[124:127]
ld v[156:159],v247,s[44:47],s53 offen offset:0 sc1
mm v[92:95],v[216:219],v[184:187],v[92:95]
aw a161,v236
aw a165,v237
mm v[92:95],v[220:223],v[152:155],v[92:95]
rt v[212:213],v242 offset:25344
rt v[214:215],v242 offset:26368
mm v[60:63],v[220:223],v[140:143],v[60:63]
ar v230,a208
ar v231,a212
mm v[60:63],v[216:219],v[172:175],v[60:63]
v_perm_b32 v232, v231, v230, s86
v_perm_b32 v233, v231, v230, s87
mm v[28:31],v[216:219],v[168:171],v[28:31]
wt vmcnt(20) lgkmcnt(4)
aw a162,v232
mm v[28:31],v[220:223],v[136:139],v[28:31]
rt v[216:217],v243 offset:16896
rt v[218:219],v243 offset:17920
mm v[0:3],v[192:195],a[0:3],v[0:3]
aw a166,v233
ar v234,a216
mm v[0:3],v[196:199],a[16:19],v[0:3]
ld v[136:139],v244,s[44:47],s53 offen offset:1024 sc1
mm v[32:35],v[196:199],a[20:23],v[32:35]
ar v235,a220
v_perm_b32 v236, v235, v234, s86
mm v[32:35],v[192:195],a[4:7],v[32:35]
rt v[220:221],v243 offset:25344
.La5_LW7_3:
s_cbranch_execz .La5_LW7_7
rt v[222:223],v243 offset:26368
mm v[64:67],v[192:195],a[8:11],v[64:67]
v_perm_b32 v237, v235, v234, s87
aw a163,v236
mm v[64:67],v[196:199],a[24:27],v[64:67]
ld v[140:143],v245,s[44:47],s53 offen offset:1024 sc1
mm v[96:99],v[196:199],a[28:31],v[96:99]
aw a167,v237
ar v230,a193
mm v[96:99],v[192:195],a[12:15],v[96:99]
rt v[196:197],v240 offset:17024
rt v[198:199],v240 offset:18048
mm v[100:103],v[200:203],a[12:15],v[100:103]
ar v231,a197
v_perm_b32 v232, v231, v230, s86
mm v[100:103],v[204:207],a[28:31],v[100:103]
ld v[152:155],v246,s[44:47],s53 offen offset:1024 sc1
mm v[68:71],v[204:207],a[24:27],v[68:71]
v_perm_b32 v233, v231, v230, s87
aw a168,v232
mm v[68:71],v[200:203],a[8:11],v[68:71]
rt v[192:193],v240 offset:25472
rt v[194:195],v240 offset:26496
mm v[36:39],v[200:203],a[4:7],v[36:39]
aw a172,v233
ar v234,a201
mm v[36:39],v[204:207],a[20:23],v[36:39]
ld v[168:171],v247,s[44:47],s53 offen offset:1024 sc1
mm v[4:7],v[204:207],a[16:19],v[4:7]
wt lgkmcnt(4)
ar v235,a205
mm v[4:7],v[200:203],a[0:3],v[4:7]
rt v[204:205],v241 offset:17024
rt v[206:207],v241 offset:18048
mm v[8:11],v[208:211],a[0:3],v[8:11]
s_add_u32 m0, s83, 121472
v_perm_b32 v236, v235, v234, s86
mm v[8:11],v[212:215],a[16:19],v[8:11]
ld v227,s[56:59],s80 offen lds
mm v[40:43],v[212:215],a[20:23],v[40:43]
s_add_u32 s80, s80, 131072
v_perm_b32 v237, v235, v234, s87
mm v[40:43],v[208:211],a[4:7],v[40:43]
rt v[200:201],v241 offset:25472
rt v[202:203],v241 offset:26496
mm v[72:75],v[208:211],a[8:11],v[72:75]
s_add_u32 m0, s83, 67584
aw a169,v236
mm v[72:75],v[212:215],a[24:27],v[72:75]
ld v224,s[56:59],s80 offen lds
mm v[104:107],v[212:215],a[28:31],v[104:107]
aw a173,v237
ar v230,a209
mm v[104:107],v[208:211],a[12:15],v[104:107]
rt v[212:213],v242 offset:17024
rt v[214:215],v242 offset:18048
mm v[108:111],v[216:219],a[12:15],v[108:111]
s_add_u32 m0, s83, 68608
ar v231,a213
mm v[108:111],v[220:223],a[28:31],v[108:111]
ld v225,s[56:59],s80 offen lds
mm v[76:79],v[220:223],a[24:27],v[76:79]
v_perm_b32 v232, v231, v230, s86
v_perm_b32 v233, v231, v230, s87
mm v[76:79],v[216:219],a[8:11],v[76:79]
rt v[208:209],v242 offset:25472
rt v[210:211],v242 offset:26496
mm v[44:47],v[216:219],a[4:7],v[44:47]
aw a170,v232
aw a174,v233
mm v[44:47],v[220:223],a[20:23],v[44:47]
ar v234,a217
ar v235,a221
mm v[12:15],v[220:223],a[16:19],v[12:15]
wt lgkmcnt(4)
v_perm_b32 v236, v235, v234, s86
mm v[12:15],v[216:219],a[0:3],v[12:15]
rt v[220:221],v243 offset:17024
rt v[222:223],v243 offset:18048
mm v[16:19],v[196:199],a[0:3],v[16:19]
s_add_u32 m0, s83, 69760
v_perm_b32 v237, v235, v234, s87
mm v[16:19],v[192:195],a[16:19],v[16:19]
ld v226,s[56:59],s80 offen lds
mm v[48:51],v[192:195],a[20:23],v[48:51]
aw a171,v236
aw a175,v237
mm v[48:51],v[196:199],a[4:7],v[48:51]
rt v[216:217],v243 offset:25472
rt v[218:219],v243 offset:26496
mm v[80:83],v[196:199],a[8:11],v[80:83]
s_add_u32 m0, s83, 70784
ar v230,a194
mm v[80:83],v[192:195],a[24:27],v[80:83]
ld v227,s[56:59],s80 offen lds
mm v[112:115],v[192:195],a[28:31],v[112:115]
s_bfe_u32 s80, s4, 262172
s_xor_b32 s80, s80, s72
mm v[112:115],v[196:199],a[12:15],v[112:115]
rt v[192:193],v240 offset:33792
rt v[194:195],v240 offset:34816
mm v[116:119],v[204:207],a[12:15],v[116:119]
s_add_u32 m0, s83, 84480
s_lshl_b32 s80, s80, 18
mm v[116:119],v[200:203],a[28:31],v[116:119]
ld v224,s[56:59],s80 offen lds
mm v[84:87],v[200:203],a[24:27],v[84:87]
ar v231,a198
v_perm_b32 v232, v231, v230, s86
mm v[84:87],v[204:207],a[8:11],v[84:87]
rt v[196:197],v240 offset:42240
rt v[198:199],v240 offset:43264
mm v[52:55],v[204:207],a[4:7],v[52:55]
s_add_u32 m0, s83, 85504
v_perm_b32 v233, v231, v230, s87
mm v[52:55],v[200:203],a[20:23],v[52:55]
ld v225,s[56:59],s80 offen lds
mm v[20:23],v[200:203],a[16:19],v[20:23]
wt lgkmcnt(4)
aw a176,v232
mm v[20:23],v[204:207],a[0:3],v[20:23]
rt v[200:201],v241 offset:33792
rt v[202:203],v241 offset:34816
mm v[24:27],v[212:215],a[0:3],v[24:27]
s_add_u32 m0, s83, 86656
aw a180,v233
mm v[24:27],v[208:211],a[16:19],v[24:27]
ld v226,s[56:59],s80 offen lds
mm v[56:59],v[208:211],a[20:23],v[56:59]
ar v234,a202
ar v235,a206
mm v[56:59],v[212:215],a[4:7],v[56:59]
rt v[204:205],v241 offset:42240
rt v[206:207],v241 offset:43264
mm v[88:91],v[212:215],a[8:11],v[88:91]
s_add_u32 m0, s83, 87680
v_perm_b32 v236, v235, v234, s86
mm v[88:91],v[208:211],a[24:27],v[88:91]
ld v227,s[56:59],s80 offen lds
mm v[120:123],v[208:211],a[28:31],v[120:123]
s_add_u32 s80, s80, 131072
v_perm_b32 v237, v235, v234, s87
mm v[120:123],v[212:215],a[12:15],v[120:123]
rt v[208:209],v242 offset:33792
rt v[210:211],v242 offset:34816
mm v[124:127],v[220:223],a[12:15],v[124:127]
s_add_u32 m0, s83, 101376
aw a177,v236
mm v[124:127],v[216:219],a[28:31],v[124:127]
ld v224,s[56:59],s80 offen lds
mm v[92:95],v[216:219],a[24:27],v[92:95]
aw a181,v237
ar v230,a210
mm v[92:95],v[220:223],a[8:11],v[92:95]
rt v[212:213],v242 offset:42240
rt v[214:215],v242 offset:43264
mm v[60:63],v[220:223],a[4:7],v[60:63]
ar v231,a214
v_perm_b32 v232, v231, v230, s86
mm v[60:63],v[216:219],a[20:23],v[60:63]
wt vmcnt(22)
v_cmp_eq_u64 vcc, v[250:251], v[252:253]
mm v[28:31],v[216:219],a[16:19],v[28:31]
wt lgkmcnt(4)
v_perm_b32 v233, v231, v230, s87
mm v[28:31],v[220:223],a[0:3],v[28:31]
rt v[216:217],v243 offset:33792
rt v[218:219],v243 offset:34816
mm v[0:3],v[192:195],a[32:35],v[0:3]
s_add_u32 m0, s83, 102400
aw a178,v232
mm v[0:3],v[196:199],a[48:51],v[0:3]
ld v225,s[56:59],s80 offen lds
mm v[32:35],v[196:199],a[52:55],v[32:35]
aw a182,v233
ar v234,a218
mm v[32:35],v[192:195],a[36:39],v[32:35]
rt v[220:221],v243 offset:42240
rt v[222:223],v243 offset:43264
mm v[64:67],v[192:195],a[40:43],v[64:67]
s_add_u32 m0, s83, 103552
ar v235,a222
mm v[64:67],v[196:199],a[56:59],v[64:67]
ld v226,s[56:59],s80 offen lds
mm v[96:99],v[196:199],a[60:63],v[96:99]
v_perm_b32 v236, v235, v234, s86
v_perm_b32 v237, v235, v234, s87
mm v[96:99],v[192:195],a[44:47],v[96:99]
rt v[196:197],v240 offset:33920
rt v[198:199],v240 offset:34944
mm v[100:103],v[200:203],a[44:47],v[100:103]
s_add_u32 m0, s83, 104576
aw a179,v236
mm v[100:103],v[204:207],a[60:63],v[100:103]
ld v227,s[56:59],s80 offen lds
mm v[68:71],v[204:207],a[56:59],v[68:71]
s_bitcmp1_b32 vcc_lo, 6
s_cbranch_scc0 .La5_LO_NC6
.La5_LO_KC6:
mm v[68:71],v[200:203],a[40:43],v[68:71]
rt v[192:193],v240 offset:42368
rt v[194:195],v240 offset:43392
mm v[36:39],v[200:203],a[36:39],v[36:39]
v_bfe_u32 v224, v255, 6, 1
v_lshrrev_b32 v225, 7, v255
mm v[36:39],v[204:207],a[52:55],v[36:39]
ld a[64:67],v244,s[44:47],s54 offen offset:0 sc1
mm v[4:7],v[204:207],a[48:51],v[4:7]
wt lgkmcnt(4)
v_and_b32 v226, 48, v255
mm v[4:7],v[200:203],a[32:35],v[4:7]
rt v[204:205],v241 offset:33920
rt v[206:207],v241 offset:34944
mm v[8:11],v[208:211],a[32:35],v[8:11]
v_lshl_add_u32 v225, v225, 6, v226
v_lshl_add_u32 v224, v224, 11, v225
mm v[8:11],v[212:215],a[48:51],v[8:11]
ld a[68:71],v245,s[44:47],s54 offen offset:0 sc1
mm v[40:43],v[212:215],a[52:55],v[40:43]
aw a183,v237
ar v230,a195
mm v[40:43],v[208:211],a[36:39],v[40:43]
rt v[200:201],v241 offset:42368
rt v[202:203],v241 offset:43392
mm v[72:75],v[208:211],a[40:43],v[72:75]
ar v231,a199
v_perm_b32 v232, v231, v230, s86
mm v[72:75],v[212:215],a[56:59],v[72:75]
ld a[72:75],v244,s[44:47],s54 offen offset:1024 sc1
mm v[104:107],v[212:215],a[60:63],v[104:107]
v_perm_b32 v233, v231, v230, s87
aw a184,v232
mm v[104:107],v[208:211],a[44:47],v[104:107]
rt v[212:213],v242 offset:33920
rt v[214:215],v242 offset:34944
mm v[108:111],v[216:219],a[44:47],v[108:111]
aw a188,v233
ar v234,a203
mm v[108:111],v[220:223],a[60:63],v[108:111]
ld a[76:79],v245,s[44:47],s54 offen offset:1024 sc1
mm v[76:79],v[220:223],a[56:59],v[76:79]
ar v235,a207
v_perm_b32 v236, v235, v234, s86
mm v[76:79],v[216:219],a[40:43],v[76:79]
rt v[208:209],v242 offset:42368
rt v[210:211],v242 offset:43392
mm v[44:47],v[216:219],a[36:39],v[44:47]
v_perm_b32 v237, v235, v234, s87
aw a185,v236
mm v[44:47],v[220:223],a[52:55],v[44:47]
aw a189,v237
ar v230,a211
mm v[12:15],v[220:223],a[48:51],v[12:15]
wt lgkmcnt(4)
ar v231,a215
mm v[12:15],v[216:219],a[32:35],v[12:15]
rt v[220:221],v243 offset:33920
rt v[222:223],v243 offset:34944
mm v[16:19],v[196:199],a[32:35],v[16:19]
v_perm_b32 v232, v231, v230, s86
v_perm_b32 v233, v231, v230, s87
mm v[16:19],v[192:195],a[48:51],v[16:19]
ld a[80:83],v244,s[44:47],s54 offen offset:2048 sc1
mm v[48:51],v[192:195],a[52:55],v[48:51]
aw a186,v232
aw a190,v233
mm v[48:51],v[196:199],a[36:39],v[48:51]
rt v[216:217],v243 offset:42368
rt v[218:219],v243 offset:43392
mm v[80:83],v[196:199],a[40:43],v[80:83]
ar v234,a219
ar v235,a223
mm v[80:83],v[192:195],a[56:59],v[80:83]
ld a[84:87],v245,s[44:47],s54 offen offset:2048 sc1
mm v[112:115],v[192:195],a[60:63],v[112:115]
wt vmcnt(30)
s_barrier
mm v[112:115],v[196:199],a[44:47],v[112:115]
rt v[192:193],v240 offset:50688
rt v[194:195],v240 offset:51712
mm v[116:119],v[204:207],a[44:47],v[116:119]
v_perm_b32 v236, v235, v234, s86
v_perm_b32 v237, v235, v234, s87
mm v[116:119],v[200:203],a[60:63],v[116:119]
ld a[88:91],v244,s[44:47],s54 offen offset:3072 sc1
mm v[84:87],v[200:203],a[56:59],v[84:87]
aw a187,v236
aw a191,v237
mm v[84:87],v[204:207],a[40:43],v[84:87]
rt v[196:197],v240 offset:59136
rt v[198:199],v240 offset:60160
mm v[52:55],v[204:207],a[36:39],v[52:55]
s_bitcmp1_b32 vcc_lo, 7
s_cbranch_scc0 .La5_LO_NC7
.La5_LO_KC7:
mm v[52:55],v[200:203],a[52:55],v[52:55]
ld a[92:95],v245,s[44:47],s54 offen offset:3072 sc1
mm v[20:23],v[200:203],a[48:51],v[20:23]
wt lgkmcnt(4)
ar v230,a224
mm v[20:23],v[204:207],a[32:35],v[20:23]
rt v[200:201],v241 offset:50688
rt v[202:203],v241 offset:51712
mm v[24:27],v[212:215],a[32:35],v[24:27]
ar v231,a228
v_perm_b32 v232, v231, v230, s86
mm v[24:27],v[208:211],a[48:51],v[24:27]
ld a[0:3],v244,s[44:47],s55 offen offset:0 sc1
mm v[56:59],v[208:211],a[52:55],v[56:59]
v_perm_b32 v233, v231, v230, s87
aw a192,v232
mm v[56:59],v[212:215],a[36:39],v[56:59]
rt v[204:205],v241 offset:59136
rt v[206:207],v241 offset:60160
mm v[88:91],v[212:215],a[40:43],v[88:91]
aw a196,v233
ar v234,a232
mm v[88:91],v[208:211],a[56:59],v[88:91]
ld a[4:7],v245,s[44:47],s55 offen offset:0 sc1
mm v[120:123],v[208:211],a[60:63],v[120:123]
ar v235,a236
v_perm_b32 v236, v235, v234, s86
mm v[120:123],v[212:215],a[44:47],v[120:123]
rt v[208:209],v242 offset:50688
rt v[210:211],v242 offset:51712
mm v[124:127],v[220:223],a[44:47],v[124:127]
v_perm_b32 v237, v235, v234, s87
aw a193,v236
mm v[124:127],v[216:219],a[60:63],v[124:127]
ld a[8:11],v244,s[44:47],s55 offen offset:1024 sc1
mm v[92:95],v[216:219],a[56:59],v[92:95]
aw a197,v237
ar v230,a240
mm v[92:95],v[220:223],a[40:43],v[92:95]
rt v[212:213],v242 offset:59136
rt v[214:215],v242 offset:60160
mm v[60:63],v[220:223],a[36:39],v[60:63]
ar v231,a244
v_perm_b32 v232, v231, v230, s86
mm v[60:63],v[216:219],a[52:55],v[60:63]
v_perm_b32 v233, v231, v230, s87
aw a194,v232
mm v[28:31],v[216:219],a[48:51],v[28:31]
wt vmcnt(24) lgkmcnt(4)
aw a198,v233
mm v[28:31],v[220:223],a[32:35],v[28:31]
rt v[216:217],v243 offset:50688
rt v[218:219],v243 offset:51712
mm v[0:3],v[192:195],v[128:131],v[0:3]
ar v234,a248
ar v235,a252
mm v[0:3],v[196:199],v[136:139],v[0:3]
ld a[12:15],v245,s[44:47],s55 offen offset:1024 sc1
mm v[32:35],v[196:199],v[140:143],v[32:35]
v_perm_b32 v236, v235, v234, s86
v_perm_b32 v237, v235, v234, s87
mm v[32:35],v[192:195],v[132:135],v[32:35]
rt v[220:221],v243 offset:59136
rt v[222:223],v243 offset:60160
mm v[64:67],v[192:195],v[144:147],v[64:67]
aw a195,v236
aw a199,v237
mm v[64:67],v[196:199],v[152:155],v[64:67]
ld a[16:19],v244,s[44:47],s55 offen offset:2048 sc1
mm v[96:99],v[196:199],v[168:171],v[96:99]
ar v230,a225
ar v231,a229
mm v[96:99],v[192:195],v[156:159],v[96:99]
rt v[196:197],v240 offset:50816
rt v[198:199],v240 offset:51840
mm v[100:103],v[200:203],v[156:159],v[100:103]
v_perm_b32 v232, v231, v230, s86
v_perm_b32 v233, v231, v230, s87
mm v[100:103],v[204:207],v[168:171],v[100:103]
ld a[20:23],v245,s[44:47],s55 offen offset:2048 sc1
mm v[68:71],v[204:207],v[152:155],v[68:71]
aw a200,v232
aw a204,v233
mm v[68:71],v[200:203],v[144:147],v[68:71]
rt v[192:193],v240 offset:59264
rt v[194:195],v240 offset:60288
mm v[36:39],v[200:203],v[132:135],v[36:39]
v_add_u32 v240, 67584, v240
ar v234,a233
mm v[36:39],v[204:207],v[140:143],v[36:39]
ld a[24:27],v244,s[44:47],s55 offen offset:3072 sc1
mm v[4:7],v[204:207],v[136:139],v[4:7]
wt lgkmcnt(4)
ar v235,a237
mm v[4:7],v[200:203],v[128:131],v[4:7]
rt v[204:205],v241 offset:50816
rt v[206:207],v241 offset:51840
mm v[8:11],v[208:211],v[128:131],v[8:11]
v_perm_b32 v236, v235, v234, s86
v_perm_b32 v237, v235, v234, s87
mm v[8:11],v[212:215],v[136:139],v[8:11]
ld a[28:31],v245,s[44:47],s55 offen offset:3072 sc1
mm v[40:43],v[212:215],v[140:143],v[40:43]
aw a201,v236
aw a205,v237
mm v[40:43],v[208:211],v[132:135],v[40:43]
rt v[200:201],v241 offset:59264
rt v[202:203],v241 offset:60288
mm v[72:75],v[208:211],v[144:147],v[72:75]
v_add_u32 v241, 67584, v241
ar v230,a241
mm v[72:75],v[212:215],v[152:155],v[72:75]
ar v231,a245
v_perm_b32 v232, v231, v230, s86
mm v[104:107],v[212:215],v[168:171],v[104:107]
v_perm_b32 v233, v231, v230, s87
aw a202,v232
mm v[104:107],v[208:211],v[156:159],v[104:107]
rt v[212:213],v242 offset:50816
rt v[214:215],v242 offset:51840
mm v[108:111],v[216:219],v[156:159],v[108:111]
aw a206,v233
ar v234,a249
mm v[108:111],v[220:223],v[168:171],v[108:111]
ar v235,a253
v_perm_b32 v236, v235, v234, s86
mm v[76:79],v[220:223],v[152:155],v[76:79]
v_perm_b32 v237, v235, v234, s87
aw a203,v236
mm v[76:79],v[216:219],v[144:147],v[76:79]
rt v[208:209],v242 offset:59264
rt v[210:211],v242 offset:60288
mm v[44:47],v[216:219],v[132:135],v[44:47]
v_add_u32 v242, 67584, v242
aw a207,v237
mm v[44:47],v[220:223],v[140:143],v[44:47]
ar v230,a226
ar v231,a230
mm v[12:15],v[220:223],v[136:139],v[12:15]
wt lgkmcnt(4)
v_perm_b32 v232, v231, v230, s86
mm v[12:15],v[216:219],v[128:131],v[12:15]
rt v[220:221],v243 offset:50816
rt v[222:223],v243 offset:51840
mm v[16:19],v[196:199],v[128:131],v[16:19]
v_perm_b32 v233, v231, v230, s87
aw a208,v232
mm v[16:19],v[192:195],v[136:139],v[16:19]
ld a[32:35],v246,s[44:47],s54 offen offset:0 sc1
mm v[48:51],v[192:195],v[140:143],v[48:51]
aw a212,v233
ar v234,a234
mm v[48:51],v[196:199],v[132:135],v[48:51]
rt v[216:217],v243 offset:59264
rt v[218:219],v243 offset:60288
mm v[80:83],v[196:199],v[144:147],v[80:83]
v_add_u32 v243, 67584, v243
ar v235,a238
mm v[80:83],v[192:195],v[152:155],v[80:83]
ld a[36:39],v247,s[44:47],s54 offen offset:0 sc1
mm v[112:115],v[192:195],v[168:171],v[112:115]
wt vmcnt(18)
s_barrier
mm v[112:115],v[196:199],v[156:159],v[112:115]
rt v[192:193],v240 offset:50688
rt v[194:195],v240 offset:51712
mm v[116:119],v[204:207],v[156:159],v[116:119]
v_perm_b32 v236, v235, v234, s86
v_perm_b32 v237, v235, v234, s87
mm v[116:119],v[200:203],v[168:171],v[116:119]
ld a[40:43],v246,s[44:47],s54 offen offset:1024 sc1
mm v[84:87],v[200:203],v[152:155],v[84:87]
aw a209,v236
aw a213,v237
mm v[84:87],v[204:207],v[144:147],v[84:87]
rt v[196:197],v240 offset:59136
rt v[198:199],v240 offset:60160
mm v[52:55],v[204:207],v[132:135],v[52:55]
ar v230,a242
ar v231,a246
mm v[52:55],v[200:203],v[140:143],v[52:55]
ld a[44:47],v247,s[44:47],s54 offen offset:1024 sc1
mm v[20:23],v[200:203],v[136:139],v[20:23]
wt lgkmcnt(4)
v_perm_b32 v232, v231, v230, s86
mm v[20:23],v[204:207],v[128:131],v[20:23]
rt v[200:201],v240 offset:0
rt v[202:203],v240 offset:1024
mm v[24:27],v[212:215],v[128:131],v[24:27]
v_perm_b32 v233, v231, v230, s87
aw a210,v232
mm v[24:27],v[208:211],v[136:139],v[24:27]
ld a[48:51],v246,s[44:47],s54 offen offset:2048 sc1
mm v[56:59],v[208:211],v[140:143],v[56:59]
aw a214,v233
ar v234,a250
mm v[56:59],v[212:215],v[132:135],v[56:59]
rt v[204:205],v240 offset:8448
rt v[206:207],v240 offset:9472
mm v[88:91],v[212:215],v[144:147],v[88:91]
ar v235,a254
v_perm_b32 v236, v235, v234, s86
mm v[88:91],v[208:211],v[152:155],v[88:91]
ld a[52:55],v247,s[44:47],s54 offen offset:2048 sc1
mm v[120:123],v[208:211],v[168:171],v[120:123]
v_perm_b32 v237, v235, v234, s87
aw a211,v236
mm v[120:123],v[212:215],v[156:159],v[120:123]
rt v[208:209],v240 offset:16896
rt v[210:211],v240 offset:17920
mm v[124:127],v[220:223],v[156:159],v[124:127]
aw a215,v237
ar v230,a227
mm v[124:127],v[216:219],v[168:171],v[124:127]
ld a[56:59],v246,s[44:47],s54 offen offset:3072 sc1
mm v[92:95],v[216:219],v[152:155],v[92:95]
ar v231,a231
v_perm_b32 v232, v231, v230, s86
mm v[92:95],v[220:223],v[144:147],v[92:95]
rt v[212:213],v240 offset:25344
rt v[214:215],v240 offset:26368
mm v[60:63],v[220:223],v[132:135],v[60:63]
v_perm_b32 v233, v231, v230, s87
aw a216,v232
mm v[60:63],v[216:219],v[140:143],v[60:63]
aw a220,v233
ar v234,a235
mm v[28:31],v[216:219],v[136:139],v[28:31]
wt vmcnt(15) lgkmcnt(4)
ar v235,a239
mm v[28:31],v[220:223],v[128:131],v[28:31]
.La5_LW7_4:
s_cbranch_execz .La5_LA_RETURN
rt v[216:217],v240 offset:33792
rt v[218:219],v240 offset:34816
mm v[0:3],v[192:195],a[64:67],v[0:3]
ld a[60:63],v247,s[44:47],s54 offen offset:3072 sc1
mm v[0:3],v[196:199],a[72:75],v[0:3]
rt v[220:221],v240 offset:42240
rt v[222:223],v240 offset:43264
mm v[32:35],v[196:199],a[76:79],v[32:35]
v_perm_b32 v236, v235, v234, s86
v_perm_b32 v237, v235, v234, s87
mm v[32:35],v[192:195],a[68:71],v[32:35]
rt v[196:197],v241 offset:50688
rt v[198:199],v241 offset:51712
mm v[32:35],v[200:203],a[84:87],v[32:35]
ld v[128:131],v246,s[44:47],s55 offen offset:0 sc1
mm v[32:35],v[204:207],a[92:95],v[32:35]
rt v[192:193],v241 offset:59136
rt v[194:195],v241 offset:60160
mm v[0:3],v[204:207],a[88:91],v[0:3]
wt vmcnt(9) lgkmcnt(4)
aw a217,v236
mm v[0:3],v[200:203],a[80:83],v[0:3]
rt v[204:205],v241 offset:0
rt v[206:207],v241 offset:1024
mm v[0:3],v[208:211],a[0:3],v[0:3]
ld v[132:135],v247,s[44:47],s55 offen offset:0 sc1
mm v[0:3],v[212:215],a[8:11],v[0:3]
rt v[200:201],v241 offset:8448
rt v[202:203],v241 offset:9472
mm v[32:35],v[212:215],a[12:15],v[32:35]
aw a221,v237
ar v230,a243
mm v[32:35],v[208:211],a[4:7],v[32:35]
rt v[212:213],v241 offset:16896
rt v[214:215],v241 offset:17920
mm v[32:35],v[216:219],a[20:23],v[32:35]
ld v[144:147],v246,s[44:47],s55 offen offset:1024 sc1
mm v[32:35],v[220:223],a[28:31],v[32:35]
rt v[208:209],v241 offset:25344
rt v[210:211],v241 offset:26368
mm v[0:3],v[220:223],a[24:27],v[0:3]
wt lgkmcnt(4)
ar v231,a247
mm v[0:3],v[216:219],a[16:19],v[0:3]
rt v[220:221],v241 offset:33792
rt v[222:223],v241 offset:34816
mm v[4:7],v[196:199],a[64:67],v[4:7]
ld v[148:151],v247,s[44:47],s55 offen offset:1024 sc1
mm v[4:7],v[192:195],a[72:75],v[4:7]
rt v[216:217],v241 offset:42240
rt v[218:219],v241 offset:43264
mm v[36:39],v[192:195],a[76:79],v[36:39]
v_perm_b32 v232, v231, v230, s86
v_perm_b32 v233, v231, v230, s87
mm v[36:39],v[196:199],a[68:71],v[36:39]
rt v[192:193],v242 offset:50688
rt v[194:195],v242 offset:51712
mm v[36:39],v[204:207],a[84:87],v[36:39]
ld v[160:163],v246,s[44:47],s55 offen offset:2048 sc1
mm v[36:39],v[200:203],a[92:95],v[36:39]
rt v[196:197],v242 offset:59136
rt v[198:199],v242 offset:60160
aw a218,v232
aw a222,v233
ar v234,a251
ar v235,a255
v_perm_b32 v236, v235, v234, s86
v_perm_b32 v237, v235, v234, s87
aw a219,v236
aw a223,v237
mm v[4:7],v[200:203],a[88:91],v[4:7]
wt lgkmcnt(4)
mm v[4:7],v[204:207],a[80:83],v[4:7]
rt v[200:201],v242 offset:0
rt v[202:203],v242 offset:1024
mm v[4:7],v[212:215],a[0:3],v[4:7]
ld v[164:167],v247,s[44:47],s55 offen offset:2048 sc1
mm v[4:7],v[208:211],a[8:11],v[4:7]
rt v[204:205],v242 offset:8448
rt v[206:207],v242 offset:9472
mm v[36:39],v[208:211],a[12:15],v[36:39]
mm v[36:39],v[212:215],a[4:7],v[36:39]
rt v[208:209],v242 offset:16896
rt v[210:211],v242 offset:17920
mm v[36:39],v[220:223],a[20:23],v[36:39]
mm v[36:39],v[216:219],a[28:31],v[36:39]
rt v[212:213],v242 offset:25344
rt v[214:215],v242 offset:26368
mm v[4:7],v[216:219],a[24:27],v[4:7]
wt lgkmcnt(4)
mm v[4:7],v[220:223],a[16:19],v[4:7]
rt v[216:217],v242 offset:33792
rt v[218:219],v242 offset:34816
mm v[8:11],v[192:195],a[64:67],v[8:11]
ld v[176:179],v246,s[44:47],s55 offen offset:3072 sc1
mm v[8:11],v[196:199],a[72:75],v[8:11]
rt v[220:221],v242 offset:42240
rt v[222:223],v242 offset:43264
mm v[40:43],v[196:199],a[76:79],v[40:43]
cv v234,v32,v33
cv v236,v36,v37
mm v[40:43],v[192:195],a[68:71],v[40:43]
rt v[196:197],v243 offset:50688
rt v[198:199],v243 offset:51712
mm v[40:43],v[200:203],a[84:87],v[40:43]
ld v[180:183],v247,s[44:47],s55 offen offset:3072 sc1
mm v[40:43],v[204:207],a[92:95],v[40:43]
rt v[192:193],v243 offset:59136
rt v[194:195],v243 offset:60160
mm v[8:11],v[204:207],a[88:91],v[8:11]
wt lgkmcnt(4)
v_bfe_u32 v250, v255, 6, 1
mm v[8:11],v[200:203],a[80:83],v[8:11]
rt v[204:205],v243 offset:0
rt v[206:207],v243 offset:1024
mm v[8:11],v[208:211],a[0:3],v[8:11]
cv v235,v34,v35
cv v237,v38,v39
mm v[8:11],v[212:215],a[8:11],v[8:11]
rt v[200:201],v243 offset:8448
rt v[202:203],v243 offset:9472
mm v[40:43],v[212:215],a[12:15],v[40:43]
v_permlane16_swap_b32 v234, v236
v_permlane16_swap_b32 v235, v237
mm v[40:43],v[208:211],a[4:7],v[40:43]
rt v[212:213],v243 offset:16896
rt v[214:215],v243 offset:17920
mm v[40:43],v[216:219],a[20:23],v[40:43]
st v[234:237],v238,s[32:35],s95 offen offset:0 sc1
mm v[40:43],v[220:223],a[28:31],v[40:43]
rt v[208:209],v243 offset:25344
rt v[210:211],v243 offset:26368
mm v[8:11],v[220:223],a[24:27],v[8:11]
wt lgkmcnt(4)
v_mul_u32_u24 v250, 7, v250
mm v[8:11],v[216:219],a[16:19],v[8:11]
rt v[220:221],v243 offset:33792
rt v[222:223],v243 offset:34816
mm v[12:15],v[196:199],a[64:67],v[12:15]
cv v230,v0,v1
cv v232,v4,v5
mm v[12:15],v[192:195],a[72:75],v[12:15]
rt v[216:217],v243 offset:42240
rt v[218:219],v243 offset:43264
mm v[44:47],v[192:195],a[76:79],v[44:47]
cv v231,v2,v3
cv v233,v6,v7
mm v[44:47],v[196:199],a[68:71],v[44:47]
rt v[192:193],v240 offset:50816
rt v[194:195],v240 offset:51840
mm v[44:47],v[204:207],a[84:87],v[44:47]
v_permlane16_swap_b32 v230, v232
v_permlane16_swap_b32 v231, v233
mm v[44:47],v[200:203],a[92:95],v[44:47]
rt v[196:197],v240 offset:59264
rt v[198:199],v240 offset:60288
mm v[12:15],v[200:203],a[88:91],v[12:15]
wt lgkmcnt(4)
v_xor_b32 v250, s3, v250
mm v[12:15],v[204:207],a[80:83],v[12:15]
rt v[200:201],v240 offset:128
rt v[202:203],v240 offset:1152
mm v[12:15],v[212:215],a[0:3],v[12:15]
st v[230:233],v238,s[32:35],s94 offen offset:0 sc1
mm v[12:15],v[208:211],a[8:11],v[12:15]
rt v[204:205],v240 offset:8576
rt v[206:207],v240 offset:9600
mm v[44:47],v[208:211],a[12:15],v[44:47]
v_bfe_u32 v251, v255, 4, 2
v_lshl_add_u32 v250, v250, 2, v251
mm v[44:47],v[212:215],a[4:7],v[44:47]
rt v[208:209],v240 offset:17024
rt v[210:211],v240 offset:18048
mm v[44:47],v[220:223],a[20:23],v[44:47]
ld v[136:139],v224,s[68:71],0 offen offset:0
mm v[44:47],v[216:219],a[28:31],v[44:47]
rt v[212:213],v240 offset:25472
rt v[214:215],v240 offset:26496
mm v[12:15],v[216:219],a[24:27],v[12:15]
wt lgkmcnt(4)
v_lshlrev_b32 v250, 9, v250
mm v[12:15],v[220:223],a[16:19],v[12:15]
rt v[216:217],v240 offset:33920
rt v[218:219],v240 offset:34944
mm v[16:19],v[192:195],a[64:67],v[16:19]
cv v234,v40,v41
cv v236,v44,v45
mm v[16:19],v[196:199],a[72:75],v[16:19]
rt v[220:221],v240 offset:42368
rt v[222:223],v240 offset:43392
mm v[48:51],v[196:199],a[76:79],v[48:51]
cv v235,v42,v43
cv v237,v46,v47
mm v[48:51],v[192:195],a[68:71],v[48:51]
rt v[196:197],v241 offset:50816
rt v[198:199],v241 offset:51840
mm v[48:51],v[200:203],a[84:87],v[48:51]
v_permlane16_swap_b32 v234, v236
v_permlane16_swap_b32 v235, v237
mm v[48:51],v[204:207],a[92:95],v[48:51]
rt v[192:193],v241 offset:59264
rt v[194:195],v241 offset:60288
mm v[16:19],v[204:207],a[88:91],v[16:19]
wt lgkmcnt(4)
v_and_b32 v251, 64, v255
mm v[16:19],v[200:203],a[80:83],v[16:19]
rt v[204:205],v241 offset:128
rt v[206:207],v241 offset:1152
mm v[16:19],v[208:211],a[0:3],v[16:19]
st v[234:237],v238,s[32:35],s95 offen offset:1024 sc1
mm v[16:19],v[212:215],a[8:11],v[16:19]
rt v[200:201],v241 offset:8576
rt v[202:203],v241 offset:9600
mm v[48:51],v[212:215],a[12:15],v[48:51]
cv v230,v8,v9
cv v232,v12,v13
mm v[48:51],v[208:211],a[4:7],v[48:51]
rt v[212:213],v241 offset:17024
rt v[214:215],v241 offset:18048
mm v[48:51],v[216:219],a[20:23],v[48:51]
cv v231,v10,v11
cv v233,v14,v15
mm v[48:51],v[220:223],a[28:31],v[48:51]
rt v[208:209],v241 offset:25472
rt v[210:211],v241 offset:26496
mm v[16:19],v[220:223],a[24:27],v[16:19]
wt lgkmcnt(4)
v_lshl_add_u32 v251, v251, 24, v250
mm v[16:19],v[216:219],a[16:19],v[16:19]
rt v[220:221],v241 offset:33920
rt v[222:223],v241 offset:34944
mm v[20:23],v[196:199],a[64:67],v[20:23]
v_permlane16_swap_b32 v230, v232
v_permlane16_swap_b32 v231, v233
mm v[20:23],v[192:195],a[72:75],v[20:23]
rt v[216:217],v241 offset:42368
rt v[218:219],v241 offset:43392
mm v[52:55],v[192:195],a[76:79],v[52:55]
v_and_b32 v248, 0x1f0, v255
v_lshrrev_b32 v248, 1, v248
mm v[52:55],v[196:199],a[68:71],v[52:55]
rt v[192:193],v242 offset:50816
rt v[194:195],v242 offset:51840
mm v[52:55],v[204:207],a[84:87],v[52:55]
st v[230:233],v238,s[32:35],s94 offen offset:1024 sc1
mm v[52:55],v[200:203],a[92:95],v[52:55]
rt v[196:197],v242 offset:59264
rt v[198:199],v242 offset:60288
mm v[20:23],v[200:203],a[88:91],v[20:23]
wt lgkmcnt(4)
mm v[20:23],v[204:207],a[80:83],v[20:23]
rt v[200:201],v242 offset:128
rt v[202:203],v242 offset:1152
mm v[20:23],v[212:215],a[0:3],v[20:23]
ld v[140:143],v225,s[48:51],0 offen offset:0
mm v[20:23],v[208:211],a[8:11],v[20:23]
rt v[204:205],v242 offset:8576
rt v[206:207],v242 offset:9600
mm v[52:55],v[208:211],a[12:15],v[52:55]
mm v[52:55],v[212:215],a[4:7],v[52:55]
rt v[208:209],v242 offset:17024
rt v[210:211],v242 offset:18048
mm v[52:55],v[220:223],a[20:23],v[52:55]
ld v[152:155],v224,s[68:71],0 offen offset:512
mm v[52:55],v[216:219],a[28:31],v[52:55]
rt v[212:213],v242 offset:25472
rt v[214:215],v242 offset:26496
mm v[20:23],v[216:219],a[24:27],v[20:23]
wt lgkmcnt(4)
s_mov_b32 s93, 0
ld a[224:227],v229,s[28:31],s93 offen offset:0
mm v[20:23],v[220:223],a[16:19],v[20:23]
rt v[216:217],v242 offset:33920
rt v[218:219],v242 offset:34944
mm v[24:27],v[192:195],a[64:67],v[24:27]
cv v234,v48,v49
cv v236,v52,v53
mm v[24:27],v[196:199],a[72:75],v[24:27]
rt v[220:221],v242 offset:42368
rt v[222:223],v242 offset:43392
mm v[56:59],v[196:199],a[76:79],v[56:59]
cv v235,v50,v51
cv v237,v54,v55
mm v[56:59],v[192:195],a[68:71],v[56:59]
rt v[196:197],v243 offset:50816
rt v[198:199],v243 offset:51840
mm v[56:59],v[200:203],a[84:87],v[56:59]
v_permlane16_swap_b32 v234, v236
v_permlane16_swap_b32 v235, v237
mm v[56:59],v[204:207],a[92:95],v[56:59]
rt v[192:193],v243 offset:59264
rt v[194:195],v243 offset:60288
mm v[24:27],v[204:207],a[88:91],v[24:27]
wt lgkmcnt(4)
ld a[228:231],v229,s[28:31],s93 offen offset:2048
mm v[24:27],v[200:203],a[80:83],v[24:27]
rt v[204:205],v243 offset:128
rt v[206:207],v243 offset:1152
mm v[24:27],v[208:211],a[0:3],v[24:27]
st v[234:237],v238,s[32:35],s95 offen offset:2048 sc1
mm v[24:27],v[212:215],a[8:11],v[24:27]
rt v[200:201],v243 offset:8576
rt v[202:203],v243 offset:9600
mm v[56:59],v[212:215],a[12:15],v[56:59]
cv v230,v16,v17
cv v232,v20,v21
mm v[56:59],v[208:211],a[4:7],v[56:59]
rt v[212:213],v243 offset:17024
rt v[214:215],v243 offset:18048
mm v[56:59],v[216:219],a[20:23],v[56:59]
cv v231,v18,v19
cv v233,v22,v23
mm v[56:59],v[220:223],a[28:31],v[56:59]
rt v[208:209],v243 offset:25472
rt v[210:211],v243 offset:26496
mm v[24:27],v[220:223],a[24:27],v[24:27]
wt lgkmcnt(4)
s_mov_b32 s93, 4096
ld a[232:235],v229,s[28:31],s93 offen offset:0
mm v[24:27],v[216:219],a[16:19],v[24:27]
rt v[220:221],v243 offset:33920
rt v[222:223],v243 offset:34944
mm v[28:31],v[196:199],a[64:67],v[28:31]
v_permlane16_swap_b32 v230, v232
v_permlane16_swap_b32 v231, v233
mm v[28:31],v[192:195],a[72:75],v[28:31]
rt v[216:217],v243 offset:42368
rt v[218:219],v243 offset:43392
mm v[60:63],v[192:195],a[76:79],v[60:63]
ld a[236:239],v229,s[28:31],s93 offen offset:2048
mm v[60:63],v[196:199],a[68:71],v[60:63]
rt v[192:193],v240 offset:50688
rt v[194:195],v240 offset:51712
mm v[60:63],v[204:207],a[84:87],v[60:63]
st v[230:233],v238,s[32:35],s94 offen offset:2048 sc1
mm v[60:63],v[200:203],a[92:95],v[60:63]
rt v[196:197],v240 offset:59136
rt v[198:199],v240 offset:60160
mm v[28:31],v[200:203],a[88:91],v[28:31]
wt lgkmcnt(4)
s_mov_b32 s93, 8192
ld a[240:243],v229,s[28:31],s93 offen offset:0
mm v[28:31],v[204:207],a[80:83],v[28:31]
rt v[200:201],v240 offset:0
rt v[202:203],v240 offset:1024
mm v[28:31],v[212:215],a[0:3],v[28:31]
ld v[156:159],v225,s[48:51],0 offen offset:512
mm v[28:31],v[208:211],a[8:11],v[28:31]
rt v[204:205],v240 offset:8448
rt v[206:207],v240 offset:9472
mm v[60:63],v[208:211],a[12:15],v[60:63]
ld a[244:247],v229,s[28:31],s93 offen offset:2048
mm v[60:63],v[212:215],a[4:7],v[60:63]
rt v[208:209],v240 offset:16896
rt v[210:211],v240 offset:17920
mm v[60:63],v[220:223],a[20:23],v[60:63]
ld v[168:171],v224,s[68:71],0 offen offset:1024
mm v[60:63],v[216:219],a[28:31],v[60:63]
rt v[212:213],v240 offset:25344
rt v[214:215],v240 offset:26368
mm v[28:31],v[216:219],a[24:27],v[28:31]
wt vmcnt(25) lgkmcnt(4)
s_mov_b32 s93, 12288
ld a[248:251],v229,s[28:31],s93 offen offset:0
mm v[28:31],v[220:223],a[16:19],v[28:31]
rt v[216:217],v240 offset:33792
rt v[218:219],v240 offset:34816
mm v[64:67],v[192:195],a[32:35],v[64:67]
cv v234,v56,v57
cv v236,v60,v61
mm v[64:67],v[196:199],a[40:43],v[64:67]
rt v[220:221],v240 offset:42240
rt v[222:223],v240 offset:43264
mm v[96:99],v[196:199],a[44:47],v[96:99]
cv v235,v58,v59
cv v237,v62,v63
mm v[96:99],v[192:195],a[36:39],v[96:99]
rt v[196:197],v241 offset:50688
rt v[198:199],v241 offset:51712
mm v[96:99],v[200:203],a[52:55],v[96:99]
v_permlane16_swap_b32 v234, v236
v_permlane16_swap_b32 v235, v237
mm v[96:99],v[204:207],a[60:63],v[96:99]
rt v[192:193],v241 offset:59136
rt v[194:195],v241 offset:60160
mm v[64:67],v[204:207],a[56:59],v[64:67]
wt vmcnt(18) lgkmcnt(4)
ld a[252:255],v229,s[28:31],s93 offen offset:2048
mm v[64:67],v[200:203],a[48:51],v[64:67]
rt v[204:205],v241 offset:0
rt v[206:207],v241 offset:1024
mm v[64:67],v[208:211],v[128:131],v[64:67]
st v[234:237],v238,s[32:35],s95 offen offset:3072 sc1
mm v[64:67],v[212:215],v[144:147],v[64:67]
rt v[200:201],v241 offset:8448
rt v[202:203],v241 offset:9472
mm v[96:99],v[212:215],v[148:151],v[96:99]
cv v230,v24,v25
cv v232,v28,v29
mm v[96:99],v[208:211],v[132:135],v[96:99]
rt v[212:213],v241 offset:16896
rt v[214:215],v241 offset:17920
mm v[96:99],v[216:219],v[164:167],v[96:99]
cv v231,v26,v27
cv v233,v30,v31
mm v[96:99],v[220:223],v[180:183],v[96:99]
rt v[208:209],v241 offset:25344
rt v[210:211],v241 offset:26368
mm v[64:67],v[220:223],v[176:179],v[64:67]
wt lgkmcnt(4)
s_mov_b32 s93, 0
ld a[64:67],v229,s[28:31],s93 offen offset:256
mm v[64:67],v[216:219],v[160:163],v[64:67]
rt v[220:221],v241 offset:33792
rt v[222:223],v241 offset:34816
mm v[68:71],v[196:199],a[32:35],v[68:71]
v_permlane16_swap_b32 v230, v232
v_permlane16_swap_b32 v231, v233
mm v[68:71],v[192:195],a[40:43],v[68:71]
rt v[216:217],v241 offset:42240
rt v[218:219],v241 offset:43264
mm v[100:103],v[192:195],a[44:47],v[100:103]
ld a[68:71],v229,s[28:31],s93 offen offset:2304
mm v[100:103],v[196:199],a[36:39],v[100:103]
rt v[192:193],v242 offset:50688
rt v[194:195],v242 offset:51712
mm v[100:103],v[204:207],a[52:55],v[100:103]
st v[230:233],v238,s[32:35],s94 offen offset:3072 sc1
mm v[100:103],v[200:203],a[60:63],v[100:103]
rt v[196:197],v242 offset:59136
rt v[198:199],v242 offset:60160
mm v[68:71],v[200:203],a[56:59],v[68:71]
wt lgkmcnt(4)
s_mov_b32 s93, 4096
ld a[72:75],v229,s[28:31],s93 offen offset:256
mm v[68:71],v[204:207],a[48:51],v[68:71]
rt v[200:201],v242 offset:0
rt v[202:203],v242 offset:1024
mm v[68:71],v[212:215],v[128:131],v[68:71]
ld v[172:175],v225,s[48:51],0 offen offset:1024
mm v[68:71],v[208:211],v[144:147],v[68:71]
rt v[204:205],v242 offset:8448
rt v[206:207],v242 offset:9472
mm v[100:103],v[208:211],v[148:151],v[100:103]
ld a[76:79],v229,s[28:31],s93 offen offset:2304
mm v[100:103],v[212:215],v[132:135],v[100:103]
rt v[208:209],v242 offset:16896
rt v[210:211],v242 offset:17920
mm v[100:103],v[220:223],v[164:167],v[100:103]
ld v[184:187],v224,s[68:71],0 offen offset:1536
mm v[100:103],v[216:219],v[180:183],v[100:103]
rt v[212:213],v242 offset:25344
rt v[214:215],v242 offset:26368
mm v[68:71],v[216:219],v[176:179],v[68:71]
wt lgkmcnt(4)
s_mov_b32 s93, 8192
ld a[80:83],v229,s[28:31],s93 offen offset:256
mm v[68:71],v[220:223],v[160:163],v[68:71]
rt v[216:217],v242 offset:33792
rt v[218:219],v242 offset:34816
mm v[72:75],v[192:195],a[32:35],v[72:75]
cv v234,v96,v97
cv v236,v100,v101
mm v[72:75],v[196:199],a[40:43],v[72:75]
rt v[220:221],v242 offset:42240
rt v[222:223],v242 offset:43264
mm v[104:107],v[196:199],a[44:47],v[104:107]
cv v235,v98,v99
cv v237,v102,v103
mm v[104:107],v[192:195],a[36:39],v[104:107]
rt v[196:197],v243 offset:50688
rt v[198:199],v243 offset:51712
mm v[104:107],v[200:203],a[52:55],v[104:107]
v_permlane16_swap_b32 v234, v236
v_permlane16_swap_b32 v235, v237
mm v[104:107],v[204:207],a[60:63],v[104:107]
rt v[192:193],v243 offset:59136
rt v[194:195],v243 offset:60160
mm v[72:75],v[204:207],a[56:59],v[72:75]
wt lgkmcnt(4)
ld a[84:87],v229,s[28:31],s93 offen offset:2304
mm v[72:75],v[200:203],a[48:51],v[72:75]
rt v[204:205],v243 offset:0
rt v[206:207],v243 offset:1024
mm v[72:75],v[208:211],v[128:131],v[72:75]
st v[234:237],v238,s[32:35],s97 offen offset:0 sc1
mm v[72:75],v[212:215],v[144:147],v[72:75]
rt v[200:201],v243 offset:8448
rt v[202:203],v243 offset:9472
mm v[104:107],v[212:215],v[148:151],v[104:107]
cv v230,v64,v65
cv v232,v68,v69
mm v[104:107],v[208:211],v[132:135],v[104:107]
rt v[212:213],v243 offset:16896
rt v[214:215],v243 offset:17920
mm v[104:107],v[216:219],v[164:167],v[104:107]
cv v231,v66,v67
cv v233,v70,v71
mm v[104:107],v[220:223],v[180:183],v[104:107]
rt v[208:209],v243 offset:25344
rt v[210:211],v243 offset:26368
mm v[72:75],v[220:223],v[176:179],v[72:75]
wt lgkmcnt(4)
s_mov_b32 s93, 12288
ld a[88:91],v229,s[28:31],s93 offen offset:256
mm v[72:75],v[216:219],v[160:163],v[72:75]
rt v[220:221],v243 offset:33792
rt v[222:223],v243 offset:34816
mm v[76:79],v[196:199],a[32:35],v[76:79]
v_permlane16_swap_b32 v230, v232
v_permlane16_swap_b32 v231, v233
mm v[76:79],v[192:195],a[40:43],v[76:79]
rt v[216:217],v243 offset:42240
rt v[218:219],v243 offset:43264
mm v[108:111],v[192:195],a[44:47],v[108:111]
ld a[92:95],v229,s[28:31],s93 offen offset:2304
mm v[108:111],v[196:199],a[36:39],v[108:111]
rt v[192:193],v240 offset:50816
rt v[194:195],v240 offset:51840
mm v[108:111],v[204:207],a[52:55],v[108:111]
st v[230:233],v238,s[32:35],s96 offen offset:0 sc1
mm v[108:111],v[200:203],a[60:63],v[108:111]
rt v[196:197],v240 offset:59264
rt v[198:199],v240 offset:60288
mm v[76:79],v[200:203],a[56:59],v[76:79]
wt lgkmcnt(4)
mm v[76:79],v[204:207],a[48:51],v[76:79]
rt v[200:201],v240 offset:128
rt v[202:203],v240 offset:1152
mm v[76:79],v[212:215],v[128:131],v[76:79]
ld v[188:191],v225,s[48:51],0 offen offset:1536
mm v[76:79],v[208:211],v[144:147],v[76:79]
rt v[204:205],v240 offset:8576
rt v[206:207],v240 offset:9600
mm v[108:111],v[208:211],v[148:151],v[108:111]
mm v[108:111],v[212:215],v[132:135],v[108:111]
rt v[208:209],v240 offset:17024
.La5_LW7_5:
s_cbranch_execz .La5_LA_RETURN
rt v[210:211],v240 offset:18048
mm v[108:111],v[220:223],v[164:167],v[108:111]
mm v[108:111],v[216:219],v[180:183],v[108:111]
rt v[212:213],v240 offset:25472
rt v[214:215],v240 offset:26496
mm v[76:79],v[216:219],v[176:179],v[76:79]
wt lgkmcnt(4)
mm v[76:79],v[220:223],v[160:163],v[76:79]
rt v[216:217],v240 offset:33920
rt v[218:219],v240 offset:34944
mm v[80:83],v[192:195],a[32:35],v[80:83]
cv v234,v104,v105
cv v236,v108,v109
mm v[80:83],v[196:199],a[40:43],v[80:83]
rt v[220:221],v240 offset:42368
rt v[222:223],v240 offset:43392
mm v[112:115],v[196:199],a[44:47],v[112:115]
cv v235,v106,v107
cv v237,v110,v111
mm v[112:115],v[192:195],a[36:39],v[112:115]
rt v[196:197],v241 offset:50816
rt v[198:199],v241 offset:51840
mm v[112:115],v[200:203],a[52:55],v[112:115]
v_permlane16_swap_b32 v234, v236
v_permlane16_swap_b32 v235, v237
mm v[112:115],v[204:207],a[60:63],v[112:115]
rt v[192:193],v241 offset:59264
rt v[194:195],v241 offset:60288
mm v[80:83],v[204:207],a[56:59],v[80:83]
wt lgkmcnt(4)
mm v[80:83],v[200:203],a[48:51],v[80:83]
rt v[204:205],v241 offset:128
rt v[206:207],v241 offset:1152
mm v[80:83],v[208:211],v[128:131],v[80:83]
st v[234:237],v238,s[32:35],s97 offen offset:1024 sc1
mm v[80:83],v[212:215],v[144:147],v[80:83]
rt v[200:201],v241 offset:8576
rt v[202:203],v241 offset:9600
mm v[112:115],v[212:215],v[148:151],v[112:115]
cv v230,v72,v73
cv v232,v76,v77
mm v[112:115],v[208:211],v[132:135],v[112:115]
rt v[212:213],v241 offset:17024
rt v[214:215],v241 offset:18048
mm v[112:115],v[216:219],v[164:167],v[112:115]
cv v231,v74,v75
cv v233,v78,v79
mm v[112:115],v[220:223],v[180:183],v[112:115]
rt v[208:209],v241 offset:25472
rt v[210:211],v241 offset:26496
mm v[80:83],v[220:223],v[176:179],v[80:83]
wt lgkmcnt(4)
mm v[80:83],v[216:219],v[160:163],v[80:83]
rt v[220:221],v241 offset:33920
rt v[222:223],v241 offset:34944
mm v[84:87],v[196:199],a[32:35],v[84:87]
v_permlane16_swap_b32 v230, v232
v_permlane16_swap_b32 v231, v233
mm v[84:87],v[192:195],a[40:43],v[84:87]
rt v[216:217],v241 offset:42368
rt v[218:219],v241 offset:43392
mm v[116:119],v[192:195],a[44:47],v[116:119]
mm v[116:119],v[196:199],a[36:39],v[116:119]
rt v[192:193],v242 offset:50816
rt v[194:195],v242 offset:51840
mm v[116:119],v[204:207],a[52:55],v[116:119]
st v[230:233],v238,s[32:35],s96 offen offset:1024 sc1
mm v[116:119],v[200:203],a[60:63],v[116:119]
rt v[196:197],v242 offset:59264
rt v[198:199],v242 offset:60288
mm v[84:87],v[200:203],a[56:59],v[84:87]
wt lgkmcnt(4)
mm v[84:87],v[204:207],a[48:51],v[84:87]
rt v[200:201],v242 offset:128
rt v[202:203],v242 offset:1152
mm v[84:87],v[212:215],v[128:131],v[84:87]
mm v[84:87],v[208:211],v[144:147],v[84:87]
rt v[204:205],v242 offset:8576
rt v[206:207],v242 offset:9600
mm v[116:119],v[208:211],v[148:151],v[116:119]
mm v[116:119],v[212:215],v[132:135],v[116:119]
rt v[208:209],v242 offset:17024
rt v[210:211],v242 offset:18048
mm v[116:119],v[220:223],v[164:167],v[116:119]
mm v[116:119],v[216:219],v[180:183],v[116:119]
rt v[212:213],v242 offset:25472
rt v[214:215],v242 offset:26496
mm v[84:87],v[216:219],v[176:179],v[84:87]
wt lgkmcnt(4)
mm v[84:87],v[220:223],v[160:163],v[84:87]
rt v[216:217],v242 offset:33920
rt v[218:219],v242 offset:34944
mm v[88:91],v[192:195],a[32:35],v[88:91]
cv v234,v112,v113
cv v236,v116,v117
mm v[88:91],v[196:199],a[40:43],v[88:91]
rt v[220:221],v242 offset:42368
rt v[222:223],v242 offset:43392
mm v[120:123],v[196:199],a[44:47],v[120:123]
cv v235,v114,v115
cv v237,v118,v119
mm v[120:123],v[192:195],a[36:39],v[120:123]
rt v[196:197],v243 offset:50816
rt v[198:199],v243 offset:51840
mm v[120:123],v[200:203],a[52:55],v[120:123]
v_permlane16_swap_b32 v234, v236
v_permlane16_swap_b32 v235, v237
mm v[120:123],v[204:207],a[60:63],v[120:123]
rt v[192:193],v243 offset:59264
rt v[194:195],v243 offset:60288
mm v[88:91],v[204:207],a[56:59],v[88:91]
wt lgkmcnt(4)
mm v[88:91],v[200:203],a[48:51],v[88:91]
rt v[204:205],v243 offset:128
rt v[206:207],v243 offset:1152
mm v[88:91],v[208:211],v[128:131],v[88:91]
st v[234:237],v238,s[32:35],s97 offen offset:2048 sc1
mm v[88:91],v[212:215],v[144:147],v[88:91]
rt v[200:201],v243 offset:8576
rt v[202:203],v243 offset:9600
mm v[120:123],v[212:215],v[148:151],v[120:123]
cv v230,v80,v81
cv v232,v84,v85
mm v[120:123],v[208:211],v[132:135],v[120:123]
rt v[212:213],v243 offset:17024
rt v[214:215],v243 offset:18048
mm v[120:123],v[216:219],v[164:167],v[120:123]
cv v231,v82,v83
cv v233,v86,v87
wt vmcnt(14)
buffer_store_dwordx2 v[252:253], v251, s[64:67], s92 offen sc1
mm v[120:123],v[220:223],v[180:183],v[120:123]
rt v[208:209],v243 offset:25472
rt v[210:211],v243 offset:26496
mm v[88:91],v[220:223],v[176:179],v[88:91]
wt lgkmcnt(4)
mm v[88:91],v[216:219],v[160:163],v[88:91]
rt v[220:221],v243 offset:33920
rt v[222:223],v243 offset:34944
mm v[92:95],v[196:199],a[32:35],v[92:95]
v_permlane16_swap_b32 v230, v232
v_permlane16_swap_b32 v231, v233
mm v[92:95],v[192:195],a[40:43],v[92:95]
rt v[216:217],v243 offset:42368
rt v[218:219],v243 offset:43392
mm v[124:127],v[192:195],a[44:47],v[124:127]
mm v[124:127],v[196:199],a[36:39],v[124:127]
mm v[124:127],v[204:207],a[52:55],v[124:127]
st v[230:233],v238,s[32:35],s96 offen offset:2048 sc1
mm v[124:127],v[200:203],a[60:63],v[124:127]
mm v[92:95],v[200:203],a[56:59],v[92:95]
wt lgkmcnt(0)
mm v[92:95],v[204:207],a[48:51],v[92:95]
mm v[92:95],v[212:215],v[128:131],v[92:95]
mm v[92:95],v[208:211],v[144:147],v[92:95]
mm v[124:127],v[208:211],v[148:151],v[124:127]
mm v[124:127],v[212:215],v[132:135],v[124:127]
mm v[124:127],v[220:223],v[164:167],v[124:127]
mm v[124:127],v[216:219],v[180:183],v[124:127]
mm v[92:95],v[216:219],v[176:179],v[92:95]
mm v[92:95],v[220:223],v[160:163],v[92:95]
s_nop 7
cv v234,v120,v121
cv v236,v124,v125
cv v235,v122,v123
cv v237,v126,v127
cv v230,v88,v89
cv v232,v92,v93
cv v231,v90,v91
cv v233,v94,v95
v_permlane16_swap_b32 v234, v236
v_permlane16_swap_b32 v235, v237
st v[234:237],v238,s[32:35],s97 offen offset:3072 sc1
v_permlane16_swap_b32 v230, v232
v_permlane16_swap_b32 v231, v233
st v[230:233],v238,s[32:35],s96 offen offset:3072 sc1
.La5_LT_0:
.La5_LW3_5:
s_cbranch_execz .La5_LA_RETURN
.La5_LW7_6:
s_cbranch_execz .La5_LA_RETURN
s_lshl_b32 s91, s76, 5
s_add_u32 s91, s91, s90
s_lshl_b32 s20, s91, 9
s_add_u32 s20, s20, 2097152
s_lshr_b32 s93, s91, 1
s_lshl_b32 s93, s93, 15
s_and_b32 s94, s91, 1
s_lshl2_add_u32 s94, s94, s3
s_lshl_b32 s94, s94, 7
s_add_u32 s97, s93, s94
s_add_u32 s32, s26, 71303168
s_addc_u32 s33, s27, 0
s_and_b32 s33, s33, 0xffff
s_mov_b32 s34, 16777216
s_mov_b32 s35, 0x20000
s_add_u32 s44, s26, 171966464
s_addc_u32 s45, s27, 0
s_and_b32 s45, s45, 0xffff
s_mov_b32 s46, 950272
s_mov_b32 s47, 0x20000
s_add_u32 s56, s38, 0
s_addc_u32 s57, s39, 0
s_and_b32 s57, s57, 0xffff
s_mov_b32 s58, 4194304
s_mov_b32 s59, 0x20000
s_add_u32 s68, s16, 0
s_addc_u32 s69, s17, 0
s_and_b32 s69, s69, 0xffff
s_mov_b32 s70, 4194304
s_mov_b32 s71, 0x20000
v_and_b32 v230, 0x70, v255
v_lshrrev_b32 v231, 7, v255
v_lshl_add_u32 v230, v231, 10, v230
v_add_u32 v240, s97, v230
v_xor_b32 v241, 8192, v240
v_xor_b32 v242, 16384, v240
v_xor_b32 v243, 24576, v240
v_mov_b32 v237, 0x3f803f80
v_mov_b32 v236, 0
wt vmcnt(7)
v_dot2_f32_bf16 v236, v140, v237, v236
v_dot2_f32_bf16 v236, v141, v237, v236
v_dot2_f32_bf16 v236, v142, v237, v236
v_dot2_f32_bf16 v236, v143, v237, v236
v_dot2_f32_bf16 v236, v156, v237, v236
v_dot2_f32_bf16 v236, v157, v237, v236
v_dot2_f32_bf16 v236, v158, v237, v236
v_dot2_f32_bf16 v236, v159, v237, v236
v_dot2_f32_bf16 v236, v172, v237, v236
v_dot2_f32_bf16 v236, v173, v237, v236
v_dot2_f32_bf16 v236, v174, v237, v236
v_dot2_f32_bf16 v236, v175, v237, v236
v_dot2_f32_bf16 v236, v188, v237, v236
v_dot2_f32_bf16 v236, v189, v237, v236
v_dot2_f32_bf16 v236, v190, v237, v236
v_dot2_f32_bf16 v236, v191, v237, v236
v_lshlrev_b32 v0, 16, v136
v_and_b32 v1, 0xffff0000, v136
v_lshlrev_b32 v2, 16, v137
v_and_b32 v3, 0xffff0000, v137
v_lshlrev_b32 v4, 16, v138
v_and_b32 v5, 0xffff0000, v138
v_lshlrev_b32 v6, 16, v139
v_and_b32 v7, 0xffff0000, v139
v_lshlrev_b32 v8, 16, v152
v_and_b32 v9, 0xffff0000, v152
v_lshlrev_b32 v10, 16, v153
v_and_b32 v11, 0xffff0000, v153
v_lshlrev_b32 v12, 16, v154
v_and_b32 v13, 0xffff0000, v154
v_lshlrev_b32 v14, 16, v155
v_and_b32 v15, 0xffff0000, v155
v_lshlrev_b32 v16, 16, v168
v_and_b32 v17, 0xffff0000, v168
v_lshlrev_b32 v18, 16, v169
v_and_b32 v19, 0xffff0000, v169
v_lshlrev_b32 v20, 16, v170
v_and_b32 v21, 0xffff0000, v170
v_lshlrev_b32 v22, 16, v171
v_and_b32 v23, 0xffff0000, v171
v_lshlrev_b32 v24, 16, v184
v_and_b32 v25, 0xffff0000, v184
v_lshlrev_b32 v26, 16, v185
v_and_b32 v27, 0xffff0000, v185
v_lshlrev_b32 v28, 16, v186
v_and_b32 v29, 0xffff0000, v186
v_lshlrev_b32 v30, 16, v187
v_and_b32 v31, 0xffff0000, v187
s_nop 1
v_add_f32_dpp v236, v236, v236 quad_perm:[1,0,3,2] row_mask:0xf bank_mask:0xf
s_nop 1
v_add_f32_dpp v236, v236, v236 quad_perm:[2,3,0,1] row_mask:0xf bank_mask:0xf
s_nop 1
v_add_f32_dpp v236, v236, v236 row_ror:8 row_mask:0xf bank_mask:0xf
v_mov_b32 v230, v236
s_nop 1
v_permlane16_swap_b32 v230, v236
s_nop 1
v_add_f32 v236, v236, v230
v_mov_b32 v230, v236
s_nop 1
v_permlane32_swap_b32 v230, v236
s_nop 1
v_add_f32 v236, v236, v230
s_mov_b64 exec, 0
s_cmp_eq_u32 s3, 0
s_cbranch_scc1 .La5_LW4_0
s_cmp_eq_u32 s3, 1
s_cbranch_scc1 .La5_LW4_1
s_cmp_eq_u32 s3, 2
s_cbranch_scc1 .La5_LW4_2
s_branch .La5_LW4_3
.La5_LW4_R0:
s_mov_b64 exec, -1
wt vmcnt(0)
buffer_store_dwordx2 v[252:253], v250, s[64:67], s92 offen sc1
s_mov_b32 s85, 99
s_cmp_eq_u32 s30, 0
s_cbranch_scc1 .La5_LT_ROWS
; ---- h = x + attn, LayerNorm statistics, router score, two best of eight rows
.La5_LT_ROWS:
s_mov_b32 s96, 0
.La5_LT_P3:
buffer_load_dwordx2 v[250:251], v248, s[64:67], s20 offen sc1
wt vmcnt(0)
.La5_LT_P2:
v_cmp_eq_u64 vcc, v[250:251], v[252:253]
s_add_u32 s96, s96, 1
s_lshr_b32 s92, vcc_lo, 1
s_and_b32 s92, s92, vcc_lo
s_lshr_b32 s93, s92, 2
s_and_b32 s92, s92, s93
s_lshr_b32 s93, s92, 4
s_and_b32 s92, s92, s93
s_lshr_b32 s93, s96, 20
s_mul_i32 s93, s93, 0x01010101
s_or_b32 s92, s92, s93
s_andn2_b32 s92, 0x01010101, s92
s_bcnt1_i32_b32 s93, s92
s_cmp_gt_u32 s93, 1
s_cbranch_scc1 .La5_LT_P3S
s_or_b32 s93, s92, 0x01000000
s_ff1_i32_b32 s93, s93
s_lshl_b32 s80, s93, 19
s_xor_b32 s93, s80, 8388608
s_xor_b32 s94, s80, 12582912
s_xor_b32 s95, s80, 4194304
ld v[208:211],v240,s[32:35],s93 offen sc1
ld v[212:215],v241,s[32:35],s93 offen sc1
ld v[216:219],v242,s[32:35],s93 offen sc1
ld v[220:223],v243,s[32:35],s93 offen sc1
ld v[112:115],v240,s[32:35],s94 offen sc1
ld v[116:119],v241,s[32:35],s94 offen sc1
ld v[120:123],v242,s[32:35],s94 offen sc1
ld v[124:127],v243,s[32:35],s94 offen sc1
ld v[96:99],v240,s[32:35],s95 offen sc1
ld v[100:103],v241,s[32:35],s95 offen sc1
ld v[104:107],v242,s[32:35],s95 offen sc1
ld v[108:111],v243,s[32:35],s95 offen sc1
s_cmp_lg_u32 s92, 0
s_nop 0
s_nop 0
s_cbranch_scc1 .La5_LT_P4
ld v[192:195],v240,s[32:35],s80 offen sc1
ld v[196:199],v241,s[32:35],s80 offen sc1
ld v[200:203],v242,s[32:35],s80 offen sc1
ld v[204:207],v243,s[32:35],s80 offen sc1
.La5_LT_P4:
s_lshr_b32 s93, s80, 16
v_and_b32 v248, 56, v248
s_add_u32 s20, s20, s93
buffer_load_dwordx2 v[250:251], v248, s[64:67], s20 offen sc1
wt vmcnt(8)
v_lshlrev_b32 v32, 16, v208
v_and_b32 v33, 0xffff0000, v208
v_lshlrev_b32 v230, 16, v112
v_and_b32 v231, 0xffff0000, v112
v_pk_add_f32 v[32:33], v[32:33], v[230:231]
v_lshlrev_b32 v34, 16, v209
v_and_b32 v35, 0xffff0000, v209
v_lshlrev_b32 v230, 16, v113
v_and_b32 v231, 0xffff0000, v113
v_pk_add_f32 v[34:35], v[34:35], v[230:231]
v_lshlrev_b32 v36, 16, v210
v_and_b32 v37, 0xffff0000, v210
v_lshlrev_b32 v230, 16, v114
v_and_b32 v231, 0xffff0000, v114
v_pk_add_f32 v[36:37], v[36:37], v[230:231]
v_lshlrev_b32 v38, 16, v211
v_and_b32 v39, 0xffff0000, v211
v_lshlrev_b32 v230, 16, v115
v_and_b32 v231, 0xffff0000, v115
v_pk_add_f32 v[38:39], v[38:39], v[230:231]
wt vmcnt(7)
v_lshlrev_b32 v40, 16, v212
v_and_b32 v41, 0xffff0000, v212
v_lshlrev_b32 v230, 16, v116
v_and_b32 v231, 0xffff0000, v116
v_pk_add_f32 v[40:41], v[40:41], v[230:231]
v_lshlrev_b32 v42, 16, v213
v_and_b32 v43, 0xffff0000, v213
v_lshlrev_b32 v230, 16, v117
v_and_b32 v231, 0xffff0000, v117
v_pk_add_f32 v[42:43], v[42:43], v[230:231]
v_lshlrev_b32 v44, 16, v214
v_and_b32 v45, 0xffff0000, v214
v_lshlrev_b32 v230, 16, v118
v_and_b32 v231, 0xffff0000, v118
v_pk_add_f32 v[44:45], v[44:45], v[230:231]
v_lshlrev_b32 v46, 16, v215
v_and_b32 v47, 0xffff0000, v215
v_lshlrev_b32 v230, 16, v119
v_and_b32 v231, 0xffff0000, v119
v_pk_add_f32 v[46:47], v[46:47], v[230:231]
wt vmcnt(6)
v_lshlrev_b32 v48, 16, v216
v_and_b32 v49, 0xffff0000, v216
v_lshlrev_b32 v230, 16, v120
v_and_b32 v231, 0xffff0000, v120
v_pk_add_f32 v[48:49], v[48:49], v[230:231]
v_lshlrev_b32 v50, 16, v217
v_and_b32 v51, 0xffff0000, v217
v_lshlrev_b32 v230, 16, v121
v_and_b32 v231, 0xffff0000, v121
v_pk_add_f32 v[50:51], v[50:51], v[230:231]
v_lshlrev_b32 v52, 16, v218
v_and_b32 v53, 0xffff0000, v218
v_lshlrev_b32 v230, 16, v122
v_and_b32 v231, 0xffff0000, v122
v_pk_add_f32 v[52:53], v[52:53], v[230:231]
v_lshlrev_b32 v54, 16, v219
v_and_b32 v55, 0xffff0000, v219
v_lshlrev_b32 v230, 16, v123
v_and_b32 v231, 0xffff0000, v123
v_pk_add_f32 v[54:55], v[54:55], v[230:231]
wt vmcnt(5)
v_lshlrev_b32 v56, 16, v220
v_and_b32 v57, 0xffff0000, v220
v_lshlrev_b32 v230, 16, v124
v_and_b32 v231, 0xffff0000, v124
v_pk_add_f32 v[56:57], v[56:57], v[230:231]
v_lshlrev_b32 v58, 16, v221
v_and_b32 v59, 0xffff0000, v221
v_lshlrev_b32 v230, 16, v125
v_and_b32 v231, 0xffff0000, v125
v_pk_add_f32 v[58:59], v[58:59], v[230:231]
v_lshlrev_b32 v60, 16, v222
v_and_b32 v61, 0xffff0000, v222
v_lshlrev_b32 v230, 16, v126
v_and_b32 v231, 0xffff0000, v126
v_pk_add_f32 v[60:61], v[60:61], v[230:231]
v_lshlrev_b32 v62, 16, v223
v_and_b32 v63, 0xffff0000, v223
v_lshlrev_b32 v230, 16, v127
v_and_b32 v231, 0xffff0000, v127
v_pk_add_f32 v[62:63], v[62:63], v[230:231]
wt vmcnt(1)
v_lshlrev_b32 v64, 16, v96
v_and_b32 v65, 0xffff0000, v96
v_lshlrev_b32 v66, 16, v97
v_and_b32 v67, 0xffff0000, v97
v_lshlrev_b32 v68, 16, v98
v_and_b32 v69, 0xffff0000, v98
v_lshlrev_b32 v70, 16, v99
v_and_b32 v71, 0xffff0000, v99
v_lshlrev_b32 v72, 16, v100
v_and_b32 v73, 0xffff0000, v100
v_lshlrev_b32 v74, 16, v101
v_and_b32 v75, 0xffff0000, v101
v_lshlrev_b32 v76, 16, v102
v_and_b32 v77, 0xffff0000, v102
v_lshlrev_b32 v78, 16, v103
v_and_b32 v79, 0xffff0000, v103
v_lshlrev_b32 v80, 16, v104
v_and_b32 v81, 0xffff0000, v104
v_lshlrev_b32 v82, 16, v105
v_and_b32 v83, 0xffff0000, v105
v_lshlrev_b32 v84, 16, v106
v_and_b32 v85, 0xffff0000, v106
v_lshlrev_b32 v86, 16, v107
v_and_b32 v87, 0xffff0000, v107
v_lshlrev_b32 v88, 16, v108
v_and_b32 v89, 0xffff0000, v108
v_lshlrev_b32 v90, 16, v109
v_and_b32 v91, 0xffff0000, v109
v_lshlrev_b32 v92, 16, v110
v_and_b32 v93, 0xffff0000, v110
v_lshlrev_b32 v94, 16, v111
v_and_b32 v95, 0xffff0000, v111
v_mov_b32 v232, 0
v_mov_b32 v233, 0
v_mov_b32_e64 v234, 0
s_cmp_eq_u32 s92, 0
s_cbranch_scc1 .La5_LT_SUM
.La5_LT_P5:
wt vmcnt(0)
v_cmp_eq_u64 vcc, v[250:251], v[252:253]
s_add_u32 s96, s96, 1
s_cmp_eq_u64 vcc, -1
s_cbranch_scc1 .La5_LT_P6
s_lshr_b32 s93, s96, 20
s_nop 0
s_cbranch_scc1 .La5_LT_P6
s_sleep 3
buffer_load_dwordx2 v[250:251], v248, s[64:67], s20 offen sc1
s_branch .La5_LT_P5
.La5_LT_P3S:
s_sleep 24
s_branch .La5_LT_P3
.La5_LT_P6:
ld v[192:195],v240,s[32:35],s80 offen sc1
ld v[196:199],v241,s[32:35],s80 offen sc1
ld v[200:203],v242,s[32:35],s80 offen sc1
ld v[204:207],v243,s[32:35],s80 offen sc1
.La5_LT_SUM:
wt vmcnt(3)
v_lshlrev_b32 v208, 16, v192
v_and_b32 v209, 0xffff0000, v192
v_lshlrev_b32 v210, 16, v193
v_and_b32 v211, 0xffff0000, v193
v_lshlrev_b32 v212, 16, v194
v_and_b32 v213, 0xffff0000, v194
v_lshlrev_b32 v214, 16, v195
v_and_b32 v215, 0xffff0000, v195
v_pk_add_f32 v[208:209], v[208:209], v[64:65]
v_pk_add_f32 v[210:211], v[210:211], v[66:67]
v_pk_add_f32 v[212:213], v[212:213], v[68:69]
v_pk_add_f32 v[214:215], v[214:215], v[70:71]
v_pk_add_f32 v[208:209], v[208:209], v[32:33]
v_pk_add_f32 v[210:211], v[210:211], v[34:35]
v_pk_add_f32 v[212:213], v[212:213], v[36:37]
v_pk_add_f32 v[214:215], v[214:215], v[38:39]
v_pk_add_f32 v[208:209], v[208:209], v[0:1]
v_pk_add_f32 v[210:211], v[210:211], v[2:3]
v_pk_add_f32 v[212:213], v[212:213], v[4:5]
v_pk_add_f32 v[214:215], v[214:215], v[6:7]
cv v96,v208,v209
cv v97,v210,v211
cv v98,v212,v213
cv v99,v214,v215
v_dot2_f32_bf16 v232, v96, v237, v232
v_dot2_f32_bf16 v233, v96, v96, v233
v_dot2_f32_bf16 v234, v96, v140, v234
v_dot2_f32_bf16 v232, v97, v237, v232
v_dot2_f32_bf16 v233, v97, v97, v233
v_dot2_f32_bf16 v234, v97, v141, v234
v_dot2_f32_bf16 v232, v98, v237, v232
v_dot2_f32_bf16 v233, v98, v98, v233
v_dot2_f32_bf16 v234, v98, v142, v234
v_dot2_f32_bf16 v232, v99, v237, v232
v_dot2_f32_bf16 v233, v99, v99, v233
v_dot2_f32_bf16 v234, v99, v143, v234
wt vmcnt(2)
v_lshlrev_b32 v208, 16, v196
v_and_b32 v209, 0xffff0000, v196
v_lshlrev_b32 v210, 16, v197
v_and_b32 v211, 0xffff0000, v197
v_lshlrev_b32 v212, 16, v198
v_and_b32 v213, 0xffff0000, v198
v_lshlrev_b32 v214, 16, v199
v_and_b32 v215, 0xffff0000, v199
v_pk_add_f32 v[208:209], v[208:209], v[72:73]
v_pk_add_f32 v[210:211], v[210:211], v[74:75]
v_pk_add_f32 v[212:213], v[212:213], v[76:77]
v_pk_add_f32 v[214:215], v[214:215], v[78:79]
v_pk_add_f32 v[208:209], v[208:209], v[40:41]
v_pk_add_f32 v[210:211], v[210:211], v[42:43]
v_pk_add_f32 v[212:213], v[212:213], v[44:45]
v_pk_add_f32 v[214:215], v[214:215], v[46:47]
v_pk_add_f32 v[208:209], v[208:209], v[8:9]
v_pk_add_f32 v[210:211], v[210:211], v[10:11]
v_pk_add_f32 v[212:213], v[212:213], v[12:13]
v_pk_add_f32 v[214:215], v[214:215], v[14:15]
cv v100,v208,v209
cv v101,v210,v211
cv v102,v212,v213
cv v103,v214,v215
v_dot2_f32_bf16 v232, v100, v237, v232
v_dot2_f32_bf16 v233, v100, v100, v233
v_dot2_f32_bf16 v234, v100, v156, v234
v_dot2_f32_bf16 v232, v101, v237, v232
v_dot2_f32_bf16 v233, v101, v101, v233
v_dot2_f32_bf16 v234, v101, v157, v234
v_dot2_f32_bf16 v232, v102, v237, v232
v_dot2_f32_bf16 v233, v102, v102, v233
v_dot2_f32_bf16 v234, v102, v158, v234
v_dot2_f32_bf16 v232, v103, v237, v232
v_dot2_f32_bf16 v233, v103, v103, v233
v_dot2_f32_bf16 v234, v103, v159, v234
wt vmcnt(1)
v_lshlrev_b32 v208, 16, v200
v_and_b32 v209, 0xffff0000, v200
v_lshlrev_b32 v210, 16, v201
v_and_b32 v211, 0xffff0000, v201
v_lshlrev_b32 v212, 16, v202
v_and_b32 v213, 0xffff0000, v202
v_lshlrev_b32 v214, 16, v203
v_and_b32 v215, 0xffff0000, v203
v_pk_add_f32 v[208:209], v[208:209], v[80:81]
v_pk_add_f32 v[210:211], v[210:211], v[82:83]
v_pk_add_f32 v[212:213], v[212:213], v[84:85]
v_pk_add_f32 v[214:215], v[214:215], v[86:87]
v_pk_add_f32 v[208:209], v[208:209], v[48:49]
v_pk_add_f32 v[210:211], v[210:211], v[50:51]
v_pk_add_f32 v[212:213], v[212:213], v[52:53]
v_pk_add_f32 v[214:215], v[214:215], v[54:55]
v_pk_add_f32 v[208:209], v[208:209], v[16:17]
v_pk_add_f32 v[210:211], v[210:211], v[18:19]
v_pk_add_f32 v[212:213], v[212:213], v[20:21]
v_pk_add_f32 v[214:215], v[214:215], v[22:23]
cv v104,v208,v209
cv v105,v210,v211
cv v106,v212,v213
cv v107,v214,v215
v_dot2_f32_bf16 v232, v104, v237, v232
v_dot2_f32_bf16 v233, v104, v104, v233
v_dot2_f32_bf16 v234, v104, v172, v234
v_dot2_f32_bf16 v232, v105, v237, v232
v_dot2_f32_bf16 v233, v105, v105, v233
v_dot2_f32_bf16 v234, v105, v173, v234
v_dot2_f32_bf16 v232, v106, v237, v232
v_dot2_f32_bf16 v233, v106, v106, v233
v_dot2_f32_bf16 v234, v106, v174, v234
v_dot2_f32_bf16 v232, v107, v237, v232
v_dot2_f32_bf16 v233, v107, v107, v233
v_dot2_f32_bf16 v234, v107, v175, v234
wt vmcnt(0)
v_lshlrev_b32 v208, 16, v204
v_and_b32 v209, 0xffff0000, v204
v_lshlrev_b32 v210, 16, v205
v_and_b32 v211, 0xffff0000, v205
v_lshlrev_b32 v212, 16, v206
v_and_b32 v213, 0xffff0000, v206
v_lshlrev_b32 v214, 16, v207
v_and_b32 v215, 0xffff0000, v207
v_pk_add_f32 v[208:209], v[208:209], v[88:89]
v_pk_add_f32 v[210:211], v[210:211], v[90:91]
v_pk_add_f32 v[212:213], v[212:213], v[92:93]
v_pk_add_f32 v[214:215], v[214:215], v[94:95]
v_pk_add_f32 v[208:209], v[208:209], v[56:57]
v_pk_add_f32 v[210:211], v[210:211], v[58:59]
v_pk_add_f32 v[212:213], v[212:213], v[60:61]
.La5_LW4_0:
s_cbranch_execz .La5_LW4_4
v_pk_add_f32 v[214:215], v[214:215], v[62:63]
v_pk_add_f32 v[208:209], v[208:209], v[24:25]
v_pk_add_f32 v[210:211], v[210:211], v[26:27]
v_pk_add_f32 v[212:213], v[212:213], v[28:29]
v_pk_add_f32 v[214:215], v[214:215], v[30:31]
cv v108,v208,v209
cv v109,v210,v211
cv v110,v212,v213
cv v111,v214,v215
v_dot2_f32_bf16 v232, v108, v237, v232
v_dot2_f32_bf16 v233, v108, v108, v233
v_dot2_f32_bf16 v234, v108, v188, v234
v_dot2_f32_bf16 v232, v109, v237, v232
v_dot2_f32_bf16 v233, v109, v109, v233
v_dot2_f32_bf16 v234, v109, v189, v234
v_dot2_f32_bf16 v232, v110, v237, v232
v_dot2_f32_bf16 v233, v110, v110, v233
v_dot2_f32_bf16 v234, v110, v190, v234
v_dot2_f32_bf16 v232, v111, v237, v232
v_dot2_f32_bf16 v233, v111, v111, v233
v_dot2_f32_bf16 v234, v111, v191, v234
wt vmcnt(0)
s_lshl2_add_u32 s92, s91, s3
s_lshl_b32 s92, s92, 1
s_sub_u32 s92, 0xffff, s92
v_add_f32_dpp v232, v232, v232 quad_perm:[1,0,3,2] row_mask:0xf bank_mask:0xf
v_add_f32_dpp v233, v233, v233 quad_perm:[1,0,3,2] row_mask:0xf bank_mask:0xf
v_add_f32_dpp v234, v234, v234 quad_perm:[1,0,3,2] row_mask:0xf bank_mask:0xf
v_add_f32_dpp v232, v232, v232 quad_perm:[2,3,0,1] row_mask:0xf bank_mask:0xf
v_add_f32_dpp v233, v233, v233 quad_perm:[2,3,0,1] row_mask:0xf bank_mask:0xf
v_add_f32_dpp v234, v234, v234 quad_perm:[2,3,0,1] row_mask:0xf bank_mask:0xf
v_add_f32_dpp v232, v232, v232 row_ror:8 row_mask:0xf bank_mask:0xf
v_add_f32_dpp v233, v233, v233 row_ror:8 row_mask:0xf bank_mask:0xf
v_add_f32_dpp v234, v234, v234 row_ror:8 row_mask:0xf bank_mask:0xf
v_mov_b32 v230, v232
v_mov_b32 v231, v233
v_mov_b32 v235, v234
v_permlane16_swap_b32 v230, v232
v_permlane16_swap_b32 v231, v233
v_permlane16_swap_b32 v235, v234
v_add_f32 v232, v232, v230
v_add_f32 v233, v233, v231
v_add_f32 v234, v234, v235
v_mov_b32 v230, v232
v_mov_b32 v231, v233
v_mov_b32 v235, v234
v_permlane32_swap_b32 v230, v232
v_permlane32_swap_b32 v231, v233
v_permlane32_swap_b32 v235, v234
v_add_f32 v232, v232, v230
v_add_f32 v233, v233, v231
v_add_f32 v234, v234, v235
v_mul_f32 v244, 0x3a800000, v232
v_mul_f32 v233, 0x3a800000, v233
v_fma_f32 v230, -v244, v244, v233
v_mul_f32 v233, 0x3a800000, v233
v_cmp_nge_f32 vcc, v230, v233
v_bfe_u32 v235, v255, 6, 1
v_sub_u32 v235, s92, v235
s_cbranch_vccnz .La5_LT_VAR
.La5_LT_VOK:
cv v244,v244,v244
v_lshlrev_b32 v244, 16, v244
cv v230,v230,v230
v_lshlrev_b32 v230, 16, v230
v_fma_f32 v234, -v244, v236, v234
v_add_f32 v230, 0x3727c5ac, v230
cv v230,v230,v230
v_lshlrev_b32 v230, 16, v230
v_sqrt_f32 v230, v230
v_lshrrev_b32 v232, 3, v255
cv v230,v230,v230
v_lshlrev_b32 v231, 16, v230
v_rcp_f32 v246, v231
v_lshrrev_b32 v230, 16, v230
v_or_b32 v233, v244, v230
v_mul_f32 v234, v234, v246
s_lshl_b32 s93, s3, 4
cv v234,v234,v234
v_and_b32 v230, 8, v232
v_ashrrev_i32 v232, 31, v234
v_or_b32 v232, 0x80000000, v232
v_xor_b32 v232, v232, v234
v_and_b32 v232, 0xffff0000, v232
v_add_u32 v230, s93, v230
v_or_b32 v232, v232, v235
ds_write_b64 v230, v[232:233] offset:0
v_lshrrev_b32 v230, 1, v255
v_and_b32 v230, 56, v230
wt lgkmcnt(0)
s_barrier
s_cmp_lg_u32 s3, 0
s_cbranch_scc1 .La5_LT_OUT
ds_read_b64 v[232:233], v230 offset:0
s_lshl3_add_u32 s92, s90, s76
s_lshl_b32 s92, s92, 4
v_lshrrev_b32 v235, 7, v255
v_lshlrev_b32 v235, 12, v235
wt lgkmcnt(0)
v_mov_b32 v234, v232
s_nop 1
v_max_u32_dpp v234, v234, v234 quad_perm:[1,0,3,2] row_mask:0xf bank_mask:0xf
s_nop 1
v_max_u32_dpp v234, v234, v234 quad_perm:[2,3,0,1] row_mask:0xf bank_mask:0xf
s_nop 1
v_max_u32_dpp v234, v234, v234 row_half_mirror row_mask:0xf bank_mask:0xf
s_nop 1
v_cmp_eq_u32 vcc, v232, v234
v_add_u32 v231, 8, v235
s_nop 0
v_cndmask_b32 v234, v232, 0, vcc
v_cndmask_b32 v235, v231, v235, vcc
s_nop 0
v_max_u32_dpp v234, v234, v234 quad_perm:[1,0,3,2] row_mask:0xf bank_mask:0xf
s_nop 1
v_max_u32_dpp v234, v234, v234 quad_perm:[2,3,0,1] row_mask:0xf bank_mask:0xf
s_nop 1
v_max_u32_dpp v234, v234, v234 row_half_mirror row_mask:0xf bank_mask:0xf
s_nop 1
v_cmp_ge_u32 vcc, v232, v234
s_nop 1
s_mov_b64 exec, vcc
buffer_store_dwordx2 v[232:233], v235, s[44:47], s92 offen sc1
s_mov_b64 exec, -1
; ---- out = h + .La5_LN(h) of the own rows; FFN weights into MFMA order
.La5_LT_OUT:
v_mul_f32 v244, v244, v246
v_mov_b32 v247, v246
v_xor_b32 v244, 0x80000000, v244
v_mov_b32 v245, v244
v_lshlrev_b32 v230, 16, v96
v_and_b32 v231, 0xffff0000, v96
v_pk_fma_f32 v[232:233], v[230:231], v[246:247], v[244:245]
cv v232,v232,v233
v_lshlrev_b32 v234, 16, v232
v_and_b32 v235, 0xffff0000, v232
v_pk_add_f32 v[230:231], v[230:231], v[234:235]
cv v192,v230,v231
v_lshlrev_b32 v230, 16, v97
v_and_b32 v231, 0xffff0000, v97
v_pk_fma_f32 v[232:233], v[230:231], v[246:247], v[244:245]
cv v232,v232,v233
v_lshlrev_b32 v234, 16, v232
v_and_b32 v235, 0xffff0000, v232
v_pk_add_f32 v[230:231], v[230:231], v[234:235]
cv v193,v230,v231
v_lshlrev_b32 v230, 16, v98
v_and_b32 v231, 0xffff0000, v98
v_pk_fma_f32 v[232:233], v[230:231], v[246:247], v[244:245]
cv v232,v232,v233
v_lshlrev_b32 v234, 16, v232
v_and_b32 v235, 0xffff0000, v232
v_pk_add_f32 v[230:231], v[230:231], v[234:235]
cv v194,v230,v231
v_lshlrev_b32 v230, 16, v99
v_and_b32 v231, 0xffff0000, v99
v_pk_fma_f32 v[232:233], v[230:231], v[246:247], v[244:245]
cv v232,v232,v233
v_lshlrev_b32 v234, 16, v232
v_and_b32 v235, 0xffff0000, v232
v_pk_add_f32 v[230:231], v[230:231], v[234:235]
cv v195,v230,v231
v_lshlrev_b32 v230, 16, v100
v_and_b32 v231, 0xffff0000, v100
v_pk_fma_f32 v[232:233], v[230:231], v[246:247], v[244:245]
cv v232,v232,v233
v_lshlrev_b32 v234, 16, v232
v_and_b32 v235, 0xffff0000, v232
v_pk_add_f32 v[230:231], v[230:231], v[234:235]
cv v196,v230,v231
v_lshlrev_b32 v230, 16, v101
v_and_b32 v231, 0xffff0000, v101
v_pk_fma_f32 v[232:233], v[230:231], v[246:247], v[244:245]
cv v232,v232,v233
v_lshlrev_b32 v234, 16, v232
v_and_b32 v235, 0xffff0000, v232
v_pk_add_f32 v[230:231], v[230:231], v[234:235]
cv v197,v230,v231
v_lshlrev_b32 v230, 16, v102
v_and_b32 v231, 0xffff0000, v102
v_pk_fma_f32 v[232:233], v[230:231], v[246:247], v[244:245]
cv v232,v232,v233
v_lshlrev_b32 v234, 16, v232
v_and_b32 v235, 0xffff0000, v232
v_pk_add_f32 v[230:231], v[230:231], v[234:235]
cv v198,v230,v231
v_lshlrev_b32 v230, 16, v103
v_and_b32 v231, 0xffff0000, v103
v_pk_fma_f32 v[232:233], v[230:231], v[246:247], v[244:245]
cv v232,v232,v233
v_lshlrev_b32 v234, 16, v232
v_and_b32 v235, 0xffff0000, v232
v_pk_add_f32 v[230:231], v[230:231], v[234:235]
cv v199,v230,v231
v_lshlrev_b32 v230, 16, v104
v_and_b32 v231, 0xffff0000, v104
v_pk_fma_f32 v[232:233], v[230:231], v[246:247], v[244:245]
cv v232,v232,v233
v_lshlrev_b32 v234, 16, v232
v_and_b32 v235, 0xffff0000, v232
v_pk_add_f32 v[230:231], v[230:231], v[234:235]
cv v200,v230,v231
v_lshlrev_b32 v230, 16, v105
v_and_b32 v231, 0xffff0000, v105
v_pk_fma_f32 v[232:233], v[230:231], v[246:247], v[244:245]
cv v232,v232,v233
v_lshlrev_b32 v234, 16, v232
v_and_b32 v235, 0xffff0000, v232
v_pk_add_f32 v[230:231], v[230:231], v[234:235]
cv v201,v230,v231
v_lshlrev_b32 v230, 16, v106
v_and_b32 v231, 0xffff0000, v106
v_pk_fma_f32 v[232:233], v[230:231], v[246:247], v[244:245]
cv v232,v232,v233
v_lshlrev_b32 v234, 16, v232
v_and_b32 v235, 0xffff0000, v232
v_pk_add_f32 v[230:231], v[230:231], v[234:235]
cv v202,v230,v231
v_lshlrev_b32 v230, 16, v107
v_and_b32 v231, 0xffff0000, v107
v_pk_fma_f32 v[232:233], v[230:231], v[246:247], v[244:245]
cv v232,v232,v233
v_lshlrev_b32 v234, 16, v232
v_and_b32 v235, 0xffff0000, v232
v_pk_add_f32 v[230:231], v[230:231], v[234:235]
cv v203,v230,v231
v_lshlrev_b32 v230, 16, v108
v_and_b32 v231, 0xffff0000, v108
v_pk_fma_f32 v[232:233], v[230:231], v[246:247], v[244:245]
cv v232,v232,v233
v_lshlrev_b32 v234, 16, v232
v_and_b32 v235, 0xffff0000, v232
v_pk_add_f32 v[230:231], v[230:231], v[234:235]
cv v204,v230,v231
v_lshlrev_b32 v230, 16, v109
v_and_b32 v231, 0xffff0000, v109
v_pk_fma_f32 v[232:233], v[230:231], v[246:247], v[244:245]
cv v232,v232,v233
v_lshlrev_b32 v234, 16, v232
v_and_b32 v235, 0xffff0000, v232
v_pk_add_f32 v[230:231], v[230:231], v[234:235]
cv v205,v230,v231
v_lshlrev_b32 v230, 16, v110
v_and_b32 v231, 0xffff0000, v110
v_pk_fma_f32 v[232:233], v[230:231], v[246:247], v[244:245]
cv v232,v232,v233
v_lshlrev_b32 v234, 16, v232
v_and_b32 v235, 0xffff0000, v232
v_pk_add_f32 v[230:231], v[230:231], v[234:235]
cv v206,v230,v231
v_lshlrev_b32 v230, 16, v111
v_and_b32 v231, 0xffff0000, v111
v_pk_fma_f32 v[232:233], v[230:231], v[246:247], v[244:245]
cv v232,v232,v233
v_lshlrev_b32 v234, 16, v232
v_and_b32 v235, 0xffff0000, v232
v_pk_add_f32 v[230:231], v[230:231], v[234:235]
cv v207,v230,v231
s_cmp_eq_u32 s30, 0
s_cbranch_scc1 .La5_LT_SEL
s_mov_b32 s94, 0x05040100
s_mov_b32 s95, 0x07060302
wt vmcnt(0)
ar v230,a224
ar v231,a228
v_perm_b32 v128, v231, v230, s94
v_perm_b32 v132, v231, v230, s95
ar v230,a232
ar v231,a236
v_perm_b32 v129, v231, v230, s94
v_perm_b32 v133, v231, v230, s95
ar v230,a240
ar v231,a244
v_perm_b32 v130, v231, v230, s94
v_perm_b32 v134, v231, v230, s95
ar v230,a248
ar v231,a252
v_perm_b32 v131, v231, v230, s94
v_perm_b32 v135, v231, v230, s95
ar v230,a225
ar v231,a229
v_perm_b32 v136, v231, v230, s94
v_perm_b32 v140, v231, v230, s95
ar v230,a233
ar v231,a237
v_perm_b32 v137, v231, v230, s94
v_perm_b32 v141, v231, v230, s95
ar v230,a241
ar v231,a245
v_perm_b32 v138, v231, v230, s94
v_perm_b32 v142, v231, v230, s95
ar v230,a249
ar v231,a253
v_perm_b32 v139, v231, v230, s94
v_perm_b32 v143, v231, v230, s95
ar v230,a226
ar v231,a230
v_perm_b32 v144, v231, v230, s94
v_perm_b32 v148, v231, v230, s95
ar v230,a234
ar v231,a238
v_perm_b32 v145, v231, v230, s94
v_perm_b32 v149, v231, v230, s95
ar v230,a242
ar v231,a246
v_perm_b32 v146, v231, v230, s94
v_perm_b32 v150, v231, v230, s95
ar v230,a250
ar v231,a254
v_perm_b32 v147, v231, v230, s94
v_perm_b32 v151, v231, v230, s95
ar v230,a227
ar v231,a231
v_perm_b32 v152, v231, v230, s94
v_perm_b32 v156, v231, v230, s95
ar v230,a235
ar v231,a239
v_perm_b32 v153, v231, v230, s94
v_perm_b32 v157, v231, v230, s95
ar v230,a243
ar v231,a247
v_perm_b32 v154, v231, v230, s94
v_perm_b32 v158, v231, v230, s95
ar v230,a251
ar v231,a255
v_perm_b32 v155, v231, v230, s94
v_perm_b32 v159, v231, v230, s95
ar v230,a64
ar v231,a68
v_perm_b32 v160, v231, v230, s94
v_perm_b32 v164, v231, v230, s95
ar v230,a72
ar v231,a76
v_perm_b32 v161, v231, v230, s94
v_perm_b32 v165, v231, v230, s95
ar v230,a80
ar v231,a84
v_perm_b32 v162, v231, v230, s94
v_perm_b32 v166, v231, v230, s95
ar v230,a88
ar v231,a92
v_perm_b32 v163, v231, v230, s94
v_perm_b32 v167, v231, v230, s95
ar v230,a65
ar v231,a69
v_perm_b32 v168, v231, v230, s94
v_perm_b32 v172, v231, v230, s95
ar v230,a73
ar v231,a77
v_perm_b32 v169, v231, v230, s94
v_perm_b32 v173, v231, v230, s95
ar v230,a81
ar v231,a85
v_perm_b32 v170, v231, v230, s94
v_perm_b32 v174, v231, v230, s95
ar v230,a89
ar v231,a93
v_perm_b32 v171, v231, v230, s94
v_perm_b32 v175, v231, v230, s95
ar v230,a66
ar v231,a70
v_perm_b32 v176, v231, v230, s94
v_perm_b32 v180, v231, v230, s95
ar v230,a74
ar v231,a78
v_perm_b32 v177, v231, v230, s94
v_perm_b32 v181, v231, v230, s95
ar v230,a82
ar v231,a86
v_perm_b32 v178, v231, v230, s94
v_perm_b32 v182, v231, v230, s95
ar v230,a90
ar v231,a94
v_perm_b32 v179, v231, v230, s94
v_perm_b32 v183, v231, v230, s95
ar v230,a67
ar v231,a71
v_perm_b32 v184, v231, v230, s94
v_perm_b32 v188, v231, v230, s95
ar v230,a75
ar v231,a79
v_perm_b32 v185, v231, v230, s94
v_perm_b32 v189, v231, v230, s95
ar v230,a83
ar v231,a87
v_perm_b32 v186, v231, v230, s94
v_perm_b32 v190, v231, v230, s95
ar v230,a91
ar v231,a95
v_perm_b32 v187, v231, v230, s94
v_perm_b32 v191, v231, v230, s95
s_and_b32 s92, s3, 1
s_lshr_b32 s93, s3, 1
v_lshrrev_b32 v230, 6, v255
v_and_b32 v231, 48, v255
v_lshl_add_u32 v240, v230, 10, v231
s_lshl_b32 s96, s92, 14
v_add_u32 v240, s96, v240
s_lshl_b32 s96, s92, 10
v_add_u32 v241, s96, v255
s_lshl_b32 s97, s3, 10
v_add_u32 v208, s97, v255
v_bfe_u32 v230, v255, 4, 1
v_bfe_u32 v231, v255, 6, 2
v_lshrrev_b32 v232, 8, v255
v_lshlrev_b32 v209, 11, v230
v_lshl_add_u32 v209, v231, 7, v209
v_lshl_add_u32 v209, v232, 5, v209
s_lshr_b32 s96, s96, 1
v_add_u32 v209, s96, v209
v_bfe_u32 v233, v255, 4, 2
v_mul_u32_u24 v210, 640, v233
v_lshl_add_u32 v210, v231, 3, v210
s_mul_i32 s96, s93, 2560
s_lshl_b32 s92, s92, 5
s_add_u32 s96, s96, s92
v_add_u32 v210, s96, v210
v_cmp_eq_u32 s[98:99], v231, v232
v_mul_u32_u24 v211, 5, v255
s_lshl_b32 s96, s3, 7
v_lshrrev_b32 v212, 3, v255
v_and_b32 v212, 62, v212
v_add_u32 v212, s96, v212
v_lshlrev_b32 v234, 6, v230
v_lshl_add_u32 v234, v232, 4, v234
v_add_u32 v234, s96, v234
v_mov_b32 v235, 1024
v_cmp_eq_u32 vcc, 0, v231
s_nop 1
v_cndmask_b32 v216, v235, v234, vcc
v_cmp_eq_u32 vcc, 1, v231
s_nop 1
v_cndmask_b32 v217, v235, v234, vcc
v_cmp_eq_u32 vcc, 2, v231
s_nop 1
v_cndmask_b32 v218, v235, v234, vcc
v_cmp_eq_u32 vcc, 3, v231
s_nop 1
v_cndmask_b32 v219, v235, v234, vcc
v_mov_b32 v220, 0
v_mov_b32 v221, 0
v_mov_b32 v222, 0
v_mov_b32_e64 v223, 0
ds_write_b128 v235, v[220:223] offset:10240
s_lshl_b32 s92, s100, 13
s_add_u32 s80, s92, 32768
s_add_u32 s20, s80, 4096
; ---- top two rows of the sequence; SwiGLU slice of 32 hidden units
.La5_LT_SEL:
.La5_LW4_1:
s_cbranch_execz .La5_LW4_R0
s_and_b32 s97, s2, 7
s_lshl_b32 s97, s97, 12
s_cmp_lg_u32 s3, 0
s_cbranch_scc1 .La5_LT_S2
s_mov_b32 s96, 0
v_and_b32 v229, 255, v255
.La5_LT_SA:
s_sleep 2
ld v[224:227],v229,s[44:47],s97 offen offset:0 sc1
s_add_u32 s96, s96, 1
s_lshr_b32 s92, s96, 20
wt vmcnt(0)
v_cmp_ne_u32 vcc, 0, v224
s_nop 0
s_cbranch_scc1 .La5_LT_S1
s_cmp_eq_u64 vcc, -1
s_cbranch_scc0 .La5_LT_SA
.La5_LT_S1:
s_cmp_lg_u32 s30, 0
s_cbranch_scc1 .La5_LT_S0
s_sleep 6
.La5_LT_S0:
ld v[224:227],v255,s[44:47],s97 offen offset:0 sc1
ld v[232:235],v255,s[44:47],s97 offen offset:1024 sc1
ld v[244:247],v255,s[44:47],s97 offen offset:2048 sc1
ld v[248:251],v255,s[44:47],s97 offen offset:3072 sc1
s_add_u32 s96, s96, 1
s_lshr_b32 s92, s96, 20
wt vmcnt(3)
v_min3_u32 v228, v224, v225, v226
v_min_u32 v228, v227, v228
wt vmcnt(2)
v_min3_u32 v228, v232, v233, v228
v_min3_u32 v228, v234, v235, v228
wt vmcnt(1)
v_min3_u32 v228, v244, v245, v228
v_min3_u32 v228, v246, v247, v228
wt vmcnt(0)
v_min3_u32 v228, v248, v249, v228
v_min3_u32 v228, v250, v251, v228
v_cmp_ne_u32 vcc, 0, v228
v_max_u32 v229, v224, v232
v_max_u32 v230, v244, v248
s_nop 0
s_nop 0
s_cbranch_scc1 .La5_LT_S3
s_cmp_eq_u64 vcc, -1
s_cbranch_scc0 .La5_LT_S1
.La5_LT_S3:
v_max_u32 v228, v229, v230
v_min_u32 v229, v229, v230
v_mov_b32 v231, v228
v_min_u32 v224, v224, v232
v_min_u32 v244, v244, v248
v_max3_u32 v229, v229, v224, v244
v_max3_u32 v230, v226, v234, v246
v_max3_u32 v229, v229, v230, v250
s_nop 1
v_max_u32_dpp v231, v231, v231 row_shr:1 row_mask:0xf bank_mask:0xf
s_nop 1
v_max_u32_dpp v231, v231, v231 row_shr:2 row_mask:0xf bank_mask:0xf
s_nop 1
v_max_u32_dpp v231, v231, v231 row_shr:4 row_mask:0xf bank_mask:0xf
s_nop 1
v_max_u32_dpp v231, v231, v231 row_shr:8 row_mask:0xf bank_mask:0xf
s_nop 1
v_max_u32_dpp v231, v231, v231 row_bcast:15 row_mask:0xa bank_mask:0xf
s_nop 1
v_max_u32_dpp v231, v231, v231 row_bcast:31 row_mask:0xc bank_mask:0xf
s_nop 0
v_readlane_b32 s82, v231, 63
s_nop 1
v_cmp_eq_u32 vcc, s82, v228
v_mov_b32 v230, s82
v_mov_b32 v224, 0
v_cndmask_b32 v228, v228, v229, vcc
s_nop 1
v_max_u32_dpp v228, v228, v228 row_shr:1 row_mask:0xf bank_mask:0xf
s_nop 1
v_max_u32_dpp v228, v228, v228 row_shr:2 row_mask:0xf bank_mask:0xf
s_nop 1
v_max_u32_dpp v228, v228, v228 row_shr:4 row_mask:0xf bank_mask:0xf
s_nop 1
v_max_u32_dpp v228, v228, v228 row_shr:8 row_mask:0xf bank_mask:0xf
s_nop 1
v_max_u32_dpp v228, v228, v228 row_bcast:15 row_mask:0xa bank_mask:0xf
s_nop 1
v_max_u32_dpp v228, v228, v228 row_bcast:31 row_mask:0xc bank_mask:0xf
s_nop 0
v_readlane_b32 s81, v228, 63
s_nop 1
v_mov_b32 v231, s81
ds_write_b64 v224, v[230:231] offset:64
.La5_LT_S2:
v_mov_b32_e64 v224, 0
wt lgkmcnt(0)
s_barrier
ds_read_b64 v[228:229], v224 offset:64
wt lgkmcnt(0)
v_readfirstlane_b32 s82, v228
v_readfirstlane_b32 s81, v229
s_nop 0
s_andn2_b32 s84, 0x7ff, s82
s_andn2_b32 s21, 0x7ff, s81
s_cmp_eq_u32 s30, 0
s_cbranch_scc1 .La5_LT_ST
s_cmp_lt_u32 s3, 2
s_cselect_b32 s91, s84, s21
s_cselect_b32 s85, s82, s81
s_lshr_b32 s92, s91, 4
s_lshl_b32 s92, s92, 15
s_and_b32 s93, s91, 15
s_lshl_b32 s93, s93, 6
s_add_u32 s92, s92, s93
s_add_u32 s93, s92, 4194304
s_add_u32 s94, s92, 8388608
s_add_u32 s95, s92, 12582912
ld v[224:227],v240,s[32:35],s92 offen sc1
ld v[228:231],v240,s[32:35],s93 offen sc1
ld v[232:235],v240,s[32:35],s94 offen sc1
ld v[244:247],v240,s[32:35],s95 offen sc1
s_lshl_b32 s96, s91, 11
s_and_b32 s92, s91, 0xf8
s_lshl4_add_u32 s97, s92, s97
s_lshr_b32 s92, s91, 8
s_lshl4_add_u32 s97, s92, s97
ld v[248:251],v241,s[68:71],s96 offen
ld v[220:223],off,s[44:47],s97 sc1
wt vmcnt(4)
v_lshlrev_b32 v236, 16, v224
v_and_b32 v237, 0xffff0000, v224
v_lshlrev_b32 v238, 16, v228
v_and_b32 v239, 0xffff0000, v228
v_pk_add_f32 v[236:237], v[236:237], v[238:239]
v_lshlrev_b32 v242, 16, v225
v_and_b32 v243, 0xffff0000, v225
v_lshlrev_b32 v238, 16, v229
v_and_b32 v239, 0xffff0000, v229
v_pk_add_f32 v[242:243], v[242:243], v[238:239]
v_lshlrev_b32 v252, 16, v226
v_and_b32 v253, 0xffff0000, v226
v_lshlrev_b32 v238, 16, v230
v_and_b32 v239, 0xffff0000, v230
v_pk_add_f32 v[252:253], v[252:253], v[238:239]
v_lshlrev_b32 v214, 16, v227
v_and_b32 v215, 0xffff0000, v227
v_lshlrev_b32 v238, 16, v231
v_and_b32 v239, 0xffff0000, v231
v_pk_add_f32 v[214:215], v[214:215], v[238:239]
wt vmcnt(2)
v_lshlrev_b32 v224, 16, v232
v_and_b32 v225, 0xffff0000, v232
v_lshlrev_b32 v226, 16, v244
v_and_b32 v227, 0xffff0000, v244
v_pk_add_f32 v[224:225], v[224:225], v[226:227]
v_pk_add_f32 v[236:237], v[236:237], v[224:225]
v_lshlrev_b32 v224, 16, v233
v_and_b32 v225, 0xffff0000, v233
v_lshlrev_b32 v226, 16, v245
v_and_b32 v227, 0xffff0000, v245
v_pk_add_f32 v[224:225], v[224:225], v[226:227]
v_pk_add_f32 v[242:243], v[242:243], v[224:225]
v_lshlrev_b32 v224, 16, v234
v_and_b32 v225, 0xffff0000, v234
v_lshlrev_b32 v226, 16, v246
v_and_b32 v227, 0xffff0000, v246
v_pk_add_f32 v[224:225], v[224:225], v[226:227]
v_pk_add_f32 v[252:253], v[252:253], v[224:225]
v_lshlrev_b32 v224, 16, v235
v_and_b32 v225, 0xffff0000, v235
v_lshlrev_b32 v226, 16, v247
v_and_b32 v227, 0xffff0000, v247
v_pk_add_f32 v[224:225], v[224:225], v[226:227]
v_pk_add_f32 v[214:215], v[214:215], v[224:225]
wt vmcnt(0)
v_cmp_eq_u32 vcc, s85, v220
s_nop 1
v_cndmask_b32 v220, v223, v221, vcc
v_lshlrev_b32 v222, 16, v220
v_rcp_f32 v222, v222
v_and_b32 v220, 0xffff0000, v220
v_mul_f32 v220, v220, v222
v_mov_b32 v223, v222
v_xor_b32 v220, 0x80000000, v220
v_mov_b32 v221, v220
v_lshlrev_b32 v224, 16, v248
v_and_b32 v225, 0xffff0000, v248
v_pk_add_f32 v[236:237], v[236:237], v[224:225]
cv v224,v236,v237
v_lshlrev_b32 v226, 16, v224
v_and_b32 v227, 0xffff0000, v224
v_pk_fma_f32 v[226:227], v[226:227], v[222:223], v[220:221]
cv v228,v226,v227
v_lshlrev_b32 v224, 16, v249
v_and_b32 v225, 0xffff0000, v249
v_pk_add_f32 v[242:243], v[242:243], v[224:225]
cv v224,v242,v243
v_lshlrev_b32 v226, 16, v224
v_and_b32 v227, 0xffff0000, v224
.La5_LW4_2:
s_cbranch_execz .La5_LW4_R0
v_pk_fma_f32 v[226:227], v[226:227], v[222:223], v[220:221]
cv v229,v226,v227
v_lshlrev_b32 v224, 16, v250
v_and_b32 v225, 0xffff0000, v250
v_pk_add_f32 v[252:253], v[252:253], v[224:225]
cv v224,v252,v253
v_lshlrev_b32 v226, 16, v224
v_and_b32 v227, 0xffff0000, v224
v_pk_fma_f32 v[226:227], v[226:227], v[222:223], v[220:221]
cv v230,v226,v227
v_lshlrev_b32 v224, 16, v251
v_and_b32 v225, 0xffff0000, v251
v_pk_add_f32 v[214:215], v[214:215], v[224:225]
cv v224,v214,v215
v_lshlrev_b32 v226, 16, v224
v_and_b32 v227, 0xffff0000, v224
v_pk_fma_f32 v[226:227], v[226:227], v[222:223], v[220:221]
cv v231,v226,v227
ds_write_b128 v208, v[228:231] offset:1024
wt lgkmcnt(0)
s_barrier
rd v[224:227],v209 offset:1024
rd v[232:235],v209 offset:1040
rd v[244:247],v209 offset:2048
rd v[248:251],v209 offset:2064
wt lgkmcnt(3)
mm a[0:3],v[224:227],a[96:99],0
mm a[4:7],v[224:227],a[100:103],0
mm a[8:11],v[224:227],a[104:107],0
mm a[12:15],v[224:227],a[108:111],0
mm a[16:19],v[224:227],a[112:115],0
mm a[20:23],v[224:227],a[116:119],0
mm a[24:27],v[224:227],a[120:123],0
mm a[28:31],v[224:227],a[124:127],0
wt lgkmcnt(2)
mm a[0:3],v[232:235],a[128:131],a[0:3]
mm a[4:7],v[232:235],a[132:135],a[4:7]
mm a[8:11],v[232:235],a[136:139],a[8:11]
mm a[12:15],v[232:235],a[140:143],a[12:15]
mm a[16:19],v[232:235],a[144:147],a[16:19]
mm a[20:23],v[232:235],a[148:151],a[20:23]
mm a[24:27],v[232:235],a[152:155],a[24:27]
mm a[28:31],v[232:235],a[156:159],a[28:31]
wt lgkmcnt(1)
mm a[0:3],v[244:247],a[160:163],a[0:3]
mm a[4:7],v[244:247],a[164:167],a[4:7]
mm a[8:11],v[244:247],a[168:171],a[8:11]
mm a[12:15],v[244:247],a[172:175],a[12:15]
mm a[16:19],v[244:247],a[176:179],a[16:19]
mm a[20:23],v[244:247],a[180:183],a[20:23]
mm a[24:27],v[244:247],a[184:187],a[24:27]
mm a[28:31],v[244:247],a[188:191],a[28:31]
wt lgkmcnt(0)
mm a[0:3],v[248:251],a[192:195],a[0:3]
mm a[4:7],v[248:251],a[196:199],a[4:7]
mm a[8:11],v[248:251],a[200:203],a[8:11]
mm a[12:15],v[248:251],a[204:207],a[12:15]
mm a[16:19],v[248:251],a[208:211],a[16:19]
mm a[20:23],v[248:251],a[212:215],a[20:23]
mm a[24:27],v[248:251],a[216:219],a[24:27]
mm a[28:31],v[248:251],a[220:223],a[28:31]
s_mov_b64 exec, s[98:99]
ds_write_b64 v210, a[0:1] offset:5120
ds_write_b64 v210, a[4:5] offset:5200
ds_write_b64 v210, a[8:9] offset:5280
ds_write_b64 v210, a[12:13] offset:5360
ds_write_b64 v210, a[16:17] offset:5440
s_nop 3
ds_write_b64 v210, a[20:21] offset:5520
s_nop 3
ds_write_b64 v210, a[24:25] offset:5600
s_nop 3
ds_write_b64 v210, a[28:29] offset:5680
s_mov_b64 exec, -1
wt lgkmcnt(0)
s_barrier
rd v[224:227],v211 offset:5120
rd v[232:235],v211 offset:5136
rd v[244:247],v211 offset:5152
rd v[248:251],v211 offset:5168
wt lgkmcnt(2)
v_pk_add_f32 v[224:225], v[224:225], v[226:227]
v_pk_add_f32 v[232:233], v[232:233], v[234:235]
wt lgkmcnt(0)
v_pk_add_f32 v[244:245], v[244:245], v[246:247]
v_pk_add_f32 v[248:249], v[248:249], v[250:251]
v_pk_add_f32 v[224:225], v[224:225], v[232:233]
v_pk_add_f32 v[244:245], v[244:245], v[248:249]
v_pk_add_f32 v[224:225], v[224:225], v[244:245]
cv v224,v224,v225
v_mov_b32 v226, v224
s_nop 1
v_permlane32_swap_b32 v224, v226
s_nop 1
v_lshlrev_b32 v228, 16, v224
v_and_b32 v229, 0xffff0000, v224
v_lshlrev_b32 v232, 16, v226
v_and_b32 v233, 0xffff0000, v226
v_mul_f32 v230, 0xbfb8aa3b, v228
v_mul_f32 v231, 0xbfb8aa3b, v229
ex v230,v230
ex v231,v231
v_add_f32 v230, 1.0, v230
v_add_f32 v231, 1.0, v231
v_rcp_f32 v230, v230
v_rcp_f32 v231, v231
s_nop 0
v_pk_mul_f32 v[228:229], v[228:229], v[230:231]
cv v228,v228,v229
v_lshlrev_b32 v230, 16, v228
v_and_b32 v231, 0xffff0000, v228
v_pk_mul_f32 v[230:231], v[230:231], v[232:233]
cv v228,v230,v231
ds_write_b16 v212, v228 offset:10240
ds_write_b16_d16_hi v212, v228 offset:10304
wt lgkmcnt(0)
rd v[224:227],v216 offset:10240
rd v[232:235],v217 offset:10240
rd v[244:247],v218 offset:10240
rd v[248:251],v219 offset:10240
wt lgkmcnt(3)
mm v[0:3],v[224:227],v[128:131],0
mm v[4:7],v[224:227],v[132:135],0
mm v[8:11],v[224:227],v[136:139],0
mm v[12:15],v[224:227],v[140:143],0
wt lgkmcnt(2)
mm v[0:3],v[232:235],v[144:147],v[0:3]
mm v[4:7],v[232:235],v[148:151],v[4:7]
mm v[8:11],v[232:235],v[152:155],v[8:11]
mm v[12:15],v[232:235],v[156:159],v[12:15]
wt lgkmcnt(1)
mm v[0:3],v[244:247],v[160:163],v[0:3]
mm v[4:7],v[244:247],v[164:167],v[4:7]
mm v[8:11],v[244:247],v[168:171],v[8:11]
mm v[12:15],v[244:247],v[172:175],v[12:15]
wt lgkmcnt(0)
mm v[0:3],v[248:251],v[176:179],v[0:3]
mm v[4:7],v[248:251],v[180:183],v[4:7]
mm v[8:11],v[248:251],v[184:187],v[8:11]
mm v[12:15],v[248:251],v[188:191],v[12:15]
s_nop 7
v_or_b32 v16, 1, v0
v_or_b32 v20, 1, v1
v_or_b32 v17, 1, v4
v_or_b32 v21, 1, v5
v_or_b32 v18, 1, v8
v_or_b32 v22, 1, v9
v_or_b32 v19, 1, v12
v_or_b32 v23, 1, v13
st v[16:19],v208,s[44:47],s80 offen sc1
st v[20:23],v208,s[44:47],s20 offen sc1
; ---- rows that were not routed leave; final sums of the routed rows
.La5_LT_ST:
.La5_LW4_3:
s_cbranch_execz .La5_LW4_R0
s_lshl_b32 s92, s76, 5
s_add_u32 s92, s92, s90
s_lshl2_add_u32 s92, s92, s3
s_lshl_b32 s93, s92, 1
v_bfe_u32 v230, v255, 6, 1
v_add_u32 v231, s93, v230
v_cmp_ne_u32 vcc, s84, v231
v_cmp_ne_u32 s[94:95], s21, v231
v_lshrrev_b32 v232, 7, v255
v_and_b32 v233, 48, v255
v_lshl_add_u32 v232, v232, 6, v233
v_lshl_add_u32 v232, v230, 11, v232
s_lshl_b32 s92, s92, 12
s_and_b64 exec, vcc, s[94:95]
s_cbranch_scc0 .La5_LT_F0
st v[192:195],v232,s[56:59],s92 offen offset:0 sc1
st v[196:199],v232,s[56:59],s92 offen offset:512 sc1
st v[200:203],v232,s[56:59],s92 offen offset:1024 sc1
st v[204:207],v232,s[56:59],s92 offen offset:1536 sc1
.La5_LT_F0:
s_mov_b64 exec, -1
s_cmp_eq_u32 s75, 1
s_nop 0
s_nop 0
s_cbranch_scc1 .La5_LT_END
s_lshr_b32 s92, s2, 6
s_lshl2_add_u32 s92, s92, s74
s_and_b32 s93, s2, 7
s_lshl3_add_u32 s91, s92, s93
s_cmp_lt_u32 s91, 64
s_cselect_b32 s84, s84, s21
s_cselect_b32 s82, s82, s81
s_bfe_u32 s92, s91, 0x30003
s_and_b32 s93, s91, 3
s_lshl2_add_u32 s96, s92, s93
s_and_b32 s85, s91, 4
s_lshl_b32 s85, s85, 1
v_bfe_u32 v0, v255, 6, 2
v_bfe_u32 v1, v255, 4, 2
v_lshlrev_b32 v1, 1, v1
v_lshl_add_u32 v0, v0, 4, v1
v_add_u32 v0, s85, v0
s_lshl_b32 s85, s96, 10
v_add_u32 v1, s85, v0
s_lshl_b32 s85, s96, 6
v_add_u32 v0, s85, v0
s_lshr_b32 s92, s84, 4
s_lshl_b32 s92, s92, 15
s_and_b32 s93, s84, 15
s_lshl_b32 s93, s93, 6
s_add_u32 s92, s92, s93
s_add_u32 s93, s92, 4194304
s_add_u32 s94, s92, 8388608
s_add_u32 s95, s92, 12582912
buffer_load_ushort v4, v1, s[32:35], s92 offen sc1
buffer_load_ushort v5, v1, s[32:35], s93 offen sc1
buffer_load_ushort v6, v1, s[32:35], s94 offen sc1
buffer_load_ushort v7, v1, s[32:35], s95 offen sc1
s_lshl_b32 s21, s84, 11
s_and_b32 s92, s84, 0xf8
s_lshl4_add_u32 s97, s92, s97
s_lshr_b32 s92, s84, 8
s_lshl4_add_u32 s97, s92, s97
buffer_load_ushort v8, v0, s[68:71], s21 offen
ld v[12:15],off,s[44:47],s97 sc1
v_lshrrev_b32 v2, 6, v255
v_lshlrev_b32 v2, 13, v2
v_and_b32 v3, 48, v255
v_add_u32 v2, v2, v3
s_lshl_b32 s92, s3, 17
s_lshl_b32 s93, s91, 6
s_add_u32 s92, s92, s93
s_add_u32 s94, s92, 32768
s_cmp_eq_u32 s3, 3
s_cselect_b32 s95, 0, 524288
s_cselect_b32 s96, 0, -1
s_add_u32 s95, s95, s94
s_ashr_i32 s92, s82, 31
s_orn2_b32 s92, 0x80000000, s92
s_xor_b32 s92, s92, s82
s_and_b32 s92, s92, 0xffff0000
v_mov_b32 v9, 0xbfb8aa3b
v_mul_f32 v9, s92, v9
ex v9,v9
v_lshrrev_b32 v3, 8, v255
s_lshl_b32 s92, s3, 4
v_add_f32 v9, 1.0, v9
v_lshl_add_u32 v3, v3, 2, s92
v_rcp_f32 v9, v9
v_and_b32 v10, 48, v255
v_mul_u32_u24 v10, 20, v10
v_add_u32 v3, v3, v10
cv v9,v9,v9
v_lshlrev_b32 v9, 16, v9
v_and_b32 v10, 0xf0, v255
v_mul_u32_u24 v10, 5, v10
wt vmcnt(0)
v_cmp_eq_u32 vcc, s82, v12
s_nop 1
v_cndmask_b32 v12, v15, v13, vcc
v_lshlrev_b32 v14, 16, v12
v_rcp_f32 v14, v14
v_and_b32 v12, 0xffff0000, v12
v_mul_f32 v12, v12, v14
v_mov_b32 v15, v14
v_xor_b32 v12, 0x80000000, v12
v_mov_b32 v13, v12
v_lshlrev_b32 v4, 16, v4
v_lshlrev_b32 v5, 16, v5
v_lshlrev_b32 v6, 16, v6
v_lshlrev_b32 v7, 16, v7
v_lshlrev_b32 v8, 16, v8
v_add_f32 v4, v4, v5
v_add_f32 v6, v6, v7
v_add_f32 v4, v4, v6
v_add_f32 v4, v4, v8
cv v4,v4,v4
v_lshlrev_b32 v4, 16, v4
v_fma_f32 v5, v4, v14, v12
cv v5,v5,v5
v_lshlrev_b32 v5, 16, v5
s_sleep 54
s_mov_b32 s93, 0
.La5_LT_Y:
ld v[16:19],v2,s[44:47],s94 offen sc1
ld v[20:23],v2,s[44:47],s95 offen sc1
s_add_u32 s93, s93, 1
s_lshr_b32 s92, s93, 20
wt vmcnt(0)
v_min3_u32 v24, v16, v17, v18
v_min3_u32 v25, v20, v21, v22
v_min3_u32 v24, v24, v19, v23
v_min_u32 v24, v24, v25
v_cmp_ne_u32 vcc, 0, v24
v_and_b32 v20, s96, v20
v_and_b32 v21, s96, v21
v_and_b32 v22, s96, v22
v_and_b32 v23, s96, v23
s_cbranch_scc1 .La5_LT_Y1
s_cmp_eq_u64 vcc, -1
s_cbranch_scc0 .La5_LT_Y
.La5_LT_Y1:
v_pk_add_f32 v[16:17], v[16:17], v[20:21]
v_pk_add_f32 v[18:19], v[18:19], v[22:23]
s_nop 0
v_add_f32_dpp v16, v16, v16 row_ror:4 row_mask:0xf bank_mask:0xf
v_add_f32_dpp v17, v17, v17 row_ror:4 row_mask:0xf bank_mask:0xf
v_add_f32_dpp v18, v18, v18 row_ror:4 row_mask:0xf bank_mask:0xf
v_add_f32_dpp v19, v19, v19 row_ror:4 row_mask:0xf bank_mask:0xf
v_add_f32_dpp v16, v16, v16 row_ror:8 row_mask:0xf bank_mask:0xf
v_add_f32_dpp v17, v17, v17 row_ror:8 row_mask:0xf bank_mask:0xf
v_add_f32_dpp v18, v18, v18 row_ror:8 row_mask:0xf bank_mask:0xf
v_add_f32_dpp v19, v19, v19 row_ror:8 row_mask:0xf bank_mask:0xf
ds_write_b32 v3, v16 offset:5120
ds_write_b32 v3, v17 offset:5200
ds_write_b32 v3, v18 offset:5280
ds_write_b32 v3, v19 offset:5360
wt lgkmcnt(0)
s_barrier
rd v[16:19],v10 offset:5120
rd v[20:23],v10 offset:5136
rd v[24:27],v10 offset:5152
rd v[28:31],v10 offset:5168
wt lgkmcnt(2)
v_pk_add_f32 v[16:17], v[16:17], v[18:19]
v_pk_add_f32 v[20:21], v[20:21], v[22:23]
wt lgkmcnt(0)
v_pk_add_f32 v[24:25], v[24:25], v[26:27]
v_pk_add_f32 v[28:29], v[28:29], v[30:31]
v_pk_add_f32 v[16:17], v[16:17], v[20:21]
v_pk_add_f32 v[24:25], v[24:25], v[28:29]
v_pk_add_f32 v[16:17], v[16:17], v[24:25]
v_add_f32 v16, v16, v17
cv v16,v16,v16
v_lshlrev_b32 v16, 16, v16
v_mul_f32 v16, v16, v9
cv v16,v16,v16
v_lshlrev_b32 v16, 16, v16
v_add_f32 v16, v16, v5
cv v16,v16,v16
v_lshlrev_b32 v16, 16, v16
v_add_f32 v16, v16, v4
cv v16,v16,v16
s_cmp_lg_u32 s3, 0
s_cbranch_scc1 .La5_LT_END
s_mov_b64 exec, 0xffff
buffer_store_short v16, v0, s[56:59], s21 offen
s_mov_b64 exec, -1
.La5_LT_END:
.La5_LW4_4:
s_cbranch_execz .La5_LW4_R0
s_lshl2_add_u32 s92, s2, s3
s_cmp_lg_u32 s92, 992
s_cbranch_scc1 .La5_LT_E1
buffer_store_dwordx2 v[252:253], off, s[64:67], 0 sc1
.La5_LT_E1:
s_branch .La5_LEND
; ---- fallback: the earlier 32 rows against all their keys with the true running maximum (online softmax)
.La5_LA_SLOW0:
wt lgkmcnt(0)
s_nop 7
s_nop 7
aw a160,v192
aw a161,v193
aw a162,v194
aw a163,v195
aw a164,v196
aw a165,v197
aw a166,v198
aw a167,v199
aw a168,v200
aw a169,v201
aw a170,v202
aw a171,v203
aw a172,v204
aw a173,v205
aw a174,v206
aw a175,v207
aw a176,v208
aw a177,v209
aw a178,v210
aw a179,v211
aw a180,v212
aw a181,v213
aw a182,v214
aw a183,v215
aw a184,v216
aw a185,v217
aw a186,v218
aw a187,v219
aw a188,v220
aw a189,v221
aw a190,v222
aw a191,v223
s_lshl_b32 s96, s72, 19
s_add_u32 s96, s96, 20971520
s_add_u32 s92, s26, s96
s_addc_u32 s93, s27, 0
s_and_b32 s93, s93, 0xffff
s_mov_b32 s94, 17301504
s_mov_b32 s95, 0x20000
v_mov_b32 v0, 0
v_mov_b32 v1, 0
v_mov_b32 v2, 0
v_mov_b32 v3, 0
v_mov_b32 v4, 0
v_mov_b32 v5, 0
v_mov_b32 v6, 0
v_mov_b32 v7, 0
v_mov_b32 v8, 0
v_mov_b32 v9, 0
v_mov_b32 v10, 0
v_mov_b32 v11, 0
v_mov_b32 v12, 0
v_mov_b32 v13, 0
v_mov_b32 v14, 0
v_mov_b32 v15, 0
v_mov_b32 v16, 0
v_mov_b32 v17, 0
v_mov_b32 v18, 0
v_mov_b32 v19, 0
v_mov_b32 v20, 0
v_mov_b32 v21, 0
v_mov_b32 v22, 0
v_mov_b32 v23, 0
v_mov_b32 v24, 0
v_mov_b32 v25, 0
v_mov_b32 v26, 0
v_mov_b32 v27, 0
v_mov_b32 v28, 0
v_mov_b32 v29, 0
v_mov_b32 v30, 0
v_mov_b32 v31, 0
v_mov_b32 v32, 0
v_mov_b32 v33, 0
v_mov_b32 v34, 0
v_mov_b32 v35, 0
v_mov_b32 v36, 0
v_mov_b32 v37, 0
v_mov_b32 v38, 0
v_mov_b32 v39, 0
v_mov_b32 v40, 0
v_mov_b32 v41, 0
v_mov_b32 v42, 0
v_mov_b32 v43, 0
v_mov_b32 v44, 0
v_mov_b32 v45, 0
v_mov_b32 v46, 0
v_mov_b32 v47, 0
v_mov_b32 v48, 0
v_mov_b32 v49, 0
v_mov_b32 v50, 0
v_mov_b32 v51, 0
v_mov_b32 v52, 0
v_mov_b32 v53, 0
v_mov_b32 v54, 0
v_mov_b32 v55, 0
v_mov_b32 v56, 0
v_mov_b32 v57, 0
v_mov_b32 v58, 0
v_mov_b32 v59, 0
v_mov_b32 v60, 0
v_mov_b32 v61, 0
v_mov_b32 v62, 0
v_mov_b32 v63, 0
v_mov_b32 v216, 0xff61b1e6
v_mov_b32 v217, 0xff61b1e6
v_mov_b32 v218, 0
v_mov_b32 v219, 0
s_mov_b32 s7, 0
.La5_LA_SLOW0_L:
s_lshl_b32 s96, s7, 13
s_add_u32 s97, s96, 4096
ld a[128:131],v255,s[92:95],s96 offen offset:0 sc1
ld a[132:135],v255,s[92:95],s96 offen offset:1024 sc1
ld a[136:139],v255,s[92:95],s96 offen offset:2048 sc1
ld a[140:143],v255,s[92:95],s96 offen offset:3072 sc1
ld a[144:147],v255,s[92:95],s97 offen offset:0 sc1
ld a[148:151],v255,s[92:95],s97 offen offset:1024 sc1
ld a[152:155],v255,s[92:95],s97 offen offset:2048 sc1
ld a[156:159],v255,s[92:95],s97 offen offset:3072 sc1
s_lshl_b32 s96, s76, 3
s_sub_u32 s96, s96, s7
s_add_u32 s96, s96, s3
s_lshl_b32 s96, s96, 5
v_bfe_u32 v220, v255, 4, 4
v_lshrrev_b32 v221, 8, v255
v_lshlrev_b32 v221, 2, v221
v_sub_u32 v220, v220, v221
v_add_u32 v220, s96, v220
v_mov_b32 v222, 0xceabfb8f
wt vmcnt(0)
mm v[192:195],a[128:131],v[128:131],0
mm v[192:195],a[132:135],v[132:135],v[192:195]
mm v[192:195],a[136:139],v[136:139],v[192:195]
mm v[192:195],a[140:143],v[140:143],v[192:195]
mm v[196:199],a[144:147],v[128:131],0
mm v[196:199],a[148:151],v[132:135],v[196:199]
mm v[196:199],a[152:155],v[136:139],v[196:199]
mm v[196:199],a[156:159],v[140:143],v[196:199]
mm v[200:203],a[128:131],v[144:147],0
mm v[200:203],a[132:135],v[148:151],v[200:203]
mm v[200:203],a[136:139],v[152:155],v[200:203]
mm v[200:203],a[140:143],v[156:159],v[200:203]
mm v[204:207],a[144:147],v[144:147],0
mm v[204:207],a[148:151],v[148:151],v[204:207]
mm v[204:207],a[152:155],v[152:155],v[204:207]
mm v[204:207],a[156:159],v[156:159],v[204:207]
s_nop 7
s_nop 7
s_lshl_b32 s96, s7, 13
s_add_u32 s96, s96, 16777216
s_add_u32 s97, s96, 4096
ld a[128:131],v255,s[92:95],s96 offen offset:0 sc1
ld a[132:135],v255,s[92:95],s96 offen offset:1024 sc1
ld a[136:139],v255,s[92:95],s96 offen offset:2048 sc1
ld a[140:143],v255,s[92:95],s96 offen offset:3072 sc1
ld a[144:147],v255,s[92:95],s97 offen offset:0 sc1
ld a[148:151],v255,s[92:95],s97 offen offset:1024 sc1
ld a[152:155],v255,s[92:95],s97 offen offset:2048 sc1
ld a[156:159],v255,s[92:95],s97 offen offset:3072 sc1
v_cmp_gt_i32 vcc, 0, v220
s_nop 1
v_cndmask_b32 v192, v192, v222, vcc
v_cmp_gt_i32 vcc, 1, v220
s_nop 1
v_cndmask_b32 v193, v193, v222, vcc
v_cmp_gt_i32 vcc, 2, v220
s_nop 1
v_cndmask_b32 v194, v194, v222, vcc
v_cmp_gt_i32 vcc, 3, v220
s_nop 1
v_cndmask_b32 v195, v195, v222, vcc
v_cmp_gt_i32 vcc, 16, v220
s_nop 1
v_cndmask_b32 v196, v196, v222, vcc
v_cmp_gt_i32 vcc, 17, v220
s_nop 1
v_cndmask_b32 v197, v197, v222, vcc
v_cmp_gt_i32 vcc, 18, v220
s_nop 1
v_cndmask_b32 v198, v198, v222, vcc
v_cmp_gt_i32 vcc, 19, v220
s_nop 1
v_cndmask_b32 v199, v199, v222, vcc
v_cmp_gt_i32 vcc, -16, v220
s_nop 1
v_cndmask_b32 v200, v200, v222, vcc
v_cmp_gt_i32 vcc, -15, v220
s_nop 1
v_cndmask_b32 v201, v201, v222, vcc
v_cmp_gt_i32 vcc, -14, v220
s_nop 1
v_cndmask_b32 v202, v202, v222, vcc
v_cmp_gt_i32 vcc, -13, v220
s_nop 1
v_cndmask_b32 v203, v203, v222, vcc
v_cmp_gt_i32 vcc, 0, v220
s_nop 1
v_cndmask_b32 v204, v204, v222, vcc
v_cmp_gt_i32 vcc, 1, v220
s_nop 1
v_cndmask_b32 v205, v205, v222, vcc
v_cmp_gt_i32 vcc, 2, v220
s_nop 1
v_cndmask_b32 v206, v206, v222, vcc
v_cmp_gt_i32 vcc, 3, v220
s_nop 1
v_cndmask_b32 v207, v207, v222, vcc
v_max3_f32 v222, v192, v193, v216
v_max3_f32 v222, v194, v195, v222
v_max3_f32 v222, v196, v197, v222
v_max3_f32 v222, v198, v199, v222
v_mov_b32 v221, v222
s_nop 1
v_permlane16_swap_b32 v221, v222
s_nop 1
v_max_f32 v222, v222, v221
v_mov_b32 v221, v222
s_nop 1
v_permlane32_swap_b32 v221, v222
s_nop 1
v_max_f32 v222, v222, v221
v_sub_f32 v223, v216, v222
v_mov_b32 v216, v222
ex v223,v223
v_sub_f32 v192, v192, v222
v_sub_f32 v193, v193, v222
v_sub_f32 v194, v194, v222
v_sub_f32 v195, v195, v222
v_sub_f32 v196, v196, v222
v_sub_f32 v197, v197, v222
v_sub_f32 v198, v198, v222
v_sub_f32 v199, v199, v222
ex v192,v192
ex v193,v193
ex v194,v194
ex v195,v195
ex v196,v196
ex v197,v197
ex v198,v198
ex v199,v199
v_mul_f32 v218, v218, v223
v_add_f32 v218, v218, v192
v_add_f32 v218, v218, v193
v_add_f32 v218, v218, v194
v_add_f32 v218, v218, v195
v_add_f32 v218, v218, v196
v_add_f32 v218, v218, v197
v_add_f32 v218, v218, v198
v_add_f32 v218, v218, v199
cv v208,v192,v193
cv v209,v194,v195
cv v210,v196,v197
cv v211,v198,v199
v_mul_f32 v0, v0, v223
v_mul_f32 v1, v1, v223
v_mul_f32 v2, v2, v223
v_mul_f32 v3, v3, v223
v_mul_f32 v4, v4, v223
v_mul_f32 v5, v5, v223
v_mul_f32 v6, v6, v223
v_mul_f32 v7, v7, v223
v_mul_f32 v8, v8, v223
v_mul_f32 v9, v9, v223
v_mul_f32 v10, v10, v223
v_mul_f32 v11, v11, v223
v_mul_f32 v12, v12, v223
v_mul_f32 v13, v13, v223
v_mul_f32 v14, v14, v223
v_mul_f32 v15, v15, v223
v_mul_f32 v16, v16, v223
v_mul_f32 v17, v17, v223
v_mul_f32 v18, v18, v223
v_mul_f32 v19, v19, v223
v_mul_f32 v20, v20, v223
v_mul_f32 v21, v21, v223
v_mul_f32 v22, v22, v223
v_mul_f32 v23, v23, v223
v_mul_f32 v24, v24, v223
v_mul_f32 v25, v25, v223
v_mul_f32 v26, v26, v223
v_mul_f32 v27, v27, v223
v_mul_f32 v28, v28, v223
v_mul_f32 v29, v29, v223
v_mul_f32 v30, v30, v223
v_mul_f32 v31, v31, v223
v_max3_f32 v222, v200, v201, v217
v_max3_f32 v222, v202, v203, v222
v_max3_f32 v222, v204, v205, v222
v_max3_f32 v222, v206, v207, v222
v_mov_b32 v221, v222
s_nop 1
v_permlane16_swap_b32 v221, v222
s_nop 1
v_max_f32 v222, v222, v221
v_mov_b32 v221, v222
s_nop 1
v_permlane32_swap_b32 v221, v222
s_nop 1
v_max_f32 v222, v222, v221
v_sub_f32 v223, v217, v222
v_mov_b32 v217, v222
ex v223,v223
v_sub_f32 v200, v200, v222
v_sub_f32 v201, v201, v222
v_sub_f32 v202, v202, v222
v_sub_f32 v203, v203, v222
v_sub_f32 v204, v204, v222
v_sub_f32 v205, v205, v222
v_sub_f32 v206, v206, v222
v_sub_f32 v207, v207, v222
ex v200,v200
ex v201,v201
ex v202,v202
ex v203,v203
ex v204,v204
ex v205,v205
ex v206,v206
ex v207,v207
v_mul_f32 v219, v219, v223
v_add_f32 v219, v219, v200
v_add_f32 v219, v219, v201
v_add_f32 v219, v219, v202
v_add_f32 v219, v219, v203
v_add_f32 v219, v219, v204
v_add_f32 v219, v219, v205
v_add_f32 v219, v219, v206
v_add_f32 v219, v219, v207
cv v212,v200,v201
cv v213,v202,v203
cv v214,v204,v205
cv v215,v206,v207
v_mul_f32 v32, v32, v223
v_mul_f32 v33, v33, v223
v_mul_f32 v34, v34, v223
v_mul_f32 v35, v35, v223
v_mul_f32 v36, v36, v223
v_mul_f32 v37, v37, v223
v_mul_f32 v38, v38, v223
v_mul_f32 v39, v39, v223
v_mul_f32 v40, v40, v223
v_mul_f32 v41, v41, v223
v_mul_f32 v42, v42, v223
v_mul_f32 v43, v43, v223
v_mul_f32 v44, v44, v223
v_mul_f32 v45, v45, v223
v_mul_f32 v46, v46, v223
v_mul_f32 v47, v47, v223
v_mul_f32 v48, v48, v223
v_mul_f32 v49, v49, v223
v_mul_f32 v50, v50, v223
v_mul_f32 v51, v51, v223
v_mul_f32 v52, v52, v223
v_mul_f32 v53, v53, v223
v_mul_f32 v54, v54, v223
v_mul_f32 v55, v55, v223
v_mul_f32 v56, v56, v223
v_mul_f32 v57, v57, v223
v_mul_f32 v58, v58, v223
v_mul_f32 v59, v59, v223
v_mul_f32 v60, v60, v223
v_mul_f32 v61, v61, v223
v_mul_f32 v62, v62, v223
v_mul_f32_e64 v63, v63, v223
wt vmcnt(0)
mm v[0:3],a[128:131],v[208:211],v[0:3]
mm v[32:35],a[128:131],v[212:215],v[32:35]
mm v[4:7],a[132:135],v[208:211],v[4:7]
mm v[36:39],a[132:135],v[212:215],v[36:39]
mm v[8:11],a[136:139],v[208:211],v[8:11]
mm v[40:43],a[136:139],v[212:215],v[40:43]
mm v[12:15],a[140:143],v[208:211],v[12:15]
mm v[44:47],a[140:143],v[212:215],v[44:47]
mm v[16:19],a[144:147],v[208:211],v[16:19]
mm v[48:51],a[144:147],v[212:215],v[48:51]
mm v[20:23],a[148:151],v[208:211],v[20:23]
mm v[52:55],a[148:151],v[212:215],v[52:55]
mm v[24:27],a[152:155],v[208:211],v[24:27]
mm v[56:59],a[152:155],v[212:215],v[56:59]
mm v[28:31],a[156:159],v[208:211],v[28:31]
mm v[60:63],a[156:159],v[212:215],v[60:63]
s_nop 7
s_nop 7
s_add_u32 s7, s7, 1
s_lshr_b32 s96, s7, 3
s_cmp_le_u32 s96, s76
s_cbranch_scc1 .La5_LA_SLOW0_L
v_mov_b32 v221, v218
s_nop 1
v_permlane16_swap_b32 v221, v218
s_nop 1
v_add_f32 v218, v218, v221
v_mov_b32 v221, v218
s_nop 1
v_permlane32_swap_b32 v221, v218
s_nop 1
v_add_f32 v218, v218, v221
v_mov_b32 v221, v219
s_nop 1
v_permlane16_swap_b32 v221, v219
s_nop 1
v_add_f32 v219, v219, v221
v_mov_b32 v221, v219
s_nop 1
v_permlane32_swap_b32 v221, v219
s_nop 1
v_add_f32 v219, v219, v221
v_rcp_f32 v242, v218
v_rcp_f32 v243, v219
ar v192,a160
ar v193,a161
ar v194,a162
ar v195,a163
ar v196,a164
ar v197,a165
ar v198,a166
ar v199,a167
ar v200,a168
ar v201,a169
ar v202,a170
ar v203,a171
ar v204,a172
ar v205,a173
ar v206,a174
ar v207,a175
ar v208,a176
ar v209,a177
ar v210,a178
ar v211,a179
ar v212,a180
ar v213,a181
ar v214,a182
ar v215,a183
ar v216,a184
ar v217,a185
ar v218,a186
ar v219,a187
ar v220,a188
ar v221,a189
ar v222,a190
ar v223,a191
s_mov_b32 s95, 4096
s_mov_b32 s96, 8192
s_mov_b32 s97, 12288
s_mov_b64 vcc, 0
s_nop 1
s_setpc_b64 s[24:25]
; ---- fallback: the later 32 rows
.La5_LA_SLOW1:
wt lgkmcnt(0)
s_nop 7
s_nop 7
aw a160,v192
aw a161,v193
aw a162,v194
aw a163,v195
aw a164,v196
aw a165,v197
aw a166,v198
aw a167,v199
aw a168,v200
aw a169,v201
aw a170,v202
aw a171,v203
aw a172,v204
aw a173,v205
aw a174,v206
aw a175,v207
aw a176,v208
aw a177,v209
aw a178,v210
aw a179,v211
aw a180,v212
aw a181,v213
aw a182,v214
aw a183,v215
aw a184,v216
aw a185,v217
aw a186,v218
aw a187,v219
aw a188,v220
aw a189,v221
aw a190,v222
aw a191,v223
s_lshl_b32 s96, s72, 19
s_add_u32 s96, s96, 20971520
s_add_u32 s92, s26, s96
s_addc_u32 s93, s27, 0
s_and_b32 s93, s93, 0xffff
s_mov_b32 s94, 17301504
s_mov_b32 s95, 0x20000
v_mov_b32 v64, 0
v_mov_b32 v65, 0
v_mov_b32 v66, 0
v_mov_b32 v67, 0
v_mov_b32 v68, 0
v_mov_b32 v69, 0
v_mov_b32 v70, 0
v_mov_b32 v71, 0
v_mov_b32 v72, 0
v_mov_b32 v73, 0
v_mov_b32 v74, 0
v_mov_b32 v75, 0
v_mov_b32 v76, 0
v_mov_b32 v77, 0
v_mov_b32 v78, 0
v_mov_b32 v79, 0
v_mov_b32 v80, 0
v_mov_b32 v81, 0
v_mov_b32 v82, 0
v_mov_b32 v83, 0
v_mov_b32 v84, 0
v_mov_b32 v85, 0
v_mov_b32 v86, 0
v_mov_b32 v87, 0
v_mov_b32 v88, 0
v_mov_b32 v89, 0
v_mov_b32 v90, 0
v_mov_b32 v91, 0
v_mov_b32 v92, 0
v_mov_b32 v93, 0
v_mov_b32 v94, 0
v_mov_b32 v95, 0
v_mov_b32 v96, 0
v_mov_b32 v97, 0
v_mov_b32 v98, 0
v_mov_b32 v99, 0
v_mov_b32 v100, 0
v_mov_b32 v101, 0
v_mov_b32 v102, 0
v_mov_b32 v103, 0
v_mov_b32 v104, 0
v_mov_b32 v105, 0
v_mov_b32 v106, 0
v_mov_b32 v107, 0
v_mov_b32 v108, 0
v_mov_b32 v109, 0
v_mov_b32 v110, 0
v_mov_b32 v111, 0
v_mov_b32 v112, 0
v_mov_b32 v113, 0
v_mov_b32 v114, 0
v_mov_b32 v115, 0
v_mov_b32 v116, 0
v_mov_b32 v117, 0
v_mov_b32 v118, 0
v_mov_b32 v119, 0
v_mov_b32 v120, 0
v_mov_b32 v121, 0
v_mov_b32 v122, 0
v_mov_b32 v123, 0
v_mov_b32 v124, 0
v_mov_b32 v125, 0
v_mov_b32 v126, 0
v_mov_b32 v127, 0
v_mov_b32 v216, 0xff61b1e6
v_mov_b32 v217, 0xff61b1e6
v_mov_b32 v218, 0
v_mov_b32 v219, 0
s_mov_b32 s7, 0
.La5_LA_SLOW1_L:
s_lshl_b32 s96, s7, 13
s_add_u32 s97, s96, 4096
ld a[128:131],v255,s[92:95],s96 offen offset:0 sc1
ld a[132:135],v255,s[92:95],s96 offen offset:1024 sc1
ld a[136:139],v255,s[92:95],s96 offen offset:2048 sc1
ld a[140:143],v255,s[92:95],s96 offen offset:3072 sc1
ld a[144:147],v255,s[92:95],s97 offen offset:0 sc1
ld a[148:151],v255,s[92:95],s97 offen offset:1024 sc1
ld a[152:155],v255,s[92:95],s97 offen offset:2048 sc1
ld a[156:159],v255,s[92:95],s97 offen offset:3072 sc1
s_lshl_b32 s96, s76, 3
s_sub_u32 s96, s96, s7
s_sub_u32 s96, s96, s3
s_add_u32 s96, s96, 7
s_lshl_b32 s96, s96, 5
v_bfe_u32 v220, v255, 4, 4
v_lshrrev_b32 v221, 8, v255
v_lshlrev_b32 v221, 2, v221
v_sub_u32 v220, v220, v221
v_add_u32 v220, s96, v220
v_mov_b32 v222, 0xceabfb8f
wt vmcnt(0)
mm v[192:195],a[128:131],v[160:163],0
mm v[192:195],a[132:135],v[164:167],v[192:195]
mm v[192:195],a[136:139],v[168:171],v[192:195]
mm v[192:195],a[140:143],v[172:175],v[192:195]
mm v[196:199],a[144:147],v[160:163],0
mm v[196:199],a[148:151],v[164:167],v[196:199]
mm v[196:199],a[152:155],v[168:171],v[196:199]
mm v[196:199],a[156:159],v[172:175],v[196:199]
mm v[200:203],a[128:131],v[176:179],0
mm v[200:203],a[132:135],v[180:183],v[200:203]
mm v[200:203],a[136:139],v[184:187],v[200:203]
mm v[200:203],a[140:143],v[188:191],v[200:203]
mm v[204:207],a[144:147],v[176:179],0
mm v[204:207],a[148:151],v[180:183],v[204:207]
mm v[204:207],a[152:155],v[184:187],v[204:207]
mm v[204:207],a[156:159],v[188:191],v[204:207]
s_nop 7
s_nop 7
s_lshl_b32 s96, s7, 13
s_add_u32 s96, s96, 16777216
s_add_u32 s97, s96, 4096
ld a[128:131],v255,s[92:95],s96 offen offset:0 sc1
ld a[132:135],v255,s[92:95],s96 offen offset:1024 sc1
ld a[136:139],v255,s[92:95],s96 offen offset:2048 sc1
ld a[140:143],v255,s[92:95],s96 offen offset:3072 sc1
ld a[144:147],v255,s[92:95],s97 offen offset:0 sc1
ld a[148:151],v255,s[92:95],s97 offen offset:1024 sc1
ld a[152:155],v255,s[92:95],s97 offen offset:2048 sc1
ld a[156:159],v255,s[92:95],s97 offen offset:3072 sc1
v_cmp_gt_i32 vcc, 0, v220
s_nop 1
v_cndmask_b32 v192, v192, v222, vcc
v_cmp_gt_i32 vcc, 1, v220
s_nop 1
v_cndmask_b32 v193, v193, v222, vcc
v_cmp_gt_i32 vcc, 2, v220
s_nop 1
v_cndmask_b32 v194, v194, v222, vcc
v_cmp_gt_i32 vcc, 3, v220
s_nop 1
v_cndmask_b32 v195, v195, v222, vcc
v_cmp_gt_i32 vcc, 16, v220
s_nop 1
v_cndmask_b32 v196, v196, v222, vcc
v_cmp_gt_i32 vcc, 17, v220
s_nop 1
v_cndmask_b32 v197, v197, v222, vcc
v_cmp_gt_i32 vcc, 18, v220
s_nop 1
v_cndmask_b32 v198, v198, v222, vcc
v_cmp_gt_i32 vcc, 19, v220
s_nop 1
v_cndmask_b32 v199, v199, v222, vcc
v_cmp_gt_i32 vcc, -16, v220
s_nop 1
v_cndmask_b32 v200, v200, v222, vcc
v_cmp_gt_i32 vcc, -15, v220
s_nop 1
v_cndmask_b32 v201, v201, v222, vcc
v_cmp_gt_i32 vcc, -14, v220
s_nop 1
v_cndmask_b32 v202, v202, v222, vcc
v_cmp_gt_i32 vcc, -13, v220
s_nop 1
v_cndmask_b32 v203, v203, v222, vcc
v_cmp_gt_i32 vcc, 0, v220
s_nop 1
v_cndmask_b32 v204, v204, v222, vcc
v_cmp_gt_i32 vcc, 1, v220
s_nop 1
v_cndmask_b32 v205, v205, v222, vcc
v_cmp_gt_i32 vcc, 2, v220
s_nop 1
v_cndmask_b32 v206, v206, v222, vcc
v_cmp_gt_i32 vcc, 3, v220
s_nop 1
v_cndmask_b32 v207, v207, v222, vcc
v_max3_f32 v222, v192, v193, v216
v_max3_f32 v222, v194, v195, v222
v_max3_f32 v222, v196, v197, v222
v_max3_f32 v222, v198, v199, v222
v_mov_b32 v221, v222
s_nop 1
v_permlane16_swap_b32 v221, v222
s_nop 1
v_max_f32 v222, v222, v221
v_mov_b32 v221, v222
s_nop 1
v_permlane32_swap_b32 v221, v222
s_nop 1
v_max_f32 v222, v222, v221
v_sub_f32 v223, v216, v222
v_mov_b32 v216, v222
ex v223,v223
v_sub_f32 v192, v192, v222
v_sub_f32 v193, v193, v222
v_sub_f32 v194, v194, v222
v_sub_f32 v195, v195, v222
v_sub_f32 v196, v196, v222
v_sub_f32 v197, v197, v222
v_sub_f32 v198, v198, v222
v_sub_f32 v199, v199, v222
ex v192,v192
ex v193,v193
ex v194,v194
ex v195,v195
ex v196,v196
ex v197,v197
ex v198,v198
ex v199,v199
v_mul_f32 v218, v218, v223
v_add_f32 v218, v218, v192
v_add_f32 v218, v218, v193
v_add_f32 v218, v218, v194
v_add_f32 v218, v218, v195
v_add_f32 v218, v218, v196
v_add_f32 v218, v218, v197
v_add_f32 v218, v218, v198
v_add_f32 v218, v218, v199
cv v208,v192,v193
cv v209,v194,v195
cv v210,v196,v197
cv v211,v198,v199
v_mul_f32 v64, v64, v223
v_mul_f32 v65, v65, v223
v_mul_f32 v66, v66, v223
v_mul_f32 v67, v67, v223
v_mul_f32 v68, v68, v223
v_mul_f32 v69, v69, v223
v_mul_f32 v70, v70, v223
v_mul_f32 v71, v71, v223
v_mul_f32 v72, v72, v223
v_mul_f32 v73, v73, v223
v_mul_f32 v74, v74, v223
v_mul_f32 v75, v75, v223
v_mul_f32 v76, v76, v223
v_mul_f32 v77, v77, v223
v_mul_f32 v78, v78, v223
v_mul_f32 v79, v79, v223
v_mul_f32 v80, v80, v223
v_mul_f32 v81, v81, v223
v_mul_f32 v82, v82, v223
v_mul_f32 v83, v83, v223
v_mul_f32 v84, v84, v223
v_mul_f32 v85, v85, v223
v_mul_f32 v86, v86, v223
v_mul_f32 v87, v87, v223
v_mul_f32 v88, v88, v223
v_mul_f32 v89, v89, v223
v_mul_f32 v90, v90, v223
v_mul_f32 v91, v91, v223
v_mul_f32 v92, v92, v223
v_mul_f32 v93, v93, v223
v_mul_f32 v94, v94, v223
v_mul_f32 v95, v95, v223
v_max3_f32 v222, v200, v201, v217
v_max3_f32 v222, v202, v203, v222
v_max3_f32 v222, v204, v205, v222
v_max3_f32 v222, v206, v207, v222
v_mov_b32 v221, v222
s_nop 1
v_permlane16_swap_b32 v221, v222
s_nop 1
v_max_f32 v222, v222, v221
v_mov_b32 v221, v222
s_nop 1
v_permlane32_swap_b32 v221, v222
s_nop 1
v_max_f32 v222, v222, v221
v_sub_f32 v223, v217, v222
v_mov_b32 v217, v222
ex v223,v223
v_sub_f32 v200, v200, v222
v_sub_f32 v201, v201, v222
v_sub_f32 v202, v202, v222
v_sub_f32 v203, v203, v222
v_sub_f32 v204, v204, v222
v_sub_f32 v205, v205, v222
v_sub_f32 v206, v206, v222
v_sub_f32 v207, v207, v222
ex v200,v200
ex v201,v201
ex v202,v202
ex v203,v203
ex v204,v204
ex v205,v205
ex v206,v206
ex v207,v207
v_mul_f32 v219, v219, v223
v_add_f32 v219, v219, v200
v_add_f32 v219, v219, v201
v_add_f32 v219, v219, v202
v_add_f32 v219, v219, v203
v_add_f32 v219, v219, v204
v_add_f32 v219, v219, v205
v_add_f32 v219, v219, v206
v_add_f32 v219, v219, v207
cv v212,v200,v201
cv v213,v202,v203
cv v214,v204,v205
cv v215,v206,v207
v_mul_f32 v96, v96, v223
v_mul_f32 v97, v97, v223
v_mul_f32 v98, v98, v223
v_mul_f32 v99, v99, v223
v_mul_f32 v100, v100, v223
v_mul_f32 v101, v101, v223
v_mul_f32 v102, v102, v223
v_mul_f32 v103, v103, v223
v_mul_f32 v104, v104, v223
v_mul_f32 v105, v105, v223
v_mul_f32 v106, v106, v223
v_mul_f32 v107, v107, v223
v_mul_f32 v108, v108, v223
v_mul_f32 v109, v109, v223
v_mul_f32 v110, v110, v223
v_mul_f32 v111, v111, v223
v_mul_f32 v112, v112, v223
v_mul_f32 v113, v113, v223
v_mul_f32 v114, v114, v223
v_mul_f32 v115, v115, v223
v_mul_f32 v116, v116, v223
v_mul_f32 v117, v117, v223
v_mul_f32 v118, v118, v223
v_mul_f32 v119, v119, v223
v_mul_f32 v120, v120, v223
v_mul_f32 v121, v121, v223
v_mul_f32 v122, v122, v223
v_mul_f32 v123, v123, v223
v_mul_f32 v124, v124, v223
v_mul_f32 v125, v125, v223
v_mul_f32 v126, v126, v223
v_mul_f32_e64 v127, v127, v223
wt vmcnt(0)
mm v[64:67],a[128:131],v[208:211],v[64:67]
mm v[96:99],a[128:131],v[212:215],v[96:99]
mm v[68:71],a[132:135],v[208:211],v[68:71]
mm v[100:103],a[132:135],v[212:215],v[100:103]
mm v[72:75],a[136:139],v[208:211],v[72:75]
mm v[104:107],a[136:139],v[212:215],v[104:107]
mm v[76:79],a[140:143],v[208:211],v[76:79]
mm v[108:111],a[140:143],v[212:215],v[108:111]
mm v[80:83],a[144:147],v[208:211],v[80:83]
mm v[112:115],a[144:147],v[212:215],v[112:115]
mm v[84:87],a[148:151],v[208:211],v[84:87]
mm v[116:119],a[148:151],v[212:215],v[116:119]
mm v[88:91],a[152:155],v[208:211],v[88:91]
mm v[120:123],a[152:155],v[212:215],v[120:123]
mm v[92:95],a[156:159],v[208:211],v[92:95]
mm v[124:127],a[156:159],v[212:215],v[124:127]
s_nop 7
s_nop 7
s_add_u32 s7, s7, 1
s_lshr_b32 s96, s7, 3
s_cmp_le_u32 s96, s76
s_cbranch_scc1 .La5_LA_SLOW1_L
v_mov_b32 v221, v218
s_nop 1
v_permlane16_swap_b32 v221, v218
s_nop 1
v_add_f32 v218, v218, v221
v_mov_b32 v221, v218
s_nop 1
v_permlane32_swap_b32 v221, v218
s_nop 1
v_add_f32 v218, v218, v221
v_mov_b32 v221, v219
s_nop 1
v_permlane16_swap_b32 v221, v219
s_nop 1
v_add_f32 v219, v219, v221
v_mov_b32 v221, v219
s_nop 1
v_permlane32_swap_b32 v221, v219
s_nop 1
v_add_f32 v219, v219, v221
v_rcp_f32 v242, v218
v_rcp_f32 v243, v219
ar v192,a160
ar v193,a161
ar v194,a162
ar v195,a163
ar v196,a164
ar v197,a165
ar v198,a166
ar v199,a167
ar v200,a168
ar v201,a169
ar v202,a170
ar v203,a171
ar v204,a172
ar v205,a173
ar v206,a174
ar v207,a175
ar v208,a176
ar v209,a177
ar v210,a178
ar v211,a179
ar v212,a180
ar v213,a181
ar v214,a182
ar v215,a183
ar v216,a184
ar v217,a185
ar v218,a186
ar v219,a187
ar v220,a188
ar v221,a189
ar v222,a190
ar v223,a191
s_mov_b32 s95, 4096
s_mov_b32 s96, 8192
s_mov_b32 s97, 12288
s_mov_b64 vcc, 0
s_nop 1
s_setpc_b64 s[24:25]
.La5_LA_SC1:
s_call_b64 s[24:25], .La5_LA_SLOW0
s_branch .La5_LA_SR1
.La5_LA_SC2:
s_call_b64 s[24:25], .La5_LA_SLOW1
s_branch .La5_LA_SR2
.La5_LA_PS0:
s_mov_b32 s84, 3
s_mov_b32 s70, 17408
wt vmcnt(2)
ld a[220:223],v255,s[68:71],s94 offen sc1
ld a[0:3],v255,s[68:71],0 offen offset:0 sc1
ld a[4:7],v255,s[68:71],0 offen offset:1024 sc1
ld a[8:11],v255,s[68:71],0 offen offset:2048 sc1
ld a[12:15],v255,s[68:71],0 offen offset:3072 sc1
ld a[16:19],v255,s[68:71],s95 offen offset:0 sc1
ld a[20:23],v255,s[68:71],s95 offen offset:1024 sc1
ld a[24:27],v255,s[68:71],s95 offen offset:2048 sc1
s_branch .La5_LA_PS1
.La5_LA_PS2:
s_mov_b32 s84, 3
s_mov_b32 s70, 17408
wt vmcnt(0)
ld a[220:223],v255,s[68:71],s94 offen sc1
ld a[0:3],v255,s[68:71],0 offen offset:0 sc1
ld a[4:7],v255,s[68:71],0 offen offset:1024 sc1
ld a[8:11],v255,s[68:71],0 offen offset:2048 sc1
ld a[12:15],v255,s[68:71],0 offen offset:3072 sc1
ld a[16:19],v255,s[68:71],s95 offen offset:0 sc1
ld a[20:23],v255,s[68:71],s95 offen offset:1024 sc1
ld a[24:27],v255,s[68:71],s95 offen offset:2048 sc1
ld a[28:31],v255,s[68:71],s95 offen offset:3072 sc1
ld a[32:35],v255,s[68:71],s96 offen offset:0 sc1
ld a[36:39],v255,s[68:71],s96 offen offset:1024 sc1
ld a[40:43],v255,s[68:71],s96 offen offset:2048 sc1
ld a[44:47],v255,s[68:71],s96 offen offset:3072 sc1
ld a[48:51],v255,s[68:71],s97 offen offset:0 sc1
ld a[52:55],v255,s[68:71],s97 offen offset:1024 sc1
ld a[56:59],v255,s[68:71],s97 offen offset:2048 sc1
ld a[60:63],v255,s[68:71],s97 offen offset:3072 sc1
s_branch .La5_LA_PS3
.La5_LA_PSPIN:
s_mov_b32 s93, 0
.La5_LA_PSPIN_L:
s_add_u32 s93, s93, 1
s_cmp_gt_u32 s93, 2097152
s_cbranch_scc1 .La5_LA_PSOK
s_sleep 8
buffer_load_dword v250, v251, s[64:67], s20 offen sc1
wt vmcnt(0)
v_cmp_eq_u32 vcc, v250, v249
s_nop 1
s_and_b32 s92, vcc_lo, 3
s_cmp_eq_u32 s92, 3
s_cbranch_scc0 .La5_LA_PSPIN_L
s_branch .La5_LA_PSOK
.La5_LA_PSOK:
s_mov_b32 s70, 17408
ld a[220:223],v255,s[68:71],s94 offen sc1
ld a[0:3],v255,s[68:71],0 offen offset:0 sc1
ld a[4:7],v255,s[68:71],0 offen offset:1024 sc1
ld a[8:11],v255,s[68:71],0 offen offset:2048 sc1
ld a[12:15],v255,s[68:71],0 offen offset:3072 sc1
ld a[16:19],v255,s[68:71],s95 offen offset:0 sc1
ld a[20:23],v255,s[68:71],s95 offen offset:1024 sc1
ld a[24:27],v255,s[68:71],s95 offen offset:2048 sc1
ld a[28:31],v255,s[68:71],s95 offen offset:3072 sc1
ld a[32:35],v255,s[68:71],s96 offen offset:0 sc1
ld a[36:39],v255,s[68:71],s96 offen offset:1024 sc1
ld a[40:43],v255,s[68:71],s96 offen offset:2048 sc1
ld a[44:47],v255,s[68:71],s96 offen offset:3072 sc1
ld a[48:51],v255,s[68:71],s97 offen offset:0 sc1
ld a[52:55],v255,s[68:71],s97 offen offset:1024 sc1
ld a[56:59],v255,s[68:71],s97 offen offset:2048 sc1
ld a[60:63],v255,s[68:71],s97 offen offset:3072 sc1
s_branch .La5_LA_DC1
.La5_LO_NB1:
.La5_LW3_6:
s_cbranch_execz .La5_LA_RETURN
s_mov_b32 s93, 0
.La5_LO_NB1_L:
s_add_u32 s93, s93, 1
s_cmp_gt_u32 s93, 2097152
s_cbranch_scc1 .La5_LO_KB1
s_sleep 8
buffer_load_dwordx2 v[250:251], v248, s[64:67], s5 offen sc1
wt vmcnt(0)
v_cmp_eq_u64 vcc, v[250:251], v[252:253]
s_nop 1
s_bitcmp1_b32 vcc_lo, 1
s_cbranch_scc0 .La5_LO_NB1_L
s_branch .La5_LO_KB1
.La5_LO_NB2:
s_mov_b32 s93, 0
.La5_LO_NB2_L:
s_add_u32 s93, s93, 1
s_cmp_gt_u32 s93, 2097152
s_nop 0
s_cbranch_scc1 .La5_LO_KB2
s_sleep 8
buffer_load_dwordx2 v[250:251], v248, s[64:67], s5 offen sc1
wt vmcnt(0)
v_cmp_eq_u64 vcc, v[250:251], v[252:253]
s_nop 1
s_bitcmp1_b32 vcc_lo, 2
s_cbranch_scc0 .La5_LO_NB2_L
s_branch .La5_LO_KB2
.La5_LO_NB3:
s_mov_b32 s93, 0
.La5_LO_NB3_L:
s_add_u32 s93, s93, 1
s_cmp_gt_u32 s93, 2097152
s_nop 0
s_nop 0
s_cbranch_scc1 .La5_LO_KB3
s_sleep 8
buffer_load_dwordx2 v[250:251], v248, s[64:67], s5 offen sc1
wt vmcnt(0)
v_cmp_eq_u64 vcc, v[250:251], v[252:253]
s_nop 1
s_bitcmp1_b32 vcc_lo, 3
s_cbranch_scc0 .La5_LO_NB3_L
s_branch .La5_LO_KB3
.La5_LO_NB4:
s_mov_b32 s93, 0
.La5_LO_NB4_L:
s_add_u32 s93, s93, 1
s_cmp_gt_u32 s93, 2097152
s_nop 0
s_nop 0
s_cbranch_scc1 .La5_LO_KB4
s_sleep 8
buffer_load_dwordx2 v[250:251], v248, s[64:67], s5 offen sc1
wt vmcnt(0)
v_cmp_eq_u64 vcc, v[250:251], v[252:253]
s_nop 1
s_bitcmp1_b32 vcc_lo, 4
s_cbranch_scc0 .La5_LO_NB4_L
s_branch .La5_LO_KB4
.La5_LO_NB5:
s_mov_b32 s93, 0
.La5_LO_NB5_L:
s_add_u32 s93, s93, 1
s_cmp_gt_u32 s93, 2097152
s_nop 0
s_nop 0
s_cbranch_scc1 .La5_LO_KB5
s_sleep 8
buffer_load_dwordx2 v[250:251], v248, s[64:67], s5 offen sc1
wt vmcnt(0)
v_cmp_eq_u64 vcc, v[250:251], v[252:253]
s_nop 1
s_bitcmp1_b32 vcc_lo, 5
s_cbranch_scc0 .La5_LO_NB5_L
s_branch .La5_LO_KB5
.La5_LO_NB8:
s_mov_b32 s93, 0
.La5_LO_NB8_L:
s_add_u32 s93, s93, 1
s_cmp_gt_u32 s93, 2097152
s_nop 0
s_nop 0
s_cbranch_scc1 .La5_LO_KB8
s_sleep 8
buffer_load_dwordx2 v[250:251], v248, s[64:67], s5 offen sc1
wt vmcnt(0)
v_cmp_eq_u64 vcc, v[250:251], v[252:253]
s_nop 1
s_bitcmp1_b32 vcc_lo, 8
s_cbranch_scc0 .La5_LO_NB8_L
s_branch .La5_LO_KB8
.La5_LO_NB6:
s_mov_b32 s93, 0
.La5_LO_NB6_L:
s_add_u32 s93, s93, 1
s_cmp_gt_u32 s93, 2097152
s_nop 0
s_nop 0
s_cbranch_scc1 .La5_LO_KB6
s_sleep 8
buffer_load_dwordx2 v[250:251], v248, s[64:67], s5 offen sc1
wt vmcnt(0)
v_cmp_eq_u64 vcc, v[250:251], v[252:253]
s_nop 1
s_bitcmp1_b32 vcc_lo, 6
s_cbranch_scc0 .La5_LO_NB6_L
s_branch .La5_LO_KB6
.La5_LO_NB7:
s_mov_b32 s93, 0
.La5_LO_NB7_L:
s_add_u32 s93, s93, 1
s_cmp_gt_u32 s93, 2097152
s_nop 0
s_nop 0
s_cbranch_scc1 .La5_LO_KB7
s_sleep 8
buffer_load_dwordx2 v[250:251], v248, s[64:67], s5 offen sc1
wt vmcnt(0)
v_cmp_eq_u64 vcc, v[250:251], v[252:253]
s_nop 1
s_bitcmp1_b32 vcc_lo, 7
s_cbranch_scc0 .La5_LO_NB7_L
s_branch .La5_LO_KB7
.La5_LO_NC1:
.La5_LW7_7:
s_cbranch_execz .La5_LA_RETURN
s_mov_b32 s93, 0
.La5_LO_NC1_L:
s_add_u32 s93, s93, 1
s_cmp_gt_u32 s93, 2097152
s_nop 0
s_cbranch_scc1 .La5_LO_KC1
s_sleep 8
buffer_load_dwordx2 v[250:251], v248, s[64:67], s5 offen sc1
wt vmcnt(0)
v_cmp_eq_u64 vcc, v[250:251], v[252:253]
s_nop 1
s_bitcmp1_b32 vcc_lo, 1
s_cbranch_scc0 .La5_LO_NC1_L
s_branch .La5_LO_KC1
.La5_LO_NC2:
s_mov_b32 s93, 0
.La5_LO_NC2_L:
s_add_u32 s93, s93, 1
s_cmp_gt_u32 s93, 2097152
s_nop 0
s_nop 0
s_cbranch_scc1 .La5_LO_KC2
s_sleep 8
buffer_load_dwordx2 v[250:251], v248, s[64:67], s5 offen sc1
wt vmcnt(0)
v_cmp_eq_u64 vcc, v[250:251], v[252:253]
s_nop 1
s_bitcmp1_b32 vcc_lo, 2
s_cbranch_scc0 .La5_LO_NC2_L
s_branch .La5_LO_KC2
.La5_LO_NC3:
s_mov_b32 s93, 0
.La5_LO_NC3_L:
s_add_u32 s93, s93, 1
s_cmp_gt_u32 s93, 2097152
s_nop 0
s_nop 0
s_cbranch_scc1 .La5_LO_KC3
s_sleep 8
buffer_load_dwordx2 v[250:251], v248, s[64:67], s5 offen sc1
wt vmcnt(0)
v_cmp_eq_u64 vcc, v[250:251], v[252:253]
s_nop 1
s_bitcmp1_b32 vcc_lo, 3
s_cbranch_scc0 .La5_LO_NC3_L
s_branch .La5_LO_KC3
.La5_LO_NC4:
s_mov_b32 s93, 0
.La5_LO_NC4_L:
s_add_u32 s93, s93, 1
s_cmp_gt_u32 s93, 2097152
s_nop 0
s_nop 0
s_cbranch_scc1 .La5_LO_KC4
s_sleep 8
buffer_load_dwordx2 v[250:251], v248, s[64:67], s5 offen sc1
wt vmcnt(0)
v_cmp_eq_u64 vcc, v[250:251], v[252:253]
s_nop 1
s_bitcmp1_b32 vcc_lo, 4
s_cbranch_scc0 .La5_LO_NC4_L
s_branch .La5_LO_KC4
.La5_LO_NC5:
s_mov_b32 s93, 0
.La5_LO_NC5_L:
s_add_u32 s93, s93, 1
s_cmp_gt_u32 s93, 2097152
s_nop 0
s_nop 0
s_cbranch_scc1 .La5_LO_KC5
s_sleep 8
buffer_load_dwordx2 v[250:251], v248, s[64:67], s5 offen sc1
wt vmcnt(0)
v_cmp_eq_u64 vcc, v[250:251], v[252:253]
s_nop 1
s_bitcmp1_b32 vcc_lo, 5
s_cbranch_scc0 .La5_LO_NC5_L
s_branch .La5_LO_KC5
.La5_LO_NC6:
s_mov_b32 s93, 0
.La5_LO_NC6_L:
s_add_u32 s93, s93, 1
s_cmp_gt_u32 s93, 2097152
s_nop 0
s_nop 0
s_cbranch_scc1 .La5_LO_KC6
s_sleep 8
buffer_load_dwordx2 v[250:251], v248, s[64:67], s5 offen sc1
wt vmcnt(0)
v_cmp_eq_u64 vcc, v[250:251], v[252:253]
s_nop 1
s_bitcmp1_b32 vcc_lo, 6
s_cbranch_scc0 .La5_LO_NC6_L
s_branch .La5_LO_KC6
.La5_LO_NC7:
s_mov_b32 s93, 0
.La5_LO_NC7_L:
s_add_u32 s93, s93, 1
s_cmp_gt_u32 s93, 2097152
s_nop 0
s_nop 0
s_cbranch_scc1 .La5_LO_KC7
s_sleep 8
buffer_load_dwordx2 v[250:251], v248, s[64:67], s5 offen sc1
wt vmcnt(0)
v_cmp_eq_u64 vcc, v[250:251], v[252:253]
s_nop 1
s_bitcmp1_b32 vcc_lo, 7
s_cbranch_scc0 .La5_LO_NC7_L
s_branch .La5_LO_KC7
.La5_LA_SC3:
s_call_b64 s[24:25], .La5_LA_SLOW0
aw a0,0
aw a1,0
aw a2,0
aw a3,0
aw a4,0
aw a5,0
aw a6,0
aw a7,0
aw a8,0
aw a9,0
aw a10,0
aw a11,0
aw a12,0
aw a13,0
aw a14,0
aw a15,0
aw a16,0
aw a17,0
aw a18,0
aw a19,0
aw a20,0
aw a21,0
aw a22,0
aw a23,0
aw a24,0
aw a25,0
aw a26,0
aw a27,0
aw a28,0
aw a29,0
aw a30,0
aw a31,0
s_nop 1
v_mov_b32 v250, v242
v_mov_b32 v249, v243
v_xor_b32 v242, 64, v240
v_xor_b32 v243, 96, v240
ar v254,a229
s_mov_b32 s93, 0
ld a[128:131],v254,s[40:43],s93 offen
s_mov_b32 s93, 7168
ld a[132:135],v254,s[40:43],s93 offen
s_mov_b32 s93, 14336
ld a[136:139],v254,s[40:43],s93 offen
s_mov_b32 s93, 21504
ld a[140:143],v254,s[40:43],s93 offen
s_mov_b32 s93, 28672
ld a[144:147],v254,s[40:43],s93 offen
s_mov_b32 s93, 35840
ld a[148:151],v254,s[40:43],s93 offen
s_mov_b32 s93, 43008
ld a[152:155],v254,s[40:43],s93 offen
s_mov_b32 s93, 50176
ld a[156:159],v254,s[40:43],s93 offen
s_mov_b32 s93, 57344
ld a[160:163],v254,s[40:43],s93 offen
s_mov_b32 s93, 64512
ld a[164:167],v254,s[40:43],s93 offen
s_mov_b32 s93, 71680
ld a[168:171],v254,s[40:43],s93 offen
s_mov_b32 s93, 78848
ld a[172:175],v254,s[40:43],s93 offen
s_mov_b32 s93, 86016
ld a[176:179],v254,s[40:43],s93 offen
s_mov_b32 s93, 93184
ld a[180:183],v254,s[40:43],s93 offen
s_mov_b32 s93, 100352
ld a[184:187],v254,s[40:43],s93 offen
s_mov_b32 s93, 107520
ld a[188:191],v254,s[40:43],s93 offen
s_branch .La5_LA_SR3
.La5_LA_SC4:
s_call_b64 s[24:25], .La5_LA_SLOW1
aw a32,0
aw a33,0
aw a34,0
aw a35,0
aw a36,0
aw a37,0
aw a38,0
aw a39,0
aw a40,0
aw a41,0
aw a42,0
aw a43,0
aw a44,0
aw a45,0
aw a46,0
aw a47,0
aw a48,0
aw a49,0
aw a50,0
aw a51,0
aw a52,0
aw a53,0
aw a54,0
aw a55,0
aw a56,0
aw a57,0
aw a58,0
aw a59,0
aw a60,0
aw a61,0
aw a62,0
aw a63,0
s_nop 1
v_mov_b32 v250, v242
v_mov_b32 v249, v243
v_xor_b32 v242, 64, v240
v_xor_b32 v243, 96, v240
ar v254,a229
s_mov_b32 s93, 0
ld a[128:131],v254,s[40:43],s93 offen
s_mov_b32 s93, 7168
ld a[132:135],v254,s[40:43],s93 offen
s_mov_b32 s93, 14336
ld a[136:139],v254,s[40:43],s93 offen
s_mov_b32 s93, 21504
ld a[140:143],v254,s[40:43],s93 offen
s_mov_b32 s93, 28672
ld a[144:147],v254,s[40:43],s93 offen
s_mov_b32 s93, 35840
ld a[148:151],v254,s[40:43],s93 offen
s_mov_b32 s93, 43008
ld a[152:155],v254,s[40:43],s93 offen
s_mov_b32 s93, 50176
ld a[156:159],v254,s[40:43],s93 offen
s_mov_b32 s93, 57344
ld a[160:163],v254,s[40:43],s93 offen
s_mov_b32 s93, 64512
ld a[164:167],v254,s[40:43],s93 offen
s_mov_b32 s93, 71680
ld a[168:171],v254,s[40:43],s93 offen
s_mov_b32 s93, 78848
ld a[172:175],v254,s[40:43],s93 offen
s_mov_b32 s93, 86016
ld a[176:179],v254,s[40:43],s93 offen
s_mov_b32 s93, 93184
ld a[180:183],v254,s[40:43],s93 offen
s_mov_b32 s93, 100352
ld a[184:187],v254,s[40:43],s93 offen
s_mov_b32 s93, 107520
ld a[188:191],v254,s[40:43],s93 offen
s_branch .La5_LA_SR4
.La5_LO_VC1:
.La5_LW5_4:
s_cbranch_execz .La5_LW5_R0
.La5_LW6_8:
s_cbranch_execz .La5_LW6_R0
s_getpc_b64 s[24:25]
.La5_LW7_P0:
s_add_u32 s24, s24, .La5_LW7_R0-.La5_LW7_P0
s_addc_u32 s25, s25, 0
s_mov_b64 exec, 0
s_cmp_eq_u32 s3, 0
s_cbranch_scc1 .La5_LW7_0
s_cmp_eq_u32 s3, 1
s_cbranch_scc1 .La5_LW7_1
s_cmp_eq_u32 s3, 2
s_cbranch_scc1 .La5_LW7_2
s_branch .La5_LW7_3
.La5_LW7_R0:
s_mov_b64 exec, -1
s_branch .La5_LO_VE1
.La5_LO_W:
s_mov_b32 s93, 3670016
ld a[192:195],v228,s[40:43],s93 offen
s_mov_b32 s93, 3677184
ld a[196:199],v228,s[40:43],s93 offen
s_mov_b32 s93, 3684352
ld a[200:203],v228,s[40:43],s93 offen
s_mov_b32 s93, 3691520
ld a[204:207],v228,s[40:43],s93 offen
s_mov_b32 s93, 3698688
ld a[208:211],v228,s[40:43],s93 offen
s_mov_b32 s93, 3705856
ld a[212:215],v228,s[40:43],s93 offen
s_mov_b32 s93, 3713024
ld a[216:219],v228,s[40:43],s93 offen
s_mov_b32 s93, 3720192
ld a[220:223],v228,s[40:43],s93 offen
s_mov_b32 s93, 3727360
ld a[224:227],v228,s[40:43],s93 offen
s_mov_b32 s93, 3734528
ld a[228:231],v228,s[40:43],s93 offen
s_mov_b32 s93, 3741696
ld a[232:235],v228,s[40:43],s93 offen
s_mov_b32 s93, 3748864
ld a[236:239],v228,s[40:43],s93 offen
s_mov_b32 s93, 3756032
ld a[240:243],v228,s[40:43],s93 offen
s_mov_b32 s93, 3763200
ld a[244:247],v228,s[40:43],s93 offen
s_mov_b32 s93, 3770368
ld a[248:251],v228,s[40:43],s93 offen
s_mov_b32 s93, 3777536
ld a[252:255],v228,s[40:43],s93 offen
s_branch .La5_LO_START
.La5_LO_VC2:
s_getpc_b64 s[24:25]
.La5_LW7_P1:
s_add_u32 s24, s24, .La5_LW7_R1-.La5_LW7_P1
s_addc_u32 s25, s25, 0
s_mov_b64 exec, 0
s_cmp_eq_u32 s3, 0
s_cbranch_scc1 .La5_LW7_0
s_cmp_eq_u32 s3, 1
s_cbranch_scc1 .La5_LW7_1
s_cmp_eq_u32 s3, 2
s_cbranch_scc1 .La5_LW7_2
s_branch .La5_LW7_3
.La5_LW7_R1:
s_mov_b64 exec, -1
s_branch .La5_LO_VE2
.La5_LT_VAR:
v_mov_b32 v230, 0
v_lshlrev_b32 v232, 16, v96
v_and_b32 v233, 0xffff0000, v96
v_sub_f32 v232, v232, v244
v_sub_f32 v233, v233, v244
v_fmac_f32 v230, v232, v232
v_fmac_f32 v230, v233, v233
v_lshlrev_b32 v232, 16, v97
v_and_b32 v233, 0xffff0000, v97
v_sub_f32 v232, v232, v244
v_sub_f32 v233, v233, v244
v_fmac_f32 v230, v232, v232
v_fmac_f32 v230, v233, v233
v_lshlrev_b32 v232, 16, v98
v_and_b32 v233, 0xffff0000, v98
v_sub_f32 v232, v232, v244
v_sub_f32 v233, v233, v244
v_fmac_f32 v230, v232, v232
v_fmac_f32 v230, v233, v233
v_lshlrev_b32 v232, 16, v99
v_and_b32 v233, 0xffff0000, v99
v_sub_f32 v232, v232, v244
v_sub_f32 v233, v233, v244
v_fmac_f32 v230, v232, v232
v_fmac_f32 v230, v233, v233
v_lshlrev_b32 v232, 16, v100
v_and_b32 v233, 0xffff0000, v100
v_sub_f32 v232, v232, v244
v_sub_f32 v233, v233, v244
v_fmac_f32 v230, v232, v232
v_fmac_f32 v230, v233, v233
v_lshlrev_b32 v232, 16, v101
v_and_b32 v233, 0xffff0000, v101
v_sub_f32 v232, v232, v244
v_sub_f32 v233, v233, v244
v_fmac_f32 v230, v232, v232
v_fmac_f32 v230, v233, v233
v_lshlrev_b32 v232, 16, v102
v_and_b32 v233, 0xffff0000, v102
v_sub_f32 v232, v232, v244
v_sub_f32 v233, v233, v244
v_fmac_f32 v230, v232, v232
v_fmac_f32 v230, v233, v233
v_lshlrev_b32 v232, 16, v103
v_and_b32 v233, 0xffff0000, v103
v_sub_f32 v232, v232, v244
v_sub_f32 v233, v233, v244
v_fmac_f32 v230, v232, v232
v_fmac_f32 v230, v233, v233
v_lshlrev_b32 v232, 16, v104
v_and_b32 v233, 0xffff0000, v104
v_sub_f32 v232, v232, v244
v_sub_f32 v233, v233, v244
v_fmac_f32 v230, v232, v232
v_fmac_f32 v230, v233, v233
v_lshlrev_b32 v232, 16, v105
v_and_b32 v233, 0xffff0000, v105
v_sub_f32 v232, v232, v244
v_sub_f32 v233, v233, v244
v_fmac_f32 v230, v232, v232
v_fmac_f32 v230, v233, v233
v_lshlrev_b32 v232, 16, v106
v_and_b32 v233, 0xffff0000, v106
v_sub_f32 v232, v232, v244
v_sub_f32 v233, v233, v244
v_fmac_f32 v230, v232, v232
v_fmac_f32 v230, v233, v233
v_lshlrev_b32 v232, 16, v107
v_and_b32 v233, 0xffff0000, v107
v_sub_f32 v232, v232, v244
v_sub_f32 v233, v233, v244
v_fmac_f32 v230, v232, v232
v_fmac_f32 v230, v233, v233
v_lshlrev_b32 v232, 16, v108
v_and_b32 v233, 0xffff0000, v108
v_sub_f32 v232, v232, v244
v_sub_f32 v233, v233, v244
v_fmac_f32 v230, v232, v232
v_fmac_f32 v230, v233, v233
v_lshlrev_b32 v232, 16, v109
v_and_b32 v233, 0xffff0000, v109
v_sub_f32 v232, v232, v244
v_sub_f32 v233, v233, v244
v_fmac_f32 v230, v232, v232
v_fmac_f32 v230, v233, v233
v_lshlrev_b32 v232, 16, v110
v_and_b32 v233, 0xffff0000, v110
v_sub_f32 v232, v232, v244
v_sub_f32 v233, v233, v244
v_fmac_f32 v230, v232, v232
v_fmac_f32 v230, v233, v233
v_lshlrev_b32 v232, 16, v111
v_and_b32 v233, 0xffff0000, v111
v_sub_f32 v232, v232, v244
v_sub_f32 v233, v233, v244
v_fmac_f32 v230, v232, v232
v_fmac_f32 v230, v233, v233
s_nop 1
v_add_f32_dpp v230, v230, v230 quad_perm:[1,0,3,2] row_mask:0xf bank_mask:0xf
s_nop 1
v_add_f32_dpp v230, v230, v230 quad_perm:[2,3,0,1] row_mask:0xf bank_mask:0xf
s_nop 1
v_add_f32_dpp v230, v230, v230 row_ror:8 row_mask:0xf bank_mask:0xf
v_mov_b32 v231, v230
s_nop 1
v_permlane16_swap_b32 v231, v230
s_nop 1
v_add_f32 v230, v230, v231
v_mov_b32_e64 v231, v230
s_nop 1
v_permlane32_swap_b32 v231, v230
s_nop 1
v_add_f32_e64 v230, v230, v231
v_mul_f32 v230, 0x3a800000, v230
s_branch .La5_LT_VOK
.La5_LA_KSPIN:
s_mov_b32 s93, 0
.La5_LA_KSPIN_L:
s_add_u32 s93, s93, 1
s_cmp_gt_u32 s93, 2097152
s_cbranch_scc1 .La5_LA_KOK
s_sleep 8
buffer_load_dword v250, v251, s[64:67], s20 offen sc1
wt vmcnt(0)
v_cmp_eq_u32 vcc, v250, v249
s_nop 1
s_or_b32 vcc_hi, vcc_hi, s79
s_cmp_eq_u64 vcc, -1
s_cbranch_scc0 .La5_LA_KSPIN_L
s_branch .La5_LA_KOK
.La5_LA_QSPIN:
s_mov_b32 s93, 0
.La5_LA_QSPIN_L:
s_add_u32 s93, s93, 1
s_cmp_gt_u32 s93, 2097152
s_nop 0
s_cbranch_scc1 .La5_LA_QOK
s_sleep 8
buffer_load_dword v250, v251, s[64:67], s20 offen sc1
wt vmcnt(0)
v_cmp_eq_u32 vcc, v250, v249
s_nop 1
s_and_b32 s92, vcc_lo, 3
s_cmp_eq_u32 s92, 3
s_cbranch_scc0 .La5_LA_QSPIN_L
s_branch .La5_LA_QOK
.La5_LEND:
.La5_LCODEEND:
)ASM"
        :: "{s[0:1]}"(__builtin_amdgcn_kernarg_segment_ptr()), "{s2}"(blockIdx.x), "{s3}"(__builtin_amdgcn_readfirstlane(threadIdx.x >> 6)), "{s[4:5]}"(DBGP)
        : "v0","v1","v2","v3","v4","v5","v6","v7","v8","v9","v10","v11","v12","v13","v14","v15","v16","v17","v18","v19","v20","v21","v22","v23","v24","v25","v26","v27","v28","v29","v30","v31","v32","v33","v34","v35","v36","v37","v38","v39","v40","v41","v42","v43","v44","v45","v46","v47","v48","v49","v50","v51","v52","v53","v54","v55","v56","v57","v58","v59","v60","v61","v62","v63","v64","v65","v66","v67","v68","v69","v70","v71","v72","v73","v74","v75","v76","v77","v78","v79","v80","v81","v82","v83","v84","v85","v86","v87","v88","v89","v90","v91","v92","v93","v94","v95","v96","v97","v98","v99","v100","v101","v102","v103","v104","v105","v106","v107","v108","v109","v110","v111","v112","v113","v114","v115","v116","v117","v118","v119","v120","v121","v122","v123","v124","v125","v126","v127","v128","v129","v130","v131","v132","v133","v134","v135","v136","v137","v138","v139","v140","v141","v142","v143","v144","v145","v146","v147","v148","v149","v150","v151","v152","v153","v154","v155","v156","v157","v158","v159","v160","v161","v162","v163","v164","v165","v166","v167","v168","v169","v170","v171","v172","v173","v174","v175","v176","v177","v178","v179","v180","v181","v182","v183","v184","v185","v186","v187","v188","v189","v190","v191","v192","v193","v194","v195","v196","v197","v198","v199","v200","v201","v202","v203","v204","v205","v206","v207","v208","v209","v210","v211","v212","v213","v214","v215","v216","v217","v218","v219","v220","v221","v222","v223","v224","v225","v226","v227","v228","v229","v230","v231","v232","v233","v234","v235","v236","v237","v238","v239","v240","v241","v242","v243","v244","v245","v246","v247","v248","v249","v250","v251","v252","v253","v254","v255","a0","a1","a2","a3","a4","a5","a6","a7","a8","a9","a10","a11","a12","a13","a14","a15","a16","a17","a18","a19","a20","a21","a22","a23","a24","a25","a26","a27","a28","a29","a30","a31","a32","a33","a34","a35","a36","a37","a38","a39","a40","a41","a42","a43","a44","a45","a46","a47","a48","a49","a50","a51","a52","a53","a54","a55","a56","a57","a58","a59","a60","a61","a62","a63","a64","a65","a66","a67","a68","a69","a70","a71","a72","a73","a74","a75","a76","a77","a78","a79","a80","a81","a82","a83","a84","a85","a86","a87","a88","a89","a90","a91","a92","a93","a94","a95","a96","a97","a98","a99","a100","a101","a102","a103","a104","a105","a106","a107","a108","a109","a110","a111","a112","a113","a114","a115","a116","a117","a118","a119","a120","a121","a122","a123","a124","a125","a126","a127","a128","a129","a130","a131","a132","a133","a134","a135","a136","a137","a138","a139","a140","a141","a142","a143","a144","a145","a146","a147","a148","a149","a150","a151","a152","a153","a154","a155","a156","a157","a158","a159","a160","a161","a162","a163","a164","a165","a166","a167","a168","a169","a170","a171","a172","a173","a174","a175","a176","a177","a178","a179","a180","a181","a182","a183","a184","a185","a186","a187","a188","a189","a190","a191","a192","a193","a194","a195","a196","a197","a198","a199","a200","a201","a202","a203","a204","a205","a206","a207","a208","a209","a210","a211","a212","a213","a214","a215","a216","a217","a218","a219","a220","a221","a222","a223","a224","a225","a226","a227","a228","a229","a230","a231","a232","a233","a234","a235","a236","a237","a238","a239","a240","a241","a242","a243","a244","a245","a246","a247","a248","a249","a250","a251","a252","a253","a254","a255","s6","s7","s8","s9","s10","s11","s12","s13","s14","s15","s16","s17","s18","s19","s20","s21","s22","s23","s24","s25","s26","s27","s28","s29","s30","s31","s32","s33","s34","s35","s36","s37","s38","s39","s40","s41","s42","s43","s44","s45","s46","s47","s48","s49","s50","s51","s52","s53","s54","s55","s56","s57","s58","s59","s60","s61","s62","s63","s64","s65","s66","s67","s68","s69","s70","s71","s72","s73","s74","s75","s76","s77","s78","s79","s80","s81","s82","s83","s84","s85","s86","s87","s88","s89","s90","s91","s92","s93","s94","s95","s96","s97","s98","s99","s100","s101","vcc","scc","m0","memory");
}

extern "C" void kernel_launch(void* const* d_in, const int* in_sizes, int n_in, void* d_out, int out_size, void* d_ws, size_t ws_size,
                              hipStream_t stream) {
    (void)hipFuncSetAttribute((const void*)fa_kernel, hipFuncAttributeMaxDynamicSharedMemorySize, LDS_BYTES);
    fa_kernel<<<256, 256, LDS_BYTES, stream>>>((const bf16*)d_in[0], (const bf16*)d_in[1], (const bf16*)d_in[2], (const bf16*)d_in[3], (const bf16*)d_in[4], (char*)d_ws,
                                               (const bf16*)d_in[5], (const bf16*)d_in[8], (const bf16*)d_in[9], (const bf16*)d_in[10], (const bf16*)d_in[7], (bf16*)d_out);
}
```

```cpp
#include <hip/hip_runtime.h>
#include <hip/hip_bf16.h>
#include <stdint.h>
#pragma clang diagnostic ignored "-Winline-asm"
#pragma clang diagnostic ignored "-Woverlength-strings"

typedef __hip_bfloat16 bf16;
#define DBGP ((unsigned*)nullptr)
constexpr int LDS_BYTES = 163840;
extern "C" __global__ __attribute__((aligned(4096))) __launch_bounds__(256, 1) void fa_kernel(const bf16* __restrict__ px, const bf16* __restrict__ ckv, const bf16* __restrict__ wq,
                                                    const bf16* __restrict__ wuk, const bf16* __restrict__ wuv, char* pws,
                                                    const bf16* __restrict__ pwo, const bf16* __restrict__ pwg, const bf16* __restrict__ pwu,
                                                    const bf16* __restrict__ pwd, const bf16* __restrict__ pwr, bf16* __restrict__ pout) {
    asm volatile(R"ASM(
.macro mm x:vararg
v_mfma_f32_16x16x32_bf16 \x
.endm
.macro rt x:vararg
ds_read_b64_tr_b16 \x
.endm
.macro rd x:vararg
ds_read_b128 \x
.endm
.macro ld x:vararg
buffer_load_dwordx4 \x
.endm
.macro st x:vararg
buffer_store_dwordx4 \x
.endm
.macro ar x:vararg
v_accvgpr_read_b32 \x
.endm
.macro aw x:vararg
v_accvgpr_write_b32 \x
.endm
.macro cv x:vararg
v_cvt_pk_bf16_f32 \x
.endm
.macro ex x:vararg
v_exp_f32 \x
.endm
.macro wt x:vararg
s_waitcnt \x
.endm
; ---- roles: head = 4 xcd + (wg >> 6), j = (wg >> 3) & 7; odd j: block j >> 1 (then helps with block 7 - (j >> 1)), even j: block 7 - (j >> 1). First operands, addresses
.La5_LSTART:
s_load_dwordx16 s[16:31], s[0:1], 0
s_load_dwordx8 s[32:39], s[0:1], 64
.p2align 6
v_mbcnt_lo_u32_b32 v64, -1, 0
v_mbcnt_hi_u32_b32 v64, -1, v64
s_getpc_b64 s[92:93]
.La5_LPC:
s_sub_u32 s48, s92, .La5_LPC-.La5_LSTART
s_subb_u32 s49, s93, 0
s_and_b32 s49, s49, 0xffff
s_mov_b32 s50, .La5_LCODEEND-.La5_LSTART
s_mov_b32 s51, 0x20000
s_lshr_b32 s97, s2, 3
s_lshl2_add_u32 s87, s97, s3
v_lshlrev_b32 v133, 7, v64
s_lshl_b32 s87, s87, 13
buffer_load_dword v254, v133, s[48:51], s87 offen
s_and_b32 s92, s2, 7
s_lshr_b32 s93, s2, 6
s_lshl2_add_u32 s72, s92, s93
s_bfe_u32 s73, s2, 0x30003
s_lshr_b32 s74, s73, 1
s_xor_b32 s92, s74, 7
s_and_b32 s75, s73, 1
s_cselect_b32 s76, s74, s92
s_lshr_b32 s77, s3, 1
s_and_b32 s78, s3, 1
v_and_b32 v65, 15, v64
v_lshrrev_b32 v66, 4, v64
v_lshlrev_b32 v255, 4, v64
v_lshlrev_b32 v67, 1, v66
v_xor_b32 v67, v67, v65
v_lshlrev_b32 v67, 4, v67
v_lshl_add_u32 v67, v66, 13, v67
s_lshl_b32 s94, s78, 17
v_add_u32 v232, s94, v67
v_add_u32 v233, 32768, v232
v_add_u32 v234, 65536, v232
v_add_u32 v235, 98304, v232
v_and_b32 v67, 7, v64
v_xor_b32 v68, v67, v66
v_lshlrev_b32 v68, 4, v68
v_lshrrev_b32 v69, 3, v64
v_lshl_add_u32 v68, v69, 10, v68
s_lshl2_add_u32 s94, s3, s93
s_lshl_b32 s82, s94, 11
s_lshl_b32 s94, s94, 14
v_add_u32 v224, s94, v68
v_xor_b32 v225, 0x2040, v224
v_xor_b32 v226, 16384, v224
v_xor_b32 v227, 16384, v225
v_xor_b32 v228, 32768, v224
v_xor_b32 v229, 32768, v225
v_xor_b32 v230, 49152, v224
v_xor_b32 v231, 49152, v225
s_mul_i32 s94, s77, 8448
s_mul_i32 s95, s78, 4224
s_add_u32 s83, s94, s95
s_add_u32 s83, s83, 96256
v_lshlrev_b32 v135, 7, v67
v_lshl_add_u32 v135, v69, 13, v135
v_lshrrev_b32 v132, 5, v64
v_lshl_add_u32 v132, v132, 28, v135
v_lshl_add_u32 v133, v66, 28, v133
v_lshlrev_b32 v134, 10, v65
v_lshl_add_u32 v134, v66, 7, v134
v_lshrrev_b32 v68, 4, v64
v_lshl_add_u32 v134, v68, 28, v134
s_lshl1_add_u32 s97, s97, s78
s_lshl_b32 s7, s97, 13
s_lshl_b32 s97, s97, 15
s_lshl_b32 s96, s93, 8
s_sub_u32 s7, s7, s96
s_sub_u32 s87, s87, s96
s_sub_u32 s97, s97, s96
s_add_u32 s86, s97, 2097152
s_lshl2_add_u32 s101, s72, s3
s_lshl_b32 s101, s101, 11
s_lshl2_add_u32 s100, s93, s3
s_lshl_b32 s100, s100, 14
v_lshlrev_b32 v118, 11, v67
s_lshl_b32 s93, s3, 9
v_lshl_add_u32 v119, v69, 4, v118
v_add_u32 v119, s93, v119
s_lshl_b32 s97, s2, 14
s_lshl_b32 s94, s72, 8
s_lshl_b32 s95, s76, 18
s_sub_u32 s46, 4194304, s94
s_mov_b32 s47, 0x20000
s_mov_b32 s42, 262144
s_mov_b32 s43, 0x20000
s_mov_b32 s62, 16384
s_mov_b32 s63, 0x20000
s_mov_b32 s79, 512
s_mov_b32 s80, 2097152
s_add_u32 s100, s100, 512
s_mov_b32 s81, 262144
s_cmp_eq_u32 s77, 0
wt lgkmcnt(0)
s_cselect_b64 s[92:93], s[22:23], s[24:25]
s_add_u32 s44, s92, s94
s_addc_u32 s45, s93, 0
s_and_b32 s45, s45, 0xffff
s_add_u32 s40, s18, s95
s_addc_u32 s41, s19, 0
s_and_b32 s41, s41, 0xffff
s_add_u32 s60, s16, s97
s_addc_u32 s61, s17, 0
s_and_b32 s61, s61, 0xffff
buffer_load_dword v254, v132, s[44:47], s86 offen
s_add_u32 s86, s86, 2097152
s_and_b32 s86, s86, 4194303
buffer_load_dword v254, v133, s[40:43], s101 offen
buffer_load_dword v254, v134, s[40:43], s100 offen
s_add_u32 m0, s83, 0
s_nop 0
ld v232,s[44:47],s80 offen lds
s_add_u32 m0, s83, 1024
s_nop 0
ld v233,s[44:47],s80 offen lds
s_add_u32 m0, s83, 2176
s_nop 0
ld v234,s[44:47],s80 offen lds
s_add_u32 m0, s83, 3200
s_nop 0
ld v235,s[44:47],s80 offen lds
s_add_u32 s80, s80, s81
s_and_b32 s80, s80, 4194303
v_lshrrev_b32 v67, 1, v65
v_xor_b32 v67, v67, v66
v_lshlrev_b32 v67, 4, v67
v_lshl_add_u32 v67, v65, 7, v67
s_lshl_b32 s92, s77, 12
v_add_u32 v236, s92, v67
v_xor_b32 v237, 64, v236
v_bfe_u32 v120, v64, 2, 2
v_add_u32 v67, 1, v66
v_lshrrev_b32 v67, 1, v67
v_lshlrev_b32 v67, 7, v67
v_lshl_add_u32 v67, v66, 11, v67
v_lshl_add_u32 v67, v120, 8, v67
v_and_b32 v68, 3, v64
v_lshl_add_u32 v67, v68, 3, v67
s_mul_i32 s92, s78, 8448
v_lshl_add_u32 v67, v120, 5, v67
v_add_u32 v67, s92, v67
v_add_u32 v240, 96256, v67
v_xor_b32 v241, 32, v240
v_xor_b32 v242, 64, v240
v_xor_b32 v243, 96, v240
s_add_u32 s64, s26, 142606336
s_addc_u32 s65, s27, 0
s_and_b32 s65, s65, 0xffff
s_mov_b32 s66, 3145728
s_mov_b32 s67, 0x20000
s_mov_b32 s88, 4096
s_lshr_b32 s92, s2, 1
s_lshl_b32 s92, s92, 15
s_and_b32 s93, s2, 1
s_lshl_b32 s93, s93, 7
s_add_u32 s92, s92, s93
s_lshl_b32 s93, s3, 13
s_add_u32 s92, s92, s93
s_add_u32 s92, s92, 88080384
s_add_u32 s56, s26, s92
s_addc_u32 s57, s27, 0
s_and_b32 s57, s57, 0xffff
s_mov_b32 s58, 8576
s_mov_b32 s59, 0x20000
v_and_b32 v71, 0x380, v255
v_and_b32 v127, 0x70, v255
v_lshl_add_u32 v71, v71, 1, v127
v_xor_b32 v67, 3, v69
v_min_u32 v67, 1, v67
v_xor_b32 v67, 1, v67
s_mov_b32 s93, 0
v_mul_lo_u32 v67, v67, s93
v_add_u32 v136, v67, v255
v_and_b32 v67, 3, v69
v_xor_b32 v67, 1, v67
v_min_u32 v67, 1, v67
v_xor_b32 v67, 1, v67
v_and_b32 v67, s2, v67
v_mul_lo_u32 v67, v67, s93
v_add_u32 v71, v71, v67
s_lshl_b32 s92, s3, 3
v_lshlrev_b32 v126, 1, v127
v_or_b32 v126, 65536, v126
v_add_u32 v127, s92, v126
s_add_u32 s52, s26, 171966464
s_addc_u32 s53, s27, 0
s_and_b32 s53, s53, 0xffff
s_mov_b32 s54, 950272
s_mov_b32 s55, 0x20000
s_lshl2_add_u32 s95, s2, s3
s_lshl_b32 s91, s95, 3
s_lshl_b32 s95, s95, 10
s_lshl_b32 s89, s76, 10
v_lshl_add_u32 v135, v69, 28, v135
s_mov_b32 s96, s87
s_mov_b32 s84, 33792
s_mov_b32 s85, 0
s_add_u32 s97, s83, 67584
s_mov_b32 s90, s83
s_lshl_b32 s92, s72, 8
s_sub_u32 s93, 8388608, s92
s_add_u32 s68, s20, s92
s_addc_u32 s69, s21, 0
s_and_b32 s69, s69, 0xffff
s_mov_b32 s70, s93
s_mov_b32 s71, 0x20000
s_lshr_b32 s92, s20, 7
s_sub_u32 s92, 3, s92
s_bfe_u32 s99, s92, 0x20001
s_lshr_b32 s93, s2, 6
s_sub_u32 s99, s99, s93
s_lshl_b32 s92, s99, 8
s_lshr_b32 s93, s2, 3
s_lshl2_add_u32 s93, s93, s3
s_lshl_b32 s98, s93, 16
s_add_u32 s98, s98, s92
v_lshlrev_b32 v138, 4, v65
v_lshl_add_u32 v137, v66, 13, v138
v_lshl_add_u32 v138, v66, 9, v138
s_lshl_b32 s93, s93, 12
v_add_u32 v138, s93, v138
s_and_b32 s93, s2, 7
s_lshl_b32 s92, s93, 19
s_add_u32 s92, s92, 138412032
s_add_u32 s4, s26, s92
s_addc_u32 s5, s27, 0
s_and_b32 s5, s5, 0xffff
s_mov_b32 s6, 0
s_mov_b32 s100, 1073741824
s_add_u32 s101, s91, 65536
v_lshrrev_b32 v139, 1, v64
v_lshlrev_b32 v139, 8, v139
v_and_or_b32 v139, v255, 16, v139
s_lshl_b32 s93, s93, 5
s_add_u32 s93, s93, 64512
v_add_u32 v139, s93, v139
s_mov_b64 exec, 0
s_cmp_eq_u32 s3, 0
s_cbranch_scc1 .La5_LW1_0
s_cmp_eq_u32 s3, 1
s_cbranch_scc1 .La5_LW1_1
s_cmp_eq_u32 s3, 2
s_cbranch_scc1 .La5_LW1_2
s_branch .La5_LW1_3
.La5_LW1_R0:
s_mov_b64 exec, -1
s_xor_b32 m0, s82, 0
s_nop 0
ld v224,s[40:43],s79 offen lds
s_xor_b32 m0, s82, 1024
s_nop 0
ld v225,s[40:43],s79 offen lds
s_xor_b32 m0, s82, 2048
s_nop 0
ld v226,s[40:43],s79 offen lds
s_xor_b32 m0, s82, 3072
s_nop 0
ld v227,s[40:43],s79 offen lds
s_xor_b32 m0, s82, 4096
s_nop 0
ld v228,s[40:43],s79 offen lds
s_xor_b32 m0, s82, 5120
s_nop 0
ld v229,s[40:43],s79 offen lds
s_xor_b32 m0, s82, 6144
s_nop 0
ld v230,s[40:43],s79 offen lds
s_xor_b32 m0, s82, 7168
s_nop 0
ld v231,s[40:43],s79 offen lds
s_add_u32 s79, s79, 128
s_and_b32 s79, s79, 1023
s_add_u32 m0, s83, 16896
s_nop 0
ld v232,s[44:47],s80 offen lds
s_add_u32 m0, s83, 17920
s_nop 0
ld v233,s[44:47],s80 offen lds
s_add_u32 m0, s83, 19072
s_nop 0
ld v234,s[44:47],s80 offen lds
s_add_u32 m0, s83, 20096
s_nop 0
ld v235,s[44:47],s80 offen lds
s_add_u32 s80, s80, s81
s_and_b32 s80, s80, 4194303
buffer_load_dwordx2 v[252:253], off, s[64:67], 0 sc1
wt vmcnt(1)
s_barrier
rd v[160:163],v236 offset:24576
rd v[164:167],v237 offset:24576
rt v[192:193],v240 offset:0
rt v[194:195],v240 offset:1024
rt v[196:197],v240 offset:16896
rt v[198:199],v240 offset:17920
rt v[200:201],v241 offset:0
rt v[202:203],v241 offset:1024
rt v[204:205],v241 offset:16896
rt v[206:207],v241 offset:17920
rt v[208:209],v242 offset:0
rt v[210:211],v242 offset:1024
rt v[212:213],v242 offset:16896
rt v[214:215],v242 offset:17920
rt v[216:217],v243 offset:0
rt v[218:219],v243 offset:1024
rt v[220:221],v243 offset:16896
rt v[222:223],v243 offset:17920
rd v[168:171],v236 offset:26624
rd v[172:175],v237 offset:26624
rd v[176:179],v236 offset:16384
rd v[180:183],v237 offset:16384
rd v[184:187],v236 offset:18432
rd v[188:191],v237 offset:18432
wt lgkmcnt(0)
s_cmp_eq_u32 s78, 1
s_cbranch_scc1 .La5_LV_START
; ---- K tile (waves 0, 2): 256 keys x 128 columns, K = 512; LayerNorm of 8 rows of x in the gaps (softmax scale folded in)
.La5_LK_START:
mm a[96:99],v[192:195],v[160:163],0
s_add_u32 m0, s83, 33792
mm a[96:99],v[196:199],v[164:167],a[96:99]
ld v232,s[44:47],s80 offen lds
mm a[100:103],v[204:207],v[164:167],0
rd v[0:3],v236 offset:8192
rd v[4:7],v237 offset:8192
s_add_u32 m0, s83, 34816
mm a[100:103],v[200:203],v[160:163],a[100:103]
rd v[8:11],v236 offset:10240
rd v[12:15],v237 offset:10240
ld v233,s[44:47],s80 offen lds
mm a[104:107],v[208:211],v[160:163],0
s_add_u32 m0, s83, 35968
mm a[104:107],v[212:215],v[164:167],a[104:107]
ld v234,s[44:47],s80 offen lds
mm a[108:111],v[220:223],v[164:167],0
rd v[16:19],v236 offset:0
rd v[20:23],v237 offset:0
s_add_u32 m0, s83, 36992
mm a[108:111],v[216:219],v[160:163],a[108:111]
rd v[24:27],v236 offset:2048
rd v[28:31],v237 offset:2048
ld v235,s[44:47],s80 offen lds
mm a[140:143],v[216:219],v[168:171],0
s_mov_b32 s80, 2883584
mm a[140:143],v[220:223],v[172:175],a[140:143]
mm a[136:139],v[212:215],v[172:175],0
s_xor_b32 m0, s82, 32768
mm a[136:139],v[208:211],v[168:171],a[136:139]
rt v[32:33],v240 offset:128
rt v[34:35],v240 offset:1152
ld v224,s[40:43],s79 offen lds
mm a[124:127],v[200:203],v[168:171],0
s_xor_b32 m0, s82, 33792
mm a[124:127],v[204:207],v[172:175],a[124:127]
ld v225,s[40:43],s79 offen lds
mm a[120:123],v[196:199],v[172:175],0
s_xor_b32 m0, s82, 34816
mm a[120:123],v[192:195],v[168:171],a[120:123]
rt v[36:37],v240 offset:17024
rt v[38:39],v240 offset:18048
ld v226,s[40:43],s79 offen lds
mm a[232:235],v[192:195],v[176:179],0
s_xor_b32 m0, s82, 35840
mm a[232:235],v[196:199],v[180:183],a[232:235]
ld v227,s[40:43],s79 offen lds
mm a[236:239],v[204:207],v[180:183],0
s_xor_b32 m0, s82, 36864
mm a[236:239],v[200:203],v[176:179],a[236:239]
rt v[40:41],v241 offset:128
rt v[42:43],v241 offset:1152
ld v228,s[40:43],s79 offen lds
mm a[152:155],v[208:211],v[176:179],0
s_xor_b32 m0, s82, 37888
mm a[152:155],v[212:215],v[180:183],a[152:155]
ld v229,s[40:43],s79 offen lds
mm a[156:159],v[220:223],v[180:183],0
s_xor_b32 m0, s82, 38912
mm a[156:159],v[216:219],v[176:179],a[156:159]
ld v230,s[40:43],s79 offen lds
mm a[172:175],v[216:219],v[184:187],0
s_xor_b32 m0, s82, 39936
mm a[172:175],v[220:223],v[188:191],a[172:175]
ld v231,s[40:43],s79 offen lds
mm a[168:171],v[212:215],v[188:191],0
s_mov_b32 s79, 768
mm a[168:171],v[208:211],v[184:187],a[168:171]
rt v[44:45],v241 offset:17024
rt v[46:47],v241 offset:18048
mm a[220:223],v[200:203],v[184:187],0
s_add_u32 m0, s83, 50688
mm a[220:223],v[204:207],v[188:191],a[220:223]
ld v232,s[44:47],s80 offen lds
mm a[216:219],v[196:199],v[188:191],0
wt lgkmcnt(8)
s_add_u32 m0, s83, 51712
mm a[216:219],v[192:195],v[184:187],a[216:219]
ld v233,s[44:47],s80 offen lds
mm a[200:203],v[192:195],v[0:3],0
s_add_u32 m0, s83, 52864
mm a[200:203],v[196:199],v[4:7],a[200:203]
ld v234,s[44:47],s80 offen lds
mm a[204:207],v[204:207],v[4:7],0
s_add_u32 m0, s83, 53888
mm a[204:207],v[200:203],v[0:3],a[204:207]
rt v[48:49],v242 offset:128
rt v[50:51],v242 offset:1152
ld v235,s[44:47],s80 offen lds
mm a[184:187],v[208:211],v[0:3],0
s_mov_b32 s80, 3145728
mm a[184:187],v[212:215],v[4:7],a[184:187]
mm a[188:191],v[220:223],v[4:7],0
mm a[188:191],v[216:219],v[0:3],a[188:191]
mm a[12:15],v[216:219],v[8:11],0
mm a[12:15],v[220:223],v[12:15],a[12:15]
buffer_load_dword v70, v118, s[60:63], 0 offen
mm a[8:11],v[212:215],v[12:15],0
mm a[8:11],v[208:211],v[8:11],a[8:11]
rt v[52:53],v242 offset:17024
rt v[54:55],v242 offset:18048
mm a[76:79],v[200:203],v[8:11],0
mm a[76:79],v[204:207],v[12:15],a[76:79]
ld v[72:75],v119,s[60:63],0 offen offset:0
mm a[72:75],v[196:199],v[12:15],0
mm a[72:75],v[192:195],v[8:11],a[72:75]
rt v[56:57],v243 offset:128
rt v[58:59],v243 offset:1152
mm a[88:91],v[192:195],v[16:19],0
mm a[88:91],v[196:199],v[20:23],a[88:91]
ld v[76:79],v119,s[60:63],0 offen offset:128
mm a[92:95],v[204:207],v[20:23],0
mm a[92:95],v[200:203],v[16:19],a[92:95]
rt v[60:61],v243 offset:17024
rt v[62:63],v243 offset:18048
mm a[24:27],v[208:211],v[16:19],0
mm a[24:27],v[212:215],v[20:23],a[24:27]
ld v[80:83],v119,s[60:63],0 offen offset:256
mm a[28:31],v[220:223],v[20:23],0
mm a[28:31],v[216:219],v[16:19],a[28:31]
mm a[44:47],v[216:219],v[24:27],0
mm a[44:47],v[220:223],v[28:31],a[44:47]
ld v[84:87],v119,s[60:63],0 offen offset:384
mm a[40:43],v[212:215],v[28:31],0
mm a[40:43],v[208:211],v[24:27],a[40:43]
mm a[252:255],v[200:203],v[24:27],0
mm a[252:255],v[204:207],v[28:31],a[252:255]
mm a[248:251],v[196:199],v[28:31],0
mm a[248:251],v[192:195],v[24:27],a[248:251]
s_mov_b64 exec, 0
s_cmp_eq_u32 s3, 0
s_cbranch_scc1 .La5_LW2_0
s_branch .La5_LW2_R0
.La5_LW2_R0:
s_mov_b64 exec, -1
wt vmcnt(5) lgkmcnt(0)
s_barrier
mm a[112:115],v[32:35],v[160:163],0
mm a[112:115],v[36:39],v[164:167],a[112:115]
mm a[116:119],v[44:47],v[164:167],0
rt v[192:193],v240 offset:33792
rt v[194:195],v240 offset:34816
mm a[116:119],v[40:43],v[160:163],a[116:119]
rt v[196:197],v240 offset:50688
rt v[198:199],v240 offset:51712
mm a[128:131],v[48:51],v[160:163],0
mm a[128:131],v[52:55],v[164:167],a[128:131]
mm a[132:135],v[60:63],v[164:167],0
mm a[132:135],v[56:59],v[160:163],a[132:135]
rd v[160:163],v236 offset:57344
rd v[164:167],v237 offset:57344
mm a[148:151],v[56:59],v[168:171],0
mm a[148:151],v[60:63],v[172:175],a[148:151]
mm a[144:147],v[52:55],v[172:175],0
mm a[144:147],v[48:51],v[168:171],a[144:147]
rt v[200:201],v241 offset:33792
rt v[202:203],v241 offset:34816
mm a[228:231],v[40:43],v[168:171],0
mm a[228:231],v[44:47],v[172:175],a[228:231]
mm a[224:227],v[36:39],v[172:175],0
mm a[224:227],v[32:35],v[168:171],a[224:227]
rd v[168:171],v236 offset:59392
rd v[172:175],v237 offset:59392
mm a[208:211],v[32:35],v[176:179],0
s_xor_b32 m0, s82, 0
mm a[208:211],v[36:39],v[180:183],a[208:211]
ld v224,s[40:43],s79 offen lds
mm a[212:215],v[44:47],v[180:183],0
mm a[212:215],v[40:43],v[176:179],a[212:215]
rt v[204:205],v241 offset:50688
rt v[206:207],v241 offset:51712
s_xor_b32 m0, s82, 1024
mm a[160:163],v[48:51],v[176:179],0
ld v225,s[40:43],s79 offen lds
mm a[160:163],v[52:55],v[180:183],a[160:163]
mm a[164:167],v[60:63],v[180:183],0
s_xor_b32 m0, s82, 2048
mm a[164:167],v[56:59],v[176:179],a[164:167]
rd v[176:179],v236 offset:49152
rd v[180:183],v237 offset:49152
ld v226,s[40:43],s79 offen lds
mm a[180:183],v[56:59],v[184:187],0
mm a[180:183],v[60:63],v[188:191],a[180:183]
s_xor_b32 m0, s82, 3072
mm a[176:179],v[52:55],v[188:191],0
ld v227,s[40:43],s79 offen lds
mm a[176:179],v[48:51],v[184:187],a[176:179]
rt v[208:209],v242 offset:33792
rt v[210:211],v242 offset:34816
mm a[196:199],v[40:43],v[184:187],0
s_xor_b32 m0, s82, 4096
mm a[196:199],v[44:47],v[188:191],a[196:199]
ld v228,s[40:43],s79 offen lds
mm a[192:195],v[36:39],v[188:191],0
mm a[192:195],v[32:35],v[184:187],a[192:195]
rd v[184:187],v236 offset:51200
rd v[188:191],v237 offset:51200
s_xor_b32 m0, s82, 5120
mm a[64:67],v[32:35],v[0:3],0
ld v229,s[40:43],s79 offen lds
mm a[64:67],v[36:39],v[4:7],a[64:67]
mm a[68:71],v[44:47],v[4:7],0
s_xor_b32 m0, s82, 6144
mm a[68:71],v[40:43],v[0:3],a[68:71]
rt v[212:213],v242 offset:50688
rt v[214:215],v242 offset:51712
ld v230,s[40:43],s79 offen lds
mm a[0:3],v[48:51],v[0:3],0
mm a[0:3],v[52:55],v[4:7],a[0:3]
s_xor_b32 m0, s82, 7168
mm a[4:7],v[60:63],v[4:7],0
ld v231,s[40:43],s79 offen lds
mm a[4:7],v[56:59],v[0:3],a[4:7]
rd v[0:3],v236 offset:40960
rd v[4:7],v237 offset:40960
s_mov_b32 s79, 896
mm a[20:23],v[56:59],v[8:11],0
mm a[20:23],v[60:63],v[12:15],a[20:23]
s_add_u32 m0, s83, 0
mm a[16:19],v[52:55],v[12:15],0
ld v232,s[44:47],s80 offen lds
mm a[16:19],v[48:51],v[8:11],a[16:19]
rt v[216:217],v243 offset:33792
rt v[218:219],v243 offset:34816
mm a[84:87],v[40:43],v[8:11],0
s_add_u32 m0, s83, 1024
mm a[84:87],v[44:47],v[12:15],a[84:87]
ld v233,s[44:47],s80 offen lds
mm a[80:83],v[36:39],v[12:15],0
wt vmcnt(10)
v_lshlrev_b32 v70, 16, v70
mm a[80:83],v[32:35],v[8:11],a[80:83]
rd v[8:11],v236 offset:43008
rd v[12:15],v237 offset:43008
s_add_u32 m0, s83, 2176
mm a[240:243],v[32:35],v[16:19],0
ld v234,s[44:47],s80 offen lds
mm a[240:243],v[36:39],v[20:23],a[240:243]
v_lshlrev_b32 v88, 16, v72
v_and_b32 v89, 0xffff0000, v72
mm a[244:247],v[44:47],v[20:23],0
s_add_u32 m0, s83, 3200
v_sub_f32 v88, v88, v70
mm a[244:247],v[40:43],v[16:19],a[244:247]
rt v[220:221],v243 offset:50688
rt v[222:223],v243 offset:51712
ld v235,s[44:47],s80 offen lds
mm a[32:35],v[48:51],v[16:19],0
s_mov_b32 s80, 3407872
v_sub_f32 v89, v89, v70
mm a[32:35],v[52:55],v[20:23],a[32:35]
v_add_f32 v120, v88, v89
v_mul_f32 v122, v88, v88
mm a[36:39],v[60:63],v[20:23],0
s_add_u32 m0, s83, 16896
v_mul_f32 v123, v89, v89
mm a[36:39],v[56:59],v[16:19],a[36:39]
rd v[16:19],v236 offset:32768
rd v[20:23],v237 offset:32768
ld v232,s[44:47],s80 offen lds
mm a[60:63],v[56:59],v[24:27],0
v_lshlrev_b32 v90, 16, v73
v_and_b32 v91, 0xffff0000, v73
mm a[60:63],v[60:63],v[28:31],a[60:63]
s_add_u32 m0, s83, 17920
v_sub_f32 v90, v90, v70
mm a[56:59],v[52:55],v[28:31],0
ld v233,s[44:47],s80 offen lds
mm a[56:59],v[48:51],v[24:27],a[56:59]
v_sub_f32 v91, v91, v70
v_add_f32 v124, v90, v91
mm a[52:55],v[40:43],v[24:27],0
s_add_u32 m0, s83, 19072
v_fmac_f32 v122, v90, v90
mm a[52:55],v[44:47],v[28:31],a[52:55]
ld v234,s[44:47],s80 offen lds
mm a[48:51],v[36:39],v[28:31],0
wt lgkmcnt(2)
v_add_f32 v120, v120, v124
mm a[48:51],v[32:35],v[24:27],a[48:51]
rd v[24:27],v236 offset:34816
rd v[28:31],v237 offset:34816
s_add_u32 m0, s83, 20096
mm a[96:99],v[192:195],v[160:163],a[96:99]
ld v235,s[44:47],s80 offen lds
.La5_LW1_0:
s_cbranch_execz .La5_LW1_4
mm a[96:99],v[196:199],v[164:167],a[96:99]
s_mov_b32 s80, 3670016
v_fmac_f32 v123, v91, v91
mm a[100:103],v[204:207],v[164:167],a[100:103]
v_lshlrev_b32 v92, 16, v74
v_and_b32 v93, 0xffff0000, v74
mm a[100:103],v[200:203],v[160:163],a[100:103]
rt v[32:33],v240 offset:33920
rt v[34:35],v240 offset:34944
mm a[104:107],v[208:211],v[160:163],a[104:107]
v_sub_f32 v92, v92, v70
v_sub_f32 v93, v93, v70
mm a[104:107],v[212:215],v[164:167],a[104:107]
v_add_f32 v124, v92, v93
v_fmac_f32 v122, v92, v92
mm a[108:111],v[220:223],v[164:167],a[108:111]
wt lgkmcnt(2)
s_barrier
mm a[108:111],v[216:219],v[160:163],a[108:111]
v_add_f32 v120, v120, v124
v_fmac_f32 v123, v93, v93
mm a[140:143],v[216:219],v[168:171],a[140:143]
s_xor_b32 m0, s82, 32768
v_lshlrev_b32 v94, 16, v75
mm a[140:143],v[220:223],v[172:175],a[140:143]
ld v224,s[40:43],s79 offen lds
mm a[136:139],v[212:215],v[172:175],a[136:139]
v_and_b32 v95, 0xffff0000, v75
v_sub_f32 v94, v94, v70
mm a[136:139],v[208:211],v[168:171],a[136:139]
rt v[36:37],v240 offset:50816
rt v[38:39],v240 offset:51840
mm a[124:127],v[200:203],v[168:171],a[124:127]
s_xor_b32 m0, s82, 33792
v_sub_f32 v95, v95, v70
mm a[124:127],v[204:207],v[172:175],a[124:127]
ld v225,s[40:43],s79 offen lds
mm a[120:123],v[196:199],v[172:175],a[120:123]
v_add_f32 v124, v94, v95
v_fmac_f32 v122, v94, v94
mm a[120:123],v[192:195],v[168:171],a[120:123]
v_add_f32 v120, v120, v124
v_fmac_f32 v123, v95, v95
mm a[232:235],v[192:195],v[176:179],a[232:235]
s_xor_b32 m0, s82, 34816
v_lshlrev_b32 v96, 16, v76
mm a[232:235],v[196:199],v[180:183],a[232:235]
ld v226,s[40:43],s79 offen lds
mm a[236:239],v[204:207],v[180:183],a[236:239]
v_and_b32 v97, 0xffff0000, v76
v_sub_f32 v96, v96, v70
mm a[236:239],v[200:203],v[176:179],a[236:239]
rt v[40:41],v241 offset:33920
rt v[42:43],v241 offset:34944
mm a[152:155],v[208:211],v[176:179],a[152:155]
s_xor_b32 m0, s82, 35840
v_sub_f32 v97, v97, v70
mm a[152:155],v[212:215],v[180:183],a[152:155]
ld v227,s[40:43],s79 offen lds
mm a[156:159],v[220:223],v[180:183],a[156:159]
v_add_f32 v124, v96, v97
v_fmac_f32 v122, v96, v96
mm a[156:159],v[216:219],v[176:179],a[156:159]
v_add_f32 v120, v120, v124
v_fmac_f32 v123, v97, v97
mm a[172:175],v[216:219],v[184:187],a[172:175]
s_xor_b32 m0, s82, 36864
v_lshlrev_b32 v98, 16, v77
mm a[172:175],v[220:223],v[188:191],a[172:175]
ld v228,s[40:43],s79 offen lds
mm a[168:171],v[212:215],v[188:191],a[168:171]
v_and_b32 v99, 0xffff0000, v77
v_sub_f32 v98, v98, v70
mm a[168:171],v[208:211],v[184:187],a[168:171]
rt v[44:45],v241 offset:50816
rt v[46:47],v241 offset:51840
mm a[220:223],v[200:203],v[184:187],a[220:223]
s_xor_b32 m0, s82, 37888
v_sub_f32 v99, v99, v70
mm a[220:223],v[204:207],v[188:191],a[220:223]
ld v229,s[40:43],s79 offen lds
mm a[216:219],v[196:199],v[188:191],a[216:219]
v_add_f32 v124, v98, v99
v_fmac_f32 v122, v98, v98
mm a[216:219],v[192:195],v[184:187],a[216:219]
v_add_f32 v120, v120, v124
v_fmac_f32 v123, v99, v99
mm a[200:203],v[192:195],v[0:3],a[200:203]
s_xor_b32 m0, s82, 38912
v_lshlrev_b32 v100, 16, v78
mm a[200:203],v[196:199],v[4:7],a[200:203]
ld v230,s[40:43],s79 offen lds
mm a[204:207],v[204:207],v[4:7],a[204:207]
v_and_b32 v101, 0xffff0000, v78
v_sub_f32 v100, v100, v70
mm a[204:207],v[200:203],v[0:3],a[204:207]
rt v[48:49],v242 offset:33920
rt v[50:51],v242 offset:34944
mm a[184:187],v[208:211],v[0:3],a[184:187]
s_xor_b32 m0, s82, 39936
v_sub_f32 v101, v101, v70
mm a[184:187],v[212:215],v[4:7],a[184:187]
ld v231,s[40:43],s79 offen lds
mm a[188:191],v[220:223],v[4:7],a[188:191]
s_mov_b32 s79, 0
v_add_f32 v124, v100, v101
mm a[188:191],v[216:219],v[0:3],a[188:191]
v_fmac_f32 v122, v100, v100
v_add_f32 v120, v120, v124
mm a[12:15],v[216:219],v[8:11],a[12:15]
v_fmac_f32 v123, v101, v101
v_lshlrev_b32 v102, 16, v79
mm a[12:15],v[220:223],v[12:15],a[12:15]
buffer_load_dword v254, v132, s[44:47], s86 offen
mm a[8:11],v[212:215],v[12:15],a[8:11]
s_add_u32 s86, s86, 2097152
s_and_b32 s86, s86, 4194303
mm a[8:11],v[208:211],v[8:11],a[8:11]
rt v[52:53],v242 offset:50816
rt v[54:55],v242 offset:51840
mm a[76:79],v[200:203],v[8:11],a[76:79]
v_and_b32 v103, 0xffff0000, v79
v_sub_f32 v102, v102, v70
mm a[76:79],v[204:207],v[12:15],a[76:79]
v_sub_f32 v103, v103, v70
v_add_f32 v124, v102, v103
mm a[72:75],v[196:199],v[12:15],a[72:75]
v_fmac_f32 v122, v102, v102
v_add_f32 v120, v120, v124
mm a[72:75],v[192:195],v[8:11],a[72:75]
rt v[56:57],v243 offset:33920
rt v[58:59],v243 offset:34944
mm a[88:91],v[192:195],v[16:19],a[88:91]
v_fmac_f32 v123, v103, v103
v_lshlrev_b32 v104, 16, v80
mm a[88:91],v[196:199],v[20:23],a[88:91]
v_and_b32 v105, 0xffff0000, v80
v_sub_f32 v104, v104, v70
mm a[92:95],v[204:207],v[20:23],a[92:95]
v_sub_f32 v105, v105, v70
v_add_f32 v124, v104, v105
mm a[92:95],v[200:203],v[16:19],a[92:95]
rt v[60:61],v243 offset:50816
rt v[62:63],v243 offset:51840
mm a[24:27],v[208:211],v[16:19],a[24:27]
v_fmac_f32 v122, v104, v104
v_add_f32 v120, v120, v124
mm a[24:27],v[212:215],v[20:23],a[24:27]
v_fmac_f32 v123, v105, v105
v_lshlrev_b32 v106, 16, v81
mm a[28:31],v[220:223],v[20:23],a[28:31]
v_and_b32 v107, 0xffff0000, v81
v_sub_f32 v106, v106, v70
mm a[28:31],v[216:219],v[16:19],a[28:31]
v_sub_f32 v107, v107, v70
v_add_f32 v124, v106, v107
mm a[44:47],v[216:219],v[24:27],a[44:47]
v_fmac_f32 v122, v106, v106
v_add_f32 v120, v120, v124
mm a[44:47],v[220:223],v[28:31],a[44:47]
v_fmac_f32 v123, v107, v107
v_lshlrev_b32 v108, 16, v82
mm a[40:43],v[212:215],v[28:31],a[40:43]
v_and_b32 v109, 0xffff0000, v82
v_sub_f32 v108, v108, v70
mm a[40:43],v[208:211],v[24:27],a[40:43]
v_sub_f32 v109, v109, v70
v_add_f32 v124, v108, v109
mm a[252:255],v[200:203],v[24:27],a[252:255]
v_fmac_f32 v122, v108, v108
v_add_f32 v120, v120, v124
mm a[252:255],v[204:207],v[28:31],a[252:255]
v_fmac_f32 v123, v109, v109
v_lshlrev_b32 v110, 16, v83
mm a[248:251],v[196:199],v[28:31],a[248:251]
wt vmcnt(9) lgkmcnt(0)
s_barrier
mm a[248:251],v[192:195],v[24:27],a[248:251]
v_and_b32 v111, 0xffff0000, v83
v_sub_f32 v110, v110, v70
mm a[112:115],v[32:35],v[160:163],a[112:115]
v_sub_f32 v111, v111, v70
v_add_f32 v124, v110, v111
mm a[112:115],v[36:39],v[164:167],a[112:115]
v_fmac_f32 v122, v110, v110
v_add_f32 v120, v120, v124
mm a[116:119],v[44:47],v[164:167],a[116:119]
rt v[192:193],v240 offset:0
rt v[194:195],v240 offset:1024
mm a[116:119],v[40:43],v[160:163],a[116:119]
rt v[196:197],v240 offset:16896
rt v[198:199],v240 offset:17920
mm a[128:131],v[48:51],v[160:163],a[128:131]
v_fmac_f32 v123, v111, v111
v_lshlrev_b32 v112, 16, v84
mm a[128:131],v[52:55],v[164:167],a[128:131]
v_and_b32 v113, 0xffff0000, v84
v_sub_f32 v112, v112, v70
mm a[132:135],v[60:63],v[164:167],a[132:135]
v_sub_f32 v113, v113, v70
v_add_f32 v124, v112, v113
mm a[132:135],v[56:59],v[160:163],a[132:135]
rd v[160:163],v236 offset:24576
rd v[164:167],v237 offset:24576
mm a[148:151],v[56:59],v[168:171],a[148:151]
v_fmac_f32 v122, v112, v112
v_add_f32 v120, v120, v124
mm a[148:151],v[60:63],v[172:175],a[148:151]
v_fmac_f32 v123, v113, v113
v_lshlrev_b32 v114, 16, v85
mm a[144:147],v[52:55],v[172:175],a[144:147]
v_and_b32 v115, 0xffff0000, v85
v_sub_f32 v114, v114, v70
mm a[144:147],v[48:51],v[168:171],a[144:147]
rt v[200:201],v241 offset:0
rt v[202:203],v241 offset:1024
mm a[228:231],v[40:43],v[168:171],a[228:231]
v_sub_f32 v115, v115, v70
v_add_f32 v124, v114, v115
mm a[228:231],v[44:47],v[172:175],a[228:231]
v_fmac_f32 v122, v114, v114
v_add_f32 v120, v120, v124
mm a[224:227],v[36:39],v[172:175],a[224:227]
v_fmac_f32 v123, v115, v115
v_lshlrev_b32 v116, 16, v86
mm a[224:227],v[32:35],v[168:171],a[224:227]
rd v[168:171],v236 offset:26624
rd v[172:175],v237 offset:26624
mm a[208:211],v[32:35],v[176:179],a[208:211]
v_and_b32 v117, 0xffff0000, v86
v_sub_f32 v116, v116, v70
mm a[208:211],v[36:39],v[180:183],a[208:211]
v_sub_f32 v117, v117, v70
v_add_f32 v124, v116, v117
mm a[212:215],v[44:47],v[180:183],a[212:215]
v_fmac_f32 v122, v116, v116
v_add_f32 v120, v120, v124
mm a[212:215],v[40:43],v[176:179],a[212:215]
rt v[204:205],v241 offset:16896
rt v[206:207],v241 offset:17920
mm a[160:163],v[48:51],v[176:179],a[160:163]
v_fmac_f32 v123, v117, v117
v_lshlrev_b32 v118, 16, v87
mm a[160:163],v[52:55],v[180:183],a[160:163]
v_and_b32 v119, 0xffff0000, v87
v_sub_f32 v118, v118, v70
mm a[164:167],v[60:63],v[180:183],a[164:167]
v_sub_f32 v119, v119, v70
v_add_f32 v124, v118, v119
mm a[164:167],v[56:59],v[176:179],a[164:167]
rd v[176:179],v236 offset:16384
rd v[180:183],v237 offset:16384
mm a[180:183],v[56:59],v[184:187],a[180:183]
v_fmac_f32 v122, v118, v118
v_add_f32 v120, v120, v124
mm a[180:183],v[60:63],v[188:191],a[180:183]
v_fmac_f32 v123, v119, v119
v_add_f32 v121, v122, v123
mm a[176:179],v[52:55],v[188:191],a[176:179]
v_add_f32_dpp v120, v120, v120 row_ror:8 row_mask:0xf bank_mask:0xf
mm a[176:179],v[48:51],v[184:187],a[176:179]
rt v[208:209],v242 offset:0
rt v[210:211],v242 offset:1024
mm a[196:199],v[40:43],v[184:187],a[196:199]
s_add_u32 m0, s83, 33792
v_add_f32_dpp v121, v121, v121 row_ror:8 row_mask:0xf bank_mask:0xf
mm a[196:199],v[44:47],v[188:191],a[196:199]
ld v232,s[44:47],s80 offen lds
mm a[192:195],v[36:39],v[188:191],a[192:195]
v_mov_b32 v124, v120
v_mov_b32 v125, v121
mm a[192:195],v[32:35],v[184:187],a[192:195]
rd v[184:187],v236 offset:18432
rd v[188:191],v237 offset:18432
mm a[64:67],v[32:35],v[0:3],a[64:67]
s_add_u32 m0, s83, 34816
v_permlane16_swap_b32 v124, v120
mm a[64:67],v[36:39],v[4:7],a[64:67]
ld v233,s[44:47],s80 offen lds
mm a[68:71],v[44:47],v[4:7],a[68:71]
v_permlane16_swap_b32 v125, v121
mm a[68:71],v[40:43],v[0:3],a[68:71]
rt v[212:213],v242 offset:16896
rt v[214:215],v242 offset:17920
mm a[0:3],v[48:51],v[0:3],a[0:3]
s_add_u32 m0, s83, 35968
v_add_f32 v120, v120, v124
mm a[0:3],v[52:55],v[4:7],a[0:3]
ld v234,s[44:47],s80 offen lds
mm a[4:7],v[60:63],v[4:7],a[4:7]
v_add_f32 v121, v121, v125
v_mov_b32 v124, v120
mm a[4:7],v[56:59],v[0:3],a[4:7]
rd v[0:3],v236 offset:8192
rd v[4:7],v237 offset:8192
mm a[20:23],v[56:59],v[8:11],a[20:23]
s_add_u32 m0, s83, 36992
v_mov_b32 v125, v121
mm a[20:23],v[60:63],v[12:15],a[20:23]
ld v235,s[44:47],s80 offen lds
mm a[16:19],v[52:55],v[12:15],a[16:19]
s_mov_b32 s80, 3932160
v_permlane32_swap_b32 v124, v120
mm a[16:19],v[48:51],v[8:11],a[16:19]
rt v[216:217],v243 offset:0
rt v[218:219],v243 offset:1024
mm a[84:87],v[40:43],v[8:11],a[84:87]
s_add_u32 m0, s83, 50688
v_permlane32_swap_b32 v125, v121
mm a[84:87],v[44:47],v[12:15],a[84:87]
ld v232,s[44:47],s80 offen lds
mm a[80:83],v[36:39],v[12:15],a[80:83]
v_add_f32 v120, v120, v124
v_add_f32 v121, v121, v125
mm a[80:83],v[32:35],v[8:11],a[80:83]
rd v[8:11],v236 offset:10240
rd v[12:15],v237 offset:10240
mm a[240:243],v[32:35],v[16:19],a[240:243]
s_add_u32 m0, s83, 51712
ds_write_b64 v127, v[120:121]
mm a[240:243],v[36:39],v[20:23],a[240:243]
ld v233,s[44:47],s80 offen lds
mm a[244:247],v[44:47],v[20:23],a[244:247]
s_nop 0
v_readfirstlane_b32 s92, v252
mm a[244:247],v[40:43],v[16:19],a[244:247]
rt v[220:221],v243 offset:16896
rt v[222:223],v243 offset:17920
mm a[32:35],v[48:51],v[16:19],a[32:35]
s_add_u32 m0, s83, 52864
v_readfirstlane_b32 s93, v253
mm a[32:35],v[52:55],v[20:23],a[32:35]
ld v234,s[44:47],s80 offen lds
mm a[36:39],v[60:63],v[20:23],a[36:39]
s_add_u32 s92, s92, 1
s_addc_u32 s93, s93, 0
mm a[36:39],v[56:59],v[16:19],a[36:39]
rd v[16:19],v236 offset:0
rd v[20:23],v237 offset:0
mm a[60:63],v[56:59],v[24:27],a[60:63]
s_add_u32 m0, s83, 53888
v_mov_b32 v252, s92
mm a[60:63],v[60:63],v[28:31],a[60:63]
ld v235,s[44:47],s80 offen lds
mm a[56:59],v[52:55],v[28:31],a[56:59]
s_mov_b32 s80, 0
v_mov_b32 v253, s93
mm a[56:59],v[48:51],v[24:27],a[56:59]
v_mov_b32 v128, 0
v_mov_b32 v129, 0
mm a[52:55],v[40:43],v[24:27],a[52:55]
v_mov_b32 v130, 0
v_mov_b32 v131, 0
mm a[52:55],v[44:47],v[28:31],a[52:55]
st v[128:131],v255,s[52:55],s95 offen sc1
mm a[48:51],v[36:39],v[28:31],a[48:51]
wt lgkmcnt(2)
mm a[48:51],v[32:35],v[24:27],a[48:51]
rd v[24:27],v236 offset:2048
rd v[28:31],v237 offset:2048
mm a[96:99],v[192:195],v[160:163],a[96:99]
mm a[96:99],v[196:199],v[164:167],a[96:99]
mm a[100:103],v[204:207],v[164:167],a[100:103]
mm a[100:103],v[200:203],v[160:163],a[100:103]
rt v[32:33],v240 offset:128
rt v[34:35],v240 offset:1152
mm a[104:107],v[208:211],v[160:163],a[104:107]
mm a[104:107],v[212:215],v[164:167],a[104:107]
mm a[108:111],v[220:223],v[164:167],a[108:111]
wt lgkmcnt(2)
s_barrier
mm a[108:111],v[216:219],v[160:163],a[108:111]
rd v[72:75],v126
rd v[76:79],v126 offset:16
mm a[140:143],v[216:219],v[168:171],a[140:143]
s_xor_b32 m0, s82, 0
mm a[140:143],v[220:223],v[172:175],a[140:143]
ld v224,s[40:43],s79 offen lds
mm a[136:139],v[212:215],v[172:175],a[136:139]
mm a[136:139],v[208:211],v[168:171],a[136:139]
rt v[36:37],v240 offset:17024
rt v[38:39],v240 offset:18048
mm a[124:127],v[200:203],v[168:171],a[124:127]
s_xor_b32 m0, s82, 1024
mm a[124:127],v[204:207],v[172:175],a[124:127]
ld v225,s[40:43],s79 offen lds
mm a[120:123],v[196:199],v[172:175],a[120:123]
wt lgkmcnt(2)
v_add_f32 v72, v72, v74
mm a[120:123],v[192:195],v[168:171],a[120:123]
v_add_f32 v73, v73, v75
v_add_f32 v76, v76, v78
mm a[232:235],v[192:195],v[176:179],a[232:235]
s_xor_b32 m0, s82, 2048
v_add_f32 v77, v77, v79
mm a[232:235],v[196:199],v[180:183],a[232:235]
ld v226,s[40:43],s79 offen lds
mm a[236:239],v[204:207],v[180:183],a[236:239]
v_add_f32 v72, v72, v76
v_add_f32 v73, v73, v77
mm a[236:239],v[200:203],v[176:179],a[236:239]
rt v[40:41],v241 offset:128
rt v[42:43],v241 offset:1152
mm a[152:155],v[208:211],v[176:179],a[152:155]
s_xor_b32 m0, s82, 3072
v_mul_f32 v72, 0x3a800000, v72
mm a[152:155],v[212:215],v[180:183],a[152:155]
ld v227,s[40:43],s79 offen lds
mm a[156:159],v[220:223],v[180:183],a[156:159]
v_mul_f32 v73, 0x3a800000, v73
v_add_f32 v124, v70, v72
mm a[156:159],v[216:219],v[176:179],a[156:159]
v_fma_f32 v125, -v72, v72, v73
cv v124,v124,v124
mm a[172:175],v[216:219],v[184:187],a[172:175]
s_xor_b32 m0, s82, 4096
v_lshlrev_b32 v124, 16, v124
mm a[172:175],v[220:223],v[188:191],a[172:175]
ld v228,s[40:43],s79 offen lds
mm a[168:171],v[212:215],v[188:191],a[168:171]
v_max_f32 v125, 0, v125
cv v125,v125,v125
mm a[168:171],v[208:211],v[184:187],a[168:171]
rt v[44:45],v241 offset:17024
rt v[46:47],v241 offset:18048
mm a[220:223],v[200:203],v[184:187],a[220:223]
s_xor_b32 m0, s82, 5120
v_lshlrev_b32 v125, 16, v125
mm a[220:223],v[204:207],v[188:191],a[220:223]
ld v229,s[40:43],s79 offen lds
mm a[216:219],v[196:199],v[188:191],a[216:219]
v_add_f32 v125, 0x3727c5ac, v125
cv v125,v125,v125
mm a[216:219],v[192:195],v[184:187],a[216:219]
v_lshlrev_b32 v125, 16, v125
mm a[200:203],v[192:195],v[0:3],a[200:203]
s_xor_b32 m0, s82, 6144
mm a[200:203],v[196:199],v[4:7],a[200:203]
ld v230,s[40:43],s79 offen lds
mm a[204:207],v[204:207],v[4:7],a[204:207]
v_sqrt_f32 v125, v125
mm a[204:207],v[200:203],v[0:3],a[204:207]
rt v[48:49],v242 offset:128
rt v[50:51],v242 offset:1152
mm a[184:187],v[208:211],v[0:3],a[184:187]
s_xor_b32 m0, s82, 7168
v_sub_f32 v124, v70, v124
mm a[184:187],v[212:215],v[4:7],a[184:187]
ld v231,s[40:43],s79 offen lds
mm a[188:191],v[220:223],v[4:7],a[188:191]
s_mov_b32 s79, 128
cv v125,v125,v125
mm a[188:191],v[216:219],v[0:3],a[188:191]
v_lshlrev_b32 v125, 16, v125
mm a[12:15],v[216:219],v[8:11],a[12:15]
v_rcp_f32 v125, v125
mm a[12:15],v[220:223],v[12:15],a[12:15]
v_mul_f32 v125, 0x3e0293ee, v125
v_mul_f32 v124, v124, v125
mm a[8:11],v[212:215],v[12:15],a[8:11]
v_fma_f32 v88, v88, v125, v124
v_fma_f32 v89, v89, v125, v124
mm a[8:11],v[208:211],v[8:11],a[8:11]
rt v[52:53],v242 offset:17024
rt v[54:55],v242 offset:18048
mm a[76:79],v[200:203],v[8:11],a[76:79]
cv v72,v88,v89
v_fma_f32 v90, v90, v125, v124
mm a[76:79],v[204:207],v[12:15],a[76:79]
v_fma_f32 v91, v91, v125, v124
cv v73,v90,v91
mm a[72:75],v[196:199],v[12:15],a[72:75]
v_fma_f32 v92, v92, v125, v124
v_fma_f32 v93, v93, v125, v124
mm a[72:75],v[192:195],v[8:11],a[72:75]
rt v[56:57],v243 offset:128
rt v[58:59],v243 offset:1152
mm a[88:91],v[192:195],v[16:19],a[88:91]
cv v74,v92,v93
v_fma_f32 v94, v94, v125, v124
mm a[88:91],v[196:199],v[20:23],a[88:91]
v_fma_f32 v95, v95, v125, v124
cv v75,v94,v95
mm a[92:95],v[204:207],v[20:23],a[92:95]
st v[72:75],v71,s[56:59],0 offen offset:0 sc1
mm a[92:95],v[200:203],v[16:19],a[92:95]
rt v[60:61],v243 offset:17024
rt v[62:63],v243 offset:18048
mm a[24:27],v[208:211],v[16:19],a[24:27]
v_fma_f32 v96, v96, v125, v124
v_fma_f32 v97, v97, v125, v124
mm a[24:27],v[212:215],v[20:23],a[24:27]
cv v76,v96,v97
v_fma_f32 v98, v98, v125, v124
mm a[28:31],v[220:223],v[20:23],a[28:31]
v_fma_f32 v99, v99, v125, v124
cv v77,v98,v99
mm a[28:31],v[216:219],v[16:19],a[28:31]
v_fma_f32 v100, v100, v125, v124
v_fma_f32 v101, v101, v125, v124
mm a[44:47],v[216:219],v[24:27],a[44:47]
cv v78,v100,v101
v_fma_f32 v102, v102, v125, v124
mm a[44:47],v[220:223],v[28:31],a[44:47]
v_fma_f32 v103, v103, v125, v124
cv v79,v102,v103
mm a[40:43],v[212:215],v[28:31],a[40:43]
st v[76:79],v71,s[56:59],0 offen offset:2048 sc1
mm a[40:43],v[208:211],v[24:27],a[40:43]
v_fma_f32 v104, v104, v125, v124
v_fma_f32 v105, v105, v125, v124
mm a[252:255],v[200:203],v[24:27],a[252:255]
cv v80,v104,v105
v_fma_f32 v106, v106, v125, v124
mm a[252:255],v[204:207],v[28:31],a[252:255]
v_fma_f32 v107, v107, v125, v124
cv v81,v106,v107
mm a[248:251],v[196:199],v[28:31],a[248:251]
wt vmcnt(11) lgkmcnt(0)
s_barrier
mm a[248:251],v[192:195],v[24:27],a[248:251]
v_fma_f32 v108, v108, v125, v124
v_fma_f32 v109, v109, v125, v124
mm a[112:115],v[32:35],v[160:163],a[112:115]
cv v82,v108,v109
v_fma_f32 v110, v110, v125, v124
mm a[112:115],v[36:39],v[164:167],a[112:115]
v_fma_f32 v111, v111, v125, v124
cv v83,v110,v111
mm a[116:119],v[44:47],v[164:167],a[116:119]
rt v[192:193],v240 offset:33792
rt v[194:195],v240 offset:34816
mm a[116:119],v[40:43],v[160:163],a[116:119]
rt v[196:197],v240 offset:50688
rt v[198:199],v240 offset:51712
mm a[128:131],v[48:51],v[160:163],a[128:131]
st v[80:83],v71,s[56:59],s88 offen offset:0 sc1
mm a[128:131],v[52:55],v[164:167],a[128:131]
v_fma_f32 v112, v112, v125, v124
v_fma_f32 v113, v113, v125, v124
mm a[132:135],v[60:63],v[164:167],a[132:135]
cv v84,v112,v113
v_fma_f32 v114, v114, v125, v124
mm a[132:135],v[56:59],v[160:163],a[132:135]
rd v[160:163],v236 offset:57344
rd v[164:167],v237 offset:57344
mm a[148:151],v[56:59],v[168:171],a[148:151]
v_fma_f32 v115, v115, v125, v124
cv v85,v114,v115
mm a[148:151],v[60:63],v[172:175],a[148:151]
v_fma_f32 v116, v116, v125, v124
v_fma_f32 v117, v117, v125, v124
mm a[144:147],v[52:55],v[172:175],a[144:147]
cv v86,v116,v117
v_fma_f32 v118, v118, v125, v124
mm a[144:147],v[48:51],v[168:171],a[144:147]
rt v[200:201],v241 offset:33792
rt v[202:203],v241 offset:34816
mm a[228:231],v[40:43],v[168:171],a[228:231]
v_fma_f32 v119, v119, v125, v124
cv v87,v118,v119
mm a[228:231],v[44:47],v[172:175],a[228:231]
st v[84:87],v71,s[56:59],s88 offen offset:2048 sc1
mm a[224:227],v[36:39],v[172:175],a[224:227]
mm a[224:227],v[32:35],v[168:171],a[224:227]
rd v[168:171],v236 offset:59392
rd v[172:175],v237 offset:59392
mm a[208:211],v[32:35],v[176:179],a[208:211]
mm a[208:211],v[36:39],v[180:183],a[208:211]
mm a[212:215],v[44:47],v[180:183],a[212:215]
mm a[212:215],v[40:43],v[176:179],a[212:215]
rt v[204:205],v241 offset:50688
rt v[206:207],v241 offset:51712
mm a[160:163],v[48:51],v[176:179],a[160:163]
mm a[160:163],v[52:55],v[180:183],a[160:163]
mm a[164:167],v[60:63],v[180:183],a[164:167]
mm a[164:167],v[56:59],v[176:179],a[164:167]
rd v[176:179],v236 offset:49152
rd v[180:183],v237 offset:49152
mm a[180:183],v[56:59],v[184:187],a[180:183]
mm a[180:183],v[60:63],v[188:191],a[180:183]
mm a[176:179],v[52:55],v[188:191],a[176:179]
mm a[176:179],v[48:51],v[184:187],a[176:179]
rt v[208:209],v242 offset:33792
rt v[210:211],v242 offset:34816
mm a[196:199],v[40:43],v[184:187],a[196:199]
s_add_u32 m0, s83, 0
mm a[196:199],v[44:47],v[188:191],a[196:199]
.La5_LW1_1:
s_cbranch_execz .La5_LW1_5
ld v232,s[44:47],s80 offen lds
mm a[192:195],v[36:39],v[188:191],a[192:195]
mm a[192:195],v[32:35],v[184:187],a[192:195]
rd v[184:187],v236 offset:51200
rd v[188:191],v237 offset:51200
mm a[64:67],v[32:35],v[0:3],a[64:67]
s_add_u32 m0, s83, 1024
mm a[64:67],v[36:39],v[4:7],a[64:67]
ld v233,s[44:47],s80 offen lds
mm a[68:71],v[44:47],v[4:7],a[68:71]
mm a[68:71],v[40:43],v[0:3],a[68:71]
rt v[212:213],v242 offset:50688
rt v[214:215],v242 offset:51712
mm a[0:3],v[48:51],v[0:3],a[0:3]
s_add_u32 m0, s83, 2176
mm a[0:3],v[52:55],v[4:7],a[0:3]
ld v234,s[44:47],s80 offen lds
mm a[4:7],v[60:63],v[4:7],a[4:7]
mm a[4:7],v[56:59],v[0:3],a[4:7]
rd v[0:3],v236 offset:40960
rd v[4:7],v237 offset:40960
mm a[20:23],v[56:59],v[8:11],a[20:23]
s_add_u32 m0, s83, 3200
mm a[20:23],v[60:63],v[12:15],a[20:23]
ld v235,s[44:47],s80 offen lds
mm a[16:19],v[52:55],v[12:15],a[16:19]
s_mov_b32 s80, 262144
mm a[16:19],v[48:51],v[8:11],a[16:19]
rt v[216:217],v243 offset:33792
rt v[218:219],v243 offset:34816
mm a[84:87],v[40:43],v[8:11],a[84:87]
s_add_u32 m0, s83, 16896
mm a[84:87],v[44:47],v[12:15],a[84:87]
ld v232,s[44:47],s80 offen lds
mm a[80:83],v[36:39],v[12:15],a[80:83]
mm a[80:83],v[32:35],v[8:11],a[80:83]
rd v[8:11],v236 offset:43008
rd v[12:15],v237 offset:43008
mm a[240:243],v[32:35],v[16:19],a[240:243]
s_add_u32 m0, s83, 17920
mm a[240:243],v[36:39],v[20:23],a[240:243]
ld v233,s[44:47],s80 offen lds
mm a[244:247],v[44:47],v[20:23],a[244:247]
mm a[244:247],v[40:43],v[16:19],a[244:247]
rt v[220:221],v243 offset:50688
rt v[222:223],v243 offset:51712
mm a[32:35],v[48:51],v[16:19],a[32:35]
s_add_u32 m0, s83, 19072
mm a[32:35],v[52:55],v[20:23],a[32:35]
ld v234,s[44:47],s80 offen lds
mm a[36:39],v[60:63],v[20:23],a[36:39]
mm a[36:39],v[56:59],v[16:19],a[36:39]
rd v[16:19],v236 offset:32768
rd v[20:23],v237 offset:32768
mm a[60:63],v[56:59],v[24:27],a[60:63]
s_add_u32 m0, s83, 20096
mm a[60:63],v[60:63],v[28:31],a[60:63]
ld v235,s[44:47],s80 offen lds
mm a[56:59],v[52:55],v[28:31],a[56:59]
s_mov_b32 s80, 524288
mm a[56:59],v[48:51],v[24:27],a[56:59]
mm a[52:55],v[40:43],v[24:27],a[52:55]
mm a[52:55],v[44:47],v[28:31],a[52:55]
mm a[48:51],v[36:39],v[28:31],a[48:51]
wt lgkmcnt(2)
mm a[48:51],v[32:35],v[24:27],a[48:51]
rd v[24:27],v236 offset:34816
rd v[28:31],v237 offset:34816
mm a[96:99],v[192:195],v[160:163],a[96:99]
mm a[96:99],v[196:199],v[164:167],a[96:99]
mm a[100:103],v[204:207],v[164:167],a[100:103]
mm a[100:103],v[200:203],v[160:163],a[100:103]
rt v[32:33],v240 offset:33920
rt v[34:35],v240 offset:34944
mm a[104:107],v[208:211],v[160:163],a[104:107]
mm a[104:107],v[212:215],v[164:167],a[104:107]
mm a[108:111],v[220:223],v[164:167],a[108:111]
wt lgkmcnt(2)
s_barrier
mm a[108:111],v[216:219],v[160:163],a[108:111]
mm a[140:143],v[216:219],v[168:171],a[140:143]
s_xor_b32 m0, s82, 32768
mm a[140:143],v[220:223],v[172:175],a[140:143]
ld v224,s[40:43],s79 offen lds
mm a[136:139],v[212:215],v[172:175],a[136:139]
mm a[136:139],v[208:211],v[168:171],a[136:139]
rt v[36:37],v240 offset:50816
rt v[38:39],v240 offset:51840
mm a[124:127],v[200:203],v[168:171],a[124:127]
s_xor_b32 m0, s82, 33792
mm a[124:127],v[204:207],v[172:175],a[124:127]
ld v225,s[40:43],s79 offen lds
mm a[120:123],v[196:199],v[172:175],a[120:123]
mm a[120:123],v[192:195],v[168:171],a[120:123]
mm a[232:235],v[192:195],v[176:179],a[232:235]
s_xor_b32 m0, s82, 34816
mm a[232:235],v[196:199],v[180:183],a[232:235]
ld v226,s[40:43],s79 offen lds
mm a[236:239],v[204:207],v[180:183],a[236:239]
mm a[236:239],v[200:203],v[176:179],a[236:239]
rt v[40:41],v241 offset:33920
rt v[42:43],v241 offset:34944
mm a[152:155],v[208:211],v[176:179],a[152:155]
s_xor_b32 m0, s82, 35840
mm a[152:155],v[212:215],v[180:183],a[152:155]
ld v227,s[40:43],s79 offen lds
mm a[156:159],v[220:223],v[180:183],a[156:159]
mm a[156:159],v[216:219],v[176:179],a[156:159]
mm a[172:175],v[216:219],v[184:187],a[172:175]
s_xor_b32 m0, s82, 36864
mm a[172:175],v[220:223],v[188:191],a[172:175]
ld v228,s[40:43],s79 offen lds
mm a[168:171],v[212:215],v[188:191],a[168:171]
mm a[168:171],v[208:211],v[184:187],a[168:171]
rt v[44:45],v241 offset:50816
rt v[46:47],v241 offset:51840
mm a[220:223],v[200:203],v[184:187],a[220:223]
s_xor_b32 m0, s82, 37888
mm a[220:223],v[204:207],v[188:191],a[220:223]
ld v229,s[40:43],s79 offen lds
mm a[216:219],v[196:199],v[188:191],a[216:219]
mm a[216:219],v[192:195],v[184:187],a[216:219]
mm a[200:203],v[192:195],v[0:3],a[200:203]
s_xor_b32 m0, s82, 38912
mm a[200:203],v[196:199],v[4:7],a[200:203]
ld v230,s[40:43],s79 offen lds
mm a[204:207],v[204:207],v[4:7],a[204:207]
mm a[204:207],v[200:203],v[0:3],a[204:207]
rt v[48:49],v242 offset:33920
rt v[50:51],v242 offset:34944
mm a[184:187],v[208:211],v[0:3],a[184:187]
s_xor_b32 m0, s82, 39936
mm a[184:187],v[212:215],v[4:7],a[184:187]
ld v231,s[40:43],s79 offen lds
mm a[188:191],v[220:223],v[4:7],a[188:191]
s_mov_b32 s79, 256
mm a[188:191],v[216:219],v[0:3],a[188:191]
mm a[12:15],v[216:219],v[8:11],a[12:15]
mm a[12:15],v[220:223],v[12:15],a[12:15]
buffer_load_dword v254, v135, s[68:71], s96 offen
mm a[8:11],v[212:215],v[12:15],a[8:11]
s_add_u32 s96, s96, 1048576
mm a[8:11],v[208:211],v[8:11],a[8:11]
rt v[52:53],v242 offset:50816
rt v[54:55],v242 offset:51840
mm a[76:79],v[200:203],v[8:11],a[76:79]
mm a[76:79],v[204:207],v[12:15],a[76:79]
buffer_load_dword v254, v135, s[68:71], s96 offen
mm a[72:75],v[196:199],v[12:15],a[72:75]
s_add_u32 s96, s96, 1048576
mm a[72:75],v[192:195],v[8:11],a[72:75]
rt v[56:57],v243 offset:33920
rt v[58:59],v243 offset:34944
mm a[88:91],v[192:195],v[16:19],a[88:91]
mm a[88:91],v[196:199],v[20:23],a[88:91]
wt vmcnt(18)
buffer_store_dwordx2 v[252:253], off, s[64:67], s91 offset:1024 sc1
mm a[92:95],v[204:207],v[20:23],a[92:95]
s_mov_b32 s91, 0
s_lshl_b32 s92, s76, 19
mm a[92:95],v[200:203],v[16:19],a[92:95]
rt v[60:61],v243 offset:50816
rt v[62:63],v243 offset:51840
mm a[24:27],v[208:211],v[16:19],a[24:27]
s_add_u32 s92, s92, 88080384
mm a[24:27],v[212:215],v[20:23],a[24:27]
s_add_u32 s52, s26, s92
s_addc_u32 s53, s27, 0
mm a[28:31],v[220:223],v[20:23],a[28:31]
s_and_b32 s53, s53, 0xffff
s_mov_b32 s54, 524672
mm a[28:31],v[216:219],v[16:19],a[28:31]
s_mov_b32 s55, 0x20000
s_lshl_b32 s92, s3, 16
mm a[44:47],v[216:219],v[24:27],a[44:47]
s_sub_u32 s93, 458752, s92
v_add_u32 v244, s92, v136
mm a[44:47],v[220:223],v[28:31],a[44:47]
v_add_u32 v246, s93, v136
v_add_u32 v245, 0x8000, v244
mm a[40:43],v[212:215],v[28:31],a[40:43]
v_add_u32 v247, 0x8000, v246
s_lshl_b32 s94, s72, 19
mm a[40:43],v[208:211],v[24:27],a[40:43]
s_lshl_b32 s93, s76, 16
s_add_u32 s92, s94, s93
mm a[252:255],v[200:203],v[24:27],a[252:255]
s_lshl_b32 s93, s77, 13
s_add_u32 s92, s92, s93
mm a[252:255],v[204:207],v[28:31],a[252:255]
s_add_u32 s92, s92, 20971520
mm a[248:251],v[196:199],v[28:31],a[248:251]
wt vmcnt(11) lgkmcnt(0)
s_barrier
mm a[248:251],v[192:195],v[24:27],a[248:251]
s_add_u32 s48, s26, s92
s_addc_u32 s49, s27, 0
mm a[112:115],v[32:35],v[160:163],a[112:115]
s_and_b32 s49, s49, 0xffff
s_mov_b32 s50, 57344
mm a[112:115],v[36:39],v[164:167],a[112:115]
s_mov_b32 s51, 0x20000
s_lshl_b32 s93, s77, 24
mm a[116:119],v[44:47],v[164:167],a[116:119]
rt v[192:193],v240 offset:0
rt v[194:195],v240 offset:1024
mm a[116:119],v[40:43],v[160:163],a[116:119]
rt v[196:197],v240 offset:16896
rt v[198:199],v240 offset:17920
mm a[128:131],v[48:51],v[160:163],a[128:131]
s_add_u32 s92, s94, s93
s_add_u32 s92, s92, 20971520
mm a[128:131],v[52:55],v[164:167],a[128:131]
s_add_u32 s60, s26, s92
s_addc_u32 s61, s27, 0
mm a[132:135],v[60:63],v[164:167],a[132:135]
s_and_b32 s61, s61, 0xffff
s_mov_b32 s62, 524288
mm a[132:135],v[56:59],v[160:163],a[132:135]
rd v[160:163],v236 offset:24576
rd v[164:167],v237 offset:24576
mm a[148:151],v[56:59],v[168:171],a[148:151]
s_mov_b32 s63, 0x20000
s_lshl_b32 s92, s77, 14
mm a[148:151],v[60:63],v[172:175],a[148:151]
s_sub_u32 s95, 16384, s92
v_add_u32 v238, s95, v255
mm a[144:147],v[52:55],v[172:175],a[144:147]
mm a[144:147],v[48:51],v[168:171],a[144:147]
rt v[200:201],v241 offset:0
rt v[202:203],v241 offset:1024
mm a[228:231],v[40:43],v[168:171],a[228:231]
mm a[228:231],v[44:47],v[172:175],a[228:231]
mm a[224:227],v[36:39],v[172:175],a[224:227]
mm a[224:227],v[32:35],v[168:171],a[224:227]
rd v[168:171],v236 offset:26624
rd v[172:175],v237 offset:26624
mm a[208:211],v[32:35],v[176:179],a[208:211]
mm a[208:211],v[36:39],v[180:183],a[208:211]
mm a[212:215],v[44:47],v[180:183],a[212:215]
mm a[212:215],v[40:43],v[176:179],a[212:215]
rt v[204:205],v241 offset:16896
rt v[206:207],v241 offset:17920
mm a[160:163],v[48:51],v[176:179],a[160:163]
mm a[160:163],v[52:55],v[180:183],a[160:163]
mm a[164:167],v[60:63],v[180:183],a[164:167]
mm a[164:167],v[56:59],v[176:179],a[164:167]
rd v[176:179],v236 offset:16384
rd v[180:183],v237 offset:16384
mm a[180:183],v[56:59],v[184:187],a[180:183]
mm a[180:183],v[60:63],v[188:191],a[180:183]
mm a[176:179],v[52:55],v[188:191],a[176:179]
mm a[176:179],v[48:51],v[184:187],a[176:179]
rt v[208:209],v242 offset:0
rt v[210:211],v242 offset:1024
mm a[196:199],v[40:43],v[184:187],a[196:199]
s_add_u32 m0, s83, 33792
mm a[196:199],v[44:47],v[188:191],a[196:199]
ld v232,s[44:47],s80 offen lds
mm a[192:195],v[36:39],v[188:191],a[192:195]
mm a[192:195],v[32:35],v[184:187],a[192:195]
rd v[184:187],v236 offset:18432
rd v[188:191],v237 offset:18432
mm a[64:67],v[32:35],v[0:3],a[64:67]
s_add_u32 m0, s83, 34816
mm a[64:67],v[36:39],v[4:7],a[64:67]
ld v233,s[44:47],s80 offen lds
mm a[68:71],v[44:47],v[4:7],a[68:71]
mm a[68:71],v[40:43],v[0:3],a[68:71]
rt v[212:213],v242 offset:16896
rt v[214:215],v242 offset:17920
mm a[0:3],v[48:51],v[0:3],a[0:3]
s_add_u32 m0, s83, 35968
mm a[0:3],v[52:55],v[4:7],a[0:3]
ld v234,s[44:47],s80 offen lds
mm a[4:7],v[60:63],v[4:7],a[4:7]
mm a[4:7],v[56:59],v[0:3],a[4:7]
rd v[0:3],v236 offset:8192
rd v[4:7],v237 offset:8192
mm a[20:23],v[56:59],v[8:11],a[20:23]
s_add_u32 m0, s83, 36992
mm a[20:23],v[60:63],v[12:15],a[20:23]
ld v235,s[44:47],s80 offen lds
mm a[16:19],v[52:55],v[12:15],a[16:19]
s_mov_b32 s80, 786432
mm a[16:19],v[48:51],v[8:11],a[16:19]
rt v[216:217],v243 offset:0
rt v[218:219],v243 offset:1024
mm a[84:87],v[40:43],v[8:11],a[84:87]
s_add_u32 m0, s83, 50688
mm a[84:87],v[44:47],v[12:15],a[84:87]
ld v232,s[44:47],s80 offen lds
mm a[80:83],v[36:39],v[12:15],a[80:83]
mm a[80:83],v[32:35],v[8:11],a[80:83]
rd v[8:11],v236 offset:10240
rd v[12:15],v237 offset:10240
mm a[240:243],v[32:35],v[16:19],a[240:243]
s_add_u32 m0, s83, 51712
mm a[240:243],v[36:39],v[20:23],a[240:243]
ld v233,s[44:47],s80 offen lds
mm a[244:247],v[44:47],v[20:23],a[244:247]
mm a[244:247],v[40:43],v[16:19],a[244:247]
rt v[220:221],v243 offset:16896
rt v[222:223],v243 offset:17920
mm a[32:35],v[48:51],v[16:19],a[32:35]
s_add_u32 m0, s83, 52864
mm a[32:35],v[52:55],v[20:23],a[32:35]
ld v234,s[44:47],s80 offen lds
mm a[36:39],v[60:63],v[20:23],a[36:39]
mm a[36:39],v[56:59],v[16:19],a[36:39]
rd v[16:19],v236 offset:0
rd v[20:23],v237 offset:0
mm a[60:63],v[56:59],v[24:27],a[60:63]
s_add_u32 m0, s83, 53888
mm a[60:63],v[60:63],v[28:31],a[60:63]
ld v235,s[44:47],s80 offen lds
mm a[56:59],v[52:55],v[28:31],a[56:59]
s_mov_b32 s80, 1048576
mm a[56:59],v[48:51],v[24:27],a[56:59]
mm a[52:55],v[40:43],v[24:27],a[52:55]
mm a[52:55],v[44:47],v[28:31],a[52:55]
mm a[48:51],v[36:39],v[28:31],a[48:51]
wt lgkmcnt(2)
mm a[48:51],v[32:35],v[24:27],a[48:51]
rd v[24:27],v236 offset:2048
rd v[28:31],v237 offset:2048
mm a[96:99],v[192:195],v[160:163],a[96:99]
mm a[96:99],v[196:199],v[164:167],a[96:99]
mm a[100:103],v[204:207],v[164:167],a[100:103]
mm a[100:103],v[200:203],v[160:163],a[100:103]
rt v[32:33],v240 offset:128
rt v[34:35],v240 offset:1152
mm a[104:107],v[208:211],v[160:163],a[104:107]
mm a[104:107],v[212:215],v[164:167],a[104:107]
mm a[108:111],v[220:223],v[164:167],a[108:111]
wt lgkmcnt(2)
s_barrier
mm a[108:111],v[216:219],v[160:163],a[108:111]
mm a[140:143],v[216:219],v[168:171],a[140:143]
s_xor_b32 m0, s82, 0
mm a[140:143],v[220:223],v[172:175],a[140:143]
ld v224,s[40:43],s79 offen lds
mm a[136:139],v[212:215],v[172:175],a[136:139]
mm a[136:139],v[208:211],v[168:171],a[136:139]
rt v[36:37],v240 offset:17024
rt v[38:39],v240 offset:18048
mm a[124:127],v[200:203],v[168:171],a[124:127]
s_xor_b32 m0, s82, 1024
mm a[124:127],v[204:207],v[172:175],a[124:127]
ld v225,s[40:43],s79 offen lds
mm a[120:123],v[196:199],v[172:175],a[120:123]
mm a[120:123],v[192:195],v[168:171],a[120:123]
mm a[232:235],v[192:195],v[176:179],a[232:235]
s_xor_b32 m0, s82, 2048
mm a[232:235],v[196:199],v[180:183],a[232:235]
ld v226,s[40:43],s79 offen lds
mm a[236:239],v[204:207],v[180:183],a[236:239]
mm a[236:239],v[200:203],v[176:179],a[236:239]
rt v[40:41],v241 offset:128
rt v[42:43],v241 offset:1152
mm a[152:155],v[208:211],v[176:179],a[152:155]
s_xor_b32 m0, s82, 3072
mm a[152:155],v[212:215],v[180:183],a[152:155]
ld v227,s[40:43],s79 offen lds
mm a[156:159],v[220:223],v[180:183],a[156:159]
mm a[156:159],v[216:219],v[176:179],a[156:159]
mm a[172:175],v[216:219],v[184:187],a[172:175]
s_xor_b32 m0, s82, 4096
mm a[172:175],v[220:223],v[188:191],a[172:175]
ld v228,s[40:43],s79 offen lds
mm a[168:171],v[212:215],v[188:191],a[168:171]
mm a[168:171],v[208:211],v[184:187],a[168:171]
rt v[44:45],v241 offset:17024
rt v[46:47],v241 offset:18048
mm a[220:223],v[200:203],v[184:187],a[220:223]
s_xor_b32 m0, s82, 5120
mm a[220:223],v[204:207],v[188:191],a[220:223]
ld v229,s[40:43],s79 offen lds
mm a[216:219],v[196:199],v[188:191],a[216:219]
mm a[216:219],v[192:195],v[184:187],a[216:219]
mm a[200:203],v[192:195],v[0:3],a[200:203]
s_xor_b32 m0, s82, 6144
mm a[200:203],v[196:199],v[4:7],a[200:203]
ld v230,s[40:43],s79 offen lds
mm a[204:207],v[204:207],v[4:7],a[204:207]
mm a[204:207],v[200:203],v[0:3],a[204:207]
rt v[48:49],v242 offset:128
rt v[50:51],v242 offset:1152
mm a[184:187],v[208:211],v[0:3],a[184:187]
s_xor_b32 m0, s82, 7168
mm a[184:187],v[212:215],v[4:7],a[184:187]
ld v231,s[40:43],s79 offen lds
mm a[188:191],v[220:223],v[4:7],a[188:191]
s_mov_b32 s79, 384
mm a[188:191],v[216:219],v[0:3],a[188:191]
mm a[12:15],v[216:219],v[8:11],a[12:15]
mm a[12:15],v[220:223],v[12:15],a[12:15]
buffer_load_dword v254, v135, s[68:71], s96 offen
mm a[8:11],v[212:215],v[12:15],a[8:11]
s_add_u32 s96, s96, 1048576
mm a[8:11],v[208:211],v[8:11],a[8:11]
rt v[52:53],v242 offset:17024
rt v[54:55],v242 offset:18048
mm a[76:79],v[200:203],v[8:11],a[76:79]
mm a[76:79],v[204:207],v[12:15],a[76:79]
buffer_load_dword v254, v135, s[68:71], s96 offen
mm a[72:75],v[196:199],v[12:15],a[72:75]
s_add_u32 s96, s96, 1048576
mm a[72:75],v[192:195],v[8:11],a[72:75]
rt v[56:57],v243 offset:128
rt v[58:59],v243 offset:1152
mm a[88:91],v[192:195],v[16:19],a[88:91]
mm a[88:91],v[196:199],v[20:23],a[88:91]
mm a[92:95],v[204:207],v[20:23],a[92:95]
mm a[92:95],v[200:203],v[16:19],a[92:95]
rt v[60:61],v243 offset:17024
rt v[62:63],v243 offset:18048
mm a[24:27],v[208:211],v[16:19],a[24:27]
mm a[24:27],v[212:215],v[20:23],a[24:27]
mm a[28:31],v[220:223],v[20:23],a[28:31]
mm a[28:31],v[216:219],v[16:19],a[28:31]
mm a[44:47],v[216:219],v[24:27],a[44:47]
mm a[44:47],v[220:223],v[28:31],a[44:47]
mm a[40:43],v[212:215],v[28:31],a[40:43]
mm a[40:43],v[208:211],v[24:27],a[40:43]
mm a[252:255],v[200:203],v[24:27],a[252:255]
mm a[252:255],v[204:207],v[28:31],a[252:255]
mm a[248:251],v[196:199],v[28:31],a[248:251]
wt vmcnt(10) lgkmcnt(0)
s_barrier
mm a[248:251],v[192:195],v[24:27],a[248:251]
mm a[112:115],v[32:35],v[160:163],a[112:115]
mm a[112:115],v[36:39],v[164:167],a[112:115]
mm a[116:119],v[44:47],v[164:167],a[116:119]
rt v[192:193],v240 offset:33792
rt v[194:195],v240 offset:34816
mm a[116:119],v[40:43],v[160:163],a[116:119]
rt v[196:197],v240 offset:50688
rt v[198:199],v240 offset:51712
mm a[128:131],v[48:51],v[160:163],a[128:131]
mm a[128:131],v[52:55],v[164:167],a[128:131]
mm a[132:135],v[60:63],v[164:167],a[132:135]
mm a[132:135],v[56:59],v[160:163],a[132:135]
rd v[160:163],v236 offset:57344
rd v[164:167],v237 offset:57344
mm a[148:151],v[56:59],v[168:171],a[148:151]
mm a[148:151],v[60:63],v[172:175],a[148:151]
mm a[144:147],v[52:55],v[172:175],a[144:147]
mm a[144:147],v[48:51],v[168:171],a[144:147]
rt v[200:201],v241 offset:33792
rt v[202:203],v241 offset:34816
mm a[228:231],v[40:43],v[168:171],a[228:231]
mm a[228:231],v[44:47],v[172:175],a[228:231]
mm a[224:227],v[36:39],v[172:175],a[224:227]
mm a[224:227],v[32:35],v[168:171],a[224:227]
rd v[168:171],v236 offset:59392
rd v[172:175],v237 offset:59392
mm a[208:211],v[32:35],v[176:179],a[208:211]
mm a[208:211],v[36:39],v[180:183],a[208:211]
mm a[212:215],v[44:47],v[180:183],a[212:215]
mm a[212:215],v[40:43],v[176:179],a[212:215]
rt v[204:205],v241 offset:50688
rt v[206:207],v241 offset:51712
mm a[160:163],v[48:51],v[176:179],a[160:163]
mm a[160:163],v[52:55],v[180:183],a[160:163]
mm a[164:167],v[60:63],v[180:183],a[164:167]
mm a[164:167],v[56:59],v[176:179],a[164:167]
rd v[176:179],v236 offset:49152
rd v[180:183],v237 offset:49152
mm a[180:183],v[56:59],v[184:187],a[180:183]
mm a[180:183],v[60:63],v[188:191],a[180:183]
mm a[176:179],v[52:55],v[188:191],a[176:179]
mm a[176:179],v[48:51],v[184:187],a[176:179]
rt v[208:209],v242 offset:33792
rt v[210:211],v242 offset:34816
mm a[196:199],v[40:43],v[184:187],a[196:199]
s_add_u32 m0, s83, 0
mm a[196:199],v[44:47],v[188:191],a[196:199]
ld v232,s[44:47],s80 offen lds
mm a[192:195],v[36:39],v[188:191],a[192:195]
mm a[192:195],v[32:35],v[184:187],a[192:195]
rd v[184:187],v236 offset:51200
rd v[188:191],v237 offset:51200
mm a[64:67],v[32:35],v[0:3],a[64:67]
s_add_u32 m0, s83, 1024
mm a[64:67],v[36:39],v[4:7],a[64:67]
ld v233,s[44:47],s80 offen lds
mm a[68:71],v[44:47],v[4:7],a[68:71]
mm a[68:71],v[40:43],v[0:3],a[68:71]
rt v[212:213],v242 offset:50688
rt v[214:215],v242 offset:51712
mm a[0:3],v[48:51],v[0:3],a[0:3]
s_add_u32 m0, s83, 2176
mm a[0:3],v[52:55],v[4:7],a[0:3]
ld v234,s[44:47],s80 offen lds
mm a[4:7],v[60:63],v[4:7],a[4:7]
mm a[4:7],v[56:59],v[0:3],a[4:7]
rd v[0:3],v236 offset:40960
rd v[4:7],v237 offset:40960
mm a[20:23],v[56:59],v[8:11],a[20:23]
s_add_u32 m0, s83, 3200
mm a[20:23],v[60:63],v[12:15],a[20:23]
ld v235,s[44:47],s80 offen lds
mm a[16:19],v[52:55],v[12:15],a[16:19]
s_mov_b32 s80, 1310720
mm a[16:19],v[48:51],v[8:11],a[16:19]
rt v[216:217],v243 offset:33792
rt v[218:219],v243 offset:34816
mm a[84:87],v[40:43],v[8:11],a[84:87]
s_add_u32 m0, s83, 16896
mm a[84:87],v[44:47],v[12:15],a[84:87]
ld v232,s[44:47],s80 offen lds
mm a[80:83],v[36:39],v[12:15],a[80:83]
mm a[80:83],v[32:35],v[8:11],a[80:83]
rd v[8:11],v236 offset:43008
rd v[12:15],v237 offset:43008
mm a[240:243],v[32:35],v[16:19],a[240:243]
s_add_u32 m0, s83, 17920
mm a[240:243],v[36:39],v[20:23],a[240:243]
ld v233,s[44:47],s80 offen lds
mm a[244:247],v[44:47],v[20:23],a[244:247]
mm a[244:247],v[40:43],v[16:19],a[244:247]
rt v[220:221],v243 offset:50688
rt v[222:223],v243 offset:51712
mm a[32:35],v[48:51],v[16:19],a[32:35]
s_add_u32 m0, s83, 19072
mm a[32:35],v[52:55],v[20:23],a[32:35]
ld v234,s[44:47],s80 offen lds
.La5_LW1_2:
s_cbranch_execz .La5_LW1_6
mm a[36:39],v[60:63],v[20:23],a[36:39]
mm a[36:39],v[56:59],v[16:19],a[36:39]
rd v[16:19],v236 offset:32768
rd v[20:23],v237 offset:32768
mm a[60:63],v[56:59],v[24:27],a[60:63]
s_add_u32 m0, s83, 20096
mm a[60:63],v[60:63],v[28:31],a[60:63]
ld v235,s[44:47],s80 offen lds
mm a[56:59],v[52:55],v[28:31],a[56:59]
s_mov_b32 s80, 1572864
mm a[56:59],v[48:51],v[24:27],a[56:59]
mm a[52:55],v[40:43],v[24:27],a[52:55]
mm a[52:55],v[44:47],v[28:31],a[52:55]
mm a[48:51],v[36:39],v[28:31],a[48:51]
wt lgkmcnt(2)
mm a[48:51],v[32:35],v[24:27],a[48:51]
rd v[24:27],v236 offset:34816
rd v[28:31],v237 offset:34816
mm a[96:99],v[192:195],v[160:163],a[96:99]
mm a[96:99],v[196:199],v[164:167],a[96:99]
mm a[100:103],v[204:207],v[164:167],a[100:103]
mm a[100:103],v[200:203],v[160:163],a[100:103]
rt v[32:33],v240 offset:33920
rt v[34:35],v240 offset:34944
mm a[104:107],v[208:211],v[160:163],a[104:107]
mm a[104:107],v[212:215],v[164:167],a[104:107]
mm a[108:111],v[220:223],v[164:167],a[108:111]
wt lgkmcnt(2)
s_barrier
mm a[108:111],v[216:219],v[160:163],a[108:111]
mm a[140:143],v[216:219],v[168:171],a[140:143]
s_xor_b32 m0, s82, 32768
mm a[140:143],v[220:223],v[172:175],a[140:143]
ld v224,s[40:43],s79 offen lds
mm a[136:139],v[212:215],v[172:175],a[136:139]
mm a[136:139],v[208:211],v[168:171],a[136:139]
rt v[36:37],v240 offset:50816
rt v[38:39],v240 offset:51840
mm a[124:127],v[200:203],v[168:171],a[124:127]
s_xor_b32 m0, s82, 33792
mm a[124:127],v[204:207],v[172:175],a[124:127]
ld v225,s[40:43],s79 offen lds
mm a[120:123],v[196:199],v[172:175],a[120:123]
mm a[120:123],v[192:195],v[168:171],a[120:123]
mm a[232:235],v[192:195],v[176:179],a[232:235]
s_xor_b32 m0, s82, 34816
mm a[232:235],v[196:199],v[180:183],a[232:235]
ld v226,s[40:43],s79 offen lds
mm a[236:239],v[204:207],v[180:183],a[236:239]
mm a[236:239],v[200:203],v[176:179],a[236:239]
rt v[40:41],v241 offset:33920
rt v[42:43],v241 offset:34944
mm a[152:155],v[208:211],v[176:179],a[152:155]
s_xor_b32 m0, s82, 35840
mm a[152:155],v[212:215],v[180:183],a[152:155]
ld v227,s[40:43],s79 offen lds
mm a[156:159],v[220:223],v[180:183],a[156:159]
mm a[156:159],v[216:219],v[176:179],a[156:159]
mm a[172:175],v[216:219],v[184:187],a[172:175]
s_xor_b32 m0, s82, 36864
mm a[172:175],v[220:223],v[188:191],a[172:175]
ld v228,s[40:43],s79 offen lds
mm a[168:171],v[212:215],v[188:191],a[168:171]
mm a[168:171],v[208:211],v[184:187],a[168:171]
rt v[44:45],v241 offset:50816
rt v[46:47],v241 offset:51840
mm a[220:223],v[200:203],v[184:187],a[220:223]
s_xor_b32 m0, s82, 37888
mm a[220:223],v[204:207],v[188:191],a[220:223]
ld v229,s[40:43],s79 offen lds
mm a[216:219],v[196:199],v[188:191],a[216:219]
mm a[216:219],v[192:195],v[184:187],a[216:219]
mm a[200:203],v[192:195],v[0:3],a[200:203]
s_xor_b32 m0, s82, 38912
mm a[200:203],v[196:199],v[4:7],a[200:203]
ld v230,s[40:43],s79 offen lds
mm a[204:207],v[204:207],v[4:7],a[204:207]
mm a[204:207],v[200:203],v[0:3],a[204:207]
rt v[48:49],v242 offset:33920
rt v[50:51],v242 offset:34944
mm a[184:187],v[208:211],v[0:3],a[184:187]
s_xor_b32 m0, s82, 39936
mm a[184:187],v[212:215],v[4:7],a[184:187]
ld v231,s[40:43],s79 offen lds
mm a[188:191],v[220:223],v[4:7],a[188:191]
s_mov_b32 s79, 512
mm a[188:191],v[216:219],v[0:3],a[188:191]
mm a[12:15],v[216:219],v[8:11],a[12:15]
mm a[12:15],v[220:223],v[12:15],a[12:15]
buffer_load_dword v254, v135, s[68:71], s96 offen
mm a[8:11],v[212:215],v[12:15],a[8:11]
s_add_u32 s96, s96, 1048576
mm a[8:11],v[208:211],v[8:11],a[8:11]
rt v[52:53],v242 offset:50816
rt v[54:55],v242 offset:51840
mm a[76:79],v[200:203],v[8:11],a[76:79]
mm a[76:79],v[204:207],v[12:15],a[76:79]
buffer_load_dword v254, v135, s[68:71], s96 offen
mm a[72:75],v[196:199],v[12:15],a[72:75]
s_add_u32 s96, s96, 1048576
mm a[72:75],v[192:195],v[8:11],a[72:75]
rt v[56:57],v243 offset:33920
rt v[58:59],v243 offset:34944
mm a[88:91],v[192:195],v[16:19],a[88:91]
mm a[88:91],v[196:199],v[20:23],a[88:91]
mm a[92:95],v[204:207],v[20:23],a[92:95]
mm a[92:95],v[200:203],v[16:19],a[92:95]
rt v[60:61],v243 offset:50816
rt v[62:63],v243 offset:51840
mm a[24:27],v[208:211],v[16:19],a[24:27]
mm a[24:27],v[212:215],v[20:23],a[24:27]
mm a[28:31],v[220:223],v[20:23],a[28:31]
mm a[28:31],v[216:219],v[16:19],a[28:31]
mm a[44:47],v[216:219],v[24:27],a[44:47]
mm a[44:47],v[220:223],v[28:31],a[44:47]
mm a[40:43],v[212:215],v[28:31],a[40:43]
mm a[40:43],v[208:211],v[24:27],a[40:43]
mm a[252:255],v[200:203],v[24:27],a[252:255]
mm a[252:255],v[204:207],v[28:31],a[252:255]
mm a[248:251],v[196:199],v[28:31],a[248:251]
wt vmcnt(10) lgkmcnt(0)
s_barrier
mm a[248:251],v[192:195],v[24:27],a[248:251]
mm a[112:115],v[32:35],v[160:163],a[112:115]
mm a[112:115],v[36:39],v[164:167],a[112:115]
mm a[116:119],v[44:47],v[164:167],a[116:119]
rt v[192:193],v240 offset:0
rt v[194:195],v240 offset:1024
mm a[116:119],v[40:43],v[160:163],a[116:119]
rt v[196:197],v240 offset:16896
rt v[198:199],v240 offset:17920
mm a[128:131],v[48:51],v[160:163],a[128:131]
mm a[128:131],v[52:55],v[164:167],a[128:131]
mm a[132:135],v[60:63],v[164:167],a[132:135]
mm a[132:135],v[56:59],v[160:163],a[132:135]
rd v[160:163],v236 offset:24576
rd v[164:167],v237 offset:24576
mm a[148:151],v[56:59],v[168:171],a[148:151]
mm a[148:151],v[60:63],v[172:175],a[148:151]
mm a[144:147],v[52:55],v[172:175],a[144:147]
mm a[144:147],v[48:51],v[168:171],a[144:147]
rt v[200:201],v241 offset:0
rt v[202:203],v241 offset:1024
mm a[228:231],v[40:43],v[168:171],a[228:231]
mm a[228:231],v[44:47],v[172:175],a[228:231]
mm a[224:227],v[36:39],v[172:175],a[224:227]
mm a[224:227],v[32:35],v[168:171],a[224:227]
rd v[168:171],v236 offset:26624
rd v[172:175],v237 offset:26624
mm a[208:211],v[32:35],v[176:179],a[208:211]
mm a[208:211],v[36:39],v[180:183],a[208:211]
mm a[212:215],v[44:47],v[180:183],a[212:215]
mm a[212:215],v[40:43],v[176:179],a[212:215]
rt v[204:205],v241 offset:16896
rt v[206:207],v241 offset:17920
mm a[160:163],v[48:51],v[176:179],a[160:163]
mm a[160:163],v[52:55],v[180:183],a[160:163]
mm a[164:167],v[60:63],v[180:183],a[164:167]
mm a[164:167],v[56:59],v[176:179],a[164:167]
rd v[176:179],v236 offset:16384
rd v[180:183],v237 offset:16384
mm a[180:183],v[56:59],v[184:187],a[180:183]
mm a[180:183],v[60:63],v[188:191],a[180:183]
mm a[176:179],v[52:55],v[188:191],a[176:179]
mm a[176:179],v[48:51],v[184:187],a[176:179]
rt v[208:209],v242 offset:0
rt v[210:211],v242 offset:1024
mm a[196:199],v[40:43],v[184:187],a[196:199]
s_add_u32 m0, s83, 33792
mm a[196:199],v[44:47],v[188:191],a[196:199]
ld v232,s[44:47],s80 offen lds
mm a[192:195],v[36:39],v[188:191],a[192:195]
mm a[192:195],v[32:35],v[184:187],a[192:195]
rd v[184:187],v236 offset:18432
rd v[188:191],v237 offset:18432
mm a[64:67],v[32:35],v[0:3],a[64:67]
s_add_u32 m0, s83, 34816
mm a[64:67],v[36:39],v[4:7],a[64:67]
ld v233,s[44:47],s80 offen lds
mm a[68:71],v[44:47],v[4:7],a[68:71]
mm a[68:71],v[40:43],v[0:3],a[68:71]
rt v[212:213],v242 offset:16896
rt v[214:215],v242 offset:17920
mm a[0:3],v[48:51],v[0:3],a[0:3]
s_add_u32 m0, s83, 35968
mm a[0:3],v[52:55],v[4:7],a[0:3]
ld v234,s[44:47],s80 offen lds
mm a[4:7],v[60:63],v[4:7],a[4:7]
mm a[4:7],v[56:59],v[0:3],a[4:7]
rd v[0:3],v236 offset:8192
rd v[4:7],v237 offset:8192
mm a[20:23],v[56:59],v[8:11],a[20:23]
s_add_u32 m0, s83, 36992
mm a[20:23],v[60:63],v[12:15],a[20:23]
ld v235,s[44:47],s80 offen lds
mm a[16:19],v[52:55],v[12:15],a[16:19]
s_mov_b32 s80, 1835008
mm a[16:19],v[48:51],v[8:11],a[16:19]
rt v[216:217],v243 offset:0
rt v[218:219],v243 offset:1024
mm a[84:87],v[40:43],v[8:11],a[84:87]
s_add_u32 m0, s83, 50688
mm a[84:87],v[44:47],v[12:15],a[84:87]
ld v232,s[44:47],s80 offen lds
mm a[80:83],v[36:39],v[12:15],a[80:83]
mm a[80:83],v[32:35],v[8:11],a[80:83]
rd v[8:11],v236 offset:10240
rd v[12:15],v237 offset:10240
mm a[240:243],v[32:35],v[16:19],a[240:243]
s_add_u32 m0, s83, 51712
mm a[240:243],v[36:39],v[20:23],a[240:243]
ld v233,s[44:47],s80 offen lds
mm a[244:247],v[44:47],v[20:23],a[244:247]
mm a[244:247],v[40:43],v[16:19],a[244:247]
rt v[220:221],v243 offset:16896
rt v[222:223],v243 offset:17920
mm a[32:35],v[48:51],v[16:19],a[32:35]
s_add_u32 m0, s83, 52864
mm a[32:35],v[52:55],v[20:23],a[32:35]
ld v234,s[44:47],s80 offen lds
mm a[36:39],v[60:63],v[20:23],a[36:39]
mm a[36:39],v[56:59],v[16:19],a[36:39]
rd v[16:19],v236 offset:0
rd v[20:23],v237 offset:0
mm a[60:63],v[56:59],v[24:27],a[60:63]
s_add_u32 m0, s83, 53888
mm a[60:63],v[60:63],v[28:31],a[60:63]
ld v235,s[44:47],s80 offen lds
mm a[56:59],v[52:55],v[28:31],a[56:59]
s_mov_b64 s[44:45], s[68:69]
s_mov_b32 s46, s70
mm a[56:59],v[48:51],v[24:27],a[56:59]
mm a[52:55],v[40:43],v[24:27],a[52:55]
mm a[52:55],v[44:47],v[28:31],a[52:55]
mm a[48:51],v[36:39],v[28:31],a[48:51]
wt lgkmcnt(2)
s_lshl_b32 s80, s77, 18
mm a[48:51],v[32:35],v[24:27],a[48:51]
rd v[24:27],v236 offset:2048
rd v[28:31],v237 offset:2048
mm a[96:99],v[192:195],v[160:163],a[96:99]
mm a[96:99],v[196:199],v[164:167],a[96:99]
mm a[100:103],v[204:207],v[164:167],a[100:103]
mm a[100:103],v[200:203],v[160:163],a[100:103]
rt v[32:33],v240 offset:128
rt v[34:35],v240 offset:1152
mm a[104:107],v[208:211],v[160:163],a[104:107]
mm a[104:107],v[212:215],v[164:167],a[104:107]
mm a[108:111],v[220:223],v[164:167],a[108:111]
wt lgkmcnt(2)
s_barrier
mm a[108:111],v[216:219],v[160:163],a[108:111]
mm a[140:143],v[216:219],v[168:171],a[140:143]
mm a[140:143],v[220:223],v[172:175],a[140:143]
mm a[136:139],v[212:215],v[172:175],a[136:139]
mm a[136:139],v[208:211],v[168:171],a[136:139]
rt v[36:37],v240 offset:17024
rt v[38:39],v240 offset:18048
mm a[124:127],v[200:203],v[168:171],a[124:127]
mm a[124:127],v[204:207],v[172:175],a[124:127]
mm a[120:123],v[196:199],v[172:175],a[120:123]
mm a[120:123],v[192:195],v[168:171],a[120:123]
mm a[232:235],v[192:195],v[176:179],a[232:235]
mm a[232:235],v[196:199],v[180:183],a[232:235]
mm a[236:239],v[204:207],v[180:183],a[236:239]
mm a[236:239],v[200:203],v[176:179],a[236:239]
rt v[40:41],v241 offset:128
rt v[42:43],v241 offset:1152
mm a[152:155],v[208:211],v[176:179],a[152:155]
mm a[152:155],v[212:215],v[180:183],a[152:155]
mm a[156:159],v[220:223],v[180:183],a[156:159]
mm a[156:159],v[216:219],v[176:179],a[156:159]
mm a[172:175],v[216:219],v[184:187],a[172:175]
mm a[172:175],v[220:223],v[188:191],a[172:175]
mm a[168:171],v[212:215],v[188:191],a[168:171]
mm a[168:171],v[208:211],v[184:187],a[168:171]
rt v[44:45],v241 offset:17024
rt v[46:47],v241 offset:18048
mm a[220:223],v[200:203],v[184:187],a[220:223]
mm a[220:223],v[204:207],v[188:191],a[220:223]
mm a[216:219],v[196:199],v[188:191],a[216:219]
mm a[216:219],v[192:195],v[184:187],a[216:219]
mm a[200:203],v[192:195],v[0:3],a[200:203]
mm a[200:203],v[196:199],v[4:7],a[200:203]
mm a[204:207],v[204:207],v[4:7],a[204:207]
mm a[204:207],v[200:203],v[0:3],a[204:207]
rt v[48:49],v242 offset:128
rt v[50:51],v242 offset:1152
mm a[184:187],v[208:211],v[0:3],a[184:187]
mm a[184:187],v[212:215],v[4:7],a[184:187]
mm a[188:191],v[220:223],v[4:7],a[188:191]
mm a[188:191],v[216:219],v[0:3],a[188:191]
mm a[12:15],v[216:219],v[8:11],a[12:15]
mm a[12:15],v[220:223],v[12:15],a[12:15]
buffer_load_dword v254, v135, s[68:71], s96 offen
mm a[8:11],v[212:215],v[12:15],a[8:11]
s_add_u32 s96, s96, 1048576
mm a[8:11],v[208:211],v[8:11],a[8:11]
rt v[52:53],v242 offset:17024
rt v[54:55],v242 offset:18048
mm a[76:79],v[200:203],v[8:11],a[76:79]
mm a[76:79],v[204:207],v[12:15],a[76:79]
buffer_load_dword v254, v135, s[68:71], s96 offen
mm a[72:75],v[196:199],v[12:15],a[72:75]
s_add_u32 s96, s96, 1048576
mm a[72:75],v[192:195],v[8:11],a[72:75]
rt v[56:57],v243 offset:128
rt v[58:59],v243 offset:1152
mm a[88:91],v[192:195],v[16:19],a[88:91]
mm a[88:91],v[196:199],v[20:23],a[88:91]
mm a[92:95],v[204:207],v[20:23],a[92:95]
mm a[92:95],v[200:203],v[16:19],a[92:95]
rt v[60:61],v243 offset:17024
rt v[62:63],v243 offset:18048
mm a[24:27],v[208:211],v[16:19],a[24:27]
mm a[24:27],v[212:215],v[20:23],a[24:27]
mm a[28:31],v[220:223],v[20:23],a[28:31]
mm a[28:31],v[216:219],v[16:19],a[28:31]
mm a[44:47],v[216:219],v[24:27],a[44:47]
mm a[44:47],v[220:223],v[28:31],a[44:47]
ld v[72:75],v255,s[64:67],s89 offen offset:1024 sc1
mm a[40:43],v[212:215],v[28:31],a[40:43]
mm a[40:43],v[208:211],v[24:27],a[40:43]
mm a[252:255],v[200:203],v[24:27],a[252:255]
mm a[252:255],v[204:207],v[28:31],a[252:255]
mm a[248:251],v[196:199],v[28:31],a[248:251]
wt vmcnt(3) lgkmcnt(0)
s_barrier
mm a[248:251],v[192:195],v[24:27],a[248:251]
mm a[112:115],v[32:35],v[160:163],a[112:115]
mm a[112:115],v[36:39],v[164:167],a[112:115]
mm a[116:119],v[44:47],v[164:167],a[116:119]
rt v[192:193],v240 offset:33792
rt v[194:195],v240 offset:34816
mm a[116:119],v[40:43],v[160:163],a[116:119]
rt v[196:197],v240 offset:50688
rt v[198:199],v240 offset:51712
mm a[128:131],v[48:51],v[160:163],a[128:131]
mm a[128:131],v[52:55],v[164:167],a[128:131]
mm a[132:135],v[60:63],v[164:167],a[132:135]
mm a[132:135],v[56:59],v[160:163],a[132:135]
rd v[160:163],v236 offset:57344
rd v[164:167],v237 offset:57344
mm a[148:151],v[56:59],v[168:171],a[148:151]
mm a[148:151],v[60:63],v[172:175],a[148:151]
mm a[144:147],v[52:55],v[172:175],a[144:147]
mm a[144:147],v[48:51],v[168:171],a[144:147]
rt v[200:201],v241 offset:33792
rt v[202:203],v241 offset:34816
mm a[228:231],v[40:43],v[168:171],a[228:231]
mm a[228:231],v[44:47],v[172:175],a[228:231]
mm a[224:227],v[36:39],v[172:175],a[224:227]
mm a[224:227],v[32:35],v[168:171],a[224:227]
rd v[168:171],v236 offset:59392
rd v[172:175],v237 offset:59392
mm a[208:211],v[32:35],v[176:179],a[208:211]
mm a[208:211],v[36:39],v[180:183],a[208:211]
mm a[212:215],v[44:47],v[180:183],a[212:215]
mm a[212:215],v[40:43],v[176:179],a[212:215]
rt v[204:205],v241 offset:50688
rt v[206:207],v241 offset:51712
mm a[160:163],v[48:51],v[176:179],a[160:163]
mm a[160:163],v[52:55],v[180:183],a[160:163]
mm a[164:167],v[60:63],v[180:183],a[164:167]
mm a[164:167],v[56:59],v[176:179],a[164:167]
rd v[176:179],v236 offset:49152
rd v[180:183],v237 offset:49152
mm a[180:183],v[56:59],v[184:187],a[180:183]
mm a[180:183],v[60:63],v[188:191],a[180:183]
mm a[176:179],v[52:55],v[188:191],a[176:179]
mm a[176:179],v[48:51],v[184:187],a[176:179]
rt v[208:209],v242 offset:33792
rt v[210:211],v242 offset:34816
mm a[196:199],v[40:43],v[184:187],a[196:199]
s_add_u32 m0, s83, 0
mm a[196:199],v[44:47],v[188:191],a[196:199]
ld v232,s[44:47],s80 offen lds
mm a[192:195],v[36:39],v[188:191],a[192:195]
mm a[192:195],v[32:35],v[184:187],a[192:195]
rd v[184:187],v236 offset:51200
rd v[188:191],v237 offset:51200
mm a[64:67],v[32:35],v[0:3],a[64:67]
s_add_u32 m0, s83, 1024
mm a[64:67],v[36:39],v[4:7],a[64:67]
ld v233,s[44:47],s80 offen lds
mm a[68:71],v[44:47],v[4:7],a[68:71]
mm a[68:71],v[40:43],v[0:3],a[68:71]
rt v[212:213],v242 offset:50688
rt v[214:215],v242 offset:51712
mm a[0:3],v[48:51],v[0:3],a[0:3]
s_add_u32 m0, s83, 2176
mm a[0:3],v[52:55],v[4:7],a[0:3]
ld v234,s[44:47],s80 offen lds
mm a[4:7],v[60:63],v[4:7],a[4:7]
mm a[4:7],v[56:59],v[0:3],a[4:7]
rd v[0:3],v236 offset:40960
rd v[4:7],v237 offset:40960
mm a[20:23],v[56:59],v[8:11],a[20:23]
s_add_u32 m0, s83, 3200
mm a[20:23],v[60:63],v[12:15],a[20:23]
ld v235,s[44:47],s80 offen lds
mm a[16:19],v[52:55],v[12:15],a[16:19]
s_add_u32 s80, s80, 524288
mm a[16:19],v[48:51],v[8:11],a[16:19]
rt v[216:217],v243 offset:33792
rt v[218:219],v243 offset:34816
mm a[84:87],v[40:43],v[8:11],a[84:87]
s_add_u32 m0, s83, 16896
mm a[84:87],v[44:47],v[12:15],a[84:87]
ld v232,s[44:47],s80 offen lds
mm a[80:83],v[36:39],v[12:15],a[80:83]
mm a[80:83],v[32:35],v[8:11],a[80:83]
rd v[8:11],v236 offset:43008
rd v[12:15],v237 offset:43008
mm a[240:243],v[32:35],v[16:19],a[240:243]
s_add_u32 m0, s83, 17920
mm a[240:243],v[36:39],v[20:23],a[240:243]
ld v233,s[44:47],s80 offen lds
mm a[244:247],v[44:47],v[20:23],a[244:247]
mm a[244:247],v[40:43],v[16:19],a[244:247]
rt v[220:221],v243 offset:50688
rt v[222:223],v243 offset:51712
mm a[32:35],v[48:51],v[16:19],a[32:35]
s_add_u32 m0, s83, 19072
mm a[32:35],v[52:55],v[20:23],a[32:35]
ld v234,s[44:47],s80 offen lds
mm a[36:39],v[60:63],v[20:23],a[36:39]
mm a[36:39],v[56:59],v[16:19],a[36:39]
rd v[16:19],v236 offset:32768
rd v[20:23],v237 offset:32768
mm a[60:63],v[56:59],v[24:27],a[60:63]
s_add_u32 m0, s83, 20096
mm a[60:63],v[60:63],v[28:31],a[60:63]
ld v235,s[44:47],s80 offen lds
mm a[56:59],v[52:55],v[28:31],a[56:59]
s_add_u32 s80, s80, 524288
mm a[56:59],v[48:51],v[24:27],a[56:59]
mm a[52:55],v[40:43],v[24:27],a[52:55]
mm a[52:55],v[44:47],v[28:31],a[52:55]
mm a[48:51],v[36:39],v[28:31],a[48:51]
wt lgkmcnt(2)
mm a[48:51],v[32:35],v[24:27],a[48:51]
rd v[24:27],v236 offset:34816
rd v[28:31],v237 offset:34816
mm a[96:99],v[192:195],v[160:163],a[96:99]
mm a[96:99],v[196:199],v[164:167],a[96:99]
mm a[100:103],v[204:207],v[164:167],a[100:103]
mm a[100:103],v[200:203],v[160:163],a[100:103]
rt v[32:33],v240 offset:33920
rt v[34:35],v240 offset:34944
mm a[104:107],v[208:211],v[160:163],a[104:107]
mm a[104:107],v[212:215],v[164:167],a[104:107]
mm a[108:111],v[220:223],v[164:167],a[108:111]
wt lgkmcnt(2)
s_barrier
mm a[108:111],v[216:219],v[160:163],a[108:111]
mm a[140:143],v[216:219],v[168:171],a[140:143]
wt vmcnt(8)
v_cmp_eq_u64 vcc, v[72:73], v[252:253]
mm a[140:143],v[220:223],v[172:175],a[140:143]
v_cmp_eq_u64 s[96:97], v[74:75], v[252:253]
mm a[136:139],v[212:215],v[172:175],a[136:139]
s_and_b64 vcc, vcc, s[96:97]
s_cmp_eq_u64 vcc, -1
s_cbranch_scc0 .La5_LK_XSPIN
.La5_LK_XOK:
mm a[136:139],v[208:211],v[168:171],a[136:139]
rt v[36:37],v240 offset:50816
rt v[38:39],v240 offset:51840
mm a[124:127],v[200:203],v[168:171],a[124:127]
s_mov_b32 s89, 49152
mm a[124:127],v[204:207],v[172:175],a[124:127]
mm a[120:123],v[196:199],v[172:175],a[120:123]
ar v224,a96
ar v225,a97
mm a[120:123],v[192:195],v[168:171],a[120:123]
ar v226,a98
ar v227,a99
mm a[232:235],v[192:195],v[176:179],a[232:235]
ar v228,a100
ar v229,a101
mm a[232:235],v[196:199],v[180:183],a[232:235]
ld v[128:131],v244,s[52:55],s91 offen offset:0 sc1
mm a[236:239],v[204:207],v[180:183],a[236:239]
ar v230,a102
ar v231,a103
mm a[236:239],v[200:203],v[176:179],a[236:239]
rt v[40:41],v241 offset:33920
rt v[42:43],v241 offset:34944
mm a[152:155],v[208:211],v[176:179],a[152:155]
cv v248,v224,v225
cv v249,v226,v227
mm a[152:155],v[212:215],v[180:183],a[152:155]
ld v[132:135],v245,s[52:55],s91 offen offset:0 sc1
mm a[156:159],v[220:223],v[180:183],a[156:159]
cv v250,v228,v229
cv v251,v230,v231
mm a[156:159],v[216:219],v[176:179],a[156:159]
st v[248:251],v255,s[48:51],s89 offen sc1
mm a[172:175],v[216:219],v[184:187],a[172:175]
s_mov_b32 s89, 50176
ds_write_b128 v238, v[248:251] offset:0
mm a[172:175],v[220:223],v[188:191],a[172:175]
ld v[136:139],v246,s[52:55],s91 offen offset:0 sc1
mm a[168:171],v[212:215],v[188:191],a[168:171]
ar v224,a104
ar v225,a105
mm a[168:171],v[208:211],v[184:187],a[168:171]
rt v[44:45],v241 offset:50816
rt v[46:47],v241 offset:51840
mm a[220:223],v[200:203],v[184:187],a[220:223]
ar v226,a106
ar v227,a107
mm a[220:223],v[204:207],v[188:191],a[220:223]
ld v[140:143],v247,s[52:55],s91 offen offset:0 sc1
mm a[216:219],v[196:199],v[188:191],a[216:219]
ar v228,a108
ar v229,a109
mm a[216:219],v[192:195],v[184:187],a[216:219]
ar v230,a110
ar v231,a111
mm a[200:203],v[192:195],v[0:3],a[200:203]
cv v248,v224,v225
cv v249,v226,v227
.La5_LW1_3:
s_cbranch_execz .La5_LW1_7
mm a[200:203],v[196:199],v[4:7],a[200:203]
ld v[144:147],v244,s[52:55],s91 offen offset:1024 sc1
mm a[204:207],v[204:207],v[4:7],a[204:207]
cv v250,v228,v229
cv v251,v230,v231
mm a[204:207],v[200:203],v[0:3],a[204:207]
rt v[48:49],v242 offset:33920
rt v[50:51],v242 offset:34944
mm a[184:187],v[208:211],v[0:3],a[184:187]
st v[248:251],v255,s[48:51],s89 offen sc1
mm a[184:187],v[212:215],v[4:7],a[184:187]
ld v[148:151],v245,s[52:55],s91 offen offset:1024 sc1
mm a[188:191],v[220:223],v[4:7],a[188:191]
s_mov_b32 s89, 51200
ds_write_b128 v238, v[248:251] offset:1024
mm a[188:191],v[216:219],v[0:3],a[188:191]
mm a[12:15],v[216:219],v[8:11],a[12:15]
mm a[12:15],v[220:223],v[12:15],a[12:15]
ld v[152:155],v246,s[52:55],s91 offen offset:1024 sc1
mm a[8:11],v[212:215],v[12:15],a[8:11]
mm a[8:11],v[208:211],v[8:11],a[8:11]
rt v[52:53],v242 offset:50816
rt v[54:55],v242 offset:51840
mm a[76:79],v[200:203],v[8:11],a[76:79]
mm a[76:79],v[204:207],v[12:15],a[76:79]
ld v[156:159],v247,s[52:55],s91 offen offset:1024 sc1
mm a[72:75],v[196:199],v[12:15],a[72:75]
mm a[72:75],v[192:195],v[8:11],a[72:75]
rt v[56:57],v243 offset:33920
rt v[58:59],v243 offset:34944
mm a[88:91],v[192:195],v[16:19],a[88:91]
mm a[88:91],v[196:199],v[20:23],a[88:91]
mm a[92:95],v[204:207],v[20:23],a[92:95]
mm a[92:95],v[200:203],v[16:19],a[92:95]
rt v[60:61],v243 offset:50816
rt v[62:63],v243 offset:51840
mm a[24:27],v[208:211],v[16:19],a[24:27]
mm a[24:27],v[212:215],v[20:23],a[24:27]
mm a[28:31],v[220:223],v[20:23],a[28:31]
mm a[28:31],v[216:219],v[16:19],a[28:31]
mm a[44:47],v[216:219],v[24:27],a[44:47]
mm a[44:47],v[220:223],v[28:31],a[44:47]
mm a[40:43],v[212:215],v[28:31],a[40:43]
mm a[40:43],v[208:211],v[24:27],a[40:43]
mm a[252:255],v[200:203],v[24:27],a[252:255]
mm a[252:255],v[204:207],v[28:31],a[252:255]
mm a[248:251],v[196:199],v[28:31],a[248:251]
wt vmcnt(10) lgkmcnt(0)
s_barrier
mm a[248:251],v[192:195],v[24:27],a[248:251]
mm a[112:115],v[32:35],v[160:163],a[112:115]
s_add_u32 m0, s83, 33792
mm a[112:115],v[36:39],v[164:167],a[112:115]
ld v232,s[44:47],s80 offen lds
mm a[116:119],v[44:47],v[164:167],a[116:119]
mm a[116:119],v[40:43],v[160:163],a[116:119]
rt v[192:193],v240 offset:0
rt v[194:195],v240 offset:1024
mm a[128:131],v[48:51],v[160:163],a[128:131]
s_add_u32 m0, s83, 34816
mm a[128:131],v[52:55],v[164:167],a[128:131]
ld v233,s[44:47],s80 offen lds
mm a[132:135],v[60:63],v[164:167],a[132:135]
mm a[132:135],v[56:59],v[160:163],a[132:135]
rt v[196:197],v240 offset:8448
rt v[198:199],v240 offset:9472
mm a[148:151],v[56:59],v[168:171],a[148:151]
s_add_u32 m0, s83, 35968
mm a[148:151],v[60:63],v[172:175],a[148:151]
ld v234,s[44:47],s80 offen lds
mm a[144:147],v[52:55],v[172:175],a[144:147]
ar v224,a112
ar v225,a113
mm a[144:147],v[48:51],v[168:171],a[144:147]
rt v[200:201],v241 offset:0
rt v[202:203],v241 offset:1024
mm a[228:231],v[40:43],v[168:171],a[228:231]
s_add_u32 m0, s83, 36992
ar v226,a114
mm a[228:231],v[44:47],v[172:175],a[228:231]
ld v235,s[44:47],s80 offen lds
mm a[224:227],v[36:39],v[172:175],a[224:227]
s_add_u32 s80, s80, 524288
ar v227,a115
mm a[224:227],v[32:35],v[168:171],a[224:227]
rt v[204:205],v241 offset:8448
rt v[206:207],v241 offset:9472
mm a[208:211],v[32:35],v[176:179],a[208:211]
s_add_u32 m0, s83, 50688
ar v228,a116
mm a[208:211],v[36:39],v[180:183],a[208:211]
ld v232,s[44:47],s80 offen lds
mm a[212:215],v[44:47],v[180:183],a[212:215]
ar v229,a117
ar v230,a118
mm a[212:215],v[40:43],v[176:179],a[212:215]
rt v[208:209],v242 offset:0
rt v[210:211],v242 offset:1024
mm a[160:163],v[48:51],v[176:179],a[160:163]
s_add_u32 m0, s83, 51712
ar v231,a119
mm a[160:163],v[52:55],v[180:183],a[160:163]
ld v233,s[44:47],s80 offen lds
mm a[164:167],v[60:63],v[180:183],a[164:167]
cv v248,v224,v225
cv v249,v226,v227
mm a[164:167],v[56:59],v[176:179],a[164:167]
rt v[212:213],v242 offset:8448
rt v[214:215],v242 offset:9472
mm a[180:183],v[56:59],v[184:187],a[180:183]
s_add_u32 m0, s83, 52864
cv v250,v228,v229
mm a[180:183],v[60:63],v[188:191],a[180:183]
ld v234,s[44:47],s80 offen lds
mm a[176:179],v[52:55],v[188:191],a[176:179]
cv v251,v230,v231
st v[248:251],v255,s[48:51],s89 offen sc1
mm a[176:179],v[48:51],v[184:187],a[176:179]
rt v[216:217],v243 offset:0
rt v[218:219],v243 offset:1024
mm a[196:199],v[40:43],v[184:187],a[196:199]
s_add_u32 m0, s83, 53888
s_mov_b32 s89, 52224
mm a[196:199],v[44:47],v[188:191],a[196:199]
ld v235,s[44:47],s80 offen lds
mm a[192:195],v[36:39],v[188:191],a[192:195]
s_add_u32 s80, s80, 524288
ds_write_b128 v238, v[248:251] offset:2048
mm a[192:195],v[32:35],v[184:187],a[192:195]
rt v[220:221],v243 offset:8448
rt v[222:223],v243 offset:9472
mm a[64:67],v[32:35],v[0:3],a[64:67]
mm a[64:67],v[36:39],v[4:7],a[64:67]
ld v[160:163],v244,s[52:55],s91 offen offset:2048 sc1
mm a[68:71],v[44:47],v[4:7],a[68:71]
mm a[68:71],v[40:43],v[0:3],a[68:71]
mm a[0:3],v[48:51],v[0:3],a[0:3]
mm a[0:3],v[52:55],v[4:7],a[0:3]
ld v[164:167],v245,s[52:55],s91 offen offset:2048 sc1
mm a[4:7],v[60:63],v[4:7],a[4:7]
mm a[4:7],v[56:59],v[0:3],a[4:7]
mm a[20:23],v[56:59],v[8:11],a[20:23]
mm a[20:23],v[60:63],v[12:15],a[20:23]
ld v[168:171],v246,s[52:55],s91 offen offset:2048 sc1
mm a[16:19],v[52:55],v[12:15],a[16:19]
mm a[16:19],v[48:51],v[8:11],a[16:19]
mm a[84:87],v[40:43],v[8:11],a[84:87]
mm a[84:87],v[44:47],v[12:15],a[84:87]
ld v[172:175],v247,s[52:55],s91 offen offset:2048 sc1
mm a[80:83],v[36:39],v[12:15],a[80:83]
mm a[80:83],v[32:35],v[8:11],a[80:83]
mm a[240:243],v[32:35],v[16:19],a[240:243]
mm a[240:243],v[36:39],v[20:23],a[240:243]
mm a[244:247],v[44:47],v[20:23],a[244:247]
mm a[244:247],v[40:43],v[16:19],a[244:247]
mm a[32:35],v[48:51],v[16:19],a[32:35]
mm a[32:35],v[52:55],v[20:23],a[32:35]
mm a[36:39],v[60:63],v[20:23],a[36:39]
mm a[36:39],v[56:59],v[16:19],a[36:39]
mm a[60:63],v[56:59],v[24:27],a[60:63]
mm a[60:63],v[60:63],v[28:31],a[60:63]
mm a[56:59],v[52:55],v[28:31],a[56:59]
mm a[56:59],v[48:51],v[24:27],a[56:59]
mm a[52:55],v[40:43],v[24:27],a[52:55]
mm a[52:55],v[44:47],v[28:31],a[52:55]
mm a[48:51],v[36:39],v[28:31],a[48:51]
mm a[48:51],v[32:35],v[24:27],a[48:51]
s_branch .La5_LQ_START
.La5_LV_XSPIN:
s_mov_b32 s93, 0
.La5_LV_XSPIN_L:
s_add_u32 s93, s93, 1
s_cmp_gt_u32 s93, 2097152
s_cbranch_scc1 .La5_LV_XOK
s_sleep 8
ld v[72:75],v255,s[64:67],s89 offen offset:1024 sc1
wt vmcnt(0)
v_cmp_eq_u64 vcc, v[72:73], v[252:253]
v_cmp_eq_u64 s[96:97], v[74:75], v[252:253]
s_nop 1
s_and_b64 vcc, vcc, s[96:97]
s_cmp_eq_u64 vcc, -1
s_nop 0
s_nop 0
s_cbranch_scc0 .La5_LV_XSPIN_L
s_branch .La5_LV_XOK
.La5_LK_XSPIN:
s_mov_b32 s93, 0
.La5_LK_XSPIN_L:
s_add_u32 s93, s93, 1
s_cmp_gt_u32 s93, 2097152
s_nop 0
s_nop 0
s_cbranch_scc1 .La5_LK_XOK
s_sleep 8
ld v[72:75],v255,s[64:67],s89 offen offset:1024 sc1
wt vmcnt(0)
v_cmp_eq_u64 vcc, v[72:73], v[252:253]
v_cmp_eq_u64 s[96:97], v[74:75], v[252:253]
s_nop 1
s_and_b64 vcc, vcc, s[96:97]
s_cmp_eq_u64 vcc, -1
s_cbranch_scc0 .La5_LK_XSPIN_L
s_branch .La5_LK_XOK
; ---- V tile (waves 1, 3): the same with the operands exchanged (V comes out transposed)
.La5_LV_START:
.La5_LW1_4:
s_cbranch_execz .La5_LW1_8
mm a[96:99],v[160:163],v[192:195],0
s_add_u32 m0, s83, 33792
mm a[96:99],v[164:167],v[196:199],a[96:99]
ld v232,s[44:47],s80 offen lds
mm a[104:107],v[164:167],v[204:207],0
rd v[0:3],v236 offset:8192
rd v[4:7],v237 offset:8192
s_add_u32 m0, s83, 34816
mm a[104:107],v[160:163],v[200:203],a[104:107]
rd v[8:11],v236 offset:10240
rd v[12:15],v237 offset:10240
ld v233,s[44:47],s80 offen lds
mm a[112:115],v[160:163],v[208:211],0
s_add_u32 m0, s83, 35968
mm a[112:115],v[164:167],v[212:215],a[112:115]
ld v234,s[44:47],s80 offen lds
mm a[128:131],v[164:167],v[220:223],0
rd v[16:19],v236 offset:0
rd v[20:23],v237 offset:0
s_add_u32 m0, s83, 36992
mm a[128:131],v[160:163],v[216:219],a[128:131]
rd v[24:27],v236 offset:2048
rd v[28:31],v237 offset:2048
ld v235,s[44:47],s80 offen lds
mm a[132:135],v[168:171],v[216:219],0
s_mov_b32 s80, 2883584
mm a[132:135],v[172:175],v[220:223],a[132:135]
mm a[116:119],v[172:175],v[212:215],0
s_xor_b32 m0, s82, 32768
mm a[116:119],v[168:171],v[208:211],a[116:119]
rt v[32:33],v240 offset:128
rt v[34:35],v240 offset:1152
ld v224,s[40:43],s79 offen lds
mm a[108:111],v[168:171],v[200:203],0
s_xor_b32 m0, s82, 33792
mm a[108:111],v[172:175],v[204:207],a[108:111]
ld v225,s[40:43],s79 offen lds
mm a[100:103],v[172:175],v[196:199],0
s_xor_b32 m0, s82, 34816
mm a[100:103],v[168:171],v[192:195],a[100:103]
rt v[36:37],v240 offset:17024
rt v[38:39],v240 offset:18048
ld v226,s[40:43],s79 offen lds
mm a[232:235],v[176:179],v[192:195],0
s_xor_b32 m0, s82, 35840
mm a[232:235],v[180:183],v[196:199],a[232:235]
ld v227,s[40:43],s79 offen lds
mm a[152:155],v[180:183],v[204:207],0
s_xor_b32 m0, s82, 36864
mm a[152:155],v[176:179],v[200:203],a[152:155]
rt v[40:41],v241 offset:128
rt v[42:43],v241 offset:1152
ld v228,s[40:43],s79 offen lds
mm a[208:211],v[176:179],v[208:211],0
s_xor_b32 m0, s82, 37888
mm a[208:211],v[180:183],v[212:215],a[208:211]
ld v229,s[40:43],s79 offen lds
mm a[160:163],v[180:183],v[220:223],0
s_xor_b32 m0, s82, 38912
mm a[160:163],v[176:179],v[216:219],a[160:163]
ld v230,s[40:43],s79 offen lds
mm a[164:167],v[184:187],v[216:219],0
s_xor_b32 m0, s82, 39936
mm a[164:167],v[188:191],v[220:223],a[164:167]
ld v231,s[40:43],s79 offen lds
mm a[212:215],v[188:191],v[212:215],0
s_mov_b32 s79, 768
mm a[212:215],v[184:187],v[208:211],a[212:215]
rt v[44:45],v241 offset:17024
rt v[46:47],v241 offset:18048
mm a[156:159],v[184:187],v[200:203],0
s_add_u32 m0, s83, 50688
mm a[156:159],v[188:191],v[204:207],a[156:159]
ld v232,s[44:47],s80 offen lds
mm a[236:239],v[188:191],v[196:199],0
wt lgkmcnt(8)
s_add_u32 m0, s83, 51712
mm a[236:239],v[184:187],v[192:195],a[236:239]
ld v233,s[44:47],s80 offen lds
mm a[200:203],v[0:3],v[192:195],0
s_add_u32 m0, s83, 52864
mm a[200:203],v[4:7],v[196:199],a[200:203]
ld v234,s[44:47],s80 offen lds
mm a[184:187],v[4:7],v[204:207],0
s_add_u32 m0, s83, 53888
mm a[184:187],v[0:3],v[200:203],a[184:187]
rt v[48:49],v242 offset:128
rt v[50:51],v242 offset:1152
ld v235,s[44:47],s80 offen lds
mm a[64:67],v[0:3],v[208:211],0
s_mov_b32 s80, 3145728
mm a[64:67],v[4:7],v[212:215],a[64:67]
mm a[0:3],v[4:7],v[220:223],0
mm a[0:3],v[0:3],v[216:219],a[0:3]
mm a[4:7],v[8:11],v[216:219],0
mm a[4:7],v[12:15],v[220:223],a[4:7]
buffer_load_dword v70, v118, s[60:63], 0 offen
mm a[68:71],v[12:15],v[212:215],0
mm a[68:71],v[8:11],v[208:211],a[68:71]
rt v[52:53],v242 offset:17024
rt v[54:55],v242 offset:18048
mm a[188:191],v[8:11],v[200:203],0
mm a[188:191],v[12:15],v[204:207],a[188:191]
ld v[72:75],v119,s[60:63],0 offen offset:0
mm a[204:207],v[12:15],v[196:199],0
mm a[204:207],v[8:11],v[192:195],a[204:207]
rt v[56:57],v243 offset:128
rt v[58:59],v243 offset:1152
mm a[88:91],v[16:19],v[192:195],0
mm a[88:91],v[20:23],v[196:199],a[88:91]
ld v[76:79],v119,s[60:63],0 offen offset:128
mm a[24:27],v[20:23],v[204:207],0
mm a[24:27],v[16:19],v[200:203],a[24:27]
rt v[60:61],v243 offset:17024
rt v[62:63],v243 offset:18048
mm a[240:243],v[16:19],v[208:211],0
mm a[240:243],v[20:23],v[212:215],a[240:243]
ld v[80:83],v119,s[60:63],0 offen offset:256
mm a[32:35],v[20:23],v[220:223],0
mm a[32:35],v[16:19],v[216:219],a[32:35]
mm a[36:39],v[24:27],v[216:219],0
mm a[36:39],v[28:31],v[220:223],a[36:39]
ld v[84:87],v119,s[60:63],0 offen offset:384
mm a[244:247],v[28:31],v[212:215],0
mm a[244:247],v[24:27],v[208:211],a[244:247]
mm a[28:31],v[24:27],v[200:203],0
mm a[28:31],v[28:31],v[204:207],a[28:31]
mm a[92:95],v[28:31],v[196:199],0
mm a[92:95],v[24:27],v[192:195],a[92:95]
s_mov_b64 exec, 0
s_cmp_eq_u32 s3, 1
s_cbranch_scc1 .La5_LW2_1
s_branch .La5_LW2_R1
.La5_LW2_R1:
s_mov_b64 exec, -1
wt vmcnt(5) lgkmcnt(0)
s_barrier
mm a[120:123],v[160:163],v[32:35],0
mm a[120:123],v[164:167],v[36:39],a[120:123]
mm a[136:139],v[164:167],v[44:47],0
rt v[192:193],v240 offset:33792
rt v[194:195],v240 offset:34816
mm a[136:139],v[160:163],v[40:43],a[136:139]
rt v[196:197],v240 offset:50688
rt v[198:199],v240 offset:51712
mm a[224:227],v[160:163],v[48:51],0
mm a[224:227],v[164:167],v[52:55],a[224:227]
mm a[144:147],v[164:167],v[60:63],0
mm a[144:147],v[160:163],v[56:59],a[144:147]
rd v[160:163],v236 offset:57344
rd v[164:167],v237 offset:57344
mm a[148:151],v[168:171],v[56:59],0
mm a[148:151],v[172:175],v[60:63],a[148:151]
mm a[228:231],v[172:175],v[52:55],0
mm a[228:231],v[168:171],v[48:51],a[228:231]
rt v[200:201],v241 offset:33792
rt v[202:203],v241 offset:34816
mm a[140:143],v[168:171],v[40:43],0
mm a[140:143],v[172:175],v[44:47],a[140:143]
mm a[124:127],v[172:175],v[36:39],0
mm a[124:127],v[168:171],v[32:35],a[124:127]
rd v[168:171],v236 offset:59392
rd v[172:175],v237 offset:59392
mm a[216:219],v[176:179],v[32:35],0
s_xor_b32 m0, s82, 0
mm a[216:219],v[180:183],v[36:39],a[216:219]
ld v224,s[40:43],s79 offen lds
mm a[168:171],v[180:183],v[44:47],0
mm a[168:171],v[176:179],v[40:43],a[168:171]
rt v[204:205],v241 offset:50688
rt v[206:207],v241 offset:51712
s_xor_b32 m0, s82, 1024
mm a[192:195],v[176:179],v[48:51],0
ld v225,s[40:43],s79 offen lds
mm a[192:195],v[180:183],v[52:55],a[192:195]
mm a[176:179],v[180:183],v[60:63],0
s_xor_b32 m0, s82, 2048
mm a[176:179],v[176:179],v[56:59],a[176:179]
rd v[176:179],v236 offset:49152
rd v[180:183],v237 offset:49152
ld v226,s[40:43],s79 offen lds
mm a[180:183],v[184:187],v[56:59],0
mm a[180:183],v[188:191],v[60:63],a[180:183]
s_xor_b32 m0, s82, 3072
mm a[196:199],v[188:191],v[52:55],0
ld v227,s[40:43],s79 offen lds
mm a[196:199],v[184:187],v[48:51],a[196:199]
rt v[208:209],v242 offset:33792
rt v[210:211],v242 offset:34816
mm a[172:175],v[184:187],v[40:43],0
s_xor_b32 m0, s82, 4096
mm a[172:175],v[188:191],v[44:47],a[172:175]
ld v228,s[40:43],s79 offen lds
mm a[220:223],v[188:191],v[36:39],0
mm a[220:223],v[184:187],v[32:35],a[220:223]
rd v[184:187],v236 offset:51200
rd v[188:191],v237 offset:51200
s_xor_b32 m0, s82, 5120
mm a[72:75],v[0:3],v[32:35],0
ld v229,s[40:43],s79 offen lds
mm a[72:75],v[4:7],v[36:39],a[72:75]
mm a[8:11],v[4:7],v[44:47],0
s_xor_b32 m0, s82, 6144
mm a[8:11],v[0:3],v[40:43],a[8:11]
rt v[212:213],v242 offset:50688
rt v[214:215],v242 offset:51712
ld v230,s[40:43],s79 offen lds
mm a[80:83],v[0:3],v[48:51],0
mm a[80:83],v[4:7],v[52:55],a[80:83]
s_xor_b32 m0, s82, 7168
mm a[16:19],v[4:7],v[60:63],0
ld v231,s[40:43],s79 offen lds
mm a[16:19],v[0:3],v[56:59],a[16:19]
rd v[0:3],v236 offset:40960
rd v[4:7],v237 offset:40960
s_mov_b32 s79, 896
mm a[20:23],v[8:11],v[56:59],0
mm a[20:23],v[12:15],v[60:63],a[20:23]
s_add_u32 m0, s83, 0
mm a[84:87],v[12:15],v[52:55],0
ld v232,s[44:47],s80 offen lds
mm a[84:87],v[8:11],v[48:51],a[84:87]
rt v[216:217],v243 offset:33792
rt v[218:219],v243 offset:34816
mm a[12:15],v[8:11],v[40:43],0
s_add_u32 m0, s83, 1024
mm a[12:15],v[12:15],v[44:47],a[12:15]
ld v233,s[44:47],s80 offen lds
mm a[76:79],v[12:15],v[36:39],0
wt vmcnt(10)
v_lshlrev_b32 v70, 16, v70
mm a[76:79],v[8:11],v[32:35],a[76:79]
rd v[8:11],v236 offset:43008
rd v[12:15],v237 offset:43008
s_add_u32 m0, s83, 2176
mm a[248:251],v[16:19],v[32:35],0
ld v234,s[44:47],s80 offen lds
mm a[248:251],v[20:23],v[36:39],a[248:251]
v_lshlrev_b32 v88, 16, v72
v_and_b32 v89, 0xffff0000, v72
mm a[40:43],v[20:23],v[44:47],0
s_add_u32 m0, s83, 3200
v_sub_f32 v88, v88, v70
mm a[40:43],v[16:19],v[40:43],a[40:43]
rt v[220:221],v243 offset:50688
rt v[222:223],v243 offset:51712
ld v235,s[44:47],s80 offen lds
mm a[48:51],v[16:19],v[48:51],0
s_mov_b32 s80, 3407872
v_sub_f32 v89, v89, v70
mm a[48:51],v[20:23],v[52:55],a[48:51]
v_add_f32 v120, v88, v89
v_mul_f32 v122, v88, v88
mm a[56:59],v[20:23],v[60:63],0
s_add_u32 m0, s83, 16896
v_mul_f32 v123, v89, v89
mm a[56:59],v[16:19],v[56:59],a[56:59]
rd v[16:19],v236 offset:32768
rd v[20:23],v237 offset:32768
ld v232,s[44:47],s80 offen lds
mm a[60:63],v[24:27],v[56:59],0
v_lshlrev_b32 v90, 16, v73
v_and_b32 v91, 0xffff0000, v73
mm a[60:63],v[28:31],v[60:63],a[60:63]
s_add_u32 m0, s83, 17920
v_sub_f32 v90, v90, v70
mm a[52:55],v[28:31],v[52:55],0
ld v233,s[44:47],s80 offen lds
mm a[52:55],v[24:27],v[48:51],a[52:55]
v_sub_f32 v91, v91, v70
v_add_f32 v124, v90, v91
mm a[44:47],v[24:27],v[40:43],0
s_add_u32 m0, s83, 19072
v_fmac_f32 v122, v90, v90
mm a[44:47],v[28:31],v[44:47],a[44:47]
ld v234,s[44:47],s80 offen lds
mm a[252:255],v[28:31],v[36:39],0
wt lgkmcnt(2)
v_add_f32 v120, v120, v124
mm a[252:255],v[24:27],v[32:35],a[252:255]
rd v[24:27],v236 offset:34816
rd v[28:31],v237 offset:34816
s_add_u32 m0, s83, 20096
mm a[96:99],v[160:163],v[192:195],a[96:99]
ld v235,s[44:47],s80 offen lds
mm a[96:99],v[164:167],v[196:199],a[96:99]
s_mov_b32 s80, 3670016
v_fmac_f32 v123, v91, v91
mm a[104:107],v[164:167],v[204:207],a[104:107]
v_lshlrev_b32 v92, 16, v74
v_and_b32 v93, 0xffff0000, v74
mm a[104:107],v[160:163],v[200:203],a[104:107]
rt v[32:33],v240 offset:33920
rt v[34:35],v240 offset:34944
mm a[112:115],v[160:163],v[208:211],a[112:115]
v_sub_f32 v92, v92, v70
v_sub_f32 v93, v93, v70
mm a[112:115],v[164:167],v[212:215],a[112:115]
v_add_f32 v124, v92, v93
v_fmac_f32 v122, v92, v92
mm a[128:131],v[164:167],v[220:223],a[128:131]
wt lgkmcnt(2)
s_barrier
mm a[128:131],v[160:163],v[216:219],a[128:131]
v_add_f32 v120, v120, v124
v_fmac_f32 v123, v93, v93
mm a[132:135],v[168:171],v[216:219],a[132:135]
s_xor_b32 m0, s82, 32768
v_lshlrev_b32 v94, 16, v75
mm a[132:135],v[172:175],v[220:223],a[132:135]
ld v224,s[40:43],s79 offen lds
mm a[116:119],v[172:175],v[212:215],a[116:119]
v_and_b32 v95, 0xffff0000, v75
v_sub_f32 v94, v94, v70
mm a[116:119],v[168:171],v[208:211],a[116:119]
rt v[36:37],v240 offset:50816
rt v[38:39],v240 offset:51840
mm a[108:111],v[168:171],v[200:203],a[108:111]
s_xor_b32 m0, s82, 33792
v_sub_f32 v95, v95, v70
mm a[108:111],v[172:175],v[204:207],a[108:111]
ld v225,s[40:43],s79 offen lds
mm a[100:103],v[172:175],v[196:199],a[100:103]
v_add_f32 v124, v94, v95
v_fmac_f32 v122, v94, v94
mm a[100:103],v[168:171],v[192:195],a[100:103]
v_add_f32 v120, v120, v124
v_fmac_f32 v123, v95, v95
mm a[232:235],v[176:179],v[192:195],a[232:235]
s_xor_b32 m0, s82, 34816
v_lshlrev_b32 v96, 16, v76
mm a[232:235],v[180:183],v[196:199],a[232:235]
ld v226,s[40:43],s79 offen lds
mm a[152:155],v[180:183],v[204:207],a[152:155]
v_and_b32 v97, 0xffff0000, v76
v_sub_f32 v96, v96, v70
mm a[152:155],v[176:179],v[200:203],a[152:155]
rt v[40:41],v241 offset:33920
rt v[42:43],v241 offset:34944
mm a[208:211],v[176:179],v[208:211],a[208:211]
s_xor_b32 m0, s82, 35840
v_sub_f32 v97, v97, v70
mm a[208:211],v[180:183],v[212:215],a[208:211]
ld v227,s[40:43],s79 offen lds
mm a[160:163],v[180:183],v[220:223],a[160:163]
v_add_f32 v124, v96, v97
v_fmac_f32 v122, v96, v96
mm a[160:163],v[176:179],v[216:219],a[160:163]
v_add_f32 v120, v120, v124
v_fmac_f32 v123, v97, v97
mm a[164:167],v[184:187],v[216:219],a[164:167]
.La5_LW1_5:
s_cbranch_execz .La5_LW1_9
s_xor_b32 m0, s82, 36864
v_lshlrev_b32 v98, 16, v77
mm a[164:167],v[188:191],v[220:223],a[164:167]
ld v228,s[40:43],s79 offen lds
mm a[212:215],v[188:191],v[212:215],a[212:215]
v_and_b32 v99, 0xffff0000, v77
v_sub_f32 v98, v98, v70
mm a[212:215],v[184:187],v[208:211],a[212:215]
rt v[44:45],v241 offset:50816
rt v[46:47],v241 offset:51840
mm a[156:159],v[184:187],v[200:203],a[156:159]
s_xor_b32 m0, s82, 37888
v_sub_f32 v99, v99, v70
mm a[156:159],v[188:191],v[204:207],a[156:159]
ld v229,s[40:43],s79 offen lds
mm a[236:239],v[188:191],v[196:199],a[236:239]
v_add_f32 v124, v98, v99
v_fmac_f32 v122, v98, v98
mm a[236:239],v[184:187],v[192:195],a[236:239]
v_add_f32 v120, v120, v124
v_fmac_f32 v123, v99, v99
mm a[200:203],v[0:3],v[192:195],a[200:203]
s_xor_b32 m0, s82, 38912
v_lshlrev_b32 v100, 16, v78
mm a[200:203],v[4:7],v[196:199],a[200:203]
ld v230,s[40:43],s79 offen lds
mm a[184:187],v[4:7],v[204:207],a[184:187]
v_and_b32 v101, 0xffff0000, v78
v_sub_f32 v100, v100, v70
mm a[184:187],v[0:3],v[200:203],a[184:187]
rt v[48:49],v242 offset:33920
rt v[50:51],v242 offset:34944
mm a[64:67],v[0:3],v[208:211],a[64:67]
s_xor_b32 m0, s82, 39936
v_sub_f32 v101, v101, v70
mm a[64:67],v[4:7],v[212:215],a[64:67]
ld v231,s[40:43],s79 offen lds
mm a[0:3],v[4:7],v[220:223],a[0:3]
s_mov_b32 s79, 0
v_add_f32 v124, v100, v101
mm a[0:3],v[0:3],v[216:219],a[0:3]
v_fmac_f32 v122, v100, v100
v_add_f32 v120, v120, v124
mm a[4:7],v[8:11],v[216:219],a[4:7]
v_fmac_f32 v123, v101, v101
v_lshlrev_b32 v102, 16, v79
mm a[4:7],v[12:15],v[220:223],a[4:7]
buffer_load_dword v254, v132, s[44:47], s86 offen
mm a[68:71],v[12:15],v[212:215],a[68:71]
s_add_u32 s86, s86, 2097152
s_and_b32 s86, s86, 4194303
mm a[68:71],v[8:11],v[208:211],a[68:71]
rt v[52:53],v242 offset:50816
rt v[54:55],v242 offset:51840
mm a[188:191],v[8:11],v[200:203],a[188:191]
v_and_b32 v103, 0xffff0000, v79
v_sub_f32 v102, v102, v70
mm a[188:191],v[12:15],v[204:207],a[188:191]
v_sub_f32 v103, v103, v70
v_add_f32 v124, v102, v103
mm a[204:207],v[12:15],v[196:199],a[204:207]
v_fmac_f32 v122, v102, v102
v_add_f32 v120, v120, v124
mm a[204:207],v[8:11],v[192:195],a[204:207]
rt v[56:57],v243 offset:33920
rt v[58:59],v243 offset:34944
mm a[88:91],v[16:19],v[192:195],a[88:91]
v_fmac_f32 v123, v103, v103
v_lshlrev_b32 v104, 16, v80
mm a[88:91],v[20:23],v[196:199],a[88:91]
v_and_b32 v105, 0xffff0000, v80
v_sub_f32 v104, v104, v70
mm a[24:27],v[20:23],v[204:207],a[24:27]
v_sub_f32 v105, v105, v70
v_add_f32 v124, v104, v105
mm a[24:27],v[16:19],v[200:203],a[24:27]
rt v[60:61],v243 offset:50816
rt v[62:63],v243 offset:51840
mm a[240:243],v[16:19],v[208:211],a[240:243]
v_fmac_f32 v122, v104, v104
v_add_f32 v120, v120, v124
mm a[240:243],v[20:23],v[212:215],a[240:243]
v_fmac_f32 v123, v105, v105
v_lshlrev_b32 v106, 16, v81
mm a[32:35],v[20:23],v[220:223],a[32:35]
v_and_b32 v107, 0xffff0000, v81
v_sub_f32 v106, v106, v70
mm a[32:35],v[16:19],v[216:219],a[32:35]
v_sub_f32 v107, v107, v70
v_add_f32 v124, v106, v107
mm a[36:39],v[24:27],v[216:219],a[36:39]
v_fmac_f32 v122, v106, v106
v_add_f32 v120, v120, v124
mm a[36:39],v[28:31],v[220:223],a[36:39]
v_fmac_f32 v123, v107, v107
v_lshlrev_b32 v108, 16, v82
mm a[244:247],v[28:31],v[212:215],a[244:247]
v_and_b32 v109, 0xffff0000, v82
v_sub_f32 v108, v108, v70
mm a[244:247],v[24:27],v[208:211],a[244:247]
v_sub_f32 v109, v109, v70
v_add_f32 v124, v108, v109
mm a[28:31],v[24:27],v[200:203],a[28:31]
v_fmac_f32 v122, v108, v108
v_add_f32 v120, v120, v124
mm a[28:31],v[28:31],v[204:207],a[28:31]
v_fmac_f32 v123, v109, v109
v_lshlrev_b32 v110, 16, v83
mm a[92:95],v[28:31],v[196:199],a[92:95]
wt vmcnt(9) lgkmcnt(0)
s_barrier
mm a[92:95],v[24:27],v[192:195],a[92:95]
v_and_b32 v111, 0xffff0000, v83
v_sub_f32 v110, v110, v70
mm a[120:123],v[160:163],v[32:35],a[120:123]
v_sub_f32 v111, v111, v70
v_add_f32 v124, v110, v111
mm a[120:123],v[164:167],v[36:39],a[120:123]
v_fmac_f32 v122, v110, v110
v_add_f32 v120, v120, v124
mm a[136:139],v[164:167],v[44:47],a[136:139]
rt v[192:193],v240 offset:0
rt v[194:195],v240 offset:1024
mm a[136:139],v[160:163],v[40:43],a[136:139]
rt v[196:197],v240 offset:16896
rt v[198:199],v240 offset:17920
mm a[224:227],v[160:163],v[48:51],a[224:227]
v_fmac_f32 v123, v111, v111
v_lshlrev_b32 v112, 16, v84
mm a[224:227],v[164:167],v[52:55],a[224:227]
v_and_b32 v113, 0xffff0000, v84
v_sub_f32 v112, v112, v70
mm a[144:147],v[164:167],v[60:63],a[144:147]
v_sub_f32 v113, v113, v70
v_add_f32 v124, v112, v113
mm a[144:147],v[160:163],v[56:59],a[144:147]
rd v[160:163],v236 offset:24576
rd v[164:167],v237 offset:24576
mm a[148:151],v[168:171],v[56:59],a[148:151]
v_fmac_f32 v122, v112, v112
v_add_f32 v120, v120, v124
mm a[148:151],v[172:175],v[60:63],a[148:151]
v_fmac_f32 v123, v113, v113
v_lshlrev_b32 v114, 16, v85
mm a[228:231],v[172:175],v[52:55],a[228:231]
v_and_b32 v115, 0xffff0000, v85
v_sub_f32 v114, v114, v70
mm a[228:231],v[168:171],v[48:51],a[228:231]
rt v[200:201],v241 offset:0
rt v[202:203],v241 offset:1024
mm a[140:143],v[168:171],v[40:43],a[140:143]
v_sub_f32 v115, v115, v70
v_add_f32 v124, v114, v115
mm a[140:143],v[172:175],v[44:47],a[140:143]
v_fmac_f32 v122, v114, v114
v_add_f32 v120, v120, v124
mm a[124:127],v[172:175],v[36:39],a[124:127]
v_fmac_f32 v123, v115, v115
v_lshlrev_b32 v116, 16, v86
mm a[124:127],v[168:171],v[32:35],a[124:127]
rd v[168:171],v236 offset:26624
rd v[172:175],v237 offset:26624
mm a[216:219],v[176:179],v[32:35],a[216:219]
v_and_b32 v117, 0xffff0000, v86
v_sub_f32 v116, v116, v70
mm a[216:219],v[180:183],v[36:39],a[216:219]
v_sub_f32 v117, v117, v70
v_add_f32 v124, v116, v117
mm a[168:171],v[180:183],v[44:47],a[168:171]
v_fmac_f32 v122, v116, v116
v_add_f32 v120, v120, v124
mm a[168:171],v[176:179],v[40:43],a[168:171]
rt v[204:205],v241 offset:16896
rt v[206:207],v241 offset:17920
mm a[192:195],v[176:179],v[48:51],a[192:195]
v_fmac_f32 v123, v117, v117
v_lshlrev_b32 v118, 16, v87
mm a[192:195],v[180:183],v[52:55],a[192:195]
v_and_b32 v119, 0xffff0000, v87
v_sub_f32 v118, v118, v70
mm a[176:179],v[180:183],v[60:63],a[176:179]
v_sub_f32 v119, v119, v70
v_add_f32 v124, v118, v119
mm a[176:179],v[176:179],v[56:59],a[176:179]
rd v[176:179],v236 offset:16384
rd v[180:183],v237 offset:16384
mm a[180:183],v[184:187],v[56:59],a[180:183]
v_fmac_f32 v122, v118, v118
v_add_f32 v120, v120, v124
mm a[180:183],v[188:191],v[60:63],a[180:183]
v_fmac_f32 v123, v119, v119
v_add_f32 v121, v122, v123
mm a[196:199],v[188:191],v[52:55],a[196:199]
v_add_f32_dpp v120, v120, v120 row_ror:8 row_mask:0xf bank_mask:0xf
mm a[196:199],v[184:187],v[48:51],a[196:199]
rt v[208:209],v242 offset:0
rt v[210:211],v242 offset:1024
mm a[172:175],v[184:187],v[40:43],a[172:175]
s_add_u32 m0, s83, 33792
v_add_f32_dpp v121, v121, v121 row_ror:8 row_mask:0xf bank_mask:0xf
mm a[172:175],v[188:191],v[44:47],a[172:175]
ld v232,s[44:47],s80 offen lds
mm a[220:223],v[188:191],v[36:39],a[220:223]
v_mov_b32 v124, v120
v_mov_b32 v125, v121
mm a[220:223],v[184:187],v[32:35],a[220:223]
rd v[184:187],v236 offset:18432
rd v[188:191],v237 offset:18432
mm a[72:75],v[0:3],v[32:35],a[72:75]
s_add_u32 m0, s83, 34816
v_permlane16_swap_b32 v124, v120
mm a[72:75],v[4:7],v[36:39],a[72:75]
ld v233,s[44:47],s80 offen lds
mm a[8:11],v[4:7],v[44:47],a[8:11]
v_permlane16_swap_b32 v125, v121
mm a[8:11],v[0:3],v[40:43],a[8:11]
rt v[212:213],v242 offset:16896
rt v[214:215],v242 offset:17920
mm a[80:83],v[0:3],v[48:51],a[80:83]
s_add_u32 m0, s83, 35968
v_add_f32 v120, v120, v124
mm a[80:83],v[4:7],v[52:55],a[80:83]
ld v234,s[44:47],s80 offen lds
mm a[16:19],v[4:7],v[60:63],a[16:19]
v_add_f32 v121, v121, v125
v_mov_b32 v124, v120
mm a[16:19],v[0:3],v[56:59],a[16:19]
rd v[0:3],v236 offset:8192
rd v[4:7],v237 offset:8192
mm a[20:23],v[8:11],v[56:59],a[20:23]
s_add_u32 m0, s83, 36992
v_mov_b32 v125, v121
mm a[20:23],v[12:15],v[60:63],a[20:23]
ld v235,s[44:47],s80 offen lds
mm a[84:87],v[12:15],v[52:55],a[84:87]
s_mov_b32 s80, 3932160
v_permlane32_swap_b32 v124, v120
mm a[84:87],v[8:11],v[48:51],a[84:87]
rt v[216:217],v243 offset:0
rt v[218:219],v243 offset:1024
mm a[12:15],v[8:11],v[40:43],a[12:15]
s_add_u32 m0, s83, 50688
v_permlane32_swap_b32 v125, v121
mm a[12:15],v[12:15],v[44:47],a[12:15]
ld v232,s[44:47],s80 offen lds
mm a[76:79],v[12:15],v[36:39],a[76:79]
v_add_f32 v120, v120, v124
v_add_f32 v121, v121, v125
mm a[76:79],v[8:11],v[32:35],a[76:79]
rd v[8:11],v236 offset:10240
rd v[12:15],v237 offset:10240
mm a[248:251],v[16:19],v[32:35],a[248:251]
s_add_u32 m0, s83, 51712
ds_write_b64 v127, v[120:121]
mm a[248:251],v[20:23],v[36:39],a[248:251]
ld v233,s[44:47],s80 offen lds
mm a[40:43],v[20:23],v[44:47],a[40:43]
s_nop 0
v_readfirstlane_b32 s92, v252
mm a[40:43],v[16:19],v[40:43],a[40:43]
rt v[220:221],v243 offset:16896
rt v[222:223],v243 offset:17920
mm a[48:51],v[16:19],v[48:51],a[48:51]
s_add_u32 m0, s83, 52864
v_readfirstlane_b32 s93, v253
mm a[48:51],v[20:23],v[52:55],a[48:51]
ld v234,s[44:47],s80 offen lds
mm a[56:59],v[20:23],v[60:63],a[56:59]
s_add_u32 s92, s92, 1
s_addc_u32 s93, s93, 0
mm a[56:59],v[16:19],v[56:59],a[56:59]
rd v[16:19],v236 offset:0
rd v[20:23],v237 offset:0
mm a[60:63],v[24:27],v[56:59],a[60:63]
s_add_u32 m0, s83, 53888
v_mov_b32 v252, s92
mm a[60:63],v[28:31],v[60:63],a[60:63]
ld v235,s[44:47],s80 offen lds
mm a[52:55],v[28:31],v[52:55],a[52:55]
s_mov_b32 s80, 0
v_mov_b32 v253, s93
mm a[52:55],v[24:27],v[48:51],a[52:55]
v_mov_b32 v128, 0
v_mov_b32 v129, 0
mm a[44:47],v[24:27],v[40:43],a[44:47]
v_mov_b32 v130, 0
v_mov_b32 v131, 0
mm a[44:47],v[28:31],v[44:47],a[44:47]
st v[128:131],v255,s[52:55],s95 offen sc1
mm a[252:255],v[28:31],v[36:39],a[252:255]
wt lgkmcnt(2)
mm a[252:255],v[24:27],v[32:35],a[252:255]
rd v[24:27],v236 offset:2048
rd v[28:31],v237 offset:2048
mm a[96:99],v[160:163],v[192:195],a[96:99]
mm a[96:99],v[164:167],v[196:199],a[96:99]
mm a[104:107],v[164:167],v[204:207],a[104:107]
mm a[104:107],v[160:163],v[200:203],a[104:107]
rt v[32:33],v240 offset:128
rt v[34:35],v240 offset:1152
mm a[112:115],v[160:163],v[208:211],a[112:115]
mm a[112:115],v[164:167],v[212:215],a[112:115]
mm a[128:131],v[164:167],v[220:223],a[128:131]
wt lgkmcnt(2)
s_barrier
mm a[128:131],v[160:163],v[216:219],a[128:131]
rd v[72:75],v126
rd v[76:79],v126 offset:16
mm a[132:135],v[168:171],v[216:219],a[132:135]
s_xor_b32 m0, s82, 0
mm a[132:135],v[172:175],v[220:223],a[132:135]
ld v224,s[40:43],s79 offen lds
mm a[116:119],v[172:175],v[212:215],a[116:119]
mm a[116:119],v[168:171],v[208:211],a[116:119]
rt v[36:37],v240 offset:17024
rt v[38:39],v240 offset:18048
mm a[108:111],v[168:171],v[200:203],a[108:111]
s_xor_b32 m0, s82, 1024
mm a[108:111],v[172:175],v[204:207],a[108:111]
ld v225,s[40:43],s79 offen lds
mm a[100:103],v[172:175],v[196:199],a[100:103]
wt lgkmcnt(2)
v_add_f32 v72, v72, v74
mm a[100:103],v[168:171],v[192:195],a[100:103]
v_add_f32 v73, v73, v75
v_add_f32 v76, v76, v78
mm a[232:235],v[176:179],v[192:195],a[232:235]
s_xor_b32 m0, s82, 2048
v_add_f32 v77, v77, v79
mm a[232:235],v[180:183],v[196:199],a[232:235]
ld v226,s[40:43],s79 offen lds
mm a[152:155],v[180:183],v[204:207],a[152:155]
v_add_f32 v72, v72, v76
v_add_f32 v73, v73, v77
mm a[152:155],v[176:179],v[200:203],a[152:155]
rt v[40:41],v241 offset:128
rt v[42:43],v241 offset:1152
mm a[208:211],v[176:179],v[208:211],a[208:211]
s_xor_b32 m0, s82, 3072
v_mul_f32 v72, 0x3a800000, v72
mm a[208:211],v[180:183],v[212:215],a[208:211]
ld v227,s[40:43],s79 offen lds
mm a[160:163],v[180:183],v[220:223],a[160:163]
v_mul_f32 v73, 0x3a800000, v73
v_add_f32 v124, v70, v72
mm a[160:163],v[176:179],v[216:219],a[160:163]
v_fma_f32 v125, -v72, v72, v73
cv v124,v124,v124
mm a[164:167],v[184:187],v[216:219],a[164:167]
s_xor_b32 m0, s82, 4096
v_lshlrev_b32 v124, 16, v124
mm a[164:167],v[188:191],v[220:223],a[164:167]
ld v228,s[40:43],s79 offen lds
mm a[212:215],v[188:191],v[212:215],a[212:215]
v_max_f32 v125, 0, v125
cv v125,v125,v125
mm a[212:215],v[184:187],v[208:211],a[212:215]
rt v[44:45],v241 offset:17024
rt v[46:47],v241 offset:18048
mm a[156:159],v[184:187],v[200:203],a[156:159]
s_xor_b32 m0, s82, 5120
v_lshlrev_b32 v125, 16, v125
mm a[156:159],v[188:191],v[204:207],a[156:159]
ld v229,s[40:43],s79 offen lds
mm a[236:239],v[188:191],v[196:199],a[236:239]
v_add_f32 v125, 0x3727c5ac, v125
cv v125,v125,v125
mm a[236:239],v[184:187],v[192:195],a[236:239]
v_lshlrev_b32 v125, 16, v125
mm a[200:203],v[0:3],v[192:195],a[200:203]
s_xor_b32 m0, s82, 6144
mm a[200:203],v[4:7],v[196:199],a[200:203]
ld v230,s[40:43],s79 offen lds
mm a[184:187],v[4:7],v[204:207],a[184:187]
v_sqrt_f32 v125, v125
mm a[184:187],v[0:3],v[200:203],a[184:187]
rt v[48:49],v242 offset:128
rt v[50:51],v242 offset:1152
mm a[64:67],v[0:3],v[208:211],a[64:67]
s_xor_b32 m0, s82, 7168
v_sub_f32 v124, v70, v124
mm a[64:67],v[4:7],v[212:215],a[64:67]
ld v231,s[40:43],s79 offen lds
mm a[0:3],v[4:7],v[220:223],a[0:3]
s_mov_b32 s79, 128
cv v125,v125,v125
mm a[0:3],v[0:3],v[216:219],a[0:3]
v_lshlrev_b32 v125, 16, v125
mm a[4:7],v[8:11],v[216:219],a[4:7]
v_rcp_f32 v125, v125
mm a[4:7],v[12:15],v[220:223],a[4:7]
v_mul_f32 v125, 0x3e0293ee, v125
v_mul_f32 v124, v124, v125
mm a[68:71],v[12:15],v[212:215],a[68:71]
v_fma_f32 v88, v88, v125, v124
v_fma_f32 v89, v89, v125, v124
mm a[68:71],v[8:11],v[208:211],a[68:71]
rt v[52:53],v242 offset:17024
rt v[54:55],v242 offset:18048
mm a[188:191],v[8:11],v[200:203],a[188:191]
cv v72,v88,v89
v_fma_f32 v90, v90, v125, v124
mm a[188:191],v[12:15],v[204:207],a[188:191]
v_fma_f32 v91, v91, v125, v124
cv v73,v90,v91
mm a[204:207],v[12:15],v[196:199],a[204:207]
v_fma_f32 v92, v92, v125, v124
v_fma_f32 v93, v93, v125, v124
mm a[204:207],v[8:11],v[192:195],a[204:207]
rt v[56:57],v243 offset:128
rt v[58:59],v243 offset:1152
mm a[88:91],v[16:19],v[192:195],a[88:91]
cv v74,v92,v93
v_fma_f32 v94, v94, v125, v124
mm a[88:91],v[20:23],v[196:199],a[88:91]
v_fma_f32 v95, v95, v125, v124
cv v75,v94,v95
mm a[24:27],v[20:23],v[204:207],a[24:27]
st v[72:75],v71,s[56:59],0 offen offset:0 sc1
mm a[24:27],v[16:19],v[200:203],a[24:27]
rt v[60:61],v243 offset:17024
rt v[62:63],v243 offset:18048
mm a[240:243],v[16:19],v[208:211],a[240:243]
v_fma_f32 v96, v96, v125, v124
v_fma_f32 v97, v97, v125, v124
mm a[240:243],v[20:23],v[212:215],a[240:243]
cv v76,v96,v97
v_fma_f32 v98, v98, v125, v124
mm a[32:35],v[20:23],v[220:223],a[32:35]
v_fma_f32 v99, v99, v125, v124
cv v77,v98,v99
mm a[32:35],v[16:19],v[216:219],a[32:35]
v_fma_f32 v100, v100, v125, v124
v_fma_f32 v101, v101, v125, v124
mm a[36:39],v[24:27],v[216:219],a[36:39]
cv v78,v100,v101
v_fma_f32 v102, v102, v125, v124
mm a[36:39],v[28:31],v[220:223],a[36:39]
v_fma_f32 v103, v103, v125, v124
cv v79,v102,v103
mm a[244:247],v[28:31],v[212:215],a[244:247]
st v[76:79],v71,s[56:59],0 offen offset:2048 sc1
mm a[244:247],v[24:27],v[208:211],a[244:247]
v_fma_f32 v104, v104, v125, v124
v_fma_f32 v105, v105, v125, v124
mm a[28:31],v[24:27],v[200:203],a[28:31]
cv v80,v104,v105
v_fma_f32 v106, v106, v125, v124
mm a[28:31],v[28:31],v[204:207],a[28:31]
v_fma_f32 v107, v107, v125, v124
cv v81,v106,v107
mm a[92:95],v[28:31],v[196:199],a[92:95]
wt vmcnt(11) lgkmcnt(0)
s_barrier
mm a[92:95],v[24:27],v[192:195],a[92:95]
v_fma_f32 v108, v108, v125, v124
v_fma_f32 v109, v109, v125, v124
mm a[120:123],v[160:163],v[32:35],a[120:123]
cv v82,v108,v109
v_fma_f32 v110, v110, v125, v124
mm a[120:123],v[164:167],v[36:39],a[120:123]
v_fma_f32 v111, v111, v125, v124
cv v83,v110,v111
mm a[136:139],v[164:167],v[44:47],a[136:139]
rt v[192:193],v240 offset:33792
rt v[194:195],v240 offset:34816
mm a[136:139],v[160:163],v[40:43],a[136:139]
rt v[196:197],v240 offset:50688
rt v[198:199],v240 offset:51712
mm a[224:227],v[160:163],v[48:51],a[224:227]
st v[80:83],v71,s[56:59],s88 offen offset:0 sc1
mm a[224:227],v[164:167],v[52:55],a[224:227]
v_fma_f32 v112, v112, v125, v124
v_fma_f32 v113, v113, v125, v124
mm a[144:147],v[164:167],v[60:63],a[144:147]
cv v84,v112,v113
v_fma_f32 v114, v114, v125, v124
mm a[144:147],v[160:163],v[56:59],a[144:147]
rd v[160:163],v236 offset:57344
rd v[164:167],v237 offset:57344
mm a[148:151],v[168:171],v[56:59],a[148:151]
v_fma_f32 v115, v115, v125, v124
cv v85,v114,v115
mm a[148:151],v[172:175],v[60:63],a[148:151]
v_fma_f32 v116, v116, v125, v124
v_fma_f32 v117, v117, v125, v124
mm a[228:231],v[172:175],v[52:55],a[228:231]
cv v86,v116,v117
v_fma_f32 v118, v118, v125, v124
mm a[228:231],v[168:171],v[48:51],a[228:231]
rt v[200:201],v241 offset:33792
rt v[202:203],v241 offset:34816
mm a[140:143],v[168:171],v[40:43],a[140:143]
v_fma_f32 v119, v119, v125, v124
cv v87,v118,v119
mm a[140:143],v[172:175],v[44:47],a[140:143]
st v[84:87],v71,s[56:59],s88 offen offset:2048 sc1
mm a[124:127],v[172:175],v[36:39],a[124:127]
mm a[124:127],v[168:171],v[32:35],a[124:127]
rd v[168:171],v236 offset:59392
rd v[172:175],v237 offset:59392
mm a[216:219],v[176:179],v[32:35],a[216:219]
mm a[216:219],v[180:183],v[36:39],a[216:219]
mm a[168:171],v[180:183],v[44:47],a[168:171]
mm a[168:171],v[176:179],v[40:43],a[168:171]
rt v[204:205],v241 offset:50688
rt v[206:207],v241 offset:51712
mm a[192:195],v[176:179],v[48:51],a[192:195]
mm a[192:195],v[180:183],v[52:55],a[192:195]
mm a[176:179],v[180:183],v[60:63],a[176:179]
mm a[176:179],v[176:179],v[56:59],a[176:179]
rd v[176:179],v236 offset:49152
rd v[180:183],v237 offset:49152
mm a[180:183],v[184:187],v[56:59],a[180:183]
mm a[180:183],v[188:191],v[60:63],a[180:183]
mm a[196:199],v[188:191],v[52:55],a[196:199]
mm a[196:199],v[184:187],v[48:51],a[196:199]
rt v[208:209],v242 offset:33792
rt v[210:211],v242 offset:34816
mm a[172:175],v[184:187],v[40:43],a[172:175]
s_add_u32 m0, s83, 0
mm a[172:175],v[188:191],v[44:47],a[172:175]
ld v232,s[44:47],s80 offen lds
mm a[220:223],v[188:191],v[36:39],a[220:223]
mm a[220:223],v[184:187],v[32:35],a[220:223]
rd v[184:187],v236 offset:51200
rd v[188:191],v237 offset:51200
mm a[72:75],v[0:3],v[32:35],a[72:75]
s_add_u32 m0, s83, 1024
mm a[72:75],v[4:7],v[36:39],a[72:75]
ld v233,s[44:47],s80 offen lds
mm a[8:11],v[4:7],v[44:47],a[8:11]
mm a[8:11],v[0:3],v[40:43],a[8:11]
rt v[212:213],v242 offset:50688
rt v[214:215],v242 offset:51712
mm a[80:83],v[0:3],v[48:51],a[80:83]
s_add_u32 m0, s83, 2176
mm a[80:83],v[4:7],v[52:55],a[80:83]
ld v234,s[44:47],s80 offen lds
mm a[16:19],v[4:7],v[60:63],a[16:19]
mm a[16:19],v[0:3],v[56:59],a[16:19]
rd v[0:3],v236 offset:40960
rd v[4:7],v237 offset:40960
mm a[20:23],v[8:11],v[56:59],a[20:23]
s_add_u32 m0, s83, 3200
mm a[20:23],v[12:15],v[60:63],a[20:23]
ld v235,s[44:47],s80 offen lds
mm a[84:87],v[12:15],v[52:55],a[84:87]
s_mov_b32 s80, 262144
mm a[84:87],v[8:11],v[48:51],a[84:87]
rt v[216:217],v243 offset:33792
rt v[218:219],v243 offset:34816
mm a[12:15],v[8:11],v[40:43],a[12:15]
s_add_u32 m0, s83, 16896
mm a[12:15],v[12:15],v[44:47],a[12:15]
ld v232,s[44:47],s80 offen lds
mm a[76:79],v[12:15],v[36:39],a[76:79]
mm a[76:79],v[8:11],v[32:35],a[76:79]
rd v[8:11],v236 offset:43008
rd v[12:15],v237 offset:43008
mm a[248:251],v[16:19],v[32:35],a[248:251]
s_add_u32 m0, s83, 17920
mm a[248:251],v[20:23],v[36:39],a[248:251]
ld v233,s[44:47],s80 offen lds
mm a[40:43],v[20:23],v[44:47],a[40:43]
mm a[40:43],v[16:19],v[40:43],a[40:43]
rt v[220:221],v243 offset:50688
rt v[222:223],v243 offset:51712
mm a[48:51],v[16:19],v[48:51],a[48:51]
s_add_u32 m0, s83, 19072
mm a[48:51],v[20:23],v[52:55],a[48:51]
ld v234,s[44:47],s80 offen lds
mm a[56:59],v[20:23],v[60:63],a[56:59]
mm a[56:59],v[16:19],v[56:59],a[56:59]
rd v[16:19],v236 offset:32768
rd v[20:23],v237 offset:32768
.La5_LW1_6:
s_cbranch_execz .La5_LW1_10
mm a[60:63],v[24:27],v[56:59],a[60:63]
s_add_u32 m0, s83, 20096
mm a[60:63],v[28:31],v[60:63],a[60:63]
ld v235,s[44:47],s80 offen lds
mm a[52:55],v[28:31],v[52:55],a[52:55]
s_mov_b32 s80, 524288
mm a[52:55],v[24:27],v[48:51],a[52:55]
mm a[44:47],v[24:27],v[40:43],a[44:47]
mm a[44:47],v[28:31],v[44:47],a[44:47]
mm a[252:255],v[28:31],v[36:39],a[252:255]
wt lgkmcnt(2)
mm a[252:255],v[24:27],v[32:35],a[252:255]
rd v[24:27],v236 offset:34816
rd v[28:31],v237 offset:34816
mm a[96:99],v[160:163],v[192:195],a[96:99]
mm a[96:99],v[164:167],v[196:199],a[96:99]
mm a[104:107],v[164:167],v[204:207],a[104:107]
mm a[104:107],v[160:163],v[200:203],a[104:107]
rt v[32:33],v240 offset:33920
rt v[34:35],v240 offset:34944
mm a[112:115],v[160:163],v[208:211],a[112:115]
mm a[112:115],v[164:167],v[212:215],a[112:115]
mm a[128:131],v[164:167],v[220:223],a[128:131]
wt lgkmcnt(2)
s_barrier
mm a[128:131],v[160:163],v[216:219],a[128:131]
mm a[132:135],v[168:171],v[216:219],a[132:135]
s_xor_b32 m0, s82, 32768
mm a[132:135],v[172:175],v[220:223],a[132:135]
ld v224,s[40:43],s79 offen lds
mm a[116:119],v[172:175],v[212:215],a[116:119]
mm a[116:119],v[168:171],v[208:211],a[116:119]
rt v[36:37],v240 offset:50816
rt v[38:39],v240 offset:51840
mm a[108:111],v[168:171],v[200:203],a[108:111]
s_xor_b32 m0, s82, 33792
mm a[108:111],v[172:175],v[204:207],a[108:111]
ld v225,s[40:43],s79 offen lds
mm a[100:103],v[172:175],v[196:199],a[100:103]
mm a[100:103],v[168:171],v[192:195],a[100:103]
mm a[232:235],v[176:179],v[192:195],a[232:235]
s_xor_b32 m0, s82, 34816
mm a[232:235],v[180:183],v[196:199],a[232:235]
ld v226,s[40:43],s79 offen lds
mm a[152:155],v[180:183],v[204:207],a[152:155]
mm a[152:155],v[176:179],v[200:203],a[152:155]
rt v[40:41],v241 offset:33920
rt v[42:43],v241 offset:34944
mm a[208:211],v[176:179],v[208:211],a[208:211]
s_xor_b32 m0, s82, 35840
mm a[208:211],v[180:183],v[212:215],a[208:211]
ld v227,s[40:43],s79 offen lds
mm a[160:163],v[180:183],v[220:223],a[160:163]
mm a[160:163],v[176:179],v[216:219],a[160:163]
mm a[164:167],v[184:187],v[216:219],a[164:167]
s_xor_b32 m0, s82, 36864
mm a[164:167],v[188:191],v[220:223],a[164:167]
ld v228,s[40:43],s79 offen lds
mm a[212:215],v[188:191],v[212:215],a[212:215]
mm a[212:215],v[184:187],v[208:211],a[212:215]
rt v[44:45],v241 offset:50816
rt v[46:47],v241 offset:51840
mm a[156:159],v[184:187],v[200:203],a[156:159]
s_xor_b32 m0, s82, 37888
mm a[156:159],v[188:191],v[204:207],a[156:159]
ld v229,s[40:43],s79 offen lds
mm a[236:239],v[188:191],v[196:199],a[236:239]
mm a[236:239],v[184:187],v[192:195],a[236:239]
mm a[200:203],v[0:3],v[192:195],a[200:203]
s_xor_b32 m0, s82, 38912
mm a[200:203],v[4:7],v[196:199],a[200:203]
ld v230,s[40:43],s79 offen lds
mm a[184:187],v[4:7],v[204:207],a[184:187]
mm a[184:187],v[0:3],v[200:203],a[184:187]
rt v[48:49],v242 offset:33920
rt v[50:51],v242 offset:34944
mm a[64:67],v[0:3],v[208:211],a[64:67]
s_xor_b32 m0, s82, 39936
mm a[64:67],v[4:7],v[212:215],a[64:67]
ld v231,s[40:43],s79 offen lds
mm a[0:3],v[4:7],v[220:223],a[0:3]
s_mov_b32 s79, 256
mm a[0:3],v[0:3],v[216:219],a[0:3]
mm a[4:7],v[8:11],v[216:219],a[4:7]
mm a[4:7],v[12:15],v[220:223],a[4:7]
buffer_load_dword v254, v135, s[68:71], s96 offen
mm a[68:71],v[12:15],v[212:215],a[68:71]
s_add_u32 s96, s96, 1048576
mm a[68:71],v[8:11],v[208:211],a[68:71]
rt v[52:53],v242 offset:50816
rt v[54:55],v242 offset:51840
mm a[188:191],v[8:11],v[200:203],a[188:191]
mm a[188:191],v[12:15],v[204:207],a[188:191]
buffer_load_dword v254, v135, s[68:71], s96 offen
mm a[204:207],v[12:15],v[196:199],a[204:207]
s_add_u32 s96, s96, 1048576
mm a[204:207],v[8:11],v[192:195],a[204:207]
rt v[56:57],v243 offset:33920
rt v[58:59],v243 offset:34944
mm a[88:91],v[16:19],v[192:195],a[88:91]
mm a[88:91],v[20:23],v[196:199],a[88:91]
wt vmcnt(18)
buffer_store_dwordx2 v[252:253], off, s[64:67], s91 offset:1024 sc1
mm a[24:27],v[20:23],v[204:207],a[24:27]
s_mov_b32 s91, 0
s_lshl_b32 s92, s76, 19
mm a[24:27],v[16:19],v[200:203],a[24:27]
rt v[60:61],v243 offset:50816
rt v[62:63],v243 offset:51840
mm a[240:243],v[16:19],v[208:211],a[240:243]
s_add_u32 s92, s92, 88080384
mm a[240:243],v[20:23],v[212:215],a[240:243]
s_add_u32 s52, s26, s92
s_addc_u32 s53, s27, 0
mm a[32:35],v[20:23],v[220:223],a[32:35]
s_and_b32 s53, s53, 0xffff
s_mov_b32 s54, 524672
mm a[32:35],v[16:19],v[216:219],a[32:35]
s_mov_b32 s55, 0x20000
s_lshl_b32 s92, s3, 16
mm a[36:39],v[24:27],v[216:219],a[36:39]
s_sub_u32 s93, 458752, s92
v_add_u32 v244, s92, v136
mm a[36:39],v[28:31],v[220:223],a[36:39]
v_add_u32 v246, s93, v136
v_add_u32 v245, 0x8000, v244
mm a[244:247],v[28:31],v[212:215],a[244:247]
v_add_u32 v247, 0x8000, v246
s_lshl_b32 s94, s72, 19
mm a[244:247],v[24:27],v[208:211],a[244:247]
s_lshl_b32 s93, s76, 16
s_add_u32 s92, s94, s93
mm a[28:31],v[24:27],v[200:203],a[28:31]
s_lshl_b32 s93, s77, 13
s_add_u32 s92, s92, s93
mm a[28:31],v[28:31],v[204:207],a[28:31]
s_add_u32 s92, s92, 37748736
mm a[92:95],v[28:31],v[196:199],a[92:95]
wt vmcnt(11) lgkmcnt(0)
s_barrier
mm a[92:95],v[24:27],v[192:195],a[92:95]
s_add_u32 s48, s26, s92
s_addc_u32 s49, s27, 0
mm a[120:123],v[160:163],v[32:35],a[120:123]
s_and_b32 s49, s49, 0xffff
s_mov_b32 s50, 57344
mm a[120:123],v[164:167],v[36:39],a[120:123]
s_mov_b32 s51, 0x20000
s_lshl_b32 s93, s77, 24
mm a[136:139],v[164:167],v[44:47],a[136:139]
rt v[192:193],v240 offset:0
rt v[194:195],v240 offset:1024
mm a[136:139],v[160:163],v[40:43],a[136:139]
rt v[196:197],v240 offset:16896
rt v[198:199],v240 offset:17920
mm a[224:227],v[160:163],v[48:51],a[224:227]
s_add_u32 s92, s94, s93
s_add_u32 s92, s92, 20975616
mm a[224:227],v[164:167],v[52:55],a[224:227]
s_add_u32 s60, s26, s92
s_addc_u32 s61, s27, 0
mm a[144:147],v[164:167],v[60:63],a[144:147]
s_and_b32 s61, s61, 0xffff
s_mov_b32 s62, 520192
mm a[144:147],v[160:163],v[56:59],a[144:147]
rd v[160:163],v236 offset:24576
rd v[164:167],v237 offset:24576
mm a[148:151],v[168:171],v[56:59],a[148:151]
s_mov_b32 s63, 0x20000
s_lshl_b32 s92, s77, 14
mm a[148:151],v[172:175],v[60:63],a[148:151]
s_sub_u32 s95, 24576, s92
v_add_u32 v238, s95, v255
mm a[228:231],v[172:175],v[52:55],a[228:231]
mm a[228:231],v[168:171],v[48:51],a[228:231]
rt v[200:201],v241 offset:0
rt v[202:203],v241 offset:1024
mm a[140:143],v[168:171],v[40:43],a[140:143]
mm a[140:143],v[172:175],v[44:47],a[140:143]
mm a[124:127],v[172:175],v[36:39],a[124:127]
mm a[124:127],v[168:171],v[32:35],a[124:127]
rd v[168:171],v236 offset:26624
rd v[172:175],v237 offset:26624
mm a[216:219],v[176:179],v[32:35],a[216:219]
mm a[216:219],v[180:183],v[36:39],a[216:219]
mm a[168:171],v[180:183],v[44:47],a[168:171]
mm a[168:171],v[176:179],v[40:43],a[168:171]
rt v[204:205],v241 offset:16896
rt v[206:207],v241 offset:17920
mm a[192:195],v[176:179],v[48:51],a[192:195]
mm a[192:195],v[180:183],v[52:55],a[192:195]
mm a[176:179],v[180:183],v[60:63],a[176:179]
mm a[176:179],v[176:179],v[56:59],a[176:179]
rd v[176:179],v236 offset:16384
rd v[180:183],v237 offset:16384
mm a[180:183],v[184:187],v[56:59],a[180:183]
mm a[180:183],v[188:191],v[60:63],a[180:183]
mm a[196:199],v[188:191],v[52:55],a[196:199]
mm a[196:199],v[184:187],v[48:51],a[196:199]
rt v[208:209],v242 offset:0
rt v[210:211],v242 offset:1024
mm a[172:175],v[184:187],v[40:43],a[172:175]
s_add_u32 m0, s83, 33792
mm a[172:175],v[188:191],v[44:47],a[172:175]
ld v232,s[44:47],s80 offen lds
mm a[220:223],v[188:191],v[36:39],a[220:223]
mm a[220:223],v[184:187],v[32:35],a[220:223]
rd v[184:187],v236 offset:18432
rd v[188:191],v237 offset:18432
mm a[72:75],v[0:3],v[32:35],a[72:75]
s_add_u32 m0, s83, 34816
mm a[72:75],v[4:7],v[36:39],a[72:75]
ld v233,s[44:47],s80 offen lds
mm a[8:11],v[4:7],v[44:47],a[8:11]
mm a[8:11],v[0:3],v[40:43],a[8:11]
rt v[212:213],v242 offset:16896
rt v[214:215],v242 offset:17920
mm a[80:83],v[0:3],v[48:51],a[80:83]
s_add_u32 m0, s83, 35968
mm a[80:83],v[4:7],v[52:55],a[80:83]
ld v234,s[44:47],s80 offen lds
mm a[16:19],v[4:7],v[60:63],a[16:19]
mm a[16:19],v[0:3],v[56:59],a[16:19]
rd v[0:3],v236 offset:8192
rd v[4:7],v237 offset:8192
mm a[20:23],v[8:11],v[56:59],a[20:23]
s_add_u32 m0, s83, 36992
mm a[20:23],v[12:15],v[60:63],a[20:23]
ld v235,s[44:47],s80 offen lds
mm a[84:87],v[12:15],v[52:55],a[84:87]
s_mov_b32 s80, 786432
mm a[84:87],v[8:11],v[48:51],a[84:87]
rt v[216:217],v243 offset:0
rt v[218:219],v243 offset:1024
mm a[12:15],v[8:11],v[40:43],a[12:15]
s_add_u32 m0, s83, 50688
mm a[12:15],v[12:15],v[44:47],a[12:15]
ld v232,s[44:47],s80 offen lds
mm a[76:79],v[12:15],v[36:39],a[76:79]
mm a[76:79],v[8:11],v[32:35],a[76:79]
rd v[8:11],v236 offset:10240
rd v[12:15],v237 offset:10240
mm a[248:251],v[16:19],v[32:35],a[248:251]
s_add_u32 m0, s83, 51712
mm a[248:251],v[20:23],v[36:39],a[248:251]
ld v233,s[44:47],s80 offen lds
mm a[40:43],v[20:23],v[44:47],a[40:43]
mm a[40:43],v[16:19],v[40:43],a[40:43]
rt v[220:221],v243 offset:16896
rt v[222:223],v243 offset:17920
mm a[48:51],v[16:19],v[48:51],a[48:51]
s_add_u32 m0, s83, 52864
mm a[48:51],v[20:23],v[52:55],a[48:51]
ld v234,s[44:47],s80 offen lds
mm a[56:59],v[20:23],v[60:63],a[56:59]
mm a[56:59],v[16:19],v[56:59],a[56:59]
rd v[16:19],v236 offset:0
rd v[20:23],v237 offset:0
mm a[60:63],v[24:27],v[56:59],a[60:63]
s_add_u32 m0, s83, 53888
mm a[60:63],v[28:31],v[60:63],a[60:63]
ld v235,s[44:47],s80 offen lds
mm a[52:55],v[28:31],v[52:55],a[52:55]
s_mov_b32 s80, 1048576
mm a[52:55],v[24:27],v[48:51],a[52:55]
mm a[44:47],v[24:27],v[40:43],a[44:47]
mm a[44:47],v[28:31],v[44:47],a[44:47]
mm a[252:255],v[28:31],v[36:39],a[252:255]
wt lgkmcnt(2)
mm a[252:255],v[24:27],v[32:35],a[252:255]
rd v[24:27],v236 offset:2048
rd v[28:31],v237 offset:2048
mm a[96:99],v[160:163],v[192:195],a[96:99]
mm a[96:99],v[164:167],v[196:199],a[96:99]
mm a[104:107],v[164:167],v[204:207],a[104:107]
mm a[104:107],v[160:163],v[200:203],a[104:107]
rt v[32:33],v240 offset:128
rt v[34:35],v240 offset:1152
mm a[112:115],v[160:163],v[208:211],a[112:115]
mm a[112:115],v[164:167],v[212:215],a[112:115]
mm a[128:131],v[164:167],v[220:223],a[128:131]
wt lgkmcnt(2)
s_barrier
mm a[128:131],v[160:163],v[216:219],a[128:131]
mm a[132:135],v[168:171],v[216:219],a[132:135]
s_xor_b32 m0, s82, 0
mm a[132:135],v[172:175],v[220:223],a[132:135]
ld v224,s[40:43],s79 offen lds
mm a[116:119],v[172:175],v[212:215],a[116:119]
mm a[116:119],v[168:171],v[208:211],a[116:119]
rt v[36:37],v240 offset:17024
rt v[38:39],v240 offset:18048
mm a[108:111],v[168:171],v[200:203],a[108:111]
s_xor_b32 m0, s82, 1024
mm a[108:111],v[172:175],v[204:207],a[108:111]
ld v225,s[40:43],s79 offen lds
mm a[100:103],v[172:175],v[196:199],a[100:103]
mm a[100:103],v[168:171],v[192:195],a[100:103]
mm a[232:235],v[176:179],v[192:195],a[232:235]
s_xor_b32 m0, s82, 2048
mm a[232:235],v[180:183],v[196:199],a[232:235]
ld v226,s[40:43],s79 offen lds
mm a[152:155],v[180:183],v[204:207],a[152:155]
mm a[152:155],v[176:179],v[200:203],a[152:155]
rt v[40:41],v241 offset:128
rt v[42:43],v241 offset:1152
mm a[208:211],v[176:179],v[208:211],a[208:211]
s_xor_b32 m0, s82, 3072
mm a[208:211],v[180:183],v[212:215],a[208:211]
ld v227,s[40:43],s79 offen lds
mm a[160:163],v[180:183],v[220:223],a[160:163]
mm a[160:163],v[176:179],v[216:219],a[160:163]
mm a[164:167],v[184:187],v[216:219],a[164:167]
s_xor_b32 m0, s82, 4096
mm a[164:167],v[188:191],v[220:223],a[164:167]
ld v228,s[40:43],s79 offen lds
mm a[212:215],v[188:191],v[212:215],a[212:215]
mm a[212:215],v[184:187],v[208:211],a[212:215]
rt v[44:45],v241 offset:17024
rt v[46:47],v241 offset:18048
mm a[156:159],v[184:187],v[200:203],a[156:159]
s_xor_b32 m0, s82, 5120
mm a[156:159],v[188:191],v[204:207],a[156:159]
ld v229,s[40:43],s79 offen lds
mm a[236:239],v[188:191],v[196:199],a[236:239]
mm a[236:239],v[184:187],v[192:195],a[236:239]
mm a[200:203],v[0:3],v[192:195],a[200:203]
s_xor_b32 m0, s82, 6144
mm a[200:203],v[4:7],v[196:199],a[200:203]
ld v230,s[40:43],s79 offen lds
mm a[184:187],v[4:7],v[204:207],a[184:187]
mm a[184:187],v[0:3],v[200:203],a[184:187]
rt v[48:49],v242 offset:128
rt v[50:51],v242 offset:1152
mm a[64:67],v[0:3],v[208:211],a[64:67]
s_xor_b32 m0, s82, 7168
mm a[64:67],v[4:7],v[212:215],a[64:67]
ld v231,s[40:43],s79 offen lds
mm a[0:3],v[4:7],v[220:223],a[0:3]
s_mov_b32 s79, 384
mm a[0:3],v[0:3],v[216:219],a[0:3]
mm a[4:7],v[8:11],v[216:219],a[4:7]
mm a[4:7],v[12:15],v[220:223],a[4:7]
buffer_load_dword v254, v135, s[68:71], s96 offen
mm a[68:71],v[12:15],v[212:215],a[68:71]
s_add_u32 s96, s96, 1048576
mm a[68:71],v[8:11],v[208:211],a[68:71]
rt v[52:53],v242 offset:17024
rt v[54:55],v242 offset:18048
mm a[188:191],v[8:11],v[200:203],a[188:191]
mm a[188:191],v[12:15],v[204:207],a[188:191]
buffer_load_dword v254, v135, s[68:71], s96 offen
mm a[204:207],v[12:15],v[196:199],a[204:207]
s_add_u32 s96, s96, 1048576
mm a[204:207],v[8:11],v[192:195],a[204:207]
rt v[56:57],v243 offset:128
rt v[58:59],v243 offset:1152
mm a[88:91],v[16:19],v[192:195],a[88:91]
mm a[88:91],v[20:23],v[196:199],a[88:91]
mm a[24:27],v[20:23],v[204:207],a[24:27]
mm a[24:27],v[16:19],v[200:203],a[24:27]
rt v[60:61],v243 offset:17024
rt v[62:63],v243 offset:18048
mm a[240:243],v[16:19],v[208:211],a[240:243]
mm a[240:243],v[20:23],v[212:215],a[240:243]
mm a[32:35],v[20:23],v[220:223],a[32:35]
mm a[32:35],v[16:19],v[216:219],a[32:35]
mm a[36:39],v[24:27],v[216:219],a[36:39]
mm a[36:39],v[28:31],v[220:223],a[36:39]
mm a[244:247],v[28:31],v[212:215],a[244:247]
mm a[244:247],v[24:27],v[208:211],a[244:247]
mm a[28:31],v[24:27],v[200:203],a[28:31]
mm a[28:31],v[28:31],v[204:207],a[28:31]
mm a[92:95],v[28:31],v[196:199],a[92:95]
wt vmcnt(10) lgkmcnt(0)
s_barrier
mm a[92:95],v[24:27],v[192:195],a[92:95]
mm a[120:123],v[160:163],v[32:35],a[120:123]
mm a[120:123],v[164:167],v[36:39],a[120:123]
mm a[136:139],v[164:167],v[44:47],a[136:139]
rt v[192:193],v240 offset:33792
rt v[194:195],v240 offset:34816
mm a[136:139],v[160:163],v[40:43],a[136:139]
rt v[196:197],v240 offset:50688
rt v[198:199],v240 offset:51712
mm a[224:227],v[160:163],v[48:51],a[224:227]
mm a[224:227],v[164:167],v[52:55],a[224:227]
mm a[144:147],v[164:167],v[60:63],a[144:147]
mm a[144:147],v[160:163],v[56:59],a[144:147]
rd v[160:163],v236 offset:57344
rd v[164:167],v237 offset:57344
mm a[148:151],v[168:171],v[56:59],a[148:151]
mm a[148:151],v[172:175],v[60:63],a[148:151]
mm a[228:231],v[172:175],v[52:55],a[228:231]
mm a[228:231],v[168:171],v[48:51],a[228:231]
rt v[200:201],v241 offset:33792
rt v[202:203],v241 offset:34816
mm a[140:143],v[168:171],v[40:43],a[140:143]
mm a[140:143],v[172:175],v[44:47],a[140:143]
mm a[124:127],v[172:175],v[36:39],a[124:127]
mm a[124:127],v[168:171],v[32:35],a[124:127]
rd v[168:171],v236 offset:59392
rd v[172:175],v237 offset:59392
mm a[216:219],v[176:179],v[32:35],a[216:219]
mm a[216:219],v[180:183],v[36:39],a[216:219]
mm a[168:171],v[180:183],v[44:47],a[168:171]
mm a[168:171],v[176:179],v[40:43],a[168:171]
rt v[204:205],v241 offset:50688
rt v[206:207],v241 offset:51712
mm a[192:195],v[176:179],v[48:51],a[192:195]
mm a[192:195],v[180:183],v[52:55],a[192:195]
mm a[176:179],v[180:183],v[60:63],a[176:179]
mm a[176:179],v[176:179],v[56:59],a[176:179]
rd v[176:179],v236 offset:49152
rd v[180:183],v237 offset:49152
mm a[180:183],v[184:187],v[56:59],a[180:183]
mm a[180:183],v[188:191],v[60:63],a[180:183]
mm a[196:199],v[188:191],v[52:55],a[196:199]
mm a[196:199],v[184:187],v[48:51],a[196:199]
rt v[208:209],v242 offset:33792
rt v[210:211],v242 offset:34816
mm a[172:175],v[184:187],v[40:43],a[172:175]
s_add_u32 m0, s83, 0
mm a[172:175],v[188:191],v[44:47],a[172:175]
ld v232,s[44:47],s80 offen lds
mm a[220:223],v[188:191],v[36:39],a[220:223]
mm a[220:223],v[184:187],v[32:35],a[220:223]
rd v[184:187],v236 offset:51200
rd v[188:191],v237 offset:51200
mm a[72:75],v[0:3],v[32:35],a[72:75]
s_add_u32 m0, s83, 1024
mm a[72:75],v[4:7],v[36:39],a[72:75]
ld v233,s[44:47],s80 offen lds
mm a[8:11],v[4:7],v[44:47],a[8:11]
mm a[8:11],v[0:3],v[40:43],a[8:11]
rt v[212:213],v242 offset:50688
rt v[214:215],v242 offset:51712
mm a[80:83],v[0:3],v[48:51],a[80:83]
s_add_u32 m0, s83, 2176
mm a[80:83],v[4:7],v[52:55],a[80:83]
ld v234,s[44:47],s80 offen lds
mm a[16:19],v[4:7],v[60:63],a[16:19]
mm a[16:19],v[0:3],v[56:59],a[16:19]
rd v[0:3],v236 offset:40960
rd v[4:7],v237 offset:40960
mm a[20:23],v[8:11],v[56:59],a[20:23]
s_add_u32 m0, s83, 3200
mm a[20:23],v[12:15],v[60:63],a[20:23]
ld v235,s[44:47],s80 offen lds
mm a[84:87],v[12:15],v[52:55],a[84:87]
s_mov_b32 s80, 1310720
mm a[84:87],v[8:11],v[48:51],a[84:87]
rt v[216:217],v243 offset:33792
rt v[218:219],v243 offset:34816
mm a[12:15],v[8:11],v[40:43],a[12:15]
s_add_u32 m0, s83, 16896
mm a[12:15],v[12:15],v[44:47],a[12:15]
ld v232,s[44:47],s80 offen lds
mm a[76:79],v[12:15],v[36:39],a[76:79]
mm a[76:79],v[8:11],v[32:35],a[76:79]
rd v[8:11],v236 offset:43008
rd v[12:15],v237 offset:43008
mm a[248:251],v[16:19],v[32:35],a[248:251]
s_add_u32 m0, s83, 17920
mm a[248:251],v[20:23],v[36:39],a[248:251]
ld v233,s[44:47],s80 offen lds
mm a[40:43],v[20:23],v[44:47],a[40:43]
mm a[40:43],v[16:19],v[40:43],a[40:43]
rt v[220:221],v243 offset:50688
rt v[222:223],v243 offset:51712
mm a[48:51],v[16:19],v[48:51],a[48:51]
s_add_u32 m0, s83, 19072
mm a[48:51],v[20:23],v[52:55],a[48:51]
ld v234,s[44:47],s80 offen lds
mm a[56:59],v[20:23],v[60:63],a[56:59]
mm a[56:59],v[16:19],v[56:59],a[56:59]
rd v[16:19],v236 offset:32768
rd v[20:23],v237 offset:32768
mm a[60:63],v[24:27],v[56:59],a[60:63]
s_add_u32 m0, s83, 20096
mm a[60:63],v[28:31],v[60:63],a[60:63]
ld v235,s[44:47],s80 offen lds
mm a[52:55],v[28:31],v[52:55],a[52:55]
s_mov_b32 s80, 1572864
mm a[52:55],v[24:27],v[48:51],a[52:55]
mm a[44:47],v[24:27],v[40:43],a[44:47]
mm a[44:47],v[28:31],v[44:47],a[44:47]
mm a[252:255],v[28:31],v[36:39],a[252:255]
wt lgkmcnt(2)
mm a[252:255],v[24:27],v[32:35],a[252:255]
rd v[24:27],v236 offset:34816
rd v[28:31],v237 offset:34816
mm a[96:99],v[160:163],v[192:195],a[96:99]
mm a[96:99],v[164:167],v[196:199],a[96:99]
mm a[104:107],v[164:167],v[204:207],a[104:107]
mm a[104:107],v[160:163],v[200:203],a[104:107]
rt v[32:33],v240 offset:33920
rt v[34:35],v240 offset:34944
mm a[112:115],v[160:163],v[208:211],a[112:115]
mm a[112:115],v[164:167],v[212:215],a[112:115]
mm a[128:131],v[164:167],v[220:223],a[128:131]
wt lgkmcnt(2)
s_barrier
mm a[128:131],v[160:163],v[216:219],a[128:131]
mm a[132:135],v[168:171],v[216:219],a[132:135]
s_xor_b32 m0, s82, 32768
mm a[132:135],v[172:175],v[220:223],a[132:135]
ld v224,s[40:43],s79 offen lds
mm a[116:119],v[172:175],v[212:215],a[116:119]
mm a[116:119],v[168:171],v[208:211],a[116:119]
rt v[36:37],v240 offset:50816
rt v[38:39],v240 offset:51840
mm a[108:111],v[168:171],v[200:203],a[108:111]
s_xor_b32 m0, s82, 33792
mm a[108:111],v[172:175],v[204:207],a[108:111]
ld v225,s[40:43],s79 offen lds
mm a[100:103],v[172:175],v[196:199],a[100:103]
mm a[100:103],v[168:171],v[192:195],a[100:103]
mm a[232:235],v[176:179],v[192:195],a[232:235]
s_xor_b32 m0, s82, 34816
mm a[232:235],v[180:183],v[196:199],a[232:235]
ld v226,s[40:43],s79 offen lds
mm a[152:155],v[180:183],v[204:207],a[152:155]
mm a[152:155],v[176:179],v[200:203],a[152:155]
rt v[40:41],v241 offset:33920
rt v[42:43],v241 offset:34944
mm a[208:211],v[176:179],v[208:211],a[208:211]
s_xor_b32 m0, s82, 35840
mm a[208:211],v[180:183],v[212:215],a[208:211]
.La5_LW1_7:
s_cbranch_execz .La5_LW1_11
ld v227,s[40:43],s79 offen lds
mm a[160:163],v[180:183],v[220:223],a[160:163]
mm a[160:163],v[176:179],v[216:219],a[160:163]
mm a[164:167],v[184:187],v[216:219],a[164:167]
s_xor_b32 m0, s82, 36864
mm a[164:167],v[188:191],v[220:223],a[164:167]
ld v228,s[40:43],s79 offen lds
mm a[212:215],v[188:191],v[212:215],a[212:215]
mm a[212:215],v[184:187],v[208:211],a[212:215]
rt v[44:45],v241 offset:50816
rt v[46:47],v241 offset:51840
mm a[156:159],v[184:187],v[200:203],a[156:159]
s_xor_b32 m0, s82, 37888
mm a[156:159],v[188:191],v[204:207],a[156:159]
ld v229,s[40:43],s79 offen lds
mm a[236:239],v[188:191],v[196:199],a[236:239]
mm a[236:239],v[184:187],v[192:195],a[236:239]
mm a[200:203],v[0:3],v[192:195],a[200:203]
s_xor_b32 m0, s82, 38912
mm a[200:203],v[4:7],v[196:199],a[200:203]
ld v230,s[40:43],s79 offen lds
mm a[184:187],v[4:7],v[204:207],a[184:187]
mm a[184:187],v[0:3],v[200:203],a[184:187]
rt v[48:49],v242 offset:33920
rt v[50:51],v242 offset:34944
mm a[64:67],v[0:3],v[208:211],a[64:67]
s_xor_b32 m0, s82, 39936
mm a[64:67],v[4:7],v[212:215],a[64:67]
ld v231,s[40:43],s79 offen lds
mm a[0:3],v[4:7],v[220:223],a[0:3]
s_mov_b32 s79, 512
mm a[0:3],v[0:3],v[216:219],a[0:3]
mm a[4:7],v[8:11],v[216:219],a[4:7]
mm a[4:7],v[12:15],v[220:223],a[4:7]
buffer_load_dword v254, v135, s[68:71], s96 offen
mm a[68:71],v[12:15],v[212:215],a[68:71]
s_add_u32 s96, s96, 1048576
mm a[68:71],v[8:11],v[208:211],a[68:71]
rt v[52:53],v242 offset:50816
rt v[54:55],v242 offset:51840
mm a[188:191],v[8:11],v[200:203],a[188:191]
mm a[188:191],v[12:15],v[204:207],a[188:191]
buffer_load_dword v254, v135, s[68:71], s96 offen
mm a[204:207],v[12:15],v[196:199],a[204:207]
s_add_u32 s96, s96, 1048576
mm a[204:207],v[8:11],v[192:195],a[204:207]
rt v[56:57],v243 offset:33920
rt v[58:59],v243 offset:34944
mm a[88:91],v[16:19],v[192:195],a[88:91]
mm a[88:91],v[20:23],v[196:199],a[88:91]
mm a[24:27],v[20:23],v[204:207],a[24:27]
mm a[24:27],v[16:19],v[200:203],a[24:27]
rt v[60:61],v243 offset:50816
rt v[62:63],v243 offset:51840
mm a[240:243],v[16:19],v[208:211],a[240:243]
mm a[240:243],v[20:23],v[212:215],a[240:243]
mm a[32:35],v[20:23],v[220:223],a[32:35]
mm a[32:35],v[16:19],v[216:219],a[32:35]
mm a[36:39],v[24:27],v[216:219],a[36:39]
mm a[36:39],v[28:31],v[220:223],a[36:39]
mm a[244:247],v[28:31],v[212:215],a[244:247]
mm a[244:247],v[24:27],v[208:211],a[244:247]
mm a[28:31],v[24:27],v[200:203],a[28:31]
mm a[28:31],v[28:31],v[204:207],a[28:31]
mm a[92:95],v[28:31],v[196:199],a[92:95]
wt vmcnt(10) lgkmcnt(0)
s_barrier
mm a[92:95],v[24:27],v[192:195],a[92:95]
mm a[120:123],v[160:163],v[32:35],a[120:123]
mm a[120:123],v[164:167],v[36:39],a[120:123]
mm a[136:139],v[164:167],v[44:47],a[136:139]
rt v[192:193],v240 offset:0
rt v[194:195],v240 offset:1024
mm a[136:139],v[160:163],v[40:43],a[136:139]
rt v[196:197],v240 offset:16896
rt v[198:199],v240 offset:17920
mm a[224:227],v[160:163],v[48:51],a[224:227]
mm a[224:227],v[164:167],v[52:55],a[224:227]
mm a[144:147],v[164:167],v[60:63],a[144:147]
mm a[144:147],v[160:163],v[56:59],a[144:147]
rd v[160:163],v236 offset:24576
rd v[164:167],v237 offset:24576
mm a[148:151],v[168:171],v[56:59],a[148:151]
mm a[148:151],v[172:175],v[60:63],a[148:151]
mm a[228:231],v[172:175],v[52:55],a[228:231]
mm a[228:231],v[168:171],v[48:51],a[228:231]
rt v[200:201],v241 offset:0
rt v[202:203],v241 offset:1024
mm a[140:143],v[168:171],v[40:43],a[140:143]
mm a[140:143],v[172:175],v[44:47],a[140:143]
mm a[124:127],v[172:175],v[36:39],a[124:127]
mm a[124:127],v[168:171],v[32:35],a[124:127]
rd v[168:171],v236 offset:26624
rd v[172:175],v237 offset:26624
mm a[216:219],v[176:179],v[32:35],a[216:219]
mm a[216:219],v[180:183],v[36:39],a[216:219]
mm a[168:171],v[180:183],v[44:47],a[168:171]
mm a[168:171],v[176:179],v[40:43],a[168:171]
rt v[204:205],v241 offset:16896
rt v[206:207],v241 offset:17920
mm a[192:195],v[176:179],v[48:51],a[192:195]
mm a[192:195],v[180:183],v[52:55],a[192:195]
mm a[176:179],v[180:183],v[60:63],a[176:179]
mm a[176:179],v[176:179],v[56:59],a[176:179]
rd v[176:179],v236 offset:16384
rd v[180:183],v237 offset:16384
mm a[180:183],v[184:187],v[56:59],a[180:183]
mm a[180:183],v[188:191],v[60:63],a[180:183]
mm a[196:199],v[188:191],v[52:55],a[196:199]
mm a[196:199],v[184:187],v[48:51],a[196:199]
rt v[208:209],v242 offset:0
rt v[210:211],v242 offset:1024
mm a[172:175],v[184:187],v[40:43],a[172:175]
s_add_u32 m0, s83, 33792
mm a[172:175],v[188:191],v[44:47],a[172:175]
ld v232,s[44:47],s80 offen lds
mm a[220:223],v[188:191],v[36:39],a[220:223]
mm a[220:223],v[184:187],v[32:35],a[220:223]
rd v[184:187],v236 offset:18432
rd v[188:191],v237 offset:18432
mm a[72:75],v[0:3],v[32:35],a[72:75]
s_add_u32 m0, s83, 34816
mm a[72:75],v[4:7],v[36:39],a[72:75]
ld v233,s[44:47],s80 offen lds
mm a[8:11],v[4:7],v[44:47],a[8:11]
mm a[8:11],v[0:3],v[40:43],a[8:11]
rt v[212:213],v242 offset:16896
rt v[214:215],v242 offset:17920
mm a[80:83],v[0:3],v[48:51],a[80:83]
s_add_u32 m0, s83, 35968
mm a[80:83],v[4:7],v[52:55],a[80:83]
ld v234,s[44:47],s80 offen lds
mm a[16:19],v[4:7],v[60:63],a[16:19]
mm a[16:19],v[0:3],v[56:59],a[16:19]
rd v[0:3],v236 offset:8192
rd v[4:7],v237 offset:8192
mm a[20:23],v[8:11],v[56:59],a[20:23]
s_add_u32 m0, s83, 36992
mm a[20:23],v[12:15],v[60:63],a[20:23]
ld v235,s[44:47],s80 offen lds
mm a[84:87],v[12:15],v[52:55],a[84:87]
s_mov_b32 s80, 1835008
mm a[84:87],v[8:11],v[48:51],a[84:87]
rt v[216:217],v243 offset:0
rt v[218:219],v243 offset:1024
mm a[12:15],v[8:11],v[40:43],a[12:15]
s_add_u32 m0, s83, 50688
mm a[12:15],v[12:15],v[44:47],a[12:15]
ld v232,s[44:47],s80 offen lds
mm a[76:79],v[12:15],v[36:39],a[76:79]
mm a[76:79],v[8:11],v[32:35],a[76:79]
rd v[8:11],v236 offset:10240
rd v[12:15],v237 offset:10240
mm a[248:251],v[16:19],v[32:35],a[248:251]
s_add_u32 m0, s83, 51712
mm a[248:251],v[20:23],v[36:39],a[248:251]
ld v233,s[44:47],s80 offen lds
mm a[40:43],v[20:23],v[44:47],a[40:43]
mm a[40:43],v[16:19],v[40:43],a[40:43]
rt v[220:221],v243 offset:16896
rt v[222:223],v243 offset:17920
mm a[48:51],v[16:19],v[48:51],a[48:51]
s_add_u32 m0, s83, 52864
mm a[48:51],v[20:23],v[52:55],a[48:51]
ld v234,s[44:47],s80 offen lds
mm a[56:59],v[20:23],v[60:63],a[56:59]
mm a[56:59],v[16:19],v[56:59],a[56:59]
rd v[16:19],v236 offset:0
rd v[20:23],v237 offset:0
mm a[60:63],v[24:27],v[56:59],a[60:63]
s_add_u32 m0, s83, 53888
mm a[60:63],v[28:31],v[60:63],a[60:63]
ld v235,s[44:47],s80 offen lds
mm a[52:55],v[28:31],v[52:55],a[52:55]
s_mov_b64 s[44:45], s[68:69]
s_mov_b32 s46, s70
mm a[52:55],v[24:27],v[48:51],a[52:55]
mm a[44:47],v[24:27],v[40:43],a[44:47]
mm a[44:47],v[28:31],v[44:47],a[44:47]
mm a[252:255],v[28:31],v[36:39],a[252:255]
wt lgkmcnt(2)
s_lshl_b32 s80, s77, 18
mm a[252:255],v[24:27],v[32:35],a[252:255]
rd v[24:27],v236 offset:2048
rd v[28:31],v237 offset:2048
mm a[96:99],v[160:163],v[192:195],a[96:99]
mm a[96:99],v[164:167],v[196:199],a[96:99]
mm a[104:107],v[164:167],v[204:207],a[104:107]
mm a[104:107],v[160:163],v[200:203],a[104:107]
rt v[32:33],v240 offset:128
rt v[34:35],v240 offset:1152
mm a[112:115],v[160:163],v[208:211],a[112:115]
mm a[112:115],v[164:167],v[212:215],a[112:115]
mm a[128:131],v[164:167],v[220:223],a[128:131]
wt lgkmcnt(2)
s_barrier
mm a[128:131],v[160:163],v[216:219],a[128:131]
mm a[132:135],v[168:171],v[216:219],a[132:135]
mm a[132:135],v[172:175],v[220:223],a[132:135]
mm a[116:119],v[172:175],v[212:215],a[116:119]
mm a[116:119],v[168:171],v[208:211],a[116:119]
rt v[36:37],v240 offset:17024
rt v[38:39],v240 offset:18048
mm a[108:111],v[168:171],v[200:203],a[108:111]
mm a[108:111],v[172:175],v[204:207],a[108:111]
mm a[100:103],v[172:175],v[196:199],a[100:103]
mm a[100:103],v[168:171],v[192:195],a[100:103]
mm a[232:235],v[176:179],v[192:195],a[232:235]
mm a[232:235],v[180:183],v[196:199],a[232:235]
mm a[152:155],v[180:183],v[204:207],a[152:155]
mm a[152:155],v[176:179],v[200:203],a[152:155]
rt v[40:41],v241 offset:128
rt v[42:43],v241 offset:1152
mm a[208:211],v[176:179],v[208:211],a[208:211]
mm a[208:211],v[180:183],v[212:215],a[208:211]
mm a[160:163],v[180:183],v[220:223],a[160:163]
mm a[160:163],v[176:179],v[216:219],a[160:163]
mm a[164:167],v[184:187],v[216:219],a[164:167]
mm a[164:167],v[188:191],v[220:223],a[164:167]
mm a[212:215],v[188:191],v[212:215],a[212:215]
mm a[212:215],v[184:187],v[208:211],a[212:215]
rt v[44:45],v241 offset:17024
rt v[46:47],v241 offset:18048
mm a[156:159],v[184:187],v[200:203],a[156:159]
mm a[156:159],v[188:191],v[204:207],a[156:159]
mm a[236:239],v[188:191],v[196:199],a[236:239]
mm a[236:239],v[184:187],v[192:195],a[236:239]
mm a[200:203],v[0:3],v[192:195],a[200:203]
mm a[200:203],v[4:7],v[196:199],a[200:203]
mm a[184:187],v[4:7],v[204:207],a[184:187]
mm a[184:187],v[0:3],v[200:203],a[184:187]
rt v[48:49],v242 offset:128
rt v[50:51],v242 offset:1152
mm a[64:67],v[0:3],v[208:211],a[64:67]
mm a[64:67],v[4:7],v[212:215],a[64:67]
mm a[0:3],v[4:7],v[220:223],a[0:3]
mm a[0:3],v[0:3],v[216:219],a[0:3]
mm a[4:7],v[8:11],v[216:219],a[4:7]
mm a[4:7],v[12:15],v[220:223],a[4:7]
buffer_load_dword v254, v135, s[68:71], s96 offen
mm a[68:71],v[12:15],v[212:215],a[68:71]
s_add_u32 s96, s96, 1048576
mm a[68:71],v[8:11],v[208:211],a[68:71]
rt v[52:53],v242 offset:17024
rt v[54:55],v242 offset:18048
mm a[188:191],v[8:11],v[200:203],a[188:191]
mm a[188:191],v[12:15],v[204:207],a[188:191]
buffer_load_dword v254, v135, s[68:71], s96 offen
mm a[204:207],v[12:15],v[196:199],a[204:207]
s_add_u32 s96, s96, 1048576
mm a[204:207],v[8:11],v[192:195],a[204:207]
rt v[56:57],v243 offset:128
rt v[58:59],v243 offset:1152
mm a[88:91],v[16:19],v[192:195],a[88:91]
mm a[88:91],v[20:23],v[196:199],a[88:91]
mm a[24:27],v[20:23],v[204:207],a[24:27]
mm a[24:27],v[16:19],v[200:203],a[24:27]
rt v[60:61],v243 offset:17024
rt v[62:63],v243 offset:18048
mm a[240:243],v[16:19],v[208:211],a[240:243]
mm a[240:243],v[20:23],v[212:215],a[240:243]
mm a[32:35],v[20:23],v[220:223],a[32:35]
mm a[32:35],v[16:19],v[216:219],a[32:35]
mm a[36:39],v[24:27],v[216:219],a[36:39]
mm a[36:39],v[28:31],v[220:223],a[36:39]
ld v[72:75],v255,s[64:67],s89 offen offset:1024 sc1
mm a[244:247],v[28:31],v[212:215],a[244:247]
mm a[244:247],v[24:27],v[208:211],a[244:247]
mm a[28:31],v[24:27],v[200:203],a[28:31]
mm a[28:31],v[28:31],v[204:207],a[28:31]
mm a[92:95],v[28:31],v[196:199],a[92:95]
wt vmcnt(3) lgkmcnt(0)
s_barrier
mm a[92:95],v[24:27],v[192:195],a[92:95]
mm a[120:123],v[160:163],v[32:35],a[120:123]
mm a[120:123],v[164:167],v[36:39],a[120:123]
mm a[136:139],v[164:167],v[44:47],a[136:139]
rt v[192:193],v240 offset:33792
rt v[194:195],v240 offset:34816
mm a[136:139],v[160:163],v[40:43],a[136:139]
rt v[196:197],v240 offset:50688
rt v[198:199],v240 offset:51712
mm a[224:227],v[160:163],v[48:51],a[224:227]
mm a[224:227],v[164:167],v[52:55],a[224:227]
mm a[144:147],v[164:167],v[60:63],a[144:147]
mm a[144:147],v[160:163],v[56:59],a[144:147]
rd v[160:163],v236 offset:57344
rd v[164:167],v237 offset:57344
mm a[148:151],v[168:171],v[56:59],a[148:151]
mm a[148:151],v[172:175],v[60:63],a[148:151]
mm a[228:231],v[172:175],v[52:55],a[228:231]
mm a[228:231],v[168:171],v[48:51],a[228:231]
rt v[200:201],v241 offset:33792
rt v[202:203],v241 offset:34816
mm a[140:143],v[168:171],v[40:43],a[140:143]
mm a[140:143],v[172:175],v[44:47],a[140:143]
mm a[124:127],v[172:175],v[36:39],a[124:127]
mm a[124:127],v[168:171],v[32:35],a[124:127]
rd v[168:171],v236 offset:59392
rd v[172:175],v237 offset:59392
mm a[216:219],v[176:179],v[32:35],a[216:219]
mm a[216:219],v[180:183],v[36:39],a[216:219]
mm a[168:171],v[180:183],v[44:47],a[168:171]
mm a[168:171],v[176:179],v[40:43],a[168:171]
rt v[204:205],v241 offset:50688
rt v[206:207],v241 offset:51712
mm a[192:195],v[176:179],v[48:51],a[192:195]
mm a[192:195],v[180:183],v[52:55],a[192:195]
mm a[176:179],v[180:183],v[60:63],a[176:179]
mm a[176:179],v[176:179],v[56:59],a[176:179]
rd v[176:179],v236 offset:49152
rd v[180:183],v237 offset:49152
mm a[180:183],v[184:187],v[56:59],a[180:183]
mm a[180:183],v[188:191],v[60:63],a[180:183]
mm a[196:199],v[188:191],v[52:55],a[196:199]
mm a[196:199],v[184:187],v[48:51],a[196:199]
rt v[208:209],v242 offset:33792
rt v[210:211],v242 offset:34816
mm a[172:175],v[184:187],v[40:43],a[172:175]
s_add_u32 m0, s83, 0
mm a[172:175],v[188:191],v[44:47],a[172:175]
ld v232,s[44:47],s80 offen lds
mm a[220:223],v[188:191],v[36:39],a[220:223]
mm a[220:223],v[184:187],v[32:35],a[220:223]
rd v[184:187],v236 offset:51200
rd v[188:191],v237 offset:51200
mm a[72:75],v[0:3],v[32:35],a[72:75]
s_add_u32 m0, s83, 1024
mm a[72:75],v[4:7],v[36:39],a[72:75]
ld v233,s[44:47],s80 offen lds
mm a[8:11],v[4:7],v[44:47],a[8:11]
mm a[8:11],v[0:3],v[40:43],a[8:11]
rt v[212:213],v242 offset:50688
rt v[214:215],v242 offset:51712
mm a[80:83],v[0:3],v[48:51],a[80:83]
s_add_u32 m0, s83, 2176
mm a[80:83],v[4:7],v[52:55],a[80:83]
ld v234,s[44:47],s80 offen lds
mm a[16:19],v[4:7],v[60:63],a[16:19]
mm a[16:19],v[0:3],v[56:59],a[16:19]
rd v[0:3],v236 offset:40960
rd v[4:7],v237 offset:40960
mm a[20:23],v[8:11],v[56:59],a[20:23]
s_add_u32 m0, s83, 3200
mm a[20:23],v[12:15],v[60:63],a[20:23]
ld v235,s[44:47],s80 offen lds
mm a[84:87],v[12:15],v[52:55],a[84:87]
s_add_u32 s80, s80, 524288
mm a[84:87],v[8:11],v[48:51],a[84:87]
rt v[216:217],v243 offset:33792
rt v[218:219],v243 offset:34816
mm a[12:15],v[8:11],v[40:43],a[12:15]
s_add_u32 m0, s83, 16896
mm a[12:15],v[12:15],v[44:47],a[12:15]
ld v232,s[44:47],s80 offen lds
mm a[76:79],v[12:15],v[36:39],a[76:79]
mm a[76:79],v[8:11],v[32:35],a[76:79]
rd v[8:11],v236 offset:43008
rd v[12:15],v237 offset:43008
mm a[248:251],v[16:19],v[32:35],a[248:251]
s_add_u32 m0, s83, 17920
mm a[248:251],v[20:23],v[36:39],a[248:251]
ld v233,s[44:47],s80 offen lds
mm a[40:43],v[20:23],v[44:47],a[40:43]
mm a[40:43],v[16:19],v[40:43],a[40:43]
rt v[220:221],v243 offset:50688
rt v[222:223],v243 offset:51712
mm a[48:51],v[16:19],v[48:51],a[48:51]
s_add_u32 m0, s83, 19072
mm a[48:51],v[20:23],v[52:55],a[48:51]
ld v234,s[44:47],s80 offen lds
mm a[56:59],v[20:23],v[60:63],a[56:59]
mm a[56:59],v[16:19],v[56:59],a[56:59]
rd v[16:19],v236 offset:32768
rd v[20:23],v237 offset:32768
mm a[60:63],v[24:27],v[56:59],a[60:63]
s_add_u32 m0, s83, 20096
mm a[60:63],v[28:31],v[60:63],a[60:63]
ld v235,s[44:47],s80 offen lds
mm a[52:55],v[28:31],v[52:55],a[52:55]
s_add_u32 s80, s80, 524288
mm a[52:55],v[24:27],v[48:51],a[52:55]
mm a[44:47],v[24:27],v[40:43],a[44:47]
mm a[44:47],v[28:31],v[44:47],a[44:47]
mm a[252:255],v[28:31],v[36:39],a[252:255]
wt lgkmcnt(2)
mm a[252:255],v[24:27],v[32:35],a[252:255]
rd v[24:27],v236 offset:34816
rd v[28:31],v237 offset:34816
mm a[96:99],v[160:163],v[192:195],a[96:99]
mm a[96:99],v[164:167],v[196:199],a[96:99]
mm a[104:107],v[164:167],v[204:207],a[104:107]
mm a[104:107],v[160:163],v[200:203],a[104:107]
rt v[32:33],v240 offset:33920
rt v[34:35],v240 offset:34944
mm a[112:115],v[160:163],v[208:211],a[112:115]
mm a[112:115],v[164:167],v[212:215],a[112:115]
mm a[128:131],v[164:167],v[220:223],a[128:131]
wt lgkmcnt(2)
s_barrier
mm a[128:131],v[160:163],v[216:219],a[128:131]
mm a[132:135],v[168:171],v[216:219],a[132:135]
wt vmcnt(8)
v_cmp_eq_u64 vcc, v[72:73], v[252:253]
mm a[132:135],v[172:175],v[220:223],a[132:135]
v_cmp_eq_u64 s[96:97], v[74:75], v[252:253]
mm a[116:119],v[172:175],v[212:215],a[116:119]
s_and_b64 vcc, vcc, s[96:97]
s_cmp_eq_u64 vcc, -1
s_cbranch_scc0 .La5_LV_XSPIN
.La5_LV_XOK:
mm a[116:119],v[168:171],v[208:211],a[116:119]
rt v[36:37],v240 offset:50816
rt v[38:39],v240 offset:51840
mm a[108:111],v[168:171],v[200:203],a[108:111]
s_mov_b32 s89, 49152
mm a[108:111],v[172:175],v[204:207],a[108:111]
mm a[100:103],v[172:175],v[196:199],a[100:103]
mm a[100:103],v[168:171],v[192:195],a[100:103]
mm a[232:235],v[176:179],v[192:195],a[232:235]
mm a[232:235],v[180:183],v[196:199],a[232:235]
ld v[128:131],v244,s[52:55],s91 offen offset:0 sc1
mm a[152:155],v[180:183],v[204:207],a[152:155]
ar v224,a96
ar v225,a97
mm a[152:155],v[176:179],v[200:203],a[152:155]
rt v[40:41],v241 offset:33920
rt v[42:43],v241 offset:34944
mm a[208:211],v[176:179],v[208:211],a[208:211]
ar v226,a98
ar v227,a99
mm a[208:211],v[180:183],v[212:215],a[208:211]
ld v[132:135],v245,s[52:55],s91 offen offset:0 sc1
mm a[160:163],v[180:183],v[220:223],a[160:163]
ar v228,a100
ar v229,a101
mm a[160:163],v[176:179],v[216:219],a[160:163]
ar v230,a102
ar v231,a103
mm a[164:167],v[184:187],v[216:219],a[164:167]
cv v248,v224,v225
cv v249,v226,v227
mm a[164:167],v[188:191],v[220:223],a[164:167]
ld v[136:139],v246,s[52:55],s91 offen offset:0 sc1
mm a[212:215],v[188:191],v[212:215],a[212:215]
cv v250,v228,v229
cv v251,v230,v231
mm a[212:215],v[184:187],v[208:211],a[212:215]
rt v[44:45],v241 offset:50816
rt v[46:47],v241 offset:51840
mm a[156:159],v[184:187],v[200:203],a[156:159]
st v[248:251],v255,s[48:51],s89 offen sc1
mm a[156:159],v[188:191],v[204:207],a[156:159]
ld v[140:143],v247,s[52:55],s91 offen offset:0 sc1
mm a[236:239],v[188:191],v[196:199],a[236:239]
s_mov_b32 s89, 50176
ds_write_b128 v238, v[248:251] offset:0
mm a[236:239],v[184:187],v[192:195],a[236:239]
ar v224,a104
ar v225,a105
mm a[200:203],v[0:3],v[192:195],a[200:203]
ar v226,a106
ar v227,a107
mm a[200:203],v[4:7],v[196:199],a[200:203]
ld v[144:147],v244,s[52:55],s91 offen offset:1024 sc1
mm a[184:187],v[4:7],v[204:207],a[184:187]
ar v228,a108
ar v229,a109
mm a[184:187],v[0:3],v[200:203],a[184:187]
rt v[48:49],v242 offset:33920
rt v[50:51],v242 offset:34944
mm a[64:67],v[0:3],v[208:211],a[64:67]
ar v230,a110
ar v231,a111
mm a[64:67],v[4:7],v[212:215],a[64:67]
ld v[148:151],v245,s[52:55],s91 offen offset:1024 sc1
mm a[0:3],v[4:7],v[220:223],a[0:3]
cv v248,v224,v225
cv v249,v226,v227
mm a[0:3],v[0:3],v[216:219],a[0:3]
cv v250,v228,v229
cv v251,v230,v231
mm a[4:7],v[8:11],v[216:219],a[4:7]
st v[248:251],v255,s[48:51],s89 offen sc1
mm a[4:7],v[12:15],v[220:223],a[4:7]
ld v[152:155],v246,s[52:55],s91 offen offset:1024 sc1
mm a[68:71],v[12:15],v[212:215],a[68:71]
s_mov_b32 s89, 51200
ds_write_b128 v238, v[248:251] offset:1024
mm a[68:71],v[8:11],v[208:211],a[68:71]
rt v[52:53],v242 offset:50816
rt v[54:55],v242 offset:51840
mm a[188:191],v[8:11],v[200:203],a[188:191]
ar v224,a112
ar v225,a113
mm a[188:191],v[12:15],v[204:207],a[188:191]
ld v[156:159],v247,s[52:55],s91 offen offset:1024 sc1
mm a[204:207],v[12:15],v[196:199],a[204:207]
ar v226,a114
ar v227,a115
mm a[204:207],v[8:11],v[192:195],a[204:207]
rt v[56:57],v243 offset:33920
rt v[58:59],v243 offset:34944
mm a[88:91],v[16:19],v[192:195],a[88:91]
ar v228,a116
ar v229,a117
mm a[88:91],v[20:23],v[196:199],a[88:91]
ar v230,a118
ar v231,a119
mm a[24:27],v[20:23],v[204:207],a[24:27]
cv v248,v224,v225
cv v249,v226,v227
mm a[24:27],v[16:19],v[200:203],a[24:27]
rt v[60:61],v243 offset:50816
rt v[62:63],v243 offset:51840
mm a[240:243],v[16:19],v[208:211],a[240:243]
v_subrev_u32 v240, 8448, v240
v_subrev_u32 v241, 8448, v241
mm a[240:243],v[20:23],v[212:215],a[240:243]
v_subrev_u32 v242, 8448, v242
v_subrev_u32 v243, 8448, v243
mm a[32:35],v[20:23],v[220:223],a[32:35]
.La5_LW1_8:
s_cbranch_execz .La5_LW1_12
cv v250,v228,v229
cv v251,v230,v231
mm a[32:35],v[16:19],v[216:219],a[32:35]
st v[248:251],v255,s[48:51],s89 offen sc1
mm a[36:39],v[24:27],v[216:219],a[36:39]
s_mov_b32 s89, 52224
ds_write_b128 v238, v[248:251] offset:2048
mm a[36:39],v[28:31],v[220:223],a[36:39]
mm a[244:247],v[28:31],v[212:215],a[244:247]
mm a[244:247],v[24:27],v[208:211],a[244:247]
mm a[28:31],v[24:27],v[200:203],a[28:31]
mm a[28:31],v[28:31],v[204:207],a[28:31]
mm a[92:95],v[28:31],v[196:199],a[92:95]
wt vmcnt(11) lgkmcnt(1)
s_barrier
mm a[92:95],v[24:27],v[192:195],a[92:95]
mm a[120:123],v[160:163],v[32:35],a[120:123]
s_add_u32 m0, s83, 33792
mm a[120:123],v[164:167],v[36:39],a[120:123]
ld v232,s[44:47],s80 offen lds
mm a[136:139],v[164:167],v[44:47],a[136:139]
mm a[136:139],v[160:163],v[40:43],a[136:139]
rt v[192:193],v240 offset:0
rt v[194:195],v240 offset:1024
mm a[224:227],v[160:163],v[48:51],a[224:227]
s_add_u32 m0, s83, 34816
mm a[224:227],v[164:167],v[52:55],a[224:227]
ld v233,s[44:47],s80 offen lds
mm a[144:147],v[164:167],v[60:63],a[144:147]
mm a[144:147],v[160:163],v[56:59],a[144:147]
rt v[196:197],v240 offset:8448
rt v[198:199],v240 offset:9472
mm a[148:151],v[168:171],v[56:59],a[148:151]
s_add_u32 m0, s83, 35968
mm a[148:151],v[172:175],v[60:63],a[148:151]
ld v234,s[44:47],s80 offen lds
mm a[228:231],v[172:175],v[52:55],a[228:231]
mm a[228:231],v[168:171],v[48:51],a[228:231]
rt v[200:201],v241 offset:0
rt v[202:203],v241 offset:1024
mm a[140:143],v[168:171],v[40:43],a[140:143]
s_add_u32 m0, s83, 36992
mm a[140:143],v[172:175],v[44:47],a[140:143]
ld v235,s[44:47],s80 offen lds
mm a[124:127],v[172:175],v[36:39],a[124:127]
s_add_u32 s80, s80, 524288
mm a[124:127],v[168:171],v[32:35],a[124:127]
rt v[204:205],v241 offset:8448
rt v[206:207],v241 offset:9472
mm a[216:219],v[176:179],v[32:35],a[216:219]
s_add_u32 m0, s83, 50688
mm a[216:219],v[180:183],v[36:39],a[216:219]
ld v232,s[44:47],s80 offen lds
mm a[168:171],v[180:183],v[44:47],a[168:171]
mm a[168:171],v[176:179],v[40:43],a[168:171]
rt v[208:209],v242 offset:0
rt v[210:211],v242 offset:1024
mm a[192:195],v[176:179],v[48:51],a[192:195]
s_add_u32 m0, s83, 51712
mm a[192:195],v[180:183],v[52:55],a[192:195]
ld v233,s[44:47],s80 offen lds
mm a[176:179],v[180:183],v[60:63],a[176:179]
mm a[176:179],v[176:179],v[56:59],a[176:179]
rt v[212:213],v242 offset:8448
rt v[214:215],v242 offset:9472
mm a[180:183],v[184:187],v[56:59],a[180:183]
s_add_u32 m0, s83, 52864
mm a[180:183],v[188:191],v[60:63],a[180:183]
ld v234,s[44:47],s80 offen lds
mm a[196:199],v[188:191],v[52:55],a[196:199]
mm a[196:199],v[184:187],v[48:51],a[196:199]
rt v[216:217],v243 offset:0
rt v[218:219],v243 offset:1024
mm a[172:175],v[184:187],v[40:43],a[172:175]
s_add_u32 m0, s83, 53888
mm a[172:175],v[188:191],v[44:47],a[172:175]
ld v235,s[44:47],s80 offen lds
mm a[220:223],v[188:191],v[36:39],a[220:223]
s_add_u32 s80, s80, 524288
mm a[220:223],v[184:187],v[32:35],a[220:223]
rt v[220:221],v243 offset:8448
rt v[222:223],v243 offset:9472
mm a[72:75],v[0:3],v[32:35],a[72:75]
mm a[72:75],v[4:7],v[36:39],a[72:75]
ld v[160:163],v244,s[52:55],s91 offen offset:2048 sc1
mm a[8:11],v[4:7],v[44:47],a[8:11]
mm a[8:11],v[0:3],v[40:43],a[8:11]
mm a[80:83],v[0:3],v[48:51],a[80:83]
mm a[80:83],v[4:7],v[52:55],a[80:83]
ld v[164:167],v245,s[52:55],s91 offen offset:2048 sc1
mm a[16:19],v[4:7],v[60:63],a[16:19]
mm a[16:19],v[0:3],v[56:59],a[16:19]
mm a[20:23],v[8:11],v[56:59],a[20:23]
mm a[20:23],v[12:15],v[60:63],a[20:23]
ld v[168:171],v246,s[52:55],s91 offen offset:2048 sc1
mm a[84:87],v[12:15],v[52:55],a[84:87]
mm a[84:87],v[8:11],v[48:51],a[84:87]
mm a[12:15],v[8:11],v[40:43],a[12:15]
mm a[12:15],v[12:15],v[44:47],a[12:15]
ld v[172:175],v247,s[52:55],s91 offen offset:2048 sc1
mm a[76:79],v[12:15],v[36:39],a[76:79]
mm a[76:79],v[8:11],v[32:35],a[76:79]
mm a[248:251],v[16:19],v[32:35],a[248:251]
mm a[248:251],v[20:23],v[36:39],a[248:251]
mm a[40:43],v[20:23],v[44:47],a[40:43]
mm a[40:43],v[16:19],v[40:43],a[40:43]
mm a[48:51],v[16:19],v[48:51],a[48:51]
mm a[48:51],v[20:23],v[52:55],a[48:51]
mm a[56:59],v[20:23],v[60:63],a[56:59]
mm a[56:59],v[16:19],v[56:59],a[56:59]
mm a[60:63],v[24:27],v[56:59],a[60:63]
mm a[60:63],v[28:31],v[60:63],a[60:63]
mm a[52:55],v[28:31],v[52:55],a[52:55]
mm a[52:55],v[24:27],v[48:51],a[52:55]
mm a[44:47],v[24:27],v[40:43],a[44:47]
mm a[44:47],v[28:31],v[44:47],a[44:47]
mm a[252:255],v[28:31],v[36:39],a[252:255]
mm a[252:255],v[24:27],v[32:35],a[252:255]
; ---- Q tile: 256 rows x 128 columns, K = 1024, while K / V leave the accumulators (memory + .La5_LDS ring); behind it the first step on the diagonal
.La5_LQ_START:
.La5_LW1_9:
s_cbranch_execz .La5_LW1_13
wt vmcnt(13) lgkmcnt(4)
mm v[0:3],v[192:195],v[128:131],0
ar v224,a128
ar v225,a129
s_mov_b32 s85, s95
mm v[0:3],v[196:199],v[144:147],v[0:3]
ld v[176:179],v244,s[52:55],s91 offen offset:3072 sc1
mm v[32:35],v[196:199],v[148:151],0
ar v226,a130
ar v227,a131
mm v[32:35],v[192:195],v[132:135],v[32:35]
ar v228,a132
ar v229,a133
mm v[64:67],v[192:195],v[136:139],0
ar v230,a134
ar v231,a135
mm v[64:67],v[196:199],v[152:155],v[64:67]
ld v[180:183],v245,s[52:55],s91 offen offset:3072 sc1
mm v[96:99],v[196:199],v[156:159],0
cv v248,v224,v225
cv v249,v226,v227
mm v[96:99],v[192:195],v[140:143],v[96:99]
rt v[196:197],v240 offset:128
rt v[198:199],v240 offset:1152
mm v[100:103],v[200:203],v[140:143],0
cv v250,v228,v229
cv v251,v230,v231
mm v[100:103],v[204:207],v[156:159],v[100:103]
ld v[184:187],v246,s[52:55],s91 offen offset:3072 sc1
mm v[68:71],v[204:207],v[152:155],0
aw a128,v248
aw a129,v249
mm v[68:71],v[200:203],v[136:139],v[68:71]
rt v[192:193],v240 offset:8576
rt v[194:195],v240 offset:9600
mm v[36:39],v[200:203],v[132:135],0
aw a130,v250
aw a131,v251
mm v[36:39],v[204:207],v[148:151],v[36:39]
ld v[188:191],v247,s[52:55],s91 offen offset:3072 sc1
mm v[4:7],v[204:207],v[144:147],0
wt lgkmcnt(4)
s_add_u32 s91, s91, 4096
mm v[4:7],v[200:203],v[128:131],v[4:7]
rt v[204:205],v241 offset:128
rt v[206:207],v241 offset:1152
mm v[8:11],v[208:211],v[128:131],0
s_and_b32 s97, s2, 7
s_mul_i32 s95, s97, 7
mm v[8:11],v[212:215],v[144:147],v[8:11]
ld a[96:99],v244,s[52:55],s91 offen offset:0 sc1
mm v[40:43],v[212:215],v[148:151],0
s_lshr_b32 s96, s30, 7
s_add_u32 s95, s95, s96
mm v[40:43],v[208:211],v[132:135],v[40:43]
rt v[200:201],v241 offset:8576
rt v[202:203],v241 offset:9600
mm v[72:75],v[208:211],v[136:139],0
s_sub_u32 s95, 3, s95
s_and_b32 s95, s95, 7
mm v[72:75],v[212:215],v[152:155],v[72:75]
ld a[100:103],v245,s[52:55],s91 offen offset:0 sc1
mm v[104:107],v[212:215],v[156:159],0
s_min_u32 s95, s95, 6
s_lshl_b32 s95, s95, 1
mm v[104:107],v[208:211],v[140:143],v[104:107]
rt v[212:213],v242 offset:128
rt v[214:215],v242 offset:1152
mm v[108:111],v[216:219],v[140:143],0
s_lshr_b32 s92, s2, 6
s_mul_i32 s93, s92, 3
mm v[108:111],v[220:223],v[156:159],v[108:111]
ld a[104:107],v246,s[52:55],s91 offen offset:0 sc1
mm v[76:79],v[220:223],v[152:155],0
s_add_u32 s93, s93, s74
mm v[76:79],v[216:219],v[136:139],v[76:79]
rt v[208:209],v242 offset:8576
rt v[210:211],v242 offset:9600
mm v[44:47],v[216:219],v[132:135],0
ds_write_b128 v238, v[248:251] offset:3072
mm v[44:47],v[220:223],v[148:151],v[44:47]
st v[248:251],v255,s[48:51],s89 offen offset:0 sc1
mm v[12:15],v[220:223],v[144:147],0
wt lgkmcnt(5)
mm v[12:15],v[216:219],v[128:131],v[12:15]
rt v[220:221],v243 offset:128
rt v[222:223],v243 offset:1152
mm v[16:19],v[196:199],v[128:131],0
ar v224,a120
ar v225,a121
mm v[16:19],v[192:195],v[144:147],v[16:19]
ld a[108:111],v247,s[52:55],s91 offen offset:0 sc1
mm v[48:51],v[192:195],v[148:151],0
ar v226,a122
ar v227,a123
mm v[48:51],v[196:199],v[132:135],v[48:51]
rt v[216:217],v243 offset:8576
rt v[218:219],v243 offset:9600
mm v[80:83],v[196:199],v[136:139],0
ar v228,a124
ar v229,a125
mm v[80:83],v[192:195],v[152:155],v[80:83]
ld a[112:115],v244,s[52:55],s91 offen offset:1024 sc1
mm v[112:115],v[192:195],v[156:159],0
ar v230,a126
ar v231,a127
mm v[112:115],v[196:199],v[140:143],v[112:115]
cv v248,v224,v225
cv v249,v226,v227
mm v[116:119],v[204:207],v[140:143],0
wt lgkmcnt(0)
s_barrier
mm v[116:119],v[200:203],v[156:159],v[116:119]
ld a[116:119],v245,s[52:55],s91 offen offset:1024 sc1
mm v[84:87],v[200:203],v[152:155],0
rt v[192:193],v240 offset:16896
rt v[194:195],v240 offset:17920
mm v[84:87],v[204:207],v[136:139],v[84:87]
rt v[196:197],v240 offset:25344
rt v[198:199],v240 offset:26368
mm v[52:55],v[204:207],v[132:135],0
s_add_u32 m0, s83, 0
cv v250,v228,v229
mm v[52:55],v[200:203],v[148:151],v[52:55]
ld v232,s[44:47],s80 offen lds
mm v[20:23],v[200:203],v[144:147],0
cv v251,v230,v231
aw a132,v248
mm v[20:23],v[204:207],v[128:131],v[20:23]
rt v[200:201],v241 offset:16896
rt v[202:203],v241 offset:17920
mm v[24:27],v[212:215],v[128:131],0
aw a133,v249
aw a134,v250
mm v[24:27],v[208:211],v[144:147],v[24:27]
aw a135,v251
mm v[56:59],v[208:211],v[148:151],0
s_cmp_ge_u32 s93, s95
s_cselect_b32 s96, 2, 0
mm v[56:59],v[212:215],v[132:135],v[56:59]
rt v[204:205],v241 offset:25344
rt v[206:207],v241 offset:26368
mm v[88:91],v[212:215],v[136:139],0
s_add_u32 m0, s83, 1024
s_add_u32 s93, s93, s96
mm v[88:91],v[208:211],v[152:155],v[88:91]
ld v233,s[44:47],s80 offen lds
mm v[120:123],v[208:211],v[156:159],0
s_add_u32 s95, s95, s92
mm v[120:123],v[212:215],v[140:143],v[120:123]
rt v[208:209],v242 offset:16896
rt v[210:211],v242 offset:17920
mm v[124:127],v[220:223],v[140:143],0
s_cmp_lt_u32 s92, 2
s_cselect_b32 s94, s75, 0
mm v[124:127],v[216:219],v[156:159],v[124:127]
ld v[140:143],v246,s[52:55],s91 offen offset:1024 sc1
mm v[92:95],v[216:219],v[152:155],0
s_cmp_eq_u32 s74, 3
s_cselect_b32 s92, s95, s93
s_cselect_b32 s94, s94, s75
mm v[92:95],v[220:223],v[136:139],v[92:95]
rt v[212:213],v242 offset:25344
rt v[214:215],v242 offset:26368
mm v[60:63],v[220:223],v[132:135],0
ds_write_b128 v238, v[248:251] offset:4096
mm v[60:63],v[216:219],v[148:151],v[60:63]
s_mul_i32 s93, s97, 14
s_add_u32 s92, s92, s93
mm v[28:31],v[216:219],v[144:147],0
wt vmcnt(10) lgkmcnt(5)
s_mul_i32 s96, s92, s94
mm v[28:31],v[220:223],v[128:131],v[28:31]
rt v[216:217],v243 offset:16896
rt v[218:219],v243 offset:17920
mm v[0:3],v[192:195],v[160:163],v[0:3]
ar v224,a136
ar v225,a137
mm v[0:3],v[196:199],v[176:179],v[0:3]
ld v[128:131],v247,s[52:55],s91 offen offset:1024 sc1
mm v[32:35],v[196:199],v[180:183],v[32:35]
ar v226,a138
ar v227,a139
mm v[32:35],v[192:195],v[164:167],v[32:35]
rt v[220:221],v243 offset:25344
rt v[222:223],v243 offset:26368
mm v[64:67],v[192:195],v[168:171],v[64:67]
ar v228,a140
ar v229,a141
mm v[64:67],v[196:199],v[184:187],v[64:67]
ld v[132:135],v244,s[52:55],s91 offen offset:2048 sc1
mm v[96:99],v[196:199],v[188:191],v[96:99]
ar v230,a142
ar v231,a143
mm v[96:99],v[192:195],v[172:175],v[96:99]
rt v[196:197],v240 offset:17024
rt v[198:199],v240 offset:18048
mm v[100:103],v[200:203],v[172:175],v[100:103]
s_add_u32 m0, s83, 2176
cv v248,v224,v225
mm v[100:103],v[204:207],v[188:191],v[100:103]
ld v234,s[44:47],s80 offen lds
mm v[68:71],v[204:207],v[184:187],v[68:71]
cv v249,v226,v227
cv v250,v228,v229
mm v[68:71],v[200:203],v[168:171],v[68:71]
rt v[192:193],v240 offset:25472
rt v[194:195],v240 offset:26496
mm v[36:39],v[200:203],v[164:167],v[36:39]
cv v251,v230,v231
aw a136,v248
mm v[36:39],v[204:207],v[180:183],v[36:39]
ld v[136:139],v245,s[52:55],s91 offen offset:2048 sc1
mm v[4:7],v[204:207],v[176:179],v[4:7]
wt lgkmcnt(4)
aw a137,v249
mm v[4:7],v[200:203],v[160:163],v[4:7]
rt v[204:205],v241 offset:17024
rt v[206:207],v241 offset:18048
mm v[8:11],v[208:211],v[160:163],v[8:11]
s_add_u32 m0, s83, 3200
aw a138,v250
mm v[8:11],v[212:215],v[176:179],v[8:11]
ld v235,s[44:47],s80 offen lds
mm v[40:43],v[212:215],v[180:183],v[40:43]
s_add_u32 s80, s80, 524288
aw a139,v251
mm v[40:43],v[208:211],v[164:167],v[40:43]
rt v[200:201],v241 offset:25472
rt v[202:203],v241 offset:26496
mm v[72:75],v[208:211],v[168:171],v[72:75]
s_mov_b32 s100, s96
s_and_b32 s93, s2, 7
mm v[72:75],v[212:215],v[184:187],v[72:75]
ld v[144:147],v246,s[52:55],s91 offen offset:2048 sc1
mm v[104:107],v[212:215],v[188:191],v[104:107]
s_lshr_b32 s95, s93, 1
s_xor_b32 s97, s95, s93
mm v[104:107],v[208:211],v[172:175],v[104:107]
rt v[212:213],v242 offset:17024
rt v[214:215],v242 offset:18048
mm v[108:111],v[216:219],v[172:175],v[108:111]
s_lshl_b32 s95, s95, 21
s_and_b32 s97, s97, 1
mm v[108:111],v[220:223],v[188:191],v[108:111]
ld v[148:151],v247,s[52:55],s91 offen offset:2048 sc1
mm v[76:79],v[220:223],v[184:187],v[76:79]
s_mov_b32 s18, 53248
s_lshl_b32 s97, s97, 10
mm v[76:79],v[216:219],v[168:171],v[76:79]
rt v[208:209],v242 offset:25472
rt v[210:211],v242 offset:26496
mm v[44:47],v[216:219],v[164:167],v[44:47]
ds_write_b128 v238, v[248:251] offset:5120
mm v[44:47],v[220:223],v[180:183],v[44:47]
st a[132:135],v255,s[48:51],s18 offen offset:0 sc1
mm v[12:15],v[220:223],v[176:179],v[12:15]
wt lgkmcnt(5)
s_add_u32 s95, s95, s97
mm v[12:15],v[216:219],v[160:163],v[12:15]
rt v[220:221],v243 offset:17024
rt v[222:223],v243 offset:18048
mm v[16:19],v[196:199],v[160:163],v[16:19]
ar v224,a224
ar v225,a225
mm v[16:19],v[192:195],v[176:179],v[16:19]
ld v[152:155],v244,s[52:55],s91 offen offset:3072 sc1
mm v[48:51],v[192:195],v[180:183],v[48:51]
ar v226,a226
ar v227,a227
mm v[48:51],v[196:199],v[164:167],v[48:51]
rt v[216:217],v243 offset:25472
rt v[218:219],v243 offset:26496
mm v[80:83],v[196:199],v[168:171],v[80:83]
ar v228,a228
ar v229,a229
mm v[80:83],v[192:195],v[184:187],v[80:83]
ld v[156:159],v245,s[52:55],s91 offen offset:3072 sc1
mm v[112:115],v[192:195],v[188:191],v[112:115]
ar v230,a230
ar v231,a231
mm v[112:115],v[196:199],v[172:175],v[112:115]
cv v248,v224,v225
cv v249,v226,v227
mm v[116:119],v[204:207],v[172:175],v[116:119]
wt lgkmcnt(0)
s_barrier
mm v[116:119],v[200:203],v[188:191],v[116:119]
ld a[120:123],v246,s[52:55],s91 offen offset:3072 sc1
mm v[84:87],v[200:203],v[184:187],v[84:87]
rt v[192:193],v240 offset:33792
rt v[194:195],v240 offset:34816
mm v[84:87],v[204:207],v[168:171],v[84:87]
rt v[196:197],v240 offset:42240
rt v[198:199],v240 offset:43264
mm v[52:55],v[204:207],v[164:167],v[52:55]
s_add_u32 m0, s83, 16896
cv v250,v228,v229
mm v[52:55],v[200:203],v[180:183],v[52:55]
ld v232,s[44:47],s80 offen lds
mm v[20:23],v[200:203],v[176:179],v[20:23]
cv v251,v230,v231
aw a140,v248
mm v[20:23],v[204:207],v[160:163],v[20:23]
rt v[200:201],v241 offset:33792
rt v[202:203],v241 offset:34816
mm v[24:27],v[212:215],v[160:163],v[24:27]
aw a141,v249
aw a142,v250
mm v[24:27],v[208:211],v[176:179],v[24:27]
ld a[124:127],v247,s[52:55],s91 offen offset:3072 sc1
mm v[56:59],v[208:211],v[180:183],v[56:59]
s_add_u32 s91, s91, 4096
aw a143,v251
mm v[56:59],v[212:215],v[164:167],v[56:59]
rt v[204:205],v241 offset:42240
rt v[206:207],v241 offset:43264
mm v[88:91],v[212:215],v[168:171],v[88:91]
s_add_u32 m0, s83, 17920
s_mul_i32 s97, s93, 896
mm v[88:91],v[208:211],v[184:187],v[88:91]
ld v233,s[44:47],s80 offen lds
mm v[120:123],v[208:211],v[188:191],v[120:123]
s_cmp_eq_u32 s3, 0
s_cselect_b32 s95, s95, s97
s_cselect_b64 s[36:37], s[28:29], s[30:31]
s_cselect_b32 s21, 2, 7
s_cselect_b32 s93, s93, 0
mm v[120:123],v[212:215],v[172:175],v[120:123]
rt v[208:209],v242 offset:33792
rt v[210:211],v242 offset:34816
mm v[124:127],v[220:223],v[172:175],v[124:127]
s_cmp_eq_u32 s3, 2
s_cselect_b64 s[36:37], s[32:33], s[36:37]
mm v[124:127],v[216:219],v[188:191],v[124:127]
s_lshl_b32 s97, s97, 10
s_mov_b32 s92, 672
mm v[92:95],v[216:219],v[184:187],v[92:95]
s_mov_b32 s90, 352
mm v[92:95],v[220:223],v[168:171],v[92:95]
rt v[212:213],v242 offset:42240
rt v[214:215],v242 offset:43264
mm v[60:63],v[220:223],v[164:167],v[60:63]
ds_write_b128 v238, v[248:251] offset:6144
mm v[60:63],v[216:219],v[180:183],v[60:63]
st a[136:139],v255,s[48:51],s18 offen offset:1024 sc1
mm v[28:31],v[216:219],v[176:179],v[28:31]
wt vmcnt(14) lgkmcnt(5)
mm v[28:31],v[220:223],v[160:163],v[28:31]
rt v[216:217],v243 offset:33792
rt v[218:219],v243 offset:34816
mm v[0:3],v[192:195],a[96:99],v[0:3]
ar v224,a144
ar v225,a145
mm v[0:3],v[196:199],a[112:115],v[0:3]
ld v[160:163],v244,s[52:55],s91 offen offset:0 sc1
mm v[32:35],v[196:199],a[116:119],v[32:35]
ar v226,a146
ar v227,a147
mm v[32:35],v[192:195],a[100:103],v[32:35]
rt v[220:221],v243 offset:42240
rt v[222:223],v243 offset:43264
mm v[64:67],v[192:195],a[104:107],v[64:67]
ar v228,a148
ar v229,a149
mm v[64:67],v[196:199],v[140:143],v[64:67]
ld v[164:167],v245,s[52:55],s91 offen offset:0 sc1
mm v[96:99],v[196:199],v[128:131],v[96:99]
ar v230,a150
ar v231,a151
mm v[96:99],v[192:195],a[108:111],v[96:99]
rt v[196:197],v240 offset:33920
rt v[198:199],v240 offset:34944
mm v[100:103],v[200:203],a[108:111],v[100:103]
s_add_u32 m0, s83, 19072
cv v248,v224,v225
mm v[100:103],v[204:207],v[128:131],v[100:103]
ld v234,s[44:47],s80 offen lds
mm v[68:71],v[204:207],v[140:143],v[68:71]
cv v249,v226,v227
cv v250,v228,v229
mm v[68:71],v[200:203],a[104:107],v[68:71]
rt v[192:193],v240 offset:42368
rt v[194:195],v240 offset:43392
mm v[36:39],v[200:203],a[100:103],v[36:39]
cv v251,v230,v231
aw a144,v248
mm v[36:39],v[204:207],a[116:119],v[36:39]
ld v[168:171],v246,s[52:55],s91 offen offset:0 sc1
mm v[4:7],v[204:207],a[112:115],v[4:7]
wt lgkmcnt(4)
aw a145,v249
mm v[4:7],v[200:203],a[96:99],v[4:7]
rt v[204:205],v241 offset:33920
rt v[206:207],v241 offset:34944
mm v[8:11],v[208:211],a[96:99],v[8:11]
s_add_u32 m0, s83, 20096
aw a146,v250
mm v[8:11],v[212:215],a[112:115],v[8:11]
ld v235,s[44:47],s80 offen lds
mm v[40:43],v[212:215],a[116:119],v[40:43]
s_add_u32 s80, s80, 524288
aw a147,v251
mm v[40:43],v[208:211],a[100:103],v[40:43]
rt v[200:201],v241 offset:42368
rt v[202:203],v241 offset:43392
mm v[72:75],v[208:211],a[104:107],v[72:75]
s_cmp_eq_u32 s3, 3
s_cselect_b32 s95, s97, s95
s_cselect_b64 s[36:37], s[34:35], s[36:37]
s_cselect_b32 s21, 1, s21
s_cselect_b32 s92, 544, s92
mm v[72:75],v[212:215],v[140:143],v[72:75]
ld v[172:175],v247,s[52:55],s91 offen offset:0 sc1
mm v[104:107],v[212:215],v[128:131],v[104:107]
s_lshl_b32 s21, s21, 10
s_and_b32 s7, s93, 1
mm v[104:107],v[208:211],a[108:111],v[104:107]
rt v[212:213],v242 offset:33920
rt v[214:215],v242 offset:34944
mm v[108:111],v[216:219],a[108:111],v[108:111]
s_cmp_eq_u32 s75, 1
s_cselect_b32 s97, s90, s92
s_cselect_b32 s92, s92, s90
mm v[108:111],v[220:223],v[128:131],v[108:111]
.La5_LW1_10:
s_cbranch_execz .La5_LW1_14
s_cmp_eq_u32 s75, s7
s_cselect_b32 s92, s92, 0
mm v[76:79],v[220:223],v[140:143],v[76:79]
s_mul_i32 s92, s92, s21
s_add_u32 s95, s95, s92
mm v[76:79],v[216:219],a[104:107],v[76:79]
rt v[208:209],v242 offset:42368
rt v[210:211],v242 offset:43392
mm v[44:47],v[216:219],a[100:103],v[44:47]
ds_write_b128 v238, v[248:251] offset:7168
mm v[44:47],v[220:223],a[116:119],v[44:47]
st a[140:143],v255,s[48:51],s18 offen offset:2048 sc1
mm v[12:15],v[220:223],a[112:115],v[12:15]
wt lgkmcnt(5)
s_mul_i32 s97, s97, s21
mm v[12:15],v[216:219],a[96:99],v[12:15]
rt v[220:221],v243 offset:33920
rt v[222:223],v243 offset:34944
mm v[16:19],v[196:199],a[96:99],v[16:19]
ar v224,a232
ar v225,a233
mm v[16:19],v[192:195],a[112:115],v[16:19]
ld v[176:179],v244,s[52:55],s91 offen offset:1024 sc1
mm v[48:51],v[192:195],a[116:119],v[48:51]
ar v226,a234
ar v227,a235
mm v[48:51],v[196:199],a[100:103],v[48:51]
rt v[216:217],v243 offset:42368
rt v[218:219],v243 offset:43392
mm v[80:83],v[196:199],a[104:107],v[80:83]
ar v228,a236
ar v229,a237
mm v[80:83],v[192:195],v[140:143],v[80:83]
ld v[180:183],v245,s[52:55],s91 offen offset:1024 sc1
mm v[112:115],v[192:195],v[128:131],v[112:115]
ar v230,a238
ar v231,a239
mm v[112:115],v[196:199],a[108:111],v[112:115]
cv v248,v224,v225
cv v249,v226,v227
mm v[116:119],v[204:207],a[108:111],v[116:119]
wt lgkmcnt(0)
s_barrier
mm v[116:119],v[200:203],v[128:131],v[116:119]
ld v[184:187],v246,s[52:55],s91 offen offset:1024 sc1
mm v[84:87],v[200:203],v[140:143],v[84:87]
rt v[192:193],v240 offset:50688
rt v[194:195],v240 offset:51712
mm v[84:87],v[204:207],a[104:107],v[84:87]
rt v[196:197],v240 offset:59136
rt v[198:199],v240 offset:60160
mm v[52:55],v[204:207],a[100:103],v[52:55]
s_add_u32 m0, s83, 33792
cv v250,v228,v229
mm v[52:55],v[200:203],a[116:119],v[52:55]
ld v232,s[44:47],s80 offen lds
mm v[20:23],v[200:203],a[112:115],v[20:23]
cv v251,v230,v231
aw a148,v248
mm v[20:23],v[204:207],a[96:99],v[20:23]
rt v[200:201],v241 offset:50688
rt v[202:203],v241 offset:51712
mm v[24:27],v[212:215],a[96:99],v[24:27]
aw a149,v249
aw a150,v250
mm v[24:27],v[208:211],a[112:115],v[24:27]
ld v[188:191],v247,s[52:55],s91 offen offset:1024 sc1
mm v[56:59],v[208:211],a[116:119],v[56:59]
aw a151,v251
s_mul_i32 s90, s97, s7
mm v[56:59],v[212:215],a[100:103],v[56:59]
rt v[204:205],v241 offset:59136
rt v[206:207],v241 offset:60160
mm v[88:91],v[212:215],a[104:107],v[88:91]
s_add_u32 m0, s83, 34816
s_sub_u32 s97, s97, s21
mm v[88:91],v[208:211],v[140:143],v[88:91]
ld v233,s[44:47],s80 offen lds
mm v[120:123],v[208:211],v[128:131],v[120:123]
s_add_u32 s97, s97, 896
mm v[120:123],v[212:215],a[108:111],v[120:123]
rt v[208:209],v242 offset:50688
rt v[210:211],v242 offset:51712
mm v[124:127],v[220:223],a[108:111],v[124:127]
s_add_u32 s36, s36, s95
s_addc_u32 s37, s37, 0
mm v[124:127],v[216:219],v[128:131],v[124:127]
s_and_b32 s37, s37, 0xffff
s_mov_b32 s38, s97
mm v[92:95],v[216:219],v[140:143],v[92:95]
s_mov_b32 s39, 0x20000
s_lshl_b32 s93, s96, 6
mm v[92:95],v[220:223],a[104:107],v[92:95]
rt v[212:213],v242 offset:59136
rt v[214:215],v242 offset:60160
mm v[60:63],v[220:223],a[100:103],v[60:63]
ds_write_b128 v238, v[248:251] offset:32768
mm v[60:63],v[216:219],a[116:119],v[60:63]
s_mul_i32 s95, s94, 7332928
mm v[28:31],v[216:219],a[112:115],v[28:31]
wt vmcnt(15) lgkmcnt(5)
mm v[28:31],v[220:223],a[96:99],v[28:31]
rt v[216:217],v243 offset:50688
rt v[218:219],v243 offset:51712
mm v[0:3],v[192:195],v[132:135],v[0:3]
ar v224,a152
ar v225,a153
mm v[0:3],v[196:199],v[152:155],v[0:3]
ld v[128:131],v244,s[52:55],s91 offen offset:2048 sc1
mm v[32:35],v[196:199],v[156:159],v[32:35]
ar v226,a154
ar v227,a155
mm v[32:35],v[192:195],v[136:139],v[32:35]
rt v[220:221],v243 offset:59136
rt v[222:223],v243 offset:60160
mm v[64:67],v[192:195],v[144:147],v[64:67]
ar v228,a156
ar v229,a157
mm v[64:67],v[196:199],a[120:123],v[64:67]
ld v[140:143],v245,s[52:55],s91 offen offset:2048 sc1
mm v[96:99],v[196:199],a[124:127],v[96:99]
ar v230,a158
ar v231,a159
mm v[96:99],v[192:195],v[148:151],v[96:99]
rt v[196:197],v240 offset:50816
rt v[198:199],v240 offset:51840
mm v[100:103],v[200:203],v[148:151],v[100:103]
s_add_u32 m0, s83, 35968
cv v248,v224,v225
mm v[100:103],v[204:207],a[124:127],v[100:103]
ld v234,s[44:47],s80 offen lds
mm v[68:71],v[204:207],a[120:123],v[68:71]
cv v249,v226,v227
cv v250,v228,v229
mm v[68:71],v[200:203],v[144:147],v[68:71]
rt v[192:193],v240 offset:59264
rt v[194:195],v240 offset:60288
mm v[36:39],v[200:203],v[136:139],v[36:39]
cv v251,v230,v231
aw a152,v248
mm v[36:39],v[204:207],v[156:159],v[36:39]
ld a[96:99],v246,s[52:55],s91 offen offset:2048 sc1
mm v[4:7],v[204:207],v[152:155],v[4:7]
wt lgkmcnt(4)
aw a153,v249
mm v[4:7],v[200:203],v[132:135],v[4:7]
rt v[204:205],v241 offset:50816
rt v[206:207],v241 offset:51840
mm v[8:11],v[208:211],v[132:135],v[8:11]
s_add_u32 m0, s83, 36992
aw a154,v250
mm v[8:11],v[212:215],v[152:155],v[8:11]
ld v235,s[44:47],s80 offen lds
mm v[40:43],v[212:215],v[156:159],v[40:43]
s_add_u32 s80, s80, 524288
aw a155,v251
mm v[40:43],v[208:211],v[136:139],v[40:43]
rt v[200:201],v241 offset:59264
rt v[202:203],v241 offset:60288
mm v[72:75],v[208:211],v[144:147],v[72:75]
s_cmp_eq_u32 s77, 0
s_cselect_b64 s[40:41], s[30:31], s[32:33]
mm v[72:75],v[212:215],a[120:123],v[72:75]
ld a[100:103],v247,s[52:55],s91 offen offset:2048 sc1
mm v[104:107],v[212:215],a[124:127],v[104:107]
s_add_u32 s40, s40, s93
s_addc_u32 s41, s41, 0
mm v[104:107],v[208:211],v[148:151],v[104:107]
rt v[212:213],v242 offset:50816
rt v[214:215],v242 offset:51840
mm v[108:111],v[216:219],v[148:151],v[108:111]
s_and_b32 s41, s41, 0xffff
s_mov_b32 s42, s95
mm v[108:111],v[220:223],a[124:127],v[108:111]
s_mov_b32 s43, 0x20000
s_lshr_b32 s92, s72, 1
mm v[76:79],v[220:223],a[120:123],v[76:79]
s_and_b32 s92, s92, 4
s_xor_b32 s92, s92, s72
mm v[76:79],v[216:219],v[144:147],v[76:79]
rt v[208:209],v242 offset:59264
rt v[210:211],v242 offset:60288
mm v[44:47],v[216:219],v[136:139],v[44:47]
ds_write_b128 v238, v[248:251] offset:33792
mm v[44:47],v[220:223],v[156:159],v[44:47]
st a[144:147],v255,s[48:51],s18 offen offset:3072 sc1
mm v[12:15],v[220:223],v[152:155],v[12:15]
wt lgkmcnt(5)
s_xor_b32 s92, s92, 1
mm v[12:15],v[216:219],v[132:135],v[12:15]
rt v[220:221],v243 offset:50816
rt v[222:223],v243 offset:51840
mm v[16:19],v[196:199],v[132:135],v[16:19]
ar v224,a208
ar v225,a209
mm v[16:19],v[192:195],v[152:155],v[16:19]
ld a[104:107],v244,s[52:55],s91 offen offset:3072 sc1
mm v[48:51],v[192:195],v[156:159],v[48:51]
ar v226,a210
ar v227,a211
mm v[48:51],v[196:199],v[136:139],v[48:51]
rt v[216:217],v243 offset:59264
rt v[218:219],v243 offset:60288
mm v[80:83],v[196:199],v[144:147],v[80:83]
ar v228,a212
ar v229,a213
mm v[80:83],v[192:195],a[120:123],v[80:83]
ld a[108:111],v245,s[52:55],s91 offen offset:3072 sc1
mm v[112:115],v[192:195],a[124:127],v[112:115]
ar v230,a214
ar v231,a215
mm v[112:115],v[196:199],v[148:151],v[112:115]
cv v248,v224,v225
cv v249,v226,v227
mm v[116:119],v[204:207],v[148:151],v[116:119]
wt lgkmcnt(0)
s_barrier
mm v[116:119],v[200:203],a[124:127],v[116:119]
ld a[112:115],v246,s[52:55],s91 offen offset:3072 sc1
mm v[84:87],v[200:203],a[120:123],v[84:87]
rt v[192:193],v240 offset:0
rt v[194:195],v240 offset:1024
mm v[84:87],v[204:207],v[144:147],v[84:87]
rt v[196:197],v240 offset:8448
rt v[198:199],v240 offset:9472
mm v[52:55],v[204:207],v[136:139],v[52:55]
s_add_u32 m0, s83, 50688
cv v250,v228,v229
mm v[52:55],v[200:203],v[156:159],v[52:55]
ld v232,s[44:47],s80 offen lds
mm v[20:23],v[200:203],v[152:155],v[20:23]
cv v251,v230,v231
aw a156,v248
mm v[20:23],v[204:207],v[132:135],v[20:23]
rt v[200:201],v241 offset:0
rt v[202:203],v241 offset:1024
mm v[24:27],v[212:215],v[132:135],v[24:27]
aw a157,v249
aw a158,v250
mm v[24:27],v[208:211],v[152:155],v[24:27]
ld a[116:119],v247,s[52:55],s91 offen offset:3072 sc1
mm v[56:59],v[208:211],v[156:159],v[56:59]
s_add_u32 s91, s91, 4096
aw a159,v251
mm v[56:59],v[212:215],v[136:139],v[56:59]
rt v[204:205],v241 offset:8448
rt v[206:207],v241 offset:9472
mm v[88:91],v[212:215],v[144:147],v[88:91]
s_add_u32 m0, s83, 51712
s_and_b32 s101, s92, 7
mm v[88:91],v[208:211],a[120:123],v[88:91]
ld v233,s[44:47],s80 offen lds
mm v[120:123],v[208:211],a[124:127],v[120:123]
s_lshl_b32 s92, s101, 8
s_sub_u32 s93, 8388608, s92
mm v[120:123],v[212:215],v[148:151],v[120:123]
rt v[208:209],v242 offset:0
rt v[210:211],v242 offset:1024
mm v[124:127],v[220:223],v[148:151],v[124:127]
s_add_u32 s56, s28, s92
s_addc_u32 s57, s29, 0
mm v[124:127],v[216:219],a[124:127],v[124:127]
s_and_b32 s57, s57, 0xffff
s_mov_b32 s58, s93
mm v[92:95],v[216:219],a[120:123],v[92:95]
s_mov_b32 s18, 32768
s_mov_b32 s59, 0x20000
mm v[92:95],v[220:223],v[144:147],v[92:95]
rt v[212:213],v242 offset:8448
rt v[214:215],v242 offset:9472
mm v[60:63],v[220:223],v[136:139],v[60:63]
ds_write_b128 v238, v[248:251] offset:34816
mm v[60:63],v[216:219],v[156:159],v[60:63]
st a[148:151],v255,s[48:51],s18 offen offset:0 sc1
mm v[28:31],v[216:219],v[152:155],v[28:31]
wt vmcnt(15) lgkmcnt(5)
s_lshl_b32 s93, s96, 16
mm v[28:31],v[220:223],v[132:135],v[28:31]
rt v[216:217],v243 offset:0
rt v[218:219],v243 offset:1024
mm v[0:3],v[192:195],v[160:163],v[0:3]
ar v224,a160
ar v225,a161
mm v[0:3],v[196:199],v[176:179],v[0:3]
ld v[132:135],v244,s[52:55],s91 offen offset:0 sc1
mm v[32:35],v[196:199],v[180:183],v[32:35]
ar v226,a162
ar v227,a163
mm v[32:35],v[192:195],v[164:167],v[32:35]
rt v[220:221],v243 offset:8448
rt v[222:223],v243 offset:9472
mm v[64:67],v[192:195],v[168:171],v[64:67]
ar v228,a164
ar v229,a165
mm v[64:67],v[196:199],v[184:187],v[64:67]
ld v[136:139],v245,s[52:55],s91 offen offset:0 sc1
mm v[96:99],v[196:199],v[188:191],v[96:99]
ar v230,a166
ar v231,a167
mm v[96:99],v[192:195],v[172:175],v[96:99]
rt v[196:197],v240 offset:128
rt v[198:199],v240 offset:1152
mm v[100:103],v[200:203],v[172:175],v[100:103]
s_add_u32 m0, s83, 52864
cv v248,v224,v225
mm v[100:103],v[204:207],v[188:191],v[100:103]
ld v234,s[44:47],s80 offen lds
mm v[68:71],v[204:207],v[184:187],v[68:71]
cv v249,v226,v227
cv v250,v228,v229
mm v[68:71],v[200:203],v[168:171],v[68:71]
rt v[192:193],v240 offset:8576
rt v[194:195],v240 offset:9600
mm v[36:39],v[200:203],v[164:167],v[36:39]
cv v251,v230,v231
aw a160,v248
mm v[36:39],v[204:207],v[180:183],v[36:39]
ld v[144:147],v246,s[52:55],s91 offen offset:0 sc1
mm v[4:7],v[204:207],v[176:179],v[4:7]
wt lgkmcnt(4)
aw a161,v249
mm v[4:7],v[200:203],v[160:163],v[4:7]
rt v[204:205],v241 offset:128
rt v[206:207],v241 offset:1152
mm v[8:11],v[208:211],v[160:163],v[8:11]
s_add_u32 m0, s83, 53888
aw a162,v250
mm v[8:11],v[212:215],v[176:179],v[8:11]
ld v235,s[44:47],s80 offen lds
mm v[40:43],v[212:215],v[180:183],v[40:43]
s_add_u32 s80, s80, 524288
aw a163,v251
mm v[40:43],v[208:211],v[164:167],v[40:43]
rt v[200:201],v241 offset:8576
rt v[202:203],v241 offset:9600
mm v[72:75],v[208:211],v[168:171],v[72:75]
s_lshl_b32 s95, s94, 16
mm v[72:75],v[212:215],v[184:187],v[72:75]
ld v[148:151],v247,s[52:55],s91 offen offset:0 sc1
mm v[104:107],v[212:215],v[188:191],v[104:107]
s_add_u32 s28, s34, s93
s_addc_u32 s29, s35, 0
mm v[104:107],v[208:211],v[172:175],v[104:107]
rt v[212:213],v242 offset:128
rt v[214:215],v242 offset:1152
mm v[108:111],v[216:219],v[172:175],v[108:111]
s_and_b32 s29, s29, 0xffff
s_mov_b32 s30, s95
mm v[108:111],v[220:223],v[188:191],v[108:111]
s_mov_b32 s31, 0x20000
s_lshl_b32 s93, s101, 12
mm v[76:79],v[220:223],v[184:187],v[76:79]
s_add_u32 s92, s93, 71303168
s_sub_u32 s93, 16777216, s93
mm v[76:79],v[216:219],v[168:171],v[76:79]
rt v[208:209],v242 offset:8576
rt v[210:211],v242 offset:9600
mm v[44:47],v[216:219],v[164:167],v[44:47]
ds_write_b128 v238, v[248:251] offset:35840
mm v[44:47],v[220:223],v[180:183],v[44:47]
st a[152:155],v255,s[48:51],s18 offen offset:1024 sc1
mm v[12:15],v[220:223],v[176:179],v[12:15]
wt lgkmcnt(5)
mm v[12:15],v[216:219],v[160:163],v[12:15]
rt v[220:221],v243 offset:128
rt v[222:223],v243 offset:1152
mm v[16:19],v[196:199],v[160:163],v[16:19]
ar v224,a216
ar v225,a217
mm v[16:19],v[192:195],v[176:179],v[16:19]
ld v[152:155],v244,s[52:55],s91 offen offset:1024 sc1
mm v[48:51],v[192:195],v[180:183],v[48:51]
ar v226,a218
ar v227,a219
mm v[48:51],v[196:199],v[164:167],v[48:51]
rt v[216:217],v243 offset:8576
rt v[218:219],v243 offset:9600
mm v[80:83],v[196:199],v[168:171],v[80:83]
ar v228,a220
ar v229,a221
mm v[80:83],v[192:195],v[184:187],v[80:83]
ld v[156:159],v245,s[52:55],s91 offen offset:1024 sc1
mm v[112:115],v[192:195],v[188:191],v[112:115]
ar v230,a222
ar v231,a223
mm v[112:115],v[196:199],v[172:175],v[112:115]
cv v248,v224,v225
cv v249,v226,v227
mm v[116:119],v[204:207],v[172:175],v[116:119]
wt lgkmcnt(0)
s_barrier
mm v[116:119],v[200:203],v[188:191],v[116:119]
ld a[120:123],v246,s[52:55],s91 offen offset:1024 sc1
mm v[84:87],v[200:203],v[184:187],v[84:87]
rt v[192:193],v240 offset:16896
rt v[194:195],v240 offset:17920
mm v[84:87],v[204:207],v[168:171],v[84:87]
rt v[196:197],v240 offset:25344
rt v[198:199],v240 offset:26368
mm v[52:55],v[204:207],v[164:167],v[52:55]
s_add_u32 m0, s83, 0
cv v250,v228,v229
mm v[52:55],v[200:203],v[180:183],v[52:55]
ld v232,s[44:47],s80 offen lds
mm v[20:23],v[200:203],v[176:179],v[20:23]
cv v251,v230,v231
aw a164,v248
mm v[20:23],v[204:207],v[160:163],v[20:23]
rt v[200:201],v241 offset:16896
rt v[202:203],v241 offset:17920
mm v[24:27],v[212:215],v[160:163],v[24:27]
aw a165,v249
aw a166,v250
mm v[24:27],v[208:211],v[176:179],v[24:27]
ld a[124:127],v247,s[52:55],s91 offen offset:1024 sc1
mm v[56:59],v[208:211],v[180:183],v[56:59]
aw a167,v251
mm v[56:59],v[212:215],v[164:167],v[56:59]
rt v[204:205],v241 offset:25344
rt v[206:207],v241 offset:26368
mm v[88:91],v[212:215],v[168:171],v[88:91]
s_add_u32 m0, s83, 1024
mm v[88:91],v[208:211],v[184:187],v[88:91]
ld v233,s[44:47],s80 offen lds
mm v[120:123],v[208:211],v[188:191],v[120:123]
s_add_u32 s32, s26, s92
s_addc_u32 s33, s27, 0
mm v[120:123],v[212:215],v[172:175],v[120:123]
rt v[208:209],v242 offset:16896
rt v[210:211],v242 offset:17920
mm v[124:127],v[220:223],v[172:175],v[124:127]
s_and_b32 s33, s33, 0xffff
s_mov_b32 s34, s93
mm v[124:127],v[216:219],v[188:191],v[124:127]
s_mov_b32 s35, 0x20000
s_lshl2_add_u32 s92, s72, s74
mm v[92:95],v[216:219],v[184:187],v[92:95]
s_lshl2_add_u32 s92, s92, s3
s_lshl_b32 s88, s92, 8
mm v[92:95],v[220:223],v[168:171],v[92:95]
rt v[212:213],v242 offset:25344
rt v[214:215],v242 offset:26368
mm v[60:63],v[220:223],v[164:167],v[60:63]
ds_write_b128 v238, v[248:251] offset:36864
mm v[60:63],v[216:219],v[180:183],v[60:63]
st a[156:159],v255,s[48:51],s18 offen offset:2048 sc1
mm v[28:31],v[216:219],v[176:179],v[28:31]
wt vmcnt(16) lgkmcnt(5)
s_mul_i32 s93, s92, 17408
mm v[28:31],v[220:223],v[160:163],v[28:31]
rt v[216:217],v243 offset:16896
rt v[218:219],v243 offset:17920
mm v[0:3],v[192:195],v[128:131],v[0:3]
ar v224,a168
ar v225,a169
mm v[0:3],v[196:199],a[104:107],v[0:3]
ld v[160:163],v244,s[52:55],s91 offen offset:2048 sc1
mm v[32:35],v[196:199],a[108:111],v[32:35]
ar v226,a170
ar v227,a171
mm v[32:35],v[192:195],v[140:143],v[32:35]
rt v[220:221],v243 offset:25344
rt v[222:223],v243 offset:26368
mm v[64:67],v[192:195],a[96:99],v[64:67]
ar v228,a172
ar v229,a173
mm v[64:67],v[196:199],a[112:115],v[64:67]
ld v[164:167],v245,s[52:55],s91 offen offset:2048 sc1
mm v[96:99],v[196:199],a[116:119],v[96:99]
ar v230,a174
ar v231,a175
mm v[96:99],v[192:195],a[100:103],v[96:99]
rt v[196:197],v240 offset:17024
rt v[198:199],v240 offset:18048
mm v[100:103],v[200:203],a[100:103],v[100:103]
s_add_u32 m0, s83, 2176
cv v248,v224,v225
mm v[100:103],v[204:207],a[116:119],v[100:103]
ld v234,s[44:47],s80 offen lds
mm v[68:71],v[204:207],a[112:115],v[68:71]
cv v249,v226,v227
cv v250,v228,v229
mm v[68:71],v[200:203],a[96:99],v[68:71]
rt v[192:193],v240 offset:25472
rt v[194:195],v240 offset:26496
mm v[36:39],v[200:203],v[140:143],v[36:39]
cv v251,v230,v231
aw a168,v248
mm v[36:39],v[204:207],a[108:111],v[36:39]
ld v[168:171],v246,s[52:55],s91 offen offset:2048 sc1
mm v[4:7],v[204:207],a[104:107],v[4:7]
wt lgkmcnt(4)
aw a169,v249
mm v[4:7],v[200:203],v[128:131],v[4:7]
rt v[204:205],v241 offset:17024
rt v[206:207],v241 offset:18048
mm v[8:11],v[208:211],v[128:131],v[8:11]
s_add_u32 m0, s83, 3200
aw a170,v250
mm v[8:11],v[212:215],a[104:107],v[8:11]
ld v235,s[44:47],s80 offen lds
mm v[40:43],v[212:215],a[108:111],v[40:43]
s_add_u32 s80, s80, 524288
aw a171,v251
mm v[40:43],v[208:211],v[140:143],v[40:43]
rt v[200:201],v241 offset:25472
rt v[202:203],v241 offset:26496
mm v[72:75],v[208:211],a[96:99],v[72:75]
s_lshl_b32 s92, s92, 14
s_add_u32 s92, s92, 4194304
mm v[72:75],v[212:215],a[112:115],v[72:75]
ld v[172:175],v247,s[52:55],s91 offen offset:2048 sc1
mm v[104:107],v[212:215],a[116:119],v[104:107]
s_add_u32 s86, s26, s92
s_addc_u32 s87, s27, 0
mm v[104:107],v[208:211],a[100:103],v[104:107]
rt v[212:213],v242 offset:17024
rt v[214:215],v242 offset:18048
mm v[108:111],v[216:219],a[100:103],v[108:111]
s_and_b32 s87, s87, 0xffff
s_add_u32 s93, s93, 150994944
mm v[108:111],v[220:223],a[116:119],v[108:111]
s_add_u32 s68, s26, s93
s_addc_u32 s69, s27, 0
mm v[76:79],v[220:223],a[112:115],v[76:79]
s_and_b32 s69, s69, 0xffff
s_mov_b32 s70, 17408
mm v[76:79],v[216:219],a[96:99],v[76:79]
rt v[208:209],v242 offset:25472
rt v[210:211],v242 offset:26496
mm v[44:47],v[216:219],v[140:143],v[44:47]
ds_write_b128 v238, v[248:251] offset:37888
mm v[44:47],v[220:223],a[108:111],v[44:47]
st a[160:163],v255,s[48:51],s18 offen offset:3072 sc1
mm v[12:15],v[220:223],a[104:107],v[12:15]
wt lgkmcnt(5)
s_mov_b32 s96, 153158689
mm v[12:15],v[216:219],v[128:131],v[12:15]
rt v[220:221],v243 offset:17024
rt v[222:223],v243 offset:18048
mm v[16:19],v[196:199],v[128:131],v[16:19]
.La5_LW1_11:
s_cbranch_execz .La5_LW1_15
ar v224,a192
ar v225,a193
mm v[16:19],v[192:195],a[104:107],v[16:19]
ld v[176:179],v244,s[52:55],s91 offen offset:3072 sc1
mm v[48:51],v[192:195],a[108:111],v[48:51]
ar v226,a194
ar v227,a195
mm v[48:51],v[196:199],v[140:143],v[48:51]
rt v[216:217],v243 offset:25472
rt v[218:219],v243 offset:26496
mm v[80:83],v[196:199],a[96:99],v[80:83]
ar v228,a196
ar v229,a197
mm v[80:83],v[192:195],a[112:115],v[80:83]
ld v[180:183],v245,s[52:55],s91 offen offset:3072 sc1
mm v[112:115],v[192:195],a[116:119],v[112:115]
ar v230,a198
ar v231,a199
mm v[112:115],v[196:199],a[100:103],v[112:115]
cv v248,v224,v225
cv v249,v226,v227
mm v[116:119],v[204:207],a[100:103],v[116:119]
wt lgkmcnt(0)
s_barrier
mm v[116:119],v[200:203],a[116:119],v[116:119]
ld v[184:187],v246,s[52:55],s91 offen offset:3072 sc1
mm v[84:87],v[200:203],a[112:115],v[84:87]
rt v[192:193],v240 offset:33792
rt v[194:195],v240 offset:34816
mm v[84:87],v[204:207],a[96:99],v[84:87]
rt v[196:197],v240 offset:42240
rt v[198:199],v240 offset:43264
mm v[52:55],v[204:207],v[140:143],v[52:55]
s_add_u32 m0, s83, 16896
cv v250,v228,v229
mm v[52:55],v[200:203],a[108:111],v[52:55]
ld v232,s[44:47],s80 offen lds
mm v[20:23],v[200:203],a[104:107],v[20:23]
cv v251,v230,v231
aw a172,v248
mm v[20:23],v[204:207],v[128:131],v[20:23]
rt v[200:201],v241 offset:33792
rt v[202:203],v241 offset:34816
mm v[24:27],v[212:215],v[128:131],v[24:27]
aw a173,v249
aw a174,v250
mm v[24:27],v[208:211],a[104:107],v[24:27]
ld v[188:191],v247,s[52:55],s91 offen offset:3072 sc1
mm v[56:59],v[208:211],a[108:111],v[56:59]
s_add_u32 s91, s91, 4096
aw a175,v251
mm v[56:59],v[212:215],v[140:143],v[56:59]
rt v[204:205],v241 offset:42240
rt v[206:207],v241 offset:43264
mm v[88:91],v[212:215],a[96:99],v[88:91]
s_add_u32 m0, s83, 17920
s_mov_b32 s97, 371265825
mm v[88:91],v[208:211],a[112:115],v[88:91]
ld v233,s[44:47],s80 offen lds
mm v[120:123],v[208:211],a[116:119],v[120:123]
s_lshl_b32 s95, s73, 3
s_lshr_b64 s[96:97], s[96:97], s95
mm v[120:123],v[212:215],a[100:103],v[120:123]
rt v[208:209],v242 offset:33792
rt v[210:211],v242 offset:34816
mm v[124:127],v[220:223],a[100:103],v[124:127]
s_and_b32 s82, s96, 255
s_lshl_b64 s[98:99], 1, s82
mm v[124:127],v[216:219],a[116:119],v[124:127]
s_lshl_b32 s96, s75, 22
s_or_b32 s95, s98, s96
mm v[92:95],v[216:219],a[112:115],v[92:95]
s_cmp_eq_u32 s73, 1
s_cselect_b32 s96, 12, 4
s_cselect_b32 s79, 0, 0xff000000
mm v[92:95],v[220:223],a[96:99],v[92:95]
rt v[212:213],v242 offset:42240
rt v[214:215],v242 offset:43264
mm v[60:63],v[220:223],v[140:143],v[60:63]
ds_write_b128 v238, v[248:251] offset:38912
mm v[60:63],v[216:219],a[108:111],v[60:63]
s_or_b32 s98, s95, s96
mm v[28:31],v[216:219],a[104:107],v[28:31]
wt vmcnt(15) lgkmcnt(5)
mm v[28:31],v[220:223],v[128:131],v[28:31]
rt v[216:217],v243 offset:33792
rt v[218:219],v243 offset:34816
mm v[0:3],v[192:195],v[132:135],v[0:3]
ar v224,a176
ar v225,a177
mm v[0:3],v[196:199],v[152:155],v[0:3]
ld v[128:131],v244,s[52:55],s91 offen offset:0 sc1
mm v[32:35],v[196:199],v[156:159],v[32:35]
ar v226,a178
ar v227,a179
mm v[32:35],v[192:195],v[136:139],v[32:35]
rt v[220:221],v243 offset:42240
rt v[222:223],v243 offset:43264
mm v[64:67],v[192:195],v[144:147],v[64:67]
ar v228,a180
ar v229,a181
mm v[64:67],v[196:199],a[120:123],v[64:67]
ld v[140:143],v245,s[52:55],s91 offen offset:0 sc1
mm v[96:99],v[196:199],a[124:127],v[96:99]
ar v230,a182
ar v231,a183
mm v[96:99],v[192:195],v[148:151],v[96:99]
rt v[196:197],v240 offset:33920
rt v[198:199],v240 offset:34944
mm v[100:103],v[200:203],v[148:151],v[100:103]
s_add_u32 m0, s83, 19072
cv v248,v224,v225
mm v[100:103],v[204:207],a[124:127],v[100:103]
ld v234,s[44:47],s80 offen lds
mm v[68:71],v[204:207],a[120:123],v[68:71]
cv v249,v226,v227
cv v250,v228,v229
mm v[68:71],v[200:203],v[144:147],v[68:71]
rt v[192:193],v240 offset:42368
rt v[194:195],v240 offset:43392
mm v[36:39],v[200:203],v[136:139],v[36:39]
cv v251,v230,v231
aw a176,v248
mm v[36:39],v[204:207],v[156:159],v[36:39]
ld a[96:99],v246,s[52:55],s91 offen offset:0 sc1
mm v[4:7],v[204:207],v[152:155],v[4:7]
wt lgkmcnt(4)
aw a177,v249
mm v[4:7],v[200:203],v[132:135],v[4:7]
rt v[204:205],v241 offset:33920
rt v[206:207],v241 offset:34944
mm v[8:11],v[208:211],v[132:135],v[8:11]
s_add_u32 m0, s83, 20096
aw a178,v250
mm v[8:11],v[212:215],v[152:155],v[8:11]
ld v235,s[44:47],s80 offen lds
mm v[40:43],v[212:215],v[156:159],v[40:43]
s_add_u32 s80, s80, 524288
aw a179,v251
mm v[40:43],v[208:211],v[136:139],v[40:43]
rt v[200:201],v241 offset:42368
rt v[202:203],v241 offset:43392
mm v[72:75],v[208:211],v[144:147],v[72:75]
s_cmp_eq_u32 s74, 3
s_cselect_b32 s23, 3, 0
s_cselect_b32 s22, 0, s75
mm v[72:75],v[212:215],a[120:123],v[72:75]
ld a[100:103],v247,s[52:55],s91 offen offset:0 sc1
mm v[104:107],v[212:215],a[124:127],v[104:107]
s_lshl_b32 s95, s76, 5
s_add_u32 s95, s95, s3
mm v[104:107],v[208:211],v[148:151],v[104:107]
rt v[212:213],v242 offset:33920
rt v[214:215],v242 offset:34944
mm v[108:111],v[216:219],v[148:151],v[108:111]
s_lshl_b32 s95, s95, 5
s_add_u32 s95, s95, s72
mm v[108:111],v[220:223],a[124:127],v[108:111]
s_lshl_b32 s95, s95, 3
s_add_u32 s6, s95, 1048576
mm v[76:79],v[220:223],a[120:123],v[76:79]
s_mov_b32 s18, 36864
v_and_b32 v239, 0x70, v255
mm v[76:79],v[216:219],v[144:147],v[76:79]
rt v[208:209],v242 offset:42368
rt v[210:211],v242 offset:43392
mm v[44:47],v[216:219],v[136:139],v[44:47]
ds_write_b128 v238, v[248:251] offset:39936
mm v[44:47],v[220:223],v[156:159],v[44:47]
st a[164:167],v255,s[48:51],s18 offen offset:0 sc1
mm v[12:15],v[220:223],v[152:155],v[12:15]
wt lgkmcnt(5)
v_lshlrev_b32 v239, 6, v239
mm v[12:15],v[216:219],v[132:135],v[12:15]
rt v[220:221],v243 offset:33920
rt v[222:223],v243 offset:34944
mm v[16:19],v[196:199],v[132:135],v[16:19]
ar v224,a200
ar v225,a201
mm v[16:19],v[192:195],v[152:155],v[16:19]
ld a[104:107],v244,s[52:55],s91 offen offset:1024 sc1
mm v[48:51],v[192:195],v[156:159],v[48:51]
ar v226,a202
ar v227,a203
mm v[48:51],v[196:199],v[136:139],v[48:51]
rt v[216:217],v243 offset:42368
rt v[218:219],v243 offset:43392
mm v[80:83],v[196:199],v[144:147],v[80:83]
ar v228,a204
ar v229,a205
mm v[80:83],v[192:195],a[120:123],v[80:83]
ld a[108:111],v245,s[52:55],s91 offen offset:1024 sc1
mm v[112:115],v[192:195],a[124:127],v[112:115]
ar v230,a206
ar v231,a207
mm v[112:115],v[196:199],v[148:151],v[112:115]
cv v248,v224,v225
cv v249,v226,v227
mm v[116:119],v[204:207],v[148:151],v[116:119]
wt lgkmcnt(0)
s_barrier
mm v[116:119],v[200:203],a[124:127],v[116:119]
ld a[112:115],v246,s[52:55],s91 offen offset:1024 sc1
mm v[84:87],v[200:203],a[120:123],v[84:87]
rt v[192:193],v240 offset:50688
rt v[194:195],v240 offset:51712
mm v[84:87],v[204:207],v[144:147],v[84:87]
rt v[196:197],v240 offset:59136
rt v[198:199],v240 offset:60160
mm v[52:55],v[204:207],v[136:139],v[52:55]
s_add_u32 m0, s83, 33792
cv v250,v228,v229
mm v[52:55],v[200:203],v[156:159],v[52:55]
ld v232,s[44:47],s80 offen lds
mm v[20:23],v[200:203],v[152:155],v[20:23]
cv v251,v230,v231
aw a180,v248
mm v[20:23],v[204:207],v[132:135],v[20:23]
rt v[200:201],v241 offset:50688
rt v[202:203],v241 offset:51712
mm v[24:27],v[212:215],v[132:135],v[24:27]
aw a181,v249
aw a182,v250
mm v[24:27],v[208:211],v[152:155],v[24:27]
ld a[116:119],v247,s[52:55],s91 offen offset:1024 sc1
mm v[56:59],v[208:211],v[156:159],v[56:59]
aw a183,v251
s_add_u32 s85, s85, 65536
mm v[56:59],v[212:215],v[136:139],v[56:59]
rt v[204:205],v241 offset:59136
rt v[206:207],v241 offset:60160
mm v[88:91],v[212:215],v[144:147],v[88:91]
s_add_u32 m0, s83, 34816
v_add_u32 v238, s85, v255
mm v[88:91],v[208:211],a[120:123],v[88:91]
ld v233,s[44:47],s80 offen lds
mm v[120:123],v[208:211],a[124:127],v[120:123]
s_mov_b32 s85, 0
aw a231,v239
mm v[120:123],v[212:215],v[148:151],v[120:123]
rt v[208:209],v242 offset:50688
rt v[210:211],v242 offset:51712
mm v[124:127],v[220:223],v[148:151],v[124:127]
s_mul_i32 s95, s75, 0
s_add_u32 s95, s95, 2
mm v[124:127],v[216:219],a[124:127],v[124:127]
s_sub_u32 s96, 4, s95
s_lshr_b32 s97, s2, 4
mm v[92:95],v[216:219],a[120:123],v[92:95]
s_mul_i32 s97, s97, s95
v_lshrrev_b32 v239, 7, v255
mm v[92:95],v[220:223],v[144:147],v[92:95]
rt v[212:213],v242 offset:59136
rt v[214:215],v242 offset:60160
mm v[60:63],v[220:223],v[136:139],v[60:63]
ds_write_b128 v238, v[248:251] offset:0
mm v[60:63],v[216:219],v[156:159],v[60:63]
st a[168:171],v255,s[48:51],s18 offen offset:1024 sc1
mm v[28:31],v[216:219],v[152:155],v[28:31]
wt vmcnt(15) lgkmcnt(5)
v_add_u32 v254, s97, v239
mm v[28:31],v[220:223],v[132:135],v[28:31]
rt v[216:217],v243 offset:50688
rt v[218:219],v243 offset:51712
mm v[0:3],v[192:195],v[160:163],v[0:3]
ar v224,a184
ar v225,a185
mm v[0:3],v[196:199],v[176:179],v[0:3]
ld v[132:135],v244,s[52:55],s91 offen offset:2048 sc1
mm v[32:35],v[196:199],v[180:183],v[32:35]
ar v226,a186
ar v227,a187
mm v[32:35],v[192:195],v[164:167],v[32:35]
rt v[220:221],v243 offset:59136
rt v[222:223],v243 offset:60160
mm v[64:67],v[192:195],v[168:171],v[64:67]
ar v228,a188
ar v229,a189
mm v[64:67],v[196:199],v[184:187],v[64:67]
ld v[136:139],v245,s[52:55],s91 offen offset:2048 sc1
mm v[96:99],v[196:199],v[188:191],v[96:99]
ar v230,a190
ar v231,a191
mm v[96:99],v[192:195],v[172:175],v[96:99]
rt v[196:197],v240 offset:50816
rt v[198:199],v240 offset:51840
mm v[100:103],v[200:203],v[172:175],v[100:103]
s_add_u32 m0, s83, 35968
cv v248,v224,v225
mm v[100:103],v[204:207],v[188:191],v[100:103]
ld v234,s[44:47],s80 offen lds
mm v[68:71],v[204:207],v[184:187],v[68:71]
cv v249,v226,v227
cv v250,v228,v229
mm v[68:71],v[200:203],v[168:171],v[68:71]
rt v[192:193],v240 offset:59264
rt v[194:195],v240 offset:60288
mm v[36:39],v[200:203],v[164:167],v[36:39]
cv v251,v230,v231
aw a184,v248
mm v[36:39],v[204:207],v[180:183],v[36:39]
ld v[144:147],v246,s[52:55],s91 offen offset:2048 sc1
mm v[4:7],v[204:207],v[176:179],v[4:7]
wt lgkmcnt(4)
aw a185,v249
mm v[4:7],v[200:203],v[160:163],v[4:7]
rt v[204:205],v241 offset:50816
rt v[206:207],v241 offset:51840
mm v[8:11],v[208:211],v[160:163],v[8:11]
s_add_u32 m0, s83, 36992
aw a186,v250
mm v[8:11],v[212:215],v[176:179],v[8:11]
ld v235,s[44:47],s80 offen lds
mm v[40:43],v[212:215],v[180:183],v[40:43]
s_add_u32 s80, s80, 524288
aw a187,v251
mm v[40:43],v[208:211],v[164:167],v[40:43]
rt v[200:201],v241 offset:59264
rt v[202:203],v241 offset:60288
mm v[72:75],v[208:211],v[168:171],v[72:75]
v_mul_u32_u24 v254, s21, v254
v_add_u32 v239, s96, v239
mm v[72:75],v[212:215],v[184:187],v[72:75]
ld v[148:151],v247,s[52:55],s91 offen offset:2048 sc1
mm v[104:107],v[212:215],v[188:191],v[104:107]
v_lshrrev_b32 v239, 2, v239
v_lshl_add_u32 v254, v239, 28, v254
mm v[104:107],v[208:211],v[172:175],v[104:107]
rt v[212:213],v242 offset:50816
rt v[214:215],v242 offset:51840
mm v[108:111],v[216:219],v[172:175],v[108:111]
v_and_b32 v239, 0x70, v255
v_lshl_add_u32 v254, v239, 3, v254
mm v[108:111],v[220:223],v[188:191],v[108:111]
s_lshl_b32 s96, s7, 5
s_sub_u32 s96, 16, s96
mm v[76:79],v[220:223],v[184:187],v[76:79]
s_mul_i32 s95, s95, s96
s_mul_i32 s21, s21, s95
mm v[76:79],v[216:219],v[168:171],v[76:79]
rt v[208:209],v242 offset:59264
rt v[210:211],v242 offset:60288
mm v[44:47],v[216:219],v[164:167],v[44:47]
ds_write_b128 v238, v[248:251] offset:1024
mm v[44:47],v[220:223],v[180:183],v[44:47]
st a[172:175],v255,s[48:51],s18 offen offset:2048 sc1
mm v[12:15],v[220:223],v[176:179],v[12:15]
wt lgkmcnt(5)
v_lshrrev_b32 v236, 8, v255
mm v[12:15],v[216:219],v[160:163],v[12:15]
rt v[220:221],v243 offset:50816
rt v[222:223],v243 offset:51840
mm v[16:19],v[196:199],v[160:163],v[16:19]
ar v224,a64
ar v225,a65
mm v[16:19],v[192:195],v[176:179],v[16:19]
ld v[152:155],v244,s[52:55],s91 offen offset:3072 sc1
mm v[48:51],v[192:195],v[180:183],v[48:51]
ar v226,a66
ar v227,a67
mm v[48:51],v[196:199],v[164:167],v[48:51]
rt v[216:217],v243 offset:59264
rt v[218:219],v243 offset:60288
mm v[80:83],v[196:199],v[168:171],v[80:83]
ar v228,a68
ar v229,a69
mm v[80:83],v[192:195],v[184:187],v[80:83]
ld v[156:159],v245,s[52:55],s91 offen offset:3072 sc1
mm v[112:115],v[192:195],v[188:191],v[112:115]
ar v230,a70
ar v231,a71
mm v[112:115],v[196:199],v[172:175],v[112:115]
cv v248,v224,v225
cv v249,v226,v227
mm v[116:119],v[204:207],v[172:175],v[116:119]
wt lgkmcnt(0)
s_barrier
mm v[116:119],v[200:203],v[188:191],v[116:119]
ld a[120:123],v246,s[52:55],s91 offen offset:3072 sc1
mm v[84:87],v[200:203],v[184:187],v[84:87]
rt v[192:193],v240 offset:0
rt v[194:195],v240 offset:1024
mm v[84:87],v[204:207],v[168:171],v[84:87]
rt v[196:197],v240 offset:8448
rt v[198:199],v240 offset:9472
mm v[52:55],v[204:207],v[164:167],v[52:55]
s_add_u32 m0, s83, 50688
cv v250,v228,v229
mm v[52:55],v[200:203],v[180:183],v[52:55]
ld v232,s[44:47],s80 offen lds
mm v[20:23],v[200:203],v[176:179],v[20:23]
cv v251,v230,v231
aw a188,v248
mm v[20:23],v[204:207],v[160:163],v[20:23]
rt v[200:201],v241 offset:0
rt v[202:203],v241 offset:1024
mm v[24:27],v[212:215],v[160:163],v[24:27]
aw a189,v249
aw a190,v250
mm v[24:27],v[208:211],v[176:179],v[24:27]
ld a[124:127],v247,s[52:55],s91 offen offset:3072 sc1
mm v[56:59],v[208:211],v[180:183],v[56:59]
s_add_u32 s91, s91, 4096
aw a191,v251
mm v[56:59],v[212:215],v[164:167],v[56:59]
rt v[204:205],v241 offset:8448
rt v[206:207],v241 offset:9472
mm v[88:91],v[212:215],v[168:171],v[88:91]
s_add_u32 m0, s83, 51712
v_lshlrev_b32 v239, 5, v236
mm v[88:91],v[208:211],v[184:187],v[88:91]
ld v233,s[44:47],s80 offen lds
mm v[120:123],v[208:211],v[188:191],v[120:123]
v_xor_b32 v239, v239, v255
v_and_b32 v239, 0xf0, v239
mm v[120:123],v[212:215],v[172:175],v[120:123]
rt v[208:209],v242 offset:0
rt v[210:211],v242 offset:1024
mm v[124:127],v[220:223],v[172:175],v[124:127]
v_lshl_add_u32 v239, v236, 11, v239
s_lshl_b32 s92, s77, 16
mm v[124:127],v[216:219],v[188:191],v[124:127]
s_lshl_b32 s93, s78, 14
s_add_u32 s92, s92, s93
mm v[92:95],v[216:219],v[184:187],v[92:95]
v_add_u32 v239, s92, v239
aw a228,v239
mm v[92:95],v[220:223],v[168:171],v[92:95]
rt v[212:213],v242 offset:8448
rt v[214:215],v242 offset:9472
mm v[60:63],v[220:223],v[164:167],v[60:63]
ds_write_b128 v238, v[248:251] offset:2048
mm v[60:63],v[216:219],v[180:183],v[60:63]
st a[176:179],v255,s[48:51],s18 offen offset:3072 sc1
mm v[28:31],v[216:219],v[176:179],v[28:31]
wt vmcnt(16) lgkmcnt(5)
v_bfe_u32 v239, v255, 6, 2
mm v[28:31],v[220:223],v[160:163],v[28:31]
rt v[216:217],v243 offset:0
rt v[218:219],v243 offset:1024
mm v[0:3],v[192:195],v[128:131],v[0:3]
ar v224,a0
ar v225,a1
mm v[0:3],v[196:199],a[104:107],v[0:3]
ld v[160:163],v244,s[52:55],s91 offen offset:0 sc1
mm v[32:35],v[196:199],a[108:111],v[32:35]
ar v226,a2
ar v227,a3
mm v[32:35],v[192:195],v[140:143],v[32:35]
rt v[220:221],v243 offset:8448
rt v[222:223],v243 offset:9472
mm v[64:67],v[192:195],a[96:99],v[64:67]
ar v228,a4
ar v229,a5
mm v[64:67],v[196:199],a[112:115],v[64:67]
ld v[164:167],v245,s[52:55],s91 offen offset:0 sc1
mm v[96:99],v[196:199],a[116:119],v[96:99]
ar v230,a6
ar v231,a7
mm v[96:99],v[192:195],a[100:103],v[96:99]
rt v[196:197],v240 offset:128
rt v[198:199],v240 offset:1152
mm v[100:103],v[200:203],a[100:103],v[100:103]
s_add_u32 m0, s83, 52864
cv v248,v224,v225
mm v[100:103],v[204:207],a[116:119],v[100:103]
ld v234,s[44:47],s80 offen lds
mm v[68:71],v[204:207],a[112:115],v[68:71]
cv v249,v226,v227
cv v250,v228,v229
mm v[68:71],v[200:203],a[96:99],v[68:71]
rt v[192:193],v240 offset:8576
rt v[194:195],v240 offset:9600
mm v[36:39],v[200:203],v[140:143],v[36:39]
cv v251,v230,v231
aw a0,v248
mm v[36:39],v[204:207],a[108:111],v[36:39]
ld v[168:171],v246,s[52:55],s91 offen offset:0 sc1
mm v[4:7],v[204:207],a[104:107],v[4:7]
wt lgkmcnt(4)
aw a1,v249
mm v[4:7],v[200:203],v[128:131],v[4:7]
rt v[204:205],v241 offset:128
rt v[206:207],v241 offset:1152
mm v[8:11],v[208:211],v[128:131],v[8:11]
s_add_u32 m0, s83, 53888
aw a2,v250
mm v[8:11],v[212:215],a[104:107],v[8:11]
ld v235,s[44:47],s80 offen lds
mm v[40:43],v[212:215],a[108:111],v[40:43]
s_add_u32 s80, s80, 524288
aw a3,v251
mm v[40:43],v[208:211],v[140:143],v[40:43]
rt v[200:201],v241 offset:8576
rt v[202:203],v241 offset:9600
mm v[72:75],v[208:211],a[96:99],v[72:75]
v_lshlrev_b32 v239, 6, v239
v_lshl_add_u32 v239, v236, 4, v239
mm v[72:75],v[212:215],a[112:115],v[72:75]
ld v[172:175],v247,s[52:55],s91 offen offset:0 sc1
mm v[104:107],v[212:215],a[116:119],v[104:107]
s_lshl_b32 s92, s78, 8
v_add_u32 v239, s92, v239
mm v[104:107],v[208:211],a[100:103],v[104:107]
rt v[212:213],v242 offset:128
rt v[214:215],v242 offset:1152
mm v[108:111],v[216:219],a[100:103],v[108:111]
v_mul_u32_u24 v239, 7168, v239
v_and_or_b32 v239, v255, 48, v239
mm v[108:111],v[220:223],a[116:119],v[108:111]
aw a229,v239
v_and_b32 v239, 0xf0, v255
mm v[76:79],v[220:223],a[112:115],v[76:79]
s_mov_b32 s18, 16384
v_lshl_add_u32 v239, v236, 14, v239
mm v[76:79],v[216:219],a[96:99],v[76:79]
rt v[208:209],v242 offset:8576
rt v[210:211],v242 offset:9600
mm v[44:47],v[216:219],v[140:143],v[44:47]
ds_write_b128 v238, v[248:251] offset:3072
mm v[44:47],v[220:223],a[108:111],v[44:47]
st a[180:183],v255,s[48:51],s18 offen offset:0 sc1
mm v[12:15],v[220:223],a[104:107],v[12:15]
wt lgkmcnt(5)
s_lshl_b32 s92, s3, 9
mm v[12:15],v[216:219],v[128:131],v[12:15]
rt v[220:221],v243 offset:128
rt v[222:223],v243 offset:1152
mm v[16:19],v[196:199],v[128:131],v[16:19]
ar v224,a72
ar v225,a73
mm v[16:19],v[192:195],a[104:107],v[16:19]
ld v[176:179],v244,s[52:55],s91 offen offset:1024 sc1
mm v[48:51],v[192:195],a[108:111],v[48:51]
ar v226,a74
ar v227,a75
mm v[48:51],v[196:199],v[140:143],v[48:51]
rt v[216:217],v243 offset:8576
rt v[218:219],v243 offset:9600
mm v[80:83],v[196:199],a[96:99],v[80:83]
ar v228,a76
.La5_LW1_12:
s_cbranch_execz .La5_LW1_R0
ar v229,a77
mm v[80:83],v[192:195],a[112:115],v[80:83]
ld v[180:183],v245,s[52:55],s91 offen offset:1024 sc1
mm v[112:115],v[192:195],a[116:119],v[112:115]
ar v230,a78
ar v231,a79
mm v[112:115],v[196:199],a[100:103],v[112:115]
cv v248,v224,v225
cv v249,v226,v227
mm v[116:119],v[204:207],a[100:103],v[116:119]
wt lgkmcnt(0)
s_barrier
mm v[116:119],v[200:203],a[116:119],v[116:119]
ld v[184:187],v246,s[52:55],s91 offen offset:1024 sc1
mm v[84:87],v[200:203],a[112:115],v[84:87]
rt v[192:193],v240 offset:16896
rt v[194:195],v240 offset:17920
mm v[84:87],v[204:207],a[96:99],v[84:87]
rt v[196:197],v240 offset:25344
rt v[198:199],v240 offset:26368
mm v[52:55],v[204:207],v[140:143],v[52:55]
s_add_u32 m0, s83, 0
cv v250,v228,v229
mm v[52:55],v[200:203],a[108:111],v[52:55]
ld v232,s[44:47],s80 offen lds
mm v[20:23],v[200:203],a[104:107],v[20:23]
cv v251,v230,v231
aw a4,v248
mm v[20:23],v[204:207],v[128:131],v[20:23]
rt v[200:201],v241 offset:16896
rt v[202:203],v241 offset:17920
mm v[24:27],v[212:215],v[128:131],v[24:27]
aw a5,v249
aw a6,v250
mm v[24:27],v[208:211],a[104:107],v[24:27]
ld v[188:191],v247,s[52:55],s91 offen offset:1024 sc1
mm v[56:59],v[208:211],a[108:111],v[56:59]
aw a7,v251
v_add_u32 v239, s92, v239
mm v[56:59],v[212:215],v[140:143],v[56:59]
rt v[204:205],v241 offset:25344
rt v[206:207],v241 offset:26368
mm v[88:91],v[212:215],a[96:99],v[88:91]
s_add_u32 m0, s83, 1024
aw a230,v239
mm v[88:91],v[208:211],a[112:115],v[88:91]
ld v233,s[44:47],s80 offen lds
mm v[120:123],v[208:211],a[116:119],v[120:123]
v_add_u32 v239, 4294886912, v240
aw a224,v239
mm v[120:123],v[212:215],a[100:103],v[120:123]
rt v[208:209],v242 offset:16896
rt v[210:211],v242 offset:17920
mm v[124:127],v[220:223],a[100:103],v[124:127]
v_bfe_u32 v239, v255, 4, 4
v_lshlrev_b32 v236, 2, v236
mm v[124:127],v[216:219],a[116:119],v[124:127]
v_sub_u32 v239, v239, v236
v_subrev_u32 v236, 0, v239
mm v[92:95],v[216:219],a[112:115],v[92:95]
v_ashrrev_i32 v236, 31, v236
v_and_b32 v236, 0xff800000, v236
mm v[92:95],v[220:223],a[96:99],v[92:95]
rt v[212:213],v242 offset:25344
rt v[214:215],v242 offset:26368
mm v[60:63],v[220:223],v[140:143],v[60:63]
ds_write_b128 v238, v[248:251] offset:4096
mm v[60:63],v[216:219],a[108:111],v[60:63]
aw a236,v236
v_subrev_u32 v236, 1, v239
mm v[28:31],v[216:219],a[104:107],v[28:31]
wt vmcnt(15) lgkmcnt(5)
v_ashrrev_i32 v236, 31, v236
mm v[28:31],v[220:223],v[128:131],v[28:31]
rt v[216:217],v243 offset:16896
rt v[218:219],v243 offset:17920
mm v[0:3],v[192:195],v[132:135],v[0:3]
ar v224,a8
ar v225,a9
mm v[0:3],v[196:199],v[152:155],v[0:3]
ld v[128:131],v244,s[52:55],s91 offen offset:2048 sc1
mm v[32:35],v[196:199],v[156:159],v[32:35]
ar v226,a10
ar v227,a11
mm v[32:35],v[192:195],v[136:139],v[32:35]
rt v[220:221],v243 offset:25344
rt v[222:223],v243 offset:26368
mm v[64:67],v[192:195],v[144:147],v[64:67]
ar v228,a12
ar v229,a13
mm v[64:67],v[196:199],a[120:123],v[64:67]
ld v[140:143],v245,s[52:55],s91 offen offset:2048 sc1
mm v[96:99],v[196:199],a[124:127],v[96:99]
ar v230,a14
ar v231,a15
mm v[96:99],v[192:195],v[148:151],v[96:99]
rt v[196:197],v240 offset:17024
rt v[198:199],v240 offset:18048
mm v[100:103],v[200:203],v[148:151],v[100:103]
s_add_u32 m0, s83, 2176
cv v248,v224,v225
mm v[100:103],v[204:207],a[124:127],v[100:103]
ld v234,s[44:47],s80 offen lds
mm v[68:71],v[204:207],a[120:123],v[68:71]
cv v249,v226,v227
cv v250,v228,v229
mm v[68:71],v[200:203],v[144:147],v[68:71]
rt v[192:193],v240 offset:25472
rt v[194:195],v240 offset:26496
mm v[36:39],v[200:203],v[136:139],v[36:39]
cv v251,v230,v231
aw a8,v248
mm v[36:39],v[204:207],v[156:159],v[36:39]
ld a[96:99],v246,s[52:55],s91 offen offset:2048 sc1
mm v[4:7],v[204:207],v[152:155],v[4:7]
wt lgkmcnt(4)
aw a9,v249
mm v[4:7],v[200:203],v[132:135],v[4:7]
rt v[204:205],v241 offset:17024
rt v[206:207],v241 offset:18048
mm v[8:11],v[208:211],v[132:135],v[8:11]
s_add_u32 m0, s83, 3200
aw a10,v250
mm v[8:11],v[212:215],v[152:155],v[8:11]
ld v235,s[44:47],s80 offen lds
mm v[40:43],v[212:215],v[156:159],v[40:43]
s_add_u32 s80, s80, 524288
aw a11,v251
mm v[40:43],v[208:211],v[136:139],v[40:43]
rt v[200:201],v241 offset:25472
rt v[202:203],v241 offset:26496
mm v[72:75],v[208:211],v[144:147],v[72:75]
v_and_b32 v236, 0xff800000, v236
aw a237,v236
mm v[72:75],v[212:215],a[120:123],v[72:75]
ld a[100:103],v247,s[52:55],s91 offen offset:2048 sc1
mm v[104:107],v[212:215],a[124:127],v[104:107]
v_subrev_u32 v236, 2, v239
v_ashrrev_i32 v236, 31, v236
mm v[104:107],v[208:211],v[148:151],v[104:107]
rt v[212:213],v242 offset:17024
rt v[214:215],v242 offset:18048
mm v[108:111],v[216:219],v[148:151],v[108:111]
v_and_b32 v236, 0xff800000, v236
aw a238,v236
mm v[108:111],v[220:223],a[124:127],v[108:111]
v_subrev_u32 v236, 3, v239
v_ashrrev_i32 v236, 31, v236
mm v[76:79],v[220:223],a[120:123],v[76:79]
v_and_b32 v236, 0xff800000, v236
aw a239,v236
mm v[76:79],v[216:219],v[144:147],v[76:79]
rt v[208:209],v242 offset:25472
rt v[210:211],v242 offset:26496
mm v[44:47],v[216:219],v[136:139],v[44:47]
ds_write_b128 v238, v[248:251] offset:5120
mm v[44:47],v[220:223],v[156:159],v[44:47]
st a[184:187],v255,s[48:51],s18 offen offset:1024 sc1
mm v[12:15],v[220:223],v[152:155],v[12:15]
wt lgkmcnt(5)
v_lshlrev_b32 v236, 4, v239
mm v[12:15],v[216:219],v[132:135],v[12:15]
rt v[220:221],v243 offset:17024
rt v[222:223],v243 offset:18048
mm v[16:19],v[196:199],v[132:135],v[16:19]
ar v224,a80
ar v225,a81
mm v[16:19],v[192:195],v[152:155],v[16:19]
ld a[104:107],v244,s[52:55],s91 offen offset:3072 sc1
mm v[48:51],v[192:195],v[156:159],v[48:51]
ar v226,a82
ar v227,a83
mm v[48:51],v[196:199],v[136:139],v[48:51]
rt v[216:217],v243 offset:25472
rt v[218:219],v243 offset:26496
mm v[80:83],v[196:199],v[144:147],v[80:83]
ar v228,a84
ar v229,a85
mm v[80:83],v[192:195],a[120:123],v[80:83]
ld a[108:111],v245,s[52:55],s91 offen offset:3072 sc1
mm v[112:115],v[192:195],a[124:127],v[112:115]
ar v230,a86
ar v231,a87
mm v[112:115],v[196:199],v[148:151],v[112:115]
cv v248,v224,v225
cv v249,v226,v227
mm v[116:119],v[204:207],v[148:151],v[116:119]
wt lgkmcnt(0)
s_barrier
mm v[116:119],v[200:203],a[124:127],v[116:119]
ld a[112:115],v246,s[52:55],s91 offen offset:3072 sc1
mm v[84:87],v[200:203],a[120:123],v[84:87]
rt v[192:193],v240 offset:33792
rt v[194:195],v240 offset:34816
mm v[84:87],v[204:207],v[144:147],v[84:87]
rt v[196:197],v240 offset:42240
rt v[198:199],v240 offset:43264
mm v[52:55],v[204:207],v[136:139],v[52:55]
s_add_u32 m0, s83, 16896
cv v250,v228,v229
mm v[52:55],v[200:203],v[156:159],v[52:55]
ld v232,s[44:47],s80 offen lds
mm v[20:23],v[200:203],v[152:155],v[20:23]
cv v251,v230,v231
aw a12,v248
mm v[20:23],v[204:207],v[132:135],v[20:23]
rt v[200:201],v241 offset:33792
rt v[202:203],v241 offset:34816
mm v[24:27],v[212:215],v[132:135],v[24:27]
aw a13,v249
aw a14,v250
mm v[24:27],v[208:211],v[152:155],v[24:27]
ld a[116:119],v247,s[52:55],s91 offen offset:3072 sc1
mm v[56:59],v[208:211],v[156:159],v[56:59]
s_add_u32 s91, s91, 4096
aw a15,v251
mm v[56:59],v[212:215],v[136:139],v[56:59]
rt v[204:205],v241 offset:42240
rt v[206:207],v241 offset:43264
mm v[88:91],v[212:215],v[144:147],v[88:91]
s_add_u32 m0, s83, 17920
s_mov_b32 s92, 0x3f80
mm v[88:91],v[208:211],a[120:123],v[88:91]
ld v233,s[44:47],s80 offen lds
mm v[120:123],v[208:211],a[124:127],v[120:123]
v_lshlrev_b32 v236, v236, s92
v_cmp_gt_u32 vcc, 2, v239
mm v[120:123],v[212:215],v[148:151],v[120:123]
rt v[208:209],v242 offset:33792
rt v[210:211],v242 offset:34816
mm v[124:127],v[220:223],v[148:151],v[124:127]
v_subrev_u32 v239, 2, v239
v_cmp_gt_u32 s[92:93], 2, v239
mm v[124:127],v[216:219],a[124:127],v[124:127]
v_cndmask_b32 v239, 0, v236, vcc
aw a212,0
mm v[92:95],v[216:219],a[120:123],v[92:95]
aw a214,v239
v_cndmask_b32 v239, 0, v236, s[92:93]
mm v[92:95],v[220:223],v[144:147],v[92:95]
rt v[212:213],v242 offset:42240
rt v[214:215],v242 offset:43264
mm v[60:63],v[220:223],v[136:139],v[60:63]
v_mov_b32 v236, 0x3f803f80
aw a215,v239
mm v[60:63],v[216:219],v[156:159],v[60:63]
st a[188:191],v255,s[48:51],s18 offen offset:2048 sc1
mm v[28:31],v[216:219],v[152:155],v[28:31]
wt vmcnt(15) lgkmcnt(4)
aw a213,0
mm v[28:31],v[220:223],v[132:135],v[28:31]
rt v[216:217],v243 offset:33792
rt v[218:219],v243 offset:34816
mm v[0:3],v[192:195],v[160:163],v[0:3]
ar v224,a16
ar v225,a17
mm v[0:3],v[196:199],v[176:179],v[0:3]
ld v[132:135],v244,s[52:55],s91 offen offset:0 sc1
mm v[32:35],v[196:199],v[180:183],v[32:35]
ar v226,a18
ar v227,a19
mm v[32:35],v[192:195],v[164:167],v[32:35]
rt v[220:221],v243 offset:42240
rt v[222:223],v243 offset:43264
mm v[64:67],v[192:195],v[168:171],v[64:67]
ar v228,a20
ar v229,a21
mm v[64:67],v[196:199],v[184:187],v[64:67]
ld v[136:139],v245,s[52:55],s91 offen offset:0 sc1
mm v[96:99],v[196:199],v[188:191],v[96:99]
ar v230,a22
ar v231,a23
mm v[96:99],v[192:195],v[172:175],v[96:99]
rt v[196:197],v240 offset:33920
rt v[198:199],v240 offset:34944
mm v[100:103],v[200:203],v[172:175],v[100:103]
s_add_u32 m0, s83, 19072
cv v248,v224,v225
mm v[100:103],v[204:207],v[188:191],v[100:103]
ld v234,s[44:47],s80 offen lds
mm v[68:71],v[204:207],v[184:187],v[68:71]
cv v249,v226,v227
cv v250,v228,v229
mm v[68:71],v[200:203],v[168:171],v[68:71]
rt v[192:193],v240 offset:42368
rt v[194:195],v240 offset:43392
mm v[36:39],v[200:203],v[164:167],v[36:39]
cv v251,v230,v231
aw a16,v248
mm v[36:39],v[204:207],v[180:183],v[36:39]
ld v[144:147],v246,s[52:55],s91 offen offset:0 sc1
mm v[4:7],v[204:207],v[176:179],v[4:7]
wt lgkmcnt(4)
aw a17,v249
mm v[4:7],v[200:203],v[160:163],v[4:7]
rt v[204:205],v241 offset:33920
rt v[206:207],v241 offset:34944
mm v[8:11],v[208:211],v[160:163],v[8:11]
s_add_u32 m0, s83, 20096
aw a18,v250
mm v[8:11],v[212:215],v[176:179],v[8:11]
ld v235,s[44:47],s80 offen lds
mm v[40:43],v[212:215],v[180:183],v[40:43]
s_add_u32 s80, s80, 524288
aw a19,v251
mm v[40:43],v[208:211],v[164:167],v[40:43]
rt v[200:201],v241 offset:42368
rt v[202:203],v241 offset:43392
mm v[72:75],v[208:211],v[168:171],v[72:75]
aw a216,0
aw a217,0
mm v[72:75],v[212:215],v[184:187],v[72:75]
ld v[148:151],v247,s[52:55],s91 offen offset:0 sc1
mm v[104:107],v[212:215],v[188:191],v[104:107]
v_lshrrev_b32 v239, 4, v255
v_xor_b32 v239, v239, v255
mm v[104:107],v[208:211],v[172:175],v[104:107]
rt v[212:213],v242 offset:33920
rt v[214:215],v242 offset:34944
mm v[108:111],v[216:219],v[172:175],v[108:111]
v_and_b32 v239, 32, v239
v_cmp_eq_u32 vcc, 0, v239
mm v[108:111],v[220:223],v[188:191],v[108:111]
s_mov_b32 s78, 4294950911
s_mul_i32 s70, s70, s22
mm v[76:79],v[220:223],v[184:187],v[76:79]
v_cndmask_b32 v236, 0, v236, vcc
s_sub_u32 s92, 4294967294, s3
mm v[76:79],v[216:219],v[168:171],v[76:79]
rt v[208:209],v242 offset:42368
rt v[210:211],v242 offset:43392
mm v[44:47],v[216:219],v[164:167],v[44:47]
s_add_u32 s92, s92, s7
s_mul_i32 s92, s92, s21
mm v[44:47],v[220:223],v[180:183],v[44:47]
st a[0:3],v255,s[48:51],s18 offen offset:3072 sc1
mm v[12:15],v[220:223],v[176:179],v[12:15]
wt lgkmcnt(4)
s_add_u32 s90, s90, s92
mm v[12:15],v[216:219],v[160:163],v[12:15]
rt v[220:221],v243 offset:33920
rt v[222:223],v243 offset:34944
mm v[16:19],v[196:199],v[160:163],v[16:19]
ar v224,a88
ar v225,a89
mm v[16:19],v[192:195],v[176:179],v[16:19]
ld v[152:155],v244,s[52:55],s91 offen offset:1024 sc1
mm v[48:51],v[192:195],v[180:183],v[48:51]
ar v226,a90
ar v227,a91
mm v[48:51],v[196:199],v[164:167],v[48:51]
rt v[216:217],v243 offset:42368
rt v[218:219],v243 offset:43392
mm v[80:83],v[196:199],v[168:171],v[80:83]
ar v228,a92
ar v229,a93
mm v[80:83],v[192:195],v[184:187],v[80:83]
ld v[156:159],v245,s[52:55],s91 offen offset:1024 sc1
mm v[112:115],v[192:195],v[188:191],v[112:115]
ar v230,a94
ar v231,a95
mm v[112:115],v[196:199],v[172:175],v[112:115]
cv v248,v224,v225
cv v249,v226,v227
mm v[116:119],v[204:207],v[172:175],v[116:119]
wt lgkmcnt(0)
s_barrier
mm v[116:119],v[200:203],v[188:191],v[116:119]
ld a[120:123],v246,s[52:55],s91 offen offset:1024 sc1
mm v[84:87],v[200:203],v[184:187],v[84:87]
rt v[192:193],v240 offset:50688
rt v[194:195],v240 offset:51712
mm v[84:87],v[204:207],v[168:171],v[84:87]
rt v[196:197],v240 offset:59136
rt v[198:199],v240 offset:60160
mm v[52:55],v[204:207],v[164:167],v[52:55]
s_add_u32 m0, s83, 33792
cv v250,v228,v229
mm v[52:55],v[200:203],v[180:183],v[52:55]
ld v232,s[44:47],s80 offen lds
mm v[20:23],v[200:203],v[176:179],v[20:23]
cv v251,v230,v231
aw a20,v248
mm v[20:23],v[204:207],v[160:163],v[20:23]
rt v[200:201],v241 offset:50688
rt v[202:203],v241 offset:51712
mm v[24:27],v[212:215],v[160:163],v[24:27]
aw a21,v249
aw a22,v250
mm v[24:27],v[208:211],v[176:179],v[24:27]
ld a[124:127],v247,s[52:55],s91 offen offset:1024 sc1
mm v[56:59],v[208:211],v[180:183],v[56:59]
aw a23,v251
s_mov_b32 s17, 1073741824
mm v[56:59],v[212:215],v[164:167],v[56:59]
rt v[204:205],v241 offset:59136
rt v[206:207],v241 offset:60160
mm v[88:91],v[212:215],v[168:171],v[88:91]
s_add_u32 m0, s83, 34816
s_lshl_b32 s77, s76, 16
mm v[88:91],v[208:211],v[184:187],v[88:91]
ld v233,s[44:47],s80 offen lds
mm v[120:123],v[208:211],v[188:191],v[120:123]
s_sub_u32 s77, s77, 8192
s_lshl4_add_u32 s81, s75, s74
mm v[120:123],v[212:215],v[172:175],v[120:123]
rt v[208:209],v242 offset:50688
rt v[210:211],v242 offset:51712
mm v[124:127],v[220:223],v[172:175],v[124:127]
s_lshl3_add_u32 s81, s81, 1
mm v[124:127],v[216:219],v[188:191],v[124:127]
s_cmp_eq_u32 s73, 0
s_cselect_b32 s16, s77, 0
mm v[92:95],v[216:219],v[184:187],v[92:95]
s_mov_b32 s18, 20480
s_mul_i32 s92, s75, 4294836224
mm v[92:95],v[220:223],v[168:171],v[92:95]
rt v[212:213],v242 offset:59136
rt v[214:215],v242 offset:60160
mm v[60:63],v[220:223],v[164:167],v[60:63]
s_add_u32 s92, s92, s88
s_add_u32 s19, s92, 393216
mm v[60:63],v[216:219],v[180:183],v[60:63]
st a[4:7],v255,s[48:51],s18 offen offset:0 sc1
mm v[28:31],v[216:219],v[176:179],v[28:31]
wt vmcnt(16) lgkmcnt(4)
aw a208,v236
mm v[28:31],v[220:223],v[160:163],v[28:31]
rt v[216:217],v243 offset:50688
rt v[218:219],v243 offset:51712
mm v[0:3],v[192:195],v[128:131],v[0:3]
ar v224,a24
ar v225,a25
mm v[0:3],v[196:199],a[104:107],v[0:3]
ld v[160:163],v244,s[52:55],s91 offen offset:2048 sc1
mm v[32:35],v[196:199],a[108:111],v[32:35]
ar v226,a26
ar v227,a27
mm v[32:35],v[192:195],v[140:143],v[32:35]
rt v[220:221],v243 offset:59136
rt v[222:223],v243 offset:60160
mm v[64:67],v[192:195],a[96:99],v[64:67]
ar v228,a28
ar v229,a29
mm v[64:67],v[196:199],a[112:115],v[64:67]
ld v[164:167],v245,s[52:55],s91 offen offset:2048 sc1
mm v[96:99],v[196:199],a[116:119],v[96:99]
ar v230,a30
ar v231,a31
mm v[96:99],v[192:195],a[100:103],v[96:99]
rt v[196:197],v240 offset:50816
rt v[198:199],v240 offset:51840
mm v[100:103],v[200:203],a[100:103],v[100:103]
s_add_u32 m0, s83, 35968
cv v248,v224,v225
mm v[100:103],v[204:207],a[116:119],v[100:103]
ld v234,s[44:47],s80 offen lds
mm v[68:71],v[204:207],a[112:115],v[68:71]
cv v249,v226,v227
cv v250,v228,v229
mm v[68:71],v[200:203],a[96:99],v[68:71]
rt v[192:193],v240 offset:59264
rt v[194:195],v240 offset:60288
mm v[36:39],v[200:203],v[140:143],v[36:39]
cv v251,v230,v231
aw a24,v248
mm v[36:39],v[204:207],a[108:111],v[36:39]
ld v[168:171],v246,s[52:55],s91 offen offset:2048 sc1
mm v[4:7],v[204:207],a[104:107],v[4:7]
wt lgkmcnt(4)
aw a25,v249
mm v[4:7],v[200:203],v[128:131],v[4:7]
rt v[204:205],v241 offset:50816
rt v[206:207],v241 offset:51840
mm v[8:11],v[208:211],v[128:131],v[8:11]
s_add_u32 m0, s83, 36992
aw a26,v250
mm v[8:11],v[212:215],a[104:107],v[8:11]
ld v235,s[44:47],s80 offen lds
mm v[40:43],v[212:215],a[108:111],v[40:43]
s_add_u32 s80, s80, 524288
aw a27,v251
mm v[40:43],v[208:211],v[140:143],v[40:43]
rt v[200:201],v241 offset:59264
rt v[202:203],v241 offset:60288
mm v[72:75],v[208:211],a[96:99],v[72:75]
aw a209,v236
aw a210,v236
mm v[72:75],v[212:215],a[112:115],v[72:75]
ld v[172:175],v247,s[52:55],s91 offen offset:2048 sc1
mm v[104:107],v[212:215],a[116:119],v[104:107]
aw a211,v236
aw a232,0
mm v[104:107],v[208:211],a[100:103],v[104:107]
rt v[212:213],v242 offset:50816
rt v[214:215],v242 offset:51840
mm v[108:111],v[216:219],a[100:103],v[108:111]
aw a233,0
aw a234,0
mm v[108:111],v[220:223],a[116:119],v[108:111]
aw a235,0
s_lshl_b32 s20, s72, 10
mm v[76:79],v[220:223],a[112:115],v[76:79]
s_add_u32 s20, s20, 16384
s_mov_b32 s4, 1466176288
mm v[76:79],v[216:219],a[96:99],v[76:79]
rt v[208:209],v242 offset:59264
rt v[210:211],v242 offset:60288
mm v[44:47],v[216:219],v[140:143],v[44:47]
s_cmp_ge_u32 s2, 64
s_cselect_b32 s4, 1182081808, s4
mm v[44:47],v[220:223],a[108:111],v[44:47]
st a[8:11],v255,s[48:51],s18 offen offset:1024 sc1
mm v[12:15],v[220:223],a[104:107],v[12:15]
wt lgkmcnt(4)
mm v[12:15],v[216:219],v[128:131],v[12:15]
rt v[220:221],v243 offset:50816
rt v[222:223],v243 offset:51840
mm v[16:19],v[196:199],v[128:131],v[16:19]
ar v224,a240
ar v225,a241
mm v[16:19],v[192:195],a[104:107],v[16:19]
ld v[176:179],v244,s[52:55],s91 offen offset:3072 sc1
mm v[48:51],v[192:195],a[108:111],v[48:51]
ar v226,a242
ar v227,a243
mm v[48:51],v[196:199],v[140:143],v[48:51]
rt v[216:217],v243 offset:59264
rt v[218:219],v243 offset:60288
mm v[80:83],v[196:199],a[96:99],v[80:83]
ar v228,a244
ar v229,a245
mm v[80:83],v[192:195],a[112:115],v[80:83]
ld v[180:183],v245,s[52:55],s91 offen offset:3072 sc1
mm v[112:115],v[192:195],a[116:119],v[112:115]
ar v230,a246
ar v231,a247
.La5_LW1_13:
s_cbranch_execz .La5_LW1_R0
mm v[112:115],v[196:199],a[100:103],v[112:115]
cv v248,v224,v225
cv v249,v226,v227
mm v[116:119],v[204:207],a[100:103],v[116:119]
wt lgkmcnt(0)
s_barrier
mm v[116:119],v[200:203],a[116:119],v[116:119]
ld v[184:187],v246,s[52:55],s91 offen offset:3072 sc1
mm v[84:87],v[200:203],a[112:115],v[84:87]
rt v[192:193],v240 offset:0
rt v[194:195],v240 offset:1024
mm v[84:87],v[204:207],a[96:99],v[84:87]
rt v[196:197],v240 offset:8448
rt v[198:199],v240 offset:9472
mm v[52:55],v[204:207],v[140:143],v[52:55]
s_add_u32 m0, s83, 50688
cv v250,v228,v229
mm v[52:55],v[200:203],a[108:111],v[52:55]
ld v232,s[44:47],s80 offen lds
mm v[20:23],v[200:203],a[104:107],v[20:23]
cv v251,v230,v231
aw a28,v248
mm v[20:23],v[204:207],v[128:131],v[20:23]
rt v[200:201],v241 offset:0
rt v[202:203],v241 offset:1024
mm v[24:27],v[212:215],v[128:131],v[24:27]
aw a29,v249
aw a30,v250
mm v[24:27],v[208:211],a[104:107],v[24:27]
ld v[188:191],v247,s[52:55],s91 offen offset:3072 sc1
mm v[56:59],v[208:211],a[108:111],v[56:59]
s_add_u32 s91, s91, 4096
aw a31,v251
mm v[56:59],v[212:215],v[140:143],v[56:59]
rt v[204:205],v241 offset:8448
rt v[206:207],v241 offset:9472
mm v[88:91],v[212:215],a[96:99],v[88:91]
s_add_u32 m0, s83, 51712
mm v[88:91],v[208:211],a[112:115],v[88:91]
ld v233,s[44:47],s80 offen lds
mm v[120:123],v[208:211],a[116:119],v[120:123]
s_cmp_ge_u32 s2, 128
s_cselect_b32 s4, 1967534368, s4
mm v[120:123],v[212:215],a[100:103],v[120:123]
rt v[208:209],v242 offset:0
rt v[210:211],v242 offset:1024
mm v[124:127],v[220:223],a[100:103],v[124:127]
s_cmp_ge_u32 s2, 192
s_cselect_b32 s4, 1683431728, s4
mm v[124:127],v[216:219],a[116:119],v[124:127]
v_bfe_u32 v236, v255, 4, 3
v_lshlrev_b32 v236, 2, v236
mm v[92:95],v[216:219],a[112:115],v[92:95]
v_lshrrev_b32 v236, v236, s4
v_and_b32 v236, 15, v236
mm v[92:95],v[220:223],a[96:99],v[92:95]
rt v[212:213],v242 offset:8448
rt v[214:215],v242 offset:9472
mm v[60:63],v[220:223],v[140:143],v[60:63]
v_bfe_u32 v239, v255, 7, 1
v_mul_u32_u24 v239, 28, v239
mm v[60:63],v[216:219],a[108:111],v[60:63]
v_xor_b32 v236, v236, v239
v_xor_b32 v236, s72, v236
mm v[28:31],v[216:219],a[104:107],v[28:31]
wt vmcnt(15) lgkmcnt(4)
v_lshlrev_b32 v236, 3, v236
mm v[28:31],v[220:223],v[128:131],v[28:31]
rt v[216:217],v243 offset:0
rt v[218:219],v243 offset:1024
mm v[0:3],v[192:195],v[132:135],v[0:3]
ar v224,a32
ar v225,a33
aw a225,v236
mm v[0:3],v[196:199],v[152:155],v[0:3]
ld v[128:131],v244,s[52:55],s91 offen offset:0 sc1
s_lshl3_add_u32 s5, s76, s101
mm v[32:35],v[196:199],v[156:159],v[32:35]
ar v226,a34
ar v227,a35
s_lshl2_add_u32 s5, s5, s3
mm v[32:35],v[192:195],v[136:139],v[32:35]
rt v[220:221],v243 offset:8448
rt v[222:223],v243 offset:9472
s_lshl_b32 s5, s5, 8
mm v[64:67],v[192:195],v[144:147],v[64:67]
ar v228,a36
ar v229,a37
s_add_u32 s5, s5, 1048576
mm v[64:67],v[196:199],a[120:123],v[64:67]
ld v[140:143],v245,s[52:55],s91 offen offset:0 sc1
mm v[96:99],v[196:199],a[124:127],v[96:99]
ar v230,a38
ar v231,a39
mm v[96:99],v[192:195],v[148:151],v[96:99]
rt v[196:197],v240 offset:128
rt v[198:199],v240 offset:1152
mm v[100:103],v[200:203],v[148:151],v[100:103]
s_add_u32 m0, s83, 52864
cv v248,v224,v225
mm v[100:103],v[204:207],a[124:127],v[100:103]
ld v234,s[44:47],s80 offen lds
mm v[68:71],v[204:207],a[120:123],v[68:71]
cv v249,v226,v227
cv v250,v228,v229
mm v[68:71],v[200:203],v[144:147],v[68:71]
rt v[192:193],v240 offset:8576
rt v[194:195],v240 offset:9600
mm v[36:39],v[200:203],v[136:139],v[36:39]
cv v251,v230,v231
aw a32,v248
mm v[36:39],v[204:207],v[156:159],v[36:39]
ld a[192:195],v246,s[52:55],s91 offen offset:0 sc1
mm v[4:7],v[204:207],v[152:155],v[4:7]
wt lgkmcnt(4)
aw a33,v249
mm v[4:7],v[200:203],v[132:135],v[4:7]
rt v[204:205],v241 offset:128
rt v[206:207],v241 offset:1152
mm v[8:11],v[208:211],v[132:135],v[8:11]
s_add_u32 m0, s83, 53888
aw a34,v250
mm v[8:11],v[212:215],v[152:155],v[8:11]
ld v235,s[44:47],s80 offen lds
mm v[40:43],v[212:215],v[156:159],v[40:43]
aw a35,v251
mm v[40:43],v[208:211],v[136:139],v[40:43]
rt v[200:201],v241 offset:8576
rt v[202:203],v241 offset:9600
mm v[72:75],v[208:211],v[144:147],v[72:75]
mm v[72:75],v[212:215],a[120:123],v[72:75]
ld a[196:199],v247,s[52:55],s91 offen offset:0 sc1
mm v[104:107],v[212:215],a[124:127],v[104:107]
mm v[104:107],v[208:211],v[148:151],v[104:107]
rt v[212:213],v242 offset:128
rt v[214:215],v242 offset:1152
mm v[108:111],v[216:219],v[148:151],v[108:111]
mm v[108:111],v[220:223],a[124:127],v[108:111]
mm v[76:79],v[220:223],a[120:123],v[76:79]
mm v[76:79],v[216:219],v[144:147],v[76:79]
rt v[208:209],v242 offset:8576
rt v[210:211],v242 offset:9600
mm v[44:47],v[216:219],v[136:139],v[44:47]
mm v[44:47],v[220:223],v[156:159],v[44:47]
st a[12:15],v255,s[48:51],s18 offen offset:2048 sc1
mm v[12:15],v[220:223],v[152:155],v[12:15]
wt lgkmcnt(4)
mm v[12:15],v[216:219],v[132:135],v[12:15]
rt v[220:221],v243 offset:128
rt v[222:223],v243 offset:1152
mm v[16:19],v[196:199],v[132:135],v[16:19]
ar v224,a248
ar v225,a249
mm v[16:19],v[192:195],v[152:155],v[16:19]
ld a[200:203],v244,s[52:55],s91 offen offset:1024 sc1
mm v[48:51],v[192:195],v[156:159],v[48:51]
ar v226,a250
ar v227,a251
mm v[48:51],v[196:199],v[136:139],v[48:51]
rt v[216:217],v243 offset:8576
rt v[218:219],v243 offset:9600
mm v[80:83],v[196:199],v[144:147],v[80:83]
ar v228,a252
ar v229,a253
mm v[80:83],v[192:195],a[120:123],v[80:83]
ld a[204:207],v245,s[52:55],s91 offen offset:1024 sc1
mm v[112:115],v[192:195],a[124:127],v[112:115]
ar v230,a254
ar v231,a255
mm v[112:115],v[196:199],v[148:151],v[112:115]
cv v248,v224,v225
cv v249,v226,v227
mm v[116:119],v[204:207],v[148:151],v[116:119]
wt lgkmcnt(0)
s_barrier
mm v[116:119],v[200:203],a[124:127],v[116:119]
cv v250,v228,v229
cv v251,v230,v231
mm v[84:87],v[200:203],a[120:123],v[84:87]
rt v[192:193],v240 offset:16896
rt v[194:195],v240 offset:17920
mm v[84:87],v[204:207],v[144:147],v[84:87]
rt v[196:197],v240 offset:25344
rt v[198:199],v240 offset:26368
mm v[52:55],v[204:207],v[136:139],v[52:55]
aw a36,v248
aw a37,v249
mm v[52:55],v[200:203],v[156:159],v[52:55]
aw a38,v250
aw a39,v251
mm v[20:23],v[200:203],v[152:155],v[20:23]
s_getpc_b64 s[92:93]
.La5_LCT2:
s_and_b32 s93, s93, 0xffff
mm v[20:23],v[204:207],v[132:135],v[20:23]
rt v[200:201],v241 offset:16896
rt v[202:203],v241 offset:17920
mm v[24:27],v[212:215],v[132:135],v[24:27]
s_lshr_b32 s94, s2, 3
s_lshl2_add_u32 s94, s94, s3
mm v[24:27],v[208:211],v[152:155],v[24:27]
s_lshl_b32 s94, s94, 10
v_lshrrev_b32 v233, 7, v255
mm v[56:59],v[208:211],v[156:159],v[56:59]
v_lshlrev_b32 v232, 3, v255
v_lshl_add_u32 v232, v233, 28, v232
mm v[56:59],v[212:215],v[136:139],v[56:59]
rt v[204:205],v241 offset:25344
rt v[206:207],v241 offset:26368
mm v[88:91],v[212:215],v[144:147],v[88:91]
v_add_u32 v232, s94, v232
s_mov_b32 s94, .La5_LCODEEND-.La5_LCT2
mm v[88:91],v[208:211],a[120:123],v[88:91]
s_mov_b32 s95, 0x20000
buffer_load_dword a253, v232, s[92:95], 0 offen
mm v[120:123],v[208:211],a[124:127],v[120:123]
s_mov_b32 s80, 8192
mm v[120:123],v[212:215],v[148:151],v[120:123]
rt v[208:209],v242 offset:16896
rt v[210:211],v242 offset:17920
mm v[124:127],v[220:223],v[148:151],v[124:127]
s_cmp_eq_u32 s73, 0
s_cselect_b32 s80, -8192, s80
mm v[124:127],v[216:219],a[124:127],v[124:127]
ld v[148:151],v246,s[52:55],s91 offen offset:1024 sc1
mm v[92:95],v[216:219],a[120:123],v[92:95]
s_add_u32 s83, s83, 4294886912
s_lshl_b32 s92, s76, 21
mm v[92:95],v[220:223],v[144:147],v[92:95]
rt v[212:213],v242 offset:25344
rt v[214:215],v242 offset:26368
mm v[60:63],v[220:223],v[136:139],v[60:63]
ds_write_b128 v238, a[12:15] offset:6144
mm v[60:63],v[216:219],v[156:159],v[60:63]
st a[16:19],v255,s[48:51],s18 offen offset:3072 sc1
mm v[28:31],v[216:219],v[152:155],v[28:31]
wt vmcnt(13) lgkmcnt(5)
s_add_u32 s92, s92, 54525952
mm v[28:31],v[220:223],v[132:135],v[28:31]
rt v[216:217],v243 offset:16896
rt v[218:219],v243 offset:17920
mm v[0:3],v[192:195],v[160:163],v[0:3]
ar v224,a40
ar v225,a41
mm v[0:3],v[196:199],v[176:179],v[0:3]
ld v[132:135],v247,s[52:55],s91 offen offset:1024 sc1
mm v[32:35],v[196:199],v[180:183],v[32:35]
ar v226,a42
ar v227,a43
mm v[32:35],v[192:195],v[164:167],v[32:35]
rt v[220:221],v243 offset:25344
rt v[222:223],v243 offset:26368
mm v[64:67],v[192:195],v[168:171],v[64:67]
ar v228,a44
ar v229,a45
mm v[64:67],v[196:199],v[184:187],v[64:67]
ld a[96:99],v244,s[52:55],s91 offen offset:2048 sc1
mm v[96:99],v[196:199],v[188:191],v[96:99]
ar v230,a46
ar v231,a47
mm v[96:99],v[192:195],v[172:175],v[96:99]
rt v[196:197],v240 offset:17024
rt v[198:199],v240 offset:18048
mm v[100:103],v[200:203],v[172:175],v[100:103]
cv v248,v224,v225
cv v249,v226,v227
mm v[100:103],v[204:207],v[188:191],v[100:103]
ld a[100:103],v245,s[52:55],s91 offen offset:2048 sc1
mm v[68:71],v[204:207],v[184:187],v[68:71]
cv v250,v228,v229
cv v251,v230,v231
mm v[68:71],v[200:203],v[168:171],v[68:71]
rt v[192:193],v240 offset:25472
rt v[194:195],v240 offset:26496
mm v[36:39],v[200:203],v[164:167],v[36:39]
aw a40,v248
aw a41,v249
mm v[36:39],v[204:207],v[180:183],v[36:39]
ld a[104:107],v246,s[52:55],s91 offen offset:2048 sc1
mm v[4:7],v[204:207],v[176:179],v[4:7]
wt lgkmcnt(4)
aw a42,v250
mm v[4:7],v[200:203],v[160:163],v[4:7]
rt v[204:205],v241 offset:17024
rt v[206:207],v241 offset:18048
mm v[8:11],v[208:211],v[160:163],v[8:11]
aw a43,v251
mm v[8:11],v[212:215],v[176:179],v[8:11]
ld a[108:111],v247,s[52:55],s91 offen offset:2048 sc1
mm v[40:43],v[212:215],v[180:183],v[40:43]
s_add_u32 s44, s26, s92
s_addc_u32 s45, s27, 0
mm v[40:43],v[208:211],v[164:167],v[40:43]
rt v[200:201],v241 offset:25472
rt v[202:203],v241 offset:26496
mm v[72:75],v[208:211],v[168:171],v[72:75]
s_and_b32 s45, s45, 0xffff
s_mov_b32 s46, 2097536
mm v[72:75],v[212:215],v[184:187],v[72:75]
s_mov_b32 s95, 4096
s_mov_b32 s96, 8192
mm v[104:107],v[212:215],v[188:191],v[104:107]
s_mov_b32 s18, 0
s_mov_b32 s97, 12288
mm v[104:107],v[208:211],v[172:175],v[104:107]
rt v[212:213],v242 offset:17024
rt v[214:215],v242 offset:18048
mm v[108:111],v[216:219],v[172:175],v[108:111]
s_mul_i32 s92, s3, 196608
s_sub_u32 s93, 1376256, s92
mm v[108:111],v[220:223],v[188:191],v[108:111]
st a[20:23],v255,s[48:51],s18 offen offset:0 sc1
mm v[76:79],v[220:223],v[184:187],v[76:79]
mm v[76:79],v[216:219],v[168:171],v[76:79]
rt v[208:209],v242 offset:25472
rt v[210:211],v242 offset:26496
mm v[44:47],v[216:219],v[164:167],v[44:47]
ds_write_b128 v238, a[16:19] offset:7168
mm v[44:47],v[220:223],v[180:183],v[44:47]
st a[24:27],v255,s[48:51],s18 offen offset:1024 sc1
mm v[12:15],v[220:223],v[176:179],v[12:15]
wt lgkmcnt(5)
mm v[12:15],v[216:219],v[160:163],v[12:15]
rt v[220:221],v243 offset:17024
rt v[222:223],v243 offset:18048
mm v[16:19],v[196:199],v[160:163],v[16:19]
ar v224,a48
ar v225,a49
mm v[16:19],v[192:195],v[176:179],v[16:19]
ld a[112:115],v244,s[52:55],s91 offen offset:3072 sc1
mm v[48:51],v[192:195],v[180:183],v[48:51]
ar v226,a50
ar v227,a51
mm v[48:51],v[196:199],v[164:167],v[48:51]
rt v[216:217],v243 offset:25472
rt v[218:219],v243 offset:26496
mm v[80:83],v[196:199],v[168:171],v[80:83]
ar v228,a52
ar v229,a53
mm v[80:83],v[192:195],v[184:187],v[80:83]
ld a[116:119],v245,s[52:55],s91 offen offset:3072 sc1
mm v[112:115],v[192:195],v[188:191],v[112:115]
ar v230,a54
ar v231,a55
mm v[112:115],v[196:199],v[172:175],v[112:115]
cv v248,v224,v225
cv v249,v226,v227
mm v[116:119],v[204:207],v[172:175],v[116:119]
wt lgkmcnt(0)
s_barrier
mm v[116:119],v[200:203],v[188:191],v[116:119]
ld a[120:123],v246,s[52:55],s91 offen offset:3072 sc1
mm v[84:87],v[200:203],v[184:187],v[84:87]
rt v[192:193],v240 offset:33792
rt v[194:195],v240 offset:34816
mm v[84:87],v[204:207],v[168:171],v[84:87]
rt v[196:197],v240 offset:42240
rt v[198:199],v240 offset:43264
mm v[52:55],v[204:207],v[164:167],v[52:55]
ds_write_b128 v238, a[20:23] offset:32768
mm v[52:55],v[200:203],v[180:183],v[52:55]
ld a[124:127],v247,s[52:55],s91 offen offset:3072 sc1
mm v[20:23],v[200:203],v[176:179],v[20:23]
cv v250,v228,v229
cv v251,v230,v231
mm v[20:23],v[204:207],v[160:163],v[20:23]
rt v[200:201],v241 offset:33792
rt v[202:203],v241 offset:34816
mm v[24:27],v[212:215],v[160:163],v[24:27]
aw a44,v248
aw a45,v249
mm v[24:27],v[208:211],v[176:179],v[24:27]
aw a46,v250
aw a47,v251
mm v[56:59],v[208:211],v[180:183],v[56:59]
mm v[56:59],v[212:215],v[164:167],v[56:59]
rt v[204:205],v241 offset:42240
rt v[206:207],v241 offset:43264
mm v[88:91],v[212:215],v[168:171],v[88:91]
mm v[88:91],v[208:211],v[184:187],v[88:91]
mm v[120:123],v[208:211],v[188:191],v[120:123]
mm v[120:123],v[212:215],v[172:175],v[120:123]
rt v[208:209],v242 offset:33792
rt v[210:211],v242 offset:34816
mm v[124:127],v[220:223],v[172:175],v[124:127]
mm v[124:127],v[216:219],v[188:191],v[124:127]
st a[28:31],v255,s[48:51],s18 offen offset:2048 sc1
mm v[92:95],v[216:219],v[184:187],v[92:95]
mm v[92:95],v[220:223],v[168:171],v[92:95]
rt v[212:213],v242 offset:42240
rt v[214:215],v242 offset:43264
mm v[60:63],v[220:223],v[164:167],v[60:63]
ds_write_b128 v238, a[24:27] offset:33792
mm v[60:63],v[216:219],v[180:183],v[60:63]
st a[32:35],v255,s[48:51],s18 offen offset:3072 sc1
mm v[28:31],v[216:219],v[176:179],v[28:31]
wt vmcnt(12) lgkmcnt(5)
mm v[28:31],v[220:223],v[160:163],v[28:31]
rt v[216:217],v243 offset:33792
rt v[218:219],v243 offset:34816
mm v[0:3],v[192:195],v[128:131],v[0:3]
ar v224,a56
ar v225,a57
mm v[0:3],v[196:199],a[200:203],v[0:3]
ar v226,a58
ar v227,a59
mm v[32:35],v[196:199],a[204:207],v[32:35]
ar v228,a60
ar v229,a61
mm v[32:35],v[192:195],v[140:143],v[32:35]
rt v[220:221],v243 offset:42240
rt v[222:223],v243 offset:43264
mm v[64:67],v[192:195],a[192:195],v[64:67]
ar v230,a62
ar v231,a63
mm v[64:67],v[196:199],v[148:151],v[64:67]
cv v248,v224,v225
cv v249,v226,v227
mm v[96:99],v[196:199],v[132:135],v[96:99]
cv v250,v228,v229
cv v251,v230,v231
mm v[96:99],v[192:195],a[196:199],v[96:99]
rt v[196:197],v240 offset:33920
rt v[198:199],v240 offset:34944
mm v[100:103],v[200:203],a[196:199],v[100:103]
aw a48,v248
aw a49,v249
mm v[100:103],v[204:207],v[132:135],v[100:103]
aw a50,v250
aw a51,v251
mm v[68:71],v[204:207],v[148:151],v[68:71]
v_add_u32 v244, s92, v244
v_add_u32 v246, s93, v246
mm v[68:71],v[200:203],a[192:195],v[68:71]
rt v[192:193],v240 offset:42368
rt v[194:195],v240 offset:43392
mm v[36:39],v[200:203],v[140:143],v[36:39]
ds_write_b128 v238, a[28:31] offset:34816
mm v[36:39],v[204:207],a[204:207],v[36:39]
v_add_u32 v245, 0x1000, v244
v_add_u32 v247, 0x1000, v246
mm v[4:7],v[204:207],a[200:203],v[4:7]
wt lgkmcnt(5)
s_lshl_b32 s91, s72, 13
mm v[4:7],v[200:203],v[128:131],v[4:7]
rt v[204:205],v241 offset:33920
rt v[206:207],v241 offset:34944
mm v[8:11],v[208:211],v[128:131],v[8:11]
s_lshl4_add_u32 s92, s76, s3
s_lshl3_add_u32 s92, s92, s20
mm v[8:11],v[212:215],a[200:203],v[8:11]
s_bitset1_b32 s20, 30
mm v[40:43],v[212:215],a[204:207],v[40:43]
mm v[40:43],v[208:211],v[140:143],v[40:43]
rt v[200:201],v241 offset:42368
rt v[202:203],v241 offset:43392
mm v[72:75],v[208:211],a[192:195],v[72:75]
mm v[72:75],v[212:215],v[148:151],v[72:75]
mm v[104:107],v[212:215],v[132:135],v[104:107]
s_mov_b32 s18, 4096
mm v[104:107],v[208:211],a[196:199],v[104:107]
rt v[212:213],v242 offset:33920
rt v[214:215],v242 offset:34944
mm v[108:111],v[216:219],a[196:199],v[108:111]
mm v[108:111],v[220:223],v[132:135],v[108:111]
st a[36:39],v255,s[48:51],s18 offen offset:0 sc1
mm v[76:79],v[220:223],v[148:151],v[76:79]
mm v[76:79],v[216:219],a[192:195],v[76:79]
rt v[208:209],v242 offset:42368
rt v[210:211],v242 offset:43392
mm v[44:47],v[216:219],v[140:143],v[44:47]
ds_write_b128 v238, a[32:35] offset:35840
mm v[44:47],v[220:223],a[204:207],v[44:47]
st a[40:43],v255,s[48:51],s18 offen offset:1024 sc1
mm v[12:15],v[220:223],a[200:203],v[12:15]
wt lgkmcnt(5)
mm v[12:15],v[216:219],v[128:131],v[12:15]
rt v[220:221],v243 offset:33920
rt v[222:223],v243 offset:34944
mm v[16:19],v[196:199],v[128:131],v[16:19]
s_mov_b64 s[52:53], s[86:87]
mm v[16:19],v[192:195],a[200:203],v[16:19]
s_bitcmp1_b32 21, s73
s_cselect_b32 s54, 16384, 0
s_cselect_b32 s86, s88, 1073741824
mm v[48:51],v[192:195],a[204:207],v[48:51]
s_lshl_b32 s89, s3, 14
v_add_u32 v248, s89, v255
mm v[48:51],v[196:199],v[140:143],v[48:51]
rt v[216:217],v243 offset:42368
rt v[218:219],v243 offset:43392
mm v[80:83],v[196:199],a[192:195],v[80:83]
ds_write_b128 v238, a[36:39] offset:36864
mm v[80:83],v[192:195],v[148:151],v[80:83]
s_add_u32 s86, s86, 262144
mm v[112:115],v[192:195],v[132:135],v[112:115]
s_cmp_eq_u32 s73, 0
s_cselect_b32 s86, s92, s86
s_cselect_b32 s92, 1073741824, s92
mm v[112:115],v[196:199],a[196:199],v[112:115]
s_mov_b32 s87, s92
s_add_u32 s89, s89, 16384
mm v[116:119],v[204:207],a[196:199],v[116:119]
wt lgkmcnt(1)
s_barrier
mm v[116:119],v[200:203],v[132:135],v[116:119]
s_lshl_b32 m0, s3, 12
s_add_u32 m0, m0, 131072
mm v[84:87],v[200:203],v[148:151],v[84:87]
rt v[192:193],v240 offset:50688
rt v[194:195],v240 offset:51712
mm v[84:87],v[204:207],a[192:195],v[84:87]
rt v[196:197],v240 offset:59136
rt v[198:199],v240 offset:60160
mm v[52:55],v[204:207],v[140:143],v[52:55]
ds_write_b128 v238, a[40:43] offset:37888
mm v[52:55],v[200:203],a[204:207],v[52:55]
v_bfe_u32 v251, v255, 2, 5
v_and_b32 v224, 0x380, v255
mm v[20:23],v[200:203],a[200:203],v[20:23]
v_add_u32 v251, v251, v224
v_and_b32 v224, 16, v255
mm v[20:23],v[204:207],v[128:131],v[20:23]
rt v[200:201],v241 offset:50688
rt v[202:203],v241 offset:51712
mm v[24:27],v[212:215],v[128:131],v[24:27]
v_cmp_eq_u32 vcc, 0, v224
mm v[24:27],v[208:211],a[200:203],v[24:27]
v_cndmask_b32 v249, v253, v252, vcc
mm v[56:59],v[208:211],a[204:207],v[56:59]
mm v[56:59],v[212:215],v[140:143],v[56:59]
rt v[204:205],v241 offset:59136
rt v[206:207],v241 offset:60160
mm v[88:91],v[212:215],a[192:195],v[88:91]
ds_write_b128 v238, a[44:47] offset:38912
mm v[88:91],v[208:211],v[148:151],v[88:91]
st a[44:47],v255,s[48:51],s18 offen offset:2048 sc1
mm v[120:123],v[208:211],v[132:135],v[120:123]
mm v[120:123],v[212:215],a[196:199],v[120:123]
rt v[208:209],v242 offset:50688
rt v[210:211],v242 offset:51712
.La5_LW1_14:
s_cbranch_execz .La5_LW1_R0
mm v[124:127],v[220:223],a[196:199],v[124:127]
mm v[124:127],v[216:219],v[132:135],v[124:127]
mm v[92:95],v[216:219],v[148:151],v[92:95]
mm v[92:95],v[220:223],a[192:195],v[92:95]
rt v[212:213],v242 offset:59136
rt v[214:215],v242 offset:60160
mm v[60:63],v[220:223],v[140:143],v[60:63]
mm v[60:63],v[216:219],a[204:207],v[60:63]
st a[48:51],v255,s[48:51],s18 offen offset:3072 sc1
mm v[28:31],v[216:219],a[200:203],v[28:31]
wt vmcnt(6) lgkmcnt(5)
mm v[28:31],v[220:223],v[128:131],v[28:31]
rt v[216:217],v243 offset:50688
rt v[218:219],v243 offset:51712
mm v[0:3],v[192:195],a[96:99],v[0:3]
mm v[0:3],v[196:199],a[112:115],v[0:3]
mm v[32:35],v[196:199],a[116:119],v[32:35]
mm v[32:35],v[192:195],a[100:103],v[32:35]
rt v[220:221],v243 offset:59136
rt v[222:223],v243 offset:60160
mm v[64:67],v[192:195],a[104:107],v[64:67]
mm v[64:67],v[196:199],a[120:123],v[64:67]
mm v[96:99],v[196:199],a[124:127],v[96:99]
mm v[96:99],v[192:195],a[108:111],v[96:99]
rt v[196:197],v240 offset:50816
rt v[198:199],v240 offset:51840
mm v[100:103],v[200:203],a[108:111],v[100:103]
mm v[100:103],v[204:207],a[124:127],v[100:103]
mm v[68:71],v[204:207],a[120:123],v[68:71]
mm v[68:71],v[200:203],a[104:107],v[68:71]
rt v[192:193],v240 offset:59264
rt v[194:195],v240 offset:60288
mm v[36:39],v[200:203],a[100:103],v[36:39]
mm v[36:39],v[204:207],a[116:119],v[36:39]
mm v[4:7],v[204:207],a[112:115],v[4:7]
wt lgkmcnt(4)
mm v[4:7],v[200:203],a[96:99],v[4:7]
rt v[204:205],v241 offset:50816
rt v[206:207],v241 offset:51840
mm v[8:11],v[208:211],a[96:99],v[8:11]
mm v[8:11],v[212:215],a[112:115],v[8:11]
mm v[40:43],v[212:215],a[116:119],v[40:43]
cv v128,v0,v1
cv v129,v2,v3
mm v[40:43],v[208:211],a[100:103],v[40:43]
rt v[200:201],v241 offset:59264
rt v[202:203],v241 offset:60288
mm v[72:75],v[208:211],a[104:107],v[72:75]
cv v130,v4,v5
cv v131,v6,v7
mm v[72:75],v[212:215],a[120:123],v[72:75]
cv v144,v32,v33
cv v145,v34,v35
mm v[104:107],v[212:215],a[124:127],v[104:107]
cv v146,v36,v37
cv v147,v38,v39
mm v[104:107],v[208:211],a[108:111],v[104:107]
rt v[212:213],v242 offset:50816
rt v[214:215],v242 offset:51840
mm v[108:111],v[216:219],a[108:111],v[108:111]
cv v160,v64,v65
cv v161,v66,v67
mm v[108:111],v[220:223],a[124:127],v[108:111]
cv v162,v68,v69
cv v163,v70,v71
mm v[76:79],v[220:223],a[120:123],v[76:79]
cv v176,v96,v97
cv v177,v98,v99
mm v[76:79],v[216:219],a[104:107],v[76:79]
rt v[208:209],v242 offset:59264
rt v[210:211],v242 offset:60288
mm v[44:47],v[216:219],a[100:103],v[44:47]
cv v178,v100,v101
cv v179,v102,v103
mm v[44:47],v[220:223],a[116:119],v[44:47]
mm v[12:15],v[220:223],a[112:115],v[12:15]
wt lgkmcnt(4)
mm v[12:15],v[216:219],a[96:99],v[12:15]
rt v[220:221],v243 offset:50816
rt v[222:223],v243 offset:51840
mm v[16:19],v[196:199],a[96:99],v[16:19]
v_mul_u32_u24 v241, 0x5555, v224
v_lshrrev_b32 v241, 4, v241
v_add_u32 v241, 0x8888, v241
mm v[16:19],v[192:195],a[112:115],v[16:19]
st v[128:131],v255,s[52:55],0 offen offset:0 sc1
mm v[48:51],v[192:195],a[116:119],v[48:51]
cv v132,v8,v9
cv v133,v10,v11
cv v181,v106,v107
mm v[48:51],v[196:199],a[100:103],v[48:51]
rt v[216:217],v243 offset:59264
rt v[218:219],v243 offset:60288
cv v182,v108,v109
mm v[80:83],v[196:199],a[104:107],v[80:83]
cv v134,v12,v13
cv v135,v14,v15
cv v183,v110,v111
mm v[80:83],v[192:195],a[120:123],v[80:83]
st v[144:147],v255,s[52:55],s95 offen offset:0 sc1
ar v34,a238
mm v[112:115],v[192:195],a[124:127],v[112:115]
cv v148,v40,v41
cv v149,v42,v43
ar v35,a239
mm v[112:115],v[196:199],a[108:111],v[112:115]
cv v150,v44,v45
cv v151,v46,v47
v_mov_b32 v36, 0xff800000
mm v[116:119],v[204:207],a[108:111],v[116:119]
wt lgkmcnt(0)
s_barrier
v_mov_b32 v37, 0xff800000
mm v[116:119],v[200:203],a[124:127],v[116:119]
st v[160:163],v255,s[52:55],s96 offen offset:0 sc1
v_mov_b32 v38, 0xff800000
mm v[84:87],v[200:203],a[120:123],v[84:87]
cv v164,v72,v73
cv v165,v74,v75
v_mov_b32 v39, 0xff800000
mm v[84:87],v[204:207],a[104:107],v[84:87]
rd a[64:67],v248 offset:0
rd a[68:71],v248 offset:1024
mm v[52:55],v[204:207],a[100:103],v[52:55]
cv v166,v76,v77
cv v167,v78,v79
mm v[52:55],v[200:203],a[116:119],v[52:55]
st v[176:179],v255,s[52:55],s97 offen offset:0 sc1
mm v[20:23],v[200:203],a[112:115],v[20:23]
s_nop 0
cv v180,v104,v105
mm v[20:23],v[204:207],a[96:99],v[20:23]
rd a[72:75],v248 offset:2048
rd a[76:79],v248 offset:3072
mm v[24:27],v[212:215],a[96:99],v[24:27]
ar v32,a236
ar v33,a237
mm v[24:27],v[208:211],a[112:115],v[24:27]
st v[132:135],v255,s[52:55],0 offen offset:1024 sc1
mm v[56:59],v[208:211],a[116:119],v[56:59]
cv v136,v16,v17
cv v137,v18,v19
mm v[56:59],v[212:215],a[100:103],v[56:59]
rd a[80:83],v248 offset:4096
rd a[84:87],v248 offset:5120
mm v[88:91],v[212:215],a[104:107],v[88:91]
cv v138,v20,v21
cv v139,v22,v23
mm v[88:91],v[208:211],a[120:123],v[88:91]
st v[148:151],v255,s[52:55],s95 offen offset:1024 sc1
mm v[120:123],v[208:211],a[124:127],v[120:123]
cv v152,v48,v49
cv v153,v50,v51
mm v[120:123],v[212:215],a[108:111],v[120:123]
rd a[88:91],v248 offset:6144
rd a[92:95],v248 offset:7168
mm v[124:127],v[220:223],a[108:111],v[124:127]
ds_write_b128 v238, a[48:51] offset:39936
mm v[124:127],v[216:219],a[124:127],v[124:127]
st v[164:167],v255,s[52:55],s96 offen offset:1024 sc1
mm v[92:95],v[216:219],a[120:123],v[92:95]
cv v154,v52,v53
cv v155,v54,v55
mm v[92:95],v[220:223],a[104:107],v[92:95]
cv v168,v80,v81
cv v169,v82,v83
mm v[60:63],v[220:223],a[100:103],v[60:63]
cv v170,v84,v85
cv v171,v86,v87
mm v[60:63],v[216:219],a[116:119],v[60:63]
st v[180:183],v255,s[52:55],s97 offen offset:1024 sc1
mm v[28:31],v[216:219],a[112:115],v[28:31]
cv v184,v112,v113
cv v185,v114,v115
mm v[28:31],v[220:223],a[96:99],v[28:31]
cv v186,v116,v117
cv v187,v118,v119
wt lgkmcnt(1)
mm v[208:211],a[64:67],v[160:163],v[32:35]
mm v[216:219],a[64:67],v[176:179],0
st v[136:139],v255,s[52:55],0 offen offset:2048 sc1
mm v[212:215],a[80:83],v[160:163],v[36:39]
cv v172,v88,v89
cv v173,v90,v91
cv v174,v92,v93
mm v[220:223],a[80:83],v[176:179],v[32:35]
cv v175,v94,v95
cv v188,v120,v121
cv v189,v122,v123
mm v[208:211],a[68:71],v[164:167],v[208:211]
cv v190,v124,v125
cv v191,v126,v127
cv v140,v24,v25
mm v[216:219],a[68:71],v[180:183],v[216:219]
st v[152:155],v255,s[52:55],s95 offen offset:2048 sc1
cv v141,v26,v27
mm v[212:215],a[84:87],v[164:167],v[212:215]
cv v142,v28,v29
cv v143,v30,v31
cv v156,v56,v57
mm v[220:223],a[84:87],v[180:183],v[220:223]
cv v157,v58,v59
cv v158,v60,v61
cv v159,v62,v63
mm v[208:211],a[72:75],v[168:171],v[208:211]
mm v[216:219],a[72:75],v[184:187],v[216:219]
st v[168:171],v255,s[52:55],s96 offen offset:2048 sc1
mm v[212:215],a[88:91],v[168:171],v[212:215]
mm v[220:223],a[88:91],v[184:187],v[220:223]
mm v[208:211],a[76:79],v[172:175],v[208:211]
mm v[216:219],a[76:79],v[188:191],v[216:219]
st v[184:187],v255,s[52:55],s97 offen offset:2048 sc1
mm v[212:215],a[92:95],v[172:175],v[212:215]
rd a[64:67],v248 offset:16384
mm v[220:223],a[92:95],v[188:191],v[220:223]
rd a[96:99],v248 offset:8192
ex v0,v208
wt vmcnt(12)
buffer_store_dwordx2 v[252:253], off, s[64:67], s87 sc1
mm v[64:67],a[232:235],a[232:235],0
rd a[68:71],v248 offset:17408
ex v1,v209
mm v[68:71],a[232:235],a[232:235],0
rd a[100:103],v248 offset:9216
st v[140:143],v255,s[52:55],0 offen offset:3072 sc1
mm v[72:75],a[232:235],a[232:235],0
rd a[72:75],v248 offset:18432
ex v2,v210
mm v[76:79],a[232:235],a[232:235],0
rd a[104:107],v248 offset:10240
ex v3,v211
mm v[80:83],a[232:235],a[232:235],0
rd a[76:79],v248 offset:19456
ex v8,v216
mm v[84:87],a[232:235],a[232:235],0
rd a[108:111],v248 offset:11264
st v[156:159],v255,s[52:55],s95 offen offset:3072 sc1
mm v[88:91],a[232:235],a[232:235],0
rd a[80:83],v248 offset:20480
ex v9,v217
mm v[92:95],a[232:235],a[232:235],0
rd a[112:115],v248 offset:12288
ex v10,v218
mm v[96:99],a[232:235],a[232:235],0
rd a[84:87],v248 offset:21504
ex v11,v219
mm v[100:103],a[232:235],a[232:235],0
rd a[116:119],v248 offset:13312
st v[172:175],v255,s[52:55],s96 offen offset:3072 sc1
mm v[104:107],a[232:235],a[232:235],0
rd a[88:91],v248 offset:22528
ex v4,v212
mm v[108:111],a[232:235],a[232:235],0
rd a[120:123],v248 offset:14336
ex v5,v213
mm v[112:115],a[232:235],a[232:235],0
rd a[92:95],v248 offset:23552
ex v6,v214
mm v[116:119],a[232:235],a[232:235],0
rd a[124:127],v248 offset:15360
st v[188:191],v255,s[52:55],s97 offen offset:3072 sc1
ex v7,v215
mm v[120:123],a[232:235],a[232:235],0
ex v12,v220
mm v[124:127],a[232:235],a[232:235],0
aw a192,0
aw a193,0
aw a194,0
aw a195,0
aw a196,0
aw a197,0
aw a198,0
aw a199,0
v_add_u32 v248, 0x4000, v248
s_cmp_eq_u32 s3, 3
s_cbranch_scc1 .La5_LA_FH
.p2align 6
; ---- attention, 32 keys per window: S = K Q^T (causal mask through SrcC), P = exp2(S), O += V^T P, row sums l += 1 P; here the later 32 rows only
.La5_LA_HH:
wt lgkmcnt(1)
mm v[208:211],a[64:67],v[160:163],0
ex v13,v221
mm v[208:211],a[68:71],v[164:167],v[208:211]
cv v232,v0,v1
cv v233,v2,v3
mm v[208:211],a[72:75],v[168:171],v[208:211]
ex v14,v222
mm v[208:211],a[76:79],v[172:175],v[208:211]
cv v236,v8,v9
cv v237,v10,v11
mm v[216:219],a[76:79],v[188:191],0
ex v15,v223
mm v[216:219],a[72:75],v[184:187],v[216:219]
cv v234,v4,v5
cv v235,v6,v7
mm v[216:219],a[68:71],v[180:183],v[216:219]
ex v0,v208
mm v[216:219],a[64:67],v[176:179],v[216:219]
cv v238,v12,v13
cv v239,v14,v15
mm v[212:215],a[80:83],v[160:163],0
ex v1,v209
mm v[212:215],a[84:87],v[164:167],v[212:215]
rd a[64:67],v248 offset:16384
rd a[68:71],v248 offset:17408
mm v[212:215],a[88:91],v[168:171],v[212:215]
ex v2,v210
mm v[212:215],a[92:95],v[172:175],v[212:215]
rd a[72:75],v248 offset:18432
rd a[76:79],v248 offset:19456
mm v[220:223],a[92:95],v[188:191],0
ex v3,v211
mm v[220:223],a[88:91],v[184:187],v[220:223]
rd a[92:95],v248 offset:23552
mm v[220:223],a[84:87],v[180:183],v[220:223]
ex v8,v216
mm v[220:223],a[80:83],v[176:179],v[220:223]
wt lgkmcnt(5)
rd a[88:91],v248 offset:22528
mm v[64:67],a[96:99],v[232:235],v[64:67]
ex v9,v217
mm v[96:99],a[96:99],v[236:239],v[96:99]
rd a[96:99],v248 offset:8192
rd a[84:87],v248 offset:21504
mm v[100:103],a[100:103],v[236:239],v[100:103]
ex v10,v218
mm v[68:71],a[100:103],v[232:235],v[68:71]
rd a[100:103],v248 offset:9216
rd a[80:83],v248 offset:20480
mm v[72:75],a[104:107],v[232:235],v[72:75]
ex v11,v219
mm v[104:107],a[104:107],v[236:239],v[104:107]
rd a[104:107],v248 offset:10240
mm v[108:111],a[108:111],v[236:239],v[108:111]
ex v4,v212
mm v[76:79],a[108:111],v[232:235],v[76:79]
rd a[108:111],v248 offset:11264
s_add_u32 s85, s85, 1
mm v[80:83],a[112:115],v[232:235],v[80:83]
ex v5,v213
mm v[112:115],a[112:115],v[236:239],v[112:115]
rd a[112:115],v248 offset:12288
s_add_u32 s89, s89, 32768
mm v[116:119],a[116:119],v[236:239],v[116:119]
ex v6,v214
mm v[84:87],a[116:119],v[232:235],v[84:87]
rd a[116:119],v248 offset:13312
s_add_u32 s92, s85, s3
mm v[88:91],a[120:123],v[232:235],v[88:91]
ex v7,v215
mm v[120:123],a[120:123],v[236:239],v[120:123]
rd a[120:123],v248 offset:14336
buffer_load_dword v250, v251, s[64:67], s20 offen sc1
mm v[124:127],a[124:127],v[236:239],v[124:127]
ex v12,v220
mm v[92:95],a[124:127],v[232:235],v[92:95]
rd a[124:127],v248 offset:15360
s_barrier
v_smfmac_f32_16x16x64_bf16 a[196:199], a[208:211], v[232:239], v241
wt lgkmcnt(6)
mm v[208:211],a[64:67],v[160:163],0
ex v13,v221
mm v[208:211],a[68:71],v[164:167],v[208:211]
cv v232,v0,v1
cv v233,v2,v3
mm v[208:211],a[72:75],v[168:171],v[208:211]
ex v14,v222
mm v[208:211],a[76:79],v[172:175],v[208:211]
cv v236,v8,v9
cv v237,v10,v11
mm v[216:219],a[76:79],v[188:191],0
ex v15,v223
mm v[216:219],a[72:75],v[184:187],v[216:219]
cv v234,v4,v5
cv v235,v6,v7
mm v[216:219],a[68:71],v[180:183],v[216:219]
ex v0,v208
mm v[216:219],a[64:67],v[176:179],v[216:219]
cv v238,v12,v13
cv v239,v14,v15
mm v[212:215],a[80:83],v[160:163],0
ex v1,v209
mm v[212:215],a[84:87],v[164:167],v[212:215]
rd a[64:67],v248 offset:32768
rd a[68:71],v248 offset:33792
mm v[212:215],a[88:91],v[168:171],v[212:215]
ex v2,v210
mm v[212:215],a[92:95],v[172:175],v[212:215]
rd a[72:75],v248 offset:34816
rd a[76:79],v248 offset:35840
mm v[220:223],a[92:95],v[188:191],0
ex v3,v211
mm v[220:223],a[88:91],v[184:187],v[220:223]
rd a[92:95],v248 offset:39936
s_bitcmp1_b64 s[98:99], s85
mm v[220:223],a[84:87],v[180:183],v[220:223]
ex v8,v216
mm v[220:223],a[80:83],v[176:179],v[220:223]
wt lgkmcnt(5)
rd a[88:91],v248 offset:38912
mm v[64:67],a[96:99],v[232:235],v[64:67]
ex v9,v217
mm v[96:99],a[96:99],v[236:239],v[96:99]
rd a[96:99],v248 offset:24576
rd a[84:87],v248 offset:37888
mm v[100:103],a[100:103],v[236:239],v[100:103]
ex v10,v218
mm v[68:71],a[100:103],v[232:235],v[68:71]
rd a[100:103],v248 offset:25600
rd a[80:83],v248 offset:36864
mm v[72:75],a[104:107],v[232:235],v[72:75]
ex v11,v219
mm v[104:107],a[104:107],v[236:239],v[104:107]
rd a[104:107],v248 offset:26624
mm v[108:111],a[108:111],v[236:239],v[108:111]
ex v4,v212
mm v[76:79],a[108:111],v[232:235],v[76:79]
rd a[108:111],v248 offset:27648
mm v[80:83],a[112:115],v[232:235],v[80:83]
ex v5,v213
mm v[112:115],a[112:115],v[236:239],v[112:115]
rd a[112:115],v248 offset:28672
mm v[116:119],a[116:119],v[236:239],v[116:119]
ex v6,v214
mm v[84:87],a[116:119],v[232:235],v[84:87]
rd a[116:119],v248 offset:29696
wt vmcnt(1)
buffer_store_dwordx2 v[252:253], off, s[64:67], s86 sc1
s_mov_b32 s86, 1073741824
s_bitset0_b32 s20, 30
mm v[88:91],a[120:123],v[232:235],v[88:91]
ex v7,v215
mm v[120:123],a[120:123],v[236:239],v[120:123]
rd a[120:123],v248 offset:30720
mm v[124:127],a[124:127],v[236:239],v[124:127]
v_exp_f32_e64 v12, v220
mm v[92:95],a[124:127],v[232:235],v[92:95]
rd a[124:127],v248 offset:31744
v_smfmac_f32_16x16x64_bf16 a[196:199], a[208:211], v[232:239], v241
v_add_u32 v248, 0x8000, v248
s_cbranch_scc0 .La5_LA_HH1
s_call_b64 s[24:25], .La5_LA_EVENT
s_add_u32 s92, s85, s3
.La5_LA_HH1:
s_cmp_lt_u32 s92, 3
s_cbranch_scc1 .La5_LA_HH
; ---- first window with both row groups (diagonal of the earlier group)
.La5_LA_FH:
.La5_LW1_15:
s_cbranch_execz .La5_LW1_R0
wt lgkmcnt(9)
mm v[192:195],a[64:67],v[128:131],v[32:35]
ex v13,v221
mm v[192:195],a[68:71],v[132:135],v[192:195]
cv v232,v0,v1
cv v233,v2,v3
mm v[192:195],a[72:75],v[136:139],v[192:195]
ex v14,v222
mm v[192:195],a[76:79],v[140:143],v[192:195]
ld v255,s[60:63],s17 offen offset:0 sc1 lds
mm v[200:203],a[76:79],v[156:159],0
ex v15,v223
mm v[200:203],a[72:75],v[152:155],v[200:203]
cv v236,v8,v9
cv v237,v10,v11
mm v[200:203],a[68:71],v[148:151],v[200:203]
ex v192,v192
mm v[200:203],a[64:67],v[144:147],v[200:203]
wt lgkmcnt(1)
cv v234,v4,v5
mm v[196:199],a[80:83],v[128:131],v[36:39]
ex v193,v193
mm v[196:199],a[84:87],v[132:135],v[196:199]
cv v224,v192,v193
s_add_u32 s85, s85, 1
mm v[196:199],a[88:91],v[136:139],v[196:199]
ex v194,v194
mm v[196:199],a[92:95],v[140:143],v[196:199]
ld v255,s[60:63],s17 offen offset:1024 sc1 lds
mm v[204:207],a[92:95],v[156:159],v[32:35]
ex v195,v195
mm v[204:207],a[88:91],v[152:155],v[204:207]
cv v225,v194,v195
s_add_u32 s89, s89, 16384
mm v[204:207],a[84:87],v[148:151],v[204:207]
ex v200,v200
mm v[204:207],a[80:83],v[144:147],v[204:207]
ld v255,s[60:63],s17 offen offset:2048 sc1 lds
mm v[208:211],a[64:67],v[160:163],0
ex v201,v201
mm v[208:211],a[68:71],v[164:167],v[208:211]
cv v228,v200,v201
s_and_b32 s89, s89, 0x1ffff
mm v[208:211],a[72:75],v[168:171],v[208:211]
ex v202,v202
mm v[208:211],a[76:79],v[172:175],v[208:211]
ld v255,s[60:63],s17 offen offset:3072 sc1 lds
mm v[216:219],a[76:79],v[188:191],0
ex v203,v203
mm v[216:219],a[72:75],v[184:187],v[216:219]
cv v229,v202,v203
mm v[216:219],a[68:71],v[180:183],v[216:219]
ex v196,v196
mm v[216:219],a[64:67],v[176:179],v[216:219]
cv v235,v6,v7
cv v238,v12,v13
mm v[212:215],a[80:83],v[160:163],0
ex v197,v197
s_add_u32 s17, s17, s80
mm v[212:215],a[84:87],v[164:167],v[212:215]
cv v226,v196,v197
s_add_u32 m0, m0, 16384
s_and_b32 m0, m0, s78
mm v[212:215],a[88:91],v[168:171],v[212:215]
ex v198,v198
mm v[212:215],a[92:95],v[172:175],v[212:215]
cv v239,v14,v15
wt lgkmcnt(0)
mm v[220:223],a[92:95],v[188:191],0
ex v199,v199
mm v[220:223],a[88:91],v[184:187],v[220:223]
cv v227,v198,v199
mm v[220:223],a[84:87],v[180:183],v[220:223]
ex v204,v204
mm v[220:223],a[80:83],v[176:179],v[220:223]
wt vmcnt(10)
s_barrier
mm v[64:67],a[96:99],v[232:235],v[64:67]
ex v205,v205
mm v[96:99],a[96:99],v[236:239],v[96:99]
rd a[96:99],v248 offset:8192
cv v230,v204,v205
mm v[100:103],a[100:103],v[236:239],v[100:103]
ex v206,v206
mm v[68:71],a[100:103],v[232:235],v[68:71]
rd a[100:103],v248 offset:9216
buffer_load_dword v250, v251, s[64:67], s20 offen sc1
mm v[72:75],a[104:107],v[232:235],v[72:75]
ex v207,v207
mm v[104:107],a[104:107],v[236:239],v[104:107]
rd a[104:107],v248 offset:10240
cv v231,v206,v207
mm v[108:111],a[108:111],v[236:239],v[108:111]
ex v208,v208
mm v[76:79],a[108:111],v[232:235],v[76:79]
rd a[108:111],v248 offset:11264
mm v[80:83],a[112:115],v[232:235],v[80:83]
ex v209,v209
mm v[112:115],a[112:115],v[236:239],v[112:115]
rd a[112:115],v248 offset:12288
mm v[116:119],a[116:119],v[236:239],v[116:119]
ex v210,v210
mm v[84:87],a[116:119],v[232:235],v[84:87]
rd a[116:119],v248 offset:13312
mm v[88:91],a[120:123],v[232:235],v[88:91]
ex v211,v211
mm v[120:123],a[120:123],v[236:239],v[120:123]
rd a[120:123],v248 offset:14336
s_bitcmp1_b64 s[98:99], s85
mm v[124:127],a[124:127],v[236:239],v[124:127]
ex v216,v216
mm v[92:95],a[124:127],v[232:235],v[92:95]
rd a[124:127],v248 offset:15360
v_or_b32 v248, s89, v255
.La5_LW2_0:
s_cbranch_execz .La5_LW2_R0
v_smfmac_f32_16x16x64_bf16 a[196:199], a[208:211], v[232:239], v241
wt lgkmcnt(4)
v_smfmac_f32_16x16x64_bf16 a[192:195], a[208:211], v[224:231], v241
rd a[64:67],v248 offset:0
mm v[0:3],a[96:99],v[224:227],0
ex v217,v217
mm v[32:35],a[96:99],v[228:231],0
rd a[68:71],v248 offset:1024
cv v232,v208,v209
mm v[36:39],a[100:103],v[228:231],0
ex v218,v218
mm v[4:7],a[100:103],v[224:227],0
rd a[72:75],v248 offset:2048
cv v233,v210,v211
mm v[8:11],a[104:107],v[224:227],0
ex v219,v219
mm v[40:43],a[104:107],v[228:231],0
rd a[76:79],v248 offset:3072
cv v236,v216,v217
mm v[44:47],a[108:111],v[228:231],0
ex v212,v212
mm v[12:15],a[108:111],v[224:227],0
rd a[80:83],v248 offset:4096
wt lgkmcnt(5)
mm v[16:19],a[112:115],v[224:227],0
ex v213,v213
mm v[48:51],a[112:115],v[228:231],0
rd a[84:87],v248 offset:5120
cv v237,v218,v219
mm v[52:55],a[116:119],v[228:231],0
ex v214,v214
mm v[20:23],a[116:119],v[224:227],0
rd a[88:91],v248 offset:6144
cv v234,v212,v213
mm v[24:27],a[120:123],v[224:227],0
ex v215,v215
mm v[56:59],a[120:123],v[228:231],0
rd a[92:95],v248 offset:7168
cv v235,v214,v215
mm v[60:63],a[124:127],v[228:231],0
ex v220,v220
mm v[28:31],a[124:127],v[224:227],0
wt vmcnt(5)
buffer_store_dwordx2 v[252:253], off, s[64:67], s86 sc1
s_mov_b32 s86, 1073741824
s_bitset0_b32 s20, 30
s_cbranch_scc1 .La5_LA_FE0
.La5_LA_FS0:
wt lgkmcnt(4)
mm v[192:195],a[64:67],v[128:131],0
ex v221,v221
mm v[192:195],a[68:71],v[132:135],v[192:195]
cv v238,v220,v221
s_add_u32 s85, s85, 1
mm v[192:195],a[72:75],v[136:139],v[192:195]
ex v222,v222
mm v[192:195],a[76:79],v[140:143],v[192:195]
ld v255,s[60:63],s17 offen offset:0 sc1 lds
mm v[200:203],a[76:79],v[156:159],0
ex v223,v223
mm v[200:203],a[72:75],v[152:155],v[200:203]
cv v239,v222,v223
s_add_u32 s89, s89, 16384
mm v[200:203],a[68:71],v[148:151],v[200:203]
ex v192,v192
mm v[200:203],a[64:67],v[144:147],v[200:203]
wt lgkmcnt(0)
s_and_b32 s89, s89, 0x1ffff
mm v[196:199],a[80:83],v[128:131],0
ex v193,v193
mm v[196:199],a[84:87],v[132:135],v[196:199]
cv v224,v192,v193
mm v[196:199],a[88:91],v[136:139],v[196:199]
ex v194,v194
mm v[196:199],a[92:95],v[140:143],v[196:199]
ld v255,s[60:63],s17 offen offset:1024 sc1 lds
mm v[204:207],a[92:95],v[156:159],0
ex v195,v195
mm v[204:207],a[88:91],v[152:155],v[204:207]
cv v225,v194,v195
mm v[204:207],a[84:87],v[148:151],v[204:207]
ex v200,v200
mm v[204:207],a[80:83],v[144:147],v[204:207]
ld v255,s[60:63],s17 offen offset:2048 sc1 lds
mm v[208:211],a[64:67],v[160:163],0
ex v201,v201
mm v[208:211],a[68:71],v[164:167],v[208:211]
cv v228,v200,v201
mm v[208:211],a[72:75],v[168:171],v[208:211]
ex v202,v202
mm v[208:211],a[76:79],v[172:175],v[208:211]
ld v255,s[60:63],s17 offen offset:3072 sc1 lds
mm v[216:219],a[76:79],v[188:191],0
ex v203,v203
mm v[216:219],a[72:75],v[184:187],v[216:219]
cv v229,v202,v203
mm v[216:219],a[68:71],v[180:183],v[216:219]
ex v196,v196
mm v[216:219],a[64:67],v[176:179],v[216:219]
buffer_load_dword a253, v254, s[36:39], s90 offen
mm v[212:215],a[80:83],v[160:163],0
ex v197,v197
mm v[212:215],a[84:87],v[164:167],v[212:215]
cv v226,v196,v197
s_add_u32 s90, s90, s21
mm v[212:215],a[88:91],v[168:171],v[212:215]
ex v198,v198
mm v[212:215],a[92:95],v[172:175],v[212:215]
s_add_u32 s17, s17, s80
s_add_u32 m0, m0, 16384
mm v[220:223],a[92:95],v[188:191],0
ex v199,v199
mm v[220:223],a[88:91],v[184:187],v[220:223]
cv v227,v198,v199
s_and_b32 m0, m0, s78
mm v[220:223],a[84:87],v[180:183],v[220:223]
ex v204,v204
mm v[220:223],a[80:83],v[176:179],v[220:223]
wt vmcnt(11)
s_barrier
mm v[64:67],a[96:99],v[232:235],v[64:67]
ex v205,v205
mm v[96:99],a[96:99],v[236:239],v[96:99]
rd a[96:99],v248 offset:8192
cv v230,v204,v205
mm v[100:103],a[100:103],v[236:239],v[100:103]
ex v206,v206
mm v[68:71],a[100:103],v[232:235],v[68:71]
rd a[100:103],v248 offset:9216
s_cmp_eq_u32 s85, s81
buffer_load_dword v250, v251, s[64:67], s20 offen sc1
mm v[72:75],a[104:107],v[232:235],v[72:75]
ex v207,v207
mm v[104:107],a[104:107],v[236:239],v[104:107]
rd a[104:107],v248 offset:10240
cv v231,v206,v207
mm v[108:111],a[108:111],v[236:239],v[108:111]
ex v208,v208
mm v[76:79],a[108:111],v[232:235],v[76:79]
rd a[108:111],v248 offset:11264
s_cselect_b32 s17, s77, s17
mm v[80:83],a[112:115],v[232:235],v[80:83]
ex v209,v209
mm v[112:115],a[112:115],v[236:239],v[112:115]
rd a[112:115],v248 offset:12288
s_cselect_b32 s80, -8192, s80
mm v[116:119],a[116:119],v[236:239],v[116:119]
ex v210,v210
mm v[84:87],a[116:119],v[232:235],v[84:87]
rd a[116:119],v248 offset:13312
mm v[88:91],a[120:123],v[232:235],v[88:91]
ex v211,v211
mm v[120:123],a[120:123],v[236:239],v[120:123]
rd a[120:123],v248 offset:14336
s_bitcmp1_b64 s[98:99], s85
mm v[124:127],a[124:127],v[236:239],v[124:127]
ex v216,v216
mm v[92:95],a[124:127],v[232:235],v[92:95]
rd a[124:127],v248 offset:15360
v_or_b32 v248, s89, v255
v_smfmac_f32_16x16x64_bf16 a[196:199], a[208:211], v[232:239], v241
wt lgkmcnt(4)
v_smfmac_f32_16x16x64_bf16 a[192:195], a[208:211], v[224:231], v241
rd a[64:67],v248 offset:0
mm v[0:3],a[96:99],v[224:227],v[0:3]
ex v217,v217
mm v[32:35],a[96:99],v[228:231],v[32:35]
rd a[68:71],v248 offset:1024
cv v232,v208,v209
mm v[36:39],a[100:103],v[228:231],v[36:39]
ex v218,v218
mm v[4:7],a[100:103],v[224:227],v[4:7]
rd a[72:75],v248 offset:2048
cv v233,v210,v211
mm v[8:11],a[104:107],v[224:227],v[8:11]
ex v219,v219
mm v[40:43],a[104:107],v[228:231],v[40:43]
rd a[76:79],v248 offset:3072
cv v236,v216,v217
mm v[44:47],a[108:111],v[228:231],v[44:47]
ex v212,v212
mm v[12:15],a[108:111],v[224:227],v[12:15]
rd a[80:83],v248 offset:4096
wt lgkmcnt(5)
mm v[16:19],a[112:115],v[224:227],v[16:19]
ex v213,v213
mm v[48:51],a[112:115],v[228:231],v[48:51]
rd a[84:87],v248 offset:5120
cv v237,v218,v219
mm v[52:55],a[116:119],v[228:231],v[52:55]
ex v214,v214
mm v[20:23],a[116:119],v[224:227],v[20:23]
rd a[88:91],v248 offset:6144
cv v234,v212,v213
mm v[24:27],a[120:123],v[224:227],v[24:27]
ex v215,v215
mm v[56:59],a[120:123],v[228:231],v[56:59]
rd a[92:95],v248 offset:7168
cv v235,v214,v215
mm v[60:63],a[124:127],v[228:231],v[60:63]
v_exp_f32_e64 v220, v220
mm v[28:31],a[124:127],v[224:227],v[28:31]
s_cbranch_scc1 .La5_LA_F1E2
.p2align 6
; ---- steady windows
.La5_LA_F1:
wt lgkmcnt(4)
mm v[192:195],a[64:67],v[128:131],0
ex v221,v221
mm v[192:195],a[68:71],v[132:135],v[192:195]
cv v238,v220,v221
s_add_u32 s85, s85, 1
mm v[192:195],a[72:75],v[136:139],v[192:195]
ex v222,v222
mm v[192:195],a[76:79],v[140:143],v[192:195]
ld v255,s[60:63],s17 offen offset:0 sc1 lds
mm v[200:203],a[76:79],v[156:159],0
ex v223,v223
mm v[200:203],a[72:75],v[152:155],v[200:203]
cv v239,v222,v223
s_add_u32 s89, s89, 16384
mm v[200:203],a[68:71],v[148:151],v[200:203]
ex v192,v192
mm v[200:203],a[64:67],v[144:147],v[200:203]
wt lgkmcnt(0)
s_and_b32 s89, s89, 0x1ffff
mm v[196:199],a[80:83],v[128:131],0
ex v193,v193
mm v[196:199],a[84:87],v[132:135],v[196:199]
cv v224,v192,v193
mm v[196:199],a[88:91],v[136:139],v[196:199]
ex v194,v194
mm v[196:199],a[92:95],v[140:143],v[196:199]
ld v255,s[60:63],s17 offen offset:1024 sc1 lds
mm v[204:207],a[92:95],v[156:159],0
ex v195,v195
mm v[204:207],a[88:91],v[152:155],v[204:207]
cv v225,v194,v195
mm v[204:207],a[84:87],v[148:151],v[204:207]
ex v200,v200
mm v[204:207],a[80:83],v[144:147],v[204:207]
ld v255,s[60:63],s17 offen offset:2048 sc1 lds
mm v[208:211],a[64:67],v[160:163],0
ex v201,v201
mm v[208:211],a[68:71],v[164:167],v[208:211]
cv v228,v200,v201
mm v[208:211],a[72:75],v[168:171],v[208:211]
ex v202,v202
mm v[208:211],a[76:79],v[172:175],v[208:211]
ld v255,s[60:63],s17 offen offset:3072 sc1 lds
mm v[216:219],a[76:79],v[188:191],0
ex v203,v203
mm v[216:219],a[72:75],v[184:187],v[216:219]
cv v229,v202,v203
mm v[216:219],a[68:71],v[180:183],v[216:219]
ex v196,v196
mm v[216:219],a[64:67],v[176:179],v[216:219]
buffer_load_dword a253, v254, s[36:39], s90 offen
mm v[212:215],a[80:83],v[160:163],0
ex v197,v197
mm v[212:215],a[84:87],v[164:167],v[212:215]
cv v226,v196,v197
s_add_u32 s90, s90, s21
mm v[212:215],a[88:91],v[168:171],v[212:215]
ex v198,v198
mm v[212:215],a[92:95],v[172:175],v[212:215]
s_add_u32 s17, s17, s80
s_add_u32 m0, m0, 16384
mm v[220:223],a[92:95],v[188:191],0
ex v199,v199
mm v[220:223],a[88:91],v[184:187],v[220:223]
cv v227,v198,v199
s_and_b32 m0, m0, s78
mm v[220:223],a[84:87],v[180:183],v[220:223]
ex v204,v204
mm v[220:223],a[80:83],v[176:179],v[220:223]
wt vmcnt(11)
s_barrier
mm v[64:67],a[96:99],v[232:235],v[64:67]
ex v205,v205
mm v[96:99],a[96:99],v[236:239],v[96:99]
rd a[96:99],v248 offset:8192
cv v230,v204,v205
mm v[100:103],a[100:103],v[236:239],v[100:103]
ex v206,v206
mm v[68:71],a[100:103],v[232:235],v[68:71]
rd a[100:103],v248 offset:9216
s_cmp_eq_u32 s85, s81
mm v[72:75],a[104:107],v[232:235],v[72:75]
ex v207,v207
mm v[104:107],a[104:107],v[236:239],v[104:107]
rd a[104:107],v248 offset:10240
cv v231,v206,v207
mm v[108:111],a[108:111],v[236:239],v[108:111]
ex v208,v208
mm v[76:79],a[108:111],v[232:235],v[76:79]
rd a[108:111],v248 offset:11264
s_cselect_b32 s17, s77, s17
mm v[80:83],a[112:115],v[232:235],v[80:83]
ex v209,v209
mm v[112:115],a[112:115],v[236:239],v[112:115]
rd a[112:115],v248 offset:12288
s_cselect_b32 s80, -8192, s80
mm v[116:119],a[116:119],v[236:239],v[116:119]
ex v210,v210
mm v[84:87],a[116:119],v[232:235],v[84:87]
rd a[116:119],v248 offset:13312
mm v[88:91],a[120:123],v[232:235],v[88:91]
ex v211,v211
mm v[120:123],a[120:123],v[236:239],v[120:123]
rd a[120:123],v248 offset:14336
s_bitcmp1_b64 s[98:99], s85
mm v[124:127],a[124:127],v[236:239],v[124:127]
ex v216,v216
mm v[92:95],a[124:127],v[232:235],v[92:95]
rd a[124:127],v248 offset:15360
v_or_b32 v248, s89, v255
v_smfmac_f32_16x16x64_bf16 a[196:199], a[208:211], v[232:239], v241
wt lgkmcnt(4)
v_smfmac_f32_16x16x64_bf16 a[192:195], a[208:211], v[224:231], v241
rd a[64:67],v248 offset:0
mm v[0:3],a[96:99],v[224:227],v[0:3]
ex v217,v217
mm v[32:35],a[96:99],v[228:231],v[32:35]
rd a[68:71],v248 offset:1024
cv v232,v208,v209
mm v[36:39],a[100:103],v[228:231],v[36:39]
ex v218,v218
mm v[4:7],a[100:103],v[224:227],v[4:7]
rd a[72:75],v248 offset:2048
cv v233,v210,v211
mm v[8:11],a[104:107],v[224:227],v[8:11]
ex v219,v219
mm v[40:43],a[104:107],v[228:231],v[40:43]
rd a[76:79],v248 offset:3072
cv v236,v216,v217
mm v[44:47],a[108:111],v[228:231],v[44:47]
ex v212,v212
mm v[12:15],a[108:111],v[224:227],v[12:15]
rd a[80:83],v248 offset:4096
wt lgkmcnt(5)
mm v[16:19],a[112:115],v[224:227],v[16:19]
ex v213,v213
mm v[48:51],a[112:115],v[228:231],v[48:51]
rd a[84:87],v248 offset:5120
cv v237,v218,v219
mm v[52:55],a[116:119],v[228:231],v[52:55]
ex v214,v214
mm v[20:23],a[116:119],v[224:227],v[20:23]
rd a[88:91],v248 offset:6144
cv v234,v212,v213
mm v[24:27],a[120:123],v[224:227],v[24:27]
ex v215,v215
mm v[56:59],a[120:123],v[228:231],v[56:59]
rd a[92:95],v248 offset:7168
cv v235,v214,v215
mm v[60:63],a[124:127],v[228:231],v[60:63]
ex v220,v220
mm v[28:31],a[124:127],v[224:227],v[28:31]
s_cbranch_scc1 .La5_LA_F1E2
wt lgkmcnt(4)
mm v[192:195],a[64:67],v[128:131],0
ex v221,v221
mm v[192:195],a[68:71],v[132:135],v[192:195]
cv v238,v220,v221
s_add_u32 s85, s85, 1
mm v[192:195],a[72:75],v[136:139],v[192:195]
ex v222,v222
mm v[192:195],a[76:79],v[140:143],v[192:195]
ld v255,s[60:63],s17 offen offset:0 sc1 lds
mm v[200:203],a[76:79],v[156:159],0
ex v223,v223
mm v[200:203],a[72:75],v[152:155],v[200:203]
cv v239,v222,v223
s_add_u32 s89, s89, 16384
mm v[200:203],a[68:71],v[148:151],v[200:203]
ex v192,v192
mm v[200:203],a[64:67],v[144:147],v[200:203]
wt lgkmcnt(0)
s_and_b32 s89, s89, 0x1ffff
mm v[196:199],a[80:83],v[128:131],0
ex v193,v193
mm v[196:199],a[84:87],v[132:135],v[196:199]
cv v224,v192,v193
mm v[196:199],a[88:91],v[136:139],v[196:199]
ex v194,v194
mm v[196:199],a[92:95],v[140:143],v[196:199]
ld v255,s[60:63],s17 offen offset:1024 sc1 lds
mm v[204:207],a[92:95],v[156:159],0
ex v195,v195
mm v[204:207],a[88:91],v[152:155],v[204:207]
cv v225,v194,v195
mm v[204:207],a[84:87],v[148:151],v[204:207]
ex v200,v200
mm v[204:207],a[80:83],v[144:147],v[204:207]
ld v255,s[60:63],s17 offen offset:2048 sc1 lds
mm v[208:211],a[64:67],v[160:163],0
ex v201,v201
mm v[208:211],a[68:71],v[164:167],v[208:211]
cv v228,v200,v201
mm v[208:211],a[72:75],v[168:171],v[208:211]
ex v202,v202
mm v[208:211],a[76:79],v[172:175],v[208:211]
ld v255,s[60:63],s17 offen offset:3072 sc1 lds
mm v[216:219],a[76:79],v[188:191],0
ex v203,v203
mm v[216:219],a[72:75],v[184:187],v[216:219]
cv v229,v202,v203
mm v[216:219],a[68:71],v[180:183],v[216:219]
ex v196,v196
mm v[216:219],a[64:67],v[176:179],v[216:219]
buffer_load_dword a253, v254, s[36:39], s90 offen
mm v[212:215],a[80:83],v[160:163],0
ex v197,v197
mm v[212:215],a[84:87],v[164:167],v[212:215]
cv v226,v196,v197
s_add_u32 s90, s90, s21
mm v[212:215],a[88:91],v[168:171],v[212:215]
ex v198,v198
mm v[212:215],a[92:95],v[172:175],v[212:215]
s_add_u32 s17, s17, s80
s_add_u32 m0, m0, 16384
mm v[220:223],a[92:95],v[188:191],0
ex v199,v199
mm v[220:223],a[88:91],v[184:187],v[220:223]
cv v227,v198,v199
s_and_b32 m0, m0, s78
mm v[220:223],a[84:87],v[180:183],v[220:223]
ex v204,v204
mm v[220:223],a[80:83],v[176:179],v[220:223]
wt vmcnt(10)
s_barrier
mm v[64:67],a[96:99],v[232:235],v[64:67]
ex v205,v205
mm v[96:99],a[96:99],v[236:239],v[96:99]
rd a[96:99],v248 offset:8192
cv v230,v204,v205
mm v[100:103],a[100:103],v[236:239],v[100:103]
ex v206,v206
mm v[68:71],a[100:103],v[232:235],v[68:71]
rd a[100:103],v248 offset:9216
s_cmp_eq_u32 s85, s81
mm v[72:75],a[104:107],v[232:235],v[72:75]
ex v207,v207
mm v[104:107],a[104:107],v[236:239],v[104:107]
rd a[104:107],v248 offset:10240
cv v231,v206,v207
mm v[108:111],a[108:111],v[236:239],v[108:111]
ex v208,v208
mm v[76:79],a[108:111],v[232:235],v[76:79]
rd a[108:111],v248 offset:11264
s_cselect_b32 s17, s77, s17
mm v[80:83],a[112:115],v[232:235],v[80:83]
ex v209,v209
mm v[112:115],a[112:115],v[236:239],v[112:115]
rd a[112:115],v248 offset:12288
s_cselect_b32 s80, -8192, s80
mm v[116:119],a[116:119],v[236:239],v[116:119]
ex v210,v210
mm v[84:87],a[116:119],v[232:235],v[84:87]
rd a[116:119],v248 offset:13312
mm v[88:91],a[120:123],v[232:235],v[88:91]
ex v211,v211
mm v[120:123],a[120:123],v[236:239],v[120:123]
rd a[120:123],v248 offset:14336
s_bitcmp1_b64 s[98:99], s85
mm v[124:127],a[124:127],v[236:239],v[124:127]
ex v216,v216
mm v[92:95],a[124:127],v[232:235],v[92:95]
rd a[124:127],v248 offset:15360
v_or_b32 v248, s89, v255
v_smfmac_f32_16x16x64_bf16 a[196:199], a[208:211], v[232:239], v241
wt lgkmcnt(4)
v_smfmac_f32_16x16x64_bf16 a[192:195], a[208:211], v[224:231], v241
rd a[64:67],v248 offset:0
mm v[0:3],a[96:99],v[224:227],v[0:3]
ex v217,v217
mm v[32:35],a[96:99],v[228:231],v[32:35]
rd a[68:71],v248 offset:1024
cv v232,v208,v209
mm v[36:39],a[100:103],v[228:231],v[36:39]
ex v218,v218
mm v[4:7],a[100:103],v[224:227],v[4:7]
rd a[72:75],v248 offset:2048
cv v233,v210,v211
mm v[8:11],a[104:107],v[224:227],v[8:11]
ex v219,v219
mm v[40:43],a[104:107],v[228:231],v[40:43]
rd a[76:79],v248 offset:3072
cv v236,v216,v217
mm v[44:47],a[108:111],v[228:231],v[44:47]
ex v212,v212
mm v[12:15],a[108:111],v[224:227],v[12:15]
rd a[80:83],v248 offset:4096
wt lgkmcnt(5)
mm v[16:19],a[112:115],v[224:227],v[16:19]
ex v213,v213
mm v[48:51],a[112:115],v[228:231],v[48:51]
rd a[84:87],v248 offset:5120
cv v237,v218,v219
mm v[52:55],a[116:119],v[228:231],v[52:55]
ex v214,v214
mm v[20:23],a[116:119],v[224:227],v[20:23]
rd a[88:91],v248 offset:6144
cv v234,v212,v213
mm v[24:27],a[120:123],v[224:227],v[24:27]
ex v215,v215
mm v[56:59],a[120:123],v[228:231],v[56:59]
rd a[92:95],v248 offset:7168
cv v235,v214,v215
mm v[60:63],a[124:127],v[228:231],v[60:63]
ex v220,v220
mm v[28:31],a[124:127],v[224:227],v[28:31]
.La5_LA_F1E:
s_cbranch_scc0 .La5_LA_F1
.La5_LA_F1E2:
s_cmp_eq_u32 s85, s82
s_cbranch_scc1 .La5_LA_F1X
s_call_b64 s[24:25], .La5_LA_EVENT
s_branch .La5_LA_F1
.La5_LA_FE0:
s_nop 0
s_nop 0
.La5_LW2_1:
s_cbranch_execz .La5_LW2_R1
s_cmp_eq_u32 s85, s82
s_cbranch_scc1 .La5_LA_F1X
s_call_b64 s[24:25], .La5_LA_EVENT
s_branch .La5_LA_FS0
; ---- hand-offs between windows
.La5_LA_EVENT:
s_cmp_lg_u32 s85, 2
s_nop 0
s_nop 0
s_cbranch_scc1 .La5_LA_E1
wt vmcnt(0)
v_cmp_eq_u32 vcc, v250, v249
s_mov_b32 s17, s16
s_mov_b32 s78, 0x1ffff
s_lshl_b32 m0, s3, 12
s_or_b32 vcc_hi, vcc_hi, s79
s_cmp_eq_u64 vcc, -1
s_cbranch_scc0 .La5_LA_KSPIN
.La5_LA_KOK:
ld v255,s[60:63],s17 offen offset:0 sc1 lds
ld v255,s[60:63],s17 offen offset:1024 sc1 lds
ld v255,s[60:63],s17 offen offset:2048 sc1 lds
ld v255,s[60:63],s17 offen offset:3072 sc1 lds
s_add_u32 s17, s17, s80
s_add_u32 m0, m0, 16384
s_mov_b32 s20, s19
v_and_b32 v251, 4, v251
s_cmp_lg_u32 s3, 0
s_cbranch_scc1 .La5_LA_POLL
s_nop 0
ld v255,s[60:63],s17 offen offset:0 sc1 lds
ld v255,s[60:63],s17 offen offset:1024 sc1 lds
ld v255,s[60:63],s17 offen offset:2048 sc1 lds
ld v255,s[60:63],s17 offen offset:3072 sc1 lds
s_add_u32 s17, s17, s80
s_add_u32 m0, m0, 16384
.La5_LA_POLL:
s_cmp_lg_u32 s22, 0
s_nop 0
s_nop 0
s_cbranch_scc1 .La5_LA_WP
s_mov_b64 exec, 0
s_cmp_eq_u32 s3, 0
s_cbranch_scc1 .La5_LW5_0
s_cmp_eq_u32 s3, 1
s_cbranch_scc1 .La5_LW5_1
s_cmp_eq_u32 s3, 2
s_cbranch_scc1 .La5_LW5_2
s_branch .La5_LW5_3
.La5_LW5_R0:
s_mov_b64 exec, -1
.La5_LA_RETURN:
s_setpc_b64 s[24:25]
.La5_LA_WP:
s_mov_b64 exec, 0
s_cmp_eq_u32 s3, 0
s_cbranch_scc1 .La5_LW6_0
s_cmp_eq_u32 s3, 1
s_cbranch_scc1 .La5_LW6_1
s_cmp_eq_u32 s3, 2
s_cbranch_scc1 .La5_LW6_2
s_branch .La5_LW6_3
.La5_LW6_R0:
s_mov_b64 exec, -1
s_cmp_eq_u32 s73, 1
s_cbranch_scc1 .La5_LA_E0
buffer_load_dword v250, v251, s[64:67], s20 offen sc1
s_setpc_b64 s[24:25]
.La5_LA_E0:
s_mov_b32 s54, 16384
ld a[0:3],v255,s[52:55],0 offen offset:0 sc1
ld a[4:7],v255,s[52:55],0 offen offset:1024 sc1
ld a[8:11],v255,s[52:55],0 offen offset:2048 sc1
ld a[12:15],v255,s[52:55],0 offen offset:3072 sc1
ld a[16:19],v255,s[52:55],s95 offen offset:0 sc1
ld a[20:23],v255,s[52:55],s95 offen offset:1024 sc1
s_setpc_b64 s[24:25]
.La5_LA_E1:
ld a[24:27],v255,s[52:55],s95 offen offset:2048 sc1
ld a[28:31],v255,s[52:55],s95 offen offset:3072 sc1
ld a[32:35],v255,s[52:55],s96 offen offset:0 sc1
ld a[36:39],v255,s[52:55],s96 offen offset:1024 sc1
ld a[40:43],v255,s[52:55],s96 offen offset:2048 sc1
ld a[44:47],v255,s[52:55],s96 offen offset:3072 sc1
ld a[48:51],v255,s[52:55],s97 offen offset:0 sc1
ld a[52:55],v255,s[52:55],s97 offen offset:1024 sc1
ld a[56:59],v255,s[52:55],s97 offen offset:2048 sc1
ld a[60:63],v255,s[52:55],s97 offen offset:3072 sc1
s_setpc_b64 s[24:25]
; ---- helper: own block finished: l is checked (.La5_LA_SLOW if outside [2^-60, 2^60] or NaN), O / l leaves; the partner-s rows start
.La5_LA_F1X:
s_cmp_eq_u32 s75, 0
s_nop 0
s_nop 0
s_cbranch_scc1 .La5_LA_DC3
s_cmp_eq_u32 s22, 0
s_cbranch_scc1 .La5_LA_DC
s_cmp_eq_u32 s73, 1
s_cbranch_scc1 .La5_LA_F1Y
s_nop 0
v_cmp_eq_u32 vcc, v250, v249
s_mov_b32 s54, 16384
s_and_b32 s92, vcc_lo, 3
s_cmp_eq_u32 s92, 3
s_cbranch_scc0 .La5_LA_QSPIN
.La5_LA_QOK:
wt lgkmcnt(4)
mm v[192:195],a[64:67],v[128:131],0
ex v221,v221
mm v[192:195],a[68:71],v[132:135],v[192:195]
cv v238,v220,v221
s_add_u32 s85, s85, 1
mm v[192:195],a[72:75],v[136:139],v[192:195]
ex v222,v222
mm v[192:195],a[76:79],v[140:143],v[192:195]
ld v255,s[60:63],s17 offen offset:0 sc1 lds
mm v[200:203],a[76:79],v[156:159],0
ex v223,v223
mm v[200:203],a[72:75],v[152:155],v[200:203]
.La5_LW6_0:
s_cbranch_execz .La5_LW6_4
cv v239,v222,v223
s_add_u32 s89, s89, 16384
mm v[200:203],a[68:71],v[148:151],v[200:203]
ex v192,v192
mm v[200:203],a[64:67],v[144:147],v[200:203]
wt lgkmcnt(0)
s_and_b32 s89, s89, 0x1ffff
mm v[196:199],a[80:83],v[128:131],0
ex v193,v193
mm v[196:199],a[84:87],v[132:135],v[196:199]
cv v224,v192,v193
mm v[196:199],a[88:91],v[136:139],v[196:199]
ex v194,v194
mm v[196:199],a[92:95],v[140:143],v[196:199]
ld v255,s[60:63],s17 offen offset:1024 sc1 lds
mm v[204:207],a[92:95],v[156:159],0
ex v195,v195
mm v[204:207],a[88:91],v[152:155],v[204:207]
cv v225,v194,v195
mm v[204:207],a[84:87],v[148:151],v[204:207]
ex v200,v200
mm v[204:207],a[80:83],v[144:147],v[204:207]
ld v255,s[60:63],s17 offen offset:2048 sc1 lds
mm v[208:211],a[64:67],v[160:163],0
ex v201,v201
mm v[208:211],a[68:71],v[164:167],v[208:211]
cv v228,v200,v201
mm v[208:211],a[72:75],v[168:171],v[208:211]
ex v202,v202
mm v[208:211],a[76:79],v[172:175],v[208:211]
ld v255,s[60:63],s17 offen offset:3072 sc1 lds
mm v[216:219],a[76:79],v[188:191],0
ex v203,v203
mm v[216:219],a[72:75],v[184:187],v[216:219]
cv v229,v202,v203
mm v[216:219],a[68:71],v[180:183],v[216:219]
ex v196,v196
mm v[216:219],a[64:67],v[176:179],v[216:219]
ld a[0:3],v255,s[52:55],0 offen offset:0 sc1
mm v[212:215],a[80:83],v[160:163],0
ex v197,v197
mm v[212:215],a[84:87],v[164:167],v[212:215]
cv v226,v196,v197
s_add_u32 s17, s17, s80
mm v[212:215],a[88:91],v[168:171],v[212:215]
ex v198,v198
mm v[212:215],a[92:95],v[172:175],v[212:215]
s_add_u32 m0, m0, 16384
s_and_b32 m0, m0, s78
ld a[4:7],v255,s[52:55],0 offen offset:1024 sc1
mm v[220:223],a[92:95],v[188:191],0
ex v199,v199
mm v[220:223],a[88:91],v[184:187],v[220:223]
cv v227,v198,v199
mm v[220:223],a[84:87],v[180:183],v[220:223]
ex v204,v204
mm v[220:223],a[80:83],v[176:179],v[220:223]
wt vmcnt(12)
s_barrier
mm v[64:67],a[96:99],v[232:235],v[64:67]
ex v205,v205
mm v[96:99],a[96:99],v[236:239],v[96:99]
rd a[96:99],v248 offset:8192
cv v230,v204,v205
mm v[100:103],a[100:103],v[236:239],v[100:103]
ex v206,v206
mm v[68:71],a[100:103],v[232:235],v[68:71]
rd a[100:103],v248 offset:9216
ld a[8:11],v255,s[52:55],0 offen offset:2048 sc1
mm v[72:75],a[104:107],v[232:235],v[72:75]
ex v207,v207
mm v[104:107],a[104:107],v[236:239],v[104:107]
rd a[104:107],v248 offset:10240
cv v231,v206,v207
mm v[108:111],a[108:111],v[236:239],v[108:111]
ex v208,v208
mm v[76:79],a[108:111],v[232:235],v[76:79]
rd a[108:111],v248 offset:11264
ld a[12:15],v255,s[52:55],0 offen offset:3072 sc1
mm v[80:83],a[112:115],v[232:235],v[80:83]
ex v209,v209
mm v[112:115],a[112:115],v[236:239],v[112:115]
rd a[112:115],v248 offset:12288
mm v[116:119],a[116:119],v[236:239],v[116:119]
ex v210,v210
mm v[84:87],a[116:119],v[232:235],v[84:87]
rd a[116:119],v248 offset:13312
ld a[16:19],v255,s[52:55],s95 offen offset:0 sc1
mm v[88:91],a[120:123],v[232:235],v[88:91]
ex v211,v211
mm v[120:123],a[120:123],v[236:239],v[120:123]
rd a[120:123],v248 offset:14336
s_bitcmp1_b64 s[98:99], s85
mm v[124:127],a[124:127],v[236:239],v[124:127]
ex v216,v216
mm v[92:95],a[124:127],v[232:235],v[92:95]
rd a[124:127],v248 offset:15360
v_or_b32 v248, s89, v255
v_smfmac_f32_16x16x64_bf16 a[196:199], a[208:211], v[232:239], v241
wt lgkmcnt(4)
v_smfmac_f32_16x16x64_bf16 a[192:195], a[208:211], v[224:231], v241
rd a[64:67],v248 offset:0
mm v[0:3],a[96:99],v[224:227],v[0:3]
ex v217,v217
mm v[32:35],a[96:99],v[228:231],v[32:35]
rd a[68:71],v248 offset:1024
cv v232,v208,v209
mm v[36:39],a[100:103],v[228:231],v[36:39]
ex v218,v218
mm v[4:7],a[100:103],v[224:227],v[4:7]
rd a[72:75],v248 offset:2048
cv v233,v210,v211
mm v[8:11],a[104:107],v[224:227],v[8:11]
ex v219,v219
mm v[40:43],a[104:107],v[228:231],v[40:43]
rd a[76:79],v248 offset:3072
cv v236,v216,v217
mm v[44:47],a[108:111],v[228:231],v[44:47]
ex v212,v212
mm v[12:15],a[108:111],v[224:227],v[12:15]
rd a[80:83],v248 offset:4096
wt lgkmcnt(5)
mm v[16:19],a[112:115],v[224:227],v[16:19]
ex v213,v213
mm v[48:51],a[112:115],v[228:231],v[48:51]
rd a[84:87],v248 offset:5120
cv v237,v218,v219
mm v[52:55],a[116:119],v[228:231],v[52:55]
ex v214,v214
mm v[20:23],a[116:119],v[224:227],v[20:23]
rd a[88:91],v248 offset:6144
cv v234,v212,v213
mm v[24:27],a[120:123],v[224:227],v[24:27]
ex v215,v215
mm v[56:59],a[120:123],v[228:231],v[56:59]
rd a[92:95],v248 offset:7168
cv v235,v214,v215
mm v[60:63],a[124:127],v[228:231],v[60:63]
ex v220,v220
mm v[28:31],a[124:127],v[224:227],v[28:31]
ld a[20:23],v255,s[52:55],s95 offen offset:1024 sc1
wt lgkmcnt(4)
mm v[192:195],a[64:67],v[128:131],0
ex v221,v221
mm v[192:195],a[68:71],v[132:135],v[192:195]
cv v238,v220,v221
s_add_u32 s85, s85, 1
mm v[192:195],a[72:75],v[136:139],v[192:195]
ex v222,v222
mm v[192:195],a[76:79],v[140:143],v[192:195]
ld v255,s[60:63],s17 offen offset:0 sc1 lds
mm v[200:203],a[76:79],v[156:159],0
ex v223,v223
mm v[200:203],a[72:75],v[152:155],v[200:203]
cv v239,v222,v223
s_add_u32 s89, s89, 16384
mm v[200:203],a[68:71],v[148:151],v[200:203]
ex v192,v192
mm v[200:203],a[64:67],v[144:147],v[200:203]
wt lgkmcnt(0)
s_and_b32 s89, s89, 0x1ffff
ld a[24:27],v255,s[52:55],s95 offen offset:2048 sc1
mm v[196:199],a[80:83],v[128:131],0
ex v193,v193
mm v[196:199],a[84:87],v[132:135],v[196:199]
cv v224,v192,v193
mm v[196:199],a[88:91],v[136:139],v[196:199]
ex v194,v194
mm v[196:199],a[92:95],v[140:143],v[196:199]
ld v255,s[60:63],s17 offen offset:1024 sc1 lds
mm v[204:207],a[92:95],v[156:159],0
ex v195,v195
mm v[204:207],a[88:91],v[152:155],v[204:207]
cv v225,v194,v195
mm v[204:207],a[84:87],v[148:151],v[204:207]
ex v200,v200
mm v[204:207],a[80:83],v[144:147],v[204:207]
ld v255,s[60:63],s17 offen offset:2048 sc1 lds
mm v[208:211],a[64:67],v[160:163],0
ex v201,v201
mm v[208:211],a[68:71],v[164:167],v[208:211]
cv v228,v200,v201
mm v[208:211],a[72:75],v[168:171],v[208:211]
ex v202,v202
mm v[208:211],a[76:79],v[172:175],v[208:211]
ld v255,s[60:63],s17 offen offset:3072 sc1 lds
mm v[216:219],a[76:79],v[188:191],0
ex v203,v203
mm v[216:219],a[72:75],v[184:187],v[216:219]
cv v229,v202,v203
mm v[216:219],a[68:71],v[180:183],v[216:219]
ex v196,v196
mm v[216:219],a[64:67],v[176:179],v[216:219]
ld a[28:31],v255,s[52:55],s95 offen offset:3072 sc1
mm v[212:215],a[80:83],v[160:163],0
ex v197,v197
mm v[212:215],a[84:87],v[164:167],v[212:215]
cv v226,v196,v197
s_add_u32 s17, s17, s80
mm v[212:215],a[88:91],v[168:171],v[212:215]
ex v198,v198
mm v[212:215],a[92:95],v[172:175],v[212:215]
s_add_u32 m0, m0, 16384
s_and_b32 m0, m0, s78
ld a[32:35],v255,s[52:55],s96 offen offset:0 sc1
mm v[220:223],a[92:95],v[188:191],0
ex v199,v199
mm v[220:223],a[88:91],v[184:187],v[220:223]
cv v227,v198,v199
mm v[220:223],a[84:87],v[180:183],v[220:223]
ex v204,v204
mm v[220:223],a[80:83],v[176:179],v[220:223]
wt vmcnt(18)
s_barrier
mm v[64:67],a[96:99],v[232:235],v[64:67]
ex v205,v205
mm v[96:99],a[96:99],v[236:239],v[96:99]
rd a[96:99],v248 offset:8192
cv v230,v204,v205
mm v[100:103],a[100:103],v[236:239],v[100:103]
ex v206,v206
mm v[68:71],a[100:103],v[232:235],v[68:71]
rd a[100:103],v248 offset:9216
ld a[36:39],v255,s[52:55],s96 offen offset:1024 sc1
mm v[72:75],a[104:107],v[232:235],v[72:75]
ex v207,v207
mm v[104:107],a[104:107],v[236:239],v[104:107]
rd a[104:107],v248 offset:10240
cv v231,v206,v207
mm v[108:111],a[108:111],v[236:239],v[108:111]
ex v208,v208
mm v[76:79],a[108:111],v[232:235],v[76:79]
rd a[108:111],v248 offset:11264
ld a[40:43],v255,s[52:55],s96 offen offset:2048 sc1
mm v[80:83],a[112:115],v[232:235],v[80:83]
ex v209,v209
mm v[112:115],a[112:115],v[236:239],v[112:115]
rd a[112:115],v248 offset:12288
mm v[116:119],a[116:119],v[236:239],v[116:119]
ex v210,v210
mm v[84:87],a[116:119],v[232:235],v[84:87]
rd a[116:119],v248 offset:13312
ld a[44:47],v255,s[52:55],s96 offen offset:3072 sc1
mm v[88:91],a[120:123],v[232:235],v[88:91]
ex v211,v211
mm v[120:123],a[120:123],v[236:239],v[120:123]
rd a[120:123],v248 offset:14336
s_bitcmp1_b64 s[98:99], s85
mm v[124:127],a[124:127],v[236:239],v[124:127]
ex v216,v216
mm v[92:95],a[124:127],v[232:235],v[92:95]
rd a[124:127],v248 offset:15360
v_or_b32 v248, s89, v255
v_smfmac_f32_16x16x64_bf16 a[196:199], a[208:211], v[232:239], v241
wt lgkmcnt(4)
v_smfmac_f32_16x16x64_bf16 a[192:195], a[208:211], v[224:231], v241
rd a[64:67],v248 offset:0
mm v[0:3],a[96:99],v[224:227],v[0:3]
ex v217,v217
mm v[32:35],a[96:99],v[228:231],v[32:35]
rd a[68:71],v248 offset:1024
cv v232,v208,v209
mm v[36:39],a[100:103],v[228:231],v[36:39]
ex v218,v218
mm v[4:7],a[100:103],v[224:227],v[4:7]
rd a[72:75],v248 offset:2048
cv v233,v210,v211
mm v[8:11],a[104:107],v[224:227],v[8:11]
ex v219,v219
mm v[40:43],a[104:107],v[228:231],v[40:43]
rd a[76:79],v248 offset:3072
cv v236,v216,v217
mm v[44:47],a[108:111],v[228:231],v[44:47]
ex v212,v212
mm v[12:15],a[108:111],v[224:227],v[12:15]
rd a[80:83],v248 offset:4096
wt lgkmcnt(5)
mm v[16:19],a[112:115],v[224:227],v[16:19]
ex v213,v213
mm v[48:51],a[112:115],v[228:231],v[48:51]
rd a[84:87],v248 offset:5120
cv v237,v218,v219
mm v[52:55],a[116:119],v[228:231],v[52:55]
ex v214,v214
mm v[20:23],a[116:119],v[224:227],v[20:23]
rd a[88:91],v248 offset:6144
cv v234,v212,v213
mm v[24:27],a[120:123],v[224:227],v[24:27]
ex v215,v215
mm v[56:59],a[120:123],v[228:231],v[56:59]
rd a[92:95],v248 offset:7168
cv v235,v214,v215
mm v[60:63],a[124:127],v[228:231],v[60:63]
ex v220,v220
mm v[28:31],a[124:127],v[224:227],v[28:31]
ld a[48:51],v255,s[52:55],s97 offen offset:0 sc1
wt lgkmcnt(4)
mm v[192:195],a[64:67],v[128:131],0
ex v221,v221
mm v[192:195],a[68:71],v[132:135],v[192:195]
cv v238,v220,v221
s_add_u32 s85, s85, 1
mm v[192:195],a[72:75],v[136:139],v[192:195]
ex v222,v222
mm v[192:195],a[76:79],v[140:143],v[192:195]
ld v255,s[60:63],s17 offen offset:0 sc1 lds
mm v[200:203],a[76:79],v[156:159],0
ex v223,v223
mm v[200:203],a[72:75],v[152:155],v[200:203]
cv v239,v222,v223
s_add_u32 s89, s89, 16384
mm v[200:203],a[68:71],v[148:151],v[200:203]
ex v192,v192
mm v[200:203],a[64:67],v[144:147],v[200:203]
wt lgkmcnt(0)
s_and_b32 s89, s89, 0x1ffff
ld a[52:55],v255,s[52:55],s97 offen offset:1024 sc1
mm v[196:199],a[80:83],v[128:131],0
ex v193,v193
mm v[196:199],a[84:87],v[132:135],v[196:199]
cv v224,v192,v193
mm v[196:199],a[88:91],v[136:139],v[196:199]
ex v194,v194
mm v[196:199],a[92:95],v[140:143],v[196:199]
ld v255,s[60:63],s17 offen offset:1024 sc1 lds
mm v[204:207],a[92:95],v[156:159],0
ex v195,v195
mm v[204:207],a[88:91],v[152:155],v[204:207]
cv v225,v194,v195
mm v[204:207],a[84:87],v[148:151],v[204:207]
ex v200,v200
mm v[204:207],a[80:83],v[144:147],v[204:207]
ld v255,s[60:63],s17 offen offset:2048 sc1 lds
mm v[208:211],a[64:67],v[160:163],0
ex v201,v201
mm v[208:211],a[68:71],v[164:167],v[208:211]
cv v228,v200,v201
mm v[208:211],a[72:75],v[168:171],v[208:211]
ex v202,v202
mm v[208:211],a[76:79],v[172:175],v[208:211]
ld v255,s[60:63],s17 offen offset:3072 sc1 lds
mm v[216:219],a[76:79],v[188:191],0
ex v203,v203
mm v[216:219],a[72:75],v[184:187],v[216:219]
cv v229,v202,v203
mm v[216:219],a[68:71],v[180:183],v[216:219]
ex v196,v196
mm v[216:219],a[64:67],v[176:179],v[216:219]
ld a[56:59],v255,s[52:55],s97 offen offset:2048 sc1
mm v[212:215],a[80:83],v[160:163],0
ex v197,v197
mm v[212:215],a[84:87],v[164:167],v[212:215]
cv v226,v196,v197
s_add_u32 s17, s17, s80
mm v[212:215],a[88:91],v[168:171],v[212:215]
ex v198,v198
mm v[212:215],a[92:95],v[172:175],v[212:215]
s_add_u32 m0, m0, 16384
s_and_b32 m0, m0, s78
ld a[60:63],v255,s[52:55],s97 offen offset:3072 sc1
mm v[220:223],a[92:95],v[188:191],0
ex v199,v199
mm v[220:223],a[88:91],v[184:187],v[220:223]
cv v227,v198,v199
mm v[220:223],a[84:87],v[180:183],v[220:223]
ex v204,v204
mm v[220:223],a[80:83],v[176:179],v[220:223]
wt vmcnt(24)
s_barrier
mm v[64:67],a[96:99],v[232:235],v[64:67]
ex v205,v205
mm v[96:99],a[96:99],v[236:239],v[96:99]
rd a[96:99],v248 offset:8192
cv v230,v204,v205
mm v[100:103],a[100:103],v[236:239],v[100:103]
ex v206,v206
mm v[68:71],a[100:103],v[232:235],v[68:71]
rd a[100:103],v248 offset:9216
mm v[72:75],a[104:107],v[232:235],v[72:75]
ex v207,v207
mm v[104:107],a[104:107],v[236:239],v[104:107]
rd a[104:107],v248 offset:10240
cv v231,v206,v207
mm v[108:111],a[108:111],v[236:239],v[108:111]
ex v208,v208
mm v[76:79],a[108:111],v[232:235],v[76:79]
rd a[108:111],v248 offset:11264
mm v[80:83],a[112:115],v[232:235],v[80:83]
ex v209,v209
mm v[112:115],a[112:115],v[236:239],v[112:115]
rd a[112:115],v248 offset:12288
mm v[116:119],a[116:119],v[236:239],v[116:119]
ex v210,v210
mm v[84:87],a[116:119],v[232:235],v[84:87]
rd a[116:119],v248 offset:13312
mm v[88:91],a[120:123],v[232:235],v[88:91]
ex v211,v211
mm v[120:123],a[120:123],v[236:239],v[120:123]
rd a[120:123],v248 offset:14336
s_bitcmp1_b64 s[98:99], s85
mm v[124:127],a[124:127],v[236:239],v[124:127]
ex v216,v216
mm v[92:95],a[124:127],v[232:235],v[92:95]
rd a[124:127],v248 offset:15360
v_or_b32 v248, s89, v255
v_smfmac_f32_16x16x64_bf16 a[196:199], a[208:211], v[232:239], v241
wt lgkmcnt(4)
v_smfmac_f32_16x16x64_bf16 a[192:195], a[208:211], v[224:231], v241
rd a[64:67],v248 offset:0
mm v[0:3],a[96:99],v[224:227],v[0:3]
ex v217,v217
mm v[32:35],a[96:99],v[228:231],v[32:35]
rd a[68:71],v248 offset:1024
cv v232,v208,v209
mm v[36:39],a[100:103],v[228:231],v[36:39]
ex v218,v218
mm v[4:7],a[100:103],v[224:227],v[4:7]
rd a[72:75],v248 offset:2048
cv v233,v210,v211
mm v[8:11],a[104:107],v[224:227],v[8:11]
ex v219,v219
mm v[40:43],a[104:107],v[228:231],v[40:43]
rd a[76:79],v248 offset:3072
cv v236,v216,v217
mm v[44:47],a[108:111],v[228:231],v[44:47]
ex v212,v212
mm v[12:15],a[108:111],v[224:227],v[12:15]
rd a[80:83],v248 offset:4096
wt lgkmcnt(5)
mm v[16:19],a[112:115],v[224:227],v[16:19]
ex v213,v213
mm v[48:51],a[112:115],v[228:231],v[48:51]
rd a[84:87],v248 offset:5120
cv v237,v218,v219
mm v[52:55],a[116:119],v[228:231],v[52:55]
ex v214,v214
mm v[20:23],a[116:119],v[224:227],v[20:23]
rd a[88:91],v248 offset:6144
cv v234,v212,v213
mm v[24:27],a[120:123],v[224:227],v[24:27]
ex v215,v215
mm v[56:59],a[120:123],v[228:231],v[56:59]
rd a[92:95],v248 offset:7168
cv v235,v214,v215
mm v[60:63],a[124:127],v[228:231],v[60:63]
ex v220,v220
mm v[28:31],a[124:127],v[224:227],v[28:31]
.La5_LA_F1Y:
.La5_LW6_1:
s_cbranch_execz .La5_LW6_5
wt lgkmcnt(4)
wt vmcnt(0)
mm v[192:195],a[64:67],a[0:3],0
ex v221,v221
mm v[192:195],a[68:71],a[4:7],v[192:195]
cv v238,v220,v221
s_add_u32 s89, s89, 16384
mm v[192:195],a[72:75],a[8:11],v[192:195]
ex v222,v222
mm v[192:195],a[76:79],a[12:15],v[192:195]
ld v255,s[60:63],s17 offen offset:0 sc1 lds
ar v242,a192
ar v240,a193
v_add_f32 v242, v242, v240
mm v[200:203],a[76:79],a[28:31],0
ex v223,v223
ar v243,a194
ar v240,a195
v_add_f32 v243, v243, v240
mm v[200:203],a[72:75],a[24:27],v[200:203]
cv v239,v222,v223
s_and_b32 s89, s89, 0x1ffff
aw a192,0
aw a193,0
aw a194,0
mm v[200:203],a[68:71],a[20:23],v[200:203]
ex v192,v192
aw a195,0
v_subrev_u32 v240, 562036736, v242
mm v[200:203],a[64:67],a[16:19],v[200:203]
wt lgkmcnt(0)
v_rcp_f32 v242, v242
v_cmp_le_u32 vcc, 1006632960, v240
v_subrev_u32 v240, 562036736, v243
mm v[196:199],a[80:83],a[0:3],0
ex v193,v193
s_mov_b64 s[92:93], vcc
v_cmp_le_u32 vcc, 1006632960, v240
mm v[196:199],a[84:87],a[4:7],v[196:199]
cv v224,v192,v193
v_rcp_f32_e64 v243, v243
mm v[196:199],a[88:91],a[8:11],v[196:199]
v_exp_f32_e64 v194, v194
s_or_b64 vcc, vcc, s[92:93]
s_cbranch_scc1 .La5_LA_SC1
.La5_LA_SR1:
v_mul_f32 v0, v0, v242
mm v[196:199],a[92:95],a[12:15],v[196:199]
ld v255,s[60:63],s17 offen offset:1024 sc1 lds
v_mul_f32 v1, v1, v242
v_mul_f32 v2, v2, v242
v_mul_f32 v3, v3, v242
mm v[204:207],a[92:95],a[28:31],0
ex v195,v195
v_mul_f32 v4, v4, v242
v_mul_f32 v5, v5, v242
v_mul_f32 v6, v6, v242
mm v[204:207],a[88:91],a[24:27],v[204:207]
cv v225,v194,v195
v_mul_f32 v7, v7, v242
cv v128,v0,v1
cv v129,v2,v3
cv v130,v4,v5
mm v[204:207],a[84:87],a[20:23],v[204:207]
ex v200,v200
cv v131,v6,v7
v_mul_f32 v8, v8, v242
v_mul_f32 v9, v9, v242
mm v[204:207],a[80:83],a[16:19],v[204:207]
ld v255,s[60:63],s17 offen offset:2048 sc1 lds
v_mul_f32 v10, v10, v242
v_mul_f32 v11, v11, v242
v_mul_f32 v12, v12, v242
mm v[208:211],a[64:67],a[32:35],0
ex v201,v201
v_mul_f32 v13, v13, v242
v_mul_f32 v14, v14, v242
v_mul_f32 v15, v15, v242
mm v[208:211],a[68:71],a[36:39],v[208:211]
cv v228,v200,v201
cv v132,v8,v9
cv v133,v10,v11
cv v134,v12,v13
cv v135,v14,v15
mm v[208:211],a[72:75],a[40:43],v[208:211]
ex v202,v202
v_mul_f32 v16, v16, v242
v_mul_f32 v17, v17, v242
v_mul_f32 v18, v18, v242
mm v[208:211],a[76:79],a[44:47],v[208:211]
ld v255,s[60:63],s17 offen offset:3072 sc1 lds
v_mul_f32 v19, v19, v242
v_mul_f32 v20, v20, v242
v_mul_f32 v21, v21, v242
mm v[216:219],a[76:79],a[60:63],0
ex v203,v203
v_mul_f32 v22, v22, v242
v_mul_f32 v23, v23, v242
cv v136,v16,v17
mm v[216:219],a[72:75],a[56:59],v[216:219]
cv v229,v202,v203
cv v137,v18,v19
cv v138,v20,v21
cv v139,v22,v23
v_mul_f32 v24, v24, v242
mm v[216:219],a[68:71],a[52:55],v[216:219]
ex v196,v196
v_mul_f32 v25, v25, v242
v_mul_f32 v26, v26, v242
v_mul_f32 v27, v27, v242
mm v[216:219],a[64:67],a[48:51],v[216:219]
v_mul_f32 v28, v28, v242
v_mul_f32 v29, v29, v242
v_mul_f32 v30, v30, v242
v_mul_f32 v31, v31, v242
cv v140,v24,v25
mm v[212:215],a[80:83],a[32:35],0
ex v197,v197
s_add_u32 s17, s17, s80
cv v141,v26,v27
cv v142,v28,v29
mm v[212:215],a[84:87],a[36:39],v[212:215]
cv v226,v196,v197
s_add_u32 m0, m0, 16384
s_and_b32 m0, m0, s78
cv v143,v30,v31
v_mul_f32 v32, v32, v243
mm v[212:215],a[88:91],a[40:43],v[212:215]
ex v198,v198
v_mul_f32 v33, v33, v243
v_mul_f32 v34, v34, v243
v_mul_f32 v35, v35, v243
mm v[212:215],a[92:95],a[44:47],v[212:215]
st v[128:131],v244,s[44:47],s91 offen offset:0 sc1
v_mul_f32 v36, v36, v243
v_mul_f32 v37, v37, v243
v_mul_f32 v38, v38, v243
mm v[220:223],a[92:95],a[60:63],0
ex v199,v199
v_mul_f32 v39, v39, v243
cv v144,v32,v33
cv v145,v34,v35
mm v[220:223],a[88:91],a[56:59],v[220:223]
cv v227,v198,v199
cv v146,v36,v37
cv v147,v38,v39
v_mul_f32 v40, v40, v243
v_mul_f32 v41, v41, v243
mm v[220:223],a[84:87],a[52:55],v[220:223]
ex v204,v204
v_mul_f32 v42, v42, v243
v_mul_f32 v43, v43, v243
v_mul_f32 v44, v44, v243
mm v[220:223],a[80:83],a[48:51],v[220:223]
s_nop 0
s_barrier
v_mul_f32 v45, v45, v243
v_mul_f32 v46, v46, v243
v_mul_f32 v47, v47, v243
mm v[64:67],a[96:99],v[232:235],v[64:67]
ex v205,v205
cv v148,v40,v41
cv v149,v42,v43
cv v150,v44,v45
mm v[96:99],a[96:99],v[236:239],v[96:99]
.La5_LW6_2:
s_cbranch_execz .La5_LW6_6
rd a[96:99],v248 offset:8192
cv v230,v204,v205
cv v151,v46,v47
v_mul_f32 v48, v48, v243
v_mul_f32 v49, v49, v243
mm v[100:103],a[100:103],v[236:239],v[100:103]
ex v206,v206
v_mul_f32 v50, v50, v243
v_mul_f32 v51, v51, v243
v_mul_f32 v52, v52, v243
mm v[68:71],a[100:103],v[232:235],v[68:71]
rd a[100:103],v248 offset:9216
st v[132:135],v244,s[44:47],s91 offen offset:1024 sc1
v_mul_f32 v53, v53, v243
v_mul_f32 v54, v54, v243
mm v[72:75],a[104:107],v[232:235],v[72:75]
ex v207,v207
v_mul_f32 v55, v55, v243
cv v152,v48,v49
cv v153,v50,v51
mm v[104:107],a[104:107],v[236:239],v[104:107]
rd a[104:107],v248 offset:10240
cv v231,v206,v207
cv v154,v52,v53
cv v155,v54,v55
v_mul_f32 v56, v56, v243
mm v[108:111],a[108:111],v[236:239],v[108:111]
ex v208,v208
v_mul_f32 v57, v57, v243
v_mul_f32 v58, v58, v243
v_mul_f32 v59, v59, v243
mm v[76:79],a[108:111],v[232:235],v[76:79]
rd a[108:111],v248 offset:11264
v_mul_f32 v60, v60, v243
v_mul_f32 v61, v61, v243
v_mul_f32 v62, v62, v243
v_mul_f32 v63, v63, v243
mm v[80:83],a[112:115],v[232:235],v[80:83]
ex v209,v209
cv v156,v56,v57
cv v157,v58,v59
cv v158,v60,v61
mm v[112:115],a[112:115],v[236:239],v[112:115]
rd a[112:115],v248 offset:12288
st v[136:139],v244,s[44:47],s91 offen offset:2048 sc1
cv v159,v62,v63
mm v[116:119],a[116:119],v[236:239],v[116:119]
ex v210,v210
mm v[84:87],a[116:119],v[232:235],v[84:87]
rd a[116:119],v248 offset:13312
mm v[88:91],a[120:123],v[232:235],v[88:91]
ex v211,v211
mm v[120:123],a[120:123],v[236:239],v[120:123]
rd a[120:123],v248 offset:14336
st v[140:143],v244,s[44:47],s91 offen offset:3072 sc1
mm v[124:127],a[124:127],v[236:239],v[124:127]
ex v216,v216
mm v[92:95],a[124:127],v[232:235],v[92:95]
rd a[124:127],v248 offset:15360
v_or_b32 v248, s89, v255
v_smfmac_f32_16x16x64_bf16 a[196:199], a[208:211], v[232:239], v241
wt lgkmcnt(4)
v_smfmac_f32_16x16x64_bf16 a[192:195], a[208:211], v[224:231], v241
rd a[64:67],v248 offset:0
mm v[0:3],a[96:99],v[224:227],0
ex v217,v217
mm v[32:35],a[96:99],v[228:231],0
rd a[68:71],v248 offset:1024
cv v232,v208,v209
ar v242,a196
ar v240,a197
v_add_f32 v242, v242, v240
mm v[36:39],a[100:103],v[228:231],0
ex v218,v218
ar v243,a198
ar v240,a199
v_add_f32 v243, v243, v240
mm v[4:7],a[100:103],v[224:227],0
rd a[72:75],v248 offset:2048
cv v233,v210,v211
st v[144:147],v245,s[44:47],s91 offen offset:0 sc1
aw a196,0
mm v[8:11],a[104:107],v[224:227],0
ex v219,v219
aw a197,0
aw a198,0
aw a199,0
mm v[40:43],a[104:107],v[228:231],0
rd a[76:79],v248 offset:3072
cv v236,v216,v217
v_subrev_u32 v240, 562036736, v242
v_rcp_f32 v242, v242
mm v[44:47],a[108:111],v[228:231],0
ex v212,v212
v_cmp_le_u32 vcc, 1006632960, v240
v_subrev_u32 v240, 562036736, v243
s_mov_b64 s[92:93], vcc
mm v[12:15],a[108:111],v[224:227],0
rd a[80:83],v248 offset:4096
wt lgkmcnt(5)
v_cmp_le_u32 vcc, 1006632960, v240
v_rcp_f32_e64 v243, v243
mm v[16:19],a[112:115],v[224:227],0
v_exp_f32_e64 v213, v213
s_or_b64 vcc, vcc, s[92:93]
s_cbranch_scc1 .La5_LA_SC2
.La5_LA_SR2:
v_mul_f32 v64, v64, v242
mm v[48:51],a[112:115],v[228:231],0
rd a[84:87],v248 offset:5120
cv v237,v218,v219
v_mul_f32 v65, v65, v242
v_mul_f32 v66, v66, v242
v_mul_f32 v67, v67, v242
mm v[52:55],a[116:119],v[228:231],0
ex v214,v214
v_mul_f32 v68, v68, v242
v_mul_f32 v69, v69, v242
v_mul_f32 v70, v70, v242
mm v[20:23],a[116:119],v[224:227],0
rd a[88:91],v248 offset:6144
cv v234,v212,v213
st v[148:151],v245,s[44:47],s91 offen offset:1024 sc1
v_mul_f32 v71, v71, v242
mm v[24:27],a[120:123],v[224:227],0
ex v215,v215
cv v160,v64,v65
cv v161,v66,v67
cv v162,v68,v69
mm v[56:59],a[120:123],v[228:231],0
rd a[92:95],v248 offset:7168
cv v235,v214,v215
cv v163,v70,v71
v_mul_f32 v72, v72, v242
v_mul_f32 v73, v73, v242
mm v[60:63],a[124:127],v[228:231],0
ex v220,v220
v_mul_f32 v74, v74, v242
v_mul_f32 v75, v75, v242
v_mul_f32 v76, v76, v242
mm v[28:31],a[124:127],v[224:227],0
s_add_u32 s85, s85, 1
v_mul_f32 v77, v77, v242
v_mul_f32 v78, v78, v242
v_mul_f32 v79, v79, v242
cv v164,v72,v73
wt lgkmcnt(4)
mm v[192:195],a[64:67],a[0:3],0
ex v221,v221
cv v165,v74,v75
cv v166,v76,v77
cv v167,v78,v79
mm v[192:195],a[68:71],a[4:7],v[192:195]
cv v238,v220,v221
s_add_u32 s89, s89, 16384
v_mul_f32 v80, v80, v242
v_mul_f32 v81, v81, v242
v_mul_f32 v82, v82, v242
mm v[192:195],a[72:75],a[8:11],v[192:195]
ex v222,v222
v_mul_f32 v83, v83, v242
v_mul_f32 v84, v84, v242
v_mul_f32 v85, v85, v242
mm v[192:195],a[76:79],a[12:15],v[192:195]
ld v255,s[60:63],s17 offen offset:0 sc1 lds
v_mul_f32 v86, v86, v242
v_mul_f32 v87, v87, v242
cv v168,v80,v81
mm v[200:203],a[76:79],a[28:31],0
ex v223,v223
cv v169,v82,v83
cv v170,v84,v85
cv v171,v86,v87
mm v[200:203],a[72:75],a[24:27],v[200:203]
cv v239,v222,v223
s_and_b32 s89, s89, 0x1ffff
v_mul_f32 v88, v88, v242
v_mul_f32 v89, v89, v242
v_mul_f32 v90, v90, v242
mm v[200:203],a[68:71],a[20:23],v[200:203]
ex v192,v192
v_mul_f32 v91, v91, v242
v_mul_f32 v92, v92, v242
v_mul_f32 v93, v93, v242
mm v[200:203],a[64:67],a[16:19],v[200:203]
wt lgkmcnt(0)
st v[152:155],v245,s[44:47],s91 offen offset:2048 sc1
v_mul_f32 v94, v94, v242
v_mul_f32 v95, v95, v242
mm v[196:199],a[80:83],a[0:3],0
ex v193,v193
cv v172,v88,v89
cv v173,v90,v91
cv v174,v92,v93
mm v[196:199],a[84:87],a[4:7],v[196:199]
cv v224,v192,v193
cv v175,v94,v95
v_mul_f32 v96, v96, v243
v_mul_f32 v97, v97, v243
v_mul_f32 v98, v98, v243
mm v[196:199],a[88:91],a[8:11],v[196:199]
ex v194,v194
v_mul_f32 v99, v99, v243
v_mul_f32 v100, v100, v243
v_mul_f32 v101, v101, v243
mm v[196:199],a[92:95],a[12:15],v[196:199]
ld v255,s[60:63],s17 offen offset:1024 sc1 lds
v_mul_f32 v102, v102, v243
v_mul_f32 v103, v103, v243
cv v176,v96,v97
mm v[204:207],a[92:95],a[28:31],0
ex v195,v195
cv v177,v98,v99
cv v178,v100,v101
cv v179,v102,v103
mm v[204:207],a[88:91],a[24:27],v[204:207]
cv v225,v194,v195
st v[156:159],v245,s[44:47],s91 offen offset:3072 sc1
v_mul_f32 v104, v104, v243
v_mul_f32 v105, v105, v243
mm v[204:207],a[84:87],a[20:23],v[204:207]
ex v200,v200
v_mul_f32 v106, v106, v243
v_mul_f32 v107, v107, v243
v_mul_f32 v108, v108, v243
mm v[204:207],a[80:83],a[16:19],v[204:207]
ld v255,s[60:63],s17 offen offset:2048 sc1 lds
v_mul_f32 v109, v109, v243
v_mul_f32 v110, v110, v243
v_mul_f32 v111, v111, v243
mm v[208:211],a[64:67],a[32:35],0
ex v201,v201
cv v180,v104,v105
cv v181,v106,v107
cv v182,v108,v109
mm v[208:211],a[68:71],a[36:39],v[208:211]
cv v228,v200,v201
cv v183,v110,v111
v_mul_f32 v112, v112, v243
v_mul_f32 v113, v113, v243
v_mul_f32 v114, v114, v243
mm v[208:211],a[72:75],a[40:43],v[208:211]
ex v202,v202
v_mul_f32 v115, v115, v243
v_mul_f32 v116, v116, v243
v_mul_f32 v117, v117, v243
mm v[208:211],a[76:79],a[44:47],v[208:211]
ld v255,s[60:63],s17 offen offset:3072 sc1 lds
v_mul_f32 v118, v118, v243
v_mul_f32 v119, v119, v243
cv v184,v112,v113
mm v[216:219],a[76:79],a[60:63],0
ex v203,v203
cv v185,v114,v115
cv v186,v116,v117
cv v187,v118,v119
mm v[216:219],a[72:75],a[56:59],v[216:219]
cv v229,v202,v203
v_mul_f32 v120, v120, v243
v_mul_f32 v121, v121, v243
v_mul_f32 v122, v122, v243
v_mul_f32 v123, v123, v243
mm v[216:219],a[68:71],a[52:55],v[216:219]
ex v196,v196
v_mul_f32 v124, v124, v243
v_mul_f32 v125, v125, v243
v_mul_f32 v126, v126, v243
mm v[216:219],a[64:67],a[48:51],v[216:219]
v_mul_f32 v127, v127, v243
cv v188,v120,v121
cv v189,v122,v123
cv v190,v124,v125
cv v191,v126,v127
mm v[212:215],a[80:83],a[32:35],0
ex v197,v197
s_add_u32 s17, s17, s80
mm v[212:215],a[84:87],a[36:39],v[212:215]
cv v226,v196,v197
s_add_u32 m0, m0, 16384
s_and_b32 m0, m0, s78
mm v[212:215],a[88:91],a[40:43],v[212:215]
ex v198,v198
mm v[212:215],a[92:95],a[44:47],v[212:215]
st v[160:163],v246,s[44:47],s91 offen offset:0 sc1
mm v[220:223],a[92:95],a[60:63],0
ex v199,v199
mm v[220:223],a[88:91],a[56:59],v[220:223]
cv v227,v198,v199
mm v[220:223],a[84:87],a[52:55],v[220:223]
ex v204,v204
mm v[220:223],a[80:83],a[48:51],v[220:223]
wt vmcnt(18)
s_barrier
mm v[64:67],a[96:99],v[232:235],0
ex v205,v205
mm v[96:99],a[96:99],v[236:239],0
rd a[96:99],v248 offset:8192
cv v230,v204,v205
mm v[100:103],a[100:103],v[236:239],0
ex v206,v206
mm v[68:71],a[100:103],v[232:235],0
rd a[100:103],v248 offset:9216
st v[164:167],v246,s[44:47],s91 offen offset:1024 sc1
mm v[72:75],a[104:107],v[232:235],0
ex v207,v207
mm v[104:107],a[104:107],v[236:239],0
rd a[104:107],v248 offset:10240
cv v231,v206,v207
mm v[108:111],a[108:111],v[236:239],0
ex v208,v208
mm v[76:79],a[108:111],v[232:235],0
rd a[108:111],v248 offset:11264
st v[168:171],v246,s[44:47],s91 offen offset:2048 sc1
mm v[80:83],a[112:115],v[232:235],0
ex v209,v209
mm v[112:115],a[112:115],v[236:239],0
rd a[112:115],v248 offset:12288
mm v[116:119],a[116:119],v[236:239],0
ex v210,v210
mm v[84:87],a[116:119],v[232:235],0
rd a[116:119],v248 offset:13312
st v[172:175],v246,s[44:47],s91 offen offset:3072 sc1
mm v[88:91],a[120:123],v[232:235],0
ex v211,v211
mm v[120:123],a[120:123],v[236:239],0
rd a[120:123],v248 offset:14336
mm v[124:127],a[124:127],v[236:239],0
ex v216,v216
mm v[92:95],a[124:127],v[232:235],0
rd a[124:127],v248 offset:15360
v_or_b32 v248, s89, v255
st v[176:179],v247,s[44:47],s91 offen offset:0 sc1
v_smfmac_f32_16x16x64_bf16 a[196:199], a[208:211], v[232:239], v241
wt lgkmcnt(4)
v_smfmac_f32_16x16x64_bf16 a[192:195], a[208:211], v[224:231], v241
rd a[64:67],v248 offset:0
mm v[0:3],a[96:99],v[224:227],v[0:3]
ex v217,v217
mm v[32:35],a[96:99],v[228:231],v[32:35]
rd a[68:71],v248 offset:1024
cv v232,v208,v209
mm v[36:39],a[100:103],v[228:231],v[36:39]
ex v218,v218
mm v[4:7],a[100:103],v[224:227],v[4:7]
rd a[72:75],v248 offset:2048
cv v233,v210,v211
st v[180:183],v247,s[44:47],s91 offen offset:1024 sc1
mm v[8:11],a[104:107],v[224:227],v[8:11]
ex v219,v219
mm v[40:43],a[104:107],v[228:231],v[40:43]
rd a[76:79],v248 offset:3072
cv v236,v216,v217
mm v[44:47],a[108:111],v[228:231],v[44:47]
ex v212,v212
mm v[12:15],a[108:111],v[224:227],v[12:15]
rd a[80:83],v248 offset:4096
wt lgkmcnt(5)
mm v[16:19],a[112:115],v[224:227],v[16:19]
ex v213,v213
mm v[48:51],a[112:115],v[228:231],v[48:51]
rd a[84:87],v248 offset:5120
cv v237,v218,v219
st v[184:187],v247,s[44:47],s91 offen offset:2048 sc1
mm v[52:55],a[116:119],v[228:231],v[52:55]
ex v214,v214
mm v[20:23],a[116:119],v[224:227],v[20:23]
rd a[88:91],v248 offset:6144
cv v234,v212,v213
mm v[24:27],a[120:123],v[224:227],v[24:27]
ex v215,v215
mm v[56:59],a[120:123],v[228:231],v[56:59]
rd a[92:95],v248 offset:7168
cv v235,v214,v215
st v[188:191],v247,s[44:47],s91 offen offset:3072 sc1
mm v[60:63],a[124:127],v[228:231],v[60:63]
ex v220,v220
mm v[28:31],a[124:127],v[224:227],v[28:31]
s_add_u32 s85, s85, 1
s_cmp_eq_u32 s85, 22
s_cbranch_scc1 .La5_LA_F3
.p2align 6
.La5_LA_F2:
wt lgkmcnt(4)
mm v[192:195],a[64:67],a[0:3],0
ex v221,v221
mm v[192:195],a[68:71],a[4:7],v[192:195]
cv v238,v220,v221
s_add_u32 s85, s85, 1
mm v[192:195],a[72:75],a[8:11],v[192:195]
ex v222,v222
mm v[192:195],a[76:79],a[12:15],v[192:195]
ld v255,s[60:63],s17 offen offset:0 sc1 lds
mm v[200:203],a[76:79],a[28:31],0
ex v223,v223
mm v[200:203],a[72:75],a[24:27],v[200:203]
cv v239,v222,v223
s_add_u32 s89, s89, 16384
mm v[200:203],a[68:71],a[20:23],v[200:203]
ex v192,v192
mm v[200:203],a[64:67],a[16:19],v[200:203]
wt lgkmcnt(0)
s_and_b32 s89, s89, 0x1ffff
mm v[196:199],a[80:83],a[0:3],0
ex v193,v193
mm v[196:199],a[84:87],a[4:7],v[196:199]
cv v224,v192,v193
mm v[196:199],a[88:91],a[8:11],v[196:199]
ex v194,v194
mm v[196:199],a[92:95],a[12:15],v[196:199]
ld v255,s[60:63],s17 offen offset:1024 sc1 lds
mm v[204:207],a[92:95],a[28:31],0
ex v195,v195
mm v[204:207],a[88:91],a[24:27],v[204:207]
cv v225,v194,v195
mm v[204:207],a[84:87],a[20:23],v[204:207]
ex v200,v200
mm v[204:207],a[80:83],a[16:19],v[204:207]
ld v255,s[60:63],s17 offen offset:2048 sc1 lds
mm v[208:211],a[64:67],a[32:35],0
ex v201,v201
mm v[208:211],a[68:71],a[36:39],v[208:211]
cv v228,v200,v201
mm v[208:211],a[72:75],a[40:43],v[208:211]
ex v202,v202
mm v[208:211],a[76:79],a[44:47],v[208:211]
ld v255,s[60:63],s17 offen offset:3072 sc1 lds
mm v[216:219],a[76:79],a[60:63],0
ex v203,v203
mm v[216:219],a[72:75],a[56:59],v[216:219]
cv v229,v202,v203
mm v[216:219],a[68:71],a[52:55],v[216:219]
ex v196,v196
mm v[216:219],a[64:67],a[48:51],v[216:219]
buffer_load_dword a253, v254, s[36:39], s90 offen
mm v[212:215],a[80:83],a[32:35],0
ex v197,v197
mm v[212:215],a[84:87],a[36:39],v[212:215]
cv v226,v196,v197
s_add_u32 s90, s90, s21
mm v[212:215],a[88:91],a[40:43],v[212:215]
ex v198,v198
mm v[212:215],a[92:95],a[44:47],v[212:215]
s_add_u32 s17, s17, s80
s_add_u32 m0, m0, 16384
mm v[220:223],a[92:95],a[60:63],0
ex v199,v199
mm v[220:223],a[88:91],a[56:59],v[220:223]
cv v227,v198,v199
s_and_b32 m0, m0, s78
mm v[220:223],a[84:87],a[52:55],v[220:223]
ex v204,v204
mm v[220:223],a[80:83],a[48:51],v[220:223]
wt vmcnt(11)
s_barrier
mm v[64:67],a[96:99],v[232:235],v[64:67]
ex v205,v205
mm v[96:99],a[96:99],v[236:239],v[96:99]
rd a[96:99],v248 offset:8192
cv v230,v204,v205
mm v[100:103],a[100:103],v[236:239],v[100:103]
ex v206,v206
mm v[68:71],a[100:103],v[232:235],v[68:71]
rd a[100:103],v248 offset:9216
mm v[72:75],a[104:107],v[232:235],v[72:75]
ex v207,v207
mm v[104:107],a[104:107],v[236:239],v[104:107]
rd a[104:107],v248 offset:10240
cv v231,v206,v207
mm v[108:111],a[108:111],v[236:239],v[108:111]
ex v208,v208
mm v[76:79],a[108:111],v[232:235],v[76:79]
rd a[108:111],v248 offset:11264
mm v[80:83],a[112:115],v[232:235],v[80:83]
ex v209,v209
mm v[112:115],a[112:115],v[236:239],v[112:115]
rd a[112:115],v248 offset:12288
mm v[116:119],a[116:119],v[236:239],v[116:119]
ex v210,v210
mm v[84:87],a[116:119],v[232:235],v[84:87]
rd a[116:119],v248 offset:13312
mm v[88:91],a[120:123],v[232:235],v[88:91]
ex v211,v211
mm v[120:123],a[120:123],v[236:239],v[120:123]
rd a[120:123],v248 offset:14336
s_bitcmp1_b64 s[98:99], s85
mm v[124:127],a[124:127],v[236:239],v[124:127]
ex v216,v216
mm v[92:95],a[124:127],v[232:235],v[92:95]
rd a[124:127],v248 offset:15360
v_or_b32 v248, s89, v255
v_smfmac_f32_16x16x64_bf16 a[196:199], a[208:211], v[232:239], v241
wt lgkmcnt(4)
v_smfmac_f32_16x16x64_bf16 a[192:195], a[208:211], v[224:231], v241
rd a[64:67],v248 offset:0
mm v[0:3],a[96:99],v[224:227],v[0:3]
ex v217,v217
mm v[32:35],a[96:99],v[228:231],v[32:35]
rd a[68:71],v248 offset:1024
cv v232,v208,v209
mm v[36:39],a[100:103],v[228:231],v[36:39]
ex v218,v218
mm v[4:7],a[100:103],v[224:227],v[4:7]
rd a[72:75],v248 offset:2048
cv v233,v210,v211
mm v[8:11],a[104:107],v[224:227],v[8:11]
ex v219,v219
mm v[40:43],a[104:107],v[228:231],v[40:43]
rd a[76:79],v248 offset:3072
cv v236,v216,v217
mm v[44:47],a[108:111],v[228:231],v[44:47]
ex v212,v212
mm v[12:15],a[108:111],v[224:227],v[12:15]
rd a[80:83],v248 offset:4096
wt lgkmcnt(5)
mm v[16:19],a[112:115],v[224:227],v[16:19]
ex v213,v213
mm v[48:51],a[112:115],v[228:231],v[48:51]
rd a[84:87],v248 offset:5120
cv v237,v218,v219
mm v[52:55],a[116:119],v[228:231],v[52:55]
ex v214,v214
mm v[20:23],a[116:119],v[224:227],v[20:23]
rd a[88:91],v248 offset:6144
cv v234,v212,v213
mm v[24:27],a[120:123],v[224:227],v[24:27]
ex v215,v215
mm v[56:59],a[120:123],v[228:231],v[56:59]
rd a[92:95],v248 offset:7168
cv v235,v214,v215
mm v[60:63],a[124:127],v[228:231],v[60:63]
ex v220,v220
mm v[28:31],a[124:127],v[224:227],v[28:31]
s_cbranch_scc1 .La5_LA_F2E2
wt lgkmcnt(4)
mm v[192:195],a[64:67],a[0:3],0
ex v221,v221
mm v[192:195],a[68:71],a[4:7],v[192:195]
cv v238,v220,v221
s_add_u32 s85, s85, 1
mm v[192:195],a[72:75],a[8:11],v[192:195]
ex v222,v222
mm v[192:195],a[76:79],a[12:15],v[192:195]
ld v255,s[60:63],s17 offen offset:0 sc1 lds
mm v[200:203],a[76:79],a[28:31],0
ex v223,v223
mm v[200:203],a[72:75],a[24:27],v[200:203]
cv v239,v222,v223
s_add_u32 s89, s89, 16384
mm v[200:203],a[68:71],a[20:23],v[200:203]
ex v192,v192
mm v[200:203],a[64:67],a[16:19],v[200:203]
wt lgkmcnt(0)
s_and_b32 s89, s89, 0x1ffff
mm v[196:199],a[80:83],a[0:3],0
ex v193,v193
mm v[196:199],a[84:87],a[4:7],v[196:199]
cv v224,v192,v193
mm v[196:199],a[88:91],a[8:11],v[196:199]
ex v194,v194
mm v[196:199],a[92:95],a[12:15],v[196:199]
ld v255,s[60:63],s17 offen offset:1024 sc1 lds
mm v[204:207],a[92:95],a[28:31],0
ex v195,v195
mm v[204:207],a[88:91],a[24:27],v[204:207]
cv v225,v194,v195
mm v[204:207],a[84:87],a[20:23],v[204:207]
ex v200,v200
mm v[204:207],a[80:83],a[16:19],v[204:207]
ld v255,s[60:63],s17 offen offset:2048 sc1 lds
mm v[208:211],a[64:67],a[32:35],0
ex v201,v201
mm v[208:211],a[68:71],a[36:39],v[208:211]
cv v228,v200,v201
mm v[208:211],a[72:75],a[40:43],v[208:211]
ex v202,v202
mm v[208:211],a[76:79],a[44:47],v[208:211]
ld v255,s[60:63],s17 offen offset:3072 sc1 lds
mm v[216:219],a[76:79],a[60:63],0
ex v203,v203
mm v[216:219],a[72:75],a[56:59],v[216:219]
cv v229,v202,v203
mm v[216:219],a[68:71],a[52:55],v[216:219]
ex v196,v196
mm v[216:219],a[64:67],a[48:51],v[216:219]
buffer_load_dword a253, v254, s[36:39], s90 offen
mm v[212:215],a[80:83],a[32:35],0
ex v197,v197
mm v[212:215],a[84:87],a[36:39],v[212:215]
cv v226,v196,v197
s_add_u32 s90, s90, s21
mm v[212:215],a[88:91],a[40:43],v[212:215]
ex v198,v198
mm v[212:215],a[92:95],a[44:47],v[212:215]
s_add_u32 s17, s17, s80
s_add_u32 m0, m0, 16384
mm v[220:223],a[92:95],a[60:63],0
ex v199,v199
mm v[220:223],a[88:91],a[56:59],v[220:223]
cv v227,v198,v199
s_and_b32 m0, m0, s78
mm v[220:223],a[84:87],a[52:55],v[220:223]
ex v204,v204
mm v[220:223],a[80:83],a[48:51],v[220:223]
wt vmcnt(11)
s_barrier
mm v[64:67],a[96:99],v[232:235],v[64:67]
ex v205,v205
mm v[96:99],a[96:99],v[236:239],v[96:99]
rd a[96:99],v248 offset:8192
cv v230,v204,v205
mm v[100:103],a[100:103],v[236:239],v[100:103]
ex v206,v206
mm v[68:71],a[100:103],v[232:235],v[68:71]
rd a[100:103],v248 offset:9216
mm v[72:75],a[104:107],v[232:235],v[72:75]
ex v207,v207
mm v[104:107],a[104:107],v[236:239],v[104:107]
rd a[104:107],v248 offset:10240
.La5_LW6_3:
s_cbranch_execz .La5_LW6_7
cv v231,v206,v207
mm v[108:111],a[108:111],v[236:239],v[108:111]
ex v208,v208
mm v[76:79],a[108:111],v[232:235],v[76:79]
rd a[108:111],v248 offset:11264
mm v[80:83],a[112:115],v[232:235],v[80:83]
ex v209,v209
mm v[112:115],a[112:115],v[236:239],v[112:115]
rd a[112:115],v248 offset:12288
mm v[116:119],a[116:119],v[236:239],v[116:119]
ex v210,v210
mm v[84:87],a[116:119],v[232:235],v[84:87]
rd a[116:119],v248 offset:13312
mm v[88:91],a[120:123],v[232:235],v[88:91]
ex v211,v211
mm v[120:123],a[120:123],v[236:239],v[120:123]
rd a[120:123],v248 offset:14336
s_bitcmp1_b64 s[98:99], s85
mm v[124:127],a[124:127],v[236:239],v[124:127]
ex v216,v216
mm v[92:95],a[124:127],v[232:235],v[92:95]
rd a[124:127],v248 offset:15360
v_or_b32 v248, s89, v255
v_smfmac_f32_16x16x64_bf16 a[196:199], a[208:211], v[232:239], v241
wt lgkmcnt(4)
v_smfmac_f32_16x16x64_bf16 a[192:195], a[208:211], v[224:231], v241
rd a[64:67],v248 offset:0
mm v[0:3],a[96:99],v[224:227],v[0:3]
ex v217,v217
mm v[32:35],a[96:99],v[228:231],v[32:35]
rd a[68:71],v248 offset:1024
cv v232,v208,v209
mm v[36:39],a[100:103],v[228:231],v[36:39]
ex v218,v218
mm v[4:7],a[100:103],v[224:227],v[4:7]
rd a[72:75],v248 offset:2048
cv v233,v210,v211
mm v[8:11],a[104:107],v[224:227],v[8:11]
ex v219,v219
mm v[40:43],a[104:107],v[228:231],v[40:43]
rd a[76:79],v248 offset:3072
cv v236,v216,v217
mm v[44:47],a[108:111],v[228:231],v[44:47]
ex v212,v212
mm v[12:15],a[108:111],v[224:227],v[12:15]
rd a[80:83],v248 offset:4096
wt lgkmcnt(5)
mm v[16:19],a[112:115],v[224:227],v[16:19]
ex v213,v213
mm v[48:51],a[112:115],v[228:231],v[48:51]
rd a[84:87],v248 offset:5120
cv v237,v218,v219
mm v[52:55],a[116:119],v[228:231],v[52:55]
ex v214,v214
mm v[20:23],a[116:119],v[224:227],v[20:23]
rd a[88:91],v248 offset:6144
cv v234,v212,v213
mm v[24:27],a[120:123],v[224:227],v[24:27]
ex v215,v215
mm v[56:59],a[120:123],v[228:231],v[56:59]
rd a[92:95],v248 offset:7168
cv v235,v214,v215
mm v[60:63],a[124:127],v[228:231],v[60:63]
ex v220,v220
mm v[28:31],a[124:127],v[224:227],v[28:31]
s_cbranch_scc0 .La5_LA_F2
.La5_LA_F2E2:
.La5_LA_F3:
.La5_LW6_4:
s_cbranch_execz .La5_LW6_8
wt lgkmcnt(4)
mm v[192:195],a[64:67],a[0:3],0
ex v221,v221
mm v[192:195],a[68:71],a[4:7],v[192:195]
cv v238,v220,v221
s_add_u32 s89, s89, 16384
mm v[192:195],a[72:75],a[8:11],v[192:195]
ex v222,v222
mm v[192:195],a[76:79],a[12:15],v[192:195]
ld v255,s[60:63],s17 offen offset:0 sc1 lds
mm v[200:203],a[76:79],a[28:31],0
ex v223,v223
mm v[200:203],a[72:75],a[24:27],v[200:203]
cv v239,v222,v223
s_and_b32 s89, s89, 0x1ffff
mm v[200:203],a[68:71],a[20:23],v[200:203]
ex v192,v192
mm v[200:203],a[64:67],a[16:19],v[200:203]
wt lgkmcnt(0)
mm v[196:199],a[80:83],a[0:3],0
ex v193,v193
mm v[196:199],a[84:87],a[4:7],v[196:199]
cv v224,v192,v193
ar v243,a229
mm v[196:199],a[88:91],a[8:11],v[196:199]
ex v194,v194
mm v[196:199],a[92:95],a[12:15],v[196:199]
ld v255,s[60:63],s17 offen offset:1024 sc1 lds
mm v[204:207],a[92:95],a[28:31],0
ex v195,v195
mm v[204:207],a[88:91],a[24:27],v[204:207]
cv v225,v194,v195
mm v[204:207],a[84:87],a[20:23],v[204:207]
ex v200,v200
mm v[204:207],a[80:83],a[16:19],v[204:207]
ld v255,s[60:63],s17 offen offset:2048 sc1 lds
mm v[208:211],a[64:67],a[32:35],0
ex v201,v201
mm v[208:211],a[68:71],a[36:39],v[208:211]
cv v228,v200,v201
mm v[208:211],a[72:75],a[40:43],v[208:211]
ex v202,v202
mm v[208:211],a[76:79],a[44:47],v[208:211]
ld v255,s[60:63],s17 offen offset:3072 sc1 lds
mm v[216:219],a[76:79],a[60:63],0
ex v203,v203
mm v[216:219],a[72:75],a[56:59],v[216:219]
cv v229,v202,v203
s_mov_b32 s93, 0
mm v[216:219],a[68:71],a[52:55],v[216:219]
ex v196,v196
mm v[216:219],a[64:67],a[48:51],v[216:219]
ld a[128:131],v243,s[40:43],s93 offen
mm v[212:215],a[80:83],a[32:35],0
ex v197,v197
s_add_u32 s17, s17, s80
mm v[212:215],a[84:87],a[36:39],v[212:215]
cv v226,v196,v197
s_mov_b32 s93, 7168
s_add_u32 m0, m0, 16384
mm v[212:215],a[88:91],a[40:43],v[212:215]
ex v198,v198
s_and_b32 m0, m0, s78
mm v[212:215],a[92:95],a[44:47],v[212:215]
ld a[132:135],v243,s[40:43],s93 offen
mm v[220:223],a[92:95],a[60:63],0
ex v199,v199
mm v[220:223],a[88:91],a[56:59],v[220:223]
cv v227,v198,v199
mm v[220:223],a[84:87],a[52:55],v[220:223]
ex v204,v204
mm v[220:223],a[80:83],a[48:51],v[220:223]
wt vmcnt(12)
s_barrier
mm v[64:67],a[96:99],v[232:235],v[64:67]
ex v205,v205
mm v[96:99],a[96:99],v[236:239],v[96:99]
rd a[96:99],v248 offset:8192
cv v230,v204,v205
mm v[100:103],a[100:103],v[236:239],v[100:103]
ex v206,v206
mm v[68:71],a[100:103],v[232:235],v[68:71]
rd a[100:103],v248 offset:9216
mm v[72:75],a[104:107],v[232:235],v[72:75]
ex v207,v207
mm v[104:107],a[104:107],v[236:239],v[104:107]
rd a[104:107],v248 offset:10240
cv v231,v206,v207
s_mov_b32 s93, 14336
mm v[108:111],a[108:111],v[236:239],v[108:111]
ex v208,v208
mm v[76:79],a[108:111],v[232:235],v[76:79]
rd a[108:111],v248 offset:11264
ld a[136:139],v243,s[40:43],s93 offen
mm v[80:83],a[112:115],v[232:235],v[80:83]
ex v209,v209
mm v[112:115],a[112:115],v[236:239],v[112:115]
rd a[112:115],v248 offset:12288
mm v[116:119],a[116:119],v[236:239],v[116:119]
ex v210,v210
mm v[84:87],a[116:119],v[232:235],v[84:87]
rd a[116:119],v248 offset:13312
mm v[88:91],a[120:123],v[232:235],v[88:91]
ex v211,v211
mm v[120:123],a[120:123],v[236:239],v[120:123]
rd a[120:123],v248 offset:14336
mm v[124:127],a[124:127],v[236:239],v[124:127]
ex v216,v216
mm v[92:95],a[124:127],v[232:235],v[92:95]
rd a[124:127],v248 offset:15360
v_or_b32 v248, s89, v255
v_smfmac_f32_16x16x64_bf16 a[196:199], a[208:211], v[232:239], v241
wt lgkmcnt(4)
v_smfmac_f32_16x16x64_bf16 a[192:195], a[208:211], v[224:231], v241
rd a[64:67],v248 offset:0
mm v[0:3],a[96:99],v[224:227],v[0:3]
ex v217,v217
mm v[32:35],a[96:99],v[228:231],v[32:35]
rd a[68:71],v248 offset:1024
cv v232,v208,v209
mm v[36:39],a[100:103],v[228:231],v[36:39]
ex v218,v218
mm v[4:7],a[100:103],v[224:227],v[4:7]
rd a[72:75],v248 offset:2048
cv v233,v210,v211
mm v[8:11],a[104:107],v[224:227],v[8:11]
ex v219,v219
mm v[40:43],a[104:107],v[228:231],v[40:43]
rd a[76:79],v248 offset:3072
cv v236,v216,v217
mm v[44:47],a[108:111],v[228:231],v[44:47]
ex v212,v212
mm v[12:15],a[108:111],v[224:227],v[12:15]
rd a[80:83],v248 offset:4096
wt lgkmcnt(5)
mm v[16:19],a[112:115],v[224:227],v[16:19]
ex v213,v213
mm v[48:51],a[112:115],v[228:231],v[48:51]
rd a[84:87],v248 offset:5120
cv v237,v218,v219
mm v[52:55],a[116:119],v[228:231],v[52:55]
ex v214,v214
mm v[20:23],a[116:119],v[224:227],v[20:23]
rd a[88:91],v248 offset:6144
cv v234,v212,v213
mm v[24:27],a[120:123],v[224:227],v[24:27]
ex v215,v215
mm v[56:59],a[120:123],v[228:231],v[56:59]
rd a[92:95],v248 offset:7168
cv v235,v214,v215
mm v[60:63],a[124:127],v[228:231],v[60:63]
ex v220,v220
mm v[28:31],a[124:127],v[224:227],v[28:31]
wt lgkmcnt(4)
mm v[192:195],a[64:67],a[0:3],0
ex v221,v221
mm v[192:195],a[68:71],a[4:7],v[192:195]
cv v238,v220,v221
s_add_u32 s89, s89, 16384
mm v[192:195],a[72:75],a[8:11],v[192:195]
ex v222,v222
mm v[192:195],a[76:79],a[12:15],v[192:195]
ld v255,s[60:63],s17 offen offset:0 sc1 lds
mm v[200:203],a[76:79],a[28:31],0
ex v223,v223
mm v[200:203],a[72:75],a[24:27],v[200:203]
cv v239,v222,v223
s_and_b32 s89, s89, 0x1ffff
mm v[200:203],a[68:71],a[20:23],v[200:203]
ex v192,v192
mm v[200:203],a[64:67],a[16:19],v[200:203]
wt lgkmcnt(0)
mm v[196:199],a[80:83],a[0:3],0
ex v193,v193
mm v[196:199],a[84:87],a[4:7],v[196:199]
cv v224,v192,v193
mm v[196:199],a[88:91],a[8:11],v[196:199]
ex v194,v194
mm v[196:199],a[92:95],a[12:15],v[196:199]
ld v255,s[60:63],s17 offen offset:1024 sc1 lds
mm v[204:207],a[92:95],a[28:31],0
ex v195,v195
mm v[204:207],a[88:91],a[24:27],v[204:207]
cv v225,v194,v195
mm v[204:207],a[84:87],a[20:23],v[204:207]
ex v200,v200
mm v[204:207],a[80:83],a[16:19],v[204:207]
ld v255,s[60:63],s17 offen offset:2048 sc1 lds
mm v[208:211],a[64:67],a[32:35],0
ex v201,v201
mm v[208:211],a[68:71],a[36:39],v[208:211]
cv v228,v200,v201
mm v[208:211],a[72:75],a[40:43],v[208:211]
ex v202,v202
mm v[208:211],a[76:79],a[44:47],v[208:211]
ld v255,s[60:63],s17 offen offset:3072 sc1 lds
mm v[216:219],a[76:79],a[60:63],0
ex v203,v203
mm v[216:219],a[72:75],a[56:59],v[216:219]
cv v229,v202,v203
s_mov_b32 s93, 21504
mm v[216:219],a[68:71],a[52:55],v[216:219]
ex v196,v196
mm v[216:219],a[64:67],a[48:51],v[216:219]
ld a[140:143],v243,s[40:43],s93 offen
mm v[212:215],a[80:83],a[32:35],0
ex v197,v197
s_add_u32 s17, s17, s80
mm v[212:215],a[84:87],a[36:39],v[212:215]
cv v226,v196,v197
s_mov_b32 s93, 28672
s_add_u32 m0, m0, 16384
mm v[212:215],a[88:91],a[40:43],v[212:215]
ex v198,v198
s_and_b32 m0, m0, s78
mm v[212:215],a[92:95],a[44:47],v[212:215]
ld a[144:147],v243,s[40:43],s93 offen
mm v[220:223],a[92:95],a[60:63],0
ex v199,v199
mm v[220:223],a[88:91],a[56:59],v[220:223]
cv v227,v198,v199
mm v[220:223],a[84:87],a[52:55],v[220:223]
ex v204,v204
mm v[220:223],a[80:83],a[48:51],v[220:223]
wt vmcnt(14)
s_barrier
mm v[64:67],a[96:99],v[232:235],v[64:67]
ex v205,v205
mm v[96:99],a[96:99],v[236:239],v[96:99]
rd a[96:99],v248 offset:8192
cv v230,v204,v205
mm v[100:103],a[100:103],v[236:239],v[100:103]
ex v206,v206
mm v[68:71],a[100:103],v[232:235],v[68:71]
rd a[100:103],v248 offset:9216
mm v[72:75],a[104:107],v[232:235],v[72:75]
ex v207,v207
mm v[104:107],a[104:107],v[236:239],v[104:107]
rd a[104:107],v248 offset:10240
cv v231,v206,v207
s_mov_b32 s93, 35840
mm v[108:111],a[108:111],v[236:239],v[108:111]
ex v208,v208
mm v[76:79],a[108:111],v[232:235],v[76:79]
rd a[108:111],v248 offset:11264
ld a[148:151],v243,s[40:43],s93 offen
mm v[80:83],a[112:115],v[232:235],v[80:83]
ex v209,v209
mm v[112:115],a[112:115],v[236:239],v[112:115]
rd a[112:115],v248 offset:12288
mm v[116:119],a[116:119],v[236:239],v[116:119]
ex v210,v210
mm v[84:87],a[116:119],v[232:235],v[84:87]
rd a[116:119],v248 offset:13312
mm v[88:91],a[120:123],v[232:235],v[88:91]
ex v211,v211
mm v[120:123],a[120:123],v[236:239],v[120:123]
rd a[120:123],v248 offset:14336
ar v242,a231
wt vmcnt(14)
mm v[124:127],a[124:127],v[236:239],v[124:127]
ex v216,v216
mm v[92:95],a[124:127],v[232:235],v[92:95]
rd a[124:127],v248 offset:15360
v_or_b32 v248, s89, v255
v_smfmac_f32_16x16x64_bf16 a[196:199], a[208:211], v[232:239], v241
wt lgkmcnt(4)
v_smfmac_f32_16x16x64_bf16 a[192:195], a[208:211], v[224:231], v241
rd a[64:67],v248 offset:0
mm v[0:3],a[96:99],v[224:227],v[0:3]
ex v217,v217
mm v[32:35],a[96:99],v[228:231],v[32:35]
rd a[68:71],v248 offset:1024
cv v232,v208,v209
mm v[36:39],a[100:103],v[228:231],v[36:39]
ex v218,v218
mm v[4:7],a[100:103],v[224:227],v[4:7]
rd a[72:75],v248 offset:2048
cv v233,v210,v211
mm v[8:11],a[104:107],v[224:227],v[8:11]
ex v219,v219
mm v[40:43],a[104:107],v[228:231],v[40:43]
rd a[76:79],v248 offset:3072
cv v236,v216,v217
mm v[44:47],a[108:111],v[228:231],v[44:47]
ex v212,v212
mm v[12:15],a[108:111],v[224:227],v[12:15]
rd a[80:83],v248 offset:4096
wt lgkmcnt(5)
mm v[16:19],a[112:115],v[224:227],v[16:19]
ex v213,v213
mm v[48:51],a[112:115],v[228:231],v[48:51]
rd a[84:87],v248 offset:5120
cv v237,v218,v219
mm v[52:55],a[116:119],v[228:231],v[52:55]
ex v214,v214
mm v[20:23],a[116:119],v[224:227],v[20:23]
rd a[88:91],v248 offset:6144
cv v234,v212,v213
mm v[24:27],a[120:123],v[224:227],v[24:27]
ex v215,v215
mm v[56:59],a[120:123],v[228:231],v[56:59]
rd a[92:95],v248 offset:7168
cv v235,v214,v215
mm v[60:63],a[124:127],v[228:231],v[60:63]
ex v220,v220
mm v[28:31],a[124:127],v[224:227],v[28:31]
buffer_store_dwordx2 v[252:253], v242, s[64:67], s6 offen offset:0 sc1
wt lgkmcnt(4)
mm v[192:195],a[64:67],a[0:3],0
ex v221,v221
mm v[192:195],a[68:71],a[4:7],v[192:195]
cv v238,v220,v221
s_add_u32 s89, s89, 16384
mm v[192:195],a[72:75],a[8:11],v[192:195]
ex v222,v222
mm v[192:195],a[76:79],a[12:15],v[192:195]
ld v255,s[60:63],s17 offen offset:0 sc1 lds
mm v[200:203],a[76:79],a[28:31],0
ex v223,v223
mm v[200:203],a[72:75],a[24:27],v[200:203]
cv v239,v222,v223
s_and_b32 s89, s89, 0x1ffff
mm v[200:203],a[68:71],a[20:23],v[200:203]
ex v192,v192
mm v[200:203],a[64:67],a[16:19],v[200:203]
wt lgkmcnt(0)
mm v[196:199],a[80:83],a[0:3],0
ex v193,v193
mm v[196:199],a[84:87],a[4:7],v[196:199]
cv v224,v192,v193
mm v[196:199],a[88:91],a[8:11],v[196:199]
ex v194,v194
mm v[196:199],a[92:95],a[12:15],v[196:199]
ld v255,s[60:63],s17 offen offset:1024 sc1 lds
mm v[204:207],a[92:95],a[28:31],0
ex v195,v195
mm v[204:207],a[88:91],a[24:27],v[204:207]
cv v225,v194,v195
mm v[204:207],a[84:87],a[20:23],v[204:207]
ex v200,v200
mm v[204:207],a[80:83],a[16:19],v[204:207]
ld v255,s[60:63],s17 offen offset:2048 sc1 lds
mm v[208:211],a[64:67],a[32:35],0
ex v201,v201
mm v[208:211],a[68:71],a[36:39],v[208:211]
cv v228,v200,v201
mm v[208:211],a[72:75],a[40:43],v[208:211]
ex v202,v202
mm v[208:211],a[76:79],a[44:47],v[208:211]
ld v255,s[60:63],s17 offen offset:3072 sc1 lds
mm v[216:219],a[76:79],a[60:63],0
ex v203,v203
mm v[216:219],a[72:75],a[56:59],v[216:219]
cv v229,v202,v203
s_mov_b32 s93, 43008
mm v[216:219],a[68:71],a[52:55],v[216:219]
ex v196,v196
mm v[216:219],a[64:67],a[48:51],v[216:219]
ld a[152:155],v243,s[40:43],s93 offen
mm v[212:215],a[80:83],a[32:35],0
ex v197,v197
s_add_u32 s17, s17, s80
mm v[212:215],a[84:87],a[36:39],v[212:215]
cv v226,v196,v197
s_mov_b32 s93, 50176
s_add_u32 m0, m0, 16384
mm v[212:215],a[88:91],a[40:43],v[212:215]
ex v198,v198
s_and_b32 m0, m0, s78
mm v[212:215],a[92:95],a[44:47],v[212:215]
ld a[156:159],v243,s[40:43],s93 offen
mm v[220:223],a[92:95],a[60:63],0
ex v199,v199
mm v[220:223],a[88:91],a[56:59],v[220:223]
cv v227,v198,v199
mm v[220:223],a[84:87],a[52:55],v[220:223]
ex v204,v204
mm v[220:223],a[80:83],a[48:51],v[220:223]
wt vmcnt(17)
s_barrier
mm v[64:67],a[96:99],v[232:235],v[64:67]
ex v205,v205
mm v[96:99],a[96:99],v[236:239],v[96:99]
rd a[96:99],v248 offset:8192
cv v230,v204,v205
mm v[100:103],a[100:103],v[236:239],v[100:103]
ex v206,v206
mm v[68:71],a[100:103],v[232:235],v[68:71]
rd a[100:103],v248 offset:9216
mm v[72:75],a[104:107],v[232:235],v[72:75]
ex v207,v207
mm v[104:107],a[104:107],v[236:239],v[104:107]
rd a[104:107],v248 offset:10240
cv v231,v206,v207
s_mov_b32 s93, 57344
mm v[108:111],a[108:111],v[236:239],v[108:111]
ex v208,v208
mm v[76:79],a[108:111],v[232:235],v[76:79]
rd a[108:111],v248 offset:11264
ld a[160:163],v243,s[40:43],s93 offen
mm v[80:83],a[112:115],v[232:235],v[80:83]
ex v209,v209
mm v[112:115],a[112:115],v[236:239],v[112:115]
rd a[112:115],v248 offset:12288
mm v[116:119],a[116:119],v[236:239],v[116:119]
ex v210,v210
mm v[84:87],a[116:119],v[232:235],v[84:87]
rd a[116:119],v248 offset:13312
mm v[88:91],a[120:123],v[232:235],v[88:91]
ex v211,v211
mm v[120:123],a[120:123],v[236:239],v[120:123]
rd a[120:123],v248 offset:14336
mm v[124:127],a[124:127],v[236:239],v[124:127]
ex v216,v216
mm v[92:95],a[124:127],v[232:235],v[92:95]
rd a[124:127],v248 offset:15360
v_or_b32 v248, s89, v255
v_smfmac_f32_16x16x64_bf16 a[196:199], a[208:211], v[232:239], v241
wt lgkmcnt(4)
v_smfmac_f32_16x16x64_bf16 a[192:195], a[208:211], v[224:231], v241
rd a[64:67],v248 offset:0
mm v[0:3],a[96:99],v[224:227],v[0:3]
ex v217,v217
mm v[32:35],a[96:99],v[228:231],v[32:35]
rd a[68:71],v248 offset:1024
cv v232,v208,v209
mm v[36:39],a[100:103],v[228:231],v[36:39]
ex v218,v218
mm v[4:7],a[100:103],v[224:227],v[4:7]
rd a[72:75],v248 offset:2048
cv v233,v210,v211
mm v[8:11],a[104:107],v[224:227],v[8:11]
ex v219,v219
mm v[40:43],a[104:107],v[228:231],v[40:43]
rd a[76:79],v248 offset:3072
cv v236,v216,v217
mm v[44:47],a[108:111],v[228:231],v[44:47]
ex v212,v212
mm v[12:15],a[108:111],v[224:227],v[12:15]
rd a[80:83],v248 offset:4096
wt lgkmcnt(5)
mm v[16:19],a[112:115],v[224:227],v[16:19]
ex v213,v213
mm v[48:51],a[112:115],v[228:231],v[48:51]
rd a[84:87],v248 offset:5120
cv v237,v218,v219
mm v[52:55],a[116:119],v[228:231],v[52:55]
ex v214,v214
mm v[20:23],a[116:119],v[224:227],v[20:23]
rd a[88:91],v248 offset:6144
cv v234,v212,v213
mm v[24:27],a[120:123],v[224:227],v[24:27]
ex v215,v215
mm v[56:59],a[120:123],v[228:231],v[56:59]
rd a[92:95],v248 offset:7168
cv v235,v214,v215
mm v[60:63],a[124:127],v[228:231],v[60:63]
ex v220,v220
mm v[28:31],a[124:127],v[224:227],v[28:31]
wt lgkmcnt(4)
mm v[192:195],a[64:67],a[0:3],0
ex v221,v221
mm v[192:195],a[68:71],a[4:7],v[192:195]
cv v238,v220,v221
ar v240,a228
mm v[192:195],a[72:75],a[8:11],v[192:195]
ex v222,v222
mm v[192:195],a[76:79],a[12:15],v[192:195]
s_add_u32 m0, s83, 16896
s_lshl_b32 s17, s72, 18
mm v[200:203],a[76:79],a[28:31],0
ex v223,v223
mm v[200:203],a[72:75],a[24:27],v[200:203]
cv v239,v222,v223
s_add_u32 s89, s89, 16384
mm v[200:203],a[68:71],a[20:23],v[200:203]
ex v192,v192
mm v[200:203],a[64:67],a[16:19],v[200:203]
wt lgkmcnt(0)
s_and_b32 s89, s89, 0x1ffff
mm v[196:199],a[80:83],a[0:3],0
ex v193,v193
mm v[196:199],a[84:87],a[4:7],v[196:199]
cv v224,v192,v193
mm v[196:199],a[88:91],a[8:11],v[196:199]
ex v194,v194
mm v[196:199],a[92:95],a[12:15],v[196:199]
ld v240,s[56:59],s17 offen lds
mm v[204:207],a[92:95],a[28:31],0
ex v195,v195
s_add_u32 s17, s17, 32768
mm v[204:207],a[88:91],a[24:27],v[204:207]
cv v225,v194,v195
s_add_u32 m0, m0, 1024
mm v[204:207],a[84:87],a[20:23],v[204:207]
ex v200,v200
mm v[204:207],a[80:83],a[16:19],v[204:207]
ld v240,s[56:59],s17 offen lds
mm v[208:211],a[64:67],a[32:35],0
ex v201,v201
s_add_u32 s17, s17, 4294942720
mm v[208:211],a[68:71],a[36:39],v[208:211]
cv v228,v200,v201
s_add_u32 m0, m0, 1152
mm v[208:211],a[72:75],a[40:43],v[208:211]
ex v202,v202
mm v[208:211],a[76:79],a[44:47],v[208:211]
ld v240,s[56:59],s17 offen lds
mm v[216:219],a[76:79],a[60:63],0
ex v203,v203
s_add_u32 s17, s17, 32768
mm v[216:219],a[72:75],a[56:59],v[216:219]
cv v229,v202,v203
s_add_u32 m0, m0, 1024
mm v[216:219],a[68:71],a[52:55],v[216:219]
ex v196,v196
mm v[216:219],a[64:67],a[48:51],v[216:219]
ld v240,s[56:59],s17 offen lds
mm v[212:215],a[80:83],a[32:35],0
ex v197,v197
s_add_u32 s17, s17, 90112
mm v[212:215],a[84:87],a[36:39],v[212:215]
cv v226,v196,v197
s_mov_b32 s93, 64512
.La5_LW6_5:
s_cbranch_execz .La5_LW6_R0
s_add_u32 m0, s83, 33792
mm v[212:215],a[88:91],a[40:43],v[212:215]
ex v198,v198
mm v[212:215],a[92:95],a[44:47],v[212:215]
ld a[164:167],v243,s[40:43],s93 offen
mm v[220:223],a[92:95],a[60:63],0
ex v199,v199
mm v[220:223],a[88:91],a[56:59],v[220:223]
cv v227,v198,v199
mm v[220:223],a[84:87],a[52:55],v[220:223]
ex v204,v204
mm v[220:223],a[80:83],a[48:51],v[220:223]
wt vmcnt(16)
s_barrier
mm v[64:67],a[96:99],v[232:235],v[64:67]
ex v205,v205
mm v[96:99],a[96:99],v[236:239],v[96:99]
rd a[96:99],v248 offset:8192
cv v230,v204,v205
mm v[100:103],a[100:103],v[236:239],v[100:103]
ex v206,v206
mm v[68:71],a[100:103],v[232:235],v[68:71]
rd a[100:103],v248 offset:9216
mm v[72:75],a[104:107],v[232:235],v[72:75]
ex v207,v207
mm v[104:107],a[104:107],v[236:239],v[104:107]
rd a[104:107],v248 offset:10240
cv v231,v206,v207
s_mov_b32 s93, 71680
mm v[108:111],a[108:111],v[236:239],v[108:111]
ex v208,v208
mm v[76:79],a[108:111],v[232:235],v[76:79]
rd a[108:111],v248 offset:11264
ld a[168:171],v243,s[40:43],s93 offen
mm v[80:83],a[112:115],v[232:235],v[80:83]
ex v209,v209
mm v[112:115],a[112:115],v[236:239],v[112:115]
rd a[112:115],v248 offset:12288
s_mov_b32 s93, 78848
mm v[116:119],a[116:119],v[236:239],v[116:119]
ex v210,v210
mm v[84:87],a[116:119],v[232:235],v[84:87]
rd a[116:119],v248 offset:13312
ld a[172:175],v243,s[40:43],s93 offen
mm v[88:91],a[120:123],v[232:235],v[88:91]
ex v211,v211
mm v[120:123],a[120:123],v[236:239],v[120:123]
rd a[120:123],v248 offset:14336
mm v[124:127],a[124:127],v[236:239],v[124:127]
ex v216,v216
mm v[92:95],a[124:127],v[232:235],v[92:95]
rd a[124:127],v248 offset:15360
v_or_b32 v248, s89, v255
v_smfmac_f32_16x16x64_bf16 a[196:199], a[208:211], v[232:239], v241
wt lgkmcnt(4)
v_smfmac_f32_16x16x64_bf16 a[192:195], a[208:211], v[224:231], v241
rd a[64:67],v248 offset:0
mm v[0:3],a[96:99],v[224:227],v[0:3]
ex v217,v217
mm v[32:35],a[96:99],v[228:231],v[32:35]
rd a[68:71],v248 offset:1024
cv v232,v208,v209
mm v[36:39],a[100:103],v[228:231],v[36:39]
ex v218,v218
mm v[4:7],a[100:103],v[224:227],v[4:7]
rd a[72:75],v248 offset:2048
cv v233,v210,v211
mm v[8:11],a[104:107],v[224:227],v[8:11]
ex v219,v219
mm v[40:43],a[104:107],v[228:231],v[40:43]
rd a[76:79],v248 offset:3072
cv v236,v216,v217
mm v[44:47],a[108:111],v[228:231],v[44:47]
ex v212,v212
mm v[12:15],a[108:111],v[224:227],v[12:15]
rd a[80:83],v248 offset:4096
wt lgkmcnt(5)
mm v[16:19],a[112:115],v[224:227],v[16:19]
ex v213,v213
mm v[48:51],a[112:115],v[228:231],v[48:51]
rd a[84:87],v248 offset:5120
cv v237,v218,v219
mm v[52:55],a[116:119],v[228:231],v[52:55]
ex v214,v214
mm v[20:23],a[116:119],v[224:227],v[20:23]
rd a[88:91],v248 offset:6144
cv v234,v212,v213
mm v[24:27],a[120:123],v[224:227],v[24:27]
ex v215,v215
mm v[56:59],a[120:123],v[228:231],v[56:59]
rd a[92:95],v248 offset:7168
cv v235,v214,v215
mm v[60:63],a[124:127],v[228:231],v[60:63]
ex v220,v220
mm v[28:31],a[124:127],v[224:227],v[28:31]
wt lgkmcnt(4)
mm v[192:195],a[64:67],a[0:3],0
ex v221,v221
mm v[192:195],a[68:71],a[4:7],v[192:195]
cv v238,v220,v221
s_add_u32 s89, s89, 16384
mm v[192:195],a[72:75],a[8:11],v[192:195]
ex v222,v222
mm v[192:195],a[76:79],a[12:15],v[192:195]
ld v240,s[56:59],s17 offen lds
mm v[200:203],a[76:79],a[28:31],0
ex v223,v223
s_add_u32 s17, s17, 32768
mm v[200:203],a[72:75],a[24:27],v[200:203]
cv v239,v222,v223
s_add_u32 m0, m0, 1024
mm v[200:203],a[68:71],a[20:23],v[200:203]
ex v192,v192
mm v[200:203],a[64:67],a[16:19],v[200:203]
wt lgkmcnt(0)
s_and_b32 s89, s89, 0x1ffff
mm v[196:199],a[80:83],a[0:3],0
ex v193,v193
mm v[196:199],a[84:87],a[4:7],v[196:199]
cv v224,v192,v193
mm v[196:199],a[88:91],a[8:11],v[196:199]
ex v194,v194
mm v[196:199],a[92:95],a[12:15],v[196:199]
ld v240,s[56:59],s17 offen lds
mm v[204:207],a[92:95],a[28:31],0
ex v195,v195
s_add_u32 s17, s17, 4294942720
mm v[204:207],a[88:91],a[24:27],v[204:207]
cv v225,v194,v195
s_add_u32 m0, m0, 1152
mm v[204:207],a[84:87],a[20:23],v[204:207]
ex v200,v200
mm v[204:207],a[80:83],a[16:19],v[204:207]
ld v240,s[56:59],s17 offen lds
mm v[208:211],a[64:67],a[32:35],0
ex v201,v201
s_add_u32 s17, s17, 32768
mm v[208:211],a[68:71],a[36:39],v[208:211]
cv v228,v200,v201
s_add_u32 m0, m0, 1024
mm v[208:211],a[72:75],a[40:43],v[208:211]
ex v202,v202
mm v[208:211],a[76:79],a[44:47],v[208:211]
ld v240,s[56:59],s17 offen lds
mm v[216:219],a[76:79],a[60:63],0
ex v203,v203
mm v[216:219],a[72:75],a[56:59],v[216:219]
cv v229,v202,v203
s_mov_b32 s93, 86016
mm v[216:219],a[68:71],a[52:55],v[216:219]
ex v196,v196
mm v[216:219],a[64:67],a[48:51],v[216:219]
ld a[176:179],v243,s[40:43],s93 offen
mm v[212:215],a[80:83],a[32:35],0
ex v197,v197
s_bfe_u32 s17, s4, 262148
mm v[212:215],a[84:87],a[36:39],v[212:215]
cv v226,v196,v197
s_mov_b32 s93, 93184
s_xor_b32 s17, s17, s72
mm v[212:215],a[88:91],a[40:43],v[212:215]
ex v198,v198
s_lshl_b32 s17, s17, 18
mm v[212:215],a[92:95],a[44:47],v[212:215]
ld a[180:183],v243,s[40:43],s93 offen
s_add_u32 m0, s83, 50688
mm v[220:223],a[92:95],a[60:63],0
ex v199,v199
mm v[220:223],a[88:91],a[56:59],v[220:223]
cv v227,v198,v199
mm v[220:223],a[84:87],a[52:55],v[220:223]
ex v204,v204
mm v[220:223],a[80:83],a[48:51],v[220:223]
wt vmcnt(16)
s_barrier
mm v[64:67],a[96:99],v[232:235],v[64:67]
ex v205,v205
mm v[96:99],a[96:99],v[236:239],v[96:99]
rd a[96:99],v248 offset:8192
cv v230,v204,v205
mm v[100:103],a[100:103],v[236:239],v[100:103]
ex v206,v206
mm v[68:71],a[100:103],v[232:235],v[68:71]
rd a[100:103],v248 offset:9216
mm v[72:75],a[104:107],v[232:235],v[72:75]
ex v207,v207
mm v[104:107],a[104:107],v[236:239],v[104:107]
rd a[104:107],v248 offset:10240
cv v231,v206,v207
s_mov_b32 s93, 100352
mm v[108:111],a[108:111],v[236:239],v[108:111]
ex v208,v208
mm v[76:79],a[108:111],v[232:235],v[76:79]
rd a[108:111],v248 offset:11264
ld a[184:187],v243,s[40:43],s93 offen
mm v[80:83],a[112:115],v[232:235],v[80:83]
ex v209,v209
mm v[112:115],a[112:115],v[236:239],v[112:115]
rd a[112:115],v248 offset:12288
mm v[116:119],a[116:119],v[236:239],v[116:119]
ex v210,v210
mm v[84:87],a[116:119],v[232:235],v[84:87]
rd a[116:119],v248 offset:13312
mm v[88:91],a[120:123],v[232:235],v[88:91]
ex v211,v211
mm v[120:123],a[120:123],v[236:239],v[120:123]
rd a[120:123],v248 offset:14336
mm v[124:127],a[124:127],v[236:239],v[124:127]
ex v216,v216
mm v[92:95],a[124:127],v[232:235],v[92:95]
rd a[124:127],v248 offset:15360
v_or_b32 v248, s89, v255
v_smfmac_f32_16x16x64_bf16 a[196:199], a[208:211], v[232:239], v241
wt lgkmcnt(4)
v_smfmac_f32_16x16x64_bf16 a[192:195], a[208:211], v[224:231], v241
rd a[64:67],v248 offset:0
mm v[0:3],a[96:99],v[224:227],v[0:3]
ex v217,v217
mm v[32:35],a[96:99],v[228:231],v[32:35]
rd a[68:71],v248 offset:1024
cv v232,v208,v209
mm v[36:39],a[100:103],v[228:231],v[36:39]
ex v218,v218
mm v[4:7],a[100:103],v[224:227],v[4:7]
rd a[72:75],v248 offset:2048
cv v233,v210,v211
mm v[8:11],a[104:107],v[224:227],v[8:11]
ex v219,v219
mm v[40:43],a[104:107],v[228:231],v[40:43]
rd a[76:79],v248 offset:3072
cv v236,v216,v217
mm v[44:47],a[108:111],v[228:231],v[44:47]
ex v212,v212
mm v[12:15],a[108:111],v[224:227],v[12:15]
rd a[80:83],v248 offset:4096
wt lgkmcnt(5)
mm v[16:19],a[112:115],v[224:227],v[16:19]
ex v213,v213
mm v[48:51],a[112:115],v[228:231],v[48:51]
rd a[84:87],v248 offset:5120
cv v237,v218,v219
mm v[52:55],a[116:119],v[228:231],v[52:55]
ex v214,v214
mm v[20:23],a[116:119],v[224:227],v[20:23]
rd a[88:91],v248 offset:6144
cv v234,v212,v213
mm v[24:27],a[120:123],v[224:227],v[24:27]
ex v215,v215
mm v[56:59],a[120:123],v[228:231],v[56:59]
rd a[92:95],v248 offset:7168
cv v235,v214,v215
mm v[60:63],a[124:127],v[228:231],v[60:63]
ex v220,v220
mm v[28:31],a[124:127],v[224:227],v[28:31]
wt lgkmcnt(4)
mm v[192:195],a[64:67],a[0:3],0
ex v221,v221
mm v[192:195],a[68:71],a[4:7],v[192:195]
cv v238,v220,v221
mm v[192:195],a[72:75],a[8:11],v[192:195]
ex v222,v222
mm v[192:195],a[76:79],a[12:15],v[192:195]
ld v240,s[56:59],s17 offen lds
mm v[200:203],a[76:79],a[28:31],0
ex v223,v223
s_add_u32 s17, s17, 32768
mm v[200:203],a[72:75],a[24:27],v[200:203]
cv v239,v222,v223
s_add_u32 m0, m0, 1024
mm v[200:203],a[68:71],a[20:23],v[200:203]
ex v192,v192
mm v[200:203],a[64:67],a[16:19],v[200:203]
wt lgkmcnt(0)
mm v[196:199],a[80:83],a[0:3],0
ex v193,v193
mm v[196:199],a[84:87],a[4:7],v[196:199]
cv v224,v192,v193
mm v[196:199],a[88:91],a[8:11],v[196:199]
ex v194,v194
mm v[196:199],a[92:95],a[12:15],v[196:199]
ld v240,s[56:59],s17 offen lds
mm v[204:207],a[92:95],a[28:31],0
ex v195,v195
s_add_u32 s17, s17, 4294942720
mm v[204:207],a[88:91],a[24:27],v[204:207]
cv v225,v194,v195
s_add_u32 m0, m0, 1152
mm v[204:207],a[84:87],a[20:23],v[204:207]
ex v200,v200
mm v[204:207],a[80:83],a[16:19],v[204:207]
ld v240,s[56:59],s17 offen lds
mm v[208:211],a[64:67],a[32:35],0
ex v201,v201
s_add_u32 s17, s17, 32768
mm v[208:211],a[68:71],a[36:39],v[208:211]
cv v228,v200,v201
s_add_u32 m0, m0, 1024
mm v[208:211],a[72:75],a[40:43],v[208:211]
ex v202,v202
mm v[208:211],a[76:79],a[44:47],v[208:211]
ld v240,s[56:59],s17 offen lds
mm v[216:219],a[76:79],a[60:63],0
ex v203,v203
mm v[216:219],a[72:75],a[56:59],v[216:219]
cv v229,v202,v203
s_mov_b32 s93, 107520
mm v[216:219],a[68:71],a[52:55],v[216:219]
ex v196,v196
mm v[216:219],a[64:67],a[48:51],v[216:219]
ld a[188:191],v243,s[40:43],s93 offen
mm v[212:215],a[80:83],a[32:35],0
ex v197,v197
mm v[212:215],a[84:87],a[36:39],v[212:215]
cv v226,v196,v197
mm v[212:215],a[88:91],a[40:43],v[212:215]
ex v198,v198
mm v[212:215],a[92:95],a[44:47],v[212:215]
mm v[220:223],a[92:95],a[60:63],0
ex v199,v199
mm v[220:223],a[88:91],a[56:59],v[220:223]
cv v227,v198,v199
mm v[220:223],a[84:87],a[52:55],v[220:223]
ex v204,v204
mm v[220:223],a[80:83],a[48:51],v[220:223]
mm v[64:67],a[96:99],v[232:235],v[64:67]
ex v205,v205
mm v[96:99],a[96:99],v[236:239],v[96:99]
rd a[96:99],v248 offset:8192
cv v230,v204,v205
mm v[100:103],a[100:103],v[236:239],v[100:103]
ex v206,v206
mm v[68:71],a[100:103],v[232:235],v[68:71]
rd a[100:103],v248 offset:9216
mm v[72:75],a[104:107],v[232:235],v[72:75]
ex v207,v207
mm v[104:107],a[104:107],v[236:239],v[104:107]
rd a[104:107],v248 offset:10240
cv v231,v206,v207
mm v[108:111],a[108:111],v[236:239],v[108:111]
ex v208,v208
mm v[76:79],a[108:111],v[232:235],v[76:79]
rd a[108:111],v248 offset:11264
mm v[80:83],a[112:115],v[232:235],v[80:83]
ex v209,v209
mm v[112:115],a[112:115],v[236:239],v[112:115]
rd a[112:115],v248 offset:12288
mm v[116:119],a[116:119],v[236:239],v[116:119]
ex v210,v210
mm v[84:87],a[116:119],v[232:235],v[84:87]
rd a[116:119],v248 offset:13312
mm v[88:91],a[120:123],v[232:235],v[88:91]
ex v211,v211
mm v[120:123],a[120:123],v[236:239],v[120:123]
rd a[120:123],v248 offset:14336
mm v[124:127],a[124:127],v[236:239],v[124:127]
ex v216,v216
mm v[92:95],a[124:127],v[232:235],v[92:95]
rd a[124:127],v248 offset:15360
v_smfmac_f32_16x16x64_bf16 a[196:199], a[208:211], v[232:239], v241
wt lgkmcnt(4)
v_smfmac_f32_16x16x64_bf16 a[192:195], a[208:211], v[224:231], v241
mm v[0:3],a[96:99],v[224:227],v[0:3]
ex v217,v217
mm v[32:35],a[96:99],v[228:231],v[32:35]
cv v232,v208,v209
mm v[36:39],a[100:103],v[228:231],v[36:39]
ex v218,v218
mm v[4:7],a[100:103],v[224:227],v[4:7]
cv v233,v210,v211
mm v[8:11],a[104:107],v[224:227],v[8:11]
ex v219,v219
mm v[40:43],a[104:107],v[228:231],v[40:43]
cv v236,v216,v217
mm v[44:47],a[108:111],v[228:231],v[44:47]
ex v212,v212
mm v[12:15],a[108:111],v[224:227],v[12:15]
wt lgkmcnt(0)
mm v[16:19],a[112:115],v[224:227],v[16:19]
ex v213,v213
mm v[48:51],a[112:115],v[228:231],v[48:51]
cv v237,v218,v219
mm v[52:55],a[116:119],v[228:231],v[52:55]
ex v214,v214
mm v[20:23],a[116:119],v[224:227],v[20:23]
cv v234,v212,v213
mm v[24:27],a[120:123],v[224:227],v[24:27]
ex v215,v215
mm v[56:59],a[120:123],v[228:231],v[56:59]
cv v235,v214,v215
mm v[60:63],a[124:127],v[228:231],v[60:63]
ex v220,v220
mm v[28:31],a[124:127],v[224:227],v[28:31]
; ---- helper: partial O and l go to the partner
.La5_LA_DP:
ex v221,v221
ex v222,v222
ex v223,v223
cv v238,v220,v221
s_nop 0
cv v239,v222,v223
s_nop 1
v_smfmac_f32_16x16x64_bf16 a[196:199], a[208:211], v[232:239], v241
cv v0,v0,v1
cv v1,v2,v3
cv v2,v4,v5
cv v3,v6,v7
st v[0:3],v255,s[68:71],0 offen offset:0 sc1
mm v[64:67],a[96:99],v[232:235],v[64:67]
cv v8,v8,v9
cv v9,v10,v11
cv v10,v12,v13
cv v11,v14,v15
st v[8:11],v255,s[68:71],0 offen offset:1024 sc1
mm v[96:99],a[96:99],v[236:239],v[96:99]
ar v240,a224
v_xor_b32 v241, 32, v240
cv v16,v16,v17
cv v17,v18,v19
cv v18,v20,v21
cv v19,v22,v23
mm v[100:103],a[100:103],v[236:239],v[100:103]
v_xor_b32 v242, 64, v240
v_xor_b32 v243, 96, v240
st v[16:19],v255,s[68:71],0 offen offset:2048 sc1
cv v24,v24,v25
cv v25,v26,v27
mm v[68:71],a[100:103],v[232:235],v[68:71]
cv v26,v28,v29
cv v27,v30,v31
st v[24:27],v255,s[68:71],0 offen offset:3072 sc1
cv v32,v32,v33
cv v33,v34,v35
mm v[72:75],a[104:107],v[232:235],v[72:75]
cv v34,v36,v37
cv v35,v38,v39
st v[32:35],v255,s[68:71],s95 offen offset:0 sc1
cv v40,v40,v41
cv v41,v42,v43
mm v[104:107],a[104:107],v[236:239],v[104:107]
wt vmcnt(13)
s_barrier
cv v42,v44,v45
cv v43,v46,v47
st v[40:43],v255,s[68:71],s95 offen offset:1024 sc1
mm v[108:111],a[108:111],v[236:239],v[108:111]
rt v[192:193],v240 offset:16896
rt v[194:195],v240 offset:17920
cv v48,v48,v49
cv v49,v50,v51
cv v50,v52,v53
cv v51,v54,v55
mm v[76:79],a[108:111],v[232:235],v[76:79]
rt v[196:197],v240 offset:25344
rt v[198:199],v240 offset:26368
st v[48:51],v255,s[68:71],s95 offen offset:2048 sc1
cv v56,v56,v57
cv v57,v58,v59
mm v[80:83],a[112:115],v[232:235],v[80:83]
rt v[200:201],v241 offset:16896
rt v[202:203],v241 offset:17920
cv v58,v60,v61
cv v59,v62,v63
st v[56:59],v255,s[68:71],s95 offen offset:3072 sc1
mm v[112:115],a[112:115],v[236:239],v[112:115]
rt v[204:205],v241 offset:25344
rt v[206:207],v241 offset:26368
mm v[116:119],a[116:119],v[236:239],v[116:119]
rt v[208:209],v242 offset:16896
rt v[210:211],v242 offset:17920
mm v[84:87],a[116:119],v[232:235],v[84:87]
rt v[212:213],v242 offset:25344
rt v[214:215],v242 offset:26368
mm v[88:91],a[120:123],v[232:235],v[88:91]
rt v[216:217],v243 offset:16896
rt v[218:219],v243 offset:17920
mm v[120:123],a[120:123],v[236:239],v[120:123]
rt v[220:221],v243 offset:25344
rt v[222:223],v243 offset:26368
mm v[124:127],a[124:127],v[236:239],v[124:127]
mm v[92:95],a[124:127],v[232:235],v[92:95]
wt lgkmcnt(12)
mm v[0:3],v[192:195],v[128:131],0
mm v[0:3],v[196:199],v[132:135],v[0:3]
mm v[32:35],v[196:199],v[148:151],0
wt lgkmcnt(8)
ar v224,a192
ar v228,a193
ar v225,a194
ar v229,a195
ar v226,a196
mm v[32:35],v[192:195],v[144:147],v[32:35]
rt v[196:197],v240 offset:17024
rt v[198:199],v240 offset:18048
ar v230,a197
ar v227,a198
ar v231,a199
v_add_f32 v224, v224, v228
mm v[36:39],v[200:203],v[144:147],0
v_add_f32 v225, v225, v229
v_add_f32 v226, v226, v230
v_add_f32 v227, v227, v231
s_mov_b32 s92, 16384
st v[224:227],v255,s[68:71],s92 offen sc1
mm v[36:39],v[204:207],v[148:151],v[36:39]
rt v[192:193],v240 offset:25472
rt v[194:195],v240 offset:26496
cv v64,v64,v65
cv v65,v66,v67
cv v66,v68,v69
cv v67,v70,v71
mm v[4:7],v[204:207],v[132:135],0
wt lgkmcnt(4)
st v[64:67],v255,s[68:71],s96 offen offset:0 sc1
cv v72,v72,v73
cv v73,v74,v75
cv v74,v76,v77
mm v[4:7],v[200:203],v[128:131],v[4:7]
rt v[204:205],v241 offset:17024
rt v[206:207],v241 offset:18048
cv v75,v78,v79
st v[72:75],v255,s[68:71],s96 offen offset:1024 sc1
cv v80,v80,v81
mm v[8:11],v[208:211],v[128:131],0
cv v81,v82,v83
cv v82,v84,v85
cv v83,v86,v87
st v[80:83],v255,s[68:71],s96 offen offset:2048 sc1
cv v88,v88,v89
mm v[8:11],v[212:215],v[132:135],v[8:11]
rt v[200:201],v241 offset:25472
rt v[202:203],v241 offset:26496
cv v89,v90,v91
cv v90,v92,v93
cv v91,v94,v95
st v[88:91],v255,s[68:71],s96 offen offset:3072 sc1
mm v[40:43],v[212:215],v[148:151],0
cv v96,v96,v97
cv v97,v98,v99
cv v98,v100,v101
cv v99,v102,v103
st v[96:99],v255,s[68:71],s97 offen offset:0 sc1
mm v[40:43],v[208:211],v[144:147],v[40:43]
rt v[212:213],v242 offset:17024
rt v[214:215],v242 offset:18048
cv v104,v104,v105
cv v105,v106,v107
cv v106,v108,v109
cv v107,v110,v111
mm v[44:47],v[216:219],v[144:147],0
st v[104:107],v255,s[68:71],s97 offen offset:1024 sc1
cv v112,v112,v113
cv v113,v114,v115
cv v114,v116,v117
cv v115,v118,v119
mm v[44:47],v[220:223],v[148:151],v[44:47]
rt v[208:209],v242 offset:25472
rt v[210:211],v242 offset:26496
st v[112:115],v255,s[68:71],s97 offen offset:2048 sc1
cv v120,v120,v121
cv v121,v122,v123
mm v[12:15],v[220:223],v[132:135],0
wt lgkmcnt(4)
cv v122,v124,v125
cv v123,v126,v127
st v[120:123],v255,s[68:71],s97 offen offset:3072 sc1
v_mov_b32 v249, 0
mm v[12:15],v[216:219],v[128:131],v[12:15]
rt v[220:221],v243 offset:17024
rt v[222:223],v243 offset:18048
s_add_u32 s6, s88, 393216
mm v[16:19],v[196:199],v[128:131],0
ar v224,a228
v_add_u32 v225, 0x8000, v224
.La5_LW6_6:
s_cbranch_execz .La5_LW6_R0
v_add_u32 v226, 0x2000, v224
v_add_u32 v227, 0xa000, v224
mm v[16:19],v[192:195],v[132:135],v[16:19]
rt v[216:217],v243 offset:25472
rt v[218:219],v243 offset:26496
ar v248,a225
ar v228,a229
mm v[48:51],v[192:195],v[148:151],0
ar v229,a230
mm v[48:51],v[196:199],v[144:147],v[48:51]
rt v[192:193],v240 offset:33792
rt v[194:195],v240 offset:34816
mm v[52:55],v[204:207],v[144:147],0
mm v[52:55],v[200:203],v[148:151],v[52:55]
rt v[196:197],v240 offset:42240
rt v[198:199],v240 offset:43264
mm v[20:23],v[200:203],v[132:135],0
wt lgkmcnt(4)
mm v[20:23],v[204:207],v[128:131],v[20:23]
rt v[200:201],v241 offset:33792
rt v[202:203],v241 offset:34816
mm v[24:27],v[212:215],v[128:131],0
s_bitcmp1_b32 2863311530, s2
s_nop 0
s_cbranch_scc1 .La5_LO_VC2
s_getpc_b64 s[24:25]
.La5_LW3_P0:
s_add_u32 s24, s24, .La5_LW3_R0-.La5_LW3_P0
s_addc_u32 s25, s25, 0
s_mov_b64 exec, 0
s_cmp_eq_u32 s3, 0
s_cbranch_scc1 .La5_LW3_0
s_cmp_eq_u32 s3, 1
s_cbranch_scc1 .La5_LW3_1
s_cmp_eq_u32 s3, 2
s_cbranch_scc1 .La5_LW3_2
s_branch .La5_LW3_3
.La5_LW3_R0:
s_mov_b64 exec, -1
.La5_LO_VE2:
wt vmcnt(0)
buffer_store_dwordx2 v[252:253], v249, s[64:67], s6 offen offset:0 sc1
mm v[24:27],v[208:211],v[132:135],v[24:27]
rt v[204:205],v241 offset:42240
rt v[206:207],v241 offset:43264
mm v[56:59],v[208:211],v[148:151],0
s_mov_b32 s93, 3670016
ld a[192:195],v228,s[40:43],s93 offen
mm v[56:59],v[212:215],v[144:147],v[56:59]
rt v[208:209],v242 offset:33792
rt v[210:211],v242 offset:34816
mm v[60:63],v[220:223],v[144:147],0
s_mov_b32 s93, 3677184
ld a[196:199],v228,s[40:43],s93 offen
mm v[60:63],v[216:219],v[148:151],v[60:63]
rt v[212:213],v242 offset:42240
rt v[214:215],v242 offset:43264
mm v[28:31],v[216:219],v[132:135],0
wt lgkmcnt(4)
s_mov_b32 s93, 3684352
ld a[200:203],v228,s[40:43],s93 offen
mm v[28:31],v[220:223],v[128:131],v[28:31]
rt v[216:217],v243 offset:33792
rt v[218:219],v243 offset:34816
mm v[0:3],v[192:195],v[136:139],v[0:3]
s_mov_b32 s93, 3691520
ld a[204:207],v228,s[40:43],s93 offen
mm v[0:3],v[196:199],v[140:143],v[0:3]
rt v[220:221],v243 offset:42240
rt v[222:223],v243 offset:43264
mm v[32:35],v[196:199],v[156:159],v[32:35]
s_mov_b32 s93, 3698688
ld a[208:211],v228,s[40:43],s93 offen
mm v[32:35],v[192:195],v[152:155],v[32:35]
rt v[196:197],v240 offset:33920
rt v[198:199],v240 offset:34944
mm v[36:39],v[200:203],v[152:155],v[36:39]
s_mov_b32 s93, 3705856
ld a[212:215],v228,s[40:43],s93 offen
mm v[36:39],v[204:207],v[156:159],v[36:39]
rt v[192:193],v240 offset:42368
rt v[194:195],v240 offset:43392
mm v[4:7],v[204:207],v[140:143],v[4:7]
wt lgkmcnt(4)
s_mov_b32 s93, 3713024
ld a[216:219],v228,s[40:43],s93 offen
mm v[4:7],v[200:203],v[136:139],v[4:7]
rt v[204:205],v241 offset:33920
rt v[206:207],v241 offset:34944
mm v[8:11],v[208:211],v[136:139],v[8:11]
s_mov_b32 s93, 3720192
ld a[220:223],v228,s[40:43],s93 offen
mm v[8:11],v[212:215],v[140:143],v[8:11]
rt v[200:201],v241 offset:42368
rt v[202:203],v241 offset:43392
mm v[40:43],v[212:215],v[156:159],v[40:43]
s_mov_b32 s93, 3727360
ld a[224:227],v228,s[40:43],s93 offen
mm v[40:43],v[208:211],v[152:155],v[40:43]
rt v[212:213],v242 offset:33920
rt v[214:215],v242 offset:34944
mm v[44:47],v[216:219],v[152:155],v[44:47]
s_mov_b32 s93, 3734528
ld a[228:231],v228,s[40:43],s93 offen
mm v[44:47],v[220:223],v[156:159],v[44:47]
rt v[208:209],v242 offset:42368
rt v[210:211],v242 offset:43392
mm v[12:15],v[220:223],v[140:143],v[12:15]
wt lgkmcnt(4)
s_mov_b32 s93, 3741696
ld a[232:235],v228,s[40:43],s93 offen
mm v[12:15],v[216:219],v[136:139],v[12:15]
rt v[220:221],v243 offset:33920
rt v[222:223],v243 offset:34944
mm v[16:19],v[196:199],v[136:139],v[16:19]
s_mov_b32 s93, 3748864
ld a[236:239],v228,s[40:43],s93 offen
mm v[16:19],v[192:195],v[140:143],v[16:19]
rt v[216:217],v243 offset:42368
rt v[218:219],v243 offset:43392
mm v[48:51],v[192:195],v[156:159],v[48:51]
s_mov_b32 s93, 3756032
ld a[240:243],v228,s[40:43],s93 offen
mm v[48:51],v[196:199],v[152:155],v[48:51]
rt v[192:193],v240 offset:16896
rt v[194:195],v240 offset:17920
mm v[52:55],v[204:207],v[152:155],v[52:55]
s_mov_b32 s93, 3763200
ld a[244:247],v228,s[40:43],s93 offen
mm v[52:55],v[200:203],v[156:159],v[52:55]
rt v[196:197],v240 offset:25344
rt v[198:199],v240 offset:26368
mm v[20:23],v[200:203],v[140:143],v[20:23]
wt lgkmcnt(4)
s_mov_b32 s93, 3770368
ld a[248:251],v228,s[40:43],s93 offen
mm v[20:23],v[204:207],v[136:139],v[20:23]
rt v[200:201],v241 offset:16896
rt v[202:203],v241 offset:17920
mm v[24:27],v[212:215],v[136:139],v[24:27]
s_mov_b32 s93, 3777536
ld a[252:255],v228,s[40:43],s93 offen
mm v[24:27],v[208:211],v[140:143],v[24:27]
rt v[204:205],v241 offset:25344
rt v[206:207],v241 offset:26368
mm v[56:59],v[208:211],v[156:159],v[56:59]
mm v[56:59],v[212:215],v[152:155],v[56:59]
rt v[208:209],v242 offset:16896
rt v[210:211],v242 offset:17920
mm v[60:63],v[220:223],v[152:155],v[60:63]
mm v[60:63],v[216:219],v[156:159],v[60:63]
rt v[212:213],v242 offset:25344
rt v[214:215],v242 offset:26368
mm v[28:31],v[216:219],v[140:143],v[28:31]
wt lgkmcnt(4)
mm v[28:31],v[220:223],v[136:139],v[28:31]
rt v[216:217],v243 offset:16896
rt v[218:219],v243 offset:17920
s_branch .La5_LO_START
; ---- owner: last window, partner-s part added, l checked, O / l leaves
.La5_LA_DC:
.La5_LW5_0:
s_cbranch_execz .La5_LW5_4
wt lgkmcnt(4)
mm v[192:195],a[64:67],v[128:131],0
ex v221,v221
mm v[192:195],a[68:71],v[132:135],v[192:195]
cv v238,v220,v221
s_add_u32 s89, s89, 16384
mm v[192:195],a[72:75],v[136:139],v[192:195]
ex v222,v222
mm v[192:195],a[76:79],v[140:143],v[192:195]
ld v255,s[60:63],s17 offen offset:0 sc1 lds
mm v[200:203],a[76:79],v[156:159],0
ex v223,v223
mm v[200:203],a[72:75],v[152:155],v[200:203]
cv v239,v222,v223
s_and_b32 s89, s89, 0x1ffff
mm v[200:203],a[68:71],v[148:151],v[200:203]
ex v192,v192
mm v[200:203],a[64:67],v[144:147],v[200:203]
wt lgkmcnt(0)
mm v[196:199],a[80:83],v[128:131],0
ex v193,v193
mm v[196:199],a[84:87],v[132:135],v[196:199]
cv v224,v192,v193
ar v243,a229
mm v[196:199],a[88:91],v[136:139],v[196:199]
ex v194,v194
mm v[196:199],a[92:95],v[140:143],v[196:199]
ld v255,s[60:63],s17 offen offset:1024 sc1 lds
mm v[204:207],a[92:95],v[156:159],0
ex v195,v195
mm v[204:207],a[88:91],v[152:155],v[204:207]
cv v225,v194,v195
mm v[204:207],a[84:87],v[148:151],v[204:207]
ex v200,v200
mm v[204:207],a[80:83],v[144:147],v[204:207]
ld v255,s[60:63],s17 offen offset:2048 sc1 lds
mm v[208:211],a[64:67],v[160:163],0
ex v201,v201
mm v[208:211],a[68:71],v[164:167],v[208:211]
cv v228,v200,v201
mm v[208:211],a[72:75],v[168:171],v[208:211]
ex v202,v202
mm v[208:211],a[76:79],v[172:175],v[208:211]
ld v255,s[60:63],s17 offen offset:3072 sc1 lds
mm v[216:219],a[76:79],v[188:191],0
ex v203,v203
mm v[216:219],a[72:75],v[184:187],v[216:219]
cv v229,v202,v203
s_mov_b32 s93, 0
mm v[216:219],a[68:71],v[180:183],v[216:219]
ex v196,v196
mm v[216:219],a[64:67],v[176:179],v[216:219]
ld a[128:131],v243,s[40:43],s93 offen
mm v[212:215],a[80:83],v[160:163],0
ex v197,v197
s_add_u32 s17, s17, s80
mm v[212:215],a[84:87],v[164:167],v[212:215]
cv v226,v196,v197
s_mov_b32 s93, 7168
s_add_u32 m0, m0, 16384
mm v[212:215],a[88:91],v[168:171],v[212:215]
ex v198,v198
s_and_b32 m0, m0, s78
mm v[212:215],a[92:95],v[172:175],v[212:215]
ld a[132:135],v243,s[40:43],s93 offen
mm v[220:223],a[92:95],v[188:191],0
ex v199,v199
mm v[220:223],a[88:91],v[184:187],v[220:223]
cv v227,v198,v199
mm v[220:223],a[84:87],v[180:183],v[220:223]
ex v204,v204
mm v[220:223],a[80:83],v[176:179],v[220:223]
wt vmcnt(12)
s_barrier
mm v[64:67],a[96:99],v[232:235],v[64:67]
ex v205,v205
mm v[96:99],a[96:99],v[236:239],v[96:99]
rd a[96:99],v248 offset:8192
cv v230,v204,v205
s_mov_b32 s93, 28672
mm v[100:103],a[100:103],v[236:239],v[100:103]
ex v206,v206
mm v[68:71],a[100:103],v[232:235],v[68:71]
rd a[100:103],v248 offset:9216
ld a[144:147],v243,s[40:43],s93 offen
mm v[72:75],a[104:107],v[232:235],v[72:75]
ex v207,v207
mm v[104:107],a[104:107],v[236:239],v[104:107]
rd a[104:107],v248 offset:10240
cv v231,v206,v207
s_mov_b32 s93, 14336
mm v[108:111],a[108:111],v[236:239],v[108:111]
ex v208,v208
mm v[76:79],a[108:111],v[232:235],v[76:79]
rd a[108:111],v248 offset:11264
ld a[136:139],v243,s[40:43],s93 offen
mm v[80:83],a[112:115],v[232:235],v[80:83]
ex v209,v209
mm v[112:115],a[112:115],v[236:239],v[112:115]
rd a[112:115],v248 offset:12288
s_mov_b32 s93, 21504
mm v[116:119],a[116:119],v[236:239],v[116:119]
ex v210,v210
mm v[84:87],a[116:119],v[232:235],v[84:87]
rd a[116:119],v248 offset:13312
ld a[140:143],v243,s[40:43],s93 offen
mm v[88:91],a[120:123],v[232:235],v[88:91]
ex v211,v211
mm v[120:123],a[120:123],v[236:239],v[120:123]
rd a[120:123],v248 offset:14336
s_mov_b32 s93, 35840
mm v[124:127],a[124:127],v[236:239],v[124:127]
ex v216,v216
mm v[92:95],a[124:127],v[232:235],v[92:95]
rd a[124:127],v248 offset:15360
ld a[148:151],v243,s[40:43],s93 offen
v_or_b32 v248, s89, v255
v_smfmac_f32_16x16x64_bf16 a[196:199], a[208:211], v[232:239], v241
wt lgkmcnt(4)
v_smfmac_f32_16x16x64_bf16 a[192:195], a[208:211], v[224:231], v241
rd a[64:67],v248 offset:0
mm v[0:3],a[96:99],v[224:227],v[0:3]
ex v217,v217
mm v[32:35],a[96:99],v[228:231],v[32:35]
rd a[68:71],v248 offset:1024
cv v232,v208,v209
mm v[36:39],a[100:103],v[228:231],v[36:39]
ex v218,v218
mm v[4:7],a[100:103],v[224:227],v[4:7]
rd a[72:75],v248 offset:2048
cv v233,v210,v211
mm v[8:11],a[104:107],v[224:227],v[8:11]
ex v219,v219
mm v[40:43],a[104:107],v[228:231],v[40:43]
rd a[76:79],v248 offset:3072
cv v236,v216,v217
mm v[44:47],a[108:111],v[228:231],v[44:47]
ex v212,v212
mm v[12:15],a[108:111],v[224:227],v[12:15]
rd a[80:83],v248 offset:4096
wt lgkmcnt(5)
mm v[16:19],a[112:115],v[224:227],v[16:19]
ex v213,v213
mm v[48:51],a[112:115],v[228:231],v[48:51]
rd a[84:87],v248 offset:5120
cv v237,v218,v219
mm v[52:55],a[116:119],v[228:231],v[52:55]
ex v214,v214
mm v[20:23],a[116:119],v[224:227],v[20:23]
rd a[88:91],v248 offset:6144
cv v234,v212,v213
mm v[24:27],a[120:123],v[224:227],v[24:27]
ex v215,v215
mm v[56:59],a[120:123],v[228:231],v[56:59]
rd a[92:95],v248 offset:7168
cv v235,v214,v215
mm v[60:63],a[124:127],v[228:231],v[60:63]
ex v220,v220
mm v[28:31],a[124:127],v[224:227],v[28:31]
wt lgkmcnt(4)
mm v[192:195],a[64:67],v[128:131],0
ex v221,v221
mm v[192:195],a[68:71],v[132:135],v[192:195]
cv v238,v220,v221
s_add_u32 s89, s89, 16384
mm v[192:195],a[72:75],v[136:139],v[192:195]
ex v222,v222
mm v[192:195],a[76:79],v[140:143],v[192:195]
ld v255,s[60:63],s17 offen offset:0 sc1 lds
mm v[200:203],a[76:79],v[156:159],0
ex v223,v223
mm v[200:203],a[72:75],v[152:155],v[200:203]
cv v239,v222,v223
s_and_b32 s89, s89, 0x1ffff
mm v[200:203],a[68:71],v[148:151],v[200:203]
ex v192,v192
mm v[200:203],a[64:67],v[144:147],v[200:203]
wt lgkmcnt(0)
mm v[196:199],a[80:83],v[128:131],0
ex v193,v193
mm v[196:199],a[84:87],v[132:135],v[196:199]
cv v224,v192,v193
mm v[196:199],a[88:91],v[136:139],v[196:199]
ex v194,v194
mm v[196:199],a[92:95],v[140:143],v[196:199]
ld v255,s[60:63],s17 offen offset:1024 sc1 lds
mm v[204:207],a[92:95],v[156:159],0
ex v195,v195
mm v[204:207],a[88:91],v[152:155],v[204:207]
cv v225,v194,v195
mm v[204:207],a[84:87],v[148:151],v[204:207]
ex v200,v200
mm v[204:207],a[80:83],v[144:147],v[204:207]
ld v255,s[60:63],s17 offen offset:2048 sc1 lds
mm v[208:211],a[64:67],v[160:163],0
ex v201,v201
mm v[208:211],a[68:71],v[164:167],v[208:211]
cv v228,v200,v201
mm v[208:211],a[72:75],v[168:171],v[208:211]
ex v202,v202
mm v[208:211],a[76:79],v[172:175],v[208:211]
ld v255,s[60:63],s17 offen offset:3072 sc1 lds
mm v[216:219],a[76:79],v[188:191],0
ex v203,v203
mm v[216:219],a[72:75],v[184:187],v[216:219]
cv v229,v202,v203
s_mov_b32 s93, 43008
mm v[216:219],a[68:71],v[180:183],v[216:219]
ex v196,v196
mm v[216:219],a[64:67],v[176:179],v[216:219]
ld a[152:155],v243,s[40:43],s93 offen
mm v[212:215],a[80:83],v[160:163],0
ex v197,v197
s_add_u32 s17, s17, s80
mm v[212:215],a[84:87],v[164:167],v[212:215]
cv v226,v196,v197
s_mov_b32 s93, 50176
s_add_u32 m0, m0, 16384
mm v[212:215],a[88:91],v[168:171],v[212:215]
ex v198,v198
s_and_b32 m0, m0, s78
mm v[212:215],a[92:95],v[172:175],v[212:215]
ld a[156:159],v243,s[40:43],s93 offen
mm v[220:223],a[92:95],v[188:191],0
ex v199,v199
mm v[220:223],a[88:91],v[184:187],v[220:223]
cv v227,v198,v199
mm v[220:223],a[84:87],v[180:183],v[220:223]
ex v204,v204
mm v[220:223],a[80:83],v[176:179],v[220:223]
wt vmcnt(17)
s_barrier
mm v[64:67],a[96:99],v[232:235],v[64:67]
ex v205,v205
mm v[96:99],a[96:99],v[236:239],v[96:99]
rd a[96:99],v248 offset:8192
cv v230,v204,v205
s_mov_b32 s93, 71680
mm v[100:103],a[100:103],v[236:239],v[100:103]
ex v206,v206
mm v[68:71],a[100:103],v[232:235],v[68:71]
rd a[100:103],v248 offset:9216
ld a[168:171],v243,s[40:43],s93 offen
mm v[72:75],a[104:107],v[232:235],v[72:75]
ex v207,v207
mm v[104:107],a[104:107],v[236:239],v[104:107]
rd a[104:107],v248 offset:10240
cv v231,v206,v207
s_mov_b32 s93, 57344
mm v[108:111],a[108:111],v[236:239],v[108:111]
ex v208,v208
mm v[76:79],a[108:111],v[232:235],v[76:79]
rd a[108:111],v248 offset:11264
ld a[160:163],v243,s[40:43],s93 offen
mm v[80:83],a[112:115],v[232:235],v[80:83]
ex v209,v209
mm v[112:115],a[112:115],v[236:239],v[112:115]
rd a[112:115],v248 offset:12288
s_mov_b32 s93, 64512
mm v[116:119],a[116:119],v[236:239],v[116:119]
ex v210,v210
mm v[84:87],a[116:119],v[232:235],v[84:87]
rd a[116:119],v248 offset:13312
ld a[164:167],v243,s[40:43],s93 offen
mm v[88:91],a[120:123],v[232:235],v[88:91]
ex v211,v211
mm v[120:123],a[120:123],v[236:239],v[120:123]
rd a[120:123],v248 offset:14336
mm v[124:127],a[124:127],v[236:239],v[124:127]
ex v216,v216
mm v[92:95],a[124:127],v[232:235],v[92:95]
rd a[124:127],v248 offset:15360
v_or_b32 v248, s89, v255
v_smfmac_f32_16x16x64_bf16 a[196:199], a[208:211], v[232:239], v241
wt lgkmcnt(4)
v_smfmac_f32_16x16x64_bf16 a[192:195], a[208:211], v[224:231], v241
rd a[64:67],v248 offset:0
mm v[0:3],a[96:99],v[224:227],v[0:3]
ex v217,v217
mm v[32:35],a[96:99],v[228:231],v[32:35]
rd a[68:71],v248 offset:1024
cv v232,v208,v209
mm v[36:39],a[100:103],v[228:231],v[36:39]
ex v218,v218
mm v[4:7],a[100:103],v[224:227],v[4:7]
rd a[72:75],v248 offset:2048
cv v233,v210,v211
mm v[8:11],a[104:107],v[224:227],v[8:11]
ex v219,v219
mm v[40:43],a[104:107],v[228:231],v[40:43]
rd a[76:79],v248 offset:3072
cv v236,v216,v217
mm v[44:47],a[108:111],v[228:231],v[44:47]
ex v212,v212
mm v[12:15],a[108:111],v[224:227],v[12:15]
rd a[80:83],v248 offset:4096
wt lgkmcnt(5)
mm v[16:19],a[112:115],v[224:227],v[16:19]
ex v213,v213
mm v[48:51],a[112:115],v[228:231],v[48:51]
rd a[84:87],v248 offset:5120
cv v237,v218,v219
mm v[52:55],a[116:119],v[228:231],v[52:55]
ex v214,v214
mm v[20:23],a[116:119],v[224:227],v[20:23]
rd a[88:91],v248 offset:6144
cv v234,v212,v213
mm v[24:27],a[120:123],v[224:227],v[24:27]
ex v215,v215
mm v[56:59],a[120:123],v[228:231],v[56:59]
rd a[92:95],v248 offset:7168
cv v235,v214,v215
mm v[60:63],a[124:127],v[228:231],v[60:63]
ex v220,v220
mm v[28:31],a[124:127],v[224:227],v[28:31]
wt lgkmcnt(4)
mm v[192:195],a[64:67],v[128:131],0
ex v221,v221
mm v[192:195],a[68:71],v[132:135],v[192:195]
cv v238,v220,v221
s_add_u32 s89, s89, 16384
mm v[192:195],a[72:75],v[136:139],v[192:195]
ex v222,v222
mm v[192:195],a[76:79],v[140:143],v[192:195]
ld v255,s[60:63],s17 offen offset:0 sc1 lds
mm v[200:203],a[76:79],v[156:159],0
ex v223,v223
mm v[200:203],a[72:75],v[152:155],v[200:203]
cv v239,v222,v223
s_and_b32 s89, s89, 0x1ffff
mm v[200:203],a[68:71],v[148:151],v[200:203]
ex v192,v192
mm v[200:203],a[64:67],v[144:147],v[200:203]
wt lgkmcnt(0)
mm v[196:199],a[80:83],v[128:131],0
ex v193,v193
mm v[196:199],a[84:87],v[132:135],v[196:199]
cv v224,v192,v193
mm v[196:199],a[88:91],v[136:139],v[196:199]
ex v194,v194
mm v[196:199],a[92:95],v[140:143],v[196:199]
ld v255,s[60:63],s17 offen offset:1024 sc1 lds
mm v[204:207],a[92:95],v[156:159],0
ex v195,v195
mm v[204:207],a[88:91],v[152:155],v[204:207]
cv v225,v194,v195
mm v[204:207],a[84:87],v[148:151],v[204:207]
ex v200,v200
mm v[204:207],a[80:83],v[144:147],v[204:207]
ld v255,s[60:63],s17 offen offset:2048 sc1 lds
mm v[208:211],a[64:67],v[160:163],0
ex v201,v201
mm v[208:211],a[68:71],v[164:167],v[208:211]
cv v228,v200,v201
mm v[208:211],a[72:75],v[168:171],v[208:211]
ex v202,v202
mm v[208:211],a[76:79],v[172:175],v[208:211]
ld v255,s[60:63],s17 offen offset:3072 sc1 lds
mm v[216:219],a[76:79],v[188:191],0
ex v203,v203
mm v[216:219],a[72:75],v[184:187],v[216:219]
cv v229,v202,v203
s_mov_b32 s93, 78848
mm v[216:219],a[68:71],v[180:183],v[216:219]
ex v196,v196
mm v[216:219],a[64:67],v[176:179],v[216:219]
ld a[172:175],v243,s[40:43],s93 offen
mm v[212:215],a[80:83],v[160:163],0
ex v197,v197
s_add_u32 s17, s17, s80
mm v[212:215],a[84:87],v[164:167],v[212:215]
cv v226,v196,v197
s_mov_b32 s93, 86016
s_add_u32 m0, m0, 16384
mm v[212:215],a[88:91],v[168:171],v[212:215]
ex v198,v198
s_and_b32 m0, m0, s78
mm v[212:215],a[92:95],v[172:175],v[212:215]
ld a[176:179],v243,s[40:43],s93 offen
mm v[220:223],a[92:95],v[188:191],0
ex v199,v199
mm v[220:223],a[88:91],v[184:187],v[220:223]
cv v227,v198,v199
mm v[220:223],a[84:87],v[180:183],v[220:223]
ex v204,v204
mm v[220:223],a[80:83],v[176:179],v[220:223]
wt vmcnt(21)
s_barrier
mm v[64:67],a[96:99],v[232:235],v[64:67]
ex v205,v205
mm v[96:99],a[96:99],v[236:239],v[96:99]
rd a[96:99],v248 offset:8192
cv v230,v204,v205
s_mov_b32 s93, 107520
mm v[100:103],a[100:103],v[236:239],v[100:103]
ex v206,v206
mm v[68:71],a[100:103],v[232:235],v[68:71]
rd a[100:103],v248 offset:9216
ld a[188:191],v243,s[40:43],s93 offen
mm v[72:75],a[104:107],v[232:235],v[72:75]
ex v207,v207
mm v[104:107],a[104:107],v[236:239],v[104:107]
rd a[104:107],v248 offset:10240
cv v231,v206,v207
s_mov_b32 s93, 93184
mm v[108:111],a[108:111],v[236:239],v[108:111]
ex v208,v208
mm v[76:79],a[108:111],v[232:235],v[76:79]
rd a[108:111],v248 offset:11264
ld a[180:183],v243,s[40:43],s93 offen
mm v[80:83],a[112:115],v[232:235],v[80:83]
ex v209,v209
mm v[112:115],a[112:115],v[236:239],v[112:115]
rd a[112:115],v248 offset:12288
s_mov_b32 s93, 100352
mm v[116:119],a[116:119],v[236:239],v[116:119]
ex v210,v210
mm v[84:87],a[116:119],v[232:235],v[84:87]
rd a[116:119],v248 offset:13312
ld a[184:187],v243,s[40:43],s93 offen
mm v[88:91],a[120:123],v[232:235],v[88:91]
.La5_LW5_1:
s_cbranch_execz .La5_LW5_R0
ex v211,v211
mm v[120:123],a[120:123],v[236:239],v[120:123]
rd a[120:123],v248 offset:14336
mm v[124:127],a[124:127],v[236:239],v[124:127]
ex v216,v216
mm v[92:95],a[124:127],v[232:235],v[92:95]
rd a[124:127],v248 offset:15360
v_or_b32 v248, s89, v255
v_smfmac_f32_16x16x64_bf16 a[196:199], a[208:211], v[232:239], v241
wt lgkmcnt(4)
v_smfmac_f32_16x16x64_bf16 a[192:195], a[208:211], v[224:231], v241
rd a[64:67],v248 offset:0
mm v[0:3],a[96:99],v[224:227],v[0:3]
ex v217,v217
mm v[32:35],a[96:99],v[228:231],v[32:35]
rd a[68:71],v248 offset:1024
cv v232,v208,v209
mm v[36:39],a[100:103],v[228:231],v[36:39]
ex v218,v218
mm v[4:7],a[100:103],v[224:227],v[4:7]
rd a[72:75],v248 offset:2048
cv v233,v210,v211
mm v[8:11],a[104:107],v[224:227],v[8:11]
ex v219,v219
mm v[40:43],a[104:107],v[228:231],v[40:43]
rd a[76:79],v248 offset:3072
cv v236,v216,v217
mm v[44:47],a[108:111],v[228:231],v[44:47]
ex v212,v212
mm v[12:15],a[108:111],v[224:227],v[12:15]
rd a[80:83],v248 offset:4096
wt lgkmcnt(5)
mm v[16:19],a[112:115],v[224:227],v[16:19]
ex v213,v213
mm v[48:51],a[112:115],v[228:231],v[48:51]
rd a[84:87],v248 offset:5120
cv v237,v218,v219
mm v[52:55],a[116:119],v[228:231],v[52:55]
ex v214,v214
mm v[20:23],a[116:119],v[224:227],v[20:23]
rd a[88:91],v248 offset:6144
cv v234,v212,v213
mm v[24:27],a[120:123],v[224:227],v[24:27]
ex v215,v215
mm v[56:59],a[120:123],v[228:231],v[56:59]
rd a[92:95],v248 offset:7168
cv v235,v214,v215
mm v[60:63],a[124:127],v[228:231],v[60:63]
ex v220,v220
mm v[28:31],a[124:127],v[224:227],v[28:31]
.La5_LA_DC3:
.La5_LW5_2:
s_cbranch_execz .La5_LW5_R0
wt lgkmcnt(4)
mm v[192:195],a[64:67],v[128:131],0
ex v221,v221
mm v[192:195],a[68:71],v[132:135],v[192:195]
cv v238,v220,v221
ar v240,a228
mm v[192:195],a[72:75],v[136:139],v[192:195]
ex v222,v222
mm v[192:195],a[76:79],v[140:143],v[192:195]
s_add_u32 m0, s83, 16896
s_lshl_b32 s17, s72, 18
mm v[200:203],a[76:79],v[156:159],0
ex v223,v223
mm v[200:203],a[72:75],v[152:155],v[200:203]
cv v239,v222,v223
s_add_u32 s89, s89, 16384
mm v[200:203],a[68:71],v[148:151],v[200:203]
ex v192,v192
mm v[200:203],a[64:67],v[144:147],v[200:203]
wt lgkmcnt(0)
s_and_b32 s89, s89, 0x1ffff
mm v[196:199],a[80:83],v[128:131],0
ex v193,v193
mm v[196:199],a[84:87],v[132:135],v[196:199]
cv v224,v192,v193
mm v[196:199],a[88:91],v[136:139],v[196:199]
ex v194,v194
mm v[196:199],a[92:95],v[140:143],v[196:199]
ld v240,s[56:59],s17 offen lds
mm v[204:207],a[92:95],v[156:159],0
ex v195,v195
s_add_u32 s17, s17, 32768
mm v[204:207],a[88:91],v[152:155],v[204:207]
cv v225,v194,v195
s_add_u32 m0, m0, 1024
mm v[204:207],a[84:87],v[148:151],v[204:207]
ex v200,v200
mm v[204:207],a[80:83],v[144:147],v[204:207]
ld v240,s[56:59],s17 offen lds
mm v[208:211],a[64:67],v[160:163],0
ex v201,v201
s_add_u32 s17, s17, 4294942720
mm v[208:211],a[68:71],v[164:167],v[208:211]
cv v228,v200,v201
s_add_u32 m0, m0, 1152
mm v[208:211],a[72:75],v[168:171],v[208:211]
ex v202,v202
mm v[208:211],a[76:79],v[172:175],v[208:211]
ld v240,s[56:59],s17 offen lds
mm v[216:219],a[76:79],v[188:191],0
ex v203,v203
s_add_u32 s17, s17, 32768
mm v[216:219],a[72:75],v[184:187],v[216:219]
cv v229,v202,v203
s_add_u32 m0, m0, 1024
mm v[216:219],a[68:71],v[180:183],v[216:219]
ex v196,v196
mm v[216:219],a[64:67],v[176:179],v[216:219]
ld v240,s[56:59],s17 offen lds
mm v[212:215],a[80:83],v[160:163],0
ex v197,v197
s_add_u32 s17, s17, 90112
mm v[212:215],a[84:87],v[164:167],v[212:215]
cv v226,v196,v197
s_add_u32 m0, s83, 33792
mm v[212:215],a[88:91],v[168:171],v[212:215]
ex v198,v198
mm v[212:215],a[92:95],v[172:175],v[212:215]
mm v[220:223],a[92:95],v[188:191],0
ex v199,v199
mm v[220:223],a[88:91],v[184:187],v[220:223]
cv v227,v198,v199
mm v[220:223],a[84:87],v[180:183],v[220:223]
ex v204,v204
mm v[220:223],a[80:83],v[176:179],v[220:223]
wt vmcnt(10)
s_barrier
mm v[64:67],a[96:99],v[232:235],v[64:67]
ex v205,v205
mm v[96:99],a[96:99],v[236:239],v[96:99]
rd a[96:99],v248 offset:8192
cv v230,v204,v205
mm v[100:103],a[100:103],v[236:239],v[100:103]
ex v206,v206
mm v[68:71],a[100:103],v[232:235],v[68:71]
rd a[100:103],v248 offset:9216
mm v[72:75],a[104:107],v[232:235],v[72:75]
ex v207,v207
mm v[104:107],a[104:107],v[236:239],v[104:107]
rd a[104:107],v248 offset:10240
cv v231,v206,v207
mm v[108:111],a[108:111],v[236:239],v[108:111]
ex v208,v208
mm v[76:79],a[108:111],v[232:235],v[76:79]
rd a[108:111],v248 offset:11264
mm v[80:83],a[112:115],v[232:235],v[80:83]
ex v209,v209
mm v[112:115],a[112:115],v[236:239],v[112:115]
rd a[112:115],v248 offset:12288
mm v[116:119],a[116:119],v[236:239],v[116:119]
ex v210,v210
mm v[84:87],a[116:119],v[232:235],v[84:87]
rd a[116:119],v248 offset:13312
mm v[88:91],a[120:123],v[232:235],v[88:91]
ex v211,v211
mm v[120:123],a[120:123],v[236:239],v[120:123]
rd a[120:123],v248 offset:14336
mm v[124:127],a[124:127],v[236:239],v[124:127]
ex v216,v216
mm v[92:95],a[124:127],v[232:235],v[92:95]
rd a[124:127],v248 offset:15360
v_or_b32 v248, s89, v255
v_smfmac_f32_16x16x64_bf16 a[196:199], a[208:211], v[232:239], v241
wt lgkmcnt(4)
v_smfmac_f32_16x16x64_bf16 a[192:195], a[208:211], v[224:231], v241
rd a[64:67],v248 offset:0
mm v[0:3],a[96:99],v[224:227],v[0:3]
ex v217,v217
mm v[32:35],a[96:99],v[228:231],v[32:35]
rd a[68:71],v248 offset:1024
cv v232,v208,v209
mm v[36:39],a[100:103],v[228:231],v[36:39]
ex v218,v218
mm v[4:7],a[100:103],v[224:227],v[4:7]
rd a[72:75],v248 offset:2048
cv v233,v210,v211
mm v[8:11],a[104:107],v[224:227],v[8:11]
ex v219,v219
mm v[40:43],a[104:107],v[228:231],v[40:43]
rd a[76:79],v248 offset:3072
cv v236,v216,v217
mm v[44:47],a[108:111],v[228:231],v[44:47]
ex v212,v212
mm v[12:15],a[108:111],v[224:227],v[12:15]
rd a[80:83],v248 offset:4096
wt lgkmcnt(5)
mm v[16:19],a[112:115],v[224:227],v[16:19]
ex v213,v213
mm v[48:51],a[112:115],v[228:231],v[48:51]
rd a[84:87],v248 offset:5120
cv v237,v218,v219
mm v[52:55],a[116:119],v[228:231],v[52:55]
ex v214,v214
mm v[20:23],a[116:119],v[224:227],v[20:23]
rd a[88:91],v248 offset:6144
cv v234,v212,v213
mm v[24:27],a[120:123],v[224:227],v[24:27]
ex v215,v215
mm v[56:59],a[120:123],v[228:231],v[56:59]
rd a[92:95],v248 offset:7168
cv v235,v214,v215
mm v[60:63],a[124:127],v[228:231],v[60:63]
ex v220,v220
mm v[28:31],a[124:127],v[224:227],v[28:31]
buffer_load_dword v250, v251, s[64:67], s20 offen sc1
wt lgkmcnt(4)
mm v[192:195],a[64:67],v[128:131],0
ex v221,v221
mm v[192:195],a[68:71],v[132:135],v[192:195]
cv v238,v220,v221
s_add_u32 s89, s89, 16384
mm v[192:195],a[72:75],v[136:139],v[192:195]
ex v222,v222
mm v[192:195],a[76:79],v[140:143],v[192:195]
ld v240,s[56:59],s17 offen lds
mm v[200:203],a[76:79],v[156:159],0
ex v223,v223
s_add_u32 s17, s17, 32768
mm v[200:203],a[72:75],v[152:155],v[200:203]
cv v239,v222,v223
s_add_u32 m0, m0, 1024
mm v[200:203],a[68:71],v[148:151],v[200:203]
ex v192,v192
mm v[200:203],a[64:67],v[144:147],v[200:203]
wt lgkmcnt(0)
s_and_b32 s89, s89, 0x1ffff
mm v[196:199],a[80:83],v[128:131],0
ex v193,v193
mm v[196:199],a[84:87],v[132:135],v[196:199]
cv v224,v192,v193
mm v[196:199],a[88:91],v[136:139],v[196:199]
ex v194,v194
mm v[196:199],a[92:95],v[140:143],v[196:199]
ld v240,s[56:59],s17 offen lds
mm v[204:207],a[92:95],v[156:159],0
ex v195,v195
s_add_u32 s17, s17, 4294942720
mm v[204:207],a[88:91],v[152:155],v[204:207]
cv v225,v194,v195
s_add_u32 m0, m0, 1152
mm v[204:207],a[84:87],v[148:151],v[204:207]
ex v200,v200
mm v[204:207],a[80:83],v[144:147],v[204:207]
ld v240,s[56:59],s17 offen lds
mm v[208:211],a[64:67],v[160:163],0
ex v201,v201
s_add_u32 s17, s17, 32768
mm v[208:211],a[68:71],v[164:167],v[208:211]
cv v228,v200,v201
s_add_u32 m0, m0, 1024
mm v[208:211],a[72:75],v[168:171],v[208:211]
ex v202,v202
mm v[208:211],a[76:79],v[172:175],v[208:211]
ld v240,s[56:59],s17 offen lds
mm v[216:219],a[76:79],v[188:191],0
ex v203,v203
mm v[216:219],a[72:75],v[184:187],v[216:219]
cv v229,v202,v203
mm v[216:219],a[68:71],v[180:183],v[216:219]
ex v196,v196
mm v[216:219],a[64:67],v[176:179],v[216:219]
mm v[212:215],a[80:83],v[160:163],0
ex v197,v197
s_bfe_u32 s17, s4, 262148
mm v[212:215],a[84:87],v[164:167],v[212:215]
cv v226,v196,v197
s_xor_b32 s17, s17, s72
s_lshl_b32 s17, s17, 18
mm v[212:215],a[88:91],v[168:171],v[212:215]
ex v198,v198
s_add_u32 m0, s83, 50688
mm v[212:215],a[92:95],v[172:175],v[212:215]
mm v[220:223],a[92:95],v[188:191],0
ex v199,v199
mm v[220:223],a[88:91],v[184:187],v[220:223]
cv v227,v198,v199
wt vmcnt(4)
v_cmp_eq_u32 s[92:93], v250, v249
s_mov_b32 s94, 16384
mm v[220:223],a[84:87],v[180:183],v[220:223]
ex v204,v204
mm v[220:223],a[80:83],v[176:179],v[220:223]
s_nop 0
s_barrier
mm v[64:67],a[96:99],v[232:235],v[64:67]
ex v205,v205
mm v[96:99],a[96:99],v[236:239],v[96:99]
rd a[96:99],v248 offset:8192
cv v230,v204,v205
s_and_b32 s92, s92, 3
s_andn2_b32 s92, s92, s23
s_cmp_eq_u32 s92, 3
s_cselect_b32 s70, 17408, 0
s_cselect_b32 s84, 3, s23
mm v[100:103],a[100:103],v[236:239],v[100:103]
ex v206,v206
mm v[68:71],a[100:103],v[232:235],v[68:71]
rd a[100:103],v248 offset:9216
ld a[220:223],v255,s[68:71],s94 offen sc1
mm v[72:75],a[104:107],v[232:235],v[72:75]
ex v207,v207
mm v[104:107],a[104:107],v[236:239],v[104:107]
rd a[104:107],v248 offset:10240
cv v231,v206,v207
mm v[108:111],a[108:111],v[236:239],v[108:111]
ex v208,v208
mm v[76:79],a[108:111],v[232:235],v[76:79]
rd a[108:111],v248 offset:11264
ld a[0:3],v255,s[68:71],0 offen offset:0 sc1
mm v[80:83],a[112:115],v[232:235],v[80:83]
ex v209,v209
mm v[112:115],a[112:115],v[236:239],v[112:115]
rd a[112:115],v248 offset:12288
mm v[116:119],a[116:119],v[236:239],v[116:119]
ex v210,v210
mm v[84:87],a[116:119],v[232:235],v[84:87]
rd a[116:119],v248 offset:13312
ld a[4:7],v255,s[68:71],0 offen offset:1024 sc1
mm v[88:91],a[120:123],v[232:235],v[88:91]
ex v211,v211
mm v[120:123],a[120:123],v[236:239],v[120:123]
rd a[120:123],v248 offset:14336
mm v[124:127],a[124:127],v[236:239],v[124:127]
ex v216,v216
mm v[92:95],a[124:127],v[232:235],v[92:95]
rd a[124:127],v248 offset:15360
v_or_b32 v248, s89, v255
buffer_load_dword v250, v251, s[64:67], s20 offen sc1
v_smfmac_f32_16x16x64_bf16 a[196:199], a[208:211], v[232:239], v241
wt lgkmcnt(4)
v_smfmac_f32_16x16x64_bf16 a[192:195], a[208:211], v[224:231], v241
rd a[64:67],v248 offset:0
mm v[0:3],a[96:99],v[224:227],v[0:3]
ex v217,v217
mm v[32:35],a[96:99],v[228:231],v[32:35]
rd a[68:71],v248 offset:1024
cv v232,v208,v209
ld a[8:11],v255,s[68:71],0 offen offset:2048 sc1
mm v[36:39],a[100:103],v[228:231],v[36:39]
ex v218,v218
mm v[4:7],a[100:103],v[224:227],v[4:7]
rd a[72:75],v248 offset:2048
cv v233,v210,v211
mm v[8:11],a[104:107],v[224:227],v[8:11]
ex v219,v219
mm v[40:43],a[104:107],v[228:231],v[40:43]
rd a[76:79],v248 offset:3072
cv v236,v216,v217
ld a[12:15],v255,s[68:71],0 offen offset:3072 sc1
mm v[44:47],a[108:111],v[228:231],v[44:47]
ex v212,v212
mm v[12:15],a[108:111],v[224:227],v[12:15]
rd a[80:83],v248 offset:4096
wt lgkmcnt(5)
mm v[16:19],a[112:115],v[224:227],v[16:19]
ex v213,v213
mm v[48:51],a[112:115],v[228:231],v[48:51]
rd a[84:87],v248 offset:5120
cv v237,v218,v219
ld a[16:19],v255,s[68:71],s95 offen offset:0 sc1
mm v[52:55],a[116:119],v[228:231],v[52:55]
ex v214,v214
mm v[20:23],a[116:119],v[224:227],v[20:23]
rd a[88:91],v248 offset:6144
cv v234,v212,v213
mm v[24:27],a[120:123],v[224:227],v[24:27]
ex v215,v215
mm v[56:59],a[120:123],v[228:231],v[56:59]
rd a[92:95],v248 offset:7168
cv v235,v214,v215
ld a[20:23],v255,s[68:71],s95 offen offset:1024 sc1
mm v[60:63],a[124:127],v[228:231],v[60:63]
ex v220,v220
mm v[28:31],a[124:127],v[224:227],v[28:31]
ld a[24:27],v255,s[68:71],s95 offen offset:2048 sc1
wt lgkmcnt(4)
mm v[192:195],a[64:67],v[128:131],0
ex v221,v221
mm v[192:195],a[68:71],v[132:135],v[192:195]
cv v238,v220,v221
mm v[192:195],a[72:75],v[136:139],v[192:195]
ex v222,v222
mm v[192:195],a[76:79],v[140:143],v[192:195]
ld v240,s[56:59],s17 offen lds
mm v[200:203],a[76:79],v[156:159],0
ex v223,v223
s_add_u32 s17, s17, 32768
mm v[200:203],a[72:75],v[152:155],v[200:203]
cv v239,v222,v223
s_add_u32 m0, m0, 1024
mm v[200:203],a[68:71],v[148:151],v[200:203]
ex v192,v192
mm v[200:203],a[64:67],v[144:147],v[200:203]
wt lgkmcnt(0)
mm v[196:199],a[80:83],v[128:131],0
ex v193,v193
mm v[196:199],a[84:87],v[132:135],v[196:199]
cv v224,v192,v193
wt vmcnt(6)
v_cmp_eq_u32 s[92:93], v250, v249
mm v[196:199],a[88:91],v[136:139],v[196:199]
ex v194,v194
mm v[196:199],a[92:95],v[140:143],v[196:199]
ld v240,s[56:59],s17 offen lds
mm v[204:207],a[92:95],v[156:159],0
ex v195,v195
s_add_u32 s17, s17, 4294942720
mm v[204:207],a[88:91],v[152:155],v[204:207]
cv v225,v194,v195
s_add_u32 m0, m0, 1152
s_and_b32 s92, s92, 3
s_sub_u32 s92, s92, s84
s_cmp_eq_u32 s92, 3
s_cbranch_scc1 .La5_LA_PS0
.La5_LA_PS1:
mm v[204:207],a[84:87],v[148:151],v[204:207]
ex v200,v200
mm v[204:207],a[80:83],v[144:147],v[204:207]
ld v240,s[56:59],s17 offen lds
mm v[208:211],a[64:67],v[160:163],0
ex v201,v201
s_add_u32 s17, s17, 32768
mm v[208:211],a[68:71],v[164:167],v[208:211]
cv v228,v200,v201
s_add_u32 m0, m0, 1024
ld a[28:31],v255,s[68:71],s95 offen offset:3072 sc1
mm v[208:211],a[72:75],v[168:171],v[208:211]
ex v202,v202
mm v[208:211],a[76:79],v[172:175],v[208:211]
ld v240,s[56:59],s17 offen lds
mm v[216:219],a[76:79],v[188:191],0
ex v203,v203
mm v[216:219],a[72:75],v[184:187],v[216:219]
cv v229,v202,v203
ld a[32:35],v255,s[68:71],s96 offen offset:0 sc1
mm v[216:219],a[68:71],v[180:183],v[216:219]
ex v196,v196
mm v[216:219],a[64:67],v[176:179],v[216:219]
mm v[212:215],a[80:83],v[160:163],0
ex v197,v197
mm v[212:215],a[84:87],v[164:167],v[212:215]
cv v226,v196,v197
buffer_load_dword v250, v251, s[64:67], s20 offen sc1
mm v[212:215],a[88:91],v[168:171],v[212:215]
ex v198,v198
mm v[212:215],a[92:95],v[172:175],v[212:215]
ld a[36:39],v255,s[68:71],s96 offen offset:1024 sc1
mm v[220:223],a[92:95],v[188:191],0
ex v199,v199
mm v[220:223],a[88:91],v[184:187],v[220:223]
cv v227,v198,v199
mm v[220:223],a[84:87],v[180:183],v[220:223]
ex v204,v204
mm v[220:223],a[80:83],v[176:179],v[220:223]
ld a[40:43],v255,s[68:71],s96 offen offset:2048 sc1
mm v[64:67],a[96:99],v[232:235],v[64:67]
ex v205,v205
mm v[96:99],a[96:99],v[236:239],v[96:99]
rd a[96:99],v248 offset:8192
cv v230,v204,v205
mm v[100:103],a[100:103],v[236:239],v[100:103]
ex v206,v206
mm v[68:71],a[100:103],v[232:235],v[68:71]
rd a[100:103],v248 offset:9216
ld a[44:47],v255,s[68:71],s96 offen offset:3072 sc1
mm v[72:75],a[104:107],v[232:235],v[72:75]
ex v207,v207
mm v[104:107],a[104:107],v[236:239],v[104:107]
rd a[104:107],v248 offset:10240
cv v231,v206,v207
mm v[108:111],a[108:111],v[236:239],v[108:111]
ex v208,v208
mm v[76:79],a[108:111],v[232:235],v[76:79]
rd a[108:111],v248 offset:11264
ld a[48:51],v255,s[68:71],s97 offen offset:0 sc1
mm v[80:83],a[112:115],v[232:235],v[80:83]
ex v209,v209
mm v[112:115],a[112:115],v[236:239],v[112:115]
rd a[112:115],v248 offset:12288
mm v[116:119],a[116:119],v[236:239],v[116:119]
ex v210,v210
mm v[84:87],a[116:119],v[232:235],v[84:87]
rd a[116:119],v248 offset:13312
ld a[52:55],v255,s[68:71],s97 offen offset:1024 sc1
mm v[88:91],a[120:123],v[232:235],v[88:91]
ex v211,v211
mm v[120:123],a[120:123],v[236:239],v[120:123]
rd a[120:123],v248 offset:14336
mm v[124:127],a[124:127],v[236:239],v[124:127]
ex v216,v216
mm v[92:95],a[124:127],v[232:235],v[92:95]
rd a[124:127],v248 offset:15360
ld a[56:59],v255,s[68:71],s97 offen offset:2048 sc1
v_smfmac_f32_16x16x64_bf16 a[196:199], a[208:211], v[232:239], v241
wt lgkmcnt(4)
v_smfmac_f32_16x16x64_bf16 a[192:195], a[208:211], v[224:231], v241
ld a[60:63],v255,s[68:71],s97 offen offset:3072 sc1
mm v[0:3],a[96:99],v[224:227],v[0:3]
ex v217,v217
mm v[32:35],a[96:99],v[228:231],v[32:35]
cv v232,v208,v209
mm v[36:39],a[100:103],v[228:231],v[36:39]
ex v218,v218
mm v[4:7],a[100:103],v[224:227],v[4:7]
cv v233,v210,v211
mm v[8:11],a[104:107],v[224:227],v[8:11]
ex v219,v219
mm v[40:43],a[104:107],v[228:231],v[40:43]
cv v236,v216,v217
mm v[44:47],a[108:111],v[228:231],v[44:47]
ex v212,v212
wt vmcnt(7)
v_cmp_eq_u32 s[92:93], v250, v249
mm v[12:15],a[108:111],v[224:227],v[12:15]
wt lgkmcnt(0)
mm v[16:19],a[112:115],v[224:227],v[16:19]
ex v213,v213
mm v[48:51],a[112:115],v[228:231],v[48:51]
cv v237,v218,v219
s_and_b32 s92, s92, 3
s_sub_u32 s92, s92, s84
mm v[52:55],a[116:119],v[228:231],v[52:55]
ex v214,v214
mm v[20:23],a[116:119],v[224:227],v[20:23]
cv v234,v212,v213
mm v[24:27],a[120:123],v[224:227],v[24:27]
ex v215,v215
mm v[56:59],a[120:123],v[228:231],v[56:59]
cv v235,v214,v215
mm v[60:63],a[124:127],v[228:231],v[60:63]
ex v220,v220
mm v[28:31],a[124:127],v[224:227],v[28:31]
s_cmp_eq_u32 s92, 3
s_cbranch_scc1 .La5_LA_PS2
.La5_LA_PS3:
s_cmp_lg_u32 s84, 3
s_cbranch_scc1 .La5_LA_PSPIN
.La5_LA_DC1:
wt vmcnt(8)
mm v[0:3],a[214:217],a[0:3],v[0:3]
ar v250,a192
ar v251,a193
v_add_f32 v250, v250, v251
ar v251,a220
v_add_f32 v250, v250, v251
ar v249,a194
mm v[4:7],a[212:215],a[0:3],v[4:7]
ar v251,a195
v_add_f32 v249, v249, v251
ar v251,a221
v_add_f32 v249, v249, v251
v_subrev_u32 v251, 562036736, v250
mm v[8:11],a[214:217],a[4:7],v[8:11]
v_rcp_f32 v250, v250
v_cmp_le_u32 vcc, 1006632960, v251
v_subrev_u32 v251, 562036736, v249
s_mov_b64 s[92:93], vcc
v_cmp_le_u32 vcc, 1006632960, v251
mm v[12:15],a[212:215],a[4:7],v[12:15]
v_rcp_f32 v249, v249
mm v[16:19],a[214:217],a[8:11],v[16:19]
s_or_b64 vcc, vcc, s[92:93]
s_cbranch_scc1 .La5_LA_SC3
.La5_LA_SR3:
ex v221,v221
ex v222,v222
mm v[20:23],a[212:215],a[8:11],v[20:23]
ex v223,v223
cv v238,v220,v221
v_mul_f32 v0, v0, v250
v_mul_f32 v1, v1, v250
v_mul_f32 v2, v2, v250
mm v[24:27],a[214:217],a[12:15],v[24:27]
v_mul_f32 v3, v3, v250
v_mul_f32 v4, v4, v250
v_mul_f32 v5, v5, v250
v_mul_f32 v6, v6, v250
v_mul_f32 v7, v7, v250
cv v128,v0,v1
mm v[28:31],a[212:215],a[12:15],v[28:31]
cv v129,v2,v3
cv v130,v4,v5
cv v131,v6,v7
st v[128:131],v244,s[44:47],s91 offen offset:0 sc1
cv v239,v222,v223
mm v[32:35],a[214:217],a[16:19],v[32:35]
v_mul_f32 v8, v8, v250
v_mul_f32 v9, v9, v250
v_mul_f32 v10, v10, v250
v_mul_f32 v11, v11, v250
v_mul_f32 v12, v12, v250
v_mul_f32 v13, v13, v250
mm v[36:39],a[212:215],a[16:19],v[36:39]
v_mul_f32 v14, v14, v250
v_mul_f32 v15, v15, v250
cv v132,v8,v9
cv v133,v10,v11
cv v134,v12,v13
cv v135,v14,v15
mm v[40:43],a[214:217],a[20:23],v[40:43]
st v[132:135],v244,s[44:47],s91 offen offset:1024 sc1
v_mul_f32 v16, v16, v250
v_mul_f32 v17, v17, v250
v_mul_f32 v18, v18, v250
.La5_LW5_3:
s_cbranch_execz .La5_LW5_R0
v_mul_f32 v19, v19, v250
mm v[44:47],a[212:215],a[20:23],v[44:47]
v_mul_f32 v20, v20, v250
v_mul_f32 v21, v21, v250
v_mul_f32 v22, v22, v250
v_mul_f32 v23, v23, v250
cv v136,v16,v17
cv v137,v18,v19
mm v[48:51],a[214:217],a[24:27],v[48:51]
cv v138,v20,v21
cv v139,v22,v23
st v[136:139],v244,s[44:47],s91 offen offset:2048 sc1
v_mul_f32 v24, v24, v250
v_mul_f32 v25, v25, v250
mm v[52:55],a[212:215],a[24:27],v[52:55]
v_mul_f32 v26, v26, v250
v_mul_f32 v27, v27, v250
v_mul_f32 v28, v28, v250
v_mul_f32 v29, v29, v250
v_mul_f32 v30, v30, v250
v_mul_f32 v31, v31, v250
mm v[56:59],a[214:217],a[28:31],v[56:59]
cv v140,v24,v25
cv v141,v26,v27
cv v142,v28,v29
cv v143,v30,v31
st v[140:143],v244,s[44:47],s91 offen offset:3072 sc1
mm v[60:63],a[212:215],a[28:31],v[60:63]
v_mul_f32 v32, v32, v249
v_mul_f32 v33, v33, v249
v_mul_f32 v34, v34, v249
v_mul_f32 v35, v35, v249
v_mul_f32 v36, v36, v249
v_mul_f32 v37, v37, v249
v_smfmac_f32_16x16x64_bf16 a[196:199], a[208:211], v[232:239], v241
v_mul_f32 v38, v38, v249
v_mul_f32 v39, v39, v249
cv v144,v32,v33
cv v145,v34,v35
cv v146,v36,v37
mm v[64:67],a[96:99],v[232:235],v[64:67]
cv v147,v38,v39
st v[144:147],v245,s[44:47],s91 offen offset:0 sc1
v_mul_f32 v40, v40, v249
v_mul_f32 v41, v41, v249
v_mul_f32 v42, v42, v249
mm v[96:99],a[96:99],v[236:239],v[96:99]
v_mul_f32 v43, v43, v249
v_mul_f32 v44, v44, v249
v_mul_f32 v45, v45, v249
v_mul_f32 v46, v46, v249
v_mul_f32 v47, v47, v249
cv v148,v40,v41
mm v[100:103],a[100:103],v[236:239],v[100:103]
cv v149,v42,v43
cv v150,v44,v45
cv v151,v46,v47
st v[148:151],v245,s[44:47],s91 offen offset:1024 sc1
v_mul_f32 v48, v48, v249
mm v[68:71],a[100:103],v[232:235],v[68:71]
v_mul_f32 v49, v49, v249
v_mul_f32 v50, v50, v249
v_mul_f32 v51, v51, v249
v_mul_f32 v52, v52, v249
v_mul_f32 v53, v53, v249
v_mul_f32 v54, v54, v249
mm v[72:75],a[104:107],v[232:235],v[72:75]
v_mul_f32 v55, v55, v249
cv v152,v48,v49
cv v153,v50,v51
cv v154,v52,v53
cv v155,v54,v55
st v[152:155],v245,s[44:47],s91 offen offset:2048 sc1
mm v[104:107],a[104:107],v[236:239],v[104:107]
v_mul_f32 v56, v56, v249
v_mul_f32 v57, v57, v249
v_mul_f32 v58, v58, v249
v_mul_f32 v59, v59, v249
v_mul_f32 v60, v60, v249
v_mul_f32 v61, v61, v249
mm v[108:111],a[108:111],v[236:239],v[108:111]
v_mul_f32 v62, v62, v249
v_mul_f32 v63, v63, v249
cv v156,v56,v57
cv v157,v58,v59
cv v158,v60,v61
cv v159,v62,v63
mm v[76:79],a[108:111],v[232:235],v[76:79]
st v[156:159],v245,s[44:47],s91 offen offset:3072 sc1
ar v240,a224
v_xor_b32 v241, 32, v240
v_xor_b32 v242, 64, v240
v_xor_b32 v243, 96, v240
mm v[80:83],a[112:115],v[232:235],v[80:83]
mm v[112:115],a[112:115],v[236:239],v[112:115]
mm v[116:119],a[116:119],v[236:239],v[116:119]
mm v[84:87],a[116:119],v[232:235],v[84:87]
mm v[88:91],a[120:123],v[232:235],v[88:91]
mm v[120:123],a[120:123],v[236:239],v[120:123]
mm v[124:127],a[124:127],v[236:239],v[124:127]
mm v[92:95],a[124:127],v[232:235],v[92:95]
s_nop 0
s_barrier
wt vmcnt(8)
mm v[64:67],a[214:217],a[32:35],v[64:67]
rt v[192:193],v240 offset:16896
rt v[194:195],v240 offset:17920
mm v[68:71],a[212:215],a[32:35],v[68:71]
rt v[196:197],v240 offset:25344
rt v[198:199],v240 offset:26368
mm v[72:75],a[214:217],a[36:39],v[72:75]
rt v[200:201],v241 offset:16896
rt v[202:203],v241 offset:17920
ar v250,a196
ar v251,a197
v_add_f32 v250, v250, v251
ar v251,a222
mm v[76:79],a[212:215],a[36:39],v[76:79]
rt v[204:205],v241 offset:25344
rt v[206:207],v241 offset:26368
v_add_f32 v250, v250, v251
ar v249,a198
ar v251,a199
v_add_f32 v249, v249, v251
mm v[80:83],a[214:217],a[40:43],v[80:83]
rt v[208:209],v242 offset:16896
rt v[210:211],v242 offset:17920
ar v251,a223
v_add_f32 v249, v249, v251
v_subrev_u32 v251, 562036736, v250
mm v[84:87],a[212:215],a[40:43],v[84:87]
rt v[212:213],v242 offset:25344
rt v[214:215],v242 offset:26368
v_rcp_f32 v250, v250
v_cmp_le_u32 vcc, 1006632960, v251
v_subrev_u32 v251, 562036736, v249
mm v[88:91],a[214:217],a[44:47],v[88:91]
rt v[216:217],v243 offset:16896
rt v[218:219],v243 offset:17920
s_mov_b64 s[92:93], vcc
v_cmp_le_u32 vcc, 1006632960, v251
v_rcp_f32 v249, v249
mm v[92:95],a[212:215],a[44:47],v[92:95]
rt v[220:221],v243 offset:25344
rt v[222:223],v243 offset:26368
s_or_b64 vcc, vcc, s[92:93]
s_cbranch_scc1 .La5_LA_SC4
.La5_LA_SR4:
v_mul_f32 v64, v64, v250
v_mul_f32 v65, v65, v250
mm v[96:99],a[214:217],a[48:51],v[96:99]
v_mul_f32 v66, v66, v250
v_mul_f32 v67, v67, v250
v_mul_f32 v68, v68, v250
v_mul_f32 v69, v69, v250
v_mul_f32 v70, v70, v250
v_mul_f32 v71, v71, v250
mm v[100:103],a[212:215],a[48:51],v[100:103]
cv v160,v64,v65
cv v161,v66,v67
cv v162,v68,v69
cv v163,v70,v71
st v[160:163],v246,s[44:47],s91 offen offset:0 sc1
mm v[104:107],a[214:217],a[52:55],v[104:107]
v_mul_f32 v72, v72, v250
v_mul_f32 v73, v73, v250
v_mul_f32 v74, v74, v250
v_mul_f32 v75, v75, v250
v_mul_f32 v76, v76, v250
v_mul_f32 v77, v77, v250
mm v[108:111],a[212:215],a[52:55],v[108:111]
v_mul_f32 v78, v78, v250
v_mul_f32 v79, v79, v250
cv v164,v72,v73
cv v165,v74,v75
cv v166,v76,v77
cv v167,v78,v79
mm v[112:115],a[214:217],a[56:59],v[112:115]
st v[164:167],v246,s[44:47],s91 offen offset:1024 sc1
v_mul_f32 v80, v80, v250
v_mul_f32 v81, v81, v250
v_mul_f32 v82, v82, v250
v_mul_f32 v83, v83, v250
mm v[116:119],a[212:215],a[56:59],v[116:119]
v_mul_f32 v84, v84, v250
v_mul_f32 v85, v85, v250
v_mul_f32 v86, v86, v250
v_mul_f32 v87, v87, v250
cv v168,v80,v81
cv v169,v82,v83
mm v[120:123],a[214:217],a[60:63],v[120:123]
cv v170,v84,v85
cv v171,v86,v87
st v[168:171],v246,s[44:47],s91 offen offset:2048 sc1
v_mul_f32 v88, v88, v250
v_mul_f32 v89, v89, v250
mm v[124:127],a[212:215],a[60:63],v[124:127]
wt lgkmcnt(12)
v_mul_f32 v90, v90, v250
v_mul_f32 v91, v91, v250
v_mul_f32 v92, v92, v250
v_mul_f32 v93, v93, v250
v_mul_f32 v94, v94, v250
mm v[0:3],v[192:195],v[128:131],0
v_mul_f32 v95, v95, v250
cv v172,v88,v89
cv v173,v90,v91
cv v174,v92,v93
cv v175,v94,v95
st v[172:175],v246,s[44:47],s91 offen offset:3072 sc1
mm v[0:3],v[196:199],v[132:135],v[0:3]
v_mul_f32 v96, v96, v249
v_mul_f32 v97, v97, v249
v_mul_f32 v98, v98, v249
v_mul_f32 v99, v99, v249
v_mul_f32 v100, v100, v249
v_mul_f32 v101, v101, v249
mm v[32:35],v[196:199],v[148:151],0
wt lgkmcnt(8)
v_mul_f32 v102, v102, v249
v_mul_f32 v103, v103, v249
cv v176,v96,v97
cv v177,v98,v99
cv v178,v100,v101
mm v[32:35],v[192:195],v[144:147],v[32:35]
rt v[196:197],v240 offset:17024
rt v[198:199],v240 offset:18048
cv v179,v102,v103
st v[176:179],v247,s[44:47],s91 offen offset:0 sc1
v_mul_f32 v104, v104, v249
mm v[36:39],v[200:203],v[144:147],0
v_mul_f32 v105, v105, v249
v_mul_f32 v106, v106, v249
v_mul_f32 v107, v107, v249
v_mul_f32 v108, v108, v249
v_mul_f32 v109, v109, v249
v_mul_f32 v110, v110, v249
mm v[36:39],v[204:207],v[148:151],v[36:39]
rt v[192:193],v240 offset:25472
rt v[194:195],v240 offset:26496
v_mul_f32 v111, v111, v249
cv v180,v104,v105
cv v181,v106,v107
cv v182,v108,v109
mm v[4:7],v[204:207],v[132:135],0
wt lgkmcnt(4)
cv v183,v110,v111
st v[180:183],v247,s[44:47],s91 offen offset:1024 sc1
v_mul_f32 v112, v112, v249
v_mul_f32 v113, v113, v249
mm v[4:7],v[200:203],v[128:131],v[4:7]
rt v[204:205],v241 offset:17024
rt v[206:207],v241 offset:18048
v_mul_f32 v114, v114, v249
v_mul_f32 v115, v115, v249
v_mul_f32 v116, v116, v249
v_mul_f32 v117, v117, v249
mm v[8:11],v[208:211],v[128:131],0
v_mul_f32 v118, v118, v249
v_mul_f32 v119, v119, v249
cv v184,v112,v113
cv v185,v114,v115
cv v186,v116,v117
cv v187,v118,v119
mm v[8:11],v[212:215],v[132:135],v[8:11]
rt v[200:201],v241 offset:25472
rt v[202:203],v241 offset:26496
st v[184:187],v247,s[44:47],s91 offen offset:2048 sc1
v_mul_f32 v120, v120, v249
v_mul_f32 v121, v121, v249
mm v[40:43],v[212:215],v[148:151],0
v_mul_f32 v122, v122, v249
v_mul_f32 v123, v123, v249
v_mul_f32 v124, v124, v249
v_mul_f32 v125, v125, v249
v_mul_f32 v126, v126, v249
v_mul_f32 v127, v127, v249
mm v[40:43],v[208:211],v[144:147],v[40:43]
rt v[212:213],v242 offset:17024
rt v[214:215],v242 offset:18048
cv v188,v120,v121
cv v189,v122,v123
cv v190,v124,v125
cv v191,v126,v127
mm v[44:47],v[216:219],v[144:147],0
st v[188:191],v247,s[44:47],s91 offen offset:3072 sc1
mm v[44:47],v[220:223],v[148:151],v[44:47]
rt v[208:209],v242 offset:25472
rt v[210:211],v242 offset:26496
mm v[12:15],v[220:223],v[132:135],0
wt lgkmcnt(4)
mm v[12:15],v[216:219],v[128:131],v[12:15]
rt v[220:221],v243 offset:17024
rt v[222:223],v243 offset:18048
mm v[16:19],v[196:199],v[128:131],0
ar v224,a228
v_add_u32 v225, 0x8000, v224
v_add_u32 v226, 0x2000, v224
v_add_u32 v227, 0xa000, v224
mm v[16:19],v[192:195],v[132:135],v[16:19]
rt v[216:217],v243 offset:25472
rt v[218:219],v243 offset:26496
ar v248,a225
ar v228,a229
mm v[48:51],v[192:195],v[148:151],0
ar v229,a230
mm v[48:51],v[196:199],v[144:147],v[48:51]
rt v[192:193],v240 offset:33792
rt v[194:195],v240 offset:34816
mm v[52:55],v[204:207],v[144:147],0
mm v[52:55],v[200:203],v[148:151],v[52:55]
rt v[196:197],v240 offset:42240
rt v[198:199],v240 offset:43264
mm v[20:23],v[200:203],v[132:135],0
wt lgkmcnt(4)
mm v[20:23],v[204:207],v[128:131],v[20:23]
rt v[200:201],v241 offset:33792
rt v[202:203],v241 offset:34816
mm v[24:27],v[212:215],v[128:131],0
mm v[24:27],v[208:211],v[132:135],v[24:27]
rt v[204:205],v241 offset:42240
rt v[206:207],v241 offset:43264
mm v[56:59],v[208:211],v[148:151],0
mm v[56:59],v[212:215],v[144:147],v[56:59]
rt v[208:209],v242 offset:33792
rt v[210:211],v242 offset:34816
mm v[60:63],v[220:223],v[144:147],0
mm v[60:63],v[216:219],v[148:151],v[60:63]
rt v[212:213],v242 offset:42240
rt v[214:215],v242 offset:43264
mm v[28:31],v[216:219],v[132:135],0
wt lgkmcnt(4)
mm v[28:31],v[220:223],v[128:131],v[28:31]
rt v[216:217],v243 offset:33792
rt v[218:219],v243 offset:34816
mm v[0:3],v[192:195],v[136:139],v[0:3]
ar v249,a231
s_bitcmp1_b32 2863311530, s2
s_cbranch_scc1 .La5_LO_VC1
s_getpc_b64 s[24:25]
.La5_LW3_P1:
s_add_u32 s24, s24, .La5_LW3_R1-.La5_LW3_P1
s_addc_u32 s25, s25, 0
s_mov_b64 exec, 0
s_cmp_eq_u32 s3, 0
s_cbranch_scc1 .La5_LW3_0
s_cmp_eq_u32 s3, 1
s_cbranch_scc1 .La5_LW3_1
s_cmp_eq_u32 s3, 2
s_cbranch_scc1 .La5_LW3_2
s_branch .La5_LW3_3
.La5_LW3_R1:
s_mov_b64 exec, -1
.La5_LO_VE1:
wt vmcnt(0)
buffer_store_dwordx2 v[252:253], v249, s[64:67], s6 offen offset:0 sc1
mm v[0:3],v[196:199],v[140:143],v[0:3]
rt v[220:221],v243 offset:42240
rt v[222:223],v243 offset:43264
mm v[32:35],v[196:199],v[156:159],v[32:35]
mm v[32:35],v[192:195],v[152:155],v[32:35]
rt v[196:197],v240 offset:33920
rt v[198:199],v240 offset:34944
mm v[36:39],v[200:203],v[152:155],v[36:39]
mm v[36:39],v[204:207],v[156:159],v[36:39]
rt v[192:193],v240 offset:42368
rt v[194:195],v240 offset:43392
mm v[4:7],v[204:207],v[140:143],v[4:7]
wt lgkmcnt(4)
mm v[4:7],v[200:203],v[136:139],v[4:7]
rt v[204:205],v241 offset:33920
rt v[206:207],v241 offset:34944
mm v[8:11],v[208:211],v[136:139],v[8:11]
mm v[8:11],v[212:215],v[140:143],v[8:11]
rt v[200:201],v241 offset:42368
rt v[202:203],v241 offset:43392
mm v[40:43],v[212:215],v[156:159],v[40:43]
mm v[40:43],v[208:211],v[152:155],v[40:43]
rt v[212:213],v242 offset:33920
rt v[214:215],v242 offset:34944
mm v[44:47],v[216:219],v[152:155],v[44:47]
mm v[44:47],v[220:223],v[156:159],v[44:47]
rt v[208:209],v242 offset:42368
rt v[210:211],v242 offset:43392
mm v[12:15],v[220:223],v[140:143],v[12:15]
wt lgkmcnt(4)
mm v[12:15],v[216:219],v[136:139],v[12:15]
rt v[220:221],v243 offset:33920
rt v[222:223],v243 offset:34944
mm v[16:19],v[196:199],v[136:139],v[16:19]
mm v[16:19],v[192:195],v[140:143],v[16:19]
rt v[216:217],v243 offset:42368
rt v[218:219],v243 offset:43392
mm v[48:51],v[192:195],v[156:159],v[48:51]
mm v[48:51],v[196:199],v[152:155],v[48:51]
rt v[192:193],v240 offset:16896
rt v[194:195],v240 offset:17920
mm v[52:55],v[204:207],v[152:155],v[52:55]
mm v[52:55],v[200:203],v[156:159],v[52:55]
rt v[196:197],v240 offset:25344
rt v[198:199],v240 offset:26368
mm v[20:23],v[200:203],v[140:143],v[20:23]
wt lgkmcnt(4)
mm v[20:23],v[204:207],v[136:139],v[20:23]
rt v[200:201],v241 offset:16896
rt v[202:203],v241 offset:17920
mm v[24:27],v[212:215],v[136:139],v[24:27]
mm v[24:27],v[208:211],v[140:143],v[24:27]
rt v[204:205],v241 offset:25344
rt v[206:207],v241 offset:26368
mm v[56:59],v[208:211],v[156:159],v[56:59]
mm v[56:59],v[212:215],v[152:155],v[56:59]
rt v[208:209],v242 offset:16896
rt v[210:211],v242 offset:17920
mm v[60:63],v[220:223],v[152:155],v[60:63]
mm v[60:63],v[216:219],v[156:159],v[60:63]
rt v[212:213],v242 offset:25344
rt v[214:215],v242 offset:26368
mm v[28:31],v[216:219],v[140:143],v[28:31]
wt lgkmcnt(4)
mm v[28:31],v[220:223],v[136:139],v[28:31]
rt v[216:217],v243 offset:16896
rt v[218:219],v243 offset:17920
s_cmp_lg_u32 s30, 0
s_nop 0
s_nop 0
s_cbranch_scc1 .La5_LO_W
; ---- O projection: 256 x 128 tile over eight heads
.La5_LO_START:
.La5_LW6_7:
s_cbranch_execz .La5_LW6_R0
s_bitcmp1_b32 2863311530, s2
s_cbranch_scc1 .La5_LO_SC
.La5_LO_SB:
mm v[64:67],v[192:195],v[160:163],0
buffer_load_dwordx2 v[250:251], v248, s[64:67], s5 offen sc1
mm v[64:67],v[196:199],v[164:167],v[64:67]
rt v[220:221],v243 offset:25344
rt v[222:223],v243 offset:26368
mm v[96:99],v[196:199],v[180:183],0
s_add_u32 m0, s83, 0
s_bfe_u32 s80, s4, 262148
mm v[96:99],v[192:195],v[176:179],v[96:99]
rt v[196:197],v240 offset:17024
rt v[198:199],v240 offset:18048
s_xor_b32 s80, s80, s72
s_lshl_b32 s80, s80, 18
s_add_u32 s80, s80, 131072
mm v[100:103],v[200:203],v[176:179],0
ld v224,s[56:59],s80 offen lds
mm v[100:103],v[204:207],v[180:183],v[100:103]
rt v[192:193],v240 offset:25472
rt v[194:195],v240 offset:26496
mm v[68:71],v[204:207],v[164:167],0
s_add_u32 m0, s83, 1024
wt lgkmcnt(4)
mm v[68:71],v[200:203],v[160:163],v[68:71]
rt v[204:205],v241 offset:17024
rt v[206:207],v241 offset:18048
mm v[72:75],v[208:211],v[160:163],0
ld v225,s[56:59],s80 offen lds
mm v[72:75],v[212:215],v[164:167],v[72:75]
rt v[200:201],v241 offset:25472
rt v[202:203],v241 offset:26496
mm v[104:107],v[212:215],v[180:183],0
s_add_u32 m0, s83, 2176
s_bfe_u32 s53, s4, 262148
mm v[104:107],v[208:211],v[176:179],v[104:107]
rt v[212:213],v242 offset:17024
rt v[214:215],v242 offset:18048
mm v[108:111],v[216:219],v[176:179],0
ld v226,s[56:59],s80 offen lds
mm v[108:111],v[220:223],v[180:183],v[108:111]
rt v[208:209],v242 offset:25472
rt v[210:211],v242 offset:26496
mm v[76:79],v[220:223],v[164:167],0
s_add_u32 m0, s83, 3200
wt lgkmcnt(4)
mm v[76:79],v[216:219],v[160:163],v[76:79]
rt v[220:221],v243 offset:17024
rt v[222:223],v243 offset:18048
mm v[80:83],v[196:199],v[160:163],0
ld v227,s[56:59],s80 offen lds
mm v[80:83],v[192:195],v[164:167],v[80:83]
rt v[216:217],v243 offset:25472
rt v[218:219],v243 offset:26496
mm v[112:115],v[192:195],v[180:183],0
s_add_u32 m0, s83, 118272
s_bfe_u32 s80, s4, 262152
mm v[112:115],v[196:199],v[176:179],v[112:115]
rt v[192:193],v240 offset:33792
rt v[194:195],v240 offset:34816
s_xor_b32 s80, s80, s72
s_lshl_b32 s80, s80, 18
mm v[116:119],v[204:207],v[176:179],0
ld v224,s[56:59],s80 offen lds
mm v[116:119],v[200:203],v[180:183],v[116:119]
rt v[196:197],v240 offset:42240
rt v[198:199],v240 offset:43264
mm v[84:87],v[200:203],v[164:167],0
s_add_u32 m0, s83, 119296
wt lgkmcnt(4)
mm v[84:87],v[204:207],v[160:163],v[84:87]
rt v[200:201],v241 offset:33792
rt v[202:203],v241 offset:34816
mm v[88:91],v[212:215],v[160:163],0
ld v225,s[56:59],s80 offen lds
mm v[88:91],v[208:211],v[164:167],v[88:91]
rt v[204:205],v241 offset:42240
rt v[206:207],v241 offset:43264
mm v[120:123],v[208:211],v[180:183],0
s_add_u32 m0, s83, 120448
s_xor_b32 s53, s53, s72
mm v[120:123],v[212:215],v[176:179],v[120:123]
rt v[208:209],v242 offset:33792
rt v[210:211],v242 offset:34816
mm v[124:127],v[220:223],v[176:179],0
ld v226,s[56:59],s80 offen lds
mm v[124:127],v[216:219],v[180:183],v[124:127]
rt v[212:213],v242 offset:42240
rt v[214:215],v242 offset:43264
mm v[92:95],v[216:219],v[164:167],0
wt vmcnt(7)
v_cmp_eq_u64 vcc, v[250:251], v[252:253]
s_nop 0
s_bitcmp1_b32 vcc_lo, 1
s_cbranch_scc0 .La5_LO_NB1
.La5_LO_KB1:
wt lgkmcnt(4)
mm v[92:95],v[220:223],v[160:163],v[92:95]
rt v[216:217],v243 offset:33792
rt v[218:219],v243 offset:34816
s_lshl_b32 s53, s53, 13
mm v[64:67],v[192:195],v[168:171],v[64:67]
ld a[0:3],v244,s[44:47],s53 offen offset:0 sc1
mm v[64:67],v[196:199],v[172:175],v[64:67]
rt v[220:221],v243 offset:42240
rt v[222:223],v243 offset:43264
mm v[96:99],v[196:199],v[188:191],v[96:99]
ld a[4:7],v245,s[44:47],s53 offen offset:0 sc1
mm v[96:99],v[192:195],v[184:187],v[96:99]
rt v[196:197],v240 offset:33920
rt v[198:199],v240 offset:34944
mm v[100:103],v[200:203],v[184:187],v[100:103]
ld a[8:11],v246,s[44:47],s53 offen offset:0 sc1
mm v[100:103],v[204:207],v[188:191],v[100:103]
rt v[192:193],v240 offset:42368
rt v[194:195],v240 offset:43392
mm v[68:71],v[204:207],v[172:175],v[68:71]
wt lgkmcnt(4)
s_bfe_u32 s54, s4, 262152
mm v[68:71],v[200:203],v[168:171],v[68:71]
rt v[204:205],v241 offset:33920
rt v[206:207],v241 offset:34944
mm v[72:75],v[208:211],v[168:171],v[72:75]
ld a[12:15],v247,s[44:47],s53 offen offset:0 sc1
mm v[72:75],v[212:215],v[172:175],v[72:75]
rt v[200:201],v241 offset:42368
rt v[202:203],v241 offset:43392
mm v[104:107],v[212:215],v[188:191],v[104:107]
ld a[16:19],v244,s[44:47],s53 offen offset:1024 sc1
mm v[104:107],v[208:211],v[184:187],v[104:107]
rt v[212:213],v242 offset:33920
rt v[214:215],v242 offset:34944
mm v[108:111],v[216:219],v[184:187],v[108:111]
ld a[20:23],v245,s[44:47],s53 offen offset:1024 sc1
mm v[108:111],v[220:223],v[188:191],v[108:111]
rt v[208:209],v242 offset:42368
rt v[210:211],v242 offset:43392
mm v[76:79],v[220:223],v[172:175],v[76:79]
s_nop 0
s_barrier
wt lgkmcnt(4)
mm v[76:79],v[216:219],v[168:171],v[76:79]
rt v[220:221],v243 offset:33920
rt v[222:223],v243 offset:34944
mm v[80:83],v[196:199],v[168:171],v[80:83]
ld a[24:27],v246,s[44:47],s53 offen offset:1024 sc1
mm v[80:83],v[192:195],v[172:175],v[80:83]
rt v[216:217],v243 offset:42368
rt v[218:219],v243 offset:43392
mm v[112:115],v[192:195],v[188:191],v[112:115]
ld a[28:31],v247,s[44:47],s53 offen offset:1024 sc1
mm v[112:115],v[196:199],v[184:187],v[112:115]
rt v[192:193],v240 offset:50688
rt v[194:195],v240 offset:51712
mm v[116:119],v[204:207],v[184:187],v[116:119]
ld a[32:35],v244,s[44:47],s53 offen offset:2048 sc1
mm v[116:119],v[200:203],v[188:191],v[116:119]
rt v[196:197],v240 offset:59136
rt v[198:199],v240 offset:60160
mm v[84:87],v[200:203],v[172:175],v[84:87]
wt lgkmcnt(4)
s_xor_b32 s54, s54, s72
mm v[84:87],v[204:207],v[168:171],v[84:87]
rt v[200:201],v241 offset:50688
rt v[202:203],v241 offset:51712
mm v[88:91],v[212:215],v[168:171],v[88:91]
ld a[36:39],v245,s[44:47],s53 offen offset:2048 sc1
mm v[88:91],v[208:211],v[172:175],v[88:91]
rt v[204:205],v241 offset:59136
rt v[206:207],v241 offset:60160
mm v[120:123],v[208:211],v[188:191],v[120:123]
ld a[40:43],v246,s[44:47],s53 offen offset:2048 sc1
mm v[120:123],v[212:215],v[184:187],v[120:123]
rt v[208:209],v242 offset:50688
rt v[210:211],v242 offset:51712
mm v[124:127],v[220:223],v[184:187],v[124:127]
.La5_LW3_0:
s_cbranch_execz .La5_LW3_4
ld a[44:47],v247,s[44:47],s53 offen offset:2048 sc1
mm v[124:127],v[216:219],v[188:191],v[124:127]
rt v[212:213],v242 offset:59136
rt v[214:215],v242 offset:60160
mm v[92:95],v[216:219],v[172:175],v[92:95]
wt vmcnt(4) lgkmcnt(4)
s_lshl_b32 s54, s54, 13
mm v[92:95],v[220:223],v[168:171],v[92:95]
rt v[216:217],v243 offset:50688
rt v[218:219],v243 offset:51712
mm v[0:3],v[192:195],a[0:3],v[0:3]
s_bfe_u32 s55, s4, 262156
s_xor_b32 s55, s55, s72
mm v[0:3],v[196:199],a[16:19],v[0:3]
ld a[48:51],v244,s[44:47],s53 offen offset:3072 sc1
mm v[32:35],v[196:199],a[20:23],v[32:35]
s_lshl_b32 s55, s55, 13
s_bfe_u32 s52, s4, 262160
mm v[32:35],v[192:195],a[4:7],v[32:35]
rt v[220:221],v243 offset:59136
rt v[222:223],v243 offset:60160
mm v[64:67],v[192:195],a[8:11],v[64:67]
s_xor_b32 s52, s52, s72
s_lshl_b32 s52, s52, 13
mm v[64:67],v[196:199],a[24:27],v[64:67]
ld a[52:55],v245,s[44:47],s53 offen offset:3072 sc1
mm v[96:99],v[196:199],a[28:31],v[96:99]
mm v[96:99],v[192:195],a[12:15],v[96:99]
rt v[196:197],v240 offset:50816
rt v[198:199],v240 offset:51840
mm v[100:103],v[200:203],a[12:15],v[100:103]
mm v[100:103],v[204:207],a[28:31],v[100:103]
buffer_load_dwordx2 v[250:251], v248, s[64:67], s5 offen sc1
mm v[68:71],v[204:207],a[24:27],v[68:71]
mm v[68:71],v[200:203],a[8:11],v[68:71]
rt v[192:193],v240 offset:59264
rt v[194:195],v240 offset:60288
mm v[36:39],v[200:203],a[4:7],v[36:39]
mm v[36:39],v[204:207],a[20:23],v[36:39]
ld a[56:59],v246,s[44:47],s53 offen offset:3072 sc1
mm v[4:7],v[204:207],a[16:19],v[4:7]
wt lgkmcnt(4)
mm v[4:7],v[200:203],a[0:3],v[4:7]
rt v[204:205],v241 offset:50816
rt v[206:207],v241 offset:51840
mm v[8:11],v[208:211],a[0:3],v[8:11]
mm v[8:11],v[212:215],a[16:19],v[8:11]
ld a[60:63],v247,s[44:47],s53 offen offset:3072 sc1
mm v[40:43],v[212:215],a[20:23],v[40:43]
s_bfe_u32 s53, s4, 262164
s_xor_b32 s53, s53, s72
mm v[40:43],v[208:211],a[4:7],v[40:43]
rt v[200:201],v241 offset:59264
rt v[202:203],v241 offset:60288
mm v[72:75],v[208:211],a[8:11],v[72:75]
s_add_u32 m0, s83, 121472
s_lshl_b32 s53, s53, 13
mm v[72:75],v[212:215],a[24:27],v[72:75]
ld v227,s[56:59],s80 offen lds
mm v[104:107],v[212:215],a[28:31],v[104:107]
s_add_u32 s80, s80, 131072
mm v[104:107],v[208:211],a[12:15],v[104:107]
rt v[212:213],v242 offset:50816
rt v[214:215],v242 offset:51840
mm v[108:111],v[216:219],a[12:15],v[108:111]
s_add_u32 m0, s83, 67584
mm v[108:111],v[220:223],a[28:31],v[108:111]
ld v224,s[56:59],s80 offen lds
mm v[76:79],v[220:223],a[24:27],v[76:79]
mm v[76:79],v[216:219],a[8:11],v[76:79]
rt v[208:209],v242 offset:59264
rt v[210:211],v242 offset:60288
mm v[44:47],v[216:219],a[4:7],v[44:47]
s_add_u32 m0, s83, 68608
mm v[44:47],v[220:223],a[20:23],v[44:47]
ld v225,s[56:59],s80 offen lds
mm v[12:15],v[220:223],a[16:19],v[12:15]
wt lgkmcnt(4)
mm v[12:15],v[216:219],a[0:3],v[12:15]
rt v[220:221],v243 offset:50816
rt v[222:223],v243 offset:51840
mm v[16:19],v[196:199],a[0:3],v[16:19]
s_add_u32 m0, s83, 69760
mm v[16:19],v[192:195],a[16:19],v[16:19]
ld v226,s[56:59],s80 offen lds
mm v[48:51],v[192:195],a[20:23],v[48:51]
mm v[48:51],v[196:199],a[4:7],v[48:51]
rt v[216:217],v243 offset:59264
rt v[218:219],v243 offset:60288
mm v[80:83],v[196:199],a[8:11],v[80:83]
s_add_u32 m0, s83, 70784
s_nop 0
s_barrier
mm v[80:83],v[192:195],a[24:27],v[80:83]
ld v227,s[56:59],s80 offen lds
mm v[112:115],v[192:195],a[28:31],v[112:115]
s_bfe_u32 s80, s4, 262156
s_xor_b32 s80, s80, s72
mm v[112:115],v[196:199],a[12:15],v[112:115]
rt v[192:193],v240 offset:0
rt v[194:195],v240 offset:1024
mm v[116:119],v[204:207],a[12:15],v[116:119]
s_add_u32 m0, s83, 84480
s_lshl_b32 s80, s80, 18
mm v[116:119],v[200:203],a[28:31],v[116:119]
ld v224,s[56:59],s80 offen lds
mm v[84:87],v[200:203],a[24:27],v[84:87]
mm v[84:87],v[204:207],a[8:11],v[84:87]
rt v[196:197],v240 offset:8448
rt v[198:199],v240 offset:9472
mm v[52:55],v[204:207],a[4:7],v[52:55]
s_add_u32 m0, s83, 85504
mm v[52:55],v[200:203],a[20:23],v[52:55]
ld v225,s[56:59],s80 offen lds
mm v[20:23],v[200:203],a[16:19],v[20:23]
wt lgkmcnt(4)
mm v[20:23],v[204:207],a[0:3],v[20:23]
rt v[200:201],v241 offset:0
rt v[202:203],v241 offset:1024
mm v[24:27],v[212:215],a[0:3],v[24:27]
s_add_u32 m0, s83, 86656
mm v[24:27],v[208:211],a[16:19],v[24:27]
ld v226,s[56:59],s80 offen lds
mm v[56:59],v[208:211],a[20:23],v[56:59]
wt vmcnt(10)
v_cmp_eq_u64 vcc, v[250:251], v[252:253]
mm v[56:59],v[212:215],a[4:7],v[56:59]
rt v[204:205],v241 offset:8448
rt v[206:207],v241 offset:9472
mm v[88:91],v[212:215],a[8:11],v[88:91]
s_add_u32 m0, s83, 87680
mm v[88:91],v[208:211],a[24:27],v[88:91]
ld v227,s[56:59],s80 offen lds
mm v[120:123],v[208:211],a[28:31],v[120:123]
s_bitcmp1_b32 vcc_lo, 2
s_nop 0
s_nop 0
s_cbranch_scc0 .La5_LO_NB2
.La5_LO_KB2:
mm v[120:123],v[212:215],a[12:15],v[120:123]
rt v[208:209],v242 offset:0
rt v[210:211],v242 offset:1024
mm v[124:127],v[220:223],a[12:15],v[124:127]
s_add_u32 s80, s80, 131072
mm v[124:127],v[216:219],a[28:31],v[124:127]
ld a[12:15],v244,s[44:47],s54 offen offset:0 sc1
mm v[92:95],v[216:219],a[24:27],v[92:95]
mm v[92:95],v[220:223],a[8:11],v[92:95]
rt v[212:213],v242 offset:8448
rt v[214:215],v242 offset:9472
mm v[60:63],v[220:223],a[4:7],v[60:63]
mm v[60:63],v[216:219],a[20:23],v[60:63]
mm v[28:31],v[216:219],a[16:19],v[28:31]
wt vmcnt(10) lgkmcnt(4)
mm v[28:31],v[220:223],a[0:3],v[28:31]
rt v[216:217],v243 offset:0
rt v[218:219],v243 offset:1024
mm v[0:3],v[192:195],a[32:35],v[0:3]
mm v[0:3],v[196:199],a[48:51],v[0:3]
ld a[0:3],v245,s[44:47],s54 offen offset:0 sc1
mm v[32:35],v[196:199],a[52:55],v[32:35]
mm v[32:35],v[192:195],a[36:39],v[32:35]
rt v[220:221],v243 offset:8448
rt v[222:223],v243 offset:9472
mm v[64:67],v[192:195],a[40:43],v[64:67]
mm v[64:67],v[196:199],a[56:59],v[64:67]
ld a[4:7],v246,s[44:47],s54 offen offset:0 sc1
mm v[96:99],v[196:199],a[60:63],v[96:99]
mm v[96:99],v[192:195],a[44:47],v[96:99]
rt v[196:197],v240 offset:128
rt v[198:199],v240 offset:1152
mm v[100:103],v[200:203],a[44:47],v[100:103]
s_mov_b32 s86, 0x05040100
s_mov_b32 s87, 0x07060302
mm v[100:103],v[204:207],a[60:63],v[100:103]
ld a[8:11],v247,s[44:47],s54 offen offset:0 sc1
mm v[68:71],v[204:207],a[56:59],v[68:71]
s_nop 0
ar v230,a128
mm v[68:71],v[200:203],a[40:43],v[68:71]
rt v[192:193],v240 offset:8576
rt v[194:195],v240 offset:9600
mm v[36:39],v[200:203],a[36:39],v[36:39]
v_add_u32 v240, 67584, v240
ar v231,a132
mm v[36:39],v[204:207],a[52:55],v[36:39]
ld a[16:19],v244,s[44:47],s54 offen offset:1024 sc1
mm v[4:7],v[204:207],a[48:51],v[4:7]
wt lgkmcnt(4)
v_perm_b32 v232, v231, v230, s86
mm v[4:7],v[200:203],a[32:35],v[4:7]
rt v[204:205],v241 offset:128
rt v[206:207],v241 offset:1152
mm v[8:11],v[208:211],a[32:35],v[8:11]
v_perm_b32 v233, v231, v230, s87
aw a96,v232
mm v[8:11],v[212:215],a[48:51],v[8:11]
ld a[20:23],v245,s[44:47],s54 offen offset:1024 sc1
mm v[40:43],v[212:215],a[52:55],v[40:43]
aw a100,v233
ar v234,a136
mm v[40:43],v[208:211],a[36:39],v[40:43]
rt v[200:201],v241 offset:8576
rt v[202:203],v241 offset:9600
mm v[72:75],v[208:211],a[40:43],v[72:75]
v_add_u32 v241, 67584, v241
ar v235,a140
mm v[72:75],v[212:215],a[56:59],v[72:75]
ld a[24:27],v246,s[44:47],s54 offen offset:1024 sc1
mm v[104:107],v[212:215],a[60:63],v[104:107]
v_perm_b32 v236, v235, v234, s86
v_perm_b32 v237, v235, v234, s87
mm v[104:107],v[208:211],a[44:47],v[104:107]
rt v[212:213],v242 offset:128
rt v[214:215],v242 offset:1152
mm v[108:111],v[216:219],a[44:47],v[108:111]
aw a97,v236
aw a101,v237
mm v[108:111],v[220:223],a[60:63],v[108:111]
ld a[28:31],v247,s[44:47],s54 offen offset:1024 sc1
mm v[76:79],v[220:223],a[56:59],v[76:79]
ar v230,a144
ar v231,a148
mm v[76:79],v[216:219],a[40:43],v[76:79]
rt v[208:209],v242 offset:8576
rt v[210:211],v242 offset:9600
mm v[44:47],v[216:219],a[36:39],v[44:47]
v_add_u32 v242, 67584, v242
v_perm_b32 v232, v231, v230, s86
mm v[44:47],v[220:223],a[52:55],v[44:47]
v_perm_b32 v233, v231, v230, s87
aw a98,v232
mm v[12:15],v[220:223],a[48:51],v[12:15]
wt lgkmcnt(4)
aw a102,v233
mm v[12:15],v[216:219],a[32:35],v[12:15]
rt v[220:221],v243 offset:128
rt v[222:223],v243 offset:1152
mm v[16:19],v[196:199],a[32:35],v[16:19]
s_add_u32 m0, s83, 101376
ar v234,a152
mm v[16:19],v[192:195],a[48:51],v[16:19]
ld v224,s[56:59],s80 offen lds
mm v[48:51],v[192:195],a[52:55],v[48:51]
ar v235,a156
v_perm_b32 v236, v235, v234, s86
mm v[48:51],v[196:199],a[36:39],v[48:51]
rt v[216:217],v243 offset:8576
rt v[218:219],v243 offset:9600
mm v[80:83],v[196:199],a[40:43],v[80:83]
s_add_u32 m0, s83, 102400
v_add_u32 v243, 67584, v243
mm v[80:83],v[192:195],a[56:59],v[80:83]
ld v225,s[56:59],s80 offen lds
mm v[112:115],v[192:195],a[60:63],v[112:115]
wt vmcnt(14)
s_barrier
mm v[112:115],v[196:199],a[44:47],v[112:115]
rt v[192:193],v240 offset:50688
rt v[194:195],v240 offset:51712
mm v[116:119],v[204:207],a[44:47],v[116:119]
s_add_u32 m0, s83, 103552
v_perm_b32 v237, v235, v234, s87
mm v[116:119],v[200:203],a[60:63],v[116:119]
ld v226,s[56:59],s80 offen lds
mm v[84:87],v[200:203],a[56:59],v[84:87]
aw a99,v236
aw a103,v237
mm v[84:87],v[204:207],a[40:43],v[84:87]
rt v[196:197],v240 offset:59136
rt v[198:199],v240 offset:60160
mm v[52:55],v[204:207],a[36:39],v[52:55]
ar v230,a129
ar v231,a133
mm v[52:55],v[200:203],a[52:55],v[52:55]
ld a[64:67],v244,s[44:47],s54 offen offset:2048 sc1
mm v[20:23],v[200:203],a[48:51],v[20:23]
wt lgkmcnt(4)
v_perm_b32 v232, v231, v230, s86
mm v[20:23],v[204:207],a[32:35],v[20:23]
rt v[200:201],v241 offset:50688
rt v[202:203],v241 offset:51712
mm v[24:27],v[212:215],a[32:35],v[24:27]
v_perm_b32 v233, v231, v230, s87
aw a104,v232
mm v[24:27],v[208:211],a[48:51],v[24:27]
ld a[68:71],v245,s[44:47],s54 offen offset:2048 sc1
mm v[56:59],v[208:211],a[52:55],v[56:59]
aw a108,v233
ar v234,a137
mm v[56:59],v[212:215],a[36:39],v[56:59]
rt v[204:205],v241 offset:59136
rt v[206:207],v241 offset:60160
mm v[88:91],v[212:215],a[40:43],v[88:91]
ar v235,a141
v_perm_b32 v236, v235, v234, s86
mm v[88:91],v[208:211],a[56:59],v[88:91]
ld a[72:75],v246,s[44:47],s54 offen offset:2048 sc1
mm v[120:123],v[208:211],a[60:63],v[120:123]
v_perm_b32 v237, v235, v234, s87
aw a105,v236
mm v[120:123],v[212:215],a[44:47],v[120:123]
rt v[208:209],v242 offset:50688
rt v[210:211],v242 offset:51712
mm v[124:127],v[220:223],a[44:47],v[124:127]
aw a109,v237
ar v230,a145
mm v[124:127],v[216:219],a[60:63],v[124:127]
buffer_load_dwordx2 v[250:251], v248, s[64:67], s5 offen sc1
mm v[92:95],v[216:219],a[56:59],v[92:95]
ar v231,a149
v_perm_b32 v232, v231, v230, s86
mm v[92:95],v[220:223],a[40:43],v[92:95]
rt v[212:213],v242 offset:59136
rt v[214:215],v242 offset:60160
mm v[60:63],v[220:223],a[36:39],v[60:63]
v_perm_b32 v233, v231, v230, s87
aw a106,v232
mm v[60:63],v[216:219],a[52:55],v[60:63]
ld a[36:39],v247,s[44:47],s54 offen offset:2048 sc1
mm v[28:31],v[216:219],a[48:51],v[28:31]
wt vmcnt(8) lgkmcnt(4)
aw a110,v233
mm v[28:31],v[220:223],a[32:35],v[28:31]
rt v[216:217],v243 offset:50688
rt v[218:219],v243 offset:51712
mm v[0:3],v[192:195],a[12:15],v[0:3]
ar v234,a153
ar v235,a157
mm v[0:3],v[196:199],a[16:19],v[0:3]
ld a[32:35],v244,s[44:47],s54 offen offset:3072 sc1
mm v[32:35],v[196:199],a[20:23],v[32:35]
v_perm_b32 v236, v235, v234, s86
v_perm_b32 v237, v235, v234, s87
mm v[32:35],v[192:195],a[0:3],v[32:35]
rt v[220:221],v243 offset:59136
rt v[222:223],v243 offset:60160
mm v[64:67],v[192:195],a[4:7],v[64:67]
aw a107,v236
aw a111,v237
mm v[64:67],v[196:199],a[24:27],v[64:67]
ld a[40:43],v245,s[44:47],s54 offen offset:3072 sc1
mm v[96:99],v[196:199],a[28:31],v[96:99]
ar v230,a130
ar v231,a134
mm v[96:99],v[192:195],a[8:11],v[96:99]
rt v[196:197],v240 offset:50816
rt v[198:199],v240 offset:51840
mm v[100:103],v[200:203],a[8:11],v[100:103]
v_perm_b32 v232, v231, v230, s86
v_perm_b32 v233, v231, v230, s87
mm v[100:103],v[204:207],a[28:31],v[100:103]
ld a[44:47],v246,s[44:47],s54 offen offset:3072 sc1
mm v[68:71],v[204:207],a[24:27],v[68:71]
aw a112,v232
aw a116,v233
mm v[68:71],v[200:203],a[4:7],v[68:71]
rt v[192:193],v240 offset:59264
rt v[194:195],v240 offset:60288
mm v[36:39],v[200:203],a[0:3],v[36:39]
ar v234,a138
ar v235,a142
mm v[36:39],v[204:207],a[20:23],v[36:39]
ld a[48:51],v247,s[44:47],s54 offen offset:3072 sc1
mm v[4:7],v[204:207],a[16:19],v[4:7]
wt lgkmcnt(4)
s_bfe_u32 s54, s4, 262164
mm v[4:7],v[200:203],a[12:15],v[4:7]
rt v[204:205],v241 offset:50816
rt v[206:207],v241 offset:51840
mm v[8:11],v[208:211],a[12:15],v[8:11]
s_add_u32 m0, s83, 104576
s_xor_b32 s54, s54, s72
mm v[8:11],v[212:215],a[16:19],v[8:11]
ld v227,s[56:59],s80 offen lds
mm v[40:43],v[212:215],a[20:23],v[40:43]
s_bitcmp1_b32 vcc_lo, 3
s_cbranch_scc0 .La5_LO_NB3
.La5_LO_KB3:
mm v[40:43],v[208:211],a[0:3],v[40:43]
rt v[200:201],v241 offset:59264
rt v[202:203],v241 offset:60288
mm v[72:75],v[208:211],a[4:7],v[72:75]
s_bfe_u32 s80, s4, 262160
s_xor_b32 s80, s80, s72
mm v[72:75],v[212:215],a[24:27],v[72:75]
ld a[52:55],v244,s[44:47],s55 offen offset:0 sc1
mm v[104:107],v[212:215],a[28:31],v[104:107]
s_lshl_b32 s80, s80, 18
s_lshl_b32 s54, s54, 13
mm v[104:107],v[208:211],a[8:11],v[104:107]
rt v[212:213],v242 offset:50816
rt v[214:215],v242 offset:51840
mm v[108:111],v[216:219],a[8:11],v[108:111]
s_xor_b32 s54, s54, 229376
v_perm_b32 v236, v235, v234, s86
mm v[108:111],v[220:223],a[28:31],v[108:111]
ld a[56:59],v245,s[44:47],s55 offen offset:0 sc1
mm v[76:79],v[220:223],a[24:27],v[76:79]
v_perm_b32 v237, v235, v234, s87
aw a113,v236
mm v[76:79],v[216:219],a[4:7],v[76:79]
rt v[208:209],v242 offset:59264
rt v[210:211],v242 offset:60288
mm v[44:47],v[216:219],a[0:3],v[44:47]
aw a117,v237
ar v230,a146
mm v[44:47],v[220:223],a[20:23],v[44:47]
ar v231,a150
v_perm_b32 v232, v231, v230, s86
mm v[12:15],v[220:223],a[16:19],v[12:15]
wt lgkmcnt(4)
v_perm_b32 v233, v231, v230, s87
mm v[12:15],v[216:219],a[12:15],v[12:15]
rt v[220:221],v243 offset:50816
rt v[222:223],v243 offset:51840
mm v[16:19],v[196:199],a[12:15],v[16:19]
aw a114,v232
aw a118,v233
mm v[16:19],v[192:195],a[16:19],v[16:19]
ld a[60:63],v246,s[44:47],s55 offen offset:0 sc1
mm v[48:51],v[192:195],a[20:23],v[48:51]
ar v234,a154
ar v235,a158
mm v[48:51],v[196:199],a[0:3],v[48:51]
rt v[216:217],v243 offset:59264
rt v[218:219],v243 offset:60288
mm v[80:83],v[196:199],a[4:7],v[80:83]
v_perm_b32 v236, v235, v234, s86
v_perm_b32 v237, v235, v234, s87
mm v[80:83],v[192:195],a[24:27],v[80:83]
ld a[76:79],v247,s[44:47],s55 offen offset:0 sc1
mm v[112:115],v[192:195],a[28:31],v[112:115]
aw a115,v236
aw a119,v237
mm v[112:115],v[196:199],a[8:11],v[112:115]
rt v[192:193],v240 offset:0
rt v[194:195],v240 offset:1024
mm v[116:119],v[204:207],a[8:11],v[116:119]
ar v230,a131
ar v231,a135
mm v[116:119],v[200:203],a[28:31],v[116:119]
ld a[80:83],v244,s[44:47],s55 offen offset:1024 sc1
mm v[84:87],v[200:203],a[24:27],v[84:87]
v_perm_b32 v232, v231, v230, s86
v_perm_b32 v233, v231, v230, s87
mm v[84:87],v[204:207],a[4:7],v[84:87]
rt v[196:197],v240 offset:8448
rt v[198:199],v240 offset:9472
mm v[52:55],v[204:207],a[0:3],v[52:55]
s_load_dwordx2 s[16:17], s[0:1], 0
s_load_dwordx4 s[36:39], s[0:1], 80
mm v[52:55],v[200:203],a[20:23],v[52:55]
ld a[84:87],v245,s[44:47],s55 offen offset:1024 sc1
mm v[20:23],v[200:203],a[16:19],v[20:23]
wt lgkmcnt(4)
aw a120,v232
mm v[20:23],v[204:207],a[12:15],v[20:23]
rt v[200:201],v241 offset:0
rt v[202:203],v241 offset:1024
mm v[24:27],v[212:215],a[12:15],v[24:27]
aw a124,v233
ar v234,a139
mm v[24:27],v[208:211],a[16:19],v[24:27]
ld a[88:91],v246,s[44:47],s55 offen offset:1024 sc1
mm v[56:59],v[208:211],a[20:23],v[56:59]
ar v235,a143
v_perm_b32 v236, v235, v234, s86
mm v[56:59],v[212:215],a[0:3],v[56:59]
rt v[204:205],v241 offset:8448
rt v[206:207],v241 offset:9472
mm v[88:91],v[212:215],a[4:7],v[88:91]
v_perm_b32 v237, v235, v234, s87
aw a121,v236
mm v[88:91],v[208:211],a[24:27],v[88:91]
ld a[92:95],v247,s[44:47],s55 offen offset:1024 sc1
mm v[120:123],v[208:211],a[28:31],v[120:123]
aw a125,v237
ar v230,a147
mm v[120:123],v[212:215],a[8:11],v[120:123]
rt v[208:209],v242 offset:0
rt v[210:211],v242 offset:1024
mm v[124:127],v[220:223],a[8:11],v[124:127]
s_add_u32 m0, s83, 16896
ar v231,a151
mm v[124:127],v[216:219],a[28:31],v[124:127]
ld v224,s[56:59],s80 offen lds
mm v[92:95],v[216:219],a[24:27],v[92:95]
v_perm_b32 v232, v231, v230, s86
v_perm_b32 v233, v231, v230, s87
mm v[92:95],v[220:223],a[4:7],v[92:95]
rt v[212:213],v242 offset:8448
rt v[214:215],v242 offset:9472
mm v[60:63],v[220:223],a[0:3],v[60:63]
aw a122,v232
aw a126,v233
mm v[60:63],v[216:219],a[20:23],v[60:63]
ar v234,a155
ar v235,a159
mm v[28:31],v[216:219],a[16:19],v[28:31]
wt vmcnt(10) lgkmcnt(4)
v_perm_b32 v236, v235, v234, s86
mm v[28:31],v[220:223],a[12:15],v[28:31]
rt v[216:217],v243 offset:0
rt v[218:219],v243 offset:1024
mm v[0:3],v[192:195],a[64:67],v[0:3]
s_add_u32 m0, s83, 17920
v_perm_b32 v237, v235, v234, s87
mm v[0:3],v[196:199],a[32:35],v[0:3]
ld v225,s[56:59],s80 offen lds
mm v[32:35],v[196:199],a[40:43],v[32:35]
aw a123,v236
aw a127,v237
mm v[32:35],v[192:195],a[68:71],v[32:35]
rt v[220:221],v243 offset:8448
rt v[222:223],v243 offset:9472
mm v[64:67],v[192:195],a[72:75],v[64:67]
s_add_u32 m0, s83, 19072
ar v230,a160
mm v[64:67],v[196:199],a[44:47],v[64:67]
ld v226,s[56:59],s80 offen lds
mm v[96:99],v[196:199],a[48:51],v[96:99]
ar v231,a164
v_perm_b32 v232, v231, v230, s86
mm v[96:99],v[192:195],a[36:39],v[96:99]
rt v[196:197],v240 offset:128
rt v[198:199],v240 offset:1152
mm v[100:103],v[200:203],a[36:39],v[100:103]
v_perm_b32 v233, v231, v230, s87
aw a128,v232
mm v[100:103],v[204:207],a[48:51],v[100:103]
ld a[0:3],v244,s[44:47],s55 offen offset:2048 sc1
mm v[68:71],v[204:207],a[44:47],v[68:71]
.La5_LW3_1:
s_cbranch_execz .La5_LW3_5
aw a132,v233
ar v234,a168
mm v[68:71],v[200:203],a[72:75],v[68:71]
rt v[192:193],v240 offset:8576
rt v[194:195],v240 offset:9600
mm v[36:39],v[200:203],a[68:71],v[36:39]
ar v235,a172
v_perm_b32 v236, v235, v234, s86
mm v[36:39],v[204:207],a[40:43],v[36:39]
ld a[4:7],v245,s[44:47],s55 offen offset:2048 sc1
mm v[4:7],v[204:207],a[32:35],v[4:7]
wt lgkmcnt(4)
v_perm_b32 v237, v235, v234, s87
mm v[4:7],v[200:203],a[64:67],v[4:7]
rt v[204:205],v241 offset:128
rt v[206:207],v241 offset:1152
mm v[8:11],v[208:211],a[64:67],v[8:11]
aw a129,v236
aw a133,v237
mm v[8:11],v[212:215],a[32:35],v[8:11]
ld a[8:11],v246,s[44:47],s55 offen offset:2048 sc1
mm v[40:43],v[212:215],a[40:43],v[40:43]
ar v230,a176
ar v231,a180
mm v[40:43],v[208:211],a[68:71],v[40:43]
rt v[200:201],v241 offset:8576
rt v[202:203],v241 offset:9600
mm v[72:75],v[208:211],a[72:75],v[72:75]
v_perm_b32 v232, v231, v230, s86
v_perm_b32 v233, v231, v230, s87
mm v[72:75],v[212:215],a[44:47],v[72:75]
ld a[12:15],v247,s[44:47],s55 offen offset:2048 sc1
mm v[104:107],v[212:215],a[48:51],v[104:107]
aw a130,v232
aw a134,v233
mm v[104:107],v[208:211],a[36:39],v[104:107]
rt v[212:213],v242 offset:128
rt v[214:215],v242 offset:1152
mm v[108:111],v[216:219],a[36:39],v[108:111]
ar v234,a184
ar v235,a188
mm v[108:111],v[220:223],a[48:51],v[108:111]
ld a[16:19],v244,s[44:47],s55 offen offset:3072 sc1
mm v[76:79],v[220:223],a[44:47],v[76:79]
v_perm_b32 v236, v235, v234, s86
v_perm_b32 v237, v235, v234, s87
mm v[76:79],v[216:219],a[72:75],v[76:79]
rt v[208:209],v242 offset:8576
rt v[210:211],v242 offset:9600
mm v[44:47],v[216:219],a[68:71],v[44:47]
aw a131,v236
aw a135,v237
mm v[44:47],v[220:223],a[40:43],v[44:47]
s_nop 0
v_cmp_eq_u64 vcc, v[250:251], v[252:253]
mm v[12:15],v[220:223],a[32:35],v[12:15]
wt lgkmcnt(4)
ar v230,a161
mm v[12:15],v[216:219],a[64:67],v[12:15]
rt v[220:221],v243 offset:128
rt v[222:223],v243 offset:1152
mm v[16:19],v[196:199],a[64:67],v[16:19]
ar v231,a165
v_perm_b32 v232, v231, v230, s86
mm v[16:19],v[192:195],a[32:35],v[16:19]
ld a[20:23],v245,s[44:47],s55 offen offset:3072 sc1
mm v[48:51],v[192:195],a[40:43],v[48:51]
v_perm_b32 v233, v231, v230, s87
aw a136,v232
mm v[48:51],v[196:199],a[68:71],v[48:51]
rt v[216:217],v243 offset:8576
rt v[218:219],v243 offset:9600
mm v[80:83],v[196:199],a[72:75],v[80:83]
aw a140,v233
ar v234,a169
mm v[80:83],v[192:195],a[44:47],v[80:83]
ld a[24:27],v246,s[44:47],s55 offen offset:3072 sc1
mm v[112:115],v[192:195],a[48:51],v[112:115]
wt vmcnt(18) lgkmcnt(0)
s_barrier
mm v[112:115],v[196:199],a[36:39],v[112:115]
rt v[192:193],v240 offset:16896
rt v[194:195],v240 offset:17920
mm v[116:119],v[204:207],a[36:39],v[116:119]
s_xor_b32 s90, s72, 0
s_lshl_b32 s92, s76, 5
mm v[116:119],v[200:203],a[48:51],v[116:119]
buffer_load_dwordx2 v[250:251], v248, s[64:67], s5 offen sc1
mm v[84:87],v[200:203],a[44:47],v[84:87]
s_add_u32 s92, s92, s90
s_lshl2_add_u32 s92, s92, s3
mm v[84:87],v[204:207],a[72:75],v[84:87]
rt v[196:197],v240 offset:25344
rt v[198:199],v240 offset:26368
mm v[52:55],v[204:207],a[68:71],v[52:55]
s_lshl_b32 s92, s92, 12
ar v235,a173
mm v[52:55],v[200:203],a[40:43],v[52:55]
ld a[28:31],v247,s[44:47],s55 offen offset:3072 sc1
mm v[20:23],v[200:203],a[32:35],v[20:23]
s_nop 0
s_bfe_u32 s55, s4, 262168
mm v[20:23],v[204:207],a[64:67],v[20:23]
rt v[200:201],v241 offset:16896
rt v[202:203],v241 offset:17920
mm v[24:27],v[212:215],a[64:67],v[24:27]
s_add_u32 m0, s83, 20096
s_xor_b32 s55, s55, s72
mm v[24:27],v[208:211],a[32:35],v[24:27]
ld v227,s[56:59],s80 offen lds
mm v[56:59],v[208:211],a[40:43],v[56:59]
s_add_u32 s80, s80, 131072
s_lshl_b32 s55, s55, 13
mm v[56:59],v[212:215],a[68:71],v[56:59]
rt v[204:205],v241 offset:25344
rt v[206:207],v241 offset:26368
mm v[88:91],v[212:215],a[72:75],v[88:91]
s_add_u32 m0, s83, 33792
v_perm_b32 v236, v235, v234, s86
mm v[88:91],v[208:211],a[44:47],v[88:91]
ld v224,s[56:59],s80 offen lds
mm v[120:123],v[208:211],a[48:51],v[120:123]
s_add_u32 s68, s16, s92
s_addc_u32 s69, s17, 0
mm v[120:123],v[212:215],a[36:39],v[120:123]
rt v[208:209],v242 offset:16896
rt v[210:211],v242 offset:17920
mm v[124:127],v[220:223],a[36:39],v[124:127]
s_add_u32 m0, s83, 34816
s_and_b32 s69, s69, 0xffff
mm v[124:127],v[216:219],a[48:51],v[124:127]
ld v225,s[56:59],s80 offen lds
mm v[92:95],v[216:219],a[44:47],v[92:95]
s_mov_b32 s70, 4096
s_mov_b32 s71, 0x20000
mm v[92:95],v[220:223],a[72:75],v[92:95]
rt v[212:213],v242 offset:25344
rt v[214:215],v242 offset:26368
mm v[60:63],v[220:223],a[68:71],v[60:63]
s_add_u32 m0, s83, 35968
v_perm_b32 v237, v235, v234, s87
mm v[60:63],v[216:219],a[40:43],v[60:63]
ld v226,s[56:59],s80 offen lds
mm v[28:31],v[216:219],a[32:35],v[28:31]
wt vmcnt(16) lgkmcnt(4)
aw a137,v236
mm v[28:31],v[220:223],a[64:67],v[28:31]
rt v[216:217],v243 offset:16896
rt v[218:219],v243 offset:17920
mm v[0:3],v[192:195],a[52:55],v[0:3]
s_add_u32 m0, s83, 36992
aw a141,v237
mm v[0:3],v[196:199],a[80:83],v[0:3]
ld v227,s[56:59],s80 offen lds
mm v[32:35],v[196:199],a[84:87],v[32:35]
s_bitcmp1_b32 vcc_lo, 4
s_cbranch_scc0 .La5_LO_NB4
.La5_LO_KB4:
mm v[32:35],v[192:195],a[56:59],v[32:35]
rt v[220:221],v243 offset:25344
rt v[222:223],v243 offset:26368
mm v[64:67],v[192:195],a[60:63],v[64:67]
s_bfe_u32 s80, s4, 262164
s_xor_b32 s80, s80, s72
mm v[64:67],v[196:199],a[88:91],v[64:67]
ld a[32:35],v244,s[44:47],s52 offen offset:0 sc1
mm v[96:99],v[196:199],a[92:95],v[96:99]
s_lshl_b32 s80, s80, 18
ar v230,a177
mm v[96:99],v[192:195],a[76:79],v[96:99]
rt v[196:197],v240 offset:17024
rt v[198:199],v240 offset:18048
mm v[100:103],v[200:203],a[76:79],v[100:103]
s_add_u32 s48, s36, 0
s_addc_u32 s49, s37, 0
mm v[100:103],v[204:207],a[92:95],v[100:103]
ld a[36:39],v245,s[44:47],s52 offen offset:0 sc1
mm v[68:71],v[204:207],a[88:91],v[68:71]
s_and_b32 s49, s49, 0xffff
s_mov_b32 s50, 2048
mm v[68:71],v[200:203],a[60:63],v[68:71]
rt v[192:193],v240 offset:25472
rt v[194:195],v240 offset:26496
mm v[36:39],v[200:203],a[56:59],v[36:39]
s_mov_b32 s51, 0x20000
v_and_b32 v238, 0xf0, v255
mm v[36:39],v[204:207],a[84:87],v[36:39]
ld a[40:43],v246,s[44:47],s52 offen offset:0 sc1
mm v[4:7],v[204:207],a[80:83],v[4:7]
wt lgkmcnt(4)
v_lshlrev_b32 v238, 2, v238
mm v[4:7],v[200:203],a[52:55],v[4:7]
rt v[204:205],v241 offset:17024
rt v[206:207],v241 offset:18048
mm v[8:11],v[208:211],a[52:55],v[8:11]
v_bfe_u32 v249, v255, 8, 1
v_lshl_add_u32 v238, v249, 5, v238
mm v[8:11],v[212:215],a[80:83],v[8:11]
ld a[44:47],v247,s[44:47],s52 offen offset:0 sc1
mm v[40:43],v[212:215],a[84:87],v[40:43]
v_lshrrev_b32 v249, 9, v255
v_lshl_add_u32 v238, v249, 4, v238
mm v[40:43],v[208:211],a[56:59],v[40:43]
rt v[200:201],v241 offset:25472
rt v[202:203],v241 offset:26496
mm v[72:75],v[208:211],a[60:63],v[72:75]
s_lshr_b32 s23, s72, 3
s_lshl3_add_u32 s92, s23, s76
mm v[72:75],v[212:215],a[88:91],v[72:75]
ld a[48:51],v244,s[44:47],s52 offen offset:1024 sc1
mm v[104:107],v[212:215],a[92:95],v[104:107]
s_lshl_b32 s92, s92, 19
s_lshl_b32 s97, s3, 16
mm v[104:107],v[208:211],a[76:79],v[104:107]
rt v[212:213],v242 offset:17024
rt v[214:215],v242 offset:18048
mm v[108:111],v[216:219],a[76:79],v[108:111]
s_add_u32 s94, s92, s97
s_add_u32 s95, s94, 32768
mm v[108:111],v[220:223],a[92:95],v[108:111]
ld a[64:67],v245,s[44:47],s52 offen offset:1024 sc1
mm v[76:79],v[220:223],a[88:91],v[76:79]
s_sub_u32 s96, s92, s97
s_add_u32 s96, s96, 458752
mm v[76:79],v[216:219],a[60:63],v[76:79]
rt v[208:209],v242 offset:25472
rt v[210:211],v242 offset:26496
mm v[44:47],v[216:219],a[56:59],v[44:47]
s_add_u32 s97, s96, 32768
s_lshl3_add_u32 s92, s23, s101
mm v[44:47],v[220:223],a[84:87],v[44:47]
s_lshl_b32 s85, s76, 14
s_lshl3_add_u32 s92, s92, s85
mm v[12:15],v[220:223],a[80:83],v[12:15]
wt lgkmcnt(4)
s_add_u32 s92, s92, 2097152
mm v[12:15],v[216:219],a[52:55],v[12:15]
rt v[220:221],v243 offset:17024
rt v[222:223],v243 offset:18048
mm v[16:19],v[196:199],a[52:55],v[16:19]
ar v231,a181
v_perm_b32 v232, v231, v230, s86
mm v[16:19],v[192:195],a[80:83],v[16:19]
ld a[68:71],v246,s[44:47],s52 offen offset:1024 sc1
mm v[48:51],v[192:195],a[84:87],v[48:51]
v_perm_b32 v233, v231, v230, s87
aw a138,v232
mm v[48:51],v[196:199],a[56:59],v[48:51]
rt v[216:217],v243 offset:25472
rt v[218:219],v243 offset:26496
mm v[80:83],v[196:199],a[60:63],v[80:83]
aw a142,v233
ar v234,a185
mm v[80:83],v[192:195],a[88:91],v[80:83]
ld a[72:75],v247,s[44:47],s52 offen offset:1024 sc1
mm v[112:115],v[192:195],a[92:95],v[112:115]
ar v235,a189
v_perm_b32 v236, v235, v234, s86
mm v[112:115],v[196:199],a[76:79],v[112:115]
rt v[192:193],v240 offset:33792
rt v[194:195],v240 offset:34816
mm v[116:119],v[204:207],a[76:79],v[116:119]
s_add_u32 m0, s83, 50688
v_perm_b32 v237, v235, v234, s87
mm v[116:119],v[200:203],a[92:95],v[116:119]
ld v224,s[56:59],s80 offen lds
mm v[84:87],v[200:203],a[88:91],v[84:87]
aw a139,v236
aw a143,v237
mm v[84:87],v[204:207],a[60:63],v[84:87]
rt v[196:197],v240 offset:42240
rt v[198:199],v240 offset:43264
mm v[52:55],v[204:207],a[56:59],v[52:55]
s_add_u32 m0, s83, 51712
ar v230,a162
mm v[52:55],v[200:203],a[84:87],v[52:55]
ld v225,s[56:59],s80 offen lds
mm v[20:23],v[200:203],a[80:83],v[20:23]
wt lgkmcnt(4)
ar v231,a166
mm v[20:23],v[204:207],a[52:55],v[20:23]
rt v[200:201],v241 offset:33792
rt v[202:203],v241 offset:34816
mm v[24:27],v[212:215],a[52:55],v[24:27]
s_add_u32 m0, s83, 52864
v_perm_b32 v232, v231, v230, s86
mm v[24:27],v[208:211],a[80:83],v[24:27]
ld v226,s[56:59],s80 offen lds
mm v[56:59],v[208:211],a[84:87],v[56:59]
v_perm_b32 v233, v231, v230, s87
aw a144,v232
mm v[56:59],v[212:215],a[56:59],v[56:59]
rt v[204:205],v241 offset:42240
rt v[206:207],v241 offset:43264
mm v[88:91],v[212:215],a[60:63],v[88:91]
aw a148,v233
ar v234,a170
mm v[88:91],v[208:211],a[88:91],v[88:91]
ld v[136:139],v244,s[44:47],s52 offen offset:2048 sc1
mm v[120:123],v[208:211],a[92:95],v[120:123]
ar v235,a174
v_perm_b32 v236, v235, v234, s86
mm v[120:123],v[212:215],a[76:79],v[120:123]
rt v[208:209],v242 offset:33792
rt v[210:211],v242 offset:34816
mm v[124:127],v[220:223],a[76:79],v[124:127]
v_perm_b32 v237, v235, v234, s87
aw a145,v236
mm v[124:127],v[216:219],a[92:95],v[124:127]
ld v[140:143],v245,s[44:47],s52 offen offset:2048 sc1
mm v[92:95],v[216:219],a[88:91],v[92:95]
aw a149,v237
ar v230,a178
mm v[92:95],v[220:223],a[60:63],v[92:95]
rt v[212:213],v242 offset:42240
rt v[214:215],v242 offset:43264
mm v[60:63],v[220:223],a[56:59],v[60:63]
ar v231,a182
v_perm_b32 v232, v231, v230, s86
mm v[60:63],v[216:219],a[84:87],v[60:63]
v_perm_b32 v233, v231, v230, s87
aw a146,v232
mm v[28:31],v[216:219],a[80:83],v[28:31]
wt vmcnt(18) lgkmcnt(4)
aw a150,v233
mm v[28:31],v[220:223],a[52:55],v[28:31]
rt v[216:217],v243 offset:33792
rt v[218:219],v243 offset:34816
mm v[0:3],v[192:195],a[0:3],v[0:3]
ar v234,a186
ar v235,a190
mm v[0:3],v[196:199],a[16:19],v[0:3]
ld v[152:155],v246,s[44:47],s52 offen offset:2048 sc1
mm v[32:35],v[196:199],a[20:23],v[32:35]
v_perm_b32 v236, v235, v234, s86
v_perm_b32 v237, v235, v234, s87
mm v[32:35],v[192:195],a[4:7],v[32:35]
rt v[220:221],v243 offset:42240
rt v[222:223],v243 offset:43264
mm v[64:67],v[192:195],a[8:11],v[64:67]
aw a147,v236
aw a151,v237
mm v[64:67],v[196:199],a[24:27],v[64:67]
ld v[156:159],v247,s[44:47],s52 offen offset:2048 sc1
mm v[96:99],v[196:199],a[28:31],v[96:99]
ar v230,a163
ar v231,a167
mm v[96:99],v[192:195],a[12:15],v[96:99]
rt v[196:197],v240 offset:33920
rt v[198:199],v240 offset:34944
mm v[100:103],v[200:203],a[12:15],v[100:103]
v_perm_b32 v232, v231, v230, s86
v_perm_b32 v233, v231, v230, s87
mm v[100:103],v[204:207],a[28:31],v[100:103]
ld v[168:171],v244,s[44:47],s52 offen offset:3072 sc1
mm v[68:71],v[204:207],a[24:27],v[68:71]
aw a152,v232
aw a156,v233
mm v[68:71],v[200:203],a[8:11],v[68:71]
rt v[192:193],v240 offset:42368
rt v[194:195],v240 offset:43392
mm v[36:39],v[200:203],a[4:7],v[36:39]
v_add_u32 v240, 4294899712, v240
ar v234,a171
mm v[36:39],v[204:207],a[20:23],v[36:39]
ld v[172:175],v245,s[44:47],s52 offen offset:3072 sc1
mm v[4:7],v[204:207],a[16:19],v[4:7]
wt lgkmcnt(4)
ar v235,a175
mm v[4:7],v[200:203],a[0:3],v[4:7]
rt v[204:205],v241 offset:33920
rt v[206:207],v241 offset:34944
mm v[8:11],v[208:211],a[0:3],v[8:11]
s_nop 0
v_cmp_eq_u64 vcc, v[250:251], v[252:253]
mm v[8:11],v[212:215],a[16:19],v[8:11]
ld v[184:187],v246,s[44:47],s52 offen offset:3072 sc1
mm v[40:43],v[212:215],a[20:23],v[40:43]
v_perm_b32 v236, v235, v234, s86
v_perm_b32 v237, v235, v234, s87
mm v[40:43],v[208:211],a[4:7],v[40:43]
rt v[200:201],v241 offset:42368
rt v[202:203],v241 offset:43392
mm v[72:75],v[208:211],a[8:11],v[72:75]
v_add_u32 v241, 4294899712, v241
aw a153,v236
mm v[72:75],v[212:215],a[24:27],v[72:75]
ld v[188:191],v247,s[44:47],s52 offen offset:3072 sc1
mm v[104:107],v[212:215],a[28:31],v[104:107]
s_bfe_u32 s52, s4, 262172
s_xor_b32 s52, s52, s72
mm v[104:107],v[208:211],a[12:15],v[104:107]
rt v[212:213],v242 offset:33920
rt v[214:215],v242 offset:34944
mm v[108:111],v[216:219],a[12:15],v[108:111]
s_lshl_b32 s52, s52, 13
aw a157,v237
mm v[108:111],v[220:223],a[28:31],v[108:111]
buffer_load_dwordx2 v[250:251], v248, s[64:67], s5 offen sc1
mm v[76:79],v[220:223],a[24:27],v[76:79]
ar v230,a179
ar v231,a183
mm v[76:79],v[216:219],a[8:11],v[76:79]
rt v[208:209],v242 offset:42368
rt v[210:211],v242 offset:43392
mm v[44:47],v[216:219],a[4:7],v[44:47]
s_add_u32 m0, s83, 53888
v_add_u32 v242, 4294899712, v242
mm v[44:47],v[220:223],a[20:23],v[44:47]
ld v227,s[56:59],s80 offen lds
mm v[12:15],v[220:223],a[16:19],v[12:15]
wt lgkmcnt(4)
s_add_u32 s80, s80, 131072
mm v[12:15],v[216:219],a[0:3],v[12:15]
rt v[220:221],v243 offset:33920
rt v[222:223],v243 offset:34944
mm v[16:19],v[196:199],a[0:3],v[16:19]
s_add_u32 m0, s83, 0
v_perm_b32 v232, v231, v230, s86
mm v[16:19],v[192:195],a[16:19],v[16:19]
ld v224,s[56:59],s80 offen lds
mm v[48:51],v[192:195],a[20:23],v[48:51]
v_perm_b32 v233, v231, v230, s87
aw a154,v232
mm v[48:51],v[196:199],a[4:7],v[48:51]
rt v[216:217],v243 offset:42368
rt v[218:219],v243 offset:43392
mm v[80:83],v[196:199],a[8:11],v[80:83]
s_add_u32 m0, s83, 1024
v_add_u32 v243, 4294899712, v243
mm v[80:83],v[192:195],a[24:27],v[80:83]
ld v225,s[56:59],s80 offen lds
mm v[112:115],v[192:195],a[28:31],v[112:115]
wt vmcnt(23) lgkmcnt(0)
s_barrier
mm v[112:115],v[196:199],a[12:15],v[112:115]
rt v[192:193],v240 offset:16896
rt v[194:195],v240 offset:17920
mm v[116:119],v[204:207],a[12:15],v[116:119]
s_add_u32 m0, s83, 2176
aw a158,v233
mm v[116:119],v[200:203],a[28:31],v[116:119]
ld v226,s[56:59],s80 offen lds
mm v[84:87],v[200:203],a[24:27],v[84:87]
ar v234,a187
ar v235,a191
mm v[84:87],v[204:207],a[8:11],v[84:87]
rt v[196:197],v240 offset:25344
rt v[198:199],v240 offset:26368
mm v[52:55],v[204:207],a[4:7],v[52:55]
s_add_u32 m0, s83, 3200
v_perm_b32 v236, v235, v234, s86
mm v[52:55],v[200:203],a[20:23],v[52:55]
ld v227,s[56:59],s80 offen lds
mm v[20:23],v[200:203],a[16:19],v[20:23]
s_nop 0
s_bfe_u32 s80, s4, 262164
mm v[20:23],v[204:207],a[0:3],v[20:23]
rt v[200:201],v241 offset:16896
rt v[202:203],v241 offset:17920
mm v[24:27],v[212:215],a[0:3],v[24:27]
s_bitcmp1_b32 vcc_lo, 5
s_cbranch_scc0 .La5_LO_NB5
.La5_LO_KB5:
mm v[24:27],v[208:211],a[16:19],v[24:27]
ld a[52:55],v244,s[44:47],s53 offen offset:0 sc1
mm v[56:59],v[208:211],a[20:23],v[56:59]
s_xor_b32 s80, s80, s72
s_lshl_b32 s80, s80, 18
mm v[56:59],v[212:215],a[4:7],v[56:59]
rt v[204:205],v241 offset:25344
rt v[206:207],v241 offset:26368
mm v[88:91],v[212:215],a[8:11],v[88:91]
s_xor_b32 s80, s80, 7340032
s_add_u32 s80, s80, 131072
mm v[88:91],v[208:211],a[24:27],v[88:91]
ld a[56:59],v245,s[44:47],s53 offen offset:0 sc1
mm v[120:123],v[208:211],a[28:31],v[120:123]
v_perm_b32 v237, v235, v234, s87
aw a155,v236
mm v[120:123],v[212:215],a[12:15],v[120:123]
rt v[208:209],v242 offset:16896
rt v[210:211],v242 offset:17920
mm v[124:127],v[220:223],a[12:15],v[124:127]
aw a159,v237
ar v230,a192
mm v[124:127],v[216:219],a[28:31],v[124:127]
ld a[12:15],v246,s[44:47],s53 offen offset:0 sc1
mm v[92:95],v[216:219],a[24:27],v[92:95]
ar v231,a196
v_perm_b32 v232, v231, v230, s86
mm v[92:95],v[220:223],a[8:11],v[92:95]
rt v[212:213],v242 offset:25344
rt v[214:215],v242 offset:26368
mm v[60:63],v[220:223],a[4:7],v[60:63]
v_perm_b32 v233, v231, v230, s87
aw a160,v232
mm v[60:63],v[216:219],a[20:23],v[60:63]
aw a164,v233
ar v234,a200
mm v[28:31],v[216:219],a[16:19],v[28:31]
wt vmcnt(20) lgkmcnt(4)
ar v235,a204
mm v[28:31],v[220:223],a[0:3],v[28:31]
rt v[216:217],v243 offset:16896
rt v[218:219],v243 offset:17920
mm v[0:3],v[192:195],a[32:35],v[0:3]
v_perm_b32 v236, v235, v234, s86
v_perm_b32 v237, v235, v234, s87
mm v[0:3],v[196:199],a[48:51],v[0:3]
ld a[0:3],v247,s[44:47],s53 offen offset:0 sc1
mm v[32:35],v[196:199],a[64:67],v[32:35]
aw a161,v236
aw a165,v237
mm v[32:35],v[192:195],a[36:39],v[32:35]
rt v[220:221],v243 offset:25344
rt v[222:223],v243 offset:26368
mm v[64:67],v[192:195],a[40:43],v[64:67]
ar v230,a208
ar v231,a212
mm v[64:67],v[196:199],a[68:71],v[64:67]
ld a[4:7],v244,s[44:47],s53 offen offset:1024 sc1
mm v[96:99],v[196:199],a[72:75],v[96:99]
v_perm_b32 v232, v231, v230, s86
v_perm_b32 v233, v231, v230, s87
mm v[96:99],v[192:195],a[44:47],v[96:99]
rt v[196:197],v240 offset:17024
rt v[198:199],v240 offset:18048
mm v[100:103],v[200:203],a[44:47],v[100:103]
aw a162,v232
aw a166,v233
mm v[100:103],v[204:207],a[72:75],v[100:103]
ld a[8:11],v245,s[44:47],s53 offen offset:1024 sc1
mm v[68:71],v[204:207],a[68:71],v[68:71]
ar v234,a216
ar v235,a220
mm v[68:71],v[200:203],a[40:43],v[68:71]
rt v[192:193],v240 offset:25472
rt v[194:195],v240 offset:26496
mm v[36:39],v[200:203],a[36:39],v[36:39]
.La5_LW3_2:
s_cbranch_execz .La5_LW3_6
v_perm_b32 v236, v235, v234, s86
v_perm_b32 v237, v235, v234, s87
mm v[36:39],v[204:207],a[64:67],v[36:39]
ld a[16:19],v246,s[44:47],s53 offen offset:1024 sc1
mm v[4:7],v[204:207],a[48:51],v[4:7]
wt lgkmcnt(4)
aw a163,v236
mm v[4:7],v[200:203],a[32:35],v[4:7]
rt v[204:205],v241 offset:17024
rt v[206:207],v241 offset:18048
mm v[8:11],v[208:211],a[32:35],v[8:11]
aw a167,v237
ar v230,a193
mm v[8:11],v[212:215],a[48:51],v[8:11]
ld a[20:23],v247,s[44:47],s53 offen offset:1024 sc1
mm v[40:43],v[212:215],a[64:67],v[40:43]
ar v231,a197
v_perm_b32 v232, v231, v230, s86
mm v[40:43],v[208:211],a[36:39],v[40:43]
rt v[200:201],v241 offset:25472
rt v[202:203],v241 offset:26496
mm v[72:75],v[208:211],a[40:43],v[72:75]
v_perm_b32 v233, v231, v230, s87
aw a168,v232
mm v[72:75],v[212:215],a[68:71],v[72:75]
aw a172,v233
ar v234,a201
mm v[104:107],v[212:215],a[72:75],v[104:107]
ar v235,a205
v_perm_b32 v236, v235, v234, s86
mm v[104:107],v[208:211],a[44:47],v[104:107]
rt v[212:213],v242 offset:17024
rt v[214:215],v242 offset:18048
mm v[108:111],v[216:219],a[44:47],v[108:111]
v_perm_b32 v237, v235, v234, s87
aw a169,v236
mm v[108:111],v[220:223],a[72:75],v[108:111]
aw a173,v237
ar v230,a209
mm v[76:79],v[220:223],a[68:71],v[76:79]
ar v231,a213
v_perm_b32 v232, v231, v230, s86
mm v[76:79],v[216:219],a[40:43],v[76:79]
rt v[208:209],v242 offset:25472
rt v[210:211],v242 offset:26496
mm v[44:47],v[216:219],a[36:39],v[44:47]
v_perm_b32 v233, v231, v230, s87
aw a170,v232
mm v[44:47],v[220:223],a[64:67],v[44:47]
aw a174,v233
ar v234,a217
mm v[12:15],v[220:223],a[48:51],v[12:15]
wt lgkmcnt(4)
ar v235,a221
mm v[12:15],v[216:219],a[32:35],v[12:15]
rt v[220:221],v243 offset:17024
rt v[222:223],v243 offset:18048
mm v[16:19],v[196:199],a[32:35],v[16:19]
v_perm_b32 v236, v235, v234, s86
v_perm_b32 v237, v235, v234, s87
mm v[16:19],v[192:195],a[48:51],v[16:19]
ld a[24:27],v244,s[44:47],s53 offen offset:2048 sc1
mm v[48:51],v[192:195],a[64:67],v[48:51]
aw a171,v236
aw a175,v237
mm v[48:51],v[196:199],a[36:39],v[48:51]
rt v[216:217],v243 offset:25472
rt v[218:219],v243 offset:26496
mm v[80:83],v[196:199],a[40:43],v[80:83]
ar v230,a194
ar v231,a198
mm v[80:83],v[192:195],a[68:71],v[80:83]
ld a[28:31],v245,s[44:47],s53 offen offset:2048 sc1
mm v[112:115],v[192:195],a[72:75],v[112:115]
v_perm_b32 v232, v231, v230, s86
v_perm_b32 v233, v231, v230, s87
mm v[112:115],v[196:199],a[44:47],v[112:115]
rt v[192:193],v240 offset:33792
rt v[194:195],v240 offset:34816
mm v[116:119],v[204:207],a[44:47],v[116:119]
aw a176,v232
aw a180,v233
mm v[116:119],v[200:203],a[72:75],v[116:119]
ld a[60:63],v246,s[44:47],s53 offen offset:2048 sc1
mm v[84:87],v[200:203],a[68:71],v[84:87]
ar v234,a202
ar v235,a206
mm v[84:87],v[204:207],a[40:43],v[84:87]
rt v[196:197],v240 offset:42240
rt v[198:199],v240 offset:43264
mm v[52:55],v[204:207],a[36:39],v[52:55]
v_perm_b32 v236, v235, v234, s86
v_perm_b32 v237, v235, v234, s87
mm v[52:55],v[200:203],a[64:67],v[52:55]
ld v[128:131],v247,s[44:47],s53 offen offset:2048 sc1
mm v[20:23],v[200:203],a[48:51],v[20:23]
wt lgkmcnt(4)
aw a177,v236
mm v[20:23],v[204:207],a[32:35],v[20:23]
rt v[200:201],v241 offset:33792
rt v[202:203],v241 offset:34816
mm v[24:27],v[212:215],a[32:35],v[24:27]
aw a181,v237
ar v230,a210
mm v[24:27],v[208:211],a[48:51],v[24:27]
ld v[132:135],v244,s[44:47],s53 offen offset:3072 sc1
mm v[56:59],v[208:211],a[64:67],v[56:59]
ar v231,a214
v_perm_b32 v232, v231, v230, s86
mm v[56:59],v[212:215],a[36:39],v[56:59]
rt v[204:205],v241 offset:42240
rt v[206:207],v241 offset:43264
mm v[88:91],v[212:215],a[40:43],v[88:91]
v_perm_b32 v233, v231, v230, s87
aw a178,v232
mm v[88:91],v[208:211],a[68:71],v[88:91]
ld v[144:147],v245,s[44:47],s53 offen offset:3072 sc1
mm v[120:123],v[208:211],a[72:75],v[120:123]
aw a182,v233
ar v234,a218
mm v[120:123],v[212:215],a[44:47],v[120:123]
rt v[208:209],v242 offset:33792
rt v[210:211],v242 offset:34816
mm v[124:127],v[220:223],a[44:47],v[124:127]
ar v235,a222
v_perm_b32 v236, v235, v234, s86
mm v[124:127],v[216:219],a[72:75],v[124:127]
ld a[44:47],v246,s[44:47],s53 offen offset:3072 sc1
mm v[92:95],v[216:219],a[68:71],v[92:95]
v_perm_b32 v237, v235, v234, s87
aw a179,v236
mm v[92:95],v[220:223],a[40:43],v[92:95]
rt v[212:213],v242 offset:42240
rt v[214:215],v242 offset:43264
mm v[60:63],v[220:223],a[36:39],v[60:63]
aw a183,v237
ar v230,a195
mm v[60:63],v[216:219],a[64:67],v[60:63]
wt vmcnt(20)
v_cmp_eq_u64 vcc, v[250:251], v[252:253]
mm v[28:31],v[216:219],a[48:51],v[28:31]
wt lgkmcnt(4)
ar v231,a199
mm v[28:31],v[220:223],a[32:35],v[28:31]
rt v[216:217],v243 offset:33792
rt v[218:219],v243 offset:34816
mm v[0:3],v[192:195],v[136:139],v[0:3]
v_perm_b32 v232, v231, v230, s86
v_perm_b32 v233, v231, v230, s87
mm v[0:3],v[196:199],v[168:171],v[0:3]
ld a[32:35],v247,s[44:47],s53 offen offset:3072 sc1
mm v[32:35],v[196:199],v[172:175],v[32:35]
aw a184,v232
aw a188,v233
mm v[32:35],v[192:195],v[140:143],v[32:35]
rt v[220:221],v243 offset:42240
rt v[222:223],v243 offset:43264
mm v[64:67],v[192:195],v[152:155],v[64:67]
ar v234,a203
ar v235,a207
mm v[64:67],v[196:199],v[184:187],v[64:67]
v_perm_b32 v236, v235, v234, s86
v_perm_b32 v237, v235, v234, s87
mm v[96:99],v[196:199],v[188:191],v[96:99]
aw a185,v236
aw a189,v237
mm v[96:99],v[192:195],v[156:159],v[96:99]
rt v[196:197],v240 offset:33920
rt v[198:199],v240 offset:34944
mm v[100:103],v[200:203],v[156:159],v[100:103]
ar v230,a211
ar v231,a215
mm v[100:103],v[204:207],v[188:191],v[100:103]
buffer_load_dwordx2 v[250:251], v248, s[64:67], s5 offen sc1
mm v[68:71],v[204:207],v[184:187],v[68:71]
v_perm_b32 v232, v231, v230, s86
v_perm_b32 v233, v231, v230, s87
mm v[68:71],v[200:203],v[152:155],v[68:71]
rt v[192:193],v240 offset:42368
rt v[194:195],v240 offset:43392
mm v[36:39],v[200:203],v[140:143],v[36:39]
aw a186,v232
aw a190,v233
mm v[36:39],v[204:207],v[172:175],v[36:39]
ar v234,a219
ar v235,a223
mm v[4:7],v[204:207],v[168:171],v[4:7]
wt lgkmcnt(4)
v_perm_b32 v236, v235, v234, s86
mm v[4:7],v[200:203],v[136:139],v[4:7]
rt v[204:205],v241 offset:33920
rt v[206:207],v241 offset:34944
mm v[8:11],v[208:211],v[136:139],v[8:11]
v_perm_b32 v237, v235, v234, s87
aw a187,v236
mm v[8:11],v[212:215],v[168:171],v[8:11]
aw a191,v237
ar v230,a224
mm v[40:43],v[212:215],v[172:175],v[40:43]
ar v231,a228
v_perm_b32 v232, v231, v230, s86
mm v[40:43],v[208:211],v[140:143],v[40:43]
rt v[200:201],v241 offset:42368
rt v[202:203],v241 offset:43392
mm v[72:75],v[208:211],v[152:155],v[72:75]
v_perm_b32 v233, v231, v230, s87
aw a192,v232
mm v[72:75],v[212:215],v[184:187],v[72:75]
aw a196,v233
ar v234,a232
mm v[104:107],v[212:215],v[188:191],v[104:107]
ar v235,a236
v_perm_b32 v236, v235, v234, s86
mm v[104:107],v[208:211],v[156:159],v[104:107]
rt v[212:213],v242 offset:33920
rt v[214:215],v242 offset:34944
mm v[108:111],v[216:219],v[156:159],v[108:111]
v_perm_b32 v237, v235, v234, s87
aw a193,v236
mm v[108:111],v[220:223],v[188:191],v[108:111]
aw a197,v237
ar v230,a240
mm v[76:79],v[220:223],v[184:187],v[76:79]
ar v231,a244
v_perm_b32 v232, v231, v230, s86
mm v[76:79],v[216:219],v[152:155],v[76:79]
rt v[208:209],v242 offset:42368
rt v[210:211],v242 offset:43392
mm v[44:47],v[216:219],v[140:143],v[44:47]
s_bitcmp1_b32 vcc_lo, 13
s_nop 0
s_nop 0
s_cbranch_scc0 .La5_LO_NB13
.La5_LO_KB13:
mm v[44:47],v[220:223],v[172:175],v[44:47]
v_perm_b32 v233, v231, v230, s87
aw a194,v232
mm v[12:15],v[220:223],v[168:171],v[12:15]
wt lgkmcnt(4)
aw a198,v233
mm v[12:15],v[216:219],v[136:139],v[12:15]
rt v[220:221],v243 offset:33920
rt v[222:223],v243 offset:34944
mm v[16:19],v[196:199],v[136:139],v[16:19]
ar v234,a248
ar v235,a252
mm v[16:19],v[192:195],v[168:171],v[16:19]
ld a[36:39],v244,s[44:47],s54 offen offset:2048 sc1
mm v[48:51],v[192:195],v[172:175],v[48:51]
v_perm_b32 v236, v235, v234, s86
v_perm_b32 v237, v235, v234, s87
mm v[48:51],v[196:199],v[140:143],v[48:51]
rt v[216:217],v243 offset:42368
rt v[218:219],v243 offset:43392
mm v[80:83],v[196:199],v[152:155],v[80:83]
aw a195,v236
aw a199,v237
mm v[80:83],v[192:195],v[184:187],v[80:83]
ld a[40:43],v245,s[44:47],s54 offen offset:2048 sc1
mm v[112:115],v[192:195],v[188:191],v[112:115]
wt vmcnt(19)
s_barrier
mm v[112:115],v[196:199],v[156:159],v[112:115]
rt v[192:193],v240 offset:50688
rt v[194:195],v240 offset:51712
mm v[116:119],v[204:207],v[156:159],v[116:119]
s_add_u32 m0, s83, 16896
ar v230,a225
mm v[116:119],v[200:203],v[188:191],v[116:119]
ld v224,s[56:59],s80 offen lds
mm v[84:87],v[200:203],v[184:187],v[84:87]
ar v231,a229
v_perm_b32 v232, v231, v230, s86
mm v[84:87],v[204:207],v[152:155],v[84:87]
rt v[196:197],v240 offset:59136
rt v[198:199],v240 offset:60160
mm v[52:55],v[204:207],v[140:143],v[52:55]
s_add_u32 m0, s83, 17920
v_perm_b32 v233, v231, v230, s87
mm v[52:55],v[200:203],v[172:175],v[52:55]
ld v225,s[56:59],s80 offen lds
mm v[20:23],v[200:203],v[168:171],v[20:23]
wt lgkmcnt(4)
aw a200,v232
mm v[20:23],v[204:207],v[136:139],v[20:23]
rt v[200:201],v241 offset:50688
rt v[202:203],v241 offset:51712
mm v[24:27],v[212:215],v[136:139],v[24:27]
s_add_u32 m0, s83, 19072
aw a204,v233
mm v[24:27],v[208:211],v[168:171],v[24:27]
ld v226,s[56:59],s80 offen lds
mm v[56:59],v[208:211],v[172:175],v[56:59]
ar v234,a233
ar v235,a237
mm v[56:59],v[212:215],v[140:143],v[56:59]
rt v[204:205],v241 offset:59136
rt v[206:207],v241 offset:60160
mm v[88:91],v[212:215],v[152:155],v[88:91]
s_add_u32 m0, s83, 20096
v_perm_b32 v236, v235, v234, s86
mm v[88:91],v[208:211],v[184:187],v[88:91]
ld v227,s[56:59],s80 offen lds
mm v[120:123],v[208:211],v[188:191],v[120:123]
s_bfe_u32 s80, s4, 262168
s_xor_b32 s80, s80, s72
mm v[120:123],v[212:215],v[156:159],v[120:123]
rt v[208:209],v242 offset:50688
rt v[210:211],v242 offset:51712
mm v[124:127],v[220:223],v[156:159],v[124:127]
s_add_u32 m0, s83, 118272
s_lshl_b32 s80, s80, 18
mm v[124:127],v[216:219],v[188:191],v[124:127]
ld v224,s[56:59],s80 offen lds
mm v[92:95],v[216:219],v[184:187],v[92:95]
v_perm_b32 v237, v235, v234, s87
aw a201,v236
mm v[92:95],v[220:223],v[152:155],v[92:95]
rt v[212:213],v242 offset:59136
rt v[214:215],v242 offset:60160
mm v[60:63],v[220:223],v[140:143],v[60:63]
aw a205,v237
ar v230,a241
mm v[60:63],v[216:219],v[172:175],v[60:63]
ar v231,a245
v_perm_b32 v232, v231, v230, s86
mm v[28:31],v[216:219],v[168:171],v[28:31]
wt vmcnt(16) lgkmcnt(4)
v_perm_b32 v233, v231, v230, s87
mm v[28:31],v[220:223],v[136:139],v[28:31]
rt v[216:217],v243 offset:50688
rt v[218:219],v243 offset:51712
mm v[0:3],v[192:195],a[52:55],v[0:3]
s_add_u32 m0, s83, 119296
aw a202,v232
mm v[0:3],v[196:199],a[4:7],v[0:3]
ld v225,s[56:59],s80 offen lds
mm v[32:35],v[196:199],a[8:11],v[32:35]
aw a206,v233
ar v234,a249
mm v[32:35],v[192:195],a[56:59],v[32:35]
rt v[220:221],v243 offset:59136
rt v[222:223],v243 offset:60160
mm v[64:67],v[192:195],a[12:15],v[64:67]
s_add_u32 m0, s83, 120448
ar v235,a253
mm v[64:67],v[196:199],a[16:19],v[64:67]
ld v226,s[56:59],s80 offen lds
mm v[96:99],v[196:199],a[20:23],v[96:99]
v_perm_b32 v236, v235, v234, s86
v_perm_b32 v237, v235, v234, s87
mm v[96:99],v[192:195],a[0:3],v[96:99]
rt v[196:197],v240 offset:50816
rt v[198:199],v240 offset:51840
mm v[100:103],v[200:203],a[0:3],v[100:103]
aw a203,v236
aw a207,v237
mm v[100:103],v[204:207],a[20:23],v[100:103]
ld v[136:139],v246,s[44:47],s54 offen offset:2048 sc1
mm v[68:71],v[204:207],a[16:19],v[68:71]
ar v230,a226
ar v231,a230
mm v[68:71],v[200:203],a[12:15],v[68:71]
rt v[192:193],v240 offset:59264
rt v[194:195],v240 offset:60288
mm v[36:39],v[200:203],a[56:59],v[36:39]
v_perm_b32 v232, v231, v230, s86
v_perm_b32 v233, v231, v230, s87
mm v[36:39],v[204:207],a[8:11],v[36:39]
ld v[140:143],v247,s[44:47],s54 offen offset:2048 sc1
mm v[4:7],v[204:207],a[4:7],v[4:7]
wt lgkmcnt(4)
aw a208,v232
mm v[4:7],v[200:203],a[52:55],v[4:7]
rt v[204:205],v241 offset:50816
rt v[206:207],v241 offset:51840
mm v[8:11],v[208:211],a[52:55],v[8:11]
aw a212,v233
ar v234,a234
mm v[8:11],v[212:215],a[4:7],v[8:11]
ld v[152:155],v244,s[44:47],s54 offen offset:3072 sc1
mm v[40:43],v[212:215],a[8:11],v[40:43]
ar v235,a238
v_perm_b32 v236, v235, v234, s86
mm v[40:43],v[208:211],a[56:59],v[40:43]
rt v[200:201],v241 offset:59264
rt v[202:203],v241 offset:60288
mm v[72:75],v[208:211],a[12:15],v[72:75]
v_perm_b32 v237, v235, v234, s87
aw a209,v236
mm v[72:75],v[212:215],a[16:19],v[72:75]
ld v[156:159],v245,s[44:47],s54 offen offset:3072 sc1
mm v[104:107],v[212:215],a[20:23],v[104:107]
aw a213,v237
ar v230,a242
mm v[104:107],v[208:211],a[0:3],v[104:107]
rt v[212:213],v242 offset:50816
rt v[214:215],v242 offset:51840
mm v[108:111],v[216:219],a[0:3],v[108:111]
ar v231,a246
v_perm_b32 v232, v231, v230, s86
mm v[108:111],v[220:223],a[20:23],v[108:111]
ld v[168:171],v246,s[44:47],s54 offen offset:3072 sc1
mm v[76:79],v[220:223],a[16:19],v[76:79]
v_perm_b32 v233, v231, v230, s87
aw a210,v232
mm v[76:79],v[216:219],a[12:15],v[76:79]
rt v[208:209],v242 offset:59264
rt v[210:211],v242 offset:60288
mm v[44:47],v[216:219],a[56:59],v[44:47]
aw a214,v233
ar v234,a250
mm v[44:47],v[220:223],a[8:11],v[44:47]
ar v235,a254
v_perm_b32 v236, v235, v234, s86
mm v[12:15],v[220:223],a[4:7],v[12:15]
wt lgkmcnt(4)
v_perm_b32 v237, v235, v234, s87
mm v[12:15],v[216:219],a[52:55],v[12:15]
rt v[220:221],v243 offset:50816
rt v[222:223],v243 offset:51840
mm v[16:19],v[196:199],a[52:55],v[16:19]
aw a211,v236
aw a215,v237
mm v[16:19],v[192:195],a[4:7],v[16:19]
ld v[172:175],v247,s[44:47],s54 offen offset:3072 sc1
mm v[48:51],v[192:195],a[8:11],v[48:51]
ar v230,a227
ar v231,a231
mm v[48:51],v[196:199],a[56:59],v[48:51]
rt v[216:217],v243 offset:59264
rt v[218:219],v243 offset:60288
mm v[80:83],v[196:199],a[12:15],v[80:83]
s_add_u32 m0, s83, 121472
v_perm_b32 v232, v231, v230, s86
mm v[80:83],v[192:195],a[16:19],v[80:83]
ld v227,s[56:59],s80 offen lds
mm v[112:115],v[192:195],a[20:23],v[112:115]
s_add_u32 s80, s80, 131072
v_perm_b32 v233, v231, v230, s87
mm v[112:115],v[196:199],a[0:3],v[112:115]
rt v[192:193],v240 offset:0
rt v[194:195],v240 offset:1024
mm v[116:119],v[204:207],a[0:3],v[116:119]
s_add_u32 m0, s83, 67584
aw a216,v232
mm v[116:119],v[200:203],a[20:23],v[116:119]
ld v224,s[56:59],s80 offen lds
mm v[84:87],v[200:203],a[16:19],v[84:87]
aw a220,v233
ar v234,a235
mm v[84:87],v[204:207],a[12:15],v[84:87]
rt v[196:197],v240 offset:8448
rt v[198:199],v240 offset:9472
mm v[52:55],v[204:207],a[56:59],v[52:55]
s_add_u32 m0, s83, 68608
ar v235,a239
mm v[52:55],v[200:203],a[8:11],v[52:55]
ld v225,s[56:59],s80 offen lds
mm v[20:23],v[200:203],a[4:7],v[20:23]
wt lgkmcnt(4)
v_perm_b32 v236, v235, v234, s86
mm v[20:23],v[204:207],a[52:55],v[20:23]
rt v[200:201],v241 offset:0
rt v[202:203],v241 offset:1024
mm v[24:27],v[212:215],a[52:55],v[24:27]
s_add_u32 m0, s83, 69760
v_perm_b32 v237, v235, v234, s87
mm v[24:27],v[208:211],a[4:7],v[24:27]
ld v226,s[56:59],s80 offen lds
wt vmcnt(19)
v_cmp_eq_u64 vcc, v[250:251], v[252:253]
mm v[56:59],v[208:211],a[8:11],v[56:59]
aw a217,v236
aw a221,v237
mm v[56:59],v[212:215],a[56:59],v[56:59]
rt v[204:205],v241 offset:8448
rt v[206:207],v241 offset:9472
mm v[88:91],v[212:215],a[12:15],v[88:91]
s_add_u32 m0, s83, 70784
ar v230,a243
mm v[88:91],v[208:211],a[16:19],v[88:91]
ld v227,s[56:59],s80 offen lds
mm v[120:123],v[208:211],a[20:23],v[120:123]
s_bfe_u32 s80, s4, 262172
s_xor_b32 s80, s80, s72
mm v[120:123],v[212:215],a[0:3],v[120:123]
rt v[208:209],v242 offset:0
rt v[210:211],v242 offset:1024
mm v[124:127],v[220:223],a[0:3],v[124:127]
s_lshl_b32 s80, s80, 18
ar v231,a247
mm v[124:127],v[216:219],a[20:23],v[124:127]
buffer_load_dwordx2 v[250:251], v248, s[64:67], s5 offen sc1
mm v[92:95],v[216:219],a[16:19],v[92:95]
v_perm_b32 v232, v231, v230, s86
v_perm_b32 v233, v231, v230, s87
mm v[92:95],v[220:223],a[12:15],v[92:95]
rt v[212:213],v242 offset:8448
rt v[214:215],v242 offset:9472
mm v[60:63],v[220:223],a[56:59],v[60:63]
s_add_u32 m0, s83, 84480
aw a218,v232
mm v[60:63],v[216:219],a[8:11],v[60:63]
ld v224,s[56:59],s80 offen lds
mm v[28:31],v[216:219],a[4:7],v[28:31]
wt lgkmcnt(4)
aw a222,v233
mm v[28:31],v[220:223],a[52:55],v[28:31]
rt v[216:217],v243 offset:0
rt v[218:219],v243 offset:1024
mm v[0:3],v[192:195],a[24:27],v[0:3]
s_add_u32 m0, s83, 85504
ar v234,a251
mm v[0:3],v[196:199],v[132:135],v[0:3]
ld v225,s[56:59],s80 offen lds
mm v[32:35],v[196:199],v[144:147],v[32:35]
ar v235,a255
v_perm_b32 v236, v235, v234, s86
mm v[32:35],v[192:195],a[28:31],v[32:35]
rt v[220:221],v243 offset:8448
rt v[222:223],v243 offset:9472
mm v[64:67],v[192:195],a[60:63],v[64:67]
s_add_u32 m0, s83, 86656
v_perm_b32 v237, v235, v234, s87
.La5_LW3_3:
s_cbranch_execz .La5_LA_RETURN
mm v[64:67],v[196:199],a[44:47],v[64:67]
ld v226,s[56:59],s80 offen lds
mm v[96:99],v[196:199],a[32:35],v[96:99]
aw a219,v236
aw a223,v237
mm v[96:99],v[192:195],v[128:131],v[96:99]
rt v[196:197],v240 offset:128
rt v[198:199],v240 offset:1152
mm v[100:103],v[200:203],v[128:131],v[100:103]
s_add_u32 m0, s83, 87680
mm v[100:103],v[204:207],a[32:35],v[100:103]
ld v227,s[56:59],s80 offen lds
mm v[68:71],v[204:207],a[44:47],v[68:71]
s_add_u32 s80, s80, 131072
mm v[68:71],v[200:203],a[60:63],v[68:71]
rt v[192:193],v240 offset:8576
rt v[194:195],v240 offset:9600
mm v[36:39],v[200:203],a[28:31],v[36:39]
s_add_u32 m0, s83, 101376
mm v[36:39],v[204:207],v[144:147],v[36:39]
ld v224,s[56:59],s80 offen lds
mm v[4:7],v[204:207],v[132:135],v[4:7]
wt lgkmcnt(4)
mm v[4:7],v[200:203],a[24:27],v[4:7]
rt v[204:205],v241 offset:128
rt v[206:207],v241 offset:1152
mm v[8:11],v[208:211],a[24:27],v[8:11]
s_add_u32 m0, s83, 102400
mm v[8:11],v[212:215],v[132:135],v[8:11]
ld v225,s[56:59],s80 offen lds
mm v[40:43],v[212:215],v[144:147],v[40:43]
mm v[40:43],v[208:211],a[28:31],v[40:43]
rt v[200:201],v241 offset:8576
rt v[202:203],v241 offset:9600
mm v[72:75],v[208:211],a[60:63],v[72:75]
s_add_u32 m0, s83, 103552
mm v[72:75],v[212:215],a[44:47],v[72:75]
ld v226,s[56:59],s80 offen lds
mm v[104:107],v[212:215],a[32:35],v[104:107]
mm v[104:107],v[208:211],v[128:131],v[104:107]
rt v[212:213],v242 offset:128
rt v[214:215],v242 offset:1152
mm v[108:111],v[216:219],v[128:131],v[108:111]
s_add_u32 m0, s83, 104576
mm v[108:111],v[220:223],a[32:35],v[108:111]
ld v227,s[56:59],s80 offen lds
mm v[76:79],v[220:223],a[44:47],v[76:79]
v_bfe_u32 v224, v255, 6, 1
v_lshrrev_b32 v225, 7, v255
mm v[76:79],v[216:219],a[60:63],v[76:79]
rt v[208:209],v242 offset:8576
rt v[210:211],v242 offset:9600
mm v[44:47],v[216:219],a[28:31],v[44:47]
s_bitcmp1_b32 vcc_lo, 6
s_cbranch_scc0 .La5_LO_NB6
.La5_LO_KB6:
mm v[44:47],v[220:223],v[144:147],v[44:47]
v_and_b32 v226, 48, v255
v_lshl_add_u32 v225, v225, 6, v226
mm v[12:15],v[220:223],v[132:135],v[12:15]
wt lgkmcnt(4)
v_lshl_add_u32 v224, v224, 11, v225
mm v[12:15],v[216:219],a[24:27],v[12:15]
rt v[220:221],v243 offset:128
rt v[222:223],v243 offset:1152
mm v[16:19],v[196:199],a[24:27],v[16:19]
mm v[16:19],v[192:195],v[132:135],v[16:19]
ld a[64:67],v244,s[44:47],s55 offen offset:0 sc1
mm v[48:51],v[192:195],v[144:147],v[48:51]
mm v[48:51],v[196:199],a[28:31],v[48:51]
rt v[216:217],v243 offset:8576
rt v[218:219],v243 offset:9600
mm v[80:83],v[196:199],a[60:63],v[80:83]
mm v[80:83],v[192:195],a[44:47],v[80:83]
ld a[68:71],v245,s[44:47],s55 offen offset:0 sc1
mm v[112:115],v[192:195],a[32:35],v[112:115]
wt vmcnt(25)
s_barrier
mm v[112:115],v[196:199],v[128:131],v[112:115]
rt v[192:193],v240 offset:16896
rt v[194:195],v240 offset:17920
mm v[116:119],v[204:207],v[128:131],v[116:119]
mm v[116:119],v[200:203],a[32:35],v[116:119]
ld a[72:75],v244,s[44:47],s55 offen offset:1024 sc1
mm v[84:87],v[200:203],a[44:47],v[84:87]
mm v[84:87],v[204:207],a[60:63],v[84:87]
rt v[196:197],v240 offset:25344
rt v[198:199],v240 offset:26368
mm v[52:55],v[204:207],a[28:31],v[52:55]
mm v[52:55],v[200:203],v[144:147],v[52:55]
ld a[76:79],v245,s[44:47],s55 offen offset:1024 sc1
mm v[20:23],v[200:203],v[132:135],v[20:23]
wt lgkmcnt(4)
mm v[20:23],v[204:207],a[24:27],v[20:23]
rt v[200:201],v241 offset:16896
rt v[202:203],v241 offset:17920
mm v[24:27],v[212:215],a[24:27],v[24:27]
mm v[24:27],v[208:211],v[132:135],v[24:27]
ld a[80:83],v244,s[44:47],s55 offen offset:2048 sc1
mm v[56:59],v[208:211],v[144:147],v[56:59]
mm v[56:59],v[212:215],a[28:31],v[56:59]
rt v[204:205],v241 offset:25344
rt v[206:207],v241 offset:26368
mm v[88:91],v[212:215],a[60:63],v[88:91]
mm v[88:91],v[208:211],a[44:47],v[88:91]
ld a[84:87],v245,s[44:47],s55 offen offset:2048 sc1
mm v[120:123],v[208:211],a[32:35],v[120:123]
mm v[120:123],v[212:215],v[128:131],v[120:123]
rt v[208:209],v242 offset:16896
rt v[210:211],v242 offset:17920
mm v[124:127],v[220:223],v[128:131],v[124:127]
mm v[124:127],v[216:219],a[32:35],v[124:127]
ld a[88:91],v244,s[44:47],s55 offen offset:3072 sc1
mm v[92:95],v[216:219],a[44:47],v[92:95]
mm v[92:95],v[220:223],a[60:63],v[92:95]
rt v[212:213],v242 offset:25344
rt v[214:215],v242 offset:26368
mm v[60:63],v[220:223],a[28:31],v[60:63]
mm v[60:63],v[216:219],v[144:147],v[60:63]
mm v[28:31],v[216:219],v[132:135],v[28:31]
wt vmcnt(21) lgkmcnt(4)
mm v[28:31],v[220:223],a[24:27],v[28:31]
rt v[216:217],v243 offset:16896
rt v[218:219],v243 offset:17920
mm v[0:3],v[192:195],a[36:39],v[0:3]
s_bitcmp1_b32 vcc_lo, 7
s_cbranch_scc0 .La5_LO_NB7
.La5_LO_KB7:
mm v[0:3],v[196:199],v[152:155],v[0:3]
ld a[92:95],v245,s[44:47],s55 offen offset:3072 sc1
mm v[32:35],v[196:199],v[156:159],v[32:35]
mm v[32:35],v[192:195],a[40:43],v[32:35]
rt v[220:221],v243 offset:25344
rt v[222:223],v243 offset:26368
mm v[64:67],v[192:195],v[136:139],v[64:67]
mm v[64:67],v[196:199],v[168:171],v[64:67]
ld a[0:3],v244,s[44:47],s52 offen offset:0 sc1
mm v[96:99],v[196:199],v[172:175],v[96:99]
mm v[96:99],v[192:195],v[140:143],v[96:99]
rt v[196:197],v240 offset:17024
rt v[198:199],v240 offset:18048
mm v[100:103],v[200:203],v[140:143],v[100:103]
mm v[100:103],v[204:207],v[172:175],v[100:103]
ld a[4:7],v245,s[44:47],s52 offen offset:0 sc1
mm v[68:71],v[204:207],v[168:171],v[68:71]
mm v[68:71],v[200:203],v[136:139],v[68:71]
rt v[192:193],v240 offset:25472
rt v[194:195],v240 offset:26496
mm v[36:39],v[200:203],a[40:43],v[36:39]
v_add_u32 v240, 67584, v240
mm v[36:39],v[204:207],v[156:159],v[36:39]
ld a[8:11],v244,s[44:47],s52 offen offset:1024 sc1
mm v[4:7],v[204:207],v[152:155],v[4:7]
wt lgkmcnt(4)
mm v[4:7],v[200:203],a[36:39],v[4:7]
rt v[204:205],v241 offset:17024
rt v[206:207],v241 offset:18048
mm v[8:11],v[208:211],a[36:39],v[8:11]
mm v[8:11],v[212:215],v[152:155],v[8:11]
ld a[12:15],v245,s[44:47],s52 offen offset:1024 sc1
mm v[40:43],v[212:215],v[156:159],v[40:43]
mm v[40:43],v[208:211],a[40:43],v[40:43]
rt v[200:201],v241 offset:25472
rt v[202:203],v241 offset:26496
mm v[72:75],v[208:211],v[136:139],v[72:75]
v_add_u32 v241, 67584, v241
mm v[72:75],v[212:215],v[168:171],v[72:75]
ld a[16:19],v244,s[44:47],s52 offen offset:2048 sc1
mm v[104:107],v[212:215],v[172:175],v[104:107]
mm v[104:107],v[208:211],v[140:143],v[104:107]
rt v[212:213],v242 offset:17024
rt v[214:215],v242 offset:18048
mm v[108:111],v[216:219],v[140:143],v[108:111]
mm v[108:111],v[220:223],v[172:175],v[108:111]
ld a[20:23],v245,s[44:47],s52 offen offset:2048 sc1
mm v[76:79],v[220:223],v[168:171],v[76:79]
mm v[76:79],v[216:219],v[136:139],v[76:79]
rt v[208:209],v242 offset:25472
rt v[210:211],v242 offset:26496
mm v[44:47],v[216:219],a[40:43],v[44:47]
v_add_u32 v242, 67584, v242
mm v[44:47],v[220:223],v[156:159],v[44:47]
wt vmcnt(22)
v_cmp_eq_u64 vcc, v[250:251], v[252:253]
mm v[12:15],v[220:223],v[152:155],v[12:15]
wt lgkmcnt(4)
mm v[12:15],v[216:219],a[36:39],v[12:15]
rt v[220:221],v243 offset:17024
rt v[222:223],v243 offset:18048
mm v[16:19],v[196:199],a[36:39],v[16:19]
mm v[16:19],v[192:195],v[152:155],v[16:19]
ld a[24:27],v244,s[44:47],s52 offen offset:3072 sc1
mm v[48:51],v[192:195],v[156:159],v[48:51]
mm v[48:51],v[196:199],a[40:43],v[48:51]
rt v[216:217],v243 offset:25472
rt v[218:219],v243 offset:26496
mm v[80:83],v[196:199],v[136:139],v[80:83]
v_add_u32 v243, 67584, v243
mm v[80:83],v[192:195],v[168:171],v[80:83]
ld a[28:31],v245,s[44:47],s52 offen offset:3072 sc1
mm v[112:115],v[192:195],v[172:175],v[112:115]
wt vmcnt(16)
s_barrier
mm v[112:115],v[196:199],v[140:143],v[112:115]
rt v[192:193],v240 offset:50688
rt v[194:195],v240 offset:51712
mm v[116:119],v[204:207],v[140:143],v[116:119]
mm v[116:119],v[200:203],v[172:175],v[116:119]
ld a[32:35],v246,s[44:47],s55 offen offset:0 sc1
mm v[84:87],v[200:203],v[168:171],v[84:87]
mm v[84:87],v[204:207],v[136:139],v[84:87]
rt v[196:197],v240 offset:59136
rt v[198:199],v240 offset:60160
mm v[52:55],v[204:207],a[40:43],v[52:55]
mm v[52:55],v[200:203],v[156:159],v[52:55]
ld a[44:47],v247,s[44:47],s55 offen offset:0 sc1
mm v[20:23],v[200:203],v[152:155],v[20:23]
wt lgkmcnt(4)
mm v[20:23],v[204:207],a[36:39],v[20:23]
rt v[200:201],v240 offset:0
rt v[202:203],v240 offset:1024
mm v[24:27],v[212:215],a[36:39],v[24:27]
mm v[24:27],v[208:211],v[152:155],v[24:27]
ld a[48:51],v246,s[44:47],s55 offen offset:1024 sc1
mm v[56:59],v[208:211],v[156:159],v[56:59]
mm v[56:59],v[212:215],a[40:43],v[56:59]
rt v[204:205],v240 offset:8448
rt v[206:207],v240 offset:9472
mm v[88:91],v[212:215],v[136:139],v[88:91]
mm v[88:91],v[208:211],v[168:171],v[88:91]
ld a[52:55],v247,s[44:47],s55 offen offset:1024 sc1
mm v[120:123],v[208:211],v[172:175],v[120:123]
mm v[120:123],v[212:215],v[140:143],v[120:123]
rt v[208:209],v240 offset:16896
rt v[210:211],v240 offset:17920
mm v[124:127],v[220:223],v[140:143],v[124:127]
mm v[124:127],v[216:219],v[172:175],v[124:127]
ld a[56:59],v246,s[44:47],s55 offen offset:2048 sc1
mm v[92:95],v[216:219],v[168:171],v[92:95]
mm v[92:95],v[220:223],v[136:139],v[92:95]
rt v[212:213],v240 offset:25344
rt v[214:215],v240 offset:26368
mm v[60:63],v[220:223],a[40:43],v[60:63]
mm v[60:63],v[216:219],v[156:159],v[60:63]
mm v[28:31],v[216:219],v[152:155],v[28:31]
wt vmcnt(13) lgkmcnt(4)
mm v[28:31],v[220:223],a[36:39],v[28:31]
rt v[216:217],v240 offset:33792
rt v[218:219],v240 offset:34816
mm v[0:3],v[192:195],a[64:67],v[0:3]
ld a[36:39],v247,s[44:47],s55 offen offset:2048 sc1
mm v[0:3],v[196:199],a[72:75],v[0:3]
rt v[220:221],v240 offset:42240
rt v[222:223],v240 offset:43264
mm v[32:35],v[196:199],a[76:79],v[32:35]
mm v[32:35],v[192:195],a[68:71],v[32:35]
rt v[196:197],v241 offset:50688
rt v[198:199],v241 offset:51712
mm v[32:35],v[200:203],a[84:87],v[32:35]
ld a[40:43],v246,s[44:47],s55 offen offset:3072 sc1
mm v[32:35],v[204:207],a[92:95],v[32:35]
rt v[192:193],v241 offset:59136
rt v[194:195],v241 offset:60160
mm v[0:3],v[204:207],a[88:91],v[0:3]
wt vmcnt(7) lgkmcnt(4)
mm v[0:3],v[200:203],a[80:83],v[0:3]
rt v[204:205],v241 offset:0
rt v[206:207],v241 offset:1024
mm v[0:3],v[208:211],a[0:3],v[0:3]
ld a[60:63],v247,s[44:47],s55 offen offset:3072 sc1
mm v[0:3],v[212:215],a[8:11],v[0:3]
rt v[200:201],v241 offset:8448
rt v[202:203],v241 offset:9472
mm v[32:35],v[212:215],a[12:15],v[32:35]
mm v[32:35],v[208:211],a[4:7],v[32:35]
rt v[212:213],v241 offset:16896
rt v[214:215],v241 offset:17920
mm v[32:35],v[216:219],a[20:23],v[32:35]
ld v[128:131],v246,s[44:47],s52 offen offset:0 sc1
mm v[32:35],v[220:223],a[28:31],v[32:35]
rt v[208:209],v241 offset:25344
rt v[210:211],v241 offset:26368
mm v[0:3],v[220:223],a[24:27],v[0:3]
wt lgkmcnt(4)
mm v[0:3],v[216:219],a[16:19],v[0:3]
rt v[220:221],v241 offset:33792
rt v[222:223],v241 offset:34816
mm v[4:7],v[196:199],a[64:67],v[4:7]
ld v[132:135],v247,s[44:47],s52 offen offset:0 sc1
mm v[4:7],v[192:195],a[72:75],v[4:7]
rt v[216:217],v241 offset:42240
rt v[218:219],v241 offset:43264
mm v[36:39],v[192:195],a[76:79],v[36:39]
mm v[36:39],v[196:199],a[68:71],v[36:39]
rt v[192:193],v242 offset:50688
rt v[194:195],v242 offset:51712
mm v[36:39],v[204:207],a[84:87],v[36:39]
ld v[144:147],v246,s[44:47],s52 offen offset:1024 sc1
mm v[36:39],v[200:203],a[92:95],v[36:39]
rt v[196:197],v242 offset:59136
rt v[198:199],v242 offset:60160
mm v[4:7],v[200:203],a[88:91],v[4:7]
wt lgkmcnt(4)
mm v[4:7],v[204:207],a[80:83],v[4:7]
rt v[200:201],v242 offset:0
rt v[202:203],v242 offset:1024
mm v[4:7],v[212:215],a[0:3],v[4:7]
ld v[148:151],v247,s[44:47],s52 offen offset:1024 sc1
mm v[4:7],v[208:211],a[8:11],v[4:7]
rt v[204:205],v242 offset:8448
rt v[206:207],v242 offset:9472
mm v[36:39],v[208:211],a[12:15],v[36:39]
mm v[36:39],v[212:215],a[4:7],v[36:39]
rt v[208:209],v242 offset:16896
rt v[210:211],v242 offset:17920
mm v[36:39],v[220:223],a[20:23],v[36:39]
mm v[36:39],v[216:219],a[28:31],v[36:39]
rt v[212:213],v242 offset:25344
rt v[214:215],v242 offset:26368
mm v[4:7],v[216:219],a[24:27],v[4:7]
wt lgkmcnt(4)
mm v[4:7],v[220:223],a[16:19],v[4:7]
rt v[216:217],v242 offset:33792
rt v[218:219],v242 offset:34816
mm v[8:11],v[192:195],a[64:67],v[8:11]
ld v[160:163],v246,s[44:47],s52 offen offset:2048 sc1
mm v[8:11],v[196:199],a[72:75],v[8:11]
rt v[220:221],v242 offset:42240
rt v[222:223],v242 offset:43264
mm v[40:43],v[196:199],a[76:79],v[40:43]
cv v234,v32,v33
cv v236,v36,v37
mm v[40:43],v[192:195],a[68:71],v[40:43]
rt v[196:197],v243 offset:50688
rt v[198:199],v243 offset:51712
mm v[40:43],v[200:203],a[84:87],v[40:43]
ld v[164:167],v247,s[44:47],s52 offen offset:2048 sc1
mm v[40:43],v[204:207],a[92:95],v[40:43]
rt v[192:193],v243 offset:59136
rt v[194:195],v243 offset:60160
mm v[8:11],v[204:207],a[88:91],v[8:11]
wt lgkmcnt(4)
mm v[8:11],v[200:203],a[80:83],v[8:11]
rt v[204:205],v243 offset:0
rt v[206:207],v243 offset:1024
mm v[8:11],v[208:211],a[0:3],v[8:11]
ld v[176:179],v246,s[44:47],s52 offen offset:3072 sc1
mm v[8:11],v[212:215],a[8:11],v[8:11]
rt v[200:201],v243 offset:8448
rt v[202:203],v243 offset:9472
mm v[40:43],v[212:215],a[12:15],v[40:43]
cv v235,v34,v35
cv v237,v38,v39
mm v[40:43],v[208:211],a[4:7],v[40:43]
rt v[212:213],v243 offset:16896
rt v[214:215],v243 offset:17920
mm v[40:43],v[216:219],a[20:23],v[40:43]
ld v[180:183],v247,s[44:47],s52 offen offset:3072 sc1
mm v[40:43],v[220:223],a[28:31],v[40:43]
rt v[208:209],v243 offset:25344
rt v[210:211],v243 offset:26368
mm v[8:11],v[220:223],a[24:27],v[8:11]
wt lgkmcnt(4)
v_bfe_u32 v250, v255, 6, 1
mm v[8:11],v[216:219],a[16:19],v[8:11]
rt v[220:221],v243 offset:33792
rt v[222:223],v243 offset:34816
mm v[12:15],v[196:199],a[64:67],v[12:15]
v_permlane16_swap_b32 v234, v236
v_permlane16_swap_b32 v235, v237
mm v[12:15],v[192:195],a[72:75],v[12:15]
rt v[216:217],v243 offset:42240
rt v[218:219],v243 offset:43264
mm v[44:47],v[192:195],a[76:79],v[44:47]
cv v230,v0,v1
cv v232,v4,v5
mm v[44:47],v[196:199],a[68:71],v[44:47]
rt v[192:193],v240 offset:50816
rt v[194:195],v240 offset:51840
mm v[44:47],v[204:207],a[84:87],v[44:47]
st v[234:237],v238,s[32:35],s95 offen offset:0 sc1
mm v[44:47],v[200:203],a[92:95],v[44:47]
rt v[196:197],v240 offset:59264
rt v[198:199],v240 offset:60288
mm v[12:15],v[200:203],a[88:91],v[12:15]
wt lgkmcnt(4)
v_mul_u32_u24 v250, 7, v250
mm v[12:15],v[204:207],a[80:83],v[12:15]
rt v[200:201],v240 offset:128
rt v[202:203],v240 offset:1152
mm v[12:15],v[212:215],a[0:3],v[12:15]
cv v231,v2,v3
cv v233,v6,v7
mm v[12:15],v[208:211],a[8:11],v[12:15]
rt v[204:205],v240 offset:8576
rt v[206:207],v240 offset:9600
mm v[44:47],v[208:211],a[12:15],v[44:47]
v_permlane16_swap_b32 v230, v232
v_permlane16_swap_b32 v231, v233
mm v[44:47],v[212:215],a[4:7],v[44:47]
rt v[208:209],v240 offset:17024
rt v[210:211],v240 offset:18048
mm v[44:47],v[220:223],a[20:23],v[44:47]
st v[230:233],v238,s[32:35],s94 offen offset:0 sc1
mm v[44:47],v[216:219],a[28:31],v[44:47]
rt v[212:213],v240 offset:25472
rt v[214:215],v240 offset:26496
mm v[12:15],v[216:219],a[24:27],v[12:15]
wt lgkmcnt(4)
v_xor_b32 v250, s3, v250
mm v[12:15],v[220:223],a[16:19],v[12:15]
rt v[216:217],v240 offset:33920
rt v[218:219],v240 offset:34944
mm v[16:19],v[192:195],a[64:67],v[16:19]
cv v234,v40,v41
cv v236,v44,v45
mm v[16:19],v[196:199],a[72:75],v[16:19]
rt v[220:221],v240 offset:42368
rt v[222:223],v240 offset:43392
mm v[48:51],v[196:199],a[76:79],v[48:51]
cv v235,v42,v43
cv v237,v46,v47
mm v[48:51],v[192:195],a[68:71],v[48:51]
rt v[196:197],v241 offset:50816
rt v[198:199],v241 offset:51840
mm v[48:51],v[200:203],a[84:87],v[48:51]
v_permlane16_swap_b32 v234, v236
v_permlane16_swap_b32 v235, v237
mm v[48:51],v[204:207],a[92:95],v[48:51]
rt v[192:193],v241 offset:59264
rt v[194:195],v241 offset:60288
mm v[16:19],v[204:207],a[88:91],v[16:19]
wt lgkmcnt(4)
v_bfe_u32 v251, v255, 4, 2
mm v[16:19],v[200:203],a[80:83],v[16:19]
rt v[204:205],v241 offset:128
rt v[206:207],v241 offset:1152
mm v[16:19],v[208:211],a[0:3],v[16:19]
st v[234:237],v238,s[32:35],s95 offen offset:1024 sc1
mm v[16:19],v[212:215],a[8:11],v[16:19]
rt v[200:201],v241 offset:8576
rt v[202:203],v241 offset:9600
mm v[48:51],v[212:215],a[12:15],v[48:51]
cv v230,v8,v9
cv v232,v12,v13
mm v[48:51],v[208:211],a[4:7],v[48:51]
rt v[212:213],v241 offset:17024
rt v[214:215],v241 offset:18048
mm v[48:51],v[216:219],a[20:23],v[48:51]
cv v231,v10,v11
cv v233,v14,v15
mm v[48:51],v[220:223],a[28:31],v[48:51]
rt v[208:209],v241 offset:25472
rt v[210:211],v241 offset:26496
mm v[16:19],v[220:223],a[24:27],v[16:19]
wt lgkmcnt(4)
v_lshl_add_u32 v250, v250, 2, v251
mm v[16:19],v[216:219],a[16:19],v[16:19]
rt v[220:221],v241 offset:33920
rt v[222:223],v241 offset:34944
mm v[20:23],v[196:199],a[64:67],v[20:23]
v_permlane16_swap_b32 v230, v232
v_permlane16_swap_b32 v231, v233
mm v[20:23],v[192:195],a[72:75],v[20:23]
rt v[216:217],v241 offset:42368
rt v[218:219],v241 offset:43392
mm v[52:55],v[192:195],a[76:79],v[52:55]
v_lshlrev_b32 v250, 9, v250
v_and_b32 v251, 64, v255
mm v[52:55],v[196:199],a[68:71],v[52:55]
rt v[192:193],v242 offset:50816
rt v[194:195],v242 offset:51840
mm v[52:55],v[204:207],a[84:87],v[52:55]
st v[230:233],v238,s[32:35],s94 offen offset:1024 sc1
mm v[52:55],v[200:203],a[92:95],v[52:55]
rt v[196:197],v242 offset:59264
rt v[198:199],v242 offset:60288
mm v[20:23],v[200:203],a[88:91],v[20:23]
wt lgkmcnt(4)
v_lshl_add_u32 v251, v251, 24, v250
mm v[20:23],v[204:207],a[80:83],v[20:23]
rt v[200:201],v242 offset:128
rt v[202:203],v242 offset:1152
mm v[20:23],v[212:215],a[0:3],v[20:23]
ld v[136:139],v224,s[68:71],0 offen offset:0
mm v[20:23],v[208:211],a[8:11],v[20:23]
rt v[204:205],v242 offset:8576
rt v[206:207],v242 offset:9600
mm v[52:55],v[208:211],a[12:15],v[52:55]
v_and_b32 v248, 0x1f0, v255
v_lshrrev_b32 v248, 1, v248
mm v[52:55],v[212:215],a[4:7],v[52:55]
rt v[208:209],v242 offset:17024
rt v[210:211],v242 offset:18048
mm v[52:55],v[220:223],a[20:23],v[52:55]
ld v[140:143],v225,s[48:51],0 offen offset:0
mm v[52:55],v[216:219],a[28:31],v[52:55]
rt v[212:213],v242 offset:25472
rt v[214:215],v242 offset:26496
mm v[20:23],v[216:219],a[24:27],v[20:23]
wt lgkmcnt(4)
s_mov_b32 s93, 0
ld a[224:227],v229,s[28:31],s93 offen offset:0
mm v[20:23],v[220:223],a[16:19],v[20:23]
rt v[216:217],v242 offset:33920
rt v[218:219],v242 offset:34944
mm v[24:27],v[192:195],a[64:67],v[24:27]
cv v234,v48,v49
cv v236,v52,v53
mm v[24:27],v[196:199],a[72:75],v[24:27]
rt v[220:221],v242 offset:42368
rt v[222:223],v242 offset:43392
mm v[56:59],v[196:199],a[76:79],v[56:59]
cv v235,v50,v51
cv v237,v54,v55
mm v[56:59],v[192:195],a[68:71],v[56:59]
rt v[196:197],v243 offset:50816
rt v[198:199],v243 offset:51840
mm v[56:59],v[200:203],a[84:87],v[56:59]
v_permlane16_swap_b32 v234, v236
v_permlane16_swap_b32 v235, v237
mm v[56:59],v[204:207],a[92:95],v[56:59]
rt v[192:193],v243 offset:59264
rt v[194:195],v243 offset:60288
mm v[24:27],v[204:207],a[88:91],v[24:27]
wt lgkmcnt(4)
ld a[228:231],v229,s[28:31],s93 offen offset:2048
mm v[24:27],v[200:203],a[80:83],v[24:27]
rt v[204:205],v243 offset:128
rt v[206:207],v243 offset:1152
mm v[24:27],v[208:211],a[0:3],v[24:27]
st v[234:237],v238,s[32:35],s95 offen offset:2048 sc1
mm v[24:27],v[212:215],a[8:11],v[24:27]
rt v[200:201],v243 offset:8576
rt v[202:203],v243 offset:9600
mm v[56:59],v[212:215],a[12:15],v[56:59]
cv v230,v16,v17
cv v232,v20,v21
.La5_LW3_4:
s_cbranch_execz .La5_LA_RETURN
mm v[56:59],v[208:211],a[4:7],v[56:59]
rt v[212:213],v243 offset:17024
rt v[214:215],v243 offset:18048
mm v[56:59],v[216:219],a[20:23],v[56:59]
cv v231,v18,v19
cv v233,v22,v23
mm v[56:59],v[220:223],a[28:31],v[56:59]
rt v[208:209],v243 offset:25472
rt v[210:211],v243 offset:26496
mm v[24:27],v[220:223],a[24:27],v[24:27]
wt lgkmcnt(4)
s_mov_b32 s93, 4096
ld a[232:235],v229,s[28:31],s93 offen offset:0
mm v[24:27],v[216:219],a[16:19],v[24:27]
rt v[220:221],v243 offset:33920
rt v[222:223],v243 offset:34944
mm v[28:31],v[196:199],a[64:67],v[28:31]
v_permlane16_swap_b32 v230, v232
v_permlane16_swap_b32 v231, v233
mm v[28:31],v[192:195],a[72:75],v[28:31]
rt v[216:217],v243 offset:42368
rt v[218:219],v243 offset:43392
mm v[60:63],v[192:195],a[76:79],v[60:63]
ld a[236:239],v229,s[28:31],s93 offen offset:2048
mm v[60:63],v[196:199],a[68:71],v[60:63]
rt v[192:193],v240 offset:50688
rt v[194:195],v240 offset:51712
mm v[60:63],v[204:207],a[84:87],v[60:63]
st v[230:233],v238,s[32:35],s94 offen offset:2048 sc1
mm v[60:63],v[200:203],a[92:95],v[60:63]
rt v[196:197],v240 offset:59136
rt v[198:199],v240 offset:60160
mm v[28:31],v[200:203],a[88:91],v[28:31]
wt lgkmcnt(4)
s_mov_b32 s93, 8192
ld a[240:243],v229,s[28:31],s93 offen offset:0
mm v[28:31],v[204:207],a[80:83],v[28:31]
rt v[200:201],v240 offset:0
rt v[202:203],v240 offset:1024
mm v[28:31],v[212:215],a[0:3],v[28:31]
ld v[152:155],v224,s[68:71],0 offen offset:512
mm v[28:31],v[208:211],a[8:11],v[28:31]
rt v[204:205],v240 offset:8448
rt v[206:207],v240 offset:9472
mm v[60:63],v[208:211],a[12:15],v[60:63]
ld a[244:247],v229,s[28:31],s93 offen offset:2048
mm v[60:63],v[212:215],a[4:7],v[60:63]
rt v[208:209],v240 offset:16896
rt v[210:211],v240 offset:17920
mm v[60:63],v[220:223],a[20:23],v[60:63]
ld v[156:159],v225,s[48:51],0 offen offset:512
mm v[60:63],v[216:219],a[28:31],v[60:63]
rt v[212:213],v240 offset:25344
rt v[214:215],v240 offset:26368
mm v[28:31],v[216:219],a[24:27],v[28:31]
wt vmcnt(24) lgkmcnt(4)
s_mov_b32 s93, 12288
ld a[248:251],v229,s[28:31],s93 offen offset:0
mm v[28:31],v[220:223],a[16:19],v[28:31]
rt v[216:217],v240 offset:33792
rt v[218:219],v240 offset:34816
mm v[64:67],v[192:195],a[32:35],v[64:67]
cv v234,v56,v57
cv v236,v60,v61
mm v[64:67],v[196:199],a[48:51],v[64:67]
rt v[220:221],v240 offset:42240
rt v[222:223],v240 offset:43264
mm v[96:99],v[196:199],a[52:55],v[96:99]
cv v235,v58,v59
cv v237,v62,v63
mm v[96:99],v[192:195],a[44:47],v[96:99]
rt v[196:197],v241 offset:50688
rt v[198:199],v241 offset:51712
mm v[96:99],v[200:203],a[36:39],v[96:99]
v_permlane16_swap_b32 v234, v236
v_permlane16_swap_b32 v235, v237
mm v[96:99],v[204:207],a[60:63],v[96:99]
rt v[192:193],v241 offset:59136
rt v[194:195],v241 offset:60160
mm v[64:67],v[204:207],a[40:43],v[64:67]
wt vmcnt(17) lgkmcnt(4)
ld a[252:255],v229,s[28:31],s93 offen offset:2048
mm v[64:67],v[200:203],a[56:59],v[64:67]
rt v[204:205],v241 offset:0
rt v[206:207],v241 offset:1024
mm v[64:67],v[208:211],v[128:131],v[64:67]
st v[234:237],v238,s[32:35],s95 offen offset:3072 sc1
mm v[64:67],v[212:215],v[144:147],v[64:67]
rt v[200:201],v241 offset:8448
rt v[202:203],v241 offset:9472
mm v[96:99],v[212:215],v[148:151],v[96:99]
cv v230,v24,v25
cv v232,v28,v29
mm v[96:99],v[208:211],v[132:135],v[96:99]
rt v[212:213],v241 offset:16896
rt v[214:215],v241 offset:17920
mm v[96:99],v[216:219],v[164:167],v[96:99]
cv v231,v26,v27
cv v233,v30,v31
mm v[96:99],v[220:223],v[180:183],v[96:99]
rt v[208:209],v241 offset:25344
rt v[210:211],v241 offset:26368
mm v[64:67],v[220:223],v[176:179],v[64:67]
wt lgkmcnt(4)
s_mov_b32 s93, 0
ld a[64:67],v229,s[28:31],s93 offen offset:256
mm v[64:67],v[216:219],v[160:163],v[64:67]
rt v[220:221],v241 offset:33792
rt v[222:223],v241 offset:34816
mm v[68:71],v[196:199],a[32:35],v[68:71]
v_permlane16_swap_b32 v230, v232
v_permlane16_swap_b32 v231, v233
mm v[68:71],v[192:195],a[48:51],v[68:71]
rt v[216:217],v241 offset:42240
rt v[218:219],v241 offset:43264
mm v[100:103],v[192:195],a[52:55],v[100:103]
ld a[68:71],v229,s[28:31],s93 offen offset:2304
mm v[100:103],v[196:199],a[44:47],v[100:103]
rt v[192:193],v242 offset:50688
rt v[194:195],v242 offset:51712
mm v[100:103],v[204:207],a[36:39],v[100:103]
st v[230:233],v238,s[32:35],s94 offen offset:3072 sc1
mm v[100:103],v[200:203],a[60:63],v[100:103]
rt v[196:197],v242 offset:59136
rt v[198:199],v242 offset:60160
mm v[68:71],v[200:203],a[40:43],v[68:71]
wt lgkmcnt(4)
s_mov_b32 s93, 4096
ld a[72:75],v229,s[28:31],s93 offen offset:256
mm v[68:71],v[204:207],a[56:59],v[68:71]
rt v[200:201],v242 offset:0
rt v[202:203],v242 offset:1024
mm v[68:71],v[212:215],v[128:131],v[68:71]
ld v[168:171],v224,s[68:71],0 offen offset:1024
mm v[68:71],v[208:211],v[144:147],v[68:71]
rt v[204:205],v242 offset:8448
rt v[206:207],v242 offset:9472
mm v[100:103],v[208:211],v[148:151],v[100:103]
ld a[76:79],v229,s[28:31],s93 offen offset:2304
mm v[100:103],v[212:215],v[132:135],v[100:103]
rt v[208:209],v242 offset:16896
rt v[210:211],v242 offset:17920
mm v[100:103],v[220:223],v[164:167],v[100:103]
ld v[172:175],v225,s[48:51],0 offen offset:1024
mm v[100:103],v[216:219],v[180:183],v[100:103]
rt v[212:213],v242 offset:25344
rt v[214:215],v242 offset:26368
mm v[68:71],v[216:219],v[176:179],v[68:71]
wt lgkmcnt(4)
s_mov_b32 s93, 8192
ld a[80:83],v229,s[28:31],s93 offen offset:256
mm v[68:71],v[220:223],v[160:163],v[68:71]
rt v[216:217],v242 offset:33792
rt v[218:219],v242 offset:34816
mm v[72:75],v[192:195],a[32:35],v[72:75]
cv v234,v96,v97
cv v236,v100,v101
mm v[72:75],v[196:199],a[48:51],v[72:75]
rt v[220:221],v242 offset:42240
rt v[222:223],v242 offset:43264
mm v[104:107],v[196:199],a[52:55],v[104:107]
cv v235,v98,v99
cv v237,v102,v103
mm v[104:107],v[192:195],a[44:47],v[104:107]
rt v[196:197],v243 offset:50688
rt v[198:199],v243 offset:51712
mm v[104:107],v[200:203],a[36:39],v[104:107]
v_permlane16_swap_b32 v234, v236
v_permlane16_swap_b32 v235, v237
mm v[104:107],v[204:207],a[60:63],v[104:107]
rt v[192:193],v243 offset:59136
rt v[194:195],v243 offset:60160
mm v[72:75],v[204:207],a[40:43],v[72:75]
wt lgkmcnt(4)
ld a[84:87],v229,s[28:31],s93 offen offset:2304
mm v[72:75],v[200:203],a[56:59],v[72:75]
rt v[204:205],v243 offset:0
rt v[206:207],v243 offset:1024
mm v[72:75],v[208:211],v[128:131],v[72:75]
st v[234:237],v238,s[32:35],s97 offen offset:0 sc1
mm v[72:75],v[212:215],v[144:147],v[72:75]
rt v[200:201],v243 offset:8448
rt v[202:203],v243 offset:9472
mm v[104:107],v[212:215],v[148:151],v[104:107]
cv v230,v64,v65
cv v232,v68,v69
mm v[104:107],v[208:211],v[132:135],v[104:107]
rt v[212:213],v243 offset:16896
rt v[214:215],v243 offset:17920
mm v[104:107],v[216:219],v[164:167],v[104:107]
cv v231,v66,v67
cv v233,v70,v71
mm v[104:107],v[220:223],v[180:183],v[104:107]
rt v[208:209],v243 offset:25344
rt v[210:211],v243 offset:26368
mm v[72:75],v[220:223],v[176:179],v[72:75]
wt lgkmcnt(4)
s_mov_b32 s93, 12288
ld a[88:91],v229,s[28:31],s93 offen offset:256
mm v[72:75],v[216:219],v[160:163],v[72:75]
rt v[220:221],v243 offset:33792
rt v[222:223],v243 offset:34816
mm v[76:79],v[196:199],a[32:35],v[76:79]
v_permlane16_swap_b32 v230, v232
v_permlane16_swap_b32 v231, v233
mm v[76:79],v[192:195],a[48:51],v[76:79]
rt v[216:217],v243 offset:42240
rt v[218:219],v243 offset:43264
mm v[108:111],v[192:195],a[52:55],v[108:111]
ld a[92:95],v229,s[28:31],s93 offen offset:2304
mm v[108:111],v[196:199],a[44:47],v[108:111]
rt v[192:193],v240 offset:50816
rt v[194:195],v240 offset:51840
mm v[108:111],v[204:207],a[36:39],v[108:111]
st v[230:233],v238,s[32:35],s96 offen offset:0 sc1
mm v[108:111],v[200:203],a[60:63],v[108:111]
rt v[196:197],v240 offset:59264
rt v[198:199],v240 offset:60288
mm v[76:79],v[200:203],a[40:43],v[76:79]
wt lgkmcnt(4)
mm v[76:79],v[204:207],a[56:59],v[76:79]
rt v[200:201],v240 offset:128
rt v[202:203],v240 offset:1152
mm v[76:79],v[212:215],v[128:131],v[76:79]
ld v[184:187],v224,s[68:71],0 offen offset:1536
mm v[76:79],v[208:211],v[144:147],v[76:79]
rt v[204:205],v240 offset:8576
rt v[206:207],v240 offset:9600
mm v[108:111],v[208:211],v[148:151],v[108:111]
mm v[108:111],v[212:215],v[132:135],v[108:111]
rt v[208:209],v240 offset:17024
rt v[210:211],v240 offset:18048
mm v[108:111],v[220:223],v[164:167],v[108:111]
ld v[188:191],v225,s[48:51],0 offen offset:1536
mm v[108:111],v[216:219],v[180:183],v[108:111]
rt v[212:213],v240 offset:25472
rt v[214:215],v240 offset:26496
mm v[76:79],v[216:219],v[176:179],v[76:79]
wt lgkmcnt(4)
mm v[76:79],v[220:223],v[160:163],v[76:79]
rt v[216:217],v240 offset:33920
rt v[218:219],v240 offset:34944
mm v[80:83],v[192:195],a[32:35],v[80:83]
cv v234,v104,v105
cv v236,v108,v109
mm v[80:83],v[196:199],a[48:51],v[80:83]
rt v[220:221],v240 offset:42368
rt v[222:223],v240 offset:43392
mm v[112:115],v[196:199],a[52:55],v[112:115]
cv v235,v106,v107
cv v237,v110,v111
mm v[112:115],v[192:195],a[44:47],v[112:115]
rt v[196:197],v241 offset:50816
rt v[198:199],v241 offset:51840
mm v[112:115],v[200:203],a[36:39],v[112:115]
v_permlane16_swap_b32 v234, v236
v_permlane16_swap_b32 v235, v237
mm v[112:115],v[204:207],a[60:63],v[112:115]
rt v[192:193],v241 offset:59264
rt v[194:195],v241 offset:60288
mm v[80:83],v[204:207],a[40:43],v[80:83]
wt lgkmcnt(4)
mm v[80:83],v[200:203],a[56:59],v[80:83]
rt v[204:205],v241 offset:128
rt v[206:207],v241 offset:1152
mm v[80:83],v[208:211],v[128:131],v[80:83]
st v[234:237],v238,s[32:35],s97 offen offset:1024 sc1
mm v[80:83],v[212:215],v[144:147],v[80:83]
rt v[200:201],v241 offset:8576
rt v[202:203],v241 offset:9600
mm v[112:115],v[212:215],v[148:151],v[112:115]
cv v230,v72,v73
cv v232,v76,v77
mm v[112:115],v[208:211],v[132:135],v[112:115]
rt v[212:213],v241 offset:17024
rt v[214:215],v241 offset:18048
mm v[112:115],v[216:219],v[164:167],v[112:115]
cv v231,v74,v75
cv v233,v78,v79
mm v[112:115],v[220:223],v[180:183],v[112:115]
rt v[208:209],v241 offset:25472
rt v[210:211],v241 offset:26496
mm v[80:83],v[220:223],v[176:179],v[80:83]
wt lgkmcnt(4)
mm v[80:83],v[216:219],v[160:163],v[80:83]
rt v[220:221],v241 offset:33920
rt v[222:223],v241 offset:34944
mm v[84:87],v[196:199],a[32:35],v[84:87]
v_permlane16_swap_b32 v230, v232
v_permlane16_swap_b32 v231, v233
mm v[84:87],v[192:195],a[48:51],v[84:87]
rt v[216:217],v241 offset:42368
rt v[218:219],v241 offset:43392
mm v[116:119],v[192:195],a[52:55],v[116:119]
mm v[116:119],v[196:199],a[44:47],v[116:119]
rt v[192:193],v242 offset:50816
rt v[194:195],v242 offset:51840
mm v[116:119],v[204:207],a[36:39],v[116:119]
st v[230:233],v238,s[32:35],s96 offen offset:1024 sc1
mm v[116:119],v[200:203],a[60:63],v[116:119]
rt v[196:197],v242 offset:59264
rt v[198:199],v242 offset:60288
mm v[84:87],v[200:203],a[40:43],v[84:87]
wt lgkmcnt(4)
mm v[84:87],v[204:207],a[56:59],v[84:87]
rt v[200:201],v242 offset:128
rt v[202:203],v242 offset:1152
mm v[84:87],v[212:215],v[128:131],v[84:87]
mm v[84:87],v[208:211],v[144:147],v[84:87]
rt v[204:205],v242 offset:8576
rt v[206:207],v242 offset:9600
mm v[116:119],v[208:211],v[148:151],v[116:119]
mm v[116:119],v[212:215],v[132:135],v[116:119]
rt v[208:209],v242 offset:17024
rt v[210:211],v242 offset:18048
mm v[116:119],v[220:223],v[164:167],v[116:119]
mm v[116:119],v[216:219],v[180:183],v[116:119]
rt v[212:213],v242 offset:25472
rt v[214:215],v242 offset:26496
mm v[84:87],v[216:219],v[176:179],v[84:87]
wt lgkmcnt(4)
mm v[84:87],v[220:223],v[160:163],v[84:87]
rt v[216:217],v242 offset:33920
rt v[218:219],v242 offset:34944
mm v[88:91],v[192:195],a[32:35],v[88:91]
cv v234,v112,v113
cv v236,v116,v117
mm v[88:91],v[196:199],a[48:51],v[88:91]
rt v[220:221],v242 offset:42368
rt v[222:223],v242 offset:43392
mm v[120:123],v[196:199],a[52:55],v[120:123]
cv v235,v114,v115
cv v237,v118,v119
mm v[120:123],v[192:195],a[44:47],v[120:123]
rt v[196:197],v243 offset:50816
rt v[198:199],v243 offset:51840
mm v[120:123],v[200:203],a[36:39],v[120:123]
v_permlane16_swap_b32 v234, v236
v_permlane16_swap_b32 v235, v237
mm v[120:123],v[204:207],a[60:63],v[120:123]
rt v[192:193],v243 offset:59264
rt v[194:195],v243 offset:60288
mm v[88:91],v[204:207],a[40:43],v[88:91]
wt lgkmcnt(4)
mm v[88:91],v[200:203],a[56:59],v[88:91]
rt v[204:205],v243 offset:128
rt v[206:207],v243 offset:1152
mm v[88:91],v[208:211],v[128:131],v[88:91]
st v[234:237],v238,s[32:35],s97 offen offset:2048 sc1
mm v[88:91],v[212:215],v[144:147],v[88:91]
rt v[200:201],v243 offset:8576
rt v[202:203],v243 offset:9600
mm v[120:123],v[212:215],v[148:151],v[120:123]
cv v230,v80,v81
cv v232,v84,v85
mm v[120:123],v[208:211],v[132:135],v[120:123]
rt v[212:213],v243 offset:17024
rt v[214:215],v243 offset:18048
mm v[120:123],v[216:219],v[164:167],v[120:123]
cv v231,v82,v83
cv v233,v86,v87
wt vmcnt(15)
buffer_store_dwordx2 v[252:253], v251, s[64:67], s92 offen sc1
mm v[120:123],v[220:223],v[180:183],v[120:123]
rt v[208:209],v243 offset:25472
rt v[210:211],v243 offset:26496
mm v[88:91],v[220:223],v[176:179],v[88:91]
wt lgkmcnt(4)
mm v[88:91],v[216:219],v[160:163],v[88:91]
rt v[220:221],v243 offset:33920
rt v[222:223],v243 offset:34944
mm v[92:95],v[196:199],a[32:35],v[92:95]
v_permlane16_swap_b32 v230, v232
v_permlane16_swap_b32 v231, v233
mm v[92:95],v[192:195],a[48:51],v[92:95]
rt v[216:217],v243 offset:42368
rt v[218:219],v243 offset:43392
mm v[124:127],v[192:195],a[52:55],v[124:127]
mm v[124:127],v[196:199],a[44:47],v[124:127]
mm v[124:127],v[204:207],a[36:39],v[124:127]
st v[230:233],v238,s[32:35],s96 offen offset:2048 sc1
mm v[124:127],v[200:203],a[60:63],v[124:127]
mm v[92:95],v[200:203],a[40:43],v[92:95]
wt lgkmcnt(0)
mm v[92:95],v[204:207],a[56:59],v[92:95]
mm v[92:95],v[212:215],v[128:131],v[92:95]
mm v[92:95],v[208:211],v[144:147],v[92:95]
mm v[124:127],v[208:211],v[148:151],v[124:127]
mm v[124:127],v[212:215],v[132:135],v[124:127]
mm v[124:127],v[220:223],v[164:167],v[124:127]
mm v[124:127],v[216:219],v[180:183],v[124:127]
mm v[92:95],v[216:219],v[176:179],v[92:95]
mm v[92:95],v[220:223],v[160:163],v[92:95]
s_nop 7
cv v234,v120,v121
cv v236,v124,v125
cv v235,v122,v123
cv v237,v126,v127
cv v230,v88,v89
cv v232,v92,v93
cv v231,v90,v91
cv v233,v94,v95
v_permlane16_swap_b32 v234, v236
v_permlane16_swap_b32 v235, v237
st v[234:237],v238,s[32:35],s97 offen offset:3072 sc1
v_permlane16_swap_b32 v230, v232
v_permlane16_swap_b32 v231, v233
st v[230:233],v238,s[32:35],s96 offen offset:3072 sc1
s_branch .La5_LT_0
.La5_LO_SC:
s_nop 0
.La5_LW7_0:
s_cbranch_execz .La5_LW7_4
mm v[64:67],v[192:195],v[160:163],0
buffer_load_dwordx2 v[250:251], v248, s[64:67], s5 offen sc1
mm v[64:67],v[196:199],v[164:167],v[64:67]
rt v[220:221],v243 offset:25344
rt v[222:223],v243 offset:26368
mm v[96:99],v[196:199],v[180:183],0
s_add_u32 m0, s83, 0
s_bfe_u32 s80, s4, 262148
mm v[96:99],v[192:195],v[176:179],v[96:99]
rt v[196:197],v240 offset:17024
rt v[198:199],v240 offset:18048
s_xor_b32 s80, s80, s72
s_lshl_b32 s80, s80, 18
s_add_u32 s80, s80, 131072
mm v[100:103],v[200:203],v[176:179],0
ld v224,s[56:59],s80 offen lds
mm v[100:103],v[204:207],v[180:183],v[100:103]
rt v[192:193],v240 offset:25472
rt v[194:195],v240 offset:26496
mm v[68:71],v[204:207],v[164:167],0
s_add_u32 m0, s83, 1024
wt lgkmcnt(4)
mm v[68:71],v[200:203],v[160:163],v[68:71]
rt v[204:205],v241 offset:17024
rt v[206:207],v241 offset:18048
mm v[72:75],v[208:211],v[160:163],0
ld v225,s[56:59],s80 offen lds
mm v[72:75],v[212:215],v[164:167],v[72:75]
rt v[200:201],v241 offset:25472
rt v[202:203],v241 offset:26496
mm v[104:107],v[212:215],v[180:183],0
s_add_u32 m0, s83, 2176
s_bfe_u32 s53, s4, 262148
mm v[104:107],v[208:211],v[176:179],v[104:107]
rt v[212:213],v242 offset:17024
rt v[214:215],v242 offset:18048
mm v[108:111],v[216:219],v[176:179],0
ld v226,s[56:59],s80 offen lds
mm v[108:111],v[220:223],v[180:183],v[108:111]
rt v[208:209],v242 offset:25472
rt v[210:211],v242 offset:26496
mm v[76:79],v[220:223],v[164:167],0
s_add_u32 m0, s83, 3200
wt lgkmcnt(4)
mm v[76:79],v[216:219],v[160:163],v[76:79]
rt v[220:221],v243 offset:17024
rt v[222:223],v243 offset:18048
mm v[80:83],v[196:199],v[160:163],0
ld v227,s[56:59],s80 offen lds
mm v[80:83],v[192:195],v[164:167],v[80:83]
rt v[216:217],v243 offset:25472
rt v[218:219],v243 offset:26496
mm v[112:115],v[192:195],v[180:183],0
s_add_u32 m0, s83, 118272
s_bfe_u32 s80, s4, 262152
mm v[112:115],v[196:199],v[176:179],v[112:115]
rt v[192:193],v240 offset:33792
rt v[194:195],v240 offset:34816
s_xor_b32 s80, s80, s72
s_lshl_b32 s80, s80, 18
mm v[116:119],v[204:207],v[176:179],0
ld v224,s[56:59],s80 offen lds
mm v[116:119],v[200:203],v[180:183],v[116:119]
rt v[196:197],v240 offset:42240
rt v[198:199],v240 offset:43264
mm v[84:87],v[200:203],v[164:167],0
s_add_u32 m0, s83, 119296
wt lgkmcnt(4)
mm v[84:87],v[204:207],v[160:163],v[84:87]
rt v[200:201],v241 offset:33792
rt v[202:203],v241 offset:34816
mm v[88:91],v[212:215],v[160:163],0
ld v225,s[56:59],s80 offen lds
mm v[88:91],v[208:211],v[164:167],v[88:91]
rt v[204:205],v241 offset:42240
rt v[206:207],v241 offset:43264
mm v[120:123],v[208:211],v[180:183],0
s_add_u32 m0, s83, 120448
s_xor_b32 s53, s53, s72
mm v[120:123],v[212:215],v[176:179],v[120:123]
rt v[208:209],v242 offset:33792
rt v[210:211],v242 offset:34816
mm v[124:127],v[220:223],v[176:179],0
ld v226,s[56:59],s80 offen lds
mm v[124:127],v[216:219],v[180:183],v[124:127]
rt v[212:213],v242 offset:42240
rt v[214:215],v242 offset:43264
mm v[92:95],v[216:219],v[164:167],0
wt vmcnt(7)
v_cmp_eq_u64 vcc, v[250:251], v[252:253]
s_nop 0
s_bitcmp1_b32 vcc_lo, 1
s_cbranch_scc0 .La5_LO_NC1
.La5_LO_KC1:
wt lgkmcnt(4)
mm v[92:95],v[220:223],v[160:163],v[92:95]
rt v[216:217],v243 offset:33792
rt v[218:219],v243 offset:34816
s_lshl_b32 s53, s53, 13
mm v[64:67],v[192:195],v[168:171],v[64:67]
ld a[0:3],v244,s[44:47],s53 offen offset:0 sc1
mm v[64:67],v[196:199],v[172:175],v[64:67]
rt v[220:221],v243 offset:42240
rt v[222:223],v243 offset:43264
mm v[96:99],v[196:199],v[188:191],v[96:99]
ld a[4:7],v245,s[44:47],s53 offen offset:0 sc1
mm v[96:99],v[192:195],v[184:187],v[96:99]
rt v[196:197],v240 offset:33920
rt v[198:199],v240 offset:34944
mm v[100:103],v[200:203],v[184:187],v[100:103]
ld a[8:11],v246,s[44:47],s53 offen offset:0 sc1
mm v[100:103],v[204:207],v[188:191],v[100:103]
rt v[192:193],v240 offset:42368
rt v[194:195],v240 offset:43392
mm v[68:71],v[204:207],v[172:175],v[68:71]
wt lgkmcnt(4)
s_bfe_u32 s54, s4, 262152
mm v[68:71],v[200:203],v[168:171],v[68:71]
rt v[204:205],v241 offset:33920
rt v[206:207],v241 offset:34944
mm v[72:75],v[208:211],v[168:171],v[72:75]
ld a[12:15],v247,s[44:47],s53 offen offset:0 sc1
mm v[72:75],v[212:215],v[172:175],v[72:75]
rt v[200:201],v241 offset:42368
rt v[202:203],v241 offset:43392
mm v[104:107],v[212:215],v[188:191],v[104:107]
ld a[16:19],v244,s[44:47],s53 offen offset:1024 sc1
mm v[104:107],v[208:211],v[184:187],v[104:107]
rt v[212:213],v242 offset:33920
rt v[214:215],v242 offset:34944
mm v[108:111],v[216:219],v[184:187],v[108:111]
ld a[20:23],v245,s[44:47],s53 offen offset:1024 sc1
mm v[108:111],v[220:223],v[188:191],v[108:111]
.La5_LW7_1:
s_cbranch_execz .La5_LW7_5
rt v[208:209],v242 offset:42368
rt v[210:211],v242 offset:43392
mm v[76:79],v[220:223],v[172:175],v[76:79]
s_nop 0
s_barrier
wt lgkmcnt(4)
mm v[76:79],v[216:219],v[168:171],v[76:79]
rt v[220:221],v243 offset:33920
rt v[222:223],v243 offset:34944
mm v[80:83],v[196:199],v[168:171],v[80:83]
ld a[24:27],v246,s[44:47],s53 offen offset:1024 sc1
mm v[80:83],v[192:195],v[172:175],v[80:83]
rt v[216:217],v243 offset:42368
rt v[218:219],v243 offset:43392
mm v[112:115],v[192:195],v[188:191],v[112:115]
ld a[28:31],v247,s[44:47],s53 offen offset:1024 sc1
mm v[112:115],v[196:199],v[184:187],v[112:115]
rt v[192:193],v240 offset:50688
rt v[194:195],v240 offset:51712
mm v[116:119],v[204:207],v[184:187],v[116:119]
ld a[32:35],v244,s[44:47],s53 offen offset:2048 sc1
mm v[116:119],v[200:203],v[188:191],v[116:119]
rt v[196:197],v240 offset:59136
rt v[198:199],v240 offset:60160
mm v[84:87],v[200:203],v[172:175],v[84:87]
wt lgkmcnt(4)
s_xor_b32 s54, s54, s72
mm v[84:87],v[204:207],v[168:171],v[84:87]
rt v[200:201],v241 offset:50688
rt v[202:203],v241 offset:51712
mm v[88:91],v[212:215],v[168:171],v[88:91]
ld a[36:39],v245,s[44:47],s53 offen offset:2048 sc1
mm v[88:91],v[208:211],v[172:175],v[88:91]
rt v[204:205],v241 offset:59136
rt v[206:207],v241 offset:60160
mm v[120:123],v[208:211],v[188:191],v[120:123]
ld a[40:43],v246,s[44:47],s53 offen offset:2048 sc1
mm v[120:123],v[212:215],v[184:187],v[120:123]
rt v[208:209],v242 offset:50688
rt v[210:211],v242 offset:51712
mm v[124:127],v[220:223],v[184:187],v[124:127]
ld a[44:47],v247,s[44:47],s53 offen offset:2048 sc1
mm v[124:127],v[216:219],v[188:191],v[124:127]
rt v[212:213],v242 offset:59136
rt v[214:215],v242 offset:60160
mm v[92:95],v[216:219],v[172:175],v[92:95]
wt vmcnt(4) lgkmcnt(4)
s_lshl_b32 s54, s54, 13
mm v[92:95],v[220:223],v[168:171],v[92:95]
rt v[216:217],v243 offset:50688
rt v[218:219],v243 offset:51712
mm v[0:3],v[192:195],a[0:3],v[0:3]
s_bfe_u32 s55, s4, 262156
s_xor_b32 s55, s55, s72
mm v[0:3],v[196:199],a[16:19],v[0:3]
ld a[48:51],v244,s[44:47],s53 offen offset:3072 sc1
mm v[32:35],v[196:199],a[20:23],v[32:35]
s_lshl_b32 s55, s55, 13
s_bfe_u32 s52, s4, 262160
mm v[32:35],v[192:195],a[4:7],v[32:35]
rt v[220:221],v243 offset:59136
rt v[222:223],v243 offset:60160
mm v[64:67],v[192:195],a[8:11],v[64:67]
s_xor_b32 s52, s52, s72
s_lshl_b32 s52, s52, 13
mm v[64:67],v[196:199],a[24:27],v[64:67]
ld a[52:55],v245,s[44:47],s53 offen offset:3072 sc1
mm v[96:99],v[196:199],a[28:31],v[96:99]
mm v[96:99],v[192:195],a[12:15],v[96:99]
rt v[196:197],v240 offset:50816
rt v[198:199],v240 offset:51840
mm v[100:103],v[200:203],a[12:15],v[100:103]
mm v[100:103],v[204:207],a[28:31],v[100:103]
buffer_load_dwordx2 v[250:251], v248, s[64:67], s5 offen sc1
mm v[68:71],v[204:207],a[24:27],v[68:71]
mm v[68:71],v[200:203],a[8:11],v[68:71]
rt v[192:193],v240 offset:59264
rt v[194:195],v240 offset:60288
mm v[36:39],v[200:203],a[4:7],v[36:39]
mm v[36:39],v[204:207],a[20:23],v[36:39]
ld a[56:59],v246,s[44:47],s53 offen offset:3072 sc1
mm v[4:7],v[204:207],a[16:19],v[4:7]
wt lgkmcnt(4)
mm v[4:7],v[200:203],a[0:3],v[4:7]
rt v[204:205],v241 offset:50816
rt v[206:207],v241 offset:51840
mm v[8:11],v[208:211],a[0:3],v[8:11]
mm v[8:11],v[212:215],a[16:19],v[8:11]
ld a[60:63],v247,s[44:47],s53 offen offset:3072 sc1
mm v[40:43],v[212:215],a[20:23],v[40:43]
s_bfe_u32 s53, s4, 262164
s_xor_b32 s53, s53, s72
mm v[40:43],v[208:211],a[4:7],v[40:43]
rt v[200:201],v241 offset:59264
rt v[202:203],v241 offset:60288
mm v[72:75],v[208:211],a[8:11],v[72:75]
s_add_u32 m0, s83, 121472
s_lshl_b32 s53, s53, 13
mm v[72:75],v[212:215],a[24:27],v[72:75]
ld v227,s[56:59],s80 offen lds
mm v[104:107],v[212:215],a[28:31],v[104:107]
s_add_u32 s80, s80, 131072
mm v[104:107],v[208:211],a[12:15],v[104:107]
rt v[212:213],v242 offset:50816
rt v[214:215],v242 offset:51840
mm v[108:111],v[216:219],a[12:15],v[108:111]
s_add_u32 m0, s83, 67584
mm v[108:111],v[220:223],a[28:31],v[108:111]
ld v224,s[56:59],s80 offen lds
mm v[76:79],v[220:223],a[24:27],v[76:79]
mm v[76:79],v[216:219],a[8:11],v[76:79]
rt v[208:209],v242 offset:59264
rt v[210:211],v242 offset:60288
mm v[44:47],v[216:219],a[4:7],v[44:47]
s_add_u32 m0, s83, 68608
mm v[44:47],v[220:223],a[20:23],v[44:47]
ld v225,s[56:59],s80 offen lds
mm v[12:15],v[220:223],a[16:19],v[12:15]
wt lgkmcnt(4)
mm v[12:15],v[216:219],a[0:3],v[12:15]
rt v[220:221],v243 offset:50816
rt v[222:223],v243 offset:51840
mm v[16:19],v[196:199],a[0:3],v[16:19]
s_add_u32 m0, s83, 69760
mm v[16:19],v[192:195],a[16:19],v[16:19]
ld v226,s[56:59],s80 offen lds
mm v[48:51],v[192:195],a[20:23],v[48:51]
mm v[48:51],v[196:199],a[4:7],v[48:51]
rt v[216:217],v243 offset:59264
rt v[218:219],v243 offset:60288
mm v[80:83],v[196:199],a[8:11],v[80:83]
s_add_u32 m0, s83, 70784
s_nop 0
s_barrier
mm v[80:83],v[192:195],a[24:27],v[80:83]
ld v227,s[56:59],s80 offen lds
mm v[112:115],v[192:195],a[28:31],v[112:115]
s_bfe_u32 s80, s4, 262156
s_xor_b32 s80, s80, s72
mm v[112:115],v[196:199],a[12:15],v[112:115]
rt v[192:193],v240 offset:0
rt v[194:195],v240 offset:1024
mm v[116:119],v[204:207],a[12:15],v[116:119]
s_add_u32 m0, s83, 84480
s_lshl_b32 s80, s80, 18
mm v[116:119],v[200:203],a[28:31],v[116:119]
ld v224,s[56:59],s80 offen lds
mm v[84:87],v[200:203],a[24:27],v[84:87]
mm v[84:87],v[204:207],a[8:11],v[84:87]
rt v[196:197],v240 offset:8448
rt v[198:199],v240 offset:9472
mm v[52:55],v[204:207],a[4:7],v[52:55]
s_add_u32 m0, s83, 85504
mm v[52:55],v[200:203],a[20:23],v[52:55]
ld v225,s[56:59],s80 offen lds
mm v[20:23],v[200:203],a[16:19],v[20:23]
wt lgkmcnt(4)
mm v[20:23],v[204:207],a[0:3],v[20:23]
rt v[200:201],v241 offset:0
rt v[202:203],v241 offset:1024
mm v[24:27],v[212:215],a[0:3],v[24:27]
s_add_u32 m0, s83, 86656
mm v[24:27],v[208:211],a[16:19],v[24:27]
ld v226,s[56:59],s80 offen lds
mm v[56:59],v[208:211],a[20:23],v[56:59]
wt vmcnt(10)
v_cmp_eq_u64 vcc, v[250:251], v[252:253]
mm v[56:59],v[212:215],a[4:7],v[56:59]
rt v[204:205],v241 offset:8448
rt v[206:207],v241 offset:9472
mm v[88:91],v[212:215],a[8:11],v[88:91]
s_add_u32 m0, s83, 87680
mm v[88:91],v[208:211],a[24:27],v[88:91]
ld v227,s[56:59],s80 offen lds
mm v[120:123],v[208:211],a[28:31],v[120:123]
s_bitcmp1_b32 vcc_lo, 2
s_cbranch_scc0 .La5_LO_NC2
.La5_LO_KC2:
mm v[120:123],v[212:215],a[12:15],v[120:123]
rt v[208:209],v242 offset:0
rt v[210:211],v242 offset:1024
mm v[124:127],v[220:223],a[12:15],v[124:127]
s_add_u32 s80, s80, 131072
mm v[124:127],v[216:219],a[28:31],v[124:127]
ld a[12:15],v244,s[44:47],s54 offen offset:0 sc1
mm v[92:95],v[216:219],a[24:27],v[92:95]
mm v[92:95],v[220:223],a[8:11],v[92:95]
rt v[212:213],v242 offset:8448
rt v[214:215],v242 offset:9472
mm v[60:63],v[220:223],a[4:7],v[60:63]
mm v[60:63],v[216:219],a[20:23],v[60:63]
mm v[28:31],v[216:219],a[16:19],v[28:31]
wt vmcnt(10) lgkmcnt(4)
mm v[28:31],v[220:223],a[0:3],v[28:31]
rt v[216:217],v243 offset:0
rt v[218:219],v243 offset:1024
mm v[0:3],v[192:195],a[32:35],v[0:3]
mm v[0:3],v[196:199],a[48:51],v[0:3]
ld a[0:3],v245,s[44:47],s54 offen offset:0 sc1
mm v[32:35],v[196:199],a[52:55],v[32:35]
mm v[32:35],v[192:195],a[36:39],v[32:35]
rt v[220:221],v243 offset:8448
rt v[222:223],v243 offset:9472
mm v[64:67],v[192:195],a[40:43],v[64:67]
mm v[64:67],v[196:199],a[56:59],v[64:67]
ld a[4:7],v246,s[44:47],s54 offen offset:0 sc1
mm v[96:99],v[196:199],a[60:63],v[96:99]
mm v[96:99],v[192:195],a[44:47],v[96:99]
rt v[196:197],v240 offset:128
rt v[198:199],v240 offset:1152
mm v[100:103],v[200:203],a[44:47],v[100:103]
s_mov_b32 s86, 0x05040100
s_mov_b32 s87, 0x07060302
mm v[100:103],v[204:207],a[60:63],v[100:103]
ld a[8:11],v247,s[44:47],s54 offen offset:0 sc1
mm v[68:71],v[204:207],a[56:59],v[68:71]
s_nop 0
ar v230,a128
mm v[68:71],v[200:203],a[40:43],v[68:71]
rt v[192:193],v240 offset:8576
rt v[194:195],v240 offset:9600
mm v[36:39],v[200:203],a[36:39],v[36:39]
v_add_u32 v240, 67584, v240
ar v231,a132
mm v[36:39],v[204:207],a[52:55],v[36:39]
ld a[16:19],v244,s[44:47],s54 offen offset:1024 sc1
mm v[4:7],v[204:207],a[48:51],v[4:7]
wt lgkmcnt(4)
v_perm_b32 v232, v231, v230, s86
mm v[4:7],v[200:203],a[32:35],v[4:7]
rt v[204:205],v241 offset:128
rt v[206:207],v241 offset:1152
mm v[8:11],v[208:211],a[32:35],v[8:11]
v_perm_b32 v233, v231, v230, s87
aw a96,v232
mm v[8:11],v[212:215],a[48:51],v[8:11]
ld a[20:23],v245,s[44:47],s54 offen offset:1024 sc1
mm v[40:43],v[212:215],a[52:55],v[40:43]
aw a100,v233
ar v234,a136
mm v[40:43],v[208:211],a[36:39],v[40:43]
rt v[200:201],v241 offset:8576
rt v[202:203],v241 offset:9600
mm v[72:75],v[208:211],a[40:43],v[72:75]
v_add_u32 v241, 67584, v241
ar v235,a140
mm v[72:75],v[212:215],a[56:59],v[72:75]
ld a[24:27],v246,s[44:47],s54 offen offset:1024 sc1
mm v[104:107],v[212:215],a[60:63],v[104:107]
v_perm_b32 v236, v235, v234, s86
v_perm_b32 v237, v235, v234, s87
mm v[104:107],v[208:211],a[44:47],v[104:107]
rt v[212:213],v242 offset:128
rt v[214:215],v242 offset:1152
mm v[108:111],v[216:219],a[44:47],v[108:111]
aw a97,v236
aw a101,v237
mm v[108:111],v[220:223],a[60:63],v[108:111]
ld a[28:31],v247,s[44:47],s54 offen offset:1024 sc1
mm v[76:79],v[220:223],a[56:59],v[76:79]
ar v230,a144
ar v231,a148
mm v[76:79],v[216:219],a[40:43],v[76:79]
rt v[208:209],v242 offset:8576
rt v[210:211],v242 offset:9600
mm v[44:47],v[216:219],a[36:39],v[44:47]
v_add_u32 v242, 67584, v242
v_perm_b32 v232, v231, v230, s86
mm v[44:47],v[220:223],a[52:55],v[44:47]
v_perm_b32 v233, v231, v230, s87
aw a98,v232
mm v[12:15],v[220:223],a[48:51],v[12:15]
wt lgkmcnt(4)
aw a102,v233
mm v[12:15],v[216:219],a[32:35],v[12:15]
rt v[220:221],v243 offset:128
rt v[222:223],v243 offset:1152
mm v[16:19],v[196:199],a[32:35],v[16:19]
s_add_u32 m0, s83, 101376
ar v234,a152
mm v[16:19],v[192:195],a[48:51],v[16:19]
ld v224,s[56:59],s80 offen lds
mm v[48:51],v[192:195],a[52:55],v[48:51]
ar v235,a156
v_perm_b32 v236, v235, v234, s86
mm v[48:51],v[196:199],a[36:39],v[48:51]
rt v[216:217],v243 offset:8576
rt v[218:219],v243 offset:9600
mm v[80:83],v[196:199],a[40:43],v[80:83]
s_add_u32 m0, s83, 102400
v_add_u32 v243, 67584, v243
mm v[80:83],v[192:195],a[56:59],v[80:83]
ld v225,s[56:59],s80 offen lds
mm v[112:115],v[192:195],a[60:63],v[112:115]
wt vmcnt(14)
s_barrier
mm v[112:115],v[196:199],a[44:47],v[112:115]
rt v[192:193],v240 offset:50688
rt v[194:195],v240 offset:51712
mm v[116:119],v[204:207],a[44:47],v[116:119]
s_add_u32 m0, s83, 103552
v_perm_b32 v237, v235, v234, s87
mm v[116:119],v[200:203],a[60:63],v[116:119]
ld v226,s[56:59],s80 offen lds
mm v[84:87],v[200:203],a[56:59],v[84:87]
aw a99,v236
aw a103,v237
mm v[84:87],v[204:207],a[40:43],v[84:87]
rt v[196:197],v240 offset:59136
rt v[198:199],v240 offset:60160
mm v[52:55],v[204:207],a[36:39],v[52:55]
ar v230,a129
ar v231,a133
mm v[52:55],v[200:203],a[52:55],v[52:55]
ld a[64:67],v244,s[44:47],s54 offen offset:2048 sc1
mm v[20:23],v[200:203],a[48:51],v[20:23]
wt lgkmcnt(4)
v_perm_b32 v232, v231, v230, s86
mm v[20:23],v[204:207],a[32:35],v[20:23]
rt v[200:201],v241 offset:50688
rt v[202:203],v241 offset:51712
mm v[24:27],v[212:215],a[32:35],v[24:27]
v_perm_b32 v233, v231, v230, s87
aw a104,v232
mm v[24:27],v[208:211],a[48:51],v[24:27]
ld a[68:71],v245,s[44:47],s54 offen offset:2048 sc1
mm v[56:59],v[208:211],a[52:55],v[56:59]
aw a108,v233
ar v234,a137
mm v[56:59],v[212:215],a[36:39],v[56:59]
rt v[204:205],v241 offset:59136
rt v[206:207],v241 offset:60160
mm v[88:91],v[212:215],a[40:43],v[88:91]
ar v235,a141
v_perm_b32 v236, v235, v234, s86
mm v[88:91],v[208:211],a[56:59],v[88:91]
ld a[72:75],v246,s[44:47],s54 offen offset:2048 sc1
mm v[120:123],v[208:211],a[60:63],v[120:123]
v_perm_b32 v237, v235, v234, s87
aw a105,v236
mm v[120:123],v[212:215],a[44:47],v[120:123]
rt v[208:209],v242 offset:50688
rt v[210:211],v242 offset:51712
mm v[124:127],v[220:223],a[44:47],v[124:127]
aw a109,v237
ar v230,a145
mm v[124:127],v[216:219],a[60:63],v[124:127]
buffer_load_dwordx2 v[250:251], v248, s[64:67], s5 offen sc1
mm v[92:95],v[216:219],a[56:59],v[92:95]
ar v231,a149
v_perm_b32 v232, v231, v230, s86
mm v[92:95],v[220:223],a[40:43],v[92:95]
rt v[212:213],v242 offset:59136
rt v[214:215],v242 offset:60160
mm v[60:63],v[220:223],a[36:39],v[60:63]
v_perm_b32 v233, v231, v230, s87
aw a106,v232
mm v[60:63],v[216:219],a[52:55],v[60:63]
ld a[36:39],v247,s[44:47],s54 offen offset:2048 sc1
mm v[28:31],v[216:219],a[48:51],v[28:31]
wt vmcnt(8) lgkmcnt(4)
aw a110,v233
mm v[28:31],v[220:223],a[32:35],v[28:31]
rt v[216:217],v243 offset:50688
rt v[218:219],v243 offset:51712
mm v[0:3],v[192:195],a[12:15],v[0:3]
ar v234,a153
ar v235,a157
mm v[0:3],v[196:199],a[16:19],v[0:3]
ld a[32:35],v244,s[44:47],s54 offen offset:3072 sc1
mm v[32:35],v[196:199],a[20:23],v[32:35]
v_perm_b32 v236, v235, v234, s86
v_perm_b32 v237, v235, v234, s87
mm v[32:35],v[192:195],a[0:3],v[32:35]
rt v[220:221],v243 offset:59136
rt v[222:223],v243 offset:60160
mm v[64:67],v[192:195],a[4:7],v[64:67]
aw a107,v236
aw a111,v237
mm v[64:67],v[196:199],a[24:27],v[64:67]
ld a[40:43],v245,s[44:47],s54 offen offset:3072 sc1
mm v[96:99],v[196:199],a[28:31],v[96:99]
ar v230,a130
ar v231,a134
mm v[96:99],v[192:195],a[8:11],v[96:99]
rt v[196:197],v240 offset:50816
rt v[198:199],v240 offset:51840
mm v[100:103],v[200:203],a[8:11],v[100:103]
v_perm_b32 v232, v231, v230, s86
v_perm_b32 v233, v231, v230, s87
mm v[100:103],v[204:207],a[28:31],v[100:103]
ld a[44:47],v246,s[44:47],s54 offen offset:3072 sc1
mm v[68:71],v[204:207],a[24:27],v[68:71]
aw a112,v232
aw a116,v233
mm v[68:71],v[200:203],a[4:7],v[68:71]
rt v[192:193],v240 offset:59264
rt v[194:195],v240 offset:60288
mm v[36:39],v[200:203],a[0:3],v[36:39]
ar v234,a138
ar v235,a142
mm v[36:39],v[204:207],a[20:23],v[36:39]
ld a[48:51],v247,s[44:47],s54 offen offset:3072 sc1
mm v[4:7],v[204:207],a[16:19],v[4:7]
wt lgkmcnt(4)
s_bfe_u32 s54, s4, 262168
mm v[4:7],v[200:203],a[12:15],v[4:7]
rt v[204:205],v241 offset:50816
rt v[206:207],v241 offset:51840
mm v[8:11],v[208:211],a[12:15],v[8:11]
s_add_u32 m0, s83, 104576
s_xor_b32 s54, s54, s72
mm v[8:11],v[212:215],a[16:19],v[8:11]
ld v227,s[56:59],s80 offen lds
mm v[40:43],v[212:215],a[20:23],v[40:43]
s_bitcmp1_b32 vcc_lo, 3
s_cbranch_scc0 .La5_LO_NC3
.La5_LO_KC3:
mm v[40:43],v[208:211],a[0:3],v[40:43]
rt v[200:201],v241 offset:59264
rt v[202:203],v241 offset:60288
mm v[72:75],v[208:211],a[4:7],v[72:75]
s_bfe_u32 s80, s4, 262160
s_xor_b32 s80, s80, s72
mm v[72:75],v[212:215],a[24:27],v[72:75]
ld a[52:55],v244,s[44:47],s55 offen offset:0 sc1
mm v[104:107],v[212:215],a[28:31],v[104:107]
s_lshl_b32 s80, s80, 18
s_lshl_b32 s54, s54, 13
mm v[104:107],v[208:211],a[8:11],v[104:107]
rt v[212:213],v242 offset:50816
rt v[214:215],v242 offset:51840
mm v[108:111],v[216:219],a[8:11],v[108:111]
v_perm_b32 v236, v235, v234, s86
v_perm_b32 v237, v235, v234, s87
mm v[108:111],v[220:223],a[28:31],v[108:111]
ld a[56:59],v245,s[44:47],s55 offen offset:0 sc1
mm v[76:79],v[220:223],a[24:27],v[76:79]
aw a113,v236
aw a117,v237
mm v[76:79],v[216:219],a[4:7],v[76:79]
rt v[208:209],v242 offset:59264
rt v[210:211],v242 offset:60288
mm v[44:47],v[216:219],a[0:3],v[44:47]
ar v230,a146
ar v231,a150
mm v[44:47],v[220:223],a[20:23],v[44:47]
v_perm_b32 v232, v231, v230, s86
v_perm_b32 v233, v231, v230, s87
mm v[12:15],v[220:223],a[16:19],v[12:15]
wt lgkmcnt(4)
aw a114,v232
mm v[12:15],v[216:219],a[12:15],v[12:15]
rt v[220:221],v243 offset:50816
rt v[222:223],v243 offset:51840
mm v[16:19],v[196:199],a[12:15],v[16:19]
aw a118,v233
ar v234,a154
mm v[16:19],v[192:195],a[16:19],v[16:19]
ld a[60:63],v246,s[44:47],s55 offen offset:0 sc1
mm v[48:51],v[192:195],a[20:23],v[48:51]
ar v235,a158
v_perm_b32 v236, v235, v234, s86
mm v[48:51],v[196:199],a[0:3],v[48:51]
rt v[216:217],v243 offset:59264
rt v[218:219],v243 offset:60288
mm v[80:83],v[196:199],a[4:7],v[80:83]
v_perm_b32 v237, v235, v234, s87
aw a115,v236
mm v[80:83],v[192:195],a[24:27],v[80:83]
ld a[76:79],v247,s[44:47],s55 offen offset:0 sc1
mm v[112:115],v[192:195],a[28:31],v[112:115]
aw a119,v237
ar v230,a131
mm v[112:115],v[196:199],a[8:11],v[112:115]
rt v[192:193],v240 offset:0
rt v[194:195],v240 offset:1024
mm v[116:119],v[204:207],a[8:11],v[116:119]
ar v231,a135
v_perm_b32 v232, v231, v230, s86
mm v[116:119],v[200:203],a[28:31],v[116:119]
ld a[80:83],v244,s[44:47],s55 offen offset:1024 sc1
mm v[84:87],v[200:203],a[24:27],v[84:87]
v_perm_b32 v233, v231, v230, s87
aw a120,v232
mm v[84:87],v[204:207],a[4:7],v[84:87]
rt v[196:197],v240 offset:8448
rt v[198:199],v240 offset:9472
mm v[52:55],v[204:207],a[0:3],v[52:55]
s_load_dwordx2 s[16:17], s[0:1], 0
s_load_dwordx4 s[36:39], s[0:1], 80
mm v[52:55],v[200:203],a[20:23],v[52:55]
ld a[84:87],v245,s[44:47],s55 offen offset:1024 sc1
mm v[20:23],v[200:203],a[16:19],v[20:23]
wt lgkmcnt(4)
aw a124,v233
mm v[20:23],v[204:207],a[12:15],v[20:23]
rt v[200:201],v241 offset:0
rt v[202:203],v241 offset:1024
mm v[24:27],v[212:215],a[12:15],v[24:27]
ar v234,a139
ar v235,a143
mm v[24:27],v[208:211],a[16:19],v[24:27]
ld a[88:91],v246,s[44:47],s55 offen offset:1024 sc1
mm v[56:59],v[208:211],a[20:23],v[56:59]
v_perm_b32 v236, v235, v234, s86
v_perm_b32 v237, v235, v234, s87
mm v[56:59],v[212:215],a[0:3],v[56:59]
rt v[204:205],v241 offset:8448
rt v[206:207],v241 offset:9472
mm v[88:91],v[212:215],a[4:7],v[88:91]
aw a121,v236
aw a125,v237
mm v[88:91],v[208:211],a[24:27],v[88:91]
ld a[92:95],v247,s[44:47],s55 offen offset:1024 sc1
mm v[120:123],v[208:211],a[28:31],v[120:123]
ar v230,a147
ar v231,a151
mm v[120:123],v[212:215],a[8:11],v[120:123]
rt v[208:209],v242 offset:0
rt v[210:211],v242 offset:1024
mm v[124:127],v[220:223],a[8:11],v[124:127]
s_add_u32 m0, s83, 16896
v_perm_b32 v232, v231, v230, s86
mm v[124:127],v[216:219],a[28:31],v[124:127]
ld v224,s[56:59],s80 offen lds
mm v[92:95],v[216:219],a[24:27],v[92:95]
v_perm_b32 v233, v231, v230, s87
aw a122,v232
mm v[92:95],v[220:223],a[4:7],v[92:95]
rt v[212:213],v242 offset:8448
rt v[214:215],v242 offset:9472
mm v[60:63],v[220:223],a[0:3],v[60:63]
aw a126,v233
.La5_LW7_2:
s_cbranch_execz .La5_LW7_6
ar v234,a155
mm v[60:63],v[216:219],a[20:23],v[60:63]
ar v235,a159
v_perm_b32 v236, v235, v234, s86
mm v[28:31],v[216:219],a[16:19],v[28:31]
wt vmcnt(10) lgkmcnt(4)
v_perm_b32 v237, v235, v234, s87
mm v[28:31],v[220:223],a[12:15],v[28:31]
rt v[216:217],v243 offset:0
rt v[218:219],v243 offset:1024
mm v[0:3],v[192:195],a[64:67],v[0:3]
s_add_u32 m0, s83, 17920
aw a123,v236
mm v[0:3],v[196:199],a[32:35],v[0:3]
ld v225,s[56:59],s80 offen lds
mm v[32:35],v[196:199],a[40:43],v[32:35]
aw a127,v237
ar v230,a160
mm v[32:35],v[192:195],a[68:71],v[32:35]
rt v[220:221],v243 offset:8448
rt v[222:223],v243 offset:9472
mm v[64:67],v[192:195],a[72:75],v[64:67]
s_add_u32 m0, s83, 19072
ar v231,a164
mm v[64:67],v[196:199],a[44:47],v[64:67]
ld v226,s[56:59],s80 offen lds
mm v[96:99],v[196:199],a[48:51],v[96:99]
v_perm_b32 v232, v231, v230, s86
v_perm_b32 v233, v231, v230, s87
mm v[96:99],v[192:195],a[36:39],v[96:99]
rt v[196:197],v240 offset:128
rt v[198:199],v240 offset:1152
mm v[100:103],v[200:203],a[36:39],v[100:103]
aw a128,v232
aw a132,v233
mm v[100:103],v[204:207],a[48:51],v[100:103]
ld v[136:139],v244,s[44:47],s55 offen offset:2048 sc1
mm v[68:71],v[204:207],a[44:47],v[68:71]
ar v234,a168
ar v235,a172
mm v[68:71],v[200:203],a[72:75],v[68:71]
rt v[192:193],v240 offset:8576
rt v[194:195],v240 offset:9600
mm v[36:39],v[200:203],a[68:71],v[36:39]
v_perm_b32 v236, v235, v234, s86
v_perm_b32 v237, v235, v234, s87
mm v[36:39],v[204:207],a[40:43],v[36:39]
ld v[140:143],v245,s[44:47],s55 offen offset:2048 sc1
mm v[4:7],v[204:207],a[32:35],v[4:7]
wt lgkmcnt(4)
aw a129,v236
mm v[4:7],v[200:203],a[64:67],v[4:7]
rt v[204:205],v241 offset:128
rt v[206:207],v241 offset:1152
mm v[8:11],v[208:211],a[64:67],v[8:11]
aw a133,v237
ar v230,a176
mm v[8:11],v[212:215],a[32:35],v[8:11]
ld v[152:155],v246,s[44:47],s55 offen offset:2048 sc1
mm v[40:43],v[212:215],a[40:43],v[40:43]
ar v231,a180
v_perm_b32 v232, v231, v230, s86
mm v[40:43],v[208:211],a[68:71],v[40:43]
rt v[200:201],v241 offset:8576
rt v[202:203],v241 offset:9600
mm v[72:75],v[208:211],a[72:75],v[72:75]
v_perm_b32 v233, v231, v230, s87
aw a130,v232
mm v[72:75],v[212:215],a[44:47],v[72:75]
ld v[156:159],v247,s[44:47],s55 offen offset:2048 sc1
mm v[104:107],v[212:215],a[48:51],v[104:107]
aw a134,v233
ar v234,a184
mm v[104:107],v[208:211],a[36:39],v[104:107]
rt v[212:213],v242 offset:128
rt v[214:215],v242 offset:1152
mm v[108:111],v[216:219],a[36:39],v[108:111]
ar v235,a188
v_perm_b32 v236, v235, v234, s86
mm v[108:111],v[220:223],a[48:51],v[108:111]
ld v[168:171],v244,s[44:47],s55 offen offset:3072 sc1
mm v[76:79],v[220:223],a[44:47],v[76:79]
v_perm_b32 v237, v235, v234, s87
aw a131,v236
mm v[76:79],v[216:219],a[72:75],v[76:79]
rt v[208:209],v242 offset:8576
rt v[210:211],v242 offset:9600
mm v[44:47],v[216:219],a[68:71],v[44:47]
aw a135,v237
ar v230,a161
mm v[44:47],v[220:223],a[40:43],v[44:47]
s_nop 0
v_cmp_eq_u64 vcc, v[250:251], v[252:253]
mm v[12:15],v[220:223],a[32:35],v[12:15]
wt lgkmcnt(4)
ar v231,a165
mm v[12:15],v[216:219],a[64:67],v[12:15]
rt v[220:221],v243 offset:128
rt v[222:223],v243 offset:1152
mm v[16:19],v[196:199],a[64:67],v[16:19]
v_perm_b32 v232, v231, v230, s86
v_perm_b32 v233, v231, v230, s87
mm v[16:19],v[192:195],a[32:35],v[16:19]
ld v[172:175],v245,s[44:47],s55 offen offset:3072 sc1
mm v[48:51],v[192:195],a[40:43],v[48:51]
aw a136,v232
aw a140,v233
mm v[48:51],v[196:199],a[68:71],v[48:51]
rt v[216:217],v243 offset:8576
rt v[218:219],v243 offset:9600
mm v[80:83],v[196:199],a[72:75],v[80:83]
ar v234,a169
ar v235,a173
mm v[80:83],v[192:195],a[44:47],v[80:83]
ld v[184:187],v246,s[44:47],s55 offen offset:3072 sc1
mm v[112:115],v[192:195],a[48:51],v[112:115]
wt vmcnt(18) lgkmcnt(0)
s_barrier
mm v[112:115],v[196:199],a[36:39],v[112:115]
rt v[192:193],v240 offset:16896
rt v[194:195],v240 offset:17920
mm v[116:119],v[204:207],a[36:39],v[116:119]
s_xor_b32 s90, s72, 0
s_lshl_b32 s92, s76, 5
mm v[116:119],v[200:203],a[48:51],v[116:119]
buffer_load_dwordx2 v[250:251], v248, s[64:67], s5 offen sc1
mm v[84:87],v[200:203],a[44:47],v[84:87]
s_add_u32 s92, s92, s90
s_lshl2_add_u32 s92, s92, s3
mm v[84:87],v[204:207],a[72:75],v[84:87]
rt v[196:197],v240 offset:25344
rt v[198:199],v240 offset:26368
mm v[52:55],v[204:207],a[68:71],v[52:55]
s_lshl_b32 s92, s92, 12
v_perm_b32 v236, v235, v234, s86
mm v[52:55],v[200:203],a[40:43],v[52:55]
ld v[188:191],v247,s[44:47],s55 offen offset:3072 sc1
mm v[20:23],v[200:203],a[32:35],v[20:23]
s_nop 0
s_bfe_u32 s55, s4, 262172
mm v[20:23],v[204:207],a[64:67],v[20:23]
rt v[200:201],v241 offset:16896
rt v[202:203],v241 offset:17920
mm v[24:27],v[212:215],a[64:67],v[24:27]
s_add_u32 m0, s83, 20096
s_xor_b32 s55, s55, s72
mm v[24:27],v[208:211],a[32:35],v[24:27]
ld v227,s[56:59],s80 offen lds
mm v[56:59],v[208:211],a[40:43],v[56:59]
s_add_u32 s80, s80, 131072
s_lshl_b32 s55, s55, 13
mm v[56:59],v[212:215],a[68:71],v[56:59]
rt v[204:205],v241 offset:25344
rt v[206:207],v241 offset:26368
mm v[88:91],v[212:215],a[72:75],v[88:91]
s_add_u32 m0, s83, 33792
v_perm_b32 v237, v235, v234, s87
mm v[88:91],v[208:211],a[44:47],v[88:91]
ld v224,s[56:59],s80 offen lds
mm v[120:123],v[208:211],a[48:51],v[120:123]
s_add_u32 s68, s16, s92
s_addc_u32 s69, s17, 0
mm v[120:123],v[212:215],a[36:39],v[120:123]
rt v[208:209],v242 offset:16896
rt v[210:211],v242 offset:17920
mm v[124:127],v[220:223],a[36:39],v[124:127]
s_add_u32 m0, s83, 34816
s_and_b32 s69, s69, 0xffff
mm v[124:127],v[216:219],a[48:51],v[124:127]
ld v225,s[56:59],s80 offen lds
mm v[92:95],v[216:219],a[44:47],v[92:95]
s_mov_b32 s70, 4096
s_mov_b32 s71, 0x20000
mm v[92:95],v[220:223],a[72:75],v[92:95]
rt v[212:213],v242 offset:25344
rt v[214:215],v242 offset:26368
mm v[60:63],v[220:223],a[68:71],v[60:63]
s_add_u32 m0, s83, 35968
aw a137,v236
mm v[60:63],v[216:219],a[40:43],v[60:63]
ld v226,s[56:59],s80 offen lds
mm v[28:31],v[216:219],a[32:35],v[28:31]
wt vmcnt(16) lgkmcnt(4)
aw a141,v237
mm v[28:31],v[220:223],a[64:67],v[28:31]
rt v[216:217],v243 offset:16896
rt v[218:219],v243 offset:17920
mm v[0:3],v[192:195],a[52:55],v[0:3]
s_add_u32 m0, s83, 36992
ar v230,a177
mm v[0:3],v[196:199],a[80:83],v[0:3]
ld v227,s[56:59],s80 offen lds
mm v[32:35],v[196:199],a[84:87],v[32:35]
s_bitcmp1_b32 vcc_lo, 4
s_cbranch_scc0 .La5_LO_NC4
.La5_LO_KC4:
mm v[32:35],v[192:195],a[56:59],v[32:35]
rt v[220:221],v243 offset:25344
rt v[222:223],v243 offset:26368
mm v[64:67],v[192:195],a[60:63],v[64:67]
s_bfe_u32 s80, s4, 262164
s_xor_b32 s80, s80, s72
mm v[64:67],v[196:199],a[88:91],v[64:67]
ld a[0:3],v244,s[44:47],s52 offen offset:0 sc1
mm v[96:99],v[196:199],a[92:95],v[96:99]
s_lshl_b32 s80, s80, 18
ar v231,a181
mm v[96:99],v[192:195],a[76:79],v[96:99]
rt v[196:197],v240 offset:17024
rt v[198:199],v240 offset:18048
mm v[100:103],v[200:203],a[76:79],v[100:103]
s_add_u32 s48, s36, 0
s_addc_u32 s49, s37, 0
mm v[100:103],v[204:207],a[92:95],v[100:103]
ld a[4:7],v245,s[44:47],s52 offen offset:0 sc1
mm v[68:71],v[204:207],a[88:91],v[68:71]
s_and_b32 s49, s49, 0xffff
s_mov_b32 s50, 2048
mm v[68:71],v[200:203],a[60:63],v[68:71]
rt v[192:193],v240 offset:25472
rt v[194:195],v240 offset:26496
mm v[36:39],v[200:203],a[56:59],v[36:39]
s_mov_b32 s51, 0x20000
v_and_b32 v238, 0xf0, v255
mm v[36:39],v[204:207],a[84:87],v[36:39]
ld a[8:11],v246,s[44:47],s52 offen offset:0 sc1
mm v[4:7],v[204:207],a[80:83],v[4:7]
wt lgkmcnt(4)
v_lshlrev_b32 v238, 2, v238
mm v[4:7],v[200:203],a[52:55],v[4:7]
rt v[204:205],v241 offset:17024
rt v[206:207],v241 offset:18048
mm v[8:11],v[208:211],a[52:55],v[8:11]
v_bfe_u32 v249, v255, 8, 1
v_lshl_add_u32 v238, v249, 5, v238
mm v[8:11],v[212:215],a[80:83],v[8:11]
ld a[12:15],v247,s[44:47],s52 offen offset:0 sc1
mm v[40:43],v[212:215],a[84:87],v[40:43]
v_lshrrev_b32_e64 v249, 9, v255
v_lshl_add_u32 v238, v249, 4, v238
mm v[40:43],v[208:211],a[56:59],v[40:43]
rt v[200:201],v241 offset:25472
rt v[202:203],v241 offset:26496
mm v[72:75],v[208:211],a[60:63],v[72:75]
s_lshr_b32 s23, s72, 3
s_lshl3_add_u32 s92, s23, s76
mm v[72:75],v[212:215],a[88:91],v[72:75]
ld a[16:19],v244,s[44:47],s52 offen offset:1024 sc1
mm v[104:107],v[212:215],a[92:95],v[104:107]
s_lshl_b32 s92, s92, 19
s_lshl_b32 s97, s3, 16
mm v[104:107],v[208:211],a[76:79],v[104:107]
rt v[212:213],v242 offset:17024
rt v[214:215],v242 offset:18048
mm v[108:111],v[216:219],a[76:79],v[108:111]
s_add_u32 s94, s92, s97
s_add_u32 s95, s94, 32768
mm v[108:111],v[220:223],a[92:95],v[108:111]
ld a[20:23],v245,s[44:47],s52 offen offset:1024 sc1
mm v[76:79],v[220:223],a[88:91],v[76:79]
s_sub_u32 s96, s92, s97
s_add_u32 s96, s96, 458752
mm v[76:79],v[216:219],a[60:63],v[76:79]
rt v[208:209],v242 offset:25472
rt v[210:211],v242 offset:26496
mm v[44:47],v[216:219],a[56:59],v[44:47]
s_add_u32 s97, s96, 32768
s_lshl3_add_u32 s92, s23, s101
mm v[44:47],v[220:223],a[84:87],v[44:47]
s_lshl_b32 s85, s76, 14
s_lshl3_add_u32 s92, s92, s85
mm v[12:15],v[220:223],a[80:83],v[12:15]
wt lgkmcnt(4)
s_add_u32 s92, s92, 2097152
mm v[12:15],v[216:219],a[52:55],v[12:15]
rt v[220:221],v243 offset:17024
rt v[222:223],v243 offset:18048
mm v[16:19],v[196:199],a[52:55],v[16:19]
v_perm_b32 v232, v231, v230, s86
v_perm_b32 v233, v231, v230, s87
mm v[16:19],v[192:195],a[80:83],v[16:19]
ld a[24:27],v246,s[44:47],s52 offen offset:1024 sc1
mm v[48:51],v[192:195],a[84:87],v[48:51]
aw a138,v232
aw a142,v233
mm v[48:51],v[196:199],a[56:59],v[48:51]
rt v[216:217],v243 offset:25472
rt v[218:219],v243 offset:26496
mm v[80:83],v[196:199],a[60:63],v[80:83]
ar v234,a185
ar v235,a189
mm v[80:83],v[192:195],a[88:91],v[80:83]
ld a[28:31],v247,s[44:47],s52 offen offset:1024 sc1
mm v[112:115],v[192:195],a[92:95],v[112:115]
v_perm_b32 v236, v235, v234, s86
v_perm_b32 v237, v235, v234, s87
mm v[112:115],v[196:199],a[76:79],v[112:115]
rt v[192:193],v240 offset:33792
rt v[194:195],v240 offset:34816
mm v[116:119],v[204:207],a[76:79],v[116:119]
s_add_u32 m0, s83, 50688
aw a139,v236
mm v[116:119],v[200:203],a[92:95],v[116:119]
ld v224,s[56:59],s80 offen lds
mm v[84:87],v[200:203],a[88:91],v[84:87]
aw a143,v237
ar v230,a162
mm v[84:87],v[204:207],a[60:63],v[84:87]
rt v[196:197],v240 offset:42240
rt v[198:199],v240 offset:43264
mm v[52:55],v[204:207],a[56:59],v[52:55]
s_add_u32 m0, s83, 51712
ar v231,a166
mm v[52:55],v[200:203],a[84:87],v[52:55]
ld v225,s[56:59],s80 offen lds
mm v[20:23],v[200:203],a[80:83],v[20:23]
wt lgkmcnt(4)
v_perm_b32 v232, v231, v230, s86
mm v[20:23],v[204:207],a[52:55],v[20:23]
rt v[200:201],v241 offset:33792
rt v[202:203],v241 offset:34816
mm v[24:27],v[212:215],a[52:55],v[24:27]
s_add_u32 m0, s83, 52864
v_perm_b32 v233, v231, v230, s87
mm v[24:27],v[208:211],a[80:83],v[24:27]
ld v226,s[56:59],s80 offen lds
mm v[56:59],v[208:211],a[84:87],v[56:59]
aw a144,v232
aw a148,v233
mm v[56:59],v[212:215],a[56:59],v[56:59]
rt v[204:205],v241 offset:42240
rt v[206:207],v241 offset:43264
mm v[88:91],v[212:215],a[60:63],v[88:91]
ar v234,a170
ar v235,a174
mm v[88:91],v[208:211],a[88:91],v[88:91]
ld a[32:35],v244,s[44:47],s52 offen offset:2048 sc1
mm v[120:123],v[208:211],a[92:95],v[120:123]
v_perm_b32 v236, v235, v234, s86
v_perm_b32 v237, v235, v234, s87
mm v[120:123],v[212:215],a[76:79],v[120:123]
rt v[208:209],v242 offset:33792
rt v[210:211],v242 offset:34816
mm v[124:127],v[220:223],a[76:79],v[124:127]
aw a145,v236
aw a149,v237
mm v[124:127],v[216:219],a[92:95],v[124:127]
ld a[36:39],v245,s[44:47],s52 offen offset:2048 sc1
mm v[92:95],v[216:219],a[88:91],v[92:95]
ar v230,a178
ar v231,a182
mm v[92:95],v[220:223],a[60:63],v[92:95]
rt v[212:213],v242 offset:42240
rt v[214:215],v242 offset:43264
mm v[60:63],v[220:223],a[56:59],v[60:63]
v_perm_b32 v232, v231, v230, s86
v_perm_b32 v233, v231, v230, s87
mm v[60:63],v[216:219],a[84:87],v[60:63]
aw a146,v232
aw a150,v233
mm v[28:31],v[216:219],a[80:83],v[28:31]
wt vmcnt(18) lgkmcnt(4)
ar v234,a186
mm v[28:31],v[220:223],a[52:55],v[28:31]
rt v[216:217],v243 offset:33792
rt v[218:219],v243 offset:34816
mm v[0:3],v[192:195],v[136:139],v[0:3]
ar v235,a190
v_perm_b32 v236, v235, v234, s86
mm v[0:3],v[196:199],v[168:171],v[0:3]
ld a[40:43],v246,s[44:47],s52 offen offset:2048 sc1
mm v[32:35],v[196:199],v[172:175],v[32:35]
v_perm_b32 v237, v235, v234, s87
aw a147,v236
mm v[32:35],v[192:195],v[140:143],v[32:35]
rt v[220:221],v243 offset:42240
rt v[222:223],v243 offset:43264
mm v[64:67],v[192:195],v[152:155],v[64:67]
aw a151,v237
ar v230,a163
mm v[64:67],v[196:199],v[184:187],v[64:67]
ld a[44:47],v247,s[44:47],s52 offen offset:2048 sc1
mm v[96:99],v[196:199],v[188:191],v[96:99]
ar v231,a167
v_perm_b32 v232, v231, v230, s86
mm v[96:99],v[192:195],v[156:159],v[96:99]
rt v[196:197],v240 offset:33920
rt v[198:199],v240 offset:34944
mm v[100:103],v[200:203],v[156:159],v[100:103]
v_perm_b32 v233, v231, v230, s87
aw a152,v232
mm v[100:103],v[204:207],v[188:191],v[100:103]
ld a[48:51],v244,s[44:47],s52 offen offset:3072 sc1
mm v[68:71],v[204:207],v[184:187],v[68:71]
aw a156,v233
ar v234,a171
mm v[68:71],v[200:203],v[152:155],v[68:71]
rt v[192:193],v240 offset:42368
rt v[194:195],v240 offset:43392
mm v[36:39],v[200:203],v[140:143],v[36:39]
v_add_u32 v240, 4294899712, v240
ar v235,a175
mm v[36:39],v[204:207],v[172:175],v[36:39]
ld a[52:55],v245,s[44:47],s52 offen offset:3072 sc1
mm v[4:7],v[204:207],v[168:171],v[4:7]
wt lgkmcnt(4)
v_perm_b32 v236, v235, v234, s86
mm v[4:7],v[200:203],v[136:139],v[4:7]
rt v[204:205],v241 offset:33920
rt v[206:207],v241 offset:34944
mm v[8:11],v[208:211],v[136:139],v[8:11]
s_nop 0
v_cmp_eq_u64 vcc, v[250:251], v[252:253]
mm v[8:11],v[212:215],v[168:171],v[8:11]
ld a[56:59],v246,s[44:47],s52 offen offset:3072 sc1
mm v[40:43],v[212:215],v[172:175],v[40:43]
v_perm_b32 v237, v235, v234, s87
aw a153,v236
mm v[40:43],v[208:211],v[140:143],v[40:43]
rt v[200:201],v241 offset:42368
rt v[202:203],v241 offset:43392
mm v[72:75],v[208:211],v[152:155],v[72:75]
v_add_u32 v241, 4294899712, v241
aw a157,v237
mm v[72:75],v[212:215],v[184:187],v[72:75]
ld a[60:63],v247,s[44:47],s52 offen offset:3072 sc1
mm v[104:107],v[212:215],v[188:191],v[104:107]
ar v230,a179
ar v231,a183
mm v[104:107],v[208:211],v[156:159],v[104:107]
rt v[212:213],v242 offset:33920
rt v[214:215],v242 offset:34944
mm v[108:111],v[216:219],v[156:159],v[108:111]
v_perm_b32 v232, v231, v230, s86
v_perm_b32 v233, v231, v230, s87
mm v[108:111],v[220:223],v[188:191],v[108:111]
buffer_load_dwordx2 v[250:251], v248, s[64:67], s5 offen sc1
mm v[76:79],v[220:223],v[184:187],v[76:79]
aw a154,v232
aw a158,v233
mm v[76:79],v[216:219],v[152:155],v[76:79]
rt v[208:209],v242 offset:42368
rt v[210:211],v242 offset:43392
mm v[44:47],v[216:219],v[140:143],v[44:47]
s_add_u32 m0, s83, 53888
v_add_u32 v242, 4294899712, v242
mm v[44:47],v[220:223],v[172:175],v[44:47]
ld v227,s[56:59],s80 offen lds
mm v[12:15],v[220:223],v[168:171],v[12:15]
wt lgkmcnt(4)
s_bfe_u32 s80, s4, 262168
mm v[12:15],v[216:219],v[136:139],v[12:15]
rt v[220:221],v243 offset:33920
rt v[222:223],v243 offset:34944
mm v[16:19],v[196:199],v[136:139],v[16:19]
s_add_u32 m0, s83, 118272
s_xor_b32 s80, s80, s72
s_lshl_b32 s80, s80, 18
mm v[16:19],v[192:195],v[168:171],v[16:19]
ld v224,s[56:59],s80 offen lds
mm v[48:51],v[192:195],v[172:175],v[48:51]
ar v234,a187
ar v235,a191
mm v[48:51],v[196:199],v[140:143],v[48:51]
rt v[216:217],v243 offset:42368
rt v[218:219],v243 offset:43392
mm v[80:83],v[196:199],v[152:155],v[80:83]
s_add_u32 m0, s83, 119296
v_add_u32 v243, 4294899712, v243
mm v[80:83],v[192:195],v[184:187],v[80:83]
ld v225,s[56:59],s80 offen lds
mm v[112:115],v[192:195],v[188:191],v[112:115]
wt vmcnt(23) lgkmcnt(0)
s_barrier
mm v[112:115],v[196:199],v[156:159],v[112:115]
rt v[192:193],v240 offset:16896
rt v[194:195],v240 offset:17920
mm v[116:119],v[204:207],v[156:159],v[116:119]
s_add_u32 m0, s83, 120448
v_perm_b32 v236, v235, v234, s86
mm v[116:119],v[200:203],v[188:191],v[116:119]
ld v226,s[56:59],s80 offen lds
mm v[84:87],v[200:203],v[184:187],v[84:87]
s_bitcmp1_b32 vcc_lo, 5
s_cbranch_scc0 .La5_LO_NC5
.La5_LO_KC5:
mm v[84:87],v[204:207],v[152:155],v[84:87]
rt v[196:197],v240 offset:25344
rt v[198:199],v240 offset:26368
mm v[52:55],v[204:207],v[140:143],v[52:55]
v_perm_b32 v237, v235, v234, s87
aw a155,v236
mm v[52:55],v[200:203],v[172:175],v[52:55]
ld v[128:131],v244,s[44:47],s53 offen offset:0 sc1
mm v[20:23],v[200:203],v[168:171],v[20:23]
s_nop 0
aw a159,v237
mm v[20:23],v[204:207],v[136:139],v[20:23]
rt v[200:201],v241 offset:16896
rt v[202:203],v241 offset:17920
mm v[24:27],v[212:215],v[136:139],v[24:27]
ar v230,a192
ar v231,a196
mm v[24:27],v[208:211],v[168:171],v[24:27]
ld v[132:135],v245,s[44:47],s53 offen offset:0 sc1
mm v[56:59],v[208:211],v[172:175],v[56:59]
v_perm_b32 v232, v231, v230, s86
v_perm_b32 v233, v231, v230, s87
mm v[56:59],v[212:215],v[140:143],v[56:59]
rt v[204:205],v241 offset:25344
rt v[206:207],v241 offset:26368
mm v[88:91],v[212:215],v[152:155],v[88:91]
aw a160,v232
aw a164,v233
mm v[88:91],v[208:211],v[184:187],v[88:91]
ld v[144:147],v246,s[44:47],s53 offen offset:0 sc1
mm v[120:123],v[208:211],v[188:191],v[120:123]
ar v234,a200
ar v235,a204
mm v[120:123],v[212:215],v[156:159],v[120:123]
rt v[208:209],v242 offset:16896
rt v[210:211],v242 offset:17920
mm v[124:127],v[220:223],v[156:159],v[124:127]
v_perm_b32 v236, v235, v234, s86
v_perm_b32 v237, v235, v234, s87
mm v[124:127],v[216:219],v[188:191],v[124:127]
ld v[156:159],v247,s[44:47],s53 offen offset:0 sc1
mm v[92:95],v[216:219],v[184:187],v[92:95]
aw a161,v236
aw a165,v237
mm v[92:95],v[220:223],v[152:155],v[92:95]
rt v[212:213],v242 offset:25344
rt v[214:215],v242 offset:26368
mm v[60:63],v[220:223],v[140:143],v[60:63]
ar v230,a208
ar v231,a212
mm v[60:63],v[216:219],v[172:175],v[60:63]
v_perm_b32 v232, v231, v230, s86
.La5_LW7_3:
s_cbranch_execz .La5_LW7_7
v_perm_b32 v233, v231, v230, s87
mm v[28:31],v[216:219],v[168:171],v[28:31]
wt vmcnt(20) lgkmcnt(4)
aw a162,v232
mm v[28:31],v[220:223],v[136:139],v[28:31]
rt v[216:217],v243 offset:16896
rt v[218:219],v243 offset:17920
mm v[0:3],v[192:195],a[0:3],v[0:3]
aw a166,v233
ar v234,a216
mm v[0:3],v[196:199],a[16:19],v[0:3]
ld v[136:139],v244,s[44:47],s53 offen offset:1024 sc1
mm v[32:35],v[196:199],a[20:23],v[32:35]
ar v235,a220
v_perm_b32 v236, v235, v234, s86
mm v[32:35],v[192:195],a[4:7],v[32:35]
rt v[220:221],v243 offset:25344
rt v[222:223],v243 offset:26368
mm v[64:67],v[192:195],a[8:11],v[64:67]
v_perm_b32 v237, v235, v234, s87
aw a163,v236
mm v[64:67],v[196:199],a[24:27],v[64:67]
ld v[140:143],v245,s[44:47],s53 offen offset:1024 sc1
mm v[96:99],v[196:199],a[28:31],v[96:99]
aw a167,v237
ar v230,a193
mm v[96:99],v[192:195],a[12:15],v[96:99]
rt v[196:197],v240 offset:17024
rt v[198:199],v240 offset:18048
mm v[100:103],v[200:203],a[12:15],v[100:103]
ar v231,a197
v_perm_b32 v232, v231, v230, s86
mm v[100:103],v[204:207],a[28:31],v[100:103]
ld v[152:155],v246,s[44:47],s53 offen offset:1024 sc1
mm v[68:71],v[204:207],a[24:27],v[68:71]
v_perm_b32 v233, v231, v230, s87
aw a168,v232
mm v[68:71],v[200:203],a[8:11],v[68:71]
rt v[192:193],v240 offset:25472
rt v[194:195],v240 offset:26496
mm v[36:39],v[200:203],a[4:7],v[36:39]
aw a172,v233
ar v234,a201
mm v[36:39],v[204:207],a[20:23],v[36:39]
ld v[168:171],v247,s[44:47],s53 offen offset:1024 sc1
mm v[4:7],v[204:207],a[16:19],v[4:7]
wt lgkmcnt(4)
ar v235,a205
mm v[4:7],v[200:203],a[0:3],v[4:7]
rt v[204:205],v241 offset:17024
rt v[206:207],v241 offset:18048
mm v[8:11],v[208:211],a[0:3],v[8:11]
s_add_u32 m0, s83, 121472
v_perm_b32 v236, v235, v234, s86
mm v[8:11],v[212:215],a[16:19],v[8:11]
ld v227,s[56:59],s80 offen lds
mm v[40:43],v[212:215],a[20:23],v[40:43]
s_add_u32 s80, s80, 131072
v_perm_b32 v237, v235, v234, s87
mm v[40:43],v[208:211],a[4:7],v[40:43]
rt v[200:201],v241 offset:25472
rt v[202:203],v241 offset:26496
mm v[72:75],v[208:211],a[8:11],v[72:75]
s_add_u32 m0, s83, 67584
aw a169,v236
mm v[72:75],v[212:215],a[24:27],v[72:75]
ld v224,s[56:59],s80 offen lds
mm v[104:107],v[212:215],a[28:31],v[104:107]
aw a173,v237
ar v230,a209
mm v[104:107],v[208:211],a[12:15],v[104:107]
rt v[212:213],v242 offset:17024
rt v[214:215],v242 offset:18048
mm v[108:111],v[216:219],a[12:15],v[108:111]
s_add_u32 m0, s83, 68608
ar v231,a213
mm v[108:111],v[220:223],a[28:31],v[108:111]
ld v225,s[56:59],s80 offen lds
mm v[76:79],v[220:223],a[24:27],v[76:79]
v_perm_b32 v232, v231, v230, s86
v_perm_b32 v233, v231, v230, s87
mm v[76:79],v[216:219],a[8:11],v[76:79]
rt v[208:209],v242 offset:25472
rt v[210:211],v242 offset:26496
mm v[44:47],v[216:219],a[4:7],v[44:47]
aw a170,v232
aw a174,v233
mm v[44:47],v[220:223],a[20:23],v[44:47]
ar v234,a217
ar v235,a221
mm v[12:15],v[220:223],a[16:19],v[12:15]
wt lgkmcnt(4)
v_perm_b32 v236, v235, v234, s86
mm v[12:15],v[216:219],a[0:3],v[12:15]
rt v[220:221],v243 offset:17024
rt v[222:223],v243 offset:18048
mm v[16:19],v[196:199],a[0:3],v[16:19]
s_add_u32 m0, s83, 69760
v_perm_b32 v237, v235, v234, s87
mm v[16:19],v[192:195],a[16:19],v[16:19]
ld v226,s[56:59],s80 offen lds
mm v[48:51],v[192:195],a[20:23],v[48:51]
aw a171,v236
aw a175,v237
mm v[48:51],v[196:199],a[4:7],v[48:51]
rt v[216:217],v243 offset:25472
rt v[218:219],v243 offset:26496
mm v[80:83],v[196:199],a[8:11],v[80:83]
s_add_u32 m0, s83, 70784
ar v230,a194
mm v[80:83],v[192:195],a[24:27],v[80:83]
ld v227,s[56:59],s80 offen lds
mm v[112:115],v[192:195],a[28:31],v[112:115]
s_bfe_u32 s80, s4, 262172
s_xor_b32 s80, s80, s72
mm v[112:115],v[196:199],a[12:15],v[112:115]
rt v[192:193],v240 offset:33792
rt v[194:195],v240 offset:34816
mm v[116:119],v[204:207],a[12:15],v[116:119]
s_add_u32 m0, s83, 84480
s_lshl_b32 s80, s80, 18
mm v[116:119],v[200:203],a[28:31],v[116:119]
ld v224,s[56:59],s80 offen lds
mm v[84:87],v[200:203],a[24:27],v[84:87]
ar v231,a198
v_perm_b32 v232, v231, v230, s86
mm v[84:87],v[204:207],a[8:11],v[84:87]
rt v[196:197],v240 offset:42240
rt v[198:199],v240 offset:43264
mm v[52:55],v[204:207],a[4:7],v[52:55]
s_add_u32 m0, s83, 85504
v_perm_b32 v233, v231, v230, s87
mm v[52:55],v[200:203],a[20:23],v[52:55]
ld v225,s[56:59],s80 offen lds
mm v[20:23],v[200:203],a[16:19],v[20:23]
wt lgkmcnt(4)
aw a176,v232
mm v[20:23],v[204:207],a[0:3],v[20:23]
rt v[200:201],v241 offset:33792
rt v[202:203],v241 offset:34816
mm v[24:27],v[212:215],a[0:3],v[24:27]
s_add_u32 m0, s83, 86656
aw a180,v233
mm v[24:27],v[208:211],a[16:19],v[24:27]
ld v226,s[56:59],s80 offen lds
mm v[56:59],v[208:211],a[20:23],v[56:59]
ar v234,a202
ar v235,a206
mm v[56:59],v[212:215],a[4:7],v[56:59]
rt v[204:205],v241 offset:42240
rt v[206:207],v241 offset:43264
mm v[88:91],v[212:215],a[8:11],v[88:91]
s_add_u32 m0, s83, 87680
v_perm_b32 v236, v235, v234, s86
mm v[88:91],v[208:211],a[24:27],v[88:91]
ld v227,s[56:59],s80 offen lds
mm v[120:123],v[208:211],a[28:31],v[120:123]
s_add_u32 s80, s80, 131072
v_perm_b32 v237, v235, v234, s87
mm v[120:123],v[212:215],a[12:15],v[120:123]
rt v[208:209],v242 offset:33792
rt v[210:211],v242 offset:34816
mm v[124:127],v[220:223],a[12:15],v[124:127]
s_add_u32 m0, s83, 101376
aw a177,v236
mm v[124:127],v[216:219],a[28:31],v[124:127]
ld v224,s[56:59],s80 offen lds
mm v[92:95],v[216:219],a[24:27],v[92:95]
aw a181,v237
ar v230,a210
mm v[92:95],v[220:223],a[8:11],v[92:95]
rt v[212:213],v242 offset:42240
rt v[214:215],v242 offset:43264
mm v[60:63],v[220:223],a[4:7],v[60:63]
ar v231,a214
v_perm_b32 v232, v231, v230, s86
mm v[60:63],v[216:219],a[20:23],v[60:63]
wt vmcnt(22)
v_cmp_eq_u64 vcc, v[250:251], v[252:253]
mm v[28:31],v[216:219],a[16:19],v[28:31]
wt lgkmcnt(4)
v_perm_b32 v233, v231, v230, s87
mm v[28:31],v[220:223],a[0:3],v[28:31]
rt v[216:217],v243 offset:33792
rt v[218:219],v243 offset:34816
mm v[0:3],v[192:195],a[32:35],v[0:3]
s_add_u32 m0, s83, 102400
aw a178,v232
mm v[0:3],v[196:199],a[48:51],v[0:3]
ld v225,s[56:59],s80 offen lds
mm v[32:35],v[196:199],a[52:55],v[32:35]
aw a182,v233
ar v234,a218
mm v[32:35],v[192:195],a[36:39],v[32:35]
rt v[220:221],v243 offset:42240
rt v[222:223],v243 offset:43264
mm v[64:67],v[192:195],a[40:43],v[64:67]
s_add_u32 m0, s83, 103552
ar v235,a222
mm v[64:67],v[196:199],a[56:59],v[64:67]
ld v226,s[56:59],s80 offen lds
mm v[96:99],v[196:199],a[60:63],v[96:99]
v_perm_b32 v236, v235, v234, s86
v_perm_b32 v237, v235, v234, s87
mm v[96:99],v[192:195],a[44:47],v[96:99]
rt v[196:197],v240 offset:33920
rt v[198:199],v240 offset:34944
mm v[100:103],v[200:203],a[44:47],v[100:103]
s_add_u32 m0, s83, 104576
aw a179,v236
mm v[100:103],v[204:207],a[60:63],v[100:103]
ld v227,s[56:59],s80 offen lds
mm v[68:71],v[204:207],a[56:59],v[68:71]
s_bitcmp1_b32 vcc_lo, 6
s_cbranch_scc0 .La5_LO_NC6
.La5_LO_KC6:
mm v[68:71],v[200:203],a[40:43],v[68:71]
rt v[192:193],v240 offset:42368
rt v[194:195],v240 offset:43392
mm v[36:39],v[200:203],a[36:39],v[36:39]
v_bfe_u32 v224, v255, 6, 1
v_lshrrev_b32 v225, 7, v255
mm v[36:39],v[204:207],a[52:55],v[36:39]
ld a[64:67],v244,s[44:47],s54 offen offset:0 sc1
mm v[4:7],v[204:207],a[48:51],v[4:7]
wt lgkmcnt(4)
v_and_b32 v226, 48, v255
mm v[4:7],v[200:203],a[32:35],v[4:7]
rt v[204:205],v241 offset:33920
rt v[206:207],v241 offset:34944
mm v[8:11],v[208:211],a[32:35],v[8:11]
v_lshl_add_u32 v225, v225, 6, v226
v_lshl_add_u32 v224, v224, 11, v225
mm v[8:11],v[212:215],a[48:51],v[8:11]
ld a[68:71],v245,s[44:47],s54 offen offset:0 sc1
mm v[40:43],v[212:215],a[52:55],v[40:43]
aw a183,v237
ar v230,a195
mm v[40:43],v[208:211],a[36:39],v[40:43]
rt v[200:201],v241 offset:42368
rt v[202:203],v241 offset:43392
mm v[72:75],v[208:211],a[40:43],v[72:75]
ar v231,a199
v_perm_b32 v232, v231, v230, s86
mm v[72:75],v[212:215],a[56:59],v[72:75]
ld a[72:75],v244,s[44:47],s54 offen offset:1024 sc1
mm v[104:107],v[212:215],a[60:63],v[104:107]
v_perm_b32 v233, v231, v230, s87
aw a184,v232
mm v[104:107],v[208:211],a[44:47],v[104:107]
rt v[212:213],v242 offset:33920
rt v[214:215],v242 offset:34944
mm v[108:111],v[216:219],a[44:47],v[108:111]
aw a188,v233
ar v234,a203
mm v[108:111],v[220:223],a[60:63],v[108:111]
ld a[76:79],v245,s[44:47],s54 offen offset:1024 sc1
mm v[76:79],v[220:223],a[56:59],v[76:79]
ar v235,a207
v_perm_b32 v236, v235, v234, s86
mm v[76:79],v[216:219],a[40:43],v[76:79]
rt v[208:209],v242 offset:42368
rt v[210:211],v242 offset:43392
mm v[44:47],v[216:219],a[36:39],v[44:47]
v_perm_b32 v237, v235, v234, s87
aw a185,v236
mm v[44:47],v[220:223],a[52:55],v[44:47]
aw a189,v237
ar v230,a211
mm v[12:15],v[220:223],a[48:51],v[12:15]
wt lgkmcnt(4)
ar v231,a215
mm v[12:15],v[216:219],a[32:35],v[12:15]
rt v[220:221],v243 offset:33920
rt v[222:223],v243 offset:34944
mm v[16:19],v[196:199],a[32:35],v[16:19]
v_perm_b32 v232, v231, v230, s86
v_perm_b32 v233, v231, v230, s87
mm v[16:19],v[192:195],a[48:51],v[16:19]
ld a[80:83],v244,s[44:47],s54 offen offset:2048 sc1
mm v[48:51],v[192:195],a[52:55],v[48:51]
aw a186,v232
aw a190,v233
mm v[48:51],v[196:199],a[36:39],v[48:51]
rt v[216:217],v243 offset:42368
rt v[218:219],v243 offset:43392
mm v[80:83],v[196:199],a[40:43],v[80:83]
ar v234,a219
ar v235,a223
mm v[80:83],v[192:195],a[56:59],v[80:83]
ld a[84:87],v245,s[44:47],s54 offen offset:2048 sc1
mm v[112:115],v[192:195],a[60:63],v[112:115]
wt vmcnt(30)
s_barrier
mm v[112:115],v[196:199],a[44:47],v[112:115]
rt v[192:193],v240 offset:50688
rt v[194:195],v240 offset:51712
mm v[116:119],v[204:207],a[44:47],v[116:119]
v_perm_b32 v236, v235, v234, s86
v_perm_b32 v237, v235, v234, s87
mm v[116:119],v[200:203],a[60:63],v[116:119]
ld a[88:91],v244,s[44:47],s54 offen offset:3072 sc1
mm v[84:87],v[200:203],a[56:59],v[84:87]
aw a187,v236
aw a191,v237
mm v[84:87],v[204:207],a[40:43],v[84:87]
rt v[196:197],v240 offset:59136
rt v[198:199],v240 offset:60160
mm v[52:55],v[204:207],a[36:39],v[52:55]
s_bitcmp1_b32 vcc_lo, 7
s_cbranch_scc0 .La5_LO_NC7
.La5_LO_KC7:
mm v[52:55],v[200:203],a[52:55],v[52:55]
ld a[92:95],v245,s[44:47],s54 offen offset:3072 sc1
mm v[20:23],v[200:203],a[48:51],v[20:23]
wt lgkmcnt(4)
ar v230,a224
mm v[20:23],v[204:207],a[32:35],v[20:23]
rt v[200:201],v241 offset:50688
rt v[202:203],v241 offset:51712
mm v[24:27],v[212:215],a[32:35],v[24:27]
ar v231,a228
v_perm_b32 v232, v231, v230, s86
mm v[24:27],v[208:211],a[48:51],v[24:27]
ld a[0:3],v244,s[44:47],s55 offen offset:0 sc1
mm v[56:59],v[208:211],a[52:55],v[56:59]
v_perm_b32 v233, v231, v230, s87
aw a192,v232
mm v[56:59],v[212:215],a[36:39],v[56:59]
rt v[204:205],v241 offset:59136
rt v[206:207],v241 offset:60160
mm v[88:91],v[212:215],a[40:43],v[88:91]
aw a196,v233
ar v234,a232
mm v[88:91],v[208:211],a[56:59],v[88:91]
ld a[4:7],v245,s[44:47],s55 offen offset:0 sc1
mm v[120:123],v[208:211],a[60:63],v[120:123]
ar v235,a236
v_perm_b32 v236, v235, v234, s86
mm v[120:123],v[212:215],a[44:47],v[120:123]
rt v[208:209],v242 offset:50688
rt v[210:211],v242 offset:51712
mm v[124:127],v[220:223],a[44:47],v[124:127]
v_perm_b32 v237, v235, v234, s87
aw a193,v236
mm v[124:127],v[216:219],a[60:63],v[124:127]
ld a[8:11],v244,s[44:47],s55 offen offset:1024 sc1
mm v[92:95],v[216:219],a[56:59],v[92:95]
aw a197,v237
ar v230,a240
mm v[92:95],v[220:223],a[40:43],v[92:95]
rt v[212:213],v242 offset:59136
rt v[214:215],v242 offset:60160
mm v[60:63],v[220:223],a[36:39],v[60:63]
ar v231,a244
v_perm_b32 v232, v231, v230, s86
mm v[60:63],v[216:219],a[52:55],v[60:63]
v_perm_b32 v233, v231, v230, s87
aw a194,v232
mm v[28:31],v[216:219],a[48:51],v[28:31]
wt vmcnt(24) lgkmcnt(4)
aw a198,v233
mm v[28:31],v[220:223],a[32:35],v[28:31]
rt v[216:217],v243 offset:50688
rt v[218:219],v243 offset:51712
mm v[0:3],v[192:195],v[128:131],v[0:3]
ar v234,a248
ar v235,a252
mm v[0:3],v[196:199],v[136:139],v[0:3]
ld a[12:15],v245,s[44:47],s55 offen offset:1024 sc1
mm v[32:35],v[196:199],v[140:143],v[32:35]
v_perm_b32 v236, v235, v234, s86
v_perm_b32 v237, v235, v234, s87
mm v[32:35],v[192:195],v[132:135],v[32:35]
rt v[220:221],v243 offset:59136
rt v[222:223],v243 offset:60160
mm v[64:67],v[192:195],v[144:147],v[64:67]
aw a195,v236
aw a199,v237
mm v[64:67],v[196:199],v[152:155],v[64:67]
ld a[16:19],v244,s[44:47],s55 offen offset:2048 sc1
mm v[96:99],v[196:199],v[168:171],v[96:99]
ar v230,a225
ar v231,a229
mm v[96:99],v[192:195],v[156:159],v[96:99]
rt v[196:197],v240 offset:50816
rt v[198:199],v240 offset:51840
mm v[100:103],v[200:203],v[156:159],v[100:103]
v_perm_b32 v232, v231, v230, s86
v_perm_b32 v233, v231, v230, s87
mm v[100:103],v[204:207],v[168:171],v[100:103]
ld a[20:23],v245,s[44:47],s55 offen offset:2048 sc1
mm v[68:71],v[204:207],v[152:155],v[68:71]
aw a200,v232
aw a204,v233
mm v[68:71],v[200:203],v[144:147],v[68:71]
rt v[192:193],v240 offset:59264
rt v[194:195],v240 offset:60288
mm v[36:39],v[200:203],v[132:135],v[36:39]
v_add_u32 v240, 67584, v240
ar v234,a233
mm v[36:39],v[204:207],v[140:143],v[36:39]
ld a[24:27],v244,s[44:47],s55 offen offset:3072 sc1
mm v[4:7],v[204:207],v[136:139],v[4:7]
wt lgkmcnt(4)
ar v235,a237
mm v[4:7],v[200:203],v[128:131],v[4:7]
rt v[204:205],v241 offset:50816
rt v[206:207],v241 offset:51840
mm v[8:11],v[208:211],v[128:131],v[8:11]
v_perm_b32 v236, v235, v234, s86
v_perm_b32 v237, v235, v234, s87
mm v[8:11],v[212:215],v[136:139],v[8:11]
ld a[28:31],v245,s[44:47],s55 offen offset:3072 sc1
mm v[40:43],v[212:215],v[140:143],v[40:43]
aw a201,v236
aw a205,v237
mm v[40:43],v[208:211],v[132:135],v[40:43]
rt v[200:201],v241 offset:59264
rt v[202:203],v241 offset:60288
mm v[72:75],v[208:211],v[144:147],v[72:75]
v_add_u32 v241, 67584, v241
ar v230,a241
mm v[72:75],v[212:215],v[152:155],v[72:75]
ar v231,a245
v_perm_b32 v232, v231, v230, s86
mm v[104:107],v[212:215],v[168:171],v[104:107]
v_perm_b32 v233, v231, v230, s87
aw a202,v232
mm v[104:107],v[208:211],v[156:159],v[104:107]
rt v[212:213],v242 offset:50816
rt v[214:215],v242 offset:51840
mm v[108:111],v[216:219],v[156:159],v[108:111]
aw a206,v233
ar v234,a249
mm v[108:111],v[220:223],v[168:171],v[108:111]
ar v235,a253
v_perm_b32 v236, v235, v234, s86
mm v[76:79],v[220:223],v[152:155],v[76:79]
v_perm_b32 v237, v235, v234, s87
aw a203,v236
mm v[76:79],v[216:219],v[144:147],v[76:79]
rt v[208:209],v242 offset:59264
rt v[210:211],v242 offset:60288
mm v[44:47],v[216:219],v[132:135],v[44:47]
v_add_u32 v242, 67584, v242
aw a207,v237
mm v[44:47],v[220:223],v[140:143],v[44:47]
ar v230,a226
ar v231,a230
mm v[12:15],v[220:223],v[136:139],v[12:15]
wt lgkmcnt(4)
v_perm_b32 v232, v231, v230, s86
mm v[12:15],v[216:219],v[128:131],v[12:15]
rt v[220:221],v243 offset:50816
rt v[222:223],v243 offset:51840
mm v[16:19],v[196:199],v[128:131],v[16:19]
v_perm_b32 v233, v231, v230, s87
aw a208,v232
mm v[16:19],v[192:195],v[136:139],v[16:19]
ld a[32:35],v246,s[44:47],s54 offen offset:0 sc1
mm v[48:51],v[192:195],v[140:143],v[48:51]
aw a212,v233
ar v234,a234
mm v[48:51],v[196:199],v[132:135],v[48:51]
rt v[216:217],v243 offset:59264
rt v[218:219],v243 offset:60288
mm v[80:83],v[196:199],v[144:147],v[80:83]
v_add_u32 v243, 67584, v243
ar v235,a238
mm v[80:83],v[192:195],v[152:155],v[80:83]
ld a[36:39],v247,s[44:47],s54 offen offset:0 sc1
mm v[112:115],v[192:195],v[168:171],v[112:115]
wt vmcnt(18)
s_barrier
mm v[112:115],v[196:199],v[156:159],v[112:115]
rt v[192:193],v240 offset:50688
rt v[194:195],v240 offset:51712
mm v[116:119],v[204:207],v[156:159],v[116:119]
v_perm_b32 v236, v235, v234, s86
v_perm_b32 v237, v235, v234, s87
mm v[116:119],v[200:203],v[168:171],v[116:119]
ld a[40:43],v246,s[44:47],s54 offen offset:1024 sc1
mm v[84:87],v[200:203],v[152:155],v[84:87]
aw a209,v236
aw a213,v237
mm v[84:87],v[204:207],v[144:147],v[84:87]
rt v[196:197],v240 offset:59136
rt v[198:199],v240 offset:60160
mm v[52:55],v[204:207],v[132:135],v[52:55]
ar v230,a242
ar v231,a246
mm v[52:55],v[200:203],v[140:143],v[52:55]
ld a[44:47],v247,s[44:47],s54 offen offset:1024 sc1
mm v[20:23],v[200:203],v[136:139],v[20:23]
wt lgkmcnt(4)
v_perm_b32 v232, v231, v230, s86
mm v[20:23],v[204:207],v[128:131],v[20:23]
rt v[200:201],v240 offset:0
rt v[202:203],v240 offset:1024
mm v[24:27],v[212:215],v[128:131],v[24:27]
v_perm_b32 v233, v231, v230, s87
aw a210,v232
mm v[24:27],v[208:211],v[136:139],v[24:27]
ld a[48:51],v246,s[44:47],s54 offen offset:2048 sc1
mm v[56:59],v[208:211],v[140:143],v[56:59]
aw a214,v233
ar v234,a250
mm v[56:59],v[212:215],v[132:135],v[56:59]
rt v[204:205],v240 offset:8448
rt v[206:207],v240 offset:9472
mm v[88:91],v[212:215],v[144:147],v[88:91]
ar v235,a254
v_perm_b32 v236, v235, v234, s86
mm v[88:91],v[208:211],v[152:155],v[88:91]
ld a[52:55],v247,s[44:47],s54 offen offset:2048 sc1
mm v[120:123],v[208:211],v[168:171],v[120:123]
v_perm_b32 v237, v235, v234, s87
aw a211,v236
mm v[120:123],v[212:215],v[156:159],v[120:123]
rt v[208:209],v240 offset:16896
rt v[210:211],v240 offset:17920
mm v[124:127],v[220:223],v[156:159],v[124:127]
aw a215,v237
ar v230,a227
mm v[124:127],v[216:219],v[168:171],v[124:127]
ld a[56:59],v246,s[44:47],s54 offen offset:3072 sc1
.La5_LW7_4:
s_cbranch_execz .La5_LA_RETURN
mm v[92:95],v[216:219],v[152:155],v[92:95]
ar v231,a231
v_perm_b32 v232, v231, v230, s86
mm v[92:95],v[220:223],v[144:147],v[92:95]
rt v[212:213],v240 offset:25344
rt v[214:215],v240 offset:26368
mm v[60:63],v[220:223],v[132:135],v[60:63]
v_perm_b32 v233, v231, v230, s87
aw a216,v232
mm v[60:63],v[216:219],v[140:143],v[60:63]
aw a220,v233
ar v234,a235
mm v[28:31],v[216:219],v[136:139],v[28:31]
wt vmcnt(15) lgkmcnt(4)
ar v235,a239
mm v[28:31],v[220:223],v[128:131],v[28:31]
rt v[216:217],v240 offset:33792
rt v[218:219],v240 offset:34816
mm v[0:3],v[192:195],a[64:67],v[0:3]
ld a[60:63],v247,s[44:47],s54 offen offset:3072 sc1
mm v[0:3],v[196:199],a[72:75],v[0:3]
rt v[220:221],v240 offset:42240
rt v[222:223],v240 offset:43264
mm v[32:35],v[196:199],a[76:79],v[32:35]
v_perm_b32 v236, v235, v234, s86
v_perm_b32 v237, v235, v234, s87
mm v[32:35],v[192:195],a[68:71],v[32:35]
rt v[196:197],v241 offset:50688
rt v[198:199],v241 offset:51712
mm v[32:35],v[200:203],a[84:87],v[32:35]
ld v[128:131],v246,s[44:47],s55 offen offset:0 sc1
mm v[32:35],v[204:207],a[92:95],v[32:35]
rt v[192:193],v241 offset:59136
rt v[194:195],v241 offset:60160
mm v[0:3],v[204:207],a[88:91],v[0:3]
wt vmcnt(9) lgkmcnt(4)
aw a217,v236
mm v[0:3],v[200:203],a[80:83],v[0:3]
rt v[204:205],v241 offset:0
rt v[206:207],v241 offset:1024
mm v[0:3],v[208:211],a[0:3],v[0:3]
ld v[132:135],v247,s[44:47],s55 offen offset:0 sc1
mm v[0:3],v[212:215],a[8:11],v[0:3]
rt v[200:201],v241 offset:8448
rt v[202:203],v241 offset:9472
mm v[32:35],v[212:215],a[12:15],v[32:35]
aw a221,v237
ar v230,a243
mm v[32:35],v[208:211],a[4:7],v[32:35]
rt v[212:213],v241 offset:16896
rt v[214:215],v241 offset:17920
mm v[32:35],v[216:219],a[20:23],v[32:35]
ld v[144:147],v246,s[44:47],s55 offen offset:1024 sc1
mm v[32:35],v[220:223],a[28:31],v[32:35]
rt v[208:209],v241 offset:25344
rt v[210:211],v241 offset:26368
mm v[0:3],v[220:223],a[24:27],v[0:3]
wt lgkmcnt(4)
ar v231,a247
mm v[0:3],v[216:219],a[16:19],v[0:3]
rt v[220:221],v241 offset:33792
rt v[222:223],v241 offset:34816
mm v[4:7],v[196:199],a[64:67],v[4:7]
ld v[148:151],v247,s[44:47],s55 offen offset:1024 sc1
mm v[4:7],v[192:195],a[72:75],v[4:7]
rt v[216:217],v241 offset:42240
rt v[218:219],v241 offset:43264
mm v[36:39],v[192:195],a[76:79],v[36:39]
v_perm_b32 v232, v231, v230, s86
v_perm_b32 v233, v231, v230, s87
mm v[36:39],v[196:199],a[68:71],v[36:39]
rt v[192:193],v242 offset:50688
rt v[194:195],v242 offset:51712
mm v[36:39],v[204:207],a[84:87],v[36:39]
ld v[160:163],v246,s[44:47],s55 offen offset:2048 sc1
mm v[36:39],v[200:203],a[92:95],v[36:39]
rt v[196:197],v242 offset:59136
rt v[198:199],v242 offset:60160
aw a218,v232
aw a222,v233
ar v234,a251
ar v235,a255
v_perm_b32 v236, v235, v234, s86
v_perm_b32 v237, v235, v234, s87
aw a219,v236
aw a223,v237
mm v[4:7],v[200:203],a[88:91],v[4:7]
wt lgkmcnt(4)
mm v[4:7],v[204:207],a[80:83],v[4:7]
rt v[200:201],v242 offset:0
rt v[202:203],v242 offset:1024
mm v[4:7],v[212:215],a[0:3],v[4:7]
ld v[164:167],v247,s[44:47],s55 offen offset:2048 sc1
mm v[4:7],v[208:211],a[8:11],v[4:7]
rt v[204:205],v242 offset:8448
rt v[206:207],v242 offset:9472
mm v[36:39],v[208:211],a[12:15],v[36:39]
mm v[36:39],v[212:215],a[4:7],v[36:39]
rt v[208:209],v242 offset:16896
rt v[210:211],v242 offset:17920
mm v[36:39],v[220:223],a[20:23],v[36:39]
mm v[36:39],v[216:219],a[28:31],v[36:39]
rt v[212:213],v242 offset:25344
rt v[214:215],v242 offset:26368
mm v[4:7],v[216:219],a[24:27],v[4:7]
wt lgkmcnt(4)
mm v[4:7],v[220:223],a[16:19],v[4:7]
rt v[216:217],v242 offset:33792
rt v[218:219],v242 offset:34816
mm v[8:11],v[192:195],a[64:67],v[8:11]
ld v[176:179],v246,s[44:47],s55 offen offset:3072 sc1
mm v[8:11],v[196:199],a[72:75],v[8:11]
rt v[220:221],v242 offset:42240
rt v[222:223],v242 offset:43264
mm v[40:43],v[196:199],a[76:79],v[40:43]
cv v234,v32,v33
cv v236,v36,v37
mm v[40:43],v[192:195],a[68:71],v[40:43]
rt v[196:197],v243 offset:50688
rt v[198:199],v243 offset:51712
mm v[40:43],v[200:203],a[84:87],v[40:43]
ld v[180:183],v247,s[44:47],s55 offen offset:3072 sc1
mm v[40:43],v[204:207],a[92:95],v[40:43]
rt v[192:193],v243 offset:59136
rt v[194:195],v243 offset:60160
mm v[8:11],v[204:207],a[88:91],v[8:11]
wt lgkmcnt(4)
v_bfe_u32 v250, v255, 6, 1
mm v[8:11],v[200:203],a[80:83],v[8:11]
rt v[204:205],v243 offset:0
rt v[206:207],v243 offset:1024
mm v[8:11],v[208:211],a[0:3],v[8:11]
cv v235,v34,v35
cv v237,v38,v39
mm v[8:11],v[212:215],a[8:11],v[8:11]
rt v[200:201],v243 offset:8448
rt v[202:203],v243 offset:9472
mm v[40:43],v[212:215],a[12:15],v[40:43]
v_permlane16_swap_b32 v234, v236
v_permlane16_swap_b32 v235, v237
mm v[40:43],v[208:211],a[4:7],v[40:43]
rt v[212:213],v243 offset:16896
rt v[214:215],v243 offset:17920
mm v[40:43],v[216:219],a[20:23],v[40:43]
st v[234:237],v238,s[32:35],s95 offen offset:0 sc1
mm v[40:43],v[220:223],a[28:31],v[40:43]
rt v[208:209],v243 offset:25344
rt v[210:211],v243 offset:26368
mm v[8:11],v[220:223],a[24:27],v[8:11]
wt lgkmcnt(4)
v_mul_u32_u24 v250, 7, v250
mm v[8:11],v[216:219],a[16:19],v[8:11]
rt v[220:221],v243 offset:33792
rt v[222:223],v243 offset:34816
mm v[12:15],v[196:199],a[64:67],v[12:15]
cv v230,v0,v1
cv v232,v4,v5
mm v[12:15],v[192:195],a[72:75],v[12:15]
rt v[216:217],v243 offset:42240
rt v[218:219],v243 offset:43264
mm v[44:47],v[192:195],a[76:79],v[44:47]
cv v231,v2,v3
cv v233,v6,v7
mm v[44:47],v[196:199],a[68:71],v[44:47]
rt v[192:193],v240 offset:50816
rt v[194:195],v240 offset:51840
mm v[44:47],v[204:207],a[84:87],v[44:47]
v_permlane16_swap_b32 v230, v232
v_permlane16_swap_b32 v231, v233
mm v[44:47],v[200:203],a[92:95],v[44:47]
rt v[196:197],v240 offset:59264
rt v[198:199],v240 offset:60288
mm v[12:15],v[200:203],a[88:91],v[12:15]
wt lgkmcnt(4)
v_xor_b32 v250, s3, v250
mm v[12:15],v[204:207],a[80:83],v[12:15]
rt v[200:201],v240 offset:128
rt v[202:203],v240 offset:1152
mm v[12:15],v[212:215],a[0:3],v[12:15]
st v[230:233],v238,s[32:35],s94 offen offset:0 sc1
mm v[12:15],v[208:211],a[8:11],v[12:15]
rt v[204:205],v240 offset:8576
rt v[206:207],v240 offset:9600
mm v[44:47],v[208:211],a[12:15],v[44:47]
v_bfe_u32 v251, v255, 4, 2
v_lshl_add_u32 v250, v250, 2, v251
mm v[44:47],v[212:215],a[4:7],v[44:47]
rt v[208:209],v240 offset:17024
rt v[210:211],v240 offset:18048
mm v[44:47],v[220:223],a[20:23],v[44:47]
ld v[136:139],v224,s[68:71],0 offen offset:0
mm v[44:47],v[216:219],a[28:31],v[44:47]
rt v[212:213],v240 offset:25472
rt v[214:215],v240 offset:26496
mm v[12:15],v[216:219],a[24:27],v[12:15]
wt lgkmcnt(4)
v_lshlrev_b32 v250, 9, v250
mm v[12:15],v[220:223],a[16:19],v[12:15]
rt v[216:217],v240 offset:33920
rt v[218:219],v240 offset:34944
mm v[16:19],v[192:195],a[64:67],v[16:19]
cv v234,v40,v41
cv v236,v44,v45
mm v[16:19],v[196:199],a[72:75],v[16:19]
rt v[220:221],v240 offset:42368
rt v[222:223],v240 offset:43392
mm v[48:51],v[196:199],a[76:79],v[48:51]
cv v235,v42,v43
cv v237,v46,v47
mm v[48:51],v[192:195],a[68:71],v[48:51]
rt v[196:197],v241 offset:50816
rt v[198:199],v241 offset:51840
mm v[48:51],v[200:203],a[84:87],v[48:51]
v_permlane16_swap_b32 v234, v236
v_permlane16_swap_b32 v235, v237
mm v[48:51],v[204:207],a[92:95],v[48:51]
rt v[192:193],v241 offset:59264
rt v[194:195],v241 offset:60288
mm v[16:19],v[204:207],a[88:91],v[16:19]
wt lgkmcnt(4)
v_and_b32 v251, 64, v255
mm v[16:19],v[200:203],a[80:83],v[16:19]
rt v[204:205],v241 offset:128
rt v[206:207],v241 offset:1152
mm v[16:19],v[208:211],a[0:3],v[16:19]
st v[234:237],v238,s[32:35],s95 offen offset:1024 sc1
mm v[16:19],v[212:215],a[8:11],v[16:19]
rt v[200:201],v241 offset:8576
rt v[202:203],v241 offset:9600
mm v[48:51],v[212:215],a[12:15],v[48:51]
cv v230,v8,v9
cv v232,v12,v13
mm v[48:51],v[208:211],a[4:7],v[48:51]
rt v[212:213],v241 offset:17024
rt v[214:215],v241 offset:18048
mm v[48:51],v[216:219],a[20:23],v[48:51]
cv v231,v10,v11
cv v233,v14,v15
mm v[48:51],v[220:223],a[28:31],v[48:51]
rt v[208:209],v241 offset:25472
rt v[210:211],v241 offset:26496
mm v[16:19],v[220:223],a[24:27],v[16:19]
wt lgkmcnt(4)
v_lshl_add_u32 v251, v251, 24, v250
mm v[16:19],v[216:219],a[16:19],v[16:19]
rt v[220:221],v241 offset:33920
rt v[222:223],v241 offset:34944
mm v[20:23],v[196:199],a[64:67],v[20:23]
v_permlane16_swap_b32 v230, v232
v_permlane16_swap_b32 v231, v233
mm v[20:23],v[192:195],a[72:75],v[20:23]
rt v[216:217],v241 offset:42368
rt v[218:219],v241 offset:43392
mm v[52:55],v[192:195],a[76:79],v[52:55]
v_and_b32 v248, 0x1f0, v255
v_lshrrev_b32 v248, 1, v248
mm v[52:55],v[196:199],a[68:71],v[52:55]
rt v[192:193],v242 offset:50816
rt v[194:195],v242 offset:51840
mm v[52:55],v[204:207],a[84:87],v[52:55]
st v[230:233],v238,s[32:35],s94 offen offset:1024 sc1
mm v[52:55],v[200:203],a[92:95],v[52:55]
rt v[196:197],v242 offset:59264
rt v[198:199],v242 offset:60288
mm v[20:23],v[200:203],a[88:91],v[20:23]
wt lgkmcnt(4)
mm v[20:23],v[204:207],a[80:83],v[20:23]
rt v[200:201],v242 offset:128
rt v[202:203],v242 offset:1152
mm v[20:23],v[212:215],a[0:3],v[20:23]
ld v[140:143],v225,s[48:51],0 offen offset:0
mm v[20:23],v[208:211],a[8:11],v[20:23]
rt v[204:205],v242 offset:8576
rt v[206:207],v242 offset:9600
mm v[52:55],v[208:211],a[12:15],v[52:55]
mm v[52:55],v[212:215],a[4:7],v[52:55]
rt v[208:209],v242 offset:17024
rt v[210:211],v242 offset:18048
mm v[52:55],v[220:223],a[20:23],v[52:55]
ld v[152:155],v224,s[68:71],0 offen offset:512
mm v[52:55],v[216:219],a[28:31],v[52:55]
rt v[212:213],v242 offset:25472
rt v[214:215],v242 offset:26496
mm v[20:23],v[216:219],a[24:27],v[20:23]
wt lgkmcnt(4)
s_mov_b32 s93, 0
ld a[224:227],v229,s[28:31],s93 offen offset:0
mm v[20:23],v[220:223],a[16:19],v[20:23]
rt v[216:217],v242 offset:33920
rt v[218:219],v242 offset:34944
mm v[24:27],v[192:195],a[64:67],v[24:27]
cv v234,v48,v49
cv v236,v52,v53
mm v[24:27],v[196:199],a[72:75],v[24:27]
rt v[220:221],v242 offset:42368
rt v[222:223],v242 offset:43392
mm v[56:59],v[196:199],a[76:79],v[56:59]
cv v235,v50,v51
cv v237,v54,v55
mm v[56:59],v[192:195],a[68:71],v[56:59]
rt v[196:197],v243 offset:50816
rt v[198:199],v243 offset:51840
mm v[56:59],v[200:203],a[84:87],v[56:59]
v_permlane16_swap_b32 v234, v236
v_permlane16_swap_b32 v235, v237
mm v[56:59],v[204:207],a[92:95],v[56:59]
rt v[192:193],v243 offset:59264
rt v[194:195],v243 offset:60288
mm v[24:27],v[204:207],a[88:91],v[24:27]
wt lgkmcnt(4)
ld a[228:231],v229,s[28:31],s93 offen offset:2048
mm v[24:27],v[200:203],a[80:83],v[24:27]
rt v[204:205],v243 offset:128
rt v[206:207],v243 offset:1152
mm v[24:27],v[208:211],a[0:3],v[24:27]
st v[234:237],v238,s[32:35],s95 offen offset:2048 sc1
mm v[24:27],v[212:215],a[8:11],v[24:27]
rt v[200:201],v243 offset:8576
rt v[202:203],v243 offset:9600
mm v[56:59],v[212:215],a[12:15],v[56:59]
cv v230,v16,v17
cv v232,v20,v21
mm v[56:59],v[208:211],a[4:7],v[56:59]
rt v[212:213],v243 offset:17024
rt v[214:215],v243 offset:18048
mm v[56:59],v[216:219],a[20:23],v[56:59]
cv v231,v18,v19
cv v233,v22,v23
mm v[56:59],v[220:223],a[28:31],v[56:59]
rt v[208:209],v243 offset:25472
rt v[210:211],v243 offset:26496
mm v[24:27],v[220:223],a[24:27],v[24:27]
wt lgkmcnt(4)
s_mov_b32 s93, 4096
ld a[232:235],v229,s[28:31],s93 offen offset:0
mm v[24:27],v[216:219],a[16:19],v[24:27]
rt v[220:221],v243 offset:33920
rt v[222:223],v243 offset:34944
mm v[28:31],v[196:199],a[64:67],v[28:31]
v_permlane16_swap_b32 v230, v232
v_permlane16_swap_b32 v231, v233
mm v[28:31],v[192:195],a[72:75],v[28:31]
rt v[216:217],v243 offset:42368
rt v[218:219],v243 offset:43392
mm v[60:63],v[192:195],a[76:79],v[60:63]
ld a[236:239],v229,s[28:31],s93 offen offset:2048
mm v[60:63],v[196:199],a[68:71],v[60:63]
rt v[192:193],v240 offset:50688
rt v[194:195],v240 offset:51712
mm v[60:63],v[204:207],a[84:87],v[60:63]
st v[230:233],v238,s[32:35],s94 offen offset:2048 sc1
mm v[60:63],v[200:203],a[92:95],v[60:63]
rt v[196:197],v240 offset:59136
rt v[198:199],v240 offset:60160
mm v[28:31],v[200:203],a[88:91],v[28:31]
wt lgkmcnt(4)
s_mov_b32 s93, 8192
ld a[240:243],v229,s[28:31],s93 offen offset:0
mm v[28:31],v[204:207],a[80:83],v[28:31]
rt v[200:201],v240 offset:0
rt v[202:203],v240 offset:1024
mm v[28:31],v[212:215],a[0:3],v[28:31]
ld v[156:159],v225,s[48:51],0 offen offset:512
mm v[28:31],v[208:211],a[8:11],v[28:31]
rt v[204:205],v240 offset:8448
rt v[206:207],v240 offset:9472
mm v[60:63],v[208:211],a[12:15],v[60:63]
ld a[244:247],v229,s[28:31],s93 offen offset:2048
mm v[60:63],v[212:215],a[4:7],v[60:63]
rt v[208:209],v240 offset:16896
rt v[210:211],v240 offset:17920
mm v[60:63],v[220:223],a[20:23],v[60:63]
ld v[168:171],v224,s[68:71],0 offen offset:1024
mm v[60:63],v[216:219],a[28:31],v[60:63]
rt v[212:213],v240 offset:25344
rt v[214:215],v240 offset:26368
mm v[28:31],v[216:219],a[24:27],v[28:31]
wt vmcnt(25) lgkmcnt(4)
s_mov_b32 s93, 12288
ld a[248:251],v229,s[28:31],s93 offen offset:0
mm v[28:31],v[220:223],a[16:19],v[28:31]
rt v[216:217],v240 offset:33792
rt v[218:219],v240 offset:34816
mm v[64:67],v[192:195],a[32:35],v[64:67]
cv v234,v56,v57
cv v236,v60,v61
mm v[64:67],v[196:199],a[40:43],v[64:67]
rt v[220:221],v240 offset:42240
rt v[222:223],v240 offset:43264
mm v[96:99],v[196:199],a[44:47],v[96:99]
cv v235,v58,v59
cv v237,v62,v63
mm v[96:99],v[192:195],a[36:39],v[96:99]
rt v[196:197],v241 offset:50688
rt v[198:199],v241 offset:51712
mm v[96:99],v[200:203],a[52:55],v[96:99]
v_permlane16_swap_b32 v234, v236
v_permlane16_swap_b32 v235, v237
mm v[96:99],v[204:207],a[60:63],v[96:99]
rt v[192:193],v241 offset:59136
rt v[194:195],v241 offset:60160
mm v[64:67],v[204:207],a[56:59],v[64:67]
wt vmcnt(18) lgkmcnt(4)
ld a[252:255],v229,s[28:31],s93 offen offset:2048
mm v[64:67],v[200:203],a[48:51],v[64:67]
rt v[204:205],v241 offset:0
rt v[206:207],v241 offset:1024
mm v[64:67],v[208:211],v[128:131],v[64:67]
st v[234:237],v238,s[32:35],s95 offen offset:3072 sc1
mm v[64:67],v[212:215],v[144:147],v[64:67]
rt v[200:201],v241 offset:8448
rt v[202:203],v241 offset:9472
mm v[96:99],v[212:215],v[148:151],v[96:99]
cv v230,v24,v25
cv v232,v28,v29
mm v[96:99],v[208:211],v[132:135],v[96:99]
rt v[212:213],v241 offset:16896
rt v[214:215],v241 offset:17920
mm v[96:99],v[216:219],v[164:167],v[96:99]
cv v231,v26,v27
cv v233,v30,v31
mm v[96:99],v[220:223],v[180:183],v[96:99]
rt v[208:209],v241 offset:25344
rt v[210:211],v241 offset:26368
mm v[64:67],v[220:223],v[176:179],v[64:67]
wt lgkmcnt(4)
s_mov_b32 s93, 0
ld a[64:67],v229,s[28:31],s93 offen offset:256
mm v[64:67],v[216:219],v[160:163],v[64:67]
rt v[220:221],v241 offset:33792
rt v[222:223],v241 offset:34816
mm v[68:71],v[196:199],a[32:35],v[68:71]
v_permlane16_swap_b32 v230, v232
v_permlane16_swap_b32 v231, v233
mm v[68:71],v[192:195],a[40:43],v[68:71]
rt v[216:217],v241 offset:42240
rt v[218:219],v241 offset:43264
mm v[100:103],v[192:195],a[44:47],v[100:103]
ld a[68:71],v229,s[28:31],s93 offen offset:2304
mm v[100:103],v[196:199],a[36:39],v[100:103]
rt v[192:193],v242 offset:50688
rt v[194:195],v242 offset:51712
mm v[100:103],v[204:207],a[52:55],v[100:103]
st v[230:233],v238,s[32:35],s94 offen offset:3072 sc1
mm v[100:103],v[200:203],a[60:63],v[100:103]
rt v[196:197],v242 offset:59136
rt v[198:199],v242 offset:60160
mm v[68:71],v[200:203],a[56:59],v[68:71]
wt lgkmcnt(4)
s_mov_b32 s93, 4096
ld a[72:75],v229,s[28:31],s93 offen offset:256
mm v[68:71],v[204:207],a[48:51],v[68:71]
rt v[200:201],v242 offset:0
rt v[202:203],v242 offset:1024
mm v[68:71],v[212:215],v[128:131],v[68:71]
ld v[172:175],v225,s[48:51],0 offen offset:1024
mm v[68:71],v[208:211],v[144:147],v[68:71]
rt v[204:205],v242 offset:8448
rt v[206:207],v242 offset:9472
mm v[100:103],v[208:211],v[148:151],v[100:103]
ld a[76:79],v229,s[28:31],s93 offen offset:2304
mm v[100:103],v[212:215],v[132:135],v[100:103]
rt v[208:209],v242 offset:16896
rt v[210:211],v242 offset:17920
mm v[100:103],v[220:223],v[164:167],v[100:103]
ld v[184:187],v224,s[68:71],0 offen offset:1536
mm v[100:103],v[216:219],v[180:183],v[100:103]
rt v[212:213],v242 offset:25344
rt v[214:215],v242 offset:26368
mm v[68:71],v[216:219],v[176:179],v[68:71]
wt lgkmcnt(4)
s_mov_b32 s93, 8192
ld a[80:83],v229,s[28:31],s93 offen offset:256
mm v[68:71],v[220:223],v[160:163],v[68:71]
rt v[216:217],v242 offset:33792
rt v[218:219],v242 offset:34816
mm v[72:75],v[192:195],a[32:35],v[72:75]
cv v234,v96,v97
cv v236,v100,v101
mm v[72:75],v[196:199],a[40:43],v[72:75]
rt v[220:221],v242 offset:42240
rt v[222:223],v242 offset:43264
mm v[104:107],v[196:199],a[44:47],v[104:107]
cv v235,v98,v99
cv v237,v102,v103
mm v[104:107],v[192:195],a[36:39],v[104:107]
rt v[196:197],v243 offset:50688
rt v[198:199],v243 offset:51712
mm v[104:107],v[200:203],a[52:55],v[104:107]
v_permlane16_swap_b32 v234, v236
v_permlane16_swap_b32 v235, v237
mm v[104:107],v[204:207],a[60:63],v[104:107]
rt v[192:193],v243 offset:59136
rt v[194:195],v243 offset:60160
mm v[72:75],v[204:207],a[56:59],v[72:75]
wt lgkmcnt(4)
ld a[84:87],v229,s[28:31],s93 offen offset:2304
mm v[72:75],v[200:203],a[48:51],v[72:75]
rt v[204:205],v243 offset:0
rt v[206:207],v243 offset:1024
mm v[72:75],v[208:211],v[128:131],v[72:75]
st v[234:237],v238,s[32:35],s97 offen offset:0 sc1
mm v[72:75],v[212:215],v[144:147],v[72:75]
rt v[200:201],v243 offset:8448
rt v[202:203],v243 offset:9472
mm v[104:107],v[212:215],v[148:151],v[104:107]
cv v230,v64,v65
cv v232,v68,v69
mm v[104:107],v[208:211],v[132:135],v[104:107]
rt v[212:213],v243 offset:16896
rt v[214:215],v243 offset:17920
mm v[104:107],v[216:219],v[164:167],v[104:107]
cv v231,v66,v67
cv v233,v70,v71
mm v[104:107],v[220:223],v[180:183],v[104:107]
rt v[208:209],v243 offset:25344
rt v[210:211],v243 offset:26368
mm v[72:75],v[220:223],v[176:179],v[72:75]
wt lgkmcnt(4)
s_mov_b32 s93, 12288
ld a[88:91],v229,s[28:31],s93 offen offset:256
mm v[72:75],v[216:219],v[160:163],v[72:75]
rt v[220:221],v243 offset:33792
rt v[222:223],v243 offset:34816
mm v[76:79],v[196:199],a[32:35],v[76:79]
v_permlane16_swap_b32 v230, v232
v_permlane16_swap_b32 v231, v233
mm v[76:79],v[192:195],a[40:43],v[76:79]
rt v[216:217],v243 offset:42240
rt v[218:219],v243 offset:43264
mm v[108:111],v[192:195],a[44:47],v[108:111]
ld a[92:95],v229,s[28:31],s93 offen offset:2304
mm v[108:111],v[196:199],a[36:39],v[108:111]
rt v[192:193],v240 offset:50816
rt v[194:195],v240 offset:51840
mm v[108:111],v[204:207],a[52:55],v[108:111]
.La5_LW7_5:
s_cbranch_execz .La5_LA_RETURN
st v[230:233],v238,s[32:35],s96 offen offset:0 sc1
mm v[108:111],v[200:203],a[60:63],v[108:111]
rt v[196:197],v240 offset:59264
rt v[198:199],v240 offset:60288
mm v[76:79],v[200:203],a[56:59],v[76:79]
wt lgkmcnt(4)
mm v[76:79],v[204:207],a[48:51],v[76:79]
rt v[200:201],v240 offset:128
rt v[202:203],v240 offset:1152
mm v[76:79],v[212:215],v[128:131],v[76:79]
ld v[188:191],v225,s[48:51],0 offen offset:1536
mm v[76:79],v[208:211],v[144:147],v[76:79]
rt v[204:205],v240 offset:8576
rt v[206:207],v240 offset:9600
mm v[108:111],v[208:211],v[148:151],v[108:111]
mm v[108:111],v[212:215],v[132:135],v[108:111]
rt v[208:209],v240 offset:17024
rt v[210:211],v240 offset:18048
mm v[108:111],v[220:223],v[164:167],v[108:111]
mm v[108:111],v[216:219],v[180:183],v[108:111]
rt v[212:213],v240 offset:25472
rt v[214:215],v240 offset:26496
mm v[76:79],v[216:219],v[176:179],v[76:79]
wt lgkmcnt(4)
mm v[76:79],v[220:223],v[160:163],v[76:79]
rt v[216:217],v240 offset:33920
rt v[218:219],v240 offset:34944
mm v[80:83],v[192:195],a[32:35],v[80:83]
cv v234,v104,v105
cv v236,v108,v109
mm v[80:83],v[196:199],a[40:43],v[80:83]
rt v[220:221],v240 offset:42368
rt v[222:223],v240 offset:43392
mm v[112:115],v[196:199],a[44:47],v[112:115]
cv v235,v106,v107
cv v237,v110,v111
mm v[112:115],v[192:195],a[36:39],v[112:115]
rt v[196:197],v241 offset:50816
rt v[198:199],v241 offset:51840
mm v[112:115],v[200:203],a[52:55],v[112:115]
v_permlane16_swap_b32 v234, v236
v_permlane16_swap_b32 v235, v237
mm v[112:115],v[204:207],a[60:63],v[112:115]
rt v[192:193],v241 offset:59264
rt v[194:195],v241 offset:60288
mm v[80:83],v[204:207],a[56:59],v[80:83]
wt lgkmcnt(4)
mm v[80:83],v[200:203],a[48:51],v[80:83]
rt v[204:205],v241 offset:128
rt v[206:207],v241 offset:1152
mm v[80:83],v[208:211],v[128:131],v[80:83]
st v[234:237],v238,s[32:35],s97 offen offset:1024 sc1
mm v[80:83],v[212:215],v[144:147],v[80:83]
rt v[200:201],v241 offset:8576
rt v[202:203],v241 offset:9600
mm v[112:115],v[212:215],v[148:151],v[112:115]
cv v230,v72,v73
cv v232,v76,v77
mm v[112:115],v[208:211],v[132:135],v[112:115]
rt v[212:213],v241 offset:17024
rt v[214:215],v241 offset:18048
mm v[112:115],v[216:219],v[164:167],v[112:115]
cv v231,v74,v75
cv v233,v78,v79
mm v[112:115],v[220:223],v[180:183],v[112:115]
rt v[208:209],v241 offset:25472
rt v[210:211],v241 offset:26496
mm v[80:83],v[220:223],v[176:179],v[80:83]
wt lgkmcnt(4)
mm v[80:83],v[216:219],v[160:163],v[80:83]
rt v[220:221],v241 offset:33920
rt v[222:223],v241 offset:34944
mm v[84:87],v[196:199],a[32:35],v[84:87]
v_permlane16_swap_b32 v230, v232
v_permlane16_swap_b32 v231, v233
mm v[84:87],v[192:195],a[40:43],v[84:87]
rt v[216:217],v241 offset:42368
rt v[218:219],v241 offset:43392
mm v[116:119],v[192:195],a[44:47],v[116:119]
mm v[116:119],v[196:199],a[36:39],v[116:119]
rt v[192:193],v242 offset:50816
rt v[194:195],v242 offset:51840
mm v[116:119],v[204:207],a[52:55],v[116:119]
st v[230:233],v238,s[32:35],s96 offen offset:1024 sc1
mm v[116:119],v[200:203],a[60:63],v[116:119]
rt v[196:197],v242 offset:59264
rt v[198:199],v242 offset:60288
mm v[84:87],v[200:203],a[56:59],v[84:87]
wt lgkmcnt(4)
mm v[84:87],v[204:207],a[48:51],v[84:87]
rt v[200:201],v242 offset:128
rt v[202:203],v242 offset:1152
mm v[84:87],v[212:215],v[128:131],v[84:87]
mm v[84:87],v[208:211],v[144:147],v[84:87]
rt v[204:205],v242 offset:8576
rt v[206:207],v242 offset:9600
mm v[116:119],v[208:211],v[148:151],v[116:119]
mm v[116:119],v[212:215],v[132:135],v[116:119]
rt v[208:209],v242 offset:17024
rt v[210:211],v242 offset:18048
mm v[116:119],v[220:223],v[164:167],v[116:119]
mm v[116:119],v[216:219],v[180:183],v[116:119]
rt v[212:213],v242 offset:25472
rt v[214:215],v242 offset:26496
mm v[84:87],v[216:219],v[176:179],v[84:87]
wt lgkmcnt(4)
mm v[84:87],v[220:223],v[160:163],v[84:87]
rt v[216:217],v242 offset:33920
rt v[218:219],v242 offset:34944
mm v[88:91],v[192:195],a[32:35],v[88:91]
cv v234,v112,v113
cv v236,v116,v117
mm v[88:91],v[196:199],a[40:43],v[88:91]
rt v[220:221],v242 offset:42368
rt v[222:223],v242 offset:43392
mm v[120:123],v[196:199],a[44:47],v[120:123]
cv v235,v114,v115
cv v237,v118,v119
mm v[120:123],v[192:195],a[36:39],v[120:123]
rt v[196:197],v243 offset:50816
rt v[198:199],v243 offset:51840
mm v[120:123],v[200:203],a[52:55],v[120:123]
v_permlane16_swap_b32 v234, v236
v_permlane16_swap_b32 v235, v237
mm v[120:123],v[204:207],a[60:63],v[120:123]
rt v[192:193],v243 offset:59264
rt v[194:195],v243 offset:60288
mm v[88:91],v[204:207],a[56:59],v[88:91]
wt lgkmcnt(4)
mm v[88:91],v[200:203],a[48:51],v[88:91]
rt v[204:205],v243 offset:128
rt v[206:207],v243 offset:1152
mm v[88:91],v[208:211],v[128:131],v[88:91]
st v[234:237],v238,s[32:35],s97 offen offset:2048 sc1
mm v[88:91],v[212:215],v[144:147],v[88:91]
rt v[200:201],v243 offset:8576
rt v[202:203],v243 offset:9600
mm v[120:123],v[212:215],v[148:151],v[120:123]
cv v230,v80,v81
cv v232,v84,v85
mm v[120:123],v[208:211],v[132:135],v[120:123]
rt v[212:213],v243 offset:17024
rt v[214:215],v243 offset:18048
mm v[120:123],v[216:219],v[164:167],v[120:123]
cv v231,v82,v83
cv v233,v86,v87
wt vmcnt(14)
buffer_store_dwordx2 v[252:253], v251, s[64:67], s92 offen sc1
mm v[120:123],v[220:223],v[180:183],v[120:123]
rt v[208:209],v243 offset:25472
rt v[210:211],v243 offset:26496
mm v[88:91],v[220:223],v[176:179],v[88:91]
wt lgkmcnt(4)
mm v[88:91],v[216:219],v[160:163],v[88:91]
rt v[220:221],v243 offset:33920
rt v[222:223],v243 offset:34944
mm v[92:95],v[196:199],a[32:35],v[92:95]
v_permlane16_swap_b32 v230, v232
v_permlane16_swap_b32 v231, v233
mm v[92:95],v[192:195],a[40:43],v[92:95]
rt v[216:217],v243 offset:42368
rt v[218:219],v243 offset:43392
mm v[124:127],v[192:195],a[44:47],v[124:127]
mm v[124:127],v[196:199],a[36:39],v[124:127]
mm v[124:127],v[204:207],a[52:55],v[124:127]
st v[230:233],v238,s[32:35],s96 offen offset:2048 sc1
mm v[124:127],v[200:203],a[60:63],v[124:127]
mm v[92:95],v[200:203],a[56:59],v[92:95]
wt lgkmcnt(0)
mm v[92:95],v[204:207],a[48:51],v[92:95]
mm v[92:95],v[212:215],v[128:131],v[92:95]
mm v[92:95],v[208:211],v[144:147],v[92:95]
mm v[124:127],v[208:211],v[148:151],v[124:127]
mm v[124:127],v[212:215],v[132:135],v[124:127]
mm v[124:127],v[220:223],v[164:167],v[124:127]
mm v[124:127],v[216:219],v[180:183],v[124:127]
mm v[92:95],v[216:219],v[176:179],v[92:95]
mm v[92:95],v[220:223],v[160:163],v[92:95]
s_nop 7
cv v234,v120,v121
cv v236,v124,v125
cv v235,v122,v123
cv v237,v126,v127
cv v230,v88,v89
cv v232,v92,v93
cv v231,v90,v91
cv v233,v94,v95
v_permlane16_swap_b32 v234, v236
v_permlane16_swap_b32 v235, v237
st v[234:237],v238,s[32:35],s97 offen offset:3072 sc1
v_permlane16_swap_b32 v230, v232
v_permlane16_swap_b32 v231, v233
st v[230:233],v238,s[32:35],s96 offen offset:3072 sc1
.La5_LT_0:
.La5_LW3_5:
s_cbranch_execz .La5_LA_RETURN
.La5_LW7_6:
s_cbranch_execz .La5_LA_RETURN
s_lshl_b32 s91, s76, 5
s_add_u32 s91, s91, s90
s_lshl_b32 s20, s91, 9
s_add_u32 s20, s20, 2097152
s_lshr_b32 s93, s91, 1
s_lshl_b32 s93, s93, 15
s_and_b32 s94, s91, 1
s_lshl2_add_u32 s94, s94, s3
s_lshl_b32 s94, s94, 7
s_add_u32 s97, s93, s94
s_add_u32 s32, s26, 71303168
s_addc_u32 s33, s27, 0
s_and_b32 s33, s33, 0xffff
s_mov_b32 s34, 16777216
s_mov_b32 s35, 0x20000
s_add_u32 s44, s26, 171966464
s_addc_u32 s45, s27, 0
s_and_b32 s45, s45, 0xffff
s_mov_b32 s46, 950272
s_mov_b32 s47, 0x20000
s_add_u32 s56, s38, 0
s_addc_u32 s57, s39, 0
s_and_b32 s57, s57, 0xffff
s_mov_b32 s58, 4194304
s_mov_b32 s59, 0x20000
s_add_u32 s68, s16, 0
s_addc_u32 s69, s17, 0
s_and_b32 s69, s69, 0xffff
s_mov_b32 s70, 4194304
s_mov_b32 s71, 0x20000
v_and_b32 v230, 0x70, v255
v_lshrrev_b32 v231, 7, v255
v_lshl_add_u32 v230, v231, 10, v230
v_add_u32 v240, s97, v230
v_xor_b32 v241, 8192, v240
v_xor_b32 v242, 16384, v240
v_xor_b32 v243, 24576, v240
v_mov_b32 v237, 0x3f803f80
v_mov_b32 v236, 0
wt vmcnt(7)
v_dot2_f32_bf16 v236, v140, v237, v236
v_dot2_f32_bf16 v236, v141, v237, v236
v_dot2_f32_bf16 v236, v142, v237, v236
v_dot2_f32_bf16 v236, v143, v237, v236
v_dot2_f32_bf16 v236, v156, v237, v236
v_dot2_f32_bf16 v236, v157, v237, v236
v_dot2_f32_bf16 v236, v158, v237, v236
v_dot2_f32_bf16 v236, v159, v237, v236
v_dot2_f32_bf16 v236, v172, v237, v236
v_dot2_f32_bf16 v236, v173, v237, v236
v_dot2_f32_bf16 v236, v174, v237, v236
v_dot2_f32_bf16 v236, v175, v237, v236
v_dot2_f32_bf16 v236, v188, v237, v236
v_dot2_f32_bf16 v236, v189, v237, v236
v_dot2_f32_bf16 v236, v190, v237, v236
v_dot2_f32_bf16 v236, v191, v237, v236
v_lshlrev_b32 v0, 16, v136
v_and_b32 v1, 0xffff0000, v136
v_lshlrev_b32 v2, 16, v137
v_and_b32 v3, 0xffff0000, v137
v_lshlrev_b32 v4, 16, v138
v_and_b32 v5, 0xffff0000, v138
v_lshlrev_b32 v6, 16, v139
v_and_b32 v7, 0xffff0000, v139
v_lshlrev_b32 v8, 16, v152
v_and_b32 v9, 0xffff0000, v152
v_lshlrev_b32 v10, 16, v153
v_and_b32 v11, 0xffff0000, v153
v_lshlrev_b32 v12, 16, v154
v_and_b32 v13, 0xffff0000, v154
v_lshlrev_b32 v14, 16, v155
v_and_b32 v15, 0xffff0000, v155
v_lshlrev_b32 v16, 16, v168
v_and_b32 v17, 0xffff0000, v168
v_lshlrev_b32 v18, 16, v169
v_and_b32 v19, 0xffff0000, v169
v_lshlrev_b32 v20, 16, v170
v_and_b32 v21, 0xffff0000, v170
v_lshlrev_b32 v22, 16, v171
v_and_b32 v23, 0xffff0000, v171
v_lshlrev_b32 v24, 16, v184
v_and_b32 v25, 0xffff0000, v184
v_lshlrev_b32 v26, 16, v185
v_and_b32 v27, 0xffff0000, v185
v_lshlrev_b32 v28, 16, v186
v_and_b32 v29, 0xffff0000, v186
v_lshlrev_b32 v30, 16, v187
v_and_b32 v31, 0xffff0000, v187
s_nop 1
v_add_f32_dpp v236, v236, v236 quad_perm:[1,0,3,2] row_mask:0xf bank_mask:0xf
s_nop 1
v_add_f32_dpp v236, v236, v236 quad_perm:[2,3,0,1] row_mask:0xf bank_mask:0xf
s_nop 1
v_add_f32_dpp v236, v236, v236 row_ror:8 row_mask:0xf bank_mask:0xf
v_mov_b32 v230, v236
s_nop 1
v_permlane16_swap_b32 v230, v236
s_nop 1
v_add_f32 v236, v236, v230
v_mov_b32 v230, v236
s_nop 1
v_permlane32_swap_b32 v230, v236
s_nop 1
v_add_f32 v236, v236, v230
s_mov_b64 exec, 0
s_cmp_eq_u32 s3, 0
s_cbranch_scc1 .La5_LW4_0
s_cmp_eq_u32 s3, 1
s_cbranch_scc1 .La5_LW4_1
s_cmp_eq_u32 s3, 2
s_cbranch_scc1 .La5_LW4_2
s_branch .La5_LW4_3
.La5_LW4_R0:
s_mov_b64 exec, -1
wt vmcnt(0)
buffer_store_dwordx2 v[252:253], v250, s[64:67], s92 offen sc1
s_mov_b32 s85, 99
s_cmp_eq_u32 s30, 0
s_cbranch_scc1 .La5_LT_ROWS
; ---- h = x + attn, LayerNorm statistics, router score, two best of eight rows
.La5_LT_ROWS:
s_mov_b32 s96, 0
.La5_LT_P3:
buffer_load_dwordx2 v[250:251], v248, s[64:67], s20 offen sc1
wt vmcnt(0)
.La5_LT_P2:
v_cmp_eq_u64 vcc, v[250:251], v[252:253]
s_add_u32 s96, s96, 1
s_lshr_b32 s92, vcc_lo, 1
s_and_b32 s92, s92, vcc_lo
s_lshr_b32 s93, s92, 2
s_and_b32 s92, s92, s93
s_lshr_b32 s93, s92, 4
s_and_b32 s92, s92, s93
s_lshr_b32 s93, s96, 20
s_mul_i32 s93, s93, 0x01010101
s_or_b32 s92, s92, s93
s_andn2_b32 s92, 0x01010101, s92
s_bcnt1_i32_b32 s93, s92
s_cmp_gt_u32 s93, 1
s_cbranch_scc1 .La5_LT_P3S
s_or_b32 s93, s92, 0x01000000
s_ff1_i32_b32 s93, s93
s_lshl_b32 s80, s93, 19
s_xor_b32 s93, s80, 8388608
s_xor_b32 s94, s80, 12582912
s_xor_b32 s95, s80, 4194304
ld v[208:211],v240,s[32:35],s93 offen sc1
ld v[212:215],v241,s[32:35],s93 offen sc1
ld v[216:219],v242,s[32:35],s93 offen sc1
ld v[220:223],v243,s[32:35],s93 offen sc1
ld v[112:115],v240,s[32:35],s94 offen sc1
ld v[116:119],v241,s[32:35],s94 offen sc1
ld v[120:123],v242,s[32:35],s94 offen sc1
ld v[124:127],v243,s[32:35],s94 offen sc1
ld v[96:99],v240,s[32:35],s95 offen sc1
ld v[100:103],v241,s[32:35],s95 offen sc1
ld v[104:107],v242,s[32:35],s95 offen sc1
ld v[108:111],v243,s[32:35],s95 offen sc1
s_cmp_lg_u32 s92, 0
s_nop 0
s_nop 0
s_cbranch_scc1 .La5_LT_P4
ld v[192:195],v240,s[32:35],s80 offen sc1
ld v[196:199],v241,s[32:35],s80 offen sc1
ld v[200:203],v242,s[32:35],s80 offen sc1
ld v[204:207],v243,s[32:35],s80 offen sc1
.La5_LT_P4:
s_lshr_b32 s93, s80, 16
v_and_b32 v248, 56, v248
s_add_u32 s20, s20, s93
buffer_load_dwordx2 v[250:251], v248, s[64:67], s20 offen sc1
wt vmcnt(8)
v_lshlrev_b32 v32, 16, v208
v_and_b32 v33, 0xffff0000, v208
v_lshlrev_b32 v230, 16, v112
v_and_b32 v231, 0xffff0000, v112
v_pk_add_f32 v[32:33], v[32:33], v[230:231]
v_lshlrev_b32 v34, 16, v209
v_and_b32 v35, 0xffff0000, v209
v_lshlrev_b32 v230, 16, v113
v_and_b32 v231, 0xffff0000, v113
v_pk_add_f32 v[34:35], v[34:35], v[230:231]
v_lshlrev_b32 v36, 16, v210
v_and_b32 v37, 0xffff0000, v210
v_lshlrev_b32 v230, 16, v114
v_and_b32 v231, 0xffff0000, v114
v_pk_add_f32 v[36:37], v[36:37], v[230:231]
v_lshlrev_b32 v38, 16, v211
v_and_b32 v39, 0xffff0000, v211
v_lshlrev_b32 v230, 16, v115
v_and_b32 v231, 0xffff0000, v115
v_pk_add_f32 v[38:39], v[38:39], v[230:231]
wt vmcnt(7)
v_lshlrev_b32 v40, 16, v212
v_and_b32 v41, 0xffff0000, v212
v_lshlrev_b32 v230, 16, v116
v_and_b32 v231, 0xffff0000, v116
v_pk_add_f32 v[40:41], v[40:41], v[230:231]
v_lshlrev_b32 v42, 16, v213
v_and_b32 v43, 0xffff0000, v213
v_lshlrev_b32 v230, 16, v117
v_and_b32 v231, 0xffff0000, v117
v_pk_add_f32 v[42:43], v[42:43], v[230:231]
v_lshlrev_b32 v44, 16, v214
v_and_b32 v45, 0xffff0000, v214
v_lshlrev_b32 v230, 16, v118
v_and_b32 v231, 0xffff0000, v118
v_pk_add_f32 v[44:45], v[44:45], v[230:231]
v_lshlrev_b32 v46, 16, v215
v_and_b32 v47, 0xffff0000, v215
v_lshlrev_b32 v230, 16, v119
v_and_b32 v231, 0xffff0000, v119
v_pk_add_f32 v[46:47], v[46:47], v[230:231]
wt vmcnt(6)
v_lshlrev_b32 v48, 16, v216
v_and_b32 v49, 0xffff0000, v216
v_lshlrev_b32 v230, 16, v120
v_and_b32 v231, 0xffff0000, v120
v_pk_add_f32 v[48:49], v[48:49], v[230:231]
v_lshlrev_b32 v50, 16, v217
v_and_b32 v51, 0xffff0000, v217
v_lshlrev_b32 v230, 16, v121
v_and_b32 v231, 0xffff0000, v121
v_pk_add_f32 v[50:51], v[50:51], v[230:231]
v_lshlrev_b32 v52, 16, v218
v_and_b32 v53, 0xffff0000, v218
v_lshlrev_b32 v230, 16, v122
v_and_b32 v231, 0xffff0000, v122
v_pk_add_f32 v[52:53], v[52:53], v[230:231]
v_lshlrev_b32 v54, 16, v219
v_and_b32 v55, 0xffff0000, v219
v_lshlrev_b32 v230, 16, v123
v_and_b32 v231, 0xffff0000, v123
v_pk_add_f32 v[54:55], v[54:55], v[230:231]
wt vmcnt(5)
v_lshlrev_b32 v56, 16, v220
v_and_b32 v57, 0xffff0000, v220
v_lshlrev_b32 v230, 16, v124
v_and_b32 v231, 0xffff0000, v124
v_pk_add_f32 v[56:57], v[56:57], v[230:231]
v_lshlrev_b32 v58, 16, v221
v_and_b32 v59, 0xffff0000, v221
v_lshlrev_b32 v230, 16, v125
v_and_b32 v231, 0xffff0000, v125
v_pk_add_f32 v[58:59], v[58:59], v[230:231]
v_lshlrev_b32 v60, 16, v222
v_and_b32 v61, 0xffff0000, v222
v_lshlrev_b32 v230, 16, v126
v_and_b32 v231, 0xffff0000, v126
v_pk_add_f32 v[60:61], v[60:61], v[230:231]
v_lshlrev_b32 v62, 16, v223
v_and_b32 v63, 0xffff0000, v223
v_lshlrev_b32 v230, 16, v127
v_and_b32 v231, 0xffff0000, v127
v_pk_add_f32 v[62:63], v[62:63], v[230:231]
wt vmcnt(1)
v_lshlrev_b32 v64, 16, v96
v_and_b32 v65, 0xffff0000, v96
v_lshlrev_b32 v66, 16, v97
v_and_b32 v67, 0xffff0000, v97
v_lshlrev_b32 v68, 16, v98
v_and_b32 v69, 0xffff0000, v98
v_lshlrev_b32 v70, 16, v99
v_and_b32 v71, 0xffff0000, v99
v_lshlrev_b32 v72, 16, v100
v_and_b32 v73, 0xffff0000, v100
v_lshlrev_b32 v74, 16, v101
v_and_b32 v75, 0xffff0000, v101
v_lshlrev_b32 v76, 16, v102
v_and_b32 v77, 0xffff0000, v102
v_lshlrev_b32 v78, 16, v103
v_and_b32 v79, 0xffff0000, v103
v_lshlrev_b32 v80, 16, v104
v_and_b32 v81, 0xffff0000, v104
v_lshlrev_b32 v82, 16, v105
v_and_b32 v83, 0xffff0000, v105
v_lshlrev_b32 v84, 16, v106
v_and_b32 v85, 0xffff0000, v106
v_lshlrev_b32 v86, 16, v107
v_and_b32 v87, 0xffff0000, v107
v_lshlrev_b32 v88, 16, v108
v_and_b32 v89, 0xffff0000, v108
v_lshlrev_b32 v90, 16, v109
v_and_b32 v91, 0xffff0000, v109
v_lshlrev_b32 v92, 16, v110
v_and_b32 v93, 0xffff0000, v110
v_lshlrev_b32 v94, 16, v111
v_and_b32 v95, 0xffff0000, v111
v_mov_b32 v232, 0
v_mov_b32 v233, 0
v_mov_b32_e64 v234, 0
s_cmp_eq_u32 s92, 0
s_cbranch_scc1 .La5_LT_SUM
.La5_LT_P5:
wt vmcnt(0)
v_cmp_eq_u64 vcc, v[250:251], v[252:253]
s_add_u32 s96, s96, 1
s_cmp_eq_u64 vcc, -1
s_cbranch_scc1 .La5_LT_P6
s_lshr_b32 s93, s96, 20
s_nop 0
s_cbranch_scc1 .La5_LT_P6
s_sleep 3
buffer_load_dwordx2 v[250:251], v248, s[64:67], s20 offen sc1
s_branch .La5_LT_P5
.La5_LT_P3S:
s_sleep 24
s_branch .La5_LT_P3
.La5_LT_P6:
ld v[192:195],v240,s[32:35],s80 offen sc1
ld v[196:199],v241,s[32:35],s80 offen sc1
ld v[200:203],v242,s[32:35],s80 offen sc1
ld v[204:207],v243,s[32:35],s80 offen sc1
.La5_LT_SUM:
wt vmcnt(3)
v_lshlrev_b32 v208, 16, v192
v_and_b32 v209, 0xffff0000, v192
v_lshlrev_b32 v210, 16, v193
v_and_b32 v211, 0xffff0000, v193
v_lshlrev_b32 v212, 16, v194
v_and_b32 v213, 0xffff0000, v194
v_lshlrev_b32 v214, 16, v195
v_and_b32 v215, 0xffff0000, v195
v_pk_add_f32 v[208:209], v[208:209], v[64:65]
v_pk_add_f32 v[210:211], v[210:211], v[66:67]
v_pk_add_f32 v[212:213], v[212:213], v[68:69]
v_pk_add_f32 v[214:215], v[214:215], v[70:71]
v_pk_add_f32 v[208:209], v[208:209], v[32:33]
v_pk_add_f32 v[210:211], v[210:211], v[34:35]
v_pk_add_f32 v[212:213], v[212:213], v[36:37]
v_pk_add_f32 v[214:215], v[214:215], v[38:39]
v_pk_add_f32 v[208:209], v[208:209], v[0:1]
v_pk_add_f32 v[210:211], v[210:211], v[2:3]
v_pk_add_f32 v[212:213], v[212:213], v[4:5]
v_pk_add_f32 v[214:215], v[214:215], v[6:7]
cv v96,v208,v209
cv v97,v210,v211
cv v98,v212,v213
cv v99,v214,v215
v_dot2_f32_bf16 v232, v96, v237, v232
v_dot2_f32_bf16 v233, v96, v96, v233
v_dot2_f32_bf16 v234, v96, v140, v234
v_dot2_f32_bf16 v232, v97, v237, v232
v_dot2_f32_bf16 v233, v97, v97, v233
v_dot2_f32_bf16 v234, v97, v141, v234
v_dot2_f32_bf16 v232, v98, v237, v232
v_dot2_f32_bf16 v233, v98, v98, v233
v_dot2_f32_bf16 v234, v98, v142, v234
v_dot2_f32_bf16 v232, v99, v237, v232
v_dot2_f32_bf16 v233, v99, v99, v233
v_dot2_f32_bf16 v234, v99, v143, v234
wt vmcnt(2)
v_lshlrev_b32 v208, 16, v196
v_and_b32 v209, 0xffff0000, v196
v_lshlrev_b32 v210, 16, v197
v_and_b32 v211, 0xffff0000, v197
v_lshlrev_b32 v212, 16, v198
v_and_b32 v213, 0xffff0000, v198
v_lshlrev_b32 v214, 16, v199
v_and_b32 v215, 0xffff0000, v199
v_pk_add_f32 v[208:209], v[208:209], v[72:73]
v_pk_add_f32 v[210:211], v[210:211], v[74:75]
v_pk_add_f32 v[212:213], v[212:213], v[76:77]
v_pk_add_f32 v[214:215], v[214:215], v[78:79]
v_pk_add_f32 v[208:209], v[208:209], v[40:41]
v_pk_add_f32 v[210:211], v[210:211], v[42:43]
v_pk_add_f32 v[212:213], v[212:213], v[44:45]
v_pk_add_f32 v[214:215], v[214:215], v[46:47]
v_pk_add_f32 v[208:209], v[208:209], v[8:9]
v_pk_add_f32 v[210:211], v[210:211], v[10:11]
v_pk_add_f32 v[212:213], v[212:213], v[12:13]
v_pk_add_f32 v[214:215], v[214:215], v[14:15]
cv v100,v208,v209
cv v101,v210,v211
cv v102,v212,v213
cv v103,v214,v215
v_dot2_f32_bf16 v232, v100, v237, v232
v_dot2_f32_bf16 v233, v100, v100, v233
v_dot2_f32_bf16 v234, v100, v156, v234
v_dot2_f32_bf16 v232, v101, v237, v232
v_dot2_f32_bf16 v233, v101, v101, v233
v_dot2_f32_bf16 v234, v101, v157, v234
v_dot2_f32_bf16 v232, v102, v237, v232
v_dot2_f32_bf16 v233, v102, v102, v233
v_dot2_f32_bf16 v234, v102, v158, v234
v_dot2_f32_bf16 v232, v103, v237, v232
v_dot2_f32_bf16 v233, v103, v103, v233
v_dot2_f32_bf16 v234, v103, v159, v234
wt vmcnt(1)
v_lshlrev_b32 v208, 16, v200
v_and_b32 v209, 0xffff0000, v200
v_lshlrev_b32 v210, 16, v201
v_and_b32 v211, 0xffff0000, v201
v_lshlrev_b32 v212, 16, v202
v_and_b32 v213, 0xffff0000, v202
v_lshlrev_b32 v214, 16, v203
v_and_b32 v215, 0xffff0000, v203
v_pk_add_f32 v[208:209], v[208:209], v[80:81]
v_pk_add_f32 v[210:211], v[210:211], v[82:83]
v_pk_add_f32 v[212:213], v[212:213], v[84:85]
v_pk_add_f32 v[214:215], v[214:215], v[86:87]
v_pk_add_f32 v[208:209], v[208:209], v[48:49]
v_pk_add_f32 v[210:211], v[210:211], v[50:51]
v_pk_add_f32 v[212:213], v[212:213], v[52:53]
v_pk_add_f32 v[214:215], v[214:215], v[54:55]
v_pk_add_f32 v[208:209], v[208:209], v[16:17]
v_pk_add_f32 v[210:211], v[210:211], v[18:19]
v_pk_add_f32 v[212:213], v[212:213], v[20:21]
v_pk_add_f32 v[214:215], v[214:215], v[22:23]
cv v104,v208,v209
cv v105,v210,v211
cv v106,v212,v213
cv v107,v214,v215
v_dot2_f32_bf16 v232, v104, v237, v232
v_dot2_f32_bf16 v233, v104, v104, v233
v_dot2_f32_bf16 v234, v104, v172, v234
v_dot2_f32_bf16 v232, v105, v237, v232
v_dot2_f32_bf16 v233, v105, v105, v233
v_dot2_f32_bf16 v234, v105, v173, v234
v_dot2_f32_bf16 v232, v106, v237, v232
v_dot2_f32_bf16 v233, v106, v106, v233
v_dot2_f32_bf16 v234, v106, v174, v234
.La5_LW4_0:
s_cbranch_execz .La5_LW4_4
v_dot2_f32_bf16 v232, v107, v237, v232
v_dot2_f32_bf16 v233, v107, v107, v233
v_dot2_f32_bf16 v234, v107, v175, v234
wt vmcnt(0)
v_lshlrev_b32 v208, 16, v204
v_and_b32 v209, 0xffff0000, v204
v_lshlrev_b32 v210, 16, v205
v_and_b32 v211, 0xffff0000, v205
v_lshlrev_b32 v212, 16, v206
v_and_b32 v213, 0xffff0000, v206
v_lshlrev_b32 v214, 16, v207
v_and_b32 v215, 0xffff0000, v207
v_pk_add_f32 v[208:209], v[208:209], v[88:89]
v_pk_add_f32 v[210:211], v[210:211], v[90:91]
v_pk_add_f32 v[212:213], v[212:213], v[92:93]
v_pk_add_f32 v[214:215], v[214:215], v[94:95]
v_pk_add_f32 v[208:209], v[208:209], v[56:57]
v_pk_add_f32 v[210:211], v[210:211], v[58:59]
v_pk_add_f32 v[212:213], v[212:213], v[60:61]
v_pk_add_f32 v[214:215], v[214:215], v[62:63]
v_pk_add_f32 v[208:209], v[208:209], v[24:25]
v_pk_add_f32 v[210:211], v[210:211], v[26:27]
v_pk_add_f32 v[212:213], v[212:213], v[28:29]
v_pk_add_f32 v[214:215], v[214:215], v[30:31]
cv v108,v208,v209
cv v109,v210,v211
cv v110,v212,v213
cv v111,v214,v215
v_dot2_f32_bf16 v232, v108, v237, v232
v_dot2_f32_bf16 v233, v108, v108, v233
v_dot2_f32_bf16 v234, v108, v188, v234
v_dot2_f32_bf16 v232, v109, v237, v232
v_dot2_f32_bf16 v233, v109, v109, v233
v_dot2_f32_bf16 v234, v109, v189, v234
v_dot2_f32_bf16 v232, v110, v237, v232
v_dot2_f32_bf16 v233, v110, v110, v233
v_dot2_f32_bf16 v234, v110, v190, v234
v_dot2_f32_bf16 v232, v111, v237, v232
v_dot2_f32_bf16 v233, v111, v111, v233
v_dot2_f32_bf16 v234, v111, v191, v234
wt vmcnt(0)
s_lshl2_add_u32 s92, s91, s3
s_lshl_b32 s92, s92, 1
s_sub_u32 s92, 0xffff, s92
v_add_f32_dpp v232, v232, v232 quad_perm:[1,0,3,2] row_mask:0xf bank_mask:0xf
v_add_f32_dpp v233, v233, v233 quad_perm:[1,0,3,2] row_mask:0xf bank_mask:0xf
v_add_f32_dpp v234, v234, v234 quad_perm:[1,0,3,2] row_mask:0xf bank_mask:0xf
v_add_f32_dpp v232, v232, v232 quad_perm:[2,3,0,1] row_mask:0xf bank_mask:0xf
v_add_f32_dpp v233, v233, v233 quad_perm:[2,3,0,1] row_mask:0xf bank_mask:0xf
v_add_f32_dpp v234, v234, v234 quad_perm:[2,3,0,1] row_mask:0xf bank_mask:0xf
v_add_f32_dpp v232, v232, v232 row_ror:8 row_mask:0xf bank_mask:0xf
v_add_f32_dpp v233, v233, v233 row_ror:8 row_mask:0xf bank_mask:0xf
v_add_f32_dpp v234, v234, v234 row_ror:8 row_mask:0xf bank_mask:0xf
v_mov_b32 v230, v232
v_mov_b32 v231, v233
v_mov_b32 v235, v234
v_permlane16_swap_b32 v230, v232
v_permlane16_swap_b32 v231, v233
v_permlane16_swap_b32 v235, v234
v_add_f32 v232, v232, v230
v_add_f32 v233, v233, v231
v_add_f32 v234, v234, v235
v_mov_b32 v230, v232
v_mov_b32 v231, v233
v_mov_b32 v235, v234
v_permlane32_swap_b32 v230, v232
v_permlane32_swap_b32 v231, v233
v_permlane32_swap_b32 v235, v234
v_add_f32 v232, v232, v230
v_add_f32 v233, v233, v231
v_add_f32 v234, v234, v235
v_mul_f32 v244, 0x3a800000, v232
v_mul_f32 v233, 0x3a800000, v233
v_fma_f32 v230, -v244, v244, v233
v_mul_f32 v233, 0x3a800000, v233
v_cmp_nge_f32 vcc, v230, v233
v_bfe_u32 v235, v255, 6, 1
v_sub_u32 v235, s92, v235
s_cbranch_vccnz .La5_LT_VAR
.La5_LT_VOK:
cv v244,v244,v244
v_lshlrev_b32 v244, 16, v244
cv v230,v230,v230
v_lshlrev_b32 v230, 16, v230
v_fma_f32 v234, -v244, v236, v234
v_add_f32 v230, 0x3727c5ac, v230
cv v230,v230,v230
v_lshlrev_b32 v230, 16, v230
v_sqrt_f32 v230, v230
v_lshrrev_b32 v232, 3, v255
cv v230,v230,v230
v_lshlrev_b32 v231, 16, v230
v_rcp_f32 v246, v231
v_lshrrev_b32 v230, 16, v230
v_or_b32 v233, v244, v230
v_mul_f32 v234, v234, v246
s_lshl_b32 s93, s3, 4
cv v234,v234,v234
v_and_b32 v230, 8, v232
v_ashrrev_i32 v232, 31, v234
v_or_b32 v232, 0x80000000, v232
v_xor_b32 v232, v232, v234
v_and_b32 v232, 0xffff0000, v232
v_add_u32 v230, s93, v230
v_or_b32 v232, v232, v235
ds_write_b64 v230, v[232:233] offset:0
v_lshrrev_b32 v230, 1, v255
v_and_b32 v230, 56, v230
wt lgkmcnt(0)
s_barrier
s_cmp_lg_u32 s3, 0
s_cbranch_scc1 .La5_LT_OUT
ds_read_b64 v[232:233], v230 offset:0
s_lshl3_add_u32 s92, s90, s76
s_lshl_b32 s92, s92, 4
v_lshrrev_b32 v235, 7, v255
v_lshlrev_b32 v235, 12, v235
wt lgkmcnt(0)
v_mov_b32 v234, v232
s_nop 1
v_max_u32_dpp v234, v234, v234 quad_perm:[1,0,3,2] row_mask:0xf bank_mask:0xf
s_nop 1
v_max_u32_dpp v234, v234, v234 quad_perm:[2,3,0,1] row_mask:0xf bank_mask:0xf
s_nop 1
v_max_u32_dpp v234, v234, v234 row_half_mirror row_mask:0xf bank_mask:0xf
s_nop 1
v_cmp_eq_u32 vcc, v232, v234
v_add_u32 v231, 8, v235
s_nop 0
v_cndmask_b32 v234, v232, 0, vcc
v_cndmask_b32 v235, v231, v235, vcc
s_nop 0
v_max_u32_dpp v234, v234, v234 quad_perm:[1,0,3,2] row_mask:0xf bank_mask:0xf
s_nop 1
v_max_u32_dpp v234, v234, v234 quad_perm:[2,3,0,1] row_mask:0xf bank_mask:0xf
s_nop 1
v_max_u32_dpp v234, v234, v234 row_half_mirror row_mask:0xf bank_mask:0xf
s_nop 1
v_cmp_ge_u32 vcc, v232, v234
s_nop 1
s_mov_b64 exec, vcc
buffer_store_dwordx2 v[232:233], v235, s[44:47], s92 offen sc1
s_mov_b64 exec, -1
; ---- out = h + .La5_LN(h) of the own rows; FFN weights into MFMA order
.La5_LT_OUT:
v_mul_f32 v244, v244, v246
v_mov_b32 v247, v246
v_xor_b32 v244, 0x80000000, v244
v_mov_b32 v245, v244
v_lshlrev_b32 v230, 16, v96
v_and_b32 v231, 0xffff0000, v96
v_pk_fma_f32 v[232:233], v[230:231], v[246:247], v[244:245]
cv v232,v232,v233
v_lshlrev_b32 v234, 16, v232
v_and_b32 v235, 0xffff0000, v232
v_pk_add_f32 v[230:231], v[230:231], v[234:235]
cv v192,v230,v231
v_lshlrev_b32 v230, 16, v97
v_and_b32 v231, 0xffff0000, v97
v_pk_fma_f32 v[232:233], v[230:231], v[246:247], v[244:245]
cv v232,v232,v233
v_lshlrev_b32 v234, 16, v232
v_and_b32 v235, 0xffff0000, v232
v_pk_add_f32 v[230:231], v[230:231], v[234:235]
cv v193,v230,v231
v_lshlrev_b32 v230, 16, v98
v_and_b32 v231, 0xffff0000, v98
v_pk_fma_f32 v[232:233], v[230:231], v[246:247], v[244:245]
cv v232,v232,v233
v_lshlrev_b32 v234, 16, v232
v_and_b32 v235, 0xffff0000, v232
v_pk_add_f32 v[230:231], v[230:231], v[234:235]
cv v194,v230,v231
v_lshlrev_b32 v230, 16, v99
v_and_b32 v231, 0xffff0000, v99
v_pk_fma_f32 v[232:233], v[230:231], v[246:247], v[244:245]
cv v232,v232,v233
v_lshlrev_b32 v234, 16, v232
v_and_b32 v235, 0xffff0000, v232
v_pk_add_f32 v[230:231], v[230:231], v[234:235]
cv v195,v230,v231
v_lshlrev_b32 v230, 16, v100
v_and_b32 v231, 0xffff0000, v100
v_pk_fma_f32 v[232:233], v[230:231], v[246:247], v[244:245]
cv v232,v232,v233
v_lshlrev_b32 v234, 16, v232
v_and_b32 v235, 0xffff0000, v232
v_pk_add_f32 v[230:231], v[230:231], v[234:235]
cv v196,v230,v231
v_lshlrev_b32 v230, 16, v101
v_and_b32 v231, 0xffff0000, v101
v_pk_fma_f32 v[232:233], v[230:231], v[246:247], v[244:245]
cv v232,v232,v233
v_lshlrev_b32 v234, 16, v232
v_and_b32 v235, 0xffff0000, v232
v_pk_add_f32 v[230:231], v[230:231], v[234:235]
cv v197,v230,v231
v_lshlrev_b32 v230, 16, v102
v_and_b32 v231, 0xffff0000, v102
v_pk_fma_f32 v[232:233], v[230:231], v[246:247], v[244:245]
cv v232,v232,v233
v_lshlrev_b32 v234, 16, v232
v_and_b32 v235, 0xffff0000, v232
v_pk_add_f32 v[230:231], v[230:231], v[234:235]
cv v198,v230,v231
v_lshlrev_b32 v230, 16, v103
v_and_b32 v231, 0xffff0000, v103
v_pk_fma_f32 v[232:233], v[230:231], v[246:247], v[244:245]
cv v232,v232,v233
v_lshlrev_b32 v234, 16, v232
v_and_b32 v235, 0xffff0000, v232
v_pk_add_f32 v[230:231], v[230:231], v[234:235]
cv v199,v230,v231
v_lshlrev_b32 v230, 16, v104
v_and_b32 v231, 0xffff0000, v104
v_pk_fma_f32 v[232:233], v[230:231], v[246:247], v[244:245]
cv v232,v232,v233
v_lshlrev_b32 v234, 16, v232
v_and_b32 v235, 0xffff0000, v232
v_pk_add_f32 v[230:231], v[230:231], v[234:235]
cv v200,v230,v231
v_lshlrev_b32 v230, 16, v105
v_and_b32 v231, 0xffff0000, v105
v_pk_fma_f32 v[232:233], v[230:231], v[246:247], v[244:245]
cv v232,v232,v233
v_lshlrev_b32 v234, 16, v232
v_and_b32 v235, 0xffff0000, v232
v_pk_add_f32 v[230:231], v[230:231], v[234:235]
cv v201,v230,v231
v_lshlrev_b32 v230, 16, v106
v_and_b32 v231, 0xffff0000, v106
v_pk_fma_f32 v[232:233], v[230:231], v[246:247], v[244:245]
cv v232,v232,v233
v_lshlrev_b32 v234, 16, v232
v_and_b32 v235, 0xffff0000, v232
v_pk_add_f32 v[230:231], v[230:231], v[234:235]
cv v202,v230,v231
v_lshlrev_b32 v230, 16, v107
v_and_b32 v231, 0xffff0000, v107
v_pk_fma_f32 v[232:233], v[230:231], v[246:247], v[244:245]
cv v232,v232,v233
v_lshlrev_b32 v234, 16, v232
v_and_b32 v235, 0xffff0000, v232
v_pk_add_f32 v[230:231], v[230:231], v[234:235]
cv v203,v230,v231
v_lshlrev_b32 v230, 16, v108
v_and_b32 v231, 0xffff0000, v108
v_pk_fma_f32 v[232:233], v[230:231], v[246:247], v[244:245]
cv v232,v232,v233
v_lshlrev_b32 v234, 16, v232
v_and_b32 v235, 0xffff0000, v232
v_pk_add_f32 v[230:231], v[230:231], v[234:235]
cv v204,v230,v231
v_lshlrev_b32 v230, 16, v109
v_and_b32 v231, 0xffff0000, v109
v_pk_fma_f32 v[232:233], v[230:231], v[246:247], v[244:245]
cv v232,v232,v233
v_lshlrev_b32 v234, 16, v232
v_and_b32 v235, 0xffff0000, v232
v_pk_add_f32 v[230:231], v[230:231], v[234:235]
cv v205,v230,v231
v_lshlrev_b32 v230, 16, v110
v_and_b32 v231, 0xffff0000, v110
v_pk_fma_f32 v[232:233], v[230:231], v[246:247], v[244:245]
cv v232,v232,v233
v_lshlrev_b32 v234, 16, v232
v_and_b32 v235, 0xffff0000, v232
v_pk_add_f32 v[230:231], v[230:231], v[234:235]
cv v206,v230,v231
v_lshlrev_b32 v230, 16, v111
v_and_b32 v231, 0xffff0000, v111
v_pk_fma_f32 v[232:233], v[230:231], v[246:247], v[244:245]
cv v232,v232,v233
v_lshlrev_b32 v234, 16, v232
v_and_b32 v235, 0xffff0000, v232
v_pk_add_f32 v[230:231], v[230:231], v[234:235]
cv v207,v230,v231
s_cmp_eq_u32 s30, 0
s_cbranch_scc1 .La5_LT_SEL
s_mov_b32 s94, 0x05040100
s_mov_b32 s95, 0x07060302
wt vmcnt(0)
ar v230,a224
ar v231,a228
v_perm_b32 v128, v231, v230, s94
v_perm_b32 v132, v231, v230, s95
ar v230,a232
ar v231,a236
v_perm_b32 v129, v231, v230, s94
v_perm_b32 v133, v231, v230, s95
ar v230,a240
ar v231,a244
v_perm_b32 v130, v231, v230, s94
v_perm_b32 v134, v231, v230, s95
ar v230,a248
ar v231,a252
v_perm_b32 v131, v231, v230, s94
v_perm_b32 v135, v231, v230, s95
ar v230,a225
ar v231,a229
v_perm_b32 v136, v231, v230, s94
v_perm_b32 v140, v231, v230, s95
ar v230,a233
ar v231,a237
v_perm_b32 v137, v231, v230, s94
v_perm_b32 v141, v231, v230, s95
ar v230,a241
ar v231,a245
v_perm_b32 v138, v231, v230, s94
v_perm_b32 v142, v231, v230, s95
ar v230,a249
ar v231,a253
v_perm_b32 v139, v231, v230, s94
v_perm_b32 v143, v231, v230, s95
ar v230,a226
ar v231,a230
v_perm_b32 v144, v231, v230, s94
v_perm_b32 v148, v231, v230, s95
ar v230,a234
ar v231,a238
v_perm_b32 v145, v231, v230, s94
v_perm_b32 v149, v231, v230, s95
ar v230,a242
ar v231,a246
v_perm_b32 v146, v231, v230, s94
v_perm_b32 v150, v231, v230, s95
ar v230,a250
ar v231,a254
v_perm_b32 v147, v231, v230, s94
v_perm_b32 v151, v231, v230, s95
ar v230,a227
ar v231,a231
v_perm_b32 v152, v231, v230, s94
v_perm_b32 v156, v231, v230, s95
ar v230,a235
ar v231,a239
v_perm_b32 v153, v231, v230, s94
v_perm_b32 v157, v231, v230, s95
ar v230,a243
ar v231,a247
v_perm_b32 v154, v231, v230, s94
v_perm_b32 v158, v231, v230, s95
ar v230,a251
ar v231,a255
v_perm_b32 v155, v231, v230, s94
v_perm_b32 v159, v231, v230, s95
ar v230,a64
ar v231,a68
v_perm_b32 v160, v231, v230, s94
v_perm_b32 v164, v231, v230, s95
ar v230,a72
ar v231,a76
v_perm_b32 v161, v231, v230, s94
v_perm_b32 v165, v231, v230, s95
ar v230,a80
ar v231,a84
v_perm_b32 v162, v231, v230, s94
v_perm_b32 v166, v231, v230, s95
ar v230,a88
ar v231,a92
v_perm_b32 v163, v231, v230, s94
v_perm_b32 v167, v231, v230, s95
ar v230,a65
ar v231,a69
v_perm_b32 v168, v231, v230, s94
v_perm_b32 v172, v231, v230, s95
ar v230,a73
ar v231,a77
v_perm_b32 v169, v231, v230, s94
v_perm_b32 v173, v231, v230, s95
ar v230,a81
ar v231,a85
v_perm_b32 v170, v231, v230, s94
v_perm_b32 v174, v231, v230, s95
ar v230,a89
ar v231,a93
v_perm_b32 v171, v231, v230, s94
v_perm_b32 v175, v231, v230, s95
ar v230,a66
ar v231,a70
v_perm_b32 v176, v231, v230, s94
v_perm_b32 v180, v231, v230, s95
ar v230,a74
ar v231,a78
v_perm_b32 v177, v231, v230, s94
v_perm_b32 v181, v231, v230, s95
ar v230,a82
ar v231,a86
v_perm_b32 v178, v231, v230, s94
v_perm_b32 v182, v231, v230, s95
ar v230,a90
ar v231,a94
v_perm_b32 v179, v231, v230, s94
v_perm_b32 v183, v231, v230, s95
ar v230,a67
ar v231,a71
v_perm_b32 v184, v231, v230, s94
v_perm_b32 v188, v231, v230, s95
ar v230,a75
ar v231,a79
v_perm_b32 v185, v231, v230, s94
v_perm_b32 v189, v231, v230, s95
ar v230,a83
ar v231,a87
v_perm_b32 v186, v231, v230, s94
v_perm_b32 v190, v231, v230, s95
ar v230,a91
ar v231,a95
v_perm_b32 v187, v231, v230, s94
v_perm_b32 v191, v231, v230, s95
s_and_b32 s92, s3, 1
s_lshr_b32 s93, s3, 1
v_lshrrev_b32 v230, 6, v255
v_and_b32 v231, 48, v255
v_lshl_add_u32 v240, v230, 10, v231
s_lshl_b32 s96, s92, 14
v_add_u32 v240, s96, v240
s_lshl_b32 s96, s92, 10
v_add_u32 v241, s96, v255
s_lshl_b32 s97, s3, 10
v_add_u32 v208, s97, v255
v_bfe_u32 v230, v255, 4, 1
v_bfe_u32 v231, v255, 6, 2
v_lshrrev_b32 v232, 8, v255
v_lshlrev_b32 v209, 11, v230
v_lshl_add_u32 v209, v231, 7, v209
v_lshl_add_u32 v209, v232, 5, v209
s_lshr_b32 s96, s96, 1
v_add_u32 v209, s96, v209
v_bfe_u32 v233, v255, 4, 2
v_mul_u32_u24 v210, 640, v233
v_lshl_add_u32 v210, v231, 3, v210
s_mul_i32 s96, s93, 2560
s_lshl_b32 s92, s92, 5
s_add_u32 s96, s96, s92
v_add_u32 v210, s96, v210
v_cmp_eq_u32 s[98:99], v231, v232
v_mul_u32_u24 v211, 5, v255
s_lshl_b32 s96, s3, 7
v_lshrrev_b32 v212, 3, v255
v_and_b32 v212, 62, v212
v_add_u32 v212, s96, v212
v_lshlrev_b32 v234, 6, v230
v_lshl_add_u32 v234, v232, 4, v234
v_add_u32 v234, s96, v234
v_mov_b32 v235, 1024
v_cmp_eq_u32 vcc, 0, v231
s_nop 1
v_cndmask_b32 v216, v235, v234, vcc
v_cmp_eq_u32 vcc, 1, v231
s_nop 1
v_cndmask_b32 v217, v235, v234, vcc
v_cmp_eq_u32 vcc, 2, v231
s_nop 1
v_cndmask_b32 v218, v235, v234, vcc
v_cmp_eq_u32 vcc, 3, v231
s_nop 1
v_cndmask_b32 v219, v235, v234, vcc
v_mov_b32 v220, 0
v_mov_b32 v221, 0
v_mov_b32 v222, 0
v_mov_b32_e64 v223, 0
ds_write_b128 v235, v[220:223] offset:10240
s_lshl_b32 s92, s100, 13
s_add_u32 s80, s92, 32768
s_add_u32 s20, s80, 4096
; ---- top two rows of the sequence; SwiGLU slice of 32 hidden units
.La5_LT_SEL:
.La5_LW4_1:
s_cbranch_execz .La5_LW4_R0
s_and_b32 s97, s2, 7
s_lshl_b32 s97, s97, 12
s_cmp_lg_u32 s3, 0
s_cbranch_scc1 .La5_LT_S2
s_mov_b32 s96, 0
v_and_b32 v229, 255, v255
.La5_LT_SA:
s_sleep 2
ld v[224:227],v229,s[44:47],s97 offen offset:0 sc1
s_add_u32 s96, s96, 1
s_lshr_b32 s92, s96, 20
wt vmcnt(0)
v_cmp_ne_u32 vcc, 0, v224
s_nop 0
s_cbranch_scc1 .La5_LT_S1
s_cmp_eq_u64 vcc, -1
s_cbranch_scc0 .La5_LT_SA
.La5_LT_S1:
s_cmp_lg_u32 s30, 0
s_cbranch_scc1 .La5_LT_S0
s_sleep 6
.La5_LT_S0:
ld v[224:227],v255,s[44:47],s97 offen offset:0 sc1
ld v[232:235],v255,s[44:47],s97 offen offset:1024 sc1
ld v[244:247],v255,s[44:47],s97 offen offset:2048 sc1
ld v[248:251],v255,s[44:47],s97 offen offset:3072 sc1
s_add_u32 s96, s96, 1
s_lshr_b32 s92, s96, 20
wt vmcnt(3)
v_min3_u32 v228, v224, v225, v226
v_min_u32 v228, v227, v228
wt vmcnt(2)
v_min3_u32 v228, v232, v233, v228
v_min3_u32 v228, v234, v235, v228
wt vmcnt(1)
v_min3_u32 v228, v244, v245, v228
v_min3_u32 v228, v246, v247, v228
wt vmcnt(0)
v_min3_u32 v228, v248, v249, v228
v_min3_u32 v228, v250, v251, v228
v_cmp_ne_u32 vcc, 0, v228
v_max_u32 v229, v224, v232
v_max_u32 v230, v244, v248
s_nop 0
s_nop 0
s_cbranch_scc1 .La5_LT_S3
s_cmp_eq_u64 vcc, -1
s_cbranch_scc0 .La5_LT_S1
.La5_LT_S3:
v_max_u32 v228, v229, v230
v_min_u32 v229, v229, v230
v_mov_b32 v231, v228
v_min_u32 v224, v224, v232
v_min_u32 v244, v244, v248
v_max3_u32 v229, v229, v224, v244
v_max3_u32 v230, v226, v234, v246
v_max3_u32 v229, v229, v230, v250
s_nop 1
v_max_u32_dpp v231, v231, v231 row_shr:1 row_mask:0xf bank_mask:0xf
s_nop 1
v_max_u32_dpp v231, v231, v231 row_shr:2 row_mask:0xf bank_mask:0xf
s_nop 1
v_max_u32_dpp v231, v231, v231 row_shr:4 row_mask:0xf bank_mask:0xf
s_nop 1
v_max_u32_dpp v231, v231, v231 row_shr:8 row_mask:0xf bank_mask:0xf
s_nop 1
v_max_u32_dpp v231, v231, v231 row_bcast:15 row_mask:0xa bank_mask:0xf
s_nop 1
v_max_u32_dpp v231, v231, v231 row_bcast:31 row_mask:0xc bank_mask:0xf
s_nop 0
v_readlane_b32 s82, v231, 63
s_nop 1
v_cmp_eq_u32 vcc, s82, v228
v_mov_b32 v230, s82
v_mov_b32 v224, 0
v_cndmask_b32 v228, v228, v229, vcc
s_nop 1
v_max_u32_dpp v228, v228, v228 row_shr:1 row_mask:0xf bank_mask:0xf
s_nop 1
v_max_u32_dpp v228, v228, v228 row_shr:2 row_mask:0xf bank_mask:0xf
s_nop 1
v_max_u32_dpp v228, v228, v228 row_shr:4 row_mask:0xf bank_mask:0xf
s_nop 1
v_max_u32_dpp v228, v228, v228 row_shr:8 row_mask:0xf bank_mask:0xf
s_nop 1
v_max_u32_dpp v228, v228, v228 row_bcast:15 row_mask:0xa bank_mask:0xf
s_nop 1
v_max_u32_dpp v228, v228, v228 row_bcast:31 row_mask:0xc bank_mask:0xf
s_nop 0
v_readlane_b32 s81, v228, 63
s_nop 1
v_mov_b32 v231, s81
ds_write_b64 v224, v[230:231] offset:64
.La5_LT_S2:
v_mov_b32_e64 v224, 0
wt lgkmcnt(0)
s_barrier
ds_read_b64 v[228:229], v224 offset:64
wt lgkmcnt(0)
v_readfirstlane_b32 s82, v228
v_readfirstlane_b32 s81, v229
s_nop 0
s_andn2_b32 s84, 0x7ff, s82
s_andn2_b32 s21, 0x7ff, s81
s_cmp_eq_u32 s30, 0
s_cbranch_scc1 .La5_LT_ST
s_cmp_lt_u32 s3, 2
s_cselect_b32 s91, s84, s21
s_cselect_b32 s85, s82, s81
s_lshr_b32 s92, s91, 4
s_lshl_b32 s92, s92, 15
s_and_b32 s93, s91, 15
s_lshl_b32 s93, s93, 6
s_add_u32 s92, s92, s93
s_add_u32 s93, s92, 4194304
s_add_u32 s94, s92, 8388608
s_add_u32 s95, s92, 12582912
ld v[224:227],v240,s[32:35],s92 offen sc1
ld v[228:231],v240,s[32:35],s93 offen sc1
ld v[232:235],v240,s[32:35],s94 offen sc1
ld v[244:247],v240,s[32:35],s95 offen sc1
s_lshl_b32 s96, s91, 11
s_and_b32 s92, s91, 0xf8
s_lshl4_add_u32 s97, s92, s97
s_lshr_b32 s92, s91, 8
s_lshl4_add_u32 s97, s92, s97
ld v[248:251],v241,s[68:71],s96 offen
ld v[220:223],off,s[44:47],s97 sc1
wt vmcnt(4)
v_lshlrev_b32 v236, 16, v224
v_and_b32 v237, 0xffff0000, v224
v_lshlrev_b32 v238, 16, v228
v_and_b32 v239, 0xffff0000, v228
v_pk_add_f32 v[236:237], v[236:237], v[238:239]
v_lshlrev_b32 v242, 16, v225
v_and_b32 v243, 0xffff0000, v225
v_lshlrev_b32 v238, 16, v229
v_and_b32 v239, 0xffff0000, v229
v_pk_add_f32 v[242:243], v[242:243], v[238:239]
v_lshlrev_b32 v252, 16, v226
v_and_b32 v253, 0xffff0000, v226
v_lshlrev_b32 v238, 16, v230
v_and_b32 v239, 0xffff0000, v230
v_pk_add_f32 v[252:253], v[252:253], v[238:239]
v_lshlrev_b32 v214, 16, v227
v_and_b32 v215, 0xffff0000, v227
v_lshlrev_b32 v238, 16, v231
v_and_b32 v239, 0xffff0000, v231
v_pk_add_f32 v[214:215], v[214:215], v[238:239]
wt vmcnt(2)
v_lshlrev_b32 v224, 16, v232
v_and_b32 v225, 0xffff0000, v232
v_lshlrev_b32 v226, 16, v244
v_and_b32 v227, 0xffff0000, v244
v_pk_add_f32 v[224:225], v[224:225], v[226:227]
v_pk_add_f32 v[236:237], v[236:237], v[224:225]
v_lshlrev_b32 v224, 16, v233
v_and_b32 v225, 0xffff0000, v233
v_lshlrev_b32 v226, 16, v245
v_and_b32 v227, 0xffff0000, v245
v_pk_add_f32 v[224:225], v[224:225], v[226:227]
v_pk_add_f32 v[242:243], v[242:243], v[224:225]
v_lshlrev_b32 v224, 16, v234
v_and_b32 v225, 0xffff0000, v234
v_lshlrev_b32 v226, 16, v246
v_and_b32 v227, 0xffff0000, v246
v_pk_add_f32 v[224:225], v[224:225], v[226:227]
v_pk_add_f32 v[252:253], v[252:253], v[224:225]
v_lshlrev_b32 v224, 16, v235
v_and_b32 v225, 0xffff0000, v235
v_lshlrev_b32 v226, 16, v247
v_and_b32 v227, 0xffff0000, v247
v_pk_add_f32 v[224:225], v[224:225], v[226:227]
v_pk_add_f32 v[214:215], v[214:215], v[224:225]
wt vmcnt(0)
v_cmp_eq_u32 vcc, s85, v220
s_nop 1
v_cndmask_b32 v220, v223, v221, vcc
v_lshlrev_b32 v222, 16, v220
.La5_LW4_2:
s_cbranch_execz .La5_LW4_R0
v_rcp_f32 v222, v222
v_and_b32 v220, 0xffff0000, v220
v_mul_f32 v220, v220, v222
v_mov_b32 v223, v222
v_xor_b32 v220, 0x80000000, v220
v_mov_b32 v221, v220
v_lshlrev_b32 v224, 16, v248
v_and_b32 v225, 0xffff0000, v248
v_pk_add_f32 v[236:237], v[236:237], v[224:225]
cv v224,v236,v237
v_lshlrev_b32 v226, 16, v224
v_and_b32 v227, 0xffff0000, v224
v_pk_fma_f32 v[226:227], v[226:227], v[222:223], v[220:221]
cv v228,v226,v227
v_lshlrev_b32 v224, 16, v249
v_and_b32 v225, 0xffff0000, v249
v_pk_add_f32 v[242:243], v[242:243], v[224:225]
cv v224,v242,v243
v_lshlrev_b32 v226, 16, v224
v_and_b32 v227, 0xffff0000, v224
v_pk_fma_f32 v[226:227], v[226:227], v[222:223], v[220:221]
cv v229,v226,v227
v_lshlrev_b32 v224, 16, v250
v_and_b32 v225, 0xffff0000, v250
v_pk_add_f32 v[252:253], v[252:253], v[224:225]
cv v224,v252,v253
v_lshlrev_b32 v226, 16, v224
v_and_b32 v227, 0xffff0000, v224
v_pk_fma_f32 v[226:227], v[226:227], v[222:223], v[220:221]
cv v230,v226,v227
v_lshlrev_b32 v224, 16, v251
v_and_b32 v225, 0xffff0000, v251
v_pk_add_f32 v[214:215], v[214:215], v[224:225]
cv v224,v214,v215
v_lshlrev_b32 v226, 16, v224
v_and_b32 v227, 0xffff0000, v224
v_pk_fma_f32 v[226:227], v[226:227], v[222:223], v[220:221]
cv v231,v226,v227
ds_write_b128 v208, v[228:231] offset:1024
wt lgkmcnt(0)
s_barrier
rd v[224:227],v209 offset:1024
rd v[232:235],v209 offset:1040
rd v[244:247],v209 offset:2048
rd v[248:251],v209 offset:2064
wt lgkmcnt(3)
mm a[0:3],v[224:227],a[96:99],0
mm a[4:7],v[224:227],a[100:103],0
mm a[8:11],v[224:227],a[104:107],0
mm a[12:15],v[224:227],a[108:111],0
mm a[16:19],v[224:227],a[112:115],0
mm a[20:23],v[224:227],a[116:119],0
mm a[24:27],v[224:227],a[120:123],0
mm a[28:31],v[224:227],a[124:127],0
wt lgkmcnt(2)
mm a[0:3],v[232:235],a[128:131],a[0:3]
mm a[4:7],v[232:235],a[132:135],a[4:7]
mm a[8:11],v[232:235],a[136:139],a[8:11]
mm a[12:15],v[232:235],a[140:143],a[12:15]
mm a[16:19],v[232:235],a[144:147],a[16:19]
mm a[20:23],v[232:235],a[148:151],a[20:23]
mm a[24:27],v[232:235],a[152:155],a[24:27]
mm a[28:31],v[232:235],a[156:159],a[28:31]
wt lgkmcnt(1)
mm a[0:3],v[244:247],a[160:163],a[0:3]
mm a[4:7],v[244:247],a[164:167],a[4:7]
mm a[8:11],v[244:247],a[168:171],a[8:11]
mm a[12:15],v[244:247],a[172:175],a[12:15]
mm a[16:19],v[244:247],a[176:179],a[16:19]
mm a[20:23],v[244:247],a[180:183],a[20:23]
mm a[24:27],v[244:247],a[184:187],a[24:27]
mm a[28:31],v[244:247],a[188:191],a[28:31]
wt lgkmcnt(0)
mm a[0:3],v[248:251],a[192:195],a[0:3]
mm a[4:7],v[248:251],a[196:199],a[4:7]
mm a[8:11],v[248:251],a[200:203],a[8:11]
mm a[12:15],v[248:251],a[204:207],a[12:15]
mm a[16:19],v[248:251],a[208:211],a[16:19]
mm a[20:23],v[248:251],a[212:215],a[20:23]
mm a[24:27],v[248:251],a[216:219],a[24:27]
mm a[28:31],v[248:251],a[220:223],a[28:31]
s_mov_b64 exec, s[98:99]
ds_write_b64 v210, a[0:1] offset:5120
ds_write_b64 v210, a[4:5] offset:5200
ds_write_b64 v210, a[8:9] offset:5280
ds_write_b64 v210, a[12:13] offset:5360
ds_write_b64 v210, a[16:17] offset:5440
s_nop 3
ds_write_b64 v210, a[20:21] offset:5520
s_nop 3
ds_write_b64 v210, a[24:25] offset:5600
s_nop 3
ds_write_b64 v210, a[28:29] offset:5680
s_mov_b64 exec, -1
wt lgkmcnt(0)
s_barrier
rd v[224:227],v211 offset:5120
rd v[232:235],v211 offset:5136
rd v[244:247],v211 offset:5152
rd v[248:251],v211 offset:5168
wt lgkmcnt(2)
v_pk_add_f32 v[224:225], v[224:225], v[226:227]
v_pk_add_f32 v[232:233], v[232:233], v[234:235]
wt lgkmcnt(0)
v_pk_add_f32 v[244:245], v[244:245], v[246:247]
v_pk_add_f32 v[248:249], v[248:249], v[250:251]
v_pk_add_f32 v[224:225], v[224:225], v[232:233]
v_pk_add_f32 v[244:245], v[244:245], v[248:249]
v_pk_add_f32 v[224:225], v[224:225], v[244:245]
cv v224,v224,v225
v_mov_b32 v226, v224
s_nop 1
v_permlane32_swap_b32 v224, v226
s_nop 1
v_lshlrev_b32 v228, 16, v224
v_and_b32 v229, 0xffff0000, v224
v_lshlrev_b32 v232, 16, v226
v_and_b32 v233, 0xffff0000, v226
v_mul_f32 v230, 0xbfb8aa3b, v228
v_mul_f32 v231, 0xbfb8aa3b, v229
ex v230,v230
ex v231,v231
v_add_f32 v230, 1.0, v230
v_add_f32 v231, 1.0, v231
v_rcp_f32 v230, v230
v_rcp_f32 v231, v231
s_nop 0
v_pk_mul_f32 v[228:229], v[228:229], v[230:231]
cv v228,v228,v229
v_lshlrev_b32 v230, 16, v228
v_and_b32 v231, 0xffff0000, v228
v_pk_mul_f32 v[230:231], v[230:231], v[232:233]
cv v228,v230,v231
ds_write_b16 v212, v228 offset:10240
ds_write_b16_d16_hi v212, v228 offset:10304
wt lgkmcnt(0)
rd v[224:227],v216 offset:10240
rd v[232:235],v217 offset:10240
rd v[244:247],v218 offset:10240
rd v[248:251],v219 offset:10240
wt lgkmcnt(3)
mm v[0:3],v[224:227],v[128:131],0
mm v[4:7],v[224:227],v[132:135],0
mm v[8:11],v[224:227],v[136:139],0
mm v[12:15],v[224:227],v[140:143],0
wt lgkmcnt(2)
mm v[0:3],v[232:235],v[144:147],v[0:3]
mm v[4:7],v[232:235],v[148:151],v[4:7]
mm v[8:11],v[232:235],v[152:155],v[8:11]
mm v[12:15],v[232:235],v[156:159],v[12:15]
wt lgkmcnt(1)
mm v[0:3],v[244:247],v[160:163],v[0:3]
mm v[4:7],v[244:247],v[164:167],v[4:7]
mm v[8:11],v[244:247],v[168:171],v[8:11]
mm v[12:15],v[244:247],v[172:175],v[12:15]
wt lgkmcnt(0)
mm v[0:3],v[248:251],v[176:179],v[0:3]
mm v[4:7],v[248:251],v[180:183],v[4:7]
mm v[8:11],v[248:251],v[184:187],v[8:11]
mm v[12:15],v[248:251],v[188:191],v[12:15]
s_nop 7
v_or_b32 v16, 1, v0
v_or_b32 v20, 1, v1
v_or_b32 v17, 1, v4
v_or_b32 v21, 1, v5
v_or_b32 v18, 1, v8
v_or_b32 v22, 1, v9
v_or_b32 v19, 1, v12
v_or_b32 v23, 1, v13
st v[16:19],v208,s[44:47],s80 offen sc1
st v[20:23],v208,s[44:47],s20 offen sc1
; ---- rows that were not routed leave; final sums of the routed rows
.La5_LT_ST:
.La5_LW4_3:
s_cbranch_execz .La5_LW4_R0
s_lshl_b32 s92, s76, 5
s_add_u32 s92, s92, s90
s_lshl2_add_u32 s92, s92, s3
s_lshl_b32 s93, s92, 1
v_bfe_u32 v230, v255, 6, 1
v_add_u32 v231, s93, v230
v_cmp_ne_u32 vcc, s84, v231
v_cmp_ne_u32 s[94:95], s21, v231
v_lshrrev_b32 v232, 7, v255
v_and_b32 v233, 48, v255
v_lshl_add_u32 v232, v232, 6, v233
v_lshl_add_u32 v232, v230, 11, v232
s_lshl_b32 s92, s92, 12
s_and_b64 exec, vcc, s[94:95]
s_cbranch_scc0 .La5_LT_F0
st v[192:195],v232,s[56:59],s92 offen offset:0 sc1
st v[196:199],v232,s[56:59],s92 offen offset:512 sc1
st v[200:203],v232,s[56:59],s92 offen offset:1024 sc1
st v[204:207],v232,s[56:59],s92 offen offset:1536 sc1
.La5_LT_F0:
s_mov_b64 exec, -1
s_cmp_eq_u32 s75, 1
s_nop 0
s_nop 0
s_cbranch_scc1 .La5_LT_END
s_lshr_b32 s92, s2, 6
s_lshl2_add_u32 s92, s92, s74
s_and_b32 s93, s2, 7
s_lshl3_add_u32 s91, s92, s93
s_cmp_lt_u32 s91, 64
s_cselect_b32 s84, s84, s21
s_cselect_b32 s82, s82, s81
s_bfe_u32 s92, s91, 0x30003
s_and_b32 s93, s91, 3
s_lshl2_add_u32 s96, s92, s93
s_and_b32 s85, s91, 4
s_lshl_b32 s85, s85, 1
v_bfe_u32 v0, v255, 6, 2
v_bfe_u32 v1, v255, 4, 2
v_lshlrev_b32 v1, 1, v1
v_lshl_add_u32 v0, v0, 4, v1
v_add_u32 v0, s85, v0
s_lshl_b32 s85, s96, 10
v_add_u32 v1, s85, v0
s_lshl_b32 s85, s96, 6
v_add_u32 v0, s85, v0
s_lshr_b32 s92, s84, 4
s_lshl_b32 s92, s92, 15
s_and_b32 s93, s84, 15
s_lshl_b32 s93, s93, 6
s_add_u32 s92, s92, s93
s_add_u32 s93, s92, 4194304
s_add_u32 s94, s92, 8388608
s_add_u32 s95, s92, 12582912
buffer_load_ushort v4, v1, s[32:35], s92 offen sc1
buffer_load_ushort v5, v1, s[32:35], s93 offen sc1
buffer_load_ushort v6, v1, s[32:35], s94 offen sc1
buffer_load_ushort v7, v1, s[32:35], s95 offen sc1
s_lshl_b32 s21, s84, 11
s_and_b32 s92, s84, 0xf8
s_lshl4_add_u32 s97, s92, s97
s_lshr_b32 s92, s84, 8
s_lshl4_add_u32 s97, s92, s97
buffer_load_ushort v8, v0, s[68:71], s21 offen
ld v[12:15],off,s[44:47],s97 sc1
v_lshrrev_b32 v2, 6, v255
v_lshlrev_b32 v2, 13, v2
v_and_b32 v3, 48, v255
v_add_u32 v2, v2, v3
s_lshl_b32 s92, s3, 17
s_lshl_b32 s93, s91, 6
s_add_u32 s92, s92, s93
s_add_u32 s94, s92, 32768
s_cmp_eq_u32 s3, 3
s_cselect_b32 s95, 0, 524288
s_cselect_b32 s96, 0, -1
s_add_u32 s95, s95, s94
s_ashr_i32 s92, s82, 31
s_orn2_b32 s92, 0x80000000, s92
s_xor_b32 s92, s92, s82
s_and_b32 s92, s92, 0xffff0000
v_mov_b32 v9, 0xbfb8aa3b
v_mul_f32 v9, s92, v9
ex v9,v9
v_lshrrev_b32 v3, 8, v255
s_lshl_b32 s92, s3, 4
v_add_f32 v9, 1.0, v9
v_lshl_add_u32 v3, v3, 2, s92
v_rcp_f32 v9, v9
v_and_b32 v10, 48, v255
v_mul_u32_u24 v10, 20, v10
v_add_u32 v3, v3, v10
cv v9,v9,v9
v_lshlrev_b32 v9, 16, v9
v_and_b32 v10, 0xf0, v255
v_mul_u32_u24 v10, 5, v10
wt vmcnt(0)
v_cmp_eq_u32 vcc, s82, v12
s_nop 1
v_cndmask_b32 v12, v15, v13, vcc
v_lshlrev_b32 v14, 16, v12
v_rcp_f32 v14, v14
v_and_b32 v12, 0xffff0000, v12
v_mul_f32 v12, v12, v14
v_mov_b32 v15, v14
v_xor_b32 v12, 0x80000000, v12
v_mov_b32 v13, v12
v_lshlrev_b32 v4, 16, v4
v_lshlrev_b32 v5, 16, v5
v_lshlrev_b32 v6, 16, v6
v_lshlrev_b32 v7, 16, v7
v_lshlrev_b32 v8, 16, v8
v_add_f32 v4, v4, v5
v_add_f32 v6, v6, v7
v_add_f32 v4, v4, v6
v_add_f32 v4, v4, v8
cv v4,v4,v4
v_lshlrev_b32 v4, 16, v4
v_fma_f32 v5, v4, v14, v12
cv v5,v5,v5
v_lshlrev_b32 v5, 16, v5
s_sleep 54
s_mov_b32 s93, 0
.La5_LT_Y:
ld v[16:19],v2,s[44:47],s94 offen sc1
ld v[20:23],v2,s[44:47],s95 offen sc1
s_add_u32 s93, s93, 1
s_lshr_b32 s92, s93, 20
wt vmcnt(0)
v_min3_u32 v24, v16, v17, v18
v_min3_u32 v25, v20, v21, v22
v_min3_u32 v24, v24, v19, v23
v_min_u32 v24, v24, v25
v_cmp_ne_u32 vcc, 0, v24
v_and_b32 v20, s96, v20
v_and_b32 v21, s96, v21
v_and_b32 v22, s96, v22
v_and_b32 v23, s96, v23
s_cbranch_scc1 .La5_LT_Y1
s_cmp_eq_u64 vcc, -1
s_cbranch_scc0 .La5_LT_Y
.La5_LT_Y1:
v_pk_add_f32 v[16:17], v[16:17], v[20:21]
v_pk_add_f32 v[18:19], v[18:19], v[22:23]
s_nop 0
v_add_f32_dpp v16, v16, v16 row_ror:4 row_mask:0xf bank_mask:0xf
v_add_f32_dpp v17, v17, v17 row_ror:4 row_mask:0xf bank_mask:0xf
v_add_f32_dpp v18, v18, v18 row_ror:4 row_mask:0xf bank_mask:0xf
v_add_f32_dpp v19, v19, v19 row_ror:4 row_mask:0xf bank_mask:0xf
v_add_f32_dpp v16, v16, v16 row_ror:8 row_mask:0xf bank_mask:0xf
v_add_f32_dpp v17, v17, v17 row_ror:8 row_mask:0xf bank_mask:0xf
v_add_f32_dpp v18, v18, v18 row_ror:8 row_mask:0xf bank_mask:0xf
v_add_f32_dpp v19, v19, v19 row_ror:8 row_mask:0xf bank_mask:0xf
ds_write_b32 v3, v16 offset:5120
ds_write_b32 v3, v17 offset:5200
ds_write_b32 v3, v18 offset:5280
ds_write_b32 v3, v19 offset:5360
wt lgkmcnt(0)
s_barrier
rd v[16:19],v10 offset:5120
rd v[20:23],v10 offset:5136
rd v[24:27],v10 offset:5152
rd v[28:31],v10 offset:5168
wt lgkmcnt(2)
v_pk_add_f32 v[16:17], v[16:17], v[18:19]
v_pk_add_f32 v[20:21], v[20:21], v[22:23]
wt lgkmcnt(0)
v_pk_add_f32 v[24:25], v[24:25], v[26:27]
v_pk_add_f32 v[28:29], v[28:29], v[30:31]
v_pk_add_f32 v[16:17], v[16:17], v[20:21]
v_pk_add_f32 v[24:25], v[24:25], v[28:29]
v_pk_add_f32 v[16:17], v[16:17], v[24:25]
v_add_f32 v16, v16, v17
cv v16,v16,v16
v_lshlrev_b32 v16, 16, v16
v_mul_f32 v16, v16, v9
cv v16,v16,v16
v_lshlrev_b32 v16, 16, v16
v_add_f32 v16, v16, v5
cv v16,v16,v16
v_lshlrev_b32 v16, 16, v16
v_add_f32 v16, v16, v4
cv v16,v16,v16
s_cmp_lg_u32 s3, 0
s_cbranch_scc1 .La5_LT_END
s_mov_b64 exec, 0xffff
buffer_store_short v16, v0, s[56:59], s21 offen
s_mov_b64 exec, -1
.La5_LT_END:
.La5_LW4_4:
s_cbranch_execz .La5_LW4_R0
s_lshl2_add_u32 s92, s2, s3
s_cmp_lg_u32 s92, 992
s_cbranch_scc1 .La5_LT_E1
buffer_store_dwordx2 v[252:253], off, s[64:67], 0 sc1
.La5_LT_E1:
s_branch .La5_LEND
; ---- fallback: the earlier 32 rows against all their keys with the true running maximum (online softmax)
.La5_LA_SLOW0:
wt lgkmcnt(0)
s_nop 7
s_nop 7
aw a160,v192
aw a161,v193
aw a162,v194
aw a163,v195
aw a164,v196
aw a165,v197
aw a166,v198
aw a167,v199
aw a168,v200
aw a169,v201
aw a170,v202
aw a171,v203
aw a172,v204
aw a173,v205
aw a174,v206
aw a175,v207
aw a176,v208
aw a177,v209
aw a178,v210
aw a179,v211
aw a180,v212
aw a181,v213
aw a182,v214
aw a183,v215
aw a184,v216
aw a185,v217
aw a186,v218
aw a187,v219
aw a188,v220
aw a189,v221
aw a190,v222
aw a191,v223
s_lshl_b32 s96, s72, 19
s_add_u32 s96, s96, 20971520
s_add_u32 s92, s26, s96
s_addc_u32 s93, s27, 0
s_and_b32 s93, s93, 0xffff
s_mov_b32 s94, 17301504
s_mov_b32 s95, 0x20000
v_mov_b32 v0, 0
v_mov_b32 v1, 0
v_mov_b32 v2, 0
v_mov_b32 v3, 0
v_mov_b32 v4, 0
v_mov_b32 v5, 0
v_mov_b32 v6, 0
v_mov_b32 v7, 0
v_mov_b32 v8, 0
v_mov_b32 v9, 0
v_mov_b32 v10, 0
v_mov_b32 v11, 0
v_mov_b32 v12, 0
v_mov_b32 v13, 0
v_mov_b32 v14, 0
v_mov_b32 v15, 0
v_mov_b32 v16, 0
v_mov_b32 v17, 0
v_mov_b32 v18, 0
v_mov_b32 v19, 0
v_mov_b32 v20, 0
v_mov_b32 v21, 0
v_mov_b32 v22, 0
v_mov_b32 v23, 0
v_mov_b32 v24, 0
v_mov_b32 v25, 0
v_mov_b32 v26, 0
v_mov_b32 v27, 0
v_mov_b32 v28, 0
v_mov_b32 v29, 0
v_mov_b32 v30, 0
v_mov_b32 v31, 0
v_mov_b32 v32, 0
v_mov_b32 v33, 0
v_mov_b32 v34, 0
v_mov_b32 v35, 0
v_mov_b32 v36, 0
v_mov_b32 v37, 0
v_mov_b32 v38, 0
v_mov_b32 v39, 0
v_mov_b32 v40, 0
v_mov_b32 v41, 0
v_mov_b32 v42, 0
v_mov_b32 v43, 0
v_mov_b32 v44, 0
v_mov_b32 v45, 0
v_mov_b32 v46, 0
v_mov_b32 v47, 0
v_mov_b32 v48, 0
v_mov_b32 v49, 0
v_mov_b32 v50, 0
v_mov_b32 v51, 0
v_mov_b32 v52, 0
v_mov_b32 v53, 0
v_mov_b32 v54, 0
v_mov_b32 v55, 0
v_mov_b32 v56, 0
v_mov_b32 v57, 0
v_mov_b32 v58, 0
v_mov_b32 v59, 0
v_mov_b32 v60, 0
v_mov_b32 v61, 0
v_mov_b32 v62, 0
v_mov_b32 v63, 0
v_mov_b32 v216, 0xff61b1e6
v_mov_b32 v217, 0xff61b1e6
v_mov_b32 v218, 0
v_mov_b32 v219, 0
s_mov_b32 s7, 0
.La5_LA_SLOW0_L:
s_lshl_b32 s96, s7, 13
s_add_u32 s97, s96, 4096
ld a[128:131],v255,s[92:95],s96 offen offset:0 sc1
ld a[132:135],v255,s[92:95],s96 offen offset:1024 sc1
ld a[136:139],v255,s[92:95],s96 offen offset:2048 sc1
ld a[140:143],v255,s[92:95],s96 offen offset:3072 sc1
ld a[144:147],v255,s[92:95],s97 offen offset:0 sc1
ld a[148:151],v255,s[92:95],s97 offen offset:1024 sc1
ld a[152:155],v255,s[92:95],s97 offen offset:2048 sc1
ld a[156:159],v255,s[92:95],s97 offen offset:3072 sc1
s_lshl_b32 s96, s76, 3
s_sub_u32 s96, s96, s7
s_add_u32 s96, s96, s3
s_lshl_b32 s96, s96, 5
v_bfe_u32 v220, v255, 4, 4
v_lshrrev_b32 v221, 8, v255
v_lshlrev_b32 v221, 2, v221
v_sub_u32 v220, v220, v221
v_add_u32 v220, s96, v220
v_mov_b32 v222, 0xceabfb8f
wt vmcnt(0)
mm v[192:195],a[128:131],v[128:131],0
mm v[192:195],a[132:135],v[132:135],v[192:195]
mm v[192:195],a[136:139],v[136:139],v[192:195]
mm v[192:195],a[140:143],v[140:143],v[192:195]
mm v[196:199],a[144:147],v[128:131],0
mm v[196:199],a[148:151],v[132:135],v[196:199]
mm v[196:199],a[152:155],v[136:139],v[196:199]
mm v[196:199],a[156:159],v[140:143],v[196:199]
mm v[200:203],a[128:131],v[144:147],0
mm v[200:203],a[132:135],v[148:151],v[200:203]
mm v[200:203],a[136:139],v[152:155],v[200:203]
mm v[200:203],a[140:143],v[156:159],v[200:203]
mm v[204:207],a[144:147],v[144:147],0
mm v[204:207],a[148:151],v[148:151],v[204:207]
mm v[204:207],a[152:155],v[152:155],v[204:207]
mm v[204:207],a[156:159],v[156:159],v[204:207]
s_nop 7
s_nop 7
s_lshl_b32 s96, s7, 13
s_add_u32 s96, s96, 16777216
s_add_u32 s97, s96, 4096
ld a[128:131],v255,s[92:95],s96 offen offset:0 sc1
ld a[132:135],v255,s[92:95],s96 offen offset:1024 sc1
ld a[136:139],v255,s[92:95],s96 offen offset:2048 sc1
ld a[140:143],v255,s[92:95],s96 offen offset:3072 sc1
ld a[144:147],v255,s[92:95],s97 offen offset:0 sc1
ld a[148:151],v255,s[92:95],s97 offen offset:1024 sc1
ld a[152:155],v255,s[92:95],s97 offen offset:2048 sc1
ld a[156:159],v255,s[92:95],s97 offen offset:3072 sc1
v_cmp_gt_i32 vcc, 0, v220
s_nop 1
v_cndmask_b32 v192, v192, v222, vcc
v_cmp_gt_i32 vcc, 1, v220
s_nop 1
v_cndmask_b32 v193, v193, v222, vcc
v_cmp_gt_i32 vcc, 2, v220
s_nop 1
v_cndmask_b32 v194, v194, v222, vcc
v_cmp_gt_i32 vcc, 3, v220
s_nop 1
v_cndmask_b32 v195, v195, v222, vcc
v_cmp_gt_i32 vcc, 16, v220
s_nop 1
v_cndmask_b32 v196, v196, v222, vcc
v_cmp_gt_i32 vcc, 17, v220
s_nop 1
v_cndmask_b32 v197, v197, v222, vcc
v_cmp_gt_i32 vcc, 18, v220
s_nop 1
v_cndmask_b32 v198, v198, v222, vcc
v_cmp_gt_i32 vcc, 19, v220
s_nop 1
v_cndmask_b32 v199, v199, v222, vcc
v_cmp_gt_i32 vcc, -16, v220
s_nop 1
v_cndmask_b32 v200, v200, v222, vcc
v_cmp_gt_i32 vcc, -15, v220
s_nop 1
v_cndmask_b32 v201, v201, v222, vcc
v_cmp_gt_i32 vcc, -14, v220
s_nop 1
v_cndmask_b32 v202, v202, v222, vcc
v_cmp_gt_i32 vcc, -13, v220
s_nop 1
v_cndmask_b32 v203, v203, v222, vcc
v_cmp_gt_i32 vcc, 0, v220
s_nop 1
v_cndmask_b32 v204, v204, v222, vcc
v_cmp_gt_i32 vcc, 1, v220
s_nop 1
v_cndmask_b32 v205, v205, v222, vcc
v_cmp_gt_i32 vcc, 2, v220
s_nop 1
v_cndmask_b32 v206, v206, v222, vcc
v_cmp_gt_i32 vcc, 3, v220
s_nop 1
v_cndmask_b32 v207, v207, v222, vcc
v_max3_f32 v222, v192, v193, v216
v_max3_f32 v222, v194, v195, v222
v_max3_f32 v222, v196, v197, v222
v_max3_f32 v222, v198, v199, v222
v_mov_b32 v221, v222
s_nop 1
v_permlane16_swap_b32 v221, v222
s_nop 1
v_max_f32 v222, v222, v221
v_mov_b32 v221, v222
s_nop 1
v_permlane32_swap_b32 v221, v222
s_nop 1
v_max_f32 v222, v222, v221
v_sub_f32 v223, v216, v222
v_mov_b32 v216, v222
ex v223,v223
v_sub_f32 v192, v192, v222
v_sub_f32 v193, v193, v222
v_sub_f32 v194, v194, v222
v_sub_f32 v195, v195, v222
v_sub_f32 v196, v196, v222
v_sub_f32 v197, v197, v222
v_sub_f32 v198, v198, v222
v_sub_f32 v199, v199, v222
ex v192,v192
ex v193,v193
ex v194,v194
ex v195,v195
ex v196,v196
ex v197,v197
ex v198,v198
ex v199,v199
v_mul_f32 v218, v218, v223
v_add_f32 v218, v218, v192
v_add_f32 v218, v218, v193
v_add_f32 v218, v218, v194
v_add_f32 v218, v218, v195
v_add_f32 v218, v218, v196
v_add_f32 v218, v218, v197
v_add_f32 v218, v218, v198
v_add_f32 v218, v218, v199
cv v208,v192,v193
cv v209,v194,v195
cv v210,v196,v197
cv v211,v198,v199
v_mul_f32 v0, v0, v223
v_mul_f32 v1, v1, v223
v_mul_f32 v2, v2, v223
v_mul_f32 v3, v3, v223
v_mul_f32 v4, v4, v223
v_mul_f32 v5, v5, v223
v_mul_f32 v6, v6, v223
v_mul_f32 v7, v7, v223
v_mul_f32 v8, v8, v223
v_mul_f32 v9, v9, v223
v_mul_f32 v10, v10, v223
v_mul_f32 v11, v11, v223
v_mul_f32 v12, v12, v223
v_mul_f32 v13, v13, v223
v_mul_f32 v14, v14, v223
v_mul_f32 v15, v15, v223
v_mul_f32 v16, v16, v223
v_mul_f32 v17, v17, v223
v_mul_f32 v18, v18, v223
v_mul_f32 v19, v19, v223
v_mul_f32 v20, v20, v223
v_mul_f32 v21, v21, v223
v_mul_f32 v22, v22, v223
v_mul_f32 v23, v23, v223
v_mul_f32 v24, v24, v223
v_mul_f32 v25, v25, v223
v_mul_f32 v26, v26, v223
v_mul_f32 v27, v27, v223
v_mul_f32 v28, v28, v223
v_mul_f32 v29, v29, v223
v_mul_f32 v30, v30, v223
v_mul_f32 v31, v31, v223
v_max3_f32 v222, v200, v201, v217
v_max3_f32 v222, v202, v203, v222
v_max3_f32 v222, v204, v205, v222
v_max3_f32 v222, v206, v207, v222
v_mov_b32 v221, v222
s_nop 1
v_permlane16_swap_b32 v221, v222
s_nop 1
v_max_f32 v222, v222, v221
v_mov_b32 v221, v222
s_nop 1
v_permlane32_swap_b32 v221, v222
s_nop 1
v_max_f32 v222, v222, v221
v_sub_f32 v223, v217, v222
v_mov_b32 v217, v222
ex v223,v223
v_sub_f32 v200, v200, v222
v_sub_f32 v201, v201, v222
v_sub_f32 v202, v202, v222
v_sub_f32 v203, v203, v222
v_sub_f32 v204, v204, v222
v_sub_f32 v205, v205, v222
v_sub_f32 v206, v206, v222
v_sub_f32 v207, v207, v222
ex v200,v200
ex v201,v201
ex v202,v202
ex v203,v203
ex v204,v204
ex v205,v205
ex v206,v206
ex v207,v207
v_mul_f32 v219, v219, v223
v_add_f32 v219, v219, v200
v_add_f32 v219, v219, v201
v_add_f32 v219, v219, v202
v_add_f32 v219, v219, v203
v_add_f32 v219, v219, v204
v_add_f32 v219, v219, v205
v_add_f32 v219, v219, v206
v_add_f32 v219, v219, v207
cv v212,v200,v201
cv v213,v202,v203
cv v214,v204,v205
cv v215,v206,v207
v_mul_f32 v32, v32, v223
v_mul_f32 v33, v33, v223
v_mul_f32 v34, v34, v223
v_mul_f32 v35, v35, v223
v_mul_f32 v36, v36, v223
v_mul_f32 v37, v37, v223
v_mul_f32 v38, v38, v223
v_mul_f32 v39, v39, v223
v_mul_f32 v40, v40, v223
v_mul_f32 v41, v41, v223
v_mul_f32 v42, v42, v223
v_mul_f32 v43, v43, v223
v_mul_f32 v44, v44, v223
v_mul_f32 v45, v45, v223
v_mul_f32 v46, v46, v223
v_mul_f32 v47, v47, v223
v_mul_f32 v48, v48, v223
v_mul_f32 v49, v49, v223
v_mul_f32 v50, v50, v223
v_mul_f32 v51, v51, v223
v_mul_f32 v52, v52, v223
v_mul_f32 v53, v53, v223
v_mul_f32 v54, v54, v223
v_mul_f32 v55, v55, v223
v_mul_f32 v56, v56, v223
v_mul_f32 v57, v57, v223
v_mul_f32 v58, v58, v223
v_mul_f32 v59, v59, v223
v_mul_f32 v60, v60, v223
v_mul_f32 v61, v61, v223
v_mul_f32 v62, v62, v223
v_mul_f32_e64 v63, v63, v223
wt vmcnt(0)
mm v[0:3],a[128:131],v[208:211],v[0:3]
mm v[32:35],a[128:131],v[212:215],v[32:35]
mm v[4:7],a[132:135],v[208:211],v[4:7]
mm v[36:39],a[132:135],v[212:215],v[36:39]
mm v[8:11],a[136:139],v[208:211],v[8:11]
mm v[40:43],a[136:139],v[212:215],v[40:43]
mm v[12:15],a[140:143],v[208:211],v[12:15]
mm v[44:47],a[140:143],v[212:215],v[44:47]
mm v[16:19],a[144:147],v[208:211],v[16:19]
mm v[48:51],a[144:147],v[212:215],v[48:51]
mm v[20:23],a[148:151],v[208:211],v[20:23]
mm v[52:55],a[148:151],v[212:215],v[52:55]
mm v[24:27],a[152:155],v[208:211],v[24:27]
mm v[56:59],a[152:155],v[212:215],v[56:59]
mm v[28:31],a[156:159],v[208:211],v[28:31]
mm v[60:63],a[156:159],v[212:215],v[60:63]
s_nop 7
s_nop 7
s_add_u32 s7, s7, 1
s_lshr_b32 s96, s7, 3
s_cmp_le_u32 s96, s76
s_cbranch_scc1 .La5_LA_SLOW0_L
v_mov_b32 v221, v218
s_nop 1
v_permlane16_swap_b32 v221, v218
s_nop 1
v_add_f32 v218, v218, v221
v_mov_b32 v221, v218
s_nop 1
v_permlane32_swap_b32 v221, v218
s_nop 1
v_add_f32 v218, v218, v221
v_mov_b32 v221, v219
s_nop 1
v_permlane16_swap_b32 v221, v219
s_nop 1
v_add_f32 v219, v219, v221
v_mov_b32 v221, v219
s_nop 1
v_permlane32_swap_b32 v221, v219
s_nop 1
v_add_f32 v219, v219, v221
v_rcp_f32 v242, v218
v_rcp_f32 v243, v219
ar v192,a160
ar v193,a161
ar v194,a162
ar v195,a163
ar v196,a164
ar v197,a165
ar v198,a166
ar v199,a167
ar v200,a168
ar v201,a169
ar v202,a170
ar v203,a171
ar v204,a172
ar v205,a173
ar v206,a174
ar v207,a175
ar v208,a176
ar v209,a177
ar v210,a178
ar v211,a179
ar v212,a180
ar v213,a181
ar v214,a182
ar v215,a183
ar v216,a184
ar v217,a185
ar v218,a186
ar v219,a187
ar v220,a188
ar v221,a189
ar v222,a190
ar v223,a191
s_mov_b32 s95, 4096
s_mov_b32 s96, 8192
s_mov_b32 s97, 12288
s_mov_b64 vcc, 0
s_nop 1
s_setpc_b64 s[24:25]
; ---- fallback: the later 32 rows
.La5_LA_SLOW1:
wt lgkmcnt(0)
s_nop 7
s_nop 7
aw a160,v192
aw a161,v193
aw a162,v194
aw a163,v195
aw a164,v196
aw a165,v197
aw a166,v198
aw a167,v199
aw a168,v200
aw a169,v201
aw a170,v202
aw a171,v203
aw a172,v204
aw a173,v205
aw a174,v206
aw a175,v207
aw a176,v208
aw a177,v209
aw a178,v210
aw a179,v211
aw a180,v212
aw a181,v213
aw a182,v214
aw a183,v215
aw a184,v216
aw a185,v217
aw a186,v218
aw a187,v219
aw a188,v220
aw a189,v221
aw a190,v222
aw a191,v223
s_lshl_b32 s96, s72, 19
s_add_u32 s96, s96, 20971520
s_add_u32 s92, s26, s96
s_addc_u32 s93, s27, 0
s_and_b32 s93, s93, 0xffff
s_mov_b32 s94, 17301504
s_mov_b32 s95, 0x20000
v_mov_b32 v64, 0
v_mov_b32 v65, 0
v_mov_b32 v66, 0
v_mov_b32 v67, 0
v_mov_b32 v68, 0
v_mov_b32 v69, 0
v_mov_b32 v70, 0
v_mov_b32 v71, 0
v_mov_b32 v72, 0
v_mov_b32 v73, 0
v_mov_b32 v74, 0
v_mov_b32 v75, 0
v_mov_b32 v76, 0
v_mov_b32 v77, 0
v_mov_b32 v78, 0
v_mov_b32 v79, 0
v_mov_b32 v80, 0
v_mov_b32 v81, 0
v_mov_b32 v82, 0
v_mov_b32 v83, 0
v_mov_b32 v84, 0
v_mov_b32 v85, 0
v_mov_b32 v86, 0
v_mov_b32 v87, 0
v_mov_b32 v88, 0
v_mov_b32 v89, 0
v_mov_b32 v90, 0
v_mov_b32 v91, 0
v_mov_b32 v92, 0
v_mov_b32 v93, 0
v_mov_b32 v94, 0
v_mov_b32 v95, 0
v_mov_b32 v96, 0
v_mov_b32 v97, 0
v_mov_b32 v98, 0
v_mov_b32 v99, 0
v_mov_b32 v100, 0
v_mov_b32 v101, 0
v_mov_b32 v102, 0
v_mov_b32 v103, 0
v_mov_b32 v104, 0
v_mov_b32 v105, 0
v_mov_b32 v106, 0
v_mov_b32 v107, 0
v_mov_b32 v108, 0
v_mov_b32 v109, 0
v_mov_b32 v110, 0
v_mov_b32 v111, 0
v_mov_b32 v112, 0
v_mov_b32 v113, 0
v_mov_b32 v114, 0
v_mov_b32 v115, 0
v_mov_b32 v116, 0
v_mov_b32 v117, 0
v_mov_b32 v118, 0
v_mov_b32 v119, 0
v_mov_b32 v120, 0
v_mov_b32 v121, 0
v_mov_b32 v122, 0
v_mov_b32 v123, 0
v_mov_b32 v124, 0
v_mov_b32 v125, 0
v_mov_b32 v126, 0
v_mov_b32 v127, 0
v_mov_b32 v216, 0xff61b1e6
v_mov_b32 v217, 0xff61b1e6
v_mov_b32 v218, 0
v_mov_b32 v219, 0
s_mov_b32 s7, 0
.La5_LA_SLOW1_L:
s_lshl_b32 s96, s7, 13
s_add_u32 s97, s96, 4096
ld a[128:131],v255,s[92:95],s96 offen offset:0 sc1
ld a[132:135],v255,s[92:95],s96 offen offset:1024 sc1
ld a[136:139],v255,s[92:95],s96 offen offset:2048 sc1
ld a[140:143],v255,s[92:95],s96 offen offset:3072 sc1
ld a[144:147],v255,s[92:95],s97 offen offset:0 sc1
ld a[148:151],v255,s[92:95],s97 offen offset:1024 sc1
ld a[152:155],v255,s[92:95],s97 offen offset:2048 sc1
ld a[156:159],v255,s[92:95],s97 offen offset:3072 sc1
s_lshl_b32 s96, s76, 3
s_sub_u32 s96, s96, s7
s_sub_u32 s96, s96, s3
s_add_u32 s96, s96, 7
s_lshl_b32 s96, s96, 5
v_bfe_u32 v220, v255, 4, 4
v_lshrrev_b32 v221, 8, v255
v_lshlrev_b32 v221, 2, v221
v_sub_u32 v220, v220, v221
v_add_u32 v220, s96, v220
v_mov_b32 v222, 0xceabfb8f
wt vmcnt(0)
mm v[192:195],a[128:131],v[160:163],0
mm v[192:195],a[132:135],v[164:167],v[192:195]
mm v[192:195],a[136:139],v[168:171],v[192:195]
mm v[192:195],a[140:143],v[172:175],v[192:195]
mm v[196:199],a[144:147],v[160:163],0
mm v[196:199],a[148:151],v[164:167],v[196:199]
mm v[196:199],a[152:155],v[168:171],v[196:199]
mm v[196:199],a[156:159],v[172:175],v[196:199]
mm v[200:203],a[128:131],v[176:179],0
mm v[200:203],a[132:135],v[180:183],v[200:203]
mm v[200:203],a[136:139],v[184:187],v[200:203]
mm v[200:203],a[140:143],v[188:191],v[200:203]
mm v[204:207],a[144:147],v[176:179],0
mm v[204:207],a[148:151],v[180:183],v[204:207]
mm v[204:207],a[152:155],v[184:187],v[204:207]
mm v[204:207],a[156:159],v[188:191],v[204:207]
s_nop 7
s_nop 7
s_lshl_b32 s96, s7, 13
s_add_u32 s96, s96, 16777216
s_add_u32 s97, s96, 4096
ld a[128:131],v255,s[92:95],s96 offen offset:0 sc1
ld a[132:135],v255,s[92:95],s96 offen offset:1024 sc1
ld a[136:139],v255,s[92:95],s96 offen offset:2048 sc1
ld a[140:143],v255,s[92:95],s96 offen offset:3072 sc1
ld a[144:147],v255,s[92:95],s97 offen offset:0 sc1
ld a[148:151],v255,s[92:95],s97 offen offset:1024 sc1
ld a[152:155],v255,s[92:95],s97 offen offset:2048 sc1
ld a[156:159],v255,s[92:95],s97 offen offset:3072 sc1
v_cmp_gt_i32 vcc, 0, v220
s_nop 1
v_cndmask_b32 v192, v192, v222, vcc
v_cmp_gt_i32 vcc, 1, v220
s_nop 1
v_cndmask_b32 v193, v193, v222, vcc
v_cmp_gt_i32 vcc, 2, v220
s_nop 1
v_cndmask_b32 v194, v194, v222, vcc
v_cmp_gt_i32 vcc, 3, v220
s_nop 1
v_cndmask_b32 v195, v195, v222, vcc
v_cmp_gt_i32 vcc, 16, v220
s_nop 1
v_cndmask_b32 v196, v196, v222, vcc
v_cmp_gt_i32 vcc, 17, v220
s_nop 1
v_cndmask_b32 v197, v197, v222, vcc
v_cmp_gt_i32 vcc, 18, v220
s_nop 1
v_cndmask_b32 v198, v198, v222, vcc
v_cmp_gt_i32 vcc, 19, v220
s_nop 1
v_cndmask_b32 v199, v199, v222, vcc
v_cmp_gt_i32 vcc, -16, v220
s_nop 1
v_cndmask_b32 v200, v200, v222, vcc
v_cmp_gt_i32 vcc, -15, v220
s_nop 1
v_cndmask_b32 v201, v201, v222, vcc
v_cmp_gt_i32 vcc, -14, v220
s_nop 1
v_cndmask_b32 v202, v202, v222, vcc
v_cmp_gt_i32 vcc, -13, v220
s_nop 1
v_cndmask_b32 v203, v203, v222, vcc
v_cmp_gt_i32 vcc, 0, v220
s_nop 1
v_cndmask_b32 v204, v204, v222, vcc
v_cmp_gt_i32 vcc, 1, v220
s_nop 1
v_cndmask_b32 v205, v205, v222, vcc
v_cmp_gt_i32 vcc, 2, v220
s_nop 1
v_cndmask_b32 v206, v206, v222, vcc
v_cmp_gt_i32 vcc, 3, v220
s_nop 1
v_cndmask_b32 v207, v207, v222, vcc
v_max3_f32 v222, v192, v193, v216
v_max3_f32 v222, v194, v195, v222
v_max3_f32 v222, v196, v197, v222
v_max3_f32 v222, v198, v199, v222
v_mov_b32 v221, v222
s_nop 1
v_permlane16_swap_b32 v221, v222
s_nop 1
v_max_f32 v222, v222, v221
v_mov_b32 v221, v222
s_nop 1
v_permlane32_swap_b32 v221, v222
s_nop 1
v_max_f32 v222, v222, v221
v_sub_f32 v223, v216, v222
v_mov_b32 v216, v222
ex v223,v223
v_sub_f32 v192, v192, v222
v_sub_f32 v193, v193, v222
v_sub_f32 v194, v194, v222
v_sub_f32 v195, v195, v222
v_sub_f32 v196, v196, v222
v_sub_f32 v197, v197, v222
v_sub_f32 v198, v198, v222
v_sub_f32 v199, v199, v222
ex v192,v192
ex v193,v193
ex v194,v194
ex v195,v195
ex v196,v196
ex v197,v197
ex v198,v198
ex v199,v199
v_mul_f32 v218, v218, v223
v_add_f32 v218, v218, v192
v_add_f32 v218, v218, v193
v_add_f32 v218, v218, v194
v_add_f32 v218, v218, v195
v_add_f32 v218, v218, v196
v_add_f32 v218, v218, v197
v_add_f32 v218, v218, v198
v_add_f32 v218, v218, v199
cv v208,v192,v193
cv v209,v194,v195
cv v210,v196,v197
cv v211,v198,v199
v_mul_f32 v64, v64, v223
v_mul_f32 v65, v65, v223
v_mul_f32 v66, v66, v223
v_mul_f32 v67, v67, v223
v_mul_f32 v68, v68, v223
v_mul_f32 v69, v69, v223
v_mul_f32 v70, v70, v223
v_mul_f32 v71, v71, v223
v_mul_f32 v72, v72, v223
v_mul_f32 v73, v73, v223
v_mul_f32 v74, v74, v223
v_mul_f32 v75, v75, v223
v_mul_f32 v76, v76, v223
v_mul_f32 v77, v77, v223
v_mul_f32 v78, v78, v223
v_mul_f32 v79, v79, v223
v_mul_f32 v80, v80, v223
v_mul_f32 v81, v81, v223
v_mul_f32 v82, v82, v223
v_mul_f32 v83, v83, v223
v_mul_f32 v84, v84, v223
v_mul_f32 v85, v85, v223
v_mul_f32 v86, v86, v223
v_mul_f32 v87, v87, v223
v_mul_f32 v88, v88, v223
v_mul_f32 v89, v89, v223
v_mul_f32 v90, v90, v223
v_mul_f32 v91, v91, v223
v_mul_f32 v92, v92, v223
v_mul_f32 v93, v93, v223
v_mul_f32 v94, v94, v223
v_mul_f32 v95, v95, v223
v_max3_f32 v222, v200, v201, v217
v_max3_f32 v222, v202, v203, v222
v_max3_f32 v222, v204, v205, v222
v_max3_f32 v222, v206, v207, v222
v_mov_b32 v221, v222
s_nop 1
v_permlane16_swap_b32 v221, v222
s_nop 1
v_max_f32 v222, v222, v221
v_mov_b32 v221, v222
s_nop 1
v_permlane32_swap_b32 v221, v222
s_nop 1
v_max_f32 v222, v222, v221
v_sub_f32 v223, v217, v222
v_mov_b32 v217, v222
ex v223,v223
v_sub_f32 v200, v200, v222
v_sub_f32 v201, v201, v222
v_sub_f32 v202, v202, v222
v_sub_f32 v203, v203, v222
v_sub_f32 v204, v204, v222
v_sub_f32 v205, v205, v222
v_sub_f32 v206, v206, v222
v_sub_f32 v207, v207, v222
ex v200,v200
ex v201,v201
ex v202,v202
ex v203,v203
ex v204,v204
ex v205,v205
ex v206,v206
ex v207,v207
v_mul_f32 v219, v219, v223
v_add_f32 v219, v219, v200
v_add_f32 v219, v219, v201
v_add_f32 v219, v219, v202
v_add_f32 v219, v219, v203
v_add_f32 v219, v219, v204
v_add_f32 v219, v219, v205
v_add_f32 v219, v219, v206
v_add_f32 v219, v219, v207
cv v212,v200,v201
cv v213,v202,v203
cv v214,v204,v205
cv v215,v206,v207
v_mul_f32 v96, v96, v223
v_mul_f32 v97, v97, v223
v_mul_f32 v98, v98, v223
v_mul_f32 v99, v99, v223
v_mul_f32 v100, v100, v223
v_mul_f32 v101, v101, v223
v_mul_f32 v102, v102, v223
v_mul_f32 v103, v103, v223
v_mul_f32 v104, v104, v223
v_mul_f32 v105, v105, v223
v_mul_f32 v106, v106, v223
v_mul_f32 v107, v107, v223
v_mul_f32 v108, v108, v223
v_mul_f32 v109, v109, v223
v_mul_f32 v110, v110, v223
v_mul_f32 v111, v111, v223
v_mul_f32 v112, v112, v223
v_mul_f32 v113, v113, v223
v_mul_f32 v114, v114, v223
v_mul_f32 v115, v115, v223
v_mul_f32 v116, v116, v223
v_mul_f32 v117, v117, v223
v_mul_f32 v118, v118, v223
v_mul_f32 v119, v119, v223
v_mul_f32 v120, v120, v223
v_mul_f32 v121, v121, v223
v_mul_f32 v122, v122, v223
v_mul_f32 v123, v123, v223
v_mul_f32 v124, v124, v223
v_mul_f32 v125, v125, v223
v_mul_f32 v126, v126, v223
v_mul_f32_e64 v127, v127, v223
wt vmcnt(0)
mm v[64:67],a[128:131],v[208:211],v[64:67]
mm v[96:99],a[128:131],v[212:215],v[96:99]
mm v[68:71],a[132:135],v[208:211],v[68:71]
mm v[100:103],a[132:135],v[212:215],v[100:103]
mm v[72:75],a[136:139],v[208:211],v[72:75]
mm v[104:107],a[136:139],v[212:215],v[104:107]
mm v[76:79],a[140:143],v[208:211],v[76:79]
mm v[108:111],a[140:143],v[212:215],v[108:111]
mm v[80:83],a[144:147],v[208:211],v[80:83]
mm v[112:115],a[144:147],v[212:215],v[112:115]
mm v[84:87],a[148:151],v[208:211],v[84:87]
mm v[116:119],a[148:151],v[212:215],v[116:119]
mm v[88:91],a[152:155],v[208:211],v[88:91]
mm v[120:123],a[152:155],v[212:215],v[120:123]
mm v[92:95],a[156:159],v[208:211],v[92:95]
mm v[124:127],a[156:159],v[212:215],v[124:127]
s_nop 7
s_nop 7
s_add_u32 s7, s7, 1
s_lshr_b32 s96, s7, 3
s_cmp_le_u32 s96, s76
s_cbranch_scc1 .La5_LA_SLOW1_L
v_mov_b32 v221, v218
s_nop 1
v_permlane16_swap_b32 v221, v218
s_nop 1
v_add_f32 v218, v218, v221
v_mov_b32 v221, v218
s_nop 1
v_permlane32_swap_b32 v221, v218
s_nop 1
v_add_f32 v218, v218, v221
v_mov_b32 v221, v219
s_nop 1
v_permlane16_swap_b32 v221, v219
s_nop 1
v_add_f32 v219, v219, v221
v_mov_b32 v221, v219
s_nop 1
v_permlane32_swap_b32 v221, v219
s_nop 1
v_add_f32 v219, v219, v221
v_rcp_f32 v242, v218
v_rcp_f32 v243, v219
ar v192,a160
ar v193,a161
ar v194,a162
ar v195,a163
ar v196,a164
ar v197,a165
ar v198,a166
ar v199,a167
ar v200,a168
ar v201,a169
ar v202,a170
ar v203,a171
ar v204,a172
ar v205,a173
ar v206,a174
ar v207,a175
ar v208,a176
ar v209,a177
ar v210,a178
ar v211,a179
ar v212,a180
ar v213,a181
ar v214,a182
ar v215,a183
ar v216,a184
ar v217,a185
ar v218,a186
ar v219,a187
ar v220,a188
ar v221,a189
ar v222,a190
ar v223,a191
s_mov_b32 s95, 4096
s_mov_b32 s96, 8192
s_mov_b32 s97, 12288
s_mov_b64 vcc, 0
s_nop 1
s_setpc_b64 s[24:25]
.La5_LA_SC1:
s_call_b64 s[24:25], .La5_LA_SLOW0
s_branch .La5_LA_SR1
.La5_LA_SC2:
s_call_b64 s[24:25], .La5_LA_SLOW1
s_branch .La5_LA_SR2
.La5_LA_PS0:
s_mov_b32 s84, 3
s_mov_b32 s70, 17408
wt vmcnt(2)
ld a[220:223],v255,s[68:71],s94 offen sc1
ld a[0:3],v255,s[68:71],0 offen offset:0 sc1
ld a[4:7],v255,s[68:71],0 offen offset:1024 sc1
ld a[8:11],v255,s[68:71],0 offen offset:2048 sc1
ld a[12:15],v255,s[68:71],0 offen offset:3072 sc1
ld a[16:19],v255,s[68:71],s95 offen offset:0 sc1
ld a[20:23],v255,s[68:71],s95 offen offset:1024 sc1
ld a[24:27],v255,s[68:71],s95 offen offset:2048 sc1
s_branch .La5_LA_PS1
.La5_LA_PS2:
s_mov_b32 s84, 3
s_mov_b32 s70, 17408
wt vmcnt(0)
ld a[220:223],v255,s[68:71],s94 offen sc1
ld a[0:3],v255,s[68:71],0 offen offset:0 sc1
ld a[4:7],v255,s[68:71],0 offen offset:1024 sc1
ld a[8:11],v255,s[68:71],0 offen offset:2048 sc1
ld a[12:15],v255,s[68:71],0 offen offset:3072 sc1
ld a[16:19],v255,s[68:71],s95 offen offset:0 sc1
ld a[20:23],v255,s[68:71],s95 offen offset:1024 sc1
ld a[24:27],v255,s[68:71],s95 offen offset:2048 sc1
ld a[28:31],v255,s[68:71],s95 offen offset:3072 sc1
ld a[32:35],v255,s[68:71],s96 offen offset:0 sc1
ld a[36:39],v255,s[68:71],s96 offen offset:1024 sc1
ld a[40:43],v255,s[68:71],s96 offen offset:2048 sc1
ld a[44:47],v255,s[68:71],s96 offen offset:3072 sc1
ld a[48:51],v255,s[68:71],s97 offen offset:0 sc1
ld a[52:55],v255,s[68:71],s97 offen offset:1024 sc1
ld a[56:59],v255,s[68:71],s97 offen offset:2048 sc1
ld a[60:63],v255,s[68:71],s97 offen offset:3072 sc1
s_branch .La5_LA_PS3
.La5_LA_PSPIN:
s_mov_b32 s93, 0
.La5_LA_PSPIN_L:
s_add_u32 s93, s93, 1
s_cmp_gt_u32 s93, 2097152
s_cbranch_scc1 .La5_LA_PSOK
s_sleep 8
buffer_load_dword v250, v251, s[64:67], s20 offen sc1
wt vmcnt(0)
v_cmp_eq_u32 vcc, v250, v249
s_nop 1
s_and_b32 s92, vcc_lo, 3
s_cmp_eq_u32 s92, 3
s_cbranch_scc0 .La5_LA_PSPIN_L
s_branch .La5_LA_PSOK
.La5_LA_PSOK:
s_mov_b32 s70, 17408
ld a[220:223],v255,s[68:71],s94 offen sc1
ld a[0:3],v255,s[68:71],0 offen offset:0 sc1
ld a[4:7],v255,s[68:71],0 offen offset:1024 sc1
ld a[8:11],v255,s[68:71],0 offen offset:2048 sc1
ld a[12:15],v255,s[68:71],0 offen offset:3072 sc1
ld a[16:19],v255,s[68:71],s95 offen offset:0 sc1
ld a[20:23],v255,s[68:71],s95 offen offset:1024 sc1
ld a[24:27],v255,s[68:71],s95 offen offset:2048 sc1
ld a[28:31],v255,s[68:71],s95 offen offset:3072 sc1
ld a[32:35],v255,s[68:71],s96 offen offset:0 sc1
ld a[36:39],v255,s[68:71],s96 offen offset:1024 sc1
ld a[40:43],v255,s[68:71],s96 offen offset:2048 sc1
ld a[44:47],v255,s[68:71],s96 offen offset:3072 sc1
ld a[48:51],v255,s[68:71],s97 offen offset:0 sc1
ld a[52:55],v255,s[68:71],s97 offen offset:1024 sc1
ld a[56:59],v255,s[68:71],s97 offen offset:2048 sc1
ld a[60:63],v255,s[68:71],s97 offen offset:3072 sc1
s_branch .La5_LA_DC1
.La5_LO_NB1:
.La5_LW3_6:
s_cbranch_execz .La5_LA_RETURN
s_mov_b32 s93, 0
.La5_LO_NB1_L:
s_add_u32 s93, s93, 1
s_cmp_gt_u32 s93, 2097152
s_cbranch_scc1 .La5_LO_KB1
s_sleep 8
buffer_load_dwordx2 v[250:251], v248, s[64:67], s5 offen sc1
wt vmcnt(0)
v_cmp_eq_u64 vcc, v[250:251], v[252:253]
s_nop 1
s_bitcmp1_b32 vcc_lo, 1
s_cbranch_scc0 .La5_LO_NB1_L
s_branch .La5_LO_KB1
.La5_LO_NB2:
s_mov_b32 s93, 0
.La5_LO_NB2_L:
s_add_u32 s93, s93, 1
s_cmp_gt_u32 s93, 2097152
s_nop 0
s_cbranch_scc1 .La5_LO_KB2
s_sleep 8
buffer_load_dwordx2 v[250:251], v248, s[64:67], s5 offen sc1
wt vmcnt(0)
v_cmp_eq_u64 vcc, v[250:251], v[252:253]
s_nop 1
s_bitcmp1_b32 vcc_lo, 2
s_cbranch_scc0 .La5_LO_NB2_L
s_branch .La5_LO_KB2
.La5_LO_NB3:
s_mov_b32 s93, 0
.La5_LO_NB3_L:
s_add_u32 s93, s93, 1
s_cmp_gt_u32 s93, 2097152
s_nop 0
s_nop 0
s_cbranch_scc1 .La5_LO_KB3
s_sleep 8
buffer_load_dwordx2 v[250:251], v248, s[64:67], s5 offen sc1
wt vmcnt(0)
v_cmp_eq_u64 vcc, v[250:251], v[252:253]
s_nop 1
s_bitcmp1_b32 vcc_lo, 3
s_cbranch_scc0 .La5_LO_NB3_L
s_branch .La5_LO_KB3
.La5_LO_NB4:
s_mov_b32 s93, 0
.La5_LO_NB4_L:
s_add_u32 s93, s93, 1
s_cmp_gt_u32 s93, 2097152
s_nop 0
s_nop 0
s_cbranch_scc1 .La5_LO_KB4
s_sleep 8
buffer_load_dwordx2 v[250:251], v248, s[64:67], s5 offen sc1
wt vmcnt(0)
v_cmp_eq_u64 vcc, v[250:251], v[252:253]
s_nop 1
s_bitcmp1_b32 vcc_lo, 4
s_cbranch_scc0 .La5_LO_NB4_L
s_branch .La5_LO_KB4
.La5_LO_NB5:
s_mov_b32 s93, 0
.La5_LO_NB5_L:
s_add_u32 s93, s93, 1
s_cmp_gt_u32 s93, 2097152
s_nop 0
s_nop 0
s_cbranch_scc1 .La5_LO_KB5
s_sleep 8
buffer_load_dwordx2 v[250:251], v248, s[64:67], s5 offen sc1
wt vmcnt(0)
v_cmp_eq_u64 vcc, v[250:251], v[252:253]
s_nop 1
s_bitcmp1_b32 vcc_lo, 5
s_cbranch_scc0 .La5_LO_NB5_L
s_branch .La5_LO_KB5
.La5_LO_NB13:
s_mov_b32 s93, 0
.La5_LO_NB13_L:
s_add_u32 s93, s93, 1
s_cmp_gt_u32 s93, 2097152
s_nop 0
s_nop 0
s_cbranch_scc1 .La5_LO_KB13
s_sleep 8
buffer_load_dwordx2 v[250:251], v248, s[64:67], s5 offen sc1
wt vmcnt(0)
v_cmp_eq_u64 vcc, v[250:251], v[252:253]
s_nop 1
s_bitcmp1_b32 vcc_lo, 13
s_cbranch_scc0 .La5_LO_NB13_L
s_branch .La5_LO_KB13
.La5_LO_NB6:
s_mov_b32 s93, 0
.La5_LO_NB6_L:
s_add_u32 s93, s93, 1
s_cmp_gt_u32 s93, 2097152
s_nop 0
s_nop 0
s_cbranch_scc1 .La5_LO_KB6
s_sleep 8
buffer_load_dwordx2 v[250:251], v248, s[64:67], s5 offen sc1
wt vmcnt(0)
v_cmp_eq_u64 vcc, v[250:251], v[252:253]
s_nop 1
s_bitcmp1_b32 vcc_lo, 6
s_cbranch_scc0 .La5_LO_NB6_L
s_branch .La5_LO_KB6
.La5_LO_NB7:
s_mov_b32 s93, 0
.La5_LO_NB7_L:
s_add_u32 s93, s93, 1
s_cmp_gt_u32 s93, 2097152
s_nop 0
s_nop 0
s_cbranch_scc1 .La5_LO_KB7
s_sleep 8
buffer_load_dwordx2 v[250:251], v248, s[64:67], s5 offen sc1
wt vmcnt(0)
v_cmp_eq_u64 vcc, v[250:251], v[252:253]
s_nop 1
s_bitcmp1_b32 vcc_lo, 7
s_cbranch_scc0 .La5_LO_NB7_L
s_branch .La5_LO_KB7
.La5_LO_NC1:
.La5_LW7_7:
s_cbranch_execz .La5_LA_RETURN
s_mov_b32 s93, 0
.La5_LO_NC1_L:
s_add_u32 s93, s93, 1
s_cmp_gt_u32 s93, 2097152
s_nop 0
s_cbranch_scc1 .La5_LO_KC1
s_sleep 8
buffer_load_dwordx2 v[250:251], v248, s[64:67], s5 offen sc1
wt vmcnt(0)
v_cmp_eq_u64 vcc, v[250:251], v[252:253]
s_nop 1
s_bitcmp1_b32 vcc_lo, 1
s_cbranch_scc0 .La5_LO_NC1_L
s_branch .La5_LO_KC1
.La5_LO_NC2:
s_mov_b32 s93, 0
.La5_LO_NC2_L:
s_add_u32 s93, s93, 1
s_cmp_gt_u32 s93, 2097152
s_nop 0
s_nop 0
s_cbranch_scc1 .La5_LO_KC2
s_sleep 8
buffer_load_dwordx2 v[250:251], v248, s[64:67], s5 offen sc1
wt vmcnt(0)
v_cmp_eq_u64 vcc, v[250:251], v[252:253]
s_nop 1
s_bitcmp1_b32 vcc_lo, 2
s_cbranch_scc0 .La5_LO_NC2_L
s_branch .La5_LO_KC2
.La5_LO_NC3:
s_mov_b32 s93, 0
.La5_LO_NC3_L:
s_add_u32 s93, s93, 1
s_cmp_gt_u32 s93, 2097152
s_nop 0
s_nop 0
s_cbranch_scc1 .La5_LO_KC3
s_sleep 8
buffer_load_dwordx2 v[250:251], v248, s[64:67], s5 offen sc1
wt vmcnt(0)
v_cmp_eq_u64 vcc, v[250:251], v[252:253]
s_nop 1
s_bitcmp1_b32 vcc_lo, 3
s_cbranch_scc0 .La5_LO_NC3_L
s_branch .La5_LO_KC3
.La5_LO_NC4:
s_mov_b32 s93, 0
.La5_LO_NC4_L:
s_add_u32 s93, s93, 1
s_cmp_gt_u32 s93, 2097152
s_nop 0
s_nop 0
s_cbranch_scc1 .La5_LO_KC4
s_sleep 8
buffer_load_dwordx2 v[250:251], v248, s[64:67], s5 offen sc1
wt vmcnt(0)
v_cmp_eq_u64 vcc, v[250:251], v[252:253]
s_nop 1
s_bitcmp1_b32 vcc_lo, 4
s_cbranch_scc0 .La5_LO_NC4_L
s_branch .La5_LO_KC4
.La5_LO_NC5:
s_mov_b32 s93, 0
.La5_LO_NC5_L:
s_add_u32 s93, s93, 1
s_cmp_gt_u32 s93, 2097152
s_nop 0
s_nop 0
s_cbranch_scc1 .La5_LO_KC5
s_sleep 8
buffer_load_dwordx2 v[250:251], v248, s[64:67], s5 offen sc1
wt vmcnt(0)
v_cmp_eq_u64 vcc, v[250:251], v[252:253]
s_nop 1
s_bitcmp1_b32 vcc_lo, 5
s_cbranch_scc0 .La5_LO_NC5_L
s_branch .La5_LO_KC5
.La5_LO_NC6:
s_mov_b32 s93, 0
.La5_LO_NC6_L:
s_add_u32 s93, s93, 1
s_cmp_gt_u32 s93, 2097152
s_nop 0
s_nop 0
s_cbranch_scc1 .La5_LO_KC6
s_sleep 8
buffer_load_dwordx2 v[250:251], v248, s[64:67], s5 offen sc1
wt vmcnt(0)
v_cmp_eq_u64 vcc, v[250:251], v[252:253]
s_nop 1
s_bitcmp1_b32 vcc_lo, 6
s_cbranch_scc0 .La5_LO_NC6_L
s_branch .La5_LO_KC6
.La5_LO_NC7:
s_mov_b32 s93, 0
.La5_LO_NC7_L:
s_add_u32 s93, s93, 1
s_cmp_gt_u32 s93, 2097152
s_nop 0
s_nop 0
s_cbranch_scc1 .La5_LO_KC7
s_sleep 8
buffer_load_dwordx2 v[250:251], v248, s[64:67], s5 offen sc1
wt vmcnt(0)
v_cmp_eq_u64 vcc, v[250:251], v[252:253]
s_nop 1
s_bitcmp1_b32 vcc_lo, 7
s_cbranch_scc0 .La5_LO_NC7_L
s_branch .La5_LO_KC7
.La5_LA_SC3:
s_call_b64 s[24:25], .La5_LA_SLOW0
aw a0,0
aw a1,0
aw a2,0
aw a3,0
aw a4,0
aw a5,0
aw a6,0
aw a7,0
aw a8,0
aw a9,0
aw a10,0
aw a11,0
aw a12,0
aw a13,0
aw a14,0
aw a15,0
aw a16,0
aw a17,0
aw a18,0
aw a19,0
aw a20,0
aw a21,0
aw a22,0
aw a23,0
aw a24,0
aw a25,0
aw a26,0
aw a27,0
aw a28,0
aw a29,0
aw a30,0
aw a31,0
s_nop 1
v_mov_b32 v250, v242
v_mov_b32 v249, v243
v_xor_b32 v242, 64, v240
v_xor_b32 v243, 96, v240
ar v254,a229
s_mov_b32 s93, 0
ld a[128:131],v254,s[40:43],s93 offen
s_mov_b32 s93, 7168
ld a[132:135],v254,s[40:43],s93 offen
s_mov_b32 s93, 14336
ld a[136:139],v254,s[40:43],s93 offen
s_mov_b32 s93, 21504
ld a[140:143],v254,s[40:43],s93 offen
s_mov_b32 s93, 28672
ld a[144:147],v254,s[40:43],s93 offen
s_mov_b32 s93, 35840
ld a[148:151],v254,s[40:43],s93 offen
s_mov_b32 s93, 43008
ld a[152:155],v254,s[40:43],s93 offen
s_mov_b32 s93, 50176
ld a[156:159],v254,s[40:43],s93 offen
s_mov_b32 s93, 57344
ld a[160:163],v254,s[40:43],s93 offen
s_mov_b32 s93, 64512
ld a[164:167],v254,s[40:43],s93 offen
s_mov_b32 s93, 71680
ld a[168:171],v254,s[40:43],s93 offen
s_mov_b32 s93, 78848
ld a[172:175],v254,s[40:43],s93 offen
s_mov_b32 s93, 86016
ld a[176:179],v254,s[40:43],s93 offen
s_mov_b32 s93, 93184
ld a[180:183],v254,s[40:43],s93 offen
s_mov_b32 s93, 100352
ld a[184:187],v254,s[40:43],s93 offen
s_mov_b32 s93, 107520
ld a[188:191],v254,s[40:43],s93 offen
s_branch .La5_LA_SR3
.La5_LA_SC4:
s_call_b64 s[24:25], .La5_LA_SLOW1
aw a32,0
aw a33,0
aw a34,0
aw a35,0
aw a36,0
aw a37,0
aw a38,0
aw a39,0
aw a40,0
aw a41,0
aw a42,0
aw a43,0
aw a44,0
aw a45,0
aw a46,0
aw a47,0
aw a48,0
aw a49,0
aw a50,0
aw a51,0
aw a52,0
aw a53,0
aw a54,0
aw a55,0
aw a56,0
aw a57,0
aw a58,0
aw a59,0
aw a60,0
aw a61,0
aw a62,0
aw a63,0
s_nop 1
v_mov_b32 v250, v242
v_mov_b32 v249, v243
v_xor_b32 v242, 64, v240
v_xor_b32 v243, 96, v240
ar v254,a229
s_mov_b32 s93, 0
ld a[128:131],v254,s[40:43],s93 offen
s_mov_b32 s93, 7168
ld a[132:135],v254,s[40:43],s93 offen
s_mov_b32 s93, 14336
ld a[136:139],v254,s[40:43],s93 offen
s_mov_b32 s93, 21504
ld a[140:143],v254,s[40:43],s93 offen
s_mov_b32 s93, 28672
ld a[144:147],v254,s[40:43],s93 offen
s_mov_b32 s93, 35840
ld a[148:151],v254,s[40:43],s93 offen
s_mov_b32 s93, 43008
ld a[152:155],v254,s[40:43],s93 offen
s_mov_b32 s93, 50176
ld a[156:159],v254,s[40:43],s93 offen
s_mov_b32 s93, 57344
ld a[160:163],v254,s[40:43],s93 offen
s_mov_b32 s93, 64512
ld a[164:167],v254,s[40:43],s93 offen
s_mov_b32 s93, 71680
ld a[168:171],v254,s[40:43],s93 offen
s_mov_b32 s93, 78848
ld a[172:175],v254,s[40:43],s93 offen
s_mov_b32 s93, 86016
ld a[176:179],v254,s[40:43],s93 offen
s_mov_b32 s93, 93184
ld a[180:183],v254,s[40:43],s93 offen
s_mov_b32 s93, 100352
ld a[184:187],v254,s[40:43],s93 offen
s_mov_b32 s93, 107520
ld a[188:191],v254,s[40:43],s93 offen
s_branch .La5_LA_SR4
.La5_LO_VC1:
.La5_LW5_4:
s_cbranch_execz .La5_LW5_R0
.La5_LW6_8:
s_cbranch_execz .La5_LW6_R0
s_getpc_b64 s[24:25]
.La5_LW7_P0:
s_add_u32 s24, s24, .La5_LW7_R0-.La5_LW7_P0
s_addc_u32 s25, s25, 0
s_mov_b64 exec, 0
s_cmp_eq_u32 s3, 0
s_cbranch_scc1 .La5_LW7_0
s_cmp_eq_u32 s3, 1
s_cbranch_scc1 .La5_LW7_1
s_cmp_eq_u32 s3, 2
s_cbranch_scc1 .La5_LW7_2
s_branch .La5_LW7_3
.La5_LW7_R0:
s_mov_b64 exec, -1
s_branch .La5_LO_VE1
.La5_LO_W:
s_mov_b32 s93, 3670016
ld a[192:195],v228,s[40:43],s93 offen
s_mov_b32 s93, 3677184
ld a[196:199],v228,s[40:43],s93 offen
s_mov_b32 s93, 3684352
ld a[200:203],v228,s[40:43],s93 offen
s_mov_b32 s93, 3691520
ld a[204:207],v228,s[40:43],s93 offen
s_mov_b32 s93, 3698688
ld a[208:211],v228,s[40:43],s93 offen
s_mov_b32 s93, 3705856
ld a[212:215],v228,s[40:43],s93 offen
s_mov_b32 s93, 3713024
ld a[216:219],v228,s[40:43],s93 offen
s_mov_b32 s93, 3720192
ld a[220:223],v228,s[40:43],s93 offen
s_mov_b32 s93, 3727360
ld a[224:227],v228,s[40:43],s93 offen
s_mov_b32 s93, 3734528
ld a[228:231],v228,s[40:43],s93 offen
s_mov_b32 s93, 3741696
ld a[232:235],v228,s[40:43],s93 offen
s_mov_b32 s93, 3748864
ld a[236:239],v228,s[40:43],s93 offen
s_mov_b32 s93, 3756032
ld a[240:243],v228,s[40:43],s93 offen
s_mov_b32 s93, 3763200
ld a[244:247],v228,s[40:43],s93 offen
s_mov_b32 s93, 3770368
ld a[248:251],v228,s[40:43],s93 offen
s_mov_b32 s93, 3777536
ld a[252:255],v228,s[40:43],s93 offen
s_branch .La5_LO_START
.La5_LO_VC2:
s_getpc_b64 s[24:25]
.La5_LW7_P1:
s_add_u32 s24, s24, .La5_LW7_R1-.La5_LW7_P1
s_addc_u32 s25, s25, 0
s_mov_b64 exec, 0
s_cmp_eq_u32 s3, 0
s_cbranch_scc1 .La5_LW7_0
s_cmp_eq_u32 s3, 1
s_cbranch_scc1 .La5_LW7_1
s_cmp_eq_u32 s3, 2
s_cbranch_scc1 .La5_LW7_2
s_branch .La5_LW7_3
.La5_LW7_R1:
s_mov_b64 exec, -1
s_branch .La5_LO_VE2
.La5_LT_VAR:
v_mov_b32 v230, 0
v_lshlrev_b32 v232, 16, v96
v_and_b32 v233, 0xffff0000, v96
v_sub_f32 v232, v232, v244
v_sub_f32 v233, v233, v244
v_fmac_f32 v230, v232, v232
v_fmac_f32 v230, v233, v233
v_lshlrev_b32 v232, 16, v97
v_and_b32 v233, 0xffff0000, v97
v_sub_f32 v232, v232, v244
v_sub_f32 v233, v233, v244
v_fmac_f32 v230, v232, v232
v_fmac_f32 v230, v233, v233
v_lshlrev_b32 v232, 16, v98
v_and_b32 v233, 0xffff0000, v98
v_sub_f32 v232, v232, v244
v_sub_f32 v233, v233, v244
v_fmac_f32 v230, v232, v232
v_fmac_f32 v230, v233, v233
v_lshlrev_b32 v232, 16, v99
v_and_b32 v233, 0xffff0000, v99
v_sub_f32 v232, v232, v244
v_sub_f32 v233, v233, v244
v_fmac_f32 v230, v232, v232
v_fmac_f32 v230, v233, v233
v_lshlrev_b32 v232, 16, v100
v_and_b32 v233, 0xffff0000, v100
v_sub_f32 v232, v232, v244
v_sub_f32 v233, v233, v244
v_fmac_f32 v230, v232, v232
v_fmac_f32 v230, v233, v233
v_lshlrev_b32 v232, 16, v101
v_and_b32 v233, 0xffff0000, v101
v_sub_f32 v232, v232, v244
v_sub_f32 v233, v233, v244
v_fmac_f32 v230, v232, v232
v_fmac_f32 v230, v233, v233
v_lshlrev_b32 v232, 16, v102
v_and_b32 v233, 0xffff0000, v102
v_sub_f32 v232, v232, v244
v_sub_f32 v233, v233, v244
v_fmac_f32 v230, v232, v232
v_fmac_f32 v230, v233, v233
v_lshlrev_b32 v232, 16, v103
v_and_b32 v233, 0xffff0000, v103
v_sub_f32 v232, v232, v244
v_sub_f32 v233, v233, v244
v_fmac_f32 v230, v232, v232
v_fmac_f32 v230, v233, v233
v_lshlrev_b32 v232, 16, v104
v_and_b32 v233, 0xffff0000, v104
v_sub_f32 v232, v232, v244
v_sub_f32 v233, v233, v244
v_fmac_f32 v230, v232, v232
v_fmac_f32 v230, v233, v233
v_lshlrev_b32 v232, 16, v105
v_and_b32 v233, 0xffff0000, v105
v_sub_f32 v232, v232, v244
v_sub_f32 v233, v233, v244
v_fmac_f32 v230, v232, v232
v_fmac_f32 v230, v233, v233
v_lshlrev_b32 v232, 16, v106
v_and_b32 v233, 0xffff0000, v106
v_sub_f32 v232, v232, v244
v_sub_f32 v233, v233, v244
v_fmac_f32 v230, v232, v232
v_fmac_f32 v230, v233, v233
v_lshlrev_b32 v232, 16, v107
v_and_b32 v233, 0xffff0000, v107
v_sub_f32 v232, v232, v244
v_sub_f32 v233, v233, v244
v_fmac_f32 v230, v232, v232
v_fmac_f32 v230, v233, v233
v_lshlrev_b32 v232, 16, v108
v_and_b32 v233, 0xffff0000, v108
v_sub_f32 v232, v232, v244
v_sub_f32 v233, v233, v244
v_fmac_f32 v230, v232, v232
v_fmac_f32 v230, v233, v233
v_lshlrev_b32 v232, 16, v109
v_and_b32 v233, 0xffff0000, v109
v_sub_f32 v232, v232, v244
v_sub_f32 v233, v233, v244
v_fmac_f32 v230, v232, v232
v_fmac_f32 v230, v233, v233
v_lshlrev_b32 v232, 16, v110
v_and_b32 v233, 0xffff0000, v110
v_sub_f32 v232, v232, v244
v_sub_f32 v233, v233, v244
v_fmac_f32 v230, v232, v232
v_fmac_f32 v230, v233, v233
v_lshlrev_b32 v232, 16, v111
v_and_b32 v233, 0xffff0000, v111
v_sub_f32 v232, v232, v244
v_sub_f32 v233, v233, v244
v_fmac_f32 v230, v232, v232
v_fmac_f32 v230, v233, v233
s_nop 1
v_add_f32_dpp v230, v230, v230 quad_perm:[1,0,3,2] row_mask:0xf bank_mask:0xf
s_nop 1
v_add_f32_dpp v230, v230, v230 quad_perm:[2,3,0,1] row_mask:0xf bank_mask:0xf
s_nop 1
v_add_f32_dpp v230, v230, v230 row_ror:8 row_mask:0xf bank_mask:0xf
v_mov_b32 v231, v230
s_nop 1
v_permlane16_swap_b32 v231, v230
s_nop 1
v_add_f32 v230, v230, v231
v_mov_b32_e64 v231, v230
s_nop 1
v_permlane32_swap_b32 v231, v230
s_nop 1
v_add_f32_e64 v230, v230, v231
v_mul_f32 v230, 0x3a800000, v230
s_branch .La5_LT_VOK
.La5_LA_KSPIN:
s_mov_b32 s93, 0
.La5_LA_KSPIN_L:
s_add_u32 s93, s93, 1
s_cmp_gt_u32 s93, 2097152
s_cbranch_scc1 .La5_LA_KOK
s_sleep 8
buffer_load_dword v250, v251, s[64:67], s20 offen sc1
wt vmcnt(0)
v_cmp_eq_u32 vcc, v250, v249
s_nop 1
s_or_b32 vcc_hi, vcc_hi, s79
s_cmp_eq_u64 vcc, -1
s_cbranch_scc0 .La5_LA_KSPIN_L
s_branch .La5_LA_KOK
.La5_LA_QSPIN:
s_mov_b32 s93, 0
.La5_LA_QSPIN_L:
s_add_u32 s93, s93, 1
s_cmp_gt_u32 s93, 2097152
s_nop 0
s_cbranch_scc1 .La5_LA_QOK
s_sleep 8
buffer_load_dword v250, v251, s[64:67], s20 offen sc1
wt vmcnt(0)
v_cmp_eq_u32 vcc, v250, v249
s_nop 1
s_and_b32 s92, vcc_lo, 3
s_cmp_eq_u32 s92, 3
s_cbranch_scc0 .La5_LA_QSPIN_L
s_branch .La5_LA_QOK
.La5_LEND:
.La5_LCODEEND:
)ASM"
        :: "{s[0:1]}"(__builtin_amdgcn_kernarg_segment_ptr()), "{s2}"(blockIdx.x), "{s3}"(__builtin_amdgcn_readfirstlane(threadIdx.x >> 6)), "{s[4:5]}"(DBGP)
        : "v0","v1","v2","v3","v4","v5","v6","v7","v8","v9","v10","v11","v12","v13","v14","v15","v16","v17","v18","v19","v20","v21","v22","v23","v24","v25","v26","v27","v28","v29","v30","v31","v32","v33","v34","v35","v36","v37","v38","v39","v40","v41","v42","v43","v44","v45","v46","v47","v48","v49","v50","v51","v52","v53","v54","v55","v56","v57","v58","v59","v60","v61","v62","v63","v64","v65","v66","v67","v68","v69","v70","v71","v72","v73","v74","v75","v76","v77","v78","v79","v80","v81","v82","v83","v84","v85","v86","v87","v88","v89","v90","v91","v92","v93","v94","v95","v96","v97","v98","v99","v100","v101","v102","v103","v104","v105","v106","v107","v108","v109","v110","v111","v112","v113","v114","v115","v116","v117","v118","v119","v120","v121","v122","v123","v124","v125","v126","v127","v128","v129","v130","v131","v132","v133","v134","v135","v136","v137","v138","v139","v140","v141","v142","v143","v144","v145","v146","v147","v148","v149","v150","v151","v152","v153","v154","v155","v156","v157","v158","v159","v160","v161","v162","v163","v164","v165","v166","v167","v168","v169","v170","v171","v172","v173","v174","v175","v176","v177","v178","v179","v180","v181","v182","v183","v184","v185","v186","v187","v188","v189","v190","v191","v192","v193","v194","v195","v196","v197","v198","v199","v200","v201","v202","v203","v204","v205","v206","v207","v208","v209","v210","v211","v212","v213","v214","v215","v216","v217","v218","v219","v220","v221","v222","v223","v224","v225","v226","v227","v228","v229","v230","v231","v232","v233","v234","v235","v236","v237","v238","v239","v240","v241","v242","v243","v244","v245","v246","v247","v248","v249","v250","v251","v252","v253","v254","v255","a0","a1","a2","a3","a4","a5","a6","a7","a8","a9","a10","a11","a12","a13","a14","a15","a16","a17","a18","a19","a20","a21","a22","a23","a24","a25","a26","a27","a28","a29","a30","a31","a32","a33","a34","a35","a36","a37","a38","a39","a40","a41","a42","a43","a44","a45","a46","a47","a48","a49","a50","a51","a52","a53","a54","a55","a56","a57","a58","a59","a60","a61","a62","a63","a64","a65","a66","a67","a68","a69","a70","a71","a72","a73","a74","a75","a76","a77","a78","a79","a80","a81","a82","a83","a84","a85","a86","a87","a88","a89","a90","a91","a92","a93","a94","a95","a96","a97","a98","a99","a100","a101","a102","a103","a104","a105","a106","a107","a108","a109","a110","a111","a112","a113","a114","a115","a116","a117","a118","a119","a120","a121","a122","a123","a124","a125","a126","a127","a128","a129","a130","a131","a132","a133","a134","a135","a136","a137","a138","a139","a140","a141","a142","a143","a144","a145","a146","a147","a148","a149","a150","a151","a152","a153","a154","a155","a156","a157","a158","a159","a160","a161","a162","a163","a164","a165","a166","a167","a168","a169","a170","a171","a172","a173","a174","a175","a176","a177","a178","a179","a180","a181","a182","a183","a184","a185","a186","a187","a188","a189","a190","a191","a192","a193","a194","a195","a196","a197","a198","a199","a200","a201","a202","a203","a204","a205","a206","a207","a208","a209","a210","a211","a212","a213","a214","a215","a216","a217","a218","a219","a220","a221","a222","a223","a224","a225","a226","a227","a228","a229","a230","a231","a232","a233","a234","a235","a236","a237","a238","a239","a240","a241","a242","a243","a244","a245","a246","a247","a248","a249","a250","a251","a252","a253","a254","a255","s6","s7","s8","s9","s10","s11","s12","s13","s14","s15","s16","s17","s18","s19","s20","s21","s22","s23","s24","s25","s26","s27","s28","s29","s30","s31","s32","s33","s34","s35","s36","s37","s38","s39","s40","s41","s42","s43","s44","s45","s46","s47","s48","s49","s50","s51","s52","s53","s54","s55","s56","s57","s58","s59","s60","s61","s62","s63","s64","s65","s66","s67","s68","s69","s70","s71","s72","s73","s74","s75","s76","s77","s78","s79","s80","s81","s82","s83","s84","s85","s86","s87","s88","s89","s90","s91","s92","s93","s94","s95","s96","s97","s98","s99","s100","s101","vcc","scc","m0","memory");
}

extern "C" void kernel_launch(void* const* d_in, const int* in_sizes, int n_in, void* d_out, int out_size, void* d_ws, size_t ws_size,
                              hipStream_t stream) {
    (void)hipFuncSetAttribute((const void*)fa_kernel, hipFuncAttributeMaxDynamicSharedMemorySize, LDS_BYTES);
    fa_kernel<<<256, 256, LDS_BYTES, stream>>>((const bf16*)d_in[0], (const bf16*)d_in[1], (const bf16*)d_in[2], (const bf16*)d_in[3], (const bf16*)d_in[4], (char*)d_ws,
                                               (const bf16*)d_in[5], (const bf16*)d_in[8], (const bf16*)d_in[9], (const bf16*)d_in[10], (const bf16*)d_in[7], (bf16*)d_out);
}
```

```cpp
#include <hip/hip_runtime.h>
#include <hip/hip_bf16.h>
#include <stdint.h>
#pragma clang diagnostic ignored "-Winline-asm"
#pragma clang diagnostic ignored "-Woverlength-strings"

typedef __hip_bfloat16 bf16;
#define DBGP ((unsigned*)nullptr)
constexpr int LDS_BYTES = 163840;
extern "C" __global__ __attribute__((aligned(4096))) __launch_bounds__(256, 1) void fa_kernel(const bf16* __restrict__ px, const bf16* __restrict__ ckv, const bf16* __restrict__ wq,
                                                    const bf16* __restrict__ wuk, const bf16* __restrict__ wuv, char* pws,
                                                    const bf16* __restrict__ pwo, const bf16* __restrict__ pwg, const bf16* __restrict__ pwu,
                                                    const bf16* __restrict__ pwd, const bf16* __restrict__ pwr, bf16* __restrict__ pout) {
    asm volatile(R"ASM(
.macro mm x:vararg
v_mfma_f32_16x16x32_bf16 \x
.endm
.macro rt x:vararg
ds_read_b64_tr_b16 \x
.endm
.macro rd x:vararg
ds_read_b128 \x
.endm
.macro ld x:vararg
buffer_load_dwordx4 \x
.endm
.macro st x:vararg
buffer_store_dwordx4 \x
.endm
.macro ar x:vararg
v_accvgpr_read_b32 \x
.endm
.macro aw x:vararg
v_accvgpr_write_b32 \x
.endm
.macro cv x:vararg
v_cvt_pk_bf16_f32 \x
.endm
.macro ex x:vararg
v_exp_f32 \x
.endm
.macro wt x:vararg
s_waitcnt \x
.endm
; ---- roles: head = 4 xcd + (wg >> 6), j = (wg >> 3) & 7; odd j: block j >> 1 (then helps with block 7 - (j >> 1)), even j: block 7 - (j >> 1). First operands, addresses
.La5_LSTART:
s_load_dwordx16 s[16:31], s[0:1], 0
s_load_dwordx8 s[32:39], s[0:1], 64
.p2align 6
v_mbcnt_lo_u32_b32 v64, -1, 0
v_mbcnt_hi_u32_b32 v64, -1, v64
s_getpc_b64 s[92:93]
.La5_LPC:
s_sub_u32 s48, s92, .La5_LPC-.La5_LSTART
s_subb_u32 s49, s93, 0
s_and_b32 s49, s49, 0xffff
s_mov_b32 s50, .La5_LCODEEND-.La5_LSTART
s_mov_b32 s51, 0x20000
s_lshr_b32 s97, s2, 3
s_lshl2_add_u32 s87, s97, s3
v_lshlrev_b32 v133, 7, v64
s_lshl_b32 s87, s87, 13
buffer_load_dword v254, v133, s[48:51], s87 offen
s_and_b32 s92, s2, 7
s_lshr_b32 s93, s2, 6
s_lshl2_add_u32 s72, s92, s93
s_bfe_u32 s73, s2, 0x30003
s_lshr_b32 s74, s73, 1
s_xor_b32 s92, s74, 7
s_and_b32 s75, s73, 1
s_cselect_b32 s76, s74, s92
s_lshr_b32 s77, s3, 1
s_and_b32 s78, s3, 1
v_and_b32 v65, 15, v64
v_lshrrev_b32 v66, 4, v64
v_lshlrev_b32 v255, 4, v64
v_lshlrev_b32 v67, 1, v66
v_xor_b32 v67, v67, v65
v_lshlrev_b32 v67, 4, v67
v_lshl_add_u32 v67, v66, 13, v67
s_lshl_b32 s94, s78, 17
v_add_u32 v232, s94, v67
v_add_u32 v233, 32768, v232
v_add_u32 v234, 65536, v232
v_add_u32 v235, 98304, v232
v_and_b32 v67, 7, v64
v_xor_b32 v68, v67, v66
v_lshlrev_b32 v68, 4, v68
v_lshrrev_b32 v69, 3, v64
v_lshl_add_u32 v68, v69, 10, v68
s_lshl2_add_u32 s94, s3, s93
s_lshl_b32 s82, s94, 11
s_lshl_b32 s94, s94, 14
v_add_u32 v224, s94, v68
v_xor_b32 v225, 0x2040, v224
v_xor_b32 v226, 16384, v224
v_xor_b32 v227, 16384, v225
v_xor_b32 v228, 32768, v224
v_xor_b32 v229, 32768, v225
v_xor_b32 v230, 49152, v224
v_xor_b32 v231, 49152, v225
s_mul_i32 s94, s77, 8448
s_mul_i32 s95, s78, 4224
s_add_u32 s83, s94, s95
s_add_u32 s83, s83, 96256
v_lshlrev_b32 v135, 7, v67
v_lshl_add_u32 v135, v69, 13, v135
v_lshrrev_b32 v132, 5, v64
v_lshl_add_u32 v132, v132, 28, v135
v_lshl_add_u32 v133, v66, 28, v133
v_lshlrev_b32 v134, 10, v65
v_lshl_add_u32 v134, v66, 7, v134
v_lshrrev_b32 v68, 4, v64
v_lshl_add_u32 v134, v68, 28, v134
s_lshl1_add_u32 s97, s97, s78
s_lshl_b32 s7, s97, 13
s_lshl_b32 s97, s97, 15
s_lshl_b32 s96, s93, 8
s_sub_u32 s7, s7, s96
s_sub_u32 s87, s87, s96
s_sub_u32 s97, s97, s96
s_add_u32 s86, s97, 2097152
s_lshl2_add_u32 s101, s72, s3
s_lshl_b32 s101, s101, 11
s_lshl2_add_u32 s100, s93, s3
s_lshl_b32 s100, s100, 14
v_lshlrev_b32 v118, 11, v67
s_lshl_b32 s93, s3, 9
v_lshl_add_u32 v119, v69, 4, v118
v_add_u32 v119, s93, v119
s_lshl_b32 s97, s2, 14
s_lshl_b32 s94, s72, 8
s_lshl_b32 s95, s76, 18
s_sub_u32 s46, 4194304, s94
s_mov_b32 s47, 0x20000
s_mov_b32 s42, 262144
s_mov_b32 s43, 0x20000
s_mov_b32 s62, 16384
s_mov_b32 s63, 0x20000
s_mov_b32 s79, 512
s_mov_b32 s80, 2097152
s_add_u32 s100, s100, 512
s_mov_b32 s81, 262144
s_cmp_eq_u32 s77, 0
wt lgkmcnt(0)
s_cselect_b64 s[92:93], s[22:23], s[24:25]
s_add_u32 s44, s92, s94
s_addc_u32 s45, s93, 0
s_and_b32 s45, s45, 0xffff
s_add_u32 s40, s18, s95
s_addc_u32 s41, s19, 0
s_and_b32 s41, s41, 0xffff
s_add_u32 s60, s16, s97
s_addc_u32 s61, s17, 0
s_and_b32 s61, s61, 0xffff
buffer_load_dword v254, v132, s[44:47], s86 offen
s_add_u32 s86, s86, 2097152
s_and_b32 s86, s86, 4194303
buffer_load_dword v254, v133, s[40:43], s101 offen
buffer_load_dword v254, v134, s[40:43], s100 offen
s_add_u32 m0, s83, 0
s_nop 0
ld v232,s[44:47],s80 offen lds
s_add_u32 m0, s83, 1024
s_nop 0
ld v233,s[44:47],s80 offen lds
s_add_u32 m0, s83, 2176
s_nop 0
ld v234,s[44:47],s80 offen lds
s_add_u32 m0, s83, 3200
s_nop 0
ld v235,s[44:47],s80 offen lds
s_add_u32 s80, s80, s81
s_and_b32 s80, s80, 4194303
v_lshrrev_b32 v67, 1, v65
v_xor_b32 v67, v67, v66
v_lshlrev_b32 v67, 4, v67
v_lshl_add_u32 v67, v65, 7, v67
s_lshl_b32 s92, s77, 12
v_add_u32 v236, s92, v67
v_xor_b32 v237, 64, v236
v_bfe_u32 v120, v64, 2, 2
v_add_u32 v67, 1, v66
v_lshrrev_b32 v67, 1, v67
v_lshlrev_b32 v67, 7, v67
v_lshl_add_u32 v67, v66, 11, v67
v_lshl_add_u32 v67, v120, 8, v67
v_and_b32 v68, 3, v64
v_lshl_add_u32 v67, v68, 3, v67
s_mul_i32 s92, s78, 8448
v_lshl_add_u32 v67, v120, 5, v67
v_add_u32 v67, s92, v67
v_add_u32 v240, 96256, v67
v_xor_b32 v241, 32, v240
v_xor_b32 v242, 64, v240
v_xor_b32 v243, 96, v240
s_add_u32 s64, s26, 142606336
s_addc_u32 s65, s27, 0
s_and_b32 s65, s65, 0xffff
s_mov_b32 s66, 3145728
s_mov_b32 s67, 0x20000
s_mov_b32 s88, 4096
s_lshr_b32 s92, s2, 1
s_lshl_b32 s92, s92, 15
s_and_b32 s93, s2, 1
s_lshl_b32 s93, s93, 7
s_add_u32 s92, s92, s93
s_lshl_b32 s93, s3, 13
s_add_u32 s92, s92, s93
s_add_u32 s92, s92, 88080384
s_add_u32 s56, s26, s92
s_addc_u32 s57, s27, 0
s_and_b32 s57, s57, 0xffff
s_mov_b32 s58, 8576
s_mov_b32 s59, 0x20000
v_and_b32 v71, 0x380, v255
v_and_b32 v127, 0x70, v255
v_lshl_add_u32 v71, v71, 1, v127
v_xor_b32 v67, 3, v69
v_min_u32 v67, 1, v67
v_xor_b32 v67, 1, v67
s_mov_b32 s93, 0
v_mul_lo_u32 v67, v67, s93
v_add_u32 v136, v67, v255
v_and_b32 v67, 3, v69
v_xor_b32 v67, 1, v67
v_min_u32 v67, 1, v67
v_xor_b32 v67, 1, v67
v_and_b32 v67, s2, v67
v_mul_lo_u32 v67, v67, s93
v_add_u32 v71, v71, v67
s_lshl_b32 s92, s3, 3
v_lshlrev_b32 v126, 1, v127
v_or_b32 v126, 65536, v126
v_add_u32 v127, s92, v126
s_add_u32 s52, s26, 171966464
s_addc_u32 s53, s27, 0
s_and_b32 s53, s53, 0xffff
s_mov_b32 s54, 950272
s_mov_b32 s55, 0x20000
s_lshl2_add_u32 s95, s2, s3
s_lshl_b32 s91, s95, 3
s_lshl_b32 s95, s95, 10
s_lshl_b32 s89, s76, 10
v_lshl_add_u32 v135, v69, 28, v135
s_mov_b32 s96, s87
s_mov_b32 s84, 33792
s_mov_b32 s85, 0
s_add_u32 s97, s83, 67584
s_mov_b32 s90, s83
s_lshl_b32 s92, s72, 8
s_sub_u32 s93, 8388608, s92
s_add_u32 s68, s20, s92
s_addc_u32 s69, s21, 0
s_and_b32 s69, s69, 0xffff
s_mov_b32 s70, s93
s_mov_b32 s71, 0x20000
s_lshr_b32 s92, s20, 7
s_sub_u32 s92, 3, s92
s_bfe_u32 s99, s92, 0x20001
s_lshr_b32 s93, s2, 6
s_sub_u32 s99, s99, s93
s_lshl_b32 s92, s99, 8
s_lshr_b32 s93, s2, 3
s_lshl2_add_u32 s93, s93, s3
s_lshl_b32 s98, s93, 16
s_add_u32 s98, s98, s92
v_lshlrev_b32 v138, 4, v65
v_lshl_add_u32 v137, v66, 13, v138
v_lshl_add_u32 v138, v66, 9, v138
s_lshl_b32 s93, s93, 12
v_add_u32 v138, s93, v138
s_and_b32 s93, s2, 7
s_lshl_b32 s92, s93, 19
s_add_u32 s92, s92, 138412032
s_add_u32 s4, s26, s92
s_addc_u32 s5, s27, 0
s_and_b32 s5, s5, 0xffff
s_mov_b32 s6, 0
s_mov_b32 s100, 1073741824
s_add_u32 s101, s91, 65536
v_lshrrev_b32 v139, 1, v64
v_lshlrev_b32 v139, 8, v139
v_and_or_b32 v139, v255, 16, v139
s_lshl_b32 s93, s93, 5
s_add_u32 s93, s93, 64512
v_add_u32 v139, s93, v139
s_mov_b64 exec, 0
s_cmp_eq_u32 s3, 0
s_cbranch_scc1 .La5_LW1_0
s_cmp_eq_u32 s3, 1
s_cbranch_scc1 .La5_LW1_1
s_cmp_eq_u32 s3, 2
s_cbranch_scc1 .La5_LW1_2
s_branch .La5_LW1_3
.La5_LW1_R0:
s_mov_b64 exec, -1
s_xor_b32 m0, s82, 0
s_nop 0
ld v224,s[40:43],s79 offen lds
s_xor_b32 m0, s82, 1024
s_nop 0
ld v225,s[40:43],s79 offen lds
s_xor_b32 m0, s82, 2048
s_nop 0
ld v226,s[40:43],s79 offen lds
s_xor_b32 m0, s82, 3072
s_nop 0
ld v227,s[40:43],s79 offen lds
s_xor_b32 m0, s82, 4096
s_nop 0
ld v228,s[40:43],s79 offen lds
s_xor_b32 m0, s82, 5120
s_nop 0
ld v229,s[40:43],s79 offen lds
s_xor_b32 m0, s82, 6144
s_nop 0
ld v230,s[40:43],s79 offen lds
s_xor_b32 m0, s82, 7168
s_nop 0
ld v231,s[40:43],s79 offen lds
s_add_u32 s79, s79, 128
s_and_b32 s79, s79, 1023
s_add_u32 m0, s83, 16896
s_nop 0
ld v232,s[44:47],s80 offen lds
s_add_u32 m0, s83, 17920
s_nop 0
ld v233,s[44:47],s80 offen lds
s_add_u32 m0, s83, 19072
s_nop 0
ld v234,s[44:47],s80 offen lds
s_add_u32 m0, s83, 20096
s_nop 0
ld v235,s[44:47],s80 offen lds
s_add_u32 s80, s80, s81
s_and_b32 s80, s80, 4194303
buffer_load_dwordx2 v[252:253], off, s[64:67], 0 sc1
wt vmcnt(1)
s_barrier
rd v[160:163],v236 offset:24576
rd v[164:167],v237 offset:24576
rt v[192:193],v240 offset:0
rt v[194:195],v240 offset:1024
rt v[196:197],v240 offset:16896
rt v[198:199],v240 offset:17920
rt v[200:201],v241 offset:0
rt v[202:203],v241 offset:1024
rt v[204:205],v241 offset:16896
rt v[206:207],v241 offset:17920
rt v[208:209],v242 offset:0
rt v[210:211],v242 offset:1024
rt v[212:213],v242 offset:16896
rt v[214:215],v242 offset:17920
rt v[216:217],v243 offset:0
rt v[218:219],v243 offset:1024
rt v[220:221],v243 offset:16896
rt v[222:223],v243 offset:17920
rd v[168:171],v236 offset:26624
rd v[172:175],v237 offset:26624
rd v[176:179],v236 offset:16384
rd v[180:183],v237 offset:16384
rd v[184:187],v236 offset:18432
rd v[188:191],v237 offset:18432
wt lgkmcnt(0)
s_cmp_eq_u32 s78, 1
s_cbranch_scc1 .La5_LV_START
; ---- K tile (waves 0, 2): 256 keys x 128 columns, K = 512; LayerNorm of 8 rows of x in the gaps (softmax scale folded in)
.La5_LK_START:
mm a[96:99],v[192:195],v[160:163],0
s_add_u32 m0, s83, 33792
mm a[96:99],v[196:199],v[164:167],a[96:99]
ld v232,s[44:47],s80 offen lds
mm a[100:103],v[204:207],v[164:167],0
rd v[0:3],v236 offset:8192
rd v[4:7],v237 offset:8192
s_add_u32 m0, s83, 34816
mm a[100:103],v[200:203],v[160:163],a[100:103]
rd v[8:11],v236 offset:10240
rd v[12:15],v237 offset:10240
ld v233,s[44:47],s80 offen lds
mm a[104:107],v[208:211],v[160:163],0
s_add_u32 m0, s83, 35968
mm a[104:107],v[212:215],v[164:167],a[104:107]
ld v234,s[44:47],s80 offen lds
mm a[108:111],v[220:223],v[164:167],0
rd v[16:19],v236 offset:0
rd v[20:23],v237 offset:0
s_add_u32 m0, s83, 36992
mm a[108:111],v[216:219],v[160:163],a[108:111]
rd v[24:27],v236 offset:2048
rd v[28:31],v237 offset:2048
ld v235,s[44:47],s80 offen lds
mm a[140:143],v[216:219],v[168:171],0
s_mov_b32 s80, 2883584
mm a[140:143],v[220:223],v[172:175],a[140:143]
mm a[136:139],v[212:215],v[172:175],0
s_xor_b32 m0, s82, 32768
mm a[136:139],v[208:211],v[168:171],a[136:139]
rt v[32:33],v240 offset:128
rt v[34:35],v240 offset:1152
ld v224,s[40:43],s79 offen lds
mm a[124:127],v[200:203],v[168:171],0
s_xor_b32 m0, s82, 33792
mm a[124:127],v[204:207],v[172:175],a[124:127]
ld v225,s[40:43],s79 offen lds
mm a[120:123],v[196:199],v[172:175],0
s_xor_b32 m0, s82, 34816
mm a[120:123],v[192:195],v[168:171],a[120:123]
rt v[36:37],v240 offset:17024
rt v[38:39],v240 offset:18048
ld v226,s[40:43],s79 offen lds
mm a[232:235],v[192:195],v[176:179],0
s_xor_b32 m0, s82, 35840
mm a[232:235],v[196:199],v[180:183],a[232:235]
ld v227,s[40:43],s79 offen lds
mm a[236:239],v[204:207],v[180:183],0
s_xor_b32 m0, s82, 36864
mm a[236:239],v[200:203],v[176:179],a[236:239]
rt v[40:41],v241 offset:128
rt v[42:43],v241 offset:1152
ld v228,s[40:43],s79 offen lds
mm a[152:155],v[208:211],v[176:179],0
s_xor_b32 m0, s82, 37888
mm a[152:155],v[212:215],v[180:183],a[152:155]
ld v229,s[40:43],s79 offen lds
mm a[156:159],v[220:223],v[180:183],0
s_xor_b32 m0, s82, 38912
mm a[156:159],v[216:219],v[176:179],a[156:159]
ld v230,s[40:43],s79 offen lds
mm a[172:175],v[216:219],v[184:187],0
s_xor_b32 m0, s82, 39936
mm a[172:175],v[220:223],v[188:191],a[172:175]
ld v231,s[40:43],s79 offen lds
mm a[168:171],v[212:215],v[188:191],0
s_mov_b32 s79, 768
mm a[168:171],v[208:211],v[184:187],a[168:171]
rt v[44:45],v241 offset:17024
rt v[46:47],v241 offset:18048
mm a[220:223],v[200:203],v[184:187],0
s_add_u32 m0, s83, 50688
mm a[220:223],v[204:207],v[188:191],a[220:223]
ld v232,s[44:47],s80 offen lds
mm a[216:219],v[196:199],v[188:191],0
wt lgkmcnt(8)
s_add_u32 m0, s83, 51712
mm a[216:219],v[192:195],v[184:187],a[216:219]
ld v233,s[44:47],s80 offen lds
mm a[200:203],v[192:195],v[0:3],0
s_add_u32 m0, s83, 52864
mm a[200:203],v[196:199],v[4:7],a[200:203]
ld v234,s[44:47],s80 offen lds
mm a[204:207],v[204:207],v[4:7],0
s_add_u32 m0, s83, 53888
mm a[204:207],v[200:203],v[0:3],a[204:207]
rt v[48:49],v242 offset:128
rt v[50:51],v242 offset:1152
ld v235,s[44:47],s80 offen lds
mm a[184:187],v[208:211],v[0:3],0
s_mov_b32 s80, 3145728
mm a[184:187],v[212:215],v[4:7],a[184:187]
mm a[188:191],v[220:223],v[4:7],0
mm a[188:191],v[216:219],v[0:3],a[188:191]
mm a[12:15],v[216:219],v[8:11],0
mm a[12:15],v[220:223],v[12:15],a[12:15]
buffer_load_dword v70, v118, s[60:63], 0 offen
mm a[8:11],v[212:215],v[12:15],0
mm a[8:11],v[208:211],v[8:11],a[8:11]
rt v[52:53],v242 offset:17024
rt v[54:55],v242 offset:18048
mm a[76:79],v[200:203],v[8:11],0
mm a[76:79],v[204:207],v[12:15],a[76:79]
ld v[72:75],v119,s[60:63],0 offen offset:0
mm a[72:75],v[196:199],v[12:15],0
mm a[72:75],v[192:195],v[8:11],a[72:75]
rt v[56:57],v243 offset:128
rt v[58:59],v243 offset:1152
mm a[88:91],v[192:195],v[16:19],0
mm a[88:91],v[196:199],v[20:23],a[88:91]
ld v[76:79],v119,s[60:63],0 offen offset:128
mm a[92:95],v[204:207],v[20:23],0
mm a[92:95],v[200:203],v[16:19],a[92:95]
rt v[60:61],v243 offset:17024
rt v[62:63],v243 offset:18048
mm a[24:27],v[208:211],v[16:19],0
mm a[24:27],v[212:215],v[20:23],a[24:27]
ld v[80:83],v119,s[60:63],0 offen offset:256
mm a[28:31],v[220:223],v[20:23],0
mm a[28:31],v[216:219],v[16:19],a[28:31]
mm a[44:47],v[216:219],v[24:27],0
mm a[44:47],v[220:223],v[28:31],a[44:47]
ld v[84:87],v119,s[60:63],0 offen offset:384
mm a[40:43],v[212:215],v[28:31],0
mm a[40:43],v[208:211],v[24:27],a[40:43]
mm a[252:255],v[200:203],v[24:27],0
mm a[252:255],v[204:207],v[28:31],a[252:255]
mm a[248:251],v[196:199],v[28:31],0
mm a[248:251],v[192:195],v[24:27],a[248:251]
s_mov_b64 exec, 0
s_cmp_eq_u32 s3, 0
s_cbranch_scc1 .La5_LW2_0
s_branch .La5_LW2_R0
.La5_LW2_R0:
s_mov_b64 exec, -1
wt vmcnt(5) lgkmcnt(0)
s_barrier
mm a[112:115],v[32:35],v[160:163],0
mm a[112:115],v[36:39],v[164:167],a[112:115]
mm a[116:119],v[44:47],v[164:167],0
rt v[192:193],v240 offset:33792
rt v[194:195],v240 offset:34816
mm a[116:119],v[40:43],v[160:163],a[116:119]
rt v[196:197],v240 offset:50688
rt v[198:199],v240 offset:51712
mm a[128:131],v[48:51],v[160:163],0
mm a[128:131],v[52:55],v[164:167],a[128:131]
mm a[132:135],v[60:63],v[164:167],0
mm a[132:135],v[56:59],v[160:163],a[132:135]
rd v[160:163],v236 offset:57344
rd v[164:167],v237 offset:57344
mm a[148:151],v[56:59],v[168:171],0
mm a[148:151],v[60:63],v[172:175],a[148:151]
mm a[144:147],v[52:55],v[172:175],0
mm a[144:147],v[48:51],v[168:171],a[144:147]
rt v[200:201],v241 offset:33792
rt v[202:203],v241 offset:34816
mm a[228:231],v[40:43],v[168:171],0
mm a[228:231],v[44:47],v[172:175],a[228:231]
mm a[224:227],v[36:39],v[172:175],0
mm a[224:227],v[32:35],v[168:171],a[224:227]
rd v[168:171],v236 offset:59392
rd v[172:175],v237 offset:59392
mm a[208:211],v[32:35],v[176:179],0
s_xor_b32 m0, s82, 0
mm a[208:211],v[36:39],v[180:183],a[208:211]
ld v224,s[40:43],s79 offen lds
mm a[212:215],v[44:47],v[180:183],0
mm a[212:215],v[40:43],v[176:179],a[212:215]
rt v[204:205],v241 offset:50688
rt v[206:207],v241 offset:51712
s_xor_b32 m0, s82, 1024
mm a[160:163],v[48:51],v[176:179],0
ld v225,s[40:43],s79 offen lds
mm a[160:163],v[52:55],v[180:183],a[160:163]
mm a[164:167],v[60:63],v[180:183],0
s_xor_b32 m0, s82, 2048
mm a[164:167],v[56:59],v[176:179],a[164:167]
rd v[176:179],v236 offset:49152
rd v[180:183],v237 offset:49152
ld v226,s[40:43],s79 offen lds
mm a[180:183],v[56:59],v[184:187],0
mm a[180:183],v[60:63],v[188:191],a[180:183]
s_xor_b32 m0, s82, 3072
mm a[176:179],v[52:55],v[188:191],0
ld v227,s[40:43],s79 offen lds
mm a[176:179],v[48:51],v[184:187],a[176:179]
rt v[208:209],v242 offset:33792
rt v[210:211],v242 offset:34816
mm a[196:199],v[40:43],v[184:187],0
s_xor_b32 m0, s82, 4096
mm a[196:199],v[44:47],v[188:191],a[196:199]
ld v228,s[40:43],s79 offen lds
mm a[192:195],v[36:39],v[188:191],0
mm a[192:195],v[32:35],v[184:187],a[192:195]
rd v[184:187],v236 offset:51200
rd v[188:191],v237 offset:51200
s_xor_b32 m0, s82, 5120
mm a[64:67],v[32:35],v[0:3],0
ld v229,s[40:43],s79 offen lds
mm a[64:67],v[36:39],v[4:7],a[64:67]
mm a[68:71],v[44:47],v[4:7],0
s_xor_b32 m0, s82, 6144
mm a[68:71],v[40:43],v[0:3],a[68:71]
rt v[212:213],v242 offset:50688
rt v[214:215],v242 offset:51712
ld v230,s[40:43],s79 offen lds
mm a[0:3],v[48:51],v[0:3],0
mm a[0:3],v[52:55],v[4:7],a[0:3]
s_xor_b32 m0, s82, 7168
mm a[4:7],v[60:63],v[4:7],0
ld v231,s[40:43],s79 offen lds
mm a[4:7],v[56:59],v[0:3],a[4:7]
rd v[0:3],v236 offset:40960
rd v[4:7],v237 offset:40960
s_mov_b32 s79, 896
mm a[20:23],v[56:59],v[8:11],0
mm a[20:23],v[60:63],v[12:15],a[20:23]
s_add_u32 m0, s83, 0
mm a[16:19],v[52:55],v[12:15],0
ld v232,s[44:47],s80 offen lds
mm a[16:19],v[48:51],v[8:11],a[16:19]
rt v[216:217],v243 offset:33792
rt v[218:219],v243 offset:34816
mm a[84:87],v[40:43],v[8:11],0
s_add_u32 m0, s83, 1024
mm a[84:87],v[44:47],v[12:15],a[84:87]
ld v233,s[44:47],s80 offen lds
mm a[80:83],v[36:39],v[12:15],0
wt vmcnt(10)
v_lshlrev_b32 v70, 16, v70
mm a[80:83],v[32:35],v[8:11],a[80:83]
rd v[8:11],v236 offset:43008
rd v[12:15],v237 offset:43008
s_add_u32 m0, s83, 2176
mm a[240:243],v[32:35],v[16:19],0
ld v234,s[44:47],s80 offen lds
mm a[240:243],v[36:39],v[20:23],a[240:243]
v_lshlrev_b32 v88, 16, v72
v_and_b32 v89, 0xffff0000, v72
mm a[244:247],v[44:47],v[20:23],0
s_add_u32 m0, s83, 3200
v_sub_f32 v88, v88, v70
mm a[244:247],v[40:43],v[16:19],a[244:247]
rt v[220:221],v243 offset:50688
rt v[222:223],v243 offset:51712
ld v235,s[44:47],s80 offen lds
mm a[32:35],v[48:51],v[16:19],0
s_mov_b32 s80, 3407872
v_sub_f32 v89, v89, v70
mm a[32:35],v[52:55],v[20:23],a[32:35]
v_add_f32 v120, v88, v89
v_mul_f32 v122, v88, v88
mm a[36:39],v[60:63],v[20:23],0
s_add_u32 m0, s83, 16896
v_mul_f32 v123, v89, v89
mm a[36:39],v[56:59],v[16:19],a[36:39]
rd v[16:19],v236 offset:32768
rd v[20:23],v237 offset:32768
ld v232,s[44:47],s80 offen lds
mm a[60:63],v[56:59],v[24:27],0
v_lshlrev_b32 v90, 16, v73
v_and_b32 v91, 0xffff0000, v73
mm a[60:63],v[60:63],v[28:31],a[60:63]
s_add_u32 m0, s83, 17920
v_sub_f32 v90, v90, v70
mm a[56:59],v[52:55],v[28:31],0
ld v233,s[44:47],s80 offen lds
mm a[56:59],v[48:51],v[24:27],a[56:59]
v_sub_f32 v91, v91, v70
v_add_f32 v124, v90, v91
mm a[52:55],v[40:43],v[24:27],0
s_add_u32 m0, s83, 19072
v_fmac_f32 v122, v90, v90
mm a[52:55],v[44:47],v[28:31],a[52:55]
ld v234,s[44:47],s80 offen lds
mm a[48:51],v[36:39],v[28:31],0
wt lgkmcnt(2)
v_add_f32 v120, v120, v124
mm a[48:51],v[32:35],v[24:27],a[48:51]
rd v[24:27],v236 offset:34816
rd v[28:31],v237 offset:34816
s_add_u32 m0, s83, 20096
mm a[96:99],v[192:195],v[160:163],a[96:99]
ld v235,s[44:47],s80 offen lds
.La5_LW1_0:
s_cbranch_execz .La5_LW1_4
mm a[96:99],v[196:199],v[164:167],a[96:99]
s_mov_b32 s80, 3670016
v_fmac_f32 v123, v91, v91
mm a[100:103],v[204:207],v[164:167],a[100:103]
v_lshlrev_b32 v92, 16, v74
v_and_b32 v93, 0xffff0000, v74
mm a[100:103],v[200:203],v[160:163],a[100:103]
rt v[32:33],v240 offset:33920
rt v[34:35],v240 offset:34944
mm a[104:107],v[208:211],v[160:163],a[104:107]
v_sub_f32 v92, v92, v70
v_sub_f32 v93, v93, v70
mm a[104:107],v[212:215],v[164:167],a[104:107]
v_add_f32 v124, v92, v93
v_fmac_f32 v122, v92, v92
mm a[108:111],v[220:223],v[164:167],a[108:111]
wt lgkmcnt(2)
s_barrier
mm a[108:111],v[216:219],v[160:163],a[108:111]
v_add_f32 v120, v120, v124
v_fmac_f32 v123, v93, v93
mm a[140:143],v[216:219],v[168:171],a[140:143]
s_xor_b32 m0, s82, 32768
v_lshlrev_b32 v94, 16, v75
mm a[140:143],v[220:223],v[172:175],a[140:143]
ld v224,s[40:43],s79 offen lds
mm a[136:139],v[212:215],v[172:175],a[136:139]
v_and_b32 v95, 0xffff0000, v75
v_sub_f32 v94, v94, v70
mm a[136:139],v[208:211],v[168:171],a[136:139]
rt v[36:37],v240 offset:50816
rt v[38:39],v240 offset:51840
mm a[124:127],v[200:203],v[168:171],a[124:127]
s_xor_b32 m0, s82, 33792
v_sub_f32 v95, v95, v70
mm a[124:127],v[204:207],v[172:175],a[124:127]
ld v225,s[40:43],s79 offen lds
mm a[120:123],v[196:199],v[172:175],a[120:123]
v_add_f32 v124, v94, v95
v_fmac_f32 v122, v94, v94
mm a[120:123],v[192:195],v[168:171],a[120:123]
v_add_f32 v120, v120, v124
v_fmac_f32 v123, v95, v95
mm a[232:235],v[192:195],v[176:179],a[232:235]
s_xor_b32 m0, s82, 34816
v_lshlrev_b32 v96, 16, v76
mm a[232:235],v[196:199],v[180:183],a[232:235]
ld v226,s[40:43],s79 offen lds
mm a[236:239],v[204:207],v[180:183],a[236:239]
v_and_b32 v97, 0xffff0000, v76
v_sub_f32 v96, v96, v70
mm a[236:239],v[200:203],v[176:179],a[236:239]
rt v[40:41],v241 offset:33920
rt v[42:43],v241 offset:34944
mm a[152:155],v[208:211],v[176:179],a[152:155]
s_xor_b32 m0, s82, 35840
v_sub_f32 v97, v97, v70
mm a[152:155],v[212:215],v[180:183],a[152:155]
ld v227,s[40:43],s79 offen lds
mm a[156:159],v[220:223],v[180:183],a[156:159]
v_add_f32 v124, v96, v97
v_fmac_f32 v122, v96, v96
mm a[156:159],v[216:219],v[176:179],a[156:159]
v_add_f32 v120, v120, v124
v_fmac_f32 v123, v97, v97
mm a[172:175],v[216:219],v[184:187],a[172:175]
s_xor_b32 m0, s82, 36864
v_lshlrev_b32 v98, 16, v77
mm a[172:175],v[220:223],v[188:191],a[172:175]
ld v228,s[40:43],s79 offen lds
mm a[168:171],v[212:215],v[188:191],a[168:171]
v_and_b32 v99, 0xffff0000, v77
v_sub_f32 v98, v98, v70
mm a[168:171],v[208:211],v[184:187],a[168:171]
rt v[44:45],v241 offset:50816
rt v[46:47],v241 offset:51840
mm a[220:223],v[200:203],v[184:187],a[220:223]
s_xor_b32 m0, s82, 37888
v_sub_f32 v99, v99, v70
mm a[220:223],v[204:207],v[188:191],a[220:223]
ld v229,s[40:43],s79 offen lds
mm a[216:219],v[196:199],v[188:191],a[216:219]
v_add_f32 v124, v98, v99
v_fmac_f32 v122, v98, v98
mm a[216:219],v[192:195],v[184:187],a[216:219]
v_add_f32 v120, v120, v124
v_fmac_f32 v123, v99, v99
mm a[200:203],v[192:195],v[0:3],a[200:203]
s_xor_b32 m0, s82, 38912
v_lshlrev_b32 v100, 16, v78
mm a[200:203],v[196:199],v[4:7],a[200:203]
ld v230,s[40:43],s79 offen lds
mm a[204:207],v[204:207],v[4:7],a[204:207]
v_and_b32 v101, 0xffff0000, v78
v_sub_f32 v100, v100, v70
mm a[204:207],v[200:203],v[0:3],a[204:207]
rt v[48:49],v242 offset:33920
rt v[50:51],v242 offset:34944
mm a[184:187],v[208:211],v[0:3],a[184:187]
s_xor_b32 m0, s82, 39936
v_sub_f32 v101, v101, v70
mm a[184:187],v[212:215],v[4:7],a[184:187]
ld v231,s[40:43],s79 offen lds
mm a[188:191],v[220:223],v[4:7],a[188:191]
s_mov_b32 s79, 0
v_add_f32 v124, v100, v101
mm a[188:191],v[216:219],v[0:3],a[188:191]
v_fmac_f32 v122, v100, v100
v_add_f32 v120, v120, v124
mm a[12:15],v[216:219],v[8:11],a[12:15]
v_fmac_f32 v123, v101, v101
v_lshlrev_b32 v102, 16, v79
mm a[12:15],v[220:223],v[12:15],a[12:15]
buffer_load_dword v254, v132, s[44:47], s86 offen
mm a[8:11],v[212:215],v[12:15],a[8:11]
s_add_u32 s86, s86, 2097152
s_and_b32 s86, s86, 4194303
mm a[8:11],v[208:211],v[8:11],a[8:11]
rt v[52:53],v242 offset:50816
rt v[54:55],v242 offset:51840
mm a[76:79],v[200:203],v[8:11],a[76:79]
v_and_b32 v103, 0xffff0000, v79
v_sub_f32 v102, v102, v70
mm a[76:79],v[204:207],v[12:15],a[76:79]
v_sub_f32 v103, v103, v70
v_add_f32 v124, v102, v103
mm a[72:75],v[196:199],v[12:15],a[72:75]
v_fmac_f32 v122, v102, v102
v_add_f32 v120, v120, v124
mm a[72:75],v[192:195],v[8:11],a[72:75]
rt v[56:57],v243 offset:33920
rt v[58:59],v243 offset:34944
mm a[88:91],v[192:195],v[16:19],a[88:91]
v_fmac_f32 v123, v103, v103
v_lshlrev_b32 v104, 16, v80
mm a[88:91],v[196:199],v[20:23],a[88:91]
v_and_b32 v105, 0xffff0000, v80
v_sub_f32 v104, v104, v70
mm a[92:95],v[204:207],v[20:23],a[92:95]
v_sub_f32 v105, v105, v70
v_add_f32 v124, v104, v105
mm a[92:95],v[200:203],v[16:19],a[92:95]
rt v[60:61],v243 offset:50816
rt v[62:63],v243 offset:51840
mm a[24:27],v[208:211],v[16:19],a[24:27]
v_fmac_f32 v122, v104, v104
v_add_f32 v120, v120, v124
mm a[24:27],v[212:215],v[20:23],a[24:27]
v_fmac_f32 v123, v105, v105
v_lshlrev_b32 v106, 16, v81
mm a[28:31],v[220:223],v[20:23],a[28:31]
v_and_b32 v107, 0xffff0000, v81
v_sub_f32 v106, v106, v70
mm a[28:31],v[216:219],v[16:19],a[28:31]
v_sub_f32 v107, v107, v70
v_add_f32 v124, v106, v107
mm a[44:47],v[216:219],v[24:27],a[44:47]
v_fmac_f32 v122, v106, v106
v_add_f32 v120, v120, v124
mm a[44:47],v[220:223],v[28:31],a[44:47]
v_fmac_f32 v123, v107, v107
v_lshlrev_b32 v108, 16, v82
mm a[40:43],v[212:215],v[28:31],a[40:43]
v_and_b32 v109, 0xffff0000, v82
v_sub_f32 v108, v108, v70
mm a[40:43],v[208:211],v[24:27],a[40:43]
v_sub_f32 v109, v109, v70
v_add_f32 v124, v108, v109
mm a[252:255],v[200:203],v[24:27],a[252:255]
v_fmac_f32 v122, v108, v108
v_add_f32 v120, v120, v124
mm a[252:255],v[204:207],v[28:31],a[252:255]
v_fmac_f32 v123, v109, v109
v_lshlrev_b32 v110, 16, v83
mm a[248:251],v[196:199],v[28:31],a[248:251]
wt vmcnt(9) lgkmcnt(0)
s_barrier
mm a[248:251],v[192:195],v[24:27],a[248:251]
v_and_b32 v111, 0xffff0000, v83
v_sub_f32 v110, v110, v70
mm a[112:115],v[32:35],v[160:163],a[112:115]
v_sub_f32 v111, v111, v70
v_add_f32 v124, v110, v111
mm a[112:115],v[36:39],v[164:167],a[112:115]
v_fmac_f32 v122, v110, v110
v_add_f32 v120, v120, v124
mm a[116:119],v[44:47],v[164:167],a[116:119]
rt v[192:193],v240 offset:0
rt v[194:195],v240 offset:1024
mm a[116:119],v[40:43],v[160:163],a[116:119]
rt v[196:197],v240 offset:16896
rt v[198:199],v240 offset:17920
mm a[128:131],v[48:51],v[160:163],a[128:131]
v_fmac_f32 v123, v111, v111
v_lshlrev_b32 v112, 16, v84
mm a[128:131],v[52:55],v[164:167],a[128:131]
v_and_b32 v113, 0xffff0000, v84
v_sub_f32 v112, v112, v70
mm a[132:135],v[60:63],v[164:167],a[132:135]
v_sub_f32 v113, v113, v70
v_add_f32 v124, v112, v113
mm a[132:135],v[56:59],v[160:163],a[132:135]
rd v[160:163],v236 offset:24576
rd v[164:167],v237 offset:24576
mm a[148:151],v[56:59],v[168:171],a[148:151]
v_fmac_f32 v122, v112, v112
v_add_f32 v120, v120, v124
mm a[148:151],v[60:63],v[172:175],a[148:151]
v_fmac_f32 v123, v113, v113
v_lshlrev_b32 v114, 16, v85
mm a[144:147],v[52:55],v[172:175],a[144:147]
v_and_b32 v115, 0xffff0000, v85
v_sub_f32 v114, v114, v70
mm a[144:147],v[48:51],v[168:171],a[144:147]
rt v[200:201],v241 offset:0
rt v[202:203],v241 offset:1024
mm a[228:231],v[40:43],v[168:171],a[228:231]
v_sub_f32 v115, v115, v70
v_add_f32 v124, v114, v115
mm a[228:231],v[44:47],v[172:175],a[228:231]
v_fmac_f32 v122, v114, v114
v_add_f32 v120, v120, v124
mm a[224:227],v[36:39],v[172:175],a[224:227]
v_fmac_f32 v123, v115, v115
v_lshlrev_b32 v116, 16, v86
mm a[224:227],v[32:35],v[168:171],a[224:227]
rd v[168:171],v236 offset:26624
rd v[172:175],v237 offset:26624
mm a[208:211],v[32:35],v[176:179],a[208:211]
v_and_b32 v117, 0xffff0000, v86
v_sub_f32 v116, v116, v70
mm a[208:211],v[36:39],v[180:183],a[208:211]
v_sub_f32 v117, v117, v70
v_add_f32 v124, v116, v117
mm a[212:215],v[44:47],v[180:183],a[212:215]
v_fmac_f32 v122, v116, v116
v_add_f32 v120, v120, v124
mm a[212:215],v[40:43],v[176:179],a[212:215]
rt v[204:205],v241 offset:16896
rt v[206:207],v241 offset:17920
mm a[160:163],v[48:51],v[176:179],a[160:163]
v_fmac_f32 v123, v117, v117
v_lshlrev_b32 v118, 16, v87
mm a[160:163],v[52:55],v[180:183],a[160:163]
v_and_b32 v119, 0xffff0000, v87
v_sub_f32 v118, v118, v70
mm a[164:167],v[60:63],v[180:183],a[164:167]
v_sub_f32 v119, v119, v70
v_add_f32 v124, v118, v119
mm a[164:167],v[56:59],v[176:179],a[164:167]
rd v[176:179],v236 offset:16384
rd v[180:183],v237 offset:16384
mm a[180:183],v[56:59],v[184:187],a[180:183]
v_fmac_f32 v122, v118, v118
v_add_f32 v120, v120, v124
mm a[180:183],v[60:63],v[188:191],a[180:183]
v_fmac_f32 v123, v119, v119
v_add_f32 v121, v122, v123
mm a[176:179],v[52:55],v[188:191],a[176:179]
v_add_f32_dpp v120, v120, v120 row_ror:8 row_mask:0xf bank_mask:0xf
mm a[176:179],v[48:51],v[184:187],a[176:179]
rt v[208:209],v242 offset:0
rt v[210:211],v242 offset:1024
mm a[196:199],v[40:43],v[184:187],a[196:199]
s_add_u32 m0, s83, 33792
v_add_f32_dpp v121, v121, v121 row_ror:8 row_mask:0xf bank_mask:0xf
mm a[196:199],v[44:47],v[188:191],a[196:199]
ld v232,s[44:47],s80 offen lds
mm a[192:195],v[36:39],v[188:191],a[192:195]
v_mov_b32 v124, v120
v_mov_b32 v125, v121
mm a[192:195],v[32:35],v[184:187],a[192:195]
rd v[184:187],v236 offset:18432
rd v[188:191],v237 offset:18432
mm a[64:67],v[32:35],v[0:3],a[64:67]
s_add_u32 m0, s83, 34816
v_permlane16_swap_b32 v124, v120
mm a[64:67],v[36:39],v[4:7],a[64:67]
ld v233,s[44:47],s80 offen lds
mm a[68:71],v[44:47],v[4:7],a[68:71]
v_permlane16_swap_b32 v125, v121
mm a[68:71],v[40:43],v[0:3],a[68:71]
rt v[212:213],v242 offset:16896
rt v[214:215],v242 offset:17920
mm a[0:3],v[48:51],v[0:3],a[0:3]
s_add_u32 m0, s83, 35968
v_add_f32 v120, v120, v124
mm a[0:3],v[52:55],v[4:7],a[0:3]
ld v234,s[44:47],s80 offen lds
mm a[4:7],v[60:63],v[4:7],a[4:7]
v_add_f32 v121, v121, v125
v_mov_b32 v124, v120
mm a[4:7],v[56:59],v[0:3],a[4:7]
rd v[0:3],v236 offset:8192
rd v[4:7],v237 offset:8192
mm a[20:23],v[56:59],v[8:11],a[20:23]
s_add_u32 m0, s83, 36992
v_mov_b32 v125, v121
mm a[20:23],v[60:63],v[12:15],a[20:23]
ld v235,s[44:47],s80 offen lds
mm a[16:19],v[52:55],v[12:15],a[16:19]
s_mov_b32 s80, 3932160
v_permlane32_swap_b32 v124, v120
mm a[16:19],v[48:51],v[8:11],a[16:19]
rt v[216:217],v243 offset:0
rt v[218:219],v243 offset:1024
mm a[84:87],v[40:43],v[8:11],a[84:87]
s_add_u32 m0, s83, 50688
v_permlane32_swap_b32 v125, v121
mm a[84:87],v[44:47],v[12:15],a[84:87]
ld v232,s[44:47],s80 offen lds
mm a[80:83],v[36:39],v[12:15],a[80:83]
v_add_f32 v120, v120, v124
v_add_f32 v121, v121, v125
mm a[80:83],v[32:35],v[8:11],a[80:83]
rd v[8:11],v236 offset:10240
rd v[12:15],v237 offset:10240
mm a[240:243],v[32:35],v[16:19],a[240:243]
s_add_u32 m0, s83, 51712
ds_write_b64 v127, v[120:121]
mm a[240:243],v[36:39],v[20:23],a[240:243]
ld v233,s[44:47],s80 offen lds
mm a[244:247],v[44:47],v[20:23],a[244:247]
s_nop 0
v_readfirstlane_b32 s92, v252
mm a[244:247],v[40:43],v[16:19],a[244:247]
rt v[220:221],v243 offset:16896
rt v[222:223],v243 offset:17920
mm a[32:35],v[48:51],v[16:19],a[32:35]
s_add_u32 m0, s83, 52864
v_readfirstlane_b32 s93, v253
mm a[32:35],v[52:55],v[20:23],a[32:35]
ld v234,s[44:47],s80 offen lds
mm a[36:39],v[60:63],v[20:23],a[36:39]
s_add_u32 s92, s92, 1
s_addc_u32 s93, s93, 0
mm a[36:39],v[56:59],v[16:19],a[36:39]
rd v[16:19],v236 offset:0
rd v[20:23],v237 offset:0
mm a[60:63],v[56:59],v[24:27],a[60:63]
s_add_u32 m0, s83, 53888
v_mov_b32 v252, s92
mm a[60:63],v[60:63],v[28:31],a[60:63]
ld v235,s[44:47],s80 offen lds
mm a[56:59],v[52:55],v[28:31],a[56:59]
s_mov_b32 s80, 0
v_mov_b32 v253, s93
mm a[56:59],v[48:51],v[24:27],a[56:59]
v_mov_b32 v128, 0
v_mov_b32 v129, 0
mm a[52:55],v[40:43],v[24:27],a[52:55]
v_mov_b32 v130, 0
v_mov_b32 v131, 0
mm a[52:55],v[44:47],v[28:31],a[52:55]
st v[128:131],v255,s[52:55],s95 offen sc1
mm a[48:51],v[36:39],v[28:31],a[48:51]
wt lgkmcnt(2)
mm a[48:51],v[32:35],v[24:27],a[48:51]
rd v[24:27],v236 offset:2048
rd v[28:31],v237 offset:2048
mm a[96:99],v[192:195],v[160:163],a[96:99]
mm a[96:99],v[196:199],v[164:167],a[96:99]
mm a[100:103],v[204:207],v[164:167],a[100:103]
mm a[100:103],v[200:203],v[160:163],a[100:103]
rt v[32:33],v240 offset:128
rt v[34:35],v240 offset:1152
mm a[104:107],v[208:211],v[160:163],a[104:107]
mm a[104:107],v[212:215],v[164:167],a[104:107]
mm a[108:111],v[220:223],v[164:167],a[108:111]
wt lgkmcnt(2)
s_barrier
mm a[108:111],v[216:219],v[160:163],a[108:111]
rd v[72:75],v126
rd v[76:79],v126 offset:16
mm a[140:143],v[216:219],v[168:171],a[140:143]
s_xor_b32 m0, s82, 0
mm a[140:143],v[220:223],v[172:175],a[140:143]
ld v224,s[40:43],s79 offen lds
mm a[136:139],v[212:215],v[172:175],a[136:139]
mm a[136:139],v[208:211],v[168:171],a[136:139]
rt v[36:37],v240 offset:17024
rt v[38:39],v240 offset:18048
mm a[124:127],v[200:203],v[168:171],a[124:127]
s_xor_b32 m0, s82, 1024
mm a[124:127],v[204:207],v[172:175],a[124:127]
ld v225,s[40:43],s79 offen lds
mm a[120:123],v[196:199],v[172:175],a[120:123]
wt lgkmcnt(2)
v_add_f32 v72, v72, v74
mm a[120:123],v[192:195],v[168:171],a[120:123]
v_add_f32 v73, v73, v75
v_add_f32 v76, v76, v78
mm a[232:235],v[192:195],v[176:179],a[232:235]
s_xor_b32 m0, s82, 2048
v_add_f32 v77, v77, v79
mm a[232:235],v[196:199],v[180:183],a[232:235]
ld v226,s[40:43],s79 offen lds
mm a[236:239],v[204:207],v[180:183],a[236:239]
v_add_f32 v72, v72, v76
v_add_f32 v73, v73, v77
mm a[236:239],v[200:203],v[176:179],a[236:239]
rt v[40:41],v241 offset:128
rt v[42:43],v241 offset:1152
mm a[152:155],v[208:211],v[176:179],a[152:155]
s_xor_b32 m0, s82, 3072
v_mul_f32 v72, 0x3a800000, v72
mm a[152:155],v[212:215],v[180:183],a[152:155]
ld v227,s[40:43],s79 offen lds
mm a[156:159],v[220:223],v[180:183],a[156:159]
v_mul_f32 v73, 0x3a800000, v73
v_add_f32 v124, v70, v72
mm a[156:159],v[216:219],v[176:179],a[156:159]
v_fma_f32 v125, -v72, v72, v73
cv v124,v124,v124
mm a[172:175],v[216:219],v[184:187],a[172:175]
s_xor_b32 m0, s82, 4096
v_lshlrev_b32 v124, 16, v124
mm a[172:175],v[220:223],v[188:191],a[172:175]
ld v228,s[40:43],s79 offen lds
mm a[168:171],v[212:215],v[188:191],a[168:171]
v_max_f32 v125, 0, v125
cv v125,v125,v125
mm a[168:171],v[208:211],v[184:187],a[168:171]
rt v[44:45],v241 offset:17024
rt v[46:47],v241 offset:18048
mm a[220:223],v[200:203],v[184:187],a[220:223]
s_xor_b32 m0, s82, 5120
v_lshlrev_b32 v125, 16, v125
mm a[220:223],v[204:207],v[188:191],a[220:223]
ld v229,s[40:43],s79 offen lds
mm a[216:219],v[196:199],v[188:191],a[216:219]
v_add_f32 v125, 0x3727c5ac, v125
cv v125,v125,v125
mm a[216:219],v[192:195],v[184:187],a[216:219]
v_lshlrev_b32 v125, 16, v125
mm a[200:203],v[192:195],v[0:3],a[200:203]
s_xor_b32 m0, s82, 6144
mm a[200:203],v[196:199],v[4:7],a[200:203]
ld v230,s[40:43],s79 offen lds
mm a[204:207],v[204:207],v[4:7],a[204:207]
v_sqrt_f32 v125, v125
mm a[204:207],v[200:203],v[0:3],a[204:207]
rt v[48:49],v242 offset:128
rt v[50:51],v242 offset:1152
mm a[184:187],v[208:211],v[0:3],a[184:187]
s_xor_b32 m0, s82, 7168
v_sub_f32 v124, v70, v124
mm a[184:187],v[212:215],v[4:7],a[184:187]
ld v231,s[40:43],s79 offen lds
mm a[188:191],v[220:223],v[4:7],a[188:191]
s_mov_b32 s79, 128
cv v125,v125,v125
mm a[188:191],v[216:219],v[0:3],a[188:191]
v_lshlrev_b32 v125, 16, v125
mm a[12:15],v[216:219],v[8:11],a[12:15]
v_rcp_f32 v125, v125
mm a[12:15],v[220:223],v[12:15],a[12:15]
v_mul_f32 v125, 0x3e0293ee, v125
v_mul_f32 v124, v124, v125
mm a[8:11],v[212:215],v[12:15],a[8:11]
v_fma_f32 v88, v88, v125, v124
v_fma_f32 v89, v89, v125, v124
mm a[8:11],v[208:211],v[8:11],a[8:11]
rt v[52:53],v242 offset:17024
rt v[54:55],v242 offset:18048
mm a[76:79],v[200:203],v[8:11],a[76:79]
cv v72,v88,v89
v_fma_f32 v90, v90, v125, v124
mm a[76:79],v[204:207],v[12:15],a[76:79]
v_fma_f32 v91, v91, v125, v124
cv v73,v90,v91
mm a[72:75],v[196:199],v[12:15],a[72:75]
v_fma_f32 v92, v92, v125, v124
v_fma_f32 v93, v93, v125, v124
mm a[72:75],v[192:195],v[8:11],a[72:75]
rt v[56:57],v243 offset:128
rt v[58:59],v243 offset:1152
mm a[88:91],v[192:195],v[16:19],a[88:91]
cv v74,v92,v93
v_fma_f32 v94, v94, v125, v124
mm a[88:91],v[196:199],v[20:23],a[88:91]
v_fma_f32 v95, v95, v125, v124
cv v75,v94,v95
mm a[92:95],v[204:207],v[20:23],a[92:95]
st v[72:75],v71,s[56:59],0 offen offset:0 sc1
mm a[92:95],v[200:203],v[16:19],a[92:95]
rt v[60:61],v243 offset:17024
rt v[62:63],v243 offset:18048
mm a[24:27],v[208:211],v[16:19],a[24:27]
v_fma_f32 v96, v96, v125, v124
v_fma_f32 v97, v97, v125, v124
mm a[24:27],v[212:215],v[20:23],a[24:27]
cv v76,v96,v97
v_fma_f32 v98, v98, v125, v124
mm a[28:31],v[220:223],v[20:23],a[28:31]
v_fma_f32 v99, v99, v125, v124
cv v77,v98,v99
mm a[28:31],v[216:219],v[16:19],a[28:31]
v_fma_f32 v100, v100, v125, v124
v_fma_f32 v101, v101, v125, v124
mm a[44:47],v[216:219],v[24:27],a[44:47]
cv v78,v100,v101
v_fma_f32 v102, v102, v125, v124
mm a[44:47],v[220:223],v[28:31],a[44:47]
v_fma_f32 v103, v103, v125, v124
cv v79,v102,v103
mm a[40:43],v[212:215],v[28:31],a[40:43]
st v[76:79],v71,s[56:59],0 offen offset:2048 sc1
mm a[40:43],v[208:211],v[24:27],a[40:43]
v_fma_f32 v104, v104, v125, v124
v_fma_f32 v105, v105, v125, v124
mm a[252:255],v[200:203],v[24:27],a[252:255]
cv v80,v104,v105
v_fma_f32 v106, v106, v125, v124
mm a[252:255],v[204:207],v[28:31],a[252:255]
v_fma_f32 v107, v107, v125, v124
cv v81,v106,v107
mm a[248:251],v[196:199],v[28:31],a[248:251]
wt vmcnt(11) lgkmcnt(0)
s_barrier
mm a[248:251],v[192:195],v[24:27],a[248:251]
v_fma_f32 v108, v108, v125, v124
v_fma_f32 v109, v109, v125, v124
mm a[112:115],v[32:35],v[160:163],a[112:115]
cv v82,v108,v109
v_fma_f32 v110, v110, v125, v124
mm a[112:115],v[36:39],v[164:167],a[112:115]
v_fma_f32 v111, v111, v125, v124
cv v83,v110,v111
mm a[116:119],v[44:47],v[164:167],a[116:119]
rt v[192:193],v240 offset:33792
rt v[194:195],v240 offset:34816
mm a[116:119],v[40:43],v[160:163],a[116:119]
rt v[196:197],v240 offset:50688
rt v[198:199],v240 offset:51712
mm a[128:131],v[48:51],v[160:163],a[128:131]
st v[80:83],v71,s[56:59],s88 offen offset:0 sc1
mm a[128:131],v[52:55],v[164:167],a[128:131]
v_fma_f32 v112, v112, v125, v124
v_fma_f32 v113, v113, v125, v124
mm a[132:135],v[60:63],v[164:167],a[132:135]
cv v84,v112,v113
v_fma_f32 v114, v114, v125, v124
mm a[132:135],v[56:59],v[160:163],a[132:135]
rd v[160:163],v236 offset:57344
rd v[164:167],v237 offset:57344
mm a[148:151],v[56:59],v[168:171],a[148:151]
v_fma_f32 v115, v115, v125, v124
cv v85,v114,v115
mm a[148:151],v[60:63],v[172:175],a[148:151]
v_fma_f32 v116, v116, v125, v124
v_fma_f32 v117, v117, v125, v124
mm a[144:147],v[52:55],v[172:175],a[144:147]
cv v86,v116,v117
v_fma_f32 v118, v118, v125, v124
mm a[144:147],v[48:51],v[168:171],a[144:147]
rt v[200:201],v241 offset:33792
rt v[202:203],v241 offset:34816
mm a[228:231],v[40:43],v[168:171],a[228:231]
v_fma_f32 v119, v119, v125, v124
cv v87,v118,v119
mm a[228:231],v[44:47],v[172:175],a[228:231]
st v[84:87],v71,s[56:59],s88 offen offset:2048 sc1
mm a[224:227],v[36:39],v[172:175],a[224:227]
mm a[224:227],v[32:35],v[168:171],a[224:227]
rd v[168:171],v236 offset:59392
rd v[172:175],v237 offset:59392
mm a[208:211],v[32:35],v[176:179],a[208:211]
mm a[208:211],v[36:39],v[180:183],a[208:211]
mm a[212:215],v[44:47],v[180:183],a[212:215]
mm a[212:215],v[40:43],v[176:179],a[212:215]
rt v[204:205],v241 offset:50688
rt v[206:207],v241 offset:51712
mm a[160:163],v[48:51],v[176:179],a[160:163]
mm a[160:163],v[52:55],v[180:183],a[160:163]
mm a[164:167],v[60:63],v[180:183],a[164:167]
mm a[164:167],v[56:59],v[176:179],a[164:167]
rd v[176:179],v236 offset:49152
rd v[180:183],v237 offset:49152
mm a[180:183],v[56:59],v[184:187],a[180:183]
mm a[180:183],v[60:63],v[188:191],a[180:183]
mm a[176:179],v[52:55],v[188:191],a[176:179]
mm a[176:179],v[48:51],v[184:187],a[176:179]
rt v[208:209],v242 offset:33792
rt v[210:211],v242 offset:34816
mm a[196:199],v[40:43],v[184:187],a[196:199]
s_add_u32 m0, s83, 0
mm a[196:199],v[44:47],v[188:191],a[196:199]
.La5_LW1_1:
s_cbranch_execz .La5_LW1_5
ld v232,s[44:47],s80 offen lds
mm a[192:195],v[36:39],v[188:191],a[192:195]
mm a[192:195],v[32:35],v[184:187],a[192:195]
rd v[184:187],v236 offset:51200
rd v[188:191],v237 offset:51200
mm a[64:67],v[32:35],v[0:3],a[64:67]
s_add_u32 m0, s83, 1024
mm a[64:67],v[36:39],v[4:7],a[64:67]
ld v233,s[44:47],s80 offen lds
mm a[68:71],v[44:47],v[4:7],a[68:71]
mm a[68:71],v[40:43],v[0:3],a[68:71]
rt v[212:213],v242 offset:50688
rt v[214:215],v242 offset:51712
mm a[0:3],v[48:51],v[0:3],a[0:3]
s_add_u32 m0, s83, 2176
mm a[0:3],v[52:55],v[4:7],a[0:3]
ld v234,s[44:47],s80 offen lds
mm a[4:7],v[60:63],v[4:7],a[4:7]
mm a[4:7],v[56:59],v[0:3],a[4:7]
rd v[0:3],v236 offset:40960
rd v[4:7],v237 offset:40960
mm a[20:23],v[56:59],v[8:11],a[20:23]
s_add_u32 m0, s83, 3200
mm a[20:23],v[60:63],v[12:15],a[20:23]
ld v235,s[44:47],s80 offen lds
mm a[16:19],v[52:55],v[12:15],a[16:19]
s_mov_b32 s80, 262144
mm a[16:19],v[48:51],v[8:11],a[16:19]
rt v[216:217],v243 offset:33792
rt v[218:219],v243 offset:34816
mm a[84:87],v[40:43],v[8:11],a[84:87]
s_add_u32 m0, s83, 16896
mm a[84:87],v[44:47],v[12:15],a[84:87]
ld v232,s[44:47],s80 offen lds
mm a[80:83],v[36:39],v[12:15],a[80:83]
mm a[80:83],v[32:35],v[8:11],a[80:83]
rd v[8:11],v236 offset:43008
rd v[12:15],v237 offset:43008
mm a[240:243],v[32:35],v[16:19],a[240:243]
s_add_u32 m0, s83, 17920
mm a[240:243],v[36:39],v[20:23],a[240:243]
ld v233,s[44:47],s80 offen lds
mm a[244:247],v[44:47],v[20:23],a[244:247]
mm a[244:247],v[40:43],v[16:19],a[244:247]
rt v[220:221],v243 offset:50688
rt v[222:223],v243 offset:51712
mm a[32:35],v[48:51],v[16:19],a[32:35]
s_add_u32 m0, s83, 19072
mm a[32:35],v[52:55],v[20:23],a[32:35]
ld v234,s[44:47],s80 offen lds
mm a[36:39],v[60:63],v[20:23],a[36:39]
mm a[36:39],v[56:59],v[16:19],a[36:39]
rd v[16:19],v236 offset:32768
rd v[20:23],v237 offset:32768
mm a[60:63],v[56:59],v[24:27],a[60:63]
s_add_u32 m0, s83, 20096
mm a[60:63],v[60:63],v[28:31],a[60:63]
ld v235,s[44:47],s80 offen lds
mm a[56:59],v[52:55],v[28:31],a[56:59]
s_mov_b32 s80, 524288
mm a[56:59],v[48:51],v[24:27],a[56:59]
mm a[52:55],v[40:43],v[24:27],a[52:55]
mm a[52:55],v[44:47],v[28:31],a[52:55]
mm a[48:51],v[36:39],v[28:31],a[48:51]
wt lgkmcnt(2)
mm a[48:51],v[32:35],v[24:27],a[48:51]
rd v[24:27],v236 offset:34816
rd v[28:31],v237 offset:34816
mm a[96:99],v[192:195],v[160:163],a[96:99]
mm a[96:99],v[196:199],v[164:167],a[96:99]
mm a[100:103],v[204:207],v[164:167],a[100:103]
mm a[100:103],v[200:203],v[160:163],a[100:103]
rt v[32:33],v240 offset:33920
rt v[34:35],v240 offset:34944
mm a[104:107],v[208:211],v[160:163],a[104:107]
mm a[104:107],v[212:215],v[164:167],a[104:107]
mm a[108:111],v[220:223],v[164:167],a[108:111]
wt lgkmcnt(2)
s_barrier
mm a[108:111],v[216:219],v[160:163],a[108:111]
mm a[140:143],v[216:219],v[168:171],a[140:143]
s_xor_b32 m0, s82, 32768
mm a[140:143],v[220:223],v[172:175],a[140:143]
ld v224,s[40:43],s79 offen lds
mm a[136:139],v[212:215],v[172:175],a[136:139]
mm a[136:139],v[208:211],v[168:171],a[136:139]
rt v[36:37],v240 offset:50816
rt v[38:39],v240 offset:51840
mm a[124:127],v[200:203],v[168:171],a[124:127]
s_xor_b32 m0, s82, 33792
mm a[124:127],v[204:207],v[172:175],a[124:127]
ld v225,s[40:43],s79 offen lds
mm a[120:123],v[196:199],v[172:175],a[120:123]
mm a[120:123],v[192:195],v[168:171],a[120:123]
mm a[232:235],v[192:195],v[176:179],a[232:235]
s_xor_b32 m0, s82, 34816
mm a[232:235],v[196:199],v[180:183],a[232:235]
ld v226,s[40:43],s79 offen lds
mm a[236:239],v[204:207],v[180:183],a[236:239]
mm a[236:239],v[200:203],v[176:179],a[236:239]
rt v[40:41],v241 offset:33920
rt v[42:43],v241 offset:34944
mm a[152:155],v[208:211],v[176:179],a[152:155]
s_xor_b32 m0, s82, 35840
mm a[152:155],v[212:215],v[180:183],a[152:155]
ld v227,s[40:43],s79 offen lds
mm a[156:159],v[220:223],v[180:183],a[156:159]
mm a[156:159],v[216:219],v[176:179],a[156:159]
mm a[172:175],v[216:219],v[184:187],a[172:175]
s_xor_b32 m0, s82, 36864
mm a[172:175],v[220:223],v[188:191],a[172:175]
ld v228,s[40:43],s79 offen lds
mm a[168:171],v[212:215],v[188:191],a[168:171]
mm a[168:171],v[208:211],v[184:187],a[168:171]
rt v[44:45],v241 offset:50816
rt v[46:47],v241 offset:51840
mm a[220:223],v[200:203],v[184:187],a[220:223]
s_xor_b32 m0, s82, 37888
mm a[220:223],v[204:207],v[188:191],a[220:223]
ld v229,s[40:43],s79 offen lds
mm a[216:219],v[196:199],v[188:191],a[216:219]
mm a[216:219],v[192:195],v[184:187],a[216:219]
mm a[200:203],v[192:195],v[0:3],a[200:203]
s_xor_b32 m0, s82, 38912
mm a[200:203],v[196:199],v[4:7],a[200:203]
ld v230,s[40:43],s79 offen lds
mm a[204:207],v[204:207],v[4:7],a[204:207]
mm a[204:207],v[200:203],v[0:3],a[204:207]
rt v[48:49],v242 offset:33920
rt v[50:51],v242 offset:34944
mm a[184:187],v[208:211],v[0:3],a[184:187]
s_xor_b32 m0, s82, 39936
mm a[184:187],v[212:215],v[4:7],a[184:187]
ld v231,s[40:43],s79 offen lds
mm a[188:191],v[220:223],v[4:7],a[188:191]
s_mov_b32 s79, 256
mm a[188:191],v[216:219],v[0:3],a[188:191]
mm a[12:15],v[216:219],v[8:11],a[12:15]
mm a[12:15],v[220:223],v[12:15],a[12:15]
buffer_load_dword v254, v135, s[68:71], s96 offen
mm a[8:11],v[212:215],v[12:15],a[8:11]
s_add_u32 s96, s96, 1048576
mm a[8:11],v[208:211],v[8:11],a[8:11]
rt v[52:53],v242 offset:50816
rt v[54:55],v242 offset:51840
mm a[76:79],v[200:203],v[8:11],a[76:79]
mm a[76:79],v[204:207],v[12:15],a[76:79]
buffer_load_dword v254, v135, s[68:71], s96 offen
mm a[72:75],v[196:199],v[12:15],a[72:75]
s_add_u32 s96, s96, 1048576
mm a[72:75],v[192:195],v[8:11],a[72:75]
rt v[56:57],v243 offset:33920
rt v[58:59],v243 offset:34944
mm a[88:91],v[192:195],v[16:19],a[88:91]
mm a[88:91],v[196:199],v[20:23],a[88:91]
wt vmcnt(18)
buffer_store_dwordx2 v[252:253], off, s[64:67], s91 offset:1024 sc1
mm a[92:95],v[204:207],v[20:23],a[92:95]
s_mov_b32 s91, 0
s_lshl_b32 s92, s76, 19
mm a[92:95],v[200:203],v[16:19],a[92:95]
rt v[60:61],v243 offset:50816
rt v[62:63],v243 offset:51840
mm a[24:27],v[208:211],v[16:19],a[24:27]
s_add_u32 s92, s92, 88080384
mm a[24:27],v[212:215],v[20:23],a[24:27]
s_add_u32 s52, s26, s92
s_addc_u32 s53, s27, 0
mm a[28:31],v[220:223],v[20:23],a[28:31]
s_and_b32 s53, s53, 0xffff
s_mov_b32 s54, 524672
mm a[28:31],v[216:219],v[16:19],a[28:31]
s_mov_b32 s55, 0x20000
s_lshl_b32 s92, s3, 16
mm a[44:47],v[216:219],v[24:27],a[44:47]
s_sub_u32 s93, 458752, s92
v_add_u32 v244, s92, v136
mm a[44:47],v[220:223],v[28:31],a[44:47]
v_add_u32 v246, s93, v136
v_add_u32 v245, 0x8000, v244
mm a[40:43],v[212:215],v[28:31],a[40:43]
v_add_u32 v247, 0x8000, v246
s_lshl_b32 s94, s72, 19
mm a[40:43],v[208:211],v[24:27],a[40:43]
s_lshl_b32 s93, s76, 16
s_add_u32 s92, s94, s93
mm a[252:255],v[200:203],v[24:27],a[252:255]
s_lshl_b32 s93, s77, 13
s_add_u32 s92, s92, s93
mm a[252:255],v[204:207],v[28:31],a[252:255]
s_add_u32 s92, s92, 20971520
mm a[248:251],v[196:199],v[28:31],a[248:251]
wt vmcnt(11) lgkmcnt(0)
s_barrier
mm a[248:251],v[192:195],v[24:27],a[248:251]
s_add_u32 s48, s26, s92
s_addc_u32 s49, s27, 0
mm a[112:115],v[32:35],v[160:163],a[112:115]
s_and_b32 s49, s49, 0xffff
s_mov_b32 s50, 57344
mm a[112:115],v[36:39],v[164:167],a[112:115]
s_mov_b32 s51, 0x20000
s_lshl_b32 s93, s77, 24
mm a[116:119],v[44:47],v[164:167],a[116:119]
rt v[192:193],v240 offset:0
rt v[194:195],v240 offset:1024
mm a[116:119],v[40:43],v[160:163],a[116:119]
rt v[196:197],v240 offset:16896
rt v[198:199],v240 offset:17920
mm a[128:131],v[48:51],v[160:163],a[128:131]
s_add_u32 s92, s94, s93
s_add_u32 s92, s92, 20971520
mm a[128:131],v[52:55],v[164:167],a[128:131]
s_add_u32 s60, s26, s92
s_addc_u32 s61, s27, 0
mm a[132:135],v[60:63],v[164:167],a[132:135]
s_and_b32 s61, s61, 0xffff
s_mov_b32 s62, 524288
mm a[132:135],v[56:59],v[160:163],a[132:135]
rd v[160:163],v236 offset:24576
rd v[164:167],v237 offset:24576
mm a[148:151],v[56:59],v[168:171],a[148:151]
s_mov_b32 s63, 0x20000
s_lshl_b32 s92, s77, 14
mm a[148:151],v[60:63],v[172:175],a[148:151]
s_sub_u32 s95, 16384, s92
v_add_u32 v238, s95, v255
mm a[144:147],v[52:55],v[172:175],a[144:147]
mm a[144:147],v[48:51],v[168:171],a[144:147]
rt v[200:201],v241 offset:0
rt v[202:203],v241 offset:1024
mm a[228:231],v[40:43],v[168:171],a[228:231]
mm a[228:231],v[44:47],v[172:175],a[228:231]
mm a[224:227],v[36:39],v[172:175],a[224:227]
mm a[224:227],v[32:35],v[168:171],a[224:227]
rd v[168:171],v236 offset:26624
rd v[172:175],v237 offset:26624
mm a[208:211],v[32:35],v[176:179],a[208:211]
mm a[208:211],v[36:39],v[180:183],a[208:211]
mm a[212:215],v[44:47],v[180:183],a[212:215]
mm a[212:215],v[40:43],v[176:179],a[212:215]
rt v[204:205],v241 offset:16896
rt v[206:207],v241 offset:17920
mm a[160:163],v[48:51],v[176:179],a[160:163]
mm a[160:163],v[52:55],v[180:183],a[160:163]
mm a[164:167],v[60:63],v[180:183],a[164:167]
mm a[164:167],v[56:59],v[176:179],a[164:167]
rd v[176:179],v236 offset:16384
rd v[180:183],v237 offset:16384
mm a[180:183],v[56:59],v[184:187],a[180:183]
mm a[180:183],v[60:63],v[188:191],a[180:183]
mm a[176:179],v[52:55],v[188:191],a[176:179]
mm a[176:179],v[48:51],v[184:187],a[176:179]
rt v[208:209],v242 offset:0
rt v[210:211],v242 offset:1024
mm a[196:199],v[40:43],v[184:187],a[196:199]
s_add_u32 m0, s83, 33792
mm a[196:199],v[44:47],v[188:191],a[196:199]
ld v232,s[44:47],s80 offen lds
mm a[192:195],v[36:39],v[188:191],a[192:195]
mm a[192:195],v[32:35],v[184:187],a[192:195]
rd v[184:187],v236 offset:18432
rd v[188:191],v237 offset:18432
mm a[64:67],v[32:35],v[0:3],a[64:67]
s_add_u32 m0, s83, 34816
mm a[64:67],v[36:39],v[4:7],a[64:67]
ld v233,s[44:47],s80 offen lds
mm a[68:71],v[44:47],v[4:7],a[68:71]
mm a[68:71],v[40:43],v[0:3],a[68:71]
rt v[212:213],v242 offset:16896
rt v[214:215],v242 offset:17920
mm a[0:3],v[48:51],v[0:3],a[0:3]
s_add_u32 m0, s83, 35968
mm a[0:3],v[52:55],v[4:7],a[0:3]
ld v234,s[44:47],s80 offen lds
mm a[4:7],v[60:63],v[4:7],a[4:7]
mm a[4:7],v[56:59],v[0:3],a[4:7]
rd v[0:3],v236 offset:8192
rd v[4:7],v237 offset:8192
mm a[20:23],v[56:59],v[8:11],a[20:23]
s_add_u32 m0, s83, 36992
mm a[20:23],v[60:63],v[12:15],a[20:23]
ld v235,s[44:47],s80 offen lds
mm a[16:19],v[52:55],v[12:15],a[16:19]
s_mov_b32 s80, 786432
mm a[16:19],v[48:51],v[8:11],a[16:19]
rt v[216:217],v243 offset:0
rt v[218:219],v243 offset:1024
mm a[84:87],v[40:43],v[8:11],a[84:87]
s_add_u32 m0, s83, 50688
mm a[84:87],v[44:47],v[12:15],a[84:87]
ld v232,s[44:47],s80 offen lds
mm a[80:83],v[36:39],v[12:15],a[80:83]
mm a[80:83],v[32:35],v[8:11],a[80:83]
rd v[8:11],v236 offset:10240
rd v[12:15],v237 offset:10240
mm a[240:243],v[32:35],v[16:19],a[240:243]
s_add_u32 m0, s83, 51712
mm a[240:243],v[36:39],v[20:23],a[240:243]
ld v233,s[44:47],s80 offen lds
mm a[244:247],v[44:47],v[20:23],a[244:247]
mm a[244:247],v[40:43],v[16:19],a[244:247]
rt v[220:221],v243 offset:16896
rt v[222:223],v243 offset:17920
mm a[32:35],v[48:51],v[16:19],a[32:35]
s_add_u32 m0, s83, 52864
mm a[32:35],v[52:55],v[20:23],a[32:35]
ld v234,s[44:47],s80 offen lds
mm a[36:39],v[60:63],v[20:23],a[36:39]
mm a[36:39],v[56:59],v[16:19],a[36:39]
rd v[16:19],v236 offset:0
rd v[20:23],v237 offset:0
mm a[60:63],v[56:59],v[24:27],a[60:63]
s_add_u32 m0, s83, 53888
mm a[60:63],v[60:63],v[28:31],a[60:63]
ld v235,s[44:47],s80 offen lds
mm a[56:59],v[52:55],v[28:31],a[56:59]
s_mov_b32 s80, 1048576
mm a[56:59],v[48:51],v[24:27],a[56:59]
mm a[52:55],v[40:43],v[24:27],a[52:55]
mm a[52:55],v[44:47],v[28:31],a[52:55]
mm a[48:51],v[36:39],v[28:31],a[48:51]
wt lgkmcnt(2)
mm a[48:51],v[32:35],v[24:27],a[48:51]
rd v[24:27],v236 offset:2048
rd v[28:31],v237 offset:2048
mm a[96:99],v[192:195],v[160:163],a[96:99]
mm a[96:99],v[196:199],v[164:167],a[96:99]
mm a[100:103],v[204:207],v[164:167],a[100:103]
mm a[100:103],v[200:203],v[160:163],a[100:103]
rt v[32:33],v240 offset:128
rt v[34:35],v240 offset:1152
mm a[104:107],v[208:211],v[160:163],a[104:107]
mm a[104:107],v[212:215],v[164:167],a[104:107]
mm a[108:111],v[220:223],v[164:167],a[108:111]
wt lgkmcnt(2)
s_barrier
mm a[108:111],v[216:219],v[160:163],a[108:111]
mm a[140:143],v[216:219],v[168:171],a[140:143]
s_xor_b32 m0, s82, 0
mm a[140:143],v[220:223],v[172:175],a[140:143]
ld v224,s[40:43],s79 offen lds
mm a[136:139],v[212:215],v[172:175],a[136:139]
mm a[136:139],v[208:211],v[168:171],a[136:139]
rt v[36:37],v240 offset:17024
rt v[38:39],v240 offset:18048
mm a[124:127],v[200:203],v[168:171],a[124:127]
s_xor_b32 m0, s82, 1024
mm a[124:127],v[204:207],v[172:175],a[124:127]
ld v225,s[40:43],s79 offen lds
mm a[120:123],v[196:199],v[172:175],a[120:123]
mm a[120:123],v[192:195],v[168:171],a[120:123]
mm a[232:235],v[192:195],v[176:179],a[232:235]
s_xor_b32 m0, s82, 2048
mm a[232:235],v[196:199],v[180:183],a[232:235]
ld v226,s[40:43],s79 offen lds
mm a[236:239],v[204:207],v[180:183],a[236:239]
mm a[236:239],v[200:203],v[176:179],a[236:239]
rt v[40:41],v241 offset:128
rt v[42:43],v241 offset:1152
mm a[152:155],v[208:211],v[176:179],a[152:155]
s_xor_b32 m0, s82, 3072
mm a[152:155],v[212:215],v[180:183],a[152:155]
ld v227,s[40:43],s79 offen lds
mm a[156:159],v[220:223],v[180:183],a[156:159]
mm a[156:159],v[216:219],v[176:179],a[156:159]
mm a[172:175],v[216:219],v[184:187],a[172:175]
s_xor_b32 m0, s82, 4096
mm a[172:175],v[220:223],v[188:191],a[172:175]
ld v228,s[40:43],s79 offen lds
mm a[168:171],v[212:215],v[188:191],a[168:171]
mm a[168:171],v[208:211],v[184:187],a[168:171]
rt v[44:45],v241 offset:17024
rt v[46:47],v241 offset:18048
mm a[220:223],v[200:203],v[184:187],a[220:223]
s_xor_b32 m0, s82, 5120
mm a[220:223],v[204:207],v[188:191],a[220:223]
ld v229,s[40:43],s79 offen lds
mm a[216:219],v[196:199],v[188:191],a[216:219]
mm a[216:219],v[192:195],v[184:187],a[216:219]
mm a[200:203],v[192:195],v[0:3],a[200:203]
s_xor_b32 m0, s82, 6144
mm a[200:203],v[196:199],v[4:7],a[200:203]
ld v230,s[40:43],s79 offen lds
mm a[204:207],v[204:207],v[4:7],a[204:207]
mm a[204:207],v[200:203],v[0:3],a[204:207]
rt v[48:49],v242 offset:128
rt v[50:51],v242 offset:1152
mm a[184:187],v[208:211],v[0:3],a[184:187]
s_xor_b32 m0, s82, 7168
mm a[184:187],v[212:215],v[4:7],a[184:187]
ld v231,s[40:43],s79 offen lds
mm a[188:191],v[220:223],v[4:7],a[188:191]
s_mov_b32 s79, 384
mm a[188:191],v[216:219],v[0:3],a[188:191]
mm a[12:15],v[216:219],v[8:11],a[12:15]
mm a[12:15],v[220:223],v[12:15],a[12:15]
buffer_load_dword v254, v135, s[68:71], s96 offen
mm a[8:11],v[212:215],v[12:15],a[8:11]
s_add_u32 s96, s96, 1048576
mm a[8:11],v[208:211],v[8:11],a[8:11]
rt v[52:53],v242 offset:17024
rt v[54:55],v242 offset:18048
mm a[76:79],v[200:203],v[8:11],a[76:79]
mm a[76:79],v[204:207],v[12:15],a[76:79]
buffer_load_dword v254, v135, s[68:71], s96 offen
mm a[72:75],v[196:199],v[12:15],a[72:75]
s_add_u32 s96, s96, 1048576
mm a[72:75],v[192:195],v[8:11],a[72:75]
rt v[56:57],v243 offset:128
rt v[58:59],v243 offset:1152
mm a[88:91],v[192:195],v[16:19],a[88:91]
mm a[88:91],v[196:199],v[20:23],a[88:91]
mm a[92:95],v[204:207],v[20:23],a[92:95]
mm a[92:95],v[200:203],v[16:19],a[92:95]
rt v[60:61],v243 offset:17024
rt v[62:63],v243 offset:18048
mm a[24:27],v[208:211],v[16:19],a[24:27]
mm a[24:27],v[212:215],v[20:23],a[24:27]
mm a[28:31],v[220:223],v[20:23],a[28:31]
mm a[28:31],v[216:219],v[16:19],a[28:31]
mm a[44:47],v[216:219],v[24:27],a[44:47]
mm a[44:47],v[220:223],v[28:31],a[44:47]
mm a[40:43],v[212:215],v[28:31],a[40:43]
mm a[40:43],v[208:211],v[24:27],a[40:43]
mm a[252:255],v[200:203],v[24:27],a[252:255]
mm a[252:255],v[204:207],v[28:31],a[252:255]
mm a[248:251],v[196:199],v[28:31],a[248:251]
wt vmcnt(10) lgkmcnt(0)
s_barrier
mm a[248:251],v[192:195],v[24:27],a[248:251]
mm a[112:115],v[32:35],v[160:163],a[112:115]
mm a[112:115],v[36:39],v[164:167],a[112:115]
mm a[116:119],v[44:47],v[164:167],a[116:119]
rt v[192:193],v240 offset:33792
rt v[194:195],v240 offset:34816
mm a[116:119],v[40:43],v[160:163],a[116:119]
rt v[196:197],v240 offset:50688
rt v[198:199],v240 offset:51712
mm a[128:131],v[48:51],v[160:163],a[128:131]
mm a[128:131],v[52:55],v[164:167],a[128:131]
mm a[132:135],v[60:63],v[164:167],a[132:135]
mm a[132:135],v[56:59],v[160:163],a[132:135]
rd v[160:163],v236 offset:57344
rd v[164:167],v237 offset:57344
mm a[148:151],v[56:59],v[168:171],a[148:151]
mm a[148:151],v[60:63],v[172:175],a[148:151]
mm a[144:147],v[52:55],v[172:175],a[144:147]
mm a[144:147],v[48:51],v[168:171],a[144:147]
rt v[200:201],v241 offset:33792
rt v[202:203],v241 offset:34816
mm a[228:231],v[40:43],v[168:171],a[228:231]
mm a[228:231],v[44:47],v[172:175],a[228:231]
mm a[224:227],v[36:39],v[172:175],a[224:227]
mm a[224:227],v[32:35],v[168:171],a[224:227]
rd v[168:171],v236 offset:59392
rd v[172:175],v237 offset:59392
mm a[208:211],v[32:35],v[176:179],a[208:211]
mm a[208:211],v[36:39],v[180:183],a[208:211]
mm a[212:215],v[44:47],v[180:183],a[212:215]
mm a[212:215],v[40:43],v[176:179],a[212:215]
rt v[204:205],v241 offset:50688
rt v[206:207],v241 offset:51712
mm a[160:163],v[48:51],v[176:179],a[160:163]
mm a[160:163],v[52:55],v[180:183],a[160:163]
mm a[164:167],v[60:63],v[180:183],a[164:167]
mm a[164:167],v[56:59],v[176:179],a[164:167]
rd v[176:179],v236 offset:49152
rd v[180:183],v237 offset:49152
mm a[180:183],v[56:59],v[184:187],a[180:183]
mm a[180:183],v[60:63],v[188:191],a[180:183]
mm a[176:179],v[52:55],v[188:191],a[176:179]
mm a[176:179],v[48:51],v[184:187],a[176:179]
rt v[208:209],v242 offset:33792
rt v[210:211],v242 offset:34816
mm a[196:199],v[40:43],v[184:187],a[196:199]
s_add_u32 m0, s83, 0
mm a[196:199],v[44:47],v[188:191],a[196:199]
ld v232,s[44:47],s80 offen lds
mm a[192:195],v[36:39],v[188:191],a[192:195]
mm a[192:195],v[32:35],v[184:187],a[192:195]
rd v[184:187],v236 offset:51200
rd v[188:191],v237 offset:51200
mm a[64:67],v[32:35],v[0:3],a[64:67]
s_add_u32 m0, s83, 1024
mm a[64:67],v[36:39],v[4:7],a[64:67]
ld v233,s[44:47],s80 offen lds
mm a[68:71],v[44:47],v[4:7],a[68:71]
mm a[68:71],v[40:43],v[0:3],a[68:71]
rt v[212:213],v242 offset:50688
rt v[214:215],v242 offset:51712
mm a[0:3],v[48:51],v[0:3],a[0:3]
s_add_u32 m0, s83, 2176
mm a[0:3],v[52:55],v[4:7],a[0:3]
ld v234,s[44:47],s80 offen lds
mm a[4:7],v[60:63],v[4:7],a[4:7]
mm a[4:7],v[56:59],v[0:3],a[4:7]
rd v[0:3],v236 offset:40960
rd v[4:7],v237 offset:40960
mm a[20:23],v[56:59],v[8:11],a[20:23]
s_add_u32 m0, s83, 3200
mm a[20:23],v[60:63],v[12:15],a[20:23]
ld v235,s[44:47],s80 offen lds
mm a[16:19],v[52:55],v[12:15],a[16:19]
s_mov_b32 s80, 1310720
mm a[16:19],v[48:51],v[8:11],a[16:19]
rt v[216:217],v243 offset:33792
rt v[218:219],v243 offset:34816
mm a[84:87],v[40:43],v[8:11],a[84:87]
s_add_u32 m0, s83, 16896
mm a[84:87],v[44:47],v[12:15],a[84:87]
ld v232,s[44:47],s80 offen lds
mm a[80:83],v[36:39],v[12:15],a[80:83]
mm a[80:83],v[32:35],v[8:11],a[80:83]
rd v[8:11],v236 offset:43008
rd v[12:15],v237 offset:43008
mm a[240:243],v[32:35],v[16:19],a[240:243]
s_add_u32 m0, s83, 17920
mm a[240:243],v[36:39],v[20:23],a[240:243]
ld v233,s[44:47],s80 offen lds
mm a[244:247],v[44:47],v[20:23],a[244:247]
mm a[244:247],v[40:43],v[16:19],a[244:247]
rt v[220:221],v243 offset:50688
rt v[222:223],v243 offset:51712
mm a[32:35],v[48:51],v[16:19],a[32:35]
s_add_u32 m0, s83, 19072
mm a[32:35],v[52:55],v[20:23],a[32:35]
ld v234,s[44:47],s80 offen lds
.La5_LW1_2:
s_cbranch_execz .La5_LW1_6
mm a[36:39],v[60:63],v[20:23],a[36:39]
mm a[36:39],v[56:59],v[16:19],a[36:39]
rd v[16:19],v236 offset:32768
rd v[20:23],v237 offset:32768
mm a[60:63],v[56:59],v[24:27],a[60:63]
s_add_u32 m0, s83, 20096
mm a[60:63],v[60:63],v[28:31],a[60:63]
ld v235,s[44:47],s80 offen lds
mm a[56:59],v[52:55],v[28:31],a[56:59]
s_mov_b32 s80, 1572864
mm a[56:59],v[48:51],v[24:27],a[56:59]
mm a[52:55],v[40:43],v[24:27],a[52:55]
mm a[52:55],v[44:47],v[28:31],a[52:55]
mm a[48:51],v[36:39],v[28:31],a[48:51]
wt lgkmcnt(2)
mm a[48:51],v[32:35],v[24:27],a[48:51]
rd v[24:27],v236 offset:34816
rd v[28:31],v237 offset:34816
mm a[96:99],v[192:195],v[160:163],a[96:99]
mm a[96:99],v[196:199],v[164:167],a[96:99]
mm a[100:103],v[204:207],v[164:167],a[100:103]
mm a[100:103],v[200:203],v[160:163],a[100:103]
rt v[32:33],v240 offset:33920
rt v[34:35],v240 offset:34944
mm a[104:107],v[208:211],v[160:163],a[104:107]
mm a[104:107],v[212:215],v[164:167],a[104:107]
mm a[108:111],v[220:223],v[164:167],a[108:111]
wt lgkmcnt(2)
s_barrier
mm a[108:111],v[216:219],v[160:163],a[108:111]
mm a[140:143],v[216:219],v[168:171],a[140:143]
s_xor_b32 m0, s82, 32768
mm a[140:143],v[220:223],v[172:175],a[140:143]
ld v224,s[40:43],s79 offen lds
mm a[136:139],v[212:215],v[172:175],a[136:139]
mm a[136:139],v[208:211],v[168:171],a[136:139]
rt v[36:37],v240 offset:50816
rt v[38:39],v240 offset:51840
mm a[124:127],v[200:203],v[168:171],a[124:127]
s_xor_b32 m0, s82, 33792
mm a[124:127],v[204:207],v[172:175],a[124:127]
ld v225,s[40:43],s79 offen lds
mm a[120:123],v[196:199],v[172:175],a[120:123]
mm a[120:123],v[192:195],v[168:171],a[120:123]
mm a[232:235],v[192:195],v[176:179],a[232:235]
s_xor_b32 m0, s82, 34816
mm a[232:235],v[196:199],v[180:183],a[232:235]
ld v226,s[40:43],s79 offen lds
mm a[236:239],v[204:207],v[180:183],a[236:239]
mm a[236:239],v[200:203],v[176:179],a[236:239]
rt v[40:41],v241 offset:33920
rt v[42:43],v241 offset:34944
mm a[152:155],v[208:211],v[176:179],a[152:155]
s_xor_b32 m0, s82, 35840
mm a[152:155],v[212:215],v[180:183],a[152:155]
ld v227,s[40:43],s79 offen lds
mm a[156:159],v[220:223],v[180:183],a[156:159]
mm a[156:159],v[216:219],v[176:179],a[156:159]
mm a[172:175],v[216:219],v[184:187],a[172:175]
s_xor_b32 m0, s82, 36864
mm a[172:175],v[220:223],v[188:191],a[172:175]
ld v228,s[40:43],s79 offen lds
mm a[168:171],v[212:215],v[188:191],a[168:171]
mm a[168:171],v[208:211],v[184:187],a[168:171]
rt v[44:45],v241 offset:50816
rt v[46:47],v241 offset:51840
mm a[220:223],v[200:203],v[184:187],a[220:223]
s_xor_b32 m0, s82, 37888
mm a[220:223],v[204:207],v[188:191],a[220:223]
ld v229,s[40:43],s79 offen lds
mm a[216:219],v[196:199],v[188:191],a[216:219]
mm a[216:219],v[192:195],v[184:187],a[216:219]
mm a[200:203],v[192:195],v[0:3],a[200:203]
s_xor_b32 m0, s82, 38912
mm a[200:203],v[196:199],v[4:7],a[200:203]
ld v230,s[40:43],s79 offen lds
mm a[204:207],v[204:207],v[4:7],a[204:207]
mm a[204:207],v[200:203],v[0:3],a[204:207]
rt v[48:49],v242 offset:33920
rt v[50:51],v242 offset:34944
mm a[184:187],v[208:211],v[0:3],a[184:187]
s_xor_b32 m0, s82, 39936
mm a[184:187],v[212:215],v[4:7],a[184:187]
ld v231,s[40:43],s79 offen lds
mm a[188:191],v[220:223],v[4:7],a[188:191]
s_mov_b32 s79, 512
mm a[188:191],v[216:219],v[0:3],a[188:191]
mm a[12:15],v[216:219],v[8:11],a[12:15]
mm a[12:15],v[220:223],v[12:15],a[12:15]
buffer_load_dword v254, v135, s[68:71], s96 offen
mm a[8:11],v[212:215],v[12:15],a[8:11]
s_add_u32 s96, s96, 1048576
mm a[8:11],v[208:211],v[8:11],a[8:11]
rt v[52:53],v242 offset:50816
rt v[54:55],v242 offset:51840
mm a[76:79],v[200:203],v[8:11],a[76:79]
mm a[76:79],v[204:207],v[12:15],a[76:79]
buffer_load_dword v254, v135, s[68:71], s96 offen
mm a[72:75],v[196:199],v[12:15],a[72:75]
s_add_u32 s96, s96, 1048576
mm a[72:75],v[192:195],v[8:11],a[72:75]
rt v[56:57],v243 offset:33920
rt v[58:59],v243 offset:34944
mm a[88:91],v[192:195],v[16:19],a[88:91]
mm a[88:91],v[196:199],v[20:23],a[88:91]
mm a[92:95],v[204:207],v[20:23],a[92:95]
mm a[92:95],v[200:203],v[16:19],a[92:95]
rt v[60:61],v243 offset:50816
rt v[62:63],v243 offset:51840
mm a[24:27],v[208:211],v[16:19],a[24:27]
mm a[24:27],v[212:215],v[20:23],a[24:27]
mm a[28:31],v[220:223],v[20:23],a[28:31]
mm a[28:31],v[216:219],v[16:19],a[28:31]
mm a[44:47],v[216:219],v[24:27],a[44:47]
mm a[44:47],v[220:223],v[28:31],a[44:47]
mm a[40:43],v[212:215],v[28:31],a[40:43]
mm a[40:43],v[208:211],v[24:27],a[40:43]
mm a[252:255],v[200:203],v[24:27],a[252:255]
mm a[252:255],v[204:207],v[28:31],a[252:255]
mm a[248:251],v[196:199],v[28:31],a[248:251]
wt vmcnt(10) lgkmcnt(0)
s_barrier
mm a[248:251],v[192:195],v[24:27],a[248:251]
mm a[112:115],v[32:35],v[160:163],a[112:115]
mm a[112:115],v[36:39],v[164:167],a[112:115]
mm a[116:119],v[44:47],v[164:167],a[116:119]
rt v[192:193],v240 offset:0
rt v[194:195],v240 offset:1024
mm a[116:119],v[40:43],v[160:163],a[116:119]
rt v[196:197],v240 offset:16896
rt v[198:199],v240 offset:17920
mm a[128:131],v[48:51],v[160:163],a[128:131]
mm a[128:131],v[52:55],v[164:167],a[128:131]
mm a[132:135],v[60:63],v[164:167],a[132:135]
mm a[132:135],v[56:59],v[160:163],a[132:135]
rd v[160:163],v236 offset:24576
rd v[164:167],v237 offset:24576
mm a[148:151],v[56:59],v[168:171],a[148:151]
mm a[148:151],v[60:63],v[172:175],a[148:151]
mm a[144:147],v[52:55],v[172:175],a[144:147]
mm a[144:147],v[48:51],v[168:171],a[144:147]
rt v[200:201],v241 offset:0
rt v[202:203],v241 offset:1024
mm a[228:231],v[40:43],v[168:171],a[228:231]
mm a[228:231],v[44:47],v[172:175],a[228:231]
mm a[224:227],v[36:39],v[172:175],a[224:227]
mm a[224:227],v[32:35],v[168:171],a[224:227]
rd v[168:171],v236 offset:26624
rd v[172:175],v237 offset:26624
mm a[208:211],v[32:35],v[176:179],a[208:211]
mm a[208:211],v[36:39],v[180:183],a[208:211]
mm a[212:215],v[44:47],v[180:183],a[212:215]
mm a[212:215],v[40:43],v[176:179],a[212:215]
rt v[204:205],v241 offset:16896
rt v[206:207],v241 offset:17920
mm a[160:163],v[48:51],v[176:179],a[160:163]
mm a[160:163],v[52:55],v[180:183],a[160:163]
mm a[164:167],v[60:63],v[180:183],a[164:167]
mm a[164:167],v[56:59],v[176:179],a[164:167]
rd v[176:179],v236 offset:16384
rd v[180:183],v237 offset:16384
mm a[180:183],v[56:59],v[184:187],a[180:183]
mm a[180:183],v[60:63],v[188:191],a[180:183]
mm a[176:179],v[52:55],v[188:191],a[176:179]
mm a[176:179],v[48:51],v[184:187],a[176:179]
rt v[208:209],v242 offset:0
rt v[210:211],v242 offset:1024
mm a[196:199],v[40:43],v[184:187],a[196:199]
s_add_u32 m0, s83, 33792
mm a[196:199],v[44:47],v[188:191],a[196:199]
ld v232,s[44:47],s80 offen lds
mm a[192:195],v[36:39],v[188:191],a[192:195]
mm a[192:195],v[32:35],v[184:187],a[192:195]
rd v[184:187],v236 offset:18432
rd v[188:191],v237 offset:18432
mm a[64:67],v[32:35],v[0:3],a[64:67]
s_add_u32 m0, s83, 34816
mm a[64:67],v[36:39],v[4:7],a[64:67]
ld v233,s[44:47],s80 offen lds
mm a[68:71],v[44:47],v[4:7],a[68:71]
mm a[68:71],v[40:43],v[0:3],a[68:71]
rt v[212:213],v242 offset:16896
rt v[214:215],v242 offset:17920
mm a[0:3],v[48:51],v[0:3],a[0:3]
s_add_u32 m0, s83, 35968
mm a[0:3],v[52:55],v[4:7],a[0:3]
ld v234,s[44:47],s80 offen lds
mm a[4:7],v[60:63],v[4:7],a[4:7]
mm a[4:7],v[56:59],v[0:3],a[4:7]
rd v[0:3],v236 offset:8192
rd v[4:7],v237 offset:8192
mm a[20:23],v[56:59],v[8:11],a[20:23]
s_add_u32 m0, s83, 36992
mm a[20:23],v[60:63],v[12:15],a[20:23]
ld v235,s[44:47],s80 offen lds
mm a[16:19],v[52:55],v[12:15],a[16:19]
s_mov_b32 s80, 1835008
mm a[16:19],v[48:51],v[8:11],a[16:19]
rt v[216:217],v243 offset:0
rt v[218:219],v243 offset:1024
mm a[84:87],v[40:43],v[8:11],a[84:87]
s_add_u32 m0, s83, 50688
mm a[84:87],v[44:47],v[12:15],a[84:87]
ld v232,s[44:47],s80 offen lds
mm a[80:83],v[36:39],v[12:15],a[80:83]
mm a[80:83],v[32:35],v[8:11],a[80:83]
rd v[8:11],v236 offset:10240
rd v[12:15],v237 offset:10240
mm a[240:243],v[32:35],v[16:19],a[240:243]
s_add_u32 m0, s83, 51712
mm a[240:243],v[36:39],v[20:23],a[240:243]
ld v233,s[44:47],s80 offen lds
mm a[244:247],v[44:47],v[20:23],a[244:247]
mm a[244:247],v[40:43],v[16:19],a[244:247]
rt v[220:221],v243 offset:16896
rt v[222:223],v243 offset:17920
mm a[32:35],v[48:51],v[16:19],a[32:35]
s_add_u32 m0, s83, 52864
mm a[32:35],v[52:55],v[20:23],a[32:35]
ld v234,s[44:47],s80 offen lds
mm a[36:39],v[60:63],v[20:23],a[36:39]
mm a[36:39],v[56:59],v[16:19],a[36:39]
rd v[16:19],v236 offset:0
rd v[20:23],v237 offset:0
mm a[60:63],v[56:59],v[24:27],a[60:63]
s_add_u32 m0, s83, 53888
mm a[60:63],v[60:63],v[28:31],a[60:63]
ld v235,s[44:47],s80 offen lds
mm a[56:59],v[52:55],v[28:31],a[56:59]
s_mov_b64 s[44:45], s[68:69]
s_mov_b32 s46, s70
mm a[56:59],v[48:51],v[24:27],a[56:59]
mm a[52:55],v[40:43],v[24:27],a[52:55]
mm a[52:55],v[44:47],v[28:31],a[52:55]
mm a[48:51],v[36:39],v[28:31],a[48:51]
wt lgkmcnt(2)
s_lshl_b32 s80, s77, 18
mm a[48:51],v[32:35],v[24:27],a[48:51]
rd v[24:27],v236 offset:2048
rd v[28:31],v237 offset:2048
mm a[96:99],v[192:195],v[160:163],a[96:99]
mm a[96:99],v[196:199],v[164:167],a[96:99]
mm a[100:103],v[204:207],v[164:167],a[100:103]
mm a[100:103],v[200:203],v[160:163],a[100:103]
rt v[32:33],v240 offset:128
rt v[34:35],v240 offset:1152
mm a[104:107],v[208:211],v[160:163],a[104:107]
mm a[104:107],v[212:215],v[164:167],a[104:107]
mm a[108:111],v[220:223],v[164:167],a[108:111]
wt lgkmcnt(2)
s_barrier
mm a[108:111],v[216:219],v[160:163],a[108:111]
mm a[140:143],v[216:219],v[168:171],a[140:143]
mm a[140:143],v[220:223],v[172:175],a[140:143]
mm a[136:139],v[212:215],v[172:175],a[136:139]
mm a[136:139],v[208:211],v[168:171],a[136:139]
rt v[36:37],v240 offset:17024
rt v[38:39],v240 offset:18048
mm a[124:127],v[200:203],v[168:171],a[124:127]
mm a[124:127],v[204:207],v[172:175],a[124:127]
mm a[120:123],v[196:199],v[172:175],a[120:123]
mm a[120:123],v[192:195],v[168:171],a[120:123]
mm a[232:235],v[192:195],v[176:179],a[232:235]
mm a[232:235],v[196:199],v[180:183],a[232:235]
mm a[236:239],v[204:207],v[180:183],a[236:239]
mm a[236:239],v[200:203],v[176:179],a[236:239]
rt v[40:41],v241 offset:128
rt v[42:43],v241 offset:1152
mm a[152:155],v[208:211],v[176:179],a[152:155]
mm a[152:155],v[212:215],v[180:183],a[152:155]
mm a[156:159],v[220:223],v[180:183],a[156:159]
mm a[156:159],v[216:219],v[176:179],a[156:159]
mm a[172:175],v[216:219],v[184:187],a[172:175]
mm a[172:175],v[220:223],v[188:191],a[172:175]
mm a[168:171],v[212:215],v[188:191],a[168:171]
mm a[168:171],v[208:211],v[184:187],a[168:171]
rt v[44:45],v241 offset:17024
rt v[46:47],v241 offset:18048
mm a[220:223],v[200:203],v[184:187],a[220:223]
mm a[220:223],v[204:207],v[188:191],a[220:223]
mm a[216:219],v[196:199],v[188:191],a[216:219]
mm a[216:219],v[192:195],v[184:187],a[216:219]
mm a[200:203],v[192:195],v[0:3],a[200:203]
mm a[200:203],v[196:199],v[4:7],a[200:203]
mm a[204:207],v[204:207],v[4:7],a[204:207]
mm a[204:207],v[200:203],v[0:3],a[204:207]
rt v[48:49],v242 offset:128
rt v[50:51],v242 offset:1152
mm a[184:187],v[208:211],v[0:3],a[184:187]
mm a[184:187],v[212:215],v[4:7],a[184:187]
mm a[188:191],v[220:223],v[4:7],a[188:191]
mm a[188:191],v[216:219],v[0:3],a[188:191]
mm a[12:15],v[216:219],v[8:11],a[12:15]
mm a[12:15],v[220:223],v[12:15],a[12:15]
buffer_load_dword v254, v135, s[68:71], s96 offen
mm a[8:11],v[212:215],v[12:15],a[8:11]
s_add_u32 s96, s96, 1048576
mm a[8:11],v[208:211],v[8:11],a[8:11]
rt v[52:53],v242 offset:17024
rt v[54:55],v242 offset:18048
mm a[76:79],v[200:203],v[8:11],a[76:79]
mm a[76:79],v[204:207],v[12:15],a[76:79]
buffer_load_dword v254, v135, s[68:71], s96 offen
mm a[72:75],v[196:199],v[12:15],a[72:75]
s_add_u32 s96, s96, 1048576
mm a[72:75],v[192:195],v[8:11],a[72:75]
rt v[56:57],v243 offset:128
rt v[58:59],v243 offset:1152
mm a[88:91],v[192:195],v[16:19],a[88:91]
mm a[88:91],v[196:199],v[20:23],a[88:91]
mm a[92:95],v[204:207],v[20:23],a[92:95]
mm a[92:95],v[200:203],v[16:19],a[92:95]
rt v[60:61],v243 offset:17024
rt v[62:63],v243 offset:18048
mm a[24:27],v[208:211],v[16:19],a[24:27]
mm a[24:27],v[212:215],v[20:23],a[24:27]
mm a[28:31],v[220:223],v[20:23],a[28:31]
mm a[28:31],v[216:219],v[16:19],a[28:31]
mm a[44:47],v[216:219],v[24:27],a[44:47]
mm a[44:47],v[220:223],v[28:31],a[44:47]
ld v[72:75],v255,s[64:67],s89 offen offset:1024 sc1
mm a[40:43],v[212:215],v[28:31],a[40:43]
mm a[40:43],v[208:211],v[24:27],a[40:43]
mm a[252:255],v[200:203],v[24:27],a[252:255]
mm a[252:255],v[204:207],v[28:31],a[252:255]
mm a[248:251],v[196:199],v[28:31],a[248:251]
wt vmcnt(3) lgkmcnt(0)
s_barrier
mm a[248:251],v[192:195],v[24:27],a[248:251]
mm a[112:115],v[32:35],v[160:163],a[112:115]
mm a[112:115],v[36:39],v[164:167],a[112:115]
mm a[116:119],v[44:47],v[164:167],a[116:119]
rt v[192:193],v240 offset:33792
rt v[194:195],v240 offset:34816
mm a[116:119],v[40:43],v[160:163],a[116:119]
rt v[196:197],v240 offset:50688
rt v[198:199],v240 offset:51712
mm a[128:131],v[48:51],v[160:163],a[128:131]
mm a[128:131],v[52:55],v[164:167],a[128:131]
mm a[132:135],v[60:63],v[164:167],a[132:135]
mm a[132:135],v[56:59],v[160:163],a[132:135]
rd v[160:163],v236 offset:57344
rd v[164:167],v237 offset:57344
mm a[148:151],v[56:59],v[168:171],a[148:151]
mm a[148:151],v[60:63],v[172:175],a[148:151]
mm a[144:147],v[52:55],v[172:175],a[144:147]
mm a[144:147],v[48:51],v[168:171],a[144:147]
rt v[200:201],v241 offset:33792
rt v[202:203],v241 offset:34816
mm a[228:231],v[40:43],v[168:171],a[228:231]
mm a[228:231],v[44:47],v[172:175],a[228:231]
mm a[224:227],v[36:39],v[172:175],a[224:227]
mm a[224:227],v[32:35],v[168:171],a[224:227]
rd v[168:171],v236 offset:59392
rd v[172:175],v237 offset:59392
mm a[208:211],v[32:35],v[176:179],a[208:211]
mm a[208:211],v[36:39],v[180:183],a[208:211]
mm a[212:215],v[44:47],v[180:183],a[212:215]
mm a[212:215],v[40:43],v[176:179],a[212:215]
rt v[204:205],v241 offset:50688
rt v[206:207],v241 offset:51712
mm a[160:163],v[48:51],v[176:179],a[160:163]
mm a[160:163],v[52:55],v[180:183],a[160:163]
mm a[164:167],v[60:63],v[180:183],a[164:167]
mm a[164:167],v[56:59],v[176:179],a[164:167]
rd v[176:179],v236 offset:49152
rd v[180:183],v237 offset:49152
mm a[180:183],v[56:59],v[184:187],a[180:183]
mm a[180:183],v[60:63],v[188:191],a[180:183]
mm a[176:179],v[52:55],v[188:191],a[176:179]
mm a[176:179],v[48:51],v[184:187],a[176:179]
rt v[208:209],v242 offset:33792
rt v[210:211],v242 offset:34816
mm a[196:199],v[40:43],v[184:187],a[196:199]
s_add_u32 m0, s83, 0
mm a[196:199],v[44:47],v[188:191],a[196:199]
ld v232,s[44:47],s80 offen lds
mm a[192:195],v[36:39],v[188:191],a[192:195]
mm a[192:195],v[32:35],v[184:187],a[192:195]
rd v[184:187],v236 offset:51200
rd v[188:191],v237 offset:51200
mm a[64:67],v[32:35],v[0:3],a[64:67]
s_add_u32 m0, s83, 1024
mm a[64:67],v[36:39],v[4:7],a[64:67]
ld v233,s[44:47],s80 offen lds
mm a[68:71],v[44:47],v[4:7],a[68:71]
mm a[68:71],v[40:43],v[0:3],a[68:71]
rt v[212:213],v242 offset:50688
rt v[214:215],v242 offset:51712
mm a[0:3],v[48:51],v[0:3],a[0:3]
s_add_u32 m0, s83, 2176
mm a[0:3],v[52:55],v[4:7],a[0:3]
ld v234,s[44:47],s80 offen lds
mm a[4:7],v[60:63],v[4:7],a[4:7]
mm a[4:7],v[56:59],v[0:3],a[4:7]
rd v[0:3],v236 offset:40960
rd v[4:7],v237 offset:40960
mm a[20:23],v[56:59],v[8:11],a[20:23]
s_add_u32 m0, s83, 3200
mm a[20:23],v[60:63],v[12:15],a[20:23]
ld v235,s[44:47],s80 offen lds
mm a[16:19],v[52:55],v[12:15],a[16:19]
s_add_u32 s80, s80, 524288
mm a[16:19],v[48:51],v[8:11],a[16:19]
rt v[216:217],v243 offset:33792
rt v[218:219],v243 offset:34816
mm a[84:87],v[40:43],v[8:11],a[84:87]
s_add_u32 m0, s83, 16896
mm a[84:87],v[44:47],v[12:15],a[84:87]
ld v232,s[44:47],s80 offen lds
mm a[80:83],v[36:39],v[12:15],a[80:83]
mm a[80:83],v[32:35],v[8:11],a[80:83]
rd v[8:11],v236 offset:43008
rd v[12:15],v237 offset:43008
mm a[240:243],v[32:35],v[16:19],a[240:243]
s_add_u32 m0, s83, 17920
mm a[240:243],v[36:39],v[20:23],a[240:243]
ld v233,s[44:47],s80 offen lds
mm a[244:247],v[44:47],v[20:23],a[244:247]
mm a[244:247],v[40:43],v[16:19],a[244:247]
rt v[220:221],v243 offset:50688
rt v[222:223],v243 offset:51712
mm a[32:35],v[48:51],v[16:19],a[32:35]
s_add_u32 m0, s83, 19072
mm a[32:35],v[52:55],v[20:23],a[32:35]
ld v234,s[44:47],s80 offen lds
mm a[36:39],v[60:63],v[20:23],a[36:39]
mm a[36:39],v[56:59],v[16:19],a[36:39]
rd v[16:19],v236 offset:32768
rd v[20:23],v237 offset:32768
mm a[60:63],v[56:59],v[24:27],a[60:63]
s_add_u32 m0, s83, 20096
mm a[60:63],v[60:63],v[28:31],a[60:63]
ld v235,s[44:47],s80 offen lds
mm a[56:59],v[52:55],v[28:31],a[56:59]
s_add_u32 s80, s80, 524288
mm a[56:59],v[48:51],v[24:27],a[56:59]
mm a[52:55],v[40:43],v[24:27],a[52:55]
mm a[52:55],v[44:47],v[28:31],a[52:55]
mm a[48:51],v[36:39],v[28:31],a[48:51]
wt lgkmcnt(2)
mm a[48:51],v[32:35],v[24:27],a[48:51]
rd v[24:27],v236 offset:34816
rd v[28:31],v237 offset:34816
mm a[96:99],v[192:195],v[160:163],a[96:99]
mm a[96:99],v[196:199],v[164:167],a[96:99]
mm a[100:103],v[204:207],v[164:167],a[100:103]
mm a[100:103],v[200:203],v[160:163],a[100:103]
rt v[32:33],v240 offset:33920
rt v[34:35],v240 offset:34944
mm a[104:107],v[208:211],v[160:163],a[104:107]
mm a[104:107],v[212:215],v[164:167],a[104:107]
mm a[108:111],v[220:223],v[164:167],a[108:111]
wt lgkmcnt(2)
s_barrier
mm a[108:111],v[216:219],v[160:163],a[108:111]
mm a[140:143],v[216:219],v[168:171],a[140:143]
wt vmcnt(8)
v_cmp_eq_u64 vcc, v[72:73], v[252:253]
mm a[140:143],v[220:223],v[172:175],a[140:143]
v_cmp_eq_u64 s[96:97], v[74:75], v[252:253]
mm a[136:139],v[212:215],v[172:175],a[136:139]
s_and_b64 vcc, vcc, s[96:97]
s_cmp_eq_u64 vcc, -1
s_cbranch_scc0 .La5_LK_XSPIN
.La5_LK_XOK:
mm a[136:139],v[208:211],v[168:171],a[136:139]
rt v[36:37],v240 offset:50816
rt v[38:39],v240 offset:51840
mm a[124:127],v[200:203],v[168:171],a[124:127]
s_mov_b32 s89, 49152
mm a[124:127],v[204:207],v[172:175],a[124:127]
mm a[120:123],v[196:199],v[172:175],a[120:123]
ar v224,a96
ar v225,a97
mm a[120:123],v[192:195],v[168:171],a[120:123]
ar v226,a98
ar v227,a99
mm a[232:235],v[192:195],v[176:179],a[232:235]
ar v228,a100
ar v229,a101
mm a[232:235],v[196:199],v[180:183],a[232:235]
ld v[128:131],v244,s[52:55],s91 offen offset:0 sc1
mm a[236:239],v[204:207],v[180:183],a[236:239]
ar v230,a102
ar v231,a103
mm a[236:239],v[200:203],v[176:179],a[236:239]
rt v[40:41],v241 offset:33920
rt v[42:43],v241 offset:34944
mm a[152:155],v[208:211],v[176:179],a[152:155]
cv v248,v224,v225
cv v249,v226,v227
mm a[152:155],v[212:215],v[180:183],a[152:155]
ld v[132:135],v245,s[52:55],s91 offen offset:0 sc1
mm a[156:159],v[220:223],v[180:183],a[156:159]
cv v250,v228,v229
cv v251,v230,v231
mm a[156:159],v[216:219],v[176:179],a[156:159]
st v[248:251],v255,s[48:51],s89 offen sc1
mm a[172:175],v[216:219],v[184:187],a[172:175]
s_mov_b32 s89, 50176
ds_write_b128 v238, v[248:251] offset:0
mm a[172:175],v[220:223],v[188:191],a[172:175]
ld v[136:139],v246,s[52:55],s91 offen offset:0 sc1
mm a[168:171],v[212:215],v[188:191],a[168:171]
ar v224,a104
ar v225,a105
mm a[168:171],v[208:211],v[184:187],a[168:171]
rt v[44:45],v241 offset:50816
rt v[46:47],v241 offset:51840
mm a[220:223],v[200:203],v[184:187],a[220:223]
ar v226,a106
ar v227,a107
mm a[220:223],v[204:207],v[188:191],a[220:223]
ld v[140:143],v247,s[52:55],s91 offen offset:0 sc1
mm a[216:219],v[196:199],v[188:191],a[216:219]
ar v228,a108
ar v229,a109
mm a[216:219],v[192:195],v[184:187],a[216:219]
ar v230,a110
ar v231,a111
mm a[200:203],v[192:195],v[0:3],a[200:203]
cv v248,v224,v225
cv v249,v226,v227
.La5_LW1_3:
s_cbranch_execz .La5_LW1_7
mm a[200:203],v[196:199],v[4:7],a[200:203]
ld v[144:147],v244,s[52:55],s91 offen offset:1024 sc1
mm a[204:207],v[204:207],v[4:7],a[204:207]
cv v250,v228,v229
cv v251,v230,v231
mm a[204:207],v[200:203],v[0:3],a[204:207]
rt v[48:49],v242 offset:33920
rt v[50:51],v242 offset:34944
mm a[184:187],v[208:211],v[0:3],a[184:187]
st v[248:251],v255,s[48:51],s89 offen sc1
mm a[184:187],v[212:215],v[4:7],a[184:187]
ld v[148:151],v245,s[52:55],s91 offen offset:1024 sc1
mm a[188:191],v[220:223],v[4:7],a[188:191]
s_mov_b32 s89, 51200
ds_write_b128 v238, v[248:251] offset:1024
mm a[188:191],v[216:219],v[0:3],a[188:191]
mm a[12:15],v[216:219],v[8:11],a[12:15]
mm a[12:15],v[220:223],v[12:15],a[12:15]
ld v[152:155],v246,s[52:55],s91 offen offset:1024 sc1
mm a[8:11],v[212:215],v[12:15],a[8:11]
mm a[8:11],v[208:211],v[8:11],a[8:11]
rt v[52:53],v242 offset:50816
rt v[54:55],v242 offset:51840
mm a[76:79],v[200:203],v[8:11],a[76:79]
mm a[76:79],v[204:207],v[12:15],a[76:79]
ld v[156:159],v247,s[52:55],s91 offen offset:1024 sc1
mm a[72:75],v[196:199],v[12:15],a[72:75]
mm a[72:75],v[192:195],v[8:11],a[72:75]
rt v[56:57],v243 offset:33920
rt v[58:59],v243 offset:34944
mm a[88:91],v[192:195],v[16:19],a[88:91]
mm a[88:91],v[196:199],v[20:23],a[88:91]
mm a[92:95],v[204:207],v[20:23],a[92:95]
mm a[92:95],v[200:203],v[16:19],a[92:95]
rt v[60:61],v243 offset:50816
rt v[62:63],v243 offset:51840
mm a[24:27],v[208:211],v[16:19],a[24:27]
mm a[24:27],v[212:215],v[20:23],a[24:27]
mm a[28:31],v[220:223],v[20:23],a[28:31]
mm a[28:31],v[216:219],v[16:19],a[28:31]
mm a[44:47],v[216:219],v[24:27],a[44:47]
mm a[44:47],v[220:223],v[28:31],a[44:47]
mm a[40:43],v[212:215],v[28:31],a[40:43]
mm a[40:43],v[208:211],v[24:27],a[40:43]
mm a[252:255],v[200:203],v[24:27],a[252:255]
mm a[252:255],v[204:207],v[28:31],a[252:255]
mm a[248:251],v[196:199],v[28:31],a[248:251]
wt vmcnt(10) lgkmcnt(0)
s_barrier
mm a[248:251],v[192:195],v[24:27],a[248:251]
mm a[112:115],v[32:35],v[160:163],a[112:115]
s_add_u32 m0, s83, 33792
mm a[112:115],v[36:39],v[164:167],a[112:115]
ld v232,s[44:47],s80 offen lds
mm a[116:119],v[44:47],v[164:167],a[116:119]
mm a[116:119],v[40:43],v[160:163],a[116:119]
rt v[192:193],v240 offset:0
rt v[194:195],v240 offset:1024
mm a[128:131],v[48:51],v[160:163],a[128:131]
s_add_u32 m0, s83, 34816
mm a[128:131],v[52:55],v[164:167],a[128:131]
ld v233,s[44:47],s80 offen lds
mm a[132:135],v[60:63],v[164:167],a[132:135]
mm a[132:135],v[56:59],v[160:163],a[132:135]
rt v[196:197],v240 offset:8448
rt v[198:199],v240 offset:9472
mm a[148:151],v[56:59],v[168:171],a[148:151]
s_add_u32 m0, s83, 35968
mm a[148:151],v[60:63],v[172:175],a[148:151]
ld v234,s[44:47],s80 offen lds
mm a[144:147],v[52:55],v[172:175],a[144:147]
ar v224,a112
ar v225,a113
mm a[144:147],v[48:51],v[168:171],a[144:147]
rt v[200:201],v241 offset:0
rt v[202:203],v241 offset:1024
mm a[228:231],v[40:43],v[168:171],a[228:231]
s_add_u32 m0, s83, 36992
ar v226,a114
mm a[228:231],v[44:47],v[172:175],a[228:231]
ld v235,s[44:47],s80 offen lds
mm a[224:227],v[36:39],v[172:175],a[224:227]
s_add_u32 s80, s80, 524288
ar v227,a115
mm a[224:227],v[32:35],v[168:171],a[224:227]
rt v[204:205],v241 offset:8448
rt v[206:207],v241 offset:9472
mm a[208:211],v[32:35],v[176:179],a[208:211]
s_add_u32 m0, s83, 50688
ar v228,a116
mm a[208:211],v[36:39],v[180:183],a[208:211]
ld v232,s[44:47],s80 offen lds
mm a[212:215],v[44:47],v[180:183],a[212:215]
ar v229,a117
ar v230,a118
mm a[212:215],v[40:43],v[176:179],a[212:215]
rt v[208:209],v242 offset:0
rt v[210:211],v242 offset:1024
mm a[160:163],v[48:51],v[176:179],a[160:163]
s_add_u32 m0, s83, 51712
ar v231,a119
mm a[160:163],v[52:55],v[180:183],a[160:163]
ld v233,s[44:47],s80 offen lds
mm a[164:167],v[60:63],v[180:183],a[164:167]
cv v248,v224,v225
cv v249,v226,v227
mm a[164:167],v[56:59],v[176:179],a[164:167]
rt v[212:213],v242 offset:8448
rt v[214:215],v242 offset:9472
mm a[180:183],v[56:59],v[184:187],a[180:183]
s_add_u32 m0, s83, 52864
cv v250,v228,v229
mm a[180:183],v[60:63],v[188:191],a[180:183]
ld v234,s[44:47],s80 offen lds
mm a[176:179],v[52:55],v[188:191],a[176:179]
cv v251,v230,v231
st v[248:251],v255,s[48:51],s89 offen sc1
mm a[176:179],v[48:51],v[184:187],a[176:179]
rt v[216:217],v243 offset:0
rt v[218:219],v243 offset:1024
mm a[196:199],v[40:43],v[184:187],a[196:199]
s_add_u32 m0, s83, 53888
s_mov_b32 s89, 52224
mm a[196:199],v[44:47],v[188:191],a[196:199]
ld v235,s[44:47],s80 offen lds
mm a[192:195],v[36:39],v[188:191],a[192:195]
s_add_u32 s80, s80, 524288
ds_write_b128 v238, v[248:251] offset:2048
mm a[192:195],v[32:35],v[184:187],a[192:195]
rt v[220:221],v243 offset:8448
rt v[222:223],v243 offset:9472
mm a[64:67],v[32:35],v[0:3],a[64:67]
mm a[64:67],v[36:39],v[4:7],a[64:67]
ld v[160:163],v244,s[52:55],s91 offen offset:2048 sc1
mm a[68:71],v[44:47],v[4:7],a[68:71]
mm a[68:71],v[40:43],v[0:3],a[68:71]
mm a[0:3],v[48:51],v[0:3],a[0:3]
mm a[0:3],v[52:55],v[4:7],a[0:3]
ld v[164:167],v245,s[52:55],s91 offen offset:2048 sc1
mm a[4:7],v[60:63],v[4:7],a[4:7]
mm a[4:7],v[56:59],v[0:3],a[4:7]
mm a[20:23],v[56:59],v[8:11],a[20:23]
mm a[20:23],v[60:63],v[12:15],a[20:23]
ld v[168:171],v246,s[52:55],s91 offen offset:2048 sc1
mm a[16:19],v[52:55],v[12:15],a[16:19]
mm a[16:19],v[48:51],v[8:11],a[16:19]
mm a[84:87],v[40:43],v[8:11],a[84:87]
mm a[84:87],v[44:47],v[12:15],a[84:87]
ld v[172:175],v247,s[52:55],s91 offen offset:2048 sc1
mm a[80:83],v[36:39],v[12:15],a[80:83]
mm a[80:83],v[32:35],v[8:11],a[80:83]
mm a[240:243],v[32:35],v[16:19],a[240:243]
mm a[240:243],v[36:39],v[20:23],a[240:243]
mm a[244:247],v[44:47],v[20:23],a[244:247]
mm a[244:247],v[40:43],v[16:19],a[244:247]
mm a[32:35],v[48:51],v[16:19],a[32:35]
mm a[32:35],v[52:55],v[20:23],a[32:35]
mm a[36:39],v[60:63],v[20:23],a[36:39]
mm a[36:39],v[56:59],v[16:19],a[36:39]
mm a[60:63],v[56:59],v[24:27],a[60:63]
mm a[60:63],v[60:63],v[28:31],a[60:63]
mm a[56:59],v[52:55],v[28:31],a[56:59]
mm a[56:59],v[48:51],v[24:27],a[56:59]
mm a[52:55],v[40:43],v[24:27],a[52:55]
mm a[52:55],v[44:47],v[28:31],a[52:55]
mm a[48:51],v[36:39],v[28:31],a[48:51]
mm a[48:51],v[32:35],v[24:27],a[48:51]
s_branch .La5_LQ_START
.La5_LV_XSPIN:
s_mov_b32 s93, 0
.La5_LV_XSPIN_L:
s_add_u32 s93, s93, 1
s_cmp_gt_u32 s93, 2097152
s_cbranch_scc1 .La5_LV_XOK
s_sleep 8
ld v[72:75],v255,s[64:67],s89 offen offset:1024 sc1
wt vmcnt(0)
v_cmp_eq_u64 vcc, v[72:73], v[252:253]
v_cmp_eq_u64 s[96:97], v[74:75], v[252:253]
s_nop 1
s_and_b64 vcc, vcc, s[96:97]
s_cmp_eq_u64 vcc, -1
s_nop 0
s_nop 0
s_cbranch_scc0 .La5_LV_XSPIN_L
s_branch .La5_LV_XOK
.La5_LK_XSPIN:
s_mov_b32 s93, 0
.La5_LK_XSPIN_L:
s_add_u32 s93, s93, 1
s_cmp_gt_u32 s93, 2097152
s_nop 0
s_nop 0
s_cbranch_scc1 .La5_LK_XOK
s_sleep 8
ld v[72:75],v255,s[64:67],s89 offen offset:1024 sc1
wt vmcnt(0)
v_cmp_eq_u64 vcc, v[72:73], v[252:253]
v_cmp_eq_u64 s[96:97], v[74:75], v[252:253]
s_nop 1
s_and_b64 vcc, vcc, s[96:97]
s_cmp_eq_u64 vcc, -1
s_cbranch_scc0 .La5_LK_XSPIN_L
s_branch .La5_LK_XOK
; ---- V tile (waves 1, 3): the same with the operands exchanged (V comes out transposed)
.La5_LV_START:
.La5_LW1_4:
s_cbranch_execz .La5_LW1_8
mm a[96:99],v[160:163],v[192:195],0
s_add_u32 m0, s83, 33792
mm a[96:99],v[164:167],v[196:199],a[96:99]
ld v232,s[44:47],s80 offen lds
mm a[104:107],v[164:167],v[204:207],0
rd v[0:3],v236 offset:8192
rd v[4:7],v237 offset:8192
s_add_u32 m0, s83, 34816
mm a[104:107],v[160:163],v[200:203],a[104:107]
rd v[8:11],v236 offset:10240
rd v[12:15],v237 offset:10240
ld v233,s[44:47],s80 offen lds
mm a[112:115],v[160:163],v[208:211],0
s_add_u32 m0, s83, 35968
mm a[112:115],v[164:167],v[212:215],a[112:115]
ld v234,s[44:47],s80 offen lds
mm a[128:131],v[164:167],v[220:223],0
rd v[16:19],v236 offset:0
rd v[20:23],v237 offset:0
s_add_u32 m0, s83, 36992
mm a[128:131],v[160:163],v[216:219],a[128:131]
rd v[24:27],v236 offset:2048
rd v[28:31],v237 offset:2048
ld v235,s[44:47],s80 offen lds
mm a[132:135],v[168:171],v[216:219],0
s_mov_b32 s80, 2883584
mm a[132:135],v[172:175],v[220:223],a[132:135]
mm a[116:119],v[172:175],v[212:215],0
s_xor_b32 m0, s82, 32768
mm a[116:119],v[168:171],v[208:211],a[116:119]
rt v[32:33],v240 offset:128
rt v[34:35],v240 offset:1152
ld v224,s[40:43],s79 offen lds
mm a[108:111],v[168:171],v[200:203],0
s_xor_b32 m0, s82, 33792
mm a[108:111],v[172:175],v[204:207],a[108:111]
ld v225,s[40:43],s79 offen lds
mm a[100:103],v[172:175],v[196:199],0
s_xor_b32 m0, s82, 34816
mm a[100:103],v[168:171],v[192:195],a[100:103]
rt v[36:37],v240 offset:17024
rt v[38:39],v240 offset:18048
ld v226,s[40:43],s79 offen lds
mm a[232:235],v[176:179],v[192:195],0
s_xor_b32 m0, s82, 35840
mm a[232:235],v[180:183],v[196:199],a[232:235]
ld v227,s[40:43],s79 offen lds
mm a[152:155],v[180:183],v[204:207],0
s_xor_b32 m0, s82, 36864
mm a[152:155],v[176:179],v[200:203],a[152:155]
rt v[40:41],v241 offset:128
rt v[42:43],v241 offset:1152
ld v228,s[40:43],s79 offen lds
mm a[208:211],v[176:179],v[208:211],0
s_xor_b32 m0, s82, 37888
mm a[208:211],v[180:183],v[212:215],a[208:211]
ld v229,s[40:43],s79 offen lds
mm a[160:163],v[180:183],v[220:223],0
s_xor_b32 m0, s82, 38912
mm a[160:163],v[176:179],v[216:219],a[160:163]
ld v230,s[40:43],s79 offen lds
mm a[164:167],v[184:187],v[216:219],0
s_xor_b32 m0, s82, 39936
mm a[164:167],v[188:191],v[220:223],a[164:167]
ld v231,s[40:43],s79 offen lds
mm a[212:215],v[188:191],v[212:215],0
s_mov_b32 s79, 768
mm a[212:215],v[184:187],v[208:211],a[212:215]
rt v[44:45],v241 offset:17024
rt v[46:47],v241 offset:18048
mm a[156:159],v[184:187],v[200:203],0
s_add_u32 m0, s83, 50688
mm a[156:159],v[188:191],v[204:207],a[156:159]
ld v232,s[44:47],s80 offen lds
mm a[236:239],v[188:191],v[196:199],0
wt lgkmcnt(8)
s_add_u32 m0, s83, 51712
mm a[236:239],v[184:187],v[192:195],a[236:239]
ld v233,s[44:47],s80 offen lds
mm a[200:203],v[0:3],v[192:195],0
s_add_u32 m0, s83, 52864
mm a[200:203],v[4:7],v[196:199],a[200:203]
ld v234,s[44:47],s80 offen lds
mm a[184:187],v[4:7],v[204:207],0
s_add_u32 m0, s83, 53888
mm a[184:187],v[0:3],v[200:203],a[184:187]
rt v[48:49],v242 offset:128
rt v[50:51],v242 offset:1152
ld v235,s[44:47],s80 offen lds
mm a[64:67],v[0:3],v[208:211],0
s_mov_b32 s80, 3145728
mm a[64:67],v[4:7],v[212:215],a[64:67]
mm a[0:3],v[4:7],v[220:223],0
mm a[0:3],v[0:3],v[216:219],a[0:3]
mm a[4:7],v[8:11],v[216:219],0
mm a[4:7],v[12:15],v[220:223],a[4:7]
buffer_load_dword v70, v118, s[60:63], 0 offen
mm a[68:71],v[12:15],v[212:215],0
mm a[68:71],v[8:11],v[208:211],a[68:71]
rt v[52:53],v242 offset:17024
rt v[54:55],v242 offset:18048
mm a[188:191],v[8:11],v[200:203],0
mm a[188:191],v[12:15],v[204:207],a[188:191]
ld v[72:75],v119,s[60:63],0 offen offset:0
mm a[204:207],v[12:15],v[196:199],0
mm a[204:207],v[8:11],v[192:195],a[204:207]
rt v[56:57],v243 offset:128
rt v[58:59],v243 offset:1152
mm a[88:91],v[16:19],v[192:195],0
mm a[88:91],v[20:23],v[196:199],a[88:91]
ld v[76:79],v119,s[60:63],0 offen offset:128
mm a[24:27],v[20:23],v[204:207],0
mm a[24:27],v[16:19],v[200:203],a[24:27]
rt v[60:61],v243 offset:17024
rt v[62:63],v243 offset:18048
mm a[240:243],v[16:19],v[208:211],0
mm a[240:243],v[20:23],v[212:215],a[240:243]
ld v[80:83],v119,s[60:63],0 offen offset:256
mm a[32:35],v[20:23],v[220:223],0
mm a[32:35],v[16:19],v[216:219],a[32:35]
mm a[36:39],v[24:27],v[216:219],0
mm a[36:39],v[28:31],v[220:223],a[36:39]
ld v[84:87],v119,s[60:63],0 offen offset:384
mm a[244:247],v[28:31],v[212:215],0
mm a[244:247],v[24:27],v[208:211],a[244:247]
mm a[28:31],v[24:27],v[200:203],0
mm a[28:31],v[28:31],v[204:207],a[28:31]
mm a[92:95],v[28:31],v[196:199],0
mm a[92:95],v[24:27],v[192:195],a[92:95]
s_mov_b64 exec, 0
s_cmp_eq_u32 s3, 1
s_cbranch_scc1 .La5_LW2_1
s_branch .La5_LW2_R1
.La5_LW2_R1:
s_mov_b64 exec, -1
wt vmcnt(5) lgkmcnt(0)
s_barrier
mm a[120:123],v[160:163],v[32:35],0
mm a[120:123],v[164:167],v[36:39],a[120:123]
mm a[136:139],v[164:167],v[44:47],0
rt v[192:193],v240 offset:33792
rt v[194:195],v240 offset:34816
mm a[136:139],v[160:163],v[40:43],a[136:139]
rt v[196:197],v240 offset:50688
rt v[198:199],v240 offset:51712
mm a[224:227],v[160:163],v[48:51],0
mm a[224:227],v[164:167],v[52:55],a[224:227]
mm a[144:147],v[164:167],v[60:63],0
mm a[144:147],v[160:163],v[56:59],a[144:147]
rd v[160:163],v236 offset:57344
rd v[164:167],v237 offset:57344
mm a[148:151],v[168:171],v[56:59],0
mm a[148:151],v[172:175],v[60:63],a[148:151]
mm a[228:231],v[172:175],v[52:55],0
mm a[228:231],v[168:171],v[48:51],a[228:231]
rt v[200:201],v241 offset:33792
rt v[202:203],v241 offset:34816
mm a[140:143],v[168:171],v[40:43],0
mm a[140:143],v[172:175],v[44:47],a[140:143]
mm a[124:127],v[172:175],v[36:39],0
mm a[124:127],v[168:171],v[32:35],a[124:127]
rd v[168:171],v236 offset:59392
rd v[172:175],v237 offset:59392
mm a[216:219],v[176:179],v[32:35],0
s_xor_b32 m0, s82, 0
mm a[216:219],v[180:183],v[36:39],a[216:219]
ld v224,s[40:43],s79 offen lds
mm a[168:171],v[180:183],v[44:47],0
mm a[168:171],v[176:179],v[40:43],a[168:171]
rt v[204:205],v241 offset:50688
rt v[206:207],v241 offset:51712
s_xor_b32 m0, s82, 1024
mm a[192:195],v[176:179],v[48:51],0
ld v225,s[40:43],s79 offen lds
mm a[192:195],v[180:183],v[52:55],a[192:195]
mm a[176:179],v[180:183],v[60:63],0
s_xor_b32 m0, s82, 2048
mm a[176:179],v[176:179],v[56:59],a[176:179]
rd v[176:179],v236 offset:49152
rd v[180:183],v237 offset:49152
ld v226,s[40:43],s79 offen lds
mm a[180:183],v[184:187],v[56:59],0
mm a[180:183],v[188:191],v[60:63],a[180:183]
s_xor_b32 m0, s82, 3072
mm a[196:199],v[188:191],v[52:55],0
ld v227,s[40:43],s79 offen lds
mm a[196:199],v[184:187],v[48:51],a[196:199]
rt v[208:209],v242 offset:33792
rt v[210:211],v242 offset:34816
mm a[172:175],v[184:187],v[40:43],0
s_xor_b32 m0, s82, 4096
mm a[172:175],v[188:191],v[44:47],a[172:175]
ld v228,s[40:43],s79 offen lds
mm a[220:223],v[188:191],v[36:39],0
mm a[220:223],v[184:187],v[32:35],a[220:223]
rd v[184:187],v236 offset:51200
rd v[188:191],v237 offset:51200
s_xor_b32 m0, s82, 5120
mm a[72:75],v[0:3],v[32:35],0
ld v229,s[40:43],s79 offen lds
mm a[72:75],v[4:7],v[36:39],a[72:75]
mm a[8:11],v[4:7],v[44:47],0
s_xor_b32 m0, s82, 6144
mm a[8:11],v[0:3],v[40:43],a[8:11]
rt v[212:213],v242 offset:50688
rt v[214:215],v242 offset:51712
ld v230,s[40:43],s79 offen lds
mm a[80:83],v[0:3],v[48:51],0
mm a[80:83],v[4:7],v[52:55],a[80:83]
s_xor_b32 m0, s82, 7168
mm a[16:19],v[4:7],v[60:63],0
ld v231,s[40:43],s79 offen lds
mm a[16:19],v[0:3],v[56:59],a[16:19]
rd v[0:3],v236 offset:40960
rd v[4:7],v237 offset:40960
s_mov_b32 s79, 896
mm a[20:23],v[8:11],v[56:59],0
mm a[20:23],v[12:15],v[60:63],a[20:23]
s_add_u32 m0, s83, 0
mm a[84:87],v[12:15],v[52:55],0
ld v232,s[44:47],s80 offen lds
mm a[84:87],v[8:11],v[48:51],a[84:87]
rt v[216:217],v243 offset:33792
rt v[218:219],v243 offset:34816
mm a[12:15],v[8:11],v[40:43],0
s_add_u32 m0, s83, 1024
mm a[12:15],v[12:15],v[44:47],a[12:15]
ld v233,s[44:47],s80 offen lds
mm a[76:79],v[12:15],v[36:39],0
wt vmcnt(10)
v_lshlrev_b32 v70, 16, v70
mm a[76:79],v[8:11],v[32:35],a[76:79]
rd v[8:11],v236 offset:43008
rd v[12:15],v237 offset:43008
s_add_u32 m0, s83, 2176
mm a[248:251],v[16:19],v[32:35],0
ld v234,s[44:47],s80 offen lds
mm a[248:251],v[20:23],v[36:39],a[248:251]
v_lshlrev_b32 v88, 16, v72
v_and_b32 v89, 0xffff0000, v72
mm a[40:43],v[20:23],v[44:47],0
s_add_u32 m0, s83, 3200
v_sub_f32 v88, v88, v70
mm a[40:43],v[16:19],v[40:43],a[40:43]
rt v[220:221],v243 offset:50688
rt v[222:223],v243 offset:51712
ld v235,s[44:47],s80 offen lds
mm a[48:51],v[16:19],v[48:51],0
s_mov_b32 s80, 3407872
v_sub_f32 v89, v89, v70
mm a[48:51],v[20:23],v[52:55],a[48:51]
v_add_f32 v120, v88, v89
v_mul_f32 v122, v88, v88
mm a[56:59],v[20:23],v[60:63],0
s_add_u32 m0, s83, 16896
v_mul_f32 v123, v89, v89
mm a[56:59],v[16:19],v[56:59],a[56:59]
rd v[16:19],v236 offset:32768
rd v[20:23],v237 offset:32768
ld v232,s[44:47],s80 offen lds
mm a[60:63],v[24:27],v[56:59],0
v_lshlrev_b32 v90, 16, v73
v_and_b32 v91, 0xffff0000, v73
mm a[60:63],v[28:31],v[60:63],a[60:63]
s_add_u32 m0, s83, 17920
v_sub_f32 v90, v90, v70
mm a[52:55],v[28:31],v[52:55],0
ld v233,s[44:47],s80 offen lds
mm a[52:55],v[24:27],v[48:51],a[52:55]
v_sub_f32 v91, v91, v70
v_add_f32 v124, v90, v91
mm a[44:47],v[24:27],v[40:43],0
s_add_u32 m0, s83, 19072
v_fmac_f32 v122, v90, v90
mm a[44:47],v[28:31],v[44:47],a[44:47]
ld v234,s[44:47],s80 offen lds
mm a[252:255],v[28:31],v[36:39],0
wt lgkmcnt(2)
v_add_f32 v120, v120, v124
mm a[252:255],v[24:27],v[32:35],a[252:255]
rd v[24:27],v236 offset:34816
rd v[28:31],v237 offset:34816
s_add_u32 m0, s83, 20096
mm a[96:99],v[160:163],v[192:195],a[96:99]
ld v235,s[44:47],s80 offen lds
mm a[96:99],v[164:167],v[196:199],a[96:99]
s_mov_b32 s80, 3670016
v_fmac_f32 v123, v91, v91
mm a[104:107],v[164:167],v[204:207],a[104:107]
v_lshlrev_b32 v92, 16, v74
v_and_b32 v93, 0xffff0000, v74
mm a[104:107],v[160:163],v[200:203],a[104:107]
rt v[32:33],v240 offset:33920
rt v[34:35],v240 offset:34944
mm a[112:115],v[160:163],v[208:211],a[112:115]
v_sub_f32 v92, v92, v70
v_sub_f32 v93, v93, v70
mm a[112:115],v[164:167],v[212:215],a[112:115]
v_add_f32 v124, v92, v93
v_fmac_f32 v122, v92, v92
mm a[128:131],v[164:167],v[220:223],a[128:131]
wt lgkmcnt(2)
s_barrier
mm a[128:131],v[160:163],v[216:219],a[128:131]
v_add_f32 v120, v120, v124
v_fmac_f32 v123, v93, v93
mm a[132:135],v[168:171],v[216:219],a[132:135]
s_xor_b32 m0, s82, 32768
v_lshlrev_b32 v94, 16, v75
mm a[132:135],v[172:175],v[220:223],a[132:135]
ld v224,s[40:43],s79 offen lds
mm a[116:119],v[172:175],v[212:215],a[116:119]
v_and_b32 v95, 0xffff0000, v75
v_sub_f32 v94, v94, v70
mm a[116:119],v[168:171],v[208:211],a[116:119]
rt v[36:37],v240 offset:50816
rt v[38:39],v240 offset:51840
mm a[108:111],v[168:171],v[200:203],a[108:111]
s_xor_b32 m0, s82, 33792
v_sub_f32 v95, v95, v70
mm a[108:111],v[172:175],v[204:207],a[108:111]
ld v225,s[40:43],s79 offen lds
mm a[100:103],v[172:175],v[196:199],a[100:103]
v_add_f32 v124, v94, v95
v_fmac_f32 v122, v94, v94
mm a[100:103],v[168:171],v[192:195],a[100:103]
v_add_f32 v120, v120, v124
v_fmac_f32 v123, v95, v95
mm a[232:235],v[176:179],v[192:195],a[232:235]
s_xor_b32 m0, s82, 34816
v_lshlrev_b32 v96, 16, v76
mm a[232:235],v[180:183],v[196:199],a[232:235]
ld v226,s[40:43],s79 offen lds
mm a[152:155],v[180:183],v[204:207],a[152:155]
v_and_b32 v97, 0xffff0000, v76
v_sub_f32 v96, v96, v70
mm a[152:155],v[176:179],v[200:203],a[152:155]
rt v[40:41],v241 offset:33920
rt v[42:43],v241 offset:34944
mm a[208:211],v[176:179],v[208:211],a[208:211]
s_xor_b32 m0, s82, 35840
v_sub_f32 v97, v97, v70
mm a[208:211],v[180:183],v[212:215],a[208:211]
ld v227,s[40:43],s79 offen lds
mm a[160:163],v[180:183],v[220:223],a[160:163]
v_add_f32 v124, v96, v97
v_fmac_f32 v122, v96, v96
mm a[160:163],v[176:179],v[216:219],a[160:163]
v_add_f32 v120, v120, v124
v_fmac_f32 v123, v97, v97
mm a[164:167],v[184:187],v[216:219],a[164:167]
.La5_LW1_5:
s_cbranch_execz .La5_LW1_9
s_xor_b32 m0, s82, 36864
v_lshlrev_b32 v98, 16, v77
mm a[164:167],v[188:191],v[220:223],a[164:167]
ld v228,s[40:43],s79 offen lds
mm a[212:215],v[188:191],v[212:215],a[212:215]
v_and_b32 v99, 0xffff0000, v77
v_sub_f32 v98, v98, v70
mm a[212:215],v[184:187],v[208:211],a[212:215]
rt v[44:45],v241 offset:50816
rt v[46:47],v241 offset:51840
mm a[156:159],v[184:187],v[200:203],a[156:159]
s_xor_b32 m0, s82, 37888
v_sub_f32 v99, v99, v70
mm a[156:159],v[188:191],v[204:207],a[156:159]
ld v229,s[40:43],s79 offen lds
mm a[236:239],v[188:191],v[196:199],a[236:239]
v_add_f32 v124, v98, v99
v_fmac_f32 v122, v98, v98
mm a[236:239],v[184:187],v[192:195],a[236:239]
v_add_f32 v120, v120, v124
v_fmac_f32 v123, v99, v99
mm a[200:203],v[0:3],v[192:195],a[200:203]
s_xor_b32 m0, s82, 38912
v_lshlrev_b32 v100, 16, v78
mm a[200:203],v[4:7],v[196:199],a[200:203]
ld v230,s[40:43],s79 offen lds
mm a[184:187],v[4:7],v[204:207],a[184:187]
v_and_b32 v101, 0xffff0000, v78
v_sub_f32 v100, v100, v70
mm a[184:187],v[0:3],v[200:203],a[184:187]
rt v[48:49],v242 offset:33920
rt v[50:51],v242 offset:34944
mm a[64:67],v[0:3],v[208:211],a[64:67]
s_xor_b32 m0, s82, 39936
v_sub_f32 v101, v101, v70
mm a[64:67],v[4:7],v[212:215],a[64:67]
ld v231,s[40:43],s79 offen lds
mm a[0:3],v[4:7],v[220:223],a[0:3]
s_mov_b32 s79, 0
v_add_f32 v124, v100, v101
mm a[0:3],v[0:3],v[216:219],a[0:3]
v_fmac_f32 v122, v100, v100
v_add_f32 v120, v120, v124
mm a[4:7],v[8:11],v[216:219],a[4:7]
v_fmac_f32 v123, v101, v101
v_lshlrev_b32 v102, 16, v79
mm a[4:7],v[12:15],v[220:223],a[4:7]
buffer_load_dword v254, v132, s[44:47], s86 offen
mm a[68:71],v[12:15],v[212:215],a[68:71]
s_add_u32 s86, s86, 2097152
s_and_b32 s86, s86, 4194303
mm a[68:71],v[8:11],v[208:211],a[68:71]
rt v[52:53],v242 offset:50816
rt v[54:55],v242 offset:51840
mm a[188:191],v[8:11],v[200:203],a[188:191]
v_and_b32 v103, 0xffff0000, v79
v_sub_f32 v102, v102, v70
mm a[188:191],v[12:15],v[204:207],a[188:191]
v_sub_f32 v103, v103, v70
v_add_f32 v124, v102, v103
mm a[204:207],v[12:15],v[196:199],a[204:207]
v_fmac_f32 v122, v102, v102
v_add_f32 v120, v120, v124
mm a[204:207],v[8:11],v[192:195],a[204:207]
rt v[56:57],v243 offset:33920
rt v[58:59],v243 offset:34944
mm a[88:91],v[16:19],v[192:195],a[88:91]
v_fmac_f32 v123, v103, v103
v_lshlrev_b32 v104, 16, v80
mm a[88:91],v[20:23],v[196:199],a[88:91]
v_and_b32 v105, 0xffff0000, v80
v_sub_f32 v104, v104, v70
mm a[24:27],v[20:23],v[204:207],a[24:27]
v_sub_f32 v105, v105, v70
v_add_f32 v124, v104, v105
mm a[24:27],v[16:19],v[200:203],a[24:27]
rt v[60:61],v243 offset:50816
rt v[62:63],v243 offset:51840
mm a[240:243],v[16:19],v[208:211],a[240:243]
v_fmac_f32 v122, v104, v104
v_add_f32 v120, v120, v124
mm a[240:243],v[20:23],v[212:215],a[240:243]
v_fmac_f32 v123, v105, v105
v_lshlrev_b32 v106, 16, v81
mm a[32:35],v[20:23],v[220:223],a[32:35]
v_and_b32 v107, 0xffff0000, v81
v_sub_f32 v106, v106, v70
mm a[32:35],v[16:19],v[216:219],a[32:35]
v_sub_f32 v107, v107, v70
v_add_f32 v124, v106, v107
mm a[36:39],v[24:27],v[216:219],a[36:39]
v_fmac_f32 v122, v106, v106
v_add_f32 v120, v120, v124
mm a[36:39],v[28:31],v[220:223],a[36:39]
v_fmac_f32 v123, v107, v107
v_lshlrev_b32 v108, 16, v82
mm a[244:247],v[28:31],v[212:215],a[244:247]
v_and_b32 v109, 0xffff0000, v82
v_sub_f32 v108, v108, v70
mm a[244:247],v[24:27],v[208:211],a[244:247]
v_sub_f32 v109, v109, v70
v_add_f32 v124, v108, v109
mm a[28:31],v[24:27],v[200:203],a[28:31]
v_fmac_f32 v122, v108, v108
v_add_f32 v120, v120, v124
mm a[28:31],v[28:31],v[204:207],a[28:31]
v_fmac_f32 v123, v109, v109
v_lshlrev_b32 v110, 16, v83
mm a[92:95],v[28:31],v[196:199],a[92:95]
wt vmcnt(9) lgkmcnt(0)
s_barrier
mm a[92:95],v[24:27],v[192:195],a[92:95]
v_and_b32 v111, 0xffff0000, v83
v_sub_f32 v110, v110, v70
mm a[120:123],v[160:163],v[32:35],a[120:123]
v_sub_f32 v111, v111, v70
v_add_f32 v124, v110, v111
mm a[120:123],v[164:167],v[36:39],a[120:123]
v_fmac_f32 v122, v110, v110
v_add_f32 v120, v120, v124
mm a[136:139],v[164:167],v[44:47],a[136:139]
rt v[192:193],v240 offset:0
rt v[194:195],v240 offset:1024
mm a[136:139],v[160:163],v[40:43],a[136:139]
rt v[196:197],v240 offset:16896
rt v[198:199],v240 offset:17920
mm a[224:227],v[160:163],v[48:51],a[224:227]
v_fmac_f32 v123, v111, v111
v_lshlrev_b32 v112, 16, v84
mm a[224:227],v[164:167],v[52:55],a[224:227]
v_and_b32 v113, 0xffff0000, v84
v_sub_f32 v112, v112, v70
mm a[144:147],v[164:167],v[60:63],a[144:147]
v_sub_f32 v113, v113, v70
v_add_f32 v124, v112, v113
mm a[144:147],v[160:163],v[56:59],a[144:147]
rd v[160:163],v236 offset:24576
rd v[164:167],v237 offset:24576
mm a[148:151],v[168:171],v[56:59],a[148:151]
v_fmac_f32 v122, v112, v112
v_add_f32 v120, v120, v124
mm a[148:151],v[172:175],v[60:63],a[148:151]
v_fmac_f32 v123, v113, v113
v_lshlrev_b32 v114, 16, v85
mm a[228:231],v[172:175],v[52:55],a[228:231]
v_and_b32 v115, 0xffff0000, v85
v_sub_f32 v114, v114, v70
mm a[228:231],v[168:171],v[48:51],a[228:231]
rt v[200:201],v241 offset:0
rt v[202:203],v241 offset:1024
mm a[140:143],v[168:171],v[40:43],a[140:143]
v_sub_f32 v115, v115, v70
v_add_f32 v124, v114, v115
mm a[140:143],v[172:175],v[44:47],a[140:143]
v_fmac_f32 v122, v114, v114
v_add_f32 v120, v120, v124
mm a[124:127],v[172:175],v[36:39],a[124:127]
v_fmac_f32 v123, v115, v115
v_lshlrev_b32 v116, 16, v86
mm a[124:127],v[168:171],v[32:35],a[124:127]
rd v[168:171],v236 offset:26624
rd v[172:175],v237 offset:26624
mm a[216:219],v[176:179],v[32:35],a[216:219]
v_and_b32 v117, 0xffff0000, v86
v_sub_f32 v116, v116, v70
mm a[216:219],v[180:183],v[36:39],a[216:219]
v_sub_f32 v117, v117, v70
v_add_f32 v124, v116, v117
mm a[168:171],v[180:183],v[44:47],a[168:171]
v_fmac_f32 v122, v116, v116
v_add_f32 v120, v120, v124
mm a[168:171],v[176:179],v[40:43],a[168:171]
rt v[204:205],v241 offset:16896
rt v[206:207],v241 offset:17920
mm a[192:195],v[176:179],v[48:51],a[192:195]
v_fmac_f32 v123, v117, v117
v_lshlrev_b32 v118, 16, v87
mm a[192:195],v[180:183],v[52:55],a[192:195]
v_and_b32 v119, 0xffff0000, v87
v_sub_f32 v118, v118, v70
mm a[176:179],v[180:183],v[60:63],a[176:179]
v_sub_f32 v119, v119, v70
v_add_f32 v124, v118, v119
mm a[176:179],v[176:179],v[56:59],a[176:179]
rd v[176:179],v236 offset:16384
rd v[180:183],v237 offset:16384
mm a[180:183],v[184:187],v[56:59],a[180:183]
v_fmac_f32 v122, v118, v118
v_add_f32 v120, v120, v124
mm a[180:183],v[188:191],v[60:63],a[180:183]
v_fmac_f32 v123, v119, v119
v_add_f32 v121, v122, v123
mm a[196:199],v[188:191],v[52:55],a[196:199]
v_add_f32_dpp v120, v120, v120 row_ror:8 row_mask:0xf bank_mask:0xf
mm a[196:199],v[184:187],v[48:51],a[196:199]
rt v[208:209],v242 offset:0
rt v[210:211],v242 offset:1024
mm a[172:175],v[184:187],v[40:43],a[172:175]
s_add_u32 m0, s83, 33792
v_add_f32_dpp v121, v121, v121 row_ror:8 row_mask:0xf bank_mask:0xf
mm a[172:175],v[188:191],v[44:47],a[172:175]
ld v232,s[44:47],s80 offen lds
mm a[220:223],v[188:191],v[36:39],a[220:223]
v_mov_b32 v124, v120
v_mov_b32 v125, v121
mm a[220:223],v[184:187],v[32:35],a[220:223]
rd v[184:187],v236 offset:18432
rd v[188:191],v237 offset:18432
mm a[72:75],v[0:3],v[32:35],a[72:75]
s_add_u32 m0, s83, 34816
v_permlane16_swap_b32 v124, v120
mm a[72:75],v[4:7],v[36:39],a[72:75]
ld v233,s[44:47],s80 offen lds
mm a[8:11],v[4:7],v[44:47],a[8:11]
v_permlane16_swap_b32 v125, v121
mm a[8:11],v[0:3],v[40:43],a[8:11]
rt v[212:213],v242 offset:16896
rt v[214:215],v242 offset:17920
mm a[80:83],v[0:3],v[48:51],a[80:83]
s_add_u32 m0, s83, 35968
v_add_f32 v120, v120, v124
mm a[80:83],v[4:7],v[52:55],a[80:83]
ld v234,s[44:47],s80 offen lds
mm a[16:19],v[4:7],v[60:63],a[16:19]
v_add_f32 v121, v121, v125
v_mov_b32 v124, v120
mm a[16:19],v[0:3],v[56:59],a[16:19]
rd v[0:3],v236 offset:8192
rd v[4:7],v237 offset:8192
mm a[20:23],v[8:11],v[56:59],a[20:23]
s_add_u32 m0, s83, 36992
v_mov_b32 v125, v121
mm a[20:23],v[12:15],v[60:63],a[20:23]
ld v235,s[44:47],s80 offen lds
mm a[84:87],v[12:15],v[52:55],a[84:87]
s_mov_b32 s80, 3932160
v_permlane32_swap_b32 v124, v120
mm a[84:87],v[8:11],v[48:51],a[84:87]
rt v[216:217],v243 offset:0
rt v[218:219],v243 offset:1024
mm a[12:15],v[8:11],v[40:43],a[12:15]
s_add_u32 m0, s83, 50688
v_permlane32_swap_b32 v125, v121
mm a[12:15],v[12:15],v[44:47],a[12:15]
ld v232,s[44:47],s80 offen lds
mm a[76:79],v[12:15],v[36:39],a[76:79]
v_add_f32 v120, v120, v124
v_add_f32 v121, v121, v125
mm a[76:79],v[8:11],v[32:35],a[76:79]
rd v[8:11],v236 offset:10240
rd v[12:15],v237 offset:10240
mm a[248:251],v[16:19],v[32:35],a[248:251]
s_add_u32 m0, s83, 51712
ds_write_b64 v127, v[120:121]
mm a[248:251],v[20:23],v[36:39],a[248:251]
ld v233,s[44:47],s80 offen lds
mm a[40:43],v[20:23],v[44:47],a[40:43]
s_nop 0
v_readfirstlane_b32 s92, v252
mm a[40:43],v[16:19],v[40:43],a[40:43]
rt v[220:221],v243 offset:16896
rt v[222:223],v243 offset:17920
mm a[48:51],v[16:19],v[48:51],a[48:51]
s_add_u32 m0, s83, 52864
v_readfirstlane_b32 s93, v253
mm a[48:51],v[20:23],v[52:55],a[48:51]
ld v234,s[44:47],s80 offen lds
mm a[56:59],v[20:23],v[60:63],a[56:59]
s_add_u32 s92, s92, 1
s_addc_u32 s93, s93, 0
mm a[56:59],v[16:19],v[56:59],a[56:59]
rd v[16:19],v236 offset:0
rd v[20:23],v237 offset:0
mm a[60:63],v[24:27],v[56:59],a[60:63]
s_add_u32 m0, s83, 53888
v_mov_b32 v252, s92
mm a[60:63],v[28:31],v[60:63],a[60:63]
ld v235,s[44:47],s80 offen lds
mm a[52:55],v[28:31],v[52:55],a[52:55]
s_mov_b32 s80, 0
v_mov_b32 v253, s93
mm a[52:55],v[24:27],v[48:51],a[52:55]
v_mov_b32 v128, 0
v_mov_b32 v129, 0
mm a[44:47],v[24:27],v[40:43],a[44:47]
v_mov_b32 v130, 0
v_mov_b32 v131, 0
mm a[44:47],v[28:31],v[44:47],a[44:47]
st v[128:131],v255,s[52:55],s95 offen sc1
mm a[252:255],v[28:31],v[36:39],a[252:255]
wt lgkmcnt(2)
mm a[252:255],v[24:27],v[32:35],a[252:255]
rd v[24:27],v236 offset:2048
rd v[28:31],v237 offset:2048
mm a[96:99],v[160:163],v[192:195],a[96:99]
mm a[96:99],v[164:167],v[196:199],a[96:99]
mm a[104:107],v[164:167],v[204:207],a[104:107]
mm a[104:107],v[160:163],v[200:203],a[104:107]
rt v[32:33],v240 offset:128
rt v[34:35],v240 offset:1152
mm a[112:115],v[160:163],v[208:211],a[112:115]
mm a[112:115],v[164:167],v[212:215],a[112:115]
mm a[128:131],v[164:167],v[220:223],a[128:131]
wt lgkmcnt(2)
s_barrier
mm a[128:131],v[160:163],v[216:219],a[128:131]
rd v[72:75],v126
rd v[76:79],v126 offset:16
mm a[132:135],v[168:171],v[216:219],a[132:135]
s_xor_b32 m0, s82, 0
mm a[132:135],v[172:175],v[220:223],a[132:135]
ld v224,s[40:43],s79 offen lds
mm a[116:119],v[172:175],v[212:215],a[116:119]
mm a[116:119],v[168:171],v[208:211],a[116:119]
rt v[36:37],v240 offset:17024
rt v[38:39],v240 offset:18048
mm a[108:111],v[168:171],v[200:203],a[108:111]
s_xor_b32 m0, s82, 1024
mm a[108:111],v[172:175],v[204:207],a[108:111]
ld v225,s[40:43],s79 offen lds
mm a[100:103],v[172:175],v[196:199],a[100:103]
wt lgkmcnt(2)
v_add_f32 v72, v72, v74
mm a[100:103],v[168:171],v[192:195],a[100:103]
v_add_f32 v73, v73, v75
v_add_f32 v76, v76, v78
mm a[232:235],v[176:179],v[192:195],a[232:235]
s_xor_b32 m0, s82, 2048
v_add_f32 v77, v77, v79
mm a[232:235],v[180:183],v[196:199],a[232:235]
ld v226,s[40:43],s79 offen lds
mm a[152:155],v[180:183],v[204:207],a[152:155]
v_add_f32 v72, v72, v76
v_add_f32 v73, v73, v77
mm a[152:155],v[176:179],v[200:203],a[152:155]
rt v[40:41],v241 offset:128
rt v[42:43],v241 offset:1152
mm a[208:211],v[176:179],v[208:211],a[208:211]
s_xor_b32 m0, s82, 3072
v_mul_f32 v72, 0x3a800000, v72
mm a[208:211],v[180:183],v[212:215],a[208:211]
ld v227,s[40:43],s79 offen lds
mm a[160:163],v[180:183],v[220:223],a[160:163]
v_mul_f32 v73, 0x3a800000, v73
v_add_f32 v124, v70, v72
mm a[160:163],v[176:179],v[216:219],a[160:163]
v_fma_f32 v125, -v72, v72, v73
cv v124,v124,v124
mm a[164:167],v[184:187],v[216:219],a[164:167]
s_xor_b32 m0, s82, 4096
v_lshlrev_b32 v124, 16, v124
mm a[164:167],v[188:191],v[220:223],a[164:167]
ld v228,s[40:43],s79 offen lds
mm a[212:215],v[188:191],v[212:215],a[212:215]
v_max_f32 v125, 0, v125
cv v125,v125,v125
mm a[212:215],v[184:187],v[208:211],a[212:215]
rt v[44:45],v241 offset:17024
rt v[46:47],v241 offset:18048
mm a[156:159],v[184:187],v[200:203],a[156:159]
s_xor_b32 m0, s82, 5120
v_lshlrev_b32 v125, 16, v125
mm a[156:159],v[188:191],v[204:207],a[156:159]
ld v229,s[40:43],s79 offen lds
mm a[236:239],v[188:191],v[196:199],a[236:239]
v_add_f32 v125, 0x3727c5ac, v125
cv v125,v125,v125
mm a[236:239],v[184:187],v[192:195],a[236:239]
v_lshlrev_b32 v125, 16, v125
mm a[200:203],v[0:3],v[192:195],a[200:203]
s_xor_b32 m0, s82, 6144
mm a[200:203],v[4:7],v[196:199],a[200:203]
ld v230,s[40:43],s79 offen lds
mm a[184:187],v[4:7],v[204:207],a[184:187]
v_sqrt_f32 v125, v125
mm a[184:187],v[0:3],v[200:203],a[184:187]
rt v[48:49],v242 offset:128
rt v[50:51],v242 offset:1152
mm a[64:67],v[0:3],v[208:211],a[64:67]
s_xor_b32 m0, s82, 7168
v_sub_f32 v124, v70, v124
mm a[64:67],v[4:7],v[212:215],a[64:67]
ld v231,s[40:43],s79 offen lds
mm a[0:3],v[4:7],v[220:223],a[0:3]
s_mov_b32 s79, 128
cv v125,v125,v125
mm a[0:3],v[0:3],v[216:219],a[0:3]
v_lshlrev_b32 v125, 16, v125
mm a[4:7],v[8:11],v[216:219],a[4:7]
v_rcp_f32 v125, v125
mm a[4:7],v[12:15],v[220:223],a[4:7]
v_mul_f32 v125, 0x3e0293ee, v125
v_mul_f32 v124, v124, v125
mm a[68:71],v[12:15],v[212:215],a[68:71]
v_fma_f32 v88, v88, v125, v124
v_fma_f32 v89, v89, v125, v124
mm a[68:71],v[8:11],v[208:211],a[68:71]
rt v[52:53],v242 offset:17024
rt v[54:55],v242 offset:18048
mm a[188:191],v[8:11],v[200:203],a[188:191]
cv v72,v88,v89
v_fma_f32 v90, v90, v125, v124
mm a[188:191],v[12:15],v[204:207],a[188:191]
v_fma_f32 v91, v91, v125, v124
cv v73,v90,v91
mm a[204:207],v[12:15],v[196:199],a[204:207]
v_fma_f32 v92, v92, v125, v124
v_fma_f32 v93, v93, v125, v124
mm a[204:207],v[8:11],v[192:195],a[204:207]
rt v[56:57],v243 offset:128
rt v[58:59],v243 offset:1152
mm a[88:91],v[16:19],v[192:195],a[88:91]
cv v74,v92,v93
v_fma_f32 v94, v94, v125, v124
mm a[88:91],v[20:23],v[196:199],a[88:91]
v_fma_f32 v95, v95, v125, v124
cv v75,v94,v95
mm a[24:27],v[20:23],v[204:207],a[24:27]
st v[72:75],v71,s[56:59],0 offen offset:0 sc1
mm a[24:27],v[16:19],v[200:203],a[24:27]
rt v[60:61],v243 offset:17024
rt v[62:63],v243 offset:18048
mm a[240:243],v[16:19],v[208:211],a[240:243]
v_fma_f32 v96, v96, v125, v124
v_fma_f32 v97, v97, v125, v124
mm a[240:243],v[20:23],v[212:215],a[240:243]
cv v76,v96,v97
v_fma_f32 v98, v98, v125, v124
mm a[32:35],v[20:23],v[220:223],a[32:35]
v_fma_f32 v99, v99, v125, v124
cv v77,v98,v99
mm a[32:35],v[16:19],v[216:219],a[32:35]
v_fma_f32 v100, v100, v125, v124
v_fma_f32 v101, v101, v125, v124
mm a[36:39],v[24:27],v[216:219],a[36:39]
cv v78,v100,v101
v_fma_f32 v102, v102, v125, v124
mm a[36:39],v[28:31],v[220:223],a[36:39]
v_fma_f32 v103, v103, v125, v124
cv v79,v102,v103
mm a[244:247],v[28:31],v[212:215],a[244:247]
st v[76:79],v71,s[56:59],0 offen offset:2048 sc1
mm a[244:247],v[24:27],v[208:211],a[244:247]
v_fma_f32 v104, v104, v125, v124
v_fma_f32 v105, v105, v125, v124
mm a[28:31],v[24:27],v[200:203],a[28:31]
cv v80,v104,v105
v_fma_f32 v106, v106, v125, v124
mm a[28:31],v[28:31],v[204:207],a[28:31]
v_fma_f32 v107, v107, v125, v124
cv v81,v106,v107
mm a[92:95],v[28:31],v[196:199],a[92:95]
wt vmcnt(11) lgkmcnt(0)
s_barrier
mm a[92:95],v[24:27],v[192:195],a[92:95]
v_fma_f32 v108, v108, v125, v124
v_fma_f32 v109, v109, v125, v124
mm a[120:123],v[160:163],v[32:35],a[120:123]
cv v82,v108,v109
v_fma_f32 v110, v110, v125, v124
mm a[120:123],v[164:167],v[36:39],a[120:123]
v_fma_f32 v111, v111, v125, v124
cv v83,v110,v111
mm a[136:139],v[164:167],v[44:47],a[136:139]
rt v[192:193],v240 offset:33792
rt v[194:195],v240 offset:34816
mm a[136:139],v[160:163],v[40:43],a[136:139]
rt v[196:197],v240 offset:50688
rt v[198:199],v240 offset:51712
mm a[224:227],v[160:163],v[48:51],a[224:227]
st v[80:83],v71,s[56:59],s88 offen offset:0 sc1
mm a[224:227],v[164:167],v[52:55],a[224:227]
v_fma_f32 v112, v112, v125, v124
v_fma_f32 v113, v113, v125, v124
mm a[144:147],v[164:167],v[60:63],a[144:147]
cv v84,v112,v113
v_fma_f32 v114, v114, v125, v124
mm a[144:147],v[160:163],v[56:59],a[144:147]
rd v[160:163],v236 offset:57344
rd v[164:167],v237 offset:57344
mm a[148:151],v[168:171],v[56:59],a[148:151]
v_fma_f32 v115, v115, v125, v124
cv v85,v114,v115
mm a[148:151],v[172:175],v[60:63],a[148:151]
v_fma_f32 v116, v116, v125, v124
v_fma_f32 v117, v117, v125, v124
mm a[228:231],v[172:175],v[52:55],a[228:231]
cv v86,v116,v117
v_fma_f32 v118, v118, v125, v124
mm a[228:231],v[168:171],v[48:51],a[228:231]
rt v[200:201],v241 offset:33792
rt v[202:203],v241 offset:34816
mm a[140:143],v[168:171],v[40:43],a[140:143]
v_fma_f32 v119, v119, v125, v124
cv v87,v118,v119
mm a[140:143],v[172:175],v[44:47],a[140:143]
st v[84:87],v71,s[56:59],s88 offen offset:2048 sc1
mm a[124:127],v[172:175],v[36:39],a[124:127]
mm a[124:127],v[168:171],v[32:35],a[124:127]
rd v[168:171],v236 offset:59392
rd v[172:175],v237 offset:59392
mm a[216:219],v[176:179],v[32:35],a[216:219]
mm a[216:219],v[180:183],v[36:39],a[216:219]
mm a[168:171],v[180:183],v[44:47],a[168:171]
mm a[168:171],v[176:179],v[40:43],a[168:171]
rt v[204:205],v241 offset:50688
rt v[206:207],v241 offset:51712
mm a[192:195],v[176:179],v[48:51],a[192:195]
mm a[192:195],v[180:183],v[52:55],a[192:195]
mm a[176:179],v[180:183],v[60:63],a[176:179]
mm a[176:179],v[176:179],v[56:59],a[176:179]
rd v[176:179],v236 offset:49152
rd v[180:183],v237 offset:49152
mm a[180:183],v[184:187],v[56:59],a[180:183]
mm a[180:183],v[188:191],v[60:63],a[180:183]
mm a[196:199],v[188:191],v[52:55],a[196:199]
mm a[196:199],v[184:187],v[48:51],a[196:199]
rt v[208:209],v242 offset:33792
rt v[210:211],v242 offset:34816
mm a[172:175],v[184:187],v[40:43],a[172:175]
s_add_u32 m0, s83, 0
mm a[172:175],v[188:191],v[44:47],a[172:175]
ld v232,s[44:47],s80 offen lds
mm a[220:223],v[188:191],v[36:39],a[220:223]
mm a[220:223],v[184:187],v[32:35],a[220:223]
rd v[184:187],v236 offset:51200
rd v[188:191],v237 offset:51200
mm a[72:75],v[0:3],v[32:35],a[72:75]
s_add_u32 m0, s83, 1024
mm a[72:75],v[4:7],v[36:39],a[72:75]
ld v233,s[44:47],s80 offen lds
mm a[8:11],v[4:7],v[44:47],a[8:11]
mm a[8:11],v[0:3],v[40:43],a[8:11]
rt v[212:213],v242 offset:50688
rt v[214:215],v242 offset:51712
mm a[80:83],v[0:3],v[48:51],a[80:83]
s_add_u32 m0, s83, 2176
mm a[80:83],v[4:7],v[52:55],a[80:83]
ld v234,s[44:47],s80 offen lds
mm a[16:19],v[4:7],v[60:63],a[16:19]
mm a[16:19],v[0:3],v[56:59],a[16:19]
rd v[0:3],v236 offset:40960
rd v[4:7],v237 offset:40960
mm a[20:23],v[8:11],v[56:59],a[20:23]
s_add_u32 m0, s83, 3200
mm a[20:23],v[12:15],v[60:63],a[20:23]
ld v235,s[44:47],s80 offen lds
mm a[84:87],v[12:15],v[52:55],a[84:87]
s_mov_b32 s80, 262144
mm a[84:87],v[8:11],v[48:51],a[84:87]
rt v[216:217],v243 offset:33792
rt v[218:219],v243 offset:34816
mm a[12:15],v[8:11],v[40:43],a[12:15]
s_add_u32 m0, s83, 16896
mm a[12:15],v[12:15],v[44:47],a[12:15]
ld v232,s[44:47],s80 offen lds
mm a[76:79],v[12:15],v[36:39],a[76:79]
mm a[76:79],v[8:11],v[32:35],a[76:79]
rd v[8:11],v236 offset:43008
rd v[12:15],v237 offset:43008
mm a[248:251],v[16:19],v[32:35],a[248:251]
s_add_u32 m0, s83, 17920
mm a[248:251],v[20:23],v[36:39],a[248:251]
ld v233,s[44:47],s80 offen lds
mm a[40:43],v[20:23],v[44:47],a[40:43]
mm a[40:43],v[16:19],v[40:43],a[40:43]
rt v[220:221],v243 offset:50688
rt v[222:223],v243 offset:51712
mm a[48:51],v[16:19],v[48:51],a[48:51]
s_add_u32 m0, s83, 19072
mm a[48:51],v[20:23],v[52:55],a[48:51]
ld v234,s[44:47],s80 offen lds
mm a[56:59],v[20:23],v[60:63],a[56:59]
mm a[56:59],v[16:19],v[56:59],a[56:59]
rd v[16:19],v236 offset:32768
rd v[20:23],v237 offset:32768
.La5_LW1_6:
s_cbranch_execz .La5_LW1_10
mm a[60:63],v[24:27],v[56:59],a[60:63]
s_add_u32 m0, s83, 20096
mm a[60:63],v[28:31],v[60:63],a[60:63]
ld v235,s[44:47],s80 offen lds
mm a[52:55],v[28:31],v[52:55],a[52:55]
s_mov_b32 s80, 524288
mm a[52:55],v[24:27],v[48:51],a[52:55]
mm a[44:47],v[24:27],v[40:43],a[44:47]
mm a[44:47],v[28:31],v[44:47],a[44:47]
mm a[252:255],v[28:31],v[36:39],a[252:255]
wt lgkmcnt(2)
mm a[252:255],v[24:27],v[32:35],a[252:255]
rd v[24:27],v236 offset:34816
rd v[28:31],v237 offset:34816
mm a[96:99],v[160:163],v[192:195],a[96:99]
mm a[96:99],v[164:167],v[196:199],a[96:99]
mm a[104:107],v[164:167],v[204:207],a[104:107]
mm a[104:107],v[160:163],v[200:203],a[104:107]
rt v[32:33],v240 offset:33920
rt v[34:35],v240 offset:34944
mm a[112:115],v[160:163],v[208:211],a[112:115]
mm a[112:115],v[164:167],v[212:215],a[112:115]
mm a[128:131],v[164:167],v[220:223],a[128:131]
wt lgkmcnt(2)
s_barrier
mm a[128:131],v[160:163],v[216:219],a[128:131]
mm a[132:135],v[168:171],v[216:219],a[132:135]
s_xor_b32 m0, s82, 32768
mm a[132:135],v[172:175],v[220:223],a[132:135]
ld v224,s[40:43],s79 offen lds
mm a[116:119],v[172:175],v[212:215],a[116:119]
mm a[116:119],v[168:171],v[208:211],a[116:119]
rt v[36:37],v240 offset:50816
rt v[38:39],v240 offset:51840
mm a[108:111],v[168:171],v[200:203],a[108:111]
s_xor_b32 m0, s82, 33792
mm a[108:111],v[172:175],v[204:207],a[108:111]
ld v225,s[40:43],s79 offen lds
mm a[100:103],v[172:175],v[196:199],a[100:103]
mm a[100:103],v[168:171],v[192:195],a[100:103]
mm a[232:235],v[176:179],v[192:195],a[232:235]
s_xor_b32 m0, s82, 34816
mm a[232:235],v[180:183],v[196:199],a[232:235]
ld v226,s[40:43],s79 offen lds
mm a[152:155],v[180:183],v[204:207],a[152:155]
mm a[152:155],v[176:179],v[200:203],a[152:155]
rt v[40:41],v241 offset:33920
rt v[42:43],v241 offset:34944
mm a[208:211],v[176:179],v[208:211],a[208:211]
s_xor_b32 m0, s82, 35840
mm a[208:211],v[180:183],v[212:215],a[208:211]
ld v227,s[40:43],s79 offen lds
mm a[160:163],v[180:183],v[220:223],a[160:163]
mm a[160:163],v[176:179],v[216:219],a[160:163]
mm a[164:167],v[184:187],v[216:219],a[164:167]
s_xor_b32 m0, s82, 36864
mm a[164:167],v[188:191],v[220:223],a[164:167]
ld v228,s[40:43],s79 offen lds
mm a[212:215],v[188:191],v[212:215],a[212:215]
mm a[212:215],v[184:187],v[208:211],a[212:215]
rt v[44:45],v241 offset:50816
rt v[46:47],v241 offset:51840
mm a[156:159],v[184:187],v[200:203],a[156:159]
s_xor_b32 m0, s82, 37888
mm a[156:159],v[188:191],v[204:207],a[156:159]
ld v229,s[40:43],s79 offen lds
mm a[236:239],v[188:191],v[196:199],a[236:239]
mm a[236:239],v[184:187],v[192:195],a[236:239]
mm a[200:203],v[0:3],v[192:195],a[200:203]
s_xor_b32 m0, s82, 38912
mm a[200:203],v[4:7],v[196:199],a[200:203]
ld v230,s[40:43],s79 offen lds
mm a[184:187],v[4:7],v[204:207],a[184:187]
mm a[184:187],v[0:3],v[200:203],a[184:187]
rt v[48:49],v242 offset:33920
rt v[50:51],v242 offset:34944
mm a[64:67],v[0:3],v[208:211],a[64:67]
s_xor_b32 m0, s82, 39936
mm a[64:67],v[4:7],v[212:215],a[64:67]
ld v231,s[40:43],s79 offen lds
mm a[0:3],v[4:7],v[220:223],a[0:3]
s_mov_b32 s79, 256
mm a[0:3],v[0:3],v[216:219],a[0:3]
mm a[4:7],v[8:11],v[216:219],a[4:7]
mm a[4:7],v[12:15],v[220:223],a[4:7]
buffer_load_dword v254, v135, s[68:71], s96 offen
mm a[68:71],v[12:15],v[212:215],a[68:71]
s_add_u32 s96, s96, 1048576
mm a[68:71],v[8:11],v[208:211],a[68:71]
rt v[52:53],v242 offset:50816
rt v[54:55],v242 offset:51840
mm a[188:191],v[8:11],v[200:203],a[188:191]
mm a[188:191],v[12:15],v[204:207],a[188:191]
buffer_load_dword v254, v135, s[68:71], s96 offen
mm a[204:207],v[12:15],v[196:199],a[204:207]
s_add_u32 s96, s96, 1048576
mm a[204:207],v[8:11],v[192:195],a[204:207]
rt v[56:57],v243 offset:33920
rt v[58:59],v243 offset:34944
mm a[88:91],v[16:19],v[192:195],a[88:91]
mm a[88:91],v[20:23],v[196:199],a[88:91]
wt vmcnt(18)
buffer_store_dwordx2 v[252:253], off, s[64:67], s91 offset:1024 sc1
mm a[24:27],v[20:23],v[204:207],a[24:27]
s_mov_b32 s91, 0
s_lshl_b32 s92, s76, 19
mm a[24:27],v[16:19],v[200:203],a[24:27]
rt v[60:61],v243 offset:50816
rt v[62:63],v243 offset:51840
mm a[240:243],v[16:19],v[208:211],a[240:243]
s_add_u32 s92, s92, 88080384
mm a[240:243],v[20:23],v[212:215],a[240:243]
s_add_u32 s52, s26, s92
s_addc_u32 s53, s27, 0
mm a[32:35],v[20:23],v[220:223],a[32:35]
s_and_b32 s53, s53, 0xffff
s_mov_b32 s54, 524672
mm a[32:35],v[16:19],v[216:219],a[32:35]
s_mov_b32 s55, 0x20000
s_lshl_b32 s92, s3, 16
mm a[36:39],v[24:27],v[216:219],a[36:39]
s_sub_u32 s93, 458752, s92
v_add_u32 v244, s92, v136
mm a[36:39],v[28:31],v[220:223],a[36:39]
v_add_u32 v246, s93, v136
v_add_u32 v245, 0x8000, v244
mm a[244:247],v[28:31],v[212:215],a[244:247]
v_add_u32 v247, 0x8000, v246
s_lshl_b32 s94, s72, 19
mm a[244:247],v[24:27],v[208:211],a[244:247]
s_lshl_b32 s93, s76, 16
s_add_u32 s92, s94, s93
mm a[28:31],v[24:27],v[200:203],a[28:31]
s_lshl_b32 s93, s77, 13
s_add_u32 s92, s92, s93
mm a[28:31],v[28:31],v[204:207],a[28:31]
s_add_u32 s92, s92, 37748736
mm a[92:95],v[28:31],v[196:199],a[92:95]
wt vmcnt(11) lgkmcnt(0)
s_barrier
mm a[92:95],v[24:27],v[192:195],a[92:95]
s_add_u32 s48, s26, s92
s_addc_u32 s49, s27, 0
mm a[120:123],v[160:163],v[32:35],a[120:123]
s_and_b32 s49, s49, 0xffff
s_mov_b32 s50, 57344
mm a[120:123],v[164:167],v[36:39],a[120:123]
s_mov_b32 s51, 0x20000
s_lshl_b32 s93, s77, 24
mm a[136:139],v[164:167],v[44:47],a[136:139]
rt v[192:193],v240 offset:0
rt v[194:195],v240 offset:1024
mm a[136:139],v[160:163],v[40:43],a[136:139]
rt v[196:197],v240 offset:16896
rt v[198:199],v240 offset:17920
mm a[224:227],v[160:163],v[48:51],a[224:227]
s_add_u32 s92, s94, s93
s_add_u32 s92, s92, 20975616
mm a[224:227],v[164:167],v[52:55],a[224:227]
s_add_u32 s60, s26, s92
s_addc_u32 s61, s27, 0
mm a[144:147],v[164:167],v[60:63],a[144:147]
s_and_b32 s61, s61, 0xffff
s_mov_b32 s62, 520192
mm a[144:147],v[160:163],v[56:59],a[144:147]
rd v[160:163],v236 offset:24576
rd v[164:167],v237 offset:24576
mm a[148:151],v[168:171],v[56:59],a[148:151]
s_mov_b32 s63, 0x20000
s_lshl_b32 s92, s77, 14
mm a[148:151],v[172:175],v[60:63],a[148:151]
s_sub_u32 s95, 24576, s92
v_add_u32 v238, s95, v255
mm a[228:231],v[172:175],v[52:55],a[228:231]
mm a[228:231],v[168:171],v[48:51],a[228:231]
rt v[200:201],v241 offset:0
rt v[202:203],v241 offset:1024
mm a[140:143],v[168:171],v[40:43],a[140:143]
mm a[140:143],v[172:175],v[44:47],a[140:143]
mm a[124:127],v[172:175],v[36:39],a[124:127]
mm a[124:127],v[168:171],v[32:35],a[124:127]
rd v[168:171],v236 offset:26624
rd v[172:175],v237 offset:26624
mm a[216:219],v[176:179],v[32:35],a[216:219]
mm a[216:219],v[180:183],v[36:39],a[216:219]
mm a[168:171],v[180:183],v[44:47],a[168:171]
mm a[168:171],v[176:179],v[40:43],a[168:171]
rt v[204:205],v241 offset:16896
rt v[206:207],v241 offset:17920
mm a[192:195],v[176:179],v[48:51],a[192:195]
mm a[192:195],v[180:183],v[52:55],a[192:195]
mm a[176:179],v[180:183],v[60:63],a[176:179]
mm a[176:179],v[176:179],v[56:59],a[176:179]
rd v[176:179],v236 offset:16384
rd v[180:183],v237 offset:16384
mm a[180:183],v[184:187],v[56:59],a[180:183]
mm a[180:183],v[188:191],v[60:63],a[180:183]
mm a[196:199],v[188:191],v[52:55],a[196:199]
mm a[196:199],v[184:187],v[48:51],a[196:199]
rt v[208:209],v242 offset:0
rt v[210:211],v242 offset:1024
mm a[172:175],v[184:187],v[40:43],a[172:175]
s_add_u32 m0, s83, 33792
mm a[172:175],v[188:191],v[44:47],a[172:175]
ld v232,s[44:47],s80 offen lds
mm a[220:223],v[188:191],v[36:39],a[220:223]
mm a[220:223],v[184:187],v[32:35],a[220:223]
rd v[184:187],v236 offset:18432
rd v[188:191],v237 offset:18432
mm a[72:75],v[0:3],v[32:35],a[72:75]
s_add_u32 m0, s83, 34816
mm a[72:75],v[4:7],v[36:39],a[72:75]
ld v233,s[44:47],s80 offen lds
mm a[8:11],v[4:7],v[44:47],a[8:11]
mm a[8:11],v[0:3],v[40:43],a[8:11]
rt v[212:213],v242 offset:16896
rt v[214:215],v242 offset:17920
mm a[80:83],v[0:3],v[48:51],a[80:83]
s_add_u32 m0, s83, 35968
mm a[80:83],v[4:7],v[52:55],a[80:83]
ld v234,s[44:47],s80 offen lds
mm a[16:19],v[4:7],v[60:63],a[16:19]
mm a[16:19],v[0:3],v[56:59],a[16:19]
rd v[0:3],v236 offset:8192
rd v[4:7],v237 offset:8192
mm a[20:23],v[8:11],v[56:59],a[20:23]
s_add_u32 m0, s83, 36992
mm a[20:23],v[12:15],v[60:63],a[20:23]
ld v235,s[44:47],s80 offen lds
mm a[84:87],v[12:15],v[52:55],a[84:87]
s_mov_b32 s80, 786432
mm a[84:87],v[8:11],v[48:51],a[84:87]
rt v[216:217],v243 offset:0
rt v[218:219],v243 offset:1024
mm a[12:15],v[8:11],v[40:43],a[12:15]
s_add_u32 m0, s83, 50688
mm a[12:15],v[12:15],v[44:47],a[12:15]
ld v232,s[44:47],s80 offen lds
mm a[76:79],v[12:15],v[36:39],a[76:79]
mm a[76:79],v[8:11],v[32:35],a[76:79]
rd v[8:11],v236 offset:10240
rd v[12:15],v237 offset:10240
mm a[248:251],v[16:19],v[32:35],a[248:251]
s_add_u32 m0, s83, 51712
mm a[248:251],v[20:23],v[36:39],a[248:251]
ld v233,s[44:47],s80 offen lds
mm a[40:43],v[20:23],v[44:47],a[40:43]
mm a[40:43],v[16:19],v[40:43],a[40:43]
rt v[220:221],v243 offset:16896
rt v[222:223],v243 offset:17920
mm a[48:51],v[16:19],v[48:51],a[48:51]
s_add_u32 m0, s83, 52864
mm a[48:51],v[20:23],v[52:55],a[48:51]
ld v234,s[44:47],s80 offen lds
mm a[56:59],v[20:23],v[60:63],a[56:59]
mm a[56:59],v[16:19],v[56:59],a[56:59]
rd v[16:19],v236 offset:0
rd v[20:23],v237 offset:0
mm a[60:63],v[24:27],v[56:59],a[60:63]
s_add_u32 m0, s83, 53888
mm a[60:63],v[28:31],v[60:63],a[60:63]
ld v235,s[44:47],s80 offen lds
mm a[52:55],v[28:31],v[52:55],a[52:55]
s_mov_b32 s80, 1048576
mm a[52:55],v[24:27],v[48:51],a[52:55]
mm a[44:47],v[24:27],v[40:43],a[44:47]
mm a[44:47],v[28:31],v[44:47],a[44:47]
mm a[252:255],v[28:31],v[36:39],a[252:255]
wt lgkmcnt(2)
mm a[252:255],v[24:27],v[32:35],a[252:255]
rd v[24:27],v236 offset:2048
rd v[28:31],v237 offset:2048
mm a[96:99],v[160:163],v[192:195],a[96:99]
mm a[96:99],v[164:167],v[196:199],a[96:99]
mm a[104:107],v[164:167],v[204:207],a[104:107]
mm a[104:107],v[160:163],v[200:203],a[104:107]
rt v[32:33],v240 offset:128
rt v[34:35],v240 offset:1152
mm a[112:115],v[160:163],v[208:211],a[112:115]
mm a[112:115],v[164:167],v[212:215],a[112:115]
mm a[128:131],v[164:167],v[220:223],a[128:131]
wt lgkmcnt(2)
s_barrier
mm a[128:131],v[160:163],v[216:219],a[128:131]
mm a[132:135],v[168:171],v[216:219],a[132:135]
s_xor_b32 m0, s82, 0
mm a[132:135],v[172:175],v[220:223],a[132:135]
ld v224,s[40:43],s79 offen lds
mm a[116:119],v[172:175],v[212:215],a[116:119]
mm a[116:119],v[168:171],v[208:211],a[116:119]
rt v[36:37],v240 offset:17024
rt v[38:39],v240 offset:18048
mm a[108:111],v[168:171],v[200:203],a[108:111]
s_xor_b32 m0, s82, 1024
mm a[108:111],v[172:175],v[204:207],a[108:111]
ld v225,s[40:43],s79 offen lds
mm a[100:103],v[172:175],v[196:199],a[100:103]
mm a[100:103],v[168:171],v[192:195],a[100:103]
mm a[232:235],v[176:179],v[192:195],a[232:235]
s_xor_b32 m0, s82, 2048
mm a[232:235],v[180:183],v[196:199],a[232:235]
ld v226,s[40:43],s79 offen lds
mm a[152:155],v[180:183],v[204:207],a[152:155]
mm a[152:155],v[176:179],v[200:203],a[152:155]
rt v[40:41],v241 offset:128
rt v[42:43],v241 offset:1152
mm a[208:211],v[176:179],v[208:211],a[208:211]
s_xor_b32 m0, s82, 3072
mm a[208:211],v[180:183],v[212:215],a[208:211]
ld v227,s[40:43],s79 offen lds
mm a[160:163],v[180:183],v[220:223],a[160:163]
mm a[160:163],v[176:179],v[216:219],a[160:163]
mm a[164:167],v[184:187],v[216:219],a[164:167]
s_xor_b32 m0, s82, 4096
mm a[164:167],v[188:191],v[220:223],a[164:167]
ld v228,s[40:43],s79 offen lds
mm a[212:215],v[188:191],v[212:215],a[212:215]
mm a[212:215],v[184:187],v[208:211],a[212:215]
rt v[44:45],v241 offset:17024
rt v[46:47],v241 offset:18048
mm a[156:159],v[184:187],v[200:203],a[156:159]
s_xor_b32 m0, s82, 5120
mm a[156:159],v[188:191],v[204:207],a[156:159]
ld v229,s[40:43],s79 offen lds
mm a[236:239],v[188:191],v[196:199],a[236:239]
mm a[236:239],v[184:187],v[192:195],a[236:239]
mm a[200:203],v[0:3],v[192:195],a[200:203]
s_xor_b32 m0, s82, 6144
mm a[200:203],v[4:7],v[196:199],a[200:203]
ld v230,s[40:43],s79 offen lds
mm a[184:187],v[4:7],v[204:207],a[184:187]
mm a[184:187],v[0:3],v[200:203],a[184:187]
rt v[48:49],v242 offset:128
rt v[50:51],v242 offset:1152
mm a[64:67],v[0:3],v[208:211],a[64:67]
s_xor_b32 m0, s82, 7168
mm a[64:67],v[4:7],v[212:215],a[64:67]
ld v231,s[40:43],s79 offen lds
mm a[0:3],v[4:7],v[220:223],a[0:3]
s_mov_b32 s79, 384
mm a[0:3],v[0:3],v[216:219],a[0:3]
mm a[4:7],v[8:11],v[216:219],a[4:7]
mm a[4:7],v[12:15],v[220:223],a[4:7]
buffer_load_dword v254, v135, s[68:71], s96 offen
mm a[68:71],v[12:15],v[212:215],a[68:71]
s_add_u32 s96, s96, 1048576
mm a[68:71],v[8:11],v[208:211],a[68:71]
rt v[52:53],v242 offset:17024
rt v[54:55],v242 offset:18048
mm a[188:191],v[8:11],v[200:203],a[188:191]
mm a[188:191],v[12:15],v[204:207],a[188:191]
buffer_load_dword v254, v135, s[68:71], s96 offen
mm a[204:207],v[12:15],v[196:199],a[204:207]
s_add_u32 s96, s96, 1048576
mm a[204:207],v[8:11],v[192:195],a[204:207]
rt v[56:57],v243 offset:128
rt v[58:59],v243 offset:1152
mm a[88:91],v[16:19],v[192:195],a[88:91]
mm a[88:91],v[20:23],v[196:199],a[88:91]
mm a[24:27],v[20:23],v[204:207],a[24:27]
mm a[24:27],v[16:19],v[200:203],a[24:27]
rt v[60:61],v243 offset:17024
rt v[62:63],v243 offset:18048
mm a[240:243],v[16:19],v[208:211],a[240:243]
mm a[240:243],v[20:23],v[212:215],a[240:243]
mm a[32:35],v[20:23],v[220:223],a[32:35]
mm a[32:35],v[16:19],v[216:219],a[32:35]
mm a[36:39],v[24:27],v[216:219],a[36:39]
mm a[36:39],v[28:31],v[220:223],a[36:39]
mm a[244:247],v[28:31],v[212:215],a[244:247]
mm a[244:247],v[24:27],v[208:211],a[244:247]
mm a[28:31],v[24:27],v[200:203],a[28:31]
mm a[28:31],v[28:31],v[204:207],a[28:31]
mm a[92:95],v[28:31],v[196:199],a[92:95]
wt vmcnt(10) lgkmcnt(0)
s_barrier
mm a[92:95],v[24:27],v[192:195],a[92:95]
mm a[120:123],v[160:163],v[32:35],a[120:123]
mm a[120:123],v[164:167],v[36:39],a[120:123]
mm a[136:139],v[164:167],v[44:47],a[136:139]
rt v[192:193],v240 offset:33792
rt v[194:195],v240 offset:34816
mm a[136:139],v[160:163],v[40:43],a[136:139]
rt v[196:197],v240 offset:50688
rt v[198:199],v240 offset:51712
mm a[224:227],v[160:163],v[48:51],a[224:227]
mm a[224:227],v[164:167],v[52:55],a[224:227]
mm a[144:147],v[164:167],v[60:63],a[144:147]
mm a[144:147],v[160:163],v[56:59],a[144:147]
rd v[160:163],v236 offset:57344
rd v[164:167],v237 offset:57344
mm a[148:151],v[168:171],v[56:59],a[148:151]
mm a[148:151],v[172:175],v[60:63],a[148:151]
mm a[228:231],v[172:175],v[52:55],a[228:231]
mm a[228:231],v[168:171],v[48:51],a[228:231]
rt v[200:201],v241 offset:33792
rt v[202:203],v241 offset:34816
mm a[140:143],v[168:171],v[40:43],a[140:143]
mm a[140:143],v[172:175],v[44:47],a[140:143]
mm a[124:127],v[172:175],v[36:39],a[124:127]
mm a[124:127],v[168:171],v[32:35],a[124:127]
rd v[168:171],v236 offset:59392
rd v[172:175],v237 offset:59392
mm a[216:219],v[176:179],v[32:35],a[216:219]
mm a[216:219],v[180:183],v[36:39],a[216:219]
mm a[168:171],v[180:183],v[44:47],a[168:171]
mm a[168:171],v[176:179],v[40:43],a[168:171]
rt v[204:205],v241 offset:50688
rt v[206:207],v241 offset:51712
mm a[192:195],v[176:179],v[48:51],a[192:195]
mm a[192:195],v[180:183],v[52:55],a[192:195]
mm a[176:179],v[180:183],v[60:63],a[176:179]
mm a[176:179],v[176:179],v[56:59],a[176:179]
rd v[176:179],v236 offset:49152
rd v[180:183],v237 offset:49152
mm a[180:183],v[184:187],v[56:59],a[180:183]
mm a[180:183],v[188:191],v[60:63],a[180:183]
mm a[196:199],v[188:191],v[52:55],a[196:199]
mm a[196:199],v[184:187],v[48:51],a[196:199]
rt v[208:209],v242 offset:33792
rt v[210:211],v242 offset:34816
mm a[172:175],v[184:187],v[40:43],a[172:175]
s_add_u32 m0, s83, 0
mm a[172:175],v[188:191],v[44:47],a[172:175]
ld v232,s[44:47],s80 offen lds
mm a[220:223],v[188:191],v[36:39],a[220:223]
mm a[220:223],v[184:187],v[32:35],a[220:223]
rd v[184:187],v236 offset:51200
rd v[188:191],v237 offset:51200
mm a[72:75],v[0:3],v[32:35],a[72:75]
s_add_u32 m0, s83, 1024
mm a[72:75],v[4:7],v[36:39],a[72:75]
ld v233,s[44:47],s80 offen lds
mm a[8:11],v[4:7],v[44:47],a[8:11]
mm a[8:11],v[0:3],v[40:43],a[8:11]
rt v[212:213],v242 offset:50688
rt v[214:215],v242 offset:51712
mm a[80:83],v[0:3],v[48:51],a[80:83]
s_add_u32 m0, s83, 2176
mm a[80:83],v[4:7],v[52:55],a[80:83]
ld v234,s[44:47],s80 offen lds
mm a[16:19],v[4:7],v[60:63],a[16:19]
mm a[16:19],v[0:3],v[56:59],a[16:19]
rd v[0:3],v236 offset:40960
rd v[4:7],v237 offset:40960
mm a[20:23],v[8:11],v[56:59],a[20:23]
s_add_u32 m0, s83, 3200
mm a[20:23],v[12:15],v[60:63],a[20:23]
ld v235,s[44:47],s80 offen lds
mm a[84:87],v[12:15],v[52:55],a[84:87]
s_mov_b32 s80, 1310720
mm a[84:87],v[8:11],v[48:51],a[84:87]
rt v[216:217],v243 offset:33792
rt v[218:219],v243 offset:34816
mm a[12:15],v[8:11],v[40:43],a[12:15]
s_add_u32 m0, s83, 16896
mm a[12:15],v[12:15],v[44:47],a[12:15]
ld v232,s[44:47],s80 offen lds
mm a[76:79],v[12:15],v[36:39],a[76:79]
mm a[76:79],v[8:11],v[32:35],a[76:79]
rd v[8:11],v236 offset:43008
rd v[12:15],v237 offset:43008
mm a[248:251],v[16:19],v[32:35],a[248:251]
s_add_u32 m0, s83, 17920
mm a[248:251],v[20:23],v[36:39],a[248:251]
ld v233,s[44:47],s80 offen lds
mm a[40:43],v[20:23],v[44:47],a[40:43]
mm a[40:43],v[16:19],v[40:43],a[40:43]
rt v[220:221],v243 offset:50688
rt v[222:223],v243 offset:51712
mm a[48:51],v[16:19],v[48:51],a[48:51]
s_add_u32 m0, s83, 19072
mm a[48:51],v[20:23],v[52:55],a[48:51]
ld v234,s[44:47],s80 offen lds
mm a[56:59],v[20:23],v[60:63],a[56:59]
mm a[56:59],v[16:19],v[56:59],a[56:59]
rd v[16:19],v236 offset:32768
rd v[20:23],v237 offset:32768
mm a[60:63],v[24:27],v[56:59],a[60:63]
s_add_u32 m0, s83, 20096
mm a[60:63],v[28:31],v[60:63],a[60:63]
ld v235,s[44:47],s80 offen lds
mm a[52:55],v[28:31],v[52:55],a[52:55]
s_mov_b32 s80, 1572864
mm a[52:55],v[24:27],v[48:51],a[52:55]
mm a[44:47],v[24:27],v[40:43],a[44:47]
mm a[44:47],v[28:31],v[44:47],a[44:47]
mm a[252:255],v[28:31],v[36:39],a[252:255]
wt lgkmcnt(2)
mm a[252:255],v[24:27],v[32:35],a[252:255]
rd v[24:27],v236 offset:34816
rd v[28:31],v237 offset:34816
mm a[96:99],v[160:163],v[192:195],a[96:99]
mm a[96:99],v[164:167],v[196:199],a[96:99]
mm a[104:107],v[164:167],v[204:207],a[104:107]
mm a[104:107],v[160:163],v[200:203],a[104:107]
rt v[32:33],v240 offset:33920
rt v[34:35],v240 offset:34944
mm a[112:115],v[160:163],v[208:211],a[112:115]
mm a[112:115],v[164:167],v[212:215],a[112:115]
mm a[128:131],v[164:167],v[220:223],a[128:131]
wt lgkmcnt(2)
s_barrier
mm a[128:131],v[160:163],v[216:219],a[128:131]
mm a[132:135],v[168:171],v[216:219],a[132:135]
s_xor_b32 m0, s82, 32768
mm a[132:135],v[172:175],v[220:223],a[132:135]
ld v224,s[40:43],s79 offen lds
mm a[116:119],v[172:175],v[212:215],a[116:119]
mm a[116:119],v[168:171],v[208:211],a[116:119]
rt v[36:37],v240 offset:50816
rt v[38:39],v240 offset:51840
mm a[108:111],v[168:171],v[200:203],a[108:111]
s_xor_b32 m0, s82, 33792
mm a[108:111],v[172:175],v[204:207],a[108:111]
ld v225,s[40:43],s79 offen lds
mm a[100:103],v[172:175],v[196:199],a[100:103]
mm a[100:103],v[168:171],v[192:195],a[100:103]
mm a[232:235],v[176:179],v[192:195],a[232:235]
s_xor_b32 m0, s82, 34816
mm a[232:235],v[180:183],v[196:199],a[232:235]
ld v226,s[40:43],s79 offen lds
mm a[152:155],v[180:183],v[204:207],a[152:155]
mm a[152:155],v[176:179],v[200:203],a[152:155]
rt v[40:41],v241 offset:33920
rt v[42:43],v241 offset:34944
mm a[208:211],v[176:179],v[208:211],a[208:211]
s_xor_b32 m0, s82, 35840
mm a[208:211],v[180:183],v[212:215],a[208:211]
.La5_LW1_7:
s_cbranch_execz .La5_LW1_11
ld v227,s[40:43],s79 offen lds
mm a[160:163],v[180:183],v[220:223],a[160:163]
mm a[160:163],v[176:179],v[216:219],a[160:163]
mm a[164:167],v[184:187],v[216:219],a[164:167]
s_xor_b32 m0, s82, 36864
mm a[164:167],v[188:191],v[220:223],a[164:167]
ld v228,s[40:43],s79 offen lds
mm a[212:215],v[188:191],v[212:215],a[212:215]
mm a[212:215],v[184:187],v[208:211],a[212:215]
rt v[44:45],v241 offset:50816
rt v[46:47],v241 offset:51840
mm a[156:159],v[184:187],v[200:203],a[156:159]
s_xor_b32 m0, s82, 37888
mm a[156:159],v[188:191],v[204:207],a[156:159]
ld v229,s[40:43],s79 offen lds
mm a[236:239],v[188:191],v[196:199],a[236:239]
mm a[236:239],v[184:187],v[192:195],a[236:239]
mm a[200:203],v[0:3],v[192:195],a[200:203]
s_xor_b32 m0, s82, 38912
mm a[200:203],v[4:7],v[196:199],a[200:203]
ld v230,s[40:43],s79 offen lds
mm a[184:187],v[4:7],v[204:207],a[184:187]
mm a[184:187],v[0:3],v[200:203],a[184:187]
rt v[48:49],v242 offset:33920
rt v[50:51],v242 offset:34944
mm a[64:67],v[0:3],v[208:211],a[64:67]
s_xor_b32 m0, s82, 39936
mm a[64:67],v[4:7],v[212:215],a[64:67]
ld v231,s[40:43],s79 offen lds
mm a[0:3],v[4:7],v[220:223],a[0:3]
s_mov_b32 s79, 512
mm a[0:3],v[0:3],v[216:219],a[0:3]
mm a[4:7],v[8:11],v[216:219],a[4:7]
mm a[4:7],v[12:15],v[220:223],a[4:7]
buffer_load_dword v254, v135, s[68:71], s96 offen
mm a[68:71],v[12:15],v[212:215],a[68:71]
s_add_u32 s96, s96, 1048576
mm a[68:71],v[8:11],v[208:211],a[68:71]
rt v[52:53],v242 offset:50816
rt v[54:55],v242 offset:51840
mm a[188:191],v[8:11],v[200:203],a[188:191]
mm a[188:191],v[12:15],v[204:207],a[188:191]
buffer_load_dword v254, v135, s[68:71], s96 offen
mm a[204:207],v[12:15],v[196:199],a[204:207]
s_add_u32 s96, s96, 1048576
mm a[204:207],v[8:11],v[192:195],a[204:207]
rt v[56:57],v243 offset:33920
rt v[58:59],v243 offset:34944
mm a[88:91],v[16:19],v[192:195],a[88:91]
mm a[88:91],v[20:23],v[196:199],a[88:91]
mm a[24:27],v[20:23],v[204:207],a[24:27]
mm a[24:27],v[16:19],v[200:203],a[24:27]
rt v[60:61],v243 offset:50816
rt v[62:63],v243 offset:51840
mm a[240:243],v[16:19],v[208:211],a[240:243]
mm a[240:243],v[20:23],v[212:215],a[240:243]
mm a[32:35],v[20:23],v[220:223],a[32:35]
mm a[32:35],v[16:19],v[216:219],a[32:35]
mm a[36:39],v[24:27],v[216:219],a[36:39]
mm a[36:39],v[28:31],v[220:223],a[36:39]
mm a[244:247],v[28:31],v[212:215],a[244:247]
mm a[244:247],v[24:27],v[208:211],a[244:247]
mm a[28:31],v[24:27],v[200:203],a[28:31]
mm a[28:31],v[28:31],v[204:207],a[28:31]
mm a[92:95],v[28:31],v[196:199],a[92:95]
wt vmcnt(10) lgkmcnt(0)
s_barrier
mm a[92:95],v[24:27],v[192:195],a[92:95]
mm a[120:123],v[160:163],v[32:35],a[120:123]
mm a[120:123],v[164:167],v[36:39],a[120:123]
mm a[136:139],v[164:167],v[44:47],a[136:139]
rt v[192:193],v240 offset:0
rt v[194:195],v240 offset:1024
mm a[136:139],v[160:163],v[40:43],a[136:139]
rt v[196:197],v240 offset:16896
rt v[198:199],v240 offset:17920
mm a[224:227],v[160:163],v[48:51],a[224:227]
mm a[224:227],v[164:167],v[52:55],a[224:227]
mm a[144:147],v[164:167],v[60:63],a[144:147]
mm a[144:147],v[160:163],v[56:59],a[144:147]
rd v[160:163],v236 offset:24576
rd v[164:167],v237 offset:24576
mm a[148:151],v[168:171],v[56:59],a[148:151]
mm a[148:151],v[172:175],v[60:63],a[148:151]
mm a[228:231],v[172:175],v[52:55],a[228:231]
mm a[228:231],v[168:171],v[48:51],a[228:231]
rt v[200:201],v241 offset:0
rt v[202:203],v241 offset:1024
mm a[140:143],v[168:171],v[40:43],a[140:143]
mm a[140:143],v[172:175],v[44:47],a[140:143]
mm a[124:127],v[172:175],v[36:39],a[124:127]
mm a[124:127],v[168:171],v[32:35],a[124:127]
rd v[168:171],v236 offset:26624
rd v[172:175],v237 offset:26624
mm a[216:219],v[176:179],v[32:35],a[216:219]
mm a[216:219],v[180:183],v[36:39],a[216:219]
mm a[168:171],v[180:183],v[44:47],a[168:171]
mm a[168:171],v[176:179],v[40:43],a[168:171]
rt v[204:205],v241 offset:16896
rt v[206:207],v241 offset:17920
mm a[192:195],v[176:179],v[48:51],a[192:195]
mm a[192:195],v[180:183],v[52:55],a[192:195]
mm a[176:179],v[180:183],v[60:63],a[176:179]
mm a[176:179],v[176:179],v[56:59],a[176:179]
rd v[176:179],v236 offset:16384
rd v[180:183],v237 offset:16384
mm a[180:183],v[184:187],v[56:59],a[180:183]
mm a[180:183],v[188:191],v[60:63],a[180:183]
mm a[196:199],v[188:191],v[52:55],a[196:199]
mm a[196:199],v[184:187],v[48:51],a[196:199]
rt v[208:209],v242 offset:0
rt v[210:211],v242 offset:1024
mm a[172:175],v[184:187],v[40:43],a[172:175]
s_add_u32 m0, s83, 33792
mm a[172:175],v[188:191],v[44:47],a[172:175]
ld v232,s[44:47],s80 offen lds
mm a[220:223],v[188:191],v[36:39],a[220:223]
mm a[220:223],v[184:187],v[32:35],a[220:223]
rd v[184:187],v236 offset:18432
rd v[188:191],v237 offset:18432
mm a[72:75],v[0:3],v[32:35],a[72:75]
s_add_u32 m0, s83, 34816
mm a[72:75],v[4:7],v[36:39],a[72:75]
ld v233,s[44:47],s80 offen lds
mm a[8:11],v[4:7],v[44:47],a[8:11]
mm a[8:11],v[0:3],v[40:43],a[8:11]
rt v[212:213],v242 offset:16896
rt v[214:215],v242 offset:17920
mm a[80:83],v[0:3],v[48:51],a[80:83]
s_add_u32 m0, s83, 35968
mm a[80:83],v[4:7],v[52:55],a[80:83]
ld v234,s[44:47],s80 offen lds
mm a[16:19],v[4:7],v[60:63],a[16:19]
mm a[16:19],v[0:3],v[56:59],a[16:19]
rd v[0:3],v236 offset:8192
rd v[4:7],v237 offset:8192
mm a[20:23],v[8:11],v[56:59],a[20:23]
s_add_u32 m0, s83, 36992
mm a[20:23],v[12:15],v[60:63],a[20:23]
ld v235,s[44:47],s80 offen lds
mm a[84:87],v[12:15],v[52:55],a[84:87]
s_mov_b32 s80, 1835008
mm a[84:87],v[8:11],v[48:51],a[84:87]
rt v[216:217],v243 offset:0
rt v[218:219],v243 offset:1024
mm a[12:15],v[8:11],v[40:43],a[12:15]
s_add_u32 m0, s83, 50688
mm a[12:15],v[12:15],v[44:47],a[12:15]
ld v232,s[44:47],s80 offen lds
mm a[76:79],v[12:15],v[36:39],a[76:79]
mm a[76:79],v[8:11],v[32:35],a[76:79]
rd v[8:11],v236 offset:10240
rd v[12:15],v237 offset:10240
mm a[248:251],v[16:19],v[32:35],a[248:251]
s_add_u32 m0, s83, 51712
mm a[248:251],v[20:23],v[36:39],a[248:251]
ld v233,s[44:47],s80 offen lds
mm a[40:43],v[20:23],v[44:47],a[40:43]
mm a[40:43],v[16:19],v[40:43],a[40:43]
rt v[220:221],v243 offset:16896
rt v[222:223],v243 offset:17920
mm a[48:51],v[16:19],v[48:51],a[48:51]
s_add_u32 m0, s83, 52864
mm a[48:51],v[20:23],v[52:55],a[48:51]
ld v234,s[44:47],s80 offen lds
mm a[56:59],v[20:23],v[60:63],a[56:59]
mm a[56:59],v[16:19],v[56:59],a[56:59]
rd v[16:19],v236 offset:0
rd v[20:23],v237 offset:0
mm a[60:63],v[24:27],v[56:59],a[60:63]
s_add_u32 m0, s83, 53888
mm a[60:63],v[28:31],v[60:63],a[60:63]
ld v235,s[44:47],s80 offen lds
mm a[52:55],v[28:31],v[52:55],a[52:55]
s_mov_b64 s[44:45], s[68:69]
s_mov_b32 s46, s70
mm a[52:55],v[24:27],v[48:51],a[52:55]
mm a[44:47],v[24:27],v[40:43],a[44:47]
mm a[44:47],v[28:31],v[44:47],a[44:47]
mm a[252:255],v[28:31],v[36:39],a[252:255]
wt lgkmcnt(2)
s_lshl_b32 s80, s77, 18
mm a[252:255],v[24:27],v[32:35],a[252:255]
rd v[24:27],v236 offset:2048
rd v[28:31],v237 offset:2048
mm a[96:99],v[160:163],v[192:195],a[96:99]
mm a[96:99],v[164:167],v[196:199],a[96:99]
mm a[104:107],v[164:167],v[204:207],a[104:107]
mm a[104:107],v[160:163],v[200:203],a[104:107]
rt v[32:33],v240 offset:128
rt v[34:35],v240 offset:1152
mm a[112:115],v[160:163],v[208:211],a[112:115]
mm a[112:115],v[164:167],v[212:215],a[112:115]
mm a[128:131],v[164:167],v[220:223],a[128:131]
wt lgkmcnt(2)
s_barrier
mm a[128:131],v[160:163],v[216:219],a[128:131]
mm a[132:135],v[168:171],v[216:219],a[132:135]
mm a[132:135],v[172:175],v[220:223],a[132:135]
mm a[116:119],v[172:175],v[212:215],a[116:119]
mm a[116:119],v[168:171],v[208:211],a[116:119]
rt v[36:37],v240 offset:17024
rt v[38:39],v240 offset:18048
mm a[108:111],v[168:171],v[200:203],a[108:111]
mm a[108:111],v[172:175],v[204:207],a[108:111]
mm a[100:103],v[172:175],v[196:199],a[100:103]
mm a[100:103],v[168:171],v[192:195],a[100:103]
mm a[232:235],v[176:179],v[192:195],a[232:235]
mm a[232:235],v[180:183],v[196:199],a[232:235]
mm a[152:155],v[180:183],v[204:207],a[152:155]
mm a[152:155],v[176:179],v[200:203],a[152:155]
rt v[40:41],v241 offset:128
rt v[42:43],v241 offset:1152
mm a[208:211],v[176:179],v[208:211],a[208:211]
mm a[208:211],v[180:183],v[212:215],a[208:211]
mm a[160:163],v[180:183],v[220:223],a[160:163]
mm a[160:163],v[176:179],v[216:219],a[160:163]
mm a[164:167],v[184:187],v[216:219],a[164:167]
mm a[164:167],v[188:191],v[220:223],a[164:167]
mm a[212:215],v[188:191],v[212:215],a[212:215]
mm a[212:215],v[184:187],v[208:211],a[212:215]
rt v[44:45],v241 offset:17024
rt v[46:47],v241 offset:18048
mm a[156:159],v[184:187],v[200:203],a[156:159]
mm a[156:159],v[188:191],v[204:207],a[156:159]
mm a[236:239],v[188:191],v[196:199],a[236:239]
mm a[236:239],v[184:187],v[192:195],a[236:239]
mm a[200:203],v[0:3],v[192:195],a[200:203]
mm a[200:203],v[4:7],v[196:199],a[200:203]
mm a[184:187],v[4:7],v[204:207],a[184:187]
mm a[184:187],v[0:3],v[200:203],a[184:187]
rt v[48:49],v242 offset:128
rt v[50:51],v242 offset:1152
mm a[64:67],v[0:3],v[208:211],a[64:67]
mm a[64:67],v[4:7],v[212:215],a[64:67]
mm a[0:3],v[4:7],v[220:223],a[0:3]
mm a[0:3],v[0:3],v[216:219],a[0:3]
mm a[4:7],v[8:11],v[216:219],a[4:7]
mm a[4:7],v[12:15],v[220:223],a[4:7]
buffer_load_dword v254, v135, s[68:71], s96 offen
mm a[68:71],v[12:15],v[212:215],a[68:71]
s_add_u32 s96, s96, 1048576
mm a[68:71],v[8:11],v[208:211],a[68:71]
rt v[52:53],v242 offset:17024
rt v[54:55],v242 offset:18048
mm a[188:191],v[8:11],v[200:203],a[188:191]
mm a[188:191],v[12:15],v[204:207],a[188:191]
buffer_load_dword v254, v135, s[68:71], s96 offen
mm a[204:207],v[12:15],v[196:199],a[204:207]
s_add_u32 s96, s96, 1048576
mm a[204:207],v[8:11],v[192:195],a[204:207]
rt v[56:57],v243 offset:128
rt v[58:59],v243 offset:1152
mm a[88:91],v[16:19],v[192:195],a[88:91]
mm a[88:91],v[20:23],v[196:199],a[88:91]
mm a[24:27],v[20:23],v[204:207],a[24:27]
mm a[24:27],v[16:19],v[200:203],a[24:27]
rt v[60:61],v243 offset:17024
rt v[62:63],v243 offset:18048
mm a[240:243],v[16:19],v[208:211],a[240:243]
mm a[240:243],v[20:23],v[212:215],a[240:243]
mm a[32:35],v[20:23],v[220:223],a[32:35]
mm a[32:35],v[16:19],v[216:219],a[32:35]
mm a[36:39],v[24:27],v[216:219],a[36:39]
mm a[36:39],v[28:31],v[220:223],a[36:39]
ld v[72:75],v255,s[64:67],s89 offen offset:1024 sc1
mm a[244:247],v[28:31],v[212:215],a[244:247]
mm a[244:247],v[24:27],v[208:211],a[244:247]
mm a[28:31],v[24:27],v[200:203],a[28:31]
mm a[28:31],v[28:31],v[204:207],a[28:31]
mm a[92:95],v[28:31],v[196:199],a[92:95]
wt vmcnt(3) lgkmcnt(0)
s_barrier
mm a[92:95],v[24:27],v[192:195],a[92:95]
mm a[120:123],v[160:163],v[32:35],a[120:123]
mm a[120:123],v[164:167],v[36:39],a[120:123]
mm a[136:139],v[164:167],v[44:47],a[136:139]
rt v[192:193],v240 offset:33792
rt v[194:195],v240 offset:34816
mm a[136:139],v[160:163],v[40:43],a[136:139]
rt v[196:197],v240 offset:50688
rt v[198:199],v240 offset:51712
mm a[224:227],v[160:163],v[48:51],a[224:227]
mm a[224:227],v[164:167],v[52:55],a[224:227]
mm a[144:147],v[164:167],v[60:63],a[144:147]
mm a[144:147],v[160:163],v[56:59],a[144:147]
rd v[160:163],v236 offset:57344
rd v[164:167],v237 offset:57344
mm a[148:151],v[168:171],v[56:59],a[148:151]
mm a[148:151],v[172:175],v[60:63],a[148:151]
mm a[228:231],v[172:175],v[52:55],a[228:231]
mm a[228:231],v[168:171],v[48:51],a[228:231]
rt v[200:201],v241 offset:33792
rt v[202:203],v241 offset:34816
mm a[140:143],v[168:171],v[40:43],a[140:143]
mm a[140:143],v[172:175],v[44:47],a[140:143]
mm a[124:127],v[172:175],v[36:39],a[124:127]
mm a[124:127],v[168:171],v[32:35],a[124:127]
rd v[168:171],v236 offset:59392
rd v[172:175],v237 offset:59392
mm a[216:219],v[176:179],v[32:35],a[216:219]
mm a[216:219],v[180:183],v[36:39],a[216:219]
mm a[168:171],v[180:183],v[44:47],a[168:171]
mm a[168:171],v[176:179],v[40:43],a[168:171]
rt v[204:205],v241 offset:50688
rt v[206:207],v241 offset:51712
mm a[192:195],v[176:179],v[48:51],a[192:195]
mm a[192:195],v[180:183],v[52:55],a[192:195]
mm a[176:179],v[180:183],v[60:63],a[176:179]
mm a[176:179],v[176:179],v[56:59],a[176:179]
rd v[176:179],v236 offset:49152
rd v[180:183],v237 offset:49152
mm a[180:183],v[184:187],v[56:59],a[180:183]
mm a[180:183],v[188:191],v[60:63],a[180:183]
mm a[196:199],v[188:191],v[52:55],a[196:199]
mm a[196:199],v[184:187],v[48:51],a[196:199]
rt v[208:209],v242 offset:33792
rt v[210:211],v242 offset:34816
mm a[172:175],v[184:187],v[40:43],a[172:175]
s_add_u32 m0, s83, 0
mm a[172:175],v[188:191],v[44:47],a[172:175]
ld v232,s[44:47],s80 offen lds
mm a[220:223],v[188:191],v[36:39],a[220:223]
mm a[220:223],v[184:187],v[32:35],a[220:223]
rd v[184:187],v236 offset:51200
rd v[188:191],v237 offset:51200
mm a[72:75],v[0:3],v[32:35],a[72:75]
s_add_u32 m0, s83, 1024
mm a[72:75],v[4:7],v[36:39],a[72:75]
ld v233,s[44:47],s80 offen lds
mm a[8:11],v[4:7],v[44:47],a[8:11]
mm a[8:11],v[0:3],v[40:43],a[8:11]
rt v[212:213],v242 offset:50688
rt v[214:215],v242 offset:51712
mm a[80:83],v[0:3],v[48:51],a[80:83]
s_add_u32 m0, s83, 2176
mm a[80:83],v[4:7],v[52:55],a[80:83]
ld v234,s[44:47],s80 offen lds
mm a[16:19],v[4:7],v[60:63],a[16:19]
mm a[16:19],v[0:3],v[56:59],a[16:19]
rd v[0:3],v236 offset:40960
rd v[4:7],v237 offset:40960
mm a[20:23],v[8:11],v[56:59],a[20:23]
s_add_u32 m0, s83, 3200
mm a[20:23],v[12:15],v[60:63],a[20:23]
ld v235,s[44:47],s80 offen lds
mm a[84:87],v[12:15],v[52:55],a[84:87]
s_add_u32 s80, s80, 524288
mm a[84:87],v[8:11],v[48:51],a[84:87]
rt v[216:217],v243 offset:33792
rt v[218:219],v243 offset:34816
mm a[12:15],v[8:11],v[40:43],a[12:15]
s_add_u32 m0, s83, 16896
mm a[12:15],v[12:15],v[44:47],a[12:15]
ld v232,s[44:47],s80 offen lds
mm a[76:79],v[12:15],v[36:39],a[76:79]
mm a[76:79],v[8:11],v[32:35],a[76:79]
rd v[8:11],v236 offset:43008
rd v[12:15],v237 offset:43008
mm a[248:251],v[16:19],v[32:35],a[248:251]
s_add_u32 m0, s83, 17920
mm a[248:251],v[20:23],v[36:39],a[248:251]
ld v233,s[44:47],s80 offen lds
mm a[40:43],v[20:23],v[44:47],a[40:43]
mm a[40:43],v[16:19],v[40:43],a[40:43]
rt v[220:221],v243 offset:50688
rt v[222:223],v243 offset:51712
mm a[48:51],v[16:19],v[48:51],a[48:51]
s_add_u32 m0, s83, 19072
mm a[48:51],v[20:23],v[52:55],a[48:51]
ld v234,s[44:47],s80 offen lds
mm a[56:59],v[20:23],v[60:63],a[56:59]
mm a[56:59],v[16:19],v[56:59],a[56:59]
rd v[16:19],v236 offset:32768
rd v[20:23],v237 offset:32768
mm a[60:63],v[24:27],v[56:59],a[60:63]
s_add_u32 m0, s83, 20096
mm a[60:63],v[28:31],v[60:63],a[60:63]
ld v235,s[44:47],s80 offen lds
mm a[52:55],v[28:31],v[52:55],a[52:55]
s_add_u32 s80, s80, 524288
mm a[52:55],v[24:27],v[48:51],a[52:55]
mm a[44:47],v[24:27],v[40:43],a[44:47]
mm a[44:47],v[28:31],v[44:47],a[44:47]
mm a[252:255],v[28:31],v[36:39],a[252:255]
wt lgkmcnt(2)
mm a[252:255],v[24:27],v[32:35],a[252:255]
rd v[24:27],v236 offset:34816
rd v[28:31],v237 offset:34816
mm a[96:99],v[160:163],v[192:195],a[96:99]
mm a[96:99],v[164:167],v[196:199],a[96:99]
mm a[104:107],v[164:167],v[204:207],a[104:107]
mm a[104:107],v[160:163],v[200:203],a[104:107]
rt v[32:33],v240 offset:33920
rt v[34:35],v240 offset:34944
mm a[112:115],v[160:163],v[208:211],a[112:115]
mm a[112:115],v[164:167],v[212:215],a[112:115]
mm a[128:131],v[164:167],v[220:223],a[128:131]
wt lgkmcnt(2)
s_barrier
mm a[128:131],v[160:163],v[216:219],a[128:131]
mm a[132:135],v[168:171],v[216:219],a[132:135]
wt vmcnt(8)
v_cmp_eq_u64 vcc, v[72:73], v[252:253]
mm a[132:135],v[172:175],v[220:223],a[132:135]
v_cmp_eq_u64 s[96:97], v[74:75], v[252:253]
mm a[116:119],v[172:175],v[212:215],a[116:119]
s_and_b64 vcc, vcc, s[96:97]
s_cmp_eq_u64 vcc, -1
s_cbranch_scc0 .La5_LV_XSPIN
.La5_LV_XOK:
mm a[116:119],v[168:171],v[208:211],a[116:119]
rt v[36:37],v240 offset:50816
rt v[38:39],v240 offset:51840
mm a[108:111],v[168:171],v[200:203],a[108:111]
s_mov_b32 s89, 49152
mm a[108:111],v[172:175],v[204:207],a[108:111]
mm a[100:103],v[172:175],v[196:199],a[100:103]
mm a[100:103],v[168:171],v[192:195],a[100:103]
mm a[232:235],v[176:179],v[192:195],a[232:235]
mm a[232:235],v[180:183],v[196:199],a[232:235]
ld v[128:131],v244,s[52:55],s91 offen offset:0 sc1
mm a[152:155],v[180:183],v[204:207],a[152:155]
ar v224,a96
ar v225,a97
mm a[152:155],v[176:179],v[200:203],a[152:155]
rt v[40:41],v241 offset:33920
rt v[42:43],v241 offset:34944
mm a[208:211],v[176:179],v[208:211],a[208:211]
ar v226,a98
ar v227,a99
mm a[208:211],v[180:183],v[212:215],a[208:211]
ld v[132:135],v245,s[52:55],s91 offen offset:0 sc1
mm a[160:163],v[180:183],v[220:223],a[160:163]
ar v228,a100
ar v229,a101
mm a[160:163],v[176:179],v[216:219],a[160:163]
ar v230,a102
ar v231,a103
mm a[164:167],v[184:187],v[216:219],a[164:167]
cv v248,v224,v225
cv v249,v226,v227
mm a[164:167],v[188:191],v[220:223],a[164:167]
ld v[136:139],v246,s[52:55],s91 offen offset:0 sc1
mm a[212:215],v[188:191],v[212:215],a[212:215]
cv v250,v228,v229
cv v251,v230,v231
mm a[212:215],v[184:187],v[208:211],a[212:215]
rt v[44:45],v241 offset:50816
rt v[46:47],v241 offset:51840
mm a[156:159],v[184:187],v[200:203],a[156:159]
st v[248:251],v255,s[48:51],s89 offen sc1
mm a[156:159],v[188:191],v[204:207],a[156:159]
ld v[140:143],v247,s[52:55],s91 offen offset:0 sc1
mm a[236:239],v[188:191],v[196:199],a[236:239]
s_mov_b32 s89, 50176
ds_write_b128 v238, v[248:251] offset:0
mm a[236:239],v[184:187],v[192:195],a[236:239]
ar v224,a104
ar v225,a105
mm a[200:203],v[0:3],v[192:195],a[200:203]
ar v226,a106
ar v227,a107
mm a[200:203],v[4:7],v[196:199],a[200:203]
ld v[144:147],v244,s[52:55],s91 offen offset:1024 sc1
mm a[184:187],v[4:7],v[204:207],a[184:187]
ar v228,a108
ar v229,a109
mm a[184:187],v[0:3],v[200:203],a[184:187]
rt v[48:49],v242 offset:33920
rt v[50:51],v242 offset:34944
mm a[64:67],v[0:3],v[208:211],a[64:67]
ar v230,a110
ar v231,a111
mm a[64:67],v[4:7],v[212:215],a[64:67]
ld v[148:151],v245,s[52:55],s91 offen offset:1024 sc1
mm a[0:3],v[4:7],v[220:223],a[0:3]
cv v248,v224,v225
cv v249,v226,v227
mm a[0:3],v[0:3],v[216:219],a[0:3]
cv v250,v228,v229
cv v251,v230,v231
mm a[4:7],v[8:11],v[216:219],a[4:7]
st v[248:251],v255,s[48:51],s89 offen sc1
mm a[4:7],v[12:15],v[220:223],a[4:7]
ld v[152:155],v246,s[52:55],s91 offen offset:1024 sc1
mm a[68:71],v[12:15],v[212:215],a[68:71]
s_mov_b32 s89, 51200
ds_write_b128 v238, v[248:251] offset:1024
mm a[68:71],v[8:11],v[208:211],a[68:71]
rt v[52:53],v242 offset:50816
rt v[54:55],v242 offset:51840
mm a[188:191],v[8:11],v[200:203],a[188:191]
ar v224,a112
ar v225,a113
mm a[188:191],v[12:15],v[204:207],a[188:191]
ld v[156:159],v247,s[52:55],s91 offen offset:1024 sc1
mm a[204:207],v[12:15],v[196:199],a[204:207]
ar v226,a114
ar v227,a115
mm a[204:207],v[8:11],v[192:195],a[204:207]
rt v[56:57],v243 offset:33920
rt v[58:59],v243 offset:34944
mm a[88:91],v[16:19],v[192:195],a[88:91]
ar v228,a116
ar v229,a117
mm a[88:91],v[20:23],v[196:199],a[88:91]
ar v230,a118
ar v231,a119
mm a[24:27],v[20:23],v[204:207],a[24:27]
cv v248,v224,v225
cv v249,v226,v227
mm a[24:27],v[16:19],v[200:203],a[24:27]
rt v[60:61],v243 offset:50816
rt v[62:63],v243 offset:51840
mm a[240:243],v[16:19],v[208:211],a[240:243]
v_subrev_u32 v240, 8448, v240
v_subrev_u32 v241, 8448, v241
mm a[240:243],v[20:23],v[212:215],a[240:243]
v_subrev_u32 v242, 8448, v242
v_subrev_u32 v243, 8448, v243
mm a[32:35],v[20:23],v[220:223],a[32:35]
.La5_LW1_8:
s_cbranch_execz .La5_LW1_12
cv v250,v228,v229
cv v251,v230,v231
mm a[32:35],v[16:19],v[216:219],a[32:35]
st v[248:251],v255,s[48:51],s89 offen sc1
mm a[36:39],v[24:27],v[216:219],a[36:39]
s_mov_b32 s89, 52224
ds_write_b128 v238, v[248:251] offset:2048
mm a[36:39],v[28:31],v[220:223],a[36:39]
mm a[244:247],v[28:31],v[212:215],a[244:247]
mm a[244:247],v[24:27],v[208:211],a[244:247]
mm a[28:31],v[24:27],v[200:203],a[28:31]
mm a[28:31],v[28:31],v[204:207],a[28:31]
mm a[92:95],v[28:31],v[196:199],a[92:95]
wt vmcnt(11) lgkmcnt(1)
s_barrier
mm a[92:95],v[24:27],v[192:195],a[92:95]
mm a[120:123],v[160:163],v[32:35],a[120:123]
s_add_u32 m0, s83, 33792
mm a[120:123],v[164:167],v[36:39],a[120:123]
ld v232,s[44:47],s80 offen lds
mm a[136:139],v[164:167],v[44:47],a[136:139]
mm a[136:139],v[160:163],v[40:43],a[136:139]
rt v[192:193],v240 offset:0
rt v[194:195],v240 offset:1024
mm a[224:227],v[160:163],v[48:51],a[224:227]
s_add_u32 m0, s83, 34816
mm a[224:227],v[164:167],v[52:55],a[224:227]
ld v233,s[44:47],s80 offen lds
mm a[144:147],v[164:167],v[60:63],a[144:147]
mm a[144:147],v[160:163],v[56:59],a[144:147]
rt v[196:197],v240 offset:8448
rt v[198:199],v240 offset:9472
mm a[148:151],v[168:171],v[56:59],a[148:151]
s_add_u32 m0, s83, 35968
mm a[148:151],v[172:175],v[60:63],a[148:151]
ld v234,s[44:47],s80 offen lds
mm a[228:231],v[172:175],v[52:55],a[228:231]
mm a[228:231],v[168:171],v[48:51],a[228:231]
rt v[200:201],v241 offset:0
rt v[202:203],v241 offset:1024
mm a[140:143],v[168:171],v[40:43],a[140:143]
s_add_u32 m0, s83, 36992
mm a[140:143],v[172:175],v[44:47],a[140:143]
ld v235,s[44:47],s80 offen lds
mm a[124:127],v[172:175],v[36:39],a[124:127]
s_add_u32 s80, s80, 524288
mm a[124:127],v[168:171],v[32:35],a[124:127]
rt v[204:205],v241 offset:8448
rt v[206:207],v241 offset:9472
mm a[216:219],v[176:179],v[32:35],a[216:219]
s_add_u32 m0, s83, 50688
mm a[216:219],v[180:183],v[36:39],a[216:219]
ld v232,s[44:47],s80 offen lds
mm a[168:171],v[180:183],v[44:47],a[168:171]
mm a[168:171],v[176:179],v[40:43],a[168:171]
rt v[208:209],v242 offset:0
rt v[210:211],v242 offset:1024
mm a[192:195],v[176:179],v[48:51],a[192:195]
s_add_u32 m0, s83, 51712
mm a[192:195],v[180:183],v[52:55],a[192:195]
ld v233,s[44:47],s80 offen lds
mm a[176:179],v[180:183],v[60:63],a[176:179]
mm a[176:179],v[176:179],v[56:59],a[176:179]
rt v[212:213],v242 offset:8448
rt v[214:215],v242 offset:9472
mm a[180:183],v[184:187],v[56:59],a[180:183]
s_add_u32 m0, s83, 52864
mm a[180:183],v[188:191],v[60:63],a[180:183]
ld v234,s[44:47],s80 offen lds
mm a[196:199],v[188:191],v[52:55],a[196:199]
mm a[196:199],v[184:187],v[48:51],a[196:199]
rt v[216:217],v243 offset:0
rt v[218:219],v243 offset:1024
mm a[172:175],v[184:187],v[40:43],a[172:175]
s_add_u32 m0, s83, 53888
mm a[172:175],v[188:191],v[44:47],a[172:175]
ld v235,s[44:47],s80 offen lds
mm a[220:223],v[188:191],v[36:39],a[220:223]
s_add_u32 s80, s80, 524288
mm a[220:223],v[184:187],v[32:35],a[220:223]
rt v[220:221],v243 offset:8448
rt v[222:223],v243 offset:9472
mm a[72:75],v[0:3],v[32:35],a[72:75]
mm a[72:75],v[4:7],v[36:39],a[72:75]
ld v[160:163],v244,s[52:55],s91 offen offset:2048 sc1
mm a[8:11],v[4:7],v[44:47],a[8:11]
mm a[8:11],v[0:3],v[40:43],a[8:11]
mm a[80:83],v[0:3],v[48:51],a[80:83]
mm a[80:83],v[4:7],v[52:55],a[80:83]
ld v[164:167],v245,s[52:55],s91 offen offset:2048 sc1
mm a[16:19],v[4:7],v[60:63],a[16:19]
mm a[16:19],v[0:3],v[56:59],a[16:19]
mm a[20:23],v[8:11],v[56:59],a[20:23]
mm a[20:23],v[12:15],v[60:63],a[20:23]
ld v[168:171],v246,s[52:55],s91 offen offset:2048 sc1
mm a[84:87],v[12:15],v[52:55],a[84:87]
mm a[84:87],v[8:11],v[48:51],a[84:87]
mm a[12:15],v[8:11],v[40:43],a[12:15]
mm a[12:15],v[12:15],v[44:47],a[12:15]
ld v[172:175],v247,s[52:55],s91 offen offset:2048 sc1
mm a[76:79],v[12:15],v[36:39],a[76:79]
mm a[76:79],v[8:11],v[32:35],a[76:79]
mm a[248:251],v[16:19],v[32:35],a[248:251]
mm a[248:251],v[20:23],v[36:39],a[248:251]
mm a[40:43],v[20:23],v[44:47],a[40:43]
mm a[40:43],v[16:19],v[40:43],a[40:43]
mm a[48:51],v[16:19],v[48:51],a[48:51]
mm a[48:51],v[20:23],v[52:55],a[48:51]
mm a[56:59],v[20:23],v[60:63],a[56:59]
mm a[56:59],v[16:19],v[56:59],a[56:59]
mm a[60:63],v[24:27],v[56:59],a[60:63]
mm a[60:63],v[28:31],v[60:63],a[60:63]
mm a[52:55],v[28:31],v[52:55],a[52:55]
mm a[52:55],v[24:27],v[48:51],a[52:55]
mm a[44:47],v[24:27],v[40:43],a[44:47]
mm a[44:47],v[28:31],v[44:47],a[44:47]
mm a[252:255],v[28:31],v[36:39],a[252:255]
mm a[252:255],v[24:27],v[32:35],a[252:255]
; ---- Q tile: 256 rows x 128 columns, K = 1024, while K / V leave the accumulators (memory + .La5_LDS ring); behind it the first step on the diagonal
.La5_LQ_START:
.La5_LW1_9:
s_cbranch_execz .La5_LW1_13
wt vmcnt(13) lgkmcnt(4)
mm v[0:3],v[192:195],v[128:131],0
ar v224,a128
ar v225,a129
s_mov_b32 s85, s95
mm v[0:3],v[196:199],v[144:147],v[0:3]
ld v[176:179],v244,s[52:55],s91 offen offset:3072 sc1
mm v[32:35],v[196:199],v[148:151],0
ar v226,a130
ar v227,a131
mm v[32:35],v[192:195],v[132:135],v[32:35]
ar v228,a132
ar v229,a133
mm v[64:67],v[192:195],v[136:139],0
ar v230,a134
ar v231,a135
mm v[64:67],v[196:199],v[152:155],v[64:67]
ld v[180:183],v245,s[52:55],s91 offen offset:3072 sc1
mm v[96:99],v[196:199],v[156:159],0
cv v248,v224,v225
cv v249,v226,v227
mm v[96:99],v[192:195],v[140:143],v[96:99]
rt v[196:197],v240 offset:128
rt v[198:199],v240 offset:1152
mm v[100:103],v[200:203],v[140:143],0
cv v250,v228,v229
cv v251,v230,v231
mm v[100:103],v[204:207],v[156:159],v[100:103]
ld v[184:187],v246,s[52:55],s91 offen offset:3072 sc1
mm v[68:71],v[204:207],v[152:155],0
aw a128,v248
aw a129,v249
mm v[68:71],v[200:203],v[136:139],v[68:71]
rt v[192:193],v240 offset:8576
rt v[194:195],v240 offset:9600
mm v[36:39],v[200:203],v[132:135],0
aw a130,v250
aw a131,v251
mm v[36:39],v[204:207],v[148:151],v[36:39]
ld v[188:191],v247,s[52:55],s91 offen offset:3072 sc1
mm v[4:7],v[204:207],v[144:147],0
wt lgkmcnt(4)
s_add_u32 s91, s91, 4096
mm v[4:7],v[200:203],v[128:131],v[4:7]
rt v[204:205],v241 offset:128
rt v[206:207],v241 offset:1152
mm v[8:11],v[208:211],v[128:131],0
s_and_b32 s97, s2, 7
s_mul_i32 s95, s97, 7
mm v[8:11],v[212:215],v[144:147],v[8:11]
ld a[96:99],v244,s[52:55],s91 offen offset:0 sc1
mm v[40:43],v[212:215],v[148:151],0
s_lshr_b32 s96, s30, 7
s_add_u32 s95, s95, s96
mm v[40:43],v[208:211],v[132:135],v[40:43]
rt v[200:201],v241 offset:8576
rt v[202:203],v241 offset:9600
mm v[72:75],v[208:211],v[136:139],0
s_sub_u32 s95, 3, s95
s_and_b32 s95, s95, 7
mm v[72:75],v[212:215],v[152:155],v[72:75]
ld a[100:103],v245,s[52:55],s91 offen offset:0 sc1
mm v[104:107],v[212:215],v[156:159],0
s_min_u32 s95, s95, 6
s_lshl_b32 s95, s95, 1
mm v[104:107],v[208:211],v[140:143],v[104:107]
rt v[212:213],v242 offset:128
rt v[214:215],v242 offset:1152
mm v[108:111],v[216:219],v[140:143],0
s_lshr_b32 s92, s2, 6
s_mul_i32 s93, s92, 3
mm v[108:111],v[220:223],v[156:159],v[108:111]
ld a[104:107],v246,s[52:55],s91 offen offset:0 sc1
mm v[76:79],v[220:223],v[152:155],0
s_add_u32 s93, s93, s74
mm v[76:79],v[216:219],v[136:139],v[76:79]
rt v[208:209],v242 offset:8576
rt v[210:211],v242 offset:9600
mm v[44:47],v[216:219],v[132:135],0
ds_write_b128 v238, v[248:251] offset:3072
mm v[44:47],v[220:223],v[148:151],v[44:47]
st v[248:251],v255,s[48:51],s89 offen offset:0 sc1
mm v[12:15],v[220:223],v[144:147],0
wt lgkmcnt(5)
mm v[12:15],v[216:219],v[128:131],v[12:15]
rt v[220:221],v243 offset:128
rt v[222:223],v243 offset:1152
mm v[16:19],v[196:199],v[128:131],0
ar v224,a120
ar v225,a121
mm v[16:19],v[192:195],v[144:147],v[16:19]
ld a[108:111],v247,s[52:55],s91 offen offset:0 sc1
mm v[48:51],v[192:195],v[148:151],0
ar v226,a122
ar v227,a123
mm v[48:51],v[196:199],v[132:135],v[48:51]
rt v[216:217],v243 offset:8576
rt v[218:219],v243 offset:9600
mm v[80:83],v[196:199],v[136:139],0
ar v228,a124
ar v229,a125
mm v[80:83],v[192:195],v[152:155],v[80:83]
ld a[112:115],v244,s[52:55],s91 offen offset:1024 sc1
mm v[112:115],v[192:195],v[156:159],0
ar v230,a126
ar v231,a127
mm v[112:115],v[196:199],v[140:143],v[112:115]
cv v248,v224,v225
cv v249,v226,v227
mm v[116:119],v[204:207],v[140:143],0
wt lgkmcnt(0)
s_barrier
mm v[116:119],v[200:203],v[156:159],v[116:119]
ld a[116:119],v245,s[52:55],s91 offen offset:1024 sc1
mm v[84:87],v[200:203],v[152:155],0
rt v[192:193],v240 offset:16896
rt v[194:195],v240 offset:17920
mm v[84:87],v[204:207],v[136:139],v[84:87]
rt v[196:197],v240 offset:25344
rt v[198:199],v240 offset:26368
mm v[52:55],v[204:207],v[132:135],0
s_add_u32 m0, s83, 0
cv v250,v228,v229
mm v[52:55],v[200:203],v[148:151],v[52:55]
ld v232,s[44:47],s80 offen lds
mm v[20:23],v[200:203],v[144:147],0
cv v251,v230,v231
aw a132,v248
mm v[20:23],v[204:207],v[128:131],v[20:23]
rt v[200:201],v241 offset:16896
rt v[202:203],v241 offset:17920
mm v[24:27],v[212:215],v[128:131],0
aw a133,v249
aw a134,v250
mm v[24:27],v[208:211],v[144:147],v[24:27]
aw a135,v251
mm v[56:59],v[208:211],v[148:151],0
s_cmp_ge_u32 s93, s95
s_cselect_b32 s96, 2, 0
mm v[56:59],v[212:215],v[132:135],v[56:59]
rt v[204:205],v241 offset:25344
rt v[206:207],v241 offset:26368
mm v[88:91],v[212:215],v[136:139],0
s_add_u32 m0, s83, 1024
s_add_u32 s93, s93, s96
mm v[88:91],v[208:211],v[152:155],v[88:91]
ld v233,s[44:47],s80 offen lds
mm v[120:123],v[208:211],v[156:159],0
s_add_u32 s95, s95, s92
mm v[120:123],v[212:215],v[140:143],v[120:123]
rt v[208:209],v242 offset:16896
rt v[210:211],v242 offset:17920
mm v[124:127],v[220:223],v[140:143],0
s_cmp_lt_u32 s92, 2
s_cselect_b32 s94, s75, 0
mm v[124:127],v[216:219],v[156:159],v[124:127]
ld v[140:143],v246,s[52:55],s91 offen offset:1024 sc1
mm v[92:95],v[216:219],v[152:155],0
s_cmp_eq_u32 s74, 3
s_cselect_b32 s92, s95, s93
s_cselect_b32 s94, s94, s75
mm v[92:95],v[220:223],v[136:139],v[92:95]
rt v[212:213],v242 offset:25344
rt v[214:215],v242 offset:26368
mm v[60:63],v[220:223],v[132:135],0
ds_write_b128 v238, v[248:251] offset:4096
mm v[60:63],v[216:219],v[148:151],v[60:63]
s_mul_i32 s93, s97, 14
s_add_u32 s92, s92, s93
mm v[28:31],v[216:219],v[144:147],0
wt vmcnt(10) lgkmcnt(5)
s_mul_i32 s96, s92, s94
mm v[28:31],v[220:223],v[128:131],v[28:31]
rt v[216:217],v243 offset:16896
rt v[218:219],v243 offset:17920
mm v[0:3],v[192:195],v[160:163],v[0:3]
ar v224,a136
ar v225,a137
mm v[0:3],v[196:199],v[176:179],v[0:3]
ld v[128:131],v247,s[52:55],s91 offen offset:1024 sc1
mm v[32:35],v[196:199],v[180:183],v[32:35]
ar v226,a138
ar v227,a139
mm v[32:35],v[192:195],v[164:167],v[32:35]
rt v[220:221],v243 offset:25344
rt v[222:223],v243 offset:26368
mm v[64:67],v[192:195],v[168:171],v[64:67]
ar v228,a140
ar v229,a141
mm v[64:67],v[196:199],v[184:187],v[64:67]
ld v[132:135],v244,s[52:55],s91 offen offset:2048 sc1
mm v[96:99],v[196:199],v[188:191],v[96:99]
ar v230,a142
ar v231,a143
mm v[96:99],v[192:195],v[172:175],v[96:99]
rt v[196:197],v240 offset:17024
rt v[198:199],v240 offset:18048
mm v[100:103],v[200:203],v[172:175],v[100:103]
s_add_u32 m0, s83, 2176
cv v248,v224,v225
mm v[100:103],v[204:207],v[188:191],v[100:103]
ld v234,s[44:47],s80 offen lds
mm v[68:71],v[204:207],v[184:187],v[68:71]
cv v249,v226,v227
cv v250,v228,v229
mm v[68:71],v[200:203],v[168:171],v[68:71]
rt v[192:193],v240 offset:25472
rt v[194:195],v240 offset:26496
mm v[36:39],v[200:203],v[164:167],v[36:39]
cv v251,v230,v231
aw a136,v248
mm v[36:39],v[204:207],v[180:183],v[36:39]
ld v[136:139],v245,s[52:55],s91 offen offset:2048 sc1
mm v[4:7],v[204:207],v[176:179],v[4:7]
wt lgkmcnt(4)
aw a137,v249
mm v[4:7],v[200:203],v[160:163],v[4:7]
rt v[204:205],v241 offset:17024
rt v[206:207],v241 offset:18048
mm v[8:11],v[208:211],v[160:163],v[8:11]
s_add_u32 m0, s83, 3200
aw a138,v250
mm v[8:11],v[212:215],v[176:179],v[8:11]
ld v235,s[44:47],s80 offen lds
mm v[40:43],v[212:215],v[180:183],v[40:43]
s_add_u32 s80, s80, 524288
aw a139,v251
mm v[40:43],v[208:211],v[164:167],v[40:43]
rt v[200:201],v241 offset:25472
rt v[202:203],v241 offset:26496
mm v[72:75],v[208:211],v[168:171],v[72:75]
s_mov_b32 s100, s96
s_and_b32 s93, s2, 7
mm v[72:75],v[212:215],v[184:187],v[72:75]
ld v[144:147],v246,s[52:55],s91 offen offset:2048 sc1
mm v[104:107],v[212:215],v[188:191],v[104:107]
s_lshr_b32 s95, s93, 1
s_xor_b32 s97, s95, s93
mm v[104:107],v[208:211],v[172:175],v[104:107]
rt v[212:213],v242 offset:17024
rt v[214:215],v242 offset:18048
mm v[108:111],v[216:219],v[172:175],v[108:111]
s_lshl_b32 s95, s95, 21
s_and_b32 s97, s97, 1
mm v[108:111],v[220:223],v[188:191],v[108:111]
ld v[148:151],v247,s[52:55],s91 offen offset:2048 sc1
mm v[76:79],v[220:223],v[184:187],v[76:79]
s_mov_b32 s18, 53248
s_lshl_b32 s97, s97, 10
mm v[76:79],v[216:219],v[168:171],v[76:79]
rt v[208:209],v242 offset:25472
rt v[210:211],v242 offset:26496
mm v[44:47],v[216:219],v[164:167],v[44:47]
ds_write_b128 v238, v[248:251] offset:5120
mm v[44:47],v[220:223],v[180:183],v[44:47]
st a[132:135],v255,s[48:51],s18 offen offset:0 sc1
mm v[12:15],v[220:223],v[176:179],v[12:15]
wt lgkmcnt(5)
s_add_u32 s95, s95, s97
mm v[12:15],v[216:219],v[160:163],v[12:15]
rt v[220:221],v243 offset:17024
rt v[222:223],v243 offset:18048
mm v[16:19],v[196:199],v[160:163],v[16:19]
ar v224,a224
ar v225,a225
mm v[16:19],v[192:195],v[176:179],v[16:19]
ld v[152:155],v244,s[52:55],s91 offen offset:3072 sc1
mm v[48:51],v[192:195],v[180:183],v[48:51]
ar v226,a226
ar v227,a227
mm v[48:51],v[196:199],v[164:167],v[48:51]
rt v[216:217],v243 offset:25472
rt v[218:219],v243 offset:26496
mm v[80:83],v[196:199],v[168:171],v[80:83]
ar v228,a228
ar v229,a229
mm v[80:83],v[192:195],v[184:187],v[80:83]
ld v[156:159],v245,s[52:55],s91 offen offset:3072 sc1
mm v[112:115],v[192:195],v[188:191],v[112:115]
ar v230,a230
ar v231,a231
mm v[112:115],v[196:199],v[172:175],v[112:115]
cv v248,v224,v225
cv v249,v226,v227
mm v[116:119],v[204:207],v[172:175],v[116:119]
wt lgkmcnt(0)
s_barrier
mm v[116:119],v[200:203],v[188:191],v[116:119]
ld a[120:123],v246,s[52:55],s91 offen offset:3072 sc1
mm v[84:87],v[200:203],v[184:187],v[84:87]
rt v[192:193],v240 offset:33792
rt v[194:195],v240 offset:34816
mm v[84:87],v[204:207],v[168:171],v[84:87]
rt v[196:197],v240 offset:42240
rt v[198:199],v240 offset:43264
mm v[52:55],v[204:207],v[164:167],v[52:55]
s_add_u32 m0, s83, 16896
cv v250,v228,v229
mm v[52:55],v[200:203],v[180:183],v[52:55]
ld v232,s[44:47],s80 offen lds
mm v[20:23],v[200:203],v[176:179],v[20:23]
cv v251,v230,v231
aw a140,v248
mm v[20:23],v[204:207],v[160:163],v[20:23]
rt v[200:201],v241 offset:33792
rt v[202:203],v241 offset:34816
mm v[24:27],v[212:215],v[160:163],v[24:27]
aw a141,v249
aw a142,v250
mm v[24:27],v[208:211],v[176:179],v[24:27]
ld a[124:127],v247,s[52:55],s91 offen offset:3072 sc1
mm v[56:59],v[208:211],v[180:183],v[56:59]
s_add_u32 s91, s91, 4096
aw a143,v251
mm v[56:59],v[212:215],v[164:167],v[56:59]
rt v[204:205],v241 offset:42240
rt v[206:207],v241 offset:43264
mm v[88:91],v[212:215],v[168:171],v[88:91]
s_add_u32 m0, s83, 17920
s_mul_i32 s97, s93, 896
mm v[88:91],v[208:211],v[184:187],v[88:91]
ld v233,s[44:47],s80 offen lds
mm v[120:123],v[208:211],v[188:191],v[120:123]
s_cmp_eq_u32 s3, 0
s_cselect_b32 s95, s95, s97
s_cselect_b64 s[36:37], s[28:29], s[30:31]
s_cselect_b32 s21, 2, 7
s_cselect_b32 s93, s93, 0
mm v[120:123],v[212:215],v[172:175],v[120:123]
rt v[208:209],v242 offset:33792
rt v[210:211],v242 offset:34816
mm v[124:127],v[220:223],v[172:175],v[124:127]
s_cmp_eq_u32 s3, 2
s_cselect_b64 s[36:37], s[32:33], s[36:37]
mm v[124:127],v[216:219],v[188:191],v[124:127]
s_lshl_b32 s97, s97, 10
s_mov_b32 s92, 672
mm v[92:95],v[216:219],v[184:187],v[92:95]
s_mov_b32 s90, 352
mm v[92:95],v[220:223],v[168:171],v[92:95]
rt v[212:213],v242 offset:42240
rt v[214:215],v242 offset:43264
mm v[60:63],v[220:223],v[164:167],v[60:63]
ds_write_b128 v238, v[248:251] offset:6144
mm v[60:63],v[216:219],v[180:183],v[60:63]
st a[136:139],v255,s[48:51],s18 offen offset:1024 sc1
mm v[28:31],v[216:219],v[176:179],v[28:31]
wt vmcnt(14) lgkmcnt(5)
mm v[28:31],v[220:223],v[160:163],v[28:31]
rt v[216:217],v243 offset:33792
rt v[218:219],v243 offset:34816
mm v[0:3],v[192:195],a[96:99],v[0:3]
ar v224,a144
ar v225,a145
mm v[0:3],v[196:199],a[112:115],v[0:3]
ld v[160:163],v244,s[52:55],s91 offen offset:0 sc1
mm v[32:35],v[196:199],a[116:119],v[32:35]
ar v226,a146
ar v227,a147
mm v[32:35],v[192:195],a[100:103],v[32:35]
rt v[220:221],v243 offset:42240
rt v[222:223],v243 offset:43264
mm v[64:67],v[192:195],a[104:107],v[64:67]
ar v228,a148
ar v229,a149
mm v[64:67],v[196:199],v[140:143],v[64:67]
ld v[164:167],v245,s[52:55],s91 offen offset:0 sc1
mm v[96:99],v[196:199],v[128:131],v[96:99]
ar v230,a150
ar v231,a151
mm v[96:99],v[192:195],a[108:111],v[96:99]
rt v[196:197],v240 offset:33920
rt v[198:199],v240 offset:34944
mm v[100:103],v[200:203],a[108:111],v[100:103]
s_add_u32 m0, s83, 19072
cv v248,v224,v225
mm v[100:103],v[204:207],v[128:131],v[100:103]
ld v234,s[44:47],s80 offen lds
mm v[68:71],v[204:207],v[140:143],v[68:71]
cv v249,v226,v227
cv v250,v228,v229
mm v[68:71],v[200:203],a[104:107],v[68:71]
rt v[192:193],v240 offset:42368
rt v[194:195],v240 offset:43392
mm v[36:39],v[200:203],a[100:103],v[36:39]
cv v251,v230,v231
aw a144,v248
mm v[36:39],v[204:207],a[116:119],v[36:39]
ld v[168:171],v246,s[52:55],s91 offen offset:0 sc1
mm v[4:7],v[204:207],a[112:115],v[4:7]
wt lgkmcnt(4)
aw a145,v249
mm v[4:7],v[200:203],a[96:99],v[4:7]
rt v[204:205],v241 offset:33920
rt v[206:207],v241 offset:34944
mm v[8:11],v[208:211],a[96:99],v[8:11]
s_add_u32 m0, s83, 20096
aw a146,v250
mm v[8:11],v[212:215],a[112:115],v[8:11]
ld v235,s[44:47],s80 offen lds
mm v[40:43],v[212:215],a[116:119],v[40:43]
s_add_u32 s80, s80, 524288
aw a147,v251
mm v[40:43],v[208:211],a[100:103],v[40:43]
rt v[200:201],v241 offset:42368
rt v[202:203],v241 offset:43392
mm v[72:75],v[208:211],a[104:107],v[72:75]
s_cmp_eq_u32 s3, 3
s_cselect_b32 s95, s97, s95
s_cselect_b64 s[36:37], s[34:35], s[36:37]
s_cselect_b32 s21, 1, s21
s_cselect_b32 s92, 544, s92
mm v[72:75],v[212:215],v[140:143],v[72:75]
ld v[172:175],v247,s[52:55],s91 offen offset:0 sc1
mm v[104:107],v[212:215],v[128:131],v[104:107]
s_lshl_b32 s21, s21, 10
s_and_b32 s7, s93, 1
mm v[104:107],v[208:211],a[108:111],v[104:107]
rt v[212:213],v242 offset:33920
rt v[214:215],v242 offset:34944
mm v[108:111],v[216:219],a[108:111],v[108:111]
s_cmp_eq_u32 s75, 1
s_cselect_b32 s97, s90, s92
s_cselect_b32 s92, s92, s90
mm v[108:111],v[220:223],v[128:131],v[108:111]
.La5_LW1_10:
s_cbranch_execz .La5_LW1_14
s_cmp_eq_u32 s75, s7
s_cselect_b32 s92, s92, 0
mm v[76:79],v[220:223],v[140:143],v[76:79]
s_mul_i32 s92, s92, s21
s_add_u32 s95, s95, s92
mm v[76:79],v[216:219],a[104:107],v[76:79]
rt v[208:209],v242 offset:42368
rt v[210:211],v242 offset:43392
mm v[44:47],v[216:219],a[100:103],v[44:47]
ds_write_b128 v238, v[248:251] offset:7168
mm v[44:47],v[220:223],a[116:119],v[44:47]
st a[140:143],v255,s[48:51],s18 offen offset:2048 sc1
mm v[12:15],v[220:223],a[112:115],v[12:15]
wt lgkmcnt(5)
s_mul_i32 s97, s97, s21
mm v[12:15],v[216:219],a[96:99],v[12:15]
rt v[220:221],v243 offset:33920
rt v[222:223],v243 offset:34944
mm v[16:19],v[196:199],a[96:99],v[16:19]
ar v224,a232
ar v225,a233
mm v[16:19],v[192:195],a[112:115],v[16:19]
ld v[176:179],v244,s[52:55],s91 offen offset:1024 sc1
mm v[48:51],v[192:195],a[116:119],v[48:51]
ar v226,a234
ar v227,a235
mm v[48:51],v[196:199],a[100:103],v[48:51]
rt v[216:217],v243 offset:42368
rt v[218:219],v243 offset:43392
mm v[80:83],v[196:199],a[104:107],v[80:83]
ar v228,a236
ar v229,a237
mm v[80:83],v[192:195],v[140:143],v[80:83]
ld v[180:183],v245,s[52:55],s91 offen offset:1024 sc1
mm v[112:115],v[192:195],v[128:131],v[112:115]
ar v230,a238
ar v231,a239
mm v[112:115],v[196:199],a[108:111],v[112:115]
cv v248,v224,v225
cv v249,v226,v227
mm v[116:119],v[204:207],a[108:111],v[116:119]
wt lgkmcnt(0)
s_barrier
mm v[116:119],v[200:203],v[128:131],v[116:119]
ld v[184:187],v246,s[52:55],s91 offen offset:1024 sc1
mm v[84:87],v[200:203],v[140:143],v[84:87]
rt v[192:193],v240 offset:50688
rt v[194:195],v240 offset:51712
mm v[84:87],v[204:207],a[104:107],v[84:87]
rt v[196:197],v240 offset:59136
rt v[198:199],v240 offset:60160
mm v[52:55],v[204:207],a[100:103],v[52:55]
s_add_u32 m0, s83, 33792
cv v250,v228,v229
mm v[52:55],v[200:203],a[116:119],v[52:55]
ld v232,s[44:47],s80 offen lds
mm v[20:23],v[200:203],a[112:115],v[20:23]
cv v251,v230,v231
aw a148,v248
mm v[20:23],v[204:207],a[96:99],v[20:23]
rt v[200:201],v241 offset:50688
rt v[202:203],v241 offset:51712
mm v[24:27],v[212:215],a[96:99],v[24:27]
aw a149,v249
aw a150,v250
mm v[24:27],v[208:211],a[112:115],v[24:27]
ld v[188:191],v247,s[52:55],s91 offen offset:1024 sc1
mm v[56:59],v[208:211],a[116:119],v[56:59]
aw a151,v251
s_mul_i32 s90, s97, s7
mm v[56:59],v[212:215],a[100:103],v[56:59]
rt v[204:205],v241 offset:59136
rt v[206:207],v241 offset:60160
mm v[88:91],v[212:215],a[104:107],v[88:91]
s_add_u32 m0, s83, 34816
s_sub_u32 s97, s97, s21
mm v[88:91],v[208:211],v[140:143],v[88:91]
ld v233,s[44:47],s80 offen lds
mm v[120:123],v[208:211],v[128:131],v[120:123]
s_add_u32 s97, s97, 896
mm v[120:123],v[212:215],a[108:111],v[120:123]
rt v[208:209],v242 offset:50688
rt v[210:211],v242 offset:51712
mm v[124:127],v[220:223],a[108:111],v[124:127]
s_add_u32 s36, s36, s95
s_addc_u32 s37, s37, 0
mm v[124:127],v[216:219],v[128:131],v[124:127]
s_and_b32 s37, s37, 0xffff
s_mov_b32 s38, s97
mm v[92:95],v[216:219],v[140:143],v[92:95]
s_mov_b32 s39, 0x20000
s_lshl_b32 s93, s96, 6
mm v[92:95],v[220:223],a[104:107],v[92:95]
rt v[212:213],v242 offset:59136
rt v[214:215],v242 offset:60160
mm v[60:63],v[220:223],a[100:103],v[60:63]
ds_write_b128 v238, v[248:251] offset:32768
mm v[60:63],v[216:219],a[116:119],v[60:63]
s_mul_i32 s95, s94, 7332928
mm v[28:31],v[216:219],a[112:115],v[28:31]
wt vmcnt(15) lgkmcnt(5)
mm v[28:31],v[220:223],a[96:99],v[28:31]
rt v[216:217],v243 offset:50688
rt v[218:219],v243 offset:51712
mm v[0:3],v[192:195],v[132:135],v[0:3]
ar v224,a152
ar v225,a153
mm v[0:3],v[196:199],v[152:155],v[0:3]
ld v[128:131],v244,s[52:55],s91 offen offset:2048 sc1
mm v[32:35],v[196:199],v[156:159],v[32:35]
ar v226,a154
ar v227,a155
mm v[32:35],v[192:195],v[136:139],v[32:35]
rt v[220:221],v243 offset:59136
rt v[222:223],v243 offset:60160
mm v[64:67],v[192:195],v[144:147],v[64:67]
ar v228,a156
ar v229,a157
mm v[64:67],v[196:199],a[120:123],v[64:67]
ld v[140:143],v245,s[52:55],s91 offen offset:2048 sc1
mm v[96:99],v[196:199],a[124:127],v[96:99]
ar v230,a158
ar v231,a159
mm v[96:99],v[192:195],v[148:151],v[96:99]
rt v[196:197],v240 offset:50816
rt v[198:199],v240 offset:51840
mm v[100:103],v[200:203],v[148:151],v[100:103]
s_add_u32 m0, s83, 35968
cv v248,v224,v225
mm v[100:103],v[204:207],a[124:127],v[100:103]
ld v234,s[44:47],s80 offen lds
mm v[68:71],v[204:207],a[120:123],v[68:71]
cv v249,v226,v227
cv v250,v228,v229
mm v[68:71],v[200:203],v[144:147],v[68:71]
rt v[192:193],v240 offset:59264
rt v[194:195],v240 offset:60288
mm v[36:39],v[200:203],v[136:139],v[36:39]
cv v251,v230,v231
aw a152,v248
mm v[36:39],v[204:207],v[156:159],v[36:39]
ld a[96:99],v246,s[52:55],s91 offen offset:2048 sc1
mm v[4:7],v[204:207],v[152:155],v[4:7]
wt lgkmcnt(4)
aw a153,v249
mm v[4:7],v[200:203],v[132:135],v[4:7]
rt v[204:205],v241 offset:50816
rt v[206:207],v241 offset:51840
mm v[8:11],v[208:211],v[132:135],v[8:11]
s_add_u32 m0, s83, 36992
aw a154,v250
mm v[8:11],v[212:215],v[152:155],v[8:11]
ld v235,s[44:47],s80 offen lds
mm v[40:43],v[212:215],v[156:159],v[40:43]
s_add_u32 s80, s80, 524288
aw a155,v251
mm v[40:43],v[208:211],v[136:139],v[40:43]
rt v[200:201],v241 offset:59264
rt v[202:203],v241 offset:60288
mm v[72:75],v[208:211],v[144:147],v[72:75]
s_cmp_eq_u32 s77, 0
s_cselect_b64 s[40:41], s[30:31], s[32:33]
mm v[72:75],v[212:215],a[120:123],v[72:75]
ld a[100:103],v247,s[52:55],s91 offen offset:2048 sc1
mm v[104:107],v[212:215],a[124:127],v[104:107]
s_add_u32 s40, s40, s93
s_addc_u32 s41, s41, 0
mm v[104:107],v[208:211],v[148:151],v[104:107]
rt v[212:213],v242 offset:50816
rt v[214:215],v242 offset:51840
mm v[108:111],v[216:219],v[148:151],v[108:111]
s_and_b32 s41, s41, 0xffff
s_mov_b32 s42, s95
mm v[108:111],v[220:223],a[124:127],v[108:111]
s_mov_b32 s43, 0x20000
s_lshr_b32 s92, s72, 1
mm v[76:79],v[220:223],a[120:123],v[76:79]
s_and_b32 s92, s92, 4
s_xor_b32 s92, s92, s72
mm v[76:79],v[216:219],v[144:147],v[76:79]
rt v[208:209],v242 offset:59264
rt v[210:211],v242 offset:60288
mm v[44:47],v[216:219],v[136:139],v[44:47]
ds_write_b128 v238, v[248:251] offset:33792
mm v[44:47],v[220:223],v[156:159],v[44:47]
st a[144:147],v255,s[48:51],s18 offen offset:3072 sc1
mm v[12:15],v[220:223],v[152:155],v[12:15]
wt lgkmcnt(5)
s_xor_b32 s92, s92, 1
mm v[12:15],v[216:219],v[132:135],v[12:15]
rt v[220:221],v243 offset:50816
rt v[222:223],v243 offset:51840
mm v[16:19],v[196:199],v[132:135],v[16:19]
ar v224,a208
ar v225,a209
mm v[16:19],v[192:195],v[152:155],v[16:19]
ld a[104:107],v244,s[52:55],s91 offen offset:3072 sc1
mm v[48:51],v[192:195],v[156:159],v[48:51]
ar v226,a210
ar v227,a211
mm v[48:51],v[196:199],v[136:139],v[48:51]
rt v[216:217],v243 offset:59264
rt v[218:219],v243 offset:60288
mm v[80:83],v[196:199],v[144:147],v[80:83]
ar v228,a212
ar v229,a213
mm v[80:83],v[192:195],a[120:123],v[80:83]
ld a[108:111],v245,s[52:55],s91 offen offset:3072 sc1
mm v[112:115],v[192:195],a[124:127],v[112:115]
ar v230,a214
ar v231,a215
mm v[112:115],v[196:199],v[148:151],v[112:115]
cv v248,v224,v225
cv v249,v226,v227
mm v[116:119],v[204:207],v[148:151],v[116:119]
wt lgkmcnt(0)
s_barrier
mm v[116:119],v[200:203],a[124:127],v[116:119]
ld a[112:115],v246,s[52:55],s91 offen offset:3072 sc1
mm v[84:87],v[200:203],a[120:123],v[84:87]
rt v[192:193],v240 offset:0
rt v[194:195],v240 offset:1024
mm v[84:87],v[204:207],v[144:147],v[84:87]
rt v[196:197],v240 offset:8448
rt v[198:199],v240 offset:9472
mm v[52:55],v[204:207],v[136:139],v[52:55]
s_add_u32 m0, s83, 50688
cv v250,v228,v229
mm v[52:55],v[200:203],v[156:159],v[52:55]
ld v232,s[44:47],s80 offen lds
mm v[20:23],v[200:203],v[152:155],v[20:23]
cv v251,v230,v231
aw a156,v248
mm v[20:23],v[204:207],v[132:135],v[20:23]
rt v[200:201],v241 offset:0
rt v[202:203],v241 offset:1024
mm v[24:27],v[212:215],v[132:135],v[24:27]
aw a157,v249
aw a158,v250
mm v[24:27],v[208:211],v[152:155],v[24:27]
ld a[116:119],v247,s[52:55],s91 offen offset:3072 sc1
mm v[56:59],v[208:211],v[156:159],v[56:59]
s_add_u32 s91, s91, 4096
aw a159,v251
mm v[56:59],v[212:215],v[136:139],v[56:59]
rt v[204:205],v241 offset:8448
rt v[206:207],v241 offset:9472
mm v[88:91],v[212:215],v[144:147],v[88:91]
s_add_u32 m0, s83, 51712
s_and_b32 s101, s92, 7
mm v[88:91],v[208:211],a[120:123],v[88:91]
ld v233,s[44:47],s80 offen lds
mm v[120:123],v[208:211],a[124:127],v[120:123]
s_lshl_b32 s92, s101, 8
s_sub_u32 s93, 8388608, s92
mm v[120:123],v[212:215],v[148:151],v[120:123]
rt v[208:209],v242 offset:0
rt v[210:211],v242 offset:1024
mm v[124:127],v[220:223],v[148:151],v[124:127]
s_add_u32 s56, s28, s92
s_addc_u32 s57, s29, 0
mm v[124:127],v[216:219],a[124:127],v[124:127]
s_and_b32 s57, s57, 0xffff
s_mov_b32 s58, s93
mm v[92:95],v[216:219],a[120:123],v[92:95]
s_mov_b32 s18, 32768
s_mov_b32 s59, 0x20000
mm v[92:95],v[220:223],v[144:147],v[92:95]
rt v[212:213],v242 offset:8448
rt v[214:215],v242 offset:9472
mm v[60:63],v[220:223],v[136:139],v[60:63]
ds_write_b128 v238, v[248:251] offset:34816
mm v[60:63],v[216:219],v[156:159],v[60:63]
st a[148:151],v255,s[48:51],s18 offen offset:0 sc1
mm v[28:31],v[216:219],v[152:155],v[28:31]
wt vmcnt(15) lgkmcnt(5)
s_lshl_b32 s93, s96, 16
mm v[28:31],v[220:223],v[132:135],v[28:31]
rt v[216:217],v243 offset:0
rt v[218:219],v243 offset:1024
mm v[0:3],v[192:195],v[160:163],v[0:3]
ar v224,a160
ar v225,a161
mm v[0:3],v[196:199],v[176:179],v[0:3]
ld v[132:135],v244,s[52:55],s91 offen offset:0 sc1
mm v[32:35],v[196:199],v[180:183],v[32:35]
ar v226,a162
ar v227,a163
mm v[32:35],v[192:195],v[164:167],v[32:35]
rt v[220:221],v243 offset:8448
rt v[222:223],v243 offset:9472
mm v[64:67],v[192:195],v[168:171],v[64:67]
ar v228,a164
ar v229,a165
mm v[64:67],v[196:199],v[184:187],v[64:67]
ld v[136:139],v245,s[52:55],s91 offen offset:0 sc1
mm v[96:99],v[196:199],v[188:191],v[96:99]
ar v230,a166
ar v231,a167
mm v[96:99],v[192:195],v[172:175],v[96:99]
rt v[196:197],v240 offset:128
rt v[198:199],v240 offset:1152
mm v[100:103],v[200:203],v[172:175],v[100:103]
s_add_u32 m0, s83, 52864
cv v248,v224,v225
mm v[100:103],v[204:207],v[188:191],v[100:103]
ld v234,s[44:47],s80 offen lds
mm v[68:71],v[204:207],v[184:187],v[68:71]
cv v249,v226,v227
cv v250,v228,v229
mm v[68:71],v[200:203],v[168:171],v[68:71]
rt v[192:193],v240 offset:8576
rt v[194:195],v240 offset:9600
mm v[36:39],v[200:203],v[164:167],v[36:39]
cv v251,v230,v231
aw a160,v248
mm v[36:39],v[204:207],v[180:183],v[36:39]
ld v[144:147],v246,s[52:55],s91 offen offset:0 sc1
mm v[4:7],v[204:207],v[176:179],v[4:7]
wt lgkmcnt(4)
aw a161,v249
mm v[4:7],v[200:203],v[160:163],v[4:7]
rt v[204:205],v241 offset:128
rt v[206:207],v241 offset:1152
mm v[8:11],v[208:211],v[160:163],v[8:11]
s_add_u32 m0, s83, 53888
aw a162,v250
mm v[8:11],v[212:215],v[176:179],v[8:11]
ld v235,s[44:47],s80 offen lds
mm v[40:43],v[212:215],v[180:183],v[40:43]
s_add_u32 s80, s80, 524288
aw a163,v251
mm v[40:43],v[208:211],v[164:167],v[40:43]
rt v[200:201],v241 offset:8576
rt v[202:203],v241 offset:9600
mm v[72:75],v[208:211],v[168:171],v[72:75]
s_lshl_b32 s95, s94, 16
mm v[72:75],v[212:215],v[184:187],v[72:75]
ld v[148:151],v247,s[52:55],s91 offen offset:0 sc1
mm v[104:107],v[212:215],v[188:191],v[104:107]
s_add_u32 s28, s34, s93
s_addc_u32 s29, s35, 0
mm v[104:107],v[208:211],v[172:175],v[104:107]
rt v[212:213],v242 offset:128
rt v[214:215],v242 offset:1152
mm v[108:111],v[216:219],v[172:175],v[108:111]
s_and_b32 s29, s29, 0xffff
s_mov_b32 s30, s95
mm v[108:111],v[220:223],v[188:191],v[108:111]
s_mov_b32 s31, 0x20000
s_lshl_b32 s93, s101, 12
mm v[76:79],v[220:223],v[184:187],v[76:79]
s_add_u32 s92, s93, 71303168
s_sub_u32 s93, 16777216, s93
mm v[76:79],v[216:219],v[168:171],v[76:79]
rt v[208:209],v242 offset:8576
rt v[210:211],v242 offset:9600
mm v[44:47],v[216:219],v[164:167],v[44:47]
ds_write_b128 v238, v[248:251] offset:35840
mm v[44:47],v[220:223],v[180:183],v[44:47]
st a[152:155],v255,s[48:51],s18 offen offset:1024 sc1
mm v[12:15],v[220:223],v[176:179],v[12:15]
wt lgkmcnt(5)
mm v[12:15],v[216:219],v[160:163],v[12:15]
rt v[220:221],v243 offset:128
rt v[222:223],v243 offset:1152
mm v[16:19],v[196:199],v[160:163],v[16:19]
ar v224,a216
ar v225,a217
mm v[16:19],v[192:195],v[176:179],v[16:19]
ld v[152:155],v244,s[52:55],s91 offen offset:1024 sc1
mm v[48:51],v[192:195],v[180:183],v[48:51]
ar v226,a218
ar v227,a219
mm v[48:51],v[196:199],v[164:167],v[48:51]
rt v[216:217],v243 offset:8576
rt v[218:219],v243 offset:9600
mm v[80:83],v[196:199],v[168:171],v[80:83]
ar v228,a220
ar v229,a221
mm v[80:83],v[192:195],v[184:187],v[80:83]
ld v[156:159],v245,s[52:55],s91 offen offset:1024 sc1
mm v[112:115],v[192:195],v[188:191],v[112:115]
ar v230,a222
ar v231,a223
mm v[112:115],v[196:199],v[172:175],v[112:115]
cv v248,v224,v225
cv v249,v226,v227
mm v[116:119],v[204:207],v[172:175],v[116:119]
wt lgkmcnt(0)
s_barrier
mm v[116:119],v[200:203],v[188:191],v[116:119]
ld a[120:123],v246,s[52:55],s91 offen offset:1024 sc1
mm v[84:87],v[200:203],v[184:187],v[84:87]
rt v[192:193],v240 offset:16896
rt v[194:195],v240 offset:17920
mm v[84:87],v[204:207],v[168:171],v[84:87]
rt v[196:197],v240 offset:25344
rt v[198:199],v240 offset:26368
mm v[52:55],v[204:207],v[164:167],v[52:55]
s_add_u32 m0, s83, 0
cv v250,v228,v229
mm v[52:55],v[200:203],v[180:183],v[52:55]
ld v232,s[44:47],s80 offen lds
mm v[20:23],v[200:203],v[176:179],v[20:23]
cv v251,v230,v231
aw a164,v248
mm v[20:23],v[204:207],v[160:163],v[20:23]
rt v[200:201],v241 offset:16896
rt v[202:203],v241 offset:17920
mm v[24:27],v[212:215],v[160:163],v[24:27]
aw a165,v249
aw a166,v250
mm v[24:27],v[208:211],v[176:179],v[24:27]
ld a[124:127],v247,s[52:55],s91 offen offset:1024 sc1
mm v[56:59],v[208:211],v[180:183],v[56:59]
aw a167,v251
mm v[56:59],v[212:215],v[164:167],v[56:59]
rt v[204:205],v241 offset:25344
rt v[206:207],v241 offset:26368
mm v[88:91],v[212:215],v[168:171],v[88:91]
s_add_u32 m0, s83, 1024
mm v[88:91],v[208:211],v[184:187],v[88:91]
ld v233,s[44:47],s80 offen lds
mm v[120:123],v[208:211],v[188:191],v[120:123]
s_add_u32 s32, s26, s92
s_addc_u32 s33, s27, 0
mm v[120:123],v[212:215],v[172:175],v[120:123]
rt v[208:209],v242 offset:16896
rt v[210:211],v242 offset:17920
mm v[124:127],v[220:223],v[172:175],v[124:127]
s_and_b32 s33, s33, 0xffff
s_mov_b32 s34, s93
mm v[124:127],v[216:219],v[188:191],v[124:127]
s_mov_b32 s35, 0x20000
s_lshl2_add_u32 s92, s72, s74
mm v[92:95],v[216:219],v[184:187],v[92:95]
s_lshl2_add_u32 s92, s92, s3
s_lshl_b32 s88, s92, 8
mm v[92:95],v[220:223],v[168:171],v[92:95]
rt v[212:213],v242 offset:25344
rt v[214:215],v242 offset:26368
mm v[60:63],v[220:223],v[164:167],v[60:63]
ds_write_b128 v238, v[248:251] offset:36864
mm v[60:63],v[216:219],v[180:183],v[60:63]
st a[156:159],v255,s[48:51],s18 offen offset:2048 sc1
mm v[28:31],v[216:219],v[176:179],v[28:31]
wt vmcnt(16) lgkmcnt(5)
s_mul_i32 s93, s92, 17408
mm v[28:31],v[220:223],v[160:163],v[28:31]
rt v[216:217],v243 offset:16896
rt v[218:219],v243 offset:17920
mm v[0:3],v[192:195],v[128:131],v[0:3]
ar v224,a168
ar v225,a169
mm v[0:3],v[196:199],a[104:107],v[0:3]
ld v[160:163],v244,s[52:55],s91 offen offset:2048 sc1
mm v[32:35],v[196:199],a[108:111],v[32:35]
ar v226,a170
ar v227,a171
mm v[32:35],v[192:195],v[140:143],v[32:35]
rt v[220:221],v243 offset:25344
rt v[222:223],v243 offset:26368
mm v[64:67],v[192:195],a[96:99],v[64:67]
ar v228,a172
ar v229,a173
mm v[64:67],v[196:199],a[112:115],v[64:67]
ld v[164:167],v245,s[52:55],s91 offen offset:2048 sc1
mm v[96:99],v[196:199],a[116:119],v[96:99]
ar v230,a174
ar v231,a175
mm v[96:99],v[192:195],a[100:103],v[96:99]
rt v[196:197],v240 offset:17024
rt v[198:199],v240 offset:18048
mm v[100:103],v[200:203],a[100:103],v[100:103]
s_add_u32 m0, s83, 2176
cv v248,v224,v225
mm v[100:103],v[204:207],a[116:119],v[100:103]
ld v234,s[44:47],s80 offen lds
mm v[68:71],v[204:207],a[112:115],v[68:71]
cv v249,v226,v227
cv v250,v228,v229
mm v[68:71],v[200:203],a[96:99],v[68:71]
rt v[192:193],v240 offset:25472
rt v[194:195],v240 offset:26496
mm v[36:39],v[200:203],v[140:143],v[36:39]
cv v251,v230,v231
aw a168,v248
mm v[36:39],v[204:207],a[108:111],v[36:39]
ld v[168:171],v246,s[52:55],s91 offen offset:2048 sc1
mm v[4:7],v[204:207],a[104:107],v[4:7]
wt lgkmcnt(4)
aw a169,v249
mm v[4:7],v[200:203],v[128:131],v[4:7]
rt v[204:205],v241 offset:17024
rt v[206:207],v241 offset:18048
mm v[8:11],v[208:211],v[128:131],v[8:11]
s_add_u32 m0, s83, 3200
aw a170,v250
mm v[8:11],v[212:215],a[104:107],v[8:11]
ld v235,s[44:47],s80 offen lds
mm v[40:43],v[212:215],a[108:111],v[40:43]
s_add_u32 s80, s80, 524288
aw a171,v251
mm v[40:43],v[208:211],v[140:143],v[40:43]
rt v[200:201],v241 offset:25472
rt v[202:203],v241 offset:26496
mm v[72:75],v[208:211],a[96:99],v[72:75]
s_lshl_b32 s92, s92, 14
s_add_u32 s92, s92, 4194304
mm v[72:75],v[212:215],a[112:115],v[72:75]
ld v[172:175],v247,s[52:55],s91 offen offset:2048 sc1
mm v[104:107],v[212:215],a[116:119],v[104:107]
s_add_u32 s86, s26, s92
s_addc_u32 s87, s27, 0
mm v[104:107],v[208:211],a[100:103],v[104:107]
rt v[212:213],v242 offset:17024
rt v[214:215],v242 offset:18048
mm v[108:111],v[216:219],a[100:103],v[108:111]
s_and_b32 s87, s87, 0xffff
s_add_u32 s93, s93, 150994944
mm v[108:111],v[220:223],a[116:119],v[108:111]
s_add_u32 s68, s26, s93
s_addc_u32 s69, s27, 0
mm v[76:79],v[220:223],a[112:115],v[76:79]
s_and_b32 s69, s69, 0xffff
s_mov_b32 s70, 17408
mm v[76:79],v[216:219],a[96:99],v[76:79]
rt v[208:209],v242 offset:25472
rt v[210:211],v242 offset:26496
mm v[44:47],v[216:219],v[140:143],v[44:47]
ds_write_b128 v238, v[248:251] offset:37888
mm v[44:47],v[220:223],a[108:111],v[44:47]
st a[160:163],v255,s[48:51],s18 offen offset:3072 sc1
mm v[12:15],v[220:223],a[104:107],v[12:15]
wt lgkmcnt(5)
s_mov_b32 s96, 153158689
mm v[12:15],v[216:219],v[128:131],v[12:15]
rt v[220:221],v243 offset:17024
rt v[222:223],v243 offset:18048
mm v[16:19],v[196:199],v[128:131],v[16:19]
.La5_LW1_11:
s_cbranch_execz .La5_LW1_15
ar v224,a192
ar v225,a193
mm v[16:19],v[192:195],a[104:107],v[16:19]
ld v[176:179],v244,s[52:55],s91 offen offset:3072 sc1
mm v[48:51],v[192:195],a[108:111],v[48:51]
ar v226,a194
ar v227,a195
mm v[48:51],v[196:199],v[140:143],v[48:51]
rt v[216:217],v243 offset:25472
rt v[218:219],v243 offset:26496
mm v[80:83],v[196:199],a[96:99],v[80:83]
ar v228,a196
ar v229,a197
mm v[80:83],v[192:195],a[112:115],v[80:83]
ld v[180:183],v245,s[52:55],s91 offen offset:3072 sc1
mm v[112:115],v[192:195],a[116:119],v[112:115]
ar v230,a198
ar v231,a199
mm v[112:115],v[196:199],a[100:103],v[112:115]
cv v248,v224,v225
cv v249,v226,v227
mm v[116:119],v[204:207],a[100:103],v[116:119]
wt lgkmcnt(0)
s_barrier
mm v[116:119],v[200:203],a[116:119],v[116:119]
ld v[184:187],v246,s[52:55],s91 offen offset:3072 sc1
mm v[84:87],v[200:203],a[112:115],v[84:87]
rt v[192:193],v240 offset:33792
rt v[194:195],v240 offset:34816
mm v[84:87],v[204:207],a[96:99],v[84:87]
rt v[196:197],v240 offset:42240
rt v[198:199],v240 offset:43264
mm v[52:55],v[204:207],v[140:143],v[52:55]
s_add_u32 m0, s83, 16896
cv v250,v228,v229
mm v[52:55],v[200:203],a[108:111],v[52:55]
ld v232,s[44:47],s80 offen lds
mm v[20:23],v[200:203],a[104:107],v[20:23]
cv v251,v230,v231
aw a172,v248
mm v[20:23],v[204:207],v[128:131],v[20:23]
rt v[200:201],v241 offset:33792
rt v[202:203],v241 offset:34816
mm v[24:27],v[212:215],v[128:131],v[24:27]
aw a173,v249
aw a174,v250
mm v[24:27],v[208:211],a[104:107],v[24:27]
ld v[188:191],v247,s[52:55],s91 offen offset:3072 sc1
mm v[56:59],v[208:211],a[108:111],v[56:59]
s_add_u32 s91, s91, 4096
aw a175,v251
mm v[56:59],v[212:215],v[140:143],v[56:59]
rt v[204:205],v241 offset:42240
rt v[206:207],v241 offset:43264
mm v[88:91],v[212:215],a[96:99],v[88:91]
s_add_u32 m0, s83, 17920
s_mov_b32 s97, 371265825
mm v[88:91],v[208:211],a[112:115],v[88:91]
ld v233,s[44:47],s80 offen lds
mm v[120:123],v[208:211],a[116:119],v[120:123]
s_lshl_b32 s95, s73, 3
s_lshr_b64 s[96:97], s[96:97], s95
mm v[120:123],v[212:215],a[100:103],v[120:123]
rt v[208:209],v242 offset:33792
rt v[210:211],v242 offset:34816
mm v[124:127],v[220:223],a[100:103],v[124:127]
s_and_b32 s82, s96, 255
s_lshl_b64 s[98:99], 1, s82
mm v[124:127],v[216:219],a[116:119],v[124:127]
s_lshl_b32 s96, s75, 22
s_or_b32 s95, s98, s96
mm v[92:95],v[216:219],a[112:115],v[92:95]
s_cmp_eq_u32 s73, 1
s_cselect_b32 s96, 12, 4
s_cselect_b32 s79, 0, 0xff000000
mm v[92:95],v[220:223],a[96:99],v[92:95]
rt v[212:213],v242 offset:42240
rt v[214:215],v242 offset:43264
mm v[60:63],v[220:223],v[140:143],v[60:63]
ds_write_b128 v238, v[248:251] offset:38912
mm v[60:63],v[216:219],a[108:111],v[60:63]
s_or_b32 s98, s95, s96
mm v[28:31],v[216:219],a[104:107],v[28:31]
wt vmcnt(15) lgkmcnt(5)
mm v[28:31],v[220:223],v[128:131],v[28:31]
rt v[216:217],v243 offset:33792
rt v[218:219],v243 offset:34816
mm v[0:3],v[192:195],v[132:135],v[0:3]
ar v224,a176
ar v225,a177
mm v[0:3],v[196:199],v[152:155],v[0:3]
ld v[128:131],v244,s[52:55],s91 offen offset:0 sc1
mm v[32:35],v[196:199],v[156:159],v[32:35]
ar v226,a178
ar v227,a179
mm v[32:35],v[192:195],v[136:139],v[32:35]
rt v[220:221],v243 offset:42240
rt v[222:223],v243 offset:43264
mm v[64:67],v[192:195],v[144:147],v[64:67]
ar v228,a180
ar v229,a181
mm v[64:67],v[196:199],a[120:123],v[64:67]
ld v[140:143],v245,s[52:55],s91 offen offset:0 sc1
mm v[96:99],v[196:199],a[124:127],v[96:99]
ar v230,a182
ar v231,a183
mm v[96:99],v[192:195],v[148:151],v[96:99]
rt v[196:197],v240 offset:33920
rt v[198:199],v240 offset:34944
mm v[100:103],v[200:203],v[148:151],v[100:103]
s_add_u32 m0, s83, 19072
cv v248,v224,v225
mm v[100:103],v[204:207],a[124:127],v[100:103]
ld v234,s[44:47],s80 offen lds
mm v[68:71],v[204:207],a[120:123],v[68:71]
cv v249,v226,v227
cv v250,v228,v229
mm v[68:71],v[200:203],v[144:147],v[68:71]
rt v[192:193],v240 offset:42368
rt v[194:195],v240 offset:43392
mm v[36:39],v[200:203],v[136:139],v[36:39]
cv v251,v230,v231
aw a176,v248
mm v[36:39],v[204:207],v[156:159],v[36:39]
ld a[96:99],v246,s[52:55],s91 offen offset:0 sc1
mm v[4:7],v[204:207],v[152:155],v[4:7]
wt lgkmcnt(4)
aw a177,v249
mm v[4:7],v[200:203],v[132:135],v[4:7]
rt v[204:205],v241 offset:33920
rt v[206:207],v241 offset:34944
mm v[8:11],v[208:211],v[132:135],v[8:11]
s_add_u32 m0, s83, 20096
aw a178,v250
mm v[8:11],v[212:215],v[152:155],v[8:11]
ld v235,s[44:47],s80 offen lds
mm v[40:43],v[212:215],v[156:159],v[40:43]
s_add_u32 s80, s80, 524288
aw a179,v251
mm v[40:43],v[208:211],v[136:139],v[40:43]
rt v[200:201],v241 offset:42368
rt v[202:203],v241 offset:43392
mm v[72:75],v[208:211],v[144:147],v[72:75]
s_cmp_eq_u32 s74, 3
s_cselect_b32 s23, 3, 0
s_cselect_b32 s22, 0, s75
mm v[72:75],v[212:215],a[120:123],v[72:75]
ld a[100:103],v247,s[52:55],s91 offen offset:0 sc1
mm v[104:107],v[212:215],a[124:127],v[104:107]
s_lshl_b32 s95, s76, 5
s_add_u32 s95, s95, s3
mm v[104:107],v[208:211],v[148:151],v[104:107]
rt v[212:213],v242 offset:33920
rt v[214:215],v242 offset:34944
mm v[108:111],v[216:219],v[148:151],v[108:111]
s_lshl_b32 s95, s95, 5
s_add_u32 s95, s95, s72
mm v[108:111],v[220:223],a[124:127],v[108:111]
s_lshl_b32 s95, s95, 3
s_add_u32 s6, s95, 1048576
mm v[76:79],v[220:223],a[120:123],v[76:79]
s_mov_b32 s18, 36864
v_and_b32 v239, 0x70, v255
mm v[76:79],v[216:219],v[144:147],v[76:79]
rt v[208:209],v242 offset:42368
rt v[210:211],v242 offset:43392
mm v[44:47],v[216:219],v[136:139],v[44:47]
ds_write_b128 v238, v[248:251] offset:39936
mm v[44:47],v[220:223],v[156:159],v[44:47]
st a[164:167],v255,s[48:51],s18 offen offset:0 sc1
mm v[12:15],v[220:223],v[152:155],v[12:15]
wt lgkmcnt(5)
v_lshlrev_b32 v239, 6, v239
mm v[12:15],v[216:219],v[132:135],v[12:15]
rt v[220:221],v243 offset:33920
rt v[222:223],v243 offset:34944
mm v[16:19],v[196:199],v[132:135],v[16:19]
ar v224,a200
ar v225,a201
mm v[16:19],v[192:195],v[152:155],v[16:19]
ld a[104:107],v244,s[52:55],s91 offen offset:1024 sc1
mm v[48:51],v[192:195],v[156:159],v[48:51]
ar v226,a202
ar v227,a203
mm v[48:51],v[196:199],v[136:139],v[48:51]
rt v[216:217],v243 offset:42368
rt v[218:219],v243 offset:43392
mm v[80:83],v[196:199],v[144:147],v[80:83]
ar v228,a204
ar v229,a205
mm v[80:83],v[192:195],a[120:123],v[80:83]
ld a[108:111],v245,s[52:55],s91 offen offset:1024 sc1
mm v[112:115],v[192:195],a[124:127],v[112:115]
ar v230,a206
ar v231,a207
mm v[112:115],v[196:199],v[148:151],v[112:115]
cv v248,v224,v225
cv v249,v226,v227
mm v[116:119],v[204:207],v[148:151],v[116:119]
wt lgkmcnt(0)
s_barrier
mm v[116:119],v[200:203],a[124:127],v[116:119]
ld a[112:115],v246,s[52:55],s91 offen offset:1024 sc1
mm v[84:87],v[200:203],a[120:123],v[84:87]
rt v[192:193],v240 offset:50688
rt v[194:195],v240 offset:51712
mm v[84:87],v[204:207],v[144:147],v[84:87]
rt v[196:197],v240 offset:59136
rt v[198:199],v240 offset:60160
mm v[52:55],v[204:207],v[136:139],v[52:55]
s_add_u32 m0, s83, 33792
cv v250,v228,v229
mm v[52:55],v[200:203],v[156:159],v[52:55]
ld v232,s[44:47],s80 offen lds
mm v[20:23],v[200:203],v[152:155],v[20:23]
cv v251,v230,v231
aw a180,v248
mm v[20:23],v[204:207],v[132:135],v[20:23]
rt v[200:201],v241 offset:50688
rt v[202:203],v241 offset:51712
mm v[24:27],v[212:215],v[132:135],v[24:27]
aw a181,v249
aw a182,v250
mm v[24:27],v[208:211],v[152:155],v[24:27]
ld a[116:119],v247,s[52:55],s91 offen offset:1024 sc1
mm v[56:59],v[208:211],v[156:159],v[56:59]
aw a183,v251
s_add_u32 s85, s85, 65536
mm v[56:59],v[212:215],v[136:139],v[56:59]
rt v[204:205],v241 offset:59136
rt v[206:207],v241 offset:60160
mm v[88:91],v[212:215],v[144:147],v[88:91]
s_add_u32 m0, s83, 34816
v_add_u32 v238, s85, v255
mm v[88:91],v[208:211],a[120:123],v[88:91]
ld v233,s[44:47],s80 offen lds
mm v[120:123],v[208:211],a[124:127],v[120:123]
s_mov_b32 s85, 0
aw a231,v239
mm v[120:123],v[212:215],v[148:151],v[120:123]
rt v[208:209],v242 offset:50688
rt v[210:211],v242 offset:51712
mm v[124:127],v[220:223],v[148:151],v[124:127]
s_mul_i32 s95, s75, 0
s_add_u32 s95, s95, 2
mm v[124:127],v[216:219],a[124:127],v[124:127]
s_sub_u32 s96, 4, s95
s_lshr_b32 s97, s2, 4
mm v[92:95],v[216:219],a[120:123],v[92:95]
s_mul_i32 s97, s97, s95
v_lshrrev_b32 v239, 7, v255
mm v[92:95],v[220:223],v[144:147],v[92:95]
rt v[212:213],v242 offset:59136
rt v[214:215],v242 offset:60160
mm v[60:63],v[220:223],v[136:139],v[60:63]
ds_write_b128 v238, v[248:251] offset:0
mm v[60:63],v[216:219],v[156:159],v[60:63]
st a[168:171],v255,s[48:51],s18 offen offset:1024 sc1
mm v[28:31],v[216:219],v[152:155],v[28:31]
wt vmcnt(15) lgkmcnt(5)
v_add_u32 v254, s97, v239
mm v[28:31],v[220:223],v[132:135],v[28:31]
rt v[216:217],v243 offset:50688
rt v[218:219],v243 offset:51712
mm v[0:3],v[192:195],v[160:163],v[0:3]
ar v224,a184
ar v225,a185
mm v[0:3],v[196:199],v[176:179],v[0:3]
ld v[132:135],v244,s[52:55],s91 offen offset:2048 sc1
mm v[32:35],v[196:199],v[180:183],v[32:35]
ar v226,a186
ar v227,a187
mm v[32:35],v[192:195],v[164:167],v[32:35]
rt v[220:221],v243 offset:59136
rt v[222:223],v243 offset:60160
mm v[64:67],v[192:195],v[168:171],v[64:67]
ar v228,a188
ar v229,a189
mm v[64:67],v[196:199],v[184:187],v[64:67]
ld v[136:139],v245,s[52:55],s91 offen offset:2048 sc1
mm v[96:99],v[196:199],v[188:191],v[96:99]
ar v230,a190
ar v231,a191
mm v[96:99],v[192:195],v[172:175],v[96:99]
rt v[196:197],v240 offset:50816
rt v[198:199],v240 offset:51840
mm v[100:103],v[200:203],v[172:175],v[100:103]
s_add_u32 m0, s83, 35968
cv v248,v224,v225
mm v[100:103],v[204:207],v[188:191],v[100:103]
ld v234,s[44:47],s80 offen lds
mm v[68:71],v[204:207],v[184:187],v[68:71]
cv v249,v226,v227
cv v250,v228,v229
mm v[68:71],v[200:203],v[168:171],v[68:71]
rt v[192:193],v240 offset:59264
rt v[194:195],v240 offset:60288
mm v[36:39],v[200:203],v[164:167],v[36:39]
cv v251,v230,v231
aw a184,v248
mm v[36:39],v[204:207],v[180:183],v[36:39]
ld v[144:147],v246,s[52:55],s91 offen offset:2048 sc1
mm v[4:7],v[204:207],v[176:179],v[4:7]
wt lgkmcnt(4)
aw a185,v249
mm v[4:7],v[200:203],v[160:163],v[4:7]
rt v[204:205],v241 offset:50816
rt v[206:207],v241 offset:51840
mm v[8:11],v[208:211],v[160:163],v[8:11]
s_add_u32 m0, s83, 36992
aw a186,v250
mm v[8:11],v[212:215],v[176:179],v[8:11]
ld v235,s[44:47],s80 offen lds
mm v[40:43],v[212:215],v[180:183],v[40:43]
s_add_u32 s80, s80, 524288
aw a187,v251
mm v[40:43],v[208:211],v[164:167],v[40:43]
rt v[200:201],v241 offset:59264
rt v[202:203],v241 offset:60288
mm v[72:75],v[208:211],v[168:171],v[72:75]
v_mul_u32_u24 v254, s21, v254
v_add_u32 v239, s96, v239
mm v[72:75],v[212:215],v[184:187],v[72:75]
ld v[148:151],v247,s[52:55],s91 offen offset:2048 sc1
mm v[104:107],v[212:215],v[188:191],v[104:107]
v_lshrrev_b32 v239, 2, v239
v_lshl_add_u32 v254, v239, 28, v254
mm v[104:107],v[208:211],v[172:175],v[104:107]
rt v[212:213],v242 offset:50816
rt v[214:215],v242 offset:51840
mm v[108:111],v[216:219],v[172:175],v[108:111]
v_and_b32 v239, 0x70, v255
v_lshl_add_u32 v254, v239, 3, v254
mm v[108:111],v[220:223],v[188:191],v[108:111]
s_lshl_b32 s96, s7, 5
s_sub_u32 s96, 16, s96
mm v[76:79],v[220:223],v[184:187],v[76:79]
s_mul_i32 s95, s95, s96
s_mul_i32 s21, s21, s95
mm v[76:79],v[216:219],v[168:171],v[76:79]
rt v[208:209],v242 offset:59264
rt v[210:211],v242 offset:60288
mm v[44:47],v[216:219],v[164:167],v[44:47]
ds_write_b128 v238, v[248:251] offset:1024
mm v[44:47],v[220:223],v[180:183],v[44:47]
st a[172:175],v255,s[48:51],s18 offen offset:2048 sc1
mm v[12:15],v[220:223],v[176:179],v[12:15]
wt lgkmcnt(5)
v_lshrrev_b32 v236, 8, v255
mm v[12:15],v[216:219],v[160:163],v[12:15]
rt v[220:221],v243 offset:50816
rt v[222:223],v243 offset:51840
mm v[16:19],v[196:199],v[160:163],v[16:19]
ar v224,a64
ar v225,a65
mm v[16:19],v[192:195],v[176:179],v[16:19]
ld v[152:155],v244,s[52:55],s91 offen offset:3072 sc1
mm v[48:51],v[192:195],v[180:183],v[48:51]
ar v226,a66
ar v227,a67
mm v[48:51],v[196:199],v[164:167],v[48:51]
rt v[216:217],v243 offset:59264
rt v[218:219],v243 offset:60288
mm v[80:83],v[196:199],v[168:171],v[80:83]
ar v228,a68
ar v229,a69
mm v[80:83],v[192:195],v[184:187],v[80:83]
ld v[156:159],v245,s[52:55],s91 offen offset:3072 sc1
mm v[112:115],v[192:195],v[188:191],v[112:115]
ar v230,a70
ar v231,a71
mm v[112:115],v[196:199],v[172:175],v[112:115]
cv v248,v224,v225
cv v249,v226,v227
mm v[116:119],v[204:207],v[172:175],v[116:119]
wt lgkmcnt(0)
s_barrier
mm v[116:119],v[200:203],v[188:191],v[116:119]
ld a[120:123],v246,s[52:55],s91 offen offset:3072 sc1
mm v[84:87],v[200:203],v[184:187],v[84:87]
rt v[192:193],v240 offset:0
rt v[194:195],v240 offset:1024
mm v[84:87],v[204:207],v[168:171],v[84:87]
rt v[196:197],v240 offset:8448
rt v[198:199],v240 offset:9472
mm v[52:55],v[204:207],v[164:167],v[52:55]
s_add_u32 m0, s83, 50688
cv v250,v228,v229
mm v[52:55],v[200:203],v[180:183],v[52:55]
ld v232,s[44:47],s80 offen lds
mm v[20:23],v[200:203],v[176:179],v[20:23]
cv v251,v230,v231
aw a188,v248
mm v[20:23],v[204:207],v[160:163],v[20:23]
rt v[200:201],v241 offset:0
rt v[202:203],v241 offset:1024
mm v[24:27],v[212:215],v[160:163],v[24:27]
aw a189,v249
aw a190,v250
mm v[24:27],v[208:211],v[176:179],v[24:27]
ld a[124:127],v247,s[52:55],s91 offen offset:3072 sc1
mm v[56:59],v[208:211],v[180:183],v[56:59]
s_add_u32 s91, s91, 4096
aw a191,v251
mm v[56:59],v[212:215],v[164:167],v[56:59]
rt v[204:205],v241 offset:8448
rt v[206:207],v241 offset:9472
mm v[88:91],v[212:215],v[168:171],v[88:91]
s_add_u32 m0, s83, 51712
v_lshlrev_b32 v239, 5, v236
mm v[88:91],v[208:211],v[184:187],v[88:91]
ld v233,s[44:47],s80 offen lds
mm v[120:123],v[208:211],v[188:191],v[120:123]
v_xor_b32 v239, v239, v255
v_and_b32 v239, 0xf0, v239
mm v[120:123],v[212:215],v[172:175],v[120:123]
rt v[208:209],v242 offset:0
rt v[210:211],v242 offset:1024
mm v[124:127],v[220:223],v[172:175],v[124:127]
v_lshl_add_u32 v239, v236, 11, v239
s_lshl_b32 s92, s77, 16
mm v[124:127],v[216:219],v[188:191],v[124:127]
s_lshl_b32 s93, s78, 14
s_add_u32 s92, s92, s93
mm v[92:95],v[216:219],v[184:187],v[92:95]
v_add_u32 v239, s92, v239
aw a228,v239
mm v[92:95],v[220:223],v[168:171],v[92:95]
rt v[212:213],v242 offset:8448
rt v[214:215],v242 offset:9472
mm v[60:63],v[220:223],v[164:167],v[60:63]
ds_write_b128 v238, v[248:251] offset:2048
mm v[60:63],v[216:219],v[180:183],v[60:63]
st a[176:179],v255,s[48:51],s18 offen offset:3072 sc1
mm v[28:31],v[216:219],v[176:179],v[28:31]
wt vmcnt(16) lgkmcnt(5)
v_bfe_u32 v239, v255, 6, 2
mm v[28:31],v[220:223],v[160:163],v[28:31]
rt v[216:217],v243 offset:0
rt v[218:219],v243 offset:1024
mm v[0:3],v[192:195],v[128:131],v[0:3]
ar v224,a0
ar v225,a1
mm v[0:3],v[196:199],a[104:107],v[0:3]
ld v[160:163],v244,s[52:55],s91 offen offset:0 sc1
mm v[32:35],v[196:199],a[108:111],v[32:35]
ar v226,a2
ar v227,a3
mm v[32:35],v[192:195],v[140:143],v[32:35]
rt v[220:221],v243 offset:8448
rt v[222:223],v243 offset:9472
mm v[64:67],v[192:195],a[96:99],v[64:67]
ar v228,a4
ar v229,a5
mm v[64:67],v[196:199],a[112:115],v[64:67]
ld v[164:167],v245,s[52:55],s91 offen offset:0 sc1
mm v[96:99],v[196:199],a[116:119],v[96:99]
ar v230,a6
ar v231,a7
mm v[96:99],v[192:195],a[100:103],v[96:99]
rt v[196:197],v240 offset:128
rt v[198:199],v240 offset:1152
mm v[100:103],v[200:203],a[100:103],v[100:103]
s_add_u32 m0, s83, 52864
cv v248,v224,v225
mm v[100:103],v[204:207],a[116:119],v[100:103]
ld v234,s[44:47],s80 offen lds
mm v[68:71],v[204:207],a[112:115],v[68:71]
cv v249,v226,v227
cv v250,v228,v229
mm v[68:71],v[200:203],a[96:99],v[68:71]
rt v[192:193],v240 offset:8576
rt v[194:195],v240 offset:9600
mm v[36:39],v[200:203],v[140:143],v[36:39]
cv v251,v230,v231
aw a0,v248
mm v[36:39],v[204:207],a[108:111],v[36:39]
ld v[168:171],v246,s[52:55],s91 offen offset:0 sc1
mm v[4:7],v[204:207],a[104:107],v[4:7]
wt lgkmcnt(4)
aw a1,v249
mm v[4:7],v[200:203],v[128:131],v[4:7]
rt v[204:205],v241 offset:128
rt v[206:207],v241 offset:1152
mm v[8:11],v[208:211],v[128:131],v[8:11]
s_add_u32 m0, s83, 53888
aw a2,v250
mm v[8:11],v[212:215],a[104:107],v[8:11]
ld v235,s[44:47],s80 offen lds
mm v[40:43],v[212:215],a[108:111],v[40:43]
s_add_u32 s80, s80, 524288
aw a3,v251
mm v[40:43],v[208:211],v[140:143],v[40:43]
rt v[200:201],v241 offset:8576
rt v[202:203],v241 offset:9600
mm v[72:75],v[208:211],a[96:99],v[72:75]
v_lshlrev_b32 v239, 6, v239
v_lshl_add_u32 v239, v236, 4, v239
mm v[72:75],v[212:215],a[112:115],v[72:75]
ld v[172:175],v247,s[52:55],s91 offen offset:0 sc1
mm v[104:107],v[212:215],a[116:119],v[104:107]
s_lshl_b32 s92, s78, 8
v_add_u32 v239, s92, v239
mm v[104:107],v[208:211],a[100:103],v[104:107]
rt v[212:213],v242 offset:128
rt v[214:215],v242 offset:1152
mm v[108:111],v[216:219],a[100:103],v[108:111]
v_mul_u32_u24 v239, 7168, v239
v_and_or_b32 v239, v255, 48, v239
mm v[108:111],v[220:223],a[116:119],v[108:111]
aw a229,v239
v_and_b32 v239, 0xf0, v255
mm v[76:79],v[220:223],a[112:115],v[76:79]
s_mov_b32 s18, 16384
v_lshl_add_u32 v239, v236, 14, v239
mm v[76:79],v[216:219],a[96:99],v[76:79]
rt v[208:209],v242 offset:8576
rt v[210:211],v242 offset:9600
mm v[44:47],v[216:219],v[140:143],v[44:47]
ds_write_b128 v238, v[248:251] offset:3072
mm v[44:47],v[220:223],a[108:111],v[44:47]
st a[180:183],v255,s[48:51],s18 offen offset:0 sc1
mm v[12:15],v[220:223],a[104:107],v[12:15]
wt lgkmcnt(5)
s_lshl_b32 s92, s3, 9
mm v[12:15],v[216:219],v[128:131],v[12:15]
rt v[220:221],v243 offset:128
rt v[222:223],v243 offset:1152
mm v[16:19],v[196:199],v[128:131],v[16:19]
ar v224,a72
ar v225,a73
mm v[16:19],v[192:195],a[104:107],v[16:19]
ld v[176:179],v244,s[52:55],s91 offen offset:1024 sc1
mm v[48:51],v[192:195],a[108:111],v[48:51]
ar v226,a74
ar v227,a75
mm v[48:51],v[196:199],v[140:143],v[48:51]
rt v[216:217],v243 offset:8576
rt v[218:219],v243 offset:9600
mm v[80:83],v[196:199],a[96:99],v[80:83]
ar v228,a76
.La5_LW1_12:
s_cbranch_execz .La5_LW1_R0
ar v229,a77
mm v[80:83],v[192:195],a[112:115],v[80:83]
ld v[180:183],v245,s[52:55],s91 offen offset:1024 sc1
mm v[112:115],v[192:195],a[116:119],v[112:115]
ar v230,a78
ar v231,a79
mm v[112:115],v[196:199],a[100:103],v[112:115]
cv v248,v224,v225
cv v249,v226,v227
mm v[116:119],v[204:207],a[100:103],v[116:119]
wt lgkmcnt(0)
s_barrier
mm v[116:119],v[200:203],a[116:119],v[116:119]
ld v[184:187],v246,s[52:55],s91 offen offset:1024 sc1
mm v[84:87],v[200:203],a[112:115],v[84:87]
rt v[192:193],v240 offset:16896
rt v[194:195],v240 offset:17920
mm v[84:87],v[204:207],a[96:99],v[84:87]
rt v[196:197],v240 offset:25344
rt v[198:199],v240 offset:26368
mm v[52:55],v[204:207],v[140:143],v[52:55]
s_add_u32 m0, s83, 0
cv v250,v228,v229
mm v[52:55],v[200:203],a[108:111],v[52:55]
ld v232,s[44:47],s80 offen lds
mm v[20:23],v[200:203],a[104:107],v[20:23]
cv v251,v230,v231
aw a4,v248
mm v[20:23],v[204:207],v[128:131],v[20:23]
rt v[200:201],v241 offset:16896
rt v[202:203],v241 offset:17920
mm v[24:27],v[212:215],v[128:131],v[24:27]
aw a5,v249
aw a6,v250
mm v[24:27],v[208:211],a[104:107],v[24:27]
ld v[188:191],v247,s[52:55],s91 offen offset:1024 sc1
mm v[56:59],v[208:211],a[108:111],v[56:59]
aw a7,v251
v_add_u32 v239, s92, v239
mm v[56:59],v[212:215],v[140:143],v[56:59]
rt v[204:205],v241 offset:25344
rt v[206:207],v241 offset:26368
mm v[88:91],v[212:215],a[96:99],v[88:91]
s_add_u32 m0, s83, 1024
aw a230,v239
mm v[88:91],v[208:211],a[112:115],v[88:91]
ld v233,s[44:47],s80 offen lds
mm v[120:123],v[208:211],a[116:119],v[120:123]
v_add_u32 v239, 4294886912, v240
aw a224,v239
mm v[120:123],v[212:215],a[100:103],v[120:123]
rt v[208:209],v242 offset:16896
rt v[210:211],v242 offset:17920
mm v[124:127],v[220:223],a[100:103],v[124:127]
v_bfe_u32 v239, v255, 4, 4
v_lshlrev_b32 v236, 2, v236
mm v[124:127],v[216:219],a[116:119],v[124:127]
v_sub_u32 v239, v239, v236
v_subrev_u32 v236, 0, v239
mm v[92:95],v[216:219],a[112:115],v[92:95]
v_ashrrev_i32 v236, 31, v236
v_and_b32 v236, 0xff800000, v236
mm v[92:95],v[220:223],a[96:99],v[92:95]
rt v[212:213],v242 offset:25344
rt v[214:215],v242 offset:26368
mm v[60:63],v[220:223],v[140:143],v[60:63]
ds_write_b128 v238, v[248:251] offset:4096
mm v[60:63],v[216:219],a[108:111],v[60:63]
aw a236,v236
v_subrev_u32 v236, 1, v239
mm v[28:31],v[216:219],a[104:107],v[28:31]
wt vmcnt(15) lgkmcnt(5)
v_ashrrev_i32 v236, 31, v236
mm v[28:31],v[220:223],v[128:131],v[28:31]
rt v[216:217],v243 offset:16896
rt v[218:219],v243 offset:17920
mm v[0:3],v[192:195],v[132:135],v[0:3]
ar v224,a8
ar v225,a9
mm v[0:3],v[196:199],v[152:155],v[0:3]
ld v[128:131],v244,s[52:55],s91 offen offset:2048 sc1
mm v[32:35],v[196:199],v[156:159],v[32:35]
ar v226,a10
ar v227,a11
mm v[32:35],v[192:195],v[136:139],v[32:35]
rt v[220:221],v243 offset:25344
rt v[222:223],v243 offset:26368
mm v[64:67],v[192:195],v[144:147],v[64:67]
ar v228,a12
ar v229,a13
mm v[64:67],v[196:199],a[120:123],v[64:67]
ld v[140:143],v245,s[52:55],s91 offen offset:2048 sc1
mm v[96:99],v[196:199],a[124:127],v[96:99]
ar v230,a14
ar v231,a15
mm v[96:99],v[192:195],v[148:151],v[96:99]
rt v[196:197],v240 offset:17024
rt v[198:199],v240 offset:18048
mm v[100:103],v[200:203],v[148:151],v[100:103]
s_add_u32 m0, s83, 2176
cv v248,v224,v225
mm v[100:103],v[204:207],a[124:127],v[100:103]
ld v234,s[44:47],s80 offen lds
mm v[68:71],v[204:207],a[120:123],v[68:71]
cv v249,v226,v227
cv v250,v228,v229
mm v[68:71],v[200:203],v[144:147],v[68:71]
rt v[192:193],v240 offset:25472
rt v[194:195],v240 offset:26496
mm v[36:39],v[200:203],v[136:139],v[36:39]
cv v251,v230,v231
aw a8,v248
mm v[36:39],v[204:207],v[156:159],v[36:39]
ld a[96:99],v246,s[52:55],s91 offen offset:2048 sc1
mm v[4:7],v[204:207],v[152:155],v[4:7]
wt lgkmcnt(4)
aw a9,v249
mm v[4:7],v[200:203],v[132:135],v[4:7]
rt v[204:205],v241 offset:17024
rt v[206:207],v241 offset:18048
mm v[8:11],v[208:211],v[132:135],v[8:11]
s_add_u32 m0, s83, 3200
aw a10,v250
mm v[8:11],v[212:215],v[152:155],v[8:11]
ld v235,s[44:47],s80 offen lds
mm v[40:43],v[212:215],v[156:159],v[40:43]
s_add_u32 s80, s80, 524288
aw a11,v251
mm v[40:43],v[208:211],v[136:139],v[40:43]
rt v[200:201],v241 offset:25472
rt v[202:203],v241 offset:26496
mm v[72:75],v[208:211],v[144:147],v[72:75]
v_and_b32 v236, 0xff800000, v236
aw a237,v236
mm v[72:75],v[212:215],a[120:123],v[72:75]
ld a[100:103],v247,s[52:55],s91 offen offset:2048 sc1
mm v[104:107],v[212:215],a[124:127],v[104:107]
v_subrev_u32 v236, 2, v239
v_ashrrev_i32 v236, 31, v236
mm v[104:107],v[208:211],v[148:151],v[104:107]
rt v[212:213],v242 offset:17024
rt v[214:215],v242 offset:18048
mm v[108:111],v[216:219],v[148:151],v[108:111]
v_and_b32 v236, 0xff800000, v236
aw a238,v236
mm v[108:111],v[220:223],a[124:127],v[108:111]
v_subrev_u32 v236, 3, v239
v_ashrrev_i32 v236, 31, v236
mm v[76:79],v[220:223],a[120:123],v[76:79]
v_and_b32 v236, 0xff800000, v236
aw a239,v236
mm v[76:79],v[216:219],v[144:147],v[76:79]
rt v[208:209],v242 offset:25472
rt v[210:211],v242 offset:26496
mm v[44:47],v[216:219],v[136:139],v[44:47]
ds_write_b128 v238, v[248:251] offset:5120
mm v[44:47],v[220:223],v[156:159],v[44:47]
st a[184:187],v255,s[48:51],s18 offen offset:1024 sc1
mm v[12:15],v[220:223],v[152:155],v[12:15]
wt lgkmcnt(5)
v_lshlrev_b32 v236, 4, v239
mm v[12:15],v[216:219],v[132:135],v[12:15]
rt v[220:221],v243 offset:17024
rt v[222:223],v243 offset:18048
mm v[16:19],v[196:199],v[132:135],v[16:19]
ar v224,a80
ar v225,a81
mm v[16:19],v[192:195],v[152:155],v[16:19]
ld a[104:107],v244,s[52:55],s91 offen offset:3072 sc1
mm v[48:51],v[192:195],v[156:159],v[48:51]
ar v226,a82
ar v227,a83
mm v[48:51],v[196:199],v[136:139],v[48:51]
rt v[216:217],v243 offset:25472
rt v[218:219],v243 offset:26496
mm v[80:83],v[196:199],v[144:147],v[80:83]
ar v228,a84
ar v229,a85
mm v[80:83],v[192:195],a[120:123],v[80:83]
ld a[108:111],v245,s[52:55],s91 offen offset:3072 sc1
mm v[112:115],v[192:195],a[124:127],v[112:115]
ar v230,a86
ar v231,a87
mm v[112:115],v[196:199],v[148:151],v[112:115]
cv v248,v224,v225
cv v249,v226,v227
mm v[116:119],v[204:207],v[148:151],v[116:119]
wt lgkmcnt(0)
s_barrier
mm v[116:119],v[200:203],a[124:127],v[116:119]
ld a[112:115],v246,s[52:55],s91 offen offset:3072 sc1
mm v[84:87],v[200:203],a[120:123],v[84:87]
rt v[192:193],v240 offset:33792
rt v[194:195],v240 offset:34816
mm v[84:87],v[204:207],v[144:147],v[84:87]
rt v[196:197],v240 offset:42240
rt v[198:199],v240 offset:43264
mm v[52:55],v[204:207],v[136:139],v[52:55]
s_add_u32 m0, s83, 16896
cv v250,v228,v229
mm v[52:55],v[200:203],v[156:159],v[52:55]
ld v232,s[44:47],s80 offen lds
mm v[20:23],v[200:203],v[152:155],v[20:23]
cv v251,v230,v231
aw a12,v248
mm v[20:23],v[204:207],v[132:135],v[20:23]
rt v[200:201],v241 offset:33792
rt v[202:203],v241 offset:34816
mm v[24:27],v[212:215],v[132:135],v[24:27]
aw a13,v249
aw a14,v250
mm v[24:27],v[208:211],v[152:155],v[24:27]
ld a[116:119],v247,s[52:55],s91 offen offset:3072 sc1
mm v[56:59],v[208:211],v[156:159],v[56:59]
s_add_u32 s91, s91, 4096
aw a15,v251
mm v[56:59],v[212:215],v[136:139],v[56:59]
rt v[204:205],v241 offset:42240
rt v[206:207],v241 offset:43264
mm v[88:91],v[212:215],v[144:147],v[88:91]
s_add_u32 m0, s83, 17920
s_mov_b32 s92, 0x3f80
mm v[88:91],v[208:211],a[120:123],v[88:91]
ld v233,s[44:47],s80 offen lds
mm v[120:123],v[208:211],a[124:127],v[120:123]
v_lshlrev_b32 v236, v236, s92
v_cmp_gt_u32 vcc, 2, v239
mm v[120:123],v[212:215],v[148:151],v[120:123]
rt v[208:209],v242 offset:33792
rt v[210:211],v242 offset:34816
mm v[124:127],v[220:223],v[148:151],v[124:127]
v_subrev_u32 v239, 2, v239
v_cmp_gt_u32 s[92:93], 2, v239
mm v[124:127],v[216:219],a[124:127],v[124:127]
v_cndmask_b32 v239, 0, v236, vcc
aw a212,0
mm v[92:95],v[216:219],a[120:123],v[92:95]
aw a214,v239
v_cndmask_b32 v239, 0, v236, s[92:93]
mm v[92:95],v[220:223],v[144:147],v[92:95]
rt v[212:213],v242 offset:42240
rt v[214:215],v242 offset:43264
mm v[60:63],v[220:223],v[136:139],v[60:63]
v_mov_b32 v236, 0x3f803f80
aw a215,v239
mm v[60:63],v[216:219],v[156:159],v[60:63]
st a[188:191],v255,s[48:51],s18 offen offset:2048 sc1
mm v[28:31],v[216:219],v[152:155],v[28:31]
wt vmcnt(15) lgkmcnt(4)
aw a213,0
mm v[28:31],v[220:223],v[132:135],v[28:31]
rt v[216:217],v243 offset:33792
rt v[218:219],v243 offset:34816
mm v[0:3],v[192:195],v[160:163],v[0:3]
ar v224,a16
ar v225,a17
mm v[0:3],v[196:199],v[176:179],v[0:3]
ld v[132:135],v244,s[52:55],s91 offen offset:0 sc1
mm v[32:35],v[196:199],v[180:183],v[32:35]
ar v226,a18
ar v227,a19
mm v[32:35],v[192:195],v[164:167],v[32:35]
rt v[220:221],v243 offset:42240
rt v[222:223],v243 offset:43264
mm v[64:67],v[192:195],v[168:171],v[64:67]
ar v228,a20
ar v229,a21
mm v[64:67],v[196:199],v[184:187],v[64:67]
ld v[136:139],v245,s[52:55],s91 offen offset:0 sc1
mm v[96:99],v[196:199],v[188:191],v[96:99]
ar v230,a22
ar v231,a23
mm v[96:99],v[192:195],v[172:175],v[96:99]
rt v[196:197],v240 offset:33920
rt v[198:199],v240 offset:34944
mm v[100:103],v[200:203],v[172:175],v[100:103]
s_add_u32 m0, s83, 19072
cv v248,v224,v225
mm v[100:103],v[204:207],v[188:191],v[100:103]
ld v234,s[44:47],s80 offen lds
mm v[68:71],v[204:207],v[184:187],v[68:71]
cv v249,v226,v227
cv v250,v228,v229
mm v[68:71],v[200:203],v[168:171],v[68:71]
rt v[192:193],v240 offset:42368
rt v[194:195],v240 offset:43392
mm v[36:39],v[200:203],v[164:167],v[36:39]
cv v251,v230,v231
aw a16,v248
mm v[36:39],v[204:207],v[180:183],v[36:39]
ld v[144:147],v246,s[52:55],s91 offen offset:0 sc1
mm v[4:7],v[204:207],v[176:179],v[4:7]
wt lgkmcnt(4)
aw a17,v249
mm v[4:7],v[200:203],v[160:163],v[4:7]
rt v[204:205],v241 offset:33920
rt v[206:207],v241 offset:34944
mm v[8:11],v[208:211],v[160:163],v[8:11]
s_add_u32 m0, s83, 20096
aw a18,v250
mm v[8:11],v[212:215],v[176:179],v[8:11]
ld v235,s[44:47],s80 offen lds
mm v[40:43],v[212:215],v[180:183],v[40:43]
s_add_u32 s80, s80, 524288
aw a19,v251
mm v[40:43],v[208:211],v[164:167],v[40:43]
rt v[200:201],v241 offset:42368
rt v[202:203],v241 offset:43392
mm v[72:75],v[208:211],v[168:171],v[72:75]
aw a216,0
aw a217,0
mm v[72:75],v[212:215],v[184:187],v[72:75]
ld v[148:151],v247,s[52:55],s91 offen offset:0 sc1
mm v[104:107],v[212:215],v[188:191],v[104:107]
v_lshrrev_b32 v239, 4, v255
v_xor_b32 v239, v239, v255
mm v[104:107],v[208:211],v[172:175],v[104:107]
rt v[212:213],v242 offset:33920
rt v[214:215],v242 offset:34944
mm v[108:111],v[216:219],v[172:175],v[108:111]
v_and_b32 v239, 32, v239
v_cmp_eq_u32 vcc, 0, v239
mm v[108:111],v[220:223],v[188:191],v[108:111]
s_mov_b32 s78, 4294950911
s_mul_i32 s70, s70, s22
mm v[76:79],v[220:223],v[184:187],v[76:79]
v_cndmask_b32 v236, 0, v236, vcc
s_sub_u32 s92, 4294967294, s3
mm v[76:79],v[216:219],v[168:171],v[76:79]
rt v[208:209],v242 offset:42368
rt v[210:211],v242 offset:43392
mm v[44:47],v[216:219],v[164:167],v[44:47]
s_add_u32 s92, s92, s7
s_mul_i32 s92, s92, s21
mm v[44:47],v[220:223],v[180:183],v[44:47]
st a[0:3],v255,s[48:51],s18 offen offset:3072 sc1
mm v[12:15],v[220:223],v[176:179],v[12:15]
wt lgkmcnt(4)
s_add_u32 s90, s90, s92
mm v[12:15],v[216:219],v[160:163],v[12:15]
rt v[220:221],v243 offset:33920
rt v[222:223],v243 offset:34944
mm v[16:19],v[196:199],v[160:163],v[16:19]
ar v224,a88
ar v225,a89
mm v[16:19],v[192:195],v[176:179],v[16:19]
ld v[152:155],v244,s[52:55],s91 offen offset:1024 sc1
mm v[48:51],v[192:195],v[180:183],v[48:51]
ar v226,a90
ar v227,a91
mm v[48:51],v[196:199],v[164:167],v[48:51]
rt v[216:217],v243 offset:42368
rt v[218:219],v243 offset:43392
mm v[80:83],v[196:199],v[168:171],v[80:83]
ar v228,a92
ar v229,a93
mm v[80:83],v[192:195],v[184:187],v[80:83]
ld v[156:159],v245,s[52:55],s91 offen offset:1024 sc1
mm v[112:115],v[192:195],v[188:191],v[112:115]
ar v230,a94
ar v231,a95
mm v[112:115],v[196:199],v[172:175],v[112:115]
cv v248,v224,v225
cv v249,v226,v227
mm v[116:119],v[204:207],v[172:175],v[116:119]
wt lgkmcnt(0)
s_barrier
mm v[116:119],v[200:203],v[188:191],v[116:119]
ld a[120:123],v246,s[52:55],s91 offen offset:1024 sc1
mm v[84:87],v[200:203],v[184:187],v[84:87]
rt v[192:193],v240 offset:50688
rt v[194:195],v240 offset:51712
mm v[84:87],v[204:207],v[168:171],v[84:87]
rt v[196:197],v240 offset:59136
rt v[198:199],v240 offset:60160
mm v[52:55],v[204:207],v[164:167],v[52:55]
s_add_u32 m0, s83, 33792
cv v250,v228,v229
mm v[52:55],v[200:203],v[180:183],v[52:55]
ld v232,s[44:47],s80 offen lds
mm v[20:23],v[200:203],v[176:179],v[20:23]
cv v251,v230,v231
aw a20,v248
mm v[20:23],v[204:207],v[160:163],v[20:23]
rt v[200:201],v241 offset:50688
rt v[202:203],v241 offset:51712
mm v[24:27],v[212:215],v[160:163],v[24:27]
aw a21,v249
aw a22,v250
mm v[24:27],v[208:211],v[176:179],v[24:27]
ld a[124:127],v247,s[52:55],s91 offen offset:1024 sc1
mm v[56:59],v[208:211],v[180:183],v[56:59]
aw a23,v251
s_mov_b32 s17, 1073741824
mm v[56:59],v[212:215],v[164:167],v[56:59]
rt v[204:205],v241 offset:59136
rt v[206:207],v241 offset:60160
mm v[88:91],v[212:215],v[168:171],v[88:91]
s_add_u32 m0, s83, 34816
s_lshl_b32 s77, s76, 16
mm v[88:91],v[208:211],v[184:187],v[88:91]
ld v233,s[44:47],s80 offen lds
mm v[120:123],v[208:211],v[188:191],v[120:123]
s_sub_u32 s77, s77, 8192
s_lshl4_add_u32 s81, s75, s74
mm v[120:123],v[212:215],v[172:175],v[120:123]
rt v[208:209],v242 offset:50688
rt v[210:211],v242 offset:51712
mm v[124:127],v[220:223],v[172:175],v[124:127]
s_lshl3_add_u32 s81, s81, 1
mm v[124:127],v[216:219],v[188:191],v[124:127]
s_cmp_eq_u32 s73, 0
s_cselect_b32 s16, s77, 0
mm v[92:95],v[216:219],v[184:187],v[92:95]
s_mov_b32 s18, 20480
s_mul_i32 s92, s75, 4294836224
mm v[92:95],v[220:223],v[168:171],v[92:95]
rt v[212:213],v242 offset:59136
rt v[214:215],v242 offset:60160
mm v[60:63],v[220:223],v[164:167],v[60:63]
s_add_u32 s92, s92, s88
s_add_u32 s19, s92, 393216
mm v[60:63],v[216:219],v[180:183],v[60:63]
st a[4:7],v255,s[48:51],s18 offen offset:0 sc1
mm v[28:31],v[216:219],v[176:179],v[28:31]
wt vmcnt(16) lgkmcnt(4)
aw a208,v236
mm v[28:31],v[220:223],v[160:163],v[28:31]
rt v[216:217],v243 offset:50688
rt v[218:219],v243 offset:51712
mm v[0:3],v[192:195],v[128:131],v[0:3]
ar v224,a24
ar v225,a25
mm v[0:3],v[196:199],a[104:107],v[0:3]
ld v[160:163],v244,s[52:55],s91 offen offset:2048 sc1
mm v[32:35],v[196:199],a[108:111],v[32:35]
ar v226,a26
ar v227,a27
mm v[32:35],v[192:195],v[140:143],v[32:35]
rt v[220:221],v243 offset:59136
rt v[222:223],v243 offset:60160
mm v[64:67],v[192:195],a[96:99],v[64:67]
ar v228,a28
ar v229,a29
mm v[64:67],v[196:199],a[112:115],v[64:67]
ld v[164:167],v245,s[52:55],s91 offen offset:2048 sc1
mm v[96:99],v[196:199],a[116:119],v[96:99]
ar v230,a30
ar v231,a31
mm v[96:99],v[192:195],a[100:103],v[96:99]
rt v[196:197],v240 offset:50816
rt v[198:199],v240 offset:51840
mm v[100:103],v[200:203],a[100:103],v[100:103]
s_add_u32 m0, s83, 35968
cv v248,v224,v225
mm v[100:103],v[204:207],a[116:119],v[100:103]
ld v234,s[44:47],s80 offen lds
mm v[68:71],v[204:207],a[112:115],v[68:71]
cv v249,v226,v227
cv v250,v228,v229
mm v[68:71],v[200:203],a[96:99],v[68:71]
rt v[192:193],v240 offset:59264
rt v[194:195],v240 offset:60288
mm v[36:39],v[200:203],v[140:143],v[36:39]
cv v251,v230,v231
aw a24,v248
mm v[36:39],v[204:207],a[108:111],v[36:39]
ld v[168:171],v246,s[52:55],s91 offen offset:2048 sc1
mm v[4:7],v[204:207],a[104:107],v[4:7]
wt lgkmcnt(4)
aw a25,v249
mm v[4:7],v[200:203],v[128:131],v[4:7]
rt v[204:205],v241 offset:50816
rt v[206:207],v241 offset:51840
mm v[8:11],v[208:211],v[128:131],v[8:11]
s_add_u32 m0, s83, 36992
aw a26,v250
mm v[8:11],v[212:215],a[104:107],v[8:11]
ld v235,s[44:47],s80 offen lds
mm v[40:43],v[212:215],a[108:111],v[40:43]
s_add_u32 s80, s80, 524288
aw a27,v251
mm v[40:43],v[208:211],v[140:143],v[40:43]
rt v[200:201],v241 offset:59264
rt v[202:203],v241 offset:60288
mm v[72:75],v[208:211],a[96:99],v[72:75]
aw a209,v236
aw a210,v236
mm v[72:75],v[212:215],a[112:115],v[72:75]
ld v[172:175],v247,s[52:55],s91 offen offset:2048 sc1
mm v[104:107],v[212:215],a[116:119],v[104:107]
aw a211,v236
aw a232,0
mm v[104:107],v[208:211],a[100:103],v[104:107]
rt v[212:213],v242 offset:50816
rt v[214:215],v242 offset:51840
mm v[108:111],v[216:219],a[100:103],v[108:111]
aw a233,0
aw a234,0
mm v[108:111],v[220:223],a[116:119],v[108:111]
aw a235,0
s_lshl_b32 s20, s72, 10
mm v[76:79],v[220:223],a[112:115],v[76:79]
s_add_u32 s20, s20, 16384
s_mov_b32 s4, 1466176288
mm v[76:79],v[216:219],a[96:99],v[76:79]
rt v[208:209],v242 offset:59264
rt v[210:211],v242 offset:60288
mm v[44:47],v[216:219],v[140:143],v[44:47]
s_cmp_ge_u32 s2, 64
s_cselect_b32 s4, 1182081808, s4
mm v[44:47],v[220:223],a[108:111],v[44:47]
st a[8:11],v255,s[48:51],s18 offen offset:1024 sc1
mm v[12:15],v[220:223],a[104:107],v[12:15]
wt lgkmcnt(4)
mm v[12:15],v[216:219],v[128:131],v[12:15]
rt v[220:221],v243 offset:50816
rt v[222:223],v243 offset:51840
mm v[16:19],v[196:199],v[128:131],v[16:19]
ar v224,a240
ar v225,a241
mm v[16:19],v[192:195],a[104:107],v[16:19]
ld v[176:179],v244,s[52:55],s91 offen offset:3072 sc1
mm v[48:51],v[192:195],a[108:111],v[48:51]
ar v226,a242
ar v227,a243
mm v[48:51],v[196:199],v[140:143],v[48:51]
rt v[216:217],v243 offset:59264
rt v[218:219],v243 offset:60288
mm v[80:83],v[196:199],a[96:99],v[80:83]
ar v228,a244
ar v229,a245
mm v[80:83],v[192:195],a[112:115],v[80:83]
ld v[180:183],v245,s[52:55],s91 offen offset:3072 sc1
mm v[112:115],v[192:195],a[116:119],v[112:115]
ar v230,a246
ar v231,a247
.La5_LW1_13:
s_cbranch_execz .La5_LW1_R0
mm v[112:115],v[196:199],a[100:103],v[112:115]
cv v248,v224,v225
cv v249,v226,v227
mm v[116:119],v[204:207],a[100:103],v[116:119]
wt lgkmcnt(0)
s_barrier
mm v[116:119],v[200:203],a[116:119],v[116:119]
ld v[184:187],v246,s[52:55],s91 offen offset:3072 sc1
mm v[84:87],v[200:203],a[112:115],v[84:87]
rt v[192:193],v240 offset:0
rt v[194:195],v240 offset:1024
mm v[84:87],v[204:207],a[96:99],v[84:87]
rt v[196:197],v240 offset:8448
rt v[198:199],v240 offset:9472
mm v[52:55],v[204:207],v[140:143],v[52:55]
s_add_u32 m0, s83, 50688
cv v250,v228,v229
mm v[52:55],v[200:203],a[108:111],v[52:55]
ld v232,s[44:47],s80 offen lds
mm v[20:23],v[200:203],a[104:107],v[20:23]
cv v251,v230,v231
aw a28,v248
mm v[20:23],v[204:207],v[128:131],v[20:23]
rt v[200:201],v241 offset:0
rt v[202:203],v241 offset:1024
mm v[24:27],v[212:215],v[128:131],v[24:27]
aw a29,v249
aw a30,v250
mm v[24:27],v[208:211],a[104:107],v[24:27]
ld v[188:191],v247,s[52:55],s91 offen offset:3072 sc1
mm v[56:59],v[208:211],a[108:111],v[56:59]
s_add_u32 s91, s91, 4096
aw a31,v251
mm v[56:59],v[212:215],v[140:143],v[56:59]
rt v[204:205],v241 offset:8448
rt v[206:207],v241 offset:9472
mm v[88:91],v[212:215],a[96:99],v[88:91]
s_add_u32 m0, s83, 51712
mm v[88:91],v[208:211],a[112:115],v[88:91]
ld v233,s[44:47],s80 offen lds
mm v[120:123],v[208:211],a[116:119],v[120:123]
s_cmp_ge_u32 s2, 128
s_cselect_b32 s4, 1967534368, s4
mm v[120:123],v[212:215],a[100:103],v[120:123]
rt v[208:209],v242 offset:0
rt v[210:211],v242 offset:1024
mm v[124:127],v[220:223],a[100:103],v[124:127]
s_cmp_ge_u32 s2, 192
s_cselect_b32 s4, 1683431728, s4
mm v[124:127],v[216:219],a[116:119],v[124:127]
v_bfe_u32 v236, v255, 4, 3
v_lshlrev_b32 v236, 2, v236
mm v[92:95],v[216:219],a[112:115],v[92:95]
v_lshrrev_b32 v236, v236, s4
v_and_b32 v236, 15, v236
mm v[92:95],v[220:223],a[96:99],v[92:95]
rt v[212:213],v242 offset:8448
rt v[214:215],v242 offset:9472
mm v[60:63],v[220:223],v[140:143],v[60:63]
v_bfe_u32 v239, v255, 7, 1
v_mul_u32_u24 v239, 28, v239
mm v[60:63],v[216:219],a[108:111],v[60:63]
v_xor_b32 v236, v236, v239
v_xor_b32 v236, s72, v236
mm v[28:31],v[216:219],a[104:107],v[28:31]
wt vmcnt(15) lgkmcnt(4)
v_lshlrev_b32 v236, 3, v236
mm v[28:31],v[220:223],v[128:131],v[28:31]
rt v[216:217],v243 offset:0
rt v[218:219],v243 offset:1024
mm v[0:3],v[192:195],v[132:135],v[0:3]
ar v224,a32
ar v225,a33
aw a225,v236
mm v[0:3],v[196:199],v[152:155],v[0:3]
ld v[128:131],v244,s[52:55],s91 offen offset:0 sc1
s_lshl3_add_u32 s5, s76, s101
mm v[32:35],v[196:199],v[156:159],v[32:35]
ar v226,a34
ar v227,a35
s_lshl2_add_u32 s5, s5, s3
mm v[32:35],v[192:195],v[136:139],v[32:35]
rt v[220:221],v243 offset:8448
rt v[222:223],v243 offset:9472
s_lshl_b32 s5, s5, 8
mm v[64:67],v[192:195],v[144:147],v[64:67]
ar v228,a36
ar v229,a37
s_add_u32 s5, s5, 1048576
mm v[64:67],v[196:199],a[120:123],v[64:67]
ld v[140:143],v245,s[52:55],s91 offen offset:0 sc1
mm v[96:99],v[196:199],a[124:127],v[96:99]
ar v230,a38
ar v231,a39
mm v[96:99],v[192:195],v[148:151],v[96:99]
rt v[196:197],v240 offset:128
rt v[198:199],v240 offset:1152
mm v[100:103],v[200:203],v[148:151],v[100:103]
s_add_u32 m0, s83, 52864
cv v248,v224,v225
mm v[100:103],v[204:207],a[124:127],v[100:103]
ld v234,s[44:47],s80 offen lds
mm v[68:71],v[204:207],a[120:123],v[68:71]
cv v249,v226,v227
cv v250,v228,v229
mm v[68:71],v[200:203],v[144:147],v[68:71]
rt v[192:193],v240 offset:8576
rt v[194:195],v240 offset:9600
mm v[36:39],v[200:203],v[136:139],v[36:39]
cv v251,v230,v231
aw a32,v248
mm v[36:39],v[204:207],v[156:159],v[36:39]
ld a[192:195],v246,s[52:55],s91 offen offset:0 sc1
mm v[4:7],v[204:207],v[152:155],v[4:7]
wt lgkmcnt(4)
aw a33,v249
mm v[4:7],v[200:203],v[132:135],v[4:7]
rt v[204:205],v241 offset:128
rt v[206:207],v241 offset:1152
mm v[8:11],v[208:211],v[132:135],v[8:11]
s_add_u32 m0, s83, 53888
aw a34,v250
mm v[8:11],v[212:215],v[152:155],v[8:11]
ld v235,s[44:47],s80 offen lds
mm v[40:43],v[212:215],v[156:159],v[40:43]
aw a35,v251
mm v[40:43],v[208:211],v[136:139],v[40:43]
rt v[200:201],v241 offset:8576
rt v[202:203],v241 offset:9600
mm v[72:75],v[208:211],v[144:147],v[72:75]
mm v[72:75],v[212:215],a[120:123],v[72:75]
ld a[196:199],v247,s[52:55],s91 offen offset:0 sc1
mm v[104:107],v[212:215],a[124:127],v[104:107]
mm v[104:107],v[208:211],v[148:151],v[104:107]
rt v[212:213],v242 offset:128
rt v[214:215],v242 offset:1152
mm v[108:111],v[216:219],v[148:151],v[108:111]
mm v[108:111],v[220:223],a[124:127],v[108:111]
mm v[76:79],v[220:223],a[120:123],v[76:79]
mm v[76:79],v[216:219],v[144:147],v[76:79]
rt v[208:209],v242 offset:8576
rt v[210:211],v242 offset:9600
mm v[44:47],v[216:219],v[136:139],v[44:47]
mm v[44:47],v[220:223],v[156:159],v[44:47]
st a[12:15],v255,s[48:51],s18 offen offset:2048 sc1
mm v[12:15],v[220:223],v[152:155],v[12:15]
wt lgkmcnt(4)
mm v[12:15],v[216:219],v[132:135],v[12:15]
rt v[220:221],v243 offset:128
rt v[222:223],v243 offset:1152
mm v[16:19],v[196:199],v[132:135],v[16:19]
ar v224,a248
ar v225,a249
mm v[16:19],v[192:195],v[152:155],v[16:19]
ld a[200:203],v244,s[52:55],s91 offen offset:1024 sc1
mm v[48:51],v[192:195],v[156:159],v[48:51]
ar v226,a250
ar v227,a251
mm v[48:51],v[196:199],v[136:139],v[48:51]
rt v[216:217],v243 offset:8576
rt v[218:219],v243 offset:9600
mm v[80:83],v[196:199],v[144:147],v[80:83]
ar v228,a252
ar v229,a253
mm v[80:83],v[192:195],a[120:123],v[80:83]
ld a[204:207],v245,s[52:55],s91 offen offset:1024 sc1
mm v[112:115],v[192:195],a[124:127],v[112:115]
ar v230,a254
ar v231,a255
mm v[112:115],v[196:199],v[148:151],v[112:115]
cv v248,v224,v225
cv v249,v226,v227
mm v[116:119],v[204:207],v[148:151],v[116:119]
wt lgkmcnt(0)
s_barrier
mm v[116:119],v[200:203],a[124:127],v[116:119]
cv v250,v228,v229
cv v251,v230,v231
mm v[84:87],v[200:203],a[120:123],v[84:87]
rt v[192:193],v240 offset:16896
rt v[194:195],v240 offset:17920
mm v[84:87],v[204:207],v[144:147],v[84:87]
rt v[196:197],v240 offset:25344
rt v[198:199],v240 offset:26368
mm v[52:55],v[204:207],v[136:139],v[52:55]
aw a36,v248
aw a37,v249
mm v[52:55],v[200:203],v[156:159],v[52:55]
aw a38,v250
aw a39,v251
mm v[20:23],v[200:203],v[152:155],v[20:23]
s_getpc_b64 s[92:93]
.La5_LCT2:
s_and_b32 s93, s93, 0xffff
mm v[20:23],v[204:207],v[132:135],v[20:23]
rt v[200:201],v241 offset:16896
rt v[202:203],v241 offset:17920
mm v[24:27],v[212:215],v[132:135],v[24:27]
s_lshr_b32 s94, s2, 3
s_lshl2_add_u32 s94, s94, s3
mm v[24:27],v[208:211],v[152:155],v[24:27]
s_lshl_b32 s94, s94, 10
v_lshrrev_b32 v233, 7, v255
mm v[56:59],v[208:211],v[156:159],v[56:59]
v_lshlrev_b32 v232, 3, v255
v_lshl_add_u32 v232, v233, 28, v232
mm v[56:59],v[212:215],v[136:139],v[56:59]
rt v[204:205],v241 offset:25344
rt v[206:207],v241 offset:26368
mm v[88:91],v[212:215],v[144:147],v[88:91]
v_add_u32 v232, s94, v232
s_mov_b32 s94, .La5_LCODEEND-.La5_LCT2
mm v[88:91],v[208:211],a[120:123],v[88:91]
s_mov_b32 s95, 0x20000
buffer_load_dword a253, v232, s[92:95], 0 offen
mm v[120:123],v[208:211],a[124:127],v[120:123]
s_mov_b32 s80, 8192
mm v[120:123],v[212:215],v[148:151],v[120:123]
rt v[208:209],v242 offset:16896
rt v[210:211],v242 offset:17920
mm v[124:127],v[220:223],v[148:151],v[124:127]
s_cmp_eq_u32 s73, 0
s_cselect_b32 s80, -8192, s80
mm v[124:127],v[216:219],a[124:127],v[124:127]
ld v[148:151],v246,s[52:55],s91 offen offset:1024 sc1
mm v[92:95],v[216:219],a[120:123],v[92:95]
s_add_u32 s83, s83, 4294886912
s_lshl_b32 s92, s76, 21
mm v[92:95],v[220:223],v[144:147],v[92:95]
rt v[212:213],v242 offset:25344
rt v[214:215],v242 offset:26368
mm v[60:63],v[220:223],v[136:139],v[60:63]
ds_write_b128 v238, a[12:15] offset:6144
mm v[60:63],v[216:219],v[156:159],v[60:63]
st a[16:19],v255,s[48:51],s18 offen offset:3072 sc1
mm v[28:31],v[216:219],v[152:155],v[28:31]
wt vmcnt(13) lgkmcnt(5)
s_add_u32 s92, s92, 54525952
mm v[28:31],v[220:223],v[132:135],v[28:31]
rt v[216:217],v243 offset:16896
rt v[218:219],v243 offset:17920
mm v[0:3],v[192:195],v[160:163],v[0:3]
ar v224,a40
ar v225,a41
mm v[0:3],v[196:199],v[176:179],v[0:3]
ld v[132:135],v247,s[52:55],s91 offen offset:1024 sc1
mm v[32:35],v[196:199],v[180:183],v[32:35]
ar v226,a42
ar v227,a43
mm v[32:35],v[192:195],v[164:167],v[32:35]
rt v[220:221],v243 offset:25344
rt v[222:223],v243 offset:26368
mm v[64:67],v[192:195],v[168:171],v[64:67]
ar v228,a44
ar v229,a45
mm v[64:67],v[196:199],v[184:187],v[64:67]
ld a[96:99],v244,s[52:55],s91 offen offset:2048 sc1
mm v[96:99],v[196:199],v[188:191],v[96:99]
ar v230,a46
ar v231,a47
mm v[96:99],v[192:195],v[172:175],v[96:99]
rt v[196:197],v240 offset:17024
rt v[198:199],v240 offset:18048
mm v[100:103],v[200:203],v[172:175],v[100:103]
cv v248,v224,v225
cv v249,v226,v227
mm v[100:103],v[204:207],v[188:191],v[100:103]
ld a[100:103],v245,s[52:55],s91 offen offset:2048 sc1
mm v[68:71],v[204:207],v[184:187],v[68:71]
cv v250,v228,v229
cv v251,v230,v231
mm v[68:71],v[200:203],v[168:171],v[68:71]
rt v[192:193],v240 offset:25472
rt v[194:195],v240 offset:26496
mm v[36:39],v[200:203],v[164:167],v[36:39]
aw a40,v248
aw a41,v249
mm v[36:39],v[204:207],v[180:183],v[36:39]
ld a[104:107],v246,s[52:55],s91 offen offset:2048 sc1
mm v[4:7],v[204:207],v[176:179],v[4:7]
wt lgkmcnt(4)
aw a42,v250
mm v[4:7],v[200:203],v[160:163],v[4:7]
rt v[204:205],v241 offset:17024
rt v[206:207],v241 offset:18048
mm v[8:11],v[208:211],v[160:163],v[8:11]
aw a43,v251
mm v[8:11],v[212:215],v[176:179],v[8:11]
ld a[108:111],v247,s[52:55],s91 offen offset:2048 sc1
mm v[40:43],v[212:215],v[180:183],v[40:43]
s_add_u32 s44, s26, s92
s_addc_u32 s45, s27, 0
mm v[40:43],v[208:211],v[164:167],v[40:43]
rt v[200:201],v241 offset:25472
rt v[202:203],v241 offset:26496
mm v[72:75],v[208:211],v[168:171],v[72:75]
s_and_b32 s45, s45, 0xffff
s_mov_b32 s46, 2097536
mm v[72:75],v[212:215],v[184:187],v[72:75]
s_mov_b32 s95, 4096
s_mov_b32 s96, 8192
mm v[104:107],v[212:215],v[188:191],v[104:107]
s_mov_b32 s18, 0
s_mov_b32 s97, 12288
mm v[104:107],v[208:211],v[172:175],v[104:107]
rt v[212:213],v242 offset:17024
rt v[214:215],v242 offset:18048
mm v[108:111],v[216:219],v[172:175],v[108:111]
s_mul_i32 s92, s3, 196608
s_sub_u32 s93, 1376256, s92
mm v[108:111],v[220:223],v[188:191],v[108:111]
st a[20:23],v255,s[48:51],s18 offen offset:0 sc1
mm v[76:79],v[220:223],v[184:187],v[76:79]
mm v[76:79],v[216:219],v[168:171],v[76:79]
rt v[208:209],v242 offset:25472
rt v[210:211],v242 offset:26496
mm v[44:47],v[216:219],v[164:167],v[44:47]
ds_write_b128 v238, a[16:19] offset:7168
mm v[44:47],v[220:223],v[180:183],v[44:47]
st a[24:27],v255,s[48:51],s18 offen offset:1024 sc1
mm v[12:15],v[220:223],v[176:179],v[12:15]
wt lgkmcnt(5)
mm v[12:15],v[216:219],v[160:163],v[12:15]
rt v[220:221],v243 offset:17024
rt v[222:223],v243 offset:18048
mm v[16:19],v[196:199],v[160:163],v[16:19]
ar v224,a48
ar v225,a49
mm v[16:19],v[192:195],v[176:179],v[16:19]
ld a[112:115],v244,s[52:55],s91 offen offset:3072 sc1
mm v[48:51],v[192:195],v[180:183],v[48:51]
ar v226,a50
ar v227,a51
mm v[48:51],v[196:199],v[164:167],v[48:51]
rt v[216:217],v243 offset:25472
rt v[218:219],v243 offset:26496
mm v[80:83],v[196:199],v[168:171],v[80:83]
ar v228,a52
ar v229,a53
mm v[80:83],v[192:195],v[184:187],v[80:83]
ld a[116:119],v245,s[52:55],s91 offen offset:3072 sc1
mm v[112:115],v[192:195],v[188:191],v[112:115]
ar v230,a54
ar v231,a55
mm v[112:115],v[196:199],v[172:175],v[112:115]
cv v248,v224,v225
cv v249,v226,v227
mm v[116:119],v[204:207],v[172:175],v[116:119]
wt lgkmcnt(0)
s_barrier
mm v[116:119],v[200:203],v[188:191],v[116:119]
ld a[120:123],v246,s[52:55],s91 offen offset:3072 sc1
mm v[84:87],v[200:203],v[184:187],v[84:87]
rt v[192:193],v240 offset:33792
rt v[194:195],v240 offset:34816
mm v[84:87],v[204:207],v[168:171],v[84:87]
rt v[196:197],v240 offset:42240
rt v[198:199],v240 offset:43264
mm v[52:55],v[204:207],v[164:167],v[52:55]
ds_write_b128 v238, a[20:23] offset:32768
mm v[52:55],v[200:203],v[180:183],v[52:55]
ld a[124:127],v247,s[52:55],s91 offen offset:3072 sc1
mm v[20:23],v[200:203],v[176:179],v[20:23]
cv v250,v228,v229
cv v251,v230,v231
mm v[20:23],v[204:207],v[160:163],v[20:23]
rt v[200:201],v241 offset:33792
rt v[202:203],v241 offset:34816
mm v[24:27],v[212:215],v[160:163],v[24:27]
aw a44,v248
aw a45,v249
mm v[24:27],v[208:211],v[176:179],v[24:27]
aw a46,v250
aw a47,v251
mm v[56:59],v[208:211],v[180:183],v[56:59]
mm v[56:59],v[212:215],v[164:167],v[56:59]
rt v[204:205],v241 offset:42240
rt v[206:207],v241 offset:43264
mm v[88:91],v[212:215],v[168:171],v[88:91]
mm v[88:91],v[208:211],v[184:187],v[88:91]
mm v[120:123],v[208:211],v[188:191],v[120:123]
mm v[120:123],v[212:215],v[172:175],v[120:123]
rt v[208:209],v242 offset:33792
rt v[210:211],v242 offset:34816
mm v[124:127],v[220:223],v[172:175],v[124:127]
mm v[124:127],v[216:219],v[188:191],v[124:127]
st a[28:31],v255,s[48:51],s18 offen offset:2048 sc1
mm v[92:95],v[216:219],v[184:187],v[92:95]
mm v[92:95],v[220:223],v[168:171],v[92:95]
rt v[212:213],v242 offset:42240
rt v[214:215],v242 offset:43264
mm v[60:63],v[220:223],v[164:167],v[60:63]
ds_write_b128 v238, a[24:27] offset:33792
mm v[60:63],v[216:219],v[180:183],v[60:63]
st a[32:35],v255,s[48:51],s18 offen offset:3072 sc1
mm v[28:31],v[216:219],v[176:179],v[28:31]
wt vmcnt(12) lgkmcnt(5)
mm v[28:31],v[220:223],v[160:163],v[28:31]
rt v[216:217],v243 offset:33792
rt v[218:219],v243 offset:34816
mm v[0:3],v[192:195],v[128:131],v[0:3]
ar v224,a56
ar v225,a57
mm v[0:3],v[196:199],a[200:203],v[0:3]
ar v226,a58
ar v227,a59
mm v[32:35],v[196:199],a[204:207],v[32:35]
ar v228,a60
ar v229,a61
mm v[32:35],v[192:195],v[140:143],v[32:35]
rt v[220:221],v243 offset:42240
rt v[222:223],v243 offset:43264
mm v[64:67],v[192:195],a[192:195],v[64:67]
ar v230,a62
ar v231,a63
mm v[64:67],v[196:199],v[148:151],v[64:67]
cv v248,v224,v225
cv v249,v226,v227
mm v[96:99],v[196:199],v[132:135],v[96:99]
cv v250,v228,v229
cv v251,v230,v231
mm v[96:99],v[192:195],a[196:199],v[96:99]
rt v[196:197],v240 offset:33920
rt v[198:199],v240 offset:34944
mm v[100:103],v[200:203],a[196:199],v[100:103]
aw a48,v248
aw a49,v249
mm v[100:103],v[204:207],v[132:135],v[100:103]
aw a50,v250
aw a51,v251
mm v[68:71],v[204:207],v[148:151],v[68:71]
v_add_u32 v244, s92, v244
v_add_u32 v246, s93, v246
mm v[68:71],v[200:203],a[192:195],v[68:71]
rt v[192:193],v240 offset:42368
rt v[194:195],v240 offset:43392
mm v[36:39],v[200:203],v[140:143],v[36:39]
ds_write_b128 v238, a[28:31] offset:34816
mm v[36:39],v[204:207],a[204:207],v[36:39]
v_add_u32 v245, 0x1000, v244
v_add_u32 v247, 0x1000, v246
mm v[4:7],v[204:207],a[200:203],v[4:7]
wt lgkmcnt(5)
s_lshl_b32 s91, s72, 13
mm v[4:7],v[200:203],v[128:131],v[4:7]
rt v[204:205],v241 offset:33920
rt v[206:207],v241 offset:34944
mm v[8:11],v[208:211],v[128:131],v[8:11]
s_lshl4_add_u32 s92, s76, s3
s_lshl3_add_u32 s92, s92, s20
mm v[8:11],v[212:215],a[200:203],v[8:11]
s_bitset1_b32 s20, 30
mm v[40:43],v[212:215],a[204:207],v[40:43]
mm v[40:43],v[208:211],v[140:143],v[40:43]
rt v[200:201],v241 offset:42368
rt v[202:203],v241 offset:43392
mm v[72:75],v[208:211],a[192:195],v[72:75]
mm v[72:75],v[212:215],v[148:151],v[72:75]
mm v[104:107],v[212:215],v[132:135],v[104:107]
s_mov_b32 s18, 4096
mm v[104:107],v[208:211],a[196:199],v[104:107]
rt v[212:213],v242 offset:33920
rt v[214:215],v242 offset:34944
mm v[108:111],v[216:219],a[196:199],v[108:111]
mm v[108:111],v[220:223],v[132:135],v[108:111]
st a[36:39],v255,s[48:51],s18 offen offset:0 sc1
mm v[76:79],v[220:223],v[148:151],v[76:79]
mm v[76:79],v[216:219],a[192:195],v[76:79]
rt v[208:209],v242 offset:42368
rt v[210:211],v242 offset:43392
mm v[44:47],v[216:219],v[140:143],v[44:47]
ds_write_b128 v238, a[32:35] offset:35840
mm v[44:47],v[220:223],a[204:207],v[44:47]
st a[40:43],v255,s[48:51],s18 offen offset:1024 sc1
mm v[12:15],v[220:223],a[200:203],v[12:15]
wt lgkmcnt(5)
mm v[12:15],v[216:219],v[128:131],v[12:15]
rt v[220:221],v243 offset:33920
rt v[222:223],v243 offset:34944
mm v[16:19],v[196:199],v[128:131],v[16:19]
s_mov_b64 s[52:53], s[86:87]
mm v[16:19],v[192:195],a[200:203],v[16:19]
s_bitcmp1_b32 21, s73
s_cselect_b32 s54, 16384, 0
s_cselect_b32 s86, s88, 1073741824
mm v[48:51],v[192:195],a[204:207],v[48:51]
s_lshl_b32 s89, s3, 14
v_add_u32 v248, s89, v255
mm v[48:51],v[196:199],v[140:143],v[48:51]
rt v[216:217],v243 offset:42368
rt v[218:219],v243 offset:43392
mm v[80:83],v[196:199],a[192:195],v[80:83]
ds_write_b128 v238, a[36:39] offset:36864
mm v[80:83],v[192:195],v[148:151],v[80:83]
s_add_u32 s86, s86, 262144
mm v[112:115],v[192:195],v[132:135],v[112:115]
s_cmp_eq_u32 s73, 0
s_cselect_b32 s86, s92, s86
s_cselect_b32 s92, 1073741824, s92
mm v[112:115],v[196:199],a[196:199],v[112:115]
s_mov_b32 s87, s92
s_add_u32 s89, s89, 16384
mm v[116:119],v[204:207],a[196:199],v[116:119]
wt lgkmcnt(1)
s_barrier
mm v[116:119],v[200:203],v[132:135],v[116:119]
s_lshl_b32 m0, s3, 12
s_add_u32 m0, m0, 131072
mm v[84:87],v[200:203],v[148:151],v[84:87]
rt v[192:193],v240 offset:50688
rt v[194:195],v240 offset:51712
mm v[84:87],v[204:207],a[192:195],v[84:87]
rt v[196:197],v240 offset:59136
rt v[198:199],v240 offset:60160
mm v[52:55],v[204:207],v[140:143],v[52:55]
ds_write_b128 v238, a[40:43] offset:37888
mm v[52:55],v[200:203],a[204:207],v[52:55]
v_bfe_u32 v251, v255, 2, 5
v_and_b32 v224, 0x380, v255
mm v[20:23],v[200:203],a[200:203],v[20:23]
v_add_u32 v251, v251, v224
v_and_b32 v224, 16, v255
mm v[20:23],v[204:207],v[128:131],v[20:23]
rt v[200:201],v241 offset:50688
rt v[202:203],v241 offset:51712
mm v[24:27],v[212:215],v[128:131],v[24:27]
v_cmp_eq_u32 vcc, 0, v224
mm v[24:27],v[208:211],a[200:203],v[24:27]
v_cndmask_b32 v249, v253, v252, vcc
mm v[56:59],v[208:211],a[204:207],v[56:59]
mm v[56:59],v[212:215],v[140:143],v[56:59]
rt v[204:205],v241 offset:59136
rt v[206:207],v241 offset:60160
mm v[88:91],v[212:215],a[192:195],v[88:91]
ds_write_b128 v238, a[44:47] offset:38912
mm v[88:91],v[208:211],v[148:151],v[88:91]
st a[44:47],v255,s[48:51],s18 offen offset:2048 sc1
mm v[120:123],v[208:211],v[132:135],v[120:123]
mm v[120:123],v[212:215],a[196:199],v[120:123]
rt v[208:209],v242 offset:50688
rt v[210:211],v242 offset:51712
.La5_LW1_14:
s_cbranch_execz .La5_LW1_R0
mm v[124:127],v[220:223],a[196:199],v[124:127]
mm v[124:127],v[216:219],v[132:135],v[124:127]
mm v[92:95],v[216:219],v[148:151],v[92:95]
mm v[92:95],v[220:223],a[192:195],v[92:95]
rt v[212:213],v242 offset:59136
rt v[214:215],v242 offset:60160
mm v[60:63],v[220:223],v[140:143],v[60:63]
mm v[60:63],v[216:219],a[204:207],v[60:63]
st a[48:51],v255,s[48:51],s18 offen offset:3072 sc1
mm v[28:31],v[216:219],a[200:203],v[28:31]
wt vmcnt(6) lgkmcnt(5)
mm v[28:31],v[220:223],v[128:131],v[28:31]
rt v[216:217],v243 offset:50688
rt v[218:219],v243 offset:51712
mm v[0:3],v[192:195],a[96:99],v[0:3]
mm v[0:3],v[196:199],a[112:115],v[0:3]
mm v[32:35],v[196:199],a[116:119],v[32:35]
mm v[32:35],v[192:195],a[100:103],v[32:35]
rt v[220:221],v243 offset:59136
rt v[222:223],v243 offset:60160
mm v[64:67],v[192:195],a[104:107],v[64:67]
mm v[64:67],v[196:199],a[120:123],v[64:67]
mm v[96:99],v[196:199],a[124:127],v[96:99]
mm v[96:99],v[192:195],a[108:111],v[96:99]
rt v[196:197],v240 offset:50816
rt v[198:199],v240 offset:51840
mm v[100:103],v[200:203],a[108:111],v[100:103]
mm v[100:103],v[204:207],a[124:127],v[100:103]
mm v[68:71],v[204:207],a[120:123],v[68:71]
mm v[68:71],v[200:203],a[104:107],v[68:71]
rt v[192:193],v240 offset:59264
rt v[194:195],v240 offset:60288
mm v[36:39],v[200:203],a[100:103],v[36:39]
mm v[36:39],v[204:207],a[116:119],v[36:39]
mm v[4:7],v[204:207],a[112:115],v[4:7]
wt lgkmcnt(4)
mm v[4:7],v[200:203],a[96:99],v[4:7]
rt v[204:205],v241 offset:50816
rt v[206:207],v241 offset:51840
mm v[8:11],v[208:211],a[96:99],v[8:11]
mm v[8:11],v[212:215],a[112:115],v[8:11]
mm v[40:43],v[212:215],a[116:119],v[40:43]
cv v128,v0,v1
cv v129,v2,v3
mm v[40:43],v[208:211],a[100:103],v[40:43]
rt v[200:201],v241 offset:59264
rt v[202:203],v241 offset:60288
mm v[72:75],v[208:211],a[104:107],v[72:75]
cv v130,v4,v5
cv v131,v6,v7
mm v[72:75],v[212:215],a[120:123],v[72:75]
cv v144,v32,v33
cv v145,v34,v35
mm v[104:107],v[212:215],a[124:127],v[104:107]
cv v146,v36,v37
cv v147,v38,v39
mm v[104:107],v[208:211],a[108:111],v[104:107]
rt v[212:213],v242 offset:50816
rt v[214:215],v242 offset:51840
mm v[108:111],v[216:219],a[108:111],v[108:111]
cv v160,v64,v65
cv v161,v66,v67
mm v[108:111],v[220:223],a[124:127],v[108:111]
cv v162,v68,v69
cv v163,v70,v71
mm v[76:79],v[220:223],a[120:123],v[76:79]
cv v176,v96,v97
cv v177,v98,v99
mm v[76:79],v[216:219],a[104:107],v[76:79]
rt v[208:209],v242 offset:59264
rt v[210:211],v242 offset:60288
mm v[44:47],v[216:219],a[100:103],v[44:47]
cv v178,v100,v101
cv v179,v102,v103
mm v[44:47],v[220:223],a[116:119],v[44:47]
mm v[12:15],v[220:223],a[112:115],v[12:15]
wt lgkmcnt(4)
mm v[12:15],v[216:219],a[96:99],v[12:15]
rt v[220:221],v243 offset:50816
rt v[222:223],v243 offset:51840
mm v[16:19],v[196:199],a[96:99],v[16:19]
v_mul_u32_u24 v241, 0x5555, v224
v_lshrrev_b32 v241, 4, v241
v_add_u32 v241, 0x8888, v241
mm v[16:19],v[192:195],a[112:115],v[16:19]
st v[128:131],v255,s[52:55],0 offen offset:0 sc1
mm v[48:51],v[192:195],a[116:119],v[48:51]
cv v132,v8,v9
cv v133,v10,v11
cv v181,v106,v107
mm v[48:51],v[196:199],a[100:103],v[48:51]
rt v[216:217],v243 offset:59264
rt v[218:219],v243 offset:60288
cv v182,v108,v109
mm v[80:83],v[196:199],a[104:107],v[80:83]
cv v134,v12,v13
cv v135,v14,v15
cv v183,v110,v111
mm v[80:83],v[192:195],a[120:123],v[80:83]
st v[144:147],v255,s[52:55],s95 offen offset:0 sc1
ar v34,a238
mm v[112:115],v[192:195],a[124:127],v[112:115]
cv v148,v40,v41
cv v149,v42,v43
ar v35,a239
mm v[112:115],v[196:199],a[108:111],v[112:115]
cv v150,v44,v45
cv v151,v46,v47
v_mov_b32 v36, 0xff800000
mm v[116:119],v[204:207],a[108:111],v[116:119]
wt lgkmcnt(0)
s_barrier
v_mov_b32 v37, 0xff800000
mm v[116:119],v[200:203],a[124:127],v[116:119]
st v[160:163],v255,s[52:55],s96 offen offset:0 sc1
v_mov_b32 v38, 0xff800000
mm v[84:87],v[200:203],a[120:123],v[84:87]
cv v164,v72,v73
cv v165,v74,v75
v_mov_b32 v39, 0xff800000
mm v[84:87],v[204:207],a[104:107],v[84:87]
rd a[64:67],v248 offset:0
rd a[68:71],v248 offset:1024
mm v[52:55],v[204:207],a[100:103],v[52:55]
cv v166,v76,v77
cv v167,v78,v79
mm v[52:55],v[200:203],a[116:119],v[52:55]
st v[176:179],v255,s[52:55],s97 offen offset:0 sc1
mm v[20:23],v[200:203],a[112:115],v[20:23]
s_nop 0
cv v180,v104,v105
mm v[20:23],v[204:207],a[96:99],v[20:23]
rd a[72:75],v248 offset:2048
rd a[76:79],v248 offset:3072
mm v[24:27],v[212:215],a[96:99],v[24:27]
ar v32,a236
ar v33,a237
mm v[24:27],v[208:211],a[112:115],v[24:27]
st v[132:135],v255,s[52:55],0 offen offset:1024 sc1
mm v[56:59],v[208:211],a[116:119],v[56:59]
cv v136,v16,v17
cv v137,v18,v19
mm v[56:59],v[212:215],a[100:103],v[56:59]
rd a[80:83],v248 offset:4096
rd a[84:87],v248 offset:5120
mm v[88:91],v[212:215],a[104:107],v[88:91]
cv v138,v20,v21
cv v139,v22,v23
mm v[88:91],v[208:211],a[120:123],v[88:91]
st v[148:151],v255,s[52:55],s95 offen offset:1024 sc1
mm v[120:123],v[208:211],a[124:127],v[120:123]
cv v152,v48,v49
cv v153,v50,v51
mm v[120:123],v[212:215],a[108:111],v[120:123]
rd a[88:91],v248 offset:6144
rd a[92:95],v248 offset:7168
mm v[124:127],v[220:223],a[108:111],v[124:127]
ds_write_b128 v238, a[48:51] offset:39936
mm v[124:127],v[216:219],a[124:127],v[124:127]
st v[164:167],v255,s[52:55],s96 offen offset:1024 sc1
mm v[92:95],v[216:219],a[120:123],v[92:95]
cv v154,v52,v53
cv v155,v54,v55
mm v[92:95],v[220:223],a[104:107],v[92:95]
cv v168,v80,v81
cv v169,v82,v83
mm v[60:63],v[220:223],a[100:103],v[60:63]
cv v170,v84,v85
cv v171,v86,v87
mm v[60:63],v[216:219],a[116:119],v[60:63]
st v[180:183],v255,s[52:55],s97 offen offset:1024 sc1
mm v[28:31],v[216:219],a[112:115],v[28:31]
cv v184,v112,v113
cv v185,v114,v115
mm v[28:31],v[220:223],a[96:99],v[28:31]
cv v186,v116,v117
cv v187,v118,v119
wt lgkmcnt(1)
mm v[208:211],a[64:67],v[160:163],v[32:35]
mm v[216:219],a[64:67],v[176:179],0
st v[136:139],v255,s[52:55],0 offen offset:2048 sc1
mm v[212:215],a[80:83],v[160:163],v[36:39]
cv v172,v88,v89
cv v173,v90,v91
cv v174,v92,v93
mm v[220:223],a[80:83],v[176:179],v[32:35]
cv v175,v94,v95
cv v188,v120,v121
cv v189,v122,v123
mm v[208:211],a[68:71],v[164:167],v[208:211]
cv v190,v124,v125
cv v191,v126,v127
cv v140,v24,v25
mm v[216:219],a[68:71],v[180:183],v[216:219]
st v[152:155],v255,s[52:55],s95 offen offset:2048 sc1
cv v141,v26,v27
mm v[212:215],a[84:87],v[164:167],v[212:215]
cv v142,v28,v29
cv v143,v30,v31
cv v156,v56,v57
mm v[220:223],a[84:87],v[180:183],v[220:223]
cv v157,v58,v59
cv v158,v60,v61
cv v159,v62,v63
mm v[208:211],a[72:75],v[168:171],v[208:211]
mm v[216:219],a[72:75],v[184:187],v[216:219]
st v[168:171],v255,s[52:55],s96 offen offset:2048 sc1
mm v[212:215],a[88:91],v[168:171],v[212:215]
mm v[220:223],a[88:91],v[184:187],v[220:223]
mm v[208:211],a[76:79],v[172:175],v[208:211]
mm v[216:219],a[76:79],v[188:191],v[216:219]
st v[184:187],v255,s[52:55],s97 offen offset:2048 sc1
mm v[212:215],a[92:95],v[172:175],v[212:215]
rd a[64:67],v248 offset:16384
mm v[220:223],a[92:95],v[188:191],v[220:223]
rd a[96:99],v248 offset:8192
ex v0,v208
wt vmcnt(12)
buffer_store_dwordx2 v[252:253], off, s[64:67], s87 sc1
mm v[64:67],a[232:235],a[232:235],0
rd a[68:71],v248 offset:17408
ex v1,v209
mm v[68:71],a[232:235],a[232:235],0
rd a[100:103],v248 offset:9216
st v[140:143],v255,s[52:55],0 offen offset:3072 sc1
mm v[72:75],a[232:235],a[232:235],0
rd a[72:75],v248 offset:18432
ex v2,v210
mm v[76:79],a[232:235],a[232:235],0
rd a[104:107],v248 offset:10240
ex v3,v211
mm v[80:83],a[232:235],a[232:235],0
rd a[76:79],v248 offset:19456
ex v8,v216
mm v[84:87],a[232:235],a[232:235],0
rd a[108:111],v248 offset:11264
st v[156:159],v255,s[52:55],s95 offen offset:3072 sc1
mm v[88:91],a[232:235],a[232:235],0
rd a[80:83],v248 offset:20480
ex v9,v217
mm v[92:95],a[232:235],a[232:235],0
rd a[112:115],v248 offset:12288
ex v10,v218
mm v[96:99],a[232:235],a[232:235],0
rd a[84:87],v248 offset:21504
ex v11,v219
mm v[100:103],a[232:235],a[232:235],0
rd a[116:119],v248 offset:13312
st v[172:175],v255,s[52:55],s96 offen offset:3072 sc1
mm v[104:107],a[232:235],a[232:235],0
rd a[88:91],v248 offset:22528
ex v4,v212
mm v[108:111],a[232:235],a[232:235],0
rd a[120:123],v248 offset:14336
ex v5,v213
mm v[112:115],a[232:235],a[232:235],0
rd a[92:95],v248 offset:23552
ex v6,v214
mm v[116:119],a[232:235],a[232:235],0
rd a[124:127],v248 offset:15360
st v[188:191],v255,s[52:55],s97 offen offset:3072 sc1
ex v7,v215
mm v[120:123],a[232:235],a[232:235],0
ex v12,v220
mm v[124:127],a[232:235],a[232:235],0
aw a192,0
aw a193,0
aw a194,0
aw a195,0
aw a196,0
aw a197,0
aw a198,0
aw a199,0
v_add_u32 v248, 0x4000, v248
s_cmp_eq_u32 s3, 3
s_cbranch_scc1 .La5_LA_FH
.p2align 6
; ---- attention, 32 keys per window: S = K Q^T (causal mask through SrcC), P = exp2(S), O += V^T P, row sums l += 1 P (one sparse MFMA for both tiles); here the later 32 rows only
.La5_LA_HH:
wt lgkmcnt(1)
mm v[208:211],a[64:67],v[160:163],0
ex v13,v221
mm v[208:211],a[68:71],v[164:167],v[208:211]
cv v232,v0,v1
cv v233,v2,v3
mm v[208:211],a[72:75],v[168:171],v[208:211]
ex v14,v222
mm v[208:211],a[76:79],v[172:175],v[208:211]
cv v236,v8,v9
cv v237,v10,v11
mm v[216:219],a[76:79],v[188:191],0
ex v15,v223
mm v[216:219],a[72:75],v[184:187],v[216:219]
cv v234,v4,v5
cv v235,v6,v7
mm v[216:219],a[68:71],v[180:183],v[216:219]
ex v0,v208
mm v[216:219],a[64:67],v[176:179],v[216:219]
cv v238,v12,v13
cv v239,v14,v15
mm v[212:215],a[80:83],v[160:163],0
ex v1,v209
mm v[212:215],a[84:87],v[164:167],v[212:215]
rd a[64:67],v248 offset:16384
rd a[68:71],v248 offset:17408
mm v[212:215],a[88:91],v[168:171],v[212:215]
ex v2,v210
mm v[212:215],a[92:95],v[172:175],v[212:215]
rd a[72:75],v248 offset:18432
rd a[76:79],v248 offset:19456
mm v[220:223],a[92:95],v[188:191],0
ex v3,v211
mm v[220:223],a[88:91],v[184:187],v[220:223]
rd a[92:95],v248 offset:23552
mm v[220:223],a[84:87],v[180:183],v[220:223]
ex v8,v216
mm v[220:223],a[80:83],v[176:179],v[220:223]
wt lgkmcnt(5)
rd a[88:91],v248 offset:22528
mm v[64:67],a[96:99],v[232:235],v[64:67]
ex v9,v217
mm v[96:99],a[96:99],v[236:239],v[96:99]
rd a[96:99],v248 offset:8192
rd a[84:87],v248 offset:21504
mm v[100:103],a[100:103],v[236:239],v[100:103]
ex v10,v218
mm v[68:71],a[100:103],v[232:235],v[68:71]
rd a[100:103],v248 offset:9216
rd a[80:83],v248 offset:20480
mm v[72:75],a[104:107],v[232:235],v[72:75]
ex v11,v219
mm v[104:107],a[104:107],v[236:239],v[104:107]
rd a[104:107],v248 offset:10240
mm v[108:111],a[108:111],v[236:239],v[108:111]
ex v4,v212
mm v[76:79],a[108:111],v[232:235],v[76:79]
rd a[108:111],v248 offset:11264
s_add_u32 s85, s85, 1
mm v[80:83],a[112:115],v[232:235],v[80:83]
ex v5,v213
mm v[112:115],a[112:115],v[236:239],v[112:115]
rd a[112:115],v248 offset:12288
s_add_u32 s89, s89, 32768
mm v[116:119],a[116:119],v[236:239],v[116:119]
ex v6,v214
mm v[84:87],a[116:119],v[232:235],v[84:87]
rd a[116:119],v248 offset:13312
s_add_u32 s92, s85, s3
mm v[88:91],a[120:123],v[232:235],v[88:91]
ex v7,v215
mm v[120:123],a[120:123],v[236:239],v[120:123]
rd a[120:123],v248 offset:14336
buffer_load_dword v250, v251, s[64:67], s20 offen sc1
mm v[124:127],a[124:127],v[236:239],v[124:127]
ex v12,v220
mm v[92:95],a[124:127],v[232:235],v[92:95]
rd a[124:127],v248 offset:15360
s_barrier
v_smfmac_f32_16x16x64_bf16 a[196:199], a[208:211], v[232:239], v241
wt lgkmcnt(6)
mm v[208:211],a[64:67],v[160:163],0
ex v13,v221
mm v[208:211],a[68:71],v[164:167],v[208:211]
cv v232,v0,v1
cv v233,v2,v3
mm v[208:211],a[72:75],v[168:171],v[208:211]
ex v14,v222
mm v[208:211],a[76:79],v[172:175],v[208:211]
cv v236,v8,v9
cv v237,v10,v11
mm v[216:219],a[76:79],v[188:191],0
ex v15,v223
mm v[216:219],a[72:75],v[184:187],v[216:219]
cv v234,v4,v5
cv v235,v6,v7
mm v[216:219],a[68:71],v[180:183],v[216:219]
ex v0,v208
mm v[216:219],a[64:67],v[176:179],v[216:219]
cv v238,v12,v13
cv v239,v14,v15
mm v[212:215],a[80:83],v[160:163],0
ex v1,v209
mm v[212:215],a[84:87],v[164:167],v[212:215]
rd a[64:67],v248 offset:32768
rd a[68:71],v248 offset:33792
mm v[212:215],a[88:91],v[168:171],v[212:215]
ex v2,v210
mm v[212:215],a[92:95],v[172:175],v[212:215]
rd a[72:75],v248 offset:34816
rd a[76:79],v248 offset:35840
mm v[220:223],a[92:95],v[188:191],0
ex v3,v211
mm v[220:223],a[88:91],v[184:187],v[220:223]
rd a[92:95],v248 offset:39936
s_bitcmp1_b64 s[98:99], s85
mm v[220:223],a[84:87],v[180:183],v[220:223]
ex v8,v216
mm v[220:223],a[80:83],v[176:179],v[220:223]
wt lgkmcnt(5)
rd a[88:91],v248 offset:38912
mm v[64:67],a[96:99],v[232:235],v[64:67]
ex v9,v217
mm v[96:99],a[96:99],v[236:239],v[96:99]
rd a[96:99],v248 offset:24576
rd a[84:87],v248 offset:37888
mm v[100:103],a[100:103],v[236:239],v[100:103]
ex v10,v218
mm v[68:71],a[100:103],v[232:235],v[68:71]
rd a[100:103],v248 offset:25600
rd a[80:83],v248 offset:36864
mm v[72:75],a[104:107],v[232:235],v[72:75]
ex v11,v219
mm v[104:107],a[104:107],v[236:239],v[104:107]
rd a[104:107],v248 offset:26624
mm v[108:111],a[108:111],v[236:239],v[108:111]
ex v4,v212
mm v[76:79],a[108:111],v[232:235],v[76:79]
rd a[108:111],v248 offset:27648
mm v[80:83],a[112:115],v[232:235],v[80:83]
ex v5,v213
mm v[112:115],a[112:115],v[236:239],v[112:115]
rd a[112:115],v248 offset:28672
mm v[116:119],a[116:119],v[236:239],v[116:119]
ex v6,v214
mm v[84:87],a[116:119],v[232:235],v[84:87]
rd a[116:119],v248 offset:29696
wt vmcnt(1)
buffer_store_dwordx2 v[252:253], off, s[64:67], s86 sc1
s_mov_b32 s86, 1073741824
s_bitset0_b32 s20, 30
mm v[88:91],a[120:123],v[232:235],v[88:91]
ex v7,v215
mm v[120:123],a[120:123],v[236:239],v[120:123]
rd a[120:123],v248 offset:30720
mm v[124:127],a[124:127],v[236:239],v[124:127]
v_exp_f32_e64 v12, v220
mm v[92:95],a[124:127],v[232:235],v[92:95]
rd a[124:127],v248 offset:31744
v_smfmac_f32_16x16x64_bf16 a[196:199], a[208:211], v[232:239], v241
v_add_u32 v248, 0x8000, v248
s_cbranch_scc0 .La5_LA_HH1
s_call_b64 s[24:25], .La5_LA_EVENT
s_add_u32 s92, s85, s3
.La5_LA_HH1:
s_cmp_lt_u32 s92, 3
s_cbranch_scc1 .La5_LA_HH
; ---- first window with both row groups (diagonal of the earlier group)
.La5_LA_FH:
.La5_LW1_15:
s_cbranch_execz .La5_LW1_R0
wt lgkmcnt(9)
mm v[192:195],a[64:67],v[128:131],v[32:35]
ex v13,v221
mm v[192:195],a[68:71],v[132:135],v[192:195]
cv v232,v0,v1
cv v233,v2,v3
mm v[192:195],a[72:75],v[136:139],v[192:195]
ex v14,v222
mm v[192:195],a[76:79],v[140:143],v[192:195]
ld v255,s[60:63],s17 offen offset:0 sc1 lds
mm v[200:203],a[76:79],v[156:159],0
ex v15,v223
mm v[200:203],a[72:75],v[152:155],v[200:203]
cv v236,v8,v9
cv v237,v10,v11
mm v[200:203],a[68:71],v[148:151],v[200:203]
ex v192,v192
mm v[200:203],a[64:67],v[144:147],v[200:203]
wt lgkmcnt(1)
cv v234,v4,v5
mm v[196:199],a[80:83],v[128:131],v[36:39]
ex v193,v193
mm v[196:199],a[84:87],v[132:135],v[196:199]
cv v224,v192,v193
s_add_u32 s85, s85, 1
mm v[196:199],a[88:91],v[136:139],v[196:199]
ex v194,v194
mm v[196:199],a[92:95],v[140:143],v[196:199]
ld v255,s[60:63],s17 offen offset:1024 sc1 lds
mm v[204:207],a[92:95],v[156:159],v[32:35]
ex v195,v195
mm v[204:207],a[88:91],v[152:155],v[204:207]
cv v225,v194,v195
s_add_u32 s89, s89, 16384
mm v[204:207],a[84:87],v[148:151],v[204:207]
ex v200,v200
mm v[204:207],a[80:83],v[144:147],v[204:207]
ld v255,s[60:63],s17 offen offset:2048 sc1 lds
mm v[208:211],a[64:67],v[160:163],0
ex v201,v201
mm v[208:211],a[68:71],v[164:167],v[208:211]
cv v228,v200,v201
s_and_b32 s89, s89, 0x1ffff
mm v[208:211],a[72:75],v[168:171],v[208:211]
ex v202,v202
mm v[208:211],a[76:79],v[172:175],v[208:211]
ld v255,s[60:63],s17 offen offset:3072 sc1 lds
mm v[216:219],a[76:79],v[188:191],0
ex v203,v203
mm v[216:219],a[72:75],v[184:187],v[216:219]
cv v229,v202,v203
mm v[216:219],a[68:71],v[180:183],v[216:219]
ex v196,v196
mm v[216:219],a[64:67],v[176:179],v[216:219]
cv v235,v6,v7
cv v238,v12,v13
mm v[212:215],a[80:83],v[160:163],0
ex v197,v197
s_add_u32 s17, s17, s80
mm v[212:215],a[84:87],v[164:167],v[212:215]
cv v226,v196,v197
s_add_u32 m0, m0, 16384
s_and_b32 m0, m0, s78
mm v[212:215],a[88:91],v[168:171],v[212:215]
ex v198,v198
mm v[212:215],a[92:95],v[172:175],v[212:215]
cv v239,v14,v15
wt lgkmcnt(0)
mm v[220:223],a[92:95],v[188:191],0
ex v199,v199
mm v[220:223],a[88:91],v[184:187],v[220:223]
cv v227,v198,v199
mm v[220:223],a[84:87],v[180:183],v[220:223]
ex v204,v204
mm v[220:223],a[80:83],v[176:179],v[220:223]
wt vmcnt(10)
s_barrier
mm v[64:67],a[96:99],v[232:235],v[64:67]
ex v205,v205
mm v[96:99],a[96:99],v[236:239],v[96:99]
rd a[96:99],v248 offset:8192
cv v230,v204,v205
mm v[100:103],a[100:103],v[236:239],v[100:103]
ex v206,v206
mm v[68:71],a[100:103],v[232:235],v[68:71]
rd a[100:103],v248 offset:9216
buffer_load_dword v250, v251, s[64:67], s20 offen sc1
mm v[72:75],a[104:107],v[232:235],v[72:75]
ex v207,v207
mm v[104:107],a[104:107],v[236:239],v[104:107]
rd a[104:107],v248 offset:10240
cv v231,v206,v207
mm v[108:111],a[108:111],v[236:239],v[108:111]
ex v208,v208
mm v[76:79],a[108:111],v[232:235],v[76:79]
rd a[108:111],v248 offset:11264
mm v[80:83],a[112:115],v[232:235],v[80:83]
ex v209,v209
mm v[112:115],a[112:115],v[236:239],v[112:115]
rd a[112:115],v248 offset:12288
mm v[116:119],a[116:119],v[236:239],v[116:119]
ex v210,v210
mm v[84:87],a[116:119],v[232:235],v[84:87]
rd a[116:119],v248 offset:13312
mm v[88:91],a[120:123],v[232:235],v[88:91]
ex v211,v211
mm v[120:123],a[120:123],v[236:239],v[120:123]
rd a[120:123],v248 offset:14336
s_bitcmp1_b64 s[98:99], s85
mm v[124:127],a[124:127],v[236:239],v[124:127]
ex v216,v216
mm v[92:95],a[124:127],v[232:235],v[92:95]
rd a[124:127],v248 offset:15360
v_or_b32 v248, s89, v255
.La5_LW2_0:
s_cbranch_execz .La5_LW2_R0
v_smfmac_f32_16x16x64_bf16 a[196:199], a[208:211], v[232:239], v241
wt lgkmcnt(4)
v_smfmac_f32_16x16x64_bf16 a[192:195], a[208:211], v[224:231], v241
rd a[64:67],v248 offset:0
mm v[0:3],a[96:99],v[224:227],0
ex v217,v217
mm v[32:35],a[96:99],v[228:231],0
rd a[68:71],v248 offset:1024
cv v232,v208,v209
mm v[36:39],a[100:103],v[228:231],0
ex v218,v218
mm v[4:7],a[100:103],v[224:227],0
rd a[72:75],v248 offset:2048
cv v233,v210,v211
mm v[8:11],a[104:107],v[224:227],0
ex v219,v219
mm v[40:43],a[104:107],v[228:231],0
rd a[76:79],v248 offset:3072
cv v236,v216,v217
mm v[44:47],a[108:111],v[228:231],0
ex v212,v212
mm v[12:15],a[108:111],v[224:227],0
rd a[80:83],v248 offset:4096
wt lgkmcnt(5)
mm v[16:19],a[112:115],v[224:227],0
ex v213,v213
mm v[48:51],a[112:115],v[228:231],0
rd a[84:87],v248 offset:5120
cv v237,v218,v219
mm v[52:55],a[116:119],v[228:231],0
ex v214,v214
mm v[20:23],a[116:119],v[224:227],0
rd a[88:91],v248 offset:6144
cv v234,v212,v213
mm v[24:27],a[120:123],v[224:227],0
ex v215,v215
mm v[56:59],a[120:123],v[228:231],0
rd a[92:95],v248 offset:7168
cv v235,v214,v215
mm v[60:63],a[124:127],v[228:231],0
ex v220,v220
mm v[28:31],a[124:127],v[224:227],0
wt vmcnt(5)
buffer_store_dwordx2 v[252:253], off, s[64:67], s86 sc1
s_mov_b32 s86, 1073741824
s_bitset0_b32 s20, 30
s_cbranch_scc1 .La5_LA_FE0
.La5_LA_FS0:
wt lgkmcnt(4)
mm v[192:195],a[64:67],v[128:131],0
ex v221,v221
mm v[192:195],a[68:71],v[132:135],v[192:195]
cv v238,v220,v221
s_add_u32 s85, s85, 1
mm v[192:195],a[72:75],v[136:139],v[192:195]
ex v222,v222
mm v[192:195],a[76:79],v[140:143],v[192:195]
ld v255,s[60:63],s17 offen offset:0 sc1 lds
mm v[200:203],a[76:79],v[156:159],0
ex v223,v223
mm v[200:203],a[72:75],v[152:155],v[200:203]
cv v239,v222,v223
s_add_u32 s89, s89, 16384
mm v[200:203],a[68:71],v[148:151],v[200:203]
ex v192,v192
mm v[200:203],a[64:67],v[144:147],v[200:203]
wt lgkmcnt(0)
s_and_b32 s89, s89, 0x1ffff
mm v[196:199],a[80:83],v[128:131],0
ex v193,v193
mm v[196:199],a[84:87],v[132:135],v[196:199]
cv v224,v192,v193
mm v[196:199],a[88:91],v[136:139],v[196:199]
ex v194,v194
mm v[196:199],a[92:95],v[140:143],v[196:199]
ld v255,s[60:63],s17 offen offset:1024 sc1 lds
mm v[204:207],a[92:95],v[156:159],0
ex v195,v195
mm v[204:207],a[88:91],v[152:155],v[204:207]
cv v225,v194,v195
mm v[204:207],a[84:87],v[148:151],v[204:207]
ex v200,v200
mm v[204:207],a[80:83],v[144:147],v[204:207]
ld v255,s[60:63],s17 offen offset:2048 sc1 lds
mm v[208:211],a[64:67],v[160:163],0
ex v201,v201
mm v[208:211],a[68:71],v[164:167],v[208:211]
cv v228,v200,v201
mm v[208:211],a[72:75],v[168:171],v[208:211]
ex v202,v202
mm v[208:211],a[76:79],v[172:175],v[208:211]
ld v255,s[60:63],s17 offen offset:3072 sc1 lds
mm v[216:219],a[76:79],v[188:191],0
ex v203,v203
mm v[216:219],a[72:75],v[184:187],v[216:219]
cv v229,v202,v203
mm v[216:219],a[68:71],v[180:183],v[216:219]
ex v196,v196
mm v[216:219],a[64:67],v[176:179],v[216:219]
buffer_load_dword a253, v254, s[36:39], s90 offen
mm v[212:215],a[80:83],v[160:163],0
ex v197,v197
mm v[212:215],a[84:87],v[164:167],v[212:215]
cv v226,v196,v197
s_add_u32 s90, s90, s21
mm v[212:215],a[88:91],v[168:171],v[212:215]
ex v198,v198
mm v[212:215],a[92:95],v[172:175],v[212:215]
s_add_u32 s17, s17, s80
s_add_u32 m0, m0, 16384
mm v[220:223],a[92:95],v[188:191],0
ex v199,v199
mm v[220:223],a[88:91],v[184:187],v[220:223]
cv v227,v198,v199
s_and_b32 m0, m0, s78
mm v[220:223],a[84:87],v[180:183],v[220:223]
ex v204,v204
mm v[220:223],a[80:83],v[176:179],v[220:223]
wt vmcnt(11)
s_barrier
mm v[64:67],a[96:99],v[232:235],v[64:67]
ex v205,v205
mm v[96:99],a[96:99],v[236:239],v[96:99]
rd a[96:99],v248 offset:8192
cv v230,v204,v205
mm v[100:103],a[100:103],v[236:239],v[100:103]
ex v206,v206
mm v[68:71],a[100:103],v[232:235],v[68:71]
rd a[100:103],v248 offset:9216
s_cmp_eq_u32 s85, s81
buffer_load_dword v250, v251, s[64:67], s20 offen sc1
mm v[72:75],a[104:107],v[232:235],v[72:75]
ex v207,v207
mm v[104:107],a[104:107],v[236:239],v[104:107]
rd a[104:107],v248 offset:10240
cv v231,v206,v207
mm v[108:111],a[108:111],v[236:239],v[108:111]
ex v208,v208
mm v[76:79],a[108:111],v[232:235],v[76:79]
rd a[108:111],v248 offset:11264
s_cselect_b32 s17, s77, s17
mm v[80:83],a[112:115],v[232:235],v[80:83]
ex v209,v209
mm v[112:115],a[112:115],v[236:239],v[112:115]
rd a[112:115],v248 offset:12288
s_cselect_b32 s80, -8192, s80
mm v[116:119],a[116:119],v[236:239],v[116:119]
ex v210,v210
mm v[84:87],a[116:119],v[232:235],v[84:87]
rd a[116:119],v248 offset:13312
mm v[88:91],a[120:123],v[232:235],v[88:91]
ex v211,v211
mm v[120:123],a[120:123],v[236:239],v[120:123]
rd a[120:123],v248 offset:14336
s_bitcmp1_b64 s[98:99], s85
mm v[124:127],a[124:127],v[236:239],v[124:127]
ex v216,v216
mm v[92:95],a[124:127],v[232:235],v[92:95]
rd a[124:127],v248 offset:15360
v_or_b32 v248, s89, v255
v_smfmac_f32_16x16x64_bf16 a[196:199], a[208:211], v[232:239], v241
wt lgkmcnt(4)
v_smfmac_f32_16x16x64_bf16 a[192:195], a[208:211], v[224:231], v241
rd a[64:67],v248 offset:0
mm v[0:3],a[96:99],v[224:227],v[0:3]
ex v217,v217
mm v[32:35],a[96:99],v[228:231],v[32:35]
rd a[68:71],v248 offset:1024
cv v232,v208,v209
mm v[36:39],a[100:103],v[228:231],v[36:39]
ex v218,v218
mm v[4:7],a[100:103],v[224:227],v[4:7]
rd a[72:75],v248 offset:2048
cv v233,v210,v211
mm v[8:11],a[104:107],v[224:227],v[8:11]
ex v219,v219
mm v[40:43],a[104:107],v[228:231],v[40:43]
rd a[76:79],v248 offset:3072
cv v236,v216,v217
mm v[44:47],a[108:111],v[228:231],v[44:47]
ex v212,v212
mm v[12:15],a[108:111],v[224:227],v[12:15]
rd a[80:83],v248 offset:4096
wt lgkmcnt(5)
mm v[16:19],a[112:115],v[224:227],v[16:19]
ex v213,v213
mm v[48:51],a[112:115],v[228:231],v[48:51]
rd a[84:87],v248 offset:5120
cv v237,v218,v219
mm v[52:55],a[116:119],v[228:231],v[52:55]
ex v214,v214
mm v[20:23],a[116:119],v[224:227],v[20:23]
rd a[88:91],v248 offset:6144
cv v234,v212,v213
mm v[24:27],a[120:123],v[224:227],v[24:27]
ex v215,v215
mm v[56:59],a[120:123],v[228:231],v[56:59]
rd a[92:95],v248 offset:7168
cv v235,v214,v215
mm v[60:63],a[124:127],v[228:231],v[60:63]
v_exp_f32_e64 v220, v220
mm v[28:31],a[124:127],v[224:227],v[28:31]
s_cbranch_scc1 .La5_LA_F1E2
.p2align 6
; ---- steady windows
.La5_LA_F1:
wt lgkmcnt(4)
mm v[192:195],a[64:67],v[128:131],0
ex v221,v221
mm v[192:195],a[68:71],v[132:135],v[192:195]
cv v238,v220,v221
s_add_u32 s85, s85, 1
mm v[192:195],a[72:75],v[136:139],v[192:195]
ex v222,v222
mm v[192:195],a[76:79],v[140:143],v[192:195]
ld v255,s[60:63],s17 offen offset:0 sc1 lds
mm v[200:203],a[76:79],v[156:159],0
ex v223,v223
mm v[200:203],a[72:75],v[152:155],v[200:203]
cv v239,v222,v223
s_add_u32 s89, s89, 16384
mm v[200:203],a[68:71],v[148:151],v[200:203]
ex v192,v192
mm v[200:203],a[64:67],v[144:147],v[200:203]
wt lgkmcnt(0)
s_and_b32 s89, s89, 0x1ffff
mm v[196:199],a[80:83],v[128:131],0
ex v193,v193
mm v[196:199],a[84:87],v[132:135],v[196:199]
cv v224,v192,v193
mm v[196:199],a[88:91],v[136:139],v[196:199]
ex v194,v194
mm v[196:199],a[92:95],v[140:143],v[196:199]
ld v255,s[60:63],s17 offen offset:1024 sc1 lds
mm v[204:207],a[92:95],v[156:159],0
ex v195,v195
mm v[204:207],a[88:91],v[152:155],v[204:207]
cv v225,v194,v195
mm v[204:207],a[84:87],v[148:151],v[204:207]
ex v200,v200
mm v[204:207],a[80:83],v[144:147],v[204:207]
ld v255,s[60:63],s17 offen offset:2048 sc1 lds
mm v[208:211],a[64:67],v[160:163],0
ex v201,v201
mm v[208:211],a[68:71],v[164:167],v[208:211]
cv v228,v200,v201
mm v[208:211],a[72:75],v[168:171],v[208:211]
ex v202,v202
mm v[208:211],a[76:79],v[172:175],v[208:211]
ld v255,s[60:63],s17 offen offset:3072 sc1 lds
mm v[216:219],a[76:79],v[188:191],0
ex v203,v203
mm v[216:219],a[72:75],v[184:187],v[216:219]
cv v229,v202,v203
mm v[216:219],a[68:71],v[180:183],v[216:219]
ex v196,v196
mm v[216:219],a[64:67],v[176:179],v[216:219]
buffer_load_dword a253, v254, s[36:39], s90 offen
mm v[212:215],a[80:83],v[160:163],0
ex v197,v197
mm v[212:215],a[84:87],v[164:167],v[212:215]
cv v226,v196,v197
s_add_u32 s90, s90, s21
mm v[212:215],a[88:91],v[168:171],v[212:215]
ex v198,v198
mm v[212:215],a[92:95],v[172:175],v[212:215]
s_add_u32 s17, s17, s80
s_add_u32 m0, m0, 16384
mm v[220:223],a[92:95],v[188:191],0
ex v199,v199
mm v[220:223],a[88:91],v[184:187],v[220:223]
cv v227,v198,v199
s_and_b32 m0, m0, s78
mm v[220:223],a[84:87],v[180:183],v[220:223]
ex v204,v204
mm v[220:223],a[80:83],v[176:179],v[220:223]
wt vmcnt(11)
s_barrier
mm v[64:67],a[96:99],v[232:235],v[64:67]
ex v205,v205
mm v[96:99],a[96:99],v[236:239],v[96:99]
rd a[96:99],v248 offset:8192
cv v230,v204,v205
mm v[100:103],a[100:103],v[236:239],v[100:103]
ex v206,v206
mm v[68:71],a[100:103],v[232:235],v[68:71]
rd a[100:103],v248 offset:9216
s_cmp_eq_u32 s85, s81
mm v[72:75],a[104:107],v[232:235],v[72:75]
ex v207,v207
mm v[104:107],a[104:107],v[236:239],v[104:107]
rd a[104:107],v248 offset:10240
cv v231,v206,v207
mm v[108:111],a[108:111],v[236:239],v[108:111]
ex v208,v208
mm v[76:79],a[108:111],v[232:235],v[76:79]
rd a[108:111],v248 offset:11264
s_cselect_b32 s17, s77, s17
mm v[80:83],a[112:115],v[232:235],v[80:83]
ex v209,v209
mm v[112:115],a[112:115],v[236:239],v[112:115]
rd a[112:115],v248 offset:12288
s_cselect_b32 s80, -8192, s80
mm v[116:119],a[116:119],v[236:239],v[116:119]
ex v210,v210
mm v[84:87],a[116:119],v[232:235],v[84:87]
rd a[116:119],v248 offset:13312
mm v[88:91],a[120:123],v[232:235],v[88:91]
ex v211,v211
mm v[120:123],a[120:123],v[236:239],v[120:123]
rd a[120:123],v248 offset:14336
s_bitcmp1_b64 s[98:99], s85
mm v[124:127],a[124:127],v[236:239],v[124:127]
ex v216,v216
mm v[92:95],a[124:127],v[232:235],v[92:95]
rd a[124:127],v248 offset:15360
v_or_b32 v248, s89, v255
v_smfmac_f32_16x16x64_bf16 a[196:199], a[208:211], v[232:239], v241
wt lgkmcnt(4)
v_smfmac_f32_16x16x64_bf16 a[192:195], a[208:211], v[224:231], v241
rd a[64:67],v248 offset:0
mm v[0:3],a[96:99],v[224:227],v[0:3]
ex v217,v217
mm v[32:35],a[96:99],v[228:231],v[32:35]
rd a[68:71],v248 offset:1024
cv v232,v208,v209
mm v[36:39],a[100:103],v[228:231],v[36:39]
ex v218,v218
mm v[4:7],a[100:103],v[224:227],v[4:7]
rd a[72:75],v248 offset:2048
cv v233,v210,v211
mm v[8:11],a[104:107],v[224:227],v[8:11]
ex v219,v219
mm v[40:43],a[104:107],v[228:231],v[40:43]
rd a[76:79],v248 offset:3072
cv v236,v216,v217
mm v[44:47],a[108:111],v[228:231],v[44:47]
ex v212,v212
mm v[12:15],a[108:111],v[224:227],v[12:15]
rd a[80:83],v248 offset:4096
wt lgkmcnt(5)
mm v[16:19],a[112:115],v[224:227],v[16:19]
ex v213,v213
mm v[48:51],a[112:115],v[228:231],v[48:51]
rd a[84:87],v248 offset:5120
cv v237,v218,v219
mm v[52:55],a[116:119],v[228:231],v[52:55]
ex v214,v214
mm v[20:23],a[116:119],v[224:227],v[20:23]
rd a[88:91],v248 offset:6144
cv v234,v212,v213
mm v[24:27],a[120:123],v[224:227],v[24:27]
ex v215,v215
mm v[56:59],a[120:123],v[228:231],v[56:59]
rd a[92:95],v248 offset:7168
cv v235,v214,v215
mm v[60:63],a[124:127],v[228:231],v[60:63]
ex v220,v220
mm v[28:31],a[124:127],v[224:227],v[28:31]
s_cbranch_scc1 .La5_LA_F1E2
wt lgkmcnt(4)
mm v[192:195],a[64:67],v[128:131],0
ex v221,v221
mm v[192:195],a[68:71],v[132:135],v[192:195]
cv v238,v220,v221
s_add_u32 s85, s85, 1
mm v[192:195],a[72:75],v[136:139],v[192:195]
ex v222,v222
mm v[192:195],a[76:79],v[140:143],v[192:195]
ld v255,s[60:63],s17 offen offset:0 sc1 lds
mm v[200:203],a[76:79],v[156:159],0
ex v223,v223
mm v[200:203],a[72:75],v[152:155],v[200:203]
cv v239,v222,v223
s_add_u32 s89, s89, 16384
mm v[200:203],a[68:71],v[148:151],v[200:203]
ex v192,v192
mm v[200:203],a[64:67],v[144:147],v[200:203]
wt lgkmcnt(0)
s_and_b32 s89, s89, 0x1ffff
mm v[196:199],a[80:83],v[128:131],0
ex v193,v193
mm v[196:199],a[84:87],v[132:135],v[196:199]
cv v224,v192,v193
mm v[196:199],a[88:91],v[136:139],v[196:199]
ex v194,v194
mm v[196:199],a[92:95],v[140:143],v[196:199]
ld v255,s[60:63],s17 offen offset:1024 sc1 lds
mm v[204:207],a[92:95],v[156:159],0
ex v195,v195
mm v[204:207],a[88:91],v[152:155],v[204:207]
cv v225,v194,v195
mm v[204:207],a[84:87],v[148:151],v[204:207]
ex v200,v200
mm v[204:207],a[80:83],v[144:147],v[204:207]
ld v255,s[60:63],s17 offen offset:2048 sc1 lds
mm v[208:211],a[64:67],v[160:163],0
ex v201,v201
mm v[208:211],a[68:71],v[164:167],v[208:211]
cv v228,v200,v201
mm v[208:211],a[72:75],v[168:171],v[208:211]
ex v202,v202
mm v[208:211],a[76:79],v[172:175],v[208:211]
ld v255,s[60:63],s17 offen offset:3072 sc1 lds
mm v[216:219],a[76:79],v[188:191],0
ex v203,v203
mm v[216:219],a[72:75],v[184:187],v[216:219]
cv v229,v202,v203
mm v[216:219],a[68:71],v[180:183],v[216:219]
ex v196,v196
mm v[216:219],a[64:67],v[176:179],v[216:219]
buffer_load_dword a253, v254, s[36:39], s90 offen
mm v[212:215],a[80:83],v[160:163],0
ex v197,v197
mm v[212:215],a[84:87],v[164:167],v[212:215]
cv v226,v196,v197
s_add_u32 s90, s90, s21
mm v[212:215],a[88:91],v[168:171],v[212:215]
ex v198,v198
mm v[212:215],a[92:95],v[172:175],v[212:215]
s_add_u32 s17, s17, s80
s_add_u32 m0, m0, 16384
mm v[220:223],a[92:95],v[188:191],0
ex v199,v199
mm v[220:223],a[88:91],v[184:187],v[220:223]
cv v227,v198,v199
s_and_b32 m0, m0, s78
mm v[220:223],a[84:87],v[180:183],v[220:223]
ex v204,v204
mm v[220:223],a[80:83],v[176:179],v[220:223]
wt vmcnt(10)
s_barrier
mm v[64:67],a[96:99],v[232:235],v[64:67]
ex v205,v205
mm v[96:99],a[96:99],v[236:239],v[96:99]
rd a[96:99],v248 offset:8192
cv v230,v204,v205
mm v[100:103],a[100:103],v[236:239],v[100:103]
ex v206,v206
mm v[68:71],a[100:103],v[232:235],v[68:71]
rd a[100:103],v248 offset:9216
s_cmp_eq_u32 s85, s81
mm v[72:75],a[104:107],v[232:235],v[72:75]
ex v207,v207
mm v[104:107],a[104:107],v[236:239],v[104:107]
rd a[104:107],v248 offset:10240
cv v231,v206,v207
mm v[108:111],a[108:111],v[236:239],v[108:111]
ex v208,v208
mm v[76:79],a[108:111],v[232:235],v[76:79]
rd a[108:111],v248 offset:11264
s_cselect_b32 s17, s77, s17
mm v[80:83],a[112:115],v[232:235],v[80:83]
ex v209,v209
mm v[112:115],a[112:115],v[236:239],v[112:115]
rd a[112:115],v248 offset:12288
s_cselect_b32 s80, -8192, s80
mm v[116:119],a[116:119],v[236:239],v[116:119]
ex v210,v210
mm v[84:87],a[116:119],v[232:235],v[84:87]
rd a[116:119],v248 offset:13312
mm v[88:91],a[120:123],v[232:235],v[88:91]
ex v211,v211
mm v[120:123],a[120:123],v[236:239],v[120:123]
rd a[120:123],v248 offset:14336
s_bitcmp1_b64 s[98:99], s85
mm v[124:127],a[124:127],v[236:239],v[124:127]
ex v216,v216
mm v[92:95],a[124:127],v[232:235],v[92:95]
rd a[124:127],v248 offset:15360
v_or_b32 v248, s89, v255
v_smfmac_f32_16x16x64_bf16 a[196:199], a[208:211], v[232:239], v241
wt lgkmcnt(4)
v_smfmac_f32_16x16x64_bf16 a[192:195], a[208:211], v[224:231], v241
rd a[64:67],v248 offset:0
mm v[0:3],a[96:99],v[224:227],v[0:3]
ex v217,v217
mm v[32:35],a[96:99],v[228:231],v[32:35]
rd a[68:71],v248 offset:1024
cv v232,v208,v209
mm v[36:39],a[100:103],v[228:231],v[36:39]
ex v218,v218
mm v[4:7],a[100:103],v[224:227],v[4:7]
rd a[72:75],v248 offset:2048
cv v233,v210,v211
mm v[8:11],a[104:107],v[224:227],v[8:11]
ex v219,v219
mm v[40:43],a[104:107],v[228:231],v[40:43]
rd a[76:79],v248 offset:3072
cv v236,v216,v217
mm v[44:47],a[108:111],v[228:231],v[44:47]
ex v212,v212
mm v[12:15],a[108:111],v[224:227],v[12:15]
rd a[80:83],v248 offset:4096
wt lgkmcnt(5)
mm v[16:19],a[112:115],v[224:227],v[16:19]
ex v213,v213
mm v[48:51],a[112:115],v[228:231],v[48:51]
rd a[84:87],v248 offset:5120
cv v237,v218,v219
mm v[52:55],a[116:119],v[228:231],v[52:55]
ex v214,v214
mm v[20:23],a[116:119],v[224:227],v[20:23]
rd a[88:91],v248 offset:6144
cv v234,v212,v213
mm v[24:27],a[120:123],v[224:227],v[24:27]
ex v215,v215
mm v[56:59],a[120:123],v[228:231],v[56:59]
rd a[92:95],v248 offset:7168
cv v235,v214,v215
mm v[60:63],a[124:127],v[228:231],v[60:63]
ex v220,v220
mm v[28:31],a[124:127],v[224:227],v[28:31]
.La5_LA_F1E:
s_cbranch_scc0 .La5_LA_F1
.La5_LA_F1E2:
s_cmp_eq_u32 s85, s82
s_cbranch_scc1 .La5_LA_F1X
s_call_b64 s[24:25], .La5_LA_EVENT
s_branch .La5_LA_F1
.La5_LA_FE0:
s_nop 0
s_nop 0
.La5_LW2_1:
s_cbranch_execz .La5_LW2_R1
s_cmp_eq_u32 s85, s82
s_cbranch_scc1 .La5_LA_F1X
s_call_b64 s[24:25], .La5_LA_EVENT
s_branch .La5_LA_FS0
; ---- hand-offs between windows
.La5_LA_EVENT:
s_cmp_lg_u32 s85, 2
s_nop 0
s_nop 0
s_cbranch_scc1 .La5_LA_E1
wt vmcnt(0)
v_cmp_eq_u32 vcc, v250, v249
s_mov_b32 s17, s16
s_mov_b32 s78, 0x1ffff
s_lshl_b32 m0, s3, 12
s_or_b32 vcc_hi, vcc_hi, s79
s_cmp_eq_u64 vcc, -1
s_cbranch_scc0 .La5_LA_KSPIN
.La5_LA_KOK:
ld v255,s[60:63],s17 offen offset:0 sc1 lds
ld v255,s[60:63],s17 offen offset:1024 sc1 lds
ld v255,s[60:63],s17 offen offset:2048 sc1 lds
ld v255,s[60:63],s17 offen offset:3072 sc1 lds
s_add_u32 s17, s17, s80
s_add_u32 m0, m0, 16384
s_mov_b32 s20, s19
v_and_b32 v251, 4, v251
s_cmp_lg_u32 s3, 0
s_cbranch_scc1 .La5_LA_POLL
s_nop 0
ld v255,s[60:63],s17 offen offset:0 sc1 lds
ld v255,s[60:63],s17 offen offset:1024 sc1 lds
ld v255,s[60:63],s17 offen offset:2048 sc1 lds
ld v255,s[60:63],s17 offen offset:3072 sc1 lds
s_add_u32 s17, s17, s80
s_add_u32 m0, m0, 16384
.La5_LA_POLL:
s_cmp_lg_u32 s22, 0
s_nop 0
s_nop 0
s_cbranch_scc1 .La5_LA_WP
s_mov_b64 exec, 0
s_cmp_eq_u32 s3, 0
s_cbranch_scc1 .La5_LW5_0
s_cmp_eq_u32 s3, 1
s_cbranch_scc1 .La5_LW5_1
s_cmp_eq_u32 s3, 2
s_cbranch_scc1 .La5_LW5_2
s_branch .La5_LW5_3
.La5_LW5_R0:
s_mov_b64 exec, -1
.La5_LA_RETURN:
s_setpc_b64 s[24:25]
.La5_LA_WP:
s_mov_b64 exec, 0
s_cmp_eq_u32 s3, 0
s_cbranch_scc1 .La5_LW6_0
s_cmp_eq_u32 s3, 1
s_cbranch_scc1 .La5_LW6_1
s_cmp_eq_u32 s3, 2
s_cbranch_scc1 .La5_LW6_2
s_branch .La5_LW6_3
.La5_LW6_R0:
s_mov_b64 exec, -1
s_cmp_eq_u32 s73, 1
s_cbranch_scc1 .La5_LA_E0
buffer_load_dword v250, v251, s[64:67], s20 offen sc1
s_setpc_b64 s[24:25]
.La5_LA_E0:
s_mov_b32 s54, 16384
ld a[0:3],v255,s[52:55],0 offen offset:0 sc1
ld a[4:7],v255,s[52:55],0 offen offset:1024 sc1
ld a[8:11],v255,s[52:55],0 offen offset:2048 sc1
ld a[12:15],v255,s[52:55],0 offen offset:3072 sc1
ld a[16:19],v255,s[52:55],s95 offen offset:0 sc1
ld a[20:23],v255,s[52:55],s95 offen offset:1024 sc1
s_setpc_b64 s[24:25]
.La5_LA_E1:
ld a[24:27],v255,s[52:55],s95 offen offset:2048 sc1
ld a[28:31],v255,s[52:55],s95 offen offset:3072 sc1
ld a[32:35],v255,s[52:55],s96 offen offset:0 sc1
ld a[36:39],v255,s[52:55],s96 offen offset:1024 sc1
ld a[40:43],v255,s[52:55],s96 offen offset:2048 sc1
ld a[44:47],v255,s[52:55],s96 offen offset:3072 sc1
ld a[48:51],v255,s[52:55],s97 offen offset:0 sc1
ld a[52:55],v255,s[52:55],s97 offen offset:1024 sc1
ld a[56:59],v255,s[52:55],s97 offen offset:2048 sc1
ld a[60:63],v255,s[52:55],s97 offen offset:3072 sc1
s_setpc_b64 s[24:25]
; ---- helper: the partner-s q rows arrive during the last windows of the own block
.La5_LA_F1X:
s_cmp_eq_u32 s75, 0
s_nop 0
s_nop 0
s_cbranch_scc1 .La5_LA_DC3
s_cmp_eq_u32 s22, 0
s_cbranch_scc1 .La5_LA_DC
s_cmp_eq_u32 s73, 1
s_cbranch_scc1 .La5_LA_F1Y
s_nop 0
v_cmp_eq_u32 vcc, v250, v249
s_mov_b32 s54, 16384
s_and_b32 s92, vcc_lo, 3
s_cmp_eq_u32 s92, 3
s_cbranch_scc0 .La5_LA_QSPIN
.La5_LA_QOK:
wt lgkmcnt(4)
mm v[192:195],a[64:67],v[128:131],0
ex v221,v221
mm v[192:195],a[68:71],v[132:135],v[192:195]
cv v238,v220,v221
s_add_u32 s85, s85, 1
mm v[192:195],a[72:75],v[136:139],v[192:195]
ex v222,v222
mm v[192:195],a[76:79],v[140:143],v[192:195]
ld v255,s[60:63],s17 offen offset:0 sc1 lds
mm v[200:203],a[76:79],v[156:159],0
ex v223,v223
mm v[200:203],a[72:75],v[152:155],v[200:203]
.La5_LW6_0:
s_cbranch_execz .La5_LW6_4
cv v239,v222,v223
s_add_u32 s89, s89, 16384
mm v[200:203],a[68:71],v[148:151],v[200:203]
ex v192,v192
mm v[200:203],a[64:67],v[144:147],v[200:203]
wt lgkmcnt(0)
s_and_b32 s89, s89, 0x1ffff
mm v[196:199],a[80:83],v[128:131],0
ex v193,v193
mm v[196:199],a[84:87],v[132:135],v[196:199]
cv v224,v192,v193
mm v[196:199],a[88:91],v[136:139],v[196:199]
ex v194,v194
mm v[196:199],a[92:95],v[140:143],v[196:199]
ld v255,s[60:63],s17 offen offset:1024 sc1 lds
mm v[204:207],a[92:95],v[156:159],0
ex v195,v195
mm v[204:207],a[88:91],v[152:155],v[204:207]
cv v225,v194,v195
mm v[204:207],a[84:87],v[148:151],v[204:207]
ex v200,v200
mm v[204:207],a[80:83],v[144:147],v[204:207]
ld v255,s[60:63],s17 offen offset:2048 sc1 lds
mm v[208:211],a[64:67],v[160:163],0
ex v201,v201
mm v[208:211],a[68:71],v[164:167],v[208:211]
cv v228,v200,v201
mm v[208:211],a[72:75],v[168:171],v[208:211]
ex v202,v202
mm v[208:211],a[76:79],v[172:175],v[208:211]
ld v255,s[60:63],s17 offen offset:3072 sc1 lds
mm v[216:219],a[76:79],v[188:191],0
ex v203,v203
mm v[216:219],a[72:75],v[184:187],v[216:219]
cv v229,v202,v203
mm v[216:219],a[68:71],v[180:183],v[216:219]
ex v196,v196
mm v[216:219],a[64:67],v[176:179],v[216:219]
ld a[0:3],v255,s[52:55],0 offen offset:0 sc1
mm v[212:215],a[80:83],v[160:163],0
ex v197,v197
mm v[212:215],a[84:87],v[164:167],v[212:215]
cv v226,v196,v197
s_add_u32 s17, s17, s80
mm v[212:215],a[88:91],v[168:171],v[212:215]
ex v198,v198
mm v[212:215],a[92:95],v[172:175],v[212:215]
s_add_u32 m0, m0, 16384
s_and_b32 m0, m0, s78
ld a[4:7],v255,s[52:55],0 offen offset:1024 sc1
mm v[220:223],a[92:95],v[188:191],0
ex v199,v199
mm v[220:223],a[88:91],v[184:187],v[220:223]
cv v227,v198,v199
mm v[220:223],a[84:87],v[180:183],v[220:223]
ex v204,v204
mm v[220:223],a[80:83],v[176:179],v[220:223]
wt vmcnt(12)
s_barrier
mm v[64:67],a[96:99],v[232:235],v[64:67]
ex v205,v205
mm v[96:99],a[96:99],v[236:239],v[96:99]
rd a[96:99],v248 offset:8192
cv v230,v204,v205
mm v[100:103],a[100:103],v[236:239],v[100:103]
ex v206,v206
mm v[68:71],a[100:103],v[232:235],v[68:71]
rd a[100:103],v248 offset:9216
ld a[8:11],v255,s[52:55],0 offen offset:2048 sc1
mm v[72:75],a[104:107],v[232:235],v[72:75]
ex v207,v207
mm v[104:107],a[104:107],v[236:239],v[104:107]
rd a[104:107],v248 offset:10240
cv v231,v206,v207
mm v[108:111],a[108:111],v[236:239],v[108:111]
ex v208,v208
mm v[76:79],a[108:111],v[232:235],v[76:79]
rd a[108:111],v248 offset:11264
ld a[12:15],v255,s[52:55],0 offen offset:3072 sc1
mm v[80:83],a[112:115],v[232:235],v[80:83]
ex v209,v209
mm v[112:115],a[112:115],v[236:239],v[112:115]
rd a[112:115],v248 offset:12288
mm v[116:119],a[116:119],v[236:239],v[116:119]
ex v210,v210
mm v[84:87],a[116:119],v[232:235],v[84:87]
rd a[116:119],v248 offset:13312
ld a[16:19],v255,s[52:55],s95 offen offset:0 sc1
mm v[88:91],a[120:123],v[232:235],v[88:91]
ex v211,v211
mm v[120:123],a[120:123],v[236:239],v[120:123]
rd a[120:123],v248 offset:14336
s_bitcmp1_b64 s[98:99], s85
mm v[124:127],a[124:127],v[236:239],v[124:127]
ex v216,v216
mm v[92:95],a[124:127],v[232:235],v[92:95]
rd a[124:127],v248 offset:15360
v_or_b32 v248, s89, v255
v_smfmac_f32_16x16x64_bf16 a[196:199], a[208:211], v[232:239], v241
wt lgkmcnt(4)
v_smfmac_f32_16x16x64_bf16 a[192:195], a[208:211], v[224:231], v241
rd a[64:67],v248 offset:0
mm v[0:3],a[96:99],v[224:227],v[0:3]
ex v217,v217
mm v[32:35],a[96:99],v[228:231],v[32:35]
rd a[68:71],v248 offset:1024
cv v232,v208,v209
mm v[36:39],a[100:103],v[228:231],v[36:39]
ex v218,v218
mm v[4:7],a[100:103],v[224:227],v[4:7]
rd a[72:75],v248 offset:2048
cv v233,v210,v211
mm v[8:11],a[104:107],v[224:227],v[8:11]
ex v219,v219
mm v[40:43],a[104:107],v[228:231],v[40:43]
rd a[76:79],v248 offset:3072
cv v236,v216,v217
mm v[44:47],a[108:111],v[228:231],v[44:47]
ex v212,v212
mm v[12:15],a[108:111],v[224:227],v[12:15]
rd a[80:83],v248 offset:4096
wt lgkmcnt(5)
mm v[16:19],a[112:115],v[224:227],v[16:19]
ex v213,v213
mm v[48:51],a[112:115],v[228:231],v[48:51]
rd a[84:87],v248 offset:5120
cv v237,v218,v219
mm v[52:55],a[116:119],v[228:231],v[52:55]
ex v214,v214
mm v[20:23],a[116:119],v[224:227],v[20:23]
rd a[88:91],v248 offset:6144
cv v234,v212,v213
mm v[24:27],a[120:123],v[224:227],v[24:27]
ex v215,v215
mm v[56:59],a[120:123],v[228:231],v[56:59]
rd a[92:95],v248 offset:7168
cv v235,v214,v215
mm v[60:63],a[124:127],v[228:231],v[60:63]
ex v220,v220
mm v[28:31],a[124:127],v[224:227],v[28:31]
ld a[20:23],v255,s[52:55],s95 offen offset:1024 sc1
wt lgkmcnt(4)
mm v[192:195],a[64:67],v[128:131],0
ex v221,v221
mm v[192:195],a[68:71],v[132:135],v[192:195]
cv v238,v220,v221
s_add_u32 s85, s85, 1
mm v[192:195],a[72:75],v[136:139],v[192:195]
ex v222,v222
mm v[192:195],a[76:79],v[140:143],v[192:195]
ld v255,s[60:63],s17 offen offset:0 sc1 lds
mm v[200:203],a[76:79],v[156:159],0
ex v223,v223
mm v[200:203],a[72:75],v[152:155],v[200:203]
cv v239,v222,v223
s_add_u32 s89, s89, 16384
mm v[200:203],a[68:71],v[148:151],v[200:203]
ex v192,v192
mm v[200:203],a[64:67],v[144:147],v[200:203]
wt lgkmcnt(0)
s_and_b32 s89, s89, 0x1ffff
ld a[24:27],v255,s[52:55],s95 offen offset:2048 sc1
mm v[196:199],a[80:83],v[128:131],0
ex v193,v193
mm v[196:199],a[84:87],v[132:135],v[196:199]
cv v224,v192,v193
mm v[196:199],a[88:91],v[136:139],v[196:199]
ex v194,v194
mm v[196:199],a[92:95],v[140:143],v[196:199]
ld v255,s[60:63],s17 offen offset:1024 sc1 lds
mm v[204:207],a[92:95],v[156:159],0
ex v195,v195
mm v[204:207],a[88:91],v[152:155],v[204:207]
cv v225,v194,v195
mm v[204:207],a[84:87],v[148:151],v[204:207]
ex v200,v200
mm v[204:207],a[80:83],v[144:147],v[204:207]
ld v255,s[60:63],s17 offen offset:2048 sc1 lds
mm v[208:211],a[64:67],v[160:163],0
ex v201,v201
mm v[208:211],a[68:71],v[164:167],v[208:211]
cv v228,v200,v201
mm v[208:211],a[72:75],v[168:171],v[208:211]
ex v202,v202
mm v[208:211],a[76:79],v[172:175],v[208:211]
ld v255,s[60:63],s17 offen offset:3072 sc1 lds
mm v[216:219],a[76:79],v[188:191],0
ex v203,v203
mm v[216:219],a[72:75],v[184:187],v[216:219]
cv v229,v202,v203
mm v[216:219],a[68:71],v[180:183],v[216:219]
ex v196,v196
mm v[216:219],a[64:67],v[176:179],v[216:219]
ld a[28:31],v255,s[52:55],s95 offen offset:3072 sc1
mm v[212:215],a[80:83],v[160:163],0
ex v197,v197
mm v[212:215],a[84:87],v[164:167],v[212:215]
cv v226,v196,v197
s_add_u32 s17, s17, s80
mm v[212:215],a[88:91],v[168:171],v[212:215]
ex v198,v198
mm v[212:215],a[92:95],v[172:175],v[212:215]
s_add_u32 m0, m0, 16384
s_and_b32 m0, m0, s78
ld a[32:35],v255,s[52:55],s96 offen offset:0 sc1
mm v[220:223],a[92:95],v[188:191],0
ex v199,v199
mm v[220:223],a[88:91],v[184:187],v[220:223]
cv v227,v198,v199
mm v[220:223],a[84:87],v[180:183],v[220:223]
ex v204,v204
mm v[220:223],a[80:83],v[176:179],v[220:223]
wt vmcnt(18)
s_barrier
mm v[64:67],a[96:99],v[232:235],v[64:67]
ex v205,v205
mm v[96:99],a[96:99],v[236:239],v[96:99]
rd a[96:99],v248 offset:8192
cv v230,v204,v205
mm v[100:103],a[100:103],v[236:239],v[100:103]
ex v206,v206
mm v[68:71],a[100:103],v[232:235],v[68:71]
rd a[100:103],v248 offset:9216
ld a[36:39],v255,s[52:55],s96 offen offset:1024 sc1
mm v[72:75],a[104:107],v[232:235],v[72:75]
ex v207,v207
mm v[104:107],a[104:107],v[236:239],v[104:107]
rd a[104:107],v248 offset:10240
cv v231,v206,v207
mm v[108:111],a[108:111],v[236:239],v[108:111]
ex v208,v208
mm v[76:79],a[108:111],v[232:235],v[76:79]
rd a[108:111],v248 offset:11264
ld a[40:43],v255,s[52:55],s96 offen offset:2048 sc1
mm v[80:83],a[112:115],v[232:235],v[80:83]
ex v209,v209
mm v[112:115],a[112:115],v[236:239],v[112:115]
rd a[112:115],v248 offset:12288
mm v[116:119],a[116:119],v[236:239],v[116:119]
ex v210,v210
mm v[84:87],a[116:119],v[232:235],v[84:87]
rd a[116:119],v248 offset:13312
ld a[44:47],v255,s[52:55],s96 offen offset:3072 sc1
mm v[88:91],a[120:123],v[232:235],v[88:91]
ex v211,v211
mm v[120:123],a[120:123],v[236:239],v[120:123]
rd a[120:123],v248 offset:14336
s_bitcmp1_b64 s[98:99], s85
mm v[124:127],a[124:127],v[236:239],v[124:127]
ex v216,v216
mm v[92:95],a[124:127],v[232:235],v[92:95]
rd a[124:127],v248 offset:15360
v_or_b32 v248, s89, v255
v_smfmac_f32_16x16x64_bf16 a[196:199], a[208:211], v[232:239], v241
wt lgkmcnt(4)
v_smfmac_f32_16x16x64_bf16 a[192:195], a[208:211], v[224:231], v241
rd a[64:67],v248 offset:0
mm v[0:3],a[96:99],v[224:227],v[0:3]
ex v217,v217
mm v[32:35],a[96:99],v[228:231],v[32:35]
rd a[68:71],v248 offset:1024
cv v232,v208,v209
mm v[36:39],a[100:103],v[228:231],v[36:39]
ex v218,v218
mm v[4:7],a[100:103],v[224:227],v[4:7]
rd a[72:75],v248 offset:2048
cv v233,v210,v211
mm v[8:11],a[104:107],v[224:227],v[8:11]
ex v219,v219
mm v[40:43],a[104:107],v[228:231],v[40:43]
rd a[76:79],v248 offset:3072
cv v236,v216,v217
mm v[44:47],a[108:111],v[228:231],v[44:47]
ex v212,v212
mm v[12:15],a[108:111],v[224:227],v[12:15]
rd a[80:83],v248 offset:4096
wt lgkmcnt(5)
mm v[16:19],a[112:115],v[224:227],v[16:19]
ex v213,v213
mm v[48:51],a[112:115],v[228:231],v[48:51]
rd a[84:87],v248 offset:5120
cv v237,v218,v219
mm v[52:55],a[116:119],v[228:231],v[52:55]
ex v214,v214
mm v[20:23],a[116:119],v[224:227],v[20:23]
rd a[88:91],v248 offset:6144
cv v234,v212,v213
mm v[24:27],a[120:123],v[224:227],v[24:27]
ex v215,v215
mm v[56:59],a[120:123],v[228:231],v[56:59]
rd a[92:95],v248 offset:7168
cv v235,v214,v215
mm v[60:63],a[124:127],v[228:231],v[60:63]
ex v220,v220
mm v[28:31],a[124:127],v[224:227],v[28:31]
ld a[48:51],v255,s[52:55],s97 offen offset:0 sc1
wt lgkmcnt(4)
mm v[192:195],a[64:67],v[128:131],0
ex v221,v221
mm v[192:195],a[68:71],v[132:135],v[192:195]
cv v238,v220,v221
s_add_u32 s85, s85, 1
mm v[192:195],a[72:75],v[136:139],v[192:195]
ex v222,v222
mm v[192:195],a[76:79],v[140:143],v[192:195]
ld v255,s[60:63],s17 offen offset:0 sc1 lds
mm v[200:203],a[76:79],v[156:159],0
ex v223,v223
mm v[200:203],a[72:75],v[152:155],v[200:203]
cv v239,v222,v223
s_add_u32 s89, s89, 16384
mm v[200:203],a[68:71],v[148:151],v[200:203]
ex v192,v192
mm v[200:203],a[64:67],v[144:147],v[200:203]
wt lgkmcnt(0)
s_and_b32 s89, s89, 0x1ffff
ld a[52:55],v255,s[52:55],s97 offen offset:1024 sc1
mm v[196:199],a[80:83],v[128:131],0
ex v193,v193
mm v[196:199],a[84:87],v[132:135],v[196:199]
cv v224,v192,v193
mm v[196:199],a[88:91],v[136:139],v[196:199]
ex v194,v194
mm v[196:199],a[92:95],v[140:143],v[196:199]
ld v255,s[60:63],s17 offen offset:1024 sc1 lds
mm v[204:207],a[92:95],v[156:159],0
ex v195,v195
mm v[204:207],a[88:91],v[152:155],v[204:207]
cv v225,v194,v195
mm v[204:207],a[84:87],v[148:151],v[204:207]
ex v200,v200
mm v[204:207],a[80:83],v[144:147],v[204:207]
ld v255,s[60:63],s17 offen offset:2048 sc1 lds
mm v[208:211],a[64:67],v[160:163],0
ex v201,v201
mm v[208:211],a[68:71],v[164:167],v[208:211]
cv v228,v200,v201
mm v[208:211],a[72:75],v[168:171],v[208:211]
ex v202,v202
mm v[208:211],a[76:79],v[172:175],v[208:211]
ld v255,s[60:63],s17 offen offset:3072 sc1 lds
mm v[216:219],a[76:79],v[188:191],0
ex v203,v203
mm v[216:219],a[72:75],v[184:187],v[216:219]
cv v229,v202,v203
mm v[216:219],a[68:71],v[180:183],v[216:219]
ex v196,v196
mm v[216:219],a[64:67],v[176:179],v[216:219]
ld a[56:59],v255,s[52:55],s97 offen offset:2048 sc1
mm v[212:215],a[80:83],v[160:163],0
ex v197,v197
mm v[212:215],a[84:87],v[164:167],v[212:215]
cv v226,v196,v197
s_add_u32 s17, s17, s80
mm v[212:215],a[88:91],v[168:171],v[212:215]
ex v198,v198
mm v[212:215],a[92:95],v[172:175],v[212:215]
s_add_u32 m0, m0, 16384
s_and_b32 m0, m0, s78
ld a[60:63],v255,s[52:55],s97 offen offset:3072 sc1
mm v[220:223],a[92:95],v[188:191],0
ex v199,v199
mm v[220:223],a[88:91],v[184:187],v[220:223]
cv v227,v198,v199
mm v[220:223],a[84:87],v[180:183],v[220:223]
ex v204,v204
mm v[220:223],a[80:83],v[176:179],v[220:223]
wt vmcnt(24)
s_barrier
mm v[64:67],a[96:99],v[232:235],v[64:67]
ex v205,v205
mm v[96:99],a[96:99],v[236:239],v[96:99]
rd a[96:99],v248 offset:8192
cv v230,v204,v205
mm v[100:103],a[100:103],v[236:239],v[100:103]
ex v206,v206
mm v[68:71],a[100:103],v[232:235],v[68:71]
rd a[100:103],v248 offset:9216
mm v[72:75],a[104:107],v[232:235],v[72:75]
ex v207,v207
mm v[104:107],a[104:107],v[236:239],v[104:107]
rd a[104:107],v248 offset:10240
cv v231,v206,v207
mm v[108:111],a[108:111],v[236:239],v[108:111]
ex v208,v208
mm v[76:79],a[108:111],v[232:235],v[76:79]
rd a[108:111],v248 offset:11264
mm v[80:83],a[112:115],v[232:235],v[80:83]
ex v209,v209
mm v[112:115],a[112:115],v[236:239],v[112:115]
rd a[112:115],v248 offset:12288
mm v[116:119],a[116:119],v[236:239],v[116:119]
ex v210,v210
mm v[84:87],a[116:119],v[232:235],v[84:87]
rd a[116:119],v248 offset:13312
mm v[88:91],a[120:123],v[232:235],v[88:91]
ex v211,v211
mm v[120:123],a[120:123],v[236:239],v[120:123]
rd a[120:123],v248 offset:14336
s_bitcmp1_b64 s[98:99], s85
mm v[124:127],a[124:127],v[236:239],v[124:127]
ex v216,v216
mm v[92:95],a[124:127],v[232:235],v[92:95]
rd a[124:127],v248 offset:15360
v_or_b32 v248, s89, v255
v_smfmac_f32_16x16x64_bf16 a[196:199], a[208:211], v[232:239], v241
wt lgkmcnt(4)
v_smfmac_f32_16x16x64_bf16 a[192:195], a[208:211], v[224:231], v241
rd a[64:67],v248 offset:0
mm v[0:3],a[96:99],v[224:227],v[0:3]
ex v217,v217
mm v[32:35],a[96:99],v[228:231],v[32:35]
rd a[68:71],v248 offset:1024
cv v232,v208,v209
mm v[36:39],a[100:103],v[228:231],v[36:39]
ex v218,v218
mm v[4:7],a[100:103],v[224:227],v[4:7]
rd a[72:75],v248 offset:2048
cv v233,v210,v211
mm v[8:11],a[104:107],v[224:227],v[8:11]
ex v219,v219
mm v[40:43],a[104:107],v[228:231],v[40:43]
rd a[76:79],v248 offset:3072
cv v236,v216,v217
mm v[44:47],a[108:111],v[228:231],v[44:47]
ex v212,v212
mm v[12:15],a[108:111],v[224:227],v[12:15]
rd a[80:83],v248 offset:4096
wt lgkmcnt(5)
mm v[16:19],a[112:115],v[224:227],v[16:19]
ex v213,v213
mm v[48:51],a[112:115],v[228:231],v[48:51]
rd a[84:87],v248 offset:5120
cv v237,v218,v219
mm v[52:55],a[116:119],v[228:231],v[52:55]
ex v214,v214
mm v[20:23],a[116:119],v[224:227],v[20:23]
rd a[88:91],v248 offset:6144
cv v234,v212,v213
mm v[24:27],a[120:123],v[224:227],v[24:27]
ex v215,v215
mm v[56:59],a[120:123],v[228:231],v[56:59]
rd a[92:95],v248 offset:7168
cv v235,v214,v215
mm v[60:63],a[124:127],v[228:231],v[60:63]
ex v220,v220
mm v[28:31],a[124:127],v[224:227],v[28:31]
; ---- helper: own block finished: l is checked (.La5_LA_SLOW if outside [2^-60, 2^60] or NaN), O / l leaves; the partner-s rows start
.La5_LA_F1Y:
.La5_LW6_1:
s_cbranch_execz .La5_LW6_5
wt lgkmcnt(4)
wt vmcnt(0)
mm v[192:195],a[64:67],a[0:3],0
ex v221,v221
mm v[192:195],a[68:71],a[4:7],v[192:195]
cv v238,v220,v221
s_add_u32 s89, s89, 16384
mm v[192:195],a[72:75],a[8:11],v[192:195]
ex v222,v222
mm v[192:195],a[76:79],a[12:15],v[192:195]
ld v255,s[60:63],s17 offen offset:0 sc1 lds
ar v242,a192
ar v240,a193
v_add_f32 v242, v242, v240
mm v[200:203],a[76:79],a[28:31],0
ex v223,v223
ar v243,a194
ar v240,a195
v_add_f32 v243, v243, v240
mm v[200:203],a[72:75],a[24:27],v[200:203]
cv v239,v222,v223
s_and_b32 s89, s89, 0x1ffff
aw a192,0
aw a193,0
aw a194,0
mm v[200:203],a[68:71],a[20:23],v[200:203]
ex v192,v192
aw a195,0
v_subrev_u32 v240, 562036736, v242
mm v[200:203],a[64:67],a[16:19],v[200:203]
wt lgkmcnt(0)
v_rcp_f32 v242, v242
v_cmp_le_u32 vcc, 1006632960, v240
v_subrev_u32 v240, 562036736, v243
mm v[196:199],a[80:83],a[0:3],0
ex v193,v193
s_mov_b64 s[92:93], vcc
v_cmp_le_u32 vcc, 1006632960, v240
mm v[196:199],a[84:87],a[4:7],v[196:199]
cv v224,v192,v193
v_rcp_f32_e64 v243, v243
mm v[196:199],a[88:91],a[8:11],v[196:199]
v_exp_f32_e64 v194, v194
s_or_b64 vcc, vcc, s[92:93]
s_cbranch_scc1 .La5_LA_SC1
.La5_LA_SR1:
v_mul_f32 v0, v0, v242
mm v[196:199],a[92:95],a[12:15],v[196:199]
ld v255,s[60:63],s17 offen offset:1024 sc1 lds
v_mul_f32 v1, v1, v242
v_mul_f32 v2, v2, v242
v_mul_f32 v3, v3, v242
mm v[204:207],a[92:95],a[28:31],0
ex v195,v195
v_mul_f32 v4, v4, v242
v_mul_f32 v5, v5, v242
v_mul_f32 v6, v6, v242
mm v[204:207],a[88:91],a[24:27],v[204:207]
cv v225,v194,v195
v_mul_f32 v7, v7, v242
cv v128,v0,v1
cv v129,v2,v3
cv v130,v4,v5
mm v[204:207],a[84:87],a[20:23],v[204:207]
ex v200,v200
cv v131,v6,v7
v_mul_f32 v8, v8, v242
v_mul_f32 v9, v9, v242
mm v[204:207],a[80:83],a[16:19],v[204:207]
ld v255,s[60:63],s17 offen offset:2048 sc1 lds
v_mul_f32 v10, v10, v242
v_mul_f32 v11, v11, v242
v_mul_f32 v12, v12, v242
mm v[208:211],a[64:67],a[32:35],0
ex v201,v201
v_mul_f32 v13, v13, v242
v_mul_f32 v14, v14, v242
v_mul_f32 v15, v15, v242
mm v[208:211],a[68:71],a[36:39],v[208:211]
cv v228,v200,v201
cv v132,v8,v9
cv v133,v10,v11
cv v134,v12,v13
cv v135,v14,v15
mm v[208:211],a[72:75],a[40:43],v[208:211]
ex v202,v202
v_mul_f32 v16, v16, v242
v_mul_f32 v17, v17, v242
v_mul_f32 v18, v18, v242
mm v[208:211],a[76:79],a[44:47],v[208:211]
ld v255,s[60:63],s17 offen offset:3072 sc1 lds
v_mul_f32 v19, v19, v242
v_mul_f32 v20, v20, v242
v_mul_f32 v21, v21, v242
mm v[216:219],a[76:79],a[60:63],0
ex v203,v203
v_mul_f32 v22, v22, v242
v_mul_f32 v23, v23, v242
cv v136,v16,v17
mm v[216:219],a[72:75],a[56:59],v[216:219]
cv v229,v202,v203
cv v137,v18,v19
cv v138,v20,v21
cv v139,v22,v23
v_mul_f32 v24, v24, v242
mm v[216:219],a[68:71],a[52:55],v[216:219]
ex v196,v196
v_mul_f32 v25, v25, v242
v_mul_f32 v26, v26, v242
v_mul_f32 v27, v27, v242
mm v[216:219],a[64:67],a[48:51],v[216:219]
v_mul_f32 v28, v28, v242
v_mul_f32 v29, v29, v242
v_mul_f32 v30, v30, v242
v_mul_f32 v31, v31, v242
cv v140,v24,v25
mm v[212:215],a[80:83],a[32:35],0
ex v197,v197
s_add_u32 s17, s17, s80
cv v141,v26,v27
cv v142,v28,v29
mm v[212:215],a[84:87],a[36:39],v[212:215]
cv v226,v196,v197
s_add_u32 m0, m0, 16384
s_and_b32 m0, m0, s78
cv v143,v30,v31
v_mul_f32 v32, v32, v243
mm v[212:215],a[88:91],a[40:43],v[212:215]
ex v198,v198
v_mul_f32 v33, v33, v243
v_mul_f32 v34, v34, v243
v_mul_f32 v35, v35, v243
mm v[212:215],a[92:95],a[44:47],v[212:215]
st v[128:131],v244,s[44:47],s91 offen offset:0 sc1
v_mul_f32 v36, v36, v243
v_mul_f32 v37, v37, v243
v_mul_f32 v38, v38, v243
mm v[220:223],a[92:95],a[60:63],0
ex v199,v199
v_mul_f32 v39, v39, v243
cv v144,v32,v33
cv v145,v34,v35
mm v[220:223],a[88:91],a[56:59],v[220:223]
cv v227,v198,v199
cv v146,v36,v37
cv v147,v38,v39
v_mul_f32 v40, v40, v243
v_mul_f32 v41, v41, v243
mm v[220:223],a[84:87],a[52:55],v[220:223]
ex v204,v204
v_mul_f32 v42, v42, v243
v_mul_f32 v43, v43, v243
v_mul_f32 v44, v44, v243
mm v[220:223],a[80:83],a[48:51],v[220:223]
s_nop 0
s_barrier
v_mul_f32 v45, v45, v243
v_mul_f32 v46, v46, v243
v_mul_f32 v47, v47, v243
mm v[64:67],a[96:99],v[232:235],v[64:67]
ex v205,v205
cv v148,v40,v41
cv v149,v42,v43
cv v150,v44,v45
mm v[96:99],a[96:99],v[236:239],v[96:99]
.La5_LW6_2:
s_cbranch_execz .La5_LW6_6
rd a[96:99],v248 offset:8192
cv v230,v204,v205
cv v151,v46,v47
v_mul_f32 v48, v48, v243
v_mul_f32 v49, v49, v243
mm v[100:103],a[100:103],v[236:239],v[100:103]
ex v206,v206
v_mul_f32 v50, v50, v243
v_mul_f32 v51, v51, v243
v_mul_f32 v52, v52, v243
mm v[68:71],a[100:103],v[232:235],v[68:71]
rd a[100:103],v248 offset:9216
st v[132:135],v244,s[44:47],s91 offen offset:1024 sc1
v_mul_f32 v53, v53, v243
v_mul_f32 v54, v54, v243
mm v[72:75],a[104:107],v[232:235],v[72:75]
ex v207,v207
v_mul_f32 v55, v55, v243
cv v152,v48,v49
cv v153,v50,v51
mm v[104:107],a[104:107],v[236:239],v[104:107]
rd a[104:107],v248 offset:10240
cv v231,v206,v207
cv v154,v52,v53
cv v155,v54,v55
v_mul_f32 v56, v56, v243
mm v[108:111],a[108:111],v[236:239],v[108:111]
ex v208,v208
v_mul_f32 v57, v57, v243
v_mul_f32 v58, v58, v243
v_mul_f32 v59, v59, v243
mm v[76:79],a[108:111],v[232:235],v[76:79]
rd a[108:111],v248 offset:11264
v_mul_f32 v60, v60, v243
v_mul_f32 v61, v61, v243
v_mul_f32 v62, v62, v243
v_mul_f32 v63, v63, v243
mm v[80:83],a[112:115],v[232:235],v[80:83]
ex v209,v209
cv v156,v56,v57
cv v157,v58,v59
cv v158,v60,v61
mm v[112:115],a[112:115],v[236:239],v[112:115]
rd a[112:115],v248 offset:12288
st v[136:139],v244,s[44:47],s91 offen offset:2048 sc1
cv v159,v62,v63
mm v[116:119],a[116:119],v[236:239],v[116:119]
ex v210,v210
mm v[84:87],a[116:119],v[232:235],v[84:87]
rd a[116:119],v248 offset:13312
mm v[88:91],a[120:123],v[232:235],v[88:91]
ex v211,v211
mm v[120:123],a[120:123],v[236:239],v[120:123]
rd a[120:123],v248 offset:14336
st v[140:143],v244,s[44:47],s91 offen offset:3072 sc1
mm v[124:127],a[124:127],v[236:239],v[124:127]
ex v216,v216
mm v[92:95],a[124:127],v[232:235],v[92:95]
rd a[124:127],v248 offset:15360
v_or_b32 v248, s89, v255
v_smfmac_f32_16x16x64_bf16 a[196:199], a[208:211], v[232:239], v241
wt lgkmcnt(4)
v_smfmac_f32_16x16x64_bf16 a[192:195], a[208:211], v[224:231], v241
rd a[64:67],v248 offset:0
mm v[0:3],a[96:99],v[224:227],0
ex v217,v217
mm v[32:35],a[96:99],v[228:231],0
rd a[68:71],v248 offset:1024
cv v232,v208,v209
ar v242,a196
ar v240,a197
v_add_f32 v242, v242, v240
mm v[36:39],a[100:103],v[228:231],0
ex v218,v218
ar v243,a198
ar v240,a199
v_add_f32 v243, v243, v240
mm v[4:7],a[100:103],v[224:227],0
rd a[72:75],v248 offset:2048
cv v233,v210,v211
st v[144:147],v245,s[44:47],s91 offen offset:0 sc1
aw a196,0
mm v[8:11],a[104:107],v[224:227],0
ex v219,v219
aw a197,0
aw a198,0
aw a199,0
mm v[40:43],a[104:107],v[228:231],0
rd a[76:79],v248 offset:3072
cv v236,v216,v217
v_subrev_u32 v240, 562036736, v242
v_rcp_f32 v242, v242
mm v[44:47],a[108:111],v[228:231],0
ex v212,v212
v_cmp_le_u32 vcc, 1006632960, v240
v_subrev_u32 v240, 562036736, v243
s_mov_b64 s[92:93], vcc
mm v[12:15],a[108:111],v[224:227],0
rd a[80:83],v248 offset:4096
wt lgkmcnt(5)
v_cmp_le_u32 vcc, 1006632960, v240
v_rcp_f32_e64 v243, v243
mm v[16:19],a[112:115],v[224:227],0
v_exp_f32_e64 v213, v213
s_or_b64 vcc, vcc, s[92:93]
s_cbranch_scc1 .La5_LA_SC2
.La5_LA_SR2:
v_mul_f32 v64, v64, v242
mm v[48:51],a[112:115],v[228:231],0
rd a[84:87],v248 offset:5120
cv v237,v218,v219
v_mul_f32 v65, v65, v242
v_mul_f32 v66, v66, v242
v_mul_f32 v67, v67, v242
mm v[52:55],a[116:119],v[228:231],0
ex v214,v214
v_mul_f32 v68, v68, v242
v_mul_f32 v69, v69, v242
v_mul_f32 v70, v70, v242
mm v[20:23],a[116:119],v[224:227],0
rd a[88:91],v248 offset:6144
cv v234,v212,v213
st v[148:151],v245,s[44:47],s91 offen offset:1024 sc1
v_mul_f32 v71, v71, v242
mm v[24:27],a[120:123],v[224:227],0
ex v215,v215
cv v160,v64,v65
cv v161,v66,v67
cv v162,v68,v69
mm v[56:59],a[120:123],v[228:231],0
rd a[92:95],v248 offset:7168
cv v235,v214,v215
cv v163,v70,v71
v_mul_f32 v72, v72, v242
v_mul_f32 v73, v73, v242
mm v[60:63],a[124:127],v[228:231],0
ex v220,v220
v_mul_f32 v74, v74, v242
v_mul_f32 v75, v75, v242
v_mul_f32 v76, v76, v242
mm v[28:31],a[124:127],v[224:227],0
s_add_u32 s85, s85, 1
v_mul_f32 v77, v77, v242
v_mul_f32 v78, v78, v242
v_mul_f32 v79, v79, v242
cv v164,v72,v73
wt lgkmcnt(4)
mm v[192:195],a[64:67],a[0:3],0
ex v221,v221
cv v165,v74,v75
cv v166,v76,v77
cv v167,v78,v79
mm v[192:195],a[68:71],a[4:7],v[192:195]
cv v238,v220,v221
s_add_u32 s89, s89, 16384
v_mul_f32 v80, v80, v242
v_mul_f32 v81, v81, v242
v_mul_f32 v82, v82, v242
mm v[192:195],a[72:75],a[8:11],v[192:195]
ex v222,v222
v_mul_f32 v83, v83, v242
v_mul_f32 v84, v84, v242
v_mul_f32 v85, v85, v242
mm v[192:195],a[76:79],a[12:15],v[192:195]
ld v255,s[60:63],s17 offen offset:0 sc1 lds
v_mul_f32 v86, v86, v242
v_mul_f32 v87, v87, v242
cv v168,v80,v81
mm v[200:203],a[76:79],a[28:31],0
ex v223,v223
cv v169,v82,v83
cv v170,v84,v85
cv v171,v86,v87
mm v[200:203],a[72:75],a[24:27],v[200:203]
cv v239,v222,v223
s_and_b32 s89, s89, 0x1ffff
v_mul_f32 v88, v88, v242
v_mul_f32 v89, v89, v242
v_mul_f32 v90, v90, v242
mm v[200:203],a[68:71],a[20:23],v[200:203]
ex v192,v192
v_mul_f32 v91, v91, v242
v_mul_f32 v92, v92, v242
v_mul_f32 v93, v93, v242
mm v[200:203],a[64:67],a[16:19],v[200:203]
wt lgkmcnt(0)
st v[152:155],v245,s[44:47],s91 offen offset:2048 sc1
v_mul_f32 v94, v94, v242
v_mul_f32 v95, v95, v242
mm v[196:199],a[80:83],a[0:3],0
ex v193,v193
cv v172,v88,v89
cv v173,v90,v91
cv v174,v92,v93
mm v[196:199],a[84:87],a[4:7],v[196:199]
cv v224,v192,v193
cv v175,v94,v95
v_mul_f32 v96, v96, v243
v_mul_f32 v97, v97, v243
v_mul_f32 v98, v98, v243
mm v[196:199],a[88:91],a[8:11],v[196:199]
ex v194,v194
v_mul_f32 v99, v99, v243
v_mul_f32 v100, v100, v243
v_mul_f32 v101, v101, v243
mm v[196:199],a[92:95],a[12:15],v[196:199]
ld v255,s[60:63],s17 offen offset:1024 sc1 lds
v_mul_f32 v102, v102, v243
v_mul_f32 v103, v103, v243
cv v176,v96,v97
mm v[204:207],a[92:95],a[28:31],0
ex v195,v195
cv v177,v98,v99
cv v178,v100,v101
cv v179,v102,v103
mm v[204:207],a[88:91],a[24:27],v[204:207]
cv v225,v194,v195
st v[156:159],v245,s[44:47],s91 offen offset:3072 sc1
v_mul_f32 v104, v104, v243
v_mul_f32 v105, v105, v243
mm v[204:207],a[84:87],a[20:23],v[204:207]
ex v200,v200
v_mul_f32 v106, v106, v243
v_mul_f32 v107, v107, v243
v_mul_f32 v108, v108, v243
mm v[204:207],a[80:83],a[16:19],v[204:207]
ld v255,s[60:63],s17 offen offset:2048 sc1 lds
v_mul_f32 v109, v109, v243
v_mul_f32 v110, v110, v243
v_mul_f32 v111, v111, v243
mm v[208:211],a[64:67],a[32:35],0
ex v201,v201
cv v180,v104,v105
cv v181,v106,v107
cv v182,v108,v109
mm v[208:211],a[68:71],a[36:39],v[208:211]
cv v228,v200,v201
cv v183,v110,v111
v_mul_f32 v112, v112, v243
v_mul_f32 v113, v113, v243
v_mul_f32 v114, v114, v243
mm v[208:211],a[72:75],a[40:43],v[208:211]
ex v202,v202
v_mul_f32 v115, v115, v243
v_mul_f32 v116, v116, v243
v_mul_f32 v117, v117, v243
mm v[208:211],a[76:79],a[44:47],v[208:211]
ld v255,s[60:63],s17 offen offset:3072 sc1 lds
v_mul_f32 v118, v118, v243
v_mul_f32 v119, v119, v243
cv v184,v112,v113
mm v[216:219],a[76:79],a[60:63],0
ex v203,v203
cv v185,v114,v115
cv v186,v116,v117
cv v187,v118,v119
mm v[216:219],a[72:75],a[56:59],v[216:219]
cv v229,v202,v203
v_mul_f32 v120, v120, v243
v_mul_f32 v121, v121, v243
v_mul_f32 v122, v122, v243
v_mul_f32 v123, v123, v243
mm v[216:219],a[68:71],a[52:55],v[216:219]
ex v196,v196
v_mul_f32 v124, v124, v243
v_mul_f32 v125, v125, v243
v_mul_f32 v126, v126, v243
mm v[216:219],a[64:67],a[48:51],v[216:219]
v_mul_f32 v127, v127, v243
cv v188,v120,v121
cv v189,v122,v123
cv v190,v124,v125
cv v191,v126,v127
mm v[212:215],a[80:83],a[32:35],0
ex v197,v197
s_add_u32 s17, s17, s80
mm v[212:215],a[84:87],a[36:39],v[212:215]
cv v226,v196,v197
s_add_u32 m0, m0, 16384
s_and_b32 m0, m0, s78
mm v[212:215],a[88:91],a[40:43],v[212:215]
ex v198,v198
mm v[212:215],a[92:95],a[44:47],v[212:215]
st v[160:163],v246,s[44:47],s91 offen offset:0 sc1
mm v[220:223],a[92:95],a[60:63],0
ex v199,v199
mm v[220:223],a[88:91],a[56:59],v[220:223]
cv v227,v198,v199
mm v[220:223],a[84:87],a[52:55],v[220:223]
ex v204,v204
mm v[220:223],a[80:83],a[48:51],v[220:223]
wt vmcnt(18)
s_barrier
mm v[64:67],a[96:99],v[232:235],0
ex v205,v205
mm v[96:99],a[96:99],v[236:239],0
rd a[96:99],v248 offset:8192
cv v230,v204,v205
mm v[100:103],a[100:103],v[236:239],0
ex v206,v206
mm v[68:71],a[100:103],v[232:235],0
rd a[100:103],v248 offset:9216
st v[164:167],v246,s[44:47],s91 offen offset:1024 sc1
mm v[72:75],a[104:107],v[232:235],0
ex v207,v207
mm v[104:107],a[104:107],v[236:239],0
rd a[104:107],v248 offset:10240
cv v231,v206,v207
mm v[108:111],a[108:111],v[236:239],0
ex v208,v208
mm v[76:79],a[108:111],v[232:235],0
rd a[108:111],v248 offset:11264
st v[168:171],v246,s[44:47],s91 offen offset:2048 sc1
mm v[80:83],a[112:115],v[232:235],0
ex v209,v209
mm v[112:115],a[112:115],v[236:239],0
rd a[112:115],v248 offset:12288
mm v[116:119],a[116:119],v[236:239],0
ex v210,v210
mm v[84:87],a[116:119],v[232:235],0
rd a[116:119],v248 offset:13312
st v[172:175],v246,s[44:47],s91 offen offset:3072 sc1
mm v[88:91],a[120:123],v[232:235],0
ex v211,v211
mm v[120:123],a[120:123],v[236:239],0
rd a[120:123],v248 offset:14336
mm v[124:127],a[124:127],v[236:239],0
ex v216,v216
mm v[92:95],a[124:127],v[232:235],0
rd a[124:127],v248 offset:15360
v_or_b32 v248, s89, v255
st v[176:179],v247,s[44:47],s91 offen offset:0 sc1
v_smfmac_f32_16x16x64_bf16 a[196:199], a[208:211], v[232:239], v241
wt lgkmcnt(4)
v_smfmac_f32_16x16x64_bf16 a[192:195], a[208:211], v[224:231], v241
rd a[64:67],v248 offset:0
mm v[0:3],a[96:99],v[224:227],v[0:3]
ex v217,v217
mm v[32:35],a[96:99],v[228:231],v[32:35]
rd a[68:71],v248 offset:1024
cv v232,v208,v209
mm v[36:39],a[100:103],v[228:231],v[36:39]
ex v218,v218
mm v[4:7],a[100:103],v[224:227],v[4:7]
rd a[72:75],v248 offset:2048
cv v233,v210,v211
st v[180:183],v247,s[44:47],s91 offen offset:1024 sc1
mm v[8:11],a[104:107],v[224:227],v[8:11]
ex v219,v219
mm v[40:43],a[104:107],v[228:231],v[40:43]
rd a[76:79],v248 offset:3072
cv v236,v216,v217
mm v[44:47],a[108:111],v[228:231],v[44:47]
ex v212,v212
mm v[12:15],a[108:111],v[224:227],v[12:15]
rd a[80:83],v248 offset:4096
wt lgkmcnt(5)
mm v[16:19],a[112:115],v[224:227],v[16:19]
ex v213,v213
mm v[48:51],a[112:115],v[228:231],v[48:51]
rd a[84:87],v248 offset:5120
cv v237,v218,v219
st v[184:187],v247,s[44:47],s91 offen offset:2048 sc1
mm v[52:55],a[116:119],v[228:231],v[52:55]
ex v214,v214
mm v[20:23],a[116:119],v[224:227],v[20:23]
rd a[88:91],v248 offset:6144
cv v234,v212,v213
mm v[24:27],a[120:123],v[224:227],v[24:27]
ex v215,v215
mm v[56:59],a[120:123],v[228:231],v[56:59]
rd a[92:95],v248 offset:7168
cv v235,v214,v215
st v[188:191],v247,s[44:47],s91 offen offset:3072 sc1
mm v[60:63],a[124:127],v[228:231],v[60:63]
ex v220,v220
mm v[28:31],a[124:127],v[224:227],v[28:31]
s_add_u32 s85, s85, 1
s_cmp_eq_u32 s85, 22
s_cbranch_scc1 .La5_LA_F3
.p2align 6
.La5_LA_F2:
wt lgkmcnt(4)
mm v[192:195],a[64:67],a[0:3],0
ex v221,v221
mm v[192:195],a[68:71],a[4:7],v[192:195]
cv v238,v220,v221
s_add_u32 s85, s85, 1
mm v[192:195],a[72:75],a[8:11],v[192:195]
ex v222,v222
mm v[192:195],a[76:79],a[12:15],v[192:195]
ld v255,s[60:63],s17 offen offset:0 sc1 lds
mm v[200:203],a[76:79],a[28:31],0
ex v223,v223
mm v[200:203],a[72:75],a[24:27],v[200:203]
cv v239,v222,v223
s_add_u32 s89, s89, 16384
mm v[200:203],a[68:71],a[20:23],v[200:203]
ex v192,v192
mm v[200:203],a[64:67],a[16:19],v[200:203]
wt lgkmcnt(0)
s_and_b32 s89, s89, 0x1ffff
mm v[196:199],a[80:83],a[0:3],0
ex v193,v193
mm v[196:199],a[84:87],a[4:7],v[196:199]
cv v224,v192,v193
mm v[196:199],a[88:91],a[8:11],v[196:199]
ex v194,v194
mm v[196:199],a[92:95],a[12:15],v[196:199]
ld v255,s[60:63],s17 offen offset:1024 sc1 lds
mm v[204:207],a[92:95],a[28:31],0
ex v195,v195
mm v[204:207],a[88:91],a[24:27],v[204:207]
cv v225,v194,v195
mm v[204:207],a[84:87],a[20:23],v[204:207]
ex v200,v200
mm v[204:207],a[80:83],a[16:19],v[204:207]
ld v255,s[60:63],s17 offen offset:2048 sc1 lds
mm v[208:211],a[64:67],a[32:35],0
ex v201,v201
mm v[208:211],a[68:71],a[36:39],v[208:211]
cv v228,v200,v201
mm v[208:211],a[72:75],a[40:43],v[208:211]
ex v202,v202
mm v[208:211],a[76:79],a[44:47],v[208:211]
ld v255,s[60:63],s17 offen offset:3072 sc1 lds
mm v[216:219],a[76:79],a[60:63],0
ex v203,v203
mm v[216:219],a[72:75],a[56:59],v[216:219]
cv v229,v202,v203
mm v[216:219],a[68:71],a[52:55],v[216:219]
ex v196,v196
mm v[216:219],a[64:67],a[48:51],v[216:219]
buffer_load_dword a253, v254, s[36:39], s90 offen
mm v[212:215],a[80:83],a[32:35],0
ex v197,v197
mm v[212:215],a[84:87],a[36:39],v[212:215]
cv v226,v196,v197
s_add_u32 s90, s90, s21
mm v[212:215],a[88:91],a[40:43],v[212:215]
ex v198,v198
mm v[212:215],a[92:95],a[44:47],v[212:215]
s_add_u32 s17, s17, s80
s_add_u32 m0, m0, 16384
mm v[220:223],a[92:95],a[60:63],0
ex v199,v199
mm v[220:223],a[88:91],a[56:59],v[220:223]
cv v227,v198,v199
s_and_b32 m0, m0, s78
mm v[220:223],a[84:87],a[52:55],v[220:223]
ex v204,v204
mm v[220:223],a[80:83],a[48:51],v[220:223]
wt vmcnt(11)
s_barrier
mm v[64:67],a[96:99],v[232:235],v[64:67]
ex v205,v205
mm v[96:99],a[96:99],v[236:239],v[96:99]
rd a[96:99],v248 offset:8192
cv v230,v204,v205
mm v[100:103],a[100:103],v[236:239],v[100:103]
ex v206,v206
mm v[68:71],a[100:103],v[232:235],v[68:71]
rd a[100:103],v248 offset:9216
mm v[72:75],a[104:107],v[232:235],v[72:75]
ex v207,v207
mm v[104:107],a[104:107],v[236:239],v[104:107]
rd a[104:107],v248 offset:10240
cv v231,v206,v207
mm v[108:111],a[108:111],v[236:239],v[108:111]
ex v208,v208
mm v[76:79],a[108:111],v[232:235],v[76:79]
rd a[108:111],v248 offset:11264
mm v[80:83],a[112:115],v[232:235],v[80:83]
ex v209,v209
mm v[112:115],a[112:115],v[236:239],v[112:115]
rd a[112:115],v248 offset:12288
mm v[116:119],a[116:119],v[236:239],v[116:119]
ex v210,v210
mm v[84:87],a[116:119],v[232:235],v[84:87]
rd a[116:119],v248 offset:13312
mm v[88:91],a[120:123],v[232:235],v[88:91]
ex v211,v211
mm v[120:123],a[120:123],v[236:239],v[120:123]
rd a[120:123],v248 offset:14336
s_bitcmp1_b64 s[98:99], s85
mm v[124:127],a[124:127],v[236:239],v[124:127]
ex v216,v216
mm v[92:95],a[124:127],v[232:235],v[92:95]
rd a[124:127],v248 offset:15360
v_or_b32 v248, s89, v255
v_smfmac_f32_16x16x64_bf16 a[196:199], a[208:211], v[232:239], v241
wt lgkmcnt(4)
v_smfmac_f32_16x16x64_bf16 a[192:195], a[208:211], v[224:231], v241
rd a[64:67],v248 offset:0
mm v[0:3],a[96:99],v[224:227],v[0:3]
ex v217,v217
mm v[32:35],a[96:99],v[228:231],v[32:35]
rd a[68:71],v248 offset:1024
cv v232,v208,v209
mm v[36:39],a[100:103],v[228:231],v[36:39]
ex v218,v218
mm v[4:7],a[100:103],v[224:227],v[4:7]
rd a[72:75],v248 offset:2048
cv v233,v210,v211
mm v[8:11],a[104:107],v[224:227],v[8:11]
ex v219,v219
mm v[40:43],a[104:107],v[228:231],v[40:43]
rd a[76:79],v248 offset:3072
cv v236,v216,v217
mm v[44:47],a[108:111],v[228:231],v[44:47]
ex v212,v212
mm v[12:15],a[108:111],v[224:227],v[12:15]
rd a[80:83],v248 offset:4096
wt lgkmcnt(5)
mm v[16:19],a[112:115],v[224:227],v[16:19]
ex v213,v213
mm v[48:51],a[112:115],v[228:231],v[48:51]
rd a[84:87],v248 offset:5120
cv v237,v218,v219
mm v[52:55],a[116:119],v[228:231],v[52:55]
ex v214,v214
mm v[20:23],a[116:119],v[224:227],v[20:23]
rd a[88:91],v248 offset:6144
cv v234,v212,v213
mm v[24:27],a[120:123],v[224:227],v[24:27]
ex v215,v215
mm v[56:59],a[120:123],v[228:231],v[56:59]
rd a[92:95],v248 offset:7168
cv v235,v214,v215
mm v[60:63],a[124:127],v[228:231],v[60:63]
ex v220,v220
mm v[28:31],a[124:127],v[224:227],v[28:31]
s_cbranch_scc1 .La5_LA_F2E2
wt lgkmcnt(4)
mm v[192:195],a[64:67],a[0:3],0
ex v221,v221
mm v[192:195],a[68:71],a[4:7],v[192:195]
cv v238,v220,v221
s_add_u32 s85, s85, 1
mm v[192:195],a[72:75],a[8:11],v[192:195]
ex v222,v222
mm v[192:195],a[76:79],a[12:15],v[192:195]
ld v255,s[60:63],s17 offen offset:0 sc1 lds
mm v[200:203],a[76:79],a[28:31],0
ex v223,v223
mm v[200:203],a[72:75],a[24:27],v[200:203]
cv v239,v222,v223
s_add_u32 s89, s89, 16384
mm v[200:203],a[68:71],a[20:23],v[200:203]
ex v192,v192
mm v[200:203],a[64:67],a[16:19],v[200:203]
wt lgkmcnt(0)
s_and_b32 s89, s89, 0x1ffff
mm v[196:199],a[80:83],a[0:3],0
ex v193,v193
mm v[196:199],a[84:87],a[4:7],v[196:199]
cv v224,v192,v193
mm v[196:199],a[88:91],a[8:11],v[196:199]
ex v194,v194
mm v[196:199],a[92:95],a[12:15],v[196:199]
ld v255,s[60:63],s17 offen offset:1024 sc1 lds
mm v[204:207],a[92:95],a[28:31],0
ex v195,v195
mm v[204:207],a[88:91],a[24:27],v[204:207]
cv v225,v194,v195
mm v[204:207],a[84:87],a[20:23],v[204:207]
ex v200,v200
mm v[204:207],a[80:83],a[16:19],v[204:207]
ld v255,s[60:63],s17 offen offset:2048 sc1 lds
mm v[208:211],a[64:67],a[32:35],0
ex v201,v201
mm v[208:211],a[68:71],a[36:39],v[208:211]
cv v228,v200,v201
mm v[208:211],a[72:75],a[40:43],v[208:211]
ex v202,v202
mm v[208:211],a[76:79],a[44:47],v[208:211]
ld v255,s[60:63],s17 offen offset:3072 sc1 lds
mm v[216:219],a[76:79],a[60:63],0
ex v203,v203
mm v[216:219],a[72:75],a[56:59],v[216:219]
cv v229,v202,v203
mm v[216:219],a[68:71],a[52:55],v[216:219]
ex v196,v196
mm v[216:219],a[64:67],a[48:51],v[216:219]
buffer_load_dword a253, v254, s[36:39], s90 offen
mm v[212:215],a[80:83],a[32:35],0
ex v197,v197
mm v[212:215],a[84:87],a[36:39],v[212:215]
cv v226,v196,v197
s_add_u32 s90, s90, s21
mm v[212:215],a[88:91],a[40:43],v[212:215]
ex v198,v198
mm v[212:215],a[92:95],a[44:47],v[212:215]
s_add_u32 s17, s17, s80
s_add_u32 m0, m0, 16384
mm v[220:223],a[92:95],a[60:63],0
ex v199,v199
mm v[220:223],a[88:91],a[56:59],v[220:223]
cv v227,v198,v199
s_and_b32 m0, m0, s78
mm v[220:223],a[84:87],a[52:55],v[220:223]
ex v204,v204
mm v[220:223],a[80:83],a[48:51],v[220:223]
wt vmcnt(11)
s_barrier
mm v[64:67],a[96:99],v[232:235],v[64:67]
ex v205,v205
mm v[96:99],a[96:99],v[236:239],v[96:99]
rd a[96:99],v248 offset:8192
cv v230,v204,v205
mm v[100:103],a[100:103],v[236:239],v[100:103]
ex v206,v206
mm v[68:71],a[100:103],v[232:235],v[68:71]
rd a[100:103],v248 offset:9216
mm v[72:75],a[104:107],v[232:235],v[72:75]
ex v207,v207
mm v[104:107],a[104:107],v[236:239],v[104:107]
rd a[104:107],v248 offset:10240
.La5_LW6_3:
s_cbranch_execz .La5_LW6_7
cv v231,v206,v207
mm v[108:111],a[108:111],v[236:239],v[108:111]
ex v208,v208
mm v[76:79],a[108:111],v[232:235],v[76:79]
rd a[108:111],v248 offset:11264
mm v[80:83],a[112:115],v[232:235],v[80:83]
ex v209,v209
mm v[112:115],a[112:115],v[236:239],v[112:115]
rd a[112:115],v248 offset:12288
mm v[116:119],a[116:119],v[236:239],v[116:119]
ex v210,v210
mm v[84:87],a[116:119],v[232:235],v[84:87]
rd a[116:119],v248 offset:13312
mm v[88:91],a[120:123],v[232:235],v[88:91]
ex v211,v211
mm v[120:123],a[120:123],v[236:239],v[120:123]
rd a[120:123],v248 offset:14336
s_bitcmp1_b64 s[98:99], s85
mm v[124:127],a[124:127],v[236:239],v[124:127]
ex v216,v216
mm v[92:95],a[124:127],v[232:235],v[92:95]
rd a[124:127],v248 offset:15360
v_or_b32 v248, s89, v255
v_smfmac_f32_16x16x64_bf16 a[196:199], a[208:211], v[232:239], v241
wt lgkmcnt(4)
v_smfmac_f32_16x16x64_bf16 a[192:195], a[208:211], v[224:231], v241
rd a[64:67],v248 offset:0
mm v[0:3],a[96:99],v[224:227],v[0:3]
ex v217,v217
mm v[32:35],a[96:99],v[228:231],v[32:35]
rd a[68:71],v248 offset:1024
cv v232,v208,v209
mm v[36:39],a[100:103],v[228:231],v[36:39]
ex v218,v218
mm v[4:7],a[100:103],v[224:227],v[4:7]
rd a[72:75],v248 offset:2048
cv v233,v210,v211
mm v[8:11],a[104:107],v[224:227],v[8:11]
ex v219,v219
mm v[40:43],a[104:107],v[228:231],v[40:43]
rd a[76:79],v248 offset:3072
cv v236,v216,v217
mm v[44:47],a[108:111],v[228:231],v[44:47]
ex v212,v212
mm v[12:15],a[108:111],v[224:227],v[12:15]
rd a[80:83],v248 offset:4096
wt lgkmcnt(5)
mm v[16:19],a[112:115],v[224:227],v[16:19]
ex v213,v213
mm v[48:51],a[112:115],v[228:231],v[48:51]
rd a[84:87],v248 offset:5120
cv v237,v218,v219
mm v[52:55],a[116:119],v[228:231],v[52:55]
ex v214,v214
mm v[20:23],a[116:119],v[224:227],v[20:23]
rd a[88:91],v248 offset:6144
cv v234,v212,v213
mm v[24:27],a[120:123],v[224:227],v[24:27]
ex v215,v215
mm v[56:59],a[120:123],v[228:231],v[56:59]
rd a[92:95],v248 offset:7168
cv v235,v214,v215
mm v[60:63],a[124:127],v[228:231],v[60:63]
ex v220,v220
mm v[28:31],a[124:127],v[224:227],v[28:31]
s_cbranch_scc0 .La5_LA_F2
.La5_LA_F2E2:
; ---- helper: last windows; FFN weights and the first weights of the O projection come in
.La5_LA_F3:
.La5_LW6_4:
s_cbranch_execz .La5_LW6_8
wt lgkmcnt(4)
mm v[192:195],a[64:67],a[0:3],0
ex v221,v221
mm v[192:195],a[68:71],a[4:7],v[192:195]
cv v238,v220,v221
s_add_u32 s89, s89, 16384
mm v[192:195],a[72:75],a[8:11],v[192:195]
ex v222,v222
mm v[192:195],a[76:79],a[12:15],v[192:195]
ld v255,s[60:63],s17 offen offset:0 sc1 lds
mm v[200:203],a[76:79],a[28:31],0
ex v223,v223
mm v[200:203],a[72:75],a[24:27],v[200:203]
cv v239,v222,v223
s_and_b32 s89, s89, 0x1ffff
mm v[200:203],a[68:71],a[20:23],v[200:203]
ex v192,v192
mm v[200:203],a[64:67],a[16:19],v[200:203]
wt lgkmcnt(0)
mm v[196:199],a[80:83],a[0:3],0
ex v193,v193
mm v[196:199],a[84:87],a[4:7],v[196:199]
cv v224,v192,v193
ar v243,a229
mm v[196:199],a[88:91],a[8:11],v[196:199]
ex v194,v194
mm v[196:199],a[92:95],a[12:15],v[196:199]
ld v255,s[60:63],s17 offen offset:1024 sc1 lds
mm v[204:207],a[92:95],a[28:31],0
ex v195,v195
mm v[204:207],a[88:91],a[24:27],v[204:207]
cv v225,v194,v195
mm v[204:207],a[84:87],a[20:23],v[204:207]
ex v200,v200
mm v[204:207],a[80:83],a[16:19],v[204:207]
ld v255,s[60:63],s17 offen offset:2048 sc1 lds
mm v[208:211],a[64:67],a[32:35],0
ex v201,v201
mm v[208:211],a[68:71],a[36:39],v[208:211]
cv v228,v200,v201
mm v[208:211],a[72:75],a[40:43],v[208:211]
ex v202,v202
mm v[208:211],a[76:79],a[44:47],v[208:211]
ld v255,s[60:63],s17 offen offset:3072 sc1 lds
mm v[216:219],a[76:79],a[60:63],0
ex v203,v203
mm v[216:219],a[72:75],a[56:59],v[216:219]
cv v229,v202,v203
s_mov_b32 s93, 0
mm v[216:219],a[68:71],a[52:55],v[216:219]
ex v196,v196
mm v[216:219],a[64:67],a[48:51],v[216:219]
ld a[128:131],v243,s[40:43],s93 offen
mm v[212:215],a[80:83],a[32:35],0
ex v197,v197
s_add_u32 s17, s17, s80
mm v[212:215],a[84:87],a[36:39],v[212:215]
cv v226,v196,v197
s_mov_b32 s93, 7168
s_add_u32 m0, m0, 16384
mm v[212:215],a[88:91],a[40:43],v[212:215]
ex v198,v198
s_and_b32 m0, m0, s78
mm v[212:215],a[92:95],a[44:47],v[212:215]
ld a[132:135],v243,s[40:43],s93 offen
mm v[220:223],a[92:95],a[60:63],0
ex v199,v199
mm v[220:223],a[88:91],a[56:59],v[220:223]
cv v227,v198,v199
mm v[220:223],a[84:87],a[52:55],v[220:223]
ex v204,v204
mm v[220:223],a[80:83],a[48:51],v[220:223]
wt vmcnt(12)
s_barrier
mm v[64:67],a[96:99],v[232:235],v[64:67]
ex v205,v205
mm v[96:99],a[96:99],v[236:239],v[96:99]
rd a[96:99],v248 offset:8192
cv v230,v204,v205
mm v[100:103],a[100:103],v[236:239],v[100:103]
ex v206,v206
mm v[68:71],a[100:103],v[232:235],v[68:71]
rd a[100:103],v248 offset:9216
mm v[72:75],a[104:107],v[232:235],v[72:75]
ex v207,v207
mm v[104:107],a[104:107],v[236:239],v[104:107]
rd a[104:107],v248 offset:10240
cv v231,v206,v207
s_mov_b32 s93, 14336
mm v[108:111],a[108:111],v[236:239],v[108:111]
ex v208,v208
mm v[76:79],a[108:111],v[232:235],v[76:79]
rd a[108:111],v248 offset:11264
ld a[136:139],v243,s[40:43],s93 offen
mm v[80:83],a[112:115],v[232:235],v[80:83]
ex v209,v209
mm v[112:115],a[112:115],v[236:239],v[112:115]
rd a[112:115],v248 offset:12288
mm v[116:119],a[116:119],v[236:239],v[116:119]
ex v210,v210
mm v[84:87],a[116:119],v[232:235],v[84:87]
rd a[116:119],v248 offset:13312
mm v[88:91],a[120:123],v[232:235],v[88:91]
ex v211,v211
mm v[120:123],a[120:123],v[236:239],v[120:123]
rd a[120:123],v248 offset:14336
mm v[124:127],a[124:127],v[236:239],v[124:127]
ex v216,v216
mm v[92:95],a[124:127],v[232:235],v[92:95]
rd a[124:127],v248 offset:15360
v_or_b32 v248, s89, v255
v_smfmac_f32_16x16x64_bf16 a[196:199], a[208:211], v[232:239], v241
wt lgkmcnt(4)
v_smfmac_f32_16x16x64_bf16 a[192:195], a[208:211], v[224:231], v241
rd a[64:67],v248 offset:0
mm v[0:3],a[96:99],v[224:227],v[0:3]
ex v217,v217
mm v[32:35],a[96:99],v[228:231],v[32:35]
rd a[68:71],v248 offset:1024
cv v232,v208,v209
mm v[36:39],a[100:103],v[228:231],v[36:39]
ex v218,v218
mm v[4:7],a[100:103],v[224:227],v[4:7]
rd a[72:75],v248 offset:2048
cv v233,v210,v211
mm v[8:11],a[104:107],v[224:227],v[8:11]
ex v219,v219
mm v[40:43],a[104:107],v[228:231],v[40:43]
rd a[76:79],v248 offset:3072
cv v236,v216,v217
mm v[44:47],a[108:111],v[228:231],v[44:47]
ex v212,v212
mm v[12:15],a[108:111],v[224:227],v[12:15]
rd a[80:83],v248 offset:4096
wt lgkmcnt(5)
mm v[16:19],a[112:115],v[224:227],v[16:19]
ex v213,v213
mm v[48:51],a[112:115],v[228:231],v[48:51]
rd a[84:87],v248 offset:5120
cv v237,v218,v219
mm v[52:55],a[116:119],v[228:231],v[52:55]
ex v214,v214
mm v[20:23],a[116:119],v[224:227],v[20:23]
rd a[88:91],v248 offset:6144
cv v234,v212,v213
mm v[24:27],a[120:123],v[224:227],v[24:27]
ex v215,v215
mm v[56:59],a[120:123],v[228:231],v[56:59]
rd a[92:95],v248 offset:7168
cv v235,v214,v215
mm v[60:63],a[124:127],v[228:231],v[60:63]
ex v220,v220
mm v[28:31],a[124:127],v[224:227],v[28:31]
wt lgkmcnt(4)
mm v[192:195],a[64:67],a[0:3],0
ex v221,v221
mm v[192:195],a[68:71],a[4:7],v[192:195]
cv v238,v220,v221
s_add_u32 s89, s89, 16384
mm v[192:195],a[72:75],a[8:11],v[192:195]
ex v222,v222
mm v[192:195],a[76:79],a[12:15],v[192:195]
ld v255,s[60:63],s17 offen offset:0 sc1 lds
mm v[200:203],a[76:79],a[28:31],0
ex v223,v223
mm v[200:203],a[72:75],a[24:27],v[200:203]
cv v239,v222,v223
s_and_b32 s89, s89, 0x1ffff
mm v[200:203],a[68:71],a[20:23],v[200:203]
ex v192,v192
mm v[200:203],a[64:67],a[16:19],v[200:203]
wt lgkmcnt(0)
mm v[196:199],a[80:83],a[0:3],0
ex v193,v193
mm v[196:199],a[84:87],a[4:7],v[196:199]
cv v224,v192,v193
mm v[196:199],a[88:91],a[8:11],v[196:199]
ex v194,v194
mm v[196:199],a[92:95],a[12:15],v[196:199]
ld v255,s[60:63],s17 offen offset:1024 sc1 lds
mm v[204:207],a[92:95],a[28:31],0
ex v195,v195
mm v[204:207],a[88:91],a[24:27],v[204:207]
cv v225,v194,v195
mm v[204:207],a[84:87],a[20:23],v[204:207]
ex v200,v200
mm v[204:207],a[80:83],a[16:19],v[204:207]
ld v255,s[60:63],s17 offen offset:2048 sc1 lds
mm v[208:211],a[64:67],a[32:35],0
ex v201,v201
mm v[208:211],a[68:71],a[36:39],v[208:211]
cv v228,v200,v201
mm v[208:211],a[72:75],a[40:43],v[208:211]
ex v202,v202
mm v[208:211],a[76:79],a[44:47],v[208:211]
ld v255,s[60:63],s17 offen offset:3072 sc1 lds
mm v[216:219],a[76:79],a[60:63],0
ex v203,v203
mm v[216:219],a[72:75],a[56:59],v[216:219]
cv v229,v202,v203
s_mov_b32 s93, 21504
mm v[216:219],a[68:71],a[52:55],v[216:219]
ex v196,v196
mm v[216:219],a[64:67],a[48:51],v[216:219]
ld a[140:143],v243,s[40:43],s93 offen
mm v[212:215],a[80:83],a[32:35],0
ex v197,v197
s_add_u32 s17, s17, s80
mm v[212:215],a[84:87],a[36:39],v[212:215]
cv v226,v196,v197
s_mov_b32 s93, 28672
s_add_u32 m0, m0, 16384
mm v[212:215],a[88:91],a[40:43],v[212:215]
ex v198,v198
s_and_b32 m0, m0, s78
mm v[212:215],a[92:95],a[44:47],v[212:215]
ld a[144:147],v243,s[40:43],s93 offen
mm v[220:223],a[92:95],a[60:63],0
ex v199,v199
mm v[220:223],a[88:91],a[56:59],v[220:223]
cv v227,v198,v199
mm v[220:223],a[84:87],a[52:55],v[220:223]
ex v204,v204
mm v[220:223],a[80:83],a[48:51],v[220:223]
wt vmcnt(14)
s_barrier
mm v[64:67],a[96:99],v[232:235],v[64:67]
ex v205,v205
mm v[96:99],a[96:99],v[236:239],v[96:99]
rd a[96:99],v248 offset:8192
cv v230,v204,v205
mm v[100:103],a[100:103],v[236:239],v[100:103]
ex v206,v206
mm v[68:71],a[100:103],v[232:235],v[68:71]
rd a[100:103],v248 offset:9216
mm v[72:75],a[104:107],v[232:235],v[72:75]
ex v207,v207
mm v[104:107],a[104:107],v[236:239],v[104:107]
rd a[104:107],v248 offset:10240
cv v231,v206,v207
s_mov_b32 s93, 35840
mm v[108:111],a[108:111],v[236:239],v[108:111]
ex v208,v208
mm v[76:79],a[108:111],v[232:235],v[76:79]
rd a[108:111],v248 offset:11264
ld a[148:151],v243,s[40:43],s93 offen
mm v[80:83],a[112:115],v[232:235],v[80:83]
ex v209,v209
mm v[112:115],a[112:115],v[236:239],v[112:115]
rd a[112:115],v248 offset:12288
mm v[116:119],a[116:119],v[236:239],v[116:119]
ex v210,v210
mm v[84:87],a[116:119],v[232:235],v[84:87]
rd a[116:119],v248 offset:13312
mm v[88:91],a[120:123],v[232:235],v[88:91]
ex v211,v211
mm v[120:123],a[120:123],v[236:239],v[120:123]
rd a[120:123],v248 offset:14336
ar v242,a231
wt vmcnt(14)
mm v[124:127],a[124:127],v[236:239],v[124:127]
ex v216,v216
mm v[92:95],a[124:127],v[232:235],v[92:95]
rd a[124:127],v248 offset:15360
v_or_b32 v248, s89, v255
v_smfmac_f32_16x16x64_bf16 a[196:199], a[208:211], v[232:239], v241
wt lgkmcnt(4)
v_smfmac_f32_16x16x64_bf16 a[192:195], a[208:211], v[224:231], v241
rd a[64:67],v248 offset:0
mm v[0:3],a[96:99],v[224:227],v[0:3]
ex v217,v217
mm v[32:35],a[96:99],v[228:231],v[32:35]
rd a[68:71],v248 offset:1024
cv v232,v208,v209
mm v[36:39],a[100:103],v[228:231],v[36:39]
ex v218,v218
mm v[4:7],a[100:103],v[224:227],v[4:7]
rd a[72:75],v248 offset:2048
cv v233,v210,v211
mm v[8:11],a[104:107],v[224:227],v[8:11]
ex v219,v219
mm v[40:43],a[104:107],v[228:231],v[40:43]
rd a[76:79],v248 offset:3072
cv v236,v216,v217
mm v[44:47],a[108:111],v[228:231],v[44:47]
ex v212,v212
mm v[12:15],a[108:111],v[224:227],v[12:15]
rd a[80:83],v248 offset:4096
wt lgkmcnt(5)
mm v[16:19],a[112:115],v[224:227],v[16:19]
ex v213,v213
mm v[48:51],a[112:115],v[228:231],v[48:51]
rd a[84:87],v248 offset:5120
cv v237,v218,v219
mm v[52:55],a[116:119],v[228:231],v[52:55]
ex v214,v214
mm v[20:23],a[116:119],v[224:227],v[20:23]
rd a[88:91],v248 offset:6144
cv v234,v212,v213
mm v[24:27],a[120:123],v[224:227],v[24:27]
ex v215,v215
mm v[56:59],a[120:123],v[228:231],v[56:59]
rd a[92:95],v248 offset:7168
cv v235,v214,v215
mm v[60:63],a[124:127],v[228:231],v[60:63]
ex v220,v220
mm v[28:31],a[124:127],v[224:227],v[28:31]
buffer_store_dwordx2 v[252:253], v242, s[64:67], s6 offen offset:0 sc1
wt lgkmcnt(4)
mm v[192:195],a[64:67],a[0:3],0
ex v221,v221
mm v[192:195],a[68:71],a[4:7],v[192:195]
cv v238,v220,v221
s_add_u32 s89, s89, 16384
mm v[192:195],a[72:75],a[8:11],v[192:195]
ex v222,v222
mm v[192:195],a[76:79],a[12:15],v[192:195]
ld v255,s[60:63],s17 offen offset:0 sc1 lds
mm v[200:203],a[76:79],a[28:31],0
ex v223,v223
mm v[200:203],a[72:75],a[24:27],v[200:203]
cv v239,v222,v223
s_and_b32 s89, s89, 0x1ffff
mm v[200:203],a[68:71],a[20:23],v[200:203]
ex v192,v192
mm v[200:203],a[64:67],a[16:19],v[200:203]
wt lgkmcnt(0)
mm v[196:199],a[80:83],a[0:3],0
ex v193,v193
mm v[196:199],a[84:87],a[4:7],v[196:199]
cv v224,v192,v193
mm v[196:199],a[88:91],a[8:11],v[196:199]
ex v194,v194
mm v[196:199],a[92:95],a[12:15],v[196:199]
ld v255,s[60:63],s17 offen offset:1024 sc1 lds
mm v[204:207],a[92:95],a[28:31],0
ex v195,v195
mm v[204:207],a[88:91],a[24:27],v[204:207]
cv v225,v194,v195
mm v[204:207],a[84:87],a[20:23],v[204:207]
ex v200,v200
mm v[204:207],a[80:83],a[16:19],v[204:207]
ld v255,s[60:63],s17 offen offset:2048 sc1 lds
mm v[208:211],a[64:67],a[32:35],0
ex v201,v201
mm v[208:211],a[68:71],a[36:39],v[208:211]
cv v228,v200,v201
mm v[208:211],a[72:75],a[40:43],v[208:211]
ex v202,v202
mm v[208:211],a[76:79],a[44:47],v[208:211]
ld v255,s[60:63],s17 offen offset:3072 sc1 lds
mm v[216:219],a[76:79],a[60:63],0
ex v203,v203
mm v[216:219],a[72:75],a[56:59],v[216:219]
cv v229,v202,v203
s_mov_b32 s93, 43008
mm v[216:219],a[68:71],a[52:55],v[216:219]
ex v196,v196
mm v[216:219],a[64:67],a[48:51],v[216:219]
ld a[152:155],v243,s[40:43],s93 offen
mm v[212:215],a[80:83],a[32:35],0
ex v197,v197
s_add_u32 s17, s17, s80
mm v[212:215],a[84:87],a[36:39],v[212:215]
cv v226,v196,v197
s_mov_b32 s93, 50176
s_add_u32 m0, m0, 16384
mm v[212:215],a[88:91],a[40:43],v[212:215]
ex v198,v198
s_and_b32 m0, m0, s78
mm v[212:215],a[92:95],a[44:47],v[212:215]
ld a[156:159],v243,s[40:43],s93 offen
mm v[220:223],a[92:95],a[60:63],0
ex v199,v199
mm v[220:223],a[88:91],a[56:59],v[220:223]
cv v227,v198,v199
mm v[220:223],a[84:87],a[52:55],v[220:223]
ex v204,v204
mm v[220:223],a[80:83],a[48:51],v[220:223]
wt vmcnt(17)
s_barrier
mm v[64:67],a[96:99],v[232:235],v[64:67]
ex v205,v205
mm v[96:99],a[96:99],v[236:239],v[96:99]
rd a[96:99],v248 offset:8192
cv v230,v204,v205
mm v[100:103],a[100:103],v[236:239],v[100:103]
ex v206,v206
mm v[68:71],a[100:103],v[232:235],v[68:71]
rd a[100:103],v248 offset:9216
mm v[72:75],a[104:107],v[232:235],v[72:75]
ex v207,v207
mm v[104:107],a[104:107],v[236:239],v[104:107]
rd a[104:107],v248 offset:10240
cv v231,v206,v207
s_mov_b32 s93, 57344
mm v[108:111],a[108:111],v[236:239],v[108:111]
ex v208,v208
mm v[76:79],a[108:111],v[232:235],v[76:79]
rd a[108:111],v248 offset:11264
ld a[160:163],v243,s[40:43],s93 offen
mm v[80:83],a[112:115],v[232:235],v[80:83]
ex v209,v209
mm v[112:115],a[112:115],v[236:239],v[112:115]
rd a[112:115],v248 offset:12288
mm v[116:119],a[116:119],v[236:239],v[116:119]
ex v210,v210
mm v[84:87],a[116:119],v[232:235],v[84:87]
rd a[116:119],v248 offset:13312
mm v[88:91],a[120:123],v[232:235],v[88:91]
ex v211,v211
mm v[120:123],a[120:123],v[236:239],v[120:123]
rd a[120:123],v248 offset:14336
mm v[124:127],a[124:127],v[236:239],v[124:127]
ex v216,v216
mm v[92:95],a[124:127],v[232:235],v[92:95]
rd a[124:127],v248 offset:15360
v_or_b32 v248, s89, v255
v_smfmac_f32_16x16x64_bf16 a[196:199], a[208:211], v[232:239], v241
wt lgkmcnt(4)
v_smfmac_f32_16x16x64_bf16 a[192:195], a[208:211], v[224:231], v241
rd a[64:67],v248 offset:0
mm v[0:3],a[96:99],v[224:227],v[0:3]
ex v217,v217
mm v[32:35],a[96:99],v[228:231],v[32:35]
rd a[68:71],v248 offset:1024
cv v232,v208,v209
mm v[36:39],a[100:103],v[228:231],v[36:39]
ex v218,v218
mm v[4:7],a[100:103],v[224:227],v[4:7]
rd a[72:75],v248 offset:2048
cv v233,v210,v211
mm v[8:11],a[104:107],v[224:227],v[8:11]
ex v219,v219
mm v[40:43],a[104:107],v[228:231],v[40:43]
rd a[76:79],v248 offset:3072
cv v236,v216,v217
mm v[44:47],a[108:111],v[228:231],v[44:47]
ex v212,v212
mm v[12:15],a[108:111],v[224:227],v[12:15]
rd a[80:83],v248 offset:4096
wt lgkmcnt(5)
mm v[16:19],a[112:115],v[224:227],v[16:19]
ex v213,v213
mm v[48:51],a[112:115],v[228:231],v[48:51]
rd a[84:87],v248 offset:5120
cv v237,v218,v219
mm v[52:55],a[116:119],v[228:231],v[52:55]
ex v214,v214
mm v[20:23],a[116:119],v[224:227],v[20:23]
rd a[88:91],v248 offset:6144
cv v234,v212,v213
mm v[24:27],a[120:123],v[224:227],v[24:27]
ex v215,v215
mm v[56:59],a[120:123],v[228:231],v[56:59]
rd a[92:95],v248 offset:7168
cv v235,v214,v215
mm v[60:63],a[124:127],v[228:231],v[60:63]
ex v220,v220
mm v[28:31],a[124:127],v[224:227],v[28:31]
wt lgkmcnt(4)
mm v[192:195],a[64:67],a[0:3],0
ex v221,v221
mm v[192:195],a[68:71],a[4:7],v[192:195]
cv v238,v220,v221
ar v240,a228
mm v[192:195],a[72:75],a[8:11],v[192:195]
ex v222,v222
mm v[192:195],a[76:79],a[12:15],v[192:195]
s_add_u32 m0, s83, 16896
s_lshl_b32 s17, s72, 18
mm v[200:203],a[76:79],a[28:31],0
ex v223,v223
mm v[200:203],a[72:75],a[24:27],v[200:203]
cv v239,v222,v223
s_add_u32 s89, s89, 16384
mm v[200:203],a[68:71],a[20:23],v[200:203]
ex v192,v192
mm v[200:203],a[64:67],a[16:19],v[200:203]
wt lgkmcnt(0)
s_and_b32 s89, s89, 0x1ffff
mm v[196:199],a[80:83],a[0:3],0
ex v193,v193
mm v[196:199],a[84:87],a[4:7],v[196:199]
cv v224,v192,v193
mm v[196:199],a[88:91],a[8:11],v[196:199]
ex v194,v194
mm v[196:199],a[92:95],a[12:15],v[196:199]
ld v240,s[56:59],s17 offen lds
mm v[204:207],a[92:95],a[28:31],0
ex v195,v195
s_add_u32 s17, s17, 32768
mm v[204:207],a[88:91],a[24:27],v[204:207]
cv v225,v194,v195
s_add_u32 m0, m0, 1024
mm v[204:207],a[84:87],a[20:23],v[204:207]
ex v200,v200
mm v[204:207],a[80:83],a[16:19],v[204:207]
ld v240,s[56:59],s17 offen lds
mm v[208:211],a[64:67],a[32:35],0
ex v201,v201
s_add_u32 s17, s17, 4294942720
mm v[208:211],a[68:71],a[36:39],v[208:211]
cv v228,v200,v201
s_add_u32 m0, m0, 1152
mm v[208:211],a[72:75],a[40:43],v[208:211]
ex v202,v202
mm v[208:211],a[76:79],a[44:47],v[208:211]
ld v240,s[56:59],s17 offen lds
mm v[216:219],a[76:79],a[60:63],0
ex v203,v203
s_add_u32 s17, s17, 32768
mm v[216:219],a[72:75],a[56:59],v[216:219]
cv v229,v202,v203
s_add_u32 m0, m0, 1024
mm v[216:219],a[68:71],a[52:55],v[216:219]
ex v196,v196
mm v[216:219],a[64:67],a[48:51],v[216:219]
ld v240,s[56:59],s17 offen lds
mm v[212:215],a[80:83],a[32:35],0
ex v197,v197
s_add_u32 s17, s17, 90112
mm v[212:215],a[84:87],a[36:39],v[212:215]
cv v226,v196,v197
s_mov_b32 s93, 64512
.La5_LW6_5:
s_cbranch_execz .La5_LW6_R0
s_add_u32 m0, s83, 33792
mm v[212:215],a[88:91],a[40:43],v[212:215]
ex v198,v198
mm v[212:215],a[92:95],a[44:47],v[212:215]
ld a[164:167],v243,s[40:43],s93 offen
mm v[220:223],a[92:95],a[60:63],0
ex v199,v199
mm v[220:223],a[88:91],a[56:59],v[220:223]
cv v227,v198,v199
mm v[220:223],a[84:87],a[52:55],v[220:223]
ex v204,v204
mm v[220:223],a[80:83],a[48:51],v[220:223]
wt vmcnt(16)
s_barrier
mm v[64:67],a[96:99],v[232:235],v[64:67]
ex v205,v205
mm v[96:99],a[96:99],v[236:239],v[96:99]
rd a[96:99],v248 offset:8192
cv v230,v204,v205
mm v[100:103],a[100:103],v[236:239],v[100:103]
ex v206,v206
mm v[68:71],a[100:103],v[232:235],v[68:71]
rd a[100:103],v248 offset:9216
mm v[72:75],a[104:107],v[232:235],v[72:75]
ex v207,v207
mm v[104:107],a[104:107],v[236:239],v[104:107]
rd a[104:107],v248 offset:10240
cv v231,v206,v207
s_mov_b32 s93, 71680
mm v[108:111],a[108:111],v[236:239],v[108:111]
ex v208,v208
mm v[76:79],a[108:111],v[232:235],v[76:79]
rd a[108:111],v248 offset:11264
ld a[168:171],v243,s[40:43],s93 offen
mm v[80:83],a[112:115],v[232:235],v[80:83]
ex v209,v209
mm v[112:115],a[112:115],v[236:239],v[112:115]
rd a[112:115],v248 offset:12288
s_mov_b32 s93, 78848
mm v[116:119],a[116:119],v[236:239],v[116:119]
ex v210,v210
mm v[84:87],a[116:119],v[232:235],v[84:87]
rd a[116:119],v248 offset:13312
ld a[172:175],v243,s[40:43],s93 offen
mm v[88:91],a[120:123],v[232:235],v[88:91]
ex v211,v211
mm v[120:123],a[120:123],v[236:239],v[120:123]
rd a[120:123],v248 offset:14336
mm v[124:127],a[124:127],v[236:239],v[124:127]
ex v216,v216
mm v[92:95],a[124:127],v[232:235],v[92:95]
rd a[124:127],v248 offset:15360
v_or_b32 v248, s89, v255
v_smfmac_f32_16x16x64_bf16 a[196:199], a[208:211], v[232:239], v241
wt lgkmcnt(4)
v_smfmac_f32_16x16x64_bf16 a[192:195], a[208:211], v[224:231], v241
rd a[64:67],v248 offset:0
mm v[0:3],a[96:99],v[224:227],v[0:3]
ex v217,v217
mm v[32:35],a[96:99],v[228:231],v[32:35]
rd a[68:71],v248 offset:1024
cv v232,v208,v209
mm v[36:39],a[100:103],v[228:231],v[36:39]
ex v218,v218
mm v[4:7],a[100:103],v[224:227],v[4:7]
rd a[72:75],v248 offset:2048
cv v233,v210,v211
mm v[8:11],a[104:107],v[224:227],v[8:11]
ex v219,v219
mm v[40:43],a[104:107],v[228:231],v[40:43]
rd a[76:79],v248 offset:3072
cv v236,v216,v217
mm v[44:47],a[108:111],v[228:231],v[44:47]
ex v212,v212
mm v[12:15],a[108:111],v[224:227],v[12:15]
rd a[80:83],v248 offset:4096
wt lgkmcnt(5)
mm v[16:19],a[112:115],v[224:227],v[16:19]
ex v213,v213
mm v[48:51],a[112:115],v[228:231],v[48:51]
rd a[84:87],v248 offset:5120
cv v237,v218,v219
mm v[52:55],a[116:119],v[228:231],v[52:55]
ex v214,v214
mm v[20:23],a[116:119],v[224:227],v[20:23]
rd a[88:91],v248 offset:6144
cv v234,v212,v213
mm v[24:27],a[120:123],v[224:227],v[24:27]
ex v215,v215
mm v[56:59],a[120:123],v[228:231],v[56:59]
rd a[92:95],v248 offset:7168
cv v235,v214,v215
mm v[60:63],a[124:127],v[228:231],v[60:63]
ex v220,v220
mm v[28:31],a[124:127],v[224:227],v[28:31]
wt lgkmcnt(4)
mm v[192:195],a[64:67],a[0:3],0
ex v221,v221
mm v[192:195],a[68:71],a[4:7],v[192:195]
cv v238,v220,v221
s_add_u32 s89, s89, 16384
mm v[192:195],a[72:75],a[8:11],v[192:195]
ex v222,v222
mm v[192:195],a[76:79],a[12:15],v[192:195]
ld v240,s[56:59],s17 offen lds
mm v[200:203],a[76:79],a[28:31],0
ex v223,v223
s_add_u32 s17, s17, 32768
mm v[200:203],a[72:75],a[24:27],v[200:203]
cv v239,v222,v223
s_add_u32 m0, m0, 1024
mm v[200:203],a[68:71],a[20:23],v[200:203]
ex v192,v192
mm v[200:203],a[64:67],a[16:19],v[200:203]
wt lgkmcnt(0)
s_and_b32 s89, s89, 0x1ffff
mm v[196:199],a[80:83],a[0:3],0
ex v193,v193
mm v[196:199],a[84:87],a[4:7],v[196:199]
cv v224,v192,v193
mm v[196:199],a[88:91],a[8:11],v[196:199]
ex v194,v194
mm v[196:199],a[92:95],a[12:15],v[196:199]
ld v240,s[56:59],s17 offen lds
mm v[204:207],a[92:95],a[28:31],0
ex v195,v195
s_add_u32 s17, s17, 4294942720
mm v[204:207],a[88:91],a[24:27],v[204:207]
cv v225,v194,v195
s_add_u32 m0, m0, 1152
mm v[204:207],a[84:87],a[20:23],v[204:207]
ex v200,v200
mm v[204:207],a[80:83],a[16:19],v[204:207]
ld v240,s[56:59],s17 offen lds
mm v[208:211],a[64:67],a[32:35],0
ex v201,v201
s_add_u32 s17, s17, 32768
mm v[208:211],a[68:71],a[36:39],v[208:211]
cv v228,v200,v201
s_add_u32 m0, m0, 1024
mm v[208:211],a[72:75],a[40:43],v[208:211]
ex v202,v202
mm v[208:211],a[76:79],a[44:47],v[208:211]
ld v240,s[56:59],s17 offen lds
mm v[216:219],a[76:79],a[60:63],0
ex v203,v203
mm v[216:219],a[72:75],a[56:59],v[216:219]
cv v229,v202,v203
s_mov_b32 s93, 86016
mm v[216:219],a[68:71],a[52:55],v[216:219]
ex v196,v196
mm v[216:219],a[64:67],a[48:51],v[216:219]
ld a[176:179],v243,s[40:43],s93 offen
mm v[212:215],a[80:83],a[32:35],0
ex v197,v197
s_bfe_u32 s17, s4, 262148
mm v[212:215],a[84:87],a[36:39],v[212:215]
cv v226,v196,v197
s_mov_b32 s93, 93184
s_xor_b32 s17, s17, s72
mm v[212:215],a[88:91],a[40:43],v[212:215]
ex v198,v198
s_lshl_b32 s17, s17, 18
mm v[212:215],a[92:95],a[44:47],v[212:215]
ld a[180:183],v243,s[40:43],s93 offen
s_add_u32 m0, s83, 50688
mm v[220:223],a[92:95],a[60:63],0
ex v199,v199
mm v[220:223],a[88:91],a[56:59],v[220:223]
cv v227,v198,v199
mm v[220:223],a[84:87],a[52:55],v[220:223]
ex v204,v204
mm v[220:223],a[80:83],a[48:51],v[220:223]
wt vmcnt(16)
s_barrier
mm v[64:67],a[96:99],v[232:235],v[64:67]
ex v205,v205
mm v[96:99],a[96:99],v[236:239],v[96:99]
rd a[96:99],v248 offset:8192
cv v230,v204,v205
mm v[100:103],a[100:103],v[236:239],v[100:103]
ex v206,v206
mm v[68:71],a[100:103],v[232:235],v[68:71]
rd a[100:103],v248 offset:9216
mm v[72:75],a[104:107],v[232:235],v[72:75]
ex v207,v207
mm v[104:107],a[104:107],v[236:239],v[104:107]
rd a[104:107],v248 offset:10240
cv v231,v206,v207
s_mov_b32 s93, 100352
mm v[108:111],a[108:111],v[236:239],v[108:111]
ex v208,v208
mm v[76:79],a[108:111],v[232:235],v[76:79]
rd a[108:111],v248 offset:11264
ld a[184:187],v243,s[40:43],s93 offen
mm v[80:83],a[112:115],v[232:235],v[80:83]
ex v209,v209
mm v[112:115],a[112:115],v[236:239],v[112:115]
rd a[112:115],v248 offset:12288
mm v[116:119],a[116:119],v[236:239],v[116:119]
ex v210,v210
mm v[84:87],a[116:119],v[232:235],v[84:87]
rd a[116:119],v248 offset:13312
mm v[88:91],a[120:123],v[232:235],v[88:91]
ex v211,v211
mm v[120:123],a[120:123],v[236:239],v[120:123]
rd a[120:123],v248 offset:14336
mm v[124:127],a[124:127],v[236:239],v[124:127]
ex v216,v216
mm v[92:95],a[124:127],v[232:235],v[92:95]
rd a[124:127],v248 offset:15360
v_or_b32 v248, s89, v255
v_smfmac_f32_16x16x64_bf16 a[196:199], a[208:211], v[232:239], v241
wt lgkmcnt(4)
v_smfmac_f32_16x16x64_bf16 a[192:195], a[208:211], v[224:231], v241
rd a[64:67],v248 offset:0
mm v[0:3],a[96:99],v[224:227],v[0:3]
ex v217,v217
mm v[32:35],a[96:99],v[228:231],v[32:35]
rd a[68:71],v248 offset:1024
cv v232,v208,v209
mm v[36:39],a[100:103],v[228:231],v[36:39]
ex v218,v218
mm v[4:7],a[100:103],v[224:227],v[4:7]
rd a[72:75],v248 offset:2048
cv v233,v210,v211
mm v[8:11],a[104:107],v[224:227],v[8:11]
ex v219,v219
mm v[40:43],a[104:107],v[228:231],v[40:43]
rd a[76:79],v248 offset:3072
cv v236,v216,v217
mm v[44:47],a[108:111],v[228:231],v[44:47]
ex v212,v212
mm v[12:15],a[108:111],v[224:227],v[12:15]
rd a[80:83],v248 offset:4096
wt lgkmcnt(5)
mm v[16:19],a[112:115],v[224:227],v[16:19]
ex v213,v213
mm v[48:51],a[112:115],v[228:231],v[48:51]
rd a[84:87],v248 offset:5120
cv v237,v218,v219
mm v[52:55],a[116:119],v[228:231],v[52:55]
ex v214,v214
mm v[20:23],a[116:119],v[224:227],v[20:23]
rd a[88:91],v248 offset:6144
cv v234,v212,v213
mm v[24:27],a[120:123],v[224:227],v[24:27]
ex v215,v215
mm v[56:59],a[120:123],v[228:231],v[56:59]
rd a[92:95],v248 offset:7168
cv v235,v214,v215
mm v[60:63],a[124:127],v[228:231],v[60:63]
ex v220,v220
mm v[28:31],a[124:127],v[224:227],v[28:31]
wt lgkmcnt(4)
mm v[192:195],a[64:67],a[0:3],0
ex v221,v221
mm v[192:195],a[68:71],a[4:7],v[192:195]
cv v238,v220,v221
mm v[192:195],a[72:75],a[8:11],v[192:195]
ex v222,v222
mm v[192:195],a[76:79],a[12:15],v[192:195]
ld v240,s[56:59],s17 offen lds
mm v[200:203],a[76:79],a[28:31],0
ex v223,v223
s_add_u32 s17, s17, 32768
mm v[200:203],a[72:75],a[24:27],v[200:203]
cv v239,v222,v223
s_add_u32 m0, m0, 1024
mm v[200:203],a[68:71],a[20:23],v[200:203]
ex v192,v192
mm v[200:203],a[64:67],a[16:19],v[200:203]
wt lgkmcnt(0)
mm v[196:199],a[80:83],a[0:3],0
ex v193,v193
mm v[196:199],a[84:87],a[4:7],v[196:199]
cv v224,v192,v193
mm v[196:199],a[88:91],a[8:11],v[196:199]
ex v194,v194
mm v[196:199],a[92:95],a[12:15],v[196:199]
ld v240,s[56:59],s17 offen lds
mm v[204:207],a[92:95],a[28:31],0
ex v195,v195
s_add_u32 s17, s17, 4294942720
mm v[204:207],a[88:91],a[24:27],v[204:207]
cv v225,v194,v195
s_add_u32 m0, m0, 1152
mm v[204:207],a[84:87],a[20:23],v[204:207]
ex v200,v200
mm v[204:207],a[80:83],a[16:19],v[204:207]
ld v240,s[56:59],s17 offen lds
mm v[208:211],a[64:67],a[32:35],0
ex v201,v201
s_add_u32 s17, s17, 32768
mm v[208:211],a[68:71],a[36:39],v[208:211]
cv v228,v200,v201
s_add_u32 m0, m0, 1024
mm v[208:211],a[72:75],a[40:43],v[208:211]
ex v202,v202
mm v[208:211],a[76:79],a[44:47],v[208:211]
ld v240,s[56:59],s17 offen lds
mm v[216:219],a[76:79],a[60:63],0
ex v203,v203
mm v[216:219],a[72:75],a[56:59],v[216:219]
cv v229,v202,v203
s_mov_b32 s93, 107520
mm v[216:219],a[68:71],a[52:55],v[216:219]
ex v196,v196
mm v[216:219],a[64:67],a[48:51],v[216:219]
ld a[188:191],v243,s[40:43],s93 offen
mm v[212:215],a[80:83],a[32:35],0
ex v197,v197
mm v[212:215],a[84:87],a[36:39],v[212:215]
cv v226,v196,v197
mm v[212:215],a[88:91],a[40:43],v[212:215]
ex v198,v198
mm v[212:215],a[92:95],a[44:47],v[212:215]
mm v[220:223],a[92:95],a[60:63],0
ex v199,v199
mm v[220:223],a[88:91],a[56:59],v[220:223]
cv v227,v198,v199
mm v[220:223],a[84:87],a[52:55],v[220:223]
ex v204,v204
mm v[220:223],a[80:83],a[48:51],v[220:223]
mm v[64:67],a[96:99],v[232:235],v[64:67]
ex v205,v205
mm v[96:99],a[96:99],v[236:239],v[96:99]
rd a[96:99],v248 offset:8192
cv v230,v204,v205
mm v[100:103],a[100:103],v[236:239],v[100:103]
ex v206,v206
mm v[68:71],a[100:103],v[232:235],v[68:71]
rd a[100:103],v248 offset:9216
mm v[72:75],a[104:107],v[232:235],v[72:75]
ex v207,v207
mm v[104:107],a[104:107],v[236:239],v[104:107]
rd a[104:107],v248 offset:10240
cv v231,v206,v207
mm v[108:111],a[108:111],v[236:239],v[108:111]
ex v208,v208
mm v[76:79],a[108:111],v[232:235],v[76:79]
rd a[108:111],v248 offset:11264
mm v[80:83],a[112:115],v[232:235],v[80:83]
ex v209,v209
mm v[112:115],a[112:115],v[236:239],v[112:115]
rd a[112:115],v248 offset:12288
mm v[116:119],a[116:119],v[236:239],v[116:119]
ex v210,v210
mm v[84:87],a[116:119],v[232:235],v[84:87]
rd a[116:119],v248 offset:13312
mm v[88:91],a[120:123],v[232:235],v[88:91]
ex v211,v211
mm v[120:123],a[120:123],v[236:239],v[120:123]
rd a[120:123],v248 offset:14336
mm v[124:127],a[124:127],v[236:239],v[124:127]
ex v216,v216
mm v[92:95],a[124:127],v[232:235],v[92:95]
rd a[124:127],v248 offset:15360
v_smfmac_f32_16x16x64_bf16 a[196:199], a[208:211], v[232:239], v241
wt lgkmcnt(4)
v_smfmac_f32_16x16x64_bf16 a[192:195], a[208:211], v[224:231], v241
mm v[0:3],a[96:99],v[224:227],v[0:3]
ex v217,v217
mm v[32:35],a[96:99],v[228:231],v[32:35]
cv v232,v208,v209
mm v[36:39],a[100:103],v[228:231],v[36:39]
ex v218,v218
mm v[4:7],a[100:103],v[224:227],v[4:7]
cv v233,v210,v211
mm v[8:11],a[104:107],v[224:227],v[8:11]
ex v219,v219
mm v[40:43],a[104:107],v[228:231],v[40:43]
cv v236,v216,v217
mm v[44:47],a[108:111],v[228:231],v[44:47]
ex v212,v212
mm v[12:15],a[108:111],v[224:227],v[12:15]
wt lgkmcnt(0)
mm v[16:19],a[112:115],v[224:227],v[16:19]
ex v213,v213
mm v[48:51],a[112:115],v[228:231],v[48:51]
cv v237,v218,v219
mm v[52:55],a[116:119],v[228:231],v[52:55]
ex v214,v214
mm v[20:23],a[116:119],v[224:227],v[20:23]
cv v234,v212,v213
mm v[24:27],a[120:123],v[224:227],v[24:27]
ex v215,v215
mm v[56:59],a[120:123],v[228:231],v[56:59]
cv v235,v214,v215
mm v[60:63],a[124:127],v[228:231],v[60:63]
ex v220,v220
mm v[28:31],a[124:127],v[224:227],v[28:31]
; ---- helper: partial O and l go to the partner
.La5_LA_DP:
ex v221,v221
ex v222,v222
ex v223,v223
cv v238,v220,v221
s_nop 0
cv v239,v222,v223
s_nop 1
v_smfmac_f32_16x16x64_bf16 a[196:199], a[208:211], v[232:239], v241
cv v0,v0,v1
cv v1,v2,v3
cv v2,v4,v5
cv v3,v6,v7
st v[0:3],v255,s[68:71],0 offen offset:0 sc1
mm v[64:67],a[96:99],v[232:235],v[64:67]
cv v8,v8,v9
cv v9,v10,v11
cv v10,v12,v13
cv v11,v14,v15
st v[8:11],v255,s[68:71],0 offen offset:1024 sc1
mm v[96:99],a[96:99],v[236:239],v[96:99]
ar v240,a224
v_xor_b32 v241, 32, v240
cv v16,v16,v17
cv v17,v18,v19
cv v18,v20,v21
cv v19,v22,v23
mm v[100:103],a[100:103],v[236:239],v[100:103]
v_xor_b32 v242, 64, v240
v_xor_b32 v243, 96, v240
st v[16:19],v255,s[68:71],0 offen offset:2048 sc1
cv v24,v24,v25
cv v25,v26,v27
mm v[68:71],a[100:103],v[232:235],v[68:71]
cv v26,v28,v29
cv v27,v30,v31
st v[24:27],v255,s[68:71],0 offen offset:3072 sc1
cv v32,v32,v33
cv v33,v34,v35
mm v[72:75],a[104:107],v[232:235],v[72:75]
cv v34,v36,v37
cv v35,v38,v39
st v[32:35],v255,s[68:71],s95 offen offset:0 sc1
cv v40,v40,v41
cv v41,v42,v43
mm v[104:107],a[104:107],v[236:239],v[104:107]
wt vmcnt(13)
s_barrier
cv v42,v44,v45
cv v43,v46,v47
st v[40:43],v255,s[68:71],s95 offen offset:1024 sc1
mm v[108:111],a[108:111],v[236:239],v[108:111]
rt v[192:193],v240 offset:16896
rt v[194:195],v240 offset:17920
cv v48,v48,v49
cv v49,v50,v51
cv v50,v52,v53
cv v51,v54,v55
mm v[76:79],a[108:111],v[232:235],v[76:79]
rt v[196:197],v240 offset:25344
rt v[198:199],v240 offset:26368
st v[48:51],v255,s[68:71],s95 offen offset:2048 sc1
cv v56,v56,v57
cv v57,v58,v59
mm v[80:83],a[112:115],v[232:235],v[80:83]
rt v[200:201],v241 offset:16896
rt v[202:203],v241 offset:17920
cv v58,v60,v61
cv v59,v62,v63
st v[56:59],v255,s[68:71],s95 offen offset:3072 sc1
mm v[112:115],a[112:115],v[236:239],v[112:115]
rt v[204:205],v241 offset:25344
rt v[206:207],v241 offset:26368
mm v[116:119],a[116:119],v[236:239],v[116:119]
rt v[208:209],v242 offset:16896
rt v[210:211],v242 offset:17920
mm v[84:87],a[116:119],v[232:235],v[84:87]
rt v[212:213],v242 offset:25344
rt v[214:215],v242 offset:26368
mm v[88:91],a[120:123],v[232:235],v[88:91]
rt v[216:217],v243 offset:16896
rt v[218:219],v243 offset:17920
mm v[120:123],a[120:123],v[236:239],v[120:123]
rt v[220:221],v243 offset:25344
rt v[222:223],v243 offset:26368
mm v[124:127],a[124:127],v[236:239],v[124:127]
mm v[92:95],a[124:127],v[232:235],v[92:95]
wt lgkmcnt(12)
mm v[0:3],v[192:195],v[128:131],0
mm v[0:3],v[196:199],v[132:135],v[0:3]
mm v[32:35],v[196:199],v[148:151],0
wt lgkmcnt(8)
ar v224,a192
ar v228,a193
ar v225,a194
ar v229,a195
ar v226,a196
mm v[32:35],v[192:195],v[144:147],v[32:35]
rt v[196:197],v240 offset:17024
rt v[198:199],v240 offset:18048
ar v230,a197
ar v227,a198
ar v231,a199
v_add_f32 v224, v224, v228
mm v[36:39],v[200:203],v[144:147],0
v_add_f32 v225, v225, v229
v_add_f32 v226, v226, v230
v_add_f32 v227, v227, v231
s_mov_b32 s92, 16384
st v[224:227],v255,s[68:71],s92 offen sc1
mm v[36:39],v[204:207],v[148:151],v[36:39]
rt v[192:193],v240 offset:25472
rt v[194:195],v240 offset:26496
cv v64,v64,v65
cv v65,v66,v67
cv v66,v68,v69
cv v67,v70,v71
mm v[4:7],v[204:207],v[132:135],0
wt lgkmcnt(4)
st v[64:67],v255,s[68:71],s96 offen offset:0 sc1
cv v72,v72,v73
cv v73,v74,v75
cv v74,v76,v77
mm v[4:7],v[200:203],v[128:131],v[4:7]
rt v[204:205],v241 offset:17024
rt v[206:207],v241 offset:18048
cv v75,v78,v79
st v[72:75],v255,s[68:71],s96 offen offset:1024 sc1
cv v80,v80,v81
mm v[8:11],v[208:211],v[128:131],0
cv v81,v82,v83
cv v82,v84,v85
cv v83,v86,v87
st v[80:83],v255,s[68:71],s96 offen offset:2048 sc1
cv v88,v88,v89
mm v[8:11],v[212:215],v[132:135],v[8:11]
rt v[200:201],v241 offset:25472
rt v[202:203],v241 offset:26496
cv v89,v90,v91
cv v90,v92,v93
cv v91,v94,v95
st v[88:91],v255,s[68:71],s96 offen offset:3072 sc1
mm v[40:43],v[212:215],v[148:151],0
cv v96,v96,v97
cv v97,v98,v99
cv v98,v100,v101
cv v99,v102,v103
st v[96:99],v255,s[68:71],s97 offen offset:0 sc1
mm v[40:43],v[208:211],v[144:147],v[40:43]
rt v[212:213],v242 offset:17024
rt v[214:215],v242 offset:18048
cv v104,v104,v105
cv v105,v106,v107
cv v106,v108,v109
cv v107,v110,v111
mm v[44:47],v[216:219],v[144:147],0
st v[104:107],v255,s[68:71],s97 offen offset:1024 sc1
cv v112,v112,v113
cv v113,v114,v115
cv v114,v116,v117
cv v115,v118,v119
mm v[44:47],v[220:223],v[148:151],v[44:47]
rt v[208:209],v242 offset:25472
rt v[210:211],v242 offset:26496
st v[112:115],v255,s[68:71],s97 offen offset:2048 sc1
cv v120,v120,v121
cv v121,v122,v123
mm v[12:15],v[220:223],v[132:135],0
wt lgkmcnt(4)
cv v122,v124,v125
cv v123,v126,v127
st v[120:123],v255,s[68:71],s97 offen offset:3072 sc1
v_mov_b32 v249, 0
mm v[12:15],v[216:219],v[128:131],v[12:15]
rt v[220:221],v243 offset:17024
rt v[222:223],v243 offset:18048
s_add_u32 s6, s88, 393216
mm v[16:19],v[196:199],v[128:131],0
ar v224,a228
v_add_u32 v225, 0x8000, v224
.La5_LW6_6:
s_cbranch_execz .La5_LW6_R0
v_add_u32 v226, 0x2000, v224
v_add_u32 v227, 0xa000, v224
mm v[16:19],v[192:195],v[132:135],v[16:19]
rt v[216:217],v243 offset:25472
rt v[218:219],v243 offset:26496
ar v248,a225
ar v228,a229
mm v[48:51],v[192:195],v[148:151],0
ar v229,a230
mm v[48:51],v[196:199],v[144:147],v[48:51]
rt v[192:193],v240 offset:33792
rt v[194:195],v240 offset:34816
mm v[52:55],v[204:207],v[144:147],0
mm v[52:55],v[200:203],v[148:151],v[52:55]
rt v[196:197],v240 offset:42240
rt v[198:199],v240 offset:43264
mm v[20:23],v[200:203],v[132:135],0
wt lgkmcnt(4)
mm v[20:23],v[204:207],v[128:131],v[20:23]
rt v[200:201],v241 offset:33792
rt v[202:203],v241 offset:34816
mm v[24:27],v[212:215],v[128:131],0
s_bitcmp1_b32 2863311530, s2
s_nop 0
s_cbranch_scc1 .La5_LO_VC2
s_getpc_b64 s[24:25]
.La5_LW3_P0:
s_add_u32 s24, s24, .La5_LW3_R0-.La5_LW3_P0
s_addc_u32 s25, s25, 0
s_mov_b64 exec, 0
s_cmp_eq_u32 s3, 0
s_cbranch_scc1 .La5_LW3_0
s_cmp_eq_u32 s3, 1
s_cbranch_scc1 .La5_LW3_1
s_cmp_eq_u32 s3, 2
s_cbranch_scc1 .La5_LW3_2
s_branch .La5_LW3_3
.La5_LW3_R0:
s_mov_b64 exec, -1
.La5_LO_VE2:
wt vmcnt(0)
buffer_store_dwordx2 v[252:253], v249, s[64:67], s6 offen offset:0 sc1
mm v[24:27],v[208:211],v[132:135],v[24:27]
rt v[204:205],v241 offset:42240
rt v[206:207],v241 offset:43264
mm v[56:59],v[208:211],v[148:151],0
s_mov_b32 s93, 3670016
ld a[192:195],v228,s[40:43],s93 offen
mm v[56:59],v[212:215],v[144:147],v[56:59]
rt v[208:209],v242 offset:33792
rt v[210:211],v242 offset:34816
mm v[60:63],v[220:223],v[144:147],0
s_mov_b32 s93, 3677184
ld a[196:199],v228,s[40:43],s93 offen
mm v[60:63],v[216:219],v[148:151],v[60:63]
rt v[212:213],v242 offset:42240
rt v[214:215],v242 offset:43264
mm v[28:31],v[216:219],v[132:135],0
wt lgkmcnt(4)
s_mov_b32 s93, 3684352
ld a[200:203],v228,s[40:43],s93 offen
mm v[28:31],v[220:223],v[128:131],v[28:31]
rt v[216:217],v243 offset:33792
rt v[218:219],v243 offset:34816
mm v[0:3],v[192:195],v[136:139],v[0:3]
s_mov_b32 s93, 3691520
ld a[204:207],v228,s[40:43],s93 offen
mm v[0:3],v[196:199],v[140:143],v[0:3]
rt v[220:221],v243 offset:42240
rt v[222:223],v243 offset:43264
mm v[32:35],v[196:199],v[156:159],v[32:35]
s_mov_b32 s93, 3698688
ld a[208:211],v228,s[40:43],s93 offen
mm v[32:35],v[192:195],v[152:155],v[32:35]
rt v[196:197],v240 offset:33920
rt v[198:199],v240 offset:34944
mm v[36:39],v[200:203],v[152:155],v[36:39]
s_mov_b32 s93, 3705856
ld a[212:215],v228,s[40:43],s93 offen
mm v[36:39],v[204:207],v[156:159],v[36:39]
rt v[192:193],v240 offset:42368
rt v[194:195],v240 offset:43392
mm v[4:7],v[204:207],v[140:143],v[4:7]
wt lgkmcnt(4)
s_mov_b32 s93, 3713024
ld a[216:219],v228,s[40:43],s93 offen
mm v[4:7],v[200:203],v[136:139],v[4:7]
rt v[204:205],v241 offset:33920
rt v[206:207],v241 offset:34944
mm v[8:11],v[208:211],v[136:139],v[8:11]
s_mov_b32 s93, 3720192
ld a[220:223],v228,s[40:43],s93 offen
mm v[8:11],v[212:215],v[140:143],v[8:11]
rt v[200:201],v241 offset:42368
rt v[202:203],v241 offset:43392
mm v[40:43],v[212:215],v[156:159],v[40:43]
s_mov_b32 s93, 3727360
ld a[224:227],v228,s[40:43],s93 offen
mm v[40:43],v[208:211],v[152:155],v[40:43]
rt v[212:213],v242 offset:33920
rt v[214:215],v242 offset:34944
mm v[44:47],v[216:219],v[152:155],v[44:47]
s_mov_b32 s93, 3734528
ld a[228:231],v228,s[40:43],s93 offen
mm v[44:47],v[220:223],v[156:159],v[44:47]
rt v[208:209],v242 offset:42368
rt v[210:211],v242 offset:43392
mm v[12:15],v[220:223],v[140:143],v[12:15]
wt lgkmcnt(4)
s_mov_b32 s93, 3741696
ld a[232:235],v228,s[40:43],s93 offen
mm v[12:15],v[216:219],v[136:139],v[12:15]
rt v[220:221],v243 offset:33920
rt v[222:223],v243 offset:34944
mm v[16:19],v[196:199],v[136:139],v[16:19]
s_mov_b32 s93, 3748864
ld a[236:239],v228,s[40:43],s93 offen
mm v[16:19],v[192:195],v[140:143],v[16:19]
rt v[216:217],v243 offset:42368
rt v[218:219],v243 offset:43392
mm v[48:51],v[192:195],v[156:159],v[48:51]
s_mov_b32 s93, 3756032
ld a[240:243],v228,s[40:43],s93 offen
mm v[48:51],v[196:199],v[152:155],v[48:51]
rt v[192:193],v240 offset:16896
rt v[194:195],v240 offset:17920
mm v[52:55],v[204:207],v[152:155],v[52:55]
s_mov_b32 s93, 3763200
ld a[244:247],v228,s[40:43],s93 offen
mm v[52:55],v[200:203],v[156:159],v[52:55]
rt v[196:197],v240 offset:25344
rt v[198:199],v240 offset:26368
mm v[20:23],v[200:203],v[140:143],v[20:23]
wt lgkmcnt(4)
s_mov_b32 s93, 3770368
ld a[248:251],v228,s[40:43],s93 offen
mm v[20:23],v[204:207],v[136:139],v[20:23]
rt v[200:201],v241 offset:16896
rt v[202:203],v241 offset:17920
mm v[24:27],v[212:215],v[136:139],v[24:27]
s_mov_b32 s93, 3777536
ld a[252:255],v228,s[40:43],s93 offen
mm v[24:27],v[208:211],v[140:143],v[24:27]
rt v[204:205],v241 offset:25344
rt v[206:207],v241 offset:26368
mm v[56:59],v[208:211],v[156:159],v[56:59]
mm v[56:59],v[212:215],v[152:155],v[56:59]
rt v[208:209],v242 offset:16896
rt v[210:211],v242 offset:17920
mm v[60:63],v[220:223],v[152:155],v[60:63]
mm v[60:63],v[216:219],v[156:159],v[60:63]
rt v[212:213],v242 offset:25344
rt v[214:215],v242 offset:26368
mm v[28:31],v[216:219],v[140:143],v[28:31]
wt lgkmcnt(4)
mm v[28:31],v[220:223],v[136:139],v[28:31]
rt v[216:217],v243 offset:16896
rt v[218:219],v243 offset:17920
s_branch .La5_LO_START
; ---- owner: last windows
.La5_LA_DC:
.La5_LW5_0:
s_cbranch_execz .La5_LW5_4
wt lgkmcnt(4)
mm v[192:195],a[64:67],v[128:131],0
ex v221,v221
mm v[192:195],a[68:71],v[132:135],v[192:195]
cv v238,v220,v221
s_add_u32 s89, s89, 16384
mm v[192:195],a[72:75],v[136:139],v[192:195]
ex v222,v222
mm v[192:195],a[76:79],v[140:143],v[192:195]
ld v255,s[60:63],s17 offen offset:0 sc1 lds
mm v[200:203],a[76:79],v[156:159],0
ex v223,v223
mm v[200:203],a[72:75],v[152:155],v[200:203]
cv v239,v222,v223
s_and_b32 s89, s89, 0x1ffff
mm v[200:203],a[68:71],v[148:151],v[200:203]
ex v192,v192
mm v[200:203],a[64:67],v[144:147],v[200:203]
wt lgkmcnt(0)
mm v[196:199],a[80:83],v[128:131],0
ex v193,v193
mm v[196:199],a[84:87],v[132:135],v[196:199]
cv v224,v192,v193
ar v243,a229
mm v[196:199],a[88:91],v[136:139],v[196:199]
ex v194,v194
mm v[196:199],a[92:95],v[140:143],v[196:199]
ld v255,s[60:63],s17 offen offset:1024 sc1 lds
mm v[204:207],a[92:95],v[156:159],0
ex v195,v195
mm v[204:207],a[88:91],v[152:155],v[204:207]
cv v225,v194,v195
mm v[204:207],a[84:87],v[148:151],v[204:207]
ex v200,v200
mm v[204:207],a[80:83],v[144:147],v[204:207]
ld v255,s[60:63],s17 offen offset:2048 sc1 lds
mm v[208:211],a[64:67],v[160:163],0
ex v201,v201
mm v[208:211],a[68:71],v[164:167],v[208:211]
cv v228,v200,v201
mm v[208:211],a[72:75],v[168:171],v[208:211]
ex v202,v202
mm v[208:211],a[76:79],v[172:175],v[208:211]
ld v255,s[60:63],s17 offen offset:3072 sc1 lds
mm v[216:219],a[76:79],v[188:191],0
ex v203,v203
mm v[216:219],a[72:75],v[184:187],v[216:219]
cv v229,v202,v203
s_mov_b32 s93, 0
mm v[216:219],a[68:71],v[180:183],v[216:219]
ex v196,v196
mm v[216:219],a[64:67],v[176:179],v[216:219]
ld a[128:131],v243,s[40:43],s93 offen
mm v[212:215],a[80:83],v[160:163],0
ex v197,v197
s_add_u32 s17, s17, s80
mm v[212:215],a[84:87],v[164:167],v[212:215]
cv v226,v196,v197
s_mov_b32 s93, 7168
s_add_u32 m0, m0, 16384
mm v[212:215],a[88:91],v[168:171],v[212:215]
ex v198,v198
s_and_b32 m0, m0, s78
mm v[212:215],a[92:95],v[172:175],v[212:215]
ld a[132:135],v243,s[40:43],s93 offen
mm v[220:223],a[92:95],v[188:191],0
ex v199,v199
mm v[220:223],a[88:91],v[184:187],v[220:223]
cv v227,v198,v199
mm v[220:223],a[84:87],v[180:183],v[220:223]
ex v204,v204
mm v[220:223],a[80:83],v[176:179],v[220:223]
wt vmcnt(12)
s_barrier
mm v[64:67],a[96:99],v[232:235],v[64:67]
ex v205,v205
mm v[96:99],a[96:99],v[236:239],v[96:99]
rd a[96:99],v248 offset:8192
cv v230,v204,v205
s_mov_b32 s93, 28672
mm v[100:103],a[100:103],v[236:239],v[100:103]
ex v206,v206
mm v[68:71],a[100:103],v[232:235],v[68:71]
rd a[100:103],v248 offset:9216
ld a[144:147],v243,s[40:43],s93 offen
mm v[72:75],a[104:107],v[232:235],v[72:75]
ex v207,v207
mm v[104:107],a[104:107],v[236:239],v[104:107]
rd a[104:107],v248 offset:10240
cv v231,v206,v207
s_mov_b32 s93, 14336
mm v[108:111],a[108:111],v[236:239],v[108:111]
ex v208,v208
mm v[76:79],a[108:111],v[232:235],v[76:79]
rd a[108:111],v248 offset:11264
ld a[136:139],v243,s[40:43],s93 offen
mm v[80:83],a[112:115],v[232:235],v[80:83]
ex v209,v209
mm v[112:115],a[112:115],v[236:239],v[112:115]
rd a[112:115],v248 offset:12288
s_mov_b32 s93, 21504
mm v[116:119],a[116:119],v[236:239],v[116:119]
ex v210,v210
mm v[84:87],a[116:119],v[232:235],v[84:87]
rd a[116:119],v248 offset:13312
ld a[140:143],v243,s[40:43],s93 offen
mm v[88:91],a[120:123],v[232:235],v[88:91]
ex v211,v211
mm v[120:123],a[120:123],v[236:239],v[120:123]
rd a[120:123],v248 offset:14336
s_mov_b32 s93, 35840
mm v[124:127],a[124:127],v[236:239],v[124:127]
ex v216,v216
mm v[92:95],a[124:127],v[232:235],v[92:95]
rd a[124:127],v248 offset:15360
ld a[148:151],v243,s[40:43],s93 offen
v_or_b32 v248, s89, v255
v_smfmac_f32_16x16x64_bf16 a[196:199], a[208:211], v[232:239], v241
wt lgkmcnt(4)
v_smfmac_f32_16x16x64_bf16 a[192:195], a[208:211], v[224:231], v241
rd a[64:67],v248 offset:0
mm v[0:3],a[96:99],v[224:227],v[0:3]
ex v217,v217
mm v[32:35],a[96:99],v[228:231],v[32:35]
rd a[68:71],v248 offset:1024
cv v232,v208,v209
mm v[36:39],a[100:103],v[228:231],v[36:39]
ex v218,v218
mm v[4:7],a[100:103],v[224:227],v[4:7]
rd a[72:75],v248 offset:2048
cv v233,v210,v211
mm v[8:11],a[104:107],v[224:227],v[8:11]
ex v219,v219
mm v[40:43],a[104:107],v[228:231],v[40:43]
rd a[76:79],v248 offset:3072
cv v236,v216,v217
mm v[44:47],a[108:111],v[228:231],v[44:47]
ex v212,v212
mm v[12:15],a[108:111],v[224:227],v[12:15]
rd a[80:83],v248 offset:4096
wt lgkmcnt(5)
mm v[16:19],a[112:115],v[224:227],v[16:19]
ex v213,v213
mm v[48:51],a[112:115],v[228:231],v[48:51]
rd a[84:87],v248 offset:5120
cv v237,v218,v219
mm v[52:55],a[116:119],v[228:231],v[52:55]
ex v214,v214
mm v[20:23],a[116:119],v[224:227],v[20:23]
rd a[88:91],v248 offset:6144
cv v234,v212,v213
mm v[24:27],a[120:123],v[224:227],v[24:27]
ex v215,v215
mm v[56:59],a[120:123],v[228:231],v[56:59]
rd a[92:95],v248 offset:7168
cv v235,v214,v215
mm v[60:63],a[124:127],v[228:231],v[60:63]
ex v220,v220
mm v[28:31],a[124:127],v[224:227],v[28:31]
wt lgkmcnt(4)
mm v[192:195],a[64:67],v[128:131],0
ex v221,v221
mm v[192:195],a[68:71],v[132:135],v[192:195]
cv v238,v220,v221
s_add_u32 s89, s89, 16384
mm v[192:195],a[72:75],v[136:139],v[192:195]
ex v222,v222
mm v[192:195],a[76:79],v[140:143],v[192:195]
ld v255,s[60:63],s17 offen offset:0 sc1 lds
mm v[200:203],a[76:79],v[156:159],0
ex v223,v223
mm v[200:203],a[72:75],v[152:155],v[200:203]
cv v239,v222,v223
s_and_b32 s89, s89, 0x1ffff
mm v[200:203],a[68:71],v[148:151],v[200:203]
ex v192,v192
mm v[200:203],a[64:67],v[144:147],v[200:203]
wt lgkmcnt(0)
mm v[196:199],a[80:83],v[128:131],0
ex v193,v193
mm v[196:199],a[84:87],v[132:135],v[196:199]
cv v224,v192,v193
mm v[196:199],a[88:91],v[136:139],v[196:199]
ex v194,v194
mm v[196:199],a[92:95],v[140:143],v[196:199]
ld v255,s[60:63],s17 offen offset:1024 sc1 lds
mm v[204:207],a[92:95],v[156:159],0
ex v195,v195
mm v[204:207],a[88:91],v[152:155],v[204:207]
cv v225,v194,v195
mm v[204:207],a[84:87],v[148:151],v[204:207]
ex v200,v200
mm v[204:207],a[80:83],v[144:147],v[204:207]
ld v255,s[60:63],s17 offen offset:2048 sc1 lds
mm v[208:211],a[64:67],v[160:163],0
ex v201,v201
mm v[208:211],a[68:71],v[164:167],v[208:211]
cv v228,v200,v201
mm v[208:211],a[72:75],v[168:171],v[208:211]
ex v202,v202
mm v[208:211],a[76:79],v[172:175],v[208:211]
ld v255,s[60:63],s17 offen offset:3072 sc1 lds
mm v[216:219],a[76:79],v[188:191],0
ex v203,v203
mm v[216:219],a[72:75],v[184:187],v[216:219]
cv v229,v202,v203
s_mov_b32 s93, 43008
mm v[216:219],a[68:71],v[180:183],v[216:219]
ex v196,v196
mm v[216:219],a[64:67],v[176:179],v[216:219]
ld a[152:155],v243,s[40:43],s93 offen
mm v[212:215],a[80:83],v[160:163],0
ex v197,v197
s_add_u32 s17, s17, s80
mm v[212:215],a[84:87],v[164:167],v[212:215]
cv v226,v196,v197
s_mov_b32 s93, 50176
s_add_u32 m0, m0, 16384
mm v[212:215],a[88:91],v[168:171],v[212:215]
ex v198,v198
s_and_b32 m0, m0, s78
mm v[212:215],a[92:95],v[172:175],v[212:215]
ld a[156:159],v243,s[40:43],s93 offen
mm v[220:223],a[92:95],v[188:191],0
ex v199,v199
mm v[220:223],a[88:91],v[184:187],v[220:223]
cv v227,v198,v199
mm v[220:223],a[84:87],v[180:183],v[220:223]
ex v204,v204
mm v[220:223],a[80:83],v[176:179],v[220:223]
wt vmcnt(17)
s_barrier
mm v[64:67],a[96:99],v[232:235],v[64:67]
ex v205,v205
mm v[96:99],a[96:99],v[236:239],v[96:99]
rd a[96:99],v248 offset:8192
cv v230,v204,v205
s_mov_b32 s93, 71680
mm v[100:103],a[100:103],v[236:239],v[100:103]
ex v206,v206
mm v[68:71],a[100:103],v[232:235],v[68:71]
rd a[100:103],v248 offset:9216
ld a[168:171],v243,s[40:43],s93 offen
mm v[72:75],a[104:107],v[232:235],v[72:75]
ex v207,v207
mm v[104:107],a[104:107],v[236:239],v[104:107]
rd a[104:107],v248 offset:10240
cv v231,v206,v207
s_mov_b32 s93, 57344
mm v[108:111],a[108:111],v[236:239],v[108:111]
ex v208,v208
mm v[76:79],a[108:111],v[232:235],v[76:79]
rd a[108:111],v248 offset:11264
ld a[160:163],v243,s[40:43],s93 offen
mm v[80:83],a[112:115],v[232:235],v[80:83]
ex v209,v209
mm v[112:115],a[112:115],v[236:239],v[112:115]
rd a[112:115],v248 offset:12288
s_mov_b32 s93, 64512
mm v[116:119],a[116:119],v[236:239],v[116:119]
ex v210,v210
mm v[84:87],a[116:119],v[232:235],v[84:87]
rd a[116:119],v248 offset:13312
ld a[164:167],v243,s[40:43],s93 offen
mm v[88:91],a[120:123],v[232:235],v[88:91]
ex v211,v211
mm v[120:123],a[120:123],v[236:239],v[120:123]
rd a[120:123],v248 offset:14336
mm v[124:127],a[124:127],v[236:239],v[124:127]
ex v216,v216
mm v[92:95],a[124:127],v[232:235],v[92:95]
rd a[124:127],v248 offset:15360
v_or_b32 v248, s89, v255
v_smfmac_f32_16x16x64_bf16 a[196:199], a[208:211], v[232:239], v241
wt lgkmcnt(4)
v_smfmac_f32_16x16x64_bf16 a[192:195], a[208:211], v[224:231], v241
rd a[64:67],v248 offset:0
mm v[0:3],a[96:99],v[224:227],v[0:3]
ex v217,v217
mm v[32:35],a[96:99],v[228:231],v[32:35]
rd a[68:71],v248 offset:1024
cv v232,v208,v209
mm v[36:39],a[100:103],v[228:231],v[36:39]
ex v218,v218
mm v[4:7],a[100:103],v[224:227],v[4:7]
rd a[72:75],v248 offset:2048
cv v233,v210,v211
mm v[8:11],a[104:107],v[224:227],v[8:11]
ex v219,v219
mm v[40:43],a[104:107],v[228:231],v[40:43]
rd a[76:79],v248 offset:3072
cv v236,v216,v217
mm v[44:47],a[108:111],v[228:231],v[44:47]
ex v212,v212
mm v[12:15],a[108:111],v[224:227],v[12:15]
rd a[80:83],v248 offset:4096
wt lgkmcnt(5)
mm v[16:19],a[112:115],v[224:227],v[16:19]
ex v213,v213
mm v[48:51],a[112:115],v[228:231],v[48:51]
rd a[84:87],v248 offset:5120
cv v237,v218,v219
mm v[52:55],a[116:119],v[228:231],v[52:55]
ex v214,v214
mm v[20:23],a[116:119],v[224:227],v[20:23]
rd a[88:91],v248 offset:6144
cv v234,v212,v213
mm v[24:27],a[120:123],v[224:227],v[24:27]
ex v215,v215
mm v[56:59],a[120:123],v[228:231],v[56:59]
rd a[92:95],v248 offset:7168
cv v235,v214,v215
mm v[60:63],a[124:127],v[228:231],v[60:63]
ex v220,v220
mm v[28:31],a[124:127],v[224:227],v[28:31]
wt lgkmcnt(4)
mm v[192:195],a[64:67],v[128:131],0
ex v221,v221
mm v[192:195],a[68:71],v[132:135],v[192:195]
cv v238,v220,v221
s_add_u32 s89, s89, 16384
mm v[192:195],a[72:75],v[136:139],v[192:195]
ex v222,v222
mm v[192:195],a[76:79],v[140:143],v[192:195]
ld v255,s[60:63],s17 offen offset:0 sc1 lds
mm v[200:203],a[76:79],v[156:159],0
ex v223,v223
mm v[200:203],a[72:75],v[152:155],v[200:203]
cv v239,v222,v223
s_and_b32 s89, s89, 0x1ffff
mm v[200:203],a[68:71],v[148:151],v[200:203]
ex v192,v192
mm v[200:203],a[64:67],v[144:147],v[200:203]
wt lgkmcnt(0)
mm v[196:199],a[80:83],v[128:131],0
ex v193,v193
mm v[196:199],a[84:87],v[132:135],v[196:199]
cv v224,v192,v193
mm v[196:199],a[88:91],v[136:139],v[196:199]
ex v194,v194
mm v[196:199],a[92:95],v[140:143],v[196:199]
ld v255,s[60:63],s17 offen offset:1024 sc1 lds
mm v[204:207],a[92:95],v[156:159],0
ex v195,v195
mm v[204:207],a[88:91],v[152:155],v[204:207]
cv v225,v194,v195
mm v[204:207],a[84:87],v[148:151],v[204:207]
ex v200,v200
mm v[204:207],a[80:83],v[144:147],v[204:207]
ld v255,s[60:63],s17 offen offset:2048 sc1 lds
mm v[208:211],a[64:67],v[160:163],0
ex v201,v201
mm v[208:211],a[68:71],v[164:167],v[208:211]
cv v228,v200,v201
mm v[208:211],a[72:75],v[168:171],v[208:211]
ex v202,v202
mm v[208:211],a[76:79],v[172:175],v[208:211]
ld v255,s[60:63],s17 offen offset:3072 sc1 lds
mm v[216:219],a[76:79],v[188:191],0
ex v203,v203
mm v[216:219],a[72:75],v[184:187],v[216:219]
cv v229,v202,v203
s_mov_b32 s93, 78848
mm v[216:219],a[68:71],v[180:183],v[216:219]
ex v196,v196
mm v[216:219],a[64:67],v[176:179],v[216:219]
ld a[172:175],v243,s[40:43],s93 offen
mm v[212:215],a[80:83],v[160:163],0
ex v197,v197
s_add_u32 s17, s17, s80
mm v[212:215],a[84:87],v[164:167],v[212:215]
cv v226,v196,v197
s_mov_b32 s93, 86016
s_add_u32 m0, m0, 16384
mm v[212:215],a[88:91],v[168:171],v[212:215]
ex v198,v198
s_and_b32 m0, m0, s78
mm v[212:215],a[92:95],v[172:175],v[212:215]
ld a[176:179],v243,s[40:43],s93 offen
mm v[220:223],a[92:95],v[188:191],0
ex v199,v199
mm v[220:223],a[88:91],v[184:187],v[220:223]
cv v227,v198,v199
mm v[220:223],a[84:87],v[180:183],v[220:223]
ex v204,v204
mm v[220:223],a[80:83],v[176:179],v[220:223]
wt vmcnt(21)
s_barrier
mm v[64:67],a[96:99],v[232:235],v[64:67]
ex v205,v205
mm v[96:99],a[96:99],v[236:239],v[96:99]
rd a[96:99],v248 offset:8192
cv v230,v204,v205
s_mov_b32 s93, 107520
mm v[100:103],a[100:103],v[236:239],v[100:103]
ex v206,v206
mm v[68:71],a[100:103],v[232:235],v[68:71]
rd a[100:103],v248 offset:9216
ld a[188:191],v243,s[40:43],s93 offen
mm v[72:75],a[104:107],v[232:235],v[72:75]
ex v207,v207
mm v[104:107],a[104:107],v[236:239],v[104:107]
rd a[104:107],v248 offset:10240
cv v231,v206,v207
s_mov_b32 s93, 93184
mm v[108:111],a[108:111],v[236:239],v[108:111]
ex v208,v208
mm v[76:79],a[108:111],v[232:235],v[76:79]
rd a[108:111],v248 offset:11264
ld a[180:183],v243,s[40:43],s93 offen
mm v[80:83],a[112:115],v[232:235],v[80:83]
ex v209,v209
mm v[112:115],a[112:115],v[236:239],v[112:115]
rd a[112:115],v248 offset:12288
s_mov_b32 s93, 100352
mm v[116:119],a[116:119],v[236:239],v[116:119]
ex v210,v210
mm v[84:87],a[116:119],v[232:235],v[84:87]
rd a[116:119],v248 offset:13312
ld a[184:187],v243,s[40:43],s93 offen
mm v[88:91],a[120:123],v[232:235],v[88:91]
.La5_LW5_1:
s_cbranch_execz .La5_LW5_R0
ex v211,v211
mm v[120:123],a[120:123],v[236:239],v[120:123]
rd a[120:123],v248 offset:14336
mm v[124:127],a[124:127],v[236:239],v[124:127]
ex v216,v216
mm v[92:95],a[124:127],v[232:235],v[92:95]
rd a[124:127],v248 offset:15360
v_or_b32 v248, s89, v255
v_smfmac_f32_16x16x64_bf16 a[196:199], a[208:211], v[232:239], v241
wt lgkmcnt(4)
v_smfmac_f32_16x16x64_bf16 a[192:195], a[208:211], v[224:231], v241
rd a[64:67],v248 offset:0
mm v[0:3],a[96:99],v[224:227],v[0:3]
ex v217,v217
mm v[32:35],a[96:99],v[228:231],v[32:35]
rd a[68:71],v248 offset:1024
cv v232,v208,v209
mm v[36:39],a[100:103],v[228:231],v[36:39]
ex v218,v218
mm v[4:7],a[100:103],v[224:227],v[4:7]
rd a[72:75],v248 offset:2048
cv v233,v210,v211
mm v[8:11],a[104:107],v[224:227],v[8:11]
ex v219,v219
mm v[40:43],a[104:107],v[228:231],v[40:43]
rd a[76:79],v248 offset:3072
cv v236,v216,v217
mm v[44:47],a[108:111],v[228:231],v[44:47]
ex v212,v212
mm v[12:15],a[108:111],v[224:227],v[12:15]
rd a[80:83],v248 offset:4096
wt lgkmcnt(5)
mm v[16:19],a[112:115],v[224:227],v[16:19]
ex v213,v213
mm v[48:51],a[112:115],v[228:231],v[48:51]
rd a[84:87],v248 offset:5120
cv v237,v218,v219
mm v[52:55],a[116:119],v[228:231],v[52:55]
ex v214,v214
mm v[20:23],a[116:119],v[224:227],v[20:23]
rd a[88:91],v248 offset:6144
cv v234,v212,v213
mm v[24:27],a[120:123],v[224:227],v[24:27]
ex v215,v215
mm v[56:59],a[120:123],v[228:231],v[56:59]
rd a[92:95],v248 offset:7168
cv v235,v214,v215
mm v[60:63],a[124:127],v[228:231],v[60:63]
ex v220,v220
mm v[28:31],a[124:127],v[224:227],v[28:31]
.La5_LA_DC3:
.La5_LW5_2:
s_cbranch_execz .La5_LW5_R0
wt lgkmcnt(4)
mm v[192:195],a[64:67],v[128:131],0
ex v221,v221
mm v[192:195],a[68:71],v[132:135],v[192:195]
cv v238,v220,v221
ar v240,a228
mm v[192:195],a[72:75],v[136:139],v[192:195]
ex v222,v222
mm v[192:195],a[76:79],v[140:143],v[192:195]
s_add_u32 m0, s83, 16896
s_lshl_b32 s17, s72, 18
mm v[200:203],a[76:79],v[156:159],0
ex v223,v223
mm v[200:203],a[72:75],v[152:155],v[200:203]
cv v239,v222,v223
s_add_u32 s89, s89, 16384
mm v[200:203],a[68:71],v[148:151],v[200:203]
ex v192,v192
mm v[200:203],a[64:67],v[144:147],v[200:203]
wt lgkmcnt(0)
s_and_b32 s89, s89, 0x1ffff
mm v[196:199],a[80:83],v[128:131],0
ex v193,v193
mm v[196:199],a[84:87],v[132:135],v[196:199]
cv v224,v192,v193
mm v[196:199],a[88:91],v[136:139],v[196:199]
ex v194,v194
mm v[196:199],a[92:95],v[140:143],v[196:199]
ld v240,s[56:59],s17 offen lds
mm v[204:207],a[92:95],v[156:159],0
ex v195,v195
s_add_u32 s17, s17, 32768
mm v[204:207],a[88:91],v[152:155],v[204:207]
cv v225,v194,v195
s_add_u32 m0, m0, 1024
mm v[204:207],a[84:87],v[148:151],v[204:207]
ex v200,v200
mm v[204:207],a[80:83],v[144:147],v[204:207]
ld v240,s[56:59],s17 offen lds
mm v[208:211],a[64:67],v[160:163],0
ex v201,v201
s_add_u32 s17, s17, 4294942720
mm v[208:211],a[68:71],v[164:167],v[208:211]
cv v228,v200,v201
s_add_u32 m0, m0, 1152
mm v[208:211],a[72:75],v[168:171],v[208:211]
ex v202,v202
mm v[208:211],a[76:79],v[172:175],v[208:211]
ld v240,s[56:59],s17 offen lds
mm v[216:219],a[76:79],v[188:191],0
ex v203,v203
s_add_u32 s17, s17, 32768
mm v[216:219],a[72:75],v[184:187],v[216:219]
cv v229,v202,v203
s_add_u32 m0, m0, 1024
mm v[216:219],a[68:71],v[180:183],v[216:219]
ex v196,v196
mm v[216:219],a[64:67],v[176:179],v[216:219]
ld v240,s[56:59],s17 offen lds
mm v[212:215],a[80:83],v[160:163],0
ex v197,v197
s_add_u32 s17, s17, 90112
mm v[212:215],a[84:87],v[164:167],v[212:215]
cv v226,v196,v197
s_add_u32 m0, s83, 33792
mm v[212:215],a[88:91],v[168:171],v[212:215]
ex v198,v198
mm v[212:215],a[92:95],v[172:175],v[212:215]
mm v[220:223],a[92:95],v[188:191],0
ex v199,v199
mm v[220:223],a[88:91],v[184:187],v[220:223]
cv v227,v198,v199
mm v[220:223],a[84:87],v[180:183],v[220:223]
ex v204,v204
mm v[220:223],a[80:83],v[176:179],v[220:223]
wt vmcnt(10)
s_barrier
mm v[64:67],a[96:99],v[232:235],v[64:67]
ex v205,v205
mm v[96:99],a[96:99],v[236:239],v[96:99]
rd a[96:99],v248 offset:8192
cv v230,v204,v205
mm v[100:103],a[100:103],v[236:239],v[100:103]
ex v206,v206
mm v[68:71],a[100:103],v[232:235],v[68:71]
rd a[100:103],v248 offset:9216
mm v[72:75],a[104:107],v[232:235],v[72:75]
ex v207,v207
mm v[104:107],a[104:107],v[236:239],v[104:107]
rd a[104:107],v248 offset:10240
cv v231,v206,v207
mm v[108:111],a[108:111],v[236:239],v[108:111]
ex v208,v208
mm v[76:79],a[108:111],v[232:235],v[76:79]
rd a[108:111],v248 offset:11264
mm v[80:83],a[112:115],v[232:235],v[80:83]
ex v209,v209
mm v[112:115],a[112:115],v[236:239],v[112:115]
rd a[112:115],v248 offset:12288
mm v[116:119],a[116:119],v[236:239],v[116:119]
ex v210,v210
mm v[84:87],a[116:119],v[232:235],v[84:87]
rd a[116:119],v248 offset:13312
mm v[88:91],a[120:123],v[232:235],v[88:91]
ex v211,v211
mm v[120:123],a[120:123],v[236:239],v[120:123]
rd a[120:123],v248 offset:14336
mm v[124:127],a[124:127],v[236:239],v[124:127]
ex v216,v216
mm v[92:95],a[124:127],v[232:235],v[92:95]
rd a[124:127],v248 offset:15360
v_or_b32 v248, s89, v255
v_smfmac_f32_16x16x64_bf16 a[196:199], a[208:211], v[232:239], v241
wt lgkmcnt(4)
v_smfmac_f32_16x16x64_bf16 a[192:195], a[208:211], v[224:231], v241
rd a[64:67],v248 offset:0
mm v[0:3],a[96:99],v[224:227],v[0:3]
ex v217,v217
mm v[32:35],a[96:99],v[228:231],v[32:35]
rd a[68:71],v248 offset:1024
cv v232,v208,v209
mm v[36:39],a[100:103],v[228:231],v[36:39]
ex v218,v218
mm v[4:7],a[100:103],v[224:227],v[4:7]
rd a[72:75],v248 offset:2048
cv v233,v210,v211
mm v[8:11],a[104:107],v[224:227],v[8:11]
ex v219,v219
mm v[40:43],a[104:107],v[228:231],v[40:43]
rd a[76:79],v248 offset:3072
cv v236,v216,v217
mm v[44:47],a[108:111],v[228:231],v[44:47]
ex v212,v212
mm v[12:15],a[108:111],v[224:227],v[12:15]
rd a[80:83],v248 offset:4096
wt lgkmcnt(5)
mm v[16:19],a[112:115],v[224:227],v[16:19]
ex v213,v213
mm v[48:51],a[112:115],v[228:231],v[48:51]
rd a[84:87],v248 offset:5120
cv v237,v218,v219
mm v[52:55],a[116:119],v[228:231],v[52:55]
ex v214,v214
mm v[20:23],a[116:119],v[224:227],v[20:23]
rd a[88:91],v248 offset:6144
cv v234,v212,v213
mm v[24:27],a[120:123],v[224:227],v[24:27]
ex v215,v215
mm v[56:59],a[120:123],v[228:231],v[56:59]
rd a[92:95],v248 offset:7168
cv v235,v214,v215
mm v[60:63],a[124:127],v[228:231],v[60:63]
ex v220,v220
mm v[28:31],a[124:127],v[224:227],v[28:31]
buffer_load_dword v250, v251, s[64:67], s20 offen sc1
wt lgkmcnt(4)
mm v[192:195],a[64:67],v[128:131],0
ex v221,v221
mm v[192:195],a[68:71],v[132:135],v[192:195]
cv v238,v220,v221
s_add_u32 s89, s89, 16384
mm v[192:195],a[72:75],v[136:139],v[192:195]
ex v222,v222
mm v[192:195],a[76:79],v[140:143],v[192:195]
ld v240,s[56:59],s17 offen lds
mm v[200:203],a[76:79],v[156:159],0
ex v223,v223
s_add_u32 s17, s17, 32768
mm v[200:203],a[72:75],v[152:155],v[200:203]
cv v239,v222,v223
s_add_u32 m0, m0, 1024
mm v[200:203],a[68:71],v[148:151],v[200:203]
ex v192,v192
mm v[200:203],a[64:67],v[144:147],v[200:203]
wt lgkmcnt(0)
s_and_b32 s89, s89, 0x1ffff
mm v[196:199],a[80:83],v[128:131],0
ex v193,v193
mm v[196:199],a[84:87],v[132:135],v[196:199]
cv v224,v192,v193
mm v[196:199],a[88:91],v[136:139],v[196:199]
ex v194,v194
mm v[196:199],a[92:95],v[140:143],v[196:199]
ld v240,s[56:59],s17 offen lds
mm v[204:207],a[92:95],v[156:159],0
ex v195,v195
s_add_u32 s17, s17, 4294942720
mm v[204:207],a[88:91],v[152:155],v[204:207]
cv v225,v194,v195
s_add_u32 m0, m0, 1152
mm v[204:207],a[84:87],v[148:151],v[204:207]
ex v200,v200
mm v[204:207],a[80:83],v[144:147],v[204:207]
ld v240,s[56:59],s17 offen lds
mm v[208:211],a[64:67],v[160:163],0
ex v201,v201
s_add_u32 s17, s17, 32768
mm v[208:211],a[68:71],v[164:167],v[208:211]
cv v228,v200,v201
s_add_u32 m0, m0, 1024
mm v[208:211],a[72:75],v[168:171],v[208:211]
ex v202,v202
mm v[208:211],a[76:79],v[172:175],v[208:211]
ld v240,s[56:59],s17 offen lds
mm v[216:219],a[76:79],v[188:191],0
ex v203,v203
mm v[216:219],a[72:75],v[184:187],v[216:219]
cv v229,v202,v203
mm v[216:219],a[68:71],v[180:183],v[216:219]
ex v196,v196
mm v[216:219],a[64:67],v[176:179],v[216:219]
mm v[212:215],a[80:83],v[160:163],0
ex v197,v197
s_bfe_u32 s17, s4, 262148
mm v[212:215],a[84:87],v[164:167],v[212:215]
cv v226,v196,v197
s_xor_b32 s17, s17, s72
s_lshl_b32 s17, s17, 18
mm v[212:215],a[88:91],v[168:171],v[212:215]
ex v198,v198
s_add_u32 m0, s83, 50688
mm v[212:215],a[92:95],v[172:175],v[212:215]
mm v[220:223],a[92:95],v[188:191],0
ex v199,v199
mm v[220:223],a[88:91],v[184:187],v[220:223]
cv v227,v198,v199
wt vmcnt(4)
v_cmp_eq_u32 s[92:93], v250, v249
s_mov_b32 s94, 16384
mm v[220:223],a[84:87],v[180:183],v[220:223]
ex v204,v204
mm v[220:223],a[80:83],v[176:179],v[220:223]
s_nop 0
s_barrier
mm v[64:67],a[96:99],v[232:235],v[64:67]
ex v205,v205
mm v[96:99],a[96:99],v[236:239],v[96:99]
rd a[96:99],v248 offset:8192
cv v230,v204,v205
s_and_b32 s92, s92, 3
s_andn2_b32 s92, s92, s23
s_cmp_eq_u32 s92, 3
s_cselect_b32 s70, 17408, 0
s_cselect_b32 s84, 3, s23
mm v[100:103],a[100:103],v[236:239],v[100:103]
ex v206,v206
mm v[68:71],a[100:103],v[232:235],v[68:71]
rd a[100:103],v248 offset:9216
ld a[220:223],v255,s[68:71],s94 offen sc1
mm v[72:75],a[104:107],v[232:235],v[72:75]
ex v207,v207
mm v[104:107],a[104:107],v[236:239],v[104:107]
rd a[104:107],v248 offset:10240
cv v231,v206,v207
mm v[108:111],a[108:111],v[236:239],v[108:111]
ex v208,v208
mm v[76:79],a[108:111],v[232:235],v[76:79]
rd a[108:111],v248 offset:11264
ld a[0:3],v255,s[68:71],0 offen offset:0 sc1
mm v[80:83],a[112:115],v[232:235],v[80:83]
ex v209,v209
mm v[112:115],a[112:115],v[236:239],v[112:115]
rd a[112:115],v248 offset:12288
mm v[116:119],a[116:119],v[236:239],v[116:119]
ex v210,v210
mm v[84:87],a[116:119],v[232:235],v[84:87]
rd a[116:119],v248 offset:13312
ld a[4:7],v255,s[68:71],0 offen offset:1024 sc1
mm v[88:91],a[120:123],v[232:235],v[88:91]
ex v211,v211
mm v[120:123],a[120:123],v[236:239],v[120:123]
rd a[120:123],v248 offset:14336
mm v[124:127],a[124:127],v[236:239],v[124:127]
ex v216,v216
mm v[92:95],a[124:127],v[232:235],v[92:95]
rd a[124:127],v248 offset:15360
v_or_b32 v248, s89, v255
buffer_load_dword v250, v251, s[64:67], s20 offen sc1
v_smfmac_f32_16x16x64_bf16 a[196:199], a[208:211], v[232:239], v241
wt lgkmcnt(4)
v_smfmac_f32_16x16x64_bf16 a[192:195], a[208:211], v[224:231], v241
rd a[64:67],v248 offset:0
mm v[0:3],a[96:99],v[224:227],v[0:3]
ex v217,v217
mm v[32:35],a[96:99],v[228:231],v[32:35]
rd a[68:71],v248 offset:1024
cv v232,v208,v209
ld a[8:11],v255,s[68:71],0 offen offset:2048 sc1
mm v[36:39],a[100:103],v[228:231],v[36:39]
ex v218,v218
mm v[4:7],a[100:103],v[224:227],v[4:7]
rd a[72:75],v248 offset:2048
cv v233,v210,v211
mm v[8:11],a[104:107],v[224:227],v[8:11]
ex v219,v219
mm v[40:43],a[104:107],v[228:231],v[40:43]
rd a[76:79],v248 offset:3072
cv v236,v216,v217
ld a[12:15],v255,s[68:71],0 offen offset:3072 sc1
mm v[44:47],a[108:111],v[228:231],v[44:47]
ex v212,v212
mm v[12:15],a[108:111],v[224:227],v[12:15]
rd a[80:83],v248 offset:4096
wt lgkmcnt(5)
mm v[16:19],a[112:115],v[224:227],v[16:19]
ex v213,v213
mm v[48:51],a[112:115],v[228:231],v[48:51]
rd a[84:87],v248 offset:5120
cv v237,v218,v219
ld a[16:19],v255,s[68:71],s95 offen offset:0 sc1
mm v[52:55],a[116:119],v[228:231],v[52:55]
ex v214,v214
mm v[20:23],a[116:119],v[224:227],v[20:23]
rd a[88:91],v248 offset:6144
cv v234,v212,v213
mm v[24:27],a[120:123],v[224:227],v[24:27]
ex v215,v215
mm v[56:59],a[120:123],v[228:231],v[56:59]
rd a[92:95],v248 offset:7168
cv v235,v214,v215
ld a[20:23],v255,s[68:71],s95 offen offset:1024 sc1
mm v[60:63],a[124:127],v[228:231],v[60:63]
ex v220,v220
mm v[28:31],a[124:127],v[224:227],v[28:31]
ld a[24:27],v255,s[68:71],s95 offen offset:2048 sc1
wt lgkmcnt(4)
mm v[192:195],a[64:67],v[128:131],0
ex v221,v221
mm v[192:195],a[68:71],v[132:135],v[192:195]
cv v238,v220,v221
mm v[192:195],a[72:75],v[136:139],v[192:195]
ex v222,v222
mm v[192:195],a[76:79],v[140:143],v[192:195]
ld v240,s[56:59],s17 offen lds
mm v[200:203],a[76:79],v[156:159],0
ex v223,v223
s_add_u32 s17, s17, 32768
mm v[200:203],a[72:75],v[152:155],v[200:203]
cv v239,v222,v223
s_add_u32 m0, m0, 1024
mm v[200:203],a[68:71],v[148:151],v[200:203]
ex v192,v192
mm v[200:203],a[64:67],v[144:147],v[200:203]
wt lgkmcnt(0)
mm v[196:199],a[80:83],v[128:131],0
ex v193,v193
mm v[196:199],a[84:87],v[132:135],v[196:199]
cv v224,v192,v193
wt vmcnt(6)
v_cmp_eq_u32 s[92:93], v250, v249
mm v[196:199],a[88:91],v[136:139],v[196:199]
ex v194,v194
mm v[196:199],a[92:95],v[140:143],v[196:199]
ld v240,s[56:59],s17 offen lds
mm v[204:207],a[92:95],v[156:159],0
ex v195,v195
s_add_u32 s17, s17, 4294942720
mm v[204:207],a[88:91],v[152:155],v[204:207]
cv v225,v194,v195
s_add_u32 m0, m0, 1152
s_and_b32 s92, s92, 3
s_sub_u32 s92, s92, s84
s_cmp_eq_u32 s92, 3
s_cbranch_scc1 .La5_LA_PS0
.La5_LA_PS1:
mm v[204:207],a[84:87],v[148:151],v[204:207]
ex v200,v200
mm v[204:207],a[80:83],v[144:147],v[204:207]
ld v240,s[56:59],s17 offen lds
mm v[208:211],a[64:67],v[160:163],0
ex v201,v201
s_add_u32 s17, s17, 32768
mm v[208:211],a[68:71],v[164:167],v[208:211]
cv v228,v200,v201
s_add_u32 m0, m0, 1024
ld a[28:31],v255,s[68:71],s95 offen offset:3072 sc1
mm v[208:211],a[72:75],v[168:171],v[208:211]
ex v202,v202
mm v[208:211],a[76:79],v[172:175],v[208:211]
ld v240,s[56:59],s17 offen lds
mm v[216:219],a[76:79],v[188:191],0
ex v203,v203
mm v[216:219],a[72:75],v[184:187],v[216:219]
cv v229,v202,v203
ld a[32:35],v255,s[68:71],s96 offen offset:0 sc1
mm v[216:219],a[68:71],v[180:183],v[216:219]
ex v196,v196
mm v[216:219],a[64:67],v[176:179],v[216:219]
mm v[212:215],a[80:83],v[160:163],0
ex v197,v197
mm v[212:215],a[84:87],v[164:167],v[212:215]
cv v226,v196,v197
buffer_load_dword v250, v251, s[64:67], s20 offen sc1
mm v[212:215],a[88:91],v[168:171],v[212:215]
ex v198,v198
mm v[212:215],a[92:95],v[172:175],v[212:215]
ld a[36:39],v255,s[68:71],s96 offen offset:1024 sc1
mm v[220:223],a[92:95],v[188:191],0
ex v199,v199
mm v[220:223],a[88:91],v[184:187],v[220:223]
cv v227,v198,v199
mm v[220:223],a[84:87],v[180:183],v[220:223]
ex v204,v204
mm v[220:223],a[80:83],v[176:179],v[220:223]
ld a[40:43],v255,s[68:71],s96 offen offset:2048 sc1
mm v[64:67],a[96:99],v[232:235],v[64:67]
ex v205,v205
mm v[96:99],a[96:99],v[236:239],v[96:99]
rd a[96:99],v248 offset:8192
cv v230,v204,v205
mm v[100:103],a[100:103],v[236:239],v[100:103]
ex v206,v206
mm v[68:71],a[100:103],v[232:235],v[68:71]
rd a[100:103],v248 offset:9216
ld a[44:47],v255,s[68:71],s96 offen offset:3072 sc1
mm v[72:75],a[104:107],v[232:235],v[72:75]
ex v207,v207
mm v[104:107],a[104:107],v[236:239],v[104:107]
rd a[104:107],v248 offset:10240
cv v231,v206,v207
mm v[108:111],a[108:111],v[236:239],v[108:111]
ex v208,v208
mm v[76:79],a[108:111],v[232:235],v[76:79]
rd a[108:111],v248 offset:11264
ld a[48:51],v255,s[68:71],s97 offen offset:0 sc1
mm v[80:83],a[112:115],v[232:235],v[80:83]
ex v209,v209
mm v[112:115],a[112:115],v[236:239],v[112:115]
rd a[112:115],v248 offset:12288
mm v[116:119],a[116:119],v[236:239],v[116:119]
ex v210,v210
mm v[84:87],a[116:119],v[232:235],v[84:87]
rd a[116:119],v248 offset:13312
ld a[52:55],v255,s[68:71],s97 offen offset:1024 sc1
mm v[88:91],a[120:123],v[232:235],v[88:91]
ex v211,v211
mm v[120:123],a[120:123],v[236:239],v[120:123]
rd a[120:123],v248 offset:14336
mm v[124:127],a[124:127],v[236:239],v[124:127]
ex v216,v216
mm v[92:95],a[124:127],v[232:235],v[92:95]
rd a[124:127],v248 offset:15360
ld a[56:59],v255,s[68:71],s97 offen offset:2048 sc1
v_smfmac_f32_16x16x64_bf16 a[196:199], a[208:211], v[232:239], v241
wt lgkmcnt(4)
v_smfmac_f32_16x16x64_bf16 a[192:195], a[208:211], v[224:231], v241
ld a[60:63],v255,s[68:71],s97 offen offset:3072 sc1
mm v[0:3],a[96:99],v[224:227],v[0:3]
ex v217,v217
mm v[32:35],a[96:99],v[228:231],v[32:35]
cv v232,v208,v209
mm v[36:39],a[100:103],v[228:231],v[36:39]
ex v218,v218
mm v[4:7],a[100:103],v[224:227],v[4:7]
cv v233,v210,v211
mm v[8:11],a[104:107],v[224:227],v[8:11]
ex v219,v219
mm v[40:43],a[104:107],v[228:231],v[40:43]
cv v236,v216,v217
mm v[44:47],a[108:111],v[228:231],v[44:47]
ex v212,v212
wt vmcnt(7)
v_cmp_eq_u32 s[92:93], v250, v249
mm v[12:15],a[108:111],v[224:227],v[12:15]
wt lgkmcnt(0)
mm v[16:19],a[112:115],v[224:227],v[16:19]
ex v213,v213
mm v[48:51],a[112:115],v[228:231],v[48:51]
cv v237,v218,v219
s_and_b32 s92, s92, 3
s_sub_u32 s92, s92, s84
mm v[52:55],a[116:119],v[228:231],v[52:55]
ex v214,v214
mm v[20:23],a[116:119],v[224:227],v[20:23]
cv v234,v212,v213
mm v[24:27],a[120:123],v[224:227],v[24:27]
ex v215,v215
mm v[56:59],a[120:123],v[228:231],v[56:59]
cv v235,v214,v215
mm v[60:63],a[124:127],v[228:231],v[60:63]
ex v220,v220
mm v[28:31],a[124:127],v[224:227],v[28:31]
s_cmp_eq_u32 s92, 3
s_cbranch_scc1 .La5_LA_PS2
.La5_LA_PS3:
s_cmp_lg_u32 s84, 3
s_cbranch_scc1 .La5_LA_PSPIN
; ---- owner: partner-s part added, l checked, O / l leaves; first steps of the O projection
.La5_LA_DC1:
wt vmcnt(8)
mm v[0:3],a[214:217],a[0:3],v[0:3]
ar v250,a192
ar v251,a193
v_add_f32 v250, v250, v251
ar v251,a220
v_add_f32 v250, v250, v251
ar v249,a194
mm v[4:7],a[212:215],a[0:3],v[4:7]
ar v251,a195
v_add_f32 v249, v249, v251
ar v251,a221
v_add_f32 v249, v249, v251
v_subrev_u32 v251, 562036736, v250
mm v[8:11],a[214:217],a[4:7],v[8:11]
v_rcp_f32 v250, v250
v_cmp_le_u32 vcc, 1006632960, v251
v_subrev_u32 v251, 562036736, v249
s_mov_b64 s[92:93], vcc
v_cmp_le_u32 vcc, 1006632960, v251
mm v[12:15],a[212:215],a[4:7],v[12:15]
v_rcp_f32 v249, v249
mm v[16:19],a[214:217],a[8:11],v[16:19]
s_or_b64 vcc, vcc, s[92:93]
s_cbranch_scc1 .La5_LA_SC3
.La5_LA_SR3:
ex v221,v221
ex v222,v222
mm v[20:23],a[212:215],a[8:11],v[20:23]
ex v223,v223
cv v238,v220,v221
v_mul_f32 v0, v0, v250
v_mul_f32 v1, v1, v250
v_mul_f32 v2, v2, v250
mm v[24:27],a[214:217],a[12:15],v[24:27]
v_mul_f32 v3, v3, v250
v_mul_f32 v4, v4, v250
v_mul_f32 v5, v5, v250
v_mul_f32 v6, v6, v250
v_mul_f32 v7, v7, v250
cv v128,v0,v1
mm v[28:31],a[212:215],a[12:15],v[28:31]
cv v129,v2,v3
cv v130,v4,v5
cv v131,v6,v7
st v[128:131],v244,s[44:47],s91 offen offset:0 sc1
cv v239,v222,v223
mm v[32:35],a[214:217],a[16:19],v[32:35]
v_mul_f32 v8, v8, v250
v_mul_f32 v9, v9, v250
v_mul_f32 v10, v10, v250
v_mul_f32 v11, v11, v250
v_mul_f32 v12, v12, v250
v_mul_f32 v13, v13, v250
mm v[36:39],a[212:215],a[16:19],v[36:39]
v_mul_f32 v14, v14, v250
v_mul_f32 v15, v15, v250
cv v132,v8,v9
cv v133,v10,v11
cv v134,v12,v13
cv v135,v14,v15
mm v[40:43],a[214:217],a[20:23],v[40:43]
st v[132:135],v244,s[44:47],s91 offen offset:1024 sc1
v_mul_f32 v16, v16, v250
v_mul_f32 v17, v17, v250
v_mul_f32 v18, v18, v250
.La5_LW5_3:
s_cbranch_execz .La5_LW5_R0
v_mul_f32 v19, v19, v250
mm v[44:47],a[212:215],a[20:23],v[44:47]
v_mul_f32 v20, v20, v250
v_mul_f32 v21, v21, v250
v_mul_f32 v22, v22, v250
v_mul_f32 v23, v23, v250
cv v136,v16,v17
cv v137,v18,v19
mm v[48:51],a[214:217],a[24:27],v[48:51]
cv v138,v20,v21
cv v139,v22,v23
st v[136:139],v244,s[44:47],s91 offen offset:2048 sc1
v_mul_f32 v24, v24, v250
v_mul_f32 v25, v25, v250
mm v[52:55],a[212:215],a[24:27],v[52:55]
v_mul_f32 v26, v26, v250
v_mul_f32 v27, v27, v250
v_mul_f32 v28, v28, v250
v_mul_f32 v29, v29, v250
v_mul_f32 v30, v30, v250
v_mul_f32 v31, v31, v250
mm v[56:59],a[214:217],a[28:31],v[56:59]
cv v140,v24,v25
cv v141,v26,v27
cv v142,v28,v29
cv v143,v30,v31
st v[140:143],v244,s[44:47],s91 offen offset:3072 sc1
mm v[60:63],a[212:215],a[28:31],v[60:63]
v_mul_f32 v32, v32, v249
v_mul_f32 v33, v33, v249
v_mul_f32 v34, v34, v249
v_mul_f32 v35, v35, v249
v_mul_f32 v36, v36, v249
v_mul_f32 v37, v37, v249
v_smfmac_f32_16x16x64_bf16 a[196:199], a[208:211], v[232:239], v241
v_mul_f32 v38, v38, v249
v_mul_f32 v39, v39, v249
cv v144,v32,v33
cv v145,v34,v35
cv v146,v36,v37
mm v[64:67],a[96:99],v[232:235],v[64:67]
cv v147,v38,v39
st v[144:147],v245,s[44:47],s91 offen offset:0 sc1
v_mul_f32 v40, v40, v249
v_mul_f32 v41, v41, v249
v_mul_f32 v42, v42, v249
mm v[96:99],a[96:99],v[236:239],v[96:99]
v_mul_f32 v43, v43, v249
v_mul_f32 v44, v44, v249
v_mul_f32 v45, v45, v249
v_mul_f32 v46, v46, v249
v_mul_f32 v47, v47, v249
cv v148,v40,v41
mm v[100:103],a[100:103],v[236:239],v[100:103]
cv v149,v42,v43
cv v150,v44,v45
cv v151,v46,v47
st v[148:151],v245,s[44:47],s91 offen offset:1024 sc1
v_mul_f32 v48, v48, v249
mm v[68:71],a[100:103],v[232:235],v[68:71]
v_mul_f32 v49, v49, v249
v_mul_f32 v50, v50, v249
v_mul_f32 v51, v51, v249
v_mul_f32 v52, v52, v249
v_mul_f32 v53, v53, v249
v_mul_f32 v54, v54, v249
mm v[72:75],a[104:107],v[232:235],v[72:75]
v_mul_f32 v55, v55, v249
cv v152,v48,v49
cv v153,v50,v51
cv v154,v52,v53
cv v155,v54,v55
st v[152:155],v245,s[44:47],s91 offen offset:2048 sc1
mm v[104:107],a[104:107],v[236:239],v[104:107]
v_mul_f32 v56, v56, v249
v_mul_f32 v57, v57, v249
v_mul_f32 v58, v58, v249
v_mul_f32 v59, v59, v249
v_mul_f32 v60, v60, v249
v_mul_f32 v61, v61, v249
mm v[108:111],a[108:111],v[236:239],v[108:111]
v_mul_f32 v62, v62, v249
v_mul_f32 v63, v63, v249
cv v156,v56,v57
cv v157,v58,v59
cv v158,v60,v61
cv v159,v62,v63
mm v[76:79],a[108:111],v[232:235],v[76:79]
st v[156:159],v245,s[44:47],s91 offen offset:3072 sc1
ar v240,a224
v_xor_b32 v241, 32, v240
v_xor_b32 v242, 64, v240
v_xor_b32 v243, 96, v240
mm v[80:83],a[112:115],v[232:235],v[80:83]
mm v[112:115],a[112:115],v[236:239],v[112:115]
mm v[116:119],a[116:119],v[236:239],v[116:119]
mm v[84:87],a[116:119],v[232:235],v[84:87]
mm v[88:91],a[120:123],v[232:235],v[88:91]
mm v[120:123],a[120:123],v[236:239],v[120:123]
mm v[124:127],a[124:127],v[236:239],v[124:127]
mm v[92:95],a[124:127],v[232:235],v[92:95]
s_nop 0
s_barrier
wt vmcnt(8)
mm v[64:67],a[214:217],a[32:35],v[64:67]
rt v[192:193],v240 offset:16896
rt v[194:195],v240 offset:17920
mm v[68:71],a[212:215],a[32:35],v[68:71]
rt v[196:197],v240 offset:25344
rt v[198:199],v240 offset:26368
mm v[72:75],a[214:217],a[36:39],v[72:75]
rt v[200:201],v241 offset:16896
rt v[202:203],v241 offset:17920
ar v250,a196
ar v251,a197
v_add_f32 v250, v250, v251
ar v251,a222
mm v[76:79],a[212:215],a[36:39],v[76:79]
rt v[204:205],v241 offset:25344
rt v[206:207],v241 offset:26368
v_add_f32 v250, v250, v251
ar v249,a198
ar v251,a199
v_add_f32 v249, v249, v251
mm v[80:83],a[214:217],a[40:43],v[80:83]
rt v[208:209],v242 offset:16896
rt v[210:211],v242 offset:17920
ar v251,a223
v_add_f32 v249, v249, v251
v_subrev_u32 v251, 562036736, v250
mm v[84:87],a[212:215],a[40:43],v[84:87]
rt v[212:213],v242 offset:25344
rt v[214:215],v242 offset:26368
v_rcp_f32 v250, v250
v_cmp_le_u32 vcc, 1006632960, v251
v_subrev_u32 v251, 562036736, v249
mm v[88:91],a[214:217],a[44:47],v[88:91]
rt v[216:217],v243 offset:16896
rt v[218:219],v243 offset:17920
s_mov_b64 s[92:93], vcc
v_cmp_le_u32 vcc, 1006632960, v251
v_rcp_f32 v249, v249
mm v[92:95],a[212:215],a[44:47],v[92:95]
rt v[220:221],v243 offset:25344
rt v[222:223],v243 offset:26368
s_or_b64 vcc, vcc, s[92:93]
s_cbranch_scc1 .La5_LA_SC4
.La5_LA_SR4:
v_mul_f32 v64, v64, v250
v_mul_f32 v65, v65, v250
mm v[96:99],a[214:217],a[48:51],v[96:99]
v_mul_f32 v66, v66, v250
v_mul_f32 v67, v67, v250
v_mul_f32 v68, v68, v250
v_mul_f32 v69, v69, v250
v_mul_f32 v70, v70, v250
v_mul_f32 v71, v71, v250
mm v[100:103],a[212:215],a[48:51],v[100:103]
cv v160,v64,v65
cv v161,v66,v67
cv v162,v68,v69
cv v163,v70,v71
st v[160:163],v246,s[44:47],s91 offen offset:0 sc1
mm v[104:107],a[214:217],a[52:55],v[104:107]
v_mul_f32 v72, v72, v250
v_mul_f32 v73, v73, v250
v_mul_f32 v74, v74, v250
v_mul_f32 v75, v75, v250
v_mul_f32 v76, v76, v250
v_mul_f32 v77, v77, v250
mm v[108:111],a[212:215],a[52:55],v[108:111]
v_mul_f32 v78, v78, v250
v_mul_f32 v79, v79, v250
cv v164,v72,v73
cv v165,v74,v75
cv v166,v76,v77
cv v167,v78,v79
mm v[112:115],a[214:217],a[56:59],v[112:115]
st v[164:167],v246,s[44:47],s91 offen offset:1024 sc1
v_mul_f32 v80, v80, v250
v_mul_f32 v81, v81, v250
v_mul_f32 v82, v82, v250
v_mul_f32 v83, v83, v250
mm v[116:119],a[212:215],a[56:59],v[116:119]
v_mul_f32 v84, v84, v250
v_mul_f32 v85, v85, v250
v_mul_f32 v86, v86, v250
v_mul_f32 v87, v87, v250
cv v168,v80,v81
cv v169,v82,v83
mm v[120:123],a[214:217],a[60:63],v[120:123]
cv v170,v84,v85
cv v171,v86,v87
st v[168:171],v246,s[44:47],s91 offen offset:2048 sc1
v_mul_f32 v88, v88, v250
v_mul_f32 v89, v89, v250
mm v[124:127],a[212:215],a[60:63],v[124:127]
wt lgkmcnt(12)
v_mul_f32 v90, v90, v250
v_mul_f32 v91, v91, v250
v_mul_f32 v92, v92, v250
v_mul_f32 v93, v93, v250
v_mul_f32 v94, v94, v250
mm v[0:3],v[192:195],v[128:131],0
v_mul_f32 v95, v95, v250
cv v172,v88,v89
cv v173,v90,v91
cv v174,v92,v93
cv v175,v94,v95
st v[172:175],v246,s[44:47],s91 offen offset:3072 sc1
mm v[0:3],v[196:199],v[132:135],v[0:3]
v_mul_f32 v96, v96, v249
v_mul_f32 v97, v97, v249
v_mul_f32 v98, v98, v249
v_mul_f32 v99, v99, v249
v_mul_f32 v100, v100, v249
v_mul_f32 v101, v101, v249
mm v[32:35],v[196:199],v[148:151],0
wt lgkmcnt(8)
v_mul_f32 v102, v102, v249
v_mul_f32 v103, v103, v249
cv v176,v96,v97
cv v177,v98,v99
cv v178,v100,v101
mm v[32:35],v[192:195],v[144:147],v[32:35]
rt v[196:197],v240 offset:17024
rt v[198:199],v240 offset:18048
cv v179,v102,v103
st v[176:179],v247,s[44:47],s91 offen offset:0 sc1
v_mul_f32 v104, v104, v249
mm v[36:39],v[200:203],v[144:147],0
v_mul_f32 v105, v105, v249
v_mul_f32 v106, v106, v249
v_mul_f32 v107, v107, v249
v_mul_f32 v108, v108, v249
v_mul_f32 v109, v109, v249
v_mul_f32 v110, v110, v249
mm v[36:39],v[204:207],v[148:151],v[36:39]
rt v[192:193],v240 offset:25472
rt v[194:195],v240 offset:26496
v_mul_f32 v111, v111, v249
cv v180,v104,v105
cv v181,v106,v107
cv v182,v108,v109
mm v[4:7],v[204:207],v[132:135],0
wt lgkmcnt(4)
cv v183,v110,v111
st v[180:183],v247,s[44:47],s91 offen offset:1024 sc1
v_mul_f32 v112, v112, v249
v_mul_f32 v113, v113, v249
mm v[4:7],v[200:203],v[128:131],v[4:7]
rt v[204:205],v241 offset:17024
rt v[206:207],v241 offset:18048
v_mul_f32 v114, v114, v249
v_mul_f32 v115, v115, v249
v_mul_f32 v116, v116, v249
v_mul_f32 v117, v117, v249
mm v[8:11],v[208:211],v[128:131],0
v_mul_f32 v118, v118, v249
v_mul_f32 v119, v119, v249
cv v184,v112,v113
cv v185,v114,v115
cv v186,v116,v117
cv v187,v118,v119
mm v[8:11],v[212:215],v[132:135],v[8:11]
rt v[200:201],v241 offset:25472
rt v[202:203],v241 offset:26496
st v[184:187],v247,s[44:47],s91 offen offset:2048 sc1
v_mul_f32 v120, v120, v249
v_mul_f32 v121, v121, v249
mm v[40:43],v[212:215],v[148:151],0
v_mul_f32 v122, v122, v249
v_mul_f32 v123, v123, v249
v_mul_f32 v124, v124, v249
v_mul_f32 v125, v125, v249
v_mul_f32 v126, v126, v249
v_mul_f32 v127, v127, v249
mm v[40:43],v[208:211],v[144:147],v[40:43]
rt v[212:213],v242 offset:17024
rt v[214:215],v242 offset:18048
cv v188,v120,v121
cv v189,v122,v123
cv v190,v124,v125
cv v191,v126,v127
mm v[44:47],v[216:219],v[144:147],0
st v[188:191],v247,s[44:47],s91 offen offset:3072 sc1
mm v[44:47],v[220:223],v[148:151],v[44:47]
rt v[208:209],v242 offset:25472
rt v[210:211],v242 offset:26496
mm v[12:15],v[220:223],v[132:135],0
wt lgkmcnt(4)
mm v[12:15],v[216:219],v[128:131],v[12:15]
rt v[220:221],v243 offset:17024
rt v[222:223],v243 offset:18048
mm v[16:19],v[196:199],v[128:131],0
ar v224,a228
v_add_u32 v225, 0x8000, v224
v_add_u32 v226, 0x2000, v224
v_add_u32 v227, 0xa000, v224
mm v[16:19],v[192:195],v[132:135],v[16:19]
rt v[216:217],v243 offset:25472
rt v[218:219],v243 offset:26496
ar v248,a225
ar v228,a229
mm v[48:51],v[192:195],v[148:151],0
ar v229,a230
mm v[48:51],v[196:199],v[144:147],v[48:51]
rt v[192:193],v240 offset:33792
rt v[194:195],v240 offset:34816
mm v[52:55],v[204:207],v[144:147],0
mm v[52:55],v[200:203],v[148:151],v[52:55]
rt v[196:197],v240 offset:42240
rt v[198:199],v240 offset:43264
mm v[20:23],v[200:203],v[132:135],0
wt lgkmcnt(4)
mm v[20:23],v[204:207],v[128:131],v[20:23]
rt v[200:201],v241 offset:33792
rt v[202:203],v241 offset:34816
mm v[24:27],v[212:215],v[128:131],0
mm v[24:27],v[208:211],v[132:135],v[24:27]
rt v[204:205],v241 offset:42240
rt v[206:207],v241 offset:43264
mm v[56:59],v[208:211],v[148:151],0
mm v[56:59],v[212:215],v[144:147],v[56:59]
rt v[208:209],v242 offset:33792
rt v[210:211],v242 offset:34816
mm v[60:63],v[220:223],v[144:147],0
mm v[60:63],v[216:219],v[148:151],v[60:63]
rt v[212:213],v242 offset:42240
rt v[214:215],v242 offset:43264
mm v[28:31],v[216:219],v[132:135],0
wt lgkmcnt(4)
mm v[28:31],v[220:223],v[128:131],v[28:31]
rt v[216:217],v243 offset:33792
rt v[218:219],v243 offset:34816
mm v[0:3],v[192:195],v[136:139],v[0:3]
ar v249,a231
s_bitcmp1_b32 2863311530, s2
s_cbranch_scc1 .La5_LO_VC1
s_getpc_b64 s[24:25]
.La5_LW3_P1:
s_add_u32 s24, s24, .La5_LW3_R1-.La5_LW3_P1
s_addc_u32 s25, s25, 0
s_mov_b64 exec, 0
s_cmp_eq_u32 s3, 0
s_cbranch_scc1 .La5_LW3_0
s_cmp_eq_u32 s3, 1
s_cbranch_scc1 .La5_LW3_1
s_cmp_eq_u32 s3, 2
s_cbranch_scc1 .La5_LW3_2
s_branch .La5_LW3_3
.La5_LW3_R1:
s_mov_b64 exec, -1
.La5_LO_VE1:
wt vmcnt(0)
buffer_store_dwordx2 v[252:253], v249, s[64:67], s6 offen offset:0 sc1
mm v[0:3],v[196:199],v[140:143],v[0:3]
rt v[220:221],v243 offset:42240
rt v[222:223],v243 offset:43264
mm v[32:35],v[196:199],v[156:159],v[32:35]
mm v[32:35],v[192:195],v[152:155],v[32:35]
rt v[196:197],v240 offset:33920
rt v[198:199],v240 offset:34944
mm v[36:39],v[200:203],v[152:155],v[36:39]
mm v[36:39],v[204:207],v[156:159],v[36:39]
rt v[192:193],v240 offset:42368
rt v[194:195],v240 offset:43392
mm v[4:7],v[204:207],v[140:143],v[4:7]
wt lgkmcnt(4)
mm v[4:7],v[200:203],v[136:139],v[4:7]
rt v[204:205],v241 offset:33920
rt v[206:207],v241 offset:34944
mm v[8:11],v[208:211],v[136:139],v[8:11]
mm v[8:11],v[212:215],v[140:143],v[8:11]
rt v[200:201],v241 offset:42368
rt v[202:203],v241 offset:43392
mm v[40:43],v[212:215],v[156:159],v[40:43]
mm v[40:43],v[208:211],v[152:155],v[40:43]
rt v[212:213],v242 offset:33920
rt v[214:215],v242 offset:34944
mm v[44:47],v[216:219],v[152:155],v[44:47]
mm v[44:47],v[220:223],v[156:159],v[44:47]
rt v[208:209],v242 offset:42368
rt v[210:211],v242 offset:43392
mm v[12:15],v[220:223],v[140:143],v[12:15]
wt lgkmcnt(4)
mm v[12:15],v[216:219],v[136:139],v[12:15]
rt v[220:221],v243 offset:33920
rt v[222:223],v243 offset:34944
mm v[16:19],v[196:199],v[136:139],v[16:19]
mm v[16:19],v[192:195],v[140:143],v[16:19]
rt v[216:217],v243 offset:42368
rt v[218:219],v243 offset:43392
mm v[48:51],v[192:195],v[156:159],v[48:51]
mm v[48:51],v[196:199],v[152:155],v[48:51]
rt v[192:193],v240 offset:16896
rt v[194:195],v240 offset:17920
mm v[52:55],v[204:207],v[152:155],v[52:55]
mm v[52:55],v[200:203],v[156:159],v[52:55]
rt v[196:197],v240 offset:25344
rt v[198:199],v240 offset:26368
mm v[20:23],v[200:203],v[140:143],v[20:23]
wt lgkmcnt(4)
mm v[20:23],v[204:207],v[136:139],v[20:23]
rt v[200:201],v241 offset:16896
rt v[202:203],v241 offset:17920
mm v[24:27],v[212:215],v[136:139],v[24:27]
mm v[24:27],v[208:211],v[140:143],v[24:27]
rt v[204:205],v241 offset:25344
rt v[206:207],v241 offset:26368
mm v[56:59],v[208:211],v[156:159],v[56:59]
mm v[56:59],v[212:215],v[152:155],v[56:59]
rt v[208:209],v242 offset:16896
rt v[210:211],v242 offset:17920
mm v[60:63],v[220:223],v[152:155],v[60:63]
mm v[60:63],v[216:219],v[156:159],v[60:63]
rt v[212:213],v242 offset:25344
rt v[214:215],v242 offset:26368
mm v[28:31],v[216:219],v[140:143],v[28:31]
wt lgkmcnt(4)
mm v[28:31],v[220:223],v[136:139],v[28:31]
rt v[216:217],v243 offset:16896
rt v[218:219],v243 offset:17920
s_cmp_lg_u32 s30, 0
s_nop 0
s_nop 0
s_cbranch_scc1 .La5_LO_W
; ---- O projection: 256 x 128 tile over eight heads
.La5_LO_START:
.La5_LW6_7:
s_cbranch_execz .La5_LW6_R0
s_bitcmp1_b32 2863311530, s2
s_cbranch_scc1 .La5_LO_SC
; ---- O projection, even XCDs: eight and a half heads
.La5_LO_SB:
mm v[64:67],v[192:195],v[160:163],0
buffer_load_dwordx2 v[250:251], v248, s[64:67], s5 offen sc1
mm v[64:67],v[196:199],v[164:167],v[64:67]
rt v[220:221],v243 offset:25344
rt v[222:223],v243 offset:26368
mm v[96:99],v[196:199],v[180:183],0
s_add_u32 m0, s83, 0
s_bfe_u32 s80, s4, 262148
mm v[96:99],v[192:195],v[176:179],v[96:99]
rt v[196:197],v240 offset:17024
rt v[198:199],v240 offset:18048
s_xor_b32 s80, s80, s72
s_lshl_b32 s80, s80, 18
s_add_u32 s80, s80, 131072
mm v[100:103],v[200:203],v[176:179],0
ld v224,s[56:59],s80 offen lds
mm v[100:103],v[204:207],v[180:183],v[100:103]
rt v[192:193],v240 offset:25472
rt v[194:195],v240 offset:26496
mm v[68:71],v[204:207],v[164:167],0
s_add_u32 m0, s83, 1024
wt lgkmcnt(4)
mm v[68:71],v[200:203],v[160:163],v[68:71]
rt v[204:205],v241 offset:17024
rt v[206:207],v241 offset:18048
mm v[72:75],v[208:211],v[160:163],0
ld v225,s[56:59],s80 offen lds
mm v[72:75],v[212:215],v[164:167],v[72:75]
rt v[200:201],v241 offset:25472
rt v[202:203],v241 offset:26496
mm v[104:107],v[212:215],v[180:183],0
s_add_u32 m0, s83, 2176
s_bfe_u32 s53, s4, 262148
mm v[104:107],v[208:211],v[176:179],v[104:107]
rt v[212:213],v242 offset:17024
rt v[214:215],v242 offset:18048
mm v[108:111],v[216:219],v[176:179],0
ld v226,s[56:59],s80 offen lds
mm v[108:111],v[220:223],v[180:183],v[108:111]
rt v[208:209],v242 offset:25472
rt v[210:211],v242 offset:26496
mm v[76:79],v[220:223],v[164:167],0
s_add_u32 m0, s83, 3200
wt lgkmcnt(4)
mm v[76:79],v[216:219],v[160:163],v[76:79]
rt v[220:221],v243 offset:17024
rt v[222:223],v243 offset:18048
mm v[80:83],v[196:199],v[160:163],0
ld v227,s[56:59],s80 offen lds
mm v[80:83],v[192:195],v[164:167],v[80:83]
rt v[216:217],v243 offset:25472
rt v[218:219],v243 offset:26496
mm v[112:115],v[192:195],v[180:183],0
s_add_u32 m0, s83, 118272
s_bfe_u32 s80, s4, 262152
mm v[112:115],v[196:199],v[176:179],v[112:115]
rt v[192:193],v240 offset:33792
rt v[194:195],v240 offset:34816
s_xor_b32 s80, s80, s72
s_lshl_b32 s80, s80, 18
mm v[116:119],v[204:207],v[176:179],0
ld v224,s[56:59],s80 offen lds
mm v[116:119],v[200:203],v[180:183],v[116:119]
rt v[196:197],v240 offset:42240
rt v[198:199],v240 offset:43264
mm v[84:87],v[200:203],v[164:167],0
s_add_u32 m0, s83, 119296
wt lgkmcnt(4)
mm v[84:87],v[204:207],v[160:163],v[84:87]
rt v[200:201],v241 offset:33792
rt v[202:203],v241 offset:34816
mm v[88:91],v[212:215],v[160:163],0
ld v225,s[56:59],s80 offen lds
mm v[88:91],v[208:211],v[164:167],v[88:91]
rt v[204:205],v241 offset:42240
rt v[206:207],v241 offset:43264
mm v[120:123],v[208:211],v[180:183],0
s_add_u32 m0, s83, 120448
s_xor_b32 s53, s53, s72
mm v[120:123],v[212:215],v[176:179],v[120:123]
rt v[208:209],v242 offset:33792
rt v[210:211],v242 offset:34816
mm v[124:127],v[220:223],v[176:179],0
ld v226,s[56:59],s80 offen lds
mm v[124:127],v[216:219],v[180:183],v[124:127]
rt v[212:213],v242 offset:42240
rt v[214:215],v242 offset:43264
mm v[92:95],v[216:219],v[164:167],0
wt vmcnt(7)
v_cmp_eq_u64 vcc, v[250:251], v[252:253]
s_nop 0
s_bitcmp1_b32 vcc_lo, 1
s_cbranch_scc0 .La5_LO_NB1
.La5_LO_KB1:
wt lgkmcnt(4)
mm v[92:95],v[220:223],v[160:163],v[92:95]
rt v[216:217],v243 offset:33792
rt v[218:219],v243 offset:34816
s_lshl_b32 s53, s53, 13
mm v[64:67],v[192:195],v[168:171],v[64:67]
ld a[0:3],v244,s[44:47],s53 offen offset:0 sc1
mm v[64:67],v[196:199],v[172:175],v[64:67]
rt v[220:221],v243 offset:42240
rt v[222:223],v243 offset:43264
mm v[96:99],v[196:199],v[188:191],v[96:99]
ld a[4:7],v245,s[44:47],s53 offen offset:0 sc1
mm v[96:99],v[192:195],v[184:187],v[96:99]
rt v[196:197],v240 offset:33920
rt v[198:199],v240 offset:34944
mm v[100:103],v[200:203],v[184:187],v[100:103]
ld a[8:11],v246,s[44:47],s53 offen offset:0 sc1
mm v[100:103],v[204:207],v[188:191],v[100:103]
rt v[192:193],v240 offset:42368
rt v[194:195],v240 offset:43392
mm v[68:71],v[204:207],v[172:175],v[68:71]
wt lgkmcnt(4)
s_bfe_u32 s54, s4, 262152
mm v[68:71],v[200:203],v[168:171],v[68:71]
rt v[204:205],v241 offset:33920
rt v[206:207],v241 offset:34944
mm v[72:75],v[208:211],v[168:171],v[72:75]
ld a[12:15],v247,s[44:47],s53 offen offset:0 sc1
mm v[72:75],v[212:215],v[172:175],v[72:75]
rt v[200:201],v241 offset:42368
rt v[202:203],v241 offset:43392
mm v[104:107],v[212:215],v[188:191],v[104:107]
ld a[16:19],v244,s[44:47],s53 offen offset:1024 sc1
mm v[104:107],v[208:211],v[184:187],v[104:107]
rt v[212:213],v242 offset:33920
rt v[214:215],v242 offset:34944
mm v[108:111],v[216:219],v[184:187],v[108:111]
ld a[20:23],v245,s[44:47],s53 offen offset:1024 sc1
mm v[108:111],v[220:223],v[188:191],v[108:111]
rt v[208:209],v242 offset:42368
rt v[210:211],v242 offset:43392
mm v[76:79],v[220:223],v[172:175],v[76:79]
s_nop 0
s_barrier
wt lgkmcnt(4)
mm v[76:79],v[216:219],v[168:171],v[76:79]
rt v[220:221],v243 offset:33920
rt v[222:223],v243 offset:34944
mm v[80:83],v[196:199],v[168:171],v[80:83]
ld a[24:27],v246,s[44:47],s53 offen offset:1024 sc1
mm v[80:83],v[192:195],v[172:175],v[80:83]
rt v[216:217],v243 offset:42368
rt v[218:219],v243 offset:43392
mm v[112:115],v[192:195],v[188:191],v[112:115]
ld a[28:31],v247,s[44:47],s53 offen offset:1024 sc1
mm v[112:115],v[196:199],v[184:187],v[112:115]
rt v[192:193],v240 offset:50688
rt v[194:195],v240 offset:51712
mm v[116:119],v[204:207],v[184:187],v[116:119]
ld a[32:35],v244,s[44:47],s53 offen offset:2048 sc1
mm v[116:119],v[200:203],v[188:191],v[116:119]
rt v[196:197],v240 offset:59136
rt v[198:199],v240 offset:60160
mm v[84:87],v[200:203],v[172:175],v[84:87]
wt lgkmcnt(4)
s_xor_b32 s54, s54, s72
mm v[84:87],v[204:207],v[168:171],v[84:87]
rt v[200:201],v241 offset:50688
rt v[202:203],v241 offset:51712
mm v[88:91],v[212:215],v[168:171],v[88:91]
ld a[36:39],v245,s[44:47],s53 offen offset:2048 sc1
mm v[88:91],v[208:211],v[172:175],v[88:91]
rt v[204:205],v241 offset:59136
rt v[206:207],v241 offset:60160
mm v[120:123],v[208:211],v[188:191],v[120:123]
ld a[40:43],v246,s[44:47],s53 offen offset:2048 sc1
mm v[120:123],v[212:215],v[184:187],v[120:123]
rt v[208:209],v242 offset:50688
rt v[210:211],v242 offset:51712
mm v[124:127],v[220:223],v[184:187],v[124:127]
.La5_LW3_0:
s_cbranch_execz .La5_LW3_4
ld a[44:47],v247,s[44:47],s53 offen offset:2048 sc1
mm v[124:127],v[216:219],v[188:191],v[124:127]
rt v[212:213],v242 offset:59136
rt v[214:215],v242 offset:60160
mm v[92:95],v[216:219],v[172:175],v[92:95]
wt vmcnt(4) lgkmcnt(4)
s_lshl_b32 s54, s54, 13
mm v[92:95],v[220:223],v[168:171],v[92:95]
rt v[216:217],v243 offset:50688
rt v[218:219],v243 offset:51712
mm v[0:3],v[192:195],a[0:3],v[0:3]
s_bfe_u32 s55, s4, 262156
s_xor_b32 s55, s55, s72
mm v[0:3],v[196:199],a[16:19],v[0:3]
ld a[48:51],v244,s[44:47],s53 offen offset:3072 sc1
mm v[32:35],v[196:199],a[20:23],v[32:35]
s_lshl_b32 s55, s55, 13
s_bfe_u32 s52, s4, 262160
mm v[32:35],v[192:195],a[4:7],v[32:35]
rt v[220:221],v243 offset:59136
rt v[222:223],v243 offset:60160
mm v[64:67],v[192:195],a[8:11],v[64:67]
s_xor_b32 s52, s52, s72
s_lshl_b32 s52, s52, 13
mm v[64:67],v[196:199],a[24:27],v[64:67]
ld a[52:55],v245,s[44:47],s53 offen offset:3072 sc1
mm v[96:99],v[196:199],a[28:31],v[96:99]
mm v[96:99],v[192:195],a[12:15],v[96:99]
rt v[196:197],v240 offset:50816
rt v[198:199],v240 offset:51840
mm v[100:103],v[200:203],a[12:15],v[100:103]
mm v[100:103],v[204:207],a[28:31],v[100:103]
buffer_load_dwordx2 v[250:251], v248, s[64:67], s5 offen sc1
mm v[68:71],v[204:207],a[24:27],v[68:71]
mm v[68:71],v[200:203],a[8:11],v[68:71]
rt v[192:193],v240 offset:59264
rt v[194:195],v240 offset:60288
mm v[36:39],v[200:203],a[4:7],v[36:39]
mm v[36:39],v[204:207],a[20:23],v[36:39]
ld a[56:59],v246,s[44:47],s53 offen offset:3072 sc1
mm v[4:7],v[204:207],a[16:19],v[4:7]
wt lgkmcnt(4)
mm v[4:7],v[200:203],a[0:3],v[4:7]
rt v[204:205],v241 offset:50816
rt v[206:207],v241 offset:51840
mm v[8:11],v[208:211],a[0:3],v[8:11]
mm v[8:11],v[212:215],a[16:19],v[8:11]
ld a[60:63],v247,s[44:47],s53 offen offset:3072 sc1
mm v[40:43],v[212:215],a[20:23],v[40:43]
s_bfe_u32 s53, s4, 262164
s_xor_b32 s53, s53, s72
mm v[40:43],v[208:211],a[4:7],v[40:43]
rt v[200:201],v241 offset:59264
rt v[202:203],v241 offset:60288
mm v[72:75],v[208:211],a[8:11],v[72:75]
s_add_u32 m0, s83, 121472
s_lshl_b32 s53, s53, 13
mm v[72:75],v[212:215],a[24:27],v[72:75]
ld v227,s[56:59],s80 offen lds
mm v[104:107],v[212:215],a[28:31],v[104:107]
s_add_u32 s80, s80, 131072
mm v[104:107],v[208:211],a[12:15],v[104:107]
rt v[212:213],v242 offset:50816
rt v[214:215],v242 offset:51840
mm v[108:111],v[216:219],a[12:15],v[108:111]
s_add_u32 m0, s83, 67584
mm v[108:111],v[220:223],a[28:31],v[108:111]
ld v224,s[56:59],s80 offen lds
mm v[76:79],v[220:223],a[24:27],v[76:79]
mm v[76:79],v[216:219],a[8:11],v[76:79]
rt v[208:209],v242 offset:59264
rt v[210:211],v242 offset:60288
mm v[44:47],v[216:219],a[4:7],v[44:47]
s_add_u32 m0, s83, 68608
mm v[44:47],v[220:223],a[20:23],v[44:47]
ld v225,s[56:59],s80 offen lds
mm v[12:15],v[220:223],a[16:19],v[12:15]
wt lgkmcnt(4)
mm v[12:15],v[216:219],a[0:3],v[12:15]
rt v[220:221],v243 offset:50816
rt v[222:223],v243 offset:51840
mm v[16:19],v[196:199],a[0:3],v[16:19]
s_add_u32 m0, s83, 69760
mm v[16:19],v[192:195],a[16:19],v[16:19]
ld v226,s[56:59],s80 offen lds
mm v[48:51],v[192:195],a[20:23],v[48:51]
mm v[48:51],v[196:199],a[4:7],v[48:51]
rt v[216:217],v243 offset:59264
rt v[218:219],v243 offset:60288
mm v[80:83],v[196:199],a[8:11],v[80:83]
s_add_u32 m0, s83, 70784
s_nop 0
s_barrier
mm v[80:83],v[192:195],a[24:27],v[80:83]
ld v227,s[56:59],s80 offen lds
mm v[112:115],v[192:195],a[28:31],v[112:115]
s_bfe_u32 s80, s4, 262156
s_xor_b32 s80, s80, s72
mm v[112:115],v[196:199],a[12:15],v[112:115]
rt v[192:193],v240 offset:0
rt v[194:195],v240 offset:1024
mm v[116:119],v[204:207],a[12:15],v[116:119]
s_add_u32 m0, s83, 84480
s_lshl_b32 s80, s80, 18
mm v[116:119],v[200:203],a[28:31],v[116:119]
ld v224,s[56:59],s80 offen lds
mm v[84:87],v[200:203],a[24:27],v[84:87]
mm v[84:87],v[204:207],a[8:11],v[84:87]
rt v[196:197],v240 offset:8448
rt v[198:199],v240 offset:9472
mm v[52:55],v[204:207],a[4:7],v[52:55]
s_add_u32 m0, s83, 85504
mm v[52:55],v[200:203],a[20:23],v[52:55]
ld v225,s[56:59],s80 offen lds
mm v[20:23],v[200:203],a[16:19],v[20:23]
wt lgkmcnt(4)
mm v[20:23],v[204:207],a[0:3],v[20:23]
rt v[200:201],v241 offset:0
rt v[202:203],v241 offset:1024
mm v[24:27],v[212:215],a[0:3],v[24:27]
s_add_u32 m0, s83, 86656
mm v[24:27],v[208:211],a[16:19],v[24:27]
ld v226,s[56:59],s80 offen lds
mm v[56:59],v[208:211],a[20:23],v[56:59]
wt vmcnt(10)
v_cmp_eq_u64 vcc, v[250:251], v[252:253]
mm v[56:59],v[212:215],a[4:7],v[56:59]
rt v[204:205],v241 offset:8448
rt v[206:207],v241 offset:9472
mm v[88:91],v[212:215],a[8:11],v[88:91]
s_add_u32 m0, s83, 87680
mm v[88:91],v[208:211],a[24:27],v[88:91]
ld v227,s[56:59],s80 offen lds
mm v[120:123],v[208:211],a[28:31],v[120:123]
s_bitcmp1_b32 vcc_lo, 2
s_nop 0
s_nop 0
s_cbranch_scc0 .La5_LO_NB2
.La5_LO_KB2:
mm v[120:123],v[212:215],a[12:15],v[120:123]
rt v[208:209],v242 offset:0
rt v[210:211],v242 offset:1024
mm v[124:127],v[220:223],a[12:15],v[124:127]
s_add_u32 s80, s80, 131072
mm v[124:127],v[216:219],a[28:31],v[124:127]
ld a[12:15],v244,s[44:47],s54 offen offset:0 sc1
mm v[92:95],v[216:219],a[24:27],v[92:95]
mm v[92:95],v[220:223],a[8:11],v[92:95]
rt v[212:213],v242 offset:8448
rt v[214:215],v242 offset:9472
mm v[60:63],v[220:223],a[4:7],v[60:63]
mm v[60:63],v[216:219],a[20:23],v[60:63]
mm v[28:31],v[216:219],a[16:19],v[28:31]
wt vmcnt(10) lgkmcnt(4)
mm v[28:31],v[220:223],a[0:3],v[28:31]
rt v[216:217],v243 offset:0
rt v[218:219],v243 offset:1024
mm v[0:3],v[192:195],a[32:35],v[0:3]
mm v[0:3],v[196:199],a[48:51],v[0:3]
ld a[0:3],v245,s[44:47],s54 offen offset:0 sc1
mm v[32:35],v[196:199],a[52:55],v[32:35]
mm v[32:35],v[192:195],a[36:39],v[32:35]
rt v[220:221],v243 offset:8448
rt v[222:223],v243 offset:9472
mm v[64:67],v[192:195],a[40:43],v[64:67]
mm v[64:67],v[196:199],a[56:59],v[64:67]
ld a[4:7],v246,s[44:47],s54 offen offset:0 sc1
mm v[96:99],v[196:199],a[60:63],v[96:99]
mm v[96:99],v[192:195],a[44:47],v[96:99]
rt v[196:197],v240 offset:128
rt v[198:199],v240 offset:1152
mm v[100:103],v[200:203],a[44:47],v[100:103]
s_mov_b32 s86, 0x05040100
s_mov_b32 s87, 0x07060302
mm v[100:103],v[204:207],a[60:63],v[100:103]
ld a[8:11],v247,s[44:47],s54 offen offset:0 sc1
mm v[68:71],v[204:207],a[56:59],v[68:71]
s_nop 0
ar v230,a128
mm v[68:71],v[200:203],a[40:43],v[68:71]
rt v[192:193],v240 offset:8576
rt v[194:195],v240 offset:9600
mm v[36:39],v[200:203],a[36:39],v[36:39]
v_add_u32 v240, 67584, v240
ar v231,a132
mm v[36:39],v[204:207],a[52:55],v[36:39]
ld a[16:19],v244,s[44:47],s54 offen offset:1024 sc1
mm v[4:7],v[204:207],a[48:51],v[4:7]
wt lgkmcnt(4)
v_perm_b32 v232, v231, v230, s86
mm v[4:7],v[200:203],a[32:35],v[4:7]
rt v[204:205],v241 offset:128
rt v[206:207],v241 offset:1152
mm v[8:11],v[208:211],a[32:35],v[8:11]
v_perm_b32 v233, v231, v230, s87
aw a96,v232
mm v[8:11],v[212:215],a[48:51],v[8:11]
ld a[20:23],v245,s[44:47],s54 offen offset:1024 sc1
mm v[40:43],v[212:215],a[52:55],v[40:43]
aw a100,v233
ar v234,a136
mm v[40:43],v[208:211],a[36:39],v[40:43]
rt v[200:201],v241 offset:8576
rt v[202:203],v241 offset:9600
mm v[72:75],v[208:211],a[40:43],v[72:75]
v_add_u32 v241, 67584, v241
ar v235,a140
mm v[72:75],v[212:215],a[56:59],v[72:75]
ld a[24:27],v246,s[44:47],s54 offen offset:1024 sc1
mm v[104:107],v[212:215],a[60:63],v[104:107]
v_perm_b32 v236, v235, v234, s86
v_perm_b32 v237, v235, v234, s87
mm v[104:107],v[208:211],a[44:47],v[104:107]
rt v[212:213],v242 offset:128
rt v[214:215],v242 offset:1152
mm v[108:111],v[216:219],a[44:47],v[108:111]
aw a97,v236
aw a101,v237
mm v[108:111],v[220:223],a[60:63],v[108:111]
ld a[28:31],v247,s[44:47],s54 offen offset:1024 sc1
mm v[76:79],v[220:223],a[56:59],v[76:79]
ar v230,a144
ar v231,a148
mm v[76:79],v[216:219],a[40:43],v[76:79]
rt v[208:209],v242 offset:8576
rt v[210:211],v242 offset:9600
mm v[44:47],v[216:219],a[36:39],v[44:47]
v_add_u32 v242, 67584, v242
v_perm_b32 v232, v231, v230, s86
mm v[44:47],v[220:223],a[52:55],v[44:47]
v_perm_b32 v233, v231, v230, s87
aw a98,v232
mm v[12:15],v[220:223],a[48:51],v[12:15]
wt lgkmcnt(4)
aw a102,v233
mm v[12:15],v[216:219],a[32:35],v[12:15]
rt v[220:221],v243 offset:128
rt v[222:223],v243 offset:1152
mm v[16:19],v[196:199],a[32:35],v[16:19]
s_add_u32 m0, s83, 101376
ar v234,a152
mm v[16:19],v[192:195],a[48:51],v[16:19]
ld v224,s[56:59],s80 offen lds
mm v[48:51],v[192:195],a[52:55],v[48:51]
ar v235,a156
v_perm_b32 v236, v235, v234, s86
mm v[48:51],v[196:199],a[36:39],v[48:51]
rt v[216:217],v243 offset:8576
rt v[218:219],v243 offset:9600
mm v[80:83],v[196:199],a[40:43],v[80:83]
s_add_u32 m0, s83, 102400
v_add_u32 v243, 67584, v243
mm v[80:83],v[192:195],a[56:59],v[80:83]
ld v225,s[56:59],s80 offen lds
mm v[112:115],v[192:195],a[60:63],v[112:115]
wt vmcnt(14)
s_barrier
mm v[112:115],v[196:199],a[44:47],v[112:115]
rt v[192:193],v240 offset:50688
rt v[194:195],v240 offset:51712
mm v[116:119],v[204:207],a[44:47],v[116:119]
s_add_u32 m0, s83, 103552
v_perm_b32 v237, v235, v234, s87
mm v[116:119],v[200:203],a[60:63],v[116:119]
ld v226,s[56:59],s80 offen lds
mm v[84:87],v[200:203],a[56:59],v[84:87]
aw a99,v236
aw a103,v237
mm v[84:87],v[204:207],a[40:43],v[84:87]
rt v[196:197],v240 offset:59136
rt v[198:199],v240 offset:60160
mm v[52:55],v[204:207],a[36:39],v[52:55]
ar v230,a129
ar v231,a133
mm v[52:55],v[200:203],a[52:55],v[52:55]
ld a[64:67],v244,s[44:47],s54 offen offset:2048 sc1
mm v[20:23],v[200:203],a[48:51],v[20:23]
wt lgkmcnt(4)
v_perm_b32 v232, v231, v230, s86
mm v[20:23],v[204:207],a[32:35],v[20:23]
rt v[200:201],v241 offset:50688
rt v[202:203],v241 offset:51712
mm v[24:27],v[212:215],a[32:35],v[24:27]
v_perm_b32 v233, v231, v230, s87
aw a104,v232
mm v[24:27],v[208:211],a[48:51],v[24:27]
ld a[68:71],v245,s[44:47],s54 offen offset:2048 sc1
mm v[56:59],v[208:211],a[52:55],v[56:59]
aw a108,v233
ar v234,a137
mm v[56:59],v[212:215],a[36:39],v[56:59]
rt v[204:205],v241 offset:59136
rt v[206:207],v241 offset:60160
mm v[88:91],v[212:215],a[40:43],v[88:91]
ar v235,a141
v_perm_b32 v236, v235, v234, s86
mm v[88:91],v[208:211],a[56:59],v[88:91]
ld a[72:75],v246,s[44:47],s54 offen offset:2048 sc1
mm v[120:123],v[208:211],a[60:63],v[120:123]
v_perm_b32 v237, v235, v234, s87
aw a105,v236
mm v[120:123],v[212:215],a[44:47],v[120:123]
rt v[208:209],v242 offset:50688
rt v[210:211],v242 offset:51712
mm v[124:127],v[220:223],a[44:47],v[124:127]
aw a109,v237
ar v230,a145
mm v[124:127],v[216:219],a[60:63],v[124:127]
buffer_load_dwordx2 v[250:251], v248, s[64:67], s5 offen sc1
mm v[92:95],v[216:219],a[56:59],v[92:95]
ar v231,a149
v_perm_b32 v232, v231, v230, s86
mm v[92:95],v[220:223],a[40:43],v[92:95]
rt v[212:213],v242 offset:59136
rt v[214:215],v242 offset:60160
mm v[60:63],v[220:223],a[36:39],v[60:63]
v_perm_b32 v233, v231, v230, s87
aw a106,v232
mm v[60:63],v[216:219],a[52:55],v[60:63]
ld a[36:39],v247,s[44:47],s54 offen offset:2048 sc1
mm v[28:31],v[216:219],a[48:51],v[28:31]
wt vmcnt(8) lgkmcnt(4)
aw a110,v233
mm v[28:31],v[220:223],a[32:35],v[28:31]
rt v[216:217],v243 offset:50688
rt v[218:219],v243 offset:51712
mm v[0:3],v[192:195],a[12:15],v[0:3]
ar v234,a153
ar v235,a157
mm v[0:3],v[196:199],a[16:19],v[0:3]
ld a[32:35],v244,s[44:47],s54 offen offset:3072 sc1
mm v[32:35],v[196:199],a[20:23],v[32:35]
v_perm_b32 v236, v235, v234, s86
v_perm_b32 v237, v235, v234, s87
mm v[32:35],v[192:195],a[0:3],v[32:35]
rt v[220:221],v243 offset:59136
rt v[222:223],v243 offset:60160
mm v[64:67],v[192:195],a[4:7],v[64:67]
aw a107,v236
aw a111,v237
mm v[64:67],v[196:199],a[24:27],v[64:67]
ld a[40:43],v245,s[44:47],s54 offen offset:3072 sc1
mm v[96:99],v[196:199],a[28:31],v[96:99]
ar v230,a130
ar v231,a134
mm v[96:99],v[192:195],a[8:11],v[96:99]
rt v[196:197],v240 offset:50816
rt v[198:199],v240 offset:51840
mm v[100:103],v[200:203],a[8:11],v[100:103]
v_perm_b32 v232, v231, v230, s86
v_perm_b32 v233, v231, v230, s87
mm v[100:103],v[204:207],a[28:31],v[100:103]
ld a[44:47],v246,s[44:47],s54 offen offset:3072 sc1
mm v[68:71],v[204:207],a[24:27],v[68:71]
aw a112,v232
aw a116,v233
mm v[68:71],v[200:203],a[4:7],v[68:71]
rt v[192:193],v240 offset:59264
rt v[194:195],v240 offset:60288
mm v[36:39],v[200:203],a[0:3],v[36:39]
ar v234,a138
ar v235,a142
mm v[36:39],v[204:207],a[20:23],v[36:39]
ld a[48:51],v247,s[44:47],s54 offen offset:3072 sc1
mm v[4:7],v[204:207],a[16:19],v[4:7]
wt lgkmcnt(4)
s_bfe_u32 s54, s4, 262164
mm v[4:7],v[200:203],a[12:15],v[4:7]
rt v[204:205],v241 offset:50816
rt v[206:207],v241 offset:51840
mm v[8:11],v[208:211],a[12:15],v[8:11]
s_add_u32 m0, s83, 104576
s_xor_b32 s54, s54, s72
mm v[8:11],v[212:215],a[16:19],v[8:11]
ld v227,s[56:59],s80 offen lds
mm v[40:43],v[212:215],a[20:23],v[40:43]
s_bitcmp1_b32 vcc_lo, 3
s_cbranch_scc0 .La5_LO_NB3
.La5_LO_KB3:
mm v[40:43],v[208:211],a[0:3],v[40:43]
rt v[200:201],v241 offset:59264
rt v[202:203],v241 offset:60288
mm v[72:75],v[208:211],a[4:7],v[72:75]
s_bfe_u32 s80, s4, 262160
s_xor_b32 s80, s80, s72
mm v[72:75],v[212:215],a[24:27],v[72:75]
ld a[52:55],v244,s[44:47],s55 offen offset:0 sc1
mm v[104:107],v[212:215],a[28:31],v[104:107]
s_lshl_b32 s80, s80, 18
s_lshl_b32 s54, s54, 13
mm v[104:107],v[208:211],a[8:11],v[104:107]
rt v[212:213],v242 offset:50816
rt v[214:215],v242 offset:51840
mm v[108:111],v[216:219],a[8:11],v[108:111]
s_xor_b32 s54, s54, 229376
v_perm_b32 v236, v235, v234, s86
mm v[108:111],v[220:223],a[28:31],v[108:111]
ld a[56:59],v245,s[44:47],s55 offen offset:0 sc1
mm v[76:79],v[220:223],a[24:27],v[76:79]
v_perm_b32 v237, v235, v234, s87
aw a113,v236
mm v[76:79],v[216:219],a[4:7],v[76:79]
rt v[208:209],v242 offset:59264
rt v[210:211],v242 offset:60288
mm v[44:47],v[216:219],a[0:3],v[44:47]
aw a117,v237
ar v230,a146
mm v[44:47],v[220:223],a[20:23],v[44:47]
ar v231,a150
v_perm_b32 v232, v231, v230, s86
mm v[12:15],v[220:223],a[16:19],v[12:15]
wt lgkmcnt(4)
v_perm_b32 v233, v231, v230, s87
mm v[12:15],v[216:219],a[12:15],v[12:15]
rt v[220:221],v243 offset:50816
rt v[222:223],v243 offset:51840
mm v[16:19],v[196:199],a[12:15],v[16:19]
aw a114,v232
aw a118,v233
mm v[16:19],v[192:195],a[16:19],v[16:19]
ld a[60:63],v246,s[44:47],s55 offen offset:0 sc1
mm v[48:51],v[192:195],a[20:23],v[48:51]
ar v234,a154
ar v235,a158
mm v[48:51],v[196:199],a[0:3],v[48:51]
rt v[216:217],v243 offset:59264
rt v[218:219],v243 offset:60288
mm v[80:83],v[196:199],a[4:7],v[80:83]
v_perm_b32 v236, v235, v234, s86
v_perm_b32 v237, v235, v234, s87
mm v[80:83],v[192:195],a[24:27],v[80:83]
ld a[76:79],v247,s[44:47],s55 offen offset:0 sc1
mm v[112:115],v[192:195],a[28:31],v[112:115]
aw a115,v236
aw a119,v237
mm v[112:115],v[196:199],a[8:11],v[112:115]
rt v[192:193],v240 offset:0
rt v[194:195],v240 offset:1024
mm v[116:119],v[204:207],a[8:11],v[116:119]
ar v230,a131
ar v231,a135
mm v[116:119],v[200:203],a[28:31],v[116:119]
ld a[80:83],v244,s[44:47],s55 offen offset:1024 sc1
mm v[84:87],v[200:203],a[24:27],v[84:87]
v_perm_b32 v232, v231, v230, s86
v_perm_b32 v233, v231, v230, s87
mm v[84:87],v[204:207],a[4:7],v[84:87]
rt v[196:197],v240 offset:8448
rt v[198:199],v240 offset:9472
mm v[52:55],v[204:207],a[0:3],v[52:55]
s_load_dwordx2 s[16:17], s[0:1], 0
s_load_dwordx4 s[36:39], s[0:1], 80
mm v[52:55],v[200:203],a[20:23],v[52:55]
ld a[84:87],v245,s[44:47],s55 offen offset:1024 sc1
mm v[20:23],v[200:203],a[16:19],v[20:23]
wt lgkmcnt(4)
aw a120,v232
mm v[20:23],v[204:207],a[12:15],v[20:23]
rt v[200:201],v241 offset:0
rt v[202:203],v241 offset:1024
mm v[24:27],v[212:215],a[12:15],v[24:27]
aw a124,v233
ar v234,a139
mm v[24:27],v[208:211],a[16:19],v[24:27]
ld a[88:91],v246,s[44:47],s55 offen offset:1024 sc1
mm v[56:59],v[208:211],a[20:23],v[56:59]
ar v235,a143
v_perm_b32 v236, v235, v234, s86
mm v[56:59],v[212:215],a[0:3],v[56:59]
rt v[204:205],v241 offset:8448
rt v[206:207],v241 offset:9472
mm v[88:91],v[212:215],a[4:7],v[88:91]
v_perm_b32 v237, v235, v234, s87
aw a121,v236
mm v[88:91],v[208:211],a[24:27],v[88:91]
ld a[92:95],v247,s[44:47],s55 offen offset:1024 sc1
mm v[120:123],v[208:211],a[28:31],v[120:123]
aw a125,v237
ar v230,a147
mm v[120:123],v[212:215],a[8:11],v[120:123]
rt v[208:209],v242 offset:0
rt v[210:211],v242 offset:1024
mm v[124:127],v[220:223],a[8:11],v[124:127]
s_add_u32 m0, s83, 16896
ar v231,a151
mm v[124:127],v[216:219],a[28:31],v[124:127]
ld v224,s[56:59],s80 offen lds
mm v[92:95],v[216:219],a[24:27],v[92:95]
v_perm_b32 v232, v231, v230, s86
v_perm_b32 v233, v231, v230, s87
mm v[92:95],v[220:223],a[4:7],v[92:95]
rt v[212:213],v242 offset:8448
rt v[214:215],v242 offset:9472
mm v[60:63],v[220:223],a[0:3],v[60:63]
aw a122,v232
aw a126,v233
mm v[60:63],v[216:219],a[20:23],v[60:63]
ar v234,a155
ar v235,a159
mm v[28:31],v[216:219],a[16:19],v[28:31]
wt vmcnt(10) lgkmcnt(4)
v_perm_b32 v236, v235, v234, s86
mm v[28:31],v[220:223],a[12:15],v[28:31]
rt v[216:217],v243 offset:0
rt v[218:219],v243 offset:1024
mm v[0:3],v[192:195],a[64:67],v[0:3]
s_add_u32 m0, s83, 17920
v_perm_b32 v237, v235, v234, s87
mm v[0:3],v[196:199],a[32:35],v[0:3]
ld v225,s[56:59],s80 offen lds
mm v[32:35],v[196:199],a[40:43],v[32:35]
aw a123,v236
aw a127,v237
mm v[32:35],v[192:195],a[68:71],v[32:35]
rt v[220:221],v243 offset:8448
rt v[222:223],v243 offset:9472
mm v[64:67],v[192:195],a[72:75],v[64:67]
s_add_u32 m0, s83, 19072
ar v230,a160
mm v[64:67],v[196:199],a[44:47],v[64:67]
ld v226,s[56:59],s80 offen lds
mm v[96:99],v[196:199],a[48:51],v[96:99]
ar v231,a164
v_perm_b32 v232, v231, v230, s86
mm v[96:99],v[192:195],a[36:39],v[96:99]
rt v[196:197],v240 offset:128
rt v[198:199],v240 offset:1152
mm v[100:103],v[200:203],a[36:39],v[100:103]
v_perm_b32 v233, v231, v230, s87
aw a128,v232
mm v[100:103],v[204:207],a[48:51],v[100:103]
ld a[0:3],v244,s[44:47],s55 offen offset:2048 sc1
mm v[68:71],v[204:207],a[44:47],v[68:71]
.La5_LW3_1:
s_cbranch_execz .La5_LW3_5
aw a132,v233
ar v234,a168
mm v[68:71],v[200:203],a[72:75],v[68:71]
rt v[192:193],v240 offset:8576
rt v[194:195],v240 offset:9600
mm v[36:39],v[200:203],a[68:71],v[36:39]
ar v235,a172
v_perm_b32 v236, v235, v234, s86
mm v[36:39],v[204:207],a[40:43],v[36:39]
ld a[4:7],v245,s[44:47],s55 offen offset:2048 sc1
mm v[4:7],v[204:207],a[32:35],v[4:7]
wt lgkmcnt(4)
v_perm_b32 v237, v235, v234, s87
mm v[4:7],v[200:203],a[64:67],v[4:7]
rt v[204:205],v241 offset:128
rt v[206:207],v241 offset:1152
mm v[8:11],v[208:211],a[64:67],v[8:11]
aw a129,v236
aw a133,v237
mm v[8:11],v[212:215],a[32:35],v[8:11]
ld a[8:11],v246,s[44:47],s55 offen offset:2048 sc1
mm v[40:43],v[212:215],a[40:43],v[40:43]
ar v230,a176
ar v231,a180
mm v[40:43],v[208:211],a[68:71],v[40:43]
rt v[200:201],v241 offset:8576
rt v[202:203],v241 offset:9600
mm v[72:75],v[208:211],a[72:75],v[72:75]
v_perm_b32 v232, v231, v230, s86
v_perm_b32 v233, v231, v230, s87
mm v[72:75],v[212:215],a[44:47],v[72:75]
ld a[12:15],v247,s[44:47],s55 offen offset:2048 sc1
mm v[104:107],v[212:215],a[48:51],v[104:107]
aw a130,v232
aw a134,v233
mm v[104:107],v[208:211],a[36:39],v[104:107]
rt v[212:213],v242 offset:128
rt v[214:215],v242 offset:1152
mm v[108:111],v[216:219],a[36:39],v[108:111]
ar v234,a184
ar v235,a188
mm v[108:111],v[220:223],a[48:51],v[108:111]
ld a[16:19],v244,s[44:47],s55 offen offset:3072 sc1
mm v[76:79],v[220:223],a[44:47],v[76:79]
v_perm_b32 v236, v235, v234, s86
v_perm_b32 v237, v235, v234, s87
mm v[76:79],v[216:219],a[72:75],v[76:79]
rt v[208:209],v242 offset:8576
rt v[210:211],v242 offset:9600
mm v[44:47],v[216:219],a[68:71],v[44:47]
aw a131,v236
aw a135,v237
mm v[44:47],v[220:223],a[40:43],v[44:47]
s_nop 0
v_cmp_eq_u64 vcc, v[250:251], v[252:253]
mm v[12:15],v[220:223],a[32:35],v[12:15]
wt lgkmcnt(4)
ar v230,a161
mm v[12:15],v[216:219],a[64:67],v[12:15]
rt v[220:221],v243 offset:128
rt v[222:223],v243 offset:1152
mm v[16:19],v[196:199],a[64:67],v[16:19]
ar v231,a165
v_perm_b32 v232, v231, v230, s86
mm v[16:19],v[192:195],a[32:35],v[16:19]
ld a[20:23],v245,s[44:47],s55 offen offset:3072 sc1
mm v[48:51],v[192:195],a[40:43],v[48:51]
v_perm_b32 v233, v231, v230, s87
aw a136,v232
mm v[48:51],v[196:199],a[68:71],v[48:51]
rt v[216:217],v243 offset:8576
rt v[218:219],v243 offset:9600
mm v[80:83],v[196:199],a[72:75],v[80:83]
aw a140,v233
ar v234,a169
mm v[80:83],v[192:195],a[44:47],v[80:83]
ld a[24:27],v246,s[44:47],s55 offen offset:3072 sc1
mm v[112:115],v[192:195],a[48:51],v[112:115]
wt vmcnt(18) lgkmcnt(0)
s_barrier
mm v[112:115],v[196:199],a[36:39],v[112:115]
rt v[192:193],v240 offset:16896
rt v[194:195],v240 offset:17920
mm v[116:119],v[204:207],a[36:39],v[116:119]
s_xor_b32 s90, s72, 0
s_lshl_b32 s92, s76, 5
mm v[116:119],v[200:203],a[48:51],v[116:119]
buffer_load_dwordx2 v[250:251], v248, s[64:67], s5 offen sc1
mm v[84:87],v[200:203],a[44:47],v[84:87]
s_add_u32 s92, s92, s90
s_lshl2_add_u32 s92, s92, s3
mm v[84:87],v[204:207],a[72:75],v[84:87]
rt v[196:197],v240 offset:25344
rt v[198:199],v240 offset:26368
mm v[52:55],v[204:207],a[68:71],v[52:55]
s_lshl_b32 s92, s92, 12
ar v235,a173
mm v[52:55],v[200:203],a[40:43],v[52:55]
ld a[28:31],v247,s[44:47],s55 offen offset:3072 sc1
mm v[20:23],v[200:203],a[32:35],v[20:23]
s_nop 0
s_bfe_u32 s55, s4, 262168
mm v[20:23],v[204:207],a[64:67],v[20:23]
rt v[200:201],v241 offset:16896
rt v[202:203],v241 offset:17920
mm v[24:27],v[212:215],a[64:67],v[24:27]
s_add_u32 m0, s83, 20096
s_xor_b32 s55, s55, s72
mm v[24:27],v[208:211],a[32:35],v[24:27]
ld v227,s[56:59],s80 offen lds
mm v[56:59],v[208:211],a[40:43],v[56:59]
s_add_u32 s80, s80, 131072
s_lshl_b32 s55, s55, 13
mm v[56:59],v[212:215],a[68:71],v[56:59]
rt v[204:205],v241 offset:25344
rt v[206:207],v241 offset:26368
mm v[88:91],v[212:215],a[72:75],v[88:91]
s_add_u32 m0, s83, 33792
v_perm_b32 v236, v235, v234, s86
mm v[88:91],v[208:211],a[44:47],v[88:91]
ld v224,s[56:59],s80 offen lds
mm v[120:123],v[208:211],a[48:51],v[120:123]
s_add_u32 s68, s16, s92
s_addc_u32 s69, s17, 0
mm v[120:123],v[212:215],a[36:39],v[120:123]
rt v[208:209],v242 offset:16896
rt v[210:211],v242 offset:17920
mm v[124:127],v[220:223],a[36:39],v[124:127]
s_add_u32 m0, s83, 34816
s_and_b32 s69, s69, 0xffff
mm v[124:127],v[216:219],a[48:51],v[124:127]
ld v225,s[56:59],s80 offen lds
mm v[92:95],v[216:219],a[44:47],v[92:95]
s_mov_b32 s70, 4096
s_mov_b32 s71, 0x20000
mm v[92:95],v[220:223],a[72:75],v[92:95]
rt v[212:213],v242 offset:25344
rt v[214:215],v242 offset:26368
mm v[60:63],v[220:223],a[68:71],v[60:63]
s_add_u32 m0, s83, 35968
v_perm_b32 v237, v235, v234, s87
mm v[60:63],v[216:219],a[40:43],v[60:63]
ld v226,s[56:59],s80 offen lds
mm v[28:31],v[216:219],a[32:35],v[28:31]
wt vmcnt(16) lgkmcnt(4)
aw a137,v236
mm v[28:31],v[220:223],a[64:67],v[28:31]
rt v[216:217],v243 offset:16896
rt v[218:219],v243 offset:17920
mm v[0:3],v[192:195],a[52:55],v[0:3]
s_add_u32 m0, s83, 36992
aw a141,v237
mm v[0:3],v[196:199],a[80:83],v[0:3]
ld v227,s[56:59],s80 offen lds
mm v[32:35],v[196:199],a[84:87],v[32:35]
s_bitcmp1_b32 vcc_lo, 4
s_cbranch_scc0 .La5_LO_NB4
.La5_LO_KB4:
mm v[32:35],v[192:195],a[56:59],v[32:35]
rt v[220:221],v243 offset:25344
rt v[222:223],v243 offset:26368
mm v[64:67],v[192:195],a[60:63],v[64:67]
s_bfe_u32 s80, s4, 262164
s_xor_b32 s80, s80, s72
mm v[64:67],v[196:199],a[88:91],v[64:67]
ld a[32:35],v244,s[44:47],s52 offen offset:0 sc1
mm v[96:99],v[196:199],a[92:95],v[96:99]
s_lshl_b32 s80, s80, 18
ar v230,a177
mm v[96:99],v[192:195],a[76:79],v[96:99]
rt v[196:197],v240 offset:17024
rt v[198:199],v240 offset:18048
mm v[100:103],v[200:203],a[76:79],v[100:103]
s_add_u32 s48, s36, 0
s_addc_u32 s49, s37, 0
mm v[100:103],v[204:207],a[92:95],v[100:103]
ld a[36:39],v245,s[44:47],s52 offen offset:0 sc1
mm v[68:71],v[204:207],a[88:91],v[68:71]
s_and_b32 s49, s49, 0xffff
s_mov_b32 s50, 2048
mm v[68:71],v[200:203],a[60:63],v[68:71]
rt v[192:193],v240 offset:25472
rt v[194:195],v240 offset:26496
mm v[36:39],v[200:203],a[56:59],v[36:39]
s_mov_b32 s51, 0x20000
v_and_b32 v238, 0xf0, v255
mm v[36:39],v[204:207],a[84:87],v[36:39]
ld a[40:43],v246,s[44:47],s52 offen offset:0 sc1
mm v[4:7],v[204:207],a[80:83],v[4:7]
wt lgkmcnt(4)
v_lshlrev_b32 v238, 2, v238
mm v[4:7],v[200:203],a[52:55],v[4:7]
rt v[204:205],v241 offset:17024
rt v[206:207],v241 offset:18048
mm v[8:11],v[208:211],a[52:55],v[8:11]
v_bfe_u32 v249, v255, 8, 1
v_lshl_add_u32 v238, v249, 5, v238
mm v[8:11],v[212:215],a[80:83],v[8:11]
ld a[44:47],v247,s[44:47],s52 offen offset:0 sc1
mm v[40:43],v[212:215],a[84:87],v[40:43]
v_lshrrev_b32 v249, 9, v255
v_lshl_add_u32 v238, v249, 4, v238
mm v[40:43],v[208:211],a[56:59],v[40:43]
rt v[200:201],v241 offset:25472
rt v[202:203],v241 offset:26496
mm v[72:75],v[208:211],a[60:63],v[72:75]
s_lshr_b32 s23, s72, 3
s_lshl3_add_u32 s92, s23, s76
mm v[72:75],v[212:215],a[88:91],v[72:75]
ld a[48:51],v244,s[44:47],s52 offen offset:1024 sc1
mm v[104:107],v[212:215],a[92:95],v[104:107]
s_lshl_b32 s92, s92, 19
s_lshl_b32 s97, s3, 16
mm v[104:107],v[208:211],a[76:79],v[104:107]
rt v[212:213],v242 offset:17024
rt v[214:215],v242 offset:18048
mm v[108:111],v[216:219],a[76:79],v[108:111]
s_add_u32 s94, s92, s97
s_add_u32 s95, s94, 32768
mm v[108:111],v[220:223],a[92:95],v[108:111]
ld a[64:67],v245,s[44:47],s52 offen offset:1024 sc1
mm v[76:79],v[220:223],a[88:91],v[76:79]
s_sub_u32 s96, s92, s97
s_add_u32 s96, s96, 458752
mm v[76:79],v[216:219],a[60:63],v[76:79]
rt v[208:209],v242 offset:25472
rt v[210:211],v242 offset:26496
mm v[44:47],v[216:219],a[56:59],v[44:47]
s_add_u32 s97, s96, 32768
s_lshl3_add_u32 s92, s23, s101
mm v[44:47],v[220:223],a[84:87],v[44:47]
s_lshl_b32 s85, s76, 14
s_lshl3_add_u32 s92, s92, s85
mm v[12:15],v[220:223],a[80:83],v[12:15]
wt lgkmcnt(4)
s_add_u32 s92, s92, 2097152
mm v[12:15],v[216:219],a[52:55],v[12:15]
rt v[220:221],v243 offset:17024
rt v[222:223],v243 offset:18048
mm v[16:19],v[196:199],a[52:55],v[16:19]
ar v231,a181
v_perm_b32 v232, v231, v230, s86
mm v[16:19],v[192:195],a[80:83],v[16:19]
ld a[68:71],v246,s[44:47],s52 offen offset:1024 sc1
mm v[48:51],v[192:195],a[84:87],v[48:51]
v_perm_b32 v233, v231, v230, s87
aw a138,v232
mm v[48:51],v[196:199],a[56:59],v[48:51]
rt v[216:217],v243 offset:25472
rt v[218:219],v243 offset:26496
mm v[80:83],v[196:199],a[60:63],v[80:83]
aw a142,v233
ar v234,a185
mm v[80:83],v[192:195],a[88:91],v[80:83]
ld a[72:75],v247,s[44:47],s52 offen offset:1024 sc1
mm v[112:115],v[192:195],a[92:95],v[112:115]
ar v235,a189
v_perm_b32 v236, v235, v234, s86
mm v[112:115],v[196:199],a[76:79],v[112:115]
rt v[192:193],v240 offset:33792
rt v[194:195],v240 offset:34816
mm v[116:119],v[204:207],a[76:79],v[116:119]
s_add_u32 m0, s83, 50688
v_perm_b32 v237, v235, v234, s87
mm v[116:119],v[200:203],a[92:95],v[116:119]
ld v224,s[56:59],s80 offen lds
mm v[84:87],v[200:203],a[88:91],v[84:87]
aw a139,v236
aw a143,v237
mm v[84:87],v[204:207],a[60:63],v[84:87]
rt v[196:197],v240 offset:42240
rt v[198:199],v240 offset:43264
mm v[52:55],v[204:207],a[56:59],v[52:55]
s_add_u32 m0, s83, 51712
ar v230,a162
mm v[52:55],v[200:203],a[84:87],v[52:55]
ld v225,s[56:59],s80 offen lds
mm v[20:23],v[200:203],a[80:83],v[20:23]
wt lgkmcnt(4)
ar v231,a166
mm v[20:23],v[204:207],a[52:55],v[20:23]
rt v[200:201],v241 offset:33792
rt v[202:203],v241 offset:34816
mm v[24:27],v[212:215],a[52:55],v[24:27]
s_add_u32 m0, s83, 52864
v_perm_b32 v232, v231, v230, s86
mm v[24:27],v[208:211],a[80:83],v[24:27]
ld v226,s[56:59],s80 offen lds
mm v[56:59],v[208:211],a[84:87],v[56:59]
v_perm_b32 v233, v231, v230, s87
aw a144,v232
mm v[56:59],v[212:215],a[56:59],v[56:59]
rt v[204:205],v241 offset:42240
rt v[206:207],v241 offset:43264
mm v[88:91],v[212:215],a[60:63],v[88:91]
aw a148,v233
ar v234,a170
mm v[88:91],v[208:211],a[88:91],v[88:91]
ld v[136:139],v244,s[44:47],s52 offen offset:2048 sc1
mm v[120:123],v[208:211],a[92:95],v[120:123]
ar v235,a174
v_perm_b32 v236, v235, v234, s86
mm v[120:123],v[212:215],a[76:79],v[120:123]
rt v[208:209],v242 offset:33792
rt v[210:211],v242 offset:34816
mm v[124:127],v[220:223],a[76:79],v[124:127]
v_perm_b32 v237, v235, v234, s87
aw a145,v236
mm v[124:127],v[216:219],a[92:95],v[124:127]
ld v[140:143],v245,s[44:47],s52 offen offset:2048 sc1
mm v[92:95],v[216:219],a[88:91],v[92:95]
aw a149,v237
ar v230,a178
mm v[92:95],v[220:223],a[60:63],v[92:95]
rt v[212:213],v242 offset:42240
rt v[214:215],v242 offset:43264
mm v[60:63],v[220:223],a[56:59],v[60:63]
ar v231,a182
v_perm_b32 v232, v231, v230, s86
mm v[60:63],v[216:219],a[84:87],v[60:63]
v_perm_b32 v233, v231, v230, s87
aw a146,v232
mm v[28:31],v[216:219],a[80:83],v[28:31]
wt vmcnt(18) lgkmcnt(4)
aw a150,v233
mm v[28:31],v[220:223],a[52:55],v[28:31]
rt v[216:217],v243 offset:33792
rt v[218:219],v243 offset:34816
mm v[0:3],v[192:195],a[0:3],v[0:3]
ar v234,a186
ar v235,a190
mm v[0:3],v[196:199],a[16:19],v[0:3]
ld v[152:155],v246,s[44:47],s52 offen offset:2048 sc1
mm v[32:35],v[196:199],a[20:23],v[32:35]
v_perm_b32 v236, v235, v234, s86
v_perm_b32 v237, v235, v234, s87
mm v[32:35],v[192:195],a[4:7],v[32:35]
rt v[220:221],v243 offset:42240
rt v[222:223],v243 offset:43264
mm v[64:67],v[192:195],a[8:11],v[64:67]
aw a147,v236
aw a151,v237
mm v[64:67],v[196:199],a[24:27],v[64:67]
ld v[156:159],v247,s[44:47],s52 offen offset:2048 sc1
mm v[96:99],v[196:199],a[28:31],v[96:99]
ar v230,a163
ar v231,a167
mm v[96:99],v[192:195],a[12:15],v[96:99]
rt v[196:197],v240 offset:33920
rt v[198:199],v240 offset:34944
mm v[100:103],v[200:203],a[12:15],v[100:103]
v_perm_b32 v232, v231, v230, s86
v_perm_b32 v233, v231, v230, s87
mm v[100:103],v[204:207],a[28:31],v[100:103]
ld v[168:171],v244,s[44:47],s52 offen offset:3072 sc1
mm v[68:71],v[204:207],a[24:27],v[68:71]
aw a152,v232
aw a156,v233
mm v[68:71],v[200:203],a[8:11],v[68:71]
rt v[192:193],v240 offset:42368
rt v[194:195],v240 offset:43392
mm v[36:39],v[200:203],a[4:7],v[36:39]
v_add_u32 v240, 4294899712, v240
ar v234,a171
mm v[36:39],v[204:207],a[20:23],v[36:39]
ld v[172:175],v245,s[44:47],s52 offen offset:3072 sc1
mm v[4:7],v[204:207],a[16:19],v[4:7]
wt lgkmcnt(4)
ar v235,a175
mm v[4:7],v[200:203],a[0:3],v[4:7]
rt v[204:205],v241 offset:33920
rt v[206:207],v241 offset:34944
mm v[8:11],v[208:211],a[0:3],v[8:11]
s_nop 0
v_cmp_eq_u64 vcc, v[250:251], v[252:253]
mm v[8:11],v[212:215],a[16:19],v[8:11]
ld v[184:187],v246,s[44:47],s52 offen offset:3072 sc1
mm v[40:43],v[212:215],a[20:23],v[40:43]
v_perm_b32 v236, v235, v234, s86
v_perm_b32 v237, v235, v234, s87
mm v[40:43],v[208:211],a[4:7],v[40:43]
rt v[200:201],v241 offset:42368
rt v[202:203],v241 offset:43392
mm v[72:75],v[208:211],a[8:11],v[72:75]
v_add_u32 v241, 4294899712, v241
aw a153,v236
mm v[72:75],v[212:215],a[24:27],v[72:75]
ld v[188:191],v247,s[44:47],s52 offen offset:3072 sc1
mm v[104:107],v[212:215],a[28:31],v[104:107]
s_bfe_u32 s52, s4, 262172
s_xor_b32 s52, s52, s72
mm v[104:107],v[208:211],a[12:15],v[104:107]
rt v[212:213],v242 offset:33920
rt v[214:215],v242 offset:34944
mm v[108:111],v[216:219],a[12:15],v[108:111]
s_lshl_b32 s52, s52, 13
aw a157,v237
mm v[108:111],v[220:223],a[28:31],v[108:111]
buffer_load_dwordx2 v[250:251], v248, s[64:67], s5 offen sc1
mm v[76:79],v[220:223],a[24:27],v[76:79]
ar v230,a179
ar v231,a183
mm v[76:79],v[216:219],a[8:11],v[76:79]
rt v[208:209],v242 offset:42368
rt v[210:211],v242 offset:43392
mm v[44:47],v[216:219],a[4:7],v[44:47]
s_add_u32 m0, s83, 53888
v_add_u32 v242, 4294899712, v242
mm v[44:47],v[220:223],a[20:23],v[44:47]
ld v227,s[56:59],s80 offen lds
mm v[12:15],v[220:223],a[16:19],v[12:15]
wt lgkmcnt(4)
s_add_u32 s80, s80, 131072
mm v[12:15],v[216:219],a[0:3],v[12:15]
rt v[220:221],v243 offset:33920
rt v[222:223],v243 offset:34944
mm v[16:19],v[196:199],a[0:3],v[16:19]
s_add_u32 m0, s83, 0
v_perm_b32 v232, v231, v230, s86
mm v[16:19],v[192:195],a[16:19],v[16:19]
ld v224,s[56:59],s80 offen lds
mm v[48:51],v[192:195],a[20:23],v[48:51]
v_perm_b32 v233, v231, v230, s87
aw a154,v232
mm v[48:51],v[196:199],a[4:7],v[48:51]
rt v[216:217],v243 offset:42368
rt v[218:219],v243 offset:43392
mm v[80:83],v[196:199],a[8:11],v[80:83]
s_add_u32 m0, s83, 1024
v_add_u32 v243, 4294899712, v243
mm v[80:83],v[192:195],a[24:27],v[80:83]
ld v225,s[56:59],s80 offen lds
mm v[112:115],v[192:195],a[28:31],v[112:115]
wt vmcnt(23) lgkmcnt(0)
s_barrier
mm v[112:115],v[196:199],a[12:15],v[112:115]
rt v[192:193],v240 offset:16896
rt v[194:195],v240 offset:17920
mm v[116:119],v[204:207],a[12:15],v[116:119]
s_add_u32 m0, s83, 2176
aw a158,v233
mm v[116:119],v[200:203],a[28:31],v[116:119]
ld v226,s[56:59],s80 offen lds
mm v[84:87],v[200:203],a[24:27],v[84:87]
ar v234,a187
ar v235,a191
mm v[84:87],v[204:207],a[8:11],v[84:87]
rt v[196:197],v240 offset:25344
rt v[198:199],v240 offset:26368
mm v[52:55],v[204:207],a[4:7],v[52:55]
s_add_u32 m0, s83, 3200
v_perm_b32 v236, v235, v234, s86
mm v[52:55],v[200:203],a[20:23],v[52:55]
ld v227,s[56:59],s80 offen lds
mm v[20:23],v[200:203],a[16:19],v[20:23]
s_nop 0
s_bfe_u32 s80, s4, 262164
mm v[20:23],v[204:207],a[0:3],v[20:23]
rt v[200:201],v241 offset:16896
rt v[202:203],v241 offset:17920
mm v[24:27],v[212:215],a[0:3],v[24:27]
s_bitcmp1_b32 vcc_lo, 5
s_cbranch_scc0 .La5_LO_NB5
.La5_LO_KB5:
mm v[24:27],v[208:211],a[16:19],v[24:27]
ld a[52:55],v244,s[44:47],s53 offen offset:0 sc1
mm v[56:59],v[208:211],a[20:23],v[56:59]
s_xor_b32 s80, s80, s72
s_lshl_b32 s80, s80, 18
mm v[56:59],v[212:215],a[4:7],v[56:59]
rt v[204:205],v241 offset:25344
rt v[206:207],v241 offset:26368
mm v[88:91],v[212:215],a[8:11],v[88:91]
s_xor_b32 s80, s80, 7340032
s_add_u32 s80, s80, 131072
mm v[88:91],v[208:211],a[24:27],v[88:91]
ld a[56:59],v245,s[44:47],s53 offen offset:0 sc1
mm v[120:123],v[208:211],a[28:31],v[120:123]
v_perm_b32 v237, v235, v234, s87
aw a155,v236
mm v[120:123],v[212:215],a[12:15],v[120:123]
rt v[208:209],v242 offset:16896
rt v[210:211],v242 offset:17920
mm v[124:127],v[220:223],a[12:15],v[124:127]
aw a159,v237
ar v230,a192
mm v[124:127],v[216:219],a[28:31],v[124:127]
ld a[12:15],v246,s[44:47],s53 offen offset:0 sc1
mm v[92:95],v[216:219],a[24:27],v[92:95]
ar v231,a196
v_perm_b32 v232, v231, v230, s86
mm v[92:95],v[220:223],a[8:11],v[92:95]
rt v[212:213],v242 offset:25344
rt v[214:215],v242 offset:26368
mm v[60:63],v[220:223],a[4:7],v[60:63]
v_perm_b32 v233, v231, v230, s87
aw a160,v232
mm v[60:63],v[216:219],a[20:23],v[60:63]
aw a164,v233
ar v234,a200
mm v[28:31],v[216:219],a[16:19],v[28:31]
wt vmcnt(20) lgkmcnt(4)
ar v235,a204
mm v[28:31],v[220:223],a[0:3],v[28:31]
rt v[216:217],v243 offset:16896
rt v[218:219],v243 offset:17920
mm v[0:3],v[192:195],a[32:35],v[0:3]
v_perm_b32 v236, v235, v234, s86
v_perm_b32 v237, v235, v234, s87
mm v[0:3],v[196:199],a[48:51],v[0:3]
ld a[0:3],v247,s[44:47],s53 offen offset:0 sc1
mm v[32:35],v[196:199],a[64:67],v[32:35]
aw a161,v236
aw a165,v237
mm v[32:35],v[192:195],a[36:39],v[32:35]
rt v[220:221],v243 offset:25344
rt v[222:223],v243 offset:26368
mm v[64:67],v[192:195],a[40:43],v[64:67]
ar v230,a208
ar v231,a212
mm v[64:67],v[196:199],a[68:71],v[64:67]
ld a[4:7],v244,s[44:47],s53 offen offset:1024 sc1
mm v[96:99],v[196:199],a[72:75],v[96:99]
v_perm_b32 v232, v231, v230, s86
v_perm_b32 v233, v231, v230, s87
mm v[96:99],v[192:195],a[44:47],v[96:99]
rt v[196:197],v240 offset:17024
rt v[198:199],v240 offset:18048
mm v[100:103],v[200:203],a[44:47],v[100:103]
aw a162,v232
aw a166,v233
mm v[100:103],v[204:207],a[72:75],v[100:103]
ld a[8:11],v245,s[44:47],s53 offen offset:1024 sc1
mm v[68:71],v[204:207],a[68:71],v[68:71]
ar v234,a216
ar v235,a220
mm v[68:71],v[200:203],a[40:43],v[68:71]
rt v[192:193],v240 offset:25472
rt v[194:195],v240 offset:26496
mm v[36:39],v[200:203],a[36:39],v[36:39]
.La5_LW3_2:
s_cbranch_execz .La5_LW3_6
v_perm_b32 v236, v235, v234, s86
v_perm_b32 v237, v235, v234, s87
mm v[36:39],v[204:207],a[64:67],v[36:39]
ld a[16:19],v246,s[44:47],s53 offen offset:1024 sc1
mm v[4:7],v[204:207],a[48:51],v[4:7]
wt lgkmcnt(4)
aw a163,v236
mm v[4:7],v[200:203],a[32:35],v[4:7]
rt v[204:205],v241 offset:17024
rt v[206:207],v241 offset:18048
mm v[8:11],v[208:211],a[32:35],v[8:11]
aw a167,v237
ar v230,a193
mm v[8:11],v[212:215],a[48:51],v[8:11]
ld a[20:23],v247,s[44:47],s53 offen offset:1024 sc1
mm v[40:43],v[212:215],a[64:67],v[40:43]
ar v231,a197
v_perm_b32 v232, v231, v230, s86
mm v[40:43],v[208:211],a[36:39],v[40:43]
rt v[200:201],v241 offset:25472
rt v[202:203],v241 offset:26496
mm v[72:75],v[208:211],a[40:43],v[72:75]
v_perm_b32 v233, v231, v230, s87
aw a168,v232
mm v[72:75],v[212:215],a[68:71],v[72:75]
aw a172,v233
ar v234,a201
mm v[104:107],v[212:215],a[72:75],v[104:107]
ar v235,a205
v_perm_b32 v236, v235, v234, s86
mm v[104:107],v[208:211],a[44:47],v[104:107]
rt v[212:213],v242 offset:17024
rt v[214:215],v242 offset:18048
mm v[108:111],v[216:219],a[44:47],v[108:111]
v_perm_b32 v237, v235, v234, s87
aw a169,v236
mm v[108:111],v[220:223],a[72:75],v[108:111]
aw a173,v237
ar v230,a209
mm v[76:79],v[220:223],a[68:71],v[76:79]
ar v231,a213
v_perm_b32 v232, v231, v230, s86
mm v[76:79],v[216:219],a[40:43],v[76:79]
rt v[208:209],v242 offset:25472
rt v[210:211],v242 offset:26496
mm v[44:47],v[216:219],a[36:39],v[44:47]
v_perm_b32 v233, v231, v230, s87
aw a170,v232
mm v[44:47],v[220:223],a[64:67],v[44:47]
aw a174,v233
ar v234,a217
mm v[12:15],v[220:223],a[48:51],v[12:15]
wt lgkmcnt(4)
ar v235,a221
mm v[12:15],v[216:219],a[32:35],v[12:15]
rt v[220:221],v243 offset:17024
rt v[222:223],v243 offset:18048
mm v[16:19],v[196:199],a[32:35],v[16:19]
v_perm_b32 v236, v235, v234, s86
v_perm_b32 v237, v235, v234, s87
mm v[16:19],v[192:195],a[48:51],v[16:19]
ld a[24:27],v244,s[44:47],s53 offen offset:2048 sc1
mm v[48:51],v[192:195],a[64:67],v[48:51]
aw a171,v236
aw a175,v237
mm v[48:51],v[196:199],a[36:39],v[48:51]
rt v[216:217],v243 offset:25472
rt v[218:219],v243 offset:26496
mm v[80:83],v[196:199],a[40:43],v[80:83]
ar v230,a194
ar v231,a198
mm v[80:83],v[192:195],a[68:71],v[80:83]
ld a[28:31],v245,s[44:47],s53 offen offset:2048 sc1
mm v[112:115],v[192:195],a[72:75],v[112:115]
v_perm_b32 v232, v231, v230, s86
v_perm_b32 v233, v231, v230, s87
mm v[112:115],v[196:199],a[44:47],v[112:115]
rt v[192:193],v240 offset:33792
rt v[194:195],v240 offset:34816
mm v[116:119],v[204:207],a[44:47],v[116:119]
aw a176,v232
aw a180,v233
mm v[116:119],v[200:203],a[72:75],v[116:119]
ld a[60:63],v246,s[44:47],s53 offen offset:2048 sc1
mm v[84:87],v[200:203],a[68:71],v[84:87]
ar v234,a202
ar v235,a206
mm v[84:87],v[204:207],a[40:43],v[84:87]
rt v[196:197],v240 offset:42240
rt v[198:199],v240 offset:43264
mm v[52:55],v[204:207],a[36:39],v[52:55]
v_perm_b32 v236, v235, v234, s86
v_perm_b32 v237, v235, v234, s87
mm v[52:55],v[200:203],a[64:67],v[52:55]
ld v[128:131],v247,s[44:47],s53 offen offset:2048 sc1
mm v[20:23],v[200:203],a[48:51],v[20:23]
wt lgkmcnt(4)
aw a177,v236
mm v[20:23],v[204:207],a[32:35],v[20:23]
rt v[200:201],v241 offset:33792
rt v[202:203],v241 offset:34816
mm v[24:27],v[212:215],a[32:35],v[24:27]
aw a181,v237
ar v230,a210
mm v[24:27],v[208:211],a[48:51],v[24:27]
ld v[132:135],v244,s[44:47],s53 offen offset:3072 sc1
mm v[56:59],v[208:211],a[64:67],v[56:59]
ar v231,a214
v_perm_b32 v232, v231, v230, s86
mm v[56:59],v[212:215],a[36:39],v[56:59]
rt v[204:205],v241 offset:42240
rt v[206:207],v241 offset:43264
mm v[88:91],v[212:215],a[40:43],v[88:91]
v_perm_b32 v233, v231, v230, s87
aw a178,v232
mm v[88:91],v[208:211],a[68:71],v[88:91]
ld v[144:147],v245,s[44:47],s53 offen offset:3072 sc1
mm v[120:123],v[208:211],a[72:75],v[120:123]
aw a182,v233
ar v234,a218
mm v[120:123],v[212:215],a[44:47],v[120:123]
rt v[208:209],v242 offset:33792
rt v[210:211],v242 offset:34816
mm v[124:127],v[220:223],a[44:47],v[124:127]
ar v235,a222
v_perm_b32 v236, v235, v234, s86
mm v[124:127],v[216:219],a[72:75],v[124:127]
ld a[44:47],v246,s[44:47],s53 offen offset:3072 sc1
mm v[92:95],v[216:219],a[68:71],v[92:95]
v_perm_b32 v237, v235, v234, s87
aw a179,v236
mm v[92:95],v[220:223],a[40:43],v[92:95]
rt v[212:213],v242 offset:42240
rt v[214:215],v242 offset:43264
mm v[60:63],v[220:223],a[36:39],v[60:63]
aw a183,v237
ar v230,a195
mm v[60:63],v[216:219],a[64:67],v[60:63]
wt vmcnt(20)
v_cmp_eq_u64 vcc, v[250:251], v[252:253]
mm v[28:31],v[216:219],a[48:51],v[28:31]
wt lgkmcnt(4)
ar v231,a199
mm v[28:31],v[220:223],a[32:35],v[28:31]
rt v[216:217],v243 offset:33792
rt v[218:219],v243 offset:34816
mm v[0:3],v[192:195],v[136:139],v[0:3]
v_perm_b32 v232, v231, v230, s86
v_perm_b32 v233, v231, v230, s87
mm v[0:3],v[196:199],v[168:171],v[0:3]
ld a[32:35],v247,s[44:47],s53 offen offset:3072 sc1
mm v[32:35],v[196:199],v[172:175],v[32:35]
aw a184,v232
aw a188,v233
mm v[32:35],v[192:195],v[140:143],v[32:35]
rt v[220:221],v243 offset:42240
rt v[222:223],v243 offset:43264
mm v[64:67],v[192:195],v[152:155],v[64:67]
ar v234,a203
ar v235,a207
mm v[64:67],v[196:199],v[184:187],v[64:67]
v_perm_b32 v236, v235, v234, s86
v_perm_b32 v237, v235, v234, s87
mm v[96:99],v[196:199],v[188:191],v[96:99]
aw a185,v236
aw a189,v237
mm v[96:99],v[192:195],v[156:159],v[96:99]
rt v[196:197],v240 offset:33920
rt v[198:199],v240 offset:34944
mm v[100:103],v[200:203],v[156:159],v[100:103]
ar v230,a211
ar v231,a215
mm v[100:103],v[204:207],v[188:191],v[100:103]
buffer_load_dwordx2 v[250:251], v248, s[64:67], s5 offen sc1
mm v[68:71],v[204:207],v[184:187],v[68:71]
v_perm_b32 v232, v231, v230, s86
v_perm_b32 v233, v231, v230, s87
mm v[68:71],v[200:203],v[152:155],v[68:71]
rt v[192:193],v240 offset:42368
rt v[194:195],v240 offset:43392
mm v[36:39],v[200:203],v[140:143],v[36:39]
aw a186,v232
aw a190,v233
mm v[36:39],v[204:207],v[172:175],v[36:39]
ar v234,a219
ar v235,a223
mm v[4:7],v[204:207],v[168:171],v[4:7]
wt lgkmcnt(4)
v_perm_b32 v236, v235, v234, s86
mm v[4:7],v[200:203],v[136:139],v[4:7]
rt v[204:205],v241 offset:33920
rt v[206:207],v241 offset:34944
mm v[8:11],v[208:211],v[136:139],v[8:11]
v_perm_b32 v237, v235, v234, s87
aw a187,v236
mm v[8:11],v[212:215],v[168:171],v[8:11]
aw a191,v237
ar v230,a224
mm v[40:43],v[212:215],v[172:175],v[40:43]
ar v231,a228
v_perm_b32 v232, v231, v230, s86
mm v[40:43],v[208:211],v[140:143],v[40:43]
rt v[200:201],v241 offset:42368
rt v[202:203],v241 offset:43392
mm v[72:75],v[208:211],v[152:155],v[72:75]
v_perm_b32 v233, v231, v230, s87
aw a192,v232
mm v[72:75],v[212:215],v[184:187],v[72:75]
aw a196,v233
ar v234,a232
mm v[104:107],v[212:215],v[188:191],v[104:107]
ar v235,a236
v_perm_b32 v236, v235, v234, s86
mm v[104:107],v[208:211],v[156:159],v[104:107]
rt v[212:213],v242 offset:33920
rt v[214:215],v242 offset:34944
mm v[108:111],v[216:219],v[156:159],v[108:111]
v_perm_b32 v237, v235, v234, s87
aw a193,v236
mm v[108:111],v[220:223],v[188:191],v[108:111]
aw a197,v237
ar v230,a240
mm v[76:79],v[220:223],v[184:187],v[76:79]
ar v231,a244
v_perm_b32 v232, v231, v230, s86
mm v[76:79],v[216:219],v[152:155],v[76:79]
rt v[208:209],v242 offset:42368
rt v[210:211],v242 offset:43392
mm v[44:47],v[216:219],v[140:143],v[44:47]
s_bitcmp1_b32 vcc_lo, 13
s_nop 0
s_nop 0
s_cbranch_scc0 .La5_LO_NB13
.La5_LO_KB13:
mm v[44:47],v[220:223],v[172:175],v[44:47]
v_perm_b32 v233, v231, v230, s87
aw a194,v232
mm v[12:15],v[220:223],v[168:171],v[12:15]
wt lgkmcnt(4)
aw a198,v233
mm v[12:15],v[216:219],v[136:139],v[12:15]
rt v[220:221],v243 offset:33920
rt v[222:223],v243 offset:34944
mm v[16:19],v[196:199],v[136:139],v[16:19]
ar v234,a248
ar v235,a252
mm v[16:19],v[192:195],v[168:171],v[16:19]
ld a[36:39],v244,s[44:47],s54 offen offset:2048 sc1
mm v[48:51],v[192:195],v[172:175],v[48:51]
v_perm_b32 v236, v235, v234, s86
v_perm_b32 v237, v235, v234, s87
mm v[48:51],v[196:199],v[140:143],v[48:51]
rt v[216:217],v243 offset:42368
rt v[218:219],v243 offset:43392
mm v[80:83],v[196:199],v[152:155],v[80:83]
aw a195,v236
aw a199,v237
mm v[80:83],v[192:195],v[184:187],v[80:83]
ld a[40:43],v245,s[44:47],s54 offen offset:2048 sc1
mm v[112:115],v[192:195],v[188:191],v[112:115]
wt vmcnt(19)
s_barrier
mm v[112:115],v[196:199],v[156:159],v[112:115]
rt v[192:193],v240 offset:50688
rt v[194:195],v240 offset:51712
mm v[116:119],v[204:207],v[156:159],v[116:119]
s_add_u32 m0, s83, 16896
ar v230,a225
mm v[116:119],v[200:203],v[188:191],v[116:119]
ld v224,s[56:59],s80 offen lds
mm v[84:87],v[200:203],v[184:187],v[84:87]
ar v231,a229
v_perm_b32 v232, v231, v230, s86
mm v[84:87],v[204:207],v[152:155],v[84:87]
rt v[196:197],v240 offset:59136
rt v[198:199],v240 offset:60160
mm v[52:55],v[204:207],v[140:143],v[52:55]
s_add_u32 m0, s83, 17920
v_perm_b32 v233, v231, v230, s87
mm v[52:55],v[200:203],v[172:175],v[52:55]
ld v225,s[56:59],s80 offen lds
mm v[20:23],v[200:203],v[168:171],v[20:23]
wt lgkmcnt(4)
aw a200,v232
mm v[20:23],v[204:207],v[136:139],v[20:23]
rt v[200:201],v241 offset:50688
rt v[202:203],v241 offset:51712
mm v[24:27],v[212:215],v[136:139],v[24:27]
s_add_u32 m0, s83, 19072
aw a204,v233
mm v[24:27],v[208:211],v[168:171],v[24:27]
ld v226,s[56:59],s80 offen lds
mm v[56:59],v[208:211],v[172:175],v[56:59]
ar v234,a233
ar v235,a237
mm v[56:59],v[212:215],v[140:143],v[56:59]
rt v[204:205],v241 offset:59136
rt v[206:207],v241 offset:60160
mm v[88:91],v[212:215],v[152:155],v[88:91]
s_add_u32 m0, s83, 20096
v_perm_b32 v236, v235, v234, s86
mm v[88:91],v[208:211],v[184:187],v[88:91]
ld v227,s[56:59],s80 offen lds
mm v[120:123],v[208:211],v[188:191],v[120:123]
s_bfe_u32 s80, s4, 262168
s_xor_b32 s80, s80, s72
mm v[120:123],v[212:215],v[156:159],v[120:123]
rt v[208:209],v242 offset:50688
rt v[210:211],v242 offset:51712
mm v[124:127],v[220:223],v[156:159],v[124:127]
s_add_u32 m0, s83, 118272
s_lshl_b32 s80, s80, 18
mm v[124:127],v[216:219],v[188:191],v[124:127]
ld v224,s[56:59],s80 offen lds
mm v[92:95],v[216:219],v[184:187],v[92:95]
v_perm_b32 v237, v235, v234, s87
aw a201,v236
mm v[92:95],v[220:223],v[152:155],v[92:95]
rt v[212:213],v242 offset:59136
rt v[214:215],v242 offset:60160
mm v[60:63],v[220:223],v[140:143],v[60:63]
aw a205,v237
ar v230,a241
mm v[60:63],v[216:219],v[172:175],v[60:63]
ar v231,a245
v_perm_b32 v232, v231, v230, s86
mm v[28:31],v[216:219],v[168:171],v[28:31]
wt vmcnt(16) lgkmcnt(4)
v_perm_b32 v233, v231, v230, s87
mm v[28:31],v[220:223],v[136:139],v[28:31]
rt v[216:217],v243 offset:50688
rt v[218:219],v243 offset:51712
mm v[0:3],v[192:195],a[52:55],v[0:3]
s_add_u32 m0, s83, 119296
aw a202,v232
mm v[0:3],v[196:199],a[4:7],v[0:3]
ld v225,s[56:59],s80 offen lds
mm v[32:35],v[196:199],a[8:11],v[32:35]
aw a206,v233
ar v234,a249
mm v[32:35],v[192:195],a[56:59],v[32:35]
rt v[220:221],v243 offset:59136
rt v[222:223],v243 offset:60160
mm v[64:67],v[192:195],a[12:15],v[64:67]
s_add_u32 m0, s83, 120448
ar v235,a253
mm v[64:67],v[196:199],a[16:19],v[64:67]
ld v226,s[56:59],s80 offen lds
mm v[96:99],v[196:199],a[20:23],v[96:99]
v_perm_b32 v236, v235, v234, s86
v_perm_b32 v237, v235, v234, s87
mm v[96:99],v[192:195],a[0:3],v[96:99]
rt v[196:197],v240 offset:50816
rt v[198:199],v240 offset:51840
mm v[100:103],v[200:203],a[0:3],v[100:103]
aw a203,v236
aw a207,v237
mm v[100:103],v[204:207],a[20:23],v[100:103]
ld v[136:139],v246,s[44:47],s54 offen offset:2048 sc1
mm v[68:71],v[204:207],a[16:19],v[68:71]
ar v230,a226
ar v231,a230
mm v[68:71],v[200:203],a[12:15],v[68:71]
rt v[192:193],v240 offset:59264
rt v[194:195],v240 offset:60288
mm v[36:39],v[200:203],a[56:59],v[36:39]
v_perm_b32 v232, v231, v230, s86
v_perm_b32 v233, v231, v230, s87
mm v[36:39],v[204:207],a[8:11],v[36:39]
ld v[140:143],v247,s[44:47],s54 offen offset:2048 sc1
mm v[4:7],v[204:207],a[4:7],v[4:7]
wt lgkmcnt(4)
aw a208,v232
mm v[4:7],v[200:203],a[52:55],v[4:7]
rt v[204:205],v241 offset:50816
rt v[206:207],v241 offset:51840
mm v[8:11],v[208:211],a[52:55],v[8:11]
aw a212,v233
ar v234,a234
mm v[8:11],v[212:215],a[4:7],v[8:11]
ld v[152:155],v244,s[44:47],s54 offen offset:3072 sc1
mm v[40:43],v[212:215],a[8:11],v[40:43]
ar v235,a238
v_perm_b32 v236, v235, v234, s86
mm v[40:43],v[208:211],a[56:59],v[40:43]
rt v[200:201],v241 offset:59264
rt v[202:203],v241 offset:60288
mm v[72:75],v[208:211],a[12:15],v[72:75]
v_perm_b32 v237, v235, v234, s87
aw a209,v236
mm v[72:75],v[212:215],a[16:19],v[72:75]
ld v[156:159],v245,s[44:47],s54 offen offset:3072 sc1
mm v[104:107],v[212:215],a[20:23],v[104:107]
aw a213,v237
ar v230,a242
mm v[104:107],v[208:211],a[0:3],v[104:107]
rt v[212:213],v242 offset:50816
rt v[214:215],v242 offset:51840
mm v[108:111],v[216:219],a[0:3],v[108:111]
ar v231,a246
v_perm_b32 v232, v231, v230, s86
mm v[108:111],v[220:223],a[20:23],v[108:111]
ld v[168:171],v246,s[44:47],s54 offen offset:3072 sc1
mm v[76:79],v[220:223],a[16:19],v[76:79]
v_perm_b32 v233, v231, v230, s87
aw a210,v232
mm v[76:79],v[216:219],a[12:15],v[76:79]
rt v[208:209],v242 offset:59264
rt v[210:211],v242 offset:60288
mm v[44:47],v[216:219],a[56:59],v[44:47]
aw a214,v233
ar v234,a250
mm v[44:47],v[220:223],a[8:11],v[44:47]
ar v235,a254
v_perm_b32 v236, v235, v234, s86
mm v[12:15],v[220:223],a[4:7],v[12:15]
wt lgkmcnt(4)
v_perm_b32 v237, v235, v234, s87
mm v[12:15],v[216:219],a[52:55],v[12:15]
rt v[220:221],v243 offset:50816
rt v[222:223],v243 offset:51840
mm v[16:19],v[196:199],a[52:55],v[16:19]
aw a211,v236
aw a215,v237
mm v[16:19],v[192:195],a[4:7],v[16:19]
ld v[172:175],v247,s[44:47],s54 offen offset:3072 sc1
mm v[48:51],v[192:195],a[8:11],v[48:51]
ar v230,a227
ar v231,a231
mm v[48:51],v[196:199],a[56:59],v[48:51]
rt v[216:217],v243 offset:59264
rt v[218:219],v243 offset:60288
mm v[80:83],v[196:199],a[12:15],v[80:83]
s_add_u32 m0, s83, 121472
v_perm_b32 v232, v231, v230, s86
mm v[80:83],v[192:195],a[16:19],v[80:83]
ld v227,s[56:59],s80 offen lds
mm v[112:115],v[192:195],a[20:23],v[112:115]
s_add_u32 s80, s80, 131072
v_perm_b32 v233, v231, v230, s87
mm v[112:115],v[196:199],a[0:3],v[112:115]
rt v[192:193],v240 offset:0
rt v[194:195],v240 offset:1024
mm v[116:119],v[204:207],a[0:3],v[116:119]
s_add_u32 m0, s83, 67584
aw a216,v232
mm v[116:119],v[200:203],a[20:23],v[116:119]
ld v224,s[56:59],s80 offen lds
mm v[84:87],v[200:203],a[16:19],v[84:87]
aw a220,v233
ar v234,a235
mm v[84:87],v[204:207],a[12:15],v[84:87]
rt v[196:197],v240 offset:8448
rt v[198:199],v240 offset:9472
mm v[52:55],v[204:207],a[56:59],v[52:55]
s_add_u32 m0, s83, 68608
ar v235,a239
mm v[52:55],v[200:203],a[8:11],v[52:55]
ld v225,s[56:59],s80 offen lds
mm v[20:23],v[200:203],a[4:7],v[20:23]
wt lgkmcnt(4)
v_perm_b32 v236, v235, v234, s86
mm v[20:23],v[204:207],a[52:55],v[20:23]
rt v[200:201],v241 offset:0
rt v[202:203],v241 offset:1024
mm v[24:27],v[212:215],a[52:55],v[24:27]
s_add_u32 m0, s83, 69760
v_perm_b32 v237, v235, v234, s87
mm v[24:27],v[208:211],a[4:7],v[24:27]
ld v226,s[56:59],s80 offen lds
wt vmcnt(19)
v_cmp_eq_u64 vcc, v[250:251], v[252:253]
mm v[56:59],v[208:211],a[8:11],v[56:59]
aw a217,v236
aw a221,v237
mm v[56:59],v[212:215],a[56:59],v[56:59]
rt v[204:205],v241 offset:8448
rt v[206:207],v241 offset:9472
mm v[88:91],v[212:215],a[12:15],v[88:91]
s_add_u32 m0, s83, 70784
ar v230,a243
mm v[88:91],v[208:211],a[16:19],v[88:91]
ld v227,s[56:59],s80 offen lds
mm v[120:123],v[208:211],a[20:23],v[120:123]
s_bfe_u32 s80, s4, 262172
s_xor_b32 s80, s80, s72
mm v[120:123],v[212:215],a[0:3],v[120:123]
rt v[208:209],v242 offset:0
rt v[210:211],v242 offset:1024
mm v[124:127],v[220:223],a[0:3],v[124:127]
s_lshl_b32 s80, s80, 18
ar v231,a247
mm v[124:127],v[216:219],a[20:23],v[124:127]
buffer_load_dwordx2 v[250:251], v248, s[64:67], s5 offen sc1
mm v[92:95],v[216:219],a[16:19],v[92:95]
v_perm_b32 v232, v231, v230, s86
v_perm_b32 v233, v231, v230, s87
mm v[92:95],v[220:223],a[12:15],v[92:95]
rt v[212:213],v242 offset:8448
rt v[214:215],v242 offset:9472
mm v[60:63],v[220:223],a[56:59],v[60:63]
s_add_u32 m0, s83, 84480
aw a218,v232
mm v[60:63],v[216:219],a[8:11],v[60:63]
ld v224,s[56:59],s80 offen lds
mm v[28:31],v[216:219],a[4:7],v[28:31]
wt lgkmcnt(4)
aw a222,v233
mm v[28:31],v[220:223],a[52:55],v[28:31]
rt v[216:217],v243 offset:0
rt v[218:219],v243 offset:1024
mm v[0:3],v[192:195],a[24:27],v[0:3]
s_add_u32 m0, s83, 85504
ar v234,a251
mm v[0:3],v[196:199],v[132:135],v[0:3]
ld v225,s[56:59],s80 offen lds
mm v[32:35],v[196:199],v[144:147],v[32:35]
ar v235,a255
v_perm_b32 v236, v235, v234, s86
mm v[32:35],v[192:195],a[28:31],v[32:35]
rt v[220:221],v243 offset:8448
rt v[222:223],v243 offset:9472
mm v[64:67],v[192:195],a[60:63],v[64:67]
s_add_u32 m0, s83, 86656
v_perm_b32 v237, v235, v234, s87
.La5_LW3_3:
s_cbranch_execz .La5_LA_RETURN
mm v[64:67],v[196:199],a[44:47],v[64:67]
ld v226,s[56:59],s80 offen lds
mm v[96:99],v[196:199],a[32:35],v[96:99]
aw a219,v236
aw a223,v237
mm v[96:99],v[192:195],v[128:131],v[96:99]
rt v[196:197],v240 offset:128
rt v[198:199],v240 offset:1152
mm v[100:103],v[200:203],v[128:131],v[100:103]
s_add_u32 m0, s83, 87680
mm v[100:103],v[204:207],a[32:35],v[100:103]
ld v227,s[56:59],s80 offen lds
mm v[68:71],v[204:207],a[44:47],v[68:71]
s_add_u32 s80, s80, 131072
mm v[68:71],v[200:203],a[60:63],v[68:71]
rt v[192:193],v240 offset:8576
rt v[194:195],v240 offset:9600
mm v[36:39],v[200:203],a[28:31],v[36:39]
s_add_u32 m0, s83, 101376
mm v[36:39],v[204:207],v[144:147],v[36:39]
ld v224,s[56:59],s80 offen lds
mm v[4:7],v[204:207],v[132:135],v[4:7]
wt lgkmcnt(4)
mm v[4:7],v[200:203],a[24:27],v[4:7]
rt v[204:205],v241 offset:128
rt v[206:207],v241 offset:1152
mm v[8:11],v[208:211],a[24:27],v[8:11]
s_add_u32 m0, s83, 102400
mm v[8:11],v[212:215],v[132:135],v[8:11]
ld v225,s[56:59],s80 offen lds
mm v[40:43],v[212:215],v[144:147],v[40:43]
mm v[40:43],v[208:211],a[28:31],v[40:43]
rt v[200:201],v241 offset:8576
rt v[202:203],v241 offset:9600
mm v[72:75],v[208:211],a[60:63],v[72:75]
s_add_u32 m0, s83, 103552
mm v[72:75],v[212:215],a[44:47],v[72:75]
ld v226,s[56:59],s80 offen lds
mm v[104:107],v[212:215],a[32:35],v[104:107]
mm v[104:107],v[208:211],v[128:131],v[104:107]
rt v[212:213],v242 offset:128
rt v[214:215],v242 offset:1152
mm v[108:111],v[216:219],v[128:131],v[108:111]
s_add_u32 m0, s83, 104576
mm v[108:111],v[220:223],a[32:35],v[108:111]
ld v227,s[56:59],s80 offen lds
mm v[76:79],v[220:223],a[44:47],v[76:79]
v_bfe_u32 v224, v255, 6, 1
v_lshrrev_b32 v225, 7, v255
mm v[76:79],v[216:219],a[60:63],v[76:79]
rt v[208:209],v242 offset:8576
rt v[210:211],v242 offset:9600
mm v[44:47],v[216:219],a[28:31],v[44:47]
s_bitcmp1_b32 vcc_lo, 6
s_cbranch_scc0 .La5_LO_NB6
.La5_LO_KB6:
mm v[44:47],v[220:223],v[144:147],v[44:47]
v_and_b32 v226, 48, v255
v_lshl_add_u32 v225, v225, 6, v226
mm v[12:15],v[220:223],v[132:135],v[12:15]
wt lgkmcnt(4)
v_lshl_add_u32 v224, v224, 11, v225
mm v[12:15],v[216:219],a[24:27],v[12:15]
rt v[220:221],v243 offset:128
rt v[222:223],v243 offset:1152
mm v[16:19],v[196:199],a[24:27],v[16:19]
mm v[16:19],v[192:195],v[132:135],v[16:19]
ld a[64:67],v244,s[44:47],s55 offen offset:0 sc1
mm v[48:51],v[192:195],v[144:147],v[48:51]
mm v[48:51],v[196:199],a[28:31],v[48:51]
rt v[216:217],v243 offset:8576
rt v[218:219],v243 offset:9600
mm v[80:83],v[196:199],a[60:63],v[80:83]
mm v[80:83],v[192:195],a[44:47],v[80:83]
ld a[68:71],v245,s[44:47],s55 offen offset:0 sc1
mm v[112:115],v[192:195],a[32:35],v[112:115]
wt vmcnt(25)
s_barrier
mm v[112:115],v[196:199],v[128:131],v[112:115]
rt v[192:193],v240 offset:16896
rt v[194:195],v240 offset:17920
mm v[116:119],v[204:207],v[128:131],v[116:119]
mm v[116:119],v[200:203],a[32:35],v[116:119]
ld a[72:75],v244,s[44:47],s55 offen offset:1024 sc1
mm v[84:87],v[200:203],a[44:47],v[84:87]
mm v[84:87],v[204:207],a[60:63],v[84:87]
rt v[196:197],v240 offset:25344
rt v[198:199],v240 offset:26368
mm v[52:55],v[204:207],a[28:31],v[52:55]
mm v[52:55],v[200:203],v[144:147],v[52:55]
ld a[76:79],v245,s[44:47],s55 offen offset:1024 sc1
mm v[20:23],v[200:203],v[132:135],v[20:23]
wt lgkmcnt(4)
mm v[20:23],v[204:207],a[24:27],v[20:23]
rt v[200:201],v241 offset:16896
rt v[202:203],v241 offset:17920
mm v[24:27],v[212:215],a[24:27],v[24:27]
mm v[24:27],v[208:211],v[132:135],v[24:27]
ld a[80:83],v244,s[44:47],s55 offen offset:2048 sc1
mm v[56:59],v[208:211],v[144:147],v[56:59]
mm v[56:59],v[212:215],a[28:31],v[56:59]
rt v[204:205],v241 offset:25344
rt v[206:207],v241 offset:26368
mm v[88:91],v[212:215],a[60:63],v[88:91]
mm v[88:91],v[208:211],a[44:47],v[88:91]
ld a[84:87],v245,s[44:47],s55 offen offset:2048 sc1
mm v[120:123],v[208:211],a[32:35],v[120:123]
mm v[120:123],v[212:215],v[128:131],v[120:123]
rt v[208:209],v242 offset:16896
rt v[210:211],v242 offset:17920
mm v[124:127],v[220:223],v[128:131],v[124:127]
mm v[124:127],v[216:219],a[32:35],v[124:127]
ld a[88:91],v244,s[44:47],s55 offen offset:3072 sc1
mm v[92:95],v[216:219],a[44:47],v[92:95]
mm v[92:95],v[220:223],a[60:63],v[92:95]
rt v[212:213],v242 offset:25344
rt v[214:215],v242 offset:26368
mm v[60:63],v[220:223],a[28:31],v[60:63]
mm v[60:63],v[216:219],v[144:147],v[60:63]
mm v[28:31],v[216:219],v[132:135],v[28:31]
wt vmcnt(21) lgkmcnt(4)
mm v[28:31],v[220:223],a[24:27],v[28:31]
rt v[216:217],v243 offset:16896
rt v[218:219],v243 offset:17920
mm v[0:3],v[192:195],a[36:39],v[0:3]
s_bitcmp1_b32 vcc_lo, 7
s_cbranch_scc0 .La5_LO_NB7
.La5_LO_KB7:
mm v[0:3],v[196:199],v[152:155],v[0:3]
ld a[92:95],v245,s[44:47],s55 offen offset:3072 sc1
mm v[32:35],v[196:199],v[156:159],v[32:35]
mm v[32:35],v[192:195],a[40:43],v[32:35]
rt v[220:221],v243 offset:25344
rt v[222:223],v243 offset:26368
mm v[64:67],v[192:195],v[136:139],v[64:67]
mm v[64:67],v[196:199],v[168:171],v[64:67]
ld a[0:3],v244,s[44:47],s52 offen offset:0 sc1
mm v[96:99],v[196:199],v[172:175],v[96:99]
mm v[96:99],v[192:195],v[140:143],v[96:99]
rt v[196:197],v240 offset:17024
rt v[198:199],v240 offset:18048
mm v[100:103],v[200:203],v[140:143],v[100:103]
mm v[100:103],v[204:207],v[172:175],v[100:103]
ld a[4:7],v245,s[44:47],s52 offen offset:0 sc1
mm v[68:71],v[204:207],v[168:171],v[68:71]
mm v[68:71],v[200:203],v[136:139],v[68:71]
rt v[192:193],v240 offset:25472
rt v[194:195],v240 offset:26496
mm v[36:39],v[200:203],a[40:43],v[36:39]
v_add_u32 v240, 67584, v240
mm v[36:39],v[204:207],v[156:159],v[36:39]
ld a[8:11],v244,s[44:47],s52 offen offset:1024 sc1
mm v[4:7],v[204:207],v[152:155],v[4:7]
wt lgkmcnt(4)
mm v[4:7],v[200:203],a[36:39],v[4:7]
rt v[204:205],v241 offset:17024
rt v[206:207],v241 offset:18048
mm v[8:11],v[208:211],a[36:39],v[8:11]
mm v[8:11],v[212:215],v[152:155],v[8:11]
ld a[12:15],v245,s[44:47],s52 offen offset:1024 sc1
mm v[40:43],v[212:215],v[156:159],v[40:43]
mm v[40:43],v[208:211],a[40:43],v[40:43]
rt v[200:201],v241 offset:25472
rt v[202:203],v241 offset:26496
mm v[72:75],v[208:211],v[136:139],v[72:75]
v_add_u32 v241, 67584, v241
mm v[72:75],v[212:215],v[168:171],v[72:75]
ld a[16:19],v244,s[44:47],s52 offen offset:2048 sc1
mm v[104:107],v[212:215],v[172:175],v[104:107]
mm v[104:107],v[208:211],v[140:143],v[104:107]
rt v[212:213],v242 offset:17024
rt v[214:215],v242 offset:18048
mm v[108:111],v[216:219],v[140:143],v[108:111]
mm v[108:111],v[220:223],v[172:175],v[108:111]
ld a[20:23],v245,s[44:47],s52 offen offset:2048 sc1
mm v[76:79],v[220:223],v[168:171],v[76:79]
mm v[76:79],v[216:219],v[136:139],v[76:79]
rt v[208:209],v242 offset:25472
rt v[210:211],v242 offset:26496
mm v[44:47],v[216:219],a[40:43],v[44:47]
v_add_u32 v242, 67584, v242
mm v[44:47],v[220:223],v[156:159],v[44:47]
wt vmcnt(22)
v_cmp_eq_u64 vcc, v[250:251], v[252:253]
mm v[12:15],v[220:223],v[152:155],v[12:15]
wt lgkmcnt(4)
mm v[12:15],v[216:219],a[36:39],v[12:15]
rt v[220:221],v243 offset:17024
rt v[222:223],v243 offset:18048
mm v[16:19],v[196:199],a[36:39],v[16:19]
mm v[16:19],v[192:195],v[152:155],v[16:19]
ld a[24:27],v244,s[44:47],s52 offen offset:3072 sc1
mm v[48:51],v[192:195],v[156:159],v[48:51]
mm v[48:51],v[196:199],a[40:43],v[48:51]
rt v[216:217],v243 offset:25472
rt v[218:219],v243 offset:26496
mm v[80:83],v[196:199],v[136:139],v[80:83]
v_add_u32 v243, 67584, v243
mm v[80:83],v[192:195],v[168:171],v[80:83]
ld a[28:31],v245,s[44:47],s52 offen offset:3072 sc1
mm v[112:115],v[192:195],v[172:175],v[112:115]
wt vmcnt(16)
s_barrier
mm v[112:115],v[196:199],v[140:143],v[112:115]
rt v[192:193],v240 offset:50688
rt v[194:195],v240 offset:51712
mm v[116:119],v[204:207],v[140:143],v[116:119]
mm v[116:119],v[200:203],v[172:175],v[116:119]
ld a[32:35],v246,s[44:47],s55 offen offset:0 sc1
mm v[84:87],v[200:203],v[168:171],v[84:87]
mm v[84:87],v[204:207],v[136:139],v[84:87]
rt v[196:197],v240 offset:59136
rt v[198:199],v240 offset:60160
mm v[52:55],v[204:207],a[40:43],v[52:55]
mm v[52:55],v[200:203],v[156:159],v[52:55]
ld a[44:47],v247,s[44:47],s55 offen offset:0 sc1
mm v[20:23],v[200:203],v[152:155],v[20:23]
wt lgkmcnt(4)
mm v[20:23],v[204:207],a[36:39],v[20:23]
rt v[200:201],v240 offset:0
rt v[202:203],v240 offset:1024
mm v[24:27],v[212:215],a[36:39],v[24:27]
mm v[24:27],v[208:211],v[152:155],v[24:27]
ld a[48:51],v246,s[44:47],s55 offen offset:1024 sc1
mm v[56:59],v[208:211],v[156:159],v[56:59]
mm v[56:59],v[212:215],a[40:43],v[56:59]
rt v[204:205],v240 offset:8448
rt v[206:207],v240 offset:9472
mm v[88:91],v[212:215],v[136:139],v[88:91]
mm v[88:91],v[208:211],v[168:171],v[88:91]
ld a[52:55],v247,s[44:47],s55 offen offset:1024 sc1
mm v[120:123],v[208:211],v[172:175],v[120:123]
mm v[120:123],v[212:215],v[140:143],v[120:123]
rt v[208:209],v240 offset:16896
rt v[210:211],v240 offset:17920
mm v[124:127],v[220:223],v[140:143],v[124:127]
mm v[124:127],v[216:219],v[172:175],v[124:127]
ld a[56:59],v246,s[44:47],s55 offen offset:2048 sc1
mm v[92:95],v[216:219],v[168:171],v[92:95]
mm v[92:95],v[220:223],v[136:139],v[92:95]
rt v[212:213],v240 offset:25344
rt v[214:215],v240 offset:26368
mm v[60:63],v[220:223],a[40:43],v[60:63]
mm v[60:63],v[216:219],v[156:159],v[60:63]
mm v[28:31],v[216:219],v[152:155],v[28:31]
wt vmcnt(13) lgkmcnt(4)
mm v[28:31],v[220:223],a[36:39],v[28:31]
rt v[216:217],v240 offset:33792
rt v[218:219],v240 offset:34816
mm v[0:3],v[192:195],a[64:67],v[0:3]
ld a[36:39],v247,s[44:47],s55 offen offset:2048 sc1
mm v[0:3],v[196:199],a[72:75],v[0:3]
rt v[220:221],v240 offset:42240
rt v[222:223],v240 offset:43264
mm v[32:35],v[196:199],a[76:79],v[32:35]
mm v[32:35],v[192:195],a[68:71],v[32:35]
rt v[196:197],v241 offset:50688
rt v[198:199],v241 offset:51712
mm v[32:35],v[200:203],a[84:87],v[32:35]
ld a[40:43],v246,s[44:47],s55 offen offset:3072 sc1
mm v[32:35],v[204:207],a[92:95],v[32:35]
rt v[192:193],v241 offset:59136
rt v[194:195],v241 offset:60160
mm v[0:3],v[204:207],a[88:91],v[0:3]
wt vmcnt(7) lgkmcnt(4)
mm v[0:3],v[200:203],a[80:83],v[0:3]
rt v[204:205],v241 offset:0
rt v[206:207],v241 offset:1024
mm v[0:3],v[208:211],a[0:3],v[0:3]
ld a[60:63],v247,s[44:47],s55 offen offset:3072 sc1
mm v[0:3],v[212:215],a[8:11],v[0:3]
rt v[200:201],v241 offset:8448
rt v[202:203],v241 offset:9472
mm v[32:35],v[212:215],a[12:15],v[32:35]
mm v[32:35],v[208:211],a[4:7],v[32:35]
rt v[212:213],v241 offset:16896
rt v[214:215],v241 offset:17920
mm v[32:35],v[216:219],a[20:23],v[32:35]
ld v[128:131],v246,s[44:47],s52 offen offset:0 sc1
mm v[32:35],v[220:223],a[28:31],v[32:35]
rt v[208:209],v241 offset:25344
rt v[210:211],v241 offset:26368
mm v[0:3],v[220:223],a[24:27],v[0:3]
wt lgkmcnt(4)
mm v[0:3],v[216:219],a[16:19],v[0:3]
rt v[220:221],v241 offset:33792
rt v[222:223],v241 offset:34816
mm v[4:7],v[196:199],a[64:67],v[4:7]
ld v[132:135],v247,s[44:47],s52 offen offset:0 sc1
mm v[4:7],v[192:195],a[72:75],v[4:7]
rt v[216:217],v241 offset:42240
rt v[218:219],v241 offset:43264
mm v[36:39],v[192:195],a[76:79],v[36:39]
mm v[36:39],v[196:199],a[68:71],v[36:39]
rt v[192:193],v242 offset:50688
rt v[194:195],v242 offset:51712
mm v[36:39],v[204:207],a[84:87],v[36:39]
ld v[144:147],v246,s[44:47],s52 offen offset:1024 sc1
mm v[36:39],v[200:203],a[92:95],v[36:39]
rt v[196:197],v242 offset:59136
rt v[198:199],v242 offset:60160
mm v[4:7],v[200:203],a[88:91],v[4:7]
wt lgkmcnt(4)
mm v[4:7],v[204:207],a[80:83],v[4:7]
rt v[200:201],v242 offset:0
rt v[202:203],v242 offset:1024
mm v[4:7],v[212:215],a[0:3],v[4:7]
ld v[148:151],v247,s[44:47],s52 offen offset:1024 sc1
mm v[4:7],v[208:211],a[8:11],v[4:7]
rt v[204:205],v242 offset:8448
rt v[206:207],v242 offset:9472
mm v[36:39],v[208:211],a[12:15],v[36:39]
mm v[36:39],v[212:215],a[4:7],v[36:39]
rt v[208:209],v242 offset:16896
rt v[210:211],v242 offset:17920
mm v[36:39],v[220:223],a[20:23],v[36:39]
mm v[36:39],v[216:219],a[28:31],v[36:39]
rt v[212:213],v242 offset:25344
rt v[214:215],v242 offset:26368
mm v[4:7],v[216:219],a[24:27],v[4:7]
wt lgkmcnt(4)
mm v[4:7],v[220:223],a[16:19],v[4:7]
rt v[216:217],v242 offset:33792
rt v[218:219],v242 offset:34816
mm v[8:11],v[192:195],a[64:67],v[8:11]
ld v[160:163],v246,s[44:47],s52 offen offset:2048 sc1
mm v[8:11],v[196:199],a[72:75],v[8:11]
rt v[220:221],v242 offset:42240
rt v[222:223],v242 offset:43264
mm v[40:43],v[196:199],a[76:79],v[40:43]
cv v234,v32,v33
cv v236,v36,v37
mm v[40:43],v[192:195],a[68:71],v[40:43]
rt v[196:197],v243 offset:50688
rt v[198:199],v243 offset:51712
mm v[40:43],v[200:203],a[84:87],v[40:43]
ld v[164:167],v247,s[44:47],s52 offen offset:2048 sc1
mm v[40:43],v[204:207],a[92:95],v[40:43]
rt v[192:193],v243 offset:59136
rt v[194:195],v243 offset:60160
mm v[8:11],v[204:207],a[88:91],v[8:11]
wt lgkmcnt(4)
mm v[8:11],v[200:203],a[80:83],v[8:11]
rt v[204:205],v243 offset:0
rt v[206:207],v243 offset:1024
mm v[8:11],v[208:211],a[0:3],v[8:11]
ld v[176:179],v246,s[44:47],s52 offen offset:3072 sc1
mm v[8:11],v[212:215],a[8:11],v[8:11]
rt v[200:201],v243 offset:8448
rt v[202:203],v243 offset:9472
mm v[40:43],v[212:215],a[12:15],v[40:43]
cv v235,v34,v35
cv v237,v38,v39
mm v[40:43],v[208:211],a[4:7],v[40:43]
rt v[212:213],v243 offset:16896
rt v[214:215],v243 offset:17920
mm v[40:43],v[216:219],a[20:23],v[40:43]
ld v[180:183],v247,s[44:47],s52 offen offset:3072 sc1
mm v[40:43],v[220:223],a[28:31],v[40:43]
rt v[208:209],v243 offset:25344
rt v[210:211],v243 offset:26368
mm v[8:11],v[220:223],a[24:27],v[8:11]
wt lgkmcnt(4)
v_bfe_u32 v250, v255, 6, 1
mm v[8:11],v[216:219],a[16:19],v[8:11]
rt v[220:221],v243 offset:33792
rt v[222:223],v243 offset:34816
mm v[12:15],v[196:199],a[64:67],v[12:15]
v_permlane16_swap_b32 v234, v236
v_permlane16_swap_b32 v235, v237
mm v[12:15],v[192:195],a[72:75],v[12:15]
rt v[216:217],v243 offset:42240
rt v[218:219],v243 offset:43264
mm v[44:47],v[192:195],a[76:79],v[44:47]
cv v230,v0,v1
cv v232,v4,v5
mm v[44:47],v[196:199],a[68:71],v[44:47]
rt v[192:193],v240 offset:50816
rt v[194:195],v240 offset:51840
mm v[44:47],v[204:207],a[84:87],v[44:47]
st v[234:237],v238,s[32:35],s95 offen offset:0 sc1
mm v[44:47],v[200:203],a[92:95],v[44:47]
rt v[196:197],v240 offset:59264
rt v[198:199],v240 offset:60288
mm v[12:15],v[200:203],a[88:91],v[12:15]
wt lgkmcnt(4)
v_mul_u32_u24 v250, 7, v250
mm v[12:15],v[204:207],a[80:83],v[12:15]
rt v[200:201],v240 offset:128
rt v[202:203],v240 offset:1152
mm v[12:15],v[212:215],a[0:3],v[12:15]
cv v231,v2,v3
cv v233,v6,v7
mm v[12:15],v[208:211],a[8:11],v[12:15]
rt v[204:205],v240 offset:8576
rt v[206:207],v240 offset:9600
mm v[44:47],v[208:211],a[12:15],v[44:47]
v_permlane16_swap_b32 v230, v232
v_permlane16_swap_b32 v231, v233
mm v[44:47],v[212:215],a[4:7],v[44:47]
rt v[208:209],v240 offset:17024
rt v[210:211],v240 offset:18048
mm v[44:47],v[220:223],a[20:23],v[44:47]
st v[230:233],v238,s[32:35],s94 offen offset:0 sc1
mm v[44:47],v[216:219],a[28:31],v[44:47]
rt v[212:213],v240 offset:25472
rt v[214:215],v240 offset:26496
mm v[12:15],v[216:219],a[24:27],v[12:15]
wt lgkmcnt(4)
v_xor_b32 v250, s3, v250
mm v[12:15],v[220:223],a[16:19],v[12:15]
rt v[216:217],v240 offset:33920
rt v[218:219],v240 offset:34944
mm v[16:19],v[192:195],a[64:67],v[16:19]
cv v234,v40,v41
cv v236,v44,v45
mm v[16:19],v[196:199],a[72:75],v[16:19]
rt v[220:221],v240 offset:42368
rt v[222:223],v240 offset:43392
mm v[48:51],v[196:199],a[76:79],v[48:51]
cv v235,v42,v43
cv v237,v46,v47
mm v[48:51],v[192:195],a[68:71],v[48:51]
rt v[196:197],v241 offset:50816
rt v[198:199],v241 offset:51840
mm v[48:51],v[200:203],a[84:87],v[48:51]
v_permlane16_swap_b32 v234, v236
v_permlane16_swap_b32 v235, v237
mm v[48:51],v[204:207],a[92:95],v[48:51]
rt v[192:193],v241 offset:59264
rt v[194:195],v241 offset:60288
mm v[16:19],v[204:207],a[88:91],v[16:19]
wt lgkmcnt(4)
v_bfe_u32 v251, v255, 4, 2
mm v[16:19],v[200:203],a[80:83],v[16:19]
rt v[204:205],v241 offset:128
rt v[206:207],v241 offset:1152
mm v[16:19],v[208:211],a[0:3],v[16:19]
st v[234:237],v238,s[32:35],s95 offen offset:1024 sc1
mm v[16:19],v[212:215],a[8:11],v[16:19]
rt v[200:201],v241 offset:8576
rt v[202:203],v241 offset:9600
mm v[48:51],v[212:215],a[12:15],v[48:51]
cv v230,v8,v9
cv v232,v12,v13
mm v[48:51],v[208:211],a[4:7],v[48:51]
rt v[212:213],v241 offset:17024
rt v[214:215],v241 offset:18048
mm v[48:51],v[216:219],a[20:23],v[48:51]
cv v231,v10,v11
cv v233,v14,v15
mm v[48:51],v[220:223],a[28:31],v[48:51]
rt v[208:209],v241 offset:25472
rt v[210:211],v241 offset:26496
mm v[16:19],v[220:223],a[24:27],v[16:19]
wt lgkmcnt(4)
v_lshl_add_u32 v250, v250, 2, v251
mm v[16:19],v[216:219],a[16:19],v[16:19]
rt v[220:221],v241 offset:33920
rt v[222:223],v241 offset:34944
mm v[20:23],v[196:199],a[64:67],v[20:23]
v_permlane16_swap_b32 v230, v232
v_permlane16_swap_b32 v231, v233
mm v[20:23],v[192:195],a[72:75],v[20:23]
rt v[216:217],v241 offset:42368
rt v[218:219],v241 offset:43392
mm v[52:55],v[192:195],a[76:79],v[52:55]
v_lshlrev_b32 v250, 9, v250
v_and_b32 v251, 64, v255
mm v[52:55],v[196:199],a[68:71],v[52:55]
rt v[192:193],v242 offset:50816
rt v[194:195],v242 offset:51840
mm v[52:55],v[204:207],a[84:87],v[52:55]
st v[230:233],v238,s[32:35],s94 offen offset:1024 sc1
mm v[52:55],v[200:203],a[92:95],v[52:55]
rt v[196:197],v242 offset:59264
rt v[198:199],v242 offset:60288
mm v[20:23],v[200:203],a[88:91],v[20:23]
wt lgkmcnt(4)
v_lshl_add_u32 v251, v251, 24, v250
mm v[20:23],v[204:207],a[80:83],v[20:23]
rt v[200:201],v242 offset:128
rt v[202:203],v242 offset:1152
mm v[20:23],v[212:215],a[0:3],v[20:23]
ld v[136:139],v224,s[68:71],0 offen offset:0
mm v[20:23],v[208:211],a[8:11],v[20:23]
rt v[204:205],v242 offset:8576
rt v[206:207],v242 offset:9600
mm v[52:55],v[208:211],a[12:15],v[52:55]
v_and_b32 v248, 0x1f0, v255
v_lshrrev_b32 v248, 1, v248
mm v[52:55],v[212:215],a[4:7],v[52:55]
rt v[208:209],v242 offset:17024
rt v[210:211],v242 offset:18048
mm v[52:55],v[220:223],a[20:23],v[52:55]
ld v[140:143],v225,s[48:51],0 offen offset:0
mm v[52:55],v[216:219],a[28:31],v[52:55]
rt v[212:213],v242 offset:25472
rt v[214:215],v242 offset:26496
mm v[20:23],v[216:219],a[24:27],v[20:23]
wt lgkmcnt(4)
s_mov_b32 s93, 0
ld a[224:227],v229,s[28:31],s93 offen offset:0
mm v[20:23],v[220:223],a[16:19],v[20:23]
rt v[216:217],v242 offset:33920
rt v[218:219],v242 offset:34944
mm v[24:27],v[192:195],a[64:67],v[24:27]
cv v234,v48,v49
cv v236,v52,v53
mm v[24:27],v[196:199],a[72:75],v[24:27]
rt v[220:221],v242 offset:42368
rt v[222:223],v242 offset:43392
mm v[56:59],v[196:199],a[76:79],v[56:59]
cv v235,v50,v51
cv v237,v54,v55
mm v[56:59],v[192:195],a[68:71],v[56:59]
rt v[196:197],v243 offset:50816
rt v[198:199],v243 offset:51840
mm v[56:59],v[200:203],a[84:87],v[56:59]
v_permlane16_swap_b32 v234, v236
v_permlane16_swap_b32 v235, v237
mm v[56:59],v[204:207],a[92:95],v[56:59]
rt v[192:193],v243 offset:59264
rt v[194:195],v243 offset:60288
mm v[24:27],v[204:207],a[88:91],v[24:27]
wt lgkmcnt(4)
ld a[228:231],v229,s[28:31],s93 offen offset:2048
mm v[24:27],v[200:203],a[80:83],v[24:27]
rt v[204:205],v243 offset:128
rt v[206:207],v243 offset:1152
mm v[24:27],v[208:211],a[0:3],v[24:27]
st v[234:237],v238,s[32:35],s95 offen offset:2048 sc1
mm v[24:27],v[212:215],a[8:11],v[24:27]
rt v[200:201],v243 offset:8576
rt v[202:203],v243 offset:9600
mm v[56:59],v[212:215],a[12:15],v[56:59]
cv v230,v16,v17
cv v232,v20,v21
.La5_LW3_4:
s_cbranch_execz .La5_LA_RETURN
mm v[56:59],v[208:211],a[4:7],v[56:59]
rt v[212:213],v243 offset:17024
rt v[214:215],v243 offset:18048
mm v[56:59],v[216:219],a[20:23],v[56:59]
cv v231,v18,v19
cv v233,v22,v23
mm v[56:59],v[220:223],a[28:31],v[56:59]
rt v[208:209],v243 offset:25472
rt v[210:211],v243 offset:26496
mm v[24:27],v[220:223],a[24:27],v[24:27]
wt lgkmcnt(4)
s_mov_b32 s93, 4096
ld a[232:235],v229,s[28:31],s93 offen offset:0
mm v[24:27],v[216:219],a[16:19],v[24:27]
rt v[220:221],v243 offset:33920
rt v[222:223],v243 offset:34944
mm v[28:31],v[196:199],a[64:67],v[28:31]
v_permlane16_swap_b32 v230, v232
v_permlane16_swap_b32 v231, v233
mm v[28:31],v[192:195],a[72:75],v[28:31]
rt v[216:217],v243 offset:42368
rt v[218:219],v243 offset:43392
mm v[60:63],v[192:195],a[76:79],v[60:63]
ld a[236:239],v229,s[28:31],s93 offen offset:2048
mm v[60:63],v[196:199],a[68:71],v[60:63]
rt v[192:193],v240 offset:50688
rt v[194:195],v240 offset:51712
mm v[60:63],v[204:207],a[84:87],v[60:63]
st v[230:233],v238,s[32:35],s94 offen offset:2048 sc1
mm v[60:63],v[200:203],a[92:95],v[60:63]
rt v[196:197],v240 offset:59136
rt v[198:199],v240 offset:60160
mm v[28:31],v[200:203],a[88:91],v[28:31]
wt lgkmcnt(4)
s_mov_b32 s93, 8192
ld a[240:243],v229,s[28:31],s93 offen offset:0
mm v[28:31],v[204:207],a[80:83],v[28:31]
rt v[200:201],v240 offset:0
rt v[202:203],v240 offset:1024
mm v[28:31],v[212:215],a[0:3],v[28:31]
ld v[152:155],v224,s[68:71],0 offen offset:512
mm v[28:31],v[208:211],a[8:11],v[28:31]
rt v[204:205],v240 offset:8448
rt v[206:207],v240 offset:9472
mm v[60:63],v[208:211],a[12:15],v[60:63]
ld a[244:247],v229,s[28:31],s93 offen offset:2048
mm v[60:63],v[212:215],a[4:7],v[60:63]
rt v[208:209],v240 offset:16896
rt v[210:211],v240 offset:17920
mm v[60:63],v[220:223],a[20:23],v[60:63]
ld v[156:159],v225,s[48:51],0 offen offset:512
mm v[60:63],v[216:219],a[28:31],v[60:63]
rt v[212:213],v240 offset:25344
rt v[214:215],v240 offset:26368
mm v[28:31],v[216:219],a[24:27],v[28:31]
wt vmcnt(24) lgkmcnt(4)
s_mov_b32 s93, 12288
ld a[248:251],v229,s[28:31],s93 offen offset:0
mm v[28:31],v[220:223],a[16:19],v[28:31]
rt v[216:217],v240 offset:33792
rt v[218:219],v240 offset:34816
mm v[64:67],v[192:195],a[32:35],v[64:67]
cv v234,v56,v57
cv v236,v60,v61
mm v[64:67],v[196:199],a[48:51],v[64:67]
rt v[220:221],v240 offset:42240
rt v[222:223],v240 offset:43264
mm v[96:99],v[196:199],a[52:55],v[96:99]
cv v235,v58,v59
cv v237,v62,v63
mm v[96:99],v[192:195],a[44:47],v[96:99]
rt v[196:197],v241 offset:50688
rt v[198:199],v241 offset:51712
mm v[96:99],v[200:203],a[36:39],v[96:99]
v_permlane16_swap_b32 v234, v236
v_permlane16_swap_b32 v235, v237
mm v[96:99],v[204:207],a[60:63],v[96:99]
rt v[192:193],v241 offset:59136
rt v[194:195],v241 offset:60160
mm v[64:67],v[204:207],a[40:43],v[64:67]
wt vmcnt(17) lgkmcnt(4)
ld a[252:255],v229,s[28:31],s93 offen offset:2048
mm v[64:67],v[200:203],a[56:59],v[64:67]
rt v[204:205],v241 offset:0
rt v[206:207],v241 offset:1024
mm v[64:67],v[208:211],v[128:131],v[64:67]
st v[234:237],v238,s[32:35],s95 offen offset:3072 sc1
mm v[64:67],v[212:215],v[144:147],v[64:67]
rt v[200:201],v241 offset:8448
rt v[202:203],v241 offset:9472
mm v[96:99],v[212:215],v[148:151],v[96:99]
cv v230,v24,v25
cv v232,v28,v29
mm v[96:99],v[208:211],v[132:135],v[96:99]
rt v[212:213],v241 offset:16896
rt v[214:215],v241 offset:17920
mm v[96:99],v[216:219],v[164:167],v[96:99]
cv v231,v26,v27
cv v233,v30,v31
mm v[96:99],v[220:223],v[180:183],v[96:99]
rt v[208:209],v241 offset:25344
rt v[210:211],v241 offset:26368
mm v[64:67],v[220:223],v[176:179],v[64:67]
wt lgkmcnt(4)
s_mov_b32 s93, 0
ld a[64:67],v229,s[28:31],s93 offen offset:256
mm v[64:67],v[216:219],v[160:163],v[64:67]
rt v[220:221],v241 offset:33792
rt v[222:223],v241 offset:34816
mm v[68:71],v[196:199],a[32:35],v[68:71]
v_permlane16_swap_b32 v230, v232
v_permlane16_swap_b32 v231, v233
mm v[68:71],v[192:195],a[48:51],v[68:71]
rt v[216:217],v241 offset:42240
rt v[218:219],v241 offset:43264
mm v[100:103],v[192:195],a[52:55],v[100:103]
ld a[68:71],v229,s[28:31],s93 offen offset:2304
mm v[100:103],v[196:199],a[44:47],v[100:103]
rt v[192:193],v242 offset:50688
rt v[194:195],v242 offset:51712
mm v[100:103],v[204:207],a[36:39],v[100:103]
st v[230:233],v238,s[32:35],s94 offen offset:3072 sc1
mm v[100:103],v[200:203],a[60:63],v[100:103]
rt v[196:197],v242 offset:59136
rt v[198:199],v242 offset:60160
mm v[68:71],v[200:203],a[40:43],v[68:71]
wt lgkmcnt(4)
s_mov_b32 s93, 4096
ld a[72:75],v229,s[28:31],s93 offen offset:256
mm v[68:71],v[204:207],a[56:59],v[68:71]
rt v[200:201],v242 offset:0
rt v[202:203],v242 offset:1024
mm v[68:71],v[212:215],v[128:131],v[68:71]
ld v[168:171],v224,s[68:71],0 offen offset:1024
mm v[68:71],v[208:211],v[144:147],v[68:71]
rt v[204:205],v242 offset:8448
rt v[206:207],v242 offset:9472
mm v[100:103],v[208:211],v[148:151],v[100:103]
ld a[76:79],v229,s[28:31],s93 offen offset:2304
mm v[100:103],v[212:215],v[132:135],v[100:103]
rt v[208:209],v242 offset:16896
rt v[210:211],v242 offset:17920
mm v[100:103],v[220:223],v[164:167],v[100:103]
ld v[172:175],v225,s[48:51],0 offen offset:1024
mm v[100:103],v[216:219],v[180:183],v[100:103]
rt v[212:213],v242 offset:25344
rt v[214:215],v242 offset:26368
mm v[68:71],v[216:219],v[176:179],v[68:71]
wt lgkmcnt(4)
s_mov_b32 s93, 8192
ld a[80:83],v229,s[28:31],s93 offen offset:256
mm v[68:71],v[220:223],v[160:163],v[68:71]
rt v[216:217],v242 offset:33792
rt v[218:219],v242 offset:34816
mm v[72:75],v[192:195],a[32:35],v[72:75]
cv v234,v96,v97
cv v236,v100,v101
mm v[72:75],v[196:199],a[48:51],v[72:75]
rt v[220:221],v242 offset:42240
rt v[222:223],v242 offset:43264
mm v[104:107],v[196:199],a[52:55],v[104:107]
cv v235,v98,v99
cv v237,v102,v103
mm v[104:107],v[192:195],a[44:47],v[104:107]
rt v[196:197],v243 offset:50688
rt v[198:199],v243 offset:51712
mm v[104:107],v[200:203],a[36:39],v[104:107]
v_permlane16_swap_b32 v234, v236
v_permlane16_swap_b32 v235, v237
mm v[104:107],v[204:207],a[60:63],v[104:107]
rt v[192:193],v243 offset:59136
rt v[194:195],v243 offset:60160
mm v[72:75],v[204:207],a[40:43],v[72:75]
wt lgkmcnt(4)
ld a[84:87],v229,s[28:31],s93 offen offset:2304
mm v[72:75],v[200:203],a[56:59],v[72:75]
rt v[204:205],v243 offset:0
rt v[206:207],v243 offset:1024
mm v[72:75],v[208:211],v[128:131],v[72:75]
st v[234:237],v238,s[32:35],s97 offen offset:0 sc1
mm v[72:75],v[212:215],v[144:147],v[72:75]
rt v[200:201],v243 offset:8448
rt v[202:203],v243 offset:9472
mm v[104:107],v[212:215],v[148:151],v[104:107]
cv v230,v64,v65
cv v232,v68,v69
mm v[104:107],v[208:211],v[132:135],v[104:107]
rt v[212:213],v243 offset:16896
rt v[214:215],v243 offset:17920
mm v[104:107],v[216:219],v[164:167],v[104:107]
cv v231,v66,v67
cv v233,v70,v71
mm v[104:107],v[220:223],v[180:183],v[104:107]
rt v[208:209],v243 offset:25344
rt v[210:211],v243 offset:26368
mm v[72:75],v[220:223],v[176:179],v[72:75]
wt lgkmcnt(4)
s_mov_b32 s93, 12288
ld a[88:91],v229,s[28:31],s93 offen offset:256
mm v[72:75],v[216:219],v[160:163],v[72:75]
rt v[220:221],v243 offset:33792
rt v[222:223],v243 offset:34816
mm v[76:79],v[196:199],a[32:35],v[76:79]
v_permlane16_swap_b32 v230, v232
v_permlane16_swap_b32 v231, v233
mm v[76:79],v[192:195],a[48:51],v[76:79]
rt v[216:217],v243 offset:42240
rt v[218:219],v243 offset:43264
mm v[108:111],v[192:195],a[52:55],v[108:111]
ld a[92:95],v229,s[28:31],s93 offen offset:2304
mm v[108:111],v[196:199],a[44:47],v[108:111]
rt v[192:193],v240 offset:50816
rt v[194:195],v240 offset:51840
mm v[108:111],v[204:207],a[36:39],v[108:111]
st v[230:233],v238,s[32:35],s96 offen offset:0 sc1
mm v[108:111],v[200:203],a[60:63],v[108:111]
rt v[196:197],v240 offset:59264
rt v[198:199],v240 offset:60288
mm v[76:79],v[200:203],a[40:43],v[76:79]
wt lgkmcnt(4)
mm v[76:79],v[204:207],a[56:59],v[76:79]
rt v[200:201],v240 offset:128
rt v[202:203],v240 offset:1152
mm v[76:79],v[212:215],v[128:131],v[76:79]
ld v[184:187],v224,s[68:71],0 offen offset:1536
mm v[76:79],v[208:211],v[144:147],v[76:79]
rt v[204:205],v240 offset:8576
rt v[206:207],v240 offset:9600
mm v[108:111],v[208:211],v[148:151],v[108:111]
mm v[108:111],v[212:215],v[132:135],v[108:111]
rt v[208:209],v240 offset:17024
rt v[210:211],v240 offset:18048
mm v[108:111],v[220:223],v[164:167],v[108:111]
ld v[188:191],v225,s[48:51],0 offen offset:1536
mm v[108:111],v[216:219],v[180:183],v[108:111]
rt v[212:213],v240 offset:25472
rt v[214:215],v240 offset:26496
mm v[76:79],v[216:219],v[176:179],v[76:79]
wt lgkmcnt(4)
mm v[76:79],v[220:223],v[160:163],v[76:79]
rt v[216:217],v240 offset:33920
rt v[218:219],v240 offset:34944
mm v[80:83],v[192:195],a[32:35],v[80:83]
cv v234,v104,v105
cv v236,v108,v109
mm v[80:83],v[196:199],a[48:51],v[80:83]
rt v[220:221],v240 offset:42368
rt v[222:223],v240 offset:43392
mm v[112:115],v[196:199],a[52:55],v[112:115]
cv v235,v106,v107
cv v237,v110,v111
mm v[112:115],v[192:195],a[44:47],v[112:115]
rt v[196:197],v241 offset:50816
rt v[198:199],v241 offset:51840
mm v[112:115],v[200:203],a[36:39],v[112:115]
v_permlane16_swap_b32 v234, v236
v_permlane16_swap_b32 v235, v237
mm v[112:115],v[204:207],a[60:63],v[112:115]
rt v[192:193],v241 offset:59264
rt v[194:195],v241 offset:60288
mm v[80:83],v[204:207],a[40:43],v[80:83]
wt lgkmcnt(4)
mm v[80:83],v[200:203],a[56:59],v[80:83]
rt v[204:205],v241 offset:128
rt v[206:207],v241 offset:1152
mm v[80:83],v[208:211],v[128:131],v[80:83]
st v[234:237],v238,s[32:35],s97 offen offset:1024 sc1
mm v[80:83],v[212:215],v[144:147],v[80:83]
rt v[200:201],v241 offset:8576
rt v[202:203],v241 offset:9600
mm v[112:115],v[212:215],v[148:151],v[112:115]
cv v230,v72,v73
cv v232,v76,v77
mm v[112:115],v[208:211],v[132:135],v[112:115]
rt v[212:213],v241 offset:17024
rt v[214:215],v241 offset:18048
mm v[112:115],v[216:219],v[164:167],v[112:115]
cv v231,v74,v75
cv v233,v78,v79
mm v[112:115],v[220:223],v[180:183],v[112:115]
rt v[208:209],v241 offset:25472
rt v[210:211],v241 offset:26496
mm v[80:83],v[220:223],v[176:179],v[80:83]
wt lgkmcnt(4)
mm v[80:83],v[216:219],v[160:163],v[80:83]
rt v[220:221],v241 offset:33920
rt v[222:223],v241 offset:34944
mm v[84:87],v[196:199],a[32:35],v[84:87]
v_permlane16_swap_b32 v230, v232
v_permlane16_swap_b32 v231, v233
mm v[84:87],v[192:195],a[48:51],v[84:87]
rt v[216:217],v241 offset:42368
rt v[218:219],v241 offset:43392
mm v[116:119],v[192:195],a[52:55],v[116:119]
mm v[116:119],v[196:199],a[44:47],v[116:119]
rt v[192:193],v242 offset:50816
rt v[194:195],v242 offset:51840
mm v[116:119],v[204:207],a[36:39],v[116:119]
st v[230:233],v238,s[32:35],s96 offen offset:1024 sc1
mm v[116:119],v[200:203],a[60:63],v[116:119]
rt v[196:197],v242 offset:59264
rt v[198:199],v242 offset:60288
mm v[84:87],v[200:203],a[40:43],v[84:87]
wt lgkmcnt(4)
mm v[84:87],v[204:207],a[56:59],v[84:87]
rt v[200:201],v242 offset:128
rt v[202:203],v242 offset:1152
mm v[84:87],v[212:215],v[128:131],v[84:87]
mm v[84:87],v[208:211],v[144:147],v[84:87]
rt v[204:205],v242 offset:8576
rt v[206:207],v242 offset:9600
mm v[116:119],v[208:211],v[148:151],v[116:119]
mm v[116:119],v[212:215],v[132:135],v[116:119]
rt v[208:209],v242 offset:17024
rt v[210:211],v242 offset:18048
mm v[116:119],v[220:223],v[164:167],v[116:119]
mm v[116:119],v[216:219],v[180:183],v[116:119]
rt v[212:213],v242 offset:25472
rt v[214:215],v242 offset:26496
mm v[84:87],v[216:219],v[176:179],v[84:87]
wt lgkmcnt(4)
mm v[84:87],v[220:223],v[160:163],v[84:87]
rt v[216:217],v242 offset:33920
rt v[218:219],v242 offset:34944
mm v[88:91],v[192:195],a[32:35],v[88:91]
cv v234,v112,v113
cv v236,v116,v117
mm v[88:91],v[196:199],a[48:51],v[88:91]
rt v[220:221],v242 offset:42368
rt v[222:223],v242 offset:43392
mm v[120:123],v[196:199],a[52:55],v[120:123]
cv v235,v114,v115
cv v237,v118,v119
mm v[120:123],v[192:195],a[44:47],v[120:123]
rt v[196:197],v243 offset:50816
rt v[198:199],v243 offset:51840
mm v[120:123],v[200:203],a[36:39],v[120:123]
v_permlane16_swap_b32 v234, v236
v_permlane16_swap_b32 v235, v237
mm v[120:123],v[204:207],a[60:63],v[120:123]
rt v[192:193],v243 offset:59264
rt v[194:195],v243 offset:60288
mm v[88:91],v[204:207],a[40:43],v[88:91]
wt lgkmcnt(4)
mm v[88:91],v[200:203],a[56:59],v[88:91]
rt v[204:205],v243 offset:128
rt v[206:207],v243 offset:1152
mm v[88:91],v[208:211],v[128:131],v[88:91]
st v[234:237],v238,s[32:35],s97 offen offset:2048 sc1
mm v[88:91],v[212:215],v[144:147],v[88:91]
rt v[200:201],v243 offset:8576
rt v[202:203],v243 offset:9600
mm v[120:123],v[212:215],v[148:151],v[120:123]
cv v230,v80,v81
cv v232,v84,v85
mm v[120:123],v[208:211],v[132:135],v[120:123]
rt v[212:213],v243 offset:17024
rt v[214:215],v243 offset:18048
mm v[120:123],v[216:219],v[164:167],v[120:123]
cv v231,v82,v83
cv v233,v86,v87
wt vmcnt(15)
buffer_store_dwordx2 v[252:253], v251, s[64:67], s92 offen sc1
mm v[120:123],v[220:223],v[180:183],v[120:123]
rt v[208:209],v243 offset:25472
rt v[210:211],v243 offset:26496
mm v[88:91],v[220:223],v[176:179],v[88:91]
wt lgkmcnt(4)
mm v[88:91],v[216:219],v[160:163],v[88:91]
rt v[220:221],v243 offset:33920
rt v[222:223],v243 offset:34944
mm v[92:95],v[196:199],a[32:35],v[92:95]
v_permlane16_swap_b32 v230, v232
v_permlane16_swap_b32 v231, v233
mm v[92:95],v[192:195],a[48:51],v[92:95]
rt v[216:217],v243 offset:42368
rt v[218:219],v243 offset:43392
mm v[124:127],v[192:195],a[52:55],v[124:127]
mm v[124:127],v[196:199],a[44:47],v[124:127]
mm v[124:127],v[204:207],a[36:39],v[124:127]
st v[230:233],v238,s[32:35],s96 offen offset:2048 sc1
mm v[124:127],v[200:203],a[60:63],v[124:127]
mm v[92:95],v[200:203],a[40:43],v[92:95]
wt lgkmcnt(0)
mm v[92:95],v[204:207],a[56:59],v[92:95]
mm v[92:95],v[212:215],v[128:131],v[92:95]
mm v[92:95],v[208:211],v[144:147],v[92:95]
mm v[124:127],v[208:211],v[148:151],v[124:127]
mm v[124:127],v[212:215],v[132:135],v[124:127]
mm v[124:127],v[220:223],v[164:167],v[124:127]
mm v[124:127],v[216:219],v[180:183],v[124:127]
mm v[92:95],v[216:219],v[176:179],v[92:95]
mm v[92:95],v[220:223],v[160:163],v[92:95]
s_nop 7
cv v234,v120,v121
cv v236,v124,v125
cv v235,v122,v123
cv v237,v126,v127
cv v230,v88,v89
cv v232,v92,v93
cv v231,v90,v91
cv v233,v94,v95
v_permlane16_swap_b32 v234, v236
v_permlane16_swap_b32 v235, v237
st v[234:237],v238,s[32:35],s97 offen offset:3072 sc1
v_permlane16_swap_b32 v230, v232
v_permlane16_swap_b32 v231, v233
st v[230:233],v238,s[32:35],s96 offen offset:3072 sc1
s_branch .La5_LT_0
; ---- O projection, odd XCDs: seven and a half heads
.La5_LO_SC:
s_nop 0
.La5_LW7_0:
s_cbranch_execz .La5_LW7_4
mm v[64:67],v[192:195],v[160:163],0
buffer_load_dwordx2 v[250:251], v248, s[64:67], s5 offen sc1
mm v[64:67],v[196:199],v[164:167],v[64:67]
rt v[220:221],v243 offset:25344
rt v[222:223],v243 offset:26368
mm v[96:99],v[196:199],v[180:183],0
s_add_u32 m0, s83, 0
s_bfe_u32 s80, s4, 262148
mm v[96:99],v[192:195],v[176:179],v[96:99]
rt v[196:197],v240 offset:17024
rt v[198:199],v240 offset:18048
s_xor_b32 s80, s80, s72
s_lshl_b32 s80, s80, 18
s_add_u32 s80, s80, 131072
mm v[100:103],v[200:203],v[176:179],0
ld v224,s[56:59],s80 offen lds
mm v[100:103],v[204:207],v[180:183],v[100:103]
rt v[192:193],v240 offset:25472
rt v[194:195],v240 offset:26496
mm v[68:71],v[204:207],v[164:167],0
s_add_u32 m0, s83, 1024
wt lgkmcnt(4)
mm v[68:71],v[200:203],v[160:163],v[68:71]
rt v[204:205],v241 offset:17024
rt v[206:207],v241 offset:18048
mm v[72:75],v[208:211],v[160:163],0
ld v225,s[56:59],s80 offen lds
mm v[72:75],v[212:215],v[164:167],v[72:75]
rt v[200:201],v241 offset:25472
rt v[202:203],v241 offset:26496
mm v[104:107],v[212:215],v[180:183],0
s_add_u32 m0, s83, 2176
s_bfe_u32 s53, s4, 262148
mm v[104:107],v[208:211],v[176:179],v[104:107]
rt v[212:213],v242 offset:17024
rt v[214:215],v242 offset:18048
mm v[108:111],v[216:219],v[176:179],0
ld v226,s[56:59],s80 offen lds
mm v[108:111],v[220:223],v[180:183],v[108:111]
rt v[208:209],v242 offset:25472
rt v[210:211],v242 offset:26496
mm v[76:79],v[220:223],v[164:167],0
s_add_u32 m0, s83, 3200
wt lgkmcnt(4)
mm v[76:79],v[216:219],v[160:163],v[76:79]
rt v[220:221],v243 offset:17024
rt v[222:223],v243 offset:18048
mm v[80:83],v[196:199],v[160:163],0
ld v227,s[56:59],s80 offen lds
mm v[80:83],v[192:195],v[164:167],v[80:83]
rt v[216:217],v243 offset:25472
rt v[218:219],v243 offset:26496
mm v[112:115],v[192:195],v[180:183],0
s_add_u32 m0, s83, 118272
s_bfe_u32 s80, s4, 262152
mm v[112:115],v[196:199],v[176:179],v[112:115]
rt v[192:193],v240 offset:33792
rt v[194:195],v240 offset:34816
s_xor_b32 s80, s80, s72
s_lshl_b32 s80, s80, 18
mm v[116:119],v[204:207],v[176:179],0
ld v224,s[56:59],s80 offen lds
mm v[116:119],v[200:203],v[180:183],v[116:119]
rt v[196:197],v240 offset:42240
rt v[198:199],v240 offset:43264
mm v[84:87],v[200:203],v[164:167],0
s_add_u32 m0, s83, 119296
wt lgkmcnt(4)
mm v[84:87],v[204:207],v[160:163],v[84:87]
rt v[200:201],v241 offset:33792
rt v[202:203],v241 offset:34816
mm v[88:91],v[212:215],v[160:163],0
ld v225,s[56:59],s80 offen lds
mm v[88:91],v[208:211],v[164:167],v[88:91]
rt v[204:205],v241 offset:42240
rt v[206:207],v241 offset:43264
mm v[120:123],v[208:211],v[180:183],0
s_add_u32 m0, s83, 120448
s_xor_b32 s53, s53, s72
mm v[120:123],v[212:215],v[176:179],v[120:123]
rt v[208:209],v242 offset:33792
rt v[210:211],v242 offset:34816
mm v[124:127],v[220:223],v[176:179],0
ld v226,s[56:59],s80 offen lds
mm v[124:127],v[216:219],v[180:183],v[124:127]
rt v[212:213],v242 offset:42240
rt v[214:215],v242 offset:43264
mm v[92:95],v[216:219],v[164:167],0
wt vmcnt(7)
v_cmp_eq_u64 vcc, v[250:251], v[252:253]
s_nop 0
s_bitcmp1_b32 vcc_lo, 1
s_cbranch_scc0 .La5_LO_NC1
.La5_LO_KC1:
wt lgkmcnt(4)
mm v[92:95],v[220:223],v[160:163],v[92:95]
rt v[216:217],v243 offset:33792
rt v[218:219],v243 offset:34816
s_lshl_b32 s53, s53, 13
mm v[64:67],v[192:195],v[168:171],v[64:67]
ld a[0:3],v244,s[44:47],s53 offen offset:0 sc1
mm v[64:67],v[196:199],v[172:175],v[64:67]
rt v[220:221],v243 offset:42240
rt v[222:223],v243 offset:43264
mm v[96:99],v[196:199],v[188:191],v[96:99]
ld a[4:7],v245,s[44:47],s53 offen offset:0 sc1
mm v[96:99],v[192:195],v[184:187],v[96:99]
rt v[196:197],v240 offset:33920
rt v[198:199],v240 offset:34944
mm v[100:103],v[200:203],v[184:187],v[100:103]
ld a[8:11],v246,s[44:47],s53 offen offset:0 sc1
mm v[100:103],v[204:207],v[188:191],v[100:103]
rt v[192:193],v240 offset:42368
rt v[194:195],v240 offset:43392
mm v[68:71],v[204:207],v[172:175],v[68:71]
wt lgkmcnt(4)
s_bfe_u32 s54, s4, 262152
mm v[68:71],v[200:203],v[168:171],v[68:71]
rt v[204:205],v241 offset:33920
rt v[206:207],v241 offset:34944
mm v[72:75],v[208:211],v[168:171],v[72:75]
ld a[12:15],v247,s[44:47],s53 offen offset:0 sc1
mm v[72:75],v[212:215],v[172:175],v[72:75]
rt v[200:201],v241 offset:42368
rt v[202:203],v241 offset:43392
mm v[104:107],v[212:215],v[188:191],v[104:107]
ld a[16:19],v244,s[44:47],s53 offen offset:1024 sc1
mm v[104:107],v[208:211],v[184:187],v[104:107]
rt v[212:213],v242 offset:33920
rt v[214:215],v242 offset:34944
mm v[108:111],v[216:219],v[184:187],v[108:111]
ld a[20:23],v245,s[44:47],s53 offen offset:1024 sc1
mm v[108:111],v[220:223],v[188:191],v[108:111]
.La5_LW7_1:
s_cbranch_execz .La5_LW7_5
rt v[208:209],v242 offset:42368
rt v[210:211],v242 offset:43392
mm v[76:79],v[220:223],v[172:175],v[76:79]
s_nop 0
s_barrier
wt lgkmcnt(4)
mm v[76:79],v[216:219],v[168:171],v[76:79]
rt v[220:221],v243 offset:33920
rt v[222:223],v243 offset:34944
mm v[80:83],v[196:199],v[168:171],v[80:83]
ld a[24:27],v246,s[44:47],s53 offen offset:1024 sc1
mm v[80:83],v[192:195],v[172:175],v[80:83]
rt v[216:217],v243 offset:42368
rt v[218:219],v243 offset:43392
mm v[112:115],v[192:195],v[188:191],v[112:115]
ld a[28:31],v247,s[44:47],s53 offen offset:1024 sc1
mm v[112:115],v[196:199],v[184:187],v[112:115]
rt v[192:193],v240 offset:50688
rt v[194:195],v240 offset:51712
mm v[116:119],v[204:207],v[184:187],v[116:119]
ld a[32:35],v244,s[44:47],s53 offen offset:2048 sc1
mm v[116:119],v[200:203],v[188:191],v[116:119]
rt v[196:197],v240 offset:59136
rt v[198:199],v240 offset:60160
mm v[84:87],v[200:203],v[172:175],v[84:87]
wt lgkmcnt(4)
s_xor_b32 s54, s54, s72
mm v[84:87],v[204:207],v[168:171],v[84:87]
rt v[200:201],v241 offset:50688
rt v[202:203],v241 offset:51712
mm v[88:91],v[212:215],v[168:171],v[88:91]
ld a[36:39],v245,s[44:47],s53 offen offset:2048 sc1
mm v[88:91],v[208:211],v[172:175],v[88:91]
rt v[204:205],v241 offset:59136
rt v[206:207],v241 offset:60160
mm v[120:123],v[208:211],v[188:191],v[120:123]
ld a[40:43],v246,s[44:47],s53 offen offset:2048 sc1
mm v[120:123],v[212:215],v[184:187],v[120:123]
rt v[208:209],v242 offset:50688
rt v[210:211],v242 offset:51712
mm v[124:127],v[220:223],v[184:187],v[124:127]
ld a[44:47],v247,s[44:47],s53 offen offset:2048 sc1
mm v[124:127],v[216:219],v[188:191],v[124:127]
rt v[212:213],v242 offset:59136
rt v[214:215],v242 offset:60160
mm v[92:95],v[216:219],v[172:175],v[92:95]
wt vmcnt(4) lgkmcnt(4)
s_lshl_b32 s54, s54, 13
mm v[92:95],v[220:223],v[168:171],v[92:95]
rt v[216:217],v243 offset:50688
rt v[218:219],v243 offset:51712
mm v[0:3],v[192:195],a[0:3],v[0:3]
s_bfe_u32 s55, s4, 262156
s_xor_b32 s55, s55, s72
mm v[0:3],v[196:199],a[16:19],v[0:3]
ld a[48:51],v244,s[44:47],s53 offen offset:3072 sc1
mm v[32:35],v[196:199],a[20:23],v[32:35]
s_lshl_b32 s55, s55, 13
s_bfe_u32 s52, s4, 262160
mm v[32:35],v[192:195],a[4:7],v[32:35]
rt v[220:221],v243 offset:59136
rt v[222:223],v243 offset:60160
mm v[64:67],v[192:195],a[8:11],v[64:67]
s_xor_b32 s52, s52, s72
s_lshl_b32 s52, s52, 13
mm v[64:67],v[196:199],a[24:27],v[64:67]
ld a[52:55],v245,s[44:47],s53 offen offset:3072 sc1
mm v[96:99],v[196:199],a[28:31],v[96:99]
mm v[96:99],v[192:195],a[12:15],v[96:99]
rt v[196:197],v240 offset:50816
rt v[198:199],v240 offset:51840
mm v[100:103],v[200:203],a[12:15],v[100:103]
mm v[100:103],v[204:207],a[28:31],v[100:103]
buffer_load_dwordx2 v[250:251], v248, s[64:67], s5 offen sc1
mm v[68:71],v[204:207],a[24:27],v[68:71]
mm v[68:71],v[200:203],a[8:11],v[68:71]
rt v[192:193],v240 offset:59264
rt v[194:195],v240 offset:60288
mm v[36:39],v[200:203],a[4:7],v[36:39]
mm v[36:39],v[204:207],a[20:23],v[36:39]
ld a[56:59],v246,s[44:47],s53 offen offset:3072 sc1
mm v[4:7],v[204:207],a[16:19],v[4:7]
wt lgkmcnt(4)
mm v[4:7],v[200:203],a[0:3],v[4:7]
rt v[204:205],v241 offset:50816
rt v[206:207],v241 offset:51840
mm v[8:11],v[208:211],a[0:3],v[8:11]
mm v[8:11],v[212:215],a[16:19],v[8:11]
ld a[60:63],v247,s[44:47],s53 offen offset:3072 sc1
mm v[40:43],v[212:215],a[20:23],v[40:43]
s_bfe_u32 s53, s4, 262164
s_xor_b32 s53, s53, s72
mm v[40:43],v[208:211],a[4:7],v[40:43]
rt v[200:201],v241 offset:59264
rt v[202:203],v241 offset:60288
mm v[72:75],v[208:211],a[8:11],v[72:75]
s_add_u32 m0, s83, 121472
s_lshl_b32 s53, s53, 13
mm v[72:75],v[212:215],a[24:27],v[72:75]
ld v227,s[56:59],s80 offen lds
mm v[104:107],v[212:215],a[28:31],v[104:107]
s_add_u32 s80, s80, 131072
mm v[104:107],v[208:211],a[12:15],v[104:107]
rt v[212:213],v242 offset:50816
rt v[214:215],v242 offset:51840
mm v[108:111],v[216:219],a[12:15],v[108:111]
s_add_u32 m0, s83, 67584
mm v[108:111],v[220:223],a[28:31],v[108:111]
ld v224,s[56:59],s80 offen lds
mm v[76:79],v[220:223],a[24:27],v[76:79]
mm v[76:79],v[216:219],a[8:11],v[76:79]
rt v[208:209],v242 offset:59264
rt v[210:211],v242 offset:60288
mm v[44:47],v[216:219],a[4:7],v[44:47]
s_add_u32 m0, s83, 68608
mm v[44:47],v[220:223],a[20:23],v[44:47]
ld v225,s[56:59],s80 offen lds
mm v[12:15],v[220:223],a[16:19],v[12:15]
wt lgkmcnt(4)
mm v[12:15],v[216:219],a[0:3],v[12:15]
rt v[220:221],v243 offset:50816
rt v[222:223],v243 offset:51840
mm v[16:19],v[196:199],a[0:3],v[16:19]
s_add_u32 m0, s83, 69760
mm v[16:19],v[192:195],a[16:19],v[16:19]
ld v226,s[56:59],s80 offen lds
mm v[48:51],v[192:195],a[20:23],v[48:51]
mm v[48:51],v[196:199],a[4:7],v[48:51]
rt v[216:217],v243 offset:59264
rt v[218:219],v243 offset:60288
mm v[80:83],v[196:199],a[8:11],v[80:83]
s_add_u32 m0, s83, 70784
s_nop 0
s_barrier
mm v[80:83],v[192:195],a[24:27],v[80:83]
ld v227,s[56:59],s80 offen lds
mm v[112:115],v[192:195],a[28:31],v[112:115]
s_bfe_u32 s80, s4, 262156
s_xor_b32 s80, s80, s72
mm v[112:115],v[196:199],a[12:15],v[112:115]
rt v[192:193],v240 offset:0
rt v[194:195],v240 offset:1024
mm v[116:119],v[204:207],a[12:15],v[116:119]
s_add_u32 m0, s83, 84480
s_lshl_b32 s80, s80, 18
mm v[116:119],v[200:203],a[28:31],v[116:119]
ld v224,s[56:59],s80 offen lds
mm v[84:87],v[200:203],a[24:27],v[84:87]
mm v[84:87],v[204:207],a[8:11],v[84:87]
rt v[196:197],v240 offset:8448
rt v[198:199],v240 offset:9472
mm v[52:55],v[204:207],a[4:7],v[52:55]
s_add_u32 m0, s83, 85504
mm v[52:55],v[200:203],a[20:23],v[52:55]
ld v225,s[56:59],s80 offen lds
mm v[20:23],v[200:203],a[16:19],v[20:23]
wt lgkmcnt(4)
mm v[20:23],v[204:207],a[0:3],v[20:23]
rt v[200:201],v241 offset:0
rt v[202:203],v241 offset:1024
mm v[24:27],v[212:215],a[0:3],v[24:27]
s_add_u32 m0, s83, 86656
mm v[24:27],v[208:211],a[16:19],v[24:27]
ld v226,s[56:59],s80 offen lds
mm v[56:59],v[208:211],a[20:23],v[56:59]
wt vmcnt(10)
v_cmp_eq_u64 vcc, v[250:251], v[252:253]
mm v[56:59],v[212:215],a[4:7],v[56:59]
rt v[204:205],v241 offset:8448
rt v[206:207],v241 offset:9472
mm v[88:91],v[212:215],a[8:11],v[88:91]
s_add_u32 m0, s83, 87680
mm v[88:91],v[208:211],a[24:27],v[88:91]
ld v227,s[56:59],s80 offen lds
mm v[120:123],v[208:211],a[28:31],v[120:123]
s_bitcmp1_b32 vcc_lo, 2
s_cbranch_scc0 .La5_LO_NC2
.La5_LO_KC2:
mm v[120:123],v[212:215],a[12:15],v[120:123]
rt v[208:209],v242 offset:0
rt v[210:211],v242 offset:1024
mm v[124:127],v[220:223],a[12:15],v[124:127]
s_add_u32 s80, s80, 131072
mm v[124:127],v[216:219],a[28:31],v[124:127]
ld a[12:15],v244,s[44:47],s54 offen offset:0 sc1
mm v[92:95],v[216:219],a[24:27],v[92:95]
mm v[92:95],v[220:223],a[8:11],v[92:95]
rt v[212:213],v242 offset:8448
rt v[214:215],v242 offset:9472
mm v[60:63],v[220:223],a[4:7],v[60:63]
mm v[60:63],v[216:219],a[20:23],v[60:63]
mm v[28:31],v[216:219],a[16:19],v[28:31]
wt vmcnt(10) lgkmcnt(4)
mm v[28:31],v[220:223],a[0:3],v[28:31]
rt v[216:217],v243 offset:0
rt v[218:219],v243 offset:1024
mm v[0:3],v[192:195],a[32:35],v[0:3]
mm v[0:3],v[196:199],a[48:51],v[0:3]
ld a[0:3],v245,s[44:47],s54 offen offset:0 sc1
mm v[32:35],v[196:199],a[52:55],v[32:35]
mm v[32:35],v[192:195],a[36:39],v[32:35]
rt v[220:221],v243 offset:8448
rt v[222:223],v243 offset:9472
mm v[64:67],v[192:195],a[40:43],v[64:67]
mm v[64:67],v[196:199],a[56:59],v[64:67]
ld a[4:7],v246,s[44:47],s54 offen offset:0 sc1
mm v[96:99],v[196:199],a[60:63],v[96:99]
mm v[96:99],v[192:195],a[44:47],v[96:99]
rt v[196:197],v240 offset:128
rt v[198:199],v240 offset:1152
mm v[100:103],v[200:203],a[44:47],v[100:103]
s_mov_b32 s86, 0x05040100
s_mov_b32 s87, 0x07060302
mm v[100:103],v[204:207],a[60:63],v[100:103]
ld a[8:11],v247,s[44:47],s54 offen offset:0 sc1
mm v[68:71],v[204:207],a[56:59],v[68:71]
s_nop 0
ar v230,a128
mm v[68:71],v[200:203],a[40:43],v[68:71]
rt v[192:193],v240 offset:8576
rt v[194:195],v240 offset:9600
mm v[36:39],v[200:203],a[36:39],v[36:39]
v_add_u32 v240, 67584, v240
ar v231,a132
mm v[36:39],v[204:207],a[52:55],v[36:39]
ld a[16:19],v244,s[44:47],s54 offen offset:1024 sc1
mm v[4:7],v[204:207],a[48:51],v[4:7]
wt lgkmcnt(4)
v_perm_b32 v232, v231, v230, s86
mm v[4:7],v[200:203],a[32:35],v[4:7]
rt v[204:205],v241 offset:128
rt v[206:207],v241 offset:1152
mm v[8:11],v[208:211],a[32:35],v[8:11]
v_perm_b32 v233, v231, v230, s87
aw a96,v232
mm v[8:11],v[212:215],a[48:51],v[8:11]
ld a[20:23],v245,s[44:47],s54 offen offset:1024 sc1
mm v[40:43],v[212:215],a[52:55],v[40:43]
aw a100,v233
ar v234,a136
mm v[40:43],v[208:211],a[36:39],v[40:43]
rt v[200:201],v241 offset:8576
rt v[202:203],v241 offset:9600
mm v[72:75],v[208:211],a[40:43],v[72:75]
v_add_u32 v241, 67584, v241
ar v235,a140
mm v[72:75],v[212:215],a[56:59],v[72:75]
ld a[24:27],v246,s[44:47],s54 offen offset:1024 sc1
mm v[104:107],v[212:215],a[60:63],v[104:107]
v_perm_b32 v236, v235, v234, s86
v_perm_b32 v237, v235, v234, s87
mm v[104:107],v[208:211],a[44:47],v[104:107]
rt v[212:213],v242 offset:128
rt v[214:215],v242 offset:1152
mm v[108:111],v[216:219],a[44:47],v[108:111]
aw a97,v236
aw a101,v237
mm v[108:111],v[220:223],a[60:63],v[108:111]
ld a[28:31],v247,s[44:47],s54 offen offset:1024 sc1
mm v[76:79],v[220:223],a[56:59],v[76:79]
ar v230,a144
ar v231,a148
mm v[76:79],v[216:219],a[40:43],v[76:79]
rt v[208:209],v242 offset:8576
rt v[210:211],v242 offset:9600
mm v[44:47],v[216:219],a[36:39],v[44:47]
v_add_u32 v242, 67584, v242
v_perm_b32 v232, v231, v230, s86
mm v[44:47],v[220:223],a[52:55],v[44:47]
v_perm_b32 v233, v231, v230, s87
aw a98,v232
mm v[12:15],v[220:223],a[48:51],v[12:15]
wt lgkmcnt(4)
aw a102,v233
mm v[12:15],v[216:219],a[32:35],v[12:15]
rt v[220:221],v243 offset:128
rt v[222:223],v243 offset:1152
mm v[16:19],v[196:199],a[32:35],v[16:19]
s_add_u32 m0, s83, 101376
ar v234,a152
mm v[16:19],v[192:195],a[48:51],v[16:19]
ld v224,s[56:59],s80 offen lds
mm v[48:51],v[192:195],a[52:55],v[48:51]
ar v235,a156
v_perm_b32 v236, v235, v234, s86
mm v[48:51],v[196:199],a[36:39],v[48:51]
rt v[216:217],v243 offset:8576
rt v[218:219],v243 offset:9600
mm v[80:83],v[196:199],a[40:43],v[80:83]
s_add_u32 m0, s83, 102400
v_add_u32 v243, 67584, v243
mm v[80:83],v[192:195],a[56:59],v[80:83]
ld v225,s[56:59],s80 offen lds
mm v[112:115],v[192:195],a[60:63],v[112:115]
wt vmcnt(14)
s_barrier
mm v[112:115],v[196:199],a[44:47],v[112:115]
rt v[192:193],v240 offset:50688
rt v[194:195],v240 offset:51712
mm v[116:119],v[204:207],a[44:47],v[116:119]
s_add_u32 m0, s83, 103552
v_perm_b32 v237, v235, v234, s87
mm v[116:119],v[200:203],a[60:63],v[116:119]
ld v226,s[56:59],s80 offen lds
mm v[84:87],v[200:203],a[56:59],v[84:87]
aw a99,v236
aw a103,v237
mm v[84:87],v[204:207],a[40:43],v[84:87]
rt v[196:197],v240 offset:59136
rt v[198:199],v240 offset:60160
mm v[52:55],v[204:207],a[36:39],v[52:55]
ar v230,a129
ar v231,a133
mm v[52:55],v[200:203],a[52:55],v[52:55]
ld a[64:67],v244,s[44:47],s54 offen offset:2048 sc1
mm v[20:23],v[200:203],a[48:51],v[20:23]
wt lgkmcnt(4)
v_perm_b32 v232, v231, v230, s86
mm v[20:23],v[204:207],a[32:35],v[20:23]
rt v[200:201],v241 offset:50688
rt v[202:203],v241 offset:51712
mm v[24:27],v[212:215],a[32:35],v[24:27]
v_perm_b32 v233, v231, v230, s87
aw a104,v232
mm v[24:27],v[208:211],a[48:51],v[24:27]
ld a[68:71],v245,s[44:47],s54 offen offset:2048 sc1
mm v[56:59],v[208:211],a[52:55],v[56:59]
aw a108,v233
ar v234,a137
mm v[56:59],v[212:215],a[36:39],v[56:59]
rt v[204:205],v241 offset:59136
rt v[206:207],v241 offset:60160
mm v[88:91],v[212:215],a[40:43],v[88:91]
ar v235,a141
v_perm_b32 v236, v235, v234, s86
mm v[88:91],v[208:211],a[56:59],v[88:91]
ld a[72:75],v246,s[44:47],s54 offen offset:2048 sc1
mm v[120:123],v[208:211],a[60:63],v[120:123]
v_perm_b32 v237, v235, v234, s87
aw a105,v236
mm v[120:123],v[212:215],a[44:47],v[120:123]
rt v[208:209],v242 offset:50688
rt v[210:211],v242 offset:51712
mm v[124:127],v[220:223],a[44:47],v[124:127]
aw a109,v237
ar v230,a145
mm v[124:127],v[216:219],a[60:63],v[124:127]
buffer_load_dwordx2 v[250:251], v248, s[64:67], s5 offen sc1
mm v[92:95],v[216:219],a[56:59],v[92:95]
ar v231,a149
v_perm_b32 v232, v231, v230, s86
mm v[92:95],v[220:223],a[40:43],v[92:95]
rt v[212:213],v242 offset:59136
rt v[214:215],v242 offset:60160
mm v[60:63],v[220:223],a[36:39],v[60:63]
v_perm_b32 v233, v231, v230, s87
aw a106,v232
mm v[60:63],v[216:219],a[52:55],v[60:63]
ld a[36:39],v247,s[44:47],s54 offen offset:2048 sc1
mm v[28:31],v[216:219],a[48:51],v[28:31]
wt vmcnt(8) lgkmcnt(4)
aw a110,v233
mm v[28:31],v[220:223],a[32:35],v[28:31]
rt v[216:217],v243 offset:50688
rt v[218:219],v243 offset:51712
mm v[0:3],v[192:195],a[12:15],v[0:3]
ar v234,a153
ar v235,a157
mm v[0:3],v[196:199],a[16:19],v[0:3]
ld a[32:35],v244,s[44:47],s54 offen offset:3072 sc1
mm v[32:35],v[196:199],a[20:23],v[32:35]
v_perm_b32 v236, v235, v234, s86
v_perm_b32 v237, v235, v234, s87
mm v[32:35],v[192:195],a[0:3],v[32:35]
rt v[220:221],v243 offset:59136
rt v[222:223],v243 offset:60160
mm v[64:67],v[192:195],a[4:7],v[64:67]
aw a107,v236
aw a111,v237
mm v[64:67],v[196:199],a[24:27],v[64:67]
ld a[40:43],v245,s[44:47],s54 offen offset:3072 sc1
mm v[96:99],v[196:199],a[28:31],v[96:99]
ar v230,a130
ar v231,a134
mm v[96:99],v[192:195],a[8:11],v[96:99]
rt v[196:197],v240 offset:50816
rt v[198:199],v240 offset:51840
mm v[100:103],v[200:203],a[8:11],v[100:103]
v_perm_b32 v232, v231, v230, s86
v_perm_b32 v233, v231, v230, s87
mm v[100:103],v[204:207],a[28:31],v[100:103]
ld a[44:47],v246,s[44:47],s54 offen offset:3072 sc1
mm v[68:71],v[204:207],a[24:27],v[68:71]
aw a112,v232
aw a116,v233
mm v[68:71],v[200:203],a[4:7],v[68:71]
rt v[192:193],v240 offset:59264
rt v[194:195],v240 offset:60288
mm v[36:39],v[200:203],a[0:3],v[36:39]
ar v234,a138
ar v235,a142
mm v[36:39],v[204:207],a[20:23],v[36:39]
ld a[48:51],v247,s[44:47],s54 offen offset:3072 sc1
mm v[4:7],v[204:207],a[16:19],v[4:7]
wt lgkmcnt(4)
s_bfe_u32 s54, s4, 262168
mm v[4:7],v[200:203],a[12:15],v[4:7]
rt v[204:205],v241 offset:50816
rt v[206:207],v241 offset:51840
mm v[8:11],v[208:211],a[12:15],v[8:11]
s_add_u32 m0, s83, 104576
s_xor_b32 s54, s54, s72
mm v[8:11],v[212:215],a[16:19],v[8:11]
ld v227,s[56:59],s80 offen lds
mm v[40:43],v[212:215],a[20:23],v[40:43]
s_bitcmp1_b32 vcc_lo, 3
s_cbranch_scc0 .La5_LO_NC3
.La5_LO_KC3:
mm v[40:43],v[208:211],a[0:3],v[40:43]
rt v[200:201],v241 offset:59264
rt v[202:203],v241 offset:60288
mm v[72:75],v[208:211],a[4:7],v[72:75]
s_bfe_u32 s80, s4, 262160
s_xor_b32 s80, s80, s72
mm v[72:75],v[212:215],a[24:27],v[72:75]
ld a[52:55],v244,s[44:47],s55 offen offset:0 sc1
mm v[104:107],v[212:215],a[28:31],v[104:107]
s_lshl_b32 s80, s80, 18
s_lshl_b32 s54, s54, 13
mm v[104:107],v[208:211],a[8:11],v[104:107]
rt v[212:213],v242 offset:50816
rt v[214:215],v242 offset:51840
mm v[108:111],v[216:219],a[8:11],v[108:111]
v_perm_b32 v236, v235, v234, s86
v_perm_b32 v237, v235, v234, s87
mm v[108:111],v[220:223],a[28:31],v[108:111]
ld a[56:59],v245,s[44:47],s55 offen offset:0 sc1
mm v[76:79],v[220:223],a[24:27],v[76:79]
aw a113,v236
aw a117,v237
mm v[76:79],v[216:219],a[4:7],v[76:79]
rt v[208:209],v242 offset:59264
rt v[210:211],v242 offset:60288
mm v[44:47],v[216:219],a[0:3],v[44:47]
ar v230,a146
ar v231,a150
mm v[44:47],v[220:223],a[20:23],v[44:47]
v_perm_b32 v232, v231, v230, s86
v_perm_b32 v233, v231, v230, s87
mm v[12:15],v[220:223],a[16:19],v[12:15]
wt lgkmcnt(4)
aw a114,v232
mm v[12:15],v[216:219],a[12:15],v[12:15]
rt v[220:221],v243 offset:50816
rt v[222:223],v243 offset:51840
mm v[16:19],v[196:199],a[12:15],v[16:19]
aw a118,v233
ar v234,a154
mm v[16:19],v[192:195],a[16:19],v[16:19]
ld a[60:63],v246,s[44:47],s55 offen offset:0 sc1
mm v[48:51],v[192:195],a[20:23],v[48:51]
ar v235,a158
v_perm_b32 v236, v235, v234, s86
mm v[48:51],v[196:199],a[0:3],v[48:51]
rt v[216:217],v243 offset:59264
rt v[218:219],v243 offset:60288
mm v[80:83],v[196:199],a[4:7],v[80:83]
v_perm_b32 v237, v235, v234, s87
aw a115,v236
mm v[80:83],v[192:195],a[24:27],v[80:83]
ld a[76:79],v247,s[44:47],s55 offen offset:0 sc1
mm v[112:115],v[192:195],a[28:31],v[112:115]
aw a119,v237
ar v230,a131
mm v[112:115],v[196:199],a[8:11],v[112:115]
rt v[192:193],v240 offset:0
rt v[194:195],v240 offset:1024
mm v[116:119],v[204:207],a[8:11],v[116:119]
ar v231,a135
v_perm_b32 v232, v231, v230, s86
mm v[116:119],v[200:203],a[28:31],v[116:119]
ld a[80:83],v244,s[44:47],s55 offen offset:1024 sc1
mm v[84:87],v[200:203],a[24:27],v[84:87]
v_perm_b32 v233, v231, v230, s87
aw a120,v232
mm v[84:87],v[204:207],a[4:7],v[84:87]
rt v[196:197],v240 offset:8448
rt v[198:199],v240 offset:9472
mm v[52:55],v[204:207],a[0:3],v[52:55]
s_load_dwordx2 s[16:17], s[0:1], 0
s_load_dwordx4 s[36:39], s[0:1], 80
mm v[52:55],v[200:203],a[20:23],v[52:55]
ld a[84:87],v245,s[44:47],s55 offen offset:1024 sc1
mm v[20:23],v[200:203],a[16:19],v[20:23]
wt lgkmcnt(4)
aw a124,v233
mm v[20:23],v[204:207],a[12:15],v[20:23]
rt v[200:201],v241 offset:0
rt v[202:203],v241 offset:1024
mm v[24:27],v[212:215],a[12:15],v[24:27]
ar v234,a139
ar v235,a143
mm v[24:27],v[208:211],a[16:19],v[24:27]
ld a[88:91],v246,s[44:47],s55 offen offset:1024 sc1
mm v[56:59],v[208:211],a[20:23],v[56:59]
v_perm_b32 v236, v235, v234, s86
v_perm_b32 v237, v235, v234, s87
mm v[56:59],v[212:215],a[0:3],v[56:59]
rt v[204:205],v241 offset:8448
rt v[206:207],v241 offset:9472
mm v[88:91],v[212:215],a[4:7],v[88:91]
aw a121,v236
aw a125,v237
mm v[88:91],v[208:211],a[24:27],v[88:91]
ld a[92:95],v247,s[44:47],s55 offen offset:1024 sc1
mm v[120:123],v[208:211],a[28:31],v[120:123]
ar v230,a147
ar v231,a151
mm v[120:123],v[212:215],a[8:11],v[120:123]
rt v[208:209],v242 offset:0
rt v[210:211],v242 offset:1024
mm v[124:127],v[220:223],a[8:11],v[124:127]
s_add_u32 m0, s83, 16896
v_perm_b32 v232, v231, v230, s86
mm v[124:127],v[216:219],a[28:31],v[124:127]
ld v224,s[56:59],s80 offen lds
mm v[92:95],v[216:219],a[24:27],v[92:95]
v_perm_b32 v233, v231, v230, s87
aw a122,v232
mm v[92:95],v[220:223],a[4:7],v[92:95]
rt v[212:213],v242 offset:8448
rt v[214:215],v242 offset:9472
mm v[60:63],v[220:223],a[0:3],v[60:63]
aw a126,v233
.La5_LW7_2:
s_cbranch_execz .La5_LW7_6
ar v234,a155
mm v[60:63],v[216:219],a[20:23],v[60:63]
ar v235,a159
v_perm_b32 v236, v235, v234, s86
mm v[28:31],v[216:219],a[16:19],v[28:31]
wt vmcnt(10) lgkmcnt(4)
v_perm_b32 v237, v235, v234, s87
mm v[28:31],v[220:223],a[12:15],v[28:31]
rt v[216:217],v243 offset:0
rt v[218:219],v243 offset:1024
mm v[0:3],v[192:195],a[64:67],v[0:3]
s_add_u32 m0, s83, 17920
aw a123,v236
mm v[0:3],v[196:199],a[32:35],v[0:3]
ld v225,s[56:59],s80 offen lds
mm v[32:35],v[196:199],a[40:43],v[32:35]
aw a127,v237
ar v230,a160
mm v[32:35],v[192:195],a[68:71],v[32:35]
rt v[220:221],v243 offset:8448
rt v[222:223],v243 offset:9472
mm v[64:67],v[192:195],a[72:75],v[64:67]
s_add_u32 m0, s83, 19072
ar v231,a164
mm v[64:67],v[196:199],a[44:47],v[64:67]
ld v226,s[56:59],s80 offen lds
mm v[96:99],v[196:199],a[48:51],v[96:99]
v_perm_b32 v232, v231, v230, s86
v_perm_b32 v233, v231, v230, s87
mm v[96:99],v[192:195],a[36:39],v[96:99]
rt v[196:197],v240 offset:128
rt v[198:199],v240 offset:1152
mm v[100:103],v[200:203],a[36:39],v[100:103]
aw a128,v232
aw a132,v233
mm v[100:103],v[204:207],a[48:51],v[100:103]
ld v[136:139],v244,s[44:47],s55 offen offset:2048 sc1
mm v[68:71],v[204:207],a[44:47],v[68:71]
ar v234,a168
ar v235,a172
mm v[68:71],v[200:203],a[72:75],v[68:71]
rt v[192:193],v240 offset:8576
rt v[194:195],v240 offset:9600
mm v[36:39],v[200:203],a[68:71],v[36:39]
v_perm_b32 v236, v235, v234, s86
v_perm_b32 v237, v235, v234, s87
mm v[36:39],v[204:207],a[40:43],v[36:39]
ld v[140:143],v245,s[44:47],s55 offen offset:2048 sc1
mm v[4:7],v[204:207],a[32:35],v[4:7]
wt lgkmcnt(4)
aw a129,v236
mm v[4:7],v[200:203],a[64:67],v[4:7]
rt v[204:205],v241 offset:128
rt v[206:207],v241 offset:1152
mm v[8:11],v[208:211],a[64:67],v[8:11]
aw a133,v237
ar v230,a176
mm v[8:11],v[212:215],a[32:35],v[8:11]
ld v[152:155],v246,s[44:47],s55 offen offset:2048 sc1
mm v[40:43],v[212:215],a[40:43],v[40:43]
ar v231,a180
v_perm_b32 v232, v231, v230, s86
mm v[40:43],v[208:211],a[68:71],v[40:43]
rt v[200:201],v241 offset:8576
rt v[202:203],v241 offset:9600
mm v[72:75],v[208:211],a[72:75],v[72:75]
v_perm_b32 v233, v231, v230, s87
aw a130,v232
mm v[72:75],v[212:215],a[44:47],v[72:75]
ld v[156:159],v247,s[44:47],s55 offen offset:2048 sc1
mm v[104:107],v[212:215],a[48:51],v[104:107]
aw a134,v233
ar v234,a184
mm v[104:107],v[208:211],a[36:39],v[104:107]
rt v[212:213],v242 offset:128
rt v[214:215],v242 offset:1152
mm v[108:111],v[216:219],a[36:39],v[108:111]
ar v235,a188
v_perm_b32 v236, v235, v234, s86
mm v[108:111],v[220:223],a[48:51],v[108:111]
ld v[168:171],v244,s[44:47],s55 offen offset:3072 sc1
mm v[76:79],v[220:223],a[44:47],v[76:79]
v_perm_b32 v237, v235, v234, s87
aw a131,v236
mm v[76:79],v[216:219],a[72:75],v[76:79]
rt v[208:209],v242 offset:8576
rt v[210:211],v242 offset:9600
mm v[44:47],v[216:219],a[68:71],v[44:47]
aw a135,v237
ar v230,a161
mm v[44:47],v[220:223],a[40:43],v[44:47]
s_nop 0
v_cmp_eq_u64 vcc, v[250:251], v[252:253]
mm v[12:15],v[220:223],a[32:35],v[12:15]
wt lgkmcnt(4)
ar v231,a165
mm v[12:15],v[216:219],a[64:67],v[12:15]
rt v[220:221],v243 offset:128
rt v[222:223],v243 offset:1152
mm v[16:19],v[196:199],a[64:67],v[16:19]
v_perm_b32 v232, v231, v230, s86
v_perm_b32 v233, v231, v230, s87
mm v[16:19],v[192:195],a[32:35],v[16:19]
ld v[172:175],v245,s[44:47],s55 offen offset:3072 sc1
mm v[48:51],v[192:195],a[40:43],v[48:51]
aw a136,v232
aw a140,v233
mm v[48:51],v[196:199],a[68:71],v[48:51]
rt v[216:217],v243 offset:8576
rt v[218:219],v243 offset:9600
mm v[80:83],v[196:199],a[72:75],v[80:83]
ar v234,a169
ar v235,a173
mm v[80:83],v[192:195],a[44:47],v[80:83]
ld v[184:187],v246,s[44:47],s55 offen offset:3072 sc1
mm v[112:115],v[192:195],a[48:51],v[112:115]
wt vmcnt(18) lgkmcnt(0)
s_barrier
mm v[112:115],v[196:199],a[36:39],v[112:115]
rt v[192:193],v240 offset:16896
rt v[194:195],v240 offset:17920
mm v[116:119],v[204:207],a[36:39],v[116:119]
s_xor_b32 s90, s72, 0
s_lshl_b32 s92, s76, 5
mm v[116:119],v[200:203],a[48:51],v[116:119]
buffer_load_dwordx2 v[250:251], v248, s[64:67], s5 offen sc1
mm v[84:87],v[200:203],a[44:47],v[84:87]
s_add_u32 s92, s92, s90
s_lshl2_add_u32 s92, s92, s3
mm v[84:87],v[204:207],a[72:75],v[84:87]
rt v[196:197],v240 offset:25344
rt v[198:199],v240 offset:26368
mm v[52:55],v[204:207],a[68:71],v[52:55]
s_lshl_b32 s92, s92, 12
v_perm_b32 v236, v235, v234, s86
mm v[52:55],v[200:203],a[40:43],v[52:55]
ld v[188:191],v247,s[44:47],s55 offen offset:3072 sc1
mm v[20:23],v[200:203],a[32:35],v[20:23]
s_nop 0
s_bfe_u32 s55, s4, 262172
mm v[20:23],v[204:207],a[64:67],v[20:23]
rt v[200:201],v241 offset:16896
rt v[202:203],v241 offset:17920
mm v[24:27],v[212:215],a[64:67],v[24:27]
s_add_u32 m0, s83, 20096
s_xor_b32 s55, s55, s72
mm v[24:27],v[208:211],a[32:35],v[24:27]
ld v227,s[56:59],s80 offen lds
mm v[56:59],v[208:211],a[40:43],v[56:59]
s_add_u32 s80, s80, 131072
s_lshl_b32 s55, s55, 13
mm v[56:59],v[212:215],a[68:71],v[56:59]
rt v[204:205],v241 offset:25344
rt v[206:207],v241 offset:26368
mm v[88:91],v[212:215],a[72:75],v[88:91]
s_add_u32 m0, s83, 33792
v_perm_b32 v237, v235, v234, s87
mm v[88:91],v[208:211],a[44:47],v[88:91]
ld v224,s[56:59],s80 offen lds
mm v[120:123],v[208:211],a[48:51],v[120:123]
s_add_u32 s68, s16, s92
s_addc_u32 s69, s17, 0
mm v[120:123],v[212:215],a[36:39],v[120:123]
rt v[208:209],v242 offset:16896
rt v[210:211],v242 offset:17920
mm v[124:127],v[220:223],a[36:39],v[124:127]
s_add_u32 m0, s83, 34816
s_and_b32 s69, s69, 0xffff
mm v[124:127],v[216:219],a[48:51],v[124:127]
ld v225,s[56:59],s80 offen lds
mm v[92:95],v[216:219],a[44:47],v[92:95]
s_mov_b32 s70, 4096
s_mov_b32 s71, 0x20000
mm v[92:95],v[220:223],a[72:75],v[92:95]
rt v[212:213],v242 offset:25344
rt v[214:215],v242 offset:26368
mm v[60:63],v[220:223],a[68:71],v[60:63]
s_add_u32 m0, s83, 35968
aw a137,v236
mm v[60:63],v[216:219],a[40:43],v[60:63]
ld v226,s[56:59],s80 offen lds
mm v[28:31],v[216:219],a[32:35],v[28:31]
wt vmcnt(16) lgkmcnt(4)
aw a141,v237
mm v[28:31],v[220:223],a[64:67],v[28:31]
rt v[216:217],v243 offset:16896
rt v[218:219],v243 offset:17920
mm v[0:3],v[192:195],a[52:55],v[0:3]
s_add_u32 m0, s83, 36992
ar v230,a177
mm v[0:3],v[196:199],a[80:83],v[0:3]
ld v227,s[56:59],s80 offen lds
mm v[32:35],v[196:199],a[84:87],v[32:35]
s_bitcmp1_b32 vcc_lo, 4
s_cbranch_scc0 .La5_LO_NC4
.La5_LO_KC4:
mm v[32:35],v[192:195],a[56:59],v[32:35]
rt v[220:221],v243 offset:25344
rt v[222:223],v243 offset:26368
mm v[64:67],v[192:195],a[60:63],v[64:67]
s_bfe_u32 s80, s4, 262164
s_xor_b32 s80, s80, s72
mm v[64:67],v[196:199],a[88:91],v[64:67]
ld a[0:3],v244,s[44:47],s52 offen offset:0 sc1
mm v[96:99],v[196:199],a[92:95],v[96:99]
s_lshl_b32 s80, s80, 18
ar v231,a181
mm v[96:99],v[192:195],a[76:79],v[96:99]
rt v[196:197],v240 offset:17024
rt v[198:199],v240 offset:18048
mm v[100:103],v[200:203],a[76:79],v[100:103]
s_add_u32 s48, s36, 0
s_addc_u32 s49, s37, 0
mm v[100:103],v[204:207],a[92:95],v[100:103]
ld a[4:7],v245,s[44:47],s52 offen offset:0 sc1
mm v[68:71],v[204:207],a[88:91],v[68:71]
s_and_b32 s49, s49, 0xffff
s_mov_b32 s50, 2048
mm v[68:71],v[200:203],a[60:63],v[68:71]
rt v[192:193],v240 offset:25472
rt v[194:195],v240 offset:26496
mm v[36:39],v[200:203],a[56:59],v[36:39]
s_mov_b32 s51, 0x20000
v_and_b32 v238, 0xf0, v255
mm v[36:39],v[204:207],a[84:87],v[36:39]
ld a[8:11],v246,s[44:47],s52 offen offset:0 sc1
mm v[4:7],v[204:207],a[80:83],v[4:7]
wt lgkmcnt(4)
v_lshlrev_b32 v238, 2, v238
mm v[4:7],v[200:203],a[52:55],v[4:7]
rt v[204:205],v241 offset:17024
rt v[206:207],v241 offset:18048
mm v[8:11],v[208:211],a[52:55],v[8:11]
v_bfe_u32 v249, v255, 8, 1
v_lshl_add_u32 v238, v249, 5, v238
mm v[8:11],v[212:215],a[80:83],v[8:11]
ld a[12:15],v247,s[44:47],s52 offen offset:0 sc1
mm v[40:43],v[212:215],a[84:87],v[40:43]
v_lshrrev_b32_e64 v249, 9, v255
v_lshl_add_u32 v238, v249, 4, v238
mm v[40:43],v[208:211],a[56:59],v[40:43]
rt v[200:201],v241 offset:25472
rt v[202:203],v241 offset:26496
mm v[72:75],v[208:211],a[60:63],v[72:75]
s_lshr_b32 s23, s72, 3
s_lshl3_add_u32 s92, s23, s76
mm v[72:75],v[212:215],a[88:91],v[72:75]
ld a[16:19],v244,s[44:47],s52 offen offset:1024 sc1
mm v[104:107],v[212:215],a[92:95],v[104:107]
s_lshl_b32 s92, s92, 19
s_lshl_b32 s97, s3, 16
mm v[104:107],v[208:211],a[76:79],v[104:107]
rt v[212:213],v242 offset:17024
rt v[214:215],v242 offset:18048
mm v[108:111],v[216:219],a[76:79],v[108:111]
s_add_u32 s94, s92, s97
s_add_u32 s95, s94, 32768
mm v[108:111],v[220:223],a[92:95],v[108:111]
ld a[20:23],v245,s[44:47],s52 offen offset:1024 sc1
mm v[76:79],v[220:223],a[88:91],v[76:79]
s_sub_u32 s96, s92, s97
s_add_u32 s96, s96, 458752
mm v[76:79],v[216:219],a[60:63],v[76:79]
rt v[208:209],v242 offset:25472
rt v[210:211],v242 offset:26496
mm v[44:47],v[216:219],a[56:59],v[44:47]
s_add_u32 s97, s96, 32768
s_lshl3_add_u32 s92, s23, s101
mm v[44:47],v[220:223],a[84:87],v[44:47]
s_lshl_b32 s85, s76, 14
s_lshl3_add_u32 s92, s92, s85
mm v[12:15],v[220:223],a[80:83],v[12:15]
wt lgkmcnt(4)
s_add_u32 s92, s92, 2097152
mm v[12:15],v[216:219],a[52:55],v[12:15]
rt v[220:221],v243 offset:17024
rt v[222:223],v243 offset:18048
mm v[16:19],v[196:199],a[52:55],v[16:19]
v_perm_b32 v232, v231, v230, s86
v_perm_b32 v233, v231, v230, s87
mm v[16:19],v[192:195],a[80:83],v[16:19]
ld a[24:27],v246,s[44:47],s52 offen offset:1024 sc1
mm v[48:51],v[192:195],a[84:87],v[48:51]
aw a138,v232
aw a142,v233
mm v[48:51],v[196:199],a[56:59],v[48:51]
rt v[216:217],v243 offset:25472
rt v[218:219],v243 offset:26496
mm v[80:83],v[196:199],a[60:63],v[80:83]
ar v234,a185
ar v235,a189
mm v[80:83],v[192:195],a[88:91],v[80:83]
ld a[28:31],v247,s[44:47],s52 offen offset:1024 sc1
mm v[112:115],v[192:195],a[92:95],v[112:115]
v_perm_b32 v236, v235, v234, s86
v_perm_b32 v237, v235, v234, s87
mm v[112:115],v[196:199],a[76:79],v[112:115]
rt v[192:193],v240 offset:33792
rt v[194:195],v240 offset:34816
mm v[116:119],v[204:207],a[76:79],v[116:119]
s_add_u32 m0, s83, 50688
aw a139,v236
mm v[116:119],v[200:203],a[92:95],v[116:119]
ld v224,s[56:59],s80 offen lds
mm v[84:87],v[200:203],a[88:91],v[84:87]
aw a143,v237
ar v230,a162
mm v[84:87],v[204:207],a[60:63],v[84:87]
rt v[196:197],v240 offset:42240
rt v[198:199],v240 offset:43264
mm v[52:55],v[204:207],a[56:59],v[52:55]
s_add_u32 m0, s83, 51712
ar v231,a166
mm v[52:55],v[200:203],a[84:87],v[52:55]
ld v225,s[56:59],s80 offen lds
mm v[20:23],v[200:203],a[80:83],v[20:23]
wt lgkmcnt(4)
v_perm_b32 v232, v231, v230, s86
mm v[20:23],v[204:207],a[52:55],v[20:23]
rt v[200:201],v241 offset:33792
rt v[202:203],v241 offset:34816
mm v[24:27],v[212:215],a[52:55],v[24:27]
s_add_u32 m0, s83, 52864
v_perm_b32 v233, v231, v230, s87
mm v[24:27],v[208:211],a[80:83],v[24:27]
ld v226,s[56:59],s80 offen lds
mm v[56:59],v[208:211],a[84:87],v[56:59]
aw a144,v232
aw a148,v233
mm v[56:59],v[212:215],a[56:59],v[56:59]
rt v[204:205],v241 offset:42240
rt v[206:207],v241 offset:43264
mm v[88:91],v[212:215],a[60:63],v[88:91]
ar v234,a170
ar v235,a174
mm v[88:91],v[208:211],a[88:91],v[88:91]
ld a[32:35],v244,s[44:47],s52 offen offset:2048 sc1
mm v[120:123],v[208:211],a[92:95],v[120:123]
v_perm_b32 v236, v235, v234, s86
v_perm_b32 v237, v235, v234, s87
mm v[120:123],v[212:215],a[76:79],v[120:123]
rt v[208:209],v242 offset:33792
rt v[210:211],v242 offset:34816
mm v[124:127],v[220:223],a[76:79],v[124:127]
aw a145,v236
aw a149,v237
mm v[124:127],v[216:219],a[92:95],v[124:127]
ld a[36:39],v245,s[44:47],s52 offen offset:2048 sc1
mm v[92:95],v[216:219],a[88:91],v[92:95]
ar v230,a178
ar v231,a182
mm v[92:95],v[220:223],a[60:63],v[92:95]
rt v[212:213],v242 offset:42240
rt v[214:215],v242 offset:43264
mm v[60:63],v[220:223],a[56:59],v[60:63]
v_perm_b32 v232, v231, v230, s86
v_perm_b32 v233, v231, v230, s87
mm v[60:63],v[216:219],a[84:87],v[60:63]
aw a146,v232
aw a150,v233
mm v[28:31],v[216:219],a[80:83],v[28:31]
wt vmcnt(18) lgkmcnt(4)
ar v234,a186
mm v[28:31],v[220:223],a[52:55],v[28:31]
rt v[216:217],v243 offset:33792
rt v[218:219],v243 offset:34816
mm v[0:3],v[192:195],v[136:139],v[0:3]
ar v235,a190
v_perm_b32 v236, v235, v234, s86
mm v[0:3],v[196:199],v[168:171],v[0:3]
ld a[40:43],v246,s[44:47],s52 offen offset:2048 sc1
mm v[32:35],v[196:199],v[172:175],v[32:35]
v_perm_b32 v237, v235, v234, s87
aw a147,v236
mm v[32:35],v[192:195],v[140:143],v[32:35]
rt v[220:221],v243 offset:42240
rt v[222:223],v243 offset:43264
mm v[64:67],v[192:195],v[152:155],v[64:67]
aw a151,v237
ar v230,a163
mm v[64:67],v[196:199],v[184:187],v[64:67]
ld a[44:47],v247,s[44:47],s52 offen offset:2048 sc1
mm v[96:99],v[196:199],v[188:191],v[96:99]
ar v231,a167
v_perm_b32 v232, v231, v230, s86
mm v[96:99],v[192:195],v[156:159],v[96:99]
rt v[196:197],v240 offset:33920
rt v[198:199],v240 offset:34944
mm v[100:103],v[200:203],v[156:159],v[100:103]
v_perm_b32 v233, v231, v230, s87
aw a152,v232
mm v[100:103],v[204:207],v[188:191],v[100:103]
ld a[48:51],v244,s[44:47],s52 offen offset:3072 sc1
mm v[68:71],v[204:207],v[184:187],v[68:71]
aw a156,v233
ar v234,a171
mm v[68:71],v[200:203],v[152:155],v[68:71]
rt v[192:193],v240 offset:42368
rt v[194:195],v240 offset:43392
mm v[36:39],v[200:203],v[140:143],v[36:39]
v_add_u32 v240, 4294899712, v240
ar v235,a175
mm v[36:39],v[204:207],v[172:175],v[36:39]
ld a[52:55],v245,s[44:47],s52 offen offset:3072 sc1
mm v[4:7],v[204:207],v[168:171],v[4:7]
wt lgkmcnt(4)
v_perm_b32 v236, v235, v234, s86
mm v[4:7],v[200:203],v[136:139],v[4:7]
rt v[204:205],v241 offset:33920
rt v[206:207],v241 offset:34944
mm v[8:11],v[208:211],v[136:139],v[8:11]
s_nop 0
v_cmp_eq_u64 vcc, v[250:251], v[252:253]
mm v[8:11],v[212:215],v[168:171],v[8:11]
ld a[56:59],v246,s[44:47],s52 offen offset:3072 sc1
mm v[40:43],v[212:215],v[172:175],v[40:43]
v_perm_b32 v237, v235, v234, s87
aw a153,v236
mm v[40:43],v[208:211],v[140:143],v[40:43]
rt v[200:201],v241 offset:42368
rt v[202:203],v241 offset:43392
mm v[72:75],v[208:211],v[152:155],v[72:75]
v_add_u32 v241, 4294899712, v241
aw a157,v237
mm v[72:75],v[212:215],v[184:187],v[72:75]
ld a[60:63],v247,s[44:47],s52 offen offset:3072 sc1
mm v[104:107],v[212:215],v[188:191],v[104:107]
ar v230,a179
ar v231,a183
mm v[104:107],v[208:211],v[156:159],v[104:107]
rt v[212:213],v242 offset:33920
rt v[214:215],v242 offset:34944
mm v[108:111],v[216:219],v[156:159],v[108:111]
v_perm_b32 v232, v231, v230, s86
v_perm_b32 v233, v231, v230, s87
mm v[108:111],v[220:223],v[188:191],v[108:111]
buffer_load_dwordx2 v[250:251], v248, s[64:67], s5 offen sc1
mm v[76:79],v[220:223],v[184:187],v[76:79]
aw a154,v232
aw a158,v233
mm v[76:79],v[216:219],v[152:155],v[76:79]
rt v[208:209],v242 offset:42368
rt v[210:211],v242 offset:43392
mm v[44:47],v[216:219],v[140:143],v[44:47]
s_add_u32 m0, s83, 53888
v_add_u32 v242, 4294899712, v242
mm v[44:47],v[220:223],v[172:175],v[44:47]
ld v227,s[56:59],s80 offen lds
mm v[12:15],v[220:223],v[168:171],v[12:15]
wt lgkmcnt(4)
s_bfe_u32 s80, s4, 262168
mm v[12:15],v[216:219],v[136:139],v[12:15]
rt v[220:221],v243 offset:33920
rt v[222:223],v243 offset:34944
mm v[16:19],v[196:199],v[136:139],v[16:19]
s_add_u32 m0, s83, 118272
s_xor_b32 s80, s80, s72
s_lshl_b32 s80, s80, 18
mm v[16:19],v[192:195],v[168:171],v[16:19]
ld v224,s[56:59],s80 offen lds
mm v[48:51],v[192:195],v[172:175],v[48:51]
ar v234,a187
ar v235,a191
mm v[48:51],v[196:199],v[140:143],v[48:51]
rt v[216:217],v243 offset:42368
rt v[218:219],v243 offset:43392
mm v[80:83],v[196:199],v[152:155],v[80:83]
s_add_u32 m0, s83, 119296
v_add_u32 v243, 4294899712, v243
mm v[80:83],v[192:195],v[184:187],v[80:83]
ld v225,s[56:59],s80 offen lds
mm v[112:115],v[192:195],v[188:191],v[112:115]
wt vmcnt(23) lgkmcnt(0)
s_barrier
mm v[112:115],v[196:199],v[156:159],v[112:115]
rt v[192:193],v240 offset:16896
rt v[194:195],v240 offset:17920
mm v[116:119],v[204:207],v[156:159],v[116:119]
s_add_u32 m0, s83, 120448
v_perm_b32 v236, v235, v234, s86
mm v[116:119],v[200:203],v[188:191],v[116:119]
ld v226,s[56:59],s80 offen lds
mm v[84:87],v[200:203],v[184:187],v[84:87]
s_bitcmp1_b32 vcc_lo, 5
s_cbranch_scc0 .La5_LO_NC5
.La5_LO_KC5:
mm v[84:87],v[204:207],v[152:155],v[84:87]
rt v[196:197],v240 offset:25344
rt v[198:199],v240 offset:26368
mm v[52:55],v[204:207],v[140:143],v[52:55]
v_perm_b32 v237, v235, v234, s87
aw a155,v236
mm v[52:55],v[200:203],v[172:175],v[52:55]
ld v[128:131],v244,s[44:47],s53 offen offset:0 sc1
mm v[20:23],v[200:203],v[168:171],v[20:23]
s_nop 0
aw a159,v237
mm v[20:23],v[204:207],v[136:139],v[20:23]
rt v[200:201],v241 offset:16896
rt v[202:203],v241 offset:17920
mm v[24:27],v[212:215],v[136:139],v[24:27]
ar v230,a192
ar v231,a196
mm v[24:27],v[208:211],v[168:171],v[24:27]
ld v[132:135],v245,s[44:47],s53 offen offset:0 sc1
mm v[56:59],v[208:211],v[172:175],v[56:59]
v_perm_b32 v232, v231, v230, s86
v_perm_b32 v233, v231, v230, s87
mm v[56:59],v[212:215],v[140:143],v[56:59]
rt v[204:205],v241 offset:25344
rt v[206:207],v241 offset:26368
mm v[88:91],v[212:215],v[152:155],v[88:91]
aw a160,v232
aw a164,v233
mm v[88:91],v[208:211],v[184:187],v[88:91]
ld v[144:147],v246,s[44:47],s53 offen offset:0 sc1
mm v[120:123],v[208:211],v[188:191],v[120:123]
ar v234,a200
ar v235,a204
mm v[120:123],v[212:215],v[156:159],v[120:123]
rt v[208:209],v242 offset:16896
rt v[210:211],v242 offset:17920
mm v[124:127],v[220:223],v[156:159],v[124:127]
v_perm_b32 v236, v235, v234, s86
v_perm_b32 v237, v235, v234, s87
mm v[124:127],v[216:219],v[188:191],v[124:127]
ld v[156:159],v247,s[44:47],s53 offen offset:0 sc1
mm v[92:95],v[216:219],v[184:187],v[92:95]
aw a161,v236
aw a165,v237
mm v[92:95],v[220:223],v[152:155],v[92:95]
rt v[212:213],v242 offset:25344
rt v[214:215],v242 offset:26368
mm v[60:63],v[220:223],v[140:143],v[60:63]
ar v230,a208
ar v231,a212
mm v[60:63],v[216:219],v[172:175],v[60:63]
v_perm_b32 v232, v231, v230, s86
.La5_LW7_3:
s_cbranch_execz .La5_LW7_7
v_perm_b32 v233, v231, v230, s87
mm v[28:31],v[216:219],v[168:171],v[28:31]
wt vmcnt(20) lgkmcnt(4)
aw a162,v232
mm v[28:31],v[220:223],v[136:139],v[28:31]
rt v[216:217],v243 offset:16896
rt v[218:219],v243 offset:17920
mm v[0:3],v[192:195],a[0:3],v[0:3]
aw a166,v233
ar v234,a216
mm v[0:3],v[196:199],a[16:19],v[0:3]
ld v[136:139],v244,s[44:47],s53 offen offset:1024 sc1
mm v[32:35],v[196:199],a[20:23],v[32:35]
ar v235,a220
v_perm_b32 v236, v235, v234, s86
mm v[32:35],v[192:195],a[4:7],v[32:35]
rt v[220:221],v243 offset:25344
rt v[222:223],v243 offset:26368
mm v[64:67],v[192:195],a[8:11],v[64:67]
v_perm_b32 v237, v235, v234, s87
aw a163,v236
mm v[64:67],v[196:199],a[24:27],v[64:67]
ld v[140:143],v245,s[44:47],s53 offen offset:1024 sc1
mm v[96:99],v[196:199],a[28:31],v[96:99]
aw a167,v237
ar v230,a193
mm v[96:99],v[192:195],a[12:15],v[96:99]
rt v[196:197],v240 offset:17024
rt v[198:199],v240 offset:18048
mm v[100:103],v[200:203],a[12:15],v[100:103]
ar v231,a197
v_perm_b32 v232, v231, v230, s86
mm v[100:103],v[204:207],a[28:31],v[100:103]
ld v[152:155],v246,s[44:47],s53 offen offset:1024 sc1
mm v[68:71],v[204:207],a[24:27],v[68:71]
v_perm_b32 v233, v231, v230, s87
aw a168,v232
mm v[68:71],v[200:203],a[8:11],v[68:71]
rt v[192:193],v240 offset:25472
rt v[194:195],v240 offset:26496
mm v[36:39],v[200:203],a[4:7],v[36:39]
aw a172,v233
ar v234,a201
mm v[36:39],v[204:207],a[20:23],v[36:39]
ld v[168:171],v247,s[44:47],s53 offen offset:1024 sc1
mm v[4:7],v[204:207],a[16:19],v[4:7]
wt lgkmcnt(4)
ar v235,a205
mm v[4:7],v[200:203],a[0:3],v[4:7]
rt v[204:205],v241 offset:17024
rt v[206:207],v241 offset:18048
mm v[8:11],v[208:211],a[0:3],v[8:11]
s_add_u32 m0, s83, 121472
v_perm_b32 v236, v235, v234, s86
mm v[8:11],v[212:215],a[16:19],v[8:11]
ld v227,s[56:59],s80 offen lds
mm v[40:43],v[212:215],a[20:23],v[40:43]
s_add_u32 s80, s80, 131072
v_perm_b32 v237, v235, v234, s87
mm v[40:43],v[208:211],a[4:7],v[40:43]
rt v[200:201],v241 offset:25472
rt v[202:203],v241 offset:26496
mm v[72:75],v[208:211],a[8:11],v[72:75]
s_add_u32 m0, s83, 67584
aw a169,v236
mm v[72:75],v[212:215],a[24:27],v[72:75]
ld v224,s[56:59],s80 offen lds
mm v[104:107],v[212:215],a[28:31],v[104:107]
aw a173,v237
ar v230,a209
mm v[104:107],v[208:211],a[12:15],v[104:107]
rt v[212:213],v242 offset:17024
rt v[214:215],v242 offset:18048
mm v[108:111],v[216:219],a[12:15],v[108:111]
s_add_u32 m0, s83, 68608
ar v231,a213
mm v[108:111],v[220:223],a[28:31],v[108:111]
ld v225,s[56:59],s80 offen lds
mm v[76:79],v[220:223],a[24:27],v[76:79]
v_perm_b32 v232, v231, v230, s86
v_perm_b32 v233, v231, v230, s87
mm v[76:79],v[216:219],a[8:11],v[76:79]
rt v[208:209],v242 offset:25472
rt v[210:211],v242 offset:26496
mm v[44:47],v[216:219],a[4:7],v[44:47]
aw a170,v232
aw a174,v233
mm v[44:47],v[220:223],a[20:23],v[44:47]
ar v234,a217
ar v235,a221
mm v[12:15],v[220:223],a[16:19],v[12:15]
wt lgkmcnt(4)
v_perm_b32 v236, v235, v234, s86
mm v[12:15],v[216:219],a[0:3],v[12:15]
rt v[220:221],v243 offset:17024
rt v[222:223],v243 offset:18048
mm v[16:19],v[196:199],a[0:3],v[16:19]
s_add_u32 m0, s83, 69760
v_perm_b32 v237, v235, v234, s87
mm v[16:19],v[192:195],a[16:19],v[16:19]
ld v226,s[56:59],s80 offen lds
mm v[48:51],v[192:195],a[20:23],v[48:51]
aw a171,v236
aw a175,v237
mm v[48:51],v[196:199],a[4:7],v[48:51]
rt v[216:217],v243 offset:25472
rt v[218:219],v243 offset:26496
mm v[80:83],v[196:199],a[8:11],v[80:83]
s_add_u32 m0, s83, 70784
ar v230,a194
mm v[80:83],v[192:195],a[24:27],v[80:83]
ld v227,s[56:59],s80 offen lds
mm v[112:115],v[192:195],a[28:31],v[112:115]
s_bfe_u32 s80, s4, 262172
s_xor_b32 s80, s80, s72
mm v[112:115],v[196:199],a[12:15],v[112:115]
rt v[192:193],v240 offset:33792
rt v[194:195],v240 offset:34816
mm v[116:119],v[204:207],a[12:15],v[116:119]
s_add_u32 m0, s83, 84480
s_lshl_b32 s80, s80, 18
mm v[116:119],v[200:203],a[28:31],v[116:119]
ld v224,s[56:59],s80 offen lds
mm v[84:87],v[200:203],a[24:27],v[84:87]
ar v231,a198
v_perm_b32 v232, v231, v230, s86
mm v[84:87],v[204:207],a[8:11],v[84:87]
rt v[196:197],v240 offset:42240
rt v[198:199],v240 offset:43264
mm v[52:55],v[204:207],a[4:7],v[52:55]
s_add_u32 m0, s83, 85504
v_perm_b32 v233, v231, v230, s87
mm v[52:55],v[200:203],a[20:23],v[52:55]
ld v225,s[56:59],s80 offen lds
mm v[20:23],v[200:203],a[16:19],v[20:23]
wt lgkmcnt(4)
aw a176,v232
mm v[20:23],v[204:207],a[0:3],v[20:23]
rt v[200:201],v241 offset:33792
rt v[202:203],v241 offset:34816
mm v[24:27],v[212:215],a[0:3],v[24:27]
s_add_u32 m0, s83, 86656
aw a180,v233
mm v[24:27],v[208:211],a[16:19],v[24:27]
ld v226,s[56:59],s80 offen lds
mm v[56:59],v[208:211],a[20:23],v[56:59]
ar v234,a202
ar v235,a206
mm v[56:59],v[212:215],a[4:7],v[56:59]
rt v[204:205],v241 offset:42240
rt v[206:207],v241 offset:43264
mm v[88:91],v[212:215],a[8:11],v[88:91]
s_add_u32 m0, s83, 87680
v_perm_b32 v236, v235, v234, s86
mm v[88:91],v[208:211],a[24:27],v[88:91]
ld v227,s[56:59],s80 offen lds
mm v[120:123],v[208:211],a[28:31],v[120:123]
s_add_u32 s80, s80, 131072
v_perm_b32 v237, v235, v234, s87
mm v[120:123],v[212:215],a[12:15],v[120:123]
rt v[208:209],v242 offset:33792
rt v[210:211],v242 offset:34816
mm v[124:127],v[220:223],a[12:15],v[124:127]
s_add_u32 m0, s83, 101376
aw a177,v236
mm v[124:127],v[216:219],a[28:31],v[124:127]
ld v224,s[56:59],s80 offen lds
mm v[92:95],v[216:219],a[24:27],v[92:95]
aw a181,v237
ar v230,a210
mm v[92:95],v[220:223],a[8:11],v[92:95]
rt v[212:213],v242 offset:42240
rt v[214:215],v242 offset:43264
mm v[60:63],v[220:223],a[4:7],v[60:63]
ar v231,a214
v_perm_b32 v232, v231, v230, s86
mm v[60:63],v[216:219],a[20:23],v[60:63]
wt vmcnt(22)
v_cmp_eq_u64 vcc, v[250:251], v[252:253]
mm v[28:31],v[216:219],a[16:19],v[28:31]
wt lgkmcnt(4)
v_perm_b32 v233, v231, v230, s87
mm v[28:31],v[220:223],a[0:3],v[28:31]
rt v[216:217],v243 offset:33792
rt v[218:219],v243 offset:34816
mm v[0:3],v[192:195],a[32:35],v[0:3]
s_add_u32 m0, s83, 102400
aw a178,v232
mm v[0:3],v[196:199],a[48:51],v[0:3]
ld v225,s[56:59],s80 offen lds
mm v[32:35],v[196:199],a[52:55],v[32:35]
aw a182,v233
ar v234,a218
mm v[32:35],v[192:195],a[36:39],v[32:35]
rt v[220:221],v243 offset:42240
rt v[222:223],v243 offset:43264
mm v[64:67],v[192:195],a[40:43],v[64:67]
s_add_u32 m0, s83, 103552
ar v235,a222
mm v[64:67],v[196:199],a[56:59],v[64:67]
ld v226,s[56:59],s80 offen lds
mm v[96:99],v[196:199],a[60:63],v[96:99]
v_perm_b32 v236, v235, v234, s86
v_perm_b32 v237, v235, v234, s87
mm v[96:99],v[192:195],a[44:47],v[96:99]
rt v[196:197],v240 offset:33920
rt v[198:199],v240 offset:34944
mm v[100:103],v[200:203],a[44:47],v[100:103]
s_add_u32 m0, s83, 104576
aw a179,v236
mm v[100:103],v[204:207],a[60:63],v[100:103]
ld v227,s[56:59],s80 offen lds
mm v[68:71],v[204:207],a[56:59],v[68:71]
s_bitcmp1_b32 vcc_lo, 6
s_cbranch_scc0 .La5_LO_NC6
.La5_LO_KC6:
mm v[68:71],v[200:203],a[40:43],v[68:71]
rt v[192:193],v240 offset:42368
rt v[194:195],v240 offset:43392
mm v[36:39],v[200:203],a[36:39],v[36:39]
v_bfe_u32 v224, v255, 6, 1
v_lshrrev_b32 v225, 7, v255
mm v[36:39],v[204:207],a[52:55],v[36:39]
ld a[64:67],v244,s[44:47],s54 offen offset:0 sc1
mm v[4:7],v[204:207],a[48:51],v[4:7]
wt lgkmcnt(4)
v_and_b32 v226, 48, v255
mm v[4:7],v[200:203],a[32:35],v[4:7]
rt v[204:205],v241 offset:33920
rt v[206:207],v241 offset:34944
mm v[8:11],v[208:211],a[32:35],v[8:11]
v_lshl_add_u32 v225, v225, 6, v226
v_lshl_add_u32 v224, v224, 11, v225
mm v[8:11],v[212:215],a[48:51],v[8:11]
ld a[68:71],v245,s[44:47],s54 offen offset:0 sc1
mm v[40:43],v[212:215],a[52:55],v[40:43]
aw a183,v237
ar v230,a195
mm v[40:43],v[208:211],a[36:39],v[40:43]
rt v[200:201],v241 offset:42368
rt v[202:203],v241 offset:43392
mm v[72:75],v[208:211],a[40:43],v[72:75]
ar v231,a199
v_perm_b32 v232, v231, v230, s86
mm v[72:75],v[212:215],a[56:59],v[72:75]
ld a[72:75],v244,s[44:47],s54 offen offset:1024 sc1
mm v[104:107],v[212:215],a[60:63],v[104:107]
v_perm_b32 v233, v231, v230, s87
aw a184,v232
mm v[104:107],v[208:211],a[44:47],v[104:107]
rt v[212:213],v242 offset:33920
rt v[214:215],v242 offset:34944
mm v[108:111],v[216:219],a[44:47],v[108:111]
aw a188,v233
ar v234,a203
mm v[108:111],v[220:223],a[60:63],v[108:111]
ld a[76:79],v245,s[44:47],s54 offen offset:1024 sc1
mm v[76:79],v[220:223],a[56:59],v[76:79]
ar v235,a207
v_perm_b32 v236, v235, v234, s86
mm v[76:79],v[216:219],a[40:43],v[76:79]
rt v[208:209],v242 offset:42368
rt v[210:211],v242 offset:43392
mm v[44:47],v[216:219],a[36:39],v[44:47]
v_perm_b32 v237, v235, v234, s87
aw a185,v236
mm v[44:47],v[220:223],a[52:55],v[44:47]
aw a189,v237
ar v230,a211
mm v[12:15],v[220:223],a[48:51],v[12:15]
wt lgkmcnt(4)
ar v231,a215
mm v[12:15],v[216:219],a[32:35],v[12:15]
rt v[220:221],v243 offset:33920
rt v[222:223],v243 offset:34944
mm v[16:19],v[196:199],a[32:35],v[16:19]
v_perm_b32 v232, v231, v230, s86
v_perm_b32 v233, v231, v230, s87
mm v[16:19],v[192:195],a[48:51],v[16:19]
ld a[80:83],v244,s[44:47],s54 offen offset:2048 sc1
mm v[48:51],v[192:195],a[52:55],v[48:51]
aw a186,v232
aw a190,v233
mm v[48:51],v[196:199],a[36:39],v[48:51]
rt v[216:217],v243 offset:42368
rt v[218:219],v243 offset:43392
mm v[80:83],v[196:199],a[40:43],v[80:83]
ar v234,a219
ar v235,a223
mm v[80:83],v[192:195],a[56:59],v[80:83]
ld a[84:87],v245,s[44:47],s54 offen offset:2048 sc1
mm v[112:115],v[192:195],a[60:63],v[112:115]
wt vmcnt(30)
s_barrier
mm v[112:115],v[196:199],a[44:47],v[112:115]
rt v[192:193],v240 offset:50688
rt v[194:195],v240 offset:51712
mm v[116:119],v[204:207],a[44:47],v[116:119]
v_perm_b32 v236, v235, v234, s86
v_perm_b32 v237, v235, v234, s87
mm v[116:119],v[200:203],a[60:63],v[116:119]
ld a[88:91],v244,s[44:47],s54 offen offset:3072 sc1
mm v[84:87],v[200:203],a[56:59],v[84:87]
aw a187,v236
aw a191,v237
mm v[84:87],v[204:207],a[40:43],v[84:87]
rt v[196:197],v240 offset:59136
rt v[198:199],v240 offset:60160
mm v[52:55],v[204:207],a[36:39],v[52:55]
s_bitcmp1_b32 vcc_lo, 7
s_cbranch_scc0 .La5_LO_NC7
.La5_LO_KC7:
mm v[52:55],v[200:203],a[52:55],v[52:55]
ld a[92:95],v245,s[44:47],s54 offen offset:3072 sc1
mm v[20:23],v[200:203],a[48:51],v[20:23]
wt lgkmcnt(4)
ar v230,a224
mm v[20:23],v[204:207],a[32:35],v[20:23]
rt v[200:201],v241 offset:50688
rt v[202:203],v241 offset:51712
mm v[24:27],v[212:215],a[32:35],v[24:27]
ar v231,a228
v_perm_b32 v232, v231, v230, s86
mm v[24:27],v[208:211],a[48:51],v[24:27]
ld a[0:3],v244,s[44:47],s55 offen offset:0 sc1
mm v[56:59],v[208:211],a[52:55],v[56:59]
v_perm_b32 v233, v231, v230, s87
aw a192,v232
mm v[56:59],v[212:215],a[36:39],v[56:59]
rt v[204:205],v241 offset:59136
rt v[206:207],v241 offset:60160
mm v[88:91],v[212:215],a[40:43],v[88:91]
aw a196,v233
ar v234,a232
mm v[88:91],v[208:211],a[56:59],v[88:91]
ld a[4:7],v245,s[44:47],s55 offen offset:0 sc1
mm v[120:123],v[208:211],a[60:63],v[120:123]
ar v235,a236
v_perm_b32 v236, v235, v234, s86
mm v[120:123],v[212:215],a[44:47],v[120:123]
rt v[208:209],v242 offset:50688
rt v[210:211],v242 offset:51712
mm v[124:127],v[220:223],a[44:47],v[124:127]
v_perm_b32 v237, v235, v234, s87
aw a193,v236
mm v[124:127],v[216:219],a[60:63],v[124:127]
ld a[8:11],v244,s[44:47],s55 offen offset:1024 sc1
mm v[92:95],v[216:219],a[56:59],v[92:95]
aw a197,v237
ar v230,a240
mm v[92:95],v[220:223],a[40:43],v[92:95]
rt v[212:213],v242 offset:59136
rt v[214:215],v242 offset:60160
mm v[60:63],v[220:223],a[36:39],v[60:63]
ar v231,a244
v_perm_b32 v232, v231, v230, s86
mm v[60:63],v[216:219],a[52:55],v[60:63]
v_perm_b32 v233, v231, v230, s87
aw a194,v232
mm v[28:31],v[216:219],a[48:51],v[28:31]
wt vmcnt(24) lgkmcnt(4)
aw a198,v233
mm v[28:31],v[220:223],a[32:35],v[28:31]
rt v[216:217],v243 offset:50688
rt v[218:219],v243 offset:51712
mm v[0:3],v[192:195],v[128:131],v[0:3]
ar v234,a248
ar v235,a252
mm v[0:3],v[196:199],v[136:139],v[0:3]
ld a[12:15],v245,s[44:47],s55 offen offset:1024 sc1
mm v[32:35],v[196:199],v[140:143],v[32:35]
v_perm_b32 v236, v235, v234, s86
v_perm_b32 v237, v235, v234, s87
mm v[32:35],v[192:195],v[132:135],v[32:35]
rt v[220:221],v243 offset:59136
rt v[222:223],v243 offset:60160
mm v[64:67],v[192:195],v[144:147],v[64:67]
aw a195,v236
aw a199,v237
mm v[64:67],v[196:199],v[152:155],v[64:67]
ld a[16:19],v244,s[44:47],s55 offen offset:2048 sc1
mm v[96:99],v[196:199],v[168:171],v[96:99]
ar v230,a225
ar v231,a229
mm v[96:99],v[192:195],v[156:159],v[96:99]
rt v[196:197],v240 offset:50816
rt v[198:199],v240 offset:51840
mm v[100:103],v[200:203],v[156:159],v[100:103]
v_perm_b32 v232, v231, v230, s86
v_perm_b32 v233, v231, v230, s87
mm v[100:103],v[204:207],v[168:171],v[100:103]
ld a[20:23],v245,s[44:47],s55 offen offset:2048 sc1
mm v[68:71],v[204:207],v[152:155],v[68:71]
aw a200,v232
aw a204,v233
mm v[68:71],v[200:203],v[144:147],v[68:71]
rt v[192:193],v240 offset:59264
rt v[194:195],v240 offset:60288
mm v[36:39],v[200:203],v[132:135],v[36:39]
v_add_u32 v240, 67584, v240
ar v234,a233
mm v[36:39],v[204:207],v[140:143],v[36:39]
ld a[24:27],v244,s[44:47],s55 offen offset:3072 sc1
mm v[4:7],v[204:207],v[136:139],v[4:7]
wt lgkmcnt(4)
ar v235,a237
mm v[4:7],v[200:203],v[128:131],v[4:7]
rt v[204:205],v241 offset:50816
rt v[206:207],v241 offset:51840
mm v[8:11],v[208:211],v[128:131],v[8:11]
v_perm_b32 v236, v235, v234, s86
v_perm_b32 v237, v235, v234, s87
mm v[8:11],v[212:215],v[136:139],v[8:11]
ld a[28:31],v245,s[44:47],s55 offen offset:3072 sc1
mm v[40:43],v[212:215],v[140:143],v[40:43]
aw a201,v236
aw a205,v237
mm v[40:43],v[208:211],v[132:135],v[40:43]
rt v[200:201],v241 offset:59264
rt v[202:203],v241 offset:60288
mm v[72:75],v[208:211],v[144:147],v[72:75]
v_add_u32 v241, 67584, v241
ar v230,a241
mm v[72:75],v[212:215],v[152:155],v[72:75]
ar v231,a245
v_perm_b32 v232, v231, v230, s86
mm v[104:107],v[212:215],v[168:171],v[104:107]
v_perm_b32 v233, v231, v230, s87
aw a202,v232
mm v[104:107],v[208:211],v[156:159],v[104:107]
rt v[212:213],v242 offset:50816
rt v[214:215],v242 offset:51840
mm v[108:111],v[216:219],v[156:159],v[108:111]
aw a206,v233
ar v234,a249
mm v[108:111],v[220:223],v[168:171],v[108:111]
ar v235,a253
v_perm_b32 v236, v235, v234, s86
mm v[76:79],v[220:223],v[152:155],v[76:79]
v_perm_b32 v237, v235, v234, s87
aw a203,v236
mm v[76:79],v[216:219],v[144:147],v[76:79]
rt v[208:209],v242 offset:59264
rt v[210:211],v242 offset:60288
mm v[44:47],v[216:219],v[132:135],v[44:47]
v_add_u32 v242, 67584, v242
aw a207,v237
mm v[44:47],v[220:223],v[140:143],v[44:47]
ar v230,a226
ar v231,a230
mm v[12:15],v[220:223],v[136:139],v[12:15]
wt lgkmcnt(4)
v_perm_b32 v232, v231, v230, s86
mm v[12:15],v[216:219],v[128:131],v[12:15]
rt v[220:221],v243 offset:50816
rt v[222:223],v243 offset:51840
mm v[16:19],v[196:199],v[128:131],v[16:19]
v_perm_b32 v233, v231, v230, s87
aw a208,v232
mm v[16:19],v[192:195],v[136:139],v[16:19]
ld a[32:35],v246,s[44:47],s54 offen offset:0 sc1
mm v[48:51],v[192:195],v[140:143],v[48:51]
aw a212,v233
ar v234,a234
mm v[48:51],v[196:199],v[132:135],v[48:51]
rt v[216:217],v243 offset:59264
rt v[218:219],v243 offset:60288
mm v[80:83],v[196:199],v[144:147],v[80:83]
v_add_u32 v243, 67584, v243
ar v235,a238
mm v[80:83],v[192:195],v[152:155],v[80:83]
ld a[36:39],v247,s[44:47],s54 offen offset:0 sc1
mm v[112:115],v[192:195],v[168:171],v[112:115]
wt vmcnt(18)
s_barrier
mm v[112:115],v[196:199],v[156:159],v[112:115]
rt v[192:193],v240 offset:50688
rt v[194:195],v240 offset:51712
mm v[116:119],v[204:207],v[156:159],v[116:119]
v_perm_b32 v236, v235, v234, s86
v_perm_b32 v237, v235, v234, s87
mm v[116:119],v[200:203],v[168:171],v[116:119]
ld a[40:43],v246,s[44:47],s54 offen offset:1024 sc1
mm v[84:87],v[200:203],v[152:155],v[84:87]
aw a209,v236
aw a213,v237
mm v[84:87],v[204:207],v[144:147],v[84:87]
rt v[196:197],v240 offset:59136
rt v[198:199],v240 offset:60160
mm v[52:55],v[204:207],v[132:135],v[52:55]
ar v230,a242
ar v231,a246
mm v[52:55],v[200:203],v[140:143],v[52:55]
ld a[44:47],v247,s[44:47],s54 offen offset:1024 sc1
mm v[20:23],v[200:203],v[136:139],v[20:23]
wt lgkmcnt(4)
v_perm_b32 v232, v231, v230, s86
mm v[20:23],v[204:207],v[128:131],v[20:23]
rt v[200:201],v240 offset:0
rt v[202:203],v240 offset:1024
mm v[24:27],v[212:215],v[128:131],v[24:27]
v_perm_b32 v233, v231, v230, s87
aw a210,v232
mm v[24:27],v[208:211],v[136:139],v[24:27]
ld a[48:51],v246,s[44:47],s54 offen offset:2048 sc1
mm v[56:59],v[208:211],v[140:143],v[56:59]
aw a214,v233
ar v234,a250
mm v[56:59],v[212:215],v[132:135],v[56:59]
rt v[204:205],v240 offset:8448
rt v[206:207],v240 offset:9472
mm v[88:91],v[212:215],v[144:147],v[88:91]
ar v235,a254
v_perm_b32 v236, v235, v234, s86
mm v[88:91],v[208:211],v[152:155],v[88:91]
ld a[52:55],v247,s[44:47],s54 offen offset:2048 sc1
mm v[120:123],v[208:211],v[168:171],v[120:123]
v_perm_b32 v237, v235, v234, s87
aw a211,v236
mm v[120:123],v[212:215],v[156:159],v[120:123]
rt v[208:209],v240 offset:16896
rt v[210:211],v240 offset:17920
mm v[124:127],v[220:223],v[156:159],v[124:127]
aw a215,v237
ar v230,a227
mm v[124:127],v[216:219],v[168:171],v[124:127]
ld a[56:59],v246,s[44:47],s54 offen offset:3072 sc1
.La5_LW7_4:
s_cbranch_execz .La5_LA_RETURN
mm v[92:95],v[216:219],v[152:155],v[92:95]
ar v231,a231
v_perm_b32 v232, v231, v230, s86
mm v[92:95],v[220:223],v[144:147],v[92:95]
rt v[212:213],v240 offset:25344
rt v[214:215],v240 offset:26368
mm v[60:63],v[220:223],v[132:135],v[60:63]
v_perm_b32 v233, v231, v230, s87
aw a216,v232
mm v[60:63],v[216:219],v[140:143],v[60:63]
aw a220,v233
ar v234,a235
mm v[28:31],v[216:219],v[136:139],v[28:31]
wt vmcnt(15) lgkmcnt(4)
ar v235,a239
mm v[28:31],v[220:223],v[128:131],v[28:31]
rt v[216:217],v240 offset:33792
rt v[218:219],v240 offset:34816
mm v[0:3],v[192:195],a[64:67],v[0:3]
ld a[60:63],v247,s[44:47],s54 offen offset:3072 sc1
mm v[0:3],v[196:199],a[72:75],v[0:3]
rt v[220:221],v240 offset:42240
rt v[222:223],v240 offset:43264
mm v[32:35],v[196:199],a[76:79],v[32:35]
v_perm_b32 v236, v235, v234, s86
v_perm_b32 v237, v235, v234, s87
mm v[32:35],v[192:195],a[68:71],v[32:35]
rt v[196:197],v241 offset:50688
rt v[198:199],v241 offset:51712
mm v[32:35],v[200:203],a[84:87],v[32:35]
ld v[128:131],v246,s[44:47],s55 offen offset:0 sc1
mm v[32:35],v[204:207],a[92:95],v[32:35]
rt v[192:193],v241 offset:59136
rt v[194:195],v241 offset:60160
mm v[0:3],v[204:207],a[88:91],v[0:3]
wt vmcnt(9) lgkmcnt(4)
aw a217,v236
mm v[0:3],v[200:203],a[80:83],v[0:3]
rt v[204:205],v241 offset:0
rt v[206:207],v241 offset:1024
mm v[0:3],v[208:211],a[0:3],v[0:3]
ld v[132:135],v247,s[44:47],s55 offen offset:0 sc1
mm v[0:3],v[212:215],a[8:11],v[0:3]
rt v[200:201],v241 offset:8448
rt v[202:203],v241 offset:9472
mm v[32:35],v[212:215],a[12:15],v[32:35]
aw a221,v237
ar v230,a243
mm v[32:35],v[208:211],a[4:7],v[32:35]
rt v[212:213],v241 offset:16896
rt v[214:215],v241 offset:17920
mm v[32:35],v[216:219],a[20:23],v[32:35]
ld v[144:147],v246,s[44:47],s55 offen offset:1024 sc1
mm v[32:35],v[220:223],a[28:31],v[32:35]
rt v[208:209],v241 offset:25344
rt v[210:211],v241 offset:26368
mm v[0:3],v[220:223],a[24:27],v[0:3]
wt lgkmcnt(4)
ar v231,a247
mm v[0:3],v[216:219],a[16:19],v[0:3]
rt v[220:221],v241 offset:33792
rt v[222:223],v241 offset:34816
mm v[4:7],v[196:199],a[64:67],v[4:7]
ld v[148:151],v247,s[44:47],s55 offen offset:1024 sc1
mm v[4:7],v[192:195],a[72:75],v[4:7]
rt v[216:217],v241 offset:42240
rt v[218:219],v241 offset:43264
mm v[36:39],v[192:195],a[76:79],v[36:39]
v_perm_b32 v232, v231, v230, s86
v_perm_b32 v233, v231, v230, s87
mm v[36:39],v[196:199],a[68:71],v[36:39]
rt v[192:193],v242 offset:50688
rt v[194:195],v242 offset:51712
mm v[36:39],v[204:207],a[84:87],v[36:39]
ld v[160:163],v246,s[44:47],s55 offen offset:2048 sc1
mm v[36:39],v[200:203],a[92:95],v[36:39]
rt v[196:197],v242 offset:59136
rt v[198:199],v242 offset:60160
aw a218,v232
aw a222,v233
ar v234,a251
ar v235,a255
v_perm_b32 v236, v235, v234, s86
v_perm_b32 v237, v235, v234, s87
aw a219,v236
aw a223,v237
mm v[4:7],v[200:203],a[88:91],v[4:7]
wt lgkmcnt(4)
mm v[4:7],v[204:207],a[80:83],v[4:7]
rt v[200:201],v242 offset:0
rt v[202:203],v242 offset:1024
mm v[4:7],v[212:215],a[0:3],v[4:7]
ld v[164:167],v247,s[44:47],s55 offen offset:2048 sc1
mm v[4:7],v[208:211],a[8:11],v[4:7]
rt v[204:205],v242 offset:8448
rt v[206:207],v242 offset:9472
mm v[36:39],v[208:211],a[12:15],v[36:39]
mm v[36:39],v[212:215],a[4:7],v[36:39]
rt v[208:209],v242 offset:16896
rt v[210:211],v242 offset:17920
mm v[36:39],v[220:223],a[20:23],v[36:39]
mm v[36:39],v[216:219],a[28:31],v[36:39]
rt v[212:213],v242 offset:25344
rt v[214:215],v242 offset:26368
mm v[4:7],v[216:219],a[24:27],v[4:7]
wt lgkmcnt(4)
mm v[4:7],v[220:223],a[16:19],v[4:7]
rt v[216:217],v242 offset:33792
rt v[218:219],v242 offset:34816
mm v[8:11],v[192:195],a[64:67],v[8:11]
ld v[176:179],v246,s[44:47],s55 offen offset:3072 sc1
mm v[8:11],v[196:199],a[72:75],v[8:11]
rt v[220:221],v242 offset:42240
rt v[222:223],v242 offset:43264
mm v[40:43],v[196:199],a[76:79],v[40:43]
cv v234,v32,v33
cv v236,v36,v37
mm v[40:43],v[192:195],a[68:71],v[40:43]
rt v[196:197],v243 offset:50688
rt v[198:199],v243 offset:51712
mm v[40:43],v[200:203],a[84:87],v[40:43]
ld v[180:183],v247,s[44:47],s55 offen offset:3072 sc1
mm v[40:43],v[204:207],a[92:95],v[40:43]
rt v[192:193],v243 offset:59136
rt v[194:195],v243 offset:60160
mm v[8:11],v[204:207],a[88:91],v[8:11]
wt lgkmcnt(4)
v_bfe_u32 v250, v255, 6, 1
mm v[8:11],v[200:203],a[80:83],v[8:11]
rt v[204:205],v243 offset:0
rt v[206:207],v243 offset:1024
mm v[8:11],v[208:211],a[0:3],v[8:11]
cv v235,v34,v35
cv v237,v38,v39
mm v[8:11],v[212:215],a[8:11],v[8:11]
rt v[200:201],v243 offset:8448
rt v[202:203],v243 offset:9472
mm v[40:43],v[212:215],a[12:15],v[40:43]
v_permlane16_swap_b32 v234, v236
v_permlane16_swap_b32 v235, v237
mm v[40:43],v[208:211],a[4:7],v[40:43]
rt v[212:213],v243 offset:16896
rt v[214:215],v243 offset:17920
mm v[40:43],v[216:219],a[20:23],v[40:43]
st v[234:237],v238,s[32:35],s95 offen offset:0 sc1
mm v[40:43],v[220:223],a[28:31],v[40:43]
rt v[208:209],v243 offset:25344
rt v[210:211],v243 offset:26368
mm v[8:11],v[220:223],a[24:27],v[8:11]
wt lgkmcnt(4)
v_mul_u32_u24 v250, 7, v250
mm v[8:11],v[216:219],a[16:19],v[8:11]
rt v[220:221],v243 offset:33792
rt v[222:223],v243 offset:34816
mm v[12:15],v[196:199],a[64:67],v[12:15]
cv v230,v0,v1
cv v232,v4,v5
mm v[12:15],v[192:195],a[72:75],v[12:15]
rt v[216:217],v243 offset:42240
rt v[218:219],v243 offset:43264
mm v[44:47],v[192:195],a[76:79],v[44:47]
cv v231,v2,v3
cv v233,v6,v7
mm v[44:47],v[196:199],a[68:71],v[44:47]
rt v[192:193],v240 offset:50816
rt v[194:195],v240 offset:51840
mm v[44:47],v[204:207],a[84:87],v[44:47]
v_permlane16_swap_b32 v230, v232
v_permlane16_swap_b32 v231, v233
mm v[44:47],v[200:203],a[92:95],v[44:47]
rt v[196:197],v240 offset:59264
rt v[198:199],v240 offset:60288
mm v[12:15],v[200:203],a[88:91],v[12:15]
wt lgkmcnt(4)
v_xor_b32 v250, s3, v250
mm v[12:15],v[204:207],a[80:83],v[12:15]
rt v[200:201],v240 offset:128
rt v[202:203],v240 offset:1152
mm v[12:15],v[212:215],a[0:3],v[12:15]
st v[230:233],v238,s[32:35],s94 offen offset:0 sc1
mm v[12:15],v[208:211],a[8:11],v[12:15]
rt v[204:205],v240 offset:8576
rt v[206:207],v240 offset:9600
mm v[44:47],v[208:211],a[12:15],v[44:47]
v_bfe_u32 v251, v255, 4, 2
v_lshl_add_u32 v250, v250, 2, v251
mm v[44:47],v[212:215],a[4:7],v[44:47]
rt v[208:209],v240 offset:17024
rt v[210:211],v240 offset:18048
mm v[44:47],v[220:223],a[20:23],v[44:47]
ld v[136:139],v224,s[68:71],0 offen offset:0
mm v[44:47],v[216:219],a[28:31],v[44:47]
rt v[212:213],v240 offset:25472
rt v[214:215],v240 offset:26496
mm v[12:15],v[216:219],a[24:27],v[12:15]
wt lgkmcnt(4)
v_lshlrev_b32 v250, 9, v250
mm v[12:15],v[220:223],a[16:19],v[12:15]
rt v[216:217],v240 offset:33920
rt v[218:219],v240 offset:34944
mm v[16:19],v[192:195],a[64:67],v[16:19]
cv v234,v40,v41
cv v236,v44,v45
mm v[16:19],v[196:199],a[72:75],v[16:19]
rt v[220:221],v240 offset:42368
rt v[222:223],v240 offset:43392
mm v[48:51],v[196:199],a[76:79],v[48:51]
cv v235,v42,v43
cv v237,v46,v47
mm v[48:51],v[192:195],a[68:71],v[48:51]
rt v[196:197],v241 offset:50816
rt v[198:199],v241 offset:51840
mm v[48:51],v[200:203],a[84:87],v[48:51]
v_permlane16_swap_b32 v234, v236
v_permlane16_swap_b32 v235, v237
mm v[48:51],v[204:207],a[92:95],v[48:51]
rt v[192:193],v241 offset:59264
rt v[194:195],v241 offset:60288
mm v[16:19],v[204:207],a[88:91],v[16:19]
wt lgkmcnt(4)
v_and_b32 v251, 64, v255
mm v[16:19],v[200:203],a[80:83],v[16:19]
rt v[204:205],v241 offset:128
rt v[206:207],v241 offset:1152
mm v[16:19],v[208:211],a[0:3],v[16:19]
st v[234:237],v238,s[32:35],s95 offen offset:1024 sc1
mm v[16:19],v[212:215],a[8:11],v[16:19]
rt v[200:201],v241 offset:8576
rt v[202:203],v241 offset:9600
mm v[48:51],v[212:215],a[12:15],v[48:51]
cv v230,v8,v9
cv v232,v12,v13
mm v[48:51],v[208:211],a[4:7],v[48:51]
rt v[212:213],v241 offset:17024
rt v[214:215],v241 offset:18048
mm v[48:51],v[216:219],a[20:23],v[48:51]
cv v231,v10,v11
cv v233,v14,v15
mm v[48:51],v[220:223],a[28:31],v[48:51]
rt v[208:209],v241 offset:25472
rt v[210:211],v241 offset:26496
mm v[16:19],v[220:223],a[24:27],v[16:19]
wt lgkmcnt(4)
v_lshl_add_u32 v251, v251, 24, v250
mm v[16:19],v[216:219],a[16:19],v[16:19]
rt v[220:221],v241 offset:33920
rt v[222:223],v241 offset:34944
mm v[20:23],v[196:199],a[64:67],v[20:23]
v_permlane16_swap_b32 v230, v232
v_permlane16_swap_b32 v231, v233
mm v[20:23],v[192:195],a[72:75],v[20:23]
rt v[216:217],v241 offset:42368
rt v[218:219],v241 offset:43392
mm v[52:55],v[192:195],a[76:79],v[52:55]
v_and_b32 v248, 0x1f0, v255
v_lshrrev_b32 v248, 1, v248
mm v[52:55],v[196:199],a[68:71],v[52:55]
rt v[192:193],v242 offset:50816
rt v[194:195],v242 offset:51840
mm v[52:55],v[204:207],a[84:87],v[52:55]
st v[230:233],v238,s[32:35],s94 offen offset:1024 sc1
mm v[52:55],v[200:203],a[92:95],v[52:55]
rt v[196:197],v242 offset:59264
rt v[198:199],v242 offset:60288
mm v[20:23],v[200:203],a[88:91],v[20:23]
wt lgkmcnt(4)
mm v[20:23],v[204:207],a[80:83],v[20:23]
rt v[200:201],v242 offset:128
rt v[202:203],v242 offset:1152
mm v[20:23],v[212:215],a[0:3],v[20:23]
ld v[140:143],v225,s[48:51],0 offen offset:0
mm v[20:23],v[208:211],a[8:11],v[20:23]
rt v[204:205],v242 offset:8576
rt v[206:207],v242 offset:9600
mm v[52:55],v[208:211],a[12:15],v[52:55]
mm v[52:55],v[212:215],a[4:7],v[52:55]
rt v[208:209],v242 offset:17024
rt v[210:211],v242 offset:18048
mm v[52:55],v[220:223],a[20:23],v[52:55]
ld v[152:155],v224,s[68:71],0 offen offset:512
mm v[52:55],v[216:219],a[28:31],v[52:55]
rt v[212:213],v242 offset:25472
rt v[214:215],v242 offset:26496
mm v[20:23],v[216:219],a[24:27],v[20:23]
wt lgkmcnt(4)
s_mov_b32 s93, 0
ld a[224:227],v229,s[28:31],s93 offen offset:0
mm v[20:23],v[220:223],a[16:19],v[20:23]
rt v[216:217],v242 offset:33920
rt v[218:219],v242 offset:34944
mm v[24:27],v[192:195],a[64:67],v[24:27]
cv v234,v48,v49
cv v236,v52,v53
mm v[24:27],v[196:199],a[72:75],v[24:27]
rt v[220:221],v242 offset:42368
rt v[222:223],v242 offset:43392
mm v[56:59],v[196:199],a[76:79],v[56:59]
cv v235,v50,v51
cv v237,v54,v55
mm v[56:59],v[192:195],a[68:71],v[56:59]
rt v[196:197],v243 offset:50816
rt v[198:199],v243 offset:51840
mm v[56:59],v[200:203],a[84:87],v[56:59]
v_permlane16_swap_b32 v234, v236
v_permlane16_swap_b32 v235, v237
mm v[56:59],v[204:207],a[92:95],v[56:59]
rt v[192:193],v243 offset:59264
rt v[194:195],v243 offset:60288
mm v[24:27],v[204:207],a[88:91],v[24:27]
wt lgkmcnt(4)
ld a[228:231],v229,s[28:31],s93 offen offset:2048
mm v[24:27],v[200:203],a[80:83],v[24:27]
rt v[204:205],v243 offset:128
rt v[206:207],v243 offset:1152
mm v[24:27],v[208:211],a[0:3],v[24:27]
st v[234:237],v238,s[32:35],s95 offen offset:2048 sc1
mm v[24:27],v[212:215],a[8:11],v[24:27]
rt v[200:201],v243 offset:8576
rt v[202:203],v243 offset:9600
mm v[56:59],v[212:215],a[12:15],v[56:59]
cv v230,v16,v17
cv v232,v20,v21
mm v[56:59],v[208:211],a[4:7],v[56:59]
rt v[212:213],v243 offset:17024
rt v[214:215],v243 offset:18048
mm v[56:59],v[216:219],a[20:23],v[56:59]
cv v231,v18,v19
cv v233,v22,v23
mm v[56:59],v[220:223],a[28:31],v[56:59]
rt v[208:209],v243 offset:25472
rt v[210:211],v243 offset:26496
mm v[24:27],v[220:223],a[24:27],v[24:27]
wt lgkmcnt(4)
s_mov_b32 s93, 4096
ld a[232:235],v229,s[28:31],s93 offen offset:0
mm v[24:27],v[216:219],a[16:19],v[24:27]
rt v[220:221],v243 offset:33920
rt v[222:223],v243 offset:34944
mm v[28:31],v[196:199],a[64:67],v[28:31]
v_permlane16_swap_b32 v230, v232
v_permlane16_swap_b32 v231, v233
mm v[28:31],v[192:195],a[72:75],v[28:31]
rt v[216:217],v243 offset:42368
rt v[218:219],v243 offset:43392
mm v[60:63],v[192:195],a[76:79],v[60:63]
ld a[236:239],v229,s[28:31],s93 offen offset:2048
mm v[60:63],v[196:199],a[68:71],v[60:63]
rt v[192:193],v240 offset:50688
rt v[194:195],v240 offset:51712
mm v[60:63],v[204:207],a[84:87],v[60:63]
st v[230:233],v238,s[32:35],s94 offen offset:2048 sc1
mm v[60:63],v[200:203],a[92:95],v[60:63]
rt v[196:197],v240 offset:59136
rt v[198:199],v240 offset:60160
mm v[28:31],v[200:203],a[88:91],v[28:31]
wt lgkmcnt(4)
s_mov_b32 s93, 8192
ld a[240:243],v229,s[28:31],s93 offen offset:0
mm v[28:31],v[204:207],a[80:83],v[28:31]
rt v[200:201],v240 offset:0
rt v[202:203],v240 offset:1024
mm v[28:31],v[212:215],a[0:3],v[28:31]
ld v[156:159],v225,s[48:51],0 offen offset:512
mm v[28:31],v[208:211],a[8:11],v[28:31]
rt v[204:205],v240 offset:8448
rt v[206:207],v240 offset:9472
mm v[60:63],v[208:211],a[12:15],v[60:63]
ld a[244:247],v229,s[28:31],s93 offen offset:2048
mm v[60:63],v[212:215],a[4:7],v[60:63]
rt v[208:209],v240 offset:16896
rt v[210:211],v240 offset:17920
mm v[60:63],v[220:223],a[20:23],v[60:63]
ld v[168:171],v224,s[68:71],0 offen offset:1024
mm v[60:63],v[216:219],a[28:31],v[60:63]
rt v[212:213],v240 offset:25344
rt v[214:215],v240 offset:26368
mm v[28:31],v[216:219],a[24:27],v[28:31]
wt vmcnt(25) lgkmcnt(4)
s_mov_b32 s93, 12288
ld a[248:251],v229,s[28:31],s93 offen offset:0
mm v[28:31],v[220:223],a[16:19],v[28:31]
rt v[216:217],v240 offset:33792
rt v[218:219],v240 offset:34816
mm v[64:67],v[192:195],a[32:35],v[64:67]
cv v234,v56,v57
cv v236,v60,v61
mm v[64:67],v[196:199],a[40:43],v[64:67]
rt v[220:221],v240 offset:42240
rt v[222:223],v240 offset:43264
mm v[96:99],v[196:199],a[44:47],v[96:99]
cv v235,v58,v59
cv v237,v62,v63
mm v[96:99],v[192:195],a[36:39],v[96:99]
rt v[196:197],v241 offset:50688
rt v[198:199],v241 offset:51712
mm v[96:99],v[200:203],a[52:55],v[96:99]
v_permlane16_swap_b32 v234, v236
v_permlane16_swap_b32 v235, v237
mm v[96:99],v[204:207],a[60:63],v[96:99]
rt v[192:193],v241 offset:59136
rt v[194:195],v241 offset:60160
mm v[64:67],v[204:207],a[56:59],v[64:67]
wt vmcnt(18) lgkmcnt(4)
ld a[252:255],v229,s[28:31],s93 offen offset:2048
mm v[64:67],v[200:203],a[48:51],v[64:67]
rt v[204:205],v241 offset:0
rt v[206:207],v241 offset:1024
mm v[64:67],v[208:211],v[128:131],v[64:67]
st v[234:237],v238,s[32:35],s95 offen offset:3072 sc1
mm v[64:67],v[212:215],v[144:147],v[64:67]
rt v[200:201],v241 offset:8448
rt v[202:203],v241 offset:9472
mm v[96:99],v[212:215],v[148:151],v[96:99]
cv v230,v24,v25
cv v232,v28,v29
mm v[96:99],v[208:211],v[132:135],v[96:99]
rt v[212:213],v241 offset:16896
rt v[214:215],v241 offset:17920
mm v[96:99],v[216:219],v[164:167],v[96:99]
cv v231,v26,v27
cv v233,v30,v31
mm v[96:99],v[220:223],v[180:183],v[96:99]
rt v[208:209],v241 offset:25344
rt v[210:211],v241 offset:26368
mm v[64:67],v[220:223],v[176:179],v[64:67]
wt lgkmcnt(4)
s_mov_b32 s93, 0
ld a[64:67],v229,s[28:31],s93 offen offset:256
mm v[64:67],v[216:219],v[160:163],v[64:67]
rt v[220:221],v241 offset:33792
rt v[222:223],v241 offset:34816
mm v[68:71],v[196:199],a[32:35],v[68:71]
v_permlane16_swap_b32 v230, v232
v_permlane16_swap_b32 v231, v233
mm v[68:71],v[192:195],a[40:43],v[68:71]
rt v[216:217],v241 offset:42240
rt v[218:219],v241 offset:43264
mm v[100:103],v[192:195],a[44:47],v[100:103]
ld a[68:71],v229,s[28:31],s93 offen offset:2304
mm v[100:103],v[196:199],a[36:39],v[100:103]
rt v[192:193],v242 offset:50688
rt v[194:195],v242 offset:51712
mm v[100:103],v[204:207],a[52:55],v[100:103]
st v[230:233],v238,s[32:35],s94 offen offset:3072 sc1
mm v[100:103],v[200:203],a[60:63],v[100:103]
rt v[196:197],v242 offset:59136
rt v[198:199],v242 offset:60160
mm v[68:71],v[200:203],a[56:59],v[68:71]
wt lgkmcnt(4)
s_mov_b32 s93, 4096
ld a[72:75],v229,s[28:31],s93 offen offset:256
mm v[68:71],v[204:207],a[48:51],v[68:71]
rt v[200:201],v242 offset:0
rt v[202:203],v242 offset:1024
mm v[68:71],v[212:215],v[128:131],v[68:71]
ld v[172:175],v225,s[48:51],0 offen offset:1024
mm v[68:71],v[208:211],v[144:147],v[68:71]
rt v[204:205],v242 offset:8448
rt v[206:207],v242 offset:9472
mm v[100:103],v[208:211],v[148:151],v[100:103]
ld a[76:79],v229,s[28:31],s93 offen offset:2304
mm v[100:103],v[212:215],v[132:135],v[100:103]
rt v[208:209],v242 offset:16896
rt v[210:211],v242 offset:17920
mm v[100:103],v[220:223],v[164:167],v[100:103]
ld v[184:187],v224,s[68:71],0 offen offset:1536
mm v[100:103],v[216:219],v[180:183],v[100:103]
rt v[212:213],v242 offset:25344
rt v[214:215],v242 offset:26368
mm v[68:71],v[216:219],v[176:179],v[68:71]
wt lgkmcnt(4)
s_mov_b32 s93, 8192
ld a[80:83],v229,s[28:31],s93 offen offset:256
mm v[68:71],v[220:223],v[160:163],v[68:71]
rt v[216:217],v242 offset:33792
rt v[218:219],v242 offset:34816
mm v[72:75],v[192:195],a[32:35],v[72:75]
cv v234,v96,v97
cv v236,v100,v101
mm v[72:75],v[196:199],a[40:43],v[72:75]
rt v[220:221],v242 offset:42240
rt v[222:223],v242 offset:43264
mm v[104:107],v[196:199],a[44:47],v[104:107]
cv v235,v98,v99
cv v237,v102,v103
mm v[104:107],v[192:195],a[36:39],v[104:107]
rt v[196:197],v243 offset:50688
rt v[198:199],v243 offset:51712
mm v[104:107],v[200:203],a[52:55],v[104:107]
v_permlane16_swap_b32 v234, v236
v_permlane16_swap_b32 v235, v237
mm v[104:107],v[204:207],a[60:63],v[104:107]
rt v[192:193],v243 offset:59136
rt v[194:195],v243 offset:60160
mm v[72:75],v[204:207],a[56:59],v[72:75]
wt lgkmcnt(4)
ld a[84:87],v229,s[28:31],s93 offen offset:2304
mm v[72:75],v[200:203],a[48:51],v[72:75]
rt v[204:205],v243 offset:0
rt v[206:207],v243 offset:1024
mm v[72:75],v[208:211],v[128:131],v[72:75]
st v[234:237],v238,s[32:35],s97 offen offset:0 sc1
mm v[72:75],v[212:215],v[144:147],v[72:75]
rt v[200:201],v243 offset:8448
rt v[202:203],v243 offset:9472
mm v[104:107],v[212:215],v[148:151],v[104:107]
cv v230,v64,v65
cv v232,v68,v69
mm v[104:107],v[208:211],v[132:135],v[104:107]
rt v[212:213],v243 offset:16896
rt v[214:215],v243 offset:17920
mm v[104:107],v[216:219],v[164:167],v[104:107]
cv v231,v66,v67
cv v233,v70,v71
mm v[104:107],v[220:223],v[180:183],v[104:107]
rt v[208:209],v243 offset:25344
rt v[210:211],v243 offset:26368
mm v[72:75],v[220:223],v[176:179],v[72:75]
wt lgkmcnt(4)
s_mov_b32 s93, 12288
ld a[88:91],v229,s[28:31],s93 offen offset:256
mm v[72:75],v[216:219],v[160:163],v[72:75]
rt v[220:221],v243 offset:33792
rt v[222:223],v243 offset:34816
mm v[76:79],v[196:199],a[32:35],v[76:79]
v_permlane16_swap_b32 v230, v232
v_permlane16_swap_b32 v231, v233
mm v[76:79],v[192:195],a[40:43],v[76:79]
rt v[216:217],v243 offset:42240
rt v[218:219],v243 offset:43264
mm v[108:111],v[192:195],a[44:47],v[108:111]
ld a[92:95],v229,s[28:31],s93 offen offset:2304
mm v[108:111],v[196:199],a[36:39],v[108:111]
rt v[192:193],v240 offset:50816
rt v[194:195],v240 offset:51840
mm v[108:111],v[204:207],a[52:55],v[108:111]
.La5_LW7_5:
s_cbranch_execz .La5_LA_RETURN
st v[230:233],v238,s[32:35],s96 offen offset:0 sc1
mm v[108:111],v[200:203],a[60:63],v[108:111]
rt v[196:197],v240 offset:59264
rt v[198:199],v240 offset:60288
mm v[76:79],v[200:203],a[56:59],v[76:79]
wt lgkmcnt(4)
mm v[76:79],v[204:207],a[48:51],v[76:79]
rt v[200:201],v240 offset:128
rt v[202:203],v240 offset:1152
mm v[76:79],v[212:215],v[128:131],v[76:79]
ld v[188:191],v225,s[48:51],0 offen offset:1536
mm v[76:79],v[208:211],v[144:147],v[76:79]
rt v[204:205],v240 offset:8576
rt v[206:207],v240 offset:9600
mm v[108:111],v[208:211],v[148:151],v[108:111]
mm v[108:111],v[212:215],v[132:135],v[108:111]
rt v[208:209],v240 offset:17024
rt v[210:211],v240 offset:18048
mm v[108:111],v[220:223],v[164:167],v[108:111]
mm v[108:111],v[216:219],v[180:183],v[108:111]
rt v[212:213],v240 offset:25472
rt v[214:215],v240 offset:26496
mm v[76:79],v[216:219],v[176:179],v[76:79]
wt lgkmcnt(4)
mm v[76:79],v[220:223],v[160:163],v[76:79]
rt v[216:217],v240 offset:33920
rt v[218:219],v240 offset:34944
mm v[80:83],v[192:195],a[32:35],v[80:83]
cv v234,v104,v105
cv v236,v108,v109
mm v[80:83],v[196:199],a[40:43],v[80:83]
rt v[220:221],v240 offset:42368
rt v[222:223],v240 offset:43392
mm v[112:115],v[196:199],a[44:47],v[112:115]
cv v235,v106,v107
cv v237,v110,v111
mm v[112:115],v[192:195],a[36:39],v[112:115]
rt v[196:197],v241 offset:50816
rt v[198:199],v241 offset:51840
mm v[112:115],v[200:203],a[52:55],v[112:115]
v_permlane16_swap_b32 v234, v236
v_permlane16_swap_b32 v235, v237
mm v[112:115],v[204:207],a[60:63],v[112:115]
rt v[192:193],v241 offset:59264
rt v[194:195],v241 offset:60288
mm v[80:83],v[204:207],a[56:59],v[80:83]
wt lgkmcnt(4)
mm v[80:83],v[200:203],a[48:51],v[80:83]
rt v[204:205],v241 offset:128
rt v[206:207],v241 offset:1152
mm v[80:83],v[208:211],v[128:131],v[80:83]
st v[234:237],v238,s[32:35],s97 offen offset:1024 sc1
mm v[80:83],v[212:215],v[144:147],v[80:83]
rt v[200:201],v241 offset:8576
rt v[202:203],v241 offset:9600
mm v[112:115],v[212:215],v[148:151],v[112:115]
cv v230,v72,v73
cv v232,v76,v77
mm v[112:115],v[208:211],v[132:135],v[112:115]
rt v[212:213],v241 offset:17024
rt v[214:215],v241 offset:18048
mm v[112:115],v[216:219],v[164:167],v[112:115]
cv v231,v74,v75
cv v233,v78,v79
mm v[112:115],v[220:223],v[180:183],v[112:115]
rt v[208:209],v241 offset:25472
rt v[210:211],v241 offset:26496
mm v[80:83],v[220:223],v[176:179],v[80:83]
wt lgkmcnt(4)
mm v[80:83],v[216:219],v[160:163],v[80:83]
rt v[220:221],v241 offset:33920
rt v[222:223],v241 offset:34944
mm v[84:87],v[196:199],a[32:35],v[84:87]
v_permlane16_swap_b32 v230, v232
v_permlane16_swap_b32 v231, v233
mm v[84:87],v[192:195],a[40:43],v[84:87]
rt v[216:217],v241 offset:42368
rt v[218:219],v241 offset:43392
mm v[116:119],v[192:195],a[44:47],v[116:119]
mm v[116:119],v[196:199],a[36:39],v[116:119]
rt v[192:193],v242 offset:50816
rt v[194:195],v242 offset:51840
mm v[116:119],v[204:207],a[52:55],v[116:119]
st v[230:233],v238,s[32:35],s96 offen offset:1024 sc1
mm v[116:119],v[200:203],a[60:63],v[116:119]
rt v[196:197],v242 offset:59264
rt v[198:199],v242 offset:60288
mm v[84:87],v[200:203],a[56:59],v[84:87]
wt lgkmcnt(4)
mm v[84:87],v[204:207],a[48:51],v[84:87]
rt v[200:201],v242 offset:128
rt v[202:203],v242 offset:1152
mm v[84:87],v[212:215],v[128:131],v[84:87]
mm v[84:87],v[208:211],v[144:147],v[84:87]
rt v[204:205],v242 offset:8576
rt v[206:207],v242 offset:9600
mm v[116:119],v[208:211],v[148:151],v[116:119]
mm v[116:119],v[212:215],v[132:135],v[116:119]
rt v[208:209],v242 offset:17024
rt v[210:211],v242 offset:18048
mm v[116:119],v[220:223],v[164:167],v[116:119]
mm v[116:119],v[216:219],v[180:183],v[116:119]
rt v[212:213],v242 offset:25472
rt v[214:215],v242 offset:26496
mm v[84:87],v[216:219],v[176:179],v[84:87]
wt lgkmcnt(4)
mm v[84:87],v[220:223],v[160:163],v[84:87]
rt v[216:217],v242 offset:33920
rt v[218:219],v242 offset:34944
mm v[88:91],v[192:195],a[32:35],v[88:91]
cv v234,v112,v113
cv v236,v116,v117
mm v[88:91],v[196:199],a[40:43],v[88:91]
rt v[220:221],v242 offset:42368
rt v[222:223],v242 offset:43392
mm v[120:123],v[196:199],a[44:47],v[120:123]
cv v235,v114,v115
cv v237,v118,v119
mm v[120:123],v[192:195],a[36:39],v[120:123]
rt v[196:197],v243 offset:50816
rt v[198:199],v243 offset:51840
mm v[120:123],v[200:203],a[52:55],v[120:123]
v_permlane16_swap_b32 v234, v236
v_permlane16_swap_b32 v235, v237
mm v[120:123],v[204:207],a[60:63],v[120:123]
rt v[192:193],v243 offset:59264
rt v[194:195],v243 offset:60288
mm v[88:91],v[204:207],a[56:59],v[88:91]
wt lgkmcnt(4)
mm v[88:91],v[200:203],a[48:51],v[88:91]
rt v[204:205],v243 offset:128
rt v[206:207],v243 offset:1152
mm v[88:91],v[208:211],v[128:131],v[88:91]
st v[234:237],v238,s[32:35],s97 offen offset:2048 sc1
mm v[88:91],v[212:215],v[144:147],v[88:91]
rt v[200:201],v243 offset:8576
rt v[202:203],v243 offset:9600
mm v[120:123],v[212:215],v[148:151],v[120:123]
cv v230,v80,v81
cv v232,v84,v85
mm v[120:123],v[208:211],v[132:135],v[120:123]
rt v[212:213],v243 offset:17024
rt v[214:215],v243 offset:18048
mm v[120:123],v[216:219],v[164:167],v[120:123]
cv v231,v82,v83
cv v233,v86,v87
wt vmcnt(14)
buffer_store_dwordx2 v[252:253], v251, s[64:67], s92 offen sc1
mm v[120:123],v[220:223],v[180:183],v[120:123]
rt v[208:209],v243 offset:25472
rt v[210:211],v243 offset:26496
mm v[88:91],v[220:223],v[176:179],v[88:91]
wt lgkmcnt(4)
mm v[88:91],v[216:219],v[160:163],v[88:91]
rt v[220:221],v243 offset:33920
rt v[222:223],v243 offset:34944
mm v[92:95],v[196:199],a[32:35],v[92:95]
v_permlane16_swap_b32 v230, v232
v_permlane16_swap_b32 v231, v233
mm v[92:95],v[192:195],a[40:43],v[92:95]
rt v[216:217],v243 offset:42368
rt v[218:219],v243 offset:43392
mm v[124:127],v[192:195],a[44:47],v[124:127]
mm v[124:127],v[196:199],a[36:39],v[124:127]
mm v[124:127],v[204:207],a[52:55],v[124:127]
st v[230:233],v238,s[32:35],s96 offen offset:2048 sc1
mm v[124:127],v[200:203],a[60:63],v[124:127]
mm v[92:95],v[200:203],a[56:59],v[92:95]
wt lgkmcnt(0)
mm v[92:95],v[204:207],a[48:51],v[92:95]
mm v[92:95],v[212:215],v[128:131],v[92:95]
mm v[92:95],v[208:211],v[144:147],v[92:95]
mm v[124:127],v[208:211],v[148:151],v[124:127]
mm v[124:127],v[212:215],v[132:135],v[124:127]
mm v[124:127],v[220:223],v[164:167],v[124:127]
mm v[124:127],v[216:219],v[180:183],v[124:127]
mm v[92:95],v[216:219],v[176:179],v[92:95]
mm v[92:95],v[220:223],v[160:163],v[92:95]
s_nop 7
cv v234,v120,v121
cv v236,v124,v125
cv v235,v122,v123
cv v237,v126,v127
cv v230,v88,v89
cv v232,v92,v93
cv v231,v90,v91
cv v233,v94,v95
v_permlane16_swap_b32 v234, v236
v_permlane16_swap_b32 v235, v237
st v[234:237],v238,s[32:35],s97 offen offset:3072 sc1
v_permlane16_swap_b32 v230, v232
v_permlane16_swap_b32 v231, v233
st v[230:233],v238,s[32:35],s96 offen offset:3072 sc1
.La5_LT_0:
.La5_LW3_5:
s_cbranch_execz .La5_LA_RETURN
.La5_LW7_6:
s_cbranch_execz .La5_LA_RETURN
s_lshl_b32 s91, s76, 5
s_add_u32 s91, s91, s90
s_lshl_b32 s20, s91, 9
s_add_u32 s20, s20, 2097152
s_lshr_b32 s93, s91, 1
s_lshl_b32 s93, s93, 15
s_and_b32 s94, s91, 1
s_lshl2_add_u32 s94, s94, s3
s_lshl_b32 s94, s94, 7
s_add_u32 s97, s93, s94
s_add_u32 s32, s26, 71303168
s_addc_u32 s33, s27, 0
s_and_b32 s33, s33, 0xffff
s_mov_b32 s34, 16777216
s_mov_b32 s35, 0x20000
s_add_u32 s44, s26, 171966464
s_addc_u32 s45, s27, 0
s_and_b32 s45, s45, 0xffff
s_mov_b32 s46, 950272
s_mov_b32 s47, 0x20000
s_add_u32 s56, s38, 0
s_addc_u32 s57, s39, 0
s_and_b32 s57, s57, 0xffff
s_mov_b32 s58, 4194304
s_mov_b32 s59, 0x20000
s_add_u32 s68, s16, 0
s_addc_u32 s69, s17, 0
s_and_b32 s69, s69, 0xffff
s_mov_b32 s70, 4194304
s_mov_b32 s71, 0x20000
v_and_b32 v230, 0x70, v255
v_lshrrev_b32 v231, 7, v255
v_lshl_add_u32 v230, v231, 10, v230
v_add_u32 v240, s97, v230
v_xor_b32 v241, 8192, v240
v_xor_b32 v242, 16384, v240
v_xor_b32 v243, 24576, v240
v_mov_b32 v237, 0x3f803f80
v_mov_b32 v236, 0
wt vmcnt(7)
v_dot2_f32_bf16 v236, v140, v237, v236
v_dot2_f32_bf16 v236, v141, v237, v236
v_dot2_f32_bf16 v236, v142, v237, v236
v_dot2_f32_bf16 v236, v143, v237, v236
v_dot2_f32_bf16 v236, v156, v237, v236
v_dot2_f32_bf16 v236, v157, v237, v236
v_dot2_f32_bf16 v236, v158, v237, v236
v_dot2_f32_bf16 v236, v159, v237, v236
v_dot2_f32_bf16 v236, v172, v237, v236
v_dot2_f32_bf16 v236, v173, v237, v236
v_dot2_f32_bf16 v236, v174, v237, v236
v_dot2_f32_bf16 v236, v175, v237, v236
v_dot2_f32_bf16 v236, v188, v237, v236
v_dot2_f32_bf16 v236, v189, v237, v236
v_dot2_f32_bf16 v236, v190, v237, v236
v_dot2_f32_bf16 v236, v191, v237, v236
v_lshlrev_b32 v0, 16, v136
v_and_b32 v1, 0xffff0000, v136
v_lshlrev_b32 v2, 16, v137
v_and_b32 v3, 0xffff0000, v137
v_lshlrev_b32 v4, 16, v138
v_and_b32 v5, 0xffff0000, v138
v_lshlrev_b32 v6, 16, v139
v_and_b32 v7, 0xffff0000, v139
v_lshlrev_b32 v8, 16, v152
v_and_b32 v9, 0xffff0000, v152
v_lshlrev_b32 v10, 16, v153
v_and_b32 v11, 0xffff0000, v153
v_lshlrev_b32 v12, 16, v154
v_and_b32 v13, 0xffff0000, v154
v_lshlrev_b32 v14, 16, v155
v_and_b32 v15, 0xffff0000, v155
v_lshlrev_b32 v16, 16, v168
v_and_b32 v17, 0xffff0000, v168
v_lshlrev_b32 v18, 16, v169
v_and_b32 v19, 0xffff0000, v169
v_lshlrev_b32 v20, 16, v170
v_and_b32 v21, 0xffff0000, v170
v_lshlrev_b32 v22, 16, v171
v_and_b32 v23, 0xffff0000, v171
v_lshlrev_b32 v24, 16, v184
v_and_b32 v25, 0xffff0000, v184
v_lshlrev_b32 v26, 16, v185
v_and_b32 v27, 0xffff0000, v185
v_lshlrev_b32 v28, 16, v186
v_and_b32 v29, 0xffff0000, v186
v_lshlrev_b32 v30, 16, v187
v_and_b32 v31, 0xffff0000, v187
s_nop 1
v_add_f32_dpp v236, v236, v236 quad_perm:[1,0,3,2] row_mask:0xf bank_mask:0xf
s_nop 1
v_add_f32_dpp v236, v236, v236 quad_perm:[2,3,0,1] row_mask:0xf bank_mask:0xf
s_nop 1
v_add_f32_dpp v236, v236, v236 row_ror:8 row_mask:0xf bank_mask:0xf
v_mov_b32 v230, v236
s_nop 1
v_permlane16_swap_b32 v230, v236
s_nop 1
v_add_f32 v236, v236, v230
v_mov_b32 v230, v236
s_nop 1
v_permlane32_swap_b32 v230, v236
s_nop 1
v_add_f32 v236, v236, v230
s_mov_b64 exec, 0
s_cmp_eq_u32 s3, 0
s_cbranch_scc1 .La5_LW4_0
s_cmp_eq_u32 s3, 1
s_cbranch_scc1 .La5_LW4_1
s_cmp_eq_u32 s3, 2
s_cbranch_scc1 .La5_LW4_2
s_branch .La5_LW4_3
.La5_LW4_R0:
s_mov_b64 exec, -1
wt vmcnt(0)
buffer_store_dwordx2 v[252:253], v250, s[64:67], s92 offen sc1
s_mov_b32 s85, 99
s_cmp_eq_u32 s30, 0
s_cbranch_scc1 .La5_LT_ROWS
; ---- h = x + attn, LayerNorm statistics, router score, two best of eight rows
.La5_LT_ROWS:
s_mov_b32 s96, 0
.La5_LT_P3:
buffer_load_dwordx2 v[250:251], v248, s[64:67], s20 offen sc1
wt vmcnt(0)
.La5_LT_P2:
v_cmp_eq_u64 vcc, v[250:251], v[252:253]
s_add_u32 s96, s96, 1
s_lshr_b32 s92, vcc_lo, 1
s_and_b32 s92, s92, vcc_lo
s_lshr_b32 s93, s92, 2
s_and_b32 s92, s92, s93
s_lshr_b32 s93, s92, 4
s_and_b32 s92, s92, s93
s_lshr_b32 s93, s96, 20
s_mul_i32 s93, s93, 0x01010101
s_or_b32 s92, s92, s93
s_andn2_b32 s92, 0x01010101, s92
s_bcnt1_i32_b32 s93, s92
s_cmp_gt_u32 s93, 1
s_cbranch_scc1 .La5_LT_P3S
s_or_b32 s93, s92, 0x01000000
s_ff1_i32_b32 s93, s93
s_lshl_b32 s80, s93, 19
s_xor_b32 s93, s80, 8388608
s_xor_b32 s94, s80, 12582912
s_xor_b32 s95, s80, 4194304
ld v[208:211],v240,s[32:35],s93 offen sc1
ld v[212:215],v241,s[32:35],s93 offen sc1
ld v[216:219],v242,s[32:35],s93 offen sc1
ld v[220:223],v243,s[32:35],s93 offen sc1
ld v[112:115],v240,s[32:35],s94 offen sc1
ld v[116:119],v241,s[32:35],s94 offen sc1
ld v[120:123],v242,s[32:35],s94 offen sc1
ld v[124:127],v243,s[32:35],s94 offen sc1
ld v[96:99],v240,s[32:35],s95 offen sc1
ld v[100:103],v241,s[32:35],s95 offen sc1
ld v[104:107],v242,s[32:35],s95 offen sc1
ld v[108:111],v243,s[32:35],s95 offen sc1
s_cmp_lg_u32 s92, 0
s_nop 0
s_nop 0
s_cbranch_scc1 .La5_LT_P4
ld v[192:195],v240,s[32:35],s80 offen sc1
ld v[196:199],v241,s[32:35],s80 offen sc1
ld v[200:203],v242,s[32:35],s80 offen sc1
ld v[204:207],v243,s[32:35],s80 offen sc1
.La5_LT_P4:
s_lshr_b32 s93, s80, 16
v_and_b32 v248, 56, v248
s_add_u32 s20, s20, s93
buffer_load_dwordx2 v[250:251], v248, s[64:67], s20 offen sc1
wt vmcnt(8)
v_lshlrev_b32 v32, 16, v208
v_and_b32 v33, 0xffff0000, v208
v_lshlrev_b32 v230, 16, v112
v_and_b32 v231, 0xffff0000, v112
v_pk_add_f32 v[32:33], v[32:33], v[230:231]
v_lshlrev_b32 v34, 16, v209
v_and_b32 v35, 0xffff0000, v209
v_lshlrev_b32 v230, 16, v113
v_and_b32 v231, 0xffff0000, v113
v_pk_add_f32 v[34:35], v[34:35], v[230:231]
v_lshlrev_b32 v36, 16, v210
v_and_b32 v37, 0xffff0000, v210
v_lshlrev_b32 v230, 16, v114
v_and_b32 v231, 0xffff0000, v114
v_pk_add_f32 v[36:37], v[36:37], v[230:231]
v_lshlrev_b32 v38, 16, v211
v_and_b32 v39, 0xffff0000, v211
v_lshlrev_b32 v230, 16, v115
v_and_b32 v231, 0xffff0000, v115
v_pk_add_f32 v[38:39], v[38:39], v[230:231]
wt vmcnt(7)
v_lshlrev_b32 v40, 16, v212
v_and_b32 v41, 0xffff0000, v212
v_lshlrev_b32 v230, 16, v116
v_and_b32 v231, 0xffff0000, v116
v_pk_add_f32 v[40:41], v[40:41], v[230:231]
v_lshlrev_b32 v42, 16, v213
v_and_b32 v43, 0xffff0000, v213
v_lshlrev_b32 v230, 16, v117
v_and_b32 v231, 0xffff0000, v117
v_pk_add_f32 v[42:43], v[42:43], v[230:231]
v_lshlrev_b32 v44, 16, v214
v_and_b32 v45, 0xffff0000, v214
v_lshlrev_b32 v230, 16, v118
v_and_b32 v231, 0xffff0000, v118
v_pk_add_f32 v[44:45], v[44:45], v[230:231]
v_lshlrev_b32 v46, 16, v215
v_and_b32 v47, 0xffff0000, v215
v_lshlrev_b32 v230, 16, v119
v_and_b32 v231, 0xffff0000, v119
v_pk_add_f32 v[46:47], v[46:47], v[230:231]
wt vmcnt(6)
v_lshlrev_b32 v48, 16, v216
v_and_b32 v49, 0xffff0000, v216
v_lshlrev_b32 v230, 16, v120
v_and_b32 v231, 0xffff0000, v120
v_pk_add_f32 v[48:49], v[48:49], v[230:231]
v_lshlrev_b32 v50, 16, v217
v_and_b32 v51, 0xffff0000, v217
v_lshlrev_b32 v230, 16, v121
v_and_b32 v231, 0xffff0000, v121
v_pk_add_f32 v[50:51], v[50:51], v[230:231]
v_lshlrev_b32 v52, 16, v218
v_and_b32 v53, 0xffff0000, v218
v_lshlrev_b32 v230, 16, v122
v_and_b32 v231, 0xffff0000, v122
v_pk_add_f32 v[52:53], v[52:53], v[230:231]
v_lshlrev_b32 v54, 16, v219
v_and_b32 v55, 0xffff0000, v219
v_lshlrev_b32 v230, 16, v123
v_and_b32 v231, 0xffff0000, v123
v_pk_add_f32 v[54:55], v[54:55], v[230:231]
wt vmcnt(5)
v_lshlrev_b32 v56, 16, v220
v_and_b32 v57, 0xffff0000, v220
v_lshlrev_b32 v230, 16, v124
v_and_b32 v231, 0xffff0000, v124
v_pk_add_f32 v[56:57], v[56:57], v[230:231]
v_lshlrev_b32 v58, 16, v221
v_and_b32 v59, 0xffff0000, v221
v_lshlrev_b32 v230, 16, v125
v_and_b32 v231, 0xffff0000, v125
v_pk_add_f32 v[58:59], v[58:59], v[230:231]
v_lshlrev_b32 v60, 16, v222
v_and_b32 v61, 0xffff0000, v222
v_lshlrev_b32 v230, 16, v126
v_and_b32 v231, 0xffff0000, v126
v_pk_add_f32 v[60:61], v[60:61], v[230:231]
v_lshlrev_b32 v62, 16, v223
v_and_b32 v63, 0xffff0000, v223
v_lshlrev_b32 v230, 16, v127
v_and_b32 v231, 0xffff0000, v127
v_pk_add_f32 v[62:63], v[62:63], v[230:231]
.La5_LT_PH:
wt vmcnt(1)
v_lshlrev_b32 v64, 16, v96
v_and_b32 v65, 0xffff0000, v96
v_lshlrev_b32 v66, 16, v97
v_and_b32 v67, 0xffff0000, v97
v_lshlrev_b32 v68, 16, v98
v_and_b32 v69, 0xffff0000, v98
v_lshlrev_b32 v70, 16, v99
v_and_b32 v71, 0xffff0000, v99
v_lshlrev_b32 v72, 16, v100
v_and_b32 v73, 0xffff0000, v100
v_lshlrev_b32 v74, 16, v101
v_and_b32 v75, 0xffff0000, v101
v_lshlrev_b32 v76, 16, v102
v_and_b32 v77, 0xffff0000, v102
v_lshlrev_b32 v78, 16, v103
v_and_b32 v79, 0xffff0000, v103
v_lshlrev_b32 v80, 16, v104
v_and_b32 v81, 0xffff0000, v104
v_lshlrev_b32 v82, 16, v105
v_and_b32 v83, 0xffff0000, v105
v_lshlrev_b32 v84, 16, v106
v_and_b32 v85, 0xffff0000, v106
v_lshlrev_b32 v86, 16, v107
v_and_b32 v87, 0xffff0000, v107
v_lshlrev_b32 v88, 16, v108
v_and_b32 v89, 0xffff0000, v108
v_lshlrev_b32 v90, 16, v109
v_and_b32 v91, 0xffff0000, v109
v_lshlrev_b32 v92, 16, v110
v_and_b32 v93, 0xffff0000, v110
v_lshlrev_b32 v94, 16, v111
v_and_b32 v95, 0xffff0000, v111
v_mov_b32 v232, 0
v_mov_b32 v233, 0
v_mov_b32_e64 v234, 0
s_cmp_eq_u32 s92, 0
s_cbranch_scc1 .La5_LT_SUM
.La5_LT_P5:
wt vmcnt(0)
v_cmp_eq_u64 vcc, v[250:251], v[252:253]
s_add_u32 s96, s96, 1
s_cmp_eq_u64 vcc, -1
s_cbranch_scc1 .La5_LT_P6
s_lshr_b32 s93, s96, 20
s_nop 0
s_cbranch_scc1 .La5_LT_P6
s_sleep 3
buffer_load_dwordx2 v[250:251], v248, s[64:67], s20 offen sc1
s_branch .La5_LT_P5
.La5_LT_P3S:
s_sleep 4
s_bcnt0_i32_b32 s93, vcc_lo
s_cmp_le_u32 s93, 8
s_cbranch_scc1 .La5_LT_P3
s_sleep 24
s_branch .La5_LT_P3
.La5_LT_P6:
ld v[192:195],v240,s[32:35],s80 offen sc1
ld v[196:199],v241,s[32:35],s80 offen sc1
ld v[200:203],v242,s[32:35],s80 offen sc1
ld v[204:207],v243,s[32:35],s80 offen sc1
.La5_LT_SUM:
wt vmcnt(3)
v_lshlrev_b32 v208, 16, v192
v_and_b32 v209, 0xffff0000, v192
v_lshlrev_b32 v210, 16, v193
v_and_b32 v211, 0xffff0000, v193
v_lshlrev_b32 v212, 16, v194
v_and_b32 v213, 0xffff0000, v194
v_lshlrev_b32 v214, 16, v195
v_and_b32 v215, 0xffff0000, v195
v_pk_add_f32 v[208:209], v[208:209], v[64:65]
v_pk_add_f32 v[210:211], v[210:211], v[66:67]
v_pk_add_f32 v[212:213], v[212:213], v[68:69]
v_pk_add_f32 v[214:215], v[214:215], v[70:71]
v_pk_add_f32 v[208:209], v[208:209], v[32:33]
v_pk_add_f32 v[210:211], v[210:211], v[34:35]
v_pk_add_f32 v[212:213], v[212:213], v[36:37]
v_pk_add_f32 v[214:215], v[214:215], v[38:39]
v_pk_add_f32 v[208:209], v[208:209], v[0:1]
v_pk_add_f32 v[210:211], v[210:211], v[2:3]
v_pk_add_f32 v[212:213], v[212:213], v[4:5]
v_pk_add_f32 v[214:215], v[214:215], v[6:7]
cv v96,v208,v209
cv v97,v210,v211
cv v98,v212,v213
cv v99,v214,v215
v_dot2_f32_bf16 v232, v96, v237, v232
v_dot2_f32_bf16 v233, v96, v96, v233
v_dot2_f32_bf16 v234, v96, v140, v234
v_dot2_f32_bf16 v232, v97, v237, v232
v_dot2_f32_bf16 v233, v97, v97, v233
v_dot2_f32_bf16 v234, v97, v141, v234
v_dot2_f32_bf16 v232, v98, v237, v232
v_dot2_f32_bf16 v233, v98, v98, v233
v_dot2_f32_bf16 v234, v98, v142, v234
v_dot2_f32_bf16 v232, v99, v237, v232
v_dot2_f32_bf16 v233, v99, v99, v233
v_dot2_f32_bf16 v234, v99, v143, v234
wt vmcnt(2)
v_lshlrev_b32 v208, 16, v196
v_and_b32 v209, 0xffff0000, v196
v_lshlrev_b32 v210, 16, v197
v_and_b32 v211, 0xffff0000, v197
v_lshlrev_b32 v212, 16, v198
v_and_b32 v213, 0xffff0000, v198
v_lshlrev_b32 v214, 16, v199
v_and_b32 v215, 0xffff0000, v199
v_pk_add_f32 v[208:209], v[208:209], v[72:73]
v_pk_add_f32 v[210:211], v[210:211], v[74:75]
v_pk_add_f32 v[212:213], v[212:213], v[76:77]
v_pk_add_f32 v[214:215], v[214:215], v[78:79]
v_pk_add_f32 v[208:209], v[208:209], v[40:41]
v_pk_add_f32 v[210:211], v[210:211], v[42:43]
v_pk_add_f32 v[212:213], v[212:213], v[44:45]
v_pk_add_f32 v[214:215], v[214:215], v[46:47]
v_pk_add_f32 v[208:209], v[208:209], v[8:9]
v_pk_add_f32 v[210:211], v[210:211], v[10:11]
v_pk_add_f32 v[212:213], v[212:213], v[12:13]
v_pk_add_f32 v[214:215], v[214:215], v[14:15]
cv v100,v208,v209
cv v101,v210,v211
cv v102,v212,v213
cv v103,v214,v215
v_dot2_f32_bf16 v232, v100, v237, v232
v_dot2_f32_bf16 v233, v100, v100, v233
v_dot2_f32_bf16 v234, v100, v156, v234
v_dot2_f32_bf16 v232, v101, v237, v232
v_dot2_f32_bf16 v233, v101, v101, v233
v_dot2_f32_bf16 v234, v101, v157, v234
v_dot2_f32_bf16 v232, v102, v237, v232
v_dot2_f32_bf16 v233, v102, v102, v233
v_dot2_f32_bf16 v234, v102, v158, v234
v_dot2_f32_bf16 v232, v103, v237, v232
v_dot2_f32_bf16 v233, v103, v103, v233
v_dot2_f32_bf16 v234, v103, v159, v234
wt vmcnt(1)
v_lshlrev_b32 v208, 16, v200
v_and_b32 v209, 0xffff0000, v200
v_lshlrev_b32 v210, 16, v201
v_and_b32 v211, 0xffff0000, v201
v_lshlrev_b32 v212, 16, v202
v_and_b32 v213, 0xffff0000, v202
v_lshlrev_b32 v214, 16, v203
v_and_b32 v215, 0xffff0000, v203
v_pk_add_f32 v[208:209], v[208:209], v[80:81]
v_pk_add_f32 v[210:211], v[210:211], v[82:83]
v_pk_add_f32 v[212:213], v[212:213], v[84:85]
v_pk_add_f32 v[214:215], v[214:215], v[86:87]
v_pk_add_f32 v[208:209], v[208:209], v[48:49]
v_pk_add_f32 v[210:211], v[210:211], v[50:51]
v_pk_add_f32 v[212:213], v[212:213], v[52:53]
v_pk_add_f32 v[214:215], v[214:215], v[54:55]
v_pk_add_f32 v[208:209], v[208:209], v[16:17]
v_pk_add_f32 v[210:211], v[210:211], v[18:19]
v_pk_add_f32 v[212:213], v[212:213], v[20:21]
v_pk_add_f32 v[214:215], v[214:215], v[22:23]
cv v104,v208,v209
cv v105,v210,v211
cv v106,v212,v213
cv v107,v214,v215
v_dot2_f32_bf16 v232, v104, v237, v232
v_dot2_f32_bf16 v233, v104, v104, v233
v_dot2_f32_bf16 v234, v104, v172, v234
v_dot2_f32_bf16 v232, v105, v237, v232
v_dot2_f32_bf16 v233, v105, v105, v233
v_dot2_f32_bf16 v234, v105, v173, v234
v_dot2_f32_bf16 v232, v106, v237, v232
.La5_LW4_0:
s_cbranch_execz .La5_LW4_4
v_dot2_f32_bf16 v233, v106, v106, v233
v_dot2_f32_bf16 v234, v106, v174, v234
v_dot2_f32_bf16 v232, v107, v237, v232
v_dot2_f32_bf16 v233, v107, v107, v233
v_dot2_f32_bf16 v234, v107, v175, v234
wt vmcnt(0)
v_lshlrev_b32 v208, 16, v204
v_and_b32 v209, 0xffff0000, v204
v_lshlrev_b32 v210, 16, v205
v_and_b32 v211, 0xffff0000, v205
v_lshlrev_b32 v212, 16, v206
v_and_b32 v213, 0xffff0000, v206
v_lshlrev_b32 v214, 16, v207
v_and_b32 v215, 0xffff0000, v207
v_pk_add_f32 v[208:209], v[208:209], v[88:89]
v_pk_add_f32 v[210:211], v[210:211], v[90:91]
v_pk_add_f32 v[212:213], v[212:213], v[92:93]
v_pk_add_f32 v[214:215], v[214:215], v[94:95]
v_pk_add_f32 v[208:209], v[208:209], v[56:57]
v_pk_add_f32 v[210:211], v[210:211], v[58:59]
v_pk_add_f32 v[212:213], v[212:213], v[60:61]
v_pk_add_f32 v[214:215], v[214:215], v[62:63]
v_pk_add_f32 v[208:209], v[208:209], v[24:25]
v_pk_add_f32 v[210:211], v[210:211], v[26:27]
v_pk_add_f32 v[212:213], v[212:213], v[28:29]
v_pk_add_f32 v[214:215], v[214:215], v[30:31]
cv v108,v208,v209
cv v109,v210,v211
cv v110,v212,v213
cv v111,v214,v215
v_dot2_f32_bf16 v232, v108, v237, v232
v_dot2_f32_bf16 v233, v108, v108, v233
v_dot2_f32_bf16 v234, v108, v188, v234
v_dot2_f32_bf16 v232, v109, v237, v232
v_dot2_f32_bf16 v233, v109, v109, v233
v_dot2_f32_bf16 v234, v109, v189, v234
v_dot2_f32_bf16 v232, v110, v237, v232
v_dot2_f32_bf16 v233, v110, v110, v233
v_dot2_f32_bf16 v234, v110, v190, v234
v_dot2_f32_bf16 v232, v111, v237, v232
v_dot2_f32_bf16 v233, v111, v111, v233
v_dot2_f32_bf16 v234, v111, v191, v234
wt vmcnt(0)
s_lshl2_add_u32 s92, s91, s3
s_lshl_b32 s92, s92, 1
s_sub_u32 s92, 0xffff, s92
v_add_f32_dpp v232, v232, v232 quad_perm:[1,0,3,2] row_mask:0xf bank_mask:0xf
v_add_f32_dpp v233, v233, v233 quad_perm:[1,0,3,2] row_mask:0xf bank_mask:0xf
v_add_f32_dpp v234, v234, v234 quad_perm:[1,0,3,2] row_mask:0xf bank_mask:0xf
v_add_f32_dpp v232, v232, v232 quad_perm:[2,3,0,1] row_mask:0xf bank_mask:0xf
v_add_f32_dpp v233, v233, v233 quad_perm:[2,3,0,1] row_mask:0xf bank_mask:0xf
v_add_f32_dpp v234, v234, v234 quad_perm:[2,3,0,1] row_mask:0xf bank_mask:0xf
v_add_f32_dpp v232, v232, v232 row_ror:8 row_mask:0xf bank_mask:0xf
v_add_f32_dpp v233, v233, v233 row_ror:8 row_mask:0xf bank_mask:0xf
v_add_f32_dpp v234, v234, v234 row_ror:8 row_mask:0xf bank_mask:0xf
v_mov_b32 v230, v232
v_mov_b32 v231, v233
v_mov_b32 v235, v234
v_permlane16_swap_b32 v230, v232
v_permlane16_swap_b32 v231, v233
v_permlane16_swap_b32 v235, v234
v_add_f32 v232, v232, v230
v_add_f32 v233, v233, v231
v_add_f32 v234, v234, v235
v_mov_b32 v230, v232
v_mov_b32 v231, v233
v_mov_b32 v235, v234
v_permlane32_swap_b32 v230, v232
v_permlane32_swap_b32 v231, v233
v_permlane32_swap_b32 v235, v234
v_add_f32 v232, v232, v230
v_add_f32 v233, v233, v231
v_add_f32 v234, v234, v235
v_mul_f32 v244, 0x3a800000, v232
v_mul_f32 v233, 0x3a800000, v233
v_fma_f32 v230, -v244, v244, v233
v_mul_f32 v233, 0x3a800000, v233
v_cmp_nge_f32 vcc, v230, v233
v_bfe_u32 v235, v255, 6, 1
v_sub_u32 v235, s92, v235
s_cbranch_vccnz .La5_LT_VAR
.La5_LT_VOK:
cv v244,v244,v244
v_lshlrev_b32 v244, 16, v244
cv v230,v230,v230
v_lshlrev_b32 v230, 16, v230
v_fma_f32 v234, -v244, v236, v234
v_add_f32 v230, 0x3727c5ac, v230
cv v230,v230,v230
v_lshlrev_b32 v230, 16, v230
v_sqrt_f32 v230, v230
v_lshrrev_b32 v232, 3, v255
cv v230,v230,v230
v_lshlrev_b32 v231, 16, v230
v_rcp_f32 v246, v231
v_lshrrev_b32 v230, 16, v230
v_or_b32 v233, v244, v230
v_mul_f32 v234, v234, v246
s_lshl_b32 s93, s3, 4
cv v234,v234,v234
v_and_b32 v230, 8, v232
v_ashrrev_i32 v232, 31, v234
v_or_b32 v232, 0x80000000, v232
v_xor_b32 v232, v232, v234
v_and_b32 v232, 0xffff0000, v232
v_add_u32 v230, s93, v230
v_or_b32 v232, v232, v235
ds_write_b64 v230, v[232:233] offset:0
v_lshrrev_b32 v230, 1, v255
v_and_b32_e64 v230, 56, v230
wt lgkmcnt(0)
s_barrier
s_cmp_lg_u32 s3, 0
s_cbranch_scc1 .La5_LT_OUT
ds_read_b64 v[232:233], v230 offset:0
s_lshl3_add_u32 s92, s90, s76
s_lshl_b32 s92, s92, 4
v_lshrrev_b32 v235, 7, v255
v_lshlrev_b32 v235, 12, v235
wt lgkmcnt(0)
v_mov_b32 v234, v232
s_nop 1
v_max_u32_dpp v234, v234, v234 quad_perm:[1,0,3,2] row_mask:0xf bank_mask:0xf
s_nop 1
v_max_u32_dpp v234, v234, v234 quad_perm:[2,3,0,1] row_mask:0xf bank_mask:0xf
s_nop 1
v_max_u32_dpp v234, v234, v234 row_half_mirror row_mask:0xf bank_mask:0xf
s_nop 1
v_cmp_eq_u32 vcc, v232, v234
v_add_u32 v231, 8, v235
s_nop 0
v_cndmask_b32 v234, v232, 0, vcc
v_cndmask_b32 v235, v231, v235, vcc
s_nop 0
v_max_u32_dpp v234, v234, v234 quad_perm:[1,0,3,2] row_mask:0xf bank_mask:0xf
s_nop 1
v_max_u32_dpp v234, v234, v234 quad_perm:[2,3,0,1] row_mask:0xf bank_mask:0xf
s_nop 1
v_max_u32_dpp v234, v234, v234 row_half_mirror row_mask:0xf bank_mask:0xf
s_nop 1
v_cmp_ge_u32 vcc, v232, v234
s_nop 1
s_mov_b64 exec, vcc
buffer_store_dwordx2 v[232:233], v235, s[44:47], s92 offen sc1
s_mov_b64 exec, -1
; ---- out = h + .La5_LN(h) of the own rows; FFN weights into MFMA order
.La5_LT_OUT:
v_mul_f32 v244, v244, v246
v_mov_b32 v247, v246
v_xor_b32 v244, 0x80000000, v244
v_mov_b32 v245, v244
v_lshlrev_b32 v230, 16, v96
v_and_b32 v231, 0xffff0000, v96
v_pk_fma_f32 v[232:233], v[230:231], v[246:247], v[244:245]
cv v232,v232,v233
v_lshlrev_b32 v234, 16, v232
v_and_b32 v235, 0xffff0000, v232
v_pk_add_f32 v[230:231], v[230:231], v[234:235]
cv v192,v230,v231
v_lshlrev_b32 v230, 16, v97
v_and_b32 v231, 0xffff0000, v97
v_pk_fma_f32 v[232:233], v[230:231], v[246:247], v[244:245]
cv v232,v232,v233
v_lshlrev_b32 v234, 16, v232
v_and_b32 v235, 0xffff0000, v232
v_pk_add_f32 v[230:231], v[230:231], v[234:235]
cv v193,v230,v231
v_lshlrev_b32 v230, 16, v98
v_and_b32 v231, 0xffff0000, v98
v_pk_fma_f32 v[232:233], v[230:231], v[246:247], v[244:245]
cv v232,v232,v233
v_lshlrev_b32 v234, 16, v232
v_and_b32 v235, 0xffff0000, v232
v_pk_add_f32 v[230:231], v[230:231], v[234:235]
cv v194,v230,v231
v_lshlrev_b32 v230, 16, v99
v_and_b32 v231, 0xffff0000, v99
v_pk_fma_f32 v[232:233], v[230:231], v[246:247], v[244:245]
cv v232,v232,v233
v_lshlrev_b32 v234, 16, v232
v_and_b32 v235, 0xffff0000, v232
v_pk_add_f32 v[230:231], v[230:231], v[234:235]
cv v195,v230,v231
v_lshlrev_b32 v230, 16, v100
v_and_b32 v231, 0xffff0000, v100
v_pk_fma_f32 v[232:233], v[230:231], v[246:247], v[244:245]
cv v232,v232,v233
v_lshlrev_b32 v234, 16, v232
v_and_b32 v235, 0xffff0000, v232
v_pk_add_f32 v[230:231], v[230:231], v[234:235]
cv v196,v230,v231
v_lshlrev_b32 v230, 16, v101
v_and_b32 v231, 0xffff0000, v101
v_pk_fma_f32 v[232:233], v[230:231], v[246:247], v[244:245]
cv v232,v232,v233
v_lshlrev_b32 v234, 16, v232
v_and_b32 v235, 0xffff0000, v232
v_pk_add_f32 v[230:231], v[230:231], v[234:235]
cv v197,v230,v231
v_lshlrev_b32 v230, 16, v102
v_and_b32 v231, 0xffff0000, v102
v_pk_fma_f32 v[232:233], v[230:231], v[246:247], v[244:245]
cv v232,v232,v233
v_lshlrev_b32 v234, 16, v232
v_and_b32 v235, 0xffff0000, v232
v_pk_add_f32 v[230:231], v[230:231], v[234:235]
cv v198,v230,v231
v_lshlrev_b32 v230, 16, v103
v_and_b32 v231, 0xffff0000, v103
v_pk_fma_f32 v[232:233], v[230:231], v[246:247], v[244:245]
cv v232,v232,v233
v_lshlrev_b32 v234, 16, v232
v_and_b32 v235, 0xffff0000, v232
v_pk_add_f32 v[230:231], v[230:231], v[234:235]
cv v199,v230,v231
v_lshlrev_b32 v230, 16, v104
v_and_b32 v231, 0xffff0000, v104
v_pk_fma_f32 v[232:233], v[230:231], v[246:247], v[244:245]
cv v232,v232,v233
v_lshlrev_b32 v234, 16, v232
v_and_b32 v235, 0xffff0000, v232
v_pk_add_f32 v[230:231], v[230:231], v[234:235]
cv v200,v230,v231
v_lshlrev_b32 v230, 16, v105
v_and_b32 v231, 0xffff0000, v105
v_pk_fma_f32 v[232:233], v[230:231], v[246:247], v[244:245]
cv v232,v232,v233
v_lshlrev_b32 v234, 16, v232
v_and_b32 v235, 0xffff0000, v232
v_pk_add_f32 v[230:231], v[230:231], v[234:235]
cv v201,v230,v231
v_lshlrev_b32 v230, 16, v106
v_and_b32 v231, 0xffff0000, v106
v_pk_fma_f32 v[232:233], v[230:231], v[246:247], v[244:245]
cv v232,v232,v233
v_lshlrev_b32 v234, 16, v232
v_and_b32 v235, 0xffff0000, v232
v_pk_add_f32 v[230:231], v[230:231], v[234:235]
cv v202,v230,v231
v_lshlrev_b32 v230, 16, v107
v_and_b32 v231, 0xffff0000, v107
v_pk_fma_f32 v[232:233], v[230:231], v[246:247], v[244:245]
cv v232,v232,v233
v_lshlrev_b32 v234, 16, v232
v_and_b32 v235, 0xffff0000, v232
v_pk_add_f32 v[230:231], v[230:231], v[234:235]
cv v203,v230,v231
v_lshlrev_b32 v230, 16, v108
v_and_b32 v231, 0xffff0000, v108
v_pk_fma_f32 v[232:233], v[230:231], v[246:247], v[244:245]
cv v232,v232,v233
v_lshlrev_b32 v234, 16, v232
v_and_b32 v235, 0xffff0000, v232
v_pk_add_f32 v[230:231], v[230:231], v[234:235]
cv v204,v230,v231
v_lshlrev_b32 v230, 16, v109
v_and_b32 v231, 0xffff0000, v109
v_pk_fma_f32 v[232:233], v[230:231], v[246:247], v[244:245]
cv v232,v232,v233
v_lshlrev_b32 v234, 16, v232
v_and_b32 v235, 0xffff0000, v232
v_pk_add_f32 v[230:231], v[230:231], v[234:235]
cv v205,v230,v231
v_lshlrev_b32 v230, 16, v110
v_and_b32 v231, 0xffff0000, v110
v_pk_fma_f32 v[232:233], v[230:231], v[246:247], v[244:245]
cv v232,v232,v233
v_lshlrev_b32 v234, 16, v232
v_and_b32 v235, 0xffff0000, v232
v_pk_add_f32 v[230:231], v[230:231], v[234:235]
cv v206,v230,v231
v_lshlrev_b32 v230, 16, v111
v_and_b32 v231, 0xffff0000, v111
v_pk_fma_f32 v[232:233], v[230:231], v[246:247], v[244:245]
cv v232,v232,v233
v_lshlrev_b32_e64 v234, 16, v232
v_and_b32 v235, 0xffff0000, v232
v_pk_add_f32 v[230:231], v[230:231], v[234:235]
cv v207,v230,v231
s_cmp_eq_u32 s30, 0
s_cbranch_scc1 .La5_LT_SEL
s_mov_b32 s94, 0x05040100
s_mov_b32 s95, 0x07060302
wt vmcnt(0)
ar v230,a224
ar v231,a228
v_perm_b32 v128, v231, v230, s94
v_perm_b32 v132, v231, v230, s95
ar v230,a232
ar v231,a236
v_perm_b32 v129, v231, v230, s94
v_perm_b32 v133, v231, v230, s95
ar v230,a240
ar v231,a244
v_perm_b32 v130, v231, v230, s94
v_perm_b32 v134, v231, v230, s95
ar v230,a248
ar v231,a252
v_perm_b32 v131, v231, v230, s94
v_perm_b32 v135, v231, v230, s95
ar v230,a225
ar v231,a229
v_perm_b32 v136, v231, v230, s94
v_perm_b32 v140, v231, v230, s95
ar v230,a233
ar v231,a237
v_perm_b32 v137, v231, v230, s94
v_perm_b32 v141, v231, v230, s95
ar v230,a241
ar v231,a245
v_perm_b32 v138, v231, v230, s94
v_perm_b32 v142, v231, v230, s95
ar v230,a249
ar v231,a253
v_perm_b32 v139, v231, v230, s94
v_perm_b32 v143, v231, v230, s95
ar v230,a226
ar v231,a230
v_perm_b32 v144, v231, v230, s94
v_perm_b32 v148, v231, v230, s95
ar v230,a234
ar v231,a238
v_perm_b32 v145, v231, v230, s94
v_perm_b32 v149, v231, v230, s95
ar v230,a242
ar v231,a246
v_perm_b32 v146, v231, v230, s94
v_perm_b32 v150, v231, v230, s95
ar v230,a250
ar v231,a254
v_perm_b32 v147, v231, v230, s94
v_perm_b32 v151, v231, v230, s95
ar v230,a227
ar v231,a231
v_perm_b32 v152, v231, v230, s94
v_perm_b32 v156, v231, v230, s95
ar v230,a235
ar v231,a239
v_perm_b32 v153, v231, v230, s94
v_perm_b32 v157, v231, v230, s95
ar v230,a243
ar v231,a247
v_perm_b32 v154, v231, v230, s94
v_perm_b32 v158, v231, v230, s95
ar v230,a251
ar v231,a255
v_perm_b32 v155, v231, v230, s94
v_perm_b32 v159, v231, v230, s95
ar v230,a64
ar v231,a68
v_perm_b32 v160, v231, v230, s94
v_perm_b32 v164, v231, v230, s95
ar v230,a72
ar v231,a76
v_perm_b32 v161, v231, v230, s94
v_perm_b32 v165, v231, v230, s95
ar v230,a80
ar v231,a84
v_perm_b32 v162, v231, v230, s94
v_perm_b32 v166, v231, v230, s95
ar v230,a88
ar v231,a92
v_perm_b32 v163, v231, v230, s94
v_perm_b32 v167, v231, v230, s95
ar v230,a65
ar v231,a69
v_perm_b32 v168, v231, v230, s94
v_perm_b32 v172, v231, v230, s95
ar v230,a73
ar v231,a77
v_perm_b32 v169, v231, v230, s94
v_perm_b32 v173, v231, v230, s95
ar v230,a81
ar v231,a85
v_perm_b32 v170, v231, v230, s94
v_perm_b32 v174, v231, v230, s95
ar v230,a89
ar v231,a93
v_perm_b32 v171, v231, v230, s94
v_perm_b32 v175, v231, v230, s95
ar v230,a66
ar v231,a70
v_perm_b32 v176, v231, v230, s94
v_perm_b32 v180, v231, v230, s95
ar v230,a74
ar v231,a78
v_perm_b32 v177, v231, v230, s94
v_perm_b32 v181, v231, v230, s95
ar v230,a82
ar v231,a86
v_perm_b32 v178, v231, v230, s94
v_perm_b32 v182, v231, v230, s95
ar v230,a90
ar v231,a94
v_perm_b32 v179, v231, v230, s94
v_perm_b32 v183, v231, v230, s95
ar v230,a67
ar v231,a71
v_perm_b32 v184, v231, v230, s94
v_perm_b32 v188, v231, v230, s95
ar v230,a75
ar v231,a79
v_perm_b32 v185, v231, v230, s94
v_perm_b32 v189, v231, v230, s95
ar v230,a83
ar v231,a87
v_perm_b32 v186, v231, v230, s94
v_perm_b32 v190, v231, v230, s95
ar v230,a91
ar v231,a95
v_perm_b32 v187, v231, v230, s94
v_perm_b32 v191, v231, v230, s95
s_and_b32 s92, s3, 1
s_lshr_b32 s93, s3, 1
v_lshrrev_b32 v230, 6, v255
v_and_b32 v231, 48, v255
v_lshl_add_u32 v240, v230, 10, v231
s_lshl_b32 s96, s92, 14
v_add_u32 v240, s96, v240
s_lshl_b32 s96, s92, 10
v_add_u32 v241, s96, v255
s_lshl_b32 s97, s3, 10
v_add_u32 v208, s97, v255
v_bfe_u32 v230, v255, 4, 1
v_bfe_u32 v231, v255, 6, 2
v_lshrrev_b32 v232, 8, v255
v_lshlrev_b32 v209, 11, v230
v_lshl_add_u32 v209, v231, 7, v209
v_lshl_add_u32 v209, v232, 5, v209
s_lshr_b32 s96, s96, 1
v_add_u32 v209, s96, v209
v_bfe_u32 v233, v255, 4, 2
v_mul_u32_u24 v210, 640, v233
v_lshl_add_u32 v210, v231, 3, v210
s_mul_i32 s96, s93, 2560
s_lshl_b32 s92, s92, 5
s_add_u32 s96, s96, s92
v_add_u32 v210, s96, v210
v_cmp_eq_u32 s[98:99], v231, v232
v_mul_u32_u24 v211, 5, v255
s_lshl_b32 s96, s3, 7
v_lshrrev_b32 v212, 3, v255
v_and_b32 v212, 62, v212
v_add_u32 v212, s96, v212
v_lshlrev_b32 v234, 6, v230
v_lshl_add_u32 v234, v232, 4, v234
v_add_u32 v234, s96, v234
v_mov_b32 v235, 1024
v_cmp_eq_u32 vcc, 0, v231
s_nop 1
v_cndmask_b32 v216, v235, v234, vcc
v_cmp_eq_u32 vcc, 1, v231
s_nop 1
v_cndmask_b32 v217, v235, v234, vcc
v_cmp_eq_u32 vcc, 2, v231
s_nop 1
v_cndmask_b32 v218, v235, v234, vcc
v_cmp_eq_u32 vcc, 3, v231
s_nop 1
v_cndmask_b32 v219, v235, v234, vcc
v_mov_b32 v220, 0
v_mov_b32 v221, 0
v_mov_b32 v222, 0
v_mov_b32 v223, 0
ds_write_b128 v235, v[220:223] offset:10240
s_lshl_b32 s92, s100, 13
s_add_u32 s80, s92, 32768
s_add_u32 s20, s80, 4096
; ---- top two rows of the sequence; SwiGLU slice of 32 hidden units
.La5_LT_SEL:
.La5_LW4_1:
s_cbranch_execz .La5_LW4_R0
s_and_b32 s97, s2, 7
s_lshl_b32 s97, s97, 12
s_cmp_lg_u32 s3, 0
s_cbranch_scc1 .La5_LT_S2
s_mov_b32 s96, 0
v_and_b32 v229, 255, v255
.La5_LT_SA:
s_sleep 2
ld v[224:227],v229,s[44:47],s97 offen offset:0 sc1
s_add_u32 s96, s96, 1
s_lshr_b32 s92, s96, 20
wt vmcnt(0)
v_cmp_ne_u32 vcc, 0, v224
s_cbranch_scc1 .La5_LT_S1
s_cmp_eq_u64 vcc, -1
s_nop 0
s_nop 0
s_cbranch_scc0 .La5_LT_SA
.La5_LT_S1:
s_cmp_lg_u32 s30, 0
s_cbranch_scc1 .La5_LT_S0
s_sleep 6
.La5_LT_S0:
ld v[224:227],v255,s[44:47],s97 offen offset:0 sc1
ld v[232:235],v255,s[44:47],s97 offen offset:1024 sc1
ld v[244:247],v255,s[44:47],s97 offen offset:2048 sc1
ld v[248:251],v255,s[44:47],s97 offen offset:3072 sc1
s_add_u32 s96, s96, 1
s_lshr_b32 s92, s96, 20
wt vmcnt(3)
v_min3_u32 v228, v224, v225, v226
v_min_u32 v228, v227, v228
wt vmcnt(2)
v_min3_u32 v228, v232, v233, v228
v_min3_u32 v228, v234, v235, v228
wt vmcnt(1)
v_min3_u32 v228, v244, v245, v228
v_min3_u32 v228, v246, v247, v228
wt vmcnt(0)
v_min3_u32 v228, v248, v249, v228
v_min3_u32 v228, v250, v251, v228
v_cmp_ne_u32 vcc, 0, v228
v_max_u32 v229, v224, v232
v_max_u32 v230, v244, v248
s_cbranch_scc1 .La5_LT_S3
s_cmp_eq_u64 vcc, -1
s_nop 0
s_nop 0
s_cbranch_scc0 .La5_LT_S1
.La5_LT_S3:
v_max_u32 v228, v229, v230
v_min_u32 v229, v229, v230
v_mov_b32 v231, v228
v_min_u32 v224, v224, v232
v_min_u32 v244, v244, v248
v_max3_u32 v229, v229, v224, v244
v_max3_u32 v230, v226, v234, v246
v_max3_u32 v229, v229, v230, v250
s_nop 1
v_max_u32_dpp v231, v231, v231 row_shr:1 row_mask:0xf bank_mask:0xf
s_nop 1
v_max_u32_dpp v231, v231, v231 row_shr:2 row_mask:0xf bank_mask:0xf
s_nop 1
v_max_u32_dpp v231, v231, v231 row_shr:4 row_mask:0xf bank_mask:0xf
s_nop 1
v_max_u32_dpp v231, v231, v231 row_shr:8 row_mask:0xf bank_mask:0xf
s_nop 1
v_max_u32_dpp v231, v231, v231 row_bcast:15 row_mask:0xa bank_mask:0xf
s_nop 1
v_max_u32_dpp v231, v231, v231 row_bcast:31 row_mask:0xc bank_mask:0xf
s_nop 0
v_readlane_b32 s82, v231, 63
s_nop 1
v_cmp_eq_u32 vcc, s82, v228
v_mov_b32 v230, s82
v_mov_b32 v224, 0
v_cndmask_b32 v228, v228, v229, vcc
s_nop 1
v_max_u32_dpp v228, v228, v228 row_shr:1 row_mask:0xf bank_mask:0xf
s_nop 1
v_max_u32_dpp v228, v228, v228 row_shr:2 row_mask:0xf bank_mask:0xf
s_nop 1
v_max_u32_dpp v228, v228, v228 row_shr:4 row_mask:0xf bank_mask:0xf
s_nop 1
v_max_u32_dpp v228, v228, v228 row_shr:8 row_mask:0xf bank_mask:0xf
s_nop 1
v_max_u32_dpp v228, v228, v228 row_bcast:15 row_mask:0xa bank_mask:0xf
s_nop 1
v_max_u32_dpp v228, v228, v228 row_bcast:31 row_mask:0xc bank_mask:0xf
s_nop 0
v_readlane_b32 s81, v228, 63
s_nop 1
v_mov_b32 v231, s81
ds_write_b64 v224, v[230:231] offset:64
.La5_LT_S2:
v_mov_b32 v224, 0
wt lgkmcnt(0)
s_barrier
ds_read_b64 v[228:229], v224 offset:64
wt lgkmcnt(0)
v_readfirstlane_b32 s82, v228
v_readfirstlane_b32 s81, v229
s_nop 0
s_andn2_b32 s84, 0x7ff, s82
s_andn2_b32 s21, 0x7ff, s81
s_cmp_eq_u32 s30, 0
s_cbranch_scc1 .La5_LT_ST
s_cmp_lt_u32 s3, 2
s_cselect_b32 s91, s84, s21
s_cselect_b32 s85, s82, s81
s_lshr_b32 s92, s91, 4
s_lshl_b32 s92, s92, 15
s_and_b32 s93, s91, 15
s_lshl_b32 s93, s93, 6
s_add_u32 s92, s92, s93
s_add_u32 s93, s92, 4194304
s_add_u32 s94, s92, 8388608
s_add_u32 s95, s92, 12582912
ld v[224:227],v240,s[32:35],s92 offen sc1
ld v[228:231],v240,s[32:35],s93 offen sc1
ld v[232:235],v240,s[32:35],s94 offen sc1
ld v[244:247],v240,s[32:35],s95 offen sc1
s_lshl_b32 s96, s91, 11
s_and_b32 s92, s91, 0xf8
s_lshl4_add_u32 s97, s92, s97
s_lshr_b32 s92, s91, 8
s_lshl4_add_u32 s97, s92, s97
ld v[248:251],v241,s[68:71],s96 offen
ld v[220:223],off,s[44:47],s97 sc1
s_lshl_b32 s86, s76, 5
s_add_u32 s86, s86, s90
s_lshl2_add_u32 s86, s86, s3
s_lshl_b32 s87, s86, 1
v_bfe_u32 v40, v255, 6, 1
v_add_u32 v41, s87, v40
v_cmp_eq_u32 vcc, s84, v41
v_cmp_eq_u32 s[94:95], s21, v41
v_lshrrev_b32 v42, 7, v255
v_and_b32 v43, 48, v255
v_lshl_add_u32 v42, v42, 6, v43
s_or_b64 vcc, vcc, s[94:95]
v_lshl_add_u32 v42, v40, 11, v42
v_mov_b32 v43, 1073741824
s_lshl_b32 s86, s86, 12
v_cndmask_b32 v42, v42, v43, vcc
wt vmcnt(4)
v_lshlrev_b32 v236, 16, v224
v_and_b32 v237, 0xffff0000, v224
v_lshlrev_b32 v238, 16, v228
v_and_b32 v239, 0xffff0000, v228
v_pk_add_f32 v[236:237], v[236:237], v[238:239]
v_lshlrev_b32 v242, 16, v225
v_and_b32 v243, 0xffff0000, v225
v_lshlrev_b32 v238, 16, v229
v_and_b32 v239, 0xffff0000, v229
v_pk_add_f32 v[242:243], v[242:243], v[238:239]
v_lshlrev_b32 v252, 16, v226
v_and_b32 v253, 0xffff0000, v226
v_lshlrev_b32 v238, 16, v230
v_and_b32 v239, 0xffff0000, v230
v_pk_add_f32 v[252:253], v[252:253], v[238:239]
v_lshlrev_b32 v214, 16, v227
v_and_b32 v215, 0xffff0000, v227
v_lshlrev_b32 v238, 16, v231
v_and_b32 v239, 0xffff0000, v231
v_pk_add_f32 v[214:215], v[214:215], v[238:239]
wt vmcnt(2)
v_lshlrev_b32 v224, 16, v232
v_and_b32 v225, 0xffff0000, v232
v_lshlrev_b32 v226, 16, v244
v_and_b32 v227, 0xffff0000, v244
v_pk_add_f32 v[224:225], v[224:225], v[226:227]
v_pk_add_f32 v[236:237], v[236:237], v[224:225]
v_lshlrev_b32 v224, 16, v233
v_and_b32 v225, 0xffff0000, v233
v_lshlrev_b32 v226, 16, v245
v_and_b32 v227, 0xffff0000, v245
v_pk_add_f32 v[224:225], v[224:225], v[226:227]
v_pk_add_f32 v[242:243], v[242:243], v[224:225]
.La5_LW4_2:
s_cbranch_execz .La5_LW4_R0
v_lshlrev_b32 v224, 16, v234
v_and_b32 v225, 0xffff0000, v234
v_lshlrev_b32 v226, 16, v246
v_and_b32 v227, 0xffff0000, v246
v_pk_add_f32 v[224:225], v[224:225], v[226:227]
v_pk_add_f32 v[252:253], v[252:253], v[224:225]
v_lshlrev_b32 v224, 16, v235
v_and_b32 v225, 0xffff0000, v235
v_lshlrev_b32 v226, 16, v247
v_and_b32 v227, 0xffff0000, v247
v_pk_add_f32 v[224:225], v[224:225], v[226:227]
v_pk_add_f32 v[214:215], v[214:215], v[224:225]
wt vmcnt(0)
v_cmp_eq_u32 vcc, s85, v220
s_nop 1
v_cndmask_b32 v220, v223, v221, vcc
v_lshlrev_b32 v222, 16, v220
v_rcp_f32 v222, v222
v_and_b32 v220, 0xffff0000, v220
v_mul_f32 v220, v220, v222
v_mov_b32 v223, v222
v_xor_b32 v220, 0x80000000, v220
v_mov_b32 v221, v220
v_lshlrev_b32 v224, 16, v248
v_and_b32 v225, 0xffff0000, v248
v_pk_add_f32 v[236:237], v[236:237], v[224:225]
cv v224,v236,v237
v_lshlrev_b32 v226, 16, v224
v_and_b32 v227, 0xffff0000, v224
v_pk_fma_f32 v[226:227], v[226:227], v[222:223], v[220:221]
cv v228,v226,v227
v_lshlrev_b32 v224, 16, v249
v_and_b32 v225, 0xffff0000, v249
v_pk_add_f32 v[242:243], v[242:243], v[224:225]
cv v224,v242,v243
v_lshlrev_b32 v226, 16, v224
v_and_b32 v227, 0xffff0000, v224
v_pk_fma_f32 v[226:227], v[226:227], v[222:223], v[220:221]
cv v229,v226,v227
v_lshlrev_b32 v224, 16, v250
v_and_b32 v225, 0xffff0000, v250
v_pk_add_f32 v[252:253], v[252:253], v[224:225]
cv v224,v252,v253
v_lshlrev_b32 v226, 16, v224
v_and_b32 v227, 0xffff0000, v224
v_pk_fma_f32 v[226:227], v[226:227], v[222:223], v[220:221]
cv v230,v226,v227
v_lshlrev_b32 v224, 16, v251
v_and_b32 v225, 0xffff0000, v251
v_pk_add_f32 v[214:215], v[214:215], v[224:225]
cv v224,v214,v215
v_lshlrev_b32 v226, 16, v224
v_and_b32 v227, 0xffff0000, v224
v_pk_fma_f32 v[226:227], v[226:227], v[222:223], v[220:221]
cv v231,v226,v227
ds_write_b128 v208, v[228:231] offset:1024
wt lgkmcnt(0)
s_barrier
rd v[224:227],v209 offset:1024
rd v[232:235],v209 offset:1040
rd v[244:247],v209 offset:2048
rd v[248:251],v209 offset:2064
wt lgkmcnt(3)
mm a[0:3],v[224:227],a[96:99],0
mm a[4:7],v[224:227],a[100:103],0
mm a[8:11],v[224:227],a[104:107],0
mm a[12:15],v[224:227],a[108:111],0
mm a[16:19],v[224:227],a[112:115],0
mm a[20:23],v[224:227],a[116:119],0
mm a[24:27],v[224:227],a[120:123],0
mm a[28:31],v[224:227],a[124:127],0
st v[192:195],v42,s[56:59],s86 offen offset:0 sc1
wt lgkmcnt(2)
mm a[0:3],v[232:235],a[128:131],a[0:3]
mm a[4:7],v[232:235],a[132:135],a[4:7]
mm a[8:11],v[232:235],a[136:139],a[8:11]
mm a[12:15],v[232:235],a[140:143],a[12:15]
mm a[16:19],v[232:235],a[144:147],a[16:19]
mm a[20:23],v[232:235],a[148:151],a[20:23]
mm a[24:27],v[232:235],a[152:155],a[24:27]
mm a[28:31],v[232:235],a[156:159],a[28:31]
st v[196:199],v42,s[56:59],s86 offen offset:512 sc1
wt lgkmcnt(1)
mm a[0:3],v[244:247],a[160:163],a[0:3]
mm a[4:7],v[244:247],a[164:167],a[4:7]
mm a[8:11],v[244:247],a[168:171],a[8:11]
mm a[12:15],v[244:247],a[172:175],a[12:15]
mm a[16:19],v[244:247],a[176:179],a[16:19]
mm a[20:23],v[244:247],a[180:183],a[20:23]
mm a[24:27],v[244:247],a[184:187],a[24:27]
mm a[28:31],v[244:247],a[188:191],a[28:31]
st v[200:203],v42,s[56:59],s86 offen offset:1024 sc1
wt lgkmcnt(0)
mm a[0:3],v[248:251],a[192:195],a[0:3]
mm a[4:7],v[248:251],a[196:199],a[4:7]
mm a[8:11],v[248:251],a[200:203],a[8:11]
mm a[12:15],v[248:251],a[204:207],a[12:15]
mm a[16:19],v[248:251],a[208:211],a[16:19]
mm a[20:23],v[248:251],a[212:215],a[20:23]
mm a[24:27],v[248:251],a[216:219],a[24:27]
mm a[28:31],v[248:251],a[220:223],a[28:31]
st v[204:207],v42,s[56:59],s86 offen offset:1536 sc1
s_mov_b64 exec, s[98:99]
ds_write_b64 v210, a[0:1] offset:5120
ds_write_b64 v210, a[4:5] offset:5200
ds_write_b64 v210, a[8:9] offset:5280
ds_write_b64 v210, a[12:13] offset:5360
ds_write_b64 v210, a[16:17] offset:5440
s_nop 3
ds_write_b64 v210, a[20:21] offset:5520
s_nop 3
ds_write_b64 v210, a[24:25] offset:5600
s_nop 3
ds_write_b64 v210, a[28:29] offset:5680
s_mov_b64 exec, -1
wt lgkmcnt(0)
s_barrier
rd v[224:227],v211 offset:5120
rd v[232:235],v211 offset:5136
rd v[244:247],v211 offset:5152
rd v[248:251],v211 offset:5168
wt lgkmcnt(2)
v_pk_add_f32 v[224:225], v[224:225], v[226:227]
v_pk_add_f32 v[232:233], v[232:233], v[234:235]
wt lgkmcnt(0)
v_pk_add_f32 v[244:245], v[244:245], v[246:247]
v_pk_add_f32 v[248:249], v[248:249], v[250:251]
v_pk_add_f32 v[224:225], v[224:225], v[232:233]
v_pk_add_f32 v[244:245], v[244:245], v[248:249]
v_pk_add_f32 v[224:225], v[224:225], v[244:245]
cv v224,v224,v225
v_mov_b32 v226, v224
s_nop 1
v_permlane32_swap_b32 v224, v226
s_nop 1
v_lshlrev_b32 v228, 16, v224
v_and_b32 v229, 0xffff0000, v224
v_lshlrev_b32 v232, 16, v226
v_and_b32 v233, 0xffff0000, v226
v_mul_f32 v230, 0xbfb8aa3b, v228
v_mul_f32 v231, 0xbfb8aa3b, v229
ex v230,v230
ex v231,v231
v_add_f32 v230, 1.0, v230
v_add_f32 v231, 1.0, v231
v_rcp_f32 v230, v230
v_rcp_f32 v231, v231
s_nop 0
v_pk_mul_f32 v[228:229], v[228:229], v[230:231]
cv v228,v228,v229
v_lshlrev_b32 v230, 16, v228
v_and_b32 v231, 0xffff0000, v228
v_pk_mul_f32 v[230:231], v[230:231], v[232:233]
cv v228,v230,v231
ds_write_b16 v212, v228 offset:10240
ds_write_b16_d16_hi v212, v228 offset:10304
wt lgkmcnt(0)
rd v[224:227],v216 offset:10240
rd v[232:235],v217 offset:10240
rd v[244:247],v218 offset:10240
rd v[248:251],v219 offset:10240
wt lgkmcnt(3)
mm v[0:3],v[224:227],v[128:131],0
mm v[4:7],v[224:227],v[132:135],0
mm v[8:11],v[224:227],v[136:139],0
mm v[12:15],v[224:227],v[140:143],0
wt lgkmcnt(2)
mm v[0:3],v[232:235],v[144:147],v[0:3]
mm v[4:7],v[232:235],v[148:151],v[4:7]
mm v[8:11],v[232:235],v[152:155],v[8:11]
mm v[12:15],v[232:235],v[156:159],v[12:15]
wt lgkmcnt(1)
mm v[0:3],v[244:247],v[160:163],v[0:3]
mm v[4:7],v[244:247],v[164:167],v[4:7]
mm v[8:11],v[244:247],v[168:171],v[8:11]
mm v[12:15],v[244:247],v[172:175],v[12:15]
wt lgkmcnt(0)
mm v[0:3],v[248:251],v[176:179],v[0:3]
mm v[4:7],v[248:251],v[180:183],v[4:7]
mm v[8:11],v[248:251],v[184:187],v[8:11]
mm v[12:15],v[248:251],v[188:191],v[12:15]
s_nop 7
v_or_b32 v16, 1, v0
v_or_b32 v20, 1, v1
v_or_b32 v17, 1, v4
v_or_b32 v21, 1, v5
v_or_b32 v18, 1, v8
v_or_b32 v22, 1, v9
v_or_b32 v19, 1, v12
v_or_b32 v23, 1, v13
st v[16:19],v208,s[44:47],s80 offen sc1
st v[20:23],v208,s[44:47],s20 offen sc1
s_branch .La5_LT_END
; ---- rows that were not routed leave; final sums of the routed rows
.La5_LT_ST:
.La5_LW4_3:
s_cbranch_execz .La5_LW4_R0
s_sleep 28
s_lshl_b32 s92, s76, 5
s_add_u32 s92, s92, s90
s_lshl2_add_u32 s92, s92, s3
s_lshl_b32 s93, s92, 1
v_bfe_u32 v230, v255, 6, 1
v_add_u32 v231, s93, v230
v_cmp_ne_u32 vcc, s84, v231
v_cmp_ne_u32 s[94:95], s21, v231
v_lshrrev_b32_e64 v232, 7, v255
v_and_b32_e64 v233, 48, v255
v_lshl_add_u32 v232, v232, 6, v233
v_lshl_add_u32 v232, v230, 11, v232
s_lshl_b32 s92, s92, 12
s_and_b64 exec, vcc, s[94:95]
s_cbranch_scc0 .La5_LT_F0
st v[192:195],v232,s[56:59],s92 offen offset:0 sc1
st v[196:199],v232,s[56:59],s92 offen offset:512 sc1
st v[200:203],v232,s[56:59],s92 offen offset:1024 sc1
st v[204:207],v232,s[56:59],s92 offen offset:1536 sc1
.La5_LT_F0:
s_mov_b64 exec, -1
s_cmp_eq_u32 s75, 1
s_cbranch_scc1 .La5_LT_END
s_lshr_b32 s92, s2, 6
s_lshl2_add_u32 s92, s92, s74
s_and_b32 s93, s2, 7
s_lshl3_add_u32 s91, s92, s93
s_cmp_lt_u32 s91, 64
s_cselect_b32 s84, s84, s21
s_cselect_b32 s82, s82, s81
s_bfe_u32 s92, s91, 0x30003
s_and_b32 s93, s91, 3
s_lshl2_add_u32 s96, s92, s93
s_and_b32 s85, s91, 4
s_lshl_b32 s85, s85, 1
v_bfe_u32 v0, v255, 6, 2
v_bfe_u32 v1, v255, 4, 2
v_lshlrev_b32 v1, 1, v1
v_lshl_add_u32 v0, v0, 4, v1
v_add_u32 v0, s85, v0
s_lshl_b32 s85, s96, 10
v_add_u32 v1, s85, v0
s_lshl_b32 s85, s96, 6
v_add_u32 v0, s85, v0
s_lshr_b32 s92, s84, 4
s_lshl_b32 s92, s92, 15
s_and_b32 s93, s84, 15
s_lshl_b32 s93, s93, 6
s_add_u32 s92, s92, s93
s_add_u32 s93, s92, 4194304
s_add_u32 s94, s92, 8388608
s_add_u32 s95, s92, 12582912
buffer_load_ushort v4, v1, s[32:35], s92 offen sc1
buffer_load_ushort v5, v1, s[32:35], s93 offen sc1
buffer_load_ushort v6, v1, s[32:35], s94 offen sc1
buffer_load_ushort v7, v1, s[32:35], s95 offen sc1
s_lshl_b32 s21, s84, 11
s_and_b32 s92, s84, 0xf8
s_lshl4_add_u32 s97, s92, s97
s_lshr_b32 s92, s84, 8
s_lshl4_add_u32 s97, s92, s97
buffer_load_ushort v8, v0, s[68:71], s21 offen
ld v[12:15],off,s[44:47],s97 sc1
v_lshrrev_b32 v2, 6, v255
v_lshlrev_b32 v2, 13, v2
v_and_b32 v3, 48, v255
v_add_u32 v2, v2, v3
s_lshl_b32 s92, s3, 17
s_lshl_b32 s93, s91, 6
s_add_u32 s92, s92, s93
s_add_u32 s94, s92, 32768
s_cmp_eq_u32 s3, 3
s_cselect_b32 s95, 0, 524288
s_cselect_b32 s96, 0, -1
s_add_u32 s95, s95, s94
s_ashr_i32 s92, s82, 31
s_orn2_b32 s92, 0x80000000, s92
s_xor_b32 s92, s92, s82
s_and_b32 s92, s92, 0xffff0000
v_mov_b32 v9, 0xbfb8aa3b
v_mul_f32 v9, s92, v9
ex v9,v9
v_lshrrev_b32 v3, 8, v255
s_lshl_b32 s92, s3, 4
v_add_f32 v9, 1.0, v9
v_lshl_add_u32 v3, v3, 2, s92
v_rcp_f32 v9, v9
v_and_b32 v10, 48, v255
v_mul_u32_u24 v10, 20, v10
v_add_u32 v3, v3, v10
cv v9,v9,v9
v_lshlrev_b32 v9, 16, v9
v_and_b32 v10, 0xf0, v255
v_mul_u32_u24 v10, 5, v10
wt vmcnt(0)
v_cmp_eq_u32 vcc, s82, v12
s_nop 1
v_cndmask_b32 v12, v15, v13, vcc
v_lshlrev_b32 v14, 16, v12
v_rcp_f32 v14, v14
v_and_b32 v12, 0xffff0000, v12
v_mul_f32 v12, v12, v14
v_mov_b32 v15, v14
v_xor_b32 v12, 0x80000000, v12
v_mov_b32 v13, v12
v_lshlrev_b32 v4, 16, v4
v_lshlrev_b32 v5, 16, v5
v_lshlrev_b32 v6, 16, v6
v_lshlrev_b32 v7, 16, v7
v_lshlrev_b32 v8, 16, v8
v_add_f32 v4, v4, v5
v_add_f32 v6, v6, v7
v_add_f32 v4, v4, v6
v_add_f32 v4, v4, v8
cv v4,v4,v4
v_lshlrev_b32 v4, 16, v4
v_fma_f32 v5, v4, v14, v12
cv v5,v5,v5
v_lshlrev_b32 v5, 16, v5
s_sleep 20
s_mov_b32 s93, 0
.La5_LT_Y:
ld v[16:19],v2,s[44:47],s94 offen sc1
ld v[20:23],v2,s[44:47],s95 offen sc1
s_add_u32 s93, s93, 1
s_lshr_b32 s92, s93, 20
wt vmcnt(0)
v_min3_u32 v24, v16, v17, v18
v_min3_u32 v25, v20, v21, v22
v_min3_u32 v24, v24, v19, v23
v_min_u32 v24, v24, v25
v_cmp_ne_u32 vcc, 0, v24
v_and_b32 v20, s96, v20
v_and_b32 v21, s96, v21
v_and_b32 v22, s96, v22
v_and_b32 v23, s96, v23
s_cbranch_scc1 .La5_LT_Y1
s_cmp_eq_u64 vcc, -1
s_nop 0
s_nop 0
s_cbranch_scc0 .La5_LT_Y
.La5_LT_Y1:
v_pk_add_f32 v[16:17], v[16:17], v[20:21]
v_pk_add_f32 v[18:19], v[18:19], v[22:23]
s_nop 0
v_add_f32_dpp v16, v16, v16 row_ror:4 row_mask:0xf bank_mask:0xf
v_add_f32_dpp v17, v17, v17 row_ror:4 row_mask:0xf bank_mask:0xf
v_add_f32_dpp v18, v18, v18 row_ror:4 row_mask:0xf bank_mask:0xf
v_add_f32_dpp v19, v19, v19 row_ror:4 row_mask:0xf bank_mask:0xf
v_add_f32_dpp v16, v16, v16 row_ror:8 row_mask:0xf bank_mask:0xf
v_add_f32_dpp v17, v17, v17 row_ror:8 row_mask:0xf bank_mask:0xf
v_add_f32_dpp v18, v18, v18 row_ror:8 row_mask:0xf bank_mask:0xf
v_add_f32_dpp v19, v19, v19 row_ror:8 row_mask:0xf bank_mask:0xf
ds_write_b32 v3, v16 offset:5120
ds_write_b32 v3, v17 offset:5200
ds_write_b32 v3, v18 offset:5280
ds_write_b32 v3, v19 offset:5360
wt lgkmcnt(0)
s_barrier
rd v[16:19],v10 offset:5120
rd v[20:23],v10 offset:5136
rd v[24:27],v10 offset:5152
rd v[28:31],v10 offset:5168
wt lgkmcnt(2)
v_pk_add_f32 v[16:17], v[16:17], v[18:19]
v_pk_add_f32 v[20:21], v[20:21], v[22:23]
wt lgkmcnt(0)
v_pk_add_f32 v[24:25], v[24:25], v[26:27]
v_pk_add_f32 v[28:29], v[28:29], v[30:31]
v_pk_add_f32 v[16:17], v[16:17], v[20:21]
v_pk_add_f32 v[24:25], v[24:25], v[28:29]
v_pk_add_f32 v[16:17], v[16:17], v[24:25]
v_add_f32 v16, v16, v17
cv v16,v16,v16
v_lshlrev_b32 v16, 16, v16
v_mul_f32 v16, v16, v9
cv v16,v16,v16
v_lshlrev_b32 v16, 16, v16
v_add_f32 v16, v16, v5
cv v16,v16,v16
v_lshlrev_b32 v16, 16, v16
v_add_f32 v16, v16, v4
cv v16,v16,v16
s_cmp_lg_u32 s3, 0
s_cbranch_scc1 .La5_LT_END
s_mov_b64 exec, 0xffff
buffer_store_short v16, v0, s[56:59], s21 offen
s_mov_b64 exec, -1
.La5_LT_END:
s_nop 0
s_nop 0
.La5_LW4_4:
s_cbranch_execz .La5_LW4_R0
s_lshl2_add_u32 s92, s2, s3
s_cmp_lg_u32 s92, 992
s_cbranch_scc1 .La5_LT_E1
buffer_store_dwordx2 v[252:253], off, s[64:67], 0 sc1
.La5_LT_E1:
s_branch .La5_LEND
; ---- fallback: the earlier 32 rows against all their keys with the true running maximum (online softmax)
.La5_LA_SLOW0:
wt lgkmcnt(0)
s_nop 7
s_nop 7
aw a160,v192
aw a161,v193
aw a162,v194
aw a163,v195
aw a164,v196
aw a165,v197
aw a166,v198
aw a167,v199
aw a168,v200
aw a169,v201
aw a170,v202
aw a171,v203
aw a172,v204
aw a173,v205
aw a174,v206
aw a175,v207
aw a176,v208
aw a177,v209
aw a178,v210
aw a179,v211
aw a180,v212
aw a181,v213
aw a182,v214
aw a183,v215
aw a184,v216
aw a185,v217
aw a186,v218
aw a187,v219
aw a188,v220
aw a189,v221
aw a190,v222
aw a191,v223
s_lshl_b32 s96, s72, 19
s_add_u32 s96, s96, 20971520
s_add_u32 s92, s26, s96
s_addc_u32 s93, s27, 0
s_and_b32 s93, s93, 0xffff
s_mov_b32 s94, 17301504
s_mov_b32 s95, 0x20000
v_mov_b32 v0, 0
v_mov_b32 v1, 0
v_mov_b32 v2, 0
v_mov_b32 v3, 0
v_mov_b32 v4, 0
v_mov_b32 v5, 0
v_mov_b32 v6, 0
v_mov_b32 v7, 0
v_mov_b32 v8, 0
v_mov_b32 v9, 0
v_mov_b32 v10, 0
v_mov_b32 v11, 0
v_mov_b32 v12, 0
v_mov_b32 v13, 0
v_mov_b32 v14, 0
v_mov_b32 v15, 0
v_mov_b32 v16, 0
v_mov_b32 v17, 0
v_mov_b32 v18, 0
v_mov_b32 v19, 0
v_mov_b32 v20, 0
v_mov_b32 v21, 0
v_mov_b32 v22, 0
v_mov_b32 v23, 0
v_mov_b32 v24, 0
v_mov_b32 v25, 0
v_mov_b32 v26, 0
v_mov_b32 v27, 0
v_mov_b32 v28, 0
v_mov_b32 v29, 0
v_mov_b32 v30, 0
v_mov_b32 v31, 0
v_mov_b32 v32, 0
v_mov_b32 v33, 0
v_mov_b32 v34, 0
v_mov_b32 v35, 0
v_mov_b32 v36, 0
v_mov_b32 v37, 0
v_mov_b32 v38, 0
v_mov_b32 v39, 0
v_mov_b32 v40, 0
v_mov_b32 v41, 0
v_mov_b32 v42, 0
v_mov_b32 v43, 0
v_mov_b32 v44, 0
v_mov_b32 v45, 0
v_mov_b32 v46, 0
v_mov_b32 v47, 0
v_mov_b32 v48, 0
v_mov_b32 v49, 0
v_mov_b32 v50, 0
v_mov_b32 v51, 0
v_mov_b32 v52, 0
v_mov_b32 v53, 0
v_mov_b32 v54, 0
v_mov_b32 v55, 0
v_mov_b32 v56, 0
v_mov_b32 v57, 0
v_mov_b32 v58, 0
v_mov_b32 v59, 0
v_mov_b32 v60, 0
v_mov_b32 v61, 0
v_mov_b32 v62, 0
v_mov_b32 v63, 0
v_mov_b32 v216, 0xff61b1e6
v_mov_b32 v217, 0xff61b1e6
v_mov_b32 v218, 0
v_mov_b32 v219, 0
s_mov_b32 s7, 0
.La5_LA_SLOW0_L:
s_lshl_b32 s96, s7, 13
s_add_u32 s97, s96, 4096
ld a[128:131],v255,s[92:95],s96 offen offset:0 sc1
ld a[132:135],v255,s[92:95],s96 offen offset:1024 sc1
ld a[136:139],v255,s[92:95],s96 offen offset:2048 sc1
ld a[140:143],v255,s[92:95],s96 offen offset:3072 sc1
ld a[144:147],v255,s[92:95],s97 offen offset:0 sc1
ld a[148:151],v255,s[92:95],s97 offen offset:1024 sc1
ld a[152:155],v255,s[92:95],s97 offen offset:2048 sc1
ld a[156:159],v255,s[92:95],s97 offen offset:3072 sc1
s_lshl_b32 s96, s76, 3
s_sub_u32 s96, s96, s7
s_add_u32 s96, s96, s3
s_lshl_b32 s96, s96, 5
v_bfe_u32 v220, v255, 4, 4
v_lshrrev_b32 v221, 8, v255
v_lshlrev_b32 v221, 2, v221
v_sub_u32 v220, v220, v221
v_add_u32 v220, s96, v220
v_mov_b32 v222, 0xceabfb8f
wt vmcnt(0)
mm v[192:195],a[128:131],v[128:131],0
mm v[192:195],a[132:135],v[132:135],v[192:195]
mm v[192:195],a[136:139],v[136:139],v[192:195]
mm v[192:195],a[140:143],v[140:143],v[192:195]
mm v[196:199],a[144:147],v[128:131],0
mm v[196:199],a[148:151],v[132:135],v[196:199]
mm v[196:199],a[152:155],v[136:139],v[196:199]
mm v[196:199],a[156:159],v[140:143],v[196:199]
mm v[200:203],a[128:131],v[144:147],0
mm v[200:203],a[132:135],v[148:151],v[200:203]
mm v[200:203],a[136:139],v[152:155],v[200:203]
mm v[200:203],a[140:143],v[156:159],v[200:203]
mm v[204:207],a[144:147],v[144:147],0
mm v[204:207],a[148:151],v[148:151],v[204:207]
mm v[204:207],a[152:155],v[152:155],v[204:207]
mm v[204:207],a[156:159],v[156:159],v[204:207]
s_nop 7
s_nop 7
s_lshl_b32 s96, s7, 13
s_add_u32 s96, s96, 16777216
s_add_u32 s97, s96, 4096
ld a[128:131],v255,s[92:95],s96 offen offset:0 sc1
ld a[132:135],v255,s[92:95],s96 offen offset:1024 sc1
ld a[136:139],v255,s[92:95],s96 offen offset:2048 sc1
ld a[140:143],v255,s[92:95],s96 offen offset:3072 sc1
ld a[144:147],v255,s[92:95],s97 offen offset:0 sc1
ld a[148:151],v255,s[92:95],s97 offen offset:1024 sc1
ld a[152:155],v255,s[92:95],s97 offen offset:2048 sc1
ld a[156:159],v255,s[92:95],s97 offen offset:3072 sc1
v_cmp_gt_i32 vcc, 0, v220
s_nop 1
v_cndmask_b32 v192, v192, v222, vcc
v_cmp_gt_i32 vcc, 1, v220
s_nop 1
v_cndmask_b32 v193, v193, v222, vcc
v_cmp_gt_i32 vcc, 2, v220
s_nop 1
v_cndmask_b32 v194, v194, v222, vcc
v_cmp_gt_i32 vcc, 3, v220
s_nop 1
v_cndmask_b32 v195, v195, v222, vcc
v_cmp_gt_i32 vcc, 16, v220
s_nop 1
v_cndmask_b32 v196, v196, v222, vcc
v_cmp_gt_i32 vcc, 17, v220
s_nop 1
v_cndmask_b32 v197, v197, v222, vcc
v_cmp_gt_i32 vcc, 18, v220
s_nop 1
v_cndmask_b32 v198, v198, v222, vcc
v_cmp_gt_i32 vcc, 19, v220
s_nop 1
v_cndmask_b32 v199, v199, v222, vcc
v_cmp_gt_i32 vcc, -16, v220
s_nop 1
v_cndmask_b32 v200, v200, v222, vcc
v_cmp_gt_i32 vcc, -15, v220
s_nop 1
v_cndmask_b32 v201, v201, v222, vcc
v_cmp_gt_i32 vcc, -14, v220
s_nop 1
v_cndmask_b32 v202, v202, v222, vcc
v_cmp_gt_i32 vcc, -13, v220
s_nop 1
v_cndmask_b32 v203, v203, v222, vcc
v_cmp_gt_i32 vcc, 0, v220
s_nop 1
v_cndmask_b32 v204, v204, v222, vcc
v_cmp_gt_i32 vcc, 1, v220
s_nop 1
v_cndmask_b32 v205, v205, v222, vcc
v_cmp_gt_i32 vcc, 2, v220
s_nop 1
v_cndmask_b32 v206, v206, v222, vcc
v_cmp_gt_i32 vcc, 3, v220
s_nop 1
v_cndmask_b32 v207, v207, v222, vcc
v_max3_f32 v222, v192, v193, v216
v_max3_f32 v222, v194, v195, v222
v_max3_f32 v222, v196, v197, v222
v_max3_f32 v222, v198, v199, v222
v_mov_b32 v221, v222
s_nop 1
v_permlane16_swap_b32 v221, v222
s_nop 1
v_max_f32 v222, v222, v221
v_mov_b32 v221, v222
s_nop 1
v_permlane32_swap_b32 v221, v222
s_nop 1
v_max_f32 v222, v222, v221
v_sub_f32 v223, v216, v222
v_mov_b32 v216, v222
ex v223,v223
v_sub_f32 v192, v192, v222
v_sub_f32 v193, v193, v222
v_sub_f32 v194, v194, v222
v_sub_f32 v195, v195, v222
v_sub_f32 v196, v196, v222
v_sub_f32 v197, v197, v222
v_sub_f32 v198, v198, v222
v_sub_f32 v199, v199, v222
ex v192,v192
ex v193,v193
ex v194,v194
ex v195,v195
ex v196,v196
ex v197,v197
ex v198,v198
ex v199,v199
v_mul_f32 v218, v218, v223
v_add_f32 v218, v218, v192
v_add_f32 v218, v218, v193
v_add_f32 v218, v218, v194
v_add_f32 v218, v218, v195
v_add_f32 v218, v218, v196
v_add_f32 v218, v218, v197
v_add_f32 v218, v218, v198
v_add_f32 v218, v218, v199
cv v208,v192,v193
cv v209,v194,v195
cv v210,v196,v197
cv v211,v198,v199
v_mul_f32 v0, v0, v223
v_mul_f32 v1, v1, v223
v_mul_f32 v2, v2, v223
v_mul_f32 v3, v3, v223
v_mul_f32 v4, v4, v223
v_mul_f32 v5, v5, v223
v_mul_f32 v6, v6, v223
v_mul_f32 v7, v7, v223
v_mul_f32 v8, v8, v223
v_mul_f32 v9, v9, v223
v_mul_f32 v10, v10, v223
v_mul_f32 v11, v11, v223
v_mul_f32 v12, v12, v223
v_mul_f32 v13, v13, v223
v_mul_f32 v14, v14, v223
v_mul_f32 v15, v15, v223
v_mul_f32 v16, v16, v223
v_mul_f32 v17, v17, v223
v_mul_f32 v18, v18, v223
v_mul_f32 v19, v19, v223
v_mul_f32 v20, v20, v223
v_mul_f32 v21, v21, v223
v_mul_f32 v22, v22, v223
v_mul_f32 v23, v23, v223
v_mul_f32 v24, v24, v223
v_mul_f32 v25, v25, v223
v_mul_f32 v26, v26, v223
v_mul_f32 v27, v27, v223
v_mul_f32 v28, v28, v223
v_mul_f32 v29, v29, v223
v_mul_f32 v30, v30, v223
v_mul_f32 v31, v31, v223
v_max3_f32 v222, v200, v201, v217
v_max3_f32 v222, v202, v203, v222
v_max3_f32 v222, v204, v205, v222
v_max3_f32 v222, v206, v207, v222
v_mov_b32 v221, v222
s_nop 1
v_permlane16_swap_b32 v221, v222
s_nop 1
v_max_f32 v222, v222, v221
v_mov_b32 v221, v222
s_nop 1
v_permlane32_swap_b32 v221, v222
s_nop 1
v_max_f32 v222, v222, v221
v_sub_f32 v223, v217, v222
v_mov_b32 v217, v222
ex v223,v223
v_sub_f32 v200, v200, v222
v_sub_f32 v201, v201, v222
v_sub_f32 v202, v202, v222
v_sub_f32 v203, v203, v222
v_sub_f32 v204, v204, v222
v_sub_f32 v205, v205, v222
v_sub_f32 v206, v206, v222
v_sub_f32 v207, v207, v222
ex v200,v200
ex v201,v201
ex v202,v202
ex v203,v203
ex v204,v204
ex v205,v205
ex v206,v206
ex v207,v207
v_mul_f32 v219, v219, v223
v_add_f32 v219, v219, v200
v_add_f32 v219, v219, v201
v_add_f32 v219, v219, v202
v_add_f32 v219, v219, v203
v_add_f32 v219, v219, v204
v_add_f32 v219, v219, v205
v_add_f32 v219, v219, v206
v_add_f32 v219, v219, v207
cv v212,v200,v201
cv v213,v202,v203
cv v214,v204,v205
cv v215,v206,v207
v_mul_f32 v32, v32, v223
v_mul_f32 v33, v33, v223
v_mul_f32 v34, v34, v223
v_mul_f32 v35, v35, v223
v_mul_f32 v36, v36, v223
v_mul_f32 v37, v37, v223
v_mul_f32 v38, v38, v223
v_mul_f32 v39, v39, v223
v_mul_f32 v40, v40, v223
v_mul_f32 v41, v41, v223
v_mul_f32 v42, v42, v223
v_mul_f32 v43, v43, v223
v_mul_f32 v44, v44, v223
v_mul_f32 v45, v45, v223
v_mul_f32 v46, v46, v223
v_mul_f32 v47, v47, v223
v_mul_f32 v48, v48, v223
v_mul_f32 v49, v49, v223
v_mul_f32 v50, v50, v223
v_mul_f32 v51, v51, v223
v_mul_f32 v52, v52, v223
v_mul_f32 v53, v53, v223
v_mul_f32 v54, v54, v223
v_mul_f32 v55, v55, v223
v_mul_f32 v56, v56, v223
v_mul_f32 v57, v57, v223
v_mul_f32 v58, v58, v223
v_mul_f32 v59, v59, v223
v_mul_f32 v60, v60, v223
v_mul_f32 v61, v61, v223
v_mul_f32_e64 v62, v62, v223
v_mul_f32_e64 v63, v63, v223
wt vmcnt(0)
mm v[0:3],a[128:131],v[208:211],v[0:3]
mm v[32:35],a[128:131],v[212:215],v[32:35]
mm v[4:7],a[132:135],v[208:211],v[4:7]
mm v[36:39],a[132:135],v[212:215],v[36:39]
mm v[8:11],a[136:139],v[208:211],v[8:11]
mm v[40:43],a[136:139],v[212:215],v[40:43]
mm v[12:15],a[140:143],v[208:211],v[12:15]
mm v[44:47],a[140:143],v[212:215],v[44:47]
mm v[16:19],a[144:147],v[208:211],v[16:19]
mm v[48:51],a[144:147],v[212:215],v[48:51]
mm v[20:23],a[148:151],v[208:211],v[20:23]
mm v[52:55],a[148:151],v[212:215],v[52:55]
mm v[24:27],a[152:155],v[208:211],v[24:27]
mm v[56:59],a[152:155],v[212:215],v[56:59]
mm v[28:31],a[156:159],v[208:211],v[28:31]
mm v[60:63],a[156:159],v[212:215],v[60:63]
s_nop 7
s_nop 7
s_add_u32 s7, s7, 1
s_lshr_b32 s96, s7, 3
s_cmp_le_u32 s96, s76
s_cbranch_scc1 .La5_LA_SLOW0_L
v_mov_b32 v221, v218
s_nop 1
v_permlane16_swap_b32 v221, v218
s_nop 1
v_add_f32 v218, v218, v221
v_mov_b32 v221, v218
s_nop 1
v_permlane32_swap_b32 v221, v218
s_nop 1
v_add_f32 v218, v218, v221
v_mov_b32 v221, v219
s_nop 1
v_permlane16_swap_b32 v221, v219
s_nop 1
v_add_f32 v219, v219, v221
v_mov_b32 v221, v219
s_nop 1
v_permlane32_swap_b32 v221, v219
s_nop 1
v_add_f32 v219, v219, v221
v_rcp_f32 v242, v218
v_rcp_f32 v243, v219
ar v192,a160
ar v193,a161
ar v194,a162
ar v195,a163
ar v196,a164
ar v197,a165
ar v198,a166
ar v199,a167
ar v200,a168
ar v201,a169
ar v202,a170
ar v203,a171
ar v204,a172
ar v205,a173
ar v206,a174
ar v207,a175
ar v208,a176
ar v209,a177
ar v210,a178
ar v211,a179
ar v212,a180
ar v213,a181
ar v214,a182
ar v215,a183
ar v216,a184
ar v217,a185
ar v218,a186
ar v219,a187
ar v220,a188
ar v221,a189
ar v222,a190
ar v223,a191
s_mov_b32 s95, 4096
s_mov_b32 s96, 8192
s_mov_b32 s97, 12288
s_mov_b64 vcc, 0
s_nop 1
s_setpc_b64 s[24:25]
; ---- fallback: the later 32 rows
.La5_LA_SLOW1:
wt lgkmcnt(0)
s_nop 7
s_nop 7
aw a160,v192
aw a161,v193
aw a162,v194
aw a163,v195
aw a164,v196
aw a165,v197
aw a166,v198
aw a167,v199
aw a168,v200
aw a169,v201
aw a170,v202
aw a171,v203
aw a172,v204
aw a173,v205
aw a174,v206
aw a175,v207
aw a176,v208
aw a177,v209
aw a178,v210
aw a179,v211
aw a180,v212
aw a181,v213
aw a182,v214
aw a183,v215
aw a184,v216
aw a185,v217
aw a186,v218
aw a187,v219
aw a188,v220
aw a189,v221
aw a190,v222
aw a191,v223
s_lshl_b32 s96, s72, 19
s_add_u32 s96, s96, 20971520
s_add_u32 s92, s26, s96
s_addc_u32 s93, s27, 0
s_and_b32 s93, s93, 0xffff
s_mov_b32 s94, 17301504
s_mov_b32 s95, 0x20000
v_mov_b32 v64, 0
v_mov_b32 v65, 0
v_mov_b32 v66, 0
v_mov_b32 v67, 0
v_mov_b32 v68, 0
v_mov_b32 v69, 0
v_mov_b32 v70, 0
v_mov_b32 v71, 0
v_mov_b32 v72, 0
v_mov_b32 v73, 0
v_mov_b32 v74, 0
v_mov_b32 v75, 0
v_mov_b32 v76, 0
v_mov_b32 v77, 0
v_mov_b32 v78, 0
v_mov_b32 v79, 0
v_mov_b32 v80, 0
v_mov_b32 v81, 0
v_mov_b32 v82, 0
v_mov_b32 v83, 0
v_mov_b32 v84, 0
v_mov_b32 v85, 0
v_mov_b32 v86, 0
v_mov_b32 v87, 0
v_mov_b32 v88, 0
v_mov_b32 v89, 0
v_mov_b32 v90, 0
v_mov_b32 v91, 0
v_mov_b32 v92, 0
v_mov_b32 v93, 0
v_mov_b32 v94, 0
v_mov_b32 v95, 0
v_mov_b32 v96, 0
v_mov_b32 v97, 0
v_mov_b32 v98, 0
v_mov_b32 v99, 0
v_mov_b32 v100, 0
v_mov_b32 v101, 0
v_mov_b32 v102, 0
v_mov_b32 v103, 0
v_mov_b32 v104, 0
v_mov_b32 v105, 0
v_mov_b32 v106, 0
v_mov_b32 v107, 0
v_mov_b32 v108, 0
v_mov_b32 v109, 0
v_mov_b32 v110, 0
v_mov_b32 v111, 0
v_mov_b32 v112, 0
v_mov_b32 v113, 0
v_mov_b32 v114, 0
v_mov_b32 v115, 0
v_mov_b32 v116, 0
v_mov_b32 v117, 0
v_mov_b32 v118, 0
v_mov_b32 v119, 0
v_mov_b32 v120, 0
v_mov_b32 v121, 0
v_mov_b32 v122, 0
v_mov_b32 v123, 0
v_mov_b32 v124, 0
v_mov_b32 v125, 0
v_mov_b32 v126, 0
v_mov_b32 v127, 0
v_mov_b32 v216, 0xff61b1e6
v_mov_b32 v217, 0xff61b1e6
v_mov_b32 v218, 0
v_mov_b32 v219, 0
s_mov_b32 s7, 0
.La5_LA_SLOW1_L:
s_lshl_b32 s96, s7, 13
s_add_u32 s97, s96, 4096
ld a[128:131],v255,s[92:95],s96 offen offset:0 sc1
ld a[132:135],v255,s[92:95],s96 offen offset:1024 sc1
ld a[136:139],v255,s[92:95],s96 offen offset:2048 sc1
ld a[140:143],v255,s[92:95],s96 offen offset:3072 sc1
ld a[144:147],v255,s[92:95],s97 offen offset:0 sc1
ld a[148:151],v255,s[92:95],s97 offen offset:1024 sc1
ld a[152:155],v255,s[92:95],s97 offen offset:2048 sc1
ld a[156:159],v255,s[92:95],s97 offen offset:3072 sc1
s_lshl_b32 s96, s76, 3
s_sub_u32 s96, s96, s7
s_sub_u32 s96, s96, s3
s_add_u32 s96, s96, 7
s_lshl_b32 s96, s96, 5
v_bfe_u32 v220, v255, 4, 4
v_lshrrev_b32 v221, 8, v255
v_lshlrev_b32 v221, 2, v221
v_sub_u32 v220, v220, v221
v_add_u32 v220, s96, v220
v_mov_b32 v222, 0xceabfb8f
wt vmcnt(0)
mm v[192:195],a[128:131],v[160:163],0
mm v[192:195],a[132:135],v[164:167],v[192:195]
mm v[192:195],a[136:139],v[168:171],v[192:195]
mm v[192:195],a[140:143],v[172:175],v[192:195]
mm v[196:199],a[144:147],v[160:163],0
mm v[196:199],a[148:151],v[164:167],v[196:199]
mm v[196:199],a[152:155],v[168:171],v[196:199]
mm v[196:199],a[156:159],v[172:175],v[196:199]
mm v[200:203],a[128:131],v[176:179],0
mm v[200:203],a[132:135],v[180:183],v[200:203]
mm v[200:203],a[136:139],v[184:187],v[200:203]
mm v[200:203],a[140:143],v[188:191],v[200:203]
mm v[204:207],a[144:147],v[176:179],0
mm v[204:207],a[148:151],v[180:183],v[204:207]
mm v[204:207],a[152:155],v[184:187],v[204:207]
mm v[204:207],a[156:159],v[188:191],v[204:207]
s_nop 7
s_nop 7
s_lshl_b32 s96, s7, 13
s_add_u32 s96, s96, 16777216
s_add_u32 s97, s96, 4096
ld a[128:131],v255,s[92:95],s96 offen offset:0 sc1
ld a[132:135],v255,s[92:95],s96 offen offset:1024 sc1
ld a[136:139],v255,s[92:95],s96 offen offset:2048 sc1
ld a[140:143],v255,s[92:95],s96 offen offset:3072 sc1
ld a[144:147],v255,s[92:95],s97 offen offset:0 sc1
ld a[148:151],v255,s[92:95],s97 offen offset:1024 sc1
ld a[152:155],v255,s[92:95],s97 offen offset:2048 sc1
ld a[156:159],v255,s[92:95],s97 offen offset:3072 sc1
v_cmp_gt_i32 vcc, 0, v220
s_nop 1
v_cndmask_b32 v192, v192, v222, vcc
v_cmp_gt_i32 vcc, 1, v220
s_nop 1
v_cndmask_b32 v193, v193, v222, vcc
v_cmp_gt_i32 vcc, 2, v220
s_nop 1
v_cndmask_b32 v194, v194, v222, vcc
v_cmp_gt_i32 vcc, 3, v220
s_nop 1
v_cndmask_b32 v195, v195, v222, vcc
v_cmp_gt_i32 vcc, 16, v220
s_nop 1
v_cndmask_b32 v196, v196, v222, vcc
v_cmp_gt_i32 vcc, 17, v220
s_nop 1
v_cndmask_b32 v197, v197, v222, vcc
v_cmp_gt_i32 vcc, 18, v220
s_nop 1
v_cndmask_b32 v198, v198, v222, vcc
v_cmp_gt_i32 vcc, 19, v220
s_nop 1
v_cndmask_b32 v199, v199, v222, vcc
v_cmp_gt_i32 vcc, -16, v220
s_nop 1
v_cndmask_b32 v200, v200, v222, vcc
v_cmp_gt_i32 vcc, -15, v220
s_nop 1
v_cndmask_b32 v201, v201, v222, vcc
v_cmp_gt_i32 vcc, -14, v220
s_nop 1
v_cndmask_b32 v202, v202, v222, vcc
v_cmp_gt_i32 vcc, -13, v220
s_nop 1
v_cndmask_b32 v203, v203, v222, vcc
v_cmp_gt_i32 vcc, 0, v220
s_nop 1
v_cndmask_b32 v204, v204, v222, vcc
v_cmp_gt_i32 vcc, 1, v220
s_nop 1
v_cndmask_b32 v205, v205, v222, vcc
v_cmp_gt_i32 vcc, 2, v220
s_nop 1
v_cndmask_b32 v206, v206, v222, vcc
v_cmp_gt_i32 vcc, 3, v220
s_nop 1
v_cndmask_b32 v207, v207, v222, vcc
v_max3_f32 v222, v192, v193, v216
v_max3_f32 v222, v194, v195, v222
v_max3_f32 v222, v196, v197, v222
v_max3_f32 v222, v198, v199, v222
v_mov_b32 v221, v222
s_nop 1
v_permlane16_swap_b32 v221, v222
s_nop 1
v_max_f32 v222, v222, v221
v_mov_b32 v221, v222
s_nop 1
v_permlane32_swap_b32 v221, v222
s_nop 1
v_max_f32 v222, v222, v221
v_sub_f32 v223, v216, v222
v_mov_b32 v216, v222
ex v223,v223
v_sub_f32 v192, v192, v222
v_sub_f32 v193, v193, v222
v_sub_f32 v194, v194, v222
v_sub_f32 v195, v195, v222
v_sub_f32 v196, v196, v222
v_sub_f32 v197, v197, v222
v_sub_f32 v198, v198, v222
v_sub_f32 v199, v199, v222
ex v192,v192
ex v193,v193
ex v194,v194
ex v195,v195
ex v196,v196
ex v197,v197
ex v198,v198
ex v199,v199
v_mul_f32 v218, v218, v223
v_add_f32 v218, v218, v192
v_add_f32 v218, v218, v193
v_add_f32 v218, v218, v194
v_add_f32 v218, v218, v195
v_add_f32 v218, v218, v196
v_add_f32 v218, v218, v197
v_add_f32 v218, v218, v198
v_add_f32 v218, v218, v199
cv v208,v192,v193
cv v209,v194,v195
cv v210,v196,v197
cv v211,v198,v199
v_mul_f32 v64, v64, v223
v_mul_f32 v65, v65, v223
v_mul_f32 v66, v66, v223
v_mul_f32 v67, v67, v223
v_mul_f32 v68, v68, v223
v_mul_f32 v69, v69, v223
v_mul_f32 v70, v70, v223
v_mul_f32 v71, v71, v223
v_mul_f32 v72, v72, v223
v_mul_f32 v73, v73, v223
v_mul_f32 v74, v74, v223
v_mul_f32 v75, v75, v223
v_mul_f32 v76, v76, v223
v_mul_f32 v77, v77, v223
v_mul_f32 v78, v78, v223
v_mul_f32 v79, v79, v223
v_mul_f32 v80, v80, v223
v_mul_f32 v81, v81, v223
v_mul_f32 v82, v82, v223
v_mul_f32 v83, v83, v223
v_mul_f32 v84, v84, v223
v_mul_f32 v85, v85, v223
v_mul_f32 v86, v86, v223
v_mul_f32 v87, v87, v223
v_mul_f32 v88, v88, v223
v_mul_f32 v89, v89, v223
v_mul_f32 v90, v90, v223
v_mul_f32 v91, v91, v223
v_mul_f32 v92, v92, v223
v_mul_f32 v93, v93, v223
v_mul_f32 v94, v94, v223
v_mul_f32 v95, v95, v223
v_max3_f32 v222, v200, v201, v217
v_max3_f32 v222, v202, v203, v222
v_max3_f32 v222, v204, v205, v222
v_max3_f32 v222, v206, v207, v222
v_mov_b32 v221, v222
s_nop 1
v_permlane16_swap_b32 v221, v222
s_nop 1
v_max_f32 v222, v222, v221
v_mov_b32 v221, v222
s_nop 1
v_permlane32_swap_b32 v221, v222
s_nop 1
v_max_f32 v222, v222, v221
v_sub_f32 v223, v217, v222
v_mov_b32 v217, v222
ex v223,v223
v_sub_f32 v200, v200, v222
v_sub_f32 v201, v201, v222
v_sub_f32 v202, v202, v222
v_sub_f32 v203, v203, v222
v_sub_f32 v204, v204, v222
v_sub_f32 v205, v205, v222
v_sub_f32 v206, v206, v222
v_sub_f32 v207, v207, v222
ex v200,v200
ex v201,v201
ex v202,v202
ex v203,v203
ex v204,v204
ex v205,v205
ex v206,v206
ex v207,v207
v_mul_f32 v219, v219, v223
v_add_f32 v219, v219, v200
v_add_f32 v219, v219, v201
v_add_f32 v219, v219, v202
v_add_f32 v219, v219, v203
v_add_f32 v219, v219, v204
v_add_f32 v219, v219, v205
v_add_f32 v219, v219, v206
v_add_f32 v219, v219, v207
cv v212,v200,v201
cv v213,v202,v203
cv v214,v204,v205
cv v215,v206,v207
v_mul_f32 v96, v96, v223
v_mul_f32 v97, v97, v223
v_mul_f32 v98, v98, v223
v_mul_f32 v99, v99, v223
v_mul_f32 v100, v100, v223
v_mul_f32 v101, v101, v223
v_mul_f32 v102, v102, v223
v_mul_f32 v103, v103, v223
v_mul_f32 v104, v104, v223
v_mul_f32 v105, v105, v223
v_mul_f32 v106, v106, v223
v_mul_f32 v107, v107, v223
v_mul_f32 v108, v108, v223
v_mul_f32 v109, v109, v223
v_mul_f32 v110, v110, v223
v_mul_f32 v111, v111, v223
v_mul_f32 v112, v112, v223
v_mul_f32 v113, v113, v223
v_mul_f32 v114, v114, v223
v_mul_f32 v115, v115, v223
v_mul_f32 v116, v116, v223
v_mul_f32 v117, v117, v223
v_mul_f32 v118, v118, v223
v_mul_f32 v119, v119, v223
v_mul_f32 v120, v120, v223
v_mul_f32 v121, v121, v223
v_mul_f32 v122, v122, v223
v_mul_f32 v123, v123, v223
v_mul_f32 v124, v124, v223
v_mul_f32 v125, v125, v223
v_mul_f32 v126, v126, v223
v_mul_f32_e64 v127, v127, v223
wt vmcnt(0)
mm v[64:67],a[128:131],v[208:211],v[64:67]
mm v[96:99],a[128:131],v[212:215],v[96:99]
mm v[68:71],a[132:135],v[208:211],v[68:71]
mm v[100:103],a[132:135],v[212:215],v[100:103]
mm v[72:75],a[136:139],v[208:211],v[72:75]
mm v[104:107],a[136:139],v[212:215],v[104:107]
mm v[76:79],a[140:143],v[208:211],v[76:79]
mm v[108:111],a[140:143],v[212:215],v[108:111]
mm v[80:83],a[144:147],v[208:211],v[80:83]
mm v[112:115],a[144:147],v[212:215],v[112:115]
mm v[84:87],a[148:151],v[208:211],v[84:87]
mm v[116:119],a[148:151],v[212:215],v[116:119]
mm v[88:91],a[152:155],v[208:211],v[88:91]
mm v[120:123],a[152:155],v[212:215],v[120:123]
mm v[92:95],a[156:159],v[208:211],v[92:95]
mm v[124:127],a[156:159],v[212:215],v[124:127]
s_nop 7
s_nop 7
s_add_u32 s7, s7, 1
s_lshr_b32 s96, s7, 3
s_cmp_le_u32 s96, s76
s_cbranch_scc1 .La5_LA_SLOW1_L
v_mov_b32 v221, v218
s_nop 1
v_permlane16_swap_b32 v221, v218
s_nop 1
v_add_f32 v218, v218, v221
v_mov_b32 v221, v218
s_nop 1
v_permlane32_swap_b32 v221, v218
s_nop 1
v_add_f32 v218, v218, v221
v_mov_b32 v221, v219
s_nop 1
v_permlane16_swap_b32 v221, v219
s_nop 1
v_add_f32 v219, v219, v221
v_mov_b32 v221, v219
s_nop 1
v_permlane32_swap_b32 v221, v219
s_nop 1
v_add_f32 v219, v219, v221
v_rcp_f32 v242, v218
v_rcp_f32 v243, v219
ar v192,a160
ar v193,a161
ar v194,a162
ar v195,a163
ar v196,a164
ar v197,a165
ar v198,a166
ar v199,a167
ar v200,a168
ar v201,a169
ar v202,a170
ar v203,a171
ar v204,a172
ar v205,a173
ar v206,a174
ar v207,a175
ar v208,a176
ar v209,a177
ar v210,a178
ar v211,a179
ar v212,a180
ar v213,a181
ar v214,a182
ar v215,a183
ar v216,a184
ar v217,a185
ar v218,a186
ar v219,a187
ar v220,a188
ar v221,a189
ar v222,a190
ar v223,a191
s_mov_b32 s95, 4096
s_mov_b32 s96, 8192
s_mov_b32 s97, 12288
s_mov_b64 vcc, 0
s_nop 1
s_setpc_b64 s[24:25]
.La5_LA_SC1:
s_call_b64 s[24:25], .La5_LA_SLOW0
s_branch .La5_LA_SR1
.La5_LA_SC2:
s_call_b64 s[24:25], .La5_LA_SLOW1
s_branch .La5_LA_SR2
.La5_LA_PS0:
s_mov_b32 s84, 3
s_mov_b32 s70, 17408
wt vmcnt(2)
ld a[220:223],v255,s[68:71],s94 offen sc1
ld a[0:3],v255,s[68:71],0 offen offset:0 sc1
ld a[4:7],v255,s[68:71],0 offen offset:1024 sc1
ld a[8:11],v255,s[68:71],0 offen offset:2048 sc1
ld a[12:15],v255,s[68:71],0 offen offset:3072 sc1
ld a[16:19],v255,s[68:71],s95 offen offset:0 sc1
ld a[20:23],v255,s[68:71],s95 offen offset:1024 sc1
ld a[24:27],v255,s[68:71],s95 offen offset:2048 sc1
s_branch .La5_LA_PS1
.La5_LA_PS2:
s_mov_b32 s84, 3
s_mov_b32 s70, 17408
wt vmcnt(0)
ld a[220:223],v255,s[68:71],s94 offen sc1
ld a[0:3],v255,s[68:71],0 offen offset:0 sc1
ld a[4:7],v255,s[68:71],0 offen offset:1024 sc1
ld a[8:11],v255,s[68:71],0 offen offset:2048 sc1
ld a[12:15],v255,s[68:71],0 offen offset:3072 sc1
ld a[16:19],v255,s[68:71],s95 offen offset:0 sc1
ld a[20:23],v255,s[68:71],s95 offen offset:1024 sc1
ld a[24:27],v255,s[68:71],s95 offen offset:2048 sc1
ld a[28:31],v255,s[68:71],s95 offen offset:3072 sc1
ld a[32:35],v255,s[68:71],s96 offen offset:0 sc1
ld a[36:39],v255,s[68:71],s96 offen offset:1024 sc1
ld a[40:43],v255,s[68:71],s96 offen offset:2048 sc1
ld a[44:47],v255,s[68:71],s96 offen offset:3072 sc1
ld a[48:51],v255,s[68:71],s97 offen offset:0 sc1
ld a[52:55],v255,s[68:71],s97 offen offset:1024 sc1
ld a[56:59],v255,s[68:71],s97 offen offset:2048 sc1
ld a[60:63],v255,s[68:71],s97 offen offset:3072 sc1
s_branch .La5_LA_PS3
.La5_LA_PSPIN:
s_mov_b32 s93, 0
.La5_LA_PSPIN_L:
s_add_u32 s93, s93, 1
s_cmp_gt_u32 s93, 2097152
s_cbranch_scc1 .La5_LA_PSOK
s_sleep 8
buffer_load_dword v250, v251, s[64:67], s20 offen sc1
wt vmcnt(0)
v_cmp_eq_u32 vcc, v250, v249
s_nop 1
s_and_b32 s92, vcc_lo, 3
s_cmp_eq_u32 s92, 3
s_cbranch_scc0 .La5_LA_PSPIN_L
s_branch .La5_LA_PSOK
.La5_LA_PSOK:
s_mov_b32 s70, 17408
ld a[220:223],v255,s[68:71],s94 offen sc1
ld a[0:3],v255,s[68:71],0 offen offset:0 sc1
ld a[4:7],v255,s[68:71],0 offen offset:1024 sc1
ld a[8:11],v255,s[68:71],0 offen offset:2048 sc1
ld a[12:15],v255,s[68:71],0 offen offset:3072 sc1
ld a[16:19],v255,s[68:71],s95 offen offset:0 sc1
ld a[20:23],v255,s[68:71],s95 offen offset:1024 sc1
ld a[24:27],v255,s[68:71],s95 offen offset:2048 sc1
ld a[28:31],v255,s[68:71],s95 offen offset:3072 sc1
ld a[32:35],v255,s[68:71],s96 offen offset:0 sc1
ld a[36:39],v255,s[68:71],s96 offen offset:1024 sc1
ld a[40:43],v255,s[68:71],s96 offen offset:2048 sc1
ld a[44:47],v255,s[68:71],s96 offen offset:3072 sc1
ld a[48:51],v255,s[68:71],s97 offen offset:0 sc1
ld a[52:55],v255,s[68:71],s97 offen offset:1024 sc1
ld a[56:59],v255,s[68:71],s97 offen offset:2048 sc1
ld a[60:63],v255,s[68:71],s97 offen offset:3072 sc1
s_branch .La5_LA_DC1
.La5_LO_NB1:
.La5_LW3_6:
s_cbranch_execz .La5_LA_RETURN
s_mov_b32 s93, 0
.La5_LO_NB1_L:
s_add_u32 s93, s93, 1
s_cmp_gt_u32 s93, 2097152
s_cbranch_scc1 .La5_LO_KB1
s_sleep 8
buffer_load_dwordx2 v[250:251], v248, s[64:67], s5 offen sc1
wt vmcnt(0)
v_cmp_eq_u64 vcc, v[250:251], v[252:253]
s_nop 1
s_bitcmp1_b32 vcc_lo, 1
s_cbranch_scc0 .La5_LO_NB1_L
s_branch .La5_LO_KB1
.La5_LO_NB2:
s_mov_b32 s93, 0
.La5_LO_NB2_L:
s_add_u32 s93, s93, 1
s_cmp_gt_u32 s93, 2097152
s_nop 0
s_cbranch_scc1 .La5_LO_KB2
s_sleep 8
buffer_load_dwordx2 v[250:251], v248, s[64:67], s5 offen sc1
wt vmcnt(0)
v_cmp_eq_u64 vcc, v[250:251], v[252:253]
s_nop 1
s_bitcmp1_b32 vcc_lo, 2
s_cbranch_scc0 .La5_LO_NB2_L
s_branch .La5_LO_KB2
.La5_LO_NB3:
s_mov_b32 s93, 0
.La5_LO_NB3_L:
s_add_u32 s93, s93, 1
s_cmp_gt_u32 s93, 2097152
s_nop 0
s_nop 0
s_cbranch_scc1 .La5_LO_KB3
s_sleep 8
buffer_load_dwordx2 v[250:251], v248, s[64:67], s5 offen sc1
wt vmcnt(0)
v_cmp_eq_u64 vcc, v[250:251], v[252:253]
s_nop 1
s_bitcmp1_b32 vcc_lo, 3
s_cbranch_scc0 .La5_LO_NB3_L
s_branch .La5_LO_KB3
.La5_LO_NB4:
s_mov_b32 s93, 0
.La5_LO_NB4_L:
s_add_u32 s93, s93, 1
s_cmp_gt_u32 s93, 2097152
s_nop 0
s_nop 0
s_cbranch_scc1 .La5_LO_KB4
s_sleep 8
buffer_load_dwordx2 v[250:251], v248, s[64:67], s5 offen sc1
wt vmcnt(0)
v_cmp_eq_u64 vcc, v[250:251], v[252:253]
s_nop 1
s_bitcmp1_b32 vcc_lo, 4
s_cbranch_scc0 .La5_LO_NB4_L
s_branch .La5_LO_KB4
.La5_LO_NB5:
s_mov_b32 s93, 0
.La5_LO_NB5_L:
s_add_u32 s93, s93, 1
s_cmp_gt_u32 s93, 2097152
s_nop 0
s_nop 0
s_cbranch_scc1 .La5_LO_KB5
s_sleep 8
buffer_load_dwordx2 v[250:251], v248, s[64:67], s5 offen sc1
wt vmcnt(0)
v_cmp_eq_u64 vcc, v[250:251], v[252:253]
s_nop 1
s_bitcmp1_b32 vcc_lo, 5
s_cbranch_scc0 .La5_LO_NB5_L
s_branch .La5_LO_KB5
.La5_LO_NB13:
s_mov_b32 s93, 0
.La5_LO_NB13_L:
s_add_u32 s93, s93, 1
s_cmp_gt_u32 s93, 2097152
s_nop 0
s_nop 0
s_cbranch_scc1 .La5_LO_KB13
s_sleep 8
buffer_load_dwordx2 v[250:251], v248, s[64:67], s5 offen sc1
wt vmcnt(0)
v_cmp_eq_u64 vcc, v[250:251], v[252:253]
s_nop 1
s_bitcmp1_b32 vcc_lo, 13
s_cbranch_scc0 .La5_LO_NB13_L
s_branch .La5_LO_KB13
.La5_LO_NB6:
s_mov_b32 s93, 0
.La5_LO_NB6_L:
s_add_u32 s93, s93, 1
s_cmp_gt_u32 s93, 2097152
s_nop 0
s_nop 0
s_cbranch_scc1 .La5_LO_KB6
s_sleep 8
buffer_load_dwordx2 v[250:251], v248, s[64:67], s5 offen sc1
wt vmcnt(0)
v_cmp_eq_u64 vcc, v[250:251], v[252:253]
s_nop 1
s_bitcmp1_b32 vcc_lo, 6
s_cbranch_scc0 .La5_LO_NB6_L
s_branch .La5_LO_KB6
.La5_LO_NB7:
s_mov_b32 s93, 0
.La5_LO_NB7_L:
s_add_u32 s93, s93, 1
s_cmp_gt_u32 s93, 2097152
s_nop 0
s_nop 0
s_cbranch_scc1 .La5_LO_KB7
s_sleep 8
buffer_load_dwordx2 v[250:251], v248, s[64:67], s5 offen sc1
wt vmcnt(0)
v_cmp_eq_u64 vcc, v[250:251], v[252:253]
s_nop 1
s_bitcmp1_b32 vcc_lo, 7
s_cbranch_scc0 .La5_LO_NB7_L
s_branch .La5_LO_KB7
.La5_LO_NC1:
.La5_LW7_7:
s_cbranch_execz .La5_LA_RETURN
s_mov_b32 s93, 0
.La5_LO_NC1_L:
s_add_u32 s93, s93, 1
s_cmp_gt_u32 s93, 2097152
s_nop 0
s_cbranch_scc1 .La5_LO_KC1
s_sleep 8
buffer_load_dwordx2 v[250:251], v248, s[64:67], s5 offen sc1
wt vmcnt(0)
v_cmp_eq_u64 vcc, v[250:251], v[252:253]
s_nop 1
s_bitcmp1_b32 vcc_lo, 1
s_cbranch_scc0 .La5_LO_NC1_L
s_branch .La5_LO_KC1
.La5_LO_NC2:
s_mov_b32 s93, 0
.La5_LO_NC2_L:
s_add_u32 s93, s93, 1
s_cmp_gt_u32 s93, 2097152
s_nop 0
s_nop 0
s_cbranch_scc1 .La5_LO_KC2
s_sleep 8
buffer_load_dwordx2 v[250:251], v248, s[64:67], s5 offen sc1
wt vmcnt(0)
v_cmp_eq_u64 vcc, v[250:251], v[252:253]
s_nop 1
s_bitcmp1_b32 vcc_lo, 2
s_cbranch_scc0 .La5_LO_NC2_L
s_branch .La5_LO_KC2
.La5_LO_NC3:
s_mov_b32 s93, 0
.La5_LO_NC3_L:
s_add_u32 s93, s93, 1
s_cmp_gt_u32 s93, 2097152
s_nop 0
s_nop 0
s_cbranch_scc1 .La5_LO_KC3
s_sleep 8
buffer_load_dwordx2 v[250:251], v248, s[64:67], s5 offen sc1
wt vmcnt(0)
v_cmp_eq_u64 vcc, v[250:251], v[252:253]
s_nop 1
s_bitcmp1_b32 vcc_lo, 3
s_cbranch_scc0 .La5_LO_NC3_L
s_branch .La5_LO_KC3
.La5_LO_NC4:
s_mov_b32 s93, 0
.La5_LO_NC4_L:
s_add_u32 s93, s93, 1
s_cmp_gt_u32 s93, 2097152
s_nop 0
s_nop 0
s_cbranch_scc1 .La5_LO_KC4
s_sleep 8
buffer_load_dwordx2 v[250:251], v248, s[64:67], s5 offen sc1
wt vmcnt(0)
v_cmp_eq_u64 vcc, v[250:251], v[252:253]
s_nop 1
s_bitcmp1_b32 vcc_lo, 4
s_cbranch_scc0 .La5_LO_NC4_L
s_branch .La5_LO_KC4
.La5_LO_NC5:
s_mov_b32 s93, 0
.La5_LO_NC5_L:
s_add_u32 s93, s93, 1
s_cmp_gt_u32 s93, 2097152
s_nop 0
s_nop 0
s_cbranch_scc1 .La5_LO_KC5
s_sleep 8
buffer_load_dwordx2 v[250:251], v248, s[64:67], s5 offen sc1
wt vmcnt(0)
v_cmp_eq_u64 vcc, v[250:251], v[252:253]
s_nop 1
s_bitcmp1_b32 vcc_lo, 5
s_cbranch_scc0 .La5_LO_NC5_L
s_branch .La5_LO_KC5
.La5_LO_NC6:
s_mov_b32 s93, 0
.La5_LO_NC6_L:
s_add_u32 s93, s93, 1
s_cmp_gt_u32 s93, 2097152
s_nop 0
s_nop 0
s_cbranch_scc1 .La5_LO_KC6
s_sleep 8
buffer_load_dwordx2 v[250:251], v248, s[64:67], s5 offen sc1
wt vmcnt(0)
v_cmp_eq_u64 vcc, v[250:251], v[252:253]
s_nop 1
s_bitcmp1_b32 vcc_lo, 6
s_cbranch_scc0 .La5_LO_NC6_L
s_branch .La5_LO_KC6
.La5_LO_NC7:
s_mov_b32 s93, 0
.La5_LO_NC7_L:
s_add_u32 s93, s93, 1
s_cmp_gt_u32 s93, 2097152
s_nop 0
s_nop 0
s_cbranch_scc1 .La5_LO_KC7
s_sleep 8
buffer_load_dwordx2 v[250:251], v248, s[64:67], s5 offen sc1
wt vmcnt(0)
v_cmp_eq_u64 vcc, v[250:251], v[252:253]
s_nop 1
s_bitcmp1_b32 vcc_lo, 7
s_cbranch_scc0 .La5_LO_NC7_L
s_branch .La5_LO_KC7
.La5_LA_SC3:
s_call_b64 s[24:25], .La5_LA_SLOW0
aw a0,0
aw a1,0
aw a2,0
aw a3,0
aw a4,0
aw a5,0
aw a6,0
aw a7,0
aw a8,0
aw a9,0
aw a10,0
aw a11,0
aw a12,0
aw a13,0
aw a14,0
aw a15,0
aw a16,0
aw a17,0
aw a18,0
aw a19,0
aw a20,0
aw a21,0
aw a22,0
aw a23,0
aw a24,0
aw a25,0
aw a26,0
aw a27,0
aw a28,0
aw a29,0
aw a30,0
aw a31,0
s_nop 1
v_mov_b32 v250, v242
v_mov_b32 v249, v243
v_xor_b32 v242, 64, v240
v_xor_b32 v243, 96, v240
ar v254,a229
s_mov_b32 s93, 0
ld a[128:131],v254,s[40:43],s93 offen
s_mov_b32 s93, 7168
ld a[132:135],v254,s[40:43],s93 offen
s_mov_b32 s93, 14336
ld a[136:139],v254,s[40:43],s93 offen
s_mov_b32 s93, 21504
ld a[140:143],v254,s[40:43],s93 offen
s_mov_b32 s93, 28672
ld a[144:147],v254,s[40:43],s93 offen
s_mov_b32 s93, 35840
ld a[148:151],v254,s[40:43],s93 offen
s_mov_b32 s93, 43008
ld a[152:155],v254,s[40:43],s93 offen
s_mov_b32 s93, 50176
ld a[156:159],v254,s[40:43],s93 offen
s_mov_b32 s93, 57344
ld a[160:163],v254,s[40:43],s93 offen
s_mov_b32 s93, 64512
ld a[164:167],v254,s[40:43],s93 offen
s_mov_b32 s93, 71680
ld a[168:171],v254,s[40:43],s93 offen
s_mov_b32 s93, 78848
ld a[172:175],v254,s[40:43],s93 offen
s_mov_b32 s93, 86016
ld a[176:179],v254,s[40:43],s93 offen
s_mov_b32 s93, 93184
ld a[180:183],v254,s[40:43],s93 offen
s_mov_b32 s93, 100352
ld a[184:187],v254,s[40:43],s93 offen
s_mov_b32 s93, 107520
ld a[188:191],v254,s[40:43],s93 offen
s_branch .La5_LA_SR3
.La5_LA_SC4:
s_call_b64 s[24:25], .La5_LA_SLOW1
aw a32,0
aw a33,0
aw a34,0
aw a35,0
aw a36,0
aw a37,0
aw a38,0
aw a39,0
aw a40,0
aw a41,0
aw a42,0
aw a43,0
aw a44,0
aw a45,0
aw a46,0
aw a47,0
aw a48,0
aw a49,0
aw a50,0
aw a51,0
aw a52,0
aw a53,0
aw a54,0
aw a55,0
aw a56,0
aw a57,0
aw a58,0
aw a59,0
aw a60,0
aw a61,0
aw a62,0
aw a63,0
s_nop 1
v_mov_b32 v250, v242
v_mov_b32 v249, v243
v_xor_b32 v242, 64, v240
v_xor_b32 v243, 96, v240
ar v254,a229
s_mov_b32 s93, 0
ld a[128:131],v254,s[40:43],s93 offen
s_mov_b32 s93, 7168
ld a[132:135],v254,s[40:43],s93 offen
s_mov_b32 s93, 14336
ld a[136:139],v254,s[40:43],s93 offen
s_mov_b32 s93, 21504
ld a[140:143],v254,s[40:43],s93 offen
s_mov_b32 s93, 28672
ld a[144:147],v254,s[40:43],s93 offen
s_mov_b32 s93, 35840
ld a[148:151],v254,s[40:43],s93 offen
s_mov_b32 s93, 43008
ld a[152:155],v254,s[40:43],s93 offen
s_mov_b32 s93, 50176
ld a[156:159],v254,s[40:43],s93 offen
s_mov_b32 s93, 57344
ld a[160:163],v254,s[40:43],s93 offen
s_mov_b32 s93, 64512
ld a[164:167],v254,s[40:43],s93 offen
s_mov_b32 s93, 71680
ld a[168:171],v254,s[40:43],s93 offen
s_mov_b32 s93, 78848
ld a[172:175],v254,s[40:43],s93 offen
s_mov_b32 s93, 86016
ld a[176:179],v254,s[40:43],s93 offen
s_mov_b32 s93, 93184
ld a[180:183],v254,s[40:43],s93 offen
s_mov_b32 s93, 100352
ld a[184:187],v254,s[40:43],s93 offen
s_mov_b32 s93, 107520
ld a[188:191],v254,s[40:43],s93 offen
s_branch .La5_LA_SR4
.La5_LO_VC1:
.La5_LW5_4:
s_cbranch_execz .La5_LW5_R0
.La5_LW6_8:
s_cbranch_execz .La5_LW6_R0
s_getpc_b64 s[24:25]
.La5_LW7_P0:
s_add_u32 s24, s24, .La5_LW7_R0-.La5_LW7_P0
s_addc_u32 s25, s25, 0
s_mov_b64 exec, 0
s_cmp_eq_u32 s3, 0
s_cbranch_scc1 .La5_LW7_0
s_cmp_eq_u32 s3, 1
s_cbranch_scc1 .La5_LW7_1
s_cmp_eq_u32 s3, 2
s_cbranch_scc1 .La5_LW7_2
s_branch .La5_LW7_3
.La5_LW7_R0:
s_mov_b64 exec, -1
s_branch .La5_LO_VE1
.La5_LO_W:
s_mov_b32 s93, 3670016
ld a[192:195],v228,s[40:43],s93 offen
s_mov_b32 s93, 3677184
ld a[196:199],v228,s[40:43],s93 offen
s_mov_b32 s93, 3684352
ld a[200:203],v228,s[40:43],s93 offen
s_mov_b32 s93, 3691520
ld a[204:207],v228,s[40:43],s93 offen
s_mov_b32 s93, 3698688
ld a[208:211],v228,s[40:43],s93 offen
s_mov_b32 s93, 3705856
ld a[212:215],v228,s[40:43],s93 offen
s_mov_b32 s93, 3713024
ld a[216:219],v228,s[40:43],s93 offen
s_mov_b32 s93, 3720192
ld a[220:223],v228,s[40:43],s93 offen
s_mov_b32 s93, 3727360
ld a[224:227],v228,s[40:43],s93 offen
s_mov_b32 s93, 3734528
ld a[228:231],v228,s[40:43],s93 offen
s_mov_b32 s93, 3741696
ld a[232:235],v228,s[40:43],s93 offen
s_mov_b32 s93, 3748864
ld a[236:239],v228,s[40:43],s93 offen
s_mov_b32 s93, 3756032
ld a[240:243],v228,s[40:43],s93 offen
s_mov_b32 s93, 3763200
ld a[244:247],v228,s[40:43],s93 offen
s_mov_b32 s93, 3770368
ld a[248:251],v228,s[40:43],s93 offen
s_mov_b32 s93, 3777536
ld a[252:255],v228,s[40:43],s93 offen
s_branch .La5_LO_START
.La5_LO_VC2:
s_getpc_b64 s[24:25]
.La5_LW7_P1:
s_add_u32 s24, s24, .La5_LW7_R1-.La5_LW7_P1
s_addc_u32 s25, s25, 0
s_mov_b64 exec, 0
s_cmp_eq_u32 s3, 0
s_cbranch_scc1 .La5_LW7_0
s_cmp_eq_u32 s3, 1
s_cbranch_scc1 .La5_LW7_1
s_cmp_eq_u32 s3, 2
s_cbranch_scc1 .La5_LW7_2
s_branch .La5_LW7_3
.La5_LW7_R1:
s_mov_b64 exec, -1
s_branch .La5_LO_VE2
.La5_LT_VAR:
v_mov_b32 v230, 0
v_lshlrev_b32 v232, 16, v96
v_and_b32 v233, 0xffff0000, v96
v_sub_f32 v232, v232, v244
v_sub_f32 v233, v233, v244
v_fmac_f32 v230, v232, v232
v_fmac_f32 v230, v233, v233
v_lshlrev_b32 v232, 16, v97
v_and_b32 v233, 0xffff0000, v97
v_sub_f32 v232, v232, v244
v_sub_f32 v233, v233, v244
v_fmac_f32 v230, v232, v232
v_fmac_f32 v230, v233, v233
v_lshlrev_b32 v232, 16, v98
v_and_b32 v233, 0xffff0000, v98
v_sub_f32 v232, v232, v244
v_sub_f32 v233, v233, v244
v_fmac_f32 v230, v232, v232
v_fmac_f32 v230, v233, v233
v_lshlrev_b32 v232, 16, v99
v_and_b32 v233, 0xffff0000, v99
v_sub_f32 v232, v232, v244
v_sub_f32 v233, v233, v244
v_fmac_f32 v230, v232, v232
v_fmac_f32 v230, v233, v233
v_lshlrev_b32 v232, 16, v100
v_and_b32 v233, 0xffff0000, v100
v_sub_f32 v232, v232, v244
v_sub_f32 v233, v233, v244
v_fmac_f32 v230, v232, v232
v_fmac_f32 v230, v233, v233
v_lshlrev_b32 v232, 16, v101
v_and_b32 v233, 0xffff0000, v101
v_sub_f32 v232, v232, v244
v_sub_f32 v233, v233, v244
v_fmac_f32 v230, v232, v232
v_fmac_f32 v230, v233, v233
v_lshlrev_b32 v232, 16, v102
v_and_b32 v233, 0xffff0000, v102
v_sub_f32 v232, v232, v244
v_sub_f32 v233, v233, v244
v_fmac_f32 v230, v232, v232
v_fmac_f32 v230, v233, v233
v_lshlrev_b32 v232, 16, v103
v_and_b32 v233, 0xffff0000, v103
v_sub_f32 v232, v232, v244
v_sub_f32 v233, v233, v244
v_fmac_f32 v230, v232, v232
v_fmac_f32 v230, v233, v233
v_lshlrev_b32 v232, 16, v104
v_and_b32 v233, 0xffff0000, v104
v_sub_f32 v232, v232, v244
v_sub_f32 v233, v233, v244
v_fmac_f32 v230, v232, v232
v_fmac_f32 v230, v233, v233
v_lshlrev_b32 v232, 16, v105
v_and_b32 v233, 0xffff0000, v105
v_sub_f32 v232, v232, v244
v_sub_f32 v233, v233, v244
v_fmac_f32 v230, v232, v232
v_fmac_f32 v230, v233, v233
v_lshlrev_b32 v232, 16, v106
v_and_b32 v233, 0xffff0000, v106
v_sub_f32 v232, v232, v244
v_sub_f32 v233, v233, v244
v_fmac_f32 v230, v232, v232
v_fmac_f32 v230, v233, v233
v_lshlrev_b32 v232, 16, v107
v_and_b32 v233, 0xffff0000, v107
v_sub_f32 v232, v232, v244
v_sub_f32 v233, v233, v244
v_fmac_f32 v230, v232, v232
v_fmac_f32 v230, v233, v233
v_lshlrev_b32 v232, 16, v108
v_and_b32 v233, 0xffff0000, v108
v_sub_f32 v232, v232, v244
v_sub_f32 v233, v233, v244
v_fmac_f32 v230, v232, v232
v_fmac_f32 v230, v233, v233
v_lshlrev_b32 v232, 16, v109
v_and_b32 v233, 0xffff0000, v109
v_sub_f32 v232, v232, v244
v_sub_f32 v233, v233, v244
v_fmac_f32 v230, v232, v232
v_fmac_f32 v230, v233, v233
v_lshlrev_b32 v232, 16, v110
v_and_b32 v233, 0xffff0000, v110
v_sub_f32 v232, v232, v244
v_sub_f32 v233, v233, v244
v_fmac_f32 v230, v232, v232
v_fmac_f32 v230, v233, v233
v_lshlrev_b32 v232, 16, v111
v_and_b32 v233, 0xffff0000, v111
v_sub_f32 v232, v232, v244
v_sub_f32 v233, v233, v244
v_fmac_f32 v230, v232, v232
v_fmac_f32 v230, v233, v233
s_nop 1
v_add_f32_dpp v230, v230, v230 quad_perm:[1,0,3,2] row_mask:0xf bank_mask:0xf
s_nop 1
v_add_f32_dpp v230, v230, v230 quad_perm:[2,3,0,1] row_mask:0xf bank_mask:0xf
s_nop 1
v_add_f32_dpp v230, v230, v230 row_ror:8 row_mask:0xf bank_mask:0xf
v_mov_b32 v231, v230
s_nop 1
v_permlane16_swap_b32 v231, v230
s_nop 1
v_add_f32 v230, v230, v231
v_mov_b32_e64 v231, v230
s_nop 1
v_permlane32_swap_b32 v231, v230
s_nop 1
v_add_f32_e64 v230, v230, v231
v_mul_f32 v230, 0x3a800000, v230
s_branch .La5_LT_VOK
.La5_LA_KSPIN:
s_mov_b32 s93, 0
.La5_LA_KSPIN_L:
s_add_u32 s93, s93, 1
s_cmp_gt_u32 s93, 2097152
s_cbranch_scc1 .La5_LA_KOK
s_sleep 8
buffer_load_dword v250, v251, s[64:67], s20 offen sc1
wt vmcnt(0)
v_cmp_eq_u32 vcc, v250, v249
s_nop 1
s_or_b32 vcc_hi, vcc_hi, s79
s_cmp_eq_u64 vcc, -1
s_cbranch_scc0 .La5_LA_KSPIN_L
s_branch .La5_LA_KOK
.La5_LA_QSPIN:
s_mov_b32 s93, 0
.La5_LA_QSPIN_L:
s_add_u32 s93, s93, 1
s_cmp_gt_u32 s93, 2097152
s_nop 0
s_cbranch_scc1 .La5_LA_QOK
s_sleep 8
buffer_load_dword v250, v251, s[64:67], s20 offen sc1
wt vmcnt(0)
v_cmp_eq_u32 vcc, v250, v249
s_nop 1
s_and_b32 s92, vcc_lo, 3
s_cmp_eq_u32 s92, 3
s_cbranch_scc0 .La5_LA_QSPIN_L
s_branch .La5_LA_QOK
.La5_LEND:
.La5_LCODEEND:
)ASM"
        :: "{s[0:1]}"(__builtin_amdgcn_kernarg_segment_ptr()), "{s2}"(blockIdx.x), "{s3}"(__builtin_amdgcn_readfirstlane(threadIdx.x >> 6)), "{s[4:5]}"(DBGP)
        : "v0","v1","v2","v3","v4","v5","v6","v7","v8","v9","v10","v11","v12","v13","v14","v15","v16","v17","v18","v19","v20","v21","v22","v23","v24","v25","v26","v27","v28","v29","v30","v31","v32","v33","v34","v35","v36","v37","v38","v39","v40","v41","v42","v43","v44","v45","v46","v47","v48","v49","v50","v51","v52","v53","v54","v55","v56","v57","v58","v59","v60","v61","v62","v63","v64","v65","v66","v67","v68","v69","v70","v71","v72","v73","v74","v75","v76","v77","v78","v79","v80","v81","v82","v83","v84","v85","v86","v87","v88","v89","v90","v91","v92","v93","v94","v95","v96","v97","v98","v99","v100","v101","v102","v103","v104","v105","v106","v107","v108","v109","v110","v111","v112","v113","v114","v115","v116","v117","v118","v119","v120","v121","v122","v123","v124","v125","v126","v127","v128","v129","v130","v131","v132","v133","v134","v135","v136","v137","v138","v139","v140","v141","v142","v143","v144","v145","v146","v147","v148","v149","v150","v151","v152","v153","v154","v155","v156","v157","v158","v159","v160","v161","v162","v163","v164","v165","v166","v167","v168","v169","v170","v171","v172","v173","v174","v175","v176","v177","v178","v179","v180","v181","v182","v183","v184","v185","v186","v187","v188","v189","v190","v191","v192","v193","v194","v195","v196","v197","v198","v199","v200","v201","v202","v203","v204","v205","v206","v207","v208","v209","v210","v211","v212","v213","v214","v215","v216","v217","v218","v219","v220","v221","v222","v223","v224","v225","v226","v227","v228","v229","v230","v231","v232","v233","v234","v235","v236","v237","v238","v239","v240","v241","v242","v243","v244","v245","v246","v247","v248","v249","v250","v251","v252","v253","v254","v255","a0","a1","a2","a3","a4","a5","a6","a7","a8","a9","a10","a11","a12","a13","a14","a15","a16","a17","a18","a19","a20","a21","a22","a23","a24","a25","a26","a27","a28","a29","a30","a31","a32","a33","a34","a35","a36","a37","a38","a39","a40","a41","a42","a43","a44","a45","a46","a47","a48","a49","a50","a51","a52","a53","a54","a55","a56","a57","a58","a59","a60","a61","a62","a63","a64","a65","a66","a67","a68","a69","a70","a71","a72","a73","a74","a75","a76","a77","a78","a79","a80","a81","a82","a83","a84","a85","a86","a87","a88","a89","a90","a91","a92","a93","a94","a95","a96","a97","a98","a99","a100","a101","a102","a103","a104","a105","a106","a107","a108","a109","a110","a111","a112","a113","a114","a115","a116","a117","a118","a119","a120","a121","a122","a123","a124","a125","a126","a127","a128","a129","a130","a131","a132","a133","a134","a135","a136","a137","a138","a139","a140","a141","a142","a143","a144","a145","a146","a147","a148","a149","a150","a151","a152","a153","a154","a155","a156","a157","a158","a159","a160","a161","a162","a163","a164","a165","a166","a167","a168","a169","a170","a171","a172","a173","a174","a175","a176","a177","a178","a179","a180","a181","a182","a183","a184","a185","a186","a187","a188","a189","a190","a191","a192","a193","a194","a195","a196","a197","a198","a199","a200","a201","a202","a203","a204","a205","a206","a207","a208","a209","a210","a211","a212","a213","a214","a215","a216","a217","a218","a219","a220","a221","a222","a223","a224","a225","a226","a227","a228","a229","a230","a231","a232","a233","a234","a235","a236","a237","a238","a239","a240","a241","a242","a243","a244","a245","a246","a247","a248","a249","a250","a251","a252","a253","a254","a255","s6","s7","s8","s9","s10","s11","s12","s13","s14","s15","s16","s17","s18","s19","s20","s21","s22","s23","s24","s25","s26","s27","s28","s29","s30","s31","s32","s33","s34","s35","s36","s37","s38","s39","s40","s41","s42","s43","s44","s45","s46","s47","s48","s49","s50","s51","s52","s53","s54","s55","s56","s57","s58","s59","s60","s61","s62","s63","s64","s65","s66","s67","s68","s69","s70","s71","s72","s73","s74","s75","s76","s77","s78","s79","s80","s81","s82","s83","s84","s85","s86","s87","s88","s89","s90","s91","s92","s93","s94","s95","s96","s97","s98","s99","s100","s101","vcc","scc","m0","memory");
}

extern "C" void kernel_launch(void* const* d_in, const int* in_sizes, int n_in, void* d_out, int out_size, void* d_ws, size_t ws_size,
                              hipStream_t stream) {
    (void)hipFuncSetAttribute((const void*)fa_kernel, hipFuncAttributeMaxDynamicSharedMemorySize, LDS_BYTES);
    fa_kernel<<<256, 256, LDS_BYTES, stream>>>((const bf16*)d_in[0], (const bf16*)d_in[1], (const bf16*)d_in[2], (const bf16*)d_in[3], (const bf16*)d_in[4], (char*)d_ws,
                                               (const bf16*)d_in[5], (const bf16*)d_in[8], (const bf16*)d_in[9], (const bf16*)d_in[10], (const bf16*)d_in[7], (bf16*)d_out);
}
```

```cpp
#include <hip/hip_runtime.h>
#include <hip/hip_bf16.h>
#include <stdint.h>
#pragma clang diagnostic ignored "-Winline-asm"
#pragma clang diagnostic ignored "-Woverlength-strings"

typedef __hip_bfloat16 bf16;
#define DBGP ((unsigned*)nullptr)
constexpr int LDS_BYTES = 163840;
extern "C" __global__ __attribute__((aligned(4096))) __launch_bounds__(256, 1) void fa_kernel(const bf16* __restrict__ px, const bf16* __restrict__ ckv, const bf16* __restrict__ wq,
                                                    const bf16* __restrict__ wuk, const bf16* __restrict__ wuv, char* pws,
                                                    const bf16* __restrict__ pwo, const bf16* __restrict__ pwg, const bf16* __restrict__ pwu,
                                                    const bf16* __restrict__ pwd, const bf16* __restrict__ pwr, bf16* __restrict__ pout) {
    asm volatile(R"ASM(
.macro mm x:vararg
v_mfma_f32_16x16x32_bf16 \x
.endm
.macro rt x:vararg
ds_read_b64_tr_b16 \x
.endm
.macro rd x:vararg
ds_read_b128 \x
.endm
.macro ld x:vararg
buffer_load_dwordx4 \x
.endm
.macro st x:vararg
buffer_store_dwordx4 \x
.endm
.macro ar x:vararg
v_accvgpr_read_b32 \x
.endm
.macro aw x:vararg
v_accvgpr_write_b32 \x
.endm
.macro cv x:vararg
v_cvt_pk_bf16_f32 \x
.endm
.macro ex x:vararg
v_exp_f32 \x
.endm
.macro wt x:vararg
s_waitcnt \x
.endm
; ---- roles: head = 4 xcd + (wg >> 6), j = (wg >> 3) & 7; odd j: block j >> 1 (then helps with block 7 - (j >> 1)), even j: block 7 - (j >> 1). First operands, addresses
.La5_LSTART:
s_load_dwordx16 s[16:31], s[0:1], 0
s_load_dwordx8 s[32:39], s[0:1], 64
.p2align 6
v_mbcnt_lo_u32_b32 v64, -1, 0
v_mbcnt_hi_u32_b32 v64, -1, v64
s_getpc_b64 s[92:93]
.La5_LPC:
s_sub_u32 s48, s92, .La5_LPC-.La5_LSTART
s_subb_u32 s49, s93, 0
s_and_b32 s49, s49, 0xffff
s_mov_b32 s50, .La5_LCODEEND-.La5_LSTART
s_mov_b32 s51, 0x20000
s_lshr_b32 s97, s2, 3
s_lshl2_add_u32 s87, s97, s3
v_lshlrev_b32 v133, 7, v64
s_lshl_b32 s87, s87, 13
buffer_load_dword v254, v133, s[48:51], s87 offen
s_and_b32 s92, s2, 7
s_lshr_b32 s93, s2, 6
s_lshl2_add_u32 s72, s92, s93
s_bfe_u32 s73, s2, 0x30003
s_lshr_b32 s74, s73, 1
s_xor_b32 s92, s74, 7
s_and_b32 s75, s73, 1
s_cselect_b32 s76, s74, s92
s_lshr_b32 s77, s3, 1
s_and_b32 s78, s3, 1
v_and_b32 v65, 15, v64
v_lshrrev_b32 v66, 4, v64
v_lshlrev_b32 v255, 4, v64
v_lshlrev_b32 v67, 1, v66
v_xor_b32 v67, v67, v65
v_lshlrev_b32 v67, 4, v67
v_lshl_add_u32 v67, v66, 13, v67
s_lshl_b32 s94, s78, 17
v_add_u32 v232, s94, v67
v_add_u32 v233, 32768, v232
v_add_u32 v234, 65536, v232
v_add_u32 v235, 98304, v232
v_and_b32 v67, 7, v64
v_xor_b32 v68, v67, v66
v_lshlrev_b32 v68, 4, v68
v_lshrrev_b32 v69, 3, v64
v_lshl_add_u32 v68, v69, 10, v68
s_lshl2_add_u32 s94, s3, s93
s_lshl_b32 s82, s94, 11
s_lshl_b32 s94, s94, 14
v_add_u32 v224, s94, v68
v_xor_b32 v225, 0x2040, v224
v_xor_b32 v226, 16384, v224
v_xor_b32 v227, 16384, v225
v_xor_b32 v228, 32768, v224
v_xor_b32 v229, 32768, v225
v_xor_b32 v230, 49152, v224
v_xor_b32 v231, 49152, v225
s_mul_i32 s94, s77, 8448
s_mul_i32 s95, s78, 4224
s_add_u32 s83, s94, s95
s_add_u32 s83, s83, 96256
v_lshlrev_b32 v135, 7, v67
v_lshl_add_u32 v135, v69, 13, v135
v_lshrrev_b32 v132, 5, v64
v_lshl_add_u32 v132, v132, 28, v135
v_lshl_add_u32 v133, v66, 28, v133
v_lshlrev_b32 v134, 10, v65
v_lshl_add_u32 v134, v66, 7, v134
v_lshrrev_b32 v68, 4, v64
v_lshl_add_u32 v134, v68, 28, v134
)ASM" R"ASM(s_lshl1_add_u32 s97, s97, s78
s_lshl_b32 s7, s97, 13
s_lshl_b32 s97, s97, 15
s_lshl_b32 s96, s93, 8
s_sub_u32 s7, s7, s96
s_sub_u32 s87, s87, s96
s_sub_u32 s97, s97, s96
s_add_u32 s86, s97, 2097152
s_lshl2_add_u32 s101, s72, s3
s_lshl_b32 s101, s101, 11
s_lshl2_add_u32 s100, s93, s3
s_lshl_b32 s100, s100, 14
v_lshlrev_b32 v118, 11, v67
s_lshl_b32 s93, s3, 9
v_lshl_add_u32 v119, v69, 4, v118
v_add_u32 v119, s93, v119
s_lshl_b32 s97, s2, 14
s_lshl_b32 s94, s72, 8
s_lshl_b32 s95, s76, 18
s_sub_u32 s46, 4194304, s94
s_mov_b32 s47, 0x20000
s_mov_b32 s42, 262144
s_mov_b32 s43, 0x20000
s_mov_b32 s62, 16384
s_mov_b32 s63, 0x20000
s_mov_b32 s79, 512
s_mov_b32 s80, 2097152
s_add_u32 s100, s100, 512
s_mov_b32 s81, 262144
s_cmp_eq_u32 s77, 0
wt lgkmcnt(0)
s_cselect_b64 s[92:93], s[22:23], s[24:25]
s_add_u32 s44, s92, s94
s_addc_u32 s45, s93, 0
s_and_b32 s45, s45, 0xffff
s_add_u32 s40, s18, s95
s_addc_u32 s41, s19, 0
s_and_b32 s41, s41, 0xffff
s_add_u32 s60, s16, s97
s_addc_u32 s61, s17, 0
s_and_b32 s61, s61, 0xffff
buffer_load_dword v254, v132, s[44:47], s86 offen
s_add_u32 s86, s86, 2097152
s_and_b32 s86, s86, 4194303
buffer_load_dword v254, v133, s[40:43], s101 offen
buffer_load_dword v254, v134, s[40:43], s100 offen
s_add_u32 m0, s83, 0
s_nop 0
ld v232,s[44:47],s80 offen lds
s_add_u32 m0, s83, 1024
s_nop 0
ld v233,s[44:47],s80 offen lds
s_add_u32 m0, s83, 2176
s_nop 0
ld v234,s[44:47],s80 offen lds
s_add_u32 m0, s83, 3200
s_nop 0
ld v235,s[44:47],s80 offen lds
s_add_u32 s80, s80, s81
s_and_b32 s80, s80, 4194303
v_lshrrev_b32 v67, 1, v65
v_xor_b32 v67, v67, v66
v_lshlrev_b32 v67, 4, v67
v_lshl_add_u32 v67, v65, 7, v67
s_lshl_b32 s92, s77, 12
v_add_u32 v236, s92, v67
v_xor_b32 v237, 64, v236
v_bfe_u32 v120, v64, 2, 2
v_add_u32 v67, 1, v66
v_lshrrev_b32 v67, 1, v67
v_lshlrev_b32 v67, 7, v67
v_lshl_add_u32 v67, v66, 11, v67
v_lshl_add_u32 v67, v120, 8, v67
v_and_b32 v68, 3, v64
v_lshl_add_u32 v67, v68, 3, v67
s_mul_i32 s92, s78, 8448
v_lshl_add_u32 v67, v120, 5, v67
v_add_u32 v67, s92, v67
v_add_u32 v240, 96256, v67
v_xor_b32 v241, 32, v240
v_xor_b32 v242, 64, v240
v_xor_b32 v243, 96, v240
s_add_u32 s64, s26, 142606336
s_addc_u32 s65, s27, 0
s_and_b32 s65, s65, 0xffff
s_mov_b32 s66, 3145728
s_mov_b32 s67, 0x20000
s_mov_b32 s88, 4096
s_lshr_b32 s92, s2, 1
s_lshl_b32 s92, s92, 15
s_and_b32 s93, s2, 1
s_lshl_b32 s93, s93, 7
s_add_u32 s92, s92, s93
s_lshl_b32 s93, s3, 13
s_add_u32 s92, s92, s93
s_add_u32 s92, s92, 88080384
s_add_u32 s56, s26, s92
s_addc_u32 s57, s27, 0
s_and_b32 s57, s57, 0xffff
s_mov_b32 s58, 8576
)ASM" R"ASM(s_mov_b32 s59, 0x20000
v_and_b32 v71, 0x380, v255
v_and_b32 v127, 0x70, v255
v_lshl_add_u32 v71, v71, 1, v127
v_xor_b32 v67, 3, v69
v_min_u32 v67, 1, v67
v_xor_b32 v67, 1, v67
s_mov_b32 s93, 0
v_mul_lo_u32 v67, v67, s93
v_add_u32 v136, v67, v255
v_and_b32 v67, 3, v69
v_xor_b32 v67, 1, v67
v_min_u32 v67, 1, v67
v_xor_b32 v67, 1, v67
v_and_b32 v67, s2, v67
v_mul_lo_u32 v67, v67, s93
v_add_u32 v71, v71, v67
s_lshl_b32 s92, s3, 3
v_lshlrev_b32 v126, 1, v127
v_or_b32 v126, 65536, v126
v_add_u32 v127, s92, v126
s_add_u32 s52, s26, 171966464
s_addc_u32 s53, s27, 0
s_and_b32 s53, s53, 0xffff
s_mov_b32 s54, 950272
s_mov_b32 s55, 0x20000
s_lshl2_add_u32 s95, s2, s3
s_lshl_b32 s91, s95, 3
s_lshl_b32 s95, s95, 10
s_lshl_b32 s89, s76, 10
v_lshl_add_u32 v135, v69, 28, v135
s_mov_b32 s96, s87
s_mov_b32 s84, 33792
s_mov_b32 s85, 0
s_add_u32 s97, s83, 67584
s_mov_b32 s90, s83
s_lshl_b32 s92, s72, 8
s_sub_u32 s93, 8388608, s92
s_add_u32 s68, s20, s92
s_addc_u32 s69, s21, 0
s_and_b32 s69, s69, 0xffff
s_mov_b32 s70, s93
s_mov_b32 s71, 0x20000
s_lshr_b32 s92, s20, 7
s_sub_u32 s92, 3, s92
s_bfe_u32 s99, s92, 0x20001
s_lshr_b32 s93, s2, 6
s_sub_u32 s99, s99, s93
s_lshl_b32 s92, s99, 8
s_lshr_b32 s93, s2, 3
s_lshl2_add_u32 s93, s93, s3
s_lshl_b32 s98, s93, 16
s_add_u32 s98, s98, s92
v_lshlrev_b32 v138, 4, v65
v_lshl_add_u32 v137, v66, 13, v138
v_lshl_add_u32 v138, v66, 9, v138
s_lshl_b32 s93, s93, 12
v_add_u32 v138, s93, v138
s_and_b32 s93, s2, 7
s_lshl_b32 s92, s93, 19
s_add_u32 s92, s92, 138412032
s_add_u32 s4, s26, s92
s_addc_u32 s5, s27, 0
s_and_b32 s5, s5, 0xffff
s_mov_b32 s6, 0
s_mov_b32 s100, 1073741824
s_add_u32 s101, s91, 65536
v_lshrrev_b32 v139, 1, v64
v_lshlrev_b32 v139, 8, v139
v_and_or_b32 v139, v255, 16, v139
s_lshl_b32 s93, s93, 5
s_add_u32 s93, s93, 64512
v_add_u32 v139, s93, v139
s_mov_b64 exec, 0
s_cmp_eq_u32 s3, 0
s_cbranch_scc1 .La5_LW1_0
s_cmp_eq_u32 s3, 1
s_cbranch_scc1 .La5_LW1_1
s_cmp_eq_u32 s3, 2
s_cbranch_scc1 .La5_LW1_2
s_branch .La5_LW1_3
.La5_LW1_R0:
s_mov_b64 exec, -1
s_xor_b32 m0, s82, 0
s_nop 0
ld v224,s[40:43],s79 offen lds
s_xor_b32 m0, s82, 1024
s_nop 0
ld v225,s[40:43],s79 offen lds
s_xor_b32 m0, s82, 2048
s_nop 0
ld v226,s[40:43],s79 offen lds
s_xor_b32 m0, s82, 3072
s_nop 0
ld v227,s[40:43],s79 offen lds
s_xor_b32 m0, s82, 4096
s_nop 0
ld v228,s[40:43],s79 offen lds
s_xor_b32 m0, s82, 5120
s_nop 0
)ASM" R"ASM(ld v229,s[40:43],s79 offen lds
s_xor_b32 m0, s82, 6144
s_nop 0
ld v230,s[40:43],s79 offen lds
s_xor_b32 m0, s82, 7168
s_nop 0
ld v231,s[40:43],s79 offen lds
s_add_u32 s79, s79, 128
s_and_b32 s79, s79, 1023
s_add_u32 m0, s83, 16896
s_nop 0
ld v232,s[44:47],s80 offen lds
s_add_u32 m0, s83, 17920
s_nop 0
ld v233,s[44:47],s80 offen lds
s_add_u32 m0, s83, 19072
s_nop 0
ld v234,s[44:47],s80 offen lds
s_add_u32 m0, s83, 20096
s_nop 0
ld v235,s[44:47],s80 offen lds
s_add_u32 s80, s80, s81
s_and_b32 s80, s80, 4194303
buffer_load_dwordx2 v[252:253], off, s[64:67], 0 sc1
wt vmcnt(1)
s_barrier
rd v[160:163],v236 offset:24576
rd v[164:167],v237 offset:24576
rt v[192:193],v240 offset:0
rt v[194:195],v240 offset:1024
rt v[196:197],v240 offset:16896
rt v[198:199],v240 offset:17920
rt v[200:201],v241 offset:0
rt v[202:203],v241 offset:1024
rt v[204:205],v241 offset:16896
rt v[206:207],v241 offset:17920
rt v[208:209],v242 offset:0
rt v[210:211],v242 offset:1024
rt v[212:213],v242 offset:16896
rt v[214:215],v242 offset:17920
rt v[216:217],v243 offset:0
rt v[218:219],v243 offset:1024
rt v[220:221],v243 offset:16896
rt v[222:223],v243 offset:17920
rd v[168:171],v236 offset:26624
rd v[172:175],v237 offset:26624
rd v[176:179],v236 offset:16384
rd v[180:183],v237 offset:16384
rd v[184:187],v236 offset:18432
rd v[188:191],v237 offset:18432
wt lgkmcnt(0)
s_cmp_eq_u32 s78, 1
s_cbranch_scc1 .La5_LV_START
; ---- K tile (waves 0, 2): 256 keys x 128 columns, K = 512; LayerNorm of 8 rows of x in the gaps (softmax scale folded in)
.La5_LK_START:
mm a[96:99],v[192:195],v[160:163],0
s_add_u32 m0, s83, 33792
mm a[96:99],v[196:199],v[164:167],a[96:99]
ld v232,s[44:47],s80 offen lds
mm a[100:103],v[204:207],v[164:167],0
rd v[0:3],v236 offset:8192
rd v[4:7],v237 offset:8192
s_add_u32 m0, s83, 34816
mm a[100:103],v[200:203],v[160:163],a[100:103]
rd v[8:11],v236 offset:10240
rd v[12:15],v237 offset:10240
ld v233,s[44:47],s80 offen lds
mm a[104:107],v[208:211],v[160:163],0
s_add_u32 m0, s83, 35968
mm a[104:107],v[212:215],v[164:167],a[104:107]
ld v234,s[44:47],s80 offen lds
mm a[108:111],v[220:223],v[164:167],0
rd v[16:19],v236 offset:0
rd v[20:23],v237 offset:0
s_add_u32 m0, s83, 36992
mm a[108:111],v[216:219],v[160:163],a[108:111]
rd v[24:27],v236 offset:2048
rd v[28:31],v237 offset:2048
ld v235,s[44:47],s80 offen lds
mm a[140:143],v[216:219],v[168:171],0
s_mov_b32 s80, 2883584
mm a[140:143],v[220:223],v[172:175],a[140:143]
mm a[136:139],v[212:215],v[172:175],0
s_xor_b32 m0, s82, 32768
mm a[136:139],v[208:211],v[168:171],a[136:139]
rt v[32:33],v240 offset:128
rt v[34:35],v240 offset:1152
ld v224,s[40:43],s79 offen lds
mm a[124:127],v[200:203],v[168:171],0
s_xor_b32 m0, s82, 33792
mm a[124:127],v[204:207],v[172:175],a[124:127]
ld v225,s[40:43],s79 offen lds
mm a[120:123],v[196:199],v[172:175],0
s_xor_b32 m0, s82, 34816
mm a[120:123],v[192:195],v[168:171],a[120:123]
rt v[36:37],v240 offset:17024
rt v[38:39],v240 offset:18048
ld v226,s[40:43],s79 offen lds
mm a[232:235],v[192:195],v[176:179],0
s_xor_b32 m0, s82, 35840
)ASM" R"ASM(mm a[232:235],v[196:199],v[180:183],a[232:235]
ld v227,s[40:43],s79 offen lds
mm a[236:239],v[204:207],v[180:183],0
s_xor_b32 m0, s82, 36864
mm a[236:239],v[200:203],v[176:179],a[236:239]
rt v[40:41],v241 offset:128
rt v[42:43],v241 offset:1152
ld v228,s[40:43],s79 offen lds
mm a[152:155],v[208:211],v[176:179],0
s_xor_b32 m0, s82, 37888
mm a[152:155],v[212:215],v[180:183],a[152:155]
ld v229,s[40:43],s79 offen lds
mm a[156:159],v[220:223],v[180:183],0
s_xor_b32 m0, s82, 38912
mm a[156:159],v[216:219],v[176:179],a[156:159]
ld v230,s[40:43],s79 offen lds
mm a[172:175],v[216:219],v[184:187],0
s_xor_b32 m0, s82, 39936
mm a[172:175],v[220:223],v[188:191],a[172:175]
ld v231,s[40:43],s79 offen lds
mm a[168:171],v[212:215],v[188:191],0
s_mov_b32 s79, 768
mm a[168:171],v[208:211],v[184:187],a[168:171]
rt v[44:45],v241 offset:17024
rt v[46:47],v241 offset:18048
mm a[220:223],v[200:203],v[184:187],0
s_add_u32 m0, s83, 50688
mm a[220:223],v[204:207],v[188:191],a[220:223]
ld v232,s[44:47],s80 offen lds
mm a[216:219],v[196:199],v[188:191],0
wt lgkmcnt(8)
s_add_u32 m0, s83, 51712
mm a[216:219],v[192:195],v[184:187],a[216:219]
ld v233,s[44:47],s80 offen lds
mm a[200:203],v[192:195],v[0:3],0
s_add_u32 m0, s83, 52864
mm a[200:203],v[196:199],v[4:7],a[200:203]
ld v234,s[44:47],s80 offen lds
mm a[204:207],v[204:207],v[4:7],0
s_add_u32 m0, s83, 53888
mm a[204:207],v[200:203],v[0:3],a[204:207]
rt v[48:49],v242 offset:128
rt v[50:51],v242 offset:1152
ld v235,s[44:47],s80 offen lds
mm a[184:187],v[208:211],v[0:3],0
s_mov_b32 s80, 3145728
mm a[184:187],v[212:215],v[4:7],a[184:187]
mm a[188:191],v[220:223],v[4:7],0
mm a[188:191],v[216:219],v[0:3],a[188:191]
mm a[12:15],v[216:219],v[8:11],0
mm a[12:15],v[220:223],v[12:15],a[12:15]
buffer_load_dword v70, v118, s[60:63], 0 offen
mm a[8:11],v[212:215],v[12:15],0
mm a[8:11],v[208:211],v[8:11],a[8:11]
rt v[52:53],v242 offset:17024
rt v[54:55],v242 offset:18048
mm a[76:79],v[200:203],v[8:11],0
mm a[76:79],v[204:207],v[12:15],a[76:79]
ld v[72:75],v119,s[60:63],0 offen offset:0
mm a[72:75],v[196:199],v[12:15],0
mm a[72:75],v[192:195],v[8:11],a[72:75]
rt v[56:57],v243 offset:128
rt v[58:59],v243 offset:1152
mm a[88:91],v[192:195],v[16:19],0
mm a[88:91],v[196:199],v[20:23],a[88:91]
ld v[76:79],v119,s[60:63],0 offen offset:128
mm a[92:95],v[204:207],v[20:23],0
mm a[92:95],v[200:203],v[16:19],a[92:95]
rt v[60:61],v243 offset:17024
rt v[62:63],v243 offset:18048
mm a[24:27],v[208:211],v[16:19],0
mm a[24:27],v[212:215],v[20:23],a[24:27]
ld v[80:83],v119,s[60:63],0 offen offset:256
mm a[28:31],v[220:223],v[20:23],0
mm a[28:31],v[216:219],v[16:19],a[28:31]
mm a[44:47],v[216:219],v[24:27],0
mm a[44:47],v[220:223],v[28:31],a[44:47]
ld v[84:87],v119,s[60:63],0 offen offset:384
mm a[40:43],v[212:215],v[28:31],0
mm a[40:43],v[208:211],v[24:27],a[40:43]
mm a[252:255],v[200:203],v[24:27],0
mm a[252:255],v[204:207],v[28:31],a[252:255]
mm a[248:251],v[196:199],v[28:31],0
mm a[248:251],v[192:195],v[24:27],a[248:251]
s_mov_b64 exec, 0
s_cmp_eq_u32 s3, 0
s_cbranch_scc1 .La5_LW2_0
s_branch .La5_LW2_R0
.La5_LW2_R0:
s_mov_b64 exec, -1
wt vmcnt(5) lgkmcnt(0)
s_barrier
mm a[112:115],v[32:35],v[160:163],0
mm a[112:115],v[36:39],v[164:167],a[112:115]
mm a[116:119],v[44:47],v[164:167],0
rt v[192:193],v240 offset:33792
rt v[194:195],v240 offset:34816
mm a[116:119],v[40:43],v[160:163],a[116:119]
rt v[196:197],v240 offset:50688
rt v[198:199],v240 offset:51712
)ASM" R"ASM(mm a[128:131],v[48:51],v[160:163],0
mm a[128:131],v[52:55],v[164:167],a[128:131]
mm a[132:135],v[60:63],v[164:167],0
mm a[132:135],v[56:59],v[160:163],a[132:135]
rd v[160:163],v236 offset:57344
rd v[164:167],v237 offset:57344
mm a[148:151],v[56:59],v[168:171],0
mm a[148:151],v[60:63],v[172:175],a[148:151]
mm a[144:147],v[52:55],v[172:175],0
mm a[144:147],v[48:51],v[168:171],a[144:147]
rt v[200:201],v241 offset:33792
rt v[202:203],v241 offset:34816
mm a[228:231],v[40:43],v[168:171],0
mm a[228:231],v[44:47],v[172:175],a[228:231]
mm a[224:227],v[36:39],v[172:175],0
mm a[224:227],v[32:35],v[168:171],a[224:227]
rd v[168:171],v236 offset:59392
rd v[172:175],v237 offset:59392
mm a[208:211],v[32:35],v[176:179],0
s_xor_b32 m0, s82, 0
mm a[208:211],v[36:39],v[180:183],a[208:211]
ld v224,s[40:43],s79 offen lds
mm a[212:215],v[44:47],v[180:183],0
mm a[212:215],v[40:43],v[176:179],a[212:215]
rt v[204:205],v241 offset:50688
rt v[206:207],v241 offset:51712
s_xor_b32 m0, s82, 1024
mm a[160:163],v[48:51],v[176:179],0
ld v225,s[40:43],s79 offen lds
mm a[160:163],v[52:55],v[180:183],a[160:163]
mm a[164:167],v[60:63],v[180:183],0
s_xor_b32 m0, s82, 2048
mm a[164:167],v[56:59],v[176:179],a[164:167]
rd v[176:179],v236 offset:49152
rd v[180:183],v237 offset:49152
ld v226,s[40:43],s79 offen lds
mm a[180:183],v[56:59],v[184:187],0
mm a[180:183],v[60:63],v[188:191],a[180:183]
s_xor_b32 m0, s82, 3072
mm a[176:179],v[52:55],v[188:191],0
ld v227,s[40:43],s79 offen lds
mm a[176:179],v[48:51],v[184:187],a[176:179]
rt v[208:209],v242 offset:33792
rt v[210:211],v242 offset:34816
mm a[196:199],v[40:43],v[184:187],0
s_xor_b32 m0, s82, 4096
mm a[196:199],v[44:47],v[188:191],a[196:199]
ld v228,s[40:43],s79 offen lds
mm a[192:195],v[36:39],v[188:191],0
mm a[192:195],v[32:35],v[184:187],a[192:195]
rd v[184:187],v236 offset:51200
rd v[188:191],v237 offset:51200
s_xor_b32 m0, s82, 5120
mm a[64:67],v[32:35],v[0:3],0
ld v229,s[40:43],s79 offen lds
mm a[64:67],v[36:39],v[4:7],a[64:67]
mm a[68:71],v[44:47],v[4:7],0
s_xor_b32 m0, s82, 6144
mm a[68:71],v[40:43],v[0:3],a[68:71]
rt v[212:213],v242 offset:50688
rt v[214:215],v242 offset:51712
ld v230,s[40:43],s79 offen lds
mm a[0:3],v[48:51],v[0:3],0
mm a[0:3],v[52:55],v[4:7],a[0:3]
s_xor_b32 m0, s82, 7168
mm a[4:7],v[60:63],v[4:7],0
ld v231,s[40:43],s79 offen lds
mm a[4:7],v[56:59],v[0:3],a[4:7]
rd v[0:3],v236 offset:40960
rd v[4:7],v237 offset:40960
s_mov_b32 s79, 896
mm a[20:23],v[56:59],v[8:11],0
mm a[20:23],v[60:63],v[12:15],a[20:23]
s_add_u32 m0, s83, 0
mm a[16:19],v[52:55],v[12:15],0
ld v232,s[44:47],s80 offen lds
mm a[16:19],v[48:51],v[8:11],a[16:19]
rt v[216:217],v243 offset:33792
rt v[218:219],v243 offset:34816
mm a[84:87],v[40:43],v[8:11],0
s_add_u32 m0, s83, 1024
mm a[84:87],v[44:47],v[12:15],a[84:87]
ld v233,s[44:47],s80 offen lds
mm a[80:83],v[36:39],v[12:15],0
wt vmcnt(10)
v_lshlrev_b32 v70, 16, v70
mm a[80:83],v[32:35],v[8:11],a[80:83]
rd v[8:11],v236 offset:43008
rd v[12:15],v237 offset:43008
s_add_u32 m0, s83, 2176
mm a[240:243],v[32:35],v[16:19],0
ld v234,s[44:47],s80 offen lds
mm a[240:243],v[36:39],v[20:23],a[240:243]
v_lshlrev_b32 v88, 16, v72
v_and_b32 v89, 0xffff0000, v72
mm a[244:247],v[44:47],v[20:23],0
s_add_u32 m0, s83, 3200
v_sub_f32 v88, v88, v70
mm a[244:247],v[40:43],v[16:19],a[244:247]
rt v[220:221],v243 offset:50688
)ASM" R"ASM(rt v[222:223],v243 offset:51712
ld v235,s[44:47],s80 offen lds
mm a[32:35],v[48:51],v[16:19],0
s_mov_b32 s80, 3407872
v_sub_f32 v89, v89, v70
mm a[32:35],v[52:55],v[20:23],a[32:35]
v_add_f32 v120, v88, v89
v_mul_f32 v122, v88, v88
mm a[36:39],v[60:63],v[20:23],0
s_add_u32 m0, s83, 16896
v_mul_f32 v123, v89, v89
mm a[36:39],v[56:59],v[16:19],a[36:39]
rd v[16:19],v236 offset:32768
rd v[20:23],v237 offset:32768
ld v232,s[44:47],s80 offen lds
mm a[60:63],v[56:59],v[24:27],0
v_lshlrev_b32 v90, 16, v73
v_and_b32 v91, 0xffff0000, v73
mm a[60:63],v[60:63],v[28:31],a[60:63]
s_add_u32 m0, s83, 17920
v_sub_f32 v90, v90, v70
mm a[56:59],v[52:55],v[28:31],0
ld v233,s[44:47],s80 offen lds
mm a[56:59],v[48:51],v[24:27],a[56:59]
v_sub_f32 v91, v91, v70
v_add_f32 v124, v90, v91
mm a[52:55],v[40:43],v[24:27],0
s_add_u32 m0, s83, 19072
v_fmac_f32 v122, v90, v90
mm a[52:55],v[44:47],v[28:31],a[52:55]
ld v234,s[44:47],s80 offen lds
mm a[48:51],v[36:39],v[28:31],0
wt lgkmcnt(2)
v_add_f32 v120, v120, v124
mm a[48:51],v[32:35],v[24:27],a[48:51]
rd v[24:27],v236 offset:34816
rd v[28:31],v237 offset:34816
s_add_u32 m0, s83, 20096
mm a[96:99],v[192:195],v[160:163],a[96:99]
ld v235,s[44:47],s80 offen lds
.La5_LW1_0:
s_cbranch_execz .La5_LW1_4
mm a[96:99],v[196:199],v[164:167],a[96:99]
s_mov_b32 s80, 3670016
v_fmac_f32 v123, v91, v91
mm a[100:103],v[204:207],v[164:167],a[100:103]
v_lshlrev_b32 v92, 16, v74
v_and_b32 v93, 0xffff0000, v74
mm a[100:103],v[200:203],v[160:163],a[100:103]
rt v[32:33],v240 offset:33920
rt v[34:35],v240 offset:34944
mm a[104:107],v[208:211],v[160:163],a[104:107]
v_sub_f32 v92, v92, v70
v_sub_f32 v93, v93, v70
mm a[104:107],v[212:215],v[164:167],a[104:107]
v_add_f32 v124, v92, v93
v_fmac_f32 v122, v92, v92
mm a[108:111],v[220:223],v[164:167],a[108:111]
wt lgkmcnt(2)
s_barrier
mm a[108:111],v[216:219],v[160:163],a[108:111]
v_add_f32 v120, v120, v124
v_fmac_f32 v123, v93, v93
mm a[140:143],v[216:219],v[168:171],a[140:143]
s_xor_b32 m0, s82, 32768
v_lshlrev_b32 v94, 16, v75
mm a[140:143],v[220:223],v[172:175],a[140:143]
ld v224,s[40:43],s79 offen lds
mm a[136:139],v[212:215],v[172:175],a[136:139]
v_and_b32 v95, 0xffff0000, v75
v_sub_f32 v94, v94, v70
mm a[136:139],v[208:211],v[168:171],a[136:139]
rt v[36:37],v240 offset:50816
rt v[38:39],v240 offset:51840
mm a[124:127],v[200:203],v[168:171],a[124:127]
s_xor_b32 m0, s82, 33792
v_sub_f32 v95, v95, v70
mm a[124:127],v[204:207],v[172:175],a[124:127]
ld v225,s[40:43],s79 offen lds
mm a[120:123],v[196:199],v[172:175],a[120:123]
v_add_f32 v124, v94, v95
v_fmac_f32 v122, v94, v94
mm a[120:123],v[192:195],v[168:171],a[120:123]
v_add_f32 v120, v120, v124
v_fmac_f32 v123, v95, v95
mm a[232:235],v[192:195],v[176:179],a[232:235]
s_xor_b32 m0, s82, 34816
v_lshlrev_b32 v96, 16, v76
mm a[232:235],v[196:199],v[180:183],a[232:235]
ld v226,s[40:43],s79 offen lds
mm a[236:239],v[204:207],v[180:183],a[236:239]
v_and_b32 v97, 0xffff0000, v76
v_sub_f32 v96, v96, v70
mm a[236:239],v[200:203],v[176:179],a[236:239]
rt v[40:41],v241 offset:33920
rt v[42:43],v241 offset:34944
mm a[152:155],v[208:211],v[176:179],a[152:155]
s_xor_b32 m0, s82, 35840
v_sub_f32 v97, v97, v70
mm a[152:155],v[212:215],v[180:183],a[152:155]
)ASM" R"ASM(ld v227,s[40:43],s79 offen lds
mm a[156:159],v[220:223],v[180:183],a[156:159]
v_add_f32 v124, v96, v97
v_fmac_f32 v122, v96, v96
mm a[156:159],v[216:219],v[176:179],a[156:159]
v_add_f32 v120, v120, v124
v_fmac_f32 v123, v97, v97
mm a[172:175],v[216:219],v[184:187],a[172:175]
s_xor_b32 m0, s82, 36864
v_lshlrev_b32 v98, 16, v77
mm a[172:175],v[220:223],v[188:191],a[172:175]
ld v228,s[40:43],s79 offen lds
mm a[168:171],v[212:215],v[188:191],a[168:171]
v_and_b32 v99, 0xffff0000, v77
v_sub_f32 v98, v98, v70
mm a[168:171],v[208:211],v[184:187],a[168:171]
rt v[44:45],v241 offset:50816
rt v[46:47],v241 offset:51840
mm a[220:223],v[200:203],v[184:187],a[220:223]
s_xor_b32 m0, s82, 37888
v_sub_f32 v99, v99, v70
mm a[220:223],v[204:207],v[188:191],a[220:223]
ld v229,s[40:43],s79 offen lds
mm a[216:219],v[196:199],v[188:191],a[216:219]
v_add_f32 v124, v98, v99
v_fmac_f32 v122, v98, v98
mm a[216:219],v[192:195],v[184:187],a[216:219]
v_add_f32 v120, v120, v124
v_fmac_f32 v123, v99, v99
mm a[200:203],v[192:195],v[0:3],a[200:203]
s_xor_b32 m0, s82, 38912
v_lshlrev_b32 v100, 16, v78
mm a[200:203],v[196:199],v[4:7],a[200:203]
ld v230,s[40:43],s79 offen lds
mm a[204:207],v[204:207],v[4:7],a[204:207]
v_and_b32 v101, 0xffff0000, v78
v_sub_f32 v100, v100, v70
mm a[204:207],v[200:203],v[0:3],a[204:207]
rt v[48:49],v242 offset:33920
rt v[50:51],v242 offset:34944
mm a[184:187],v[208:211],v[0:3],a[184:187]
s_xor_b32 m0, s82, 39936
v_sub_f32 v101, v101, v70
mm a[184:187],v[212:215],v[4:7],a[184:187]
ld v231,s[40:43],s79 offen lds
mm a[188:191],v[220:223],v[4:7],a[188:191]
s_mov_b32 s79, 0
v_add_f32 v124, v100, v101
mm a[188:191],v[216:219],v[0:3],a[188:191]
v_fmac_f32 v122, v100, v100
v_add_f32 v120, v120, v124
mm a[12:15],v[216:219],v[8:11],a[12:15]
v_fmac_f32 v123, v101, v101
v_lshlrev_b32 v102, 16, v79
mm a[12:15],v[220:223],v[12:15],a[12:15]
buffer_load_dword v254, v132, s[44:47], s86 offen
mm a[8:11],v[212:215],v[12:15],a[8:11]
s_add_u32 s86, s86, 2097152
s_and_b32 s86, s86, 4194303
mm a[8:11],v[208:211],v[8:11],a[8:11]
rt v[52:53],v242 offset:50816
rt v[54:55],v242 offset:51840
mm a[76:79],v[200:203],v[8:11],a[76:79]
v_and_b32 v103, 0xffff0000, v79
v_sub_f32 v102, v102, v70
mm a[76:79],v[204:207],v[12:15],a[76:79]
v_sub_f32 v103, v103, v70
v_add_f32 v124, v102, v103
mm a[72:75],v[196:199],v[12:15],a[72:75]
v_fmac_f32 v122, v102, v102
v_add_f32 v120, v120, v124
mm a[72:75],v[192:195],v[8:11],a[72:75]
rt v[56:57],v243 offset:33920
rt v[58:59],v243 offset:34944
mm a[88:91],v[192:195],v[16:19],a[88:91]
v_fmac_f32 v123, v103, v103
v_lshlrev_b32 v104, 16, v80
mm a[88:91],v[196:199],v[20:23],a[88:91]
v_and_b32 v105, 0xffff0000, v80
v_sub_f32 v104, v104, v70
mm a[92:95],v[204:207],v[20:23],a[92:95]
v_sub_f32 v105, v105, v70
v_add_f32 v124, v104, v105
mm a[92:95],v[200:203],v[16:19],a[92:95]
rt v[60:61],v243 offset:50816
rt v[62:63],v243 offset:51840
mm a[24:27],v[208:211],v[16:19],a[24:27]
v_fmac_f32 v122, v104, v104
v_add_f32 v120, v120, v124
mm a[24:27],v[212:215],v[20:23],a[24:27]
v_fmac_f32 v123, v105, v105
v_lshlrev_b32 v106, 16, v81
mm a[28:31],v[220:223],v[20:23],a[28:31]
v_and_b32 v107, 0xffff0000, v81
v_sub_f32 v106, v106, v70
mm a[28:31],v[216:219],v[16:19],a[28:31]
v_sub_f32 v107, v107, v70
v_add_f32 v124, v106, v107
mm a[44:47],v[216:219],v[24:27],a[44:47]
v_fmac_f32 v122, v106, v106
)ASM" R"ASM(v_add_f32 v120, v120, v124
mm a[44:47],v[220:223],v[28:31],a[44:47]
v_fmac_f32 v123, v107, v107
v_lshlrev_b32 v108, 16, v82
mm a[40:43],v[212:215],v[28:31],a[40:43]
v_and_b32 v109, 0xffff0000, v82
v_sub_f32 v108, v108, v70
mm a[40:43],v[208:211],v[24:27],a[40:43]
v_sub_f32 v109, v109, v70
v_add_f32 v124, v108, v109
mm a[252:255],v[200:203],v[24:27],a[252:255]
v_fmac_f32 v122, v108, v108
v_add_f32 v120, v120, v124
mm a[252:255],v[204:207],v[28:31],a[252:255]
v_fmac_f32 v123, v109, v109
v_lshlrev_b32 v110, 16, v83
mm a[248:251],v[196:199],v[28:31],a[248:251]
wt vmcnt(9) lgkmcnt(0)
s_barrier
mm a[248:251],v[192:195],v[24:27],a[248:251]
v_and_b32 v111, 0xffff0000, v83
v_sub_f32 v110, v110, v70
mm a[112:115],v[32:35],v[160:163],a[112:115]
v_sub_f32 v111, v111, v70
v_add_f32 v124, v110, v111
mm a[112:115],v[36:39],v[164:167],a[112:115]
v_fmac_f32 v122, v110, v110
v_add_f32 v120, v120, v124
mm a[116:119],v[44:47],v[164:167],a[116:119]
rt v[192:193],v240 offset:0
rt v[194:195],v240 offset:1024
mm a[116:119],v[40:43],v[160:163],a[116:119]
rt v[196:197],v240 offset:16896
rt v[198:199],v240 offset:17920
mm a[128:131],v[48:51],v[160:163],a[128:131]
v_fmac_f32 v123, v111, v111
v_lshlrev_b32 v112, 16, v84
mm a[128:131],v[52:55],v[164:167],a[128:131]
v_and_b32 v113, 0xffff0000, v84
v_sub_f32 v112, v112, v70
mm a[132:135],v[60:63],v[164:167],a[132:135]
v_sub_f32 v113, v113, v70
v_add_f32 v124, v112, v113
mm a[132:135],v[56:59],v[160:163],a[132:135]
rd v[160:163],v236 offset:24576
rd v[164:167],v237 offset:24576
mm a[148:151],v[56:59],v[168:171],a[148:151]
v_fmac_f32 v122, v112, v112
v_add_f32 v120, v120, v124
mm a[148:151],v[60:63],v[172:175],a[148:151]
v_fmac_f32 v123, v113, v113
v_lshlrev_b32 v114, 16, v85
mm a[144:147],v[52:55],v[172:175],a[144:147]
v_and_b32 v115, 0xffff0000, v85
v_sub_f32 v114, v114, v70
mm a[144:147],v[48:51],v[168:171],a[144:147]
rt v[200:201],v241 offset:0
rt v[202:203],v241 offset:1024
mm a[228:231],v[40:43],v[168:171],a[228:231]
v_sub_f32 v115, v115, v70
v_add_f32 v124, v114, v115
mm a[228:231],v[44:47],v[172:175],a[228:231]
v_fmac_f32 v122, v114, v114
v_add_f32 v120, v120, v124
mm a[224:227],v[36:39],v[172:175],a[224:227]
v_fmac_f32 v123, v115, v115
v_lshlrev_b32 v116, 16, v86
mm a[224:227],v[32:35],v[168:171],a[224:227]
rd v[168:171],v236 offset:26624
rd v[172:175],v237 offset:26624
mm a[208:211],v[32:35],v[176:179],a[208:211]
v_and_b32 v117, 0xffff0000, v86
v_sub_f32 v116, v116, v70
mm a[208:211],v[36:39],v[180:183],a[208:211]
v_sub_f32 v117, v117, v70
v_add_f32 v124, v116, v117
mm a[212:215],v[44:47],v[180:183],a[212:215]
v_fmac_f32 v122, v116, v116
v_add_f32 v120, v120, v124
mm a[212:215],v[40:43],v[176:179],a[212:215]
rt v[204:205],v241 offset:16896
rt v[206:207],v241 offset:17920
mm a[160:163],v[48:51],v[176:179],a[160:163]
v_fmac_f32 v123, v117, v117
v_lshlrev_b32 v118, 16, v87
mm a[160:163],v[52:55],v[180:183],a[160:163]
v_and_b32 v119, 0xffff0000, v87
v_sub_f32 v118, v118, v70
mm a[164:167],v[60:63],v[180:183],a[164:167]
v_sub_f32 v119, v119, v70
v_add_f32 v124, v118, v119
mm a[164:167],v[56:59],v[176:179],a[164:167]
rd v[176:179],v236 offset:16384
rd v[180:183],v237 offset:16384
mm a[180:183],v[56:59],v[184:187],a[180:183]
v_fmac_f32 v122, v118, v118
v_add_f32 v120, v120, v124
mm a[180:183],v[60:63],v[188:191],a[180:183]
v_fmac_f32 v123, v119, v119
v_add_f32 v121, v122, v123
)ASM" R"ASM(mm a[176:179],v[52:55],v[188:191],a[176:179]
v_add_f32_dpp v120, v120, v120 row_ror:8 row_mask:0xf bank_mask:0xf
mm a[176:179],v[48:51],v[184:187],a[176:179]
rt v[208:209],v242 offset:0
rt v[210:211],v242 offset:1024
mm a[196:199],v[40:43],v[184:187],a[196:199]
s_add_u32 m0, s83, 33792
v_add_f32_dpp v121, v121, v121 row_ror:8 row_mask:0xf bank_mask:0xf
mm a[196:199],v[44:47],v[188:191],a[196:199]
ld v232,s[44:47],s80 offen lds
mm a[192:195],v[36:39],v[188:191],a[192:195]
v_mov_b32 v124, v120
v_mov_b32 v125, v121
mm a[192:195],v[32:35],v[184:187],a[192:195]
rd v[184:187],v236 offset:18432
rd v[188:191],v237 offset:18432
mm a[64:67],v[32:35],v[0:3],a[64:67]
s_add_u32 m0, s83, 34816
v_permlane16_swap_b32 v124, v120
mm a[64:67],v[36:39],v[4:7],a[64:67]
ld v233,s[44:47],s80 offen lds
mm a[68:71],v[44:47],v[4:7],a[68:71]
v_permlane16_swap_b32 v125, v121
mm a[68:71],v[40:43],v[0:3],a[68:71]
rt v[212:213],v242 offset:16896
rt v[214:215],v242 offset:17920
mm a[0:3],v[48:51],v[0:3],a[0:3]
s_add_u32 m0, s83, 35968
v_add_f32 v120, v120, v124
mm a[0:3],v[52:55],v[4:7],a[0:3]
ld v234,s[44:47],s80 offen lds
mm a[4:7],v[60:63],v[4:7],a[4:7]
v_add_f32 v121, v121, v125
v_mov_b32 v124, v120
mm a[4:7],v[56:59],v[0:3],a[4:7]
rd v[0:3],v236 offset:8192
rd v[4:7],v237 offset:8192
mm a[20:23],v[56:59],v[8:11],a[20:23]
s_add_u32 m0, s83, 36992
v_mov_b32 v125, v121
mm a[20:23],v[60:63],v[12:15],a[20:23]
ld v235,s[44:47],s80 offen lds
mm a[16:19],v[52:55],v[12:15],a[16:19]
s_mov_b32 s80, 3932160
v_permlane32_swap_b32 v124, v120
mm a[16:19],v[48:51],v[8:11],a[16:19]
rt v[216:217],v243 offset:0
rt v[218:219],v243 offset:1024
mm a[84:87],v[40:43],v[8:11],a[84:87]
s_add_u32 m0, s83, 50688
v_permlane32_swap_b32 v125, v121
mm a[84:87],v[44:47],v[12:15],a[84:87]
ld v232,s[44:47],s80 offen lds
mm a[80:83],v[36:39],v[12:15],a[80:83]
v_add_f32 v120, v120, v124
v_add_f32 v121, v121, v125
mm a[80:83],v[32:35],v[8:11],a[80:83]
rd v[8:11],v236 offset:10240
rd v[12:15],v237 offset:10240
mm a[240:243],v[32:35],v[16:19],a[240:243]
s_add_u32 m0, s83, 51712
ds_write_b64 v127, v[120:121]
mm a[240:243],v[36:39],v[20:23],a[240:243]
ld v233,s[44:47],s80 offen lds
mm a[244:247],v[44:47],v[20:23],a[244:247]
s_nop 0
v_readfirstlane_b32 s92, v252
mm a[244:247],v[40:43],v[16:19],a[244:247]
rt v[220:221],v243 offset:16896
rt v[222:223],v243 offset:17920
mm a[32:35],v[48:51],v[16:19],a[32:35]
s_add_u32 m0, s83, 52864
v_readfirstlane_b32 s93, v253
mm a[32:35],v[52:55],v[20:23],a[32:35]
ld v234,s[44:47],s80 offen lds
mm a[36:39],v[60:63],v[20:23],a[36:39]
s_add_u32 s92, s92, 1
s_addc_u32 s93, s93, 0
mm a[36:39],v[56:59],v[16:19],a[36:39]
rd v[16:19],v236 offset:0
rd v[20:23],v237 offset:0
mm a[60:63],v[56:59],v[24:27],a[60:63]
s_add_u32 m0, s83, 53888
v_mov_b32 v252, s92
mm a[60:63],v[60:63],v[28:31],a[60:63]
ld v235,s[44:47],s80 offen lds
mm a[56:59],v[52:55],v[28:31],a[56:59]
s_mov_b32 s80, 0
v_mov_b32 v253, s93
mm a[56:59],v[48:51],v[24:27],a[56:59]
v_mov_b32 v128, 0
v_mov_b32 v129, 0
mm a[52:55],v[40:43],v[24:27],a[52:55]
v_mov_b32 v130, 0
v_mov_b32 v131, 0
mm a[52:55],v[44:47],v[28:31],a[52:55]
st v[128:131],v255,s[52:55],s95 offen sc1
mm a[48:51],v[36:39],v[28:31],a[48:51]
wt lgkmcnt(2)
mm a[48:51],v[32:35],v[24:27],a[48:51]
)ASM" R"ASM(rd v[24:27],v236 offset:2048
rd v[28:31],v237 offset:2048
mm a[96:99],v[192:195],v[160:163],a[96:99]
mm a[96:99],v[196:199],v[164:167],a[96:99]
mm a[100:103],v[204:207],v[164:167],a[100:103]
mm a[100:103],v[200:203],v[160:163],a[100:103]
rt v[32:33],v240 offset:128
rt v[34:35],v240 offset:1152
mm a[104:107],v[208:211],v[160:163],a[104:107]
mm a[104:107],v[212:215],v[164:167],a[104:107]
mm a[108:111],v[220:223],v[164:167],a[108:111]
wt lgkmcnt(2)
s_barrier
mm a[108:111],v[216:219],v[160:163],a[108:111]
rd v[72:75],v126
rd v[76:79],v126 offset:16
mm a[140:143],v[216:219],v[168:171],a[140:143]
s_xor_b32 m0, s82, 0
mm a[140:143],v[220:223],v[172:175],a[140:143]
ld v224,s[40:43],s79 offen lds
mm a[136:139],v[212:215],v[172:175],a[136:139]
mm a[136:139],v[208:211],v[168:171],a[136:139]
rt v[36:37],v240 offset:17024
rt v[38:39],v240 offset:18048
mm a[124:127],v[200:203],v[168:171],a[124:127]
s_xor_b32 m0, s82, 1024
mm a[124:127],v[204:207],v[172:175],a[124:127]
ld v225,s[40:43],s79 offen lds
mm a[120:123],v[196:199],v[172:175],a[120:123]
wt lgkmcnt(2)
v_add_f32 v72, v72, v74
mm a[120:123],v[192:195],v[168:171],a[120:123]
v_add_f32 v73, v73, v75
v_add_f32 v76, v76, v78
mm a[232:235],v[192:195],v[176:179],a[232:235]
s_xor_b32 m0, s82, 2048
v_add_f32 v77, v77, v79
mm a[232:235],v[196:199],v[180:183],a[232:235]
ld v226,s[40:43],s79 offen lds
mm a[236:239],v[204:207],v[180:183],a[236:239]
v_add_f32 v72, v72, v76
v_add_f32 v73, v73, v77
mm a[236:239],v[200:203],v[176:179],a[236:239]
rt v[40:41],v241 offset:128
rt v[42:43],v241 offset:1152
mm a[152:155],v[208:211],v[176:179],a[152:155]
s_xor_b32 m0, s82, 3072
v_mul_f32 v72, 0x3a800000, v72
mm a[152:155],v[212:215],v[180:183],a[152:155]
ld v227,s[40:43],s79 offen lds
mm a[156:159],v[220:223],v[180:183],a[156:159]
v_mul_f32 v73, 0x3a800000, v73
v_add_f32 v124, v70, v72
mm a[156:159],v[216:219],v[176:179],a[156:159]
v_fma_f32 v125, -v72, v72, v73
cv v124,v124,v124
mm a[172:175],v[216:219],v[184:187],a[172:175]
s_xor_b32 m0, s82, 4096
v_lshlrev_b32 v124, 16, v124
mm a[172:175],v[220:223],v[188:191],a[172:175]
ld v228,s[40:43],s79 offen lds
mm a[168:171],v[212:215],v[188:191],a[168:171]
v_max_f32 v125, 0, v125
cv v125,v125,v125
mm a[168:171],v[208:211],v[184:187],a[168:171]
rt v[44:45],v241 offset:17024
rt v[46:47],v241 offset:18048
mm a[220:223],v[200:203],v[184:187],a[220:223]
s_xor_b32 m0, s82, 5120
v_lshlrev_b32 v125, 16, v125
mm a[220:223],v[204:207],v[188:191],a[220:223]
ld v229,s[40:43],s79 offen lds
mm a[216:219],v[196:199],v[188:191],a[216:219]
v_add_f32 v125, 0x3727c5ac, v125
cv v125,v125,v125
mm a[216:219],v[192:195],v[184:187],a[216:219]
v_lshlrev_b32 v125, 16, v125
mm a[200:203],v[192:195],v[0:3],a[200:203]
s_xor_b32 m0, s82, 6144
mm a[200:203],v[196:199],v[4:7],a[200:203]
ld v230,s[40:43],s79 offen lds
mm a[204:207],v[204:207],v[4:7],a[204:207]
v_sqrt_f32 v125, v125
mm a[204:207],v[200:203],v[0:3],a[204:207]
rt v[48:49],v242 offset:128
rt v[50:51],v242 offset:1152
mm a[184:187],v[208:211],v[0:3],a[184:187]
s_xor_b32 m0, s82, 7168
v_sub_f32 v124, v70, v124
mm a[184:187],v[212:215],v[4:7],a[184:187]
ld v231,s[40:43],s79 offen lds
mm a[188:191],v[220:223],v[4:7],a[188:191]
s_mov_b32 s79, 128
cv v125,v125,v125
mm a[188:191],v[216:219],v[0:3],a[188:191]
v_lshlrev_b32 v125, 16, v125
mm a[12:15],v[216:219],v[8:11],a[12:15]
v_rcp_f32 v125, v125
mm a[12:15],v[220:223],v[12:15],a[12:15]
v_mul_f32 v125, 0x3e0293ee, v125
)ASM" R"ASM(v_mul_f32 v124, v124, v125
mm a[8:11],v[212:215],v[12:15],a[8:11]
v_fma_f32 v88, v88, v125, v124
v_fma_f32 v89, v89, v125, v124
mm a[8:11],v[208:211],v[8:11],a[8:11]
rt v[52:53],v242 offset:17024
rt v[54:55],v242 offset:18048
mm a[76:79],v[200:203],v[8:11],a[76:79]
cv v72,v88,v89
v_fma_f32 v90, v90, v125, v124
mm a[76:79],v[204:207],v[12:15],a[76:79]
v_fma_f32 v91, v91, v125, v124
cv v73,v90,v91
mm a[72:75],v[196:199],v[12:15],a[72:75]
v_fma_f32 v92, v92, v125, v124
v_fma_f32 v93, v93, v125, v124
mm a[72:75],v[192:195],v[8:11],a[72:75]
rt v[56:57],v243 offset:128
rt v[58:59],v243 offset:1152
mm a[88:91],v[192:195],v[16:19],a[88:91]
cv v74,v92,v93
v_fma_f32 v94, v94, v125, v124
mm a[88:91],v[196:199],v[20:23],a[88:91]
v_fma_f32 v95, v95, v125, v124
cv v75,v94,v95
mm a[92:95],v[204:207],v[20:23],a[92:95]
st v[72:75],v71,s[56:59],0 offen offset:0 sc1
mm a[92:95],v[200:203],v[16:19],a[92:95]
rt v[60:61],v243 offset:17024
rt v[62:63],v243 offset:18048
mm a[24:27],v[208:211],v[16:19],a[24:27]
v_fma_f32 v96, v96, v125, v124
v_fma_f32 v97, v97, v125, v124
mm a[24:27],v[212:215],v[20:23],a[24:27]
cv v76,v96,v97
v_fma_f32 v98, v98, v125, v124
mm a[28:31],v[220:223],v[20:23],a[28:31]
v_fma_f32 v99, v99, v125, v124
cv v77,v98,v99
mm a[28:31],v[216:219],v[16:19],a[28:31]
v_fma_f32 v100, v100, v125, v124
v_fma_f32 v101, v101, v125, v124
mm a[44:47],v[216:219],v[24:27],a[44:47]
cv v78,v100,v101
v_fma_f32 v102, v102, v125, v124
mm a[44:47],v[220:223],v[28:31],a[44:47]
v_fma_f32 v103, v103, v125, v124
cv v79,v102,v103
mm a[40:43],v[212:215],v[28:31],a[40:43]
st v[76:79],v71,s[56:59],0 offen offset:2048 sc1
mm a[40:43],v[208:211],v[24:27],a[40:43]
v_fma_f32 v104, v104, v125, v124
v_fma_f32 v105, v105, v125, v124
mm a[252:255],v[200:203],v[24:27],a[252:255]
cv v80,v104,v105
v_fma_f32 v106, v106, v125, v124
mm a[252:255],v[204:207],v[28:31],a[252:255]
v_fma_f32 v107, v107, v125, v124
cv v81,v106,v107
mm a[248:251],v[196:199],v[28:31],a[248:251]
wt vmcnt(11) lgkmcnt(0)
s_barrier
mm a[248:251],v[192:195],v[24:27],a[248:251]
v_fma_f32 v108, v108, v125, v124
v_fma_f32 v109, v109, v125, v124
mm a[112:115],v[32:35],v[160:163],a[112:115]
cv v82,v108,v109
v_fma_f32 v110, v110, v125, v124
mm a[112:115],v[36:39],v[164:167],a[112:115]
v_fma_f32 v111, v111, v125, v124
cv v83,v110,v111
mm a[116:119],v[44:47],v[164:167],a[116:119]
rt v[192:193],v240 offset:33792
rt v[194:195],v240 offset:34816
mm a[116:119],v[40:43],v[160:163],a[116:119]
rt v[196:197],v240 offset:50688
rt v[198:199],v240 offset:51712
mm a[128:131],v[48:51],v[160:163],a[128:131]
st v[80:83],v71,s[56:59],s88 offen offset:0 sc1
mm a[128:131],v[52:55],v[164:167],a[128:131]
v_fma_f32 v112, v112, v125, v124
v_fma_f32 v113, v113, v125, v124
mm a[132:135],v[60:63],v[164:167],a[132:135]
cv v84,v112,v113
v_fma_f32 v114, v114, v125, v124
mm a[132:135],v[56:59],v[160:163],a[132:135]
rd v[160:163],v236 offset:57344
rd v[164:167],v237 offset:57344
mm a[148:151],v[56:59],v[168:171],a[148:151]
v_fma_f32 v115, v115, v125, v124
cv v85,v114,v115
mm a[148:151],v[60:63],v[172:175],a[148:151]
v_fma_f32 v116, v116, v125, v124
v_fma_f32 v117, v117, v125, v124
mm a[144:147],v[52:55],v[172:175],a[144:147]
cv v86,v116,v117
v_fma_f32 v118, v118, v125, v124
mm a[144:147],v[48:51],v[168:171],a[144:147]
rt v[200:201],v241 offset:33792
rt v[202:203],v241 offset:34816
)ASM" R"ASM(mm a[228:231],v[40:43],v[168:171],a[228:231]
v_fma_f32 v119, v119, v125, v124
cv v87,v118,v119
mm a[228:231],v[44:47],v[172:175],a[228:231]
st v[84:87],v71,s[56:59],s88 offen offset:2048 sc1
mm a[224:227],v[36:39],v[172:175],a[224:227]
mm a[224:227],v[32:35],v[168:171],a[224:227]
rd v[168:171],v236 offset:59392
rd v[172:175],v237 offset:59392
mm a[208:211],v[32:35],v[176:179],a[208:211]
mm a[208:211],v[36:39],v[180:183],a[208:211]
mm a[212:215],v[44:47],v[180:183],a[212:215]
mm a[212:215],v[40:43],v[176:179],a[212:215]
rt v[204:205],v241 offset:50688
rt v[206:207],v241 offset:51712
mm a[160:163],v[48:51],v[176:179],a[160:163]
mm a[160:163],v[52:55],v[180:183],a[160:163]
mm a[164:167],v[60:63],v[180:183],a[164:167]
mm a[164:167],v[56:59],v[176:179],a[164:167]
rd v[176:179],v236 offset:49152
rd v[180:183],v237 offset:49152
mm a[180:183],v[56:59],v[184:187],a[180:183]
mm a[180:183],v[60:63],v[188:191],a[180:183]
mm a[176:179],v[52:55],v[188:191],a[176:179]
mm a[176:179],v[48:51],v[184:187],a[176:179]
rt v[208:209],v242 offset:33792
rt v[210:211],v242 offset:34816
mm a[196:199],v[40:43],v[184:187],a[196:199]
s_add_u32 m0, s83, 0
mm a[196:199],v[44:47],v[188:191],a[196:199]
.La5_LW1_1:
s_cbranch_execz .La5_LW1_5
ld v232,s[44:47],s80 offen lds
mm a[192:195],v[36:39],v[188:191],a[192:195]
mm a[192:195],v[32:35],v[184:187],a[192:195]
rd v[184:187],v236 offset:51200
rd v[188:191],v237 offset:51200
mm a[64:67],v[32:35],v[0:3],a[64:67]
s_add_u32 m0, s83, 1024
mm a[64:67],v[36:39],v[4:7],a[64:67]
ld v233,s[44:47],s80 offen lds
mm a[68:71],v[44:47],v[4:7],a[68:71]
mm a[68:71],v[40:43],v[0:3],a[68:71]
rt v[212:213],v242 offset:50688
rt v[214:215],v242 offset:51712
mm a[0:3],v[48:51],v[0:3],a[0:3]
s_add_u32 m0, s83, 2176
mm a[0:3],v[52:55],v[4:7],a[0:3]
ld v234,s[44:47],s80 offen lds
mm a[4:7],v[60:63],v[4:7],a[4:7]
mm a[4:7],v[56:59],v[0:3],a[4:7]
rd v[0:3],v236 offset:40960
rd v[4:7],v237 offset:40960
mm a[20:23],v[56:59],v[8:11],a[20:23]
s_add_u32 m0, s83, 3200
mm a[20:23],v[60:63],v[12:15],a[20:23]
ld v235,s[44:47],s80 offen lds
mm a[16:19],v[52:55],v[12:15],a[16:19]
s_mov_b32 s80, 262144
mm a[16:19],v[48:51],v[8:11],a[16:19]
rt v[216:217],v243 offset:33792
rt v[218:219],v243 offset:34816
mm a[84:87],v[40:43],v[8:11],a[84:87]
s_add_u32 m0, s83, 16896
mm a[84:87],v[44:47],v[12:15],a[84:87]
ld v232,s[44:47],s80 offen lds
mm a[80:83],v[36:39],v[12:15],a[80:83]
mm a[80:83],v[32:35],v[8:11],a[80:83]
rd v[8:11],v236 offset:43008
rd v[12:15],v237 offset:43008
mm a[240:243],v[32:35],v[16:19],a[240:243]
s_add_u32 m0, s83, 17920
mm a[240:243],v[36:39],v[20:23],a[240:243]
ld v233,s[44:47],s80 offen lds
mm a[244:247],v[44:47],v[20:23],a[244:247]
mm a[244:247],v[40:43],v[16:19],a[244:247]
rt v[220:221],v243 offset:50688
rt v[222:223],v243 offset:51712
mm a[32:35],v[48:51],v[16:19],a[32:35]
s_add_u32 m0, s83, 19072
mm a[32:35],v[52:55],v[20:23],a[32:35]
ld v234,s[44:47],s80 offen lds
mm a[36:39],v[60:63],v[20:23],a[36:39]
mm a[36:39],v[56:59],v[16:19],a[36:39]
rd v[16:19],v236 offset:32768
rd v[20:23],v237 offset:32768
mm a[60:63],v[56:59],v[24:27],a[60:63]
s_add_u32 m0, s83, 20096
mm a[60:63],v[60:63],v[28:31],a[60:63]
ld v235,s[44:47],s80 offen lds
mm a[56:59],v[52:55],v[28:31],a[56:59]
s_mov_b32 s80, 524288
mm a[56:59],v[48:51],v[24:27],a[56:59]
mm a[52:55],v[40:43],v[24:27],a[52:55]
mm a[52:55],v[44:47],v[28:31],a[52:55]
mm a[48:51],v[36:39],v[28:31],a[48:51]
wt lgkmcnt(2)
mm a[48:51],v[32:35],v[24:27],a[48:51]
rd v[24:27],v236 offset:34816
rd v[28:31],v237 offset:34816
)ASM" R"ASM(mm a[96:99],v[192:195],v[160:163],a[96:99]
mm a[96:99],v[196:199],v[164:167],a[96:99]
mm a[100:103],v[204:207],v[164:167],a[100:103]
mm a[100:103],v[200:203],v[160:163],a[100:103]
rt v[32:33],v240 offset:33920
rt v[34:35],v240 offset:34944
mm a[104:107],v[208:211],v[160:163],a[104:107]
mm a[104:107],v[212:215],v[164:167],a[104:107]
mm a[108:111],v[220:223],v[164:167],a[108:111]
wt lgkmcnt(2)
s_barrier
mm a[108:111],v[216:219],v[160:163],a[108:111]
mm a[140:143],v[216:219],v[168:171],a[140:143]
s_xor_b32 m0, s82, 32768
mm a[140:143],v[220:223],v[172:175],a[140:143]
ld v224,s[40:43],s79 offen lds
mm a[136:139],v[212:215],v[172:175],a[136:139]
mm a[136:139],v[208:211],v[168:171],a[136:139]
rt v[36:37],v240 offset:50816
rt v[38:39],v240 offset:51840
mm a[124:127],v[200:203],v[168:171],a[124:127]
s_xor_b32 m0, s82, 33792
mm a[124:127],v[204:207],v[172:175],a[124:127]
ld v225,s[40:43],s79 offen lds
mm a[120:123],v[196:199],v[172:175],a[120:123]
mm a[120:123],v[192:195],v[168:171],a[120:123]
mm a[232:235],v[192:195],v[176:179],a[232:235]
s_xor_b32 m0, s82, 34816
mm a[232:235],v[196:199],v[180:183],a[232:235]
ld v226,s[40:43],s79 offen lds
mm a[236:239],v[204:207],v[180:183],a[236:239]
mm a[236:239],v[200:203],v[176:179],a[236:239]
rt v[40:41],v241 offset:33920
rt v[42:43],v241 offset:34944
mm a[152:155],v[208:211],v[176:179],a[152:155]
s_xor_b32 m0, s82, 35840
mm a[152:155],v[212:215],v[180:183],a[152:155]
ld v227,s[40:43],s79 offen lds
mm a[156:159],v[220:223],v[180:183],a[156:159]
mm a[156:159],v[216:219],v[176:179],a[156:159]
mm a[172:175],v[216:219],v[184:187],a[172:175]
s_xor_b32 m0, s82, 36864
mm a[172:175],v[220:223],v[188:191],a[172:175]
ld v228,s[40:43],s79 offen lds
mm a[168:171],v[212:215],v[188:191],a[168:171]
mm a[168:171],v[208:211],v[184:187],a[168:171]
rt v[44:45],v241 offset:50816
rt v[46:47],v241 offset:51840
mm a[220:223],v[200:203],v[184:187],a[220:223]
s_xor_b32 m0, s82, 37888
mm a[220:223],v[204:207],v[188:191],a[220:223]
ld v229,s[40:43],s79 offen lds
mm a[216:219],v[196:199],v[188:191],a[216:219]
mm a[216:219],v[192:195],v[184:187],a[216:219]
mm a[200:203],v[192:195],v[0:3],a[200:203]
s_xor_b32 m0, s82, 38912
mm a[200:203],v[196:199],v[4:7],a[200:203]
ld v230,s[40:43],s79 offen lds
mm a[204:207],v[204:207],v[4:7],a[204:207]
mm a[204:207],v[200:203],v[0:3],a[204:207]
rt v[48:49],v242 offset:33920
rt v[50:51],v242 offset:34944
mm a[184:187],v[208:211],v[0:3],a[184:187]
s_xor_b32 m0, s82, 39936
mm a[184:187],v[212:215],v[4:7],a[184:187]
ld v231,s[40:43],s79 offen lds
mm a[188:191],v[220:223],v[4:7],a[188:191]
s_mov_b32 s79, 256
mm a[188:191],v[216:219],v[0:3],a[188:191]
mm a[12:15],v[216:219],v[8:11],a[12:15]
mm a[12:15],v[220:223],v[12:15],a[12:15]
buffer_load_dword v254, v135, s[68:71], s96 offen
mm a[8:11],v[212:215],v[12:15],a[8:11]
s_add_u32 s96, s96, 1048576
mm a[8:11],v[208:211],v[8:11],a[8:11]
rt v[52:53],v242 offset:50816
rt v[54:55],v242 offset:51840
mm a[76:79],v[200:203],v[8:11],a[76:79]
mm a[76:79],v[204:207],v[12:15],a[76:79]
buffer_load_dword v254, v135, s[68:71], s96 offen
mm a[72:75],v[196:199],v[12:15],a[72:75]
s_add_u32 s96, s96, 1048576
mm a[72:75],v[192:195],v[8:11],a[72:75]
rt v[56:57],v243 offset:33920
rt v[58:59],v243 offset:34944
mm a[88:91],v[192:195],v[16:19],a[88:91]
mm a[88:91],v[196:199],v[20:23],a[88:91]
wt vmcnt(18)
buffer_store_dwordx2 v[252:253], off, s[64:67], s91 offset:1024 sc1
mm a[92:95],v[204:207],v[20:23],a[92:95]
s_mov_b32 s91, 0
s_lshl_b32 s92, s76, 19
mm a[92:95],v[200:203],v[16:19],a[92:95]
rt v[60:61],v243 offset:50816
rt v[62:63],v243 offset:51840
mm a[24:27],v[208:211],v[16:19],a[24:27]
s_add_u32 s92, s92, 88080384
mm a[24:27],v[212:215],v[20:23],a[24:27]
s_add_u32 s52, s26, s92
s_addc_u32 s53, s27, 0
)ASM" R"ASM(mm a[28:31],v[220:223],v[20:23],a[28:31]
s_and_b32 s53, s53, 0xffff
s_mov_b32 s54, 524672
mm a[28:31],v[216:219],v[16:19],a[28:31]
s_mov_b32 s55, 0x20000
s_lshl_b32 s92, s3, 16
mm a[44:47],v[216:219],v[24:27],a[44:47]
s_sub_u32 s93, 458752, s92
v_add_u32 v244, s92, v136
mm a[44:47],v[220:223],v[28:31],a[44:47]
v_add_u32 v246, s93, v136
v_add_u32 v245, 0x8000, v244
mm a[40:43],v[212:215],v[28:31],a[40:43]
v_add_u32 v247, 0x8000, v246
s_lshl_b32 s94, s72, 19
mm a[40:43],v[208:211],v[24:27],a[40:43]
s_lshl_b32 s93, s76, 16
s_add_u32 s92, s94, s93
mm a[252:255],v[200:203],v[24:27],a[252:255]
s_lshl_b32 s93, s77, 13
s_add_u32 s92, s92, s93
mm a[252:255],v[204:207],v[28:31],a[252:255]
s_add_u32 s92, s92, 20971520
mm a[248:251],v[196:199],v[28:31],a[248:251]
wt vmcnt(11) lgkmcnt(0)
s_barrier
mm a[248:251],v[192:195],v[24:27],a[248:251]
s_add_u32 s48, s26, s92
s_addc_u32 s49, s27, 0
mm a[112:115],v[32:35],v[160:163],a[112:115]
s_and_b32 s49, s49, 0xffff
s_mov_b32 s50, 57344
mm a[112:115],v[36:39],v[164:167],a[112:115]
s_mov_b32 s51, 0x20000
s_lshl_b32 s93, s77, 24
mm a[116:119],v[44:47],v[164:167],a[116:119]
rt v[192:193],v240 offset:0
rt v[194:195],v240 offset:1024
mm a[116:119],v[40:43],v[160:163],a[116:119]
rt v[196:197],v240 offset:16896
rt v[198:199],v240 offset:17920
mm a[128:131],v[48:51],v[160:163],a[128:131]
s_add_u32 s92, s94, s93
s_add_u32 s92, s92, 20971520
mm a[128:131],v[52:55],v[164:167],a[128:131]
s_add_u32 s60, s26, s92
s_addc_u32 s61, s27, 0
mm a[132:135],v[60:63],v[164:167],a[132:135]
s_and_b32 s61, s61, 0xffff
s_mov_b32 s62, 524288
mm a[132:135],v[56:59],v[160:163],a[132:135]
rd v[160:163],v236 offset:24576
rd v[164:167],v237 offset:24576
mm a[148:151],v[56:59],v[168:171],a[148:151]
s_mov_b32 s63, 0x20000
s_lshl_b32 s92, s77, 14
mm a[148:151],v[60:63],v[172:175],a[148:151]
s_sub_u32 s95, 16384, s92
v_add_u32 v238, s95, v255
mm a[144:147],v[52:55],v[172:175],a[144:147]
mm a[144:147],v[48:51],v[168:171],a[144:147]
rt v[200:201],v241 offset:0
rt v[202:203],v241 offset:1024
mm a[228:231],v[40:43],v[168:171],a[228:231]
mm a[228:231],v[44:47],v[172:175],a[228:231]
mm a[224:227],v[36:39],v[172:175],a[224:227]
mm a[224:227],v[32:35],v[168:171],a[224:227]
rd v[168:171],v236 offset:26624
rd v[172:175],v237 offset:26624
mm a[208:211],v[32:35],v[176:179],a[208:211]
mm a[208:211],v[36:39],v[180:183],a[208:211]
mm a[212:215],v[44:47],v[180:183],a[212:215]
mm a[212:215],v[40:43],v[176:179],a[212:215]
rt v[204:205],v241 offset:16896
rt v[206:207],v241 offset:17920
mm a[160:163],v[48:51],v[176:179],a[160:163]
mm a[160:163],v[52:55],v[180:183],a[160:163]
mm a[164:167],v[60:63],v[180:183],a[164:167]
mm a[164:167],v[56:59],v[176:179],a[164:167]
rd v[176:179],v236 offset:16384
rd v[180:183],v237 offset:16384
mm a[180:183],v[56:59],v[184:187],a[180:183]
mm a[180:183],v[60:63],v[188:191],a[180:183]
mm a[176:179],v[52:55],v[188:191],a[176:179]
mm a[176:179],v[48:51],v[184:187],a[176:179]
rt v[208:209],v242 offset:0
rt v[210:211],v242 offset:1024
mm a[196:199],v[40:43],v[184:187],a[196:199]
s_add_u32 m0, s83, 33792
mm a[196:199],v[44:47],v[188:191],a[196:199]
ld v232,s[44:47],s80 offen lds
mm a[192:195],v[36:39],v[188:191],a[192:195]
mm a[192:195],v[32:35],v[184:187],a[192:195]
rd v[184:187],v236 offset:18432
rd v[188:191],v237 offset:18432
mm a[64:67],v[32:35],v[0:3],a[64:67]
s_add_u32 m0, s83, 34816
mm a[64:67],v[36:39],v[4:7],a[64:67]
ld v233,s[44:47],s80 offen lds
mm a[68:71],v[44:47],v[4:7],a[68:71]
)ASM" R"ASM(mm a[68:71],v[40:43],v[0:3],a[68:71]
rt v[212:213],v242 offset:16896
rt v[214:215],v242 offset:17920
mm a[0:3],v[48:51],v[0:3],a[0:3]
s_add_u32 m0, s83, 35968
mm a[0:3],v[52:55],v[4:7],a[0:3]
ld v234,s[44:47],s80 offen lds
mm a[4:7],v[60:63],v[4:7],a[4:7]
mm a[4:7],v[56:59],v[0:3],a[4:7]
rd v[0:3],v236 offset:8192
rd v[4:7],v237 offset:8192
mm a[20:23],v[56:59],v[8:11],a[20:23]
s_add_u32 m0, s83, 36992
mm a[20:23],v[60:63],v[12:15],a[20:23]
ld v235,s[44:47],s80 offen lds
mm a[16:19],v[52:55],v[12:15],a[16:19]
s_mov_b32 s80, 786432
mm a[16:19],v[48:51],v[8:11],a[16:19]
rt v[216:217],v243 offset:0
rt v[218:219],v243 offset:1024
mm a[84:87],v[40:43],v[8:11],a[84:87]
s_add_u32 m0, s83, 50688
mm a[84:87],v[44:47],v[12:15],a[84:87]
ld v232,s[44:47],s80 offen lds
mm a[80:83],v[36:39],v[12:15],a[80:83]
mm a[80:83],v[32:35],v[8:11],a[80:83]
rd v[8:11],v236 offset:10240
rd v[12:15],v237 offset:10240
mm a[240:243],v[32:35],v[16:19],a[240:243]
s_add_u32 m0, s83, 51712
mm a[240:243],v[36:39],v[20:23],a[240:243]
ld v233,s[44:47],s80 offen lds
mm a[244:247],v[44:47],v[20:23],a[244:247]
mm a[244:247],v[40:43],v[16:19],a[244:247]
rt v[220:221],v243 offset:16896
rt v[222:223],v243 offset:17920
mm a[32:35],v[48:51],v[16:19],a[32:35]
s_add_u32 m0, s83, 52864
mm a[32:35],v[52:55],v[20:23],a[32:35]
ld v234,s[44:47],s80 offen lds
mm a[36:39],v[60:63],v[20:23],a[36:39]
mm a[36:39],v[56:59],v[16:19],a[36:39]
rd v[16:19],v236 offset:0
rd v[20:23],v237 offset:0
mm a[60:63],v[56:59],v[24:27],a[60:63]
s_add_u32 m0, s83, 53888
mm a[60:63],v[60:63],v[28:31],a[60:63]
ld v235,s[44:47],s80 offen lds
mm a[56:59],v[52:55],v[28:31],a[56:59]
s_mov_b32 s80, 1048576
mm a[56:59],v[48:51],v[24:27],a[56:59]
mm a[52:55],v[40:43],v[24:27],a[52:55]
mm a[52:55],v[44:47],v[28:31],a[52:55]
mm a[48:51],v[36:39],v[28:31],a[48:51]
wt lgkmcnt(2)
mm a[48:51],v[32:35],v[24:27],a[48:51]
rd v[24:27],v236 offset:2048
rd v[28:31],v237 offset:2048
mm a[96:99],v[192:195],v[160:163],a[96:99]
mm a[96:99],v[196:199],v[164:167],a[96:99]
mm a[100:103],v[204:207],v[164:167],a[100:103]
mm a[100:103],v[200:203],v[160:163],a[100:103]
rt v[32:33],v240 offset:128
rt v[34:35],v240 offset:1152
mm a[104:107],v[208:211],v[160:163],a[104:107]
mm a[104:107],v[212:215],v[164:167],a[104:107]
mm a[108:111],v[220:223],v[164:167],a[108:111]
wt lgkmcnt(2)
s_barrier
mm a[108:111],v[216:219],v[160:163],a[108:111]
mm a[140:143],v[216:219],v[168:171],a[140:143]
s_xor_b32 m0, s82, 0
mm a[140:143],v[220:223],v[172:175],a[140:143]
ld v224,s[40:43],s79 offen lds
mm a[136:139],v[212:215],v[172:175],a[136:139]
mm a[136:139],v[208:211],v[168:171],a[136:139]
rt v[36:37],v240 offset:17024
rt v[38:39],v240 offset:18048
mm a[124:127],v[200:203],v[168:171],a[124:127]
s_xor_b32 m0, s82, 1024
mm a[124:127],v[204:207],v[172:175],a[124:127]
ld v225,s[40:43],s79 offen lds
mm a[120:123],v[196:199],v[172:175],a[120:123]
mm a[120:123],v[192:195],v[168:171],a[120:123]
mm a[232:235],v[192:195],v[176:179],a[232:235]
s_xor_b32 m0, s82, 2048
mm a[232:235],v[196:199],v[180:183],a[232:235]
ld v226,s[40:43],s79 offen lds
mm a[236:239],v[204:207],v[180:183],a[236:239]
mm a[236:239],v[200:203],v[176:179],a[236:239]
rt v[40:41],v241 offset:128
rt v[42:43],v241 offset:1152
mm a[152:155],v[208:211],v[176:179],a[152:155]
s_xor_b32 m0, s82, 3072
mm a[152:155],v[212:215],v[180:183],a[152:155]
ld v227,s[40:43],s79 offen lds
mm a[156:159],v[220:223],v[180:183],a[156:159]
mm a[156:159],v[216:219],v[176:179],a[156:159]
mm a[172:175],v[216:219],v[184:187],a[172:175]
s_xor_b32 m0, s82, 4096
)ASM" R"ASM(mm a[172:175],v[220:223],v[188:191],a[172:175]
ld v228,s[40:43],s79 offen lds
mm a[168:171],v[212:215],v[188:191],a[168:171]
mm a[168:171],v[208:211],v[184:187],a[168:171]
rt v[44:45],v241 offset:17024
rt v[46:47],v241 offset:18048
mm a[220:223],v[200:203],v[184:187],a[220:223]
s_xor_b32 m0, s82, 5120
mm a[220:223],v[204:207],v[188:191],a[220:223]
ld v229,s[40:43],s79 offen lds
mm a[216:219],v[196:199],v[188:191],a[216:219]
mm a[216:219],v[192:195],v[184:187],a[216:219]
mm a[200:203],v[192:195],v[0:3],a[200:203]
s_xor_b32 m0, s82, 6144
mm a[200:203],v[196:199],v[4:7],a[200:203]
ld v230,s[40:43],s79 offen lds
mm a[204:207],v[204:207],v[4:7],a[204:207]
mm a[204:207],v[200:203],v[0:3],a[204:207]
rt v[48:49],v242 offset:128
rt v[50:51],v242 offset:1152
mm a[184:187],v[208:211],v[0:3],a[184:187]
s_xor_b32 m0, s82, 7168
mm a[184:187],v[212:215],v[4:7],a[184:187]
ld v231,s[40:43],s79 offen lds
mm a[188:191],v[220:223],v[4:7],a[188:191]
s_mov_b32 s79, 384
mm a[188:191],v[216:219],v[0:3],a[188:191]
mm a[12:15],v[216:219],v[8:11],a[12:15]
mm a[12:15],v[220:223],v[12:15],a[12:15]
buffer_load_dword v254, v135, s[68:71], s96 offen
mm a[8:11],v[212:215],v[12:15],a[8:11]
s_add_u32 s96, s96, 1048576
mm a[8:11],v[208:211],v[8:11],a[8:11]
rt v[52:53],v242 offset:17024
rt v[54:55],v242 offset:18048
mm a[76:79],v[200:203],v[8:11],a[76:79]
mm a[76:79],v[204:207],v[12:15],a[76:79]
buffer_load_dword v254, v135, s[68:71], s96 offen
mm a[72:75],v[196:199],v[12:15],a[72:75]
s_add_u32 s96, s96, 1048576
mm a[72:75],v[192:195],v[8:11],a[72:75]
rt v[56:57],v243 offset:128
rt v[58:59],v243 offset:1152
mm a[88:91],v[192:195],v[16:19],a[88:91]
mm a[88:91],v[196:199],v[20:23],a[88:91]
mm a[92:95],v[204:207],v[20:23],a[92:95]
mm a[92:95],v[200:203],v[16:19],a[92:95]
rt v[60:61],v243 offset:17024
rt v[62:63],v243 offset:18048
mm a[24:27],v[208:211],v[16:19],a[24:27]
mm a[24:27],v[212:215],v[20:23],a[24:27]
mm a[28:31],v[220:223],v[20:23],a[28:31]
mm a[28:31],v[216:219],v[16:19],a[28:31]
mm a[44:47],v[216:219],v[24:27],a[44:47]
mm a[44:47],v[220:223],v[28:31],a[44:47]
mm a[40:43],v[212:215],v[28:31],a[40:43]
mm a[40:43],v[208:211],v[24:27],a[40:43]
mm a[252:255],v[200:203],v[24:27],a[252:255]
mm a[252:255],v[204:207],v[28:31],a[252:255]
mm a[248:251],v[196:199],v[28:31],a[248:251]
wt vmcnt(10) lgkmcnt(0)
s_barrier
mm a[248:251],v[192:195],v[24:27],a[248:251]
mm a[112:115],v[32:35],v[160:163],a[112:115]
mm a[112:115],v[36:39],v[164:167],a[112:115]
mm a[116:119],v[44:47],v[164:167],a[116:119]
rt v[192:193],v240 offset:33792
rt v[194:195],v240 offset:34816
mm a[116:119],v[40:43],v[160:163],a[116:119]
rt v[196:197],v240 offset:50688
rt v[198:199],v240 offset:51712
mm a[128:131],v[48:51],v[160:163],a[128:131]
mm a[128:131],v[52:55],v[164:167],a[128:131]
mm a[132:135],v[60:63],v[164:167],a[132:135]
mm a[132:135],v[56:59],v[160:163],a[132:135]
rd v[160:163],v236 offset:57344
rd v[164:167],v237 offset:57344
mm a[148:151],v[56:59],v[168:171],a[148:151]
mm a[148:151],v[60:63],v[172:175],a[148:151]
mm a[144:147],v[52:55],v[172:175],a[144:147]
mm a[144:147],v[48:51],v[168:171],a[144:147]
rt v[200:201],v241 offset:33792
rt v[202:203],v241 offset:34816
mm a[228:231],v[40:43],v[168:171],a[228:231]
mm a[228:231],v[44:47],v[172:175],a[228:231]
mm a[224:227],v[36:39],v[172:175],a[224:227]
mm a[224:227],v[32:35],v[168:171],a[224:227]
rd v[168:171],v236 offset:59392
rd v[172:175],v237 offset:59392
mm a[208:211],v[32:35],v[176:179],a[208:211]
mm a[208:211],v[36:39],v[180:183],a[208:211]
mm a[212:215],v[44:47],v[180:183],a[212:215]
mm a[212:215],v[40:43],v[176:179],a[212:215]
rt v[204:205],v241 offset:50688
rt v[206:207],v241 offset:51712
mm a[160:163],v[48:51],v[176:179],a[160:163]
mm a[160:163],v[52:55],v[180:183],a[160:163]
mm a[164:167],v[60:63],v[180:183],a[164:167]
mm a[164:167],v[56:59],v[176:179],a[164:167]
rd v[176:179],v236 offset:49152
)ASM" R"ASM(rd v[180:183],v237 offset:49152
mm a[180:183],v[56:59],v[184:187],a[180:183]
mm a[180:183],v[60:63],v[188:191],a[180:183]
mm a[176:179],v[52:55],v[188:191],a[176:179]
mm a[176:179],v[48:51],v[184:187],a[176:179]
rt v[208:209],v242 offset:33792
rt v[210:211],v242 offset:34816
mm a[196:199],v[40:43],v[184:187],a[196:199]
s_add_u32 m0, s83, 0
mm a[196:199],v[44:47],v[188:191],a[196:199]
ld v232,s[44:47],s80 offen lds
mm a[192:195],v[36:39],v[188:191],a[192:195]
mm a[192:195],v[32:35],v[184:187],a[192:195]
rd v[184:187],v236 offset:51200
rd v[188:191],v237 offset:51200
mm a[64:67],v[32:35],v[0:3],a[64:67]
s_add_u32 m0, s83, 1024
mm a[64:67],v[36:39],v[4:7],a[64:67]
ld v233,s[44:47],s80 offen lds
mm a[68:71],v[44:47],v[4:7],a[68:71]
mm a[68:71],v[40:43],v[0:3],a[68:71]
rt v[212:213],v242 offset:50688
rt v[214:215],v242 offset:51712
mm a[0:3],v[48:51],v[0:3],a[0:3]
s_add_u32 m0, s83, 2176
mm a[0:3],v[52:55],v[4:7],a[0:3]
ld v234,s[44:47],s80 offen lds
mm a[4:7],v[60:63],v[4:7],a[4:7]
mm a[4:7],v[56:59],v[0:3],a[4:7]
rd v[0:3],v236 offset:40960
rd v[4:7],v237 offset:40960
mm a[20:23],v[56:59],v[8:11],a[20:23]
s_add_u32 m0, s83, 3200
mm a[20:23],v[60:63],v[12:15],a[20:23]
ld v235,s[44:47],s80 offen lds
mm a[16:19],v[52:55],v[12:15],a[16:19]
s_mov_b32 s80, 1310720
mm a[16:19],v[48:51],v[8:11],a[16:19]
rt v[216:217],v243 offset:33792
rt v[218:219],v243 offset:34816
mm a[84:87],v[40:43],v[8:11],a[84:87]
s_add_u32 m0, s83, 16896
mm a[84:87],v[44:47],v[12:15],a[84:87]
ld v232,s[44:47],s80 offen lds
mm a[80:83],v[36:39],v[12:15],a[80:83]
mm a[80:83],v[32:35],v[8:11],a[80:83]
rd v[8:11],v236 offset:43008
rd v[12:15],v237 offset:43008
mm a[240:243],v[32:35],v[16:19],a[240:243]
s_add_u32 m0, s83, 17920
mm a[240:243],v[36:39],v[20:23],a[240:243]
ld v233,s[44:47],s80 offen lds
mm a[244:247],v[44:47],v[20:23],a[244:247]
mm a[244:247],v[40:43],v[16:19],a[244:247]
rt v[220:221],v243 offset:50688
rt v[222:223],v243 offset:51712
mm a[32:35],v[48:51],v[16:19],a[32:35]
s_add_u32 m0, s83, 19072
mm a[32:35],v[52:55],v[20:23],a[32:35]
ld v234,s[44:47],s80 offen lds
.La5_LW1_2:
s_cbranch_execz .La5_LW1_6
mm a[36:39],v[60:63],v[20:23],a[36:39]
mm a[36:39],v[56:59],v[16:19],a[36:39]
rd v[16:19],v236 offset:32768
rd v[20:23],v237 offset:32768
mm a[60:63],v[56:59],v[24:27],a[60:63]
s_add_u32 m0, s83, 20096
mm a[60:63],v[60:63],v[28:31],a[60:63]
ld v235,s[44:47],s80 offen lds
mm a[56:59],v[52:55],v[28:31],a[56:59]
s_mov_b32 s80, 1572864
mm a[56:59],v[48:51],v[24:27],a[56:59]
mm a[52:55],v[40:43],v[24:27],a[52:55]
mm a[52:55],v[44:47],v[28:31],a[52:55]
mm a[48:51],v[36:39],v[28:31],a[48:51]
wt lgkmcnt(2)
mm a[48:51],v[32:35],v[24:27],a[48:51]
rd v[24:27],v236 offset:34816
rd v[28:31],v237 offset:34816
mm a[96:99],v[192:195],v[160:163],a[96:99]
mm a[96:99],v[196:199],v[164:167],a[96:99]
mm a[100:103],v[204:207],v[164:167],a[100:103]
mm a[100:103],v[200:203],v[160:163],a[100:103]
rt v[32:33],v240 offset:33920
rt v[34:35],v240 offset:34944
mm a[104:107],v[208:211],v[160:163],a[104:107]
mm a[104:107],v[212:215],v[164:167],a[104:107]
mm a[108:111],v[220:223],v[164:167],a[108:111]
wt lgkmcnt(2)
s_barrier
mm a[108:111],v[216:219],v[160:163],a[108:111]
mm a[140:143],v[216:219],v[168:171],a[140:143]
s_xor_b32 m0, s82, 32768
mm a[140:143],v[220:223],v[172:175],a[140:143]
ld v224,s[40:43],s79 offen lds
mm a[136:139],v[212:215],v[172:175],a[136:139]
mm a[136:139],v[208:211],v[168:171],a[136:139]
rt v[36:37],v240 offset:50816
rt v[38:39],v240 offset:51840
)ASM" R"ASM(mm a[124:127],v[200:203],v[168:171],a[124:127]
s_xor_b32 m0, s82, 33792
mm a[124:127],v[204:207],v[172:175],a[124:127]
ld v225,s[40:43],s79 offen lds
mm a[120:123],v[196:199],v[172:175],a[120:123]
mm a[120:123],v[192:195],v[168:171],a[120:123]
mm a[232:235],v[192:195],v[176:179],a[232:235]
s_xor_b32 m0, s82, 34816
mm a[232:235],v[196:199],v[180:183],a[232:235]
ld v226,s[40:43],s79 offen lds
mm a[236:239],v[204:207],v[180:183],a[236:239]
mm a[236:239],v[200:203],v[176:179],a[236:239]
rt v[40:41],v241 offset:33920
rt v[42:43],v241 offset:34944
mm a[152:155],v[208:211],v[176:179],a[152:155]
s_xor_b32 m0, s82, 35840
mm a[152:155],v[212:215],v[180:183],a[152:155]
ld v227,s[40:43],s79 offen lds
mm a[156:159],v[220:223],v[180:183],a[156:159]
mm a[156:159],v[216:219],v[176:179],a[156:159]
mm a[172:175],v[216:219],v[184:187],a[172:175]
s_xor_b32 m0, s82, 36864
mm a[172:175],v[220:223],v[188:191],a[172:175]
ld v228,s[40:43],s79 offen lds
mm a[168:171],v[212:215],v[188:191],a[168:171]
mm a[168:171],v[208:211],v[184:187],a[168:171]
rt v[44:45],v241 offset:50816
rt v[46:47],v241 offset:51840
mm a[220:223],v[200:203],v[184:187],a[220:223]
s_xor_b32 m0, s82, 37888
mm a[220:223],v[204:207],v[188:191],a[220:223]
ld v229,s[40:43],s79 offen lds
mm a[216:219],v[196:199],v[188:191],a[216:219]
mm a[216:219],v[192:195],v[184:187],a[216:219]
mm a[200:203],v[192:195],v[0:3],a[200:203]
s_xor_b32 m0, s82, 38912
mm a[200:203],v[196:199],v[4:7],a[200:203]
ld v230,s[40:43],s79 offen lds
mm a[204:207],v[204:207],v[4:7],a[204:207]
mm a[204:207],v[200:203],v[0:3],a[204:207]
rt v[48:49],v242 offset:33920
rt v[50:51],v242 offset:34944
mm a[184:187],v[208:211],v[0:3],a[184:187]
s_xor_b32 m0, s82, 39936
mm a[184:187],v[212:215],v[4:7],a[184:187]
ld v231,s[40:43],s79 offen lds
mm a[188:191],v[220:223],v[4:7],a[188:191]
s_mov_b32 s79, 512
mm a[188:191],v[216:219],v[0:3],a[188:191]
mm a[12:15],v[216:219],v[8:11],a[12:15]
mm a[12:15],v[220:223],v[12:15],a[12:15]
buffer_load_dword v254, v135, s[68:71], s96 offen
mm a[8:11],v[212:215],v[12:15],a[8:11]
s_add_u32 s96, s96, 1048576
mm a[8:11],v[208:211],v[8:11],a[8:11]
rt v[52:53],v242 offset:50816
rt v[54:55],v242 offset:51840
mm a[76:79],v[200:203],v[8:11],a[76:79]
mm a[76:79],v[204:207],v[12:15],a[76:79]
buffer_load_dword v254, v135, s[68:71], s96 offen
mm a[72:75],v[196:199],v[12:15],a[72:75]
s_add_u32 s96, s96, 1048576
mm a[72:75],v[192:195],v[8:11],a[72:75]
rt v[56:57],v243 offset:33920
rt v[58:59],v243 offset:34944
mm a[88:91],v[192:195],v[16:19],a[88:91]
mm a[88:91],v[196:199],v[20:23],a[88:91]
mm a[92:95],v[204:207],v[20:23],a[92:95]
mm a[92:95],v[200:203],v[16:19],a[92:95]
rt v[60:61],v243 offset:50816
rt v[62:63],v243 offset:51840
mm a[24:27],v[208:211],v[16:19],a[24:27]
mm a[24:27],v[212:215],v[20:23],a[24:27]
mm a[28:31],v[220:223],v[20:23],a[28:31]
mm a[28:31],v[216:219],v[16:19],a[28:31]
mm a[44:47],v[216:219],v[24:27],a[44:47]
mm a[44:47],v[220:223],v[28:31],a[44:47]
mm a[40:43],v[212:215],v[28:31],a[40:43]
mm a[40:43],v[208:211],v[24:27],a[40:43]
mm a[252:255],v[200:203],v[24:27],a[252:255]
mm a[252:255],v[204:207],v[28:31],a[252:255]
mm a[248:251],v[196:199],v[28:31],a[248:251]
wt vmcnt(10) lgkmcnt(0)
s_barrier
mm a[248:251],v[192:195],v[24:27],a[248:251]
mm a[112:115],v[32:35],v[160:163],a[112:115]
mm a[112:115],v[36:39],v[164:167],a[112:115]
mm a[116:119],v[44:47],v[164:167],a[116:119]
rt v[192:193],v240 offset:0
rt v[194:195],v240 offset:1024
mm a[116:119],v[40:43],v[160:163],a[116:119]
rt v[196:197],v240 offset:16896
rt v[198:199],v240 offset:17920
mm a[128:131],v[48:51],v[160:163],a[128:131]
mm a[128:131],v[52:55],v[164:167],a[128:131]
mm a[132:135],v[60:63],v[164:167],a[132:135]
mm a[132:135],v[56:59],v[160:163],a[132:135]
rd v[160:163],v236 offset:24576
rd v[164:167],v237 offset:24576
mm a[148:151],v[56:59],v[168:171],a[148:151]
)ASM" R"ASM(mm a[148:151],v[60:63],v[172:175],a[148:151]
mm a[144:147],v[52:55],v[172:175],a[144:147]
mm a[144:147],v[48:51],v[168:171],a[144:147]
rt v[200:201],v241 offset:0
rt v[202:203],v241 offset:1024
mm a[228:231],v[40:43],v[168:171],a[228:231]
mm a[228:231],v[44:47],v[172:175],a[228:231]
mm a[224:227],v[36:39],v[172:175],a[224:227]
mm a[224:227],v[32:35],v[168:171],a[224:227]
rd v[168:171],v236 offset:26624
rd v[172:175],v237 offset:26624
mm a[208:211],v[32:35],v[176:179],a[208:211]
mm a[208:211],v[36:39],v[180:183],a[208:211]
mm a[212:215],v[44:47],v[180:183],a[212:215]
mm a[212:215],v[40:43],v[176:179],a[212:215]
rt v[204:205],v241 offset:16896
rt v[206:207],v241 offset:17920
mm a[160:163],v[48:51],v[176:179],a[160:163]
mm a[160:163],v[52:55],v[180:183],a[160:163]
mm a[164:167],v[60:63],v[180:183],a[164:167]
mm a[164:167],v[56:59],v[176:179],a[164:167]
rd v[176:179],v236 offset:16384
rd v[180:183],v237 offset:16384
mm a[180:183],v[56:59],v[184:187],a[180:183]
mm a[180:183],v[60:63],v[188:191],a[180:183]
mm a[176:179],v[52:55],v[188:191],a[176:179]
mm a[176:179],v[48:51],v[184:187],a[176:179]
rt v[208:209],v242 offset:0
rt v[210:211],v242 offset:1024
mm a[196:199],v[40:43],v[184:187],a[196:199]
s_add_u32 m0, s83, 33792
mm a[196:199],v[44:47],v[188:191],a[196:199]
ld v232,s[44:47],s80 offen lds
mm a[192:195],v[36:39],v[188:191],a[192:195]
mm a[192:195],v[32:35],v[184:187],a[192:195]
rd v[184:187],v236 offset:18432
rd v[188:191],v237 offset:18432
mm a[64:67],v[32:35],v[0:3],a[64:67]
s_add_u32 m0, s83, 34816
mm a[64:67],v[36:39],v[4:7],a[64:67]
ld v233,s[44:47],s80 offen lds
mm a[68:71],v[44:47],v[4:7],a[68:71]
mm a[68:71],v[40:43],v[0:3],a[68:71]
rt v[212:213],v242 offset:16896
rt v[214:215],v242 offset:17920
mm a[0:3],v[48:51],v[0:3],a[0:3]
s_add_u32 m0, s83, 35968
mm a[0:3],v[52:55],v[4:7],a[0:3]
ld v234,s[44:47],s80 offen lds
mm a[4:7],v[60:63],v[4:7],a[4:7]
mm a[4:7],v[56:59],v[0:3],a[4:7]
rd v[0:3],v236 offset:8192
rd v[4:7],v237 offset:8192
mm a[20:23],v[56:59],v[8:11],a[20:23]
s_add_u32 m0, s83, 36992
mm a[20:23],v[60:63],v[12:15],a[20:23]
ld v235,s[44:47],s80 offen lds
mm a[16:19],v[52:55],v[12:15],a[16:19]
s_mov_b32 s80, 1835008
mm a[16:19],v[48:51],v[8:11],a[16:19]
rt v[216:217],v243 offset:0
rt v[218:219],v243 offset:1024
mm a[84:87],v[40:43],v[8:11],a[84:87]
s_add_u32 m0, s83, 50688
mm a[84:87],v[44:47],v[12:15],a[84:87]
ld v232,s[44:47],s80 offen lds
mm a[80:83],v[36:39],v[12:15],a[80:83]
mm a[80:83],v[32:35],v[8:11],a[80:83]
rd v[8:11],v236 offset:10240
rd v[12:15],v237 offset:10240
mm a[240:243],v[32:35],v[16:19],a[240:243]
s_add_u32 m0, s83, 51712
mm a[240:243],v[36:39],v[20:23],a[240:243]
ld v233,s[44:47],s80 offen lds
mm a[244:247],v[44:47],v[20:23],a[244:247]
mm a[244:247],v[40:43],v[16:19],a[244:247]
rt v[220:221],v243 offset:16896
rt v[222:223],v243 offset:17920
mm a[32:35],v[48:51],v[16:19],a[32:35]
s_add_u32 m0, s83, 52864
mm a[32:35],v[52:55],v[20:23],a[32:35]
ld v234,s[44:47],s80 offen lds
mm a[36:39],v[60:63],v[20:23],a[36:39]
mm a[36:39],v[56:59],v[16:19],a[36:39]
rd v[16:19],v236 offset:0
rd v[20:23],v237 offset:0
mm a[60:63],v[56:59],v[24:27],a[60:63]
s_add_u32 m0, s83, 53888
mm a[60:63],v[60:63],v[28:31],a[60:63]
ld v235,s[44:47],s80 offen lds
mm a[56:59],v[52:55],v[28:31],a[56:59]
s_mov_b64 s[44:45], s[68:69]
s_mov_b32 s46, s70
mm a[56:59],v[48:51],v[24:27],a[56:59]
mm a[52:55],v[40:43],v[24:27],a[52:55]
mm a[52:55],v[44:47],v[28:31],a[52:55]
mm a[48:51],v[36:39],v[28:31],a[48:51]
wt lgkmcnt(2)
s_lshl_b32 s80, s77, 18
mm a[48:51],v[32:35],v[24:27],a[48:51]
)ASM" R"ASM(rd v[24:27],v236 offset:2048
rd v[28:31],v237 offset:2048
mm a[96:99],v[192:195],v[160:163],a[96:99]
mm a[96:99],v[196:199],v[164:167],a[96:99]
mm a[100:103],v[204:207],v[164:167],a[100:103]
mm a[100:103],v[200:203],v[160:163],a[100:103]
rt v[32:33],v240 offset:128
rt v[34:35],v240 offset:1152
mm a[104:107],v[208:211],v[160:163],a[104:107]
mm a[104:107],v[212:215],v[164:167],a[104:107]
mm a[108:111],v[220:223],v[164:167],a[108:111]
wt lgkmcnt(2)
s_barrier
mm a[108:111],v[216:219],v[160:163],a[108:111]
mm a[140:143],v[216:219],v[168:171],a[140:143]
mm a[140:143],v[220:223],v[172:175],a[140:143]
mm a[136:139],v[212:215],v[172:175],a[136:139]
mm a[136:139],v[208:211],v[168:171],a[136:139]
rt v[36:37],v240 offset:17024
rt v[38:39],v240 offset:18048
mm a[124:127],v[200:203],v[168:171],a[124:127]
mm a[124:127],v[204:207],v[172:175],a[124:127]
mm a[120:123],v[196:199],v[172:175],a[120:123]
mm a[120:123],v[192:195],v[168:171],a[120:123]
mm a[232:235],v[192:195],v[176:179],a[232:235]
mm a[232:235],v[196:199],v[180:183],a[232:235]
mm a[236:239],v[204:207],v[180:183],a[236:239]
mm a[236:239],v[200:203],v[176:179],a[236:239]
rt v[40:41],v241 offset:128
rt v[42:43],v241 offset:1152
mm a[152:155],v[208:211],v[176:179],a[152:155]
mm a[152:155],v[212:215],v[180:183],a[152:155]
mm a[156:159],v[220:223],v[180:183],a[156:159]
mm a[156:159],v[216:219],v[176:179],a[156:159]
mm a[172:175],v[216:219],v[184:187],a[172:175]
mm a[172:175],v[220:223],v[188:191],a[172:175]
mm a[168:171],v[212:215],v[188:191],a[168:171]
mm a[168:171],v[208:211],v[184:187],a[168:171]
rt v[44:45],v241 offset:17024
rt v[46:47],v241 offset:18048
mm a[220:223],v[200:203],v[184:187],a[220:223]
mm a[220:223],v[204:207],v[188:191],a[220:223]
mm a[216:219],v[196:199],v[188:191],a[216:219]
mm a[216:219],v[192:195],v[184:187],a[216:219]
mm a[200:203],v[192:195],v[0:3],a[200:203]
mm a[200:203],v[196:199],v[4:7],a[200:203]
mm a[204:207],v[204:207],v[4:7],a[204:207]
mm a[204:207],v[200:203],v[0:3],a[204:207]
rt v[48:49],v242 offset:128
rt v[50:51],v242 offset:1152
mm a[184:187],v[208:211],v[0:3],a[184:187]
mm a[184:187],v[212:215],v[4:7],a[184:187]
mm a[188:191],v[220:223],v[4:7],a[188:191]
mm a[188:191],v[216:219],v[0:3],a[188:191]
mm a[12:15],v[216:219],v[8:11],a[12:15]
mm a[12:15],v[220:223],v[12:15],a[12:15]
buffer_load_dword v254, v135, s[68:71], s96 offen
mm a[8:11],v[212:215],v[12:15],a[8:11]
s_add_u32 s96, s96, 1048576
mm a[8:11],v[208:211],v[8:11],a[8:11]
rt v[52:53],v242 offset:17024
rt v[54:55],v242 offset:18048
mm a[76:79],v[200:203],v[8:11],a[76:79]
mm a[76:79],v[204:207],v[12:15],a[76:79]
buffer_load_dword v254, v135, s[68:71], s96 offen
mm a[72:75],v[196:199],v[12:15],a[72:75]
s_add_u32 s96, s96, 1048576
mm a[72:75],v[192:195],v[8:11],a[72:75]
rt v[56:57],v243 offset:128
rt v[58:59],v243 offset:1152
mm a[88:91],v[192:195],v[16:19],a[88:91]
mm a[88:91],v[196:199],v[20:23],a[88:91]
mm a[92:95],v[204:207],v[20:23],a[92:95]
mm a[92:95],v[200:203],v[16:19],a[92:95]
rt v[60:61],v243 offset:17024
rt v[62:63],v243 offset:18048
mm a[24:27],v[208:211],v[16:19],a[24:27]
mm a[24:27],v[212:215],v[20:23],a[24:27]
mm a[28:31],v[220:223],v[20:23],a[28:31]
mm a[28:31],v[216:219],v[16:19],a[28:31]
mm a[44:47],v[216:219],v[24:27],a[44:47]
mm a[44:47],v[220:223],v[28:31],a[44:47]
ld v[72:75],v255,s[64:67],s89 offen offset:1024 sc1
mm a[40:43],v[212:215],v[28:31],a[40:43]
mm a[40:43],v[208:211],v[24:27],a[40:43]
mm a[252:255],v[200:203],v[24:27],a[252:255]
mm a[252:255],v[204:207],v[28:31],a[252:255]
mm a[248:251],v[196:199],v[28:31],a[248:251]
wt vmcnt(3) lgkmcnt(0)
s_barrier
mm a[248:251],v[192:195],v[24:27],a[248:251]
mm a[112:115],v[32:35],v[160:163],a[112:115]
mm a[112:115],v[36:39],v[164:167],a[112:115]
mm a[116:119],v[44:47],v[164:167],a[116:119]
rt v[192:193],v240 offset:33792
rt v[194:195],v240 offset:34816
mm a[116:119],v[40:43],v[160:163],a[116:119]
rt v[196:197],v240 offset:50688
rt v[198:199],v240 offset:51712
mm a[128:131],v[48:51],v[160:163],a[128:131]
)ASM" R"ASM(mm a[128:131],v[52:55],v[164:167],a[128:131]
mm a[132:135],v[60:63],v[164:167],a[132:135]
mm a[132:135],v[56:59],v[160:163],a[132:135]
rd v[160:163],v236 offset:57344
rd v[164:167],v237 offset:57344
mm a[148:151],v[56:59],v[168:171],a[148:151]
mm a[148:151],v[60:63],v[172:175],a[148:151]
mm a[144:147],v[52:55],v[172:175],a[144:147]
mm a[144:147],v[48:51],v[168:171],a[144:147]
rt v[200:201],v241 offset:33792
rt v[202:203],v241 offset:34816
mm a[228:231],v[40:43],v[168:171],a[228:231]
mm a[228:231],v[44:47],v[172:175],a[228:231]
mm a[224:227],v[36:39],v[172:175],a[224:227]
mm a[224:227],v[32:35],v[168:171],a[224:227]
rd v[168:171],v236 offset:59392
rd v[172:175],v237 offset:59392
mm a[208:211],v[32:35],v[176:179],a[208:211]
mm a[208:211],v[36:39],v[180:183],a[208:211]
mm a[212:215],v[44:47],v[180:183],a[212:215]
mm a[212:215],v[40:43],v[176:179],a[212:215]
rt v[204:205],v241 offset:50688
rt v[206:207],v241 offset:51712
mm a[160:163],v[48:51],v[176:179],a[160:163]
mm a[160:163],v[52:55],v[180:183],a[160:163]
mm a[164:167],v[60:63],v[180:183],a[164:167]
mm a[164:167],v[56:59],v[176:179],a[164:167]
rd v[176:179],v236 offset:49152
rd v[180:183],v237 offset:49152
mm a[180:183],v[56:59],v[184:187],a[180:183]
mm a[180:183],v[60:63],v[188:191],a[180:183]
mm a[176:179],v[52:55],v[188:191],a[176:179]
mm a[176:179],v[48:51],v[184:187],a[176:179]
rt v[208:209],v242 offset:33792
rt v[210:211],v242 offset:34816
mm a[196:199],v[40:43],v[184:187],a[196:199]
s_add_u32 m0, s83, 0
mm a[196:199],v[44:47],v[188:191],a[196:199]
ld v232,s[44:47],s80 offen lds
mm a[192:195],v[36:39],v[188:191],a[192:195]
mm a[192:195],v[32:35],v[184:187],a[192:195]
rd v[184:187],v236 offset:51200
rd v[188:191],v237 offset:51200
mm a[64:67],v[32:35],v[0:3],a[64:67]
s_add_u32 m0, s83, 1024
mm a[64:67],v[36:39],v[4:7],a[64:67]
ld v233,s[44:47],s80 offen lds
mm a[68:71],v[44:47],v[4:7],a[68:71]
mm a[68:71],v[40:43],v[0:3],a[68:71]
rt v[212:213],v242 offset:50688
rt v[214:215],v242 offset:51712
mm a[0:3],v[48:51],v[0:3],a[0:3]
s_add_u32 m0, s83, 2176
mm a[0:3],v[52:55],v[4:7],a[0:3]
ld v234,s[44:47],s80 offen lds
mm a[4:7],v[60:63],v[4:7],a[4:7]
mm a[4:7],v[56:59],v[0:3],a[4:7]
rd v[0:3],v236 offset:40960
rd v[4:7],v237 offset:40960
mm a[20:23],v[56:59],v[8:11],a[20:23]
s_add_u32 m0, s83, 3200
mm a[20:23],v[60:63],v[12:15],a[20:23]
ld v235,s[44:47],s80 offen lds
mm a[16:19],v[52:55],v[12:15],a[16:19]
s_add_u32 s80, s80, 524288
mm a[16:19],v[48:51],v[8:11],a[16:19]
rt v[216:217],v243 offset:33792
rt v[218:219],v243 offset:34816
mm a[84:87],v[40:43],v[8:11],a[84:87]
s_add_u32 m0, s83, 16896
mm a[84:87],v[44:47],v[12:15],a[84:87]
ld v232,s[44:47],s80 offen lds
mm a[80:83],v[36:39],v[12:15],a[80:83]
mm a[80:83],v[32:35],v[8:11],a[80:83]
rd v[8:11],v236 offset:43008
rd v[12:15],v237 offset:43008
mm a[240:243],v[32:35],v[16:19],a[240:243]
s_add_u32 m0, s83, 17920
mm a[240:243],v[36:39],v[20:23],a[240:243]
ld v233,s[44:47],s80 offen lds
mm a[244:247],v[44:47],v[20:23],a[244:247]
mm a[244:247],v[40:43],v[16:19],a[244:247]
rt v[220:221],v243 offset:50688
rt v[222:223],v243 offset:51712
mm a[32:35],v[48:51],v[16:19],a[32:35]
s_add_u32 m0, s83, 19072
mm a[32:35],v[52:55],v[20:23],a[32:35]
ld v234,s[44:47],s80 offen lds
mm a[36:39],v[60:63],v[20:23],a[36:39]
mm a[36:39],v[56:59],v[16:19],a[36:39]
rd v[16:19],v236 offset:32768
rd v[20:23],v237 offset:32768
mm a[60:63],v[56:59],v[24:27],a[60:63]
s_add_u32 m0, s83, 20096
mm a[60:63],v[60:63],v[28:31],a[60:63]
ld v235,s[44:47],s80 offen lds
mm a[56:59],v[52:55],v[28:31],a[56:59]
s_add_u32 s80, s80, 524288
mm a[56:59],v[48:51],v[24:27],a[56:59]
mm a[52:55],v[40:43],v[24:27],a[52:55]
)ASM" R"ASM(mm a[52:55],v[44:47],v[28:31],a[52:55]
mm a[48:51],v[36:39],v[28:31],a[48:51]
wt lgkmcnt(2)
mm a[48:51],v[32:35],v[24:27],a[48:51]
rd v[24:27],v236 offset:34816
rd v[28:31],v237 offset:34816
mm a[96:99],v[192:195],v[160:163],a[96:99]
mm a[96:99],v[196:199],v[164:167],a[96:99]
mm a[100:103],v[204:207],v[164:167],a[100:103]
mm a[100:103],v[200:203],v[160:163],a[100:103]
rt v[32:33],v240 offset:33920
rt v[34:35],v240 offset:34944
mm a[104:107],v[208:211],v[160:163],a[104:107]
mm a[104:107],v[212:215],v[164:167],a[104:107]
mm a[108:111],v[220:223],v[164:167],a[108:111]
wt lgkmcnt(2)
s_barrier
mm a[108:111],v[216:219],v[160:163],a[108:111]
mm a[140:143],v[216:219],v[168:171],a[140:143]
wt vmcnt(8)
v_cmp_eq_u64 vcc, v[72:73], v[252:253]
mm a[140:143],v[220:223],v[172:175],a[140:143]
v_cmp_eq_u64 s[96:97], v[74:75], v[252:253]
mm a[136:139],v[212:215],v[172:175],a[136:139]
s_and_b64 vcc, vcc, s[96:97]
s_cmp_eq_u64 vcc, -1
s_cbranch_scc0 .La5_LK_XSPIN
.La5_LK_XOK:
mm a[136:139],v[208:211],v[168:171],a[136:139]
rt v[36:37],v240 offset:50816
rt v[38:39],v240 offset:51840
mm a[124:127],v[200:203],v[168:171],a[124:127]
s_mov_b32 s89, 49152
mm a[124:127],v[204:207],v[172:175],a[124:127]
mm a[120:123],v[196:199],v[172:175],a[120:123]
ar v224,a96
ar v225,a97
mm a[120:123],v[192:195],v[168:171],a[120:123]
ar v226,a98
ar v227,a99
mm a[232:235],v[192:195],v[176:179],a[232:235]
ar v228,a100
ar v229,a101
mm a[232:235],v[196:199],v[180:183],a[232:235]
ld v[128:131],v244,s[52:55],s91 offen offset:0 sc1
mm a[236:239],v[204:207],v[180:183],a[236:239]
ar v230,a102
ar v231,a103
mm a[236:239],v[200:203],v[176:179],a[236:239]
rt v[40:41],v241 offset:33920
rt v[42:43],v241 offset:34944
mm a[152:155],v[208:211],v[176:179],a[152:155]
cv v248,v224,v225
cv v249,v226,v227
mm a[152:155],v[212:215],v[180:183],a[152:155]
ld v[132:135],v245,s[52:55],s91 offen offset:0 sc1
mm a[156:159],v[220:223],v[180:183],a[156:159]
cv v250,v228,v229
cv v251,v230,v231
mm a[156:159],v[216:219],v[176:179],a[156:159]
st v[248:251],v255,s[48:51],s89 offen sc1
mm a[172:175],v[216:219],v[184:187],a[172:175]
s_mov_b32 s89, 50176
ds_write_b128 v238, v[248:251] offset:0
mm a[172:175],v[220:223],v[188:191],a[172:175]
ld v[136:139],v246,s[52:55],s91 offen offset:0 sc1
mm a[168:171],v[212:215],v[188:191],a[168:171]
ar v224,a104
ar v225,a105
mm a[168:171],v[208:211],v[184:187],a[168:171]
rt v[44:45],v241 offset:50816
rt v[46:47],v241 offset:51840
mm a[220:223],v[200:203],v[184:187],a[220:223]
ar v226,a106
ar v227,a107
mm a[220:223],v[204:207],v[188:191],a[220:223]
ld v[140:143],v247,s[52:55],s91 offen offset:0 sc1
mm a[216:219],v[196:199],v[188:191],a[216:219]
ar v228,a108
ar v229,a109
mm a[216:219],v[192:195],v[184:187],a[216:219]
ar v230,a110
ar v231,a111
mm a[200:203],v[192:195],v[0:3],a[200:203]
cv v248,v224,v225
cv v249,v226,v227
.La5_LW1_3:
s_cbranch_execz .La5_LW1_7
mm a[200:203],v[196:199],v[4:7],a[200:203]
ld v[144:147],v244,s[52:55],s91 offen offset:1024 sc1
mm a[204:207],v[204:207],v[4:7],a[204:207]
cv v250,v228,v229
cv v251,v230,v231
mm a[204:207],v[200:203],v[0:3],a[204:207]
rt v[48:49],v242 offset:33920
rt v[50:51],v242 offset:34944
mm a[184:187],v[208:211],v[0:3],a[184:187]
st v[248:251],v255,s[48:51],s89 offen sc1
mm a[184:187],v[212:215],v[4:7],a[184:187]
ld v[148:151],v245,s[52:55],s91 offen offset:1024 sc1
)ASM" R"ASM(mm a[188:191],v[220:223],v[4:7],a[188:191]
s_mov_b32 s89, 51200
ds_write_b128 v238, v[248:251] offset:1024
mm a[188:191],v[216:219],v[0:3],a[188:191]
mm a[12:15],v[216:219],v[8:11],a[12:15]
mm a[12:15],v[220:223],v[12:15],a[12:15]
ld v[152:155],v246,s[52:55],s91 offen offset:1024 sc1
mm a[8:11],v[212:215],v[12:15],a[8:11]
mm a[8:11],v[208:211],v[8:11],a[8:11]
rt v[52:53],v242 offset:50816
rt v[54:55],v242 offset:51840
mm a[76:79],v[200:203],v[8:11],a[76:79]
mm a[76:79],v[204:207],v[12:15],a[76:79]
ld v[156:159],v247,s[52:55],s91 offen offset:1024 sc1
mm a[72:75],v[196:199],v[12:15],a[72:75]
mm a[72:75],v[192:195],v[8:11],a[72:75]
rt v[56:57],v243 offset:33920
rt v[58:59],v243 offset:34944
mm a[88:91],v[192:195],v[16:19],a[88:91]
mm a[88:91],v[196:199],v[20:23],a[88:91]
mm a[92:95],v[204:207],v[20:23],a[92:95]
mm a[92:95],v[200:203],v[16:19],a[92:95]
rt v[60:61],v243 offset:50816
rt v[62:63],v243 offset:51840
mm a[24:27],v[208:211],v[16:19],a[24:27]
mm a[24:27],v[212:215],v[20:23],a[24:27]
mm a[28:31],v[220:223],v[20:23],a[28:31]
mm a[28:31],v[216:219],v[16:19],a[28:31]
mm a[44:47],v[216:219],v[24:27],a[44:47]
mm a[44:47],v[220:223],v[28:31],a[44:47]
mm a[40:43],v[212:215],v[28:31],a[40:43]
mm a[40:43],v[208:211],v[24:27],a[40:43]
mm a[252:255],v[200:203],v[24:27],a[252:255]
mm a[252:255],v[204:207],v[28:31],a[252:255]
mm a[248:251],v[196:199],v[28:31],a[248:251]
wt vmcnt(10) lgkmcnt(0)
s_barrier
mm a[248:251],v[192:195],v[24:27],a[248:251]
mm a[112:115],v[32:35],v[160:163],a[112:115]
s_add_u32 m0, s83, 33792
mm a[112:115],v[36:39],v[164:167],a[112:115]
ld v232,s[44:47],s80 offen lds
mm a[116:119],v[44:47],v[164:167],a[116:119]
mm a[116:119],v[40:43],v[160:163],a[116:119]
rt v[192:193],v240 offset:0
rt v[194:195],v240 offset:1024
mm a[128:131],v[48:51],v[160:163],a[128:131]
s_add_u32 m0, s83, 34816
mm a[128:131],v[52:55],v[164:167],a[128:131]
ld v233,s[44:47],s80 offen lds
mm a[132:135],v[60:63],v[164:167],a[132:135]
mm a[132:135],v[56:59],v[160:163],a[132:135]
rt v[196:197],v240 offset:8448
rt v[198:199],v240 offset:9472
mm a[148:151],v[56:59],v[168:171],a[148:151]
s_add_u32 m0, s83, 35968
mm a[148:151],v[60:63],v[172:175],a[148:151]
ld v234,s[44:47],s80 offen lds
mm a[144:147],v[52:55],v[172:175],a[144:147]
ar v224,a112
ar v225,a113
mm a[144:147],v[48:51],v[168:171],a[144:147]
rt v[200:201],v241 offset:0
rt v[202:203],v241 offset:1024
mm a[228:231],v[40:43],v[168:171],a[228:231]
s_add_u32 m0, s83, 36992
ar v226,a114
mm a[228:231],v[44:47],v[172:175],a[228:231]
ld v235,s[44:47],s80 offen lds
mm a[224:227],v[36:39],v[172:175],a[224:227]
s_add_u32 s80, s80, 524288
ar v227,a115
mm a[224:227],v[32:35],v[168:171],a[224:227]
rt v[204:205],v241 offset:8448
rt v[206:207],v241 offset:9472
mm a[208:211],v[32:35],v[176:179],a[208:211]
s_add_u32 m0, s83, 50688
ar v228,a116
mm a[208:211],v[36:39],v[180:183],a[208:211]
ld v232,s[44:47],s80 offen lds
mm a[212:215],v[44:47],v[180:183],a[212:215]
ar v229,a117
ar v230,a118
mm a[212:215],v[40:43],v[176:179],a[212:215]
rt v[208:209],v242 offset:0
rt v[210:211],v242 offset:1024
mm a[160:163],v[48:51],v[176:179],a[160:163]
s_add_u32 m0, s83, 51712
ar v231,a119
mm a[160:163],v[52:55],v[180:183],a[160:163]
ld v233,s[44:47],s80 offen lds
mm a[164:167],v[60:63],v[180:183],a[164:167]
cv v248,v224,v225
cv v249,v226,v227
mm a[164:167],v[56:59],v[176:179],a[164:167]
rt v[212:213],v242 offset:8448
rt v[214:215],v242 offset:9472
mm a[180:183],v[56:59],v[184:187],a[180:183]
s_add_u32 m0, s83, 52864
cv v250,v228,v229
)ASM" R"ASM(mm a[180:183],v[60:63],v[188:191],a[180:183]
ld v234,s[44:47],s80 offen lds
mm a[176:179],v[52:55],v[188:191],a[176:179]
cv v251,v230,v231
st v[248:251],v255,s[48:51],s89 offen sc1
mm a[176:179],v[48:51],v[184:187],a[176:179]
rt v[216:217],v243 offset:0
rt v[218:219],v243 offset:1024
mm a[196:199],v[40:43],v[184:187],a[196:199]
s_add_u32 m0, s83, 53888
s_mov_b32 s89, 52224
mm a[196:199],v[44:47],v[188:191],a[196:199]
ld v235,s[44:47],s80 offen lds
mm a[192:195],v[36:39],v[188:191],a[192:195]
s_add_u32 s80, s80, 524288
ds_write_b128 v238, v[248:251] offset:2048
mm a[192:195],v[32:35],v[184:187],a[192:195]
rt v[220:221],v243 offset:8448
rt v[222:223],v243 offset:9472
mm a[64:67],v[32:35],v[0:3],a[64:67]
mm a[64:67],v[36:39],v[4:7],a[64:67]
ld v[160:163],v244,s[52:55],s91 offen offset:2048 sc1
mm a[68:71],v[44:47],v[4:7],a[68:71]
mm a[68:71],v[40:43],v[0:3],a[68:71]
mm a[0:3],v[48:51],v[0:3],a[0:3]
mm a[0:3],v[52:55],v[4:7],a[0:3]
ld v[164:167],v245,s[52:55],s91 offen offset:2048 sc1
mm a[4:7],v[60:63],v[4:7],a[4:7]
mm a[4:7],v[56:59],v[0:3],a[4:7]
mm a[20:23],v[56:59],v[8:11],a[20:23]
mm a[20:23],v[60:63],v[12:15],a[20:23]
ld v[168:171],v246,s[52:55],s91 offen offset:2048 sc1
mm a[16:19],v[52:55],v[12:15],a[16:19]
mm a[16:19],v[48:51],v[8:11],a[16:19]
mm a[84:87],v[40:43],v[8:11],a[84:87]
mm a[84:87],v[44:47],v[12:15],a[84:87]
ld v[172:175],v247,s[52:55],s91 offen offset:2048 sc1
mm a[80:83],v[36:39],v[12:15],a[80:83]
mm a[80:83],v[32:35],v[8:11],a[80:83]
mm a[240:243],v[32:35],v[16:19],a[240:243]
mm a[240:243],v[36:39],v[20:23],a[240:243]
mm a[244:247],v[44:47],v[20:23],a[244:247]
mm a[244:247],v[40:43],v[16:19],a[244:247]
mm a[32:35],v[48:51],v[16:19],a[32:35]
mm a[32:35],v[52:55],v[20:23],a[32:35]
mm a[36:39],v[60:63],v[20:23],a[36:39]
mm a[36:39],v[56:59],v[16:19],a[36:39]
mm a[60:63],v[56:59],v[24:27],a[60:63]
mm a[60:63],v[60:63],v[28:31],a[60:63]
mm a[56:59],v[52:55],v[28:31],a[56:59]
mm a[56:59],v[48:51],v[24:27],a[56:59]
mm a[52:55],v[40:43],v[24:27],a[52:55]
mm a[52:55],v[44:47],v[28:31],a[52:55]
mm a[48:51],v[36:39],v[28:31],a[48:51]
mm a[48:51],v[32:35],v[24:27],a[48:51]
s_branch .La5_LQ_START
.La5_LV_XSPIN:
s_mov_b32 s93, 0
.La5_LV_XSPIN_L:
s_add_u32 s93, s93, 1
s_cmp_gt_u32 s93, 2097152
s_cbranch_scc1 .La5_LV_XOK
s_sleep 8
ld v[72:75],v255,s[64:67],s89 offen offset:1024 sc1
wt vmcnt(0)
v_cmp_eq_u64 vcc, v[72:73], v[252:253]
v_cmp_eq_u64 s[96:97], v[74:75], v[252:253]
s_nop 1
s_and_b64 vcc, vcc, s[96:97]
s_cmp_eq_u64 vcc, -1
s_nop 0
s_nop 0
s_cbranch_scc0 .La5_LV_XSPIN_L
s_branch .La5_LV_XOK
.La5_LK_XSPIN:
s_mov_b32 s93, 0
.La5_LK_XSPIN_L:
s_add_u32 s93, s93, 1
s_cmp_gt_u32 s93, 2097152
s_nop 0
s_nop 0
s_cbranch_scc1 .La5_LK_XOK
s_sleep 8
ld v[72:75],v255,s[64:67],s89 offen offset:1024 sc1
wt vmcnt(0)
v_cmp_eq_u64 vcc, v[72:73], v[252:253]
v_cmp_eq_u64 s[96:97], v[74:75], v[252:253]
s_nop 1
s_and_b64 vcc, vcc, s[96:97]
s_cmp_eq_u64 vcc, -1
s_cbranch_scc0 .La5_LK_XSPIN_L
s_branch .La5_LK_XOK
; ---- V tile (waves 1, 3): the same with the operands exchanged (V comes out transposed)
.La5_LV_START:
.La5_LW1_4:
s_cbranch_execz .La5_LW1_8
mm a[96:99],v[160:163],v[192:195],0
s_add_u32 m0, s83, 33792
mm a[96:99],v[164:167],v[196:199],a[96:99]
ld v232,s[44:47],s80 offen lds
)ASM" R"ASM(mm a[104:107],v[164:167],v[204:207],0
rd v[0:3],v236 offset:8192
rd v[4:7],v237 offset:8192
s_add_u32 m0, s83, 34816
mm a[104:107],v[160:163],v[200:203],a[104:107]
rd v[8:11],v236 offset:10240
rd v[12:15],v237 offset:10240
ld v233,s[44:47],s80 offen lds
mm a[112:115],v[160:163],v[208:211],0
s_add_u32 m0, s83, 35968
mm a[112:115],v[164:167],v[212:215],a[112:115]
ld v234,s[44:47],s80 offen lds
mm a[128:131],v[164:167],v[220:223],0
rd v[16:19],v236 offset:0
rd v[20:23],v237 offset:0
s_add_u32 m0, s83, 36992
mm a[128:131],v[160:163],v[216:219],a[128:131]
rd v[24:27],v236 offset:2048
rd v[28:31],v237 offset:2048
ld v235,s[44:47],s80 offen lds
mm a[132:135],v[168:171],v[216:219],0
s_mov_b32 s80, 2883584
mm a[132:135],v[172:175],v[220:223],a[132:135]
mm a[116:119],v[172:175],v[212:215],0
s_xor_b32 m0, s82, 32768
mm a[116:119],v[168:171],v[208:211],a[116:119]
rt v[32:33],v240 offset:128
rt v[34:35],v240 offset:1152
ld v224,s[40:43],s79 offen lds
mm a[108:111],v[168:171],v[200:203],0
s_xor_b32 m0, s82, 33792
mm a[108:111],v[172:175],v[204:207],a[108:111]
ld v225,s[40:43],s79 offen lds
mm a[100:103],v[172:175],v[196:199],0
s_xor_b32 m0, s82, 34816
mm a[100:103],v[168:171],v[192:195],a[100:103]
rt v[36:37],v240 offset:17024
rt v[38:39],v240 offset:18048
ld v226,s[40:43],s79 offen lds
mm a[232:235],v[176:179],v[192:195],0
s_xor_b32 m0, s82, 35840
mm a[232:235],v[180:183],v[196:199],a[232:235]
ld v227,s[40:43],s79 offen lds
mm a[152:155],v[180:183],v[204:207],0
s_xor_b32 m0, s82, 36864
mm a[152:155],v[176:179],v[200:203],a[152:155]
rt v[40:41],v241 offset:128
rt v[42:43],v241 offset:1152
ld v228,s[40:43],s79 offen lds
mm a[208:211],v[176:179],v[208:211],0
s_xor_b32 m0, s82, 37888
mm a[208:211],v[180:183],v[212:215],a[208:211]
ld v229,s[40:43],s79 offen lds
mm a[160:163],v[180:183],v[220:223],0
s_xor_b32 m0, s82, 38912
mm a[160:163],v[176:179],v[216:219],a[160:163]
ld v230,s[40:43],s79 offen lds
mm a[164:167],v[184:187],v[216:219],0
s_xor_b32 m0, s82, 39936
mm a[164:167],v[188:191],v[220:223],a[164:167]
ld v231,s[40:43],s79 offen lds
mm a[212:215],v[188:191],v[212:215],0
s_mov_b32 s79, 768
mm a[212:215],v[184:187],v[208:211],a[212:215]
rt v[44:45],v241 offset:17024
rt v[46:47],v241 offset:18048
mm a[156:159],v[184:187],v[200:203],0
s_add_u32 m0, s83, 50688
mm a[156:159],v[188:191],v[204:207],a[156:159]
ld v232,s[44:47],s80 offen lds
mm a[236:239],v[188:191],v[196:199],0
wt lgkmcnt(8)
s_add_u32 m0, s83, 51712
mm a[236:239],v[184:187],v[192:195],a[236:239]
ld v233,s[44:47],s80 offen lds
mm a[200:203],v[0:3],v[192:195],0
s_add_u32 m0, s83, 52864
mm a[200:203],v[4:7],v[196:199],a[200:203]
ld v234,s[44:47],s80 offen lds
mm a[184:187],v[4:7],v[204:207],0
s_add_u32 m0, s83, 53888
mm a[184:187],v[0:3],v[200:203],a[184:187]
rt v[48:49],v242 offset:128
rt v[50:51],v242 offset:1152
ld v235,s[44:47],s80 offen lds
mm a[64:67],v[0:3],v[208:211],0
s_mov_b32 s80, 3145728
mm a[64:67],v[4:7],v[212:215],a[64:67]
mm a[0:3],v[4:7],v[220:223],0
mm a[0:3],v[0:3],v[216:219],a[0:3]
mm a[4:7],v[8:11],v[216:219],0
mm a[4:7],v[12:15],v[220:223],a[4:7]
buffer_load_dword v70, v118, s[60:63], 0 offen
mm a[68:71],v[12:15],v[212:215],0
mm a[68:71],v[8:11],v[208:211],a[68:71]
rt v[52:53],v242 offset:17024
rt v[54:55],v242 offset:18048
mm a[188:191],v[8:11],v[200:203],0
mm a[188:191],v[12:15],v[204:207],a[188:191]
ld v[72:75],v119,s[60:63],0 offen offset:0
)ASM" R"ASM(mm a[204:207],v[12:15],v[196:199],0
mm a[204:207],v[8:11],v[192:195],a[204:207]
rt v[56:57],v243 offset:128
rt v[58:59],v243 offset:1152
mm a[88:91],v[16:19],v[192:195],0
mm a[88:91],v[20:23],v[196:199],a[88:91]
ld v[76:79],v119,s[60:63],0 offen offset:128
mm a[24:27],v[20:23],v[204:207],0
mm a[24:27],v[16:19],v[200:203],a[24:27]
rt v[60:61],v243 offset:17024
rt v[62:63],v243 offset:18048
mm a[240:243],v[16:19],v[208:211],0
mm a[240:243],v[20:23],v[212:215],a[240:243]
ld v[80:83],v119,s[60:63],0 offen offset:256
mm a[32:35],v[20:23],v[220:223],0
mm a[32:35],v[16:19],v[216:219],a[32:35]
mm a[36:39],v[24:27],v[216:219],0
mm a[36:39],v[28:31],v[220:223],a[36:39]
ld v[84:87],v119,s[60:63],0 offen offset:384
mm a[244:247],v[28:31],v[212:215],0
mm a[244:247],v[24:27],v[208:211],a[244:247]
mm a[28:31],v[24:27],v[200:203],0
mm a[28:31],v[28:31],v[204:207],a[28:31]
mm a[92:95],v[28:31],v[196:199],0
mm a[92:95],v[24:27],v[192:195],a[92:95]
s_mov_b64 exec, 0
s_cmp_eq_u32 s3, 1
s_cbranch_scc1 .La5_LW2_1
s_branch .La5_LW2_R1
.La5_LW2_R1:
s_mov_b64 exec, -1
wt vmcnt(5) lgkmcnt(0)
s_barrier
mm a[120:123],v[160:163],v[32:35],0
mm a[120:123],v[164:167],v[36:39],a[120:123]
mm a[136:139],v[164:167],v[44:47],0
rt v[192:193],v240 offset:33792
rt v[194:195],v240 offset:34816
mm a[136:139],v[160:163],v[40:43],a[136:139]
rt v[196:197],v240 offset:50688
rt v[198:199],v240 offset:51712
mm a[224:227],v[160:163],v[48:51],0
mm a[224:227],v[164:167],v[52:55],a[224:227]
mm a[144:147],v[164:167],v[60:63],0
mm a[144:147],v[160:163],v[56:59],a[144:147]
rd v[160:163],v236 offset:57344
rd v[164:167],v237 offset:57344
mm a[148:151],v[168:171],v[56:59],0
mm a[148:151],v[172:175],v[60:63],a[148:151]
mm a[228:231],v[172:175],v[52:55],0
mm a[228:231],v[168:171],v[48:51],a[228:231]
rt v[200:201],v241 offset:33792
rt v[202:203],v241 offset:34816
mm a[140:143],v[168:171],v[40:43],0
mm a[140:143],v[172:175],v[44:47],a[140:143]
mm a[124:127],v[172:175],v[36:39],0
mm a[124:127],v[168:171],v[32:35],a[124:127]
rd v[168:171],v236 offset:59392
rd v[172:175],v237 offset:59392
mm a[216:219],v[176:179],v[32:35],0
s_xor_b32 m0, s82, 0
mm a[216:219],v[180:183],v[36:39],a[216:219]
ld v224,s[40:43],s79 offen lds
mm a[168:171],v[180:183],v[44:47],0
mm a[168:171],v[176:179],v[40:43],a[168:171]
rt v[204:205],v241 offset:50688
rt v[206:207],v241 offset:51712
s_xor_b32 m0, s82, 1024
mm a[192:195],v[176:179],v[48:51],0
ld v225,s[40:43],s79 offen lds
mm a[192:195],v[180:183],v[52:55],a[192:195]
mm a[176:179],v[180:183],v[60:63],0
s_xor_b32 m0, s82, 2048
mm a[176:179],v[176:179],v[56:59],a[176:179]
rd v[176:179],v236 offset:49152
rd v[180:183],v237 offset:49152
ld v226,s[40:43],s79 offen lds
mm a[180:183],v[184:187],v[56:59],0
mm a[180:183],v[188:191],v[60:63],a[180:183]
s_xor_b32 m0, s82, 3072
mm a[196:199],v[188:191],v[52:55],0
ld v227,s[40:43],s79 offen lds
mm a[196:199],v[184:187],v[48:51],a[196:199]
rt v[208:209],v242 offset:33792
rt v[210:211],v242 offset:34816
mm a[172:175],v[184:187],v[40:43],0
s_xor_b32 m0, s82, 4096
mm a[172:175],v[188:191],v[44:47],a[172:175]
ld v228,s[40:43],s79 offen lds
mm a[220:223],v[188:191],v[36:39],0
mm a[220:223],v[184:187],v[32:35],a[220:223]
rd v[184:187],v236 offset:51200
rd v[188:191],v237 offset:51200
s_xor_b32 m0, s82, 5120
mm a[72:75],v[0:3],v[32:35],0
ld v229,s[40:43],s79 offen lds
mm a[72:75],v[4:7],v[36:39],a[72:75]
mm a[8:11],v[4:7],v[44:47],0
s_xor_b32 m0, s82, 6144
mm a[8:11],v[0:3],v[40:43],a[8:11]
)ASM" R"ASM(rt v[212:213],v242 offset:50688
rt v[214:215],v242 offset:51712
ld v230,s[40:43],s79 offen lds
mm a[80:83],v[0:3],v[48:51],0
mm a[80:83],v[4:7],v[52:55],a[80:83]
s_xor_b32 m0, s82, 7168
mm a[16:19],v[4:7],v[60:63],0
ld v231,s[40:43],s79 offen lds
mm a[16:19],v[0:3],v[56:59],a[16:19]
rd v[0:3],v236 offset:40960
rd v[4:7],v237 offset:40960
s_mov_b32 s79, 896
mm a[20:23],v[8:11],v[56:59],0
mm a[20:23],v[12:15],v[60:63],a[20:23]
s_add_u32 m0, s83, 0
mm a[84:87],v[12:15],v[52:55],0
ld v232,s[44:47],s80 offen lds
mm a[84:87],v[8:11],v[48:51],a[84:87]
rt v[216:217],v243 offset:33792
rt v[218:219],v243 offset:34816
mm a[12:15],v[8:11],v[40:43],0
s_add_u32 m0, s83, 1024
mm a[12:15],v[12:15],v[44:47],a[12:15]
ld v233,s[44:47],s80 offen lds
mm a[76:79],v[12:15],v[36:39],0
wt vmcnt(10)
v_lshlrev_b32 v70, 16, v70
mm a[76:79],v[8:11],v[32:35],a[76:79]
rd v[8:11],v236 offset:43008
rd v[12:15],v237 offset:43008
s_add_u32 m0, s83, 2176
mm a[248:251],v[16:19],v[32:35],0
ld v234,s[44:47],s80 offen lds
mm a[248:251],v[20:23],v[36:39],a[248:251]
v_lshlrev_b32 v88, 16, v72
v_and_b32 v89, 0xffff0000, v72
mm a[40:43],v[20:23],v[44:47],0
s_add_u32 m0, s83, 3200
v_sub_f32 v88, v88, v70
mm a[40:43],v[16:19],v[40:43],a[40:43]
rt v[220:221],v243 offset:50688
rt v[222:223],v243 offset:51712
ld v235,s[44:47],s80 offen lds
mm a[48:51],v[16:19],v[48:51],0
s_mov_b32 s80, 3407872
v_sub_f32 v89, v89, v70
mm a[48:51],v[20:23],v[52:55],a[48:51]
v_add_f32 v120, v88, v89
v_mul_f32 v122, v88, v88
mm a[56:59],v[20:23],v[60:63],0
s_add_u32 m0, s83, 16896
v_mul_f32 v123, v89, v89
mm a[56:59],v[16:19],v[56:59],a[56:59]
rd v[16:19],v236 offset:32768
rd v[20:23],v237 offset:32768
ld v232,s[44:47],s80 offen lds
mm a[60:63],v[24:27],v[56:59],0
v_lshlrev_b32 v90, 16, v73
v_and_b32 v91, 0xffff0000, v73
mm a[60:63],v[28:31],v[60:63],a[60:63]
s_add_u32 m0, s83, 17920
v_sub_f32 v90, v90, v70
mm a[52:55],v[28:31],v[52:55],0
ld v233,s[44:47],s80 offen lds
mm a[52:55],v[24:27],v[48:51],a[52:55]
v_sub_f32 v91, v91, v70
v_add_f32 v124, v90, v91
mm a[44:47],v[24:27],v[40:43],0
s_add_u32 m0, s83, 19072
v_fmac_f32 v122, v90, v90
mm a[44:47],v[28:31],v[44:47],a[44:47]
ld v234,s[44:47],s80 offen lds
mm a[252:255],v[28:31],v[36:39],0
wt lgkmcnt(2)
v_add_f32 v120, v120, v124
mm a[252:255],v[24:27],v[32:35],a[252:255]
rd v[24:27],v236 offset:34816
rd v[28:31],v237 offset:34816
s_add_u32 m0, s83, 20096
mm a[96:99],v[160:163],v[192:195],a[96:99]
ld v235,s[44:47],s80 offen lds
mm a[96:99],v[164:167],v[196:199],a[96:99]
s_mov_b32 s80, 3670016
v_fmac_f32 v123, v91, v91
mm a[104:107],v[164:167],v[204:207],a[104:107]
v_lshlrev_b32 v92, 16, v74
v_and_b32 v93, 0xffff0000, v74
mm a[104:107],v[160:163],v[200:203],a[104:107]
rt v[32:33],v240 offset:33920
rt v[34:35],v240 offset:34944
mm a[112:115],v[160:163],v[208:211],a[112:115]
v_sub_f32 v92, v92, v70
v_sub_f32 v93, v93, v70
mm a[112:115],v[164:167],v[212:215],a[112:115]
v_add_f32 v124, v92, v93
v_fmac_f32 v122, v92, v92
mm a[128:131],v[164:167],v[220:223],a[128:131]
wt lgkmcnt(2)
s_barrier
mm a[128:131],v[160:163],v[216:219],a[128:131]
)ASM" R"ASM(v_add_f32 v120, v120, v124
v_fmac_f32 v123, v93, v93
mm a[132:135],v[168:171],v[216:219],a[132:135]
s_xor_b32 m0, s82, 32768
v_lshlrev_b32 v94, 16, v75
mm a[132:135],v[172:175],v[220:223],a[132:135]
ld v224,s[40:43],s79 offen lds
mm a[116:119],v[172:175],v[212:215],a[116:119]
v_and_b32 v95, 0xffff0000, v75
v_sub_f32 v94, v94, v70
mm a[116:119],v[168:171],v[208:211],a[116:119]
rt v[36:37],v240 offset:50816
rt v[38:39],v240 offset:51840
mm a[108:111],v[168:171],v[200:203],a[108:111]
s_xor_b32 m0, s82, 33792
v_sub_f32 v95, v95, v70
mm a[108:111],v[172:175],v[204:207],a[108:111]
ld v225,s[40:43],s79 offen lds
mm a[100:103],v[172:175],v[196:199],a[100:103]
v_add_f32 v124, v94, v95
v_fmac_f32 v122, v94, v94
mm a[100:103],v[168:171],v[192:195],a[100:103]
v_add_f32 v120, v120, v124
v_fmac_f32 v123, v95, v95
mm a[232:235],v[176:179],v[192:195],a[232:235]
s_xor_b32 m0, s82, 34816
v_lshlrev_b32 v96, 16, v76
mm a[232:235],v[180:183],v[196:199],a[232:235]
ld v226,s[40:43],s79 offen lds
mm a[152:155],v[180:183],v[204:207],a[152:155]
v_and_b32 v97, 0xffff0000, v76
v_sub_f32 v96, v96, v70
mm a[152:155],v[176:179],v[200:203],a[152:155]
rt v[40:41],v241 offset:33920
rt v[42:43],v241 offset:34944
mm a[208:211],v[176:179],v[208:211],a[208:211]
s_xor_b32 m0, s82, 35840
v_sub_f32 v97, v97, v70
mm a[208:211],v[180:183],v[212:215],a[208:211]
ld v227,s[40:43],s79 offen lds
mm a[160:163],v[180:183],v[220:223],a[160:163]
v_add_f32 v124, v96, v97
v_fmac_f32 v122, v96, v96
mm a[160:163],v[176:179],v[216:219],a[160:163]
v_add_f32 v120, v120, v124
v_fmac_f32 v123, v97, v97
mm a[164:167],v[184:187],v[216:219],a[164:167]
.La5_LW1_5:
s_cbranch_execz .La5_LW1_9
s_xor_b32 m0, s82, 36864
v_lshlrev_b32 v98, 16, v77
mm a[164:167],v[188:191],v[220:223],a[164:167]
ld v228,s[40:43],s79 offen lds
mm a[212:215],v[188:191],v[212:215],a[212:215]
v_and_b32 v99, 0xffff0000, v77
v_sub_f32 v98, v98, v70
mm a[212:215],v[184:187],v[208:211],a[212:215]
rt v[44:45],v241 offset:50816
rt v[46:47],v241 offset:51840
mm a[156:159],v[184:187],v[200:203],a[156:159]
s_xor_b32 m0, s82, 37888
v_sub_f32 v99, v99, v70
mm a[156:159],v[188:191],v[204:207],a[156:159]
ld v229,s[40:43],s79 offen lds
mm a[236:239],v[188:191],v[196:199],a[236:239]
v_add_f32 v124, v98, v99
v_fmac_f32 v122, v98, v98
mm a[236:239],v[184:187],v[192:195],a[236:239]
v_add_f32 v120, v120, v124
v_fmac_f32 v123, v99, v99
mm a[200:203],v[0:3],v[192:195],a[200:203]
s_xor_b32 m0, s82, 38912
v_lshlrev_b32 v100, 16, v78
mm a[200:203],v[4:7],v[196:199],a[200:203]
ld v230,s[40:43],s79 offen lds
mm a[184:187],v[4:7],v[204:207],a[184:187]
v_and_b32 v101, 0xffff0000, v78
v_sub_f32 v100, v100, v70
mm a[184:187],v[0:3],v[200:203],a[184:187]
rt v[48:49],v242 offset:33920
rt v[50:51],v242 offset:34944
mm a[64:67],v[0:3],v[208:211],a[64:67]
s_xor_b32 m0, s82, 39936
v_sub_f32 v101, v101, v70
mm a[64:67],v[4:7],v[212:215],a[64:67]
ld v231,s[40:43],s79 offen lds
mm a[0:3],v[4:7],v[220:223],a[0:3]
s_mov_b32 s79, 0
v_add_f32 v124, v100, v101
mm a[0:3],v[0:3],v[216:219],a[0:3]
v_fmac_f32 v122, v100, v100
v_add_f32 v120, v120, v124
mm a[4:7],v[8:11],v[216:219],a[4:7]
v_fmac_f32 v123, v101, v101
v_lshlrev_b32 v102, 16, v79
mm a[4:7],v[12:15],v[220:223],a[4:7]
buffer_load_dword v254, v132, s[44:47], s86 offen
mm a[68:71],v[12:15],v[212:215],a[68:71]
s_add_u32 s86, s86, 2097152
s_and_b32 s86, s86, 4194303
)ASM" R"ASM(mm a[68:71],v[8:11],v[208:211],a[68:71]
rt v[52:53],v242 offset:50816
rt v[54:55],v242 offset:51840
mm a[188:191],v[8:11],v[200:203],a[188:191]
v_and_b32 v103, 0xffff0000, v79
v_sub_f32 v102, v102, v70
mm a[188:191],v[12:15],v[204:207],a[188:191]
v_sub_f32 v103, v103, v70
v_add_f32 v124, v102, v103
mm a[204:207],v[12:15],v[196:199],a[204:207]
v_fmac_f32 v122, v102, v102
v_add_f32 v120, v120, v124
mm a[204:207],v[8:11],v[192:195],a[204:207]
rt v[56:57],v243 offset:33920
rt v[58:59],v243 offset:34944
mm a[88:91],v[16:19],v[192:195],a[88:91]
v_fmac_f32 v123, v103, v103
v_lshlrev_b32 v104, 16, v80
mm a[88:91],v[20:23],v[196:199],a[88:91]
v_and_b32 v105, 0xffff0000, v80
v_sub_f32 v104, v104, v70
mm a[24:27],v[20:23],v[204:207],a[24:27]
v_sub_f32 v105, v105, v70
v_add_f32 v124, v104, v105
mm a[24:27],v[16:19],v[200:203],a[24:27]
rt v[60:61],v243 offset:50816
rt v[62:63],v243 offset:51840
mm a[240:243],v[16:19],v[208:211],a[240:243]
v_fmac_f32 v122, v104, v104
v_add_f32 v120, v120, v124
mm a[240:243],v[20:23],v[212:215],a[240:243]
v_fmac_f32 v123, v105, v105
v_lshlrev_b32 v106, 16, v81
mm a[32:35],v[20:23],v[220:223],a[32:35]
v_and_b32 v107, 0xffff0000, v81
v_sub_f32 v106, v106, v70
mm a[32:35],v[16:19],v[216:219],a[32:35]
v_sub_f32 v107, v107, v70
v_add_f32 v124, v106, v107
mm a[36:39],v[24:27],v[216:219],a[36:39]
v_fmac_f32 v122, v106, v106
v_add_f32 v120, v120, v124
mm a[36:39],v[28:31],v[220:223],a[36:39]
v_fmac_f32 v123, v107, v107
v_lshlrev_b32 v108, 16, v82
mm a[244:247],v[28:31],v[212:215],a[244:247]
v_and_b32 v109, 0xffff0000, v82
v_sub_f32 v108, v108, v70
mm a[244:247],v[24:27],v[208:211],a[244:247]
v_sub_f32 v109, v109, v70
v_add_f32 v124, v108, v109
mm a[28:31],v[24:27],v[200:203],a[28:31]
v_fmac_f32 v122, v108, v108
v_add_f32 v120, v120, v124
mm a[28:31],v[28:31],v[204:207],a[28:31]
v_fmac_f32 v123, v109, v109
v_lshlrev_b32 v110, 16, v83
mm a[92:95],v[28:31],v[196:199],a[92:95]
wt vmcnt(9) lgkmcnt(0)
s_barrier
mm a[92:95],v[24:27],v[192:195],a[92:95]
v_and_b32 v111, 0xffff0000, v83
v_sub_f32 v110, v110, v70
mm a[120:123],v[160:163],v[32:35],a[120:123]
v_sub_f32 v111, v111, v70
v_add_f32 v124, v110, v111
mm a[120:123],v[164:167],v[36:39],a[120:123]
v_fmac_f32 v122, v110, v110
v_add_f32 v120, v120, v124
mm a[136:139],v[164:167],v[44:47],a[136:139]
rt v[192:193],v240 offset:0
rt v[194:195],v240 offset:1024
mm a[136:139],v[160:163],v[40:43],a[136:139]
rt v[196:197],v240 offset:16896
rt v[198:199],v240 offset:17920
mm a[224:227],v[160:163],v[48:51],a[224:227]
v_fmac_f32 v123, v111, v111
v_lshlrev_b32 v112, 16, v84
mm a[224:227],v[164:167],v[52:55],a[224:227]
v_and_b32 v113, 0xffff0000, v84
v_sub_f32 v112, v112, v70
mm a[144:147],v[164:167],v[60:63],a[144:147]
v_sub_f32 v113, v113, v70
v_add_f32 v124, v112, v113
mm a[144:147],v[160:163],v[56:59],a[144:147]
rd v[160:163],v236 offset:24576
rd v[164:167],v237 offset:24576
mm a[148:151],v[168:171],v[56:59],a[148:151]
v_fmac_f32 v122, v112, v112
v_add_f32 v120, v120, v124
mm a[148:151],v[172:175],v[60:63],a[148:151]
v_fmac_f32 v123, v113, v113
v_lshlrev_b32 v114, 16, v85
mm a[228:231],v[172:175],v[52:55],a[228:231]
v_and_b32 v115, 0xffff0000, v85
v_sub_f32 v114, v114, v70
mm a[228:231],v[168:171],v[48:51],a[228:231]
rt v[200:201],v241 offset:0
rt v[202:203],v241 offset:1024
mm a[140:143],v[168:171],v[40:43],a[140:143]
)ASM" R"ASM(v_sub_f32 v115, v115, v70
v_add_f32 v124, v114, v115
mm a[140:143],v[172:175],v[44:47],a[140:143]
v_fmac_f32 v122, v114, v114
v_add_f32 v120, v120, v124
mm a[124:127],v[172:175],v[36:39],a[124:127]
v_fmac_f32 v123, v115, v115
v_lshlrev_b32 v116, 16, v86
mm a[124:127],v[168:171],v[32:35],a[124:127]
rd v[168:171],v236 offset:26624
rd v[172:175],v237 offset:26624
mm a[216:219],v[176:179],v[32:35],a[216:219]
v_and_b32 v117, 0xffff0000, v86
v_sub_f32 v116, v116, v70
mm a[216:219],v[180:183],v[36:39],a[216:219]
v_sub_f32 v117, v117, v70
v_add_f32 v124, v116, v117
mm a[168:171],v[180:183],v[44:47],a[168:171]
v_fmac_f32 v122, v116, v116
v_add_f32 v120, v120, v124
mm a[168:171],v[176:179],v[40:43],a[168:171]
rt v[204:205],v241 offset:16896
rt v[206:207],v241 offset:17920
mm a[192:195],v[176:179],v[48:51],a[192:195]
v_fmac_f32 v123, v117, v117
v_lshlrev_b32 v118, 16, v87
mm a[192:195],v[180:183],v[52:55],a[192:195]
v_and_b32 v119, 0xffff0000, v87
v_sub_f32 v118, v118, v70
mm a[176:179],v[180:183],v[60:63],a[176:179]
v_sub_f32 v119, v119, v70
v_add_f32 v124, v118, v119
mm a[176:179],v[176:179],v[56:59],a[176:179]
rd v[176:179],v236 offset:16384
rd v[180:183],v237 offset:16384
mm a[180:183],v[184:187],v[56:59],a[180:183]
v_fmac_f32 v122, v118, v118
v_add_f32 v120, v120, v124
mm a[180:183],v[188:191],v[60:63],a[180:183]
v_fmac_f32 v123, v119, v119
v_add_f32 v121, v122, v123
mm a[196:199],v[188:191],v[52:55],a[196:199]
v_add_f32_dpp v120, v120, v120 row_ror:8 row_mask:0xf bank_mask:0xf
mm a[196:199],v[184:187],v[48:51],a[196:199]
rt v[208:209],v242 offset:0
rt v[210:211],v242 offset:1024
mm a[172:175],v[184:187],v[40:43],a[172:175]
s_add_u32 m0, s83, 33792
v_add_f32_dpp v121, v121, v121 row_ror:8 row_mask:0xf bank_mask:0xf
mm a[172:175],v[188:191],v[44:47],a[172:175]
ld v232,s[44:47],s80 offen lds
mm a[220:223],v[188:191],v[36:39],a[220:223]
v_mov_b32 v124, v120
v_mov_b32 v125, v121
mm a[220:223],v[184:187],v[32:35],a[220:223]
rd v[184:187],v236 offset:18432
rd v[188:191],v237 offset:18432
mm a[72:75],v[0:3],v[32:35],a[72:75]
s_add_u32 m0, s83, 34816
v_permlane16_swap_b32 v124, v120
mm a[72:75],v[4:7],v[36:39],a[72:75]
ld v233,s[44:47],s80 offen lds
mm a[8:11],v[4:7],v[44:47],a[8:11]
v_permlane16_swap_b32 v125, v121
mm a[8:11],v[0:3],v[40:43],a[8:11]
rt v[212:213],v242 offset:16896
rt v[214:215],v242 offset:17920
mm a[80:83],v[0:3],v[48:51],a[80:83]
s_add_u32 m0, s83, 35968
v_add_f32 v120, v120, v124
mm a[80:83],v[4:7],v[52:55],a[80:83]
ld v234,s[44:47],s80 offen lds
mm a[16:19],v[4:7],v[60:63],a[16:19]
v_add_f32 v121, v121, v125
v_mov_b32 v124, v120
mm a[16:19],v[0:3],v[56:59],a[16:19]
rd v[0:3],v236 offset:8192
rd v[4:7],v237 offset:8192
mm a[20:23],v[8:11],v[56:59],a[20:23]
s_add_u32 m0, s83, 36992
v_mov_b32 v125, v121
mm a[20:23],v[12:15],v[60:63],a[20:23]
ld v235,s[44:47],s80 offen lds
mm a[84:87],v[12:15],v[52:55],a[84:87]
s_mov_b32 s80, 3932160
v_permlane32_swap_b32 v124, v120
mm a[84:87],v[8:11],v[48:51],a[84:87]
rt v[216:217],v243 offset:0
rt v[218:219],v243 offset:1024
mm a[12:15],v[8:11],v[40:43],a[12:15]
s_add_u32 m0, s83, 50688
v_permlane32_swap_b32 v125, v121
mm a[12:15],v[12:15],v[44:47],a[12:15]
ld v232,s[44:47],s80 offen lds
mm a[76:79],v[12:15],v[36:39],a[76:79]
v_add_f32 v120, v120, v124
v_add_f32 v121, v121, v125
mm a[76:79],v[8:11],v[32:35],a[76:79]
rd v[8:11],v236 offset:10240
rd v[12:15],v237 offset:10240
)ASM" R"ASM(mm a[248:251],v[16:19],v[32:35],a[248:251]
s_add_u32 m0, s83, 51712
ds_write_b64 v127, v[120:121]
mm a[248:251],v[20:23],v[36:39],a[248:251]
ld v233,s[44:47],s80 offen lds
mm a[40:43],v[20:23],v[44:47],a[40:43]
s_nop 0
v_readfirstlane_b32 s92, v252
mm a[40:43],v[16:19],v[40:43],a[40:43]
rt v[220:221],v243 offset:16896
rt v[222:223],v243 offset:17920
mm a[48:51],v[16:19],v[48:51],a[48:51]
s_add_u32 m0, s83, 52864
v_readfirstlane_b32 s93, v253
mm a[48:51],v[20:23],v[52:55],a[48:51]
ld v234,s[44:47],s80 offen lds
mm a[56:59],v[20:23],v[60:63],a[56:59]
s_add_u32 s92, s92, 1
s_addc_u32 s93, s93, 0
mm a[56:59],v[16:19],v[56:59],a[56:59]
rd v[16:19],v236 offset:0
rd v[20:23],v237 offset:0
mm a[60:63],v[24:27],v[56:59],a[60:63]
s_add_u32 m0, s83, 53888
v_mov_b32 v252, s92
mm a[60:63],v[28:31],v[60:63],a[60:63]
ld v235,s[44:47],s80 offen lds
mm a[52:55],v[28:31],v[52:55],a[52:55]
s_mov_b32 s80, 0
v_mov_b32 v253, s93
mm a[52:55],v[24:27],v[48:51],a[52:55]
v_mov_b32 v128, 0
v_mov_b32 v129, 0
mm a[44:47],v[24:27],v[40:43],a[44:47]
v_mov_b32 v130, 0
v_mov_b32 v131, 0
mm a[44:47],v[28:31],v[44:47],a[44:47]
st v[128:131],v255,s[52:55],s95 offen sc1
mm a[252:255],v[28:31],v[36:39],a[252:255]
wt lgkmcnt(2)
mm a[252:255],v[24:27],v[32:35],a[252:255]
rd v[24:27],v236 offset:2048
rd v[28:31],v237 offset:2048
mm a[96:99],v[160:163],v[192:195],a[96:99]
mm a[96:99],v[164:167],v[196:199],a[96:99]
mm a[104:107],v[164:167],v[204:207],a[104:107]
mm a[104:107],v[160:163],v[200:203],a[104:107]
rt v[32:33],v240 offset:128
rt v[34:35],v240 offset:1152
mm a[112:115],v[160:163],v[208:211],a[112:115]
mm a[112:115],v[164:167],v[212:215],a[112:115]
mm a[128:131],v[164:167],v[220:223],a[128:131]
wt lgkmcnt(2)
s_barrier
mm a[128:131],v[160:163],v[216:219],a[128:131]
rd v[72:75],v126
rd v[76:79],v126 offset:16
mm a[132:135],v[168:171],v[216:219],a[132:135]
s_xor_b32 m0, s82, 0
mm a[132:135],v[172:175],v[220:223],a[132:135]
ld v224,s[40:43],s79 offen lds
mm a[116:119],v[172:175],v[212:215],a[116:119]
mm a[116:119],v[168:171],v[208:211],a[116:119]
rt v[36:37],v240 offset:17024
rt v[38:39],v240 offset:18048
mm a[108:111],v[168:171],v[200:203],a[108:111]
s_xor_b32 m0, s82, 1024
mm a[108:111],v[172:175],v[204:207],a[108:111]
ld v225,s[40:43],s79 offen lds
mm a[100:103],v[172:175],v[196:199],a[100:103]
wt lgkmcnt(2)
v_add_f32 v72, v72, v74
mm a[100:103],v[168:171],v[192:195],a[100:103]
v_add_f32 v73, v73, v75
v_add_f32 v76, v76, v78
mm a[232:235],v[176:179],v[192:195],a[232:235]
s_xor_b32 m0, s82, 2048
v_add_f32 v77, v77, v79
mm a[232:235],v[180:183],v[196:199],a[232:235]
ld v226,s[40:43],s79 offen lds
mm a[152:155],v[180:183],v[204:207],a[152:155]
v_add_f32 v72, v72, v76
v_add_f32 v73, v73, v77
mm a[152:155],v[176:179],v[200:203],a[152:155]
rt v[40:41],v241 offset:128
rt v[42:43],v241 offset:1152
mm a[208:211],v[176:179],v[208:211],a[208:211]
s_xor_b32 m0, s82, 3072
v_mul_f32 v72, 0x3a800000, v72
mm a[208:211],v[180:183],v[212:215],a[208:211]
ld v227,s[40:43],s79 offen lds
mm a[160:163],v[180:183],v[220:223],a[160:163]
v_mul_f32 v73, 0x3a800000, v73
v_add_f32 v124, v70, v72
mm a[160:163],v[176:179],v[216:219],a[160:163]
v_fma_f32 v125, -v72, v72, v73
cv v124,v124,v124
mm a[164:167],v[184:187],v[216:219],a[164:167]
s_xor_b32 m0, s82, 4096
v_lshlrev_b32 v124, 16, v124
)ASM" R"ASM(mm a[164:167],v[188:191],v[220:223],a[164:167]
ld v228,s[40:43],s79 offen lds
mm a[212:215],v[188:191],v[212:215],a[212:215]
v_max_f32 v125, 0, v125
cv v125,v125,v125
mm a[212:215],v[184:187],v[208:211],a[212:215]
rt v[44:45],v241 offset:17024
rt v[46:47],v241 offset:18048
mm a[156:159],v[184:187],v[200:203],a[156:159]
s_xor_b32 m0, s82, 5120
v_lshlrev_b32 v125, 16, v125
mm a[156:159],v[188:191],v[204:207],a[156:159]
ld v229,s[40:43],s79 offen lds
mm a[236:239],v[188:191],v[196:199],a[236:239]
v_add_f32 v125, 0x3727c5ac, v125
cv v125,v125,v125
mm a[236:239],v[184:187],v[192:195],a[236:239]
v_lshlrev_b32 v125, 16, v125
mm a[200:203],v[0:3],v[192:195],a[200:203]
s_xor_b32 m0, s82, 6144
mm a[200:203],v[4:7],v[196:199],a[200:203]
ld v230,s[40:43],s79 offen lds
mm a[184:187],v[4:7],v[204:207],a[184:187]
v_sqrt_f32 v125, v125
mm a[184:187],v[0:3],v[200:203],a[184:187]
rt v[48:49],v242 offset:128
rt v[50:51],v242 offset:1152
mm a[64:67],v[0:3],v[208:211],a[64:67]
s_xor_b32 m0, s82, 7168
v_sub_f32 v124, v70, v124
mm a[64:67],v[4:7],v[212:215],a[64:67]
ld v231,s[40:43],s79 offen lds
mm a[0:3],v[4:7],v[220:223],a[0:3]
s_mov_b32 s79, 128
cv v125,v125,v125
mm a[0:3],v[0:3],v[216:219],a[0:3]
v_lshlrev_b32 v125, 16, v125
mm a[4:7],v[8:11],v[216:219],a[4:7]
v_rcp_f32 v125, v125
mm a[4:7],v[12:15],v[220:223],a[4:7]
v_mul_f32 v125, 0x3e0293ee, v125
v_mul_f32 v124, v124, v125
mm a[68:71],v[12:15],v[212:215],a[68:71]
v_fma_f32 v88, v88, v125, v124
v_fma_f32 v89, v89, v125, v124
mm a[68:71],v[8:11],v[208:211],a[68:71]
rt v[52:53],v242 offset:17024
rt v[54:55],v242 offset:18048
mm a[188:191],v[8:11],v[200:203],a[188:191]
cv v72,v88,v89
v_fma_f32 v90, v90, v125, v124
mm a[188:191],v[12:15],v[204:207],a[188:191]
v_fma_f32 v91, v91, v125, v124
cv v73,v90,v91
mm a[204:207],v[12:15],v[196:199],a[204:207]
v_fma_f32 v92, v92, v125, v124
v_fma_f32 v93, v93, v125, v124
mm a[204:207],v[8:11],v[192:195],a[204:207]
rt v[56:57],v243 offset:128
rt v[58:59],v243 offset:1152
mm a[88:91],v[16:19],v[192:195],a[88:91]
cv v74,v92,v93
v_fma_f32 v94, v94, v125, v124
mm a[88:91],v[20:23],v[196:199],a[88:91]
v_fma_f32 v95, v95, v125, v124
cv v75,v94,v95
mm a[24:27],v[20:23],v[204:207],a[24:27]
st v[72:75],v71,s[56:59],0 offen offset:0 sc1
mm a[24:27],v[16:19],v[200:203],a[24:27]
rt v[60:61],v243 offset:17024
rt v[62:63],v243 offset:18048
mm a[240:243],v[16:19],v[208:211],a[240:243]
v_fma_f32 v96, v96, v125, v124
v_fma_f32 v97, v97, v125, v124
mm a[240:243],v[20:23],v[212:215],a[240:243]
cv v76,v96,v97
v_fma_f32 v98, v98, v125, v124
mm a[32:35],v[20:23],v[220:223],a[32:35]
v_fma_f32 v99, v99, v125, v124
cv v77,v98,v99
mm a[32:35],v[16:19],v[216:219],a[32:35]
v_fma_f32 v100, v100, v125, v124
v_fma_f32 v101, v101, v125, v124
mm a[36:39],v[24:27],v[216:219],a[36:39]
cv v78,v100,v101
v_fma_f32 v102, v102, v125, v124
mm a[36:39],v[28:31],v[220:223],a[36:39]
v_fma_f32 v103, v103, v125, v124
cv v79,v102,v103
mm a[244:247],v[28:31],v[212:215],a[244:247]
st v[76:79],v71,s[56:59],0 offen offset:2048 sc1
mm a[244:247],v[24:27],v[208:211],a[244:247]
v_fma_f32 v104, v104, v125, v124
v_fma_f32 v105, v105, v125, v124
mm a[28:31],v[24:27],v[200:203],a[28:31]
cv v80,v104,v105
v_fma_f32 v106, v106, v125, v124
mm a[28:31],v[28:31],v[204:207],a[28:31]
v_fma_f32 v107, v107, v125, v124
cv v81,v106,v107
)ASM" R"ASM(mm a[92:95],v[28:31],v[196:199],a[92:95]
wt vmcnt(11) lgkmcnt(0)
s_barrier
mm a[92:95],v[24:27],v[192:195],a[92:95]
v_fma_f32 v108, v108, v125, v124
v_fma_f32 v109, v109, v125, v124
mm a[120:123],v[160:163],v[32:35],a[120:123]
cv v82,v108,v109
v_fma_f32 v110, v110, v125, v124
mm a[120:123],v[164:167],v[36:39],a[120:123]
v_fma_f32 v111, v111, v125, v124
cv v83,v110,v111
mm a[136:139],v[164:167],v[44:47],a[136:139]
rt v[192:193],v240 offset:33792
rt v[194:195],v240 offset:34816
mm a[136:139],v[160:163],v[40:43],a[136:139]
rt v[196:197],v240 offset:50688
rt v[198:199],v240 offset:51712
mm a[224:227],v[160:163],v[48:51],a[224:227]
st v[80:83],v71,s[56:59],s88 offen offset:0 sc1
mm a[224:227],v[164:167],v[52:55],a[224:227]
v_fma_f32 v112, v112, v125, v124
v_fma_f32 v113, v113, v125, v124
mm a[144:147],v[164:167],v[60:63],a[144:147]
cv v84,v112,v113
v_fma_f32 v114, v114, v125, v124
mm a[144:147],v[160:163],v[56:59],a[144:147]
rd v[160:163],v236 offset:57344
rd v[164:167],v237 offset:57344
mm a[148:151],v[168:171],v[56:59],a[148:151]
v_fma_f32 v115, v115, v125, v124
cv v85,v114,v115
mm a[148:151],v[172:175],v[60:63],a[148:151]
v_fma_f32 v116, v116, v125, v124
v_fma_f32 v117, v117, v125, v124
mm a[228:231],v[172:175],v[52:55],a[228:231]
cv v86,v116,v117
v_fma_f32 v118, v118, v125, v124
mm a[228:231],v[168:171],v[48:51],a[228:231]
rt v[200:201],v241 offset:33792
rt v[202:203],v241 offset:34816
mm a[140:143],v[168:171],v[40:43],a[140:143]
v_fma_f32 v119, v119, v125, v124
cv v87,v118,v119
mm a[140:143],v[172:175],v[44:47],a[140:143]
st v[84:87],v71,s[56:59],s88 offen offset:2048 sc1
mm a[124:127],v[172:175],v[36:39],a[124:127]
mm a[124:127],v[168:171],v[32:35],a[124:127]
rd v[168:171],v236 offset:59392
rd v[172:175],v237 offset:59392
mm a[216:219],v[176:179],v[32:35],a[216:219]
mm a[216:219],v[180:183],v[36:39],a[216:219]
mm a[168:171],v[180:183],v[44:47],a[168:171]
mm a[168:171],v[176:179],v[40:43],a[168:171]
rt v[204:205],v241 offset:50688
rt v[206:207],v241 offset:51712
mm a[192:195],v[176:179],v[48:51],a[192:195]
mm a[192:195],v[180:183],v[52:55],a[192:195]
mm a[176:179],v[180:183],v[60:63],a[176:179]
mm a[176:179],v[176:179],v[56:59],a[176:179]
rd v[176:179],v236 offset:49152
rd v[180:183],v237 offset:49152
mm a[180:183],v[184:187],v[56:59],a[180:183]
mm a[180:183],v[188:191],v[60:63],a[180:183]
mm a[196:199],v[188:191],v[52:55],a[196:199]
mm a[196:199],v[184:187],v[48:51],a[196:199]
rt v[208:209],v242 offset:33792
rt v[210:211],v242 offset:34816
mm a[172:175],v[184:187],v[40:43],a[172:175]
s_add_u32 m0, s83, 0
mm a[172:175],v[188:191],v[44:47],a[172:175]
ld v232,s[44:47],s80 offen lds
mm a[220:223],v[188:191],v[36:39],a[220:223]
mm a[220:223],v[184:187],v[32:35],a[220:223]
rd v[184:187],v236 offset:51200
rd v[188:191],v237 offset:51200
mm a[72:75],v[0:3],v[32:35],a[72:75]
s_add_u32 m0, s83, 1024
mm a[72:75],v[4:7],v[36:39],a[72:75]
ld v233,s[44:47],s80 offen lds
mm a[8:11],v[4:7],v[44:47],a[8:11]
mm a[8:11],v[0:3],v[40:43],a[8:11]
rt v[212:213],v242 offset:50688
rt v[214:215],v242 offset:51712
mm a[80:83],v[0:3],v[48:51],a[80:83]
s_add_u32 m0, s83, 2176
mm a[80:83],v[4:7],v[52:55],a[80:83]
ld v234,s[44:47],s80 offen lds
mm a[16:19],v[4:7],v[60:63],a[16:19]
mm a[16:19],v[0:3],v[56:59],a[16:19]
rd v[0:3],v236 offset:40960
rd v[4:7],v237 offset:40960
mm a[20:23],v[8:11],v[56:59],a[20:23]
s_add_u32 m0, s83, 3200
mm a[20:23],v[12:15],v[60:63],a[20:23]
ld v235,s[44:47],s80 offen lds
mm a[84:87],v[12:15],v[52:55],a[84:87]
s_mov_b32 s80, 262144
mm a[84:87],v[8:11],v[48:51],a[84:87]
rt v[216:217],v243 offset:33792
)ASM" R"ASM(rt v[218:219],v243 offset:34816
mm a[12:15],v[8:11],v[40:43],a[12:15]
s_add_u32 m0, s83, 16896
mm a[12:15],v[12:15],v[44:47],a[12:15]
ld v232,s[44:47],s80 offen lds
mm a[76:79],v[12:15],v[36:39],a[76:79]
mm a[76:79],v[8:11],v[32:35],a[76:79]
rd v[8:11],v236 offset:43008
rd v[12:15],v237 offset:43008
mm a[248:251],v[16:19],v[32:35],a[248:251]
s_add_u32 m0, s83, 17920
mm a[248:251],v[20:23],v[36:39],a[248:251]
ld v233,s[44:47],s80 offen lds
mm a[40:43],v[20:23],v[44:47],a[40:43]
mm a[40:43],v[16:19],v[40:43],a[40:43]
rt v[220:221],v243 offset:50688
rt v[222:223],v243 offset:51712
mm a[48:51],v[16:19],v[48:51],a[48:51]
s_add_u32 m0, s83, 19072
mm a[48:51],v[20:23],v[52:55],a[48:51]
ld v234,s[44:47],s80 offen lds
mm a[56:59],v[20:23],v[60:63],a[56:59]
mm a[56:59],v[16:19],v[56:59],a[56:59]
rd v[16:19],v236 offset:32768
rd v[20:23],v237 offset:32768
.La5_LW1_6:
s_cbranch_execz .La5_LW1_10
mm a[60:63],v[24:27],v[56:59],a[60:63]
s_add_u32 m0, s83, 20096
mm a[60:63],v[28:31],v[60:63],a[60:63]
ld v235,s[44:47],s80 offen lds
mm a[52:55],v[28:31],v[52:55],a[52:55]
s_mov_b32 s80, 524288
mm a[52:55],v[24:27],v[48:51],a[52:55]
mm a[44:47],v[24:27],v[40:43],a[44:47]
mm a[44:47],v[28:31],v[44:47],a[44:47]
mm a[252:255],v[28:31],v[36:39],a[252:255]
wt lgkmcnt(2)
mm a[252:255],v[24:27],v[32:35],a[252:255]
rd v[24:27],v236 offset:34816
rd v[28:31],v237 offset:34816
mm a[96:99],v[160:163],v[192:195],a[96:99]
mm a[96:99],v[164:167],v[196:199],a[96:99]
mm a[104:107],v[164:167],v[204:207],a[104:107]
mm a[104:107],v[160:163],v[200:203],a[104:107]
rt v[32:33],v240 offset:33920
rt v[34:35],v240 offset:34944
mm a[112:115],v[160:163],v[208:211],a[112:115]
mm a[112:115],v[164:167],v[212:215],a[112:115]
mm a[128:131],v[164:167],v[220:223],a[128:131]
wt lgkmcnt(2)
s_barrier
mm a[128:131],v[160:163],v[216:219],a[128:131]
mm a[132:135],v[168:171],v[216:219],a[132:135]
s_xor_b32 m0, s82, 32768
mm a[132:135],v[172:175],v[220:223],a[132:135]
ld v224,s[40:43],s79 offen lds
mm a[116:119],v[172:175],v[212:215],a[116:119]
mm a[116:119],v[168:171],v[208:211],a[116:119]
rt v[36:37],v240 offset:50816
rt v[38:39],v240 offset:51840
mm a[108:111],v[168:171],v[200:203],a[108:111]
s_xor_b32 m0, s82, 33792
mm a[108:111],v[172:175],v[204:207],a[108:111]
ld v225,s[40:43],s79 offen lds
mm a[100:103],v[172:175],v[196:199],a[100:103]
mm a[100:103],v[168:171],v[192:195],a[100:103]
mm a[232:235],v[176:179],v[192:195],a[232:235]
s_xor_b32 m0, s82, 34816
mm a[232:235],v[180:183],v[196:199],a[232:235]
ld v226,s[40:43],s79 offen lds
mm a[152:155],v[180:183],v[204:207],a[152:155]
mm a[152:155],v[176:179],v[200:203],a[152:155]
rt v[40:41],v241 offset:33920
rt v[42:43],v241 offset:34944
mm a[208:211],v[176:179],v[208:211],a[208:211]
s_xor_b32 m0, s82, 35840
mm a[208:211],v[180:183],v[212:215],a[208:211]
ld v227,s[40:43],s79 offen lds
mm a[160:163],v[180:183],v[220:223],a[160:163]
mm a[160:163],v[176:179],v[216:219],a[160:163]
mm a[164:167],v[184:187],v[216:219],a[164:167]
s_xor_b32 m0, s82, 36864
mm a[164:167],v[188:191],v[220:223],a[164:167]
ld v228,s[40:43],s79 offen lds
mm a[212:215],v[188:191],v[212:215],a[212:215]
mm a[212:215],v[184:187],v[208:211],a[212:215]
rt v[44:45],v241 offset:50816
rt v[46:47],v241 offset:51840
mm a[156:159],v[184:187],v[200:203],a[156:159]
s_xor_b32 m0, s82, 37888
mm a[156:159],v[188:191],v[204:207],a[156:159]
ld v229,s[40:43],s79 offen lds
mm a[236:239],v[188:191],v[196:199],a[236:239]
mm a[236:239],v[184:187],v[192:195],a[236:239]
mm a[200:203],v[0:3],v[192:195],a[200:203]
s_xor_b32 m0, s82, 38912
mm a[200:203],v[4:7],v[196:199],a[200:203]
ld v230,s[40:43],s79 offen lds
mm a[184:187],v[4:7],v[204:207],a[184:187]
)ASM" R"ASM(mm a[184:187],v[0:3],v[200:203],a[184:187]
rt v[48:49],v242 offset:33920
rt v[50:51],v242 offset:34944
mm a[64:67],v[0:3],v[208:211],a[64:67]
s_xor_b32 m0, s82, 39936
mm a[64:67],v[4:7],v[212:215],a[64:67]
ld v231,s[40:43],s79 offen lds
mm a[0:3],v[4:7],v[220:223],a[0:3]
s_mov_b32 s79, 256
mm a[0:3],v[0:3],v[216:219],a[0:3]
mm a[4:7],v[8:11],v[216:219],a[4:7]
mm a[4:7],v[12:15],v[220:223],a[4:7]
buffer_load_dword v254, v135, s[68:71], s96 offen
mm a[68:71],v[12:15],v[212:215],a[68:71]
s_add_u32 s96, s96, 1048576
mm a[68:71],v[8:11],v[208:211],a[68:71]
rt v[52:53],v242 offset:50816
rt v[54:55],v242 offset:51840
mm a[188:191],v[8:11],v[200:203],a[188:191]
mm a[188:191],v[12:15],v[204:207],a[188:191]
buffer_load_dword v254, v135, s[68:71], s96 offen
mm a[204:207],v[12:15],v[196:199],a[204:207]
s_add_u32 s96, s96, 1048576
mm a[204:207],v[8:11],v[192:195],a[204:207]
rt v[56:57],v243 offset:33920
rt v[58:59],v243 offset:34944
mm a[88:91],v[16:19],v[192:195],a[88:91]
mm a[88:91],v[20:23],v[196:199],a[88:91]
wt vmcnt(18)
buffer_store_dwordx2 v[252:253], off, s[64:67], s91 offset:1024 sc1
mm a[24:27],v[20:23],v[204:207],a[24:27]
s_mov_b32 s91, 0
s_lshl_b32 s92, s76, 19
mm a[24:27],v[16:19],v[200:203],a[24:27]
rt v[60:61],v243 offset:50816
rt v[62:63],v243 offset:51840
mm a[240:243],v[16:19],v[208:211],a[240:243]
s_add_u32 s92, s92, 88080384
mm a[240:243],v[20:23],v[212:215],a[240:243]
s_add_u32 s52, s26, s92
s_addc_u32 s53, s27, 0
mm a[32:35],v[20:23],v[220:223],a[32:35]
s_and_b32 s53, s53, 0xffff
s_mov_b32 s54, 524672
mm a[32:35],v[16:19],v[216:219],a[32:35]
s_mov_b32 s55, 0x20000
s_lshl_b32 s92, s3, 16
mm a[36:39],v[24:27],v[216:219],a[36:39]
s_sub_u32 s93, 458752, s92
v_add_u32 v244, s92, v136
mm a[36:39],v[28:31],v[220:223],a[36:39]
v_add_u32 v246, s93, v136
v_add_u32 v245, 0x8000, v244
mm a[244:247],v[28:31],v[212:215],a[244:247]
v_add_u32 v247, 0x8000, v246
s_lshl_b32 s94, s72, 19
mm a[244:247],v[24:27],v[208:211],a[244:247]
s_lshl_b32 s93, s76, 16
s_add_u32 s92, s94, s93
mm a[28:31],v[24:27],v[200:203],a[28:31]
s_lshl_b32 s93, s77, 13
s_add_u32 s92, s92, s93
mm a[28:31],v[28:31],v[204:207],a[28:31]
s_add_u32 s92, s92, 37748736
mm a[92:95],v[28:31],v[196:199],a[92:95]
wt vmcnt(11) lgkmcnt(0)
s_barrier
mm a[92:95],v[24:27],v[192:195],a[92:95]
s_add_u32 s48, s26, s92
s_addc_u32 s49, s27, 0
mm a[120:123],v[160:163],v[32:35],a[120:123]
s_and_b32 s49, s49, 0xffff
s_mov_b32 s50, 57344
mm a[120:123],v[164:167],v[36:39],a[120:123]
s_mov_b32 s51, 0x20000
s_lshl_b32 s93, s77, 24
mm a[136:139],v[164:167],v[44:47],a[136:139]
rt v[192:193],v240 offset:0
rt v[194:195],v240 offset:1024
mm a[136:139],v[160:163],v[40:43],a[136:139]
rt v[196:197],v240 offset:16896
rt v[198:199],v240 offset:17920
mm a[224:227],v[160:163],v[48:51],a[224:227]
s_add_u32 s92, s94, s93
s_add_u32 s92, s92, 20975616
mm a[224:227],v[164:167],v[52:55],a[224:227]
s_add_u32 s60, s26, s92
s_addc_u32 s61, s27, 0
mm a[144:147],v[164:167],v[60:63],a[144:147]
s_and_b32 s61, s61, 0xffff
s_mov_b32 s62, 520192
mm a[144:147],v[160:163],v[56:59],a[144:147]
rd v[160:163],v236 offset:24576
rd v[164:167],v237 offset:24576
mm a[148:151],v[168:171],v[56:59],a[148:151]
s_mov_b32 s63, 0x20000
s_lshl_b32 s92, s77, 14
mm a[148:151],v[172:175],v[60:63],a[148:151]
s_sub_u32 s95, 24576, s92
v_add_u32 v238, s95, v255
)ASM" R"ASM(mm a[228:231],v[172:175],v[52:55],a[228:231]
mm a[228:231],v[168:171],v[48:51],a[228:231]
rt v[200:201],v241 offset:0
rt v[202:203],v241 offset:1024
mm a[140:143],v[168:171],v[40:43],a[140:143]
mm a[140:143],v[172:175],v[44:47],a[140:143]
mm a[124:127],v[172:175],v[36:39],a[124:127]
mm a[124:127],v[168:171],v[32:35],a[124:127]
rd v[168:171],v236 offset:26624
rd v[172:175],v237 offset:26624
mm a[216:219],v[176:179],v[32:35],a[216:219]
mm a[216:219],v[180:183],v[36:39],a[216:219]
mm a[168:171],v[180:183],v[44:47],a[168:171]
mm a[168:171],v[176:179],v[40:43],a[168:171]
rt v[204:205],v241 offset:16896
rt v[206:207],v241 offset:17920
mm a[192:195],v[176:179],v[48:51],a[192:195]
mm a[192:195],v[180:183],v[52:55],a[192:195]
mm a[176:179],v[180:183],v[60:63],a[176:179]
mm a[176:179],v[176:179],v[56:59],a[176:179]
rd v[176:179],v236 offset:16384
rd v[180:183],v237 offset:16384
mm a[180:183],v[184:187],v[56:59],a[180:183]
mm a[180:183],v[188:191],v[60:63],a[180:183]
mm a[196:199],v[188:191],v[52:55],a[196:199]
mm a[196:199],v[184:187],v[48:51],a[196:199]
rt v[208:209],v242 offset:0
rt v[210:211],v242 offset:1024
mm a[172:175],v[184:187],v[40:43],a[172:175]
s_add_u32 m0, s83, 33792
mm a[172:175],v[188:191],v[44:47],a[172:175]
ld v232,s[44:47],s80 offen lds
mm a[220:223],v[188:191],v[36:39],a[220:223]
mm a[220:223],v[184:187],v[32:35],a[220:223]
rd v[184:187],v236 offset:18432
rd v[188:191],v237 offset:18432
mm a[72:75],v[0:3],v[32:35],a[72:75]
s_add_u32 m0, s83, 34816
mm a[72:75],v[4:7],v[36:39],a[72:75]
ld v233,s[44:47],s80 offen lds
mm a[8:11],v[4:7],v[44:47],a[8:11]
mm a[8:11],v[0:3],v[40:43],a[8:11]
rt v[212:213],v242 offset:16896
rt v[214:215],v242 offset:17920
mm a[80:83],v[0:3],v[48:51],a[80:83]
s_add_u32 m0, s83, 35968
mm a[80:83],v[4:7],v[52:55],a[80:83]
ld v234,s[44:47],s80 offen lds
mm a[16:19],v[4:7],v[60:63],a[16:19]
mm a[16:19],v[0:3],v[56:59],a[16:19]
rd v[0:3],v236 offset:8192
rd v[4:7],v237 offset:8192
mm a[20:23],v[8:11],v[56:59],a[20:23]
s_add_u32 m0, s83, 36992
mm a[20:23],v[12:15],v[60:63],a[20:23]
ld v235,s[44:47],s80 offen lds
mm a[84:87],v[12:15],v[52:55],a[84:87]
s_mov_b32 s80, 786432
mm a[84:87],v[8:11],v[48:51],a[84:87]
rt v[216:217],v243 offset:0
rt v[218:219],v243 offset:1024
mm a[12:15],v[8:11],v[40:43],a[12:15]
s_add_u32 m0, s83, 50688
mm a[12:15],v[12:15],v[44:47],a[12:15]
ld v232,s[44:47],s80 offen lds
mm a[76:79],v[12:15],v[36:39],a[76:79]
mm a[76:79],v[8:11],v[32:35],a[76:79]
rd v[8:11],v236 offset:10240
rd v[12:15],v237 offset:10240
mm a[248:251],v[16:19],v[32:35],a[248:251]
s_add_u32 m0, s83, 51712
mm a[248:251],v[20:23],v[36:39],a[248:251]
ld v233,s[44:47],s80 offen lds
mm a[40:43],v[20:23],v[44:47],a[40:43]
mm a[40:43],v[16:19],v[40:43],a[40:43]
rt v[220:221],v243 offset:16896
rt v[222:223],v243 offset:17920
mm a[48:51],v[16:19],v[48:51],a[48:51]
s_add_u32 m0, s83, 52864
mm a[48:51],v[20:23],v[52:55],a[48:51]
ld v234,s[44:47],s80 offen lds
mm a[56:59],v[20:23],v[60:63],a[56:59]
mm a[56:59],v[16:19],v[56:59],a[56:59]
rd v[16:19],v236 offset:0
rd v[20:23],v237 offset:0
mm a[60:63],v[24:27],v[56:59],a[60:63]
s_add_u32 m0, s83, 53888
mm a[60:63],v[28:31],v[60:63],a[60:63]
ld v235,s[44:47],s80 offen lds
mm a[52:55],v[28:31],v[52:55],a[52:55]
s_mov_b32 s80, 1048576
mm a[52:55],v[24:27],v[48:51],a[52:55]
mm a[44:47],v[24:27],v[40:43],a[44:47]
mm a[44:47],v[28:31],v[44:47],a[44:47]
mm a[252:255],v[28:31],v[36:39],a[252:255]
wt lgkmcnt(2)
mm a[252:255],v[24:27],v[32:35],a[252:255]
rd v[24:27],v236 offset:2048
rd v[28:31],v237 offset:2048
mm a[96:99],v[160:163],v[192:195],a[96:99]
)ASM" R"ASM(mm a[96:99],v[164:167],v[196:199],a[96:99]
mm a[104:107],v[164:167],v[204:207],a[104:107]
mm a[104:107],v[160:163],v[200:203],a[104:107]
rt v[32:33],v240 offset:128
rt v[34:35],v240 offset:1152
mm a[112:115],v[160:163],v[208:211],a[112:115]
mm a[112:115],v[164:167],v[212:215],a[112:115]
mm a[128:131],v[164:167],v[220:223],a[128:131]
wt lgkmcnt(2)
s_barrier
mm a[128:131],v[160:163],v[216:219],a[128:131]
mm a[132:135],v[168:171],v[216:219],a[132:135]
s_xor_b32 m0, s82, 0
mm a[132:135],v[172:175],v[220:223],a[132:135]
ld v224,s[40:43],s79 offen lds
mm a[116:119],v[172:175],v[212:215],a[116:119]
mm a[116:119],v[168:171],v[208:211],a[116:119]
rt v[36:37],v240 offset:17024
rt v[38:39],v240 offset:18048
mm a[108:111],v[168:171],v[200:203],a[108:111]
s_xor_b32 m0, s82, 1024
mm a[108:111],v[172:175],v[204:207],a[108:111]
ld v225,s[40:43],s79 offen lds
mm a[100:103],v[172:175],v[196:199],a[100:103]
mm a[100:103],v[168:171],v[192:195],a[100:103]
mm a[232:235],v[176:179],v[192:195],a[232:235]
s_xor_b32 m0, s82, 2048
mm a[232:235],v[180:183],v[196:199],a[232:235]
ld v226,s[40:43],s79 offen lds
mm a[152:155],v[180:183],v[204:207],a[152:155]
mm a[152:155],v[176:179],v[200:203],a[152:155]
rt v[40:41],v241 offset:128
rt v[42:43],v241 offset:1152
mm a[208:211],v[176:179],v[208:211],a[208:211]
s_xor_b32 m0, s82, 3072
mm a[208:211],v[180:183],v[212:215],a[208:211]
ld v227,s[40:43],s79 offen lds
mm a[160:163],v[180:183],v[220:223],a[160:163]
mm a[160:163],v[176:179],v[216:219],a[160:163]
mm a[164:167],v[184:187],v[216:219],a[164:167]
s_xor_b32 m0, s82, 4096
mm a[164:167],v[188:191],v[220:223],a[164:167]
ld v228,s[40:43],s79 offen lds
mm a[212:215],v[188:191],v[212:215],a[212:215]
mm a[212:215],v[184:187],v[208:211],a[212:215]
rt v[44:45],v241 offset:17024
rt v[46:47],v241 offset:18048
mm a[156:159],v[184:187],v[200:203],a[156:159]
s_xor_b32 m0, s82, 5120
mm a[156:159],v[188:191],v[204:207],a[156:159]
ld v229,s[40:43],s79 offen lds
mm a[236:239],v[188:191],v[196:199],a[236:239]
mm a[236:239],v[184:187],v[192:195],a[236:239]
mm a[200:203],v[0:3],v[192:195],a[200:203]
s_xor_b32 m0, s82, 6144
mm a[200:203],v[4:7],v[196:199],a[200:203]
ld v230,s[40:43],s79 offen lds
mm a[184:187],v[4:7],v[204:207],a[184:187]
mm a[184:187],v[0:3],v[200:203],a[184:187]
rt v[48:49],v242 offset:128
rt v[50:51],v242 offset:1152
mm a[64:67],v[0:3],v[208:211],a[64:67]
s_xor_b32 m0, s82, 7168
mm a[64:67],v[4:7],v[212:215],a[64:67]
ld v231,s[40:43],s79 offen lds
mm a[0:3],v[4:7],v[220:223],a[0:3]
s_mov_b32 s79, 384
mm a[0:3],v[0:3],v[216:219],a[0:3]
mm a[4:7],v[8:11],v[216:219],a[4:7]
mm a[4:7],v[12:15],v[220:223],a[4:7]
buffer_load_dword v254, v135, s[68:71], s96 offen
mm a[68:71],v[12:15],v[212:215],a[68:71]
s_add_u32 s96, s96, 1048576
mm a[68:71],v[8:11],v[208:211],a[68:71]
rt v[52:53],v242 offset:17024
rt v[54:55],v242 offset:18048
mm a[188:191],v[8:11],v[200:203],a[188:191]
mm a[188:191],v[12:15],v[204:207],a[188:191]
buffer_load_dword v254, v135, s[68:71], s96 offen
mm a[204:207],v[12:15],v[196:199],a[204:207]
s_add_u32 s96, s96, 1048576
mm a[204:207],v[8:11],v[192:195],a[204:207]
rt v[56:57],v243 offset:128
rt v[58:59],v243 offset:1152
mm a[88:91],v[16:19],v[192:195],a[88:91]
mm a[88:91],v[20:23],v[196:199],a[88:91]
mm a[24:27],v[20:23],v[204:207],a[24:27]
mm a[24:27],v[16:19],v[200:203],a[24:27]
rt v[60:61],v243 offset:17024
rt v[62:63],v243 offset:18048
mm a[240:243],v[16:19],v[208:211],a[240:243]
mm a[240:243],v[20:23],v[212:215],a[240:243]
mm a[32:35],v[20:23],v[220:223],a[32:35]
mm a[32:35],v[16:19],v[216:219],a[32:35]
mm a[36:39],v[24:27],v[216:219],a[36:39]
mm a[36:39],v[28:31],v[220:223],a[36:39]
mm a[244:247],v[28:31],v[212:215],a[244:247]
mm a[244:247],v[24:27],v[208:211],a[244:247]
mm a[28:31],v[24:27],v[200:203],a[28:31]
mm a[28:31],v[28:31],v[204:207],a[28:31]
)ASM" R"ASM(mm a[92:95],v[28:31],v[196:199],a[92:95]
wt vmcnt(10) lgkmcnt(0)
s_barrier
mm a[92:95],v[24:27],v[192:195],a[92:95]
mm a[120:123],v[160:163],v[32:35],a[120:123]
mm a[120:123],v[164:167],v[36:39],a[120:123]
mm a[136:139],v[164:167],v[44:47],a[136:139]
rt v[192:193],v240 offset:33792
rt v[194:195],v240 offset:34816
mm a[136:139],v[160:163],v[40:43],a[136:139]
rt v[196:197],v240 offset:50688
rt v[198:199],v240 offset:51712
mm a[224:227],v[160:163],v[48:51],a[224:227]
mm a[224:227],v[164:167],v[52:55],a[224:227]
mm a[144:147],v[164:167],v[60:63],a[144:147]
mm a[144:147],v[160:163],v[56:59],a[144:147]
rd v[160:163],v236 offset:57344
rd v[164:167],v237 offset:57344
mm a[148:151],v[168:171],v[56:59],a[148:151]
mm a[148:151],v[172:175],v[60:63],a[148:151]
mm a[228:231],v[172:175],v[52:55],a[228:231]
mm a[228:231],v[168:171],v[48:51],a[228:231]
rt v[200:201],v241 offset:33792
rt v[202:203],v241 offset:34816
mm a[140:143],v[168:171],v[40:43],a[140:143]
mm a[140:143],v[172:175],v[44:47],a[140:143]
mm a[124:127],v[172:175],v[36:39],a[124:127]
mm a[124:127],v[168:171],v[32:35],a[124:127]
rd v[168:171],v236 offset:59392
rd v[172:175],v237 offset:59392
mm a[216:219],v[176:179],v[32:35],a[216:219]
mm a[216:219],v[180:183],v[36:39],a[216:219]
mm a[168:171],v[180:183],v[44:47],a[168:171]
mm a[168:171],v[176:179],v[40:43],a[168:171]
rt v[204:205],v241 offset:50688
rt v[206:207],v241 offset:51712
mm a[192:195],v[176:179],v[48:51],a[192:195]
mm a[192:195],v[180:183],v[52:55],a[192:195]
mm a[176:179],v[180:183],v[60:63],a[176:179]
mm a[176:179],v[176:179],v[56:59],a[176:179]
rd v[176:179],v236 offset:49152
rd v[180:183],v237 offset:49152
mm a[180:183],v[184:187],v[56:59],a[180:183]
mm a[180:183],v[188:191],v[60:63],a[180:183]
mm a[196:199],v[188:191],v[52:55],a[196:199]
mm a[196:199],v[184:187],v[48:51],a[196:199]
rt v[208:209],v242 offset:33792
rt v[210:211],v242 offset:34816
mm a[172:175],v[184:187],v[40:43],a[172:175]
s_add_u32 m0, s83, 0
mm a[172:175],v[188:191],v[44:47],a[172:175]
ld v232,s[44:47],s80 offen lds
mm a[220:223],v[188:191],v[36:39],a[220:223]
mm a[220:223],v[184:187],v[32:35],a[220:223]
rd v[184:187],v236 offset:51200
rd v[188:191],v237 offset:51200
mm a[72:75],v[0:3],v[32:35],a[72:75]
s_add_u32 m0, s83, 1024
mm a[72:75],v[4:7],v[36:39],a[72:75]
ld v233,s[44:47],s80 offen lds
mm a[8:11],v[4:7],v[44:47],a[8:11]
mm a[8:11],v[0:3],v[40:43],a[8:11]
rt v[212:213],v242 offset:50688
rt v[214:215],v242 offset:51712
mm a[80:83],v[0:3],v[48:51],a[80:83]
s_add_u32 m0, s83, 2176
mm a[80:83],v[4:7],v[52:55],a[80:83]
ld v234,s[44:47],s80 offen lds
mm a[16:19],v[4:7],v[60:63],a[16:19]
mm a[16:19],v[0:3],v[56:59],a[16:19]
rd v[0:3],v236 offset:40960
rd v[4:7],v237 offset:40960
mm a[20:23],v[8:11],v[56:59],a[20:23]
s_add_u32 m0, s83, 3200
mm a[20:23],v[12:15],v[60:63],a[20:23]
ld v235,s[44:47],s80 offen lds
mm a[84:87],v[12:15],v[52:55],a[84:87]
s_mov_b32 s80, 1310720
mm a[84:87],v[8:11],v[48:51],a[84:87]
rt v[216:217],v243 offset:33792
rt v[218:219],v243 offset:34816
mm a[12:15],v[8:11],v[40:43],a[12:15]
s_add_u32 m0, s83, 16896
mm a[12:15],v[12:15],v[44:47],a[12:15]
ld v232,s[44:47],s80 offen lds
mm a[76:79],v[12:15],v[36:39],a[76:79]
mm a[76:79],v[8:11],v[32:35],a[76:79]
rd v[8:11],v236 offset:43008
rd v[12:15],v237 offset:43008
mm a[248:251],v[16:19],v[32:35],a[248:251]
s_add_u32 m0, s83, 17920
mm a[248:251],v[20:23],v[36:39],a[248:251]
ld v233,s[44:47],s80 offen lds
mm a[40:43],v[20:23],v[44:47],a[40:43]
mm a[40:43],v[16:19],v[40:43],a[40:43]
rt v[220:221],v243 offset:50688
rt v[222:223],v243 offset:51712
mm a[48:51],v[16:19],v[48:51],a[48:51]
s_add_u32 m0, s83, 19072
mm a[48:51],v[20:23],v[52:55],a[48:51]
)ASM" R"ASM(ld v234,s[44:47],s80 offen lds
mm a[56:59],v[20:23],v[60:63],a[56:59]
mm a[56:59],v[16:19],v[56:59],a[56:59]
rd v[16:19],v236 offset:32768
rd v[20:23],v237 offset:32768
mm a[60:63],v[24:27],v[56:59],a[60:63]
s_add_u32 m0, s83, 20096
mm a[60:63],v[28:31],v[60:63],a[60:63]
ld v235,s[44:47],s80 offen lds
mm a[52:55],v[28:31],v[52:55],a[52:55]
s_mov_b32 s80, 1572864
mm a[52:55],v[24:27],v[48:51],a[52:55]
mm a[44:47],v[24:27],v[40:43],a[44:47]
mm a[44:47],v[28:31],v[44:47],a[44:47]
mm a[252:255],v[28:31],v[36:39],a[252:255]
wt lgkmcnt(2)
mm a[252:255],v[24:27],v[32:35],a[252:255]
rd v[24:27],v236 offset:34816
rd v[28:31],v237 offset:34816
mm a[96:99],v[160:163],v[192:195],a[96:99]
mm a[96:99],v[164:167],v[196:199],a[96:99]
mm a[104:107],v[164:167],v[204:207],a[104:107]
mm a[104:107],v[160:163],v[200:203],a[104:107]
rt v[32:33],v240 offset:33920
rt v[34:35],v240 offset:34944
mm a[112:115],v[160:163],v[208:211],a[112:115]
mm a[112:115],v[164:167],v[212:215],a[112:115]
mm a[128:131],v[164:167],v[220:223],a[128:131]
wt lgkmcnt(2)
s_barrier
mm a[128:131],v[160:163],v[216:219],a[128:131]
mm a[132:135],v[168:171],v[216:219],a[132:135]
s_xor_b32 m0, s82, 32768
mm a[132:135],v[172:175],v[220:223],a[132:135]
ld v224,s[40:43],s79 offen lds
mm a[116:119],v[172:175],v[212:215],a[116:119]
mm a[116:119],v[168:171],v[208:211],a[116:119]
rt v[36:37],v240 offset:50816
rt v[38:39],v240 offset:51840
mm a[108:111],v[168:171],v[200:203],a[108:111]
s_xor_b32 m0, s82, 33792
mm a[108:111],v[172:175],v[204:207],a[108:111]
ld v225,s[40:43],s79 offen lds
mm a[100:103],v[172:175],v[196:199],a[100:103]
mm a[100:103],v[168:171],v[192:195],a[100:103]
mm a[232:235],v[176:179],v[192:195],a[232:235]
s_xor_b32 m0, s82, 34816
mm a[232:235],v[180:183],v[196:199],a[232:235]
ld v226,s[40:43],s79 offen lds
mm a[152:155],v[180:183],v[204:207],a[152:155]
mm a[152:155],v[176:179],v[200:203],a[152:155]
rt v[40:41],v241 offset:33920
rt v[42:43],v241 offset:34944
mm a[208:211],v[176:179],v[208:211],a[208:211]
s_xor_b32 m0, s82, 35840
mm a[208:211],v[180:183],v[212:215],a[208:211]
.La5_LW1_7:
s_cbranch_execz .La5_LW1_11
ld v227,s[40:43],s79 offen lds
mm a[160:163],v[180:183],v[220:223],a[160:163]
mm a[160:163],v[176:179],v[216:219],a[160:163]
mm a[164:167],v[184:187],v[216:219],a[164:167]
s_xor_b32 m0, s82, 36864
mm a[164:167],v[188:191],v[220:223],a[164:167]
ld v228,s[40:43],s79 offen lds
mm a[212:215],v[188:191],v[212:215],a[212:215]
mm a[212:215],v[184:187],v[208:211],a[212:215]
rt v[44:45],v241 offset:50816
rt v[46:47],v241 offset:51840
mm a[156:159],v[184:187],v[200:203],a[156:159]
s_xor_b32 m0, s82, 37888
mm a[156:159],v[188:191],v[204:207],a[156:159]
ld v229,s[40:43],s79 offen lds
mm a[236:239],v[188:191],v[196:199],a[236:239]
mm a[236:239],v[184:187],v[192:195],a[236:239]
mm a[200:203],v[0:3],v[192:195],a[200:203]
s_xor_b32 m0, s82, 38912
mm a[200:203],v[4:7],v[196:199],a[200:203]
ld v230,s[40:43],s79 offen lds
mm a[184:187],v[4:7],v[204:207],a[184:187]
mm a[184:187],v[0:3],v[200:203],a[184:187]
rt v[48:49],v242 offset:33920
rt v[50:51],v242 offset:34944
mm a[64:67],v[0:3],v[208:211],a[64:67]
s_xor_b32 m0, s82, 39936
mm a[64:67],v[4:7],v[212:215],a[64:67]
ld v231,s[40:43],s79 offen lds
mm a[0:3],v[4:7],v[220:223],a[0:3]
s_mov_b32 s79, 512
mm a[0:3],v[0:3],v[216:219],a[0:3]
mm a[4:7],v[8:11],v[216:219],a[4:7]
mm a[4:7],v[12:15],v[220:223],a[4:7]
buffer_load_dword v254, v135, s[68:71], s96 offen
mm a[68:71],v[12:15],v[212:215],a[68:71]
s_add_u32 s96, s96, 1048576
mm a[68:71],v[8:11],v[208:211],a[68:71]
rt v[52:53],v242 offset:50816
rt v[54:55],v242 offset:51840
mm a[188:191],v[8:11],v[200:203],a[188:191]
mm a[188:191],v[12:15],v[204:207],a[188:191]
)ASM" R"ASM(buffer_load_dword v254, v135, s[68:71], s96 offen
mm a[204:207],v[12:15],v[196:199],a[204:207]
s_add_u32 s96, s96, 1048576
mm a[204:207],v[8:11],v[192:195],a[204:207]
rt v[56:57],v243 offset:33920
rt v[58:59],v243 offset:34944
mm a[88:91],v[16:19],v[192:195],a[88:91]
mm a[88:91],v[20:23],v[196:199],a[88:91]
mm a[24:27],v[20:23],v[204:207],a[24:27]
mm a[24:27],v[16:19],v[200:203],a[24:27]
rt v[60:61],v243 offset:50816
rt v[62:63],v243 offset:51840
mm a[240:243],v[16:19],v[208:211],a[240:243]
mm a[240:243],v[20:23],v[212:215],a[240:243]
mm a[32:35],v[20:23],v[220:223],a[32:35]
mm a[32:35],v[16:19],v[216:219],a[32:35]
mm a[36:39],v[24:27],v[216:219],a[36:39]
mm a[36:39],v[28:31],v[220:223],a[36:39]
mm a[244:247],v[28:31],v[212:215],a[244:247]
mm a[244:247],v[24:27],v[208:211],a[244:247]
mm a[28:31],v[24:27],v[200:203],a[28:31]
mm a[28:31],v[28:31],v[204:207],a[28:31]
mm a[92:95],v[28:31],v[196:199],a[92:95]
wt vmcnt(10) lgkmcnt(0)
s_barrier
mm a[92:95],v[24:27],v[192:195],a[92:95]
mm a[120:123],v[160:163],v[32:35],a[120:123]
mm a[120:123],v[164:167],v[36:39],a[120:123]
mm a[136:139],v[164:167],v[44:47],a[136:139]
rt v[192:193],v240 offset:0
rt v[194:195],v240 offset:1024
mm a[136:139],v[160:163],v[40:43],a[136:139]
rt v[196:197],v240 offset:16896
rt v[198:199],v240 offset:17920
mm a[224:227],v[160:163],v[48:51],a[224:227]
mm a[224:227],v[164:167],v[52:55],a[224:227]
mm a[144:147],v[164:167],v[60:63],a[144:147]
mm a[144:147],v[160:163],v[56:59],a[144:147]
rd v[160:163],v236 offset:24576
rd v[164:167],v237 offset:24576
mm a[148:151],v[168:171],v[56:59],a[148:151]
mm a[148:151],v[172:175],v[60:63],a[148:151]
mm a[228:231],v[172:175],v[52:55],a[228:231]
mm a[228:231],v[168:171],v[48:51],a[228:231]
rt v[200:201],v241 offset:0
rt v[202:203],v241 offset:1024
mm a[140:143],v[168:171],v[40:43],a[140:143]
mm a[140:143],v[172:175],v[44:47],a[140:143]
mm a[124:127],v[172:175],v[36:39],a[124:127]
mm a[124:127],v[168:171],v[32:35],a[124:127]
rd v[168:171],v236 offset:26624
rd v[172:175],v237 offset:26624
mm a[216:219],v[176:179],v[32:35],a[216:219]
mm a[216:219],v[180:183],v[36:39],a[216:219]
mm a[168:171],v[180:183],v[44:47],a[168:171]
mm a[168:171],v[176:179],v[40:43],a[168:171]
rt v[204:205],v241 offset:16896
rt v[206:207],v241 offset:17920
mm a[192:195],v[176:179],v[48:51],a[192:195]
mm a[192:195],v[180:183],v[52:55],a[192:195]
mm a[176:179],v[180:183],v[60:63],a[176:179]
mm a[176:179],v[176:179],v[56:59],a[176:179]
rd v[176:179],v236 offset:16384
rd v[180:183],v237 offset:16384
mm a[180:183],v[184:187],v[56:59],a[180:183]
mm a[180:183],v[188:191],v[60:63],a[180:183]
mm a[196:199],v[188:191],v[52:55],a[196:199]
mm a[196:199],v[184:187],v[48:51],a[196:199]
rt v[208:209],v242 offset:0
rt v[210:211],v242 offset:1024
mm a[172:175],v[184:187],v[40:43],a[172:175]
s_add_u32 m0, s83, 33792
mm a[172:175],v[188:191],v[44:47],a[172:175]
ld v232,s[44:47],s80 offen lds
mm a[220:223],v[188:191],v[36:39],a[220:223]
mm a[220:223],v[184:187],v[32:35],a[220:223]
rd v[184:187],v236 offset:18432
rd v[188:191],v237 offset:18432
mm a[72:75],v[0:3],v[32:35],a[72:75]
s_add_u32 m0, s83, 34816
mm a[72:75],v[4:7],v[36:39],a[72:75]
ld v233,s[44:47],s80 offen lds
mm a[8:11],v[4:7],v[44:47],a[8:11]
mm a[8:11],v[0:3],v[40:43],a[8:11]
rt v[212:213],v242 offset:16896
rt v[214:215],v242 offset:17920
mm a[80:83],v[0:3],v[48:51],a[80:83]
s_add_u32 m0, s83, 35968
mm a[80:83],v[4:7],v[52:55],a[80:83]
ld v234,s[44:47],s80 offen lds
mm a[16:19],v[4:7],v[60:63],a[16:19]
mm a[16:19],v[0:3],v[56:59],a[16:19]
rd v[0:3],v236 offset:8192
rd v[4:7],v237 offset:8192
mm a[20:23],v[8:11],v[56:59],a[20:23]
s_add_u32 m0, s83, 36992
mm a[20:23],v[12:15],v[60:63],a[20:23]
ld v235,s[44:47],s80 offen lds
mm a[84:87],v[12:15],v[52:55],a[84:87]
s_mov_b32 s80, 1835008
)ASM" R"ASM(mm a[84:87],v[8:11],v[48:51],a[84:87]
rt v[216:217],v243 offset:0
rt v[218:219],v243 offset:1024
mm a[12:15],v[8:11],v[40:43],a[12:15]
s_add_u32 m0, s83, 50688
mm a[12:15],v[12:15],v[44:47],a[12:15]
ld v232,s[44:47],s80 offen lds
mm a[76:79],v[12:15],v[36:39],a[76:79]
mm a[76:79],v[8:11],v[32:35],a[76:79]
rd v[8:11],v236 offset:10240
rd v[12:15],v237 offset:10240
mm a[248:251],v[16:19],v[32:35],a[248:251]
s_add_u32 m0, s83, 51712
mm a[248:251],v[20:23],v[36:39],a[248:251]
ld v233,s[44:47],s80 offen lds
mm a[40:43],v[20:23],v[44:47],a[40:43]
mm a[40:43],v[16:19],v[40:43],a[40:43]
rt v[220:221],v243 offset:16896
rt v[222:223],v243 offset:17920
mm a[48:51],v[16:19],v[48:51],a[48:51]
s_add_u32 m0, s83, 52864
mm a[48:51],v[20:23],v[52:55],a[48:51]
ld v234,s[44:47],s80 offen lds
mm a[56:59],v[20:23],v[60:63],a[56:59]
mm a[56:59],v[16:19],v[56:59],a[56:59]
rd v[16:19],v236 offset:0
rd v[20:23],v237 offset:0
mm a[60:63],v[24:27],v[56:59],a[60:63]
s_add_u32 m0, s83, 53888
mm a[60:63],v[28:31],v[60:63],a[60:63]
ld v235,s[44:47],s80 offen lds
mm a[52:55],v[28:31],v[52:55],a[52:55]
s_mov_b64 s[44:45], s[68:69]
s_mov_b32 s46, s70
mm a[52:55],v[24:27],v[48:51],a[52:55]
mm a[44:47],v[24:27],v[40:43],a[44:47]
mm a[44:47],v[28:31],v[44:47],a[44:47]
mm a[252:255],v[28:31],v[36:39],a[252:255]
wt lgkmcnt(2)
s_lshl_b32 s80, s77, 18
mm a[252:255],v[24:27],v[32:35],a[252:255]
rd v[24:27],v236 offset:2048
rd v[28:31],v237 offset:2048
mm a[96:99],v[160:163],v[192:195],a[96:99]
mm a[96:99],v[164:167],v[196:199],a[96:99]
mm a[104:107],v[164:167],v[204:207],a[104:107]
mm a[104:107],v[160:163],v[200:203],a[104:107]
rt v[32:33],v240 offset:128
rt v[34:35],v240 offset:1152
mm a[112:115],v[160:163],v[208:211],a[112:115]
mm a[112:115],v[164:167],v[212:215],a[112:115]
mm a[128:131],v[164:167],v[220:223],a[128:131]
wt lgkmcnt(2)
s_barrier
mm a[128:131],v[160:163],v[216:219],a[128:131]
mm a[132:135],v[168:171],v[216:219],a[132:135]
mm a[132:135],v[172:175],v[220:223],a[132:135]
mm a[116:119],v[172:175],v[212:215],a[116:119]
mm a[116:119],v[168:171],v[208:211],a[116:119]
rt v[36:37],v240 offset:17024
rt v[38:39],v240 offset:18048
mm a[108:111],v[168:171],v[200:203],a[108:111]
mm a[108:111],v[172:175],v[204:207],a[108:111]
mm a[100:103],v[172:175],v[196:199],a[100:103]
mm a[100:103],v[168:171],v[192:195],a[100:103]
mm a[232:235],v[176:179],v[192:195],a[232:235]
mm a[232:235],v[180:183],v[196:199],a[232:235]
mm a[152:155],v[180:183],v[204:207],a[152:155]
mm a[152:155],v[176:179],v[200:203],a[152:155]
rt v[40:41],v241 offset:128
rt v[42:43],v241 offset:1152
mm a[208:211],v[176:179],v[208:211],a[208:211]
mm a[208:211],v[180:183],v[212:215],a[208:211]
mm a[160:163],v[180:183],v[220:223],a[160:163]
mm a[160:163],v[176:179],v[216:219],a[160:163]
mm a[164:167],v[184:187],v[216:219],a[164:167]
mm a[164:167],v[188:191],v[220:223],a[164:167]
mm a[212:215],v[188:191],v[212:215],a[212:215]
mm a[212:215],v[184:187],v[208:211],a[212:215]
rt v[44:45],v241 offset:17024
rt v[46:47],v241 offset:18048
mm a[156:159],v[184:187],v[200:203],a[156:159]
mm a[156:159],v[188:191],v[204:207],a[156:159]
mm a[236:239],v[188:191],v[196:199],a[236:239]
mm a[236:239],v[184:187],v[192:195],a[236:239]
mm a[200:203],v[0:3],v[192:195],a[200:203]
mm a[200:203],v[4:7],v[196:199],a[200:203]
mm a[184:187],v[4:7],v[204:207],a[184:187]
mm a[184:187],v[0:3],v[200:203],a[184:187]
rt v[48:49],v242 offset:128
rt v[50:51],v242 offset:1152
mm a[64:67],v[0:3],v[208:211],a[64:67]
mm a[64:67],v[4:7],v[212:215],a[64:67]
mm a[0:3],v[4:7],v[220:223],a[0:3]
mm a[0:3],v[0:3],v[216:219],a[0:3]
mm a[4:7],v[8:11],v[216:219],a[4:7]
mm a[4:7],v[12:15],v[220:223],a[4:7]
buffer_load_dword v254, v135, s[68:71], s96 offen
mm a[68:71],v[12:15],v[212:215],a[68:71]
s_add_u32 s96, s96, 1048576
)ASM" R"ASM(mm a[68:71],v[8:11],v[208:211],a[68:71]
rt v[52:53],v242 offset:17024
rt v[54:55],v242 offset:18048
mm a[188:191],v[8:11],v[200:203],a[188:191]
mm a[188:191],v[12:15],v[204:207],a[188:191]
buffer_load_dword v254, v135, s[68:71], s96 offen
mm a[204:207],v[12:15],v[196:199],a[204:207]
s_add_u32 s96, s96, 1048576
mm a[204:207],v[8:11],v[192:195],a[204:207]
rt v[56:57],v243 offset:128
rt v[58:59],v243 offset:1152
mm a[88:91],v[16:19],v[192:195],a[88:91]
mm a[88:91],v[20:23],v[196:199],a[88:91]
mm a[24:27],v[20:23],v[204:207],a[24:27]
mm a[24:27],v[16:19],v[200:203],a[24:27]
rt v[60:61],v243 offset:17024
rt v[62:63],v243 offset:18048
mm a[240:243],v[16:19],v[208:211],a[240:243]
mm a[240:243],v[20:23],v[212:215],a[240:243]
mm a[32:35],v[20:23],v[220:223],a[32:35]
mm a[32:35],v[16:19],v[216:219],a[32:35]
mm a[36:39],v[24:27],v[216:219],a[36:39]
mm a[36:39],v[28:31],v[220:223],a[36:39]
ld v[72:75],v255,s[64:67],s89 offen offset:1024 sc1
mm a[244:247],v[28:31],v[212:215],a[244:247]
mm a[244:247],v[24:27],v[208:211],a[244:247]
mm a[28:31],v[24:27],v[200:203],a[28:31]
mm a[28:31],v[28:31],v[204:207],a[28:31]
mm a[92:95],v[28:31],v[196:199],a[92:95]
wt vmcnt(3) lgkmcnt(0)
s_barrier
mm a[92:95],v[24:27],v[192:195],a[92:95]
mm a[120:123],v[160:163],v[32:35],a[120:123]
mm a[120:123],v[164:167],v[36:39],a[120:123]
mm a[136:139],v[164:167],v[44:47],a[136:139]
rt v[192:193],v240 offset:33792
rt v[194:195],v240 offset:34816
mm a[136:139],v[160:163],v[40:43],a[136:139]
rt v[196:197],v240 offset:50688
rt v[198:199],v240 offset:51712
mm a[224:227],v[160:163],v[48:51],a[224:227]
mm a[224:227],v[164:167],v[52:55],a[224:227]
mm a[144:147],v[164:167],v[60:63],a[144:147]
mm a[144:147],v[160:163],v[56:59],a[144:147]
rd v[160:163],v236 offset:57344
rd v[164:167],v237 offset:57344
mm a[148:151],v[168:171],v[56:59],a[148:151]
mm a[148:151],v[172:175],v[60:63],a[148:151]
mm a[228:231],v[172:175],v[52:55],a[228:231]
mm a[228:231],v[168:171],v[48:51],a[228:231]
rt v[200:201],v241 offset:33792
rt v[202:203],v241 offset:34816
mm a[140:143],v[168:171],v[40:43],a[140:143]
mm a[140:143],v[172:175],v[44:47],a[140:143]
mm a[124:127],v[172:175],v[36:39],a[124:127]
mm a[124:127],v[168:171],v[32:35],a[124:127]
rd v[168:171],v236 offset:59392
rd v[172:175],v237 offset:59392
mm a[216:219],v[176:179],v[32:35],a[216:219]
mm a[216:219],v[180:183],v[36:39],a[216:219]
mm a[168:171],v[180:183],v[44:47],a[168:171]
mm a[168:171],v[176:179],v[40:43],a[168:171]
rt v[204:205],v241 offset:50688
rt v[206:207],v241 offset:51712
mm a[192:195],v[176:179],v[48:51],a[192:195]
mm a[192:195],v[180:183],v[52:55],a[192:195]
mm a[176:179],v[180:183],v[60:63],a[176:179]
mm a[176:179],v[176:179],v[56:59],a[176:179]
rd v[176:179],v236 offset:49152
rd v[180:183],v237 offset:49152
mm a[180:183],v[184:187],v[56:59],a[180:183]
mm a[180:183],v[188:191],v[60:63],a[180:183]
mm a[196:199],v[188:191],v[52:55],a[196:199]
mm a[196:199],v[184:187],v[48:51],a[196:199]
rt v[208:209],v242 offset:33792
rt v[210:211],v242 offset:34816
mm a[172:175],v[184:187],v[40:43],a[172:175]
s_add_u32 m0, s83, 0
mm a[172:175],v[188:191],v[44:47],a[172:175]
ld v232,s[44:47],s80 offen lds
mm a[220:223],v[188:191],v[36:39],a[220:223]
mm a[220:223],v[184:187],v[32:35],a[220:223]
rd v[184:187],v236 offset:51200
rd v[188:191],v237 offset:51200
mm a[72:75],v[0:3],v[32:35],a[72:75]
s_add_u32 m0, s83, 1024
mm a[72:75],v[4:7],v[36:39],a[72:75]
ld v233,s[44:47],s80 offen lds
mm a[8:11],v[4:7],v[44:47],a[8:11]
mm a[8:11],v[0:3],v[40:43],a[8:11]
rt v[212:213],v242 offset:50688
rt v[214:215],v242 offset:51712
mm a[80:83],v[0:3],v[48:51],a[80:83]
s_add_u32 m0, s83, 2176
mm a[80:83],v[4:7],v[52:55],a[80:83]
ld v234,s[44:47],s80 offen lds
mm a[16:19],v[4:7],v[60:63],a[16:19]
mm a[16:19],v[0:3],v[56:59],a[16:19]
rd v[0:3],v236 offset:40960
rd v[4:7],v237 offset:40960
)ASM" R"ASM(mm a[20:23],v[8:11],v[56:59],a[20:23]
s_add_u32 m0, s83, 3200
mm a[20:23],v[12:15],v[60:63],a[20:23]
ld v235,s[44:47],s80 offen lds
mm a[84:87],v[12:15],v[52:55],a[84:87]
s_add_u32 s80, s80, 524288
mm a[84:87],v[8:11],v[48:51],a[84:87]
rt v[216:217],v243 offset:33792
rt v[218:219],v243 offset:34816
mm a[12:15],v[8:11],v[40:43],a[12:15]
s_add_u32 m0, s83, 16896
mm a[12:15],v[12:15],v[44:47],a[12:15]
ld v232,s[44:47],s80 offen lds
mm a[76:79],v[12:15],v[36:39],a[76:79]
mm a[76:79],v[8:11],v[32:35],a[76:79]
rd v[8:11],v236 offset:43008
rd v[12:15],v237 offset:43008
mm a[248:251],v[16:19],v[32:35],a[248:251]
s_add_u32 m0, s83, 17920
mm a[248:251],v[20:23],v[36:39],a[248:251]
ld v233,s[44:47],s80 offen lds
mm a[40:43],v[20:23],v[44:47],a[40:43]
mm a[40:43],v[16:19],v[40:43],a[40:43]
rt v[220:221],v243 offset:50688
rt v[222:223],v243 offset:51712
mm a[48:51],v[16:19],v[48:51],a[48:51]
s_add_u32 m0, s83, 19072
mm a[48:51],v[20:23],v[52:55],a[48:51]
ld v234,s[44:47],s80 offen lds
mm a[56:59],v[20:23],v[60:63],a[56:59]
mm a[56:59],v[16:19],v[56:59],a[56:59]
rd v[16:19],v236 offset:32768
rd v[20:23],v237 offset:32768
mm a[60:63],v[24:27],v[56:59],a[60:63]
s_add_u32 m0, s83, 20096
mm a[60:63],v[28:31],v[60:63],a[60:63]
ld v235,s[44:47],s80 offen lds
mm a[52:55],v[28:31],v[52:55],a[52:55]
s_add_u32 s80, s80, 524288
mm a[52:55],v[24:27],v[48:51],a[52:55]
mm a[44:47],v[24:27],v[40:43],a[44:47]
mm a[44:47],v[28:31],v[44:47],a[44:47]
mm a[252:255],v[28:31],v[36:39],a[252:255]
wt lgkmcnt(2)
mm a[252:255],v[24:27],v[32:35],a[252:255]
rd v[24:27],v236 offset:34816
rd v[28:31],v237 offset:34816
mm a[96:99],v[160:163],v[192:195],a[96:99]
mm a[96:99],v[164:167],v[196:199],a[96:99]
mm a[104:107],v[164:167],v[204:207],a[104:107]
mm a[104:107],v[160:163],v[200:203],a[104:107]
rt v[32:33],v240 offset:33920
rt v[34:35],v240 offset:34944
mm a[112:115],v[160:163],v[208:211],a[112:115]
mm a[112:115],v[164:167],v[212:215],a[112:115]
mm a[128:131],v[164:167],v[220:223],a[128:131]
wt lgkmcnt(2)
s_barrier
mm a[128:131],v[160:163],v[216:219],a[128:131]
mm a[132:135],v[168:171],v[216:219],a[132:135]
wt vmcnt(8)
v_cmp_eq_u64 vcc, v[72:73], v[252:253]
mm a[132:135],v[172:175],v[220:223],a[132:135]
v_cmp_eq_u64 s[96:97], v[74:75], v[252:253]
mm a[116:119],v[172:175],v[212:215],a[116:119]
s_and_b64 vcc, vcc, s[96:97]
s_cmp_eq_u64 vcc, -1
s_cbranch_scc0 .La5_LV_XSPIN
.La5_LV_XOK:
mm a[116:119],v[168:171],v[208:211],a[116:119]
rt v[36:37],v240 offset:50816
rt v[38:39],v240 offset:51840
mm a[108:111],v[168:171],v[200:203],a[108:111]
s_mov_b32 s89, 49152
mm a[108:111],v[172:175],v[204:207],a[108:111]
mm a[100:103],v[172:175],v[196:199],a[100:103]
mm a[100:103],v[168:171],v[192:195],a[100:103]
mm a[232:235],v[176:179],v[192:195],a[232:235]
mm a[232:235],v[180:183],v[196:199],a[232:235]
ld v[128:131],v244,s[52:55],s91 offen offset:0 sc1
mm a[152:155],v[180:183],v[204:207],a[152:155]
ar v224,a96
ar v225,a97
mm a[152:155],v[176:179],v[200:203],a[152:155]
rt v[40:41],v241 offset:33920
rt v[42:43],v241 offset:34944
mm a[208:211],v[176:179],v[208:211],a[208:211]
ar v226,a98
ar v227,a99
mm a[208:211],v[180:183],v[212:215],a[208:211]
ld v[132:135],v245,s[52:55],s91 offen offset:0 sc1
mm a[160:163],v[180:183],v[220:223],a[160:163]
ar v228,a100
ar v229,a101
mm a[160:163],v[176:179],v[216:219],a[160:163]
ar v230,a102
ar v231,a103
mm a[164:167],v[184:187],v[216:219],a[164:167]
cv v248,v224,v225
cv v249,v226,v227
)ASM" R"ASM(mm a[164:167],v[188:191],v[220:223],a[164:167]
ld v[136:139],v246,s[52:55],s91 offen offset:0 sc1
mm a[212:215],v[188:191],v[212:215],a[212:215]
cv v250,v228,v229
cv v251,v230,v231
mm a[212:215],v[184:187],v[208:211],a[212:215]
rt v[44:45],v241 offset:50816
rt v[46:47],v241 offset:51840
mm a[156:159],v[184:187],v[200:203],a[156:159]
st v[248:251],v255,s[48:51],s89 offen sc1
mm a[156:159],v[188:191],v[204:207],a[156:159]
ld v[140:143],v247,s[52:55],s91 offen offset:0 sc1
mm a[236:239],v[188:191],v[196:199],a[236:239]
s_mov_b32 s89, 50176
ds_write_b128 v238, v[248:251] offset:0
mm a[236:239],v[184:187],v[192:195],a[236:239]
ar v224,a104
ar v225,a105
mm a[200:203],v[0:3],v[192:195],a[200:203]
ar v226,a106
ar v227,a107
mm a[200:203],v[4:7],v[196:199],a[200:203]
ld v[144:147],v244,s[52:55],s91 offen offset:1024 sc1
mm a[184:187],v[4:7],v[204:207],a[184:187]
ar v228,a108
ar v229,a109
mm a[184:187],v[0:3],v[200:203],a[184:187]
rt v[48:49],v242 offset:33920
rt v[50:51],v242 offset:34944
mm a[64:67],v[0:3],v[208:211],a[64:67]
ar v230,a110
ar v231,a111
mm a[64:67],v[4:7],v[212:215],a[64:67]
ld v[148:151],v245,s[52:55],s91 offen offset:1024 sc1
mm a[0:3],v[4:7],v[220:223],a[0:3]
cv v248,v224,v225
cv v249,v226,v227
mm a[0:3],v[0:3],v[216:219],a[0:3]
cv v250,v228,v229
cv v251,v230,v231
mm a[4:7],v[8:11],v[216:219],a[4:7]
st v[248:251],v255,s[48:51],s89 offen sc1
mm a[4:7],v[12:15],v[220:223],a[4:7]
ld v[152:155],v246,s[52:55],s91 offen offset:1024 sc1
mm a[68:71],v[12:15],v[212:215],a[68:71]
s_mov_b32 s89, 51200
ds_write_b128 v238, v[248:251] offset:1024
mm a[68:71],v[8:11],v[208:211],a[68:71]
rt v[52:53],v242 offset:50816
rt v[54:55],v242 offset:51840
mm a[188:191],v[8:11],v[200:203],a[188:191]
ar v224,a112
ar v225,a113
mm a[188:191],v[12:15],v[204:207],a[188:191]
ld v[156:159],v247,s[52:55],s91 offen offset:1024 sc1
mm a[204:207],v[12:15],v[196:199],a[204:207]
ar v226,a114
ar v227,a115
mm a[204:207],v[8:11],v[192:195],a[204:207]
rt v[56:57],v243 offset:33920
rt v[58:59],v243 offset:34944
mm a[88:91],v[16:19],v[192:195],a[88:91]
ar v228,a116
ar v229,a117
mm a[88:91],v[20:23],v[196:199],a[88:91]
ar v230,a118
ar v231,a119
mm a[24:27],v[20:23],v[204:207],a[24:27]
cv v248,v224,v225
cv v249,v226,v227
mm a[24:27],v[16:19],v[200:203],a[24:27]
rt v[60:61],v243 offset:50816
rt v[62:63],v243 offset:51840
mm a[240:243],v[16:19],v[208:211],a[240:243]
v_subrev_u32 v240, 8448, v240
v_subrev_u32 v241, 8448, v241
mm a[240:243],v[20:23],v[212:215],a[240:243]
v_subrev_u32 v242, 8448, v242
v_subrev_u32 v243, 8448, v243
mm a[32:35],v[20:23],v[220:223],a[32:35]
.La5_LW1_8:
s_cbranch_execz .La5_LW1_12
cv v250,v228,v229
cv v251,v230,v231
mm a[32:35],v[16:19],v[216:219],a[32:35]
st v[248:251],v255,s[48:51],s89 offen sc1
mm a[36:39],v[24:27],v[216:219],a[36:39]
s_mov_b32 s89, 52224
ds_write_b128 v238, v[248:251] offset:2048
mm a[36:39],v[28:31],v[220:223],a[36:39]
mm a[244:247],v[28:31],v[212:215],a[244:247]
mm a[244:247],v[24:27],v[208:211],a[244:247]
mm a[28:31],v[24:27],v[200:203],a[28:31]
mm a[28:31],v[28:31],v[204:207],a[28:31]
mm a[92:95],v[28:31],v[196:199],a[92:95]
wt vmcnt(11) lgkmcnt(1)
s_barrier
mm a[92:95],v[24:27],v[192:195],a[92:95]
mm a[120:123],v[160:163],v[32:35],a[120:123]
s_add_u32 m0, s83, 33792
)ASM" R"ASM(mm a[120:123],v[164:167],v[36:39],a[120:123]
ld v232,s[44:47],s80 offen lds
mm a[136:139],v[164:167],v[44:47],a[136:139]
mm a[136:139],v[160:163],v[40:43],a[136:139]
rt v[192:193],v240 offset:0
rt v[194:195],v240 offset:1024
mm a[224:227],v[160:163],v[48:51],a[224:227]
s_add_u32 m0, s83, 34816
mm a[224:227],v[164:167],v[52:55],a[224:227]
ld v233,s[44:47],s80 offen lds
mm a[144:147],v[164:167],v[60:63],a[144:147]
mm a[144:147],v[160:163],v[56:59],a[144:147]
rt v[196:197],v240 offset:8448
rt v[198:199],v240 offset:9472
mm a[148:151],v[168:171],v[56:59],a[148:151]
s_add_u32 m0, s83, 35968
mm a[148:151],v[172:175],v[60:63],a[148:151]
ld v234,s[44:47],s80 offen lds
mm a[228:231],v[172:175],v[52:55],a[228:231]
mm a[228:231],v[168:171],v[48:51],a[228:231]
rt v[200:201],v241 offset:0
rt v[202:203],v241 offset:1024
mm a[140:143],v[168:171],v[40:43],a[140:143]
s_add_u32 m0, s83, 36992
mm a[140:143],v[172:175],v[44:47],a[140:143]
ld v235,s[44:47],s80 offen lds
mm a[124:127],v[172:175],v[36:39],a[124:127]
s_add_u32 s80, s80, 524288
mm a[124:127],v[168:171],v[32:35],a[124:127]
rt v[204:205],v241 offset:8448
rt v[206:207],v241 offset:9472
mm a[216:219],v[176:179],v[32:35],a[216:219]
s_add_u32 m0, s83, 50688
mm a[216:219],v[180:183],v[36:39],a[216:219]
ld v232,s[44:47],s80 offen lds
mm a[168:171],v[180:183],v[44:47],a[168:171]
mm a[168:171],v[176:179],v[40:43],a[168:171]
rt v[208:209],v242 offset:0
rt v[210:211],v242 offset:1024
mm a[192:195],v[176:179],v[48:51],a[192:195]
s_add_u32 m0, s83, 51712
mm a[192:195],v[180:183],v[52:55],a[192:195]
ld v233,s[44:47],s80 offen lds
mm a[176:179],v[180:183],v[60:63],a[176:179]
mm a[176:179],v[176:179],v[56:59],a[176:179]
rt v[212:213],v242 offset:8448
rt v[214:215],v242 offset:9472
mm a[180:183],v[184:187],v[56:59],a[180:183]
s_add_u32 m0, s83, 52864
mm a[180:183],v[188:191],v[60:63],a[180:183]
ld v234,s[44:47],s80 offen lds
mm a[196:199],v[188:191],v[52:55],a[196:199]
mm a[196:199],v[184:187],v[48:51],a[196:199]
rt v[216:217],v243 offset:0
rt v[218:219],v243 offset:1024
mm a[172:175],v[184:187],v[40:43],a[172:175]
s_add_u32 m0, s83, 53888
mm a[172:175],v[188:191],v[44:47],a[172:175]
ld v235,s[44:47],s80 offen lds
mm a[220:223],v[188:191],v[36:39],a[220:223]
s_add_u32 s80, s80, 524288
mm a[220:223],v[184:187],v[32:35],a[220:223]
rt v[220:221],v243 offset:8448
rt v[222:223],v243 offset:9472
mm a[72:75],v[0:3],v[32:35],a[72:75]
mm a[72:75],v[4:7],v[36:39],a[72:75]
ld v[160:163],v244,s[52:55],s91 offen offset:2048 sc1
mm a[8:11],v[4:7],v[44:47],a[8:11]
mm a[8:11],v[0:3],v[40:43],a[8:11]
mm a[80:83],v[0:3],v[48:51],a[80:83]
mm a[80:83],v[4:7],v[52:55],a[80:83]
ld v[164:167],v245,s[52:55],s91 offen offset:2048 sc1
mm a[16:19],v[4:7],v[60:63],a[16:19]
mm a[16:19],v[0:3],v[56:59],a[16:19]
mm a[20:23],v[8:11],v[56:59],a[20:23]
mm a[20:23],v[12:15],v[60:63],a[20:23]
ld v[168:171],v246,s[52:55],s91 offen offset:2048 sc1
mm a[84:87],v[12:15],v[52:55],a[84:87]
mm a[84:87],v[8:11],v[48:51],a[84:87]
mm a[12:15],v[8:11],v[40:43],a[12:15]
mm a[12:15],v[12:15],v[44:47],a[12:15]
ld v[172:175],v247,s[52:55],s91 offen offset:2048 sc1
mm a[76:79],v[12:15],v[36:39],a[76:79]
mm a[76:79],v[8:11],v[32:35],a[76:79]
mm a[248:251],v[16:19],v[32:35],a[248:251]
mm a[248:251],v[20:23],v[36:39],a[248:251]
mm a[40:43],v[20:23],v[44:47],a[40:43]
mm a[40:43],v[16:19],v[40:43],a[40:43]
mm a[48:51],v[16:19],v[48:51],a[48:51]
mm a[48:51],v[20:23],v[52:55],a[48:51]
mm a[56:59],v[20:23],v[60:63],a[56:59]
mm a[56:59],v[16:19],v[56:59],a[56:59]
mm a[60:63],v[24:27],v[56:59],a[60:63]
mm a[60:63],v[28:31],v[60:63],a[60:63]
mm a[52:55],v[28:31],v[52:55],a[52:55]
mm a[52:55],v[24:27],v[48:51],a[52:55]
mm a[44:47],v[24:27],v[40:43],a[44:47]
mm a[44:47],v[28:31],v[44:47],a[44:47]
mm a[252:255],v[28:31],v[36:39],a[252:255]
mm a[252:255],v[24:27],v[32:35],a[252:255]
)ASM" R"ASM(; ---- Q tile: 256 rows x 128 columns, K = 1024, while K / V leave the accumulators (memory + .La5_LDS ring); behind it the first step on the diagonal
.La5_LQ_START:
.La5_LW1_9:
s_cbranch_execz .La5_LW1_13
wt vmcnt(13) lgkmcnt(4)
mm v[0:3],v[192:195],v[128:131],0
ar v224,a128
ar v225,a129
s_mov_b32 s85, s95
mm v[0:3],v[196:199],v[144:147],v[0:3]
ld v[176:179],v244,s[52:55],s91 offen offset:3072 sc1
mm v[32:35],v[196:199],v[148:151],0
ar v226,a130
ar v227,a131
mm v[32:35],v[192:195],v[132:135],v[32:35]
ar v228,a132
ar v229,a133
mm v[64:67],v[192:195],v[136:139],0
ar v230,a134
ar v231,a135
mm v[64:67],v[196:199],v[152:155],v[64:67]
ld v[180:183],v245,s[52:55],s91 offen offset:3072 sc1
mm v[96:99],v[196:199],v[156:159],0
cv v248,v224,v225
cv v249,v226,v227
mm v[96:99],v[192:195],v[140:143],v[96:99]
rt v[196:197],v240 offset:128
rt v[198:199],v240 offset:1152
mm v[100:103],v[200:203],v[140:143],0
cv v250,v228,v229
cv v251,v230,v231
mm v[100:103],v[204:207],v[156:159],v[100:103]
ld v[184:187],v246,s[52:55],s91 offen offset:3072 sc1
mm v[68:71],v[204:207],v[152:155],0
aw a128,v248
aw a129,v249
mm v[68:71],v[200:203],v[136:139],v[68:71]
rt v[192:193],v240 offset:8576
rt v[194:195],v240 offset:9600
mm v[36:39],v[200:203],v[132:135],0
aw a130,v250
aw a131,v251
mm v[36:39],v[204:207],v[148:151],v[36:39]
ld v[188:191],v247,s[52:55],s91 offen offset:3072 sc1
mm v[4:7],v[204:207],v[144:147],0
wt lgkmcnt(4)
s_add_u32 s91, s91, 4096
mm v[4:7],v[200:203],v[128:131],v[4:7]
rt v[204:205],v241 offset:128
rt v[206:207],v241 offset:1152
mm v[8:11],v[208:211],v[128:131],0
s_and_b32 s97, s2, 7
s_mul_i32 s95, s97, 7
mm v[8:11],v[212:215],v[144:147],v[8:11]
ld a[96:99],v244,s[52:55],s91 offen offset:0 sc1
mm v[40:43],v[212:215],v[148:151],0
s_lshr_b32 s96, s30, 7
s_add_u32 s95, s95, s96
mm v[40:43],v[208:211],v[132:135],v[40:43]
rt v[200:201],v241 offset:8576
rt v[202:203],v241 offset:9600
mm v[72:75],v[208:211],v[136:139],0
s_sub_u32 s95, 3, s95
s_and_b32 s95, s95, 7
mm v[72:75],v[212:215],v[152:155],v[72:75]
ld a[100:103],v245,s[52:55],s91 offen offset:0 sc1
mm v[104:107],v[212:215],v[156:159],0
s_min_u32 s95, s95, 6
s_lshl_b32 s95, s95, 1
mm v[104:107],v[208:211],v[140:143],v[104:107]
rt v[212:213],v242 offset:128
rt v[214:215],v242 offset:1152
mm v[108:111],v[216:219],v[140:143],0
s_lshr_b32 s92, s2, 6
s_mul_i32 s93, s92, 3
mm v[108:111],v[220:223],v[156:159],v[108:111]
ld a[104:107],v246,s[52:55],s91 offen offset:0 sc1
mm v[76:79],v[220:223],v[152:155],0
s_add_u32 s93, s93, s74
mm v[76:79],v[216:219],v[136:139],v[76:79]
rt v[208:209],v242 offset:8576
rt v[210:211],v242 offset:9600
mm v[44:47],v[216:219],v[132:135],0
ds_write_b128 v238, v[248:251] offset:3072
mm v[44:47],v[220:223],v[148:151],v[44:47]
st v[248:251],v255,s[48:51],s89 offen offset:0 sc1
mm v[12:15],v[220:223],v[144:147],0
wt lgkmcnt(5)
mm v[12:15],v[216:219],v[128:131],v[12:15]
rt v[220:221],v243 offset:128
rt v[222:223],v243 offset:1152
mm v[16:19],v[196:199],v[128:131],0
ar v224,a120
ar v225,a121
mm v[16:19],v[192:195],v[144:147],v[16:19]
ld a[108:111],v247,s[52:55],s91 offen offset:0 sc1
mm v[48:51],v[192:195],v[148:151],0
ar v226,a122
ar v227,a123
mm v[48:51],v[196:199],v[132:135],v[48:51]
)ASM" R"ASM(rt v[216:217],v243 offset:8576
rt v[218:219],v243 offset:9600
mm v[80:83],v[196:199],v[136:139],0
ar v228,a124
ar v229,a125
mm v[80:83],v[192:195],v[152:155],v[80:83]
ld a[112:115],v244,s[52:55],s91 offen offset:1024 sc1
mm v[112:115],v[192:195],v[156:159],0
ar v230,a126
ar v231,a127
mm v[112:115],v[196:199],v[140:143],v[112:115]
cv v248,v224,v225
cv v249,v226,v227
mm v[116:119],v[204:207],v[140:143],0
wt lgkmcnt(0)
s_barrier
mm v[116:119],v[200:203],v[156:159],v[116:119]
ld a[116:119],v245,s[52:55],s91 offen offset:1024 sc1
mm v[84:87],v[200:203],v[152:155],0
rt v[192:193],v240 offset:16896
rt v[194:195],v240 offset:17920
mm v[84:87],v[204:207],v[136:139],v[84:87]
rt v[196:197],v240 offset:25344
rt v[198:199],v240 offset:26368
mm v[52:55],v[204:207],v[132:135],0
s_add_u32 m0, s83, 0
cv v250,v228,v229
mm v[52:55],v[200:203],v[148:151],v[52:55]
ld v232,s[44:47],s80 offen lds
mm v[20:23],v[200:203],v[144:147],0
cv v251,v230,v231
aw a132,v248
mm v[20:23],v[204:207],v[128:131],v[20:23]
rt v[200:201],v241 offset:16896
rt v[202:203],v241 offset:17920
mm v[24:27],v[212:215],v[128:131],0
aw a133,v249
aw a134,v250
mm v[24:27],v[208:211],v[144:147],v[24:27]
aw a135,v251
mm v[56:59],v[208:211],v[148:151],0
s_cmp_ge_u32 s93, s95
s_cselect_b32 s96, 2, 0
mm v[56:59],v[212:215],v[132:135],v[56:59]
rt v[204:205],v241 offset:25344
rt v[206:207],v241 offset:26368
mm v[88:91],v[212:215],v[136:139],0
s_add_u32 m0, s83, 1024
s_add_u32 s93, s93, s96
mm v[88:91],v[208:211],v[152:155],v[88:91]
ld v233,s[44:47],s80 offen lds
mm v[120:123],v[208:211],v[156:159],0
s_add_u32 s95, s95, s92
mm v[120:123],v[212:215],v[140:143],v[120:123]
rt v[208:209],v242 offset:16896
rt v[210:211],v242 offset:17920
mm v[124:127],v[220:223],v[140:143],0
s_cmp_lt_u32 s92, 2
s_cselect_b32 s94, s75, 0
mm v[124:127],v[216:219],v[156:159],v[124:127]
ld v[140:143],v246,s[52:55],s91 offen offset:1024 sc1
mm v[92:95],v[216:219],v[152:155],0
s_cmp_eq_u32 s74, 3
s_cselect_b32 s92, s95, s93
s_cselect_b32 s94, s94, s75
mm v[92:95],v[220:223],v[136:139],v[92:95]
rt v[212:213],v242 offset:25344
rt v[214:215],v242 offset:26368
mm v[60:63],v[220:223],v[132:135],0
ds_write_b128 v238, v[248:251] offset:4096
mm v[60:63],v[216:219],v[148:151],v[60:63]
s_mul_i32 s93, s97, 14
s_add_u32 s92, s92, s93
mm v[28:31],v[216:219],v[144:147],0
wt vmcnt(10) lgkmcnt(5)
s_mul_i32 s96, s92, s94
mm v[28:31],v[220:223],v[128:131],v[28:31]
rt v[216:217],v243 offset:16896
rt v[218:219],v243 offset:17920
mm v[0:3],v[192:195],v[160:163],v[0:3]
ar v224,a136
ar v225,a137
mm v[0:3],v[196:199],v[176:179],v[0:3]
ld v[128:131],v247,s[52:55],s91 offen offset:1024 sc1
mm v[32:35],v[196:199],v[180:183],v[32:35]
ar v226,a138
ar v227,a139
mm v[32:35],v[192:195],v[164:167],v[32:35]
rt v[220:221],v243 offset:25344
rt v[222:223],v243 offset:26368
mm v[64:67],v[192:195],v[168:171],v[64:67]
ar v228,a140
ar v229,a141
mm v[64:67],v[196:199],v[184:187],v[64:67]
ld v[132:135],v244,s[52:55],s91 offen offset:2048 sc1
mm v[96:99],v[196:199],v[188:191],v[96:99]
ar v230,a142
ar v231,a143
mm v[96:99],v[192:195],v[172:175],v[96:99]
rt v[196:197],v240 offset:17024
)ASM" R"ASM(rt v[198:199],v240 offset:18048
mm v[100:103],v[200:203],v[172:175],v[100:103]
s_add_u32 m0, s83, 2176
cv v248,v224,v225
mm v[100:103],v[204:207],v[188:191],v[100:103]
ld v234,s[44:47],s80 offen lds
mm v[68:71],v[204:207],v[184:187],v[68:71]
cv v249,v226,v227
cv v250,v228,v229
mm v[68:71],v[200:203],v[168:171],v[68:71]
rt v[192:193],v240 offset:25472
rt v[194:195],v240 offset:26496
mm v[36:39],v[200:203],v[164:167],v[36:39]
cv v251,v230,v231
aw a136,v248
mm v[36:39],v[204:207],v[180:183],v[36:39]
ld v[136:139],v245,s[52:55],s91 offen offset:2048 sc1
mm v[4:7],v[204:207],v[176:179],v[4:7]
wt lgkmcnt(4)
aw a137,v249
mm v[4:7],v[200:203],v[160:163],v[4:7]
rt v[204:205],v241 offset:17024
rt v[206:207],v241 offset:18048
mm v[8:11],v[208:211],v[160:163],v[8:11]
s_add_u32 m0, s83, 3200
aw a138,v250
mm v[8:11],v[212:215],v[176:179],v[8:11]
ld v235,s[44:47],s80 offen lds
mm v[40:43],v[212:215],v[180:183],v[40:43]
s_add_u32 s80, s80, 524288
aw a139,v251
mm v[40:43],v[208:211],v[164:167],v[40:43]
rt v[200:201],v241 offset:25472
rt v[202:203],v241 offset:26496
mm v[72:75],v[208:211],v[168:171],v[72:75]
s_mov_b32 s100, s96
s_and_b32 s93, s2, 7
mm v[72:75],v[212:215],v[184:187],v[72:75]
ld v[144:147],v246,s[52:55],s91 offen offset:2048 sc1
mm v[104:107],v[212:215],v[188:191],v[104:107]
s_lshr_b32 s95, s93, 1
s_xor_b32 s97, s95, s93
mm v[104:107],v[208:211],v[172:175],v[104:107]
rt v[212:213],v242 offset:17024
rt v[214:215],v242 offset:18048
mm v[108:111],v[216:219],v[172:175],v[108:111]
s_lshl_b32 s95, s95, 21
s_and_b32 s97, s97, 1
mm v[108:111],v[220:223],v[188:191],v[108:111]
ld v[148:151],v247,s[52:55],s91 offen offset:2048 sc1
mm v[76:79],v[220:223],v[184:187],v[76:79]
s_mov_b32 s18, 53248
s_lshl_b32 s97, s97, 10
mm v[76:79],v[216:219],v[168:171],v[76:79]
rt v[208:209],v242 offset:25472
rt v[210:211],v242 offset:26496
mm v[44:47],v[216:219],v[164:167],v[44:47]
ds_write_b128 v238, v[248:251] offset:5120
mm v[44:47],v[220:223],v[180:183],v[44:47]
st a[132:135],v255,s[48:51],s18 offen offset:0 sc1
mm v[12:15],v[220:223],v[176:179],v[12:15]
wt lgkmcnt(5)
s_add_u32 s95, s95, s97
mm v[12:15],v[216:219],v[160:163],v[12:15]
rt v[220:221],v243 offset:17024
rt v[222:223],v243 offset:18048
mm v[16:19],v[196:199],v[160:163],v[16:19]
ar v224,a224
ar v225,a225
mm v[16:19],v[192:195],v[176:179],v[16:19]
ld v[152:155],v244,s[52:55],s91 offen offset:3072 sc1
mm v[48:51],v[192:195],v[180:183],v[48:51]
ar v226,a226
ar v227,a227
mm v[48:51],v[196:199],v[164:167],v[48:51]
rt v[216:217],v243 offset:25472
rt v[218:219],v243 offset:26496
mm v[80:83],v[196:199],v[168:171],v[80:83]
ar v228,a228
ar v229,a229
mm v[80:83],v[192:195],v[184:187],v[80:83]
ld v[156:159],v245,s[52:55],s91 offen offset:3072 sc1
mm v[112:115],v[192:195],v[188:191],v[112:115]
ar v230,a230
ar v231,a231
mm v[112:115],v[196:199],v[172:175],v[112:115]
cv v248,v224,v225
cv v249,v226,v227
mm v[116:119],v[204:207],v[172:175],v[116:119]
wt lgkmcnt(0)
s_barrier
mm v[116:119],v[200:203],v[188:191],v[116:119]
ld a[120:123],v246,s[52:55],s91 offen offset:3072 sc1
mm v[84:87],v[200:203],v[184:187],v[84:87]
rt v[192:193],v240 offset:33792
rt v[194:195],v240 offset:34816
mm v[84:87],v[204:207],v[168:171],v[84:87]
rt v[196:197],v240 offset:42240
rt v[198:199],v240 offset:43264
mm v[52:55],v[204:207],v[164:167],v[52:55]
)ASM" R"ASM(s_add_u32 m0, s83, 16896
cv v250,v228,v229
mm v[52:55],v[200:203],v[180:183],v[52:55]
ld v232,s[44:47],s80 offen lds
mm v[20:23],v[200:203],v[176:179],v[20:23]
cv v251,v230,v231
aw a140,v248
mm v[20:23],v[204:207],v[160:163],v[20:23]
rt v[200:201],v241 offset:33792
rt v[202:203],v241 offset:34816
mm v[24:27],v[212:215],v[160:163],v[24:27]
aw a141,v249
aw a142,v250
mm v[24:27],v[208:211],v[176:179],v[24:27]
ld a[124:127],v247,s[52:55],s91 offen offset:3072 sc1
mm v[56:59],v[208:211],v[180:183],v[56:59]
s_add_u32 s91, s91, 4096
aw a143,v251
mm v[56:59],v[212:215],v[164:167],v[56:59]
rt v[204:205],v241 offset:42240
rt v[206:207],v241 offset:43264
mm v[88:91],v[212:215],v[168:171],v[88:91]
s_add_u32 m0, s83, 17920
s_mul_i32 s97, s93, 896
mm v[88:91],v[208:211],v[184:187],v[88:91]
ld v233,s[44:47],s80 offen lds
mm v[120:123],v[208:211],v[188:191],v[120:123]
s_cmp_eq_u32 s3, 0
s_cselect_b32 s95, s95, s97
s_cselect_b64 s[36:37], s[28:29], s[30:31]
s_cselect_b32 s21, 2, 7
s_cselect_b32 s93, s93, 0
mm v[120:123],v[212:215],v[172:175],v[120:123]
rt v[208:209],v242 offset:33792
rt v[210:211],v242 offset:34816
mm v[124:127],v[220:223],v[172:175],v[124:127]
s_cmp_eq_u32 s3, 2
s_cselect_b64 s[36:37], s[32:33], s[36:37]
mm v[124:127],v[216:219],v[188:191],v[124:127]
s_lshl_b32 s97, s97, 10
s_mov_b32 s92, 672
mm v[92:95],v[216:219],v[184:187],v[92:95]
s_mov_b32 s90, 352
mm v[92:95],v[220:223],v[168:171],v[92:95]
rt v[212:213],v242 offset:42240
rt v[214:215],v242 offset:43264
mm v[60:63],v[220:223],v[164:167],v[60:63]
ds_write_b128 v238, v[248:251] offset:6144
mm v[60:63],v[216:219],v[180:183],v[60:63]
st a[136:139],v255,s[48:51],s18 offen offset:1024 sc1
mm v[28:31],v[216:219],v[176:179],v[28:31]
wt vmcnt(14) lgkmcnt(5)
mm v[28:31],v[220:223],v[160:163],v[28:31]
rt v[216:217],v243 offset:33792
rt v[218:219],v243 offset:34816
mm v[0:3],v[192:195],a[96:99],v[0:3]
ar v224,a144
ar v225,a145
mm v[0:3],v[196:199],a[112:115],v[0:3]
ld v[160:163],v244,s[52:55],s91 offen offset:0 sc1
mm v[32:35],v[196:199],a[116:119],v[32:35]
ar v226,a146
ar v227,a147
mm v[32:35],v[192:195],a[100:103],v[32:35]
rt v[220:221],v243 offset:42240
rt v[222:223],v243 offset:43264
mm v[64:67],v[192:195],a[104:107],v[64:67]
ar v228,a148
ar v229,a149
mm v[64:67],v[196:199],v[140:143],v[64:67]
ld v[164:167],v245,s[52:55],s91 offen offset:0 sc1
mm v[96:99],v[196:199],v[128:131],v[96:99]
ar v230,a150
ar v231,a151
mm v[96:99],v[192:195],a[108:111],v[96:99]
rt v[196:197],v240 offset:33920
rt v[198:199],v240 offset:34944
mm v[100:103],v[200:203],a[108:111],v[100:103]
s_add_u32 m0, s83, 19072
cv v248,v224,v225
mm v[100:103],v[204:207],v[128:131],v[100:103]
ld v234,s[44:47],s80 offen lds
mm v[68:71],v[204:207],v[140:143],v[68:71]
cv v249,v226,v227
cv v250,v228,v229
mm v[68:71],v[200:203],a[104:107],v[68:71]
rt v[192:193],v240 offset:42368
rt v[194:195],v240 offset:43392
mm v[36:39],v[200:203],a[100:103],v[36:39]
cv v251,v230,v231
aw a144,v248
mm v[36:39],v[204:207],a[116:119],v[36:39]
ld v[168:171],v246,s[52:55],s91 offen offset:0 sc1
mm v[4:7],v[204:207],a[112:115],v[4:7]
wt lgkmcnt(4)
aw a145,v249
mm v[4:7],v[200:203],a[96:99],v[4:7]
rt v[204:205],v241 offset:33920
rt v[206:207],v241 offset:34944
mm v[8:11],v[208:211],a[96:99],v[8:11]
)ASM" R"ASM(s_add_u32 m0, s83, 20096
aw a146,v250
mm v[8:11],v[212:215],a[112:115],v[8:11]
ld v235,s[44:47],s80 offen lds
mm v[40:43],v[212:215],a[116:119],v[40:43]
s_add_u32 s80, s80, 524288
aw a147,v251
mm v[40:43],v[208:211],a[100:103],v[40:43]
rt v[200:201],v241 offset:42368
rt v[202:203],v241 offset:43392
mm v[72:75],v[208:211],a[104:107],v[72:75]
s_cmp_eq_u32 s3, 3
s_cselect_b32 s95, s97, s95
s_cselect_b64 s[36:37], s[34:35], s[36:37]
s_cselect_b32 s21, 1, s21
s_cselect_b32 s92, 544, s92
mm v[72:75],v[212:215],v[140:143],v[72:75]
ld v[172:175],v247,s[52:55],s91 offen offset:0 sc1
mm v[104:107],v[212:215],v[128:131],v[104:107]
s_lshl_b32 s21, s21, 10
s_and_b32 s7, s93, 1
mm v[104:107],v[208:211],a[108:111],v[104:107]
rt v[212:213],v242 offset:33920
rt v[214:215],v242 offset:34944
mm v[108:111],v[216:219],a[108:111],v[108:111]
s_cmp_eq_u32 s75, 1
s_cselect_b32 s97, s90, s92
s_cselect_b32 s92, s92, s90
mm v[108:111],v[220:223],v[128:131],v[108:111]
.La5_LW1_10:
s_cbranch_execz .La5_LW1_14
s_cmp_eq_u32 s75, s7
s_cselect_b32 s92, s92, 0
mm v[76:79],v[220:223],v[140:143],v[76:79]
s_mul_i32 s92, s92, s21
s_add_u32 s95, s95, s92
mm v[76:79],v[216:219],a[104:107],v[76:79]
rt v[208:209],v242 offset:42368
rt v[210:211],v242 offset:43392
mm v[44:47],v[216:219],a[100:103],v[44:47]
ds_write_b128 v238, v[248:251] offset:7168
mm v[44:47],v[220:223],a[116:119],v[44:47]
st a[140:143],v255,s[48:51],s18 offen offset:2048 sc1
mm v[12:15],v[220:223],a[112:115],v[12:15]
wt lgkmcnt(5)
s_mul_i32 s97, s97, s21
mm v[12:15],v[216:219],a[96:99],v[12:15]
rt v[220:221],v243 offset:33920
rt v[222:223],v243 offset:34944
mm v[16:19],v[196:199],a[96:99],v[16:19]
ar v224,a232
ar v225,a233
mm v[16:19],v[192:195],a[112:115],v[16:19]
ld v[176:179],v244,s[52:55],s91 offen offset:1024 sc1
mm v[48:51],v[192:195],a[116:119],v[48:51]
ar v226,a234
ar v227,a235
mm v[48:51],v[196:199],a[100:103],v[48:51]
rt v[216:217],v243 offset:42368
rt v[218:219],v243 offset:43392
mm v[80:83],v[196:199],a[104:107],v[80:83]
ar v228,a236
ar v229,a237
mm v[80:83],v[192:195],v[140:143],v[80:83]
ld v[180:183],v245,s[52:55],s91 offen offset:1024 sc1
mm v[112:115],v[192:195],v[128:131],v[112:115]
ar v230,a238
ar v231,a239
mm v[112:115],v[196:199],a[108:111],v[112:115]
cv v248,v224,v225
cv v249,v226,v227
mm v[116:119],v[204:207],a[108:111],v[116:119]
wt lgkmcnt(0)
s_barrier
mm v[116:119],v[200:203],v[128:131],v[116:119]
ld v[184:187],v246,s[52:55],s91 offen offset:1024 sc1
mm v[84:87],v[200:203],v[140:143],v[84:87]
rt v[192:193],v240 offset:50688
rt v[194:195],v240 offset:51712
mm v[84:87],v[204:207],a[104:107],v[84:87]
rt v[196:197],v240 offset:59136
rt v[198:199],v240 offset:60160
mm v[52:55],v[204:207],a[100:103],v[52:55]
s_add_u32 m0, s83, 33792
cv v250,v228,v229
mm v[52:55],v[200:203],a[116:119],v[52:55]
ld v232,s[44:47],s80 offen lds
mm v[20:23],v[200:203],a[112:115],v[20:23]
cv v251,v230,v231
aw a148,v248
mm v[20:23],v[204:207],a[96:99],v[20:23]
rt v[200:201],v241 offset:50688
rt v[202:203],v241 offset:51712
mm v[24:27],v[212:215],a[96:99],v[24:27]
aw a149,v249
aw a150,v250
mm v[24:27],v[208:211],a[112:115],v[24:27]
ld v[188:191],v247,s[52:55],s91 offen offset:1024 sc1
mm v[56:59],v[208:211],a[116:119],v[56:59]
aw a151,v251
)ASM" R"ASM(s_mul_i32 s90, s97, s7
mm v[56:59],v[212:215],a[100:103],v[56:59]
rt v[204:205],v241 offset:59136
rt v[206:207],v241 offset:60160
mm v[88:91],v[212:215],a[104:107],v[88:91]
s_add_u32 m0, s83, 34816
s_sub_u32 s97, s97, s21
mm v[88:91],v[208:211],v[140:143],v[88:91]
ld v233,s[44:47],s80 offen lds
mm v[120:123],v[208:211],v[128:131],v[120:123]
s_add_u32 s97, s97, 896
mm v[120:123],v[212:215],a[108:111],v[120:123]
rt v[208:209],v242 offset:50688
rt v[210:211],v242 offset:51712
mm v[124:127],v[220:223],a[108:111],v[124:127]
s_add_u32 s36, s36, s95
s_addc_u32 s37, s37, 0
mm v[124:127],v[216:219],v[128:131],v[124:127]
s_and_b32 s37, s37, 0xffff
s_mov_b32 s38, s97
mm v[92:95],v[216:219],v[140:143],v[92:95]
s_mov_b32 s39, 0x20000
s_lshl_b32 s93, s96, 6
mm v[92:95],v[220:223],a[104:107],v[92:95]
rt v[212:213],v242 offset:59136
rt v[214:215],v242 offset:60160
mm v[60:63],v[220:223],a[100:103],v[60:63]
ds_write_b128 v238, v[248:251] offset:32768
mm v[60:63],v[216:219],a[116:119],v[60:63]
s_mul_i32 s95, s94, 7332928
mm v[28:31],v[216:219],a[112:115],v[28:31]
wt vmcnt(15) lgkmcnt(5)
mm v[28:31],v[220:223],a[96:99],v[28:31]
rt v[216:217],v243 offset:50688
rt v[218:219],v243 offset:51712
mm v[0:3],v[192:195],v[132:135],v[0:3]
ar v224,a152
ar v225,a153
mm v[0:3],v[196:199],v[152:155],v[0:3]
ld v[128:131],v244,s[52:55],s91 offen offset:2048 sc1
mm v[32:35],v[196:199],v[156:159],v[32:35]
ar v226,a154
ar v227,a155
mm v[32:35],v[192:195],v[136:139],v[32:35]
rt v[220:221],v243 offset:59136
rt v[222:223],v243 offset:60160
mm v[64:67],v[192:195],v[144:147],v[64:67]
ar v228,a156
ar v229,a157
mm v[64:67],v[196:199],a[120:123],v[64:67]
ld v[140:143],v245,s[52:55],s91 offen offset:2048 sc1
mm v[96:99],v[196:199],a[124:127],v[96:99]
ar v230,a158
ar v231,a159
mm v[96:99],v[192:195],v[148:151],v[96:99]
rt v[196:197],v240 offset:50816
rt v[198:199],v240 offset:51840
mm v[100:103],v[200:203],v[148:151],v[100:103]
s_add_u32 m0, s83, 35968
cv v248,v224,v225
mm v[100:103],v[204:207],a[124:127],v[100:103]
ld v234,s[44:47],s80 offen lds
mm v[68:71],v[204:207],a[120:123],v[68:71]
cv v249,v226,v227
cv v250,v228,v229
mm v[68:71],v[200:203],v[144:147],v[68:71]
rt v[192:193],v240 offset:59264
rt v[194:195],v240 offset:60288
mm v[36:39],v[200:203],v[136:139],v[36:39]
cv v251,v230,v231
aw a152,v248
mm v[36:39],v[204:207],v[156:159],v[36:39]
ld a[96:99],v246,s[52:55],s91 offen offset:2048 sc1
mm v[4:7],v[204:207],v[152:155],v[4:7]
wt lgkmcnt(4)
aw a153,v249
mm v[4:7],v[200:203],v[132:135],v[4:7]
rt v[204:205],v241 offset:50816
rt v[206:207],v241 offset:51840
mm v[8:11],v[208:211],v[132:135],v[8:11]
s_add_u32 m0, s83, 36992
aw a154,v250
mm v[8:11],v[212:215],v[152:155],v[8:11]
ld v235,s[44:47],s80 offen lds
mm v[40:43],v[212:215],v[156:159],v[40:43]
s_add_u32 s80, s80, 524288
aw a155,v251
mm v[40:43],v[208:211],v[136:139],v[40:43]
rt v[200:201],v241 offset:59264
rt v[202:203],v241 offset:60288
mm v[72:75],v[208:211],v[144:147],v[72:75]
s_cmp_eq_u32 s77, 0
s_cselect_b64 s[40:41], s[30:31], s[32:33]
mm v[72:75],v[212:215],a[120:123],v[72:75]
ld a[100:103],v247,s[52:55],s91 offen offset:2048 sc1
mm v[104:107],v[212:215],a[124:127],v[104:107]
s_add_u32 s40, s40, s93
s_addc_u32 s41, s41, 0
mm v[104:107],v[208:211],v[148:151],v[104:107]
rt v[212:213],v242 offset:50816
)ASM" R"ASM(rt v[214:215],v242 offset:51840
mm v[108:111],v[216:219],v[148:151],v[108:111]
s_and_b32 s41, s41, 0xffff
s_mov_b32 s42, s95
mm v[108:111],v[220:223],a[124:127],v[108:111]
s_mov_b32 s43, 0x20000
s_lshr_b32 s92, s72, 1
mm v[76:79],v[220:223],a[120:123],v[76:79]
s_and_b32 s92, s92, 4
s_xor_b32 s92, s92, s72
mm v[76:79],v[216:219],v[144:147],v[76:79]
rt v[208:209],v242 offset:59264
rt v[210:211],v242 offset:60288
mm v[44:47],v[216:219],v[136:139],v[44:47]
ds_write_b128 v238, v[248:251] offset:33792
mm v[44:47],v[220:223],v[156:159],v[44:47]
st a[144:147],v255,s[48:51],s18 offen offset:3072 sc1
mm v[12:15],v[220:223],v[152:155],v[12:15]
wt lgkmcnt(5)
s_xor_b32 s92, s92, 1
mm v[12:15],v[216:219],v[132:135],v[12:15]
rt v[220:221],v243 offset:50816
rt v[222:223],v243 offset:51840
mm v[16:19],v[196:199],v[132:135],v[16:19]
ar v224,a208
ar v225,a209
mm v[16:19],v[192:195],v[152:155],v[16:19]
ld a[104:107],v244,s[52:55],s91 offen offset:3072 sc1
mm v[48:51],v[192:195],v[156:159],v[48:51]
ar v226,a210
ar v227,a211
mm v[48:51],v[196:199],v[136:139],v[48:51]
rt v[216:217],v243 offset:59264
rt v[218:219],v243 offset:60288
mm v[80:83],v[196:199],v[144:147],v[80:83]
ar v228,a212
ar v229,a213
mm v[80:83],v[192:195],a[120:123],v[80:83]
ld a[108:111],v245,s[52:55],s91 offen offset:3072 sc1
mm v[112:115],v[192:195],a[124:127],v[112:115]
ar v230,a214
ar v231,a215
mm v[112:115],v[196:199],v[148:151],v[112:115]
cv v248,v224,v225
cv v249,v226,v227
mm v[116:119],v[204:207],v[148:151],v[116:119]
wt lgkmcnt(0)
s_barrier
mm v[116:119],v[200:203],a[124:127],v[116:119]
ld a[112:115],v246,s[52:55],s91 offen offset:3072 sc1
mm v[84:87],v[200:203],a[120:123],v[84:87]
rt v[192:193],v240 offset:0
rt v[194:195],v240 offset:1024
mm v[84:87],v[204:207],v[144:147],v[84:87]
rt v[196:197],v240 offset:8448
rt v[198:199],v240 offset:9472
mm v[52:55],v[204:207],v[136:139],v[52:55]
s_add_u32 m0, s83, 50688
cv v250,v228,v229
mm v[52:55],v[200:203],v[156:159],v[52:55]
ld v232,s[44:47],s80 offen lds
mm v[20:23],v[200:203],v[152:155],v[20:23]
cv v251,v230,v231
aw a156,v248
mm v[20:23],v[204:207],v[132:135],v[20:23]
rt v[200:201],v241 offset:0
rt v[202:203],v241 offset:1024
mm v[24:27],v[212:215],v[132:135],v[24:27]
aw a157,v249
aw a158,v250
mm v[24:27],v[208:211],v[152:155],v[24:27]
ld a[116:119],v247,s[52:55],s91 offen offset:3072 sc1
mm v[56:59],v[208:211],v[156:159],v[56:59]
s_add_u32 s91, s91, 4096
aw a159,v251
mm v[56:59],v[212:215],v[136:139],v[56:59]
rt v[204:205],v241 offset:8448
rt v[206:207],v241 offset:9472
mm v[88:91],v[212:215],v[144:147],v[88:91]
s_add_u32 m0, s83, 51712
s_and_b32 s101, s92, 7
mm v[88:91],v[208:211],a[120:123],v[88:91]
ld v233,s[44:47],s80 offen lds
mm v[120:123],v[208:211],a[124:127],v[120:123]
s_lshl_b32 s92, s101, 8
s_sub_u32 s93, 8388608, s92
mm v[120:123],v[212:215],v[148:151],v[120:123]
rt v[208:209],v242 offset:0
rt v[210:211],v242 offset:1024
mm v[124:127],v[220:223],v[148:151],v[124:127]
s_add_u32 s56, s28, s92
s_addc_u32 s57, s29, 0
mm v[124:127],v[216:219],a[124:127],v[124:127]
s_and_b32 s57, s57, 0xffff
s_mov_b32 s58, s93
mm v[92:95],v[216:219],a[120:123],v[92:95]
s_mov_b32 s18, 32768
s_mov_b32 s59, 0x20000
mm v[92:95],v[220:223],v[144:147],v[92:95]
rt v[212:213],v242 offset:8448
)ASM" R"ASM(rt v[214:215],v242 offset:9472
mm v[60:63],v[220:223],v[136:139],v[60:63]
ds_write_b128 v238, v[248:251] offset:34816
mm v[60:63],v[216:219],v[156:159],v[60:63]
st a[148:151],v255,s[48:51],s18 offen offset:0 sc1
mm v[28:31],v[216:219],v[152:155],v[28:31]
wt vmcnt(15) lgkmcnt(5)
s_lshl_b32 s93, s96, 16
mm v[28:31],v[220:223],v[132:135],v[28:31]
rt v[216:217],v243 offset:0
rt v[218:219],v243 offset:1024
mm v[0:3],v[192:195],v[160:163],v[0:3]
ar v224,a160
ar v225,a161
mm v[0:3],v[196:199],v[176:179],v[0:3]
ld v[132:135],v244,s[52:55],s91 offen offset:0 sc1
mm v[32:35],v[196:199],v[180:183],v[32:35]
ar v226,a162
ar v227,a163
mm v[32:35],v[192:195],v[164:167],v[32:35]
rt v[220:221],v243 offset:8448
rt v[222:223],v243 offset:9472
mm v[64:67],v[192:195],v[168:171],v[64:67]
ar v228,a164
ar v229,a165
mm v[64:67],v[196:199],v[184:187],v[64:67]
ld v[136:139],v245,s[52:55],s91 offen offset:0 sc1
mm v[96:99],v[196:199],v[188:191],v[96:99]
ar v230,a166
ar v231,a167
mm v[96:99],v[192:195],v[172:175],v[96:99]
rt v[196:197],v240 offset:128
rt v[198:199],v240 offset:1152
mm v[100:103],v[200:203],v[172:175],v[100:103]
s_add_u32 m0, s83, 52864
cv v248,v224,v225
mm v[100:103],v[204:207],v[188:191],v[100:103]
ld v234,s[44:47],s80 offen lds
mm v[68:71],v[204:207],v[184:187],v[68:71]
cv v249,v226,v227
cv v250,v228,v229
mm v[68:71],v[200:203],v[168:171],v[68:71]
rt v[192:193],v240 offset:8576
rt v[194:195],v240 offset:9600
mm v[36:39],v[200:203],v[164:167],v[36:39]
cv v251,v230,v231
aw a160,v248
mm v[36:39],v[204:207],v[180:183],v[36:39]
ld v[144:147],v246,s[52:55],s91 offen offset:0 sc1
mm v[4:7],v[204:207],v[176:179],v[4:7]
wt lgkmcnt(4)
aw a161,v249
mm v[4:7],v[200:203],v[160:163],v[4:7]
rt v[204:205],v241 offset:128
rt v[206:207],v241 offset:1152
mm v[8:11],v[208:211],v[160:163],v[8:11]
s_add_u32 m0, s83, 53888
aw a162,v250
mm v[8:11],v[212:215],v[176:179],v[8:11]
ld v235,s[44:47],s80 offen lds
mm v[40:43],v[212:215],v[180:183],v[40:43]
s_add_u32 s80, s80, 524288
aw a163,v251
mm v[40:43],v[208:211],v[164:167],v[40:43]
rt v[200:201],v241 offset:8576
rt v[202:203],v241 offset:9600
mm v[72:75],v[208:211],v[168:171],v[72:75]
s_lshl_b32 s95, s94, 16
mm v[72:75],v[212:215],v[184:187],v[72:75]
ld v[148:151],v247,s[52:55],s91 offen offset:0 sc1
mm v[104:107],v[212:215],v[188:191],v[104:107]
s_add_u32 s28, s34, s93
s_addc_u32 s29, s35, 0
mm v[104:107],v[208:211],v[172:175],v[104:107]
rt v[212:213],v242 offset:128
rt v[214:215],v242 offset:1152
mm v[108:111],v[216:219],v[172:175],v[108:111]
s_and_b32 s29, s29, 0xffff
s_mov_b32 s30, s95
mm v[108:111],v[220:223],v[188:191],v[108:111]
s_mov_b32 s31, 0x20000
s_lshl_b32 s93, s101, 12
mm v[76:79],v[220:223],v[184:187],v[76:79]
s_add_u32 s92, s93, 71303168
s_sub_u32 s93, 16777216, s93
mm v[76:79],v[216:219],v[168:171],v[76:79]
rt v[208:209],v242 offset:8576
rt v[210:211],v242 offset:9600
mm v[44:47],v[216:219],v[164:167],v[44:47]
ds_write_b128 v238, v[248:251] offset:35840
mm v[44:47],v[220:223],v[180:183],v[44:47]
st a[152:155],v255,s[48:51],s18 offen offset:1024 sc1
mm v[12:15],v[220:223],v[176:179],v[12:15]
wt lgkmcnt(5)
mm v[12:15],v[216:219],v[160:163],v[12:15]
rt v[220:221],v243 offset:128
rt v[222:223],v243 offset:1152
mm v[16:19],v[196:199],v[160:163],v[16:19]
ar v224,a216
ar v225,a217
)ASM" R"ASM(mm v[16:19],v[192:195],v[176:179],v[16:19]
ld v[152:155],v244,s[52:55],s91 offen offset:1024 sc1
mm v[48:51],v[192:195],v[180:183],v[48:51]
ar v226,a218
ar v227,a219
mm v[48:51],v[196:199],v[164:167],v[48:51]
rt v[216:217],v243 offset:8576
rt v[218:219],v243 offset:9600
mm v[80:83],v[196:199],v[168:171],v[80:83]
ar v228,a220
ar v229,a221
mm v[80:83],v[192:195],v[184:187],v[80:83]
ld v[156:159],v245,s[52:55],s91 offen offset:1024 sc1
mm v[112:115],v[192:195],v[188:191],v[112:115]
ar v230,a222
ar v231,a223
mm v[112:115],v[196:199],v[172:175],v[112:115]
cv v248,v224,v225
cv v249,v226,v227
mm v[116:119],v[204:207],v[172:175],v[116:119]
wt lgkmcnt(0)
s_barrier
mm v[116:119],v[200:203],v[188:191],v[116:119]
ld a[120:123],v246,s[52:55],s91 offen offset:1024 sc1
mm v[84:87],v[200:203],v[184:187],v[84:87]
rt v[192:193],v240 offset:16896
rt v[194:195],v240 offset:17920
mm v[84:87],v[204:207],v[168:171],v[84:87]
rt v[196:197],v240 offset:25344
rt v[198:199],v240 offset:26368
mm v[52:55],v[204:207],v[164:167],v[52:55]
s_add_u32 m0, s83, 0
cv v250,v228,v229
mm v[52:55],v[200:203],v[180:183],v[52:55]
ld v232,s[44:47],s80 offen lds
mm v[20:23],v[200:203],v[176:179],v[20:23]
cv v251,v230,v231
aw a164,v248
mm v[20:23],v[204:207],v[160:163],v[20:23]
rt v[200:201],v241 offset:16896
rt v[202:203],v241 offset:17920
mm v[24:27],v[212:215],v[160:163],v[24:27]
aw a165,v249
aw a166,v250
mm v[24:27],v[208:211],v[176:179],v[24:27]
ld a[124:127],v247,s[52:55],s91 offen offset:1024 sc1
mm v[56:59],v[208:211],v[180:183],v[56:59]
aw a167,v251
mm v[56:59],v[212:215],v[164:167],v[56:59]
rt v[204:205],v241 offset:25344
rt v[206:207],v241 offset:26368
mm v[88:91],v[212:215],v[168:171],v[88:91]
s_add_u32 m0, s83, 1024
mm v[88:91],v[208:211],v[184:187],v[88:91]
ld v233,s[44:47],s80 offen lds
mm v[120:123],v[208:211],v[188:191],v[120:123]
s_add_u32 s32, s26, s92
s_addc_u32 s33, s27, 0
mm v[120:123],v[212:215],v[172:175],v[120:123]
rt v[208:209],v242 offset:16896
rt v[210:211],v242 offset:17920
mm v[124:127],v[220:223],v[172:175],v[124:127]
s_and_b32 s33, s33, 0xffff
s_mov_b32 s34, s93
mm v[124:127],v[216:219],v[188:191],v[124:127]
s_mov_b32 s35, 0x20000
s_lshl2_add_u32 s92, s72, s74
mm v[92:95],v[216:219],v[184:187],v[92:95]
s_lshl2_add_u32 s92, s92, s3
s_lshl_b32 s88, s92, 8
mm v[92:95],v[220:223],v[168:171],v[92:95]
rt v[212:213],v242 offset:25344
rt v[214:215],v242 offset:26368
mm v[60:63],v[220:223],v[164:167],v[60:63]
ds_write_b128 v238, v[248:251] offset:36864
mm v[60:63],v[216:219],v[180:183],v[60:63]
st a[156:159],v255,s[48:51],s18 offen offset:2048 sc1
mm v[28:31],v[216:219],v[176:179],v[28:31]
wt vmcnt(16) lgkmcnt(5)
s_mul_i32 s93, s92, 17408
mm v[28:31],v[220:223],v[160:163],v[28:31]
rt v[216:217],v243 offset:16896
rt v[218:219],v243 offset:17920
mm v[0:3],v[192:195],v[128:131],v[0:3]
ar v224,a168
ar v225,a169
mm v[0:3],v[196:199],a[104:107],v[0:3]
ld v[160:163],v244,s[52:55],s91 offen offset:2048 sc1
mm v[32:35],v[196:199],a[108:111],v[32:35]
ar v226,a170
ar v227,a171
mm v[32:35],v[192:195],v[140:143],v[32:35]
rt v[220:221],v243 offset:25344
rt v[222:223],v243 offset:26368
mm v[64:67],v[192:195],a[96:99],v[64:67]
ar v228,a172
ar v229,a173
mm v[64:67],v[196:199],a[112:115],v[64:67]
ld v[164:167],v245,s[52:55],s91 offen offset:2048 sc1
mm v[96:99],v[196:199],a[116:119],v[96:99]
)ASM" R"ASM(ar v230,a174
ar v231,a175
mm v[96:99],v[192:195],a[100:103],v[96:99]
rt v[196:197],v240 offset:17024
rt v[198:199],v240 offset:18048
mm v[100:103],v[200:203],a[100:103],v[100:103]
s_add_u32 m0, s83, 2176
cv v248,v224,v225
mm v[100:103],v[204:207],a[116:119],v[100:103]
ld v234,s[44:47],s80 offen lds
mm v[68:71],v[204:207],a[112:115],v[68:71]
cv v249,v226,v227
cv v250,v228,v229
mm v[68:71],v[200:203],a[96:99],v[68:71]
rt v[192:193],v240 offset:25472
rt v[194:195],v240 offset:26496
mm v[36:39],v[200:203],v[140:143],v[36:39]
cv v251,v230,v231
aw a168,v248
mm v[36:39],v[204:207],a[108:111],v[36:39]
ld v[168:171],v246,s[52:55],s91 offen offset:2048 sc1
mm v[4:7],v[204:207],a[104:107],v[4:7]
wt lgkmcnt(4)
aw a169,v249
mm v[4:7],v[200:203],v[128:131],v[4:7]
rt v[204:205],v241 offset:17024
rt v[206:207],v241 offset:18048
mm v[8:11],v[208:211],v[128:131],v[8:11]
s_add_u32 m0, s83, 3200
aw a170,v250
mm v[8:11],v[212:215],a[104:107],v[8:11]
ld v235,s[44:47],s80 offen lds
mm v[40:43],v[212:215],a[108:111],v[40:43]
s_add_u32 s80, s80, 524288
aw a171,v251
mm v[40:43],v[208:211],v[140:143],v[40:43]
rt v[200:201],v241 offset:25472
rt v[202:203],v241 offset:26496
mm v[72:75],v[208:211],a[96:99],v[72:75]
s_lshl_b32 s92, s92, 14
s_add_u32 s92, s92, 4194304
mm v[72:75],v[212:215],a[112:115],v[72:75]
ld v[172:175],v247,s[52:55],s91 offen offset:2048 sc1
mm v[104:107],v[212:215],a[116:119],v[104:107]
s_add_u32 s86, s26, s92
s_addc_u32 s87, s27, 0
mm v[104:107],v[208:211],a[100:103],v[104:107]
rt v[212:213],v242 offset:17024
rt v[214:215],v242 offset:18048
mm v[108:111],v[216:219],a[100:103],v[108:111]
s_and_b32 s87, s87, 0xffff
s_add_u32 s93, s93, 150994944
mm v[108:111],v[220:223],a[116:119],v[108:111]
s_add_u32 s68, s26, s93
s_addc_u32 s69, s27, 0
mm v[76:79],v[220:223],a[112:115],v[76:79]
s_and_b32 s69, s69, 0xffff
s_mov_b32 s70, 17408
mm v[76:79],v[216:219],a[96:99],v[76:79]
rt v[208:209],v242 offset:25472
rt v[210:211],v242 offset:26496
mm v[44:47],v[216:219],v[140:143],v[44:47]
ds_write_b128 v238, v[248:251] offset:37888
mm v[44:47],v[220:223],a[108:111],v[44:47]
st a[160:163],v255,s[48:51],s18 offen offset:3072 sc1
mm v[12:15],v[220:223],a[104:107],v[12:15]
wt lgkmcnt(5)
s_mov_b32 s96, 153158689
mm v[12:15],v[216:219],v[128:131],v[12:15]
rt v[220:221],v243 offset:17024
rt v[222:223],v243 offset:18048
mm v[16:19],v[196:199],v[128:131],v[16:19]
.La5_LW1_11:
s_cbranch_execz .La5_LW1_15
ar v224,a192
ar v225,a193
mm v[16:19],v[192:195],a[104:107],v[16:19]
ld v[176:179],v244,s[52:55],s91 offen offset:3072 sc1
mm v[48:51],v[192:195],a[108:111],v[48:51]
ar v226,a194
ar v227,a195
mm v[48:51],v[196:199],v[140:143],v[48:51]
rt v[216:217],v243 offset:25472
rt v[218:219],v243 offset:26496
mm v[80:83],v[196:199],a[96:99],v[80:83]
ar v228,a196
ar v229,a197
mm v[80:83],v[192:195],a[112:115],v[80:83]
ld v[180:183],v245,s[52:55],s91 offen offset:3072 sc1
mm v[112:115],v[192:195],a[116:119],v[112:115]
ar v230,a198
ar v231,a199
mm v[112:115],v[196:199],a[100:103],v[112:115]
cv v248,v224,v225
cv v249,v226,v227
mm v[116:119],v[204:207],a[100:103],v[116:119]
wt lgkmcnt(0)
s_barrier
mm v[116:119],v[200:203],a[116:119],v[116:119]
ld v[184:187],v246,s[52:55],s91 offen offset:3072 sc1
)ASM" R"ASM(mm v[84:87],v[200:203],a[112:115],v[84:87]
rt v[192:193],v240 offset:33792
rt v[194:195],v240 offset:34816
mm v[84:87],v[204:207],a[96:99],v[84:87]
rt v[196:197],v240 offset:42240
rt v[198:199],v240 offset:43264
mm v[52:55],v[204:207],v[140:143],v[52:55]
s_add_u32 m0, s83, 16896
cv v250,v228,v229
mm v[52:55],v[200:203],a[108:111],v[52:55]
ld v232,s[44:47],s80 offen lds
mm v[20:23],v[200:203],a[104:107],v[20:23]
cv v251,v230,v231
aw a172,v248
mm v[20:23],v[204:207],v[128:131],v[20:23]
rt v[200:201],v241 offset:33792
rt v[202:203],v241 offset:34816
mm v[24:27],v[212:215],v[128:131],v[24:27]
aw a173,v249
aw a174,v250
mm v[24:27],v[208:211],a[104:107],v[24:27]
ld v[188:191],v247,s[52:55],s91 offen offset:3072 sc1
mm v[56:59],v[208:211],a[108:111],v[56:59]
s_add_u32 s91, s91, 4096
aw a175,v251
mm v[56:59],v[212:215],v[140:143],v[56:59]
rt v[204:205],v241 offset:42240
rt v[206:207],v241 offset:43264
mm v[88:91],v[212:215],a[96:99],v[88:91]
s_add_u32 m0, s83, 17920
s_mov_b32 s97, 371265825
mm v[88:91],v[208:211],a[112:115],v[88:91]
ld v233,s[44:47],s80 offen lds
mm v[120:123],v[208:211],a[116:119],v[120:123]
s_lshl_b32 s95, s73, 3
s_lshr_b64 s[96:97], s[96:97], s95
mm v[120:123],v[212:215],a[100:103],v[120:123]
rt v[208:209],v242 offset:33792
rt v[210:211],v242 offset:34816
mm v[124:127],v[220:223],a[100:103],v[124:127]
s_and_b32 s82, s96, 255
s_lshl_b64 s[98:99], 1, s82
mm v[124:127],v[216:219],a[116:119],v[124:127]
s_lshl_b32 s96, s75, 22
s_or_b32 s95, s98, s96
mm v[92:95],v[216:219],a[112:115],v[92:95]
s_cmp_eq_u32 s73, 1
s_cselect_b32 s96, 12, 4
s_cselect_b32 s79, 0, 0xff000000
mm v[92:95],v[220:223],a[96:99],v[92:95]
rt v[212:213],v242 offset:42240
rt v[214:215],v242 offset:43264
mm v[60:63],v[220:223],v[140:143],v[60:63]
ds_write_b128 v238, v[248:251] offset:38912
mm v[60:63],v[216:219],a[108:111],v[60:63]
s_or_b32 s98, s95, s96
mm v[28:31],v[216:219],a[104:107],v[28:31]
wt vmcnt(15) lgkmcnt(5)
mm v[28:31],v[220:223],v[128:131],v[28:31]
rt v[216:217],v243 offset:33792
rt v[218:219],v243 offset:34816
mm v[0:3],v[192:195],v[132:135],v[0:3]
ar v224,a176
ar v225,a177
mm v[0:3],v[196:199],v[152:155],v[0:3]
ld v[128:131],v244,s[52:55],s91 offen offset:0 sc1
mm v[32:35],v[196:199],v[156:159],v[32:35]
ar v226,a178
ar v227,a179
mm v[32:35],v[192:195],v[136:139],v[32:35]
rt v[220:221],v243 offset:42240
rt v[222:223],v243 offset:43264
mm v[64:67],v[192:195],v[144:147],v[64:67]
ar v228,a180
ar v229,a181
mm v[64:67],v[196:199],a[120:123],v[64:67]
ld v[140:143],v245,s[52:55],s91 offen offset:0 sc1
mm v[96:99],v[196:199],a[124:127],v[96:99]
ar v230,a182
ar v231,a183
mm v[96:99],v[192:195],v[148:151],v[96:99]
rt v[196:197],v240 offset:33920
rt v[198:199],v240 offset:34944
mm v[100:103],v[200:203],v[148:151],v[100:103]
s_add_u32 m0, s83, 19072
cv v248,v224,v225
mm v[100:103],v[204:207],a[124:127],v[100:103]
ld v234,s[44:47],s80 offen lds
mm v[68:71],v[204:207],a[120:123],v[68:71]
cv v249,v226,v227
cv v250,v228,v229
mm v[68:71],v[200:203],v[144:147],v[68:71]
rt v[192:193],v240 offset:42368
rt v[194:195],v240 offset:43392
mm v[36:39],v[200:203],v[136:139],v[36:39]
cv v251,v230,v231
aw a176,v248
mm v[36:39],v[204:207],v[156:159],v[36:39]
ld a[96:99],v246,s[52:55],s91 offen offset:0 sc1
mm v[4:7],v[204:207],v[152:155],v[4:7]
)ASM" R"ASM(wt lgkmcnt(4)
aw a177,v249
mm v[4:7],v[200:203],v[132:135],v[4:7]
rt v[204:205],v241 offset:33920
rt v[206:207],v241 offset:34944
mm v[8:11],v[208:211],v[132:135],v[8:11]
s_add_u32 m0, s83, 20096
aw a178,v250
mm v[8:11],v[212:215],v[152:155],v[8:11]
ld v235,s[44:47],s80 offen lds
mm v[40:43],v[212:215],v[156:159],v[40:43]
s_add_u32 s80, s80, 524288
aw a179,v251
mm v[40:43],v[208:211],v[136:139],v[40:43]
rt v[200:201],v241 offset:42368
rt v[202:203],v241 offset:43392
mm v[72:75],v[208:211],v[144:147],v[72:75]
s_cmp_eq_u32 s74, 3
s_cselect_b32 s23, 3, 0
s_cselect_b32 s22, 0, s75
mm v[72:75],v[212:215],a[120:123],v[72:75]
ld a[100:103],v247,s[52:55],s91 offen offset:0 sc1
mm v[104:107],v[212:215],a[124:127],v[104:107]
s_lshl_b32 s95, s76, 5
s_add_u32 s95, s95, s3
mm v[104:107],v[208:211],v[148:151],v[104:107]
rt v[212:213],v242 offset:33920
rt v[214:215],v242 offset:34944
mm v[108:111],v[216:219],v[148:151],v[108:111]
s_lshl_b32 s95, s95, 5
s_add_u32 s95, s95, s72
mm v[108:111],v[220:223],a[124:127],v[108:111]
s_lshl_b32 s95, s95, 3
s_add_u32 s6, s95, 1048576
mm v[76:79],v[220:223],a[120:123],v[76:79]
s_mov_b32 s18, 36864
v_and_b32 v239, 0x70, v255
mm v[76:79],v[216:219],v[144:147],v[76:79]
rt v[208:209],v242 offset:42368
rt v[210:211],v242 offset:43392
mm v[44:47],v[216:219],v[136:139],v[44:47]
ds_write_b128 v238, v[248:251] offset:39936
mm v[44:47],v[220:223],v[156:159],v[44:47]
st a[164:167],v255,s[48:51],s18 offen offset:0 sc1
mm v[12:15],v[220:223],v[152:155],v[12:15]
wt lgkmcnt(5)
v_lshlrev_b32 v239, 6, v239
mm v[12:15],v[216:219],v[132:135],v[12:15]
rt v[220:221],v243 offset:33920
rt v[222:223],v243 offset:34944
mm v[16:19],v[196:199],v[132:135],v[16:19]
ar v224,a200
ar v225,a201
mm v[16:19],v[192:195],v[152:155],v[16:19]
ld a[104:107],v244,s[52:55],s91 offen offset:1024 sc1
mm v[48:51],v[192:195],v[156:159],v[48:51]
ar v226,a202
ar v227,a203
mm v[48:51],v[196:199],v[136:139],v[48:51]
rt v[216:217],v243 offset:42368
rt v[218:219],v243 offset:43392
mm v[80:83],v[196:199],v[144:147],v[80:83]
ar v228,a204
ar v229,a205
mm v[80:83],v[192:195],a[120:123],v[80:83]
ld a[108:111],v245,s[52:55],s91 offen offset:1024 sc1
mm v[112:115],v[192:195],a[124:127],v[112:115]
ar v230,a206
ar v231,a207
mm v[112:115],v[196:199],v[148:151],v[112:115]
cv v248,v224,v225
cv v249,v226,v227
mm v[116:119],v[204:207],v[148:151],v[116:119]
wt lgkmcnt(0)
s_barrier
mm v[116:119],v[200:203],a[124:127],v[116:119]
ld a[112:115],v246,s[52:55],s91 offen offset:1024 sc1
mm v[84:87],v[200:203],a[120:123],v[84:87]
rt v[192:193],v240 offset:50688
rt v[194:195],v240 offset:51712
mm v[84:87],v[204:207],v[144:147],v[84:87]
rt v[196:197],v240 offset:59136
rt v[198:199],v240 offset:60160
mm v[52:55],v[204:207],v[136:139],v[52:55]
s_add_u32 m0, s83, 33792
cv v250,v228,v229
mm v[52:55],v[200:203],v[156:159],v[52:55]
ld v232,s[44:47],s80 offen lds
mm v[20:23],v[200:203],v[152:155],v[20:23]
cv v251,v230,v231
aw a180,v248
mm v[20:23],v[204:207],v[132:135],v[20:23]
rt v[200:201],v241 offset:50688
rt v[202:203],v241 offset:51712
mm v[24:27],v[212:215],v[132:135],v[24:27]
aw a181,v249
aw a182,v250
mm v[24:27],v[208:211],v[152:155],v[24:27]
ld a[116:119],v247,s[52:55],s91 offen offset:1024 sc1
mm v[56:59],v[208:211],v[156:159],v[56:59]
)ASM" R"ASM(aw a183,v251
s_add_u32 s85, s85, 65536
mm v[56:59],v[212:215],v[136:139],v[56:59]
rt v[204:205],v241 offset:59136
rt v[206:207],v241 offset:60160
mm v[88:91],v[212:215],v[144:147],v[88:91]
s_add_u32 m0, s83, 34816
v_add_u32 v238, s85, v255
mm v[88:91],v[208:211],a[120:123],v[88:91]
ld v233,s[44:47],s80 offen lds
mm v[120:123],v[208:211],a[124:127],v[120:123]
s_mov_b32 s85, 0
aw a231,v239
mm v[120:123],v[212:215],v[148:151],v[120:123]
rt v[208:209],v242 offset:50688
rt v[210:211],v242 offset:51712
mm v[124:127],v[220:223],v[148:151],v[124:127]
s_mul_i32 s95, s75, 0
s_add_u32 s95, s95, 2
mm v[124:127],v[216:219],a[124:127],v[124:127]
s_sub_u32 s96, 4, s95
s_lshr_b32 s97, s2, 4
mm v[92:95],v[216:219],a[120:123],v[92:95]
s_mul_i32 s97, s97, s95
v_lshrrev_b32 v239, 7, v255
mm v[92:95],v[220:223],v[144:147],v[92:95]
rt v[212:213],v242 offset:59136
rt v[214:215],v242 offset:60160
mm v[60:63],v[220:223],v[136:139],v[60:63]
ds_write_b128 v238, v[248:251] offset:0
mm v[60:63],v[216:219],v[156:159],v[60:63]
st a[168:171],v255,s[48:51],s18 offen offset:1024 sc1
mm v[28:31],v[216:219],v[152:155],v[28:31]
wt vmcnt(15) lgkmcnt(5)
v_add_u32 v254, s97, v239
mm v[28:31],v[220:223],v[132:135],v[28:31]
rt v[216:217],v243 offset:50688
rt v[218:219],v243 offset:51712
mm v[0:3],v[192:195],v[160:163],v[0:3]
ar v224,a184
ar v225,a185
mm v[0:3],v[196:199],v[176:179],v[0:3]
ld v[132:135],v244,s[52:55],s91 offen offset:2048 sc1
mm v[32:35],v[196:199],v[180:183],v[32:35]
ar v226,a186
ar v227,a187
mm v[32:35],v[192:195],v[164:167],v[32:35]
rt v[220:221],v243 offset:59136
rt v[222:223],v243 offset:60160
mm v[64:67],v[192:195],v[168:171],v[64:67]
ar v228,a188
ar v229,a189
mm v[64:67],v[196:199],v[184:187],v[64:67]
ld v[136:139],v245,s[52:55],s91 offen offset:2048 sc1
mm v[96:99],v[196:199],v[188:191],v[96:99]
ar v230,a190
ar v231,a191
mm v[96:99],v[192:195],v[172:175],v[96:99]
rt v[196:197],v240 offset:50816
rt v[198:199],v240 offset:51840
mm v[100:103],v[200:203],v[172:175],v[100:103]
s_add_u32 m0, s83, 35968
cv v248,v224,v225
mm v[100:103],v[204:207],v[188:191],v[100:103]
ld v234,s[44:47],s80 offen lds
mm v[68:71],v[204:207],v[184:187],v[68:71]
cv v249,v226,v227
cv v250,v228,v229
mm v[68:71],v[200:203],v[168:171],v[68:71]
rt v[192:193],v240 offset:59264
rt v[194:195],v240 offset:60288
mm v[36:39],v[200:203],v[164:167],v[36:39]
cv v251,v230,v231
aw a184,v248
mm v[36:39],v[204:207],v[180:183],v[36:39]
ld v[144:147],v246,s[52:55],s91 offen offset:2048 sc1
mm v[4:7],v[204:207],v[176:179],v[4:7]
wt lgkmcnt(4)
aw a185,v249
mm v[4:7],v[200:203],v[160:163],v[4:7]
rt v[204:205],v241 offset:50816
rt v[206:207],v241 offset:51840
mm v[8:11],v[208:211],v[160:163],v[8:11]
s_add_u32 m0, s83, 36992
aw a186,v250
mm v[8:11],v[212:215],v[176:179],v[8:11]
ld v235,s[44:47],s80 offen lds
mm v[40:43],v[212:215],v[180:183],v[40:43]
s_add_u32 s80, s80, 524288
aw a187,v251
mm v[40:43],v[208:211],v[164:167],v[40:43]
rt v[200:201],v241 offset:59264
rt v[202:203],v241 offset:60288
mm v[72:75],v[208:211],v[168:171],v[72:75]
v_mul_u32_u24 v254, s21, v254
v_add_u32 v239, s96, v239
mm v[72:75],v[212:215],v[184:187],v[72:75]
ld v[148:151],v247,s[52:55],s91 offen offset:2048 sc1
mm v[104:107],v[212:215],v[188:191],v[104:107]
v_lshrrev_b32 v239, 2, v239
)ASM" R"ASM(v_lshl_add_u32 v254, v239, 28, v254
mm v[104:107],v[208:211],v[172:175],v[104:107]
rt v[212:213],v242 offset:50816
rt v[214:215],v242 offset:51840
mm v[108:111],v[216:219],v[172:175],v[108:111]
v_and_b32 v239, 0x70, v255
v_lshl_add_u32 v254, v239, 3, v254
mm v[108:111],v[220:223],v[188:191],v[108:111]
s_lshl_b32 s96, s7, 5
s_sub_u32 s96, 16, s96
mm v[76:79],v[220:223],v[184:187],v[76:79]
s_mul_i32 s95, s95, s96
s_mul_i32 s21, s21, s95
mm v[76:79],v[216:219],v[168:171],v[76:79]
rt v[208:209],v242 offset:59264
rt v[210:211],v242 offset:60288
mm v[44:47],v[216:219],v[164:167],v[44:47]
ds_write_b128 v238, v[248:251] offset:1024
mm v[44:47],v[220:223],v[180:183],v[44:47]
st a[172:175],v255,s[48:51],s18 offen offset:2048 sc1
mm v[12:15],v[220:223],v[176:179],v[12:15]
wt lgkmcnt(5)
v_lshrrev_b32 v236, 8, v255
mm v[12:15],v[216:219],v[160:163],v[12:15]
rt v[220:221],v243 offset:50816
rt v[222:223],v243 offset:51840
mm v[16:19],v[196:199],v[160:163],v[16:19]
ar v224,a64
ar v225,a65
mm v[16:19],v[192:195],v[176:179],v[16:19]
ld v[152:155],v244,s[52:55],s91 offen offset:3072 sc1
mm v[48:51],v[192:195],v[180:183],v[48:51]
ar v226,a66
ar v227,a67
mm v[48:51],v[196:199],v[164:167],v[48:51]
rt v[216:217],v243 offset:59264
rt v[218:219],v243 offset:60288
mm v[80:83],v[196:199],v[168:171],v[80:83]
ar v228,a68
ar v229,a69
mm v[80:83],v[192:195],v[184:187],v[80:83]
ld v[156:159],v245,s[52:55],s91 offen offset:3072 sc1
mm v[112:115],v[192:195],v[188:191],v[112:115]
ar v230,a70
ar v231,a71
mm v[112:115],v[196:199],v[172:175],v[112:115]
cv v248,v224,v225
cv v249,v226,v227
mm v[116:119],v[204:207],v[172:175],v[116:119]
wt lgkmcnt(0)
s_barrier
mm v[116:119],v[200:203],v[188:191],v[116:119]
ld a[120:123],v246,s[52:55],s91 offen offset:3072 sc1
mm v[84:87],v[200:203],v[184:187],v[84:87]
rt v[192:193],v240 offset:0
rt v[194:195],v240 offset:1024
mm v[84:87],v[204:207],v[168:171],v[84:87]
rt v[196:197],v240 offset:8448
rt v[198:199],v240 offset:9472
mm v[52:55],v[204:207],v[164:167],v[52:55]
s_add_u32 m0, s83, 50688
cv v250,v228,v229
mm v[52:55],v[200:203],v[180:183],v[52:55]
ld v232,s[44:47],s80 offen lds
mm v[20:23],v[200:203],v[176:179],v[20:23]
cv v251,v230,v231
aw a188,v248
mm v[20:23],v[204:207],v[160:163],v[20:23]
rt v[200:201],v241 offset:0
rt v[202:203],v241 offset:1024
mm v[24:27],v[212:215],v[160:163],v[24:27]
aw a189,v249
aw a190,v250
mm v[24:27],v[208:211],v[176:179],v[24:27]
ld a[124:127],v247,s[52:55],s91 offen offset:3072 sc1
mm v[56:59],v[208:211],v[180:183],v[56:59]
s_add_u32 s91, s91, 4096
aw a191,v251
mm v[56:59],v[212:215],v[164:167],v[56:59]
rt v[204:205],v241 offset:8448
rt v[206:207],v241 offset:9472
mm v[88:91],v[212:215],v[168:171],v[88:91]
s_add_u32 m0, s83, 51712
v_lshlrev_b32 v239, 5, v236
mm v[88:91],v[208:211],v[184:187],v[88:91]
ld v233,s[44:47],s80 offen lds
mm v[120:123],v[208:211],v[188:191],v[120:123]
v_xor_b32 v239, v239, v255
v_and_b32 v239, 0xf0, v239
mm v[120:123],v[212:215],v[172:175],v[120:123]
rt v[208:209],v242 offset:0
rt v[210:211],v242 offset:1024
mm v[124:127],v[220:223],v[172:175],v[124:127]
v_lshl_add_u32 v239, v236, 11, v239
s_lshl_b32 s92, s77, 16
mm v[124:127],v[216:219],v[188:191],v[124:127]
s_lshl_b32 s93, s78, 14
s_add_u32 s92, s92, s93
mm v[92:95],v[216:219],v[184:187],v[92:95]
v_add_u32 v239, s92, v239
)ASM" R"ASM(aw a228,v239
mm v[92:95],v[220:223],v[168:171],v[92:95]
rt v[212:213],v242 offset:8448
rt v[214:215],v242 offset:9472
mm v[60:63],v[220:223],v[164:167],v[60:63]
ds_write_b128 v238, v[248:251] offset:2048
mm v[60:63],v[216:219],v[180:183],v[60:63]
st a[176:179],v255,s[48:51],s18 offen offset:3072 sc1
mm v[28:31],v[216:219],v[176:179],v[28:31]
wt vmcnt(16) lgkmcnt(5)
v_bfe_u32 v239, v255, 6, 2
mm v[28:31],v[220:223],v[160:163],v[28:31]
rt v[216:217],v243 offset:0
rt v[218:219],v243 offset:1024
mm v[0:3],v[192:195],v[128:131],v[0:3]
ar v224,a0
ar v225,a1
mm v[0:3],v[196:199],a[104:107],v[0:3]
ld v[160:163],v244,s[52:55],s91 offen offset:0 sc1
mm v[32:35],v[196:199],a[108:111],v[32:35]
ar v226,a2
ar v227,a3
mm v[32:35],v[192:195],v[140:143],v[32:35]
rt v[220:221],v243 offset:8448
rt v[222:223],v243 offset:9472
mm v[64:67],v[192:195],a[96:99],v[64:67]
ar v228,a4
ar v229,a5
mm v[64:67],v[196:199],a[112:115],v[64:67]
ld v[164:167],v245,s[52:55],s91 offen offset:0 sc1
mm v[96:99],v[196:199],a[116:119],v[96:99]
ar v230,a6
ar v231,a7
mm v[96:99],v[192:195],a[100:103],v[96:99]
rt v[196:197],v240 offset:128
rt v[198:199],v240 offset:1152
mm v[100:103],v[200:203],a[100:103],v[100:103]
s_add_u32 m0, s83, 52864
cv v248,v224,v225
mm v[100:103],v[204:207],a[116:119],v[100:103]
ld v234,s[44:47],s80 offen lds
mm v[68:71],v[204:207],a[112:115],v[68:71]
cv v249,v226,v227
cv v250,v228,v229
mm v[68:71],v[200:203],a[96:99],v[68:71]
rt v[192:193],v240 offset:8576
rt v[194:195],v240 offset:9600
mm v[36:39],v[200:203],v[140:143],v[36:39]
cv v251,v230,v231
aw a0,v248
mm v[36:39],v[204:207],a[108:111],v[36:39]
ld v[168:171],v246,s[52:55],s91 offen offset:0 sc1
mm v[4:7],v[204:207],a[104:107],v[4:7]
wt lgkmcnt(4)
aw a1,v249
mm v[4:7],v[200:203],v[128:131],v[4:7]
rt v[204:205],v241 offset:128
rt v[206:207],v241 offset:1152
mm v[8:11],v[208:211],v[128:131],v[8:11]
s_add_u32 m0, s83, 53888
aw a2,v250
mm v[8:11],v[212:215],a[104:107],v[8:11]
ld v235,s[44:47],s80 offen lds
mm v[40:43],v[212:215],a[108:111],v[40:43]
s_add_u32 s80, s80, 524288
aw a3,v251
mm v[40:43],v[208:211],v[140:143],v[40:43]
rt v[200:201],v241 offset:8576
rt v[202:203],v241 offset:9600
mm v[72:75],v[208:211],a[96:99],v[72:75]
v_lshlrev_b32 v239, 6, v239
v_lshl_add_u32 v239, v236, 4, v239
mm v[72:75],v[212:215],a[112:115],v[72:75]
ld v[172:175],v247,s[52:55],s91 offen offset:0 sc1
mm v[104:107],v[212:215],a[116:119],v[104:107]
s_lshl_b32 s92, s78, 8
v_add_u32 v239, s92, v239
mm v[104:107],v[208:211],a[100:103],v[104:107]
rt v[212:213],v242 offset:128
rt v[214:215],v242 offset:1152
mm v[108:111],v[216:219],a[100:103],v[108:111]
v_mul_u32_u24 v239, 7168, v239
v_and_or_b32 v239, v255, 48, v239
mm v[108:111],v[220:223],a[116:119],v[108:111]
aw a229,v239
v_and_b32 v239, 0xf0, v255
mm v[76:79],v[220:223],a[112:115],v[76:79]
s_mov_b32 s18, 16384
v_lshl_add_u32 v239, v236, 14, v239
mm v[76:79],v[216:219],a[96:99],v[76:79]
rt v[208:209],v242 offset:8576
rt v[210:211],v242 offset:9600
mm v[44:47],v[216:219],v[140:143],v[44:47]
ds_write_b128 v238, v[248:251] offset:3072
mm v[44:47],v[220:223],a[108:111],v[44:47]
st a[180:183],v255,s[48:51],s18 offen offset:0 sc1
mm v[12:15],v[220:223],a[104:107],v[12:15]
wt lgkmcnt(5)
s_lshl_b32 s92, s3, 9
mm v[12:15],v[216:219],v[128:131],v[12:15]
)ASM" R"ASM(rt v[220:221],v243 offset:128
rt v[222:223],v243 offset:1152
mm v[16:19],v[196:199],v[128:131],v[16:19]
ar v224,a72
ar v225,a73
mm v[16:19],v[192:195],a[104:107],v[16:19]
ld v[176:179],v244,s[52:55],s91 offen offset:1024 sc1
mm v[48:51],v[192:195],a[108:111],v[48:51]
ar v226,a74
ar v227,a75
mm v[48:51],v[196:199],v[140:143],v[48:51]
rt v[216:217],v243 offset:8576
rt v[218:219],v243 offset:9600
mm v[80:83],v[196:199],a[96:99],v[80:83]
ar v228,a76
.La5_LW1_12:
s_cbranch_execz .La5_LW1_R0
ar v229,a77
mm v[80:83],v[192:195],a[112:115],v[80:83]
ld v[180:183],v245,s[52:55],s91 offen offset:1024 sc1
mm v[112:115],v[192:195],a[116:119],v[112:115]
ar v230,a78
ar v231,a79
mm v[112:115],v[196:199],a[100:103],v[112:115]
cv v248,v224,v225
cv v249,v226,v227
mm v[116:119],v[204:207],a[100:103],v[116:119]
wt lgkmcnt(0)
s_barrier
mm v[116:119],v[200:203],a[116:119],v[116:119]
ld v[184:187],v246,s[52:55],s91 offen offset:1024 sc1
mm v[84:87],v[200:203],a[112:115],v[84:87]
rt v[192:193],v240 offset:16896
rt v[194:195],v240 offset:17920
mm v[84:87],v[204:207],a[96:99],v[84:87]
rt v[196:197],v240 offset:25344
rt v[198:199],v240 offset:26368
mm v[52:55],v[204:207],v[140:143],v[52:55]
s_add_u32 m0, s83, 0
cv v250,v228,v229
mm v[52:55],v[200:203],a[108:111],v[52:55]
ld v232,s[44:47],s80 offen lds
mm v[20:23],v[200:203],a[104:107],v[20:23]
cv v251,v230,v231
aw a4,v248
mm v[20:23],v[204:207],v[128:131],v[20:23]
rt v[200:201],v241 offset:16896
rt v[202:203],v241 offset:17920
mm v[24:27],v[212:215],v[128:131],v[24:27]
aw a5,v249
aw a6,v250
mm v[24:27],v[208:211],a[104:107],v[24:27]
ld v[188:191],v247,s[52:55],s91 offen offset:1024 sc1
mm v[56:59],v[208:211],a[108:111],v[56:59]
aw a7,v251
v_add_u32 v239, s92, v239
mm v[56:59],v[212:215],v[140:143],v[56:59]
rt v[204:205],v241 offset:25344
rt v[206:207],v241 offset:26368
mm v[88:91],v[212:215],a[96:99],v[88:91]
s_add_u32 m0, s83, 1024
aw a230,v239
mm v[88:91],v[208:211],a[112:115],v[88:91]
ld v233,s[44:47],s80 offen lds
mm v[120:123],v[208:211],a[116:119],v[120:123]
v_add_u32 v239, 4294886912, v240
aw a224,v239
mm v[120:123],v[212:215],a[100:103],v[120:123]
rt v[208:209],v242 offset:16896
rt v[210:211],v242 offset:17920
mm v[124:127],v[220:223],a[100:103],v[124:127]
v_bfe_u32 v239, v255, 4, 4
v_lshlrev_b32 v236, 2, v236
mm v[124:127],v[216:219],a[116:119],v[124:127]
v_sub_u32 v239, v239, v236
v_subrev_u32 v236, 0, v239
mm v[92:95],v[216:219],a[112:115],v[92:95]
v_ashrrev_i32 v236, 31, v236
v_and_b32 v236, 0xff800000, v236
mm v[92:95],v[220:223],a[96:99],v[92:95]
rt v[212:213],v242 offset:25344
rt v[214:215],v242 offset:26368
mm v[60:63],v[220:223],v[140:143],v[60:63]
ds_write_b128 v238, v[248:251] offset:4096
mm v[60:63],v[216:219],a[108:111],v[60:63]
aw a236,v236
v_subrev_u32 v236, 1, v239
mm v[28:31],v[216:219],a[104:107],v[28:31]
wt vmcnt(15) lgkmcnt(5)
v_ashrrev_i32 v236, 31, v236
mm v[28:31],v[220:223],v[128:131],v[28:31]
rt v[216:217],v243 offset:16896
rt v[218:219],v243 offset:17920
mm v[0:3],v[192:195],v[132:135],v[0:3]
ar v224,a8
ar v225,a9
mm v[0:3],v[196:199],v[152:155],v[0:3]
ld v[128:131],v244,s[52:55],s91 offen offset:2048 sc1
mm v[32:35],v[196:199],v[156:159],v[32:35]
ar v226,a10
)ASM" R"ASM(ar v227,a11
mm v[32:35],v[192:195],v[136:139],v[32:35]
rt v[220:221],v243 offset:25344
rt v[222:223],v243 offset:26368
mm v[64:67],v[192:195],v[144:147],v[64:67]
ar v228,a12
ar v229,a13
mm v[64:67],v[196:199],a[120:123],v[64:67]
ld v[140:143],v245,s[52:55],s91 offen offset:2048 sc1
mm v[96:99],v[196:199],a[124:127],v[96:99]
ar v230,a14
ar v231,a15
mm v[96:99],v[192:195],v[148:151],v[96:99]
rt v[196:197],v240 offset:17024
rt v[198:199],v240 offset:18048
mm v[100:103],v[200:203],v[148:151],v[100:103]
s_add_u32 m0, s83, 2176
cv v248,v224,v225
mm v[100:103],v[204:207],a[124:127],v[100:103]
ld v234,s[44:47],s80 offen lds
mm v[68:71],v[204:207],a[120:123],v[68:71]
cv v249,v226,v227
cv v250,v228,v229
mm v[68:71],v[200:203],v[144:147],v[68:71]
rt v[192:193],v240 offset:25472
rt v[194:195],v240 offset:26496
mm v[36:39],v[200:203],v[136:139],v[36:39]
cv v251,v230,v231
aw a8,v248
mm v[36:39],v[204:207],v[156:159],v[36:39]
ld a[96:99],v246,s[52:55],s91 offen offset:2048 sc1
mm v[4:7],v[204:207],v[152:155],v[4:7]
wt lgkmcnt(4)
aw a9,v249
mm v[4:7],v[200:203],v[132:135],v[4:7]
rt v[204:205],v241 offset:17024
rt v[206:207],v241 offset:18048
mm v[8:11],v[208:211],v[132:135],v[8:11]
s_add_u32 m0, s83, 3200
aw a10,v250
mm v[8:11],v[212:215],v[152:155],v[8:11]
ld v235,s[44:47],s80 offen lds
mm v[40:43],v[212:215],v[156:159],v[40:43]
s_add_u32 s80, s80, 524288
aw a11,v251
mm v[40:43],v[208:211],v[136:139],v[40:43]
rt v[200:201],v241 offset:25472
rt v[202:203],v241 offset:26496
mm v[72:75],v[208:211],v[144:147],v[72:75]
v_and_b32 v236, 0xff800000, v236
aw a237,v236
mm v[72:75],v[212:215],a[120:123],v[72:75]
ld a[100:103],v247,s[52:55],s91 offen offset:2048 sc1
mm v[104:107],v[212:215],a[124:127],v[104:107]
v_subrev_u32 v236, 2, v239
v_ashrrev_i32 v236, 31, v236
mm v[104:107],v[208:211],v[148:151],v[104:107]
rt v[212:213],v242 offset:17024
rt v[214:215],v242 offset:18048
mm v[108:111],v[216:219],v[148:151],v[108:111]
v_and_b32 v236, 0xff800000, v236
aw a238,v236
mm v[108:111],v[220:223],a[124:127],v[108:111]
v_subrev_u32 v236, 3, v239
v_ashrrev_i32 v236, 31, v236
mm v[76:79],v[220:223],a[120:123],v[76:79]
v_and_b32 v236, 0xff800000, v236
aw a239,v236
mm v[76:79],v[216:219],v[144:147],v[76:79]
rt v[208:209],v242 offset:25472
rt v[210:211],v242 offset:26496
mm v[44:47],v[216:219],v[136:139],v[44:47]
ds_write_b128 v238, v[248:251] offset:5120
mm v[44:47],v[220:223],v[156:159],v[44:47]
st a[184:187],v255,s[48:51],s18 offen offset:1024 sc1
mm v[12:15],v[220:223],v[152:155],v[12:15]
wt lgkmcnt(5)
v_lshlrev_b32 v236, 4, v239
mm v[12:15],v[216:219],v[132:135],v[12:15]
rt v[220:221],v243 offset:17024
rt v[222:223],v243 offset:18048
mm v[16:19],v[196:199],v[132:135],v[16:19]
ar v224,a80
ar v225,a81
mm v[16:19],v[192:195],v[152:155],v[16:19]
ld a[104:107],v244,s[52:55],s91 offen offset:3072 sc1
mm v[48:51],v[192:195],v[156:159],v[48:51]
ar v226,a82
ar v227,a83
mm v[48:51],v[196:199],v[136:139],v[48:51]
rt v[216:217],v243 offset:25472
rt v[218:219],v243 offset:26496
mm v[80:83],v[196:199],v[144:147],v[80:83]
ar v228,a84
ar v229,a85
mm v[80:83],v[192:195],a[120:123],v[80:83]
ld a[108:111],v245,s[52:55],s91 offen offset:3072 sc1
mm v[112:115],v[192:195],a[124:127],v[112:115]
ar v230,a86
ar v231,a87
)ASM" R"ASM(mm v[112:115],v[196:199],v[148:151],v[112:115]
cv v248,v224,v225
cv v249,v226,v227
mm v[116:119],v[204:207],v[148:151],v[116:119]
wt lgkmcnt(0)
s_barrier
mm v[116:119],v[200:203],a[124:127],v[116:119]
ld a[112:115],v246,s[52:55],s91 offen offset:3072 sc1
mm v[84:87],v[200:203],a[120:123],v[84:87]
rt v[192:193],v240 offset:33792
rt v[194:195],v240 offset:34816
mm v[84:87],v[204:207],v[144:147],v[84:87]
rt v[196:197],v240 offset:42240
rt v[198:199],v240 offset:43264
mm v[52:55],v[204:207],v[136:139],v[52:55]
s_add_u32 m0, s83, 16896
cv v250,v228,v229
mm v[52:55],v[200:203],v[156:159],v[52:55]
ld v232,s[44:47],s80 offen lds
mm v[20:23],v[200:203],v[152:155],v[20:23]
cv v251,v230,v231
aw a12,v248
mm v[20:23],v[204:207],v[132:135],v[20:23]
rt v[200:201],v241 offset:33792
rt v[202:203],v241 offset:34816
mm v[24:27],v[212:215],v[132:135],v[24:27]
aw a13,v249
aw a14,v250
mm v[24:27],v[208:211],v[152:155],v[24:27]
ld a[116:119],v247,s[52:55],s91 offen offset:3072 sc1
mm v[56:59],v[208:211],v[156:159],v[56:59]
s_add_u32 s91, s91, 4096
aw a15,v251
mm v[56:59],v[212:215],v[136:139],v[56:59]
rt v[204:205],v241 offset:42240
rt v[206:207],v241 offset:43264
mm v[88:91],v[212:215],v[144:147],v[88:91]
s_add_u32 m0, s83, 17920
s_mov_b32 s92, 0x3f80
mm v[88:91],v[208:211],a[120:123],v[88:91]
ld v233,s[44:47],s80 offen lds
mm v[120:123],v[208:211],a[124:127],v[120:123]
v_lshlrev_b32 v236, v236, s92
v_cmp_gt_u32 vcc, 2, v239
mm v[120:123],v[212:215],v[148:151],v[120:123]
rt v[208:209],v242 offset:33792
rt v[210:211],v242 offset:34816
mm v[124:127],v[220:223],v[148:151],v[124:127]
v_subrev_u32 v239, 2, v239
v_cmp_gt_u32 s[92:93], 2, v239
mm v[124:127],v[216:219],a[124:127],v[124:127]
v_cndmask_b32 v239, 0, v236, vcc
aw a212,0
mm v[92:95],v[216:219],a[120:123],v[92:95]
aw a214,v239
v_cndmask_b32 v239, 0, v236, s[92:93]
mm v[92:95],v[220:223],v[144:147],v[92:95]
rt v[212:213],v242 offset:42240
rt v[214:215],v242 offset:43264
mm v[60:63],v[220:223],v[136:139],v[60:63]
v_mov_b32 v236, 0x3f803f80
aw a215,v239
mm v[60:63],v[216:219],v[156:159],v[60:63]
st a[188:191],v255,s[48:51],s18 offen offset:2048 sc1
mm v[28:31],v[216:219],v[152:155],v[28:31]
wt vmcnt(15) lgkmcnt(4)
aw a213,0
mm v[28:31],v[220:223],v[132:135],v[28:31]
rt v[216:217],v243 offset:33792
rt v[218:219],v243 offset:34816
mm v[0:3],v[192:195],v[160:163],v[0:3]
ar v224,a16
ar v225,a17
mm v[0:3],v[196:199],v[176:179],v[0:3]
ld v[132:135],v244,s[52:55],s91 offen offset:0 sc1
mm v[32:35],v[196:199],v[180:183],v[32:35]
ar v226,a18
ar v227,a19
mm v[32:35],v[192:195],v[164:167],v[32:35]
rt v[220:221],v243 offset:42240
rt v[222:223],v243 offset:43264
mm v[64:67],v[192:195],v[168:171],v[64:67]
ar v228,a20
ar v229,a21
mm v[64:67],v[196:199],v[184:187],v[64:67]
ld v[136:139],v245,s[52:55],s91 offen offset:0 sc1
mm v[96:99],v[196:199],v[188:191],v[96:99]
ar v230,a22
ar v231,a23
mm v[96:99],v[192:195],v[172:175],v[96:99]
rt v[196:197],v240 offset:33920
rt v[198:199],v240 offset:34944
mm v[100:103],v[200:203],v[172:175],v[100:103]
s_add_u32 m0, s83, 19072
cv v248,v224,v225
mm v[100:103],v[204:207],v[188:191],v[100:103]
ld v234,s[44:47],s80 offen lds
mm v[68:71],v[204:207],v[184:187],v[68:71]
cv v249,v226,v227
cv v250,v228,v229
)ASM" R"ASM(mm v[68:71],v[200:203],v[168:171],v[68:71]
rt v[192:193],v240 offset:42368
rt v[194:195],v240 offset:43392
mm v[36:39],v[200:203],v[164:167],v[36:39]
cv v251,v230,v231
aw a16,v248
mm v[36:39],v[204:207],v[180:183],v[36:39]
ld v[144:147],v246,s[52:55],s91 offen offset:0 sc1
mm v[4:7],v[204:207],v[176:179],v[4:7]
wt lgkmcnt(4)
aw a17,v249
mm v[4:7],v[200:203],v[160:163],v[4:7]
rt v[204:205],v241 offset:33920
rt v[206:207],v241 offset:34944
mm v[8:11],v[208:211],v[160:163],v[8:11]
s_add_u32 m0, s83, 20096
aw a18,v250
mm v[8:11],v[212:215],v[176:179],v[8:11]
ld v235,s[44:47],s80 offen lds
mm v[40:43],v[212:215],v[180:183],v[40:43]
s_add_u32 s80, s80, 524288
aw a19,v251
mm v[40:43],v[208:211],v[164:167],v[40:43]
rt v[200:201],v241 offset:42368
rt v[202:203],v241 offset:43392
mm v[72:75],v[208:211],v[168:171],v[72:75]
aw a216,0
aw a217,0
mm v[72:75],v[212:215],v[184:187],v[72:75]
ld v[148:151],v247,s[52:55],s91 offen offset:0 sc1
mm v[104:107],v[212:215],v[188:191],v[104:107]
v_lshrrev_b32 v239, 4, v255
v_xor_b32 v239, v239, v255
mm v[104:107],v[208:211],v[172:175],v[104:107]
rt v[212:213],v242 offset:33920
rt v[214:215],v242 offset:34944
mm v[108:111],v[216:219],v[172:175],v[108:111]
v_and_b32 v239, 32, v239
v_cmp_eq_u32 vcc, 0, v239
mm v[108:111],v[220:223],v[188:191],v[108:111]
s_mov_b32 s78, 4294950911
s_mul_i32 s70, s70, s22
mm v[76:79],v[220:223],v[184:187],v[76:79]
v_cndmask_b32 v236, 0, v236, vcc
s_sub_u32 s92, 4294967294, s3
mm v[76:79],v[216:219],v[168:171],v[76:79]
rt v[208:209],v242 offset:42368
rt v[210:211],v242 offset:43392
mm v[44:47],v[216:219],v[164:167],v[44:47]
s_add_u32 s92, s92, s7
s_mul_i32 s92, s92, s21
mm v[44:47],v[220:223],v[180:183],v[44:47]
st a[0:3],v255,s[48:51],s18 offen offset:3072 sc1
mm v[12:15],v[220:223],v[176:179],v[12:15]
wt lgkmcnt(4)
s_add_u32 s90, s90, s92
mm v[12:15],v[216:219],v[160:163],v[12:15]
rt v[220:221],v243 offset:33920
rt v[222:223],v243 offset:34944
mm v[16:19],v[196:199],v[160:163],v[16:19]
ar v224,a88
ar v225,a89
mm v[16:19],v[192:195],v[176:179],v[16:19]
ld v[152:155],v244,s[52:55],s91 offen offset:1024 sc1
mm v[48:51],v[192:195],v[180:183],v[48:51]
ar v226,a90
ar v227,a91
mm v[48:51],v[196:199],v[164:167],v[48:51]
rt v[216:217],v243 offset:42368
rt v[218:219],v243 offset:43392
mm v[80:83],v[196:199],v[168:171],v[80:83]
ar v228,a92
ar v229,a93
mm v[80:83],v[192:195],v[184:187],v[80:83]
ld v[156:159],v245,s[52:55],s91 offen offset:1024 sc1
mm v[112:115],v[192:195],v[188:191],v[112:115]
ar v230,a94
ar v231,a95
mm v[112:115],v[196:199],v[172:175],v[112:115]
cv v248,v224,v225
cv v249,v226,v227
mm v[116:119],v[204:207],v[172:175],v[116:119]
wt lgkmcnt(0)
s_barrier
mm v[116:119],v[200:203],v[188:191],v[116:119]
ld a[120:123],v246,s[52:55],s91 offen offset:1024 sc1
mm v[84:87],v[200:203],v[184:187],v[84:87]
rt v[192:193],v240 offset:50688
rt v[194:195],v240 offset:51712
mm v[84:87],v[204:207],v[168:171],v[84:87]
rt v[196:197],v240 offset:59136
rt v[198:199],v240 offset:60160
mm v[52:55],v[204:207],v[164:167],v[52:55]
s_add_u32 m0, s83, 33792
cv v250,v228,v229
mm v[52:55],v[200:203],v[180:183],v[52:55]
ld v232,s[44:47],s80 offen lds
mm v[20:23],v[200:203],v[176:179],v[20:23]
cv v251,v230,v231
aw a20,v248
)ASM" R"ASM(mm v[20:23],v[204:207],v[160:163],v[20:23]
rt v[200:201],v241 offset:50688
rt v[202:203],v241 offset:51712
mm v[24:27],v[212:215],v[160:163],v[24:27]
aw a21,v249
aw a22,v250
mm v[24:27],v[208:211],v[176:179],v[24:27]
ld a[124:127],v247,s[52:55],s91 offen offset:1024 sc1
mm v[56:59],v[208:211],v[180:183],v[56:59]
aw a23,v251
s_mov_b32 s17, 1073741824
mm v[56:59],v[212:215],v[164:167],v[56:59]
rt v[204:205],v241 offset:59136
rt v[206:207],v241 offset:60160
mm v[88:91],v[212:215],v[168:171],v[88:91]
s_add_u32 m0, s83, 34816
s_lshl_b32 s77, s76, 16
mm v[88:91],v[208:211],v[184:187],v[88:91]
ld v233,s[44:47],s80 offen lds
mm v[120:123],v[208:211],v[188:191],v[120:123]
s_sub_u32 s77, s77, 8192
s_lshl4_add_u32 s81, s75, s74
mm v[120:123],v[212:215],v[172:175],v[120:123]
rt v[208:209],v242 offset:50688
rt v[210:211],v242 offset:51712
mm v[124:127],v[220:223],v[172:175],v[124:127]
s_lshl3_add_u32 s81, s81, 1
mm v[124:127],v[216:219],v[188:191],v[124:127]
s_cmp_eq_u32 s73, 0
s_cselect_b32 s16, s77, 0
mm v[92:95],v[216:219],v[184:187],v[92:95]
s_mov_b32 s18, 20480
s_mul_i32 s92, s75, 4294836224
mm v[92:95],v[220:223],v[168:171],v[92:95]
rt v[212:213],v242 offset:59136
rt v[214:215],v242 offset:60160
mm v[60:63],v[220:223],v[164:167],v[60:63]
s_add_u32 s92, s92, s88
s_add_u32 s19, s92, 393216
mm v[60:63],v[216:219],v[180:183],v[60:63]
st a[4:7],v255,s[48:51],s18 offen offset:0 sc1
mm v[28:31],v[216:219],v[176:179],v[28:31]
wt vmcnt(16) lgkmcnt(4)
aw a208,v236
mm v[28:31],v[220:223],v[160:163],v[28:31]
rt v[216:217],v243 offset:50688
rt v[218:219],v243 offset:51712
mm v[0:3],v[192:195],v[128:131],v[0:3]
ar v224,a24
ar v225,a25
mm v[0:3],v[196:199],a[104:107],v[0:3]
ld v[160:163],v244,s[52:55],s91 offen offset:2048 sc1
mm v[32:35],v[196:199],a[108:111],v[32:35]
ar v226,a26
ar v227,a27
mm v[32:35],v[192:195],v[140:143],v[32:35]
rt v[220:221],v243 offset:59136
rt v[222:223],v243 offset:60160
mm v[64:67],v[192:195],a[96:99],v[64:67]
ar v228,a28
ar v229,a29
mm v[64:67],v[196:199],a[112:115],v[64:67]
ld v[164:167],v245,s[52:55],s91 offen offset:2048 sc1
mm v[96:99],v[196:199],a[116:119],v[96:99]
ar v230,a30
ar v231,a31
mm v[96:99],v[192:195],a[100:103],v[96:99]
rt v[196:197],v240 offset:50816
rt v[198:199],v240 offset:51840
mm v[100:103],v[200:203],a[100:103],v[100:103]
s_add_u32 m0, s83, 35968
cv v248,v224,v225
mm v[100:103],v[204:207],a[116:119],v[100:103]
ld v234,s[44:47],s80 offen lds
mm v[68:71],v[204:207],a[112:115],v[68:71]
cv v249,v226,v227
cv v250,v228,v229
mm v[68:71],v[200:203],a[96:99],v[68:71]
rt v[192:193],v240 offset:59264
rt v[194:195],v240 offset:60288
mm v[36:39],v[200:203],v[140:143],v[36:39]
cv v251,v230,v231
aw a24,v248
mm v[36:39],v[204:207],a[108:111],v[36:39]
ld v[168:171],v246,s[52:55],s91 offen offset:2048 sc1
mm v[4:7],v[204:207],a[104:107],v[4:7]
wt lgkmcnt(4)
aw a25,v249
mm v[4:7],v[200:203],v[128:131],v[4:7]
rt v[204:205],v241 offset:50816
rt v[206:207],v241 offset:51840
mm v[8:11],v[208:211],v[128:131],v[8:11]
s_add_u32 m0, s83, 36992
aw a26,v250
mm v[8:11],v[212:215],a[104:107],v[8:11]
ld v235,s[44:47],s80 offen lds
mm v[40:43],v[212:215],a[108:111],v[40:43]
s_add_u32 s80, s80, 524288
aw a27,v251
mm v[40:43],v[208:211],v[140:143],v[40:43]
)ASM" R"ASM(rt v[200:201],v241 offset:59264
rt v[202:203],v241 offset:60288
mm v[72:75],v[208:211],a[96:99],v[72:75]
aw a209,v236
aw a210,v236
mm v[72:75],v[212:215],a[112:115],v[72:75]
ld v[172:175],v247,s[52:55],s91 offen offset:2048 sc1
mm v[104:107],v[212:215],a[116:119],v[104:107]
aw a211,v236
aw a232,0
mm v[104:107],v[208:211],a[100:103],v[104:107]
rt v[212:213],v242 offset:50816
rt v[214:215],v242 offset:51840
mm v[108:111],v[216:219],a[100:103],v[108:111]
aw a233,0
aw a234,0
mm v[108:111],v[220:223],a[116:119],v[108:111]
aw a235,0
s_lshl_b32 s20, s72, 10
mm v[76:79],v[220:223],a[112:115],v[76:79]
s_add_u32 s20, s20, 16384
s_mov_b32 s4, 1466176288
mm v[76:79],v[216:219],a[96:99],v[76:79]
rt v[208:209],v242 offset:59264
rt v[210:211],v242 offset:60288
mm v[44:47],v[216:219],v[140:143],v[44:47]
s_cmp_ge_u32 s2, 64
s_cselect_b32 s4, 1182081808, s4
mm v[44:47],v[220:223],a[108:111],v[44:47]
st a[8:11],v255,s[48:51],s18 offen offset:1024 sc1
mm v[12:15],v[220:223],a[104:107],v[12:15]
wt lgkmcnt(4)
mm v[12:15],v[216:219],v[128:131],v[12:15]
rt v[220:221],v243 offset:50816
rt v[222:223],v243 offset:51840
mm v[16:19],v[196:199],v[128:131],v[16:19]
ar v224,a240
ar v225,a241
mm v[16:19],v[192:195],a[104:107],v[16:19]
ld v[176:179],v244,s[52:55],s91 offen offset:3072 sc1
mm v[48:51],v[192:195],a[108:111],v[48:51]
ar v226,a242
ar v227,a243
mm v[48:51],v[196:199],v[140:143],v[48:51]
rt v[216:217],v243 offset:59264
rt v[218:219],v243 offset:60288
mm v[80:83],v[196:199],a[96:99],v[80:83]
ar v228,a244
ar v229,a245
mm v[80:83],v[192:195],a[112:115],v[80:83]
ld v[180:183],v245,s[52:55],s91 offen offset:3072 sc1
mm v[112:115],v[192:195],a[116:119],v[112:115]
ar v230,a246
ar v231,a247
.La5_LW1_13:
s_cbranch_execz .La5_LW1_R0
mm v[112:115],v[196:199],a[100:103],v[112:115]
cv v248,v224,v225
cv v249,v226,v227
mm v[116:119],v[204:207],a[100:103],v[116:119]
wt lgkmcnt(0)
s_barrier
mm v[116:119],v[200:203],a[116:119],v[116:119]
ld v[184:187],v246,s[52:55],s91 offen offset:3072 sc1
mm v[84:87],v[200:203],a[112:115],v[84:87]
rt v[192:193],v240 offset:0
rt v[194:195],v240 offset:1024
mm v[84:87],v[204:207],a[96:99],v[84:87]
rt v[196:197],v240 offset:8448
rt v[198:199],v240 offset:9472
mm v[52:55],v[204:207],v[140:143],v[52:55]
s_add_u32 m0, s83, 50688
cv v250,v228,v229
mm v[52:55],v[200:203],a[108:111],v[52:55]
ld v232,s[44:47],s80 offen lds
mm v[20:23],v[200:203],a[104:107],v[20:23]
cv v251,v230,v231
aw a28,v248
mm v[20:23],v[204:207],v[128:131],v[20:23]
rt v[200:201],v241 offset:0
rt v[202:203],v241 offset:1024
mm v[24:27],v[212:215],v[128:131],v[24:27]
aw a29,v249
aw a30,v250
mm v[24:27],v[208:211],a[104:107],v[24:27]
ld v[188:191],v247,s[52:55],s91 offen offset:3072 sc1
mm v[56:59],v[208:211],a[108:111],v[56:59]
s_add_u32 s91, s91, 4096
aw a31,v251
mm v[56:59],v[212:215],v[140:143],v[56:59]
rt v[204:205],v241 offset:8448
rt v[206:207],v241 offset:9472
mm v[88:91],v[212:215],a[96:99],v[88:91]
s_add_u32 m0, s83, 51712
mm v[88:91],v[208:211],a[112:115],v[88:91]
ld v233,s[44:47],s80 offen lds
mm v[120:123],v[208:211],a[116:119],v[120:123]
s_cmp_ge_u32 s2, 128
s_cselect_b32 s4, 1967534368, s4
mm v[120:123],v[212:215],a[100:103],v[120:123]
)ASM" R"ASM(rt v[208:209],v242 offset:0
rt v[210:211],v242 offset:1024
mm v[124:127],v[220:223],a[100:103],v[124:127]
s_cmp_ge_u32 s2, 192
s_cselect_b32 s4, 1683431728, s4
mm v[124:127],v[216:219],a[116:119],v[124:127]
v_bfe_u32 v236, v255, 4, 3
v_lshlrev_b32 v236, 2, v236
mm v[92:95],v[216:219],a[112:115],v[92:95]
v_lshrrev_b32 v236, v236, s4
v_and_b32 v236, 15, v236
mm v[92:95],v[220:223],a[96:99],v[92:95]
rt v[212:213],v242 offset:8448
rt v[214:215],v242 offset:9472
mm v[60:63],v[220:223],v[140:143],v[60:63]
v_bfe_u32 v239, v255, 7, 1
v_mul_u32_u24 v239, 28, v239
mm v[60:63],v[216:219],a[108:111],v[60:63]
v_xor_b32 v236, v236, v239
v_xor_b32 v236, s72, v236
mm v[28:31],v[216:219],a[104:107],v[28:31]
wt vmcnt(15) lgkmcnt(4)
v_lshlrev_b32 v236, 3, v236
mm v[28:31],v[220:223],v[128:131],v[28:31]
rt v[216:217],v243 offset:0
rt v[218:219],v243 offset:1024
mm v[0:3],v[192:195],v[132:135],v[0:3]
ar v224,a32
ar v225,a33
aw a225,v236
mm v[0:3],v[196:199],v[152:155],v[0:3]
ld v[128:131],v244,s[52:55],s91 offen offset:0 sc1
s_lshl3_add_u32 s5, s76, s101
mm v[32:35],v[196:199],v[156:159],v[32:35]
ar v226,a34
ar v227,a35
s_lshl2_add_u32 s5, s5, s3
mm v[32:35],v[192:195],v[136:139],v[32:35]
rt v[220:221],v243 offset:8448
rt v[222:223],v243 offset:9472
s_lshl_b32 s5, s5, 8
mm v[64:67],v[192:195],v[144:147],v[64:67]
ar v228,a36
ar v229,a37
s_add_u32 s5, s5, 1048576
mm v[64:67],v[196:199],a[120:123],v[64:67]
ld v[140:143],v245,s[52:55],s91 offen offset:0 sc1
mm v[96:99],v[196:199],a[124:127],v[96:99]
ar v230,a38
ar v231,a39
mm v[96:99],v[192:195],v[148:151],v[96:99]
rt v[196:197],v240 offset:128
rt v[198:199],v240 offset:1152
mm v[100:103],v[200:203],v[148:151],v[100:103]
s_add_u32 m0, s83, 52864
cv v248,v224,v225
mm v[100:103],v[204:207],a[124:127],v[100:103]
ld v234,s[44:47],s80 offen lds
mm v[68:71],v[204:207],a[120:123],v[68:71]
cv v249,v226,v227
cv v250,v228,v229
mm v[68:71],v[200:203],v[144:147],v[68:71]
rt v[192:193],v240 offset:8576
rt v[194:195],v240 offset:9600
mm v[36:39],v[200:203],v[136:139],v[36:39]
cv v251,v230,v231
aw a32,v248
mm v[36:39],v[204:207],v[156:159],v[36:39]
ld a[192:195],v246,s[52:55],s91 offen offset:0 sc1
mm v[4:7],v[204:207],v[152:155],v[4:7]
wt lgkmcnt(4)
aw a33,v249
mm v[4:7],v[200:203],v[132:135],v[4:7]
rt v[204:205],v241 offset:128
rt v[206:207],v241 offset:1152
mm v[8:11],v[208:211],v[132:135],v[8:11]
s_add_u32 m0, s83, 53888
aw a34,v250
mm v[8:11],v[212:215],v[152:155],v[8:11]
ld v235,s[44:47],s80 offen lds
mm v[40:43],v[212:215],v[156:159],v[40:43]
aw a35,v251
mm v[40:43],v[208:211],v[136:139],v[40:43]
rt v[200:201],v241 offset:8576
rt v[202:203],v241 offset:9600
mm v[72:75],v[208:211],v[144:147],v[72:75]
mm v[72:75],v[212:215],a[120:123],v[72:75]
ld a[196:199],v247,s[52:55],s91 offen offset:0 sc1
mm v[104:107],v[212:215],a[124:127],v[104:107]
mm v[104:107],v[208:211],v[148:151],v[104:107]
rt v[212:213],v242 offset:128
rt v[214:215],v242 offset:1152
mm v[108:111],v[216:219],v[148:151],v[108:111]
mm v[108:111],v[220:223],a[124:127],v[108:111]
mm v[76:79],v[220:223],a[120:123],v[76:79]
mm v[76:79],v[216:219],v[144:147],v[76:79]
rt v[208:209],v242 offset:8576
rt v[210:211],v242 offset:9600
mm v[44:47],v[216:219],v[136:139],v[44:47]
mm v[44:47],v[220:223],v[156:159],v[44:47]
)ASM" R"ASM(st a[12:15],v255,s[48:51],s18 offen offset:2048 sc1
mm v[12:15],v[220:223],v[152:155],v[12:15]
wt lgkmcnt(4)
mm v[12:15],v[216:219],v[132:135],v[12:15]
rt v[220:221],v243 offset:128
rt v[222:223],v243 offset:1152
mm v[16:19],v[196:199],v[132:135],v[16:19]
ar v224,a248
ar v225,a249
mm v[16:19],v[192:195],v[152:155],v[16:19]
ld a[200:203],v244,s[52:55],s91 offen offset:1024 sc1
mm v[48:51],v[192:195],v[156:159],v[48:51]
ar v226,a250
ar v227,a251
mm v[48:51],v[196:199],v[136:139],v[48:51]
rt v[216:217],v243 offset:8576
rt v[218:219],v243 offset:9600
mm v[80:83],v[196:199],v[144:147],v[80:83]
ar v228,a252
ar v229,a253
mm v[80:83],v[192:195],a[120:123],v[80:83]
ld a[204:207],v245,s[52:55],s91 offen offset:1024 sc1
mm v[112:115],v[192:195],a[124:127],v[112:115]
ar v230,a254
ar v231,a255
mm v[112:115],v[196:199],v[148:151],v[112:115]
cv v248,v224,v225
cv v249,v226,v227
mm v[116:119],v[204:207],v[148:151],v[116:119]
wt lgkmcnt(0)
s_barrier
mm v[116:119],v[200:203],a[124:127],v[116:119]
cv v250,v228,v229
cv v251,v230,v231
mm v[84:87],v[200:203],a[120:123],v[84:87]
rt v[192:193],v240 offset:16896
rt v[194:195],v240 offset:17920
mm v[84:87],v[204:207],v[144:147],v[84:87]
rt v[196:197],v240 offset:25344
rt v[198:199],v240 offset:26368
mm v[52:55],v[204:207],v[136:139],v[52:55]
aw a36,v248
aw a37,v249
mm v[52:55],v[200:203],v[156:159],v[52:55]
aw a38,v250
aw a39,v251
mm v[20:23],v[200:203],v[152:155],v[20:23]
s_getpc_b64 s[92:93]
.La5_LCT2:
s_and_b32 s93, s93, 0xffff
mm v[20:23],v[204:207],v[132:135],v[20:23]
rt v[200:201],v241 offset:16896
rt v[202:203],v241 offset:17920
mm v[24:27],v[212:215],v[132:135],v[24:27]
s_lshr_b32 s94, s2, 3
s_lshl2_add_u32 s94, s94, s3
mm v[24:27],v[208:211],v[152:155],v[24:27]
s_lshl_b32 s94, s94, 10
v_lshrrev_b32 v233, 7, v255
mm v[56:59],v[208:211],v[156:159],v[56:59]
v_lshlrev_b32 v232, 3, v255
v_lshl_add_u32 v232, v233, 28, v232
mm v[56:59],v[212:215],v[136:139],v[56:59]
rt v[204:205],v241 offset:25344
rt v[206:207],v241 offset:26368
mm v[88:91],v[212:215],v[144:147],v[88:91]
v_add_u32 v232, s94, v232
s_mov_b32 s94, .La5_LCODEEND-.La5_LCT2
mm v[88:91],v[208:211],a[120:123],v[88:91]
s_mov_b32 s95, 0x20000
buffer_load_dword a253, v232, s[92:95], 0 offen
mm v[120:123],v[208:211],a[124:127],v[120:123]
s_mov_b32 s80, 8192
mm v[120:123],v[212:215],v[148:151],v[120:123]
rt v[208:209],v242 offset:16896
rt v[210:211],v242 offset:17920
mm v[124:127],v[220:223],v[148:151],v[124:127]
s_cmp_eq_u32 s73, 0
s_cselect_b32 s80, -8192, s80
mm v[124:127],v[216:219],a[124:127],v[124:127]
ld v[148:151],v246,s[52:55],s91 offen offset:1024 sc1
mm v[92:95],v[216:219],a[120:123],v[92:95]
s_add_u32 s83, s83, 4294886912
s_lshl_b32 s92, s76, 21
mm v[92:95],v[220:223],v[144:147],v[92:95]
rt v[212:213],v242 offset:25344
rt v[214:215],v242 offset:26368
mm v[60:63],v[220:223],v[136:139],v[60:63]
ds_write_b128 v238, a[12:15] offset:6144
mm v[60:63],v[216:219],v[156:159],v[60:63]
st a[16:19],v255,s[48:51],s18 offen offset:3072 sc1
mm v[28:31],v[216:219],v[152:155],v[28:31]
wt vmcnt(13) lgkmcnt(5)
s_add_u32 s92, s92, 54525952
mm v[28:31],v[220:223],v[132:135],v[28:31]
rt v[216:217],v243 offset:16896
rt v[218:219],v243 offset:17920
mm v[0:3],v[192:195],v[160:163],v[0:3]
ar v224,a40
ar v225,a41
)ASM" R"ASM(mm v[0:3],v[196:199],v[176:179],v[0:3]
ld v[132:135],v247,s[52:55],s91 offen offset:1024 sc1
mm v[32:35],v[196:199],v[180:183],v[32:35]
ar v226,a42
ar v227,a43
mm v[32:35],v[192:195],v[164:167],v[32:35]
rt v[220:221],v243 offset:25344
rt v[222:223],v243 offset:26368
mm v[64:67],v[192:195],v[168:171],v[64:67]
ar v228,a44
ar v229,a45
mm v[64:67],v[196:199],v[184:187],v[64:67]
ld a[96:99],v244,s[52:55],s91 offen offset:2048 sc1
mm v[96:99],v[196:199],v[188:191],v[96:99]
ar v230,a46
ar v231,a47
mm v[96:99],v[192:195],v[172:175],v[96:99]
rt v[196:197],v240 offset:17024
rt v[198:199],v240 offset:18048
mm v[100:103],v[200:203],v[172:175],v[100:103]
cv v248,v224,v225
cv v249,v226,v227
mm v[100:103],v[204:207],v[188:191],v[100:103]
ld a[100:103],v245,s[52:55],s91 offen offset:2048 sc1
mm v[68:71],v[204:207],v[184:187],v[68:71]
cv v250,v228,v229
cv v251,v230,v231
mm v[68:71],v[200:203],v[168:171],v[68:71]
rt v[192:193],v240 offset:25472
rt v[194:195],v240 offset:26496
mm v[36:39],v[200:203],v[164:167],v[36:39]
aw a40,v248
aw a41,v249
mm v[36:39],v[204:207],v[180:183],v[36:39]
ld a[104:107],v246,s[52:55],s91 offen offset:2048 sc1
mm v[4:7],v[204:207],v[176:179],v[4:7]
wt lgkmcnt(4)
aw a42,v250
mm v[4:7],v[200:203],v[160:163],v[4:7]
rt v[204:205],v241 offset:17024
rt v[206:207],v241 offset:18048
mm v[8:11],v[208:211],v[160:163],v[8:11]
aw a43,v251
mm v[8:11],v[212:215],v[176:179],v[8:11]
ld a[108:111],v247,s[52:55],s91 offen offset:2048 sc1
mm v[40:43],v[212:215],v[180:183],v[40:43]
s_add_u32 s44, s26, s92
s_addc_u32 s45, s27, 0
mm v[40:43],v[208:211],v[164:167],v[40:43]
rt v[200:201],v241 offset:25472
rt v[202:203],v241 offset:26496
mm v[72:75],v[208:211],v[168:171],v[72:75]
s_and_b32 s45, s45, 0xffff
s_mov_b32 s46, 2097536
mm v[72:75],v[212:215],v[184:187],v[72:75]
s_mov_b32 s95, 4096
s_mov_b32 s96, 8192
mm v[104:107],v[212:215],v[188:191],v[104:107]
s_mov_b32 s18, 0
s_mov_b32 s97, 12288
mm v[104:107],v[208:211],v[172:175],v[104:107]
rt v[212:213],v242 offset:17024
rt v[214:215],v242 offset:18048
mm v[108:111],v[216:219],v[172:175],v[108:111]
s_mul_i32 s92, s3, 196608
s_sub_u32 s93, 1376256, s92
mm v[108:111],v[220:223],v[188:191],v[108:111]
st a[20:23],v255,s[48:51],s18 offen offset:0 sc1
mm v[76:79],v[220:223],v[184:187],v[76:79]
mm v[76:79],v[216:219],v[168:171],v[76:79]
rt v[208:209],v242 offset:25472
rt v[210:211],v242 offset:26496
mm v[44:47],v[216:219],v[164:167],v[44:47]
ds_write_b128 v238, a[16:19] offset:7168
mm v[44:47],v[220:223],v[180:183],v[44:47]
st a[24:27],v255,s[48:51],s18 offen offset:1024 sc1
mm v[12:15],v[220:223],v[176:179],v[12:15]
wt lgkmcnt(5)
mm v[12:15],v[216:219],v[160:163],v[12:15]
rt v[220:221],v243 offset:17024
rt v[222:223],v243 offset:18048
mm v[16:19],v[196:199],v[160:163],v[16:19]
ar v224,a48
ar v225,a49
mm v[16:19],v[192:195],v[176:179],v[16:19]
ld a[112:115],v244,s[52:55],s91 offen offset:3072 sc1
mm v[48:51],v[192:195],v[180:183],v[48:51]
ar v226,a50
ar v227,a51
mm v[48:51],v[196:199],v[164:167],v[48:51]
rt v[216:217],v243 offset:25472
rt v[218:219],v243 offset:26496
mm v[80:83],v[196:199],v[168:171],v[80:83]
ar v228,a52
ar v229,a53
mm v[80:83],v[192:195],v[184:187],v[80:83]
ld a[116:119],v245,s[52:55],s91 offen offset:3072 sc1
mm v[112:115],v[192:195],v[188:191],v[112:115]
ar v230,a54
ar v231,a55
)ASM" R"ASM(mm v[112:115],v[196:199],v[172:175],v[112:115]
cv v248,v224,v225
cv v249,v226,v227
mm v[116:119],v[204:207],v[172:175],v[116:119]
wt lgkmcnt(0)
s_barrier
mm v[116:119],v[200:203],v[188:191],v[116:119]
ld a[120:123],v246,s[52:55],s91 offen offset:3072 sc1
mm v[84:87],v[200:203],v[184:187],v[84:87]
rt v[192:193],v240 offset:33792
rt v[194:195],v240 offset:34816
mm v[84:87],v[204:207],v[168:171],v[84:87]
rt v[196:197],v240 offset:42240
rt v[198:199],v240 offset:43264
mm v[52:55],v[204:207],v[164:167],v[52:55]
ds_write_b128 v238, a[20:23] offset:32768
mm v[52:55],v[200:203],v[180:183],v[52:55]
ld a[124:127],v247,s[52:55],s91 offen offset:3072 sc1
mm v[20:23],v[200:203],v[176:179],v[20:23]
cv v250,v228,v229
cv v251,v230,v231
mm v[20:23],v[204:207],v[160:163],v[20:23]
rt v[200:201],v241 offset:33792
rt v[202:203],v241 offset:34816
mm v[24:27],v[212:215],v[160:163],v[24:27]
aw a44,v248
aw a45,v249
mm v[24:27],v[208:211],v[176:179],v[24:27]
aw a46,v250
aw a47,v251
mm v[56:59],v[208:211],v[180:183],v[56:59]
mm v[56:59],v[212:215],v[164:167],v[56:59]
rt v[204:205],v241 offset:42240
rt v[206:207],v241 offset:43264
mm v[88:91],v[212:215],v[168:171],v[88:91]
mm v[88:91],v[208:211],v[184:187],v[88:91]
mm v[120:123],v[208:211],v[188:191],v[120:123]
mm v[120:123],v[212:215],v[172:175],v[120:123]
rt v[208:209],v242 offset:33792
rt v[210:211],v242 offset:34816
mm v[124:127],v[220:223],v[172:175],v[124:127]
mm v[124:127],v[216:219],v[188:191],v[124:127]
st a[28:31],v255,s[48:51],s18 offen offset:2048 sc1
mm v[92:95],v[216:219],v[184:187],v[92:95]
mm v[92:95],v[220:223],v[168:171],v[92:95]
rt v[212:213],v242 offset:42240
rt v[214:215],v242 offset:43264
mm v[60:63],v[220:223],v[164:167],v[60:63]
ds_write_b128 v238, a[24:27] offset:33792
mm v[60:63],v[216:219],v[180:183],v[60:63]
st a[32:35],v255,s[48:51],s18 offen offset:3072 sc1
mm v[28:31],v[216:219],v[176:179],v[28:31]
wt vmcnt(12) lgkmcnt(5)
mm v[28:31],v[220:223],v[160:163],v[28:31]
rt v[216:217],v243 offset:33792
rt v[218:219],v243 offset:34816
mm v[0:3],v[192:195],v[128:131],v[0:3]
ar v224,a56
ar v225,a57
mm v[0:3],v[196:199],a[200:203],v[0:3]
ar v226,a58
ar v227,a59
mm v[32:35],v[196:199],a[204:207],v[32:35]
ar v228,a60
ar v229,a61
mm v[32:35],v[192:195],v[140:143],v[32:35]
rt v[220:221],v243 offset:42240
rt v[222:223],v243 offset:43264
mm v[64:67],v[192:195],a[192:195],v[64:67]
ar v230,a62
ar v231,a63
mm v[64:67],v[196:199],v[148:151],v[64:67]
cv v248,v224,v225
cv v249,v226,v227
mm v[96:99],v[196:199],v[132:135],v[96:99]
cv v250,v228,v229
cv v251,v230,v231
mm v[96:99],v[192:195],a[196:199],v[96:99]
rt v[196:197],v240 offset:33920
rt v[198:199],v240 offset:34944
mm v[100:103],v[200:203],a[196:199],v[100:103]
aw a48,v248
aw a49,v249
mm v[100:103],v[204:207],v[132:135],v[100:103]
aw a50,v250
aw a51,v251
mm v[68:71],v[204:207],v[148:151],v[68:71]
v_add_u32 v244, s92, v244
v_add_u32 v246, s93, v246
mm v[68:71],v[200:203],a[192:195],v[68:71]
rt v[192:193],v240 offset:42368
rt v[194:195],v240 offset:43392
mm v[36:39],v[200:203],v[140:143],v[36:39]
ds_write_b128 v238, a[28:31] offset:34816
mm v[36:39],v[204:207],a[204:207],v[36:39]
v_add_u32 v245, 0x1000, v244
v_add_u32 v247, 0x1000, v246
mm v[4:7],v[204:207],a[200:203],v[4:7]
wt lgkmcnt(5)
s_lshl_b32 s91, s72, 13
)ASM" R"ASM(mm v[4:7],v[200:203],v[128:131],v[4:7]
rt v[204:205],v241 offset:33920
rt v[206:207],v241 offset:34944
mm v[8:11],v[208:211],v[128:131],v[8:11]
s_lshl4_add_u32 s92, s76, s3
s_lshl3_add_u32 s92, s92, s20
mm v[8:11],v[212:215],a[200:203],v[8:11]
s_bitset1_b32 s20, 30
mm v[40:43],v[212:215],a[204:207],v[40:43]
mm v[40:43],v[208:211],v[140:143],v[40:43]
rt v[200:201],v241 offset:42368
rt v[202:203],v241 offset:43392
mm v[72:75],v[208:211],a[192:195],v[72:75]
mm v[72:75],v[212:215],v[148:151],v[72:75]
mm v[104:107],v[212:215],v[132:135],v[104:107]
s_mov_b32 s18, 4096
mm v[104:107],v[208:211],a[196:199],v[104:107]
rt v[212:213],v242 offset:33920
rt v[214:215],v242 offset:34944
mm v[108:111],v[216:219],a[196:199],v[108:111]
mm v[108:111],v[220:223],v[132:135],v[108:111]
st a[36:39],v255,s[48:51],s18 offen offset:0 sc1
mm v[76:79],v[220:223],v[148:151],v[76:79]
mm v[76:79],v[216:219],a[192:195],v[76:79]
rt v[208:209],v242 offset:42368
rt v[210:211],v242 offset:43392
mm v[44:47],v[216:219],v[140:143],v[44:47]
ds_write_b128 v238, a[32:35] offset:35840
mm v[44:47],v[220:223],a[204:207],v[44:47]
st a[40:43],v255,s[48:51],s18 offen offset:1024 sc1
mm v[12:15],v[220:223],a[200:203],v[12:15]
wt lgkmcnt(5)
mm v[12:15],v[216:219],v[128:131],v[12:15]
rt v[220:221],v243 offset:33920
rt v[222:223],v243 offset:34944
mm v[16:19],v[196:199],v[128:131],v[16:19]
s_mov_b64 s[52:53], s[86:87]
mm v[16:19],v[192:195],a[200:203],v[16:19]
s_bitcmp1_b32 21, s73
s_cselect_b32 s54, 16384, 0
s_cselect_b32 s86, s88, 1073741824
mm v[48:51],v[192:195],a[204:207],v[48:51]
s_lshl_b32 s89, s3, 14
v_add_u32 v248, s89, v255
mm v[48:51],v[196:199],v[140:143],v[48:51]
rt v[216:217],v243 offset:42368
rt v[218:219],v243 offset:43392
mm v[80:83],v[196:199],a[192:195],v[80:83]
ds_write_b128 v238, a[36:39] offset:36864
mm v[80:83],v[192:195],v[148:151],v[80:83]
s_add_u32 s86, s86, 262144
mm v[112:115],v[192:195],v[132:135],v[112:115]
s_cmp_eq_u32 s73, 0
s_cselect_b32 s86, s92, s86
s_cselect_b32 s92, 1073741824, s92
mm v[112:115],v[196:199],a[196:199],v[112:115]
s_mov_b32 s87, s92
s_add_u32 s89, s89, 16384
mm v[116:119],v[204:207],a[196:199],v[116:119]
wt lgkmcnt(1)
s_barrier
mm v[116:119],v[200:203],v[132:135],v[116:119]
s_lshl_b32 m0, s3, 12
s_add_u32 m0, m0, 131072
mm v[84:87],v[200:203],v[148:151],v[84:87]
rt v[192:193],v240 offset:50688
rt v[194:195],v240 offset:51712
mm v[84:87],v[204:207],a[192:195],v[84:87]
rt v[196:197],v240 offset:59136
rt v[198:199],v240 offset:60160
mm v[52:55],v[204:207],v[140:143],v[52:55]
ds_write_b128 v238, a[40:43] offset:37888
mm v[52:55],v[200:203],a[204:207],v[52:55]
v_bfe_u32 v251, v255, 2, 5
v_and_b32 v224, 0x380, v255
mm v[20:23],v[200:203],a[200:203],v[20:23]
v_add_u32 v251, v251, v224
v_and_b32 v224, 16, v255
mm v[20:23],v[204:207],v[128:131],v[20:23]
rt v[200:201],v241 offset:50688
rt v[202:203],v241 offset:51712
mm v[24:27],v[212:215],v[128:131],v[24:27]
v_cmp_eq_u32 vcc, 0, v224
mm v[24:27],v[208:211],a[200:203],v[24:27]
v_cndmask_b32 v249, v253, v252, vcc
mm v[56:59],v[208:211],a[204:207],v[56:59]
mm v[56:59],v[212:215],v[140:143],v[56:59]
rt v[204:205],v241 offset:59136
rt v[206:207],v241 offset:60160
mm v[88:91],v[212:215],a[192:195],v[88:91]
ds_write_b128 v238, a[44:47] offset:38912
mm v[88:91],v[208:211],v[148:151],v[88:91]
st a[44:47],v255,s[48:51],s18 offen offset:2048 sc1
mm v[120:123],v[208:211],v[132:135],v[120:123]
mm v[120:123],v[212:215],a[196:199],v[120:123]
rt v[208:209],v242 offset:50688
rt v[210:211],v242 offset:51712
.La5_LW1_14:
s_cbranch_execz .La5_LW1_R0
mm v[124:127],v[220:223],a[196:199],v[124:127]
)ASM" R"ASM(mm v[124:127],v[216:219],v[132:135],v[124:127]
mm v[92:95],v[216:219],v[148:151],v[92:95]
mm v[92:95],v[220:223],a[192:195],v[92:95]
rt v[212:213],v242 offset:59136
rt v[214:215],v242 offset:60160
mm v[60:63],v[220:223],v[140:143],v[60:63]
mm v[60:63],v[216:219],a[204:207],v[60:63]
st a[48:51],v255,s[48:51],s18 offen offset:3072 sc1
mm v[28:31],v[216:219],a[200:203],v[28:31]
wt vmcnt(6) lgkmcnt(5)
mm v[28:31],v[220:223],v[128:131],v[28:31]
rt v[216:217],v243 offset:50688
rt v[218:219],v243 offset:51712
mm v[0:3],v[192:195],a[96:99],v[0:3]
mm v[0:3],v[196:199],a[112:115],v[0:3]
mm v[32:35],v[196:199],a[116:119],v[32:35]
mm v[32:35],v[192:195],a[100:103],v[32:35]
rt v[220:221],v243 offset:59136
rt v[222:223],v243 offset:60160
mm v[64:67],v[192:195],a[104:107],v[64:67]
mm v[64:67],v[196:199],a[120:123],v[64:67]
mm v[96:99],v[196:199],a[124:127],v[96:99]
mm v[96:99],v[192:195],a[108:111],v[96:99]
rt v[196:197],v240 offset:50816
rt v[198:199],v240 offset:51840
mm v[100:103],v[200:203],a[108:111],v[100:103]
mm v[100:103],v[204:207],a[124:127],v[100:103]
mm v[68:71],v[204:207],a[120:123],v[68:71]
mm v[68:71],v[200:203],a[104:107],v[68:71]
rt v[192:193],v240 offset:59264
rt v[194:195],v240 offset:60288
mm v[36:39],v[200:203],a[100:103],v[36:39]
mm v[36:39],v[204:207],a[116:119],v[36:39]
mm v[4:7],v[204:207],a[112:115],v[4:7]
wt lgkmcnt(4)
mm v[4:7],v[200:203],a[96:99],v[4:7]
rt v[204:205],v241 offset:50816
rt v[206:207],v241 offset:51840
mm v[8:11],v[208:211],a[96:99],v[8:11]
mm v[8:11],v[212:215],a[112:115],v[8:11]
mm v[40:43],v[212:215],a[116:119],v[40:43]
cv v128,v0,v1
cv v129,v2,v3
mm v[40:43],v[208:211],a[100:103],v[40:43]
rt v[200:201],v241 offset:59264
rt v[202:203],v241 offset:60288
mm v[72:75],v[208:211],a[104:107],v[72:75]
cv v130,v4,v5
cv v131,v6,v7
mm v[72:75],v[212:215],a[120:123],v[72:75]
cv v144,v32,v33
cv v145,v34,v35
mm v[104:107],v[212:215],a[124:127],v[104:107]
cv v146,v36,v37
cv v147,v38,v39
mm v[104:107],v[208:211],a[108:111],v[104:107]
rt v[212:213],v242 offset:50816
rt v[214:215],v242 offset:51840
mm v[108:111],v[216:219],a[108:111],v[108:111]
cv v160,v64,v65
cv v161,v66,v67
mm v[108:111],v[220:223],a[124:127],v[108:111]
cv v162,v68,v69
cv v163,v70,v71
mm v[76:79],v[220:223],a[120:123],v[76:79]
cv v176,v96,v97
cv v177,v98,v99
mm v[76:79],v[216:219],a[104:107],v[76:79]
rt v[208:209],v242 offset:59264
rt v[210:211],v242 offset:60288
mm v[44:47],v[216:219],a[100:103],v[44:47]
cv v178,v100,v101
cv v179,v102,v103
mm v[44:47],v[220:223],a[116:119],v[44:47]
mm v[12:15],v[220:223],a[112:115],v[12:15]
wt lgkmcnt(4)
mm v[12:15],v[216:219],a[96:99],v[12:15]
rt v[220:221],v243 offset:50816
rt v[222:223],v243 offset:51840
mm v[16:19],v[196:199],a[96:99],v[16:19]
v_mul_u32_u24 v241, 0x5555, v224
v_lshrrev_b32 v241, 4, v241
v_add_u32 v241, 0x8888, v241
mm v[16:19],v[192:195],a[112:115],v[16:19]
st v[128:131],v255,s[52:55],0 offen offset:0 sc1
mm v[48:51],v[192:195],a[116:119],v[48:51]
cv v132,v8,v9
cv v133,v10,v11
cv v181,v106,v107
mm v[48:51],v[196:199],a[100:103],v[48:51]
rt v[216:217],v243 offset:59264
rt v[218:219],v243 offset:60288
cv v182,v108,v109
mm v[80:83],v[196:199],a[104:107],v[80:83]
cv v134,v12,v13
cv v135,v14,v15
cv v183,v110,v111
mm v[80:83],v[192:195],a[120:123],v[80:83]
st v[144:147],v255,s[52:55],s95 offen offset:0 sc1
ar v34,a238
)ASM" R"ASM(mm v[112:115],v[192:195],a[124:127],v[112:115]
cv v148,v40,v41
cv v149,v42,v43
ar v35,a239
mm v[112:115],v[196:199],a[108:111],v[112:115]
cv v150,v44,v45
cv v151,v46,v47
v_mov_b32 v36, 0xff800000
mm v[116:119],v[204:207],a[108:111],v[116:119]
wt lgkmcnt(0)
s_barrier
v_mov_b32 v37, 0xff800000
mm v[116:119],v[200:203],a[124:127],v[116:119]
st v[160:163],v255,s[52:55],s96 offen offset:0 sc1
v_mov_b32 v38, 0xff800000
mm v[84:87],v[200:203],a[120:123],v[84:87]
cv v164,v72,v73
cv v165,v74,v75
v_mov_b32 v39, 0xff800000
mm v[84:87],v[204:207],a[104:107],v[84:87]
rd a[64:67],v248 offset:0
rd a[68:71],v248 offset:1024
mm v[52:55],v[204:207],a[100:103],v[52:55]
cv v166,v76,v77
cv v167,v78,v79
mm v[52:55],v[200:203],a[116:119],v[52:55]
st v[176:179],v255,s[52:55],s97 offen offset:0 sc1
mm v[20:23],v[200:203],a[112:115],v[20:23]
s_nop 0
cv v180,v104,v105
mm v[20:23],v[204:207],a[96:99],v[20:23]
rd a[72:75],v248 offset:2048
rd a[76:79],v248 offset:3072
mm v[24:27],v[212:215],a[96:99],v[24:27]
ar v32,a236
ar v33,a237
mm v[24:27],v[208:211],a[112:115],v[24:27]
st v[132:135],v255,s[52:55],0 offen offset:1024 sc1
mm v[56:59],v[208:211],a[116:119],v[56:59]
cv v136,v16,v17
cv v137,v18,v19
mm v[56:59],v[212:215],a[100:103],v[56:59]
rd a[80:83],v248 offset:4096
rd a[84:87],v248 offset:5120
mm v[88:91],v[212:215],a[104:107],v[88:91]
cv v138,v20,v21
cv v139,v22,v23
mm v[88:91],v[208:211],a[120:123],v[88:91]
st v[148:151],v255,s[52:55],s95 offen offset:1024 sc1
mm v[120:123],v[208:211],a[124:127],v[120:123]
cv v152,v48,v49
cv v153,v50,v51
mm v[120:123],v[212:215],a[108:111],v[120:123]
rd a[88:91],v248 offset:6144
rd a[92:95],v248 offset:7168
mm v[124:127],v[220:223],a[108:111],v[124:127]
ds_write_b128 v238, a[48:51] offset:39936
mm v[124:127],v[216:219],a[124:127],v[124:127]
st v[164:167],v255,s[52:55],s96 offen offset:1024 sc1
mm v[92:95],v[216:219],a[120:123],v[92:95]
cv v154,v52,v53
cv v155,v54,v55
mm v[92:95],v[220:223],a[104:107],v[92:95]
cv v168,v80,v81
cv v169,v82,v83
mm v[60:63],v[220:223],a[100:103],v[60:63]
cv v170,v84,v85
cv v171,v86,v87
mm v[60:63],v[216:219],a[116:119],v[60:63]
st v[180:183],v255,s[52:55],s97 offen offset:1024 sc1
mm v[28:31],v[216:219],a[112:115],v[28:31]
cv v184,v112,v113
cv v185,v114,v115
mm v[28:31],v[220:223],a[96:99],v[28:31]
cv v186,v116,v117
cv v187,v118,v119
wt lgkmcnt(1)
mm v[208:211],a[64:67],v[160:163],v[32:35]
mm v[216:219],a[64:67],v[176:179],0
st v[136:139],v255,s[52:55],0 offen offset:2048 sc1
mm v[212:215],a[80:83],v[160:163],v[36:39]
cv v172,v88,v89
cv v173,v90,v91
cv v174,v92,v93
mm v[220:223],a[80:83],v[176:179],v[32:35]
cv v175,v94,v95
cv v188,v120,v121
cv v189,v122,v123
mm v[208:211],a[68:71],v[164:167],v[208:211]
cv v190,v124,v125
cv v191,v126,v127
cv v140,v24,v25
mm v[216:219],a[68:71],v[180:183],v[216:219]
st v[152:155],v255,s[52:55],s95 offen offset:2048 sc1
cv v141,v26,v27
mm v[212:215],a[84:87],v[164:167],v[212:215]
cv v142,v28,v29
cv v143,v30,v31
cv v156,v56,v57
mm v[220:223],a[84:87],v[180:183],v[220:223]
)ASM" R"ASM(cv v157,v58,v59
cv v158,v60,v61
cv v159,v62,v63
mm v[208:211],a[72:75],v[168:171],v[208:211]
mm v[216:219],a[72:75],v[184:187],v[216:219]
st v[168:171],v255,s[52:55],s96 offen offset:2048 sc1
mm v[212:215],a[88:91],v[168:171],v[212:215]
mm v[220:223],a[88:91],v[184:187],v[220:223]
mm v[208:211],a[76:79],v[172:175],v[208:211]
mm v[216:219],a[76:79],v[188:191],v[216:219]
st v[184:187],v255,s[52:55],s97 offen offset:2048 sc1
mm v[212:215],a[92:95],v[172:175],v[212:215]
rd a[64:67],v248 offset:16384
mm v[220:223],a[92:95],v[188:191],v[220:223]
rd a[96:99],v248 offset:8192
ex v0,v208
wt vmcnt(12)
buffer_store_dwordx2 v[252:253], off, s[64:67], s87 sc1
mm v[64:67],a[232:235],a[232:235],0
rd a[68:71],v248 offset:17408
ex v1,v209
mm v[68:71],a[232:235],a[232:235],0
rd a[100:103],v248 offset:9216
st v[140:143],v255,s[52:55],0 offen offset:3072 sc1
mm v[72:75],a[232:235],a[232:235],0
rd a[72:75],v248 offset:18432
ex v2,v210
mm v[76:79],a[232:235],a[232:235],0
rd a[104:107],v248 offset:10240
ex v3,v211
mm v[80:83],a[232:235],a[232:235],0
rd a[76:79],v248 offset:19456
ex v8,v216
mm v[84:87],a[232:235],a[232:235],0
rd a[108:111],v248 offset:11264
st v[156:159],v255,s[52:55],s95 offen offset:3072 sc1
mm v[88:91],a[232:235],a[232:235],0
rd a[80:83],v248 offset:20480
ex v9,v217
mm v[92:95],a[232:235],a[232:235],0
rd a[112:115],v248 offset:12288
ex v10,v218
mm v[96:99],a[232:235],a[232:235],0
rd a[84:87],v248 offset:21504
ex v11,v219
mm v[100:103],a[232:235],a[232:235],0
rd a[116:119],v248 offset:13312
st v[172:175],v255,s[52:55],s96 offen offset:3072 sc1
mm v[104:107],a[232:235],a[232:235],0
rd a[88:91],v248 offset:22528
ex v4,v212
mm v[108:111],a[232:235],a[232:235],0
rd a[120:123],v248 offset:14336
ex v5,v213
mm v[112:115],a[232:235],a[232:235],0
rd a[92:95],v248 offset:23552
ex v6,v214
mm v[116:119],a[232:235],a[232:235],0
rd a[124:127],v248 offset:15360
st v[188:191],v255,s[52:55],s97 offen offset:3072 sc1
ex v7,v215
mm v[120:123],a[232:235],a[232:235],0
ex v12,v220
mm v[124:127],a[232:235],a[232:235],0
aw a192,0
aw a193,0
aw a194,0
aw a195,0
aw a196,0
aw a197,0
aw a198,0
aw a199,0
v_add_u32 v248, 0x4000, v248
s_cmp_eq_u32 s3, 3
s_cbranch_scc1 .La5_LA_FH
.p2align 6
; ---- attention, 32 keys per window: S = K Q^T (causal mask through SrcC), P = exp2(S), O += V^T P, row sums l += 1 P (one sparse MFMA for both tiles); here the later 32 rows only
.La5_LA_HH:
wt lgkmcnt(1)
mm v[208:211],a[64:67],v[160:163],0
ex v13,v221
mm v[208:211],a[68:71],v[164:167],v[208:211]
cv v232,v0,v1
cv v233,v2,v3
mm v[208:211],a[72:75],v[168:171],v[208:211]
ex v14,v222
mm v[208:211],a[76:79],v[172:175],v[208:211]
cv v236,v8,v9
cv v237,v10,v11
mm v[216:219],a[76:79],v[188:191],0
ex v15,v223
mm v[216:219],a[72:75],v[184:187],v[216:219]
cv v234,v4,v5
cv v235,v6,v7
mm v[216:219],a[68:71],v[180:183],v[216:219]
ex v0,v208
mm v[216:219],a[64:67],v[176:179],v[216:219]
cv v238,v12,v13
cv v239,v14,v15
mm v[212:215],a[80:83],v[160:163],0
)ASM" R"ASM(ex v1,v209
mm v[212:215],a[84:87],v[164:167],v[212:215]
rd a[64:67],v248 offset:16384
rd a[68:71],v248 offset:17408
mm v[212:215],a[88:91],v[168:171],v[212:215]
ex v2,v210
mm v[212:215],a[92:95],v[172:175],v[212:215]
rd a[72:75],v248 offset:18432
rd a[76:79],v248 offset:19456
mm v[220:223],a[92:95],v[188:191],0
ex v3,v211
mm v[220:223],a[88:91],v[184:187],v[220:223]
rd a[92:95],v248 offset:23552
mm v[220:223],a[84:87],v[180:183],v[220:223]
ex v8,v216
mm v[220:223],a[80:83],v[176:179],v[220:223]
wt lgkmcnt(5)
rd a[88:91],v248 offset:22528
mm v[64:67],a[96:99],v[232:235],v[64:67]
ex v9,v217
mm v[96:99],a[96:99],v[236:239],v[96:99]
rd a[96:99],v248 offset:8192
rd a[84:87],v248 offset:21504
mm v[100:103],a[100:103],v[236:239],v[100:103]
ex v10,v218
mm v[68:71],a[100:103],v[232:235],v[68:71]
rd a[100:103],v248 offset:9216
rd a[80:83],v248 offset:20480
mm v[72:75],a[104:107],v[232:235],v[72:75]
ex v11,v219
mm v[104:107],a[104:107],v[236:239],v[104:107]
rd a[104:107],v248 offset:10240
mm v[108:111],a[108:111],v[236:239],v[108:111]
ex v4,v212
mm v[76:79],a[108:111],v[232:235],v[76:79]
rd a[108:111],v248 offset:11264
s_add_u32 s85, s85, 1
mm v[80:83],a[112:115],v[232:235],v[80:83]
ex v5,v213
mm v[112:115],a[112:115],v[236:239],v[112:115]
rd a[112:115],v248 offset:12288
s_add_u32 s89, s89, 32768
mm v[116:119],a[116:119],v[236:239],v[116:119]
ex v6,v214
mm v[84:87],a[116:119],v[232:235],v[84:87]
rd a[116:119],v248 offset:13312
s_add_u32 s92, s85, s3
mm v[88:91],a[120:123],v[232:235],v[88:91]
ex v7,v215
mm v[120:123],a[120:123],v[236:239],v[120:123]
rd a[120:123],v248 offset:14336
buffer_load_dword v250, v251, s[64:67], s20 offen sc1
mm v[124:127],a[124:127],v[236:239],v[124:127]
ex v12,v220
mm v[92:95],a[124:127],v[232:235],v[92:95]
rd a[124:127],v248 offset:15360
s_barrier
v_smfmac_f32_16x16x64_bf16 a[196:199], a[208:211], v[232:239], v241
wt lgkmcnt(6)
mm v[208:211],a[64:67],v[160:163],0
ex v13,v221
mm v[208:211],a[68:71],v[164:167],v[208:211]
cv v232,v0,v1
cv v233,v2,v3
mm v[208:211],a[72:75],v[168:171],v[208:211]
ex v14,v222
mm v[208:211],a[76:79],v[172:175],v[208:211]
cv v236,v8,v9
cv v237,v10,v11
mm v[216:219],a[76:79],v[188:191],0
ex v15,v223
mm v[216:219],a[72:75],v[184:187],v[216:219]
cv v234,v4,v5
cv v235,v6,v7
mm v[216:219],a[68:71],v[180:183],v[216:219]
ex v0,v208
mm v[216:219],a[64:67],v[176:179],v[216:219]
cv v238,v12,v13
cv v239,v14,v15
mm v[212:215],a[80:83],v[160:163],0
ex v1,v209
mm v[212:215],a[84:87],v[164:167],v[212:215]
rd a[64:67],v248 offset:32768
rd a[68:71],v248 offset:33792
mm v[212:215],a[88:91],v[168:171],v[212:215]
ex v2,v210
mm v[212:215],a[92:95],v[172:175],v[212:215]
rd a[72:75],v248 offset:34816
rd a[76:79],v248 offset:35840
mm v[220:223],a[92:95],v[188:191],0
ex v3,v211
mm v[220:223],a[88:91],v[184:187],v[220:223]
rd a[92:95],v248 offset:39936
s_bitcmp1_b64 s[98:99], s85
mm v[220:223],a[84:87],v[180:183],v[220:223]
ex v8,v216
mm v[220:223],a[80:83],v[176:179],v[220:223]
wt lgkmcnt(5)
rd a[88:91],v248 offset:38912
mm v[64:67],a[96:99],v[232:235],v[64:67]
)ASM" R"ASM(ex v9,v217
mm v[96:99],a[96:99],v[236:239],v[96:99]
rd a[96:99],v248 offset:24576
rd a[84:87],v248 offset:37888
mm v[100:103],a[100:103],v[236:239],v[100:103]
ex v10,v218
mm v[68:71],a[100:103],v[232:235],v[68:71]
rd a[100:103],v248 offset:25600
rd a[80:83],v248 offset:36864
mm v[72:75],a[104:107],v[232:235],v[72:75]
ex v11,v219
mm v[104:107],a[104:107],v[236:239],v[104:107]
rd a[104:107],v248 offset:26624
mm v[108:111],a[108:111],v[236:239],v[108:111]
ex v4,v212
mm v[76:79],a[108:111],v[232:235],v[76:79]
rd a[108:111],v248 offset:27648
mm v[80:83],a[112:115],v[232:235],v[80:83]
ex v5,v213
mm v[112:115],a[112:115],v[236:239],v[112:115]
rd a[112:115],v248 offset:28672
mm v[116:119],a[116:119],v[236:239],v[116:119]
ex v6,v214
mm v[84:87],a[116:119],v[232:235],v[84:87]
rd a[116:119],v248 offset:29696
wt vmcnt(1)
buffer_store_dwordx2 v[252:253], off, s[64:67], s86 sc1
s_mov_b32 s86, 1073741824
s_bitset0_b32 s20, 30
mm v[88:91],a[120:123],v[232:235],v[88:91]
ex v7,v215
mm v[120:123],a[120:123],v[236:239],v[120:123]
rd a[120:123],v248 offset:30720
mm v[124:127],a[124:127],v[236:239],v[124:127]
v_exp_f32_e64 v12, v220
mm v[92:95],a[124:127],v[232:235],v[92:95]
rd a[124:127],v248 offset:31744
v_smfmac_f32_16x16x64_bf16 a[196:199], a[208:211], v[232:239], v241
v_add_u32 v248, 0x8000, v248
s_cbranch_scc0 .La5_LA_HH1
s_call_b64 s[24:25], .La5_LA_EVENT
s_add_u32 s92, s85, s3
.La5_LA_HH1:
s_cmp_lt_u32 s92, 3
s_cbranch_scc1 .La5_LA_HH
; ---- first window with both row groups (diagonal of the earlier group)
.La5_LA_FH:
.La5_LW1_15:
s_cbranch_execz .La5_LW1_R0
wt lgkmcnt(9)
mm v[192:195],a[64:67],v[128:131],v[32:35]
ex v13,v221
mm v[192:195],a[68:71],v[132:135],v[192:195]
cv v232,v0,v1
cv v233,v2,v3
mm v[192:195],a[72:75],v[136:139],v[192:195]
ex v14,v222
mm v[192:195],a[76:79],v[140:143],v[192:195]
ld v255,s[60:63],s17 offen offset:0 sc1 lds
mm v[200:203],a[76:79],v[156:159],0
ex v15,v223
mm v[200:203],a[72:75],v[152:155],v[200:203]
cv v236,v8,v9
cv v237,v10,v11
mm v[200:203],a[68:71],v[148:151],v[200:203]
ex v192,v192
mm v[200:203],a[64:67],v[144:147],v[200:203]
wt lgkmcnt(1)
cv v234,v4,v5
mm v[196:199],a[80:83],v[128:131],v[36:39]
ex v193,v193
mm v[196:199],a[84:87],v[132:135],v[196:199]
cv v224,v192,v193
s_add_u32 s85, s85, 1
mm v[196:199],a[88:91],v[136:139],v[196:199]
ex v194,v194
mm v[196:199],a[92:95],v[140:143],v[196:199]
ld v255,s[60:63],s17 offen offset:1024 sc1 lds
mm v[204:207],a[92:95],v[156:159],v[32:35]
ex v195,v195
mm v[204:207],a[88:91],v[152:155],v[204:207]
cv v225,v194,v195
s_add_u32 s89, s89, 16384
mm v[204:207],a[84:87],v[148:151],v[204:207]
ex v200,v200
mm v[204:207],a[80:83],v[144:147],v[204:207]
ld v255,s[60:63],s17 offen offset:2048 sc1 lds
mm v[208:211],a[64:67],v[160:163],0
ex v201,v201
mm v[208:211],a[68:71],v[164:167],v[208:211]
cv v228,v200,v201
s_and_b32 s89, s89, 0x1ffff
mm v[208:211],a[72:75],v[168:171],v[208:211]
ex v202,v202
mm v[208:211],a[76:79],v[172:175],v[208:211]
ld v255,s[60:63],s17 offen offset:3072 sc1 lds
mm v[216:219],a[76:79],v[188:191],0
ex v203,v203
mm v[216:219],a[72:75],v[184:187],v[216:219]
cv v229,v202,v203
)ASM" R"ASM(mm v[216:219],a[68:71],v[180:183],v[216:219]
ex v196,v196
mm v[216:219],a[64:67],v[176:179],v[216:219]
cv v235,v6,v7
cv v238,v12,v13
mm v[212:215],a[80:83],v[160:163],0
ex v197,v197
s_add_u32 s17, s17, s80
mm v[212:215],a[84:87],v[164:167],v[212:215]
cv v226,v196,v197
s_add_u32 m0, m0, 16384
s_and_b32 m0, m0, s78
mm v[212:215],a[88:91],v[168:171],v[212:215]
ex v198,v198
mm v[212:215],a[92:95],v[172:175],v[212:215]
cv v239,v14,v15
wt lgkmcnt(0)
mm v[220:223],a[92:95],v[188:191],0
ex v199,v199
mm v[220:223],a[88:91],v[184:187],v[220:223]
cv v227,v198,v199
mm v[220:223],a[84:87],v[180:183],v[220:223]
ex v204,v204
mm v[220:223],a[80:83],v[176:179],v[220:223]
wt vmcnt(10)
s_barrier
mm v[64:67],a[96:99],v[232:235],v[64:67]
ex v205,v205
mm v[96:99],a[96:99],v[236:239],v[96:99]
rd a[96:99],v248 offset:8192
cv v230,v204,v205
mm v[100:103],a[100:103],v[236:239],v[100:103]
ex v206,v206
mm v[68:71],a[100:103],v[232:235],v[68:71]
rd a[100:103],v248 offset:9216
buffer_load_dword v250, v251, s[64:67], s20 offen sc1
mm v[72:75],a[104:107],v[232:235],v[72:75]
ex v207,v207
mm v[104:107],a[104:107],v[236:239],v[104:107]
rd a[104:107],v248 offset:10240
cv v231,v206,v207
mm v[108:111],a[108:111],v[236:239],v[108:111]
ex v208,v208
mm v[76:79],a[108:111],v[232:235],v[76:79]
rd a[108:111],v248 offset:11264
mm v[80:83],a[112:115],v[232:235],v[80:83]
ex v209,v209
mm v[112:115],a[112:115],v[236:239],v[112:115]
rd a[112:115],v248 offset:12288
mm v[116:119],a[116:119],v[236:239],v[116:119]
ex v210,v210
mm v[84:87],a[116:119],v[232:235],v[84:87]
rd a[116:119],v248 offset:13312
mm v[88:91],a[120:123],v[232:235],v[88:91]
ex v211,v211
mm v[120:123],a[120:123],v[236:239],v[120:123]
rd a[120:123],v248 offset:14336
s_bitcmp1_b64 s[98:99], s85
mm v[124:127],a[124:127],v[236:239],v[124:127]
ex v216,v216
mm v[92:95],a[124:127],v[232:235],v[92:95]
rd a[124:127],v248 offset:15360
v_or_b32 v248, s89, v255
.La5_LW2_0:
s_cbranch_execz .La5_LW2_R0
v_smfmac_f32_16x16x64_bf16 a[196:199], a[208:211], v[232:239], v241
wt lgkmcnt(4)
v_smfmac_f32_16x16x64_bf16 a[192:195], a[208:211], v[224:231], v241
rd a[64:67],v248 offset:0
mm v[0:3],a[96:99],v[224:227],0
ex v217,v217
mm v[32:35],a[96:99],v[228:231],0
rd a[68:71],v248 offset:1024
cv v232,v208,v209
mm v[36:39],a[100:103],v[228:231],0
ex v218,v218
mm v[4:7],a[100:103],v[224:227],0
rd a[72:75],v248 offset:2048
cv v233,v210,v211
mm v[8:11],a[104:107],v[224:227],0
ex v219,v219
mm v[40:43],a[104:107],v[228:231],0
rd a[76:79],v248 offset:3072
cv v236,v216,v217
mm v[44:47],a[108:111],v[228:231],0
ex v212,v212
mm v[12:15],a[108:111],v[224:227],0
rd a[80:83],v248 offset:4096
wt lgkmcnt(5)
mm v[16:19],a[112:115],v[224:227],0
ex v213,v213
mm v[48:51],a[112:115],v[228:231],0
rd a[84:87],v248 offset:5120
cv v237,v218,v219
mm v[52:55],a[116:119],v[228:231],0
ex v214,v214
mm v[20:23],a[116:119],v[224:227],0
rd a[88:91],v248 offset:6144
cv v234,v212,v213
mm v[24:27],a[120:123],v[224:227],0
)ASM" R"ASM(ex v215,v215
mm v[56:59],a[120:123],v[228:231],0
rd a[92:95],v248 offset:7168
cv v235,v214,v215
mm v[60:63],a[124:127],v[228:231],0
ex v220,v220
mm v[28:31],a[124:127],v[224:227],0
wt vmcnt(5)
buffer_store_dwordx2 v[252:253], off, s[64:67], s86 sc1
s_mov_b32 s86, 1073741824
s_bitset0_b32 s20, 30
s_cbranch_scc1 .La5_LA_FE0
.La5_LA_FS0:
wt lgkmcnt(4)
mm v[192:195],a[64:67],v[128:131],0
ex v221,v221
mm v[192:195],a[68:71],v[132:135],v[192:195]
cv v238,v220,v221
s_add_u32 s85, s85, 1
mm v[192:195],a[72:75],v[136:139],v[192:195]
ex v222,v222
mm v[192:195],a[76:79],v[140:143],v[192:195]
ld v255,s[60:63],s17 offen offset:0 sc1 lds
mm v[200:203],a[76:79],v[156:159],0
ex v223,v223
mm v[200:203],a[72:75],v[152:155],v[200:203]
cv v239,v222,v223
s_add_u32 s89, s89, 16384
mm v[200:203],a[68:71],v[148:151],v[200:203]
ex v192,v192
mm v[200:203],a[64:67],v[144:147],v[200:203]
wt lgkmcnt(0)
s_and_b32 s89, s89, 0x1ffff
mm v[196:199],a[80:83],v[128:131],0
ex v193,v193
mm v[196:199],a[84:87],v[132:135],v[196:199]
cv v224,v192,v193
mm v[196:199],a[88:91],v[136:139],v[196:199]
ex v194,v194
mm v[196:199],a[92:95],v[140:143],v[196:199]
ld v255,s[60:63],s17 offen offset:1024 sc1 lds
mm v[204:207],a[92:95],v[156:159],0
ex v195,v195
mm v[204:207],a[88:91],v[152:155],v[204:207]
cv v225,v194,v195
mm v[204:207],a[84:87],v[148:151],v[204:207]
ex v200,v200
mm v[204:207],a[80:83],v[144:147],v[204:207]
ld v255,s[60:63],s17 offen offset:2048 sc1 lds
mm v[208:211],a[64:67],v[160:163],0
ex v201,v201
mm v[208:211],a[68:71],v[164:167],v[208:211]
cv v228,v200,v201
mm v[208:211],a[72:75],v[168:171],v[208:211]
ex v202,v202
mm v[208:211],a[76:79],v[172:175],v[208:211]
ld v255,s[60:63],s17 offen offset:3072 sc1 lds
mm v[216:219],a[76:79],v[188:191],0
ex v203,v203
mm v[216:219],a[72:75],v[184:187],v[216:219]
cv v229,v202,v203
mm v[216:219],a[68:71],v[180:183],v[216:219]
ex v196,v196
mm v[216:219],a[64:67],v[176:179],v[216:219]
buffer_load_dword a253, v254, s[36:39], s90 offen
mm v[212:215],a[80:83],v[160:163],0
ex v197,v197
mm v[212:215],a[84:87],v[164:167],v[212:215]
cv v226,v196,v197
s_add_u32 s90, s90, s21
mm v[212:215],a[88:91],v[168:171],v[212:215]
ex v198,v198
mm v[212:215],a[92:95],v[172:175],v[212:215]
s_add_u32 s17, s17, s80
s_add_u32 m0, m0, 16384
mm v[220:223],a[92:95],v[188:191],0
ex v199,v199
mm v[220:223],a[88:91],v[184:187],v[220:223]
cv v227,v198,v199
s_and_b32 m0, m0, s78
mm v[220:223],a[84:87],v[180:183],v[220:223]
ex v204,v204
mm v[220:223],a[80:83],v[176:179],v[220:223]
wt vmcnt(11)
s_barrier
mm v[64:67],a[96:99],v[232:235],v[64:67]
ex v205,v205
mm v[96:99],a[96:99],v[236:239],v[96:99]
rd a[96:99],v248 offset:8192
cv v230,v204,v205
mm v[100:103],a[100:103],v[236:239],v[100:103]
ex v206,v206
mm v[68:71],a[100:103],v[232:235],v[68:71]
rd a[100:103],v248 offset:9216
s_cmp_eq_u32 s85, s81
buffer_load_dword v250, v251, s[64:67], s20 offen sc1
mm v[72:75],a[104:107],v[232:235],v[72:75]
ex v207,v207
mm v[104:107],a[104:107],v[236:239],v[104:107]
rd a[104:107],v248 offset:10240
)ASM" R"ASM(cv v231,v206,v207
mm v[108:111],a[108:111],v[236:239],v[108:111]
ex v208,v208
mm v[76:79],a[108:111],v[232:235],v[76:79]
rd a[108:111],v248 offset:11264
s_cselect_b32 s17, s77, s17
mm v[80:83],a[112:115],v[232:235],v[80:83]
ex v209,v209
mm v[112:115],a[112:115],v[236:239],v[112:115]
rd a[112:115],v248 offset:12288
s_cselect_b32 s80, -8192, s80
mm v[116:119],a[116:119],v[236:239],v[116:119]
ex v210,v210
mm v[84:87],a[116:119],v[232:235],v[84:87]
rd a[116:119],v248 offset:13312
mm v[88:91],a[120:123],v[232:235],v[88:91]
ex v211,v211
mm v[120:123],a[120:123],v[236:239],v[120:123]
rd a[120:123],v248 offset:14336
s_bitcmp1_b64 s[98:99], s85
mm v[124:127],a[124:127],v[236:239],v[124:127]
ex v216,v216
mm v[92:95],a[124:127],v[232:235],v[92:95]
rd a[124:127],v248 offset:15360
v_or_b32 v248, s89, v255
v_smfmac_f32_16x16x64_bf16 a[196:199], a[208:211], v[232:239], v241
wt lgkmcnt(4)
v_smfmac_f32_16x16x64_bf16 a[192:195], a[208:211], v[224:231], v241
rd a[64:67],v248 offset:0
mm v[0:3],a[96:99],v[224:227],v[0:3]
ex v217,v217
mm v[32:35],a[96:99],v[228:231],v[32:35]
rd a[68:71],v248 offset:1024
cv v232,v208,v209
mm v[36:39],a[100:103],v[228:231],v[36:39]
ex v218,v218
mm v[4:7],a[100:103],v[224:227],v[4:7]
rd a[72:75],v248 offset:2048
cv v233,v210,v211
mm v[8:11],a[104:107],v[224:227],v[8:11]
ex v219,v219
mm v[40:43],a[104:107],v[228:231],v[40:43]
rd a[76:79],v248 offset:3072
cv v236,v216,v217
mm v[44:47],a[108:111],v[228:231],v[44:47]
ex v212,v212
mm v[12:15],a[108:111],v[224:227],v[12:15]
rd a[80:83],v248 offset:4096
wt lgkmcnt(5)
mm v[16:19],a[112:115],v[224:227],v[16:19]
ex v213,v213
mm v[48:51],a[112:115],v[228:231],v[48:51]
rd a[84:87],v248 offset:5120
cv v237,v218,v219
mm v[52:55],a[116:119],v[228:231],v[52:55]
ex v214,v214
mm v[20:23],a[116:119],v[224:227],v[20:23]
rd a[88:91],v248 offset:6144
cv v234,v212,v213
mm v[24:27],a[120:123],v[224:227],v[24:27]
ex v215,v215
mm v[56:59],a[120:123],v[228:231],v[56:59]
rd a[92:95],v248 offset:7168
cv v235,v214,v215
mm v[60:63],a[124:127],v[228:231],v[60:63]
v_exp_f32_e64 v220, v220
mm v[28:31],a[124:127],v[224:227],v[28:31]
s_cbranch_scc1 .La5_LA_F1E2
.p2align 6
; ---- steady windows
.La5_LA_F1:
wt lgkmcnt(4)
mm v[192:195],a[64:67],v[128:131],0
ex v221,v221
mm v[192:195],a[68:71],v[132:135],v[192:195]
cv v238,v220,v221
s_add_u32 s85, s85, 1
mm v[192:195],a[72:75],v[136:139],v[192:195]
ex v222,v222
mm v[192:195],a[76:79],v[140:143],v[192:195]
ld v255,s[60:63],s17 offen offset:0 sc1 lds
mm v[200:203],a[76:79],v[156:159],0
ex v223,v223
mm v[200:203],a[72:75],v[152:155],v[200:203]
cv v239,v222,v223
s_add_u32 s89, s89, 16384
mm v[200:203],a[68:71],v[148:151],v[200:203]
ex v192,v192
mm v[200:203],a[64:67],v[144:147],v[200:203]
wt lgkmcnt(0)
s_and_b32 s89, s89, 0x1ffff
mm v[196:199],a[80:83],v[128:131],0
ex v193,v193
mm v[196:199],a[84:87],v[132:135],v[196:199]
cv v224,v192,v193
mm v[196:199],a[88:91],v[136:139],v[196:199]
ex v194,v194
mm v[196:199],a[92:95],v[140:143],v[196:199]
ld v255,s[60:63],s17 offen offset:1024 sc1 lds
mm v[204:207],a[92:95],v[156:159],0
)ASM" R"ASM(ex v195,v195
mm v[204:207],a[88:91],v[152:155],v[204:207]
cv v225,v194,v195
mm v[204:207],a[84:87],v[148:151],v[204:207]
ex v200,v200
mm v[204:207],a[80:83],v[144:147],v[204:207]
ld v255,s[60:63],s17 offen offset:2048 sc1 lds
mm v[208:211],a[64:67],v[160:163],0
ex v201,v201
mm v[208:211],a[68:71],v[164:167],v[208:211]
cv v228,v200,v201
mm v[208:211],a[72:75],v[168:171],v[208:211]
ex v202,v202
mm v[208:211],a[76:79],v[172:175],v[208:211]
ld v255,s[60:63],s17 offen offset:3072 sc1 lds
mm v[216:219],a[76:79],v[188:191],0
ex v203,v203
mm v[216:219],a[72:75],v[184:187],v[216:219]
cv v229,v202,v203
mm v[216:219],a[68:71],v[180:183],v[216:219]
ex v196,v196
mm v[216:219],a[64:67],v[176:179],v[216:219]
buffer_load_dword a253, v254, s[36:39], s90 offen
mm v[212:215],a[80:83],v[160:163],0
ex v197,v197
mm v[212:215],a[84:87],v[164:167],v[212:215]
cv v226,v196,v197
s_add_u32 s90, s90, s21
mm v[212:215],a[88:91],v[168:171],v[212:215]
ex v198,v198
mm v[212:215],a[92:95],v[172:175],v[212:215]
s_add_u32 s17, s17, s80
s_add_u32 m0, m0, 16384
mm v[220:223],a[92:95],v[188:191],0
ex v199,v199
mm v[220:223],a[88:91],v[184:187],v[220:223]
cv v227,v198,v199
s_and_b32 m0, m0, s78
mm v[220:223],a[84:87],v[180:183],v[220:223]
ex v204,v204
mm v[220:223],a[80:83],v[176:179],v[220:223]
wt vmcnt(11)
s_barrier
mm v[64:67],a[96:99],v[232:235],v[64:67]
ex v205,v205
mm v[96:99],a[96:99],v[236:239],v[96:99]
rd a[96:99],v248 offset:8192
cv v230,v204,v205
mm v[100:103],a[100:103],v[236:239],v[100:103]
ex v206,v206
mm v[68:71],a[100:103],v[232:235],v[68:71]
rd a[100:103],v248 offset:9216
s_cmp_eq_u32 s85, s81
mm v[72:75],a[104:107],v[232:235],v[72:75]
ex v207,v207
mm v[104:107],a[104:107],v[236:239],v[104:107]
rd a[104:107],v248 offset:10240
cv v231,v206,v207
mm v[108:111],a[108:111],v[236:239],v[108:111]
ex v208,v208
mm v[76:79],a[108:111],v[232:235],v[76:79]
rd a[108:111],v248 offset:11264
s_cselect_b32 s17, s77, s17
mm v[80:83],a[112:115],v[232:235],v[80:83]
ex v209,v209
mm v[112:115],a[112:115],v[236:239],v[112:115]
rd a[112:115],v248 offset:12288
s_cselect_b32 s80, -8192, s80
mm v[116:119],a[116:119],v[236:239],v[116:119]
ex v210,v210
mm v[84:87],a[116:119],v[232:235],v[84:87]
rd a[116:119],v248 offset:13312
mm v[88:91],a[120:123],v[232:235],v[88:91]
ex v211,v211
mm v[120:123],a[120:123],v[236:239],v[120:123]
rd a[120:123],v248 offset:14336
s_bitcmp1_b64 s[98:99], s85
mm v[124:127],a[124:127],v[236:239],v[124:127]
ex v216,v216
mm v[92:95],a[124:127],v[232:235],v[92:95]
rd a[124:127],v248 offset:15360
v_or_b32 v248, s89, v255
v_smfmac_f32_16x16x64_bf16 a[196:199], a[208:211], v[232:239], v241
wt lgkmcnt(4)
v_smfmac_f32_16x16x64_bf16 a[192:195], a[208:211], v[224:231], v241
rd a[64:67],v248 offset:0
mm v[0:3],a[96:99],v[224:227],v[0:3]
ex v217,v217
mm v[32:35],a[96:99],v[228:231],v[32:35]
rd a[68:71],v248 offset:1024
cv v232,v208,v209
mm v[36:39],a[100:103],v[228:231],v[36:39]
ex v218,v218
mm v[4:7],a[100:103],v[224:227],v[4:7]
rd a[72:75],v248 offset:2048
cv v233,v210,v211
mm v[8:11],a[104:107],v[224:227],v[8:11]
ex v219,v219
mm v[40:43],a[104:107],v[228:231],v[40:43]
rd a[76:79],v248 offset:3072
)ASM" R"ASM(cv v236,v216,v217
mm v[44:47],a[108:111],v[228:231],v[44:47]
ex v212,v212
mm v[12:15],a[108:111],v[224:227],v[12:15]
rd a[80:83],v248 offset:4096
wt lgkmcnt(5)
mm v[16:19],a[112:115],v[224:227],v[16:19]
ex v213,v213
mm v[48:51],a[112:115],v[228:231],v[48:51]
rd a[84:87],v248 offset:5120
cv v237,v218,v219
mm v[52:55],a[116:119],v[228:231],v[52:55]
ex v214,v214
mm v[20:23],a[116:119],v[224:227],v[20:23]
rd a[88:91],v248 offset:6144
cv v234,v212,v213
mm v[24:27],a[120:123],v[224:227],v[24:27]
ex v215,v215
mm v[56:59],a[120:123],v[228:231],v[56:59]
rd a[92:95],v248 offset:7168
cv v235,v214,v215
mm v[60:63],a[124:127],v[228:231],v[60:63]
ex v220,v220
mm v[28:31],a[124:127],v[224:227],v[28:31]
s_cbranch_scc1 .La5_LA_F1E2
wt lgkmcnt(4)
mm v[192:195],a[64:67],v[128:131],0
ex v221,v221
mm v[192:195],a[68:71],v[132:135],v[192:195]
cv v238,v220,v221
s_add_u32 s85, s85, 1
mm v[192:195],a[72:75],v[136:139],v[192:195]
ex v222,v222
mm v[192:195],a[76:79],v[140:143],v[192:195]
ld v255,s[60:63],s17 offen offset:0 sc1 lds
mm v[200:203],a[76:79],v[156:159],0
ex v223,v223
mm v[200:203],a[72:75],v[152:155],v[200:203]
cv v239,v222,v223
s_add_u32 s89, s89, 16384
mm v[200:203],a[68:71],v[148:151],v[200:203]
ex v192,v192
mm v[200:203],a[64:67],v[144:147],v[200:203]
wt lgkmcnt(0)
s_and_b32 s89, s89, 0x1ffff
mm v[196:199],a[80:83],v[128:131],0
ex v193,v193
mm v[196:199],a[84:87],v[132:135],v[196:199]
cv v224,v192,v193
mm v[196:199],a[88:91],v[136:139],v[196:199]
ex v194,v194
mm v[196:199],a[92:95],v[140:143],v[196:199]
ld v255,s[60:63],s17 offen offset:1024 sc1 lds
mm v[204:207],a[92:95],v[156:159],0
ex v195,v195
mm v[204:207],a[88:91],v[152:155],v[204:207]
cv v225,v194,v195
mm v[204:207],a[84:87],v[148:151],v[204:207]
ex v200,v200
mm v[204:207],a[80:83],v[144:147],v[204:207]
ld v255,s[60:63],s17 offen offset:2048 sc1 lds
mm v[208:211],a[64:67],v[160:163],0
ex v201,v201
mm v[208:211],a[68:71],v[164:167],v[208:211]
cv v228,v200,v201
mm v[208:211],a[72:75],v[168:171],v[208:211]
ex v202,v202
mm v[208:211],a[76:79],v[172:175],v[208:211]
ld v255,s[60:63],s17 offen offset:3072 sc1 lds
mm v[216:219],a[76:79],v[188:191],0
ex v203,v203
mm v[216:219],a[72:75],v[184:187],v[216:219]
cv v229,v202,v203
mm v[216:219],a[68:71],v[180:183],v[216:219]
ex v196,v196
mm v[216:219],a[64:67],v[176:179],v[216:219]
buffer_load_dword a253, v254, s[36:39], s90 offen
mm v[212:215],a[80:83],v[160:163],0
ex v197,v197
mm v[212:215],a[84:87],v[164:167],v[212:215]
cv v226,v196,v197
s_add_u32 s90, s90, s21
mm v[212:215],a[88:91],v[168:171],v[212:215]
ex v198,v198
mm v[212:215],a[92:95],v[172:175],v[212:215]
s_add_u32 s17, s17, s80
s_add_u32 m0, m0, 16384
mm v[220:223],a[92:95],v[188:191],0
ex v199,v199
mm v[220:223],a[88:91],v[184:187],v[220:223]
cv v227,v198,v199
s_and_b32 m0, m0, s78
mm v[220:223],a[84:87],v[180:183],v[220:223]
ex v204,v204
mm v[220:223],a[80:83],v[176:179],v[220:223]
wt vmcnt(10)
s_barrier
mm v[64:67],a[96:99],v[232:235],v[64:67]
ex v205,v205
mm v[96:99],a[96:99],v[236:239],v[96:99]
)ASM" R"ASM(rd a[96:99],v248 offset:8192
cv v230,v204,v205
mm v[100:103],a[100:103],v[236:239],v[100:103]
ex v206,v206
mm v[68:71],a[100:103],v[232:235],v[68:71]
rd a[100:103],v248 offset:9216
s_cmp_eq_u32 s85, s81
mm v[72:75],a[104:107],v[232:235],v[72:75]
ex v207,v207
mm v[104:107],a[104:107],v[236:239],v[104:107]
rd a[104:107],v248 offset:10240
cv v231,v206,v207
mm v[108:111],a[108:111],v[236:239],v[108:111]
ex v208,v208
mm v[76:79],a[108:111],v[232:235],v[76:79]
rd a[108:111],v248 offset:11264
s_cselect_b32 s17, s77, s17
mm v[80:83],a[112:115],v[232:235],v[80:83]
ex v209,v209
mm v[112:115],a[112:115],v[236:239],v[112:115]
rd a[112:115],v248 offset:12288
s_cselect_b32 s80, -8192, s80
mm v[116:119],a[116:119],v[236:239],v[116:119]
ex v210,v210
mm v[84:87],a[116:119],v[232:235],v[84:87]
rd a[116:119],v248 offset:13312
mm v[88:91],a[120:123],v[232:235],v[88:91]
ex v211,v211
mm v[120:123],a[120:123],v[236:239],v[120:123]
rd a[120:123],v248 offset:14336
s_bitcmp1_b64 s[98:99], s85
mm v[124:127],a[124:127],v[236:239],v[124:127]
ex v216,v216
mm v[92:95],a[124:127],v[232:235],v[92:95]
rd a[124:127],v248 offset:15360
v_or_b32 v248, s89, v255
v_smfmac_f32_16x16x64_bf16 a[196:199], a[208:211], v[232:239], v241
wt lgkmcnt(4)
v_smfmac_f32_16x16x64_bf16 a[192:195], a[208:211], v[224:231], v241
rd a[64:67],v248 offset:0
mm v[0:3],a[96:99],v[224:227],v[0:3]
ex v217,v217
mm v[32:35],a[96:99],v[228:231],v[32:35]
rd a[68:71],v248 offset:1024
cv v232,v208,v209
mm v[36:39],a[100:103],v[228:231],v[36:39]
ex v218,v218
mm v[4:7],a[100:103],v[224:227],v[4:7]
rd a[72:75],v248 offset:2048
cv v233,v210,v211
mm v[8:11],a[104:107],v[224:227],v[8:11]
ex v219,v219
mm v[40:43],a[104:107],v[228:231],v[40:43]
rd a[76:79],v248 offset:3072
cv v236,v216,v217
mm v[44:47],a[108:111],v[228:231],v[44:47]
ex v212,v212
mm v[12:15],a[108:111],v[224:227],v[12:15]
rd a[80:83],v248 offset:4096
wt lgkmcnt(5)
mm v[16:19],a[112:115],v[224:227],v[16:19]
ex v213,v213
mm v[48:51],a[112:115],v[228:231],v[48:51]
rd a[84:87],v248 offset:5120
cv v237,v218,v219
mm v[52:55],a[116:119],v[228:231],v[52:55]
ex v214,v214
mm v[20:23],a[116:119],v[224:227],v[20:23]
rd a[88:91],v248 offset:6144
cv v234,v212,v213
mm v[24:27],a[120:123],v[224:227],v[24:27]
ex v215,v215
mm v[56:59],a[120:123],v[228:231],v[56:59]
rd a[92:95],v248 offset:7168
cv v235,v214,v215
mm v[60:63],a[124:127],v[228:231],v[60:63]
ex v220,v220
mm v[28:31],a[124:127],v[224:227],v[28:31]
.La5_LA_F1E:
s_cbranch_scc0 .La5_LA_F1
.La5_LA_F1E2:
s_cmp_eq_u32 s85, s82
s_cbranch_scc1 .La5_LA_F1X
s_call_b64 s[24:25], .La5_LA_EVENT
s_branch .La5_LA_F1
.La5_LA_FE0:
s_nop 0
s_nop 0
.La5_LW2_1:
s_cbranch_execz .La5_LW2_R1
s_cmp_eq_u32 s85, s82
s_cbranch_scc1 .La5_LA_F1X
s_call_b64 s[24:25], .La5_LA_EVENT
s_branch .La5_LA_FS0
; ---- hand-offs between windows
.La5_LA_EVENT:
s_cmp_lg_u32 s85, 2
s_nop 0
s_nop 0
s_cbranch_scc1 .La5_LA_E1
)ASM" R"ASM(wt vmcnt(0)
v_cmp_eq_u32 vcc, v250, v249
s_mov_b32 s17, s16
s_mov_b32 s78, 0x1ffff
s_lshl_b32 m0, s3, 12
s_or_b32 vcc_hi, vcc_hi, s79
s_cmp_eq_u64 vcc, -1
s_cbranch_scc0 .La5_LA_KSPIN
.La5_LA_KOK:
ld v255,s[60:63],s17 offen offset:0 sc1 lds
ld v255,s[60:63],s17 offen offset:1024 sc1 lds
ld v255,s[60:63],s17 offen offset:2048 sc1 lds
ld v255,s[60:63],s17 offen offset:3072 sc1 lds
s_add_u32 s17, s17, s80
s_add_u32 m0, m0, 16384
s_mov_b32 s20, s19
v_and_b32 v251, 4, v251
s_cmp_lg_u32 s3, 0
s_cbranch_scc1 .La5_LA_POLL
s_nop 0
ld v255,s[60:63],s17 offen offset:0 sc1 lds
ld v255,s[60:63],s17 offen offset:1024 sc1 lds
ld v255,s[60:63],s17 offen offset:2048 sc1 lds
ld v255,s[60:63],s17 offen offset:3072 sc1 lds
s_add_u32 s17, s17, s80
s_add_u32 m0, m0, 16384
.La5_LA_POLL:
s_cmp_lg_u32 s22, 0
s_nop 0
s_nop 0
s_cbranch_scc1 .La5_LA_WP
s_mov_b64 exec, 0
s_cmp_eq_u32 s3, 0
s_cbranch_scc1 .La5_LW5_0
s_cmp_eq_u32 s3, 1
s_cbranch_scc1 .La5_LW5_1
s_cmp_eq_u32 s3, 2
s_cbranch_scc1 .La5_LW5_2
s_branch .La5_LW5_3
.La5_LW5_R0:
s_mov_b64 exec, -1
.La5_LA_RETURN:
s_setpc_b64 s[24:25]
.La5_LA_WP:
s_mov_b64 exec, 0
s_cmp_eq_u32 s3, 0
s_cbranch_scc1 .La5_LW6_0
s_cmp_eq_u32 s3, 1
s_cbranch_scc1 .La5_LW6_1
s_cmp_eq_u32 s3, 2
s_cbranch_scc1 .La5_LW6_2
s_branch .La5_LW6_3
.La5_LW6_R0:
s_mov_b64 exec, -1
s_cmp_eq_u32 s73, 1
s_cbranch_scc1 .La5_LA_E0
buffer_load_dword v250, v251, s[64:67], s20 offen sc1
s_setpc_b64 s[24:25]
.La5_LA_E0:
s_mov_b32 s54, 16384
ld a[0:3],v255,s[52:55],0 offen offset:0 sc1
ld a[4:7],v255,s[52:55],0 offen offset:1024 sc1
ld a[8:11],v255,s[52:55],0 offen offset:2048 sc1
ld a[12:15],v255,s[52:55],0 offen offset:3072 sc1
ld a[16:19],v255,s[52:55],s95 offen offset:0 sc1
ld a[20:23],v255,s[52:55],s95 offen offset:1024 sc1
s_setpc_b64 s[24:25]
.La5_LA_E1:
ld a[24:27],v255,s[52:55],s95 offen offset:2048 sc1
ld a[28:31],v255,s[52:55],s95 offen offset:3072 sc1
ld a[32:35],v255,s[52:55],s96 offen offset:0 sc1
ld a[36:39],v255,s[52:55],s96 offen offset:1024 sc1
ld a[40:43],v255,s[52:55],s96 offen offset:2048 sc1
ld a[44:47],v255,s[52:55],s96 offen offset:3072 sc1
ld a[48:51],v255,s[52:55],s97 offen offset:0 sc1
ld a[52:55],v255,s[52:55],s97 offen offset:1024 sc1
ld a[56:59],v255,s[52:55],s97 offen offset:2048 sc1
ld a[60:63],v255,s[52:55],s97 offen offset:3072 sc1
s_setpc_b64 s[24:25]
; ---- helper: the partner-s q rows arrive during the last windows of the own block
.La5_LA_F1X:
s_cmp_eq_u32 s75, 0
s_nop 0
s_nop 0
s_cbranch_scc1 .La5_LA_DC3
s_cmp_eq_u32 s22, 0
s_cbranch_scc1 .La5_LA_DC
s_cmp_eq_u32 s73, 1
s_cbranch_scc1 .La5_LA_F1Y
s_nop 0
v_cmp_eq_u32 vcc, v250, v249
s_mov_b32 s54, 16384
s_and_b32 s92, vcc_lo, 3
s_cmp_eq_u32 s92, 3
s_cbranch_scc0 .La5_LA_QSPIN
.La5_LA_QOK:
wt lgkmcnt(4)
mm v[192:195],a[64:67],v[128:131],0
ex v221,v221
mm v[192:195],a[68:71],v[132:135],v[192:195]
)ASM" R"ASM(cv v238,v220,v221
s_add_u32 s85, s85, 1
mm v[192:195],a[72:75],v[136:139],v[192:195]
ex v222,v222
mm v[192:195],a[76:79],v[140:143],v[192:195]
ld v255,s[60:63],s17 offen offset:0 sc1 lds
mm v[200:203],a[76:79],v[156:159],0
ex v223,v223
mm v[200:203],a[72:75],v[152:155],v[200:203]
.La5_LW6_0:
s_cbranch_execz .La5_LW6_4
cv v239,v222,v223
s_add_u32 s89, s89, 16384
mm v[200:203],a[68:71],v[148:151],v[200:203]
ex v192,v192
mm v[200:203],a[64:67],v[144:147],v[200:203]
wt lgkmcnt(0)
s_and_b32 s89, s89, 0x1ffff
mm v[196:199],a[80:83],v[128:131],0
ex v193,v193
mm v[196:199],a[84:87],v[132:135],v[196:199]
cv v224,v192,v193
mm v[196:199],a[88:91],v[136:139],v[196:199]
ex v194,v194
mm v[196:199],a[92:95],v[140:143],v[196:199]
ld v255,s[60:63],s17 offen offset:1024 sc1 lds
mm v[204:207],a[92:95],v[156:159],0
ex v195,v195
mm v[204:207],a[88:91],v[152:155],v[204:207]
cv v225,v194,v195
mm v[204:207],a[84:87],v[148:151],v[204:207]
ex v200,v200
mm v[204:207],a[80:83],v[144:147],v[204:207]
ld v255,s[60:63],s17 offen offset:2048 sc1 lds
mm v[208:211],a[64:67],v[160:163],0
ex v201,v201
mm v[208:211],a[68:71],v[164:167],v[208:211]
cv v228,v200,v201
mm v[208:211],a[72:75],v[168:171],v[208:211]
ex v202,v202
mm v[208:211],a[76:79],v[172:175],v[208:211]
ld v255,s[60:63],s17 offen offset:3072 sc1 lds
mm v[216:219],a[76:79],v[188:191],0
ex v203,v203
mm v[216:219],a[72:75],v[184:187],v[216:219]
cv v229,v202,v203
mm v[216:219],a[68:71],v[180:183],v[216:219]
ex v196,v196
mm v[216:219],a[64:67],v[176:179],v[216:219]
ld a[0:3],v255,s[52:55],0 offen offset:0 sc1
mm v[212:215],a[80:83],v[160:163],0
ex v197,v197
mm v[212:215],a[84:87],v[164:167],v[212:215]
cv v226,v196,v197
s_add_u32 s17, s17, s80
mm v[212:215],a[88:91],v[168:171],v[212:215]
ex v198,v198
mm v[212:215],a[92:95],v[172:175],v[212:215]
s_add_u32 m0, m0, 16384
s_and_b32 m0, m0, s78
ld a[4:7],v255,s[52:55],0 offen offset:1024 sc1
mm v[220:223],a[92:95],v[188:191],0
ex v199,v199
mm v[220:223],a[88:91],v[184:187],v[220:223]
cv v227,v198,v199
mm v[220:223],a[84:87],v[180:183],v[220:223]
ex v204,v204
mm v[220:223],a[80:83],v[176:179],v[220:223]
wt vmcnt(12)
s_barrier
mm v[64:67],a[96:99],v[232:235],v[64:67]
ex v205,v205
mm v[96:99],a[96:99],v[236:239],v[96:99]
rd a[96:99],v248 offset:8192
cv v230,v204,v205
mm v[100:103],a[100:103],v[236:239],v[100:103]
ex v206,v206
mm v[68:71],a[100:103],v[232:235],v[68:71]
rd a[100:103],v248 offset:9216
ld a[8:11],v255,s[52:55],0 offen offset:2048 sc1
mm v[72:75],a[104:107],v[232:235],v[72:75]
ex v207,v207
mm v[104:107],a[104:107],v[236:239],v[104:107]
rd a[104:107],v248 offset:10240
cv v231,v206,v207
mm v[108:111],a[108:111],v[236:239],v[108:111]
ex v208,v208
mm v[76:79],a[108:111],v[232:235],v[76:79]
rd a[108:111],v248 offset:11264
ld a[12:15],v255,s[52:55],0 offen offset:3072 sc1
mm v[80:83],a[112:115],v[232:235],v[80:83]
ex v209,v209
mm v[112:115],a[112:115],v[236:239],v[112:115]
rd a[112:115],v248 offset:12288
mm v[116:119],a[116:119],v[236:239],v[116:119]
ex v210,v210
mm v[84:87],a[116:119],v[232:235],v[84:87]
rd a[116:119],v248 offset:13312
ld a[16:19],v255,s[52:55],s95 offen offset:0 sc1
mm v[88:91],a[120:123],v[232:235],v[88:91]
)ASM" R"ASM(ex v211,v211
mm v[120:123],a[120:123],v[236:239],v[120:123]
rd a[120:123],v248 offset:14336
s_bitcmp1_b64 s[98:99], s85
mm v[124:127],a[124:127],v[236:239],v[124:127]
ex v216,v216
mm v[92:95],a[124:127],v[232:235],v[92:95]
rd a[124:127],v248 offset:15360
v_or_b32 v248, s89, v255
v_smfmac_f32_16x16x64_bf16 a[196:199], a[208:211], v[232:239], v241
wt lgkmcnt(4)
v_smfmac_f32_16x16x64_bf16 a[192:195], a[208:211], v[224:231], v241
rd a[64:67],v248 offset:0
mm v[0:3],a[96:99],v[224:227],v[0:3]
ex v217,v217
mm v[32:35],a[96:99],v[228:231],v[32:35]
rd a[68:71],v248 offset:1024
cv v232,v208,v209
mm v[36:39],a[100:103],v[228:231],v[36:39]
ex v218,v218
mm v[4:7],a[100:103],v[224:227],v[4:7]
rd a[72:75],v248 offset:2048
cv v233,v210,v211
mm v[8:11],a[104:107],v[224:227],v[8:11]
ex v219,v219
mm v[40:43],a[104:107],v[228:231],v[40:43]
rd a[76:79],v248 offset:3072
cv v236,v216,v217
mm v[44:47],a[108:111],v[228:231],v[44:47]
ex v212,v212
mm v[12:15],a[108:111],v[224:227],v[12:15]
rd a[80:83],v248 offset:4096
wt lgkmcnt(5)
mm v[16:19],a[112:115],v[224:227],v[16:19]
ex v213,v213
mm v[48:51],a[112:115],v[228:231],v[48:51]
rd a[84:87],v248 offset:5120
cv v237,v218,v219
mm v[52:55],a[116:119],v[228:231],v[52:55]
ex v214,v214
mm v[20:23],a[116:119],v[224:227],v[20:23]
rd a[88:91],v248 offset:6144
cv v234,v212,v213
mm v[24:27],a[120:123],v[224:227],v[24:27]
ex v215,v215
mm v[56:59],a[120:123],v[228:231],v[56:59]
rd a[92:95],v248 offset:7168
cv v235,v214,v215
mm v[60:63],a[124:127],v[228:231],v[60:63]
ex v220,v220
mm v[28:31],a[124:127],v[224:227],v[28:31]
ld a[20:23],v255,s[52:55],s95 offen offset:1024 sc1
wt lgkmcnt(4)
mm v[192:195],a[64:67],v[128:131],0
ex v221,v221
mm v[192:195],a[68:71],v[132:135],v[192:195]
cv v238,v220,v221
s_add_u32 s85, s85, 1
mm v[192:195],a[72:75],v[136:139],v[192:195]
ex v222,v222
mm v[192:195],a[76:79],v[140:143],v[192:195]
ld v255,s[60:63],s17 offen offset:0 sc1 lds
mm v[200:203],a[76:79],v[156:159],0
ex v223,v223
mm v[200:203],a[72:75],v[152:155],v[200:203]
cv v239,v222,v223
s_add_u32 s89, s89, 16384
mm v[200:203],a[68:71],v[148:151],v[200:203]
ex v192,v192
mm v[200:203],a[64:67],v[144:147],v[200:203]
wt lgkmcnt(0)
s_and_b32 s89, s89, 0x1ffff
ld a[24:27],v255,s[52:55],s95 offen offset:2048 sc1
mm v[196:199],a[80:83],v[128:131],0
ex v193,v193
mm v[196:199],a[84:87],v[132:135],v[196:199]
cv v224,v192,v193
mm v[196:199],a[88:91],v[136:139],v[196:199]
ex v194,v194
mm v[196:199],a[92:95],v[140:143],v[196:199]
ld v255,s[60:63],s17 offen offset:1024 sc1 lds
mm v[204:207],a[92:95],v[156:159],0
ex v195,v195
mm v[204:207],a[88:91],v[152:155],v[204:207]
cv v225,v194,v195
mm v[204:207],a[84:87],v[148:151],v[204:207]
ex v200,v200
mm v[204:207],a[80:83],v[144:147],v[204:207]
ld v255,s[60:63],s17 offen offset:2048 sc1 lds
mm v[208:211],a[64:67],v[160:163],0
ex v201,v201
mm v[208:211],a[68:71],v[164:167],v[208:211]
cv v228,v200,v201
mm v[208:211],a[72:75],v[168:171],v[208:211]
ex v202,v202
mm v[208:211],a[76:79],v[172:175],v[208:211]
ld v255,s[60:63],s17 offen offset:3072 sc1 lds
mm v[216:219],a[76:79],v[188:191],0
ex v203,v203
mm v[216:219],a[72:75],v[184:187],v[216:219]
)ASM" R"ASM(cv v229,v202,v203
mm v[216:219],a[68:71],v[180:183],v[216:219]
ex v196,v196
mm v[216:219],a[64:67],v[176:179],v[216:219]
ld a[28:31],v255,s[52:55],s95 offen offset:3072 sc1
mm v[212:215],a[80:83],v[160:163],0
ex v197,v197
mm v[212:215],a[84:87],v[164:167],v[212:215]
cv v226,v196,v197
s_add_u32 s17, s17, s80
mm v[212:215],a[88:91],v[168:171],v[212:215]
ex v198,v198
mm v[212:215],a[92:95],v[172:175],v[212:215]
s_add_u32 m0, m0, 16384
s_and_b32 m0, m0, s78
ld a[32:35],v255,s[52:55],s96 offen offset:0 sc1
mm v[220:223],a[92:95],v[188:191],0
ex v199,v199
mm v[220:223],a[88:91],v[184:187],v[220:223]
cv v227,v198,v199
mm v[220:223],a[84:87],v[180:183],v[220:223]
ex v204,v204
mm v[220:223],a[80:83],v[176:179],v[220:223]
wt vmcnt(18)
s_barrier
mm v[64:67],a[96:99],v[232:235],v[64:67]
ex v205,v205
mm v[96:99],a[96:99],v[236:239],v[96:99]
rd a[96:99],v248 offset:8192
cv v230,v204,v205
mm v[100:103],a[100:103],v[236:239],v[100:103]
ex v206,v206
mm v[68:71],a[100:103],v[232:235],v[68:71]
rd a[100:103],v248 offset:9216
ld a[36:39],v255,s[52:55],s96 offen offset:1024 sc1
mm v[72:75],a[104:107],v[232:235],v[72:75]
ex v207,v207
mm v[104:107],a[104:107],v[236:239],v[104:107]
rd a[104:107],v248 offset:10240
cv v231,v206,v207
mm v[108:111],a[108:111],v[236:239],v[108:111]
ex v208,v208
mm v[76:79],a[108:111],v[232:235],v[76:79]
rd a[108:111],v248 offset:11264
ld a[40:43],v255,s[52:55],s96 offen offset:2048 sc1
mm v[80:83],a[112:115],v[232:235],v[80:83]
ex v209,v209
mm v[112:115],a[112:115],v[236:239],v[112:115]
rd a[112:115],v248 offset:12288
mm v[116:119],a[116:119],v[236:239],v[116:119]
ex v210,v210
mm v[84:87],a[116:119],v[232:235],v[84:87]
rd a[116:119],v248 offset:13312
ld a[44:47],v255,s[52:55],s96 offen offset:3072 sc1
mm v[88:91],a[120:123],v[232:235],v[88:91]
ex v211,v211
mm v[120:123],a[120:123],v[236:239],v[120:123]
rd a[120:123],v248 offset:14336
s_bitcmp1_b64 s[98:99], s85
mm v[124:127],a[124:127],v[236:239],v[124:127]
ex v216,v216
mm v[92:95],a[124:127],v[232:235],v[92:95]
rd a[124:127],v248 offset:15360
v_or_b32 v248, s89, v255
v_smfmac_f32_16x16x64_bf16 a[196:199], a[208:211], v[232:239], v241
wt lgkmcnt(4)
v_smfmac_f32_16x16x64_bf16 a[192:195], a[208:211], v[224:231], v241
rd a[64:67],v248 offset:0
mm v[0:3],a[96:99],v[224:227],v[0:3]
ex v217,v217
mm v[32:35],a[96:99],v[228:231],v[32:35]
rd a[68:71],v248 offset:1024
cv v232,v208,v209
mm v[36:39],a[100:103],v[228:231],v[36:39]
ex v218,v218
mm v[4:7],a[100:103],v[224:227],v[4:7]
rd a[72:75],v248 offset:2048
cv v233,v210,v211
mm v[8:11],a[104:107],v[224:227],v[8:11]
ex v219,v219
mm v[40:43],a[104:107],v[228:231],v[40:43]
rd a[76:79],v248 offset:3072
cv v236,v216,v217
mm v[44:47],a[108:111],v[228:231],v[44:47]
ex v212,v212
mm v[12:15],a[108:111],v[224:227],v[12:15]
rd a[80:83],v248 offset:4096
wt lgkmcnt(5)
mm v[16:19],a[112:115],v[224:227],v[16:19]
ex v213,v213
mm v[48:51],a[112:115],v[228:231],v[48:51]
rd a[84:87],v248 offset:5120
cv v237,v218,v219
mm v[52:55],a[116:119],v[228:231],v[52:55]
ex v214,v214
mm v[20:23],a[116:119],v[224:227],v[20:23]
rd a[88:91],v248 offset:6144
cv v234,v212,v213
mm v[24:27],a[120:123],v[224:227],v[24:27]
ex v215,v215
)ASM" R"ASM(mm v[56:59],a[120:123],v[228:231],v[56:59]
rd a[92:95],v248 offset:7168
cv v235,v214,v215
mm v[60:63],a[124:127],v[228:231],v[60:63]
ex v220,v220
mm v[28:31],a[124:127],v[224:227],v[28:31]
ld a[48:51],v255,s[52:55],s97 offen offset:0 sc1
wt lgkmcnt(4)
mm v[192:195],a[64:67],v[128:131],0
ex v221,v221
mm v[192:195],a[68:71],v[132:135],v[192:195]
cv v238,v220,v221
s_add_u32 s85, s85, 1
mm v[192:195],a[72:75],v[136:139],v[192:195]
ex v222,v222
mm v[192:195],a[76:79],v[140:143],v[192:195]
ld v255,s[60:63],s17 offen offset:0 sc1 lds
mm v[200:203],a[76:79],v[156:159],0
ex v223,v223
mm v[200:203],a[72:75],v[152:155],v[200:203]
cv v239,v222,v223
s_add_u32 s89, s89, 16384
mm v[200:203],a[68:71],v[148:151],v[200:203]
ex v192,v192
mm v[200:203],a[64:67],v[144:147],v[200:203]
wt lgkmcnt(0)
s_and_b32 s89, s89, 0x1ffff
ld a[52:55],v255,s[52:55],s97 offen offset:1024 sc1
mm v[196:199],a[80:83],v[128:131],0
ex v193,v193
mm v[196:199],a[84:87],v[132:135],v[196:199]
cv v224,v192,v193
mm v[196:199],a[88:91],v[136:139],v[196:199]
ex v194,v194
mm v[196:199],a[92:95],v[140:143],v[196:199]
ld v255,s[60:63],s17 offen offset:1024 sc1 lds
mm v[204:207],a[92:95],v[156:159],0
ex v195,v195
mm v[204:207],a[88:91],v[152:155],v[204:207]
cv v225,v194,v195
mm v[204:207],a[84:87],v[148:151],v[204:207]
ex v200,v200
mm v[204:207],a[80:83],v[144:147],v[204:207]
ld v255,s[60:63],s17 offen offset:2048 sc1 lds
mm v[208:211],a[64:67],v[160:163],0
ex v201,v201
mm v[208:211],a[68:71],v[164:167],v[208:211]
cv v228,v200,v201
mm v[208:211],a[72:75],v[168:171],v[208:211]
ex v202,v202
mm v[208:211],a[76:79],v[172:175],v[208:211]
ld v255,s[60:63],s17 offen offset:3072 sc1 lds
mm v[216:219],a[76:79],v[188:191],0
ex v203,v203
mm v[216:219],a[72:75],v[184:187],v[216:219]
cv v229,v202,v203
mm v[216:219],a[68:71],v[180:183],v[216:219]
ex v196,v196
mm v[216:219],a[64:67],v[176:179],v[216:219]
ld a[56:59],v255,s[52:55],s97 offen offset:2048 sc1
mm v[212:215],a[80:83],v[160:163],0
ex v197,v197
mm v[212:215],a[84:87],v[164:167],v[212:215]
cv v226,v196,v197
s_add_u32 s17, s17, s80
mm v[212:215],a[88:91],v[168:171],v[212:215]
ex v198,v198
mm v[212:215],a[92:95],v[172:175],v[212:215]
s_add_u32 m0, m0, 16384
s_and_b32 m0, m0, s78
ld a[60:63],v255,s[52:55],s97 offen offset:3072 sc1
mm v[220:223],a[92:95],v[188:191],0
ex v199,v199
mm v[220:223],a[88:91],v[184:187],v[220:223]
cv v227,v198,v199
mm v[220:223],a[84:87],v[180:183],v[220:223]
ex v204,v204
mm v[220:223],a[80:83],v[176:179],v[220:223]
wt vmcnt(24)
s_barrier
mm v[64:67],a[96:99],v[232:235],v[64:67]
ex v205,v205
mm v[96:99],a[96:99],v[236:239],v[96:99]
rd a[96:99],v248 offset:8192
cv v230,v204,v205
mm v[100:103],a[100:103],v[236:239],v[100:103]
ex v206,v206
mm v[68:71],a[100:103],v[232:235],v[68:71]
rd a[100:103],v248 offset:9216
mm v[72:75],a[104:107],v[232:235],v[72:75]
ex v207,v207
mm v[104:107],a[104:107],v[236:239],v[104:107]
rd a[104:107],v248 offset:10240
cv v231,v206,v207
mm v[108:111],a[108:111],v[236:239],v[108:111]
ex v208,v208
mm v[76:79],a[108:111],v[232:235],v[76:79]
rd a[108:111],v248 offset:11264
mm v[80:83],a[112:115],v[232:235],v[80:83]
ex v209,v209
)ASM" R"ASM(mm v[112:115],a[112:115],v[236:239],v[112:115]
rd a[112:115],v248 offset:12288
mm v[116:119],a[116:119],v[236:239],v[116:119]
ex v210,v210
mm v[84:87],a[116:119],v[232:235],v[84:87]
rd a[116:119],v248 offset:13312
mm v[88:91],a[120:123],v[232:235],v[88:91]
ex v211,v211
mm v[120:123],a[120:123],v[236:239],v[120:123]
rd a[120:123],v248 offset:14336
s_bitcmp1_b64 s[98:99], s85
mm v[124:127],a[124:127],v[236:239],v[124:127]
ex v216,v216
mm v[92:95],a[124:127],v[232:235],v[92:95]
rd a[124:127],v248 offset:15360
v_or_b32 v248, s89, v255
v_smfmac_f32_16x16x64_bf16 a[196:199], a[208:211], v[232:239], v241
wt lgkmcnt(4)
v_smfmac_f32_16x16x64_bf16 a[192:195], a[208:211], v[224:231], v241
rd a[64:67],v248 offset:0
mm v[0:3],a[96:99],v[224:227],v[0:3]
ex v217,v217
mm v[32:35],a[96:99],v[228:231],v[32:35]
rd a[68:71],v248 offset:1024
cv v232,v208,v209
mm v[36:39],a[100:103],v[228:231],v[36:39]
ex v218,v218
mm v[4:7],a[100:103],v[224:227],v[4:7]
rd a[72:75],v248 offset:2048
cv v233,v210,v211
mm v[8:11],a[104:107],v[224:227],v[8:11]
ex v219,v219
mm v[40:43],a[104:107],v[228:231],v[40:43]
rd a[76:79],v248 offset:3072
cv v236,v216,v217
mm v[44:47],a[108:111],v[228:231],v[44:47]
ex v212,v212
mm v[12:15],a[108:111],v[224:227],v[12:15]
rd a[80:83],v248 offset:4096
wt lgkmcnt(5)
mm v[16:19],a[112:115],v[224:227],v[16:19]
ex v213,v213
mm v[48:51],a[112:115],v[228:231],v[48:51]
rd a[84:87],v248 offset:5120
cv v237,v218,v219
mm v[52:55],a[116:119],v[228:231],v[52:55]
ex v214,v214
mm v[20:23],a[116:119],v[224:227],v[20:23]
rd a[88:91],v248 offset:6144
cv v234,v212,v213
mm v[24:27],a[120:123],v[224:227],v[24:27]
ex v215,v215
mm v[56:59],a[120:123],v[228:231],v[56:59]
rd a[92:95],v248 offset:7168
cv v235,v214,v215
mm v[60:63],a[124:127],v[228:231],v[60:63]
ex v220,v220
mm v[28:31],a[124:127],v[224:227],v[28:31]
; ---- helper: own block finished: l is checked (.La5_LA_SLOW if outside [2^-60, 2^60] or NaN), O / l leaves; the partner-s rows start
.La5_LA_F1Y:
.La5_LW6_1:
s_cbranch_execz .La5_LW6_5
wt lgkmcnt(4)
wt vmcnt(0)
mm v[192:195],a[64:67],a[0:3],0
ex v221,v221
mm v[192:195],a[68:71],a[4:7],v[192:195]
cv v238,v220,v221
s_add_u32 s89, s89, 16384
mm v[192:195],a[72:75],a[8:11],v[192:195]
ex v222,v222
mm v[192:195],a[76:79],a[12:15],v[192:195]
ld v255,s[60:63],s17 offen offset:0 sc1 lds
ar v242,a192
ar v240,a193
v_add_f32 v242, v242, v240
mm v[200:203],a[76:79],a[28:31],0
ex v223,v223
ar v243,a194
ar v240,a195
v_add_f32 v243, v243, v240
mm v[200:203],a[72:75],a[24:27],v[200:203]
cv v239,v222,v223
s_and_b32 s89, s89, 0x1ffff
aw a192,0
aw a193,0
aw a194,0
mm v[200:203],a[68:71],a[20:23],v[200:203]
ex v192,v192
aw a195,0
v_subrev_u32 v240, 562036736, v242
mm v[200:203],a[64:67],a[16:19],v[200:203]
wt lgkmcnt(0)
v_rcp_f32 v242, v242
v_cmp_le_u32 vcc, 1006632960, v240
v_subrev_u32 v240, 562036736, v243
mm v[196:199],a[80:83],a[0:3],0
ex v193,v193
s_mov_b64 s[92:93], vcc
v_cmp_le_u32 vcc, 1006632960, v240
)ASM" R"ASM(mm v[196:199],a[84:87],a[4:7],v[196:199]
cv v224,v192,v193
v_rcp_f32_e64 v243, v243
mm v[196:199],a[88:91],a[8:11],v[196:199]
v_exp_f32_e64 v194, v194
s_or_b64 vcc, vcc, s[92:93]
s_cbranch_scc1 .La5_LA_SC1
.La5_LA_SR1:
v_mul_f32 v0, v0, v242
mm v[196:199],a[92:95],a[12:15],v[196:199]
ld v255,s[60:63],s17 offen offset:1024 sc1 lds
v_mul_f32 v1, v1, v242
v_mul_f32 v2, v2, v242
v_mul_f32 v3, v3, v242
mm v[204:207],a[92:95],a[28:31],0
ex v195,v195
v_mul_f32 v4, v4, v242
v_mul_f32 v5, v5, v242
v_mul_f32 v6, v6, v242
mm v[204:207],a[88:91],a[24:27],v[204:207]
cv v225,v194,v195
v_mul_f32 v7, v7, v242
cv v128,v0,v1
cv v129,v2,v3
cv v130,v4,v5
mm v[204:207],a[84:87],a[20:23],v[204:207]
ex v200,v200
cv v131,v6,v7
v_mul_f32 v8, v8, v242
v_mul_f32 v9, v9, v242
mm v[204:207],a[80:83],a[16:19],v[204:207]
ld v255,s[60:63],s17 offen offset:2048 sc1 lds
v_mul_f32 v10, v10, v242
v_mul_f32 v11, v11, v242
v_mul_f32 v12, v12, v242
mm v[208:211],a[64:67],a[32:35],0
ex v201,v201
v_mul_f32 v13, v13, v242
v_mul_f32 v14, v14, v242
v_mul_f32 v15, v15, v242
mm v[208:211],a[68:71],a[36:39],v[208:211]
cv v228,v200,v201
cv v132,v8,v9
cv v133,v10,v11
cv v134,v12,v13
cv v135,v14,v15
mm v[208:211],a[72:75],a[40:43],v[208:211]
ex v202,v202
v_mul_f32 v16, v16, v242
v_mul_f32 v17, v17, v242
v_mul_f32 v18, v18, v242
mm v[208:211],a[76:79],a[44:47],v[208:211]
ld v255,s[60:63],s17 offen offset:3072 sc1 lds
v_mul_f32 v19, v19, v242
v_mul_f32 v20, v20, v242
v_mul_f32 v21, v21, v242
mm v[216:219],a[76:79],a[60:63],0
ex v203,v203
v_mul_f32 v22, v22, v242
v_mul_f32 v23, v23, v242
cv v136,v16,v17
mm v[216:219],a[72:75],a[56:59],v[216:219]
cv v229,v202,v203
cv v137,v18,v19
cv v138,v20,v21
cv v139,v22,v23
v_mul_f32 v24, v24, v242
mm v[216:219],a[68:71],a[52:55],v[216:219]
ex v196,v196
v_mul_f32 v25, v25, v242
v_mul_f32 v26, v26, v242
v_mul_f32 v27, v27, v242
mm v[216:219],a[64:67],a[48:51],v[216:219]
v_mul_f32 v28, v28, v242
v_mul_f32 v29, v29, v242
v_mul_f32 v30, v30, v242
v_mul_f32 v31, v31, v242
cv v140,v24,v25
mm v[212:215],a[80:83],a[32:35],0
ex v197,v197
s_add_u32 s17, s17, s80
cv v141,v26,v27
cv v142,v28,v29
mm v[212:215],a[84:87],a[36:39],v[212:215]
cv v226,v196,v197
s_add_u32 m0, m0, 16384
s_and_b32 m0, m0, s78
cv v143,v30,v31
v_mul_f32 v32, v32, v243
mm v[212:215],a[88:91],a[40:43],v[212:215]
ex v198,v198
v_mul_f32 v33, v33, v243
v_mul_f32 v34, v34, v243
v_mul_f32 v35, v35, v243
mm v[212:215],a[92:95],a[44:47],v[212:215]
st v[128:131],v244,s[44:47],s91 offen offset:0 sc1
v_mul_f32 v36, v36, v243
v_mul_f32 v37, v37, v243
v_mul_f32 v38, v38, v243
mm v[220:223],a[92:95],a[60:63],0
)ASM" R"ASM(ex v199,v199
v_mul_f32 v39, v39, v243
cv v144,v32,v33
cv v145,v34,v35
mm v[220:223],a[88:91],a[56:59],v[220:223]
cv v227,v198,v199
cv v146,v36,v37
cv v147,v38,v39
v_mul_f32 v40, v40, v243
v_mul_f32 v41, v41, v243
mm v[220:223],a[84:87],a[52:55],v[220:223]
ex v204,v204
v_mul_f32 v42, v42, v243
v_mul_f32 v43, v43, v243
v_mul_f32 v44, v44, v243
mm v[220:223],a[80:83],a[48:51],v[220:223]
s_nop 0
s_barrier
v_mul_f32 v45, v45, v243
v_mul_f32 v46, v46, v243
v_mul_f32 v47, v47, v243
mm v[64:67],a[96:99],v[232:235],v[64:67]
ex v205,v205
cv v148,v40,v41
cv v149,v42,v43
cv v150,v44,v45
mm v[96:99],a[96:99],v[236:239],v[96:99]
.La5_LW6_2:
s_cbranch_execz .La5_LW6_6
rd a[96:99],v248 offset:8192
cv v230,v204,v205
cv v151,v46,v47
v_mul_f32 v48, v48, v243
v_mul_f32 v49, v49, v243
mm v[100:103],a[100:103],v[236:239],v[100:103]
ex v206,v206
v_mul_f32 v50, v50, v243
v_mul_f32 v51, v51, v243
v_mul_f32 v52, v52, v243
mm v[68:71],a[100:103],v[232:235],v[68:71]
rd a[100:103],v248 offset:9216
st v[132:135],v244,s[44:47],s91 offen offset:1024 sc1
v_mul_f32 v53, v53, v243
v_mul_f32 v54, v54, v243
mm v[72:75],a[104:107],v[232:235],v[72:75]
ex v207,v207
v_mul_f32 v55, v55, v243
cv v152,v48,v49
cv v153,v50,v51
mm v[104:107],a[104:107],v[236:239],v[104:107]
rd a[104:107],v248 offset:10240
cv v231,v206,v207
cv v154,v52,v53
cv v155,v54,v55
v_mul_f32 v56, v56, v243
mm v[108:111],a[108:111],v[236:239],v[108:111]
ex v208,v208
v_mul_f32 v57, v57, v243
v_mul_f32 v58, v58, v243
v_mul_f32 v59, v59, v243
mm v[76:79],a[108:111],v[232:235],v[76:79]
rd a[108:111],v248 offset:11264
v_mul_f32 v60, v60, v243
v_mul_f32 v61, v61, v243
v_mul_f32 v62, v62, v243
v_mul_f32 v63, v63, v243
mm v[80:83],a[112:115],v[232:235],v[80:83]
ex v209,v209
cv v156,v56,v57
cv v157,v58,v59
cv v158,v60,v61
mm v[112:115],a[112:115],v[236:239],v[112:115]
rd a[112:115],v248 offset:12288
st v[136:139],v244,s[44:47],s91 offen offset:2048 sc1
cv v159,v62,v63
mm v[116:119],a[116:119],v[236:239],v[116:119]
ex v210,v210
mm v[84:87],a[116:119],v[232:235],v[84:87]
rd a[116:119],v248 offset:13312
mm v[88:91],a[120:123],v[232:235],v[88:91]
ex v211,v211
mm v[120:123],a[120:123],v[236:239],v[120:123]
rd a[120:123],v248 offset:14336
st v[140:143],v244,s[44:47],s91 offen offset:3072 sc1
mm v[124:127],a[124:127],v[236:239],v[124:127]
ex v216,v216
mm v[92:95],a[124:127],v[232:235],v[92:95]
rd a[124:127],v248 offset:15360
v_or_b32 v248, s89, v255
v_smfmac_f32_16x16x64_bf16 a[196:199], a[208:211], v[232:239], v241
wt lgkmcnt(4)
v_smfmac_f32_16x16x64_bf16 a[192:195], a[208:211], v[224:231], v241
rd a[64:67],v248 offset:0
mm v[0:3],a[96:99],v[224:227],0
ex v217,v217
mm v[32:35],a[96:99],v[228:231],0
rd a[68:71],v248 offset:1024
cv v232,v208,v209
ar v242,a196
ar v240,a197
)ASM" R"ASM(v_add_f32 v242, v242, v240
mm v[36:39],a[100:103],v[228:231],0
ex v218,v218
ar v243,a198
ar v240,a199
v_add_f32 v243, v243, v240
mm v[4:7],a[100:103],v[224:227],0
rd a[72:75],v248 offset:2048
cv v233,v210,v211
st v[144:147],v245,s[44:47],s91 offen offset:0 sc1
aw a196,0
mm v[8:11],a[104:107],v[224:227],0
ex v219,v219
aw a197,0
aw a198,0
aw a199,0
mm v[40:43],a[104:107],v[228:231],0
rd a[76:79],v248 offset:3072
cv v236,v216,v217
v_subrev_u32 v240, 562036736, v242
v_rcp_f32 v242, v242
mm v[44:47],a[108:111],v[228:231],0
ex v212,v212
v_cmp_le_u32 vcc, 1006632960, v240
v_subrev_u32 v240, 562036736, v243
s_mov_b64 s[92:93], vcc
mm v[12:15],a[108:111],v[224:227],0
rd a[80:83],v248 offset:4096
wt lgkmcnt(5)
v_cmp_le_u32 vcc, 1006632960, v240
v_rcp_f32_e64 v243, v243
mm v[16:19],a[112:115],v[224:227],0
v_exp_f32_e64 v213, v213
s_or_b64 vcc, vcc, s[92:93]
s_cbranch_scc1 .La5_LA_SC2
.La5_LA_SR2:
v_mul_f32 v64, v64, v242
mm v[48:51],a[112:115],v[228:231],0
rd a[84:87],v248 offset:5120
cv v237,v218,v219
v_mul_f32 v65, v65, v242
v_mul_f32 v66, v66, v242
v_mul_f32 v67, v67, v242
mm v[52:55],a[116:119],v[228:231],0
ex v214,v214
v_mul_f32 v68, v68, v242
v_mul_f32 v69, v69, v242
v_mul_f32 v70, v70, v242
mm v[20:23],a[116:119],v[224:227],0
rd a[88:91],v248 offset:6144
cv v234,v212,v213
st v[148:151],v245,s[44:47],s91 offen offset:1024 sc1
v_mul_f32 v71, v71, v242
mm v[24:27],a[120:123],v[224:227],0
ex v215,v215
cv v160,v64,v65
cv v161,v66,v67
cv v162,v68,v69
mm v[56:59],a[120:123],v[228:231],0
rd a[92:95],v248 offset:7168
cv v235,v214,v215
cv v163,v70,v71
v_mul_f32 v72, v72, v242
v_mul_f32 v73, v73, v242
mm v[60:63],a[124:127],v[228:231],0
ex v220,v220
v_mul_f32 v74, v74, v242
v_mul_f32 v75, v75, v242
v_mul_f32 v76, v76, v242
mm v[28:31],a[124:127],v[224:227],0
s_add_u32 s85, s85, 1
v_mul_f32 v77, v77, v242
v_mul_f32 v78, v78, v242
v_mul_f32 v79, v79, v242
cv v164,v72,v73
wt lgkmcnt(4)
mm v[192:195],a[64:67],a[0:3],0
ex v221,v221
cv v165,v74,v75
cv v166,v76,v77
cv v167,v78,v79
mm v[192:195],a[68:71],a[4:7],v[192:195]
cv v238,v220,v221
s_add_u32 s89, s89, 16384
v_mul_f32 v80, v80, v242
v_mul_f32 v81, v81, v242
v_mul_f32 v82, v82, v242
mm v[192:195],a[72:75],a[8:11],v[192:195]
ex v222,v222
v_mul_f32 v83, v83, v242
v_mul_f32 v84, v84, v242
v_mul_f32 v85, v85, v242
mm v[192:195],a[76:79],a[12:15],v[192:195]
ld v255,s[60:63],s17 offen offset:0 sc1 lds
v_mul_f32 v86, v86, v242
v_mul_f32 v87, v87, v242
cv v168,v80,v81
mm v[200:203],a[76:79],a[28:31],0
ex v223,v223
cv v169,v82,v83
)ASM" R"ASM(cv v170,v84,v85
cv v171,v86,v87
mm v[200:203],a[72:75],a[24:27],v[200:203]
cv v239,v222,v223
s_and_b32 s89, s89, 0x1ffff
v_mul_f32 v88, v88, v242
v_mul_f32 v89, v89, v242
v_mul_f32 v90, v90, v242
mm v[200:203],a[68:71],a[20:23],v[200:203]
ex v192,v192
v_mul_f32 v91, v91, v242
v_mul_f32 v92, v92, v242
v_mul_f32 v93, v93, v242
mm v[200:203],a[64:67],a[16:19],v[200:203]
wt lgkmcnt(0)
st v[152:155],v245,s[44:47],s91 offen offset:2048 sc1
v_mul_f32 v94, v94, v242
v_mul_f32 v95, v95, v242
mm v[196:199],a[80:83],a[0:3],0
ex v193,v193
cv v172,v88,v89
cv v173,v90,v91
cv v174,v92,v93
mm v[196:199],a[84:87],a[4:7],v[196:199]
cv v224,v192,v193
cv v175,v94,v95
v_mul_f32 v96, v96, v243
v_mul_f32 v97, v97, v243
v_mul_f32 v98, v98, v243
mm v[196:199],a[88:91],a[8:11],v[196:199]
ex v194,v194
v_mul_f32 v99, v99, v243
v_mul_f32 v100, v100, v243
v_mul_f32 v101, v101, v243
mm v[196:199],a[92:95],a[12:15],v[196:199]
ld v255,s[60:63],s17 offen offset:1024 sc1 lds
v_mul_f32 v102, v102, v243
v_mul_f32 v103, v103, v243
cv v176,v96,v97
mm v[204:207],a[92:95],a[28:31],0
ex v195,v195
cv v177,v98,v99
cv v178,v100,v101
cv v179,v102,v103
mm v[204:207],a[88:91],a[24:27],v[204:207]
cv v225,v194,v195
st v[156:159],v245,s[44:47],s91 offen offset:3072 sc1
v_mul_f32 v104, v104, v243
v_mul_f32 v105, v105, v243
mm v[204:207],a[84:87],a[20:23],v[204:207]
ex v200,v200
v_mul_f32 v106, v106, v243
v_mul_f32 v107, v107, v243
v_mul_f32 v108, v108, v243
mm v[204:207],a[80:83],a[16:19],v[204:207]
ld v255,s[60:63],s17 offen offset:2048 sc1 lds
v_mul_f32 v109, v109, v243
v_mul_f32 v110, v110, v243
v_mul_f32 v111, v111, v243
mm v[208:211],a[64:67],a[32:35],0
ex v201,v201
cv v180,v104,v105
cv v181,v106,v107
cv v182,v108,v109
mm v[208:211],a[68:71],a[36:39],v[208:211]
cv v228,v200,v201
cv v183,v110,v111
v_mul_f32 v112, v112, v243
v_mul_f32 v113, v113, v243
v_mul_f32 v114, v114, v243
mm v[208:211],a[72:75],a[40:43],v[208:211]
ex v202,v202
v_mul_f32 v115, v115, v243
v_mul_f32 v116, v116, v243
v_mul_f32 v117, v117, v243
mm v[208:211],a[76:79],a[44:47],v[208:211]
ld v255,s[60:63],s17 offen offset:3072 sc1 lds
v_mul_f32 v118, v118, v243
v_mul_f32 v119, v119, v243
cv v184,v112,v113
mm v[216:219],a[76:79],a[60:63],0
ex v203,v203
cv v185,v114,v115
cv v186,v116,v117
cv v187,v118,v119
mm v[216:219],a[72:75],a[56:59],v[216:219]
cv v229,v202,v203
v_mul_f32 v120, v120, v243
v_mul_f32 v121, v121, v243
v_mul_f32 v122, v122, v243
v_mul_f32 v123, v123, v243
mm v[216:219],a[68:71],a[52:55],v[216:219]
ex v196,v196
v_mul_f32 v124, v124, v243
v_mul_f32 v125, v125, v243
v_mul_f32 v126, v126, v243
mm v[216:219],a[64:67],a[48:51],v[216:219]
v_mul_f32 v127, v127, v243
cv v188,v120,v121
cv v189,v122,v123
)ASM" R"ASM(cv v190,v124,v125
cv v191,v126,v127
mm v[212:215],a[80:83],a[32:35],0
ex v197,v197
s_add_u32 s17, s17, s80
mm v[212:215],a[84:87],a[36:39],v[212:215]
cv v226,v196,v197
s_add_u32 m0, m0, 16384
s_and_b32 m0, m0, s78
mm v[212:215],a[88:91],a[40:43],v[212:215]
ex v198,v198
mm v[212:215],a[92:95],a[44:47],v[212:215]
st v[160:163],v246,s[44:47],s91 offen offset:0 sc1
mm v[220:223],a[92:95],a[60:63],0
ex v199,v199
mm v[220:223],a[88:91],a[56:59],v[220:223]
cv v227,v198,v199
mm v[220:223],a[84:87],a[52:55],v[220:223]
ex v204,v204
mm v[220:223],a[80:83],a[48:51],v[220:223]
wt vmcnt(18)
s_barrier
mm v[64:67],a[96:99],v[232:235],0
ex v205,v205
mm v[96:99],a[96:99],v[236:239],0
rd a[96:99],v248 offset:8192
cv v230,v204,v205
mm v[100:103],a[100:103],v[236:239],0
ex v206,v206
mm v[68:71],a[100:103],v[232:235],0
rd a[100:103],v248 offset:9216
st v[164:167],v246,s[44:47],s91 offen offset:1024 sc1
mm v[72:75],a[104:107],v[232:235],0
ex v207,v207
mm v[104:107],a[104:107],v[236:239],0
rd a[104:107],v248 offset:10240
cv v231,v206,v207
mm v[108:111],a[108:111],v[236:239],0
ex v208,v208
mm v[76:79],a[108:111],v[232:235],0
rd a[108:111],v248 offset:11264
st v[168:171],v246,s[44:47],s91 offen offset:2048 sc1
mm v[80:83],a[112:115],v[232:235],0
ex v209,v209
mm v[112:115],a[112:115],v[236:239],0
rd a[112:115],v248 offset:12288
mm v[116:119],a[116:119],v[236:239],0
ex v210,v210
mm v[84:87],a[116:119],v[232:235],0
rd a[116:119],v248 offset:13312
st v[172:175],v246,s[44:47],s91 offen offset:3072 sc1
mm v[88:91],a[120:123],v[232:235],0
ex v211,v211
mm v[120:123],a[120:123],v[236:239],0
rd a[120:123],v248 offset:14336
mm v[124:127],a[124:127],v[236:239],0
ex v216,v216
mm v[92:95],a[124:127],v[232:235],0
rd a[124:127],v248 offset:15360
v_or_b32 v248, s89, v255
st v[176:179],v247,s[44:47],s91 offen offset:0 sc1
v_smfmac_f32_16x16x64_bf16 a[196:199], a[208:211], v[232:239], v241
wt lgkmcnt(4)
v_smfmac_f32_16x16x64_bf16 a[192:195], a[208:211], v[224:231], v241
rd a[64:67],v248 offset:0
mm v[0:3],a[96:99],v[224:227],v[0:3]
ex v217,v217
mm v[32:35],a[96:99],v[228:231],v[32:35]
rd a[68:71],v248 offset:1024
cv v232,v208,v209
mm v[36:39],a[100:103],v[228:231],v[36:39]
ex v218,v218
mm v[4:7],a[100:103],v[224:227],v[4:7]
rd a[72:75],v248 offset:2048
cv v233,v210,v211
st v[180:183],v247,s[44:47],s91 offen offset:1024 sc1
mm v[8:11],a[104:107],v[224:227],v[8:11]
ex v219,v219
mm v[40:43],a[104:107],v[228:231],v[40:43]
rd a[76:79],v248 offset:3072
cv v236,v216,v217
mm v[44:47],a[108:111],v[228:231],v[44:47]
ex v212,v212
mm v[12:15],a[108:111],v[224:227],v[12:15]
rd a[80:83],v248 offset:4096
wt lgkmcnt(5)
mm v[16:19],a[112:115],v[224:227],v[16:19]
ex v213,v213
mm v[48:51],a[112:115],v[228:231],v[48:51]
rd a[84:87],v248 offset:5120
cv v237,v218,v219
st v[184:187],v247,s[44:47],s91 offen offset:2048 sc1
mm v[52:55],a[116:119],v[228:231],v[52:55]
ex v214,v214
mm v[20:23],a[116:119],v[224:227],v[20:23]
rd a[88:91],v248 offset:6144
cv v234,v212,v213
mm v[24:27],a[120:123],v[224:227],v[24:27]
ex v215,v215
mm v[56:59],a[120:123],v[228:231],v[56:59]
)ASM" R"ASM(rd a[92:95],v248 offset:7168
cv v235,v214,v215
st v[188:191],v247,s[44:47],s91 offen offset:3072 sc1
mm v[60:63],a[124:127],v[228:231],v[60:63]
ex v220,v220
mm v[28:31],a[124:127],v[224:227],v[28:31]
s_add_u32 s85, s85, 1
s_cmp_eq_u32 s85, 22
s_cbranch_scc1 .La5_LA_F3
.p2align 6
.La5_LA_F2:
wt lgkmcnt(4)
mm v[192:195],a[64:67],a[0:3],0
ex v221,v221
mm v[192:195],a[68:71],a[4:7],v[192:195]
cv v238,v220,v221
s_add_u32 s85, s85, 1
mm v[192:195],a[72:75],a[8:11],v[192:195]
ex v222,v222
mm v[192:195],a[76:79],a[12:15],v[192:195]
ld v255,s[60:63],s17 offen offset:0 sc1 lds
mm v[200:203],a[76:79],a[28:31],0
ex v223,v223
mm v[200:203],a[72:75],a[24:27],v[200:203]
cv v239,v222,v223
s_add_u32 s89, s89, 16384
mm v[200:203],a[68:71],a[20:23],v[200:203]
ex v192,v192
mm v[200:203],a[64:67],a[16:19],v[200:203]
wt lgkmcnt(0)
s_and_b32 s89, s89, 0x1ffff
mm v[196:199],a[80:83],a[0:3],0
ex v193,v193
mm v[196:199],a[84:87],a[4:7],v[196:199]
cv v224,v192,v193
mm v[196:199],a[88:91],a[8:11],v[196:199]
ex v194,v194
mm v[196:199],a[92:95],a[12:15],v[196:199]
ld v255,s[60:63],s17 offen offset:1024 sc1 lds
mm v[204:207],a[92:95],a[28:31],0
ex v195,v195
mm v[204:207],a[88:91],a[24:27],v[204:207]
cv v225,v194,v195
mm v[204:207],a[84:87],a[20:23],v[204:207]
ex v200,v200
mm v[204:207],a[80:83],a[16:19],v[204:207]
ld v255,s[60:63],s17 offen offset:2048 sc1 lds
mm v[208:211],a[64:67],a[32:35],0
ex v201,v201
mm v[208:211],a[68:71],a[36:39],v[208:211]
cv v228,v200,v201
mm v[208:211],a[72:75],a[40:43],v[208:211]
ex v202,v202
mm v[208:211],a[76:79],a[44:47],v[208:211]
ld v255,s[60:63],s17 offen offset:3072 sc1 lds
mm v[216:219],a[76:79],a[60:63],0
ex v203,v203
mm v[216:219],a[72:75],a[56:59],v[216:219]
cv v229,v202,v203
mm v[216:219],a[68:71],a[52:55],v[216:219]
ex v196,v196
mm v[216:219],a[64:67],a[48:51],v[216:219]
buffer_load_dword a253, v254, s[36:39], s90 offen
mm v[212:215],a[80:83],a[32:35],0
ex v197,v197
mm v[212:215],a[84:87],a[36:39],v[212:215]
cv v226,v196,v197
s_add_u32 s90, s90, s21
mm v[212:215],a[88:91],a[40:43],v[212:215]
ex v198,v198
mm v[212:215],a[92:95],a[44:47],v[212:215]
s_add_u32 s17, s17, s80
s_add_u32 m0, m0, 16384
mm v[220:223],a[92:95],a[60:63],0
ex v199,v199
mm v[220:223],a[88:91],a[56:59],v[220:223]
cv v227,v198,v199
s_and_b32 m0, m0, s78
mm v[220:223],a[84:87],a[52:55],v[220:223]
ex v204,v204
mm v[220:223],a[80:83],a[48:51],v[220:223]
wt vmcnt(11)
s_barrier
mm v[64:67],a[96:99],v[232:235],v[64:67]
ex v205,v205
mm v[96:99],a[96:99],v[236:239],v[96:99]
rd a[96:99],v248 offset:8192
cv v230,v204,v205
mm v[100:103],a[100:103],v[236:239],v[100:103]
ex v206,v206
mm v[68:71],a[100:103],v[232:235],v[68:71]
rd a[100:103],v248 offset:9216
mm v[72:75],a[104:107],v[232:235],v[72:75]
ex v207,v207
mm v[104:107],a[104:107],v[236:239],v[104:107]
rd a[104:107],v248 offset:10240
cv v231,v206,v207
mm v[108:111],a[108:111],v[236:239],v[108:111]
ex v208,v208
mm v[76:79],a[108:111],v[232:235],v[76:79]
)ASM" R"ASM(rd a[108:111],v248 offset:11264
mm v[80:83],a[112:115],v[232:235],v[80:83]
ex v209,v209
mm v[112:115],a[112:115],v[236:239],v[112:115]
rd a[112:115],v248 offset:12288
mm v[116:119],a[116:119],v[236:239],v[116:119]
ex v210,v210
mm v[84:87],a[116:119],v[232:235],v[84:87]
rd a[116:119],v248 offset:13312
mm v[88:91],a[120:123],v[232:235],v[88:91]
ex v211,v211
mm v[120:123],a[120:123],v[236:239],v[120:123]
rd a[120:123],v248 offset:14336
s_bitcmp1_b64 s[98:99], s85
mm v[124:127],a[124:127],v[236:239],v[124:127]
ex v216,v216
mm v[92:95],a[124:127],v[232:235],v[92:95]
rd a[124:127],v248 offset:15360
v_or_b32 v248, s89, v255
v_smfmac_f32_16x16x64_bf16 a[196:199], a[208:211], v[232:239], v241
wt lgkmcnt(4)
v_smfmac_f32_16x16x64_bf16 a[192:195], a[208:211], v[224:231], v241
rd a[64:67],v248 offset:0
mm v[0:3],a[96:99],v[224:227],v[0:3]
ex v217,v217
mm v[32:35],a[96:99],v[228:231],v[32:35]
rd a[68:71],v248 offset:1024
cv v232,v208,v209
mm v[36:39],a[100:103],v[228:231],v[36:39]
ex v218,v218
mm v[4:7],a[100:103],v[224:227],v[4:7]
rd a[72:75],v248 offset:2048
cv v233,v210,v211
mm v[8:11],a[104:107],v[224:227],v[8:11]
ex v219,v219
mm v[40:43],a[104:107],v[228:231],v[40:43]
rd a[76:79],v248 offset:3072
cv v236,v216,v217
mm v[44:47],a[108:111],v[228:231],v[44:47]
ex v212,v212
mm v[12:15],a[108:111],v[224:227],v[12:15]
rd a[80:83],v248 offset:4096
wt lgkmcnt(5)
mm v[16:19],a[112:115],v[224:227],v[16:19]
ex v213,v213
mm v[48:51],a[112:115],v[228:231],v[48:51]
rd a[84:87],v248 offset:5120
cv v237,v218,v219
mm v[52:55],a[116:119],v[228:231],v[52:55]
ex v214,v214
mm v[20:23],a[116:119],v[224:227],v[20:23]
rd a[88:91],v248 offset:6144
cv v234,v212,v213
mm v[24:27],a[120:123],v[224:227],v[24:27]
ex v215,v215
mm v[56:59],a[120:123],v[228:231],v[56:59]
rd a[92:95],v248 offset:7168
cv v235,v214,v215
mm v[60:63],a[124:127],v[228:231],v[60:63]
ex v220,v220
mm v[28:31],a[124:127],v[224:227],v[28:31]
s_cbranch_scc1 .La5_LA_F2E2
wt lgkmcnt(4)
mm v[192:195],a[64:67],a[0:3],0
ex v221,v221
mm v[192:195],a[68:71],a[4:7],v[192:195]
cv v238,v220,v221
s_add_u32 s85, s85, 1
mm v[192:195],a[72:75],a[8:11],v[192:195]
ex v222,v222
mm v[192:195],a[76:79],a[12:15],v[192:195]
ld v255,s[60:63],s17 offen offset:0 sc1 lds
mm v[200:203],a[76:79],a[28:31],0
ex v223,v223
mm v[200:203],a[72:75],a[24:27],v[200:203]
cv v239,v222,v223
s_add_u32 s89, s89, 16384
mm v[200:203],a[68:71],a[20:23],v[200:203]
ex v192,v192
mm v[200:203],a[64:67],a[16:19],v[200:203]
wt lgkmcnt(0)
s_and_b32 s89, s89, 0x1ffff
mm v[196:199],a[80:83],a[0:3],0
ex v193,v193
mm v[196:199],a[84:87],a[4:7],v[196:199]
cv v224,v192,v193
mm v[196:199],a[88:91],a[8:11],v[196:199]
ex v194,v194
mm v[196:199],a[92:95],a[12:15],v[196:199]
ld v255,s[60:63],s17 offen offset:1024 sc1 lds
mm v[204:207],a[92:95],a[28:31],0
ex v195,v195
mm v[204:207],a[88:91],a[24:27],v[204:207]
cv v225,v194,v195
mm v[204:207],a[84:87],a[20:23],v[204:207]
ex v200,v200
mm v[204:207],a[80:83],a[16:19],v[204:207]
ld v255,s[60:63],s17 offen offset:2048 sc1 lds
mm v[208:211],a[64:67],a[32:35],0
ex v201,v201
)ASM" R"ASM(mm v[208:211],a[68:71],a[36:39],v[208:211]
cv v228,v200,v201
mm v[208:211],a[72:75],a[40:43],v[208:211]
ex v202,v202
mm v[208:211],a[76:79],a[44:47],v[208:211]
ld v255,s[60:63],s17 offen offset:3072 sc1 lds
mm v[216:219],a[76:79],a[60:63],0
ex v203,v203
mm v[216:219],a[72:75],a[56:59],v[216:219]
cv v229,v202,v203
mm v[216:219],a[68:71],a[52:55],v[216:219]
ex v196,v196
mm v[216:219],a[64:67],a[48:51],v[216:219]
buffer_load_dword a253, v254, s[36:39], s90 offen
mm v[212:215],a[80:83],a[32:35],0
ex v197,v197
mm v[212:215],a[84:87],a[36:39],v[212:215]
cv v226,v196,v197
s_add_u32 s90, s90, s21
mm v[212:215],a[88:91],a[40:43],v[212:215]
ex v198,v198
mm v[212:215],a[92:95],a[44:47],v[212:215]
s_add_u32 s17, s17, s80
s_add_u32 m0, m0, 16384
mm v[220:223],a[92:95],a[60:63],0
ex v199,v199
mm v[220:223],a[88:91],a[56:59],v[220:223]
cv v227,v198,v199
s_and_b32 m0, m0, s78
mm v[220:223],a[84:87],a[52:55],v[220:223]
ex v204,v204
mm v[220:223],a[80:83],a[48:51],v[220:223]
wt vmcnt(11)
s_barrier
mm v[64:67],a[96:99],v[232:235],v[64:67]
ex v205,v205
mm v[96:99],a[96:99],v[236:239],v[96:99]
rd a[96:99],v248 offset:8192
cv v230,v204,v205
mm v[100:103],a[100:103],v[236:239],v[100:103]
ex v206,v206
mm v[68:71],a[100:103],v[232:235],v[68:71]
rd a[100:103],v248 offset:9216
mm v[72:75],a[104:107],v[232:235],v[72:75]
ex v207,v207
mm v[104:107],a[104:107],v[236:239],v[104:107]
rd a[104:107],v248 offset:10240
.La5_LW6_3:
s_cbranch_execz .La5_LW6_7
cv v231,v206,v207
mm v[108:111],a[108:111],v[236:239],v[108:111]
ex v208,v208
mm v[76:79],a[108:111],v[232:235],v[76:79]
rd a[108:111],v248 offset:11264
mm v[80:83],a[112:115],v[232:235],v[80:83]
ex v209,v209
mm v[112:115],a[112:115],v[236:239],v[112:115]
rd a[112:115],v248 offset:12288
mm v[116:119],a[116:119],v[236:239],v[116:119]
ex v210,v210
mm v[84:87],a[116:119],v[232:235],v[84:87]
rd a[116:119],v248 offset:13312
mm v[88:91],a[120:123],v[232:235],v[88:91]
ex v211,v211
mm v[120:123],a[120:123],v[236:239],v[120:123]
rd a[120:123],v248 offset:14336
s_bitcmp1_b64 s[98:99], s85
mm v[124:127],a[124:127],v[236:239],v[124:127]
ex v216,v216
mm v[92:95],a[124:127],v[232:235],v[92:95]
rd a[124:127],v248 offset:15360
v_or_b32 v248, s89, v255
v_smfmac_f32_16x16x64_bf16 a[196:199], a[208:211], v[232:239], v241
wt lgkmcnt(4)
v_smfmac_f32_16x16x64_bf16 a[192:195], a[208:211], v[224:231], v241
rd a[64:67],v248 offset:0
mm v[0:3],a[96:99],v[224:227],v[0:3]
ex v217,v217
mm v[32:35],a[96:99],v[228:231],v[32:35]
rd a[68:71],v248 offset:1024
cv v232,v208,v209
mm v[36:39],a[100:103],v[228:231],v[36:39]
ex v218,v218
mm v[4:7],a[100:103],v[224:227],v[4:7]
rd a[72:75],v248 offset:2048
cv v233,v210,v211
mm v[8:11],a[104:107],v[224:227],v[8:11]
ex v219,v219
mm v[40:43],a[104:107],v[228:231],v[40:43]
rd a[76:79],v248 offset:3072
cv v236,v216,v217
mm v[44:47],a[108:111],v[228:231],v[44:47]
ex v212,v212
mm v[12:15],a[108:111],v[224:227],v[12:15]
rd a[80:83],v248 offset:4096
wt lgkmcnt(5)
mm v[16:19],a[112:115],v[224:227],v[16:19]
ex v213,v213
mm v[48:51],a[112:115],v[228:231],v[48:51]
rd a[84:87],v248 offset:5120
)ASM" R"ASM(cv v237,v218,v219
mm v[52:55],a[116:119],v[228:231],v[52:55]
ex v214,v214
mm v[20:23],a[116:119],v[224:227],v[20:23]
rd a[88:91],v248 offset:6144
cv v234,v212,v213
mm v[24:27],a[120:123],v[224:227],v[24:27]
ex v215,v215
mm v[56:59],a[120:123],v[228:231],v[56:59]
rd a[92:95],v248 offset:7168
cv v235,v214,v215
mm v[60:63],a[124:127],v[228:231],v[60:63]
ex v220,v220
mm v[28:31],a[124:127],v[224:227],v[28:31]
s_cbranch_scc0 .La5_LA_F2
.La5_LA_F2E2:
; ---- helper: last windows; FFN weights and the first weights of the O projection come in
.La5_LA_F3:
.La5_LW6_4:
s_cbranch_execz .La5_LW6_8
wt lgkmcnt(4)
mm v[192:195],a[64:67],a[0:3],0
ex v221,v221
mm v[192:195],a[68:71],a[4:7],v[192:195]
cv v238,v220,v221
s_add_u32 s89, s89, 16384
mm v[192:195],a[72:75],a[8:11],v[192:195]
ex v222,v222
mm v[192:195],a[76:79],a[12:15],v[192:195]
ld v255,s[60:63],s17 offen offset:0 sc1 lds
mm v[200:203],a[76:79],a[28:31],0
ex v223,v223
mm v[200:203],a[72:75],a[24:27],v[200:203]
cv v239,v222,v223
s_and_b32 s89, s89, 0x1ffff
mm v[200:203],a[68:71],a[20:23],v[200:203]
ex v192,v192
mm v[200:203],a[64:67],a[16:19],v[200:203]
wt lgkmcnt(0)
mm v[196:199],a[80:83],a[0:3],0
ex v193,v193
mm v[196:199],a[84:87],a[4:7],v[196:199]
cv v224,v192,v193
ar v243,a229
mm v[196:199],a[88:91],a[8:11],v[196:199]
ex v194,v194
mm v[196:199],a[92:95],a[12:15],v[196:199]
ld v255,s[60:63],s17 offen offset:1024 sc1 lds
mm v[204:207],a[92:95],a[28:31],0
ex v195,v195
mm v[204:207],a[88:91],a[24:27],v[204:207]
cv v225,v194,v195
mm v[204:207],a[84:87],a[20:23],v[204:207]
ex v200,v200
mm v[204:207],a[80:83],a[16:19],v[204:207]
ld v255,s[60:63],s17 offen offset:2048 sc1 lds
mm v[208:211],a[64:67],a[32:35],0
ex v201,v201
mm v[208:211],a[68:71],a[36:39],v[208:211]
cv v228,v200,v201
mm v[208:211],a[72:75],a[40:43],v[208:211]
ex v202,v202
mm v[208:211],a[76:79],a[44:47],v[208:211]
ld v255,s[60:63],s17 offen offset:3072 sc1 lds
mm v[216:219],a[76:79],a[60:63],0
ex v203,v203
mm v[216:219],a[72:75],a[56:59],v[216:219]
cv v229,v202,v203
s_mov_b32 s93, 0
mm v[216:219],a[68:71],a[52:55],v[216:219]
ex v196,v196
mm v[216:219],a[64:67],a[48:51],v[216:219]
ld a[128:131],v243,s[40:43],s93 offen
mm v[212:215],a[80:83],a[32:35],0
ex v197,v197
s_add_u32 s17, s17, s80
mm v[212:215],a[84:87],a[36:39],v[212:215]
cv v226,v196,v197
s_mov_b32 s93, 7168
s_add_u32 m0, m0, 16384
mm v[212:215],a[88:91],a[40:43],v[212:215]
ex v198,v198
s_and_b32 m0, m0, s78
mm v[212:215],a[92:95],a[44:47],v[212:215]
ld a[132:135],v243,s[40:43],s93 offen
mm v[220:223],a[92:95],a[60:63],0
ex v199,v199
mm v[220:223],a[88:91],a[56:59],v[220:223]
cv v227,v198,v199
mm v[220:223],a[84:87],a[52:55],v[220:223]
ex v204,v204
mm v[220:223],a[80:83],a[48:51],v[220:223]
wt vmcnt(12)
s_barrier
mm v[64:67],a[96:99],v[232:235],v[64:67]
ex v205,v205
mm v[96:99],a[96:99],v[236:239],v[96:99]
rd a[96:99],v248 offset:8192
cv v230,v204,v205
mm v[100:103],a[100:103],v[236:239],v[100:103]
)ASM" R"ASM(ex v206,v206
mm v[68:71],a[100:103],v[232:235],v[68:71]
rd a[100:103],v248 offset:9216
mm v[72:75],a[104:107],v[232:235],v[72:75]
ex v207,v207
mm v[104:107],a[104:107],v[236:239],v[104:107]
rd a[104:107],v248 offset:10240
cv v231,v206,v207
s_mov_b32 s93, 14336
mm v[108:111],a[108:111],v[236:239],v[108:111]
ex v208,v208
mm v[76:79],a[108:111],v[232:235],v[76:79]
rd a[108:111],v248 offset:11264
ld a[136:139],v243,s[40:43],s93 offen
mm v[80:83],a[112:115],v[232:235],v[80:83]
ex v209,v209
mm v[112:115],a[112:115],v[236:239],v[112:115]
rd a[112:115],v248 offset:12288
mm v[116:119],a[116:119],v[236:239],v[116:119]
ex v210,v210
mm v[84:87],a[116:119],v[232:235],v[84:87]
rd a[116:119],v248 offset:13312
mm v[88:91],a[120:123],v[232:235],v[88:91]
ex v211,v211
mm v[120:123],a[120:123],v[236:239],v[120:123]
rd a[120:123],v248 offset:14336
mm v[124:127],a[124:127],v[236:239],v[124:127]
ex v216,v216
mm v[92:95],a[124:127],v[232:235],v[92:95]
rd a[124:127],v248 offset:15360
v_or_b32 v248, s89, v255
v_smfmac_f32_16x16x64_bf16 a[196:199], a[208:211], v[232:239], v241
wt lgkmcnt(4)
v_smfmac_f32_16x16x64_bf16 a[192:195], a[208:211], v[224:231], v241
rd a[64:67],v248 offset:0
mm v[0:3],a[96:99],v[224:227],v[0:3]
ex v217,v217
mm v[32:35],a[96:99],v[228:231],v[32:35]
rd a[68:71],v248 offset:1024
cv v232,v208,v209
mm v[36:39],a[100:103],v[228:231],v[36:39]
ex v218,v218
mm v[4:7],a[100:103],v[224:227],v[4:7]
rd a[72:75],v248 offset:2048
cv v233,v210,v211
mm v[8:11],a[104:107],v[224:227],v[8:11]
ex v219,v219
mm v[40:43],a[104:107],v[228:231],v[40:43]
rd a[76:79],v248 offset:3072
cv v236,v216,v217
mm v[44:47],a[108:111],v[228:231],v[44:47]
ex v212,v212
mm v[12:15],a[108:111],v[224:227],v[12:15]
rd a[80:83],v248 offset:4096
wt lgkmcnt(5)
mm v[16:19],a[112:115],v[224:227],v[16:19]
ex v213,v213
mm v[48:51],a[112:115],v[228:231],v[48:51]
rd a[84:87],v248 offset:5120
cv v237,v218,v219
mm v[52:55],a[116:119],v[228:231],v[52:55]
ex v214,v214
mm v[20:23],a[116:119],v[224:227],v[20:23]
rd a[88:91],v248 offset:6144
cv v234,v212,v213
mm v[24:27],a[120:123],v[224:227],v[24:27]
ex v215,v215
mm v[56:59],a[120:123],v[228:231],v[56:59]
rd a[92:95],v248 offset:7168
cv v235,v214,v215
mm v[60:63],a[124:127],v[228:231],v[60:63]
ex v220,v220
mm v[28:31],a[124:127],v[224:227],v[28:31]
wt lgkmcnt(4)
mm v[192:195],a[64:67],a[0:3],0
ex v221,v221
mm v[192:195],a[68:71],a[4:7],v[192:195]
cv v238,v220,v221
s_add_u32 s89, s89, 16384
mm v[192:195],a[72:75],a[8:11],v[192:195]
ex v222,v222
mm v[192:195],a[76:79],a[12:15],v[192:195]
ld v255,s[60:63],s17 offen offset:0 sc1 lds
mm v[200:203],a[76:79],a[28:31],0
ex v223,v223
mm v[200:203],a[72:75],a[24:27],v[200:203]
cv v239,v222,v223
s_and_b32 s89, s89, 0x1ffff
mm v[200:203],a[68:71],a[20:23],v[200:203]
ex v192,v192
mm v[200:203],a[64:67],a[16:19],v[200:203]
wt lgkmcnt(0)
mm v[196:199],a[80:83],a[0:3],0
ex v193,v193
mm v[196:199],a[84:87],a[4:7],v[196:199]
cv v224,v192,v193
mm v[196:199],a[88:91],a[8:11],v[196:199]
ex v194,v194
mm v[196:199],a[92:95],a[12:15],v[196:199]
ld v255,s[60:63],s17 offen offset:1024 sc1 lds
)ASM" R"ASM(mm v[204:207],a[92:95],a[28:31],0
ex v195,v195
mm v[204:207],a[88:91],a[24:27],v[204:207]
cv v225,v194,v195
mm v[204:207],a[84:87],a[20:23],v[204:207]
ex v200,v200
mm v[204:207],a[80:83],a[16:19],v[204:207]
ld v255,s[60:63],s17 offen offset:2048 sc1 lds
mm v[208:211],a[64:67],a[32:35],0
ex v201,v201
mm v[208:211],a[68:71],a[36:39],v[208:211]
cv v228,v200,v201
mm v[208:211],a[72:75],a[40:43],v[208:211]
ex v202,v202
mm v[208:211],a[76:79],a[44:47],v[208:211]
ld v255,s[60:63],s17 offen offset:3072 sc1 lds
mm v[216:219],a[76:79],a[60:63],0
ex v203,v203
mm v[216:219],a[72:75],a[56:59],v[216:219]
cv v229,v202,v203
s_mov_b32 s93, 21504
mm v[216:219],a[68:71],a[52:55],v[216:219]
ex v196,v196
mm v[216:219],a[64:67],a[48:51],v[216:219]
ld a[140:143],v243,s[40:43],s93 offen
mm v[212:215],a[80:83],a[32:35],0
ex v197,v197
s_add_u32 s17, s17, s80
mm v[212:215],a[84:87],a[36:39],v[212:215]
cv v226,v196,v197
s_mov_b32 s93, 28672
s_add_u32 m0, m0, 16384
mm v[212:215],a[88:91],a[40:43],v[212:215]
ex v198,v198
s_and_b32 m0, m0, s78
mm v[212:215],a[92:95],a[44:47],v[212:215]
ld a[144:147],v243,s[40:43],s93 offen
mm v[220:223],a[92:95],a[60:63],0
ex v199,v199
mm v[220:223],a[88:91],a[56:59],v[220:223]
cv v227,v198,v199
mm v[220:223],a[84:87],a[52:55],v[220:223]
ex v204,v204
mm v[220:223],a[80:83],a[48:51],v[220:223]
wt vmcnt(14)
s_barrier
mm v[64:67],a[96:99],v[232:235],v[64:67]
ex v205,v205
mm v[96:99],a[96:99],v[236:239],v[96:99]
rd a[96:99],v248 offset:8192
cv v230,v204,v205
mm v[100:103],a[100:103],v[236:239],v[100:103]
ex v206,v206
mm v[68:71],a[100:103],v[232:235],v[68:71]
rd a[100:103],v248 offset:9216
mm v[72:75],a[104:107],v[232:235],v[72:75]
ex v207,v207
mm v[104:107],a[104:107],v[236:239],v[104:107]
rd a[104:107],v248 offset:10240
cv v231,v206,v207
s_mov_b32 s93, 35840
mm v[108:111],a[108:111],v[236:239],v[108:111]
ex v208,v208
mm v[76:79],a[108:111],v[232:235],v[76:79]
rd a[108:111],v248 offset:11264
ld a[148:151],v243,s[40:43],s93 offen
mm v[80:83],a[112:115],v[232:235],v[80:83]
ex v209,v209
mm v[112:115],a[112:115],v[236:239],v[112:115]
rd a[112:115],v248 offset:12288
mm v[116:119],a[116:119],v[236:239],v[116:119]
ex v210,v210
mm v[84:87],a[116:119],v[232:235],v[84:87]
rd a[116:119],v248 offset:13312
mm v[88:91],a[120:123],v[232:235],v[88:91]
ex v211,v211
mm v[120:123],a[120:123],v[236:239],v[120:123]
rd a[120:123],v248 offset:14336
ar v242,a231
wt vmcnt(14)
mm v[124:127],a[124:127],v[236:239],v[124:127]
ex v216,v216
mm v[92:95],a[124:127],v[232:235],v[92:95]
rd a[124:127],v248 offset:15360
v_or_b32 v248, s89, v255
v_smfmac_f32_16x16x64_bf16 a[196:199], a[208:211], v[232:239], v241
wt lgkmcnt(4)
v_smfmac_f32_16x16x64_bf16 a[192:195], a[208:211], v[224:231], v241
rd a[64:67],v248 offset:0
mm v[0:3],a[96:99],v[224:227],v[0:3]
ex v217,v217
mm v[32:35],a[96:99],v[228:231],v[32:35]
rd a[68:71],v248 offset:1024
cv v232,v208,v209
mm v[36:39],a[100:103],v[228:231],v[36:39]
ex v218,v218
mm v[4:7],a[100:103],v[224:227],v[4:7]
rd a[72:75],v248 offset:2048
cv v233,v210,v211
mm v[8:11],a[104:107],v[224:227],v[8:11]
)ASM" R"ASM(ex v219,v219
mm v[40:43],a[104:107],v[228:231],v[40:43]
rd a[76:79],v248 offset:3072
cv v236,v216,v217
mm v[44:47],a[108:111],v[228:231],v[44:47]
ex v212,v212
mm v[12:15],a[108:111],v[224:227],v[12:15]
rd a[80:83],v248 offset:4096
wt lgkmcnt(5)
mm v[16:19],a[112:115],v[224:227],v[16:19]
ex v213,v213
mm v[48:51],a[112:115],v[228:231],v[48:51]
rd a[84:87],v248 offset:5120
cv v237,v218,v219
mm v[52:55],a[116:119],v[228:231],v[52:55]
ex v214,v214
mm v[20:23],a[116:119],v[224:227],v[20:23]
rd a[88:91],v248 offset:6144
cv v234,v212,v213
mm v[24:27],a[120:123],v[224:227],v[24:27]
ex v215,v215
mm v[56:59],a[120:123],v[228:231],v[56:59]
rd a[92:95],v248 offset:7168
cv v235,v214,v215
mm v[60:63],a[124:127],v[228:231],v[60:63]
ex v220,v220
mm v[28:31],a[124:127],v[224:227],v[28:31]
buffer_store_dwordx2 v[252:253], v242, s[64:67], s6 offen offset:0 sc1
wt lgkmcnt(4)
mm v[192:195],a[64:67],a[0:3],0
ex v221,v221
mm v[192:195],a[68:71],a[4:7],v[192:195]
cv v238,v220,v221
s_add_u32 s89, s89, 16384
mm v[192:195],a[72:75],a[8:11],v[192:195]
ex v222,v222
mm v[192:195],a[76:79],a[12:15],v[192:195]
ld v255,s[60:63],s17 offen offset:0 sc1 lds
mm v[200:203],a[76:79],a[28:31],0
ex v223,v223
mm v[200:203],a[72:75],a[24:27],v[200:203]
cv v239,v222,v223
s_and_b32 s89, s89, 0x1ffff
mm v[200:203],a[68:71],a[20:23],v[200:203]
ex v192,v192
mm v[200:203],a[64:67],a[16:19],v[200:203]
wt lgkmcnt(0)
mm v[196:199],a[80:83],a[0:3],0
ex v193,v193
mm v[196:199],a[84:87],a[4:7],v[196:199]
cv v224,v192,v193
mm v[196:199],a[88:91],a[8:11],v[196:199]
ex v194,v194
mm v[196:199],a[92:95],a[12:15],v[196:199]
ld v255,s[60:63],s17 offen offset:1024 sc1 lds
mm v[204:207],a[92:95],a[28:31],0
ex v195,v195
mm v[204:207],a[88:91],a[24:27],v[204:207]
cv v225,v194,v195
mm v[204:207],a[84:87],a[20:23],v[204:207]
ex v200,v200
mm v[204:207],a[80:83],a[16:19],v[204:207]
ld v255,s[60:63],s17 offen offset:2048 sc1 lds
mm v[208:211],a[64:67],a[32:35],0
ex v201,v201
mm v[208:211],a[68:71],a[36:39],v[208:211]
cv v228,v200,v201
mm v[208:211],a[72:75],a[40:43],v[208:211]
ex v202,v202
mm v[208:211],a[76:79],a[44:47],v[208:211]
ld v255,s[60:63],s17 offen offset:3072 sc1 lds
mm v[216:219],a[76:79],a[60:63],0
ex v203,v203
mm v[216:219],a[72:75],a[56:59],v[216:219]
cv v229,v202,v203
s_mov_b32 s93, 43008
mm v[216:219],a[68:71],a[52:55],v[216:219]
ex v196,v196
mm v[216:219],a[64:67],a[48:51],v[216:219]
ld a[152:155],v243,s[40:43],s93 offen
mm v[212:215],a[80:83],a[32:35],0
ex v197,v197
s_add_u32 s17, s17, s80
mm v[212:215],a[84:87],a[36:39],v[212:215]
cv v226,v196,v197
s_mov_b32 s93, 50176
s_add_u32 m0, m0, 16384
mm v[212:215],a[88:91],a[40:43],v[212:215]
ex v198,v198
s_and_b32 m0, m0, s78
mm v[212:215],a[92:95],a[44:47],v[212:215]
ld a[156:159],v243,s[40:43],s93 offen
mm v[220:223],a[92:95],a[60:63],0
ex v199,v199
mm v[220:223],a[88:91],a[56:59],v[220:223]
cv v227,v198,v199
mm v[220:223],a[84:87],a[52:55],v[220:223]
ex v204,v204
mm v[220:223],a[80:83],a[48:51],v[220:223]
wt vmcnt(17)
)ASM" R"ASM(s_barrier
mm v[64:67],a[96:99],v[232:235],v[64:67]
ex v205,v205
mm v[96:99],a[96:99],v[236:239],v[96:99]
rd a[96:99],v248 offset:8192
cv v230,v204,v205
mm v[100:103],a[100:103],v[236:239],v[100:103]
ex v206,v206
mm v[68:71],a[100:103],v[232:235],v[68:71]
rd a[100:103],v248 offset:9216
mm v[72:75],a[104:107],v[232:235],v[72:75]
ex v207,v207
mm v[104:107],a[104:107],v[236:239],v[104:107]
rd a[104:107],v248 offset:10240
cv v231,v206,v207
s_mov_b32 s93, 57344
mm v[108:111],a[108:111],v[236:239],v[108:111]
ex v208,v208
mm v[76:79],a[108:111],v[232:235],v[76:79]
rd a[108:111],v248 offset:11264
ld a[160:163],v243,s[40:43],s93 offen
mm v[80:83],a[112:115],v[232:235],v[80:83]
ex v209,v209
mm v[112:115],a[112:115],v[236:239],v[112:115]
rd a[112:115],v248 offset:12288
mm v[116:119],a[116:119],v[236:239],v[116:119]
ex v210,v210
mm v[84:87],a[116:119],v[232:235],v[84:87]
rd a[116:119],v248 offset:13312
mm v[88:91],a[120:123],v[232:235],v[88:91]
ex v211,v211
mm v[120:123],a[120:123],v[236:239],v[120:123]
rd a[120:123],v248 offset:14336
mm v[124:127],a[124:127],v[236:239],v[124:127]
ex v216,v216
mm v[92:95],a[124:127],v[232:235],v[92:95]
rd a[124:127],v248 offset:15360
v_or_b32 v248, s89, v255
v_smfmac_f32_16x16x64_bf16 a[196:199], a[208:211], v[232:239], v241
wt lgkmcnt(4)
v_smfmac_f32_16x16x64_bf16 a[192:195], a[208:211], v[224:231], v241
rd a[64:67],v248 offset:0
mm v[0:3],a[96:99],v[224:227],v[0:3]
ex v217,v217
mm v[32:35],a[96:99],v[228:231],v[32:35]
rd a[68:71],v248 offset:1024
cv v232,v208,v209
mm v[36:39],a[100:103],v[228:231],v[36:39]
ex v218,v218
mm v[4:7],a[100:103],v[224:227],v[4:7]
rd a[72:75],v248 offset:2048
cv v233,v210,v211
mm v[8:11],a[104:107],v[224:227],v[8:11]
ex v219,v219
mm v[40:43],a[104:107],v[228:231],v[40:43]
rd a[76:79],v248 offset:3072
cv v236,v216,v217
mm v[44:47],a[108:111],v[228:231],v[44:47]
ex v212,v212
mm v[12:15],a[108:111],v[224:227],v[12:15]
rd a[80:83],v248 offset:4096
wt lgkmcnt(5)
mm v[16:19],a[112:115],v[224:227],v[16:19]
ex v213,v213
mm v[48:51],a[112:115],v[228:231],v[48:51]
rd a[84:87],v248 offset:5120
cv v237,v218,v219
mm v[52:55],a[116:119],v[228:231],v[52:55]
ex v214,v214
mm v[20:23],a[116:119],v[224:227],v[20:23]
rd a[88:91],v248 offset:6144
cv v234,v212,v213
mm v[24:27],a[120:123],v[224:227],v[24:27]
ex v215,v215
mm v[56:59],a[120:123],v[228:231],v[56:59]
rd a[92:95],v248 offset:7168
cv v235,v214,v215
mm v[60:63],a[124:127],v[228:231],v[60:63]
ex v220,v220
mm v[28:31],a[124:127],v[224:227],v[28:31]
wt lgkmcnt(4)
mm v[192:195],a[64:67],a[0:3],0
ex v221,v221
mm v[192:195],a[68:71],a[4:7],v[192:195]
cv v238,v220,v221
ar v240,a228
mm v[192:195],a[72:75],a[8:11],v[192:195]
ex v222,v222
mm v[192:195],a[76:79],a[12:15],v[192:195]
s_add_u32 m0, s83, 16896
s_lshl_b32 s17, s72, 18
mm v[200:203],a[76:79],a[28:31],0
ex v223,v223
mm v[200:203],a[72:75],a[24:27],v[200:203]
cv v239,v222,v223
s_add_u32 s89, s89, 16384
mm v[200:203],a[68:71],a[20:23],v[200:203]
ex v192,v192
mm v[200:203],a[64:67],a[16:19],v[200:203]
wt lgkmcnt(0)
)ASM" R"ASM(s_and_b32 s89, s89, 0x1ffff
mm v[196:199],a[80:83],a[0:3],0
ex v193,v193
mm v[196:199],a[84:87],a[4:7],v[196:199]
cv v224,v192,v193
mm v[196:199],a[88:91],a[8:11],v[196:199]
ex v194,v194
mm v[196:199],a[92:95],a[12:15],v[196:199]
ld v240,s[56:59],s17 offen lds
mm v[204:207],a[92:95],a[28:31],0
ex v195,v195
s_add_u32 s17, s17, 32768
mm v[204:207],a[88:91],a[24:27],v[204:207]
cv v225,v194,v195
s_add_u32 m0, m0, 1024
mm v[204:207],a[84:87],a[20:23],v[204:207]
ex v200,v200
mm v[204:207],a[80:83],a[16:19],v[204:207]
ld v240,s[56:59],s17 offen lds
mm v[208:211],a[64:67],a[32:35],0
ex v201,v201
s_add_u32 s17, s17, 4294942720
mm v[208:211],a[68:71],a[36:39],v[208:211]
cv v228,v200,v201
s_add_u32 m0, m0, 1152
mm v[208:211],a[72:75],a[40:43],v[208:211]
ex v202,v202
mm v[208:211],a[76:79],a[44:47],v[208:211]
ld v240,s[56:59],s17 offen lds
mm v[216:219],a[76:79],a[60:63],0
ex v203,v203
s_add_u32 s17, s17, 32768
mm v[216:219],a[72:75],a[56:59],v[216:219]
cv v229,v202,v203
s_add_u32 m0, m0, 1024
mm v[216:219],a[68:71],a[52:55],v[216:219]
ex v196,v196
mm v[216:219],a[64:67],a[48:51],v[216:219]
ld v240,s[56:59],s17 offen lds
mm v[212:215],a[80:83],a[32:35],0
ex v197,v197
s_add_u32 s17, s17, 90112
mm v[212:215],a[84:87],a[36:39],v[212:215]
cv v226,v196,v197
s_mov_b32 s93, 64512
.La5_LW6_5:
s_cbranch_execz .La5_LW6_R0
s_add_u32 m0, s83, 33792
mm v[212:215],a[88:91],a[40:43],v[212:215]
ex v198,v198
mm v[212:215],a[92:95],a[44:47],v[212:215]
ld a[164:167],v243,s[40:43],s93 offen
mm v[220:223],a[92:95],a[60:63],0
ex v199,v199
mm v[220:223],a[88:91],a[56:59],v[220:223]
cv v227,v198,v199
mm v[220:223],a[84:87],a[52:55],v[220:223]
ex v204,v204
mm v[220:223],a[80:83],a[48:51],v[220:223]
wt vmcnt(16)
s_barrier
mm v[64:67],a[96:99],v[232:235],v[64:67]
ex v205,v205
mm v[96:99],a[96:99],v[236:239],v[96:99]
rd a[96:99],v248 offset:8192
cv v230,v204,v205
mm v[100:103],a[100:103],v[236:239],v[100:103]
ex v206,v206
mm v[68:71],a[100:103],v[232:235],v[68:71]
rd a[100:103],v248 offset:9216
mm v[72:75],a[104:107],v[232:235],v[72:75]
ex v207,v207
mm v[104:107],a[104:107],v[236:239],v[104:107]
rd a[104:107],v248 offset:10240
cv v231,v206,v207
s_mov_b32 s93, 71680
mm v[108:111],a[108:111],v[236:239],v[108:111]
ex v208,v208
mm v[76:79],a[108:111],v[232:235],v[76:79]
rd a[108:111],v248 offset:11264
ld a[168:171],v243,s[40:43],s93 offen
mm v[80:83],a[112:115],v[232:235],v[80:83]
ex v209,v209
mm v[112:115],a[112:115],v[236:239],v[112:115]
rd a[112:115],v248 offset:12288
s_mov_b32 s93, 78848
mm v[116:119],a[116:119],v[236:239],v[116:119]
ex v210,v210
mm v[84:87],a[116:119],v[232:235],v[84:87]
rd a[116:119],v248 offset:13312
ld a[172:175],v243,s[40:43],s93 offen
mm v[88:91],a[120:123],v[232:235],v[88:91]
ex v211,v211
mm v[120:123],a[120:123],v[236:239],v[120:123]
rd a[120:123],v248 offset:14336
mm v[124:127],a[124:127],v[236:239],v[124:127]
ex v216,v216
mm v[92:95],a[124:127],v[232:235],v[92:95]
rd a[124:127],v248 offset:15360
v_or_b32 v248, s89, v255
)ASM" R"ASM(v_smfmac_f32_16x16x64_bf16 a[196:199], a[208:211], v[232:239], v241
wt lgkmcnt(4)
v_smfmac_f32_16x16x64_bf16 a[192:195], a[208:211], v[224:231], v241
rd a[64:67],v248 offset:0
mm v[0:3],a[96:99],v[224:227],v[0:3]
ex v217,v217
mm v[32:35],a[96:99],v[228:231],v[32:35]
rd a[68:71],v248 offset:1024
cv v232,v208,v209
mm v[36:39],a[100:103],v[228:231],v[36:39]
ex v218,v218
mm v[4:7],a[100:103],v[224:227],v[4:7]
rd a[72:75],v248 offset:2048
cv v233,v210,v211
mm v[8:11],a[104:107],v[224:227],v[8:11]
ex v219,v219
mm v[40:43],a[104:107],v[228:231],v[40:43]
rd a[76:79],v248 offset:3072
cv v236,v216,v217
mm v[44:47],a[108:111],v[228:231],v[44:47]
ex v212,v212
mm v[12:15],a[108:111],v[224:227],v[12:15]
rd a[80:83],v248 offset:4096
wt lgkmcnt(5)
mm v[16:19],a[112:115],v[224:227],v[16:19]
ex v213,v213
mm v[48:51],a[112:115],v[228:231],v[48:51]
rd a[84:87],v248 offset:5120
cv v237,v218,v219
mm v[52:55],a[116:119],v[228:231],v[52:55]
ex v214,v214
mm v[20:23],a[116:119],v[224:227],v[20:23]
rd a[88:91],v248 offset:6144
cv v234,v212,v213
mm v[24:27],a[120:123],v[224:227],v[24:27]
ex v215,v215
mm v[56:59],a[120:123],v[228:231],v[56:59]
rd a[92:95],v248 offset:7168
cv v235,v214,v215
mm v[60:63],a[124:127],v[228:231],v[60:63]
ex v220,v220
mm v[28:31],a[124:127],v[224:227],v[28:31]
wt lgkmcnt(4)
mm v[192:195],a[64:67],a[0:3],0
ex v221,v221
mm v[192:195],a[68:71],a[4:7],v[192:195]
cv v238,v220,v221
s_add_u32 s89, s89, 16384
mm v[192:195],a[72:75],a[8:11],v[192:195]
ex v222,v222
mm v[192:195],a[76:79],a[12:15],v[192:195]
ld v240,s[56:59],s17 offen lds
mm v[200:203],a[76:79],a[28:31],0
ex v223,v223
s_add_u32 s17, s17, 32768
mm v[200:203],a[72:75],a[24:27],v[200:203]
cv v239,v222,v223
s_add_u32 m0, m0, 1024
mm v[200:203],a[68:71],a[20:23],v[200:203]
ex v192,v192
mm v[200:203],a[64:67],a[16:19],v[200:203]
wt lgkmcnt(0)
s_and_b32 s89, s89, 0x1ffff
mm v[196:199],a[80:83],a[0:3],0
ex v193,v193
mm v[196:199],a[84:87],a[4:7],v[196:199]
cv v224,v192,v193
mm v[196:199],a[88:91],a[8:11],v[196:199]
ex v194,v194
mm v[196:199],a[92:95],a[12:15],v[196:199]
ld v240,s[56:59],s17 offen lds
mm v[204:207],a[92:95],a[28:31],0
ex v195,v195
s_add_u32 s17, s17, 4294942720
mm v[204:207],a[88:91],a[24:27],v[204:207]
cv v225,v194,v195
s_add_u32 m0, m0, 1152
mm v[204:207],a[84:87],a[20:23],v[204:207]
ex v200,v200
mm v[204:207],a[80:83],a[16:19],v[204:207]
ld v240,s[56:59],s17 offen lds
mm v[208:211],a[64:67],a[32:35],0
ex v201,v201
s_add_u32 s17, s17, 32768
mm v[208:211],a[68:71],a[36:39],v[208:211]
cv v228,v200,v201
s_add_u32 m0, m0, 1024
mm v[208:211],a[72:75],a[40:43],v[208:211]
ex v202,v202
mm v[208:211],a[76:79],a[44:47],v[208:211]
ld v240,s[56:59],s17 offen lds
mm v[216:219],a[76:79],a[60:63],0
ex v203,v203
mm v[216:219],a[72:75],a[56:59],v[216:219]
cv v229,v202,v203
s_mov_b32 s93, 86016
mm v[216:219],a[68:71],a[52:55],v[216:219]
ex v196,v196
mm v[216:219],a[64:67],a[48:51],v[216:219]
ld a[176:179],v243,s[40:43],s93 offen
)ASM" R"ASM(mm v[212:215],a[80:83],a[32:35],0
ex v197,v197
s_bfe_u32 s17, s4, 262148
mm v[212:215],a[84:87],a[36:39],v[212:215]
cv v226,v196,v197
s_mov_b32 s93, 93184
s_xor_b32 s17, s17, s72
mm v[212:215],a[88:91],a[40:43],v[212:215]
ex v198,v198
s_lshl_b32 s17, s17, 18
mm v[212:215],a[92:95],a[44:47],v[212:215]
ld a[180:183],v243,s[40:43],s93 offen
s_add_u32 m0, s83, 50688
mm v[220:223],a[92:95],a[60:63],0
ex v199,v199
mm v[220:223],a[88:91],a[56:59],v[220:223]
cv v227,v198,v199
mm v[220:223],a[84:87],a[52:55],v[220:223]
ex v204,v204
mm v[220:223],a[80:83],a[48:51],v[220:223]
wt vmcnt(16)
s_barrier
mm v[64:67],a[96:99],v[232:235],v[64:67]
ex v205,v205
mm v[96:99],a[96:99],v[236:239],v[96:99]
rd a[96:99],v248 offset:8192
cv v230,v204,v205
mm v[100:103],a[100:103],v[236:239],v[100:103]
ex v206,v206
mm v[68:71],a[100:103],v[232:235],v[68:71]
rd a[100:103],v248 offset:9216
mm v[72:75],a[104:107],v[232:235],v[72:75]
ex v207,v207
mm v[104:107],a[104:107],v[236:239],v[104:107]
rd a[104:107],v248 offset:10240
cv v231,v206,v207
s_mov_b32 s93, 100352
mm v[108:111],a[108:111],v[236:239],v[108:111]
ex v208,v208
mm v[76:79],a[108:111],v[232:235],v[76:79]
rd a[108:111],v248 offset:11264
ld a[184:187],v243,s[40:43],s93 offen
mm v[80:83],a[112:115],v[232:235],v[80:83]
ex v209,v209
mm v[112:115],a[112:115],v[236:239],v[112:115]
rd a[112:115],v248 offset:12288
mm v[116:119],a[116:119],v[236:239],v[116:119]
ex v210,v210
mm v[84:87],a[116:119],v[232:235],v[84:87]
rd a[116:119],v248 offset:13312
mm v[88:91],a[120:123],v[232:235],v[88:91]
ex v211,v211
mm v[120:123],a[120:123],v[236:239],v[120:123]
rd a[120:123],v248 offset:14336
mm v[124:127],a[124:127],v[236:239],v[124:127]
ex v216,v216
mm v[92:95],a[124:127],v[232:235],v[92:95]
rd a[124:127],v248 offset:15360
v_or_b32 v248, s89, v255
v_smfmac_f32_16x16x64_bf16 a[196:199], a[208:211], v[232:239], v241
wt lgkmcnt(4)
v_smfmac_f32_16x16x64_bf16 a[192:195], a[208:211], v[224:231], v241
rd a[64:67],v248 offset:0
mm v[0:3],a[96:99],v[224:227],v[0:3]
ex v217,v217
mm v[32:35],a[96:99],v[228:231],v[32:35]
rd a[68:71],v248 offset:1024
cv v232,v208,v209
mm v[36:39],a[100:103],v[228:231],v[36:39]
ex v218,v218
mm v[4:7],a[100:103],v[224:227],v[4:7]
rd a[72:75],v248 offset:2048
cv v233,v210,v211
mm v[8:11],a[104:107],v[224:227],v[8:11]
ex v219,v219
mm v[40:43],a[104:107],v[228:231],v[40:43]
rd a[76:79],v248 offset:3072
cv v236,v216,v217
mm v[44:47],a[108:111],v[228:231],v[44:47]
ex v212,v212
mm v[12:15],a[108:111],v[224:227],v[12:15]
rd a[80:83],v248 offset:4096
wt lgkmcnt(5)
mm v[16:19],a[112:115],v[224:227],v[16:19]
ex v213,v213
mm v[48:51],a[112:115],v[228:231],v[48:51]
rd a[84:87],v248 offset:5120
cv v237,v218,v219
mm v[52:55],a[116:119],v[228:231],v[52:55]
ex v214,v214
mm v[20:23],a[116:119],v[224:227],v[20:23]
rd a[88:91],v248 offset:6144
cv v234,v212,v213
mm v[24:27],a[120:123],v[224:227],v[24:27]
ex v215,v215
mm v[56:59],a[120:123],v[228:231],v[56:59]
rd a[92:95],v248 offset:7168
cv v235,v214,v215
mm v[60:63],a[124:127],v[228:231],v[60:63]
ex v220,v220
)ASM" R"ASM(mm v[28:31],a[124:127],v[224:227],v[28:31]
wt lgkmcnt(4)
mm v[192:195],a[64:67],a[0:3],0
ex v221,v221
mm v[192:195],a[68:71],a[4:7],v[192:195]
cv v238,v220,v221
mm v[192:195],a[72:75],a[8:11],v[192:195]
ex v222,v222
mm v[192:195],a[76:79],a[12:15],v[192:195]
ld v240,s[56:59],s17 offen lds
mm v[200:203],a[76:79],a[28:31],0
ex v223,v223
s_add_u32 s17, s17, 32768
mm v[200:203],a[72:75],a[24:27],v[200:203]
cv v239,v222,v223
s_add_u32 m0, m0, 1024
mm v[200:203],a[68:71],a[20:23],v[200:203]
ex v192,v192
mm v[200:203],a[64:67],a[16:19],v[200:203]
wt lgkmcnt(0)
mm v[196:199],a[80:83],a[0:3],0
ex v193,v193
mm v[196:199],a[84:87],a[4:7],v[196:199]
cv v224,v192,v193
mm v[196:199],a[88:91],a[8:11],v[196:199]
ex v194,v194
mm v[196:199],a[92:95],a[12:15],v[196:199]
ld v240,s[56:59],s17 offen lds
mm v[204:207],a[92:95],a[28:31],0
ex v195,v195
s_add_u32 s17, s17, 4294942720
mm v[204:207],a[88:91],a[24:27],v[204:207]
cv v225,v194,v195
s_add_u32 m0, m0, 1152
mm v[204:207],a[84:87],a[20:23],v[204:207]
ex v200,v200
mm v[204:207],a[80:83],a[16:19],v[204:207]
ld v240,s[56:59],s17 offen lds
mm v[208:211],a[64:67],a[32:35],0
ex v201,v201
s_add_u32 s17, s17, 32768
mm v[208:211],a[68:71],a[36:39],v[208:211]
cv v228,v200,v201
s_add_u32 m0, m0, 1024
mm v[208:211],a[72:75],a[40:43],v[208:211]
ex v202,v202
mm v[208:211],a[76:79],a[44:47],v[208:211]
ld v240,s[56:59],s17 offen lds
mm v[216:219],a[76:79],a[60:63],0
ex v203,v203
mm v[216:219],a[72:75],a[56:59],v[216:219]
cv v229,v202,v203
s_mov_b32 s93, 107520
mm v[216:219],a[68:71],a[52:55],v[216:219]
ex v196,v196
mm v[216:219],a[64:67],a[48:51],v[216:219]
ld a[188:191],v243,s[40:43],s93 offen
mm v[212:215],a[80:83],a[32:35],0
ex v197,v197
mm v[212:215],a[84:87],a[36:39],v[212:215]
cv v226,v196,v197
mm v[212:215],a[88:91],a[40:43],v[212:215]
ex v198,v198
mm v[212:215],a[92:95],a[44:47],v[212:215]
mm v[220:223],a[92:95],a[60:63],0
ex v199,v199
mm v[220:223],a[88:91],a[56:59],v[220:223]
cv v227,v198,v199
mm v[220:223],a[84:87],a[52:55],v[220:223]
ex v204,v204
mm v[220:223],a[80:83],a[48:51],v[220:223]
mm v[64:67],a[96:99],v[232:235],v[64:67]
ex v205,v205
mm v[96:99],a[96:99],v[236:239],v[96:99]
rd a[96:99],v248 offset:8192
cv v230,v204,v205
mm v[100:103],a[100:103],v[236:239],v[100:103]
ex v206,v206
mm v[68:71],a[100:103],v[232:235],v[68:71]
rd a[100:103],v248 offset:9216
mm v[72:75],a[104:107],v[232:235],v[72:75]
ex v207,v207
mm v[104:107],a[104:107],v[236:239],v[104:107]
rd a[104:107],v248 offset:10240
cv v231,v206,v207
mm v[108:111],a[108:111],v[236:239],v[108:111]
ex v208,v208
mm v[76:79],a[108:111],v[232:235],v[76:79]
rd a[108:111],v248 offset:11264
mm v[80:83],a[112:115],v[232:235],v[80:83]
ex v209,v209
mm v[112:115],a[112:115],v[236:239],v[112:115]
rd a[112:115],v248 offset:12288
mm v[116:119],a[116:119],v[236:239],v[116:119]
ex v210,v210
mm v[84:87],a[116:119],v[232:235],v[84:87]
rd a[116:119],v248 offset:13312
mm v[88:91],a[120:123],v[232:235],v[88:91]
ex v211,v211
mm v[120:123],a[120:123],v[236:239],v[120:123]
)ASM" R"ASM(rd a[120:123],v248 offset:14336
mm v[124:127],a[124:127],v[236:239],v[124:127]
ex v216,v216
mm v[92:95],a[124:127],v[232:235],v[92:95]
rd a[124:127],v248 offset:15360
v_smfmac_f32_16x16x64_bf16 a[196:199], a[208:211], v[232:239], v241
wt lgkmcnt(4)
v_smfmac_f32_16x16x64_bf16 a[192:195], a[208:211], v[224:231], v241
mm v[0:3],a[96:99],v[224:227],v[0:3]
ex v217,v217
mm v[32:35],a[96:99],v[228:231],v[32:35]
cv v232,v208,v209
mm v[36:39],a[100:103],v[228:231],v[36:39]
ex v218,v218
mm v[4:7],a[100:103],v[224:227],v[4:7]
cv v233,v210,v211
mm v[8:11],a[104:107],v[224:227],v[8:11]
ex v219,v219
mm v[40:43],a[104:107],v[228:231],v[40:43]
cv v236,v216,v217
mm v[44:47],a[108:111],v[228:231],v[44:47]
ex v212,v212
mm v[12:15],a[108:111],v[224:227],v[12:15]
wt lgkmcnt(0)
mm v[16:19],a[112:115],v[224:227],v[16:19]
ex v213,v213
mm v[48:51],a[112:115],v[228:231],v[48:51]
cv v237,v218,v219
mm v[52:55],a[116:119],v[228:231],v[52:55]
ex v214,v214
mm v[20:23],a[116:119],v[224:227],v[20:23]
cv v234,v212,v213
mm v[24:27],a[120:123],v[224:227],v[24:27]
ex v215,v215
mm v[56:59],a[120:123],v[228:231],v[56:59]
cv v235,v214,v215
mm v[60:63],a[124:127],v[228:231],v[60:63]
ex v220,v220
mm v[28:31],a[124:127],v[224:227],v[28:31]
; ---- helper: partial O and l go to the partner
.La5_LA_DP:
ex v221,v221
ex v222,v222
ex v223,v223
cv v238,v220,v221
s_nop 0
cv v239,v222,v223
s_nop 1
v_smfmac_f32_16x16x64_bf16 a[196:199], a[208:211], v[232:239], v241
cv v0,v0,v1
cv v1,v2,v3
cv v2,v4,v5
cv v3,v6,v7
st v[0:3],v255,s[68:71],0 offen offset:0 sc1
mm v[64:67],a[96:99],v[232:235],v[64:67]
cv v8,v8,v9
cv v9,v10,v11
cv v10,v12,v13
cv v11,v14,v15
st v[8:11],v255,s[68:71],0 offen offset:1024 sc1
mm v[96:99],a[96:99],v[236:239],v[96:99]
ar v240,a224
v_xor_b32 v241, 32, v240
cv v16,v16,v17
cv v17,v18,v19
cv v18,v20,v21
cv v19,v22,v23
mm v[100:103],a[100:103],v[236:239],v[100:103]
v_xor_b32 v242, 64, v240
v_xor_b32 v243, 96, v240
st v[16:19],v255,s[68:71],0 offen offset:2048 sc1
cv v24,v24,v25
cv v25,v26,v27
mm v[68:71],a[100:103],v[232:235],v[68:71]
cv v26,v28,v29
cv v27,v30,v31
st v[24:27],v255,s[68:71],0 offen offset:3072 sc1
cv v32,v32,v33
cv v33,v34,v35
mm v[72:75],a[104:107],v[232:235],v[72:75]
cv v34,v36,v37
cv v35,v38,v39
st v[32:35],v255,s[68:71],s95 offen offset:0 sc1
cv v40,v40,v41
cv v41,v42,v43
mm v[104:107],a[104:107],v[236:239],v[104:107]
wt vmcnt(13)
s_barrier
cv v42,v44,v45
cv v43,v46,v47
st v[40:43],v255,s[68:71],s95 offen offset:1024 sc1
mm v[108:111],a[108:111],v[236:239],v[108:111]
rt v[192:193],v240 offset:16896
rt v[194:195],v240 offset:17920
cv v48,v48,v49
cv v49,v50,v51
cv v50,v52,v53
cv v51,v54,v55
mm v[76:79],a[108:111],v[232:235],v[76:79]
rt v[196:197],v240 offset:25344
)ASM" R"ASM(rt v[198:199],v240 offset:26368
st v[48:51],v255,s[68:71],s95 offen offset:2048 sc1
cv v56,v56,v57
cv v57,v58,v59
mm v[80:83],a[112:115],v[232:235],v[80:83]
rt v[200:201],v241 offset:16896
rt v[202:203],v241 offset:17920
cv v58,v60,v61
cv v59,v62,v63
st v[56:59],v255,s[68:71],s95 offen offset:3072 sc1
mm v[112:115],a[112:115],v[236:239],v[112:115]
rt v[204:205],v241 offset:25344
rt v[206:207],v241 offset:26368
mm v[116:119],a[116:119],v[236:239],v[116:119]
rt v[208:209],v242 offset:16896
rt v[210:211],v242 offset:17920
mm v[84:87],a[116:119],v[232:235],v[84:87]
rt v[212:213],v242 offset:25344
rt v[214:215],v242 offset:26368
mm v[88:91],a[120:123],v[232:235],v[88:91]
rt v[216:217],v243 offset:16896
rt v[218:219],v243 offset:17920
mm v[120:123],a[120:123],v[236:239],v[120:123]
rt v[220:221],v243 offset:25344
rt v[222:223],v243 offset:26368
mm v[124:127],a[124:127],v[236:239],v[124:127]
mm v[92:95],a[124:127],v[232:235],v[92:95]
wt lgkmcnt(12)
mm v[0:3],v[192:195],v[128:131],0
mm v[0:3],v[196:199],v[132:135],v[0:3]
mm v[32:35],v[196:199],v[148:151],0
wt lgkmcnt(8)
ar v224,a192
ar v228,a193
ar v225,a194
ar v229,a195
ar v226,a196
mm v[32:35],v[192:195],v[144:147],v[32:35]
rt v[196:197],v240 offset:17024
rt v[198:199],v240 offset:18048
ar v230,a197
ar v227,a198
ar v231,a199
v_add_f32 v224, v224, v228
mm v[36:39],v[200:203],v[144:147],0
v_add_f32 v225, v225, v229
v_add_f32 v226, v226, v230
v_add_f32 v227, v227, v231
s_mov_b32 s92, 16384
st v[224:227],v255,s[68:71],s92 offen sc1
mm v[36:39],v[204:207],v[148:151],v[36:39]
rt v[192:193],v240 offset:25472
rt v[194:195],v240 offset:26496
cv v64,v64,v65
cv v65,v66,v67
cv v66,v68,v69
cv v67,v70,v71
mm v[4:7],v[204:207],v[132:135],0
wt lgkmcnt(4)
st v[64:67],v255,s[68:71],s96 offen offset:0 sc1
cv v72,v72,v73
cv v73,v74,v75
cv v74,v76,v77
mm v[4:7],v[200:203],v[128:131],v[4:7]
rt v[204:205],v241 offset:17024
rt v[206:207],v241 offset:18048
cv v75,v78,v79
st v[72:75],v255,s[68:71],s96 offen offset:1024 sc1
cv v80,v80,v81
mm v[8:11],v[208:211],v[128:131],0
cv v81,v82,v83
cv v82,v84,v85
cv v83,v86,v87
st v[80:83],v255,s[68:71],s96 offen offset:2048 sc1
cv v88,v88,v89
mm v[8:11],v[212:215],v[132:135],v[8:11]
rt v[200:201],v241 offset:25472
rt v[202:203],v241 offset:26496
cv v89,v90,v91
cv v90,v92,v93
cv v91,v94,v95
st v[88:91],v255,s[68:71],s96 offen offset:3072 sc1
mm v[40:43],v[212:215],v[148:151],0
cv v96,v96,v97
cv v97,v98,v99
cv v98,v100,v101
cv v99,v102,v103
st v[96:99],v255,s[68:71],s97 offen offset:0 sc1
mm v[40:43],v[208:211],v[144:147],v[40:43]
rt v[212:213],v242 offset:17024
rt v[214:215],v242 offset:18048
cv v104,v104,v105
cv v105,v106,v107
cv v106,v108,v109
cv v107,v110,v111
mm v[44:47],v[216:219],v[144:147],0
st v[104:107],v255,s[68:71],s97 offen offset:1024 sc1
cv v112,v112,v113
cv v113,v114,v115
cv v114,v116,v117
)ASM" R"ASM(cv v115,v118,v119
mm v[44:47],v[220:223],v[148:151],v[44:47]
rt v[208:209],v242 offset:25472
rt v[210:211],v242 offset:26496
st v[112:115],v255,s[68:71],s97 offen offset:2048 sc1
cv v120,v120,v121
cv v121,v122,v123
mm v[12:15],v[220:223],v[132:135],0
wt lgkmcnt(4)
cv v122,v124,v125
cv v123,v126,v127
st v[120:123],v255,s[68:71],s97 offen offset:3072 sc1
v_mov_b32 v249, 0
mm v[12:15],v[216:219],v[128:131],v[12:15]
rt v[220:221],v243 offset:17024
rt v[222:223],v243 offset:18048
s_add_u32 s6, s88, 393216
mm v[16:19],v[196:199],v[128:131],0
ar v224,a228
v_add_u32 v225, 0x8000, v224
.La5_LW6_6:
s_cbranch_execz .La5_LW6_R0
v_add_u32 v226, 0x2000, v224
v_add_u32 v227, 0xa000, v224
mm v[16:19],v[192:195],v[132:135],v[16:19]
rt v[216:217],v243 offset:25472
rt v[218:219],v243 offset:26496
ar v248,a225
ar v228,a229
mm v[48:51],v[192:195],v[148:151],0
ar v229,a230
mm v[48:51],v[196:199],v[144:147],v[48:51]
rt v[192:193],v240 offset:33792
rt v[194:195],v240 offset:34816
mm v[52:55],v[204:207],v[144:147],0
mm v[52:55],v[200:203],v[148:151],v[52:55]
rt v[196:197],v240 offset:42240
rt v[198:199],v240 offset:43264
mm v[20:23],v[200:203],v[132:135],0
wt lgkmcnt(4)
mm v[20:23],v[204:207],v[128:131],v[20:23]
rt v[200:201],v241 offset:33792
rt v[202:203],v241 offset:34816
mm v[24:27],v[212:215],v[128:131],0
s_bitcmp1_b32 2863311530, s2
s_nop 0
s_cbranch_scc1 .La5_LO_VC2
s_getpc_b64 s[24:25]
.La5_LW3_P0:
s_add_u32 s24, s24, .La5_LW3_R0-.La5_LW3_P0
s_addc_u32 s25, s25, 0
s_mov_b64 exec, 0
s_cmp_eq_u32 s3, 0
s_cbranch_scc1 .La5_LW3_0
s_cmp_eq_u32 s3, 1
s_cbranch_scc1 .La5_LW3_1
s_cmp_eq_u32 s3, 2
s_cbranch_scc1 .La5_LW3_2
s_branch .La5_LW3_3
.La5_LW3_R0:
s_mov_b64 exec, -1
.La5_LO_VE2:
wt vmcnt(0)
buffer_store_dwordx2 v[252:253], v249, s[64:67], s6 offen offset:0 sc1
mm v[24:27],v[208:211],v[132:135],v[24:27]
rt v[204:205],v241 offset:42240
rt v[206:207],v241 offset:43264
mm v[56:59],v[208:211],v[148:151],0
s_mov_b32 s93, 3670016
ld a[192:195],v228,s[40:43],s93 offen
mm v[56:59],v[212:215],v[144:147],v[56:59]
rt v[208:209],v242 offset:33792
rt v[210:211],v242 offset:34816
mm v[60:63],v[220:223],v[144:147],0
s_mov_b32 s93, 3677184
ld a[196:199],v228,s[40:43],s93 offen
mm v[60:63],v[216:219],v[148:151],v[60:63]
rt v[212:213],v242 offset:42240
rt v[214:215],v242 offset:43264
mm v[28:31],v[216:219],v[132:135],0
wt lgkmcnt(4)
s_mov_b32 s93, 3684352
ld a[200:203],v228,s[40:43],s93 offen
mm v[28:31],v[220:223],v[128:131],v[28:31]
rt v[216:217],v243 offset:33792
rt v[218:219],v243 offset:34816
mm v[0:3],v[192:195],v[136:139],v[0:3]
s_mov_b32 s93, 3691520
ld a[204:207],v228,s[40:43],s93 offen
mm v[0:3],v[196:199],v[140:143],v[0:3]
rt v[220:221],v243 offset:42240
rt v[222:223],v243 offset:43264
mm v[32:35],v[196:199],v[156:159],v[32:35]
s_mov_b32 s93, 3698688
ld a[208:211],v228,s[40:43],s93 offen
mm v[32:35],v[192:195],v[152:155],v[32:35]
rt v[196:197],v240 offset:33920
rt v[198:199],v240 offset:34944
mm v[36:39],v[200:203],v[152:155],v[36:39]
s_mov_b32 s93, 3705856
)ASM" R"ASM(ld a[212:215],v228,s[40:43],s93 offen
mm v[36:39],v[204:207],v[156:159],v[36:39]
rt v[192:193],v240 offset:42368
rt v[194:195],v240 offset:43392
mm v[4:7],v[204:207],v[140:143],v[4:7]
wt lgkmcnt(4)
s_mov_b32 s93, 3713024
ld a[216:219],v228,s[40:43],s93 offen
mm v[4:7],v[200:203],v[136:139],v[4:7]
rt v[204:205],v241 offset:33920
rt v[206:207],v241 offset:34944
mm v[8:11],v[208:211],v[136:139],v[8:11]
s_mov_b32 s93, 3720192
ld a[220:223],v228,s[40:43],s93 offen
mm v[8:11],v[212:215],v[140:143],v[8:11]
rt v[200:201],v241 offset:42368
rt v[202:203],v241 offset:43392
mm v[40:43],v[212:215],v[156:159],v[40:43]
s_mov_b32 s93, 3727360
ld a[224:227],v228,s[40:43],s93 offen
mm v[40:43],v[208:211],v[152:155],v[40:43]
rt v[212:213],v242 offset:33920
rt v[214:215],v242 offset:34944
mm v[44:47],v[216:219],v[152:155],v[44:47]
s_mov_b32 s93, 3734528
ld a[228:231],v228,s[40:43],s93 offen
mm v[44:47],v[220:223],v[156:159],v[44:47]
rt v[208:209],v242 offset:42368
rt v[210:211],v242 offset:43392
mm v[12:15],v[220:223],v[140:143],v[12:15]
wt lgkmcnt(4)
s_mov_b32 s93, 3741696
ld a[232:235],v228,s[40:43],s93 offen
mm v[12:15],v[216:219],v[136:139],v[12:15]
rt v[220:221],v243 offset:33920
rt v[222:223],v243 offset:34944
mm v[16:19],v[196:199],v[136:139],v[16:19]
s_mov_b32 s93, 3748864
ld a[236:239],v228,s[40:43],s93 offen
mm v[16:19],v[192:195],v[140:143],v[16:19]
rt v[216:217],v243 offset:42368
rt v[218:219],v243 offset:43392
mm v[48:51],v[192:195],v[156:159],v[48:51]
s_mov_b32 s93, 3756032
ld a[240:243],v228,s[40:43],s93 offen
mm v[48:51],v[196:199],v[152:155],v[48:51]
rt v[192:193],v240 offset:16896
rt v[194:195],v240 offset:17920
mm v[52:55],v[204:207],v[152:155],v[52:55]
s_mov_b32 s93, 3763200
ld a[244:247],v228,s[40:43],s93 offen
mm v[52:55],v[200:203],v[156:159],v[52:55]
rt v[196:197],v240 offset:25344
rt v[198:199],v240 offset:26368
mm v[20:23],v[200:203],v[140:143],v[20:23]
wt lgkmcnt(4)
s_mov_b32 s93, 3770368
ld a[248:251],v228,s[40:43],s93 offen
mm v[20:23],v[204:207],v[136:139],v[20:23]
rt v[200:201],v241 offset:16896
rt v[202:203],v241 offset:17920
mm v[24:27],v[212:215],v[136:139],v[24:27]
s_mov_b32 s93, 3777536
ld a[252:255],v228,s[40:43],s93 offen
mm v[24:27],v[208:211],v[140:143],v[24:27]
rt v[204:205],v241 offset:25344
rt v[206:207],v241 offset:26368
mm v[56:59],v[208:211],v[156:159],v[56:59]
mm v[56:59],v[212:215],v[152:155],v[56:59]
rt v[208:209],v242 offset:16896
rt v[210:211],v242 offset:17920
mm v[60:63],v[220:223],v[152:155],v[60:63]
mm v[60:63],v[216:219],v[156:159],v[60:63]
rt v[212:213],v242 offset:25344
rt v[214:215],v242 offset:26368
mm v[28:31],v[216:219],v[140:143],v[28:31]
wt lgkmcnt(4)
mm v[28:31],v[220:223],v[136:139],v[28:31]
rt v[216:217],v243 offset:16896
rt v[218:219],v243 offset:17920
s_branch .La5_LO_START
; ---- owner: last windows
.La5_LA_DC:
.La5_LW5_0:
s_cbranch_execz .La5_LW5_4
wt lgkmcnt(4)
mm v[192:195],a[64:67],v[128:131],0
ex v221,v221
mm v[192:195],a[68:71],v[132:135],v[192:195]
cv v238,v220,v221
s_add_u32 s89, s89, 16384
mm v[192:195],a[72:75],v[136:139],v[192:195]
ex v222,v222
mm v[192:195],a[76:79],v[140:143],v[192:195]
ld v255,s[60:63],s17 offen offset:0 sc1 lds
mm v[200:203],a[76:79],v[156:159],0
ex v223,v223
mm v[200:203],a[72:75],v[152:155],v[200:203]
cv v239,v222,v223
s_and_b32 s89, s89, 0x1ffff
)ASM" R"ASM(mm v[200:203],a[68:71],v[148:151],v[200:203]
ex v192,v192
mm v[200:203],a[64:67],v[144:147],v[200:203]
wt lgkmcnt(0)
mm v[196:199],a[80:83],v[128:131],0
ex v193,v193
mm v[196:199],a[84:87],v[132:135],v[196:199]
cv v224,v192,v193
ar v243,a229
mm v[196:199],a[88:91],v[136:139],v[196:199]
ex v194,v194
mm v[196:199],a[92:95],v[140:143],v[196:199]
ld v255,s[60:63],s17 offen offset:1024 sc1 lds
mm v[204:207],a[92:95],v[156:159],0
ex v195,v195
mm v[204:207],a[88:91],v[152:155],v[204:207]
cv v225,v194,v195
mm v[204:207],a[84:87],v[148:151],v[204:207]
ex v200,v200
mm v[204:207],a[80:83],v[144:147],v[204:207]
ld v255,s[60:63],s17 offen offset:2048 sc1 lds
mm v[208:211],a[64:67],v[160:163],0
ex v201,v201
mm v[208:211],a[68:71],v[164:167],v[208:211]
cv v228,v200,v201
mm v[208:211],a[72:75],v[168:171],v[208:211]
ex v202,v202
mm v[208:211],a[76:79],v[172:175],v[208:211]
ld v255,s[60:63],s17 offen offset:3072 sc1 lds
mm v[216:219],a[76:79],v[188:191],0
ex v203,v203
mm v[216:219],a[72:75],v[184:187],v[216:219]
cv v229,v202,v203
s_mov_b32 s93, 0
mm v[216:219],a[68:71],v[180:183],v[216:219]
ex v196,v196
mm v[216:219],a[64:67],v[176:179],v[216:219]
ld a[128:131],v243,s[40:43],s93 offen
mm v[212:215],a[80:83],v[160:163],0
ex v197,v197
s_add_u32 s17, s17, s80
mm v[212:215],a[84:87],v[164:167],v[212:215]
cv v226,v196,v197
s_mov_b32 s93, 7168
s_add_u32 m0, m0, 16384
mm v[212:215],a[88:91],v[168:171],v[212:215]
ex v198,v198
s_and_b32 m0, m0, s78
mm v[212:215],a[92:95],v[172:175],v[212:215]
ld a[132:135],v243,s[40:43],s93 offen
mm v[220:223],a[92:95],v[188:191],0
ex v199,v199
mm v[220:223],a[88:91],v[184:187],v[220:223]
cv v227,v198,v199
mm v[220:223],a[84:87],v[180:183],v[220:223]
ex v204,v204
mm v[220:223],a[80:83],v[176:179],v[220:223]
wt vmcnt(12)
s_barrier
mm v[64:67],a[96:99],v[232:235],v[64:67]
ex v205,v205
mm v[96:99],a[96:99],v[236:239],v[96:99]
rd a[96:99],v248 offset:8192
cv v230,v204,v205
s_mov_b32 s93, 28672
mm v[100:103],a[100:103],v[236:239],v[100:103]
ex v206,v206
mm v[68:71],a[100:103],v[232:235],v[68:71]
rd a[100:103],v248 offset:9216
ld a[144:147],v243,s[40:43],s93 offen
mm v[72:75],a[104:107],v[232:235],v[72:75]
ex v207,v207
mm v[104:107],a[104:107],v[236:239],v[104:107]
rd a[104:107],v248 offset:10240
cv v231,v206,v207
s_mov_b32 s93, 14336
mm v[108:111],a[108:111],v[236:239],v[108:111]
ex v208,v208
mm v[76:79],a[108:111],v[232:235],v[76:79]
rd a[108:111],v248 offset:11264
ld a[136:139],v243,s[40:43],s93 offen
mm v[80:83],a[112:115],v[232:235],v[80:83]
ex v209,v209
mm v[112:115],a[112:115],v[236:239],v[112:115]
rd a[112:115],v248 offset:12288
s_mov_b32 s93, 21504
mm v[116:119],a[116:119],v[236:239],v[116:119]
ex v210,v210
mm v[84:87],a[116:119],v[232:235],v[84:87]
rd a[116:119],v248 offset:13312
ld a[140:143],v243,s[40:43],s93 offen
mm v[88:91],a[120:123],v[232:235],v[88:91]
ex v211,v211
mm v[120:123],a[120:123],v[236:239],v[120:123]
rd a[120:123],v248 offset:14336
s_mov_b32 s93, 35840
mm v[124:127],a[124:127],v[236:239],v[124:127]
ex v216,v216
mm v[92:95],a[124:127],v[232:235],v[92:95]
rd a[124:127],v248 offset:15360
)ASM" R"ASM(ld a[148:151],v243,s[40:43],s93 offen
v_or_b32 v248, s89, v255
v_smfmac_f32_16x16x64_bf16 a[196:199], a[208:211], v[232:239], v241
wt lgkmcnt(4)
v_smfmac_f32_16x16x64_bf16 a[192:195], a[208:211], v[224:231], v241
rd a[64:67],v248 offset:0
mm v[0:3],a[96:99],v[224:227],v[0:3]
ex v217,v217
mm v[32:35],a[96:99],v[228:231],v[32:35]
rd a[68:71],v248 offset:1024
cv v232,v208,v209
mm v[36:39],a[100:103],v[228:231],v[36:39]
ex v218,v218
mm v[4:7],a[100:103],v[224:227],v[4:7]
rd a[72:75],v248 offset:2048
cv v233,v210,v211
mm v[8:11],a[104:107],v[224:227],v[8:11]
ex v219,v219
mm v[40:43],a[104:107],v[228:231],v[40:43]
rd a[76:79],v248 offset:3072
cv v236,v216,v217
mm v[44:47],a[108:111],v[228:231],v[44:47]
ex v212,v212
mm v[12:15],a[108:111],v[224:227],v[12:15]
rd a[80:83],v248 offset:4096
wt lgkmcnt(5)
mm v[16:19],a[112:115],v[224:227],v[16:19]
ex v213,v213
mm v[48:51],a[112:115],v[228:231],v[48:51]
rd a[84:87],v248 offset:5120
cv v237,v218,v219
mm v[52:55],a[116:119],v[228:231],v[52:55]
ex v214,v214
mm v[20:23],a[116:119],v[224:227],v[20:23]
rd a[88:91],v248 offset:6144
cv v234,v212,v213
mm v[24:27],a[120:123],v[224:227],v[24:27]
ex v215,v215
mm v[56:59],a[120:123],v[228:231],v[56:59]
rd a[92:95],v248 offset:7168
cv v235,v214,v215
mm v[60:63],a[124:127],v[228:231],v[60:63]
ex v220,v220
mm v[28:31],a[124:127],v[224:227],v[28:31]
wt lgkmcnt(4)
mm v[192:195],a[64:67],v[128:131],0
ex v221,v221
mm v[192:195],a[68:71],v[132:135],v[192:195]
cv v238,v220,v221
s_add_u32 s89, s89, 16384
mm v[192:195],a[72:75],v[136:139],v[192:195]
ex v222,v222
mm v[192:195],a[76:79],v[140:143],v[192:195]
ld v255,s[60:63],s17 offen offset:0 sc1 lds
mm v[200:203],a[76:79],v[156:159],0
ex v223,v223
mm v[200:203],a[72:75],v[152:155],v[200:203]
cv v239,v222,v223
s_and_b32 s89, s89, 0x1ffff
mm v[200:203],a[68:71],v[148:151],v[200:203]
ex v192,v192
mm v[200:203],a[64:67],v[144:147],v[200:203]
wt lgkmcnt(0)
mm v[196:199],a[80:83],v[128:131],0
ex v193,v193
mm v[196:199],a[84:87],v[132:135],v[196:199]
cv v224,v192,v193
mm v[196:199],a[88:91],v[136:139],v[196:199]
ex v194,v194
mm v[196:199],a[92:95],v[140:143],v[196:199]
ld v255,s[60:63],s17 offen offset:1024 sc1 lds
mm v[204:207],a[92:95],v[156:159],0
ex v195,v195
mm v[204:207],a[88:91],v[152:155],v[204:207]
cv v225,v194,v195
mm v[204:207],a[84:87],v[148:151],v[204:207]
ex v200,v200
mm v[204:207],a[80:83],v[144:147],v[204:207]
ld v255,s[60:63],s17 offen offset:2048 sc1 lds
mm v[208:211],a[64:67],v[160:163],0
ex v201,v201
mm v[208:211],a[68:71],v[164:167],v[208:211]
cv v228,v200,v201
mm v[208:211],a[72:75],v[168:171],v[208:211]
ex v202,v202
mm v[208:211],a[76:79],v[172:175],v[208:211]
ld v255,s[60:63],s17 offen offset:3072 sc1 lds
mm v[216:219],a[76:79],v[188:191],0
ex v203,v203
mm v[216:219],a[72:75],v[184:187],v[216:219]
cv v229,v202,v203
s_mov_b32 s93, 43008
mm v[216:219],a[68:71],v[180:183],v[216:219]
ex v196,v196
mm v[216:219],a[64:67],v[176:179],v[216:219]
ld a[152:155],v243,s[40:43],s93 offen
mm v[212:215],a[80:83],v[160:163],0
ex v197,v197
s_add_u32 s17, s17, s80
mm v[212:215],a[84:87],v[164:167],v[212:215]
)ASM" R"ASM(cv v226,v196,v197
s_mov_b32 s93, 50176
s_add_u32 m0, m0, 16384
mm v[212:215],a[88:91],v[168:171],v[212:215]
ex v198,v198
s_and_b32 m0, m0, s78
mm v[212:215],a[92:95],v[172:175],v[212:215]
ld a[156:159],v243,s[40:43],s93 offen
mm v[220:223],a[92:95],v[188:191],0
ex v199,v199
mm v[220:223],a[88:91],v[184:187],v[220:223]
cv v227,v198,v199
mm v[220:223],a[84:87],v[180:183],v[220:223]
ex v204,v204
mm v[220:223],a[80:83],v[176:179],v[220:223]
wt vmcnt(17)
s_barrier
mm v[64:67],a[96:99],v[232:235],v[64:67]
ex v205,v205
mm v[96:99],a[96:99],v[236:239],v[96:99]
rd a[96:99],v248 offset:8192
cv v230,v204,v205
s_mov_b32 s93, 71680
mm v[100:103],a[100:103],v[236:239],v[100:103]
ex v206,v206
mm v[68:71],a[100:103],v[232:235],v[68:71]
rd a[100:103],v248 offset:9216
ld a[168:171],v243,s[40:43],s93 offen
mm v[72:75],a[104:107],v[232:235],v[72:75]
ex v207,v207
mm v[104:107],a[104:107],v[236:239],v[104:107]
rd a[104:107],v248 offset:10240
cv v231,v206,v207
s_mov_b32 s93, 57344
mm v[108:111],a[108:111],v[236:239],v[108:111]
ex v208,v208
mm v[76:79],a[108:111],v[232:235],v[76:79]
rd a[108:111],v248 offset:11264
ld a[160:163],v243,s[40:43],s93 offen
mm v[80:83],a[112:115],v[232:235],v[80:83]
ex v209,v209
mm v[112:115],a[112:115],v[236:239],v[112:115]
rd a[112:115],v248 offset:12288
s_mov_b32 s93, 64512
mm v[116:119],a[116:119],v[236:239],v[116:119]
ex v210,v210
mm v[84:87],a[116:119],v[232:235],v[84:87]
rd a[116:119],v248 offset:13312
ld a[164:167],v243,s[40:43],s93 offen
mm v[88:91],a[120:123],v[232:235],v[88:91]
ex v211,v211
mm v[120:123],a[120:123],v[236:239],v[120:123]
rd a[120:123],v248 offset:14336
mm v[124:127],a[124:127],v[236:239],v[124:127]
ex v216,v216
mm v[92:95],a[124:127],v[232:235],v[92:95]
rd a[124:127],v248 offset:15360
v_or_b32 v248, s89, v255
v_smfmac_f32_16x16x64_bf16 a[196:199], a[208:211], v[232:239], v241
wt lgkmcnt(4)
v_smfmac_f32_16x16x64_bf16 a[192:195], a[208:211], v[224:231], v241
rd a[64:67],v248 offset:0
mm v[0:3],a[96:99],v[224:227],v[0:3]
ex v217,v217
mm v[32:35],a[96:99],v[228:231],v[32:35]
rd a[68:71],v248 offset:1024
cv v232,v208,v209
mm v[36:39],a[100:103],v[228:231],v[36:39]
ex v218,v218
mm v[4:7],a[100:103],v[224:227],v[4:7]
rd a[72:75],v248 offset:2048
cv v233,v210,v211
mm v[8:11],a[104:107],v[224:227],v[8:11]
ex v219,v219
mm v[40:43],a[104:107],v[228:231],v[40:43]
rd a[76:79],v248 offset:3072
cv v236,v216,v217
mm v[44:47],a[108:111],v[228:231],v[44:47]
ex v212,v212
mm v[12:15],a[108:111],v[224:227],v[12:15]
rd a[80:83],v248 offset:4096
wt lgkmcnt(5)
mm v[16:19],a[112:115],v[224:227],v[16:19]
ex v213,v213
mm v[48:51],a[112:115],v[228:231],v[48:51]
rd a[84:87],v248 offset:5120
cv v237,v218,v219
mm v[52:55],a[116:119],v[228:231],v[52:55]
ex v214,v214
mm v[20:23],a[116:119],v[224:227],v[20:23]
rd a[88:91],v248 offset:6144
cv v234,v212,v213
mm v[24:27],a[120:123],v[224:227],v[24:27]
ex v215,v215
mm v[56:59],a[120:123],v[228:231],v[56:59]
rd a[92:95],v248 offset:7168
cv v235,v214,v215
mm v[60:63],a[124:127],v[228:231],v[60:63]
ex v220,v220
mm v[28:31],a[124:127],v[224:227],v[28:31]
)ASM" R"ASM(wt lgkmcnt(4)
mm v[192:195],a[64:67],v[128:131],0
ex v221,v221
mm v[192:195],a[68:71],v[132:135],v[192:195]
cv v238,v220,v221
s_add_u32 s89, s89, 16384
mm v[192:195],a[72:75],v[136:139],v[192:195]
ex v222,v222
mm v[192:195],a[76:79],v[140:143],v[192:195]
ld v255,s[60:63],s17 offen offset:0 sc1 lds
mm v[200:203],a[76:79],v[156:159],0
ex v223,v223
mm v[200:203],a[72:75],v[152:155],v[200:203]
cv v239,v222,v223
s_and_b32 s89, s89, 0x1ffff
mm v[200:203],a[68:71],v[148:151],v[200:203]
ex v192,v192
mm v[200:203],a[64:67],v[144:147],v[200:203]
wt lgkmcnt(0)
mm v[196:199],a[80:83],v[128:131],0
ex v193,v193
mm v[196:199],a[84:87],v[132:135],v[196:199]
cv v224,v192,v193
mm v[196:199],a[88:91],v[136:139],v[196:199]
ex v194,v194
mm v[196:199],a[92:95],v[140:143],v[196:199]
ld v255,s[60:63],s17 offen offset:1024 sc1 lds
mm v[204:207],a[92:95],v[156:159],0
ex v195,v195
mm v[204:207],a[88:91],v[152:155],v[204:207]
cv v225,v194,v195
mm v[204:207],a[84:87],v[148:151],v[204:207]
ex v200,v200
mm v[204:207],a[80:83],v[144:147],v[204:207]
ld v255,s[60:63],s17 offen offset:2048 sc1 lds
mm v[208:211],a[64:67],v[160:163],0
ex v201,v201
mm v[208:211],a[68:71],v[164:167],v[208:211]
cv v228,v200,v201
mm v[208:211],a[72:75],v[168:171],v[208:211]
ex v202,v202
mm v[208:211],a[76:79],v[172:175],v[208:211]
ld v255,s[60:63],s17 offen offset:3072 sc1 lds
mm v[216:219],a[76:79],v[188:191],0
ex v203,v203
mm v[216:219],a[72:75],v[184:187],v[216:219]
cv v229,v202,v203
s_mov_b32 s93, 78848
mm v[216:219],a[68:71],v[180:183],v[216:219]
ex v196,v196
mm v[216:219],a[64:67],v[176:179],v[216:219]
ld a[172:175],v243,s[40:43],s93 offen
mm v[212:215],a[80:83],v[160:163],0
ex v197,v197
s_add_u32 s17, s17, s80
mm v[212:215],a[84:87],v[164:167],v[212:215]
cv v226,v196,v197
s_mov_b32 s93, 86016
s_add_u32 m0, m0, 16384
mm v[212:215],a[88:91],v[168:171],v[212:215]
ex v198,v198
s_and_b32 m0, m0, s78
mm v[212:215],a[92:95],v[172:175],v[212:215]
ld a[176:179],v243,s[40:43],s93 offen
mm v[220:223],a[92:95],v[188:191],0
ex v199,v199
mm v[220:223],a[88:91],v[184:187],v[220:223]
cv v227,v198,v199
mm v[220:223],a[84:87],v[180:183],v[220:223]
ex v204,v204
mm v[220:223],a[80:83],v[176:179],v[220:223]
wt vmcnt(21)
s_barrier
mm v[64:67],a[96:99],v[232:235],v[64:67]
ex v205,v205
mm v[96:99],a[96:99],v[236:239],v[96:99]
rd a[96:99],v248 offset:8192
cv v230,v204,v205
s_mov_b32 s93, 107520
mm v[100:103],a[100:103],v[236:239],v[100:103]
ex v206,v206
mm v[68:71],a[100:103],v[232:235],v[68:71]
rd a[100:103],v248 offset:9216
ld a[188:191],v243,s[40:43],s93 offen
mm v[72:75],a[104:107],v[232:235],v[72:75]
ex v207,v207
mm v[104:107],a[104:107],v[236:239],v[104:107]
rd a[104:107],v248 offset:10240
cv v231,v206,v207
s_mov_b32 s93, 93184
mm v[108:111],a[108:111],v[236:239],v[108:111]
ex v208,v208
mm v[76:79],a[108:111],v[232:235],v[76:79]
rd a[108:111],v248 offset:11264
ld a[180:183],v243,s[40:43],s93 offen
mm v[80:83],a[112:115],v[232:235],v[80:83]
ex v209,v209
mm v[112:115],a[112:115],v[236:239],v[112:115]
rd a[112:115],v248 offset:12288
s_mov_b32 s93, 100352
)ASM" R"ASM(mm v[116:119],a[116:119],v[236:239],v[116:119]
ex v210,v210
mm v[84:87],a[116:119],v[232:235],v[84:87]
rd a[116:119],v248 offset:13312
ld a[184:187],v243,s[40:43],s93 offen
mm v[88:91],a[120:123],v[232:235],v[88:91]
.La5_LW5_1:
s_cbranch_execz .La5_LW5_R0
ex v211,v211
mm v[120:123],a[120:123],v[236:239],v[120:123]
rd a[120:123],v248 offset:14336
mm v[124:127],a[124:127],v[236:239],v[124:127]
ex v216,v216
mm v[92:95],a[124:127],v[232:235],v[92:95]
rd a[124:127],v248 offset:15360
v_or_b32 v248, s89, v255
v_smfmac_f32_16x16x64_bf16 a[196:199], a[208:211], v[232:239], v241
wt lgkmcnt(4)
v_smfmac_f32_16x16x64_bf16 a[192:195], a[208:211], v[224:231], v241
rd a[64:67],v248 offset:0
mm v[0:3],a[96:99],v[224:227],v[0:3]
ex v217,v217
mm v[32:35],a[96:99],v[228:231],v[32:35]
rd a[68:71],v248 offset:1024
cv v232,v208,v209
mm v[36:39],a[100:103],v[228:231],v[36:39]
ex v218,v218
mm v[4:7],a[100:103],v[224:227],v[4:7]
rd a[72:75],v248 offset:2048
cv v233,v210,v211
mm v[8:11],a[104:107],v[224:227],v[8:11]
ex v219,v219
mm v[40:43],a[104:107],v[228:231],v[40:43]
rd a[76:79],v248 offset:3072
cv v236,v216,v217
mm v[44:47],a[108:111],v[228:231],v[44:47]
ex v212,v212
mm v[12:15],a[108:111],v[224:227],v[12:15]
rd a[80:83],v248 offset:4096
wt lgkmcnt(5)
mm v[16:19],a[112:115],v[224:227],v[16:19]
ex v213,v213
mm v[48:51],a[112:115],v[228:231],v[48:51]
rd a[84:87],v248 offset:5120
cv v237,v218,v219
mm v[52:55],a[116:119],v[228:231],v[52:55]
ex v214,v214
mm v[20:23],a[116:119],v[224:227],v[20:23]
rd a[88:91],v248 offset:6144
cv v234,v212,v213
mm v[24:27],a[120:123],v[224:227],v[24:27]
ex v215,v215
mm v[56:59],a[120:123],v[228:231],v[56:59]
rd a[92:95],v248 offset:7168
cv v235,v214,v215
mm v[60:63],a[124:127],v[228:231],v[60:63]
ex v220,v220
mm v[28:31],a[124:127],v[224:227],v[28:31]
.La5_LA_DC3:
.La5_LW5_2:
s_cbranch_execz .La5_LW5_R0
wt lgkmcnt(4)
mm v[192:195],a[64:67],v[128:131],0
ex v221,v221
mm v[192:195],a[68:71],v[132:135],v[192:195]
cv v238,v220,v221
ar v240,a228
mm v[192:195],a[72:75],v[136:139],v[192:195]
ex v222,v222
mm v[192:195],a[76:79],v[140:143],v[192:195]
s_add_u32 m0, s83, 16896
s_lshl_b32 s17, s72, 18
mm v[200:203],a[76:79],v[156:159],0
ex v223,v223
mm v[200:203],a[72:75],v[152:155],v[200:203]
cv v239,v222,v223
s_add_u32 s89, s89, 16384
mm v[200:203],a[68:71],v[148:151],v[200:203]
ex v192,v192
mm v[200:203],a[64:67],v[144:147],v[200:203]
wt lgkmcnt(0)
s_and_b32 s89, s89, 0x1ffff
mm v[196:199],a[80:83],v[128:131],0
ex v193,v193
mm v[196:199],a[84:87],v[132:135],v[196:199]
cv v224,v192,v193
mm v[196:199],a[88:91],v[136:139],v[196:199]
ex v194,v194
mm v[196:199],a[92:95],v[140:143],v[196:199]
ld v240,s[56:59],s17 offen lds
mm v[204:207],a[92:95],v[156:159],0
ex v195,v195
s_add_u32 s17, s17, 32768
mm v[204:207],a[88:91],v[152:155],v[204:207]
cv v225,v194,v195
s_add_u32 m0, m0, 1024
mm v[204:207],a[84:87],v[148:151],v[204:207]
ex v200,v200
mm v[204:207],a[80:83],v[144:147],v[204:207]
ld v240,s[56:59],s17 offen lds
)ASM" R"ASM(mm v[208:211],a[64:67],v[160:163],0
ex v201,v201
s_add_u32 s17, s17, 4294942720
mm v[208:211],a[68:71],v[164:167],v[208:211]
cv v228,v200,v201
s_add_u32 m0, m0, 1152
mm v[208:211],a[72:75],v[168:171],v[208:211]
ex v202,v202
mm v[208:211],a[76:79],v[172:175],v[208:211]
ld v240,s[56:59],s17 offen lds
mm v[216:219],a[76:79],v[188:191],0
ex v203,v203
s_add_u32 s17, s17, 32768
mm v[216:219],a[72:75],v[184:187],v[216:219]
cv v229,v202,v203
s_add_u32 m0, m0, 1024
mm v[216:219],a[68:71],v[180:183],v[216:219]
ex v196,v196
mm v[216:219],a[64:67],v[176:179],v[216:219]
ld v240,s[56:59],s17 offen lds
mm v[212:215],a[80:83],v[160:163],0
ex v197,v197
s_add_u32 s17, s17, 90112
mm v[212:215],a[84:87],v[164:167],v[212:215]
cv v226,v196,v197
s_add_u32 m0, s83, 33792
mm v[212:215],a[88:91],v[168:171],v[212:215]
ex v198,v198
mm v[212:215],a[92:95],v[172:175],v[212:215]
mm v[220:223],a[92:95],v[188:191],0
ex v199,v199
mm v[220:223],a[88:91],v[184:187],v[220:223]
cv v227,v198,v199
mm v[220:223],a[84:87],v[180:183],v[220:223]
ex v204,v204
mm v[220:223],a[80:83],v[176:179],v[220:223]
wt vmcnt(10)
s_barrier
mm v[64:67],a[96:99],v[232:235],v[64:67]
ex v205,v205
mm v[96:99],a[96:99],v[236:239],v[96:99]
rd a[96:99],v248 offset:8192
cv v230,v204,v205
mm v[100:103],a[100:103],v[236:239],v[100:103]
ex v206,v206
mm v[68:71],a[100:103],v[232:235],v[68:71]
rd a[100:103],v248 offset:9216
mm v[72:75],a[104:107],v[232:235],v[72:75]
ex v207,v207
mm v[104:107],a[104:107],v[236:239],v[104:107]
rd a[104:107],v248 offset:10240
cv v231,v206,v207
mm v[108:111],a[108:111],v[236:239],v[108:111]
ex v208,v208
mm v[76:79],a[108:111],v[232:235],v[76:79]
rd a[108:111],v248 offset:11264
mm v[80:83],a[112:115],v[232:235],v[80:83]
ex v209,v209
mm v[112:115],a[112:115],v[236:239],v[112:115]
rd a[112:115],v248 offset:12288
mm v[116:119],a[116:119],v[236:239],v[116:119]
ex v210,v210
mm v[84:87],a[116:119],v[232:235],v[84:87]
rd a[116:119],v248 offset:13312
mm v[88:91],a[120:123],v[232:235],v[88:91]
ex v211,v211
mm v[120:123],a[120:123],v[236:239],v[120:123]
rd a[120:123],v248 offset:14336
mm v[124:127],a[124:127],v[236:239],v[124:127]
ex v216,v216
mm v[92:95],a[124:127],v[232:235],v[92:95]
rd a[124:127],v248 offset:15360
v_or_b32 v248, s89, v255
v_smfmac_f32_16x16x64_bf16 a[196:199], a[208:211], v[232:239], v241
wt lgkmcnt(4)
v_smfmac_f32_16x16x64_bf16 a[192:195], a[208:211], v[224:231], v241
rd a[64:67],v248 offset:0
mm v[0:3],a[96:99],v[224:227],v[0:3]
ex v217,v217
mm v[32:35],a[96:99],v[228:231],v[32:35]
rd a[68:71],v248 offset:1024
cv v232,v208,v209
mm v[36:39],a[100:103],v[228:231],v[36:39]
ex v218,v218
mm v[4:7],a[100:103],v[224:227],v[4:7]
rd a[72:75],v248 offset:2048
cv v233,v210,v211
mm v[8:11],a[104:107],v[224:227],v[8:11]
ex v219,v219
mm v[40:43],a[104:107],v[228:231],v[40:43]
rd a[76:79],v248 offset:3072
cv v236,v216,v217
mm v[44:47],a[108:111],v[228:231],v[44:47]
ex v212,v212
mm v[12:15],a[108:111],v[224:227],v[12:15]
rd a[80:83],v248 offset:4096
wt lgkmcnt(5)
mm v[16:19],a[112:115],v[224:227],v[16:19]
ex v213,v213
mm v[48:51],a[112:115],v[228:231],v[48:51]
)ASM" R"ASM(rd a[84:87],v248 offset:5120
cv v237,v218,v219
mm v[52:55],a[116:119],v[228:231],v[52:55]
ex v214,v214
mm v[20:23],a[116:119],v[224:227],v[20:23]
rd a[88:91],v248 offset:6144
cv v234,v212,v213
mm v[24:27],a[120:123],v[224:227],v[24:27]
ex v215,v215
mm v[56:59],a[120:123],v[228:231],v[56:59]
rd a[92:95],v248 offset:7168
cv v235,v214,v215
mm v[60:63],a[124:127],v[228:231],v[60:63]
ex v220,v220
mm v[28:31],a[124:127],v[224:227],v[28:31]
buffer_load_dword v250, v251, s[64:67], s20 offen sc1
wt lgkmcnt(4)
mm v[192:195],a[64:67],v[128:131],0
ex v221,v221
mm v[192:195],a[68:71],v[132:135],v[192:195]
cv v238,v220,v221
s_add_u32 s89, s89, 16384
mm v[192:195],a[72:75],v[136:139],v[192:195]
ex v222,v222
mm v[192:195],a[76:79],v[140:143],v[192:195]
ld v240,s[56:59],s17 offen lds
mm v[200:203],a[76:79],v[156:159],0
ex v223,v223
s_add_u32 s17, s17, 32768
mm v[200:203],a[72:75],v[152:155],v[200:203]
cv v239,v222,v223
s_add_u32 m0, m0, 1024
mm v[200:203],a[68:71],v[148:151],v[200:203]
ex v192,v192
mm v[200:203],a[64:67],v[144:147],v[200:203]
wt lgkmcnt(0)
s_and_b32 s89, s89, 0x1ffff
mm v[196:199],a[80:83],v[128:131],0
ex v193,v193
mm v[196:199],a[84:87],v[132:135],v[196:199]
cv v224,v192,v193
mm v[196:199],a[88:91],v[136:139],v[196:199]
ex v194,v194
mm v[196:199],a[92:95],v[140:143],v[196:199]
ld v240,s[56:59],s17 offen lds
mm v[204:207],a[92:95],v[156:159],0
ex v195,v195
s_add_u32 s17, s17, 4294942720
mm v[204:207],a[88:91],v[152:155],v[204:207]
cv v225,v194,v195
s_add_u32 m0, m0, 1152
mm v[204:207],a[84:87],v[148:151],v[204:207]
ex v200,v200
mm v[204:207],a[80:83],v[144:147],v[204:207]
ld v240,s[56:59],s17 offen lds
mm v[208:211],a[64:67],v[160:163],0
ex v201,v201
s_add_u32 s17, s17, 32768
mm v[208:211],a[68:71],v[164:167],v[208:211]
cv v228,v200,v201
s_add_u32 m0, m0, 1024
mm v[208:211],a[72:75],v[168:171],v[208:211]
ex v202,v202
mm v[208:211],a[76:79],v[172:175],v[208:211]
ld v240,s[56:59],s17 offen lds
mm v[216:219],a[76:79],v[188:191],0
ex v203,v203
mm v[216:219],a[72:75],v[184:187],v[216:219]
cv v229,v202,v203
mm v[216:219],a[68:71],v[180:183],v[216:219]
ex v196,v196
mm v[216:219],a[64:67],v[176:179],v[216:219]
mm v[212:215],a[80:83],v[160:163],0
ex v197,v197
s_bfe_u32 s17, s4, 262148
mm v[212:215],a[84:87],v[164:167],v[212:215]
cv v226,v196,v197
s_xor_b32 s17, s17, s72
s_lshl_b32 s17, s17, 18
mm v[212:215],a[88:91],v[168:171],v[212:215]
ex v198,v198
s_add_u32 m0, s83, 50688
mm v[212:215],a[92:95],v[172:175],v[212:215]
mm v[220:223],a[92:95],v[188:191],0
ex v199,v199
mm v[220:223],a[88:91],v[184:187],v[220:223]
cv v227,v198,v199
wt vmcnt(4)
v_cmp_eq_u32 s[92:93], v250, v249
s_mov_b32 s94, 16384
mm v[220:223],a[84:87],v[180:183],v[220:223]
ex v204,v204
mm v[220:223],a[80:83],v[176:179],v[220:223]
s_nop 0
s_barrier
mm v[64:67],a[96:99],v[232:235],v[64:67]
ex v205,v205
mm v[96:99],a[96:99],v[236:239],v[96:99]
rd a[96:99],v248 offset:8192
cv v230,v204,v205
)ASM" R"ASM(s_and_b32 s92, s92, 3
s_andn2_b32 s92, s92, s23
s_cmp_eq_u32 s92, 3
s_cselect_b32 s70, 17408, 0
s_cselect_b32 s84, 3, s23
mm v[100:103],a[100:103],v[236:239],v[100:103]
ex v206,v206
mm v[68:71],a[100:103],v[232:235],v[68:71]
rd a[100:103],v248 offset:9216
ld a[220:223],v255,s[68:71],s94 offen sc1
mm v[72:75],a[104:107],v[232:235],v[72:75]
ex v207,v207
mm v[104:107],a[104:107],v[236:239],v[104:107]
rd a[104:107],v248 offset:10240
cv v231,v206,v207
mm v[108:111],a[108:111],v[236:239],v[108:111]
ex v208,v208
mm v[76:79],a[108:111],v[232:235],v[76:79]
rd a[108:111],v248 offset:11264
ld a[0:3],v255,s[68:71],0 offen offset:0 sc1
mm v[80:83],a[112:115],v[232:235],v[80:83]
ex v209,v209
mm v[112:115],a[112:115],v[236:239],v[112:115]
rd a[112:115],v248 offset:12288
mm v[116:119],a[116:119],v[236:239],v[116:119]
ex v210,v210
mm v[84:87],a[116:119],v[232:235],v[84:87]
rd a[116:119],v248 offset:13312
ld a[4:7],v255,s[68:71],0 offen offset:1024 sc1
mm v[88:91],a[120:123],v[232:235],v[88:91]
ex v211,v211
mm v[120:123],a[120:123],v[236:239],v[120:123]
rd a[120:123],v248 offset:14336
mm v[124:127],a[124:127],v[236:239],v[124:127]
ex v216,v216
mm v[92:95],a[124:127],v[232:235],v[92:95]
rd a[124:127],v248 offset:15360
v_or_b32 v248, s89, v255
buffer_load_dword v250, v251, s[64:67], s20 offen sc1
v_smfmac_f32_16x16x64_bf16 a[196:199], a[208:211], v[232:239], v241
wt lgkmcnt(4)
v_smfmac_f32_16x16x64_bf16 a[192:195], a[208:211], v[224:231], v241
rd a[64:67],v248 offset:0
mm v[0:3],a[96:99],v[224:227],v[0:3]
ex v217,v217
mm v[32:35],a[96:99],v[228:231],v[32:35]
rd a[68:71],v248 offset:1024
cv v232,v208,v209
ld a[8:11],v255,s[68:71],0 offen offset:2048 sc1
mm v[36:39],a[100:103],v[228:231],v[36:39]
ex v218,v218
mm v[4:7],a[100:103],v[224:227],v[4:7]
rd a[72:75],v248 offset:2048
cv v233,v210,v211
mm v[8:11],a[104:107],v[224:227],v[8:11]
ex v219,v219
mm v[40:43],a[104:107],v[228:231],v[40:43]
rd a[76:79],v248 offset:3072
cv v236,v216,v217
ld a[12:15],v255,s[68:71],0 offen offset:3072 sc1
mm v[44:47],a[108:111],v[228:231],v[44:47]
ex v212,v212
mm v[12:15],a[108:111],v[224:227],v[12:15]
rd a[80:83],v248 offset:4096
wt lgkmcnt(5)
mm v[16:19],a[112:115],v[224:227],v[16:19]
ex v213,v213
mm v[48:51],a[112:115],v[228:231],v[48:51]
rd a[84:87],v248 offset:5120
cv v237,v218,v219
ld a[16:19],v255,s[68:71],s95 offen offset:0 sc1
mm v[52:55],a[116:119],v[228:231],v[52:55]
ex v214,v214
mm v[20:23],a[116:119],v[224:227],v[20:23]
rd a[88:91],v248 offset:6144
cv v234,v212,v213
mm v[24:27],a[120:123],v[224:227],v[24:27]
ex v215,v215
mm v[56:59],a[120:123],v[228:231],v[56:59]
rd a[92:95],v248 offset:7168
cv v235,v214,v215
ld a[20:23],v255,s[68:71],s95 offen offset:1024 sc1
mm v[60:63],a[124:127],v[228:231],v[60:63]
ex v220,v220
mm v[28:31],a[124:127],v[224:227],v[28:31]
ld a[24:27],v255,s[68:71],s95 offen offset:2048 sc1
wt lgkmcnt(4)
mm v[192:195],a[64:67],v[128:131],0
ex v221,v221
mm v[192:195],a[68:71],v[132:135],v[192:195]
cv v238,v220,v221
mm v[192:195],a[72:75],v[136:139],v[192:195]
ex v222,v222
mm v[192:195],a[76:79],v[140:143],v[192:195]
ld v240,s[56:59],s17 offen lds
mm v[200:203],a[76:79],v[156:159],0
ex v223,v223
s_add_u32 s17, s17, 32768
mm v[200:203],a[72:75],v[152:155],v[200:203]
cv v239,v222,v223
)ASM" R"ASM(s_add_u32 m0, m0, 1024
mm v[200:203],a[68:71],v[148:151],v[200:203]
ex v192,v192
mm v[200:203],a[64:67],v[144:147],v[200:203]
wt lgkmcnt(0)
mm v[196:199],a[80:83],v[128:131],0
ex v193,v193
mm v[196:199],a[84:87],v[132:135],v[196:199]
cv v224,v192,v193
wt vmcnt(6)
v_cmp_eq_u32 s[92:93], v250, v249
mm v[196:199],a[88:91],v[136:139],v[196:199]
ex v194,v194
mm v[196:199],a[92:95],v[140:143],v[196:199]
ld v240,s[56:59],s17 offen lds
mm v[204:207],a[92:95],v[156:159],0
ex v195,v195
s_add_u32 s17, s17, 4294942720
mm v[204:207],a[88:91],v[152:155],v[204:207]
cv v225,v194,v195
s_add_u32 m0, m0, 1152
s_and_b32 s92, s92, 3
s_sub_u32 s92, s92, s84
s_cmp_eq_u32 s92, 3
s_cbranch_scc1 .La5_LA_PS0
.La5_LA_PS1:
mm v[204:207],a[84:87],v[148:151],v[204:207]
ex v200,v200
mm v[204:207],a[80:83],v[144:147],v[204:207]
ld v240,s[56:59],s17 offen lds
mm v[208:211],a[64:67],v[160:163],0
ex v201,v201
s_add_u32 s17, s17, 32768
mm v[208:211],a[68:71],v[164:167],v[208:211]
cv v228,v200,v201
s_add_u32 m0, m0, 1024
ld a[28:31],v255,s[68:71],s95 offen offset:3072 sc1
mm v[208:211],a[72:75],v[168:171],v[208:211]
ex v202,v202
mm v[208:211],a[76:79],v[172:175],v[208:211]
ld v240,s[56:59],s17 offen lds
mm v[216:219],a[76:79],v[188:191],0
ex v203,v203
mm v[216:219],a[72:75],v[184:187],v[216:219]
cv v229,v202,v203
ld a[32:35],v255,s[68:71],s96 offen offset:0 sc1
mm v[216:219],a[68:71],v[180:183],v[216:219]
ex v196,v196
mm v[216:219],a[64:67],v[176:179],v[216:219]
mm v[212:215],a[80:83],v[160:163],0
ex v197,v197
mm v[212:215],a[84:87],v[164:167],v[212:215]
cv v226,v196,v197
buffer_load_dword v250, v251, s[64:67], s20 offen sc1
mm v[212:215],a[88:91],v[168:171],v[212:215]
ex v198,v198
mm v[212:215],a[92:95],v[172:175],v[212:215]
ld a[36:39],v255,s[68:71],s96 offen offset:1024 sc1
mm v[220:223],a[92:95],v[188:191],0
ex v199,v199
mm v[220:223],a[88:91],v[184:187],v[220:223]
cv v227,v198,v199
mm v[220:223],a[84:87],v[180:183],v[220:223]
ex v204,v204
mm v[220:223],a[80:83],v[176:179],v[220:223]
ld a[40:43],v255,s[68:71],s96 offen offset:2048 sc1
mm v[64:67],a[96:99],v[232:235],v[64:67]
ex v205,v205
mm v[96:99],a[96:99],v[236:239],v[96:99]
rd a[96:99],v248 offset:8192
cv v230,v204,v205
mm v[100:103],a[100:103],v[236:239],v[100:103]
ex v206,v206
mm v[68:71],a[100:103],v[232:235],v[68:71]
rd a[100:103],v248 offset:9216
ld a[44:47],v255,s[68:71],s96 offen offset:3072 sc1
mm v[72:75],a[104:107],v[232:235],v[72:75]
ex v207,v207
mm v[104:107],a[104:107],v[236:239],v[104:107]
rd a[104:107],v248 offset:10240
cv v231,v206,v207
mm v[108:111],a[108:111],v[236:239],v[108:111]
ex v208,v208
mm v[76:79],a[108:111],v[232:235],v[76:79]
rd a[108:111],v248 offset:11264
ld a[48:51],v255,s[68:71],s97 offen offset:0 sc1
mm v[80:83],a[112:115],v[232:235],v[80:83]
ex v209,v209
mm v[112:115],a[112:115],v[236:239],v[112:115]
rd a[112:115],v248 offset:12288
mm v[116:119],a[116:119],v[236:239],v[116:119]
ex v210,v210
mm v[84:87],a[116:119],v[232:235],v[84:87]
rd a[116:119],v248 offset:13312
ld a[52:55],v255,s[68:71],s97 offen offset:1024 sc1
mm v[88:91],a[120:123],v[232:235],v[88:91]
ex v211,v211
mm v[120:123],a[120:123],v[236:239],v[120:123]
rd a[120:123],v248 offset:14336
mm v[124:127],a[124:127],v[236:239],v[124:127]
)ASM" R"ASM(ex v216,v216
mm v[92:95],a[124:127],v[232:235],v[92:95]
rd a[124:127],v248 offset:15360
ld a[56:59],v255,s[68:71],s97 offen offset:2048 sc1
v_smfmac_f32_16x16x64_bf16 a[196:199], a[208:211], v[232:239], v241
wt lgkmcnt(4)
v_smfmac_f32_16x16x64_bf16 a[192:195], a[208:211], v[224:231], v241
ld a[60:63],v255,s[68:71],s97 offen offset:3072 sc1
mm v[0:3],a[96:99],v[224:227],v[0:3]
ex v217,v217
mm v[32:35],a[96:99],v[228:231],v[32:35]
cv v232,v208,v209
mm v[36:39],a[100:103],v[228:231],v[36:39]
ex v218,v218
mm v[4:7],a[100:103],v[224:227],v[4:7]
cv v233,v210,v211
mm v[8:11],a[104:107],v[224:227],v[8:11]
ex v219,v219
mm v[40:43],a[104:107],v[228:231],v[40:43]
cv v236,v216,v217
mm v[44:47],a[108:111],v[228:231],v[44:47]
ex v212,v212
wt vmcnt(7)
v_cmp_eq_u32 s[92:93], v250, v249
mm v[12:15],a[108:111],v[224:227],v[12:15]
wt lgkmcnt(0)
mm v[16:19],a[112:115],v[224:227],v[16:19]
ex v213,v213
mm v[48:51],a[112:115],v[228:231],v[48:51]
cv v237,v218,v219
s_and_b32 s92, s92, 3
s_sub_u32 s92, s92, s84
mm v[52:55],a[116:119],v[228:231],v[52:55]
ex v214,v214
mm v[20:23],a[116:119],v[224:227],v[20:23]
cv v234,v212,v213
mm v[24:27],a[120:123],v[224:227],v[24:27]
ex v215,v215
mm v[56:59],a[120:123],v[228:231],v[56:59]
cv v235,v214,v215
mm v[60:63],a[124:127],v[228:231],v[60:63]
ex v220,v220
mm v[28:31],a[124:127],v[224:227],v[28:31]
s_cmp_eq_u32 s92, 3
s_cbranch_scc1 .La5_LA_PS2
.La5_LA_PS3:
s_cmp_lg_u32 s84, 3
s_cbranch_scc1 .La5_LA_PSPIN
; ---- owner: partner-s part added, l checked, O / l leaves; first steps of the O projection
.La5_LA_DC1:
wt vmcnt(8)
mm v[0:3],a[214:217],a[0:3],v[0:3]
ar v250,a192
ar v251,a193
v_add_f32 v250, v250, v251
ar v251,a220
v_add_f32 v250, v250, v251
ar v249,a194
mm v[4:7],a[212:215],a[0:3],v[4:7]
ar v251,a195
v_add_f32 v249, v249, v251
ar v251,a221
v_add_f32 v249, v249, v251
v_subrev_u32 v251, 562036736, v250
mm v[8:11],a[214:217],a[4:7],v[8:11]
v_rcp_f32 v250, v250
v_cmp_le_u32 vcc, 1006632960, v251
v_subrev_u32 v251, 562036736, v249
s_mov_b64 s[92:93], vcc
v_cmp_le_u32 vcc, 1006632960, v251
mm v[12:15],a[212:215],a[4:7],v[12:15]
v_rcp_f32 v249, v249
mm v[16:19],a[214:217],a[8:11],v[16:19]
s_or_b64 vcc, vcc, s[92:93]
s_cbranch_scc1 .La5_LA_SC3
.La5_LA_SR3:
ex v221,v221
ex v222,v222
mm v[20:23],a[212:215],a[8:11],v[20:23]
ex v223,v223
cv v238,v220,v221
v_mul_f32 v0, v0, v250
v_mul_f32 v1, v1, v250
v_mul_f32 v2, v2, v250
mm v[24:27],a[214:217],a[12:15],v[24:27]
v_mul_f32 v3, v3, v250
v_mul_f32 v4, v4, v250
v_mul_f32 v5, v5, v250
v_mul_f32 v6, v6, v250
v_mul_f32 v7, v7, v250
cv v128,v0,v1
mm v[28:31],a[212:215],a[12:15],v[28:31]
cv v129,v2,v3
cv v130,v4,v5
cv v131,v6,v7
st v[128:131],v244,s[44:47],s91 offen offset:0 sc1
cv v239,v222,v223
mm v[32:35],a[214:217],a[16:19],v[32:35]
v_mul_f32 v8, v8, v250
v_mul_f32 v9, v9, v250
)ASM" R"ASM(v_mul_f32 v10, v10, v250
v_mul_f32 v11, v11, v250
v_mul_f32 v12, v12, v250
v_mul_f32 v13, v13, v250
mm v[36:39],a[212:215],a[16:19],v[36:39]
v_mul_f32 v14, v14, v250
v_mul_f32 v15, v15, v250
cv v132,v8,v9
cv v133,v10,v11
cv v134,v12,v13
cv v135,v14,v15
mm v[40:43],a[214:217],a[20:23],v[40:43]
st v[132:135],v244,s[44:47],s91 offen offset:1024 sc1
v_mul_f32 v16, v16, v250
v_mul_f32 v17, v17, v250
v_mul_f32 v18, v18, v250
.La5_LW5_3:
s_cbranch_execz .La5_LW5_R0
v_mul_f32 v19, v19, v250
mm v[44:47],a[212:215],a[20:23],v[44:47]
v_mul_f32 v20, v20, v250
v_mul_f32 v21, v21, v250
v_mul_f32 v22, v22, v250
v_mul_f32 v23, v23, v250
cv v136,v16,v17
cv v137,v18,v19
mm v[48:51],a[214:217],a[24:27],v[48:51]
cv v138,v20,v21
cv v139,v22,v23
st v[136:139],v244,s[44:47],s91 offen offset:2048 sc1
v_mul_f32 v24, v24, v250
v_mul_f32 v25, v25, v250
mm v[52:55],a[212:215],a[24:27],v[52:55]
v_mul_f32 v26, v26, v250
v_mul_f32 v27, v27, v250
v_mul_f32 v28, v28, v250
v_mul_f32 v29, v29, v250
v_mul_f32 v30, v30, v250
v_mul_f32 v31, v31, v250
mm v[56:59],a[214:217],a[28:31],v[56:59]
cv v140,v24,v25
cv v141,v26,v27
cv v142,v28,v29
cv v143,v30,v31
st v[140:143],v244,s[44:47],s91 offen offset:3072 sc1
mm v[60:63],a[212:215],a[28:31],v[60:63]
v_mul_f32 v32, v32, v249
v_mul_f32 v33, v33, v249
v_mul_f32 v34, v34, v249
v_mul_f32 v35, v35, v249
v_mul_f32 v36, v36, v249
v_mul_f32 v37, v37, v249
v_smfmac_f32_16x16x64_bf16 a[196:199], a[208:211], v[232:239], v241
v_mul_f32 v38, v38, v249
v_mul_f32 v39, v39, v249
cv v144,v32,v33
cv v145,v34,v35
cv v146,v36,v37
mm v[64:67],a[96:99],v[232:235],v[64:67]
cv v147,v38,v39
st v[144:147],v245,s[44:47],s91 offen offset:0 sc1
v_mul_f32 v40, v40, v249
v_mul_f32 v41, v41, v249
v_mul_f32 v42, v42, v249
mm v[96:99],a[96:99],v[236:239],v[96:99]
v_mul_f32 v43, v43, v249
v_mul_f32 v44, v44, v249
v_mul_f32 v45, v45, v249
v_mul_f32 v46, v46, v249
v_mul_f32 v47, v47, v249
cv v148,v40,v41
mm v[100:103],a[100:103],v[236:239],v[100:103]
cv v149,v42,v43
cv v150,v44,v45
cv v151,v46,v47
st v[148:151],v245,s[44:47],s91 offen offset:1024 sc1
v_mul_f32 v48, v48, v249
mm v[68:71],a[100:103],v[232:235],v[68:71]
v_mul_f32 v49, v49, v249
v_mul_f32 v50, v50, v249
v_mul_f32 v51, v51, v249
v_mul_f32 v52, v52, v249
v_mul_f32 v53, v53, v249
v_mul_f32 v54, v54, v249
mm v[72:75],a[104:107],v[232:235],v[72:75]
v_mul_f32 v55, v55, v249
cv v152,v48,v49
cv v153,v50,v51
cv v154,v52,v53
cv v155,v54,v55
st v[152:155],v245,s[44:47],s91 offen offset:2048 sc1
mm v[104:107],a[104:107],v[236:239],v[104:107]
v_mul_f32 v56, v56, v249
v_mul_f32 v57, v57, v249
v_mul_f32 v58, v58, v249
v_mul_f32 v59, v59, v249
v_mul_f32 v60, v60, v249
v_mul_f32 v61, v61, v249
mm v[108:111],a[108:111],v[236:239],v[108:111]
v_mul_f32 v62, v62, v249
)ASM" R"ASM(v_mul_f32 v63, v63, v249
cv v156,v56,v57
cv v157,v58,v59
cv v158,v60,v61
cv v159,v62,v63
mm v[76:79],a[108:111],v[232:235],v[76:79]
st v[156:159],v245,s[44:47],s91 offen offset:3072 sc1
ar v240,a224
v_xor_b32 v241, 32, v240
v_xor_b32 v242, 64, v240
v_xor_b32 v243, 96, v240
mm v[80:83],a[112:115],v[232:235],v[80:83]
mm v[112:115],a[112:115],v[236:239],v[112:115]
mm v[116:119],a[116:119],v[236:239],v[116:119]
mm v[84:87],a[116:119],v[232:235],v[84:87]
mm v[88:91],a[120:123],v[232:235],v[88:91]
mm v[120:123],a[120:123],v[236:239],v[120:123]
mm v[124:127],a[124:127],v[236:239],v[124:127]
mm v[92:95],a[124:127],v[232:235],v[92:95]
s_nop 0
s_barrier
wt vmcnt(8)
mm v[64:67],a[214:217],a[32:35],v[64:67]
rt v[192:193],v240 offset:16896
rt v[194:195],v240 offset:17920
mm v[68:71],a[212:215],a[32:35],v[68:71]
rt v[196:197],v240 offset:25344
rt v[198:199],v240 offset:26368
mm v[72:75],a[214:217],a[36:39],v[72:75]
rt v[200:201],v241 offset:16896
rt v[202:203],v241 offset:17920
ar v250,a196
ar v251,a197
v_add_f32 v250, v250, v251
ar v251,a222
mm v[76:79],a[212:215],a[36:39],v[76:79]
rt v[204:205],v241 offset:25344
rt v[206:207],v241 offset:26368
v_add_f32 v250, v250, v251
ar v249,a198
ar v251,a199
v_add_f32 v249, v249, v251
mm v[80:83],a[214:217],a[40:43],v[80:83]
rt v[208:209],v242 offset:16896
rt v[210:211],v242 offset:17920
ar v251,a223
v_add_f32 v249, v249, v251
v_subrev_u32 v251, 562036736, v250
mm v[84:87],a[212:215],a[40:43],v[84:87]
rt v[212:213],v242 offset:25344
rt v[214:215],v242 offset:26368
v_rcp_f32 v250, v250
v_cmp_le_u32 vcc, 1006632960, v251
v_subrev_u32 v251, 562036736, v249
mm v[88:91],a[214:217],a[44:47],v[88:91]
rt v[216:217],v243 offset:16896
rt v[218:219],v243 offset:17920
s_mov_b64 s[92:93], vcc
v_cmp_le_u32 vcc, 1006632960, v251
v_rcp_f32 v249, v249
mm v[92:95],a[212:215],a[44:47],v[92:95]
rt v[220:221],v243 offset:25344
rt v[222:223],v243 offset:26368
s_or_b64 vcc, vcc, s[92:93]
s_cbranch_scc1 .La5_LA_SC4
.La5_LA_SR4:
v_mul_f32 v64, v64, v250
v_mul_f32 v65, v65, v250
mm v[96:99],a[214:217],a[48:51],v[96:99]
v_mul_f32 v66, v66, v250
v_mul_f32 v67, v67, v250
v_mul_f32 v68, v68, v250
v_mul_f32 v69, v69, v250
v_mul_f32 v70, v70, v250
v_mul_f32 v71, v71, v250
mm v[100:103],a[212:215],a[48:51],v[100:103]
cv v160,v64,v65
cv v161,v66,v67
cv v162,v68,v69
cv v163,v70,v71
st v[160:163],v246,s[44:47],s91 offen offset:0 sc1
mm v[104:107],a[214:217],a[52:55],v[104:107]
v_mul_f32 v72, v72, v250
v_mul_f32 v73, v73, v250
v_mul_f32 v74, v74, v250
v_mul_f32 v75, v75, v250
v_mul_f32 v76, v76, v250
v_mul_f32 v77, v77, v250
mm v[108:111],a[212:215],a[52:55],v[108:111]
v_mul_f32 v78, v78, v250
v_mul_f32 v79, v79, v250
cv v164,v72,v73
cv v165,v74,v75
cv v166,v76,v77
cv v167,v78,v79
mm v[112:115],a[214:217],a[56:59],v[112:115]
st v[164:167],v246,s[44:47],s91 offen offset:1024 sc1
v_mul_f32 v80, v80, v250
v_mul_f32 v81, v81, v250
v_mul_f32 v82, v82, v250
)ASM" R"ASM(v_mul_f32 v83, v83, v250
mm v[116:119],a[212:215],a[56:59],v[116:119]
v_mul_f32 v84, v84, v250
v_mul_f32 v85, v85, v250
v_mul_f32 v86, v86, v250
v_mul_f32 v87, v87, v250
cv v168,v80,v81
cv v169,v82,v83
mm v[120:123],a[214:217],a[60:63],v[120:123]
cv v170,v84,v85
cv v171,v86,v87
st v[168:171],v246,s[44:47],s91 offen offset:2048 sc1
v_mul_f32 v88, v88, v250
v_mul_f32 v89, v89, v250
mm v[124:127],a[212:215],a[60:63],v[124:127]
wt lgkmcnt(12)
v_mul_f32 v90, v90, v250
v_mul_f32 v91, v91, v250
v_mul_f32 v92, v92, v250
v_mul_f32 v93, v93, v250
v_mul_f32 v94, v94, v250
mm v[0:3],v[192:195],v[128:131],0
v_mul_f32 v95, v95, v250
cv v172,v88,v89
cv v173,v90,v91
cv v174,v92,v93
cv v175,v94,v95
st v[172:175],v246,s[44:47],s91 offen offset:3072 sc1
mm v[0:3],v[196:199],v[132:135],v[0:3]
v_mul_f32 v96, v96, v249
v_mul_f32 v97, v97, v249
v_mul_f32 v98, v98, v249
v_mul_f32 v99, v99, v249
v_mul_f32 v100, v100, v249
v_mul_f32 v101, v101, v249
mm v[32:35],v[196:199],v[148:151],0
wt lgkmcnt(8)
v_mul_f32 v102, v102, v249
v_mul_f32 v103, v103, v249
cv v176,v96,v97
cv v177,v98,v99
cv v178,v100,v101
mm v[32:35],v[192:195],v[144:147],v[32:35]
rt v[196:197],v240 offset:17024
rt v[198:199],v240 offset:18048
cv v179,v102,v103
st v[176:179],v247,s[44:47],s91 offen offset:0 sc1
v_mul_f32 v104, v104, v249
mm v[36:39],v[200:203],v[144:147],0
v_mul_f32 v105, v105, v249
v_mul_f32 v106, v106, v249
v_mul_f32 v107, v107, v249
v_mul_f32 v108, v108, v249
v_mul_f32 v109, v109, v249
v_mul_f32 v110, v110, v249
mm v[36:39],v[204:207],v[148:151],v[36:39]
rt v[192:193],v240 offset:25472
rt v[194:195],v240 offset:26496
v_mul_f32 v111, v111, v249
cv v180,v104,v105
cv v181,v106,v107
cv v182,v108,v109
mm v[4:7],v[204:207],v[132:135],0
wt lgkmcnt(4)
cv v183,v110,v111
st v[180:183],v247,s[44:47],s91 offen offset:1024 sc1
v_mul_f32 v112, v112, v249
v_mul_f32 v113, v113, v249
mm v[4:7],v[200:203],v[128:131],v[4:7]
rt v[204:205],v241 offset:17024
rt v[206:207],v241 offset:18048
v_mul_f32 v114, v114, v249
v_mul_f32 v115, v115, v249
v_mul_f32 v116, v116, v249
v_mul_f32 v117, v117, v249
mm v[8:11],v[208:211],v[128:131],0
v_mul_f32 v118, v118, v249
v_mul_f32 v119, v119, v249
cv v184,v112,v113
cv v185,v114,v115
cv v186,v116,v117
cv v187,v118,v119
mm v[8:11],v[212:215],v[132:135],v[8:11]
rt v[200:201],v241 offset:25472
rt v[202:203],v241 offset:26496
st v[184:187],v247,s[44:47],s91 offen offset:2048 sc1
v_mul_f32 v120, v120, v249
v_mul_f32 v121, v121, v249
mm v[40:43],v[212:215],v[148:151],0
v_mul_f32 v122, v122, v249
v_mul_f32 v123, v123, v249
v_mul_f32 v124, v124, v249
v_mul_f32 v125, v125, v249
v_mul_f32 v126, v126, v249
v_mul_f32 v127, v127, v249
mm v[40:43],v[208:211],v[144:147],v[40:43]
rt v[212:213],v242 offset:17024
rt v[214:215],v242 offset:18048
cv v188,v120,v121
cv v189,v122,v123
)ASM" R"ASM(cv v190,v124,v125
cv v191,v126,v127
mm v[44:47],v[216:219],v[144:147],0
st v[188:191],v247,s[44:47],s91 offen offset:3072 sc1
mm v[44:47],v[220:223],v[148:151],v[44:47]
rt v[208:209],v242 offset:25472
rt v[210:211],v242 offset:26496
mm v[12:15],v[220:223],v[132:135],0
wt lgkmcnt(4)
mm v[12:15],v[216:219],v[128:131],v[12:15]
rt v[220:221],v243 offset:17024
rt v[222:223],v243 offset:18048
mm v[16:19],v[196:199],v[128:131],0
ar v224,a228
v_add_u32 v225, 0x8000, v224
v_add_u32 v226, 0x2000, v224
v_add_u32 v227, 0xa000, v224
mm v[16:19],v[192:195],v[132:135],v[16:19]
rt v[216:217],v243 offset:25472
rt v[218:219],v243 offset:26496
ar v248,a225
ar v228,a229
mm v[48:51],v[192:195],v[148:151],0
ar v229,a230
mm v[48:51],v[196:199],v[144:147],v[48:51]
rt v[192:193],v240 offset:33792
rt v[194:195],v240 offset:34816
mm v[52:55],v[204:207],v[144:147],0
mm v[52:55],v[200:203],v[148:151],v[52:55]
rt v[196:197],v240 offset:42240
rt v[198:199],v240 offset:43264
mm v[20:23],v[200:203],v[132:135],0
wt lgkmcnt(4)
mm v[20:23],v[204:207],v[128:131],v[20:23]
rt v[200:201],v241 offset:33792
rt v[202:203],v241 offset:34816
mm v[24:27],v[212:215],v[128:131],0
mm v[24:27],v[208:211],v[132:135],v[24:27]
rt v[204:205],v241 offset:42240
rt v[206:207],v241 offset:43264
mm v[56:59],v[208:211],v[148:151],0
mm v[56:59],v[212:215],v[144:147],v[56:59]
rt v[208:209],v242 offset:33792
rt v[210:211],v242 offset:34816
mm v[60:63],v[220:223],v[144:147],0
mm v[60:63],v[216:219],v[148:151],v[60:63]
rt v[212:213],v242 offset:42240
rt v[214:215],v242 offset:43264
mm v[28:31],v[216:219],v[132:135],0
wt lgkmcnt(4)
mm v[28:31],v[220:223],v[128:131],v[28:31]
rt v[216:217],v243 offset:33792
rt v[218:219],v243 offset:34816
mm v[0:3],v[192:195],v[136:139],v[0:3]
ar v249,a231
s_bitcmp1_b32 2863311530, s2
s_cbranch_scc1 .La5_LO_VC1
s_getpc_b64 s[24:25]
.La5_LW3_P1:
s_add_u32 s24, s24, .La5_LW3_R1-.La5_LW3_P1
s_addc_u32 s25, s25, 0
s_mov_b64 exec, 0
s_cmp_eq_u32 s3, 0
s_cbranch_scc1 .La5_LW3_0
s_cmp_eq_u32 s3, 1
s_cbranch_scc1 .La5_LW3_1
s_cmp_eq_u32 s3, 2
s_cbranch_scc1 .La5_LW3_2
s_branch .La5_LW3_3
.La5_LW3_R1:
s_mov_b64 exec, -1
.La5_LO_VE1:
wt vmcnt(0)
buffer_store_dwordx2 v[252:253], v249, s[64:67], s6 offen offset:0 sc1
mm v[0:3],v[196:199],v[140:143],v[0:3]
rt v[220:221],v243 offset:42240
rt v[222:223],v243 offset:43264
mm v[32:35],v[196:199],v[156:159],v[32:35]
mm v[32:35],v[192:195],v[152:155],v[32:35]
rt v[196:197],v240 offset:33920
rt v[198:199],v240 offset:34944
mm v[36:39],v[200:203],v[152:155],v[36:39]
mm v[36:39],v[204:207],v[156:159],v[36:39]
rt v[192:193],v240 offset:42368
rt v[194:195],v240 offset:43392
mm v[4:7],v[204:207],v[140:143],v[4:7]
wt lgkmcnt(4)
mm v[4:7],v[200:203],v[136:139],v[4:7]
rt v[204:205],v241 offset:33920
rt v[206:207],v241 offset:34944
mm v[8:11],v[208:211],v[136:139],v[8:11]
mm v[8:11],v[212:215],v[140:143],v[8:11]
rt v[200:201],v241 offset:42368
rt v[202:203],v241 offset:43392
mm v[40:43],v[212:215],v[156:159],v[40:43]
mm v[40:43],v[208:211],v[152:155],v[40:43]
rt v[212:213],v242 offset:33920
rt v[214:215],v242 offset:34944
mm v[44:47],v[216:219],v[152:155],v[44:47]
mm v[44:47],v[220:223],v[156:159],v[44:47]
)ASM" R"ASM(rt v[208:209],v242 offset:42368
rt v[210:211],v242 offset:43392
mm v[12:15],v[220:223],v[140:143],v[12:15]
wt lgkmcnt(4)
mm v[12:15],v[216:219],v[136:139],v[12:15]
rt v[220:221],v243 offset:33920
rt v[222:223],v243 offset:34944
mm v[16:19],v[196:199],v[136:139],v[16:19]
mm v[16:19],v[192:195],v[140:143],v[16:19]
rt v[216:217],v243 offset:42368
rt v[218:219],v243 offset:43392
mm v[48:51],v[192:195],v[156:159],v[48:51]
mm v[48:51],v[196:199],v[152:155],v[48:51]
rt v[192:193],v240 offset:16896
rt v[194:195],v240 offset:17920
mm v[52:55],v[204:207],v[152:155],v[52:55]
mm v[52:55],v[200:203],v[156:159],v[52:55]
rt v[196:197],v240 offset:25344
rt v[198:199],v240 offset:26368
mm v[20:23],v[200:203],v[140:143],v[20:23]
wt lgkmcnt(4)
mm v[20:23],v[204:207],v[136:139],v[20:23]
rt v[200:201],v241 offset:16896
rt v[202:203],v241 offset:17920
mm v[24:27],v[212:215],v[136:139],v[24:27]
mm v[24:27],v[208:211],v[140:143],v[24:27]
rt v[204:205],v241 offset:25344
rt v[206:207],v241 offset:26368
mm v[56:59],v[208:211],v[156:159],v[56:59]
mm v[56:59],v[212:215],v[152:155],v[56:59]
rt v[208:209],v242 offset:16896
rt v[210:211],v242 offset:17920
mm v[60:63],v[220:223],v[152:155],v[60:63]
mm v[60:63],v[216:219],v[156:159],v[60:63]
rt v[212:213],v242 offset:25344
rt v[214:215],v242 offset:26368
mm v[28:31],v[216:219],v[140:143],v[28:31]
wt lgkmcnt(4)
mm v[28:31],v[220:223],v[136:139],v[28:31]
rt v[216:217],v243 offset:16896
rt v[218:219],v243 offset:17920
s_cmp_lg_u32 s30, 0
s_nop 0
s_nop 0
s_cbranch_scc1 .La5_LO_W
; ---- O projection: 256 x 128 tile over eight heads
.La5_LO_START:
.La5_LW6_7:
s_cbranch_execz .La5_LW6_R0
s_bitcmp1_b32 2863311530, s2
s_cbranch_scc1 .La5_LO_SC
; ---- O projection, even XCDs: eight and a half heads
.La5_LO_SB:
mm v[64:67],v[192:195],v[160:163],0
buffer_load_dwordx2 v[250:251], v248, s[64:67], s5 offen sc1
mm v[64:67],v[196:199],v[164:167],v[64:67]
rt v[220:221],v243 offset:25344
rt v[222:223],v243 offset:26368
mm v[96:99],v[196:199],v[180:183],0
s_add_u32 m0, s83, 0
s_bfe_u32 s80, s4, 262148
mm v[96:99],v[192:195],v[176:179],v[96:99]
rt v[196:197],v240 offset:17024
rt v[198:199],v240 offset:18048
s_xor_b32 s80, s80, s72
s_lshl_b32 s80, s80, 18
s_add_u32 s80, s80, 131072
mm v[100:103],v[200:203],v[176:179],0
ld v224,s[56:59],s80 offen lds
mm v[100:103],v[204:207],v[180:183],v[100:103]
rt v[192:193],v240 offset:25472
rt v[194:195],v240 offset:26496
mm v[68:71],v[204:207],v[164:167],0
s_add_u32 m0, s83, 1024
wt lgkmcnt(4)
mm v[68:71],v[200:203],v[160:163],v[68:71]
rt v[204:205],v241 offset:17024
rt v[206:207],v241 offset:18048
mm v[72:75],v[208:211],v[160:163],0
ld v225,s[56:59],s80 offen lds
mm v[72:75],v[212:215],v[164:167],v[72:75]
rt v[200:201],v241 offset:25472
rt v[202:203],v241 offset:26496
mm v[104:107],v[212:215],v[180:183],0
s_add_u32 m0, s83, 2176
s_bfe_u32 s53, s4, 262148
mm v[104:107],v[208:211],v[176:179],v[104:107]
rt v[212:213],v242 offset:17024
rt v[214:215],v242 offset:18048
mm v[108:111],v[216:219],v[176:179],0
ld v226,s[56:59],s80 offen lds
mm v[108:111],v[220:223],v[180:183],v[108:111]
rt v[208:209],v242 offset:25472
rt v[210:211],v242 offset:26496
mm v[76:79],v[220:223],v[164:167],0
s_add_u32 m0, s83, 3200
wt lgkmcnt(4)
mm v[76:79],v[216:219],v[160:163],v[76:79]
rt v[220:221],v243 offset:17024
rt v[222:223],v243 offset:18048
)ASM" R"ASM(mm v[80:83],v[196:199],v[160:163],0
ld v227,s[56:59],s80 offen lds
mm v[80:83],v[192:195],v[164:167],v[80:83]
rt v[216:217],v243 offset:25472
rt v[218:219],v243 offset:26496
mm v[112:115],v[192:195],v[180:183],0
s_add_u32 m0, s83, 118272
s_bfe_u32 s80, s4, 262152
mm v[112:115],v[196:199],v[176:179],v[112:115]
rt v[192:193],v240 offset:33792
rt v[194:195],v240 offset:34816
s_xor_b32 s80, s80, s72
s_lshl_b32 s80, s80, 18
mm v[116:119],v[204:207],v[176:179],0
ld v224,s[56:59],s80 offen lds
mm v[116:119],v[200:203],v[180:183],v[116:119]
rt v[196:197],v240 offset:42240
rt v[198:199],v240 offset:43264
mm v[84:87],v[200:203],v[164:167],0
s_add_u32 m0, s83, 119296
wt lgkmcnt(4)
mm v[84:87],v[204:207],v[160:163],v[84:87]
rt v[200:201],v241 offset:33792
rt v[202:203],v241 offset:34816
mm v[88:91],v[212:215],v[160:163],0
ld v225,s[56:59],s80 offen lds
mm v[88:91],v[208:211],v[164:167],v[88:91]
rt v[204:205],v241 offset:42240
rt v[206:207],v241 offset:43264
mm v[120:123],v[208:211],v[180:183],0
s_add_u32 m0, s83, 120448
s_xor_b32 s53, s53, s72
mm v[120:123],v[212:215],v[176:179],v[120:123]
rt v[208:209],v242 offset:33792
rt v[210:211],v242 offset:34816
mm v[124:127],v[220:223],v[176:179],0
ld v226,s[56:59],s80 offen lds
mm v[124:127],v[216:219],v[180:183],v[124:127]
rt v[212:213],v242 offset:42240
rt v[214:215],v242 offset:43264
mm v[92:95],v[216:219],v[164:167],0
wt vmcnt(7)
v_cmp_eq_u64 vcc, v[250:251], v[252:253]
s_nop 0
s_bitcmp1_b32 vcc_lo, 1
s_cbranch_scc0 .La5_LO_NB1
.La5_LO_KB1:
wt lgkmcnt(4)
mm v[92:95],v[220:223],v[160:163],v[92:95]
rt v[216:217],v243 offset:33792
rt v[218:219],v243 offset:34816
s_lshl_b32 s53, s53, 13
mm v[64:67],v[192:195],v[168:171],v[64:67]
ld a[0:3],v244,s[44:47],s53 offen offset:0 sc1
mm v[64:67],v[196:199],v[172:175],v[64:67]
rt v[220:221],v243 offset:42240
rt v[222:223],v243 offset:43264
mm v[96:99],v[196:199],v[188:191],v[96:99]
ld a[4:7],v245,s[44:47],s53 offen offset:0 sc1
mm v[96:99],v[192:195],v[184:187],v[96:99]
rt v[196:197],v240 offset:33920
rt v[198:199],v240 offset:34944
mm v[100:103],v[200:203],v[184:187],v[100:103]
ld a[8:11],v246,s[44:47],s53 offen offset:0 sc1
mm v[100:103],v[204:207],v[188:191],v[100:103]
rt v[192:193],v240 offset:42368
rt v[194:195],v240 offset:43392
mm v[68:71],v[204:207],v[172:175],v[68:71]
wt lgkmcnt(4)
s_bfe_u32 s54, s4, 262152
mm v[68:71],v[200:203],v[168:171],v[68:71]
rt v[204:205],v241 offset:33920
rt v[206:207],v241 offset:34944
mm v[72:75],v[208:211],v[168:171],v[72:75]
ld a[12:15],v247,s[44:47],s53 offen offset:0 sc1
mm v[72:75],v[212:215],v[172:175],v[72:75]
rt v[200:201],v241 offset:42368
rt v[202:203],v241 offset:43392
mm v[104:107],v[212:215],v[188:191],v[104:107]
ld a[16:19],v244,s[44:47],s53 offen offset:1024 sc1
mm v[104:107],v[208:211],v[184:187],v[104:107]
rt v[212:213],v242 offset:33920
rt v[214:215],v242 offset:34944
mm v[108:111],v[216:219],v[184:187],v[108:111]
ld a[20:23],v245,s[44:47],s53 offen offset:1024 sc1
mm v[108:111],v[220:223],v[188:191],v[108:111]
rt v[208:209],v242 offset:42368
rt v[210:211],v242 offset:43392
mm v[76:79],v[220:223],v[172:175],v[76:79]
s_nop 0
s_barrier
wt lgkmcnt(4)
mm v[76:79],v[216:219],v[168:171],v[76:79]
rt v[220:221],v243 offset:33920
rt v[222:223],v243 offset:34944
mm v[80:83],v[196:199],v[168:171],v[80:83]
ld a[24:27],v246,s[44:47],s53 offen offset:1024 sc1
mm v[80:83],v[192:195],v[172:175],v[80:83]
rt v[216:217],v243 offset:42368
rt v[218:219],v243 offset:43392
)ASM" R"ASM(mm v[112:115],v[192:195],v[188:191],v[112:115]
ld a[28:31],v247,s[44:47],s53 offen offset:1024 sc1
mm v[112:115],v[196:199],v[184:187],v[112:115]
rt v[192:193],v240 offset:50688
rt v[194:195],v240 offset:51712
mm v[116:119],v[204:207],v[184:187],v[116:119]
ld a[32:35],v244,s[44:47],s53 offen offset:2048 sc1
mm v[116:119],v[200:203],v[188:191],v[116:119]
rt v[196:197],v240 offset:59136
rt v[198:199],v240 offset:60160
mm v[84:87],v[200:203],v[172:175],v[84:87]
wt lgkmcnt(4)
s_xor_b32 s54, s54, s72
mm v[84:87],v[204:207],v[168:171],v[84:87]
rt v[200:201],v241 offset:50688
rt v[202:203],v241 offset:51712
mm v[88:91],v[212:215],v[168:171],v[88:91]
ld a[36:39],v245,s[44:47],s53 offen offset:2048 sc1
mm v[88:91],v[208:211],v[172:175],v[88:91]
rt v[204:205],v241 offset:59136
rt v[206:207],v241 offset:60160
mm v[120:123],v[208:211],v[188:191],v[120:123]
ld a[40:43],v246,s[44:47],s53 offen offset:2048 sc1
mm v[120:123],v[212:215],v[184:187],v[120:123]
rt v[208:209],v242 offset:50688
rt v[210:211],v242 offset:51712
mm v[124:127],v[220:223],v[184:187],v[124:127]
.La5_LW3_0:
s_cbranch_execz .La5_LW3_4
ld a[44:47],v247,s[44:47],s53 offen offset:2048 sc1
mm v[124:127],v[216:219],v[188:191],v[124:127]
rt v[212:213],v242 offset:59136
rt v[214:215],v242 offset:60160
mm v[92:95],v[216:219],v[172:175],v[92:95]
wt vmcnt(4) lgkmcnt(4)
s_lshl_b32 s54, s54, 13
mm v[92:95],v[220:223],v[168:171],v[92:95]
rt v[216:217],v243 offset:50688
rt v[218:219],v243 offset:51712
mm v[0:3],v[192:195],a[0:3],v[0:3]
s_bfe_u32 s55, s4, 262156
s_xor_b32 s55, s55, s72
mm v[0:3],v[196:199],a[16:19],v[0:3]
ld a[48:51],v244,s[44:47],s53 offen offset:3072 sc1
mm v[32:35],v[196:199],a[20:23],v[32:35]
s_lshl_b32 s55, s55, 13
s_bfe_u32 s52, s4, 262160
mm v[32:35],v[192:195],a[4:7],v[32:35]
rt v[220:221],v243 offset:59136
rt v[222:223],v243 offset:60160
mm v[64:67],v[192:195],a[8:11],v[64:67]
s_xor_b32 s52, s52, s72
s_lshl_b32 s52, s52, 13
mm v[64:67],v[196:199],a[24:27],v[64:67]
ld a[52:55],v245,s[44:47],s53 offen offset:3072 sc1
mm v[96:99],v[196:199],a[28:31],v[96:99]
mm v[96:99],v[192:195],a[12:15],v[96:99]
rt v[196:197],v240 offset:50816
rt v[198:199],v240 offset:51840
mm v[100:103],v[200:203],a[12:15],v[100:103]
mm v[100:103],v[204:207],a[28:31],v[100:103]
buffer_load_dwordx2 v[250:251], v248, s[64:67], s5 offen sc1
mm v[68:71],v[204:207],a[24:27],v[68:71]
mm v[68:71],v[200:203],a[8:11],v[68:71]
rt v[192:193],v240 offset:59264
rt v[194:195],v240 offset:60288
mm v[36:39],v[200:203],a[4:7],v[36:39]
mm v[36:39],v[204:207],a[20:23],v[36:39]
ld a[56:59],v246,s[44:47],s53 offen offset:3072 sc1
mm v[4:7],v[204:207],a[16:19],v[4:7]
wt lgkmcnt(4)
mm v[4:7],v[200:203],a[0:3],v[4:7]
rt v[204:205],v241 offset:50816
rt v[206:207],v241 offset:51840
mm v[8:11],v[208:211],a[0:3],v[8:11]
mm v[8:11],v[212:215],a[16:19],v[8:11]
ld a[60:63],v247,s[44:47],s53 offen offset:3072 sc1
mm v[40:43],v[212:215],a[20:23],v[40:43]
s_bfe_u32 s53, s4, 262164
s_xor_b32 s53, s53, s72
mm v[40:43],v[208:211],a[4:7],v[40:43]
rt v[200:201],v241 offset:59264
rt v[202:203],v241 offset:60288
mm v[72:75],v[208:211],a[8:11],v[72:75]
s_add_u32 m0, s83, 121472
s_lshl_b32 s53, s53, 13
mm v[72:75],v[212:215],a[24:27],v[72:75]
ld v227,s[56:59],s80 offen lds
mm v[104:107],v[212:215],a[28:31],v[104:107]
s_add_u32 s80, s80, 131072
mm v[104:107],v[208:211],a[12:15],v[104:107]
rt v[212:213],v242 offset:50816
rt v[214:215],v242 offset:51840
mm v[108:111],v[216:219],a[12:15],v[108:111]
s_add_u32 m0, s83, 67584
mm v[108:111],v[220:223],a[28:31],v[108:111]
ld v224,s[56:59],s80 offen lds
mm v[76:79],v[220:223],a[24:27],v[76:79]
mm v[76:79],v[216:219],a[8:11],v[76:79]
rt v[208:209],v242 offset:59264
)ASM" R"ASM(rt v[210:211],v242 offset:60288
mm v[44:47],v[216:219],a[4:7],v[44:47]
s_add_u32 m0, s83, 68608
mm v[44:47],v[220:223],a[20:23],v[44:47]
ld v225,s[56:59],s80 offen lds
mm v[12:15],v[220:223],a[16:19],v[12:15]
wt lgkmcnt(4)
mm v[12:15],v[216:219],a[0:3],v[12:15]
rt v[220:221],v243 offset:50816
rt v[222:223],v243 offset:51840
mm v[16:19],v[196:199],a[0:3],v[16:19]
s_add_u32 m0, s83, 69760
mm v[16:19],v[192:195],a[16:19],v[16:19]
ld v226,s[56:59],s80 offen lds
mm v[48:51],v[192:195],a[20:23],v[48:51]
mm v[48:51],v[196:199],a[4:7],v[48:51]
rt v[216:217],v243 offset:59264
rt v[218:219],v243 offset:60288
mm v[80:83],v[196:199],a[8:11],v[80:83]
s_add_u32 m0, s83, 70784
s_nop 0
s_barrier
mm v[80:83],v[192:195],a[24:27],v[80:83]
ld v227,s[56:59],s80 offen lds
mm v[112:115],v[192:195],a[28:31],v[112:115]
s_bfe_u32 s80, s4, 262156
s_xor_b32 s80, s80, s72
mm v[112:115],v[196:199],a[12:15],v[112:115]
rt v[192:193],v240 offset:0
rt v[194:195],v240 offset:1024
mm v[116:119],v[204:207],a[12:15],v[116:119]
s_add_u32 m0, s83, 84480
s_lshl_b32 s80, s80, 18
mm v[116:119],v[200:203],a[28:31],v[116:119]
ld v224,s[56:59],s80 offen lds
mm v[84:87],v[200:203],a[24:27],v[84:87]
mm v[84:87],v[204:207],a[8:11],v[84:87]
rt v[196:197],v240 offset:8448
rt v[198:199],v240 offset:9472
mm v[52:55],v[204:207],a[4:7],v[52:55]
s_add_u32 m0, s83, 85504
mm v[52:55],v[200:203],a[20:23],v[52:55]
ld v225,s[56:59],s80 offen lds
mm v[20:23],v[200:203],a[16:19],v[20:23]
wt lgkmcnt(4)
mm v[20:23],v[204:207],a[0:3],v[20:23]
rt v[200:201],v241 offset:0
rt v[202:203],v241 offset:1024
mm v[24:27],v[212:215],a[0:3],v[24:27]
s_add_u32 m0, s83, 86656
mm v[24:27],v[208:211],a[16:19],v[24:27]
ld v226,s[56:59],s80 offen lds
mm v[56:59],v[208:211],a[20:23],v[56:59]
wt vmcnt(10)
v_cmp_eq_u64 vcc, v[250:251], v[252:253]
mm v[56:59],v[212:215],a[4:7],v[56:59]
rt v[204:205],v241 offset:8448
rt v[206:207],v241 offset:9472
mm v[88:91],v[212:215],a[8:11],v[88:91]
s_add_u32 m0, s83, 87680
mm v[88:91],v[208:211],a[24:27],v[88:91]
ld v227,s[56:59],s80 offen lds
mm v[120:123],v[208:211],a[28:31],v[120:123]
s_bitcmp1_b32 vcc_lo, 2
s_nop 0
s_nop 0
s_cbranch_scc0 .La5_LO_NB2
.La5_LO_KB2:
mm v[120:123],v[212:215],a[12:15],v[120:123]
rt v[208:209],v242 offset:0
rt v[210:211],v242 offset:1024
mm v[124:127],v[220:223],a[12:15],v[124:127]
s_add_u32 s80, s80, 131072
mm v[124:127],v[216:219],a[28:31],v[124:127]
ld a[12:15],v244,s[44:47],s54 offen offset:0 sc1
mm v[92:95],v[216:219],a[24:27],v[92:95]
mm v[92:95],v[220:223],a[8:11],v[92:95]
rt v[212:213],v242 offset:8448
rt v[214:215],v242 offset:9472
mm v[60:63],v[220:223],a[4:7],v[60:63]
mm v[60:63],v[216:219],a[20:23],v[60:63]
mm v[28:31],v[216:219],a[16:19],v[28:31]
wt vmcnt(10) lgkmcnt(4)
mm v[28:31],v[220:223],a[0:3],v[28:31]
rt v[216:217],v243 offset:0
rt v[218:219],v243 offset:1024
mm v[0:3],v[192:195],a[32:35],v[0:3]
mm v[0:3],v[196:199],a[48:51],v[0:3]
ld a[0:3],v245,s[44:47],s54 offen offset:0 sc1
mm v[32:35],v[196:199],a[52:55],v[32:35]
mm v[32:35],v[192:195],a[36:39],v[32:35]
rt v[220:221],v243 offset:8448
rt v[222:223],v243 offset:9472
mm v[64:67],v[192:195],a[40:43],v[64:67]
mm v[64:67],v[196:199],a[56:59],v[64:67]
ld a[4:7],v246,s[44:47],s54 offen offset:0 sc1
mm v[96:99],v[196:199],a[60:63],v[96:99]
mm v[96:99],v[192:195],a[44:47],v[96:99]
rt v[196:197],v240 offset:128
rt v[198:199],v240 offset:1152
)ASM" R"ASM(mm v[100:103],v[200:203],a[44:47],v[100:103]
s_mov_b32 s86, 0x05040100
s_mov_b32 s87, 0x07060302
mm v[100:103],v[204:207],a[60:63],v[100:103]
ld a[8:11],v247,s[44:47],s54 offen offset:0 sc1
mm v[68:71],v[204:207],a[56:59],v[68:71]
s_nop 0
ar v230,a128
mm v[68:71],v[200:203],a[40:43],v[68:71]
rt v[192:193],v240 offset:8576
rt v[194:195],v240 offset:9600
mm v[36:39],v[200:203],a[36:39],v[36:39]
v_add_u32 v240, 67584, v240
ar v231,a132
mm v[36:39],v[204:207],a[52:55],v[36:39]
ld a[16:19],v244,s[44:47],s54 offen offset:1024 sc1
mm v[4:7],v[204:207],a[48:51],v[4:7]
wt lgkmcnt(4)
v_perm_b32 v232, v231, v230, s86
mm v[4:7],v[200:203],a[32:35],v[4:7]
rt v[204:205],v241 offset:128
rt v[206:207],v241 offset:1152
mm v[8:11],v[208:211],a[32:35],v[8:11]
v_perm_b32 v233, v231, v230, s87
aw a96,v232
mm v[8:11],v[212:215],a[48:51],v[8:11]
ld a[20:23],v245,s[44:47],s54 offen offset:1024 sc1
mm v[40:43],v[212:215],a[52:55],v[40:43]
aw a100,v233
ar v234,a136
mm v[40:43],v[208:211],a[36:39],v[40:43]
rt v[200:201],v241 offset:8576
rt v[202:203],v241 offset:9600
mm v[72:75],v[208:211],a[40:43],v[72:75]
v_add_u32 v241, 67584, v241
ar v235,a140
mm v[72:75],v[212:215],a[56:59],v[72:75]
ld a[24:27],v246,s[44:47],s54 offen offset:1024 sc1
mm v[104:107],v[212:215],a[60:63],v[104:107]
v_perm_b32 v236, v235, v234, s86
v_perm_b32 v237, v235, v234, s87
mm v[104:107],v[208:211],a[44:47],v[104:107]
rt v[212:213],v242 offset:128
rt v[214:215],v242 offset:1152
mm v[108:111],v[216:219],a[44:47],v[108:111]
aw a97,v236
aw a101,v237
mm v[108:111],v[220:223],a[60:63],v[108:111]
ld a[28:31],v247,s[44:47],s54 offen offset:1024 sc1
mm v[76:79],v[220:223],a[56:59],v[76:79]
ar v230,a144
ar v231,a148
mm v[76:79],v[216:219],a[40:43],v[76:79]
rt v[208:209],v242 offset:8576
rt v[210:211],v242 offset:9600
mm v[44:47],v[216:219],a[36:39],v[44:47]
v_add_u32 v242, 67584, v242
v_perm_b32 v232, v231, v230, s86
mm v[44:47],v[220:223],a[52:55],v[44:47]
v_perm_b32 v233, v231, v230, s87
aw a98,v232
mm v[12:15],v[220:223],a[48:51],v[12:15]
wt lgkmcnt(4)
aw a102,v233
mm v[12:15],v[216:219],a[32:35],v[12:15]
rt v[220:221],v243 offset:128
rt v[222:223],v243 offset:1152
mm v[16:19],v[196:199],a[32:35],v[16:19]
s_add_u32 m0, s83, 101376
ar v234,a152
mm v[16:19],v[192:195],a[48:51],v[16:19]
ld v224,s[56:59],s80 offen lds
mm v[48:51],v[192:195],a[52:55],v[48:51]
ar v235,a156
v_perm_b32 v236, v235, v234, s86
mm v[48:51],v[196:199],a[36:39],v[48:51]
rt v[216:217],v243 offset:8576
rt v[218:219],v243 offset:9600
mm v[80:83],v[196:199],a[40:43],v[80:83]
s_add_u32 m0, s83, 102400
v_add_u32 v243, 67584, v243
mm v[80:83],v[192:195],a[56:59],v[80:83]
ld v225,s[56:59],s80 offen lds
mm v[112:115],v[192:195],a[60:63],v[112:115]
wt vmcnt(14)
s_barrier
mm v[112:115],v[196:199],a[44:47],v[112:115]
rt v[192:193],v240 offset:50688
rt v[194:195],v240 offset:51712
mm v[116:119],v[204:207],a[44:47],v[116:119]
s_add_u32 m0, s83, 103552
v_perm_b32 v237, v235, v234, s87
mm v[116:119],v[200:203],a[60:63],v[116:119]
ld v226,s[56:59],s80 offen lds
mm v[84:87],v[200:203],a[56:59],v[84:87]
aw a99,v236
aw a103,v237
mm v[84:87],v[204:207],a[40:43],v[84:87]
rt v[196:197],v240 offset:59136
rt v[198:199],v240 offset:60160
)ASM" R"ASM(mm v[52:55],v[204:207],a[36:39],v[52:55]
ar v230,a129
ar v231,a133
mm v[52:55],v[200:203],a[52:55],v[52:55]
ld a[64:67],v244,s[44:47],s54 offen offset:2048 sc1
mm v[20:23],v[200:203],a[48:51],v[20:23]
wt lgkmcnt(4)
v_perm_b32 v232, v231, v230, s86
mm v[20:23],v[204:207],a[32:35],v[20:23]
rt v[200:201],v241 offset:50688
rt v[202:203],v241 offset:51712
mm v[24:27],v[212:215],a[32:35],v[24:27]
v_perm_b32 v233, v231, v230, s87
aw a104,v232
mm v[24:27],v[208:211],a[48:51],v[24:27]
ld a[68:71],v245,s[44:47],s54 offen offset:2048 sc1
mm v[56:59],v[208:211],a[52:55],v[56:59]
aw a108,v233
ar v234,a137
mm v[56:59],v[212:215],a[36:39],v[56:59]
rt v[204:205],v241 offset:59136
rt v[206:207],v241 offset:60160
mm v[88:91],v[212:215],a[40:43],v[88:91]
ar v235,a141
v_perm_b32 v236, v235, v234, s86
mm v[88:91],v[208:211],a[56:59],v[88:91]
ld a[72:75],v246,s[44:47],s54 offen offset:2048 sc1
mm v[120:123],v[208:211],a[60:63],v[120:123]
v_perm_b32 v237, v235, v234, s87
aw a105,v236
mm v[120:123],v[212:215],a[44:47],v[120:123]
rt v[208:209],v242 offset:50688
rt v[210:211],v242 offset:51712
mm v[124:127],v[220:223],a[44:47],v[124:127]
aw a109,v237
ar v230,a145
mm v[124:127],v[216:219],a[60:63],v[124:127]
buffer_load_dwordx2 v[250:251], v248, s[64:67], s5 offen sc1
mm v[92:95],v[216:219],a[56:59],v[92:95]
ar v231,a149
v_perm_b32 v232, v231, v230, s86
mm v[92:95],v[220:223],a[40:43],v[92:95]
rt v[212:213],v242 offset:59136
rt v[214:215],v242 offset:60160
mm v[60:63],v[220:223],a[36:39],v[60:63]
v_perm_b32 v233, v231, v230, s87
aw a106,v232
mm v[60:63],v[216:219],a[52:55],v[60:63]
ld a[36:39],v247,s[44:47],s54 offen offset:2048 sc1
mm v[28:31],v[216:219],a[48:51],v[28:31]
wt vmcnt(8) lgkmcnt(4)
aw a110,v233
mm v[28:31],v[220:223],a[32:35],v[28:31]
rt v[216:217],v243 offset:50688
rt v[218:219],v243 offset:51712
mm v[0:3],v[192:195],a[12:15],v[0:3]
ar v234,a153
ar v235,a157
mm v[0:3],v[196:199],a[16:19],v[0:3]
ld a[32:35],v244,s[44:47],s54 offen offset:3072 sc1
mm v[32:35],v[196:199],a[20:23],v[32:35]
v_perm_b32 v236, v235, v234, s86
v_perm_b32 v237, v235, v234, s87
mm v[32:35],v[192:195],a[0:3],v[32:35]
rt v[220:221],v243 offset:59136
rt v[222:223],v243 offset:60160
mm v[64:67],v[192:195],a[4:7],v[64:67]
aw a107,v236
aw a111,v237
mm v[64:67],v[196:199],a[24:27],v[64:67]
ld a[40:43],v245,s[44:47],s54 offen offset:3072 sc1
mm v[96:99],v[196:199],a[28:31],v[96:99]
ar v230,a130
ar v231,a134
mm v[96:99],v[192:195],a[8:11],v[96:99]
rt v[196:197],v240 offset:50816
rt v[198:199],v240 offset:51840
mm v[100:103],v[200:203],a[8:11],v[100:103]
v_perm_b32 v232, v231, v230, s86
v_perm_b32 v233, v231, v230, s87
mm v[100:103],v[204:207],a[28:31],v[100:103]
ld a[44:47],v246,s[44:47],s54 offen offset:3072 sc1
mm v[68:71],v[204:207],a[24:27],v[68:71]
aw a112,v232
aw a116,v233
mm v[68:71],v[200:203],a[4:7],v[68:71]
rt v[192:193],v240 offset:59264
rt v[194:195],v240 offset:60288
mm v[36:39],v[200:203],a[0:3],v[36:39]
ar v234,a138
ar v235,a142
mm v[36:39],v[204:207],a[20:23],v[36:39]
ld a[48:51],v247,s[44:47],s54 offen offset:3072 sc1
mm v[4:7],v[204:207],a[16:19],v[4:7]
wt lgkmcnt(4)
s_bfe_u32 s54, s4, 262164
mm v[4:7],v[200:203],a[12:15],v[4:7]
rt v[204:205],v241 offset:50816
rt v[206:207],v241 offset:51840
mm v[8:11],v[208:211],a[12:15],v[8:11]
)ASM" R"ASM(s_add_u32 m0, s83, 104576
s_xor_b32 s54, s54, s72
mm v[8:11],v[212:215],a[16:19],v[8:11]
ld v227,s[56:59],s80 offen lds
mm v[40:43],v[212:215],a[20:23],v[40:43]
s_bitcmp1_b32 vcc_lo, 3
s_cbranch_scc0 .La5_LO_NB3
.La5_LO_KB3:
mm v[40:43],v[208:211],a[0:3],v[40:43]
rt v[200:201],v241 offset:59264
rt v[202:203],v241 offset:60288
mm v[72:75],v[208:211],a[4:7],v[72:75]
s_bfe_u32 s80, s4, 262160
s_xor_b32 s80, s80, s72
mm v[72:75],v[212:215],a[24:27],v[72:75]
ld a[52:55],v244,s[44:47],s55 offen offset:0 sc1
mm v[104:107],v[212:215],a[28:31],v[104:107]
s_lshl_b32 s80, s80, 18
s_lshl_b32 s54, s54, 13
mm v[104:107],v[208:211],a[8:11],v[104:107]
rt v[212:213],v242 offset:50816
rt v[214:215],v242 offset:51840
mm v[108:111],v[216:219],a[8:11],v[108:111]
s_xor_b32 s54, s54, 229376
v_perm_b32 v236, v235, v234, s86
mm v[108:111],v[220:223],a[28:31],v[108:111]
ld a[56:59],v245,s[44:47],s55 offen offset:0 sc1
mm v[76:79],v[220:223],a[24:27],v[76:79]
v_perm_b32 v237, v235, v234, s87
aw a113,v236
mm v[76:79],v[216:219],a[4:7],v[76:79]
rt v[208:209],v242 offset:59264
rt v[210:211],v242 offset:60288
mm v[44:47],v[216:219],a[0:3],v[44:47]
aw a117,v237
ar v230,a146
mm v[44:47],v[220:223],a[20:23],v[44:47]
ar v231,a150
v_perm_b32 v232, v231, v230, s86
mm v[12:15],v[220:223],a[16:19],v[12:15]
wt lgkmcnt(4)
v_perm_b32 v233, v231, v230, s87
mm v[12:15],v[216:219],a[12:15],v[12:15]
rt v[220:221],v243 offset:50816
rt v[222:223],v243 offset:51840
mm v[16:19],v[196:199],a[12:15],v[16:19]
aw a114,v232
aw a118,v233
mm v[16:19],v[192:195],a[16:19],v[16:19]
ld a[60:63],v246,s[44:47],s55 offen offset:0 sc1
mm v[48:51],v[192:195],a[20:23],v[48:51]
ar v234,a154
ar v235,a158
mm v[48:51],v[196:199],a[0:3],v[48:51]
rt v[216:217],v243 offset:59264
rt v[218:219],v243 offset:60288
mm v[80:83],v[196:199],a[4:7],v[80:83]
v_perm_b32 v236, v235, v234, s86
v_perm_b32 v237, v235, v234, s87
mm v[80:83],v[192:195],a[24:27],v[80:83]
ld a[76:79],v247,s[44:47],s55 offen offset:0 sc1
mm v[112:115],v[192:195],a[28:31],v[112:115]
aw a115,v236
aw a119,v237
mm v[112:115],v[196:199],a[8:11],v[112:115]
rt v[192:193],v240 offset:0
rt v[194:195],v240 offset:1024
mm v[116:119],v[204:207],a[8:11],v[116:119]
ar v230,a131
ar v231,a135
mm v[116:119],v[200:203],a[28:31],v[116:119]
ld a[80:83],v244,s[44:47],s55 offen offset:1024 sc1
mm v[84:87],v[200:203],a[24:27],v[84:87]
v_perm_b32 v232, v231, v230, s86
v_perm_b32 v233, v231, v230, s87
mm v[84:87],v[204:207],a[4:7],v[84:87]
rt v[196:197],v240 offset:8448
rt v[198:199],v240 offset:9472
mm v[52:55],v[204:207],a[0:3],v[52:55]
s_load_dwordx2 s[16:17], s[0:1], 0
s_load_dwordx4 s[36:39], s[0:1], 80
mm v[52:55],v[200:203],a[20:23],v[52:55]
ld a[84:87],v245,s[44:47],s55 offen offset:1024 sc1
mm v[20:23],v[200:203],a[16:19],v[20:23]
wt lgkmcnt(4)
aw a120,v232
mm v[20:23],v[204:207],a[12:15],v[20:23]
rt v[200:201],v241 offset:0
rt v[202:203],v241 offset:1024
mm v[24:27],v[212:215],a[12:15],v[24:27]
aw a124,v233
ar v234,a139
mm v[24:27],v[208:211],a[16:19],v[24:27]
ld a[88:91],v246,s[44:47],s55 offen offset:1024 sc1
mm v[56:59],v[208:211],a[20:23],v[56:59]
ar v235,a143
v_perm_b32 v236, v235, v234, s86
mm v[56:59],v[212:215],a[0:3],v[56:59]
rt v[204:205],v241 offset:8448
rt v[206:207],v241 offset:9472
)ASM" R"ASM(mm v[88:91],v[212:215],a[4:7],v[88:91]
v_perm_b32 v237, v235, v234, s87
aw a121,v236
mm v[88:91],v[208:211],a[24:27],v[88:91]
ld a[92:95],v247,s[44:47],s55 offen offset:1024 sc1
mm v[120:123],v[208:211],a[28:31],v[120:123]
aw a125,v237
ar v230,a147
mm v[120:123],v[212:215],a[8:11],v[120:123]
rt v[208:209],v242 offset:0
rt v[210:211],v242 offset:1024
mm v[124:127],v[220:223],a[8:11],v[124:127]
s_add_u32 m0, s83, 16896
ar v231,a151
mm v[124:127],v[216:219],a[28:31],v[124:127]
ld v224,s[56:59],s80 offen lds
mm v[92:95],v[216:219],a[24:27],v[92:95]
v_perm_b32 v232, v231, v230, s86
v_perm_b32 v233, v231, v230, s87
mm v[92:95],v[220:223],a[4:7],v[92:95]
rt v[212:213],v242 offset:8448
rt v[214:215],v242 offset:9472
mm v[60:63],v[220:223],a[0:3],v[60:63]
aw a122,v232
aw a126,v233
mm v[60:63],v[216:219],a[20:23],v[60:63]
ar v234,a155
ar v235,a159
mm v[28:31],v[216:219],a[16:19],v[28:31]
wt vmcnt(10) lgkmcnt(4)
v_perm_b32 v236, v235, v234, s86
mm v[28:31],v[220:223],a[12:15],v[28:31]
rt v[216:217],v243 offset:0
rt v[218:219],v243 offset:1024
mm v[0:3],v[192:195],a[64:67],v[0:3]
s_add_u32 m0, s83, 17920
v_perm_b32 v237, v235, v234, s87
mm v[0:3],v[196:199],a[32:35],v[0:3]
ld v225,s[56:59],s80 offen lds
mm v[32:35],v[196:199],a[40:43],v[32:35]
aw a123,v236
aw a127,v237
mm v[32:35],v[192:195],a[68:71],v[32:35]
rt v[220:221],v243 offset:8448
rt v[222:223],v243 offset:9472
mm v[64:67],v[192:195],a[72:75],v[64:67]
s_add_u32 m0, s83, 19072
ar v230,a160
mm v[64:67],v[196:199],a[44:47],v[64:67]
ld v226,s[56:59],s80 offen lds
mm v[96:99],v[196:199],a[48:51],v[96:99]
ar v231,a164
v_perm_b32 v232, v231, v230, s86
mm v[96:99],v[192:195],a[36:39],v[96:99]
rt v[196:197],v240 offset:128
rt v[198:199],v240 offset:1152
mm v[100:103],v[200:203],a[36:39],v[100:103]
v_perm_b32 v233, v231, v230, s87
aw a128,v232
mm v[100:103],v[204:207],a[48:51],v[100:103]
ld a[0:3],v244,s[44:47],s55 offen offset:2048 sc1
mm v[68:71],v[204:207],a[44:47],v[68:71]
.La5_LW3_1:
s_cbranch_execz .La5_LW3_5
aw a132,v233
ar v234,a168
mm v[68:71],v[200:203],a[72:75],v[68:71]
rt v[192:193],v240 offset:8576
rt v[194:195],v240 offset:9600
mm v[36:39],v[200:203],a[68:71],v[36:39]
ar v235,a172
v_perm_b32 v236, v235, v234, s86
mm v[36:39],v[204:207],a[40:43],v[36:39]
ld a[4:7],v245,s[44:47],s55 offen offset:2048 sc1
mm v[4:7],v[204:207],a[32:35],v[4:7]
wt lgkmcnt(4)
v_perm_b32 v237, v235, v234, s87
mm v[4:7],v[200:203],a[64:67],v[4:7]
rt v[204:205],v241 offset:128
rt v[206:207],v241 offset:1152
mm v[8:11],v[208:211],a[64:67],v[8:11]
aw a129,v236
aw a133,v237
mm v[8:11],v[212:215],a[32:35],v[8:11]
ld a[8:11],v246,s[44:47],s55 offen offset:2048 sc1
mm v[40:43],v[212:215],a[40:43],v[40:43]
ar v230,a176
ar v231,a180
mm v[40:43],v[208:211],a[68:71],v[40:43]
rt v[200:201],v241 offset:8576
rt v[202:203],v241 offset:9600
mm v[72:75],v[208:211],a[72:75],v[72:75]
v_perm_b32 v232, v231, v230, s86
v_perm_b32 v233, v231, v230, s87
mm v[72:75],v[212:215],a[44:47],v[72:75]
ld a[12:15],v247,s[44:47],s55 offen offset:2048 sc1
mm v[104:107],v[212:215],a[48:51],v[104:107]
aw a130,v232
aw a134,v233
mm v[104:107],v[208:211],a[36:39],v[104:107]
)ASM" R"ASM(rt v[212:213],v242 offset:128
rt v[214:215],v242 offset:1152
mm v[108:111],v[216:219],a[36:39],v[108:111]
ar v234,a184
ar v235,a188
mm v[108:111],v[220:223],a[48:51],v[108:111]
ld a[16:19],v244,s[44:47],s55 offen offset:3072 sc1
mm v[76:79],v[220:223],a[44:47],v[76:79]
v_perm_b32 v236, v235, v234, s86
v_perm_b32 v237, v235, v234, s87
mm v[76:79],v[216:219],a[72:75],v[76:79]
rt v[208:209],v242 offset:8576
rt v[210:211],v242 offset:9600
mm v[44:47],v[216:219],a[68:71],v[44:47]
aw a131,v236
aw a135,v237
mm v[44:47],v[220:223],a[40:43],v[44:47]
s_nop 0
v_cmp_eq_u64 vcc, v[250:251], v[252:253]
mm v[12:15],v[220:223],a[32:35],v[12:15]
wt lgkmcnt(4)
ar v230,a161
mm v[12:15],v[216:219],a[64:67],v[12:15]
rt v[220:221],v243 offset:128
rt v[222:223],v243 offset:1152
mm v[16:19],v[196:199],a[64:67],v[16:19]
ar v231,a165
v_perm_b32 v232, v231, v230, s86
mm v[16:19],v[192:195],a[32:35],v[16:19]
ld a[20:23],v245,s[44:47],s55 offen offset:3072 sc1
mm v[48:51],v[192:195],a[40:43],v[48:51]
v_perm_b32 v233, v231, v230, s87
aw a136,v232
mm v[48:51],v[196:199],a[68:71],v[48:51]
rt v[216:217],v243 offset:8576
rt v[218:219],v243 offset:9600
mm v[80:83],v[196:199],a[72:75],v[80:83]
aw a140,v233
ar v234,a169
mm v[80:83],v[192:195],a[44:47],v[80:83]
ld a[24:27],v246,s[44:47],s55 offen offset:3072 sc1
mm v[112:115],v[192:195],a[48:51],v[112:115]
wt vmcnt(18) lgkmcnt(0)
s_barrier
mm v[112:115],v[196:199],a[36:39],v[112:115]
rt v[192:193],v240 offset:16896
rt v[194:195],v240 offset:17920
mm v[116:119],v[204:207],a[36:39],v[116:119]
s_xor_b32 s90, s72, 0
s_lshl_b32 s92, s76, 5
mm v[116:119],v[200:203],a[48:51],v[116:119]
buffer_load_dwordx2 v[250:251], v248, s[64:67], s5 offen sc1
mm v[84:87],v[200:203],a[44:47],v[84:87]
s_add_u32 s92, s92, s90
s_lshl2_add_u32 s92, s92, s3
mm v[84:87],v[204:207],a[72:75],v[84:87]
rt v[196:197],v240 offset:25344
rt v[198:199],v240 offset:26368
mm v[52:55],v[204:207],a[68:71],v[52:55]
s_lshl_b32 s92, s92, 12
ar v235,a173
mm v[52:55],v[200:203],a[40:43],v[52:55]
ld a[28:31],v247,s[44:47],s55 offen offset:3072 sc1
mm v[20:23],v[200:203],a[32:35],v[20:23]
s_nop 0
s_bfe_u32 s55, s4, 262168
mm v[20:23],v[204:207],a[64:67],v[20:23]
rt v[200:201],v241 offset:16896
rt v[202:203],v241 offset:17920
mm v[24:27],v[212:215],a[64:67],v[24:27]
s_add_u32 m0, s83, 20096
s_xor_b32 s55, s55, s72
mm v[24:27],v[208:211],a[32:35],v[24:27]
ld v227,s[56:59],s80 offen lds
mm v[56:59],v[208:211],a[40:43],v[56:59]
s_add_u32 s80, s80, 131072
s_lshl_b32 s55, s55, 13
mm v[56:59],v[212:215],a[68:71],v[56:59]
rt v[204:205],v241 offset:25344
rt v[206:207],v241 offset:26368
mm v[88:91],v[212:215],a[72:75],v[88:91]
s_add_u32 m0, s83, 33792
v_perm_b32 v236, v235, v234, s86
mm v[88:91],v[208:211],a[44:47],v[88:91]
ld v224,s[56:59],s80 offen lds
mm v[120:123],v[208:211],a[48:51],v[120:123]
s_add_u32 s68, s16, s92
s_addc_u32 s69, s17, 0
mm v[120:123],v[212:215],a[36:39],v[120:123]
rt v[208:209],v242 offset:16896
rt v[210:211],v242 offset:17920
mm v[124:127],v[220:223],a[36:39],v[124:127]
s_add_u32 m0, s83, 34816
s_and_b32 s69, s69, 0xffff
mm v[124:127],v[216:219],a[48:51],v[124:127]
ld v225,s[56:59],s80 offen lds
mm v[92:95],v[216:219],a[44:47],v[92:95]
s_mov_b32 s70, 4096
s_mov_b32 s71, 0x20000
mm v[92:95],v[220:223],a[72:75],v[92:95]
)ASM" R"ASM(rt v[212:213],v242 offset:25344
rt v[214:215],v242 offset:26368
mm v[60:63],v[220:223],a[68:71],v[60:63]
s_add_u32 m0, s83, 35968
v_perm_b32 v237, v235, v234, s87
mm v[60:63],v[216:219],a[40:43],v[60:63]
ld v226,s[56:59],s80 offen lds
mm v[28:31],v[216:219],a[32:35],v[28:31]
wt vmcnt(16) lgkmcnt(4)
aw a137,v236
mm v[28:31],v[220:223],a[64:67],v[28:31]
rt v[216:217],v243 offset:16896
rt v[218:219],v243 offset:17920
mm v[0:3],v[192:195],a[52:55],v[0:3]
s_add_u32 m0, s83, 36992
aw a141,v237
mm v[0:3],v[196:199],a[80:83],v[0:3]
ld v227,s[56:59],s80 offen lds
mm v[32:35],v[196:199],a[84:87],v[32:35]
s_bitcmp1_b32 vcc_lo, 4
s_cbranch_scc0 .La5_LO_NB4
.La5_LO_KB4:
mm v[32:35],v[192:195],a[56:59],v[32:35]
rt v[220:221],v243 offset:25344
rt v[222:223],v243 offset:26368
mm v[64:67],v[192:195],a[60:63],v[64:67]
s_bfe_u32 s80, s4, 262164
s_xor_b32 s80, s80, s72
mm v[64:67],v[196:199],a[88:91],v[64:67]
ld a[32:35],v244,s[44:47],s52 offen offset:0 sc1
mm v[96:99],v[196:199],a[92:95],v[96:99]
s_lshl_b32 s80, s80, 18
ar v230,a177
mm v[96:99],v[192:195],a[76:79],v[96:99]
rt v[196:197],v240 offset:17024
rt v[198:199],v240 offset:18048
mm v[100:103],v[200:203],a[76:79],v[100:103]
s_add_u32 s48, s36, 0
s_addc_u32 s49, s37, 0
mm v[100:103],v[204:207],a[92:95],v[100:103]
ld a[36:39],v245,s[44:47],s52 offen offset:0 sc1
mm v[68:71],v[204:207],a[88:91],v[68:71]
s_and_b32 s49, s49, 0xffff
s_mov_b32 s50, 2048
mm v[68:71],v[200:203],a[60:63],v[68:71]
rt v[192:193],v240 offset:25472
rt v[194:195],v240 offset:26496
mm v[36:39],v[200:203],a[56:59],v[36:39]
s_mov_b32 s51, 0x20000
v_and_b32 v238, 0xf0, v255
mm v[36:39],v[204:207],a[84:87],v[36:39]
ld a[40:43],v246,s[44:47],s52 offen offset:0 sc1
mm v[4:7],v[204:207],a[80:83],v[4:7]
wt lgkmcnt(4)
v_lshlrev_b32 v238, 2, v238
mm v[4:7],v[200:203],a[52:55],v[4:7]
rt v[204:205],v241 offset:17024
rt v[206:207],v241 offset:18048
mm v[8:11],v[208:211],a[52:55],v[8:11]
v_bfe_u32 v249, v255, 8, 1
v_lshl_add_u32 v238, v249, 5, v238
mm v[8:11],v[212:215],a[80:83],v[8:11]
ld a[44:47],v247,s[44:47],s52 offen offset:0 sc1
mm v[40:43],v[212:215],a[84:87],v[40:43]
v_lshrrev_b32 v249, 9, v255
v_lshl_add_u32 v238, v249, 4, v238
mm v[40:43],v[208:211],a[56:59],v[40:43]
rt v[200:201],v241 offset:25472
rt v[202:203],v241 offset:26496
mm v[72:75],v[208:211],a[60:63],v[72:75]
s_lshr_b32 s23, s72, 3
s_lshl3_add_u32 s92, s23, s76
mm v[72:75],v[212:215],a[88:91],v[72:75]
ld a[48:51],v244,s[44:47],s52 offen offset:1024 sc1
mm v[104:107],v[212:215],a[92:95],v[104:107]
s_lshl_b32 s92, s92, 19
s_lshl_b32 s97, s3, 16
mm v[104:107],v[208:211],a[76:79],v[104:107]
rt v[212:213],v242 offset:17024
rt v[214:215],v242 offset:18048
mm v[108:111],v[216:219],a[76:79],v[108:111]
s_add_u32 s94, s92, s97
s_add_u32 s95, s94, 32768
mm v[108:111],v[220:223],a[92:95],v[108:111]
ld a[64:67],v245,s[44:47],s52 offen offset:1024 sc1
mm v[76:79],v[220:223],a[88:91],v[76:79]
s_sub_u32 s96, s92, s97
s_add_u32 s96, s96, 458752
mm v[76:79],v[216:219],a[60:63],v[76:79]
rt v[208:209],v242 offset:25472
rt v[210:211],v242 offset:26496
mm v[44:47],v[216:219],a[56:59],v[44:47]
s_add_u32 s97, s96, 32768
s_lshl3_add_u32 s92, s23, s101
mm v[44:47],v[220:223],a[84:87],v[44:47]
s_lshl_b32 s85, s76, 14
s_lshl3_add_u32 s92, s92, s85
mm v[12:15],v[220:223],a[80:83],v[12:15]
wt lgkmcnt(4)
s_add_u32 s92, s92, 2097152
)ASM" R"ASM(mm v[12:15],v[216:219],a[52:55],v[12:15]
rt v[220:221],v243 offset:17024
rt v[222:223],v243 offset:18048
mm v[16:19],v[196:199],a[52:55],v[16:19]
ar v231,a181
v_perm_b32 v232, v231, v230, s86
mm v[16:19],v[192:195],a[80:83],v[16:19]
ld a[68:71],v246,s[44:47],s52 offen offset:1024 sc1
mm v[48:51],v[192:195],a[84:87],v[48:51]
v_perm_b32 v233, v231, v230, s87
aw a138,v232
mm v[48:51],v[196:199],a[56:59],v[48:51]
rt v[216:217],v243 offset:25472
rt v[218:219],v243 offset:26496
mm v[80:83],v[196:199],a[60:63],v[80:83]
aw a142,v233
ar v234,a185
mm v[80:83],v[192:195],a[88:91],v[80:83]
ld a[72:75],v247,s[44:47],s52 offen offset:1024 sc1
mm v[112:115],v[192:195],a[92:95],v[112:115]
ar v235,a189
v_perm_b32 v236, v235, v234, s86
mm v[112:115],v[196:199],a[76:79],v[112:115]
rt v[192:193],v240 offset:33792
rt v[194:195],v240 offset:34816
mm v[116:119],v[204:207],a[76:79],v[116:119]
s_add_u32 m0, s83, 50688
v_perm_b32 v237, v235, v234, s87
mm v[116:119],v[200:203],a[92:95],v[116:119]
ld v224,s[56:59],s80 offen lds
mm v[84:87],v[200:203],a[88:91],v[84:87]
aw a139,v236
aw a143,v237
mm v[84:87],v[204:207],a[60:63],v[84:87]
rt v[196:197],v240 offset:42240
rt v[198:199],v240 offset:43264
mm v[52:55],v[204:207],a[56:59],v[52:55]
s_add_u32 m0, s83, 51712
ar v230,a162
mm v[52:55],v[200:203],a[84:87],v[52:55]
ld v225,s[56:59],s80 offen lds
mm v[20:23],v[200:203],a[80:83],v[20:23]
wt lgkmcnt(4)
ar v231,a166
mm v[20:23],v[204:207],a[52:55],v[20:23]
rt v[200:201],v241 offset:33792
rt v[202:203],v241 offset:34816
mm v[24:27],v[212:215],a[52:55],v[24:27]
s_add_u32 m0, s83, 52864
v_perm_b32 v232, v231, v230, s86
mm v[24:27],v[208:211],a[80:83],v[24:27]
ld v226,s[56:59],s80 offen lds
mm v[56:59],v[208:211],a[84:87],v[56:59]
v_perm_b32 v233, v231, v230, s87
aw a144,v232
mm v[56:59],v[212:215],a[56:59],v[56:59]
rt v[204:205],v241 offset:42240
rt v[206:207],v241 offset:43264
mm v[88:91],v[212:215],a[60:63],v[88:91]
aw a148,v233
ar v234,a170
mm v[88:91],v[208:211],a[88:91],v[88:91]
ld v[136:139],v244,s[44:47],s52 offen offset:2048 sc1
mm v[120:123],v[208:211],a[92:95],v[120:123]
ar v235,a174
v_perm_b32 v236, v235, v234, s86
mm v[120:123],v[212:215],a[76:79],v[120:123]
rt v[208:209],v242 offset:33792
rt v[210:211],v242 offset:34816
mm v[124:127],v[220:223],a[76:79],v[124:127]
v_perm_b32 v237, v235, v234, s87
aw a145,v236
mm v[124:127],v[216:219],a[92:95],v[124:127]
ld v[140:143],v245,s[44:47],s52 offen offset:2048 sc1
mm v[92:95],v[216:219],a[88:91],v[92:95]
aw a149,v237
ar v230,a178
mm v[92:95],v[220:223],a[60:63],v[92:95]
rt v[212:213],v242 offset:42240
rt v[214:215],v242 offset:43264
mm v[60:63],v[220:223],a[56:59],v[60:63]
ar v231,a182
v_perm_b32 v232, v231, v230, s86
mm v[60:63],v[216:219],a[84:87],v[60:63]
v_perm_b32 v233, v231, v230, s87
aw a146,v232
mm v[28:31],v[216:219],a[80:83],v[28:31]
wt vmcnt(18) lgkmcnt(4)
aw a150,v233
mm v[28:31],v[220:223],a[52:55],v[28:31]
rt v[216:217],v243 offset:33792
rt v[218:219],v243 offset:34816
mm v[0:3],v[192:195],a[0:3],v[0:3]
ar v234,a186
ar v235,a190
mm v[0:3],v[196:199],a[16:19],v[0:3]
ld v[152:155],v246,s[44:47],s52 offen offset:2048 sc1
mm v[32:35],v[196:199],a[20:23],v[32:35]
v_perm_b32 v236, v235, v234, s86
v_perm_b32 v237, v235, v234, s87
)ASM" R"ASM(mm v[32:35],v[192:195],a[4:7],v[32:35]
rt v[220:221],v243 offset:42240
rt v[222:223],v243 offset:43264
mm v[64:67],v[192:195],a[8:11],v[64:67]
aw a147,v236
aw a151,v237
mm v[64:67],v[196:199],a[24:27],v[64:67]
ld v[156:159],v247,s[44:47],s52 offen offset:2048 sc1
mm v[96:99],v[196:199],a[28:31],v[96:99]
ar v230,a163
ar v231,a167
mm v[96:99],v[192:195],a[12:15],v[96:99]
rt v[196:197],v240 offset:33920
rt v[198:199],v240 offset:34944
mm v[100:103],v[200:203],a[12:15],v[100:103]
v_perm_b32 v232, v231, v230, s86
v_perm_b32 v233, v231, v230, s87
mm v[100:103],v[204:207],a[28:31],v[100:103]
ld v[168:171],v244,s[44:47],s52 offen offset:3072 sc1
mm v[68:71],v[204:207],a[24:27],v[68:71]
aw a152,v232
aw a156,v233
mm v[68:71],v[200:203],a[8:11],v[68:71]
rt v[192:193],v240 offset:42368
rt v[194:195],v240 offset:43392
mm v[36:39],v[200:203],a[4:7],v[36:39]
v_add_u32 v240, 4294899712, v240
ar v234,a171
mm v[36:39],v[204:207],a[20:23],v[36:39]
ld v[172:175],v245,s[44:47],s52 offen offset:3072 sc1
mm v[4:7],v[204:207],a[16:19],v[4:7]
wt lgkmcnt(4)
ar v235,a175
mm v[4:7],v[200:203],a[0:3],v[4:7]
rt v[204:205],v241 offset:33920
rt v[206:207],v241 offset:34944
mm v[8:11],v[208:211],a[0:3],v[8:11]
s_nop 0
v_cmp_eq_u64 vcc, v[250:251], v[252:253]
mm v[8:11],v[212:215],a[16:19],v[8:11]
ld v[184:187],v246,s[44:47],s52 offen offset:3072 sc1
mm v[40:43],v[212:215],a[20:23],v[40:43]
v_perm_b32 v236, v235, v234, s86
v_perm_b32 v237, v235, v234, s87
mm v[40:43],v[208:211],a[4:7],v[40:43]
rt v[200:201],v241 offset:42368
rt v[202:203],v241 offset:43392
mm v[72:75],v[208:211],a[8:11],v[72:75]
v_add_u32 v241, 4294899712, v241
aw a153,v236
mm v[72:75],v[212:215],a[24:27],v[72:75]
ld v[188:191],v247,s[44:47],s52 offen offset:3072 sc1
mm v[104:107],v[212:215],a[28:31],v[104:107]
s_bfe_u32 s52, s4, 262172
s_xor_b32 s52, s52, s72
mm v[104:107],v[208:211],a[12:15],v[104:107]
rt v[212:213],v242 offset:33920
rt v[214:215],v242 offset:34944
mm v[108:111],v[216:219],a[12:15],v[108:111]
s_lshl_b32 s52, s52, 13
aw a157,v237
mm v[108:111],v[220:223],a[28:31],v[108:111]
buffer_load_dwordx2 v[250:251], v248, s[64:67], s5 offen sc1
mm v[76:79],v[220:223],a[24:27],v[76:79]
ar v230,a179
ar v231,a183
mm v[76:79],v[216:219],a[8:11],v[76:79]
rt v[208:209],v242 offset:42368
rt v[210:211],v242 offset:43392
mm v[44:47],v[216:219],a[4:7],v[44:47]
s_add_u32 m0, s83, 53888
v_add_u32 v242, 4294899712, v242
mm v[44:47],v[220:223],a[20:23],v[44:47]
ld v227,s[56:59],s80 offen lds
mm v[12:15],v[220:223],a[16:19],v[12:15]
wt lgkmcnt(4)
s_add_u32 s80, s80, 131072
mm v[12:15],v[216:219],a[0:3],v[12:15]
rt v[220:221],v243 offset:33920
rt v[222:223],v243 offset:34944
mm v[16:19],v[196:199],a[0:3],v[16:19]
s_add_u32 m0, s83, 0
v_perm_b32 v232, v231, v230, s86
mm v[16:19],v[192:195],a[16:19],v[16:19]
ld v224,s[56:59],s80 offen lds
mm v[48:51],v[192:195],a[20:23],v[48:51]
v_perm_b32 v233, v231, v230, s87
aw a154,v232
mm v[48:51],v[196:199],a[4:7],v[48:51]
rt v[216:217],v243 offset:42368
rt v[218:219],v243 offset:43392
mm v[80:83],v[196:199],a[8:11],v[80:83]
s_add_u32 m0, s83, 1024
v_add_u32 v243, 4294899712, v243
mm v[80:83],v[192:195],a[24:27],v[80:83]
ld v225,s[56:59],s80 offen lds
mm v[112:115],v[192:195],a[28:31],v[112:115]
wt vmcnt(23) lgkmcnt(0)
s_barrier
mm v[112:115],v[196:199],a[12:15],v[112:115]
)ASM" R"ASM(rt v[192:193],v240 offset:16896
rt v[194:195],v240 offset:17920
mm v[116:119],v[204:207],a[12:15],v[116:119]
s_add_u32 m0, s83, 2176
aw a158,v233
mm v[116:119],v[200:203],a[28:31],v[116:119]
ld v226,s[56:59],s80 offen lds
mm v[84:87],v[200:203],a[24:27],v[84:87]
ar v234,a187
ar v235,a191
mm v[84:87],v[204:207],a[8:11],v[84:87]
rt v[196:197],v240 offset:25344
rt v[198:199],v240 offset:26368
mm v[52:55],v[204:207],a[4:7],v[52:55]
s_add_u32 m0, s83, 3200
v_perm_b32 v236, v235, v234, s86
mm v[52:55],v[200:203],a[20:23],v[52:55]
ld v227,s[56:59],s80 offen lds
mm v[20:23],v[200:203],a[16:19],v[20:23]
s_nop 0
s_bfe_u32 s80, s4, 262164
mm v[20:23],v[204:207],a[0:3],v[20:23]
rt v[200:201],v241 offset:16896
rt v[202:203],v241 offset:17920
mm v[24:27],v[212:215],a[0:3],v[24:27]
s_bitcmp1_b32 vcc_lo, 5
s_cbranch_scc0 .La5_LO_NB5
.La5_LO_KB5:
mm v[24:27],v[208:211],a[16:19],v[24:27]
ld a[52:55],v244,s[44:47],s53 offen offset:0 sc1
mm v[56:59],v[208:211],a[20:23],v[56:59]
s_xor_b32 s80, s80, s72
s_lshl_b32 s80, s80, 18
mm v[56:59],v[212:215],a[4:7],v[56:59]
rt v[204:205],v241 offset:25344
rt v[206:207],v241 offset:26368
mm v[88:91],v[212:215],a[8:11],v[88:91]
s_xor_b32 s80, s80, 7340032
s_add_u32 s80, s80, 131072
mm v[88:91],v[208:211],a[24:27],v[88:91]
ld a[56:59],v245,s[44:47],s53 offen offset:0 sc1
mm v[120:123],v[208:211],a[28:31],v[120:123]
v_perm_b32 v237, v235, v234, s87
aw a155,v236
mm v[120:123],v[212:215],a[12:15],v[120:123]
rt v[208:209],v242 offset:16896
rt v[210:211],v242 offset:17920
mm v[124:127],v[220:223],a[12:15],v[124:127]
aw a159,v237
ar v230,a192
mm v[124:127],v[216:219],a[28:31],v[124:127]
ld a[12:15],v246,s[44:47],s53 offen offset:0 sc1
mm v[92:95],v[216:219],a[24:27],v[92:95]
ar v231,a196
v_perm_b32 v232, v231, v230, s86
mm v[92:95],v[220:223],a[8:11],v[92:95]
rt v[212:213],v242 offset:25344
rt v[214:215],v242 offset:26368
mm v[60:63],v[220:223],a[4:7],v[60:63]
v_perm_b32 v233, v231, v230, s87
aw a160,v232
mm v[60:63],v[216:219],a[20:23],v[60:63]
aw a164,v233
ar v234,a200
mm v[28:31],v[216:219],a[16:19],v[28:31]
wt vmcnt(20) lgkmcnt(4)
ar v235,a204
mm v[28:31],v[220:223],a[0:3],v[28:31]
rt v[216:217],v243 offset:16896
rt v[218:219],v243 offset:17920
mm v[0:3],v[192:195],a[32:35],v[0:3]
v_perm_b32 v236, v235, v234, s86
v_perm_b32 v237, v235, v234, s87
mm v[0:3],v[196:199],a[48:51],v[0:3]
ld a[0:3],v247,s[44:47],s53 offen offset:0 sc1
mm v[32:35],v[196:199],a[64:67],v[32:35]
aw a161,v236
aw a165,v237
mm v[32:35],v[192:195],a[36:39],v[32:35]
rt v[220:221],v243 offset:25344
rt v[222:223],v243 offset:26368
mm v[64:67],v[192:195],a[40:43],v[64:67]
ar v230,a208
ar v231,a212
mm v[64:67],v[196:199],a[68:71],v[64:67]
ld a[4:7],v244,s[44:47],s53 offen offset:1024 sc1
mm v[96:99],v[196:199],a[72:75],v[96:99]
v_perm_b32 v232, v231, v230, s86
v_perm_b32 v233, v231, v230, s87
mm v[96:99],v[192:195],a[44:47],v[96:99]
rt v[196:197],v240 offset:17024
rt v[198:199],v240 offset:18048
mm v[100:103],v[200:203],a[44:47],v[100:103]
aw a162,v232
aw a166,v233
mm v[100:103],v[204:207],a[72:75],v[100:103]
ld a[8:11],v245,s[44:47],s53 offen offset:1024 sc1
mm v[68:71],v[204:207],a[68:71],v[68:71]
ar v234,a216
ar v235,a220
)ASM" R"ASM(mm v[68:71],v[200:203],a[40:43],v[68:71]
rt v[192:193],v240 offset:25472
rt v[194:195],v240 offset:26496
mm v[36:39],v[200:203],a[36:39],v[36:39]
.La5_LW3_2:
s_cbranch_execz .La5_LW3_6
v_perm_b32 v236, v235, v234, s86
v_perm_b32 v237, v235, v234, s87
mm v[36:39],v[204:207],a[64:67],v[36:39]
ld a[16:19],v246,s[44:47],s53 offen offset:1024 sc1
mm v[4:7],v[204:207],a[48:51],v[4:7]
wt lgkmcnt(4)
aw a163,v236
mm v[4:7],v[200:203],a[32:35],v[4:7]
rt v[204:205],v241 offset:17024
rt v[206:207],v241 offset:18048
mm v[8:11],v[208:211],a[32:35],v[8:11]
aw a167,v237
ar v230,a193
mm v[8:11],v[212:215],a[48:51],v[8:11]
ld a[20:23],v247,s[44:47],s53 offen offset:1024 sc1
mm v[40:43],v[212:215],a[64:67],v[40:43]
ar v231,a197
v_perm_b32 v232, v231, v230, s86
mm v[40:43],v[208:211],a[36:39],v[40:43]
rt v[200:201],v241 offset:25472
rt v[202:203],v241 offset:26496
mm v[72:75],v[208:211],a[40:43],v[72:75]
v_perm_b32 v233, v231, v230, s87
aw a168,v232
mm v[72:75],v[212:215],a[68:71],v[72:75]
aw a172,v233
ar v234,a201
mm v[104:107],v[212:215],a[72:75],v[104:107]
ar v235,a205
v_perm_b32 v236, v235, v234, s86
mm v[104:107],v[208:211],a[44:47],v[104:107]
rt v[212:213],v242 offset:17024
rt v[214:215],v242 offset:18048
mm v[108:111],v[216:219],a[44:47],v[108:111]
v_perm_b32 v237, v235, v234, s87
aw a169,v236
mm v[108:111],v[220:223],a[72:75],v[108:111]
aw a173,v237
ar v230,a209
mm v[76:79],v[220:223],a[68:71],v[76:79]
ar v231,a213
v_perm_b32 v232, v231, v230, s86
mm v[76:79],v[216:219],a[40:43],v[76:79]
rt v[208:209],v242 offset:25472
rt v[210:211],v242 offset:26496
mm v[44:47],v[216:219],a[36:39],v[44:47]
v_perm_b32 v233, v231, v230, s87
aw a170,v232
mm v[44:47],v[220:223],a[64:67],v[44:47]
aw a174,v233
ar v234,a217
mm v[12:15],v[220:223],a[48:51],v[12:15]
wt lgkmcnt(4)
ar v235,a221
mm v[12:15],v[216:219],a[32:35],v[12:15]
rt v[220:221],v243 offset:17024
rt v[222:223],v243 offset:18048
mm v[16:19],v[196:199],a[32:35],v[16:19]
v_perm_b32 v236, v235, v234, s86
v_perm_b32 v237, v235, v234, s87
mm v[16:19],v[192:195],a[48:51],v[16:19]
ld a[24:27],v244,s[44:47],s53 offen offset:2048 sc1
mm v[48:51],v[192:195],a[64:67],v[48:51]
aw a171,v236
aw a175,v237
mm v[48:51],v[196:199],a[36:39],v[48:51]
rt v[216:217],v243 offset:25472
rt v[218:219],v243 offset:26496
mm v[80:83],v[196:199],a[40:43],v[80:83]
ar v230,a194
ar v231,a198
mm v[80:83],v[192:195],a[68:71],v[80:83]
ld a[28:31],v245,s[44:47],s53 offen offset:2048 sc1
mm v[112:115],v[192:195],a[72:75],v[112:115]
v_perm_b32 v232, v231, v230, s86
v_perm_b32 v233, v231, v230, s87
mm v[112:115],v[196:199],a[44:47],v[112:115]
rt v[192:193],v240 offset:33792
rt v[194:195],v240 offset:34816
mm v[116:119],v[204:207],a[44:47],v[116:119]
aw a176,v232
aw a180,v233
mm v[116:119],v[200:203],a[72:75],v[116:119]
ld a[60:63],v246,s[44:47],s53 offen offset:2048 sc1
mm v[84:87],v[200:203],a[68:71],v[84:87]
ar v234,a202
ar v235,a206
mm v[84:87],v[204:207],a[40:43],v[84:87]
rt v[196:197],v240 offset:42240
rt v[198:199],v240 offset:43264
mm v[52:55],v[204:207],a[36:39],v[52:55]
v_perm_b32 v236, v235, v234, s86
v_perm_b32 v237, v235, v234, s87
mm v[52:55],v[200:203],a[64:67],v[52:55]
)ASM" R"ASM(ld v[128:131],v247,s[44:47],s53 offen offset:2048 sc1
mm v[20:23],v[200:203],a[48:51],v[20:23]
wt lgkmcnt(4)
aw a177,v236
mm v[20:23],v[204:207],a[32:35],v[20:23]
rt v[200:201],v241 offset:33792
rt v[202:203],v241 offset:34816
mm v[24:27],v[212:215],a[32:35],v[24:27]
aw a181,v237
ar v230,a210
mm v[24:27],v[208:211],a[48:51],v[24:27]
ld v[132:135],v244,s[44:47],s53 offen offset:3072 sc1
mm v[56:59],v[208:211],a[64:67],v[56:59]
ar v231,a214
v_perm_b32 v232, v231, v230, s86
mm v[56:59],v[212:215],a[36:39],v[56:59]
rt v[204:205],v241 offset:42240
rt v[206:207],v241 offset:43264
mm v[88:91],v[212:215],a[40:43],v[88:91]
v_perm_b32 v233, v231, v230, s87
aw a178,v232
mm v[88:91],v[208:211],a[68:71],v[88:91]
ld v[144:147],v245,s[44:47],s53 offen offset:3072 sc1
mm v[120:123],v[208:211],a[72:75],v[120:123]
aw a182,v233
ar v234,a218
mm v[120:123],v[212:215],a[44:47],v[120:123]
rt v[208:209],v242 offset:33792
rt v[210:211],v242 offset:34816
mm v[124:127],v[220:223],a[44:47],v[124:127]
ar v235,a222
v_perm_b32 v236, v235, v234, s86
mm v[124:127],v[216:219],a[72:75],v[124:127]
ld a[44:47],v246,s[44:47],s53 offen offset:3072 sc1
mm v[92:95],v[216:219],a[68:71],v[92:95]
v_perm_b32 v237, v235, v234, s87
aw a179,v236
mm v[92:95],v[220:223],a[40:43],v[92:95]
rt v[212:213],v242 offset:42240
rt v[214:215],v242 offset:43264
mm v[60:63],v[220:223],a[36:39],v[60:63]
aw a183,v237
ar v230,a195
mm v[60:63],v[216:219],a[64:67],v[60:63]
wt vmcnt(20)
v_cmp_eq_u64 vcc, v[250:251], v[252:253]
mm v[28:31],v[216:219],a[48:51],v[28:31]
wt lgkmcnt(4)
ar v231,a199
mm v[28:31],v[220:223],a[32:35],v[28:31]
rt v[216:217],v243 offset:33792
rt v[218:219],v243 offset:34816
mm v[0:3],v[192:195],v[136:139],v[0:3]
v_perm_b32 v232, v231, v230, s86
v_perm_b32 v233, v231, v230, s87
mm v[0:3],v[196:199],v[168:171],v[0:3]
ld a[32:35],v247,s[44:47],s53 offen offset:3072 sc1
mm v[32:35],v[196:199],v[172:175],v[32:35]
aw a184,v232
aw a188,v233
mm v[32:35],v[192:195],v[140:143],v[32:35]
rt v[220:221],v243 offset:42240
rt v[222:223],v243 offset:43264
mm v[64:67],v[192:195],v[152:155],v[64:67]
ar v234,a203
ar v235,a207
mm v[64:67],v[196:199],v[184:187],v[64:67]
v_perm_b32 v236, v235, v234, s86
v_perm_b32 v237, v235, v234, s87
mm v[96:99],v[196:199],v[188:191],v[96:99]
aw a185,v236
aw a189,v237
mm v[96:99],v[192:195],v[156:159],v[96:99]
rt v[196:197],v240 offset:33920
rt v[198:199],v240 offset:34944
mm v[100:103],v[200:203],v[156:159],v[100:103]
ar v230,a211
ar v231,a215
mm v[100:103],v[204:207],v[188:191],v[100:103]
buffer_load_dwordx2 v[250:251], v248, s[64:67], s5 offen sc1
mm v[68:71],v[204:207],v[184:187],v[68:71]
v_perm_b32 v232, v231, v230, s86
v_perm_b32 v233, v231, v230, s87
mm v[68:71],v[200:203],v[152:155],v[68:71]
rt v[192:193],v240 offset:42368
rt v[194:195],v240 offset:43392
mm v[36:39],v[200:203],v[140:143],v[36:39]
aw a186,v232
aw a190,v233
mm v[36:39],v[204:207],v[172:175],v[36:39]
ar v234,a219
ar v235,a223
mm v[4:7],v[204:207],v[168:171],v[4:7]
wt lgkmcnt(4)
v_perm_b32 v236, v235, v234, s86
mm v[4:7],v[200:203],v[136:139],v[4:7]
rt v[204:205],v241 offset:33920
rt v[206:207],v241 offset:34944
mm v[8:11],v[208:211],v[136:139],v[8:11]
v_perm_b32 v237, v235, v234, s87
)ASM" R"ASM(aw a187,v236
mm v[8:11],v[212:215],v[168:171],v[8:11]
aw a191,v237
ar v230,a224
mm v[40:43],v[212:215],v[172:175],v[40:43]
ar v231,a228
v_perm_b32 v232, v231, v230, s86
mm v[40:43],v[208:211],v[140:143],v[40:43]
rt v[200:201],v241 offset:42368
rt v[202:203],v241 offset:43392
mm v[72:75],v[208:211],v[152:155],v[72:75]
v_perm_b32 v233, v231, v230, s87
aw a192,v232
mm v[72:75],v[212:215],v[184:187],v[72:75]
aw a196,v233
ar v234,a232
mm v[104:107],v[212:215],v[188:191],v[104:107]
ar v235,a236
v_perm_b32 v236, v235, v234, s86
mm v[104:107],v[208:211],v[156:159],v[104:107]
rt v[212:213],v242 offset:33920
rt v[214:215],v242 offset:34944
mm v[108:111],v[216:219],v[156:159],v[108:111]
v_perm_b32 v237, v235, v234, s87
aw a193,v236
mm v[108:111],v[220:223],v[188:191],v[108:111]
aw a197,v237
ar v230,a240
mm v[76:79],v[220:223],v[184:187],v[76:79]
ar v231,a244
v_perm_b32 v232, v231, v230, s86
mm v[76:79],v[216:219],v[152:155],v[76:79]
rt v[208:209],v242 offset:42368
rt v[210:211],v242 offset:43392
mm v[44:47],v[216:219],v[140:143],v[44:47]
s_bitcmp1_b32 vcc_lo, 13
s_nop 0
s_nop 0
s_cbranch_scc0 .La5_LO_NB13
.La5_LO_KB13:
mm v[44:47],v[220:223],v[172:175],v[44:47]
v_perm_b32 v233, v231, v230, s87
aw a194,v232
mm v[12:15],v[220:223],v[168:171],v[12:15]
wt lgkmcnt(4)
aw a198,v233
mm v[12:15],v[216:219],v[136:139],v[12:15]
rt v[220:221],v243 offset:33920
rt v[222:223],v243 offset:34944
mm v[16:19],v[196:199],v[136:139],v[16:19]
ar v234,a248
ar v235,a252
mm v[16:19],v[192:195],v[168:171],v[16:19]
ld a[36:39],v244,s[44:47],s54 offen offset:2048 sc1
mm v[48:51],v[192:195],v[172:175],v[48:51]
v_perm_b32 v236, v235, v234, s86
v_perm_b32 v237, v235, v234, s87
mm v[48:51],v[196:199],v[140:143],v[48:51]
rt v[216:217],v243 offset:42368
rt v[218:219],v243 offset:43392
mm v[80:83],v[196:199],v[152:155],v[80:83]
aw a195,v236
aw a199,v237
mm v[80:83],v[192:195],v[184:187],v[80:83]
ld a[40:43],v245,s[44:47],s54 offen offset:2048 sc1
mm v[112:115],v[192:195],v[188:191],v[112:115]
wt vmcnt(19)
s_barrier
mm v[112:115],v[196:199],v[156:159],v[112:115]
rt v[192:193],v240 offset:50688
rt v[194:195],v240 offset:51712
mm v[116:119],v[204:207],v[156:159],v[116:119]
s_add_u32 m0, s83, 16896
ar v230,a225
mm v[116:119],v[200:203],v[188:191],v[116:119]
ld v224,s[56:59],s80 offen lds
mm v[84:87],v[200:203],v[184:187],v[84:87]
ar v231,a229
v_perm_b32 v232, v231, v230, s86
mm v[84:87],v[204:207],v[152:155],v[84:87]
rt v[196:197],v240 offset:59136
rt v[198:199],v240 offset:60160
mm v[52:55],v[204:207],v[140:143],v[52:55]
s_add_u32 m0, s83, 17920
v_perm_b32 v233, v231, v230, s87
mm v[52:55],v[200:203],v[172:175],v[52:55]
ld v225,s[56:59],s80 offen lds
mm v[20:23],v[200:203],v[168:171],v[20:23]
wt lgkmcnt(4)
aw a200,v232
mm v[20:23],v[204:207],v[136:139],v[20:23]
rt v[200:201],v241 offset:50688
rt v[202:203],v241 offset:51712
mm v[24:27],v[212:215],v[136:139],v[24:27]
s_add_u32 m0, s83, 19072
aw a204,v233
mm v[24:27],v[208:211],v[168:171],v[24:27]
ld v226,s[56:59],s80 offen lds
mm v[56:59],v[208:211],v[172:175],v[56:59]
ar v234,a233
)ASM" R"ASM(ar v235,a237
mm v[56:59],v[212:215],v[140:143],v[56:59]
rt v[204:205],v241 offset:59136
rt v[206:207],v241 offset:60160
mm v[88:91],v[212:215],v[152:155],v[88:91]
s_add_u32 m0, s83, 20096
v_perm_b32 v236, v235, v234, s86
mm v[88:91],v[208:211],v[184:187],v[88:91]
ld v227,s[56:59],s80 offen lds
mm v[120:123],v[208:211],v[188:191],v[120:123]
s_bfe_u32 s80, s4, 262168
s_xor_b32 s80, s80, s72
mm v[120:123],v[212:215],v[156:159],v[120:123]
rt v[208:209],v242 offset:50688
rt v[210:211],v242 offset:51712
mm v[124:127],v[220:223],v[156:159],v[124:127]
s_add_u32 m0, s83, 118272
s_lshl_b32 s80, s80, 18
mm v[124:127],v[216:219],v[188:191],v[124:127]
ld v224,s[56:59],s80 offen lds
mm v[92:95],v[216:219],v[184:187],v[92:95]
v_perm_b32 v237, v235, v234, s87
aw a201,v236
mm v[92:95],v[220:223],v[152:155],v[92:95]
rt v[212:213],v242 offset:59136
rt v[214:215],v242 offset:60160
mm v[60:63],v[220:223],v[140:143],v[60:63]
aw a205,v237
ar v230,a241
mm v[60:63],v[216:219],v[172:175],v[60:63]
ar v231,a245
v_perm_b32 v232, v231, v230, s86
mm v[28:31],v[216:219],v[168:171],v[28:31]
wt vmcnt(16) lgkmcnt(4)
v_perm_b32 v233, v231, v230, s87
mm v[28:31],v[220:223],v[136:139],v[28:31]
rt v[216:217],v243 offset:50688
rt v[218:219],v243 offset:51712
mm v[0:3],v[192:195],a[52:55],v[0:3]
s_add_u32 m0, s83, 119296
aw a202,v232
mm v[0:3],v[196:199],a[4:7],v[0:3]
ld v225,s[56:59],s80 offen lds
mm v[32:35],v[196:199],a[8:11],v[32:35]
aw a206,v233
ar v234,a249
mm v[32:35],v[192:195],a[56:59],v[32:35]
rt v[220:221],v243 offset:59136
rt v[222:223],v243 offset:60160
mm v[64:67],v[192:195],a[12:15],v[64:67]
s_add_u32 m0, s83, 120448
ar v235,a253
mm v[64:67],v[196:199],a[16:19],v[64:67]
ld v226,s[56:59],s80 offen lds
mm v[96:99],v[196:199],a[20:23],v[96:99]
v_perm_b32 v236, v235, v234, s86
v_perm_b32 v237, v235, v234, s87
mm v[96:99],v[192:195],a[0:3],v[96:99]
rt v[196:197],v240 offset:50816
rt v[198:199],v240 offset:51840
mm v[100:103],v[200:203],a[0:3],v[100:103]
aw a203,v236
aw a207,v237
mm v[100:103],v[204:207],a[20:23],v[100:103]
ld v[136:139],v246,s[44:47],s54 offen offset:2048 sc1
mm v[68:71],v[204:207],a[16:19],v[68:71]
ar v230,a226
ar v231,a230
mm v[68:71],v[200:203],a[12:15],v[68:71]
rt v[192:193],v240 offset:59264
rt v[194:195],v240 offset:60288
mm v[36:39],v[200:203],a[56:59],v[36:39]
v_perm_b32 v232, v231, v230, s86
v_perm_b32 v233, v231, v230, s87
mm v[36:39],v[204:207],a[8:11],v[36:39]
ld v[140:143],v247,s[44:47],s54 offen offset:2048 sc1
mm v[4:7],v[204:207],a[4:7],v[4:7]
wt lgkmcnt(4)
aw a208,v232
mm v[4:7],v[200:203],a[52:55],v[4:7]
rt v[204:205],v241 offset:50816
rt v[206:207],v241 offset:51840
mm v[8:11],v[208:211],a[52:55],v[8:11]
aw a212,v233
ar v234,a234
mm v[8:11],v[212:215],a[4:7],v[8:11]
ld v[152:155],v244,s[44:47],s54 offen offset:3072 sc1
mm v[40:43],v[212:215],a[8:11],v[40:43]
ar v235,a238
v_perm_b32 v236, v235, v234, s86
mm v[40:43],v[208:211],a[56:59],v[40:43]
rt v[200:201],v241 offset:59264
rt v[202:203],v241 offset:60288
mm v[72:75],v[208:211],a[12:15],v[72:75]
v_perm_b32 v237, v235, v234, s87
aw a209,v236
mm v[72:75],v[212:215],a[16:19],v[72:75]
ld v[156:159],v245,s[44:47],s54 offen offset:3072 sc1
mm v[104:107],v[212:215],a[20:23],v[104:107]
aw a213,v237
)ASM" R"ASM(ar v230,a242
mm v[104:107],v[208:211],a[0:3],v[104:107]
rt v[212:213],v242 offset:50816
rt v[214:215],v242 offset:51840
mm v[108:111],v[216:219],a[0:3],v[108:111]
ar v231,a246
v_perm_b32 v232, v231, v230, s86
mm v[108:111],v[220:223],a[20:23],v[108:111]
ld v[168:171],v246,s[44:47],s54 offen offset:3072 sc1
mm v[76:79],v[220:223],a[16:19],v[76:79]
v_perm_b32 v233, v231, v230, s87
aw a210,v232
mm v[76:79],v[216:219],a[12:15],v[76:79]
rt v[208:209],v242 offset:59264
rt v[210:211],v242 offset:60288
mm v[44:47],v[216:219],a[56:59],v[44:47]
aw a214,v233
ar v234,a250
mm v[44:47],v[220:223],a[8:11],v[44:47]
ar v235,a254
v_perm_b32 v236, v235, v234, s86
mm v[12:15],v[220:223],a[4:7],v[12:15]
wt lgkmcnt(4)
v_perm_b32 v237, v235, v234, s87
mm v[12:15],v[216:219],a[52:55],v[12:15]
rt v[220:221],v243 offset:50816
rt v[222:223],v243 offset:51840
mm v[16:19],v[196:199],a[52:55],v[16:19]
aw a211,v236
aw a215,v237
mm v[16:19],v[192:195],a[4:7],v[16:19]
ld v[172:175],v247,s[44:47],s54 offen offset:3072 sc1
mm v[48:51],v[192:195],a[8:11],v[48:51]
ar v230,a227
ar v231,a231
mm v[48:51],v[196:199],a[56:59],v[48:51]
rt v[216:217],v243 offset:59264
rt v[218:219],v243 offset:60288
mm v[80:83],v[196:199],a[12:15],v[80:83]
s_add_u32 m0, s83, 121472
v_perm_b32 v232, v231, v230, s86
mm v[80:83],v[192:195],a[16:19],v[80:83]
ld v227,s[56:59],s80 offen lds
mm v[112:115],v[192:195],a[20:23],v[112:115]
s_add_u32 s80, s80, 131072
v_perm_b32 v233, v231, v230, s87
mm v[112:115],v[196:199],a[0:3],v[112:115]
rt v[192:193],v240 offset:0
rt v[194:195],v240 offset:1024
mm v[116:119],v[204:207],a[0:3],v[116:119]
s_add_u32 m0, s83, 67584
aw a216,v232
mm v[116:119],v[200:203],a[20:23],v[116:119]
ld v224,s[56:59],s80 offen lds
mm v[84:87],v[200:203],a[16:19],v[84:87]
aw a220,v233
ar v234,a235
mm v[84:87],v[204:207],a[12:15],v[84:87]
rt v[196:197],v240 offset:8448
rt v[198:199],v240 offset:9472
mm v[52:55],v[204:207],a[56:59],v[52:55]
s_add_u32 m0, s83, 68608
ar v235,a239
mm v[52:55],v[200:203],a[8:11],v[52:55]
ld v225,s[56:59],s80 offen lds
mm v[20:23],v[200:203],a[4:7],v[20:23]
wt lgkmcnt(4)
v_perm_b32 v236, v235, v234, s86
mm v[20:23],v[204:207],a[52:55],v[20:23]
rt v[200:201],v241 offset:0
rt v[202:203],v241 offset:1024
mm v[24:27],v[212:215],a[52:55],v[24:27]
s_add_u32 m0, s83, 69760
v_perm_b32 v237, v235, v234, s87
mm v[24:27],v[208:211],a[4:7],v[24:27]
ld v226,s[56:59],s80 offen lds
wt vmcnt(19)
v_cmp_eq_u64 vcc, v[250:251], v[252:253]
mm v[56:59],v[208:211],a[8:11],v[56:59]
aw a217,v236
aw a221,v237
mm v[56:59],v[212:215],a[56:59],v[56:59]
rt v[204:205],v241 offset:8448
rt v[206:207],v241 offset:9472
mm v[88:91],v[212:215],a[12:15],v[88:91]
s_add_u32 m0, s83, 70784
ar v230,a243
mm v[88:91],v[208:211],a[16:19],v[88:91]
ld v227,s[56:59],s80 offen lds
mm v[120:123],v[208:211],a[20:23],v[120:123]
s_bfe_u32 s80, s4, 262172
s_xor_b32 s80, s80, s72
mm v[120:123],v[212:215],a[0:3],v[120:123]
rt v[208:209],v242 offset:0
rt v[210:211],v242 offset:1024
mm v[124:127],v[220:223],a[0:3],v[124:127]
s_lshl_b32 s80, s80, 18
ar v231,a247
mm v[124:127],v[216:219],a[20:23],v[124:127]
buffer_load_dwordx2 v[250:251], v248, s[64:67], s5 offen sc1
)ASM" R"ASM(mm v[92:95],v[216:219],a[16:19],v[92:95]
v_perm_b32 v232, v231, v230, s86
v_perm_b32 v233, v231, v230, s87
mm v[92:95],v[220:223],a[12:15],v[92:95]
rt v[212:213],v242 offset:8448
rt v[214:215],v242 offset:9472
mm v[60:63],v[220:223],a[56:59],v[60:63]
s_add_u32 m0, s83, 84480
aw a218,v232
mm v[60:63],v[216:219],a[8:11],v[60:63]
ld v224,s[56:59],s80 offen lds
mm v[28:31],v[216:219],a[4:7],v[28:31]
wt lgkmcnt(4)
aw a222,v233
mm v[28:31],v[220:223],a[52:55],v[28:31]
rt v[216:217],v243 offset:0
rt v[218:219],v243 offset:1024
mm v[0:3],v[192:195],a[24:27],v[0:3]
s_add_u32 m0, s83, 85504
ar v234,a251
mm v[0:3],v[196:199],v[132:135],v[0:3]
ld v225,s[56:59],s80 offen lds
mm v[32:35],v[196:199],v[144:147],v[32:35]
ar v235,a255
v_perm_b32 v236, v235, v234, s86
mm v[32:35],v[192:195],a[28:31],v[32:35]
rt v[220:221],v243 offset:8448
rt v[222:223],v243 offset:9472
mm v[64:67],v[192:195],a[60:63],v[64:67]
s_add_u32 m0, s83, 86656
v_perm_b32 v237, v235, v234, s87
.La5_LW3_3:
s_cbranch_execz .La5_LA_RETURN
mm v[64:67],v[196:199],a[44:47],v[64:67]
ld v226,s[56:59],s80 offen lds
mm v[96:99],v[196:199],a[32:35],v[96:99]
aw a219,v236
aw a223,v237
mm v[96:99],v[192:195],v[128:131],v[96:99]
rt v[196:197],v240 offset:128
rt v[198:199],v240 offset:1152
mm v[100:103],v[200:203],v[128:131],v[100:103]
s_add_u32 m0, s83, 87680
mm v[100:103],v[204:207],a[32:35],v[100:103]
ld v227,s[56:59],s80 offen lds
mm v[68:71],v[204:207],a[44:47],v[68:71]
s_add_u32 s80, s80, 131072
mm v[68:71],v[200:203],a[60:63],v[68:71]
rt v[192:193],v240 offset:8576
rt v[194:195],v240 offset:9600
mm v[36:39],v[200:203],a[28:31],v[36:39]
s_add_u32 m0, s83, 101376
mm v[36:39],v[204:207],v[144:147],v[36:39]
ld v224,s[56:59],s80 offen lds
mm v[4:7],v[204:207],v[132:135],v[4:7]
wt lgkmcnt(4)
mm v[4:7],v[200:203],a[24:27],v[4:7]
rt v[204:205],v241 offset:128
rt v[206:207],v241 offset:1152
mm v[8:11],v[208:211],a[24:27],v[8:11]
s_add_u32 m0, s83, 102400
mm v[8:11],v[212:215],v[132:135],v[8:11]
ld v225,s[56:59],s80 offen lds
mm v[40:43],v[212:215],v[144:147],v[40:43]
mm v[40:43],v[208:211],a[28:31],v[40:43]
rt v[200:201],v241 offset:8576
rt v[202:203],v241 offset:9600
mm v[72:75],v[208:211],a[60:63],v[72:75]
s_add_u32 m0, s83, 103552
mm v[72:75],v[212:215],a[44:47],v[72:75]
ld v226,s[56:59],s80 offen lds
mm v[104:107],v[212:215],a[32:35],v[104:107]
mm v[104:107],v[208:211],v[128:131],v[104:107]
rt v[212:213],v242 offset:128
rt v[214:215],v242 offset:1152
mm v[108:111],v[216:219],v[128:131],v[108:111]
s_add_u32 m0, s83, 104576
mm v[108:111],v[220:223],a[32:35],v[108:111]
ld v227,s[56:59],s80 offen lds
mm v[76:79],v[220:223],a[44:47],v[76:79]
v_bfe_u32 v224, v255, 6, 1
v_lshrrev_b32 v225, 7, v255
mm v[76:79],v[216:219],a[60:63],v[76:79]
rt v[208:209],v242 offset:8576
rt v[210:211],v242 offset:9600
mm v[44:47],v[216:219],a[28:31],v[44:47]
s_bitcmp1_b32 vcc_lo, 6
s_cbranch_scc0 .La5_LO_NB6
.La5_LO_KB6:
mm v[44:47],v[220:223],v[144:147],v[44:47]
v_and_b32 v226, 48, v255
v_lshl_add_u32 v225, v225, 6, v226
mm v[12:15],v[220:223],v[132:135],v[12:15]
wt lgkmcnt(4)
v_lshl_add_u32 v224, v224, 11, v225
mm v[12:15],v[216:219],a[24:27],v[12:15]
rt v[220:221],v243 offset:128
rt v[222:223],v243 offset:1152
mm v[16:19],v[196:199],a[24:27],v[16:19]
mm v[16:19],v[192:195],v[132:135],v[16:19]
)ASM" R"ASM(ld a[64:67],v244,s[44:47],s55 offen offset:0 sc1
mm v[48:51],v[192:195],v[144:147],v[48:51]
mm v[48:51],v[196:199],a[28:31],v[48:51]
rt v[216:217],v243 offset:8576
rt v[218:219],v243 offset:9600
mm v[80:83],v[196:199],a[60:63],v[80:83]
mm v[80:83],v[192:195],a[44:47],v[80:83]
ld a[68:71],v245,s[44:47],s55 offen offset:0 sc1
mm v[112:115],v[192:195],a[32:35],v[112:115]
wt vmcnt(25)
s_barrier
mm v[112:115],v[196:199],v[128:131],v[112:115]
rt v[192:193],v240 offset:16896
rt v[194:195],v240 offset:17920
mm v[116:119],v[204:207],v[128:131],v[116:119]
mm v[116:119],v[200:203],a[32:35],v[116:119]
ld a[72:75],v244,s[44:47],s55 offen offset:1024 sc1
mm v[84:87],v[200:203],a[44:47],v[84:87]
mm v[84:87],v[204:207],a[60:63],v[84:87]
rt v[196:197],v240 offset:25344
rt v[198:199],v240 offset:26368
mm v[52:55],v[204:207],a[28:31],v[52:55]
mm v[52:55],v[200:203],v[144:147],v[52:55]
ld a[76:79],v245,s[44:47],s55 offen offset:1024 sc1
mm v[20:23],v[200:203],v[132:135],v[20:23]
wt lgkmcnt(4)
mm v[20:23],v[204:207],a[24:27],v[20:23]
rt v[200:201],v241 offset:16896
rt v[202:203],v241 offset:17920
mm v[24:27],v[212:215],a[24:27],v[24:27]
mm v[24:27],v[208:211],v[132:135],v[24:27]
ld a[80:83],v244,s[44:47],s55 offen offset:2048 sc1
mm v[56:59],v[208:211],v[144:147],v[56:59]
mm v[56:59],v[212:215],a[28:31],v[56:59]
rt v[204:205],v241 offset:25344
rt v[206:207],v241 offset:26368
mm v[88:91],v[212:215],a[60:63],v[88:91]
mm v[88:91],v[208:211],a[44:47],v[88:91]
ld a[84:87],v245,s[44:47],s55 offen offset:2048 sc1
mm v[120:123],v[208:211],a[32:35],v[120:123]
mm v[120:123],v[212:215],v[128:131],v[120:123]
rt v[208:209],v242 offset:16896
rt v[210:211],v242 offset:17920
mm v[124:127],v[220:223],v[128:131],v[124:127]
mm v[124:127],v[216:219],a[32:35],v[124:127]
ld a[88:91],v244,s[44:47],s55 offen offset:3072 sc1
mm v[92:95],v[216:219],a[44:47],v[92:95]
mm v[92:95],v[220:223],a[60:63],v[92:95]
rt v[212:213],v242 offset:25344
rt v[214:215],v242 offset:26368
mm v[60:63],v[220:223],a[28:31],v[60:63]
mm v[60:63],v[216:219],v[144:147],v[60:63]
mm v[28:31],v[216:219],v[132:135],v[28:31]
wt vmcnt(21) lgkmcnt(4)
mm v[28:31],v[220:223],a[24:27],v[28:31]
rt v[216:217],v243 offset:16896
rt v[218:219],v243 offset:17920
mm v[0:3],v[192:195],a[36:39],v[0:3]
s_bitcmp1_b32 vcc_lo, 7
s_cbranch_scc0 .La5_LO_NB7
.La5_LO_KB7:
mm v[0:3],v[196:199],v[152:155],v[0:3]
ld a[92:95],v245,s[44:47],s55 offen offset:3072 sc1
mm v[32:35],v[196:199],v[156:159],v[32:35]
mm v[32:35],v[192:195],a[40:43],v[32:35]
rt v[220:221],v243 offset:25344
rt v[222:223],v243 offset:26368
mm v[64:67],v[192:195],v[136:139],v[64:67]
mm v[64:67],v[196:199],v[168:171],v[64:67]
ld a[0:3],v244,s[44:47],s52 offen offset:0 sc1
mm v[96:99],v[196:199],v[172:175],v[96:99]
mm v[96:99],v[192:195],v[140:143],v[96:99]
rt v[196:197],v240 offset:17024
rt v[198:199],v240 offset:18048
mm v[100:103],v[200:203],v[140:143],v[100:103]
mm v[100:103],v[204:207],v[172:175],v[100:103]
ld a[4:7],v245,s[44:47],s52 offen offset:0 sc1
mm v[68:71],v[204:207],v[168:171],v[68:71]
mm v[68:71],v[200:203],v[136:139],v[68:71]
rt v[192:193],v240 offset:25472
rt v[194:195],v240 offset:26496
mm v[36:39],v[200:203],a[40:43],v[36:39]
v_add_u32 v240, 67584, v240
mm v[36:39],v[204:207],v[156:159],v[36:39]
ld a[8:11],v244,s[44:47],s52 offen offset:1024 sc1
mm v[4:7],v[204:207],v[152:155],v[4:7]
wt lgkmcnt(4)
mm v[4:7],v[200:203],a[36:39],v[4:7]
rt v[204:205],v241 offset:17024
rt v[206:207],v241 offset:18048
mm v[8:11],v[208:211],a[36:39],v[8:11]
mm v[8:11],v[212:215],v[152:155],v[8:11]
ld a[12:15],v245,s[44:47],s52 offen offset:1024 sc1
mm v[40:43],v[212:215],v[156:159],v[40:43]
mm v[40:43],v[208:211],a[40:43],v[40:43]
rt v[200:201],v241 offset:25472
rt v[202:203],v241 offset:26496
mm v[72:75],v[208:211],v[136:139],v[72:75]
v_add_u32 v241, 67584, v241
mm v[72:75],v[212:215],v[168:171],v[72:75]
)ASM" R"ASM(ld a[16:19],v244,s[44:47],s52 offen offset:2048 sc1
mm v[104:107],v[212:215],v[172:175],v[104:107]
mm v[104:107],v[208:211],v[140:143],v[104:107]
rt v[212:213],v242 offset:17024
rt v[214:215],v242 offset:18048
mm v[108:111],v[216:219],v[140:143],v[108:111]
mm v[108:111],v[220:223],v[172:175],v[108:111]
ld a[20:23],v245,s[44:47],s52 offen offset:2048 sc1
mm v[76:79],v[220:223],v[168:171],v[76:79]
mm v[76:79],v[216:219],v[136:139],v[76:79]
rt v[208:209],v242 offset:25472
rt v[210:211],v242 offset:26496
mm v[44:47],v[216:219],a[40:43],v[44:47]
v_add_u32 v242, 67584, v242
mm v[44:47],v[220:223],v[156:159],v[44:47]
wt vmcnt(22)
v_cmp_eq_u64 vcc, v[250:251], v[252:253]
mm v[12:15],v[220:223],v[152:155],v[12:15]
wt lgkmcnt(4)
mm v[12:15],v[216:219],a[36:39],v[12:15]
rt v[220:221],v243 offset:17024
rt v[222:223],v243 offset:18048
mm v[16:19],v[196:199],a[36:39],v[16:19]
mm v[16:19],v[192:195],v[152:155],v[16:19]
ld a[24:27],v244,s[44:47],s52 offen offset:3072 sc1
mm v[48:51],v[192:195],v[156:159],v[48:51]
mm v[48:51],v[196:199],a[40:43],v[48:51]
rt v[216:217],v243 offset:25472
rt v[218:219],v243 offset:26496
mm v[80:83],v[196:199],v[136:139],v[80:83]
v_add_u32 v243, 67584, v243
mm v[80:83],v[192:195],v[168:171],v[80:83]
ld a[28:31],v245,s[44:47],s52 offen offset:3072 sc1
mm v[112:115],v[192:195],v[172:175],v[112:115]
wt vmcnt(16)
s_barrier
mm v[112:115],v[196:199],v[140:143],v[112:115]
rt v[192:193],v240 offset:50688
rt v[194:195],v240 offset:51712
mm v[116:119],v[204:207],v[140:143],v[116:119]
mm v[116:119],v[200:203],v[172:175],v[116:119]
ld a[32:35],v246,s[44:47],s55 offen offset:0 sc1
mm v[84:87],v[200:203],v[168:171],v[84:87]
mm v[84:87],v[204:207],v[136:139],v[84:87]
rt v[196:197],v240 offset:59136
rt v[198:199],v240 offset:60160
mm v[52:55],v[204:207],a[40:43],v[52:55]
mm v[52:55],v[200:203],v[156:159],v[52:55]
ld a[44:47],v247,s[44:47],s55 offen offset:0 sc1
mm v[20:23],v[200:203],v[152:155],v[20:23]
wt lgkmcnt(4)
mm v[20:23],v[204:207],a[36:39],v[20:23]
rt v[200:201],v240 offset:0
rt v[202:203],v240 offset:1024
mm v[24:27],v[212:215],a[36:39],v[24:27]
mm v[24:27],v[208:211],v[152:155],v[24:27]
ld a[48:51],v246,s[44:47],s55 offen offset:1024 sc1
mm v[56:59],v[208:211],v[156:159],v[56:59]
mm v[56:59],v[212:215],a[40:43],v[56:59]
rt v[204:205],v240 offset:8448
rt v[206:207],v240 offset:9472
mm v[88:91],v[212:215],v[136:139],v[88:91]
mm v[88:91],v[208:211],v[168:171],v[88:91]
ld a[52:55],v247,s[44:47],s55 offen offset:1024 sc1
mm v[120:123],v[208:211],v[172:175],v[120:123]
mm v[120:123],v[212:215],v[140:143],v[120:123]
rt v[208:209],v240 offset:16896
rt v[210:211],v240 offset:17920
mm v[124:127],v[220:223],v[140:143],v[124:127]
mm v[124:127],v[216:219],v[172:175],v[124:127]
ld a[56:59],v246,s[44:47],s55 offen offset:2048 sc1
mm v[92:95],v[216:219],v[168:171],v[92:95]
mm v[92:95],v[220:223],v[136:139],v[92:95]
rt v[212:213],v240 offset:25344
rt v[214:215],v240 offset:26368
mm v[60:63],v[220:223],a[40:43],v[60:63]
mm v[60:63],v[216:219],v[156:159],v[60:63]
mm v[28:31],v[216:219],v[152:155],v[28:31]
wt vmcnt(13) lgkmcnt(4)
mm v[28:31],v[220:223],a[36:39],v[28:31]
rt v[216:217],v240 offset:33792
rt v[218:219],v240 offset:34816
mm v[0:3],v[192:195],a[64:67],v[0:3]
ld a[36:39],v247,s[44:47],s55 offen offset:2048 sc1
mm v[0:3],v[196:199],a[72:75],v[0:3]
rt v[220:221],v240 offset:42240
rt v[222:223],v240 offset:43264
mm v[32:35],v[196:199],a[76:79],v[32:35]
mm v[32:35],v[192:195],a[68:71],v[32:35]
rt v[196:197],v241 offset:50688
rt v[198:199],v241 offset:51712
mm v[32:35],v[200:203],a[84:87],v[32:35]
ld a[40:43],v246,s[44:47],s55 offen offset:3072 sc1
mm v[32:35],v[204:207],a[92:95],v[32:35]
rt v[192:193],v241 offset:59136
rt v[194:195],v241 offset:60160
mm v[0:3],v[204:207],a[88:91],v[0:3]
wt vmcnt(7) lgkmcnt(4)
mm v[0:3],v[200:203],a[80:83],v[0:3]
rt v[204:205],v241 offset:0
)ASM" R"ASM(rt v[206:207],v241 offset:1024
mm v[0:3],v[208:211],a[0:3],v[0:3]
ld a[60:63],v247,s[44:47],s55 offen offset:3072 sc1
mm v[0:3],v[212:215],a[8:11],v[0:3]
rt v[200:201],v241 offset:8448
rt v[202:203],v241 offset:9472
mm v[32:35],v[212:215],a[12:15],v[32:35]
mm v[32:35],v[208:211],a[4:7],v[32:35]
rt v[212:213],v241 offset:16896
rt v[214:215],v241 offset:17920
mm v[32:35],v[216:219],a[20:23],v[32:35]
ld v[128:131],v246,s[44:47],s52 offen offset:0 sc1
mm v[32:35],v[220:223],a[28:31],v[32:35]
rt v[208:209],v241 offset:25344
rt v[210:211],v241 offset:26368
mm v[0:3],v[220:223],a[24:27],v[0:3]
wt lgkmcnt(4)
mm v[0:3],v[216:219],a[16:19],v[0:3]
rt v[220:221],v241 offset:33792
rt v[222:223],v241 offset:34816
mm v[4:7],v[196:199],a[64:67],v[4:7]
ld v[132:135],v247,s[44:47],s52 offen offset:0 sc1
mm v[4:7],v[192:195],a[72:75],v[4:7]
rt v[216:217],v241 offset:42240
rt v[218:219],v241 offset:43264
mm v[36:39],v[192:195],a[76:79],v[36:39]
mm v[36:39],v[196:199],a[68:71],v[36:39]
rt v[192:193],v242 offset:50688
rt v[194:195],v242 offset:51712
mm v[36:39],v[204:207],a[84:87],v[36:39]
ld v[144:147],v246,s[44:47],s52 offen offset:1024 sc1
mm v[36:39],v[200:203],a[92:95],v[36:39]
rt v[196:197],v242 offset:59136
rt v[198:199],v242 offset:60160
mm v[4:7],v[200:203],a[88:91],v[4:7]
wt lgkmcnt(4)
mm v[4:7],v[204:207],a[80:83],v[4:7]
rt v[200:201],v242 offset:0
rt v[202:203],v242 offset:1024
mm v[4:7],v[212:215],a[0:3],v[4:7]
ld v[148:151],v247,s[44:47],s52 offen offset:1024 sc1
mm v[4:7],v[208:211],a[8:11],v[4:7]
rt v[204:205],v242 offset:8448
rt v[206:207],v242 offset:9472
mm v[36:39],v[208:211],a[12:15],v[36:39]
mm v[36:39],v[212:215],a[4:7],v[36:39]
rt v[208:209],v242 offset:16896
rt v[210:211],v242 offset:17920
mm v[36:39],v[220:223],a[20:23],v[36:39]
mm v[36:39],v[216:219],a[28:31],v[36:39]
rt v[212:213],v242 offset:25344
rt v[214:215],v242 offset:26368
mm v[4:7],v[216:219],a[24:27],v[4:7]
wt lgkmcnt(4)
mm v[4:7],v[220:223],a[16:19],v[4:7]
rt v[216:217],v242 offset:33792
rt v[218:219],v242 offset:34816
mm v[8:11],v[192:195],a[64:67],v[8:11]
ld v[160:163],v246,s[44:47],s52 offen offset:2048 sc1
mm v[8:11],v[196:199],a[72:75],v[8:11]
rt v[220:221],v242 offset:42240
rt v[222:223],v242 offset:43264
mm v[40:43],v[196:199],a[76:79],v[40:43]
cv v234,v32,v33
cv v236,v36,v37
mm v[40:43],v[192:195],a[68:71],v[40:43]
rt v[196:197],v243 offset:50688
rt v[198:199],v243 offset:51712
mm v[40:43],v[200:203],a[84:87],v[40:43]
ld v[164:167],v247,s[44:47],s52 offen offset:2048 sc1
mm v[40:43],v[204:207],a[92:95],v[40:43]
rt v[192:193],v243 offset:59136
rt v[194:195],v243 offset:60160
mm v[8:11],v[204:207],a[88:91],v[8:11]
wt lgkmcnt(4)
mm v[8:11],v[200:203],a[80:83],v[8:11]
rt v[204:205],v243 offset:0
rt v[206:207],v243 offset:1024
mm v[8:11],v[208:211],a[0:3],v[8:11]
ld v[176:179],v246,s[44:47],s52 offen offset:3072 sc1
mm v[8:11],v[212:215],a[8:11],v[8:11]
rt v[200:201],v243 offset:8448
rt v[202:203],v243 offset:9472
mm v[40:43],v[212:215],a[12:15],v[40:43]
cv v235,v34,v35
cv v237,v38,v39
mm v[40:43],v[208:211],a[4:7],v[40:43]
rt v[212:213],v243 offset:16896
rt v[214:215],v243 offset:17920
mm v[40:43],v[216:219],a[20:23],v[40:43]
ld v[180:183],v247,s[44:47],s52 offen offset:3072 sc1
mm v[40:43],v[220:223],a[28:31],v[40:43]
rt v[208:209],v243 offset:25344
rt v[210:211],v243 offset:26368
mm v[8:11],v[220:223],a[24:27],v[8:11]
wt lgkmcnt(4)
v_bfe_u32 v250, v255, 6, 1
mm v[8:11],v[216:219],a[16:19],v[8:11]
rt v[220:221],v243 offset:33792
rt v[222:223],v243 offset:34816
)ASM" R"ASM(mm v[12:15],v[196:199],a[64:67],v[12:15]
v_permlane16_swap_b32 v234, v236
v_permlane16_swap_b32 v235, v237
mm v[12:15],v[192:195],a[72:75],v[12:15]
rt v[216:217],v243 offset:42240
rt v[218:219],v243 offset:43264
mm v[44:47],v[192:195],a[76:79],v[44:47]
cv v230,v0,v1
cv v232,v4,v5
mm v[44:47],v[196:199],a[68:71],v[44:47]
rt v[192:193],v240 offset:50816
rt v[194:195],v240 offset:51840
mm v[44:47],v[204:207],a[84:87],v[44:47]
st v[234:237],v238,s[32:35],s95 offen offset:0 sc1
mm v[44:47],v[200:203],a[92:95],v[44:47]
rt v[196:197],v240 offset:59264
rt v[198:199],v240 offset:60288
mm v[12:15],v[200:203],a[88:91],v[12:15]
wt lgkmcnt(4)
v_mul_u32_u24 v250, 7, v250
mm v[12:15],v[204:207],a[80:83],v[12:15]
rt v[200:201],v240 offset:128
rt v[202:203],v240 offset:1152
mm v[12:15],v[212:215],a[0:3],v[12:15]
cv v231,v2,v3
cv v233,v6,v7
mm v[12:15],v[208:211],a[8:11],v[12:15]
rt v[204:205],v240 offset:8576
rt v[206:207],v240 offset:9600
mm v[44:47],v[208:211],a[12:15],v[44:47]
v_permlane16_swap_b32 v230, v232
v_permlane16_swap_b32 v231, v233
mm v[44:47],v[212:215],a[4:7],v[44:47]
rt v[208:209],v240 offset:17024
rt v[210:211],v240 offset:18048
mm v[44:47],v[220:223],a[20:23],v[44:47]
st v[230:233],v238,s[32:35],s94 offen offset:0 sc1
mm v[44:47],v[216:219],a[28:31],v[44:47]
rt v[212:213],v240 offset:25472
rt v[214:215],v240 offset:26496
mm v[12:15],v[216:219],a[24:27],v[12:15]
wt lgkmcnt(4)
v_xor_b32 v250, s3, v250
mm v[12:15],v[220:223],a[16:19],v[12:15]
rt v[216:217],v240 offset:33920
rt v[218:219],v240 offset:34944
mm v[16:19],v[192:195],a[64:67],v[16:19]
cv v234,v40,v41
cv v236,v44,v45
mm v[16:19],v[196:199],a[72:75],v[16:19]
rt v[220:221],v240 offset:42368
rt v[222:223],v240 offset:43392
mm v[48:51],v[196:199],a[76:79],v[48:51]
cv v235,v42,v43
cv v237,v46,v47
mm v[48:51],v[192:195],a[68:71],v[48:51]
rt v[196:197],v241 offset:50816
rt v[198:199],v241 offset:51840
mm v[48:51],v[200:203],a[84:87],v[48:51]
v_permlane16_swap_b32 v234, v236
v_permlane16_swap_b32 v235, v237
mm v[48:51],v[204:207],a[92:95],v[48:51]
rt v[192:193],v241 offset:59264
rt v[194:195],v241 offset:60288
mm v[16:19],v[204:207],a[88:91],v[16:19]
wt lgkmcnt(4)
v_bfe_u32 v251, v255, 4, 2
mm v[16:19],v[200:203],a[80:83],v[16:19]
rt v[204:205],v241 offset:128
rt v[206:207],v241 offset:1152
mm v[16:19],v[208:211],a[0:3],v[16:19]
st v[234:237],v238,s[32:35],s95 offen offset:1024 sc1
mm v[16:19],v[212:215],a[8:11],v[16:19]
rt v[200:201],v241 offset:8576
rt v[202:203],v241 offset:9600
mm v[48:51],v[212:215],a[12:15],v[48:51]
cv v230,v8,v9
cv v232,v12,v13
mm v[48:51],v[208:211],a[4:7],v[48:51]
rt v[212:213],v241 offset:17024
rt v[214:215],v241 offset:18048
mm v[48:51],v[216:219],a[20:23],v[48:51]
cv v231,v10,v11
cv v233,v14,v15
mm v[48:51],v[220:223],a[28:31],v[48:51]
rt v[208:209],v241 offset:25472
rt v[210:211],v241 offset:26496
mm v[16:19],v[220:223],a[24:27],v[16:19]
wt lgkmcnt(4)
v_lshl_add_u32 v250, v250, 2, v251
mm v[16:19],v[216:219],a[16:19],v[16:19]
rt v[220:221],v241 offset:33920
rt v[222:223],v241 offset:34944
mm v[20:23],v[196:199],a[64:67],v[20:23]
v_permlane16_swap_b32 v230, v232
v_permlane16_swap_b32 v231, v233
mm v[20:23],v[192:195],a[72:75],v[20:23]
rt v[216:217],v241 offset:42368
rt v[218:219],v241 offset:43392
mm v[52:55],v[192:195],a[76:79],v[52:55]
)ASM" R"ASM(v_lshlrev_b32 v250, 9, v250
v_and_b32 v251, 64, v255
mm v[52:55],v[196:199],a[68:71],v[52:55]
rt v[192:193],v242 offset:50816
rt v[194:195],v242 offset:51840
mm v[52:55],v[204:207],a[84:87],v[52:55]
st v[230:233],v238,s[32:35],s94 offen offset:1024 sc1
mm v[52:55],v[200:203],a[92:95],v[52:55]
rt v[196:197],v242 offset:59264
rt v[198:199],v242 offset:60288
mm v[20:23],v[200:203],a[88:91],v[20:23]
wt lgkmcnt(4)
v_lshl_add_u32 v251, v251, 24, v250
mm v[20:23],v[204:207],a[80:83],v[20:23]
rt v[200:201],v242 offset:128
rt v[202:203],v242 offset:1152
mm v[20:23],v[212:215],a[0:3],v[20:23]
ld v[136:139],v224,s[68:71],0 offen offset:0
mm v[20:23],v[208:211],a[8:11],v[20:23]
rt v[204:205],v242 offset:8576
rt v[206:207],v242 offset:9600
mm v[52:55],v[208:211],a[12:15],v[52:55]
v_and_b32 v248, 0x1f0, v255
v_lshrrev_b32 v248, 1, v248
mm v[52:55],v[212:215],a[4:7],v[52:55]
rt v[208:209],v242 offset:17024
rt v[210:211],v242 offset:18048
mm v[52:55],v[220:223],a[20:23],v[52:55]
ld v[140:143],v225,s[48:51],0 offen offset:0
mm v[52:55],v[216:219],a[28:31],v[52:55]
rt v[212:213],v242 offset:25472
rt v[214:215],v242 offset:26496
mm v[20:23],v[216:219],a[24:27],v[20:23]
wt lgkmcnt(4)
s_mov_b32 s93, 0
ld a[224:227],v229,s[28:31],s93 offen offset:0
mm v[20:23],v[220:223],a[16:19],v[20:23]
rt v[216:217],v242 offset:33920
rt v[218:219],v242 offset:34944
mm v[24:27],v[192:195],a[64:67],v[24:27]
cv v234,v48,v49
cv v236,v52,v53
mm v[24:27],v[196:199],a[72:75],v[24:27]
rt v[220:221],v242 offset:42368
rt v[222:223],v242 offset:43392
mm v[56:59],v[196:199],a[76:79],v[56:59]
cv v235,v50,v51
cv v237,v54,v55
mm v[56:59],v[192:195],a[68:71],v[56:59]
rt v[196:197],v243 offset:50816
rt v[198:199],v243 offset:51840
mm v[56:59],v[200:203],a[84:87],v[56:59]
v_permlane16_swap_b32 v234, v236
v_permlane16_swap_b32 v235, v237
mm v[56:59],v[204:207],a[92:95],v[56:59]
rt v[192:193],v243 offset:59264
rt v[194:195],v243 offset:60288
mm v[24:27],v[204:207],a[88:91],v[24:27]
wt lgkmcnt(4)
ld a[228:231],v229,s[28:31],s93 offen offset:2048
mm v[24:27],v[200:203],a[80:83],v[24:27]
rt v[204:205],v243 offset:128
rt v[206:207],v243 offset:1152
mm v[24:27],v[208:211],a[0:3],v[24:27]
st v[234:237],v238,s[32:35],s95 offen offset:2048 sc1
mm v[24:27],v[212:215],a[8:11],v[24:27]
rt v[200:201],v243 offset:8576
rt v[202:203],v243 offset:9600
mm v[56:59],v[212:215],a[12:15],v[56:59]
cv v230,v16,v17
cv v232,v20,v21
.La5_LW3_4:
s_cbranch_execz .La5_LA_RETURN
mm v[56:59],v[208:211],a[4:7],v[56:59]
rt v[212:213],v243 offset:17024
rt v[214:215],v243 offset:18048
mm v[56:59],v[216:219],a[20:23],v[56:59]
cv v231,v18,v19
cv v233,v22,v23
mm v[56:59],v[220:223],a[28:31],v[56:59]
rt v[208:209],v243 offset:25472
rt v[210:211],v243 offset:26496
mm v[24:27],v[220:223],a[24:27],v[24:27]
wt lgkmcnt(4)
s_mov_b32 s93, 4096
ld a[232:235],v229,s[28:31],s93 offen offset:0
mm v[24:27],v[216:219],a[16:19],v[24:27]
rt v[220:221],v243 offset:33920
rt v[222:223],v243 offset:34944
mm v[28:31],v[196:199],a[64:67],v[28:31]
v_permlane16_swap_b32 v230, v232
v_permlane16_swap_b32 v231, v233
mm v[28:31],v[192:195],a[72:75],v[28:31]
rt v[216:217],v243 offset:42368
rt v[218:219],v243 offset:43392
mm v[60:63],v[192:195],a[76:79],v[60:63]
ld a[236:239],v229,s[28:31],s93 offen offset:2048
mm v[60:63],v[196:199],a[68:71],v[60:63]
rt v[192:193],v240 offset:50688
rt v[194:195],v240 offset:51712
)ASM" R"ASM(mm v[60:63],v[204:207],a[84:87],v[60:63]
st v[230:233],v238,s[32:35],s94 offen offset:2048 sc1
mm v[60:63],v[200:203],a[92:95],v[60:63]
rt v[196:197],v240 offset:59136
rt v[198:199],v240 offset:60160
mm v[28:31],v[200:203],a[88:91],v[28:31]
wt lgkmcnt(4)
s_mov_b32 s93, 8192
ld a[240:243],v229,s[28:31],s93 offen offset:0
mm v[28:31],v[204:207],a[80:83],v[28:31]
rt v[200:201],v240 offset:0
rt v[202:203],v240 offset:1024
mm v[28:31],v[212:215],a[0:3],v[28:31]
ld v[152:155],v224,s[68:71],0 offen offset:512
mm v[28:31],v[208:211],a[8:11],v[28:31]
rt v[204:205],v240 offset:8448
rt v[206:207],v240 offset:9472
mm v[60:63],v[208:211],a[12:15],v[60:63]
ld a[244:247],v229,s[28:31],s93 offen offset:2048
mm v[60:63],v[212:215],a[4:7],v[60:63]
rt v[208:209],v240 offset:16896
rt v[210:211],v240 offset:17920
mm v[60:63],v[220:223],a[20:23],v[60:63]
ld v[156:159],v225,s[48:51],0 offen offset:512
mm v[60:63],v[216:219],a[28:31],v[60:63]
rt v[212:213],v240 offset:25344
rt v[214:215],v240 offset:26368
mm v[28:31],v[216:219],a[24:27],v[28:31]
wt vmcnt(24) lgkmcnt(4)
s_mov_b32 s93, 12288
ld a[248:251],v229,s[28:31],s93 offen offset:0
mm v[28:31],v[220:223],a[16:19],v[28:31]
rt v[216:217],v240 offset:33792
rt v[218:219],v240 offset:34816
mm v[64:67],v[192:195],a[32:35],v[64:67]
cv v234,v56,v57
cv v236,v60,v61
mm v[64:67],v[196:199],a[48:51],v[64:67]
rt v[220:221],v240 offset:42240
rt v[222:223],v240 offset:43264
mm v[96:99],v[196:199],a[52:55],v[96:99]
cv v235,v58,v59
cv v237,v62,v63
mm v[96:99],v[192:195],a[44:47],v[96:99]
rt v[196:197],v241 offset:50688
rt v[198:199],v241 offset:51712
mm v[96:99],v[200:203],a[36:39],v[96:99]
v_permlane16_swap_b32 v234, v236
v_permlane16_swap_b32 v235, v237
mm v[96:99],v[204:207],a[60:63],v[96:99]
rt v[192:193],v241 offset:59136
rt v[194:195],v241 offset:60160
mm v[64:67],v[204:207],a[40:43],v[64:67]
wt vmcnt(17) lgkmcnt(4)
ld a[252:255],v229,s[28:31],s93 offen offset:2048
mm v[64:67],v[200:203],a[56:59],v[64:67]
rt v[204:205],v241 offset:0
rt v[206:207],v241 offset:1024
mm v[64:67],v[208:211],v[128:131],v[64:67]
st v[234:237],v238,s[32:35],s95 offen offset:3072 sc1
mm v[64:67],v[212:215],v[144:147],v[64:67]
rt v[200:201],v241 offset:8448
rt v[202:203],v241 offset:9472
mm v[96:99],v[212:215],v[148:151],v[96:99]
cv v230,v24,v25
cv v232,v28,v29
mm v[96:99],v[208:211],v[132:135],v[96:99]
rt v[212:213],v241 offset:16896
rt v[214:215],v241 offset:17920
mm v[96:99],v[216:219],v[164:167],v[96:99]
cv v231,v26,v27
cv v233,v30,v31
mm v[96:99],v[220:223],v[180:183],v[96:99]
rt v[208:209],v241 offset:25344
rt v[210:211],v241 offset:26368
mm v[64:67],v[220:223],v[176:179],v[64:67]
wt lgkmcnt(4)
s_mov_b32 s93, 0
ld a[64:67],v229,s[28:31],s93 offen offset:256
mm v[64:67],v[216:219],v[160:163],v[64:67]
rt v[220:221],v241 offset:33792
rt v[222:223],v241 offset:34816
mm v[68:71],v[196:199],a[32:35],v[68:71]
v_permlane16_swap_b32 v230, v232
v_permlane16_swap_b32 v231, v233
mm v[68:71],v[192:195],a[48:51],v[68:71]
rt v[216:217],v241 offset:42240
rt v[218:219],v241 offset:43264
mm v[100:103],v[192:195],a[52:55],v[100:103]
ld a[68:71],v229,s[28:31],s93 offen offset:2304
mm v[100:103],v[196:199],a[44:47],v[100:103]
rt v[192:193],v242 offset:50688
rt v[194:195],v242 offset:51712
mm v[100:103],v[204:207],a[36:39],v[100:103]
st v[230:233],v238,s[32:35],s94 offen offset:3072 sc1
mm v[100:103],v[200:203],a[60:63],v[100:103]
rt v[196:197],v242 offset:59136
rt v[198:199],v242 offset:60160
mm v[68:71],v[200:203],a[40:43],v[68:71]
wt lgkmcnt(4)
)ASM" R"ASM(s_mov_b32 s93, 4096
ld a[72:75],v229,s[28:31],s93 offen offset:256
mm v[68:71],v[204:207],a[56:59],v[68:71]
rt v[200:201],v242 offset:0
rt v[202:203],v242 offset:1024
mm v[68:71],v[212:215],v[128:131],v[68:71]
ld v[168:171],v224,s[68:71],0 offen offset:1024
mm v[68:71],v[208:211],v[144:147],v[68:71]
rt v[204:205],v242 offset:8448
rt v[206:207],v242 offset:9472
mm v[100:103],v[208:211],v[148:151],v[100:103]
ld a[76:79],v229,s[28:31],s93 offen offset:2304
mm v[100:103],v[212:215],v[132:135],v[100:103]
rt v[208:209],v242 offset:16896
rt v[210:211],v242 offset:17920
mm v[100:103],v[220:223],v[164:167],v[100:103]
ld v[172:175],v225,s[48:51],0 offen offset:1024
mm v[100:103],v[216:219],v[180:183],v[100:103]
rt v[212:213],v242 offset:25344
rt v[214:215],v242 offset:26368
mm v[68:71],v[216:219],v[176:179],v[68:71]
wt lgkmcnt(4)
s_mov_b32 s93, 8192
ld a[80:83],v229,s[28:31],s93 offen offset:256
mm v[68:71],v[220:223],v[160:163],v[68:71]
rt v[216:217],v242 offset:33792
rt v[218:219],v242 offset:34816
mm v[72:75],v[192:195],a[32:35],v[72:75]
cv v234,v96,v97
cv v236,v100,v101
mm v[72:75],v[196:199],a[48:51],v[72:75]
rt v[220:221],v242 offset:42240
rt v[222:223],v242 offset:43264
mm v[104:107],v[196:199],a[52:55],v[104:107]
cv v235,v98,v99
cv v237,v102,v103
mm v[104:107],v[192:195],a[44:47],v[104:107]
rt v[196:197],v243 offset:50688
rt v[198:199],v243 offset:51712
mm v[104:107],v[200:203],a[36:39],v[104:107]
v_permlane16_swap_b32 v234, v236
v_permlane16_swap_b32 v235, v237
mm v[104:107],v[204:207],a[60:63],v[104:107]
rt v[192:193],v243 offset:59136
rt v[194:195],v243 offset:60160
mm v[72:75],v[204:207],a[40:43],v[72:75]
wt lgkmcnt(4)
ld a[84:87],v229,s[28:31],s93 offen offset:2304
mm v[72:75],v[200:203],a[56:59],v[72:75]
rt v[204:205],v243 offset:0
rt v[206:207],v243 offset:1024
mm v[72:75],v[208:211],v[128:131],v[72:75]
st v[234:237],v238,s[32:35],s97 offen offset:0 sc1
mm v[72:75],v[212:215],v[144:147],v[72:75]
rt v[200:201],v243 offset:8448
rt v[202:203],v243 offset:9472
mm v[104:107],v[212:215],v[148:151],v[104:107]
cv v230,v64,v65
cv v232,v68,v69
mm v[104:107],v[208:211],v[132:135],v[104:107]
rt v[212:213],v243 offset:16896
rt v[214:215],v243 offset:17920
mm v[104:107],v[216:219],v[164:167],v[104:107]
cv v231,v66,v67
cv v233,v70,v71
mm v[104:107],v[220:223],v[180:183],v[104:107]
rt v[208:209],v243 offset:25344
rt v[210:211],v243 offset:26368
mm v[72:75],v[220:223],v[176:179],v[72:75]
wt lgkmcnt(4)
s_mov_b32 s93, 12288
ld a[88:91],v229,s[28:31],s93 offen offset:256
mm v[72:75],v[216:219],v[160:163],v[72:75]
rt v[220:221],v243 offset:33792
rt v[222:223],v243 offset:34816
mm v[76:79],v[196:199],a[32:35],v[76:79]
v_permlane16_swap_b32 v230, v232
v_permlane16_swap_b32 v231, v233
mm v[76:79],v[192:195],a[48:51],v[76:79]
rt v[216:217],v243 offset:42240
rt v[218:219],v243 offset:43264
mm v[108:111],v[192:195],a[52:55],v[108:111]
ld a[92:95],v229,s[28:31],s93 offen offset:2304
mm v[108:111],v[196:199],a[44:47],v[108:111]
rt v[192:193],v240 offset:50816
rt v[194:195],v240 offset:51840
mm v[108:111],v[204:207],a[36:39],v[108:111]
st v[230:233],v238,s[32:35],s96 offen offset:0 sc1
mm v[108:111],v[200:203],a[60:63],v[108:111]
rt v[196:197],v240 offset:59264
rt v[198:199],v240 offset:60288
mm v[76:79],v[200:203],a[40:43],v[76:79]
wt lgkmcnt(4)
mm v[76:79],v[204:207],a[56:59],v[76:79]
rt v[200:201],v240 offset:128
rt v[202:203],v240 offset:1152
mm v[76:79],v[212:215],v[128:131],v[76:79]
ld v[184:187],v224,s[68:71],0 offen offset:1536
mm v[76:79],v[208:211],v[144:147],v[76:79]
rt v[204:205],v240 offset:8576
)ASM" R"ASM(rt v[206:207],v240 offset:9600
mm v[108:111],v[208:211],v[148:151],v[108:111]
mm v[108:111],v[212:215],v[132:135],v[108:111]
rt v[208:209],v240 offset:17024
rt v[210:211],v240 offset:18048
mm v[108:111],v[220:223],v[164:167],v[108:111]
ld v[188:191],v225,s[48:51],0 offen offset:1536
mm v[108:111],v[216:219],v[180:183],v[108:111]
rt v[212:213],v240 offset:25472
rt v[214:215],v240 offset:26496
mm v[76:79],v[216:219],v[176:179],v[76:79]
wt lgkmcnt(4)
mm v[76:79],v[220:223],v[160:163],v[76:79]
rt v[216:217],v240 offset:33920
rt v[218:219],v240 offset:34944
mm v[80:83],v[192:195],a[32:35],v[80:83]
cv v234,v104,v105
cv v236,v108,v109
mm v[80:83],v[196:199],a[48:51],v[80:83]
rt v[220:221],v240 offset:42368
rt v[222:223],v240 offset:43392
mm v[112:115],v[196:199],a[52:55],v[112:115]
cv v235,v106,v107
cv v237,v110,v111
mm v[112:115],v[192:195],a[44:47],v[112:115]
rt v[196:197],v241 offset:50816
rt v[198:199],v241 offset:51840
mm v[112:115],v[200:203],a[36:39],v[112:115]
v_permlane16_swap_b32 v234, v236
v_permlane16_swap_b32 v235, v237
mm v[112:115],v[204:207],a[60:63],v[112:115]
rt v[192:193],v241 offset:59264
rt v[194:195],v241 offset:60288
mm v[80:83],v[204:207],a[40:43],v[80:83]
wt lgkmcnt(4)
mm v[80:83],v[200:203],a[56:59],v[80:83]
rt v[204:205],v241 offset:128
rt v[206:207],v241 offset:1152
mm v[80:83],v[208:211],v[128:131],v[80:83]
st v[234:237],v238,s[32:35],s97 offen offset:1024 sc1
mm v[80:83],v[212:215],v[144:147],v[80:83]
rt v[200:201],v241 offset:8576
rt v[202:203],v241 offset:9600
mm v[112:115],v[212:215],v[148:151],v[112:115]
cv v230,v72,v73
cv v232,v76,v77
mm v[112:115],v[208:211],v[132:135],v[112:115]
rt v[212:213],v241 offset:17024
rt v[214:215],v241 offset:18048
mm v[112:115],v[216:219],v[164:167],v[112:115]
cv v231,v74,v75
cv v233,v78,v79
mm v[112:115],v[220:223],v[180:183],v[112:115]
rt v[208:209],v241 offset:25472
rt v[210:211],v241 offset:26496
mm v[80:83],v[220:223],v[176:179],v[80:83]
wt lgkmcnt(4)
mm v[80:83],v[216:219],v[160:163],v[80:83]
rt v[220:221],v241 offset:33920
rt v[222:223],v241 offset:34944
mm v[84:87],v[196:199],a[32:35],v[84:87]
v_permlane16_swap_b32 v230, v232
v_permlane16_swap_b32 v231, v233
mm v[84:87],v[192:195],a[48:51],v[84:87]
rt v[216:217],v241 offset:42368
rt v[218:219],v241 offset:43392
mm v[116:119],v[192:195],a[52:55],v[116:119]
mm v[116:119],v[196:199],a[44:47],v[116:119]
rt v[192:193],v242 offset:50816
rt v[194:195],v242 offset:51840
mm v[116:119],v[204:207],a[36:39],v[116:119]
st v[230:233],v238,s[32:35],s96 offen offset:1024 sc1
mm v[116:119],v[200:203],a[60:63],v[116:119]
rt v[196:197],v242 offset:59264
rt v[198:199],v242 offset:60288
mm v[84:87],v[200:203],a[40:43],v[84:87]
wt lgkmcnt(4)
mm v[84:87],v[204:207],a[56:59],v[84:87]
rt v[200:201],v242 offset:128
rt v[202:203],v242 offset:1152
mm v[84:87],v[212:215],v[128:131],v[84:87]
mm v[84:87],v[208:211],v[144:147],v[84:87]
rt v[204:205],v242 offset:8576
rt v[206:207],v242 offset:9600
mm v[116:119],v[208:211],v[148:151],v[116:119]
mm v[116:119],v[212:215],v[132:135],v[116:119]
rt v[208:209],v242 offset:17024
rt v[210:211],v242 offset:18048
mm v[116:119],v[220:223],v[164:167],v[116:119]
mm v[116:119],v[216:219],v[180:183],v[116:119]
rt v[212:213],v242 offset:25472
rt v[214:215],v242 offset:26496
mm v[84:87],v[216:219],v[176:179],v[84:87]
wt lgkmcnt(4)
mm v[84:87],v[220:223],v[160:163],v[84:87]
rt v[216:217],v242 offset:33920
rt v[218:219],v242 offset:34944
mm v[88:91],v[192:195],a[32:35],v[88:91]
cv v234,v112,v113
cv v236,v116,v117
)ASM" R"ASM(mm v[88:91],v[196:199],a[48:51],v[88:91]
rt v[220:221],v242 offset:42368
rt v[222:223],v242 offset:43392
mm v[120:123],v[196:199],a[52:55],v[120:123]
cv v235,v114,v115
cv v237,v118,v119
mm v[120:123],v[192:195],a[44:47],v[120:123]
rt v[196:197],v243 offset:50816
rt v[198:199],v243 offset:51840
mm v[120:123],v[200:203],a[36:39],v[120:123]
v_permlane16_swap_b32 v234, v236
v_permlane16_swap_b32 v235, v237
mm v[120:123],v[204:207],a[60:63],v[120:123]
rt v[192:193],v243 offset:59264
rt v[194:195],v243 offset:60288
mm v[88:91],v[204:207],a[40:43],v[88:91]
wt lgkmcnt(4)
mm v[88:91],v[200:203],a[56:59],v[88:91]
rt v[204:205],v243 offset:128
rt v[206:207],v243 offset:1152
mm v[88:91],v[208:211],v[128:131],v[88:91]
st v[234:237],v238,s[32:35],s97 offen offset:2048 sc1
mm v[88:91],v[212:215],v[144:147],v[88:91]
rt v[200:201],v243 offset:8576
rt v[202:203],v243 offset:9600
mm v[120:123],v[212:215],v[148:151],v[120:123]
cv v230,v80,v81
cv v232,v84,v85
mm v[120:123],v[208:211],v[132:135],v[120:123]
rt v[212:213],v243 offset:17024
rt v[214:215],v243 offset:18048
mm v[120:123],v[216:219],v[164:167],v[120:123]
cv v231,v82,v83
cv v233,v86,v87
wt vmcnt(15)
buffer_store_dwordx2 v[252:253], v251, s[64:67], s92 offen sc1
mm v[120:123],v[220:223],v[180:183],v[120:123]
rt v[208:209],v243 offset:25472
rt v[210:211],v243 offset:26496
mm v[88:91],v[220:223],v[176:179],v[88:91]
wt lgkmcnt(4)
mm v[88:91],v[216:219],v[160:163],v[88:91]
rt v[220:221],v243 offset:33920
rt v[222:223],v243 offset:34944
mm v[92:95],v[196:199],a[32:35],v[92:95]
v_permlane16_swap_b32 v230, v232
v_permlane16_swap_b32 v231, v233
mm v[92:95],v[192:195],a[48:51],v[92:95]
rt v[216:217],v243 offset:42368
rt v[218:219],v243 offset:43392
mm v[124:127],v[192:195],a[52:55],v[124:127]
mm v[124:127],v[196:199],a[44:47],v[124:127]
mm v[124:127],v[204:207],a[36:39],v[124:127]
st v[230:233],v238,s[32:35],s96 offen offset:2048 sc1
mm v[124:127],v[200:203],a[60:63],v[124:127]
mm v[92:95],v[200:203],a[40:43],v[92:95]
wt lgkmcnt(0)
mm v[92:95],v[204:207],a[56:59],v[92:95]
mm v[92:95],v[212:215],v[128:131],v[92:95]
mm v[92:95],v[208:211],v[144:147],v[92:95]
mm v[124:127],v[208:211],v[148:151],v[124:127]
mm v[124:127],v[212:215],v[132:135],v[124:127]
mm v[124:127],v[220:223],v[164:167],v[124:127]
mm v[124:127],v[216:219],v[180:183],v[124:127]
mm v[92:95],v[216:219],v[176:179],v[92:95]
mm v[92:95],v[220:223],v[160:163],v[92:95]
s_nop 7
cv v234,v120,v121
cv v236,v124,v125
cv v235,v122,v123
cv v237,v126,v127
cv v230,v88,v89
cv v232,v92,v93
cv v231,v90,v91
cv v233,v94,v95
v_permlane16_swap_b32 v234, v236
v_permlane16_swap_b32 v235, v237
st v[234:237],v238,s[32:35],s97 offen offset:3072 sc1
v_permlane16_swap_b32 v230, v232
v_permlane16_swap_b32 v231, v233
st v[230:233],v238,s[32:35],s96 offen offset:3072 sc1
s_branch .La5_LT_0
; ---- O projection, odd XCDs: seven and a half heads
.La5_LO_SC:
s_nop 0
.La5_LW7_0:
s_cbranch_execz .La5_LW7_4
mm v[64:67],v[192:195],v[160:163],0
buffer_load_dwordx2 v[250:251], v248, s[64:67], s5 offen sc1
mm v[64:67],v[196:199],v[164:167],v[64:67]
rt v[220:221],v243 offset:25344
rt v[222:223],v243 offset:26368
mm v[96:99],v[196:199],v[180:183],0
s_add_u32 m0, s83, 0
s_bfe_u32 s80, s4, 262148
mm v[96:99],v[192:195],v[176:179],v[96:99]
rt v[196:197],v240 offset:17024
rt v[198:199],v240 offset:18048
s_xor_b32 s80, s80, s72
s_lshl_b32 s80, s80, 18
)ASM" R"ASM(s_add_u32 s80, s80, 131072
mm v[100:103],v[200:203],v[176:179],0
ld v224,s[56:59],s80 offen lds
mm v[100:103],v[204:207],v[180:183],v[100:103]
rt v[192:193],v240 offset:25472
rt v[194:195],v240 offset:26496
mm v[68:71],v[204:207],v[164:167],0
s_add_u32 m0, s83, 1024
wt lgkmcnt(4)
mm v[68:71],v[200:203],v[160:163],v[68:71]
rt v[204:205],v241 offset:17024
rt v[206:207],v241 offset:18048
mm v[72:75],v[208:211],v[160:163],0
ld v225,s[56:59],s80 offen lds
mm v[72:75],v[212:215],v[164:167],v[72:75]
rt v[200:201],v241 offset:25472
rt v[202:203],v241 offset:26496
mm v[104:107],v[212:215],v[180:183],0
s_add_u32 m0, s83, 2176
s_bfe_u32 s53, s4, 262148
mm v[104:107],v[208:211],v[176:179],v[104:107]
rt v[212:213],v242 offset:17024
rt v[214:215],v242 offset:18048
mm v[108:111],v[216:219],v[176:179],0
ld v226,s[56:59],s80 offen lds
mm v[108:111],v[220:223],v[180:183],v[108:111]
rt v[208:209],v242 offset:25472
rt v[210:211],v242 offset:26496
mm v[76:79],v[220:223],v[164:167],0
s_add_u32 m0, s83, 3200
wt lgkmcnt(4)
mm v[76:79],v[216:219],v[160:163],v[76:79]
rt v[220:221],v243 offset:17024
rt v[222:223],v243 offset:18048
mm v[80:83],v[196:199],v[160:163],0
ld v227,s[56:59],s80 offen lds
mm v[80:83],v[192:195],v[164:167],v[80:83]
rt v[216:217],v243 offset:25472
rt v[218:219],v243 offset:26496
mm v[112:115],v[192:195],v[180:183],0
s_add_u32 m0, s83, 118272
s_bfe_u32 s80, s4, 262152
mm v[112:115],v[196:199],v[176:179],v[112:115]
rt v[192:193],v240 offset:33792
rt v[194:195],v240 offset:34816
s_xor_b32 s80, s80, s72
s_lshl_b32 s80, s80, 18
mm v[116:119],v[204:207],v[176:179],0
ld v224,s[56:59],s80 offen lds
mm v[116:119],v[200:203],v[180:183],v[116:119]
rt v[196:197],v240 offset:42240
rt v[198:199],v240 offset:43264
mm v[84:87],v[200:203],v[164:167],0
s_add_u32 m0, s83, 119296
wt lgkmcnt(4)
mm v[84:87],v[204:207],v[160:163],v[84:87]
rt v[200:201],v241 offset:33792
rt v[202:203],v241 offset:34816
mm v[88:91],v[212:215],v[160:163],0
ld v225,s[56:59],s80 offen lds
mm v[88:91],v[208:211],v[164:167],v[88:91]
rt v[204:205],v241 offset:42240
rt v[206:207],v241 offset:43264
mm v[120:123],v[208:211],v[180:183],0
s_add_u32 m0, s83, 120448
s_xor_b32 s53, s53, s72
mm v[120:123],v[212:215],v[176:179],v[120:123]
rt v[208:209],v242 offset:33792
rt v[210:211],v242 offset:34816
mm v[124:127],v[220:223],v[176:179],0
ld v226,s[56:59],s80 offen lds
mm v[124:127],v[216:219],v[180:183],v[124:127]
rt v[212:213],v242 offset:42240
rt v[214:215],v242 offset:43264
mm v[92:95],v[216:219],v[164:167],0
wt vmcnt(7)
v_cmp_eq_u64 vcc, v[250:251], v[252:253]
s_nop 0
s_bitcmp1_b32 vcc_lo, 1
s_cbranch_scc0 .La5_LO_NC1
.La5_LO_KC1:
wt lgkmcnt(4)
mm v[92:95],v[220:223],v[160:163],v[92:95]
rt v[216:217],v243 offset:33792
rt v[218:219],v243 offset:34816
s_lshl_b32 s53, s53, 13
mm v[64:67],v[192:195],v[168:171],v[64:67]
ld a[0:3],v244,s[44:47],s53 offen offset:0 sc1
mm v[64:67],v[196:199],v[172:175],v[64:67]
rt v[220:221],v243 offset:42240
rt v[222:223],v243 offset:43264
mm v[96:99],v[196:199],v[188:191],v[96:99]
ld a[4:7],v245,s[44:47],s53 offen offset:0 sc1
mm v[96:99],v[192:195],v[184:187],v[96:99]
rt v[196:197],v240 offset:33920
rt v[198:199],v240 offset:34944
mm v[100:103],v[200:203],v[184:187],v[100:103]
ld a[8:11],v246,s[44:47],s53 offen offset:0 sc1
mm v[100:103],v[204:207],v[188:191],v[100:103]
rt v[192:193],v240 offset:42368
)ASM" R"ASM(rt v[194:195],v240 offset:43392
mm v[68:71],v[204:207],v[172:175],v[68:71]
wt lgkmcnt(4)
s_bfe_u32 s54, s4, 262152
mm v[68:71],v[200:203],v[168:171],v[68:71]
rt v[204:205],v241 offset:33920
rt v[206:207],v241 offset:34944
mm v[72:75],v[208:211],v[168:171],v[72:75]
ld a[12:15],v247,s[44:47],s53 offen offset:0 sc1
mm v[72:75],v[212:215],v[172:175],v[72:75]
rt v[200:201],v241 offset:42368
rt v[202:203],v241 offset:43392
mm v[104:107],v[212:215],v[188:191],v[104:107]
ld a[16:19],v244,s[44:47],s53 offen offset:1024 sc1
mm v[104:107],v[208:211],v[184:187],v[104:107]
rt v[212:213],v242 offset:33920
rt v[214:215],v242 offset:34944
mm v[108:111],v[216:219],v[184:187],v[108:111]
ld a[20:23],v245,s[44:47],s53 offen offset:1024 sc1
mm v[108:111],v[220:223],v[188:191],v[108:111]
.La5_LW7_1:
s_cbranch_execz .La5_LW7_5
rt v[208:209],v242 offset:42368
rt v[210:211],v242 offset:43392
mm v[76:79],v[220:223],v[172:175],v[76:79]
s_nop 0
s_barrier
wt lgkmcnt(4)
mm v[76:79],v[216:219],v[168:171],v[76:79]
rt v[220:221],v243 offset:33920
rt v[222:223],v243 offset:34944
mm v[80:83],v[196:199],v[168:171],v[80:83]
ld a[24:27],v246,s[44:47],s53 offen offset:1024 sc1
mm v[80:83],v[192:195],v[172:175],v[80:83]
rt v[216:217],v243 offset:42368
rt v[218:219],v243 offset:43392
mm v[112:115],v[192:195],v[188:191],v[112:115]
ld a[28:31],v247,s[44:47],s53 offen offset:1024 sc1
mm v[112:115],v[196:199],v[184:187],v[112:115]
rt v[192:193],v240 offset:50688
rt v[194:195],v240 offset:51712
mm v[116:119],v[204:207],v[184:187],v[116:119]
ld a[32:35],v244,s[44:47],s53 offen offset:2048 sc1
mm v[116:119],v[200:203],v[188:191],v[116:119]
rt v[196:197],v240 offset:59136
rt v[198:199],v240 offset:60160
mm v[84:87],v[200:203],v[172:175],v[84:87]
wt lgkmcnt(4)
s_xor_b32 s54, s54, s72
mm v[84:87],v[204:207],v[168:171],v[84:87]
rt v[200:201],v241 offset:50688
rt v[202:203],v241 offset:51712
mm v[88:91],v[212:215],v[168:171],v[88:91]
ld a[36:39],v245,s[44:47],s53 offen offset:2048 sc1
mm v[88:91],v[208:211],v[172:175],v[88:91]
rt v[204:205],v241 offset:59136
rt v[206:207],v241 offset:60160
mm v[120:123],v[208:211],v[188:191],v[120:123]
ld a[40:43],v246,s[44:47],s53 offen offset:2048 sc1
mm v[120:123],v[212:215],v[184:187],v[120:123]
rt v[208:209],v242 offset:50688
rt v[210:211],v242 offset:51712
mm v[124:127],v[220:223],v[184:187],v[124:127]
ld a[44:47],v247,s[44:47],s53 offen offset:2048 sc1
mm v[124:127],v[216:219],v[188:191],v[124:127]
rt v[212:213],v242 offset:59136
rt v[214:215],v242 offset:60160
mm v[92:95],v[216:219],v[172:175],v[92:95]
wt vmcnt(4) lgkmcnt(4)
s_lshl_b32 s54, s54, 13
mm v[92:95],v[220:223],v[168:171],v[92:95]
rt v[216:217],v243 offset:50688
rt v[218:219],v243 offset:51712
mm v[0:3],v[192:195],a[0:3],v[0:3]
s_bfe_u32 s55, s4, 262156
s_xor_b32 s55, s55, s72
mm v[0:3],v[196:199],a[16:19],v[0:3]
ld a[48:51],v244,s[44:47],s53 offen offset:3072 sc1
mm v[32:35],v[196:199],a[20:23],v[32:35]
s_lshl_b32 s55, s55, 13
s_bfe_u32 s52, s4, 262160
mm v[32:35],v[192:195],a[4:7],v[32:35]
rt v[220:221],v243 offset:59136
rt v[222:223],v243 offset:60160
mm v[64:67],v[192:195],a[8:11],v[64:67]
s_xor_b32 s52, s52, s72
s_lshl_b32 s52, s52, 13
mm v[64:67],v[196:199],a[24:27],v[64:67]
ld a[52:55],v245,s[44:47],s53 offen offset:3072 sc1
mm v[96:99],v[196:199],a[28:31],v[96:99]
mm v[96:99],v[192:195],a[12:15],v[96:99]
rt v[196:197],v240 offset:50816
rt v[198:199],v240 offset:51840
mm v[100:103],v[200:203],a[12:15],v[100:103]
mm v[100:103],v[204:207],a[28:31],v[100:103]
buffer_load_dwordx2 v[250:251], v248, s[64:67], s5 offen sc1
mm v[68:71],v[204:207],a[24:27],v[68:71]
mm v[68:71],v[200:203],a[8:11],v[68:71]
rt v[192:193],v240 offset:59264
rt v[194:195],v240 offset:60288
)ASM" R"ASM(mm v[36:39],v[200:203],a[4:7],v[36:39]
mm v[36:39],v[204:207],a[20:23],v[36:39]
ld a[56:59],v246,s[44:47],s53 offen offset:3072 sc1
mm v[4:7],v[204:207],a[16:19],v[4:7]
wt lgkmcnt(4)
mm v[4:7],v[200:203],a[0:3],v[4:7]
rt v[204:205],v241 offset:50816
rt v[206:207],v241 offset:51840
mm v[8:11],v[208:211],a[0:3],v[8:11]
mm v[8:11],v[212:215],a[16:19],v[8:11]
ld a[60:63],v247,s[44:47],s53 offen offset:3072 sc1
mm v[40:43],v[212:215],a[20:23],v[40:43]
s_bfe_u32 s53, s4, 262164
s_xor_b32 s53, s53, s72
mm v[40:43],v[208:211],a[4:7],v[40:43]
rt v[200:201],v241 offset:59264
rt v[202:203],v241 offset:60288
mm v[72:75],v[208:211],a[8:11],v[72:75]
s_add_u32 m0, s83, 121472
s_lshl_b32 s53, s53, 13
mm v[72:75],v[212:215],a[24:27],v[72:75]
ld v227,s[56:59],s80 offen lds
mm v[104:107],v[212:215],a[28:31],v[104:107]
s_add_u32 s80, s80, 131072
mm v[104:107],v[208:211],a[12:15],v[104:107]
rt v[212:213],v242 offset:50816
rt v[214:215],v242 offset:51840
mm v[108:111],v[216:219],a[12:15],v[108:111]
s_add_u32 m0, s83, 67584
mm v[108:111],v[220:223],a[28:31],v[108:111]
ld v224,s[56:59],s80 offen lds
mm v[76:79],v[220:223],a[24:27],v[76:79]
mm v[76:79],v[216:219],a[8:11],v[76:79]
rt v[208:209],v242 offset:59264
rt v[210:211],v242 offset:60288
mm v[44:47],v[216:219],a[4:7],v[44:47]
s_add_u32 m0, s83, 68608
mm v[44:47],v[220:223],a[20:23],v[44:47]
ld v225,s[56:59],s80 offen lds
mm v[12:15],v[220:223],a[16:19],v[12:15]
wt lgkmcnt(4)
mm v[12:15],v[216:219],a[0:3],v[12:15]
rt v[220:221],v243 offset:50816
rt v[222:223],v243 offset:51840
mm v[16:19],v[196:199],a[0:3],v[16:19]
s_add_u32 m0, s83, 69760
mm v[16:19],v[192:195],a[16:19],v[16:19]
ld v226,s[56:59],s80 offen lds
mm v[48:51],v[192:195],a[20:23],v[48:51]
mm v[48:51],v[196:199],a[4:7],v[48:51]
rt v[216:217],v243 offset:59264
rt v[218:219],v243 offset:60288
mm v[80:83],v[196:199],a[8:11],v[80:83]
s_add_u32 m0, s83, 70784
s_nop 0
s_barrier
mm v[80:83],v[192:195],a[24:27],v[80:83]
ld v227,s[56:59],s80 offen lds
mm v[112:115],v[192:195],a[28:31],v[112:115]
s_bfe_u32 s80, s4, 262156
s_xor_b32 s80, s80, s72
mm v[112:115],v[196:199],a[12:15],v[112:115]
rt v[192:193],v240 offset:0
rt v[194:195],v240 offset:1024
mm v[116:119],v[204:207],a[12:15],v[116:119]
s_add_u32 m0, s83, 84480
s_lshl_b32 s80, s80, 18
mm v[116:119],v[200:203],a[28:31],v[116:119]
ld v224,s[56:59],s80 offen lds
mm v[84:87],v[200:203],a[24:27],v[84:87]
mm v[84:87],v[204:207],a[8:11],v[84:87]
rt v[196:197],v240 offset:8448
rt v[198:199],v240 offset:9472
mm v[52:55],v[204:207],a[4:7],v[52:55]
s_add_u32 m0, s83, 85504
mm v[52:55],v[200:203],a[20:23],v[52:55]
ld v225,s[56:59],s80 offen lds
mm v[20:23],v[200:203],a[16:19],v[20:23]
wt lgkmcnt(4)
mm v[20:23],v[204:207],a[0:3],v[20:23]
rt v[200:201],v241 offset:0
rt v[202:203],v241 offset:1024
mm v[24:27],v[212:215],a[0:3],v[24:27]
s_add_u32 m0, s83, 86656
mm v[24:27],v[208:211],a[16:19],v[24:27]
ld v226,s[56:59],s80 offen lds
mm v[56:59],v[208:211],a[20:23],v[56:59]
wt vmcnt(10)
v_cmp_eq_u64 vcc, v[250:251], v[252:253]
mm v[56:59],v[212:215],a[4:7],v[56:59]
rt v[204:205],v241 offset:8448
rt v[206:207],v241 offset:9472
mm v[88:91],v[212:215],a[8:11],v[88:91]
s_add_u32 m0, s83, 87680
mm v[88:91],v[208:211],a[24:27],v[88:91]
ld v227,s[56:59],s80 offen lds
mm v[120:123],v[208:211],a[28:31],v[120:123]
s_bitcmp1_b32 vcc_lo, 2
s_cbranch_scc0 .La5_LO_NC2
.La5_LO_KC2:
)ASM" R"ASM(mm v[120:123],v[212:215],a[12:15],v[120:123]
rt v[208:209],v242 offset:0
rt v[210:211],v242 offset:1024
mm v[124:127],v[220:223],a[12:15],v[124:127]
s_add_u32 s80, s80, 131072
mm v[124:127],v[216:219],a[28:31],v[124:127]
ld a[12:15],v244,s[44:47],s54 offen offset:0 sc1
mm v[92:95],v[216:219],a[24:27],v[92:95]
mm v[92:95],v[220:223],a[8:11],v[92:95]
rt v[212:213],v242 offset:8448
rt v[214:215],v242 offset:9472
mm v[60:63],v[220:223],a[4:7],v[60:63]
mm v[60:63],v[216:219],a[20:23],v[60:63]
mm v[28:31],v[216:219],a[16:19],v[28:31]
wt vmcnt(10) lgkmcnt(4)
mm v[28:31],v[220:223],a[0:3],v[28:31]
rt v[216:217],v243 offset:0
rt v[218:219],v243 offset:1024
mm v[0:3],v[192:195],a[32:35],v[0:3]
mm v[0:3],v[196:199],a[48:51],v[0:3]
ld a[0:3],v245,s[44:47],s54 offen offset:0 sc1
mm v[32:35],v[196:199],a[52:55],v[32:35]
mm v[32:35],v[192:195],a[36:39],v[32:35]
rt v[220:221],v243 offset:8448
rt v[222:223],v243 offset:9472
mm v[64:67],v[192:195],a[40:43],v[64:67]
mm v[64:67],v[196:199],a[56:59],v[64:67]
ld a[4:7],v246,s[44:47],s54 offen offset:0 sc1
mm v[96:99],v[196:199],a[60:63],v[96:99]
mm v[96:99],v[192:195],a[44:47],v[96:99]
rt v[196:197],v240 offset:128
rt v[198:199],v240 offset:1152
mm v[100:103],v[200:203],a[44:47],v[100:103]
s_mov_b32 s86, 0x05040100
s_mov_b32 s87, 0x07060302
mm v[100:103],v[204:207],a[60:63],v[100:103]
ld a[8:11],v247,s[44:47],s54 offen offset:0 sc1
mm v[68:71],v[204:207],a[56:59],v[68:71]
s_nop 0
ar v230,a128
mm v[68:71],v[200:203],a[40:43],v[68:71]
rt v[192:193],v240 offset:8576
rt v[194:195],v240 offset:9600
mm v[36:39],v[200:203],a[36:39],v[36:39]
v_add_u32 v240, 67584, v240
ar v231,a132
mm v[36:39],v[204:207],a[52:55],v[36:39]
ld a[16:19],v244,s[44:47],s54 offen offset:1024 sc1
mm v[4:7],v[204:207],a[48:51],v[4:7]
wt lgkmcnt(4)
v_perm_b32 v232, v231, v230, s86
mm v[4:7],v[200:203],a[32:35],v[4:7]
rt v[204:205],v241 offset:128
rt v[206:207],v241 offset:1152
mm v[8:11],v[208:211],a[32:35],v[8:11]
v_perm_b32 v233, v231, v230, s87
aw a96,v232
mm v[8:11],v[212:215],a[48:51],v[8:11]
ld a[20:23],v245,s[44:47],s54 offen offset:1024 sc1
mm v[40:43],v[212:215],a[52:55],v[40:43]
aw a100,v233
ar v234,a136
mm v[40:43],v[208:211],a[36:39],v[40:43]
rt v[200:201],v241 offset:8576
rt v[202:203],v241 offset:9600
mm v[72:75],v[208:211],a[40:43],v[72:75]
v_add_u32 v241, 67584, v241
ar v235,a140
mm v[72:75],v[212:215],a[56:59],v[72:75]
ld a[24:27],v246,s[44:47],s54 offen offset:1024 sc1
mm v[104:107],v[212:215],a[60:63],v[104:107]
v_perm_b32 v236, v235, v234, s86
v_perm_b32 v237, v235, v234, s87
mm v[104:107],v[208:211],a[44:47],v[104:107]
rt v[212:213],v242 offset:128
rt v[214:215],v242 offset:1152
mm v[108:111],v[216:219],a[44:47],v[108:111]
aw a97,v236
aw a101,v237
mm v[108:111],v[220:223],a[60:63],v[108:111]
ld a[28:31],v247,s[44:47],s54 offen offset:1024 sc1
mm v[76:79],v[220:223],a[56:59],v[76:79]
ar v230,a144
ar v231,a148
mm v[76:79],v[216:219],a[40:43],v[76:79]
rt v[208:209],v242 offset:8576
rt v[210:211],v242 offset:9600
mm v[44:47],v[216:219],a[36:39],v[44:47]
v_add_u32 v242, 67584, v242
v_perm_b32 v232, v231, v230, s86
mm v[44:47],v[220:223],a[52:55],v[44:47]
v_perm_b32 v233, v231, v230, s87
aw a98,v232
mm v[12:15],v[220:223],a[48:51],v[12:15]
wt lgkmcnt(4)
aw a102,v233
mm v[12:15],v[216:219],a[32:35],v[12:15]
rt v[220:221],v243 offset:128
rt v[222:223],v243 offset:1152
mm v[16:19],v[196:199],a[32:35],v[16:19]
)ASM" R"ASM(s_add_u32 m0, s83, 101376
ar v234,a152
mm v[16:19],v[192:195],a[48:51],v[16:19]
ld v224,s[56:59],s80 offen lds
mm v[48:51],v[192:195],a[52:55],v[48:51]
ar v235,a156
v_perm_b32 v236, v235, v234, s86
mm v[48:51],v[196:199],a[36:39],v[48:51]
rt v[216:217],v243 offset:8576
rt v[218:219],v243 offset:9600
mm v[80:83],v[196:199],a[40:43],v[80:83]
s_add_u32 m0, s83, 102400
v_add_u32 v243, 67584, v243
mm v[80:83],v[192:195],a[56:59],v[80:83]
ld v225,s[56:59],s80 offen lds
mm v[112:115],v[192:195],a[60:63],v[112:115]
wt vmcnt(14)
s_barrier
mm v[112:115],v[196:199],a[44:47],v[112:115]
rt v[192:193],v240 offset:50688
rt v[194:195],v240 offset:51712
mm v[116:119],v[204:207],a[44:47],v[116:119]
s_add_u32 m0, s83, 103552
v_perm_b32 v237, v235, v234, s87
mm v[116:119],v[200:203],a[60:63],v[116:119]
ld v226,s[56:59],s80 offen lds
mm v[84:87],v[200:203],a[56:59],v[84:87]
aw a99,v236
aw a103,v237
mm v[84:87],v[204:207],a[40:43],v[84:87]
rt v[196:197],v240 offset:59136
rt v[198:199],v240 offset:60160
mm v[52:55],v[204:207],a[36:39],v[52:55]
ar v230,a129
ar v231,a133
mm v[52:55],v[200:203],a[52:55],v[52:55]
ld a[64:67],v244,s[44:47],s54 offen offset:2048 sc1
mm v[20:23],v[200:203],a[48:51],v[20:23]
wt lgkmcnt(4)
v_perm_b32 v232, v231, v230, s86
mm v[20:23],v[204:207],a[32:35],v[20:23]
rt v[200:201],v241 offset:50688
rt v[202:203],v241 offset:51712
mm v[24:27],v[212:215],a[32:35],v[24:27]
v_perm_b32 v233, v231, v230, s87
aw a104,v232
mm v[24:27],v[208:211],a[48:51],v[24:27]
ld a[68:71],v245,s[44:47],s54 offen offset:2048 sc1
mm v[56:59],v[208:211],a[52:55],v[56:59]
aw a108,v233
ar v234,a137
mm v[56:59],v[212:215],a[36:39],v[56:59]
rt v[204:205],v241 offset:59136
rt v[206:207],v241 offset:60160
mm v[88:91],v[212:215],a[40:43],v[88:91]
ar v235,a141
v_perm_b32 v236, v235, v234, s86
mm v[88:91],v[208:211],a[56:59],v[88:91]
ld a[72:75],v246,s[44:47],s54 offen offset:2048 sc1
mm v[120:123],v[208:211],a[60:63],v[120:123]
v_perm_b32 v237, v235, v234, s87
aw a105,v236
mm v[120:123],v[212:215],a[44:47],v[120:123]
rt v[208:209],v242 offset:50688
rt v[210:211],v242 offset:51712
mm v[124:127],v[220:223],a[44:47],v[124:127]
aw a109,v237
ar v230,a145
mm v[124:127],v[216:219],a[60:63],v[124:127]
buffer_load_dwordx2 v[250:251], v248, s[64:67], s5 offen sc1
mm v[92:95],v[216:219],a[56:59],v[92:95]
ar v231,a149
v_perm_b32 v232, v231, v230, s86
mm v[92:95],v[220:223],a[40:43],v[92:95]
rt v[212:213],v242 offset:59136
rt v[214:215],v242 offset:60160
mm v[60:63],v[220:223],a[36:39],v[60:63]
v_perm_b32 v233, v231, v230, s87
aw a106,v232
mm v[60:63],v[216:219],a[52:55],v[60:63]
ld a[36:39],v247,s[44:47],s54 offen offset:2048 sc1
mm v[28:31],v[216:219],a[48:51],v[28:31]
wt vmcnt(8) lgkmcnt(4)
aw a110,v233
mm v[28:31],v[220:223],a[32:35],v[28:31]
rt v[216:217],v243 offset:50688
rt v[218:219],v243 offset:51712
mm v[0:3],v[192:195],a[12:15],v[0:3]
ar v234,a153
ar v235,a157
mm v[0:3],v[196:199],a[16:19],v[0:3]
ld a[32:35],v244,s[44:47],s54 offen offset:3072 sc1
mm v[32:35],v[196:199],a[20:23],v[32:35]
v_perm_b32 v236, v235, v234, s86
v_perm_b32 v237, v235, v234, s87
mm v[32:35],v[192:195],a[0:3],v[32:35]
rt v[220:221],v243 offset:59136
rt v[222:223],v243 offset:60160
mm v[64:67],v[192:195],a[4:7],v[64:67]
aw a107,v236
)ASM" R"ASM(aw a111,v237
mm v[64:67],v[196:199],a[24:27],v[64:67]
ld a[40:43],v245,s[44:47],s54 offen offset:3072 sc1
mm v[96:99],v[196:199],a[28:31],v[96:99]
ar v230,a130
ar v231,a134
mm v[96:99],v[192:195],a[8:11],v[96:99]
rt v[196:197],v240 offset:50816
rt v[198:199],v240 offset:51840
mm v[100:103],v[200:203],a[8:11],v[100:103]
v_perm_b32 v232, v231, v230, s86
v_perm_b32 v233, v231, v230, s87
mm v[100:103],v[204:207],a[28:31],v[100:103]
ld a[44:47],v246,s[44:47],s54 offen offset:3072 sc1
mm v[68:71],v[204:207],a[24:27],v[68:71]
aw a112,v232
aw a116,v233
mm v[68:71],v[200:203],a[4:7],v[68:71]
rt v[192:193],v240 offset:59264
rt v[194:195],v240 offset:60288
mm v[36:39],v[200:203],a[0:3],v[36:39]
ar v234,a138
ar v235,a142
mm v[36:39],v[204:207],a[20:23],v[36:39]
ld a[48:51],v247,s[44:47],s54 offen offset:3072 sc1
mm v[4:7],v[204:207],a[16:19],v[4:7]
wt lgkmcnt(4)
s_bfe_u32 s54, s4, 262168
mm v[4:7],v[200:203],a[12:15],v[4:7]
rt v[204:205],v241 offset:50816
rt v[206:207],v241 offset:51840
mm v[8:11],v[208:211],a[12:15],v[8:11]
s_add_u32 m0, s83, 104576
s_xor_b32 s54, s54, s72
mm v[8:11],v[212:215],a[16:19],v[8:11]
ld v227,s[56:59],s80 offen lds
mm v[40:43],v[212:215],a[20:23],v[40:43]
s_bitcmp1_b32 vcc_lo, 3
s_cbranch_scc0 .La5_LO_NC3
.La5_LO_KC3:
mm v[40:43],v[208:211],a[0:3],v[40:43]
rt v[200:201],v241 offset:59264
rt v[202:203],v241 offset:60288
mm v[72:75],v[208:211],a[4:7],v[72:75]
s_bfe_u32 s80, s4, 262160
s_xor_b32 s80, s80, s72
mm v[72:75],v[212:215],a[24:27],v[72:75]
ld a[52:55],v244,s[44:47],s55 offen offset:0 sc1
mm v[104:107],v[212:215],a[28:31],v[104:107]
s_lshl_b32 s80, s80, 18
s_lshl_b32 s54, s54, 13
mm v[104:107],v[208:211],a[8:11],v[104:107]
rt v[212:213],v242 offset:50816
rt v[214:215],v242 offset:51840
mm v[108:111],v[216:219],a[8:11],v[108:111]
v_perm_b32 v236, v235, v234, s86
v_perm_b32 v237, v235, v234, s87
mm v[108:111],v[220:223],a[28:31],v[108:111]
ld a[56:59],v245,s[44:47],s55 offen offset:0 sc1
mm v[76:79],v[220:223],a[24:27],v[76:79]
aw a113,v236
aw a117,v237
mm v[76:79],v[216:219],a[4:7],v[76:79]
rt v[208:209],v242 offset:59264
rt v[210:211],v242 offset:60288
mm v[44:47],v[216:219],a[0:3],v[44:47]
ar v230,a146
ar v231,a150
mm v[44:47],v[220:223],a[20:23],v[44:47]
v_perm_b32 v232, v231, v230, s86
v_perm_b32 v233, v231, v230, s87
mm v[12:15],v[220:223],a[16:19],v[12:15]
wt lgkmcnt(4)
aw a114,v232
mm v[12:15],v[216:219],a[12:15],v[12:15]
rt v[220:221],v243 offset:50816
rt v[222:223],v243 offset:51840
mm v[16:19],v[196:199],a[12:15],v[16:19]
aw a118,v233
ar v234,a154
mm v[16:19],v[192:195],a[16:19],v[16:19]
ld a[60:63],v246,s[44:47],s55 offen offset:0 sc1
mm v[48:51],v[192:195],a[20:23],v[48:51]
ar v235,a158
v_perm_b32 v236, v235, v234, s86
mm v[48:51],v[196:199],a[0:3],v[48:51]
rt v[216:217],v243 offset:59264
rt v[218:219],v243 offset:60288
mm v[80:83],v[196:199],a[4:7],v[80:83]
v_perm_b32 v237, v235, v234, s87
aw a115,v236
mm v[80:83],v[192:195],a[24:27],v[80:83]
ld a[76:79],v247,s[44:47],s55 offen offset:0 sc1
mm v[112:115],v[192:195],a[28:31],v[112:115]
aw a119,v237
ar v230,a131
mm v[112:115],v[196:199],a[8:11],v[112:115]
rt v[192:193],v240 offset:0
rt v[194:195],v240 offset:1024
mm v[116:119],v[204:207],a[8:11],v[116:119]
)ASM" R"ASM(ar v231,a135
v_perm_b32 v232, v231, v230, s86
mm v[116:119],v[200:203],a[28:31],v[116:119]
ld a[80:83],v244,s[44:47],s55 offen offset:1024 sc1
mm v[84:87],v[200:203],a[24:27],v[84:87]
v_perm_b32 v233, v231, v230, s87
aw a120,v232
mm v[84:87],v[204:207],a[4:7],v[84:87]
rt v[196:197],v240 offset:8448
rt v[198:199],v240 offset:9472
mm v[52:55],v[204:207],a[0:3],v[52:55]
s_load_dwordx2 s[16:17], s[0:1], 0
s_load_dwordx4 s[36:39], s[0:1], 80
mm v[52:55],v[200:203],a[20:23],v[52:55]
ld a[84:87],v245,s[44:47],s55 offen offset:1024 sc1
mm v[20:23],v[200:203],a[16:19],v[20:23]
wt lgkmcnt(4)
aw a124,v233
mm v[20:23],v[204:207],a[12:15],v[20:23]
rt v[200:201],v241 offset:0
rt v[202:203],v241 offset:1024
mm v[24:27],v[212:215],a[12:15],v[24:27]
ar v234,a139
ar v235,a143
mm v[24:27],v[208:211],a[16:19],v[24:27]
ld a[88:91],v246,s[44:47],s55 offen offset:1024 sc1
mm v[56:59],v[208:211],a[20:23],v[56:59]
v_perm_b32 v236, v235, v234, s86
v_perm_b32 v237, v235, v234, s87
mm v[56:59],v[212:215],a[0:3],v[56:59]
rt v[204:205],v241 offset:8448
rt v[206:207],v241 offset:9472
mm v[88:91],v[212:215],a[4:7],v[88:91]
aw a121,v236
aw a125,v237
mm v[88:91],v[208:211],a[24:27],v[88:91]
ld a[92:95],v247,s[44:47],s55 offen offset:1024 sc1
mm v[120:123],v[208:211],a[28:31],v[120:123]
ar v230,a147
ar v231,a151
mm v[120:123],v[212:215],a[8:11],v[120:123]
rt v[208:209],v242 offset:0
rt v[210:211],v242 offset:1024
mm v[124:127],v[220:223],a[8:11],v[124:127]
s_add_u32 m0, s83, 16896
v_perm_b32 v232, v231, v230, s86
mm v[124:127],v[216:219],a[28:31],v[124:127]
ld v224,s[56:59],s80 offen lds
mm v[92:95],v[216:219],a[24:27],v[92:95]
v_perm_b32 v233, v231, v230, s87
aw a122,v232
mm v[92:95],v[220:223],a[4:7],v[92:95]
rt v[212:213],v242 offset:8448
rt v[214:215],v242 offset:9472
mm v[60:63],v[220:223],a[0:3],v[60:63]
aw a126,v233
.La5_LW7_2:
s_cbranch_execz .La5_LW7_6
ar v234,a155
mm v[60:63],v[216:219],a[20:23],v[60:63]
ar v235,a159
v_perm_b32 v236, v235, v234, s86
mm v[28:31],v[216:219],a[16:19],v[28:31]
wt vmcnt(10) lgkmcnt(4)
v_perm_b32 v237, v235, v234, s87
mm v[28:31],v[220:223],a[12:15],v[28:31]
rt v[216:217],v243 offset:0
rt v[218:219],v243 offset:1024
mm v[0:3],v[192:195],a[64:67],v[0:3]
s_add_u32 m0, s83, 17920
aw a123,v236
mm v[0:3],v[196:199],a[32:35],v[0:3]
ld v225,s[56:59],s80 offen lds
mm v[32:35],v[196:199],a[40:43],v[32:35]
aw a127,v237
ar v230,a160
mm v[32:35],v[192:195],a[68:71],v[32:35]
rt v[220:221],v243 offset:8448
rt v[222:223],v243 offset:9472
mm v[64:67],v[192:195],a[72:75],v[64:67]
s_add_u32 m0, s83, 19072
ar v231,a164
mm v[64:67],v[196:199],a[44:47],v[64:67]
ld v226,s[56:59],s80 offen lds
mm v[96:99],v[196:199],a[48:51],v[96:99]
v_perm_b32 v232, v231, v230, s86
v_perm_b32 v233, v231, v230, s87
mm v[96:99],v[192:195],a[36:39],v[96:99]
rt v[196:197],v240 offset:128
rt v[198:199],v240 offset:1152
mm v[100:103],v[200:203],a[36:39],v[100:103]
aw a128,v232
aw a132,v233
mm v[100:103],v[204:207],a[48:51],v[100:103]
ld v[136:139],v244,s[44:47],s55 offen offset:2048 sc1
mm v[68:71],v[204:207],a[44:47],v[68:71]
ar v234,a168
ar v235,a172
mm v[68:71],v[200:203],a[72:75],v[68:71]
rt v[192:193],v240 offset:8576
)ASM" R"ASM(rt v[194:195],v240 offset:9600
mm v[36:39],v[200:203],a[68:71],v[36:39]
v_perm_b32 v236, v235, v234, s86
v_perm_b32 v237, v235, v234, s87
mm v[36:39],v[204:207],a[40:43],v[36:39]
ld v[140:143],v245,s[44:47],s55 offen offset:2048 sc1
mm v[4:7],v[204:207],a[32:35],v[4:7]
wt lgkmcnt(4)
aw a129,v236
mm v[4:7],v[200:203],a[64:67],v[4:7]
rt v[204:205],v241 offset:128
rt v[206:207],v241 offset:1152
mm v[8:11],v[208:211],a[64:67],v[8:11]
aw a133,v237
ar v230,a176
mm v[8:11],v[212:215],a[32:35],v[8:11]
ld v[152:155],v246,s[44:47],s55 offen offset:2048 sc1
mm v[40:43],v[212:215],a[40:43],v[40:43]
ar v231,a180
v_perm_b32 v232, v231, v230, s86
mm v[40:43],v[208:211],a[68:71],v[40:43]
rt v[200:201],v241 offset:8576
rt v[202:203],v241 offset:9600
mm v[72:75],v[208:211],a[72:75],v[72:75]
v_perm_b32 v233, v231, v230, s87
aw a130,v232
mm v[72:75],v[212:215],a[44:47],v[72:75]
ld v[156:159],v247,s[44:47],s55 offen offset:2048 sc1
mm v[104:107],v[212:215],a[48:51],v[104:107]
aw a134,v233
ar v234,a184
mm v[104:107],v[208:211],a[36:39],v[104:107]
rt v[212:213],v242 offset:128
rt v[214:215],v242 offset:1152
mm v[108:111],v[216:219],a[36:39],v[108:111]
ar v235,a188
v_perm_b32 v236, v235, v234, s86
mm v[108:111],v[220:223],a[48:51],v[108:111]
ld v[168:171],v244,s[44:47],s55 offen offset:3072 sc1
mm v[76:79],v[220:223],a[44:47],v[76:79]
v_perm_b32 v237, v235, v234, s87
aw a131,v236
mm v[76:79],v[216:219],a[72:75],v[76:79]
rt v[208:209],v242 offset:8576
rt v[210:211],v242 offset:9600
mm v[44:47],v[216:219],a[68:71],v[44:47]
aw a135,v237
ar v230,a161
mm v[44:47],v[220:223],a[40:43],v[44:47]
s_nop 0
v_cmp_eq_u64 vcc, v[250:251], v[252:253]
mm v[12:15],v[220:223],a[32:35],v[12:15]
wt lgkmcnt(4)
ar v231,a165
mm v[12:15],v[216:219],a[64:67],v[12:15]
rt v[220:221],v243 offset:128
rt v[222:223],v243 offset:1152
mm v[16:19],v[196:199],a[64:67],v[16:19]
v_perm_b32 v232, v231, v230, s86
v_perm_b32 v233, v231, v230, s87
mm v[16:19],v[192:195],a[32:35],v[16:19]
ld v[172:175],v245,s[44:47],s55 offen offset:3072 sc1
mm v[48:51],v[192:195],a[40:43],v[48:51]
aw a136,v232
aw a140,v233
mm v[48:51],v[196:199],a[68:71],v[48:51]
rt v[216:217],v243 offset:8576
rt v[218:219],v243 offset:9600
mm v[80:83],v[196:199],a[72:75],v[80:83]
ar v234,a169
ar v235,a173
mm v[80:83],v[192:195],a[44:47],v[80:83]
ld v[184:187],v246,s[44:47],s55 offen offset:3072 sc1
mm v[112:115],v[192:195],a[48:51],v[112:115]
wt vmcnt(18) lgkmcnt(0)
s_barrier
mm v[112:115],v[196:199],a[36:39],v[112:115]
rt v[192:193],v240 offset:16896
rt v[194:195],v240 offset:17920
mm v[116:119],v[204:207],a[36:39],v[116:119]
s_xor_b32 s90, s72, 0
s_lshl_b32 s92, s76, 5
mm v[116:119],v[200:203],a[48:51],v[116:119]
buffer_load_dwordx2 v[250:251], v248, s[64:67], s5 offen sc1
mm v[84:87],v[200:203],a[44:47],v[84:87]
s_add_u32 s92, s92, s90
s_lshl2_add_u32 s92, s92, s3
mm v[84:87],v[204:207],a[72:75],v[84:87]
rt v[196:197],v240 offset:25344
rt v[198:199],v240 offset:26368
mm v[52:55],v[204:207],a[68:71],v[52:55]
s_lshl_b32 s92, s92, 12
v_perm_b32 v236, v235, v234, s86
mm v[52:55],v[200:203],a[40:43],v[52:55]
ld v[188:191],v247,s[44:47],s55 offen offset:3072 sc1
mm v[20:23],v[200:203],a[32:35],v[20:23]
s_nop 0
s_bfe_u32 s55, s4, 262172
mm v[20:23],v[204:207],a[64:67],v[20:23]
rt v[200:201],v241 offset:16896
)ASM" R"ASM(rt v[202:203],v241 offset:17920
mm v[24:27],v[212:215],a[64:67],v[24:27]
s_add_u32 m0, s83, 20096
s_xor_b32 s55, s55, s72
mm v[24:27],v[208:211],a[32:35],v[24:27]
ld v227,s[56:59],s80 offen lds
mm v[56:59],v[208:211],a[40:43],v[56:59]
s_add_u32 s80, s80, 131072
s_lshl_b32 s55, s55, 13
mm v[56:59],v[212:215],a[68:71],v[56:59]
rt v[204:205],v241 offset:25344
rt v[206:207],v241 offset:26368
mm v[88:91],v[212:215],a[72:75],v[88:91]
s_add_u32 m0, s83, 33792
v_perm_b32 v237, v235, v234, s87
mm v[88:91],v[208:211],a[44:47],v[88:91]
ld v224,s[56:59],s80 offen lds
mm v[120:123],v[208:211],a[48:51],v[120:123]
s_add_u32 s68, s16, s92
s_addc_u32 s69, s17, 0
mm v[120:123],v[212:215],a[36:39],v[120:123]
rt v[208:209],v242 offset:16896
rt v[210:211],v242 offset:17920
mm v[124:127],v[220:223],a[36:39],v[124:127]
s_add_u32 m0, s83, 34816
s_and_b32 s69, s69, 0xffff
mm v[124:127],v[216:219],a[48:51],v[124:127]
ld v225,s[56:59],s80 offen lds
mm v[92:95],v[216:219],a[44:47],v[92:95]
s_mov_b32 s70, 4096
s_mov_b32 s71, 0x20000
mm v[92:95],v[220:223],a[72:75],v[92:95]
rt v[212:213],v242 offset:25344
rt v[214:215],v242 offset:26368
mm v[60:63],v[220:223],a[68:71],v[60:63]
s_add_u32 m0, s83, 35968
aw a137,v236
mm v[60:63],v[216:219],a[40:43],v[60:63]
ld v226,s[56:59],s80 offen lds
mm v[28:31],v[216:219],a[32:35],v[28:31]
wt vmcnt(16) lgkmcnt(4)
aw a141,v237
mm v[28:31],v[220:223],a[64:67],v[28:31]
rt v[216:217],v243 offset:16896
rt v[218:219],v243 offset:17920
mm v[0:3],v[192:195],a[52:55],v[0:3]
s_add_u32 m0, s83, 36992
ar v230,a177
mm v[0:3],v[196:199],a[80:83],v[0:3]
ld v227,s[56:59],s80 offen lds
mm v[32:35],v[196:199],a[84:87],v[32:35]
s_bitcmp1_b32 vcc_lo, 4
s_cbranch_scc0 .La5_LO_NC4
.La5_LO_KC4:
mm v[32:35],v[192:195],a[56:59],v[32:35]
rt v[220:221],v243 offset:25344
rt v[222:223],v243 offset:26368
mm v[64:67],v[192:195],a[60:63],v[64:67]
s_bfe_u32 s80, s4, 262164
s_xor_b32 s80, s80, s72
mm v[64:67],v[196:199],a[88:91],v[64:67]
ld a[0:3],v244,s[44:47],s52 offen offset:0 sc1
mm v[96:99],v[196:199],a[92:95],v[96:99]
s_lshl_b32 s80, s80, 18
ar v231,a181
mm v[96:99],v[192:195],a[76:79],v[96:99]
rt v[196:197],v240 offset:17024
rt v[198:199],v240 offset:18048
mm v[100:103],v[200:203],a[76:79],v[100:103]
s_add_u32 s48, s36, 0
s_addc_u32 s49, s37, 0
mm v[100:103],v[204:207],a[92:95],v[100:103]
ld a[4:7],v245,s[44:47],s52 offen offset:0 sc1
mm v[68:71],v[204:207],a[88:91],v[68:71]
s_and_b32 s49, s49, 0xffff
s_mov_b32 s50, 2048
mm v[68:71],v[200:203],a[60:63],v[68:71]
rt v[192:193],v240 offset:25472
rt v[194:195],v240 offset:26496
mm v[36:39],v[200:203],a[56:59],v[36:39]
s_mov_b32 s51, 0x20000
v_and_b32 v238, 0xf0, v255
mm v[36:39],v[204:207],a[84:87],v[36:39]
ld a[8:11],v246,s[44:47],s52 offen offset:0 sc1
mm v[4:7],v[204:207],a[80:83],v[4:7]
wt lgkmcnt(4)
v_lshlrev_b32 v238, 2, v238
mm v[4:7],v[200:203],a[52:55],v[4:7]
rt v[204:205],v241 offset:17024
rt v[206:207],v241 offset:18048
mm v[8:11],v[208:211],a[52:55],v[8:11]
v_bfe_u32 v249, v255, 8, 1
v_lshl_add_u32 v238, v249, 5, v238
mm v[8:11],v[212:215],a[80:83],v[8:11]
ld a[12:15],v247,s[44:47],s52 offen offset:0 sc1
mm v[40:43],v[212:215],a[84:87],v[40:43]
v_lshrrev_b32_e64 v249, 9, v255
v_lshl_add_u32 v238, v249, 4, v238
mm v[40:43],v[208:211],a[56:59],v[40:43]
rt v[200:201],v241 offset:25472
)ASM" R"ASM(rt v[202:203],v241 offset:26496
mm v[72:75],v[208:211],a[60:63],v[72:75]
s_lshr_b32 s23, s72, 3
s_lshl3_add_u32 s92, s23, s76
mm v[72:75],v[212:215],a[88:91],v[72:75]
ld a[16:19],v244,s[44:47],s52 offen offset:1024 sc1
mm v[104:107],v[212:215],a[92:95],v[104:107]
s_lshl_b32 s92, s92, 19
s_lshl_b32 s97, s3, 16
mm v[104:107],v[208:211],a[76:79],v[104:107]
rt v[212:213],v242 offset:17024
rt v[214:215],v242 offset:18048
mm v[108:111],v[216:219],a[76:79],v[108:111]
s_add_u32 s94, s92, s97
s_add_u32 s95, s94, 32768
mm v[108:111],v[220:223],a[92:95],v[108:111]
ld a[20:23],v245,s[44:47],s52 offen offset:1024 sc1
mm v[76:79],v[220:223],a[88:91],v[76:79]
s_sub_u32 s96, s92, s97
s_add_u32 s96, s96, 458752
mm v[76:79],v[216:219],a[60:63],v[76:79]
rt v[208:209],v242 offset:25472
rt v[210:211],v242 offset:26496
mm v[44:47],v[216:219],a[56:59],v[44:47]
s_add_u32 s97, s96, 32768
s_lshl3_add_u32 s92, s23, s101
mm v[44:47],v[220:223],a[84:87],v[44:47]
s_lshl_b32 s85, s76, 14
s_lshl3_add_u32 s92, s92, s85
mm v[12:15],v[220:223],a[80:83],v[12:15]
wt lgkmcnt(4)
s_add_u32 s92, s92, 2097152
mm v[12:15],v[216:219],a[52:55],v[12:15]
rt v[220:221],v243 offset:17024
rt v[222:223],v243 offset:18048
mm v[16:19],v[196:199],a[52:55],v[16:19]
v_perm_b32 v232, v231, v230, s86
v_perm_b32 v233, v231, v230, s87
mm v[16:19],v[192:195],a[80:83],v[16:19]
ld a[24:27],v246,s[44:47],s52 offen offset:1024 sc1
mm v[48:51],v[192:195],a[84:87],v[48:51]
aw a138,v232
aw a142,v233
mm v[48:51],v[196:199],a[56:59],v[48:51]
rt v[216:217],v243 offset:25472
rt v[218:219],v243 offset:26496
mm v[80:83],v[196:199],a[60:63],v[80:83]
ar v234,a185
ar v235,a189
mm v[80:83],v[192:195],a[88:91],v[80:83]
ld a[28:31],v247,s[44:47],s52 offen offset:1024 sc1
mm v[112:115],v[192:195],a[92:95],v[112:115]
v_perm_b32 v236, v235, v234, s86
v_perm_b32 v237, v235, v234, s87
mm v[112:115],v[196:199],a[76:79],v[112:115]
rt v[192:193],v240 offset:33792
rt v[194:195],v240 offset:34816
mm v[116:119],v[204:207],a[76:79],v[116:119]
s_add_u32 m0, s83, 50688
aw a139,v236
mm v[116:119],v[200:203],a[92:95],v[116:119]
ld v224,s[56:59],s80 offen lds
mm v[84:87],v[200:203],a[88:91],v[84:87]
aw a143,v237
ar v230,a162
mm v[84:87],v[204:207],a[60:63],v[84:87]
rt v[196:197],v240 offset:42240
rt v[198:199],v240 offset:43264
mm v[52:55],v[204:207],a[56:59],v[52:55]
s_add_u32 m0, s83, 51712
ar v231,a166
mm v[52:55],v[200:203],a[84:87],v[52:55]
ld v225,s[56:59],s80 offen lds
mm v[20:23],v[200:203],a[80:83],v[20:23]
wt lgkmcnt(4)
v_perm_b32 v232, v231, v230, s86
mm v[20:23],v[204:207],a[52:55],v[20:23]
rt v[200:201],v241 offset:33792
rt v[202:203],v241 offset:34816
mm v[24:27],v[212:215],a[52:55],v[24:27]
s_add_u32 m0, s83, 52864
v_perm_b32 v233, v231, v230, s87
mm v[24:27],v[208:211],a[80:83],v[24:27]
ld v226,s[56:59],s80 offen lds
mm v[56:59],v[208:211],a[84:87],v[56:59]
aw a144,v232
aw a148,v233
mm v[56:59],v[212:215],a[56:59],v[56:59]
rt v[204:205],v241 offset:42240
rt v[206:207],v241 offset:43264
mm v[88:91],v[212:215],a[60:63],v[88:91]
ar v234,a170
ar v235,a174
mm v[88:91],v[208:211],a[88:91],v[88:91]
ld a[32:35],v244,s[44:47],s52 offen offset:2048 sc1
mm v[120:123],v[208:211],a[92:95],v[120:123]
v_perm_b32 v236, v235, v234, s86
v_perm_b32 v237, v235, v234, s87
mm v[120:123],v[212:215],a[76:79],v[120:123]
rt v[208:209],v242 offset:33792
)ASM" R"ASM(rt v[210:211],v242 offset:34816
mm v[124:127],v[220:223],a[76:79],v[124:127]
aw a145,v236
aw a149,v237
mm v[124:127],v[216:219],a[92:95],v[124:127]
ld a[36:39],v245,s[44:47],s52 offen offset:2048 sc1
mm v[92:95],v[216:219],a[88:91],v[92:95]
ar v230,a178
ar v231,a182
mm v[92:95],v[220:223],a[60:63],v[92:95]
rt v[212:213],v242 offset:42240
rt v[214:215],v242 offset:43264
mm v[60:63],v[220:223],a[56:59],v[60:63]
v_perm_b32 v232, v231, v230, s86
v_perm_b32 v233, v231, v230, s87
mm v[60:63],v[216:219],a[84:87],v[60:63]
aw a146,v232
aw a150,v233
mm v[28:31],v[216:219],a[80:83],v[28:31]
wt vmcnt(18) lgkmcnt(4)
ar v234,a186
mm v[28:31],v[220:223],a[52:55],v[28:31]
rt v[216:217],v243 offset:33792
rt v[218:219],v243 offset:34816
mm v[0:3],v[192:195],v[136:139],v[0:3]
ar v235,a190
v_perm_b32 v236, v235, v234, s86
mm v[0:3],v[196:199],v[168:171],v[0:3]
ld a[40:43],v246,s[44:47],s52 offen offset:2048 sc1
mm v[32:35],v[196:199],v[172:175],v[32:35]
v_perm_b32 v237, v235, v234, s87
aw a147,v236
mm v[32:35],v[192:195],v[140:143],v[32:35]
rt v[220:221],v243 offset:42240
rt v[222:223],v243 offset:43264
mm v[64:67],v[192:195],v[152:155],v[64:67]
aw a151,v237
ar v230,a163
mm v[64:67],v[196:199],v[184:187],v[64:67]
ld a[44:47],v247,s[44:47],s52 offen offset:2048 sc1
mm v[96:99],v[196:199],v[188:191],v[96:99]
ar v231,a167
v_perm_b32 v232, v231, v230, s86
mm v[96:99],v[192:195],v[156:159],v[96:99]
rt v[196:197],v240 offset:33920
rt v[198:199],v240 offset:34944
mm v[100:103],v[200:203],v[156:159],v[100:103]
v_perm_b32 v233, v231, v230, s87
aw a152,v232
mm v[100:103],v[204:207],v[188:191],v[100:103]
ld a[48:51],v244,s[44:47],s52 offen offset:3072 sc1
mm v[68:71],v[204:207],v[184:187],v[68:71]
aw a156,v233
ar v234,a171
mm v[68:71],v[200:203],v[152:155],v[68:71]
rt v[192:193],v240 offset:42368
rt v[194:195],v240 offset:43392
mm v[36:39],v[200:203],v[140:143],v[36:39]
v_add_u32 v240, 4294899712, v240
ar v235,a175
mm v[36:39],v[204:207],v[172:175],v[36:39]
ld a[52:55],v245,s[44:47],s52 offen offset:3072 sc1
mm v[4:7],v[204:207],v[168:171],v[4:7]
wt lgkmcnt(4)
v_perm_b32 v236, v235, v234, s86
mm v[4:7],v[200:203],v[136:139],v[4:7]
rt v[204:205],v241 offset:33920
rt v[206:207],v241 offset:34944
mm v[8:11],v[208:211],v[136:139],v[8:11]
s_nop 0
v_cmp_eq_u64 vcc, v[250:251], v[252:253]
mm v[8:11],v[212:215],v[168:171],v[8:11]
ld a[56:59],v246,s[44:47],s52 offen offset:3072 sc1
mm v[40:43],v[212:215],v[172:175],v[40:43]
v_perm_b32 v237, v235, v234, s87
aw a153,v236
mm v[40:43],v[208:211],v[140:143],v[40:43]
rt v[200:201],v241 offset:42368
rt v[202:203],v241 offset:43392
mm v[72:75],v[208:211],v[152:155],v[72:75]
v_add_u32 v241, 4294899712, v241
aw a157,v237
mm v[72:75],v[212:215],v[184:187],v[72:75]
ld a[60:63],v247,s[44:47],s52 offen offset:3072 sc1
mm v[104:107],v[212:215],v[188:191],v[104:107]
ar v230,a179
ar v231,a183
mm v[104:107],v[208:211],v[156:159],v[104:107]
rt v[212:213],v242 offset:33920
rt v[214:215],v242 offset:34944
mm v[108:111],v[216:219],v[156:159],v[108:111]
v_perm_b32 v232, v231, v230, s86
v_perm_b32 v233, v231, v230, s87
mm v[108:111],v[220:223],v[188:191],v[108:111]
buffer_load_dwordx2 v[250:251], v248, s[64:67], s5 offen sc1
mm v[76:79],v[220:223],v[184:187],v[76:79]
aw a154,v232
aw a158,v233
mm v[76:79],v[216:219],v[152:155],v[76:79]
rt v[208:209],v242 offset:42368
)ASM" R"ASM(rt v[210:211],v242 offset:43392
mm v[44:47],v[216:219],v[140:143],v[44:47]
s_add_u32 m0, s83, 53888
v_add_u32 v242, 4294899712, v242
mm v[44:47],v[220:223],v[172:175],v[44:47]
ld v227,s[56:59],s80 offen lds
mm v[12:15],v[220:223],v[168:171],v[12:15]
wt lgkmcnt(4)
s_bfe_u32 s80, s4, 262168
mm v[12:15],v[216:219],v[136:139],v[12:15]
rt v[220:221],v243 offset:33920
rt v[222:223],v243 offset:34944
mm v[16:19],v[196:199],v[136:139],v[16:19]
s_add_u32 m0, s83, 118272
s_xor_b32 s80, s80, s72
s_lshl_b32 s80, s80, 18
mm v[16:19],v[192:195],v[168:171],v[16:19]
ld v224,s[56:59],s80 offen lds
mm v[48:51],v[192:195],v[172:175],v[48:51]
ar v234,a187
ar v235,a191
mm v[48:51],v[196:199],v[140:143],v[48:51]
rt v[216:217],v243 offset:42368
rt v[218:219],v243 offset:43392
mm v[80:83],v[196:199],v[152:155],v[80:83]
s_add_u32 m0, s83, 119296
v_add_u32 v243, 4294899712, v243
mm v[80:83],v[192:195],v[184:187],v[80:83]
ld v225,s[56:59],s80 offen lds
mm v[112:115],v[192:195],v[188:191],v[112:115]
wt vmcnt(23) lgkmcnt(0)
s_barrier
mm v[112:115],v[196:199],v[156:159],v[112:115]
rt v[192:193],v240 offset:16896
rt v[194:195],v240 offset:17920
mm v[116:119],v[204:207],v[156:159],v[116:119]
s_add_u32 m0, s83, 120448
v_perm_b32 v236, v235, v234, s86
mm v[116:119],v[200:203],v[188:191],v[116:119]
ld v226,s[56:59],s80 offen lds
mm v[84:87],v[200:203],v[184:187],v[84:87]
s_bitcmp1_b32 vcc_lo, 5
s_cbranch_scc0 .La5_LO_NC5
.La5_LO_KC5:
mm v[84:87],v[204:207],v[152:155],v[84:87]
rt v[196:197],v240 offset:25344
rt v[198:199],v240 offset:26368
mm v[52:55],v[204:207],v[140:143],v[52:55]
v_perm_b32 v237, v235, v234, s87
aw a155,v236
mm v[52:55],v[200:203],v[172:175],v[52:55]
ld v[128:131],v244,s[44:47],s53 offen offset:0 sc1
mm v[20:23],v[200:203],v[168:171],v[20:23]
s_nop 0
aw a159,v237
mm v[20:23],v[204:207],v[136:139],v[20:23]
rt v[200:201],v241 offset:16896
rt v[202:203],v241 offset:17920
mm v[24:27],v[212:215],v[136:139],v[24:27]
ar v230,a192
ar v231,a196
mm v[24:27],v[208:211],v[168:171],v[24:27]
ld v[132:135],v245,s[44:47],s53 offen offset:0 sc1
mm v[56:59],v[208:211],v[172:175],v[56:59]
v_perm_b32 v232, v231, v230, s86
v_perm_b32 v233, v231, v230, s87
mm v[56:59],v[212:215],v[140:143],v[56:59]
rt v[204:205],v241 offset:25344
rt v[206:207],v241 offset:26368
mm v[88:91],v[212:215],v[152:155],v[88:91]
aw a160,v232
aw a164,v233
mm v[88:91],v[208:211],v[184:187],v[88:91]
ld v[144:147],v246,s[44:47],s53 offen offset:0 sc1
mm v[120:123],v[208:211],v[188:191],v[120:123]
ar v234,a200
ar v235,a204
mm v[120:123],v[212:215],v[156:159],v[120:123]
rt v[208:209],v242 offset:16896
rt v[210:211],v242 offset:17920
mm v[124:127],v[220:223],v[156:159],v[124:127]
v_perm_b32 v236, v235, v234, s86
v_perm_b32 v237, v235, v234, s87
mm v[124:127],v[216:219],v[188:191],v[124:127]
ld v[156:159],v247,s[44:47],s53 offen offset:0 sc1
mm v[92:95],v[216:219],v[184:187],v[92:95]
aw a161,v236
aw a165,v237
mm v[92:95],v[220:223],v[152:155],v[92:95]
rt v[212:213],v242 offset:25344
rt v[214:215],v242 offset:26368
mm v[60:63],v[220:223],v[140:143],v[60:63]
ar v230,a208
ar v231,a212
mm v[60:63],v[216:219],v[172:175],v[60:63]
v_perm_b32 v232, v231, v230, s86
.La5_LW7_3:
s_cbranch_execz .La5_LW7_7
v_perm_b32 v233, v231, v230, s87
mm v[28:31],v[216:219],v[168:171],v[28:31]
)ASM" R"ASM(wt vmcnt(20) lgkmcnt(4)
aw a162,v232
mm v[28:31],v[220:223],v[136:139],v[28:31]
rt v[216:217],v243 offset:16896
rt v[218:219],v243 offset:17920
mm v[0:3],v[192:195],a[0:3],v[0:3]
aw a166,v233
ar v234,a216
mm v[0:3],v[196:199],a[16:19],v[0:3]
ld v[136:139],v244,s[44:47],s53 offen offset:1024 sc1
mm v[32:35],v[196:199],a[20:23],v[32:35]
ar v235,a220
v_perm_b32 v236, v235, v234, s86
mm v[32:35],v[192:195],a[4:7],v[32:35]
rt v[220:221],v243 offset:25344
rt v[222:223],v243 offset:26368
mm v[64:67],v[192:195],a[8:11],v[64:67]
v_perm_b32 v237, v235, v234, s87
aw a163,v236
mm v[64:67],v[196:199],a[24:27],v[64:67]
ld v[140:143],v245,s[44:47],s53 offen offset:1024 sc1
mm v[96:99],v[196:199],a[28:31],v[96:99]
aw a167,v237
ar v230,a193
mm v[96:99],v[192:195],a[12:15],v[96:99]
rt v[196:197],v240 offset:17024
rt v[198:199],v240 offset:18048
mm v[100:103],v[200:203],a[12:15],v[100:103]
ar v231,a197
v_perm_b32 v232, v231, v230, s86
mm v[100:103],v[204:207],a[28:31],v[100:103]
ld v[152:155],v246,s[44:47],s53 offen offset:1024 sc1
mm v[68:71],v[204:207],a[24:27],v[68:71]
v_perm_b32 v233, v231, v230, s87
aw a168,v232
mm v[68:71],v[200:203],a[8:11],v[68:71]
rt v[192:193],v240 offset:25472
rt v[194:195],v240 offset:26496
mm v[36:39],v[200:203],a[4:7],v[36:39]
aw a172,v233
ar v234,a201
mm v[36:39],v[204:207],a[20:23],v[36:39]
ld v[168:171],v247,s[44:47],s53 offen offset:1024 sc1
mm v[4:7],v[204:207],a[16:19],v[4:7]
wt lgkmcnt(4)
ar v235,a205
mm v[4:7],v[200:203],a[0:3],v[4:7]
rt v[204:205],v241 offset:17024
rt v[206:207],v241 offset:18048
mm v[8:11],v[208:211],a[0:3],v[8:11]
s_add_u32 m0, s83, 121472
v_perm_b32 v236, v235, v234, s86
mm v[8:11],v[212:215],a[16:19],v[8:11]
ld v227,s[56:59],s80 offen lds
mm v[40:43],v[212:215],a[20:23],v[40:43]
s_add_u32 s80, s80, 131072
v_perm_b32 v237, v235, v234, s87
mm v[40:43],v[208:211],a[4:7],v[40:43]
rt v[200:201],v241 offset:25472
rt v[202:203],v241 offset:26496
mm v[72:75],v[208:211],a[8:11],v[72:75]
s_add_u32 m0, s83, 67584
aw a169,v236
mm v[72:75],v[212:215],a[24:27],v[72:75]
ld v224,s[56:59],s80 offen lds
mm v[104:107],v[212:215],a[28:31],v[104:107]
aw a173,v237
ar v230,a209
mm v[104:107],v[208:211],a[12:15],v[104:107]
rt v[212:213],v242 offset:17024
rt v[214:215],v242 offset:18048
mm v[108:111],v[216:219],a[12:15],v[108:111]
s_add_u32 m0, s83, 68608
ar v231,a213
mm v[108:111],v[220:223],a[28:31],v[108:111]
ld v225,s[56:59],s80 offen lds
mm v[76:79],v[220:223],a[24:27],v[76:79]
v_perm_b32 v232, v231, v230, s86
v_perm_b32 v233, v231, v230, s87
mm v[76:79],v[216:219],a[8:11],v[76:79]
rt v[208:209],v242 offset:25472
rt v[210:211],v242 offset:26496
mm v[44:47],v[216:219],a[4:7],v[44:47]
aw a170,v232
aw a174,v233
mm v[44:47],v[220:223],a[20:23],v[44:47]
ar v234,a217
ar v235,a221
mm v[12:15],v[220:223],a[16:19],v[12:15]
wt lgkmcnt(4)
v_perm_b32 v236, v235, v234, s86
mm v[12:15],v[216:219],a[0:3],v[12:15]
rt v[220:221],v243 offset:17024
rt v[222:223],v243 offset:18048
mm v[16:19],v[196:199],a[0:3],v[16:19]
s_add_u32 m0, s83, 69760
v_perm_b32 v237, v235, v234, s87
mm v[16:19],v[192:195],a[16:19],v[16:19]
ld v226,s[56:59],s80 offen lds
mm v[48:51],v[192:195],a[20:23],v[48:51]
)ASM" R"ASM(aw a171,v236
aw a175,v237
mm v[48:51],v[196:199],a[4:7],v[48:51]
rt v[216:217],v243 offset:25472
rt v[218:219],v243 offset:26496
mm v[80:83],v[196:199],a[8:11],v[80:83]
s_add_u32 m0, s83, 70784
ar v230,a194
mm v[80:83],v[192:195],a[24:27],v[80:83]
ld v227,s[56:59],s80 offen lds
mm v[112:115],v[192:195],a[28:31],v[112:115]
s_bfe_u32 s80, s4, 262172
s_xor_b32 s80, s80, s72
mm v[112:115],v[196:199],a[12:15],v[112:115]
rt v[192:193],v240 offset:33792
rt v[194:195],v240 offset:34816
mm v[116:119],v[204:207],a[12:15],v[116:119]
s_add_u32 m0, s83, 84480
s_lshl_b32 s80, s80, 18
mm v[116:119],v[200:203],a[28:31],v[116:119]
ld v224,s[56:59],s80 offen lds
mm v[84:87],v[200:203],a[24:27],v[84:87]
ar v231,a198
v_perm_b32 v232, v231, v230, s86
mm v[84:87],v[204:207],a[8:11],v[84:87]
rt v[196:197],v240 offset:42240
rt v[198:199],v240 offset:43264
mm v[52:55],v[204:207],a[4:7],v[52:55]
s_add_u32 m0, s83, 85504
v_perm_b32 v233, v231, v230, s87
mm v[52:55],v[200:203],a[20:23],v[52:55]
ld v225,s[56:59],s80 offen lds
mm v[20:23],v[200:203],a[16:19],v[20:23]
wt lgkmcnt(4)
aw a176,v232
mm v[20:23],v[204:207],a[0:3],v[20:23]
rt v[200:201],v241 offset:33792
rt v[202:203],v241 offset:34816
mm v[24:27],v[212:215],a[0:3],v[24:27]
s_add_u32 m0, s83, 86656
aw a180,v233
mm v[24:27],v[208:211],a[16:19],v[24:27]
ld v226,s[56:59],s80 offen lds
mm v[56:59],v[208:211],a[20:23],v[56:59]
ar v234,a202
ar v235,a206
mm v[56:59],v[212:215],a[4:7],v[56:59]
rt v[204:205],v241 offset:42240
rt v[206:207],v241 offset:43264
mm v[88:91],v[212:215],a[8:11],v[88:91]
s_add_u32 m0, s83, 87680
v_perm_b32 v236, v235, v234, s86
mm v[88:91],v[208:211],a[24:27],v[88:91]
ld v227,s[56:59],s80 offen lds
mm v[120:123],v[208:211],a[28:31],v[120:123]
s_add_u32 s80, s80, 131072
v_perm_b32 v237, v235, v234, s87
mm v[120:123],v[212:215],a[12:15],v[120:123]
rt v[208:209],v242 offset:33792
rt v[210:211],v242 offset:34816
mm v[124:127],v[220:223],a[12:15],v[124:127]
s_add_u32 m0, s83, 101376
aw a177,v236
mm v[124:127],v[216:219],a[28:31],v[124:127]
ld v224,s[56:59],s80 offen lds
mm v[92:95],v[216:219],a[24:27],v[92:95]
aw a181,v237
ar v230,a210
mm v[92:95],v[220:223],a[8:11],v[92:95]
rt v[212:213],v242 offset:42240
rt v[214:215],v242 offset:43264
mm v[60:63],v[220:223],a[4:7],v[60:63]
ar v231,a214
v_perm_b32 v232, v231, v230, s86
mm v[60:63],v[216:219],a[20:23],v[60:63]
wt vmcnt(22)
v_cmp_eq_u64 vcc, v[250:251], v[252:253]
mm v[28:31],v[216:219],a[16:19],v[28:31]
wt lgkmcnt(4)
v_perm_b32 v233, v231, v230, s87
mm v[28:31],v[220:223],a[0:3],v[28:31]
rt v[216:217],v243 offset:33792
rt v[218:219],v243 offset:34816
mm v[0:3],v[192:195],a[32:35],v[0:3]
s_add_u32 m0, s83, 102400
aw a178,v232
mm v[0:3],v[196:199],a[48:51],v[0:3]
ld v225,s[56:59],s80 offen lds
mm v[32:35],v[196:199],a[52:55],v[32:35]
aw a182,v233
ar v234,a218
mm v[32:35],v[192:195],a[36:39],v[32:35]
rt v[220:221],v243 offset:42240
rt v[222:223],v243 offset:43264
mm v[64:67],v[192:195],a[40:43],v[64:67]
s_add_u32 m0, s83, 103552
ar v235,a222
mm v[64:67],v[196:199],a[56:59],v[64:67]
ld v226,s[56:59],s80 offen lds
mm v[96:99],v[196:199],a[60:63],v[96:99]
)ASM" R"ASM(v_perm_b32 v236, v235, v234, s86
v_perm_b32 v237, v235, v234, s87
mm v[96:99],v[192:195],a[44:47],v[96:99]
rt v[196:197],v240 offset:33920
rt v[198:199],v240 offset:34944
mm v[100:103],v[200:203],a[44:47],v[100:103]
s_add_u32 m0, s83, 104576
aw a179,v236
mm v[100:103],v[204:207],a[60:63],v[100:103]
ld v227,s[56:59],s80 offen lds
mm v[68:71],v[204:207],a[56:59],v[68:71]
s_bitcmp1_b32 vcc_lo, 6
s_cbranch_scc0 .La5_LO_NC6
.La5_LO_KC6:
mm v[68:71],v[200:203],a[40:43],v[68:71]
rt v[192:193],v240 offset:42368
rt v[194:195],v240 offset:43392
mm v[36:39],v[200:203],a[36:39],v[36:39]
v_bfe_u32 v224, v255, 6, 1
v_lshrrev_b32 v225, 7, v255
mm v[36:39],v[204:207],a[52:55],v[36:39]
ld a[64:67],v244,s[44:47],s54 offen offset:0 sc1
mm v[4:7],v[204:207],a[48:51],v[4:7]
wt lgkmcnt(4)
v_and_b32 v226, 48, v255
mm v[4:7],v[200:203],a[32:35],v[4:7]
rt v[204:205],v241 offset:33920
rt v[206:207],v241 offset:34944
mm v[8:11],v[208:211],a[32:35],v[8:11]
v_lshl_add_u32 v225, v225, 6, v226
v_lshl_add_u32 v224, v224, 11, v225
mm v[8:11],v[212:215],a[48:51],v[8:11]
ld a[68:71],v245,s[44:47],s54 offen offset:0 sc1
mm v[40:43],v[212:215],a[52:55],v[40:43]
aw a183,v237
ar v230,a195
mm v[40:43],v[208:211],a[36:39],v[40:43]
rt v[200:201],v241 offset:42368
rt v[202:203],v241 offset:43392
mm v[72:75],v[208:211],a[40:43],v[72:75]
ar v231,a199
v_perm_b32 v232, v231, v230, s86
mm v[72:75],v[212:215],a[56:59],v[72:75]
ld a[72:75],v244,s[44:47],s54 offen offset:1024 sc1
mm v[104:107],v[212:215],a[60:63],v[104:107]
v_perm_b32 v233, v231, v230, s87
aw a184,v232
mm v[104:107],v[208:211],a[44:47],v[104:107]
rt v[212:213],v242 offset:33920
rt v[214:215],v242 offset:34944
mm v[108:111],v[216:219],a[44:47],v[108:111]
aw a188,v233
ar v234,a203
mm v[108:111],v[220:223],a[60:63],v[108:111]
ld a[76:79],v245,s[44:47],s54 offen offset:1024 sc1
mm v[76:79],v[220:223],a[56:59],v[76:79]
ar v235,a207
v_perm_b32 v236, v235, v234, s86
mm v[76:79],v[216:219],a[40:43],v[76:79]
rt v[208:209],v242 offset:42368
rt v[210:211],v242 offset:43392
mm v[44:47],v[216:219],a[36:39],v[44:47]
v_perm_b32 v237, v235, v234, s87
aw a185,v236
mm v[44:47],v[220:223],a[52:55],v[44:47]
aw a189,v237
ar v230,a211
mm v[12:15],v[220:223],a[48:51],v[12:15]
wt lgkmcnt(4)
ar v231,a215
mm v[12:15],v[216:219],a[32:35],v[12:15]
rt v[220:221],v243 offset:33920
rt v[222:223],v243 offset:34944
mm v[16:19],v[196:199],a[32:35],v[16:19]
v_perm_b32 v232, v231, v230, s86
v_perm_b32 v233, v231, v230, s87
mm v[16:19],v[192:195],a[48:51],v[16:19]
ld a[80:83],v244,s[44:47],s54 offen offset:2048 sc1
mm v[48:51],v[192:195],a[52:55],v[48:51]
aw a186,v232
aw a190,v233
mm v[48:51],v[196:199],a[36:39],v[48:51]
rt v[216:217],v243 offset:42368
rt v[218:219],v243 offset:43392
mm v[80:83],v[196:199],a[40:43],v[80:83]
ar v234,a219
ar v235,a223
mm v[80:83],v[192:195],a[56:59],v[80:83]
ld a[84:87],v245,s[44:47],s54 offen offset:2048 sc1
mm v[112:115],v[192:195],a[60:63],v[112:115]
wt vmcnt(30)
s_barrier
mm v[112:115],v[196:199],a[44:47],v[112:115]
rt v[192:193],v240 offset:50688
rt v[194:195],v240 offset:51712
mm v[116:119],v[204:207],a[44:47],v[116:119]
v_perm_b32 v236, v235, v234, s86
v_perm_b32 v237, v235, v234, s87
mm v[116:119],v[200:203],a[60:63],v[116:119]
ld a[88:91],v244,s[44:47],s54 offen offset:3072 sc1
)ASM" R"ASM(mm v[84:87],v[200:203],a[56:59],v[84:87]
aw a187,v236
aw a191,v237
mm v[84:87],v[204:207],a[40:43],v[84:87]
rt v[196:197],v240 offset:59136
rt v[198:199],v240 offset:60160
mm v[52:55],v[204:207],a[36:39],v[52:55]
s_bitcmp1_b32 vcc_lo, 7
s_cbranch_scc0 .La5_LO_NC7
.La5_LO_KC7:
mm v[52:55],v[200:203],a[52:55],v[52:55]
ld a[92:95],v245,s[44:47],s54 offen offset:3072 sc1
mm v[20:23],v[200:203],a[48:51],v[20:23]
wt lgkmcnt(4)
ar v230,a224
mm v[20:23],v[204:207],a[32:35],v[20:23]
rt v[200:201],v241 offset:50688
rt v[202:203],v241 offset:51712
mm v[24:27],v[212:215],a[32:35],v[24:27]
ar v231,a228
v_perm_b32 v232, v231, v230, s86
mm v[24:27],v[208:211],a[48:51],v[24:27]
ld a[0:3],v244,s[44:47],s55 offen offset:0 sc1
mm v[56:59],v[208:211],a[52:55],v[56:59]
v_perm_b32 v233, v231, v230, s87
aw a192,v232
mm v[56:59],v[212:215],a[36:39],v[56:59]
rt v[204:205],v241 offset:59136
rt v[206:207],v241 offset:60160
mm v[88:91],v[212:215],a[40:43],v[88:91]
aw a196,v233
ar v234,a232
mm v[88:91],v[208:211],a[56:59],v[88:91]
ld a[4:7],v245,s[44:47],s55 offen offset:0 sc1
mm v[120:123],v[208:211],a[60:63],v[120:123]
ar v235,a236
v_perm_b32 v236, v235, v234, s86
mm v[120:123],v[212:215],a[44:47],v[120:123]
rt v[208:209],v242 offset:50688
rt v[210:211],v242 offset:51712
mm v[124:127],v[220:223],a[44:47],v[124:127]
v_perm_b32 v237, v235, v234, s87
aw a193,v236
mm v[124:127],v[216:219],a[60:63],v[124:127]
ld a[8:11],v244,s[44:47],s55 offen offset:1024 sc1
mm v[92:95],v[216:219],a[56:59],v[92:95]
aw a197,v237
ar v230,a240
mm v[92:95],v[220:223],a[40:43],v[92:95]
rt v[212:213],v242 offset:59136
rt v[214:215],v242 offset:60160
mm v[60:63],v[220:223],a[36:39],v[60:63]
ar v231,a244
v_perm_b32 v232, v231, v230, s86
mm v[60:63],v[216:219],a[52:55],v[60:63]
v_perm_b32 v233, v231, v230, s87
aw a194,v232
mm v[28:31],v[216:219],a[48:51],v[28:31]
wt vmcnt(24) lgkmcnt(4)
aw a198,v233
mm v[28:31],v[220:223],a[32:35],v[28:31]
rt v[216:217],v243 offset:50688
rt v[218:219],v243 offset:51712
mm v[0:3],v[192:195],v[128:131],v[0:3]
ar v234,a248
ar v235,a252
mm v[0:3],v[196:199],v[136:139],v[0:3]
ld a[12:15],v245,s[44:47],s55 offen offset:1024 sc1
mm v[32:35],v[196:199],v[140:143],v[32:35]
v_perm_b32 v236, v235, v234, s86
v_perm_b32 v237, v235, v234, s87
mm v[32:35],v[192:195],v[132:135],v[32:35]
rt v[220:221],v243 offset:59136
rt v[222:223],v243 offset:60160
mm v[64:67],v[192:195],v[144:147],v[64:67]
aw a195,v236
aw a199,v237
mm v[64:67],v[196:199],v[152:155],v[64:67]
ld a[16:19],v244,s[44:47],s55 offen offset:2048 sc1
mm v[96:99],v[196:199],v[168:171],v[96:99]
ar v230,a225
ar v231,a229
mm v[96:99],v[192:195],v[156:159],v[96:99]
rt v[196:197],v240 offset:50816
rt v[198:199],v240 offset:51840
mm v[100:103],v[200:203],v[156:159],v[100:103]
v_perm_b32 v232, v231, v230, s86
v_perm_b32 v233, v231, v230, s87
mm v[100:103],v[204:207],v[168:171],v[100:103]
ld a[20:23],v245,s[44:47],s55 offen offset:2048 sc1
mm v[68:71],v[204:207],v[152:155],v[68:71]
aw a200,v232
aw a204,v233
mm v[68:71],v[200:203],v[144:147],v[68:71]
rt v[192:193],v240 offset:59264
rt v[194:195],v240 offset:60288
mm v[36:39],v[200:203],v[132:135],v[36:39]
v_add_u32 v240, 67584, v240
ar v234,a233
mm v[36:39],v[204:207],v[140:143],v[36:39]
)ASM" R"ASM(ld a[24:27],v244,s[44:47],s55 offen offset:3072 sc1
mm v[4:7],v[204:207],v[136:139],v[4:7]
wt lgkmcnt(4)
ar v235,a237
mm v[4:7],v[200:203],v[128:131],v[4:7]
rt v[204:205],v241 offset:50816
rt v[206:207],v241 offset:51840
mm v[8:11],v[208:211],v[128:131],v[8:11]
v_perm_b32 v236, v235, v234, s86
v_perm_b32 v237, v235, v234, s87
mm v[8:11],v[212:215],v[136:139],v[8:11]
ld a[28:31],v245,s[44:47],s55 offen offset:3072 sc1
mm v[40:43],v[212:215],v[140:143],v[40:43]
aw a201,v236
aw a205,v237
mm v[40:43],v[208:211],v[132:135],v[40:43]
rt v[200:201],v241 offset:59264
rt v[202:203],v241 offset:60288
mm v[72:75],v[208:211],v[144:147],v[72:75]
v_add_u32 v241, 67584, v241
ar v230,a241
mm v[72:75],v[212:215],v[152:155],v[72:75]
ar v231,a245
v_perm_b32 v232, v231, v230, s86
mm v[104:107],v[212:215],v[168:171],v[104:107]
v_perm_b32 v233, v231, v230, s87
aw a202,v232
mm v[104:107],v[208:211],v[156:159],v[104:107]
rt v[212:213],v242 offset:50816
rt v[214:215],v242 offset:51840
mm v[108:111],v[216:219],v[156:159],v[108:111]
aw a206,v233
ar v234,a249
mm v[108:111],v[220:223],v[168:171],v[108:111]
ar v235,a253
v_perm_b32 v236, v235, v234, s86
mm v[76:79],v[220:223],v[152:155],v[76:79]
v_perm_b32 v237, v235, v234, s87
aw a203,v236
mm v[76:79],v[216:219],v[144:147],v[76:79]
rt v[208:209],v242 offset:59264
rt v[210:211],v242 offset:60288
mm v[44:47],v[216:219],v[132:135],v[44:47]
v_add_u32 v242, 67584, v242
aw a207,v237
mm v[44:47],v[220:223],v[140:143],v[44:47]
ar v230,a226
ar v231,a230
mm v[12:15],v[220:223],v[136:139],v[12:15]
wt lgkmcnt(4)
v_perm_b32 v232, v231, v230, s86
mm v[12:15],v[216:219],v[128:131],v[12:15]
rt v[220:221],v243 offset:50816
rt v[222:223],v243 offset:51840
mm v[16:19],v[196:199],v[128:131],v[16:19]
v_perm_b32 v233, v231, v230, s87
aw a208,v232
mm v[16:19],v[192:195],v[136:139],v[16:19]
ld a[32:35],v246,s[44:47],s54 offen offset:0 sc1
mm v[48:51],v[192:195],v[140:143],v[48:51]
aw a212,v233
ar v234,a234
mm v[48:51],v[196:199],v[132:135],v[48:51]
rt v[216:217],v243 offset:59264
rt v[218:219],v243 offset:60288
mm v[80:83],v[196:199],v[144:147],v[80:83]
v_add_u32 v243, 67584, v243
ar v235,a238
mm v[80:83],v[192:195],v[152:155],v[80:83]
ld a[36:39],v247,s[44:47],s54 offen offset:0 sc1
mm v[112:115],v[192:195],v[168:171],v[112:115]
wt vmcnt(18)
s_barrier
mm v[112:115],v[196:199],v[156:159],v[112:115]
rt v[192:193],v240 offset:50688
rt v[194:195],v240 offset:51712
mm v[116:119],v[204:207],v[156:159],v[116:119]
v_perm_b32 v236, v235, v234, s86
v_perm_b32 v237, v235, v234, s87
mm v[116:119],v[200:203],v[168:171],v[116:119]
ld a[40:43],v246,s[44:47],s54 offen offset:1024 sc1
mm v[84:87],v[200:203],v[152:155],v[84:87]
aw a209,v236
aw a213,v237
mm v[84:87],v[204:207],v[144:147],v[84:87]
rt v[196:197],v240 offset:59136
rt v[198:199],v240 offset:60160
mm v[52:55],v[204:207],v[132:135],v[52:55]
ar v230,a242
ar v231,a246
mm v[52:55],v[200:203],v[140:143],v[52:55]
ld a[44:47],v247,s[44:47],s54 offen offset:1024 sc1
mm v[20:23],v[200:203],v[136:139],v[20:23]
wt lgkmcnt(4)
v_perm_b32 v232, v231, v230, s86
mm v[20:23],v[204:207],v[128:131],v[20:23]
rt v[200:201],v240 offset:0
rt v[202:203],v240 offset:1024
mm v[24:27],v[212:215],v[128:131],v[24:27]
v_perm_b32 v233, v231, v230, s87
)ASM" R"ASM(aw a210,v232
mm v[24:27],v[208:211],v[136:139],v[24:27]
ld a[48:51],v246,s[44:47],s54 offen offset:2048 sc1
mm v[56:59],v[208:211],v[140:143],v[56:59]
aw a214,v233
ar v234,a250
mm v[56:59],v[212:215],v[132:135],v[56:59]
rt v[204:205],v240 offset:8448
rt v[206:207],v240 offset:9472
mm v[88:91],v[212:215],v[144:147],v[88:91]
ar v235,a254
v_perm_b32 v236, v235, v234, s86
mm v[88:91],v[208:211],v[152:155],v[88:91]
ld a[52:55],v247,s[44:47],s54 offen offset:2048 sc1
mm v[120:123],v[208:211],v[168:171],v[120:123]
v_perm_b32 v237, v235, v234, s87
aw a211,v236
mm v[120:123],v[212:215],v[156:159],v[120:123]
rt v[208:209],v240 offset:16896
rt v[210:211],v240 offset:17920
mm v[124:127],v[220:223],v[156:159],v[124:127]
aw a215,v237
ar v230,a227
mm v[124:127],v[216:219],v[168:171],v[124:127]
ld a[56:59],v246,s[44:47],s54 offen offset:3072 sc1
.La5_LW7_4:
s_cbranch_execz .La5_LA_RETURN
mm v[92:95],v[216:219],v[152:155],v[92:95]
ar v231,a231
v_perm_b32 v232, v231, v230, s86
mm v[92:95],v[220:223],v[144:147],v[92:95]
rt v[212:213],v240 offset:25344
rt v[214:215],v240 offset:26368
mm v[60:63],v[220:223],v[132:135],v[60:63]
v_perm_b32 v233, v231, v230, s87
aw a216,v232
mm v[60:63],v[216:219],v[140:143],v[60:63]
aw a220,v233
ar v234,a235
mm v[28:31],v[216:219],v[136:139],v[28:31]
wt vmcnt(15) lgkmcnt(4)
ar v235,a239
mm v[28:31],v[220:223],v[128:131],v[28:31]
rt v[216:217],v240 offset:33792
rt v[218:219],v240 offset:34816
mm v[0:3],v[192:195],a[64:67],v[0:3]
ld a[60:63],v247,s[44:47],s54 offen offset:3072 sc1
mm v[0:3],v[196:199],a[72:75],v[0:3]
rt v[220:221],v240 offset:42240
rt v[222:223],v240 offset:43264
mm v[32:35],v[196:199],a[76:79],v[32:35]
v_perm_b32 v236, v235, v234, s86
v_perm_b32 v237, v235, v234, s87
mm v[32:35],v[192:195],a[68:71],v[32:35]
rt v[196:197],v241 offset:50688
rt v[198:199],v241 offset:51712
mm v[32:35],v[200:203],a[84:87],v[32:35]
ld v[128:131],v246,s[44:47],s55 offen offset:0 sc1
mm v[32:35],v[204:207],a[92:95],v[32:35]
rt v[192:193],v241 offset:59136
rt v[194:195],v241 offset:60160
mm v[0:3],v[204:207],a[88:91],v[0:3]
wt vmcnt(9) lgkmcnt(4)
aw a217,v236
mm v[0:3],v[200:203],a[80:83],v[0:3]
rt v[204:205],v241 offset:0
rt v[206:207],v241 offset:1024
mm v[0:3],v[208:211],a[0:3],v[0:3]
ld v[132:135],v247,s[44:47],s55 offen offset:0 sc1
mm v[0:3],v[212:215],a[8:11],v[0:3]
rt v[200:201],v241 offset:8448
rt v[202:203],v241 offset:9472
mm v[32:35],v[212:215],a[12:15],v[32:35]
aw a221,v237
ar v230,a243
mm v[32:35],v[208:211],a[4:7],v[32:35]
rt v[212:213],v241 offset:16896
rt v[214:215],v241 offset:17920
mm v[32:35],v[216:219],a[20:23],v[32:35]
ld v[144:147],v246,s[44:47],s55 offen offset:1024 sc1
mm v[32:35],v[220:223],a[28:31],v[32:35]
rt v[208:209],v241 offset:25344
rt v[210:211],v241 offset:26368
mm v[0:3],v[220:223],a[24:27],v[0:3]
wt lgkmcnt(4)
ar v231,a247
mm v[0:3],v[216:219],a[16:19],v[0:3]
rt v[220:221],v241 offset:33792
rt v[222:223],v241 offset:34816
mm v[4:7],v[196:199],a[64:67],v[4:7]
ld v[148:151],v247,s[44:47],s55 offen offset:1024 sc1
mm v[4:7],v[192:195],a[72:75],v[4:7]
rt v[216:217],v241 offset:42240
rt v[218:219],v241 offset:43264
mm v[36:39],v[192:195],a[76:79],v[36:39]
v_perm_b32 v232, v231, v230, s86
v_perm_b32 v233, v231, v230, s87
mm v[36:39],v[196:199],a[68:71],v[36:39]
rt v[192:193],v242 offset:50688
rt v[194:195],v242 offset:51712
)ASM" R"ASM(mm v[36:39],v[204:207],a[84:87],v[36:39]
ld v[160:163],v246,s[44:47],s55 offen offset:2048 sc1
mm v[36:39],v[200:203],a[92:95],v[36:39]
rt v[196:197],v242 offset:59136
rt v[198:199],v242 offset:60160
aw a218,v232
aw a222,v233
ar v234,a251
ar v235,a255
v_perm_b32 v236, v235, v234, s86
v_perm_b32 v237, v235, v234, s87
aw a219,v236
aw a223,v237
mm v[4:7],v[200:203],a[88:91],v[4:7]
wt lgkmcnt(4)
mm v[4:7],v[204:207],a[80:83],v[4:7]
rt v[200:201],v242 offset:0
rt v[202:203],v242 offset:1024
mm v[4:7],v[212:215],a[0:3],v[4:7]
ld v[164:167],v247,s[44:47],s55 offen offset:2048 sc1
mm v[4:7],v[208:211],a[8:11],v[4:7]
rt v[204:205],v242 offset:8448
rt v[206:207],v242 offset:9472
mm v[36:39],v[208:211],a[12:15],v[36:39]
mm v[36:39],v[212:215],a[4:7],v[36:39]
rt v[208:209],v242 offset:16896
rt v[210:211],v242 offset:17920
mm v[36:39],v[220:223],a[20:23],v[36:39]
mm v[36:39],v[216:219],a[28:31],v[36:39]
rt v[212:213],v242 offset:25344
rt v[214:215],v242 offset:26368
mm v[4:7],v[216:219],a[24:27],v[4:7]
wt lgkmcnt(4)
mm v[4:7],v[220:223],a[16:19],v[4:7]
rt v[216:217],v242 offset:33792
rt v[218:219],v242 offset:34816
mm v[8:11],v[192:195],a[64:67],v[8:11]
ld v[176:179],v246,s[44:47],s55 offen offset:3072 sc1
mm v[8:11],v[196:199],a[72:75],v[8:11]
rt v[220:221],v242 offset:42240
rt v[222:223],v242 offset:43264
mm v[40:43],v[196:199],a[76:79],v[40:43]
cv v234,v32,v33
cv v236,v36,v37
mm v[40:43],v[192:195],a[68:71],v[40:43]
rt v[196:197],v243 offset:50688
rt v[198:199],v243 offset:51712
mm v[40:43],v[200:203],a[84:87],v[40:43]
ld v[180:183],v247,s[44:47],s55 offen offset:3072 sc1
mm v[40:43],v[204:207],a[92:95],v[40:43]
rt v[192:193],v243 offset:59136
rt v[194:195],v243 offset:60160
mm v[8:11],v[204:207],a[88:91],v[8:11]
wt lgkmcnt(4)
v_bfe_u32 v250, v255, 6, 1
mm v[8:11],v[200:203],a[80:83],v[8:11]
rt v[204:205],v243 offset:0
rt v[206:207],v243 offset:1024
mm v[8:11],v[208:211],a[0:3],v[8:11]
cv v235,v34,v35
cv v237,v38,v39
mm v[8:11],v[212:215],a[8:11],v[8:11]
rt v[200:201],v243 offset:8448
rt v[202:203],v243 offset:9472
mm v[40:43],v[212:215],a[12:15],v[40:43]
v_permlane16_swap_b32 v234, v236
v_permlane16_swap_b32 v235, v237
mm v[40:43],v[208:211],a[4:7],v[40:43]
rt v[212:213],v243 offset:16896
rt v[214:215],v243 offset:17920
mm v[40:43],v[216:219],a[20:23],v[40:43]
st v[234:237],v238,s[32:35],s95 offen offset:0 sc1
mm v[40:43],v[220:223],a[28:31],v[40:43]
rt v[208:209],v243 offset:25344
rt v[210:211],v243 offset:26368
mm v[8:11],v[220:223],a[24:27],v[8:11]
wt lgkmcnt(4)
v_mul_u32_u24 v250, 7, v250
mm v[8:11],v[216:219],a[16:19],v[8:11]
rt v[220:221],v243 offset:33792
rt v[222:223],v243 offset:34816
mm v[12:15],v[196:199],a[64:67],v[12:15]
cv v230,v0,v1
cv v232,v4,v5
mm v[12:15],v[192:195],a[72:75],v[12:15]
rt v[216:217],v243 offset:42240
rt v[218:219],v243 offset:43264
mm v[44:47],v[192:195],a[76:79],v[44:47]
cv v231,v2,v3
cv v233,v6,v7
mm v[44:47],v[196:199],a[68:71],v[44:47]
rt v[192:193],v240 offset:50816
rt v[194:195],v240 offset:51840
mm v[44:47],v[204:207],a[84:87],v[44:47]
v_permlane16_swap_b32 v230, v232
v_permlane16_swap_b32 v231, v233
mm v[44:47],v[200:203],a[92:95],v[44:47]
rt v[196:197],v240 offset:59264
rt v[198:199],v240 offset:60288
mm v[12:15],v[200:203],a[88:91],v[12:15]
)ASM" R"ASM(wt lgkmcnt(4)
v_xor_b32 v250, s3, v250
mm v[12:15],v[204:207],a[80:83],v[12:15]
rt v[200:201],v240 offset:128
rt v[202:203],v240 offset:1152
mm v[12:15],v[212:215],a[0:3],v[12:15]
st v[230:233],v238,s[32:35],s94 offen offset:0 sc1
mm v[12:15],v[208:211],a[8:11],v[12:15]
rt v[204:205],v240 offset:8576
rt v[206:207],v240 offset:9600
mm v[44:47],v[208:211],a[12:15],v[44:47]
v_bfe_u32 v251, v255, 4, 2
v_lshl_add_u32 v250, v250, 2, v251
mm v[44:47],v[212:215],a[4:7],v[44:47]
rt v[208:209],v240 offset:17024
rt v[210:211],v240 offset:18048
mm v[44:47],v[220:223],a[20:23],v[44:47]
ld v[136:139],v224,s[68:71],0 offen offset:0
mm v[44:47],v[216:219],a[28:31],v[44:47]
rt v[212:213],v240 offset:25472
rt v[214:215],v240 offset:26496
mm v[12:15],v[216:219],a[24:27],v[12:15]
wt lgkmcnt(4)
v_lshlrev_b32 v250, 9, v250
mm v[12:15],v[220:223],a[16:19],v[12:15]
rt v[216:217],v240 offset:33920
rt v[218:219],v240 offset:34944
mm v[16:19],v[192:195],a[64:67],v[16:19]
cv v234,v40,v41
cv v236,v44,v45
mm v[16:19],v[196:199],a[72:75],v[16:19]
rt v[220:221],v240 offset:42368
rt v[222:223],v240 offset:43392
mm v[48:51],v[196:199],a[76:79],v[48:51]
cv v235,v42,v43
cv v237,v46,v47
mm v[48:51],v[192:195],a[68:71],v[48:51]
rt v[196:197],v241 offset:50816
rt v[198:199],v241 offset:51840
mm v[48:51],v[200:203],a[84:87],v[48:51]
v_permlane16_swap_b32 v234, v236
v_permlane16_swap_b32 v235, v237
mm v[48:51],v[204:207],a[92:95],v[48:51]
rt v[192:193],v241 offset:59264
rt v[194:195],v241 offset:60288
mm v[16:19],v[204:207],a[88:91],v[16:19]
wt lgkmcnt(4)
v_and_b32 v251, 64, v255
mm v[16:19],v[200:203],a[80:83],v[16:19]
rt v[204:205],v241 offset:128
rt v[206:207],v241 offset:1152
mm v[16:19],v[208:211],a[0:3],v[16:19]
st v[234:237],v238,s[32:35],s95 offen offset:1024 sc1
mm v[16:19],v[212:215],a[8:11],v[16:19]
rt v[200:201],v241 offset:8576
rt v[202:203],v241 offset:9600
mm v[48:51],v[212:215],a[12:15],v[48:51]
cv v230,v8,v9
cv v232,v12,v13
mm v[48:51],v[208:211],a[4:7],v[48:51]
rt v[212:213],v241 offset:17024
rt v[214:215],v241 offset:18048
mm v[48:51],v[216:219],a[20:23],v[48:51]
cv v231,v10,v11
cv v233,v14,v15
mm v[48:51],v[220:223],a[28:31],v[48:51]
rt v[208:209],v241 offset:25472
rt v[210:211],v241 offset:26496
mm v[16:19],v[220:223],a[24:27],v[16:19]
wt lgkmcnt(4)
v_lshl_add_u32 v251, v251, 24, v250
mm v[16:19],v[216:219],a[16:19],v[16:19]
rt v[220:221],v241 offset:33920
rt v[222:223],v241 offset:34944
mm v[20:23],v[196:199],a[64:67],v[20:23]
v_permlane16_swap_b32 v230, v232
v_permlane16_swap_b32 v231, v233
mm v[20:23],v[192:195],a[72:75],v[20:23]
rt v[216:217],v241 offset:42368
rt v[218:219],v241 offset:43392
mm v[52:55],v[192:195],a[76:79],v[52:55]
v_and_b32 v248, 0x1f0, v255
v_lshrrev_b32 v248, 1, v248
mm v[52:55],v[196:199],a[68:71],v[52:55]
rt v[192:193],v242 offset:50816
rt v[194:195],v242 offset:51840
mm v[52:55],v[204:207],a[84:87],v[52:55]
st v[230:233],v238,s[32:35],s94 offen offset:1024 sc1
mm v[52:55],v[200:203],a[92:95],v[52:55]
rt v[196:197],v242 offset:59264
rt v[198:199],v242 offset:60288
mm v[20:23],v[200:203],a[88:91],v[20:23]
wt lgkmcnt(4)
mm v[20:23],v[204:207],a[80:83],v[20:23]
rt v[200:201],v242 offset:128
rt v[202:203],v242 offset:1152
mm v[20:23],v[212:215],a[0:3],v[20:23]
ld v[140:143],v225,s[48:51],0 offen offset:0
mm v[20:23],v[208:211],a[8:11],v[20:23]
rt v[204:205],v242 offset:8576
)ASM" R"ASM(rt v[206:207],v242 offset:9600
mm v[52:55],v[208:211],a[12:15],v[52:55]
mm v[52:55],v[212:215],a[4:7],v[52:55]
rt v[208:209],v242 offset:17024
rt v[210:211],v242 offset:18048
mm v[52:55],v[220:223],a[20:23],v[52:55]
ld v[152:155],v224,s[68:71],0 offen offset:512
mm v[52:55],v[216:219],a[28:31],v[52:55]
rt v[212:213],v242 offset:25472
rt v[214:215],v242 offset:26496
mm v[20:23],v[216:219],a[24:27],v[20:23]
wt lgkmcnt(4)
s_mov_b32 s93, 0
ld a[224:227],v229,s[28:31],s93 offen offset:0
mm v[20:23],v[220:223],a[16:19],v[20:23]
rt v[216:217],v242 offset:33920
rt v[218:219],v242 offset:34944
mm v[24:27],v[192:195],a[64:67],v[24:27]
cv v234,v48,v49
cv v236,v52,v53
mm v[24:27],v[196:199],a[72:75],v[24:27]
rt v[220:221],v242 offset:42368
rt v[222:223],v242 offset:43392
mm v[56:59],v[196:199],a[76:79],v[56:59]
cv v235,v50,v51
cv v237,v54,v55
mm v[56:59],v[192:195],a[68:71],v[56:59]
rt v[196:197],v243 offset:50816
rt v[198:199],v243 offset:51840
mm v[56:59],v[200:203],a[84:87],v[56:59]
v_permlane16_swap_b32 v234, v236
v_permlane16_swap_b32 v235, v237
mm v[56:59],v[204:207],a[92:95],v[56:59]
rt v[192:193],v243 offset:59264
rt v[194:195],v243 offset:60288
mm v[24:27],v[204:207],a[88:91],v[24:27]
wt lgkmcnt(4)
ld a[228:231],v229,s[28:31],s93 offen offset:2048
mm v[24:27],v[200:203],a[80:83],v[24:27]
rt v[204:205],v243 offset:128
rt v[206:207],v243 offset:1152
mm v[24:27],v[208:211],a[0:3],v[24:27]
st v[234:237],v238,s[32:35],s95 offen offset:2048 sc1
mm v[24:27],v[212:215],a[8:11],v[24:27]
rt v[200:201],v243 offset:8576
rt v[202:203],v243 offset:9600
mm v[56:59],v[212:215],a[12:15],v[56:59]
cv v230,v16,v17
cv v232,v20,v21
mm v[56:59],v[208:211],a[4:7],v[56:59]
rt v[212:213],v243 offset:17024
rt v[214:215],v243 offset:18048
mm v[56:59],v[216:219],a[20:23],v[56:59]
cv v231,v18,v19
cv v233,v22,v23
mm v[56:59],v[220:223],a[28:31],v[56:59]
rt v[208:209],v243 offset:25472
rt v[210:211],v243 offset:26496
mm v[24:27],v[220:223],a[24:27],v[24:27]
wt lgkmcnt(4)
s_mov_b32 s93, 4096
ld a[232:235],v229,s[28:31],s93 offen offset:0
mm v[24:27],v[216:219],a[16:19],v[24:27]
rt v[220:221],v243 offset:33920
rt v[222:223],v243 offset:34944
mm v[28:31],v[196:199],a[64:67],v[28:31]
v_permlane16_swap_b32 v230, v232
v_permlane16_swap_b32 v231, v233
mm v[28:31],v[192:195],a[72:75],v[28:31]
rt v[216:217],v243 offset:42368
rt v[218:219],v243 offset:43392
mm v[60:63],v[192:195],a[76:79],v[60:63]
ld a[236:239],v229,s[28:31],s93 offen offset:2048
mm v[60:63],v[196:199],a[68:71],v[60:63]
rt v[192:193],v240 offset:50688
rt v[194:195],v240 offset:51712
mm v[60:63],v[204:207],a[84:87],v[60:63]
st v[230:233],v238,s[32:35],s94 offen offset:2048 sc1
mm v[60:63],v[200:203],a[92:95],v[60:63]
rt v[196:197],v240 offset:59136
rt v[198:199],v240 offset:60160
mm v[28:31],v[200:203],a[88:91],v[28:31]
wt lgkmcnt(4)
s_mov_b32 s93, 8192
ld a[240:243],v229,s[28:31],s93 offen offset:0
mm v[28:31],v[204:207],a[80:83],v[28:31]
rt v[200:201],v240 offset:0
rt v[202:203],v240 offset:1024
mm v[28:31],v[212:215],a[0:3],v[28:31]
ld v[156:159],v225,s[48:51],0 offen offset:512
mm v[28:31],v[208:211],a[8:11],v[28:31]
rt v[204:205],v240 offset:8448
rt v[206:207],v240 offset:9472
mm v[60:63],v[208:211],a[12:15],v[60:63]
ld a[244:247],v229,s[28:31],s93 offen offset:2048
mm v[60:63],v[212:215],a[4:7],v[60:63]
rt v[208:209],v240 offset:16896
rt v[210:211],v240 offset:17920
mm v[60:63],v[220:223],a[20:23],v[60:63]
ld v[168:171],v224,s[68:71],0 offen offset:1024
)ASM" R"ASM(mm v[60:63],v[216:219],a[28:31],v[60:63]
rt v[212:213],v240 offset:25344
rt v[214:215],v240 offset:26368
mm v[28:31],v[216:219],a[24:27],v[28:31]
wt vmcnt(25) lgkmcnt(4)
s_mov_b32 s93, 12288
ld a[248:251],v229,s[28:31],s93 offen offset:0
mm v[28:31],v[220:223],a[16:19],v[28:31]
rt v[216:217],v240 offset:33792
rt v[218:219],v240 offset:34816
mm v[64:67],v[192:195],a[32:35],v[64:67]
cv v234,v56,v57
cv v236,v60,v61
mm v[64:67],v[196:199],a[40:43],v[64:67]
rt v[220:221],v240 offset:42240
rt v[222:223],v240 offset:43264
mm v[96:99],v[196:199],a[44:47],v[96:99]
cv v235,v58,v59
cv v237,v62,v63
mm v[96:99],v[192:195],a[36:39],v[96:99]
rt v[196:197],v241 offset:50688
rt v[198:199],v241 offset:51712
mm v[96:99],v[200:203],a[52:55],v[96:99]
v_permlane16_swap_b32 v234, v236
v_permlane16_swap_b32 v235, v237
mm v[96:99],v[204:207],a[60:63],v[96:99]
rt v[192:193],v241 offset:59136
rt v[194:195],v241 offset:60160
mm v[64:67],v[204:207],a[56:59],v[64:67]
wt vmcnt(18) lgkmcnt(4)
ld a[252:255],v229,s[28:31],s93 offen offset:2048
mm v[64:67],v[200:203],a[48:51],v[64:67]
rt v[204:205],v241 offset:0
rt v[206:207],v241 offset:1024
mm v[64:67],v[208:211],v[128:131],v[64:67]
st v[234:237],v238,s[32:35],s95 offen offset:3072 sc1
mm v[64:67],v[212:215],v[144:147],v[64:67]
rt v[200:201],v241 offset:8448
rt v[202:203],v241 offset:9472
mm v[96:99],v[212:215],v[148:151],v[96:99]
cv v230,v24,v25
cv v232,v28,v29
mm v[96:99],v[208:211],v[132:135],v[96:99]
rt v[212:213],v241 offset:16896
rt v[214:215],v241 offset:17920
mm v[96:99],v[216:219],v[164:167],v[96:99]
cv v231,v26,v27
cv v233,v30,v31
mm v[96:99],v[220:223],v[180:183],v[96:99]
rt v[208:209],v241 offset:25344
rt v[210:211],v241 offset:26368
mm v[64:67],v[220:223],v[176:179],v[64:67]
wt lgkmcnt(4)
s_mov_b32 s93, 0
ld a[64:67],v229,s[28:31],s93 offen offset:256
mm v[64:67],v[216:219],v[160:163],v[64:67]
rt v[220:221],v241 offset:33792
rt v[222:223],v241 offset:34816
mm v[68:71],v[196:199],a[32:35],v[68:71]
v_permlane16_swap_b32 v230, v232
v_permlane16_swap_b32 v231, v233
mm v[68:71],v[192:195],a[40:43],v[68:71]
rt v[216:217],v241 offset:42240
rt v[218:219],v241 offset:43264
mm v[100:103],v[192:195],a[44:47],v[100:103]
ld a[68:71],v229,s[28:31],s93 offen offset:2304
mm v[100:103],v[196:199],a[36:39],v[100:103]
rt v[192:193],v242 offset:50688
rt v[194:195],v242 offset:51712
mm v[100:103],v[204:207],a[52:55],v[100:103]
st v[230:233],v238,s[32:35],s94 offen offset:3072 sc1
mm v[100:103],v[200:203],a[60:63],v[100:103]
rt v[196:197],v242 offset:59136
rt v[198:199],v242 offset:60160
mm v[68:71],v[200:203],a[56:59],v[68:71]
wt lgkmcnt(4)
s_mov_b32 s93, 4096
ld a[72:75],v229,s[28:31],s93 offen offset:256
mm v[68:71],v[204:207],a[48:51],v[68:71]
rt v[200:201],v242 offset:0
rt v[202:203],v242 offset:1024
mm v[68:71],v[212:215],v[128:131],v[68:71]
ld v[172:175],v225,s[48:51],0 offen offset:1024
mm v[68:71],v[208:211],v[144:147],v[68:71]
rt v[204:205],v242 offset:8448
rt v[206:207],v242 offset:9472
mm v[100:103],v[208:211],v[148:151],v[100:103]
ld a[76:79],v229,s[28:31],s93 offen offset:2304
mm v[100:103],v[212:215],v[132:135],v[100:103]
rt v[208:209],v242 offset:16896
rt v[210:211],v242 offset:17920
mm v[100:103],v[220:223],v[164:167],v[100:103]
ld v[184:187],v224,s[68:71],0 offen offset:1536
mm v[100:103],v[216:219],v[180:183],v[100:103]
rt v[212:213],v242 offset:25344
rt v[214:215],v242 offset:26368
mm v[68:71],v[216:219],v[176:179],v[68:71]
wt lgkmcnt(4)
s_mov_b32 s93, 8192
ld a[80:83],v229,s[28:31],s93 offen offset:256
)ASM" R"ASM(mm v[68:71],v[220:223],v[160:163],v[68:71]
rt v[216:217],v242 offset:33792
rt v[218:219],v242 offset:34816
mm v[72:75],v[192:195],a[32:35],v[72:75]
cv v234,v96,v97
cv v236,v100,v101
mm v[72:75],v[196:199],a[40:43],v[72:75]
rt v[220:221],v242 offset:42240
rt v[222:223],v242 offset:43264
mm v[104:107],v[196:199],a[44:47],v[104:107]
cv v235,v98,v99
cv v237,v102,v103
mm v[104:107],v[192:195],a[36:39],v[104:107]
rt v[196:197],v243 offset:50688
rt v[198:199],v243 offset:51712
mm v[104:107],v[200:203],a[52:55],v[104:107]
v_permlane16_swap_b32 v234, v236
v_permlane16_swap_b32 v235, v237
mm v[104:107],v[204:207],a[60:63],v[104:107]
rt v[192:193],v243 offset:59136
rt v[194:195],v243 offset:60160
mm v[72:75],v[204:207],a[56:59],v[72:75]
wt lgkmcnt(4)
ld a[84:87],v229,s[28:31],s93 offen offset:2304
mm v[72:75],v[200:203],a[48:51],v[72:75]
rt v[204:205],v243 offset:0
rt v[206:207],v243 offset:1024
mm v[72:75],v[208:211],v[128:131],v[72:75]
st v[234:237],v238,s[32:35],s97 offen offset:0 sc1
mm v[72:75],v[212:215],v[144:147],v[72:75]
rt v[200:201],v243 offset:8448
rt v[202:203],v243 offset:9472
mm v[104:107],v[212:215],v[148:151],v[104:107]
cv v230,v64,v65
cv v232,v68,v69
mm v[104:107],v[208:211],v[132:135],v[104:107]
rt v[212:213],v243 offset:16896
rt v[214:215],v243 offset:17920
mm v[104:107],v[216:219],v[164:167],v[104:107]
cv v231,v66,v67
cv v233,v70,v71
mm v[104:107],v[220:223],v[180:183],v[104:107]
rt v[208:209],v243 offset:25344
rt v[210:211],v243 offset:26368
mm v[72:75],v[220:223],v[176:179],v[72:75]
wt lgkmcnt(4)
s_mov_b32 s93, 12288
ld a[88:91],v229,s[28:31],s93 offen offset:256
mm v[72:75],v[216:219],v[160:163],v[72:75]
rt v[220:221],v243 offset:33792
rt v[222:223],v243 offset:34816
mm v[76:79],v[196:199],a[32:35],v[76:79]
v_permlane16_swap_b32 v230, v232
v_permlane16_swap_b32 v231, v233
mm v[76:79],v[192:195],a[40:43],v[76:79]
rt v[216:217],v243 offset:42240
rt v[218:219],v243 offset:43264
mm v[108:111],v[192:195],a[44:47],v[108:111]
ld a[92:95],v229,s[28:31],s93 offen offset:2304
mm v[108:111],v[196:199],a[36:39],v[108:111]
rt v[192:193],v240 offset:50816
rt v[194:195],v240 offset:51840
mm v[108:111],v[204:207],a[52:55],v[108:111]
.La5_LW7_5:
s_cbranch_execz .La5_LA_RETURN
st v[230:233],v238,s[32:35],s96 offen offset:0 sc1
mm v[108:111],v[200:203],a[60:63],v[108:111]
rt v[196:197],v240 offset:59264
rt v[198:199],v240 offset:60288
mm v[76:79],v[200:203],a[56:59],v[76:79]
wt lgkmcnt(4)
mm v[76:79],v[204:207],a[48:51],v[76:79]
rt v[200:201],v240 offset:128
rt v[202:203],v240 offset:1152
mm v[76:79],v[212:215],v[128:131],v[76:79]
ld v[188:191],v225,s[48:51],0 offen offset:1536
mm v[76:79],v[208:211],v[144:147],v[76:79]
rt v[204:205],v240 offset:8576
rt v[206:207],v240 offset:9600
mm v[108:111],v[208:211],v[148:151],v[108:111]
mm v[108:111],v[212:215],v[132:135],v[108:111]
rt v[208:209],v240 offset:17024
rt v[210:211],v240 offset:18048
mm v[108:111],v[220:223],v[164:167],v[108:111]
mm v[108:111],v[216:219],v[180:183],v[108:111]
rt v[212:213],v240 offset:25472
rt v[214:215],v240 offset:26496
mm v[76:79],v[216:219],v[176:179],v[76:79]
wt lgkmcnt(4)
mm v[76:79],v[220:223],v[160:163],v[76:79]
rt v[216:217],v240 offset:33920
rt v[218:219],v240 offset:34944
mm v[80:83],v[192:195],a[32:35],v[80:83]
cv v234,v104,v105
cv v236,v108,v109
mm v[80:83],v[196:199],a[40:43],v[80:83]
rt v[220:221],v240 offset:42368
rt v[222:223],v240 offset:43392
mm v[112:115],v[196:199],a[44:47],v[112:115]
cv v235,v106,v107
)ASM" R"ASM(cv v237,v110,v111
mm v[112:115],v[192:195],a[36:39],v[112:115]
rt v[196:197],v241 offset:50816
rt v[198:199],v241 offset:51840
mm v[112:115],v[200:203],a[52:55],v[112:115]
v_permlane16_swap_b32 v234, v236
v_permlane16_swap_b32 v235, v237
mm v[112:115],v[204:207],a[60:63],v[112:115]
rt v[192:193],v241 offset:59264
rt v[194:195],v241 offset:60288
mm v[80:83],v[204:207],a[56:59],v[80:83]
wt lgkmcnt(4)
mm v[80:83],v[200:203],a[48:51],v[80:83]
rt v[204:205],v241 offset:128
rt v[206:207],v241 offset:1152
mm v[80:83],v[208:211],v[128:131],v[80:83]
st v[234:237],v238,s[32:35],s97 offen offset:1024 sc1
mm v[80:83],v[212:215],v[144:147],v[80:83]
rt v[200:201],v241 offset:8576
rt v[202:203],v241 offset:9600
mm v[112:115],v[212:215],v[148:151],v[112:115]
cv v230,v72,v73
cv v232,v76,v77
mm v[112:115],v[208:211],v[132:135],v[112:115]
rt v[212:213],v241 offset:17024
rt v[214:215],v241 offset:18048
mm v[112:115],v[216:219],v[164:167],v[112:115]
cv v231,v74,v75
cv v233,v78,v79
mm v[112:115],v[220:223],v[180:183],v[112:115]
rt v[208:209],v241 offset:25472
rt v[210:211],v241 offset:26496
mm v[80:83],v[220:223],v[176:179],v[80:83]
wt lgkmcnt(4)
mm v[80:83],v[216:219],v[160:163],v[80:83]
rt v[220:221],v241 offset:33920
rt v[222:223],v241 offset:34944
mm v[84:87],v[196:199],a[32:35],v[84:87]
v_permlane16_swap_b32 v230, v232
v_permlane16_swap_b32 v231, v233
mm v[84:87],v[192:195],a[40:43],v[84:87]
rt v[216:217],v241 offset:42368
rt v[218:219],v241 offset:43392
mm v[116:119],v[192:195],a[44:47],v[116:119]
mm v[116:119],v[196:199],a[36:39],v[116:119]
rt v[192:193],v242 offset:50816
rt v[194:195],v242 offset:51840
mm v[116:119],v[204:207],a[52:55],v[116:119]
st v[230:233],v238,s[32:35],s96 offen offset:1024 sc1
mm v[116:119],v[200:203],a[60:63],v[116:119]
rt v[196:197],v242 offset:59264
rt v[198:199],v242 offset:60288
mm v[84:87],v[200:203],a[56:59],v[84:87]
wt lgkmcnt(4)
mm v[84:87],v[204:207],a[48:51],v[84:87]
rt v[200:201],v242 offset:128
rt v[202:203],v242 offset:1152
mm v[84:87],v[212:215],v[128:131],v[84:87]
mm v[84:87],v[208:211],v[144:147],v[84:87]
rt v[204:205],v242 offset:8576
rt v[206:207],v242 offset:9600
mm v[116:119],v[208:211],v[148:151],v[116:119]
mm v[116:119],v[212:215],v[132:135],v[116:119]
rt v[208:209],v242 offset:17024
rt v[210:211],v242 offset:18048
mm v[116:119],v[220:223],v[164:167],v[116:119]
mm v[116:119],v[216:219],v[180:183],v[116:119]
rt v[212:213],v242 offset:25472
rt v[214:215],v242 offset:26496
mm v[84:87],v[216:219],v[176:179],v[84:87]
wt lgkmcnt(4)
mm v[84:87],v[220:223],v[160:163],v[84:87]
rt v[216:217],v242 offset:33920
rt v[218:219],v242 offset:34944
mm v[88:91],v[192:195],a[32:35],v[88:91]
cv v234,v112,v113
cv v236,v116,v117
mm v[88:91],v[196:199],a[40:43],v[88:91]
rt v[220:221],v242 offset:42368
rt v[222:223],v242 offset:43392
mm v[120:123],v[196:199],a[44:47],v[120:123]
cv v235,v114,v115
cv v237,v118,v119
mm v[120:123],v[192:195],a[36:39],v[120:123]
rt v[196:197],v243 offset:50816
rt v[198:199],v243 offset:51840
mm v[120:123],v[200:203],a[52:55],v[120:123]
v_permlane16_swap_b32 v234, v236
v_permlane16_swap_b32 v235, v237
mm v[120:123],v[204:207],a[60:63],v[120:123]
rt v[192:193],v243 offset:59264
rt v[194:195],v243 offset:60288
mm v[88:91],v[204:207],a[56:59],v[88:91]
wt lgkmcnt(4)
mm v[88:91],v[200:203],a[48:51],v[88:91]
rt v[204:205],v243 offset:128
rt v[206:207],v243 offset:1152
mm v[88:91],v[208:211],v[128:131],v[88:91]
st v[234:237],v238,s[32:35],s97 offen offset:2048 sc1
mm v[88:91],v[212:215],v[144:147],v[88:91]
)ASM" R"ASM(rt v[200:201],v243 offset:8576
rt v[202:203],v243 offset:9600
mm v[120:123],v[212:215],v[148:151],v[120:123]
cv v230,v80,v81
cv v232,v84,v85
mm v[120:123],v[208:211],v[132:135],v[120:123]
rt v[212:213],v243 offset:17024
rt v[214:215],v243 offset:18048
mm v[120:123],v[216:219],v[164:167],v[120:123]
cv v231,v82,v83
cv v233,v86,v87
wt vmcnt(14)
buffer_store_dwordx2 v[252:253], v251, s[64:67], s92 offen sc1
mm v[120:123],v[220:223],v[180:183],v[120:123]
rt v[208:209],v243 offset:25472
rt v[210:211],v243 offset:26496
mm v[88:91],v[220:223],v[176:179],v[88:91]
wt lgkmcnt(4)
mm v[88:91],v[216:219],v[160:163],v[88:91]
rt v[220:221],v243 offset:33920
rt v[222:223],v243 offset:34944
mm v[92:95],v[196:199],a[32:35],v[92:95]
v_permlane16_swap_b32 v230, v232
v_permlane16_swap_b32 v231, v233
mm v[92:95],v[192:195],a[40:43],v[92:95]
rt v[216:217],v243 offset:42368
rt v[218:219],v243 offset:43392
mm v[124:127],v[192:195],a[44:47],v[124:127]
mm v[124:127],v[196:199],a[36:39],v[124:127]
mm v[124:127],v[204:207],a[52:55],v[124:127]
st v[230:233],v238,s[32:35],s96 offen offset:2048 sc1
mm v[124:127],v[200:203],a[60:63],v[124:127]
mm v[92:95],v[200:203],a[56:59],v[92:95]
wt lgkmcnt(0)
mm v[92:95],v[204:207],a[48:51],v[92:95]
mm v[92:95],v[212:215],v[128:131],v[92:95]
mm v[92:95],v[208:211],v[144:147],v[92:95]
mm v[124:127],v[208:211],v[148:151],v[124:127]
mm v[124:127],v[212:215],v[132:135],v[124:127]
mm v[124:127],v[220:223],v[164:167],v[124:127]
mm v[124:127],v[216:219],v[180:183],v[124:127]
mm v[92:95],v[216:219],v[176:179],v[92:95]
mm v[92:95],v[220:223],v[160:163],v[92:95]
s_nop 7
cv v234,v120,v121
cv v236,v124,v125
cv v235,v122,v123
cv v237,v126,v127
cv v230,v88,v89
cv v232,v92,v93
cv v231,v90,v91
cv v233,v94,v95
v_permlane16_swap_b32 v234, v236
v_permlane16_swap_b32 v235, v237
st v[234:237],v238,s[32:35],s97 offen offset:3072 sc1
v_permlane16_swap_b32 v230, v232
v_permlane16_swap_b32 v231, v233
st v[230:233],v238,s[32:35],s96 offen offset:3072 sc1
.La5_LT_0:
.La5_LW3_5:
s_cbranch_execz .La5_LA_RETURN
.La5_LW7_6:
s_cbranch_execz .La5_LA_RETURN
s_lshl_b32 s91, s76, 5
s_add_u32 s91, s91, s90
s_lshl_b32 s20, s91, 9
s_add_u32 s20, s20, 2097152
s_lshr_b32 s93, s91, 1
s_lshl_b32 s93, s93, 15
s_and_b32 s94, s91, 1
s_lshl2_add_u32 s94, s94, s3
s_lshl_b32 s94, s94, 7
s_add_u32 s97, s93, s94
s_add_u32 s32, s26, 71303168
s_addc_u32 s33, s27, 0
s_and_b32 s33, s33, 0xffff
s_mov_b32 s34, 16777216
s_mov_b32 s35, 0x20000
s_add_u32 s44, s26, 171966464
s_addc_u32 s45, s27, 0
s_and_b32 s45, s45, 0xffff
s_mov_b32 s46, 950272
s_mov_b32 s47, 0x20000
s_add_u32 s56, s38, 0
s_addc_u32 s57, s39, 0
s_and_b32 s57, s57, 0xffff
s_mov_b32 s58, 4194304
s_mov_b32 s59, 0x20000
s_add_u32 s68, s16, 0
s_addc_u32 s69, s17, 0
s_and_b32 s69, s69, 0xffff
s_mov_b32 s70, 4194304
s_mov_b32 s71, 0x20000
v_and_b32 v230, 0x70, v255
v_lshrrev_b32 v231, 7, v255
v_lshl_add_u32 v230, v231, 10, v230
v_add_u32 v240, s97, v230
v_xor_b32 v241, 8192, v240
v_xor_b32 v242, 16384, v240
v_xor_b32 v243, 24576, v240
)ASM" R"ASM(v_mov_b32 v237, 0x3f803f80
v_mov_b32 v236, 0
wt vmcnt(7)
v_dot2_f32_bf16 v236, v140, v237, v236
v_dot2_f32_bf16 v236, v141, v237, v236
v_dot2_f32_bf16 v236, v142, v237, v236
v_dot2_f32_bf16 v236, v143, v237, v236
v_dot2_f32_bf16 v236, v156, v237, v236
v_dot2_f32_bf16 v236, v157, v237, v236
v_dot2_f32_bf16 v236, v158, v237, v236
v_dot2_f32_bf16 v236, v159, v237, v236
v_dot2_f32_bf16 v236, v172, v237, v236
v_dot2_f32_bf16 v236, v173, v237, v236
v_dot2_f32_bf16 v236, v174, v237, v236
v_dot2_f32_bf16 v236, v175, v237, v236
v_dot2_f32_bf16 v236, v188, v237, v236
v_dot2_f32_bf16 v236, v189, v237, v236
v_dot2_f32_bf16 v236, v190, v237, v236
v_dot2_f32_bf16 v236, v191, v237, v236
v_lshlrev_b32 v0, 16, v136
v_and_b32 v1, 0xffff0000, v136
v_lshlrev_b32 v2, 16, v137
v_and_b32 v3, 0xffff0000, v137
v_lshlrev_b32 v4, 16, v138
v_and_b32 v5, 0xffff0000, v138
v_lshlrev_b32 v6, 16, v139
v_and_b32 v7, 0xffff0000, v139
v_lshlrev_b32 v8, 16, v152
v_and_b32 v9, 0xffff0000, v152
v_lshlrev_b32 v10, 16, v153
v_and_b32 v11, 0xffff0000, v153
v_lshlrev_b32 v12, 16, v154
v_and_b32 v13, 0xffff0000, v154
v_lshlrev_b32 v14, 16, v155
v_and_b32 v15, 0xffff0000, v155
v_lshlrev_b32 v16, 16, v168
v_and_b32 v17, 0xffff0000, v168
v_lshlrev_b32 v18, 16, v169
v_and_b32 v19, 0xffff0000, v169
v_lshlrev_b32 v20, 16, v170
v_and_b32 v21, 0xffff0000, v170
v_lshlrev_b32 v22, 16, v171
v_and_b32 v23, 0xffff0000, v171
v_lshlrev_b32 v24, 16, v184
v_and_b32 v25, 0xffff0000, v184
v_lshlrev_b32 v26, 16, v185
v_and_b32 v27, 0xffff0000, v185
v_lshlrev_b32 v28, 16, v186
v_and_b32 v29, 0xffff0000, v186
v_lshlrev_b32 v30, 16, v187
v_and_b32 v31, 0xffff0000, v187
s_nop 1
v_add_f32_dpp v236, v236, v236 quad_perm:[1,0,3,2] row_mask:0xf bank_mask:0xf
s_nop 1
v_add_f32_dpp v236, v236, v236 quad_perm:[2,3,0,1] row_mask:0xf bank_mask:0xf
s_nop 1
v_add_f32_dpp v236, v236, v236 row_ror:8 row_mask:0xf bank_mask:0xf
v_mov_b32 v230, v236
s_nop 1
v_permlane16_swap_b32 v230, v236
s_nop 1
v_add_f32 v236, v236, v230
v_mov_b32 v230, v236
s_nop 1
v_permlane32_swap_b32 v230, v236
s_nop 1
v_add_f32 v236, v236, v230
s_mov_b64 exec, 0
s_cmp_eq_u32 s3, 0
s_cbranch_scc1 .La5_LW4_0
s_cmp_eq_u32 s3, 1
s_cbranch_scc1 .La5_LW4_1
s_cmp_eq_u32 s3, 2
s_cbranch_scc1 .La5_LW4_2
s_branch .La5_LW4_3
.La5_LW4_R0:
s_mov_b64 exec, -1
wt vmcnt(0)
buffer_store_dwordx2 v[252:253], v250, s[64:67], s92 offen sc1
s_mov_b32 s85, 99
s_cmp_eq_u32 s30, 0
s_cbranch_scc1 .La5_LT_ROWS
; ---- h = x + attn, LayerNorm statistics, router score, two best of eight rows
.La5_LT_ROWS:
s_mov_b32 s96, 0
.La5_LT_P3:
buffer_load_dwordx2 v[250:251], v248, s[64:67], s20 offen sc1
wt vmcnt(0)
.La5_LT_P2:
v_cmp_eq_u64 vcc, v[250:251], v[252:253]
s_add_u32 s96, s96, 1
s_lshr_b32 s92, vcc_lo, 1
s_and_b32 s92, s92, vcc_lo
s_lshr_b32 s93, s92, 2
s_and_b32 s92, s92, s93
s_lshr_b32 s93, s92, 4
s_and_b32 s92, s92, s93
s_lshr_b32 s93, s96, 20
s_mul_i32 s93, s93, 0x01010101
s_or_b32 s92, s92, s93
)ASM" R"ASM(s_andn2_b32 s92, 0x01010101, s92
s_bcnt1_i32_b32 s93, s92
s_cmp_gt_u32 s93, 1
s_cbranch_scc1 .La5_LT_P3S
s_or_b32 s93, s92, 0x01000000
s_ff1_i32_b32 s93, s93
s_lshl_b32 s80, s93, 19
s_xor_b32 s93, s80, 8388608
s_xor_b32 s94, s80, 12582912
s_xor_b32 s95, s80, 4194304
ld v[208:211],v240,s[32:35],s93 offen sc1
ld v[212:215],v241,s[32:35],s93 offen sc1
ld v[216:219],v242,s[32:35],s93 offen sc1
ld v[220:223],v243,s[32:35],s93 offen sc1
ld v[112:115],v240,s[32:35],s94 offen sc1
ld v[116:119],v241,s[32:35],s94 offen sc1
ld v[120:123],v242,s[32:35],s94 offen sc1
ld v[124:127],v243,s[32:35],s94 offen sc1
ld v[96:99],v240,s[32:35],s95 offen sc1
ld v[100:103],v241,s[32:35],s95 offen sc1
ld v[104:107],v242,s[32:35],s95 offen sc1
ld v[108:111],v243,s[32:35],s95 offen sc1
s_cmp_lg_u32 s92, 0
s_nop 0
s_nop 0
s_cbranch_scc1 .La5_LT_P4
ld v[192:195],v240,s[32:35],s80 offen sc1
ld v[196:199],v241,s[32:35],s80 offen sc1
ld v[200:203],v242,s[32:35],s80 offen sc1
ld v[204:207],v243,s[32:35],s80 offen sc1
.La5_LT_P4:
s_lshr_b32 s93, s80, 16
v_and_b32 v248, 56, v248
s_add_u32 s20, s20, s93
buffer_load_dwordx2 v[250:251], v248, s[64:67], s20 offen sc1
wt vmcnt(8)
v_lshlrev_b32 v32, 16, v208
v_and_b32 v33, 0xffff0000, v208
v_lshlrev_b32 v230, 16, v112
v_and_b32 v231, 0xffff0000, v112
v_pk_add_f32 v[32:33], v[32:33], v[230:231]
v_lshlrev_b32 v34, 16, v209
v_and_b32 v35, 0xffff0000, v209
v_lshlrev_b32 v230, 16, v113
v_and_b32 v231, 0xffff0000, v113
v_pk_add_f32 v[34:35], v[34:35], v[230:231]
v_lshlrev_b32 v36, 16, v210
v_and_b32 v37, 0xffff0000, v210
v_lshlrev_b32 v230, 16, v114
v_and_b32 v231, 0xffff0000, v114
v_pk_add_f32 v[36:37], v[36:37], v[230:231]
v_lshlrev_b32 v38, 16, v211
v_and_b32 v39, 0xffff0000, v211
v_lshlrev_b32 v230, 16, v115
v_and_b32 v231, 0xffff0000, v115
v_pk_add_f32 v[38:39], v[38:39], v[230:231]
wt vmcnt(7)
v_lshlrev_b32 v40, 16, v212
v_and_b32 v41, 0xffff0000, v212
v_lshlrev_b32 v230, 16, v116
v_and_b32 v231, 0xffff0000, v116
v_pk_add_f32 v[40:41], v[40:41], v[230:231]
v_lshlrev_b32 v42, 16, v213
v_and_b32 v43, 0xffff0000, v213
v_lshlrev_b32 v230, 16, v117
v_and_b32 v231, 0xffff0000, v117
v_pk_add_f32 v[42:43], v[42:43], v[230:231]
v_lshlrev_b32 v44, 16, v214
v_and_b32 v45, 0xffff0000, v214
v_lshlrev_b32 v230, 16, v118
v_and_b32 v231, 0xffff0000, v118
v_pk_add_f32 v[44:45], v[44:45], v[230:231]
v_lshlrev_b32 v46, 16, v215
v_and_b32 v47, 0xffff0000, v215
v_lshlrev_b32 v230, 16, v119
v_and_b32 v231, 0xffff0000, v119
v_pk_add_f32 v[46:47], v[46:47], v[230:231]
wt vmcnt(6)
v_lshlrev_b32 v48, 16, v216
v_and_b32 v49, 0xffff0000, v216
v_lshlrev_b32 v230, 16, v120
v_and_b32 v231, 0xffff0000, v120
v_pk_add_f32 v[48:49], v[48:49], v[230:231]
v_lshlrev_b32 v50, 16, v217
v_and_b32 v51, 0xffff0000, v217
v_lshlrev_b32 v230, 16, v121
v_and_b32 v231, 0xffff0000, v121
v_pk_add_f32 v[50:51], v[50:51], v[230:231]
v_lshlrev_b32 v52, 16, v218
v_and_b32 v53, 0xffff0000, v218
v_lshlrev_b32 v230, 16, v122
v_and_b32 v231, 0xffff0000, v122
v_pk_add_f32 v[52:53], v[52:53], v[230:231]
v_lshlrev_b32 v54, 16, v219
v_and_b32 v55, 0xffff0000, v219
v_lshlrev_b32 v230, 16, v123
v_and_b32 v231, 0xffff0000, v123
v_pk_add_f32 v[54:55], v[54:55], v[230:231]
wt vmcnt(5)
v_lshlrev_b32 v56, 16, v220
)ASM" R"ASM(v_and_b32 v57, 0xffff0000, v220
v_lshlrev_b32 v230, 16, v124
v_and_b32 v231, 0xffff0000, v124
v_pk_add_f32 v[56:57], v[56:57], v[230:231]
v_lshlrev_b32 v58, 16, v221
v_and_b32 v59, 0xffff0000, v221
v_lshlrev_b32 v230, 16, v125
v_and_b32 v231, 0xffff0000, v125
v_pk_add_f32 v[58:59], v[58:59], v[230:231]
v_lshlrev_b32 v60, 16, v222
v_and_b32 v61, 0xffff0000, v222
v_lshlrev_b32 v230, 16, v126
v_and_b32 v231, 0xffff0000, v126
v_pk_add_f32 v[60:61], v[60:61], v[230:231]
v_lshlrev_b32 v62, 16, v223
v_and_b32 v63, 0xffff0000, v223
v_lshlrev_b32 v230, 16, v127
v_and_b32 v231, 0xffff0000, v127
v_pk_add_f32 v[62:63], v[62:63], v[230:231]
.La5_LT_PH:
wt vmcnt(1)
v_lshlrev_b32 v64, 16, v96
v_and_b32 v65, 0xffff0000, v96
v_lshlrev_b32 v66, 16, v97
v_and_b32 v67, 0xffff0000, v97
v_lshlrev_b32 v68, 16, v98
v_and_b32 v69, 0xffff0000, v98
v_lshlrev_b32 v70, 16, v99
v_and_b32 v71, 0xffff0000, v99
v_lshlrev_b32 v72, 16, v100
v_and_b32 v73, 0xffff0000, v100
v_lshlrev_b32 v74, 16, v101
v_and_b32 v75, 0xffff0000, v101
v_lshlrev_b32 v76, 16, v102
v_and_b32 v77, 0xffff0000, v102
v_lshlrev_b32 v78, 16, v103
v_and_b32 v79, 0xffff0000, v103
v_lshlrev_b32 v80, 16, v104
v_and_b32 v81, 0xffff0000, v104
v_lshlrev_b32 v82, 16, v105
v_and_b32 v83, 0xffff0000, v105
v_lshlrev_b32 v84, 16, v106
v_and_b32 v85, 0xffff0000, v106
v_lshlrev_b32 v86, 16, v107
v_and_b32 v87, 0xffff0000, v107
v_lshlrev_b32 v88, 16, v108
v_and_b32 v89, 0xffff0000, v108
v_lshlrev_b32 v90, 16, v109
v_and_b32 v91, 0xffff0000, v109
v_lshlrev_b32 v92, 16, v110
v_and_b32 v93, 0xffff0000, v110
v_lshlrev_b32 v94, 16, v111
v_and_b32 v95, 0xffff0000, v111
v_mov_b32 v232, 0
v_mov_b32 v233, 0
v_mov_b32_e64 v234, 0
s_cmp_eq_u32 s92, 0
s_cbranch_scc1 .La5_LT_SUM
.La5_LT_P5:
wt vmcnt(0)
v_cmp_eq_u64 vcc, v[250:251], v[252:253]
s_add_u32 s96, s96, 1
s_cmp_eq_u64 vcc, -1
s_cbranch_scc1 .La5_LT_P6
s_lshr_b32 s93, s96, 20
s_nop 0
s_cbranch_scc1 .La5_LT_P6
s_sleep 3
buffer_load_dwordx2 v[250:251], v248, s[64:67], s20 offen sc1
s_branch .La5_LT_P5
.La5_LT_P3S:
s_sleep 4
s_bcnt0_i32_b32 s93, vcc_lo
s_cmp_le_u32 s93, 8
s_cbranch_scc1 .La5_LT_P3
s_sleep 24
s_branch .La5_LT_P3
.La5_LT_P6:
ld v[192:195],v240,s[32:35],s80 offen sc1
ld v[196:199],v241,s[32:35],s80 offen sc1
ld v[200:203],v242,s[32:35],s80 offen sc1
ld v[204:207],v243,s[32:35],s80 offen sc1
.La5_LT_SUM:
wt vmcnt(3)
v_lshlrev_b32 v208, 16, v192
v_and_b32 v209, 0xffff0000, v192
v_lshlrev_b32 v210, 16, v193
v_and_b32 v211, 0xffff0000, v193
v_lshlrev_b32 v212, 16, v194
v_and_b32 v213, 0xffff0000, v194
v_lshlrev_b32 v214, 16, v195
v_and_b32 v215, 0xffff0000, v195
v_pk_add_f32 v[208:209], v[208:209], v[64:65]
v_pk_add_f32 v[210:211], v[210:211], v[66:67]
v_pk_add_f32 v[212:213], v[212:213], v[68:69]
v_pk_add_f32 v[214:215], v[214:215], v[70:71]
v_pk_add_f32 v[208:209], v[208:209], v[32:33]
v_pk_add_f32 v[210:211], v[210:211], v[34:35]
v_pk_add_f32 v[212:213], v[212:213], v[36:37]
v_pk_add_f32 v[214:215], v[214:215], v[38:39]
)ASM" R"ASM(v_pk_add_f32 v[208:209], v[208:209], v[0:1]
v_pk_add_f32 v[210:211], v[210:211], v[2:3]
v_pk_add_f32 v[212:213], v[212:213], v[4:5]
v_pk_add_f32 v[214:215], v[214:215], v[6:7]
cv v96,v208,v209
cv v97,v210,v211
cv v98,v212,v213
cv v99,v214,v215
v_dot2_f32_bf16 v232, v96, v237, v232
v_dot2_f32_bf16 v233, v96, v96, v233
v_dot2_f32_bf16 v234, v96, v140, v234
v_dot2_f32_bf16 v232, v97, v237, v232
v_dot2_f32_bf16 v233, v97, v97, v233
v_dot2_f32_bf16 v234, v97, v141, v234
v_dot2_f32_bf16 v232, v98, v237, v232
v_dot2_f32_bf16 v233, v98, v98, v233
v_dot2_f32_bf16 v234, v98, v142, v234
v_dot2_f32_bf16 v232, v99, v237, v232
v_dot2_f32_bf16 v233, v99, v99, v233
v_dot2_f32_bf16 v234, v99, v143, v234
wt vmcnt(2)
v_lshlrev_b32 v208, 16, v196
v_and_b32 v209, 0xffff0000, v196
v_lshlrev_b32 v210, 16, v197
v_and_b32 v211, 0xffff0000, v197
v_lshlrev_b32 v212, 16, v198
v_and_b32 v213, 0xffff0000, v198
v_lshlrev_b32 v214, 16, v199
v_and_b32 v215, 0xffff0000, v199
v_pk_add_f32 v[208:209], v[208:209], v[72:73]
v_pk_add_f32 v[210:211], v[210:211], v[74:75]
v_pk_add_f32 v[212:213], v[212:213], v[76:77]
v_pk_add_f32 v[214:215], v[214:215], v[78:79]
v_pk_add_f32 v[208:209], v[208:209], v[40:41]
v_pk_add_f32 v[210:211], v[210:211], v[42:43]
v_pk_add_f32 v[212:213], v[212:213], v[44:45]
v_pk_add_f32 v[214:215], v[214:215], v[46:47]
v_pk_add_f32 v[208:209], v[208:209], v[8:9]
v_pk_add_f32 v[210:211], v[210:211], v[10:11]
v_pk_add_f32 v[212:213], v[212:213], v[12:13]
v_pk_add_f32 v[214:215], v[214:215], v[14:15]
cv v100,v208,v209
cv v101,v210,v211
cv v102,v212,v213
cv v103,v214,v215
v_dot2_f32_bf16 v232, v100, v237, v232
v_dot2_f32_bf16 v233, v100, v100, v233
v_dot2_f32_bf16 v234, v100, v156, v234
v_dot2_f32_bf16 v232, v101, v237, v232
v_dot2_f32_bf16 v233, v101, v101, v233
v_dot2_f32_bf16 v234, v101, v157, v234
v_dot2_f32_bf16 v232, v102, v237, v232
v_dot2_f32_bf16 v233, v102, v102, v233
v_dot2_f32_bf16 v234, v102, v158, v234
v_dot2_f32_bf16 v232, v103, v237, v232
v_dot2_f32_bf16 v233, v103, v103, v233
v_dot2_f32_bf16 v234, v103, v159, v234
wt vmcnt(1)
v_lshlrev_b32 v208, 16, v200
v_and_b32 v209, 0xffff0000, v200
v_lshlrev_b32 v210, 16, v201
v_and_b32 v211, 0xffff0000, v201
v_lshlrev_b32 v212, 16, v202
v_and_b32 v213, 0xffff0000, v202
v_lshlrev_b32 v214, 16, v203
v_and_b32 v215, 0xffff0000, v203
v_pk_add_f32 v[208:209], v[208:209], v[80:81]
v_pk_add_f32 v[210:211], v[210:211], v[82:83]
v_pk_add_f32 v[212:213], v[212:213], v[84:85]
v_pk_add_f32 v[214:215], v[214:215], v[86:87]
v_pk_add_f32 v[208:209], v[208:209], v[48:49]
v_pk_add_f32 v[210:211], v[210:211], v[50:51]
v_pk_add_f32 v[212:213], v[212:213], v[52:53]
v_pk_add_f32 v[214:215], v[214:215], v[54:55]
v_pk_add_f32 v[208:209], v[208:209], v[16:17]
v_pk_add_f32 v[210:211], v[210:211], v[18:19]
v_pk_add_f32 v[212:213], v[212:213], v[20:21]
v_pk_add_f32 v[214:215], v[214:215], v[22:23]
cv v104,v208,v209
cv v105,v210,v211
cv v106,v212,v213
cv v107,v214,v215
v_dot2_f32_bf16 v232, v104, v237, v232
v_dot2_f32_bf16 v233, v104, v104, v233
v_dot2_f32_bf16 v234, v104, v172, v234
v_dot2_f32_bf16 v232, v105, v237, v232
v_dot2_f32_bf16 v233, v105, v105, v233
v_dot2_f32_bf16 v234, v105, v173, v234
v_dot2_f32_bf16 v232, v106, v237, v232
.La5_LW4_0:
s_cbranch_execz .La5_LW4_4
v_dot2_f32_bf16 v233, v106, v106, v233
v_dot2_f32_bf16 v234, v106, v174, v234
v_dot2_f32_bf16 v232, v107, v237, v232
v_dot2_f32_bf16 v233, v107, v107, v233
v_dot2_f32_bf16 v234, v107, v175, v234
wt vmcnt(0)
v_lshlrev_b32 v208, 16, v204
v_and_b32 v209, 0xffff0000, v204
v_lshlrev_b32 v210, 16, v205
)ASM" R"ASM(v_and_b32 v211, 0xffff0000, v205
v_lshlrev_b32 v212, 16, v206
v_and_b32 v213, 0xffff0000, v206
v_lshlrev_b32 v214, 16, v207
v_and_b32 v215, 0xffff0000, v207
v_pk_add_f32 v[208:209], v[208:209], v[88:89]
v_pk_add_f32 v[210:211], v[210:211], v[90:91]
v_pk_add_f32 v[212:213], v[212:213], v[92:93]
v_pk_add_f32 v[214:215], v[214:215], v[94:95]
v_pk_add_f32 v[208:209], v[208:209], v[56:57]
v_pk_add_f32 v[210:211], v[210:211], v[58:59]
v_pk_add_f32 v[212:213], v[212:213], v[60:61]
v_pk_add_f32 v[214:215], v[214:215], v[62:63]
v_pk_add_f32 v[208:209], v[208:209], v[24:25]
v_pk_add_f32 v[210:211], v[210:211], v[26:27]
v_pk_add_f32 v[212:213], v[212:213], v[28:29]
v_pk_add_f32 v[214:215], v[214:215], v[30:31]
cv v108,v208,v209
cv v109,v210,v211
cv v110,v212,v213
cv v111,v214,v215
v_dot2_f32_bf16 v232, v108, v237, v232
v_dot2_f32_bf16 v233, v108, v108, v233
v_dot2_f32_bf16 v234, v108, v188, v234
v_dot2_f32_bf16 v232, v109, v237, v232
v_dot2_f32_bf16 v233, v109, v109, v233
v_dot2_f32_bf16 v234, v109, v189, v234
v_dot2_f32_bf16 v232, v110, v237, v232
v_dot2_f32_bf16 v233, v110, v110, v233
v_dot2_f32_bf16 v234, v110, v190, v234
v_dot2_f32_bf16 v232, v111, v237, v232
v_dot2_f32_bf16 v233, v111, v111, v233
v_dot2_f32_bf16 v234, v111, v191, v234
wt vmcnt(0)
s_lshl2_add_u32 s92, s91, s3
s_lshl_b32 s92, s92, 1
s_sub_u32 s92, 0xffff, s92
v_add_f32_dpp v232, v232, v232 quad_perm:[1,0,3,2] row_mask:0xf bank_mask:0xf
v_add_f32_dpp v233, v233, v233 quad_perm:[1,0,3,2] row_mask:0xf bank_mask:0xf
v_add_f32_dpp v234, v234, v234 quad_perm:[1,0,3,2] row_mask:0xf bank_mask:0xf
v_add_f32_dpp v232, v232, v232 quad_perm:[2,3,0,1] row_mask:0xf bank_mask:0xf
v_add_f32_dpp v233, v233, v233 quad_perm:[2,3,0,1] row_mask:0xf bank_mask:0xf
v_add_f32_dpp v234, v234, v234 quad_perm:[2,3,0,1] row_mask:0xf bank_mask:0xf
v_add_f32_dpp v232, v232, v232 row_ror:8 row_mask:0xf bank_mask:0xf
v_add_f32_dpp v233, v233, v233 row_ror:8 row_mask:0xf bank_mask:0xf
v_add_f32_dpp v234, v234, v234 row_ror:8 row_mask:0xf bank_mask:0xf
v_mov_b32 v230, v232
v_mov_b32 v231, v233
v_mov_b32 v235, v234
v_permlane16_swap_b32 v230, v232
v_permlane16_swap_b32 v231, v233
v_permlane16_swap_b32 v235, v234
v_add_f32 v232, v232, v230
v_add_f32 v233, v233, v231
v_add_f32 v234, v234, v235
v_mov_b32 v230, v232
v_mov_b32 v231, v233
v_mov_b32 v235, v234
v_permlane32_swap_b32 v230, v232
v_permlane32_swap_b32 v231, v233
v_permlane32_swap_b32 v235, v234
v_add_f32 v232, v232, v230
v_add_f32 v233, v233, v231
v_add_f32 v234, v234, v235
v_mul_f32 v244, 0x3a800000, v232
v_mul_f32 v233, 0x3a800000, v233
v_fma_f32 v230, -v244, v244, v233
v_mul_f32 v233, 0x3a800000, v233
v_cmp_nge_f32 vcc, v230, v233
v_bfe_u32 v235, v255, 6, 1
v_sub_u32 v235, s92, v235
s_cbranch_vccnz .La5_LT_VAR
.La5_LT_VOK:
cv v244,v244,v244
v_lshlrev_b32 v244, 16, v244
cv v230,v230,v230
v_lshlrev_b32 v230, 16, v230
v_fma_f32 v234, -v244, v236, v234
v_add_f32 v230, 0x3727c5ac, v230
cv v230,v230,v230
v_lshlrev_b32 v230, 16, v230
v_sqrt_f32 v230, v230
v_lshrrev_b32 v232, 3, v255
cv v230,v230,v230
v_lshlrev_b32 v231, 16, v230
v_rcp_f32 v246, v231
v_lshrrev_b32 v230, 16, v230
v_or_b32 v233, v244, v230
v_mul_f32 v234, v234, v246
s_lshl_b32 s93, s3, 4
cv v234,v234,v234
v_and_b32 v230, 8, v232
v_ashrrev_i32 v232, 31, v234
v_or_b32 v232, 0x80000000, v232
v_xor_b32 v232, v232, v234
v_and_b32 v232, 0xffff0000, v232
v_add_u32 v230, s93, v230
v_or_b32 v232, v232, v235
ds_write_b64 v230, v[232:233] offset:0
v_lshrrev_b32 v230, 1, v255
)ASM" R"ASM(v_and_b32_e64 v230, 56, v230
wt lgkmcnt(0)
s_barrier
s_cmp_lg_u32 s3, 0
s_cbranch_scc1 .La5_LT_OUT
ds_read_b64 v[232:233], v230 offset:0
s_lshl3_add_u32 s92, s90, s76
s_lshl_b32 s92, s92, 4
v_lshrrev_b32 v235, 7, v255
v_lshlrev_b32 v235, 12, v235
wt lgkmcnt(0)
v_mov_b32 v234, v232
s_nop 1
v_max_u32_dpp v234, v234, v234 quad_perm:[1,0,3,2] row_mask:0xf bank_mask:0xf
s_nop 1
v_max_u32_dpp v234, v234, v234 quad_perm:[2,3,0,1] row_mask:0xf bank_mask:0xf
s_nop 1
v_max_u32_dpp v234, v234, v234 row_half_mirror row_mask:0xf bank_mask:0xf
s_nop 1
v_cmp_eq_u32 vcc, v232, v234
v_add_u32 v231, 8, v235
s_nop 0
v_cndmask_b32 v234, v232, 0, vcc
v_cndmask_b32 v235, v231, v235, vcc
s_nop 0
v_max_u32_dpp v234, v234, v234 quad_perm:[1,0,3,2] row_mask:0xf bank_mask:0xf
s_nop 1
v_max_u32_dpp v234, v234, v234 quad_perm:[2,3,0,1] row_mask:0xf bank_mask:0xf
s_nop 1
v_max_u32_dpp v234, v234, v234 row_half_mirror row_mask:0xf bank_mask:0xf
s_nop 1
v_cmp_ge_u32 vcc, v232, v234
s_nop 1
s_mov_b64 exec, vcc
buffer_store_dwordx2 v[232:233], v235, s[44:47], s92 offen sc1
s_mov_b64 exec, -1
; ---- out = h + .La5_LN(h) of the own rows; FFN weights into MFMA order
.La5_LT_OUT:
v_mul_f32 v244, v244, v246
v_mov_b32 v247, v246
v_xor_b32 v244, 0x80000000, v244
v_mov_b32 v245, v244
v_lshlrev_b32 v230, 16, v96
v_and_b32 v231, 0xffff0000, v96
v_pk_fma_f32 v[232:233], v[230:231], v[246:247], v[244:245]
cv v232,v232,v233
v_lshlrev_b32 v234, 16, v232
v_and_b32 v235, 0xffff0000, v232
v_pk_add_f32 v[230:231], v[230:231], v[234:235]
cv v192,v230,v231
v_lshlrev_b32 v230, 16, v97
v_and_b32 v231, 0xffff0000, v97
v_pk_fma_f32 v[232:233], v[230:231], v[246:247], v[244:245]
cv v232,v232,v233
v_lshlrev_b32 v234, 16, v232
v_and_b32 v235, 0xffff0000, v232
v_pk_add_f32 v[230:231], v[230:231], v[234:235]
cv v193,v230,v231
v_lshlrev_b32 v230, 16, v98
v_and_b32 v231, 0xffff0000, v98
v_pk_fma_f32 v[232:233], v[230:231], v[246:247], v[244:245]
cv v232,v232,v233
v_lshlrev_b32 v234, 16, v232
v_and_b32 v235, 0xffff0000, v232
v_pk_add_f32 v[230:231], v[230:231], v[234:235]
cv v194,v230,v231
v_lshlrev_b32 v230, 16, v99
v_and_b32 v231, 0xffff0000, v99
v_pk_fma_f32 v[232:233], v[230:231], v[246:247], v[244:245]
cv v232,v232,v233
v_lshlrev_b32 v234, 16, v232
v_and_b32 v235, 0xffff0000, v232
v_pk_add_f32 v[230:231], v[230:231], v[234:235]
cv v195,v230,v231
v_lshlrev_b32 v230, 16, v100
v_and_b32 v231, 0xffff0000, v100
v_pk_fma_f32 v[232:233], v[230:231], v[246:247], v[244:245]
cv v232,v232,v233
v_lshlrev_b32 v234, 16, v232
v_and_b32 v235, 0xffff0000, v232
v_pk_add_f32 v[230:231], v[230:231], v[234:235]
cv v196,v230,v231
v_lshlrev_b32 v230, 16, v101
v_and_b32 v231, 0xffff0000, v101
v_pk_fma_f32 v[232:233], v[230:231], v[246:247], v[244:245]
cv v232,v232,v233
v_lshlrev_b32 v234, 16, v232
v_and_b32 v235, 0xffff0000, v232
v_pk_add_f32 v[230:231], v[230:231], v[234:235]
cv v197,v230,v231
v_lshlrev_b32 v230, 16, v102
v_and_b32 v231, 0xffff0000, v102
v_pk_fma_f32 v[232:233], v[230:231], v[246:247], v[244:245]
cv v232,v232,v233
v_lshlrev_b32 v234, 16, v232
v_and_b32 v235, 0xffff0000, v232
v_pk_add_f32 v[230:231], v[230:231], v[234:235]
cv v198,v230,v231
v_lshlrev_b32 v230, 16, v103
v_and_b32 v231, 0xffff0000, v103
)ASM" R"ASM(v_pk_fma_f32 v[232:233], v[230:231], v[246:247], v[244:245]
cv v232,v232,v233
v_lshlrev_b32 v234, 16, v232
v_and_b32 v235, 0xffff0000, v232
v_pk_add_f32 v[230:231], v[230:231], v[234:235]
cv v199,v230,v231
v_lshlrev_b32 v230, 16, v104
v_and_b32 v231, 0xffff0000, v104
v_pk_fma_f32 v[232:233], v[230:231], v[246:247], v[244:245]
cv v232,v232,v233
v_lshlrev_b32 v234, 16, v232
v_and_b32 v235, 0xffff0000, v232
v_pk_add_f32 v[230:231], v[230:231], v[234:235]
cv v200,v230,v231
v_lshlrev_b32 v230, 16, v105
v_and_b32 v231, 0xffff0000, v105
v_pk_fma_f32 v[232:233], v[230:231], v[246:247], v[244:245]
cv v232,v232,v233
v_lshlrev_b32 v234, 16, v232
v_and_b32 v235, 0xffff0000, v232
v_pk_add_f32 v[230:231], v[230:231], v[234:235]
cv v201,v230,v231
v_lshlrev_b32 v230, 16, v106
v_and_b32 v231, 0xffff0000, v106
v_pk_fma_f32 v[232:233], v[230:231], v[246:247], v[244:245]
cv v232,v232,v233
v_lshlrev_b32 v234, 16, v232
v_and_b32 v235, 0xffff0000, v232
v_pk_add_f32 v[230:231], v[230:231], v[234:235]
cv v202,v230,v231
v_lshlrev_b32 v230, 16, v107
v_and_b32 v231, 0xffff0000, v107
v_pk_fma_f32 v[232:233], v[230:231], v[246:247], v[244:245]
cv v232,v232,v233
v_lshlrev_b32 v234, 16, v232
v_and_b32 v235, 0xffff0000, v232
v_pk_add_f32 v[230:231], v[230:231], v[234:235]
cv v203,v230,v231
v_lshlrev_b32 v230, 16, v108
v_and_b32 v231, 0xffff0000, v108
v_pk_fma_f32 v[232:233], v[230:231], v[246:247], v[244:245]
cv v232,v232,v233
v_lshlrev_b32 v234, 16, v232
v_and_b32 v235, 0xffff0000, v232
v_pk_add_f32 v[230:231], v[230:231], v[234:235]
cv v204,v230,v231
v_lshlrev_b32 v230, 16, v109
v_and_b32 v231, 0xffff0000, v109
v_pk_fma_f32 v[232:233], v[230:231], v[246:247], v[244:245]
cv v232,v232,v233
v_lshlrev_b32 v234, 16, v232
v_and_b32 v235, 0xffff0000, v232
v_pk_add_f32 v[230:231], v[230:231], v[234:235]
cv v205,v230,v231
v_lshlrev_b32 v230, 16, v110
v_and_b32 v231, 0xffff0000, v110
v_pk_fma_f32 v[232:233], v[230:231], v[246:247], v[244:245]
cv v232,v232,v233
v_lshlrev_b32 v234, 16, v232
v_and_b32 v235, 0xffff0000, v232
v_pk_add_f32 v[230:231], v[230:231], v[234:235]
cv v206,v230,v231
v_lshlrev_b32 v230, 16, v111
v_and_b32 v231, 0xffff0000, v111
v_pk_fma_f32 v[232:233], v[230:231], v[246:247], v[244:245]
cv v232,v232,v233
v_lshlrev_b32_e64 v234, 16, v232
v_and_b32 v235, 0xffff0000, v232
v_pk_add_f32 v[230:231], v[230:231], v[234:235]
cv v207,v230,v231
s_cmp_eq_u32 s30, 0
s_cbranch_scc1 .La5_LT_SEL
s_mov_b32 s94, 0x05040100
s_mov_b32 s95, 0x07060302
wt vmcnt(0)
ar v230,a224
ar v231,a228
v_perm_b32 v128, v231, v230, s94
v_perm_b32 v132, v231, v230, s95
ar v230,a232
ar v231,a236
v_perm_b32 v129, v231, v230, s94
v_perm_b32 v133, v231, v230, s95
ar v230,a240
ar v231,a244
v_perm_b32 v130, v231, v230, s94
v_perm_b32 v134, v231, v230, s95
ar v230,a248
ar v231,a252
v_perm_b32 v131, v231, v230, s94
v_perm_b32 v135, v231, v230, s95
ar v230,a225
ar v231,a229
v_perm_b32 v136, v231, v230, s94
v_perm_b32 v140, v231, v230, s95
ar v230,a233
ar v231,a237
v_perm_b32 v137, v231, v230, s94
v_perm_b32 v141, v231, v230, s95
ar v230,a241
)ASM" R"ASM(ar v231,a245
v_perm_b32 v138, v231, v230, s94
v_perm_b32 v142, v231, v230, s95
ar v230,a249
ar v231,a253
v_perm_b32 v139, v231, v230, s94
v_perm_b32 v143, v231, v230, s95
ar v230,a226
ar v231,a230
v_perm_b32 v144, v231, v230, s94
v_perm_b32 v148, v231, v230, s95
ar v230,a234
ar v231,a238
v_perm_b32 v145, v231, v230, s94
v_perm_b32 v149, v231, v230, s95
ar v230,a242
ar v231,a246
v_perm_b32 v146, v231, v230, s94
v_perm_b32 v150, v231, v230, s95
ar v230,a250
ar v231,a254
v_perm_b32 v147, v231, v230, s94
v_perm_b32 v151, v231, v230, s95
ar v230,a227
ar v231,a231
v_perm_b32 v152, v231, v230, s94
v_perm_b32 v156, v231, v230, s95
ar v230,a235
ar v231,a239
v_perm_b32 v153, v231, v230, s94
v_perm_b32 v157, v231, v230, s95
ar v230,a243
ar v231,a247
v_perm_b32 v154, v231, v230, s94
v_perm_b32 v158, v231, v230, s95
ar v230,a251
ar v231,a255
v_perm_b32 v155, v231, v230, s94
v_perm_b32 v159, v231, v230, s95
ar v230,a64
ar v231,a68
v_perm_b32 v160, v231, v230, s94
v_perm_b32 v164, v231, v230, s95
ar v230,a72
ar v231,a76
v_perm_b32 v161, v231, v230, s94
v_perm_b32 v165, v231, v230, s95
ar v230,a80
ar v231,a84
v_perm_b32 v162, v231, v230, s94
v_perm_b32 v166, v231, v230, s95
ar v230,a88
ar v231,a92
v_perm_b32 v163, v231, v230, s94
v_perm_b32 v167, v231, v230, s95
ar v230,a65
ar v231,a69
v_perm_b32 v168, v231, v230, s94
v_perm_b32 v172, v231, v230, s95
ar v230,a73
ar v231,a77
v_perm_b32 v169, v231, v230, s94
v_perm_b32 v173, v231, v230, s95
ar v230,a81
ar v231,a85
v_perm_b32 v170, v231, v230, s94
v_perm_b32 v174, v231, v230, s95
ar v230,a89
ar v231,a93
v_perm_b32 v171, v231, v230, s94
v_perm_b32 v175, v231, v230, s95
ar v230,a66
ar v231,a70
v_perm_b32 v176, v231, v230, s94
v_perm_b32 v180, v231, v230, s95
ar v230,a74
ar v231,a78
v_perm_b32 v177, v231, v230, s94
v_perm_b32 v181, v231, v230, s95
ar v230,a82
ar v231,a86
v_perm_b32 v178, v231, v230, s94
v_perm_b32 v182, v231, v230, s95
ar v230,a90
ar v231,a94
v_perm_b32 v179, v231, v230, s94
v_perm_b32 v183, v231, v230, s95
ar v230,a67
ar v231,a71
v_perm_b32 v184, v231, v230, s94
v_perm_b32 v188, v231, v230, s95
ar v230,a75
ar v231,a79
v_perm_b32 v185, v231, v230, s94
v_perm_b32 v189, v231, v230, s95
ar v230,a83
ar v231,a87
v_perm_b32 v186, v231, v230, s94
v_perm_b32 v190, v231, v230, s95
ar v230,a91
)ASM" R"ASM(ar v231,a95
v_perm_b32 v187, v231, v230, s94
v_perm_b32 v191, v231, v230, s95
s_and_b32 s92, s3, 1
s_lshr_b32 s93, s3, 1
v_lshrrev_b32 v230, 6, v255
v_and_b32 v231, 48, v255
v_lshl_add_u32 v240, v230, 10, v231
s_lshl_b32 s96, s92, 14
v_add_u32 v240, s96, v240
s_lshl_b32 s96, s92, 10
v_add_u32 v241, s96, v255
s_lshl_b32 s97, s3, 10
v_add_u32 v208, s97, v255
v_bfe_u32 v230, v255, 4, 1
v_bfe_u32 v231, v255, 6, 2
v_lshrrev_b32 v232, 8, v255
v_lshlrev_b32 v209, 11, v230
v_lshl_add_u32 v209, v231, 7, v209
v_lshl_add_u32 v209, v232, 5, v209
s_lshr_b32 s96, s96, 1
v_add_u32 v209, s96, v209
v_bfe_u32 v233, v255, 4, 2
v_mul_u32_u24 v210, 640, v233
v_lshl_add_u32 v210, v231, 3, v210
s_mul_i32 s96, s93, 2560
s_lshl_b32 s92, s92, 5
s_add_u32 s96, s96, s92
v_add_u32 v210, s96, v210
v_cmp_eq_u32 s[98:99], v231, v232
v_mul_u32_u24 v211, 5, v255
s_lshl_b32 s96, s3, 7
v_lshrrev_b32 v212, 3, v255
v_and_b32 v212, 62, v212
v_add_u32 v212, s96, v212
v_lshlrev_b32 v234, 6, v230
v_lshl_add_u32 v234, v232, 4, v234
v_add_u32 v234, s96, v234
v_mov_b32 v235, 1024
v_cmp_eq_u32 vcc, 0, v231
s_nop 1
v_cndmask_b32 v216, v235, v234, vcc
v_cmp_eq_u32 vcc, 1, v231
s_nop 1
v_cndmask_b32 v217, v235, v234, vcc
v_cmp_eq_u32 vcc, 2, v231
s_nop 1
v_cndmask_b32 v218, v235, v234, vcc
v_cmp_eq_u32 vcc, 3, v231
s_nop 1
v_cndmask_b32 v219, v235, v234, vcc
v_mov_b32 v220, 0
v_mov_b32 v221, 0
v_mov_b32 v222, 0
v_mov_b32 v223, 0
ds_write_b128 v235, v[220:223] offset:10240
s_lshl_b32 s92, s100, 13
s_add_u32 s80, s92, 32768
s_add_u32 s20, s80, 4096
; ---- top two rows of the sequence; SwiGLU slice of 32 hidden units
.La5_LT_SEL:
.La5_LW4_1:
s_cbranch_execz .La5_LW4_R0
s_and_b32 s97, s2, 7
s_lshl_b32 s97, s97, 12
s_cmp_lg_u32 s3, 0
s_cbranch_scc1 .La5_LT_S2
s_mov_b32 s96, 0
v_and_b32 v229, 255, v255
.La5_LT_SA:
s_sleep 2
ld v[224:227],v229,s[44:47],s97 offen offset:0 sc1
s_add_u32 s96, s96, 1
s_lshr_b32 s92, s96, 20
wt vmcnt(0)
v_cmp_ne_u32 vcc, 0, v224
s_cbranch_scc1 .La5_LT_S1
s_cmp_eq_u64 vcc, -1
s_nop 0
s_nop 0
s_cbranch_scc0 .La5_LT_SA
.La5_LT_S1:
s_cmp_lg_u32 s30, 0
s_cbranch_scc1 .La5_LT_S0
s_sleep 6
.La5_LT_S0:
ld v[224:227],v255,s[44:47],s97 offen offset:0 sc1
ld v[232:235],v255,s[44:47],s97 offen offset:1024 sc1
ld v[244:247],v255,s[44:47],s97 offen offset:2048 sc1
ld v[248:251],v255,s[44:47],s97 offen offset:3072 sc1
s_add_u32 s96, s96, 1
s_lshr_b32 s92, s96, 20
wt vmcnt(3)
v_min3_u32 v228, v224, v225, v226
v_min_u32 v228, v227, v228
wt vmcnt(2)
v_min3_u32 v228, v232, v233, v228
v_min3_u32 v228, v234, v235, v228
wt vmcnt(1)
v_min3_u32 v228, v244, v245, v228
)ASM" R"ASM(v_min3_u32 v228, v246, v247, v228
wt vmcnt(0)
v_min3_u32 v228, v248, v249, v228
v_min3_u32 v228, v250, v251, v228
v_cmp_ne_u32 vcc, 0, v228
v_max_u32 v229, v224, v232
v_max_u32 v230, v244, v248
s_cbranch_scc1 .La5_LT_S3
s_cmp_eq_u64 vcc, -1
s_nop 0
s_nop 0
s_cbranch_scc0 .La5_LT_S1
.La5_LT_S3:
v_max_u32 v228, v229, v230
v_min_u32 v229, v229, v230
v_mov_b32 v231, v228
v_min_u32 v224, v224, v232
v_min_u32 v244, v244, v248
v_max3_u32 v229, v229, v224, v244
v_max3_u32 v230, v226, v234, v246
v_max3_u32 v229, v229, v230, v250
s_nop 1
v_max_u32_dpp v231, v231, v231 row_shr:1 row_mask:0xf bank_mask:0xf
s_nop 1
v_max_u32_dpp v231, v231, v231 row_shr:2 row_mask:0xf bank_mask:0xf
s_nop 1
v_max_u32_dpp v231, v231, v231 row_shr:4 row_mask:0xf bank_mask:0xf
s_nop 1
v_max_u32_dpp v231, v231, v231 row_shr:8 row_mask:0xf bank_mask:0xf
s_nop 1
v_max_u32_dpp v231, v231, v231 row_bcast:15 row_mask:0xa bank_mask:0xf
s_nop 1
v_max_u32_dpp v231, v231, v231 row_bcast:31 row_mask:0xc bank_mask:0xf
s_nop 0
v_readlane_b32 s82, v231, 63
s_nop 1
v_cmp_eq_u32 vcc, s82, v228
v_mov_b32 v230, s82
v_mov_b32 v224, 0
v_cndmask_b32 v228, v228, v229, vcc
s_nop 1
v_max_u32_dpp v228, v228, v228 row_shr:1 row_mask:0xf bank_mask:0xf
s_nop 1
v_max_u32_dpp v228, v228, v228 row_shr:2 row_mask:0xf bank_mask:0xf
s_nop 1
v_max_u32_dpp v228, v228, v228 row_shr:4 row_mask:0xf bank_mask:0xf
s_nop 1
v_max_u32_dpp v228, v228, v228 row_shr:8 row_mask:0xf bank_mask:0xf
s_nop 1
v_max_u32_dpp v228, v228, v228 row_bcast:15 row_mask:0xa bank_mask:0xf
s_nop 1
v_max_u32_dpp v228, v228, v228 row_bcast:31 row_mask:0xc bank_mask:0xf
s_nop 0
v_readlane_b32 s81, v228, 63
s_nop 1
v_mov_b32 v231, s81
ds_write_b64 v224, v[230:231] offset:64
.La5_LT_S2:
v_mov_b32 v224, 0
wt lgkmcnt(0)
s_barrier
ds_read_b64 v[228:229], v224 offset:64
wt lgkmcnt(0)
v_readfirstlane_b32 s82, v228
v_readfirstlane_b32 s81, v229
s_nop 0
s_andn2_b32 s84, 0x7ff, s82
s_andn2_b32 s21, 0x7ff, s81
s_cmp_eq_u32 s30, 0
s_cbranch_scc1 .La5_LT_ST
s_cmp_lt_u32 s3, 2
s_cselect_b32 s91, s84, s21
s_cselect_b32 s85, s82, s81
s_lshr_b32 s92, s91, 4
s_lshl_b32 s92, s92, 15
s_and_b32 s93, s91, 15
s_lshl_b32 s93, s93, 6
s_add_u32 s92, s92, s93
s_add_u32 s93, s92, 4194304
s_add_u32 s94, s92, 8388608
s_add_u32 s95, s92, 12582912
ld v[224:227],v240,s[32:35],s92 offen sc1
ld v[228:231],v240,s[32:35],s93 offen sc1
ld v[232:235],v240,s[32:35],s94 offen sc1
ld v[244:247],v240,s[32:35],s95 offen sc1
s_lshl_b32 s96, s91, 11
s_and_b32 s92, s91, 0xf8
s_lshl4_add_u32 s97, s92, s97
s_lshr_b32 s92, s91, 8
s_lshl4_add_u32 s97, s92, s97
ld v[248:251],v241,s[68:71],s96 offen
ld v[220:223],off,s[44:47],s97 sc1
s_lshl_b32 s86, s76, 5
s_add_u32 s86, s86, s90
s_lshl2_add_u32 s86, s86, s3
s_lshl_b32 s87, s86, 1
v_bfe_u32 v40, v255, 6, 1
v_add_u32 v41, s87, v40
v_cmp_eq_u32 vcc, s84, v41
v_cmp_eq_u32 s[94:95], s21, v41
)ASM" R"ASM(v_lshrrev_b32 v42, 7, v255
v_and_b32 v43, 48, v255
v_lshl_add_u32 v42, v42, 6, v43
s_or_b64 vcc, vcc, s[94:95]
v_lshl_add_u32 v42, v40, 11, v42
v_mov_b32 v43, 1073741824
s_lshl_b32 s86, s86, 12
v_cndmask_b32 v42, v42, v43, vcc
wt vmcnt(4)
v_lshlrev_b32 v236, 16, v224
v_and_b32 v237, 0xffff0000, v224
v_lshlrev_b32 v238, 16, v228
v_and_b32 v239, 0xffff0000, v228
v_pk_add_f32 v[236:237], v[236:237], v[238:239]
v_lshlrev_b32 v242, 16, v225
v_and_b32 v243, 0xffff0000, v225
v_lshlrev_b32 v238, 16, v229
v_and_b32 v239, 0xffff0000, v229
v_pk_add_f32 v[242:243], v[242:243], v[238:239]
v_lshlrev_b32 v252, 16, v226
v_and_b32 v253, 0xffff0000, v226
v_lshlrev_b32 v238, 16, v230
v_and_b32 v239, 0xffff0000, v230
v_pk_add_f32 v[252:253], v[252:253], v[238:239]
v_lshlrev_b32 v214, 16, v227
v_and_b32 v215, 0xffff0000, v227
v_lshlrev_b32 v238, 16, v231
v_and_b32 v239, 0xffff0000, v231
v_pk_add_f32 v[214:215], v[214:215], v[238:239]
wt vmcnt(2)
v_lshlrev_b32 v224, 16, v232
v_and_b32 v225, 0xffff0000, v232
v_lshlrev_b32 v226, 16, v244
v_and_b32 v227, 0xffff0000, v244
v_pk_add_f32 v[224:225], v[224:225], v[226:227]
v_pk_add_f32 v[236:237], v[236:237], v[224:225]
v_lshlrev_b32 v224, 16, v233
v_and_b32 v225, 0xffff0000, v233
v_lshlrev_b32 v226, 16, v245
v_and_b32 v227, 0xffff0000, v245
v_pk_add_f32 v[224:225], v[224:225], v[226:227]
v_pk_add_f32 v[242:243], v[242:243], v[224:225]
.La5_LW4_2:
s_cbranch_execz .La5_LW4_R0
v_lshlrev_b32 v224, 16, v234
v_and_b32 v225, 0xffff0000, v234
v_lshlrev_b32 v226, 16, v246
v_and_b32 v227, 0xffff0000, v246
v_pk_add_f32 v[224:225], v[224:225], v[226:227]
v_pk_add_f32 v[252:253], v[252:253], v[224:225]
v_lshlrev_b32 v224, 16, v235
v_and_b32 v225, 0xffff0000, v235
v_lshlrev_b32 v226, 16, v247
v_and_b32 v227, 0xffff0000, v247
v_pk_add_f32 v[224:225], v[224:225], v[226:227]
v_pk_add_f32 v[214:215], v[214:215], v[224:225]
wt vmcnt(0)
v_cmp_eq_u32 vcc, s85, v220
s_nop 1
v_cndmask_b32 v220, v223, v221, vcc
v_lshlrev_b32 v222, 16, v220
v_rcp_f32 v222, v222
v_and_b32 v220, 0xffff0000, v220
v_mul_f32 v220, v220, v222
v_mov_b32 v223, v222
v_xor_b32 v220, 0x80000000, v220
v_mov_b32 v221, v220
v_lshlrev_b32 v224, 16, v248
v_and_b32 v225, 0xffff0000, v248
v_pk_add_f32 v[236:237], v[236:237], v[224:225]
cv v224,v236,v237
v_lshlrev_b32 v226, 16, v224
v_and_b32 v227, 0xffff0000, v224
v_pk_fma_f32 v[226:227], v[226:227], v[222:223], v[220:221]
cv v228,v226,v227
v_lshlrev_b32 v224, 16, v249
v_and_b32 v225, 0xffff0000, v249
v_pk_add_f32 v[242:243], v[242:243], v[224:225]
cv v224,v242,v243
v_lshlrev_b32 v226, 16, v224
v_and_b32 v227, 0xffff0000, v224
v_pk_fma_f32 v[226:227], v[226:227], v[222:223], v[220:221]
cv v229,v226,v227
v_lshlrev_b32 v224, 16, v250
v_and_b32 v225, 0xffff0000, v250
v_pk_add_f32 v[252:253], v[252:253], v[224:225]
cv v224,v252,v253
v_lshlrev_b32 v226, 16, v224
v_and_b32 v227, 0xffff0000, v224
v_pk_fma_f32 v[226:227], v[226:227], v[222:223], v[220:221]
cv v230,v226,v227
v_lshlrev_b32 v224, 16, v251
v_and_b32 v225, 0xffff0000, v251
v_pk_add_f32 v[214:215], v[214:215], v[224:225]
cv v224,v214,v215
v_lshlrev_b32 v226, 16, v224
v_and_b32 v227, 0xffff0000, v224
v_pk_fma_f32 v[226:227], v[226:227], v[222:223], v[220:221]
cv v231,v226,v227
ds_write_b128 v208, v[228:231] offset:1024
)ASM" R"ASM(wt lgkmcnt(0)
s_barrier
rd v[224:227],v209 offset:1024
rd v[232:235],v209 offset:1040
rd v[244:247],v209 offset:2048
rd v[248:251],v209 offset:2064
wt lgkmcnt(3)
mm a[0:3],v[224:227],a[96:99],0
mm a[4:7],v[224:227],a[100:103],0
mm a[8:11],v[224:227],a[104:107],0
mm a[12:15],v[224:227],a[108:111],0
mm a[16:19],v[224:227],a[112:115],0
mm a[20:23],v[224:227],a[116:119],0
mm a[24:27],v[224:227],a[120:123],0
mm a[28:31],v[224:227],a[124:127],0
st v[192:195],v42,s[56:59],s86 offen offset:0 sc1
wt lgkmcnt(2)
mm a[0:3],v[232:235],a[128:131],a[0:3]
mm a[4:7],v[232:235],a[132:135],a[4:7]
mm a[8:11],v[232:235],a[136:139],a[8:11]
mm a[12:15],v[232:235],a[140:143],a[12:15]
mm a[16:19],v[232:235],a[144:147],a[16:19]
mm a[20:23],v[232:235],a[148:151],a[20:23]
mm a[24:27],v[232:235],a[152:155],a[24:27]
mm a[28:31],v[232:235],a[156:159],a[28:31]
st v[196:199],v42,s[56:59],s86 offen offset:512 sc1
wt lgkmcnt(1)
mm a[0:3],v[244:247],a[160:163],a[0:3]
mm a[4:7],v[244:247],a[164:167],a[4:7]
mm a[8:11],v[244:247],a[168:171],a[8:11]
mm a[12:15],v[244:247],a[172:175],a[12:15]
mm a[16:19],v[244:247],a[176:179],a[16:19]
mm a[20:23],v[244:247],a[180:183],a[20:23]
mm a[24:27],v[244:247],a[184:187],a[24:27]
mm a[28:31],v[244:247],a[188:191],a[28:31]
st v[200:203],v42,s[56:59],s86 offen offset:1024 sc1
wt lgkmcnt(0)
mm a[0:3],v[248:251],a[192:195],a[0:3]
mm a[4:7],v[248:251],a[196:199],a[4:7]
mm a[8:11],v[248:251],a[200:203],a[8:11]
mm a[12:15],v[248:251],a[204:207],a[12:15]
mm a[16:19],v[248:251],a[208:211],a[16:19]
mm a[20:23],v[248:251],a[212:215],a[20:23]
mm a[24:27],v[248:251],a[216:219],a[24:27]
mm a[28:31],v[248:251],a[220:223],a[28:31]
st v[204:207],v42,s[56:59],s86 offen offset:1536 sc1
s_mov_b64 exec, s[98:99]
ds_write_b64 v210, a[0:1] offset:5120
ds_write_b64 v210, a[4:5] offset:5200
ds_write_b64 v210, a[8:9] offset:5280
ds_write_b64 v210, a[12:13] offset:5360
ds_write_b64 v210, a[16:17] offset:5440
s_nop 3
ds_write_b64 v210, a[20:21] offset:5520
s_nop 3
ds_write_b64 v210, a[24:25] offset:5600
s_nop 3
ds_write_b64 v210, a[28:29] offset:5680
s_mov_b64 exec, -1
wt lgkmcnt(0)
s_barrier
rd v[224:227],v211 offset:5120
rd v[232:235],v211 offset:5136
rd v[244:247],v211 offset:5152
rd v[248:251],v211 offset:5168
wt lgkmcnt(2)
v_pk_add_f32 v[224:225], v[224:225], v[226:227]
v_pk_add_f32 v[232:233], v[232:233], v[234:235]
wt lgkmcnt(0)
v_pk_add_f32 v[244:245], v[244:245], v[246:247]
v_pk_add_f32 v[248:249], v[248:249], v[250:251]
v_pk_add_f32 v[224:225], v[224:225], v[232:233]
v_pk_add_f32 v[244:245], v[244:245], v[248:249]
v_pk_add_f32 v[224:225], v[224:225], v[244:245]
cv v224,v224,v225
v_mov_b32 v226, v224
s_nop 1
v_permlane32_swap_b32 v224, v226
s_nop 1
v_lshlrev_b32 v228, 16, v224
v_and_b32 v229, 0xffff0000, v224
v_lshlrev_b32 v232, 16, v226
v_and_b32 v233, 0xffff0000, v226
v_mul_f32 v230, 0xbfb8aa3b, v228
v_mul_f32 v231, 0xbfb8aa3b, v229
ex v230,v230
ex v231,v231
v_add_f32 v230, 1.0, v230
v_add_f32 v231, 1.0, v231
v_rcp_f32 v230, v230
v_rcp_f32 v231, v231
s_nop 0
v_pk_mul_f32 v[228:229], v[228:229], v[230:231]
cv v228,v228,v229
v_lshlrev_b32 v230, 16, v228
v_and_b32 v231, 0xffff0000, v228
v_pk_mul_f32 v[230:231], v[230:231], v[232:233]
cv v228,v230,v231
ds_write_b16 v212, v228 offset:10240
ds_write_b16_d16_hi v212, v228 offset:10304
)ASM" R"ASM(wt lgkmcnt(0)
rd v[224:227],v216 offset:10240
rd v[232:235],v217 offset:10240
rd v[244:247],v218 offset:10240
rd v[248:251],v219 offset:10240
wt lgkmcnt(3)
mm v[0:3],v[224:227],v[128:131],0
mm v[4:7],v[224:227],v[132:135],0
mm v[8:11],v[224:227],v[136:139],0
mm v[12:15],v[224:227],v[140:143],0
wt lgkmcnt(2)
mm v[0:3],v[232:235],v[144:147],v[0:3]
mm v[4:7],v[232:235],v[148:151],v[4:7]
mm v[8:11],v[232:235],v[152:155],v[8:11]
mm v[12:15],v[232:235],v[156:159],v[12:15]
wt lgkmcnt(1)
mm v[0:3],v[244:247],v[160:163],v[0:3]
mm v[4:7],v[244:247],v[164:167],v[4:7]
mm v[8:11],v[244:247],v[168:171],v[8:11]
mm v[12:15],v[244:247],v[172:175],v[12:15]
wt lgkmcnt(0)
mm v[0:3],v[248:251],v[176:179],v[0:3]
mm v[4:7],v[248:251],v[180:183],v[4:7]
mm v[8:11],v[248:251],v[184:187],v[8:11]
mm v[12:15],v[248:251],v[188:191],v[12:15]
s_nop 7
v_or_b32 v16, 1, v0
v_or_b32 v20, 1, v1
v_or_b32 v17, 1, v4
v_or_b32 v21, 1, v5
v_or_b32 v18, 1, v8
v_or_b32 v22, 1, v9
v_or_b32 v19, 1, v12
v_or_b32 v23, 1, v13
st v[16:19],v208,s[44:47],s80 offen sc1
st v[20:23],v208,s[44:47],s20 offen sc1
s_branch .La5_LT_END
; ---- rows that were not routed leave; final sums of the routed rows
.La5_LT_ST:
.La5_LW4_3:
s_cbranch_execz .La5_LW4_R0
s_sleep 28
s_lshl_b32 s92, s76, 5
s_add_u32 s92, s92, s90
s_lshl2_add_u32 s92, s92, s3
s_lshl_b32 s93, s92, 1
v_bfe_u32 v230, v255, 6, 1
v_add_u32 v231, s93, v230
v_cmp_ne_u32 vcc, s84, v231
v_cmp_ne_u32 s[94:95], s21, v231
v_lshrrev_b32_e64 v232, 7, v255
v_and_b32_e64 v233, 48, v255
v_lshl_add_u32 v232, v232, 6, v233
v_lshl_add_u32 v232, v230, 11, v232
s_lshl_b32 s92, s92, 12
s_and_b64 exec, vcc, s[94:95]
s_cbranch_scc0 .La5_LT_F0
st v[192:195],v232,s[56:59],s92 offen offset:0 sc1
st v[196:199],v232,s[56:59],s92 offen offset:512 sc1
st v[200:203],v232,s[56:59],s92 offen offset:1024 sc1
st v[204:207],v232,s[56:59],s92 offen offset:1536 sc1
.La5_LT_F0:
s_mov_b64 exec, -1
s_cmp_eq_u32 s75, 1
s_cbranch_scc1 .La5_LT_END
s_lshr_b32 s92, s2, 6
s_lshl2_add_u32 s92, s92, s74
s_and_b32 s93, s2, 7
s_lshl3_add_u32 s91, s92, s93
s_cmp_lt_u32 s91, 64
s_cselect_b32 s84, s84, s21
s_cselect_b32 s82, s82, s81
s_bfe_u32 s92, s91, 0x30003
s_and_b32 s93, s91, 3
s_lshl2_add_u32 s96, s92, s93
s_and_b32 s85, s91, 4
s_lshl_b32 s85, s85, 1
v_bfe_u32 v0, v255, 6, 2
v_bfe_u32 v1, v255, 4, 2
v_lshlrev_b32 v1, 1, v1
v_lshl_add_u32 v0, v0, 4, v1
v_add_u32 v0, s85, v0
s_lshl_b32 s85, s96, 10
v_add_u32 v1, s85, v0
s_lshl_b32 s85, s96, 6
v_add_u32 v0, s85, v0
s_lshr_b32 s92, s84, 4
s_lshl_b32 s92, s92, 15
s_and_b32 s93, s84, 15
s_lshl_b32 s93, s93, 6
s_add_u32 s92, s92, s93
s_add_u32 s93, s92, 4194304
s_add_u32 s94, s92, 8388608
s_add_u32 s95, s92, 12582912
buffer_load_ushort v4, v1, s[32:35], s92 offen sc1
buffer_load_ushort v5, v1, s[32:35], s93 offen sc1
buffer_load_ushort v6, v1, s[32:35], s94 offen sc1
buffer_load_ushort v7, v1, s[32:35], s95 offen sc1
s_lshl_b32 s21, s84, 11
s_and_b32 s92, s84, 0xf8
)ASM" R"ASM(s_lshl4_add_u32 s97, s92, s97
s_lshr_b32 s92, s84, 8
s_lshl4_add_u32 s97, s92, s97
buffer_load_ushort v8, v0, s[68:71], s21 offen
ld v[12:15],off,s[44:47],s97 sc1
v_lshrrev_b32 v2, 6, v255
v_lshlrev_b32 v2, 13, v2
v_and_b32 v3, 48, v255
v_add_u32 v2, v2, v3
s_lshl_b32 s92, s3, 17
s_lshl_b32 s93, s91, 6
s_add_u32 s92, s92, s93
s_add_u32 s94, s92, 32768
s_cmp_eq_u32 s3, 3
s_cselect_b32 s95, 0, 524288
s_cselect_b32 s96, 0, -1
s_add_u32 s95, s95, s94
s_ashr_i32 s92, s82, 31
s_orn2_b32 s92, 0x80000000, s92
s_xor_b32 s92, s92, s82
s_and_b32 s92, s92, 0xffff0000
v_mov_b32 v9, 0xbfb8aa3b
v_mul_f32 v9, s92, v9
ex v9,v9
v_lshrrev_b32 v3, 8, v255
s_lshl_b32 s92, s3, 4
v_add_f32 v9, 1.0, v9
v_lshl_add_u32 v3, v3, 2, s92
v_rcp_f32 v9, v9
v_and_b32 v10, 48, v255
v_mul_u32_u24 v10, 20, v10
v_add_u32 v3, v3, v10
cv v9,v9,v9
v_lshlrev_b32 v9, 16, v9
v_and_b32 v10, 0xf0, v255
v_mul_u32_u24 v10, 5, v10
wt vmcnt(0)
v_cmp_eq_u32 vcc, s82, v12
s_nop 1
v_cndmask_b32 v12, v15, v13, vcc
v_lshlrev_b32 v14, 16, v12
v_rcp_f32 v14, v14
v_and_b32 v12, 0xffff0000, v12
v_mul_f32 v12, v12, v14
v_mov_b32 v15, v14
v_xor_b32 v12, 0x80000000, v12
v_mov_b32 v13, v12
v_lshlrev_b32 v4, 16, v4
v_lshlrev_b32 v5, 16, v5
v_lshlrev_b32 v6, 16, v6
v_lshlrev_b32 v7, 16, v7
v_lshlrev_b32 v8, 16, v8
v_add_f32 v4, v4, v5
v_add_f32 v6, v6, v7
v_add_f32 v4, v4, v6
v_add_f32 v4, v4, v8
cv v4,v4,v4
v_lshlrev_b32 v4, 16, v4
v_fma_f32 v5, v4, v14, v12
cv v5,v5,v5
v_lshlrev_b32 v5, 16, v5
s_sleep 20
s_mov_b32 s93, 0
.La5_LT_Y:
ld v[16:19],v2,s[44:47],s94 offen sc1
ld v[20:23],v2,s[44:47],s95 offen sc1
s_add_u32 s93, s93, 1
s_lshr_b32 s92, s93, 20
wt vmcnt(0)
v_min3_u32 v24, v16, v17, v18
v_min3_u32 v25, v20, v21, v22
v_min3_u32 v24, v24, v19, v23
v_min_u32 v24, v24, v25
v_cmp_ne_u32 vcc, 0, v24
v_and_b32 v20, s96, v20
v_and_b32 v21, s96, v21
v_and_b32 v22, s96, v22
v_and_b32 v23, s96, v23
s_cbranch_scc1 .La5_LT_Y1
s_cmp_eq_u64 vcc, -1
s_nop 0
s_nop 0
s_cbranch_scc0 .La5_LT_Y
.La5_LT_Y1:
v_pk_add_f32 v[16:17], v[16:17], v[20:21]
v_pk_add_f32 v[18:19], v[18:19], v[22:23]
s_nop 0
v_add_f32_dpp v16, v16, v16 row_ror:4 row_mask:0xf bank_mask:0xf
v_add_f32_dpp v17, v17, v17 row_ror:4 row_mask:0xf bank_mask:0xf
v_add_f32_dpp v18, v18, v18 row_ror:4 row_mask:0xf bank_mask:0xf
v_add_f32_dpp v19, v19, v19 row_ror:4 row_mask:0xf bank_mask:0xf
v_add_f32_dpp v16, v16, v16 row_ror:8 row_mask:0xf bank_mask:0xf
v_add_f32_dpp v17, v17, v17 row_ror:8 row_mask:0xf bank_mask:0xf
v_add_f32_dpp v18, v18, v18 row_ror:8 row_mask:0xf bank_mask:0xf
v_add_f32_dpp v19, v19, v19 row_ror:8 row_mask:0xf bank_mask:0xf
ds_write_b32 v3, v16 offset:5120
ds_write_b32 v3, v17 offset:5200
ds_write_b32 v3, v18 offset:5280
ds_write_b32 v3, v19 offset:5360
wt lgkmcnt(0)
)ASM" R"ASM(s_barrier
rd v[16:19],v10 offset:5120
rd v[20:23],v10 offset:5136
rd v[24:27],v10 offset:5152
rd v[28:31],v10 offset:5168
wt lgkmcnt(2)
v_pk_add_f32 v[16:17], v[16:17], v[18:19]
v_pk_add_f32 v[20:21], v[20:21], v[22:23]
wt lgkmcnt(0)
v_pk_add_f32 v[24:25], v[24:25], v[26:27]
v_pk_add_f32 v[28:29], v[28:29], v[30:31]
v_pk_add_f32 v[16:17], v[16:17], v[20:21]
v_pk_add_f32 v[24:25], v[24:25], v[28:29]
v_pk_add_f32 v[16:17], v[16:17], v[24:25]
v_add_f32 v16, v16, v17
cv v16,v16,v16
v_lshlrev_b32 v16, 16, v16
v_mul_f32 v16, v16, v9
cv v16,v16,v16
v_lshlrev_b32 v16, 16, v16
v_add_f32 v16, v16, v5
cv v16,v16,v16
v_lshlrev_b32 v16, 16, v16
v_add_f32 v16, v16, v4
cv v16,v16,v16
s_cmp_lg_u32 s3, 0
s_cbranch_scc1 .La5_LT_END
s_mov_b64 exec, 0xffff
buffer_store_short v16, v0, s[56:59], s21 offen
s_mov_b64 exec, -1
.La5_LT_END:
s_nop 0
s_nop 0
.La5_LW4_4:
s_cbranch_execz .La5_LW4_R0
s_lshl2_add_u32 s92, s2, s3
s_cmp_lg_u32 s92, 992
s_cbranch_scc1 .La5_LT_E1
buffer_store_dwordx2 v[252:253], off, s[64:67], 0 sc1
.La5_LT_E1:
s_branch .La5_LEND
; ---- fallback: the earlier 32 rows against all their keys with the true running maximum (online softmax)
.La5_LA_SLOW0:
wt lgkmcnt(0)
s_nop 7
s_nop 7
aw a160,v192
aw a161,v193
aw a162,v194
aw a163,v195
aw a164,v196
aw a165,v197
aw a166,v198
aw a167,v199
aw a168,v200
aw a169,v201
aw a170,v202
aw a171,v203
aw a172,v204
aw a173,v205
aw a174,v206
aw a175,v207
aw a176,v208
aw a177,v209
aw a178,v210
aw a179,v211
aw a180,v212
aw a181,v213
aw a182,v214
aw a183,v215
aw a184,v216
aw a185,v217
aw a186,v218
aw a187,v219
aw a188,v220
aw a189,v221
aw a190,v222
aw a191,v223
s_lshl_b32 s96, s72, 19
s_add_u32 s96, s96, 20971520
s_add_u32 s92, s26, s96
s_addc_u32 s93, s27, 0
s_and_b32 s93, s93, 0xffff
s_mov_b32 s94, 17301504
s_mov_b32 s95, 0x20000
v_mov_b32 v0, 0
v_mov_b32 v1, 0
v_mov_b32 v2, 0
v_mov_b32 v3, 0
v_mov_b32 v4, 0
v_mov_b32 v5, 0
v_mov_b32 v6, 0
v_mov_b32 v7, 0
v_mov_b32 v8, 0
v_mov_b32 v9, 0
v_mov_b32 v10, 0
v_mov_b32 v11, 0
v_mov_b32 v12, 0
v_mov_b32 v13, 0
v_mov_b32 v14, 0
)ASM" R"ASM(v_mov_b32 v15, 0
v_mov_b32 v16, 0
v_mov_b32 v17, 0
v_mov_b32 v18, 0
v_mov_b32 v19, 0
v_mov_b32 v20, 0
v_mov_b32 v21, 0
v_mov_b32 v22, 0
v_mov_b32 v23, 0
v_mov_b32 v24, 0
v_mov_b32 v25, 0
v_mov_b32 v26, 0
v_mov_b32 v27, 0
v_mov_b32 v28, 0
v_mov_b32 v29, 0
v_mov_b32 v30, 0
v_mov_b32 v31, 0
v_mov_b32 v32, 0
v_mov_b32 v33, 0
v_mov_b32 v34, 0
v_mov_b32 v35, 0
v_mov_b32 v36, 0
v_mov_b32 v37, 0
v_mov_b32 v38, 0
v_mov_b32 v39, 0
v_mov_b32 v40, 0
v_mov_b32 v41, 0
v_mov_b32 v42, 0
v_mov_b32 v43, 0
v_mov_b32 v44, 0
v_mov_b32 v45, 0
v_mov_b32 v46, 0
v_mov_b32 v47, 0
v_mov_b32 v48, 0
v_mov_b32 v49, 0
v_mov_b32 v50, 0
v_mov_b32 v51, 0
v_mov_b32 v52, 0
v_mov_b32 v53, 0
v_mov_b32 v54, 0
v_mov_b32 v55, 0
v_mov_b32 v56, 0
v_mov_b32 v57, 0
v_mov_b32 v58, 0
v_mov_b32 v59, 0
v_mov_b32 v60, 0
v_mov_b32 v61, 0
v_mov_b32 v62, 0
v_mov_b32 v63, 0
v_mov_b32 v216, 0xff61b1e6
v_mov_b32 v217, 0xff61b1e6
v_mov_b32 v218, 0
v_mov_b32 v219, 0
s_mov_b32 s7, 0
.La5_LA_SLOW0_L:
s_lshl_b32 s96, s7, 13
s_add_u32 s97, s96, 4096
ld a[128:131],v255,s[92:95],s96 offen offset:0 sc1
ld a[132:135],v255,s[92:95],s96 offen offset:1024 sc1
ld a[136:139],v255,s[92:95],s96 offen offset:2048 sc1
ld a[140:143],v255,s[92:95],s96 offen offset:3072 sc1
ld a[144:147],v255,s[92:95],s97 offen offset:0 sc1
ld a[148:151],v255,s[92:95],s97 offen offset:1024 sc1
ld a[152:155],v255,s[92:95],s97 offen offset:2048 sc1
ld a[156:159],v255,s[92:95],s97 offen offset:3072 sc1
s_lshl_b32 s96, s76, 3
s_sub_u32 s96, s96, s7
s_add_u32 s96, s96, s3
s_lshl_b32 s96, s96, 5
v_bfe_u32 v220, v255, 4, 4
v_lshrrev_b32 v221, 8, v255
v_lshlrev_b32 v221, 2, v221
v_sub_u32 v220, v220, v221
v_add_u32 v220, s96, v220
v_mov_b32 v222, 0xceabfb8f
wt vmcnt(0)
mm v[192:195],a[128:131],v[128:131],0
mm v[192:195],a[132:135],v[132:135],v[192:195]
mm v[192:195],a[136:139],v[136:139],v[192:195]
mm v[192:195],a[140:143],v[140:143],v[192:195]
mm v[196:199],a[144:147],v[128:131],0
mm v[196:199],a[148:151],v[132:135],v[196:199]
mm v[196:199],a[152:155],v[136:139],v[196:199]
mm v[196:199],a[156:159],v[140:143],v[196:199]
mm v[200:203],a[128:131],v[144:147],0
mm v[200:203],a[132:135],v[148:151],v[200:203]
mm v[200:203],a[136:139],v[152:155],v[200:203]
mm v[200:203],a[140:143],v[156:159],v[200:203]
mm v[204:207],a[144:147],v[144:147],0
mm v[204:207],a[148:151],v[148:151],v[204:207]
mm v[204:207],a[152:155],v[152:155],v[204:207]
mm v[204:207],a[156:159],v[156:159],v[204:207]
s_nop 7
s_nop 7
s_lshl_b32 s96, s7, 13
s_add_u32 s96, s96, 16777216
s_add_u32 s97, s96, 4096
ld a[128:131],v255,s[92:95],s96 offen offset:0 sc1
ld a[132:135],v255,s[92:95],s96 offen offset:1024 sc1
ld a[136:139],v255,s[92:95],s96 offen offset:2048 sc1
)ASM" R"ASM(ld a[140:143],v255,s[92:95],s96 offen offset:3072 sc1
ld a[144:147],v255,s[92:95],s97 offen offset:0 sc1
ld a[148:151],v255,s[92:95],s97 offen offset:1024 sc1
ld a[152:155],v255,s[92:95],s97 offen offset:2048 sc1
ld a[156:159],v255,s[92:95],s97 offen offset:3072 sc1
v_cmp_gt_i32 vcc, 0, v220
s_nop 1
v_cndmask_b32 v192, v192, v222, vcc
v_cmp_gt_i32 vcc, 1, v220
s_nop 1
v_cndmask_b32 v193, v193, v222, vcc
v_cmp_gt_i32 vcc, 2, v220
s_nop 1
v_cndmask_b32 v194, v194, v222, vcc
v_cmp_gt_i32 vcc, 3, v220
s_nop 1
v_cndmask_b32 v195, v195, v222, vcc
v_cmp_gt_i32 vcc, 16, v220
s_nop 1
v_cndmask_b32 v196, v196, v222, vcc
v_cmp_gt_i32 vcc, 17, v220
s_nop 1
v_cndmask_b32 v197, v197, v222, vcc
v_cmp_gt_i32 vcc, 18, v220
s_nop 1
v_cndmask_b32 v198, v198, v222, vcc
v_cmp_gt_i32 vcc, 19, v220
s_nop 1
v_cndmask_b32 v199, v199, v222, vcc
v_cmp_gt_i32 vcc, -16, v220
s_nop 1
v_cndmask_b32 v200, v200, v222, vcc
v_cmp_gt_i32 vcc, -15, v220
s_nop 1
v_cndmask_b32 v201, v201, v222, vcc
v_cmp_gt_i32 vcc, -14, v220
s_nop 1
v_cndmask_b32 v202, v202, v222, vcc
v_cmp_gt_i32 vcc, -13, v220
s_nop 1
v_cndmask_b32 v203, v203, v222, vcc
v_cmp_gt_i32 vcc, 0, v220
s_nop 1
v_cndmask_b32 v204, v204, v222, vcc
v_cmp_gt_i32 vcc, 1, v220
s_nop 1
v_cndmask_b32 v205, v205, v222, vcc
v_cmp_gt_i32 vcc, 2, v220
s_nop 1
v_cndmask_b32 v206, v206, v222, vcc
v_cmp_gt_i32 vcc, 3, v220
s_nop 1
v_cndmask_b32 v207, v207, v222, vcc
v_max3_f32 v222, v192, v193, v216
v_max3_f32 v222, v194, v195, v222
v_max3_f32 v222, v196, v197, v222
v_max3_f32 v222, v198, v199, v222
v_mov_b32 v221, v222
s_nop 1
v_permlane16_swap_b32 v221, v222
s_nop 1
v_max_f32 v222, v222, v221
v_mov_b32 v221, v222
s_nop 1
v_permlane32_swap_b32 v221, v222
s_nop 1
v_max_f32 v222, v222, v221
v_sub_f32 v223, v216, v222
v_mov_b32 v216, v222
ex v223,v223
v_sub_f32 v192, v192, v222
v_sub_f32 v193, v193, v222
v_sub_f32 v194, v194, v222
v_sub_f32 v195, v195, v222
v_sub_f32 v196, v196, v222
v_sub_f32 v197, v197, v222
v_sub_f32 v198, v198, v222
v_sub_f32 v199, v199, v222
ex v192,v192
ex v193,v193
ex v194,v194
ex v195,v195
ex v196,v196
ex v197,v197
ex v198,v198
ex v199,v199
v_mul_f32 v218, v218, v223
v_add_f32 v218, v218, v192
v_add_f32 v218, v218, v193
v_add_f32 v218, v218, v194
v_add_f32 v218, v218, v195
v_add_f32 v218, v218, v196
v_add_f32 v218, v218, v197
v_add_f32 v218, v218, v198
v_add_f32 v218, v218, v199
cv v208,v192,v193
cv v209,v194,v195
cv v210,v196,v197
cv v211,v198,v199
v_mul_f32 v0, v0, v223
)ASM" R"ASM(v_mul_f32 v1, v1, v223
v_mul_f32 v2, v2, v223
v_mul_f32 v3, v3, v223
v_mul_f32 v4, v4, v223
v_mul_f32 v5, v5, v223
v_mul_f32 v6, v6, v223
v_mul_f32 v7, v7, v223
v_mul_f32 v8, v8, v223
v_mul_f32 v9, v9, v223
v_mul_f32 v10, v10, v223
v_mul_f32 v11, v11, v223
v_mul_f32 v12, v12, v223
v_mul_f32 v13, v13, v223
v_mul_f32 v14, v14, v223
v_mul_f32 v15, v15, v223
v_mul_f32 v16, v16, v223
v_mul_f32 v17, v17, v223
v_mul_f32 v18, v18, v223
v_mul_f32 v19, v19, v223
v_mul_f32 v20, v20, v223
v_mul_f32 v21, v21, v223
v_mul_f32 v22, v22, v223
v_mul_f32 v23, v23, v223
v_mul_f32 v24, v24, v223
v_mul_f32 v25, v25, v223
v_mul_f32 v26, v26, v223
v_mul_f32 v27, v27, v223
v_mul_f32 v28, v28, v223
v_mul_f32 v29, v29, v223
v_mul_f32 v30, v30, v223
v_mul_f32 v31, v31, v223
v_max3_f32 v222, v200, v201, v217
v_max3_f32 v222, v202, v203, v222
v_max3_f32 v222, v204, v205, v222
v_max3_f32 v222, v206, v207, v222
v_mov_b32 v221, v222
s_nop 1
v_permlane16_swap_b32 v221, v222
s_nop 1
v_max_f32 v222, v222, v221
v_mov_b32 v221, v222
s_nop 1
v_permlane32_swap_b32 v221, v222
s_nop 1
v_max_f32 v222, v222, v221
v_sub_f32 v223, v217, v222
v_mov_b32 v217, v222
ex v223,v223
v_sub_f32 v200, v200, v222
v_sub_f32 v201, v201, v222
v_sub_f32 v202, v202, v222
v_sub_f32 v203, v203, v222
v_sub_f32 v204, v204, v222
v_sub_f32 v205, v205, v222
v_sub_f32 v206, v206, v222
v_sub_f32 v207, v207, v222
ex v200,v200
ex v201,v201
ex v202,v202
ex v203,v203
ex v204,v204
ex v205,v205
ex v206,v206
ex v207,v207
v_mul_f32 v219, v219, v223
v_add_f32 v219, v219, v200
v_add_f32 v219, v219, v201
v_add_f32 v219, v219, v202
v_add_f32 v219, v219, v203
v_add_f32 v219, v219, v204
v_add_f32 v219, v219, v205
v_add_f32 v219, v219, v206
v_add_f32 v219, v219, v207
cv v212,v200,v201
cv v213,v202,v203
cv v214,v204,v205
cv v215,v206,v207
v_mul_f32 v32, v32, v223
v_mul_f32 v33, v33, v223
v_mul_f32 v34, v34, v223
v_mul_f32 v35, v35, v223
v_mul_f32 v36, v36, v223
v_mul_f32 v37, v37, v223
v_mul_f32 v38, v38, v223
v_mul_f32 v39, v39, v223
v_mul_f32 v40, v40, v223
v_mul_f32 v41, v41, v223
v_mul_f32 v42, v42, v223
v_mul_f32 v43, v43, v223
v_mul_f32 v44, v44, v223
v_mul_f32 v45, v45, v223
v_mul_f32 v46, v46, v223
v_mul_f32 v47, v47, v223
v_mul_f32 v48, v48, v223
v_mul_f32 v49, v49, v223
v_mul_f32 v50, v50, v223
v_mul_f32 v51, v51, v223
v_mul_f32 v52, v52, v223
v_mul_f32 v53, v53, v223
v_mul_f32 v54, v54, v223
)ASM" R"ASM(v_mul_f32 v55, v55, v223
v_mul_f32 v56, v56, v223
v_mul_f32 v57, v57, v223
v_mul_f32 v58, v58, v223
v_mul_f32 v59, v59, v223
v_mul_f32 v60, v60, v223
v_mul_f32 v61, v61, v223
v_mul_f32_e64 v62, v62, v223
v_mul_f32_e64 v63, v63, v223
wt vmcnt(0)
mm v[0:3],a[128:131],v[208:211],v[0:3]
mm v[32:35],a[128:131],v[212:215],v[32:35]
mm v[4:7],a[132:135],v[208:211],v[4:7]
mm v[36:39],a[132:135],v[212:215],v[36:39]
mm v[8:11],a[136:139],v[208:211],v[8:11]
mm v[40:43],a[136:139],v[212:215],v[40:43]
mm v[12:15],a[140:143],v[208:211],v[12:15]
mm v[44:47],a[140:143],v[212:215],v[44:47]
mm v[16:19],a[144:147],v[208:211],v[16:19]
mm v[48:51],a[144:147],v[212:215],v[48:51]
mm v[20:23],a[148:151],v[208:211],v[20:23]
mm v[52:55],a[148:151],v[212:215],v[52:55]
mm v[24:27],a[152:155],v[208:211],v[24:27]
mm v[56:59],a[152:155],v[212:215],v[56:59]
mm v[28:31],a[156:159],v[208:211],v[28:31]
mm v[60:63],a[156:159],v[212:215],v[60:63]
s_nop 7
s_nop 7
s_add_u32 s7, s7, 1
s_lshr_b32 s96, s7, 3
s_cmp_le_u32 s96, s76
s_cbranch_scc1 .La5_LA_SLOW0_L
v_mov_b32 v221, v218
s_nop 1
v_permlane16_swap_b32 v221, v218
s_nop 1
v_add_f32 v218, v218, v221
v_mov_b32 v221, v218
s_nop 1
v_permlane32_swap_b32 v221, v218
s_nop 1
v_add_f32 v218, v218, v221
v_mov_b32 v221, v219
s_nop 1
v_permlane16_swap_b32 v221, v219
s_nop 1
v_add_f32 v219, v219, v221
v_mov_b32 v221, v219
s_nop 1
v_permlane32_swap_b32 v221, v219
s_nop 1
v_add_f32 v219, v219, v221
v_rcp_f32 v242, v218
v_rcp_f32 v243, v219
ar v192,a160
ar v193,a161
ar v194,a162
ar v195,a163
ar v196,a164
ar v197,a165
ar v198,a166
ar v199,a167
ar v200,a168
ar v201,a169
ar v202,a170
ar v203,a171
ar v204,a172
ar v205,a173
ar v206,a174
ar v207,a175
ar v208,a176
ar v209,a177
ar v210,a178
ar v211,a179
ar v212,a180
ar v213,a181
ar v214,a182
ar v215,a183
ar v216,a184
ar v217,a185
ar v218,a186
ar v219,a187
ar v220,a188
ar v221,a189
ar v222,a190
ar v223,a191
s_mov_b32 s95, 4096
s_mov_b32 s96, 8192
s_mov_b32 s97, 12288
s_mov_b64 vcc, 0
s_nop 1
s_setpc_b64 s[24:25]
; ---- fallback: the later 32 rows
.La5_LA_SLOW1:
wt lgkmcnt(0)
s_nop 7
s_nop 7
aw a160,v192
aw a161,v193
aw a162,v194
)ASM" R"ASM(aw a163,v195
aw a164,v196
aw a165,v197
aw a166,v198
aw a167,v199
aw a168,v200
aw a169,v201
aw a170,v202
aw a171,v203
aw a172,v204
aw a173,v205
aw a174,v206
aw a175,v207
aw a176,v208
aw a177,v209
aw a178,v210
aw a179,v211
aw a180,v212
aw a181,v213
aw a182,v214
aw a183,v215
aw a184,v216
aw a185,v217
aw a186,v218
aw a187,v219
aw a188,v220
aw a189,v221
aw a190,v222
aw a191,v223
s_lshl_b32 s96, s72, 19
s_add_u32 s96, s96, 20971520
s_add_u32 s92, s26, s96
s_addc_u32 s93, s27, 0
s_and_b32 s93, s93, 0xffff
s_mov_b32 s94, 17301504
s_mov_b32 s95, 0x20000
v_mov_b32 v64, 0
v_mov_b32 v65, 0
v_mov_b32 v66, 0
v_mov_b32 v67, 0
v_mov_b32 v68, 0
v_mov_b32 v69, 0
v_mov_b32 v70, 0
v_mov_b32 v71, 0
v_mov_b32 v72, 0
v_mov_b32 v73, 0
v_mov_b32 v74, 0
v_mov_b32 v75, 0
v_mov_b32 v76, 0
v_mov_b32 v77, 0
v_mov_b32 v78, 0
v_mov_b32 v79, 0
v_mov_b32 v80, 0
v_mov_b32 v81, 0
v_mov_b32 v82, 0
v_mov_b32 v83, 0
v_mov_b32 v84, 0
v_mov_b32 v85, 0
v_mov_b32 v86, 0
v_mov_b32 v87, 0
v_mov_b32 v88, 0
v_mov_b32 v89, 0
v_mov_b32 v90, 0
v_mov_b32 v91, 0
v_mov_b32 v92, 0
v_mov_b32 v93, 0
v_mov_b32 v94, 0
v_mov_b32 v95, 0
v_mov_b32 v96, 0
v_mov_b32 v97, 0
v_mov_b32 v98, 0
v_mov_b32 v99, 0
v_mov_b32 v100, 0
v_mov_b32 v101, 0
v_mov_b32 v102, 0
v_mov_b32 v103, 0
v_mov_b32 v104, 0
v_mov_b32 v105, 0
v_mov_b32 v106, 0
v_mov_b32 v107, 0
v_mov_b32 v108, 0
v_mov_b32 v109, 0
v_mov_b32 v110, 0
v_mov_b32 v111, 0
v_mov_b32 v112, 0
v_mov_b32 v113, 0
v_mov_b32 v114, 0
v_mov_b32 v115, 0
v_mov_b32 v116, 0
v_mov_b32 v117, 0
v_mov_b32 v118, 0
v_mov_b32 v119, 0
v_mov_b32 v120, 0
v_mov_b32 v121, 0
v_mov_b32 v122, 0
v_mov_b32 v123, 0
v_mov_b32 v124, 0
v_mov_b32 v125, 0
v_mov_b32 v126, 0
v_mov_b32 v127, 0
)ASM" R"ASM(v_mov_b32 v216, 0xff61b1e6
v_mov_b32 v217, 0xff61b1e6
v_mov_b32 v218, 0
v_mov_b32 v219, 0
s_mov_b32 s7, 0
.La5_LA_SLOW1_L:
s_lshl_b32 s96, s7, 13
s_add_u32 s97, s96, 4096
ld a[128:131],v255,s[92:95],s96 offen offset:0 sc1
ld a[132:135],v255,s[92:95],s96 offen offset:1024 sc1
ld a[136:139],v255,s[92:95],s96 offen offset:2048 sc1
ld a[140:143],v255,s[92:95],s96 offen offset:3072 sc1
ld a[144:147],v255,s[92:95],s97 offen offset:0 sc1
ld a[148:151],v255,s[92:95],s97 offen offset:1024 sc1
ld a[152:155],v255,s[92:95],s97 offen offset:2048 sc1
ld a[156:159],v255,s[92:95],s97 offen offset:3072 sc1
s_lshl_b32 s96, s76, 3
s_sub_u32 s96, s96, s7
s_sub_u32 s96, s96, s3
s_add_u32 s96, s96, 7
s_lshl_b32 s96, s96, 5
v_bfe_u32 v220, v255, 4, 4
v_lshrrev_b32 v221, 8, v255
v_lshlrev_b32 v221, 2, v221
v_sub_u32 v220, v220, v221
v_add_u32 v220, s96, v220
v_mov_b32 v222, 0xceabfb8f
wt vmcnt(0)
mm v[192:195],a[128:131],v[160:163],0
mm v[192:195],a[132:135],v[164:167],v[192:195]
mm v[192:195],a[136:139],v[168:171],v[192:195]
mm v[192:195],a[140:143],v[172:175],v[192:195]
mm v[196:199],a[144:147],v[160:163],0
mm v[196:199],a[148:151],v[164:167],v[196:199]
mm v[196:199],a[152:155],v[168:171],v[196:199]
mm v[196:199],a[156:159],v[172:175],v[196:199]
mm v[200:203],a[128:131],v[176:179],0
mm v[200:203],a[132:135],v[180:183],v[200:203]
mm v[200:203],a[136:139],v[184:187],v[200:203]
mm v[200:203],a[140:143],v[188:191],v[200:203]
mm v[204:207],a[144:147],v[176:179],0
mm v[204:207],a[148:151],v[180:183],v[204:207]
mm v[204:207],a[152:155],v[184:187],v[204:207]
mm v[204:207],a[156:159],v[188:191],v[204:207]
s_nop 7
s_nop 7
s_lshl_b32 s96, s7, 13
s_add_u32 s96, s96, 16777216
s_add_u32 s97, s96, 4096
ld a[128:131],v255,s[92:95],s96 offen offset:0 sc1
ld a[132:135],v255,s[92:95],s96 offen offset:1024 sc1
ld a[136:139],v255,s[92:95],s96 offen offset:2048 sc1
ld a[140:143],v255,s[92:95],s96 offen offset:3072 sc1
ld a[144:147],v255,s[92:95],s97 offen offset:0 sc1
ld a[148:151],v255,s[92:95],s97 offen offset:1024 sc1
ld a[152:155],v255,s[92:95],s97 offen offset:2048 sc1
ld a[156:159],v255,s[92:95],s97 offen offset:3072 sc1
v_cmp_gt_i32 vcc, 0, v220
s_nop 1
v_cndmask_b32 v192, v192, v222, vcc
v_cmp_gt_i32 vcc, 1, v220
s_nop 1
v_cndmask_b32 v193, v193, v222, vcc
v_cmp_gt_i32 vcc, 2, v220
s_nop 1
v_cndmask_b32 v194, v194, v222, vcc
v_cmp_gt_i32 vcc, 3, v220
s_nop 1
v_cndmask_b32 v195, v195, v222, vcc
v_cmp_gt_i32 vcc, 16, v220
s_nop 1
v_cndmask_b32 v196, v196, v222, vcc
v_cmp_gt_i32 vcc, 17, v220
s_nop 1
v_cndmask_b32 v197, v197, v222, vcc
v_cmp_gt_i32 vcc, 18, v220
s_nop 1
v_cndmask_b32 v198, v198, v222, vcc
v_cmp_gt_i32 vcc, 19, v220
s_nop 1
v_cndmask_b32 v199, v199, v222, vcc
v_cmp_gt_i32 vcc, -16, v220
s_nop 1
v_cndmask_b32 v200, v200, v222, vcc
v_cmp_gt_i32 vcc, -15, v220
s_nop 1
v_cndmask_b32 v201, v201, v222, vcc
v_cmp_gt_i32 vcc, -14, v220
s_nop 1
v_cndmask_b32 v202, v202, v222, vcc
v_cmp_gt_i32 vcc, -13, v220
s_nop 1
v_cndmask_b32 v203, v203, v222, vcc
v_cmp_gt_i32 vcc, 0, v220
s_nop 1
v_cndmask_b32 v204, v204, v222, vcc
v_cmp_gt_i32 vcc, 1, v220
s_nop 1
v_cndmask_b32 v205, v205, v222, vcc
v_cmp_gt_i32 vcc, 2, v220
)ASM" R"ASM(s_nop 1
v_cndmask_b32 v206, v206, v222, vcc
v_cmp_gt_i32 vcc, 3, v220
s_nop 1
v_cndmask_b32 v207, v207, v222, vcc
v_max3_f32 v222, v192, v193, v216
v_max3_f32 v222, v194, v195, v222
v_max3_f32 v222, v196, v197, v222
v_max3_f32 v222, v198, v199, v222
v_mov_b32 v221, v222
s_nop 1
v_permlane16_swap_b32 v221, v222
s_nop 1
v_max_f32 v222, v222, v221
v_mov_b32 v221, v222
s_nop 1
v_permlane32_swap_b32 v221, v222
s_nop 1
v_max_f32 v222, v222, v221
v_sub_f32 v223, v216, v222
v_mov_b32 v216, v222
ex v223,v223
v_sub_f32 v192, v192, v222
v_sub_f32 v193, v193, v222
v_sub_f32 v194, v194, v222
v_sub_f32 v195, v195, v222
v_sub_f32 v196, v196, v222
v_sub_f32 v197, v197, v222
v_sub_f32 v198, v198, v222
v_sub_f32 v199, v199, v222
ex v192,v192
ex v193,v193
ex v194,v194
ex v195,v195
ex v196,v196
ex v197,v197
ex v198,v198
ex v199,v199
v_mul_f32 v218, v218, v223
v_add_f32 v218, v218, v192
v_add_f32 v218, v218, v193
v_add_f32 v218, v218, v194
v_add_f32 v218, v218, v195
v_add_f32 v218, v218, v196
v_add_f32 v218, v218, v197
v_add_f32 v218, v218, v198
v_add_f32 v218, v218, v199
cv v208,v192,v193
cv v209,v194,v195
cv v210,v196,v197
cv v211,v198,v199
v_mul_f32 v64, v64, v223
v_mul_f32 v65, v65, v223
v_mul_f32 v66, v66, v223
v_mul_f32 v67, v67, v223
v_mul_f32 v68, v68, v223
v_mul_f32 v69, v69, v223
v_mul_f32 v70, v70, v223
v_mul_f32 v71, v71, v223
v_mul_f32 v72, v72, v223
v_mul_f32 v73, v73, v223
v_mul_f32 v74, v74, v223
v_mul_f32 v75, v75, v223
v_mul_f32 v76, v76, v223
v_mul_f32 v77, v77, v223
v_mul_f32 v78, v78, v223
v_mul_f32 v79, v79, v223
v_mul_f32 v80, v80, v223
v_mul_f32 v81, v81, v223
v_mul_f32 v82, v82, v223
v_mul_f32 v83, v83, v223
v_mul_f32 v84, v84, v223
v_mul_f32 v85, v85, v223
v_mul_f32 v86, v86, v223
v_mul_f32 v87, v87, v223
v_mul_f32 v88, v88, v223
v_mul_f32 v89, v89, v223
v_mul_f32 v90, v90, v223
v_mul_f32 v91, v91, v223
v_mul_f32 v92, v92, v223
v_mul_f32 v93, v93, v223
v_mul_f32 v94, v94, v223
v_mul_f32 v95, v95, v223
v_max3_f32 v222, v200, v201, v217
v_max3_f32 v222, v202, v203, v222
v_max3_f32 v222, v204, v205, v222
v_max3_f32 v222, v206, v207, v222
v_mov_b32 v221, v222
s_nop 1
v_permlane16_swap_b32 v221, v222
s_nop 1
v_max_f32 v222, v222, v221
v_mov_b32 v221, v222
s_nop 1
v_permlane32_swap_b32 v221, v222
s_nop 1
v_max_f32 v222, v222, v221
v_sub_f32 v223, v217, v222
v_mov_b32 v217, v222
ex v223,v223
)ASM" R"ASM(v_sub_f32 v200, v200, v222
v_sub_f32 v201, v201, v222
v_sub_f32 v202, v202, v222
v_sub_f32 v203, v203, v222
v_sub_f32 v204, v204, v222
v_sub_f32 v205, v205, v222
v_sub_f32 v206, v206, v222
v_sub_f32 v207, v207, v222
ex v200,v200
ex v201,v201
ex v202,v202
ex v203,v203
ex v204,v204
ex v205,v205
ex v206,v206
ex v207,v207
v_mul_f32 v219, v219, v223
v_add_f32 v219, v219, v200
v_add_f32 v219, v219, v201
v_add_f32 v219, v219, v202
v_add_f32 v219, v219, v203
v_add_f32 v219, v219, v204
v_add_f32 v219, v219, v205
v_add_f32 v219, v219, v206
v_add_f32 v219, v219, v207
cv v212,v200,v201
cv v213,v202,v203
cv v214,v204,v205
cv v215,v206,v207
v_mul_f32 v96, v96, v223
v_mul_f32 v97, v97, v223
v_mul_f32 v98, v98, v223
v_mul_f32 v99, v99, v223
v_mul_f32 v100, v100, v223
v_mul_f32 v101, v101, v223
v_mul_f32 v102, v102, v223
v_mul_f32 v103, v103, v223
v_mul_f32 v104, v104, v223
v_mul_f32 v105, v105, v223
v_mul_f32 v106, v106, v223
v_mul_f32 v107, v107, v223
v_mul_f32 v108, v108, v223
v_mul_f32 v109, v109, v223
v_mul_f32 v110, v110, v223
v_mul_f32 v111, v111, v223
v_mul_f32 v112, v112, v223
v_mul_f32 v113, v113, v223
v_mul_f32 v114, v114, v223
v_mul_f32 v115, v115, v223
v_mul_f32 v116, v116, v223
v_mul_f32 v117, v117, v223
v_mul_f32 v118, v118, v223
v_mul_f32 v119, v119, v223
v_mul_f32 v120, v120, v223
v_mul_f32 v121, v121, v223
v_mul_f32 v122, v122, v223
v_mul_f32 v123, v123, v223
v_mul_f32 v124, v124, v223
v_mul_f32 v125, v125, v223
v_mul_f32 v126, v126, v223
v_mul_f32_e64 v127, v127, v223
wt vmcnt(0)
mm v[64:67],a[128:131],v[208:211],v[64:67]
mm v[96:99],a[128:131],v[212:215],v[96:99]
mm v[68:71],a[132:135],v[208:211],v[68:71]
mm v[100:103],a[132:135],v[212:215],v[100:103]
mm v[72:75],a[136:139],v[208:211],v[72:75]
mm v[104:107],a[136:139],v[212:215],v[104:107]
mm v[76:79],a[140:143],v[208:211],v[76:79]
mm v[108:111],a[140:143],v[212:215],v[108:111]
mm v[80:83],a[144:147],v[208:211],v[80:83]
mm v[112:115],a[144:147],v[212:215],v[112:115]
mm v[84:87],a[148:151],v[208:211],v[84:87]
mm v[116:119],a[148:151],v[212:215],v[116:119]
mm v[88:91],a[152:155],v[208:211],v[88:91]
mm v[120:123],a[152:155],v[212:215],v[120:123]
mm v[92:95],a[156:159],v[208:211],v[92:95]
mm v[124:127],a[156:159],v[212:215],v[124:127]
s_nop 7
s_nop 7
s_add_u32 s7, s7, 1
s_lshr_b32 s96, s7, 3
s_cmp_le_u32 s96, s76
s_cbranch_scc1 .La5_LA_SLOW1_L
v_mov_b32 v221, v218
s_nop 1
v_permlane16_swap_b32 v221, v218
s_nop 1
v_add_f32 v218, v218, v221
v_mov_b32 v221, v218
s_nop 1
v_permlane32_swap_b32 v221, v218
s_nop 1
v_add_f32 v218, v218, v221
v_mov_b32 v221, v219
s_nop 1
v_permlane16_swap_b32 v221, v219
s_nop 1
v_add_f32 v219, v219, v221
v_mov_b32 v221, v219
)ASM" R"ASM(s_nop 1
v_permlane32_swap_b32 v221, v219
s_nop 1
v_add_f32 v219, v219, v221
v_rcp_f32 v242, v218
v_rcp_f32 v243, v219
ar v192,a160
ar v193,a161
ar v194,a162
ar v195,a163
ar v196,a164
ar v197,a165
ar v198,a166
ar v199,a167
ar v200,a168
ar v201,a169
ar v202,a170
ar v203,a171
ar v204,a172
ar v205,a173
ar v206,a174
ar v207,a175
ar v208,a176
ar v209,a177
ar v210,a178
ar v211,a179
ar v212,a180
ar v213,a181
ar v214,a182
ar v215,a183
ar v216,a184
ar v217,a185
ar v218,a186
ar v219,a187
ar v220,a188
ar v221,a189
ar v222,a190
ar v223,a191
s_mov_b32 s95, 4096
s_mov_b32 s96, 8192
s_mov_b32 s97, 12288
s_mov_b64 vcc, 0
s_nop 1
s_setpc_b64 s[24:25]
.La5_LA_SC1:
s_call_b64 s[24:25], .La5_LA_SLOW0
s_branch .La5_LA_SR1
.La5_LA_SC2:
s_call_b64 s[24:25], .La5_LA_SLOW1
s_branch .La5_LA_SR2
.La5_LA_PS0:
s_mov_b32 s84, 3
s_mov_b32 s70, 17408
wt vmcnt(2)
ld a[220:223],v255,s[68:71],s94 offen sc1
ld a[0:3],v255,s[68:71],0 offen offset:0 sc1
ld a[4:7],v255,s[68:71],0 offen offset:1024 sc1
ld a[8:11],v255,s[68:71],0 offen offset:2048 sc1
ld a[12:15],v255,s[68:71],0 offen offset:3072 sc1
ld a[16:19],v255,s[68:71],s95 offen offset:0 sc1
ld a[20:23],v255,s[68:71],s95 offen offset:1024 sc1
ld a[24:27],v255,s[68:71],s95 offen offset:2048 sc1
s_branch .La5_LA_PS1
.La5_LA_PS2:
s_mov_b32 s84, 3
s_mov_b32 s70, 17408
wt vmcnt(0)
ld a[220:223],v255,s[68:71],s94 offen sc1
ld a[0:3],v255,s[68:71],0 offen offset:0 sc1
ld a[4:7],v255,s[68:71],0 offen offset:1024 sc1
ld a[8:11],v255,s[68:71],0 offen offset:2048 sc1
ld a[12:15],v255,s[68:71],0 offen offset:3072 sc1
ld a[16:19],v255,s[68:71],s95 offen offset:0 sc1
ld a[20:23],v255,s[68:71],s95 offen offset:1024 sc1
ld a[24:27],v255,s[68:71],s95 offen offset:2048 sc1
ld a[28:31],v255,s[68:71],s95 offen offset:3072 sc1
ld a[32:35],v255,s[68:71],s96 offen offset:0 sc1
ld a[36:39],v255,s[68:71],s96 offen offset:1024 sc1
ld a[40:43],v255,s[68:71],s96 offen offset:2048 sc1
ld a[44:47],v255,s[68:71],s96 offen offset:3072 sc1
ld a[48:51],v255,s[68:71],s97 offen offset:0 sc1
ld a[52:55],v255,s[68:71],s97 offen offset:1024 sc1
ld a[56:59],v255,s[68:71],s97 offen offset:2048 sc1
ld a[60:63],v255,s[68:71],s97 offen offset:3072 sc1
s_branch .La5_LA_PS3
.La5_LA_PSPIN:
s_mov_b32 s93, 0
.La5_LA_PSPIN_L:
s_add_u32 s93, s93, 1
s_cmp_gt_u32 s93, 2097152
s_cbranch_scc1 .La5_LA_PSOK
s_sleep 8
buffer_load_dword v250, v251, s[64:67], s20 offen sc1
wt vmcnt(0)
v_cmp_eq_u32 vcc, v250, v249
s_nop 1
s_and_b32 s92, vcc_lo, 3
s_cmp_eq_u32 s92, 3
s_cbranch_scc0 .La5_LA_PSPIN_L
s_branch .La5_LA_PSOK
)ASM" R"ASM(.La5_LA_PSOK:
s_mov_b32 s70, 17408
ld a[220:223],v255,s[68:71],s94 offen sc1
ld a[0:3],v255,s[68:71],0 offen offset:0 sc1
ld a[4:7],v255,s[68:71],0 offen offset:1024 sc1
ld a[8:11],v255,s[68:71],0 offen offset:2048 sc1
ld a[12:15],v255,s[68:71],0 offen offset:3072 sc1
ld a[16:19],v255,s[68:71],s95 offen offset:0 sc1
ld a[20:23],v255,s[68:71],s95 offen offset:1024 sc1
ld a[24:27],v255,s[68:71],s95 offen offset:2048 sc1
ld a[28:31],v255,s[68:71],s95 offen offset:3072 sc1
ld a[32:35],v255,s[68:71],s96 offen offset:0 sc1
ld a[36:39],v255,s[68:71],s96 offen offset:1024 sc1
ld a[40:43],v255,s[68:71],s96 offen offset:2048 sc1
ld a[44:47],v255,s[68:71],s96 offen offset:3072 sc1
ld a[48:51],v255,s[68:71],s97 offen offset:0 sc1
ld a[52:55],v255,s[68:71],s97 offen offset:1024 sc1
ld a[56:59],v255,s[68:71],s97 offen offset:2048 sc1
ld a[60:63],v255,s[68:71],s97 offen offset:3072 sc1
s_branch .La5_LA_DC1
.La5_LO_NB1:
.La5_LW3_6:
s_cbranch_execz .La5_LA_RETURN
s_mov_b32 s93, 0
.La5_LO_NB1_L:
s_add_u32 s93, s93, 1
s_cmp_gt_u32 s93, 2097152
s_cbranch_scc1 .La5_LO_KB1
s_sleep 8
buffer_load_dwordx2 v[250:251], v248, s[64:67], s5 offen sc1
wt vmcnt(0)
v_cmp_eq_u64 vcc, v[250:251], v[252:253]
s_nop 1
s_bitcmp1_b32 vcc_lo, 1
s_cbranch_scc0 .La5_LO_NB1_L
s_branch .La5_LO_KB1
.La5_LO_NB2:
s_mov_b32 s93, 0
.La5_LO_NB2_L:
s_add_u32 s93, s93, 1
s_cmp_gt_u32 s93, 2097152
s_nop 0
s_cbranch_scc1 .La5_LO_KB2
s_sleep 8
buffer_load_dwordx2 v[250:251], v248, s[64:67], s5 offen sc1
wt vmcnt(0)
v_cmp_eq_u64 vcc, v[250:251], v[252:253]
s_nop 1
s_bitcmp1_b32 vcc_lo, 2
s_cbranch_scc0 .La5_LO_NB2_L
s_branch .La5_LO_KB2
.La5_LO_NB3:
s_mov_b32 s93, 0
.La5_LO_NB3_L:
s_add_u32 s93, s93, 1
s_cmp_gt_u32 s93, 2097152
s_nop 0
s_nop 0
s_cbranch_scc1 .La5_LO_KB3
s_sleep 8
buffer_load_dwordx2 v[250:251], v248, s[64:67], s5 offen sc1
wt vmcnt(0)
v_cmp_eq_u64 vcc, v[250:251], v[252:253]
s_nop 1
s_bitcmp1_b32 vcc_lo, 3
s_cbranch_scc0 .La5_LO_NB3_L
s_branch .La5_LO_KB3
.La5_LO_NB4:
s_mov_b32 s93, 0
.La5_LO_NB4_L:
s_add_u32 s93, s93, 1
s_cmp_gt_u32 s93, 2097152
s_nop 0
s_nop 0
s_cbranch_scc1 .La5_LO_KB4
s_sleep 8
buffer_load_dwordx2 v[250:251], v248, s[64:67], s5 offen sc1
wt vmcnt(0)
v_cmp_eq_u64 vcc, v[250:251], v[252:253]
s_nop 1
s_bitcmp1_b32 vcc_lo, 4
s_cbranch_scc0 .La5_LO_NB4_L
s_branch .La5_LO_KB4
.La5_LO_NB5:
s_mov_b32 s93, 0
.La5_LO_NB5_L:
s_add_u32 s93, s93, 1
s_cmp_gt_u32 s93, 2097152
s_nop 0
s_nop 0
s_cbranch_scc1 .La5_LO_KB5
s_sleep 8
buffer_load_dwordx2 v[250:251], v248, s[64:67], s5 offen sc1
wt vmcnt(0)
v_cmp_eq_u64 vcc, v[250:251], v[252:253]
s_nop 1
s_bitcmp1_b32 vcc_lo, 5
s_cbranch_scc0 .La5_LO_NB5_L
s_branch .La5_LO_KB5
.La5_LO_NB13:
)ASM" R"ASM(s_mov_b32 s93, 0
.La5_LO_NB13_L:
s_add_u32 s93, s93, 1
s_cmp_gt_u32 s93, 2097152
s_nop 0
s_nop 0
s_cbranch_scc1 .La5_LO_KB13
s_sleep 8
buffer_load_dwordx2 v[250:251], v248, s[64:67], s5 offen sc1
wt vmcnt(0)
v_cmp_eq_u64 vcc, v[250:251], v[252:253]
s_nop 1
s_bitcmp1_b32 vcc_lo, 13
s_cbranch_scc0 .La5_LO_NB13_L
s_branch .La5_LO_KB13
.La5_LO_NB6:
s_mov_b32 s93, 0
.La5_LO_NB6_L:
s_add_u32 s93, s93, 1
s_cmp_gt_u32 s93, 2097152
s_nop 0
s_nop 0
s_cbranch_scc1 .La5_LO_KB6
s_sleep 8
buffer_load_dwordx2 v[250:251], v248, s[64:67], s5 offen sc1
wt vmcnt(0)
v_cmp_eq_u64 vcc, v[250:251], v[252:253]
s_nop 1
s_bitcmp1_b32 vcc_lo, 6
s_cbranch_scc0 .La5_LO_NB6_L
s_branch .La5_LO_KB6
.La5_LO_NB7:
s_mov_b32 s93, 0
.La5_LO_NB7_L:
s_add_u32 s93, s93, 1
s_cmp_gt_u32 s93, 2097152
s_nop 0
s_nop 0
s_cbranch_scc1 .La5_LO_KB7
s_sleep 8
buffer_load_dwordx2 v[250:251], v248, s[64:67], s5 offen sc1
wt vmcnt(0)
v_cmp_eq_u64 vcc, v[250:251], v[252:253]
s_nop 1
s_bitcmp1_b32 vcc_lo, 7
s_cbranch_scc0 .La5_LO_NB7_L
s_branch .La5_LO_KB7
.La5_LO_NC1:
.La5_LW7_7:
s_cbranch_execz .La5_LA_RETURN
s_mov_b32 s93, 0
.La5_LO_NC1_L:
s_add_u32 s93, s93, 1
s_cmp_gt_u32 s93, 2097152
s_nop 0
s_cbranch_scc1 .La5_LO_KC1
s_sleep 8
buffer_load_dwordx2 v[250:251], v248, s[64:67], s5 offen sc1
wt vmcnt(0)
v_cmp_eq_u64 vcc, v[250:251], v[252:253]
s_nop 1
s_bitcmp1_b32 vcc_lo, 1
s_cbranch_scc0 .La5_LO_NC1_L
s_branch .La5_LO_KC1
.La5_LO_NC2:
s_mov_b32 s93, 0
.La5_LO_NC2_L:
s_add_u32 s93, s93, 1
s_cmp_gt_u32 s93, 2097152
s_nop 0
s_nop 0
s_cbranch_scc1 .La5_LO_KC2
s_sleep 8
buffer_load_dwordx2 v[250:251], v248, s[64:67], s5 offen sc1
wt vmcnt(0)
v_cmp_eq_u64 vcc, v[250:251], v[252:253]
s_nop 1
s_bitcmp1_b32 vcc_lo, 2
s_cbranch_scc0 .La5_LO_NC2_L
s_branch .La5_LO_KC2
.La5_LO_NC3:
s_mov_b32 s93, 0
.La5_LO_NC3_L:
s_add_u32 s93, s93, 1
s_cmp_gt_u32 s93, 2097152
s_nop 0
s_nop 0
s_cbranch_scc1 .La5_LO_KC3
s_sleep 8
buffer_load_dwordx2 v[250:251], v248, s[64:67], s5 offen sc1
wt vmcnt(0)
v_cmp_eq_u64 vcc, v[250:251], v[252:253]
s_nop 1
s_bitcmp1_b32 vcc_lo, 3
s_cbranch_scc0 .La5_LO_NC3_L
s_branch .La5_LO_KC3
.La5_LO_NC4:
s_mov_b32 s93, 0
.La5_LO_NC4_L:
s_add_u32 s93, s93, 1
)ASM" R"ASM(s_cmp_gt_u32 s93, 2097152
s_nop 0
s_nop 0
s_cbranch_scc1 .La5_LO_KC4
s_sleep 8
buffer_load_dwordx2 v[250:251], v248, s[64:67], s5 offen sc1
wt vmcnt(0)
v_cmp_eq_u64 vcc, v[250:251], v[252:253]
s_nop 1
s_bitcmp1_b32 vcc_lo, 4
s_cbranch_scc0 .La5_LO_NC4_L
s_branch .La5_LO_KC4
.La5_LO_NC5:
s_mov_b32 s93, 0
.La5_LO_NC5_L:
s_add_u32 s93, s93, 1
s_cmp_gt_u32 s93, 2097152
s_nop 0
s_nop 0
s_cbranch_scc1 .La5_LO_KC5
s_sleep 8
buffer_load_dwordx2 v[250:251], v248, s[64:67], s5 offen sc1
wt vmcnt(0)
v_cmp_eq_u64 vcc, v[250:251], v[252:253]
s_nop 1
s_bitcmp1_b32 vcc_lo, 5
s_cbranch_scc0 .La5_LO_NC5_L
s_branch .La5_LO_KC5
.La5_LO_NC6:
s_mov_b32 s93, 0
.La5_LO_NC6_L:
s_add_u32 s93, s93, 1
s_cmp_gt_u32 s93, 2097152
s_nop 0
s_nop 0
s_cbranch_scc1 .La5_LO_KC6
s_sleep 8
buffer_load_dwordx2 v[250:251], v248, s[64:67], s5 offen sc1
wt vmcnt(0)
v_cmp_eq_u64 vcc, v[250:251], v[252:253]
s_nop 1
s_bitcmp1_b32 vcc_lo, 6
s_cbranch_scc0 .La5_LO_NC6_L
s_branch .La5_LO_KC6
.La5_LO_NC7:
s_mov_b32 s93, 0
.La5_LO_NC7_L:
s_add_u32 s93, s93, 1
s_cmp_gt_u32 s93, 2097152
s_nop 0
s_nop 0
s_cbranch_scc1 .La5_LO_KC7
s_sleep 8
buffer_load_dwordx2 v[250:251], v248, s[64:67], s5 offen sc1
wt vmcnt(0)
v_cmp_eq_u64 vcc, v[250:251], v[252:253]
s_nop 1
s_bitcmp1_b32 vcc_lo, 7
s_cbranch_scc0 .La5_LO_NC7_L
s_branch .La5_LO_KC7
.La5_LA_SC3:
s_call_b64 s[24:25], .La5_LA_SLOW0
aw a0,0
aw a1,0
aw a2,0
aw a3,0
aw a4,0
aw a5,0
aw a6,0
aw a7,0
aw a8,0
aw a9,0
aw a10,0
aw a11,0
aw a12,0
aw a13,0
aw a14,0
aw a15,0
aw a16,0
aw a17,0
aw a18,0
aw a19,0
aw a20,0
aw a21,0
aw a22,0
aw a23,0
aw a24,0
aw a25,0
aw a26,0
aw a27,0
aw a28,0
aw a29,0
aw a30,0
aw a31,0
s_nop 1
v_mov_b32 v250, v242
v_mov_b32 v249, v243
v_xor_b32 v242, 64, v240
v_xor_b32 v243, 96, v240
ar v254,a229
)ASM" R"ASM(s_mov_b32 s93, 0
ld a[128:131],v254,s[40:43],s93 offen
s_mov_b32 s93, 7168
ld a[132:135],v254,s[40:43],s93 offen
s_mov_b32 s93, 14336
ld a[136:139],v254,s[40:43],s93 offen
s_mov_b32 s93, 21504
ld a[140:143],v254,s[40:43],s93 offen
s_mov_b32 s93, 28672
ld a[144:147],v254,s[40:43],s93 offen
s_mov_b32 s93, 35840
ld a[148:151],v254,s[40:43],s93 offen
s_mov_b32 s93, 43008
ld a[152:155],v254,s[40:43],s93 offen
s_mov_b32 s93, 50176
ld a[156:159],v254,s[40:43],s93 offen
s_mov_b32 s93, 57344
ld a[160:163],v254,s[40:43],s93 offen
s_mov_b32 s93, 64512
ld a[164:167],v254,s[40:43],s93 offen
s_mov_b32 s93, 71680
ld a[168:171],v254,s[40:43],s93 offen
s_mov_b32 s93, 78848
ld a[172:175],v254,s[40:43],s93 offen
s_mov_b32 s93, 86016
ld a[176:179],v254,s[40:43],s93 offen
s_mov_b32 s93, 93184
ld a[180:183],v254,s[40:43],s93 offen
s_mov_b32 s93, 100352
ld a[184:187],v254,s[40:43],s93 offen
s_mov_b32 s93, 107520
ld a[188:191],v254,s[40:43],s93 offen
s_branch .La5_LA_SR3
.La5_LA_SC4:
s_call_b64 s[24:25], .La5_LA_SLOW1
aw a32,0
aw a33,0
aw a34,0
aw a35,0
aw a36,0
aw a37,0
aw a38,0
aw a39,0
aw a40,0
aw a41,0
aw a42,0
aw a43,0
aw a44,0
aw a45,0
aw a46,0
aw a47,0
aw a48,0
aw a49,0
aw a50,0
aw a51,0
aw a52,0
aw a53,0
aw a54,0
aw a55,0
aw a56,0
aw a57,0
aw a58,0
aw a59,0
aw a60,0
aw a61,0
aw a62,0
aw a63,0
s_nop 1
v_mov_b32 v250, v242
v_mov_b32 v249, v243
v_xor_b32 v242, 64, v240
v_xor_b32 v243, 96, v240
ar v254,a229
s_mov_b32 s93, 0
ld a[128:131],v254,s[40:43],s93 offen
s_mov_b32 s93, 7168
ld a[132:135],v254,s[40:43],s93 offen
s_mov_b32 s93, 14336
ld a[136:139],v254,s[40:43],s93 offen
s_mov_b32 s93, 21504
ld a[140:143],v254,s[40:43],s93 offen
s_mov_b32 s93, 28672
ld a[144:147],v254,s[40:43],s93 offen
s_mov_b32 s93, 35840
ld a[148:151],v254,s[40:43],s93 offen
s_mov_b32 s93, 43008
ld a[152:155],v254,s[40:43],s93 offen
s_mov_b32 s93, 50176
ld a[156:159],v254,s[40:43],s93 offen
s_mov_b32 s93, 57344
ld a[160:163],v254,s[40:43],s93 offen
s_mov_b32 s93, 64512
ld a[164:167],v254,s[40:43],s93 offen
s_mov_b32 s93, 71680
ld a[168:171],v254,s[40:43],s93 offen
s_mov_b32 s93, 78848
ld a[172:175],v254,s[40:43],s93 offen
s_mov_b32 s93, 86016
ld a[176:179],v254,s[40:43],s93 offen
s_mov_b32 s93, 93184
)ASM" R"ASM(ld a[180:183],v254,s[40:43],s93 offen
s_mov_b32 s93, 100352
ld a[184:187],v254,s[40:43],s93 offen
s_mov_b32 s93, 107520
ld a[188:191],v254,s[40:43],s93 offen
s_branch .La5_LA_SR4
.La5_LO_VC1:
.La5_LW5_4:
s_cbranch_execz .La5_LW5_R0
.La5_LW6_8:
s_cbranch_execz .La5_LW6_R0
s_getpc_b64 s[24:25]
.La5_LW7_P0:
s_add_u32 s24, s24, .La5_LW7_R0-.La5_LW7_P0
s_addc_u32 s25, s25, 0
s_mov_b64 exec, 0
s_cmp_eq_u32 s3, 0
s_cbranch_scc1 .La5_LW7_0
s_cmp_eq_u32 s3, 1
s_cbranch_scc1 .La5_LW7_1
s_cmp_eq_u32 s3, 2
s_cbranch_scc1 .La5_LW7_2
s_branch .La5_LW7_3
.La5_LW7_R0:
s_mov_b64 exec, -1
s_branch .La5_LO_VE1
.La5_LO_W:
s_mov_b32 s93, 3670016
ld a[192:195],v228,s[40:43],s93 offen
s_mov_b32 s93, 3677184
ld a[196:199],v228,s[40:43],s93 offen
s_mov_b32 s93, 3684352
ld a[200:203],v228,s[40:43],s93 offen
s_mov_b32 s93, 3691520
ld a[204:207],v228,s[40:43],s93 offen
s_mov_b32 s93, 3698688
ld a[208:211],v228,s[40:43],s93 offen
s_mov_b32 s93, 3705856
ld a[212:215],v228,s[40:43],s93 offen
s_mov_b32 s93, 3713024
ld a[216:219],v228,s[40:43],s93 offen
s_mov_b32 s93, 3720192
ld a[220:223],v228,s[40:43],s93 offen
s_mov_b32 s93, 3727360
ld a[224:227],v228,s[40:43],s93 offen
s_mov_b32 s93, 3734528
ld a[228:231],v228,s[40:43],s93 offen
s_mov_b32 s93, 3741696
ld a[232:235],v228,s[40:43],s93 offen
s_mov_b32 s93, 3748864
ld a[236:239],v228,s[40:43],s93 offen
s_mov_b32 s93, 3756032
ld a[240:243],v228,s[40:43],s93 offen
s_mov_b32 s93, 3763200
ld a[244:247],v228,s[40:43],s93 offen
s_mov_b32 s93, 3770368
ld a[248:251],v228,s[40:43],s93 offen
s_mov_b32 s93, 3777536
ld a[252:255],v228,s[40:43],s93 offen
s_branch .La5_LO_START
.La5_LO_VC2:
s_getpc_b64 s[24:25]
.La5_LW7_P1:
s_add_u32 s24, s24, .La5_LW7_R1-.La5_LW7_P1
s_addc_u32 s25, s25, 0
s_mov_b64 exec, 0
s_cmp_eq_u32 s3, 0
s_cbranch_scc1 .La5_LW7_0
s_cmp_eq_u32 s3, 1
s_cbranch_scc1 .La5_LW7_1
s_cmp_eq_u32 s3, 2
s_cbranch_scc1 .La5_LW7_2
s_branch .La5_LW7_3
.La5_LW7_R1:
s_mov_b64 exec, -1
s_branch .La5_LO_VE2
.La5_LT_VAR:
v_mov_b32 v230, 0
v_lshlrev_b32 v232, 16, v96
v_and_b32 v233, 0xffff0000, v96
v_sub_f32 v232, v232, v244
v_sub_f32 v233, v233, v244
v_fmac_f32 v230, v232, v232
v_fmac_f32 v230, v233, v233
v_lshlrev_b32 v232, 16, v97
v_and_b32 v233, 0xffff0000, v97
v_sub_f32 v232, v232, v244
v_sub_f32 v233, v233, v244
v_fmac_f32 v230, v232, v232
v_fmac_f32 v230, v233, v233
v_lshlrev_b32 v232, 16, v98
v_and_b32 v233, 0xffff0000, v98
v_sub_f32 v232, v232, v244
v_sub_f32 v233, v233, v244
v_fmac_f32 v230, v232, v232
v_fmac_f32 v230, v233, v233
v_lshlrev_b32 v232, 16, v99
v_and_b32 v233, 0xffff0000, v99
v_sub_f32 v232, v232, v244
v_sub_f32 v233, v233, v244
)ASM" R"ASM(v_fmac_f32 v230, v232, v232
v_fmac_f32 v230, v233, v233
v_lshlrev_b32 v232, 16, v100
v_and_b32 v233, 0xffff0000, v100
v_sub_f32 v232, v232, v244
v_sub_f32 v233, v233, v244
v_fmac_f32 v230, v232, v232
v_fmac_f32 v230, v233, v233
v_lshlrev_b32 v232, 16, v101
v_and_b32 v233, 0xffff0000, v101
v_sub_f32 v232, v232, v244
v_sub_f32 v233, v233, v244
v_fmac_f32 v230, v232, v232
v_fmac_f32 v230, v233, v233
v_lshlrev_b32 v232, 16, v102
v_and_b32 v233, 0xffff0000, v102
v_sub_f32 v232, v232, v244
v_sub_f32 v233, v233, v244
v_fmac_f32 v230, v232, v232
v_fmac_f32 v230, v233, v233
v_lshlrev_b32 v232, 16, v103
v_and_b32 v233, 0xffff0000, v103
v_sub_f32 v232, v232, v244
v_sub_f32 v233, v233, v244
v_fmac_f32 v230, v232, v232
v_fmac_f32 v230, v233, v233
v_lshlrev_b32 v232, 16, v104
v_and_b32 v233, 0xffff0000, v104
v_sub_f32 v232, v232, v244
v_sub_f32 v233, v233, v244
v_fmac_f32 v230, v232, v232
v_fmac_f32 v230, v233, v233
v_lshlrev_b32 v232, 16, v105
v_and_b32 v233, 0xffff0000, v105
v_sub_f32 v232, v232, v244
v_sub_f32 v233, v233, v244
v_fmac_f32 v230, v232, v232
v_fmac_f32 v230, v233, v233
v_lshlrev_b32 v232, 16, v106
v_and_b32 v233, 0xffff0000, v106
v_sub_f32 v232, v232, v244
v_sub_f32 v233, v233, v244
v_fmac_f32 v230, v232, v232
v_fmac_f32 v230, v233, v233
v_lshlrev_b32 v232, 16, v107
v_and_b32 v233, 0xffff0000, v107
v_sub_f32 v232, v232, v244
v_sub_f32 v233, v233, v244
v_fmac_f32 v230, v232, v232
v_fmac_f32 v230, v233, v233
v_lshlrev_b32 v232, 16, v108
v_and_b32 v233, 0xffff0000, v108
v_sub_f32 v232, v232, v244
v_sub_f32 v233, v233, v244
v_fmac_f32 v230, v232, v232
v_fmac_f32 v230, v233, v233
v_lshlrev_b32 v232, 16, v109
v_and_b32 v233, 0xffff0000, v109
v_sub_f32 v232, v232, v244
v_sub_f32 v233, v233, v244
v_fmac_f32 v230, v232, v232
v_fmac_f32 v230, v233, v233
v_lshlrev_b32 v232, 16, v110
v_and_b32 v233, 0xffff0000, v110
v_sub_f32 v232, v232, v244
v_sub_f32 v233, v233, v244
v_fmac_f32 v230, v232, v232
v_fmac_f32 v230, v233, v233
v_lshlrev_b32 v232, 16, v111
v_and_b32 v233, 0xffff0000, v111
v_sub_f32 v232, v232, v244
v_sub_f32 v233, v233, v244
v_fmac_f32 v230, v232, v232
v_fmac_f32 v230, v233, v233
s_nop 1
v_add_f32_dpp v230, v230, v230 quad_perm:[1,0,3,2] row_mask:0xf bank_mask:0xf
s_nop 1
v_add_f32_dpp v230, v230, v230 quad_perm:[2,3,0,1] row_mask:0xf bank_mask:0xf
s_nop 1
v_add_f32_dpp v230, v230, v230 row_ror:8 row_mask:0xf bank_mask:0xf
v_mov_b32 v231, v230
s_nop 1
v_permlane16_swap_b32 v231, v230
s_nop 1
v_add_f32 v230, v230, v231
v_mov_b32_e64 v231, v230
s_nop 1
v_permlane32_swap_b32 v231, v230
s_nop 1
v_add_f32_e64 v230, v230, v231
v_mul_f32 v230, 0x3a800000, v230
s_branch .La5_LT_VOK
.La5_LA_KSPIN:
s_mov_b32 s93, 0
.La5_LA_KSPIN_L:
s_add_u32 s93, s93, 1
s_cmp_gt_u32 s93, 2097152
s_cbranch_scc1 .La5_LA_KOK
s_sleep 8
buffer_load_dword v250, v251, s[64:67], s20 offen sc1
)ASM" R"ASM(wt vmcnt(0)
v_cmp_eq_u32 vcc, v250, v249
s_nop 1
s_or_b32 vcc_hi, vcc_hi, s79
s_cmp_eq_u64 vcc, -1
s_cbranch_scc0 .La5_LA_KSPIN_L
s_branch .La5_LA_KOK
.La5_LA_QSPIN:
s_mov_b32 s93, 0
.La5_LA_QSPIN_L:
s_add_u32 s93, s93, 1
s_cmp_gt_u32 s93, 2097152
s_nop 0
s_cbranch_scc1 .La5_LA_QOK
s_sleep 8
buffer_load_dword v250, v251, s[64:67], s20 offen sc1
wt vmcnt(0)
v_cmp_eq_u32 vcc, v250, v249
s_nop 1
s_and_b32 s92, vcc_lo, 3
s_cmp_eq_u32 s92, 3
s_cbranch_scc0 .La5_LA_QSPIN_L
s_branch .La5_LA_QOK
.La5_LEND:
.La5_LCODEEND:
)ASM"
        :: "{s[0:1]}"(__builtin_amdgcn_kernarg_segment_ptr()), "{s2}"(blockIdx.x), "{s3}"(__builtin_amdgcn_readfirstlane(threadIdx.x >> 6)), "{s[4:5]}"(DBGP)
        : "v0","v1","v2","v3","v4","v5","v6","v7","v8","v9","v10","v11","v12","v13","v14","v15","v16","v17","v18","v19","v20","v21","v22","v23","v24","v25","v26","v27","v28","v29","v30","v31","v32","v33","v34","v35","v36","v37","v38","v39","v40","v41","v42","v43","v44","v45","v46","v47","v48","v49","v50","v51","v52","v53","v54","v55","v56","v57","v58","v59","v60","v61","v62","v63","v64","v65","v66","v67","v68","v69","v70","v71","v72","v73","v74","v75","v76","v77","v78","v79","v80","v81","v82","v83","v84","v85","v86","v87","v88","v89","v90","v91","v92","v93","v94","v95","v96","v97","v98","v99","v100","v101","v102","v103","v104","v105","v106","v107","v108","v109","v110","v111","v112","v113","v114","v115","v116","v117","v118","v119","v120","v121","v122","v123","v124","v125","v126","v127","v128","v129","v130","v131","v132","v133","v134","v135","v136","v137","v138","v139","v140","v141","v142","v143","v144","v145","v146","v147","v148","v149","v150","v151","v152","v153","v154","v155","v156","v157","v158","v159","v160","v161","v162","v163","v164","v165","v166","v167","v168","v169","v170","v171","v172","v173","v174","v175","v176","v177","v178","v179","v180","v181","v182","v183","v184","v185","v186","v187","v188","v189","v190","v191","v192","v193","v194","v195","v196","v197","v198","v199","v200","v201","v202","v203","v204","v205","v206","v207","v208","v209","v210","v211","v212","v213","v214","v215","v216","v217","v218","v219","v220","v221","v222","v223","v224","v225","v226","v227","v228","v229","v230","v231","v232","v233","v234","v235","v236","v237","v238","v239","v240","v241","v242","v243","v244","v245","v246","v247","v248","v249","v250","v251","v252","v253","v254","v255","a0","a1","a2","a3","a4","a5","a6","a7","a8","a9","a10","a11","a12","a13","a14","a15","a16","a17","a18","a19","a20","a21","a22","a23","a24","a25","a26","a27","a28","a29","a30","a31","a32","a33","a34","a35","a36","a37","a38","a39","a40","a41","a42","a43","a44","a45","a46","a47","a48","a49","a50","a51","a52","a53","a54","a55","a56","a57","a58","a59","a60","a61","a62","a63","a64","a65","a66","a67","a68","a69","a70","a71","a72","a73","a74","a75","a76","a77","a78","a79","a80","a81","a82","a83","a84","a85","a86","a87","a88","a89","a90","a91","a92","a93","a94","a95","a96","a97","a98","a99","a100","a101","a102","a103","a104","a105","a106","a107","a108","a109","a110","a111","a112","a113","a114","a115","a116","a117","a118","a119","a120","a121","a122","a123","a124","a125","a126","a127","a128","a129","a130","a131","a132","a133","a134","a135","a136","a137","a138","a139","a140","a141","a142","a143","a144","a145","a146","a147","a148","a149","a150","a151","a152","a153","a154","a155","a156","a157","a158","a159","a160","a161","a162","a163","a164","a165","a166","a167","a168","a169","a170","a171","a172","a173","a174","a175","a176","a177","a178","a179","a180","a181","a182","a183","a184","a185","a186","a187","a188","a189","a190","a191","a192","a193","a194","a195","a196","a197","a198","a199","a200","a201","a202","a203","a204","a205","a206","a207","a208","a209","a210","a211","a212","a213","a214","a215","a216","a217","a218","a219","a220","a221","a222","a223","a224","a225","a226","a227","a228","a229","a230","a231","a232","a233","a234","a235","a236","a237","a238","a239","a240","a241","a242","a243","a244","a245","a246","a247","a248","a249","a250","a251","a252","a253","a254","a255","s6","s7","s8","s9","s10","s11","s12","s13","s14","s15","s16","s17","s18","s19","s20","s21","s22","s23","s24","s25","s26","s27","s28","s29","s30","s31","s32","s33","s34","s35","s36","s37","s38","s39","s40","s41","s42","s43","s44","s45","s46","s47","s48","s49","s50","s51","s52","s53","s54","s55","s56","s57","s58","s59","s60","s61","s62","s63","s64","s65","s66","s67","s68","s69","s70","s71","s72","s73","s74","s75","s76","s77","s78","s79","s80","s81","s82","s83","s84","s85","s86","s87","s88","s89","s90","s91","s92","s93","s94","s95","s96","s97","s98","s99","s100","s101","vcc","scc","m0","memory");
}

extern "C" void kernel_launch(void* const* d_in, const int* in_sizes, int n_in, void* d_out, int out_size, void* d_ws, size_t ws_size,
                              hipStream_t stream) {
    (void)hipFuncSetAttribute((const void*)fa_kernel, hipFuncAttributeMaxDynamicSharedMemorySize, LDS_BYTES);
    fa_kernel<<<256, 256, LDS_BYTES, stream>>>((const bf16*)d_in[0], (const bf16*)d_in[1], (const bf16*)d_in[2], (const bf16*)d_in[3], (const bf16*)d_in[4], (char*)d_ws,
                                               (const bf16*)d_in[5], (const bf16*)d_in[8], (const bf16*)d_in[9], (const bf16*)d_in[10], (const bf16*)d_in[7], (bf16*)d_out);
}
```

```cpp
#include <hip/hip_runtime.h>
#include <hip/hip_bf16.h>
#include <stdint.h>
#pragma clang diagnostic ignored "-Winline-asm"
#pragma clang diagnostic ignored "-Woverlength-strings"

typedef __hip_bfloat16 bf16;
#define DBGP ((unsigned*)nullptr)
constexpr int LDS_BYTES = 163840;
extern "C" __global__ __attribute__((aligned(4096))) __launch_bounds__(256, 1) void fa_kernel(const bf16* __restrict__ px, const bf16* __restrict__ ckv, const bf16* __restrict__ wq,
                                                    const bf16* __restrict__ wuk, const bf16* __restrict__ wuv, char* pws,
                                                    const bf16* __restrict__ pwo, const bf16* __restrict__ pwg, const bf16* __restrict__ pwu,
                                                    const bf16* __restrict__ pwd, const bf16* __restrict__ pwr, bf16* __restrict__ pout) {
    asm volatile(R"ASM(
.macro mm x:vararg
v_mfma_f32_16x16x32_bf16 \x
.endm
.macro rt x:vararg
ds_read_b64_tr_b16 \x
.endm
.macro rd x:vararg
ds_read_b128 \x
.endm
.macro ld x:vararg
buffer_load_dwordx4 \x
.endm
.macro st x:vararg
buffer_store_dwordx4 \x
.endm
.macro ar x:vararg
v_accvgpr_read_b32 \x
.endm
.macro aw x:vararg
v_accvgpr_write_b32 \x
.endm
.macro cv x:vararg
v_cvt_pk_bf16_f32 \x
.endm
.macro ex x:vararg
v_exp_f32 \x
.endm
.macro wt x:vararg
s_waitcnt \x
.endm
; ---- roles: head = 4 xcd + (wg >> 6), j = (wg >> 3) & 7; odd j: block j >> 1 (then helps with block 7 - (j >> 1)), even j: block 7 - (j >> 1). First operands, addresses
.La5_LSTART:
s_load_dwordx16 s[16:31], s[0:1], 0
s_load_dwordx8 s[32:39], s[0:1], 64
.p2align 6
v_mbcnt_lo_u32_b32 v64, -1, 0
v_mbcnt_hi_u32_b32 v64, -1, v64
s_getpc_b64 s[92:93]
.La5_LPC:
s_sub_u32 s48, s92, .La5_LPC-.La5_LSTART
s_subb_u32 s49, s93, 0
s_and_b32 s49, s49, 0xffff
s_mov_b32 s50, .La5_LCODEEND-.La5_LSTART
s_mov_b32 s51, 0x20000
s_lshr_b32 s97, s2, 3
s_lshl2_add_u32 s87, s97, s3
v_lshlrev_b32 v133, 7, v64
s_lshl_b32 s87, s87, 13
buffer_load_dword v254, v133, s[48:51], s87 offen
s_and_b32 s92, s2, 7
s_lshr_b32 s93, s2, 6
s_lshl2_add_u32 s72, s92, s93
s_bfe_u32 s73, s2, 0x30003
s_lshr_b32 s74, s73, 1
s_xor_b32 s92, s74, 7
s_and_b32 s75, s73, 1
s_cselect_b32 s76, s74, s92
s_lshr_b32 s77, s3, 1
s_and_b32 s78, s3, 1
v_and_b32 v65, 15, v64
v_lshrrev_b32 v66, 4, v64
v_lshlrev_b32 v255, 4, v64
v_lshlrev_b32 v67, 1, v66
v_xor_b32 v67, v67, v65
v_lshlrev_b32 v67, 4, v67
v_lshl_add_u32 v67, v66, 13, v67
s_lshl_b32 s94, s78, 17
v_add_u32 v232, s94, v67
v_add_u32 v233, 32768, v232
v_add_u32 v234, 65536, v232
v_add_u32 v235, 98304, v232
v_and_b32 v67, 7, v64
v_xor_b32 v68, v67, v66
v_lshlrev_b32 v68, 4, v68
v_lshrrev_b32 v69, 3, v64
v_lshl_add_u32 v68, v69, 10, v68
s_lshl2_add_u32 s94, s3, s93
s_lshl_b32 s82, s94, 11
s_lshl_b32 s94, s94, 14
v_add_u32 v224, s94, v68
v_xor_b32 v225, 0x2040, v224
v_xor_b32 v226, 16384, v224
v_xor_b32 v227, 16384, v225
v_xor_b32 v228, 32768, v224
v_xor_b32 v229, 32768, v225
v_xor_b32 v230, 49152, v224
v_xor_b32 v231, 49152, v225
s_mul_i32 s94, s77, 8448
s_mul_i32 s95, s78, 4224
s_add_u32 s83, s94, s95
s_add_u32 s83, s83, 96256
v_lshlrev_b32 v135, 7, v67
v_lshl_add_u32 v135, v69, 13, v135
v_lshrrev_b32 v132, 5, v64
v_lshl_add_u32 v132, v132, 28, v135
v_lshl_add_u32 v133, v66, 28, v133
v_lshlrev_b32 v134, 10, v65
v_lshl_add_u32 v134, v66, 7, v134
v_lshrrev_b32 v68, 4, v64
v_lshl_add_u32 v134, v68, 28, v134
)ASM" R"ASM(s_lshl1_add_u32 s97, s97, s78
s_lshl_b32 s7, s97, 13
s_lshl_b32 s97, s97, 15
s_lshl_b32 s96, s93, 8
s_sub_u32 s7, s7, s96
s_sub_u32 s87, s87, s96
s_sub_u32 s97, s97, s96
s_add_u32 s86, s97, 2097152
s_lshl2_add_u32 s101, s72, s3
s_lshl_b32 s101, s101, 11
s_lshl2_add_u32 s100, s93, s3
s_lshl_b32 s100, s100, 14
v_lshlrev_b32 v118, 11, v67
s_lshl_b32 s93, s3, 9
v_lshl_add_u32 v119, v69, 4, v118
v_add_u32 v119, s93, v119
s_lshl_b32 s97, s2, 14
s_lshl_b32 s94, s72, 8
s_lshl_b32 s95, s76, 18
s_sub_u32 s46, 4194304, s94
s_mov_b32 s47, 0x20000
s_mov_b32 s42, 262144
s_mov_b32 s43, 0x20000
s_mov_b32 s62, 16384
s_mov_b32 s63, 0x20000
s_mov_b32 s79, 512
s_mov_b32 s80, 2097152
s_add_u32 s100, s100, 512
s_mov_b32 s81, 262144
s_cmp_eq_u32 s77, 0
wt lgkmcnt(0)
s_cselect_b64 s[92:93], s[22:23], s[24:25]
s_add_u32 s44, s92, s94
s_addc_u32 s45, s93, 0
s_and_b32 s45, s45, 0xffff
s_add_u32 s40, s18, s95
s_addc_u32 s41, s19, 0
s_and_b32 s41, s41, 0xffff
s_add_u32 s60, s16, s97
s_addc_u32 s61, s17, 0
s_and_b32 s61, s61, 0xffff
buffer_load_dword v254, v132, s[44:47], s86 offen
s_add_u32 s86, s86, 2097152
s_and_b32 s86, s86, 4194303
buffer_load_dword v254, v133, s[40:43], s101 offen
buffer_load_dword v254, v134, s[40:43], s100 offen
s_add_u32 m0, s83, 0
s_nop 0
ld v232,s[44:47],s80 offen lds
s_add_u32 m0, s83, 1024
s_nop 0
ld v233,s[44:47],s80 offen lds
s_add_u32 m0, s83, 2176
s_nop 0
ld v234,s[44:47],s80 offen lds
s_add_u32 m0, s83, 3200
s_nop 0
ld v235,s[44:47],s80 offen lds
s_add_u32 s80, s80, s81
s_and_b32 s80, s80, 4194303
v_lshrrev_b32 v67, 1, v65
v_xor_b32 v67, v67, v66
v_lshlrev_b32 v67, 4, v67
v_lshl_add_u32 v67, v65, 7, v67
s_lshl_b32 s92, s77, 12
v_add_u32 v236, s92, v67
v_xor_b32 v237, 64, v236
v_bfe_u32 v120, v64, 2, 2
v_add_u32 v67, 1, v66
v_lshrrev_b32 v67, 1, v67
v_lshlrev_b32 v67, 7, v67
v_lshl_add_u32 v67, v66, 11, v67
v_lshl_add_u32 v67, v120, 8, v67
v_and_b32 v68, 3, v64
v_lshl_add_u32 v67, v68, 3, v67
s_mul_i32 s92, s78, 8448
v_lshl_add_u32 v67, v120, 5, v67
v_add_u32 v67, s92, v67
v_add_u32 v240, 96256, v67
v_xor_b32 v241, 32, v240
v_xor_b32 v242, 64, v240
v_xor_b32 v243, 96, v240
s_add_u32 s64, s26, 142606336
s_addc_u32 s65, s27, 0
s_and_b32 s65, s65, 0xffff
s_mov_b32 s66, 3145728
s_mov_b32 s67, 0x20000
s_mov_b32 s88, 4096
s_lshr_b32 s92, s2, 1
s_lshl_b32 s92, s92, 15
s_and_b32 s93, s2, 1
s_lshl_b32 s93, s93, 7
s_add_u32 s92, s92, s93
s_lshl_b32 s93, s3, 13
s_add_u32 s92, s92, s93
s_add_u32 s92, s92, 88080384
s_add_u32 s56, s26, s92
s_addc_u32 s57, s27, 0
s_and_b32 s57, s57, 0xffff
s_mov_b32 s58, 8576
)ASM" R"ASM(s_mov_b32 s59, 0x20000
v_and_b32 v71, 0x380, v255
v_and_b32 v127, 0x70, v255
v_lshl_add_u32 v71, v71, 1, v127
v_xor_b32 v67, 3, v69
v_min_u32 v67, 1, v67
v_xor_b32 v67, 1, v67
s_mov_b32 s93, 0
v_mul_lo_u32 v67, v67, s93
v_add_u32 v136, v67, v255
v_and_b32 v67, 3, v69
v_xor_b32 v67, 1, v67
v_min_u32 v67, 1, v67
v_xor_b32 v67, 1, v67
v_and_b32 v67, s2, v67
v_mul_lo_u32 v67, v67, s93
v_add_u32 v71, v71, v67
s_lshl_b32 s92, s3, 3
v_lshlrev_b32 v126, 1, v127
v_or_b32 v126, 65536, v126
v_add_u32 v127, s92, v126
s_add_u32 s52, s26, 171966464
s_addc_u32 s53, s27, 0
s_and_b32 s53, s53, 0xffff
s_mov_b32 s54, 2097152
s_mov_b32 s55, 0x20000
s_lshl2_add_u32 s95, s2, s3
s_lshl_b32 s91, s95, 3
s_lshl_b32 s95, s95, 10
s_lshl_b32 s89, s76, 10
v_lshl_add_u32 v135, v69, 28, v135
s_mov_b32 s96, s87
s_mov_b32 s84, 33792
s_mov_b32 s85, 0
s_add_u32 s97, s83, 67584
s_mov_b32 s90, s83
s_lshl_b32 s92, s72, 8
s_sub_u32 s93, 8388608, s92
s_add_u32 s68, s20, s92
s_addc_u32 s69, s21, 0
s_and_b32 s69, s69, 0xffff
s_mov_b32 s70, s93
s_mov_b32 s71, 0x20000
s_lshr_b32 s92, s20, 7
s_sub_u32 s92, 3, s92
s_bfe_u32 s99, s92, 0x20001
s_lshr_b32 s93, s2, 6
s_sub_u32 s99, s99, s93
s_lshl_b32 s92, s99, 8
s_lshr_b32 s93, s2, 3
s_lshl2_add_u32 s93, s93, s3
s_lshl_b32 s98, s93, 16
s_add_u32 s98, s98, s92
v_lshlrev_b32 v138, 4, v65
v_lshl_add_u32 v137, v66, 13, v138
v_lshl_add_u32 v138, v66, 9, v138
s_lshl_b32 s93, s93, 12
v_add_u32 v138, s93, v138
s_and_b32 s93, s2, 7
s_lshl_b32 s92, s93, 19
s_add_u32 s92, s92, 138412032
s_add_u32 s4, s26, s92
s_addc_u32 s5, s27, 0
s_and_b32 s5, s5, 0xffff
s_mov_b32 s6, 0
s_mov_b32 s100, 1073741824
s_add_u32 s101, s91, 65536
v_lshrrev_b32 v139, 1, v64
v_lshlrev_b32 v139, 8, v139
v_and_or_b32 v139, v255, 16, v139
s_lshl_b32 s93, s93, 5
s_add_u32 s93, s93, 64512
v_add_u32 v139, s93, v139
s_mov_b64 exec, 0
s_cmp_eq_u32 s3, 0
s_cbranch_scc1 .La5_LW1_0
s_cmp_eq_u32 s3, 1
s_cbranch_scc1 .La5_LW1_1
s_cmp_eq_u32 s3, 2
s_cbranch_scc1 .La5_LW1_2
s_branch .La5_LW1_3
.La5_LW1_R0:
s_mov_b64 exec, -1
s_xor_b32 m0, s82, 0
s_nop 0
ld v224,s[40:43],s79 offen lds
s_xor_b32 m0, s82, 1024
s_nop 0
ld v225,s[40:43],s79 offen lds
s_xor_b32 m0, s82, 2048
s_nop 0
ld v226,s[40:43],s79 offen lds
s_xor_b32 m0, s82, 3072
s_nop 0
ld v227,s[40:43],s79 offen lds
s_xor_b32 m0, s82, 4096
s_nop 0
ld v228,s[40:43],s79 offen lds
s_xor_b32 m0, s82, 5120
s_nop 0
)ASM" R"ASM(ld v229,s[40:43],s79 offen lds
s_xor_b32 m0, s82, 6144
s_nop 0
ld v230,s[40:43],s79 offen lds
s_xor_b32 m0, s82, 7168
s_nop 0
ld v231,s[40:43],s79 offen lds
s_add_u32 s79, s79, 128
s_and_b32 s79, s79, 1023
s_add_u32 m0, s83, 16896
s_nop 0
ld v232,s[44:47],s80 offen lds
s_add_u32 m0, s83, 17920
s_nop 0
ld v233,s[44:47],s80 offen lds
s_add_u32 m0, s83, 19072
s_nop 0
ld v234,s[44:47],s80 offen lds
s_add_u32 m0, s83, 20096
s_nop 0
ld v235,s[44:47],s80 offen lds
s_add_u32 s80, s80, s81
s_and_b32 s80, s80, 4194303
buffer_load_dwordx2 v[252:253], off, s[64:67], 0 sc1
wt vmcnt(1)
s_barrier
rd v[160:163],v236 offset:24576
rd v[164:167],v237 offset:24576
rt v[192:193],v240 offset:0
rt v[194:195],v240 offset:1024
rt v[196:197],v240 offset:16896
rt v[198:199],v240 offset:17920
rt v[200:201],v241 offset:0
rt v[202:203],v241 offset:1024
rt v[204:205],v241 offset:16896
rt v[206:207],v241 offset:17920
rt v[208:209],v242 offset:0
rt v[210:211],v242 offset:1024
rt v[212:213],v242 offset:16896
rt v[214:215],v242 offset:17920
rt v[216:217],v243 offset:0
rt v[218:219],v243 offset:1024
rt v[220:221],v243 offset:16896
rt v[222:223],v243 offset:17920
rd v[168:171],v236 offset:26624
rd v[172:175],v237 offset:26624
rd v[176:179],v236 offset:16384
rd v[180:183],v237 offset:16384
rd v[184:187],v236 offset:18432
rd v[188:191],v237 offset:18432
wt lgkmcnt(0)
s_cmp_eq_u32 s78, 1
s_cbranch_scc1 .La5_LV_START
; ---- K tile (waves 0, 2): 256 keys x 128 columns, K = 512; LayerNorm of 8 rows of x in the gaps (softmax scale folded in)
.La5_LK_START:
mm a[96:99],v[192:195],v[160:163],0
s_add_u32 m0, s83, 33792
mm a[96:99],v[196:199],v[164:167],a[96:99]
ld v232,s[44:47],s80 offen lds
mm a[100:103],v[204:207],v[164:167],0
rd v[0:3],v236 offset:8192
rd v[4:7],v237 offset:8192
s_add_u32 m0, s83, 34816
mm a[100:103],v[200:203],v[160:163],a[100:103]
rd v[8:11],v236 offset:10240
rd v[12:15],v237 offset:10240
ld v233,s[44:47],s80 offen lds
mm a[104:107],v[208:211],v[160:163],0
s_add_u32 m0, s83, 35968
mm a[104:107],v[212:215],v[164:167],a[104:107]
ld v234,s[44:47],s80 offen lds
mm a[108:111],v[220:223],v[164:167],0
rd v[16:19],v236 offset:0
rd v[20:23],v237 offset:0
s_add_u32 m0, s83, 36992
mm a[108:111],v[216:219],v[160:163],a[108:111]
rd v[24:27],v236 offset:2048
rd v[28:31],v237 offset:2048
ld v235,s[44:47],s80 offen lds
mm a[140:143],v[216:219],v[168:171],0
s_mov_b32 s80, 2883584
mm a[140:143],v[220:223],v[172:175],a[140:143]
mm a[136:139],v[212:215],v[172:175],0
s_xor_b32 m0, s82, 32768
mm a[136:139],v[208:211],v[168:171],a[136:139]
rt v[32:33],v240 offset:128
rt v[34:35],v240 offset:1152
ld v224,s[40:43],s79 offen lds
mm a[124:127],v[200:203],v[168:171],0
s_xor_b32 m0, s82, 33792
mm a[124:127],v[204:207],v[172:175],a[124:127]
ld v225,s[40:43],s79 offen lds
mm a[120:123],v[196:199],v[172:175],0
s_xor_b32 m0, s82, 34816
mm a[120:123],v[192:195],v[168:171],a[120:123]
rt v[36:37],v240 offset:17024
rt v[38:39],v240 offset:18048
ld v226,s[40:43],s79 offen lds
mm a[232:235],v[192:195],v[176:179],0
s_xor_b32 m0, s82, 35840
)ASM" R"ASM(mm a[232:235],v[196:199],v[180:183],a[232:235]
ld v227,s[40:43],s79 offen lds
mm a[236:239],v[204:207],v[180:183],0
s_xor_b32 m0, s82, 36864
mm a[236:239],v[200:203],v[176:179],a[236:239]
rt v[40:41],v241 offset:128
rt v[42:43],v241 offset:1152
ld v228,s[40:43],s79 offen lds
mm a[152:155],v[208:211],v[176:179],0
s_xor_b32 m0, s82, 37888
mm a[152:155],v[212:215],v[180:183],a[152:155]
ld v229,s[40:43],s79 offen lds
mm a[156:159],v[220:223],v[180:183],0
s_xor_b32 m0, s82, 38912
mm a[156:159],v[216:219],v[176:179],a[156:159]
ld v230,s[40:43],s79 offen lds
mm a[172:175],v[216:219],v[184:187],0
s_xor_b32 m0, s82, 39936
mm a[172:175],v[220:223],v[188:191],a[172:175]
ld v231,s[40:43],s79 offen lds
mm a[168:171],v[212:215],v[188:191],0
s_mov_b32 s79, 768
mm a[168:171],v[208:211],v[184:187],a[168:171]
rt v[44:45],v241 offset:17024
rt v[46:47],v241 offset:18048
mm a[220:223],v[200:203],v[184:187],0
s_add_u32 m0, s83, 50688
mm a[220:223],v[204:207],v[188:191],a[220:223]
ld v232,s[44:47],s80 offen lds
mm a[216:219],v[196:199],v[188:191],0
wt lgkmcnt(8)
s_add_u32 m0, s83, 51712
mm a[216:219],v[192:195],v[184:187],a[216:219]
ld v233,s[44:47],s80 offen lds
mm a[200:203],v[192:195],v[0:3],0
s_add_u32 m0, s83, 52864
mm a[200:203],v[196:199],v[4:7],a[200:203]
ld v234,s[44:47],s80 offen lds
mm a[204:207],v[204:207],v[4:7],0
s_add_u32 m0, s83, 53888
mm a[204:207],v[200:203],v[0:3],a[204:207]
rt v[48:49],v242 offset:128
rt v[50:51],v242 offset:1152
ld v235,s[44:47],s80 offen lds
mm a[184:187],v[208:211],v[0:3],0
s_mov_b32 s80, 3145728
mm a[184:187],v[212:215],v[4:7],a[184:187]
mm a[188:191],v[220:223],v[4:7],0
mm a[188:191],v[216:219],v[0:3],a[188:191]
mm a[12:15],v[216:219],v[8:11],0
mm a[12:15],v[220:223],v[12:15],a[12:15]
buffer_load_dword v70, v118, s[60:63], 0 offen
mm a[8:11],v[212:215],v[12:15],0
mm a[8:11],v[208:211],v[8:11],a[8:11]
rt v[52:53],v242 offset:17024
rt v[54:55],v242 offset:18048
mm a[76:79],v[200:203],v[8:11],0
mm a[76:79],v[204:207],v[12:15],a[76:79]
ld v[72:75],v119,s[60:63],0 offen offset:0
mm a[72:75],v[196:199],v[12:15],0
mm a[72:75],v[192:195],v[8:11],a[72:75]
rt v[56:57],v243 offset:128
rt v[58:59],v243 offset:1152
mm a[88:91],v[192:195],v[16:19],0
mm a[88:91],v[196:199],v[20:23],a[88:91]
ld v[76:79],v119,s[60:63],0 offen offset:128
mm a[92:95],v[204:207],v[20:23],0
mm a[92:95],v[200:203],v[16:19],a[92:95]
rt v[60:61],v243 offset:17024
rt v[62:63],v243 offset:18048
mm a[24:27],v[208:211],v[16:19],0
mm a[24:27],v[212:215],v[20:23],a[24:27]
ld v[80:83],v119,s[60:63],0 offen offset:256
mm a[28:31],v[220:223],v[20:23],0
mm a[28:31],v[216:219],v[16:19],a[28:31]
mm a[44:47],v[216:219],v[24:27],0
mm a[44:47],v[220:223],v[28:31],a[44:47]
ld v[84:87],v119,s[60:63],0 offen offset:384
mm a[40:43],v[212:215],v[28:31],0
mm a[40:43],v[208:211],v[24:27],a[40:43]
mm a[252:255],v[200:203],v[24:27],0
mm a[252:255],v[204:207],v[28:31],a[252:255]
mm a[248:251],v[196:199],v[28:31],0
mm a[248:251],v[192:195],v[24:27],a[248:251]
s_mov_b64 exec, 0
s_cmp_eq_u32 s3, 0
s_cbranch_scc1 .La5_LW2_0
s_branch .La5_LW2_2
.La5_LW2_R0:
s_mov_b64 exec, -1
wt vmcnt(5) lgkmcnt(0)
s_barrier
mm a[112:115],v[32:35],v[160:163],0
mm a[112:115],v[36:39],v[164:167],a[112:115]
mm a[116:119],v[44:47],v[164:167],0
rt v[192:193],v240 offset:33792
rt v[194:195],v240 offset:34816
mm a[116:119],v[40:43],v[160:163],a[116:119]
rt v[196:197],v240 offset:50688
rt v[198:199],v240 offset:51712
)ASM" R"ASM(mm a[128:131],v[48:51],v[160:163],0
mm a[128:131],v[52:55],v[164:167],a[128:131]
mm a[132:135],v[60:63],v[164:167],0
mm a[132:135],v[56:59],v[160:163],a[132:135]
rd v[160:163],v236 offset:57344
rd v[164:167],v237 offset:57344
mm a[148:151],v[56:59],v[168:171],0
mm a[148:151],v[60:63],v[172:175],a[148:151]
mm a[144:147],v[52:55],v[172:175],0
mm a[144:147],v[48:51],v[168:171],a[144:147]
rt v[200:201],v241 offset:33792
rt v[202:203],v241 offset:34816
mm a[228:231],v[40:43],v[168:171],0
mm a[228:231],v[44:47],v[172:175],a[228:231]
mm a[224:227],v[36:39],v[172:175],0
mm a[224:227],v[32:35],v[168:171],a[224:227]
rd v[168:171],v236 offset:59392
rd v[172:175],v237 offset:59392
mm a[208:211],v[32:35],v[176:179],0
s_xor_b32 m0, s82, 0
mm a[208:211],v[36:39],v[180:183],a[208:211]
ld v224,s[40:43],s79 offen lds
mm a[212:215],v[44:47],v[180:183],0
mm a[212:215],v[40:43],v[176:179],a[212:215]
rt v[204:205],v241 offset:50688
rt v[206:207],v241 offset:51712
s_xor_b32 m0, s82, 1024
mm a[160:163],v[48:51],v[176:179],0
ld v225,s[40:43],s79 offen lds
mm a[160:163],v[52:55],v[180:183],a[160:163]
mm a[164:167],v[60:63],v[180:183],0
s_xor_b32 m0, s82, 2048
mm a[164:167],v[56:59],v[176:179],a[164:167]
rd v[176:179],v236 offset:49152
rd v[180:183],v237 offset:49152
ld v226,s[40:43],s79 offen lds
mm a[180:183],v[56:59],v[184:187],0
mm a[180:183],v[60:63],v[188:191],a[180:183]
s_xor_b32 m0, s82, 3072
mm a[176:179],v[52:55],v[188:191],0
ld v227,s[40:43],s79 offen lds
mm a[176:179],v[48:51],v[184:187],a[176:179]
rt v[208:209],v242 offset:33792
rt v[210:211],v242 offset:34816
mm a[196:199],v[40:43],v[184:187],0
s_xor_b32 m0, s82, 4096
mm a[196:199],v[44:47],v[188:191],a[196:199]
ld v228,s[40:43],s79 offen lds
mm a[192:195],v[36:39],v[188:191],0
mm a[192:195],v[32:35],v[184:187],a[192:195]
rd v[184:187],v236 offset:51200
rd v[188:191],v237 offset:51200
s_xor_b32 m0, s82, 5120
mm a[64:67],v[32:35],v[0:3],0
ld v229,s[40:43],s79 offen lds
mm a[64:67],v[36:39],v[4:7],a[64:67]
mm a[68:71],v[44:47],v[4:7],0
s_xor_b32 m0, s82, 6144
mm a[68:71],v[40:43],v[0:3],a[68:71]
rt v[212:213],v242 offset:50688
rt v[214:215],v242 offset:51712
ld v230,s[40:43],s79 offen lds
mm a[0:3],v[48:51],v[0:3],0
mm a[0:3],v[52:55],v[4:7],a[0:3]
s_xor_b32 m0, s82, 7168
mm a[4:7],v[60:63],v[4:7],0
ld v231,s[40:43],s79 offen lds
mm a[4:7],v[56:59],v[0:3],a[4:7]
rd v[0:3],v236 offset:40960
rd v[4:7],v237 offset:40960
s_mov_b32 s79, 896
mm a[20:23],v[56:59],v[8:11],0
mm a[20:23],v[60:63],v[12:15],a[20:23]
s_add_u32 m0, s83, 0
mm a[16:19],v[52:55],v[12:15],0
ld v232,s[44:47],s80 offen lds
mm a[16:19],v[48:51],v[8:11],a[16:19]
rt v[216:217],v243 offset:33792
rt v[218:219],v243 offset:34816
mm a[84:87],v[40:43],v[8:11],0
s_add_u32 m0, s83, 1024
mm a[84:87],v[44:47],v[12:15],a[84:87]
ld v233,s[44:47],s80 offen lds
mm a[80:83],v[36:39],v[12:15],0
wt vmcnt(10)
v_lshlrev_b32 v70, 16, v70
mm a[80:83],v[32:35],v[8:11],a[80:83]
rd v[8:11],v236 offset:43008
rd v[12:15],v237 offset:43008
s_add_u32 m0, s83, 2176
mm a[240:243],v[32:35],v[16:19],0
ld v234,s[44:47],s80 offen lds
mm a[240:243],v[36:39],v[20:23],a[240:243]
v_lshlrev_b32 v88, 16, v72
v_and_b32 v89, 0xffff0000, v72
mm a[244:247],v[44:47],v[20:23],0
s_add_u32 m0, s83, 3200
v_sub_f32 v88, v88, v70
mm a[244:247],v[40:43],v[16:19],a[244:247]
rt v[220:221],v243 offset:50688
)ASM" R"ASM(rt v[222:223],v243 offset:51712
ld v235,s[44:47],s80 offen lds
mm a[32:35],v[48:51],v[16:19],0
s_mov_b32 s80, 3407872
v_sub_f32 v89, v89, v70
mm a[32:35],v[52:55],v[20:23],a[32:35]
v_add_f32 v120, v88, v89
v_mul_f32 v122, v88, v88
mm a[36:39],v[60:63],v[20:23],0
s_add_u32 m0, s83, 16896
v_mul_f32 v123, v89, v89
mm a[36:39],v[56:59],v[16:19],a[36:39]
rd v[16:19],v236 offset:32768
rd v[20:23],v237 offset:32768
ld v232,s[44:47],s80 offen lds
mm a[60:63],v[56:59],v[24:27],0
v_lshlrev_b32 v90, 16, v73
v_and_b32 v91, 0xffff0000, v73
mm a[60:63],v[60:63],v[28:31],a[60:63]
s_add_u32 m0, s83, 17920
v_sub_f32 v90, v90, v70
mm a[56:59],v[52:55],v[28:31],0
ld v233,s[44:47],s80 offen lds
mm a[56:59],v[48:51],v[24:27],a[56:59]
v_sub_f32 v91, v91, v70
v_add_f32 v124, v90, v91
mm a[52:55],v[40:43],v[24:27],0
s_add_u32 m0, s83, 19072
v_fmac_f32 v122, v90, v90
mm a[52:55],v[44:47],v[28:31],a[52:55]
ld v234,s[44:47],s80 offen lds
mm a[48:51],v[36:39],v[28:31],0
wt lgkmcnt(2)
v_add_f32 v120, v120, v124
mm a[48:51],v[32:35],v[24:27],a[48:51]
rd v[24:27],v236 offset:34816
rd v[28:31],v237 offset:34816
s_add_u32 m0, s83, 20096
mm a[96:99],v[192:195],v[160:163],a[96:99]
ld v235,s[44:47],s80 offen lds
.La5_LW1_0:
s_cbranch_execz .La5_LW1_4
mm a[96:99],v[196:199],v[164:167],a[96:99]
s_mov_b32 s80, 3670016
v_fmac_f32 v123, v91, v91
mm a[100:103],v[204:207],v[164:167],a[100:103]
v_lshlrev_b32 v92, 16, v74
v_and_b32 v93, 0xffff0000, v74
mm a[100:103],v[200:203],v[160:163],a[100:103]
rt v[32:33],v240 offset:33920
rt v[34:35],v240 offset:34944
mm a[104:107],v[208:211],v[160:163],a[104:107]
v_sub_f32 v92, v92, v70
v_sub_f32 v93, v93, v70
mm a[104:107],v[212:215],v[164:167],a[104:107]
v_add_f32 v124, v92, v93
v_fmac_f32 v122, v92, v92
mm a[108:111],v[220:223],v[164:167],a[108:111]
wt lgkmcnt(2)
s_barrier
mm a[108:111],v[216:219],v[160:163],a[108:111]
v_add_f32 v120, v120, v124
v_fmac_f32 v123, v93, v93
mm a[140:143],v[216:219],v[168:171],a[140:143]
s_xor_b32 m0, s82, 32768
v_lshlrev_b32 v94, 16, v75
mm a[140:143],v[220:223],v[172:175],a[140:143]
ld v224,s[40:43],s79 offen lds
mm a[136:139],v[212:215],v[172:175],a[136:139]
v_and_b32 v95, 0xffff0000, v75
v_sub_f32 v94, v94, v70
mm a[136:139],v[208:211],v[168:171],a[136:139]
rt v[36:37],v240 offset:50816
rt v[38:39],v240 offset:51840
mm a[124:127],v[200:203],v[168:171],a[124:127]
s_xor_b32 m0, s82, 33792
v_sub_f32 v95, v95, v70
mm a[124:127],v[204:207],v[172:175],a[124:127]
ld v225,s[40:43],s79 offen lds
mm a[120:123],v[196:199],v[172:175],a[120:123]
v_add_f32 v124, v94, v95
v_fmac_f32 v122, v94, v94
mm a[120:123],v[192:195],v[168:171],a[120:123]
v_add_f32 v120, v120, v124
v_fmac_f32 v123, v95, v95
mm a[232:235],v[192:195],v[176:179],a[232:235]
s_xor_b32 m0, s82, 34816
v_lshlrev_b32 v96, 16, v76
mm a[232:235],v[196:199],v[180:183],a[232:235]
ld v226,s[40:43],s79 offen lds
mm a[236:239],v[204:207],v[180:183],a[236:239]
v_and_b32 v97, 0xffff0000, v76
v_sub_f32 v96, v96, v70
mm a[236:239],v[200:203],v[176:179],a[236:239]
rt v[40:41],v241 offset:33920
rt v[42:43],v241 offset:34944
mm a[152:155],v[208:211],v[176:179],a[152:155]
s_xor_b32 m0, s82, 35840
v_sub_f32 v97, v97, v70
mm a[152:155],v[212:215],v[180:183],a[152:155]
)ASM" R"ASM(ld v227,s[40:43],s79 offen lds
mm a[156:159],v[220:223],v[180:183],a[156:159]
v_add_f32 v124, v96, v97
v_fmac_f32 v122, v96, v96
mm a[156:159],v[216:219],v[176:179],a[156:159]
v_add_f32 v120, v120, v124
v_fmac_f32 v123, v97, v97
mm a[172:175],v[216:219],v[184:187],a[172:175]
s_xor_b32 m0, s82, 36864
v_lshlrev_b32 v98, 16, v77
mm a[172:175],v[220:223],v[188:191],a[172:175]
ld v228,s[40:43],s79 offen lds
mm a[168:171],v[212:215],v[188:191],a[168:171]
v_and_b32 v99, 0xffff0000, v77
v_sub_f32 v98, v98, v70
mm a[168:171],v[208:211],v[184:187],a[168:171]
rt v[44:45],v241 offset:50816
rt v[46:47],v241 offset:51840
mm a[220:223],v[200:203],v[184:187],a[220:223]
s_xor_b32 m0, s82, 37888
v_sub_f32 v99, v99, v70
mm a[220:223],v[204:207],v[188:191],a[220:223]
ld v229,s[40:43],s79 offen lds
mm a[216:219],v[196:199],v[188:191],a[216:219]
v_add_f32 v124, v98, v99
v_fmac_f32 v122, v98, v98
mm a[216:219],v[192:195],v[184:187],a[216:219]
v_add_f32 v120, v120, v124
v_fmac_f32 v123, v99, v99
mm a[200:203],v[192:195],v[0:3],a[200:203]
s_xor_b32 m0, s82, 38912
v_lshlrev_b32 v100, 16, v78
mm a[200:203],v[196:199],v[4:7],a[200:203]
ld v230,s[40:43],s79 offen lds
mm a[204:207],v[204:207],v[4:7],a[204:207]
v_and_b32 v101, 0xffff0000, v78
v_sub_f32 v100, v100, v70
mm a[204:207],v[200:203],v[0:3],a[204:207]
rt v[48:49],v242 offset:33920
rt v[50:51],v242 offset:34944
mm a[184:187],v[208:211],v[0:3],a[184:187]
s_xor_b32 m0, s82, 39936
v_sub_f32 v101, v101, v70
mm a[184:187],v[212:215],v[4:7],a[184:187]
ld v231,s[40:43],s79 offen lds
mm a[188:191],v[220:223],v[4:7],a[188:191]
s_mov_b32 s79, 0
v_add_f32 v124, v100, v101
mm a[188:191],v[216:219],v[0:3],a[188:191]
v_fmac_f32 v122, v100, v100
v_add_f32 v120, v120, v124
mm a[12:15],v[216:219],v[8:11],a[12:15]
v_fmac_f32 v123, v101, v101
v_lshlrev_b32 v102, 16, v79
mm a[12:15],v[220:223],v[12:15],a[12:15]
buffer_load_dword v254, v132, s[44:47], s86 offen
mm a[8:11],v[212:215],v[12:15],a[8:11]
s_add_u32 s86, s86, 2097152
s_and_b32 s86, s86, 4194303
mm a[8:11],v[208:211],v[8:11],a[8:11]
rt v[52:53],v242 offset:50816
rt v[54:55],v242 offset:51840
mm a[76:79],v[200:203],v[8:11],a[76:79]
v_and_b32 v103, 0xffff0000, v79
v_sub_f32 v102, v102, v70
mm a[76:79],v[204:207],v[12:15],a[76:79]
v_sub_f32 v103, v103, v70
v_add_f32 v124, v102, v103
mm a[72:75],v[196:199],v[12:15],a[72:75]
v_fmac_f32 v122, v102, v102
v_add_f32 v120, v120, v124
mm a[72:75],v[192:195],v[8:11],a[72:75]
rt v[56:57],v243 offset:33920
rt v[58:59],v243 offset:34944
mm a[88:91],v[192:195],v[16:19],a[88:91]
v_fmac_f32 v123, v103, v103
v_lshlrev_b32 v104, 16, v80
mm a[88:91],v[196:199],v[20:23],a[88:91]
v_and_b32 v105, 0xffff0000, v80
v_sub_f32 v104, v104, v70
mm a[92:95],v[204:207],v[20:23],a[92:95]
v_sub_f32 v105, v105, v70
v_add_f32 v124, v104, v105
mm a[92:95],v[200:203],v[16:19],a[92:95]
rt v[60:61],v243 offset:50816
rt v[62:63],v243 offset:51840
mm a[24:27],v[208:211],v[16:19],a[24:27]
v_fmac_f32 v122, v104, v104
v_add_f32 v120, v120, v124
mm a[24:27],v[212:215],v[20:23],a[24:27]
v_fmac_f32 v123, v105, v105
v_lshlrev_b32 v106, 16, v81
mm a[28:31],v[220:223],v[20:23],a[28:31]
v_and_b32 v107, 0xffff0000, v81
v_sub_f32 v106, v106, v70
mm a[28:31],v[216:219],v[16:19],a[28:31]
v_sub_f32 v107, v107, v70
v_add_f32 v124, v106, v107
mm a[44:47],v[216:219],v[24:27],a[44:47]
v_fmac_f32 v122, v106, v106
)ASM" R"ASM(v_add_f32 v120, v120, v124
mm a[44:47],v[220:223],v[28:31],a[44:47]
v_fmac_f32 v123, v107, v107
v_lshlrev_b32 v108, 16, v82
mm a[40:43],v[212:215],v[28:31],a[40:43]
v_and_b32 v109, 0xffff0000, v82
v_sub_f32 v108, v108, v70
mm a[40:43],v[208:211],v[24:27],a[40:43]
v_sub_f32 v109, v109, v70
v_add_f32 v124, v108, v109
mm a[252:255],v[200:203],v[24:27],a[252:255]
v_fmac_f32 v122, v108, v108
v_add_f32 v120, v120, v124
mm a[252:255],v[204:207],v[28:31],a[252:255]
v_fmac_f32 v123, v109, v109
v_lshlrev_b32 v110, 16, v83
mm a[248:251],v[196:199],v[28:31],a[248:251]
wt vmcnt(9) lgkmcnt(0)
s_barrier
mm a[248:251],v[192:195],v[24:27],a[248:251]
v_and_b32 v111, 0xffff0000, v83
v_sub_f32 v110, v110, v70
mm a[112:115],v[32:35],v[160:163],a[112:115]
v_sub_f32 v111, v111, v70
v_add_f32 v124, v110, v111
mm a[112:115],v[36:39],v[164:167],a[112:115]
v_fmac_f32 v122, v110, v110
v_add_f32 v120, v120, v124
mm a[116:119],v[44:47],v[164:167],a[116:119]
rt v[192:193],v240 offset:0
rt v[194:195],v240 offset:1024
mm a[116:119],v[40:43],v[160:163],a[116:119]
rt v[196:197],v240 offset:16896
rt v[198:199],v240 offset:17920
mm a[128:131],v[48:51],v[160:163],a[128:131]
v_fmac_f32 v123, v111, v111
v_lshlrev_b32 v112, 16, v84
mm a[128:131],v[52:55],v[164:167],a[128:131]
v_and_b32 v113, 0xffff0000, v84
v_sub_f32 v112, v112, v70
mm a[132:135],v[60:63],v[164:167],a[132:135]
v_sub_f32 v113, v113, v70
v_add_f32 v124, v112, v113
mm a[132:135],v[56:59],v[160:163],a[132:135]
rd v[160:163],v236 offset:24576
rd v[164:167],v237 offset:24576
mm a[148:151],v[56:59],v[168:171],a[148:151]
v_fmac_f32 v122, v112, v112
v_add_f32 v120, v120, v124
mm a[148:151],v[60:63],v[172:175],a[148:151]
v_fmac_f32 v123, v113, v113
v_lshlrev_b32 v114, 16, v85
mm a[144:147],v[52:55],v[172:175],a[144:147]
v_and_b32 v115, 0xffff0000, v85
v_sub_f32 v114, v114, v70
mm a[144:147],v[48:51],v[168:171],a[144:147]
rt v[200:201],v241 offset:0
rt v[202:203],v241 offset:1024
mm a[228:231],v[40:43],v[168:171],a[228:231]
v_sub_f32 v115, v115, v70
v_add_f32 v124, v114, v115
mm a[228:231],v[44:47],v[172:175],a[228:231]
v_fmac_f32 v122, v114, v114
v_add_f32 v120, v120, v124
mm a[224:227],v[36:39],v[172:175],a[224:227]
v_fmac_f32 v123, v115, v115
v_lshlrev_b32 v116, 16, v86
mm a[224:227],v[32:35],v[168:171],a[224:227]
rd v[168:171],v236 offset:26624
rd v[172:175],v237 offset:26624
mm a[208:211],v[32:35],v[176:179],a[208:211]
v_and_b32 v117, 0xffff0000, v86
v_sub_f32 v116, v116, v70
mm a[208:211],v[36:39],v[180:183],a[208:211]
v_sub_f32 v117, v117, v70
v_add_f32 v124, v116, v117
mm a[212:215],v[44:47],v[180:183],a[212:215]
v_fmac_f32 v122, v116, v116
v_add_f32 v120, v120, v124
mm a[212:215],v[40:43],v[176:179],a[212:215]
rt v[204:205],v241 offset:16896
rt v[206:207],v241 offset:17920
mm a[160:163],v[48:51],v[176:179],a[160:163]
v_fmac_f32 v123, v117, v117
v_lshlrev_b32 v118, 16, v87
mm a[160:163],v[52:55],v[180:183],a[160:163]
v_and_b32 v119, 0xffff0000, v87
v_sub_f32 v118, v118, v70
mm a[164:167],v[60:63],v[180:183],a[164:167]
v_sub_f32 v119, v119, v70
v_add_f32 v124, v118, v119
mm a[164:167],v[56:59],v[176:179],a[164:167]
rd v[176:179],v236 offset:16384
rd v[180:183],v237 offset:16384
mm a[180:183],v[56:59],v[184:187],a[180:183]
v_fmac_f32 v122, v118, v118
v_add_f32 v120, v120, v124
mm a[180:183],v[60:63],v[188:191],a[180:183]
v_fmac_f32 v123, v119, v119
v_add_f32 v121, v122, v123
)ASM" R"ASM(mm a[176:179],v[52:55],v[188:191],a[176:179]
v_add_f32_dpp v120, v120, v120 row_ror:8 row_mask:0xf bank_mask:0xf
mm a[176:179],v[48:51],v[184:187],a[176:179]
rt v[208:209],v242 offset:0
rt v[210:211],v242 offset:1024
mm a[196:199],v[40:43],v[184:187],a[196:199]
s_add_u32 m0, s83, 33792
v_add_f32_dpp v121, v121, v121 row_ror:8 row_mask:0xf bank_mask:0xf
mm a[196:199],v[44:47],v[188:191],a[196:199]
ld v232,s[44:47],s80 offen lds
mm a[192:195],v[36:39],v[188:191],a[192:195]
v_mov_b32 v124, v120
v_mov_b32 v125, v121
mm a[192:195],v[32:35],v[184:187],a[192:195]
rd v[184:187],v236 offset:18432
rd v[188:191],v237 offset:18432
mm a[64:67],v[32:35],v[0:3],a[64:67]
s_add_u32 m0, s83, 34816
v_permlane16_swap_b32 v124, v120
mm a[64:67],v[36:39],v[4:7],a[64:67]
ld v233,s[44:47],s80 offen lds
mm a[68:71],v[44:47],v[4:7],a[68:71]
v_permlane16_swap_b32 v125, v121
mm a[68:71],v[40:43],v[0:3],a[68:71]
rt v[212:213],v242 offset:16896
rt v[214:215],v242 offset:17920
mm a[0:3],v[48:51],v[0:3],a[0:3]
s_add_u32 m0, s83, 35968
v_add_f32 v120, v120, v124
mm a[0:3],v[52:55],v[4:7],a[0:3]
ld v234,s[44:47],s80 offen lds
mm a[4:7],v[60:63],v[4:7],a[4:7]
v_add_f32 v121, v121, v125
v_mov_b32 v124, v120
mm a[4:7],v[56:59],v[0:3],a[4:7]
rd v[0:3],v236 offset:8192
rd v[4:7],v237 offset:8192
mm a[20:23],v[56:59],v[8:11],a[20:23]
s_add_u32 m0, s83, 36992
v_mov_b32 v125, v121
mm a[20:23],v[60:63],v[12:15],a[20:23]
ld v235,s[44:47],s80 offen lds
mm a[16:19],v[52:55],v[12:15],a[16:19]
s_mov_b32 s80, 3932160
v_permlane32_swap_b32 v124, v120
mm a[16:19],v[48:51],v[8:11],a[16:19]
rt v[216:217],v243 offset:0
rt v[218:219],v243 offset:1024
mm a[84:87],v[40:43],v[8:11],a[84:87]
s_add_u32 m0, s83, 50688
v_permlane32_swap_b32 v125, v121
mm a[84:87],v[44:47],v[12:15],a[84:87]
ld v232,s[44:47],s80 offen lds
mm a[80:83],v[36:39],v[12:15],a[80:83]
v_add_f32 v120, v120, v124
v_add_f32 v121, v121, v125
mm a[80:83],v[32:35],v[8:11],a[80:83]
rd v[8:11],v236 offset:10240
rd v[12:15],v237 offset:10240
mm a[240:243],v[32:35],v[16:19],a[240:243]
s_add_u32 m0, s83, 51712
ds_write_b64 v127, v[120:121]
mm a[240:243],v[36:39],v[20:23],a[240:243]
ld v233,s[44:47],s80 offen lds
mm a[244:247],v[44:47],v[20:23],a[244:247]
s_nop 0
v_readfirstlane_b32 s92, v252
mm a[244:247],v[40:43],v[16:19],a[244:247]
rt v[220:221],v243 offset:16896
rt v[222:223],v243 offset:17920
mm a[32:35],v[48:51],v[16:19],a[32:35]
s_add_u32 m0, s83, 52864
v_readfirstlane_b32 s93, v253
mm a[32:35],v[52:55],v[20:23],a[32:35]
ld v234,s[44:47],s80 offen lds
mm a[36:39],v[60:63],v[20:23],a[36:39]
s_add_u32 s92, s92, 1
s_addc_u32 s93, s93, 0
mm a[36:39],v[56:59],v[16:19],a[36:39]
rd v[16:19],v236 offset:0
rd v[20:23],v237 offset:0
mm a[60:63],v[56:59],v[24:27],a[60:63]
s_add_u32 m0, s83, 53888
v_mov_b32 v252, s92
mm a[60:63],v[60:63],v[28:31],a[60:63]
ld v235,s[44:47],s80 offen lds
mm a[56:59],v[52:55],v[28:31],a[56:59]
s_mov_b32 s80, 0
v_mov_b32 v253, s93
mm a[56:59],v[48:51],v[24:27],a[56:59]
v_mov_b32 v128, 0
v_mov_b32 v129, 0
mm a[52:55],v[40:43],v[24:27],a[52:55]
v_mov_b32 v130, 0
v_mov_b32 v131, 0
mm a[52:55],v[44:47],v[28:31],a[52:55]
st v[128:131],v255,s[52:55],s95 offen sc1
mm a[48:51],v[36:39],v[28:31],a[48:51]
wt lgkmcnt(2)
s_lshl_b32 s92, s76, 5
)ASM" R"ASM(mm a[48:51],v[32:35],v[24:27],a[48:51]
rd v[24:27],v236 offset:2048
rd v[28:31],v237 offset:2048
mm a[96:99],v[192:195],v[160:163],a[96:99]
s_add_u32 s92, s92, s72
s_lshl2_add_u32 s92, s92, s3
mm a[96:99],v[196:199],v[164:167],a[96:99]
s_lshl_b32 s92, s92, 10
s_add_u32 s92, s92, 1048576
mm a[100:103],v[204:207],v[164:167],a[100:103]
v_mov_b32 v128, -1
v_mov_b32 v129, -1
mm a[100:103],v[200:203],v[160:163],a[100:103]
rt v[32:33],v240 offset:128
rt v[34:35],v240 offset:1152
mm a[104:107],v[208:211],v[160:163],a[104:107]
v_mov_b32 v130, -1
v_mov_b32 v131, -1
mm a[104:107],v[212:215],v[164:167],a[104:107]
st v[128:131],v255,s[52:55],s92 offen sc1
mm a[108:111],v[220:223],v[164:167],a[108:111]
wt lgkmcnt(2)
s_barrier
mm a[108:111],v[216:219],v[160:163],a[108:111]
rd v[72:75],v126
rd v[76:79],v126 offset:16
mm a[140:143],v[216:219],v[168:171],a[140:143]
s_xor_b32 m0, s82, 0
mm a[140:143],v[220:223],v[172:175],a[140:143]
ld v224,s[40:43],s79 offen lds
mm a[136:139],v[212:215],v[172:175],a[136:139]
mm a[136:139],v[208:211],v[168:171],a[136:139]
rt v[36:37],v240 offset:17024
rt v[38:39],v240 offset:18048
mm a[124:127],v[200:203],v[168:171],a[124:127]
s_xor_b32 m0, s82, 1024
mm a[124:127],v[204:207],v[172:175],a[124:127]
ld v225,s[40:43],s79 offen lds
mm a[120:123],v[196:199],v[172:175],a[120:123]
wt lgkmcnt(2)
v_add_f32 v72, v72, v74
mm a[120:123],v[192:195],v[168:171],a[120:123]
v_add_f32 v73, v73, v75
v_add_f32 v76, v76, v78
mm a[232:235],v[192:195],v[176:179],a[232:235]
s_xor_b32 m0, s82, 2048
v_add_f32 v77, v77, v79
mm a[232:235],v[196:199],v[180:183],a[232:235]
ld v226,s[40:43],s79 offen lds
mm a[236:239],v[204:207],v[180:183],a[236:239]
v_add_f32 v72, v72, v76
v_add_f32 v73, v73, v77
mm a[236:239],v[200:203],v[176:179],a[236:239]
rt v[40:41],v241 offset:128
rt v[42:43],v241 offset:1152
mm a[152:155],v[208:211],v[176:179],a[152:155]
s_xor_b32 m0, s82, 3072
v_mul_f32 v72, 0x3a800000, v72
mm a[152:155],v[212:215],v[180:183],a[152:155]
ld v227,s[40:43],s79 offen lds
mm a[156:159],v[220:223],v[180:183],a[156:159]
v_mul_f32 v73, 0x3a800000, v73
v_add_f32 v124, v70, v72
mm a[156:159],v[216:219],v[176:179],a[156:159]
v_fma_f32 v125, -v72, v72, v73
cv v124,v124,v124
mm a[172:175],v[216:219],v[184:187],a[172:175]
s_xor_b32 m0, s82, 4096
v_lshlrev_b32 v124, 16, v124
mm a[172:175],v[220:223],v[188:191],a[172:175]
ld v228,s[40:43],s79 offen lds
mm a[168:171],v[212:215],v[188:191],a[168:171]
v_max_f32 v125, 0, v125
cv v125,v125,v125
mm a[168:171],v[208:211],v[184:187],a[168:171]
rt v[44:45],v241 offset:17024
rt v[46:47],v241 offset:18048
mm a[220:223],v[200:203],v[184:187],a[220:223]
s_xor_b32 m0, s82, 5120
v_lshlrev_b32 v125, 16, v125
mm a[220:223],v[204:207],v[188:191],a[220:223]
ld v229,s[40:43],s79 offen lds
mm a[216:219],v[196:199],v[188:191],a[216:219]
v_add_f32 v125, 0x3727c5ac, v125
cv v125,v125,v125
mm a[216:219],v[192:195],v[184:187],a[216:219]
v_lshlrev_b32 v125, 16, v125
mm a[200:203],v[192:195],v[0:3],a[200:203]
s_xor_b32 m0, s82, 6144
mm a[200:203],v[196:199],v[4:7],a[200:203]
ld v230,s[40:43],s79 offen lds
mm a[204:207],v[204:207],v[4:7],a[204:207]
v_sqrt_f32 v125, v125
mm a[204:207],v[200:203],v[0:3],a[204:207]
rt v[48:49],v242 offset:128
rt v[50:51],v242 offset:1152
mm a[184:187],v[208:211],v[0:3],a[184:187]
s_xor_b32 m0, s82, 7168
v_sub_f32 v124, v70, v124
mm a[184:187],v[212:215],v[4:7],a[184:187]
)ASM" R"ASM(ld v231,s[40:43],s79 offen lds
mm a[188:191],v[220:223],v[4:7],a[188:191]
s_mov_b32 s79, 128
cv v125,v125,v125
mm a[188:191],v[216:219],v[0:3],a[188:191]
v_lshlrev_b32 v125, 16, v125
mm a[12:15],v[216:219],v[8:11],a[12:15]
v_rcp_f32 v125, v125
mm a[12:15],v[220:223],v[12:15],a[12:15]
v_mul_f32 v125, 0x3e0293ee, v125
v_mul_f32 v124, v124, v125
mm a[8:11],v[212:215],v[12:15],a[8:11]
v_fma_f32 v88, v88, v125, v124
v_fma_f32 v89, v89, v125, v124
mm a[8:11],v[208:211],v[8:11],a[8:11]
rt v[52:53],v242 offset:17024
rt v[54:55],v242 offset:18048
mm a[76:79],v[200:203],v[8:11],a[76:79]
cv v72,v88,v89
v_fma_f32 v90, v90, v125, v124
mm a[76:79],v[204:207],v[12:15],a[76:79]
v_fma_f32 v91, v91, v125, v124
cv v73,v90,v91
mm a[72:75],v[196:199],v[12:15],a[72:75]
v_fma_f32 v92, v92, v125, v124
v_fma_f32 v93, v93, v125, v124
mm a[72:75],v[192:195],v[8:11],a[72:75]
rt v[56:57],v243 offset:128
rt v[58:59],v243 offset:1152
mm a[88:91],v[192:195],v[16:19],a[88:91]
cv v74,v92,v93
v_fma_f32 v94, v94, v125, v124
mm a[88:91],v[196:199],v[20:23],a[88:91]
v_fma_f32 v95, v95, v125, v124
cv v75,v94,v95
mm a[92:95],v[204:207],v[20:23],a[92:95]
st v[72:75],v71,s[56:59],0 offen offset:0 sc1
mm a[92:95],v[200:203],v[16:19],a[92:95]
rt v[60:61],v243 offset:17024
rt v[62:63],v243 offset:18048
mm a[24:27],v[208:211],v[16:19],a[24:27]
v_fma_f32 v96, v96, v125, v124
v_fma_f32 v97, v97, v125, v124
mm a[24:27],v[212:215],v[20:23],a[24:27]
cv v76,v96,v97
v_fma_f32 v98, v98, v125, v124
mm a[28:31],v[220:223],v[20:23],a[28:31]
v_fma_f32 v99, v99, v125, v124
cv v77,v98,v99
mm a[28:31],v[216:219],v[16:19],a[28:31]
v_fma_f32 v100, v100, v125, v124
v_fma_f32 v101, v101, v125, v124
mm a[44:47],v[216:219],v[24:27],a[44:47]
cv v78,v100,v101
v_fma_f32 v102, v102, v125, v124
mm a[44:47],v[220:223],v[28:31],a[44:47]
v_fma_f32 v103, v103, v125, v124
cv v79,v102,v103
mm a[40:43],v[212:215],v[28:31],a[40:43]
st v[76:79],v71,s[56:59],0 offen offset:2048 sc1
mm a[40:43],v[208:211],v[24:27],a[40:43]
v_fma_f32 v104, v104, v125, v124
v_fma_f32 v105, v105, v125, v124
mm a[252:255],v[200:203],v[24:27],a[252:255]
cv v80,v104,v105
v_fma_f32 v106, v106, v125, v124
mm a[252:255],v[204:207],v[28:31],a[252:255]
v_fma_f32 v107, v107, v125, v124
cv v81,v106,v107
mm a[248:251],v[196:199],v[28:31],a[248:251]
wt vmcnt(12) lgkmcnt(0)
s_barrier
mm a[248:251],v[192:195],v[24:27],a[248:251]
v_fma_f32 v108, v108, v125, v124
v_fma_f32 v109, v109, v125, v124
mm a[112:115],v[32:35],v[160:163],a[112:115]
cv v82,v108,v109
v_fma_f32 v110, v110, v125, v124
mm a[112:115],v[36:39],v[164:167],a[112:115]
v_fma_f32 v111, v111, v125, v124
cv v83,v110,v111
mm a[116:119],v[44:47],v[164:167],a[116:119]
rt v[192:193],v240 offset:33792
rt v[194:195],v240 offset:34816
mm a[116:119],v[40:43],v[160:163],a[116:119]
rt v[196:197],v240 offset:50688
rt v[198:199],v240 offset:51712
mm a[128:131],v[48:51],v[160:163],a[128:131]
st v[80:83],v71,s[56:59],s88 offen offset:0 sc1
mm a[128:131],v[52:55],v[164:167],a[128:131]
v_fma_f32 v112, v112, v125, v124
v_fma_f32 v113, v113, v125, v124
mm a[132:135],v[60:63],v[164:167],a[132:135]
cv v84,v112,v113
v_fma_f32 v114, v114, v125, v124
mm a[132:135],v[56:59],v[160:163],a[132:135]
rd v[160:163],v236 offset:57344
rd v[164:167],v237 offset:57344
mm a[148:151],v[56:59],v[168:171],a[148:151]
v_fma_f32 v115, v115, v125, v124
)ASM" R"ASM(cv v85,v114,v115
mm a[148:151],v[60:63],v[172:175],a[148:151]
v_fma_f32 v116, v116, v125, v124
v_fma_f32 v117, v117, v125, v124
mm a[144:147],v[52:55],v[172:175],a[144:147]
cv v86,v116,v117
v_fma_f32 v118, v118, v125, v124
mm a[144:147],v[48:51],v[168:171],a[144:147]
rt v[200:201],v241 offset:33792
rt v[202:203],v241 offset:34816
mm a[228:231],v[40:43],v[168:171],a[228:231]
v_fma_f32 v119, v119, v125, v124
cv v87,v118,v119
mm a[228:231],v[44:47],v[172:175],a[228:231]
st v[84:87],v71,s[56:59],s88 offen offset:2048 sc1
mm a[224:227],v[36:39],v[172:175],a[224:227]
mm a[224:227],v[32:35],v[168:171],a[224:227]
rd v[168:171],v236 offset:59392
rd v[172:175],v237 offset:59392
mm a[208:211],v[32:35],v[176:179],a[208:211]
mm a[208:211],v[36:39],v[180:183],a[208:211]
mm a[212:215],v[44:47],v[180:183],a[212:215]
mm a[212:215],v[40:43],v[176:179],a[212:215]
rt v[204:205],v241 offset:50688
rt v[206:207],v241 offset:51712
mm a[160:163],v[48:51],v[176:179],a[160:163]
mm a[160:163],v[52:55],v[180:183],a[160:163]
mm a[164:167],v[60:63],v[180:183],a[164:167]
mm a[164:167],v[56:59],v[176:179],a[164:167]
rd v[176:179],v236 offset:49152
rd v[180:183],v237 offset:49152
mm a[180:183],v[56:59],v[184:187],a[180:183]
mm a[180:183],v[60:63],v[188:191],a[180:183]
mm a[176:179],v[52:55],v[188:191],a[176:179]
.La5_LW1_1:
s_cbranch_execz .La5_LW1_5
mm a[176:179],v[48:51],v[184:187],a[176:179]
rt v[208:209],v242 offset:33792
rt v[210:211],v242 offset:34816
mm a[196:199],v[40:43],v[184:187],a[196:199]
s_add_u32 m0, s83, 0
mm a[196:199],v[44:47],v[188:191],a[196:199]
ld v232,s[44:47],s80 offen lds
mm a[192:195],v[36:39],v[188:191],a[192:195]
mm a[192:195],v[32:35],v[184:187],a[192:195]
rd v[184:187],v236 offset:51200
rd v[188:191],v237 offset:51200
mm a[64:67],v[32:35],v[0:3],a[64:67]
s_add_u32 m0, s83, 1024
mm a[64:67],v[36:39],v[4:7],a[64:67]
ld v233,s[44:47],s80 offen lds
mm a[68:71],v[44:47],v[4:7],a[68:71]
mm a[68:71],v[40:43],v[0:3],a[68:71]
rt v[212:213],v242 offset:50688
rt v[214:215],v242 offset:51712
mm a[0:3],v[48:51],v[0:3],a[0:3]
s_add_u32 m0, s83, 2176
mm a[0:3],v[52:55],v[4:7],a[0:3]
ld v234,s[44:47],s80 offen lds
mm a[4:7],v[60:63],v[4:7],a[4:7]
mm a[4:7],v[56:59],v[0:3],a[4:7]
rd v[0:3],v236 offset:40960
rd v[4:7],v237 offset:40960
mm a[20:23],v[56:59],v[8:11],a[20:23]
s_add_u32 m0, s83, 3200
mm a[20:23],v[60:63],v[12:15],a[20:23]
ld v235,s[44:47],s80 offen lds
mm a[16:19],v[52:55],v[12:15],a[16:19]
s_mov_b32 s80, 262144
mm a[16:19],v[48:51],v[8:11],a[16:19]
rt v[216:217],v243 offset:33792
rt v[218:219],v243 offset:34816
mm a[84:87],v[40:43],v[8:11],a[84:87]
s_add_u32 m0, s83, 16896
mm a[84:87],v[44:47],v[12:15],a[84:87]
ld v232,s[44:47],s80 offen lds
mm a[80:83],v[36:39],v[12:15],a[80:83]
mm a[80:83],v[32:35],v[8:11],a[80:83]
rd v[8:11],v236 offset:43008
rd v[12:15],v237 offset:43008
mm a[240:243],v[32:35],v[16:19],a[240:243]
s_add_u32 m0, s83, 17920
mm a[240:243],v[36:39],v[20:23],a[240:243]
ld v233,s[44:47],s80 offen lds
mm a[244:247],v[44:47],v[20:23],a[244:247]
mm a[244:247],v[40:43],v[16:19],a[244:247]
rt v[220:221],v243 offset:50688
rt v[222:223],v243 offset:51712
mm a[32:35],v[48:51],v[16:19],a[32:35]
s_add_u32 m0, s83, 19072
mm a[32:35],v[52:55],v[20:23],a[32:35]
ld v234,s[44:47],s80 offen lds
mm a[36:39],v[60:63],v[20:23],a[36:39]
mm a[36:39],v[56:59],v[16:19],a[36:39]
rd v[16:19],v236 offset:32768
rd v[20:23],v237 offset:32768
mm a[60:63],v[56:59],v[24:27],a[60:63]
s_add_u32 m0, s83, 20096
mm a[60:63],v[60:63],v[28:31],a[60:63]
ld v235,s[44:47],s80 offen lds
)ASM" R"ASM(mm a[56:59],v[52:55],v[28:31],a[56:59]
s_mov_b32 s80, 524288
mm a[56:59],v[48:51],v[24:27],a[56:59]
mm a[52:55],v[40:43],v[24:27],a[52:55]
mm a[52:55],v[44:47],v[28:31],a[52:55]
mm a[48:51],v[36:39],v[28:31],a[48:51]
wt lgkmcnt(2)
mm a[48:51],v[32:35],v[24:27],a[48:51]
rd v[24:27],v236 offset:34816
rd v[28:31],v237 offset:34816
mm a[96:99],v[192:195],v[160:163],a[96:99]
mm a[96:99],v[196:199],v[164:167],a[96:99]
mm a[100:103],v[204:207],v[164:167],a[100:103]
mm a[100:103],v[200:203],v[160:163],a[100:103]
rt v[32:33],v240 offset:33920
rt v[34:35],v240 offset:34944
mm a[104:107],v[208:211],v[160:163],a[104:107]
mm a[104:107],v[212:215],v[164:167],a[104:107]
mm a[108:111],v[220:223],v[164:167],a[108:111]
wt lgkmcnt(2)
s_barrier
mm a[108:111],v[216:219],v[160:163],a[108:111]
mm a[140:143],v[216:219],v[168:171],a[140:143]
s_xor_b32 m0, s82, 32768
mm a[140:143],v[220:223],v[172:175],a[140:143]
ld v224,s[40:43],s79 offen lds
mm a[136:139],v[212:215],v[172:175],a[136:139]
mm a[136:139],v[208:211],v[168:171],a[136:139]
rt v[36:37],v240 offset:50816
rt v[38:39],v240 offset:51840
mm a[124:127],v[200:203],v[168:171],a[124:127]
s_xor_b32 m0, s82, 33792
mm a[124:127],v[204:207],v[172:175],a[124:127]
ld v225,s[40:43],s79 offen lds
mm a[120:123],v[196:199],v[172:175],a[120:123]
mm a[120:123],v[192:195],v[168:171],a[120:123]
mm a[232:235],v[192:195],v[176:179],a[232:235]
s_xor_b32 m0, s82, 34816
mm a[232:235],v[196:199],v[180:183],a[232:235]
ld v226,s[40:43],s79 offen lds
mm a[236:239],v[204:207],v[180:183],a[236:239]
mm a[236:239],v[200:203],v[176:179],a[236:239]
rt v[40:41],v241 offset:33920
rt v[42:43],v241 offset:34944
mm a[152:155],v[208:211],v[176:179],a[152:155]
s_xor_b32 m0, s82, 35840
mm a[152:155],v[212:215],v[180:183],a[152:155]
ld v227,s[40:43],s79 offen lds
mm a[156:159],v[220:223],v[180:183],a[156:159]
mm a[156:159],v[216:219],v[176:179],a[156:159]
mm a[172:175],v[216:219],v[184:187],a[172:175]
s_xor_b32 m0, s82, 36864
mm a[172:175],v[220:223],v[188:191],a[172:175]
ld v228,s[40:43],s79 offen lds
mm a[168:171],v[212:215],v[188:191],a[168:171]
mm a[168:171],v[208:211],v[184:187],a[168:171]
rt v[44:45],v241 offset:50816
rt v[46:47],v241 offset:51840
mm a[220:223],v[200:203],v[184:187],a[220:223]
s_xor_b32 m0, s82, 37888
mm a[220:223],v[204:207],v[188:191],a[220:223]
ld v229,s[40:43],s79 offen lds
mm a[216:219],v[196:199],v[188:191],a[216:219]
mm a[216:219],v[192:195],v[184:187],a[216:219]
mm a[200:203],v[192:195],v[0:3],a[200:203]
s_xor_b32 m0, s82, 38912
mm a[200:203],v[196:199],v[4:7],a[200:203]
ld v230,s[40:43],s79 offen lds
mm a[204:207],v[204:207],v[4:7],a[204:207]
mm a[204:207],v[200:203],v[0:3],a[204:207]
rt v[48:49],v242 offset:33920
rt v[50:51],v242 offset:34944
mm a[184:187],v[208:211],v[0:3],a[184:187]
s_xor_b32 m0, s82, 39936
mm a[184:187],v[212:215],v[4:7],a[184:187]
ld v231,s[40:43],s79 offen lds
mm a[188:191],v[220:223],v[4:7],a[188:191]
s_mov_b32 s79, 256
mm a[188:191],v[216:219],v[0:3],a[188:191]
mm a[12:15],v[216:219],v[8:11],a[12:15]
mm a[12:15],v[220:223],v[12:15],a[12:15]
buffer_load_dword v254, v135, s[68:71], s96 offen
mm a[8:11],v[212:215],v[12:15],a[8:11]
s_add_u32 s96, s96, 1048576
mm a[8:11],v[208:211],v[8:11],a[8:11]
rt v[52:53],v242 offset:50816
rt v[54:55],v242 offset:51840
mm a[76:79],v[200:203],v[8:11],a[76:79]
mm a[76:79],v[204:207],v[12:15],a[76:79]
buffer_load_dword v254, v135, s[68:71], s96 offen
mm a[72:75],v[196:199],v[12:15],a[72:75]
s_add_u32 s96, s96, 1048576
mm a[72:75],v[192:195],v[8:11],a[72:75]
rt v[56:57],v243 offset:33920
rt v[58:59],v243 offset:34944
mm a[88:91],v[192:195],v[16:19],a[88:91]
mm a[88:91],v[196:199],v[20:23],a[88:91]
wt vmcnt(18)
buffer_store_dwordx2 v[252:253], off, s[64:67], s91 offset:1024 sc1
mm a[92:95],v[204:207],v[20:23],a[92:95]
)ASM" R"ASM(s_mov_b32 s91, 0
s_lshl_b32 s92, s76, 19
mm a[92:95],v[200:203],v[16:19],a[92:95]
rt v[60:61],v243 offset:50816
rt v[62:63],v243 offset:51840
mm a[24:27],v[208:211],v[16:19],a[24:27]
s_add_u32 s92, s92, 88080384
mm a[24:27],v[212:215],v[20:23],a[24:27]
s_add_u32 s52, s26, s92
s_addc_u32 s53, s27, 0
mm a[28:31],v[220:223],v[20:23],a[28:31]
s_and_b32 s53, s53, 0xffff
s_mov_b32 s54, 524672
mm a[28:31],v[216:219],v[16:19],a[28:31]
s_mov_b32 s55, 0x20000
s_lshl_b32 s92, s3, 16
mm a[44:47],v[216:219],v[24:27],a[44:47]
s_sub_u32 s93, 458752, s92
v_add_u32 v244, s92, v136
mm a[44:47],v[220:223],v[28:31],a[44:47]
v_add_u32 v246, s93, v136
v_add_u32 v245, 0x8000, v244
mm a[40:43],v[212:215],v[28:31],a[40:43]
v_add_u32 v247, 0x8000, v246
s_lshl_b32 s94, s72, 19
mm a[40:43],v[208:211],v[24:27],a[40:43]
s_lshl_b32 s93, s76, 16
s_add_u32 s92, s94, s93
mm a[252:255],v[200:203],v[24:27],a[252:255]
s_lshl_b32 s93, s77, 13
s_add_u32 s92, s92, s93
mm a[252:255],v[204:207],v[28:31],a[252:255]
s_add_u32 s92, s92, 20971520
mm a[248:251],v[196:199],v[28:31],a[248:251]
wt vmcnt(11) lgkmcnt(0)
s_barrier
mm a[248:251],v[192:195],v[24:27],a[248:251]
s_add_u32 s48, s26, s92
s_addc_u32 s49, s27, 0
mm a[112:115],v[32:35],v[160:163],a[112:115]
s_and_b32 s49, s49, 0xffff
s_mov_b32 s50, 57344
mm a[112:115],v[36:39],v[164:167],a[112:115]
s_mov_b32 s51, 0x20000
s_lshl_b32 s93, s77, 24
mm a[116:119],v[44:47],v[164:167],a[116:119]
rt v[192:193],v240 offset:0
rt v[194:195],v240 offset:1024
mm a[116:119],v[40:43],v[160:163],a[116:119]
rt v[196:197],v240 offset:16896
rt v[198:199],v240 offset:17920
mm a[128:131],v[48:51],v[160:163],a[128:131]
s_add_u32 s92, s94, s93
s_add_u32 s92, s92, 20971520
mm a[128:131],v[52:55],v[164:167],a[128:131]
s_add_u32 s60, s26, s92
s_addc_u32 s61, s27, 0
mm a[132:135],v[60:63],v[164:167],a[132:135]
s_and_b32 s61, s61, 0xffff
s_mov_b32 s62, 524288
mm a[132:135],v[56:59],v[160:163],a[132:135]
rd v[160:163],v236 offset:24576
rd v[164:167],v237 offset:24576
mm a[148:151],v[56:59],v[168:171],a[148:151]
s_mov_b32 s63, 0x20000
s_lshl_b32 s92, s77, 14
mm a[148:151],v[60:63],v[172:175],a[148:151]
s_sub_u32 s95, 16384, s92
v_add_u32 v238, s95, v255
mm a[144:147],v[52:55],v[172:175],a[144:147]
s_lshr_b32 s100, s2, 6
s_lshl_b32 s100, s100, 7
mm a[144:147],v[48:51],v[168:171],a[144:147]
rt v[200:201],v241 offset:0
rt v[202:203],v241 offset:1024
mm a[228:231],v[40:43],v[168:171],a[228:231]
s_and_b32 s92, s2, 63
s_add_u32 s100, s100, s92
mm a[228:231],v[44:47],v[172:175],a[228:231]
s_lshl_b32 s100, s100, 7
s_lshl_b32 s92, s3, 28
mm a[224:227],v[36:39],v[172:175],a[224:227]
s_add_u32 s100, s100, s92
s_add_u32 s100, s100, 2621440
mm a[224:227],v[32:35],v[168:171],a[224:227]
rd v[168:171],v236 offset:26624
rd v[172:175],v237 offset:26624
mm a[208:211],v[32:35],v[176:179],a[208:211]
s_add_u32 s101, s100, 8192
s_lshl1_add_u32 s92, s3, 157440
mm a[208:211],v[36:39],v[180:183],a[208:211]
v_mov_b32 v76, s92
mm a[212:215],v[44:47],v[180:183],a[212:215]
mm a[212:215],v[40:43],v[176:179],a[212:215]
rt v[204:205],v241 offset:16896
rt v[206:207],v241 offset:17920
mm a[160:163],v[48:51],v[176:179],a[160:163]
mm a[160:163],v[52:55],v[180:183],a[160:163]
mm a[164:167],v[60:63],v[180:183],a[164:167]
mm a[164:167],v[56:59],v[176:179],a[164:167]
)ASM" R"ASM(rd v[176:179],v236 offset:16384
rd v[180:183],v237 offset:16384
mm a[180:183],v[56:59],v[184:187],a[180:183]
mm a[180:183],v[60:63],v[188:191],a[180:183]
mm a[176:179],v[52:55],v[188:191],a[176:179]
mm a[176:179],v[48:51],v[184:187],a[176:179]
rt v[208:209],v242 offset:0
rt v[210:211],v242 offset:1024
mm a[196:199],v[40:43],v[184:187],a[196:199]
s_add_u32 m0, s83, 33792
mm a[196:199],v[44:47],v[188:191],a[196:199]
ld v232,s[44:47],s80 offen lds
mm a[192:195],v[36:39],v[188:191],a[192:195]
mm a[192:195],v[32:35],v[184:187],a[192:195]
rd v[184:187],v236 offset:18432
rd v[188:191],v237 offset:18432
mm a[64:67],v[32:35],v[0:3],a[64:67]
s_add_u32 m0, s83, 34816
mm a[64:67],v[36:39],v[4:7],a[64:67]
ld v233,s[44:47],s80 offen lds
mm a[68:71],v[44:47],v[4:7],a[68:71]
mm a[68:71],v[40:43],v[0:3],a[68:71]
rt v[212:213],v242 offset:16896
rt v[214:215],v242 offset:17920
mm a[0:3],v[48:51],v[0:3],a[0:3]
s_add_u32 m0, s83, 35968
mm a[0:3],v[52:55],v[4:7],a[0:3]
ld v234,s[44:47],s80 offen lds
mm a[4:7],v[60:63],v[4:7],a[4:7]
mm a[4:7],v[56:59],v[0:3],a[4:7]
rd v[0:3],v236 offset:8192
rd v[4:7],v237 offset:8192
mm a[20:23],v[56:59],v[8:11],a[20:23]
s_add_u32 m0, s83, 36992
mm a[20:23],v[60:63],v[12:15],a[20:23]
ld v235,s[44:47],s80 offen lds
mm a[16:19],v[52:55],v[12:15],a[16:19]
s_mov_b32 s80, 786432
mm a[16:19],v[48:51],v[8:11],a[16:19]
rt v[216:217],v243 offset:0
rt v[218:219],v243 offset:1024
mm a[84:87],v[40:43],v[8:11],a[84:87]
s_add_u32 m0, s83, 50688
mm a[84:87],v[44:47],v[12:15],a[84:87]
ld v232,s[44:47],s80 offen lds
mm a[80:83],v[36:39],v[12:15],a[80:83]
mm a[80:83],v[32:35],v[8:11],a[80:83]
rd v[8:11],v236 offset:10240
rd v[12:15],v237 offset:10240
mm a[240:243],v[32:35],v[16:19],a[240:243]
s_add_u32 m0, s83, 51712
mm a[240:243],v[36:39],v[20:23],a[240:243]
ld v233,s[44:47],s80 offen lds
mm a[244:247],v[44:47],v[20:23],a[244:247]
mm a[244:247],v[40:43],v[16:19],a[244:247]
rt v[220:221],v243 offset:16896
rt v[222:223],v243 offset:17920
mm a[32:35],v[48:51],v[16:19],a[32:35]
s_add_u32 m0, s83, 52864
mm a[32:35],v[52:55],v[20:23],a[32:35]
ld v234,s[44:47],s80 offen lds
mm a[36:39],v[60:63],v[20:23],a[36:39]
mm a[36:39],v[56:59],v[16:19],a[36:39]
rd v[16:19],v236 offset:0
rd v[20:23],v237 offset:0
mm a[60:63],v[56:59],v[24:27],a[60:63]
s_add_u32 m0, s83, 53888
mm a[60:63],v[60:63],v[28:31],a[60:63]
ld v235,s[44:47],s80 offen lds
mm a[56:59],v[52:55],v[28:31],a[56:59]
s_mov_b32 s80, 1048576
mm a[56:59],v[48:51],v[24:27],a[56:59]
mm a[52:55],v[40:43],v[24:27],a[52:55]
mm a[52:55],v[44:47],v[28:31],a[52:55]
mm a[48:51],v[36:39],v[28:31],a[48:51]
wt lgkmcnt(2)
mm a[48:51],v[32:35],v[24:27],a[48:51]
rd v[24:27],v236 offset:2048
rd v[28:31],v237 offset:2048
mm a[96:99],v[192:195],v[160:163],a[96:99]
mm a[96:99],v[196:199],v[164:167],a[96:99]
mm a[100:103],v[204:207],v[164:167],a[100:103]
mm a[100:103],v[200:203],v[160:163],a[100:103]
rt v[32:33],v240 offset:128
rt v[34:35],v240 offset:1152
mm a[104:107],v[208:211],v[160:163],a[104:107]
mm a[104:107],v[212:215],v[164:167],a[104:107]
mm a[108:111],v[220:223],v[164:167],a[108:111]
wt lgkmcnt(2)
s_barrier
mm a[108:111],v[216:219],v[160:163],a[108:111]
mm a[140:143],v[216:219],v[168:171],a[140:143]
s_xor_b32 m0, s82, 0
mm a[140:143],v[220:223],v[172:175],a[140:143]
ld v224,s[40:43],s79 offen lds
mm a[136:139],v[212:215],v[172:175],a[136:139]
mm a[136:139],v[208:211],v[168:171],a[136:139]
rt v[36:37],v240 offset:17024
rt v[38:39],v240 offset:18048
mm a[124:127],v[200:203],v[168:171],a[124:127]
)ASM" R"ASM(s_xor_b32 m0, s82, 1024
mm a[124:127],v[204:207],v[172:175],a[124:127]
ld v225,s[40:43],s79 offen lds
mm a[120:123],v[196:199],v[172:175],a[120:123]
mm a[120:123],v[192:195],v[168:171],a[120:123]
mm a[232:235],v[192:195],v[176:179],a[232:235]
s_xor_b32 m0, s82, 2048
mm a[232:235],v[196:199],v[180:183],a[232:235]
ld v226,s[40:43],s79 offen lds
mm a[236:239],v[204:207],v[180:183],a[236:239]
mm a[236:239],v[200:203],v[176:179],a[236:239]
rt v[40:41],v241 offset:128
rt v[42:43],v241 offset:1152
mm a[152:155],v[208:211],v[176:179],a[152:155]
s_xor_b32 m0, s82, 3072
mm a[152:155],v[212:215],v[180:183],a[152:155]
ld v227,s[40:43],s79 offen lds
mm a[156:159],v[220:223],v[180:183],a[156:159]
mm a[156:159],v[216:219],v[176:179],a[156:159]
mm a[172:175],v[216:219],v[184:187],a[172:175]
s_xor_b32 m0, s82, 4096
mm a[172:175],v[220:223],v[188:191],a[172:175]
ld v228,s[40:43],s79 offen lds
mm a[168:171],v[212:215],v[188:191],a[168:171]
mm a[168:171],v[208:211],v[184:187],a[168:171]
rt v[44:45],v241 offset:17024
rt v[46:47],v241 offset:18048
mm a[220:223],v[200:203],v[184:187],a[220:223]
s_xor_b32 m0, s82, 5120
mm a[220:223],v[204:207],v[188:191],a[220:223]
ld v229,s[40:43],s79 offen lds
mm a[216:219],v[196:199],v[188:191],a[216:219]
mm a[216:219],v[192:195],v[184:187],a[216:219]
mm a[200:203],v[192:195],v[0:3],a[200:203]
s_xor_b32 m0, s82, 6144
mm a[200:203],v[196:199],v[4:7],a[200:203]
ld v230,s[40:43],s79 offen lds
mm a[204:207],v[204:207],v[4:7],a[204:207]
mm a[204:207],v[200:203],v[0:3],a[204:207]
rt v[48:49],v242 offset:128
rt v[50:51],v242 offset:1152
mm a[184:187],v[208:211],v[0:3],a[184:187]
s_xor_b32 m0, s82, 7168
mm a[184:187],v[212:215],v[4:7],a[184:187]
ld v231,s[40:43],s79 offen lds
mm a[188:191],v[220:223],v[4:7],a[188:191]
s_mov_b32 s79, 384
mm a[188:191],v[216:219],v[0:3],a[188:191]
mm a[12:15],v[216:219],v[8:11],a[12:15]
mm a[12:15],v[220:223],v[12:15],a[12:15]
buffer_load_dword v254, v135, s[68:71], s96 offen
mm a[8:11],v[212:215],v[12:15],a[8:11]
s_add_u32 s96, s96, 1048576
mm a[8:11],v[208:211],v[8:11],a[8:11]
rt v[52:53],v242 offset:17024
rt v[54:55],v242 offset:18048
mm a[76:79],v[200:203],v[8:11],a[76:79]
mm a[76:79],v[204:207],v[12:15],a[76:79]
buffer_load_dword v254, v135, s[68:71], s96 offen
mm a[72:75],v[196:199],v[12:15],a[72:75]
s_add_u32 s96, s96, 1048576
mm a[72:75],v[192:195],v[8:11],a[72:75]
rt v[56:57],v243 offset:128
rt v[58:59],v243 offset:1152
mm a[88:91],v[192:195],v[16:19],a[88:91]
mm a[88:91],v[196:199],v[20:23],a[88:91]
mm a[92:95],v[204:207],v[20:23],a[92:95]
mm a[92:95],v[200:203],v[16:19],a[92:95]
rt v[60:61],v243 offset:17024
rt v[62:63],v243 offset:18048
mm a[24:27],v[208:211],v[16:19],a[24:27]
mm a[24:27],v[212:215],v[20:23],a[24:27]
mm a[28:31],v[220:223],v[20:23],a[28:31]
mm a[28:31],v[216:219],v[16:19],a[28:31]
mm a[44:47],v[216:219],v[24:27],a[44:47]
mm a[44:47],v[220:223],v[28:31],a[44:47]
mm a[40:43],v[212:215],v[28:31],a[40:43]
mm a[40:43],v[208:211],v[24:27],a[40:43]
mm a[252:255],v[200:203],v[24:27],a[252:255]
mm a[252:255],v[204:207],v[28:31],a[252:255]
mm a[248:251],v[196:199],v[28:31],a[248:251]
wt vmcnt(10) lgkmcnt(0)
s_barrier
mm a[248:251],v[192:195],v[24:27],a[248:251]
mm a[112:115],v[32:35],v[160:163],a[112:115]
mm a[112:115],v[36:39],v[164:167],a[112:115]
mm a[116:119],v[44:47],v[164:167],a[116:119]
rt v[192:193],v240 offset:33792
rt v[194:195],v240 offset:34816
mm a[116:119],v[40:43],v[160:163],a[116:119]
rt v[196:197],v240 offset:50688
rt v[198:199],v240 offset:51712
mm a[128:131],v[48:51],v[160:163],a[128:131]
mm a[128:131],v[52:55],v[164:167],a[128:131]
mm a[132:135],v[60:63],v[164:167],a[132:135]
mm a[132:135],v[56:59],v[160:163],a[132:135]
rd v[160:163],v236 offset:57344
rd v[164:167],v237 offset:57344
mm a[148:151],v[56:59],v[168:171],a[148:151]
mm a[148:151],v[60:63],v[172:175],a[148:151]
)ASM" R"ASM(mm a[144:147],v[52:55],v[172:175],a[144:147]
mm a[144:147],v[48:51],v[168:171],a[144:147]
rt v[200:201],v241 offset:33792
rt v[202:203],v241 offset:34816
mm a[228:231],v[40:43],v[168:171],a[228:231]
mm a[228:231],v[44:47],v[172:175],a[228:231]
mm a[224:227],v[36:39],v[172:175],a[224:227]
mm a[224:227],v[32:35],v[168:171],a[224:227]
rd v[168:171],v236 offset:59392
rd v[172:175],v237 offset:59392
mm a[208:211],v[32:35],v[176:179],a[208:211]
mm a[208:211],v[36:39],v[180:183],a[208:211]
mm a[212:215],v[44:47],v[180:183],a[212:215]
mm a[212:215],v[40:43],v[176:179],a[212:215]
rt v[204:205],v241 offset:50688
rt v[206:207],v241 offset:51712
mm a[160:163],v[48:51],v[176:179],a[160:163]
mm a[160:163],v[52:55],v[180:183],a[160:163]
mm a[164:167],v[60:63],v[180:183],a[164:167]
mm a[164:167],v[56:59],v[176:179],a[164:167]
rd v[176:179],v236 offset:49152
rd v[180:183],v237 offset:49152
mm a[180:183],v[56:59],v[184:187],a[180:183]
mm a[180:183],v[60:63],v[188:191],a[180:183]
mm a[176:179],v[52:55],v[188:191],a[176:179]
mm a[176:179],v[48:51],v[184:187],a[176:179]
rt v[208:209],v242 offset:33792
rt v[210:211],v242 offset:34816
mm a[196:199],v[40:43],v[184:187],a[196:199]
s_add_u32 m0, s83, 0
mm a[196:199],v[44:47],v[188:191],a[196:199]
ld v232,s[44:47],s80 offen lds
mm a[192:195],v[36:39],v[188:191],a[192:195]
mm a[192:195],v[32:35],v[184:187],a[192:195]
rd v[184:187],v236 offset:51200
rd v[188:191],v237 offset:51200
mm a[64:67],v[32:35],v[0:3],a[64:67]
s_add_u32 m0, s83, 1024
mm a[64:67],v[36:39],v[4:7],a[64:67]
ld v233,s[44:47],s80 offen lds
mm a[68:71],v[44:47],v[4:7],a[68:71]
mm a[68:71],v[40:43],v[0:3],a[68:71]
rt v[212:213],v242 offset:50688
rt v[214:215],v242 offset:51712
mm a[0:3],v[48:51],v[0:3],a[0:3]
s_add_u32 m0, s83, 2176
mm a[0:3],v[52:55],v[4:7],a[0:3]
ld v234,s[44:47],s80 offen lds
mm a[4:7],v[60:63],v[4:7],a[4:7]
mm a[4:7],v[56:59],v[0:3],a[4:7]
rd v[0:3],v236 offset:40960
rd v[4:7],v237 offset:40960
mm a[20:23],v[56:59],v[8:11],a[20:23]
s_add_u32 m0, s83, 3200
mm a[20:23],v[60:63],v[12:15],a[20:23]
ld v235,s[44:47],s80 offen lds
mm a[16:19],v[52:55],v[12:15],a[16:19]
s_mov_b32 s80, 1310720
mm a[16:19],v[48:51],v[8:11],a[16:19]
rt v[216:217],v243 offset:33792
rt v[218:219],v243 offset:34816
mm a[84:87],v[40:43],v[8:11],a[84:87]
s_add_u32 m0, s83, 16896
mm a[84:87],v[44:47],v[12:15],a[84:87]
ld v232,s[44:47],s80 offen lds
mm a[80:83],v[36:39],v[12:15],a[80:83]
mm a[80:83],v[32:35],v[8:11],a[80:83]
rd v[8:11],v236 offset:43008
.La5_LW1_2:
s_cbranch_execz .La5_LW1_6
rd v[12:15],v237 offset:43008
mm a[240:243],v[32:35],v[16:19],a[240:243]
s_add_u32 m0, s83, 17920
mm a[240:243],v[36:39],v[20:23],a[240:243]
ld v233,s[44:47],s80 offen lds
mm a[244:247],v[44:47],v[20:23],a[244:247]
mm a[244:247],v[40:43],v[16:19],a[244:247]
rt v[220:221],v243 offset:50688
rt v[222:223],v243 offset:51712
mm a[32:35],v[48:51],v[16:19],a[32:35]
s_add_u32 m0, s83, 19072
mm a[32:35],v[52:55],v[20:23],a[32:35]
ld v234,s[44:47],s80 offen lds
mm a[36:39],v[60:63],v[20:23],a[36:39]
mm a[36:39],v[56:59],v[16:19],a[36:39]
rd v[16:19],v236 offset:32768
rd v[20:23],v237 offset:32768
mm a[60:63],v[56:59],v[24:27],a[60:63]
s_add_u32 m0, s83, 20096
mm a[60:63],v[60:63],v[28:31],a[60:63]
ld v235,s[44:47],s80 offen lds
mm a[56:59],v[52:55],v[28:31],a[56:59]
s_mov_b32 s80, 1572864
mm a[56:59],v[48:51],v[24:27],a[56:59]
mm a[52:55],v[40:43],v[24:27],a[52:55]
mm a[52:55],v[44:47],v[28:31],a[52:55]
mm a[48:51],v[36:39],v[28:31],a[48:51]
wt lgkmcnt(2)
mm a[48:51],v[32:35],v[24:27],a[48:51]
rd v[24:27],v236 offset:34816
)ASM" R"ASM(rd v[28:31],v237 offset:34816
mm a[96:99],v[192:195],v[160:163],a[96:99]
mm a[96:99],v[196:199],v[164:167],a[96:99]
mm a[100:103],v[204:207],v[164:167],a[100:103]
mm a[100:103],v[200:203],v[160:163],a[100:103]
rt v[32:33],v240 offset:33920
rt v[34:35],v240 offset:34944
mm a[104:107],v[208:211],v[160:163],a[104:107]
mm a[104:107],v[212:215],v[164:167],a[104:107]
mm a[108:111],v[220:223],v[164:167],a[108:111]
wt lgkmcnt(2)
s_barrier
mm a[108:111],v[216:219],v[160:163],a[108:111]
mm a[140:143],v[216:219],v[168:171],a[140:143]
s_xor_b32 m0, s82, 32768
mm a[140:143],v[220:223],v[172:175],a[140:143]
ld v224,s[40:43],s79 offen lds
mm a[136:139],v[212:215],v[172:175],a[136:139]
mm a[136:139],v[208:211],v[168:171],a[136:139]
rt v[36:37],v240 offset:50816
rt v[38:39],v240 offset:51840
mm a[124:127],v[200:203],v[168:171],a[124:127]
s_xor_b32 m0, s82, 33792
mm a[124:127],v[204:207],v[172:175],a[124:127]
ld v225,s[40:43],s79 offen lds
mm a[120:123],v[196:199],v[172:175],a[120:123]
mm a[120:123],v[192:195],v[168:171],a[120:123]
mm a[232:235],v[192:195],v[176:179],a[232:235]
s_xor_b32 m0, s82, 34816
mm a[232:235],v[196:199],v[180:183],a[232:235]
ld v226,s[40:43],s79 offen lds
mm a[236:239],v[204:207],v[180:183],a[236:239]
mm a[236:239],v[200:203],v[176:179],a[236:239]
rt v[40:41],v241 offset:33920
rt v[42:43],v241 offset:34944
mm a[152:155],v[208:211],v[176:179],a[152:155]
s_xor_b32 m0, s82, 35840
mm a[152:155],v[212:215],v[180:183],a[152:155]
ld v227,s[40:43],s79 offen lds
mm a[156:159],v[220:223],v[180:183],a[156:159]
mm a[156:159],v[216:219],v[176:179],a[156:159]
mm a[172:175],v[216:219],v[184:187],a[172:175]
s_xor_b32 m0, s82, 36864
mm a[172:175],v[220:223],v[188:191],a[172:175]
ld v228,s[40:43],s79 offen lds
mm a[168:171],v[212:215],v[188:191],a[168:171]
mm a[168:171],v[208:211],v[184:187],a[168:171]
rt v[44:45],v241 offset:50816
rt v[46:47],v241 offset:51840
mm a[220:223],v[200:203],v[184:187],a[220:223]
s_xor_b32 m0, s82, 37888
mm a[220:223],v[204:207],v[188:191],a[220:223]
ld v229,s[40:43],s79 offen lds
mm a[216:219],v[196:199],v[188:191],a[216:219]
mm a[216:219],v[192:195],v[184:187],a[216:219]
mm a[200:203],v[192:195],v[0:3],a[200:203]
s_xor_b32 m0, s82, 38912
mm a[200:203],v[196:199],v[4:7],a[200:203]
ld v230,s[40:43],s79 offen lds
mm a[204:207],v[204:207],v[4:7],a[204:207]
mm a[204:207],v[200:203],v[0:3],a[204:207]
rt v[48:49],v242 offset:33920
rt v[50:51],v242 offset:34944
mm a[184:187],v[208:211],v[0:3],a[184:187]
s_xor_b32 m0, s82, 39936
mm a[184:187],v[212:215],v[4:7],a[184:187]
ld v231,s[40:43],s79 offen lds
mm a[188:191],v[220:223],v[4:7],a[188:191]
s_mov_b32 s79, 512
mm a[188:191],v[216:219],v[0:3],a[188:191]
mm a[12:15],v[216:219],v[8:11],a[12:15]
mm a[12:15],v[220:223],v[12:15],a[12:15]
buffer_load_dword v254, v135, s[68:71], s96 offen
mm a[8:11],v[212:215],v[12:15],a[8:11]
s_add_u32 s96, s96, 1048576
mm a[8:11],v[208:211],v[8:11],a[8:11]
rt v[52:53],v242 offset:50816
rt v[54:55],v242 offset:51840
mm a[76:79],v[200:203],v[8:11],a[76:79]
mm a[76:79],v[204:207],v[12:15],a[76:79]
buffer_load_dword v254, v135, s[68:71], s96 offen
mm a[72:75],v[196:199],v[12:15],a[72:75]
s_add_u32 s96, s96, 1048576
mm a[72:75],v[192:195],v[8:11],a[72:75]
rt v[56:57],v243 offset:33920
rt v[58:59],v243 offset:34944
mm a[88:91],v[192:195],v[16:19],a[88:91]
mm a[88:91],v[196:199],v[20:23],a[88:91]
mm a[92:95],v[204:207],v[20:23],a[92:95]
mm a[92:95],v[200:203],v[16:19],a[92:95]
rt v[60:61],v243 offset:50816
rt v[62:63],v243 offset:51840
mm a[24:27],v[208:211],v[16:19],a[24:27]
mm a[24:27],v[212:215],v[20:23],a[24:27]
mm a[28:31],v[220:223],v[20:23],a[28:31]
mm a[28:31],v[216:219],v[16:19],a[28:31]
mm a[44:47],v[216:219],v[24:27],a[44:47]
mm a[44:47],v[220:223],v[28:31],a[44:47]
mm a[40:43],v[212:215],v[28:31],a[40:43]
mm a[40:43],v[208:211],v[24:27],a[40:43]
)ASM" R"ASM(mm a[252:255],v[200:203],v[24:27],a[252:255]
mm a[252:255],v[204:207],v[28:31],a[252:255]
mm a[248:251],v[196:199],v[28:31],a[248:251]
wt vmcnt(10) lgkmcnt(0)
s_barrier
mm a[248:251],v[192:195],v[24:27],a[248:251]
mm a[112:115],v[32:35],v[160:163],a[112:115]
mm a[112:115],v[36:39],v[164:167],a[112:115]
mm a[116:119],v[44:47],v[164:167],a[116:119]
rt v[192:193],v240 offset:0
rt v[194:195],v240 offset:1024
mm a[116:119],v[40:43],v[160:163],a[116:119]
rt v[196:197],v240 offset:16896
rt v[198:199],v240 offset:17920
mm a[128:131],v[48:51],v[160:163],a[128:131]
mm a[128:131],v[52:55],v[164:167],a[128:131]
mm a[132:135],v[60:63],v[164:167],a[132:135]
mm a[132:135],v[56:59],v[160:163],a[132:135]
rd v[160:163],v236 offset:24576
rd v[164:167],v237 offset:24576
mm a[148:151],v[56:59],v[168:171],a[148:151]
mm a[148:151],v[60:63],v[172:175],a[148:151]
mm a[144:147],v[52:55],v[172:175],a[144:147]
mm a[144:147],v[48:51],v[168:171],a[144:147]
rt v[200:201],v241 offset:0
rt v[202:203],v241 offset:1024
mm a[228:231],v[40:43],v[168:171],a[228:231]
mm a[228:231],v[44:47],v[172:175],a[228:231]
mm a[224:227],v[36:39],v[172:175],a[224:227]
mm a[224:227],v[32:35],v[168:171],a[224:227]
rd v[168:171],v236 offset:26624
rd v[172:175],v237 offset:26624
mm a[208:211],v[32:35],v[176:179],a[208:211]
mm a[208:211],v[36:39],v[180:183],a[208:211]
mm a[212:215],v[44:47],v[180:183],a[212:215]
mm a[212:215],v[40:43],v[176:179],a[212:215]
rt v[204:205],v241 offset:16896
rt v[206:207],v241 offset:17920
mm a[160:163],v[48:51],v[176:179],a[160:163]
mm a[160:163],v[52:55],v[180:183],a[160:163]
mm a[164:167],v[60:63],v[180:183],a[164:167]
mm a[164:167],v[56:59],v[176:179],a[164:167]
rd v[176:179],v236 offset:16384
rd v[180:183],v237 offset:16384
mm a[180:183],v[56:59],v[184:187],a[180:183]
mm a[180:183],v[60:63],v[188:191],a[180:183]
mm a[176:179],v[52:55],v[188:191],a[176:179]
mm a[176:179],v[48:51],v[184:187],a[176:179]
rt v[208:209],v242 offset:0
rt v[210:211],v242 offset:1024
mm a[196:199],v[40:43],v[184:187],a[196:199]
s_add_u32 m0, s83, 33792
mm a[196:199],v[44:47],v[188:191],a[196:199]
ld v232,s[44:47],s80 offen lds
mm a[192:195],v[36:39],v[188:191],a[192:195]
mm a[192:195],v[32:35],v[184:187],a[192:195]
rd v[184:187],v236 offset:18432
rd v[188:191],v237 offset:18432
mm a[64:67],v[32:35],v[0:3],a[64:67]
s_add_u32 m0, s83, 34816
mm a[64:67],v[36:39],v[4:7],a[64:67]
ld v233,s[44:47],s80 offen lds
mm a[68:71],v[44:47],v[4:7],a[68:71]
mm a[68:71],v[40:43],v[0:3],a[68:71]
rt v[212:213],v242 offset:16896
rt v[214:215],v242 offset:17920
mm a[0:3],v[48:51],v[0:3],a[0:3]
s_add_u32 m0, s83, 35968
mm a[0:3],v[52:55],v[4:7],a[0:3]
ld v234,s[44:47],s80 offen lds
mm a[4:7],v[60:63],v[4:7],a[4:7]
mm a[4:7],v[56:59],v[0:3],a[4:7]
rd v[0:3],v236 offset:8192
rd v[4:7],v237 offset:8192
mm a[20:23],v[56:59],v[8:11],a[20:23]
s_add_u32 m0, s83, 36992
mm a[20:23],v[60:63],v[12:15],a[20:23]
ld v235,s[44:47],s80 offen lds
mm a[16:19],v[52:55],v[12:15],a[16:19]
s_mov_b32 s80, 1835008
mm a[16:19],v[48:51],v[8:11],a[16:19]
rt v[216:217],v243 offset:0
rt v[218:219],v243 offset:1024
mm a[84:87],v[40:43],v[8:11],a[84:87]
s_add_u32 m0, s83, 50688
mm a[84:87],v[44:47],v[12:15],a[84:87]
ld v232,s[44:47],s80 offen lds
mm a[80:83],v[36:39],v[12:15],a[80:83]
mm a[80:83],v[32:35],v[8:11],a[80:83]
rd v[8:11],v236 offset:10240
rd v[12:15],v237 offset:10240
mm a[240:243],v[32:35],v[16:19],a[240:243]
s_add_u32 m0, s83, 51712
mm a[240:243],v[36:39],v[20:23],a[240:243]
ld v233,s[44:47],s80 offen lds
mm a[244:247],v[44:47],v[20:23],a[244:247]
mm a[244:247],v[40:43],v[16:19],a[244:247]
rt v[220:221],v243 offset:16896
rt v[222:223],v243 offset:17920
mm a[32:35],v[48:51],v[16:19],a[32:35]
)ASM" R"ASM(s_add_u32 m0, s83, 52864
mm a[32:35],v[52:55],v[20:23],a[32:35]
ld v234,s[44:47],s80 offen lds
mm a[36:39],v[60:63],v[20:23],a[36:39]
mm a[36:39],v[56:59],v[16:19],a[36:39]
rd v[16:19],v236 offset:0
rd v[20:23],v237 offset:0
mm a[60:63],v[56:59],v[24:27],a[60:63]
s_add_u32 m0, s83, 53888
mm a[60:63],v[60:63],v[28:31],a[60:63]
ld v235,s[44:47],s80 offen lds
mm a[56:59],v[52:55],v[28:31],a[56:59]
s_mov_b64 s[44:45], s[68:69]
s_mov_b32 s46, s70
mm a[56:59],v[48:51],v[24:27],a[56:59]
mm a[52:55],v[40:43],v[24:27],a[52:55]
mm a[52:55],v[44:47],v[28:31],a[52:55]
mm a[48:51],v[36:39],v[28:31],a[48:51]
wt lgkmcnt(2)
s_lshl_b32 s80, s77, 18
mm a[48:51],v[32:35],v[24:27],a[48:51]
rd v[24:27],v236 offset:2048
rd v[28:31],v237 offset:2048
mm a[96:99],v[192:195],v[160:163],a[96:99]
mm a[96:99],v[196:199],v[164:167],a[96:99]
mm a[100:103],v[204:207],v[164:167],a[100:103]
mm a[100:103],v[200:203],v[160:163],a[100:103]
rt v[32:33],v240 offset:128
rt v[34:35],v240 offset:1152
mm a[104:107],v[208:211],v[160:163],a[104:107]
mm a[104:107],v[212:215],v[164:167],a[104:107]
mm a[108:111],v[220:223],v[164:167],a[108:111]
wt lgkmcnt(2)
s_barrier
mm a[108:111],v[216:219],v[160:163],a[108:111]
mm a[140:143],v[216:219],v[168:171],a[140:143]
mm a[140:143],v[220:223],v[172:175],a[140:143]
mm a[136:139],v[212:215],v[172:175],a[136:139]
mm a[136:139],v[208:211],v[168:171],a[136:139]
rt v[36:37],v240 offset:17024
rt v[38:39],v240 offset:18048
mm a[124:127],v[200:203],v[168:171],a[124:127]
mm a[124:127],v[204:207],v[172:175],a[124:127]
mm a[120:123],v[196:199],v[172:175],a[120:123]
mm a[120:123],v[192:195],v[168:171],a[120:123]
mm a[232:235],v[192:195],v[176:179],a[232:235]
mm a[232:235],v[196:199],v[180:183],a[232:235]
buffer_store_dword v255, off, s[64:67], s100 sc1
mm a[236:239],v[204:207],v[180:183],a[236:239]
buffer_store_dword v255, off, s[64:67], s101 sc1
mm a[236:239],v[200:203],v[176:179],a[236:239]
rt v[40:41],v241 offset:128
rt v[42:43],v241 offset:1152
mm a[152:155],v[208:211],v[176:179],a[152:155]
mm a[152:155],v[212:215],v[180:183],a[152:155]
mm a[156:159],v[220:223],v[180:183],a[156:159]
mm a[156:159],v[216:219],v[176:179],a[156:159]
mm a[172:175],v[216:219],v[184:187],a[172:175]
mm a[172:175],v[220:223],v[188:191],a[172:175]
mm a[168:171],v[212:215],v[188:191],a[168:171]
mm a[168:171],v[208:211],v[184:187],a[168:171]
rt v[44:45],v241 offset:17024
rt v[46:47],v241 offset:18048
mm a[220:223],v[200:203],v[184:187],a[220:223]
mm a[220:223],v[204:207],v[188:191],a[220:223]
mm a[216:219],v[196:199],v[188:191],a[216:219]
mm a[216:219],v[192:195],v[184:187],a[216:219]
mm a[200:203],v[192:195],v[0:3],a[200:203]
mm a[200:203],v[196:199],v[4:7],a[200:203]
wt vmcnt(1)
s_memtime s[100:101]
wt vmcnt(0)
s_memtime s[92:93]
wt lgkmcnt(0)
s_sub_u32 s92, s92, s100
s_cmp_lt_u32 s92, 80
s_cselect_b32 s92, 8192, 0
v_mov_b32 v77, s92
ds_write_b32 v76, v77
mm a[204:207],v[204:207],v[4:7],a[204:207]
mm a[204:207],v[200:203],v[0:3],a[204:207]
rt v[48:49],v242 offset:128
rt v[50:51],v242 offset:1152
mm a[184:187],v[208:211],v[0:3],a[184:187]
mm a[184:187],v[212:215],v[4:7],a[184:187]
mm a[188:191],v[220:223],v[4:7],a[188:191]
mm a[188:191],v[216:219],v[0:3],a[188:191]
mm a[12:15],v[216:219],v[8:11],a[12:15]
mm a[12:15],v[220:223],v[12:15],a[12:15]
buffer_load_dword v254, v135, s[68:71], s96 offen
mm a[8:11],v[212:215],v[12:15],a[8:11]
s_add_u32 s96, s96, 1048576
mm a[8:11],v[208:211],v[8:11],a[8:11]
rt v[52:53],v242 offset:17024
rt v[54:55],v242 offset:18048
mm a[76:79],v[200:203],v[8:11],a[76:79]
mm a[76:79],v[204:207],v[12:15],a[76:79]
buffer_load_dword v254, v135, s[68:71], s96 offen
mm a[72:75],v[196:199],v[12:15],a[72:75]
s_add_u32 s96, s96, 1048576
)ASM" R"ASM(mm a[72:75],v[192:195],v[8:11],a[72:75]
rt v[56:57],v243 offset:128
rt v[58:59],v243 offset:1152
mm a[88:91],v[192:195],v[16:19],a[88:91]
mm a[88:91],v[196:199],v[20:23],a[88:91]
mm a[92:95],v[204:207],v[20:23],a[92:95]
mm a[92:95],v[200:203],v[16:19],a[92:95]
rt v[60:61],v243 offset:17024
rt v[62:63],v243 offset:18048
mm a[24:27],v[208:211],v[16:19],a[24:27]
mm a[24:27],v[212:215],v[20:23],a[24:27]
mm a[28:31],v[220:223],v[20:23],a[28:31]
mm a[28:31],v[216:219],v[16:19],a[28:31]
mm a[44:47],v[216:219],v[24:27],a[44:47]
mm a[44:47],v[220:223],v[28:31],a[44:47]
ld v[72:75],v255,s[64:67],s89 offen offset:1024 sc1
mm a[40:43],v[212:215],v[28:31],a[40:43]
mm a[40:43],v[208:211],v[24:27],a[40:43]
mm a[252:255],v[200:203],v[24:27],a[252:255]
mm a[252:255],v[204:207],v[28:31],a[252:255]
mm a[248:251],v[196:199],v[28:31],a[248:251]
wt lgkmcnt(0)
s_barrier
mm a[248:251],v[192:195],v[24:27],a[248:251]
mm a[112:115],v[32:35],v[160:163],a[112:115]
mm a[112:115],v[36:39],v[164:167],a[112:115]
mm a[116:119],v[44:47],v[164:167],a[116:119]
rt v[192:193],v240 offset:33792
rt v[194:195],v240 offset:34816
mm a[116:119],v[40:43],v[160:163],a[116:119]
rt v[196:197],v240 offset:50688
rt v[198:199],v240 offset:51712
mm a[128:131],v[48:51],v[160:163],a[128:131]
mm a[128:131],v[52:55],v[164:167],a[128:131]
mm a[132:135],v[60:63],v[164:167],a[132:135]
mm a[132:135],v[56:59],v[160:163],a[132:135]
rd v[160:163],v236 offset:57344
rd v[164:167],v237 offset:57344
mm a[148:151],v[56:59],v[168:171],a[148:151]
mm a[148:151],v[60:63],v[172:175],a[148:151]
mm a[144:147],v[52:55],v[172:175],a[144:147]
mm a[144:147],v[48:51],v[168:171],a[144:147]
rt v[200:201],v241 offset:33792
rt v[202:203],v241 offset:34816
mm a[228:231],v[40:43],v[168:171],a[228:231]
mm a[228:231],v[44:47],v[172:175],a[228:231]
mm a[224:227],v[36:39],v[172:175],a[224:227]
mm a[224:227],v[32:35],v[168:171],a[224:227]
rd v[168:171],v236 offset:59392
rd v[172:175],v237 offset:59392
mm a[208:211],v[32:35],v[176:179],a[208:211]
mm a[208:211],v[36:39],v[180:183],a[208:211]
mm a[212:215],v[44:47],v[180:183],a[212:215]
mm a[212:215],v[40:43],v[176:179],a[212:215]
rt v[204:205],v241 offset:50688
rt v[206:207],v241 offset:51712
mm a[160:163],v[48:51],v[176:179],a[160:163]
mm a[160:163],v[52:55],v[180:183],a[160:163]
mm a[164:167],v[60:63],v[180:183],a[164:167]
mm a[164:167],v[56:59],v[176:179],a[164:167]
rd v[176:179],v236 offset:49152
rd v[180:183],v237 offset:49152
mm a[180:183],v[56:59],v[184:187],a[180:183]
mm a[180:183],v[60:63],v[188:191],a[180:183]
mm a[176:179],v[52:55],v[188:191],a[176:179]
mm a[176:179],v[48:51],v[184:187],a[176:179]
rt v[208:209],v242 offset:33792
rt v[210:211],v242 offset:34816
mm a[196:199],v[40:43],v[184:187],a[196:199]
s_add_u32 m0, s83, 0
mm a[196:199],v[44:47],v[188:191],a[196:199]
ld v232,s[44:47],s80 offen lds
mm a[192:195],v[36:39],v[188:191],a[192:195]
mm a[192:195],v[32:35],v[184:187],a[192:195]
rd v[184:187],v236 offset:51200
rd v[188:191],v237 offset:51200
mm a[64:67],v[32:35],v[0:3],a[64:67]
s_add_u32 m0, s83, 1024
mm a[64:67],v[36:39],v[4:7],a[64:67]
ld v233,s[44:47],s80 offen lds
mm a[68:71],v[44:47],v[4:7],a[68:71]
mm a[68:71],v[40:43],v[0:3],a[68:71]
rt v[212:213],v242 offset:50688
rt v[214:215],v242 offset:51712
mm a[0:3],v[48:51],v[0:3],a[0:3]
s_add_u32 m0, s83, 2176
mm a[0:3],v[52:55],v[4:7],a[0:3]
ld v234,s[44:47],s80 offen lds
mm a[4:7],v[60:63],v[4:7],a[4:7]
mm a[4:7],v[56:59],v[0:3],a[4:7]
rd v[0:3],v236 offset:40960
rd v[4:7],v237 offset:40960
mm a[20:23],v[56:59],v[8:11],a[20:23]
s_add_u32 m0, s83, 3200
mm a[20:23],v[60:63],v[12:15],a[20:23]
ld v235,s[44:47],s80 offen lds
mm a[16:19],v[52:55],v[12:15],a[16:19]
s_add_u32 s80, s80, 524288
mm a[16:19],v[48:51],v[8:11],a[16:19]
rt v[216:217],v243 offset:33792
)ASM" R"ASM(rt v[218:219],v243 offset:34816
mm a[84:87],v[40:43],v[8:11],a[84:87]
s_add_u32 m0, s83, 16896
mm a[84:87],v[44:47],v[12:15],a[84:87]
ld v232,s[44:47],s80 offen lds
mm a[80:83],v[36:39],v[12:15],a[80:83]
mm a[80:83],v[32:35],v[8:11],a[80:83]
rd v[8:11],v236 offset:43008
rd v[12:15],v237 offset:43008
mm a[240:243],v[32:35],v[16:19],a[240:243]
s_add_u32 m0, s83, 17920
mm a[240:243],v[36:39],v[20:23],a[240:243]
ld v233,s[44:47],s80 offen lds
mm a[244:247],v[44:47],v[20:23],a[244:247]
mm a[244:247],v[40:43],v[16:19],a[244:247]
rt v[220:221],v243 offset:50688
rt v[222:223],v243 offset:51712
mm a[32:35],v[48:51],v[16:19],a[32:35]
s_add_u32 m0, s83, 19072
mm a[32:35],v[52:55],v[20:23],a[32:35]
ld v234,s[44:47],s80 offen lds
mm a[36:39],v[60:63],v[20:23],a[36:39]
mm a[36:39],v[56:59],v[16:19],a[36:39]
rd v[16:19],v236 offset:32768
rd v[20:23],v237 offset:32768
mm a[60:63],v[56:59],v[24:27],a[60:63]
s_add_u32 m0, s83, 20096
mm a[60:63],v[60:63],v[28:31],a[60:63]
ld v235,s[44:47],s80 offen lds
mm a[56:59],v[52:55],v[28:31],a[56:59]
s_add_u32 s80, s80, 524288
mm a[56:59],v[48:51],v[24:27],a[56:59]
mm a[52:55],v[40:43],v[24:27],a[52:55]
mm a[52:55],v[44:47],v[28:31],a[52:55]
mm a[48:51],v[36:39],v[28:31],a[48:51]
wt lgkmcnt(2)
mm a[48:51],v[32:35],v[24:27],a[48:51]
rd v[24:27],v236 offset:34816
rd v[28:31],v237 offset:34816
mm a[96:99],v[192:195],v[160:163],a[96:99]
mm a[96:99],v[196:199],v[164:167],a[96:99]
mm a[100:103],v[204:207],v[164:167],a[100:103]
mm a[100:103],v[200:203],v[160:163],a[100:103]
rt v[32:33],v240 offset:33920
rt v[34:35],v240 offset:34944
mm a[104:107],v[208:211],v[160:163],a[104:107]
mm a[104:107],v[212:215],v[164:167],a[104:107]
mm a[108:111],v[220:223],v[164:167],a[108:111]
wt lgkmcnt(2)
s_barrier
mm a[108:111],v[216:219],v[160:163],a[108:111]
mm a[140:143],v[216:219],v[168:171],a[140:143]
wt vmcnt(8)
v_cmp_eq_u64 vcc, v[72:73], v[252:253]
mm a[140:143],v[220:223],v[172:175],a[140:143]
v_cmp_eq_u64 s[96:97], v[74:75], v[252:253]
mm a[136:139],v[212:215],v[172:175],a[136:139]
s_and_b64 vcc, vcc, s[96:97]
s_cmp_eq_u64 vcc, -1
s_cbranch_scc0 .La5_LK_XSPIN
.La5_LK_XOK:
mm a[136:139],v[208:211],v[168:171],a[136:139]
rt v[36:37],v240 offset:50816
rt v[38:39],v240 offset:51840
mm a[124:127],v[200:203],v[168:171],a[124:127]
s_mov_b32 s89, 49152
mm a[124:127],v[204:207],v[172:175],a[124:127]
mm a[120:123],v[196:199],v[172:175],a[120:123]
ar v224,a96
ar v225,a97
mm a[120:123],v[192:195],v[168:171],a[120:123]
ar v226,a98
ar v227,a99
mm a[232:235],v[192:195],v[176:179],a[232:235]
ar v228,a100
ar v229,a101
mm a[232:235],v[196:199],v[180:183],a[232:235]
ld v[128:131],v244,s[52:55],s91 offen offset:0 sc1
mm a[236:239],v[204:207],v[180:183],a[236:239]
ar v230,a102
ar v231,a103
mm a[236:239],v[200:203],v[176:179],a[236:239]
rt v[40:41],v241 offset:33920
rt v[42:43],v241 offset:34944
mm a[152:155],v[208:211],v[176:179],a[152:155]
cv v248,v224,v225
cv v249,v226,v227
mm a[152:155],v[212:215],v[180:183],a[152:155]
ld v[132:135],v245,s[52:55],s91 offen offset:0 sc1
mm a[156:159],v[220:223],v[180:183],a[156:159]
cv v250,v228,v229
cv v251,v230,v231
mm a[156:159],v[216:219],v[176:179],a[156:159]
st v[248:251],v255,s[48:51],s89 offen sc1
mm a[172:175],v[216:219],v[184:187],a[172:175]
s_mov_b32 s89, 50176
.La5_LW1_3:
s_cbranch_execz .La5_LW1_7
ds_write_b128 v238, v[248:251] offset:0
mm a[172:175],v[220:223],v[188:191],a[172:175]
)ASM" R"ASM(ld v[136:139],v246,s[52:55],s91 offen offset:0 sc1
mm a[168:171],v[212:215],v[188:191],a[168:171]
ar v224,a104
ar v225,a105
mm a[168:171],v[208:211],v[184:187],a[168:171]
rt v[44:45],v241 offset:50816
rt v[46:47],v241 offset:51840
mm a[220:223],v[200:203],v[184:187],a[220:223]
ar v226,a106
ar v227,a107
mm a[220:223],v[204:207],v[188:191],a[220:223]
ld v[140:143],v247,s[52:55],s91 offen offset:0 sc1
mm a[216:219],v[196:199],v[188:191],a[216:219]
ar v228,a108
ar v229,a109
mm a[216:219],v[192:195],v[184:187],a[216:219]
ar v230,a110
ar v231,a111
mm a[200:203],v[192:195],v[0:3],a[200:203]
cv v248,v224,v225
cv v249,v226,v227
mm a[200:203],v[196:199],v[4:7],a[200:203]
ld v[144:147],v244,s[52:55],s91 offen offset:1024 sc1
mm a[204:207],v[204:207],v[4:7],a[204:207]
cv v250,v228,v229
cv v251,v230,v231
mm a[204:207],v[200:203],v[0:3],a[204:207]
rt v[48:49],v242 offset:33920
rt v[50:51],v242 offset:34944
mm a[184:187],v[208:211],v[0:3],a[184:187]
st v[248:251],v255,s[48:51],s89 offen sc1
mm a[184:187],v[212:215],v[4:7],a[184:187]
ld v[148:151],v245,s[52:55],s91 offen offset:1024 sc1
mm a[188:191],v[220:223],v[4:7],a[188:191]
s_mov_b32 s89, 51200
ds_write_b128 v238, v[248:251] offset:1024
mm a[188:191],v[216:219],v[0:3],a[188:191]
mm a[12:15],v[216:219],v[8:11],a[12:15]
mm a[12:15],v[220:223],v[12:15],a[12:15]
ld v[152:155],v246,s[52:55],s91 offen offset:1024 sc1
mm a[8:11],v[212:215],v[12:15],a[8:11]
mm a[8:11],v[208:211],v[8:11],a[8:11]
rt v[52:53],v242 offset:50816
rt v[54:55],v242 offset:51840
mm a[76:79],v[200:203],v[8:11],a[76:79]
mm a[76:79],v[204:207],v[12:15],a[76:79]
ld v[156:159],v247,s[52:55],s91 offen offset:1024 sc1
mm a[72:75],v[196:199],v[12:15],a[72:75]
mm a[72:75],v[192:195],v[8:11],a[72:75]
rt v[56:57],v243 offset:33920
rt v[58:59],v243 offset:34944
mm a[88:91],v[192:195],v[16:19],a[88:91]
mm a[88:91],v[196:199],v[20:23],a[88:91]
mm a[92:95],v[204:207],v[20:23],a[92:95]
mm a[92:95],v[200:203],v[16:19],a[92:95]
rt v[60:61],v243 offset:50816
rt v[62:63],v243 offset:51840
mm a[24:27],v[208:211],v[16:19],a[24:27]
mm a[24:27],v[212:215],v[20:23],a[24:27]
mm a[28:31],v[220:223],v[20:23],a[28:31]
mm a[28:31],v[216:219],v[16:19],a[28:31]
mm a[44:47],v[216:219],v[24:27],a[44:47]
mm a[44:47],v[220:223],v[28:31],a[44:47]
mm a[40:43],v[212:215],v[28:31],a[40:43]
mm a[40:43],v[208:211],v[24:27],a[40:43]
mm a[252:255],v[200:203],v[24:27],a[252:255]
mm a[252:255],v[204:207],v[28:31],a[252:255]
mm a[248:251],v[196:199],v[28:31],a[248:251]
wt vmcnt(10) lgkmcnt(0)
s_barrier
mm a[248:251],v[192:195],v[24:27],a[248:251]
mm a[112:115],v[32:35],v[160:163],a[112:115]
s_add_u32 m0, s83, 33792
mm a[112:115],v[36:39],v[164:167],a[112:115]
ld v232,s[44:47],s80 offen lds
mm a[116:119],v[44:47],v[164:167],a[116:119]
mm a[116:119],v[40:43],v[160:163],a[116:119]
rt v[192:193],v240 offset:0
rt v[194:195],v240 offset:1024
mm a[128:131],v[48:51],v[160:163],a[128:131]
s_add_u32 m0, s83, 34816
mm a[128:131],v[52:55],v[164:167],a[128:131]
ld v233,s[44:47],s80 offen lds
mm a[132:135],v[60:63],v[164:167],a[132:135]
mm a[132:135],v[56:59],v[160:163],a[132:135]
rt v[196:197],v240 offset:8448
rt v[198:199],v240 offset:9472
mm a[148:151],v[56:59],v[168:171],a[148:151]
s_add_u32 m0, s83, 35968
mm a[148:151],v[60:63],v[172:175],a[148:151]
ld v234,s[44:47],s80 offen lds
mm a[144:147],v[52:55],v[172:175],a[144:147]
ar v224,a112
ar v225,a113
mm a[144:147],v[48:51],v[168:171],a[144:147]
rt v[200:201],v241 offset:0
rt v[202:203],v241 offset:1024
mm a[228:231],v[40:43],v[168:171],a[228:231]
s_add_u32 m0, s83, 36992
ar v226,a114
)ASM" R"ASM(mm a[228:231],v[44:47],v[172:175],a[228:231]
ld v235,s[44:47],s80 offen lds
mm a[224:227],v[36:39],v[172:175],a[224:227]
s_add_u32 s80, s80, 524288
ar v227,a115
mm a[224:227],v[32:35],v[168:171],a[224:227]
rt v[204:205],v241 offset:8448
rt v[206:207],v241 offset:9472
mm a[208:211],v[32:35],v[176:179],a[208:211]
s_add_u32 m0, s83, 50688
ar v228,a116
mm a[208:211],v[36:39],v[180:183],a[208:211]
ld v232,s[44:47],s80 offen lds
mm a[212:215],v[44:47],v[180:183],a[212:215]
ar v229,a117
ar v230,a118
mm a[212:215],v[40:43],v[176:179],a[212:215]
rt v[208:209],v242 offset:0
rt v[210:211],v242 offset:1024
mm a[160:163],v[48:51],v[176:179],a[160:163]
s_add_u32 m0, s83, 51712
ar v231,a119
mm a[160:163],v[52:55],v[180:183],a[160:163]
ld v233,s[44:47],s80 offen lds
mm a[164:167],v[60:63],v[180:183],a[164:167]
cv v248,v224,v225
cv v249,v226,v227
mm a[164:167],v[56:59],v[176:179],a[164:167]
rt v[212:213],v242 offset:8448
rt v[214:215],v242 offset:9472
mm a[180:183],v[56:59],v[184:187],a[180:183]
s_add_u32 m0, s83, 52864
cv v250,v228,v229
mm a[180:183],v[60:63],v[188:191],a[180:183]
ld v234,s[44:47],s80 offen lds
mm a[176:179],v[52:55],v[188:191],a[176:179]
cv v251,v230,v231
st v[248:251],v255,s[48:51],s89 offen sc1
mm a[176:179],v[48:51],v[184:187],a[176:179]
rt v[216:217],v243 offset:0
rt v[218:219],v243 offset:1024
mm a[196:199],v[40:43],v[184:187],a[196:199]
s_add_u32 m0, s83, 53888
s_mov_b32 s89, 52224
mm a[196:199],v[44:47],v[188:191],a[196:199]
ld v235,s[44:47],s80 offen lds
mm a[192:195],v[36:39],v[188:191],a[192:195]
s_add_u32 s80, s80, 524288
ds_write_b128 v238, v[248:251] offset:2048
mm a[192:195],v[32:35],v[184:187],a[192:195]
rt v[220:221],v243 offset:8448
rt v[222:223],v243 offset:9472
mm a[64:67],v[32:35],v[0:3],a[64:67]
mm a[64:67],v[36:39],v[4:7],a[64:67]
ld v[160:163],v244,s[52:55],s91 offen offset:2048 sc1
mm a[68:71],v[44:47],v[4:7],a[68:71]
mm a[68:71],v[40:43],v[0:3],a[68:71]
mm a[0:3],v[48:51],v[0:3],a[0:3]
mm a[0:3],v[52:55],v[4:7],a[0:3]
ld v[164:167],v245,s[52:55],s91 offen offset:2048 sc1
mm a[4:7],v[60:63],v[4:7],a[4:7]
mm a[4:7],v[56:59],v[0:3],a[4:7]
mm a[20:23],v[56:59],v[8:11],a[20:23]
mm a[20:23],v[60:63],v[12:15],a[20:23]
ld v[168:171],v246,s[52:55],s91 offen offset:2048 sc1
mm a[16:19],v[52:55],v[12:15],a[16:19]
mm a[16:19],v[48:51],v[8:11],a[16:19]
mm a[84:87],v[40:43],v[8:11],a[84:87]
mm a[84:87],v[44:47],v[12:15],a[84:87]
ld v[172:175],v247,s[52:55],s91 offen offset:2048 sc1
mm a[80:83],v[36:39],v[12:15],a[80:83]
mm a[80:83],v[32:35],v[8:11],a[80:83]
mm a[240:243],v[32:35],v[16:19],a[240:243]
mm a[240:243],v[36:39],v[20:23],a[240:243]
mm a[244:247],v[44:47],v[20:23],a[244:247]
mm a[244:247],v[40:43],v[16:19],a[244:247]
mm a[32:35],v[48:51],v[16:19],a[32:35]
mm a[32:35],v[52:55],v[20:23],a[32:35]
mm a[36:39],v[60:63],v[20:23],a[36:39]
mm a[36:39],v[56:59],v[16:19],a[36:39]
mm a[60:63],v[56:59],v[24:27],a[60:63]
mm a[60:63],v[60:63],v[28:31],a[60:63]
mm a[56:59],v[52:55],v[28:31],a[56:59]
mm a[56:59],v[48:51],v[24:27],a[56:59]
mm a[52:55],v[40:43],v[24:27],a[52:55]
mm a[52:55],v[44:47],v[28:31],a[52:55]
mm a[48:51],v[36:39],v[28:31],a[48:51]
mm a[48:51],v[32:35],v[24:27],a[48:51]
s_branch .La5_LQ_START
.La5_LV_XSPIN:
s_mov_b32 s93, 0
.La5_LV_XSPIN_L:
s_add_u32 s93, s93, 1
s_cmp_gt_u32 s93, 2097152
s_cbranch_scc1 .La5_LV_XOK
s_sleep 8
ld v[72:75],v255,s[64:67],s89 offen offset:1024 sc1
wt vmcnt(0)
v_cmp_eq_u64 vcc, v[72:73], v[252:253]
v_cmp_eq_u64 s[96:97], v[74:75], v[252:253]
)ASM" R"ASM(s_nop 1
s_and_b64 vcc, vcc, s[96:97]
s_cmp_eq_u64 vcc, -1
s_cbranch_scc0 .La5_LV_XSPIN_L
s_branch .La5_LV_XOK
.La5_LK_XSPIN:
s_mov_b32 s93, 0
.La5_LK_XSPIN_L:
s_add_u32 s93, s93, 1
s_cmp_gt_u32 s93, 2097152
s_cbranch_scc1 .La5_LK_XOK
s_sleep 8
ld v[72:75],v255,s[64:67],s89 offen offset:1024 sc1
wt vmcnt(0)
v_cmp_eq_u64 vcc, v[72:73], v[252:253]
v_cmp_eq_u64 s[96:97], v[74:75], v[252:253]
s_nop 1
s_and_b64 vcc, vcc, s[96:97]
s_cmp_eq_u64 vcc, -1
s_cbranch_scc0 .La5_LK_XSPIN_L
s_branch .La5_LK_XOK
; ---- V tile (waves 1, 3): the same with the operands exchanged (V comes out transposed)
.La5_LV_START:
s_nop 0
s_nop 0
.La5_LW1_4:
s_cbranch_execz .La5_LW1_8
mm a[96:99],v[160:163],v[192:195],0
s_add_u32 m0, s83, 33792
mm a[96:99],v[164:167],v[196:199],a[96:99]
ld v232,s[44:47],s80 offen lds
mm a[104:107],v[164:167],v[204:207],0
rd v[0:3],v236 offset:8192
rd v[4:7],v237 offset:8192
s_add_u32 m0, s83, 34816
mm a[104:107],v[160:163],v[200:203],a[104:107]
rd v[8:11],v236 offset:10240
rd v[12:15],v237 offset:10240
ld v233,s[44:47],s80 offen lds
mm a[112:115],v[160:163],v[208:211],0
s_add_u32 m0, s83, 35968
mm a[112:115],v[164:167],v[212:215],a[112:115]
ld v234,s[44:47],s80 offen lds
mm a[128:131],v[164:167],v[220:223],0
rd v[16:19],v236 offset:0
rd v[20:23],v237 offset:0
s_add_u32 m0, s83, 36992
mm a[128:131],v[160:163],v[216:219],a[128:131]
rd v[24:27],v236 offset:2048
rd v[28:31],v237 offset:2048
ld v235,s[44:47],s80 offen lds
mm a[132:135],v[168:171],v[216:219],0
s_mov_b32 s80, 2883584
mm a[132:135],v[172:175],v[220:223],a[132:135]
mm a[116:119],v[172:175],v[212:215],0
s_xor_b32 m0, s82, 32768
mm a[116:119],v[168:171],v[208:211],a[116:119]
rt v[32:33],v240 offset:128
rt v[34:35],v240 offset:1152
ld v224,s[40:43],s79 offen lds
mm a[108:111],v[168:171],v[200:203],0
s_xor_b32 m0, s82, 33792
mm a[108:111],v[172:175],v[204:207],a[108:111]
ld v225,s[40:43],s79 offen lds
mm a[100:103],v[172:175],v[196:199],0
s_xor_b32 m0, s82, 34816
mm a[100:103],v[168:171],v[192:195],a[100:103]
rt v[36:37],v240 offset:17024
rt v[38:39],v240 offset:18048
ld v226,s[40:43],s79 offen lds
mm a[232:235],v[176:179],v[192:195],0
s_xor_b32 m0, s82, 35840
mm a[232:235],v[180:183],v[196:199],a[232:235]
ld v227,s[40:43],s79 offen lds
mm a[152:155],v[180:183],v[204:207],0
s_xor_b32 m0, s82, 36864
mm a[152:155],v[176:179],v[200:203],a[152:155]
rt v[40:41],v241 offset:128
rt v[42:43],v241 offset:1152
ld v228,s[40:43],s79 offen lds
mm a[208:211],v[176:179],v[208:211],0
s_xor_b32 m0, s82, 37888
mm a[208:211],v[180:183],v[212:215],a[208:211]
ld v229,s[40:43],s79 offen lds
mm a[160:163],v[180:183],v[220:223],0
s_xor_b32 m0, s82, 38912
mm a[160:163],v[176:179],v[216:219],a[160:163]
ld v230,s[40:43],s79 offen lds
mm a[164:167],v[184:187],v[216:219],0
s_xor_b32 m0, s82, 39936
mm a[164:167],v[188:191],v[220:223],a[164:167]
ld v231,s[40:43],s79 offen lds
mm a[212:215],v[188:191],v[212:215],0
s_mov_b32 s79, 768
mm a[212:215],v[184:187],v[208:211],a[212:215]
rt v[44:45],v241 offset:17024
rt v[46:47],v241 offset:18048
mm a[156:159],v[184:187],v[200:203],0
s_add_u32 m0, s83, 50688
mm a[156:159],v[188:191],v[204:207],a[156:159]
)ASM" R"ASM(ld v232,s[44:47],s80 offen lds
mm a[236:239],v[188:191],v[196:199],0
wt lgkmcnt(8)
s_add_u32 m0, s83, 51712
mm a[236:239],v[184:187],v[192:195],a[236:239]
ld v233,s[44:47],s80 offen lds
mm a[200:203],v[0:3],v[192:195],0
s_add_u32 m0, s83, 52864
mm a[200:203],v[4:7],v[196:199],a[200:203]
ld v234,s[44:47],s80 offen lds
mm a[184:187],v[4:7],v[204:207],0
s_add_u32 m0, s83, 53888
mm a[184:187],v[0:3],v[200:203],a[184:187]
rt v[48:49],v242 offset:128
rt v[50:51],v242 offset:1152
ld v235,s[44:47],s80 offen lds
mm a[64:67],v[0:3],v[208:211],0
s_mov_b32 s80, 3145728
mm a[64:67],v[4:7],v[212:215],a[64:67]
mm a[0:3],v[4:7],v[220:223],0
mm a[0:3],v[0:3],v[216:219],a[0:3]
mm a[4:7],v[8:11],v[216:219],0
mm a[4:7],v[12:15],v[220:223],a[4:7]
buffer_load_dword v70, v118, s[60:63], 0 offen
mm a[68:71],v[12:15],v[212:215],0
mm a[68:71],v[8:11],v[208:211],a[68:71]
rt v[52:53],v242 offset:17024
rt v[54:55],v242 offset:18048
mm a[188:191],v[8:11],v[200:203],0
mm a[188:191],v[12:15],v[204:207],a[188:191]
ld v[72:75],v119,s[60:63],0 offen offset:0
mm a[204:207],v[12:15],v[196:199],0
mm a[204:207],v[8:11],v[192:195],a[204:207]
rt v[56:57],v243 offset:128
rt v[58:59],v243 offset:1152
mm a[88:91],v[16:19],v[192:195],0
mm a[88:91],v[20:23],v[196:199],a[88:91]
ld v[76:79],v119,s[60:63],0 offen offset:128
mm a[24:27],v[20:23],v[204:207],0
mm a[24:27],v[16:19],v[200:203],a[24:27]
rt v[60:61],v243 offset:17024
rt v[62:63],v243 offset:18048
mm a[240:243],v[16:19],v[208:211],0
mm a[240:243],v[20:23],v[212:215],a[240:243]
ld v[80:83],v119,s[60:63],0 offen offset:256
mm a[32:35],v[20:23],v[220:223],0
mm a[32:35],v[16:19],v[216:219],a[32:35]
mm a[36:39],v[24:27],v[216:219],0
mm a[36:39],v[28:31],v[220:223],a[36:39]
ld v[84:87],v119,s[60:63],0 offen offset:384
mm a[244:247],v[28:31],v[212:215],0
mm a[244:247],v[24:27],v[208:211],a[244:247]
mm a[28:31],v[24:27],v[200:203],0
mm a[28:31],v[28:31],v[204:207],a[28:31]
mm a[92:95],v[28:31],v[196:199],0
mm a[92:95],v[24:27],v[192:195],a[92:95]
s_mov_b64 exec, 0
s_cmp_eq_u32 s3, 1
s_cbranch_scc1 .La5_LW2_1
s_branch .La5_LW2_R1
.La5_LW2_R1:
s_mov_b64 exec, -1
wt vmcnt(5) lgkmcnt(0)
s_barrier
mm a[120:123],v[160:163],v[32:35],0
mm a[120:123],v[164:167],v[36:39],a[120:123]
mm a[136:139],v[164:167],v[44:47],0
rt v[192:193],v240 offset:33792
rt v[194:195],v240 offset:34816
mm a[136:139],v[160:163],v[40:43],a[136:139]
rt v[196:197],v240 offset:50688
rt v[198:199],v240 offset:51712
mm a[224:227],v[160:163],v[48:51],0
mm a[224:227],v[164:167],v[52:55],a[224:227]
mm a[144:147],v[164:167],v[60:63],0
mm a[144:147],v[160:163],v[56:59],a[144:147]
rd v[160:163],v236 offset:57344
rd v[164:167],v237 offset:57344
mm a[148:151],v[168:171],v[56:59],0
mm a[148:151],v[172:175],v[60:63],a[148:151]
mm a[228:231],v[172:175],v[52:55],0
mm a[228:231],v[168:171],v[48:51],a[228:231]
rt v[200:201],v241 offset:33792
rt v[202:203],v241 offset:34816
mm a[140:143],v[168:171],v[40:43],0
mm a[140:143],v[172:175],v[44:47],a[140:143]
mm a[124:127],v[172:175],v[36:39],0
mm a[124:127],v[168:171],v[32:35],a[124:127]
rd v[168:171],v236 offset:59392
rd v[172:175],v237 offset:59392
mm a[216:219],v[176:179],v[32:35],0
s_xor_b32 m0, s82, 0
mm a[216:219],v[180:183],v[36:39],a[216:219]
ld v224,s[40:43],s79 offen lds
mm a[168:171],v[180:183],v[44:47],0
mm a[168:171],v[176:179],v[40:43],a[168:171]
rt v[204:205],v241 offset:50688
rt v[206:207],v241 offset:51712
s_xor_b32 m0, s82, 1024
mm a[192:195],v[176:179],v[48:51],0
)ASM" R"ASM(ld v225,s[40:43],s79 offen lds
mm a[192:195],v[180:183],v[52:55],a[192:195]
mm a[176:179],v[180:183],v[60:63],0
s_xor_b32 m0, s82, 2048
mm a[176:179],v[176:179],v[56:59],a[176:179]
rd v[176:179],v236 offset:49152
rd v[180:183],v237 offset:49152
ld v226,s[40:43],s79 offen lds
mm a[180:183],v[184:187],v[56:59],0
mm a[180:183],v[188:191],v[60:63],a[180:183]
s_xor_b32 m0, s82, 3072
mm a[196:199],v[188:191],v[52:55],0
ld v227,s[40:43],s79 offen lds
mm a[196:199],v[184:187],v[48:51],a[196:199]
rt v[208:209],v242 offset:33792
rt v[210:211],v242 offset:34816
mm a[172:175],v[184:187],v[40:43],0
s_xor_b32 m0, s82, 4096
mm a[172:175],v[188:191],v[44:47],a[172:175]
ld v228,s[40:43],s79 offen lds
mm a[220:223],v[188:191],v[36:39],0
mm a[220:223],v[184:187],v[32:35],a[220:223]
rd v[184:187],v236 offset:51200
rd v[188:191],v237 offset:51200
s_xor_b32 m0, s82, 5120
mm a[72:75],v[0:3],v[32:35],0
ld v229,s[40:43],s79 offen lds
mm a[72:75],v[4:7],v[36:39],a[72:75]
mm a[8:11],v[4:7],v[44:47],0
s_xor_b32 m0, s82, 6144
mm a[8:11],v[0:3],v[40:43],a[8:11]
rt v[212:213],v242 offset:50688
rt v[214:215],v242 offset:51712
ld v230,s[40:43],s79 offen lds
mm a[80:83],v[0:3],v[48:51],0
mm a[80:83],v[4:7],v[52:55],a[80:83]
s_xor_b32 m0, s82, 7168
mm a[16:19],v[4:7],v[60:63],0
ld v231,s[40:43],s79 offen lds
mm a[16:19],v[0:3],v[56:59],a[16:19]
rd v[0:3],v236 offset:40960
rd v[4:7],v237 offset:40960
s_mov_b32 s79, 896
mm a[20:23],v[8:11],v[56:59],0
mm a[20:23],v[12:15],v[60:63],a[20:23]
s_add_u32 m0, s83, 0
mm a[84:87],v[12:15],v[52:55],0
ld v232,s[44:47],s80 offen lds
mm a[84:87],v[8:11],v[48:51],a[84:87]
rt v[216:217],v243 offset:33792
rt v[218:219],v243 offset:34816
mm a[12:15],v[8:11],v[40:43],0
s_add_u32 m0, s83, 1024
mm a[12:15],v[12:15],v[44:47],a[12:15]
ld v233,s[44:47],s80 offen lds
mm a[76:79],v[12:15],v[36:39],0
wt vmcnt(10)
v_lshlrev_b32 v70, 16, v70
mm a[76:79],v[8:11],v[32:35],a[76:79]
rd v[8:11],v236 offset:43008
rd v[12:15],v237 offset:43008
s_add_u32 m0, s83, 2176
mm a[248:251],v[16:19],v[32:35],0
ld v234,s[44:47],s80 offen lds
mm a[248:251],v[20:23],v[36:39],a[248:251]
v_lshlrev_b32 v88, 16, v72
v_and_b32 v89, 0xffff0000, v72
mm a[40:43],v[20:23],v[44:47],0
s_add_u32 m0, s83, 3200
v_sub_f32 v88, v88, v70
mm a[40:43],v[16:19],v[40:43],a[40:43]
rt v[220:221],v243 offset:50688
rt v[222:223],v243 offset:51712
ld v235,s[44:47],s80 offen lds
mm a[48:51],v[16:19],v[48:51],0
s_mov_b32 s80, 3407872
v_sub_f32 v89, v89, v70
mm a[48:51],v[20:23],v[52:55],a[48:51]
v_add_f32 v120, v88, v89
v_mul_f32 v122, v88, v88
mm a[56:59],v[20:23],v[60:63],0
s_add_u32 m0, s83, 16896
v_mul_f32 v123, v89, v89
mm a[56:59],v[16:19],v[56:59],a[56:59]
rd v[16:19],v236 offset:32768
rd v[20:23],v237 offset:32768
ld v232,s[44:47],s80 offen lds
mm a[60:63],v[24:27],v[56:59],0
v_lshlrev_b32 v90, 16, v73
v_and_b32 v91, 0xffff0000, v73
mm a[60:63],v[28:31],v[60:63],a[60:63]
s_add_u32 m0, s83, 17920
v_sub_f32 v90, v90, v70
mm a[52:55],v[28:31],v[52:55],0
ld v233,s[44:47],s80 offen lds
mm a[52:55],v[24:27],v[48:51],a[52:55]
v_sub_f32 v91, v91, v70
v_add_f32 v124, v90, v91
mm a[44:47],v[24:27],v[40:43],0
s_add_u32 m0, s83, 19072
)ASM" R"ASM(v_fmac_f32 v122, v90, v90
mm a[44:47],v[28:31],v[44:47],a[44:47]
ld v234,s[44:47],s80 offen lds
mm a[252:255],v[28:31],v[36:39],0
wt lgkmcnt(2)
v_add_f32 v120, v120, v124
mm a[252:255],v[24:27],v[32:35],a[252:255]
rd v[24:27],v236 offset:34816
rd v[28:31],v237 offset:34816
s_add_u32 m0, s83, 20096
mm a[96:99],v[160:163],v[192:195],a[96:99]
ld v235,s[44:47],s80 offen lds
mm a[96:99],v[164:167],v[196:199],a[96:99]
s_mov_b32 s80, 3670016
v_fmac_f32 v123, v91, v91
mm a[104:107],v[164:167],v[204:207],a[104:107]
v_lshlrev_b32 v92, 16, v74
v_and_b32 v93, 0xffff0000, v74
mm a[104:107],v[160:163],v[200:203],a[104:107]
rt v[32:33],v240 offset:33920
rt v[34:35],v240 offset:34944
mm a[112:115],v[160:163],v[208:211],a[112:115]
v_sub_f32 v92, v92, v70
v_sub_f32 v93, v93, v70
mm a[112:115],v[164:167],v[212:215],a[112:115]
v_add_f32 v124, v92, v93
v_fmac_f32 v122, v92, v92
mm a[128:131],v[164:167],v[220:223],a[128:131]
wt lgkmcnt(2)
s_barrier
mm a[128:131],v[160:163],v[216:219],a[128:131]
v_add_f32 v120, v120, v124
v_fmac_f32 v123, v93, v93
mm a[132:135],v[168:171],v[216:219],a[132:135]
s_xor_b32 m0, s82, 32768
v_lshlrev_b32 v94, 16, v75
mm a[132:135],v[172:175],v[220:223],a[132:135]
ld v224,s[40:43],s79 offen lds
mm a[116:119],v[172:175],v[212:215],a[116:119]
v_and_b32 v95, 0xffff0000, v75
v_sub_f32 v94, v94, v70
mm a[116:119],v[168:171],v[208:211],a[116:119]
rt v[36:37],v240 offset:50816
rt v[38:39],v240 offset:51840
mm a[108:111],v[168:171],v[200:203],a[108:111]
s_xor_b32 m0, s82, 33792
v_sub_f32 v95, v95, v70
mm a[108:111],v[172:175],v[204:207],a[108:111]
ld v225,s[40:43],s79 offen lds
mm a[100:103],v[172:175],v[196:199],a[100:103]
v_add_f32 v124, v94, v95
.La5_LW1_5:
s_cbranch_execz .La5_LW1_9
v_fmac_f32 v122, v94, v94
mm a[100:103],v[168:171],v[192:195],a[100:103]
v_add_f32 v120, v120, v124
v_fmac_f32 v123, v95, v95
mm a[232:235],v[176:179],v[192:195],a[232:235]
s_xor_b32 m0, s82, 34816
v_lshlrev_b32 v96, 16, v76
mm a[232:235],v[180:183],v[196:199],a[232:235]
ld v226,s[40:43],s79 offen lds
mm a[152:155],v[180:183],v[204:207],a[152:155]
v_and_b32 v97, 0xffff0000, v76
v_sub_f32 v96, v96, v70
mm a[152:155],v[176:179],v[200:203],a[152:155]
rt v[40:41],v241 offset:33920
rt v[42:43],v241 offset:34944
mm a[208:211],v[176:179],v[208:211],a[208:211]
s_xor_b32 m0, s82, 35840
v_sub_f32 v97, v97, v70
mm a[208:211],v[180:183],v[212:215],a[208:211]
ld v227,s[40:43],s79 offen lds
mm a[160:163],v[180:183],v[220:223],a[160:163]
v_add_f32 v124, v96, v97
v_fmac_f32 v122, v96, v96
mm a[160:163],v[176:179],v[216:219],a[160:163]
v_add_f32 v120, v120, v124
v_fmac_f32 v123, v97, v97
mm a[164:167],v[184:187],v[216:219],a[164:167]
s_xor_b32 m0, s82, 36864
v_lshlrev_b32 v98, 16, v77
mm a[164:167],v[188:191],v[220:223],a[164:167]
ld v228,s[40:43],s79 offen lds
mm a[212:215],v[188:191],v[212:215],a[212:215]
v_and_b32 v99, 0xffff0000, v77
v_sub_f32 v98, v98, v70
mm a[212:215],v[184:187],v[208:211],a[212:215]
rt v[44:45],v241 offset:50816
rt v[46:47],v241 offset:51840
mm a[156:159],v[184:187],v[200:203],a[156:159]
s_xor_b32 m0, s82, 37888
v_sub_f32 v99, v99, v70
mm a[156:159],v[188:191],v[204:207],a[156:159]
ld v229,s[40:43],s79 offen lds
mm a[236:239],v[188:191],v[196:199],a[236:239]
v_add_f32 v124, v98, v99
v_fmac_f32 v122, v98, v98
mm a[236:239],v[184:187],v[192:195],a[236:239]
v_add_f32 v120, v120, v124
)ASM" R"ASM(v_fmac_f32 v123, v99, v99
mm a[200:203],v[0:3],v[192:195],a[200:203]
s_xor_b32 m0, s82, 38912
v_lshlrev_b32 v100, 16, v78
mm a[200:203],v[4:7],v[196:199],a[200:203]
ld v230,s[40:43],s79 offen lds
mm a[184:187],v[4:7],v[204:207],a[184:187]
v_and_b32 v101, 0xffff0000, v78
v_sub_f32 v100, v100, v70
mm a[184:187],v[0:3],v[200:203],a[184:187]
rt v[48:49],v242 offset:33920
rt v[50:51],v242 offset:34944
mm a[64:67],v[0:3],v[208:211],a[64:67]
s_xor_b32 m0, s82, 39936
v_sub_f32 v101, v101, v70
mm a[64:67],v[4:7],v[212:215],a[64:67]
ld v231,s[40:43],s79 offen lds
mm a[0:3],v[4:7],v[220:223],a[0:3]
s_mov_b32 s79, 0
v_add_f32 v124, v100, v101
mm a[0:3],v[0:3],v[216:219],a[0:3]
v_fmac_f32 v122, v100, v100
v_add_f32 v120, v120, v124
mm a[4:7],v[8:11],v[216:219],a[4:7]
v_fmac_f32 v123, v101, v101
v_lshlrev_b32 v102, 16, v79
mm a[4:7],v[12:15],v[220:223],a[4:7]
buffer_load_dword v254, v132, s[44:47], s86 offen
mm a[68:71],v[12:15],v[212:215],a[68:71]
s_add_u32 s86, s86, 2097152
s_and_b32 s86, s86, 4194303
mm a[68:71],v[8:11],v[208:211],a[68:71]
rt v[52:53],v242 offset:50816
rt v[54:55],v242 offset:51840
mm a[188:191],v[8:11],v[200:203],a[188:191]
v_and_b32 v103, 0xffff0000, v79
v_sub_f32 v102, v102, v70
mm a[188:191],v[12:15],v[204:207],a[188:191]
v_sub_f32 v103, v103, v70
v_add_f32 v124, v102, v103
mm a[204:207],v[12:15],v[196:199],a[204:207]
v_fmac_f32 v122, v102, v102
v_add_f32 v120, v120, v124
mm a[204:207],v[8:11],v[192:195],a[204:207]
rt v[56:57],v243 offset:33920
rt v[58:59],v243 offset:34944
mm a[88:91],v[16:19],v[192:195],a[88:91]
v_fmac_f32 v123, v103, v103
v_lshlrev_b32 v104, 16, v80
mm a[88:91],v[20:23],v[196:199],a[88:91]
v_and_b32 v105, 0xffff0000, v80
v_sub_f32 v104, v104, v70
mm a[24:27],v[20:23],v[204:207],a[24:27]
v_sub_f32 v105, v105, v70
v_add_f32 v124, v104, v105
mm a[24:27],v[16:19],v[200:203],a[24:27]
rt v[60:61],v243 offset:50816
rt v[62:63],v243 offset:51840
mm a[240:243],v[16:19],v[208:211],a[240:243]
v_fmac_f32 v122, v104, v104
v_add_f32 v120, v120, v124
mm a[240:243],v[20:23],v[212:215],a[240:243]
v_fmac_f32 v123, v105, v105
v_lshlrev_b32 v106, 16, v81
mm a[32:35],v[20:23],v[220:223],a[32:35]
v_and_b32 v107, 0xffff0000, v81
v_sub_f32 v106, v106, v70
mm a[32:35],v[16:19],v[216:219],a[32:35]
v_sub_f32 v107, v107, v70
v_add_f32 v124, v106, v107
mm a[36:39],v[24:27],v[216:219],a[36:39]
v_fmac_f32 v122, v106, v106
v_add_f32 v120, v120, v124
mm a[36:39],v[28:31],v[220:223],a[36:39]
v_fmac_f32 v123, v107, v107
v_lshlrev_b32 v108, 16, v82
mm a[244:247],v[28:31],v[212:215],a[244:247]
v_and_b32 v109, 0xffff0000, v82
v_sub_f32 v108, v108, v70
mm a[244:247],v[24:27],v[208:211],a[244:247]
v_sub_f32 v109, v109, v70
v_add_f32 v124, v108, v109
mm a[28:31],v[24:27],v[200:203],a[28:31]
v_fmac_f32 v122, v108, v108
v_add_f32 v120, v120, v124
mm a[28:31],v[28:31],v[204:207],a[28:31]
v_fmac_f32 v123, v109, v109
v_lshlrev_b32 v110, 16, v83
mm a[92:95],v[28:31],v[196:199],a[92:95]
wt vmcnt(9) lgkmcnt(0)
s_barrier
mm a[92:95],v[24:27],v[192:195],a[92:95]
v_and_b32 v111, 0xffff0000, v83
v_sub_f32 v110, v110, v70
mm a[120:123],v[160:163],v[32:35],a[120:123]
v_sub_f32 v111, v111, v70
v_add_f32 v124, v110, v111
mm a[120:123],v[164:167],v[36:39],a[120:123]
v_fmac_f32 v122, v110, v110
v_add_f32 v120, v120, v124
)ASM" R"ASM(mm a[136:139],v[164:167],v[44:47],a[136:139]
rt v[192:193],v240 offset:0
rt v[194:195],v240 offset:1024
mm a[136:139],v[160:163],v[40:43],a[136:139]
rt v[196:197],v240 offset:16896
rt v[198:199],v240 offset:17920
mm a[224:227],v[160:163],v[48:51],a[224:227]
v_fmac_f32 v123, v111, v111
v_lshlrev_b32 v112, 16, v84
mm a[224:227],v[164:167],v[52:55],a[224:227]
v_and_b32 v113, 0xffff0000, v84
v_sub_f32 v112, v112, v70
mm a[144:147],v[164:167],v[60:63],a[144:147]
v_sub_f32 v113, v113, v70
v_add_f32 v124, v112, v113
mm a[144:147],v[160:163],v[56:59],a[144:147]
rd v[160:163],v236 offset:24576
rd v[164:167],v237 offset:24576
mm a[148:151],v[168:171],v[56:59],a[148:151]
v_fmac_f32 v122, v112, v112
v_add_f32 v120, v120, v124
mm a[148:151],v[172:175],v[60:63],a[148:151]
v_fmac_f32 v123, v113, v113
v_lshlrev_b32 v114, 16, v85
mm a[228:231],v[172:175],v[52:55],a[228:231]
v_and_b32 v115, 0xffff0000, v85
v_sub_f32 v114, v114, v70
mm a[228:231],v[168:171],v[48:51],a[228:231]
rt v[200:201],v241 offset:0
rt v[202:203],v241 offset:1024
mm a[140:143],v[168:171],v[40:43],a[140:143]
v_sub_f32 v115, v115, v70
v_add_f32 v124, v114, v115
mm a[140:143],v[172:175],v[44:47],a[140:143]
v_fmac_f32 v122, v114, v114
v_add_f32 v120, v120, v124
mm a[124:127],v[172:175],v[36:39],a[124:127]
v_fmac_f32 v123, v115, v115
v_lshlrev_b32 v116, 16, v86
mm a[124:127],v[168:171],v[32:35],a[124:127]
rd v[168:171],v236 offset:26624
rd v[172:175],v237 offset:26624
mm a[216:219],v[176:179],v[32:35],a[216:219]
v_and_b32 v117, 0xffff0000, v86
v_sub_f32 v116, v116, v70
mm a[216:219],v[180:183],v[36:39],a[216:219]
v_sub_f32 v117, v117, v70
v_add_f32 v124, v116, v117
mm a[168:171],v[180:183],v[44:47],a[168:171]
v_fmac_f32 v122, v116, v116
v_add_f32 v120, v120, v124
mm a[168:171],v[176:179],v[40:43],a[168:171]
rt v[204:205],v241 offset:16896
rt v[206:207],v241 offset:17920
mm a[192:195],v[176:179],v[48:51],a[192:195]
v_fmac_f32 v123, v117, v117
v_lshlrev_b32 v118, 16, v87
mm a[192:195],v[180:183],v[52:55],a[192:195]
v_and_b32 v119, 0xffff0000, v87
v_sub_f32 v118, v118, v70
mm a[176:179],v[180:183],v[60:63],a[176:179]
v_sub_f32 v119, v119, v70
v_add_f32 v124, v118, v119
mm a[176:179],v[176:179],v[56:59],a[176:179]
rd v[176:179],v236 offset:16384
rd v[180:183],v237 offset:16384
mm a[180:183],v[184:187],v[56:59],a[180:183]
v_fmac_f32 v122, v118, v118
v_add_f32 v120, v120, v124
mm a[180:183],v[188:191],v[60:63],a[180:183]
v_fmac_f32 v123, v119, v119
v_add_f32 v121, v122, v123
mm a[196:199],v[188:191],v[52:55],a[196:199]
v_add_f32_dpp v120, v120, v120 row_ror:8 row_mask:0xf bank_mask:0xf
mm a[196:199],v[184:187],v[48:51],a[196:199]
rt v[208:209],v242 offset:0
rt v[210:211],v242 offset:1024
mm a[172:175],v[184:187],v[40:43],a[172:175]
s_add_u32 m0, s83, 33792
v_add_f32_dpp v121, v121, v121 row_ror:8 row_mask:0xf bank_mask:0xf
mm a[172:175],v[188:191],v[44:47],a[172:175]
ld v232,s[44:47],s80 offen lds
mm a[220:223],v[188:191],v[36:39],a[220:223]
v_mov_b32 v124, v120
v_mov_b32 v125, v121
mm a[220:223],v[184:187],v[32:35],a[220:223]
rd v[184:187],v236 offset:18432
rd v[188:191],v237 offset:18432
mm a[72:75],v[0:3],v[32:35],a[72:75]
s_add_u32 m0, s83, 34816
v_permlane16_swap_b32 v124, v120
mm a[72:75],v[4:7],v[36:39],a[72:75]
ld v233,s[44:47],s80 offen lds
mm a[8:11],v[4:7],v[44:47],a[8:11]
v_permlane16_swap_b32 v125, v121
mm a[8:11],v[0:3],v[40:43],a[8:11]
rt v[212:213],v242 offset:16896
rt v[214:215],v242 offset:17920
mm a[80:83],v[0:3],v[48:51],a[80:83]
s_add_u32 m0, s83, 35968
)ASM" R"ASM(v_add_f32 v120, v120, v124
mm a[80:83],v[4:7],v[52:55],a[80:83]
ld v234,s[44:47],s80 offen lds
mm a[16:19],v[4:7],v[60:63],a[16:19]
v_add_f32 v121, v121, v125
v_mov_b32 v124, v120
mm a[16:19],v[0:3],v[56:59],a[16:19]
rd v[0:3],v236 offset:8192
rd v[4:7],v237 offset:8192
mm a[20:23],v[8:11],v[56:59],a[20:23]
s_add_u32 m0, s83, 36992
v_mov_b32 v125, v121
mm a[20:23],v[12:15],v[60:63],a[20:23]
ld v235,s[44:47],s80 offen lds
mm a[84:87],v[12:15],v[52:55],a[84:87]
s_mov_b32 s80, 3932160
v_permlane32_swap_b32 v124, v120
mm a[84:87],v[8:11],v[48:51],a[84:87]
rt v[216:217],v243 offset:0
rt v[218:219],v243 offset:1024
mm a[12:15],v[8:11],v[40:43],a[12:15]
s_add_u32 m0, s83, 50688
v_permlane32_swap_b32 v125, v121
mm a[12:15],v[12:15],v[44:47],a[12:15]
ld v232,s[44:47],s80 offen lds
mm a[76:79],v[12:15],v[36:39],a[76:79]
v_add_f32 v120, v120, v124
v_add_f32 v121, v121, v125
mm a[76:79],v[8:11],v[32:35],a[76:79]
rd v[8:11],v236 offset:10240
rd v[12:15],v237 offset:10240
mm a[248:251],v[16:19],v[32:35],a[248:251]
s_add_u32 m0, s83, 51712
ds_write_b64 v127, v[120:121]
mm a[248:251],v[20:23],v[36:39],a[248:251]
ld v233,s[44:47],s80 offen lds
mm a[40:43],v[20:23],v[44:47],a[40:43]
s_nop 0
v_readfirstlane_b32 s92, v252
mm a[40:43],v[16:19],v[40:43],a[40:43]
rt v[220:221],v243 offset:16896
rt v[222:223],v243 offset:17920
mm a[48:51],v[16:19],v[48:51],a[48:51]
s_add_u32 m0, s83, 52864
v_readfirstlane_b32 s93, v253
mm a[48:51],v[20:23],v[52:55],a[48:51]
ld v234,s[44:47],s80 offen lds
mm a[56:59],v[20:23],v[60:63],a[56:59]
s_add_u32 s92, s92, 1
s_addc_u32 s93, s93, 0
mm a[56:59],v[16:19],v[56:59],a[56:59]
rd v[16:19],v236 offset:0
rd v[20:23],v237 offset:0
mm a[60:63],v[24:27],v[56:59],a[60:63]
s_add_u32 m0, s83, 53888
v_mov_b32 v252, s92
mm a[60:63],v[28:31],v[60:63],a[60:63]
ld v235,s[44:47],s80 offen lds
mm a[52:55],v[28:31],v[52:55],a[52:55]
s_mov_b32 s80, 0
v_mov_b32 v253, s93
mm a[52:55],v[24:27],v[48:51],a[52:55]
v_mov_b32 v128, 0
v_mov_b32 v129, 0
mm a[44:47],v[24:27],v[40:43],a[44:47]
v_mov_b32 v130, 0
v_mov_b32 v131, 0
mm a[44:47],v[28:31],v[44:47],a[44:47]
st v[128:131],v255,s[52:55],s95 offen sc1
mm a[252:255],v[28:31],v[36:39],a[252:255]
wt lgkmcnt(2)
s_lshl_b32 s92, s76, 5
mm a[252:255],v[24:27],v[32:35],a[252:255]
rd v[24:27],v236 offset:2048
rd v[28:31],v237 offset:2048
mm a[96:99],v[160:163],v[192:195],a[96:99]
s_add_u32 s92, s92, s72
s_lshl2_add_u32 s92, s92, s3
mm a[96:99],v[164:167],v[196:199],a[96:99]
s_lshl_b32 s92, s92, 10
s_add_u32 s92, s92, 1048576
mm a[104:107],v[164:167],v[204:207],a[104:107]
v_mov_b32 v128, -1
v_mov_b32 v129, -1
mm a[104:107],v[160:163],v[200:203],a[104:107]
rt v[32:33],v240 offset:128
rt v[34:35],v240 offset:1152
mm a[112:115],v[160:163],v[208:211],a[112:115]
v_mov_b32 v130, -1
v_mov_b32 v131, -1
mm a[112:115],v[164:167],v[212:215],a[112:115]
st v[128:131],v255,s[52:55],s92 offen sc1
mm a[128:131],v[164:167],v[220:223],a[128:131]
wt lgkmcnt(2)
s_barrier
mm a[128:131],v[160:163],v[216:219],a[128:131]
rd v[72:75],v126
rd v[76:79],v126 offset:16
mm a[132:135],v[168:171],v[216:219],a[132:135]
s_xor_b32 m0, s82, 0
)ASM" R"ASM(mm a[132:135],v[172:175],v[220:223],a[132:135]
ld v224,s[40:43],s79 offen lds
mm a[116:119],v[172:175],v[212:215],a[116:119]
mm a[116:119],v[168:171],v[208:211],a[116:119]
rt v[36:37],v240 offset:17024
rt v[38:39],v240 offset:18048
mm a[108:111],v[168:171],v[200:203],a[108:111]
s_xor_b32 m0, s82, 1024
mm a[108:111],v[172:175],v[204:207],a[108:111]
ld v225,s[40:43],s79 offen lds
mm a[100:103],v[172:175],v[196:199],a[100:103]
wt lgkmcnt(2)
v_add_f32 v72, v72, v74
mm a[100:103],v[168:171],v[192:195],a[100:103]
v_add_f32 v73, v73, v75
v_add_f32 v76, v76, v78
mm a[232:235],v[176:179],v[192:195],a[232:235]
s_xor_b32 m0, s82, 2048
v_add_f32 v77, v77, v79
mm a[232:235],v[180:183],v[196:199],a[232:235]
ld v226,s[40:43],s79 offen lds
mm a[152:155],v[180:183],v[204:207],a[152:155]
v_add_f32 v72, v72, v76
v_add_f32 v73, v73, v77
mm a[152:155],v[176:179],v[200:203],a[152:155]
rt v[40:41],v241 offset:128
rt v[42:43],v241 offset:1152
mm a[208:211],v[176:179],v[208:211],a[208:211]
s_xor_b32 m0, s82, 3072
v_mul_f32 v72, 0x3a800000, v72
mm a[208:211],v[180:183],v[212:215],a[208:211]
ld v227,s[40:43],s79 offen lds
mm a[160:163],v[180:183],v[220:223],a[160:163]
v_mul_f32 v73, 0x3a800000, v73
v_add_f32 v124, v70, v72
mm a[160:163],v[176:179],v[216:219],a[160:163]
v_fma_f32 v125, -v72, v72, v73
cv v124,v124,v124
mm a[164:167],v[184:187],v[216:219],a[164:167]
s_xor_b32 m0, s82, 4096
v_lshlrev_b32 v124, 16, v124
mm a[164:167],v[188:191],v[220:223],a[164:167]
ld v228,s[40:43],s79 offen lds
mm a[212:215],v[188:191],v[212:215],a[212:215]
v_max_f32 v125, 0, v125
cv v125,v125,v125
mm a[212:215],v[184:187],v[208:211],a[212:215]
rt v[44:45],v241 offset:17024
rt v[46:47],v241 offset:18048
mm a[156:159],v[184:187],v[200:203],a[156:159]
s_xor_b32 m0, s82, 5120
v_lshlrev_b32 v125, 16, v125
mm a[156:159],v[188:191],v[204:207],a[156:159]
ld v229,s[40:43],s79 offen lds
mm a[236:239],v[188:191],v[196:199],a[236:239]
v_add_f32 v125, 0x3727c5ac, v125
cv v125,v125,v125
mm a[236:239],v[184:187],v[192:195],a[236:239]
v_lshlrev_b32 v125, 16, v125
mm a[200:203],v[0:3],v[192:195],a[200:203]
s_xor_b32 m0, s82, 6144
mm a[200:203],v[4:7],v[196:199],a[200:203]
ld v230,s[40:43],s79 offen lds
mm a[184:187],v[4:7],v[204:207],a[184:187]
v_sqrt_f32 v125, v125
mm a[184:187],v[0:3],v[200:203],a[184:187]
rt v[48:49],v242 offset:128
rt v[50:51],v242 offset:1152
mm a[64:67],v[0:3],v[208:211],a[64:67]
s_xor_b32 m0, s82, 7168
v_sub_f32 v124, v70, v124
mm a[64:67],v[4:7],v[212:215],a[64:67]
ld v231,s[40:43],s79 offen lds
mm a[0:3],v[4:7],v[220:223],a[0:3]
s_mov_b32 s79, 128
cv v125,v125,v125
mm a[0:3],v[0:3],v[216:219],a[0:3]
v_lshlrev_b32 v125, 16, v125
mm a[4:7],v[8:11],v[216:219],a[4:7]
v_rcp_f32 v125, v125
mm a[4:7],v[12:15],v[220:223],a[4:7]
v_mul_f32 v125, 0x3e0293ee, v125
v_mul_f32 v124, v124, v125
mm a[68:71],v[12:15],v[212:215],a[68:71]
v_fma_f32 v88, v88, v125, v124
v_fma_f32 v89, v89, v125, v124
mm a[68:71],v[8:11],v[208:211],a[68:71]
rt v[52:53],v242 offset:17024
rt v[54:55],v242 offset:18048
mm a[188:191],v[8:11],v[200:203],a[188:191]
cv v72,v88,v89
v_fma_f32 v90, v90, v125, v124
mm a[188:191],v[12:15],v[204:207],a[188:191]
v_fma_f32 v91, v91, v125, v124
cv v73,v90,v91
mm a[204:207],v[12:15],v[196:199],a[204:207]
v_fma_f32 v92, v92, v125, v124
v_fma_f32 v93, v93, v125, v124
mm a[204:207],v[8:11],v[192:195],a[204:207]
rt v[56:57],v243 offset:128
)ASM" R"ASM(rt v[58:59],v243 offset:1152
mm a[88:91],v[16:19],v[192:195],a[88:91]
cv v74,v92,v93
v_fma_f32 v94, v94, v125, v124
mm a[88:91],v[20:23],v[196:199],a[88:91]
v_fma_f32 v95, v95, v125, v124
cv v75,v94,v95
mm a[24:27],v[20:23],v[204:207],a[24:27]
st v[72:75],v71,s[56:59],0 offen offset:0 sc1
mm a[24:27],v[16:19],v[200:203],a[24:27]
rt v[60:61],v243 offset:17024
rt v[62:63],v243 offset:18048
mm a[240:243],v[16:19],v[208:211],a[240:243]
v_fma_f32 v96, v96, v125, v124
v_fma_f32 v97, v97, v125, v124
mm a[240:243],v[20:23],v[212:215],a[240:243]
cv v76,v96,v97
v_fma_f32 v98, v98, v125, v124
mm a[32:35],v[20:23],v[220:223],a[32:35]
v_fma_f32 v99, v99, v125, v124
cv v77,v98,v99
mm a[32:35],v[16:19],v[216:219],a[32:35]
v_fma_f32 v100, v100, v125, v124
v_fma_f32 v101, v101, v125, v124
mm a[36:39],v[24:27],v[216:219],a[36:39]
cv v78,v100,v101
v_fma_f32 v102, v102, v125, v124
mm a[36:39],v[28:31],v[220:223],a[36:39]
v_fma_f32 v103, v103, v125, v124
cv v79,v102,v103
mm a[244:247],v[28:31],v[212:215],a[244:247]
st v[76:79],v71,s[56:59],0 offen offset:2048 sc1
mm a[244:247],v[24:27],v[208:211],a[244:247]
v_fma_f32 v104, v104, v125, v124
v_fma_f32 v105, v105, v125, v124
mm a[28:31],v[24:27],v[200:203],a[28:31]
cv v80,v104,v105
v_fma_f32 v106, v106, v125, v124
mm a[28:31],v[28:31],v[204:207],a[28:31]
v_fma_f32 v107, v107, v125, v124
cv v81,v106,v107
mm a[92:95],v[28:31],v[196:199],a[92:95]
wt vmcnt(12) lgkmcnt(0)
s_barrier
mm a[92:95],v[24:27],v[192:195],a[92:95]
v_fma_f32 v108, v108, v125, v124
v_fma_f32 v109, v109, v125, v124
mm a[120:123],v[160:163],v[32:35],a[120:123]
cv v82,v108,v109
v_fma_f32 v110, v110, v125, v124
mm a[120:123],v[164:167],v[36:39],a[120:123]
v_fma_f32 v111, v111, v125, v124
cv v83,v110,v111
mm a[136:139],v[164:167],v[44:47],a[136:139]
rt v[192:193],v240 offset:33792
rt v[194:195],v240 offset:34816
mm a[136:139],v[160:163],v[40:43],a[136:139]
rt v[196:197],v240 offset:50688
rt v[198:199],v240 offset:51712
mm a[224:227],v[160:163],v[48:51],a[224:227]
st v[80:83],v71,s[56:59],s88 offen offset:0 sc1
mm a[224:227],v[164:167],v[52:55],a[224:227]
v_fma_f32 v112, v112, v125, v124
v_fma_f32 v113, v113, v125, v124
mm a[144:147],v[164:167],v[60:63],a[144:147]
cv v84,v112,v113
v_fma_f32 v114, v114, v125, v124
mm a[144:147],v[160:163],v[56:59],a[144:147]
rd v[160:163],v236 offset:57344
rd v[164:167],v237 offset:57344
mm a[148:151],v[168:171],v[56:59],a[148:151]
v_fma_f32 v115, v115, v125, v124
cv v85,v114,v115
mm a[148:151],v[172:175],v[60:63],a[148:151]
v_fma_f32 v116, v116, v125, v124
v_fma_f32 v117, v117, v125, v124
mm a[228:231],v[172:175],v[52:55],a[228:231]
cv v86,v116,v117
v_fma_f32 v118, v118, v125, v124
mm a[228:231],v[168:171],v[48:51],a[228:231]
rt v[200:201],v241 offset:33792
rt v[202:203],v241 offset:34816
mm a[140:143],v[168:171],v[40:43],a[140:143]
v_fma_f32 v119, v119, v125, v124
cv v87,v118,v119
mm a[140:143],v[172:175],v[44:47],a[140:143]
st v[84:87],v71,s[56:59],s88 offen offset:2048 sc1
mm a[124:127],v[172:175],v[36:39],a[124:127]
mm a[124:127],v[168:171],v[32:35],a[124:127]
rd v[168:171],v236 offset:59392
rd v[172:175],v237 offset:59392
mm a[216:219],v[176:179],v[32:35],a[216:219]
mm a[216:219],v[180:183],v[36:39],a[216:219]
mm a[168:171],v[180:183],v[44:47],a[168:171]
mm a[168:171],v[176:179],v[40:43],a[168:171]
rt v[204:205],v241 offset:50688
rt v[206:207],v241 offset:51712
mm a[192:195],v[176:179],v[48:51],a[192:195]
mm a[192:195],v[180:183],v[52:55],a[192:195]
mm a[176:179],v[180:183],v[60:63],a[176:179]
)ASM" R"ASM(mm a[176:179],v[176:179],v[56:59],a[176:179]
rd v[176:179],v236 offset:49152
rd v[180:183],v237 offset:49152
mm a[180:183],v[184:187],v[56:59],a[180:183]
mm a[180:183],v[188:191],v[60:63],a[180:183]
mm a[196:199],v[188:191],v[52:55],a[196:199]
mm a[196:199],v[184:187],v[48:51],a[196:199]
rt v[208:209],v242 offset:33792
rt v[210:211],v242 offset:34816
mm a[172:175],v[184:187],v[40:43],a[172:175]
s_add_u32 m0, s83, 0
mm a[172:175],v[188:191],v[44:47],a[172:175]
ld v232,s[44:47],s80 offen lds
mm a[220:223],v[188:191],v[36:39],a[220:223]
mm a[220:223],v[184:187],v[32:35],a[220:223]
rd v[184:187],v236 offset:51200
rd v[188:191],v237 offset:51200
mm a[72:75],v[0:3],v[32:35],a[72:75]
s_add_u32 m0, s83, 1024
mm a[72:75],v[4:7],v[36:39],a[72:75]
ld v233,s[44:47],s80 offen lds
mm a[8:11],v[4:7],v[44:47],a[8:11]
mm a[8:11],v[0:3],v[40:43],a[8:11]
rt v[212:213],v242 offset:50688
rt v[214:215],v242 offset:51712
mm a[80:83],v[0:3],v[48:51],a[80:83]
s_add_u32 m0, s83, 2176
mm a[80:83],v[4:7],v[52:55],a[80:83]
ld v234,s[44:47],s80 offen lds
mm a[16:19],v[4:7],v[60:63],a[16:19]
mm a[16:19],v[0:3],v[56:59],a[16:19]
rd v[0:3],v236 offset:40960
rd v[4:7],v237 offset:40960
mm a[20:23],v[8:11],v[56:59],a[20:23]
s_add_u32 m0, s83, 3200
mm a[20:23],v[12:15],v[60:63],a[20:23]
ld v235,s[44:47],s80 offen lds
mm a[84:87],v[12:15],v[52:55],a[84:87]
.La5_LW1_6:
s_cbranch_execz .La5_LW1_10
s_mov_b32 s80, 262144
mm a[84:87],v[8:11],v[48:51],a[84:87]
rt v[216:217],v243 offset:33792
rt v[218:219],v243 offset:34816
mm a[12:15],v[8:11],v[40:43],a[12:15]
s_add_u32 m0, s83, 16896
mm a[12:15],v[12:15],v[44:47],a[12:15]
ld v232,s[44:47],s80 offen lds
mm a[76:79],v[12:15],v[36:39],a[76:79]
mm a[76:79],v[8:11],v[32:35],a[76:79]
rd v[8:11],v236 offset:43008
rd v[12:15],v237 offset:43008
mm a[248:251],v[16:19],v[32:35],a[248:251]
s_add_u32 m0, s83, 17920
mm a[248:251],v[20:23],v[36:39],a[248:251]
ld v233,s[44:47],s80 offen lds
mm a[40:43],v[20:23],v[44:47],a[40:43]
mm a[40:43],v[16:19],v[40:43],a[40:43]
rt v[220:221],v243 offset:50688
rt v[222:223],v243 offset:51712
mm a[48:51],v[16:19],v[48:51],a[48:51]
s_add_u32 m0, s83, 19072
mm a[48:51],v[20:23],v[52:55],a[48:51]
ld v234,s[44:47],s80 offen lds
mm a[56:59],v[20:23],v[60:63],a[56:59]
mm a[56:59],v[16:19],v[56:59],a[56:59]
rd v[16:19],v236 offset:32768
rd v[20:23],v237 offset:32768
mm a[60:63],v[24:27],v[56:59],a[60:63]
s_add_u32 m0, s83, 20096
mm a[60:63],v[28:31],v[60:63],a[60:63]
ld v235,s[44:47],s80 offen lds
mm a[52:55],v[28:31],v[52:55],a[52:55]
s_mov_b32 s80, 524288
mm a[52:55],v[24:27],v[48:51],a[52:55]
mm a[44:47],v[24:27],v[40:43],a[44:47]
mm a[44:47],v[28:31],v[44:47],a[44:47]
mm a[252:255],v[28:31],v[36:39],a[252:255]
wt lgkmcnt(2)
mm a[252:255],v[24:27],v[32:35],a[252:255]
rd v[24:27],v236 offset:34816
rd v[28:31],v237 offset:34816
mm a[96:99],v[160:163],v[192:195],a[96:99]
mm a[96:99],v[164:167],v[196:199],a[96:99]
mm a[104:107],v[164:167],v[204:207],a[104:107]
mm a[104:107],v[160:163],v[200:203],a[104:107]
rt v[32:33],v240 offset:33920
rt v[34:35],v240 offset:34944
mm a[112:115],v[160:163],v[208:211],a[112:115]
mm a[112:115],v[164:167],v[212:215],a[112:115]
mm a[128:131],v[164:167],v[220:223],a[128:131]
wt lgkmcnt(2)
s_barrier
mm a[128:131],v[160:163],v[216:219],a[128:131]
mm a[132:135],v[168:171],v[216:219],a[132:135]
s_xor_b32 m0, s82, 32768
mm a[132:135],v[172:175],v[220:223],a[132:135]
ld v224,s[40:43],s79 offen lds
mm a[116:119],v[172:175],v[212:215],a[116:119]
mm a[116:119],v[168:171],v[208:211],a[116:119]
)ASM" R"ASM(rt v[36:37],v240 offset:50816
rt v[38:39],v240 offset:51840
mm a[108:111],v[168:171],v[200:203],a[108:111]
s_xor_b32 m0, s82, 33792
mm a[108:111],v[172:175],v[204:207],a[108:111]
ld v225,s[40:43],s79 offen lds
mm a[100:103],v[172:175],v[196:199],a[100:103]
mm a[100:103],v[168:171],v[192:195],a[100:103]
mm a[232:235],v[176:179],v[192:195],a[232:235]
s_xor_b32 m0, s82, 34816
mm a[232:235],v[180:183],v[196:199],a[232:235]
ld v226,s[40:43],s79 offen lds
mm a[152:155],v[180:183],v[204:207],a[152:155]
mm a[152:155],v[176:179],v[200:203],a[152:155]
rt v[40:41],v241 offset:33920
rt v[42:43],v241 offset:34944
mm a[208:211],v[176:179],v[208:211],a[208:211]
s_xor_b32 m0, s82, 35840
mm a[208:211],v[180:183],v[212:215],a[208:211]
ld v227,s[40:43],s79 offen lds
mm a[160:163],v[180:183],v[220:223],a[160:163]
mm a[160:163],v[176:179],v[216:219],a[160:163]
mm a[164:167],v[184:187],v[216:219],a[164:167]
s_xor_b32 m0, s82, 36864
mm a[164:167],v[188:191],v[220:223],a[164:167]
ld v228,s[40:43],s79 offen lds
mm a[212:215],v[188:191],v[212:215],a[212:215]
mm a[212:215],v[184:187],v[208:211],a[212:215]
rt v[44:45],v241 offset:50816
rt v[46:47],v241 offset:51840
mm a[156:159],v[184:187],v[200:203],a[156:159]
s_xor_b32 m0, s82, 37888
mm a[156:159],v[188:191],v[204:207],a[156:159]
ld v229,s[40:43],s79 offen lds
mm a[236:239],v[188:191],v[196:199],a[236:239]
mm a[236:239],v[184:187],v[192:195],a[236:239]
mm a[200:203],v[0:3],v[192:195],a[200:203]
s_xor_b32 m0, s82, 38912
mm a[200:203],v[4:7],v[196:199],a[200:203]
ld v230,s[40:43],s79 offen lds
mm a[184:187],v[4:7],v[204:207],a[184:187]
mm a[184:187],v[0:3],v[200:203],a[184:187]
rt v[48:49],v242 offset:33920
rt v[50:51],v242 offset:34944
mm a[64:67],v[0:3],v[208:211],a[64:67]
s_xor_b32 m0, s82, 39936
mm a[64:67],v[4:7],v[212:215],a[64:67]
ld v231,s[40:43],s79 offen lds
mm a[0:3],v[4:7],v[220:223],a[0:3]
s_mov_b32 s79, 256
mm a[0:3],v[0:3],v[216:219],a[0:3]
mm a[4:7],v[8:11],v[216:219],a[4:7]
mm a[4:7],v[12:15],v[220:223],a[4:7]
buffer_load_dword v254, v135, s[68:71], s96 offen
mm a[68:71],v[12:15],v[212:215],a[68:71]
s_add_u32 s96, s96, 1048576
mm a[68:71],v[8:11],v[208:211],a[68:71]
rt v[52:53],v242 offset:50816
rt v[54:55],v242 offset:51840
mm a[188:191],v[8:11],v[200:203],a[188:191]
mm a[188:191],v[12:15],v[204:207],a[188:191]
buffer_load_dword v254, v135, s[68:71], s96 offen
mm a[204:207],v[12:15],v[196:199],a[204:207]
s_add_u32 s96, s96, 1048576
mm a[204:207],v[8:11],v[192:195],a[204:207]
rt v[56:57],v243 offset:33920
rt v[58:59],v243 offset:34944
mm a[88:91],v[16:19],v[192:195],a[88:91]
mm a[88:91],v[20:23],v[196:199],a[88:91]
wt vmcnt(18)
buffer_store_dwordx2 v[252:253], off, s[64:67], s91 offset:1024 sc1
mm a[24:27],v[20:23],v[204:207],a[24:27]
s_mov_b32 s91, 0
s_lshl_b32 s92, s76, 19
mm a[24:27],v[16:19],v[200:203],a[24:27]
rt v[60:61],v243 offset:50816
rt v[62:63],v243 offset:51840
mm a[240:243],v[16:19],v[208:211],a[240:243]
s_add_u32 s92, s92, 88080384
mm a[240:243],v[20:23],v[212:215],a[240:243]
s_add_u32 s52, s26, s92
s_addc_u32 s53, s27, 0
mm a[32:35],v[20:23],v[220:223],a[32:35]
s_and_b32 s53, s53, 0xffff
s_mov_b32 s54, 524672
mm a[32:35],v[16:19],v[216:219],a[32:35]
s_mov_b32 s55, 0x20000
s_lshl_b32 s92, s3, 16
mm a[36:39],v[24:27],v[216:219],a[36:39]
s_sub_u32 s93, 458752, s92
v_add_u32 v244, s92, v136
mm a[36:39],v[28:31],v[220:223],a[36:39]
v_add_u32 v246, s93, v136
v_add_u32 v245, 0x8000, v244
mm a[244:247],v[28:31],v[212:215],a[244:247]
v_add_u32 v247, 0x8000, v246
s_lshl_b32 s94, s72, 19
mm a[244:247],v[24:27],v[208:211],a[244:247]
s_lshl_b32 s93, s76, 16
s_add_u32 s92, s94, s93
)ASM" R"ASM(mm a[28:31],v[24:27],v[200:203],a[28:31]
s_lshl_b32 s93, s77, 13
s_add_u32 s92, s92, s93
mm a[28:31],v[28:31],v[204:207],a[28:31]
s_add_u32 s92, s92, 37748736
mm a[92:95],v[28:31],v[196:199],a[92:95]
wt vmcnt(11) lgkmcnt(0)
s_barrier
mm a[92:95],v[24:27],v[192:195],a[92:95]
s_add_u32 s48, s26, s92
s_addc_u32 s49, s27, 0
mm a[120:123],v[160:163],v[32:35],a[120:123]
s_and_b32 s49, s49, 0xffff
s_mov_b32 s50, 57344
mm a[120:123],v[164:167],v[36:39],a[120:123]
s_mov_b32 s51, 0x20000
s_lshl_b32 s93, s77, 24
mm a[136:139],v[164:167],v[44:47],a[136:139]
rt v[192:193],v240 offset:0
rt v[194:195],v240 offset:1024
mm a[136:139],v[160:163],v[40:43],a[136:139]
rt v[196:197],v240 offset:16896
rt v[198:199],v240 offset:17920
mm a[224:227],v[160:163],v[48:51],a[224:227]
s_add_u32 s92, s94, s93
s_add_u32 s92, s92, 20975616
mm a[224:227],v[164:167],v[52:55],a[224:227]
s_add_u32 s60, s26, s92
s_addc_u32 s61, s27, 0
mm a[144:147],v[164:167],v[60:63],a[144:147]
s_and_b32 s61, s61, 0xffff
s_mov_b32 s62, 520192
mm a[144:147],v[160:163],v[56:59],a[144:147]
rd v[160:163],v236 offset:24576
rd v[164:167],v237 offset:24576
mm a[148:151],v[168:171],v[56:59],a[148:151]
s_mov_b32 s63, 0x20000
s_lshl_b32 s92, s77, 14
mm a[148:151],v[172:175],v[60:63],a[148:151]
s_sub_u32 s95, 24576, s92
v_add_u32 v238, s95, v255
mm a[228:231],v[172:175],v[52:55],a[228:231]
mm a[228:231],v[168:171],v[48:51],a[228:231]
rt v[200:201],v241 offset:0
rt v[202:203],v241 offset:1024
mm a[140:143],v[168:171],v[40:43],a[140:143]
mm a[140:143],v[172:175],v[44:47],a[140:143]
mm a[124:127],v[172:175],v[36:39],a[124:127]
mm a[124:127],v[168:171],v[32:35],a[124:127]
rd v[168:171],v236 offset:26624
rd v[172:175],v237 offset:26624
mm a[216:219],v[176:179],v[32:35],a[216:219]
mm a[216:219],v[180:183],v[36:39],a[216:219]
mm a[168:171],v[180:183],v[44:47],a[168:171]
mm a[168:171],v[176:179],v[40:43],a[168:171]
rt v[204:205],v241 offset:16896
rt v[206:207],v241 offset:17920
mm a[192:195],v[176:179],v[48:51],a[192:195]
mm a[192:195],v[180:183],v[52:55],a[192:195]
mm a[176:179],v[180:183],v[60:63],a[176:179]
mm a[176:179],v[176:179],v[56:59],a[176:179]
rd v[176:179],v236 offset:16384
rd v[180:183],v237 offset:16384
mm a[180:183],v[184:187],v[56:59],a[180:183]
mm a[180:183],v[188:191],v[60:63],a[180:183]
mm a[196:199],v[188:191],v[52:55],a[196:199]
mm a[196:199],v[184:187],v[48:51],a[196:199]
rt v[208:209],v242 offset:0
rt v[210:211],v242 offset:1024
mm a[172:175],v[184:187],v[40:43],a[172:175]
s_add_u32 m0, s83, 33792
mm a[172:175],v[188:191],v[44:47],a[172:175]
ld v232,s[44:47],s80 offen lds
mm a[220:223],v[188:191],v[36:39],a[220:223]
mm a[220:223],v[184:187],v[32:35],a[220:223]
rd v[184:187],v236 offset:18432
rd v[188:191],v237 offset:18432
mm a[72:75],v[0:3],v[32:35],a[72:75]
s_add_u32 m0, s83, 34816
mm a[72:75],v[4:7],v[36:39],a[72:75]
ld v233,s[44:47],s80 offen lds
mm a[8:11],v[4:7],v[44:47],a[8:11]
mm a[8:11],v[0:3],v[40:43],a[8:11]
rt v[212:213],v242 offset:16896
rt v[214:215],v242 offset:17920
mm a[80:83],v[0:3],v[48:51],a[80:83]
s_add_u32 m0, s83, 35968
mm a[80:83],v[4:7],v[52:55],a[80:83]
ld v234,s[44:47],s80 offen lds
mm a[16:19],v[4:7],v[60:63],a[16:19]
mm a[16:19],v[0:3],v[56:59],a[16:19]
rd v[0:3],v236 offset:8192
rd v[4:7],v237 offset:8192
mm a[20:23],v[8:11],v[56:59],a[20:23]
s_add_u32 m0, s83, 36992
mm a[20:23],v[12:15],v[60:63],a[20:23]
ld v235,s[44:47],s80 offen lds
mm a[84:87],v[12:15],v[52:55],a[84:87]
s_mov_b32 s80, 786432
mm a[84:87],v[8:11],v[48:51],a[84:87]
)ASM" R"ASM(rt v[216:217],v243 offset:0
rt v[218:219],v243 offset:1024
mm a[12:15],v[8:11],v[40:43],a[12:15]
s_add_u32 m0, s83, 50688
mm a[12:15],v[12:15],v[44:47],a[12:15]
ld v232,s[44:47],s80 offen lds
mm a[76:79],v[12:15],v[36:39],a[76:79]
mm a[76:79],v[8:11],v[32:35],a[76:79]
rd v[8:11],v236 offset:10240
rd v[12:15],v237 offset:10240
mm a[248:251],v[16:19],v[32:35],a[248:251]
s_add_u32 m0, s83, 51712
mm a[248:251],v[20:23],v[36:39],a[248:251]
ld v233,s[44:47],s80 offen lds
mm a[40:43],v[20:23],v[44:47],a[40:43]
mm a[40:43],v[16:19],v[40:43],a[40:43]
rt v[220:221],v243 offset:16896
rt v[222:223],v243 offset:17920
mm a[48:51],v[16:19],v[48:51],a[48:51]
s_add_u32 m0, s83, 52864
mm a[48:51],v[20:23],v[52:55],a[48:51]
ld v234,s[44:47],s80 offen lds
mm a[56:59],v[20:23],v[60:63],a[56:59]
mm a[56:59],v[16:19],v[56:59],a[56:59]
rd v[16:19],v236 offset:0
rd v[20:23],v237 offset:0
mm a[60:63],v[24:27],v[56:59],a[60:63]
s_add_u32 m0, s83, 53888
mm a[60:63],v[28:31],v[60:63],a[60:63]
ld v235,s[44:47],s80 offen lds
mm a[52:55],v[28:31],v[52:55],a[52:55]
s_mov_b32 s80, 1048576
mm a[52:55],v[24:27],v[48:51],a[52:55]
mm a[44:47],v[24:27],v[40:43],a[44:47]
mm a[44:47],v[28:31],v[44:47],a[44:47]
mm a[252:255],v[28:31],v[36:39],a[252:255]
wt lgkmcnt(2)
mm a[252:255],v[24:27],v[32:35],a[252:255]
rd v[24:27],v236 offset:2048
rd v[28:31],v237 offset:2048
mm a[96:99],v[160:163],v[192:195],a[96:99]
mm a[96:99],v[164:167],v[196:199],a[96:99]
mm a[104:107],v[164:167],v[204:207],a[104:107]
mm a[104:107],v[160:163],v[200:203],a[104:107]
rt v[32:33],v240 offset:128
rt v[34:35],v240 offset:1152
mm a[112:115],v[160:163],v[208:211],a[112:115]
mm a[112:115],v[164:167],v[212:215],a[112:115]
mm a[128:131],v[164:167],v[220:223],a[128:131]
wt lgkmcnt(2)
s_barrier
mm a[128:131],v[160:163],v[216:219],a[128:131]
mm a[132:135],v[168:171],v[216:219],a[132:135]
s_xor_b32 m0, s82, 0
mm a[132:135],v[172:175],v[220:223],a[132:135]
ld v224,s[40:43],s79 offen lds
mm a[116:119],v[172:175],v[212:215],a[116:119]
mm a[116:119],v[168:171],v[208:211],a[116:119]
rt v[36:37],v240 offset:17024
rt v[38:39],v240 offset:18048
mm a[108:111],v[168:171],v[200:203],a[108:111]
s_xor_b32 m0, s82, 1024
mm a[108:111],v[172:175],v[204:207],a[108:111]
ld v225,s[40:43],s79 offen lds
mm a[100:103],v[172:175],v[196:199],a[100:103]
mm a[100:103],v[168:171],v[192:195],a[100:103]
mm a[232:235],v[176:179],v[192:195],a[232:235]
s_xor_b32 m0, s82, 2048
mm a[232:235],v[180:183],v[196:199],a[232:235]
ld v226,s[40:43],s79 offen lds
mm a[152:155],v[180:183],v[204:207],a[152:155]
mm a[152:155],v[176:179],v[200:203],a[152:155]
rt v[40:41],v241 offset:128
rt v[42:43],v241 offset:1152
mm a[208:211],v[176:179],v[208:211],a[208:211]
s_xor_b32 m0, s82, 3072
mm a[208:211],v[180:183],v[212:215],a[208:211]
ld v227,s[40:43],s79 offen lds
mm a[160:163],v[180:183],v[220:223],a[160:163]
mm a[160:163],v[176:179],v[216:219],a[160:163]
mm a[164:167],v[184:187],v[216:219],a[164:167]
s_xor_b32 m0, s82, 4096
mm a[164:167],v[188:191],v[220:223],a[164:167]
ld v228,s[40:43],s79 offen lds
mm a[212:215],v[188:191],v[212:215],a[212:215]
mm a[212:215],v[184:187],v[208:211],a[212:215]
rt v[44:45],v241 offset:17024
rt v[46:47],v241 offset:18048
mm a[156:159],v[184:187],v[200:203],a[156:159]
s_xor_b32 m0, s82, 5120
mm a[156:159],v[188:191],v[204:207],a[156:159]
ld v229,s[40:43],s79 offen lds
mm a[236:239],v[188:191],v[196:199],a[236:239]
mm a[236:239],v[184:187],v[192:195],a[236:239]
mm a[200:203],v[0:3],v[192:195],a[200:203]
s_xor_b32 m0, s82, 6144
mm a[200:203],v[4:7],v[196:199],a[200:203]
ld v230,s[40:43],s79 offen lds
mm a[184:187],v[4:7],v[204:207],a[184:187]
mm a[184:187],v[0:3],v[200:203],a[184:187]
)ASM" R"ASM(rt v[48:49],v242 offset:128
rt v[50:51],v242 offset:1152
mm a[64:67],v[0:3],v[208:211],a[64:67]
s_xor_b32 m0, s82, 7168
mm a[64:67],v[4:7],v[212:215],a[64:67]
ld v231,s[40:43],s79 offen lds
mm a[0:3],v[4:7],v[220:223],a[0:3]
s_mov_b32 s79, 384
mm a[0:3],v[0:3],v[216:219],a[0:3]
mm a[4:7],v[8:11],v[216:219],a[4:7]
mm a[4:7],v[12:15],v[220:223],a[4:7]
buffer_load_dword v254, v135, s[68:71], s96 offen
mm a[68:71],v[12:15],v[212:215],a[68:71]
s_add_u32 s96, s96, 1048576
mm a[68:71],v[8:11],v[208:211],a[68:71]
rt v[52:53],v242 offset:17024
rt v[54:55],v242 offset:18048
mm a[188:191],v[8:11],v[200:203],a[188:191]
mm a[188:191],v[12:15],v[204:207],a[188:191]
buffer_load_dword v254, v135, s[68:71], s96 offen
mm a[204:207],v[12:15],v[196:199],a[204:207]
s_add_u32 s96, s96, 1048576
mm a[204:207],v[8:11],v[192:195],a[204:207]
rt v[56:57],v243 offset:128
rt v[58:59],v243 offset:1152
mm a[88:91],v[16:19],v[192:195],a[88:91]
mm a[88:91],v[20:23],v[196:199],a[88:91]
mm a[24:27],v[20:23],v[204:207],a[24:27]
mm a[24:27],v[16:19],v[200:203],a[24:27]
rt v[60:61],v243 offset:17024
rt v[62:63],v243 offset:18048
mm a[240:243],v[16:19],v[208:211],a[240:243]
mm a[240:243],v[20:23],v[212:215],a[240:243]
mm a[32:35],v[20:23],v[220:223],a[32:35]
mm a[32:35],v[16:19],v[216:219],a[32:35]
mm a[36:39],v[24:27],v[216:219],a[36:39]
mm a[36:39],v[28:31],v[220:223],a[36:39]
mm a[244:247],v[28:31],v[212:215],a[244:247]
mm a[244:247],v[24:27],v[208:211],a[244:247]
mm a[28:31],v[24:27],v[200:203],a[28:31]
mm a[28:31],v[28:31],v[204:207],a[28:31]
mm a[92:95],v[28:31],v[196:199],a[92:95]
wt vmcnt(10) lgkmcnt(0)
s_barrier
mm a[92:95],v[24:27],v[192:195],a[92:95]
mm a[120:123],v[160:163],v[32:35],a[120:123]
mm a[120:123],v[164:167],v[36:39],a[120:123]
mm a[136:139],v[164:167],v[44:47],a[136:139]
rt v[192:193],v240 offset:33792
rt v[194:195],v240 offset:34816
mm a[136:139],v[160:163],v[40:43],a[136:139]
rt v[196:197],v240 offset:50688
rt v[198:199],v240 offset:51712
mm a[224:227],v[160:163],v[48:51],a[224:227]
mm a[224:227],v[164:167],v[52:55],a[224:227]
mm a[144:147],v[164:167],v[60:63],a[144:147]
mm a[144:147],v[160:163],v[56:59],a[144:147]
rd v[160:163],v236 offset:57344
rd v[164:167],v237 offset:57344
mm a[148:151],v[168:171],v[56:59],a[148:151]
mm a[148:151],v[172:175],v[60:63],a[148:151]
mm a[228:231],v[172:175],v[52:55],a[228:231]
mm a[228:231],v[168:171],v[48:51],a[228:231]
rt v[200:201],v241 offset:33792
rt v[202:203],v241 offset:34816
mm a[140:143],v[168:171],v[40:43],a[140:143]
mm a[140:143],v[172:175],v[44:47],a[140:143]
mm a[124:127],v[172:175],v[36:39],a[124:127]
mm a[124:127],v[168:171],v[32:35],a[124:127]
rd v[168:171],v236 offset:59392
rd v[172:175],v237 offset:59392
mm a[216:219],v[176:179],v[32:35],a[216:219]
mm a[216:219],v[180:183],v[36:39],a[216:219]
mm a[168:171],v[180:183],v[44:47],a[168:171]
mm a[168:171],v[176:179],v[40:43],a[168:171]
rt v[204:205],v241 offset:50688
rt v[206:207],v241 offset:51712
mm a[192:195],v[176:179],v[48:51],a[192:195]
mm a[192:195],v[180:183],v[52:55],a[192:195]
mm a[176:179],v[180:183],v[60:63],a[176:179]
mm a[176:179],v[176:179],v[56:59],a[176:179]
rd v[176:179],v236 offset:49152
rd v[180:183],v237 offset:49152
mm a[180:183],v[184:187],v[56:59],a[180:183]
mm a[180:183],v[188:191],v[60:63],a[180:183]
mm a[196:199],v[188:191],v[52:55],a[196:199]
mm a[196:199],v[184:187],v[48:51],a[196:199]
rt v[208:209],v242 offset:33792
rt v[210:211],v242 offset:34816
mm a[172:175],v[184:187],v[40:43],a[172:175]
s_add_u32 m0, s83, 0
mm a[172:175],v[188:191],v[44:47],a[172:175]
ld v232,s[44:47],s80 offen lds
mm a[220:223],v[188:191],v[36:39],a[220:223]
mm a[220:223],v[184:187],v[32:35],a[220:223]
rd v[184:187],v236 offset:51200
rd v[188:191],v237 offset:51200
mm a[72:75],v[0:3],v[32:35],a[72:75]
s_add_u32 m0, s83, 1024
mm a[72:75],v[4:7],v[36:39],a[72:75]
)ASM" R"ASM(ld v233,s[44:47],s80 offen lds
mm a[8:11],v[4:7],v[44:47],a[8:11]
mm a[8:11],v[0:3],v[40:43],a[8:11]
rt v[212:213],v242 offset:50688
rt v[214:215],v242 offset:51712
mm a[80:83],v[0:3],v[48:51],a[80:83]
s_add_u32 m0, s83, 2176
mm a[80:83],v[4:7],v[52:55],a[80:83]
ld v234,s[44:47],s80 offen lds
mm a[16:19],v[4:7],v[60:63],a[16:19]
mm a[16:19],v[0:3],v[56:59],a[16:19]
rd v[0:3],v236 offset:40960
rd v[4:7],v237 offset:40960
mm a[20:23],v[8:11],v[56:59],a[20:23]
s_add_u32 m0, s83, 3200
mm a[20:23],v[12:15],v[60:63],a[20:23]
ld v235,s[44:47],s80 offen lds
mm a[84:87],v[12:15],v[52:55],a[84:87]
s_mov_b32 s80, 1310720
mm a[84:87],v[8:11],v[48:51],a[84:87]
rt v[216:217],v243 offset:33792
rt v[218:219],v243 offset:34816
mm a[12:15],v[8:11],v[40:43],a[12:15]
s_add_u32 m0, s83, 16896
mm a[12:15],v[12:15],v[44:47],a[12:15]
ld v232,s[44:47],s80 offen lds
mm a[76:79],v[12:15],v[36:39],a[76:79]
mm a[76:79],v[8:11],v[32:35],a[76:79]
rd v[8:11],v236 offset:43008
rd v[12:15],v237 offset:43008
mm a[248:251],v[16:19],v[32:35],a[248:251]
s_add_u32 m0, s83, 17920
mm a[248:251],v[20:23],v[36:39],a[248:251]
ld v233,s[44:47],s80 offen lds
mm a[40:43],v[20:23],v[44:47],a[40:43]
mm a[40:43],v[16:19],v[40:43],a[40:43]
rt v[220:221],v243 offset:50688
rt v[222:223],v243 offset:51712
mm a[48:51],v[16:19],v[48:51],a[48:51]
s_add_u32 m0, s83, 19072
mm a[48:51],v[20:23],v[52:55],a[48:51]
ld v234,s[44:47],s80 offen lds
mm a[56:59],v[20:23],v[60:63],a[56:59]
mm a[56:59],v[16:19],v[56:59],a[56:59]
rd v[16:19],v236 offset:32768
rd v[20:23],v237 offset:32768
mm a[60:63],v[24:27],v[56:59],a[60:63]
s_add_u32 m0, s83, 20096
mm a[60:63],v[28:31],v[60:63],a[60:63]
ld v235,s[44:47],s80 offen lds
mm a[52:55],v[28:31],v[52:55],a[52:55]
s_mov_b32 s80, 1572864
mm a[52:55],v[24:27],v[48:51],a[52:55]
mm a[44:47],v[24:27],v[40:43],a[44:47]
mm a[44:47],v[28:31],v[44:47],a[44:47]
mm a[252:255],v[28:31],v[36:39],a[252:255]
wt lgkmcnt(2)
mm a[252:255],v[24:27],v[32:35],a[252:255]
rd v[24:27],v236 offset:34816
rd v[28:31],v237 offset:34816
mm a[96:99],v[160:163],v[192:195],a[96:99]
mm a[96:99],v[164:167],v[196:199],a[96:99]
mm a[104:107],v[164:167],v[204:207],a[104:107]
mm a[104:107],v[160:163],v[200:203],a[104:107]
rt v[32:33],v240 offset:33920
rt v[34:35],v240 offset:34944
mm a[112:115],v[160:163],v[208:211],a[112:115]
mm a[112:115],v[164:167],v[212:215],a[112:115]
mm a[128:131],v[164:167],v[220:223],a[128:131]
.La5_LW1_7:
s_cbranch_execz .La5_LW1_11
wt lgkmcnt(2)
s_barrier
mm a[128:131],v[160:163],v[216:219],a[128:131]
mm a[132:135],v[168:171],v[216:219],a[132:135]
s_xor_b32 m0, s82, 32768
mm a[132:135],v[172:175],v[220:223],a[132:135]
ld v224,s[40:43],s79 offen lds
mm a[116:119],v[172:175],v[212:215],a[116:119]
mm a[116:119],v[168:171],v[208:211],a[116:119]
rt v[36:37],v240 offset:50816
rt v[38:39],v240 offset:51840
mm a[108:111],v[168:171],v[200:203],a[108:111]
s_xor_b32 m0, s82, 33792
mm a[108:111],v[172:175],v[204:207],a[108:111]
ld v225,s[40:43],s79 offen lds
mm a[100:103],v[172:175],v[196:199],a[100:103]
mm a[100:103],v[168:171],v[192:195],a[100:103]
mm a[232:235],v[176:179],v[192:195],a[232:235]
s_xor_b32 m0, s82, 34816
mm a[232:235],v[180:183],v[196:199],a[232:235]
ld v226,s[40:43],s79 offen lds
mm a[152:155],v[180:183],v[204:207],a[152:155]
mm a[152:155],v[176:179],v[200:203],a[152:155]
rt v[40:41],v241 offset:33920
rt v[42:43],v241 offset:34944
mm a[208:211],v[176:179],v[208:211],a[208:211]
s_xor_b32 m0, s82, 35840
mm a[208:211],v[180:183],v[212:215],a[208:211]
ld v227,s[40:43],s79 offen lds
)ASM" R"ASM(mm a[160:163],v[180:183],v[220:223],a[160:163]
mm a[160:163],v[176:179],v[216:219],a[160:163]
mm a[164:167],v[184:187],v[216:219],a[164:167]
s_xor_b32 m0, s82, 36864
mm a[164:167],v[188:191],v[220:223],a[164:167]
ld v228,s[40:43],s79 offen lds
mm a[212:215],v[188:191],v[212:215],a[212:215]
mm a[212:215],v[184:187],v[208:211],a[212:215]
rt v[44:45],v241 offset:50816
rt v[46:47],v241 offset:51840
mm a[156:159],v[184:187],v[200:203],a[156:159]
s_xor_b32 m0, s82, 37888
mm a[156:159],v[188:191],v[204:207],a[156:159]
ld v229,s[40:43],s79 offen lds
mm a[236:239],v[188:191],v[196:199],a[236:239]
mm a[236:239],v[184:187],v[192:195],a[236:239]
mm a[200:203],v[0:3],v[192:195],a[200:203]
s_xor_b32 m0, s82, 38912
mm a[200:203],v[4:7],v[196:199],a[200:203]
ld v230,s[40:43],s79 offen lds
mm a[184:187],v[4:7],v[204:207],a[184:187]
mm a[184:187],v[0:3],v[200:203],a[184:187]
rt v[48:49],v242 offset:33920
rt v[50:51],v242 offset:34944
mm a[64:67],v[0:3],v[208:211],a[64:67]
s_xor_b32 m0, s82, 39936
mm a[64:67],v[4:7],v[212:215],a[64:67]
ld v231,s[40:43],s79 offen lds
mm a[0:3],v[4:7],v[220:223],a[0:3]
s_mov_b32 s79, 512
mm a[0:3],v[0:3],v[216:219],a[0:3]
mm a[4:7],v[8:11],v[216:219],a[4:7]
mm a[4:7],v[12:15],v[220:223],a[4:7]
buffer_load_dword v254, v135, s[68:71], s96 offen
mm a[68:71],v[12:15],v[212:215],a[68:71]
s_add_u32 s96, s96, 1048576
mm a[68:71],v[8:11],v[208:211],a[68:71]
rt v[52:53],v242 offset:50816
rt v[54:55],v242 offset:51840
mm a[188:191],v[8:11],v[200:203],a[188:191]
mm a[188:191],v[12:15],v[204:207],a[188:191]
buffer_load_dword v254, v135, s[68:71], s96 offen
mm a[204:207],v[12:15],v[196:199],a[204:207]
s_add_u32 s96, s96, 1048576
mm a[204:207],v[8:11],v[192:195],a[204:207]
rt v[56:57],v243 offset:33920
rt v[58:59],v243 offset:34944
mm a[88:91],v[16:19],v[192:195],a[88:91]
mm a[88:91],v[20:23],v[196:199],a[88:91]
mm a[24:27],v[20:23],v[204:207],a[24:27]
mm a[24:27],v[16:19],v[200:203],a[24:27]
rt v[60:61],v243 offset:50816
rt v[62:63],v243 offset:51840
mm a[240:243],v[16:19],v[208:211],a[240:243]
mm a[240:243],v[20:23],v[212:215],a[240:243]
mm a[32:35],v[20:23],v[220:223],a[32:35]
mm a[32:35],v[16:19],v[216:219],a[32:35]
mm a[36:39],v[24:27],v[216:219],a[36:39]
mm a[36:39],v[28:31],v[220:223],a[36:39]
mm a[244:247],v[28:31],v[212:215],a[244:247]
mm a[244:247],v[24:27],v[208:211],a[244:247]
mm a[28:31],v[24:27],v[200:203],a[28:31]
mm a[28:31],v[28:31],v[204:207],a[28:31]
mm a[92:95],v[28:31],v[196:199],a[92:95]
wt vmcnt(10) lgkmcnt(0)
s_barrier
mm a[92:95],v[24:27],v[192:195],a[92:95]
mm a[120:123],v[160:163],v[32:35],a[120:123]
mm a[120:123],v[164:167],v[36:39],a[120:123]
mm a[136:139],v[164:167],v[44:47],a[136:139]
rt v[192:193],v240 offset:0
rt v[194:195],v240 offset:1024
mm a[136:139],v[160:163],v[40:43],a[136:139]
rt v[196:197],v240 offset:16896
rt v[198:199],v240 offset:17920
mm a[224:227],v[160:163],v[48:51],a[224:227]
mm a[224:227],v[164:167],v[52:55],a[224:227]
mm a[144:147],v[164:167],v[60:63],a[144:147]
mm a[144:147],v[160:163],v[56:59],a[144:147]
rd v[160:163],v236 offset:24576
rd v[164:167],v237 offset:24576
mm a[148:151],v[168:171],v[56:59],a[148:151]
mm a[148:151],v[172:175],v[60:63],a[148:151]
mm a[228:231],v[172:175],v[52:55],a[228:231]
mm a[228:231],v[168:171],v[48:51],a[228:231]
rt v[200:201],v241 offset:0
rt v[202:203],v241 offset:1024
mm a[140:143],v[168:171],v[40:43],a[140:143]
mm a[140:143],v[172:175],v[44:47],a[140:143]
mm a[124:127],v[172:175],v[36:39],a[124:127]
mm a[124:127],v[168:171],v[32:35],a[124:127]
rd v[168:171],v236 offset:26624
rd v[172:175],v237 offset:26624
mm a[216:219],v[176:179],v[32:35],a[216:219]
mm a[216:219],v[180:183],v[36:39],a[216:219]
mm a[168:171],v[180:183],v[44:47],a[168:171]
mm a[168:171],v[176:179],v[40:43],a[168:171]
rt v[204:205],v241 offset:16896
rt v[206:207],v241 offset:17920
mm a[192:195],v[176:179],v[48:51],a[192:195]
)ASM" R"ASM(mm a[192:195],v[180:183],v[52:55],a[192:195]
mm a[176:179],v[180:183],v[60:63],a[176:179]
mm a[176:179],v[176:179],v[56:59],a[176:179]
rd v[176:179],v236 offset:16384
rd v[180:183],v237 offset:16384
mm a[180:183],v[184:187],v[56:59],a[180:183]
mm a[180:183],v[188:191],v[60:63],a[180:183]
mm a[196:199],v[188:191],v[52:55],a[196:199]
mm a[196:199],v[184:187],v[48:51],a[196:199]
rt v[208:209],v242 offset:0
rt v[210:211],v242 offset:1024
mm a[172:175],v[184:187],v[40:43],a[172:175]
s_add_u32 m0, s83, 33792
mm a[172:175],v[188:191],v[44:47],a[172:175]
ld v232,s[44:47],s80 offen lds
mm a[220:223],v[188:191],v[36:39],a[220:223]
mm a[220:223],v[184:187],v[32:35],a[220:223]
rd v[184:187],v236 offset:18432
rd v[188:191],v237 offset:18432
mm a[72:75],v[0:3],v[32:35],a[72:75]
s_add_u32 m0, s83, 34816
mm a[72:75],v[4:7],v[36:39],a[72:75]
ld v233,s[44:47],s80 offen lds
mm a[8:11],v[4:7],v[44:47],a[8:11]
mm a[8:11],v[0:3],v[40:43],a[8:11]
rt v[212:213],v242 offset:16896
rt v[214:215],v242 offset:17920
mm a[80:83],v[0:3],v[48:51],a[80:83]
s_add_u32 m0, s83, 35968
mm a[80:83],v[4:7],v[52:55],a[80:83]
ld v234,s[44:47],s80 offen lds
mm a[16:19],v[4:7],v[60:63],a[16:19]
mm a[16:19],v[0:3],v[56:59],a[16:19]
rd v[0:3],v236 offset:8192
rd v[4:7],v237 offset:8192
mm a[20:23],v[8:11],v[56:59],a[20:23]
s_add_u32 m0, s83, 36992
mm a[20:23],v[12:15],v[60:63],a[20:23]
ld v235,s[44:47],s80 offen lds
mm a[84:87],v[12:15],v[52:55],a[84:87]
s_mov_b32 s80, 1835008
mm a[84:87],v[8:11],v[48:51],a[84:87]
rt v[216:217],v243 offset:0
rt v[218:219],v243 offset:1024
mm a[12:15],v[8:11],v[40:43],a[12:15]
s_add_u32 m0, s83, 50688
mm a[12:15],v[12:15],v[44:47],a[12:15]
ld v232,s[44:47],s80 offen lds
mm a[76:79],v[12:15],v[36:39],a[76:79]
mm a[76:79],v[8:11],v[32:35],a[76:79]
rd v[8:11],v236 offset:10240
rd v[12:15],v237 offset:10240
mm a[248:251],v[16:19],v[32:35],a[248:251]
s_add_u32 m0, s83, 51712
mm a[248:251],v[20:23],v[36:39],a[248:251]
ld v233,s[44:47],s80 offen lds
mm a[40:43],v[20:23],v[44:47],a[40:43]
mm a[40:43],v[16:19],v[40:43],a[40:43]
rt v[220:221],v243 offset:16896
rt v[222:223],v243 offset:17920
mm a[48:51],v[16:19],v[48:51],a[48:51]
s_add_u32 m0, s83, 52864
mm a[48:51],v[20:23],v[52:55],a[48:51]
ld v234,s[44:47],s80 offen lds
mm a[56:59],v[20:23],v[60:63],a[56:59]
mm a[56:59],v[16:19],v[56:59],a[56:59]
rd v[16:19],v236 offset:0
rd v[20:23],v237 offset:0
mm a[60:63],v[24:27],v[56:59],a[60:63]
s_add_u32 m0, s83, 53888
mm a[60:63],v[28:31],v[60:63],a[60:63]
ld v235,s[44:47],s80 offen lds
mm a[52:55],v[28:31],v[52:55],a[52:55]
s_mov_b64 s[44:45], s[68:69]
s_mov_b32 s46, s70
mm a[52:55],v[24:27],v[48:51],a[52:55]
mm a[44:47],v[24:27],v[40:43],a[44:47]
mm a[44:47],v[28:31],v[44:47],a[44:47]
mm a[252:255],v[28:31],v[36:39],a[252:255]
wt lgkmcnt(2)
s_lshl_b32 s80, s77, 18
mm a[252:255],v[24:27],v[32:35],a[252:255]
rd v[24:27],v236 offset:2048
rd v[28:31],v237 offset:2048
mm a[96:99],v[160:163],v[192:195],a[96:99]
mm a[96:99],v[164:167],v[196:199],a[96:99]
mm a[104:107],v[164:167],v[204:207],a[104:107]
mm a[104:107],v[160:163],v[200:203],a[104:107]
rt v[32:33],v240 offset:128
rt v[34:35],v240 offset:1152
mm a[112:115],v[160:163],v[208:211],a[112:115]
mm a[112:115],v[164:167],v[212:215],a[112:115]
mm a[128:131],v[164:167],v[220:223],a[128:131]
wt lgkmcnt(2)
s_barrier
mm a[128:131],v[160:163],v[216:219],a[128:131]
mm a[132:135],v[168:171],v[216:219],a[132:135]
mm a[132:135],v[172:175],v[220:223],a[132:135]
mm a[116:119],v[172:175],v[212:215],a[116:119]
mm a[116:119],v[168:171],v[208:211],a[116:119]
)ASM" R"ASM(rt v[36:37],v240 offset:17024
rt v[38:39],v240 offset:18048
mm a[108:111],v[168:171],v[200:203],a[108:111]
mm a[108:111],v[172:175],v[204:207],a[108:111]
mm a[100:103],v[172:175],v[196:199],a[100:103]
mm a[100:103],v[168:171],v[192:195],a[100:103]
mm a[232:235],v[176:179],v[192:195],a[232:235]
mm a[232:235],v[180:183],v[196:199],a[232:235]
mm a[152:155],v[180:183],v[204:207],a[152:155]
mm a[152:155],v[176:179],v[200:203],a[152:155]
rt v[40:41],v241 offset:128
rt v[42:43],v241 offset:1152
mm a[208:211],v[176:179],v[208:211],a[208:211]
mm a[208:211],v[180:183],v[212:215],a[208:211]
mm a[160:163],v[180:183],v[220:223],a[160:163]
mm a[160:163],v[176:179],v[216:219],a[160:163]
mm a[164:167],v[184:187],v[216:219],a[164:167]
mm a[164:167],v[188:191],v[220:223],a[164:167]
mm a[212:215],v[188:191],v[212:215],a[212:215]
mm a[212:215],v[184:187],v[208:211],a[212:215]
rt v[44:45],v241 offset:17024
rt v[46:47],v241 offset:18048
mm a[156:159],v[184:187],v[200:203],a[156:159]
mm a[156:159],v[188:191],v[204:207],a[156:159]
mm a[236:239],v[188:191],v[196:199],a[236:239]
mm a[236:239],v[184:187],v[192:195],a[236:239]
mm a[200:203],v[0:3],v[192:195],a[200:203]
mm a[200:203],v[4:7],v[196:199],a[200:203]
mm a[184:187],v[4:7],v[204:207],a[184:187]
mm a[184:187],v[0:3],v[200:203],a[184:187]
rt v[48:49],v242 offset:128
rt v[50:51],v242 offset:1152
mm a[64:67],v[0:3],v[208:211],a[64:67]
mm a[64:67],v[4:7],v[212:215],a[64:67]
mm a[0:3],v[4:7],v[220:223],a[0:3]
mm a[0:3],v[0:3],v[216:219],a[0:3]
mm a[4:7],v[8:11],v[216:219],a[4:7]
mm a[4:7],v[12:15],v[220:223],a[4:7]
buffer_load_dword v254, v135, s[68:71], s96 offen
mm a[68:71],v[12:15],v[212:215],a[68:71]
s_add_u32 s96, s96, 1048576
mm a[68:71],v[8:11],v[208:211],a[68:71]
rt v[52:53],v242 offset:17024
rt v[54:55],v242 offset:18048
mm a[188:191],v[8:11],v[200:203],a[188:191]
mm a[188:191],v[12:15],v[204:207],a[188:191]
buffer_load_dword v254, v135, s[68:71], s96 offen
mm a[204:207],v[12:15],v[196:199],a[204:207]
s_add_u32 s96, s96, 1048576
mm a[204:207],v[8:11],v[192:195],a[204:207]
rt v[56:57],v243 offset:128
rt v[58:59],v243 offset:1152
mm a[88:91],v[16:19],v[192:195],a[88:91]
mm a[88:91],v[20:23],v[196:199],a[88:91]
mm a[24:27],v[20:23],v[204:207],a[24:27]
mm a[24:27],v[16:19],v[200:203],a[24:27]
rt v[60:61],v243 offset:17024
rt v[62:63],v243 offset:18048
mm a[240:243],v[16:19],v[208:211],a[240:243]
mm a[240:243],v[20:23],v[212:215],a[240:243]
mm a[32:35],v[20:23],v[220:223],a[32:35]
mm a[32:35],v[16:19],v[216:219],a[32:35]
mm a[36:39],v[24:27],v[216:219],a[36:39]
mm a[36:39],v[28:31],v[220:223],a[36:39]
ld v[72:75],v255,s[64:67],s89 offen offset:1024 sc1
mm a[244:247],v[28:31],v[212:215],a[244:247]
mm a[244:247],v[24:27],v[208:211],a[244:247]
mm a[28:31],v[24:27],v[200:203],a[28:31]
mm a[28:31],v[28:31],v[204:207],a[28:31]
mm a[92:95],v[28:31],v[196:199],a[92:95]
wt vmcnt(3) lgkmcnt(0)
s_barrier
mm a[92:95],v[24:27],v[192:195],a[92:95]
mm a[120:123],v[160:163],v[32:35],a[120:123]
mm a[120:123],v[164:167],v[36:39],a[120:123]
mm a[136:139],v[164:167],v[44:47],a[136:139]
rt v[192:193],v240 offset:33792
rt v[194:195],v240 offset:34816
mm a[136:139],v[160:163],v[40:43],a[136:139]
rt v[196:197],v240 offset:50688
rt v[198:199],v240 offset:51712
mm a[224:227],v[160:163],v[48:51],a[224:227]
mm a[224:227],v[164:167],v[52:55],a[224:227]
mm a[144:147],v[164:167],v[60:63],a[144:147]
mm a[144:147],v[160:163],v[56:59],a[144:147]
rd v[160:163],v236 offset:57344
rd v[164:167],v237 offset:57344
mm a[148:151],v[168:171],v[56:59],a[148:151]
mm a[148:151],v[172:175],v[60:63],a[148:151]
mm a[228:231],v[172:175],v[52:55],a[228:231]
mm a[228:231],v[168:171],v[48:51],a[228:231]
rt v[200:201],v241 offset:33792
rt v[202:203],v241 offset:34816
mm a[140:143],v[168:171],v[40:43],a[140:143]
mm a[140:143],v[172:175],v[44:47],a[140:143]
mm a[124:127],v[172:175],v[36:39],a[124:127]
mm a[124:127],v[168:171],v[32:35],a[124:127]
rd v[168:171],v236 offset:59392
rd v[172:175],v237 offset:59392
mm a[216:219],v[176:179],v[32:35],a[216:219]
)ASM" R"ASM(mm a[216:219],v[180:183],v[36:39],a[216:219]
mm a[168:171],v[180:183],v[44:47],a[168:171]
mm a[168:171],v[176:179],v[40:43],a[168:171]
rt v[204:205],v241 offset:50688
rt v[206:207],v241 offset:51712
mm a[192:195],v[176:179],v[48:51],a[192:195]
mm a[192:195],v[180:183],v[52:55],a[192:195]
mm a[176:179],v[180:183],v[60:63],a[176:179]
mm a[176:179],v[176:179],v[56:59],a[176:179]
rd v[176:179],v236 offset:49152
rd v[180:183],v237 offset:49152
mm a[180:183],v[184:187],v[56:59],a[180:183]
mm a[180:183],v[188:191],v[60:63],a[180:183]
mm a[196:199],v[188:191],v[52:55],a[196:199]
mm a[196:199],v[184:187],v[48:51],a[196:199]
rt v[208:209],v242 offset:33792
rt v[210:211],v242 offset:34816
mm a[172:175],v[184:187],v[40:43],a[172:175]
s_add_u32 m0, s83, 0
mm a[172:175],v[188:191],v[44:47],a[172:175]
ld v232,s[44:47],s80 offen lds
mm a[220:223],v[188:191],v[36:39],a[220:223]
mm a[220:223],v[184:187],v[32:35],a[220:223]
rd v[184:187],v236 offset:51200
rd v[188:191],v237 offset:51200
mm a[72:75],v[0:3],v[32:35],a[72:75]
s_add_u32 m0, s83, 1024
mm a[72:75],v[4:7],v[36:39],a[72:75]
ld v233,s[44:47],s80 offen lds
mm a[8:11],v[4:7],v[44:47],a[8:11]
mm a[8:11],v[0:3],v[40:43],a[8:11]
rt v[212:213],v242 offset:50688
rt v[214:215],v242 offset:51712
mm a[80:83],v[0:3],v[48:51],a[80:83]
s_add_u32 m0, s83, 2176
mm a[80:83],v[4:7],v[52:55],a[80:83]
ld v234,s[44:47],s80 offen lds
mm a[16:19],v[4:7],v[60:63],a[16:19]
mm a[16:19],v[0:3],v[56:59],a[16:19]
rd v[0:3],v236 offset:40960
rd v[4:7],v237 offset:40960
mm a[20:23],v[8:11],v[56:59],a[20:23]
s_add_u32 m0, s83, 3200
mm a[20:23],v[12:15],v[60:63],a[20:23]
ld v235,s[44:47],s80 offen lds
mm a[84:87],v[12:15],v[52:55],a[84:87]
s_add_u32 s80, s80, 524288
mm a[84:87],v[8:11],v[48:51],a[84:87]
rt v[216:217],v243 offset:33792
rt v[218:219],v243 offset:34816
mm a[12:15],v[8:11],v[40:43],a[12:15]
s_add_u32 m0, s83, 16896
mm a[12:15],v[12:15],v[44:47],a[12:15]
ld v232,s[44:47],s80 offen lds
mm a[76:79],v[12:15],v[36:39],a[76:79]
mm a[76:79],v[8:11],v[32:35],a[76:79]
rd v[8:11],v236 offset:43008
rd v[12:15],v237 offset:43008
mm a[248:251],v[16:19],v[32:35],a[248:251]
s_add_u32 m0, s83, 17920
mm a[248:251],v[20:23],v[36:39],a[248:251]
ld v233,s[44:47],s80 offen lds
mm a[40:43],v[20:23],v[44:47],a[40:43]
mm a[40:43],v[16:19],v[40:43],a[40:43]
rt v[220:221],v243 offset:50688
rt v[222:223],v243 offset:51712
mm a[48:51],v[16:19],v[48:51],a[48:51]
s_add_u32 m0, s83, 19072
mm a[48:51],v[20:23],v[52:55],a[48:51]
ld v234,s[44:47],s80 offen lds
mm a[56:59],v[20:23],v[60:63],a[56:59]
mm a[56:59],v[16:19],v[56:59],a[56:59]
rd v[16:19],v236 offset:32768
rd v[20:23],v237 offset:32768
mm a[60:63],v[24:27],v[56:59],a[60:63]
s_add_u32 m0, s83, 20096
mm a[60:63],v[28:31],v[60:63],a[60:63]
ld v235,s[44:47],s80 offen lds
mm a[52:55],v[28:31],v[52:55],a[52:55]
s_add_u32 s80, s80, 524288
mm a[52:55],v[24:27],v[48:51],a[52:55]
mm a[44:47],v[24:27],v[40:43],a[44:47]
mm a[44:47],v[28:31],v[44:47],a[44:47]
mm a[252:255],v[28:31],v[36:39],a[252:255]
wt lgkmcnt(2)
mm a[252:255],v[24:27],v[32:35],a[252:255]
rd v[24:27],v236 offset:34816
rd v[28:31],v237 offset:34816
mm a[96:99],v[160:163],v[192:195],a[96:99]
mm a[96:99],v[164:167],v[196:199],a[96:99]
mm a[104:107],v[164:167],v[204:207],a[104:107]
mm a[104:107],v[160:163],v[200:203],a[104:107]
rt v[32:33],v240 offset:33920
rt v[34:35],v240 offset:34944
mm a[112:115],v[160:163],v[208:211],a[112:115]
mm a[112:115],v[164:167],v[212:215],a[112:115]
mm a[128:131],v[164:167],v[220:223],a[128:131]
wt lgkmcnt(2)
s_barrier
mm a[128:131],v[160:163],v[216:219],a[128:131]
)ASM" R"ASM(mm a[132:135],v[168:171],v[216:219],a[132:135]
wt vmcnt(8)
v_cmp_eq_u64 vcc, v[72:73], v[252:253]
mm a[132:135],v[172:175],v[220:223],a[132:135]
v_cmp_eq_u64 s[96:97], v[74:75], v[252:253]
mm a[116:119],v[172:175],v[212:215],a[116:119]
s_and_b64 vcc, vcc, s[96:97]
s_cmp_eq_u64 vcc, -1
s_cbranch_scc0 .La5_LV_XSPIN
.La5_LV_XOK:
mm a[116:119],v[168:171],v[208:211],a[116:119]
rt v[36:37],v240 offset:50816
rt v[38:39],v240 offset:51840
mm a[108:111],v[168:171],v[200:203],a[108:111]
s_mov_b32 s89, 49152
mm a[108:111],v[172:175],v[204:207],a[108:111]
mm a[100:103],v[172:175],v[196:199],a[100:103]
mm a[100:103],v[168:171],v[192:195],a[100:103]
mm a[232:235],v[176:179],v[192:195],a[232:235]
mm a[232:235],v[180:183],v[196:199],a[232:235]
ld v[128:131],v244,s[52:55],s91 offen offset:0 sc1
mm a[152:155],v[180:183],v[204:207],a[152:155]
ar v224,a96
ar v225,a97
mm a[152:155],v[176:179],v[200:203],a[152:155]
rt v[40:41],v241 offset:33920
rt v[42:43],v241 offset:34944
mm a[208:211],v[176:179],v[208:211],a[208:211]
ar v226,a98
ar v227,a99
mm a[208:211],v[180:183],v[212:215],a[208:211]
ld v[132:135],v245,s[52:55],s91 offen offset:0 sc1
mm a[160:163],v[180:183],v[220:223],a[160:163]
ar v228,a100
ar v229,a101
mm a[160:163],v[176:179],v[216:219],a[160:163]
ar v230,a102
ar v231,a103
mm a[164:167],v[184:187],v[216:219],a[164:167]
cv v248,v224,v225
cv v249,v226,v227
mm a[164:167],v[188:191],v[220:223],a[164:167]
ld v[136:139],v246,s[52:55],s91 offen offset:0 sc1
mm a[212:215],v[188:191],v[212:215],a[212:215]
cv v250,v228,v229
cv v251,v230,v231
mm a[212:215],v[184:187],v[208:211],a[212:215]
rt v[44:45],v241 offset:50816
rt v[46:47],v241 offset:51840
mm a[156:159],v[184:187],v[200:203],a[156:159]
st v[248:251],v255,s[48:51],s89 offen sc1
mm a[156:159],v[188:191],v[204:207],a[156:159]
ld v[140:143],v247,s[52:55],s91 offen offset:0 sc1
mm a[236:239],v[188:191],v[196:199],a[236:239]
s_mov_b32 s89, 50176
ds_write_b128 v238, v[248:251] offset:0
mm a[236:239],v[184:187],v[192:195],a[236:239]
ar v224,a104
ar v225,a105
mm a[200:203],v[0:3],v[192:195],a[200:203]
ar v226,a106
ar v227,a107
mm a[200:203],v[4:7],v[196:199],a[200:203]
ld v[144:147],v244,s[52:55],s91 offen offset:1024 sc1
mm a[184:187],v[4:7],v[204:207],a[184:187]
ar v228,a108
ar v229,a109
mm a[184:187],v[0:3],v[200:203],a[184:187]
rt v[48:49],v242 offset:33920
rt v[50:51],v242 offset:34944
mm a[64:67],v[0:3],v[208:211],a[64:67]
ar v230,a110
ar v231,a111
mm a[64:67],v[4:7],v[212:215],a[64:67]
ld v[148:151],v245,s[52:55],s91 offen offset:1024 sc1
mm a[0:3],v[4:7],v[220:223],a[0:3]
cv v248,v224,v225
cv v249,v226,v227
mm a[0:3],v[0:3],v[216:219],a[0:3]
cv v250,v228,v229
cv v251,v230,v231
mm a[4:7],v[8:11],v[216:219],a[4:7]
st v[248:251],v255,s[48:51],s89 offen sc1
mm a[4:7],v[12:15],v[220:223],a[4:7]
ld v[152:155],v246,s[52:55],s91 offen offset:1024 sc1
mm a[68:71],v[12:15],v[212:215],a[68:71]
s_mov_b32 s89, 51200
ds_write_b128 v238, v[248:251] offset:1024
mm a[68:71],v[8:11],v[208:211],a[68:71]
rt v[52:53],v242 offset:50816
rt v[54:55],v242 offset:51840
mm a[188:191],v[8:11],v[200:203],a[188:191]
ar v224,a112
ar v225,a113
.La5_LW1_8:
s_cbranch_execz .La5_LW1_12
mm a[188:191],v[12:15],v[204:207],a[188:191]
ld v[156:159],v247,s[52:55],s91 offen offset:1024 sc1
mm a[204:207],v[12:15],v[196:199],a[204:207]
ar v226,a114
)ASM" R"ASM(ar v227,a115
mm a[204:207],v[8:11],v[192:195],a[204:207]
rt v[56:57],v243 offset:33920
rt v[58:59],v243 offset:34944
mm a[88:91],v[16:19],v[192:195],a[88:91]
ar v228,a116
ar v229,a117
mm a[88:91],v[20:23],v[196:199],a[88:91]
ar v230,a118
ar v231,a119
mm a[24:27],v[20:23],v[204:207],a[24:27]
cv v248,v224,v225
cv v249,v226,v227
mm a[24:27],v[16:19],v[200:203],a[24:27]
rt v[60:61],v243 offset:50816
rt v[62:63],v243 offset:51840
mm a[240:243],v[16:19],v[208:211],a[240:243]
v_subrev_u32 v240, 8448, v240
v_subrev_u32 v241, 8448, v241
mm a[240:243],v[20:23],v[212:215],a[240:243]
v_subrev_u32 v242, 8448, v242
v_subrev_u32 v243, 8448, v243
mm a[32:35],v[20:23],v[220:223],a[32:35]
cv v250,v228,v229
cv v251,v230,v231
mm a[32:35],v[16:19],v[216:219],a[32:35]
st v[248:251],v255,s[48:51],s89 offen sc1
mm a[36:39],v[24:27],v[216:219],a[36:39]
s_mov_b32 s89, 52224
ds_write_b128 v238, v[248:251] offset:2048
mm a[36:39],v[28:31],v[220:223],a[36:39]
mm a[244:247],v[28:31],v[212:215],a[244:247]
mm a[244:247],v[24:27],v[208:211],a[244:247]
mm a[28:31],v[24:27],v[200:203],a[28:31]
mm a[28:31],v[28:31],v[204:207],a[28:31]
mm a[92:95],v[28:31],v[196:199],a[92:95]
wt vmcnt(11) lgkmcnt(1)
s_barrier
mm a[92:95],v[24:27],v[192:195],a[92:95]
mm a[120:123],v[160:163],v[32:35],a[120:123]
s_add_u32 m0, s83, 33792
mm a[120:123],v[164:167],v[36:39],a[120:123]
ld v232,s[44:47],s80 offen lds
mm a[136:139],v[164:167],v[44:47],a[136:139]
mm a[136:139],v[160:163],v[40:43],a[136:139]
rt v[192:193],v240 offset:0
rt v[194:195],v240 offset:1024
mm a[224:227],v[160:163],v[48:51],a[224:227]
s_add_u32 m0, s83, 34816
mm a[224:227],v[164:167],v[52:55],a[224:227]
ld v233,s[44:47],s80 offen lds
mm a[144:147],v[164:167],v[60:63],a[144:147]
mm a[144:147],v[160:163],v[56:59],a[144:147]
rt v[196:197],v240 offset:8448
rt v[198:199],v240 offset:9472
mm a[148:151],v[168:171],v[56:59],a[148:151]
s_add_u32 m0, s83, 35968
mm a[148:151],v[172:175],v[60:63],a[148:151]
ld v234,s[44:47],s80 offen lds
mm a[228:231],v[172:175],v[52:55],a[228:231]
mm a[228:231],v[168:171],v[48:51],a[228:231]
rt v[200:201],v241 offset:0
rt v[202:203],v241 offset:1024
mm a[140:143],v[168:171],v[40:43],a[140:143]
s_add_u32 m0, s83, 36992
mm a[140:143],v[172:175],v[44:47],a[140:143]
ld v235,s[44:47],s80 offen lds
mm a[124:127],v[172:175],v[36:39],a[124:127]
s_add_u32 s80, s80, 524288
mm a[124:127],v[168:171],v[32:35],a[124:127]
rt v[204:205],v241 offset:8448
rt v[206:207],v241 offset:9472
mm a[216:219],v[176:179],v[32:35],a[216:219]
s_add_u32 m0, s83, 50688
mm a[216:219],v[180:183],v[36:39],a[216:219]
ld v232,s[44:47],s80 offen lds
mm a[168:171],v[180:183],v[44:47],a[168:171]
mm a[168:171],v[176:179],v[40:43],a[168:171]
rt v[208:209],v242 offset:0
rt v[210:211],v242 offset:1024
mm a[192:195],v[176:179],v[48:51],a[192:195]
s_add_u32 m0, s83, 51712
mm a[192:195],v[180:183],v[52:55],a[192:195]
ld v233,s[44:47],s80 offen lds
mm a[176:179],v[180:183],v[60:63],a[176:179]
mm a[176:179],v[176:179],v[56:59],a[176:179]
rt v[212:213],v242 offset:8448
rt v[214:215],v242 offset:9472
mm a[180:183],v[184:187],v[56:59],a[180:183]
s_add_u32 m0, s83, 52864
mm a[180:183],v[188:191],v[60:63],a[180:183]
ld v234,s[44:47],s80 offen lds
mm a[196:199],v[188:191],v[52:55],a[196:199]
mm a[196:199],v[184:187],v[48:51],a[196:199]
rt v[216:217],v243 offset:0
rt v[218:219],v243 offset:1024
mm a[172:175],v[184:187],v[40:43],a[172:175]
s_add_u32 m0, s83, 53888
mm a[172:175],v[188:191],v[44:47],a[172:175]
ld v235,s[44:47],s80 offen lds
)ASM" R"ASM(mm a[220:223],v[188:191],v[36:39],a[220:223]
s_add_u32 s80, s80, 524288
mm a[220:223],v[184:187],v[32:35],a[220:223]
rt v[220:221],v243 offset:8448
rt v[222:223],v243 offset:9472
mm a[72:75],v[0:3],v[32:35],a[72:75]
mm a[72:75],v[4:7],v[36:39],a[72:75]
ld v[160:163],v244,s[52:55],s91 offen offset:2048 sc1
mm a[8:11],v[4:7],v[44:47],a[8:11]
mm a[8:11],v[0:3],v[40:43],a[8:11]
mm a[80:83],v[0:3],v[48:51],a[80:83]
mm a[80:83],v[4:7],v[52:55],a[80:83]
ld v[164:167],v245,s[52:55],s91 offen offset:2048 sc1
mm a[16:19],v[4:7],v[60:63],a[16:19]
mm a[16:19],v[0:3],v[56:59],a[16:19]
mm a[20:23],v[8:11],v[56:59],a[20:23]
mm a[20:23],v[12:15],v[60:63],a[20:23]
ld v[168:171],v246,s[52:55],s91 offen offset:2048 sc1
mm a[84:87],v[12:15],v[52:55],a[84:87]
mm a[84:87],v[8:11],v[48:51],a[84:87]
mm a[12:15],v[8:11],v[40:43],a[12:15]
mm a[12:15],v[12:15],v[44:47],a[12:15]
ld v[172:175],v247,s[52:55],s91 offen offset:2048 sc1
mm a[76:79],v[12:15],v[36:39],a[76:79]
mm a[76:79],v[8:11],v[32:35],a[76:79]
mm a[248:251],v[16:19],v[32:35],a[248:251]
mm a[248:251],v[20:23],v[36:39],a[248:251]
mm a[40:43],v[20:23],v[44:47],a[40:43]
mm a[40:43],v[16:19],v[40:43],a[40:43]
mm a[48:51],v[16:19],v[48:51],a[48:51]
mm a[48:51],v[20:23],v[52:55],a[48:51]
mm a[56:59],v[20:23],v[60:63],a[56:59]
mm a[56:59],v[16:19],v[56:59],a[56:59]
mm a[60:63],v[24:27],v[56:59],a[60:63]
mm a[60:63],v[28:31],v[60:63],a[60:63]
mm a[52:55],v[28:31],v[52:55],a[52:55]
mm a[52:55],v[24:27],v[48:51],a[52:55]
mm a[44:47],v[24:27],v[40:43],a[44:47]
mm a[44:47],v[28:31],v[44:47],a[44:47]
mm a[252:255],v[28:31],v[36:39],a[252:255]
mm a[252:255],v[24:27],v[32:35],a[252:255]
; ---- Q tile: 256 rows x 128 columns, K = 1024, while K / V leave the accumulators (memory + .La5_LDS ring); behind it the first step on the diagonal
.La5_LQ_START:
.La5_LW1_9:
s_cbranch_execz .La5_LW1_13
wt vmcnt(13) lgkmcnt(4)
mm v[0:3],v[192:195],v[128:131],0
ar v224,a128
ar v225,a129
s_mov_b32 s85, s95
mm v[0:3],v[196:199],v[144:147],v[0:3]
ld v[176:179],v244,s[52:55],s91 offen offset:3072 sc1
mm v[32:35],v[196:199],v[148:151],0
ar v226,a130
ar v227,a131
mm v[32:35],v[192:195],v[132:135],v[32:35]
ar v228,a132
ar v229,a133
mm v[64:67],v[192:195],v[136:139],0
ar v230,a134
ar v231,a135
mm v[64:67],v[196:199],v[152:155],v[64:67]
ld v[180:183],v245,s[52:55],s91 offen offset:3072 sc1
mm v[96:99],v[196:199],v[156:159],0
cv v248,v224,v225
cv v249,v226,v227
mm v[96:99],v[192:195],v[140:143],v[96:99]
rt v[196:197],v240 offset:128
rt v[198:199],v240 offset:1152
mm v[100:103],v[200:203],v[140:143],0
cv v250,v228,v229
cv v251,v230,v231
mm v[100:103],v[204:207],v[156:159],v[100:103]
ld v[184:187],v246,s[52:55],s91 offen offset:3072 sc1
mm v[68:71],v[204:207],v[152:155],0
aw a128,v248
aw a129,v249
mm v[68:71],v[200:203],v[136:139],v[68:71]
rt v[192:193],v240 offset:8576
rt v[194:195],v240 offset:9600
mm v[36:39],v[200:203],v[132:135],0
aw a130,v250
aw a131,v251
mm v[36:39],v[204:207],v[148:151],v[36:39]
ld v[188:191],v247,s[52:55],s91 offen offset:3072 sc1
mm v[4:7],v[204:207],v[144:147],0
wt lgkmcnt(4)
s_add_u32 s91, s91, 4096
mm v[4:7],v[200:203],v[128:131],v[4:7]
rt v[204:205],v241 offset:128
rt v[206:207],v241 offset:1152
mm v[8:11],v[208:211],v[128:131],0
s_and_b32 s97, s2, 7
s_mul_i32 s95, s97, 7
mm v[8:11],v[212:215],v[144:147],v[8:11]
ld a[96:99],v244,s[52:55],s91 offen offset:0 sc1
mm v[40:43],v[212:215],v[148:151],0
s_lshr_b32 s96, s30, 7
s_add_u32 s95, s95, s96
mm v[40:43],v[208:211],v[132:135],v[40:43]
)ASM" R"ASM(rt v[200:201],v241 offset:8576
rt v[202:203],v241 offset:9600
mm v[72:75],v[208:211],v[136:139],0
s_sub_u32 s95, 3, s95
s_and_b32 s95, s95, 7
mm v[72:75],v[212:215],v[152:155],v[72:75]
ld a[100:103],v245,s[52:55],s91 offen offset:0 sc1
mm v[104:107],v[212:215],v[156:159],0
s_min_u32 s95, s95, 6
s_lshl_b32 s95, s95, 1
mm v[104:107],v[208:211],v[140:143],v[104:107]
rt v[212:213],v242 offset:128
rt v[214:215],v242 offset:1152
mm v[108:111],v[216:219],v[140:143],0
s_lshr_b32 s92, s2, 6
s_mul_i32 s93, s92, 3
mm v[108:111],v[220:223],v[156:159],v[108:111]
ld a[104:107],v246,s[52:55],s91 offen offset:0 sc1
mm v[76:79],v[220:223],v[152:155],0
s_add_u32 s93, s93, s74
mm v[76:79],v[216:219],v[136:139],v[76:79]
rt v[208:209],v242 offset:8576
rt v[210:211],v242 offset:9600
mm v[44:47],v[216:219],v[132:135],0
ds_write_b128 v238, v[248:251] offset:3072
mm v[44:47],v[220:223],v[148:151],v[44:47]
st v[248:251],v255,s[48:51],s89 offen offset:0 sc1
mm v[12:15],v[220:223],v[144:147],0
wt lgkmcnt(5)
mm v[12:15],v[216:219],v[128:131],v[12:15]
rt v[220:221],v243 offset:128
rt v[222:223],v243 offset:1152
mm v[16:19],v[196:199],v[128:131],0
ar v224,a120
ar v225,a121
mm v[16:19],v[192:195],v[144:147],v[16:19]
ld a[108:111],v247,s[52:55],s91 offen offset:0 sc1
mm v[48:51],v[192:195],v[148:151],0
ar v226,a122
ar v227,a123
mm v[48:51],v[196:199],v[132:135],v[48:51]
rt v[216:217],v243 offset:8576
rt v[218:219],v243 offset:9600
mm v[80:83],v[196:199],v[136:139],0
ar v228,a124
ar v229,a125
mm v[80:83],v[192:195],v[152:155],v[80:83]
ld a[112:115],v244,s[52:55],s91 offen offset:1024 sc1
mm v[112:115],v[192:195],v[156:159],0
ar v230,a126
ar v231,a127
mm v[112:115],v[196:199],v[140:143],v[112:115]
cv v248,v224,v225
cv v249,v226,v227
mm v[116:119],v[204:207],v[140:143],0
wt lgkmcnt(0)
s_barrier
mm v[116:119],v[200:203],v[156:159],v[116:119]
ld a[116:119],v245,s[52:55],s91 offen offset:1024 sc1
mm v[84:87],v[200:203],v[152:155],0
rt v[192:193],v240 offset:16896
rt v[194:195],v240 offset:17920
mm v[84:87],v[204:207],v[136:139],v[84:87]
rt v[196:197],v240 offset:25344
rt v[198:199],v240 offset:26368
mm v[52:55],v[204:207],v[132:135],0
s_add_u32 m0, s83, 0
cv v250,v228,v229
mm v[52:55],v[200:203],v[148:151],v[52:55]
ld v232,s[44:47],s80 offen lds
mm v[20:23],v[200:203],v[144:147],0
cv v251,v230,v231
aw a132,v248
mm v[20:23],v[204:207],v[128:131],v[20:23]
rt v[200:201],v241 offset:16896
rt v[202:203],v241 offset:17920
mm v[24:27],v[212:215],v[128:131],0
aw a133,v249
aw a134,v250
mm v[24:27],v[208:211],v[144:147],v[24:27]
aw a135,v251
mm v[56:59],v[208:211],v[148:151],0
s_cmp_ge_u32 s93, s95
s_cselect_b32 s96, 2, 0
mm v[56:59],v[212:215],v[132:135],v[56:59]
rt v[204:205],v241 offset:25344
rt v[206:207],v241 offset:26368
mm v[88:91],v[212:215],v[136:139],0
s_add_u32 m0, s83, 1024
s_add_u32 s93, s93, s96
mm v[88:91],v[208:211],v[152:155],v[88:91]
ld v233,s[44:47],s80 offen lds
mm v[120:123],v[208:211],v[156:159],0
s_add_u32 s95, s95, s92
mm v[120:123],v[212:215],v[140:143],v[120:123]
rt v[208:209],v242 offset:16896
rt v[210:211],v242 offset:17920
mm v[124:127],v[220:223],v[140:143],0
s_cmp_lt_u32 s92, 2
s_cselect_b32 s94, s75, 0
)ASM" R"ASM(mm v[124:127],v[216:219],v[156:159],v[124:127]
ld v[140:143],v246,s[52:55],s91 offen offset:1024 sc1
mm v[92:95],v[216:219],v[152:155],0
s_cmp_eq_u32 s74, 3
s_cselect_b32 s92, s95, s93
s_cselect_b32 s94, s94, s75
mm v[92:95],v[220:223],v[136:139],v[92:95]
rt v[212:213],v242 offset:25344
rt v[214:215],v242 offset:26368
mm v[60:63],v[220:223],v[132:135],0
ds_write_b128 v238, v[248:251] offset:4096
mm v[60:63],v[216:219],v[148:151],v[60:63]
s_mul_i32 s93, s97, 14
s_add_u32 s92, s92, s93
mm v[28:31],v[216:219],v[144:147],0
wt vmcnt(10) lgkmcnt(5)
s_mul_i32 s96, s92, s94
mm v[28:31],v[220:223],v[128:131],v[28:31]
rt v[216:217],v243 offset:16896
rt v[218:219],v243 offset:17920
mm v[0:3],v[192:195],v[160:163],v[0:3]
ar v224,a136
ar v225,a137
mm v[0:3],v[196:199],v[176:179],v[0:3]
ld v[128:131],v247,s[52:55],s91 offen offset:1024 sc1
mm v[32:35],v[196:199],v[180:183],v[32:35]
ar v226,a138
ar v227,a139
mm v[32:35],v[192:195],v[164:167],v[32:35]
rt v[220:221],v243 offset:25344
rt v[222:223],v243 offset:26368
mm v[64:67],v[192:195],v[168:171],v[64:67]
ar v228,a140
ar v229,a141
mm v[64:67],v[196:199],v[184:187],v[64:67]
ld v[132:135],v244,s[52:55],s91 offen offset:2048 sc1
mm v[96:99],v[196:199],v[188:191],v[96:99]
ar v230,a142
ar v231,a143
mm v[96:99],v[192:195],v[172:175],v[96:99]
rt v[196:197],v240 offset:17024
rt v[198:199],v240 offset:18048
mm v[100:103],v[200:203],v[172:175],v[100:103]
s_add_u32 m0, s83, 2176
cv v248,v224,v225
mm v[100:103],v[204:207],v[188:191],v[100:103]
ld v234,s[44:47],s80 offen lds
mm v[68:71],v[204:207],v[184:187],v[68:71]
cv v249,v226,v227
cv v250,v228,v229
mm v[68:71],v[200:203],v[168:171],v[68:71]
rt v[192:193],v240 offset:25472
rt v[194:195],v240 offset:26496
mm v[36:39],v[200:203],v[164:167],v[36:39]
cv v251,v230,v231
aw a136,v248
mm v[36:39],v[204:207],v[180:183],v[36:39]
ld v[136:139],v245,s[52:55],s91 offen offset:2048 sc1
mm v[4:7],v[204:207],v[176:179],v[4:7]
wt lgkmcnt(4)
aw a137,v249
mm v[4:7],v[200:203],v[160:163],v[4:7]
rt v[204:205],v241 offset:17024
rt v[206:207],v241 offset:18048
mm v[8:11],v[208:211],v[160:163],v[8:11]
s_add_u32 m0, s83, 3200
aw a138,v250
mm v[8:11],v[212:215],v[176:179],v[8:11]
ld v235,s[44:47],s80 offen lds
mm v[40:43],v[212:215],v[180:183],v[40:43]
s_add_u32 s80, s80, 524288
aw a139,v251
mm v[40:43],v[208:211],v[164:167],v[40:43]
rt v[200:201],v241 offset:25472
rt v[202:203],v241 offset:26496
mm v[72:75],v[208:211],v[168:171],v[72:75]
s_mov_b32 s100, s96
s_and_b32 s93, s2, 7
mm v[72:75],v[212:215],v[184:187],v[72:75]
ld v[144:147],v246,s[52:55],s91 offen offset:2048 sc1
mm v[104:107],v[212:215],v[188:191],v[104:107]
s_lshr_b32 s95, s93, 1
s_xor_b32 s97, s95, s93
mm v[104:107],v[208:211],v[172:175],v[104:107]
rt v[212:213],v242 offset:17024
rt v[214:215],v242 offset:18048
mm v[108:111],v[216:219],v[172:175],v[108:111]
s_lshl_b32 s95, s95, 21
s_and_b32 s97, s97, 1
mm v[108:111],v[220:223],v[188:191],v[108:111]
ld v[148:151],v247,s[52:55],s91 offen offset:2048 sc1
mm v[76:79],v[220:223],v[184:187],v[76:79]
s_mov_b32 s18, 53248
s_lshl_b32 s97, s97, 10
mm v[76:79],v[216:219],v[168:171],v[76:79]
rt v[208:209],v242 offset:25472
rt v[210:211],v242 offset:26496
mm v[44:47],v[216:219],v[164:167],v[44:47]
ds_write_b128 v238, v[248:251] offset:5120
mm v[44:47],v[220:223],v[180:183],v[44:47]
)ASM" R"ASM(st a[132:135],v255,s[48:51],s18 offen offset:0 sc1
mm v[12:15],v[220:223],v[176:179],v[12:15]
wt lgkmcnt(5)
s_add_u32 s95, s95, s97
mm v[12:15],v[216:219],v[160:163],v[12:15]
rt v[220:221],v243 offset:17024
rt v[222:223],v243 offset:18048
mm v[16:19],v[196:199],v[160:163],v[16:19]
ar v224,a224
ar v225,a225
mm v[16:19],v[192:195],v[176:179],v[16:19]
ld v[152:155],v244,s[52:55],s91 offen offset:3072 sc1
mm v[48:51],v[192:195],v[180:183],v[48:51]
ar v226,a226
ar v227,a227
mm v[48:51],v[196:199],v[164:167],v[48:51]
rt v[216:217],v243 offset:25472
rt v[218:219],v243 offset:26496
mm v[80:83],v[196:199],v[168:171],v[80:83]
ar v228,a228
ar v229,a229
mm v[80:83],v[192:195],v[184:187],v[80:83]
ld v[156:159],v245,s[52:55],s91 offen offset:3072 sc1
mm v[112:115],v[192:195],v[188:191],v[112:115]
ar v230,a230
ar v231,a231
mm v[112:115],v[196:199],v[172:175],v[112:115]
cv v248,v224,v225
cv v249,v226,v227
mm v[116:119],v[204:207],v[172:175],v[116:119]
wt lgkmcnt(0)
s_barrier
mm v[116:119],v[200:203],v[188:191],v[116:119]
ld a[120:123],v246,s[52:55],s91 offen offset:3072 sc1
mm v[84:87],v[200:203],v[184:187],v[84:87]
rt v[192:193],v240 offset:33792
rt v[194:195],v240 offset:34816
mm v[84:87],v[204:207],v[168:171],v[84:87]
rt v[196:197],v240 offset:42240
rt v[198:199],v240 offset:43264
mm v[52:55],v[204:207],v[164:167],v[52:55]
s_add_u32 m0, s83, 16896
cv v250,v228,v229
mm v[52:55],v[200:203],v[180:183],v[52:55]
ld v232,s[44:47],s80 offen lds
mm v[20:23],v[200:203],v[176:179],v[20:23]
cv v251,v230,v231
aw a140,v248
mm v[20:23],v[204:207],v[160:163],v[20:23]
rt v[200:201],v241 offset:33792
rt v[202:203],v241 offset:34816
mm v[24:27],v[212:215],v[160:163],v[24:27]
aw a141,v249
aw a142,v250
mm v[24:27],v[208:211],v[176:179],v[24:27]
ld a[124:127],v247,s[52:55],s91 offen offset:3072 sc1
mm v[56:59],v[208:211],v[180:183],v[56:59]
s_add_u32 s91, s91, 4096
aw a143,v251
mm v[56:59],v[212:215],v[164:167],v[56:59]
rt v[204:205],v241 offset:42240
rt v[206:207],v241 offset:43264
mm v[88:91],v[212:215],v[168:171],v[88:91]
s_add_u32 m0, s83, 17920
s_mul_i32 s97, s93, 896
mm v[88:91],v[208:211],v[184:187],v[88:91]
ld v233,s[44:47],s80 offen lds
mm v[120:123],v[208:211],v[188:191],v[120:123]
s_cmp_eq_u32 s3, 0
s_cselect_b32 s95, s95, s97
s_cselect_b64 s[36:37], s[28:29], s[30:31]
s_cselect_b32 s21, 2, 7
s_cselect_b32 s93, s93, 0
mm v[120:123],v[212:215],v[172:175],v[120:123]
rt v[208:209],v242 offset:33792
rt v[210:211],v242 offset:34816
mm v[124:127],v[220:223],v[172:175],v[124:127]
s_cmp_eq_u32 s3, 2
s_cselect_b64 s[36:37], s[32:33], s[36:37]
mm v[124:127],v[216:219],v[188:191],v[124:127]
s_lshl_b32 s97, s97, 10
s_mov_b32 s92, 672
mm v[92:95],v[216:219],v[184:187],v[92:95]
s_mov_b32 s90, 352
mm v[92:95],v[220:223],v[168:171],v[92:95]
rt v[212:213],v242 offset:42240
rt v[214:215],v242 offset:43264
mm v[60:63],v[220:223],v[164:167],v[60:63]
ds_write_b128 v238, v[248:251] offset:6144
mm v[60:63],v[216:219],v[180:183],v[60:63]
st a[136:139],v255,s[48:51],s18 offen offset:1024 sc1
mm v[28:31],v[216:219],v[176:179],v[28:31]
wt vmcnt(14) lgkmcnt(5)
mm v[28:31],v[220:223],v[160:163],v[28:31]
rt v[216:217],v243 offset:33792
rt v[218:219],v243 offset:34816
mm v[0:3],v[192:195],a[96:99],v[0:3]
ar v224,a144
ar v225,a145
mm v[0:3],v[196:199],a[112:115],v[0:3]
)ASM" R"ASM(ld v[160:163],v244,s[52:55],s91 offen offset:0 sc1
mm v[32:35],v[196:199],a[116:119],v[32:35]
ar v226,a146
ar v227,a147
mm v[32:35],v[192:195],a[100:103],v[32:35]
rt v[220:221],v243 offset:42240
rt v[222:223],v243 offset:43264
mm v[64:67],v[192:195],a[104:107],v[64:67]
ar v228,a148
ar v229,a149
mm v[64:67],v[196:199],v[140:143],v[64:67]
ld v[164:167],v245,s[52:55],s91 offen offset:0 sc1
mm v[96:99],v[196:199],v[128:131],v[96:99]
ar v230,a150
ar v231,a151
mm v[96:99],v[192:195],a[108:111],v[96:99]
rt v[196:197],v240 offset:33920
rt v[198:199],v240 offset:34944
mm v[100:103],v[200:203],a[108:111],v[100:103]
s_add_u32 m0, s83, 19072
cv v248,v224,v225
mm v[100:103],v[204:207],v[128:131],v[100:103]
ld v234,s[44:47],s80 offen lds
mm v[68:71],v[204:207],v[140:143],v[68:71]
cv v249,v226,v227
cv v250,v228,v229
mm v[68:71],v[200:203],a[104:107],v[68:71]
rt v[192:193],v240 offset:42368
rt v[194:195],v240 offset:43392
mm v[36:39],v[200:203],a[100:103],v[36:39]
cv v251,v230,v231
aw a144,v248
mm v[36:39],v[204:207],a[116:119],v[36:39]
ld v[168:171],v246,s[52:55],s91 offen offset:0 sc1
mm v[4:7],v[204:207],a[112:115],v[4:7]
wt lgkmcnt(4)
aw a145,v249
mm v[4:7],v[200:203],a[96:99],v[4:7]
.La5_LW1_10:
s_cbranch_execz .La5_LW1_14
rt v[204:205],v241 offset:33920
rt v[206:207],v241 offset:34944
mm v[8:11],v[208:211],a[96:99],v[8:11]
s_add_u32 m0, s83, 20096
aw a146,v250
mm v[8:11],v[212:215],a[112:115],v[8:11]
ld v235,s[44:47],s80 offen lds
mm v[40:43],v[212:215],a[116:119],v[40:43]
s_add_u32 s80, s80, 524288
aw a147,v251
mm v[40:43],v[208:211],a[100:103],v[40:43]
rt v[200:201],v241 offset:42368
rt v[202:203],v241 offset:43392
mm v[72:75],v[208:211],a[104:107],v[72:75]
s_cmp_eq_u32 s3, 3
s_cselect_b32 s95, s97, s95
s_cselect_b64 s[36:37], s[34:35], s[36:37]
s_cselect_b32 s21, 1, s21
s_cselect_b32 s92, 544, s92
mm v[72:75],v[212:215],v[140:143],v[72:75]
ld v[172:175],v247,s[52:55],s91 offen offset:0 sc1
mm v[104:107],v[212:215],v[128:131],v[104:107]
s_lshl_b32 s21, s21, 10
s_and_b32 s7, s93, 1
mm v[104:107],v[208:211],a[108:111],v[104:107]
rt v[212:213],v242 offset:33920
rt v[214:215],v242 offset:34944
mm v[108:111],v[216:219],a[108:111],v[108:111]
s_cmp_eq_u32 s75, 1
s_cselect_b32 s97, s90, s92
s_cselect_b32 s92, s92, s90
mm v[108:111],v[220:223],v[128:131],v[108:111]
s_cmp_eq_u32 s75, s7
s_cselect_b32 s92, s92, 0
mm v[76:79],v[220:223],v[140:143],v[76:79]
s_mul_i32 s92, s92, s21
s_add_u32 s95, s95, s92
mm v[76:79],v[216:219],a[104:107],v[76:79]
rt v[208:209],v242 offset:42368
rt v[210:211],v242 offset:43392
mm v[44:47],v[216:219],a[100:103],v[44:47]
ds_write_b128 v238, v[248:251] offset:7168
mm v[44:47],v[220:223],a[116:119],v[44:47]
st a[140:143],v255,s[48:51],s18 offen offset:2048 sc1
mm v[12:15],v[220:223],a[112:115],v[12:15]
wt lgkmcnt(5)
s_mul_i32 s97, s97, s21
mm v[12:15],v[216:219],a[96:99],v[12:15]
rt v[220:221],v243 offset:33920
rt v[222:223],v243 offset:34944
mm v[16:19],v[196:199],a[96:99],v[16:19]
ar v224,a232
ar v225,a233
mm v[16:19],v[192:195],a[112:115],v[16:19]
ld v[176:179],v244,s[52:55],s91 offen offset:1024 sc1
mm v[48:51],v[192:195],a[116:119],v[48:51]
ar v226,a234
ar v227,a235
mm v[48:51],v[196:199],a[100:103],v[48:51]
rt v[216:217],v243 offset:42368
)ASM" R"ASM(rt v[218:219],v243 offset:43392
mm v[80:83],v[196:199],a[104:107],v[80:83]
ar v228,a236
ar v229,a237
mm v[80:83],v[192:195],v[140:143],v[80:83]
ld v[180:183],v245,s[52:55],s91 offen offset:1024 sc1
mm v[112:115],v[192:195],v[128:131],v[112:115]
ar v230,a238
ar v231,a239
mm v[112:115],v[196:199],a[108:111],v[112:115]
cv v248,v224,v225
cv v249,v226,v227
mm v[116:119],v[204:207],a[108:111],v[116:119]
wt lgkmcnt(0)
s_barrier
mm v[116:119],v[200:203],v[128:131],v[116:119]
ld v[184:187],v246,s[52:55],s91 offen offset:1024 sc1
mm v[84:87],v[200:203],v[140:143],v[84:87]
rt v[192:193],v240 offset:50688
rt v[194:195],v240 offset:51712
mm v[84:87],v[204:207],a[104:107],v[84:87]
rt v[196:197],v240 offset:59136
rt v[198:199],v240 offset:60160
mm v[52:55],v[204:207],a[100:103],v[52:55]
s_add_u32 m0, s83, 33792
cv v250,v228,v229
mm v[52:55],v[200:203],a[116:119],v[52:55]
ld v232,s[44:47],s80 offen lds
mm v[20:23],v[200:203],a[112:115],v[20:23]
cv v251,v230,v231
aw a148,v248
mm v[20:23],v[204:207],a[96:99],v[20:23]
rt v[200:201],v241 offset:50688
rt v[202:203],v241 offset:51712
mm v[24:27],v[212:215],a[96:99],v[24:27]
aw a149,v249
aw a150,v250
mm v[24:27],v[208:211],a[112:115],v[24:27]
ld v[188:191],v247,s[52:55],s91 offen offset:1024 sc1
mm v[56:59],v[208:211],a[116:119],v[56:59]
aw a151,v251
s_mul_i32 s90, s97, s7
mm v[56:59],v[212:215],a[100:103],v[56:59]
rt v[204:205],v241 offset:59136
rt v[206:207],v241 offset:60160
mm v[88:91],v[212:215],a[104:107],v[88:91]
s_add_u32 m0, s83, 34816
s_sub_u32 s97, s97, s21
mm v[88:91],v[208:211],v[140:143],v[88:91]
ld v233,s[44:47],s80 offen lds
mm v[120:123],v[208:211],v[128:131],v[120:123]
s_add_u32 s97, s97, 896
mm v[120:123],v[212:215],a[108:111],v[120:123]
rt v[208:209],v242 offset:50688
rt v[210:211],v242 offset:51712
mm v[124:127],v[220:223],a[108:111],v[124:127]
s_add_u32 s36, s36, s95
s_addc_u32 s37, s37, 0
mm v[124:127],v[216:219],v[128:131],v[124:127]
s_and_b32 s37, s37, 0xffff
s_mov_b32 s38, s97
mm v[92:95],v[216:219],v[140:143],v[92:95]
s_mov_b32 s39, 0x20000
s_lshl_b32 s93, s96, 6
mm v[92:95],v[220:223],a[104:107],v[92:95]
rt v[212:213],v242 offset:59136
rt v[214:215],v242 offset:60160
mm v[60:63],v[220:223],a[100:103],v[60:63]
ds_write_b128 v238, v[248:251] offset:32768
mm v[60:63],v[216:219],a[116:119],v[60:63]
s_mul_i32 s95, s94, 7332928
mm v[28:31],v[216:219],a[112:115],v[28:31]
wt vmcnt(15) lgkmcnt(5)
mm v[28:31],v[220:223],a[96:99],v[28:31]
rt v[216:217],v243 offset:50688
rt v[218:219],v243 offset:51712
mm v[0:3],v[192:195],v[132:135],v[0:3]
ar v224,a152
ar v225,a153
mm v[0:3],v[196:199],v[152:155],v[0:3]
ld v[128:131],v244,s[52:55],s91 offen offset:2048 sc1
mm v[32:35],v[196:199],v[156:159],v[32:35]
ar v226,a154
ar v227,a155
mm v[32:35],v[192:195],v[136:139],v[32:35]
rt v[220:221],v243 offset:59136
rt v[222:223],v243 offset:60160
mm v[64:67],v[192:195],v[144:147],v[64:67]
ar v228,a156
ar v229,a157
mm v[64:67],v[196:199],a[120:123],v[64:67]
ld v[140:143],v245,s[52:55],s91 offen offset:2048 sc1
mm v[96:99],v[196:199],a[124:127],v[96:99]
ar v230,a158
ar v231,a159
mm v[96:99],v[192:195],v[148:151],v[96:99]
rt v[196:197],v240 offset:50816
rt v[198:199],v240 offset:51840
mm v[100:103],v[200:203],v[148:151],v[100:103]
s_add_u32 m0, s83, 35968
)ASM" R"ASM(cv v248,v224,v225
mm v[100:103],v[204:207],a[124:127],v[100:103]
ld v234,s[44:47],s80 offen lds
mm v[68:71],v[204:207],a[120:123],v[68:71]
cv v249,v226,v227
cv v250,v228,v229
mm v[68:71],v[200:203],v[144:147],v[68:71]
rt v[192:193],v240 offset:59264
rt v[194:195],v240 offset:60288
mm v[36:39],v[200:203],v[136:139],v[36:39]
cv v251,v230,v231
aw a152,v248
mm v[36:39],v[204:207],v[156:159],v[36:39]
ld a[96:99],v246,s[52:55],s91 offen offset:2048 sc1
mm v[4:7],v[204:207],v[152:155],v[4:7]
wt lgkmcnt(4)
aw a153,v249
mm v[4:7],v[200:203],v[132:135],v[4:7]
rt v[204:205],v241 offset:50816
rt v[206:207],v241 offset:51840
mm v[8:11],v[208:211],v[132:135],v[8:11]
s_add_u32 m0, s83, 36992
aw a154,v250
mm v[8:11],v[212:215],v[152:155],v[8:11]
ld v235,s[44:47],s80 offen lds
mm v[40:43],v[212:215],v[156:159],v[40:43]
s_add_u32 s80, s80, 524288
aw a155,v251
mm v[40:43],v[208:211],v[136:139],v[40:43]
rt v[200:201],v241 offset:59264
rt v[202:203],v241 offset:60288
mm v[72:75],v[208:211],v[144:147],v[72:75]
s_cmp_eq_u32 s77, 0
s_cselect_b64 s[40:41], s[30:31], s[32:33]
mm v[72:75],v[212:215],a[120:123],v[72:75]
ld a[100:103],v247,s[52:55],s91 offen offset:2048 sc1
mm v[104:107],v[212:215],a[124:127],v[104:107]
s_add_u32 s40, s40, s93
s_addc_u32 s41, s41, 0
mm v[104:107],v[208:211],v[148:151],v[104:107]
rt v[212:213],v242 offset:50816
rt v[214:215],v242 offset:51840
mm v[108:111],v[216:219],v[148:151],v[108:111]
s_and_b32 s41, s41, 0xffff
s_mov_b32 s42, s95
mm v[108:111],v[220:223],a[124:127],v[108:111]
s_mov_b32 s43, 0x20000
s_lshr_b32 s92, s72, 1
mm v[76:79],v[220:223],a[120:123],v[76:79]
s_and_b32 s92, s92, 4
s_xor_b32 s92, s92, s72
mm v[76:79],v[216:219],v[144:147],v[76:79]
rt v[208:209],v242 offset:59264
rt v[210:211],v242 offset:60288
mm v[44:47],v[216:219],v[136:139],v[44:47]
ds_write_b128 v238, v[248:251] offset:33792
mm v[44:47],v[220:223],v[156:159],v[44:47]
st a[144:147],v255,s[48:51],s18 offen offset:3072 sc1
mm v[12:15],v[220:223],v[152:155],v[12:15]
wt lgkmcnt(5)
s_xor_b32 s92, s92, 1
mm v[12:15],v[216:219],v[132:135],v[12:15]
rt v[220:221],v243 offset:50816
rt v[222:223],v243 offset:51840
mm v[16:19],v[196:199],v[132:135],v[16:19]
ar v224,a208
ar v225,a209
mm v[16:19],v[192:195],v[152:155],v[16:19]
ld a[104:107],v244,s[52:55],s91 offen offset:3072 sc1
mm v[48:51],v[192:195],v[156:159],v[48:51]
ar v226,a210
ar v227,a211
mm v[48:51],v[196:199],v[136:139],v[48:51]
rt v[216:217],v243 offset:59264
rt v[218:219],v243 offset:60288
mm v[80:83],v[196:199],v[144:147],v[80:83]
ar v228,a212
ar v229,a213
mm v[80:83],v[192:195],a[120:123],v[80:83]
ld a[108:111],v245,s[52:55],s91 offen offset:3072 sc1
mm v[112:115],v[192:195],a[124:127],v[112:115]
ar v230,a214
ar v231,a215
mm v[112:115],v[196:199],v[148:151],v[112:115]
cv v248,v224,v225
cv v249,v226,v227
mm v[116:119],v[204:207],v[148:151],v[116:119]
wt lgkmcnt(0)
s_barrier
mm v[116:119],v[200:203],a[124:127],v[116:119]
ld a[112:115],v246,s[52:55],s91 offen offset:3072 sc1
mm v[84:87],v[200:203],a[120:123],v[84:87]
rt v[192:193],v240 offset:0
rt v[194:195],v240 offset:1024
mm v[84:87],v[204:207],v[144:147],v[84:87]
rt v[196:197],v240 offset:8448
rt v[198:199],v240 offset:9472
mm v[52:55],v[204:207],v[136:139],v[52:55]
s_add_u32 m0, s83, 50688
cv v250,v228,v229
)ASM" R"ASM(mm v[52:55],v[200:203],v[156:159],v[52:55]
ld v232,s[44:47],s80 offen lds
mm v[20:23],v[200:203],v[152:155],v[20:23]
cv v251,v230,v231
aw a156,v248
mm v[20:23],v[204:207],v[132:135],v[20:23]
rt v[200:201],v241 offset:0
rt v[202:203],v241 offset:1024
mm v[24:27],v[212:215],v[132:135],v[24:27]
aw a157,v249
aw a158,v250
mm v[24:27],v[208:211],v[152:155],v[24:27]
ld a[116:119],v247,s[52:55],s91 offen offset:3072 sc1
mm v[56:59],v[208:211],v[156:159],v[56:59]
s_add_u32 s91, s91, 4096
aw a159,v251
mm v[56:59],v[212:215],v[136:139],v[56:59]
rt v[204:205],v241 offset:8448
rt v[206:207],v241 offset:9472
mm v[88:91],v[212:215],v[144:147],v[88:91]
s_add_u32 m0, s83, 51712
s_and_b32 s101, s92, 7
mm v[88:91],v[208:211],a[120:123],v[88:91]
ld v233,s[44:47],s80 offen lds
mm v[120:123],v[208:211],a[124:127],v[120:123]
s_lshl_b32 s92, s101, 8
s_sub_u32 s93, 8388608, s92
mm v[120:123],v[212:215],v[148:151],v[120:123]
rt v[208:209],v242 offset:0
rt v[210:211],v242 offset:1024
mm v[124:127],v[220:223],v[148:151],v[124:127]
s_add_u32 s56, s28, s92
s_addc_u32 s57, s29, 0
mm v[124:127],v[216:219],a[124:127],v[124:127]
s_and_b32 s57, s57, 0xffff
s_mov_b32 s58, s93
mm v[92:95],v[216:219],a[120:123],v[92:95]
s_mov_b32 s18, 32768
s_mov_b32 s59, 0x20000
mm v[92:95],v[220:223],v[144:147],v[92:95]
rt v[212:213],v242 offset:8448
rt v[214:215],v242 offset:9472
mm v[60:63],v[220:223],v[136:139],v[60:63]
ds_write_b128 v238, v[248:251] offset:34816
mm v[60:63],v[216:219],v[156:159],v[60:63]
st a[148:151],v255,s[48:51],s18 offen offset:0 sc1
mm v[28:31],v[216:219],v[152:155],v[28:31]
wt vmcnt(15) lgkmcnt(5)
s_lshl_b32 s93, s96, 16
mm v[28:31],v[220:223],v[132:135],v[28:31]
rt v[216:217],v243 offset:0
rt v[218:219],v243 offset:1024
mm v[0:3],v[192:195],v[160:163],v[0:3]
ar v224,a160
ar v225,a161
mm v[0:3],v[196:199],v[176:179],v[0:3]
ld v[132:135],v244,s[52:55],s91 offen offset:0 sc1
mm v[32:35],v[196:199],v[180:183],v[32:35]
ar v226,a162
ar v227,a163
mm v[32:35],v[192:195],v[164:167],v[32:35]
rt v[220:221],v243 offset:8448
rt v[222:223],v243 offset:9472
mm v[64:67],v[192:195],v[168:171],v[64:67]
ar v228,a164
ar v229,a165
mm v[64:67],v[196:199],v[184:187],v[64:67]
ld v[136:139],v245,s[52:55],s91 offen offset:0 sc1
mm v[96:99],v[196:199],v[188:191],v[96:99]
ar v230,a166
ar v231,a167
mm v[96:99],v[192:195],v[172:175],v[96:99]
rt v[196:197],v240 offset:128
rt v[198:199],v240 offset:1152
mm v[100:103],v[200:203],v[172:175],v[100:103]
s_add_u32 m0, s83, 52864
cv v248,v224,v225
mm v[100:103],v[204:207],v[188:191],v[100:103]
ld v234,s[44:47],s80 offen lds
mm v[68:71],v[204:207],v[184:187],v[68:71]
cv v249,v226,v227
cv v250,v228,v229
mm v[68:71],v[200:203],v[168:171],v[68:71]
rt v[192:193],v240 offset:8576
rt v[194:195],v240 offset:9600
mm v[36:39],v[200:203],v[164:167],v[36:39]
cv v251,v230,v231
aw a160,v248
mm v[36:39],v[204:207],v[180:183],v[36:39]
ld v[144:147],v246,s[52:55],s91 offen offset:0 sc1
mm v[4:7],v[204:207],v[176:179],v[4:7]
wt lgkmcnt(4)
aw a161,v249
mm v[4:7],v[200:203],v[160:163],v[4:7]
rt v[204:205],v241 offset:128
rt v[206:207],v241 offset:1152
mm v[8:11],v[208:211],v[160:163],v[8:11]
s_add_u32 m0, s83, 53888
aw a162,v250
mm v[8:11],v[212:215],v[176:179],v[8:11]
)ASM" R"ASM(ld v235,s[44:47],s80 offen lds
mm v[40:43],v[212:215],v[180:183],v[40:43]
s_add_u32 s80, s80, 524288
aw a163,v251
mm v[40:43],v[208:211],v[164:167],v[40:43]
rt v[200:201],v241 offset:8576
rt v[202:203],v241 offset:9600
mm v[72:75],v[208:211],v[168:171],v[72:75]
s_lshl_b32 s95, s94, 16
mm v[72:75],v[212:215],v[184:187],v[72:75]
ld v[148:151],v247,s[52:55],s91 offen offset:0 sc1
mm v[104:107],v[212:215],v[188:191],v[104:107]
s_add_u32 s28, s34, s93
s_addc_u32 s29, s35, 0
mm v[104:107],v[208:211],v[172:175],v[104:107]
rt v[212:213],v242 offset:128
rt v[214:215],v242 offset:1152
mm v[108:111],v[216:219],v[172:175],v[108:111]
s_and_b32 s29, s29, 0xffff
s_mov_b32 s30, s95
mm v[108:111],v[220:223],v[188:191],v[108:111]
s_mov_b32 s31, 0x20000
s_lshl_b32 s93, s101, 12
mm v[76:79],v[220:223],v[184:187],v[76:79]
s_add_u32 s92, s93, 71303168
s_sub_u32 s93, 16777216, s93
mm v[76:79],v[216:219],v[168:171],v[76:79]
rt v[208:209],v242 offset:8576
rt v[210:211],v242 offset:9600
mm v[44:47],v[216:219],v[164:167],v[44:47]
ds_write_b128 v238, v[248:251] offset:35840
mm v[44:47],v[220:223],v[180:183],v[44:47]
st a[152:155],v255,s[48:51],s18 offen offset:1024 sc1
mm v[12:15],v[220:223],v[176:179],v[12:15]
wt lgkmcnt(5)
mm v[12:15],v[216:219],v[160:163],v[12:15]
rt v[220:221],v243 offset:128
rt v[222:223],v243 offset:1152
mm v[16:19],v[196:199],v[160:163],v[16:19]
ar v224,a216
ar v225,a217
mm v[16:19],v[192:195],v[176:179],v[16:19]
ld v[152:155],v244,s[52:55],s91 offen offset:1024 sc1
mm v[48:51],v[192:195],v[180:183],v[48:51]
ar v226,a218
ar v227,a219
mm v[48:51],v[196:199],v[164:167],v[48:51]
rt v[216:217],v243 offset:8576
rt v[218:219],v243 offset:9600
mm v[80:83],v[196:199],v[168:171],v[80:83]
ar v228,a220
ar v229,a221
mm v[80:83],v[192:195],v[184:187],v[80:83]
ld v[156:159],v245,s[52:55],s91 offen offset:1024 sc1
mm v[112:115],v[192:195],v[188:191],v[112:115]
ar v230,a222
ar v231,a223
mm v[112:115],v[196:199],v[172:175],v[112:115]
cv v248,v224,v225
cv v249,v226,v227
mm v[116:119],v[204:207],v[172:175],v[116:119]
wt lgkmcnt(0)
s_barrier
mm v[116:119],v[200:203],v[188:191],v[116:119]
ld a[120:123],v246,s[52:55],s91 offen offset:1024 sc1
mm v[84:87],v[200:203],v[184:187],v[84:87]
rt v[192:193],v240 offset:16896
rt v[194:195],v240 offset:17920
mm v[84:87],v[204:207],v[168:171],v[84:87]
rt v[196:197],v240 offset:25344
rt v[198:199],v240 offset:26368
mm v[52:55],v[204:207],v[164:167],v[52:55]
s_add_u32 m0, s83, 0
cv v250,v228,v229
mm v[52:55],v[200:203],v[180:183],v[52:55]
ld v232,s[44:47],s80 offen lds
mm v[20:23],v[200:203],v[176:179],v[20:23]
cv v251,v230,v231
aw a164,v248
mm v[20:23],v[204:207],v[160:163],v[20:23]
rt v[200:201],v241 offset:16896
rt v[202:203],v241 offset:17920
mm v[24:27],v[212:215],v[160:163],v[24:27]
aw a165,v249
aw a166,v250
mm v[24:27],v[208:211],v[176:179],v[24:27]
ld a[124:127],v247,s[52:55],s91 offen offset:1024 sc1
mm v[56:59],v[208:211],v[180:183],v[56:59]
aw a167,v251
mm v[56:59],v[212:215],v[164:167],v[56:59]
rt v[204:205],v241 offset:25344
rt v[206:207],v241 offset:26368
mm v[88:91],v[212:215],v[168:171],v[88:91]
s_add_u32 m0, s83, 1024
mm v[88:91],v[208:211],v[184:187],v[88:91]
ld v233,s[44:47],s80 offen lds
mm v[120:123],v[208:211],v[188:191],v[120:123]
s_add_u32 s32, s26, s92
s_addc_u32 s33, s27, 0
mm v[120:123],v[212:215],v[172:175],v[120:123]
)ASM" R"ASM(rt v[208:209],v242 offset:16896
rt v[210:211],v242 offset:17920
mm v[124:127],v[220:223],v[172:175],v[124:127]
s_and_b32 s33, s33, 0xffff
s_mov_b32 s34, s93
mm v[124:127],v[216:219],v[188:191],v[124:127]
s_mov_b32 s35, 0x20000
s_lshl2_add_u32 s92, s72, s74
mm v[92:95],v[216:219],v[184:187],v[92:95]
s_lshl2_add_u32 s92, s92, s3
s_lshl_b32 s88, s92, 8
mm v[92:95],v[220:223],v[168:171],v[92:95]
rt v[212:213],v242 offset:25344
rt v[214:215],v242 offset:26368
mm v[60:63],v[220:223],v[164:167],v[60:63]
ds_write_b128 v238, v[248:251] offset:36864
mm v[60:63],v[216:219],v[180:183],v[60:63]
st a[156:159],v255,s[48:51],s18 offen offset:2048 sc1
mm v[28:31],v[216:219],v[176:179],v[28:31]
wt vmcnt(16) lgkmcnt(5)
s_mul_i32 s93, s92, 17408
mm v[28:31],v[220:223],v[160:163],v[28:31]
rt v[216:217],v243 offset:16896
rt v[218:219],v243 offset:17920
mm v[0:3],v[192:195],v[128:131],v[0:3]
ar v224,a168
ar v225,a169
mm v[0:3],v[196:199],a[104:107],v[0:3]
ld v[160:163],v244,s[52:55],s91 offen offset:2048 sc1
mm v[32:35],v[196:199],a[108:111],v[32:35]
ar v226,a170
ar v227,a171
mm v[32:35],v[192:195],v[140:143],v[32:35]
rt v[220:221],v243 offset:25344
rt v[222:223],v243 offset:26368
mm v[64:67],v[192:195],a[96:99],v[64:67]
ar v228,a172
ar v229,a173
mm v[64:67],v[196:199],a[112:115],v[64:67]
ld v[164:167],v245,s[52:55],s91 offen offset:2048 sc1
mm v[96:99],v[196:199],a[116:119],v[96:99]
ar v230,a174
ar v231,a175
mm v[96:99],v[192:195],a[100:103],v[96:99]
rt v[196:197],v240 offset:17024
rt v[198:199],v240 offset:18048
mm v[100:103],v[200:203],a[100:103],v[100:103]
s_add_u32 m0, s83, 2176
cv v248,v224,v225
mm v[100:103],v[204:207],a[116:119],v[100:103]
ld v234,s[44:47],s80 offen lds
mm v[68:71],v[204:207],a[112:115],v[68:71]
cv v249,v226,v227
cv v250,v228,v229
mm v[68:71],v[200:203],a[96:99],v[68:71]
rt v[192:193],v240 offset:25472
rt v[194:195],v240 offset:26496
mm v[36:39],v[200:203],v[140:143],v[36:39]
cv v251,v230,v231
aw a168,v248
mm v[36:39],v[204:207],a[108:111],v[36:39]
ld v[168:171],v246,s[52:55],s91 offen offset:2048 sc1
mm v[4:7],v[204:207],a[104:107],v[4:7]
wt lgkmcnt(4)
aw a169,v249
mm v[4:7],v[200:203],v[128:131],v[4:7]
rt v[204:205],v241 offset:17024
rt v[206:207],v241 offset:18048
mm v[8:11],v[208:211],v[128:131],v[8:11]
s_add_u32 m0, s83, 3200
aw a170,v250
mm v[8:11],v[212:215],a[104:107],v[8:11]
ld v235,s[44:47],s80 offen lds
mm v[40:43],v[212:215],a[108:111],v[40:43]
s_add_u32 s80, s80, 524288
aw a171,v251
mm v[40:43],v[208:211],v[140:143],v[40:43]
rt v[200:201],v241 offset:25472
rt v[202:203],v241 offset:26496
mm v[72:75],v[208:211],a[96:99],v[72:75]
s_lshl_b32 s92, s92, 14
s_add_u32 s92, s92, 4194304
mm v[72:75],v[212:215],a[112:115],v[72:75]
.La5_LW1_11:
s_cbranch_execz .La5_LW1_15
ld v[172:175],v247,s[52:55],s91 offen offset:2048 sc1
mm v[104:107],v[212:215],a[116:119],v[104:107]
s_add_u32 s86, s26, s92
s_addc_u32 s87, s27, 0
mm v[104:107],v[208:211],a[100:103],v[104:107]
rt v[212:213],v242 offset:17024
rt v[214:215],v242 offset:18048
mm v[108:111],v[216:219],a[100:103],v[108:111]
s_and_b32 s87, s87, 0xffff
s_add_u32 s93, s93, 150994944
mm v[108:111],v[220:223],a[116:119],v[108:111]
s_add_u32 s68, s26, s93
s_addc_u32 s69, s27, 0
mm v[76:79],v[220:223],a[112:115],v[76:79]
s_and_b32 s69, s69, 0xffff
)ASM" R"ASM(s_mov_b32 s70, 17408
mm v[76:79],v[216:219],a[96:99],v[76:79]
rt v[208:209],v242 offset:25472
rt v[210:211],v242 offset:26496
mm v[44:47],v[216:219],v[140:143],v[44:47]
ds_write_b128 v238, v[248:251] offset:37888
mm v[44:47],v[220:223],a[108:111],v[44:47]
st a[160:163],v255,s[48:51],s18 offen offset:3072 sc1
mm v[12:15],v[220:223],a[104:107],v[12:15]
wt lgkmcnt(5)
s_mov_b32 s96, 153158689
mm v[12:15],v[216:219],v[128:131],v[12:15]
rt v[220:221],v243 offset:17024
rt v[222:223],v243 offset:18048
mm v[16:19],v[196:199],v[128:131],v[16:19]
ar v224,a192
ar v225,a193
mm v[16:19],v[192:195],a[104:107],v[16:19]
ld v[176:179],v244,s[52:55],s91 offen offset:3072 sc1
mm v[48:51],v[192:195],a[108:111],v[48:51]
ar v226,a194
ar v227,a195
mm v[48:51],v[196:199],v[140:143],v[48:51]
rt v[216:217],v243 offset:25472
rt v[218:219],v243 offset:26496
mm v[80:83],v[196:199],a[96:99],v[80:83]
ar v228,a196
ar v229,a197
mm v[80:83],v[192:195],a[112:115],v[80:83]
ld v[180:183],v245,s[52:55],s91 offen offset:3072 sc1
mm v[112:115],v[192:195],a[116:119],v[112:115]
ar v230,a198
ar v231,a199
mm v[112:115],v[196:199],a[100:103],v[112:115]
cv v248,v224,v225
cv v249,v226,v227
mm v[116:119],v[204:207],a[100:103],v[116:119]
wt lgkmcnt(0)
s_barrier
mm v[116:119],v[200:203],a[116:119],v[116:119]
ld v[184:187],v246,s[52:55],s91 offen offset:3072 sc1
mm v[84:87],v[200:203],a[112:115],v[84:87]
rt v[192:193],v240 offset:33792
rt v[194:195],v240 offset:34816
mm v[84:87],v[204:207],a[96:99],v[84:87]
rt v[196:197],v240 offset:42240
rt v[198:199],v240 offset:43264
mm v[52:55],v[204:207],v[140:143],v[52:55]
s_add_u32 m0, s83, 16896
cv v250,v228,v229
mm v[52:55],v[200:203],a[108:111],v[52:55]
ld v232,s[44:47],s80 offen lds
mm v[20:23],v[200:203],a[104:107],v[20:23]
cv v251,v230,v231
aw a172,v248
mm v[20:23],v[204:207],v[128:131],v[20:23]
rt v[200:201],v241 offset:33792
rt v[202:203],v241 offset:34816
mm v[24:27],v[212:215],v[128:131],v[24:27]
aw a173,v249
aw a174,v250
mm v[24:27],v[208:211],a[104:107],v[24:27]
ld v[188:191],v247,s[52:55],s91 offen offset:3072 sc1
mm v[56:59],v[208:211],a[108:111],v[56:59]
s_add_u32 s91, s91, 4096
aw a175,v251
mm v[56:59],v[212:215],v[140:143],v[56:59]
rt v[204:205],v241 offset:42240
rt v[206:207],v241 offset:43264
mm v[88:91],v[212:215],a[96:99],v[88:91]
s_add_u32 m0, s83, 17920
s_mov_b32 s97, 371265825
mm v[88:91],v[208:211],a[112:115],v[88:91]
ld v233,s[44:47],s80 offen lds
mm v[120:123],v[208:211],a[116:119],v[120:123]
s_lshl_b32 s95, s73, 3
s_lshr_b64 s[96:97], s[96:97], s95
mm v[120:123],v[212:215],a[100:103],v[120:123]
rt v[208:209],v242 offset:33792
rt v[210:211],v242 offset:34816
mm v[124:127],v[220:223],a[100:103],v[124:127]
s_and_b32 s82, s96, 255
s_lshl_b64 s[98:99], 1, s82
mm v[124:127],v[216:219],a[116:119],v[124:127]
s_lshl_b32 s96, s75, 22
s_or_b32 s95, s98, s96
mm v[92:95],v[216:219],a[112:115],v[92:95]
s_cmp_eq_u32 s73, 1
s_cselect_b32 s96, 12, 4
s_cselect_b32 s79, 0, 0xff000000
mm v[92:95],v[220:223],a[96:99],v[92:95]
rt v[212:213],v242 offset:42240
rt v[214:215],v242 offset:43264
mm v[60:63],v[220:223],v[140:143],v[60:63]
ds_write_b128 v238, v[248:251] offset:38912
mm v[60:63],v[216:219],a[108:111],v[60:63]
s_or_b32 s98, s95, s96
mm v[28:31],v[216:219],a[104:107],v[28:31]
wt vmcnt(15) lgkmcnt(5)
mm v[28:31],v[220:223],v[128:131],v[28:31]
)ASM" R"ASM(rt v[216:217],v243 offset:33792
rt v[218:219],v243 offset:34816
mm v[0:3],v[192:195],v[132:135],v[0:3]
ar v224,a176
ar v225,a177
mm v[0:3],v[196:199],v[152:155],v[0:3]
ld v[128:131],v244,s[52:55],s91 offen offset:0 sc1
mm v[32:35],v[196:199],v[156:159],v[32:35]
ar v226,a178
ar v227,a179
mm v[32:35],v[192:195],v[136:139],v[32:35]
rt v[220:221],v243 offset:42240
rt v[222:223],v243 offset:43264
mm v[64:67],v[192:195],v[144:147],v[64:67]
ar v228,a180
ar v229,a181
mm v[64:67],v[196:199],a[120:123],v[64:67]
ld v[140:143],v245,s[52:55],s91 offen offset:0 sc1
mm v[96:99],v[196:199],a[124:127],v[96:99]
ar v230,a182
ar v231,a183
mm v[96:99],v[192:195],v[148:151],v[96:99]
rt v[196:197],v240 offset:33920
rt v[198:199],v240 offset:34944
mm v[100:103],v[200:203],v[148:151],v[100:103]
s_add_u32 m0, s83, 19072
cv v248,v224,v225
mm v[100:103],v[204:207],a[124:127],v[100:103]
ld v234,s[44:47],s80 offen lds
mm v[68:71],v[204:207],a[120:123],v[68:71]
cv v249,v226,v227
cv v250,v228,v229
mm v[68:71],v[200:203],v[144:147],v[68:71]
rt v[192:193],v240 offset:42368
rt v[194:195],v240 offset:43392
mm v[36:39],v[200:203],v[136:139],v[36:39]
cv v251,v230,v231
aw a176,v248
mm v[36:39],v[204:207],v[156:159],v[36:39]
ld a[96:99],v246,s[52:55],s91 offen offset:0 sc1
mm v[4:7],v[204:207],v[152:155],v[4:7]
wt lgkmcnt(4)
aw a177,v249
mm v[4:7],v[200:203],v[132:135],v[4:7]
rt v[204:205],v241 offset:33920
rt v[206:207],v241 offset:34944
mm v[8:11],v[208:211],v[132:135],v[8:11]
s_add_u32 m0, s83, 20096
aw a178,v250
mm v[8:11],v[212:215],v[152:155],v[8:11]
ld v235,s[44:47],s80 offen lds
mm v[40:43],v[212:215],v[156:159],v[40:43]
s_add_u32 s80, s80, 524288
aw a179,v251
mm v[40:43],v[208:211],v[136:139],v[40:43]
rt v[200:201],v241 offset:42368
rt v[202:203],v241 offset:43392
mm v[72:75],v[208:211],v[144:147],v[72:75]
s_cmp_eq_u32 s74, 3
s_cselect_b32 s23, 3, 0
s_cselect_b32 s22, 0, s75
mm v[72:75],v[212:215],a[120:123],v[72:75]
ld a[100:103],v247,s[52:55],s91 offen offset:0 sc1
mm v[104:107],v[212:215],a[124:127],v[104:107]
s_lshl_b32 s95, s76, 5
s_add_u32 s95, s95, s3
mm v[104:107],v[208:211],v[148:151],v[104:107]
rt v[212:213],v242 offset:33920
rt v[214:215],v242 offset:34944
mm v[108:111],v[216:219],v[148:151],v[108:111]
s_lshl_b32 s95, s95, 5
s_add_u32 s95, s95, s72
mm v[108:111],v[220:223],a[124:127],v[108:111]
s_lshl_b32 s95, s95, 3
s_add_u32 s6, s95, 1048576
mm v[76:79],v[220:223],a[120:123],v[76:79]
s_mov_b32 s18, 36864
v_and_b32 v239, 0x70, v255
mm v[76:79],v[216:219],v[144:147],v[76:79]
rt v[208:209],v242 offset:42368
rt v[210:211],v242 offset:43392
mm v[44:47],v[216:219],v[136:139],v[44:47]
ds_write_b128 v238, v[248:251] offset:39936
mm v[44:47],v[220:223],v[156:159],v[44:47]
st a[164:167],v255,s[48:51],s18 offen offset:0 sc1
mm v[12:15],v[220:223],v[152:155],v[12:15]
wt lgkmcnt(5)
v_lshlrev_b32 v239, 6, v239
mm v[12:15],v[216:219],v[132:135],v[12:15]
rt v[220:221],v243 offset:33920
rt v[222:223],v243 offset:34944
mm v[16:19],v[196:199],v[132:135],v[16:19]
ar v224,a200
ar v225,a201
mm v[16:19],v[192:195],v[152:155],v[16:19]
ld a[104:107],v244,s[52:55],s91 offen offset:1024 sc1
mm v[48:51],v[192:195],v[156:159],v[48:51]
ar v226,a202
ar v227,a203
mm v[48:51],v[196:199],v[136:139],v[48:51]
)ASM" R"ASM(rt v[216:217],v243 offset:42368
rt v[218:219],v243 offset:43392
mm v[80:83],v[196:199],v[144:147],v[80:83]
ar v228,a204
ar v229,a205
mm v[80:83],v[192:195],a[120:123],v[80:83]
ld a[108:111],v245,s[52:55],s91 offen offset:1024 sc1
mm v[112:115],v[192:195],a[124:127],v[112:115]
ar v230,a206
ar v231,a207
mm v[112:115],v[196:199],v[148:151],v[112:115]
cv v248,v224,v225
cv v249,v226,v227
mm v[116:119],v[204:207],v[148:151],v[116:119]
wt lgkmcnt(0)
s_barrier
mm v[116:119],v[200:203],a[124:127],v[116:119]
ld a[112:115],v246,s[52:55],s91 offen offset:1024 sc1
mm v[84:87],v[200:203],a[120:123],v[84:87]
rt v[192:193],v240 offset:50688
rt v[194:195],v240 offset:51712
mm v[84:87],v[204:207],v[144:147],v[84:87]
rt v[196:197],v240 offset:59136
rt v[198:199],v240 offset:60160
mm v[52:55],v[204:207],v[136:139],v[52:55]
s_add_u32 m0, s83, 33792
cv v250,v228,v229
mm v[52:55],v[200:203],v[156:159],v[52:55]
ld v232,s[44:47],s80 offen lds
mm v[20:23],v[200:203],v[152:155],v[20:23]
cv v251,v230,v231
aw a180,v248
mm v[20:23],v[204:207],v[132:135],v[20:23]
rt v[200:201],v241 offset:50688
rt v[202:203],v241 offset:51712
mm v[24:27],v[212:215],v[132:135],v[24:27]
aw a181,v249
aw a182,v250
mm v[24:27],v[208:211],v[152:155],v[24:27]
ld a[116:119],v247,s[52:55],s91 offen offset:1024 sc1
mm v[56:59],v[208:211],v[156:159],v[56:59]
aw a183,v251
s_add_u32 s85, s85, 65536
mm v[56:59],v[212:215],v[136:139],v[56:59]
rt v[204:205],v241 offset:59136
rt v[206:207],v241 offset:60160
mm v[88:91],v[212:215],v[144:147],v[88:91]
s_add_u32 m0, s83, 34816
v_add_u32 v238, s85, v255
mm v[88:91],v[208:211],a[120:123],v[88:91]
ld v233,s[44:47],s80 offen lds
mm v[120:123],v[208:211],a[124:127],v[120:123]
s_mov_b32 s85, 0
aw a231,v239
mm v[120:123],v[212:215],v[148:151],v[120:123]
rt v[208:209],v242 offset:50688
rt v[210:211],v242 offset:51712
mm v[124:127],v[220:223],v[148:151],v[124:127]
s_mul_i32 s95, s75, 0
s_add_u32 s95, s95, 2
mm v[124:127],v[216:219],a[124:127],v[124:127]
s_sub_u32 s96, 4, s95
s_lshr_b32 s97, s2, 4
mm v[92:95],v[216:219],a[120:123],v[92:95]
s_mul_i32 s97, s97, s95
v_lshrrev_b32 v239, 7, v255
mm v[92:95],v[220:223],v[144:147],v[92:95]
rt v[212:213],v242 offset:59136
rt v[214:215],v242 offset:60160
mm v[60:63],v[220:223],v[136:139],v[60:63]
ds_write_b128 v238, v[248:251] offset:0
mm v[60:63],v[216:219],v[156:159],v[60:63]
st a[168:171],v255,s[48:51],s18 offen offset:1024 sc1
mm v[28:31],v[216:219],v[152:155],v[28:31]
wt vmcnt(15) lgkmcnt(5)
v_add_u32 v254, s97, v239
mm v[28:31],v[220:223],v[132:135],v[28:31]
rt v[216:217],v243 offset:50688
rt v[218:219],v243 offset:51712
mm v[0:3],v[192:195],v[160:163],v[0:3]
ar v224,a184
ar v225,a185
mm v[0:3],v[196:199],v[176:179],v[0:3]
ld v[132:135],v244,s[52:55],s91 offen offset:2048 sc1
mm v[32:35],v[196:199],v[180:183],v[32:35]
ar v226,a186
ar v227,a187
mm v[32:35],v[192:195],v[164:167],v[32:35]
rt v[220:221],v243 offset:59136
rt v[222:223],v243 offset:60160
mm v[64:67],v[192:195],v[168:171],v[64:67]
ar v228,a188
ar v229,a189
mm v[64:67],v[196:199],v[184:187],v[64:67]
ld v[136:139],v245,s[52:55],s91 offen offset:2048 sc1
mm v[96:99],v[196:199],v[188:191],v[96:99]
ar v230,a190
ar v231,a191
mm v[96:99],v[192:195],v[172:175],v[96:99]
rt v[196:197],v240 offset:50816
)ASM" R"ASM(rt v[198:199],v240 offset:51840
mm v[100:103],v[200:203],v[172:175],v[100:103]
s_add_u32 m0, s83, 35968
cv v248,v224,v225
mm v[100:103],v[204:207],v[188:191],v[100:103]
ld v234,s[44:47],s80 offen lds
mm v[68:71],v[204:207],v[184:187],v[68:71]
cv v249,v226,v227
cv v250,v228,v229
mm v[68:71],v[200:203],v[168:171],v[68:71]
rt v[192:193],v240 offset:59264
rt v[194:195],v240 offset:60288
mm v[36:39],v[200:203],v[164:167],v[36:39]
cv v251,v230,v231
aw a184,v248
mm v[36:39],v[204:207],v[180:183],v[36:39]
ld v[144:147],v246,s[52:55],s91 offen offset:2048 sc1
mm v[4:7],v[204:207],v[176:179],v[4:7]
wt lgkmcnt(4)
aw a185,v249
mm v[4:7],v[200:203],v[160:163],v[4:7]
rt v[204:205],v241 offset:50816
rt v[206:207],v241 offset:51840
mm v[8:11],v[208:211],v[160:163],v[8:11]
s_add_u32 m0, s83, 36992
aw a186,v250
mm v[8:11],v[212:215],v[176:179],v[8:11]
ld v235,s[44:47],s80 offen lds
mm v[40:43],v[212:215],v[180:183],v[40:43]
s_add_u32 s80, s80, 524288
aw a187,v251
mm v[40:43],v[208:211],v[164:167],v[40:43]
rt v[200:201],v241 offset:59264
rt v[202:203],v241 offset:60288
mm v[72:75],v[208:211],v[168:171],v[72:75]
v_mul_u32_u24 v254, s21, v254
v_add_u32 v239, s96, v239
mm v[72:75],v[212:215],v[184:187],v[72:75]
ld v[148:151],v247,s[52:55],s91 offen offset:2048 sc1
mm v[104:107],v[212:215],v[188:191],v[104:107]
v_lshrrev_b32 v239, 2, v239
v_lshl_add_u32 v254, v239, 28, v254
mm v[104:107],v[208:211],v[172:175],v[104:107]
rt v[212:213],v242 offset:50816
rt v[214:215],v242 offset:51840
mm v[108:111],v[216:219],v[172:175],v[108:111]
v_and_b32 v239, 0x70, v255
v_lshl_add_u32 v254, v239, 3, v254
mm v[108:111],v[220:223],v[188:191],v[108:111]
s_lshl_b32 s96, s7, 5
s_sub_u32 s96, 16, s96
mm v[76:79],v[220:223],v[184:187],v[76:79]
s_mul_i32 s95, s95, s96
s_mul_i32 s21, s21, s95
mm v[76:79],v[216:219],v[168:171],v[76:79]
rt v[208:209],v242 offset:59264
rt v[210:211],v242 offset:60288
mm v[44:47],v[216:219],v[164:167],v[44:47]
ds_write_b128 v238, v[248:251] offset:1024
mm v[44:47],v[220:223],v[180:183],v[44:47]
st a[172:175],v255,s[48:51],s18 offen offset:2048 sc1
mm v[12:15],v[220:223],v[176:179],v[12:15]
wt lgkmcnt(5)
v_lshrrev_b32 v236, 8, v255
mm v[12:15],v[216:219],v[160:163],v[12:15]
rt v[220:221],v243 offset:50816
rt v[222:223],v243 offset:51840
mm v[16:19],v[196:199],v[160:163],v[16:19]
ar v224,a64
ar v225,a65
mm v[16:19],v[192:195],v[176:179],v[16:19]
ld v[152:155],v244,s[52:55],s91 offen offset:3072 sc1
mm v[48:51],v[192:195],v[180:183],v[48:51]
ar v226,a66
ar v227,a67
mm v[48:51],v[196:199],v[164:167],v[48:51]
rt v[216:217],v243 offset:59264
rt v[218:219],v243 offset:60288
mm v[80:83],v[196:199],v[168:171],v[80:83]
ar v228,a68
ar v229,a69
mm v[80:83],v[192:195],v[184:187],v[80:83]
ld v[156:159],v245,s[52:55],s91 offen offset:3072 sc1
mm v[112:115],v[192:195],v[188:191],v[112:115]
ar v230,a70
ar v231,a71
mm v[112:115],v[196:199],v[172:175],v[112:115]
cv v248,v224,v225
cv v249,v226,v227
mm v[116:119],v[204:207],v[172:175],v[116:119]
wt lgkmcnt(0)
s_barrier
mm v[116:119],v[200:203],v[188:191],v[116:119]
ld a[120:123],v246,s[52:55],s91 offen offset:3072 sc1
mm v[84:87],v[200:203],v[184:187],v[84:87]
rt v[192:193],v240 offset:0
rt v[194:195],v240 offset:1024
mm v[84:87],v[204:207],v[168:171],v[84:87]
rt v[196:197],v240 offset:8448
rt v[198:199],v240 offset:9472
)ASM" R"ASM(mm v[52:55],v[204:207],v[164:167],v[52:55]
s_add_u32 m0, s83, 50688
cv v250,v228,v229
mm v[52:55],v[200:203],v[180:183],v[52:55]
ld v232,s[44:47],s80 offen lds
mm v[20:23],v[200:203],v[176:179],v[20:23]
cv v251,v230,v231
aw a188,v248
mm v[20:23],v[204:207],v[160:163],v[20:23]
rt v[200:201],v241 offset:0
rt v[202:203],v241 offset:1024
mm v[24:27],v[212:215],v[160:163],v[24:27]
aw a189,v249
aw a190,v250
mm v[24:27],v[208:211],v[176:179],v[24:27]
ld a[124:127],v247,s[52:55],s91 offen offset:3072 sc1
mm v[56:59],v[208:211],v[180:183],v[56:59]
s_add_u32 s91, s91, 4096
aw a191,v251
mm v[56:59],v[212:215],v[164:167],v[56:59]
rt v[204:205],v241 offset:8448
rt v[206:207],v241 offset:9472
mm v[88:91],v[212:215],v[168:171],v[88:91]
s_add_u32 m0, s83, 51712
v_lshlrev_b32 v239, 5, v236
mm v[88:91],v[208:211],v[184:187],v[88:91]
ld v233,s[44:47],s80 offen lds
mm v[120:123],v[208:211],v[188:191],v[120:123]
v_xor_b32 v239, v239, v255
v_and_b32 v239, 0xf0, v239
mm v[120:123],v[212:215],v[172:175],v[120:123]
rt v[208:209],v242 offset:0
rt v[210:211],v242 offset:1024
mm v[124:127],v[220:223],v[172:175],v[124:127]
v_lshl_add_u32 v239, v236, 11, v239
s_lshl_b32 s92, s77, 16
mm v[124:127],v[216:219],v[188:191],v[124:127]
s_lshl_b32 s93, s78, 14
s_add_u32 s92, s92, s93
mm v[92:95],v[216:219],v[184:187],v[92:95]
v_add_u32 v239, s92, v239
aw a228,v239
mm v[92:95],v[220:223],v[168:171],v[92:95]
rt v[212:213],v242 offset:8448
rt v[214:215],v242 offset:9472
mm v[60:63],v[220:223],v[164:167],v[60:63]
ds_write_b128 v238, v[248:251] offset:2048
mm v[60:63],v[216:219],v[180:183],v[60:63]
st a[176:179],v255,s[48:51],s18 offen offset:3072 sc1
mm v[28:31],v[216:219],v[176:179],v[28:31]
wt vmcnt(16) lgkmcnt(5)
v_bfe_u32 v239, v255, 6, 2
mm v[28:31],v[220:223],v[160:163],v[28:31]
rt v[216:217],v243 offset:0
rt v[218:219],v243 offset:1024
mm v[0:3],v[192:195],v[128:131],v[0:3]
ar v224,a0
ar v225,a1
mm v[0:3],v[196:199],a[104:107],v[0:3]
ld v[160:163],v244,s[52:55],s91 offen offset:0 sc1
mm v[32:35],v[196:199],a[108:111],v[32:35]
ar v226,a2
ar v227,a3
mm v[32:35],v[192:195],v[140:143],v[32:35]
rt v[220:221],v243 offset:8448
rt v[222:223],v243 offset:9472
mm v[64:67],v[192:195],a[96:99],v[64:67]
ar v228,a4
ar v229,a5
mm v[64:67],v[196:199],a[112:115],v[64:67]
ld v[164:167],v245,s[52:55],s91 offen offset:0 sc1
mm v[96:99],v[196:199],a[116:119],v[96:99]
ar v230,a6
ar v231,a7
mm v[96:99],v[192:195],a[100:103],v[96:99]
rt v[196:197],v240 offset:128
rt v[198:199],v240 offset:1152
mm v[100:103],v[200:203],a[100:103],v[100:103]
s_add_u32 m0, s83, 52864
cv v248,v224,v225
mm v[100:103],v[204:207],a[116:119],v[100:103]
ld v234,s[44:47],s80 offen lds
mm v[68:71],v[204:207],a[112:115],v[68:71]
cv v249,v226,v227
cv v250,v228,v229
mm v[68:71],v[200:203],a[96:99],v[68:71]
rt v[192:193],v240 offset:8576
rt v[194:195],v240 offset:9600
mm v[36:39],v[200:203],v[140:143],v[36:39]
cv v251,v230,v231
aw a0,v248
mm v[36:39],v[204:207],a[108:111],v[36:39]
ld v[168:171],v246,s[52:55],s91 offen offset:0 sc1
mm v[4:7],v[204:207],a[104:107],v[4:7]
wt lgkmcnt(4)
aw a1,v249
mm v[4:7],v[200:203],v[128:131],v[4:7]
rt v[204:205],v241 offset:128
rt v[206:207],v241 offset:1152
mm v[8:11],v[208:211],v[128:131],v[8:11]
)ASM" R"ASM(s_add_u32 m0, s83, 53888
aw a2,v250
mm v[8:11],v[212:215],a[104:107],v[8:11]
ld v235,s[44:47],s80 offen lds
mm v[40:43],v[212:215],a[108:111],v[40:43]
s_add_u32 s80, s80, 524288
aw a3,v251
mm v[40:43],v[208:211],v[140:143],v[40:43]
rt v[200:201],v241 offset:8576
rt v[202:203],v241 offset:9600
mm v[72:75],v[208:211],a[96:99],v[72:75]
v_lshlrev_b32 v239, 6, v239
v_lshl_add_u32 v239, v236, 4, v239
mm v[72:75],v[212:215],a[112:115],v[72:75]
ld v[172:175],v247,s[52:55],s91 offen offset:0 sc1
mm v[104:107],v[212:215],a[116:119],v[104:107]
s_lshl_b32 s92, s78, 8
v_add_u32 v239, s92, v239
mm v[104:107],v[208:211],a[100:103],v[104:107]
rt v[212:213],v242 offset:128
rt v[214:215],v242 offset:1152
mm v[108:111],v[216:219],a[100:103],v[108:111]
v_mul_u32_u24 v239, 7168, v239
v_and_or_b32 v239, v255, 48, v239
mm v[108:111],v[220:223],a[116:119],v[108:111]
aw a229,v239
v_and_b32 v239, 0xf0, v255
mm v[76:79],v[220:223],a[112:115],v[76:79]
.La5_LW1_12:
s_cbranch_execz .La5_LW1_R0
s_mov_b32 s18, 16384
v_lshl_add_u32 v239, v236, 14, v239
mm v[76:79],v[216:219],a[96:99],v[76:79]
rt v[208:209],v242 offset:8576
rt v[210:211],v242 offset:9600
mm v[44:47],v[216:219],v[140:143],v[44:47]
ds_write_b128 v238, v[248:251] offset:3072
mm v[44:47],v[220:223],a[108:111],v[44:47]
st a[180:183],v255,s[48:51],s18 offen offset:0 sc1
mm v[12:15],v[220:223],a[104:107],v[12:15]
wt lgkmcnt(5)
s_lshl_b32 s92, s3, 9
mm v[12:15],v[216:219],v[128:131],v[12:15]
rt v[220:221],v243 offset:128
rt v[222:223],v243 offset:1152
mm v[16:19],v[196:199],v[128:131],v[16:19]
ar v224,a72
ar v225,a73
mm v[16:19],v[192:195],a[104:107],v[16:19]
ld v[176:179],v244,s[52:55],s91 offen offset:1024 sc1
mm v[48:51],v[192:195],a[108:111],v[48:51]
ar v226,a74
ar v227,a75
mm v[48:51],v[196:199],v[140:143],v[48:51]
rt v[216:217],v243 offset:8576
rt v[218:219],v243 offset:9600
mm v[80:83],v[196:199],a[96:99],v[80:83]
ar v228,a76
ar v229,a77
mm v[80:83],v[192:195],a[112:115],v[80:83]
ld v[180:183],v245,s[52:55],s91 offen offset:1024 sc1
mm v[112:115],v[192:195],a[116:119],v[112:115]
ar v230,a78
ar v231,a79
mm v[112:115],v[196:199],a[100:103],v[112:115]
cv v248,v224,v225
cv v249,v226,v227
mm v[116:119],v[204:207],a[100:103],v[116:119]
wt lgkmcnt(0)
s_barrier
mm v[116:119],v[200:203],a[116:119],v[116:119]
ld v[184:187],v246,s[52:55],s91 offen offset:1024 sc1
mm v[84:87],v[200:203],a[112:115],v[84:87]
rt v[192:193],v240 offset:16896
rt v[194:195],v240 offset:17920
mm v[84:87],v[204:207],a[96:99],v[84:87]
rt v[196:197],v240 offset:25344
rt v[198:199],v240 offset:26368
mm v[52:55],v[204:207],v[140:143],v[52:55]
s_add_u32 m0, s83, 0
cv v250,v228,v229
mm v[52:55],v[200:203],a[108:111],v[52:55]
ld v232,s[44:47],s80 offen lds
mm v[20:23],v[200:203],a[104:107],v[20:23]
cv v251,v230,v231
aw a4,v248
mm v[20:23],v[204:207],v[128:131],v[20:23]
rt v[200:201],v241 offset:16896
rt v[202:203],v241 offset:17920
mm v[24:27],v[212:215],v[128:131],v[24:27]
aw a5,v249
aw a6,v250
mm v[24:27],v[208:211],a[104:107],v[24:27]
ld v[188:191],v247,s[52:55],s91 offen offset:1024 sc1
mm v[56:59],v[208:211],a[108:111],v[56:59]
aw a7,v251
v_add_u32 v239, s92, v239
mm v[56:59],v[212:215],v[140:143],v[56:59]
rt v[204:205],v241 offset:25344
rt v[206:207],v241 offset:26368
)ASM" R"ASM(mm v[88:91],v[212:215],a[96:99],v[88:91]
s_add_u32 m0, s83, 1024
aw a230,v239
mm v[88:91],v[208:211],a[112:115],v[88:91]
ld v233,s[44:47],s80 offen lds
mm v[120:123],v[208:211],a[116:119],v[120:123]
v_add_u32 v239, 4294886912, v240
aw a224,v239
mm v[120:123],v[212:215],a[100:103],v[120:123]
rt v[208:209],v242 offset:16896
rt v[210:211],v242 offset:17920
mm v[124:127],v[220:223],a[100:103],v[124:127]
v_bfe_u32 v239, v255, 4, 4
v_lshlrev_b32 v236, 2, v236
mm v[124:127],v[216:219],a[116:119],v[124:127]
v_sub_u32 v239, v239, v236
v_subrev_u32 v236, 0, v239
mm v[92:95],v[216:219],a[112:115],v[92:95]
v_ashrrev_i32 v236, 31, v236
v_and_b32 v236, 0xff800000, v236
mm v[92:95],v[220:223],a[96:99],v[92:95]
rt v[212:213],v242 offset:25344
rt v[214:215],v242 offset:26368
mm v[60:63],v[220:223],v[140:143],v[60:63]
ds_write_b128 v238, v[248:251] offset:4096
mm v[60:63],v[216:219],a[108:111],v[60:63]
aw a236,v236
v_subrev_u32 v236, 1, v239
mm v[28:31],v[216:219],a[104:107],v[28:31]
wt vmcnt(15) lgkmcnt(5)
v_ashrrev_i32 v236, 31, v236
mm v[28:31],v[220:223],v[128:131],v[28:31]
rt v[216:217],v243 offset:16896
rt v[218:219],v243 offset:17920
mm v[0:3],v[192:195],v[132:135],v[0:3]
ar v224,a8
ar v225,a9
mm v[0:3],v[196:199],v[152:155],v[0:3]
ld v[128:131],v244,s[52:55],s91 offen offset:2048 sc1
mm v[32:35],v[196:199],v[156:159],v[32:35]
ar v226,a10
ar v227,a11
mm v[32:35],v[192:195],v[136:139],v[32:35]
rt v[220:221],v243 offset:25344
rt v[222:223],v243 offset:26368
mm v[64:67],v[192:195],v[144:147],v[64:67]
ar v228,a12
ar v229,a13
mm v[64:67],v[196:199],a[120:123],v[64:67]
ld v[140:143],v245,s[52:55],s91 offen offset:2048 sc1
mm v[96:99],v[196:199],a[124:127],v[96:99]
ar v230,a14
ar v231,a15
mm v[96:99],v[192:195],v[148:151],v[96:99]
rt v[196:197],v240 offset:17024
rt v[198:199],v240 offset:18048
mm v[100:103],v[200:203],v[148:151],v[100:103]
s_add_u32 m0, s83, 2176
cv v248,v224,v225
mm v[100:103],v[204:207],a[124:127],v[100:103]
ld v234,s[44:47],s80 offen lds
mm v[68:71],v[204:207],a[120:123],v[68:71]
cv v249,v226,v227
cv v250,v228,v229
mm v[68:71],v[200:203],v[144:147],v[68:71]
rt v[192:193],v240 offset:25472
rt v[194:195],v240 offset:26496
mm v[36:39],v[200:203],v[136:139],v[36:39]
cv v251,v230,v231
aw a8,v248
mm v[36:39],v[204:207],v[156:159],v[36:39]
ld a[96:99],v246,s[52:55],s91 offen offset:2048 sc1
mm v[4:7],v[204:207],v[152:155],v[4:7]
wt lgkmcnt(4)
aw a9,v249
mm v[4:7],v[200:203],v[132:135],v[4:7]
rt v[204:205],v241 offset:17024
rt v[206:207],v241 offset:18048
mm v[8:11],v[208:211],v[132:135],v[8:11]
s_add_u32 m0, s83, 3200
aw a10,v250
mm v[8:11],v[212:215],v[152:155],v[8:11]
ld v235,s[44:47],s80 offen lds
mm v[40:43],v[212:215],v[156:159],v[40:43]
s_add_u32 s80, s80, 524288
aw a11,v251
mm v[40:43],v[208:211],v[136:139],v[40:43]
rt v[200:201],v241 offset:25472
rt v[202:203],v241 offset:26496
mm v[72:75],v[208:211],v[144:147],v[72:75]
v_and_b32 v236, 0xff800000, v236
aw a237,v236
mm v[72:75],v[212:215],a[120:123],v[72:75]
ld a[100:103],v247,s[52:55],s91 offen offset:2048 sc1
mm v[104:107],v[212:215],a[124:127],v[104:107]
v_subrev_u32 v236, 2, v239
v_ashrrev_i32 v236, 31, v236
mm v[104:107],v[208:211],v[148:151],v[104:107]
rt v[212:213],v242 offset:17024
rt v[214:215],v242 offset:18048
)ASM" R"ASM(mm v[108:111],v[216:219],v[148:151],v[108:111]
v_and_b32 v236, 0xff800000, v236
aw a238,v236
mm v[108:111],v[220:223],a[124:127],v[108:111]
v_subrev_u32 v236, 3, v239
v_ashrrev_i32 v236, 31, v236
mm v[76:79],v[220:223],a[120:123],v[76:79]
v_and_b32 v236, 0xff800000, v236
aw a239,v236
mm v[76:79],v[216:219],v[144:147],v[76:79]
rt v[208:209],v242 offset:25472
rt v[210:211],v242 offset:26496
mm v[44:47],v[216:219],v[136:139],v[44:47]
ds_write_b128 v238, v[248:251] offset:5120
mm v[44:47],v[220:223],v[156:159],v[44:47]
st a[184:187],v255,s[48:51],s18 offen offset:1024 sc1
mm v[12:15],v[220:223],v[152:155],v[12:15]
wt lgkmcnt(5)
v_lshlrev_b32 v236, 4, v239
mm v[12:15],v[216:219],v[132:135],v[12:15]
rt v[220:221],v243 offset:17024
rt v[222:223],v243 offset:18048
mm v[16:19],v[196:199],v[132:135],v[16:19]
ar v224,a80
ar v225,a81
mm v[16:19],v[192:195],v[152:155],v[16:19]
ld a[104:107],v244,s[52:55],s91 offen offset:3072 sc1
mm v[48:51],v[192:195],v[156:159],v[48:51]
ar v226,a82
ar v227,a83
mm v[48:51],v[196:199],v[136:139],v[48:51]
rt v[216:217],v243 offset:25472
rt v[218:219],v243 offset:26496
mm v[80:83],v[196:199],v[144:147],v[80:83]
ar v228,a84
ar v229,a85
mm v[80:83],v[192:195],a[120:123],v[80:83]
ld a[108:111],v245,s[52:55],s91 offen offset:3072 sc1
mm v[112:115],v[192:195],a[124:127],v[112:115]
ar v230,a86
ar v231,a87
mm v[112:115],v[196:199],v[148:151],v[112:115]
cv v248,v224,v225
cv v249,v226,v227
mm v[116:119],v[204:207],v[148:151],v[116:119]
wt lgkmcnt(0)
s_barrier
mm v[116:119],v[200:203],a[124:127],v[116:119]
ld a[112:115],v246,s[52:55],s91 offen offset:3072 sc1
mm v[84:87],v[200:203],a[120:123],v[84:87]
rt v[192:193],v240 offset:33792
rt v[194:195],v240 offset:34816
mm v[84:87],v[204:207],v[144:147],v[84:87]
rt v[196:197],v240 offset:42240
rt v[198:199],v240 offset:43264
mm v[52:55],v[204:207],v[136:139],v[52:55]
s_add_u32 m0, s83, 16896
cv v250,v228,v229
mm v[52:55],v[200:203],v[156:159],v[52:55]
ld v232,s[44:47],s80 offen lds
mm v[20:23],v[200:203],v[152:155],v[20:23]
cv v251,v230,v231
aw a12,v248
mm v[20:23],v[204:207],v[132:135],v[20:23]
rt v[200:201],v241 offset:33792
rt v[202:203],v241 offset:34816
mm v[24:27],v[212:215],v[132:135],v[24:27]
aw a13,v249
aw a14,v250
mm v[24:27],v[208:211],v[152:155],v[24:27]
ld a[116:119],v247,s[52:55],s91 offen offset:3072 sc1
mm v[56:59],v[208:211],v[156:159],v[56:59]
s_add_u32 s91, s91, 4096
aw a15,v251
mm v[56:59],v[212:215],v[136:139],v[56:59]
rt v[204:205],v241 offset:42240
rt v[206:207],v241 offset:43264
mm v[88:91],v[212:215],v[144:147],v[88:91]
s_add_u32 m0, s83, 17920
s_mov_b32 s92, 0x3f80
mm v[88:91],v[208:211],a[120:123],v[88:91]
ld v233,s[44:47],s80 offen lds
mm v[120:123],v[208:211],a[124:127],v[120:123]
v_lshlrev_b32 v236, v236, s92
v_cmp_gt_u32 vcc, 2, v239
mm v[120:123],v[212:215],v[148:151],v[120:123]
rt v[208:209],v242 offset:33792
rt v[210:211],v242 offset:34816
mm v[124:127],v[220:223],v[148:151],v[124:127]
v_subrev_u32 v239, 2, v239
v_cmp_gt_u32 s[92:93], 2, v239
mm v[124:127],v[216:219],a[124:127],v[124:127]
v_cndmask_b32 v239, 0, v236, vcc
aw a212,0
mm v[92:95],v[216:219],a[120:123],v[92:95]
aw a214,v239
v_cndmask_b32 v239, 0, v236, s[92:93]
mm v[92:95],v[220:223],v[144:147],v[92:95]
rt v[212:213],v242 offset:42240
rt v[214:215],v242 offset:43264
)ASM" R"ASM(mm v[60:63],v[220:223],v[136:139],v[60:63]
v_mov_b32 v236, 0x3f803f80
aw a215,v239
mm v[60:63],v[216:219],v[156:159],v[60:63]
st a[188:191],v255,s[48:51],s18 offen offset:2048 sc1
mm v[28:31],v[216:219],v[152:155],v[28:31]
wt vmcnt(15) lgkmcnt(4)
aw a213,0
mm v[28:31],v[220:223],v[132:135],v[28:31]
rt v[216:217],v243 offset:33792
rt v[218:219],v243 offset:34816
mm v[0:3],v[192:195],v[160:163],v[0:3]
ar v224,a16
ar v225,a17
mm v[0:3],v[196:199],v[176:179],v[0:3]
ld v[132:135],v244,s[52:55],s91 offen offset:0 sc1
mm v[32:35],v[196:199],v[180:183],v[32:35]
ar v226,a18
ar v227,a19
mm v[32:35],v[192:195],v[164:167],v[32:35]
rt v[220:221],v243 offset:42240
rt v[222:223],v243 offset:43264
mm v[64:67],v[192:195],v[168:171],v[64:67]
ar v228,a20
ar v229,a21
mm v[64:67],v[196:199],v[184:187],v[64:67]
ld v[136:139],v245,s[52:55],s91 offen offset:0 sc1
mm v[96:99],v[196:199],v[188:191],v[96:99]
ar v230,a22
ar v231,a23
mm v[96:99],v[192:195],v[172:175],v[96:99]
rt v[196:197],v240 offset:33920
rt v[198:199],v240 offset:34944
mm v[100:103],v[200:203],v[172:175],v[100:103]
s_add_u32 m0, s83, 19072
cv v248,v224,v225
mm v[100:103],v[204:207],v[188:191],v[100:103]
ld v234,s[44:47],s80 offen lds
mm v[68:71],v[204:207],v[184:187],v[68:71]
cv v249,v226,v227
cv v250,v228,v229
mm v[68:71],v[200:203],v[168:171],v[68:71]
rt v[192:193],v240 offset:42368
rt v[194:195],v240 offset:43392
mm v[36:39],v[200:203],v[164:167],v[36:39]
cv v251,v230,v231
aw a16,v248
mm v[36:39],v[204:207],v[180:183],v[36:39]
ld v[144:147],v246,s[52:55],s91 offen offset:0 sc1
mm v[4:7],v[204:207],v[176:179],v[4:7]
wt lgkmcnt(4)
aw a17,v249
mm v[4:7],v[200:203],v[160:163],v[4:7]
rt v[204:205],v241 offset:33920
rt v[206:207],v241 offset:34944
mm v[8:11],v[208:211],v[160:163],v[8:11]
s_add_u32 m0, s83, 20096
aw a18,v250
mm v[8:11],v[212:215],v[176:179],v[8:11]
ld v235,s[44:47],s80 offen lds
mm v[40:43],v[212:215],v[180:183],v[40:43]
s_add_u32 s80, s80, 524288
aw a19,v251
mm v[40:43],v[208:211],v[164:167],v[40:43]
rt v[200:201],v241 offset:42368
rt v[202:203],v241 offset:43392
mm v[72:75],v[208:211],v[168:171],v[72:75]
aw a216,0
aw a217,0
mm v[72:75],v[212:215],v[184:187],v[72:75]
ld v[148:151],v247,s[52:55],s91 offen offset:0 sc1
mm v[104:107],v[212:215],v[188:191],v[104:107]
v_lshrrev_b32 v239, 4, v255
v_xor_b32 v239, v239, v255
mm v[104:107],v[208:211],v[172:175],v[104:107]
rt v[212:213],v242 offset:33920
rt v[214:215],v242 offset:34944
mm v[108:111],v[216:219],v[172:175],v[108:111]
v_and_b32 v239, 32, v239
v_cmp_eq_u32 vcc, 0, v239
mm v[108:111],v[220:223],v[188:191],v[108:111]
s_mov_b32 s78, 4294950911
s_mul_i32 s70, s70, s22
mm v[76:79],v[220:223],v[184:187],v[76:79]
v_cndmask_b32 v236, 0, v236, vcc
s_sub_u32 s92, 4294967294, s3
mm v[76:79],v[216:219],v[168:171],v[76:79]
rt v[208:209],v242 offset:42368
rt v[210:211],v242 offset:43392
mm v[44:47],v[216:219],v[164:167],v[44:47]
s_add_u32 s92, s92, s7
s_mul_i32 s92, s92, s21
mm v[44:47],v[220:223],v[180:183],v[44:47]
st a[0:3],v255,s[48:51],s18 offen offset:3072 sc1
mm v[12:15],v[220:223],v[176:179],v[12:15]
wt lgkmcnt(4)
s_add_u32 s90, s90, s92
mm v[12:15],v[216:219],v[160:163],v[12:15]
rt v[220:221],v243 offset:33920
rt v[222:223],v243 offset:34944
)ASM" R"ASM(mm v[16:19],v[196:199],v[160:163],v[16:19]
ar v224,a88
ar v225,a89
mm v[16:19],v[192:195],v[176:179],v[16:19]
ld v[152:155],v244,s[52:55],s91 offen offset:1024 sc1
mm v[48:51],v[192:195],v[180:183],v[48:51]
ar v226,a90
ar v227,a91
mm v[48:51],v[196:199],v[164:167],v[48:51]
rt v[216:217],v243 offset:42368
rt v[218:219],v243 offset:43392
mm v[80:83],v[196:199],v[168:171],v[80:83]
ar v228,a92
ar v229,a93
mm v[80:83],v[192:195],v[184:187],v[80:83]
ld v[156:159],v245,s[52:55],s91 offen offset:1024 sc1
mm v[112:115],v[192:195],v[188:191],v[112:115]
ar v230,a94
ar v231,a95
mm v[112:115],v[196:199],v[172:175],v[112:115]
cv v248,v224,v225
cv v249,v226,v227
mm v[116:119],v[204:207],v[172:175],v[116:119]
wt lgkmcnt(0)
s_barrier
mm v[116:119],v[200:203],v[188:191],v[116:119]
ld a[120:123],v246,s[52:55],s91 offen offset:1024 sc1
mm v[84:87],v[200:203],v[184:187],v[84:87]
rt v[192:193],v240 offset:50688
rt v[194:195],v240 offset:51712
mm v[84:87],v[204:207],v[168:171],v[84:87]
rt v[196:197],v240 offset:59136
rt v[198:199],v240 offset:60160
mm v[52:55],v[204:207],v[164:167],v[52:55]
s_add_u32 m0, s83, 33792
cv v250,v228,v229
mm v[52:55],v[200:203],v[180:183],v[52:55]
ld v232,s[44:47],s80 offen lds
mm v[20:23],v[200:203],v[176:179],v[20:23]
cv v251,v230,v231
aw a20,v248
mm v[20:23],v[204:207],v[160:163],v[20:23]
rt v[200:201],v241 offset:50688
rt v[202:203],v241 offset:51712
mm v[24:27],v[212:215],v[160:163],v[24:27]
aw a21,v249
aw a22,v250
mm v[24:27],v[208:211],v[176:179],v[24:27]
ld a[124:127],v247,s[52:55],s91 offen offset:1024 sc1
mm v[56:59],v[208:211],v[180:183],v[56:59]
aw a23,v251
s_mov_b32 s17, 1073741824
mm v[56:59],v[212:215],v[164:167],v[56:59]
rt v[204:205],v241 offset:59136
rt v[206:207],v241 offset:60160
mm v[88:91],v[212:215],v[168:171],v[88:91]
s_add_u32 m0, s83, 34816
s_lshl_b32 s77, s76, 16
mm v[88:91],v[208:211],v[184:187],v[88:91]
ld v233,s[44:47],s80 offen lds
mm v[120:123],v[208:211],v[188:191],v[120:123]
s_sub_u32 s77, s77, 8192
s_lshl4_add_u32 s81, s75, s74
mm v[120:123],v[212:215],v[172:175],v[120:123]
rt v[208:209],v242 offset:50688
rt v[210:211],v242 offset:51712
mm v[124:127],v[220:223],v[172:175],v[124:127]
s_lshl3_add_u32 s81, s81, 1
mm v[124:127],v[216:219],v[188:191],v[124:127]
s_cmp_eq_u32 s73, 0
s_cselect_b32 s16, s77, 0
mm v[92:95],v[216:219],v[184:187],v[92:95]
s_mov_b32 s18, 20480
s_mul_i32 s92, s75, 4294836224
mm v[92:95],v[220:223],v[168:171],v[92:95]
rt v[212:213],v242 offset:59136
rt v[214:215],v242 offset:60160
mm v[60:63],v[220:223],v[164:167],v[60:63]
s_add_u32 s92, s92, s88
s_add_u32 s19, s92, 393216
mm v[60:63],v[216:219],v[180:183],v[60:63]
st a[4:7],v255,s[48:51],s18 offen offset:0 sc1
mm v[28:31],v[216:219],v[176:179],v[28:31]
wt vmcnt(16) lgkmcnt(4)
aw a208,v236
mm v[28:31],v[220:223],v[160:163],v[28:31]
rt v[216:217],v243 offset:50688
rt v[218:219],v243 offset:51712
mm v[0:3],v[192:195],v[128:131],v[0:3]
ar v224,a24
ar v225,a25
mm v[0:3],v[196:199],a[104:107],v[0:3]
ld v[160:163],v244,s[52:55],s91 offen offset:2048 sc1
mm v[32:35],v[196:199],a[108:111],v[32:35]
ar v226,a26
ar v227,a27
mm v[32:35],v[192:195],v[140:143],v[32:35]
rt v[220:221],v243 offset:59136
rt v[222:223],v243 offset:60160
mm v[64:67],v[192:195],a[96:99],v[64:67]
)ASM" R"ASM(ar v228,a28
ar v229,a29
mm v[64:67],v[196:199],a[112:115],v[64:67]
ld v[164:167],v245,s[52:55],s91 offen offset:2048 sc1
mm v[96:99],v[196:199],a[116:119],v[96:99]
ar v230,a30
ar v231,a31
mm v[96:99],v[192:195],a[100:103],v[96:99]
rt v[196:197],v240 offset:50816
rt v[198:199],v240 offset:51840
mm v[100:103],v[200:203],a[100:103],v[100:103]
s_add_u32 m0, s83, 35968
cv v248,v224,v225
mm v[100:103],v[204:207],a[116:119],v[100:103]
ld v234,s[44:47],s80 offen lds
mm v[68:71],v[204:207],a[112:115],v[68:71]
cv v249,v226,v227
cv v250,v228,v229
mm v[68:71],v[200:203],a[96:99],v[68:71]
rt v[192:193],v240 offset:59264
rt v[194:195],v240 offset:60288
mm v[36:39],v[200:203],v[140:143],v[36:39]
cv v251,v230,v231
aw a24,v248
mm v[36:39],v[204:207],a[108:111],v[36:39]
ld v[168:171],v246,s[52:55],s91 offen offset:2048 sc1
mm v[4:7],v[204:207],a[104:107],v[4:7]
wt lgkmcnt(4)
aw a25,v249
mm v[4:7],v[200:203],v[128:131],v[4:7]
rt v[204:205],v241 offset:50816
rt v[206:207],v241 offset:51840
mm v[8:11],v[208:211],v[128:131],v[8:11]
s_add_u32 m0, s83, 36992
aw a26,v250
mm v[8:11],v[212:215],a[104:107],v[8:11]
ld v235,s[44:47],s80 offen lds
mm v[40:43],v[212:215],a[108:111],v[40:43]
s_add_u32 s80, s80, 524288
aw a27,v251
mm v[40:43],v[208:211],v[140:143],v[40:43]
rt v[200:201],v241 offset:59264
rt v[202:203],v241 offset:60288
mm v[72:75],v[208:211],a[96:99],v[72:75]
aw a209,v236
aw a210,v236
mm v[72:75],v[212:215],a[112:115],v[72:75]
ld v[172:175],v247,s[52:55],s91 offen offset:2048 sc1
mm v[104:107],v[212:215],a[116:119],v[104:107]
aw a211,v236
aw a232,0
mm v[104:107],v[208:211],a[100:103],v[104:107]
rt v[212:213],v242 offset:50816
rt v[214:215],v242 offset:51840
mm v[108:111],v[216:219],a[100:103],v[108:111]
aw a233,0
aw a234,0
mm v[108:111],v[220:223],a[116:119],v[108:111]
aw a235,0
s_lshl_b32 s20, s72, 10
mm v[76:79],v[220:223],a[112:115],v[76:79]
s_add_u32 s20, s20, 16384
s_mov_b32 s4, 1466176288
mm v[76:79],v[216:219],a[96:99],v[76:79]
rt v[208:209],v242 offset:59264
rt v[210:211],v242 offset:60288
.La5_LW1_13:
s_cbranch_execz .La5_LW1_R0
mm v[44:47],v[216:219],v[140:143],v[44:47]
s_cmp_ge_u32 s2, 64
s_cselect_b32 s4, 1182081808, s4
mm v[44:47],v[220:223],a[108:111],v[44:47]
st a[8:11],v255,s[48:51],s18 offen offset:1024 sc1
mm v[12:15],v[220:223],a[104:107],v[12:15]
wt lgkmcnt(4)
mm v[12:15],v[216:219],v[128:131],v[12:15]
rt v[220:221],v243 offset:50816
rt v[222:223],v243 offset:51840
mm v[16:19],v[196:199],v[128:131],v[16:19]
ar v224,a240
ar v225,a241
mm v[16:19],v[192:195],a[104:107],v[16:19]
ld v[176:179],v244,s[52:55],s91 offen offset:3072 sc1
mm v[48:51],v[192:195],a[108:111],v[48:51]
ar v226,a242
ar v227,a243
mm v[48:51],v[196:199],v[140:143],v[48:51]
rt v[216:217],v243 offset:59264
rt v[218:219],v243 offset:60288
mm v[80:83],v[196:199],a[96:99],v[80:83]
ar v228,a244
ar v229,a245
mm v[80:83],v[192:195],a[112:115],v[80:83]
ld v[180:183],v245,s[52:55],s91 offen offset:3072 sc1
mm v[112:115],v[192:195],a[116:119],v[112:115]
ar v230,a246
ar v231,a247
mm v[112:115],v[196:199],a[100:103],v[112:115]
cv v248,v224,v225
cv v249,v226,v227
)ASM" R"ASM(mm v[116:119],v[204:207],a[100:103],v[116:119]
wt lgkmcnt(0)
s_barrier
mm v[116:119],v[200:203],a[116:119],v[116:119]
ld v[184:187],v246,s[52:55],s91 offen offset:3072 sc1
mm v[84:87],v[200:203],a[112:115],v[84:87]
rt v[192:193],v240 offset:0
rt v[194:195],v240 offset:1024
mm v[84:87],v[204:207],a[96:99],v[84:87]
rt v[196:197],v240 offset:8448
rt v[198:199],v240 offset:9472
mm v[52:55],v[204:207],v[140:143],v[52:55]
s_add_u32 m0, s83, 50688
cv v250,v228,v229
mm v[52:55],v[200:203],a[108:111],v[52:55]
ld v232,s[44:47],s80 offen lds
mm v[20:23],v[200:203],a[104:107],v[20:23]
cv v251,v230,v231
aw a28,v248
mm v[20:23],v[204:207],v[128:131],v[20:23]
rt v[200:201],v241 offset:0
rt v[202:203],v241 offset:1024
mm v[24:27],v[212:215],v[128:131],v[24:27]
aw a29,v249
aw a30,v250
mm v[24:27],v[208:211],a[104:107],v[24:27]
ld v[188:191],v247,s[52:55],s91 offen offset:3072 sc1
mm v[56:59],v[208:211],a[108:111],v[56:59]
s_add_u32 s91, s91, 4096
aw a31,v251
mm v[56:59],v[212:215],v[140:143],v[56:59]
rt v[204:205],v241 offset:8448
rt v[206:207],v241 offset:9472
mm v[88:91],v[212:215],a[96:99],v[88:91]
s_add_u32 m0, s83, 51712
mm v[88:91],v[208:211],a[112:115],v[88:91]
ld v233,s[44:47],s80 offen lds
mm v[120:123],v[208:211],a[116:119],v[120:123]
s_cmp_ge_u32 s2, 128
s_cselect_b32 s4, 1967534368, s4
mm v[120:123],v[212:215],a[100:103],v[120:123]
rt v[208:209],v242 offset:0
rt v[210:211],v242 offset:1024
mm v[124:127],v[220:223],a[100:103],v[124:127]
s_cmp_ge_u32 s2, 192
s_cselect_b32 s4, 1683431728, s4
mm v[124:127],v[216:219],a[116:119],v[124:127]
v_bfe_u32 v236, v255, 4, 3
v_lshlrev_b32 v236, 2, v236
mm v[92:95],v[216:219],a[112:115],v[92:95]
v_lshrrev_b32 v236, v236, s4
v_and_b32 v236, 15, v236
mm v[92:95],v[220:223],a[96:99],v[92:95]
rt v[212:213],v242 offset:8448
rt v[214:215],v242 offset:9472
mm v[60:63],v[220:223],v[140:143],v[60:63]
v_bfe_u32 v239, v255, 7, 1
v_mul_u32_u24 v239, 28, v239
mm v[60:63],v[216:219],a[108:111],v[60:63]
v_xor_b32 v236, v236, v239
v_xor_b32 v236, s72, v236
mm v[28:31],v[216:219],a[104:107],v[28:31]
wt vmcnt(15) lgkmcnt(4)
v_lshlrev_b32 v236, 3, v236
mm v[28:31],v[220:223],v[128:131],v[28:31]
rt v[216:217],v243 offset:0
rt v[218:219],v243 offset:1024
mm v[0:3],v[192:195],v[132:135],v[0:3]
ar v224,a32
ar v225,a33
aw a225,v236
mm v[0:3],v[196:199],v[152:155],v[0:3]
ld v[128:131],v244,s[52:55],s91 offen offset:0 sc1
s_lshl3_add_u32 s5, s76, s101
mm v[32:35],v[196:199],v[156:159],v[32:35]
ar v226,a34
ar v227,a35
s_lshl2_add_u32 s5, s5, s3
mm v[32:35],v[192:195],v[136:139],v[32:35]
rt v[220:221],v243 offset:8448
rt v[222:223],v243 offset:9472
s_lshl_b32 s5, s5, 8
mm v[64:67],v[192:195],v[144:147],v[64:67]
ar v228,a36
ar v229,a37
s_add_u32 s5, s5, 1048576
mm v[64:67],v[196:199],a[120:123],v[64:67]
ld v[140:143],v245,s[52:55],s91 offen offset:0 sc1
mm v[96:99],v[196:199],a[124:127],v[96:99]
ar v230,a38
ar v231,a39
mm v[96:99],v[192:195],v[148:151],v[96:99]
rt v[196:197],v240 offset:128
rt v[198:199],v240 offset:1152
mm v[100:103],v[200:203],v[148:151],v[100:103]
s_add_u32 m0, s83, 52864
cv v248,v224,v225
mm v[100:103],v[204:207],a[124:127],v[100:103]
ld v234,s[44:47],s80 offen lds
mm v[68:71],v[204:207],a[120:123],v[68:71]
)ASM" R"ASM(cv v249,v226,v227
cv v250,v228,v229
mm v[68:71],v[200:203],v[144:147],v[68:71]
rt v[192:193],v240 offset:8576
rt v[194:195],v240 offset:9600
mm v[36:39],v[200:203],v[136:139],v[36:39]
cv v251,v230,v231
aw a32,v248
mm v[36:39],v[204:207],v[156:159],v[36:39]
ld a[192:195],v246,s[52:55],s91 offen offset:0 sc1
mm v[4:7],v[204:207],v[152:155],v[4:7]
wt lgkmcnt(4)
aw a33,v249
mm v[4:7],v[200:203],v[132:135],v[4:7]
rt v[204:205],v241 offset:128
rt v[206:207],v241 offset:1152
mm v[8:11],v[208:211],v[132:135],v[8:11]
s_add_u32 m0, s83, 53888
aw a34,v250
mm v[8:11],v[212:215],v[152:155],v[8:11]
ld v235,s[44:47],s80 offen lds
mm v[40:43],v[212:215],v[156:159],v[40:43]
aw a35,v251
mm v[40:43],v[208:211],v[136:139],v[40:43]
rt v[200:201],v241 offset:8576
rt v[202:203],v241 offset:9600
mm v[72:75],v[208:211],v[144:147],v[72:75]
mm v[72:75],v[212:215],a[120:123],v[72:75]
ld a[196:199],v247,s[52:55],s91 offen offset:0 sc1
mm v[104:107],v[212:215],a[124:127],v[104:107]
mm v[104:107],v[208:211],v[148:151],v[104:107]
rt v[212:213],v242 offset:128
rt v[214:215],v242 offset:1152
mm v[108:111],v[216:219],v[148:151],v[108:111]
mm v[108:111],v[220:223],a[124:127],v[108:111]
mm v[76:79],v[220:223],a[120:123],v[76:79]
mm v[76:79],v[216:219],v[144:147],v[76:79]
rt v[208:209],v242 offset:8576
rt v[210:211],v242 offset:9600
mm v[44:47],v[216:219],v[136:139],v[44:47]
mm v[44:47],v[220:223],v[156:159],v[44:47]
st a[12:15],v255,s[48:51],s18 offen offset:2048 sc1
mm v[12:15],v[220:223],v[152:155],v[12:15]
wt lgkmcnt(4)
mm v[12:15],v[216:219],v[132:135],v[12:15]
rt v[220:221],v243 offset:128
rt v[222:223],v243 offset:1152
mm v[16:19],v[196:199],v[132:135],v[16:19]
ar v224,a248
ar v225,a249
mm v[16:19],v[192:195],v[152:155],v[16:19]
ld a[200:203],v244,s[52:55],s91 offen offset:1024 sc1
mm v[48:51],v[192:195],v[156:159],v[48:51]
ar v226,a250
ar v227,a251
mm v[48:51],v[196:199],v[136:139],v[48:51]
rt v[216:217],v243 offset:8576
rt v[218:219],v243 offset:9600
mm v[80:83],v[196:199],v[144:147],v[80:83]
ar v228,a252
ar v229,a253
mm v[80:83],v[192:195],a[120:123],v[80:83]
ld a[204:207],v245,s[52:55],s91 offen offset:1024 sc1
mm v[112:115],v[192:195],a[124:127],v[112:115]
ar v230,a254
ar v231,a255
mm v[112:115],v[196:199],v[148:151],v[112:115]
cv v248,v224,v225
cv v249,v226,v227
mm v[116:119],v[204:207],v[148:151],v[116:119]
wt lgkmcnt(0)
s_barrier
mm v[116:119],v[200:203],a[124:127],v[116:119]
cv v250,v228,v229
cv v251,v230,v231
mm v[84:87],v[200:203],a[120:123],v[84:87]
rt v[192:193],v240 offset:16896
rt v[194:195],v240 offset:17920
mm v[84:87],v[204:207],v[144:147],v[84:87]
rt v[196:197],v240 offset:25344
rt v[198:199],v240 offset:26368
mm v[52:55],v[204:207],v[136:139],v[52:55]
aw a36,v248
aw a37,v249
mm v[52:55],v[200:203],v[156:159],v[52:55]
aw a38,v250
aw a39,v251
mm v[20:23],v[200:203],v[152:155],v[20:23]
s_getpc_b64 s[92:93]
.La5_LCT2:
s_and_b32 s93, s93, 0xffff
mm v[20:23],v[204:207],v[132:135],v[20:23]
rt v[200:201],v241 offset:16896
rt v[202:203],v241 offset:17920
mm v[24:27],v[212:215],v[132:135],v[24:27]
s_lshr_b32 s94, s2, 3
s_lshl2_add_u32 s94, s94, s3
mm v[24:27],v[208:211],v[152:155],v[24:27]
s_lshl_b32 s94, s94, 10
v_lshrrev_b32 v233, 7, v255
)ASM" R"ASM(mm v[56:59],v[208:211],v[156:159],v[56:59]
v_lshlrev_b32 v232, 3, v255
v_lshl_add_u32 v232, v233, 28, v232
mm v[56:59],v[212:215],v[136:139],v[56:59]
rt v[204:205],v241 offset:25344
rt v[206:207],v241 offset:26368
mm v[88:91],v[212:215],v[144:147],v[88:91]
v_add_u32 v232, s94, v232
s_mov_b32 s94, .La5_LCODEEND-.La5_LCT2
mm v[88:91],v[208:211],a[120:123],v[88:91]
s_mov_b32 s95, 0x20000
buffer_load_dword a253, v232, s[92:95], 0 offen
mm v[120:123],v[208:211],a[124:127],v[120:123]
s_mov_b32 s80, 8192
mm v[120:123],v[212:215],v[148:151],v[120:123]
rt v[208:209],v242 offset:16896
rt v[210:211],v242 offset:17920
mm v[124:127],v[220:223],v[148:151],v[124:127]
s_cmp_eq_u32 s73, 0
s_cselect_b32 s80, -8192, s80
mm v[124:127],v[216:219],a[124:127],v[124:127]
ld v[148:151],v246,s[52:55],s91 offen offset:1024 sc1
mm v[92:95],v[216:219],a[120:123],v[92:95]
s_add_u32 s83, s83, 4294886912
s_lshl_b32 s92, s76, 21
mm v[92:95],v[220:223],v[144:147],v[92:95]
rt v[212:213],v242 offset:25344
rt v[214:215],v242 offset:26368
mm v[60:63],v[220:223],v[136:139],v[60:63]
ds_write_b128 v238, a[12:15] offset:6144
mm v[60:63],v[216:219],v[156:159],v[60:63]
st a[16:19],v255,s[48:51],s18 offen offset:3072 sc1
mm v[28:31],v[216:219],v[152:155],v[28:31]
wt vmcnt(13) lgkmcnt(5)
s_add_u32 s92, s92, 54525952
mm v[28:31],v[220:223],v[132:135],v[28:31]
rt v[216:217],v243 offset:16896
rt v[218:219],v243 offset:17920
mm v[0:3],v[192:195],v[160:163],v[0:3]
ar v224,a40
ar v225,a41
mm v[0:3],v[196:199],v[176:179],v[0:3]
ld v[132:135],v247,s[52:55],s91 offen offset:1024 sc1
mm v[32:35],v[196:199],v[180:183],v[32:35]
ar v226,a42
ar v227,a43
mm v[32:35],v[192:195],v[164:167],v[32:35]
rt v[220:221],v243 offset:25344
rt v[222:223],v243 offset:26368
mm v[64:67],v[192:195],v[168:171],v[64:67]
ar v228,a44
ar v229,a45
mm v[64:67],v[196:199],v[184:187],v[64:67]
ld a[96:99],v244,s[52:55],s91 offen offset:2048 sc1
mm v[96:99],v[196:199],v[188:191],v[96:99]
ar v230,a46
ar v231,a47
mm v[96:99],v[192:195],v[172:175],v[96:99]
rt v[196:197],v240 offset:17024
rt v[198:199],v240 offset:18048
mm v[100:103],v[200:203],v[172:175],v[100:103]
cv v248,v224,v225
cv v249,v226,v227
mm v[100:103],v[204:207],v[188:191],v[100:103]
ld a[100:103],v245,s[52:55],s91 offen offset:2048 sc1
mm v[68:71],v[204:207],v[184:187],v[68:71]
cv v250,v228,v229
cv v251,v230,v231
mm v[68:71],v[200:203],v[168:171],v[68:71]
rt v[192:193],v240 offset:25472
rt v[194:195],v240 offset:26496
mm v[36:39],v[200:203],v[164:167],v[36:39]
aw a40,v248
aw a41,v249
mm v[36:39],v[204:207],v[180:183],v[36:39]
ld a[104:107],v246,s[52:55],s91 offen offset:2048 sc1
mm v[4:7],v[204:207],v[176:179],v[4:7]
wt lgkmcnt(4)
aw a42,v250
mm v[4:7],v[200:203],v[160:163],v[4:7]
rt v[204:205],v241 offset:17024
rt v[206:207],v241 offset:18048
mm v[8:11],v[208:211],v[160:163],v[8:11]
aw a43,v251
mm v[8:11],v[212:215],v[176:179],v[8:11]
ld a[108:111],v247,s[52:55],s91 offen offset:2048 sc1
mm v[40:43],v[212:215],v[180:183],v[40:43]
s_add_u32 s44, s26, s92
s_addc_u32 s45, s27, 0
mm v[40:43],v[208:211],v[164:167],v[40:43]
rt v[200:201],v241 offset:25472
rt v[202:203],v241 offset:26496
mm v[72:75],v[208:211],v[168:171],v[72:75]
s_and_b32 s45, s45, 0xffff
s_mov_b32 s46, 2097536
mm v[72:75],v[212:215],v[184:187],v[72:75]
s_mov_b32 s95, 4096
s_mov_b32 s96, 8192
mm v[104:107],v[212:215],v[188:191],v[104:107]
s_mov_b32 s18, 0
)ASM" R"ASM(s_mov_b32 s97, 12288
mm v[104:107],v[208:211],v[172:175],v[104:107]
rt v[212:213],v242 offset:17024
rt v[214:215],v242 offset:18048
mm v[108:111],v[216:219],v[172:175],v[108:111]
s_mul_i32 s92, s3, 196608
s_sub_u32 s93, 1376256, s92
mm v[108:111],v[220:223],v[188:191],v[108:111]
st a[20:23],v255,s[48:51],s18 offen offset:0 sc1
mm v[76:79],v[220:223],v[184:187],v[76:79]
mm v[76:79],v[216:219],v[168:171],v[76:79]
rt v[208:209],v242 offset:25472
rt v[210:211],v242 offset:26496
mm v[44:47],v[216:219],v[164:167],v[44:47]
ds_write_b128 v238, a[16:19] offset:7168
mm v[44:47],v[220:223],v[180:183],v[44:47]
st a[24:27],v255,s[48:51],s18 offen offset:1024 sc1
mm v[12:15],v[220:223],v[176:179],v[12:15]
wt lgkmcnt(5)
mm v[12:15],v[216:219],v[160:163],v[12:15]
rt v[220:221],v243 offset:17024
rt v[222:223],v243 offset:18048
mm v[16:19],v[196:199],v[160:163],v[16:19]
ar v224,a48
ar v225,a49
mm v[16:19],v[192:195],v[176:179],v[16:19]
ld a[112:115],v244,s[52:55],s91 offen offset:3072 sc1
mm v[48:51],v[192:195],v[180:183],v[48:51]
ar v226,a50
ar v227,a51
mm v[48:51],v[196:199],v[164:167],v[48:51]
rt v[216:217],v243 offset:25472
rt v[218:219],v243 offset:26496
mm v[80:83],v[196:199],v[168:171],v[80:83]
ar v228,a52
ar v229,a53
mm v[80:83],v[192:195],v[184:187],v[80:83]
ld a[116:119],v245,s[52:55],s91 offen offset:3072 sc1
mm v[112:115],v[192:195],v[188:191],v[112:115]
ar v230,a54
ar v231,a55
mm v[112:115],v[196:199],v[172:175],v[112:115]
cv v248,v224,v225
cv v249,v226,v227
mm v[116:119],v[204:207],v[172:175],v[116:119]
wt lgkmcnt(0)
s_barrier
mm v[116:119],v[200:203],v[188:191],v[116:119]
ld a[120:123],v246,s[52:55],s91 offen offset:3072 sc1
mm v[84:87],v[200:203],v[184:187],v[84:87]
rt v[192:193],v240 offset:33792
rt v[194:195],v240 offset:34816
mm v[84:87],v[204:207],v[168:171],v[84:87]
rt v[196:197],v240 offset:42240
rt v[198:199],v240 offset:43264
mm v[52:55],v[204:207],v[164:167],v[52:55]
ds_write_b128 v238, a[20:23] offset:32768
mm v[52:55],v[200:203],v[180:183],v[52:55]
ld a[124:127],v247,s[52:55],s91 offen offset:3072 sc1
mm v[20:23],v[200:203],v[176:179],v[20:23]
cv v250,v228,v229
cv v251,v230,v231
mm v[20:23],v[204:207],v[160:163],v[20:23]
rt v[200:201],v241 offset:33792
rt v[202:203],v241 offset:34816
mm v[24:27],v[212:215],v[160:163],v[24:27]
aw a44,v248
aw a45,v249
mm v[24:27],v[208:211],v[176:179],v[24:27]
aw a46,v250
aw a47,v251
mm v[56:59],v[208:211],v[180:183],v[56:59]
mm v[56:59],v[212:215],v[164:167],v[56:59]
rt v[204:205],v241 offset:42240
rt v[206:207],v241 offset:43264
mm v[88:91],v[212:215],v[168:171],v[88:91]
mm v[88:91],v[208:211],v[184:187],v[88:91]
mm v[120:123],v[208:211],v[188:191],v[120:123]
mm v[120:123],v[212:215],v[172:175],v[120:123]
rt v[208:209],v242 offset:33792
rt v[210:211],v242 offset:34816
mm v[124:127],v[220:223],v[172:175],v[124:127]
mm v[124:127],v[216:219],v[188:191],v[124:127]
st a[28:31],v255,s[48:51],s18 offen offset:2048 sc1
mm v[92:95],v[216:219],v[184:187],v[92:95]
mm v[92:95],v[220:223],v[168:171],v[92:95]
rt v[212:213],v242 offset:42240
rt v[214:215],v242 offset:43264
mm v[60:63],v[220:223],v[164:167],v[60:63]
ds_write_b128 v238, a[24:27] offset:33792
mm v[60:63],v[216:219],v[180:183],v[60:63]
st a[32:35],v255,s[48:51],s18 offen offset:3072 sc1
mm v[28:31],v[216:219],v[176:179],v[28:31]
wt vmcnt(12) lgkmcnt(5)
mm v[28:31],v[220:223],v[160:163],v[28:31]
rt v[216:217],v243 offset:33792
rt v[218:219],v243 offset:34816
mm v[0:3],v[192:195],v[128:131],v[0:3]
ar v224,a56
ar v225,a57
)ASM" R"ASM(mm v[0:3],v[196:199],a[200:203],v[0:3]
ar v226,a58
ar v227,a59
mm v[32:35],v[196:199],a[204:207],v[32:35]
ar v228,a60
ar v229,a61
mm v[32:35],v[192:195],v[140:143],v[32:35]
rt v[220:221],v243 offset:42240
rt v[222:223],v243 offset:43264
mm v[64:67],v[192:195],a[192:195],v[64:67]
ar v230,a62
ar v231,a63
mm v[64:67],v[196:199],v[148:151],v[64:67]
cv v248,v224,v225
cv v249,v226,v227
mm v[96:99],v[196:199],v[132:135],v[96:99]
cv v250,v228,v229
cv v251,v230,v231
mm v[96:99],v[192:195],a[196:199],v[96:99]
rt v[196:197],v240 offset:33920
rt v[198:199],v240 offset:34944
mm v[100:103],v[200:203],a[196:199],v[100:103]
aw a48,v248
aw a49,v249
mm v[100:103],v[204:207],v[132:135],v[100:103]
aw a50,v250
aw a51,v251
mm v[68:71],v[204:207],v[148:151],v[68:71]
v_add_u32 v244, s92, v244
v_add_u32 v246, s93, v246
mm v[68:71],v[200:203],a[192:195],v[68:71]
rt v[192:193],v240 offset:42368
rt v[194:195],v240 offset:43392
mm v[36:39],v[200:203],v[140:143],v[36:39]
ds_write_b128 v238, a[28:31] offset:34816
mm v[36:39],v[204:207],a[204:207],v[36:39]
v_add_u32 v245, 0x1000, v244
v_add_u32 v247, 0x1000, v246
mm v[4:7],v[204:207],a[200:203],v[4:7]
wt lgkmcnt(5)
s_lshl_b32 s91, s72, 13
mm v[4:7],v[200:203],v[128:131],v[4:7]
rt v[204:205],v241 offset:33920
rt v[206:207],v241 offset:34944
mm v[8:11],v[208:211],v[128:131],v[8:11]
s_lshl4_add_u32 s92, s76, s3
s_lshl3_add_u32 s92, s92, s20
mm v[8:11],v[212:215],a[200:203],v[8:11]
s_bitset1_b32 s20, 30
mm v[40:43],v[212:215],a[204:207],v[40:43]
mm v[40:43],v[208:211],v[140:143],v[40:43]
rt v[200:201],v241 offset:42368
rt v[202:203],v241 offset:43392
mm v[72:75],v[208:211],a[192:195],v[72:75]
mm v[72:75],v[212:215],v[148:151],v[72:75]
mm v[104:107],v[212:215],v[132:135],v[104:107]
s_mov_b32 s18, 4096
mm v[104:107],v[208:211],a[196:199],v[104:107]
rt v[212:213],v242 offset:33920
rt v[214:215],v242 offset:34944
mm v[108:111],v[216:219],a[196:199],v[108:111]
mm v[108:111],v[220:223],v[132:135],v[108:111]
st a[36:39],v255,s[48:51],s18 offen offset:0 sc1
mm v[76:79],v[220:223],v[148:151],v[76:79]
mm v[76:79],v[216:219],a[192:195],v[76:79]
rt v[208:209],v242 offset:42368
rt v[210:211],v242 offset:43392
mm v[44:47],v[216:219],v[140:143],v[44:47]
ds_write_b128 v238, a[32:35] offset:35840
mm v[44:47],v[220:223],a[204:207],v[44:47]
st a[40:43],v255,s[48:51],s18 offen offset:1024 sc1
mm v[12:15],v[220:223],a[200:203],v[12:15]
wt lgkmcnt(5)
mm v[12:15],v[216:219],v[128:131],v[12:15]
rt v[220:221],v243 offset:33920
rt v[222:223],v243 offset:34944
mm v[16:19],v[196:199],v[128:131],v[16:19]
s_mov_b64 s[52:53], s[86:87]
mm v[16:19],v[192:195],a[200:203],v[16:19]
s_bitcmp1_b32 21, s73
s_cselect_b32 s54, 16384, 0
s_cselect_b32 s86, s88, 1073741824
mm v[48:51],v[192:195],a[204:207],v[48:51]
s_lshl_b32 s89, s3, 14
v_add_u32 v248, s89, v255
mm v[48:51],v[196:199],v[140:143],v[48:51]
rt v[216:217],v243 offset:42368
rt v[218:219],v243 offset:43392
mm v[80:83],v[196:199],a[192:195],v[80:83]
ds_write_b128 v238, a[36:39] offset:36864
mm v[80:83],v[192:195],v[148:151],v[80:83]
s_add_u32 s86, s86, 262144
mm v[112:115],v[192:195],v[132:135],v[112:115]
s_cmp_eq_u32 s73, 0
s_cselect_b32 s86, s92, s86
s_cselect_b32 s92, 1073741824, s92
mm v[112:115],v[196:199],a[196:199],v[112:115]
s_mov_b32 s87, s92
s_add_u32 s89, s89, 16384
mm v[116:119],v[204:207],a[196:199],v[116:119]
)ASM" R"ASM(wt lgkmcnt(1)
s_barrier
mm v[116:119],v[200:203],v[132:135],v[116:119]
s_lshl_b32 m0, s3, 12
s_add_u32 m0, m0, 131072
mm v[84:87],v[200:203],v[148:151],v[84:87]
rt v[192:193],v240 offset:50688
rt v[194:195],v240 offset:51712
mm v[84:87],v[204:207],a[192:195],v[84:87]
.La5_LW1_14:
s_cbranch_execz .La5_LW1_R0
rt v[196:197],v240 offset:59136
rt v[198:199],v240 offset:60160
mm v[52:55],v[204:207],v[140:143],v[52:55]
ds_write_b128 v238, a[40:43] offset:37888
mm v[52:55],v[200:203],a[204:207],v[52:55]
v_bfe_u32 v251, v255, 2, 5
v_and_b32 v224, 0x380, v255
mm v[20:23],v[200:203],a[200:203],v[20:23]
v_add_u32 v251, v251, v224
v_and_b32 v224, 16, v255
mm v[20:23],v[204:207],v[128:131],v[20:23]
rt v[200:201],v241 offset:50688
rt v[202:203],v241 offset:51712
mm v[24:27],v[212:215],v[128:131],v[24:27]
v_cmp_eq_u32 vcc, 0, v224
mm v[24:27],v[208:211],a[200:203],v[24:27]
v_cndmask_b32 v249, v253, v252, vcc
mm v[56:59],v[208:211],a[204:207],v[56:59]
mm v[56:59],v[212:215],v[140:143],v[56:59]
rt v[204:205],v241 offset:59136
rt v[206:207],v241 offset:60160
mm v[88:91],v[212:215],a[192:195],v[88:91]
ds_write_b128 v238, a[44:47] offset:38912
mm v[88:91],v[208:211],v[148:151],v[88:91]
st a[44:47],v255,s[48:51],s18 offen offset:2048 sc1
mm v[120:123],v[208:211],v[132:135],v[120:123]
mm v[120:123],v[212:215],a[196:199],v[120:123]
rt v[208:209],v242 offset:50688
rt v[210:211],v242 offset:51712
mm v[124:127],v[220:223],a[196:199],v[124:127]
mm v[124:127],v[216:219],v[132:135],v[124:127]
mm v[92:95],v[216:219],v[148:151],v[92:95]
mm v[92:95],v[220:223],a[192:195],v[92:95]
rt v[212:213],v242 offset:59136
rt v[214:215],v242 offset:60160
mm v[60:63],v[220:223],v[140:143],v[60:63]
mm v[60:63],v[216:219],a[204:207],v[60:63]
st a[48:51],v255,s[48:51],s18 offen offset:3072 sc1
mm v[28:31],v[216:219],a[200:203],v[28:31]
wt vmcnt(6) lgkmcnt(5)
mm v[28:31],v[220:223],v[128:131],v[28:31]
rt v[216:217],v243 offset:50688
rt v[218:219],v243 offset:51712
mm v[0:3],v[192:195],a[96:99],v[0:3]
mm v[0:3],v[196:199],a[112:115],v[0:3]
mm v[32:35],v[196:199],a[116:119],v[32:35]
mm v[32:35],v[192:195],a[100:103],v[32:35]
rt v[220:221],v243 offset:59136
rt v[222:223],v243 offset:60160
mm v[64:67],v[192:195],a[104:107],v[64:67]
mm v[64:67],v[196:199],a[120:123],v[64:67]
mm v[96:99],v[196:199],a[124:127],v[96:99]
mm v[96:99],v[192:195],a[108:111],v[96:99]
rt v[196:197],v240 offset:50816
rt v[198:199],v240 offset:51840
mm v[100:103],v[200:203],a[108:111],v[100:103]
mm v[100:103],v[204:207],a[124:127],v[100:103]
mm v[68:71],v[204:207],a[120:123],v[68:71]
mm v[68:71],v[200:203],a[104:107],v[68:71]
rt v[192:193],v240 offset:59264
rt v[194:195],v240 offset:60288
mm v[36:39],v[200:203],a[100:103],v[36:39]
mm v[36:39],v[204:207],a[116:119],v[36:39]
mm v[4:7],v[204:207],a[112:115],v[4:7]
wt lgkmcnt(4)
mm v[4:7],v[200:203],a[96:99],v[4:7]
rt v[204:205],v241 offset:50816
rt v[206:207],v241 offset:51840
mm v[8:11],v[208:211],a[96:99],v[8:11]
mm v[8:11],v[212:215],a[112:115],v[8:11]
mm v[40:43],v[212:215],a[116:119],v[40:43]
cv v128,v0,v1
cv v129,v2,v3
mm v[40:43],v[208:211],a[100:103],v[40:43]
rt v[200:201],v241 offset:59264
rt v[202:203],v241 offset:60288
mm v[72:75],v[208:211],a[104:107],v[72:75]
cv v130,v4,v5
cv v131,v6,v7
mm v[72:75],v[212:215],a[120:123],v[72:75]
cv v144,v32,v33
cv v145,v34,v35
mm v[104:107],v[212:215],a[124:127],v[104:107]
cv v146,v36,v37
cv v147,v38,v39
mm v[104:107],v[208:211],a[108:111],v[104:107]
rt v[212:213],v242 offset:50816
rt v[214:215],v242 offset:51840
mm v[108:111],v[216:219],a[108:111],v[108:111]
)ASM" R"ASM(cv v160,v64,v65
cv v161,v66,v67
mm v[108:111],v[220:223],a[124:127],v[108:111]
cv v162,v68,v69
cv v163,v70,v71
mm v[76:79],v[220:223],a[120:123],v[76:79]
cv v176,v96,v97
cv v177,v98,v99
mm v[76:79],v[216:219],a[104:107],v[76:79]
rt v[208:209],v242 offset:59264
rt v[210:211],v242 offset:60288
mm v[44:47],v[216:219],a[100:103],v[44:47]
cv v178,v100,v101
cv v179,v102,v103
mm v[44:47],v[220:223],a[116:119],v[44:47]
mm v[12:15],v[220:223],a[112:115],v[12:15]
wt lgkmcnt(4)
mm v[12:15],v[216:219],a[96:99],v[12:15]
rt v[220:221],v243 offset:50816
rt v[222:223],v243 offset:51840
mm v[16:19],v[196:199],a[96:99],v[16:19]
v_mul_u32_u24 v241, 0x5555, v224
v_lshrrev_b32 v241, 4, v241
v_add_u32 v241, 0x8888, v241
mm v[16:19],v[192:195],a[112:115],v[16:19]
st v[128:131],v255,s[52:55],0 offen offset:0 sc1
mm v[48:51],v[192:195],a[116:119],v[48:51]
cv v132,v8,v9
cv v133,v10,v11
cv v181,v106,v107
mm v[48:51],v[196:199],a[100:103],v[48:51]
rt v[216:217],v243 offset:59264
rt v[218:219],v243 offset:60288
cv v182,v108,v109
mm v[80:83],v[196:199],a[104:107],v[80:83]
cv v134,v12,v13
cv v135,v14,v15
cv v183,v110,v111
mm v[80:83],v[192:195],a[120:123],v[80:83]
st v[144:147],v255,s[52:55],s95 offen offset:0 sc1
ar v34,a238
mm v[112:115],v[192:195],a[124:127],v[112:115]
cv v148,v40,v41
cv v149,v42,v43
ar v35,a239
mm v[112:115],v[196:199],a[108:111],v[112:115]
cv v150,v44,v45
cv v151,v46,v47
v_mov_b32 v36, 0xff800000
mm v[116:119],v[204:207],a[108:111],v[116:119]
wt lgkmcnt(0)
s_barrier
v_mov_b32 v37, 0xff800000
mm v[116:119],v[200:203],a[124:127],v[116:119]
st v[160:163],v255,s[52:55],s96 offen offset:0 sc1
v_mov_b32 v38, 0xff800000
mm v[84:87],v[200:203],a[120:123],v[84:87]
cv v164,v72,v73
cv v165,v74,v75
v_mov_b32 v39, 0xff800000
mm v[84:87],v[204:207],a[104:107],v[84:87]
rd a[64:67],v248 offset:0
rd a[68:71],v248 offset:1024
mm v[52:55],v[204:207],a[100:103],v[52:55]
cv v166,v76,v77
cv v167,v78,v79
mm v[52:55],v[200:203],a[116:119],v[52:55]
st v[176:179],v255,s[52:55],s97 offen offset:0 sc1
mm v[20:23],v[200:203],a[112:115],v[20:23]
s_nop 0
cv v180,v104,v105
mm v[20:23],v[204:207],a[96:99],v[20:23]
rd a[72:75],v248 offset:2048
rd a[76:79],v248 offset:3072
mm v[24:27],v[212:215],a[96:99],v[24:27]
ar v32,a236
ar v33,a237
mm v[24:27],v[208:211],a[112:115],v[24:27]
st v[132:135],v255,s[52:55],0 offen offset:1024 sc1
mm v[56:59],v[208:211],a[116:119],v[56:59]
cv v136,v16,v17
cv v137,v18,v19
mm v[56:59],v[212:215],a[100:103],v[56:59]
rd a[80:83],v248 offset:4096
rd a[84:87],v248 offset:5120
mm v[88:91],v[212:215],a[104:107],v[88:91]
cv v138,v20,v21
cv v139,v22,v23
mm v[88:91],v[208:211],a[120:123],v[88:91]
st v[148:151],v255,s[52:55],s95 offen offset:1024 sc1
mm v[120:123],v[208:211],a[124:127],v[120:123]
cv v152,v48,v49
cv v153,v50,v51
mm v[120:123],v[212:215],a[108:111],v[120:123]
rd a[88:91],v248 offset:6144
rd a[92:95],v248 offset:7168
mm v[124:127],v[220:223],a[108:111],v[124:127]
ds_write_b128 v238, a[48:51] offset:39936
mm v[124:127],v[216:219],a[124:127],v[124:127]
st v[164:167],v255,s[52:55],s96 offen offset:1024 sc1
)ASM" R"ASM(mm v[92:95],v[216:219],a[120:123],v[92:95]
cv v154,v52,v53
cv v155,v54,v55
mm v[92:95],v[220:223],a[104:107],v[92:95]
cv v168,v80,v81
cv v169,v82,v83
mm v[60:63],v[220:223],a[100:103],v[60:63]
cv v170,v84,v85
cv v171,v86,v87
mm v[60:63],v[216:219],a[116:119],v[60:63]
st v[180:183],v255,s[52:55],s97 offen offset:1024 sc1
mm v[28:31],v[216:219],a[112:115],v[28:31]
cv v184,v112,v113
cv v185,v114,v115
mm v[28:31],v[220:223],a[96:99],v[28:31]
cv v186,v116,v117
cv v187,v118,v119
wt lgkmcnt(1)
mm v[208:211],a[64:67],v[160:163],v[32:35]
mm v[216:219],a[64:67],v[176:179],0
st v[136:139],v255,s[52:55],0 offen offset:2048 sc1
mm v[212:215],a[80:83],v[160:163],v[36:39]
cv v172,v88,v89
cv v173,v90,v91
cv v174,v92,v93
mm v[220:223],a[80:83],v[176:179],v[32:35]
cv v175,v94,v95
cv v188,v120,v121
cv v189,v122,v123
mm v[208:211],a[68:71],v[164:167],v[208:211]
cv v190,v124,v125
cv v191,v126,v127
cv v140,v24,v25
mm v[216:219],a[68:71],v[180:183],v[216:219]
st v[152:155],v255,s[52:55],s95 offen offset:2048 sc1
cv v141,v26,v27
mm v[212:215],a[84:87],v[164:167],v[212:215]
cv v142,v28,v29
cv v143,v30,v31
cv v156,v56,v57
mm v[220:223],a[84:87],v[180:183],v[220:223]
cv v157,v58,v59
cv v158,v60,v61
cv v159,v62,v63
mm v[208:211],a[72:75],v[168:171],v[208:211]
mm v[216:219],a[72:75],v[184:187],v[216:219]
st v[168:171],v255,s[52:55],s96 offen offset:2048 sc1
mm v[212:215],a[88:91],v[168:171],v[212:215]
mm v[220:223],a[88:91],v[184:187],v[220:223]
mm v[208:211],a[76:79],v[172:175],v[208:211]
mm v[216:219],a[76:79],v[188:191],v[216:219]
st v[184:187],v255,s[52:55],s97 offen offset:2048 sc1
mm v[212:215],a[92:95],v[172:175],v[212:215]
rd a[64:67],v248 offset:16384
mm v[220:223],a[92:95],v[188:191],v[220:223]
rd a[96:99],v248 offset:8192
ex v0,v208
wt vmcnt(12)
buffer_store_dwordx2 v[252:253], off, s[64:67], s87 sc1
mm v[64:67],a[232:235],a[232:235],0
rd a[68:71],v248 offset:17408
ex v1,v209
mm v[68:71],a[232:235],a[232:235],0
rd a[100:103],v248 offset:9216
st v[140:143],v255,s[52:55],0 offen offset:3072 sc1
mm v[72:75],a[232:235],a[232:235],0
rd a[72:75],v248 offset:18432
ex v2,v210
mm v[76:79],a[232:235],a[232:235],0
rd a[104:107],v248 offset:10240
ex v3,v211
mm v[80:83],a[232:235],a[232:235],0
rd a[76:79],v248 offset:19456
ex v8,v216
mm v[84:87],a[232:235],a[232:235],0
rd a[108:111],v248 offset:11264
st v[156:159],v255,s[52:55],s95 offen offset:3072 sc1
mm v[88:91],a[232:235],a[232:235],0
rd a[80:83],v248 offset:20480
ex v9,v217
mm v[92:95],a[232:235],a[232:235],0
rd a[112:115],v248 offset:12288
ex v10,v218
mm v[96:99],a[232:235],a[232:235],0
rd a[84:87],v248 offset:21504
ex v11,v219
mm v[100:103],a[232:235],a[232:235],0
rd a[116:119],v248 offset:13312
st v[172:175],v255,s[52:55],s96 offen offset:3072 sc1
mm v[104:107],a[232:235],a[232:235],0
rd a[88:91],v248 offset:22528
ex v4,v212
mm v[108:111],a[232:235],a[232:235],0
rd a[120:123],v248 offset:14336
ex v5,v213
mm v[112:115],a[232:235],a[232:235],0
rd a[92:95],v248 offset:23552
ex v6,v214
mm v[116:119],a[232:235],a[232:235],0
rd a[124:127],v248 offset:15360
)ASM" R"ASM(st v[188:191],v255,s[52:55],s97 offen offset:3072 sc1
ex v7,v215
mm v[120:123],a[232:235],a[232:235],0
v_exp_f32_e64 v12, v220
mm v[124:127],a[232:235],a[232:235],0
aw a192,0
aw a193,0
aw a194,0
aw a195,0
aw a196,0
aw a197,0
aw a198,0
aw a199,0
v_add_u32 v248, 0x4000, v248
s_cmp_eq_u32 s3, 3
s_cbranch_scc1 .La5_LA_FH
.p2align 6
; ---- attention, 32 keys per window: S = K Q^T (causal mask through SrcC), P = exp2(S), O += V^T P, row sums l += 1 P (one sparse MFMA for both tiles); here the later 32 rows only
.La5_LA_HH:
wt lgkmcnt(1)
mm v[208:211],a[64:67],v[160:163],0
ex v13,v221
mm v[208:211],a[68:71],v[164:167],v[208:211]
cv v232,v0,v1
cv v233,v2,v3
mm v[208:211],a[72:75],v[168:171],v[208:211]
ex v14,v222
mm v[208:211],a[76:79],v[172:175],v[208:211]
cv v236,v8,v9
cv v237,v10,v11
mm v[216:219],a[76:79],v[188:191],0
ex v15,v223
mm v[216:219],a[72:75],v[184:187],v[216:219]
cv v234,v4,v5
cv v235,v6,v7
mm v[216:219],a[68:71],v[180:183],v[216:219]
ex v0,v208
mm v[216:219],a[64:67],v[176:179],v[216:219]
cv v238,v12,v13
cv v239,v14,v15
mm v[212:215],a[80:83],v[160:163],0
ex v1,v209
mm v[212:215],a[84:87],v[164:167],v[212:215]
rd a[64:67],v248 offset:16384
rd a[68:71],v248 offset:17408
mm v[212:215],a[88:91],v[168:171],v[212:215]
ex v2,v210
mm v[212:215],a[92:95],v[172:175],v[212:215]
rd a[72:75],v248 offset:18432
rd a[76:79],v248 offset:19456
mm v[220:223],a[92:95],v[188:191],0
ex v3,v211
mm v[220:223],a[88:91],v[184:187],v[220:223]
rd a[92:95],v248 offset:23552
mm v[220:223],a[84:87],v[180:183],v[220:223]
ex v8,v216
mm v[220:223],a[80:83],v[176:179],v[220:223]
wt lgkmcnt(5)
rd a[88:91],v248 offset:22528
mm v[64:67],a[96:99],v[232:235],v[64:67]
ex v9,v217
mm v[96:99],a[96:99],v[236:239],v[96:99]
rd a[96:99],v248 offset:8192
rd a[84:87],v248 offset:21504
mm v[100:103],a[100:103],v[236:239],v[100:103]
ex v10,v218
mm v[68:71],a[100:103],v[232:235],v[68:71]
rd a[100:103],v248 offset:9216
rd a[80:83],v248 offset:20480
mm v[72:75],a[104:107],v[232:235],v[72:75]
ex v11,v219
mm v[104:107],a[104:107],v[236:239],v[104:107]
rd a[104:107],v248 offset:10240
mm v[108:111],a[108:111],v[236:239],v[108:111]
ex v4,v212
mm v[76:79],a[108:111],v[232:235],v[76:79]
rd a[108:111],v248 offset:11264
s_add_u32 s85, s85, 1
mm v[80:83],a[112:115],v[232:235],v[80:83]
ex v5,v213
mm v[112:115],a[112:115],v[236:239],v[112:115]
rd a[112:115],v248 offset:12288
s_add_u32 s89, s89, 32768
mm v[116:119],a[116:119],v[236:239],v[116:119]
ex v6,v214
mm v[84:87],a[116:119],v[232:235],v[84:87]
rd a[116:119],v248 offset:13312
s_add_u32 s92, s85, s3
mm v[88:91],a[120:123],v[232:235],v[88:91]
ex v7,v215
mm v[120:123],a[120:123],v[236:239],v[120:123]
rd a[120:123],v248 offset:14336
buffer_load_dword v250, v251, s[64:67], s20 offen sc1
mm v[124:127],a[124:127],v[236:239],v[124:127]
ex v12,v220
mm v[92:95],a[124:127],v[232:235],v[92:95]
rd a[124:127],v248 offset:15360
s_barrier
v_smfmac_f32_16x16x64_bf16 a[196:199], a[208:211], v[232:239], v241
wt lgkmcnt(6)
)ASM" R"ASM(mm v[208:211],a[64:67],v[160:163],0
ex v13,v221
mm v[208:211],a[68:71],v[164:167],v[208:211]
cv v232,v0,v1
cv v233,v2,v3
mm v[208:211],a[72:75],v[168:171],v[208:211]
ex v14,v222
mm v[208:211],a[76:79],v[172:175],v[208:211]
cv v236,v8,v9
cv v237,v10,v11
mm v[216:219],a[76:79],v[188:191],0
ex v15,v223
mm v[216:219],a[72:75],v[184:187],v[216:219]
cv v234,v4,v5
cv v235,v6,v7
mm v[216:219],a[68:71],v[180:183],v[216:219]
ex v0,v208
mm v[216:219],a[64:67],v[176:179],v[216:219]
cv v238,v12,v13
cv v239,v14,v15
mm v[212:215],a[80:83],v[160:163],0
ex v1,v209
mm v[212:215],a[84:87],v[164:167],v[212:215]
rd a[64:67],v248 offset:32768
rd a[68:71],v248 offset:33792
mm v[212:215],a[88:91],v[168:171],v[212:215]
ex v2,v210
mm v[212:215],a[92:95],v[172:175],v[212:215]
rd a[72:75],v248 offset:34816
rd a[76:79],v248 offset:35840
mm v[220:223],a[92:95],v[188:191],0
ex v3,v211
mm v[220:223],a[88:91],v[184:187],v[220:223]
rd a[92:95],v248 offset:39936
s_bitcmp1_b64 s[98:99], s85
mm v[220:223],a[84:87],v[180:183],v[220:223]
ex v8,v216
mm v[220:223],a[80:83],v[176:179],v[220:223]
wt lgkmcnt(5)
rd a[88:91],v248 offset:38912
mm v[64:67],a[96:99],v[232:235],v[64:67]
ex v9,v217
mm v[96:99],a[96:99],v[236:239],v[96:99]
rd a[96:99],v248 offset:24576
rd a[84:87],v248 offset:37888
mm v[100:103],a[100:103],v[236:239],v[100:103]
ex v10,v218
mm v[68:71],a[100:103],v[232:235],v[68:71]
rd a[100:103],v248 offset:25600
rd a[80:83],v248 offset:36864
mm v[72:75],a[104:107],v[232:235],v[72:75]
ex v11,v219
mm v[104:107],a[104:107],v[236:239],v[104:107]
rd a[104:107],v248 offset:26624
mm v[108:111],a[108:111],v[236:239],v[108:111]
ex v4,v212
mm v[76:79],a[108:111],v[232:235],v[76:79]
rd a[108:111],v248 offset:27648
mm v[80:83],a[112:115],v[232:235],v[80:83]
ex v5,v213
mm v[112:115],a[112:115],v[236:239],v[112:115]
rd a[112:115],v248 offset:28672
mm v[116:119],a[116:119],v[236:239],v[116:119]
ex v6,v214
mm v[84:87],a[116:119],v[232:235],v[84:87]
rd a[116:119],v248 offset:29696
wt vmcnt(1)
buffer_store_dwordx2 v[252:253], off, s[64:67], s86 sc1
s_mov_b32 s86, 1073741824
s_bitset0_b32 s20, 30
mm v[88:91],a[120:123],v[232:235],v[88:91]
ex v7,v215
mm v[120:123],a[120:123],v[236:239],v[120:123]
rd a[120:123],v248 offset:30720
mm v[124:127],a[124:127],v[236:239],v[124:127]
v_exp_f32_e64 v12, v220
mm v[92:95],a[124:127],v[232:235],v[92:95]
rd a[124:127],v248 offset:31744
v_smfmac_f32_16x16x64_bf16 a[196:199], a[208:211], v[232:239], v241
v_add_u32 v248, 0x8000, v248
s_cbranch_scc0 .La5_LA_HH1
s_call_b64 s[24:25], .La5_LA_EVENT
s_add_u32 s92, s85, s3
.La5_LA_HH1:
s_cmp_lt_u32 s92, 3
s_cbranch_scc1 .La5_LA_HH
; ---- first window with both row groups (diagonal of the earlier group)
.La5_LA_FH:
.La5_LW1_15:
s_cbranch_execz .La5_LW1_R0
wt lgkmcnt(9)
mm v[192:195],a[64:67],v[128:131],v[32:35]
ex v13,v221
mm v[192:195],a[68:71],v[132:135],v[192:195]
cv v232,v0,v1
cv v233,v2,v3
mm v[192:195],a[72:75],v[136:139],v[192:195]
ex v14,v222
mm v[192:195],a[76:79],v[140:143],v[192:195]
ld v255,s[60:63],s17 offen offset:0 sc1 lds
)ASM" R"ASM(mm v[200:203],a[76:79],v[156:159],0
ex v15,v223
mm v[200:203],a[72:75],v[152:155],v[200:203]
cv v236,v8,v9
cv v237,v10,v11
mm v[200:203],a[68:71],v[148:151],v[200:203]
ex v192,v192
mm v[200:203],a[64:67],v[144:147],v[200:203]
wt lgkmcnt(1)
cv v234,v4,v5
mm v[196:199],a[80:83],v[128:131],v[36:39]
ex v193,v193
mm v[196:199],a[84:87],v[132:135],v[196:199]
cv v224,v192,v193
s_add_u32 s85, s85, 1
mm v[196:199],a[88:91],v[136:139],v[196:199]
ex v194,v194
mm v[196:199],a[92:95],v[140:143],v[196:199]
ld v255,s[60:63],s17 offen offset:1024 sc1 lds
mm v[204:207],a[92:95],v[156:159],v[32:35]
ex v195,v195
mm v[204:207],a[88:91],v[152:155],v[204:207]
cv v225,v194,v195
s_add_u32 s89, s89, 16384
mm v[204:207],a[84:87],v[148:151],v[204:207]
ex v200,v200
mm v[204:207],a[80:83],v[144:147],v[204:207]
ld v255,s[60:63],s17 offen offset:2048 sc1 lds
mm v[208:211],a[64:67],v[160:163],0
ex v201,v201
mm v[208:211],a[68:71],v[164:167],v[208:211]
cv v228,v200,v201
s_and_b32 s89, s89, 0x1ffff
mm v[208:211],a[72:75],v[168:171],v[208:211]
ex v202,v202
mm v[208:211],a[76:79],v[172:175],v[208:211]
ld v255,s[60:63],s17 offen offset:3072 sc1 lds
mm v[216:219],a[76:79],v[188:191],0
ex v203,v203
mm v[216:219],a[72:75],v[184:187],v[216:219]
cv v229,v202,v203
mm v[216:219],a[68:71],v[180:183],v[216:219]
ex v196,v196
mm v[216:219],a[64:67],v[176:179],v[216:219]
cv v235,v6,v7
cv v238,v12,v13
mm v[212:215],a[80:83],v[160:163],0
ex v197,v197
s_add_u32 s17, s17, s80
mm v[212:215],a[84:87],v[164:167],v[212:215]
cv v226,v196,v197
s_add_u32 m0, m0, 16384
s_and_b32 m0, m0, s78
mm v[212:215],a[88:91],v[168:171],v[212:215]
ex v198,v198
mm v[212:215],a[92:95],v[172:175],v[212:215]
cv v239,v14,v15
wt lgkmcnt(0)
mm v[220:223],a[92:95],v[188:191],0
ex v199,v199
mm v[220:223],a[88:91],v[184:187],v[220:223]
cv v227,v198,v199
mm v[220:223],a[84:87],v[180:183],v[220:223]
ex v204,v204
mm v[220:223],a[80:83],v[176:179],v[220:223]
wt vmcnt(10)
s_barrier
mm v[64:67],a[96:99],v[232:235],v[64:67]
ex v205,v205
mm v[96:99],a[96:99],v[236:239],v[96:99]
rd a[96:99],v248 offset:8192
cv v230,v204,v205
mm v[100:103],a[100:103],v[236:239],v[100:103]
ex v206,v206
mm v[68:71],a[100:103],v[232:235],v[68:71]
rd a[100:103],v248 offset:9216
.La5_LW2_0:
s_cbranch_execz .La5_LW2_R0
buffer_load_dword v250, v251, s[64:67], s20 offen sc1
mm v[72:75],a[104:107],v[232:235],v[72:75]
ex v207,v207
mm v[104:107],a[104:107],v[236:239],v[104:107]
rd a[104:107],v248 offset:10240
cv v231,v206,v207
mm v[108:111],a[108:111],v[236:239],v[108:111]
ex v208,v208
mm v[76:79],a[108:111],v[232:235],v[76:79]
rd a[108:111],v248 offset:11264
mm v[80:83],a[112:115],v[232:235],v[80:83]
ex v209,v209
mm v[112:115],a[112:115],v[236:239],v[112:115]
rd a[112:115],v248 offset:12288
mm v[116:119],a[116:119],v[236:239],v[116:119]
ex v210,v210
mm v[84:87],a[116:119],v[232:235],v[84:87]
rd a[116:119],v248 offset:13312
mm v[88:91],a[120:123],v[232:235],v[88:91]
ex v211,v211
mm v[120:123],a[120:123],v[236:239],v[120:123]
rd a[120:123],v248 offset:14336
)ASM" R"ASM(s_bitcmp1_b64 s[98:99], s85
mm v[124:127],a[124:127],v[236:239],v[124:127]
ex v216,v216
mm v[92:95],a[124:127],v[232:235],v[92:95]
rd a[124:127],v248 offset:15360
v_or_b32 v248, s89, v255
v_smfmac_f32_16x16x64_bf16 a[196:199], a[208:211], v[232:239], v241
wt lgkmcnt(4)
v_smfmac_f32_16x16x64_bf16 a[192:195], a[208:211], v[224:231], v241
rd a[64:67],v248 offset:0
mm v[0:3],a[96:99],v[224:227],0
ex v217,v217
mm v[32:35],a[96:99],v[228:231],0
rd a[68:71],v248 offset:1024
cv v232,v208,v209
mm v[36:39],a[100:103],v[228:231],0
ex v218,v218
mm v[4:7],a[100:103],v[224:227],0
rd a[72:75],v248 offset:2048
cv v233,v210,v211
mm v[8:11],a[104:107],v[224:227],0
ex v219,v219
mm v[40:43],a[104:107],v[228:231],0
rd a[76:79],v248 offset:3072
cv v236,v216,v217
mm v[44:47],a[108:111],v[228:231],0
ex v212,v212
mm v[12:15],a[108:111],v[224:227],0
rd a[80:83],v248 offset:4096
wt lgkmcnt(5)
mm v[16:19],a[112:115],v[224:227],0
ex v213,v213
mm v[48:51],a[112:115],v[228:231],0
rd a[84:87],v248 offset:5120
cv v237,v218,v219
mm v[52:55],a[116:119],v[228:231],0
ex v214,v214
mm v[20:23],a[116:119],v[224:227],0
rd a[88:91],v248 offset:6144
cv v234,v212,v213
mm v[24:27],a[120:123],v[224:227],0
ex v215,v215
mm v[56:59],a[120:123],v[228:231],0
rd a[92:95],v248 offset:7168
cv v235,v214,v215
mm v[60:63],a[124:127],v[228:231],0
ex v220,v220
mm v[28:31],a[124:127],v[224:227],0
wt vmcnt(5)
buffer_store_dwordx2 v[252:253], off, s[64:67], s86 sc1
s_mov_b32 s86, 1073741824
s_bitset0_b32 s20, 30
s_cbranch_scc1 .La5_LA_FE0
.La5_LA_FS0:
wt lgkmcnt(4)
mm v[192:195],a[64:67],v[128:131],0
ex v221,v221
mm v[192:195],a[68:71],v[132:135],v[192:195]
cv v238,v220,v221
s_add_u32 s85, s85, 1
mm v[192:195],a[72:75],v[136:139],v[192:195]
ex v222,v222
mm v[192:195],a[76:79],v[140:143],v[192:195]
ld v255,s[60:63],s17 offen offset:0 sc1 lds
mm v[200:203],a[76:79],v[156:159],0
ex v223,v223
mm v[200:203],a[72:75],v[152:155],v[200:203]
cv v239,v222,v223
s_add_u32 s89, s89, 16384
mm v[200:203],a[68:71],v[148:151],v[200:203]
ex v192,v192
mm v[200:203],a[64:67],v[144:147],v[200:203]
wt lgkmcnt(0)
s_and_b32 s89, s89, 0x1ffff
mm v[196:199],a[80:83],v[128:131],0
ex v193,v193
mm v[196:199],a[84:87],v[132:135],v[196:199]
cv v224,v192,v193
mm v[196:199],a[88:91],v[136:139],v[196:199]
ex v194,v194
mm v[196:199],a[92:95],v[140:143],v[196:199]
ld v255,s[60:63],s17 offen offset:1024 sc1 lds
mm v[204:207],a[92:95],v[156:159],0
ex v195,v195
mm v[204:207],a[88:91],v[152:155],v[204:207]
cv v225,v194,v195
mm v[204:207],a[84:87],v[148:151],v[204:207]
ex v200,v200
mm v[204:207],a[80:83],v[144:147],v[204:207]
ld v255,s[60:63],s17 offen offset:2048 sc1 lds
mm v[208:211],a[64:67],v[160:163],0
ex v201,v201
mm v[208:211],a[68:71],v[164:167],v[208:211]
cv v228,v200,v201
mm v[208:211],a[72:75],v[168:171],v[208:211]
ex v202,v202
mm v[208:211],a[76:79],v[172:175],v[208:211]
ld v255,s[60:63],s17 offen offset:3072 sc1 lds
mm v[216:219],a[76:79],v[188:191],0
ex v203,v203
)ASM" R"ASM(mm v[216:219],a[72:75],v[184:187],v[216:219]
cv v229,v202,v203
mm v[216:219],a[68:71],v[180:183],v[216:219]
ex v196,v196
mm v[216:219],a[64:67],v[176:179],v[216:219]
buffer_load_dword a253, v254, s[36:39], s90 offen
mm v[212:215],a[80:83],v[160:163],0
ex v197,v197
mm v[212:215],a[84:87],v[164:167],v[212:215]
cv v226,v196,v197
s_add_u32 s90, s90, s21
mm v[212:215],a[88:91],v[168:171],v[212:215]
ex v198,v198
mm v[212:215],a[92:95],v[172:175],v[212:215]
s_add_u32 s17, s17, s80
s_add_u32 m0, m0, 16384
mm v[220:223],a[92:95],v[188:191],0
ex v199,v199
mm v[220:223],a[88:91],v[184:187],v[220:223]
cv v227,v198,v199
s_and_b32 m0, m0, s78
mm v[220:223],a[84:87],v[180:183],v[220:223]
ex v204,v204
mm v[220:223],a[80:83],v[176:179],v[220:223]
wt vmcnt(11)
s_barrier
mm v[64:67],a[96:99],v[232:235],v[64:67]
ex v205,v205
mm v[96:99],a[96:99],v[236:239],v[96:99]
rd a[96:99],v248 offset:8192
cv v230,v204,v205
mm v[100:103],a[100:103],v[236:239],v[100:103]
ex v206,v206
mm v[68:71],a[100:103],v[232:235],v[68:71]
rd a[100:103],v248 offset:9216
s_cmp_eq_u32 s85, s81
buffer_load_dword v250, v251, s[64:67], s20 offen sc1
mm v[72:75],a[104:107],v[232:235],v[72:75]
ex v207,v207
mm v[104:107],a[104:107],v[236:239],v[104:107]
rd a[104:107],v248 offset:10240
cv v231,v206,v207
mm v[108:111],a[108:111],v[236:239],v[108:111]
ex v208,v208
mm v[76:79],a[108:111],v[232:235],v[76:79]
rd a[108:111],v248 offset:11264
s_cselect_b32 s17, s77, s17
mm v[80:83],a[112:115],v[232:235],v[80:83]
ex v209,v209
mm v[112:115],a[112:115],v[236:239],v[112:115]
rd a[112:115],v248 offset:12288
s_cselect_b32 s80, -8192, s80
mm v[116:119],a[116:119],v[236:239],v[116:119]
ex v210,v210
mm v[84:87],a[116:119],v[232:235],v[84:87]
rd a[116:119],v248 offset:13312
mm v[88:91],a[120:123],v[232:235],v[88:91]
ex v211,v211
mm v[120:123],a[120:123],v[236:239],v[120:123]
rd a[120:123],v248 offset:14336
s_bitcmp1_b64 s[98:99], s85
mm v[124:127],a[124:127],v[236:239],v[124:127]
ex v216,v216
mm v[92:95],a[124:127],v[232:235],v[92:95]
rd a[124:127],v248 offset:15360
v_or_b32 v248, s89, v255
v_smfmac_f32_16x16x64_bf16 a[196:199], a[208:211], v[232:239], v241
wt lgkmcnt(4)
v_smfmac_f32_16x16x64_bf16 a[192:195], a[208:211], v[224:231], v241
rd a[64:67],v248 offset:0
mm v[0:3],a[96:99],v[224:227],v[0:3]
ex v217,v217
mm v[32:35],a[96:99],v[228:231],v[32:35]
rd a[68:71],v248 offset:1024
cv v232,v208,v209
mm v[36:39],a[100:103],v[228:231],v[36:39]
ex v218,v218
mm v[4:7],a[100:103],v[224:227],v[4:7]
rd a[72:75],v248 offset:2048
cv v233,v210,v211
mm v[8:11],a[104:107],v[224:227],v[8:11]
ex v219,v219
mm v[40:43],a[104:107],v[228:231],v[40:43]
rd a[76:79],v248 offset:3072
cv v236,v216,v217
mm v[44:47],a[108:111],v[228:231],v[44:47]
ex v212,v212
mm v[12:15],a[108:111],v[224:227],v[12:15]
rd a[80:83],v248 offset:4096
wt lgkmcnt(5)
mm v[16:19],a[112:115],v[224:227],v[16:19]
ex v213,v213
mm v[48:51],a[112:115],v[228:231],v[48:51]
rd a[84:87],v248 offset:5120
cv v237,v218,v219
mm v[52:55],a[116:119],v[228:231],v[52:55]
ex v214,v214
mm v[20:23],a[116:119],v[224:227],v[20:23]
rd a[88:91],v248 offset:6144
cv v234,v212,v213
)ASM" R"ASM(mm v[24:27],a[120:123],v[224:227],v[24:27]
ex v215,v215
mm v[56:59],a[120:123],v[228:231],v[56:59]
rd a[92:95],v248 offset:7168
cv v235,v214,v215
mm v[60:63],a[124:127],v[228:231],v[60:63]
v_exp_f32_e64 v220, v220
mm v[28:31],a[124:127],v[224:227],v[28:31]
s_cbranch_scc1 .La5_LA_F1E2
.p2align 6
; ---- steady windows
.La5_LA_F1:
wt lgkmcnt(4)
mm v[192:195],a[64:67],v[128:131],0
ex v221,v221
mm v[192:195],a[68:71],v[132:135],v[192:195]
cv v238,v220,v221
s_add_u32 s85, s85, 1
mm v[192:195],a[72:75],v[136:139],v[192:195]
ex v222,v222
mm v[192:195],a[76:79],v[140:143],v[192:195]
ld v255,s[60:63],s17 offen offset:0 sc1 lds
mm v[200:203],a[76:79],v[156:159],0
ex v223,v223
mm v[200:203],a[72:75],v[152:155],v[200:203]
cv v239,v222,v223
s_add_u32 s89, s89, 16384
mm v[200:203],a[68:71],v[148:151],v[200:203]
ex v192,v192
mm v[200:203],a[64:67],v[144:147],v[200:203]
wt lgkmcnt(0)
s_and_b32 s89, s89, 0x1ffff
mm v[196:199],a[80:83],v[128:131],0
ex v193,v193
mm v[196:199],a[84:87],v[132:135],v[196:199]
cv v224,v192,v193
mm v[196:199],a[88:91],v[136:139],v[196:199]
ex v194,v194
mm v[196:199],a[92:95],v[140:143],v[196:199]
ld v255,s[60:63],s17 offen offset:1024 sc1 lds
mm v[204:207],a[92:95],v[156:159],0
ex v195,v195
mm v[204:207],a[88:91],v[152:155],v[204:207]
cv v225,v194,v195
mm v[204:207],a[84:87],v[148:151],v[204:207]
ex v200,v200
mm v[204:207],a[80:83],v[144:147],v[204:207]
ld v255,s[60:63],s17 offen offset:2048 sc1 lds
mm v[208:211],a[64:67],v[160:163],0
ex v201,v201
mm v[208:211],a[68:71],v[164:167],v[208:211]
cv v228,v200,v201
mm v[208:211],a[72:75],v[168:171],v[208:211]
ex v202,v202
mm v[208:211],a[76:79],v[172:175],v[208:211]
ld v255,s[60:63],s17 offen offset:3072 sc1 lds
mm v[216:219],a[76:79],v[188:191],0
ex v203,v203
mm v[216:219],a[72:75],v[184:187],v[216:219]
cv v229,v202,v203
mm v[216:219],a[68:71],v[180:183],v[216:219]
ex v196,v196
mm v[216:219],a[64:67],v[176:179],v[216:219]
buffer_load_dword a253, v254, s[36:39], s90 offen
mm v[212:215],a[80:83],v[160:163],0
ex v197,v197
mm v[212:215],a[84:87],v[164:167],v[212:215]
cv v226,v196,v197
s_add_u32 s90, s90, s21
mm v[212:215],a[88:91],v[168:171],v[212:215]
ex v198,v198
mm v[212:215],a[92:95],v[172:175],v[212:215]
s_add_u32 s17, s17, s80
s_add_u32 m0, m0, 16384
mm v[220:223],a[92:95],v[188:191],0
ex v199,v199
mm v[220:223],a[88:91],v[184:187],v[220:223]
cv v227,v198,v199
s_and_b32 m0, m0, s78
mm v[220:223],a[84:87],v[180:183],v[220:223]
ex v204,v204
mm v[220:223],a[80:83],v[176:179],v[220:223]
wt vmcnt(11)
s_barrier
mm v[64:67],a[96:99],v[232:235],v[64:67]
ex v205,v205
mm v[96:99],a[96:99],v[236:239],v[96:99]
rd a[96:99],v248 offset:8192
cv v230,v204,v205
mm v[100:103],a[100:103],v[236:239],v[100:103]
ex v206,v206
mm v[68:71],a[100:103],v[232:235],v[68:71]
rd a[100:103],v248 offset:9216
s_cmp_eq_u32 s85, s81
mm v[72:75],a[104:107],v[232:235],v[72:75]
ex v207,v207
mm v[104:107],a[104:107],v[236:239],v[104:107]
rd a[104:107],v248 offset:10240
cv v231,v206,v207
mm v[108:111],a[108:111],v[236:239],v[108:111]
)ASM" R"ASM(ex v208,v208
mm v[76:79],a[108:111],v[232:235],v[76:79]
rd a[108:111],v248 offset:11264
s_cselect_b32 s17, s77, s17
mm v[80:83],a[112:115],v[232:235],v[80:83]
ex v209,v209
mm v[112:115],a[112:115],v[236:239],v[112:115]
rd a[112:115],v248 offset:12288
s_cselect_b32 s80, -8192, s80
mm v[116:119],a[116:119],v[236:239],v[116:119]
ex v210,v210
mm v[84:87],a[116:119],v[232:235],v[84:87]
rd a[116:119],v248 offset:13312
mm v[88:91],a[120:123],v[232:235],v[88:91]
ex v211,v211
mm v[120:123],a[120:123],v[236:239],v[120:123]
rd a[120:123],v248 offset:14336
s_bitcmp1_b64 s[98:99], s85
mm v[124:127],a[124:127],v[236:239],v[124:127]
ex v216,v216
mm v[92:95],a[124:127],v[232:235],v[92:95]
rd a[124:127],v248 offset:15360
v_or_b32 v248, s89, v255
v_smfmac_f32_16x16x64_bf16 a[196:199], a[208:211], v[232:239], v241
wt lgkmcnt(4)
v_smfmac_f32_16x16x64_bf16 a[192:195], a[208:211], v[224:231], v241
rd a[64:67],v248 offset:0
mm v[0:3],a[96:99],v[224:227],v[0:3]
ex v217,v217
mm v[32:35],a[96:99],v[228:231],v[32:35]
rd a[68:71],v248 offset:1024
cv v232,v208,v209
mm v[36:39],a[100:103],v[228:231],v[36:39]
ex v218,v218
mm v[4:7],a[100:103],v[224:227],v[4:7]
rd a[72:75],v248 offset:2048
cv v233,v210,v211
mm v[8:11],a[104:107],v[224:227],v[8:11]
ex v219,v219
mm v[40:43],a[104:107],v[228:231],v[40:43]
rd a[76:79],v248 offset:3072
cv v236,v216,v217
mm v[44:47],a[108:111],v[228:231],v[44:47]
ex v212,v212
mm v[12:15],a[108:111],v[224:227],v[12:15]
rd a[80:83],v248 offset:4096
wt lgkmcnt(5)
mm v[16:19],a[112:115],v[224:227],v[16:19]
ex v213,v213
mm v[48:51],a[112:115],v[228:231],v[48:51]
rd a[84:87],v248 offset:5120
cv v237,v218,v219
mm v[52:55],a[116:119],v[228:231],v[52:55]
ex v214,v214
mm v[20:23],a[116:119],v[224:227],v[20:23]
rd a[88:91],v248 offset:6144
cv v234,v212,v213
mm v[24:27],a[120:123],v[224:227],v[24:27]
ex v215,v215
mm v[56:59],a[120:123],v[228:231],v[56:59]
rd a[92:95],v248 offset:7168
cv v235,v214,v215
mm v[60:63],a[124:127],v[228:231],v[60:63]
ex v220,v220
mm v[28:31],a[124:127],v[224:227],v[28:31]
s_cbranch_scc1 .La5_LA_F1E2
wt lgkmcnt(4)
mm v[192:195],a[64:67],v[128:131],0
ex v221,v221
mm v[192:195],a[68:71],v[132:135],v[192:195]
cv v238,v220,v221
s_add_u32 s85, s85, 1
mm v[192:195],a[72:75],v[136:139],v[192:195]
ex v222,v222
mm v[192:195],a[76:79],v[140:143],v[192:195]
ld v255,s[60:63],s17 offen offset:0 sc1 lds
mm v[200:203],a[76:79],v[156:159],0
ex v223,v223
mm v[200:203],a[72:75],v[152:155],v[200:203]
cv v239,v222,v223
s_add_u32 s89, s89, 16384
mm v[200:203],a[68:71],v[148:151],v[200:203]
ex v192,v192
mm v[200:203],a[64:67],v[144:147],v[200:203]
wt lgkmcnt(0)
s_and_b32 s89, s89, 0x1ffff
mm v[196:199],a[80:83],v[128:131],0
ex v193,v193
mm v[196:199],a[84:87],v[132:135],v[196:199]
cv v224,v192,v193
mm v[196:199],a[88:91],v[136:139],v[196:199]
ex v194,v194
mm v[196:199],a[92:95],v[140:143],v[196:199]
ld v255,s[60:63],s17 offen offset:1024 sc1 lds
mm v[204:207],a[92:95],v[156:159],0
ex v195,v195
mm v[204:207],a[88:91],v[152:155],v[204:207]
cv v225,v194,v195
mm v[204:207],a[84:87],v[148:151],v[204:207]
ex v200,v200
)ASM" R"ASM(mm v[204:207],a[80:83],v[144:147],v[204:207]
ld v255,s[60:63],s17 offen offset:2048 sc1 lds
mm v[208:211],a[64:67],v[160:163],0
ex v201,v201
mm v[208:211],a[68:71],v[164:167],v[208:211]
cv v228,v200,v201
mm v[208:211],a[72:75],v[168:171],v[208:211]
ex v202,v202
mm v[208:211],a[76:79],v[172:175],v[208:211]
ld v255,s[60:63],s17 offen offset:3072 sc1 lds
mm v[216:219],a[76:79],v[188:191],0
ex v203,v203
mm v[216:219],a[72:75],v[184:187],v[216:219]
cv v229,v202,v203
mm v[216:219],a[68:71],v[180:183],v[216:219]
ex v196,v196
mm v[216:219],a[64:67],v[176:179],v[216:219]
buffer_load_dword a253, v254, s[36:39], s90 offen
mm v[212:215],a[80:83],v[160:163],0
ex v197,v197
mm v[212:215],a[84:87],v[164:167],v[212:215]
cv v226,v196,v197
s_add_u32 s90, s90, s21
mm v[212:215],a[88:91],v[168:171],v[212:215]
ex v198,v198
mm v[212:215],a[92:95],v[172:175],v[212:215]
s_add_u32 s17, s17, s80
s_add_u32 m0, m0, 16384
mm v[220:223],a[92:95],v[188:191],0
ex v199,v199
mm v[220:223],a[88:91],v[184:187],v[220:223]
cv v227,v198,v199
s_and_b32 m0, m0, s78
mm v[220:223],a[84:87],v[180:183],v[220:223]
ex v204,v204
mm v[220:223],a[80:83],v[176:179],v[220:223]
wt vmcnt(10)
s_barrier
mm v[64:67],a[96:99],v[232:235],v[64:67]
ex v205,v205
mm v[96:99],a[96:99],v[236:239],v[96:99]
rd a[96:99],v248 offset:8192
cv v230,v204,v205
mm v[100:103],a[100:103],v[236:239],v[100:103]
ex v206,v206
mm v[68:71],a[100:103],v[232:235],v[68:71]
rd a[100:103],v248 offset:9216
s_cmp_eq_u32 s85, s81
mm v[72:75],a[104:107],v[232:235],v[72:75]
ex v207,v207
mm v[104:107],a[104:107],v[236:239],v[104:107]
rd a[104:107],v248 offset:10240
cv v231,v206,v207
mm v[108:111],a[108:111],v[236:239],v[108:111]
ex v208,v208
mm v[76:79],a[108:111],v[232:235],v[76:79]
rd a[108:111],v248 offset:11264
s_cselect_b32 s17, s77, s17
mm v[80:83],a[112:115],v[232:235],v[80:83]
ex v209,v209
mm v[112:115],a[112:115],v[236:239],v[112:115]
rd a[112:115],v248 offset:12288
s_cselect_b32 s80, -8192, s80
mm v[116:119],a[116:119],v[236:239],v[116:119]
ex v210,v210
mm v[84:87],a[116:119],v[232:235],v[84:87]
rd a[116:119],v248 offset:13312
mm v[88:91],a[120:123],v[232:235],v[88:91]
ex v211,v211
mm v[120:123],a[120:123],v[236:239],v[120:123]
rd a[120:123],v248 offset:14336
s_bitcmp1_b64 s[98:99], s85
mm v[124:127],a[124:127],v[236:239],v[124:127]
ex v216,v216
mm v[92:95],a[124:127],v[232:235],v[92:95]
rd a[124:127],v248 offset:15360
v_or_b32 v248, s89, v255
v_smfmac_f32_16x16x64_bf16 a[196:199], a[208:211], v[232:239], v241
wt lgkmcnt(4)
v_smfmac_f32_16x16x64_bf16 a[192:195], a[208:211], v[224:231], v241
rd a[64:67],v248 offset:0
mm v[0:3],a[96:99],v[224:227],v[0:3]
ex v217,v217
mm v[32:35],a[96:99],v[228:231],v[32:35]
rd a[68:71],v248 offset:1024
cv v232,v208,v209
mm v[36:39],a[100:103],v[228:231],v[36:39]
ex v218,v218
mm v[4:7],a[100:103],v[224:227],v[4:7]
rd a[72:75],v248 offset:2048
cv v233,v210,v211
mm v[8:11],a[104:107],v[224:227],v[8:11]
ex v219,v219
mm v[40:43],a[104:107],v[228:231],v[40:43]
rd a[76:79],v248 offset:3072
cv v236,v216,v217
mm v[44:47],a[108:111],v[228:231],v[44:47]
ex v212,v212
mm v[12:15],a[108:111],v[224:227],v[12:15]
rd a[80:83],v248 offset:4096
)ASM" R"ASM(wt lgkmcnt(5)
mm v[16:19],a[112:115],v[224:227],v[16:19]
ex v213,v213
mm v[48:51],a[112:115],v[228:231],v[48:51]
rd a[84:87],v248 offset:5120
cv v237,v218,v219
mm v[52:55],a[116:119],v[228:231],v[52:55]
ex v214,v214
mm v[20:23],a[116:119],v[224:227],v[20:23]
rd a[88:91],v248 offset:6144
cv v234,v212,v213
mm v[24:27],a[120:123],v[224:227],v[24:27]
ex v215,v215
mm v[56:59],a[120:123],v[228:231],v[56:59]
rd a[92:95],v248 offset:7168
cv v235,v214,v215
mm v[60:63],a[124:127],v[228:231],v[60:63]
ex v220,v220
mm v[28:31],a[124:127],v[224:227],v[28:31]
.La5_LA_F1E:
s_cbranch_scc0 .La5_LA_F1
.La5_LA_F1E2:
s_cmp_eq_u32 s85, s82
s_cbranch_scc1 .La5_LA_F1X
s_call_b64 s[24:25], .La5_LA_EVENT
s_branch .La5_LA_F1
.La5_LA_FE0:
s_nop 0
s_nop 0
.La5_LW2_1:
s_cbranch_execz .La5_LW2_R1
s_cmp_eq_u32 s85, s82
s_cbranch_scc1 .La5_LA_F1X
s_call_b64 s[24:25], .La5_LA_EVENT
s_branch .La5_LA_FS0
; ---- hand-offs between windows
.La5_LA_EVENT:
s_cmp_lg_u32 s85, 2
s_nop 0
s_nop 0
s_cbranch_scc1 .La5_LA_E1
wt vmcnt(0)
v_cmp_eq_u32 vcc, v250, v249
s_mov_b32 s17, s16
s_mov_b32 s78, 0x1ffff
s_lshl_b32 m0, s3, 12
s_or_b32 vcc_hi, vcc_hi, s79
s_cmp_eq_u64 vcc, -1
s_cbranch_scc0 .La5_LA_KSPIN
.La5_LA_KOK:
ld v255,s[60:63],s17 offen offset:0 sc1 lds
ld v255,s[60:63],s17 offen offset:1024 sc1 lds
ld v255,s[60:63],s17 offen offset:2048 sc1 lds
ld v255,s[60:63],s17 offen offset:3072 sc1 lds
s_add_u32 s17, s17, s80
s_add_u32 m0, m0, 16384
s_mov_b32 s20, s19
v_and_b32 v251, 4, v251
s_cmp_lg_u32 s3, 0
s_cbranch_scc1 .La5_LA_POLL
s_nop 0
ld v255,s[60:63],s17 offen offset:0 sc1 lds
ld v255,s[60:63],s17 offen offset:1024 sc1 lds
ld v255,s[60:63],s17 offen offset:2048 sc1 lds
ld v255,s[60:63],s17 offen offset:3072 sc1 lds
s_add_u32 s17, s17, s80
s_add_u32 m0, m0, 16384
.La5_LA_POLL:
s_cmp_lg_u32 s22, 0
s_nop 0
s_nop 0
s_cbranch_scc1 .La5_LA_WP
s_mov_b64 exec, 0
s_cmp_eq_u32 s3, 0
s_cbranch_scc1 .La5_LW5_0
s_cmp_eq_u32 s3, 1
s_cbranch_scc1 .La5_LW5_1
s_cmp_eq_u32 s3, 2
s_cbranch_scc1 .La5_LW5_2
s_branch .La5_LW5_3
.La5_LW5_R0:
s_mov_b64 exec, -1
.La5_LA_RETURN:
s_setpc_b64 s[24:25]
.La5_LA_WP:
s_mov_b64 exec, 0
s_cmp_eq_u32 s3, 0
s_cbranch_scc1 .La5_LW6_0
s_cmp_eq_u32 s3, 1
s_cbranch_scc1 .La5_LW6_1
s_cmp_eq_u32 s3, 2
s_cbranch_scc1 .La5_LW6_2
s_branch .La5_LW6_3
.La5_LW6_R0:
s_mov_b64 exec, -1
s_cmp_eq_u32 s73, 1
s_cbranch_scc1 .La5_LA_E0
buffer_load_dword v250, v251, s[64:67], s20 offen sc1
s_setpc_b64 s[24:25]
.La5_LA_E0:
)ASM" R"ASM(s_mov_b32 s54, 16384
ld a[0:3],v255,s[52:55],0 offen offset:0 sc1
ld a[4:7],v255,s[52:55],0 offen offset:1024 sc1
ld a[8:11],v255,s[52:55],0 offen offset:2048 sc1
ld a[12:15],v255,s[52:55],0 offen offset:3072 sc1
ld a[16:19],v255,s[52:55],s95 offen offset:0 sc1
ld a[20:23],v255,s[52:55],s95 offen offset:1024 sc1
s_setpc_b64 s[24:25]
.La5_LA_E1:
ld a[24:27],v255,s[52:55],s95 offen offset:2048 sc1
ld a[28:31],v255,s[52:55],s95 offen offset:3072 sc1
ld a[32:35],v255,s[52:55],s96 offen offset:0 sc1
ld a[36:39],v255,s[52:55],s96 offen offset:1024 sc1
.La5_LW2_2:
s_cbranch_execz .La5_LW2_R0
ld a[40:43],v255,s[52:55],s96 offen offset:2048 sc1
ld a[44:47],v255,s[52:55],s96 offen offset:3072 sc1
ld a[48:51],v255,s[52:55],s97 offen offset:0 sc1
ld a[52:55],v255,s[52:55],s97 offen offset:1024 sc1
ld a[56:59],v255,s[52:55],s97 offen offset:2048 sc1
ld a[60:63],v255,s[52:55],s97 offen offset:3072 sc1
s_setpc_b64 s[24:25]
; ---- helper: the partner-s q rows arrive during the last windows of the own block
.La5_LA_F1X:
s_cmp_eq_u32 s75, 0
s_nop 0
s_cbranch_scc1 .La5_LA_DC3
s_cmp_eq_u32 s22, 0
s_cbranch_scc1 .La5_LA_DC
s_cmp_eq_u32 s73, 1
s_cbranch_scc1 .La5_LA_F1Y
s_nop 0
v_cmp_eq_u32 vcc, v250, v249
s_mov_b32 s54, 16384
s_and_b32 s92, vcc_lo, 3
s_cmp_eq_u32 s92, 3
s_cbranch_scc0 .La5_LA_QSPIN
.La5_LA_QOK:
wt lgkmcnt(4)
mm v[192:195],a[64:67],v[128:131],0
ex v221,v221
mm v[192:195],a[68:71],v[132:135],v[192:195]
cv v238,v220,v221
s_add_u32 s85, s85, 1
mm v[192:195],a[72:75],v[136:139],v[192:195]
ex v222,v222
mm v[192:195],a[76:79],v[140:143],v[192:195]
ld v255,s[60:63],s17 offen offset:0 sc1 lds
mm v[200:203],a[76:79],v[156:159],0
ex v223,v223
mm v[200:203],a[72:75],v[152:155],v[200:203]
cv v239,v222,v223
s_add_u32 s89, s89, 16384
mm v[200:203],a[68:71],v[148:151],v[200:203]
ex v192,v192
mm v[200:203],a[64:67],v[144:147],v[200:203]
wt lgkmcnt(0)
s_and_b32 s89, s89, 0x1ffff
mm v[196:199],a[80:83],v[128:131],0
ex v193,v193
mm v[196:199],a[84:87],v[132:135],v[196:199]
cv v224,v192,v193
mm v[196:199],a[88:91],v[136:139],v[196:199]
ex v194,v194
mm v[196:199],a[92:95],v[140:143],v[196:199]
ld v255,s[60:63],s17 offen offset:1024 sc1 lds
mm v[204:207],a[92:95],v[156:159],0
ex v195,v195
mm v[204:207],a[88:91],v[152:155],v[204:207]
cv v225,v194,v195
mm v[204:207],a[84:87],v[148:151],v[204:207]
ex v200,v200
mm v[204:207],a[80:83],v[144:147],v[204:207]
ld v255,s[60:63],s17 offen offset:2048 sc1 lds
mm v[208:211],a[64:67],v[160:163],0
ex v201,v201
mm v[208:211],a[68:71],v[164:167],v[208:211]
cv v228,v200,v201
mm v[208:211],a[72:75],v[168:171],v[208:211]
ex v202,v202
mm v[208:211],a[76:79],v[172:175],v[208:211]
ld v255,s[60:63],s17 offen offset:3072 sc1 lds
mm v[216:219],a[76:79],v[188:191],0
ex v203,v203
mm v[216:219],a[72:75],v[184:187],v[216:219]
cv v229,v202,v203
mm v[216:219],a[68:71],v[180:183],v[216:219]
ex v196,v196
mm v[216:219],a[64:67],v[176:179],v[216:219]
ld a[0:3],v255,s[52:55],0 offen offset:0 sc1
mm v[212:215],a[80:83],v[160:163],0
ex v197,v197
mm v[212:215],a[84:87],v[164:167],v[212:215]
cv v226,v196,v197
s_add_u32 s17, s17, s80
mm v[212:215],a[88:91],v[168:171],v[212:215]
ex v198,v198
mm v[212:215],a[92:95],v[172:175],v[212:215]
s_add_u32 m0, m0, 16384
s_and_b32 m0, m0, s78
)ASM" R"ASM(ld a[4:7],v255,s[52:55],0 offen offset:1024 sc1
mm v[220:223],a[92:95],v[188:191],0
ex v199,v199
mm v[220:223],a[88:91],v[184:187],v[220:223]
cv v227,v198,v199
mm v[220:223],a[84:87],v[180:183],v[220:223]
ex v204,v204
mm v[220:223],a[80:83],v[176:179],v[220:223]
wt vmcnt(12)
s_barrier
mm v[64:67],a[96:99],v[232:235],v[64:67]
ex v205,v205
mm v[96:99],a[96:99],v[236:239],v[96:99]
rd a[96:99],v248 offset:8192
cv v230,v204,v205
mm v[100:103],a[100:103],v[236:239],v[100:103]
ex v206,v206
mm v[68:71],a[100:103],v[232:235],v[68:71]
rd a[100:103],v248 offset:9216
ld a[8:11],v255,s[52:55],0 offen offset:2048 sc1
mm v[72:75],a[104:107],v[232:235],v[72:75]
ex v207,v207
mm v[104:107],a[104:107],v[236:239],v[104:107]
rd a[104:107],v248 offset:10240
cv v231,v206,v207
mm v[108:111],a[108:111],v[236:239],v[108:111]
ex v208,v208
mm v[76:79],a[108:111],v[232:235],v[76:79]
rd a[108:111],v248 offset:11264
ld a[12:15],v255,s[52:55],0 offen offset:3072 sc1
mm v[80:83],a[112:115],v[232:235],v[80:83]
ex v209,v209
mm v[112:115],a[112:115],v[236:239],v[112:115]
rd a[112:115],v248 offset:12288
mm v[116:119],a[116:119],v[236:239],v[116:119]
ex v210,v210
mm v[84:87],a[116:119],v[232:235],v[84:87]
rd a[116:119],v248 offset:13312
ld a[16:19],v255,s[52:55],s95 offen offset:0 sc1
mm v[88:91],a[120:123],v[232:235],v[88:91]
ex v211,v211
mm v[120:123],a[120:123],v[236:239],v[120:123]
rd a[120:123],v248 offset:14336
s_bitcmp1_b64 s[98:99], s85
mm v[124:127],a[124:127],v[236:239],v[124:127]
ex v216,v216
mm v[92:95],a[124:127],v[232:235],v[92:95]
rd a[124:127],v248 offset:15360
v_or_b32 v248, s89, v255
v_smfmac_f32_16x16x64_bf16 a[196:199], a[208:211], v[232:239], v241
wt lgkmcnt(4)
v_smfmac_f32_16x16x64_bf16 a[192:195], a[208:211], v[224:231], v241
rd a[64:67],v248 offset:0
mm v[0:3],a[96:99],v[224:227],v[0:3]
ex v217,v217
mm v[32:35],a[96:99],v[228:231],v[32:35]
rd a[68:71],v248 offset:1024
cv v232,v208,v209
mm v[36:39],a[100:103],v[228:231],v[36:39]
ex v218,v218
mm v[4:7],a[100:103],v[224:227],v[4:7]
rd a[72:75],v248 offset:2048
cv v233,v210,v211
mm v[8:11],a[104:107],v[224:227],v[8:11]
ex v219,v219
mm v[40:43],a[104:107],v[228:231],v[40:43]
rd a[76:79],v248 offset:3072
cv v236,v216,v217
mm v[44:47],a[108:111],v[228:231],v[44:47]
ex v212,v212
mm v[12:15],a[108:111],v[224:227],v[12:15]
rd a[80:83],v248 offset:4096
wt lgkmcnt(5)
mm v[16:19],a[112:115],v[224:227],v[16:19]
ex v213,v213
mm v[48:51],a[112:115],v[228:231],v[48:51]
rd a[84:87],v248 offset:5120
cv v237,v218,v219
mm v[52:55],a[116:119],v[228:231],v[52:55]
ex v214,v214
mm v[20:23],a[116:119],v[224:227],v[20:23]
rd a[88:91],v248 offset:6144
cv v234,v212,v213
mm v[24:27],a[120:123],v[224:227],v[24:27]
ex v215,v215
mm v[56:59],a[120:123],v[228:231],v[56:59]
rd a[92:95],v248 offset:7168
cv v235,v214,v215
mm v[60:63],a[124:127],v[228:231],v[60:63]
ex v220,v220
mm v[28:31],a[124:127],v[224:227],v[28:31]
ld a[20:23],v255,s[52:55],s95 offen offset:1024 sc1
wt lgkmcnt(4)
mm v[192:195],a[64:67],v[128:131],0
ex v221,v221
mm v[192:195],a[68:71],v[132:135],v[192:195]
cv v238,v220,v221
s_add_u32 s85, s85, 1
mm v[192:195],a[72:75],v[136:139],v[192:195]
ex v222,v222
)ASM" R"ASM(mm v[192:195],a[76:79],v[140:143],v[192:195]
ld v255,s[60:63],s17 offen offset:0 sc1 lds
mm v[200:203],a[76:79],v[156:159],0
ex v223,v223
mm v[200:203],a[72:75],v[152:155],v[200:203]
cv v239,v222,v223
s_add_u32 s89, s89, 16384
mm v[200:203],a[68:71],v[148:151],v[200:203]
ex v192,v192
mm v[200:203],a[64:67],v[144:147],v[200:203]
wt lgkmcnt(0)
s_and_b32 s89, s89, 0x1ffff
ld a[24:27],v255,s[52:55],s95 offen offset:2048 sc1
mm v[196:199],a[80:83],v[128:131],0
ex v193,v193
mm v[196:199],a[84:87],v[132:135],v[196:199]
cv v224,v192,v193
mm v[196:199],a[88:91],v[136:139],v[196:199]
ex v194,v194
mm v[196:199],a[92:95],v[140:143],v[196:199]
ld v255,s[60:63],s17 offen offset:1024 sc1 lds
mm v[204:207],a[92:95],v[156:159],0
ex v195,v195
mm v[204:207],a[88:91],v[152:155],v[204:207]
cv v225,v194,v195
mm v[204:207],a[84:87],v[148:151],v[204:207]
ex v200,v200
mm v[204:207],a[80:83],v[144:147],v[204:207]
ld v255,s[60:63],s17 offen offset:2048 sc1 lds
mm v[208:211],a[64:67],v[160:163],0
ex v201,v201
mm v[208:211],a[68:71],v[164:167],v[208:211]
cv v228,v200,v201
mm v[208:211],a[72:75],v[168:171],v[208:211]
ex v202,v202
mm v[208:211],a[76:79],v[172:175],v[208:211]
ld v255,s[60:63],s17 offen offset:3072 sc1 lds
mm v[216:219],a[76:79],v[188:191],0
ex v203,v203
mm v[216:219],a[72:75],v[184:187],v[216:219]
cv v229,v202,v203
mm v[216:219],a[68:71],v[180:183],v[216:219]
ex v196,v196
mm v[216:219],a[64:67],v[176:179],v[216:219]
ld a[28:31],v255,s[52:55],s95 offen offset:3072 sc1
mm v[212:215],a[80:83],v[160:163],0
ex v197,v197
mm v[212:215],a[84:87],v[164:167],v[212:215]
cv v226,v196,v197
s_add_u32 s17, s17, s80
mm v[212:215],a[88:91],v[168:171],v[212:215]
ex v198,v198
mm v[212:215],a[92:95],v[172:175],v[212:215]
s_add_u32 m0, m0, 16384
s_and_b32 m0, m0, s78
ld a[32:35],v255,s[52:55],s96 offen offset:0 sc1
mm v[220:223],a[92:95],v[188:191],0
ex v199,v199
mm v[220:223],a[88:91],v[184:187],v[220:223]
cv v227,v198,v199
mm v[220:223],a[84:87],v[180:183],v[220:223]
ex v204,v204
mm v[220:223],a[80:83],v[176:179],v[220:223]
wt vmcnt(18)
s_barrier
mm v[64:67],a[96:99],v[232:235],v[64:67]
ex v205,v205
mm v[96:99],a[96:99],v[236:239],v[96:99]
rd a[96:99],v248 offset:8192
cv v230,v204,v205
mm v[100:103],a[100:103],v[236:239],v[100:103]
ex v206,v206
mm v[68:71],a[100:103],v[232:235],v[68:71]
rd a[100:103],v248 offset:9216
ld a[36:39],v255,s[52:55],s96 offen offset:1024 sc1
mm v[72:75],a[104:107],v[232:235],v[72:75]
ex v207,v207
mm v[104:107],a[104:107],v[236:239],v[104:107]
rd a[104:107],v248 offset:10240
cv v231,v206,v207
mm v[108:111],a[108:111],v[236:239],v[108:111]
ex v208,v208
mm v[76:79],a[108:111],v[232:235],v[76:79]
rd a[108:111],v248 offset:11264
ld a[40:43],v255,s[52:55],s96 offen offset:2048 sc1
mm v[80:83],a[112:115],v[232:235],v[80:83]
ex v209,v209
mm v[112:115],a[112:115],v[236:239],v[112:115]
rd a[112:115],v248 offset:12288
mm v[116:119],a[116:119],v[236:239],v[116:119]
ex v210,v210
mm v[84:87],a[116:119],v[232:235],v[84:87]
rd a[116:119],v248 offset:13312
ld a[44:47],v255,s[52:55],s96 offen offset:3072 sc1
mm v[88:91],a[120:123],v[232:235],v[88:91]
ex v211,v211
mm v[120:123],a[120:123],v[236:239],v[120:123]
rd a[120:123],v248 offset:14336
s_bitcmp1_b64 s[98:99], s85
mm v[124:127],a[124:127],v[236:239],v[124:127]
)ASM" R"ASM(ex v216,v216
mm v[92:95],a[124:127],v[232:235],v[92:95]
rd a[124:127],v248 offset:15360
v_or_b32 v248, s89, v255
v_smfmac_f32_16x16x64_bf16 a[196:199], a[208:211], v[232:239], v241
wt lgkmcnt(4)
v_smfmac_f32_16x16x64_bf16 a[192:195], a[208:211], v[224:231], v241
rd a[64:67],v248 offset:0
mm v[0:3],a[96:99],v[224:227],v[0:3]
ex v217,v217
mm v[32:35],a[96:99],v[228:231],v[32:35]
rd a[68:71],v248 offset:1024
cv v232,v208,v209
mm v[36:39],a[100:103],v[228:231],v[36:39]
ex v218,v218
mm v[4:7],a[100:103],v[224:227],v[4:7]
rd a[72:75],v248 offset:2048
cv v233,v210,v211
mm v[8:11],a[104:107],v[224:227],v[8:11]
ex v219,v219
mm v[40:43],a[104:107],v[228:231],v[40:43]
rd a[76:79],v248 offset:3072
cv v236,v216,v217
mm v[44:47],a[108:111],v[228:231],v[44:47]
ex v212,v212
mm v[12:15],a[108:111],v[224:227],v[12:15]
rd a[80:83],v248 offset:4096
wt lgkmcnt(5)
mm v[16:19],a[112:115],v[224:227],v[16:19]
ex v213,v213
mm v[48:51],a[112:115],v[228:231],v[48:51]
rd a[84:87],v248 offset:5120
cv v237,v218,v219
mm v[52:55],a[116:119],v[228:231],v[52:55]
ex v214,v214
mm v[20:23],a[116:119],v[224:227],v[20:23]
rd a[88:91],v248 offset:6144
cv v234,v212,v213
mm v[24:27],a[120:123],v[224:227],v[24:27]
ex v215,v215
mm v[56:59],a[120:123],v[228:231],v[56:59]
rd a[92:95],v248 offset:7168
cv v235,v214,v215
mm v[60:63],a[124:127],v[228:231],v[60:63]
ex v220,v220
mm v[28:31],a[124:127],v[224:227],v[28:31]
ld a[48:51],v255,s[52:55],s97 offen offset:0 sc1
wt lgkmcnt(4)
mm v[192:195],a[64:67],v[128:131],0
ex v221,v221
mm v[192:195],a[68:71],v[132:135],v[192:195]
cv v238,v220,v221
s_add_u32 s85, s85, 1
mm v[192:195],a[72:75],v[136:139],v[192:195]
ex v222,v222
mm v[192:195],a[76:79],v[140:143],v[192:195]
ld v255,s[60:63],s17 offen offset:0 sc1 lds
mm v[200:203],a[76:79],v[156:159],0
ex v223,v223
mm v[200:203],a[72:75],v[152:155],v[200:203]
cv v239,v222,v223
s_add_u32 s89, s89, 16384
mm v[200:203],a[68:71],v[148:151],v[200:203]
ex v192,v192
mm v[200:203],a[64:67],v[144:147],v[200:203]
wt lgkmcnt(0)
s_and_b32 s89, s89, 0x1ffff
ld a[52:55],v255,s[52:55],s97 offen offset:1024 sc1
mm v[196:199],a[80:83],v[128:131],0
ex v193,v193
mm v[196:199],a[84:87],v[132:135],v[196:199]
cv v224,v192,v193
mm v[196:199],a[88:91],v[136:139],v[196:199]
ex v194,v194
mm v[196:199],a[92:95],v[140:143],v[196:199]
ld v255,s[60:63],s17 offen offset:1024 sc1 lds
mm v[204:207],a[92:95],v[156:159],0
ex v195,v195
mm v[204:207],a[88:91],v[152:155],v[204:207]
cv v225,v194,v195
mm v[204:207],a[84:87],v[148:151],v[204:207]
ex v200,v200
mm v[204:207],a[80:83],v[144:147],v[204:207]
ld v255,s[60:63],s17 offen offset:2048 sc1 lds
mm v[208:211],a[64:67],v[160:163],0
ex v201,v201
mm v[208:211],a[68:71],v[164:167],v[208:211]
cv v228,v200,v201
mm v[208:211],a[72:75],v[168:171],v[208:211]
ex v202,v202
mm v[208:211],a[76:79],v[172:175],v[208:211]
ld v255,s[60:63],s17 offen offset:3072 sc1 lds
mm v[216:219],a[76:79],v[188:191],0
ex v203,v203
mm v[216:219],a[72:75],v[184:187],v[216:219]
cv v229,v202,v203
mm v[216:219],a[68:71],v[180:183],v[216:219]
ex v196,v196
mm v[216:219],a[64:67],v[176:179],v[216:219]
ld a[56:59],v255,s[52:55],s97 offen offset:2048 sc1
)ASM" R"ASM(mm v[212:215],a[80:83],v[160:163],0
ex v197,v197
mm v[212:215],a[84:87],v[164:167],v[212:215]
cv v226,v196,v197
s_add_u32 s17, s17, s80
mm v[212:215],a[88:91],v[168:171],v[212:215]
ex v198,v198
mm v[212:215],a[92:95],v[172:175],v[212:215]
s_add_u32 m0, m0, 16384
s_and_b32 m0, m0, s78
ld a[60:63],v255,s[52:55],s97 offen offset:3072 sc1
mm v[220:223],a[92:95],v[188:191],0
ex v199,v199
mm v[220:223],a[88:91],v[184:187],v[220:223]
cv v227,v198,v199
mm v[220:223],a[84:87],v[180:183],v[220:223]
ex v204,v204
mm v[220:223],a[80:83],v[176:179],v[220:223]
wt vmcnt(24)
s_barrier
mm v[64:67],a[96:99],v[232:235],v[64:67]
ex v205,v205
mm v[96:99],a[96:99],v[236:239],v[96:99]
rd a[96:99],v248 offset:8192
cv v230,v204,v205
mm v[100:103],a[100:103],v[236:239],v[100:103]
ex v206,v206
mm v[68:71],a[100:103],v[232:235],v[68:71]
rd a[100:103],v248 offset:9216
mm v[72:75],a[104:107],v[232:235],v[72:75]
ex v207,v207
mm v[104:107],a[104:107],v[236:239],v[104:107]
rd a[104:107],v248 offset:10240
cv v231,v206,v207
mm v[108:111],a[108:111],v[236:239],v[108:111]
ex v208,v208
mm v[76:79],a[108:111],v[232:235],v[76:79]
rd a[108:111],v248 offset:11264
mm v[80:83],a[112:115],v[232:235],v[80:83]
ex v209,v209
mm v[112:115],a[112:115],v[236:239],v[112:115]
rd a[112:115],v248 offset:12288
mm v[116:119],a[116:119],v[236:239],v[116:119]
ex v210,v210
mm v[84:87],a[116:119],v[232:235],v[84:87]
rd a[116:119],v248 offset:13312
mm v[88:91],a[120:123],v[232:235],v[88:91]
ex v211,v211
mm v[120:123],a[120:123],v[236:239],v[120:123]
rd a[120:123],v248 offset:14336
s_bitcmp1_b64 s[98:99], s85
mm v[124:127],a[124:127],v[236:239],v[124:127]
ex v216,v216
mm v[92:95],a[124:127],v[232:235],v[92:95]
rd a[124:127],v248 offset:15360
v_or_b32 v248, s89, v255
v_smfmac_f32_16x16x64_bf16 a[196:199], a[208:211], v[232:239], v241
wt lgkmcnt(4)
v_smfmac_f32_16x16x64_bf16 a[192:195], a[208:211], v[224:231], v241
rd a[64:67],v248 offset:0
mm v[0:3],a[96:99],v[224:227],v[0:3]
ex v217,v217
mm v[32:35],a[96:99],v[228:231],v[32:35]
rd a[68:71],v248 offset:1024
cv v232,v208,v209
mm v[36:39],a[100:103],v[228:231],v[36:39]
ex v218,v218
mm v[4:7],a[100:103],v[224:227],v[4:7]
rd a[72:75],v248 offset:2048
cv v233,v210,v211
mm v[8:11],a[104:107],v[224:227],v[8:11]
ex v219,v219
mm v[40:43],a[104:107],v[228:231],v[40:43]
rd a[76:79],v248 offset:3072
cv v236,v216,v217
mm v[44:47],a[108:111],v[228:231],v[44:47]
ex v212,v212
mm v[12:15],a[108:111],v[224:227],v[12:15]
rd a[80:83],v248 offset:4096
wt lgkmcnt(5)
mm v[16:19],a[112:115],v[224:227],v[16:19]
ex v213,v213
mm v[48:51],a[112:115],v[228:231],v[48:51]
rd a[84:87],v248 offset:5120
cv v237,v218,v219
mm v[52:55],a[116:119],v[228:231],v[52:55]
ex v214,v214
mm v[20:23],a[116:119],v[224:227],v[20:23]
rd a[88:91],v248 offset:6144
cv v234,v212,v213
mm v[24:27],a[120:123],v[224:227],v[24:27]
ex v215,v215
mm v[56:59],a[120:123],v[228:231],v[56:59]
rd a[92:95],v248 offset:7168
cv v235,v214,v215
mm v[60:63],a[124:127],v[228:231],v[60:63]
ex v220,v220
mm v[28:31],a[124:127],v[224:227],v[28:31]
; ---- helper: own block finished: l is checked (.La5_LA_SLOW if outside [2^-60, 2^60] or NaN), O / l leaves; the partner-s rows start
.La5_LA_F1Y:
)ASM" R"ASM(.La5_LW6_0:
s_cbranch_execz .La5_LW6_4
wt lgkmcnt(4)
wt vmcnt(0)
mm v[192:195],a[64:67],a[0:3],0
ex v221,v221
mm v[192:195],a[68:71],a[4:7],v[192:195]
cv v238,v220,v221
s_add_u32 s89, s89, 16384
mm v[192:195],a[72:75],a[8:11],v[192:195]
ex v222,v222
mm v[192:195],a[76:79],a[12:15],v[192:195]
ld v255,s[60:63],s17 offen offset:0 sc1 lds
ar v242,a192
ar v240,a193
v_add_f32 v242, v242, v240
mm v[200:203],a[76:79],a[28:31],0
ex v223,v223
ar v243,a194
ar v240,a195
v_add_f32 v243, v243, v240
mm v[200:203],a[72:75],a[24:27],v[200:203]
cv v239,v222,v223
s_and_b32 s89, s89, 0x1ffff
aw a192,0
aw a193,0
aw a194,0
mm v[200:203],a[68:71],a[20:23],v[200:203]
ex v192,v192
aw a195,0
v_subrev_u32 v240, 562036736, v242
mm v[200:203],a[64:67],a[16:19],v[200:203]
wt lgkmcnt(0)
v_rcp_f32 v242, v242
v_cmp_le_u32 vcc, 1006632960, v240
v_subrev_u32 v240, 562036736, v243
mm v[196:199],a[80:83],a[0:3],0
ex v193,v193
s_mov_b64 s[92:93], vcc
v_cmp_le_u32 vcc, 1006632960, v240
mm v[196:199],a[84:87],a[4:7],v[196:199]
cv v224,v192,v193
v_rcp_f32 v243, v243
mm v[196:199],a[88:91],a[8:11],v[196:199]
ex v194,v194
s_or_b64 vcc, vcc, s[92:93]
s_cbranch_scc1 .La5_LA_SC1
.La5_LA_SR1:
v_mul_f32 v0, v0, v242
mm v[196:199],a[92:95],a[12:15],v[196:199]
ld v255,s[60:63],s17 offen offset:1024 sc1 lds
v_mul_f32 v1, v1, v242
v_mul_f32 v2, v2, v242
v_mul_f32 v3, v3, v242
mm v[204:207],a[92:95],a[28:31],0
ex v195,v195
v_mul_f32 v4, v4, v242
v_mul_f32 v5, v5, v242
v_mul_f32 v6, v6, v242
mm v[204:207],a[88:91],a[24:27],v[204:207]
cv v225,v194,v195
v_mul_f32 v7, v7, v242
cv v128,v0,v1
cv v129,v2,v3
cv v130,v4,v5
mm v[204:207],a[84:87],a[20:23],v[204:207]
ex v200,v200
cv v131,v6,v7
v_mul_f32 v8, v8, v242
v_mul_f32 v9, v9, v242
mm v[204:207],a[80:83],a[16:19],v[204:207]
ld v255,s[60:63],s17 offen offset:2048 sc1 lds
v_mul_f32 v10, v10, v242
v_mul_f32 v11, v11, v242
v_mul_f32 v12, v12, v242
mm v[208:211],a[64:67],a[32:35],0
ex v201,v201
v_mul_f32 v13, v13, v242
v_mul_f32 v14, v14, v242
v_mul_f32 v15, v15, v242
mm v[208:211],a[68:71],a[36:39],v[208:211]
cv v228,v200,v201
cv v132,v8,v9
cv v133,v10,v11
cv v134,v12,v13
cv v135,v14,v15
mm v[208:211],a[72:75],a[40:43],v[208:211]
ex v202,v202
v_mul_f32 v16, v16, v242
v_mul_f32 v17, v17, v242
v_mul_f32 v18, v18, v242
mm v[208:211],a[76:79],a[44:47],v[208:211]
ld v255,s[60:63],s17 offen offset:3072 sc1 lds
v_mul_f32 v19, v19, v242
v_mul_f32 v20, v20, v242
v_mul_f32 v21, v21, v242
mm v[216:219],a[76:79],a[60:63],0
ex v203,v203
v_mul_f32 v22, v22, v242
v_mul_f32 v23, v23, v242
)ASM" R"ASM(cv v136,v16,v17
mm v[216:219],a[72:75],a[56:59],v[216:219]
cv v229,v202,v203
cv v137,v18,v19
cv v138,v20,v21
cv v139,v22,v23
v_mul_f32 v24, v24, v242
mm v[216:219],a[68:71],a[52:55],v[216:219]
ex v196,v196
v_mul_f32 v25, v25, v242
v_mul_f32 v26, v26, v242
v_mul_f32 v27, v27, v242
mm v[216:219],a[64:67],a[48:51],v[216:219]
v_mul_f32 v28, v28, v242
v_mul_f32 v29, v29, v242
v_mul_f32 v30, v30, v242
v_mul_f32 v31, v31, v242
cv v140,v24,v25
mm v[212:215],a[80:83],a[32:35],0
ex v197,v197
s_add_u32 s17, s17, s80
cv v141,v26,v27
cv v142,v28,v29
mm v[212:215],a[84:87],a[36:39],v[212:215]
cv v226,v196,v197
s_add_u32 m0, m0, 16384
s_and_b32 m0, m0, s78
cv v143,v30,v31
v_mul_f32 v32, v32, v243
mm v[212:215],a[88:91],a[40:43],v[212:215]
ex v198,v198
v_mul_f32 v33, v33, v243
v_mul_f32 v34, v34, v243
v_mul_f32 v35, v35, v243
mm v[212:215],a[92:95],a[44:47],v[212:215]
st v[128:131],v244,s[44:47],s91 offen offset:0 sc1
.La5_LW6_1:
s_cbranch_execz .La5_LW6_5
v_mul_f32 v36, v36, v243
v_mul_f32 v37, v37, v243
v_mul_f32 v38, v38, v243
mm v[220:223],a[92:95],a[60:63],0
ex v199,v199
v_mul_f32 v39, v39, v243
cv v144,v32,v33
cv v145,v34,v35
mm v[220:223],a[88:91],a[56:59],v[220:223]
cv v227,v198,v199
cv v146,v36,v37
cv v147,v38,v39
v_mul_f32 v40, v40, v243
v_mul_f32 v41, v41, v243
mm v[220:223],a[84:87],a[52:55],v[220:223]
ex v204,v204
v_mul_f32 v42, v42, v243
v_mul_f32 v43, v43, v243
v_mul_f32 v44, v44, v243
mm v[220:223],a[80:83],a[48:51],v[220:223]
s_nop 0
s_barrier
v_mul_f32 v45, v45, v243
v_mul_f32 v46, v46, v243
v_mul_f32 v47, v47, v243
mm v[64:67],a[96:99],v[232:235],v[64:67]
ex v205,v205
cv v148,v40,v41
cv v149,v42,v43
cv v150,v44,v45
mm v[96:99],a[96:99],v[236:239],v[96:99]
rd a[96:99],v248 offset:8192
cv v230,v204,v205
cv v151,v46,v47
v_mul_f32 v48, v48, v243
v_mul_f32 v49, v49, v243
mm v[100:103],a[100:103],v[236:239],v[100:103]
ex v206,v206
v_mul_f32 v50, v50, v243
v_mul_f32 v51, v51, v243
v_mul_f32 v52, v52, v243
mm v[68:71],a[100:103],v[232:235],v[68:71]
rd a[100:103],v248 offset:9216
st v[132:135],v244,s[44:47],s91 offen offset:1024 sc1
v_mul_f32 v53, v53, v243
v_mul_f32 v54, v54, v243
mm v[72:75],a[104:107],v[232:235],v[72:75]
ex v207,v207
v_mul_f32 v55, v55, v243
cv v152,v48,v49
cv v153,v50,v51
mm v[104:107],a[104:107],v[236:239],v[104:107]
rd a[104:107],v248 offset:10240
cv v231,v206,v207
cv v154,v52,v53
cv v155,v54,v55
v_mul_f32 v56, v56, v243
mm v[108:111],a[108:111],v[236:239],v[108:111]
ex v208,v208
v_mul_f32 v57, v57, v243
v_mul_f32 v58, v58, v243
v_mul_f32 v59, v59, v243
)ASM" R"ASM(mm v[76:79],a[108:111],v[232:235],v[76:79]
rd a[108:111],v248 offset:11264
v_mul_f32 v60, v60, v243
v_mul_f32 v61, v61, v243
v_mul_f32 v62, v62, v243
v_mul_f32 v63, v63, v243
mm v[80:83],a[112:115],v[232:235],v[80:83]
ex v209,v209
cv v156,v56,v57
cv v157,v58,v59
cv v158,v60,v61
mm v[112:115],a[112:115],v[236:239],v[112:115]
rd a[112:115],v248 offset:12288
st v[136:139],v244,s[44:47],s91 offen offset:2048 sc1
cv v159,v62,v63
mm v[116:119],a[116:119],v[236:239],v[116:119]
ex v210,v210
mm v[84:87],a[116:119],v[232:235],v[84:87]
rd a[116:119],v248 offset:13312
mm v[88:91],a[120:123],v[232:235],v[88:91]
ex v211,v211
mm v[120:123],a[120:123],v[236:239],v[120:123]
rd a[120:123],v248 offset:14336
st v[140:143],v244,s[44:47],s91 offen offset:3072 sc1
mm v[124:127],a[124:127],v[236:239],v[124:127]
ex v216,v216
mm v[92:95],a[124:127],v[232:235],v[92:95]
rd a[124:127],v248 offset:15360
v_or_b32 v248, s89, v255
v_smfmac_f32_16x16x64_bf16 a[196:199], a[208:211], v[232:239], v241
wt lgkmcnt(4)
v_smfmac_f32_16x16x64_bf16 a[192:195], a[208:211], v[224:231], v241
rd a[64:67],v248 offset:0
mm v[0:3],a[96:99],v[224:227],0
ex v217,v217
mm v[32:35],a[96:99],v[228:231],0
rd a[68:71],v248 offset:1024
cv v232,v208,v209
ar v242,a196
ar v240,a197
v_add_f32 v242, v242, v240
mm v[36:39],a[100:103],v[228:231],0
ex v218,v218
ar v243,a198
ar v240,a199
v_add_f32 v243, v243, v240
mm v[4:7],a[100:103],v[224:227],0
rd a[72:75],v248 offset:2048
cv v233,v210,v211
st v[144:147],v245,s[44:47],s91 offen offset:0 sc1
aw a196,0
mm v[8:11],a[104:107],v[224:227],0
ex v219,v219
aw a197,0
aw a198,0
aw a199,0
mm v[40:43],a[104:107],v[228:231],0
rd a[76:79],v248 offset:3072
cv v236,v216,v217
v_subrev_u32 v240, 562036736, v242
v_rcp_f32 v242, v242
mm v[44:47],a[108:111],v[228:231],0
ex v212,v212
v_cmp_le_u32 vcc, 1006632960, v240
v_subrev_u32 v240, 562036736, v243
s_mov_b64 s[92:93], vcc
mm v[12:15],a[108:111],v[224:227],0
rd a[80:83],v248 offset:4096
wt lgkmcnt(5)
v_cmp_le_u32 vcc, 1006632960, v240
v_rcp_f32 v243, v243
mm v[16:19],a[112:115],v[224:227],0
ex v213,v213
s_or_b64 vcc, vcc, s[92:93]
s_cbranch_scc1 .La5_LA_SC2
.La5_LA_SR2:
v_mul_f32 v64, v64, v242
mm v[48:51],a[112:115],v[228:231],0
rd a[84:87],v248 offset:5120
cv v237,v218,v219
v_mul_f32 v65, v65, v242
v_mul_f32 v66, v66, v242
v_mul_f32 v67, v67, v242
mm v[52:55],a[116:119],v[228:231],0
ex v214,v214
v_mul_f32 v68, v68, v242
v_mul_f32 v69, v69, v242
v_mul_f32 v70, v70, v242
mm v[20:23],a[116:119],v[224:227],0
rd a[88:91],v248 offset:6144
cv v234,v212,v213
st v[148:151],v245,s[44:47],s91 offen offset:1024 sc1
v_mul_f32 v71, v71, v242
mm v[24:27],a[120:123],v[224:227],0
ex v215,v215
cv v160,v64,v65
cv v161,v66,v67
cv v162,v68,v69
mm v[56:59],a[120:123],v[228:231],0
rd a[92:95],v248 offset:7168
)ASM" R"ASM(cv v235,v214,v215
cv v163,v70,v71
v_mul_f32 v72, v72, v242
v_mul_f32 v73, v73, v242
mm v[60:63],a[124:127],v[228:231],0
ex v220,v220
v_mul_f32 v74, v74, v242
v_mul_f32 v75, v75, v242
v_mul_f32 v76, v76, v242
mm v[28:31],a[124:127],v[224:227],0
s_add_u32 s85, s85, 1
v_mul_f32 v77, v77, v242
v_mul_f32 v78, v78, v242
v_mul_f32 v79, v79, v242
cv v164,v72,v73
wt lgkmcnt(4)
mm v[192:195],a[64:67],a[0:3],0
ex v221,v221
cv v165,v74,v75
cv v166,v76,v77
cv v167,v78,v79
mm v[192:195],a[68:71],a[4:7],v[192:195]
cv v238,v220,v221
s_add_u32 s89, s89, 16384
v_mul_f32 v80, v80, v242
v_mul_f32 v81, v81, v242
v_mul_f32 v82, v82, v242
mm v[192:195],a[72:75],a[8:11],v[192:195]
ex v222,v222
v_mul_f32 v83, v83, v242
v_mul_f32 v84, v84, v242
v_mul_f32 v85, v85, v242
mm v[192:195],a[76:79],a[12:15],v[192:195]
ld v255,s[60:63],s17 offen offset:0 sc1 lds
v_mul_f32 v86, v86, v242
v_mul_f32 v87, v87, v242
cv v168,v80,v81
mm v[200:203],a[76:79],a[28:31],0
ex v223,v223
cv v169,v82,v83
cv v170,v84,v85
cv v171,v86,v87
mm v[200:203],a[72:75],a[24:27],v[200:203]
cv v239,v222,v223
s_and_b32 s89, s89, 0x1ffff
v_mul_f32 v88, v88, v242
v_mul_f32 v89, v89, v242
v_mul_f32 v90, v90, v242
mm v[200:203],a[68:71],a[20:23],v[200:203]
ex v192,v192
v_mul_f32 v91, v91, v242
v_mul_f32 v92, v92, v242
v_mul_f32 v93, v93, v242
mm v[200:203],a[64:67],a[16:19],v[200:203]
wt lgkmcnt(0)
st v[152:155],v245,s[44:47],s91 offen offset:2048 sc1
v_mul_f32 v94, v94, v242
v_mul_f32 v95, v95, v242
mm v[196:199],a[80:83],a[0:3],0
ex v193,v193
cv v172,v88,v89
cv v173,v90,v91
cv v174,v92,v93
mm v[196:199],a[84:87],a[4:7],v[196:199]
cv v224,v192,v193
cv v175,v94,v95
v_mul_f32 v96, v96, v243
v_mul_f32 v97, v97, v243
v_mul_f32 v98, v98, v243
mm v[196:199],a[88:91],a[8:11],v[196:199]
ex v194,v194
v_mul_f32 v99, v99, v243
v_mul_f32 v100, v100, v243
v_mul_f32 v101, v101, v243
mm v[196:199],a[92:95],a[12:15],v[196:199]
ld v255,s[60:63],s17 offen offset:1024 sc1 lds
v_mul_f32 v102, v102, v243
v_mul_f32 v103, v103, v243
cv v176,v96,v97
mm v[204:207],a[92:95],a[28:31],0
ex v195,v195
cv v177,v98,v99
cv v178,v100,v101
cv v179,v102,v103
mm v[204:207],a[88:91],a[24:27],v[204:207]
cv v225,v194,v195
st v[156:159],v245,s[44:47],s91 offen offset:3072 sc1
v_mul_f32 v104, v104, v243
v_mul_f32 v105, v105, v243
mm v[204:207],a[84:87],a[20:23],v[204:207]
ex v200,v200
v_mul_f32 v106, v106, v243
v_mul_f32 v107, v107, v243
v_mul_f32 v108, v108, v243
mm v[204:207],a[80:83],a[16:19],v[204:207]
ld v255,s[60:63],s17 offen offset:2048 sc1 lds
v_mul_f32 v109, v109, v243
v_mul_f32 v110, v110, v243
v_mul_f32 v111, v111, v243
mm v[208:211],a[64:67],a[32:35],0
)ASM" R"ASM(ex v201,v201
cv v180,v104,v105
cv v181,v106,v107
cv v182,v108,v109
mm v[208:211],a[68:71],a[36:39],v[208:211]
cv v228,v200,v201
cv v183,v110,v111
v_mul_f32 v112, v112, v243
v_mul_f32 v113, v113, v243
v_mul_f32 v114, v114, v243
mm v[208:211],a[72:75],a[40:43],v[208:211]
ex v202,v202
v_mul_f32 v115, v115, v243
v_mul_f32 v116, v116, v243
v_mul_f32 v117, v117, v243
mm v[208:211],a[76:79],a[44:47],v[208:211]
ld v255,s[60:63],s17 offen offset:3072 sc1 lds
v_mul_f32 v118, v118, v243
v_mul_f32 v119, v119, v243
cv v184,v112,v113
mm v[216:219],a[76:79],a[60:63],0
ex v203,v203
cv v185,v114,v115
cv v186,v116,v117
cv v187,v118,v119
mm v[216:219],a[72:75],a[56:59],v[216:219]
cv v229,v202,v203
v_mul_f32 v120, v120, v243
v_mul_f32 v121, v121, v243
v_mul_f32 v122, v122, v243
v_mul_f32 v123, v123, v243
mm v[216:219],a[68:71],a[52:55],v[216:219]
ex v196,v196
v_mul_f32 v124, v124, v243
v_mul_f32 v125, v125, v243
v_mul_f32 v126, v126, v243
mm v[216:219],a[64:67],a[48:51],v[216:219]
v_mul_f32 v127, v127, v243
cv v188,v120,v121
cv v189,v122,v123
cv v190,v124,v125
cv v191,v126,v127
mm v[212:215],a[80:83],a[32:35],0
ex v197,v197
s_add_u32 s17, s17, s80
mm v[212:215],a[84:87],a[36:39],v[212:215]
cv v226,v196,v197
s_add_u32 m0, m0, 16384
s_and_b32 m0, m0, s78
mm v[212:215],a[88:91],a[40:43],v[212:215]
ex v198,v198
mm v[212:215],a[92:95],a[44:47],v[212:215]
st v[160:163],v246,s[44:47],s91 offen offset:0 sc1
mm v[220:223],a[92:95],a[60:63],0
ex v199,v199
mm v[220:223],a[88:91],a[56:59],v[220:223]
cv v227,v198,v199
mm v[220:223],a[84:87],a[52:55],v[220:223]
ex v204,v204
mm v[220:223],a[80:83],a[48:51],v[220:223]
wt vmcnt(18)
s_barrier
mm v[64:67],a[96:99],v[232:235],0
ex v205,v205
mm v[96:99],a[96:99],v[236:239],0
rd a[96:99],v248 offset:8192
cv v230,v204,v205
mm v[100:103],a[100:103],v[236:239],0
ex v206,v206
mm v[68:71],a[100:103],v[232:235],0
rd a[100:103],v248 offset:9216
st v[164:167],v246,s[44:47],s91 offen offset:1024 sc1
mm v[72:75],a[104:107],v[232:235],0
ex v207,v207
mm v[104:107],a[104:107],v[236:239],0
rd a[104:107],v248 offset:10240
cv v231,v206,v207
mm v[108:111],a[108:111],v[236:239],0
ex v208,v208
mm v[76:79],a[108:111],v[232:235],0
rd a[108:111],v248 offset:11264
st v[168:171],v246,s[44:47],s91 offen offset:2048 sc1
mm v[80:83],a[112:115],v[232:235],0
ex v209,v209
mm v[112:115],a[112:115],v[236:239],0
rd a[112:115],v248 offset:12288
mm v[116:119],a[116:119],v[236:239],0
ex v210,v210
mm v[84:87],a[116:119],v[232:235],0
rd a[116:119],v248 offset:13312
st v[172:175],v246,s[44:47],s91 offen offset:3072 sc1
mm v[88:91],a[120:123],v[232:235],0
ex v211,v211
mm v[120:123],a[120:123],v[236:239],0
rd a[120:123],v248 offset:14336
mm v[124:127],a[124:127],v[236:239],0
ex v216,v216
mm v[92:95],a[124:127],v[232:235],0
rd a[124:127],v248 offset:15360
v_or_b32 v248, s89, v255
)ASM" R"ASM(st v[176:179],v247,s[44:47],s91 offen offset:0 sc1
v_smfmac_f32_16x16x64_bf16 a[196:199], a[208:211], v[232:239], v241
wt lgkmcnt(4)
v_smfmac_f32_16x16x64_bf16 a[192:195], a[208:211], v[224:231], v241
rd a[64:67],v248 offset:0
mm v[0:3],a[96:99],v[224:227],v[0:3]
ex v217,v217
mm v[32:35],a[96:99],v[228:231],v[32:35]
rd a[68:71],v248 offset:1024
cv v232,v208,v209
mm v[36:39],a[100:103],v[228:231],v[36:39]
ex v218,v218
mm v[4:7],a[100:103],v[224:227],v[4:7]
rd a[72:75],v248 offset:2048
cv v233,v210,v211
st v[180:183],v247,s[44:47],s91 offen offset:1024 sc1
mm v[8:11],a[104:107],v[224:227],v[8:11]
ex v219,v219
mm v[40:43],a[104:107],v[228:231],v[40:43]
rd a[76:79],v248 offset:3072
cv v236,v216,v217
mm v[44:47],a[108:111],v[228:231],v[44:47]
ex v212,v212
mm v[12:15],a[108:111],v[224:227],v[12:15]
rd a[80:83],v248 offset:4096
wt lgkmcnt(5)
mm v[16:19],a[112:115],v[224:227],v[16:19]
ex v213,v213
mm v[48:51],a[112:115],v[228:231],v[48:51]
rd a[84:87],v248 offset:5120
cv v237,v218,v219
st v[184:187],v247,s[44:47],s91 offen offset:2048 sc1
mm v[52:55],a[116:119],v[228:231],v[52:55]
ex v214,v214
mm v[20:23],a[116:119],v[224:227],v[20:23]
rd a[88:91],v248 offset:6144
cv v234,v212,v213
mm v[24:27],a[120:123],v[224:227],v[24:27]
ex v215,v215
mm v[56:59],a[120:123],v[228:231],v[56:59]
rd a[92:95],v248 offset:7168
cv v235,v214,v215
st v[188:191],v247,s[44:47],s91 offen offset:3072 sc1
mm v[60:63],a[124:127],v[228:231],v[60:63]
ex v220,v220
mm v[28:31],a[124:127],v[224:227],v[28:31]
s_add_u32 s85, s85, 1
s_cmp_eq_u32 s85, 22
s_cbranch_scc1 .La5_LA_F3
.p2align 6
.La5_LA_F2:
wt lgkmcnt(4)
mm v[192:195],a[64:67],a[0:3],0
ex v221,v221
mm v[192:195],a[68:71],a[4:7],v[192:195]
cv v238,v220,v221
s_add_u32 s85, s85, 1
mm v[192:195],a[72:75],a[8:11],v[192:195]
ex v222,v222
mm v[192:195],a[76:79],a[12:15],v[192:195]
ld v255,s[60:63],s17 offen offset:0 sc1 lds
mm v[200:203],a[76:79],a[28:31],0
ex v223,v223
mm v[200:203],a[72:75],a[24:27],v[200:203]
cv v239,v222,v223
s_add_u32 s89, s89, 16384
mm v[200:203],a[68:71],a[20:23],v[200:203]
ex v192,v192
mm v[200:203],a[64:67],a[16:19],v[200:203]
wt lgkmcnt(0)
s_and_b32 s89, s89, 0x1ffff
mm v[196:199],a[80:83],a[0:3],0
ex v193,v193
mm v[196:199],a[84:87],a[4:7],v[196:199]
cv v224,v192,v193
mm v[196:199],a[88:91],a[8:11],v[196:199]
ex v194,v194
mm v[196:199],a[92:95],a[12:15],v[196:199]
ld v255,s[60:63],s17 offen offset:1024 sc1 lds
mm v[204:207],a[92:95],a[28:31],0
ex v195,v195
mm v[204:207],a[88:91],a[24:27],v[204:207]
cv v225,v194,v195
mm v[204:207],a[84:87],a[20:23],v[204:207]
ex v200,v200
mm v[204:207],a[80:83],a[16:19],v[204:207]
ld v255,s[60:63],s17 offen offset:2048 sc1 lds
mm v[208:211],a[64:67],a[32:35],0
ex v201,v201
mm v[208:211],a[68:71],a[36:39],v[208:211]
cv v228,v200,v201
mm v[208:211],a[72:75],a[40:43],v[208:211]
ex v202,v202
mm v[208:211],a[76:79],a[44:47],v[208:211]
ld v255,s[60:63],s17 offen offset:3072 sc1 lds
mm v[216:219],a[76:79],a[60:63],0
ex v203,v203
mm v[216:219],a[72:75],a[56:59],v[216:219]
cv v229,v202,v203
mm v[216:219],a[68:71],a[52:55],v[216:219]
)ASM" R"ASM(ex v196,v196
mm v[216:219],a[64:67],a[48:51],v[216:219]
buffer_load_dword a253, v254, s[36:39], s90 offen
mm v[212:215],a[80:83],a[32:35],0
ex v197,v197
mm v[212:215],a[84:87],a[36:39],v[212:215]
cv v226,v196,v197
s_add_u32 s90, s90, s21
mm v[212:215],a[88:91],a[40:43],v[212:215]
ex v198,v198
mm v[212:215],a[92:95],a[44:47],v[212:215]
s_add_u32 s17, s17, s80
s_add_u32 m0, m0, 16384
mm v[220:223],a[92:95],a[60:63],0
ex v199,v199
mm v[220:223],a[88:91],a[56:59],v[220:223]
cv v227,v198,v199
s_and_b32 m0, m0, s78
mm v[220:223],a[84:87],a[52:55],v[220:223]
ex v204,v204
mm v[220:223],a[80:83],a[48:51],v[220:223]
wt vmcnt(11)
s_barrier
mm v[64:67],a[96:99],v[232:235],v[64:67]
ex v205,v205
mm v[96:99],a[96:99],v[236:239],v[96:99]
rd a[96:99],v248 offset:8192
cv v230,v204,v205
mm v[100:103],a[100:103],v[236:239],v[100:103]
ex v206,v206
mm v[68:71],a[100:103],v[232:235],v[68:71]
rd a[100:103],v248 offset:9216
mm v[72:75],a[104:107],v[232:235],v[72:75]
ex v207,v207
mm v[104:107],a[104:107],v[236:239],v[104:107]
rd a[104:107],v248 offset:10240
cv v231,v206,v207
mm v[108:111],a[108:111],v[236:239],v[108:111]
ex v208,v208
mm v[76:79],a[108:111],v[232:235],v[76:79]
rd a[108:111],v248 offset:11264
mm v[80:83],a[112:115],v[232:235],v[80:83]
ex v209,v209
mm v[112:115],a[112:115],v[236:239],v[112:115]
rd a[112:115],v248 offset:12288
mm v[116:119],a[116:119],v[236:239],v[116:119]
ex v210,v210
mm v[84:87],a[116:119],v[232:235],v[84:87]
rd a[116:119],v248 offset:13312
mm v[88:91],a[120:123],v[232:235],v[88:91]
ex v211,v211
mm v[120:123],a[120:123],v[236:239],v[120:123]
rd a[120:123],v248 offset:14336
s_bitcmp1_b64 s[98:99], s85
mm v[124:127],a[124:127],v[236:239],v[124:127]
ex v216,v216
mm v[92:95],a[124:127],v[232:235],v[92:95]
rd a[124:127],v248 offset:15360
v_or_b32 v248, s89, v255
v_smfmac_f32_16x16x64_bf16 a[196:199], a[208:211], v[232:239], v241
wt lgkmcnt(4)
v_smfmac_f32_16x16x64_bf16 a[192:195], a[208:211], v[224:231], v241
rd a[64:67],v248 offset:0
mm v[0:3],a[96:99],v[224:227],v[0:3]
ex v217,v217
mm v[32:35],a[96:99],v[228:231],v[32:35]
rd a[68:71],v248 offset:1024
cv v232,v208,v209
mm v[36:39],a[100:103],v[228:231],v[36:39]
ex v218,v218
mm v[4:7],a[100:103],v[224:227],v[4:7]
rd a[72:75],v248 offset:2048
cv v233,v210,v211
mm v[8:11],a[104:107],v[224:227],v[8:11]
ex v219,v219
mm v[40:43],a[104:107],v[228:231],v[40:43]
rd a[76:79],v248 offset:3072
cv v236,v216,v217
mm v[44:47],a[108:111],v[228:231],v[44:47]
ex v212,v212
mm v[12:15],a[108:111],v[224:227],v[12:15]
rd a[80:83],v248 offset:4096
wt lgkmcnt(5)
mm v[16:19],a[112:115],v[224:227],v[16:19]
ex v213,v213
mm v[48:51],a[112:115],v[228:231],v[48:51]
rd a[84:87],v248 offset:5120
cv v237,v218,v219
mm v[52:55],a[116:119],v[228:231],v[52:55]
ex v214,v214
mm v[20:23],a[116:119],v[224:227],v[20:23]
rd a[88:91],v248 offset:6144
cv v234,v212,v213
mm v[24:27],a[120:123],v[224:227],v[24:27]
ex v215,v215
mm v[56:59],a[120:123],v[228:231],v[56:59]
rd a[92:95],v248 offset:7168
cv v235,v214,v215
mm v[60:63],a[124:127],v[228:231],v[60:63]
ex v220,v220
)ASM" R"ASM(mm v[28:31],a[124:127],v[224:227],v[28:31]
s_cbranch_scc1 .La5_LA_F2E2
wt lgkmcnt(4)
mm v[192:195],a[64:67],a[0:3],0
ex v221,v221
mm v[192:195],a[68:71],a[4:7],v[192:195]
cv v238,v220,v221
s_add_u32 s85, s85, 1
mm v[192:195],a[72:75],a[8:11],v[192:195]
ex v222,v222
mm v[192:195],a[76:79],a[12:15],v[192:195]
ld v255,s[60:63],s17 offen offset:0 sc1 lds
mm v[200:203],a[76:79],a[28:31],0
ex v223,v223
mm v[200:203],a[72:75],a[24:27],v[200:203]
cv v239,v222,v223
s_add_u32 s89, s89, 16384
mm v[200:203],a[68:71],a[20:23],v[200:203]
ex v192,v192
mm v[200:203],a[64:67],a[16:19],v[200:203]
wt lgkmcnt(0)
s_and_b32 s89, s89, 0x1ffff
mm v[196:199],a[80:83],a[0:3],0
ex v193,v193
mm v[196:199],a[84:87],a[4:7],v[196:199]
cv v224,v192,v193
mm v[196:199],a[88:91],a[8:11],v[196:199]
ex v194,v194
mm v[196:199],a[92:95],a[12:15],v[196:199]
ld v255,s[60:63],s17 offen offset:1024 sc1 lds
mm v[204:207],a[92:95],a[28:31],0
ex v195,v195
mm v[204:207],a[88:91],a[24:27],v[204:207]
cv v225,v194,v195
mm v[204:207],a[84:87],a[20:23],v[204:207]
ex v200,v200
mm v[204:207],a[80:83],a[16:19],v[204:207]
ld v255,s[60:63],s17 offen offset:2048 sc1 lds
mm v[208:211],a[64:67],a[32:35],0
ex v201,v201
mm v[208:211],a[68:71],a[36:39],v[208:211]
cv v228,v200,v201
mm v[208:211],a[72:75],a[40:43],v[208:211]
ex v202,v202
mm v[208:211],a[76:79],a[44:47],v[208:211]
ld v255,s[60:63],s17 offen offset:3072 sc1 lds
mm v[216:219],a[76:79],a[60:63],0
ex v203,v203
mm v[216:219],a[72:75],a[56:59],v[216:219]
cv v229,v202,v203
mm v[216:219],a[68:71],a[52:55],v[216:219]
ex v196,v196
mm v[216:219],a[64:67],a[48:51],v[216:219]
buffer_load_dword a253, v254, s[36:39], s90 offen
mm v[212:215],a[80:83],a[32:35],0
ex v197,v197
mm v[212:215],a[84:87],a[36:39],v[212:215]
cv v226,v196,v197
s_add_u32 s90, s90, s21
mm v[212:215],a[88:91],a[40:43],v[212:215]
ex v198,v198
mm v[212:215],a[92:95],a[44:47],v[212:215]
s_add_u32 s17, s17, s80
s_add_u32 m0, m0, 16384
mm v[220:223],a[92:95],a[60:63],0
ex v199,v199
mm v[220:223],a[88:91],a[56:59],v[220:223]
cv v227,v198,v199
.La5_LW6_2:
s_cbranch_execz .La5_LW6_6
s_and_b32 m0, m0, s78
mm v[220:223],a[84:87],a[52:55],v[220:223]
ex v204,v204
mm v[220:223],a[80:83],a[48:51],v[220:223]
wt vmcnt(11)
s_barrier
mm v[64:67],a[96:99],v[232:235],v[64:67]
ex v205,v205
mm v[96:99],a[96:99],v[236:239],v[96:99]
rd a[96:99],v248 offset:8192
cv v230,v204,v205
mm v[100:103],a[100:103],v[236:239],v[100:103]
ex v206,v206
mm v[68:71],a[100:103],v[232:235],v[68:71]
rd a[100:103],v248 offset:9216
mm v[72:75],a[104:107],v[232:235],v[72:75]
ex v207,v207
mm v[104:107],a[104:107],v[236:239],v[104:107]
rd a[104:107],v248 offset:10240
cv v231,v206,v207
mm v[108:111],a[108:111],v[236:239],v[108:111]
ex v208,v208
mm v[76:79],a[108:111],v[232:235],v[76:79]
rd a[108:111],v248 offset:11264
mm v[80:83],a[112:115],v[232:235],v[80:83]
ex v209,v209
mm v[112:115],a[112:115],v[236:239],v[112:115]
rd a[112:115],v248 offset:12288
mm v[116:119],a[116:119],v[236:239],v[116:119]
ex v210,v210
)ASM" R"ASM(mm v[84:87],a[116:119],v[232:235],v[84:87]
rd a[116:119],v248 offset:13312
mm v[88:91],a[120:123],v[232:235],v[88:91]
ex v211,v211
mm v[120:123],a[120:123],v[236:239],v[120:123]
rd a[120:123],v248 offset:14336
s_bitcmp1_b64 s[98:99], s85
mm v[124:127],a[124:127],v[236:239],v[124:127]
ex v216,v216
mm v[92:95],a[124:127],v[232:235],v[92:95]
rd a[124:127],v248 offset:15360
v_or_b32 v248, s89, v255
v_smfmac_f32_16x16x64_bf16 a[196:199], a[208:211], v[232:239], v241
wt lgkmcnt(4)
v_smfmac_f32_16x16x64_bf16 a[192:195], a[208:211], v[224:231], v241
rd a[64:67],v248 offset:0
mm v[0:3],a[96:99],v[224:227],v[0:3]
ex v217,v217
mm v[32:35],a[96:99],v[228:231],v[32:35]
rd a[68:71],v248 offset:1024
cv v232,v208,v209
mm v[36:39],a[100:103],v[228:231],v[36:39]
ex v218,v218
mm v[4:7],a[100:103],v[224:227],v[4:7]
rd a[72:75],v248 offset:2048
cv v233,v210,v211
mm v[8:11],a[104:107],v[224:227],v[8:11]
ex v219,v219
mm v[40:43],a[104:107],v[228:231],v[40:43]
rd a[76:79],v248 offset:3072
cv v236,v216,v217
mm v[44:47],a[108:111],v[228:231],v[44:47]
ex v212,v212
mm v[12:15],a[108:111],v[224:227],v[12:15]
rd a[80:83],v248 offset:4096
wt lgkmcnt(5)
mm v[16:19],a[112:115],v[224:227],v[16:19]
ex v213,v213
mm v[48:51],a[112:115],v[228:231],v[48:51]
rd a[84:87],v248 offset:5120
cv v237,v218,v219
mm v[52:55],a[116:119],v[228:231],v[52:55]
ex v214,v214
mm v[20:23],a[116:119],v[224:227],v[20:23]
rd a[88:91],v248 offset:6144
cv v234,v212,v213
mm v[24:27],a[120:123],v[224:227],v[24:27]
ex v215,v215
mm v[56:59],a[120:123],v[228:231],v[56:59]
rd a[92:95],v248 offset:7168
cv v235,v214,v215
mm v[60:63],a[124:127],v[228:231],v[60:63]
ex v220,v220
mm v[28:31],a[124:127],v[224:227],v[28:31]
s_cbranch_scc0 .La5_LA_F2
.La5_LA_F2E2:
; ---- helper: last windows; FFN weights and the first weights of the O projection come in
.La5_LA_F3:
.La5_LW6_3:
s_cbranch_execz .La5_LW6_7
wt lgkmcnt(4)
mm v[192:195],a[64:67],a[0:3],0
ex v221,v221
mm v[192:195],a[68:71],a[4:7],v[192:195]
cv v238,v220,v221
s_add_u32 s89, s89, 16384
mm v[192:195],a[72:75],a[8:11],v[192:195]
ex v222,v222
mm v[192:195],a[76:79],a[12:15],v[192:195]
ld v255,s[60:63],s17 offen offset:0 sc1 lds
mm v[200:203],a[76:79],a[28:31],0
ex v223,v223
mm v[200:203],a[72:75],a[24:27],v[200:203]
cv v239,v222,v223
s_and_b32 s89, s89, 0x1ffff
mm v[200:203],a[68:71],a[20:23],v[200:203]
ex v192,v192
mm v[200:203],a[64:67],a[16:19],v[200:203]
wt lgkmcnt(0)
mm v[196:199],a[80:83],a[0:3],0
ex v193,v193
mm v[196:199],a[84:87],a[4:7],v[196:199]
cv v224,v192,v193
ar v243,a229
mm v[196:199],a[88:91],a[8:11],v[196:199]
ex v194,v194
mm v[196:199],a[92:95],a[12:15],v[196:199]
ld v255,s[60:63],s17 offen offset:1024 sc1 lds
mm v[204:207],a[92:95],a[28:31],0
ex v195,v195
mm v[204:207],a[88:91],a[24:27],v[204:207]
cv v225,v194,v195
mm v[204:207],a[84:87],a[20:23],v[204:207]
ex v200,v200
mm v[204:207],a[80:83],a[16:19],v[204:207]
ld v255,s[60:63],s17 offen offset:2048 sc1 lds
mm v[208:211],a[64:67],a[32:35],0
ex v201,v201
mm v[208:211],a[68:71],a[36:39],v[208:211]
cv v228,v200,v201
)ASM" R"ASM(mm v[208:211],a[72:75],a[40:43],v[208:211]
ex v202,v202
mm v[208:211],a[76:79],a[44:47],v[208:211]
ld v255,s[60:63],s17 offen offset:3072 sc1 lds
mm v[216:219],a[76:79],a[60:63],0
ex v203,v203
mm v[216:219],a[72:75],a[56:59],v[216:219]
cv v229,v202,v203
s_mov_b32 s93, 0
mm v[216:219],a[68:71],a[52:55],v[216:219]
ex v196,v196
mm v[216:219],a[64:67],a[48:51],v[216:219]
ld a[128:131],v243,s[40:43],s93 offen
mm v[212:215],a[80:83],a[32:35],0
ex v197,v197
s_add_u32 s17, s17, s80
mm v[212:215],a[84:87],a[36:39],v[212:215]
cv v226,v196,v197
s_mov_b32 s93, 7168
s_add_u32 m0, m0, 16384
mm v[212:215],a[88:91],a[40:43],v[212:215]
ex v198,v198
s_and_b32 m0, m0, s78
mm v[212:215],a[92:95],a[44:47],v[212:215]
ld a[132:135],v243,s[40:43],s93 offen
mm v[220:223],a[92:95],a[60:63],0
ex v199,v199
mm v[220:223],a[88:91],a[56:59],v[220:223]
cv v227,v198,v199
mm v[220:223],a[84:87],a[52:55],v[220:223]
ex v204,v204
mm v[220:223],a[80:83],a[48:51],v[220:223]
wt vmcnt(12)
s_barrier
mm v[64:67],a[96:99],v[232:235],v[64:67]
ex v205,v205
mm v[96:99],a[96:99],v[236:239],v[96:99]
rd a[96:99],v248 offset:8192
cv v230,v204,v205
mm v[100:103],a[100:103],v[236:239],v[100:103]
ex v206,v206
mm v[68:71],a[100:103],v[232:235],v[68:71]
rd a[100:103],v248 offset:9216
mm v[72:75],a[104:107],v[232:235],v[72:75]
ex v207,v207
mm v[104:107],a[104:107],v[236:239],v[104:107]
rd a[104:107],v248 offset:10240
cv v231,v206,v207
s_mov_b32 s93, 14336
mm v[108:111],a[108:111],v[236:239],v[108:111]
ex v208,v208
mm v[76:79],a[108:111],v[232:235],v[76:79]
rd a[108:111],v248 offset:11264
ld a[136:139],v243,s[40:43],s93 offen
mm v[80:83],a[112:115],v[232:235],v[80:83]
ex v209,v209
mm v[112:115],a[112:115],v[236:239],v[112:115]
rd a[112:115],v248 offset:12288
mm v[116:119],a[116:119],v[236:239],v[116:119]
ex v210,v210
mm v[84:87],a[116:119],v[232:235],v[84:87]
rd a[116:119],v248 offset:13312
mm v[88:91],a[120:123],v[232:235],v[88:91]
ex v211,v211
mm v[120:123],a[120:123],v[236:239],v[120:123]
rd a[120:123],v248 offset:14336
mm v[124:127],a[124:127],v[236:239],v[124:127]
ex v216,v216
mm v[92:95],a[124:127],v[232:235],v[92:95]
rd a[124:127],v248 offset:15360
v_or_b32 v248, s89, v255
v_smfmac_f32_16x16x64_bf16 a[196:199], a[208:211], v[232:239], v241
wt lgkmcnt(4)
v_smfmac_f32_16x16x64_bf16 a[192:195], a[208:211], v[224:231], v241
rd a[64:67],v248 offset:0
mm v[0:3],a[96:99],v[224:227],v[0:3]
ex v217,v217
mm v[32:35],a[96:99],v[228:231],v[32:35]
rd a[68:71],v248 offset:1024
cv v232,v208,v209
mm v[36:39],a[100:103],v[228:231],v[36:39]
ex v218,v218
mm v[4:7],a[100:103],v[224:227],v[4:7]
rd a[72:75],v248 offset:2048
cv v233,v210,v211
mm v[8:11],a[104:107],v[224:227],v[8:11]
ex v219,v219
mm v[40:43],a[104:107],v[228:231],v[40:43]
rd a[76:79],v248 offset:3072
cv v236,v216,v217
mm v[44:47],a[108:111],v[228:231],v[44:47]
ex v212,v212
mm v[12:15],a[108:111],v[224:227],v[12:15]
rd a[80:83],v248 offset:4096
wt lgkmcnt(5)
mm v[16:19],a[112:115],v[224:227],v[16:19]
ex v213,v213
mm v[48:51],a[112:115],v[228:231],v[48:51]
rd a[84:87],v248 offset:5120
cv v237,v218,v219
)ASM" R"ASM(mm v[52:55],a[116:119],v[228:231],v[52:55]
ex v214,v214
mm v[20:23],a[116:119],v[224:227],v[20:23]
rd a[88:91],v248 offset:6144
cv v234,v212,v213
mm v[24:27],a[120:123],v[224:227],v[24:27]
ex v215,v215
mm v[56:59],a[120:123],v[228:231],v[56:59]
rd a[92:95],v248 offset:7168
cv v235,v214,v215
mm v[60:63],a[124:127],v[228:231],v[60:63]
ex v220,v220
mm v[28:31],a[124:127],v[224:227],v[28:31]
wt lgkmcnt(4)
mm v[192:195],a[64:67],a[0:3],0
ex v221,v221
mm v[192:195],a[68:71],a[4:7],v[192:195]
cv v238,v220,v221
s_add_u32 s89, s89, 16384
mm v[192:195],a[72:75],a[8:11],v[192:195]
ex v222,v222
mm v[192:195],a[76:79],a[12:15],v[192:195]
ld v255,s[60:63],s17 offen offset:0 sc1 lds
mm v[200:203],a[76:79],a[28:31],0
ex v223,v223
mm v[200:203],a[72:75],a[24:27],v[200:203]
cv v239,v222,v223
s_and_b32 s89, s89, 0x1ffff
mm v[200:203],a[68:71],a[20:23],v[200:203]
ex v192,v192
mm v[200:203],a[64:67],a[16:19],v[200:203]
wt lgkmcnt(0)
mm v[196:199],a[80:83],a[0:3],0
ex v193,v193
mm v[196:199],a[84:87],a[4:7],v[196:199]
cv v224,v192,v193
mm v[196:199],a[88:91],a[8:11],v[196:199]
ex v194,v194
mm v[196:199],a[92:95],a[12:15],v[196:199]
ld v255,s[60:63],s17 offen offset:1024 sc1 lds
mm v[204:207],a[92:95],a[28:31],0
ex v195,v195
mm v[204:207],a[88:91],a[24:27],v[204:207]
cv v225,v194,v195
mm v[204:207],a[84:87],a[20:23],v[204:207]
ex v200,v200
mm v[204:207],a[80:83],a[16:19],v[204:207]
ld v255,s[60:63],s17 offen offset:2048 sc1 lds
mm v[208:211],a[64:67],a[32:35],0
ex v201,v201
mm v[208:211],a[68:71],a[36:39],v[208:211]
cv v228,v200,v201
mm v[208:211],a[72:75],a[40:43],v[208:211]
ex v202,v202
mm v[208:211],a[76:79],a[44:47],v[208:211]
ld v255,s[60:63],s17 offen offset:3072 sc1 lds
mm v[216:219],a[76:79],a[60:63],0
ex v203,v203
mm v[216:219],a[72:75],a[56:59],v[216:219]
cv v229,v202,v203
s_mov_b32 s93, 21504
mm v[216:219],a[68:71],a[52:55],v[216:219]
ex v196,v196
mm v[216:219],a[64:67],a[48:51],v[216:219]
ld a[140:143],v243,s[40:43],s93 offen
mm v[212:215],a[80:83],a[32:35],0
ex v197,v197
s_add_u32 s17, s17, s80
mm v[212:215],a[84:87],a[36:39],v[212:215]
cv v226,v196,v197
s_mov_b32 s93, 28672
s_add_u32 m0, m0, 16384
mm v[212:215],a[88:91],a[40:43],v[212:215]
ex v198,v198
s_and_b32 m0, m0, s78
mm v[212:215],a[92:95],a[44:47],v[212:215]
ld a[144:147],v243,s[40:43],s93 offen
mm v[220:223],a[92:95],a[60:63],0
ex v199,v199
mm v[220:223],a[88:91],a[56:59],v[220:223]
cv v227,v198,v199
mm v[220:223],a[84:87],a[52:55],v[220:223]
ex v204,v204
mm v[220:223],a[80:83],a[48:51],v[220:223]
wt vmcnt(14)
s_barrier
mm v[64:67],a[96:99],v[232:235],v[64:67]
ex v205,v205
mm v[96:99],a[96:99],v[236:239],v[96:99]
rd a[96:99],v248 offset:8192
cv v230,v204,v205
mm v[100:103],a[100:103],v[236:239],v[100:103]
ex v206,v206
mm v[68:71],a[100:103],v[232:235],v[68:71]
rd a[100:103],v248 offset:9216
mm v[72:75],a[104:107],v[232:235],v[72:75]
ex v207,v207
mm v[104:107],a[104:107],v[236:239],v[104:107]
rd a[104:107],v248 offset:10240
cv v231,v206,v207
)ASM" R"ASM(s_mov_b32 s93, 35840
mm v[108:111],a[108:111],v[236:239],v[108:111]
ex v208,v208
mm v[76:79],a[108:111],v[232:235],v[76:79]
rd a[108:111],v248 offset:11264
ld a[148:151],v243,s[40:43],s93 offen
mm v[80:83],a[112:115],v[232:235],v[80:83]
ex v209,v209
mm v[112:115],a[112:115],v[236:239],v[112:115]
rd a[112:115],v248 offset:12288
mm v[116:119],a[116:119],v[236:239],v[116:119]
ex v210,v210
mm v[84:87],a[116:119],v[232:235],v[84:87]
rd a[116:119],v248 offset:13312
mm v[88:91],a[120:123],v[232:235],v[88:91]
ex v211,v211
mm v[120:123],a[120:123],v[236:239],v[120:123]
rd a[120:123],v248 offset:14336
ar v242,a231
wt vmcnt(14)
mm v[124:127],a[124:127],v[236:239],v[124:127]
ex v216,v216
mm v[92:95],a[124:127],v[232:235],v[92:95]
rd a[124:127],v248 offset:15360
v_or_b32 v248, s89, v255
v_smfmac_f32_16x16x64_bf16 a[196:199], a[208:211], v[232:239], v241
wt lgkmcnt(4)
v_smfmac_f32_16x16x64_bf16 a[192:195], a[208:211], v[224:231], v241
rd a[64:67],v248 offset:0
mm v[0:3],a[96:99],v[224:227],v[0:3]
ex v217,v217
mm v[32:35],a[96:99],v[228:231],v[32:35]
rd a[68:71],v248 offset:1024
cv v232,v208,v209
mm v[36:39],a[100:103],v[228:231],v[36:39]
ex v218,v218
mm v[4:7],a[100:103],v[224:227],v[4:7]
rd a[72:75],v248 offset:2048
cv v233,v210,v211
mm v[8:11],a[104:107],v[224:227],v[8:11]
ex v219,v219
mm v[40:43],a[104:107],v[228:231],v[40:43]
rd a[76:79],v248 offset:3072
cv v236,v216,v217
mm v[44:47],a[108:111],v[228:231],v[44:47]
ex v212,v212
mm v[12:15],a[108:111],v[224:227],v[12:15]
rd a[80:83],v248 offset:4096
wt lgkmcnt(5)
mm v[16:19],a[112:115],v[224:227],v[16:19]
ex v213,v213
mm v[48:51],a[112:115],v[228:231],v[48:51]
rd a[84:87],v248 offset:5120
cv v237,v218,v219
mm v[52:55],a[116:119],v[228:231],v[52:55]
ex v214,v214
mm v[20:23],a[116:119],v[224:227],v[20:23]
rd a[88:91],v248 offset:6144
cv v234,v212,v213
mm v[24:27],a[120:123],v[224:227],v[24:27]
ex v215,v215
mm v[56:59],a[120:123],v[228:231],v[56:59]
rd a[92:95],v248 offset:7168
cv v235,v214,v215
mm v[60:63],a[124:127],v[228:231],v[60:63]
ex v220,v220
mm v[28:31],a[124:127],v[224:227],v[28:31]
buffer_store_dwordx2 v[252:253], v242, s[64:67], s6 offen offset:0 sc1
wt lgkmcnt(4)
mm v[192:195],a[64:67],a[0:3],0
ex v221,v221
mm v[192:195],a[68:71],a[4:7],v[192:195]
cv v238,v220,v221
s_add_u32 s89, s89, 16384
mm v[192:195],a[72:75],a[8:11],v[192:195]
ex v222,v222
mm v[192:195],a[76:79],a[12:15],v[192:195]
ld v255,s[60:63],s17 offen offset:0 sc1 lds
mm v[200:203],a[76:79],a[28:31],0
ex v223,v223
mm v[200:203],a[72:75],a[24:27],v[200:203]
cv v239,v222,v223
s_and_b32 s89, s89, 0x1ffff
mm v[200:203],a[68:71],a[20:23],v[200:203]
ex v192,v192
mm v[200:203],a[64:67],a[16:19],v[200:203]
wt lgkmcnt(0)
mm v[196:199],a[80:83],a[0:3],0
ex v193,v193
mm v[196:199],a[84:87],a[4:7],v[196:199]
cv v224,v192,v193
mm v[196:199],a[88:91],a[8:11],v[196:199]
ex v194,v194
mm v[196:199],a[92:95],a[12:15],v[196:199]
ld v255,s[60:63],s17 offen offset:1024 sc1 lds
mm v[204:207],a[92:95],a[28:31],0
ex v195,v195
mm v[204:207],a[88:91],a[24:27],v[204:207]
cv v225,v194,v195
mm v[204:207],a[84:87],a[20:23],v[204:207]
)ASM" R"ASM(ex v200,v200
mm v[204:207],a[80:83],a[16:19],v[204:207]
ld v255,s[60:63],s17 offen offset:2048 sc1 lds
mm v[208:211],a[64:67],a[32:35],0
ex v201,v201
mm v[208:211],a[68:71],a[36:39],v[208:211]
cv v228,v200,v201
mm v[208:211],a[72:75],a[40:43],v[208:211]
ex v202,v202
mm v[208:211],a[76:79],a[44:47],v[208:211]
ld v255,s[60:63],s17 offen offset:3072 sc1 lds
mm v[216:219],a[76:79],a[60:63],0
ex v203,v203
mm v[216:219],a[72:75],a[56:59],v[216:219]
cv v229,v202,v203
s_mov_b32 s93, 43008
mm v[216:219],a[68:71],a[52:55],v[216:219]
ex v196,v196
mm v[216:219],a[64:67],a[48:51],v[216:219]
ld a[152:155],v243,s[40:43],s93 offen
mm v[212:215],a[80:83],a[32:35],0
ex v197,v197
s_add_u32 s17, s17, s80
mm v[212:215],a[84:87],a[36:39],v[212:215]
cv v226,v196,v197
s_mov_b32 s93, 50176
s_add_u32 m0, m0, 16384
mm v[212:215],a[88:91],a[40:43],v[212:215]
ex v198,v198
s_and_b32 m0, m0, s78
mm v[212:215],a[92:95],a[44:47],v[212:215]
ld a[156:159],v243,s[40:43],s93 offen
mm v[220:223],a[92:95],a[60:63],0
ex v199,v199
mm v[220:223],a[88:91],a[56:59],v[220:223]
cv v227,v198,v199
mm v[220:223],a[84:87],a[52:55],v[220:223]
ex v204,v204
mm v[220:223],a[80:83],a[48:51],v[220:223]
wt vmcnt(17)
s_barrier
mm v[64:67],a[96:99],v[232:235],v[64:67]
ex v205,v205
mm v[96:99],a[96:99],v[236:239],v[96:99]
rd a[96:99],v248 offset:8192
cv v230,v204,v205
mm v[100:103],a[100:103],v[236:239],v[100:103]
ex v206,v206
mm v[68:71],a[100:103],v[232:235],v[68:71]
rd a[100:103],v248 offset:9216
mm v[72:75],a[104:107],v[232:235],v[72:75]
ex v207,v207
mm v[104:107],a[104:107],v[236:239],v[104:107]
rd a[104:107],v248 offset:10240
cv v231,v206,v207
s_mov_b32 s93, 57344
mm v[108:111],a[108:111],v[236:239],v[108:111]
ex v208,v208
mm v[76:79],a[108:111],v[232:235],v[76:79]
rd a[108:111],v248 offset:11264
ld a[160:163],v243,s[40:43],s93 offen
mm v[80:83],a[112:115],v[232:235],v[80:83]
ex v209,v209
mm v[112:115],a[112:115],v[236:239],v[112:115]
rd a[112:115],v248 offset:12288
mm v[116:119],a[116:119],v[236:239],v[116:119]
ex v210,v210
mm v[84:87],a[116:119],v[232:235],v[84:87]
rd a[116:119],v248 offset:13312
mm v[88:91],a[120:123],v[232:235],v[88:91]
ex v211,v211
mm v[120:123],a[120:123],v[236:239],v[120:123]
rd a[120:123],v248 offset:14336
mm v[124:127],a[124:127],v[236:239],v[124:127]
ex v216,v216
mm v[92:95],a[124:127],v[232:235],v[92:95]
rd a[124:127],v248 offset:15360
v_or_b32 v248, s89, v255
v_smfmac_f32_16x16x64_bf16 a[196:199], a[208:211], v[232:239], v241
wt lgkmcnt(4)
v_smfmac_f32_16x16x64_bf16 a[192:195], a[208:211], v[224:231], v241
rd a[64:67],v248 offset:0
mm v[0:3],a[96:99],v[224:227],v[0:3]
ex v217,v217
mm v[32:35],a[96:99],v[228:231],v[32:35]
rd a[68:71],v248 offset:1024
cv v232,v208,v209
mm v[36:39],a[100:103],v[228:231],v[36:39]
ex v218,v218
mm v[4:7],a[100:103],v[224:227],v[4:7]
rd a[72:75],v248 offset:2048
cv v233,v210,v211
mm v[8:11],a[104:107],v[224:227],v[8:11]
ex v219,v219
mm v[40:43],a[104:107],v[228:231],v[40:43]
rd a[76:79],v248 offset:3072
cv v236,v216,v217
mm v[44:47],a[108:111],v[228:231],v[44:47]
ex v212,v212
mm v[12:15],a[108:111],v[224:227],v[12:15]
)ASM" R"ASM(rd a[80:83],v248 offset:4096
wt lgkmcnt(5)
mm v[16:19],a[112:115],v[224:227],v[16:19]
ex v213,v213
mm v[48:51],a[112:115],v[228:231],v[48:51]
rd a[84:87],v248 offset:5120
cv v237,v218,v219
mm v[52:55],a[116:119],v[228:231],v[52:55]
ex v214,v214
mm v[20:23],a[116:119],v[224:227],v[20:23]
rd a[88:91],v248 offset:6144
cv v234,v212,v213
mm v[24:27],a[120:123],v[224:227],v[24:27]
ex v215,v215
mm v[56:59],a[120:123],v[228:231],v[56:59]
rd a[92:95],v248 offset:7168
cv v235,v214,v215
mm v[60:63],a[124:127],v[228:231],v[60:63]
ex v220,v220
mm v[28:31],a[124:127],v[224:227],v[28:31]
wt lgkmcnt(4)
mm v[192:195],a[64:67],a[0:3],0
ex v221,v221
mm v[192:195],a[68:71],a[4:7],v[192:195]
cv v238,v220,v221
ar v240,a228
mm v[192:195],a[72:75],a[8:11],v[192:195]
ex v222,v222
mm v[192:195],a[76:79],a[12:15],v[192:195]
s_add_u32 m0, s83, 16896
s_lshl_b32 s17, s72, 18
mm v[200:203],a[76:79],a[28:31],0
ex v223,v223
mm v[200:203],a[72:75],a[24:27],v[200:203]
cv v239,v222,v223
s_add_u32 s89, s89, 16384
mm v[200:203],a[68:71],a[20:23],v[200:203]
ex v192,v192
mm v[200:203],a[64:67],a[16:19],v[200:203]
wt lgkmcnt(0)
s_and_b32 s89, s89, 0x1ffff
mm v[196:199],a[80:83],a[0:3],0
ex v193,v193
mm v[196:199],a[84:87],a[4:7],v[196:199]
cv v224,v192,v193
mm v[196:199],a[88:91],a[8:11],v[196:199]
ex v194,v194
mm v[196:199],a[92:95],a[12:15],v[196:199]
ld v240,s[56:59],s17 offen lds
mm v[204:207],a[92:95],a[28:31],0
ex v195,v195
s_add_u32 s17, s17, 32768
mm v[204:207],a[88:91],a[24:27],v[204:207]
cv v225,v194,v195
s_add_u32 m0, m0, 1024
mm v[204:207],a[84:87],a[20:23],v[204:207]
ex v200,v200
mm v[204:207],a[80:83],a[16:19],v[204:207]
ld v240,s[56:59],s17 offen lds
mm v[208:211],a[64:67],a[32:35],0
ex v201,v201
s_add_u32 s17, s17, 4294942720
mm v[208:211],a[68:71],a[36:39],v[208:211]
cv v228,v200,v201
s_add_u32 m0, m0, 1152
mm v[208:211],a[72:75],a[40:43],v[208:211]
ex v202,v202
.La5_LW6_4:
s_cbranch_execz .La5_LW6_R0
mm v[208:211],a[76:79],a[44:47],v[208:211]
ld v240,s[56:59],s17 offen lds
mm v[216:219],a[76:79],a[60:63],0
ex v203,v203
s_add_u32 s17, s17, 32768
mm v[216:219],a[72:75],a[56:59],v[216:219]
cv v229,v202,v203
s_add_u32 m0, m0, 1024
mm v[216:219],a[68:71],a[52:55],v[216:219]
ex v196,v196
mm v[216:219],a[64:67],a[48:51],v[216:219]
ld v240,s[56:59],s17 offen lds
mm v[212:215],a[80:83],a[32:35],0
ex v197,v197
s_add_u32 s17, s17, 90112
mm v[212:215],a[84:87],a[36:39],v[212:215]
cv v226,v196,v197
s_mov_b32 s93, 64512
s_add_u32 m0, s83, 33792
mm v[212:215],a[88:91],a[40:43],v[212:215]
ex v198,v198
mm v[212:215],a[92:95],a[44:47],v[212:215]
ld a[164:167],v243,s[40:43],s93 offen
mm v[220:223],a[92:95],a[60:63],0
ex v199,v199
mm v[220:223],a[88:91],a[56:59],v[220:223]
cv v227,v198,v199
mm v[220:223],a[84:87],a[52:55],v[220:223]
ex v204,v204
mm v[220:223],a[80:83],a[48:51],v[220:223]
wt vmcnt(16)
)ASM" R"ASM(s_barrier
mm v[64:67],a[96:99],v[232:235],v[64:67]
ex v205,v205
mm v[96:99],a[96:99],v[236:239],v[96:99]
rd a[96:99],v248 offset:8192
cv v230,v204,v205
mm v[100:103],a[100:103],v[236:239],v[100:103]
ex v206,v206
mm v[68:71],a[100:103],v[232:235],v[68:71]
rd a[100:103],v248 offset:9216
mm v[72:75],a[104:107],v[232:235],v[72:75]
ex v207,v207
mm v[104:107],a[104:107],v[236:239],v[104:107]
rd a[104:107],v248 offset:10240
cv v231,v206,v207
s_mov_b32 s93, 71680
mm v[108:111],a[108:111],v[236:239],v[108:111]
ex v208,v208
mm v[76:79],a[108:111],v[232:235],v[76:79]
rd a[108:111],v248 offset:11264
ld a[168:171],v243,s[40:43],s93 offen
mm v[80:83],a[112:115],v[232:235],v[80:83]
ex v209,v209
mm v[112:115],a[112:115],v[236:239],v[112:115]
rd a[112:115],v248 offset:12288
s_mov_b32 s93, 78848
mm v[116:119],a[116:119],v[236:239],v[116:119]
ex v210,v210
mm v[84:87],a[116:119],v[232:235],v[84:87]
rd a[116:119],v248 offset:13312
ld a[172:175],v243,s[40:43],s93 offen
mm v[88:91],a[120:123],v[232:235],v[88:91]
ex v211,v211
mm v[120:123],a[120:123],v[236:239],v[120:123]
rd a[120:123],v248 offset:14336
mm v[124:127],a[124:127],v[236:239],v[124:127]
ex v216,v216
mm v[92:95],a[124:127],v[232:235],v[92:95]
rd a[124:127],v248 offset:15360
v_or_b32 v248, s89, v255
v_smfmac_f32_16x16x64_bf16 a[196:199], a[208:211], v[232:239], v241
wt lgkmcnt(4)
v_smfmac_f32_16x16x64_bf16 a[192:195], a[208:211], v[224:231], v241
rd a[64:67],v248 offset:0
mm v[0:3],a[96:99],v[224:227],v[0:3]
ex v217,v217
mm v[32:35],a[96:99],v[228:231],v[32:35]
rd a[68:71],v248 offset:1024
cv v232,v208,v209
mm v[36:39],a[100:103],v[228:231],v[36:39]
ex v218,v218
mm v[4:7],a[100:103],v[224:227],v[4:7]
rd a[72:75],v248 offset:2048
cv v233,v210,v211
mm v[8:11],a[104:107],v[224:227],v[8:11]
ex v219,v219
mm v[40:43],a[104:107],v[228:231],v[40:43]
rd a[76:79],v248 offset:3072
cv v236,v216,v217
mm v[44:47],a[108:111],v[228:231],v[44:47]
ex v212,v212
mm v[12:15],a[108:111],v[224:227],v[12:15]
rd a[80:83],v248 offset:4096
wt lgkmcnt(5)
mm v[16:19],a[112:115],v[224:227],v[16:19]
ex v213,v213
mm v[48:51],a[112:115],v[228:231],v[48:51]
rd a[84:87],v248 offset:5120
cv v237,v218,v219
mm v[52:55],a[116:119],v[228:231],v[52:55]
ex v214,v214
mm v[20:23],a[116:119],v[224:227],v[20:23]
rd a[88:91],v248 offset:6144
cv v234,v212,v213
mm v[24:27],a[120:123],v[224:227],v[24:27]
ex v215,v215
mm v[56:59],a[120:123],v[228:231],v[56:59]
rd a[92:95],v248 offset:7168
cv v235,v214,v215
mm v[60:63],a[124:127],v[228:231],v[60:63]
ex v220,v220
mm v[28:31],a[124:127],v[224:227],v[28:31]
wt lgkmcnt(4)
mm v[192:195],a[64:67],a[0:3],0
ex v221,v221
mm v[192:195],a[68:71],a[4:7],v[192:195]
cv v238,v220,v221
s_add_u32 s89, s89, 16384
mm v[192:195],a[72:75],a[8:11],v[192:195]
ex v222,v222
mm v[192:195],a[76:79],a[12:15],v[192:195]
ld v240,s[56:59],s17 offen lds
mm v[200:203],a[76:79],a[28:31],0
ex v223,v223
s_add_u32 s17, s17, 32768
mm v[200:203],a[72:75],a[24:27],v[200:203]
cv v239,v222,v223
s_add_u32 m0, m0, 1024
mm v[200:203],a[68:71],a[20:23],v[200:203]
ex v192,v192
)ASM" R"ASM(mm v[200:203],a[64:67],a[16:19],v[200:203]
wt lgkmcnt(0)
s_and_b32 s89, s89, 0x1ffff
mm v[196:199],a[80:83],a[0:3],0
ex v193,v193
mm v[196:199],a[84:87],a[4:7],v[196:199]
cv v224,v192,v193
mm v[196:199],a[88:91],a[8:11],v[196:199]
ex v194,v194
mm v[196:199],a[92:95],a[12:15],v[196:199]
ld v240,s[56:59],s17 offen lds
mm v[204:207],a[92:95],a[28:31],0
ex v195,v195
s_add_u32 s17, s17, 4294942720
mm v[204:207],a[88:91],a[24:27],v[204:207]
cv v225,v194,v195
s_add_u32 m0, m0, 1152
mm v[204:207],a[84:87],a[20:23],v[204:207]
ex v200,v200
mm v[204:207],a[80:83],a[16:19],v[204:207]
ld v240,s[56:59],s17 offen lds
mm v[208:211],a[64:67],a[32:35],0
ex v201,v201
s_add_u32 s17, s17, 32768
mm v[208:211],a[68:71],a[36:39],v[208:211]
cv v228,v200,v201
s_add_u32 m0, m0, 1024
mm v[208:211],a[72:75],a[40:43],v[208:211]
ex v202,v202
mm v[208:211],a[76:79],a[44:47],v[208:211]
ld v240,s[56:59],s17 offen lds
mm v[216:219],a[76:79],a[60:63],0
ex v203,v203
mm v[216:219],a[72:75],a[56:59],v[216:219]
cv v229,v202,v203
s_mov_b32 s93, 86016
mm v[216:219],a[68:71],a[52:55],v[216:219]
ex v196,v196
mm v[216:219],a[64:67],a[48:51],v[216:219]
ld a[176:179],v243,s[40:43],s93 offen
mm v[212:215],a[80:83],a[32:35],0
ex v197,v197
s_bfe_u32 s17, s4, 262148
mm v[212:215],a[84:87],a[36:39],v[212:215]
cv v226,v196,v197
s_mov_b32 s93, 93184
s_xor_b32 s17, s17, s72
mm v[212:215],a[88:91],a[40:43],v[212:215]
ex v198,v198
s_lshl_b32 s17, s17, 18
mm v[212:215],a[92:95],a[44:47],v[212:215]
ld a[180:183],v243,s[40:43],s93 offen
s_add_u32 m0, s83, 50688
mm v[220:223],a[92:95],a[60:63],0
ex v199,v199
mm v[220:223],a[88:91],a[56:59],v[220:223]
cv v227,v198,v199
mm v[220:223],a[84:87],a[52:55],v[220:223]
ex v204,v204
mm v[220:223],a[80:83],a[48:51],v[220:223]
wt vmcnt(16)
s_barrier
mm v[64:67],a[96:99],v[232:235],v[64:67]
ex v205,v205
mm v[96:99],a[96:99],v[236:239],v[96:99]
rd a[96:99],v248 offset:8192
cv v230,v204,v205
mm v[100:103],a[100:103],v[236:239],v[100:103]
ex v206,v206
mm v[68:71],a[100:103],v[232:235],v[68:71]
rd a[100:103],v248 offset:9216
mm v[72:75],a[104:107],v[232:235],v[72:75]
ex v207,v207
mm v[104:107],a[104:107],v[236:239],v[104:107]
rd a[104:107],v248 offset:10240
cv v231,v206,v207
s_mov_b32 s93, 100352
mm v[108:111],a[108:111],v[236:239],v[108:111]
ex v208,v208
mm v[76:79],a[108:111],v[232:235],v[76:79]
rd a[108:111],v248 offset:11264
ld a[184:187],v243,s[40:43],s93 offen
mm v[80:83],a[112:115],v[232:235],v[80:83]
ex v209,v209
mm v[112:115],a[112:115],v[236:239],v[112:115]
rd a[112:115],v248 offset:12288
mm v[116:119],a[116:119],v[236:239],v[116:119]
ex v210,v210
mm v[84:87],a[116:119],v[232:235],v[84:87]
rd a[116:119],v248 offset:13312
mm v[88:91],a[120:123],v[232:235],v[88:91]
ex v211,v211
mm v[120:123],a[120:123],v[236:239],v[120:123]
rd a[120:123],v248 offset:14336
mm v[124:127],a[124:127],v[236:239],v[124:127]
ex v216,v216
mm v[92:95],a[124:127],v[232:235],v[92:95]
rd a[124:127],v248 offset:15360
v_or_b32 v248, s89, v255
v_smfmac_f32_16x16x64_bf16 a[196:199], a[208:211], v[232:239], v241
)ASM" R"ASM(wt lgkmcnt(4)
v_smfmac_f32_16x16x64_bf16 a[192:195], a[208:211], v[224:231], v241
rd a[64:67],v248 offset:0
mm v[0:3],a[96:99],v[224:227],v[0:3]
ex v217,v217
mm v[32:35],a[96:99],v[228:231],v[32:35]
rd a[68:71],v248 offset:1024
cv v232,v208,v209
mm v[36:39],a[100:103],v[228:231],v[36:39]
ex v218,v218
mm v[4:7],a[100:103],v[224:227],v[4:7]
rd a[72:75],v248 offset:2048
cv v233,v210,v211
mm v[8:11],a[104:107],v[224:227],v[8:11]
ex v219,v219
mm v[40:43],a[104:107],v[228:231],v[40:43]
rd a[76:79],v248 offset:3072
cv v236,v216,v217
mm v[44:47],a[108:111],v[228:231],v[44:47]
ex v212,v212
mm v[12:15],a[108:111],v[224:227],v[12:15]
rd a[80:83],v248 offset:4096
wt lgkmcnt(5)
mm v[16:19],a[112:115],v[224:227],v[16:19]
ex v213,v213
mm v[48:51],a[112:115],v[228:231],v[48:51]
rd a[84:87],v248 offset:5120
cv v237,v218,v219
mm v[52:55],a[116:119],v[228:231],v[52:55]
ex v214,v214
mm v[20:23],a[116:119],v[224:227],v[20:23]
rd a[88:91],v248 offset:6144
cv v234,v212,v213
mm v[24:27],a[120:123],v[224:227],v[24:27]
ex v215,v215
mm v[56:59],a[120:123],v[228:231],v[56:59]
rd a[92:95],v248 offset:7168
cv v235,v214,v215
mm v[60:63],a[124:127],v[228:231],v[60:63]
ex v220,v220
mm v[28:31],a[124:127],v[224:227],v[28:31]
wt lgkmcnt(4)
mm v[192:195],a[64:67],a[0:3],0
ex v221,v221
mm v[192:195],a[68:71],a[4:7],v[192:195]
cv v238,v220,v221
mm v[192:195],a[72:75],a[8:11],v[192:195]
ex v222,v222
mm v[192:195],a[76:79],a[12:15],v[192:195]
ld v240,s[56:59],s17 offen lds
mm v[200:203],a[76:79],a[28:31],0
ex v223,v223
s_add_u32 s17, s17, 32768
mm v[200:203],a[72:75],a[24:27],v[200:203]
cv v239,v222,v223
s_add_u32 m0, m0, 1024
mm v[200:203],a[68:71],a[20:23],v[200:203]
ex v192,v192
mm v[200:203],a[64:67],a[16:19],v[200:203]
wt lgkmcnt(0)
mm v[196:199],a[80:83],a[0:3],0
ex v193,v193
mm v[196:199],a[84:87],a[4:7],v[196:199]
cv v224,v192,v193
mm v[196:199],a[88:91],a[8:11],v[196:199]
ex v194,v194
mm v[196:199],a[92:95],a[12:15],v[196:199]
ld v240,s[56:59],s17 offen lds
mm v[204:207],a[92:95],a[28:31],0
ex v195,v195
s_add_u32 s17, s17, 4294942720
mm v[204:207],a[88:91],a[24:27],v[204:207]
cv v225,v194,v195
s_add_u32 m0, m0, 1152
mm v[204:207],a[84:87],a[20:23],v[204:207]
ex v200,v200
mm v[204:207],a[80:83],a[16:19],v[204:207]
ld v240,s[56:59],s17 offen lds
mm v[208:211],a[64:67],a[32:35],0
ex v201,v201
s_add_u32 s17, s17, 32768
mm v[208:211],a[68:71],a[36:39],v[208:211]
cv v228,v200,v201
s_add_u32 m0, m0, 1024
mm v[208:211],a[72:75],a[40:43],v[208:211]
ex v202,v202
mm v[208:211],a[76:79],a[44:47],v[208:211]
ld v240,s[56:59],s17 offen lds
mm v[216:219],a[76:79],a[60:63],0
ex v203,v203
mm v[216:219],a[72:75],a[56:59],v[216:219]
cv v229,v202,v203
s_mov_b32 s93, 107520
mm v[216:219],a[68:71],a[52:55],v[216:219]
ex v196,v196
mm v[216:219],a[64:67],a[48:51],v[216:219]
ld a[188:191],v243,s[40:43],s93 offen
mm v[212:215],a[80:83],a[32:35],0
ex v197,v197
mm v[212:215],a[84:87],a[36:39],v[212:215]
)ASM" R"ASM(cv v226,v196,v197
mm v[212:215],a[88:91],a[40:43],v[212:215]
ex v198,v198
mm v[212:215],a[92:95],a[44:47],v[212:215]
mm v[220:223],a[92:95],a[60:63],0
ex v199,v199
mm v[220:223],a[88:91],a[56:59],v[220:223]
cv v227,v198,v199
mm v[220:223],a[84:87],a[52:55],v[220:223]
ex v204,v204
mm v[220:223],a[80:83],a[48:51],v[220:223]
mm v[64:67],a[96:99],v[232:235],v[64:67]
ex v205,v205
mm v[96:99],a[96:99],v[236:239],v[96:99]
rd a[96:99],v248 offset:8192
cv v230,v204,v205
mm v[100:103],a[100:103],v[236:239],v[100:103]
ex v206,v206
mm v[68:71],a[100:103],v[232:235],v[68:71]
rd a[100:103],v248 offset:9216
mm v[72:75],a[104:107],v[232:235],v[72:75]
ex v207,v207
mm v[104:107],a[104:107],v[236:239],v[104:107]
rd a[104:107],v248 offset:10240
cv v231,v206,v207
mm v[108:111],a[108:111],v[236:239],v[108:111]
ex v208,v208
mm v[76:79],a[108:111],v[232:235],v[76:79]
rd a[108:111],v248 offset:11264
mm v[80:83],a[112:115],v[232:235],v[80:83]
ex v209,v209
mm v[112:115],a[112:115],v[236:239],v[112:115]
rd a[112:115],v248 offset:12288
mm v[116:119],a[116:119],v[236:239],v[116:119]
ex v210,v210
mm v[84:87],a[116:119],v[232:235],v[84:87]
rd a[116:119],v248 offset:13312
mm v[88:91],a[120:123],v[232:235],v[88:91]
ex v211,v211
mm v[120:123],a[120:123],v[236:239],v[120:123]
rd a[120:123],v248 offset:14336
mm v[124:127],a[124:127],v[236:239],v[124:127]
ex v216,v216
mm v[92:95],a[124:127],v[232:235],v[92:95]
rd a[124:127],v248 offset:15360
v_smfmac_f32_16x16x64_bf16 a[196:199], a[208:211], v[232:239], v241
wt lgkmcnt(4)
v_smfmac_f32_16x16x64_bf16 a[192:195], a[208:211], v[224:231], v241
mm v[0:3],a[96:99],v[224:227],v[0:3]
ex v217,v217
mm v[32:35],a[96:99],v[228:231],v[32:35]
cv v232,v208,v209
mm v[36:39],a[100:103],v[228:231],v[36:39]
ex v218,v218
mm v[4:7],a[100:103],v[224:227],v[4:7]
cv v233,v210,v211
mm v[8:11],a[104:107],v[224:227],v[8:11]
ex v219,v219
mm v[40:43],a[104:107],v[228:231],v[40:43]
cv v236,v216,v217
mm v[44:47],a[108:111],v[228:231],v[44:47]
ex v212,v212
mm v[12:15],a[108:111],v[224:227],v[12:15]
wt lgkmcnt(0)
mm v[16:19],a[112:115],v[224:227],v[16:19]
ex v213,v213
mm v[48:51],a[112:115],v[228:231],v[48:51]
cv v237,v218,v219
mm v[52:55],a[116:119],v[228:231],v[52:55]
ex v214,v214
mm v[20:23],a[116:119],v[224:227],v[20:23]
cv v234,v212,v213
mm v[24:27],a[120:123],v[224:227],v[24:27]
ex v215,v215
mm v[56:59],a[120:123],v[228:231],v[56:59]
cv v235,v214,v215
mm v[60:63],a[124:127],v[228:231],v[60:63]
ex v220,v220
mm v[28:31],a[124:127],v[224:227],v[28:31]
; ---- helper: partial O and l go to the partner
.La5_LA_DP:
ex v221,v221
ex v222,v222
ex v223,v223
cv v238,v220,v221
s_nop 0
cv v239,v222,v223
s_nop 1
v_smfmac_f32_16x16x64_bf16 a[196:199], a[208:211], v[232:239], v241
cv v0,v0,v1
cv v1,v2,v3
cv v2,v4,v5
cv v3,v6,v7
st v[0:3],v255,s[68:71],0 offen offset:0 sc1
mm v[64:67],a[96:99],v[232:235],v[64:67]
cv v8,v8,v9
cv v9,v10,v11
cv v10,v12,v13
cv v11,v14,v15
st v[8:11],v255,s[68:71],0 offen offset:1024 sc1
)ASM" R"ASM(mm v[96:99],a[96:99],v[236:239],v[96:99]
ar v240,a224
v_xor_b32 v241, 32, v240
cv v16,v16,v17
cv v17,v18,v19
cv v18,v20,v21
cv v19,v22,v23
mm v[100:103],a[100:103],v[236:239],v[100:103]
v_xor_b32 v242, 64, v240
v_xor_b32 v243, 96, v240
st v[16:19],v255,s[68:71],0 offen offset:2048 sc1
cv v24,v24,v25
cv v25,v26,v27
mm v[68:71],a[100:103],v[232:235],v[68:71]
cv v26,v28,v29
cv v27,v30,v31
st v[24:27],v255,s[68:71],0 offen offset:3072 sc1
cv v32,v32,v33
cv v33,v34,v35
mm v[72:75],a[104:107],v[232:235],v[72:75]
cv v34,v36,v37
cv v35,v38,v39
st v[32:35],v255,s[68:71],s95 offen offset:0 sc1
cv v40,v40,v41
cv v41,v42,v43
mm v[104:107],a[104:107],v[236:239],v[104:107]
wt vmcnt(13)
s_barrier
cv v42,v44,v45
cv v43,v46,v47
st v[40:43],v255,s[68:71],s95 offen offset:1024 sc1
mm v[108:111],a[108:111],v[236:239],v[108:111]
rt v[192:193],v240 offset:16896
rt v[194:195],v240 offset:17920
cv v48,v48,v49
cv v49,v50,v51
cv v50,v52,v53
cv v51,v54,v55
mm v[76:79],a[108:111],v[232:235],v[76:79]
rt v[196:197],v240 offset:25344
rt v[198:199],v240 offset:26368
st v[48:51],v255,s[68:71],s95 offen offset:2048 sc1
cv v56,v56,v57
cv v57,v58,v59
mm v[80:83],a[112:115],v[232:235],v[80:83]
rt v[200:201],v241 offset:16896
rt v[202:203],v241 offset:17920
cv v58,v60,v61
cv v59,v62,v63
st v[56:59],v255,s[68:71],s95 offen offset:3072 sc1
mm v[112:115],a[112:115],v[236:239],v[112:115]
rt v[204:205],v241 offset:25344
rt v[206:207],v241 offset:26368
mm v[116:119],a[116:119],v[236:239],v[116:119]
rt v[208:209],v242 offset:16896
rt v[210:211],v242 offset:17920
mm v[84:87],a[116:119],v[232:235],v[84:87]
rt v[212:213],v242 offset:25344
rt v[214:215],v242 offset:26368
mm v[88:91],a[120:123],v[232:235],v[88:91]
rt v[216:217],v243 offset:16896
rt v[218:219],v243 offset:17920
mm v[120:123],a[120:123],v[236:239],v[120:123]
rt v[220:221],v243 offset:25344
rt v[222:223],v243 offset:26368
mm v[124:127],a[124:127],v[236:239],v[124:127]
mm v[92:95],a[124:127],v[232:235],v[92:95]
wt lgkmcnt(12)
mm v[0:3],v[192:195],v[128:131],0
mm v[0:3],v[196:199],v[132:135],v[0:3]
mm v[32:35],v[196:199],v[148:151],0
wt lgkmcnt(8)
ar v224,a192
ar v228,a193
ar v225,a194
ar v229,a195
ar v226,a196
mm v[32:35],v[192:195],v[144:147],v[32:35]
rt v[196:197],v240 offset:17024
rt v[198:199],v240 offset:18048
ar v230,a197
ar v227,a198
ar v231,a199
v_add_f32 v224, v224, v228
mm v[36:39],v[200:203],v[144:147],0
v_add_f32 v225, v225, v229
v_add_f32 v226, v226, v230
v_add_f32 v227, v227, v231
s_mov_b32 s92, 16384
st v[224:227],v255,s[68:71],s92 offen sc1
mm v[36:39],v[204:207],v[148:151],v[36:39]
rt v[192:193],v240 offset:25472
rt v[194:195],v240 offset:26496
cv v64,v64,v65
cv v65,v66,v67
cv v66,v68,v69
cv v67,v70,v71
mm v[4:7],v[204:207],v[132:135],0
wt lgkmcnt(4)
st v[64:67],v255,s[68:71],s96 offen offset:0 sc1
)ASM" R"ASM(cv v72,v72,v73
cv v73,v74,v75
cv v74,v76,v77
mm v[4:7],v[200:203],v[128:131],v[4:7]
rt v[204:205],v241 offset:17024
rt v[206:207],v241 offset:18048
cv v75,v78,v79
st v[72:75],v255,s[68:71],s96 offen offset:1024 sc1
cv v80,v80,v81
mm v[8:11],v[208:211],v[128:131],0
cv v81,v82,v83
cv v82,v84,v85
cv v83,v86,v87
st v[80:83],v255,s[68:71],s96 offen offset:2048 sc1
cv v88,v88,v89
mm v[8:11],v[212:215],v[132:135],v[8:11]
rt v[200:201],v241 offset:25472
rt v[202:203],v241 offset:26496
cv v89,v90,v91
cv v90,v92,v93
cv v91,v94,v95
st v[88:91],v255,s[68:71],s96 offen offset:3072 sc1
mm v[40:43],v[212:215],v[148:151],0
cv v96,v96,v97
cv v97,v98,v99
cv v98,v100,v101
cv v99,v102,v103
st v[96:99],v255,s[68:71],s97 offen offset:0 sc1
mm v[40:43],v[208:211],v[144:147],v[40:43]
rt v[212:213],v242 offset:17024
rt v[214:215],v242 offset:18048
cv v104,v104,v105
cv v105,v106,v107
cv v106,v108,v109
cv v107,v110,v111
mm v[44:47],v[216:219],v[144:147],0
st v[104:107],v255,s[68:71],s97 offen offset:1024 sc1
cv v112,v112,v113
cv v113,v114,v115
cv v114,v116,v117
cv v115,v118,v119
mm v[44:47],v[220:223],v[148:151],v[44:47]
rt v[208:209],v242 offset:25472
.La5_LW6_5:
s_cbranch_execz .La5_LW6_R0
rt v[210:211],v242 offset:26496
st v[112:115],v255,s[68:71],s97 offen offset:2048 sc1
cv v120,v120,v121
cv v121,v122,v123
mm v[12:15],v[220:223],v[132:135],0
wt lgkmcnt(4)
cv v122,v124,v125
cv v123,v126,v127
st v[120:123],v255,s[68:71],s97 offen offset:3072 sc1
v_mov_b32_e64 v249, 0
mm v[12:15],v[216:219],v[128:131],v[12:15]
rt v[220:221],v243 offset:17024
rt v[222:223],v243 offset:18048
s_add_u32 s6, s88, 393216
mm v[16:19],v[196:199],v[128:131],0
ar v224,a228
v_add_u32 v225, 0x8000, v224
v_add_u32 v226, 0x2000, v224
v_add_u32 v227, 0xa000, v224
mm v[16:19],v[192:195],v[132:135],v[16:19]
rt v[216:217],v243 offset:25472
rt v[218:219],v243 offset:26496
ar v248,a225
ar v228,a229
mm v[48:51],v[192:195],v[148:151],0
ar v229,a230
mm v[48:51],v[196:199],v[144:147],v[48:51]
rt v[192:193],v240 offset:33792
rt v[194:195],v240 offset:34816
mm v[52:55],v[204:207],v[144:147],0
mm v[52:55],v[200:203],v[148:151],v[52:55]
rt v[196:197],v240 offset:42240
rt v[198:199],v240 offset:43264
mm v[20:23],v[200:203],v[132:135],0
wt lgkmcnt(4)
mm v[20:23],v[204:207],v[128:131],v[20:23]
rt v[200:201],v241 offset:33792
rt v[202:203],v241 offset:34816
mm v[24:27],v[212:215],v[128:131],0
s_bitcmp1_b32 2863311530, s2
s_cbranch_scc1 .La5_LO_VC2
s_getpc_b64 s[24:25]
.La5_LW3_P0:
s_add_u32 s24, s24, .La5_LW3_R0-.La5_LW3_P0
s_addc_u32 s25, s25, 0
s_mov_b64 exec, 0
s_cmp_eq_u32 s3, 0
s_cbranch_scc1 .La5_LW3_0
s_cmp_eq_u32 s3, 1
s_cbranch_scc1 .La5_LW3_1
s_cmp_eq_u32 s3, 2
s_cbranch_scc1 .La5_LW3_2
s_branch .La5_LW3_3
.La5_LW3_R0:
s_mov_b64 exec, -1
)ASM" R"ASM(.La5_LO_VE2:
wt vmcnt(0)
buffer_store_dwordx2 v[252:253], v249, s[64:67], s6 offen offset:0 sc1
mm v[24:27],v[208:211],v[132:135],v[24:27]
rt v[204:205],v241 offset:42240
rt v[206:207],v241 offset:43264
mm v[56:59],v[208:211],v[148:151],0
s_mov_b32 s93, 3670016
ld a[192:195],v228,s[40:43],s93 offen
mm v[56:59],v[212:215],v[144:147],v[56:59]
rt v[208:209],v242 offset:33792
rt v[210:211],v242 offset:34816
mm v[60:63],v[220:223],v[144:147],0
s_mov_b32 s93, 3677184
ld a[196:199],v228,s[40:43],s93 offen
mm v[60:63],v[216:219],v[148:151],v[60:63]
rt v[212:213],v242 offset:42240
rt v[214:215],v242 offset:43264
mm v[28:31],v[216:219],v[132:135],0
wt lgkmcnt(4)
s_mov_b32 s93, 3684352
ld a[200:203],v228,s[40:43],s93 offen
mm v[28:31],v[220:223],v[128:131],v[28:31]
rt v[216:217],v243 offset:33792
rt v[218:219],v243 offset:34816
mm v[0:3],v[192:195],v[136:139],v[0:3]
s_mov_b32 s93, 3691520
ld a[204:207],v228,s[40:43],s93 offen
mm v[0:3],v[196:199],v[140:143],v[0:3]
rt v[220:221],v243 offset:42240
rt v[222:223],v243 offset:43264
mm v[32:35],v[196:199],v[156:159],v[32:35]
s_mov_b32 s93, 3698688
ld a[208:211],v228,s[40:43],s93 offen
mm v[32:35],v[192:195],v[152:155],v[32:35]
rt v[196:197],v240 offset:33920
rt v[198:199],v240 offset:34944
mm v[36:39],v[200:203],v[152:155],v[36:39]
s_mov_b32 s93, 3705856
ld a[212:215],v228,s[40:43],s93 offen
mm v[36:39],v[204:207],v[156:159],v[36:39]
rt v[192:193],v240 offset:42368
rt v[194:195],v240 offset:43392
mm v[4:7],v[204:207],v[140:143],v[4:7]
wt lgkmcnt(4)
s_mov_b32 s93, 3713024
ld a[216:219],v228,s[40:43],s93 offen
mm v[4:7],v[200:203],v[136:139],v[4:7]
rt v[204:205],v241 offset:33920
rt v[206:207],v241 offset:34944
mm v[8:11],v[208:211],v[136:139],v[8:11]
s_mov_b32 s93, 3720192
ld a[220:223],v228,s[40:43],s93 offen
mm v[8:11],v[212:215],v[140:143],v[8:11]
rt v[200:201],v241 offset:42368
rt v[202:203],v241 offset:43392
mm v[40:43],v[212:215],v[156:159],v[40:43]
s_mov_b32 s93, 3727360
ld a[224:227],v228,s[40:43],s93 offen
mm v[40:43],v[208:211],v[152:155],v[40:43]
rt v[212:213],v242 offset:33920
rt v[214:215],v242 offset:34944
mm v[44:47],v[216:219],v[152:155],v[44:47]
s_mov_b32 s93, 3734528
ld a[228:231],v228,s[40:43],s93 offen
mm v[44:47],v[220:223],v[156:159],v[44:47]
rt v[208:209],v242 offset:42368
rt v[210:211],v242 offset:43392
mm v[12:15],v[220:223],v[140:143],v[12:15]
wt lgkmcnt(4)
s_mov_b32 s93, 3741696
ld a[232:235],v228,s[40:43],s93 offen
mm v[12:15],v[216:219],v[136:139],v[12:15]
rt v[220:221],v243 offset:33920
rt v[222:223],v243 offset:34944
mm v[16:19],v[196:199],v[136:139],v[16:19]
s_mov_b32 s93, 3748864
ld a[236:239],v228,s[40:43],s93 offen
mm v[16:19],v[192:195],v[140:143],v[16:19]
rt v[216:217],v243 offset:42368
rt v[218:219],v243 offset:43392
mm v[48:51],v[192:195],v[156:159],v[48:51]
s_mov_b32 s93, 3756032
ld a[240:243],v228,s[40:43],s93 offen
mm v[48:51],v[196:199],v[152:155],v[48:51]
rt v[192:193],v240 offset:16896
rt v[194:195],v240 offset:17920
mm v[52:55],v[204:207],v[152:155],v[52:55]
s_mov_b32 s93, 3763200
ld a[244:247],v228,s[40:43],s93 offen
mm v[52:55],v[200:203],v[156:159],v[52:55]
rt v[196:197],v240 offset:25344
rt v[198:199],v240 offset:26368
mm v[20:23],v[200:203],v[140:143],v[20:23]
wt lgkmcnt(4)
s_mov_b32 s93, 3770368
ld a[248:251],v228,s[40:43],s93 offen
mm v[20:23],v[204:207],v[136:139],v[20:23]
rt v[200:201],v241 offset:16896
rt v[202:203],v241 offset:17920
)ASM" R"ASM(mm v[24:27],v[212:215],v[136:139],v[24:27]
s_mov_b32 s93, 3777536
ld a[252:255],v228,s[40:43],s93 offen
mm v[24:27],v[208:211],v[140:143],v[24:27]
rt v[204:205],v241 offset:25344
rt v[206:207],v241 offset:26368
mm v[56:59],v[208:211],v[156:159],v[56:59]
mm v[56:59],v[212:215],v[152:155],v[56:59]
rt v[208:209],v242 offset:16896
rt v[210:211],v242 offset:17920
mm v[60:63],v[220:223],v[152:155],v[60:63]
mm v[60:63],v[216:219],v[156:159],v[60:63]
rt v[212:213],v242 offset:25344
rt v[214:215],v242 offset:26368
mm v[28:31],v[216:219],v[140:143],v[28:31]
wt lgkmcnt(4)
mm v[28:31],v[220:223],v[136:139],v[28:31]
rt v[216:217],v243 offset:16896
rt v[218:219],v243 offset:17920
s_branch .La5_LO_START
; ---- owner: last windows
.La5_LA_DC:
.La5_LW5_0:
s_cbranch_execz .La5_LW5_4
wt lgkmcnt(4)
mm v[192:195],a[64:67],v[128:131],0
ex v221,v221
mm v[192:195],a[68:71],v[132:135],v[192:195]
cv v238,v220,v221
s_add_u32 s89, s89, 16384
mm v[192:195],a[72:75],v[136:139],v[192:195]
ex v222,v222
mm v[192:195],a[76:79],v[140:143],v[192:195]
ld v255,s[60:63],s17 offen offset:0 sc1 lds
mm v[200:203],a[76:79],v[156:159],0
ex v223,v223
mm v[200:203],a[72:75],v[152:155],v[200:203]
cv v239,v222,v223
s_and_b32 s89, s89, 0x1ffff
mm v[200:203],a[68:71],v[148:151],v[200:203]
ex v192,v192
mm v[200:203],a[64:67],v[144:147],v[200:203]
wt lgkmcnt(0)
mm v[196:199],a[80:83],v[128:131],0
ex v193,v193
mm v[196:199],a[84:87],v[132:135],v[196:199]
cv v224,v192,v193
ar v243,a229
mm v[196:199],a[88:91],v[136:139],v[196:199]
ex v194,v194
mm v[196:199],a[92:95],v[140:143],v[196:199]
ld v255,s[60:63],s17 offen offset:1024 sc1 lds
mm v[204:207],a[92:95],v[156:159],0
ex v195,v195
mm v[204:207],a[88:91],v[152:155],v[204:207]
cv v225,v194,v195
mm v[204:207],a[84:87],v[148:151],v[204:207]
ex v200,v200
mm v[204:207],a[80:83],v[144:147],v[204:207]
ld v255,s[60:63],s17 offen offset:2048 sc1 lds
mm v[208:211],a[64:67],v[160:163],0
ex v201,v201
mm v[208:211],a[68:71],v[164:167],v[208:211]
cv v228,v200,v201
mm v[208:211],a[72:75],v[168:171],v[208:211]
ex v202,v202
mm v[208:211],a[76:79],v[172:175],v[208:211]
ld v255,s[60:63],s17 offen offset:3072 sc1 lds
mm v[216:219],a[76:79],v[188:191],0
ex v203,v203
mm v[216:219],a[72:75],v[184:187],v[216:219]
cv v229,v202,v203
s_mov_b32 s93, 0
mm v[216:219],a[68:71],v[180:183],v[216:219]
ex v196,v196
mm v[216:219],a[64:67],v[176:179],v[216:219]
ld a[128:131],v243,s[40:43],s93 offen
mm v[212:215],a[80:83],v[160:163],0
ex v197,v197
s_add_u32 s17, s17, s80
mm v[212:215],a[84:87],v[164:167],v[212:215]
cv v226,v196,v197
s_mov_b32 s93, 7168
s_add_u32 m0, m0, 16384
mm v[212:215],a[88:91],v[168:171],v[212:215]
ex v198,v198
s_and_b32 m0, m0, s78
mm v[212:215],a[92:95],v[172:175],v[212:215]
ld a[132:135],v243,s[40:43],s93 offen
mm v[220:223],a[92:95],v[188:191],0
ex v199,v199
mm v[220:223],a[88:91],v[184:187],v[220:223]
cv v227,v198,v199
mm v[220:223],a[84:87],v[180:183],v[220:223]
ex v204,v204
mm v[220:223],a[80:83],v[176:179],v[220:223]
wt vmcnt(12)
s_barrier
mm v[64:67],a[96:99],v[232:235],v[64:67]
ex v205,v205
)ASM" R"ASM(mm v[96:99],a[96:99],v[236:239],v[96:99]
rd a[96:99],v248 offset:8192
cv v230,v204,v205
s_mov_b32 s93, 28672
mm v[100:103],a[100:103],v[236:239],v[100:103]
ex v206,v206
mm v[68:71],a[100:103],v[232:235],v[68:71]
rd a[100:103],v248 offset:9216
ld a[144:147],v243,s[40:43],s93 offen
mm v[72:75],a[104:107],v[232:235],v[72:75]
ex v207,v207
mm v[104:107],a[104:107],v[236:239],v[104:107]
rd a[104:107],v248 offset:10240
cv v231,v206,v207
s_mov_b32 s93, 14336
mm v[108:111],a[108:111],v[236:239],v[108:111]
ex v208,v208
mm v[76:79],a[108:111],v[232:235],v[76:79]
rd a[108:111],v248 offset:11264
ld a[136:139],v243,s[40:43],s93 offen
mm v[80:83],a[112:115],v[232:235],v[80:83]
ex v209,v209
mm v[112:115],a[112:115],v[236:239],v[112:115]
rd a[112:115],v248 offset:12288
s_mov_b32 s93, 21504
mm v[116:119],a[116:119],v[236:239],v[116:119]
ex v210,v210
mm v[84:87],a[116:119],v[232:235],v[84:87]
rd a[116:119],v248 offset:13312
ld a[140:143],v243,s[40:43],s93 offen
mm v[88:91],a[120:123],v[232:235],v[88:91]
ex v211,v211
mm v[120:123],a[120:123],v[236:239],v[120:123]
rd a[120:123],v248 offset:14336
s_mov_b32 s93, 35840
mm v[124:127],a[124:127],v[236:239],v[124:127]
ex v216,v216
mm v[92:95],a[124:127],v[232:235],v[92:95]
rd a[124:127],v248 offset:15360
ld a[148:151],v243,s[40:43],s93 offen
v_or_b32 v248, s89, v255
v_smfmac_f32_16x16x64_bf16 a[196:199], a[208:211], v[232:239], v241
wt lgkmcnt(4)
v_smfmac_f32_16x16x64_bf16 a[192:195], a[208:211], v[224:231], v241
rd a[64:67],v248 offset:0
mm v[0:3],a[96:99],v[224:227],v[0:3]
ex v217,v217
mm v[32:35],a[96:99],v[228:231],v[32:35]
rd a[68:71],v248 offset:1024
cv v232,v208,v209
mm v[36:39],a[100:103],v[228:231],v[36:39]
ex v218,v218
mm v[4:7],a[100:103],v[224:227],v[4:7]
rd a[72:75],v248 offset:2048
cv v233,v210,v211
mm v[8:11],a[104:107],v[224:227],v[8:11]
ex v219,v219
mm v[40:43],a[104:107],v[228:231],v[40:43]
rd a[76:79],v248 offset:3072
cv v236,v216,v217
mm v[44:47],a[108:111],v[228:231],v[44:47]
ex v212,v212
mm v[12:15],a[108:111],v[224:227],v[12:15]
rd a[80:83],v248 offset:4096
wt lgkmcnt(5)
mm v[16:19],a[112:115],v[224:227],v[16:19]
ex v213,v213
mm v[48:51],a[112:115],v[228:231],v[48:51]
rd a[84:87],v248 offset:5120
cv v237,v218,v219
mm v[52:55],a[116:119],v[228:231],v[52:55]
ex v214,v214
mm v[20:23],a[116:119],v[224:227],v[20:23]
rd a[88:91],v248 offset:6144
cv v234,v212,v213
mm v[24:27],a[120:123],v[224:227],v[24:27]
ex v215,v215
mm v[56:59],a[120:123],v[228:231],v[56:59]
rd a[92:95],v248 offset:7168
cv v235,v214,v215
mm v[60:63],a[124:127],v[228:231],v[60:63]
ex v220,v220
mm v[28:31],a[124:127],v[224:227],v[28:31]
wt lgkmcnt(4)
mm v[192:195],a[64:67],v[128:131],0
ex v221,v221
mm v[192:195],a[68:71],v[132:135],v[192:195]
cv v238,v220,v221
s_add_u32 s89, s89, 16384
mm v[192:195],a[72:75],v[136:139],v[192:195]
ex v222,v222
mm v[192:195],a[76:79],v[140:143],v[192:195]
ld v255,s[60:63],s17 offen offset:0 sc1 lds
mm v[200:203],a[76:79],v[156:159],0
ex v223,v223
mm v[200:203],a[72:75],v[152:155],v[200:203]
cv v239,v222,v223
s_and_b32 s89, s89, 0x1ffff
mm v[200:203],a[68:71],v[148:151],v[200:203]
ex v192,v192
)ASM" R"ASM(mm v[200:203],a[64:67],v[144:147],v[200:203]
wt lgkmcnt(0)
mm v[196:199],a[80:83],v[128:131],0
ex v193,v193
mm v[196:199],a[84:87],v[132:135],v[196:199]
cv v224,v192,v193
mm v[196:199],a[88:91],v[136:139],v[196:199]
ex v194,v194
mm v[196:199],a[92:95],v[140:143],v[196:199]
ld v255,s[60:63],s17 offen offset:1024 sc1 lds
mm v[204:207],a[92:95],v[156:159],0
ex v195,v195
mm v[204:207],a[88:91],v[152:155],v[204:207]
cv v225,v194,v195
mm v[204:207],a[84:87],v[148:151],v[204:207]
ex v200,v200
mm v[204:207],a[80:83],v[144:147],v[204:207]
ld v255,s[60:63],s17 offen offset:2048 sc1 lds
mm v[208:211],a[64:67],v[160:163],0
ex v201,v201
mm v[208:211],a[68:71],v[164:167],v[208:211]
cv v228,v200,v201
mm v[208:211],a[72:75],v[168:171],v[208:211]
ex v202,v202
mm v[208:211],a[76:79],v[172:175],v[208:211]
ld v255,s[60:63],s17 offen offset:3072 sc1 lds
mm v[216:219],a[76:79],v[188:191],0
ex v203,v203
mm v[216:219],a[72:75],v[184:187],v[216:219]
cv v229,v202,v203
s_mov_b32 s93, 43008
mm v[216:219],a[68:71],v[180:183],v[216:219]
ex v196,v196
mm v[216:219],a[64:67],v[176:179],v[216:219]
ld a[152:155],v243,s[40:43],s93 offen
mm v[212:215],a[80:83],v[160:163],0
ex v197,v197
s_add_u32 s17, s17, s80
mm v[212:215],a[84:87],v[164:167],v[212:215]
cv v226,v196,v197
s_mov_b32 s93, 50176
s_add_u32 m0, m0, 16384
mm v[212:215],a[88:91],v[168:171],v[212:215]
ex v198,v198
s_and_b32 m0, m0, s78
mm v[212:215],a[92:95],v[172:175],v[212:215]
ld a[156:159],v243,s[40:43],s93 offen
mm v[220:223],a[92:95],v[188:191],0
ex v199,v199
mm v[220:223],a[88:91],v[184:187],v[220:223]
cv v227,v198,v199
mm v[220:223],a[84:87],v[180:183],v[220:223]
ex v204,v204
mm v[220:223],a[80:83],v[176:179],v[220:223]
wt vmcnt(17)
s_barrier
mm v[64:67],a[96:99],v[232:235],v[64:67]
ex v205,v205
mm v[96:99],a[96:99],v[236:239],v[96:99]
rd a[96:99],v248 offset:8192
cv v230,v204,v205
s_mov_b32 s93, 71680
mm v[100:103],a[100:103],v[236:239],v[100:103]
ex v206,v206
mm v[68:71],a[100:103],v[232:235],v[68:71]
rd a[100:103],v248 offset:9216
ld a[168:171],v243,s[40:43],s93 offen
mm v[72:75],a[104:107],v[232:235],v[72:75]
ex v207,v207
mm v[104:107],a[104:107],v[236:239],v[104:107]
rd a[104:107],v248 offset:10240
cv v231,v206,v207
s_mov_b32 s93, 57344
mm v[108:111],a[108:111],v[236:239],v[108:111]
ex v208,v208
mm v[76:79],a[108:111],v[232:235],v[76:79]
rd a[108:111],v248 offset:11264
ld a[160:163],v243,s[40:43],s93 offen
mm v[80:83],a[112:115],v[232:235],v[80:83]
ex v209,v209
mm v[112:115],a[112:115],v[236:239],v[112:115]
rd a[112:115],v248 offset:12288
s_mov_b32 s93, 64512
mm v[116:119],a[116:119],v[236:239],v[116:119]
ex v210,v210
mm v[84:87],a[116:119],v[232:235],v[84:87]
rd a[116:119],v248 offset:13312
ld a[164:167],v243,s[40:43],s93 offen
mm v[88:91],a[120:123],v[232:235],v[88:91]
ex v211,v211
mm v[120:123],a[120:123],v[236:239],v[120:123]
rd a[120:123],v248 offset:14336
mm v[124:127],a[124:127],v[236:239],v[124:127]
ex v216,v216
mm v[92:95],a[124:127],v[232:235],v[92:95]
rd a[124:127],v248 offset:15360
v_or_b32 v248, s89, v255
v_smfmac_f32_16x16x64_bf16 a[196:199], a[208:211], v[232:239], v241
wt lgkmcnt(4)
v_smfmac_f32_16x16x64_bf16 a[192:195], a[208:211], v[224:231], v241
)ASM" R"ASM(rd a[64:67],v248 offset:0
mm v[0:3],a[96:99],v[224:227],v[0:3]
ex v217,v217
mm v[32:35],a[96:99],v[228:231],v[32:35]
rd a[68:71],v248 offset:1024
cv v232,v208,v209
mm v[36:39],a[100:103],v[228:231],v[36:39]
ex v218,v218
mm v[4:7],a[100:103],v[224:227],v[4:7]
rd a[72:75],v248 offset:2048
cv v233,v210,v211
mm v[8:11],a[104:107],v[224:227],v[8:11]
ex v219,v219
mm v[40:43],a[104:107],v[228:231],v[40:43]
rd a[76:79],v248 offset:3072
cv v236,v216,v217
mm v[44:47],a[108:111],v[228:231],v[44:47]
ex v212,v212
mm v[12:15],a[108:111],v[224:227],v[12:15]
rd a[80:83],v248 offset:4096
wt lgkmcnt(5)
mm v[16:19],a[112:115],v[224:227],v[16:19]
ex v213,v213
mm v[48:51],a[112:115],v[228:231],v[48:51]
rd a[84:87],v248 offset:5120
cv v237,v218,v219
mm v[52:55],a[116:119],v[228:231],v[52:55]
ex v214,v214
mm v[20:23],a[116:119],v[224:227],v[20:23]
rd a[88:91],v248 offset:6144
cv v234,v212,v213
mm v[24:27],a[120:123],v[224:227],v[24:27]
ex v215,v215
mm v[56:59],a[120:123],v[228:231],v[56:59]
rd a[92:95],v248 offset:7168
cv v235,v214,v215
mm v[60:63],a[124:127],v[228:231],v[60:63]
ex v220,v220
mm v[28:31],a[124:127],v[224:227],v[28:31]
wt lgkmcnt(4)
mm v[192:195],a[64:67],v[128:131],0
ex v221,v221
mm v[192:195],a[68:71],v[132:135],v[192:195]
cv v238,v220,v221
s_add_u32 s89, s89, 16384
mm v[192:195],a[72:75],v[136:139],v[192:195]
ex v222,v222
mm v[192:195],a[76:79],v[140:143],v[192:195]
ld v255,s[60:63],s17 offen offset:0 sc1 lds
mm v[200:203],a[76:79],v[156:159],0
ex v223,v223
mm v[200:203],a[72:75],v[152:155],v[200:203]
cv v239,v222,v223
s_and_b32 s89, s89, 0x1ffff
mm v[200:203],a[68:71],v[148:151],v[200:203]
ex v192,v192
mm v[200:203],a[64:67],v[144:147],v[200:203]
wt lgkmcnt(0)
mm v[196:199],a[80:83],v[128:131],0
ex v193,v193
mm v[196:199],a[84:87],v[132:135],v[196:199]
cv v224,v192,v193
mm v[196:199],a[88:91],v[136:139],v[196:199]
ex v194,v194
mm v[196:199],a[92:95],v[140:143],v[196:199]
ld v255,s[60:63],s17 offen offset:1024 sc1 lds
mm v[204:207],a[92:95],v[156:159],0
ex v195,v195
mm v[204:207],a[88:91],v[152:155],v[204:207]
cv v225,v194,v195
mm v[204:207],a[84:87],v[148:151],v[204:207]
ex v200,v200
mm v[204:207],a[80:83],v[144:147],v[204:207]
ld v255,s[60:63],s17 offen offset:2048 sc1 lds
mm v[208:211],a[64:67],v[160:163],0
ex v201,v201
mm v[208:211],a[68:71],v[164:167],v[208:211]
cv v228,v200,v201
mm v[208:211],a[72:75],v[168:171],v[208:211]
ex v202,v202
mm v[208:211],a[76:79],v[172:175],v[208:211]
ld v255,s[60:63],s17 offen offset:3072 sc1 lds
mm v[216:219],a[76:79],v[188:191],0
ex v203,v203
mm v[216:219],a[72:75],v[184:187],v[216:219]
cv v229,v202,v203
s_mov_b32 s93, 78848
mm v[216:219],a[68:71],v[180:183],v[216:219]
ex v196,v196
mm v[216:219],a[64:67],v[176:179],v[216:219]
ld a[172:175],v243,s[40:43],s93 offen
mm v[212:215],a[80:83],v[160:163],0
ex v197,v197
s_add_u32 s17, s17, s80
mm v[212:215],a[84:87],v[164:167],v[212:215]
cv v226,v196,v197
s_mov_b32 s93, 86016
s_add_u32 m0, m0, 16384
mm v[212:215],a[88:91],v[168:171],v[212:215]
ex v198,v198
)ASM" R"ASM(s_and_b32 m0, m0, s78
mm v[212:215],a[92:95],v[172:175],v[212:215]
ld a[176:179],v243,s[40:43],s93 offen
mm v[220:223],a[92:95],v[188:191],0
ex v199,v199
mm v[220:223],a[88:91],v[184:187],v[220:223]
cv v227,v198,v199
mm v[220:223],a[84:87],v[180:183],v[220:223]
ex v204,v204
mm v[220:223],a[80:83],v[176:179],v[220:223]
wt vmcnt(21)
s_barrier
mm v[64:67],a[96:99],v[232:235],v[64:67]
ex v205,v205
mm v[96:99],a[96:99],v[236:239],v[96:99]
rd a[96:99],v248 offset:8192
cv v230,v204,v205
s_mov_b32 s93, 107520
mm v[100:103],a[100:103],v[236:239],v[100:103]
ex v206,v206
mm v[68:71],a[100:103],v[232:235],v[68:71]
rd a[100:103],v248 offset:9216
ld a[188:191],v243,s[40:43],s93 offen
mm v[72:75],a[104:107],v[232:235],v[72:75]
ex v207,v207
mm v[104:107],a[104:107],v[236:239],v[104:107]
rd a[104:107],v248 offset:10240
.La5_LW5_1:
s_cbranch_execz .La5_LW5_R0
cv v231,v206,v207
s_mov_b32 s93, 93184
mm v[108:111],a[108:111],v[236:239],v[108:111]
ex v208,v208
mm v[76:79],a[108:111],v[232:235],v[76:79]
rd a[108:111],v248 offset:11264
ld a[180:183],v243,s[40:43],s93 offen
mm v[80:83],a[112:115],v[232:235],v[80:83]
ex v209,v209
mm v[112:115],a[112:115],v[236:239],v[112:115]
rd a[112:115],v248 offset:12288
s_mov_b32 s93, 100352
mm v[116:119],a[116:119],v[236:239],v[116:119]
ex v210,v210
mm v[84:87],a[116:119],v[232:235],v[84:87]
rd a[116:119],v248 offset:13312
ld a[184:187],v243,s[40:43],s93 offen
mm v[88:91],a[120:123],v[232:235],v[88:91]
ex v211,v211
mm v[120:123],a[120:123],v[236:239],v[120:123]
rd a[120:123],v248 offset:14336
mm v[124:127],a[124:127],v[236:239],v[124:127]
ex v216,v216
mm v[92:95],a[124:127],v[232:235],v[92:95]
rd a[124:127],v248 offset:15360
v_or_b32 v248, s89, v255
v_smfmac_f32_16x16x64_bf16 a[196:199], a[208:211], v[232:239], v241
wt lgkmcnt(4)
v_smfmac_f32_16x16x64_bf16 a[192:195], a[208:211], v[224:231], v241
rd a[64:67],v248 offset:0
mm v[0:3],a[96:99],v[224:227],v[0:3]
ex v217,v217
mm v[32:35],a[96:99],v[228:231],v[32:35]
rd a[68:71],v248 offset:1024
cv v232,v208,v209
mm v[36:39],a[100:103],v[228:231],v[36:39]
ex v218,v218
mm v[4:7],a[100:103],v[224:227],v[4:7]
rd a[72:75],v248 offset:2048
cv v233,v210,v211
mm v[8:11],a[104:107],v[224:227],v[8:11]
ex v219,v219
mm v[40:43],a[104:107],v[228:231],v[40:43]
rd a[76:79],v248 offset:3072
cv v236,v216,v217
mm v[44:47],a[108:111],v[228:231],v[44:47]
ex v212,v212
mm v[12:15],a[108:111],v[224:227],v[12:15]
rd a[80:83],v248 offset:4096
wt lgkmcnt(5)
mm v[16:19],a[112:115],v[224:227],v[16:19]
ex v213,v213
mm v[48:51],a[112:115],v[228:231],v[48:51]
rd a[84:87],v248 offset:5120
cv v237,v218,v219
mm v[52:55],a[116:119],v[228:231],v[52:55]
ex v214,v214
mm v[20:23],a[116:119],v[224:227],v[20:23]
rd a[88:91],v248 offset:6144
cv v234,v212,v213
mm v[24:27],a[120:123],v[224:227],v[24:27]
ex v215,v215
mm v[56:59],a[120:123],v[228:231],v[56:59]
rd a[92:95],v248 offset:7168
cv v235,v214,v215
mm v[60:63],a[124:127],v[228:231],v[60:63]
ex v220,v220
mm v[28:31],a[124:127],v[224:227],v[28:31]
.La5_LA_DC3:
.La5_LW5_2:
s_cbranch_execz .La5_LW5_R0
)ASM" R"ASM(wt lgkmcnt(4)
mm v[192:195],a[64:67],v[128:131],0
ex v221,v221
mm v[192:195],a[68:71],v[132:135],v[192:195]
cv v238,v220,v221
ar v240,a228
mm v[192:195],a[72:75],v[136:139],v[192:195]
ex v222,v222
mm v[192:195],a[76:79],v[140:143],v[192:195]
s_add_u32 m0, s83, 16896
s_lshl_b32 s17, s72, 18
mm v[200:203],a[76:79],v[156:159],0
ex v223,v223
mm v[200:203],a[72:75],v[152:155],v[200:203]
cv v239,v222,v223
s_add_u32 s89, s89, 16384
mm v[200:203],a[68:71],v[148:151],v[200:203]
ex v192,v192
mm v[200:203],a[64:67],v[144:147],v[200:203]
wt lgkmcnt(0)
s_and_b32 s89, s89, 0x1ffff
mm v[196:199],a[80:83],v[128:131],0
ex v193,v193
mm v[196:199],a[84:87],v[132:135],v[196:199]
cv v224,v192,v193
mm v[196:199],a[88:91],v[136:139],v[196:199]
ex v194,v194
mm v[196:199],a[92:95],v[140:143],v[196:199]
ld v240,s[56:59],s17 offen lds
mm v[204:207],a[92:95],v[156:159],0
ex v195,v195
s_add_u32 s17, s17, 32768
mm v[204:207],a[88:91],v[152:155],v[204:207]
cv v225,v194,v195
s_add_u32 m0, m0, 1024
mm v[204:207],a[84:87],v[148:151],v[204:207]
ex v200,v200
mm v[204:207],a[80:83],v[144:147],v[204:207]
ld v240,s[56:59],s17 offen lds
mm v[208:211],a[64:67],v[160:163],0
ex v201,v201
s_add_u32 s17, s17, 4294942720
mm v[208:211],a[68:71],v[164:167],v[208:211]
cv v228,v200,v201
s_add_u32 m0, m0, 1152
mm v[208:211],a[72:75],v[168:171],v[208:211]
ex v202,v202
mm v[208:211],a[76:79],v[172:175],v[208:211]
ld v240,s[56:59],s17 offen lds
mm v[216:219],a[76:79],v[188:191],0
ex v203,v203
s_add_u32 s17, s17, 32768
mm v[216:219],a[72:75],v[184:187],v[216:219]
cv v229,v202,v203
s_add_u32 m0, m0, 1024
mm v[216:219],a[68:71],v[180:183],v[216:219]
ex v196,v196
mm v[216:219],a[64:67],v[176:179],v[216:219]
ld v240,s[56:59],s17 offen lds
mm v[212:215],a[80:83],v[160:163],0
ex v197,v197
s_add_u32 s17, s17, 90112
mm v[212:215],a[84:87],v[164:167],v[212:215]
cv v226,v196,v197
s_add_u32 m0, s83, 33792
mm v[212:215],a[88:91],v[168:171],v[212:215]
ex v198,v198
mm v[212:215],a[92:95],v[172:175],v[212:215]
mm v[220:223],a[92:95],v[188:191],0
ex v199,v199
mm v[220:223],a[88:91],v[184:187],v[220:223]
cv v227,v198,v199
mm v[220:223],a[84:87],v[180:183],v[220:223]
ex v204,v204
mm v[220:223],a[80:83],v[176:179],v[220:223]
wt vmcnt(10)
s_barrier
mm v[64:67],a[96:99],v[232:235],v[64:67]
ex v205,v205
mm v[96:99],a[96:99],v[236:239],v[96:99]
rd a[96:99],v248 offset:8192
cv v230,v204,v205
mm v[100:103],a[100:103],v[236:239],v[100:103]
ex v206,v206
mm v[68:71],a[100:103],v[232:235],v[68:71]
rd a[100:103],v248 offset:9216
mm v[72:75],a[104:107],v[232:235],v[72:75]
ex v207,v207
mm v[104:107],a[104:107],v[236:239],v[104:107]
rd a[104:107],v248 offset:10240
cv v231,v206,v207
mm v[108:111],a[108:111],v[236:239],v[108:111]
ex v208,v208
mm v[76:79],a[108:111],v[232:235],v[76:79]
rd a[108:111],v248 offset:11264
mm v[80:83],a[112:115],v[232:235],v[80:83]
ex v209,v209
mm v[112:115],a[112:115],v[236:239],v[112:115]
rd a[112:115],v248 offset:12288
mm v[116:119],a[116:119],v[236:239],v[116:119]
)ASM" R"ASM(ex v210,v210
mm v[84:87],a[116:119],v[232:235],v[84:87]
rd a[116:119],v248 offset:13312
mm v[88:91],a[120:123],v[232:235],v[88:91]
ex v211,v211
mm v[120:123],a[120:123],v[236:239],v[120:123]
rd a[120:123],v248 offset:14336
mm v[124:127],a[124:127],v[236:239],v[124:127]
ex v216,v216
mm v[92:95],a[124:127],v[232:235],v[92:95]
rd a[124:127],v248 offset:15360
v_or_b32 v248, s89, v255
v_smfmac_f32_16x16x64_bf16 a[196:199], a[208:211], v[232:239], v241
wt lgkmcnt(4)
v_smfmac_f32_16x16x64_bf16 a[192:195], a[208:211], v[224:231], v241
rd a[64:67],v248 offset:0
mm v[0:3],a[96:99],v[224:227],v[0:3]
ex v217,v217
mm v[32:35],a[96:99],v[228:231],v[32:35]
rd a[68:71],v248 offset:1024
cv v232,v208,v209
mm v[36:39],a[100:103],v[228:231],v[36:39]
ex v218,v218
mm v[4:7],a[100:103],v[224:227],v[4:7]
rd a[72:75],v248 offset:2048
cv v233,v210,v211
mm v[8:11],a[104:107],v[224:227],v[8:11]
ex v219,v219
mm v[40:43],a[104:107],v[228:231],v[40:43]
rd a[76:79],v248 offset:3072
cv v236,v216,v217
mm v[44:47],a[108:111],v[228:231],v[44:47]
ex v212,v212
mm v[12:15],a[108:111],v[224:227],v[12:15]
rd a[80:83],v248 offset:4096
wt lgkmcnt(5)
mm v[16:19],a[112:115],v[224:227],v[16:19]
ex v213,v213
mm v[48:51],a[112:115],v[228:231],v[48:51]
rd a[84:87],v248 offset:5120
cv v237,v218,v219
mm v[52:55],a[116:119],v[228:231],v[52:55]
ex v214,v214
mm v[20:23],a[116:119],v[224:227],v[20:23]
rd a[88:91],v248 offset:6144
cv v234,v212,v213
mm v[24:27],a[120:123],v[224:227],v[24:27]
ex v215,v215
mm v[56:59],a[120:123],v[228:231],v[56:59]
rd a[92:95],v248 offset:7168
cv v235,v214,v215
mm v[60:63],a[124:127],v[228:231],v[60:63]
ex v220,v220
mm v[28:31],a[124:127],v[224:227],v[28:31]
buffer_load_dword v250, v251, s[64:67], s20 offen sc1
wt lgkmcnt(4)
mm v[192:195],a[64:67],v[128:131],0
ex v221,v221
mm v[192:195],a[68:71],v[132:135],v[192:195]
cv v238,v220,v221
s_add_u32 s89, s89, 16384
mm v[192:195],a[72:75],v[136:139],v[192:195]
ex v222,v222
mm v[192:195],a[76:79],v[140:143],v[192:195]
ld v240,s[56:59],s17 offen lds
mm v[200:203],a[76:79],v[156:159],0
ex v223,v223
s_add_u32 s17, s17, 32768
mm v[200:203],a[72:75],v[152:155],v[200:203]
cv v239,v222,v223
s_add_u32 m0, m0, 1024
mm v[200:203],a[68:71],v[148:151],v[200:203]
ex v192,v192
mm v[200:203],a[64:67],v[144:147],v[200:203]
wt lgkmcnt(0)
s_and_b32 s89, s89, 0x1ffff
mm v[196:199],a[80:83],v[128:131],0
ex v193,v193
mm v[196:199],a[84:87],v[132:135],v[196:199]
cv v224,v192,v193
mm v[196:199],a[88:91],v[136:139],v[196:199]
ex v194,v194
mm v[196:199],a[92:95],v[140:143],v[196:199]
ld v240,s[56:59],s17 offen lds
mm v[204:207],a[92:95],v[156:159],0
ex v195,v195
s_add_u32 s17, s17, 4294942720
mm v[204:207],a[88:91],v[152:155],v[204:207]
cv v225,v194,v195
s_add_u32 m0, m0, 1152
mm v[204:207],a[84:87],v[148:151],v[204:207]
ex v200,v200
mm v[204:207],a[80:83],v[144:147],v[204:207]
ld v240,s[56:59],s17 offen lds
mm v[208:211],a[64:67],v[160:163],0
ex v201,v201
s_add_u32 s17, s17, 32768
mm v[208:211],a[68:71],v[164:167],v[208:211]
cv v228,v200,v201
s_add_u32 m0, m0, 1024
)ASM" R"ASM(mm v[208:211],a[72:75],v[168:171],v[208:211]
ex v202,v202
mm v[208:211],a[76:79],v[172:175],v[208:211]
ld v240,s[56:59],s17 offen lds
mm v[216:219],a[76:79],v[188:191],0
ex v203,v203
mm v[216:219],a[72:75],v[184:187],v[216:219]
cv v229,v202,v203
mm v[216:219],a[68:71],v[180:183],v[216:219]
ex v196,v196
mm v[216:219],a[64:67],v[176:179],v[216:219]
mm v[212:215],a[80:83],v[160:163],0
ex v197,v197
s_bfe_u32 s17, s4, 262148
mm v[212:215],a[84:87],v[164:167],v[212:215]
cv v226,v196,v197
s_xor_b32 s17, s17, s72
s_lshl_b32 s17, s17, 18
mm v[212:215],a[88:91],v[168:171],v[212:215]
ex v198,v198
s_add_u32 m0, s83, 50688
mm v[212:215],a[92:95],v[172:175],v[212:215]
mm v[220:223],a[92:95],v[188:191],0
ex v199,v199
mm v[220:223],a[88:91],v[184:187],v[220:223]
cv v227,v198,v199
wt vmcnt(4)
v_cmp_eq_u32 s[92:93], v250, v249
s_mov_b32 s94, 16384
mm v[220:223],a[84:87],v[180:183],v[220:223]
ex v204,v204
mm v[220:223],a[80:83],v[176:179],v[220:223]
s_nop 0
s_barrier
mm v[64:67],a[96:99],v[232:235],v[64:67]
ex v205,v205
mm v[96:99],a[96:99],v[236:239],v[96:99]
rd a[96:99],v248 offset:8192
cv v230,v204,v205
s_and_b32 s92, s92, 3
s_andn2_b32 s92, s92, s23
s_cmp_eq_u32 s92, 3
s_cselect_b32 s70, 17408, 0
s_cselect_b32 s84, 3, s23
mm v[100:103],a[100:103],v[236:239],v[100:103]
ex v206,v206
mm v[68:71],a[100:103],v[232:235],v[68:71]
rd a[100:103],v248 offset:9216
ld a[220:223],v255,s[68:71],s94 offen sc1
mm v[72:75],a[104:107],v[232:235],v[72:75]
ex v207,v207
mm v[104:107],a[104:107],v[236:239],v[104:107]
rd a[104:107],v248 offset:10240
cv v231,v206,v207
mm v[108:111],a[108:111],v[236:239],v[108:111]
ex v208,v208
mm v[76:79],a[108:111],v[232:235],v[76:79]
rd a[108:111],v248 offset:11264
ld a[0:3],v255,s[68:71],0 offen offset:0 sc1
mm v[80:83],a[112:115],v[232:235],v[80:83]
ex v209,v209
mm v[112:115],a[112:115],v[236:239],v[112:115]
rd a[112:115],v248 offset:12288
mm v[116:119],a[116:119],v[236:239],v[116:119]
ex v210,v210
mm v[84:87],a[116:119],v[232:235],v[84:87]
rd a[116:119],v248 offset:13312
ld a[4:7],v255,s[68:71],0 offen offset:1024 sc1
mm v[88:91],a[120:123],v[232:235],v[88:91]
ex v211,v211
mm v[120:123],a[120:123],v[236:239],v[120:123]
rd a[120:123],v248 offset:14336
mm v[124:127],a[124:127],v[236:239],v[124:127]
ex v216,v216
mm v[92:95],a[124:127],v[232:235],v[92:95]
rd a[124:127],v248 offset:15360
v_or_b32 v248, s89, v255
buffer_load_dword v250, v251, s[64:67], s20 offen sc1
v_smfmac_f32_16x16x64_bf16 a[196:199], a[208:211], v[232:239], v241
wt lgkmcnt(4)
v_smfmac_f32_16x16x64_bf16 a[192:195], a[208:211], v[224:231], v241
rd a[64:67],v248 offset:0
mm v[0:3],a[96:99],v[224:227],v[0:3]
ex v217,v217
mm v[32:35],a[96:99],v[228:231],v[32:35]
rd a[68:71],v248 offset:1024
cv v232,v208,v209
ld a[8:11],v255,s[68:71],0 offen offset:2048 sc1
mm v[36:39],a[100:103],v[228:231],v[36:39]
ex v218,v218
mm v[4:7],a[100:103],v[224:227],v[4:7]
rd a[72:75],v248 offset:2048
cv v233,v210,v211
mm v[8:11],a[104:107],v[224:227],v[8:11]
ex v219,v219
mm v[40:43],a[104:107],v[228:231],v[40:43]
rd a[76:79],v248 offset:3072
cv v236,v216,v217
ld a[12:15],v255,s[68:71],0 offen offset:3072 sc1
mm v[44:47],a[108:111],v[228:231],v[44:47]
)ASM" R"ASM(ex v212,v212
mm v[12:15],a[108:111],v[224:227],v[12:15]
rd a[80:83],v248 offset:4096
wt lgkmcnt(5)
mm v[16:19],a[112:115],v[224:227],v[16:19]
ex v213,v213
mm v[48:51],a[112:115],v[228:231],v[48:51]
rd a[84:87],v248 offset:5120
cv v237,v218,v219
ld a[16:19],v255,s[68:71],s95 offen offset:0 sc1
mm v[52:55],a[116:119],v[228:231],v[52:55]
ex v214,v214
mm v[20:23],a[116:119],v[224:227],v[20:23]
rd a[88:91],v248 offset:6144
cv v234,v212,v213
mm v[24:27],a[120:123],v[224:227],v[24:27]
ex v215,v215
mm v[56:59],a[120:123],v[228:231],v[56:59]
rd a[92:95],v248 offset:7168
cv v235,v214,v215
ld a[20:23],v255,s[68:71],s95 offen offset:1024 sc1
mm v[60:63],a[124:127],v[228:231],v[60:63]
ex v220,v220
mm v[28:31],a[124:127],v[224:227],v[28:31]
ld a[24:27],v255,s[68:71],s95 offen offset:2048 sc1
wt lgkmcnt(4)
mm v[192:195],a[64:67],v[128:131],0
ex v221,v221
mm v[192:195],a[68:71],v[132:135],v[192:195]
cv v238,v220,v221
mm v[192:195],a[72:75],v[136:139],v[192:195]
ex v222,v222
mm v[192:195],a[76:79],v[140:143],v[192:195]
ld v240,s[56:59],s17 offen lds
mm v[200:203],a[76:79],v[156:159],0
ex v223,v223
s_add_u32 s17, s17, 32768
mm v[200:203],a[72:75],v[152:155],v[200:203]
cv v239,v222,v223
s_add_u32 m0, m0, 1024
mm v[200:203],a[68:71],v[148:151],v[200:203]
ex v192,v192
mm v[200:203],a[64:67],v[144:147],v[200:203]
wt lgkmcnt(0)
mm v[196:199],a[80:83],v[128:131],0
ex v193,v193
mm v[196:199],a[84:87],v[132:135],v[196:199]
cv v224,v192,v193
wt vmcnt(6)
v_cmp_eq_u32 s[92:93], v250, v249
mm v[196:199],a[88:91],v[136:139],v[196:199]
ex v194,v194
mm v[196:199],a[92:95],v[140:143],v[196:199]
ld v240,s[56:59],s17 offen lds
mm v[204:207],a[92:95],v[156:159],0
ex v195,v195
s_add_u32 s17, s17, 4294942720
mm v[204:207],a[88:91],v[152:155],v[204:207]
cv v225,v194,v195
s_add_u32 m0, m0, 1152
s_and_b32 s92, s92, 3
s_sub_u32 s92, s92, s84
s_cmp_eq_u32 s92, 3
s_cbranch_scc1 .La5_LA_PS0
.La5_LA_PS1:
mm v[204:207],a[84:87],v[148:151],v[204:207]
ex v200,v200
mm v[204:207],a[80:83],v[144:147],v[204:207]
ld v240,s[56:59],s17 offen lds
mm v[208:211],a[64:67],v[160:163],0
ex v201,v201
s_add_u32 s17, s17, 32768
mm v[208:211],a[68:71],v[164:167],v[208:211]
cv v228,v200,v201
s_add_u32 m0, m0, 1024
ld a[28:31],v255,s[68:71],s95 offen offset:3072 sc1
mm v[208:211],a[72:75],v[168:171],v[208:211]
ex v202,v202
mm v[208:211],a[76:79],v[172:175],v[208:211]
ld v240,s[56:59],s17 offen lds
mm v[216:219],a[76:79],v[188:191],0
ex v203,v203
mm v[216:219],a[72:75],v[184:187],v[216:219]
cv v229,v202,v203
ld a[32:35],v255,s[68:71],s96 offen offset:0 sc1
mm v[216:219],a[68:71],v[180:183],v[216:219]
ex v196,v196
mm v[216:219],a[64:67],v[176:179],v[216:219]
mm v[212:215],a[80:83],v[160:163],0
ex v197,v197
mm v[212:215],a[84:87],v[164:167],v[212:215]
cv v226,v196,v197
buffer_load_dword v250, v251, s[64:67], s20 offen sc1
mm v[212:215],a[88:91],v[168:171],v[212:215]
ex v198,v198
mm v[212:215],a[92:95],v[172:175],v[212:215]
ld a[36:39],v255,s[68:71],s96 offen offset:1024 sc1
mm v[220:223],a[92:95],v[188:191],0
ex v199,v199
mm v[220:223],a[88:91],v[184:187],v[220:223]
)ASM" R"ASM(cv v227,v198,v199
mm v[220:223],a[84:87],v[180:183],v[220:223]
ex v204,v204
mm v[220:223],a[80:83],v[176:179],v[220:223]
ld a[40:43],v255,s[68:71],s96 offen offset:2048 sc1
mm v[64:67],a[96:99],v[232:235],v[64:67]
ex v205,v205
mm v[96:99],a[96:99],v[236:239],v[96:99]
rd a[96:99],v248 offset:8192
cv v230,v204,v205
mm v[100:103],a[100:103],v[236:239],v[100:103]
ex v206,v206
mm v[68:71],a[100:103],v[232:235],v[68:71]
rd a[100:103],v248 offset:9216
ld a[44:47],v255,s[68:71],s96 offen offset:3072 sc1
mm v[72:75],a[104:107],v[232:235],v[72:75]
ex v207,v207
mm v[104:107],a[104:107],v[236:239],v[104:107]
rd a[104:107],v248 offset:10240
cv v231,v206,v207
mm v[108:111],a[108:111],v[236:239],v[108:111]
ex v208,v208
mm v[76:79],a[108:111],v[232:235],v[76:79]
rd a[108:111],v248 offset:11264
ld a[48:51],v255,s[68:71],s97 offen offset:0 sc1
mm v[80:83],a[112:115],v[232:235],v[80:83]
ex v209,v209
mm v[112:115],a[112:115],v[236:239],v[112:115]
rd a[112:115],v248 offset:12288
mm v[116:119],a[116:119],v[236:239],v[116:119]
ex v210,v210
mm v[84:87],a[116:119],v[232:235],v[84:87]
rd a[116:119],v248 offset:13312
ld a[52:55],v255,s[68:71],s97 offen offset:1024 sc1
mm v[88:91],a[120:123],v[232:235],v[88:91]
ex v211,v211
mm v[120:123],a[120:123],v[236:239],v[120:123]
rd a[120:123],v248 offset:14336
mm v[124:127],a[124:127],v[236:239],v[124:127]
ex v216,v216
mm v[92:95],a[124:127],v[232:235],v[92:95]
rd a[124:127],v248 offset:15360
ld a[56:59],v255,s[68:71],s97 offen offset:2048 sc1
v_smfmac_f32_16x16x64_bf16 a[196:199], a[208:211], v[232:239], v241
wt lgkmcnt(4)
v_smfmac_f32_16x16x64_bf16 a[192:195], a[208:211], v[224:231], v241
ld a[60:63],v255,s[68:71],s97 offen offset:3072 sc1
mm v[0:3],a[96:99],v[224:227],v[0:3]
ex v217,v217
mm v[32:35],a[96:99],v[228:231],v[32:35]
cv v232,v208,v209
mm v[36:39],a[100:103],v[228:231],v[36:39]
ex v218,v218
mm v[4:7],a[100:103],v[224:227],v[4:7]
cv v233,v210,v211
mm v[8:11],a[104:107],v[224:227],v[8:11]
ex v219,v219
mm v[40:43],a[104:107],v[228:231],v[40:43]
cv v236,v216,v217
mm v[44:47],a[108:111],v[228:231],v[44:47]
ex v212,v212
wt vmcnt(7)
v_cmp_eq_u32 s[92:93], v250, v249
mm v[12:15],a[108:111],v[224:227],v[12:15]
wt lgkmcnt(0)
mm v[16:19],a[112:115],v[224:227],v[16:19]
ex v213,v213
mm v[48:51],a[112:115],v[228:231],v[48:51]
cv v237,v218,v219
s_and_b32 s92, s92, 3
s_sub_u32 s92, s92, s84
mm v[52:55],a[116:119],v[228:231],v[52:55]
ex v214,v214
mm v[20:23],a[116:119],v[224:227],v[20:23]
cv v234,v212,v213
mm v[24:27],a[120:123],v[224:227],v[24:27]
ex v215,v215
mm v[56:59],a[120:123],v[228:231],v[56:59]
cv v235,v214,v215
mm v[60:63],a[124:127],v[228:231],v[60:63]
ex v220,v220
mm v[28:31],a[124:127],v[224:227],v[28:31]
s_cmp_eq_u32 s92, 3
s_cbranch_scc1 .La5_LA_PS2
.La5_LA_PS3:
s_cmp_lg_u32 s84, 3
s_cbranch_scc1 .La5_LA_PSPIN
; ---- owner: partner-s part added, l checked, O / l leaves; first steps of the O projection
.La5_LA_DC1:
wt vmcnt(8)
mm v[0:3],a[214:217],a[0:3],v[0:3]
ar v250,a192
ar v251,a193
v_add_f32 v250, v250, v251
ar v251,a220
v_add_f32 v250, v250, v251
ar v249,a194
mm v[4:7],a[212:215],a[0:3],v[4:7]
ar v251,a195
v_add_f32 v249, v249, v251
)ASM" R"ASM(ar v251,a221
v_add_f32 v249, v249, v251
v_subrev_u32 v251, 562036736, v250
mm v[8:11],a[214:217],a[4:7],v[8:11]
v_rcp_f32 v250, v250
v_cmp_le_u32 vcc, 1006632960, v251
v_subrev_u32 v251, 562036736, v249
s_mov_b64 s[92:93], vcc
v_cmp_le_u32 vcc, 1006632960, v251
mm v[12:15],a[212:215],a[4:7],v[12:15]
v_rcp_f32 v249, v249
mm v[16:19],a[214:217],a[8:11],v[16:19]
s_or_b64 vcc, vcc, s[92:93]
s_cbranch_scc1 .La5_LA_SC3
.La5_LA_SR3:
ex v221,v221
ex v222,v222
mm v[20:23],a[212:215],a[8:11],v[20:23]
ex v223,v223
cv v238,v220,v221
v_mul_f32 v0, v0, v250
v_mul_f32 v1, v1, v250
v_mul_f32 v2, v2, v250
mm v[24:27],a[214:217],a[12:15],v[24:27]
v_mul_f32 v3, v3, v250
v_mul_f32 v4, v4, v250
v_mul_f32 v5, v5, v250
v_mul_f32 v6, v6, v250
v_mul_f32 v7, v7, v250
cv v128,v0,v1
mm v[28:31],a[212:215],a[12:15],v[28:31]
cv v129,v2,v3
cv v130,v4,v5
cv v131,v6,v7
.La5_LW5_3:
s_cbranch_execz .La5_LW5_R0
st v[128:131],v244,s[44:47],s91 offen offset:0 sc1
cv v239,v222,v223
mm v[32:35],a[214:217],a[16:19],v[32:35]
v_mul_f32 v8, v8, v250
v_mul_f32 v9, v9, v250
v_mul_f32 v10, v10, v250
v_mul_f32 v11, v11, v250
v_mul_f32 v12, v12, v250
v_mul_f32 v13, v13, v250
mm v[36:39],a[212:215],a[16:19],v[36:39]
v_mul_f32 v14, v14, v250
v_mul_f32 v15, v15, v250
cv v132,v8,v9
cv v133,v10,v11
cv v134,v12,v13
cv v135,v14,v15
mm v[40:43],a[214:217],a[20:23],v[40:43]
st v[132:135],v244,s[44:47],s91 offen offset:1024 sc1
v_mul_f32 v16, v16, v250
v_mul_f32 v17, v17, v250
v_mul_f32 v18, v18, v250
v_mul_f32 v19, v19, v250
mm v[44:47],a[212:215],a[20:23],v[44:47]
v_mul_f32 v20, v20, v250
v_mul_f32 v21, v21, v250
v_mul_f32 v22, v22, v250
v_mul_f32 v23, v23, v250
cv v136,v16,v17
cv v137,v18,v19
mm v[48:51],a[214:217],a[24:27],v[48:51]
cv v138,v20,v21
cv v139,v22,v23
st v[136:139],v244,s[44:47],s91 offen offset:2048 sc1
v_mul_f32 v24, v24, v250
v_mul_f32 v25, v25, v250
mm v[52:55],a[212:215],a[24:27],v[52:55]
v_mul_f32 v26, v26, v250
v_mul_f32 v27, v27, v250
v_mul_f32 v28, v28, v250
v_mul_f32 v29, v29, v250
v_mul_f32 v30, v30, v250
v_mul_f32 v31, v31, v250
mm v[56:59],a[214:217],a[28:31],v[56:59]
cv v140,v24,v25
cv v141,v26,v27
cv v142,v28,v29
cv v143,v30,v31
st v[140:143],v244,s[44:47],s91 offen offset:3072 sc1
mm v[60:63],a[212:215],a[28:31],v[60:63]
v_mul_f32 v32, v32, v249
v_mul_f32 v33, v33, v249
v_mul_f32 v34, v34, v249
v_mul_f32 v35, v35, v249
v_mul_f32 v36, v36, v249
v_mul_f32 v37, v37, v249
v_smfmac_f32_16x16x64_bf16 a[196:199], a[208:211], v[232:239], v241
v_mul_f32 v38, v38, v249
v_mul_f32 v39, v39, v249
cv v144,v32,v33
cv v145,v34,v35
cv v146,v36,v37
mm v[64:67],a[96:99],v[232:235],v[64:67]
cv v147,v38,v39
st v[144:147],v245,s[44:47],s91 offen offset:0 sc1
)ASM" R"ASM(v_mul_f32 v40, v40, v249
v_mul_f32 v41, v41, v249
v_mul_f32 v42, v42, v249
mm v[96:99],a[96:99],v[236:239],v[96:99]
v_mul_f32 v43, v43, v249
v_mul_f32 v44, v44, v249
v_mul_f32 v45, v45, v249
v_mul_f32 v46, v46, v249
v_mul_f32 v47, v47, v249
cv v148,v40,v41
mm v[100:103],a[100:103],v[236:239],v[100:103]
cv v149,v42,v43
cv v150,v44,v45
cv v151,v46,v47
st v[148:151],v245,s[44:47],s91 offen offset:1024 sc1
v_mul_f32 v48, v48, v249
mm v[68:71],a[100:103],v[232:235],v[68:71]
v_mul_f32 v49, v49, v249
v_mul_f32 v50, v50, v249
v_mul_f32 v51, v51, v249
v_mul_f32 v52, v52, v249
v_mul_f32 v53, v53, v249
v_mul_f32 v54, v54, v249
mm v[72:75],a[104:107],v[232:235],v[72:75]
v_mul_f32 v55, v55, v249
cv v152,v48,v49
cv v153,v50,v51
cv v154,v52,v53
cv v155,v54,v55
st v[152:155],v245,s[44:47],s91 offen offset:2048 sc1
mm v[104:107],a[104:107],v[236:239],v[104:107]
v_mul_f32 v56, v56, v249
v_mul_f32 v57, v57, v249
v_mul_f32 v58, v58, v249
v_mul_f32 v59, v59, v249
v_mul_f32 v60, v60, v249
v_mul_f32 v61, v61, v249
mm v[108:111],a[108:111],v[236:239],v[108:111]
v_mul_f32 v62, v62, v249
v_mul_f32 v63, v63, v249
cv v156,v56,v57
cv v157,v58,v59
cv v158,v60,v61
cv v159,v62,v63
mm v[76:79],a[108:111],v[232:235],v[76:79]
st v[156:159],v245,s[44:47],s91 offen offset:3072 sc1
ar v240,a224
v_xor_b32 v241, 32, v240
v_xor_b32 v242, 64, v240
v_xor_b32 v243, 96, v240
mm v[80:83],a[112:115],v[232:235],v[80:83]
mm v[112:115],a[112:115],v[236:239],v[112:115]
mm v[116:119],a[116:119],v[236:239],v[116:119]
mm v[84:87],a[116:119],v[232:235],v[84:87]
mm v[88:91],a[120:123],v[232:235],v[88:91]
mm v[120:123],a[120:123],v[236:239],v[120:123]
mm v[124:127],a[124:127],v[236:239],v[124:127]
mm v[92:95],a[124:127],v[232:235],v[92:95]
s_nop 0
s_barrier
wt vmcnt(8)
mm v[64:67],a[214:217],a[32:35],v[64:67]
rt v[192:193],v240 offset:16896
rt v[194:195],v240 offset:17920
mm v[68:71],a[212:215],a[32:35],v[68:71]
rt v[196:197],v240 offset:25344
rt v[198:199],v240 offset:26368
mm v[72:75],a[214:217],a[36:39],v[72:75]
rt v[200:201],v241 offset:16896
rt v[202:203],v241 offset:17920
ar v250,a196
ar v251,a197
v_add_f32 v250, v250, v251
ar v251,a222
mm v[76:79],a[212:215],a[36:39],v[76:79]
rt v[204:205],v241 offset:25344
rt v[206:207],v241 offset:26368
v_add_f32 v250, v250, v251
ar v249,a198
ar v251,a199
v_add_f32 v249, v249, v251
mm v[80:83],a[214:217],a[40:43],v[80:83]
rt v[208:209],v242 offset:16896
rt v[210:211],v242 offset:17920
ar v251,a223
v_add_f32 v249, v249, v251
v_subrev_u32 v251, 562036736, v250
mm v[84:87],a[212:215],a[40:43],v[84:87]
rt v[212:213],v242 offset:25344
rt v[214:215],v242 offset:26368
v_rcp_f32 v250, v250
v_cmp_le_u32 vcc, 1006632960, v251
v_subrev_u32 v251, 562036736, v249
mm v[88:91],a[214:217],a[44:47],v[88:91]
rt v[216:217],v243 offset:16896
rt v[218:219],v243 offset:17920
s_mov_b64 s[92:93], vcc
v_cmp_le_u32 vcc, 1006632960, v251
v_rcp_f32 v249, v249
mm v[92:95],a[212:215],a[44:47],v[92:95]
)ASM" R"ASM(rt v[220:221],v243 offset:25344
rt v[222:223],v243 offset:26368
s_or_b64 vcc, vcc, s[92:93]
s_cbranch_scc1 .La5_LA_SC4
.La5_LA_SR4:
v_mul_f32 v64, v64, v250
v_mul_f32 v65, v65, v250
mm v[96:99],a[214:217],a[48:51],v[96:99]
v_mul_f32 v66, v66, v250
v_mul_f32 v67, v67, v250
v_mul_f32 v68, v68, v250
v_mul_f32 v69, v69, v250
v_mul_f32 v70, v70, v250
v_mul_f32 v71, v71, v250
mm v[100:103],a[212:215],a[48:51],v[100:103]
cv v160,v64,v65
cv v161,v66,v67
cv v162,v68,v69
cv v163,v70,v71
st v[160:163],v246,s[44:47],s91 offen offset:0 sc1
mm v[104:107],a[214:217],a[52:55],v[104:107]
v_mul_f32 v72, v72, v250
v_mul_f32 v73, v73, v250
v_mul_f32 v74, v74, v250
v_mul_f32 v75, v75, v250
v_mul_f32 v76, v76, v250
v_mul_f32 v77, v77, v250
mm v[108:111],a[212:215],a[52:55],v[108:111]
v_mul_f32 v78, v78, v250
v_mul_f32 v79, v79, v250
cv v164,v72,v73
cv v165,v74,v75
cv v166,v76,v77
cv v167,v78,v79
mm v[112:115],a[214:217],a[56:59],v[112:115]
st v[164:167],v246,s[44:47],s91 offen offset:1024 sc1
v_mul_f32 v80, v80, v250
v_mul_f32 v81, v81, v250
v_mul_f32 v82, v82, v250
v_mul_f32 v83, v83, v250
mm v[116:119],a[212:215],a[56:59],v[116:119]
v_mul_f32 v84, v84, v250
v_mul_f32 v85, v85, v250
v_mul_f32 v86, v86, v250
v_mul_f32 v87, v87, v250
cv v168,v80,v81
cv v169,v82,v83
mm v[120:123],a[214:217],a[60:63],v[120:123]
cv v170,v84,v85
cv v171,v86,v87
st v[168:171],v246,s[44:47],s91 offen offset:2048 sc1
v_mul_f32 v88, v88, v250
v_mul_f32 v89, v89, v250
mm v[124:127],a[212:215],a[60:63],v[124:127]
wt lgkmcnt(12)
v_mul_f32 v90, v90, v250
v_mul_f32 v91, v91, v250
v_mul_f32 v92, v92, v250
v_mul_f32 v93, v93, v250
v_mul_f32 v94, v94, v250
mm v[0:3],v[192:195],v[128:131],0
v_mul_f32 v95, v95, v250
cv v172,v88,v89
cv v173,v90,v91
cv v174,v92,v93
cv v175,v94,v95
st v[172:175],v246,s[44:47],s91 offen offset:3072 sc1
mm v[0:3],v[196:199],v[132:135],v[0:3]
v_mul_f32 v96, v96, v249
v_mul_f32 v97, v97, v249
v_mul_f32 v98, v98, v249
v_mul_f32 v99, v99, v249
v_mul_f32 v100, v100, v249
v_mul_f32 v101, v101, v249
mm v[32:35],v[196:199],v[148:151],0
wt lgkmcnt(8)
v_mul_f32 v102, v102, v249
v_mul_f32 v103, v103, v249
cv v176,v96,v97
cv v177,v98,v99
cv v178,v100,v101
mm v[32:35],v[192:195],v[144:147],v[32:35]
rt v[196:197],v240 offset:17024
rt v[198:199],v240 offset:18048
cv v179,v102,v103
st v[176:179],v247,s[44:47],s91 offen offset:0 sc1
v_mul_f32 v104, v104, v249
mm v[36:39],v[200:203],v[144:147],0
v_mul_f32 v105, v105, v249
v_mul_f32 v106, v106, v249
v_mul_f32 v107, v107, v249
v_mul_f32 v108, v108, v249
v_mul_f32 v109, v109, v249
v_mul_f32 v110, v110, v249
mm v[36:39],v[204:207],v[148:151],v[36:39]
rt v[192:193],v240 offset:25472
rt v[194:195],v240 offset:26496
v_mul_f32 v111, v111, v249
cv v180,v104,v105
cv v181,v106,v107
)ASM" R"ASM(cv v182,v108,v109
mm v[4:7],v[204:207],v[132:135],0
wt lgkmcnt(4)
cv v183,v110,v111
st v[180:183],v247,s[44:47],s91 offen offset:1024 sc1
v_mul_f32 v112, v112, v249
v_mul_f32 v113, v113, v249
mm v[4:7],v[200:203],v[128:131],v[4:7]
rt v[204:205],v241 offset:17024
rt v[206:207],v241 offset:18048
v_mul_f32 v114, v114, v249
v_mul_f32 v115, v115, v249
v_mul_f32 v116, v116, v249
v_mul_f32 v117, v117, v249
mm v[8:11],v[208:211],v[128:131],0
v_mul_f32 v118, v118, v249
v_mul_f32 v119, v119, v249
cv v184,v112,v113
cv v185,v114,v115
cv v186,v116,v117
cv v187,v118,v119
mm v[8:11],v[212:215],v[132:135],v[8:11]
rt v[200:201],v241 offset:25472
rt v[202:203],v241 offset:26496
st v[184:187],v247,s[44:47],s91 offen offset:2048 sc1
v_mul_f32 v120, v120, v249
v_mul_f32 v121, v121, v249
mm v[40:43],v[212:215],v[148:151],0
v_mul_f32 v122, v122, v249
v_mul_f32 v123, v123, v249
v_mul_f32 v124, v124, v249
v_mul_f32 v125, v125, v249
v_mul_f32 v126, v126, v249
v_mul_f32 v127, v127, v249
mm v[40:43],v[208:211],v[144:147],v[40:43]
rt v[212:213],v242 offset:17024
rt v[214:215],v242 offset:18048
cv v188,v120,v121
cv v189,v122,v123
cv v190,v124,v125
cv v191,v126,v127
mm v[44:47],v[216:219],v[144:147],0
st v[188:191],v247,s[44:47],s91 offen offset:3072 sc1
mm v[44:47],v[220:223],v[148:151],v[44:47]
rt v[208:209],v242 offset:25472
rt v[210:211],v242 offset:26496
mm v[12:15],v[220:223],v[132:135],0
wt lgkmcnt(4)
mm v[12:15],v[216:219],v[128:131],v[12:15]
rt v[220:221],v243 offset:17024
rt v[222:223],v243 offset:18048
mm v[16:19],v[196:199],v[128:131],0
ar v224,a228
v_add_u32 v225, 0x8000, v224
v_add_u32 v226, 0x2000, v224
v_add_u32 v227, 0xa000, v224
mm v[16:19],v[192:195],v[132:135],v[16:19]
rt v[216:217],v243 offset:25472
rt v[218:219],v243 offset:26496
ar v248,a225
ar v228,a229
mm v[48:51],v[192:195],v[148:151],0
ar v229,a230
mm v[48:51],v[196:199],v[144:147],v[48:51]
rt v[192:193],v240 offset:33792
rt v[194:195],v240 offset:34816
mm v[52:55],v[204:207],v[144:147],0
mm v[52:55],v[200:203],v[148:151],v[52:55]
rt v[196:197],v240 offset:42240
rt v[198:199],v240 offset:43264
mm v[20:23],v[200:203],v[132:135],0
wt lgkmcnt(4)
mm v[20:23],v[204:207],v[128:131],v[20:23]
rt v[200:201],v241 offset:33792
rt v[202:203],v241 offset:34816
mm v[24:27],v[212:215],v[128:131],0
mm v[24:27],v[208:211],v[132:135],v[24:27]
rt v[204:205],v241 offset:42240
rt v[206:207],v241 offset:43264
mm v[56:59],v[208:211],v[148:151],0
mm v[56:59],v[212:215],v[144:147],v[56:59]
rt v[208:209],v242 offset:33792
rt v[210:211],v242 offset:34816
mm v[60:63],v[220:223],v[144:147],0
mm v[60:63],v[216:219],v[148:151],v[60:63]
rt v[212:213],v242 offset:42240
rt v[214:215],v242 offset:43264
mm v[28:31],v[216:219],v[132:135],0
wt lgkmcnt(4)
mm v[28:31],v[220:223],v[128:131],v[28:31]
rt v[216:217],v243 offset:33792
rt v[218:219],v243 offset:34816
mm v[0:3],v[192:195],v[136:139],v[0:3]
ar v249,a231
s_bitcmp1_b32 2863311530, s2
s_cbranch_scc1 .La5_LO_VC1
s_getpc_b64 s[24:25]
.La5_LW3_P1:
s_add_u32 s24, s24, .La5_LW3_R1-.La5_LW3_P1
s_addc_u32 s25, s25, 0
)ASM" R"ASM(s_mov_b64 exec, 0
s_cmp_eq_u32 s3, 0
s_cbranch_scc1 .La5_LW3_0
s_cmp_eq_u32 s3, 1
s_cbranch_scc1 .La5_LW3_1
s_cmp_eq_u32 s3, 2
s_cbranch_scc1 .La5_LW3_2
s_branch .La5_LW3_3
.La5_LW3_R1:
s_mov_b64 exec, -1
.La5_LO_VE1:
wt vmcnt(0)
buffer_store_dwordx2 v[252:253], v249, s[64:67], s6 offen offset:0 sc1
mm v[0:3],v[196:199],v[140:143],v[0:3]
rt v[220:221],v243 offset:42240
rt v[222:223],v243 offset:43264
mm v[32:35],v[196:199],v[156:159],v[32:35]
mm v[32:35],v[192:195],v[152:155],v[32:35]
rt v[196:197],v240 offset:33920
rt v[198:199],v240 offset:34944
mm v[36:39],v[200:203],v[152:155],v[36:39]
mm v[36:39],v[204:207],v[156:159],v[36:39]
rt v[192:193],v240 offset:42368
rt v[194:195],v240 offset:43392
mm v[4:7],v[204:207],v[140:143],v[4:7]
wt lgkmcnt(4)
mm v[4:7],v[200:203],v[136:139],v[4:7]
rt v[204:205],v241 offset:33920
rt v[206:207],v241 offset:34944
mm v[8:11],v[208:211],v[136:139],v[8:11]
mm v[8:11],v[212:215],v[140:143],v[8:11]
rt v[200:201],v241 offset:42368
rt v[202:203],v241 offset:43392
mm v[40:43],v[212:215],v[156:159],v[40:43]
mm v[40:43],v[208:211],v[152:155],v[40:43]
rt v[212:213],v242 offset:33920
rt v[214:215],v242 offset:34944
mm v[44:47],v[216:219],v[152:155],v[44:47]
mm v[44:47],v[220:223],v[156:159],v[44:47]
rt v[208:209],v242 offset:42368
rt v[210:211],v242 offset:43392
mm v[12:15],v[220:223],v[140:143],v[12:15]
wt lgkmcnt(4)
mm v[12:15],v[216:219],v[136:139],v[12:15]
rt v[220:221],v243 offset:33920
rt v[222:223],v243 offset:34944
mm v[16:19],v[196:199],v[136:139],v[16:19]
mm v[16:19],v[192:195],v[140:143],v[16:19]
rt v[216:217],v243 offset:42368
rt v[218:219],v243 offset:43392
mm v[48:51],v[192:195],v[156:159],v[48:51]
mm v[48:51],v[196:199],v[152:155],v[48:51]
rt v[192:193],v240 offset:16896
rt v[194:195],v240 offset:17920
mm v[52:55],v[204:207],v[152:155],v[52:55]
mm v[52:55],v[200:203],v[156:159],v[52:55]
rt v[196:197],v240 offset:25344
rt v[198:199],v240 offset:26368
mm v[20:23],v[200:203],v[140:143],v[20:23]
wt lgkmcnt(4)
mm v[20:23],v[204:207],v[136:139],v[20:23]
rt v[200:201],v241 offset:16896
rt v[202:203],v241 offset:17920
mm v[24:27],v[212:215],v[136:139],v[24:27]
mm v[24:27],v[208:211],v[140:143],v[24:27]
rt v[204:205],v241 offset:25344
rt v[206:207],v241 offset:26368
mm v[56:59],v[208:211],v[156:159],v[56:59]
mm v[56:59],v[212:215],v[152:155],v[56:59]
rt v[208:209],v242 offset:16896
rt v[210:211],v242 offset:17920
mm v[60:63],v[220:223],v[152:155],v[60:63]
mm v[60:63],v[216:219],v[156:159],v[60:63]
rt v[212:213],v242 offset:25344
rt v[214:215],v242 offset:26368
mm v[28:31],v[216:219],v[140:143],v[28:31]
wt lgkmcnt(4)
mm v[28:31],v[220:223],v[136:139],v[28:31]
rt v[216:217],v243 offset:16896
rt v[218:219],v243 offset:17920
s_cmp_lg_u32 s30, 0
s_nop 0
s_nop 0
s_cbranch_scc1 .La5_LO_W
; ---- O projection: 256 x 128 tile over eight heads
.La5_LO_START:
.La5_LW6_6:
s_cbranch_execz .La5_LW6_R0
s_bitcmp1_b32 2863311530, s2
s_cbranch_scc1 .La5_LO_SC
; ---- O projection, even XCDs: eight and a half heads
.La5_LO_SB:
mm v[64:67],v[192:195],v[160:163],0
buffer_load_dwordx2 v[250:251], v248, s[64:67], s5 offen sc1
mm v[64:67],v[196:199],v[164:167],v[64:67]
rt v[220:221],v243 offset:25344
rt v[222:223],v243 offset:26368
mm v[96:99],v[196:199],v[180:183],0
s_add_u32 m0, s83, 0
s_bfe_u32 s80, s4, 262148
)ASM" R"ASM(mm v[96:99],v[192:195],v[176:179],v[96:99]
rt v[196:197],v240 offset:17024
rt v[198:199],v240 offset:18048
s_xor_b32 s80, s80, s72
s_lshl_b32 s80, s80, 18
s_add_u32 s80, s80, 131072
mm v[100:103],v[200:203],v[176:179],0
ld v224,s[56:59],s80 offen lds
mm v[100:103],v[204:207],v[180:183],v[100:103]
rt v[192:193],v240 offset:25472
rt v[194:195],v240 offset:26496
mm v[68:71],v[204:207],v[164:167],0
s_add_u32 m0, s83, 1024
wt lgkmcnt(4)
mm v[68:71],v[200:203],v[160:163],v[68:71]
rt v[204:205],v241 offset:17024
rt v[206:207],v241 offset:18048
mm v[72:75],v[208:211],v[160:163],0
ld v225,s[56:59],s80 offen lds
mm v[72:75],v[212:215],v[164:167],v[72:75]
rt v[200:201],v241 offset:25472
rt v[202:203],v241 offset:26496
mm v[104:107],v[212:215],v[180:183],0
s_add_u32 m0, s83, 2176
s_bfe_u32 s53, s4, 262148
mm v[104:107],v[208:211],v[176:179],v[104:107]
rt v[212:213],v242 offset:17024
rt v[214:215],v242 offset:18048
mm v[108:111],v[216:219],v[176:179],0
ld v226,s[56:59],s80 offen lds
mm v[108:111],v[220:223],v[180:183],v[108:111]
rt v[208:209],v242 offset:25472
rt v[210:211],v242 offset:26496
mm v[76:79],v[220:223],v[164:167],0
s_add_u32 m0, s83, 3200
wt lgkmcnt(4)
mm v[76:79],v[216:219],v[160:163],v[76:79]
rt v[220:221],v243 offset:17024
rt v[222:223],v243 offset:18048
mm v[80:83],v[196:199],v[160:163],0
ld v227,s[56:59],s80 offen lds
mm v[80:83],v[192:195],v[164:167],v[80:83]
rt v[216:217],v243 offset:25472
rt v[218:219],v243 offset:26496
mm v[112:115],v[192:195],v[180:183],0
s_add_u32 m0, s83, 118272
s_bfe_u32 s80, s4, 262152
mm v[112:115],v[196:199],v[176:179],v[112:115]
rt v[192:193],v240 offset:33792
rt v[194:195],v240 offset:34816
s_xor_b32 s80, s80, s72
s_lshl_b32 s80, s80, 18
mm v[116:119],v[204:207],v[176:179],0
ld v224,s[56:59],s80 offen lds
mm v[116:119],v[200:203],v[180:183],v[116:119]
rt v[196:197],v240 offset:42240
rt v[198:199],v240 offset:43264
mm v[84:87],v[200:203],v[164:167],0
s_add_u32 m0, s83, 119296
wt lgkmcnt(4)
mm v[84:87],v[204:207],v[160:163],v[84:87]
rt v[200:201],v241 offset:33792
rt v[202:203],v241 offset:34816
mm v[88:91],v[212:215],v[160:163],0
ld v225,s[56:59],s80 offen lds
mm v[88:91],v[208:211],v[164:167],v[88:91]
rt v[204:205],v241 offset:42240
rt v[206:207],v241 offset:43264
mm v[120:123],v[208:211],v[180:183],0
s_add_u32 m0, s83, 120448
s_xor_b32 s53, s53, s72
mm v[120:123],v[212:215],v[176:179],v[120:123]
rt v[208:209],v242 offset:33792
rt v[210:211],v242 offset:34816
mm v[124:127],v[220:223],v[176:179],0
ld v226,s[56:59],s80 offen lds
mm v[124:127],v[216:219],v[180:183],v[124:127]
rt v[212:213],v242 offset:42240
rt v[214:215],v242 offset:43264
mm v[92:95],v[216:219],v[164:167],0
wt vmcnt(7)
v_cmp_eq_u64 vcc, v[250:251], v[252:253]
s_nop 0
s_bitcmp1_b32 vcc_lo, 1
s_cbranch_scc0 .La5_LO_NB1
.La5_LO_KB1:
wt lgkmcnt(4)
mm v[92:95],v[220:223],v[160:163],v[92:95]
rt v[216:217],v243 offset:33792
rt v[218:219],v243 offset:34816
s_lshl_b32 s53, s53, 13
mm v[64:67],v[192:195],v[168:171],v[64:67]
ld a[0:3],v244,s[44:47],s53 offen offset:0 sc1
mm v[64:67],v[196:199],v[172:175],v[64:67]
rt v[220:221],v243 offset:42240
rt v[222:223],v243 offset:43264
mm v[96:99],v[196:199],v[188:191],v[96:99]
ld a[4:7],v245,s[44:47],s53 offen offset:0 sc1
mm v[96:99],v[192:195],v[184:187],v[96:99]
rt v[196:197],v240 offset:33920
)ASM" R"ASM(rt v[198:199],v240 offset:34944
mm v[100:103],v[200:203],v[184:187],v[100:103]
ld a[8:11],v246,s[44:47],s53 offen offset:0 sc1
mm v[100:103],v[204:207],v[188:191],v[100:103]
rt v[192:193],v240 offset:42368
rt v[194:195],v240 offset:43392
mm v[68:71],v[204:207],v[172:175],v[68:71]
wt lgkmcnt(4)
s_bfe_u32 s54, s4, 262152
mm v[68:71],v[200:203],v[168:171],v[68:71]
rt v[204:205],v241 offset:33920
rt v[206:207],v241 offset:34944
mm v[72:75],v[208:211],v[168:171],v[72:75]
ld a[12:15],v247,s[44:47],s53 offen offset:0 sc1
mm v[72:75],v[212:215],v[172:175],v[72:75]
rt v[200:201],v241 offset:42368
rt v[202:203],v241 offset:43392
mm v[104:107],v[212:215],v[188:191],v[104:107]
ld a[16:19],v244,s[44:47],s53 offen offset:1024 sc1
mm v[104:107],v[208:211],v[184:187],v[104:107]
rt v[212:213],v242 offset:33920
rt v[214:215],v242 offset:34944
mm v[108:111],v[216:219],v[184:187],v[108:111]
ld a[20:23],v245,s[44:47],s53 offen offset:1024 sc1
mm v[108:111],v[220:223],v[188:191],v[108:111]
rt v[208:209],v242 offset:42368
rt v[210:211],v242 offset:43392
mm v[76:79],v[220:223],v[172:175],v[76:79]
s_nop 0
s_barrier
wt lgkmcnt(4)
mm v[76:79],v[216:219],v[168:171],v[76:79]
rt v[220:221],v243 offset:33920
rt v[222:223],v243 offset:34944
mm v[80:83],v[196:199],v[168:171],v[80:83]
ld a[24:27],v246,s[44:47],s53 offen offset:1024 sc1
mm v[80:83],v[192:195],v[172:175],v[80:83]
rt v[216:217],v243 offset:42368
rt v[218:219],v243 offset:43392
mm v[112:115],v[192:195],v[188:191],v[112:115]
ld a[28:31],v247,s[44:47],s53 offen offset:1024 sc1
mm v[112:115],v[196:199],v[184:187],v[112:115]
rt v[192:193],v240 offset:50688
rt v[194:195],v240 offset:51712
mm v[116:119],v[204:207],v[184:187],v[116:119]
ld a[32:35],v244,s[44:47],s53 offen offset:2048 sc1
mm v[116:119],v[200:203],v[188:191],v[116:119]
rt v[196:197],v240 offset:59136
rt v[198:199],v240 offset:60160
.La5_LW3_0:
s_cbranch_execz .La5_LW3_4
mm v[84:87],v[200:203],v[172:175],v[84:87]
wt lgkmcnt(4)
s_xor_b32 s54, s54, s72
mm v[84:87],v[204:207],v[168:171],v[84:87]
rt v[200:201],v241 offset:50688
rt v[202:203],v241 offset:51712
mm v[88:91],v[212:215],v[168:171],v[88:91]
ld a[36:39],v245,s[44:47],s53 offen offset:2048 sc1
mm v[88:91],v[208:211],v[172:175],v[88:91]
rt v[204:205],v241 offset:59136
rt v[206:207],v241 offset:60160
mm v[120:123],v[208:211],v[188:191],v[120:123]
ld a[40:43],v246,s[44:47],s53 offen offset:2048 sc1
mm v[120:123],v[212:215],v[184:187],v[120:123]
rt v[208:209],v242 offset:50688
rt v[210:211],v242 offset:51712
mm v[124:127],v[220:223],v[184:187],v[124:127]
ld a[44:47],v247,s[44:47],s53 offen offset:2048 sc1
mm v[124:127],v[216:219],v[188:191],v[124:127]
rt v[212:213],v242 offset:59136
rt v[214:215],v242 offset:60160
mm v[92:95],v[216:219],v[172:175],v[92:95]
wt vmcnt(4) lgkmcnt(4)
s_lshl_b32 s54, s54, 13
mm v[92:95],v[220:223],v[168:171],v[92:95]
rt v[216:217],v243 offset:50688
rt v[218:219],v243 offset:51712
mm v[0:3],v[192:195],a[0:3],v[0:3]
s_bfe_u32 s55, s4, 262156
s_xor_b32 s55, s55, s72
mm v[0:3],v[196:199],a[16:19],v[0:3]
ld a[48:51],v244,s[44:47],s53 offen offset:3072 sc1
mm v[32:35],v[196:199],a[20:23],v[32:35]
s_lshl_b32 s55, s55, 13
s_bfe_u32 s52, s4, 262160
mm v[32:35],v[192:195],a[4:7],v[32:35]
rt v[220:221],v243 offset:59136
rt v[222:223],v243 offset:60160
mm v[64:67],v[192:195],a[8:11],v[64:67]
s_xor_b32 s52, s52, s72
s_lshl_b32 s52, s52, 13
mm v[64:67],v[196:199],a[24:27],v[64:67]
ld a[52:55],v245,s[44:47],s53 offen offset:3072 sc1
mm v[96:99],v[196:199],a[28:31],v[96:99]
mm v[96:99],v[192:195],a[12:15],v[96:99]
rt v[196:197],v240 offset:50816
rt v[198:199],v240 offset:51840
mm v[100:103],v[200:203],a[12:15],v[100:103]
mm v[100:103],v[204:207],a[28:31],v[100:103]
)ASM" R"ASM(buffer_load_dwordx2 v[250:251], v248, s[64:67], s5 offen sc1
mm v[68:71],v[204:207],a[24:27],v[68:71]
mm v[68:71],v[200:203],a[8:11],v[68:71]
rt v[192:193],v240 offset:59264
rt v[194:195],v240 offset:60288
mm v[36:39],v[200:203],a[4:7],v[36:39]
mm v[36:39],v[204:207],a[20:23],v[36:39]
ld a[56:59],v246,s[44:47],s53 offen offset:3072 sc1
mm v[4:7],v[204:207],a[16:19],v[4:7]
wt lgkmcnt(4)
mm v[4:7],v[200:203],a[0:3],v[4:7]
rt v[204:205],v241 offset:50816
rt v[206:207],v241 offset:51840
mm v[8:11],v[208:211],a[0:3],v[8:11]
mm v[8:11],v[212:215],a[16:19],v[8:11]
ld a[60:63],v247,s[44:47],s53 offen offset:3072 sc1
mm v[40:43],v[212:215],a[20:23],v[40:43]
s_bfe_u32 s53, s4, 262164
s_xor_b32 s53, s53, s72
mm v[40:43],v[208:211],a[4:7],v[40:43]
rt v[200:201],v241 offset:59264
rt v[202:203],v241 offset:60288
mm v[72:75],v[208:211],a[8:11],v[72:75]
s_add_u32 m0, s83, 121472
s_lshl_b32 s53, s53, 13
mm v[72:75],v[212:215],a[24:27],v[72:75]
ld v227,s[56:59],s80 offen lds
mm v[104:107],v[212:215],a[28:31],v[104:107]
s_add_u32 s80, s80, 131072
mm v[104:107],v[208:211],a[12:15],v[104:107]
rt v[212:213],v242 offset:50816
rt v[214:215],v242 offset:51840
mm v[108:111],v[216:219],a[12:15],v[108:111]
s_add_u32 m0, s83, 67584
mm v[108:111],v[220:223],a[28:31],v[108:111]
ld v224,s[56:59],s80 offen lds
mm v[76:79],v[220:223],a[24:27],v[76:79]
mm v[76:79],v[216:219],a[8:11],v[76:79]
rt v[208:209],v242 offset:59264
rt v[210:211],v242 offset:60288
mm v[44:47],v[216:219],a[4:7],v[44:47]
s_add_u32 m0, s83, 68608
mm v[44:47],v[220:223],a[20:23],v[44:47]
ld v225,s[56:59],s80 offen lds
mm v[12:15],v[220:223],a[16:19],v[12:15]
wt lgkmcnt(4)
mm v[12:15],v[216:219],a[0:3],v[12:15]
rt v[220:221],v243 offset:50816
rt v[222:223],v243 offset:51840
mm v[16:19],v[196:199],a[0:3],v[16:19]
s_add_u32 m0, s83, 69760
mm v[16:19],v[192:195],a[16:19],v[16:19]
ld v226,s[56:59],s80 offen lds
mm v[48:51],v[192:195],a[20:23],v[48:51]
mm v[48:51],v[196:199],a[4:7],v[48:51]
rt v[216:217],v243 offset:59264
rt v[218:219],v243 offset:60288
mm v[80:83],v[196:199],a[8:11],v[80:83]
s_add_u32 m0, s83, 70784
s_nop 0
s_barrier
mm v[80:83],v[192:195],a[24:27],v[80:83]
ld v227,s[56:59],s80 offen lds
mm v[112:115],v[192:195],a[28:31],v[112:115]
s_bfe_u32 s80, s4, 262156
s_xor_b32 s80, s80, s72
mm v[112:115],v[196:199],a[12:15],v[112:115]
rt v[192:193],v240 offset:0
rt v[194:195],v240 offset:1024
mm v[116:119],v[204:207],a[12:15],v[116:119]
s_add_u32 m0, s83, 84480
s_lshl_b32 s80, s80, 18
mm v[116:119],v[200:203],a[28:31],v[116:119]
ld v224,s[56:59],s80 offen lds
mm v[84:87],v[200:203],a[24:27],v[84:87]
mm v[84:87],v[204:207],a[8:11],v[84:87]
rt v[196:197],v240 offset:8448
rt v[198:199],v240 offset:9472
mm v[52:55],v[204:207],a[4:7],v[52:55]
s_add_u32 m0, s83, 85504
mm v[52:55],v[200:203],a[20:23],v[52:55]
ld v225,s[56:59],s80 offen lds
mm v[20:23],v[200:203],a[16:19],v[20:23]
wt lgkmcnt(4)
mm v[20:23],v[204:207],a[0:3],v[20:23]
rt v[200:201],v241 offset:0
rt v[202:203],v241 offset:1024
mm v[24:27],v[212:215],a[0:3],v[24:27]
s_add_u32 m0, s83, 86656
mm v[24:27],v[208:211],a[16:19],v[24:27]
ld v226,s[56:59],s80 offen lds
mm v[56:59],v[208:211],a[20:23],v[56:59]
wt vmcnt(10)
v_cmp_eq_u64 vcc, v[250:251], v[252:253]
mm v[56:59],v[212:215],a[4:7],v[56:59]
rt v[204:205],v241 offset:8448
rt v[206:207],v241 offset:9472
mm v[88:91],v[212:215],a[8:11],v[88:91]
s_add_u32 m0, s83, 87680
mm v[88:91],v[208:211],a[24:27],v[88:91]
)ASM" R"ASM(ld v227,s[56:59],s80 offen lds
mm v[120:123],v[208:211],a[28:31],v[120:123]
s_bitcmp1_b32 vcc_lo, 2
s_nop 0
s_nop 0
s_cbranch_scc0 .La5_LO_NB2
.La5_LO_KB2:
mm v[120:123],v[212:215],a[12:15],v[120:123]
rt v[208:209],v242 offset:0
rt v[210:211],v242 offset:1024
mm v[124:127],v[220:223],a[12:15],v[124:127]
s_add_u32 s80, s80, 131072
mm v[124:127],v[216:219],a[28:31],v[124:127]
ld a[12:15],v244,s[44:47],s54 offen offset:0 sc1
mm v[92:95],v[216:219],a[24:27],v[92:95]
mm v[92:95],v[220:223],a[8:11],v[92:95]
rt v[212:213],v242 offset:8448
rt v[214:215],v242 offset:9472
mm v[60:63],v[220:223],a[4:7],v[60:63]
mm v[60:63],v[216:219],a[20:23],v[60:63]
mm v[28:31],v[216:219],a[16:19],v[28:31]
wt vmcnt(10) lgkmcnt(4)
mm v[28:31],v[220:223],a[0:3],v[28:31]
rt v[216:217],v243 offset:0
rt v[218:219],v243 offset:1024
mm v[0:3],v[192:195],a[32:35],v[0:3]
mm v[0:3],v[196:199],a[48:51],v[0:3]
ld a[0:3],v245,s[44:47],s54 offen offset:0 sc1
mm v[32:35],v[196:199],a[52:55],v[32:35]
mm v[32:35],v[192:195],a[36:39],v[32:35]
rt v[220:221],v243 offset:8448
rt v[222:223],v243 offset:9472
mm v[64:67],v[192:195],a[40:43],v[64:67]
mm v[64:67],v[196:199],a[56:59],v[64:67]
ld a[4:7],v246,s[44:47],s54 offen offset:0 sc1
mm v[96:99],v[196:199],a[60:63],v[96:99]
mm v[96:99],v[192:195],a[44:47],v[96:99]
rt v[196:197],v240 offset:128
rt v[198:199],v240 offset:1152
mm v[100:103],v[200:203],a[44:47],v[100:103]
s_mov_b32 s86, 0x05040100
s_mov_b32 s87, 0x07060302
mm v[100:103],v[204:207],a[60:63],v[100:103]
ld a[8:11],v247,s[44:47],s54 offen offset:0 sc1
mm v[68:71],v[204:207],a[56:59],v[68:71]
s_nop 0
ar v230,a128
mm v[68:71],v[200:203],a[40:43],v[68:71]
rt v[192:193],v240 offset:8576
rt v[194:195],v240 offset:9600
mm v[36:39],v[200:203],a[36:39],v[36:39]
v_add_u32 v240, 67584, v240
ar v231,a132
mm v[36:39],v[204:207],a[52:55],v[36:39]
ld a[16:19],v244,s[44:47],s54 offen offset:1024 sc1
mm v[4:7],v[204:207],a[48:51],v[4:7]
wt lgkmcnt(4)
v_perm_b32 v232, v231, v230, s86
mm v[4:7],v[200:203],a[32:35],v[4:7]
rt v[204:205],v241 offset:128
rt v[206:207],v241 offset:1152
mm v[8:11],v[208:211],a[32:35],v[8:11]
v_perm_b32 v233, v231, v230, s87
aw a96,v232
mm v[8:11],v[212:215],a[48:51],v[8:11]
ld a[20:23],v245,s[44:47],s54 offen offset:1024 sc1
mm v[40:43],v[212:215],a[52:55],v[40:43]
aw a100,v233
ar v234,a136
mm v[40:43],v[208:211],a[36:39],v[40:43]
rt v[200:201],v241 offset:8576
rt v[202:203],v241 offset:9600
mm v[72:75],v[208:211],a[40:43],v[72:75]
v_add_u32 v241, 67584, v241
ar v235,a140
mm v[72:75],v[212:215],a[56:59],v[72:75]
ld a[24:27],v246,s[44:47],s54 offen offset:1024 sc1
mm v[104:107],v[212:215],a[60:63],v[104:107]
v_perm_b32 v236, v235, v234, s86
v_perm_b32 v237, v235, v234, s87
mm v[104:107],v[208:211],a[44:47],v[104:107]
rt v[212:213],v242 offset:128
rt v[214:215],v242 offset:1152
mm v[108:111],v[216:219],a[44:47],v[108:111]
aw a97,v236
aw a101,v237
mm v[108:111],v[220:223],a[60:63],v[108:111]
ld a[28:31],v247,s[44:47],s54 offen offset:1024 sc1
mm v[76:79],v[220:223],a[56:59],v[76:79]
ar v230,a144
ar v231,a148
mm v[76:79],v[216:219],a[40:43],v[76:79]
rt v[208:209],v242 offset:8576
rt v[210:211],v242 offset:9600
mm v[44:47],v[216:219],a[36:39],v[44:47]
v_add_u32 v242, 67584, v242
v_perm_b32 v232, v231, v230, s86
mm v[44:47],v[220:223],a[52:55],v[44:47]
v_perm_b32 v233, v231, v230, s87
aw a98,v232
)ASM" R"ASM(mm v[12:15],v[220:223],a[48:51],v[12:15]
wt lgkmcnt(4)
aw a102,v233
mm v[12:15],v[216:219],a[32:35],v[12:15]
rt v[220:221],v243 offset:128
rt v[222:223],v243 offset:1152
mm v[16:19],v[196:199],a[32:35],v[16:19]
s_add_u32 m0, s83, 101376
ar v234,a152
mm v[16:19],v[192:195],a[48:51],v[16:19]
ld v224,s[56:59],s80 offen lds
mm v[48:51],v[192:195],a[52:55],v[48:51]
ar v235,a156
v_perm_b32 v236, v235, v234, s86
mm v[48:51],v[196:199],a[36:39],v[48:51]
rt v[216:217],v243 offset:8576
rt v[218:219],v243 offset:9600
mm v[80:83],v[196:199],a[40:43],v[80:83]
s_add_u32 m0, s83, 102400
v_add_u32 v243, 67584, v243
mm v[80:83],v[192:195],a[56:59],v[80:83]
ld v225,s[56:59],s80 offen lds
mm v[112:115],v[192:195],a[60:63],v[112:115]
wt vmcnt(14)
s_barrier
mm v[112:115],v[196:199],a[44:47],v[112:115]
rt v[192:193],v240 offset:50688
rt v[194:195],v240 offset:51712
mm v[116:119],v[204:207],a[44:47],v[116:119]
s_add_u32 m0, s83, 103552
v_perm_b32 v237, v235, v234, s87
mm v[116:119],v[200:203],a[60:63],v[116:119]
ld v226,s[56:59],s80 offen lds
mm v[84:87],v[200:203],a[56:59],v[84:87]
aw a99,v236
aw a103,v237
mm v[84:87],v[204:207],a[40:43],v[84:87]
rt v[196:197],v240 offset:59136
rt v[198:199],v240 offset:60160
mm v[52:55],v[204:207],a[36:39],v[52:55]
ar v230,a129
ar v231,a133
mm v[52:55],v[200:203],a[52:55],v[52:55]
ld a[64:67],v244,s[44:47],s54 offen offset:2048 sc1
mm v[20:23],v[200:203],a[48:51],v[20:23]
wt lgkmcnt(4)
v_perm_b32 v232, v231, v230, s86
mm v[20:23],v[204:207],a[32:35],v[20:23]
rt v[200:201],v241 offset:50688
rt v[202:203],v241 offset:51712
mm v[24:27],v[212:215],a[32:35],v[24:27]
v_perm_b32 v233, v231, v230, s87
aw a104,v232
mm v[24:27],v[208:211],a[48:51],v[24:27]
ld a[68:71],v245,s[44:47],s54 offen offset:2048 sc1
mm v[56:59],v[208:211],a[52:55],v[56:59]
aw a108,v233
ar v234,a137
mm v[56:59],v[212:215],a[36:39],v[56:59]
rt v[204:205],v241 offset:59136
rt v[206:207],v241 offset:60160
mm v[88:91],v[212:215],a[40:43],v[88:91]
ar v235,a141
v_perm_b32 v236, v235, v234, s86
mm v[88:91],v[208:211],a[56:59],v[88:91]
ld a[72:75],v246,s[44:47],s54 offen offset:2048 sc1
mm v[120:123],v[208:211],a[60:63],v[120:123]
v_perm_b32 v237, v235, v234, s87
aw a105,v236
mm v[120:123],v[212:215],a[44:47],v[120:123]
rt v[208:209],v242 offset:50688
rt v[210:211],v242 offset:51712
mm v[124:127],v[220:223],a[44:47],v[124:127]
aw a109,v237
ar v230,a145
mm v[124:127],v[216:219],a[60:63],v[124:127]
buffer_load_dwordx2 v[250:251], v248, s[64:67], s5 offen sc1
mm v[92:95],v[216:219],a[56:59],v[92:95]
ar v231,a149
v_perm_b32 v232, v231, v230, s86
mm v[92:95],v[220:223],a[40:43],v[92:95]
rt v[212:213],v242 offset:59136
rt v[214:215],v242 offset:60160
mm v[60:63],v[220:223],a[36:39],v[60:63]
v_perm_b32 v233, v231, v230, s87
aw a106,v232
mm v[60:63],v[216:219],a[52:55],v[60:63]
ld a[36:39],v247,s[44:47],s54 offen offset:2048 sc1
mm v[28:31],v[216:219],a[48:51],v[28:31]
wt vmcnt(8) lgkmcnt(4)
aw a110,v233
mm v[28:31],v[220:223],a[32:35],v[28:31]
rt v[216:217],v243 offset:50688
rt v[218:219],v243 offset:51712
mm v[0:3],v[192:195],a[12:15],v[0:3]
ar v234,a153
ar v235,a157
mm v[0:3],v[196:199],a[16:19],v[0:3]
ld a[32:35],v244,s[44:47],s54 offen offset:3072 sc1
mm v[32:35],v[196:199],a[20:23],v[32:35]
)ASM" R"ASM(v_perm_b32 v236, v235, v234, s86
v_perm_b32 v237, v235, v234, s87
mm v[32:35],v[192:195],a[0:3],v[32:35]
rt v[220:221],v243 offset:59136
rt v[222:223],v243 offset:60160
mm v[64:67],v[192:195],a[4:7],v[64:67]
aw a107,v236
aw a111,v237
mm v[64:67],v[196:199],a[24:27],v[64:67]
ld a[40:43],v245,s[44:47],s54 offen offset:3072 sc1
mm v[96:99],v[196:199],a[28:31],v[96:99]
ar v230,a130
ar v231,a134
mm v[96:99],v[192:195],a[8:11],v[96:99]
rt v[196:197],v240 offset:50816
rt v[198:199],v240 offset:51840
mm v[100:103],v[200:203],a[8:11],v[100:103]
v_perm_b32 v232, v231, v230, s86
v_perm_b32 v233, v231, v230, s87
mm v[100:103],v[204:207],a[28:31],v[100:103]
ld a[44:47],v246,s[44:47],s54 offen offset:3072 sc1
mm v[68:71],v[204:207],a[24:27],v[68:71]
aw a112,v232
aw a116,v233
mm v[68:71],v[200:203],a[4:7],v[68:71]
rt v[192:193],v240 offset:59264
rt v[194:195],v240 offset:60288
mm v[36:39],v[200:203],a[0:3],v[36:39]
ar v234,a138
ar v235,a142
mm v[36:39],v[204:207],a[20:23],v[36:39]
ld a[48:51],v247,s[44:47],s54 offen offset:3072 sc1
mm v[4:7],v[204:207],a[16:19],v[4:7]
wt lgkmcnt(4)
s_bfe_u32 s54, s4, 262164
mm v[4:7],v[200:203],a[12:15],v[4:7]
rt v[204:205],v241 offset:50816
rt v[206:207],v241 offset:51840
mm v[8:11],v[208:211],a[12:15],v[8:11]
s_add_u32 m0, s83, 104576
s_xor_b32 s54, s54, s72
mm v[8:11],v[212:215],a[16:19],v[8:11]
ld v227,s[56:59],s80 offen lds
mm v[40:43],v[212:215],a[20:23],v[40:43]
s_bitcmp1_b32 vcc_lo, 3
s_cbranch_scc0 .La5_LO_NB3
.La5_LO_KB3:
mm v[40:43],v[208:211],a[0:3],v[40:43]
rt v[200:201],v241 offset:59264
rt v[202:203],v241 offset:60288
mm v[72:75],v[208:211],a[4:7],v[72:75]
s_bfe_u32 s80, s4, 262160
s_xor_b32 s80, s80, s72
mm v[72:75],v[212:215],a[24:27],v[72:75]
ld a[52:55],v244,s[44:47],s55 offen offset:0 sc1
mm v[104:107],v[212:215],a[28:31],v[104:107]
s_lshl_b32 s80, s80, 18
s_lshl_b32 s54, s54, 13
mm v[104:107],v[208:211],a[8:11],v[104:107]
rt v[212:213],v242 offset:50816
rt v[214:215],v242 offset:51840
mm v[108:111],v[216:219],a[8:11],v[108:111]
s_xor_b32 s54, s54, 229376
v_perm_b32 v236, v235, v234, s86
mm v[108:111],v[220:223],a[28:31],v[108:111]
ld a[56:59],v245,s[44:47],s55 offen offset:0 sc1
mm v[76:79],v[220:223],a[24:27],v[76:79]
v_perm_b32 v237, v235, v234, s87
aw a113,v236
mm v[76:79],v[216:219],a[4:7],v[76:79]
rt v[208:209],v242 offset:59264
rt v[210:211],v242 offset:60288
mm v[44:47],v[216:219],a[0:3],v[44:47]
aw a117,v237
ar v230,a146
mm v[44:47],v[220:223],a[20:23],v[44:47]
ar v231,a150
v_perm_b32 v232, v231, v230, s86
mm v[12:15],v[220:223],a[16:19],v[12:15]
wt lgkmcnt(4)
v_perm_b32 v233, v231, v230, s87
mm v[12:15],v[216:219],a[12:15],v[12:15]
rt v[220:221],v243 offset:50816
rt v[222:223],v243 offset:51840
mm v[16:19],v[196:199],a[12:15],v[16:19]
aw a114,v232
aw a118,v233
mm v[16:19],v[192:195],a[16:19],v[16:19]
ld a[60:63],v246,s[44:47],s55 offen offset:0 sc1
mm v[48:51],v[192:195],a[20:23],v[48:51]
ar v234,a154
ar v235,a158
mm v[48:51],v[196:199],a[0:3],v[48:51]
rt v[216:217],v243 offset:59264
rt v[218:219],v243 offset:60288
mm v[80:83],v[196:199],a[4:7],v[80:83]
v_perm_b32 v236, v235, v234, s86
v_perm_b32 v237, v235, v234, s87
mm v[80:83],v[192:195],a[24:27],v[80:83]
ld a[76:79],v247,s[44:47],s55 offen offset:0 sc1
)ASM" R"ASM(mm v[112:115],v[192:195],a[28:31],v[112:115]
aw a115,v236
aw a119,v237
mm v[112:115],v[196:199],a[8:11],v[112:115]
rt v[192:193],v240 offset:0
rt v[194:195],v240 offset:1024
mm v[116:119],v[204:207],a[8:11],v[116:119]
ar v230,a131
ar v231,a135
mm v[116:119],v[200:203],a[28:31],v[116:119]
ld a[80:83],v244,s[44:47],s55 offen offset:1024 sc1
mm v[84:87],v[200:203],a[24:27],v[84:87]
v_perm_b32 v232, v231, v230, s86
v_perm_b32 v233, v231, v230, s87
mm v[84:87],v[204:207],a[4:7],v[84:87]
rt v[196:197],v240 offset:8448
rt v[198:199],v240 offset:9472
mm v[52:55],v[204:207],a[0:3],v[52:55]
s_load_dwordx2 s[16:17], s[0:1], 0
s_load_dwordx4 s[36:39], s[0:1], 80
mm v[52:55],v[200:203],a[20:23],v[52:55]
ld a[84:87],v245,s[44:47],s55 offen offset:1024 sc1
mm v[20:23],v[200:203],a[16:19],v[20:23]
wt lgkmcnt(4)
aw a120,v232
mm v[20:23],v[204:207],a[12:15],v[20:23]
rt v[200:201],v241 offset:0
rt v[202:203],v241 offset:1024
mm v[24:27],v[212:215],a[12:15],v[24:27]
aw a124,v233
ar v234,a139
mm v[24:27],v[208:211],a[16:19],v[24:27]
ld a[88:91],v246,s[44:47],s55 offen offset:1024 sc1
mm v[56:59],v[208:211],a[20:23],v[56:59]
ar v235,a143
v_perm_b32 v236, v235, v234, s86
mm v[56:59],v[212:215],a[0:3],v[56:59]
rt v[204:205],v241 offset:8448
rt v[206:207],v241 offset:9472
mm v[88:91],v[212:215],a[4:7],v[88:91]
v_perm_b32 v237, v235, v234, s87
aw a121,v236
mm v[88:91],v[208:211],a[24:27],v[88:91]
ld a[92:95],v247,s[44:47],s55 offen offset:1024 sc1
mm v[120:123],v[208:211],a[28:31],v[120:123]
aw a125,v237
ar v230,a147
mm v[120:123],v[212:215],a[8:11],v[120:123]
rt v[208:209],v242 offset:0
rt v[210:211],v242 offset:1024
mm v[124:127],v[220:223],a[8:11],v[124:127]
s_add_u32 m0, s83, 16896
ar v231,a151
mm v[124:127],v[216:219],a[28:31],v[124:127]
ld v224,s[56:59],s80 offen lds
mm v[92:95],v[216:219],a[24:27],v[92:95]
v_perm_b32 v232, v231, v230, s86
v_perm_b32 v233, v231, v230, s87
mm v[92:95],v[220:223],a[4:7],v[92:95]
rt v[212:213],v242 offset:8448
rt v[214:215],v242 offset:9472
mm v[60:63],v[220:223],a[0:3],v[60:63]
aw a122,v232
aw a126,v233
mm v[60:63],v[216:219],a[20:23],v[60:63]
ar v234,a155
ar v235,a159
mm v[28:31],v[216:219],a[16:19],v[28:31]
wt vmcnt(10) lgkmcnt(4)
v_perm_b32 v236, v235, v234, s86
mm v[28:31],v[220:223],a[12:15],v[28:31]
rt v[216:217],v243 offset:0
rt v[218:219],v243 offset:1024
mm v[0:3],v[192:195],a[64:67],v[0:3]
s_add_u32 m0, s83, 17920
v_perm_b32 v237, v235, v234, s87
mm v[0:3],v[196:199],a[32:35],v[0:3]
ld v225,s[56:59],s80 offen lds
mm v[32:35],v[196:199],a[40:43],v[32:35]
aw a123,v236
aw a127,v237
mm v[32:35],v[192:195],a[68:71],v[32:35]
rt v[220:221],v243 offset:8448
rt v[222:223],v243 offset:9472
mm v[64:67],v[192:195],a[72:75],v[64:67]
.La5_LW3_1:
s_cbranch_execz .La5_LW3_5
s_add_u32 m0, s83, 19072
ar v230,a160
mm v[64:67],v[196:199],a[44:47],v[64:67]
ld v226,s[56:59],s80 offen lds
mm v[96:99],v[196:199],a[48:51],v[96:99]
ar v231,a164
v_perm_b32 v232, v231, v230, s86
mm v[96:99],v[192:195],a[36:39],v[96:99]
rt v[196:197],v240 offset:128
rt v[198:199],v240 offset:1152
mm v[100:103],v[200:203],a[36:39],v[100:103]
v_perm_b32 v233, v231, v230, s87
aw a128,v232
)ASM" R"ASM(mm v[100:103],v[204:207],a[48:51],v[100:103]
ld a[0:3],v244,s[44:47],s55 offen offset:2048 sc1
mm v[68:71],v[204:207],a[44:47],v[68:71]
aw a132,v233
ar v234,a168
mm v[68:71],v[200:203],a[72:75],v[68:71]
rt v[192:193],v240 offset:8576
rt v[194:195],v240 offset:9600
mm v[36:39],v[200:203],a[68:71],v[36:39]
ar v235,a172
v_perm_b32 v236, v235, v234, s86
mm v[36:39],v[204:207],a[40:43],v[36:39]
ld a[4:7],v245,s[44:47],s55 offen offset:2048 sc1
mm v[4:7],v[204:207],a[32:35],v[4:7]
wt lgkmcnt(4)
v_perm_b32 v237, v235, v234, s87
mm v[4:7],v[200:203],a[64:67],v[4:7]
rt v[204:205],v241 offset:128
rt v[206:207],v241 offset:1152
mm v[8:11],v[208:211],a[64:67],v[8:11]
aw a129,v236
aw a133,v237
mm v[8:11],v[212:215],a[32:35],v[8:11]
ld a[8:11],v246,s[44:47],s55 offen offset:2048 sc1
mm v[40:43],v[212:215],a[40:43],v[40:43]
ar v230,a176
ar v231,a180
mm v[40:43],v[208:211],a[68:71],v[40:43]
rt v[200:201],v241 offset:8576
rt v[202:203],v241 offset:9600
mm v[72:75],v[208:211],a[72:75],v[72:75]
v_perm_b32 v232, v231, v230, s86
v_perm_b32 v233, v231, v230, s87
mm v[72:75],v[212:215],a[44:47],v[72:75]
ld a[12:15],v247,s[44:47],s55 offen offset:2048 sc1
mm v[104:107],v[212:215],a[48:51],v[104:107]
aw a130,v232
aw a134,v233
mm v[104:107],v[208:211],a[36:39],v[104:107]
rt v[212:213],v242 offset:128
rt v[214:215],v242 offset:1152
mm v[108:111],v[216:219],a[36:39],v[108:111]
ar v234,a184
ar v235,a188
mm v[108:111],v[220:223],a[48:51],v[108:111]
ld a[16:19],v244,s[44:47],s55 offen offset:3072 sc1
mm v[76:79],v[220:223],a[44:47],v[76:79]
v_perm_b32 v236, v235, v234, s86
v_perm_b32 v237, v235, v234, s87
mm v[76:79],v[216:219],a[72:75],v[76:79]
rt v[208:209],v242 offset:8576
rt v[210:211],v242 offset:9600
mm v[44:47],v[216:219],a[68:71],v[44:47]
aw a131,v236
aw a135,v237
mm v[44:47],v[220:223],a[40:43],v[44:47]
s_nop 0
v_cmp_eq_u64 vcc, v[250:251], v[252:253]
mm v[12:15],v[220:223],a[32:35],v[12:15]
wt lgkmcnt(4)
ar v230,a161
mm v[12:15],v[216:219],a[64:67],v[12:15]
rt v[220:221],v243 offset:128
rt v[222:223],v243 offset:1152
mm v[16:19],v[196:199],a[64:67],v[16:19]
ar v231,a165
v_perm_b32 v232, v231, v230, s86
mm v[16:19],v[192:195],a[32:35],v[16:19]
ld a[20:23],v245,s[44:47],s55 offen offset:3072 sc1
mm v[48:51],v[192:195],a[40:43],v[48:51]
v_perm_b32 v233, v231, v230, s87
aw a136,v232
mm v[48:51],v[196:199],a[68:71],v[48:51]
rt v[216:217],v243 offset:8576
rt v[218:219],v243 offset:9600
mm v[80:83],v[196:199],a[72:75],v[80:83]
aw a140,v233
ar v234,a169
mm v[80:83],v[192:195],a[44:47],v[80:83]
ld a[24:27],v246,s[44:47],s55 offen offset:3072 sc1
mm v[112:115],v[192:195],a[48:51],v[112:115]
wt vmcnt(18) lgkmcnt(0)
s_barrier
mm v[112:115],v[196:199],a[36:39],v[112:115]
rt v[192:193],v240 offset:16896
rt v[194:195],v240 offset:17920
mm v[116:119],v[204:207],a[36:39],v[116:119]
s_xor_b32 s90, s72, 0
s_lshl_b32 s92, s76, 5
mm v[116:119],v[200:203],a[48:51],v[116:119]
buffer_load_dwordx2 v[250:251], v248, s[64:67], s5 offen sc1
mm v[84:87],v[200:203],a[44:47],v[84:87]
s_add_u32 s92, s92, s90
s_lshl2_add_u32 s92, s92, s3
mm v[84:87],v[204:207],a[72:75],v[84:87]
rt v[196:197],v240 offset:25344
rt v[198:199],v240 offset:26368
mm v[52:55],v[204:207],a[68:71],v[52:55]
s_lshl_b32 s92, s92, 12
ar v235,a173
)ASM" R"ASM(mm v[52:55],v[200:203],a[40:43],v[52:55]
ld a[28:31],v247,s[44:47],s55 offen offset:3072 sc1
mm v[20:23],v[200:203],a[32:35],v[20:23]
s_nop 0
s_bfe_u32 s55, s4, 262168
mm v[20:23],v[204:207],a[64:67],v[20:23]
rt v[200:201],v241 offset:16896
rt v[202:203],v241 offset:17920
mm v[24:27],v[212:215],a[64:67],v[24:27]
s_add_u32 m0, s83, 20096
s_xor_b32 s55, s55, s72
mm v[24:27],v[208:211],a[32:35],v[24:27]
ld v227,s[56:59],s80 offen lds
mm v[56:59],v[208:211],a[40:43],v[56:59]
s_add_u32 s80, s80, 131072
s_lshl_b32 s55, s55, 13
mm v[56:59],v[212:215],a[68:71],v[56:59]
rt v[204:205],v241 offset:25344
rt v[206:207],v241 offset:26368
mm v[88:91],v[212:215],a[72:75],v[88:91]
s_add_u32 m0, s83, 33792
v_perm_b32 v236, v235, v234, s86
mm v[88:91],v[208:211],a[44:47],v[88:91]
ld v224,s[56:59],s80 offen lds
mm v[120:123],v[208:211],a[48:51],v[120:123]
s_add_u32 s68, s16, s92
s_addc_u32 s69, s17, 0
mm v[120:123],v[212:215],a[36:39],v[120:123]
rt v[208:209],v242 offset:16896
rt v[210:211],v242 offset:17920
mm v[124:127],v[220:223],a[36:39],v[124:127]
s_add_u32 m0, s83, 34816
s_and_b32 s69, s69, 0xffff
mm v[124:127],v[216:219],a[48:51],v[124:127]
ld v225,s[56:59],s80 offen lds
mm v[92:95],v[216:219],a[44:47],v[92:95]
s_mov_b32 s70, 4096
s_mov_b32 s71, 0x20000
mm v[92:95],v[220:223],a[72:75],v[92:95]
rt v[212:213],v242 offset:25344
rt v[214:215],v242 offset:26368
mm v[60:63],v[220:223],a[68:71],v[60:63]
s_add_u32 m0, s83, 35968
v_perm_b32 v237, v235, v234, s87
mm v[60:63],v[216:219],a[40:43],v[60:63]
ld v226,s[56:59],s80 offen lds
mm v[28:31],v[216:219],a[32:35],v[28:31]
wt vmcnt(16) lgkmcnt(4)
aw a137,v236
mm v[28:31],v[220:223],a[64:67],v[28:31]
rt v[216:217],v243 offset:16896
rt v[218:219],v243 offset:17920
mm v[0:3],v[192:195],a[52:55],v[0:3]
s_add_u32 m0, s83, 36992
aw a141,v237
mm v[0:3],v[196:199],a[80:83],v[0:3]
ld v227,s[56:59],s80 offen lds
mm v[32:35],v[196:199],a[84:87],v[32:35]
s_bitcmp1_b32 vcc_lo, 4
s_cbranch_scc0 .La5_LO_NB4
.La5_LO_KB4:
mm v[32:35],v[192:195],a[56:59],v[32:35]
rt v[220:221],v243 offset:25344
rt v[222:223],v243 offset:26368
mm v[64:67],v[192:195],a[60:63],v[64:67]
s_bfe_u32 s80, s4, 262164
s_xor_b32 s80, s80, s72
mm v[64:67],v[196:199],a[88:91],v[64:67]
ld a[32:35],v244,s[44:47],s52 offen offset:0 sc1
mm v[96:99],v[196:199],a[92:95],v[96:99]
s_lshl_b32 s80, s80, 18
ar v230,a177
mm v[96:99],v[192:195],a[76:79],v[96:99]
rt v[196:197],v240 offset:17024
rt v[198:199],v240 offset:18048
mm v[100:103],v[200:203],a[76:79],v[100:103]
s_add_u32 s48, s36, 0
s_addc_u32 s49, s37, 0
mm v[100:103],v[204:207],a[92:95],v[100:103]
ld a[36:39],v245,s[44:47],s52 offen offset:0 sc1
mm v[68:71],v[204:207],a[88:91],v[68:71]
s_and_b32 s49, s49, 0xffff
s_mov_b32 s50, 2048
mm v[68:71],v[200:203],a[60:63],v[68:71]
rt v[192:193],v240 offset:25472
rt v[194:195],v240 offset:26496
mm v[36:39],v[200:203],a[56:59],v[36:39]
s_mov_b32 s51, 0x20000
v_and_b32 v238, 0xf0, v255
mm v[36:39],v[204:207],a[84:87],v[36:39]
ld a[40:43],v246,s[44:47],s52 offen offset:0 sc1
mm v[4:7],v[204:207],a[80:83],v[4:7]
wt lgkmcnt(4)
v_lshlrev_b32 v238, 2, v238
mm v[4:7],v[200:203],a[52:55],v[4:7]
rt v[204:205],v241 offset:17024
rt v[206:207],v241 offset:18048
mm v[8:11],v[208:211],a[52:55],v[8:11]
v_bfe_u32 v249, v255, 8, 1
v_lshl_add_u32 v238, v249, 5, v238
)ASM" R"ASM(mm v[8:11],v[212:215],a[80:83],v[8:11]
ld a[44:47],v247,s[44:47],s52 offen offset:0 sc1
mm v[40:43],v[212:215],a[84:87],v[40:43]
v_lshrrev_b32 v249, 9, v255
v_lshl_add_u32 v238, v249, 4, v238
mm v[40:43],v[208:211],a[56:59],v[40:43]
rt v[200:201],v241 offset:25472
rt v[202:203],v241 offset:26496
mm v[72:75],v[208:211],a[60:63],v[72:75]
s_lshr_b32 s23, s72, 3
s_lshl3_add_u32 s92, s23, s76
mm v[72:75],v[212:215],a[88:91],v[72:75]
ld a[48:51],v244,s[44:47],s52 offen offset:1024 sc1
mm v[104:107],v[212:215],a[92:95],v[104:107]
s_lshl_b32 s92, s92, 19
s_lshl_b32 s97, s3, 16
mm v[104:107],v[208:211],a[76:79],v[104:107]
rt v[212:213],v242 offset:17024
rt v[214:215],v242 offset:18048
mm v[108:111],v[216:219],a[76:79],v[108:111]
s_add_u32 s94, s92, s97
s_add_u32 s95, s94, 32768
mm v[108:111],v[220:223],a[92:95],v[108:111]
ld a[64:67],v245,s[44:47],s52 offen offset:1024 sc1
mm v[76:79],v[220:223],a[88:91],v[76:79]
s_sub_u32 s96, s92, s97
s_add_u32 s96, s96, 458752
mm v[76:79],v[216:219],a[60:63],v[76:79]
rt v[208:209],v242 offset:25472
rt v[210:211],v242 offset:26496
mm v[44:47],v[216:219],a[56:59],v[44:47]
s_add_u32 s97, s96, 32768
s_lshl3_add_u32 s92, s23, s101
mm v[44:47],v[220:223],a[84:87],v[44:47]
s_lshl_b32 s85, s76, 15
s_lshl3_add_u32 s92, s92, s85
mm v[12:15],v[220:223],a[80:83],v[12:15]
wt lgkmcnt(4)
s_add_u32 s92, s92, 2097152
mm v[12:15],v[216:219],a[52:55],v[12:15]
rt v[220:221],v243 offset:17024
rt v[222:223],v243 offset:18048
mm v[16:19],v[196:199],a[52:55],v[16:19]
ar v231,a181
v_perm_b32 v232, v231, v230, s86
mm v[16:19],v[192:195],a[80:83],v[16:19]
ld a[68:71],v246,s[44:47],s52 offen offset:1024 sc1
mm v[48:51],v[192:195],a[84:87],v[48:51]
v_perm_b32 v233, v231, v230, s87
aw a138,v232
mm v[48:51],v[196:199],a[56:59],v[48:51]
rt v[216:217],v243 offset:25472
rt v[218:219],v243 offset:26496
mm v[80:83],v[196:199],a[60:63],v[80:83]
aw a142,v233
ar v234,a185
mm v[80:83],v[192:195],a[88:91],v[80:83]
ld a[72:75],v247,s[44:47],s52 offen offset:1024 sc1
mm v[112:115],v[192:195],a[92:95],v[112:115]
ar v235,a189
v_perm_b32 v236, v235, v234, s86
mm v[112:115],v[196:199],a[76:79],v[112:115]
rt v[192:193],v240 offset:33792
rt v[194:195],v240 offset:34816
mm v[116:119],v[204:207],a[76:79],v[116:119]
s_add_u32 m0, s83, 50688
v_perm_b32 v237, v235, v234, s87
mm v[116:119],v[200:203],a[92:95],v[116:119]
ld v224,s[56:59],s80 offen lds
mm v[84:87],v[200:203],a[88:91],v[84:87]
aw a139,v236
aw a143,v237
mm v[84:87],v[204:207],a[60:63],v[84:87]
rt v[196:197],v240 offset:42240
rt v[198:199],v240 offset:43264
mm v[52:55],v[204:207],a[56:59],v[52:55]
s_add_u32 m0, s83, 51712
ar v230,a162
mm v[52:55],v[200:203],a[84:87],v[52:55]
ld v225,s[56:59],s80 offen lds
mm v[20:23],v[200:203],a[80:83],v[20:23]
wt lgkmcnt(4)
ar v231,a166
mm v[20:23],v[204:207],a[52:55],v[20:23]
rt v[200:201],v241 offset:33792
rt v[202:203],v241 offset:34816
mm v[24:27],v[212:215],a[52:55],v[24:27]
s_add_u32 m0, s83, 52864
v_perm_b32 v232, v231, v230, s86
mm v[24:27],v[208:211],a[80:83],v[24:27]
ld v226,s[56:59],s80 offen lds
mm v[56:59],v[208:211],a[84:87],v[56:59]
v_perm_b32 v233, v231, v230, s87
aw a144,v232
mm v[56:59],v[212:215],a[56:59],v[56:59]
rt v[204:205],v241 offset:42240
rt v[206:207],v241 offset:43264
mm v[88:91],v[212:215],a[60:63],v[88:91]
aw a148,v233
ar v234,a170
)ASM" R"ASM(mm v[88:91],v[208:211],a[88:91],v[88:91]
ld v[136:139],v244,s[44:47],s52 offen offset:2048 sc1
mm v[120:123],v[208:211],a[92:95],v[120:123]
ar v235,a174
v_perm_b32 v236, v235, v234, s86
mm v[120:123],v[212:215],a[76:79],v[120:123]
rt v[208:209],v242 offset:33792
rt v[210:211],v242 offset:34816
mm v[124:127],v[220:223],a[76:79],v[124:127]
v_perm_b32 v237, v235, v234, s87
aw a145,v236
mm v[124:127],v[216:219],a[92:95],v[124:127]
ld v[140:143],v245,s[44:47],s52 offen offset:2048 sc1
mm v[92:95],v[216:219],a[88:91],v[92:95]
aw a149,v237
ar v230,a178
mm v[92:95],v[220:223],a[60:63],v[92:95]
rt v[212:213],v242 offset:42240
rt v[214:215],v242 offset:43264
mm v[60:63],v[220:223],a[56:59],v[60:63]
ar v231,a182
v_perm_b32 v232, v231, v230, s86
mm v[60:63],v[216:219],a[84:87],v[60:63]
v_perm_b32 v233, v231, v230, s87
aw a146,v232
mm v[28:31],v[216:219],a[80:83],v[28:31]
wt vmcnt(18) lgkmcnt(4)
aw a150,v233
mm v[28:31],v[220:223],a[52:55],v[28:31]
rt v[216:217],v243 offset:33792
rt v[218:219],v243 offset:34816
mm v[0:3],v[192:195],a[0:3],v[0:3]
ar v234,a186
ar v235,a190
mm v[0:3],v[196:199],a[16:19],v[0:3]
ld v[152:155],v246,s[44:47],s52 offen offset:2048 sc1
mm v[32:35],v[196:199],a[20:23],v[32:35]
v_perm_b32 v236, v235, v234, s86
v_perm_b32 v237, v235, v234, s87
mm v[32:35],v[192:195],a[4:7],v[32:35]
rt v[220:221],v243 offset:42240
rt v[222:223],v243 offset:43264
mm v[64:67],v[192:195],a[8:11],v[64:67]
aw a147,v236
aw a151,v237
mm v[64:67],v[196:199],a[24:27],v[64:67]
ld v[156:159],v247,s[44:47],s52 offen offset:2048 sc1
mm v[96:99],v[196:199],a[28:31],v[96:99]
ar v230,a163
ar v231,a167
mm v[96:99],v[192:195],a[12:15],v[96:99]
rt v[196:197],v240 offset:33920
rt v[198:199],v240 offset:34944
mm v[100:103],v[200:203],a[12:15],v[100:103]
v_perm_b32 v232, v231, v230, s86
v_perm_b32 v233, v231, v230, s87
mm v[100:103],v[204:207],a[28:31],v[100:103]
ld v[168:171],v244,s[44:47],s52 offen offset:3072 sc1
mm v[68:71],v[204:207],a[24:27],v[68:71]
aw a152,v232
aw a156,v233
mm v[68:71],v[200:203],a[8:11],v[68:71]
rt v[192:193],v240 offset:42368
rt v[194:195],v240 offset:43392
mm v[36:39],v[200:203],a[4:7],v[36:39]
v_add_u32 v240, 4294899712, v240
ar v234,a171
mm v[36:39],v[204:207],a[20:23],v[36:39]
ld v[172:175],v245,s[44:47],s52 offen offset:3072 sc1
mm v[4:7],v[204:207],a[16:19],v[4:7]
wt lgkmcnt(4)
ar v235,a175
mm v[4:7],v[200:203],a[0:3],v[4:7]
rt v[204:205],v241 offset:33920
rt v[206:207],v241 offset:34944
mm v[8:11],v[208:211],a[0:3],v[8:11]
s_nop 0
v_cmp_eq_u64 vcc, v[250:251], v[252:253]
mm v[8:11],v[212:215],a[16:19],v[8:11]
ld v[184:187],v246,s[44:47],s52 offen offset:3072 sc1
mm v[40:43],v[212:215],a[20:23],v[40:43]
v_perm_b32 v236, v235, v234, s86
v_perm_b32 v237, v235, v234, s87
mm v[40:43],v[208:211],a[4:7],v[40:43]
rt v[200:201],v241 offset:42368
rt v[202:203],v241 offset:43392
mm v[72:75],v[208:211],a[8:11],v[72:75]
v_add_u32 v241, 4294899712, v241
aw a153,v236
mm v[72:75],v[212:215],a[24:27],v[72:75]
ld v[188:191],v247,s[44:47],s52 offen offset:3072 sc1
mm v[104:107],v[212:215],a[28:31],v[104:107]
s_bfe_u32 s52, s4, 262172
s_xor_b32 s52, s52, s72
mm v[104:107],v[208:211],a[12:15],v[104:107]
rt v[212:213],v242 offset:33920
rt v[214:215],v242 offset:34944
mm v[108:111],v[216:219],a[12:15],v[108:111]
s_lshl_b32 s52, s52, 13
aw a157,v237
)ASM" R"ASM(mm v[108:111],v[220:223],a[28:31],v[108:111]
buffer_load_dwordx2 v[250:251], v248, s[64:67], s5 offen sc1
mm v[76:79],v[220:223],a[24:27],v[76:79]
ar v230,a179
ar v231,a183
mm v[76:79],v[216:219],a[8:11],v[76:79]
rt v[208:209],v242 offset:42368
rt v[210:211],v242 offset:43392
mm v[44:47],v[216:219],a[4:7],v[44:47]
s_add_u32 m0, s83, 53888
v_add_u32 v242, 4294899712, v242
mm v[44:47],v[220:223],a[20:23],v[44:47]
ld v227,s[56:59],s80 offen lds
mm v[12:15],v[220:223],a[16:19],v[12:15]
wt lgkmcnt(4)
s_add_u32 s80, s80, 131072
mm v[12:15],v[216:219],a[0:3],v[12:15]
rt v[220:221],v243 offset:33920
rt v[222:223],v243 offset:34944
mm v[16:19],v[196:199],a[0:3],v[16:19]
s_add_u32 m0, s83, 0
v_perm_b32 v232, v231, v230, s86
mm v[16:19],v[192:195],a[16:19],v[16:19]
ld v224,s[56:59],s80 offen lds
mm v[48:51],v[192:195],a[20:23],v[48:51]
v_perm_b32 v233, v231, v230, s87
aw a154,v232
mm v[48:51],v[196:199],a[4:7],v[48:51]
rt v[216:217],v243 offset:42368
rt v[218:219],v243 offset:43392
mm v[80:83],v[196:199],a[8:11],v[80:83]
s_add_u32 m0, s83, 1024
v_add_u32 v243, 4294899712, v243
mm v[80:83],v[192:195],a[24:27],v[80:83]
ld v225,s[56:59],s80 offen lds
mm v[112:115],v[192:195],a[28:31],v[112:115]
wt vmcnt(23) lgkmcnt(0)
s_barrier
mm v[112:115],v[196:199],a[12:15],v[112:115]
rt v[192:193],v240 offset:16896
rt v[194:195],v240 offset:17920
mm v[116:119],v[204:207],a[12:15],v[116:119]
s_add_u32 m0, s83, 2176
aw a158,v233
mm v[116:119],v[200:203],a[28:31],v[116:119]
ld v226,s[56:59],s80 offen lds
mm v[84:87],v[200:203],a[24:27],v[84:87]
ar v234,a187
ar v235,a191
mm v[84:87],v[204:207],a[8:11],v[84:87]
rt v[196:197],v240 offset:25344
rt v[198:199],v240 offset:26368
mm v[52:55],v[204:207],a[4:7],v[52:55]
s_add_u32 m0, s83, 3200
v_perm_b32 v236, v235, v234, s86
mm v[52:55],v[200:203],a[20:23],v[52:55]
ld v227,s[56:59],s80 offen lds
mm v[20:23],v[200:203],a[16:19],v[20:23]
s_nop 0
s_bfe_u32 s80, s4, 262164
mm v[20:23],v[204:207],a[0:3],v[20:23]
rt v[200:201],v241 offset:16896
rt v[202:203],v241 offset:17920
mm v[24:27],v[212:215],a[0:3],v[24:27]
s_bitcmp1_b32 vcc_lo, 5
s_cbranch_scc0 .La5_LO_NB5
.La5_LO_KB5:
mm v[24:27],v[208:211],a[16:19],v[24:27]
ld a[52:55],v244,s[44:47],s53 offen offset:0 sc1
mm v[56:59],v[208:211],a[20:23],v[56:59]
s_xor_b32 s80, s80, s72
s_lshl_b32 s80, s80, 18
mm v[56:59],v[212:215],a[4:7],v[56:59]
rt v[204:205],v241 offset:25344
rt v[206:207],v241 offset:26368
mm v[88:91],v[212:215],a[8:11],v[88:91]
s_xor_b32 s80, s80, 7340032
s_add_u32 s80, s80, 131072
mm v[88:91],v[208:211],a[24:27],v[88:91]
ld a[56:59],v245,s[44:47],s53 offen offset:0 sc1
mm v[120:123],v[208:211],a[28:31],v[120:123]
v_perm_b32 v237, v235, v234, s87
aw a155,v236
mm v[120:123],v[212:215],a[12:15],v[120:123]
rt v[208:209],v242 offset:16896
rt v[210:211],v242 offset:17920
mm v[124:127],v[220:223],a[12:15],v[124:127]
aw a159,v237
ar v230,a192
mm v[124:127],v[216:219],a[28:31],v[124:127]
ld a[12:15],v246,s[44:47],s53 offen offset:0 sc1
mm v[92:95],v[216:219],a[24:27],v[92:95]
ar v231,a196
v_perm_b32 v232, v231, v230, s86
mm v[92:95],v[220:223],a[8:11],v[92:95]
rt v[212:213],v242 offset:25344
rt v[214:215],v242 offset:26368
mm v[60:63],v[220:223],a[4:7],v[60:63]
v_perm_b32 v233, v231, v230, s87
aw a160,v232
)ASM" R"ASM(mm v[60:63],v[216:219],a[20:23],v[60:63]
aw a164,v233
ar v234,a200
mm v[28:31],v[216:219],a[16:19],v[28:31]
wt vmcnt(20) lgkmcnt(4)
ar v235,a204
mm v[28:31],v[220:223],a[0:3],v[28:31]
rt v[216:217],v243 offset:16896
rt v[218:219],v243 offset:17920
mm v[0:3],v[192:195],a[32:35],v[0:3]
v_perm_b32 v236, v235, v234, s86
v_perm_b32 v237, v235, v234, s87
mm v[0:3],v[196:199],a[48:51],v[0:3]
ld a[0:3],v247,s[44:47],s53 offen offset:0 sc1
mm v[32:35],v[196:199],a[64:67],v[32:35]
aw a161,v236
aw a165,v237
mm v[32:35],v[192:195],a[36:39],v[32:35]
rt v[220:221],v243 offset:25344
rt v[222:223],v243 offset:26368
mm v[64:67],v[192:195],a[40:43],v[64:67]
ar v230,a208
ar v231,a212
mm v[64:67],v[196:199],a[68:71],v[64:67]
ld a[4:7],v244,s[44:47],s53 offen offset:1024 sc1
mm v[96:99],v[196:199],a[72:75],v[96:99]
v_perm_b32 v232, v231, v230, s86
.La5_LW3_2:
s_cbranch_execz .La5_LW3_6
v_perm_b32 v233, v231, v230, s87
mm v[96:99],v[192:195],a[44:47],v[96:99]
rt v[196:197],v240 offset:17024
rt v[198:199],v240 offset:18048
mm v[100:103],v[200:203],a[44:47],v[100:103]
aw a162,v232
aw a166,v233
mm v[100:103],v[204:207],a[72:75],v[100:103]
ld a[8:11],v245,s[44:47],s53 offen offset:1024 sc1
mm v[68:71],v[204:207],a[68:71],v[68:71]
ar v234,a216
ar v235,a220
mm v[68:71],v[200:203],a[40:43],v[68:71]
rt v[192:193],v240 offset:25472
rt v[194:195],v240 offset:26496
mm v[36:39],v[200:203],a[36:39],v[36:39]
v_perm_b32 v236, v235, v234, s86
v_perm_b32 v237, v235, v234, s87
mm v[36:39],v[204:207],a[64:67],v[36:39]
ld a[16:19],v246,s[44:47],s53 offen offset:1024 sc1
mm v[4:7],v[204:207],a[48:51],v[4:7]
wt lgkmcnt(4)
aw a163,v236
mm v[4:7],v[200:203],a[32:35],v[4:7]
rt v[204:205],v241 offset:17024
rt v[206:207],v241 offset:18048
mm v[8:11],v[208:211],a[32:35],v[8:11]
aw a167,v237
ar v230,a193
mm v[8:11],v[212:215],a[48:51],v[8:11]
ld a[20:23],v247,s[44:47],s53 offen offset:1024 sc1
mm v[40:43],v[212:215],a[64:67],v[40:43]
ar v231,a197
v_perm_b32 v232, v231, v230, s86
mm v[40:43],v[208:211],a[36:39],v[40:43]
rt v[200:201],v241 offset:25472
rt v[202:203],v241 offset:26496
mm v[72:75],v[208:211],a[40:43],v[72:75]
v_perm_b32 v233, v231, v230, s87
aw a168,v232
mm v[72:75],v[212:215],a[68:71],v[72:75]
aw a172,v233
ar v234,a201
mm v[104:107],v[212:215],a[72:75],v[104:107]
ar v235,a205
v_perm_b32 v236, v235, v234, s86
mm v[104:107],v[208:211],a[44:47],v[104:107]
rt v[212:213],v242 offset:17024
rt v[214:215],v242 offset:18048
mm v[108:111],v[216:219],a[44:47],v[108:111]
v_perm_b32 v237, v235, v234, s87
aw a169,v236
mm v[108:111],v[220:223],a[72:75],v[108:111]
aw a173,v237
ar v230,a209
mm v[76:79],v[220:223],a[68:71],v[76:79]
ar v231,a213
v_perm_b32 v232, v231, v230, s86
mm v[76:79],v[216:219],a[40:43],v[76:79]
rt v[208:209],v242 offset:25472
rt v[210:211],v242 offset:26496
mm v[44:47],v[216:219],a[36:39],v[44:47]
v_perm_b32 v233, v231, v230, s87
aw a170,v232
mm v[44:47],v[220:223],a[64:67],v[44:47]
aw a174,v233
ar v234,a217
mm v[12:15],v[220:223],a[48:51],v[12:15]
wt lgkmcnt(4)
ar v235,a221
mm v[12:15],v[216:219],a[32:35],v[12:15]
)ASM" R"ASM(rt v[220:221],v243 offset:17024
rt v[222:223],v243 offset:18048
mm v[16:19],v[196:199],a[32:35],v[16:19]
v_perm_b32 v236, v235, v234, s86
v_perm_b32 v237, v235, v234, s87
mm v[16:19],v[192:195],a[48:51],v[16:19]
ld a[24:27],v244,s[44:47],s53 offen offset:2048 sc1
mm v[48:51],v[192:195],a[64:67],v[48:51]
aw a171,v236
aw a175,v237
mm v[48:51],v[196:199],a[36:39],v[48:51]
rt v[216:217],v243 offset:25472
rt v[218:219],v243 offset:26496
mm v[80:83],v[196:199],a[40:43],v[80:83]
ar v230,a194
ar v231,a198
mm v[80:83],v[192:195],a[68:71],v[80:83]
ld a[28:31],v245,s[44:47],s53 offen offset:2048 sc1
mm v[112:115],v[192:195],a[72:75],v[112:115]
v_perm_b32 v232, v231, v230, s86
v_perm_b32 v233, v231, v230, s87
mm v[112:115],v[196:199],a[44:47],v[112:115]
rt v[192:193],v240 offset:33792
rt v[194:195],v240 offset:34816
mm v[116:119],v[204:207],a[44:47],v[116:119]
aw a176,v232
aw a180,v233
mm v[116:119],v[200:203],a[72:75],v[116:119]
ld a[60:63],v246,s[44:47],s53 offen offset:2048 sc1
mm v[84:87],v[200:203],a[68:71],v[84:87]
ar v234,a202
ar v235,a206
mm v[84:87],v[204:207],a[40:43],v[84:87]
rt v[196:197],v240 offset:42240
rt v[198:199],v240 offset:43264
mm v[52:55],v[204:207],a[36:39],v[52:55]
v_perm_b32 v236, v235, v234, s86
v_perm_b32 v237, v235, v234, s87
mm v[52:55],v[200:203],a[64:67],v[52:55]
ld v[128:131],v247,s[44:47],s53 offen offset:2048 sc1
mm v[20:23],v[200:203],a[48:51],v[20:23]
wt lgkmcnt(4)
aw a177,v236
mm v[20:23],v[204:207],a[32:35],v[20:23]
rt v[200:201],v241 offset:33792
rt v[202:203],v241 offset:34816
mm v[24:27],v[212:215],a[32:35],v[24:27]
aw a181,v237
ar v230,a210
mm v[24:27],v[208:211],a[48:51],v[24:27]
ld v[132:135],v244,s[44:47],s53 offen offset:3072 sc1
mm v[56:59],v[208:211],a[64:67],v[56:59]
ar v231,a214
v_perm_b32 v232, v231, v230, s86
mm v[56:59],v[212:215],a[36:39],v[56:59]
rt v[204:205],v241 offset:42240
rt v[206:207],v241 offset:43264
mm v[88:91],v[212:215],a[40:43],v[88:91]
v_perm_b32 v233, v231, v230, s87
aw a178,v232
mm v[88:91],v[208:211],a[68:71],v[88:91]
ld v[144:147],v245,s[44:47],s53 offen offset:3072 sc1
mm v[120:123],v[208:211],a[72:75],v[120:123]
aw a182,v233
ar v234,a218
mm v[120:123],v[212:215],a[44:47],v[120:123]
rt v[208:209],v242 offset:33792
rt v[210:211],v242 offset:34816
mm v[124:127],v[220:223],a[44:47],v[124:127]
ar v235,a222
v_perm_b32 v236, v235, v234, s86
mm v[124:127],v[216:219],a[72:75],v[124:127]
ld a[44:47],v246,s[44:47],s53 offen offset:3072 sc1
mm v[92:95],v[216:219],a[68:71],v[92:95]
v_perm_b32 v237, v235, v234, s87
aw a179,v236
mm v[92:95],v[220:223],a[40:43],v[92:95]
rt v[212:213],v242 offset:42240
rt v[214:215],v242 offset:43264
mm v[60:63],v[220:223],a[36:39],v[60:63]
aw a183,v237
ar v230,a195
mm v[60:63],v[216:219],a[64:67],v[60:63]
wt vmcnt(20)
v_cmp_eq_u64 vcc, v[250:251], v[252:253]
mm v[28:31],v[216:219],a[48:51],v[28:31]
wt lgkmcnt(4)
ar v231,a199
mm v[28:31],v[220:223],a[32:35],v[28:31]
rt v[216:217],v243 offset:33792
rt v[218:219],v243 offset:34816
mm v[0:3],v[192:195],v[136:139],v[0:3]
v_perm_b32 v232, v231, v230, s86
v_perm_b32 v233, v231, v230, s87
mm v[0:3],v[196:199],v[168:171],v[0:3]
ld a[32:35],v247,s[44:47],s53 offen offset:3072 sc1
mm v[32:35],v[196:199],v[172:175],v[32:35]
aw a184,v232
aw a188,v233
mm v[32:35],v[192:195],v[140:143],v[32:35]
)ASM" R"ASM(rt v[220:221],v243 offset:42240
rt v[222:223],v243 offset:43264
mm v[64:67],v[192:195],v[152:155],v[64:67]
ar v234,a203
ar v235,a207
mm v[64:67],v[196:199],v[184:187],v[64:67]
v_perm_b32 v236, v235, v234, s86
v_perm_b32 v237, v235, v234, s87
mm v[96:99],v[196:199],v[188:191],v[96:99]
aw a185,v236
aw a189,v237
mm v[96:99],v[192:195],v[156:159],v[96:99]
rt v[196:197],v240 offset:33920
rt v[198:199],v240 offset:34944
mm v[100:103],v[200:203],v[156:159],v[100:103]
ar v230,a211
ar v231,a215
mm v[100:103],v[204:207],v[188:191],v[100:103]
buffer_load_dwordx2 v[250:251], v248, s[64:67], s5 offen sc1
mm v[68:71],v[204:207],v[184:187],v[68:71]
v_perm_b32 v232, v231, v230, s86
v_perm_b32 v233, v231, v230, s87
mm v[68:71],v[200:203],v[152:155],v[68:71]
rt v[192:193],v240 offset:42368
rt v[194:195],v240 offset:43392
mm v[36:39],v[200:203],v[140:143],v[36:39]
aw a186,v232
aw a190,v233
mm v[36:39],v[204:207],v[172:175],v[36:39]
ar v234,a219
ar v235,a223
mm v[4:7],v[204:207],v[168:171],v[4:7]
wt lgkmcnt(4)
v_perm_b32 v236, v235, v234, s86
mm v[4:7],v[200:203],v[136:139],v[4:7]
rt v[204:205],v241 offset:33920
rt v[206:207],v241 offset:34944
mm v[8:11],v[208:211],v[136:139],v[8:11]
v_perm_b32 v237, v235, v234, s87
aw a187,v236
mm v[8:11],v[212:215],v[168:171],v[8:11]
aw a191,v237
ar v230,a224
mm v[40:43],v[212:215],v[172:175],v[40:43]
ar v231,a228
v_perm_b32 v232, v231, v230, s86
mm v[40:43],v[208:211],v[140:143],v[40:43]
rt v[200:201],v241 offset:42368
rt v[202:203],v241 offset:43392
mm v[72:75],v[208:211],v[152:155],v[72:75]
v_perm_b32 v233, v231, v230, s87
aw a192,v232
mm v[72:75],v[212:215],v[184:187],v[72:75]
aw a196,v233
ar v234,a232
mm v[104:107],v[212:215],v[188:191],v[104:107]
ar v235,a236
v_perm_b32 v236, v235, v234, s86
mm v[104:107],v[208:211],v[156:159],v[104:107]
rt v[212:213],v242 offset:33920
rt v[214:215],v242 offset:34944
mm v[108:111],v[216:219],v[156:159],v[108:111]
v_perm_b32 v237, v235, v234, s87
aw a193,v236
mm v[108:111],v[220:223],v[188:191],v[108:111]
aw a197,v237
ar v230,a240
mm v[76:79],v[220:223],v[184:187],v[76:79]
ar v231,a244
v_perm_b32 v232, v231, v230, s86
mm v[76:79],v[216:219],v[152:155],v[76:79]
rt v[208:209],v242 offset:42368
rt v[210:211],v242 offset:43392
mm v[44:47],v[216:219],v[140:143],v[44:47]
s_bitcmp1_b32 vcc_lo, 13
s_nop 0
s_nop 0
s_cbranch_scc0 .La5_LO_NB13
.La5_LO_KB13:
mm v[44:47],v[220:223],v[172:175],v[44:47]
v_perm_b32 v233, v231, v230, s87
aw a194,v232
mm v[12:15],v[220:223],v[168:171],v[12:15]
wt lgkmcnt(4)
aw a198,v233
mm v[12:15],v[216:219],v[136:139],v[12:15]
rt v[220:221],v243 offset:33920
rt v[222:223],v243 offset:34944
mm v[16:19],v[196:199],v[136:139],v[16:19]
ar v234,a248
ar v235,a252
mm v[16:19],v[192:195],v[168:171],v[16:19]
ld a[36:39],v244,s[44:47],s54 offen offset:2048 sc1
mm v[48:51],v[192:195],v[172:175],v[48:51]
v_perm_b32 v236, v235, v234, s86
v_perm_b32 v237, v235, v234, s87
mm v[48:51],v[196:199],v[140:143],v[48:51]
rt v[216:217],v243 offset:42368
rt v[218:219],v243 offset:43392
mm v[80:83],v[196:199],v[152:155],v[80:83]
)ASM" R"ASM(aw a195,v236
aw a199,v237
mm v[80:83],v[192:195],v[184:187],v[80:83]
ld a[40:43],v245,s[44:47],s54 offen offset:2048 sc1
mm v[112:115],v[192:195],v[188:191],v[112:115]
wt vmcnt(19)
s_barrier
mm v[112:115],v[196:199],v[156:159],v[112:115]
rt v[192:193],v240 offset:50688
rt v[194:195],v240 offset:51712
mm v[116:119],v[204:207],v[156:159],v[116:119]
s_add_u32 m0, s83, 16896
ar v230,a225
mm v[116:119],v[200:203],v[188:191],v[116:119]
ld v224,s[56:59],s80 offen lds
mm v[84:87],v[200:203],v[184:187],v[84:87]
ar v231,a229
v_perm_b32 v232, v231, v230, s86
mm v[84:87],v[204:207],v[152:155],v[84:87]
rt v[196:197],v240 offset:59136
rt v[198:199],v240 offset:60160
mm v[52:55],v[204:207],v[140:143],v[52:55]
s_add_u32 m0, s83, 17920
v_perm_b32 v233, v231, v230, s87
mm v[52:55],v[200:203],v[172:175],v[52:55]
ld v225,s[56:59],s80 offen lds
mm v[20:23],v[200:203],v[168:171],v[20:23]
wt lgkmcnt(4)
aw a200,v232
mm v[20:23],v[204:207],v[136:139],v[20:23]
rt v[200:201],v241 offset:50688
rt v[202:203],v241 offset:51712
mm v[24:27],v[212:215],v[136:139],v[24:27]
s_add_u32 m0, s83, 19072
aw a204,v233
mm v[24:27],v[208:211],v[168:171],v[24:27]
ld v226,s[56:59],s80 offen lds
mm v[56:59],v[208:211],v[172:175],v[56:59]
ar v234,a233
ar v235,a237
mm v[56:59],v[212:215],v[140:143],v[56:59]
rt v[204:205],v241 offset:59136
rt v[206:207],v241 offset:60160
mm v[88:91],v[212:215],v[152:155],v[88:91]
s_add_u32 m0, s83, 20096
v_perm_b32 v236, v235, v234, s86
mm v[88:91],v[208:211],v[184:187],v[88:91]
ld v227,s[56:59],s80 offen lds
mm v[120:123],v[208:211],v[188:191],v[120:123]
s_bfe_u32 s80, s4, 262168
s_xor_b32 s80, s80, s72
mm v[120:123],v[212:215],v[156:159],v[120:123]
rt v[208:209],v242 offset:50688
rt v[210:211],v242 offset:51712
mm v[124:127],v[220:223],v[156:159],v[124:127]
s_add_u32 m0, s83, 118272
s_lshl_b32 s80, s80, 18
mm v[124:127],v[216:219],v[188:191],v[124:127]
ld v224,s[56:59],s80 offen lds
mm v[92:95],v[216:219],v[184:187],v[92:95]
v_perm_b32 v237, v235, v234, s87
aw a201,v236
mm v[92:95],v[220:223],v[152:155],v[92:95]
rt v[212:213],v242 offset:59136
rt v[214:215],v242 offset:60160
mm v[60:63],v[220:223],v[140:143],v[60:63]
aw a205,v237
ar v230,a241
mm v[60:63],v[216:219],v[172:175],v[60:63]
ar v231,a245
v_perm_b32 v232, v231, v230, s86
mm v[28:31],v[216:219],v[168:171],v[28:31]
wt vmcnt(16) lgkmcnt(4)
v_perm_b32 v233, v231, v230, s87
mm v[28:31],v[220:223],v[136:139],v[28:31]
rt v[216:217],v243 offset:50688
rt v[218:219],v243 offset:51712
mm v[0:3],v[192:195],a[52:55],v[0:3]
s_add_u32 m0, s83, 119296
aw a202,v232
mm v[0:3],v[196:199],a[4:7],v[0:3]
ld v225,s[56:59],s80 offen lds
mm v[32:35],v[196:199],a[8:11],v[32:35]
aw a206,v233
ar v234,a249
mm v[32:35],v[192:195],a[56:59],v[32:35]
rt v[220:221],v243 offset:59136
rt v[222:223],v243 offset:60160
mm v[64:67],v[192:195],a[12:15],v[64:67]
s_add_u32 m0, s83, 120448
ar v235,a253
mm v[64:67],v[196:199],a[16:19],v[64:67]
ld v226,s[56:59],s80 offen lds
mm v[96:99],v[196:199],a[20:23],v[96:99]
v_perm_b32 v236, v235, v234, s86
v_perm_b32 v237, v235, v234, s87
mm v[96:99],v[192:195],a[0:3],v[96:99]
rt v[196:197],v240 offset:50816
rt v[198:199],v240 offset:51840
mm v[100:103],v[200:203],a[0:3],v[100:103]
)ASM" R"ASM(aw a203,v236
aw a207,v237
mm v[100:103],v[204:207],a[20:23],v[100:103]
ld v[136:139],v246,s[44:47],s54 offen offset:2048 sc1
mm v[68:71],v[204:207],a[16:19],v[68:71]
ar v230,a226
ar v231,a230
mm v[68:71],v[200:203],a[12:15],v[68:71]
rt v[192:193],v240 offset:59264
rt v[194:195],v240 offset:60288
mm v[36:39],v[200:203],a[56:59],v[36:39]
v_perm_b32 v232, v231, v230, s86
v_perm_b32 v233, v231, v230, s87
mm v[36:39],v[204:207],a[8:11],v[36:39]
ld v[140:143],v247,s[44:47],s54 offen offset:2048 sc1
mm v[4:7],v[204:207],a[4:7],v[4:7]
wt lgkmcnt(4)
aw a208,v232
mm v[4:7],v[200:203],a[52:55],v[4:7]
rt v[204:205],v241 offset:50816
rt v[206:207],v241 offset:51840
mm v[8:11],v[208:211],a[52:55],v[8:11]
aw a212,v233
ar v234,a234
mm v[8:11],v[212:215],a[4:7],v[8:11]
ld v[152:155],v244,s[44:47],s54 offen offset:3072 sc1
mm v[40:43],v[212:215],a[8:11],v[40:43]
ar v235,a238
v_perm_b32 v236, v235, v234, s86
mm v[40:43],v[208:211],a[56:59],v[40:43]
rt v[200:201],v241 offset:59264
rt v[202:203],v241 offset:60288
mm v[72:75],v[208:211],a[12:15],v[72:75]
v_perm_b32 v237, v235, v234, s87
aw a209,v236
mm v[72:75],v[212:215],a[16:19],v[72:75]
ld v[156:159],v245,s[44:47],s54 offen offset:3072 sc1
mm v[104:107],v[212:215],a[20:23],v[104:107]
aw a213,v237
ar v230,a242
mm v[104:107],v[208:211],a[0:3],v[104:107]
rt v[212:213],v242 offset:50816
rt v[214:215],v242 offset:51840
mm v[108:111],v[216:219],a[0:3],v[108:111]
ar v231,a246
v_perm_b32 v232, v231, v230, s86
mm v[108:111],v[220:223],a[20:23],v[108:111]
ld v[168:171],v246,s[44:47],s54 offen offset:3072 sc1
mm v[76:79],v[220:223],a[16:19],v[76:79]
v_perm_b32 v233, v231, v230, s87
aw a210,v232
mm v[76:79],v[216:219],a[12:15],v[76:79]
rt v[208:209],v242 offset:59264
rt v[210:211],v242 offset:60288
mm v[44:47],v[216:219],a[56:59],v[44:47]
aw a214,v233
ar v234,a250
mm v[44:47],v[220:223],a[8:11],v[44:47]
ar v235,a254
v_perm_b32 v236, v235, v234, s86
mm v[12:15],v[220:223],a[4:7],v[12:15]
wt lgkmcnt(4)
v_perm_b32 v237, v235, v234, s87
mm v[12:15],v[216:219],a[52:55],v[12:15]
rt v[220:221],v243 offset:50816
rt v[222:223],v243 offset:51840
mm v[16:19],v[196:199],a[52:55],v[16:19]
aw a211,v236
aw a215,v237
mm v[16:19],v[192:195],a[4:7],v[16:19]
ld v[172:175],v247,s[44:47],s54 offen offset:3072 sc1
mm v[48:51],v[192:195],a[8:11],v[48:51]
ar v230,a227
ar v231,a231
mm v[48:51],v[196:199],a[56:59],v[48:51]
rt v[216:217],v243 offset:59264
rt v[218:219],v243 offset:60288
mm v[80:83],v[196:199],a[12:15],v[80:83]
s_add_u32 m0, s83, 121472
v_perm_b32 v232, v231, v230, s86
mm v[80:83],v[192:195],a[16:19],v[80:83]
ld v227,s[56:59],s80 offen lds
mm v[112:115],v[192:195],a[20:23],v[112:115]
s_add_u32 s80, s80, 131072
v_perm_b32 v233, v231, v230, s87
mm v[112:115],v[196:199],a[0:3],v[112:115]
rt v[192:193],v240 offset:0
rt v[194:195],v240 offset:1024
mm v[116:119],v[204:207],a[0:3],v[116:119]
s_add_u32 m0, s83, 67584
aw a216,v232
mm v[116:119],v[200:203],a[20:23],v[116:119]
ld v224,s[56:59],s80 offen lds
mm v[84:87],v[200:203],a[16:19],v[84:87]
aw a220,v233
ar v234,a235
mm v[84:87],v[204:207],a[12:15],v[84:87]
rt v[196:197],v240 offset:8448
rt v[198:199],v240 offset:9472
mm v[52:55],v[204:207],a[56:59],v[52:55]
)ASM" R"ASM(s_add_u32 m0, s83, 68608
ar v235,a239
mm v[52:55],v[200:203],a[8:11],v[52:55]
ld v225,s[56:59],s80 offen lds
mm v[20:23],v[200:203],a[4:7],v[20:23]
wt lgkmcnt(4)
v_perm_b32 v236, v235, v234, s86
mm v[20:23],v[204:207],a[52:55],v[20:23]
rt v[200:201],v241 offset:0
rt v[202:203],v241 offset:1024
mm v[24:27],v[212:215],a[52:55],v[24:27]
s_add_u32 m0, s83, 69760
v_perm_b32 v237, v235, v234, s87
mm v[24:27],v[208:211],a[4:7],v[24:27]
ld v226,s[56:59],s80 offen lds
wt vmcnt(19)
v_cmp_eq_u64 vcc, v[250:251], v[252:253]
mm v[56:59],v[208:211],a[8:11],v[56:59]
aw a217,v236
aw a221,v237
mm v[56:59],v[212:215],a[56:59],v[56:59]
rt v[204:205],v241 offset:8448
rt v[206:207],v241 offset:9472
mm v[88:91],v[212:215],a[12:15],v[88:91]
s_add_u32 m0, s83, 70784
ar v230,a243
mm v[88:91],v[208:211],a[16:19],v[88:91]
ld v227,s[56:59],s80 offen lds
mm v[120:123],v[208:211],a[20:23],v[120:123]
s_bfe_u32 s80, s4, 262172
s_xor_b32 s80, s80, s72
mm v[120:123],v[212:215],a[0:3],v[120:123]
rt v[208:209],v242 offset:0
rt v[210:211],v242 offset:1024
mm v[124:127],v[220:223],a[0:3],v[124:127]
s_lshl_b32 s80, s80, 18
ar v231,a247
mm v[124:127],v[216:219],a[20:23],v[124:127]
buffer_load_dwordx2 v[250:251], v248, s[64:67], s5 offen sc1
mm v[92:95],v[216:219],a[16:19],v[92:95]
v_perm_b32 v232, v231, v230, s86
v_perm_b32 v233, v231, v230, s87
mm v[92:95],v[220:223],a[12:15],v[92:95]
rt v[212:213],v242 offset:8448
rt v[214:215],v242 offset:9472
mm v[60:63],v[220:223],a[56:59],v[60:63]
s_add_u32 m0, s83, 84480
aw a218,v232
mm v[60:63],v[216:219],a[8:11],v[60:63]
ld v224,s[56:59],s80 offen lds
mm v[28:31],v[216:219],a[4:7],v[28:31]
wt lgkmcnt(4)
aw a222,v233
mm v[28:31],v[220:223],a[52:55],v[28:31]
.La5_LW3_3:
s_cbranch_execz .La5_LA_RETURN
rt v[216:217],v243 offset:0
rt v[218:219],v243 offset:1024
mm v[0:3],v[192:195],a[24:27],v[0:3]
s_add_u32 m0, s83, 85504
ar v234,a251
mm v[0:3],v[196:199],v[132:135],v[0:3]
ld v225,s[56:59],s80 offen lds
mm v[32:35],v[196:199],v[144:147],v[32:35]
ar v235,a255
v_perm_b32 v236, v235, v234, s86
mm v[32:35],v[192:195],a[28:31],v[32:35]
rt v[220:221],v243 offset:8448
rt v[222:223],v243 offset:9472
mm v[64:67],v[192:195],a[60:63],v[64:67]
s_add_u32 m0, s83, 86656
v_perm_b32 v237, v235, v234, s87
mm v[64:67],v[196:199],a[44:47],v[64:67]
ld v226,s[56:59],s80 offen lds
mm v[96:99],v[196:199],a[32:35],v[96:99]
aw a219,v236
aw a223,v237
mm v[96:99],v[192:195],v[128:131],v[96:99]
rt v[196:197],v240 offset:128
rt v[198:199],v240 offset:1152
mm v[100:103],v[200:203],v[128:131],v[100:103]
s_add_u32 m0, s83, 87680
mm v[100:103],v[204:207],a[32:35],v[100:103]
ld v227,s[56:59],s80 offen lds
mm v[68:71],v[204:207],a[44:47],v[68:71]
s_add_u32 s80, s80, 131072
mm v[68:71],v[200:203],a[60:63],v[68:71]
rt v[192:193],v240 offset:8576
rt v[194:195],v240 offset:9600
mm v[36:39],v[200:203],a[28:31],v[36:39]
s_add_u32 m0, s83, 101376
mm v[36:39],v[204:207],v[144:147],v[36:39]
ld v224,s[56:59],s80 offen lds
mm v[4:7],v[204:207],v[132:135],v[4:7]
wt lgkmcnt(4)
mm v[4:7],v[200:203],a[24:27],v[4:7]
rt v[204:205],v241 offset:128
rt v[206:207],v241 offset:1152
mm v[8:11],v[208:211],a[24:27],v[8:11]
s_add_u32 m0, s83, 102400
)ASM" R"ASM(mm v[8:11],v[212:215],v[132:135],v[8:11]
ld v225,s[56:59],s80 offen lds
mm v[40:43],v[212:215],v[144:147],v[40:43]
mm v[40:43],v[208:211],a[28:31],v[40:43]
rt v[200:201],v241 offset:8576
rt v[202:203],v241 offset:9600
mm v[72:75],v[208:211],a[60:63],v[72:75]
s_add_u32 m0, s83, 103552
mm v[72:75],v[212:215],a[44:47],v[72:75]
ld v226,s[56:59],s80 offen lds
mm v[104:107],v[212:215],a[32:35],v[104:107]
mm v[104:107],v[208:211],v[128:131],v[104:107]
rt v[212:213],v242 offset:128
rt v[214:215],v242 offset:1152
mm v[108:111],v[216:219],v[128:131],v[108:111]
s_add_u32 m0, s83, 104576
mm v[108:111],v[220:223],a[32:35],v[108:111]
ld v227,s[56:59],s80 offen lds
mm v[76:79],v[220:223],a[44:47],v[76:79]
v_bfe_u32 v224, v255, 6, 1
v_lshrrev_b32 v225, 7, v255
mm v[76:79],v[216:219],a[60:63],v[76:79]
rt v[208:209],v242 offset:8576
rt v[210:211],v242 offset:9600
mm v[44:47],v[216:219],a[28:31],v[44:47]
s_bitcmp1_b32 vcc_lo, 6
s_cbranch_scc0 .La5_LO_NB6
.La5_LO_KB6:
mm v[44:47],v[220:223],v[144:147],v[44:47]
v_and_b32 v226, 48, v255
v_lshl_add_u32 v225, v225, 6, v226
mm v[12:15],v[220:223],v[132:135],v[12:15]
wt lgkmcnt(4)
v_lshl_add_u32 v224, v224, 11, v225
mm v[12:15],v[216:219],a[24:27],v[12:15]
rt v[220:221],v243 offset:128
rt v[222:223],v243 offset:1152
mm v[16:19],v[196:199],a[24:27],v[16:19]
mm v[16:19],v[192:195],v[132:135],v[16:19]
ld a[64:67],v244,s[44:47],s55 offen offset:0 sc1
mm v[48:51],v[192:195],v[144:147],v[48:51]
mm v[48:51],v[196:199],a[28:31],v[48:51]
rt v[216:217],v243 offset:8576
rt v[218:219],v243 offset:9600
mm v[80:83],v[196:199],a[60:63],v[80:83]
mm v[80:83],v[192:195],a[44:47],v[80:83]
ld a[68:71],v245,s[44:47],s55 offen offset:0 sc1
mm v[112:115],v[192:195],a[32:35],v[112:115]
wt vmcnt(25)
s_barrier
mm v[112:115],v[196:199],v[128:131],v[112:115]
rt v[192:193],v240 offset:16896
rt v[194:195],v240 offset:17920
mm v[116:119],v[204:207],v[128:131],v[116:119]
mm v[116:119],v[200:203],a[32:35],v[116:119]
ld a[72:75],v244,s[44:47],s55 offen offset:1024 sc1
mm v[84:87],v[200:203],a[44:47],v[84:87]
mm v[84:87],v[204:207],a[60:63],v[84:87]
rt v[196:197],v240 offset:25344
rt v[198:199],v240 offset:26368
mm v[52:55],v[204:207],a[28:31],v[52:55]
mm v[52:55],v[200:203],v[144:147],v[52:55]
ld a[76:79],v245,s[44:47],s55 offen offset:1024 sc1
mm v[20:23],v[200:203],v[132:135],v[20:23]
wt lgkmcnt(4)
mm v[20:23],v[204:207],a[24:27],v[20:23]
rt v[200:201],v241 offset:16896
rt v[202:203],v241 offset:17920
mm v[24:27],v[212:215],a[24:27],v[24:27]
mm v[24:27],v[208:211],v[132:135],v[24:27]
ld a[80:83],v244,s[44:47],s55 offen offset:2048 sc1
mm v[56:59],v[208:211],v[144:147],v[56:59]
mm v[56:59],v[212:215],a[28:31],v[56:59]
rt v[204:205],v241 offset:25344
rt v[206:207],v241 offset:26368
mm v[88:91],v[212:215],a[60:63],v[88:91]
mm v[88:91],v[208:211],a[44:47],v[88:91]
ld a[84:87],v245,s[44:47],s55 offen offset:2048 sc1
mm v[120:123],v[208:211],a[32:35],v[120:123]
mm v[120:123],v[212:215],v[128:131],v[120:123]
rt v[208:209],v242 offset:16896
rt v[210:211],v242 offset:17920
mm v[124:127],v[220:223],v[128:131],v[124:127]
mm v[124:127],v[216:219],a[32:35],v[124:127]
ld a[88:91],v244,s[44:47],s55 offen offset:3072 sc1
mm v[92:95],v[216:219],a[44:47],v[92:95]
mm v[92:95],v[220:223],a[60:63],v[92:95]
rt v[212:213],v242 offset:25344
rt v[214:215],v242 offset:26368
mm v[60:63],v[220:223],a[28:31],v[60:63]
mm v[60:63],v[216:219],v[144:147],v[60:63]
mm v[28:31],v[216:219],v[132:135],v[28:31]
wt vmcnt(21) lgkmcnt(4)
mm v[28:31],v[220:223],a[24:27],v[28:31]
rt v[216:217],v243 offset:16896
rt v[218:219],v243 offset:17920
mm v[0:3],v[192:195],a[36:39],v[0:3]
s_bitcmp1_b32 vcc_lo, 7
s_cbranch_scc0 .La5_LO_NB7
.La5_LO_KB7:
)ASM" R"ASM(mm v[0:3],v[196:199],v[152:155],v[0:3]
ld a[92:95],v245,s[44:47],s55 offen offset:3072 sc1
mm v[32:35],v[196:199],v[156:159],v[32:35]
mm v[32:35],v[192:195],a[40:43],v[32:35]
rt v[220:221],v243 offset:25344
rt v[222:223],v243 offset:26368
mm v[64:67],v[192:195],v[136:139],v[64:67]
mm v[64:67],v[196:199],v[168:171],v[64:67]
ld a[0:3],v244,s[44:47],s52 offen offset:0 sc1
mm v[96:99],v[196:199],v[172:175],v[96:99]
mm v[96:99],v[192:195],v[140:143],v[96:99]
rt v[196:197],v240 offset:17024
rt v[198:199],v240 offset:18048
mm v[100:103],v[200:203],v[140:143],v[100:103]
mm v[100:103],v[204:207],v[172:175],v[100:103]
ld a[4:7],v245,s[44:47],s52 offen offset:0 sc1
mm v[68:71],v[204:207],v[168:171],v[68:71]
mm v[68:71],v[200:203],v[136:139],v[68:71]
rt v[192:193],v240 offset:25472
rt v[194:195],v240 offset:26496
mm v[36:39],v[200:203],a[40:43],v[36:39]
v_add_u32 v240, 67584, v240
mm v[36:39],v[204:207],v[156:159],v[36:39]
ld a[8:11],v244,s[44:47],s52 offen offset:1024 sc1
mm v[4:7],v[204:207],v[152:155],v[4:7]
wt lgkmcnt(4)
mm v[4:7],v[200:203],a[36:39],v[4:7]
rt v[204:205],v241 offset:17024
rt v[206:207],v241 offset:18048
mm v[8:11],v[208:211],a[36:39],v[8:11]
mm v[8:11],v[212:215],v[152:155],v[8:11]
ld a[12:15],v245,s[44:47],s52 offen offset:1024 sc1
mm v[40:43],v[212:215],v[156:159],v[40:43]
mm v[40:43],v[208:211],a[40:43],v[40:43]
rt v[200:201],v241 offset:25472
rt v[202:203],v241 offset:26496
mm v[72:75],v[208:211],v[136:139],v[72:75]
v_add_u32 v241, 67584, v241
mm v[72:75],v[212:215],v[168:171],v[72:75]
ld a[16:19],v244,s[44:47],s52 offen offset:2048 sc1
mm v[104:107],v[212:215],v[172:175],v[104:107]
mm v[104:107],v[208:211],v[140:143],v[104:107]
rt v[212:213],v242 offset:17024
rt v[214:215],v242 offset:18048
mm v[108:111],v[216:219],v[140:143],v[108:111]
mm v[108:111],v[220:223],v[172:175],v[108:111]
ld a[20:23],v245,s[44:47],s52 offen offset:2048 sc1
mm v[76:79],v[220:223],v[168:171],v[76:79]
mm v[76:79],v[216:219],v[136:139],v[76:79]
rt v[208:209],v242 offset:25472
rt v[210:211],v242 offset:26496
mm v[44:47],v[216:219],a[40:43],v[44:47]
v_add_u32 v242, 67584, v242
mm v[44:47],v[220:223],v[156:159],v[44:47]
wt vmcnt(22)
v_cmp_eq_u64 vcc, v[250:251], v[252:253]
mm v[12:15],v[220:223],v[152:155],v[12:15]
wt lgkmcnt(4)
mm v[12:15],v[216:219],a[36:39],v[12:15]
rt v[220:221],v243 offset:17024
rt v[222:223],v243 offset:18048
mm v[16:19],v[196:199],a[36:39],v[16:19]
mm v[16:19],v[192:195],v[152:155],v[16:19]
ld a[24:27],v244,s[44:47],s52 offen offset:3072 sc1
mm v[48:51],v[192:195],v[156:159],v[48:51]
mm v[48:51],v[196:199],a[40:43],v[48:51]
rt v[216:217],v243 offset:25472
rt v[218:219],v243 offset:26496
mm v[80:83],v[196:199],v[136:139],v[80:83]
v_add_u32 v243, 67584, v243
mm v[80:83],v[192:195],v[168:171],v[80:83]
ld a[28:31],v245,s[44:47],s52 offen offset:3072 sc1
mm v[112:115],v[192:195],v[172:175],v[112:115]
wt vmcnt(16)
s_barrier
mm v[112:115],v[196:199],v[140:143],v[112:115]
rt v[192:193],v240 offset:50688
rt v[194:195],v240 offset:51712
mm v[116:119],v[204:207],v[140:143],v[116:119]
mm v[116:119],v[200:203],v[172:175],v[116:119]
ld a[32:35],v246,s[44:47],s55 offen offset:0 sc1
mm v[84:87],v[200:203],v[168:171],v[84:87]
mm v[84:87],v[204:207],v[136:139],v[84:87]
rt v[196:197],v240 offset:59136
rt v[198:199],v240 offset:60160
mm v[52:55],v[204:207],a[40:43],v[52:55]
mm v[52:55],v[200:203],v[156:159],v[52:55]
ld a[44:47],v247,s[44:47],s55 offen offset:0 sc1
mm v[20:23],v[200:203],v[152:155],v[20:23]
wt lgkmcnt(4)
mm v[20:23],v[204:207],a[36:39],v[20:23]
rt v[200:201],v240 offset:0
rt v[202:203],v240 offset:1024
mm v[24:27],v[212:215],a[36:39],v[24:27]
mm v[24:27],v[208:211],v[152:155],v[24:27]
ld a[48:51],v246,s[44:47],s55 offen offset:1024 sc1
mm v[56:59],v[208:211],v[156:159],v[56:59]
mm v[56:59],v[212:215],a[40:43],v[56:59]
rt v[204:205],v240 offset:8448
rt v[206:207],v240 offset:9472
)ASM" R"ASM(mm v[88:91],v[212:215],v[136:139],v[88:91]
mm v[88:91],v[208:211],v[168:171],v[88:91]
ld a[52:55],v247,s[44:47],s55 offen offset:1024 sc1
mm v[120:123],v[208:211],v[172:175],v[120:123]
mm v[120:123],v[212:215],v[140:143],v[120:123]
rt v[208:209],v240 offset:16896
rt v[210:211],v240 offset:17920
mm v[124:127],v[220:223],v[140:143],v[124:127]
mm v[124:127],v[216:219],v[172:175],v[124:127]
ld a[56:59],v246,s[44:47],s55 offen offset:2048 sc1
mm v[92:95],v[216:219],v[168:171],v[92:95]
mm v[92:95],v[220:223],v[136:139],v[92:95]
rt v[212:213],v240 offset:25344
rt v[214:215],v240 offset:26368
mm v[60:63],v[220:223],a[40:43],v[60:63]
mm v[60:63],v[216:219],v[156:159],v[60:63]
mm v[28:31],v[216:219],v[152:155],v[28:31]
wt vmcnt(13) lgkmcnt(4)
mm v[28:31],v[220:223],a[36:39],v[28:31]
rt v[216:217],v240 offset:33792
rt v[218:219],v240 offset:34816
mm v[0:3],v[192:195],a[64:67],v[0:3]
ld a[36:39],v247,s[44:47],s55 offen offset:2048 sc1
mm v[0:3],v[196:199],a[72:75],v[0:3]
rt v[220:221],v240 offset:42240
rt v[222:223],v240 offset:43264
mm v[32:35],v[196:199],a[76:79],v[32:35]
mm v[32:35],v[192:195],a[68:71],v[32:35]
rt v[196:197],v241 offset:50688
rt v[198:199],v241 offset:51712
mm v[32:35],v[200:203],a[84:87],v[32:35]
ld a[40:43],v246,s[44:47],s55 offen offset:3072 sc1
mm v[32:35],v[204:207],a[92:95],v[32:35]
rt v[192:193],v241 offset:59136
rt v[194:195],v241 offset:60160
mm v[0:3],v[204:207],a[88:91],v[0:3]
wt vmcnt(7) lgkmcnt(4)
mm v[0:3],v[200:203],a[80:83],v[0:3]
rt v[204:205],v241 offset:0
rt v[206:207],v241 offset:1024
mm v[0:3],v[208:211],a[0:3],v[0:3]
ld a[60:63],v247,s[44:47],s55 offen offset:3072 sc1
mm v[0:3],v[212:215],a[8:11],v[0:3]
rt v[200:201],v241 offset:8448
rt v[202:203],v241 offset:9472
mm v[32:35],v[212:215],a[12:15],v[32:35]
mm v[32:35],v[208:211],a[4:7],v[32:35]
rt v[212:213],v241 offset:16896
rt v[214:215],v241 offset:17920
mm v[32:35],v[216:219],a[20:23],v[32:35]
ld v[128:131],v246,s[44:47],s52 offen offset:0 sc1
mm v[32:35],v[220:223],a[28:31],v[32:35]
rt v[208:209],v241 offset:25344
rt v[210:211],v241 offset:26368
mm v[0:3],v[220:223],a[24:27],v[0:3]
wt lgkmcnt(4)
mm v[0:3],v[216:219],a[16:19],v[0:3]
rt v[220:221],v241 offset:33792
rt v[222:223],v241 offset:34816
mm v[4:7],v[196:199],a[64:67],v[4:7]
ld v[132:135],v247,s[44:47],s52 offen offset:0 sc1
mm v[4:7],v[192:195],a[72:75],v[4:7]
rt v[216:217],v241 offset:42240
rt v[218:219],v241 offset:43264
mm v[36:39],v[192:195],a[76:79],v[36:39]
mm v[36:39],v[196:199],a[68:71],v[36:39]
rt v[192:193],v242 offset:50688
rt v[194:195],v242 offset:51712
mm v[36:39],v[204:207],a[84:87],v[36:39]
ld v[144:147],v246,s[44:47],s52 offen offset:1024 sc1
mm v[36:39],v[200:203],a[92:95],v[36:39]
rt v[196:197],v242 offset:59136
rt v[198:199],v242 offset:60160
mm v[4:7],v[200:203],a[88:91],v[4:7]
wt lgkmcnt(4)
mm v[4:7],v[204:207],a[80:83],v[4:7]
rt v[200:201],v242 offset:0
rt v[202:203],v242 offset:1024
mm v[4:7],v[212:215],a[0:3],v[4:7]
ld v[148:151],v247,s[44:47],s52 offen offset:1024 sc1
mm v[4:7],v[208:211],a[8:11],v[4:7]
rt v[204:205],v242 offset:8448
rt v[206:207],v242 offset:9472
mm v[36:39],v[208:211],a[12:15],v[36:39]
mm v[36:39],v[212:215],a[4:7],v[36:39]
rt v[208:209],v242 offset:16896
rt v[210:211],v242 offset:17920
mm v[36:39],v[220:223],a[20:23],v[36:39]
mm v[36:39],v[216:219],a[28:31],v[36:39]
rt v[212:213],v242 offset:25344
rt v[214:215],v242 offset:26368
mm v[4:7],v[216:219],a[24:27],v[4:7]
wt lgkmcnt(4)
mm v[4:7],v[220:223],a[16:19],v[4:7]
rt v[216:217],v242 offset:33792
rt v[218:219],v242 offset:34816
mm v[8:11],v[192:195],a[64:67],v[8:11]
ld v[160:163],v246,s[44:47],s52 offen offset:2048 sc1
mm v[8:11],v[196:199],a[72:75],v[8:11]
rt v[220:221],v242 offset:42240
)ASM" R"ASM(rt v[222:223],v242 offset:43264
mm v[40:43],v[196:199],a[76:79],v[40:43]
cv v234,v32,v33
cv v236,v36,v37
mm v[40:43],v[192:195],a[68:71],v[40:43]
rt v[196:197],v243 offset:50688
rt v[198:199],v243 offset:51712
mm v[40:43],v[200:203],a[84:87],v[40:43]
ld v[164:167],v247,s[44:47],s52 offen offset:2048 sc1
mm v[40:43],v[204:207],a[92:95],v[40:43]
rt v[192:193],v243 offset:59136
rt v[194:195],v243 offset:60160
mm v[8:11],v[204:207],a[88:91],v[8:11]
wt lgkmcnt(4)
mm v[8:11],v[200:203],a[80:83],v[8:11]
rt v[204:205],v243 offset:0
rt v[206:207],v243 offset:1024
mm v[8:11],v[208:211],a[0:3],v[8:11]
ld v[176:179],v246,s[44:47],s52 offen offset:3072 sc1
mm v[8:11],v[212:215],a[8:11],v[8:11]
rt v[200:201],v243 offset:8448
rt v[202:203],v243 offset:9472
mm v[40:43],v[212:215],a[12:15],v[40:43]
cv v235,v34,v35
cv v237,v38,v39
mm v[40:43],v[208:211],a[4:7],v[40:43]
rt v[212:213],v243 offset:16896
rt v[214:215],v243 offset:17920
mm v[40:43],v[216:219],a[20:23],v[40:43]
ld v[180:183],v247,s[44:47],s52 offen offset:3072 sc1
mm v[40:43],v[220:223],a[28:31],v[40:43]
rt v[208:209],v243 offset:25344
rt v[210:211],v243 offset:26368
mm v[8:11],v[220:223],a[24:27],v[8:11]
wt lgkmcnt(4)
v_bfe_u32 v250, v255, 6, 1
mm v[8:11],v[216:219],a[16:19],v[8:11]
rt v[220:221],v243 offset:33792
rt v[222:223],v243 offset:34816
mm v[12:15],v[196:199],a[64:67],v[12:15]
v_permlane16_swap_b32 v234, v236
v_permlane16_swap_b32 v235, v237
mm v[12:15],v[192:195],a[72:75],v[12:15]
rt v[216:217],v243 offset:42240
rt v[218:219],v243 offset:43264
mm v[44:47],v[192:195],a[76:79],v[44:47]
cv v230,v0,v1
cv v232,v4,v5
mm v[44:47],v[196:199],a[68:71],v[44:47]
rt v[192:193],v240 offset:50816
rt v[194:195],v240 offset:51840
mm v[44:47],v[204:207],a[84:87],v[44:47]
st v[234:237],v238,s[32:35],s95 offen offset:0 sc1
mm v[44:47],v[200:203],a[92:95],v[44:47]
rt v[196:197],v240 offset:59264
rt v[198:199],v240 offset:60288
mm v[12:15],v[200:203],a[88:91],v[12:15]
wt lgkmcnt(4)
v_mul_u32_u24 v250, 7, v250
mm v[12:15],v[204:207],a[80:83],v[12:15]
rt v[200:201],v240 offset:128
rt v[202:203],v240 offset:1152
mm v[12:15],v[212:215],a[0:3],v[12:15]
cv v231,v2,v3
cv v233,v6,v7
mm v[12:15],v[208:211],a[8:11],v[12:15]
rt v[204:205],v240 offset:8576
rt v[206:207],v240 offset:9600
mm v[44:47],v[208:211],a[12:15],v[44:47]
v_permlane16_swap_b32 v230, v232
v_permlane16_swap_b32 v231, v233
mm v[44:47],v[212:215],a[4:7],v[44:47]
rt v[208:209],v240 offset:17024
rt v[210:211],v240 offset:18048
mm v[44:47],v[220:223],a[20:23],v[44:47]
st v[230:233],v238,s[32:35],s94 offen offset:0 sc1
mm v[44:47],v[216:219],a[28:31],v[44:47]
rt v[212:213],v240 offset:25472
rt v[214:215],v240 offset:26496
mm v[12:15],v[216:219],a[24:27],v[12:15]
wt lgkmcnt(4)
v_xor_b32 v250, s3, v250
mm v[12:15],v[220:223],a[16:19],v[12:15]
rt v[216:217],v240 offset:33920
rt v[218:219],v240 offset:34944
mm v[16:19],v[192:195],a[64:67],v[16:19]
cv v234,v40,v41
cv v236,v44,v45
mm v[16:19],v[196:199],a[72:75],v[16:19]
rt v[220:221],v240 offset:42368
rt v[222:223],v240 offset:43392
mm v[48:51],v[196:199],a[76:79],v[48:51]
cv v235,v42,v43
cv v237,v46,v47
mm v[48:51],v[192:195],a[68:71],v[48:51]
rt v[196:197],v241 offset:50816
rt v[198:199],v241 offset:51840
mm v[48:51],v[200:203],a[84:87],v[48:51]
v_permlane16_swap_b32 v234, v236
v_permlane16_swap_b32 v235, v237
)ASM" R"ASM(mm v[48:51],v[204:207],a[92:95],v[48:51]
rt v[192:193],v241 offset:59264
rt v[194:195],v241 offset:60288
mm v[16:19],v[204:207],a[88:91],v[16:19]
wt lgkmcnt(4)
v_bfe_u32 v251, v255, 4, 2
mm v[16:19],v[200:203],a[80:83],v[16:19]
rt v[204:205],v241 offset:128
rt v[206:207],v241 offset:1152
mm v[16:19],v[208:211],a[0:3],v[16:19]
st v[234:237],v238,s[32:35],s95 offen offset:1024 sc1
mm v[16:19],v[212:215],a[8:11],v[16:19]
rt v[200:201],v241 offset:8576
rt v[202:203],v241 offset:9600
mm v[48:51],v[212:215],a[12:15],v[48:51]
cv v230,v8,v9
cv v232,v12,v13
mm v[48:51],v[208:211],a[4:7],v[48:51]
rt v[212:213],v241 offset:17024
rt v[214:215],v241 offset:18048
mm v[48:51],v[216:219],a[20:23],v[48:51]
cv v231,v10,v11
cv v233,v14,v15
mm v[48:51],v[220:223],a[28:31],v[48:51]
rt v[208:209],v241 offset:25472
rt v[210:211],v241 offset:26496
mm v[16:19],v[220:223],a[24:27],v[16:19]
wt lgkmcnt(4)
v_lshl_add_u32 v250, v250, 2, v251
mm v[16:19],v[216:219],a[16:19],v[16:19]
rt v[220:221],v241 offset:33920
rt v[222:223],v241 offset:34944
mm v[20:23],v[196:199],a[64:67],v[20:23]
v_permlane16_swap_b32 v230, v232
v_permlane16_swap_b32 v231, v233
mm v[20:23],v[192:195],a[72:75],v[20:23]
rt v[216:217],v241 offset:42368
rt v[218:219],v241 offset:43392
mm v[52:55],v[192:195],a[76:79],v[52:55]
v_lshlrev_b32 v250, 9, v250
v_and_b32 v251, 0x2000, v250
mm v[52:55],v[196:199],a[68:71],v[52:55]
rt v[192:193],v242 offset:50816
rt v[194:195],v242 offset:51840
mm v[52:55],v[204:207],a[84:87],v[52:55]
st v[230:233],v238,s[32:35],s94 offen offset:1024 sc1
mm v[52:55],v[200:203],a[92:95],v[52:55]
rt v[196:197],v242 offset:59264
rt v[198:199],v242 offset:60288
mm v[20:23],v[200:203],a[88:91],v[20:23]
wt lgkmcnt(4)
v_add_u32 v250, v250, v251
mm v[20:23],v[204:207],a[80:83],v[20:23]
rt v[200:201],v242 offset:128
rt v[202:203],v242 offset:1152
mm v[20:23],v[212:215],a[0:3],v[20:23]
ld v[136:139],v224,s[68:71],0 offen offset:0
mm v[20:23],v[208:211],a[8:11],v[20:23]
rt v[204:205],v242 offset:8576
rt v[206:207],v242 offset:9600
mm v[52:55],v[208:211],a[12:15],v[52:55]
v_bfe_u32 v251, v255, 7, 1
v_lshl_add_u32 v250, v251, 13, v250
mm v[52:55],v[212:215],a[4:7],v[52:55]
rt v[208:209],v242 offset:17024
rt v[210:211],v242 offset:18048
mm v[52:55],v[220:223],a[20:23],v[52:55]
ld v[140:143],v225,s[48:51],0 offen offset:0
mm v[52:55],v[216:219],a[28:31],v[52:55]
rt v[212:213],v242 offset:25472
rt v[214:215],v242 offset:26496
mm v[20:23],v[216:219],a[24:27],v[20:23]
wt lgkmcnt(4)
s_mov_b32 s93, 0
ld a[224:227],v229,s[28:31],s93 offen offset:0
mm v[20:23],v[220:223],a[16:19],v[20:23]
rt v[216:217],v242 offset:33920
rt v[218:219],v242 offset:34944
mm v[24:27],v[192:195],a[64:67],v[24:27]
cv v234,v48,v49
cv v236,v52,v53
mm v[24:27],v[196:199],a[72:75],v[24:27]
rt v[220:221],v242 offset:42368
rt v[222:223],v242 offset:43392
mm v[56:59],v[196:199],a[76:79],v[56:59]
cv v235,v50,v51
cv v237,v54,v55
mm v[56:59],v[192:195],a[68:71],v[56:59]
rt v[196:197],v243 offset:50816
rt v[198:199],v243 offset:51840
mm v[56:59],v[200:203],a[84:87],v[56:59]
v_permlane16_swap_b32 v234, v236
v_permlane16_swap_b32 v235, v237
.La5_LW3_4:
s_cbranch_execz .La5_LA_RETURN
mm v[56:59],v[204:207],a[92:95],v[56:59]
rt v[192:193],v243 offset:59264
rt v[194:195],v243 offset:60288
mm v[24:27],v[204:207],a[88:91],v[24:27]
wt lgkmcnt(4)
)ASM" R"ASM(ld a[228:231],v229,s[28:31],s93 offen offset:2048
mm v[24:27],v[200:203],a[80:83],v[24:27]
rt v[204:205],v243 offset:128
rt v[206:207],v243 offset:1152
mm v[24:27],v[208:211],a[0:3],v[24:27]
st v[234:237],v238,s[32:35],s95 offen offset:2048 sc1
mm v[24:27],v[212:215],a[8:11],v[24:27]
rt v[200:201],v243 offset:8576
rt v[202:203],v243 offset:9600
mm v[56:59],v[212:215],a[12:15],v[56:59]
cv v230,v16,v17
cv v232,v20,v21
mm v[56:59],v[208:211],a[4:7],v[56:59]
rt v[212:213],v243 offset:17024
rt v[214:215],v243 offset:18048
mm v[56:59],v[216:219],a[20:23],v[56:59]
cv v231,v18,v19
cv v233,v22,v23
mm v[56:59],v[220:223],a[28:31],v[56:59]
rt v[208:209],v243 offset:25472
rt v[210:211],v243 offset:26496
mm v[24:27],v[220:223],a[24:27],v[24:27]
wt lgkmcnt(4)
s_mov_b32 s93, 4096
ld a[232:235],v229,s[28:31],s93 offen offset:0
mm v[24:27],v[216:219],a[16:19],v[24:27]
rt v[220:221],v243 offset:33920
rt v[222:223],v243 offset:34944
mm v[28:31],v[196:199],a[64:67],v[28:31]
v_permlane16_swap_b32 v230, v232
v_permlane16_swap_b32 v231, v233
mm v[28:31],v[192:195],a[72:75],v[28:31]
rt v[216:217],v243 offset:42368
rt v[218:219],v243 offset:43392
mm v[60:63],v[192:195],a[76:79],v[60:63]
ld a[236:239],v229,s[28:31],s93 offen offset:2048
mm v[60:63],v[196:199],a[68:71],v[60:63]
rt v[192:193],v240 offset:50688
rt v[194:195],v240 offset:51712
mm v[60:63],v[204:207],a[84:87],v[60:63]
st v[230:233],v238,s[32:35],s94 offen offset:2048 sc1
mm v[60:63],v[200:203],a[92:95],v[60:63]
rt v[196:197],v240 offset:59136
rt v[198:199],v240 offset:60160
mm v[28:31],v[200:203],a[88:91],v[28:31]
wt lgkmcnt(4)
s_mov_b32 s93, 8192
ld a[240:243],v229,s[28:31],s93 offen offset:0
mm v[28:31],v[204:207],a[80:83],v[28:31]
rt v[200:201],v240 offset:0
rt v[202:203],v240 offset:1024
mm v[28:31],v[212:215],a[0:3],v[28:31]
ld v[152:155],v224,s[68:71],0 offen offset:512
mm v[28:31],v[208:211],a[8:11],v[28:31]
rt v[204:205],v240 offset:8448
rt v[206:207],v240 offset:9472
mm v[60:63],v[208:211],a[12:15],v[60:63]
ld a[244:247],v229,s[28:31],s93 offen offset:2048
mm v[60:63],v[212:215],a[4:7],v[60:63]
rt v[208:209],v240 offset:16896
rt v[210:211],v240 offset:17920
mm v[60:63],v[220:223],a[20:23],v[60:63]
ld v[156:159],v225,s[48:51],0 offen offset:512
mm v[60:63],v[216:219],a[28:31],v[60:63]
rt v[212:213],v240 offset:25344
rt v[214:215],v240 offset:26368
mm v[28:31],v[216:219],a[24:27],v[28:31]
wt vmcnt(24) lgkmcnt(4)
s_mov_b32 s93, 12288
ld a[248:251],v229,s[28:31],s93 offen offset:0
mm v[28:31],v[220:223],a[16:19],v[28:31]
rt v[216:217],v240 offset:33792
rt v[218:219],v240 offset:34816
mm v[64:67],v[192:195],a[32:35],v[64:67]
cv v234,v56,v57
cv v236,v60,v61
mm v[64:67],v[196:199],a[48:51],v[64:67]
rt v[220:221],v240 offset:42240
rt v[222:223],v240 offset:43264
mm v[96:99],v[196:199],a[52:55],v[96:99]
cv v235,v58,v59
cv v237,v62,v63
mm v[96:99],v[192:195],a[44:47],v[96:99]
rt v[196:197],v241 offset:50688
rt v[198:199],v241 offset:51712
mm v[96:99],v[200:203],a[36:39],v[96:99]
v_permlane16_swap_b32 v234, v236
v_permlane16_swap_b32 v235, v237
mm v[96:99],v[204:207],a[60:63],v[96:99]
rt v[192:193],v241 offset:59136
rt v[194:195],v241 offset:60160
mm v[64:67],v[204:207],a[40:43],v[64:67]
wt vmcnt(17) lgkmcnt(4)
ld a[252:255],v229,s[28:31],s93 offen offset:2048
mm v[64:67],v[200:203],a[56:59],v[64:67]
rt v[204:205],v241 offset:0
rt v[206:207],v241 offset:1024
mm v[64:67],v[208:211],v[128:131],v[64:67]
st v[234:237],v238,s[32:35],s95 offen offset:3072 sc1
mm v[64:67],v[212:215],v[144:147],v[64:67]
)ASM" R"ASM(rt v[200:201],v241 offset:8448
rt v[202:203],v241 offset:9472
mm v[96:99],v[212:215],v[148:151],v[96:99]
cv v230,v24,v25
cv v232,v28,v29
mm v[96:99],v[208:211],v[132:135],v[96:99]
rt v[212:213],v241 offset:16896
rt v[214:215],v241 offset:17920
mm v[96:99],v[216:219],v[164:167],v[96:99]
cv v231,v26,v27
cv v233,v30,v31
mm v[96:99],v[220:223],v[180:183],v[96:99]
rt v[208:209],v241 offset:25344
rt v[210:211],v241 offset:26368
mm v[64:67],v[220:223],v[176:179],v[64:67]
wt lgkmcnt(4)
s_mov_b32 s93, 0
ld a[64:67],v229,s[28:31],s93 offen offset:256
mm v[64:67],v[216:219],v[160:163],v[64:67]
rt v[220:221],v241 offset:33792
rt v[222:223],v241 offset:34816
mm v[68:71],v[196:199],a[32:35],v[68:71]
v_permlane16_swap_b32 v230, v232
v_permlane16_swap_b32 v231, v233
mm v[68:71],v[192:195],a[48:51],v[68:71]
rt v[216:217],v241 offset:42240
rt v[218:219],v241 offset:43264
mm v[100:103],v[192:195],a[52:55],v[100:103]
ld a[68:71],v229,s[28:31],s93 offen offset:2304
mm v[100:103],v[196:199],a[44:47],v[100:103]
rt v[192:193],v242 offset:50688
rt v[194:195],v242 offset:51712
mm v[100:103],v[204:207],a[36:39],v[100:103]
st v[230:233],v238,s[32:35],s94 offen offset:3072 sc1
mm v[100:103],v[200:203],a[60:63],v[100:103]
rt v[196:197],v242 offset:59136
rt v[198:199],v242 offset:60160
mm v[68:71],v[200:203],a[40:43],v[68:71]
wt lgkmcnt(4)
s_mov_b32 s93, 4096
ld a[72:75],v229,s[28:31],s93 offen offset:256
mm v[68:71],v[204:207],a[56:59],v[68:71]
rt v[200:201],v242 offset:0
rt v[202:203],v242 offset:1024
mm v[68:71],v[212:215],v[128:131],v[68:71]
ld v[168:171],v224,s[68:71],0 offen offset:1024
mm v[68:71],v[208:211],v[144:147],v[68:71]
rt v[204:205],v242 offset:8448
rt v[206:207],v242 offset:9472
mm v[100:103],v[208:211],v[148:151],v[100:103]
ld a[76:79],v229,s[28:31],s93 offen offset:2304
mm v[100:103],v[212:215],v[132:135],v[100:103]
rt v[208:209],v242 offset:16896
rt v[210:211],v242 offset:17920
mm v[100:103],v[220:223],v[164:167],v[100:103]
ld v[172:175],v225,s[48:51],0 offen offset:1024
mm v[100:103],v[216:219],v[180:183],v[100:103]
rt v[212:213],v242 offset:25344
rt v[214:215],v242 offset:26368
mm v[68:71],v[216:219],v[176:179],v[68:71]
wt lgkmcnt(4)
s_mov_b32 s93, 8192
ld a[80:83],v229,s[28:31],s93 offen offset:256
mm v[68:71],v[220:223],v[160:163],v[68:71]
rt v[216:217],v242 offset:33792
rt v[218:219],v242 offset:34816
mm v[72:75],v[192:195],a[32:35],v[72:75]
cv v234,v96,v97
cv v236,v100,v101
mm v[72:75],v[196:199],a[48:51],v[72:75]
rt v[220:221],v242 offset:42240
rt v[222:223],v242 offset:43264
mm v[104:107],v[196:199],a[52:55],v[104:107]
cv v235,v98,v99
cv v237,v102,v103
mm v[104:107],v[192:195],a[44:47],v[104:107]
rt v[196:197],v243 offset:50688
rt v[198:199],v243 offset:51712
mm v[104:107],v[200:203],a[36:39],v[104:107]
v_permlane16_swap_b32 v234, v236
v_permlane16_swap_b32 v235, v237
mm v[104:107],v[204:207],a[60:63],v[104:107]
rt v[192:193],v243 offset:59136
rt v[194:195],v243 offset:60160
mm v[72:75],v[204:207],a[40:43],v[72:75]
wt lgkmcnt(4)
ld a[84:87],v229,s[28:31],s93 offen offset:2304
mm v[72:75],v[200:203],a[56:59],v[72:75]
rt v[204:205],v243 offset:0
rt v[206:207],v243 offset:1024
mm v[72:75],v[208:211],v[128:131],v[72:75]
st v[234:237],v238,s[32:35],s97 offen offset:0 sc1
mm v[72:75],v[212:215],v[144:147],v[72:75]
rt v[200:201],v243 offset:8448
rt v[202:203],v243 offset:9472
mm v[104:107],v[212:215],v[148:151],v[104:107]
cv v230,v64,v65
cv v232,v68,v69
mm v[104:107],v[208:211],v[132:135],v[104:107]
rt v[212:213],v243 offset:16896
)ASM" R"ASM(rt v[214:215],v243 offset:17920
mm v[104:107],v[216:219],v[164:167],v[104:107]
cv v231,v66,v67
cv v233,v70,v71
mm v[104:107],v[220:223],v[180:183],v[104:107]
rt v[208:209],v243 offset:25344
rt v[210:211],v243 offset:26368
mm v[72:75],v[220:223],v[176:179],v[72:75]
wt lgkmcnt(4)
s_mov_b32 s93, 12288
ld a[88:91],v229,s[28:31],s93 offen offset:256
mm v[72:75],v[216:219],v[160:163],v[72:75]
rt v[220:221],v243 offset:33792
rt v[222:223],v243 offset:34816
mm v[76:79],v[196:199],a[32:35],v[76:79]
v_permlane16_swap_b32 v230, v232
v_permlane16_swap_b32 v231, v233
mm v[76:79],v[192:195],a[48:51],v[76:79]
rt v[216:217],v243 offset:42240
rt v[218:219],v243 offset:43264
mm v[108:111],v[192:195],a[52:55],v[108:111]
ld a[92:95],v229,s[28:31],s93 offen offset:2304
mm v[108:111],v[196:199],a[44:47],v[108:111]
rt v[192:193],v240 offset:50816
rt v[194:195],v240 offset:51840
mm v[108:111],v[204:207],a[36:39],v[108:111]
st v[230:233],v238,s[32:35],s96 offen offset:0 sc1
mm v[108:111],v[200:203],a[60:63],v[108:111]
rt v[196:197],v240 offset:59264
rt v[198:199],v240 offset:60288
mm v[76:79],v[200:203],a[40:43],v[76:79]
wt lgkmcnt(4)
v_and_b32_e64 v251, 64, v255
mm v[76:79],v[204:207],a[56:59],v[76:79]
rt v[200:201],v240 offset:128
rt v[202:203],v240 offset:1152
mm v[76:79],v[212:215],v[128:131],v[76:79]
ld v[184:187],v224,s[68:71],0 offen offset:1536
mm v[76:79],v[208:211],v[144:147],v[76:79]
rt v[204:205],v240 offset:8576
rt v[206:207],v240 offset:9600
mm v[108:111],v[208:211],v[148:151],v[108:111]
v_lshl_add_u32 v251, v251, 24, v250
v_and_b32 v248, 0x1f0, v255
mm v[108:111],v[212:215],v[132:135],v[108:111]
rt v[208:209],v240 offset:17024
rt v[210:211],v240 offset:18048
mm v[108:111],v[220:223],v[164:167],v[108:111]
ld v[188:191],v225,s[48:51],0 offen offset:1536
mm v[108:111],v[216:219],v[180:183],v[108:111]
rt v[212:213],v240 offset:25472
rt v[214:215],v240 offset:26496
mm v[76:79],v[216:219],v[176:179],v[76:79]
wt lgkmcnt(4)
v_lshrrev_b32_e64 v248, 1, v248
mm v[76:79],v[220:223],v[160:163],v[76:79]
rt v[216:217],v240 offset:33920
rt v[218:219],v240 offset:34944
mm v[80:83],v[192:195],a[32:35],v[80:83]
cv v234,v104,v105
cv v236,v108,v109
mm v[80:83],v[196:199],a[48:51],v[80:83]
rt v[220:221],v240 offset:42368
rt v[222:223],v240 offset:43392
mm v[112:115],v[196:199],a[52:55],v[112:115]
cv v235,v106,v107
cv v237,v110,v111
mm v[112:115],v[192:195],a[44:47],v[112:115]
rt v[196:197],v241 offset:50816
rt v[198:199],v241 offset:51840
mm v[112:115],v[200:203],a[36:39],v[112:115]
v_permlane16_swap_b32 v234, v236
v_permlane16_swap_b32 v235, v237
mm v[112:115],v[204:207],a[60:63],v[112:115]
rt v[192:193],v241 offset:59264
rt v[194:195],v241 offset:60288
mm v[80:83],v[204:207],a[40:43],v[80:83]
wt lgkmcnt(4)
mm v[80:83],v[200:203],a[56:59],v[80:83]
rt v[204:205],v241 offset:128
rt v[206:207],v241 offset:1152
mm v[80:83],v[208:211],v[128:131],v[80:83]
st v[234:237],v238,s[32:35],s97 offen offset:1024 sc1
mm v[80:83],v[212:215],v[144:147],v[80:83]
rt v[200:201],v241 offset:8576
rt v[202:203],v241 offset:9600
mm v[112:115],v[212:215],v[148:151],v[112:115]
cv v230,v72,v73
cv v232,v76,v77
mm v[112:115],v[208:211],v[132:135],v[112:115]
rt v[212:213],v241 offset:17024
rt v[214:215],v241 offset:18048
mm v[112:115],v[216:219],v[164:167],v[112:115]
cv v231,v74,v75
cv v233,v78,v79
mm v[112:115],v[220:223],v[180:183],v[112:115]
rt v[208:209],v241 offset:25472
rt v[210:211],v241 offset:26496
mm v[80:83],v[220:223],v[176:179],v[80:83]
wt lgkmcnt(4)
)ASM" R"ASM(mm v[80:83],v[216:219],v[160:163],v[80:83]
rt v[220:221],v241 offset:33920
rt v[222:223],v241 offset:34944
mm v[84:87],v[196:199],a[32:35],v[84:87]
v_permlane16_swap_b32 v230, v232
v_permlane16_swap_b32 v231, v233
mm v[84:87],v[192:195],a[48:51],v[84:87]
rt v[216:217],v241 offset:42368
rt v[218:219],v241 offset:43392
mm v[116:119],v[192:195],a[52:55],v[116:119]
mm v[116:119],v[196:199],a[44:47],v[116:119]
rt v[192:193],v242 offset:50816
rt v[194:195],v242 offset:51840
mm v[116:119],v[204:207],a[36:39],v[116:119]
st v[230:233],v238,s[32:35],s96 offen offset:1024 sc1
mm v[116:119],v[200:203],a[60:63],v[116:119]
rt v[196:197],v242 offset:59264
rt v[198:199],v242 offset:60288
mm v[84:87],v[200:203],a[40:43],v[84:87]
wt lgkmcnt(4)
mm v[84:87],v[204:207],a[56:59],v[84:87]
rt v[200:201],v242 offset:128
rt v[202:203],v242 offset:1152
mm v[84:87],v[212:215],v[128:131],v[84:87]
mm v[84:87],v[208:211],v[144:147],v[84:87]
rt v[204:205],v242 offset:8576
rt v[206:207],v242 offset:9600
mm v[116:119],v[208:211],v[148:151],v[116:119]
mm v[116:119],v[212:215],v[132:135],v[116:119]
rt v[208:209],v242 offset:17024
rt v[210:211],v242 offset:18048
mm v[116:119],v[220:223],v[164:167],v[116:119]
mm v[116:119],v[216:219],v[180:183],v[116:119]
rt v[212:213],v242 offset:25472
rt v[214:215],v242 offset:26496
mm v[84:87],v[216:219],v[176:179],v[84:87]
wt lgkmcnt(4)
mm v[84:87],v[220:223],v[160:163],v[84:87]
rt v[216:217],v242 offset:33920
rt v[218:219],v242 offset:34944
mm v[88:91],v[192:195],a[32:35],v[88:91]
cv v234,v112,v113
cv v236,v116,v117
mm v[88:91],v[196:199],a[48:51],v[88:91]
rt v[220:221],v242 offset:42368
rt v[222:223],v242 offset:43392
mm v[120:123],v[196:199],a[52:55],v[120:123]
cv v235,v114,v115
cv v237,v118,v119
mm v[120:123],v[192:195],a[44:47],v[120:123]
rt v[196:197],v243 offset:50816
rt v[198:199],v243 offset:51840
mm v[120:123],v[200:203],a[36:39],v[120:123]
v_permlane16_swap_b32 v234, v236
v_permlane16_swap_b32 v235, v237
mm v[120:123],v[204:207],a[60:63],v[120:123]
rt v[192:193],v243 offset:59264
rt v[194:195],v243 offset:60288
mm v[88:91],v[204:207],a[40:43],v[88:91]
wt lgkmcnt(4)
mm v[88:91],v[200:203],a[56:59],v[88:91]
rt v[204:205],v243 offset:128
rt v[206:207],v243 offset:1152
mm v[88:91],v[208:211],v[128:131],v[88:91]
st v[234:237],v238,s[32:35],s97 offen offset:2048 sc1
mm v[88:91],v[212:215],v[144:147],v[88:91]
rt v[200:201],v243 offset:8576
rt v[202:203],v243 offset:9600
mm v[120:123],v[212:215],v[148:151],v[120:123]
cv v230,v80,v81
cv v232,v84,v85
mm v[120:123],v[208:211],v[132:135],v[120:123]
rt v[212:213],v243 offset:17024
rt v[214:215],v243 offset:18048
mm v[120:123],v[216:219],v[164:167],v[120:123]
cv v231,v82,v83
cv v233,v86,v87
wt vmcnt(15)
buffer_store_dwordx2 v[252:253], v251, s[64:67], s92 offen sc1
mm v[120:123],v[220:223],v[180:183],v[120:123]
rt v[208:209],v243 offset:25472
rt v[210:211],v243 offset:26496
mm v[88:91],v[220:223],v[176:179],v[88:91]
wt lgkmcnt(4)
mm v[88:91],v[216:219],v[160:163],v[88:91]
rt v[220:221],v243 offset:33920
rt v[222:223],v243 offset:34944
mm v[92:95],v[196:199],a[32:35],v[92:95]
v_permlane16_swap_b32 v230, v232
v_permlane16_swap_b32 v231, v233
mm v[92:95],v[192:195],a[48:51],v[92:95]
rt v[216:217],v243 offset:42368
rt v[218:219],v243 offset:43392
mm v[124:127],v[192:195],a[52:55],v[124:127]
mm v[124:127],v[196:199],a[44:47],v[124:127]
mm v[124:127],v[204:207],a[36:39],v[124:127]
st v[230:233],v238,s[32:35],s96 offen offset:2048 sc1
mm v[124:127],v[200:203],a[60:63],v[124:127]
mm v[92:95],v[200:203],a[40:43],v[92:95]
wt lgkmcnt(0)
)ASM" R"ASM(mm v[92:95],v[204:207],a[56:59],v[92:95]
mm v[92:95],v[212:215],v[128:131],v[92:95]
mm v[92:95],v[208:211],v[144:147],v[92:95]
mm v[124:127],v[208:211],v[148:151],v[124:127]
mm v[124:127],v[212:215],v[132:135],v[124:127]
mm v[124:127],v[220:223],v[164:167],v[124:127]
mm v[124:127],v[216:219],v[180:183],v[124:127]
mm v[92:95],v[216:219],v[176:179],v[92:95]
mm v[92:95],v[220:223],v[160:163],v[92:95]
s_nop 7
cv v234,v120,v121
cv v236,v124,v125
cv v235,v122,v123
cv v237,v126,v127
cv v230,v88,v89
cv v232,v92,v93
cv v231,v90,v91
cv v233,v94,v95
v_permlane16_swap_b32 v234, v236
v_permlane16_swap_b32 v235, v237
st v[234:237],v238,s[32:35],s97 offen offset:3072 sc1
v_permlane16_swap_b32 v230, v232
v_permlane16_swap_b32 v231, v233
st v[230:233],v238,s[32:35],s96 offen offset:3072 sc1
s_branch .La5_LT_0
; ---- O projection, odd XCDs: seven and a half heads
.La5_LO_SC:
.La5_LW7_0:
s_cbranch_execz .La5_LW7_4
mm v[64:67],v[192:195],v[160:163],0
buffer_load_dwordx2 v[250:251], v248, s[64:67], s5 offen sc1
mm v[64:67],v[196:199],v[164:167],v[64:67]
rt v[220:221],v243 offset:25344
rt v[222:223],v243 offset:26368
mm v[96:99],v[196:199],v[180:183],0
s_add_u32 m0, s83, 0
s_bfe_u32 s80, s4, 262148
mm v[96:99],v[192:195],v[176:179],v[96:99]
rt v[196:197],v240 offset:17024
rt v[198:199],v240 offset:18048
s_xor_b32 s80, s80, s72
s_lshl_b32 s80, s80, 18
s_add_u32 s80, s80, 131072
mm v[100:103],v[200:203],v[176:179],0
ld v224,s[56:59],s80 offen lds
mm v[100:103],v[204:207],v[180:183],v[100:103]
rt v[192:193],v240 offset:25472
rt v[194:195],v240 offset:26496
mm v[68:71],v[204:207],v[164:167],0
s_add_u32 m0, s83, 1024
wt lgkmcnt(4)
mm v[68:71],v[200:203],v[160:163],v[68:71]
rt v[204:205],v241 offset:17024
rt v[206:207],v241 offset:18048
mm v[72:75],v[208:211],v[160:163],0
ld v225,s[56:59],s80 offen lds
mm v[72:75],v[212:215],v[164:167],v[72:75]
rt v[200:201],v241 offset:25472
rt v[202:203],v241 offset:26496
mm v[104:107],v[212:215],v[180:183],0
s_add_u32 m0, s83, 2176
s_bfe_u32 s53, s4, 262148
mm v[104:107],v[208:211],v[176:179],v[104:107]
rt v[212:213],v242 offset:17024
rt v[214:215],v242 offset:18048
mm v[108:111],v[216:219],v[176:179],0
ld v226,s[56:59],s80 offen lds
mm v[108:111],v[220:223],v[180:183],v[108:111]
rt v[208:209],v242 offset:25472
rt v[210:211],v242 offset:26496
mm v[76:79],v[220:223],v[164:167],0
s_add_u32 m0, s83, 3200
wt lgkmcnt(4)
mm v[76:79],v[216:219],v[160:163],v[76:79]
rt v[220:221],v243 offset:17024
rt v[222:223],v243 offset:18048
mm v[80:83],v[196:199],v[160:163],0
ld v227,s[56:59],s80 offen lds
mm v[80:83],v[192:195],v[164:167],v[80:83]
rt v[216:217],v243 offset:25472
rt v[218:219],v243 offset:26496
mm v[112:115],v[192:195],v[180:183],0
s_add_u32 m0, s83, 118272
s_bfe_u32 s80, s4, 262152
mm v[112:115],v[196:199],v[176:179],v[112:115]
rt v[192:193],v240 offset:33792
rt v[194:195],v240 offset:34816
s_xor_b32 s80, s80, s72
s_lshl_b32 s80, s80, 18
mm v[116:119],v[204:207],v[176:179],0
ld v224,s[56:59],s80 offen lds
mm v[116:119],v[200:203],v[180:183],v[116:119]
rt v[196:197],v240 offset:42240
rt v[198:199],v240 offset:43264
mm v[84:87],v[200:203],v[164:167],0
s_add_u32 m0, s83, 119296
wt lgkmcnt(4)
mm v[84:87],v[204:207],v[160:163],v[84:87]
rt v[200:201],v241 offset:33792
rt v[202:203],v241 offset:34816
)ASM" R"ASM(mm v[88:91],v[212:215],v[160:163],0
ld v225,s[56:59],s80 offen lds
mm v[88:91],v[208:211],v[164:167],v[88:91]
rt v[204:205],v241 offset:42240
rt v[206:207],v241 offset:43264
mm v[120:123],v[208:211],v[180:183],0
s_add_u32 m0, s83, 120448
s_xor_b32 s53, s53, s72
mm v[120:123],v[212:215],v[176:179],v[120:123]
rt v[208:209],v242 offset:33792
rt v[210:211],v242 offset:34816
mm v[124:127],v[220:223],v[176:179],0
ld v226,s[56:59],s80 offen lds
mm v[124:127],v[216:219],v[180:183],v[124:127]
rt v[212:213],v242 offset:42240
rt v[214:215],v242 offset:43264
mm v[92:95],v[216:219],v[164:167],0
wt vmcnt(7)
v_cmp_eq_u64 vcc, v[250:251], v[252:253]
s_nop 0
s_bitcmp1_b32 vcc_lo, 1
s_cbranch_scc0 .La5_LO_NC1
.La5_LO_KC1:
wt lgkmcnt(4)
mm v[92:95],v[220:223],v[160:163],v[92:95]
rt v[216:217],v243 offset:33792
rt v[218:219],v243 offset:34816
s_lshl_b32 s53, s53, 13
mm v[64:67],v[192:195],v[168:171],v[64:67]
ld a[0:3],v244,s[44:47],s53 offen offset:0 sc1
mm v[64:67],v[196:199],v[172:175],v[64:67]
rt v[220:221],v243 offset:42240
rt v[222:223],v243 offset:43264
mm v[96:99],v[196:199],v[188:191],v[96:99]
ld a[4:7],v245,s[44:47],s53 offen offset:0 sc1
mm v[96:99],v[192:195],v[184:187],v[96:99]
rt v[196:197],v240 offset:33920
rt v[198:199],v240 offset:34944
mm v[100:103],v[200:203],v[184:187],v[100:103]
ld a[8:11],v246,s[44:47],s53 offen offset:0 sc1
mm v[100:103],v[204:207],v[188:191],v[100:103]
rt v[192:193],v240 offset:42368
.La5_LW7_1:
s_cbranch_execz .La5_LW7_5
rt v[194:195],v240 offset:43392
mm v[68:71],v[204:207],v[172:175],v[68:71]
wt lgkmcnt(4)
s_bfe_u32 s54, s4, 262152
mm v[68:71],v[200:203],v[168:171],v[68:71]
rt v[204:205],v241 offset:33920
rt v[206:207],v241 offset:34944
mm v[72:75],v[208:211],v[168:171],v[72:75]
ld a[12:15],v247,s[44:47],s53 offen offset:0 sc1
mm v[72:75],v[212:215],v[172:175],v[72:75]
rt v[200:201],v241 offset:42368
rt v[202:203],v241 offset:43392
mm v[104:107],v[212:215],v[188:191],v[104:107]
ld a[16:19],v244,s[44:47],s53 offen offset:1024 sc1
mm v[104:107],v[208:211],v[184:187],v[104:107]
rt v[212:213],v242 offset:33920
rt v[214:215],v242 offset:34944
mm v[108:111],v[216:219],v[184:187],v[108:111]
ld a[20:23],v245,s[44:47],s53 offen offset:1024 sc1
mm v[108:111],v[220:223],v[188:191],v[108:111]
rt v[208:209],v242 offset:42368
rt v[210:211],v242 offset:43392
mm v[76:79],v[220:223],v[172:175],v[76:79]
s_nop 0
s_barrier
wt lgkmcnt(4)
mm v[76:79],v[216:219],v[168:171],v[76:79]
rt v[220:221],v243 offset:33920
rt v[222:223],v243 offset:34944
mm v[80:83],v[196:199],v[168:171],v[80:83]
ld a[24:27],v246,s[44:47],s53 offen offset:1024 sc1
mm v[80:83],v[192:195],v[172:175],v[80:83]
rt v[216:217],v243 offset:42368
rt v[218:219],v243 offset:43392
mm v[112:115],v[192:195],v[188:191],v[112:115]
ld a[28:31],v247,s[44:47],s53 offen offset:1024 sc1
mm v[112:115],v[196:199],v[184:187],v[112:115]
rt v[192:193],v240 offset:50688
rt v[194:195],v240 offset:51712
mm v[116:119],v[204:207],v[184:187],v[116:119]
ld a[32:35],v244,s[44:47],s53 offen offset:2048 sc1
mm v[116:119],v[200:203],v[188:191],v[116:119]
rt v[196:197],v240 offset:59136
rt v[198:199],v240 offset:60160
mm v[84:87],v[200:203],v[172:175],v[84:87]
wt lgkmcnt(4)
s_xor_b32 s54, s54, s72
mm v[84:87],v[204:207],v[168:171],v[84:87]
rt v[200:201],v241 offset:50688
rt v[202:203],v241 offset:51712
mm v[88:91],v[212:215],v[168:171],v[88:91]
ld a[36:39],v245,s[44:47],s53 offen offset:2048 sc1
mm v[88:91],v[208:211],v[172:175],v[88:91]
rt v[204:205],v241 offset:59136
rt v[206:207],v241 offset:60160
mm v[120:123],v[208:211],v[188:191],v[120:123]
)ASM" R"ASM(ld a[40:43],v246,s[44:47],s53 offen offset:2048 sc1
mm v[120:123],v[212:215],v[184:187],v[120:123]
rt v[208:209],v242 offset:50688
rt v[210:211],v242 offset:51712
mm v[124:127],v[220:223],v[184:187],v[124:127]
ld a[44:47],v247,s[44:47],s53 offen offset:2048 sc1
mm v[124:127],v[216:219],v[188:191],v[124:127]
rt v[212:213],v242 offset:59136
rt v[214:215],v242 offset:60160
mm v[92:95],v[216:219],v[172:175],v[92:95]
wt vmcnt(4) lgkmcnt(4)
s_lshl_b32 s54, s54, 13
mm v[92:95],v[220:223],v[168:171],v[92:95]
rt v[216:217],v243 offset:50688
rt v[218:219],v243 offset:51712
mm v[0:3],v[192:195],a[0:3],v[0:3]
s_bfe_u32 s55, s4, 262156
s_xor_b32 s55, s55, s72
mm v[0:3],v[196:199],a[16:19],v[0:3]
ld a[48:51],v244,s[44:47],s53 offen offset:3072 sc1
mm v[32:35],v[196:199],a[20:23],v[32:35]
s_lshl_b32 s55, s55, 13
s_bfe_u32 s52, s4, 262160
mm v[32:35],v[192:195],a[4:7],v[32:35]
rt v[220:221],v243 offset:59136
rt v[222:223],v243 offset:60160
mm v[64:67],v[192:195],a[8:11],v[64:67]
s_xor_b32 s52, s52, s72
s_lshl_b32 s52, s52, 13
mm v[64:67],v[196:199],a[24:27],v[64:67]
ld a[52:55],v245,s[44:47],s53 offen offset:3072 sc1
mm v[96:99],v[196:199],a[28:31],v[96:99]
mm v[96:99],v[192:195],a[12:15],v[96:99]
rt v[196:197],v240 offset:50816
rt v[198:199],v240 offset:51840
mm v[100:103],v[200:203],a[12:15],v[100:103]
mm v[100:103],v[204:207],a[28:31],v[100:103]
buffer_load_dwordx2 v[250:251], v248, s[64:67], s5 offen sc1
mm v[68:71],v[204:207],a[24:27],v[68:71]
mm v[68:71],v[200:203],a[8:11],v[68:71]
rt v[192:193],v240 offset:59264
rt v[194:195],v240 offset:60288
mm v[36:39],v[200:203],a[4:7],v[36:39]
mm v[36:39],v[204:207],a[20:23],v[36:39]
ld a[56:59],v246,s[44:47],s53 offen offset:3072 sc1
mm v[4:7],v[204:207],a[16:19],v[4:7]
wt lgkmcnt(4)
mm v[4:7],v[200:203],a[0:3],v[4:7]
rt v[204:205],v241 offset:50816
rt v[206:207],v241 offset:51840
mm v[8:11],v[208:211],a[0:3],v[8:11]
mm v[8:11],v[212:215],a[16:19],v[8:11]
ld a[60:63],v247,s[44:47],s53 offen offset:3072 sc1
mm v[40:43],v[212:215],a[20:23],v[40:43]
s_bfe_u32 s53, s4, 262164
s_xor_b32 s53, s53, s72
mm v[40:43],v[208:211],a[4:7],v[40:43]
rt v[200:201],v241 offset:59264
rt v[202:203],v241 offset:60288
mm v[72:75],v[208:211],a[8:11],v[72:75]
s_add_u32 m0, s83, 121472
s_lshl_b32 s53, s53, 13
mm v[72:75],v[212:215],a[24:27],v[72:75]
ld v227,s[56:59],s80 offen lds
mm v[104:107],v[212:215],a[28:31],v[104:107]
s_add_u32 s80, s80, 131072
mm v[104:107],v[208:211],a[12:15],v[104:107]
rt v[212:213],v242 offset:50816
rt v[214:215],v242 offset:51840
mm v[108:111],v[216:219],a[12:15],v[108:111]
s_add_u32 m0, s83, 67584
mm v[108:111],v[220:223],a[28:31],v[108:111]
ld v224,s[56:59],s80 offen lds
mm v[76:79],v[220:223],a[24:27],v[76:79]
mm v[76:79],v[216:219],a[8:11],v[76:79]
rt v[208:209],v242 offset:59264
rt v[210:211],v242 offset:60288
mm v[44:47],v[216:219],a[4:7],v[44:47]
s_add_u32 m0, s83, 68608
mm v[44:47],v[220:223],a[20:23],v[44:47]
ld v225,s[56:59],s80 offen lds
mm v[12:15],v[220:223],a[16:19],v[12:15]
wt lgkmcnt(4)
mm v[12:15],v[216:219],a[0:3],v[12:15]
rt v[220:221],v243 offset:50816
rt v[222:223],v243 offset:51840
mm v[16:19],v[196:199],a[0:3],v[16:19]
s_add_u32 m0, s83, 69760
mm v[16:19],v[192:195],a[16:19],v[16:19]
ld v226,s[56:59],s80 offen lds
mm v[48:51],v[192:195],a[20:23],v[48:51]
mm v[48:51],v[196:199],a[4:7],v[48:51]
rt v[216:217],v243 offset:59264
rt v[218:219],v243 offset:60288
mm v[80:83],v[196:199],a[8:11],v[80:83]
s_add_u32 m0, s83, 70784
s_nop 0
s_barrier
mm v[80:83],v[192:195],a[24:27],v[80:83]
ld v227,s[56:59],s80 offen lds
)ASM" R"ASM(mm v[112:115],v[192:195],a[28:31],v[112:115]
s_bfe_u32 s80, s4, 262156
s_xor_b32 s80, s80, s72
mm v[112:115],v[196:199],a[12:15],v[112:115]
rt v[192:193],v240 offset:0
rt v[194:195],v240 offset:1024
mm v[116:119],v[204:207],a[12:15],v[116:119]
s_add_u32 m0, s83, 84480
s_lshl_b32 s80, s80, 18
mm v[116:119],v[200:203],a[28:31],v[116:119]
ld v224,s[56:59],s80 offen lds
mm v[84:87],v[200:203],a[24:27],v[84:87]
mm v[84:87],v[204:207],a[8:11],v[84:87]
rt v[196:197],v240 offset:8448
rt v[198:199],v240 offset:9472
mm v[52:55],v[204:207],a[4:7],v[52:55]
s_add_u32 m0, s83, 85504
mm v[52:55],v[200:203],a[20:23],v[52:55]
ld v225,s[56:59],s80 offen lds
mm v[20:23],v[200:203],a[16:19],v[20:23]
wt lgkmcnt(4)
mm v[20:23],v[204:207],a[0:3],v[20:23]
rt v[200:201],v241 offset:0
rt v[202:203],v241 offset:1024
mm v[24:27],v[212:215],a[0:3],v[24:27]
s_add_u32 m0, s83, 86656
mm v[24:27],v[208:211],a[16:19],v[24:27]
ld v226,s[56:59],s80 offen lds
mm v[56:59],v[208:211],a[20:23],v[56:59]
wt vmcnt(10)
v_cmp_eq_u64 vcc, v[250:251], v[252:253]
mm v[56:59],v[212:215],a[4:7],v[56:59]
rt v[204:205],v241 offset:8448
rt v[206:207],v241 offset:9472
mm v[88:91],v[212:215],a[8:11],v[88:91]
s_add_u32 m0, s83, 87680
mm v[88:91],v[208:211],a[24:27],v[88:91]
ld v227,s[56:59],s80 offen lds
mm v[120:123],v[208:211],a[28:31],v[120:123]
s_bitcmp1_b32 vcc_lo, 2
s_cbranch_scc0 .La5_LO_NC2
.La5_LO_KC2:
mm v[120:123],v[212:215],a[12:15],v[120:123]
rt v[208:209],v242 offset:0
rt v[210:211],v242 offset:1024
mm v[124:127],v[220:223],a[12:15],v[124:127]
s_add_u32 s80, s80, 131072
mm v[124:127],v[216:219],a[28:31],v[124:127]
ld a[12:15],v244,s[44:47],s54 offen offset:0 sc1
mm v[92:95],v[216:219],a[24:27],v[92:95]
mm v[92:95],v[220:223],a[8:11],v[92:95]
rt v[212:213],v242 offset:8448
rt v[214:215],v242 offset:9472
mm v[60:63],v[220:223],a[4:7],v[60:63]
mm v[60:63],v[216:219],a[20:23],v[60:63]
mm v[28:31],v[216:219],a[16:19],v[28:31]
wt vmcnt(10) lgkmcnt(4)
mm v[28:31],v[220:223],a[0:3],v[28:31]
rt v[216:217],v243 offset:0
rt v[218:219],v243 offset:1024
mm v[0:3],v[192:195],a[32:35],v[0:3]
mm v[0:3],v[196:199],a[48:51],v[0:3]
ld a[0:3],v245,s[44:47],s54 offen offset:0 sc1
mm v[32:35],v[196:199],a[52:55],v[32:35]
mm v[32:35],v[192:195],a[36:39],v[32:35]
rt v[220:221],v243 offset:8448
rt v[222:223],v243 offset:9472
mm v[64:67],v[192:195],a[40:43],v[64:67]
mm v[64:67],v[196:199],a[56:59],v[64:67]
ld a[4:7],v246,s[44:47],s54 offen offset:0 sc1
mm v[96:99],v[196:199],a[60:63],v[96:99]
mm v[96:99],v[192:195],a[44:47],v[96:99]
rt v[196:197],v240 offset:128
rt v[198:199],v240 offset:1152
mm v[100:103],v[200:203],a[44:47],v[100:103]
s_mov_b32 s86, 0x05040100
s_mov_b32 s87, 0x07060302
mm v[100:103],v[204:207],a[60:63],v[100:103]
ld a[8:11],v247,s[44:47],s54 offen offset:0 sc1
mm v[68:71],v[204:207],a[56:59],v[68:71]
s_nop 0
ar v230,a128
mm v[68:71],v[200:203],a[40:43],v[68:71]
rt v[192:193],v240 offset:8576
rt v[194:195],v240 offset:9600
mm v[36:39],v[200:203],a[36:39],v[36:39]
v_add_u32 v240, 67584, v240
ar v231,a132
mm v[36:39],v[204:207],a[52:55],v[36:39]
ld a[16:19],v244,s[44:47],s54 offen offset:1024 sc1
mm v[4:7],v[204:207],a[48:51],v[4:7]
wt lgkmcnt(4)
v_perm_b32 v232, v231, v230, s86
mm v[4:7],v[200:203],a[32:35],v[4:7]
rt v[204:205],v241 offset:128
rt v[206:207],v241 offset:1152
mm v[8:11],v[208:211],a[32:35],v[8:11]
v_perm_b32 v233, v231, v230, s87
aw a96,v232
mm v[8:11],v[212:215],a[48:51],v[8:11]
)ASM" R"ASM(ld a[20:23],v245,s[44:47],s54 offen offset:1024 sc1
mm v[40:43],v[212:215],a[52:55],v[40:43]
aw a100,v233
ar v234,a136
mm v[40:43],v[208:211],a[36:39],v[40:43]
rt v[200:201],v241 offset:8576
rt v[202:203],v241 offset:9600
mm v[72:75],v[208:211],a[40:43],v[72:75]
v_add_u32 v241, 67584, v241
ar v235,a140
mm v[72:75],v[212:215],a[56:59],v[72:75]
ld a[24:27],v246,s[44:47],s54 offen offset:1024 sc1
mm v[104:107],v[212:215],a[60:63],v[104:107]
v_perm_b32 v236, v235, v234, s86
v_perm_b32 v237, v235, v234, s87
mm v[104:107],v[208:211],a[44:47],v[104:107]
rt v[212:213],v242 offset:128
rt v[214:215],v242 offset:1152
mm v[108:111],v[216:219],a[44:47],v[108:111]
aw a97,v236
aw a101,v237
mm v[108:111],v[220:223],a[60:63],v[108:111]
ld a[28:31],v247,s[44:47],s54 offen offset:1024 sc1
mm v[76:79],v[220:223],a[56:59],v[76:79]
ar v230,a144
ar v231,a148
mm v[76:79],v[216:219],a[40:43],v[76:79]
rt v[208:209],v242 offset:8576
rt v[210:211],v242 offset:9600
mm v[44:47],v[216:219],a[36:39],v[44:47]
v_add_u32 v242, 67584, v242
v_perm_b32 v232, v231, v230, s86
mm v[44:47],v[220:223],a[52:55],v[44:47]
v_perm_b32 v233, v231, v230, s87
aw a98,v232
mm v[12:15],v[220:223],a[48:51],v[12:15]
wt lgkmcnt(4)
aw a102,v233
mm v[12:15],v[216:219],a[32:35],v[12:15]
rt v[220:221],v243 offset:128
rt v[222:223],v243 offset:1152
mm v[16:19],v[196:199],a[32:35],v[16:19]
s_add_u32 m0, s83, 101376
ar v234,a152
mm v[16:19],v[192:195],a[48:51],v[16:19]
ld v224,s[56:59],s80 offen lds
mm v[48:51],v[192:195],a[52:55],v[48:51]
ar v235,a156
v_perm_b32 v236, v235, v234, s86
mm v[48:51],v[196:199],a[36:39],v[48:51]
rt v[216:217],v243 offset:8576
rt v[218:219],v243 offset:9600
mm v[80:83],v[196:199],a[40:43],v[80:83]
s_add_u32 m0, s83, 102400
v_add_u32 v243, 67584, v243
mm v[80:83],v[192:195],a[56:59],v[80:83]
ld v225,s[56:59],s80 offen lds
mm v[112:115],v[192:195],a[60:63],v[112:115]
wt vmcnt(14)
s_barrier
mm v[112:115],v[196:199],a[44:47],v[112:115]
rt v[192:193],v240 offset:50688
rt v[194:195],v240 offset:51712
mm v[116:119],v[204:207],a[44:47],v[116:119]
s_add_u32 m0, s83, 103552
v_perm_b32 v237, v235, v234, s87
mm v[116:119],v[200:203],a[60:63],v[116:119]
ld v226,s[56:59],s80 offen lds
mm v[84:87],v[200:203],a[56:59],v[84:87]
aw a99,v236
aw a103,v237
mm v[84:87],v[204:207],a[40:43],v[84:87]
rt v[196:197],v240 offset:59136
rt v[198:199],v240 offset:60160
mm v[52:55],v[204:207],a[36:39],v[52:55]
ar v230,a129
ar v231,a133
mm v[52:55],v[200:203],a[52:55],v[52:55]
ld a[64:67],v244,s[44:47],s54 offen offset:2048 sc1
mm v[20:23],v[200:203],a[48:51],v[20:23]
wt lgkmcnt(4)
v_perm_b32 v232, v231, v230, s86
mm v[20:23],v[204:207],a[32:35],v[20:23]
rt v[200:201],v241 offset:50688
rt v[202:203],v241 offset:51712
mm v[24:27],v[212:215],a[32:35],v[24:27]
v_perm_b32 v233, v231, v230, s87
aw a104,v232
mm v[24:27],v[208:211],a[48:51],v[24:27]
ld a[68:71],v245,s[44:47],s54 offen offset:2048 sc1
mm v[56:59],v[208:211],a[52:55],v[56:59]
aw a108,v233
ar v234,a137
mm v[56:59],v[212:215],a[36:39],v[56:59]
rt v[204:205],v241 offset:59136
rt v[206:207],v241 offset:60160
mm v[88:91],v[212:215],a[40:43],v[88:91]
ar v235,a141
v_perm_b32 v236, v235, v234, s86
mm v[88:91],v[208:211],a[56:59],v[88:91]
)ASM" R"ASM(ld a[72:75],v246,s[44:47],s54 offen offset:2048 sc1
mm v[120:123],v[208:211],a[60:63],v[120:123]
v_perm_b32 v237, v235, v234, s87
aw a105,v236
mm v[120:123],v[212:215],a[44:47],v[120:123]
rt v[208:209],v242 offset:50688
rt v[210:211],v242 offset:51712
mm v[124:127],v[220:223],a[44:47],v[124:127]
aw a109,v237
ar v230,a145
mm v[124:127],v[216:219],a[60:63],v[124:127]
buffer_load_dwordx2 v[250:251], v248, s[64:67], s5 offen sc1
mm v[92:95],v[216:219],a[56:59],v[92:95]
ar v231,a149
v_perm_b32 v232, v231, v230, s86
mm v[92:95],v[220:223],a[40:43],v[92:95]
rt v[212:213],v242 offset:59136
rt v[214:215],v242 offset:60160
mm v[60:63],v[220:223],a[36:39],v[60:63]
v_perm_b32 v233, v231, v230, s87
aw a106,v232
mm v[60:63],v[216:219],a[52:55],v[60:63]
ld a[36:39],v247,s[44:47],s54 offen offset:2048 sc1
mm v[28:31],v[216:219],a[48:51],v[28:31]
wt vmcnt(8) lgkmcnt(4)
aw a110,v233
mm v[28:31],v[220:223],a[32:35],v[28:31]
rt v[216:217],v243 offset:50688
rt v[218:219],v243 offset:51712
mm v[0:3],v[192:195],a[12:15],v[0:3]
ar v234,a153
ar v235,a157
mm v[0:3],v[196:199],a[16:19],v[0:3]
ld a[32:35],v244,s[44:47],s54 offen offset:3072 sc1
mm v[32:35],v[196:199],a[20:23],v[32:35]
v_perm_b32 v236, v235, v234, s86
v_perm_b32 v237, v235, v234, s87
mm v[32:35],v[192:195],a[0:3],v[32:35]
rt v[220:221],v243 offset:59136
rt v[222:223],v243 offset:60160
mm v[64:67],v[192:195],a[4:7],v[64:67]
aw a107,v236
aw a111,v237
mm v[64:67],v[196:199],a[24:27],v[64:67]
ld a[40:43],v245,s[44:47],s54 offen offset:3072 sc1
mm v[96:99],v[196:199],a[28:31],v[96:99]
ar v230,a130
ar v231,a134
mm v[96:99],v[192:195],a[8:11],v[96:99]
rt v[196:197],v240 offset:50816
rt v[198:199],v240 offset:51840
mm v[100:103],v[200:203],a[8:11],v[100:103]
v_perm_b32 v232, v231, v230, s86
v_perm_b32 v233, v231, v230, s87
mm v[100:103],v[204:207],a[28:31],v[100:103]
ld a[44:47],v246,s[44:47],s54 offen offset:3072 sc1
mm v[68:71],v[204:207],a[24:27],v[68:71]
aw a112,v232
aw a116,v233
mm v[68:71],v[200:203],a[4:7],v[68:71]
rt v[192:193],v240 offset:59264
rt v[194:195],v240 offset:60288
mm v[36:39],v[200:203],a[0:3],v[36:39]
ar v234,a138
ar v235,a142
mm v[36:39],v[204:207],a[20:23],v[36:39]
ld a[48:51],v247,s[44:47],s54 offen offset:3072 sc1
mm v[4:7],v[204:207],a[16:19],v[4:7]
wt lgkmcnt(4)
s_bfe_u32 s54, s4, 262168
mm v[4:7],v[200:203],a[12:15],v[4:7]
rt v[204:205],v241 offset:50816
rt v[206:207],v241 offset:51840
mm v[8:11],v[208:211],a[12:15],v[8:11]
s_add_u32 m0, s83, 104576
s_xor_b32 s54, s54, s72
mm v[8:11],v[212:215],a[16:19],v[8:11]
ld v227,s[56:59],s80 offen lds
mm v[40:43],v[212:215],a[20:23],v[40:43]
s_bitcmp1_b32 vcc_lo, 3
s_cbranch_scc0 .La5_LO_NC3
.La5_LO_KC3:
mm v[40:43],v[208:211],a[0:3],v[40:43]
rt v[200:201],v241 offset:59264
rt v[202:203],v241 offset:60288
mm v[72:75],v[208:211],a[4:7],v[72:75]
s_bfe_u32 s80, s4, 262160
s_xor_b32 s80, s80, s72
mm v[72:75],v[212:215],a[24:27],v[72:75]
ld a[52:55],v244,s[44:47],s55 offen offset:0 sc1
mm v[104:107],v[212:215],a[28:31],v[104:107]
s_lshl_b32 s80, s80, 18
s_lshl_b32 s54, s54, 13
mm v[104:107],v[208:211],a[8:11],v[104:107]
rt v[212:213],v242 offset:50816
rt v[214:215],v242 offset:51840
mm v[108:111],v[216:219],a[8:11],v[108:111]
v_perm_b32 v236, v235, v234, s86
v_perm_b32 v237, v235, v234, s87
mm v[108:111],v[220:223],a[28:31],v[108:111]
)ASM" R"ASM(ld a[56:59],v245,s[44:47],s55 offen offset:0 sc1
mm v[76:79],v[220:223],a[24:27],v[76:79]
aw a113,v236
aw a117,v237
mm v[76:79],v[216:219],a[4:7],v[76:79]
rt v[208:209],v242 offset:59264
rt v[210:211],v242 offset:60288
mm v[44:47],v[216:219],a[0:3],v[44:47]
ar v230,a146
ar v231,a150
mm v[44:47],v[220:223],a[20:23],v[44:47]
v_perm_b32 v232, v231, v230, s86
v_perm_b32 v233, v231, v230, s87
mm v[12:15],v[220:223],a[16:19],v[12:15]
wt lgkmcnt(4)
aw a114,v232
mm v[12:15],v[216:219],a[12:15],v[12:15]
rt v[220:221],v243 offset:50816
rt v[222:223],v243 offset:51840
mm v[16:19],v[196:199],a[12:15],v[16:19]
aw a118,v233
ar v234,a154
mm v[16:19],v[192:195],a[16:19],v[16:19]
ld a[60:63],v246,s[44:47],s55 offen offset:0 sc1
mm v[48:51],v[192:195],a[20:23],v[48:51]
ar v235,a158
v_perm_b32 v236, v235, v234, s86
mm v[48:51],v[196:199],a[0:3],v[48:51]
rt v[216:217],v243 offset:59264
rt v[218:219],v243 offset:60288
mm v[80:83],v[196:199],a[4:7],v[80:83]
v_perm_b32 v237, v235, v234, s87
aw a115,v236
mm v[80:83],v[192:195],a[24:27],v[80:83]
ld a[76:79],v247,s[44:47],s55 offen offset:0 sc1
mm v[112:115],v[192:195],a[28:31],v[112:115]
aw a119,v237
ar v230,a131
mm v[112:115],v[196:199],a[8:11],v[112:115]
rt v[192:193],v240 offset:0
rt v[194:195],v240 offset:1024
mm v[116:119],v[204:207],a[8:11],v[116:119]
ar v231,a135
v_perm_b32 v232, v231, v230, s86
mm v[116:119],v[200:203],a[28:31],v[116:119]
ld a[80:83],v244,s[44:47],s55 offen offset:1024 sc1
mm v[84:87],v[200:203],a[24:27],v[84:87]
v_perm_b32 v233, v231, v230, s87
aw a120,v232
mm v[84:87],v[204:207],a[4:7],v[84:87]
rt v[196:197],v240 offset:8448
rt v[198:199],v240 offset:9472
mm v[52:55],v[204:207],a[0:3],v[52:55]
s_load_dwordx2 s[16:17], s[0:1], 0
s_load_dwordx4 s[36:39], s[0:1], 80
mm v[52:55],v[200:203],a[20:23],v[52:55]
ld a[84:87],v245,s[44:47],s55 offen offset:1024 sc1
mm v[20:23],v[200:203],a[16:19],v[20:23]
wt lgkmcnt(4)
aw a124,v233
mm v[20:23],v[204:207],a[12:15],v[20:23]
rt v[200:201],v241 offset:0
rt v[202:203],v241 offset:1024
mm v[24:27],v[212:215],a[12:15],v[24:27]
ar v234,a139
ar v235,a143
mm v[24:27],v[208:211],a[16:19],v[24:27]
ld a[88:91],v246,s[44:47],s55 offen offset:1024 sc1
mm v[56:59],v[208:211],a[20:23],v[56:59]
v_perm_b32 v236, v235, v234, s86
v_perm_b32 v237, v235, v234, s87
mm v[56:59],v[212:215],a[0:3],v[56:59]
rt v[204:205],v241 offset:8448
rt v[206:207],v241 offset:9472
mm v[88:91],v[212:215],a[4:7],v[88:91]
aw a121,v236
aw a125,v237
mm v[88:91],v[208:211],a[24:27],v[88:91]
.La5_LW7_2:
s_cbranch_execz .La5_LW7_6
ld a[92:95],v247,s[44:47],s55 offen offset:1024 sc1
mm v[120:123],v[208:211],a[28:31],v[120:123]
ar v230,a147
ar v231,a151
mm v[120:123],v[212:215],a[8:11],v[120:123]
rt v[208:209],v242 offset:0
rt v[210:211],v242 offset:1024
mm v[124:127],v[220:223],a[8:11],v[124:127]
s_add_u32 m0, s83, 16896
v_perm_b32 v232, v231, v230, s86
mm v[124:127],v[216:219],a[28:31],v[124:127]
ld v224,s[56:59],s80 offen lds
mm v[92:95],v[216:219],a[24:27],v[92:95]
v_perm_b32 v233, v231, v230, s87
aw a122,v232
mm v[92:95],v[220:223],a[4:7],v[92:95]
rt v[212:213],v242 offset:8448
rt v[214:215],v242 offset:9472
mm v[60:63],v[220:223],a[0:3],v[60:63]
aw a126,v233
)ASM" R"ASM(ar v234,a155
mm v[60:63],v[216:219],a[20:23],v[60:63]
ar v235,a159
v_perm_b32 v236, v235, v234, s86
mm v[28:31],v[216:219],a[16:19],v[28:31]
wt vmcnt(10) lgkmcnt(4)
v_perm_b32 v237, v235, v234, s87
mm v[28:31],v[220:223],a[12:15],v[28:31]
rt v[216:217],v243 offset:0
rt v[218:219],v243 offset:1024
mm v[0:3],v[192:195],a[64:67],v[0:3]
s_add_u32 m0, s83, 17920
aw a123,v236
mm v[0:3],v[196:199],a[32:35],v[0:3]
ld v225,s[56:59],s80 offen lds
mm v[32:35],v[196:199],a[40:43],v[32:35]
aw a127,v237
ar v230,a160
mm v[32:35],v[192:195],a[68:71],v[32:35]
rt v[220:221],v243 offset:8448
rt v[222:223],v243 offset:9472
mm v[64:67],v[192:195],a[72:75],v[64:67]
s_add_u32 m0, s83, 19072
ar v231,a164
mm v[64:67],v[196:199],a[44:47],v[64:67]
ld v226,s[56:59],s80 offen lds
mm v[96:99],v[196:199],a[48:51],v[96:99]
v_perm_b32 v232, v231, v230, s86
v_perm_b32 v233, v231, v230, s87
mm v[96:99],v[192:195],a[36:39],v[96:99]
rt v[196:197],v240 offset:128
rt v[198:199],v240 offset:1152
mm v[100:103],v[200:203],a[36:39],v[100:103]
aw a128,v232
aw a132,v233
mm v[100:103],v[204:207],a[48:51],v[100:103]
ld v[136:139],v244,s[44:47],s55 offen offset:2048 sc1
mm v[68:71],v[204:207],a[44:47],v[68:71]
ar v234,a168
ar v235,a172
mm v[68:71],v[200:203],a[72:75],v[68:71]
rt v[192:193],v240 offset:8576
rt v[194:195],v240 offset:9600
mm v[36:39],v[200:203],a[68:71],v[36:39]
v_perm_b32 v236, v235, v234, s86
v_perm_b32 v237, v235, v234, s87
mm v[36:39],v[204:207],a[40:43],v[36:39]
ld v[140:143],v245,s[44:47],s55 offen offset:2048 sc1
mm v[4:7],v[204:207],a[32:35],v[4:7]
wt lgkmcnt(4)
aw a129,v236
mm v[4:7],v[200:203],a[64:67],v[4:7]
rt v[204:205],v241 offset:128
rt v[206:207],v241 offset:1152
mm v[8:11],v[208:211],a[64:67],v[8:11]
aw a133,v237
ar v230,a176
mm v[8:11],v[212:215],a[32:35],v[8:11]
ld v[152:155],v246,s[44:47],s55 offen offset:2048 sc1
mm v[40:43],v[212:215],a[40:43],v[40:43]
ar v231,a180
v_perm_b32 v232, v231, v230, s86
mm v[40:43],v[208:211],a[68:71],v[40:43]
rt v[200:201],v241 offset:8576
rt v[202:203],v241 offset:9600
mm v[72:75],v[208:211],a[72:75],v[72:75]
v_perm_b32 v233, v231, v230, s87
aw a130,v232
mm v[72:75],v[212:215],a[44:47],v[72:75]
ld v[156:159],v247,s[44:47],s55 offen offset:2048 sc1
mm v[104:107],v[212:215],a[48:51],v[104:107]
aw a134,v233
ar v234,a184
mm v[104:107],v[208:211],a[36:39],v[104:107]
rt v[212:213],v242 offset:128
rt v[214:215],v242 offset:1152
mm v[108:111],v[216:219],a[36:39],v[108:111]
ar v235,a188
v_perm_b32 v236, v235, v234, s86
mm v[108:111],v[220:223],a[48:51],v[108:111]
ld v[168:171],v244,s[44:47],s55 offen offset:3072 sc1
mm v[76:79],v[220:223],a[44:47],v[76:79]
v_perm_b32 v237, v235, v234, s87
aw a131,v236
mm v[76:79],v[216:219],a[72:75],v[76:79]
rt v[208:209],v242 offset:8576
rt v[210:211],v242 offset:9600
mm v[44:47],v[216:219],a[68:71],v[44:47]
aw a135,v237
ar v230,a161
mm v[44:47],v[220:223],a[40:43],v[44:47]
s_nop 0
v_cmp_eq_u64 vcc, v[250:251], v[252:253]
mm v[12:15],v[220:223],a[32:35],v[12:15]
wt lgkmcnt(4)
ar v231,a165
mm v[12:15],v[216:219],a[64:67],v[12:15]
rt v[220:221],v243 offset:128
rt v[222:223],v243 offset:1152
mm v[16:19],v[196:199],a[64:67],v[16:19]
)ASM" R"ASM(v_perm_b32 v232, v231, v230, s86
v_perm_b32 v233, v231, v230, s87
mm v[16:19],v[192:195],a[32:35],v[16:19]
ld v[172:175],v245,s[44:47],s55 offen offset:3072 sc1
mm v[48:51],v[192:195],a[40:43],v[48:51]
aw a136,v232
aw a140,v233
mm v[48:51],v[196:199],a[68:71],v[48:51]
rt v[216:217],v243 offset:8576
rt v[218:219],v243 offset:9600
mm v[80:83],v[196:199],a[72:75],v[80:83]
ar v234,a169
ar v235,a173
mm v[80:83],v[192:195],a[44:47],v[80:83]
ld v[184:187],v246,s[44:47],s55 offen offset:3072 sc1
mm v[112:115],v[192:195],a[48:51],v[112:115]
wt vmcnt(18) lgkmcnt(0)
s_barrier
mm v[112:115],v[196:199],a[36:39],v[112:115]
rt v[192:193],v240 offset:16896
rt v[194:195],v240 offset:17920
mm v[116:119],v[204:207],a[36:39],v[116:119]
s_xor_b32 s90, s72, 0
s_lshl_b32 s92, s76, 5
mm v[116:119],v[200:203],a[48:51],v[116:119]
buffer_load_dwordx2 v[250:251], v248, s[64:67], s5 offen sc1
mm v[84:87],v[200:203],a[44:47],v[84:87]
s_add_u32 s92, s92, s90
s_lshl2_add_u32 s92, s92, s3
mm v[84:87],v[204:207],a[72:75],v[84:87]
rt v[196:197],v240 offset:25344
rt v[198:199],v240 offset:26368
mm v[52:55],v[204:207],a[68:71],v[52:55]
s_lshl_b32 s92, s92, 12
v_perm_b32 v236, v235, v234, s86
mm v[52:55],v[200:203],a[40:43],v[52:55]
ld v[188:191],v247,s[44:47],s55 offen offset:3072 sc1
mm v[20:23],v[200:203],a[32:35],v[20:23]
s_nop 0
s_bfe_u32 s55, s4, 262172
mm v[20:23],v[204:207],a[64:67],v[20:23]
rt v[200:201],v241 offset:16896
rt v[202:203],v241 offset:17920
mm v[24:27],v[212:215],a[64:67],v[24:27]
s_add_u32 m0, s83, 20096
s_xor_b32 s55, s55, s72
mm v[24:27],v[208:211],a[32:35],v[24:27]
ld v227,s[56:59],s80 offen lds
mm v[56:59],v[208:211],a[40:43],v[56:59]
s_add_u32 s80, s80, 131072
s_lshl_b32 s55, s55, 13
mm v[56:59],v[212:215],a[68:71],v[56:59]
rt v[204:205],v241 offset:25344
rt v[206:207],v241 offset:26368
mm v[88:91],v[212:215],a[72:75],v[88:91]
s_add_u32 m0, s83, 33792
v_perm_b32 v237, v235, v234, s87
mm v[88:91],v[208:211],a[44:47],v[88:91]
ld v224,s[56:59],s80 offen lds
mm v[120:123],v[208:211],a[48:51],v[120:123]
s_add_u32 s68, s16, s92
s_addc_u32 s69, s17, 0
mm v[120:123],v[212:215],a[36:39],v[120:123]
rt v[208:209],v242 offset:16896
rt v[210:211],v242 offset:17920
mm v[124:127],v[220:223],a[36:39],v[124:127]
s_add_u32 m0, s83, 34816
s_and_b32 s69, s69, 0xffff
mm v[124:127],v[216:219],a[48:51],v[124:127]
ld v225,s[56:59],s80 offen lds
mm v[92:95],v[216:219],a[44:47],v[92:95]
s_mov_b32 s70, 4096
s_mov_b32 s71, 0x20000
mm v[92:95],v[220:223],a[72:75],v[92:95]
rt v[212:213],v242 offset:25344
rt v[214:215],v242 offset:26368
mm v[60:63],v[220:223],a[68:71],v[60:63]
s_add_u32 m0, s83, 35968
aw a137,v236
mm v[60:63],v[216:219],a[40:43],v[60:63]
ld v226,s[56:59],s80 offen lds
mm v[28:31],v[216:219],a[32:35],v[28:31]
wt vmcnt(16) lgkmcnt(4)
aw a141,v237
mm v[28:31],v[220:223],a[64:67],v[28:31]
rt v[216:217],v243 offset:16896
rt v[218:219],v243 offset:17920
mm v[0:3],v[192:195],a[52:55],v[0:3]
s_add_u32 m0, s83, 36992
ar v230,a177
mm v[0:3],v[196:199],a[80:83],v[0:3]
ld v227,s[56:59],s80 offen lds
mm v[32:35],v[196:199],a[84:87],v[32:35]
s_bitcmp1_b32 vcc_lo, 4
s_cbranch_scc0 .La5_LO_NC4
.La5_LO_KC4:
mm v[32:35],v[192:195],a[56:59],v[32:35]
rt v[220:221],v243 offset:25344
rt v[222:223],v243 offset:26368
mm v[64:67],v[192:195],a[60:63],v[64:67]
)ASM" R"ASM(s_bfe_u32 s80, s4, 262164
s_xor_b32 s80, s80, s72
mm v[64:67],v[196:199],a[88:91],v[64:67]
ld a[0:3],v244,s[44:47],s52 offen offset:0 sc1
mm v[96:99],v[196:199],a[92:95],v[96:99]
s_lshl_b32 s80, s80, 18
ar v231,a181
mm v[96:99],v[192:195],a[76:79],v[96:99]
rt v[196:197],v240 offset:17024
rt v[198:199],v240 offset:18048
mm v[100:103],v[200:203],a[76:79],v[100:103]
s_add_u32 s48, s36, 0
s_addc_u32 s49, s37, 0
mm v[100:103],v[204:207],a[92:95],v[100:103]
ld a[4:7],v245,s[44:47],s52 offen offset:0 sc1
mm v[68:71],v[204:207],a[88:91],v[68:71]
s_and_b32 s49, s49, 0xffff
s_mov_b32 s50, 2048
mm v[68:71],v[200:203],a[60:63],v[68:71]
rt v[192:193],v240 offset:25472
rt v[194:195],v240 offset:26496
mm v[36:39],v[200:203],a[56:59],v[36:39]
s_mov_b32 s51, 0x20000
v_and_b32 v238, 0xf0, v255
mm v[36:39],v[204:207],a[84:87],v[36:39]
ld a[8:11],v246,s[44:47],s52 offen offset:0 sc1
mm v[4:7],v[204:207],a[80:83],v[4:7]
wt lgkmcnt(4)
v_lshlrev_b32 v238, 2, v238
mm v[4:7],v[200:203],a[52:55],v[4:7]
rt v[204:205],v241 offset:17024
rt v[206:207],v241 offset:18048
mm v[8:11],v[208:211],a[52:55],v[8:11]
v_bfe_u32 v249, v255, 8, 1
v_lshl_add_u32 v238, v249, 5, v238
mm v[8:11],v[212:215],a[80:83],v[8:11]
ld a[12:15],v247,s[44:47],s52 offen offset:0 sc1
mm v[40:43],v[212:215],a[84:87],v[40:43]
v_lshrrev_b32_e64 v249, 9, v255
v_lshl_add_u32 v238, v249, 4, v238
mm v[40:43],v[208:211],a[56:59],v[40:43]
rt v[200:201],v241 offset:25472
rt v[202:203],v241 offset:26496
mm v[72:75],v[208:211],a[60:63],v[72:75]
s_lshr_b32 s23, s72, 3
s_lshl3_add_u32 s92, s23, s76
mm v[72:75],v[212:215],a[88:91],v[72:75]
ld a[16:19],v244,s[44:47],s52 offen offset:1024 sc1
mm v[104:107],v[212:215],a[92:95],v[104:107]
s_lshl_b32 s92, s92, 19
s_lshl_b32 s97, s3, 16
mm v[104:107],v[208:211],a[76:79],v[104:107]
rt v[212:213],v242 offset:17024
rt v[214:215],v242 offset:18048
mm v[108:111],v[216:219],a[76:79],v[108:111]
s_add_u32 s94, s92, s97
s_add_u32 s95, s94, 32768
mm v[108:111],v[220:223],a[92:95],v[108:111]
ld a[20:23],v245,s[44:47],s52 offen offset:1024 sc1
mm v[76:79],v[220:223],a[88:91],v[76:79]
s_sub_u32 s96, s92, s97
s_add_u32 s96, s96, 458752
mm v[76:79],v[216:219],a[60:63],v[76:79]
rt v[208:209],v242 offset:25472
rt v[210:211],v242 offset:26496
mm v[44:47],v[216:219],a[56:59],v[44:47]
s_add_u32 s97, s96, 32768
s_lshl3_add_u32 s92, s23, s101
mm v[44:47],v[220:223],a[84:87],v[44:47]
s_lshl_b32 s85, s76, 15
s_lshl3_add_u32 s92, s92, s85
mm v[12:15],v[220:223],a[80:83],v[12:15]
wt lgkmcnt(4)
s_add_u32 s92, s92, 2097152
mm v[12:15],v[216:219],a[52:55],v[12:15]
rt v[220:221],v243 offset:17024
rt v[222:223],v243 offset:18048
mm v[16:19],v[196:199],a[52:55],v[16:19]
v_perm_b32 v232, v231, v230, s86
v_perm_b32 v233, v231, v230, s87
mm v[16:19],v[192:195],a[80:83],v[16:19]
ld a[24:27],v246,s[44:47],s52 offen offset:1024 sc1
mm v[48:51],v[192:195],a[84:87],v[48:51]
aw a138,v232
aw a142,v233
mm v[48:51],v[196:199],a[56:59],v[48:51]
rt v[216:217],v243 offset:25472
rt v[218:219],v243 offset:26496
mm v[80:83],v[196:199],a[60:63],v[80:83]
ar v234,a185
ar v235,a189
mm v[80:83],v[192:195],a[88:91],v[80:83]
ld a[28:31],v247,s[44:47],s52 offen offset:1024 sc1
mm v[112:115],v[192:195],a[92:95],v[112:115]
v_perm_b32 v236, v235, v234, s86
v_perm_b32 v237, v235, v234, s87
mm v[112:115],v[196:199],a[76:79],v[112:115]
rt v[192:193],v240 offset:33792
rt v[194:195],v240 offset:34816
mm v[116:119],v[204:207],a[76:79],v[116:119]
)ASM" R"ASM(s_add_u32 m0, s83, 50688
aw a139,v236
mm v[116:119],v[200:203],a[92:95],v[116:119]
ld v224,s[56:59],s80 offen lds
mm v[84:87],v[200:203],a[88:91],v[84:87]
aw a143,v237
ar v230,a162
mm v[84:87],v[204:207],a[60:63],v[84:87]
rt v[196:197],v240 offset:42240
rt v[198:199],v240 offset:43264
mm v[52:55],v[204:207],a[56:59],v[52:55]
s_add_u32 m0, s83, 51712
ar v231,a166
mm v[52:55],v[200:203],a[84:87],v[52:55]
ld v225,s[56:59],s80 offen lds
mm v[20:23],v[200:203],a[80:83],v[20:23]
wt lgkmcnt(4)
v_perm_b32 v232, v231, v230, s86
mm v[20:23],v[204:207],a[52:55],v[20:23]
rt v[200:201],v241 offset:33792
rt v[202:203],v241 offset:34816
mm v[24:27],v[212:215],a[52:55],v[24:27]
s_add_u32 m0, s83, 52864
v_perm_b32 v233, v231, v230, s87
mm v[24:27],v[208:211],a[80:83],v[24:27]
ld v226,s[56:59],s80 offen lds
mm v[56:59],v[208:211],a[84:87],v[56:59]
aw a144,v232
aw a148,v233
mm v[56:59],v[212:215],a[56:59],v[56:59]
rt v[204:205],v241 offset:42240
rt v[206:207],v241 offset:43264
mm v[88:91],v[212:215],a[60:63],v[88:91]
ar v234,a170
ar v235,a174
mm v[88:91],v[208:211],a[88:91],v[88:91]
ld a[32:35],v244,s[44:47],s52 offen offset:2048 sc1
mm v[120:123],v[208:211],a[92:95],v[120:123]
v_perm_b32 v236, v235, v234, s86
v_perm_b32 v237, v235, v234, s87
mm v[120:123],v[212:215],a[76:79],v[120:123]
rt v[208:209],v242 offset:33792
rt v[210:211],v242 offset:34816
mm v[124:127],v[220:223],a[76:79],v[124:127]
aw a145,v236
aw a149,v237
mm v[124:127],v[216:219],a[92:95],v[124:127]
ld a[36:39],v245,s[44:47],s52 offen offset:2048 sc1
mm v[92:95],v[216:219],a[88:91],v[92:95]
ar v230,a178
ar v231,a182
mm v[92:95],v[220:223],a[60:63],v[92:95]
rt v[212:213],v242 offset:42240
rt v[214:215],v242 offset:43264
mm v[60:63],v[220:223],a[56:59],v[60:63]
v_perm_b32 v232, v231, v230, s86
v_perm_b32 v233, v231, v230, s87
mm v[60:63],v[216:219],a[84:87],v[60:63]
aw a146,v232
aw a150,v233
mm v[28:31],v[216:219],a[80:83],v[28:31]
wt vmcnt(18) lgkmcnt(4)
ar v234,a186
mm v[28:31],v[220:223],a[52:55],v[28:31]
rt v[216:217],v243 offset:33792
rt v[218:219],v243 offset:34816
mm v[0:3],v[192:195],v[136:139],v[0:3]
ar v235,a190
v_perm_b32 v236, v235, v234, s86
mm v[0:3],v[196:199],v[168:171],v[0:3]
ld a[40:43],v246,s[44:47],s52 offen offset:2048 sc1
mm v[32:35],v[196:199],v[172:175],v[32:35]
v_perm_b32 v237, v235, v234, s87
aw a147,v236
mm v[32:35],v[192:195],v[140:143],v[32:35]
rt v[220:221],v243 offset:42240
rt v[222:223],v243 offset:43264
mm v[64:67],v[192:195],v[152:155],v[64:67]
aw a151,v237
ar v230,a163
mm v[64:67],v[196:199],v[184:187],v[64:67]
ld a[44:47],v247,s[44:47],s52 offen offset:2048 sc1
mm v[96:99],v[196:199],v[188:191],v[96:99]
ar v231,a167
v_perm_b32 v232, v231, v230, s86
mm v[96:99],v[192:195],v[156:159],v[96:99]
rt v[196:197],v240 offset:33920
rt v[198:199],v240 offset:34944
mm v[100:103],v[200:203],v[156:159],v[100:103]
v_perm_b32 v233, v231, v230, s87
aw a152,v232
mm v[100:103],v[204:207],v[188:191],v[100:103]
ld a[48:51],v244,s[44:47],s52 offen offset:3072 sc1
mm v[68:71],v[204:207],v[184:187],v[68:71]
aw a156,v233
ar v234,a171
mm v[68:71],v[200:203],v[152:155],v[68:71]
rt v[192:193],v240 offset:42368
rt v[194:195],v240 offset:43392
mm v[36:39],v[200:203],v[140:143],v[36:39]
)ASM" R"ASM(v_add_u32 v240, 4294899712, v240
ar v235,a175
mm v[36:39],v[204:207],v[172:175],v[36:39]
ld a[52:55],v245,s[44:47],s52 offen offset:3072 sc1
mm v[4:7],v[204:207],v[168:171],v[4:7]
wt lgkmcnt(4)
v_perm_b32 v236, v235, v234, s86
mm v[4:7],v[200:203],v[136:139],v[4:7]
rt v[204:205],v241 offset:33920
rt v[206:207],v241 offset:34944
mm v[8:11],v[208:211],v[136:139],v[8:11]
s_nop 0
v_cmp_eq_u64 vcc, v[250:251], v[252:253]
mm v[8:11],v[212:215],v[168:171],v[8:11]
ld a[56:59],v246,s[44:47],s52 offen offset:3072 sc1
mm v[40:43],v[212:215],v[172:175],v[40:43]
v_perm_b32 v237, v235, v234, s87
aw a153,v236
mm v[40:43],v[208:211],v[140:143],v[40:43]
rt v[200:201],v241 offset:42368
rt v[202:203],v241 offset:43392
mm v[72:75],v[208:211],v[152:155],v[72:75]
v_add_u32 v241, 4294899712, v241
aw a157,v237
mm v[72:75],v[212:215],v[184:187],v[72:75]
ld a[60:63],v247,s[44:47],s52 offen offset:3072 sc1
mm v[104:107],v[212:215],v[188:191],v[104:107]
ar v230,a179
ar v231,a183
mm v[104:107],v[208:211],v[156:159],v[104:107]
rt v[212:213],v242 offset:33920
rt v[214:215],v242 offset:34944
mm v[108:111],v[216:219],v[156:159],v[108:111]
v_perm_b32 v232, v231, v230, s86
v_perm_b32 v233, v231, v230, s87
mm v[108:111],v[220:223],v[188:191],v[108:111]
buffer_load_dwordx2 v[250:251], v248, s[64:67], s5 offen sc1
mm v[76:79],v[220:223],v[184:187],v[76:79]
aw a154,v232
aw a158,v233
mm v[76:79],v[216:219],v[152:155],v[76:79]
rt v[208:209],v242 offset:42368
rt v[210:211],v242 offset:43392
mm v[44:47],v[216:219],v[140:143],v[44:47]
s_add_u32 m0, s83, 53888
v_add_u32 v242, 4294899712, v242
mm v[44:47],v[220:223],v[172:175],v[44:47]
ld v227,s[56:59],s80 offen lds
mm v[12:15],v[220:223],v[168:171],v[12:15]
wt lgkmcnt(4)
s_bfe_u32 s80, s4, 262168
mm v[12:15],v[216:219],v[136:139],v[12:15]
rt v[220:221],v243 offset:33920
rt v[222:223],v243 offset:34944
mm v[16:19],v[196:199],v[136:139],v[16:19]
s_add_u32 m0, s83, 118272
s_xor_b32 s80, s80, s72
s_lshl_b32 s80, s80, 18
mm v[16:19],v[192:195],v[168:171],v[16:19]
ld v224,s[56:59],s80 offen lds
mm v[48:51],v[192:195],v[172:175],v[48:51]
ar v234,a187
ar v235,a191
mm v[48:51],v[196:199],v[140:143],v[48:51]
rt v[216:217],v243 offset:42368
rt v[218:219],v243 offset:43392
mm v[80:83],v[196:199],v[152:155],v[80:83]
s_add_u32 m0, s83, 119296
v_add_u32 v243, 4294899712, v243
mm v[80:83],v[192:195],v[184:187],v[80:83]
ld v225,s[56:59],s80 offen lds
mm v[112:115],v[192:195],v[188:191],v[112:115]
wt vmcnt(23) lgkmcnt(0)
s_barrier
mm v[112:115],v[196:199],v[156:159],v[112:115]
rt v[192:193],v240 offset:16896
rt v[194:195],v240 offset:17920
mm v[116:119],v[204:207],v[156:159],v[116:119]
s_add_u32 m0, s83, 120448
v_perm_b32 v236, v235, v234, s86
mm v[116:119],v[200:203],v[188:191],v[116:119]
ld v226,s[56:59],s80 offen lds
mm v[84:87],v[200:203],v[184:187],v[84:87]
s_bitcmp1_b32 vcc_lo, 5
s_cbranch_scc0 .La5_LO_NC5
.La5_LO_KC5:
mm v[84:87],v[204:207],v[152:155],v[84:87]
rt v[196:197],v240 offset:25344
rt v[198:199],v240 offset:26368
mm v[52:55],v[204:207],v[140:143],v[52:55]
v_perm_b32 v237, v235, v234, s87
aw a155,v236
mm v[52:55],v[200:203],v[172:175],v[52:55]
ld v[128:131],v244,s[44:47],s53 offen offset:0 sc1
mm v[20:23],v[200:203],v[168:171],v[20:23]
s_nop 0
aw a159,v237
mm v[20:23],v[204:207],v[136:139],v[20:23]
rt v[200:201],v241 offset:16896
rt v[202:203],v241 offset:17920
)ASM" R"ASM(mm v[24:27],v[212:215],v[136:139],v[24:27]
ar v230,a192
ar v231,a196
mm v[24:27],v[208:211],v[168:171],v[24:27]
ld v[132:135],v245,s[44:47],s53 offen offset:0 sc1
mm v[56:59],v[208:211],v[172:175],v[56:59]
v_perm_b32 v232, v231, v230, s86
v_perm_b32 v233, v231, v230, s87
mm v[56:59],v[212:215],v[140:143],v[56:59]
rt v[204:205],v241 offset:25344
rt v[206:207],v241 offset:26368
mm v[88:91],v[212:215],v[152:155],v[88:91]
aw a160,v232
aw a164,v233
mm v[88:91],v[208:211],v[184:187],v[88:91]
ld v[144:147],v246,s[44:47],s53 offen offset:0 sc1
mm v[120:123],v[208:211],v[188:191],v[120:123]
ar v234,a200
.La5_LW7_3:
s_cbranch_execz .La5_LW7_7
ar v235,a204
mm v[120:123],v[212:215],v[156:159],v[120:123]
rt v[208:209],v242 offset:16896
rt v[210:211],v242 offset:17920
mm v[124:127],v[220:223],v[156:159],v[124:127]
v_perm_b32 v236, v235, v234, s86
v_perm_b32 v237, v235, v234, s87
mm v[124:127],v[216:219],v[188:191],v[124:127]
ld v[156:159],v247,s[44:47],s53 offen offset:0 sc1
mm v[92:95],v[216:219],v[184:187],v[92:95]
aw a161,v236
aw a165,v237
mm v[92:95],v[220:223],v[152:155],v[92:95]
rt v[212:213],v242 offset:25344
rt v[214:215],v242 offset:26368
mm v[60:63],v[220:223],v[140:143],v[60:63]
ar v230,a208
ar v231,a212
mm v[60:63],v[216:219],v[172:175],v[60:63]
v_perm_b32 v232, v231, v230, s86
v_perm_b32 v233, v231, v230, s87
mm v[28:31],v[216:219],v[168:171],v[28:31]
wt vmcnt(20) lgkmcnt(4)
aw a162,v232
mm v[28:31],v[220:223],v[136:139],v[28:31]
rt v[216:217],v243 offset:16896
rt v[218:219],v243 offset:17920
mm v[0:3],v[192:195],a[0:3],v[0:3]
aw a166,v233
ar v234,a216
mm v[0:3],v[196:199],a[16:19],v[0:3]
ld v[136:139],v244,s[44:47],s53 offen offset:1024 sc1
mm v[32:35],v[196:199],a[20:23],v[32:35]
ar v235,a220
v_perm_b32 v236, v235, v234, s86
mm v[32:35],v[192:195],a[4:7],v[32:35]
rt v[220:221],v243 offset:25344
rt v[222:223],v243 offset:26368
mm v[64:67],v[192:195],a[8:11],v[64:67]
v_perm_b32 v237, v235, v234, s87
aw a163,v236
mm v[64:67],v[196:199],a[24:27],v[64:67]
ld v[140:143],v245,s[44:47],s53 offen offset:1024 sc1
mm v[96:99],v[196:199],a[28:31],v[96:99]
aw a167,v237
ar v230,a193
mm v[96:99],v[192:195],a[12:15],v[96:99]
rt v[196:197],v240 offset:17024
rt v[198:199],v240 offset:18048
mm v[100:103],v[200:203],a[12:15],v[100:103]
ar v231,a197
v_perm_b32 v232, v231, v230, s86
mm v[100:103],v[204:207],a[28:31],v[100:103]
ld v[152:155],v246,s[44:47],s53 offen offset:1024 sc1
mm v[68:71],v[204:207],a[24:27],v[68:71]
v_perm_b32 v233, v231, v230, s87
aw a168,v232
mm v[68:71],v[200:203],a[8:11],v[68:71]
rt v[192:193],v240 offset:25472
rt v[194:195],v240 offset:26496
mm v[36:39],v[200:203],a[4:7],v[36:39]
aw a172,v233
ar v234,a201
mm v[36:39],v[204:207],a[20:23],v[36:39]
ld v[168:171],v247,s[44:47],s53 offen offset:1024 sc1
mm v[4:7],v[204:207],a[16:19],v[4:7]
wt lgkmcnt(4)
ar v235,a205
mm v[4:7],v[200:203],a[0:3],v[4:7]
rt v[204:205],v241 offset:17024
rt v[206:207],v241 offset:18048
mm v[8:11],v[208:211],a[0:3],v[8:11]
s_add_u32 m0, s83, 121472
v_perm_b32 v236, v235, v234, s86
mm v[8:11],v[212:215],a[16:19],v[8:11]
ld v227,s[56:59],s80 offen lds
mm v[40:43],v[212:215],a[20:23],v[40:43]
s_add_u32 s80, s80, 131072
v_perm_b32 v237, v235, v234, s87
mm v[40:43],v[208:211],a[4:7],v[40:43]
)ASM" R"ASM(rt v[200:201],v241 offset:25472
rt v[202:203],v241 offset:26496
mm v[72:75],v[208:211],a[8:11],v[72:75]
s_add_u32 m0, s83, 67584
aw a169,v236
mm v[72:75],v[212:215],a[24:27],v[72:75]
ld v224,s[56:59],s80 offen lds
mm v[104:107],v[212:215],a[28:31],v[104:107]
aw a173,v237
ar v230,a209
mm v[104:107],v[208:211],a[12:15],v[104:107]
rt v[212:213],v242 offset:17024
rt v[214:215],v242 offset:18048
mm v[108:111],v[216:219],a[12:15],v[108:111]
s_add_u32 m0, s83, 68608
ar v231,a213
mm v[108:111],v[220:223],a[28:31],v[108:111]
ld v225,s[56:59],s80 offen lds
mm v[76:79],v[220:223],a[24:27],v[76:79]
v_perm_b32 v232, v231, v230, s86
v_perm_b32 v233, v231, v230, s87
mm v[76:79],v[216:219],a[8:11],v[76:79]
rt v[208:209],v242 offset:25472
rt v[210:211],v242 offset:26496
mm v[44:47],v[216:219],a[4:7],v[44:47]
aw a170,v232
aw a174,v233
mm v[44:47],v[220:223],a[20:23],v[44:47]
ar v234,a217
ar v235,a221
mm v[12:15],v[220:223],a[16:19],v[12:15]
wt lgkmcnt(4)
v_perm_b32 v236, v235, v234, s86
mm v[12:15],v[216:219],a[0:3],v[12:15]
rt v[220:221],v243 offset:17024
rt v[222:223],v243 offset:18048
mm v[16:19],v[196:199],a[0:3],v[16:19]
s_add_u32 m0, s83, 69760
v_perm_b32 v237, v235, v234, s87
mm v[16:19],v[192:195],a[16:19],v[16:19]
ld v226,s[56:59],s80 offen lds
mm v[48:51],v[192:195],a[20:23],v[48:51]
aw a171,v236
aw a175,v237
mm v[48:51],v[196:199],a[4:7],v[48:51]
rt v[216:217],v243 offset:25472
rt v[218:219],v243 offset:26496
mm v[80:83],v[196:199],a[8:11],v[80:83]
s_add_u32 m0, s83, 70784
ar v230,a194
mm v[80:83],v[192:195],a[24:27],v[80:83]
ld v227,s[56:59],s80 offen lds
mm v[112:115],v[192:195],a[28:31],v[112:115]
s_bfe_u32 s80, s4, 262172
s_xor_b32 s80, s80, s72
mm v[112:115],v[196:199],a[12:15],v[112:115]
rt v[192:193],v240 offset:33792
rt v[194:195],v240 offset:34816
mm v[116:119],v[204:207],a[12:15],v[116:119]
s_add_u32 m0, s83, 84480
s_lshl_b32 s80, s80, 18
mm v[116:119],v[200:203],a[28:31],v[116:119]
ld v224,s[56:59],s80 offen lds
mm v[84:87],v[200:203],a[24:27],v[84:87]
ar v231,a198
v_perm_b32 v232, v231, v230, s86
mm v[84:87],v[204:207],a[8:11],v[84:87]
rt v[196:197],v240 offset:42240
rt v[198:199],v240 offset:43264
mm v[52:55],v[204:207],a[4:7],v[52:55]
s_add_u32 m0, s83, 85504
v_perm_b32 v233, v231, v230, s87
mm v[52:55],v[200:203],a[20:23],v[52:55]
ld v225,s[56:59],s80 offen lds
mm v[20:23],v[200:203],a[16:19],v[20:23]
wt lgkmcnt(4)
aw a176,v232
mm v[20:23],v[204:207],a[0:3],v[20:23]
rt v[200:201],v241 offset:33792
rt v[202:203],v241 offset:34816
mm v[24:27],v[212:215],a[0:3],v[24:27]
s_add_u32 m0, s83, 86656
aw a180,v233
mm v[24:27],v[208:211],a[16:19],v[24:27]
ld v226,s[56:59],s80 offen lds
mm v[56:59],v[208:211],a[20:23],v[56:59]
ar v234,a202
ar v235,a206
mm v[56:59],v[212:215],a[4:7],v[56:59]
rt v[204:205],v241 offset:42240
rt v[206:207],v241 offset:43264
mm v[88:91],v[212:215],a[8:11],v[88:91]
s_add_u32 m0, s83, 87680
v_perm_b32 v236, v235, v234, s86
mm v[88:91],v[208:211],a[24:27],v[88:91]
ld v227,s[56:59],s80 offen lds
mm v[120:123],v[208:211],a[28:31],v[120:123]
s_add_u32 s80, s80, 131072
v_perm_b32 v237, v235, v234, s87
mm v[120:123],v[212:215],a[12:15],v[120:123]
)ASM" R"ASM(rt v[208:209],v242 offset:33792
rt v[210:211],v242 offset:34816
mm v[124:127],v[220:223],a[12:15],v[124:127]
s_add_u32 m0, s83, 101376
aw a177,v236
mm v[124:127],v[216:219],a[28:31],v[124:127]
ld v224,s[56:59],s80 offen lds
mm v[92:95],v[216:219],a[24:27],v[92:95]
aw a181,v237
ar v230,a210
mm v[92:95],v[220:223],a[8:11],v[92:95]
rt v[212:213],v242 offset:42240
rt v[214:215],v242 offset:43264
mm v[60:63],v[220:223],a[4:7],v[60:63]
ar v231,a214
v_perm_b32 v232, v231, v230, s86
mm v[60:63],v[216:219],a[20:23],v[60:63]
wt vmcnt(22)
v_cmp_eq_u64 vcc, v[250:251], v[252:253]
mm v[28:31],v[216:219],a[16:19],v[28:31]
wt lgkmcnt(4)
v_perm_b32 v233, v231, v230, s87
mm v[28:31],v[220:223],a[0:3],v[28:31]
rt v[216:217],v243 offset:33792
rt v[218:219],v243 offset:34816
mm v[0:3],v[192:195],a[32:35],v[0:3]
s_add_u32 m0, s83, 102400
aw a178,v232
mm v[0:3],v[196:199],a[48:51],v[0:3]
ld v225,s[56:59],s80 offen lds
mm v[32:35],v[196:199],a[52:55],v[32:35]
aw a182,v233
ar v234,a218
mm v[32:35],v[192:195],a[36:39],v[32:35]
rt v[220:221],v243 offset:42240
rt v[222:223],v243 offset:43264
mm v[64:67],v[192:195],a[40:43],v[64:67]
s_add_u32 m0, s83, 103552
ar v235,a222
mm v[64:67],v[196:199],a[56:59],v[64:67]
ld v226,s[56:59],s80 offen lds
mm v[96:99],v[196:199],a[60:63],v[96:99]
v_perm_b32 v236, v235, v234, s86
v_perm_b32 v237, v235, v234, s87
mm v[96:99],v[192:195],a[44:47],v[96:99]
rt v[196:197],v240 offset:33920
rt v[198:199],v240 offset:34944
mm v[100:103],v[200:203],a[44:47],v[100:103]
s_add_u32 m0, s83, 104576
aw a179,v236
mm v[100:103],v[204:207],a[60:63],v[100:103]
ld v227,s[56:59],s80 offen lds
mm v[68:71],v[204:207],a[56:59],v[68:71]
s_bitcmp1_b32 vcc_lo, 6
s_cbranch_scc0 .La5_LO_NC6
.La5_LO_KC6:
mm v[68:71],v[200:203],a[40:43],v[68:71]
rt v[192:193],v240 offset:42368
rt v[194:195],v240 offset:43392
mm v[36:39],v[200:203],a[36:39],v[36:39]
v_bfe_u32 v224, v255, 6, 1
v_lshrrev_b32 v225, 7, v255
mm v[36:39],v[204:207],a[52:55],v[36:39]
ld a[64:67],v244,s[44:47],s54 offen offset:0 sc1
mm v[4:7],v[204:207],a[48:51],v[4:7]
wt lgkmcnt(4)
v_and_b32 v226, 48, v255
mm v[4:7],v[200:203],a[32:35],v[4:7]
rt v[204:205],v241 offset:33920
rt v[206:207],v241 offset:34944
mm v[8:11],v[208:211],a[32:35],v[8:11]
v_lshl_add_u32 v225, v225, 6, v226
v_lshl_add_u32 v224, v224, 11, v225
mm v[8:11],v[212:215],a[48:51],v[8:11]
ld a[68:71],v245,s[44:47],s54 offen offset:0 sc1
mm v[40:43],v[212:215],a[52:55],v[40:43]
aw a183,v237
ar v230,a195
mm v[40:43],v[208:211],a[36:39],v[40:43]
rt v[200:201],v241 offset:42368
rt v[202:203],v241 offset:43392
mm v[72:75],v[208:211],a[40:43],v[72:75]
ar v231,a199
v_perm_b32 v232, v231, v230, s86
mm v[72:75],v[212:215],a[56:59],v[72:75]
ld a[72:75],v244,s[44:47],s54 offen offset:1024 sc1
mm v[104:107],v[212:215],a[60:63],v[104:107]
v_perm_b32 v233, v231, v230, s87
aw a184,v232
mm v[104:107],v[208:211],a[44:47],v[104:107]
rt v[212:213],v242 offset:33920
rt v[214:215],v242 offset:34944
mm v[108:111],v[216:219],a[44:47],v[108:111]
aw a188,v233
ar v234,a203
mm v[108:111],v[220:223],a[60:63],v[108:111]
ld a[76:79],v245,s[44:47],s54 offen offset:1024 sc1
mm v[76:79],v[220:223],a[56:59],v[76:79]
ar v235,a207
v_perm_b32 v236, v235, v234, s86
)ASM" R"ASM(mm v[76:79],v[216:219],a[40:43],v[76:79]
rt v[208:209],v242 offset:42368
rt v[210:211],v242 offset:43392
mm v[44:47],v[216:219],a[36:39],v[44:47]
v_perm_b32 v237, v235, v234, s87
aw a185,v236
mm v[44:47],v[220:223],a[52:55],v[44:47]
aw a189,v237
ar v230,a211
mm v[12:15],v[220:223],a[48:51],v[12:15]
wt lgkmcnt(4)
ar v231,a215
mm v[12:15],v[216:219],a[32:35],v[12:15]
rt v[220:221],v243 offset:33920
rt v[222:223],v243 offset:34944
mm v[16:19],v[196:199],a[32:35],v[16:19]
v_perm_b32 v232, v231, v230, s86
v_perm_b32 v233, v231, v230, s87
mm v[16:19],v[192:195],a[48:51],v[16:19]
ld a[80:83],v244,s[44:47],s54 offen offset:2048 sc1
mm v[48:51],v[192:195],a[52:55],v[48:51]
aw a186,v232
aw a190,v233
mm v[48:51],v[196:199],a[36:39],v[48:51]
rt v[216:217],v243 offset:42368
rt v[218:219],v243 offset:43392
mm v[80:83],v[196:199],a[40:43],v[80:83]
ar v234,a219
ar v235,a223
mm v[80:83],v[192:195],a[56:59],v[80:83]
ld a[84:87],v245,s[44:47],s54 offen offset:2048 sc1
mm v[112:115],v[192:195],a[60:63],v[112:115]
wt vmcnt(30)
s_barrier
mm v[112:115],v[196:199],a[44:47],v[112:115]
rt v[192:193],v240 offset:50688
rt v[194:195],v240 offset:51712
mm v[116:119],v[204:207],a[44:47],v[116:119]
v_perm_b32 v236, v235, v234, s86
v_perm_b32 v237, v235, v234, s87
mm v[116:119],v[200:203],a[60:63],v[116:119]
ld a[88:91],v244,s[44:47],s54 offen offset:3072 sc1
mm v[84:87],v[200:203],a[56:59],v[84:87]
aw a187,v236
aw a191,v237
mm v[84:87],v[204:207],a[40:43],v[84:87]
rt v[196:197],v240 offset:59136
rt v[198:199],v240 offset:60160
mm v[52:55],v[204:207],a[36:39],v[52:55]
s_bitcmp1_b32 vcc_lo, 7
s_cbranch_scc0 .La5_LO_NC7
.La5_LO_KC7:
mm v[52:55],v[200:203],a[52:55],v[52:55]
ld a[92:95],v245,s[44:47],s54 offen offset:3072 sc1
mm v[20:23],v[200:203],a[48:51],v[20:23]
wt lgkmcnt(4)
ar v230,a224
mm v[20:23],v[204:207],a[32:35],v[20:23]
rt v[200:201],v241 offset:50688
rt v[202:203],v241 offset:51712
mm v[24:27],v[212:215],a[32:35],v[24:27]
ar v231,a228
v_perm_b32 v232, v231, v230, s86
mm v[24:27],v[208:211],a[48:51],v[24:27]
ld a[0:3],v244,s[44:47],s55 offen offset:0 sc1
mm v[56:59],v[208:211],a[52:55],v[56:59]
v_perm_b32 v233, v231, v230, s87
aw a192,v232
mm v[56:59],v[212:215],a[36:39],v[56:59]
rt v[204:205],v241 offset:59136
rt v[206:207],v241 offset:60160
mm v[88:91],v[212:215],a[40:43],v[88:91]
aw a196,v233
ar v234,a232
mm v[88:91],v[208:211],a[56:59],v[88:91]
ld a[4:7],v245,s[44:47],s55 offen offset:0 sc1
mm v[120:123],v[208:211],a[60:63],v[120:123]
ar v235,a236
v_perm_b32 v236, v235, v234, s86
mm v[120:123],v[212:215],a[44:47],v[120:123]
rt v[208:209],v242 offset:50688
rt v[210:211],v242 offset:51712
mm v[124:127],v[220:223],a[44:47],v[124:127]
v_perm_b32 v237, v235, v234, s87
aw a193,v236
mm v[124:127],v[216:219],a[60:63],v[124:127]
ld a[8:11],v244,s[44:47],s55 offen offset:1024 sc1
mm v[92:95],v[216:219],a[56:59],v[92:95]
aw a197,v237
ar v230,a240
mm v[92:95],v[220:223],a[40:43],v[92:95]
rt v[212:213],v242 offset:59136
rt v[214:215],v242 offset:60160
mm v[60:63],v[220:223],a[36:39],v[60:63]
ar v231,a244
v_perm_b32 v232, v231, v230, s86
mm v[60:63],v[216:219],a[52:55],v[60:63]
v_perm_b32 v233, v231, v230, s87
aw a194,v232
mm v[28:31],v[216:219],a[48:51],v[28:31]
)ASM" R"ASM(wt vmcnt(24) lgkmcnt(4)
aw a198,v233
mm v[28:31],v[220:223],a[32:35],v[28:31]
rt v[216:217],v243 offset:50688
rt v[218:219],v243 offset:51712
mm v[0:3],v[192:195],v[128:131],v[0:3]
ar v234,a248
ar v235,a252
mm v[0:3],v[196:199],v[136:139],v[0:3]
ld a[12:15],v245,s[44:47],s55 offen offset:1024 sc1
mm v[32:35],v[196:199],v[140:143],v[32:35]
v_perm_b32 v236, v235, v234, s86
v_perm_b32 v237, v235, v234, s87
mm v[32:35],v[192:195],v[132:135],v[32:35]
rt v[220:221],v243 offset:59136
rt v[222:223],v243 offset:60160
mm v[64:67],v[192:195],v[144:147],v[64:67]
aw a195,v236
aw a199,v237
mm v[64:67],v[196:199],v[152:155],v[64:67]
ld a[16:19],v244,s[44:47],s55 offen offset:2048 sc1
mm v[96:99],v[196:199],v[168:171],v[96:99]
ar v230,a225
ar v231,a229
mm v[96:99],v[192:195],v[156:159],v[96:99]
rt v[196:197],v240 offset:50816
rt v[198:199],v240 offset:51840
mm v[100:103],v[200:203],v[156:159],v[100:103]
v_perm_b32 v232, v231, v230, s86
v_perm_b32 v233, v231, v230, s87
mm v[100:103],v[204:207],v[168:171],v[100:103]
ld a[20:23],v245,s[44:47],s55 offen offset:2048 sc1
mm v[68:71],v[204:207],v[152:155],v[68:71]
aw a200,v232
aw a204,v233
mm v[68:71],v[200:203],v[144:147],v[68:71]
rt v[192:193],v240 offset:59264
rt v[194:195],v240 offset:60288
mm v[36:39],v[200:203],v[132:135],v[36:39]
v_add_u32 v240, 67584, v240
ar v234,a233
mm v[36:39],v[204:207],v[140:143],v[36:39]
ld a[24:27],v244,s[44:47],s55 offen offset:3072 sc1
mm v[4:7],v[204:207],v[136:139],v[4:7]
wt lgkmcnt(4)
ar v235,a237
mm v[4:7],v[200:203],v[128:131],v[4:7]
rt v[204:205],v241 offset:50816
rt v[206:207],v241 offset:51840
mm v[8:11],v[208:211],v[128:131],v[8:11]
v_perm_b32 v236, v235, v234, s86
v_perm_b32 v237, v235, v234, s87
mm v[8:11],v[212:215],v[136:139],v[8:11]
ld a[28:31],v245,s[44:47],s55 offen offset:3072 sc1
mm v[40:43],v[212:215],v[140:143],v[40:43]
aw a201,v236
aw a205,v237
mm v[40:43],v[208:211],v[132:135],v[40:43]
rt v[200:201],v241 offset:59264
rt v[202:203],v241 offset:60288
mm v[72:75],v[208:211],v[144:147],v[72:75]
v_add_u32 v241, 67584, v241
ar v230,a241
mm v[72:75],v[212:215],v[152:155],v[72:75]
ar v231,a245
v_perm_b32 v232, v231, v230, s86
mm v[104:107],v[212:215],v[168:171],v[104:107]
v_perm_b32 v233, v231, v230, s87
aw a202,v232
mm v[104:107],v[208:211],v[156:159],v[104:107]
rt v[212:213],v242 offset:50816
rt v[214:215],v242 offset:51840
mm v[108:111],v[216:219],v[156:159],v[108:111]
aw a206,v233
ar v234,a249
mm v[108:111],v[220:223],v[168:171],v[108:111]
ar v235,a253
v_perm_b32 v236, v235, v234, s86
mm v[76:79],v[220:223],v[152:155],v[76:79]
v_perm_b32 v237, v235, v234, s87
aw a203,v236
mm v[76:79],v[216:219],v[144:147],v[76:79]
rt v[208:209],v242 offset:59264
rt v[210:211],v242 offset:60288
mm v[44:47],v[216:219],v[132:135],v[44:47]
v_add_u32 v242, 67584, v242
aw a207,v237
mm v[44:47],v[220:223],v[140:143],v[44:47]
ar v230,a226
ar v231,a230
mm v[12:15],v[220:223],v[136:139],v[12:15]
wt lgkmcnt(4)
v_perm_b32 v232, v231, v230, s86
mm v[12:15],v[216:219],v[128:131],v[12:15]
rt v[220:221],v243 offset:50816
rt v[222:223],v243 offset:51840
mm v[16:19],v[196:199],v[128:131],v[16:19]
v_perm_b32 v233, v231, v230, s87
aw a208,v232
mm v[16:19],v[192:195],v[136:139],v[16:19]
)ASM" R"ASM(ld a[32:35],v246,s[44:47],s54 offen offset:0 sc1
mm v[48:51],v[192:195],v[140:143],v[48:51]
aw a212,v233
ar v234,a234
mm v[48:51],v[196:199],v[132:135],v[48:51]
rt v[216:217],v243 offset:59264
rt v[218:219],v243 offset:60288
mm v[80:83],v[196:199],v[144:147],v[80:83]
v_add_u32 v243, 67584, v243
ar v235,a238
mm v[80:83],v[192:195],v[152:155],v[80:83]
ld a[36:39],v247,s[44:47],s54 offen offset:0 sc1
mm v[112:115],v[192:195],v[168:171],v[112:115]
wt vmcnt(18)
s_barrier
mm v[112:115],v[196:199],v[156:159],v[112:115]
rt v[192:193],v240 offset:50688
rt v[194:195],v240 offset:51712
mm v[116:119],v[204:207],v[156:159],v[116:119]
v_perm_b32 v236, v235, v234, s86
v_perm_b32 v237, v235, v234, s87
mm v[116:119],v[200:203],v[168:171],v[116:119]
ld a[40:43],v246,s[44:47],s54 offen offset:1024 sc1
mm v[84:87],v[200:203],v[152:155],v[84:87]
aw a209,v236
aw a213,v237
mm v[84:87],v[204:207],v[144:147],v[84:87]
rt v[196:197],v240 offset:59136
rt v[198:199],v240 offset:60160
mm v[52:55],v[204:207],v[132:135],v[52:55]
ar v230,a242
ar v231,a246
mm v[52:55],v[200:203],v[140:143],v[52:55]
ld a[44:47],v247,s[44:47],s54 offen offset:1024 sc1
mm v[20:23],v[200:203],v[136:139],v[20:23]
wt lgkmcnt(4)
v_perm_b32 v232, v231, v230, s86
mm v[20:23],v[204:207],v[128:131],v[20:23]
rt v[200:201],v240 offset:0
rt v[202:203],v240 offset:1024
mm v[24:27],v[212:215],v[128:131],v[24:27]
v_perm_b32 v233, v231, v230, s87
aw a210,v232
mm v[24:27],v[208:211],v[136:139],v[24:27]
ld a[48:51],v246,s[44:47],s54 offen offset:2048 sc1
mm v[56:59],v[208:211],v[140:143],v[56:59]
aw a214,v233
.La5_LW7_4:
s_cbranch_execz .La5_LA_RETURN
ar v234,a250
mm v[56:59],v[212:215],v[132:135],v[56:59]
rt v[204:205],v240 offset:8448
rt v[206:207],v240 offset:9472
mm v[88:91],v[212:215],v[144:147],v[88:91]
ar v235,a254
v_perm_b32 v236, v235, v234, s86
mm v[88:91],v[208:211],v[152:155],v[88:91]
ld a[52:55],v247,s[44:47],s54 offen offset:2048 sc1
mm v[120:123],v[208:211],v[168:171],v[120:123]
v_perm_b32 v237, v235, v234, s87
aw a211,v236
mm v[120:123],v[212:215],v[156:159],v[120:123]
rt v[208:209],v240 offset:16896
rt v[210:211],v240 offset:17920
mm v[124:127],v[220:223],v[156:159],v[124:127]
aw a215,v237
ar v230,a227
mm v[124:127],v[216:219],v[168:171],v[124:127]
ld a[56:59],v246,s[44:47],s54 offen offset:3072 sc1
mm v[92:95],v[216:219],v[152:155],v[92:95]
ar v231,a231
v_perm_b32 v232, v231, v230, s86
mm v[92:95],v[220:223],v[144:147],v[92:95]
rt v[212:213],v240 offset:25344
rt v[214:215],v240 offset:26368
mm v[60:63],v[220:223],v[132:135],v[60:63]
v_perm_b32 v233, v231, v230, s87
aw a216,v232
mm v[60:63],v[216:219],v[140:143],v[60:63]
aw a220,v233
ar v234,a235
mm v[28:31],v[216:219],v[136:139],v[28:31]
wt vmcnt(15) lgkmcnt(4)
ar v235,a239
mm v[28:31],v[220:223],v[128:131],v[28:31]
rt v[216:217],v240 offset:33792
rt v[218:219],v240 offset:34816
mm v[0:3],v[192:195],a[64:67],v[0:3]
ld a[60:63],v247,s[44:47],s54 offen offset:3072 sc1
mm v[0:3],v[196:199],a[72:75],v[0:3]
rt v[220:221],v240 offset:42240
rt v[222:223],v240 offset:43264
mm v[32:35],v[196:199],a[76:79],v[32:35]
v_perm_b32 v236, v235, v234, s86
v_perm_b32 v237, v235, v234, s87
mm v[32:35],v[192:195],a[68:71],v[32:35]
rt v[196:197],v241 offset:50688
rt v[198:199],v241 offset:51712
mm v[32:35],v[200:203],a[84:87],v[32:35]
ld v[128:131],v246,s[44:47],s55 offen offset:0 sc1
)ASM" R"ASM(mm v[32:35],v[204:207],a[92:95],v[32:35]
rt v[192:193],v241 offset:59136
rt v[194:195],v241 offset:60160
mm v[0:3],v[204:207],a[88:91],v[0:3]
wt vmcnt(9) lgkmcnt(4)
aw a217,v236
mm v[0:3],v[200:203],a[80:83],v[0:3]
rt v[204:205],v241 offset:0
rt v[206:207],v241 offset:1024
mm v[0:3],v[208:211],a[0:3],v[0:3]
ld v[132:135],v247,s[44:47],s55 offen offset:0 sc1
mm v[0:3],v[212:215],a[8:11],v[0:3]
rt v[200:201],v241 offset:8448
rt v[202:203],v241 offset:9472
mm v[32:35],v[212:215],a[12:15],v[32:35]
aw a221,v237
ar v230,a243
mm v[32:35],v[208:211],a[4:7],v[32:35]
rt v[212:213],v241 offset:16896
rt v[214:215],v241 offset:17920
mm v[32:35],v[216:219],a[20:23],v[32:35]
ld v[144:147],v246,s[44:47],s55 offen offset:1024 sc1
mm v[32:35],v[220:223],a[28:31],v[32:35]
rt v[208:209],v241 offset:25344
rt v[210:211],v241 offset:26368
mm v[0:3],v[220:223],a[24:27],v[0:3]
wt lgkmcnt(4)
ar v231,a247
mm v[0:3],v[216:219],a[16:19],v[0:3]
rt v[220:221],v241 offset:33792
rt v[222:223],v241 offset:34816
mm v[4:7],v[196:199],a[64:67],v[4:7]
ld v[148:151],v247,s[44:47],s55 offen offset:1024 sc1
mm v[4:7],v[192:195],a[72:75],v[4:7]
rt v[216:217],v241 offset:42240
rt v[218:219],v241 offset:43264
mm v[36:39],v[192:195],a[76:79],v[36:39]
v_perm_b32 v232, v231, v230, s86
v_perm_b32 v233, v231, v230, s87
mm v[36:39],v[196:199],a[68:71],v[36:39]
rt v[192:193],v242 offset:50688
rt v[194:195],v242 offset:51712
mm v[36:39],v[204:207],a[84:87],v[36:39]
ld v[160:163],v246,s[44:47],s55 offen offset:2048 sc1
mm v[36:39],v[200:203],a[92:95],v[36:39]
rt v[196:197],v242 offset:59136
rt v[198:199],v242 offset:60160
aw a218,v232
aw a222,v233
ar v234,a251
ar v235,a255
v_perm_b32 v236, v235, v234, s86
v_perm_b32 v237, v235, v234, s87
aw a219,v236
aw a223,v237
mm v[4:7],v[200:203],a[88:91],v[4:7]
wt lgkmcnt(4)
mm v[4:7],v[204:207],a[80:83],v[4:7]
rt v[200:201],v242 offset:0
rt v[202:203],v242 offset:1024
mm v[4:7],v[212:215],a[0:3],v[4:7]
ld v[164:167],v247,s[44:47],s55 offen offset:2048 sc1
mm v[4:7],v[208:211],a[8:11],v[4:7]
rt v[204:205],v242 offset:8448
rt v[206:207],v242 offset:9472
mm v[36:39],v[208:211],a[12:15],v[36:39]
mm v[36:39],v[212:215],a[4:7],v[36:39]
rt v[208:209],v242 offset:16896
rt v[210:211],v242 offset:17920
mm v[36:39],v[220:223],a[20:23],v[36:39]
mm v[36:39],v[216:219],a[28:31],v[36:39]
rt v[212:213],v242 offset:25344
rt v[214:215],v242 offset:26368
mm v[4:7],v[216:219],a[24:27],v[4:7]
wt lgkmcnt(4)
mm v[4:7],v[220:223],a[16:19],v[4:7]
rt v[216:217],v242 offset:33792
rt v[218:219],v242 offset:34816
mm v[8:11],v[192:195],a[64:67],v[8:11]
ld v[176:179],v246,s[44:47],s55 offen offset:3072 sc1
mm v[8:11],v[196:199],a[72:75],v[8:11]
rt v[220:221],v242 offset:42240
rt v[222:223],v242 offset:43264
mm v[40:43],v[196:199],a[76:79],v[40:43]
cv v234,v32,v33
cv v236,v36,v37
mm v[40:43],v[192:195],a[68:71],v[40:43]
rt v[196:197],v243 offset:50688
rt v[198:199],v243 offset:51712
mm v[40:43],v[200:203],a[84:87],v[40:43]
ld v[180:183],v247,s[44:47],s55 offen offset:3072 sc1
mm v[40:43],v[204:207],a[92:95],v[40:43]
rt v[192:193],v243 offset:59136
rt v[194:195],v243 offset:60160
mm v[8:11],v[204:207],a[88:91],v[8:11]
wt lgkmcnt(4)
v_bfe_u32 v250, v255, 6, 1
mm v[8:11],v[200:203],a[80:83],v[8:11]
rt v[204:205],v243 offset:0
rt v[206:207],v243 offset:1024
)ASM" R"ASM(mm v[8:11],v[208:211],a[0:3],v[8:11]
cv v235,v34,v35
cv v237,v38,v39
mm v[8:11],v[212:215],a[8:11],v[8:11]
rt v[200:201],v243 offset:8448
rt v[202:203],v243 offset:9472
mm v[40:43],v[212:215],a[12:15],v[40:43]
v_permlane16_swap_b32 v234, v236
v_permlane16_swap_b32 v235, v237
mm v[40:43],v[208:211],a[4:7],v[40:43]
rt v[212:213],v243 offset:16896
rt v[214:215],v243 offset:17920
mm v[40:43],v[216:219],a[20:23],v[40:43]
st v[234:237],v238,s[32:35],s95 offen offset:0 sc1
mm v[40:43],v[220:223],a[28:31],v[40:43]
rt v[208:209],v243 offset:25344
rt v[210:211],v243 offset:26368
mm v[8:11],v[220:223],a[24:27],v[8:11]
wt lgkmcnt(4)
v_mul_u32_u24 v250, 7, v250
mm v[8:11],v[216:219],a[16:19],v[8:11]
rt v[220:221],v243 offset:33792
rt v[222:223],v243 offset:34816
mm v[12:15],v[196:199],a[64:67],v[12:15]
cv v230,v0,v1
cv v232,v4,v5
mm v[12:15],v[192:195],a[72:75],v[12:15]
rt v[216:217],v243 offset:42240
rt v[218:219],v243 offset:43264
mm v[44:47],v[192:195],a[76:79],v[44:47]
cv v231,v2,v3
cv v233,v6,v7
mm v[44:47],v[196:199],a[68:71],v[44:47]
rt v[192:193],v240 offset:50816
rt v[194:195],v240 offset:51840
mm v[44:47],v[204:207],a[84:87],v[44:47]
v_permlane16_swap_b32 v230, v232
v_permlane16_swap_b32 v231, v233
mm v[44:47],v[200:203],a[92:95],v[44:47]
rt v[196:197],v240 offset:59264
rt v[198:199],v240 offset:60288
mm v[12:15],v[200:203],a[88:91],v[12:15]
wt lgkmcnt(4)
v_xor_b32 v250, s3, v250
mm v[12:15],v[204:207],a[80:83],v[12:15]
rt v[200:201],v240 offset:128
rt v[202:203],v240 offset:1152
mm v[12:15],v[212:215],a[0:3],v[12:15]
st v[230:233],v238,s[32:35],s94 offen offset:0 sc1
mm v[12:15],v[208:211],a[8:11],v[12:15]
rt v[204:205],v240 offset:8576
rt v[206:207],v240 offset:9600
mm v[44:47],v[208:211],a[12:15],v[44:47]
v_bfe_u32 v251, v255, 4, 2
v_lshl_add_u32 v250, v250, 2, v251
mm v[44:47],v[212:215],a[4:7],v[44:47]
rt v[208:209],v240 offset:17024
rt v[210:211],v240 offset:18048
mm v[44:47],v[220:223],a[20:23],v[44:47]
ld v[136:139],v224,s[68:71],0 offen offset:0
mm v[44:47],v[216:219],a[28:31],v[44:47]
rt v[212:213],v240 offset:25472
rt v[214:215],v240 offset:26496
mm v[12:15],v[216:219],a[24:27],v[12:15]
wt lgkmcnt(4)
v_lshlrev_b32 v250, 9, v250
mm v[12:15],v[220:223],a[16:19],v[12:15]
rt v[216:217],v240 offset:33920
rt v[218:219],v240 offset:34944
mm v[16:19],v[192:195],a[64:67],v[16:19]
cv v234,v40,v41
cv v236,v44,v45
mm v[16:19],v[196:199],a[72:75],v[16:19]
rt v[220:221],v240 offset:42368
rt v[222:223],v240 offset:43392
mm v[48:51],v[196:199],a[76:79],v[48:51]
cv v235,v42,v43
cv v237,v46,v47
mm v[48:51],v[192:195],a[68:71],v[48:51]
rt v[196:197],v241 offset:50816
rt v[198:199],v241 offset:51840
mm v[48:51],v[200:203],a[84:87],v[48:51]
v_permlane16_swap_b32 v234, v236
v_permlane16_swap_b32 v235, v237
mm v[48:51],v[204:207],a[92:95],v[48:51]
rt v[192:193],v241 offset:59264
rt v[194:195],v241 offset:60288
mm v[16:19],v[204:207],a[88:91],v[16:19]
wt lgkmcnt(4)
v_and_b32 v251, 0x2000, v250
mm v[16:19],v[200:203],a[80:83],v[16:19]
rt v[204:205],v241 offset:128
rt v[206:207],v241 offset:1152
mm v[16:19],v[208:211],a[0:3],v[16:19]
st v[234:237],v238,s[32:35],s95 offen offset:1024 sc1
mm v[16:19],v[212:215],a[8:11],v[16:19]
rt v[200:201],v241 offset:8576
rt v[202:203],v241 offset:9600
mm v[48:51],v[212:215],a[12:15],v[48:51]
cv v230,v8,v9
)ASM" R"ASM(cv v232,v12,v13
mm v[48:51],v[208:211],a[4:7],v[48:51]
rt v[212:213],v241 offset:17024
rt v[214:215],v241 offset:18048
mm v[48:51],v[216:219],a[20:23],v[48:51]
cv v231,v10,v11
cv v233,v14,v15
mm v[48:51],v[220:223],a[28:31],v[48:51]
rt v[208:209],v241 offset:25472
rt v[210:211],v241 offset:26496
mm v[16:19],v[220:223],a[24:27],v[16:19]
wt lgkmcnt(4)
v_add_u32 v250, v250, v251
mm v[16:19],v[216:219],a[16:19],v[16:19]
rt v[220:221],v241 offset:33920
rt v[222:223],v241 offset:34944
mm v[20:23],v[196:199],a[64:67],v[20:23]
v_permlane16_swap_b32 v230, v232
v_permlane16_swap_b32 v231, v233
mm v[20:23],v[192:195],a[72:75],v[20:23]
rt v[216:217],v241 offset:42368
rt v[218:219],v241 offset:43392
mm v[52:55],v[192:195],a[76:79],v[52:55]
v_bfe_u32 v251, v255, 7, 1
v_lshl_add_u32 v250, v251, 13, v250
mm v[52:55],v[196:199],a[68:71],v[52:55]
rt v[192:193],v242 offset:50816
rt v[194:195],v242 offset:51840
mm v[52:55],v[204:207],a[84:87],v[52:55]
st v[230:233],v238,s[32:35],s94 offen offset:1024 sc1
mm v[52:55],v[200:203],a[92:95],v[52:55]
rt v[196:197],v242 offset:59264
rt v[198:199],v242 offset:60288
mm v[20:23],v[200:203],a[88:91],v[20:23]
wt lgkmcnt(4)
v_and_b32 v251, 64, v255
mm v[20:23],v[204:207],a[80:83],v[20:23]
rt v[200:201],v242 offset:128
rt v[202:203],v242 offset:1152
mm v[20:23],v[212:215],a[0:3],v[20:23]
ld v[140:143],v225,s[48:51],0 offen offset:0
mm v[20:23],v[208:211],a[8:11],v[20:23]
rt v[204:205],v242 offset:8576
rt v[206:207],v242 offset:9600
mm v[52:55],v[208:211],a[12:15],v[52:55]
v_lshl_add_u32 v251, v251, 24, v250
v_and_b32 v248, 0x1f0, v255
mm v[52:55],v[212:215],a[4:7],v[52:55]
rt v[208:209],v242 offset:17024
rt v[210:211],v242 offset:18048
mm v[52:55],v[220:223],a[20:23],v[52:55]
ld v[152:155],v224,s[68:71],0 offen offset:512
mm v[52:55],v[216:219],a[28:31],v[52:55]
rt v[212:213],v242 offset:25472
rt v[214:215],v242 offset:26496
mm v[20:23],v[216:219],a[24:27],v[20:23]
wt lgkmcnt(4)
s_mov_b32 s93, 0
ld a[224:227],v229,s[28:31],s93 offen offset:0
mm v[20:23],v[220:223],a[16:19],v[20:23]
rt v[216:217],v242 offset:33920
rt v[218:219],v242 offset:34944
mm v[24:27],v[192:195],a[64:67],v[24:27]
cv v234,v48,v49
cv v236,v52,v53
mm v[24:27],v[196:199],a[72:75],v[24:27]
rt v[220:221],v242 offset:42368
rt v[222:223],v242 offset:43392
mm v[56:59],v[196:199],a[76:79],v[56:59]
cv v235,v50,v51
cv v237,v54,v55
mm v[56:59],v[192:195],a[68:71],v[56:59]
rt v[196:197],v243 offset:50816
rt v[198:199],v243 offset:51840
mm v[56:59],v[200:203],a[84:87],v[56:59]
v_permlane16_swap_b32 v234, v236
v_permlane16_swap_b32 v235, v237
mm v[56:59],v[204:207],a[92:95],v[56:59]
rt v[192:193],v243 offset:59264
rt v[194:195],v243 offset:60288
mm v[24:27],v[204:207],a[88:91],v[24:27]
wt lgkmcnt(4)
ld a[228:231],v229,s[28:31],s93 offen offset:2048
mm v[24:27],v[200:203],a[80:83],v[24:27]
rt v[204:205],v243 offset:128
rt v[206:207],v243 offset:1152
mm v[24:27],v[208:211],a[0:3],v[24:27]
st v[234:237],v238,s[32:35],s95 offen offset:2048 sc1
mm v[24:27],v[212:215],a[8:11],v[24:27]
rt v[200:201],v243 offset:8576
rt v[202:203],v243 offset:9600
mm v[56:59],v[212:215],a[12:15],v[56:59]
cv v230,v16,v17
cv v232,v20,v21
mm v[56:59],v[208:211],a[4:7],v[56:59]
rt v[212:213],v243 offset:17024
rt v[214:215],v243 offset:18048
mm v[56:59],v[216:219],a[20:23],v[56:59]
cv v231,v18,v19
cv v233,v22,v23
)ASM" R"ASM(mm v[56:59],v[220:223],a[28:31],v[56:59]
rt v[208:209],v243 offset:25472
rt v[210:211],v243 offset:26496
mm v[24:27],v[220:223],a[24:27],v[24:27]
wt lgkmcnt(4)
s_mov_b32 s93, 4096
ld a[232:235],v229,s[28:31],s93 offen offset:0
mm v[24:27],v[216:219],a[16:19],v[24:27]
rt v[220:221],v243 offset:33920
rt v[222:223],v243 offset:34944
mm v[28:31],v[196:199],a[64:67],v[28:31]
v_permlane16_swap_b32 v230, v232
v_permlane16_swap_b32 v231, v233
mm v[28:31],v[192:195],a[72:75],v[28:31]
rt v[216:217],v243 offset:42368
rt v[218:219],v243 offset:43392
mm v[60:63],v[192:195],a[76:79],v[60:63]
ld a[236:239],v229,s[28:31],s93 offen offset:2048
mm v[60:63],v[196:199],a[68:71],v[60:63]
rt v[192:193],v240 offset:50688
rt v[194:195],v240 offset:51712
mm v[60:63],v[204:207],a[84:87],v[60:63]
st v[230:233],v238,s[32:35],s94 offen offset:2048 sc1
mm v[60:63],v[200:203],a[92:95],v[60:63]
rt v[196:197],v240 offset:59136
rt v[198:199],v240 offset:60160
mm v[28:31],v[200:203],a[88:91],v[28:31]
wt lgkmcnt(4)
s_mov_b32 s93, 8192
ld a[240:243],v229,s[28:31],s93 offen offset:0
mm v[28:31],v[204:207],a[80:83],v[28:31]
rt v[200:201],v240 offset:0
rt v[202:203],v240 offset:1024
mm v[28:31],v[212:215],a[0:3],v[28:31]
ld v[156:159],v225,s[48:51],0 offen offset:512
mm v[28:31],v[208:211],a[8:11],v[28:31]
rt v[204:205],v240 offset:8448
rt v[206:207],v240 offset:9472
mm v[60:63],v[208:211],a[12:15],v[60:63]
ld a[244:247],v229,s[28:31],s93 offen offset:2048
mm v[60:63],v[212:215],a[4:7],v[60:63]
rt v[208:209],v240 offset:16896
rt v[210:211],v240 offset:17920
mm v[60:63],v[220:223],a[20:23],v[60:63]
ld v[168:171],v224,s[68:71],0 offen offset:1024
mm v[60:63],v[216:219],a[28:31],v[60:63]
rt v[212:213],v240 offset:25344
rt v[214:215],v240 offset:26368
mm v[28:31],v[216:219],a[24:27],v[28:31]
wt vmcnt(25) lgkmcnt(4)
s_mov_b32 s93, 12288
ld a[248:251],v229,s[28:31],s93 offen offset:0
mm v[28:31],v[220:223],a[16:19],v[28:31]
rt v[216:217],v240 offset:33792
rt v[218:219],v240 offset:34816
mm v[64:67],v[192:195],a[32:35],v[64:67]
cv v234,v56,v57
cv v236,v60,v61
mm v[64:67],v[196:199],a[40:43],v[64:67]
rt v[220:221],v240 offset:42240
rt v[222:223],v240 offset:43264
mm v[96:99],v[196:199],a[44:47],v[96:99]
cv v235,v58,v59
cv v237,v62,v63
mm v[96:99],v[192:195],a[36:39],v[96:99]
rt v[196:197],v241 offset:50688
rt v[198:199],v241 offset:51712
mm v[96:99],v[200:203],a[52:55],v[96:99]
v_permlane16_swap_b32 v234, v236
v_permlane16_swap_b32 v235, v237
mm v[96:99],v[204:207],a[60:63],v[96:99]
rt v[192:193],v241 offset:59136
rt v[194:195],v241 offset:60160
mm v[64:67],v[204:207],a[56:59],v[64:67]
wt vmcnt(18) lgkmcnt(4)
ld a[252:255],v229,s[28:31],s93 offen offset:2048
mm v[64:67],v[200:203],a[48:51],v[64:67]
rt v[204:205],v241 offset:0
rt v[206:207],v241 offset:1024
mm v[64:67],v[208:211],v[128:131],v[64:67]
st v[234:237],v238,s[32:35],s95 offen offset:3072 sc1
mm v[64:67],v[212:215],v[144:147],v[64:67]
rt v[200:201],v241 offset:8448
rt v[202:203],v241 offset:9472
mm v[96:99],v[212:215],v[148:151],v[96:99]
cv v230,v24,v25
cv v232,v28,v29
mm v[96:99],v[208:211],v[132:135],v[96:99]
rt v[212:213],v241 offset:16896
rt v[214:215],v241 offset:17920
mm v[96:99],v[216:219],v[164:167],v[96:99]
cv v231,v26,v27
cv v233,v30,v31
mm v[96:99],v[220:223],v[180:183],v[96:99]
rt v[208:209],v241 offset:25344
rt v[210:211],v241 offset:26368
mm v[64:67],v[220:223],v[176:179],v[64:67]
wt lgkmcnt(4)
s_mov_b32 s93, 0
ld a[64:67],v229,s[28:31],s93 offen offset:256
)ASM" R"ASM(mm v[64:67],v[216:219],v[160:163],v[64:67]
rt v[220:221],v241 offset:33792
rt v[222:223],v241 offset:34816
mm v[68:71],v[196:199],a[32:35],v[68:71]
v_permlane16_swap_b32 v230, v232
v_permlane16_swap_b32 v231, v233
mm v[68:71],v[192:195],a[40:43],v[68:71]
rt v[216:217],v241 offset:42240
rt v[218:219],v241 offset:43264
mm v[100:103],v[192:195],a[44:47],v[100:103]
ld a[68:71],v229,s[28:31],s93 offen offset:2304
mm v[100:103],v[196:199],a[36:39],v[100:103]
rt v[192:193],v242 offset:50688
rt v[194:195],v242 offset:51712
mm v[100:103],v[204:207],a[52:55],v[100:103]
st v[230:233],v238,s[32:35],s94 offen offset:3072 sc1
mm v[100:103],v[200:203],a[60:63],v[100:103]
rt v[196:197],v242 offset:59136
rt v[198:199],v242 offset:60160
mm v[68:71],v[200:203],a[56:59],v[68:71]
wt lgkmcnt(4)
s_mov_b32 s93, 4096
ld a[72:75],v229,s[28:31],s93 offen offset:256
mm v[68:71],v[204:207],a[48:51],v[68:71]
rt v[200:201],v242 offset:0
rt v[202:203],v242 offset:1024
mm v[68:71],v[212:215],v[128:131],v[68:71]
ld v[172:175],v225,s[48:51],0 offen offset:1024
mm v[68:71],v[208:211],v[144:147],v[68:71]
rt v[204:205],v242 offset:8448
rt v[206:207],v242 offset:9472
mm v[100:103],v[208:211],v[148:151],v[100:103]
ld a[76:79],v229,s[28:31],s93 offen offset:2304
mm v[100:103],v[212:215],v[132:135],v[100:103]
rt v[208:209],v242 offset:16896
rt v[210:211],v242 offset:17920
mm v[100:103],v[220:223],v[164:167],v[100:103]
ld v[184:187],v224,s[68:71],0 offen offset:1536
mm v[100:103],v[216:219],v[180:183],v[100:103]
rt v[212:213],v242 offset:25344
rt v[214:215],v242 offset:26368
mm v[68:71],v[216:219],v[176:179],v[68:71]
wt lgkmcnt(4)
s_mov_b32 s93, 8192
ld a[80:83],v229,s[28:31],s93 offen offset:256
mm v[68:71],v[220:223],v[160:163],v[68:71]
rt v[216:217],v242 offset:33792
rt v[218:219],v242 offset:34816
mm v[72:75],v[192:195],a[32:35],v[72:75]
cv v234,v96,v97
cv v236,v100,v101
mm v[72:75],v[196:199],a[40:43],v[72:75]
rt v[220:221],v242 offset:42240
rt v[222:223],v242 offset:43264
mm v[104:107],v[196:199],a[44:47],v[104:107]
cv v235,v98,v99
cv v237,v102,v103
mm v[104:107],v[192:195],a[36:39],v[104:107]
rt v[196:197],v243 offset:50688
rt v[198:199],v243 offset:51712
mm v[104:107],v[200:203],a[52:55],v[104:107]
v_permlane16_swap_b32 v234, v236
v_permlane16_swap_b32 v235, v237
mm v[104:107],v[204:207],a[60:63],v[104:107]
rt v[192:193],v243 offset:59136
rt v[194:195],v243 offset:60160
mm v[72:75],v[204:207],a[56:59],v[72:75]
wt lgkmcnt(4)
ld a[84:87],v229,s[28:31],s93 offen offset:2304
mm v[72:75],v[200:203],a[48:51],v[72:75]
rt v[204:205],v243 offset:0
rt v[206:207],v243 offset:1024
mm v[72:75],v[208:211],v[128:131],v[72:75]
st v[234:237],v238,s[32:35],s97 offen offset:0 sc1
mm v[72:75],v[212:215],v[144:147],v[72:75]
rt v[200:201],v243 offset:8448
rt v[202:203],v243 offset:9472
mm v[104:107],v[212:215],v[148:151],v[104:107]
cv v230,v64,v65
cv v232,v68,v69
mm v[104:107],v[208:211],v[132:135],v[104:107]
rt v[212:213],v243 offset:16896
rt v[214:215],v243 offset:17920
mm v[104:107],v[216:219],v[164:167],v[104:107]
.La5_LW7_5:
s_cbranch_execz .La5_LA_RETURN
cv v231,v66,v67
cv v233,v70,v71
mm v[104:107],v[220:223],v[180:183],v[104:107]
rt v[208:209],v243 offset:25344
rt v[210:211],v243 offset:26368
mm v[72:75],v[220:223],v[176:179],v[72:75]
wt lgkmcnt(4)
s_mov_b32 s93, 12288
ld a[88:91],v229,s[28:31],s93 offen offset:256
mm v[72:75],v[216:219],v[160:163],v[72:75]
rt v[220:221],v243 offset:33792
rt v[222:223],v243 offset:34816
mm v[76:79],v[196:199],a[32:35],v[76:79]
v_permlane16_swap_b32 v230, v232
)ASM" R"ASM(v_permlane16_swap_b32 v231, v233
mm v[76:79],v[192:195],a[40:43],v[76:79]
rt v[216:217],v243 offset:42240
rt v[218:219],v243 offset:43264
mm v[108:111],v[192:195],a[44:47],v[108:111]
ld a[92:95],v229,s[28:31],s93 offen offset:2304
mm v[108:111],v[196:199],a[36:39],v[108:111]
rt v[192:193],v240 offset:50816
rt v[194:195],v240 offset:51840
mm v[108:111],v[204:207],a[52:55],v[108:111]
st v[230:233],v238,s[32:35],s96 offen offset:0 sc1
mm v[108:111],v[200:203],a[60:63],v[108:111]
rt v[196:197],v240 offset:59264
rt v[198:199],v240 offset:60288
mm v[76:79],v[200:203],a[56:59],v[76:79]
wt lgkmcnt(4)
v_lshrrev_b32 v248, 1, v248
mm v[76:79],v[204:207],a[48:51],v[76:79]
rt v[200:201],v240 offset:128
rt v[202:203],v240 offset:1152
mm v[76:79],v[212:215],v[128:131],v[76:79]
ld v[188:191],v225,s[48:51],0 offen offset:1536
mm v[76:79],v[208:211],v[144:147],v[76:79]
rt v[204:205],v240 offset:8576
rt v[206:207],v240 offset:9600
mm v[108:111],v[208:211],v[148:151],v[108:111]
mm v[108:111],v[212:215],v[132:135],v[108:111]
rt v[208:209],v240 offset:17024
rt v[210:211],v240 offset:18048
mm v[108:111],v[220:223],v[164:167],v[108:111]
mm v[108:111],v[216:219],v[180:183],v[108:111]
rt v[212:213],v240 offset:25472
rt v[214:215],v240 offset:26496
mm v[76:79],v[216:219],v[176:179],v[76:79]
wt lgkmcnt(4)
mm v[76:79],v[220:223],v[160:163],v[76:79]
rt v[216:217],v240 offset:33920
rt v[218:219],v240 offset:34944
mm v[80:83],v[192:195],a[32:35],v[80:83]
cv v234,v104,v105
cv v236,v108,v109
mm v[80:83],v[196:199],a[40:43],v[80:83]
rt v[220:221],v240 offset:42368
rt v[222:223],v240 offset:43392
mm v[112:115],v[196:199],a[44:47],v[112:115]
cv v235,v106,v107
cv v237,v110,v111
mm v[112:115],v[192:195],a[36:39],v[112:115]
rt v[196:197],v241 offset:50816
rt v[198:199],v241 offset:51840
mm v[112:115],v[200:203],a[52:55],v[112:115]
v_permlane16_swap_b32 v234, v236
v_permlane16_swap_b32 v235, v237
mm v[112:115],v[204:207],a[60:63],v[112:115]
rt v[192:193],v241 offset:59264
rt v[194:195],v241 offset:60288
mm v[80:83],v[204:207],a[56:59],v[80:83]
wt lgkmcnt(4)
mm v[80:83],v[200:203],a[48:51],v[80:83]
rt v[204:205],v241 offset:128
rt v[206:207],v241 offset:1152
mm v[80:83],v[208:211],v[128:131],v[80:83]
st v[234:237],v238,s[32:35],s97 offen offset:1024 sc1
mm v[80:83],v[212:215],v[144:147],v[80:83]
rt v[200:201],v241 offset:8576
rt v[202:203],v241 offset:9600
mm v[112:115],v[212:215],v[148:151],v[112:115]
cv v230,v72,v73
cv v232,v76,v77
mm v[112:115],v[208:211],v[132:135],v[112:115]
rt v[212:213],v241 offset:17024
rt v[214:215],v241 offset:18048
mm v[112:115],v[216:219],v[164:167],v[112:115]
cv v231,v74,v75
cv v233,v78,v79
mm v[112:115],v[220:223],v[180:183],v[112:115]
rt v[208:209],v241 offset:25472
rt v[210:211],v241 offset:26496
mm v[80:83],v[220:223],v[176:179],v[80:83]
wt lgkmcnt(4)
mm v[80:83],v[216:219],v[160:163],v[80:83]
rt v[220:221],v241 offset:33920
rt v[222:223],v241 offset:34944
mm v[84:87],v[196:199],a[32:35],v[84:87]
v_permlane16_swap_b32 v230, v232
v_permlane16_swap_b32 v231, v233
mm v[84:87],v[192:195],a[40:43],v[84:87]
rt v[216:217],v241 offset:42368
rt v[218:219],v241 offset:43392
mm v[116:119],v[192:195],a[44:47],v[116:119]
mm v[116:119],v[196:199],a[36:39],v[116:119]
rt v[192:193],v242 offset:50816
rt v[194:195],v242 offset:51840
mm v[116:119],v[204:207],a[52:55],v[116:119]
st v[230:233],v238,s[32:35],s96 offen offset:1024 sc1
mm v[116:119],v[200:203],a[60:63],v[116:119]
rt v[196:197],v242 offset:59264
rt v[198:199],v242 offset:60288
mm v[84:87],v[200:203],a[56:59],v[84:87]
wt lgkmcnt(4)
)ASM" R"ASM(mm v[84:87],v[204:207],a[48:51],v[84:87]
rt v[200:201],v242 offset:128
rt v[202:203],v242 offset:1152
mm v[84:87],v[212:215],v[128:131],v[84:87]
mm v[84:87],v[208:211],v[144:147],v[84:87]
rt v[204:205],v242 offset:8576
rt v[206:207],v242 offset:9600
mm v[116:119],v[208:211],v[148:151],v[116:119]
mm v[116:119],v[212:215],v[132:135],v[116:119]
rt v[208:209],v242 offset:17024
rt v[210:211],v242 offset:18048
mm v[116:119],v[220:223],v[164:167],v[116:119]
mm v[116:119],v[216:219],v[180:183],v[116:119]
rt v[212:213],v242 offset:25472
rt v[214:215],v242 offset:26496
mm v[84:87],v[216:219],v[176:179],v[84:87]
wt lgkmcnt(4)
mm v[84:87],v[220:223],v[160:163],v[84:87]
rt v[216:217],v242 offset:33920
rt v[218:219],v242 offset:34944
mm v[88:91],v[192:195],a[32:35],v[88:91]
cv v234,v112,v113
cv v236,v116,v117
mm v[88:91],v[196:199],a[40:43],v[88:91]
rt v[220:221],v242 offset:42368
rt v[222:223],v242 offset:43392
mm v[120:123],v[196:199],a[44:47],v[120:123]
cv v235,v114,v115
cv v237,v118,v119
mm v[120:123],v[192:195],a[36:39],v[120:123]
rt v[196:197],v243 offset:50816
rt v[198:199],v243 offset:51840
mm v[120:123],v[200:203],a[52:55],v[120:123]
v_permlane16_swap_b32 v234, v236
v_permlane16_swap_b32 v235, v237
mm v[120:123],v[204:207],a[60:63],v[120:123]
rt v[192:193],v243 offset:59264
rt v[194:195],v243 offset:60288
mm v[88:91],v[204:207],a[56:59],v[88:91]
wt lgkmcnt(4)
mm v[88:91],v[200:203],a[48:51],v[88:91]
rt v[204:205],v243 offset:128
rt v[206:207],v243 offset:1152
mm v[88:91],v[208:211],v[128:131],v[88:91]
st v[234:237],v238,s[32:35],s97 offen offset:2048 sc1
mm v[88:91],v[212:215],v[144:147],v[88:91]
rt v[200:201],v243 offset:8576
rt v[202:203],v243 offset:9600
mm v[120:123],v[212:215],v[148:151],v[120:123]
cv v230,v80,v81
cv v232,v84,v85
mm v[120:123],v[208:211],v[132:135],v[120:123]
rt v[212:213],v243 offset:17024
rt v[214:215],v243 offset:18048
mm v[120:123],v[216:219],v[164:167],v[120:123]
cv v231,v82,v83
cv v233,v86,v87
wt vmcnt(14)
buffer_store_dwordx2 v[252:253], v251, s[64:67], s92 offen sc1
mm v[120:123],v[220:223],v[180:183],v[120:123]
rt v[208:209],v243 offset:25472
rt v[210:211],v243 offset:26496
mm v[88:91],v[220:223],v[176:179],v[88:91]
wt lgkmcnt(4)
mm v[88:91],v[216:219],v[160:163],v[88:91]
rt v[220:221],v243 offset:33920
rt v[222:223],v243 offset:34944
mm v[92:95],v[196:199],a[32:35],v[92:95]
v_permlane16_swap_b32 v230, v232
v_permlane16_swap_b32 v231, v233
mm v[92:95],v[192:195],a[40:43],v[92:95]
rt v[216:217],v243 offset:42368
rt v[218:219],v243 offset:43392
mm v[124:127],v[192:195],a[44:47],v[124:127]
mm v[124:127],v[196:199],a[36:39],v[124:127]
mm v[124:127],v[204:207],a[52:55],v[124:127]
st v[230:233],v238,s[32:35],s96 offen offset:2048 sc1
mm v[124:127],v[200:203],a[60:63],v[124:127]
mm v[92:95],v[200:203],a[56:59],v[92:95]
wt lgkmcnt(0)
mm v[92:95],v[204:207],a[48:51],v[92:95]
mm v[92:95],v[212:215],v[128:131],v[92:95]
mm v[92:95],v[208:211],v[144:147],v[92:95]
mm v[124:127],v[208:211],v[148:151],v[124:127]
mm v[124:127],v[212:215],v[132:135],v[124:127]
mm v[124:127],v[220:223],v[164:167],v[124:127]
mm v[124:127],v[216:219],v[180:183],v[124:127]
mm v[92:95],v[216:219],v[176:179],v[92:95]
mm v[92:95],v[220:223],v[160:163],v[92:95]
s_nop 7
cv v234,v120,v121
cv v236,v124,v125
cv v235,v122,v123
cv v237,v126,v127
cv v230,v88,v89
cv v232,v92,v93
cv v231,v90,v91
cv v233,v94,v95
v_permlane16_swap_b32 v234, v236
v_permlane16_swap_b32 v235, v237
)ASM" R"ASM(st v[234:237],v238,s[32:35],s97 offen offset:3072 sc1
v_permlane16_swap_b32 v230, v232
v_permlane16_swap_b32 v231, v233
st v[230:233],v238,s[32:35],s96 offen offset:3072 sc1
.La5_LT_0:
.La5_LW3_5:
s_cbranch_execz .La5_LA_RETURN
.La5_LW7_6:
s_cbranch_execz .La5_LA_RETURN
s_lshl_b32 s91, s76, 5
s_add_u32 s91, s91, s90
s_lshl_b32 s20, s91, 9
v_mov_b32 v236, 157440
ds_read_b32 v236, v236
s_andn2_b32 s93, s20, 0x1fff
s_add_u32 s20, s20, s93
s_add_u32 s20, s20, 2097152
s_lshr_b32 s93, s91, 1
s_lshl_b32 s93, s93, 15
s_and_b32 s94, s91, 1
s_lshl2_add_u32 s94, s94, s3
s_lshl_b32 s94, s94, 7
s_add_u32 s97, s93, s94
s_add_u32 s32, s26, 71303168
s_addc_u32 s33, s27, 0
s_and_b32 s33, s33, 0xffff
s_mov_b32 s34, 16777216
s_mov_b32 s35, 0x20000
s_add_u32 s44, s26, 171966464
s_addc_u32 s45, s27, 0
s_and_b32 s45, s45, 0xffff
s_mov_b32 s46, 2097152
s_mov_b32 s47, 0x20000
s_add_u32 s56, s38, 0
s_addc_u32 s57, s39, 0
s_and_b32 s57, s57, 0xffff
s_mov_b32 s58, 4194304
s_mov_b32 s59, 0x20000
s_add_u32 s68, s16, 0
s_addc_u32 s69, s17, 0
s_and_b32 s69, s69, 0xffff
s_mov_b32 s70, 4194304
s_mov_b32 s71, 0x20000
v_and_b32 v230, 0x70, v255
v_lshrrev_b32 v231, 7, v255
v_lshl_add_u32 v230, v231, 10, v230
v_add_u32 v240, s97, v230
v_xor_b32 v241, 8192, v240
v_xor_b32 v242, 16384, v240
v_xor_b32 v243, 24576, v240
wt lgkmcnt(0)
v_readfirstlane_b32 s93, v236
s_nop 0
s_and_b32 s93, s93, 8192
s_add_u32 s20, s20, s93
v_mov_b32 v237, 0x3f803f80
v_mov_b32 v236, 0
wt vmcnt(7)
v_dot2_f32_bf16 v236, v140, v237, v236
v_dot2_f32_bf16 v236, v141, v237, v236
v_dot2_f32_bf16 v236, v142, v237, v236
v_dot2_f32_bf16 v236, v143, v237, v236
v_dot2_f32_bf16 v236, v156, v237, v236
v_dot2_f32_bf16 v236, v157, v237, v236
v_dot2_f32_bf16 v236, v158, v237, v236
v_dot2_f32_bf16 v236, v159, v237, v236
v_dot2_f32_bf16 v236, v172, v237, v236
v_dot2_f32_bf16 v236, v173, v237, v236
v_dot2_f32_bf16 v236, v174, v237, v236
v_dot2_f32_bf16 v236, v175, v237, v236
v_dot2_f32_bf16 v236, v188, v237, v236
v_dot2_f32_bf16 v236, v189, v237, v236
v_dot2_f32_bf16 v236, v190, v237, v236
v_dot2_f32_bf16 v236, v191, v237, v236
v_lshlrev_b32 v0, 16, v136
v_and_b32 v1, 0xffff0000, v136
v_lshlrev_b32 v2, 16, v137
v_and_b32 v3, 0xffff0000, v137
v_lshlrev_b32 v4, 16, v138
v_and_b32 v5, 0xffff0000, v138
v_lshlrev_b32 v6, 16, v139
v_and_b32 v7, 0xffff0000, v139
v_lshlrev_b32 v8, 16, v152
v_and_b32 v9, 0xffff0000, v152
v_lshlrev_b32 v10, 16, v153
v_and_b32 v11, 0xffff0000, v153
v_lshlrev_b32 v12, 16, v154
v_and_b32 v13, 0xffff0000, v154
v_lshlrev_b32 v14, 16, v155
v_and_b32 v15, 0xffff0000, v155
v_lshlrev_b32 v16, 16, v168
v_and_b32 v17, 0xffff0000, v168
v_lshlrev_b32 v18, 16, v169
v_and_b32 v19, 0xffff0000, v169
v_lshlrev_b32 v20, 16, v170
v_and_b32 v21, 0xffff0000, v170
v_lshlrev_b32 v22, 16, v171
v_and_b32 v23, 0xffff0000, v171
v_lshlrev_b32 v24, 16, v184
v_and_b32 v25, 0xffff0000, v184
)ASM" R"ASM(v_lshlrev_b32 v26, 16, v185
v_and_b32 v27, 0xffff0000, v185
v_lshlrev_b32 v28, 16, v186
v_and_b32 v29, 0xffff0000, v186
v_lshlrev_b32 v30, 16, v187
v_and_b32 v31, 0xffff0000, v187
s_nop 1
v_add_f32_dpp v236, v236, v236 quad_perm:[1,0,3,2] row_mask:0xf bank_mask:0xf
s_nop 1
v_add_f32_dpp v236, v236, v236 quad_perm:[2,3,0,1] row_mask:0xf bank_mask:0xf
s_nop 1
v_add_f32_dpp v236, v236, v236 row_ror:8 row_mask:0xf bank_mask:0xf
v_mov_b32 v230, v236
s_nop 1
v_permlane16_swap_b32 v230, v236
s_nop 1
v_add_f32 v236, v236, v230
v_mov_b32 v230, v236
s_nop 1
v_permlane32_swap_b32 v230, v236
s_nop 1
v_add_f32 v236, v236, v230
s_mov_b64 exec, 0
s_cmp_eq_u32 s3, 0
s_cbranch_scc1 .La5_LW4_0
s_cmp_eq_u32 s3, 1
s_cbranch_scc1 .La5_LW4_1
s_cmp_eq_u32 s3, 2
s_cbranch_scc1 .La5_LW4_2
s_branch .La5_LW4_3
.La5_LW4_R0:
s_mov_b64 exec, -1
wt vmcnt(0)
buffer_store_dwordx2 v[252:253], v250, s[64:67], s92 offen sc1
s_mov_b32 s85, 99
s_cmp_eq_u32 s30, 0
s_cbranch_scc1 .La5_LT_ROWS
; ---- h = x + attn, LayerNorm statistics, router score, two best of eight rows
.La5_LT_ROWS:
s_mov_b32 s96, 0
.La5_LT_P3:
buffer_load_dwordx2 v[250:251], v248, s[64:67], s20 offen sc1
wt vmcnt(0)
.La5_LT_P2:
v_cmp_eq_u64 vcc, v[250:251], v[252:253]
s_add_u32 s96, s96, 1
s_lshr_b32 s92, vcc_lo, 1
s_and_b32 s92, s92, vcc_lo
s_lshr_b32 s93, s92, 2
s_and_b32 s92, s92, s93
s_lshr_b32 s93, s92, 4
s_and_b32 s92, s92, s93
s_lshr_b32 s93, s96, 20
s_mul_i32 s93, s93, 0x01010101
s_or_b32 s92, s92, s93
s_andn2_b32 s92, 0x01010101, s92
s_bcnt1_i32_b32 s93, s92
s_cmp_gt_u32 s93, 1
s_nop 0
s_nop 0
s_cbranch_scc1 .La5_LT_P3S
s_or_b32 s93, s92, 0x01000000
s_ff1_i32_b32 s93, s93
s_lshl_b32 s80, s93, 19
s_xor_b32 s93, s80, 8388608
s_xor_b32 s94, s80, 12582912
s_xor_b32 s95, s80, 4194304
ld v[208:211],v240,s[32:35],s93 offen sc1
ld v[212:215],v241,s[32:35],s93 offen sc1
ld v[216:219],v242,s[32:35],s93 offen sc1
ld v[220:223],v243,s[32:35],s93 offen sc1
ld v[112:115],v240,s[32:35],s94 offen sc1
ld v[116:119],v241,s[32:35],s94 offen sc1
ld v[120:123],v242,s[32:35],s94 offen sc1
ld v[124:127],v243,s[32:35],s94 offen sc1
ld v[96:99],v240,s[32:35],s95 offen sc1
ld v[100:103],v241,s[32:35],s95 offen sc1
ld v[104:107],v242,s[32:35],s95 offen sc1
ld v[108:111],v243,s[32:35],s95 offen sc1
s_cmp_lg_u32 s92, 0
s_cbranch_scc1 .La5_LT_P4
ld v[192:195],v240,s[32:35],s80 offen sc1
ld v[196:199],v241,s[32:35],s80 offen sc1
ld v[200:203],v242,s[32:35],s80 offen sc1
ld v[204:207],v243,s[32:35],s80 offen sc1
.La5_LT_P4:
s_lshr_b32 s93, s80, 16
v_and_b32 v248, 56, v248
s_add_u32 s20, s20, s93
buffer_load_dwordx2 v[250:251], v248, s[64:67], s20 offen sc1
wt vmcnt(8)
v_lshlrev_b32 v32, 16, v208
v_and_b32 v33, 0xffff0000, v208
v_lshlrev_b32 v230, 16, v112
v_and_b32 v231, 0xffff0000, v112
v_pk_add_f32 v[32:33], v[32:33], v[230:231]
v_lshlrev_b32 v34, 16, v209
v_and_b32 v35, 0xffff0000, v209
v_lshlrev_b32 v230, 16, v113
v_and_b32 v231, 0xffff0000, v113
)ASM" R"ASM(v_pk_add_f32 v[34:35], v[34:35], v[230:231]
v_lshlrev_b32 v36, 16, v210
v_and_b32 v37, 0xffff0000, v210
v_lshlrev_b32 v230, 16, v114
v_and_b32 v231, 0xffff0000, v114
v_pk_add_f32 v[36:37], v[36:37], v[230:231]
v_lshlrev_b32 v38, 16, v211
v_and_b32 v39, 0xffff0000, v211
v_lshlrev_b32 v230, 16, v115
v_and_b32 v231, 0xffff0000, v115
v_pk_add_f32 v[38:39], v[38:39], v[230:231]
wt vmcnt(7)
v_lshlrev_b32 v40, 16, v212
v_and_b32 v41, 0xffff0000, v212
v_lshlrev_b32 v230, 16, v116
v_and_b32 v231, 0xffff0000, v116
v_pk_add_f32 v[40:41], v[40:41], v[230:231]
v_lshlrev_b32 v42, 16, v213
v_and_b32 v43, 0xffff0000, v213
v_lshlrev_b32 v230, 16, v117
v_and_b32 v231, 0xffff0000, v117
v_pk_add_f32 v[42:43], v[42:43], v[230:231]
v_lshlrev_b32 v44, 16, v214
v_and_b32 v45, 0xffff0000, v214
v_lshlrev_b32 v230, 16, v118
v_and_b32 v231, 0xffff0000, v118
v_pk_add_f32 v[44:45], v[44:45], v[230:231]
v_lshlrev_b32 v46, 16, v215
v_and_b32 v47, 0xffff0000, v215
v_lshlrev_b32 v230, 16, v119
v_and_b32 v231, 0xffff0000, v119
v_pk_add_f32 v[46:47], v[46:47], v[230:231]
wt vmcnt(6)
v_lshlrev_b32 v48, 16, v216
v_and_b32 v49, 0xffff0000, v216
v_lshlrev_b32 v230, 16, v120
v_and_b32 v231, 0xffff0000, v120
v_pk_add_f32 v[48:49], v[48:49], v[230:231]
v_lshlrev_b32 v50, 16, v217
v_and_b32 v51, 0xffff0000, v217
v_lshlrev_b32 v230, 16, v121
v_and_b32 v231, 0xffff0000, v121
v_pk_add_f32 v[50:51], v[50:51], v[230:231]
v_lshlrev_b32 v52, 16, v218
v_and_b32 v53, 0xffff0000, v218
v_lshlrev_b32 v230, 16, v122
v_and_b32 v231, 0xffff0000, v122
v_pk_add_f32 v[52:53], v[52:53], v[230:231]
v_lshlrev_b32 v54, 16, v219
v_and_b32 v55, 0xffff0000, v219
v_lshlrev_b32 v230, 16, v123
v_and_b32 v231, 0xffff0000, v123
v_pk_add_f32 v[54:55], v[54:55], v[230:231]
wt vmcnt(5)
v_lshlrev_b32 v56, 16, v220
v_and_b32 v57, 0xffff0000, v220
v_lshlrev_b32 v230, 16, v124
v_and_b32 v231, 0xffff0000, v124
v_pk_add_f32 v[56:57], v[56:57], v[230:231]
v_lshlrev_b32 v58, 16, v221
v_and_b32 v59, 0xffff0000, v221
v_lshlrev_b32 v230, 16, v125
v_and_b32 v231, 0xffff0000, v125
v_pk_add_f32 v[58:59], v[58:59], v[230:231]
v_lshlrev_b32 v60, 16, v222
v_and_b32 v61, 0xffff0000, v222
v_lshlrev_b32 v230, 16, v126
v_and_b32 v231, 0xffff0000, v126
v_pk_add_f32 v[60:61], v[60:61], v[230:231]
v_lshlrev_b32 v62, 16, v223
v_and_b32 v63, 0xffff0000, v223
v_lshlrev_b32 v230, 16, v127
v_and_b32 v231, 0xffff0000, v127
v_pk_add_f32 v[62:63], v[62:63], v[230:231]
.La5_LT_PH:
wt vmcnt(1)
v_lshlrev_b32 v64, 16, v96
v_and_b32 v65, 0xffff0000, v96
v_lshlrev_b32 v66, 16, v97
v_and_b32 v67, 0xffff0000, v97
v_lshlrev_b32 v68, 16, v98
v_and_b32 v69, 0xffff0000, v98
v_lshlrev_b32 v70, 16, v99
v_and_b32 v71, 0xffff0000, v99
v_lshlrev_b32 v72, 16, v100
v_and_b32 v73, 0xffff0000, v100
v_lshlrev_b32 v74, 16, v101
v_and_b32 v75, 0xffff0000, v101
v_lshlrev_b32 v76, 16, v102
v_and_b32 v77, 0xffff0000, v102
v_lshlrev_b32 v78, 16, v103
v_and_b32 v79, 0xffff0000, v103
v_lshlrev_b32 v80, 16, v104
v_and_b32 v81, 0xffff0000, v104
v_lshlrev_b32 v82, 16, v105
v_and_b32 v83, 0xffff0000, v105
v_lshlrev_b32 v84, 16, v106
v_and_b32 v85, 0xffff0000, v106
v_lshlrev_b32 v86, 16, v107
v_and_b32 v87, 0xffff0000, v107
)ASM" R"ASM(v_lshlrev_b32 v88, 16, v108
v_and_b32 v89, 0xffff0000, v108
v_lshlrev_b32 v90, 16, v109
v_and_b32 v91, 0xffff0000, v109
v_lshlrev_b32 v92, 16, v110
v_and_b32 v93, 0xffff0000, v110
v_lshlrev_b32 v94, 16, v111
v_and_b32 v95, 0xffff0000, v111
v_mov_b32 v232, 0
v_mov_b32 v233, 0
v_mov_b32 v234, 0
s_cmp_eq_u32 s92, 0
s_cbranch_scc1 .La5_LT_SUM
.La5_LT_P5:
wt vmcnt(0)
v_cmp_eq_u64 vcc, v[250:251], v[252:253]
s_add_u32 s96, s96, 1
s_cmp_eq_u64 vcc, -1
s_cbranch_scc1 .La5_LT_P6
s_lshr_b32 s93, s96, 20
s_cbranch_scc1 .La5_LT_P6
s_sleep 3
buffer_load_dwordx2 v[250:251], v248, s[64:67], s20 offen sc1
s_branch .La5_LT_P5
.La5_LT_P3S:
s_sleep 4
s_bcnt0_i32_b32 s93, vcc_lo
s_cmp_le_u32 s93, 8
s_cbranch_scc1 .La5_LT_P3
s_sleep 24
s_branch .La5_LT_P3
.La5_LT_P6:
ld v[192:195],v240,s[32:35],s80 offen sc1
ld v[196:199],v241,s[32:35],s80 offen sc1
ld v[200:203],v242,s[32:35],s80 offen sc1
ld v[204:207],v243,s[32:35],s80 offen sc1
.La5_LT_SUM:
wt vmcnt(3)
v_lshlrev_b32 v208, 16, v192
v_and_b32 v209, 0xffff0000, v192
v_lshlrev_b32 v210, 16, v193
v_and_b32 v211, 0xffff0000, v193
v_lshlrev_b32 v212, 16, v194
v_and_b32 v213, 0xffff0000, v194
v_lshlrev_b32 v214, 16, v195
v_and_b32 v215, 0xffff0000, v195
v_pk_add_f32 v[208:209], v[208:209], v[64:65]
v_pk_add_f32 v[210:211], v[210:211], v[66:67]
v_pk_add_f32 v[212:213], v[212:213], v[68:69]
v_pk_add_f32 v[214:215], v[214:215], v[70:71]
v_pk_add_f32 v[208:209], v[208:209], v[32:33]
v_pk_add_f32 v[210:211], v[210:211], v[34:35]
v_pk_add_f32 v[212:213], v[212:213], v[36:37]
v_pk_add_f32 v[214:215], v[214:215], v[38:39]
v_pk_add_f32 v[208:209], v[208:209], v[0:1]
v_pk_add_f32 v[210:211], v[210:211], v[2:3]
v_pk_add_f32 v[212:213], v[212:213], v[4:5]
v_pk_add_f32 v[214:215], v[214:215], v[6:7]
cv v96,v208,v209
cv v97,v210,v211
cv v98,v212,v213
cv v99,v214,v215
v_dot2_f32_bf16 v232, v96, v237, v232
v_dot2_f32_bf16 v233, v96, v96, v233
v_dot2_f32_bf16 v234, v96, v140, v234
v_dot2_f32_bf16 v232, v97, v237, v232
v_dot2_f32_bf16 v233, v97, v97, v233
v_dot2_f32_bf16 v234, v97, v141, v234
v_dot2_f32_bf16 v232, v98, v237, v232
v_dot2_f32_bf16 v233, v98, v98, v233
v_dot2_f32_bf16 v234, v98, v142, v234
v_dot2_f32_bf16 v232, v99, v237, v232
v_dot2_f32_bf16 v233, v99, v99, v233
v_dot2_f32_bf16 v234, v99, v143, v234
wt vmcnt(2)
v_lshlrev_b32 v208, 16, v196
v_and_b32 v209, 0xffff0000, v196
v_lshlrev_b32 v210, 16, v197
v_and_b32 v211, 0xffff0000, v197
v_lshlrev_b32 v212, 16, v198
v_and_b32 v213, 0xffff0000, v198
v_lshlrev_b32 v214, 16, v199
v_and_b32 v215, 0xffff0000, v199
v_pk_add_f32 v[208:209], v[208:209], v[72:73]
v_pk_add_f32 v[210:211], v[210:211], v[74:75]
v_pk_add_f32 v[212:213], v[212:213], v[76:77]
v_pk_add_f32 v[214:215], v[214:215], v[78:79]
v_pk_add_f32 v[208:209], v[208:209], v[40:41]
v_pk_add_f32 v[210:211], v[210:211], v[42:43]
v_pk_add_f32 v[212:213], v[212:213], v[44:45]
v_pk_add_f32 v[214:215], v[214:215], v[46:47]
v_pk_add_f32 v[208:209], v[208:209], v[8:9]
v_pk_add_f32 v[210:211], v[210:211], v[10:11]
v_pk_add_f32 v[212:213], v[212:213], v[12:13]
v_pk_add_f32 v[214:215], v[214:215], v[14:15]
cv v100,v208,v209
cv v101,v210,v211
cv v102,v212,v213
cv v103,v214,v215
v_dot2_f32_bf16 v232, v100, v237, v232
)ASM" R"ASM(v_dot2_f32_bf16 v233, v100, v100, v233
v_dot2_f32_bf16 v234, v100, v156, v234
v_dot2_f32_bf16 v232, v101, v237, v232
v_dot2_f32_bf16 v233, v101, v101, v233
v_dot2_f32_bf16 v234, v101, v157, v234
v_dot2_f32_bf16 v232, v102, v237, v232
v_dot2_f32_bf16 v233, v102, v102, v233
v_dot2_f32_bf16 v234, v102, v158, v234
v_dot2_f32_bf16 v232, v103, v237, v232
v_dot2_f32_bf16 v233, v103, v103, v233
v_dot2_f32_bf16 v234, v103, v159, v234
wt vmcnt(1)
.La5_LW4_0:
s_cbranch_execz .La5_LW4_4
v_lshlrev_b32 v208, 16, v200
v_and_b32 v209, 0xffff0000, v200
v_lshlrev_b32 v210, 16, v201
v_and_b32 v211, 0xffff0000, v201
v_lshlrev_b32 v212, 16, v202
v_and_b32 v213, 0xffff0000, v202
v_lshlrev_b32 v214, 16, v203
v_and_b32 v215, 0xffff0000, v203
v_pk_add_f32 v[208:209], v[208:209], v[80:81]
v_pk_add_f32 v[210:211], v[210:211], v[82:83]
v_pk_add_f32 v[212:213], v[212:213], v[84:85]
v_pk_add_f32 v[214:215], v[214:215], v[86:87]
v_pk_add_f32 v[208:209], v[208:209], v[48:49]
v_pk_add_f32 v[210:211], v[210:211], v[50:51]
v_pk_add_f32 v[212:213], v[212:213], v[52:53]
v_pk_add_f32 v[214:215], v[214:215], v[54:55]
v_pk_add_f32 v[208:209], v[208:209], v[16:17]
v_pk_add_f32 v[210:211], v[210:211], v[18:19]
v_pk_add_f32 v[212:213], v[212:213], v[20:21]
v_pk_add_f32 v[214:215], v[214:215], v[22:23]
cv v104,v208,v209
cv v105,v210,v211
cv v106,v212,v213
cv v107,v214,v215
v_dot2_f32_bf16 v232, v104, v237, v232
v_dot2_f32_bf16 v233, v104, v104, v233
v_dot2_f32_bf16 v234, v104, v172, v234
v_dot2_f32_bf16 v232, v105, v237, v232
v_dot2_f32_bf16 v233, v105, v105, v233
v_dot2_f32_bf16 v234, v105, v173, v234
v_dot2_f32_bf16 v232, v106, v237, v232
v_dot2_f32_bf16 v233, v106, v106, v233
v_dot2_f32_bf16 v234, v106, v174, v234
v_dot2_f32_bf16 v232, v107, v237, v232
v_dot2_f32_bf16 v233, v107, v107, v233
v_dot2_f32_bf16 v234, v107, v175, v234
wt vmcnt(0)
v_lshlrev_b32 v208, 16, v204
v_and_b32 v209, 0xffff0000, v204
v_lshlrev_b32 v210, 16, v205
v_and_b32 v211, 0xffff0000, v205
v_lshlrev_b32 v212, 16, v206
v_and_b32 v213, 0xffff0000, v206
v_lshlrev_b32 v214, 16, v207
v_and_b32 v215, 0xffff0000, v207
v_pk_add_f32 v[208:209], v[208:209], v[88:89]
v_pk_add_f32 v[210:211], v[210:211], v[90:91]
v_pk_add_f32 v[212:213], v[212:213], v[92:93]
v_pk_add_f32 v[214:215], v[214:215], v[94:95]
v_pk_add_f32 v[208:209], v[208:209], v[56:57]
v_pk_add_f32 v[210:211], v[210:211], v[58:59]
v_pk_add_f32 v[212:213], v[212:213], v[60:61]
v_pk_add_f32 v[214:215], v[214:215], v[62:63]
v_pk_add_f32 v[208:209], v[208:209], v[24:25]
v_pk_add_f32 v[210:211], v[210:211], v[26:27]
v_pk_add_f32 v[212:213], v[212:213], v[28:29]
v_pk_add_f32 v[214:215], v[214:215], v[30:31]
cv v108,v208,v209
cv v109,v210,v211
cv v110,v212,v213
cv v111,v214,v215
v_dot2_f32_bf16 v232, v108, v237, v232
v_dot2_f32_bf16 v233, v108, v108, v233
v_dot2_f32_bf16 v234, v108, v188, v234
v_dot2_f32_bf16 v232, v109, v237, v232
v_dot2_f32_bf16 v233, v109, v109, v233
v_dot2_f32_bf16 v234, v109, v189, v234
v_dot2_f32_bf16 v232, v110, v237, v232
v_dot2_f32_bf16 v233, v110, v110, v233
v_dot2_f32_bf16 v234, v110, v190, v234
v_dot2_f32_bf16 v232, v111, v237, v232
v_dot2_f32_bf16 v233, v111, v111, v233
v_dot2_f32_bf16 v234, v111, v191, v234
wt vmcnt(0)
s_lshl2_add_u32 s92, s91, s3
s_lshl_b32 s92, s92, 1
s_sub_u32 s92, 0xffff, s92
v_add_f32_dpp v232, v232, v232 quad_perm:[1,0,3,2] row_mask:0xf bank_mask:0xf
v_add_f32_dpp v233, v233, v233 quad_perm:[1,0,3,2] row_mask:0xf bank_mask:0xf
v_add_f32_dpp v234, v234, v234 quad_perm:[1,0,3,2] row_mask:0xf bank_mask:0xf
v_add_f32_dpp v232, v232, v232 quad_perm:[2,3,0,1] row_mask:0xf bank_mask:0xf
v_add_f32_dpp v233, v233, v233 quad_perm:[2,3,0,1] row_mask:0xf bank_mask:0xf
v_add_f32_dpp v234, v234, v234 quad_perm:[2,3,0,1] row_mask:0xf bank_mask:0xf
v_add_f32_dpp v232, v232, v232 row_ror:8 row_mask:0xf bank_mask:0xf
v_add_f32_dpp v233, v233, v233 row_ror:8 row_mask:0xf bank_mask:0xf
v_add_f32_dpp v234, v234, v234 row_ror:8 row_mask:0xf bank_mask:0xf
)ASM" R"ASM(v_mov_b32 v230, v232
v_mov_b32 v231, v233
v_mov_b32 v235, v234
v_permlane16_swap_b32 v230, v232
v_permlane16_swap_b32 v231, v233
v_permlane16_swap_b32 v235, v234
v_add_f32 v232, v232, v230
v_add_f32 v233, v233, v231
v_add_f32 v234, v234, v235
v_mov_b32 v230, v232
v_mov_b32 v231, v233
v_mov_b32 v235, v234
v_permlane32_swap_b32 v230, v232
v_permlane32_swap_b32 v231, v233
v_permlane32_swap_b32 v235, v234
v_add_f32 v232, v232, v230
v_add_f32 v233, v233, v231
v_add_f32 v234, v234, v235
v_mul_f32 v244, 0x3a800000, v232
v_mul_f32 v233, 0x3a800000, v233
v_fma_f32 v230, -v244, v244, v233
v_mul_f32 v233, 0x3a800000, v233
v_cmp_nge_f32 vcc, v230, v233
v_bfe_u32 v235, v255, 6, 1
v_sub_u32_e64 v235, s92, v235
s_cbranch_vccnz .La5_LT_VAR
.La5_LT_VOK:
cv v244,v244,v244
v_lshlrev_b32 v244, 16, v244
cv v230,v230,v230
v_lshlrev_b32 v230, 16, v230
v_fma_f32 v234, -v244, v236, v234
v_add_f32 v230, 0x3727c5ac, v230
cv v230,v230,v230
v_lshlrev_b32 v230, 16, v230
v_sqrt_f32 v230, v230
v_lshrrev_b32 v232, 3, v255
cv v230,v230,v230
v_lshlrev_b32 v231, 16, v230
v_rcp_f32 v246, v231
v_lshrrev_b32 v230, 16, v230
v_or_b32 v233, v244, v230
v_mul_f32 v234, v234, v246
s_lshl_b32 s93, s3, 4
cv v234,v234,v234
v_and_b32 v230, 8, v232
v_ashrrev_i32 v232, 31, v234
v_or_b32 v232, 0x80000000, v232
v_xor_b32 v232, v232, v234
v_and_b32 v232, 0xffff0000, v232
v_add_u32 v230, s93, v230
v_or_b32 v232, v232, v235
ds_write_b64 v230, v[232:233] offset:0
v_lshrrev_b32 v230, 1, v255
v_and_b32 v230, 56, v230
v_mov_b32 v112, v232
wt lgkmcnt(0)
s_barrier
ds_read_b64 v[232:233], v230 offset:0
s_lshl3_add_u32 s92, s90, s76
s_lshl_b32 s92, s92, 4
s_lshl_b32 s93, s3, 28
s_add_u32 s92, s92, s93
v_lshrrev_b32 v235, 7, v255
v_lshlrev_b32 v235, 12, v235
wt lgkmcnt(0)
v_mov_b32 v234, v232
s_nop 1
v_max_u32_dpp v234, v234, v234 quad_perm:[1,0,3,2] row_mask:0xf bank_mask:0xf
s_nop 1
v_max_u32_dpp v234, v234, v234 quad_perm:[2,3,0,1] row_mask:0xf bank_mask:0xf
s_nop 1
v_max_u32_dpp v234, v234, v234 row_half_mirror row_mask:0xf bank_mask:0xf
s_nop 1
v_cmp_eq_u32 vcc, v232, v234
v_add_u32 v231, 8, v235
s_nop 0
v_cndmask_b32 v234, v232, 0, vcc
v_cndmask_b32 v235, v231, v235, vcc
s_nop 0
v_max_u32_dpp v234, v234, v234 quad_perm:[1,0,3,2] row_mask:0xf bank_mask:0xf
s_nop 1
v_max_u32_dpp v234, v234, v234 quad_perm:[2,3,0,1] row_mask:0xf bank_mask:0xf
s_nop 1
v_max_u32_dpp v234, v234, v234 row_half_mirror row_mask:0xf bank_mask:0xf
s_nop 1
v_cmp_ge_u32 vcc, v232, v234
s_nop 1
s_mov_b64 exec, vcc
buffer_store_dwordx2 v[232:233], v235, s[44:47], s92 offen sc1
s_mov_b64 exec, -1
; ---- out = h + .La5_LN(h) of the own rows; FFN weights into MFMA order
.La5_LT_OUT:
v_mul_f32 v244, v244, v246
v_mov_b32 v247, v246
v_xor_b32 v244, 0x80000000, v244
v_mov_b32 v245, v244
v_cmp_eq_u32 vcc, v112, v234
v_cmp_lt_u32 s[94:95], v112, v234
v_lshrrev_b32 v113, 7, v255
)ASM" R"ASM(v_and_b32 v114, 48, v255
v_lshl_add_u32 v113, v113, 6, v114
v_add_u32 v114, 2048, v113
v_mov_b32 v115, 1073741824
v_cndmask_b32 v113, v113, v114, vcc
s_lshl_b32 s92, s91, 12
v_cndmask_b32 v113, v113, v115, s[94:95]
s_add_u32 s92, s92, 1048576
v_lshlrev_b32 v0, 16, v96
v_and_b32 v1, 0xffff0000, v96
v_pk_fma_f32 v[232:233], v[0:1], v[246:247], v[244:245]
cv v64,v232,v233
v_lshlrev_b32 v2, 16, v97
v_and_b32 v3, 0xffff0000, v97
v_pk_fma_f32 v[232:233], v[2:3], v[246:247], v[244:245]
cv v65,v232,v233
v_lshlrev_b32 v4, 16, v98
v_and_b32 v5, 0xffff0000, v98
v_pk_fma_f32 v[232:233], v[4:5], v[246:247], v[244:245]
cv v66,v232,v233
v_lshlrev_b32 v6, 16, v99
v_and_b32 v7, 0xffff0000, v99
v_pk_fma_f32 v[232:233], v[6:7], v[246:247], v[244:245]
cv v67,v232,v233
st v[64:67],v113,s[44:47],s92 offen offset:0 sc1
v_lshlrev_b32 v8, 16, v100
v_and_b32 v9, 0xffff0000, v100
v_pk_fma_f32 v[232:233], v[8:9], v[246:247], v[244:245]
cv v68,v232,v233
v_lshlrev_b32 v10, 16, v101
v_and_b32 v11, 0xffff0000, v101
v_pk_fma_f32 v[232:233], v[10:11], v[246:247], v[244:245]
cv v69,v232,v233
v_lshlrev_b32 v12, 16, v102
v_and_b32 v13, 0xffff0000, v102
v_pk_fma_f32 v[232:233], v[12:13], v[246:247], v[244:245]
cv v70,v232,v233
v_lshlrev_b32 v14, 16, v103
v_and_b32 v15, 0xffff0000, v103
v_pk_fma_f32 v[232:233], v[14:15], v[246:247], v[244:245]
cv v71,v232,v233
st v[68:71],v113,s[44:47],s92 offen offset:512 sc1
v_lshlrev_b32 v16, 16, v104
v_and_b32 v17, 0xffff0000, v104
v_pk_fma_f32 v[232:233], v[16:17], v[246:247], v[244:245]
cv v72,v232,v233
v_lshlrev_b32 v18, 16, v105
v_and_b32 v19, 0xffff0000, v105
v_pk_fma_f32 v[232:233], v[18:19], v[246:247], v[244:245]
cv v73,v232,v233
v_lshlrev_b32 v20, 16, v106
v_and_b32 v21, 0xffff0000, v106
v_pk_fma_f32 v[232:233], v[20:21], v[246:247], v[244:245]
cv v74,v232,v233
v_lshlrev_b32 v22, 16, v107
v_and_b32 v23, 0xffff0000, v107
v_pk_fma_f32 v[232:233], v[22:23], v[246:247], v[244:245]
cv v75,v232,v233
st v[72:75],v113,s[44:47],s92 offen offset:1024 sc1
v_lshlrev_b32 v24, 16, v108
v_and_b32 v25, 0xffff0000, v108
v_pk_fma_f32 v[232:233], v[24:25], v[246:247], v[244:245]
cv v76,v232,v233
v_lshlrev_b32 v26, 16, v109
v_and_b32 v27, 0xffff0000, v109
v_pk_fma_f32 v[232:233], v[26:27], v[246:247], v[244:245]
cv v77,v232,v233
v_lshlrev_b32 v28, 16, v110
v_and_b32 v29, 0xffff0000, v110
v_pk_fma_f32 v[232:233], v[28:29], v[246:247], v[244:245]
cv v78,v232,v233
v_lshlrev_b32 v30, 16, v111
v_and_b32 v31, 0xffff0000, v111
v_pk_fma_f32 v[232:233], v[30:31], v[246:247], v[244:245]
cv v79,v232,v233
st v[76:79],v113,s[44:47],s92 offen offset:1536 sc1
v_lshlrev_b32 v234, 16, v64
v_and_b32 v235, 0xffff0000, v64
v_pk_add_f32 v[230:231], v[0:1], v[234:235]
cv v192,v230,v231
v_lshlrev_b32 v234, 16, v65
v_and_b32 v235, 0xffff0000, v65
v_pk_add_f32 v[230:231], v[2:3], v[234:235]
cv v193,v230,v231
v_lshlrev_b32 v234, 16, v66
v_and_b32 v235, 0xffff0000, v66
v_pk_add_f32 v[230:231], v[4:5], v[234:235]
cv v194,v230,v231
v_lshlrev_b32 v234, 16, v67
v_and_b32 v235, 0xffff0000, v67
v_pk_add_f32 v[230:231], v[6:7], v[234:235]
cv v195,v230,v231
v_lshlrev_b32 v234, 16, v68
v_and_b32 v235, 0xffff0000, v68
v_pk_add_f32 v[230:231], v[8:9], v[234:235]
cv v196,v230,v231
v_lshlrev_b32 v234, 16, v69
v_and_b32 v235, 0xffff0000, v69
v_pk_add_f32 v[230:231], v[10:11], v[234:235]
cv v197,v230,v231
)ASM" R"ASM(v_lshlrev_b32 v234, 16, v70
v_and_b32 v235, 0xffff0000, v70
v_pk_add_f32 v[230:231], v[12:13], v[234:235]
cv v198,v230,v231
v_lshlrev_b32 v234, 16, v71
v_and_b32 v235, 0xffff0000, v71
v_pk_add_f32 v[230:231], v[14:15], v[234:235]
cv v199,v230,v231
v_lshlrev_b32 v234, 16, v72
v_and_b32 v235, 0xffff0000, v72
v_pk_add_f32 v[230:231], v[16:17], v[234:235]
cv v200,v230,v231
v_lshlrev_b32 v234, 16, v73
v_and_b32 v235, 0xffff0000, v73
v_pk_add_f32 v[230:231], v[18:19], v[234:235]
cv v201,v230,v231
v_lshlrev_b32 v234, 16, v74
v_and_b32 v235, 0xffff0000, v74
v_pk_add_f32 v[230:231], v[20:21], v[234:235]
cv v202,v230,v231
v_lshlrev_b32 v234, 16, v75
v_and_b32 v235, 0xffff0000, v75
v_pk_add_f32 v[230:231], v[22:23], v[234:235]
cv v203,v230,v231
v_lshlrev_b32 v234, 16, v76
v_and_b32 v235, 0xffff0000, v76
v_pk_add_f32 v[230:231], v[24:25], v[234:235]
cv v204,v230,v231
v_lshlrev_b32 v234, 16, v77
v_and_b32 v235, 0xffff0000, v77
v_pk_add_f32 v[230:231], v[26:27], v[234:235]
cv v205,v230,v231
v_lshlrev_b32 v234, 16, v78
v_and_b32 v235, 0xffff0000, v78
v_pk_add_f32 v[230:231], v[28:29], v[234:235]
cv v206,v230,v231
v_lshlrev_b32 v234, 16, v79
v_and_b32 v235, 0xffff0000, v79
v_pk_add_f32 v[230:231], v[30:31], v[234:235]
cv v207,v230,v231
s_cmp_eq_u32 s30, 0
s_cbranch_scc1 .La5_LT_SEL
s_mov_b32 s94, 0x05040100
s_mov_b32 s95, 0x07060302
wt vmcnt(0)
ar v230,a224
ar v231,a228
v_perm_b32 v128, v231, v230, s94
v_perm_b32 v132, v231, v230, s95
ar v230,a232
ar v231,a236
v_perm_b32 v129, v231, v230, s94
v_perm_b32 v133, v231, v230, s95
ar v230,a240
ar v231,a244
v_perm_b32 v130, v231, v230, s94
v_perm_b32 v134, v231, v230, s95
ar v230,a248
ar v231,a252
v_perm_b32 v131, v231, v230, s94
v_perm_b32 v135, v231, v230, s95
ar v230,a225
ar v231,a229
v_perm_b32 v136, v231, v230, s94
v_perm_b32 v140, v231, v230, s95
ar v230,a233
ar v231,a237
v_perm_b32 v137, v231, v230, s94
v_perm_b32 v141, v231, v230, s95
ar v230,a241
ar v231,a245
v_perm_b32 v138, v231, v230, s94
v_perm_b32 v142, v231, v230, s95
ar v230,a249
ar v231,a253
v_perm_b32 v139, v231, v230, s94
v_perm_b32 v143, v231, v230, s95
ar v230,a226
ar v231,a230
v_perm_b32 v144, v231, v230, s94
v_perm_b32 v148, v231, v230, s95
ar v230,a234
ar v231,a238
v_perm_b32 v145, v231, v230, s94
v_perm_b32 v149, v231, v230, s95
ar v230,a242
ar v231,a246
v_perm_b32 v146, v231, v230, s94
v_perm_b32 v150, v231, v230, s95
ar v230,a250
ar v231,a254
v_perm_b32 v147, v231, v230, s94
v_perm_b32 v151, v231, v230, s95
ar v230,a227
ar v231,a231
v_perm_b32 v152, v231, v230, s94
v_perm_b32 v156, v231, v230, s95
ar v230,a235
ar v231,a239
v_perm_b32 v153, v231, v230, s94
)ASM" R"ASM(v_perm_b32 v157, v231, v230, s95
ar v230,a243
ar v231,a247
v_perm_b32 v154, v231, v230, s94
v_perm_b32 v158, v231, v230, s95
ar v230,a251
ar v231,a255
v_perm_b32 v155, v231, v230, s94
v_perm_b32 v159, v231, v230, s95
ar v230,a64
ar v231,a68
v_perm_b32 v160, v231, v230, s94
v_perm_b32 v164, v231, v230, s95
ar v230,a72
ar v231,a76
v_perm_b32 v161, v231, v230, s94
v_perm_b32 v165, v231, v230, s95
ar v230,a80
ar v231,a84
v_perm_b32 v162, v231, v230, s94
v_perm_b32 v166, v231, v230, s95
ar v230,a88
ar v231,a92
v_perm_b32 v163, v231, v230, s94
v_perm_b32 v167, v231, v230, s95
ar v230,a65
ar v231,a69
v_perm_b32 v168, v231, v230, s94
v_perm_b32 v172, v231, v230, s95
ar v230,a73
ar v231,a77
v_perm_b32 v169, v231, v230, s94
v_perm_b32 v173, v231, v230, s95
ar v230,a81
ar v231,a85
v_perm_b32 v170, v231, v230, s94
v_perm_b32 v174, v231, v230, s95
ar v230,a89
ar v231,a93
v_perm_b32 v171, v231, v230, s94
v_perm_b32 v175, v231, v230, s95
ar v230,a66
ar v231,a70
v_perm_b32 v176, v231, v230, s94
v_perm_b32 v180, v231, v230, s95
ar v230,a74
ar v231,a78
v_perm_b32 v177, v231, v230, s94
v_perm_b32 v181, v231, v230, s95
ar v230,a82
ar v231,a86
v_perm_b32 v178, v231, v230, s94
v_perm_b32 v182, v231, v230, s95
ar v230,a90
ar v231,a94
v_perm_b32 v179, v231, v230, s94
v_perm_b32 v183, v231, v230, s95
ar v230,a67
ar v231,a71
v_perm_b32 v184, v231, v230, s94
v_perm_b32 v188, v231, v230, s95
ar v230,a75
ar v231,a79
v_perm_b32 v185, v231, v230, s94
v_perm_b32 v189, v231, v230, s95
ar v230,a83
ar v231,a87
v_perm_b32 v186, v231, v230, s94
v_perm_b32 v190, v231, v230, s95
ar v230,a91
ar v231,a95
v_perm_b32 v187, v231, v230, s94
v_perm_b32 v191, v231, v230, s95
s_and_b32 s92, s3, 1
s_lshr_b32 s93, s3, 1
v_lshrrev_b32 v230, 6, v255
v_and_b32 v231, 48, v255
v_lshl_add_u32 v240, v230, 10, v231
s_lshl_b32 s96, s92, 14
v_add_u32 v240, s96, v240
s_lshl_b32 s96, s92, 10
v_add_u32 v241, s96, v255
s_lshl_b32 s97, s3, 10
v_add_u32 v208, s97, v255
v_bfe_u32 v230, v255, 4, 1
v_bfe_u32 v231, v255, 6, 2
v_lshrrev_b32 v232, 8, v255
v_mov_b32 v44, v230
v_mov_b32 v209, 1048576
v_lshl_add_u32 v209, v231, 7, v209
v_lshl_add_u32 v209, v232, 5, v209
s_lshr_b32 s96, s96, 1
v_add_u32 v209, s96, v209
v_bfe_u32 v233, v255, 4, 2
v_mul_u32_u24 v210, 640, v233
v_lshl_add_u32 v210, v231, 3, v210
s_mul_i32 s96, s93, 2560
s_lshl_b32 s92, s92, 5
s_add_u32 s96, s96, s92
v_add_u32 v210, s96, v210
)ASM" R"ASM(v_cmp_eq_u32 s[98:99], v231, v232
v_mul_u32_u24 v211, 5, v255
s_lshl_b32 s96, s3, 7
v_lshrrev_b32 v212, 3, v255
v_and_b32 v212, 62, v212
v_add_u32 v212, s96, v212
v_lshlrev_b32 v234, 6, v230
v_lshl_add_u32 v234, v232, 4, v234
v_add_u32 v234, s96, v234
v_mov_b32 v235, 1024
v_cmp_eq_u32 vcc, 0, v231
s_nop 1
v_cndmask_b32 v216, v235, v234, vcc
v_cmp_eq_u32 vcc, 1, v231
s_nop 1
v_cndmask_b32 v217, v235, v234, vcc
v_cmp_eq_u32 vcc, 2, v231
s_nop 1
v_cndmask_b32 v218, v235, v234, vcc
v_cmp_eq_u32 vcc, 3, v231
s_nop 1
v_cndmask_b32 v219, v235, v234, vcc
v_mov_b32 v220, 0
v_mov_b32 v221, 0
v_mov_b32 v222, 0
v_mov_b32 v223, 0
ds_write_b128 v235, v[220:223] offset:10240
s_lshl_b32 s92, s100, 13
s_add_u32 s80, s92, 32768
s_add_u32 s20, s80, 4096
; ---- top two rows of the sequence; SwiGLU slice of 32 hidden units
.La5_LT_SEL:
.La5_LW4_1:
s_cbranch_execz .La5_LW4_R0
s_and_b32 s97, s2, 7
s_lshl_b32 s97, s97, 12
s_cmp_lg_u32 s3, 0
s_cbranch_scc1 .La5_LT_S2
s_mov_b32 s96, 0
v_and_b32 v229, 255, v255
.La5_LT_SA:
s_sleep 2
ld v[224:227],v229,s[44:47],s97 offen offset:0 sc1
s_add_u32 s96, s96, 1
s_lshr_b32 s92, s96, 20
wt vmcnt(0)
v_cmp_ne_u32 vcc, 0, v224
s_cbranch_scc1 .La5_LT_S1
s_cmp_eq_u64 vcc, -1
s_cbranch_scc1 .La5_LT_S1
s_cmp_lg_u32 s96, 1
s_cbranch_scc1 .La5_LT_SA
s_lshr_b32 s92, s2, 6
s_lshl_b32 s92, s92, 7
s_and_b32 s93, s2, 63
s_add_u32 s92, s92, s93
s_lshl_b32 s92, s92, 7
s_add_u32 s92, s92, 950272
s_add_u32 s93, s92, 8192
wt vmcnt(0)
s_memtime s[16:17]
wt lgkmcnt(0)
buffer_store_dword v255, off, s[44:47], s92 sc1
wt vmcnt(0)
s_memtime s[18:19]
wt lgkmcnt(0)
buffer_store_dword v255, off, s[44:47], s93 sc1
wt vmcnt(0)
s_memtime s[24:25]
wt lgkmcnt(0)
s_add_u32 s16, s16, s24
s_lshl_b32 s18, s18, 1
s_sub_u32 s16, s16, s18
s_lshr_b32 s94, s16, 31
s_lshl_b32 s94, s94, 13
s_and_b32 s97, s2, 1
s_bfe_u32 s92, s2, 0x10001
s_lshl2_add_u32 s97, s92, s97
s_lshl_b32 s97, s97, 12
s_add_u32 s97, s97, s94
s_branch .La5_LT_SA
.La5_LT_S1:
s_cmp_lg_u32 s30, 0
s_cbranch_scc1 .La5_LT_S0
s_sleep 2
.La5_LT_S0:
ld v[224:227],v255,s[44:47],s97 offen offset:0 sc1
ld v[232:235],v255,s[44:47],s97 offen offset:1024 sc1
ld v[244:247],v255,s[44:47],s97 offen offset:2048 sc1
ld v[248:251],v255,s[44:47],s97 offen offset:3072 sc1
s_add_u32 s96, s96, 1
s_lshr_b32 s92, s96, 20
wt vmcnt(3)
v_min3_u32 v228, v224, v225, v226
v_min_u32 v228, v227, v228
wt vmcnt(2)
v_min3_u32 v228, v232, v233, v228
v_min3_u32 v228, v234, v235, v228
wt vmcnt(1)
v_min3_u32 v228, v244, v245, v228
)ASM" R"ASM(v_min3_u32 v228, v246, v247, v228
wt vmcnt(0)
v_min3_u32 v228, v248, v249, v228
v_min3_u32 v228, v250, v251, v228
v_cmp_ne_u32 vcc, 0, v228
v_max_u32 v229, v224, v232
v_max_u32 v230, v244, v248
s_cbranch_scc1 .La5_LT_S3
s_cmp_eq_u64 vcc, -1
s_nop 0
s_cbranch_scc0 .La5_LT_S1
.La5_LT_S3:
v_max_u32 v228, v229, v230
v_min_u32 v229, v229, v230
v_mov_b32 v231, v228
v_min_u32 v224, v224, v232
v_min_u32 v244, v244, v248
v_max3_u32 v229, v229, v224, v244
v_max3_u32 v230, v226, v234, v246
v_max3_u32 v229, v229, v230, v250
s_nop 1
v_max_u32_dpp v231, v231, v231 row_shr:1 row_mask:0xf bank_mask:0xf
s_nop 1
v_max_u32_dpp v231, v231, v231 row_shr:2 row_mask:0xf bank_mask:0xf
s_nop 1
v_max_u32_dpp v231, v231, v231 row_shr:4 row_mask:0xf bank_mask:0xf
s_nop 1
v_max_u32_dpp v231, v231, v231 row_shr:8 row_mask:0xf bank_mask:0xf
s_nop 1
v_max_u32_dpp v231, v231, v231 row_bcast:15 row_mask:0xa bank_mask:0xf
s_nop 1
v_max_u32_dpp v231, v231, v231 row_bcast:31 row_mask:0xc bank_mask:0xf
s_nop 0
v_readlane_b32 s82, v231, 63
s_nop 1
v_cmp_eq_u32 vcc, s82, v228
v_mov_b32 v230, s82
v_mov_b32 v224, 0
v_cndmask_b32 v228, v228, v229, vcc
s_nop 1
v_max_u32_dpp v228, v228, v228 row_shr:1 row_mask:0xf bank_mask:0xf
s_nop 1
v_max_u32_dpp v228, v228, v228 row_shr:2 row_mask:0xf bank_mask:0xf
s_nop 1
v_max_u32_dpp v228, v228, v228 row_shr:4 row_mask:0xf bank_mask:0xf
s_nop 1
v_max_u32_dpp v228, v228, v228 row_shr:8 row_mask:0xf bank_mask:0xf
s_nop 1
v_max_u32_dpp v228, v228, v228 row_bcast:15 row_mask:0xa bank_mask:0xf
s_nop 1
v_max_u32_dpp v228, v228, v228 row_bcast:31 row_mask:0xc bank_mask:0xf
s_nop 0
v_readlane_b32 s81, v228, 63
s_nop 1
v_mov_b32 v231, s81
ds_write_b64 v224, v[230:231] offset:64
.La5_LT_S2:
v_mov_b32 v224, 0
.La5_LW4_2:
s_cbranch_execz .La5_LW4_R0
wt lgkmcnt(0)
s_barrier
ds_read_b64 v[228:229], v224 offset:64
wt lgkmcnt(0)
v_readfirstlane_b32 s82, v228
v_readfirstlane_b32 s81, v229
s_nop 0
s_andn2_b32 s84, 0x7ff, s82
s_andn2_b32 s21, 0x7ff, s81
s_cmp_eq_u32 s30, 0
s_nop 0
s_nop 0
s_cbranch_scc1 .La5_LT_ST
s_lshr_b32 s92, s84, 3
s_lshr_b32 s93, s21, 3
s_cmp_eq_u32 s92, s93
s_cselect_b32 s96, 2048, 0
s_lshl_b32 s92, s92, 12
s_lshl_b32 s93, s93, 12
s_add_u32 s93, s93, s96
s_sub_u32 s93, s93, s92
v_mul_lo_u32 v44, v44, s93
s_mov_b32 s96, 0
v_add_u32 v209, v209, v44
ld v[224:227],v209,s[44:47],s92 offen offset:0 sc1
ld v[232:235],v209,s[44:47],s92 offen offset:16 sc1
ld v[244:247],v209,s[44:47],s92 offen offset:1024 sc1
ld v[248:251],v209,s[44:47],s92 offen offset:1040 sc1
s_lshl_b32 s86, s76, 5
s_add_u32 s86, s86, s90
s_lshl2_add_u32 s86, s86, s3
s_lshl_b32 s87, s86, 1
v_bfe_u32 v40, v255, 6, 1
v_add_u32 v41, s87, v40
v_cmp_eq_u32 vcc, s84, v41
v_cmp_eq_u32 s[94:95], s21, v41
v_lshrrev_b32 v42, 7, v255
v_and_b32 v43, 48, v255
v_lshl_add_u32 v42, v42, 6, v43
s_or_b64 vcc, vcc, s[94:95]
)ASM" R"ASM(v_lshl_add_u32 v42, v40, 11, v42
v_mov_b32_e64 v43, 1073741824
s_lshl_b32 s86, s86, 12
v_cndmask_b32 v42, v42, v43, vcc
.La5_LT_H0:
wt vmcnt(3)
v_max3_u32 v45, v224, v225, v226
v_max_u32 v45, v227, v45
wt vmcnt(2)
v_max3_u32 v45, v232, v233, v45
v_max3_u32 v45, v234, v235, v45
wt vmcnt(1)
v_max3_u32 v45, v244, v245, v45
v_max3_u32 v45, v246, v247, v45
wt vmcnt(0)
v_max3_u32 v45, v248, v249, v45
v_max3_u32 v45, v250, v251, v45
v_cmp_eq_u32 vcc, -1, v45
s_nop 1
s_cbranch_vccnz .La5_LT_H1
.La5_LT_H2:
s_nop 0
mm a[0:3],v[224:227],a[96:99],0
mm a[4:7],v[224:227],a[100:103],0
mm a[8:11],v[224:227],a[104:107],0
mm a[12:15],v[224:227],a[108:111],0
mm a[16:19],v[224:227],a[112:115],0
mm a[20:23],v[224:227],a[116:119],0
mm a[24:27],v[224:227],a[120:123],0
mm a[28:31],v[224:227],a[124:127],0
st v[192:195],v42,s[56:59],s86 offen offset:0 sc1
s_nop 0
mm a[0:3],v[232:235],a[128:131],a[0:3]
mm a[4:7],v[232:235],a[132:135],a[4:7]
mm a[8:11],v[232:235],a[136:139],a[8:11]
mm a[12:15],v[232:235],a[140:143],a[12:15]
mm a[16:19],v[232:235],a[144:147],a[16:19]
mm a[20:23],v[232:235],a[148:151],a[20:23]
mm a[24:27],v[232:235],a[152:155],a[24:27]
mm a[28:31],v[232:235],a[156:159],a[28:31]
st v[196:199],v42,s[56:59],s86 offen offset:512 sc1
s_nop 0
mm a[0:3],v[244:247],a[160:163],a[0:3]
mm a[4:7],v[244:247],a[164:167],a[4:7]
mm a[8:11],v[244:247],a[168:171],a[8:11]
mm a[12:15],v[244:247],a[172:175],a[12:15]
mm a[16:19],v[244:247],a[176:179],a[16:19]
mm a[20:23],v[244:247],a[180:183],a[20:23]
mm a[24:27],v[244:247],a[184:187],a[24:27]
mm a[28:31],v[244:247],a[188:191],a[28:31]
st v[200:203],v42,s[56:59],s86 offen offset:1024 sc1
s_nop 0
mm a[0:3],v[248:251],a[192:195],a[0:3]
mm a[4:7],v[248:251],a[196:199],a[4:7]
mm a[8:11],v[248:251],a[200:203],a[8:11]
mm a[12:15],v[248:251],a[204:207],a[12:15]
mm a[16:19],v[248:251],a[208:211],a[16:19]
mm a[20:23],v[248:251],a[212:215],a[20:23]
mm a[24:27],v[248:251],a[216:219],a[24:27]
mm a[28:31],v[248:251],a[220:223],a[28:31]
st v[204:207],v42,s[56:59],s86 offen offset:1536 sc1
s_mov_b64 exec, s[98:99]
ds_write_b64 v210, a[0:1] offset:5120
ds_write_b64 v210, a[4:5] offset:5200
ds_write_b64 v210, a[8:9] offset:5280
ds_write_b64 v210, a[12:13] offset:5360
ds_write_b64 v210, a[16:17] offset:5440
s_nop 3
ds_write_b64 v210, a[20:21] offset:5520
s_nop 3
ds_write_b64 v210, a[24:25] offset:5600
s_nop 3
ds_write_b64 v210, a[28:29] offset:5680
s_mov_b64 exec, -1
wt lgkmcnt(0)
s_barrier
rd v[224:227],v211 offset:5120
rd v[232:235],v211 offset:5136
rd v[244:247],v211 offset:5152
rd v[248:251],v211 offset:5168
wt lgkmcnt(2)
v_pk_add_f32 v[224:225], v[224:225], v[226:227]
v_pk_add_f32 v[232:233], v[232:233], v[234:235]
wt lgkmcnt(0)
v_pk_add_f32 v[244:245], v[244:245], v[246:247]
v_pk_add_f32 v[248:249], v[248:249], v[250:251]
v_pk_add_f32 v[224:225], v[224:225], v[232:233]
v_pk_add_f32 v[244:245], v[244:245], v[248:249]
v_pk_add_f32 v[224:225], v[224:225], v[244:245]
cv v224,v224,v225
v_mov_b32 v226, v224
s_nop 1
v_permlane32_swap_b32 v224, v226
s_nop 1
v_lshlrev_b32 v228, 16, v224
v_and_b32 v229, 0xffff0000, v224
v_lshlrev_b32 v232, 16, v226
v_and_b32 v233, 0xffff0000, v226
v_mul_f32 v230, 0xbfb8aa3b, v228
v_mul_f32 v231, 0xbfb8aa3b, v229
)ASM" R"ASM(ex v230,v230
ex v231,v231
v_add_f32 v230, 1.0, v230
v_add_f32 v231, 1.0, v231
v_rcp_f32 v230, v230
v_rcp_f32 v231, v231
s_nop 0
v_pk_mul_f32 v[228:229], v[228:229], v[230:231]
cv v228,v228,v229
v_lshlrev_b32 v230, 16, v228
v_and_b32 v231, 0xffff0000, v228
v_pk_mul_f32 v[230:231], v[230:231], v[232:233]
cv v228,v230,v231
ds_write_b16 v212, v228 offset:10240
ds_write_b16_d16_hi v212, v228 offset:10304
wt lgkmcnt(0)
rd v[224:227],v216 offset:10240
rd v[232:235],v217 offset:10240
rd v[244:247],v218 offset:10240
rd v[248:251],v219 offset:10240
wt lgkmcnt(3)
mm v[0:3],v[224:227],v[128:131],0
mm v[4:7],v[224:227],v[132:135],0
mm v[8:11],v[224:227],v[136:139],0
mm v[12:15],v[224:227],v[140:143],0
wt lgkmcnt(2)
mm v[0:3],v[232:235],v[144:147],v[0:3]
mm v[4:7],v[232:235],v[148:151],v[4:7]
mm v[8:11],v[232:235],v[152:155],v[8:11]
mm v[12:15],v[232:235],v[156:159],v[12:15]
wt lgkmcnt(1)
mm v[0:3],v[244:247],v[160:163],v[0:3]
mm v[4:7],v[244:247],v[164:167],v[4:7]
mm v[8:11],v[244:247],v[168:171],v[8:11]
mm v[12:15],v[244:247],v[172:175],v[12:15]
wt lgkmcnt(0)
mm v[0:3],v[248:251],v[176:179],v[0:3]
mm v[4:7],v[248:251],v[180:183],v[4:7]
mm v[8:11],v[248:251],v[184:187],v[8:11]
mm v[12:15],v[248:251],v[188:191],v[12:15]
s_nop 7
v_or_b32 v16, 1, v0
v_or_b32 v20, 1, v1
v_or_b32 v17, 1, v4
v_or_b32 v21, 1, v5
v_or_b32 v18, 1, v8
v_or_b32 v22, 1, v9
v_or_b32 v19, 1, v12
v_or_b32 v23, 1, v13
st v[16:19],v208,s[44:47],s80 offen sc1
st v[20:23],v208,s[44:47],s20 offen sc1
s_branch .La5_LT_END
; ---- rows that were not routed leave; final sums of the routed rows
.La5_LT_ST:
.La5_LW4_3:
s_cbranch_execz .La5_LW4_R0
s_sleep 16
s_lshl_b32 s92, s76, 5
s_add_u32 s92, s92, s90
s_lshl2_add_u32 s92, s92, s3
s_lshl_b32 s93, s92, 1
v_bfe_u32 v230, v255, 6, 1
v_add_u32 v231, s93, v230
v_cmp_ne_u32 vcc, s84, v231
v_cmp_ne_u32 s[94:95], s21, v231
v_lshrrev_b32 v232, 7, v255
v_and_b32 v233, 48, v255
v_lshl_add_u32 v232, v232, 6, v233
v_lshl_add_u32 v232, v230, 11, v232
s_lshl_b32 s92, s92, 12
s_and_b64 exec, vcc, s[94:95]
s_cbranch_scc0 .La5_LT_F0
st v[192:195],v232,s[56:59],s92 offen offset:0 sc1
st v[196:199],v232,s[56:59],s92 offen offset:512 sc1
st v[200:203],v232,s[56:59],s92 offen offset:1024 sc1
st v[204:207],v232,s[56:59],s92 offen offset:1536 sc1
.La5_LT_F0:
s_mov_b64 exec, -1
s_cmp_eq_u32 s75, 1
s_cbranch_scc1 .La5_LT_END
s_lshr_b32 s92, s2, 6
s_lshl2_add_u32 s92, s92, s74
s_and_b32 s93, s2, 7
s_lshl3_add_u32 s91, s92, s93
s_cmp_lt_u32 s91, 64
s_cselect_b32 s84, s84, s21
s_cselect_b32 s82, s82, s81
s_bfe_u32 s92, s91, 0x30003
s_and_b32 s93, s91, 3
s_lshl2_add_u32 s96, s92, s93
s_and_b32 s85, s91, 4
s_lshl_b32 s85, s85, 1
v_bfe_u32 v0, v255, 6, 2
v_bfe_u32 v1, v255, 4, 2
v_lshlrev_b32 v1, 1, v1
v_lshl_add_u32 v0, v0, 4, v1
v_add_u32 v0, s85, v0
s_lshl_b32 s85, s96, 10
v_add_u32 v1, s85, v0
s_lshl_b32 s85, s96, 6
)ASM" R"ASM(v_add_u32 v0, s85, v0
s_lshr_b32 s92, s84, 4
s_lshl_b32 s92, s92, 15
s_and_b32 s93, s84, 15
s_lshl_b32 s93, s93, 6
s_add_u32 s92, s92, s93
s_add_u32 s93, s92, 4194304
s_add_u32 s94, s92, 8388608
s_add_u32 s95, s92, 12582912
buffer_load_ushort v4, v1, s[32:35], s92 offen sc1
buffer_load_ushort v5, v1, s[32:35], s93 offen sc1
buffer_load_ushort v6, v1, s[32:35], s94 offen sc1
buffer_load_ushort v7, v1, s[32:35], s95 offen sc1
s_lshl_b32 s21, s84, 11
s_and_b32 s92, s84, 0xf8
s_lshl4_add_u32 s97, s92, s97
s_lshr_b32 s92, s84, 8
s_lshl4_add_u32 s97, s92, s97
buffer_load_ushort v8, v0, s[68:71], s21 offen
ld v[12:15],off,s[44:47],s97 sc1
v_lshrrev_b32 v2, 6, v255
v_lshlrev_b32 v2, 13, v2
v_and_b32 v3, 48, v255
v_add_u32 v2, v2, v3
s_lshl_b32 s92, s3, 17
s_lshl_b32 s93, s91, 6
s_add_u32 s92, s92, s93
s_add_u32 s94, s92, 32768
s_cmp_eq_u32 s3, 3
s_cselect_b32 s95, 0, 524288
s_cselect_b32 s96, 0, -1
s_add_u32 s95, s95, s94
s_ashr_i32 s92, s82, 31
s_orn2_b32 s92, 0x80000000, s92
s_xor_b32 s92, s92, s82
s_and_b32 s92, s92, 0xffff0000
v_mov_b32 v9, 0xbfb8aa3b
v_mul_f32 v9, s92, v9
ex v9,v9
v_lshrrev_b32 v3, 8, v255
s_lshl_b32 s92, s3, 4
v_add_f32 v9, 1.0, v9
v_lshl_add_u32 v3, v3, 2, s92
v_rcp_f32 v9, v9
v_and_b32 v10, 48, v255
v_mul_u32_u24 v10, 20, v10
v_add_u32 v3, v3, v10
cv v9,v9,v9
v_lshlrev_b32 v9, 16, v9
v_and_b32 v10, 0xf0, v255
v_mul_u32_u24 v10, 5, v10
wt vmcnt(0)
v_cmp_eq_u32 vcc, s82, v12
s_nop 1
v_cndmask_b32 v12, v15, v13, vcc
v_lshlrev_b32 v14, 16, v12
v_rcp_f32 v14, v14
v_and_b32 v12, 0xffff0000, v12
v_mul_f32 v12, v12, v14
v_mov_b32 v15, v14
v_xor_b32 v12, 0x80000000, v12
v_mov_b32 v13, v12
v_lshlrev_b32 v4, 16, v4
v_lshlrev_b32 v5, 16, v5
v_lshlrev_b32 v6, 16, v6
v_lshlrev_b32 v7, 16, v7
v_lshlrev_b32 v8, 16, v8
v_add_f32 v4, v4, v5
v_add_f32 v6, v6, v7
v_add_f32 v4, v4, v6
v_add_f32 v4, v4, v8
cv v4,v4,v4
v_lshlrev_b32 v4, 16, v4
v_fma_f32 v5, v4, v14, v12
cv v5,v5,v5
v_lshlrev_b32 v5, 16, v5
s_sleep 12
s_mov_b32 s93, 0
.La5_LT_Y:
ld v[16:19],v2,s[44:47],s94 offen sc1
ld v[20:23],v2,s[44:47],s95 offen sc1
s_add_u32 s93, s93, 1
s_lshr_b32 s92, s93, 20
wt vmcnt(0)
v_min3_u32 v24, v16, v17, v18
v_min3_u32 v25, v20, v21, v22
v_min3_u32 v24, v24, v19, v23
v_min_u32 v24, v24, v25
v_cmp_ne_u32 vcc, 0, v24
v_and_b32 v20, s96, v20
v_and_b32 v21, s96, v21
v_and_b32 v22, s96, v22
v_and_b32 v23, s96, v23
s_cbranch_scc1 .La5_LT_Y1
s_cmp_eq_u64 vcc, -1
s_nop 0
s_cbranch_scc0 .La5_LT_Y
.La5_LT_Y1:
v_pk_add_f32 v[16:17], v[16:17], v[20:21]
v_pk_add_f32 v[18:19], v[18:19], v[22:23]
)ASM" R"ASM(s_nop 0
v_add_f32_dpp v16, v16, v16 row_ror:4 row_mask:0xf bank_mask:0xf
v_add_f32_dpp v17, v17, v17 row_ror:4 row_mask:0xf bank_mask:0xf
v_add_f32_dpp v18, v18, v18 row_ror:4 row_mask:0xf bank_mask:0xf
v_add_f32_dpp v19, v19, v19 row_ror:4 row_mask:0xf bank_mask:0xf
v_add_f32_dpp v16, v16, v16 row_ror:8 row_mask:0xf bank_mask:0xf
v_add_f32_dpp v17, v17, v17 row_ror:8 row_mask:0xf bank_mask:0xf
v_add_f32_dpp v18, v18, v18 row_ror:8 row_mask:0xf bank_mask:0xf
v_add_f32_dpp v19, v19, v19 row_ror:8 row_mask:0xf bank_mask:0xf
ds_write_b32 v3, v16 offset:5120
ds_write_b32 v3, v17 offset:5200
ds_write_b32 v3, v18 offset:5280
ds_write_b32 v3, v19 offset:5360
wt lgkmcnt(0)
s_barrier
rd v[16:19],v10 offset:5120
rd v[20:23],v10 offset:5136
rd v[24:27],v10 offset:5152
rd v[28:31],v10 offset:5168
wt lgkmcnt(2)
v_pk_add_f32 v[16:17], v[16:17], v[18:19]
v_pk_add_f32 v[20:21], v[20:21], v[22:23]
wt lgkmcnt(0)
v_pk_add_f32 v[24:25], v[24:25], v[26:27]
v_pk_add_f32 v[28:29], v[28:29], v[30:31]
v_pk_add_f32 v[16:17], v[16:17], v[20:21]
v_pk_add_f32 v[24:25], v[24:25], v[28:29]
v_pk_add_f32 v[16:17], v[16:17], v[24:25]
v_add_f32 v16, v16, v17
cv v16,v16,v16
v_lshlrev_b32 v16, 16, v16
v_mul_f32 v16, v16, v9
cv v16,v16,v16
v_lshlrev_b32 v16, 16, v16
v_add_f32 v16, v16, v5
cv v16,v16,v16
v_lshlrev_b32 v16, 16, v16
v_add_f32 v16, v16, v4
cv v16,v16,v16
s_cmp_lg_u32 s3, 0
s_cbranch_scc1 .La5_LT_END
s_mov_b64 exec, 0xffff
buffer_store_short v16, v0, s[56:59], s21 offen
s_mov_b64 exec, -1
.La5_LT_END:
s_nop 0
s_nop 0
.La5_LW4_4:
s_cbranch_execz .La5_LW4_R0
s_lshl2_add_u32 s92, s2, s3
s_cmp_lg_u32 s92, 992
s_cbranch_scc1 .La5_LT_E1
buffer_store_dwordx2 v[252:253], off, s[64:67], 0 sc1
.La5_LT_E1:
s_branch .La5_LEND
; ---- fallback: the earlier 32 rows against all their keys with the true running maximum (online softmax)
.La5_LA_SLOW0:
wt lgkmcnt(0)
s_nop 7
s_nop 7
aw a160,v192
aw a161,v193
aw a162,v194
aw a163,v195
aw a164,v196
aw a165,v197
aw a166,v198
aw a167,v199
aw a168,v200
aw a169,v201
aw a170,v202
aw a171,v203
aw a172,v204
aw a173,v205
aw a174,v206
aw a175,v207
aw a176,v208
aw a177,v209
aw a178,v210
aw a179,v211
aw a180,v212
aw a181,v213
aw a182,v214
aw a183,v215
aw a184,v216
aw a185,v217
aw a186,v218
aw a187,v219
aw a188,v220
aw a189,v221
aw a190,v222
aw a191,v223
s_lshl_b32 s96, s72, 19
s_add_u32 s96, s96, 20971520
s_add_u32 s92, s26, s96
s_addc_u32 s93, s27, 0
s_and_b32 s93, s93, 0xffff
s_mov_b32 s94, 17301504
s_mov_b32 s95, 0x20000
v_mov_b32 v0, 0
)ASM" R"ASM(v_mov_b32 v1, 0
v_mov_b32 v2, 0
v_mov_b32 v3, 0
v_mov_b32 v4, 0
v_mov_b32 v5, 0
v_mov_b32 v6, 0
v_mov_b32 v7, 0
v_mov_b32 v8, 0
v_mov_b32 v9, 0
v_mov_b32 v10, 0
v_mov_b32 v11, 0
v_mov_b32 v12, 0
v_mov_b32 v13, 0
v_mov_b32 v14, 0
v_mov_b32 v15, 0
v_mov_b32 v16, 0
v_mov_b32 v17, 0
v_mov_b32 v18, 0
v_mov_b32 v19, 0
v_mov_b32 v20, 0
v_mov_b32 v21, 0
v_mov_b32 v22, 0
v_mov_b32 v23, 0
v_mov_b32 v24, 0
v_mov_b32 v25, 0
v_mov_b32 v26, 0
v_mov_b32 v27, 0
v_mov_b32 v28, 0
v_mov_b32 v29, 0
v_mov_b32 v30, 0
v_mov_b32 v31, 0
v_mov_b32 v32, 0
v_mov_b32 v33, 0
v_mov_b32 v34, 0
v_mov_b32 v35, 0
v_mov_b32 v36, 0
v_mov_b32 v37, 0
v_mov_b32 v38, 0
v_mov_b32 v39, 0
v_mov_b32 v40, 0
v_mov_b32 v41, 0
v_mov_b32 v42, 0
v_mov_b32 v43, 0
v_mov_b32 v44, 0
v_mov_b32 v45, 0
v_mov_b32 v46, 0
v_mov_b32 v47, 0
v_mov_b32 v48, 0
v_mov_b32 v49, 0
v_mov_b32 v50, 0
v_mov_b32 v51, 0
v_mov_b32 v52, 0
v_mov_b32 v53, 0
v_mov_b32 v54, 0
v_mov_b32 v55, 0
v_mov_b32 v56, 0
v_mov_b32 v57, 0
v_mov_b32 v58, 0
v_mov_b32 v59, 0
v_mov_b32 v60, 0
v_mov_b32 v61, 0
v_mov_b32 v62, 0
v_mov_b32 v63, 0
v_mov_b32 v216, 0xff61b1e6
v_mov_b32 v217, 0xff61b1e6
v_mov_b32 v218, 0
v_mov_b32 v219, 0
s_mov_b32 s7, 0
.La5_LA_SLOW0_L:
s_lshl_b32 s96, s7, 13
s_add_u32 s97, s96, 4096
ld a[128:131],v255,s[92:95],s96 offen offset:0 sc1
ld a[132:135],v255,s[92:95],s96 offen offset:1024 sc1
ld a[136:139],v255,s[92:95],s96 offen offset:2048 sc1
ld a[140:143],v255,s[92:95],s96 offen offset:3072 sc1
ld a[144:147],v255,s[92:95],s97 offen offset:0 sc1
ld a[148:151],v255,s[92:95],s97 offen offset:1024 sc1
ld a[152:155],v255,s[92:95],s97 offen offset:2048 sc1
ld a[156:159],v255,s[92:95],s97 offen offset:3072 sc1
s_lshl_b32 s96, s76, 3
s_sub_u32 s96, s96, s7
s_add_u32 s96, s96, s3
s_lshl_b32 s96, s96, 5
v_bfe_u32 v220, v255, 4, 4
v_lshrrev_b32 v221, 8, v255
v_lshlrev_b32 v221, 2, v221
v_sub_u32 v220, v220, v221
v_add_u32 v220, s96, v220
v_mov_b32 v222, 0xceabfb8f
wt vmcnt(0)
mm v[192:195],a[128:131],v[128:131],0
mm v[192:195],a[132:135],v[132:135],v[192:195]
mm v[192:195],a[136:139],v[136:139],v[192:195]
mm v[192:195],a[140:143],v[140:143],v[192:195]
mm v[196:199],a[144:147],v[128:131],0
mm v[196:199],a[148:151],v[132:135],v[196:199]
mm v[196:199],a[152:155],v[136:139],v[196:199]
mm v[196:199],a[156:159],v[140:143],v[196:199]
mm v[200:203],a[128:131],v[144:147],0
mm v[200:203],a[132:135],v[148:151],v[200:203]
)ASM" R"ASM(mm v[200:203],a[136:139],v[152:155],v[200:203]
mm v[200:203],a[140:143],v[156:159],v[200:203]
mm v[204:207],a[144:147],v[144:147],0
mm v[204:207],a[148:151],v[148:151],v[204:207]
mm v[204:207],a[152:155],v[152:155],v[204:207]
mm v[204:207],a[156:159],v[156:159],v[204:207]
s_nop 7
s_nop 7
s_lshl_b32 s96, s7, 13
s_add_u32 s96, s96, 16777216
s_add_u32 s97, s96, 4096
ld a[128:131],v255,s[92:95],s96 offen offset:0 sc1
ld a[132:135],v255,s[92:95],s96 offen offset:1024 sc1
ld a[136:139],v255,s[92:95],s96 offen offset:2048 sc1
ld a[140:143],v255,s[92:95],s96 offen offset:3072 sc1
ld a[144:147],v255,s[92:95],s97 offen offset:0 sc1
ld a[148:151],v255,s[92:95],s97 offen offset:1024 sc1
ld a[152:155],v255,s[92:95],s97 offen offset:2048 sc1
ld a[156:159],v255,s[92:95],s97 offen offset:3072 sc1
v_cmp_gt_i32 vcc, 0, v220
s_nop 1
v_cndmask_b32 v192, v192, v222, vcc
v_cmp_gt_i32 vcc, 1, v220
s_nop 1
v_cndmask_b32 v193, v193, v222, vcc
v_cmp_gt_i32 vcc, 2, v220
s_nop 1
v_cndmask_b32 v194, v194, v222, vcc
v_cmp_gt_i32 vcc, 3, v220
s_nop 1
v_cndmask_b32 v195, v195, v222, vcc
v_cmp_gt_i32 vcc, 16, v220
s_nop 1
v_cndmask_b32 v196, v196, v222, vcc
v_cmp_gt_i32 vcc, 17, v220
s_nop 1
v_cndmask_b32 v197, v197, v222, vcc
v_cmp_gt_i32 vcc, 18, v220
s_nop 1
v_cndmask_b32 v198, v198, v222, vcc
v_cmp_gt_i32 vcc, 19, v220
s_nop 1
v_cndmask_b32 v199, v199, v222, vcc
v_cmp_gt_i32 vcc, -16, v220
s_nop 1
v_cndmask_b32 v200, v200, v222, vcc
v_cmp_gt_i32 vcc, -15, v220
s_nop 1
v_cndmask_b32 v201, v201, v222, vcc
v_cmp_gt_i32 vcc, -14, v220
s_nop 1
v_cndmask_b32 v202, v202, v222, vcc
v_cmp_gt_i32 vcc, -13, v220
s_nop 1
v_cndmask_b32 v203, v203, v222, vcc
v_cmp_gt_i32 vcc, 0, v220
s_nop 1
v_cndmask_b32 v204, v204, v222, vcc
v_cmp_gt_i32 vcc, 1, v220
s_nop 1
v_cndmask_b32 v205, v205, v222, vcc
v_cmp_gt_i32 vcc, 2, v220
s_nop 1
v_cndmask_b32 v206, v206, v222, vcc
v_cmp_gt_i32 vcc, 3, v220
s_nop 1
v_cndmask_b32 v207, v207, v222, vcc
v_max3_f32 v222, v192, v193, v216
v_max3_f32 v222, v194, v195, v222
v_max3_f32 v222, v196, v197, v222
v_max3_f32 v222, v198, v199, v222
v_mov_b32 v221, v222
s_nop 1
v_permlane16_swap_b32 v221, v222
s_nop 1
v_max_f32 v222, v222, v221
v_mov_b32 v221, v222
s_nop 1
v_permlane32_swap_b32 v221, v222
s_nop 1
v_max_f32 v222, v222, v221
v_sub_f32 v223, v216, v222
v_mov_b32 v216, v222
ex v223,v223
v_sub_f32 v192, v192, v222
v_sub_f32 v193, v193, v222
v_sub_f32 v194, v194, v222
v_sub_f32 v195, v195, v222
v_sub_f32 v196, v196, v222
v_sub_f32 v197, v197, v222
v_sub_f32 v198, v198, v222
v_sub_f32 v199, v199, v222
ex v192,v192
ex v193,v193
ex v194,v194
ex v195,v195
ex v196,v196
ex v197,v197
ex v198,v198
ex v199,v199
)ASM" R"ASM(v_mul_f32 v218, v218, v223
v_add_f32 v218, v218, v192
v_add_f32 v218, v218, v193
v_add_f32 v218, v218, v194
v_add_f32 v218, v218, v195
v_add_f32 v218, v218, v196
v_add_f32 v218, v218, v197
v_add_f32 v218, v218, v198
v_add_f32 v218, v218, v199
cv v208,v192,v193
cv v209,v194,v195
cv v210,v196,v197
cv v211,v198,v199
v_mul_f32 v0, v0, v223
v_mul_f32 v1, v1, v223
v_mul_f32 v2, v2, v223
v_mul_f32 v3, v3, v223
v_mul_f32 v4, v4, v223
v_mul_f32 v5, v5, v223
v_mul_f32 v6, v6, v223
v_mul_f32 v7, v7, v223
v_mul_f32 v8, v8, v223
v_mul_f32 v9, v9, v223
v_mul_f32 v10, v10, v223
v_mul_f32 v11, v11, v223
v_mul_f32 v12, v12, v223
v_mul_f32 v13, v13, v223
v_mul_f32 v14, v14, v223
v_mul_f32 v15, v15, v223
v_mul_f32 v16, v16, v223
v_mul_f32 v17, v17, v223
v_mul_f32 v18, v18, v223
v_mul_f32 v19, v19, v223
v_mul_f32 v20, v20, v223
v_mul_f32 v21, v21, v223
v_mul_f32 v22, v22, v223
v_mul_f32 v23, v23, v223
v_mul_f32 v24, v24, v223
v_mul_f32 v25, v25, v223
v_mul_f32 v26, v26, v223
v_mul_f32 v27, v27, v223
v_mul_f32 v28, v28, v223
v_mul_f32 v29, v29, v223
v_mul_f32 v30, v30, v223
v_mul_f32 v31, v31, v223
v_max3_f32 v222, v200, v201, v217
v_max3_f32 v222, v202, v203, v222
v_max3_f32 v222, v204, v205, v222
v_max3_f32 v222, v206, v207, v222
v_mov_b32 v221, v222
s_nop 1
v_permlane16_swap_b32 v221, v222
s_nop 1
v_max_f32 v222, v222, v221
v_mov_b32 v221, v222
s_nop 1
v_permlane32_swap_b32 v221, v222
s_nop 1
v_max_f32 v222, v222, v221
v_sub_f32 v223, v217, v222
v_mov_b32 v217, v222
ex v223,v223
v_sub_f32 v200, v200, v222
v_sub_f32 v201, v201, v222
v_sub_f32 v202, v202, v222
v_sub_f32 v203, v203, v222
v_sub_f32 v204, v204, v222
v_sub_f32 v205, v205, v222
v_sub_f32 v206, v206, v222
v_sub_f32 v207, v207, v222
ex v200,v200
ex v201,v201
ex v202,v202
ex v203,v203
ex v204,v204
ex v205,v205
ex v206,v206
ex v207,v207
v_mul_f32 v219, v219, v223
v_add_f32 v219, v219, v200
v_add_f32 v219, v219, v201
v_add_f32 v219, v219, v202
v_add_f32 v219, v219, v203
v_add_f32 v219, v219, v204
v_add_f32 v219, v219, v205
v_add_f32 v219, v219, v206
v_add_f32 v219, v219, v207
cv v212,v200,v201
cv v213,v202,v203
cv v214,v204,v205
cv v215,v206,v207
v_mul_f32 v32, v32, v223
v_mul_f32 v33, v33, v223
v_mul_f32 v34, v34, v223
v_mul_f32 v35, v35, v223
v_mul_f32 v36, v36, v223
v_mul_f32 v37, v37, v223
v_mul_f32 v38, v38, v223
v_mul_f32 v39, v39, v223
v_mul_f32 v40, v40, v223
)ASM" R"ASM(v_mul_f32 v41, v41, v223
v_mul_f32 v42, v42, v223
v_mul_f32 v43, v43, v223
v_mul_f32 v44, v44, v223
v_mul_f32 v45, v45, v223
v_mul_f32 v46, v46, v223
v_mul_f32 v47, v47, v223
v_mul_f32 v48, v48, v223
v_mul_f32 v49, v49, v223
v_mul_f32 v50, v50, v223
v_mul_f32 v51, v51, v223
v_mul_f32 v52, v52, v223
v_mul_f32 v53, v53, v223
v_mul_f32 v54, v54, v223
v_mul_f32 v55, v55, v223
v_mul_f32 v56, v56, v223
v_mul_f32 v57, v57, v223
v_mul_f32 v58, v58, v223
v_mul_f32 v59, v59, v223
v_mul_f32 v60, v60, v223
v_mul_f32 v61, v61, v223
v_mul_f32_e64 v62, v62, v223
v_mul_f32_e64 v63, v63, v223
wt vmcnt(0)
mm v[0:3],a[128:131],v[208:211],v[0:3]
mm v[32:35],a[128:131],v[212:215],v[32:35]
mm v[4:7],a[132:135],v[208:211],v[4:7]
mm v[36:39],a[132:135],v[212:215],v[36:39]
mm v[8:11],a[136:139],v[208:211],v[8:11]
mm v[40:43],a[136:139],v[212:215],v[40:43]
mm v[12:15],a[140:143],v[208:211],v[12:15]
mm v[44:47],a[140:143],v[212:215],v[44:47]
mm v[16:19],a[144:147],v[208:211],v[16:19]
mm v[48:51],a[144:147],v[212:215],v[48:51]
mm v[20:23],a[148:151],v[208:211],v[20:23]
mm v[52:55],a[148:151],v[212:215],v[52:55]
mm v[24:27],a[152:155],v[208:211],v[24:27]
mm v[56:59],a[152:155],v[212:215],v[56:59]
mm v[28:31],a[156:159],v[208:211],v[28:31]
mm v[60:63],a[156:159],v[212:215],v[60:63]
s_nop 7
s_nop 7
s_add_u32 s7, s7, 1
s_lshr_b32 s96, s7, 3
s_cmp_le_u32 s96, s76
s_cbranch_scc1 .La5_LA_SLOW0_L
v_mov_b32 v221, v218
s_nop 1
v_permlane16_swap_b32 v221, v218
s_nop 1
v_add_f32 v218, v218, v221
v_mov_b32 v221, v218
s_nop 1
v_permlane32_swap_b32 v221, v218
s_nop 1
v_add_f32 v218, v218, v221
v_mov_b32 v221, v219
s_nop 1
v_permlane16_swap_b32 v221, v219
s_nop 1
v_add_f32 v219, v219, v221
v_mov_b32 v221, v219
s_nop 1
v_permlane32_swap_b32 v221, v219
s_nop 1
v_add_f32 v219, v219, v221
v_rcp_f32 v242, v218
v_rcp_f32 v243, v219
ar v192,a160
ar v193,a161
ar v194,a162
ar v195,a163
ar v196,a164
ar v197,a165
ar v198,a166
ar v199,a167
ar v200,a168
ar v201,a169
ar v202,a170
ar v203,a171
ar v204,a172
ar v205,a173
ar v206,a174
ar v207,a175
ar v208,a176
ar v209,a177
ar v210,a178
ar v211,a179
ar v212,a180
ar v213,a181
ar v214,a182
ar v215,a183
ar v216,a184
ar v217,a185
ar v218,a186
ar v219,a187
ar v220,a188
ar v221,a189
ar v222,a190
ar v223,a191
)ASM" R"ASM(s_mov_b32 s95, 4096
s_mov_b32 s96, 8192
s_mov_b32 s97, 12288
s_mov_b64 vcc, 0
s_nop 1
s_setpc_b64 s[24:25]
; ---- fallback: the later 32 rows
.La5_LA_SLOW1:
wt lgkmcnt(0)
s_nop 7
s_nop 7
aw a160,v192
aw a161,v193
aw a162,v194
aw a163,v195
aw a164,v196
aw a165,v197
aw a166,v198
aw a167,v199
aw a168,v200
aw a169,v201
aw a170,v202
aw a171,v203
aw a172,v204
aw a173,v205
aw a174,v206
aw a175,v207
aw a176,v208
aw a177,v209
aw a178,v210
aw a179,v211
aw a180,v212
aw a181,v213
aw a182,v214
aw a183,v215
aw a184,v216
aw a185,v217
aw a186,v218
aw a187,v219
aw a188,v220
aw a189,v221
aw a190,v222
aw a191,v223
s_lshl_b32 s96, s72, 19
s_add_u32 s96, s96, 20971520
s_add_u32 s92, s26, s96
s_addc_u32 s93, s27, 0
s_and_b32 s93, s93, 0xffff
s_mov_b32 s94, 17301504
s_mov_b32 s95, 0x20000
v_mov_b32 v64, 0
v_mov_b32 v65, 0
v_mov_b32 v66, 0
v_mov_b32 v67, 0
v_mov_b32 v68, 0
v_mov_b32 v69, 0
v_mov_b32 v70, 0
v_mov_b32 v71, 0
v_mov_b32 v72, 0
v_mov_b32 v73, 0
v_mov_b32 v74, 0
v_mov_b32 v75, 0
v_mov_b32 v76, 0
v_mov_b32 v77, 0
v_mov_b32 v78, 0
v_mov_b32 v79, 0
v_mov_b32 v80, 0
v_mov_b32 v81, 0
v_mov_b32 v82, 0
v_mov_b32 v83, 0
v_mov_b32 v84, 0
v_mov_b32 v85, 0
v_mov_b32 v86, 0
v_mov_b32 v87, 0
v_mov_b32 v88, 0
v_mov_b32 v89, 0
v_mov_b32 v90, 0
v_mov_b32 v91, 0
v_mov_b32 v92, 0
v_mov_b32 v93, 0
v_mov_b32 v94, 0
v_mov_b32 v95, 0
v_mov_b32 v96, 0
v_mov_b32 v97, 0
v_mov_b32 v98, 0
v_mov_b32 v99, 0
v_mov_b32 v100, 0
v_mov_b32 v101, 0
v_mov_b32 v102, 0
v_mov_b32 v103, 0
v_mov_b32 v104, 0
v_mov_b32 v105, 0
v_mov_b32 v106, 0
v_mov_b32 v107, 0
v_mov_b32 v108, 0
v_mov_b32 v109, 0
v_mov_b32 v110, 0
v_mov_b32 v111, 0
v_mov_b32 v112, 0
v_mov_b32 v113, 0
)ASM" R"ASM(v_mov_b32 v114, 0
v_mov_b32 v115, 0
v_mov_b32 v116, 0
v_mov_b32 v117, 0
v_mov_b32 v118, 0
v_mov_b32 v119, 0
v_mov_b32 v120, 0
v_mov_b32 v121, 0
v_mov_b32 v122, 0
v_mov_b32 v123, 0
v_mov_b32 v124, 0
v_mov_b32 v125, 0
v_mov_b32 v126, 0
v_mov_b32 v127, 0
v_mov_b32 v216, 0xff61b1e6
v_mov_b32 v217, 0xff61b1e6
v_mov_b32 v218, 0
v_mov_b32 v219, 0
s_mov_b32 s7, 0
.La5_LA_SLOW1_L:
s_lshl_b32 s96, s7, 13
s_add_u32 s97, s96, 4096
ld a[128:131],v255,s[92:95],s96 offen offset:0 sc1
ld a[132:135],v255,s[92:95],s96 offen offset:1024 sc1
ld a[136:139],v255,s[92:95],s96 offen offset:2048 sc1
ld a[140:143],v255,s[92:95],s96 offen offset:3072 sc1
ld a[144:147],v255,s[92:95],s97 offen offset:0 sc1
ld a[148:151],v255,s[92:95],s97 offen offset:1024 sc1
ld a[152:155],v255,s[92:95],s97 offen offset:2048 sc1
ld a[156:159],v255,s[92:95],s97 offen offset:3072 sc1
s_lshl_b32 s96, s76, 3
s_sub_u32 s96, s96, s7
s_sub_u32 s96, s96, s3
s_add_u32 s96, s96, 7
s_lshl_b32 s96, s96, 5
v_bfe_u32 v220, v255, 4, 4
v_lshrrev_b32 v221, 8, v255
v_lshlrev_b32 v221, 2, v221
v_sub_u32 v220, v220, v221
v_add_u32 v220, s96, v220
v_mov_b32 v222, 0xceabfb8f
wt vmcnt(0)
mm v[192:195],a[128:131],v[160:163],0
mm v[192:195],a[132:135],v[164:167],v[192:195]
mm v[192:195],a[136:139],v[168:171],v[192:195]
mm v[192:195],a[140:143],v[172:175],v[192:195]
mm v[196:199],a[144:147],v[160:163],0
mm v[196:199],a[148:151],v[164:167],v[196:199]
mm v[196:199],a[152:155],v[168:171],v[196:199]
mm v[196:199],a[156:159],v[172:175],v[196:199]
mm v[200:203],a[128:131],v[176:179],0
mm v[200:203],a[132:135],v[180:183],v[200:203]
mm v[200:203],a[136:139],v[184:187],v[200:203]
mm v[200:203],a[140:143],v[188:191],v[200:203]
mm v[204:207],a[144:147],v[176:179],0
mm v[204:207],a[148:151],v[180:183],v[204:207]
mm v[204:207],a[152:155],v[184:187],v[204:207]
mm v[204:207],a[156:159],v[188:191],v[204:207]
s_nop 7
s_nop 7
s_lshl_b32 s96, s7, 13
s_add_u32 s96, s96, 16777216
s_add_u32 s97, s96, 4096
ld a[128:131],v255,s[92:95],s96 offen offset:0 sc1
ld a[132:135],v255,s[92:95],s96 offen offset:1024 sc1
ld a[136:139],v255,s[92:95],s96 offen offset:2048 sc1
ld a[140:143],v255,s[92:95],s96 offen offset:3072 sc1
ld a[144:147],v255,s[92:95],s97 offen offset:0 sc1
ld a[148:151],v255,s[92:95],s97 offen offset:1024 sc1
ld a[152:155],v255,s[92:95],s97 offen offset:2048 sc1
ld a[156:159],v255,s[92:95],s97 offen offset:3072 sc1
v_cmp_gt_i32 vcc, 0, v220
s_nop 1
v_cndmask_b32 v192, v192, v222, vcc
v_cmp_gt_i32 vcc, 1, v220
s_nop 1
v_cndmask_b32 v193, v193, v222, vcc
v_cmp_gt_i32 vcc, 2, v220
s_nop 1
v_cndmask_b32 v194, v194, v222, vcc
v_cmp_gt_i32 vcc, 3, v220
s_nop 1
v_cndmask_b32 v195, v195, v222, vcc
v_cmp_gt_i32 vcc, 16, v220
s_nop 1
v_cndmask_b32 v196, v196, v222, vcc
v_cmp_gt_i32 vcc, 17, v220
s_nop 1
v_cndmask_b32 v197, v197, v222, vcc
v_cmp_gt_i32 vcc, 18, v220
s_nop 1
v_cndmask_b32 v198, v198, v222, vcc
v_cmp_gt_i32 vcc, 19, v220
s_nop 1
v_cndmask_b32 v199, v199, v222, vcc
v_cmp_gt_i32 vcc, -16, v220
s_nop 1
v_cndmask_b32 v200, v200, v222, vcc
v_cmp_gt_i32 vcc, -15, v220
s_nop 1
)ASM" R"ASM(v_cndmask_b32 v201, v201, v222, vcc
v_cmp_gt_i32 vcc, -14, v220
s_nop 1
v_cndmask_b32 v202, v202, v222, vcc
v_cmp_gt_i32 vcc, -13, v220
s_nop 1
v_cndmask_b32 v203, v203, v222, vcc
v_cmp_gt_i32 vcc, 0, v220
s_nop 1
v_cndmask_b32 v204, v204, v222, vcc
v_cmp_gt_i32 vcc, 1, v220
s_nop 1
v_cndmask_b32 v205, v205, v222, vcc
v_cmp_gt_i32 vcc, 2, v220
s_nop 1
v_cndmask_b32 v206, v206, v222, vcc
v_cmp_gt_i32 vcc, 3, v220
s_nop 1
v_cndmask_b32 v207, v207, v222, vcc
v_max3_f32 v222, v192, v193, v216
v_max3_f32 v222, v194, v195, v222
v_max3_f32 v222, v196, v197, v222
v_max3_f32 v222, v198, v199, v222
v_mov_b32 v221, v222
s_nop 1
v_permlane16_swap_b32 v221, v222
s_nop 1
v_max_f32 v222, v222, v221
v_mov_b32 v221, v222
s_nop 1
v_permlane32_swap_b32 v221, v222
s_nop 1
v_max_f32 v222, v222, v221
v_sub_f32 v223, v216, v222
v_mov_b32 v216, v222
ex v223,v223
v_sub_f32 v192, v192, v222
v_sub_f32 v193, v193, v222
v_sub_f32 v194, v194, v222
v_sub_f32 v195, v195, v222
v_sub_f32 v196, v196, v222
v_sub_f32 v197, v197, v222
v_sub_f32 v198, v198, v222
v_sub_f32 v199, v199, v222
ex v192,v192
ex v193,v193
ex v194,v194
ex v195,v195
ex v196,v196
ex v197,v197
ex v198,v198
ex v199,v199
v_mul_f32 v218, v218, v223
v_add_f32 v218, v218, v192
v_add_f32 v218, v218, v193
v_add_f32 v218, v218, v194
v_add_f32 v218, v218, v195
v_add_f32 v218, v218, v196
v_add_f32 v218, v218, v197
v_add_f32 v218, v218, v198
v_add_f32 v218, v218, v199
cv v208,v192,v193
cv v209,v194,v195
cv v210,v196,v197
cv v211,v198,v199
v_mul_f32 v64, v64, v223
v_mul_f32 v65, v65, v223
v_mul_f32 v66, v66, v223
v_mul_f32 v67, v67, v223
v_mul_f32 v68, v68, v223
v_mul_f32 v69, v69, v223
v_mul_f32 v70, v70, v223
v_mul_f32 v71, v71, v223
v_mul_f32 v72, v72, v223
v_mul_f32 v73, v73, v223
v_mul_f32 v74, v74, v223
v_mul_f32 v75, v75, v223
v_mul_f32 v76, v76, v223
v_mul_f32 v77, v77, v223
v_mul_f32 v78, v78, v223
v_mul_f32 v79, v79, v223
v_mul_f32 v80, v80, v223
v_mul_f32 v81, v81, v223
v_mul_f32 v82, v82, v223
v_mul_f32 v83, v83, v223
v_mul_f32 v84, v84, v223
v_mul_f32 v85, v85, v223
v_mul_f32 v86, v86, v223
v_mul_f32 v87, v87, v223
v_mul_f32 v88, v88, v223
v_mul_f32 v89, v89, v223
v_mul_f32 v90, v90, v223
v_mul_f32 v91, v91, v223
v_mul_f32 v92, v92, v223
v_mul_f32 v93, v93, v223
v_mul_f32 v94, v94, v223
v_mul_f32 v95, v95, v223
v_max3_f32 v222, v200, v201, v217
v_max3_f32 v222, v202, v203, v222
v_max3_f32 v222, v204, v205, v222
)ASM" R"ASM(v_max3_f32 v222, v206, v207, v222
v_mov_b32 v221, v222
s_nop 1
v_permlane16_swap_b32 v221, v222
s_nop 1
v_max_f32 v222, v222, v221
v_mov_b32 v221, v222
s_nop 1
v_permlane32_swap_b32 v221, v222
s_nop 1
v_max_f32 v222, v222, v221
v_sub_f32 v223, v217, v222
v_mov_b32 v217, v222
ex v223,v223
v_sub_f32 v200, v200, v222
v_sub_f32 v201, v201, v222
v_sub_f32 v202, v202, v222
v_sub_f32 v203, v203, v222
v_sub_f32 v204, v204, v222
v_sub_f32 v205, v205, v222
v_sub_f32 v206, v206, v222
v_sub_f32 v207, v207, v222
ex v200,v200
ex v201,v201
ex v202,v202
ex v203,v203
ex v204,v204
ex v205,v205
ex v206,v206
ex v207,v207
v_mul_f32 v219, v219, v223
v_add_f32 v219, v219, v200
v_add_f32 v219, v219, v201
v_add_f32 v219, v219, v202
v_add_f32 v219, v219, v203
v_add_f32 v219, v219, v204
v_add_f32 v219, v219, v205
v_add_f32 v219, v219, v206
v_add_f32 v219, v219, v207
cv v212,v200,v201
cv v213,v202,v203
cv v214,v204,v205
cv v215,v206,v207
v_mul_f32 v96, v96, v223
v_mul_f32 v97, v97, v223
v_mul_f32 v98, v98, v223
v_mul_f32 v99, v99, v223
v_mul_f32 v100, v100, v223
v_mul_f32 v101, v101, v223
v_mul_f32 v102, v102, v223
v_mul_f32 v103, v103, v223
v_mul_f32 v104, v104, v223
v_mul_f32 v105, v105, v223
v_mul_f32 v106, v106, v223
v_mul_f32 v107, v107, v223
v_mul_f32 v108, v108, v223
v_mul_f32 v109, v109, v223
v_mul_f32 v110, v110, v223
v_mul_f32 v111, v111, v223
v_mul_f32 v112, v112, v223
v_mul_f32 v113, v113, v223
v_mul_f32 v114, v114, v223
v_mul_f32 v115, v115, v223
v_mul_f32 v116, v116, v223
v_mul_f32 v117, v117, v223
v_mul_f32 v118, v118, v223
v_mul_f32 v119, v119, v223
v_mul_f32 v120, v120, v223
v_mul_f32 v121, v121, v223
v_mul_f32 v122, v122, v223
v_mul_f32 v123, v123, v223
v_mul_f32 v124, v124, v223
v_mul_f32 v125, v125, v223
v_mul_f32 v126, v126, v223
v_mul_f32_e64 v127, v127, v223
wt vmcnt(0)
mm v[64:67],a[128:131],v[208:211],v[64:67]
mm v[96:99],a[128:131],v[212:215],v[96:99]
mm v[68:71],a[132:135],v[208:211],v[68:71]
mm v[100:103],a[132:135],v[212:215],v[100:103]
mm v[72:75],a[136:139],v[208:211],v[72:75]
mm v[104:107],a[136:139],v[212:215],v[104:107]
mm v[76:79],a[140:143],v[208:211],v[76:79]
mm v[108:111],a[140:143],v[212:215],v[108:111]
mm v[80:83],a[144:147],v[208:211],v[80:83]
mm v[112:115],a[144:147],v[212:215],v[112:115]
mm v[84:87],a[148:151],v[208:211],v[84:87]
mm v[116:119],a[148:151],v[212:215],v[116:119]
mm v[88:91],a[152:155],v[208:211],v[88:91]
mm v[120:123],a[152:155],v[212:215],v[120:123]
mm v[92:95],a[156:159],v[208:211],v[92:95]
mm v[124:127],a[156:159],v[212:215],v[124:127]
s_nop 7
s_nop 7
s_add_u32 s7, s7, 1
s_lshr_b32 s96, s7, 3
s_cmp_le_u32 s96, s76
s_cbranch_scc1 .La5_LA_SLOW1_L
v_mov_b32 v221, v218
s_nop 1
)ASM" R"ASM(v_permlane16_swap_b32 v221, v218
s_nop 1
v_add_f32 v218, v218, v221
v_mov_b32 v221, v218
s_nop 1
v_permlane32_swap_b32 v221, v218
s_nop 1
v_add_f32 v218, v218, v221
v_mov_b32 v221, v219
s_nop 1
v_permlane16_swap_b32 v221, v219
s_nop 1
v_add_f32 v219, v219, v221
v_mov_b32 v221, v219
s_nop 1
v_permlane32_swap_b32 v221, v219
s_nop 1
v_add_f32 v219, v219, v221
v_rcp_f32 v242, v218
v_rcp_f32 v243, v219
ar v192,a160
ar v193,a161
ar v194,a162
ar v195,a163
ar v196,a164
ar v197,a165
ar v198,a166
ar v199,a167
ar v200,a168
ar v201,a169
ar v202,a170
ar v203,a171
ar v204,a172
ar v205,a173
ar v206,a174
ar v207,a175
ar v208,a176
ar v209,a177
ar v210,a178
ar v211,a179
ar v212,a180
ar v213,a181
ar v214,a182
ar v215,a183
ar v216,a184
ar v217,a185
ar v218,a186
ar v219,a187
ar v220,a188
ar v221,a189
ar v222,a190
ar v223,a191
s_mov_b32 s95, 4096
s_mov_b32 s96, 8192
s_mov_b32 s97, 12288
s_mov_b64 vcc, 0
s_nop 1
s_setpc_b64 s[24:25]
.La5_LA_SC1:
s_call_b64 s[24:25], .La5_LA_SLOW0
s_branch .La5_LA_SR1
.La5_LA_SC2:
s_call_b64 s[24:25], .La5_LA_SLOW1
s_branch .La5_LA_SR2
.La5_LA_PS0:
s_mov_b32 s84, 3
s_mov_b32 s70, 17408
wt vmcnt(2)
ld a[220:223],v255,s[68:71],s94 offen sc1
ld a[0:3],v255,s[68:71],0 offen offset:0 sc1
ld a[4:7],v255,s[68:71],0 offen offset:1024 sc1
ld a[8:11],v255,s[68:71],0 offen offset:2048 sc1
ld a[12:15],v255,s[68:71],0 offen offset:3072 sc1
ld a[16:19],v255,s[68:71],s95 offen offset:0 sc1
ld a[20:23],v255,s[68:71],s95 offen offset:1024 sc1
ld a[24:27],v255,s[68:71],s95 offen offset:2048 sc1
s_branch .La5_LA_PS1
.La5_LA_PS2:
s_mov_b32 s84, 3
s_mov_b32 s70, 17408
wt vmcnt(0)
ld a[220:223],v255,s[68:71],s94 offen sc1
ld a[0:3],v255,s[68:71],0 offen offset:0 sc1
ld a[4:7],v255,s[68:71],0 offen offset:1024 sc1
ld a[8:11],v255,s[68:71],0 offen offset:2048 sc1
ld a[12:15],v255,s[68:71],0 offen offset:3072 sc1
ld a[16:19],v255,s[68:71],s95 offen offset:0 sc1
ld a[20:23],v255,s[68:71],s95 offen offset:1024 sc1
ld a[24:27],v255,s[68:71],s95 offen offset:2048 sc1
ld a[28:31],v255,s[68:71],s95 offen offset:3072 sc1
ld a[32:35],v255,s[68:71],s96 offen offset:0 sc1
ld a[36:39],v255,s[68:71],s96 offen offset:1024 sc1
ld a[40:43],v255,s[68:71],s96 offen offset:2048 sc1
ld a[44:47],v255,s[68:71],s96 offen offset:3072 sc1
ld a[48:51],v255,s[68:71],s97 offen offset:0 sc1
ld a[52:55],v255,s[68:71],s97 offen offset:1024 sc1
ld a[56:59],v255,s[68:71],s97 offen offset:2048 sc1
ld a[60:63],v255,s[68:71],s97 offen offset:3072 sc1
s_branch .La5_LA_PS3
.La5_LA_PSPIN:
)ASM" R"ASM(s_mov_b32 s93, 0
.La5_LA_PSPIN_L:
s_add_u32 s93, s93, 1
s_cmp_gt_u32 s93, 2097152
s_cbranch_scc1 .La5_LA_PSOK
s_sleep 8
buffer_load_dword v250, v251, s[64:67], s20 offen sc1
wt vmcnt(0)
v_cmp_eq_u32 vcc, v250, v249
s_nop 1
s_and_b32 s92, vcc_lo, 3
s_cmp_eq_u32 s92, 3
s_cbranch_scc0 .La5_LA_PSPIN_L
s_branch .La5_LA_PSOK
.La5_LA_PSOK:
s_mov_b32 s70, 17408
ld a[220:223],v255,s[68:71],s94 offen sc1
ld a[0:3],v255,s[68:71],0 offen offset:0 sc1
ld a[4:7],v255,s[68:71],0 offen offset:1024 sc1
ld a[8:11],v255,s[68:71],0 offen offset:2048 sc1
ld a[12:15],v255,s[68:71],0 offen offset:3072 sc1
ld a[16:19],v255,s[68:71],s95 offen offset:0 sc1
ld a[20:23],v255,s[68:71],s95 offen offset:1024 sc1
ld a[24:27],v255,s[68:71],s95 offen offset:2048 sc1
ld a[28:31],v255,s[68:71],s95 offen offset:3072 sc1
ld a[32:35],v255,s[68:71],s96 offen offset:0 sc1
ld a[36:39],v255,s[68:71],s96 offen offset:1024 sc1
ld a[40:43],v255,s[68:71],s96 offen offset:2048 sc1
ld a[44:47],v255,s[68:71],s96 offen offset:3072 sc1
ld a[48:51],v255,s[68:71],s97 offen offset:0 sc1
ld a[52:55],v255,s[68:71],s97 offen offset:1024 sc1
ld a[56:59],v255,s[68:71],s97 offen offset:2048 sc1
ld a[60:63],v255,s[68:71],s97 offen offset:3072 sc1
s_branch .La5_LA_DC1
.La5_LO_NB1:
.La5_LW3_6:
s_cbranch_execz .La5_LA_RETURN
s_mov_b32 s93, 0
.La5_LO_NB1_L:
s_add_u32 s93, s93, 1
s_cmp_gt_u32 s93, 2097152
s_cbranch_scc1 .La5_LO_KB1
s_sleep 8
buffer_load_dwordx2 v[250:251], v248, s[64:67], s5 offen sc1
wt vmcnt(0)
v_cmp_eq_u64 vcc, v[250:251], v[252:253]
s_nop 1
s_bitcmp1_b32 vcc_lo, 1
s_cbranch_scc0 .La5_LO_NB1_L
s_branch .La5_LO_KB1
.La5_LO_NB2:
s_mov_b32 s93, 0
.La5_LO_NB2_L:
s_add_u32 s93, s93, 1
s_cmp_gt_u32 s93, 2097152
s_nop 0
s_cbranch_scc1 .La5_LO_KB2
s_sleep 8
buffer_load_dwordx2 v[250:251], v248, s[64:67], s5 offen sc1
wt vmcnt(0)
v_cmp_eq_u64 vcc, v[250:251], v[252:253]
s_nop 1
s_bitcmp1_b32 vcc_lo, 2
s_cbranch_scc0 .La5_LO_NB2_L
s_branch .La5_LO_KB2
.La5_LO_NB3:
s_mov_b32 s93, 0
.La5_LO_NB3_L:
s_add_u32 s93, s93, 1
s_cmp_gt_u32 s93, 2097152
s_nop 0
s_nop 0
s_cbranch_scc1 .La5_LO_KB3
s_sleep 8
buffer_load_dwordx2 v[250:251], v248, s[64:67], s5 offen sc1
wt vmcnt(0)
v_cmp_eq_u64 vcc, v[250:251], v[252:253]
s_nop 1
s_bitcmp1_b32 vcc_lo, 3
s_cbranch_scc0 .La5_LO_NB3_L
s_branch .La5_LO_KB3
.La5_LO_NB4:
s_mov_b32 s93, 0
.La5_LO_NB4_L:
s_add_u32 s93, s93, 1
s_cmp_gt_u32 s93, 2097152
s_nop 0
s_nop 0
s_cbranch_scc1 .La5_LO_KB4
s_sleep 8
buffer_load_dwordx2 v[250:251], v248, s[64:67], s5 offen sc1
wt vmcnt(0)
v_cmp_eq_u64 vcc, v[250:251], v[252:253]
s_nop 1
s_bitcmp1_b32 vcc_lo, 4
s_cbranch_scc0 .La5_LO_NB4_L
s_branch .La5_LO_KB4
.La5_LO_NB5:
s_mov_b32 s93, 0
.La5_LO_NB5_L:
)ASM" R"ASM(s_add_u32 s93, s93, 1
s_cmp_gt_u32 s93, 2097152
s_nop 0
s_nop 0
s_cbranch_scc1 .La5_LO_KB5
s_sleep 8
buffer_load_dwordx2 v[250:251], v248, s[64:67], s5 offen sc1
wt vmcnt(0)
v_cmp_eq_u64 vcc, v[250:251], v[252:253]
s_nop 1
s_bitcmp1_b32 vcc_lo, 5
s_cbranch_scc0 .La5_LO_NB5_L
s_branch .La5_LO_KB5
.La5_LO_NB13:
s_mov_b32 s93, 0
.La5_LO_NB13_L:
s_add_u32 s93, s93, 1
s_cmp_gt_u32 s93, 2097152
s_nop 0
s_nop 0
s_cbranch_scc1 .La5_LO_KB13
s_sleep 8
buffer_load_dwordx2 v[250:251], v248, s[64:67], s5 offen sc1
wt vmcnt(0)
v_cmp_eq_u64 vcc, v[250:251], v[252:253]
s_nop 1
s_bitcmp1_b32 vcc_lo, 13
s_cbranch_scc0 .La5_LO_NB13_L
s_branch .La5_LO_KB13
.La5_LO_NB6:
s_mov_b32 s93, 0
.La5_LO_NB6_L:
s_add_u32 s93, s93, 1
s_cmp_gt_u32 s93, 2097152
s_nop 0
s_nop 0
s_cbranch_scc1 .La5_LO_KB6
s_sleep 8
buffer_load_dwordx2 v[250:251], v248, s[64:67], s5 offen sc1
wt vmcnt(0)
v_cmp_eq_u64 vcc, v[250:251], v[252:253]
s_nop 1
s_bitcmp1_b32 vcc_lo, 6
s_cbranch_scc0 .La5_LO_NB6_L
s_branch .La5_LO_KB6
.La5_LO_NB7:
s_mov_b32 s93, 0
.La5_LO_NB7_L:
s_add_u32 s93, s93, 1
s_cmp_gt_u32 s93, 2097152
s_nop 0
s_nop 0
s_cbranch_scc1 .La5_LO_KB7
s_sleep 8
buffer_load_dwordx2 v[250:251], v248, s[64:67], s5 offen sc1
wt vmcnt(0)
v_cmp_eq_u64 vcc, v[250:251], v[252:253]
s_nop 1
s_bitcmp1_b32 vcc_lo, 7
s_cbranch_scc0 .La5_LO_NB7_L
s_branch .La5_LO_KB7
.La5_LO_NC1:
.La5_LW7_7:
s_cbranch_execz .La5_LA_RETURN
s_mov_b32 s93, 0
.La5_LO_NC1_L:
s_add_u32 s93, s93, 1
s_cmp_gt_u32 s93, 2097152
s_nop 0
s_cbranch_scc1 .La5_LO_KC1
s_sleep 8
buffer_load_dwordx2 v[250:251], v248, s[64:67], s5 offen sc1
wt vmcnt(0)
v_cmp_eq_u64 vcc, v[250:251], v[252:253]
s_nop 1
s_bitcmp1_b32 vcc_lo, 1
s_cbranch_scc0 .La5_LO_NC1_L
s_branch .La5_LO_KC1
.La5_LO_NC2:
s_mov_b32 s93, 0
.La5_LO_NC2_L:
s_add_u32 s93, s93, 1
s_cmp_gt_u32 s93, 2097152
s_nop 0
s_nop 0
s_cbranch_scc1 .La5_LO_KC2
s_sleep 8
buffer_load_dwordx2 v[250:251], v248, s[64:67], s5 offen sc1
wt vmcnt(0)
v_cmp_eq_u64 vcc, v[250:251], v[252:253]
s_nop 1
s_bitcmp1_b32 vcc_lo, 2
s_cbranch_scc0 .La5_LO_NC2_L
s_branch .La5_LO_KC2
.La5_LO_NC3:
s_mov_b32 s93, 0
.La5_LO_NC3_L:
s_add_u32 s93, s93, 1
s_cmp_gt_u32 s93, 2097152
s_nop 0
)ASM" R"ASM(s_nop 0
s_cbranch_scc1 .La5_LO_KC3
s_sleep 8
buffer_load_dwordx2 v[250:251], v248, s[64:67], s5 offen sc1
wt vmcnt(0)
v_cmp_eq_u64 vcc, v[250:251], v[252:253]
s_nop 1
s_bitcmp1_b32 vcc_lo, 3
s_cbranch_scc0 .La5_LO_NC3_L
s_branch .La5_LO_KC3
.La5_LO_NC4:
s_mov_b32 s93, 0
.La5_LO_NC4_L:
s_add_u32 s93, s93, 1
s_cmp_gt_u32 s93, 2097152
s_nop 0
s_nop 0
s_cbranch_scc1 .La5_LO_KC4
s_sleep 8
buffer_load_dwordx2 v[250:251], v248, s[64:67], s5 offen sc1
wt vmcnt(0)
v_cmp_eq_u64 vcc, v[250:251], v[252:253]
s_nop 1
s_bitcmp1_b32 vcc_lo, 4
s_cbranch_scc0 .La5_LO_NC4_L
s_branch .La5_LO_KC4
.La5_LO_NC5:
s_mov_b32 s93, 0
.La5_LO_NC5_L:
s_add_u32 s93, s93, 1
s_cmp_gt_u32 s93, 2097152
s_nop 0
s_nop 0
s_cbranch_scc1 .La5_LO_KC5
s_sleep 8
buffer_load_dwordx2 v[250:251], v248, s[64:67], s5 offen sc1
wt vmcnt(0)
v_cmp_eq_u64 vcc, v[250:251], v[252:253]
s_nop 1
s_bitcmp1_b32 vcc_lo, 5
s_cbranch_scc0 .La5_LO_NC5_L
s_branch .La5_LO_KC5
.La5_LO_NC6:
s_mov_b32 s93, 0
.La5_LO_NC6_L:
s_add_u32 s93, s93, 1
s_cmp_gt_u32 s93, 2097152
s_nop 0
s_nop 0
s_cbranch_scc1 .La5_LO_KC6
s_sleep 8
buffer_load_dwordx2 v[250:251], v248, s[64:67], s5 offen sc1
wt vmcnt(0)
v_cmp_eq_u64 vcc, v[250:251], v[252:253]
s_nop 1
s_bitcmp1_b32 vcc_lo, 6
s_cbranch_scc0 .La5_LO_NC6_L
s_branch .La5_LO_KC6
.La5_LO_NC7:
s_mov_b32 s93, 0
.La5_LO_NC7_L:
s_add_u32 s93, s93, 1
s_cmp_gt_u32 s93, 2097152
s_nop 0
s_nop 0
s_cbranch_scc1 .La5_LO_KC7
s_sleep 8
buffer_load_dwordx2 v[250:251], v248, s[64:67], s5 offen sc1
wt vmcnt(0)
v_cmp_eq_u64 vcc, v[250:251], v[252:253]
s_nop 1
s_bitcmp1_b32 vcc_lo, 7
s_cbranch_scc0 .La5_LO_NC7_L
s_branch .La5_LO_KC7
.La5_LA_SC3:
s_call_b64 s[24:25], .La5_LA_SLOW0
aw a0,0
aw a1,0
aw a2,0
aw a3,0
aw a4,0
aw a5,0
aw a6,0
aw a7,0
aw a8,0
aw a9,0
aw a10,0
aw a11,0
aw a12,0
aw a13,0
aw a14,0
aw a15,0
aw a16,0
aw a17,0
aw a18,0
aw a19,0
aw a20,0
aw a21,0
aw a22,0
aw a23,0
)ASM" R"ASM(aw a24,0
aw a25,0
aw a26,0
aw a27,0
aw a28,0
aw a29,0
aw a30,0
aw a31,0
s_nop 1
v_mov_b32 v250, v242
v_mov_b32 v249, v243
v_xor_b32 v242, 64, v240
v_xor_b32 v243, 96, v240
ar v254,a229
s_mov_b32 s93, 0
ld a[128:131],v254,s[40:43],s93 offen
s_mov_b32 s93, 7168
ld a[132:135],v254,s[40:43],s93 offen
s_mov_b32 s93, 14336
ld a[136:139],v254,s[40:43],s93 offen
s_mov_b32 s93, 21504
ld a[140:143],v254,s[40:43],s93 offen
s_mov_b32 s93, 28672
ld a[144:147],v254,s[40:43],s93 offen
s_mov_b32 s93, 35840
ld a[148:151],v254,s[40:43],s93 offen
s_mov_b32 s93, 43008
ld a[152:155],v254,s[40:43],s93 offen
s_mov_b32 s93, 50176
ld a[156:159],v254,s[40:43],s93 offen
s_mov_b32 s93, 57344
ld a[160:163],v254,s[40:43],s93 offen
s_mov_b32 s93, 64512
ld a[164:167],v254,s[40:43],s93 offen
s_mov_b32 s93, 71680
ld a[168:171],v254,s[40:43],s93 offen
s_mov_b32 s93, 78848
ld a[172:175],v254,s[40:43],s93 offen
s_mov_b32 s93, 86016
ld a[176:179],v254,s[40:43],s93 offen
s_mov_b32 s93, 93184
ld a[180:183],v254,s[40:43],s93 offen
s_mov_b32 s93, 100352
ld a[184:187],v254,s[40:43],s93 offen
s_mov_b32 s93, 107520
ld a[188:191],v254,s[40:43],s93 offen
s_branch .La5_LA_SR3
.La5_LA_SC4:
s_call_b64 s[24:25], .La5_LA_SLOW1
aw a32,0
aw a33,0
aw a34,0
aw a35,0
aw a36,0
aw a37,0
aw a38,0
aw a39,0
aw a40,0
aw a41,0
aw a42,0
aw a43,0
aw a44,0
aw a45,0
aw a46,0
aw a47,0
aw a48,0
aw a49,0
aw a50,0
aw a51,0
aw a52,0
aw a53,0
aw a54,0
aw a55,0
aw a56,0
aw a57,0
aw a58,0
aw a59,0
aw a60,0
aw a61,0
aw a62,0
aw a63,0
s_nop 1
v_mov_b32 v250, v242
v_mov_b32 v249, v243
v_xor_b32 v242, 64, v240
v_xor_b32 v243, 96, v240
ar v254,a229
s_mov_b32 s93, 0
ld a[128:131],v254,s[40:43],s93 offen
s_mov_b32 s93, 7168
ld a[132:135],v254,s[40:43],s93 offen
s_mov_b32 s93, 14336
ld a[136:139],v254,s[40:43],s93 offen
s_mov_b32 s93, 21504
ld a[140:143],v254,s[40:43],s93 offen
s_mov_b32 s93, 28672
ld a[144:147],v254,s[40:43],s93 offen
s_mov_b32 s93, 35840
ld a[148:151],v254,s[40:43],s93 offen
s_mov_b32 s93, 43008
)ASM" R"ASM(ld a[152:155],v254,s[40:43],s93 offen
s_mov_b32 s93, 50176
ld a[156:159],v254,s[40:43],s93 offen
s_mov_b32 s93, 57344
ld a[160:163],v254,s[40:43],s93 offen
s_mov_b32 s93, 64512
ld a[164:167],v254,s[40:43],s93 offen
s_mov_b32 s93, 71680
ld a[168:171],v254,s[40:43],s93 offen
s_mov_b32 s93, 78848
ld a[172:175],v254,s[40:43],s93 offen
s_mov_b32 s93, 86016
ld a[176:179],v254,s[40:43],s93 offen
s_mov_b32 s93, 93184
ld a[180:183],v254,s[40:43],s93 offen
s_mov_b32 s93, 100352
ld a[184:187],v254,s[40:43],s93 offen
s_mov_b32 s93, 107520
ld a[188:191],v254,s[40:43],s93 offen
s_branch .La5_LA_SR4
.La5_LO_VC1:
.La5_LW5_4:
s_cbranch_execz .La5_LW5_R0
.La5_LW6_7:
s_cbranch_execz .La5_LW6_R0
s_getpc_b64 s[24:25]
.La5_LW7_P0:
s_add_u32 s24, s24, .La5_LW7_R0-.La5_LW7_P0
s_addc_u32 s25, s25, 0
s_mov_b64 exec, 0
s_cmp_eq_u32 s3, 0
s_cbranch_scc1 .La5_LW7_0
s_cmp_eq_u32 s3, 1
s_cbranch_scc1 .La5_LW7_1
s_cmp_eq_u32 s3, 2
s_cbranch_scc1 .La5_LW7_2
s_branch .La5_LW7_3
.La5_LW7_R0:
s_mov_b64 exec, -1
s_branch .La5_LO_VE1
.La5_LO_W:
s_mov_b32 s93, 3670016
ld a[192:195],v228,s[40:43],s93 offen
s_mov_b32 s93, 3677184
ld a[196:199],v228,s[40:43],s93 offen
s_mov_b32 s93, 3684352
ld a[200:203],v228,s[40:43],s93 offen
s_mov_b32 s93, 3691520
ld a[204:207],v228,s[40:43],s93 offen
s_mov_b32 s93, 3698688
ld a[208:211],v228,s[40:43],s93 offen
s_mov_b32 s93, 3705856
ld a[212:215],v228,s[40:43],s93 offen
s_mov_b32 s93, 3713024
ld a[216:219],v228,s[40:43],s93 offen
s_mov_b32 s93, 3720192
ld a[220:223],v228,s[40:43],s93 offen
s_mov_b32 s93, 3727360
ld a[224:227],v228,s[40:43],s93 offen
s_mov_b32 s93, 3734528
ld a[228:231],v228,s[40:43],s93 offen
s_mov_b32 s93, 3741696
ld a[232:235],v228,s[40:43],s93 offen
s_mov_b32 s93, 3748864
ld a[236:239],v228,s[40:43],s93 offen
s_mov_b32 s93, 3756032
ld a[240:243],v228,s[40:43],s93 offen
s_mov_b32 s93, 3763200
ld a[244:247],v228,s[40:43],s93 offen
s_mov_b32 s93, 3770368
ld a[248:251],v228,s[40:43],s93 offen
s_mov_b32 s93, 3777536
ld a[252:255],v228,s[40:43],s93 offen
s_branch .La5_LO_START
.La5_LO_VC2:
s_getpc_b64 s[24:25]
.La5_LW7_P1:
s_add_u32 s24, s24, .La5_LW7_R1-.La5_LW7_P1
s_addc_u32 s25, s25, 0
s_mov_b64 exec, 0
s_cmp_eq_u32 s3, 0
s_cbranch_scc1 .La5_LW7_0
s_cmp_eq_u32 s3, 1
s_cbranch_scc1 .La5_LW7_1
s_cmp_eq_u32 s3, 2
s_cbranch_scc1 .La5_LW7_2
s_branch .La5_LW7_3
.La5_LW7_R1:
s_mov_b64 exec, -1
s_branch .La5_LO_VE2
.La5_LT_VAR:
v_mov_b32 v230, 0
v_lshlrev_b32 v232, 16, v96
v_and_b32 v233, 0xffff0000, v96
v_sub_f32 v232, v232, v244
v_sub_f32 v233, v233, v244
v_fmac_f32 v230, v232, v232
v_fmac_f32 v230, v233, v233
v_lshlrev_b32 v232, 16, v97
v_and_b32 v233, 0xffff0000, v97
)ASM" R"ASM(v_sub_f32 v232, v232, v244
v_sub_f32 v233, v233, v244
v_fmac_f32 v230, v232, v232
v_fmac_f32 v230, v233, v233
v_lshlrev_b32 v232, 16, v98
v_and_b32 v233, 0xffff0000, v98
v_sub_f32 v232, v232, v244
v_sub_f32 v233, v233, v244
v_fmac_f32 v230, v232, v232
v_fmac_f32 v230, v233, v233
v_lshlrev_b32 v232, 16, v99
v_and_b32 v233, 0xffff0000, v99
v_sub_f32 v232, v232, v244
v_sub_f32 v233, v233, v244
v_fmac_f32 v230, v232, v232
v_fmac_f32 v230, v233, v233
v_lshlrev_b32 v232, 16, v100
v_and_b32 v233, 0xffff0000, v100
v_sub_f32 v232, v232, v244
v_sub_f32 v233, v233, v244
v_fmac_f32 v230, v232, v232
v_fmac_f32 v230, v233, v233
v_lshlrev_b32 v232, 16, v101
v_and_b32 v233, 0xffff0000, v101
v_sub_f32 v232, v232, v244
v_sub_f32 v233, v233, v244
v_fmac_f32 v230, v232, v232
v_fmac_f32 v230, v233, v233
v_lshlrev_b32 v232, 16, v102
v_and_b32 v233, 0xffff0000, v102
v_sub_f32 v232, v232, v244
v_sub_f32 v233, v233, v244
v_fmac_f32 v230, v232, v232
v_fmac_f32 v230, v233, v233
v_lshlrev_b32 v232, 16, v103
v_and_b32 v233, 0xffff0000, v103
v_sub_f32 v232, v232, v244
v_sub_f32 v233, v233, v244
v_fmac_f32 v230, v232, v232
v_fmac_f32 v230, v233, v233
v_lshlrev_b32 v232, 16, v104
v_and_b32 v233, 0xffff0000, v104
v_sub_f32 v232, v232, v244
v_sub_f32 v233, v233, v244
v_fmac_f32 v230, v232, v232
v_fmac_f32 v230, v233, v233
v_lshlrev_b32 v232, 16, v105
v_and_b32 v233, 0xffff0000, v105
v_sub_f32 v232, v232, v244
v_sub_f32 v233, v233, v244
v_fmac_f32 v230, v232, v232
v_fmac_f32 v230, v233, v233
v_lshlrev_b32 v232, 16, v106
v_and_b32 v233, 0xffff0000, v106
v_sub_f32 v232, v232, v244
v_sub_f32 v233, v233, v244
v_fmac_f32 v230, v232, v232
v_fmac_f32 v230, v233, v233
v_lshlrev_b32 v232, 16, v107
v_and_b32 v233, 0xffff0000, v107
v_sub_f32 v232, v232, v244
v_sub_f32 v233, v233, v244
v_fmac_f32 v230, v232, v232
v_fmac_f32 v230, v233, v233
v_lshlrev_b32 v232, 16, v108
v_and_b32 v233, 0xffff0000, v108
v_sub_f32 v232, v232, v244
v_sub_f32 v233, v233, v244
v_fmac_f32 v230, v232, v232
v_fmac_f32 v230, v233, v233
v_lshlrev_b32 v232, 16, v109
v_and_b32 v233, 0xffff0000, v109
v_sub_f32 v232, v232, v244
v_sub_f32 v233, v233, v244
v_fmac_f32 v230, v232, v232
v_fmac_f32 v230, v233, v233
v_lshlrev_b32 v232, 16, v110
v_and_b32 v233, 0xffff0000, v110
v_sub_f32 v232, v232, v244
v_sub_f32 v233, v233, v244
v_fmac_f32 v230, v232, v232
v_fmac_f32 v230, v233, v233
v_lshlrev_b32 v232, 16, v111
v_and_b32 v233, 0xffff0000, v111
v_sub_f32 v232, v232, v244
v_sub_f32 v233, v233, v244
v_fmac_f32 v230, v232, v232
v_fmac_f32 v230, v233, v233
s_nop 1
v_add_f32_dpp v230, v230, v230 quad_perm:[1,0,3,2] row_mask:0xf bank_mask:0xf
s_nop 1
v_add_f32_dpp v230, v230, v230 quad_perm:[2,3,0,1] row_mask:0xf bank_mask:0xf
s_nop 1
v_add_f32_dpp v230, v230, v230 row_ror:8 row_mask:0xf bank_mask:0xf
v_mov_b32 v231, v230
s_nop 1
v_permlane16_swap_b32 v231, v230
s_nop 1
v_add_f32 v230, v230, v231
v_mov_b32 v231, v230
)ASM" R"ASM(s_nop 1
v_permlane32_swap_b32 v231, v230
s_nop 1
v_add_f32 v230, v230, v231
v_mul_f32 v230, 0x3a800000, v230
s_branch .La5_LT_VOK
.La5_LT_H1:
s_add_u32 s96, s96, 1
s_lshr_b32 s93, s96, 16
s_cbranch_scc1 .La5_LT_H2
ld v[224:227],v209,s[44:47],s92 offen offset:0 sc1
ld v[232:235],v209,s[44:47],s92 offen offset:16 sc1
ld v[244:247],v209,s[44:47],s92 offen offset:1024 sc1
ld v[248:251],v209,s[44:47],s92 offen offset:1040 sc1
s_branch .La5_LT_H0
.La5_LA_KSPIN:
s_mov_b32 s93, 0
.La5_LA_KSPIN_L:
s_add_u32 s93, s93, 1
s_cmp_gt_u32 s93, 2097152
s_cbranch_scc1 .La5_LA_KOK
s_sleep 8
buffer_load_dword v250, v251, s[64:67], s20 offen sc1
wt vmcnt(0)
v_cmp_eq_u32 vcc, v250, v249
s_nop 1
s_or_b32 vcc_hi, vcc_hi, s79
s_cmp_eq_u64 vcc, -1
s_cbranch_scc0 .La5_LA_KSPIN_L
s_branch .La5_LA_KOK
.La5_LA_QSPIN:
s_mov_b32 s93, 0
.La5_LA_QSPIN_L:
s_add_u32 s93, s93, 1
s_cmp_gt_u32 s93, 2097152
s_cbranch_scc1 .La5_LA_QOK
s_sleep 8
buffer_load_dword v250, v251, s[64:67], s20 offen sc1
wt vmcnt(0)
v_cmp_eq_u32 vcc, v250, v249
s_nop 1
s_and_b32 s92, vcc_lo, 3
s_cmp_eq_u32 s92, 3
s_cbranch_scc0 .La5_LA_QSPIN_L
s_branch .La5_LA_QOK
.La5_LEND:
.La5_LCODEEND:
)ASM"
        :: "{s[0:1]}"(__builtin_amdgcn_kernarg_segment_ptr()), "{s2}"(blockIdx.x), "{s3}"(__builtin_amdgcn_readfirstlane(threadIdx.x >> 6)), "{s[4:5]}"(DBGP)
        : "v0","v1","v2","v3","v4","v5","v6","v7","v8","v9","v10","v11","v12","v13","v14","v15","v16","v17","v18","v19","v20","v21","v22","v23","v24","v25","v26","v27","v28","v29","v30","v31","v32","v33","v34","v35","v36","v37","v38","v39","v40","v41","v42","v43","v44","v45","v46","v47","v48","v49","v50","v51","v52","v53","v54","v55","v56","v57","v58","v59","v60","v61","v62","v63","v64","v65","v66","v67","v68","v69","v70","v71","v72","v73","v74","v75","v76","v77","v78","v79","v80","v81","v82","v83","v84","v85","v86","v87","v88","v89","v90","v91","v92","v93","v94","v95","v96","v97","v98","v99","v100","v101","v102","v103","v104","v105","v106","v107","v108","v109","v110","v111","v112","v113","v114","v115","v116","v117","v118","v119","v120","v121","v122","v123","v124","v125","v126","v127","v128","v129","v130","v131","v132","v133","v134","v135","v136","v137","v138","v139","v140","v141","v142","v143","v144","v145","v146","v147","v148","v149","v150","v151","v152","v153","v154","v155","v156","v157","v158","v159","v160","v161","v162","v163","v164","v165","v166","v167","v168","v169","v170","v171","v172","v173","v174","v175","v176","v177","v178","v179","v180","v181","v182","v183","v184","v185","v186","v187","v188","v189","v190","v191","v192","v193","v194","v195","v196","v197","v198","v199","v200","v201","v202","v203","v204","v205","v206","v207","v208","v209","v210","v211","v212","v213","v214","v215","v216","v217","v218","v219","v220","v221","v222","v223","v224","v225","v226","v227","v228","v229","v230","v231","v232","v233","v234","v235","v236","v237","v238","v239","v240","v241","v242","v243","v244","v245","v246","v247","v248","v249","v250","v251","v252","v253","v254","v255","a0","a1","a2","a3","a4","a5","a6","a7","a8","a9","a10","a11","a12","a13","a14","a15","a16","a17","a18","a19","a20","a21","a22","a23","a24","a25","a26","a27","a28","a29","a30","a31","a32","a33","a34","a35","a36","a37","a38","a39","a40","a41","a42","a43","a44","a45","a46","a47","a48","a49","a50","a51","a52","a53","a54","a55","a56","a57","a58","a59","a60","a61","a62","a63","a64","a65","a66","a67","a68","a69","a70","a71","a72","a73","a74","a75","a76","a77","a78","a79","a80","a81","a82","a83","a84","a85","a86","a87","a88","a89","a90","a91","a92","a93","a94","a95","a96","a97","a98","a99","a100","a101","a102","a103","a104","a105","a106","a107","a108","a109","a110","a111","a112","a113","a114","a115","a116","a117","a118","a119","a120","a121","a122","a123","a124","a125","a126","a127","a128","a129","a130","a131","a132","a133","a134","a135","a136","a137","a138","a139","a140","a141","a142","a143","a144","a145","a146","a147","a148","a149","a150","a151","a152","a153","a154","a155","a156","a157","a158","a159","a160","a161","a162","a163","a164","a165","a166","a167","a168","a169","a170","a171","a172","a173","a174","a175","a176","a177","a178","a179","a180","a181","a182","a183","a184","a185","a186","a187","a188","a189","a190","a191","a192","a193","a194","a195","a196","a197","a198","a199","a200","a201","a202","a203","a204","a205","a206","a207","a208","a209","a210","a211","a212","a213","a214","a215","a216","a217","a218","a219","a220","a221","a222","a223","a224","a225","a226","a227","a228","a229","a230","a231","a232","a233","a234","a235","a236","a237","a238","a239","a240","a241","a242","a243","a244","a245","a246","a247","a248","a249","a250","a251","a252","a253","a254","a255","s6","s7","s8","s9","s10","s11","s12","s13","s14","s15","s16","s17","s18","s19","s20","s21","s22","s23","s24","s25","s26","s27","s28","s29","s30","s31","s32","s33","s34","s35","s36","s37","s38","s39","s40","s41","s42","s43","s44","s45","s46","s47","s48","s49","s50","s51","s52","s53","s54","s55","s56","s57","s58","s59","s60","s61","s62","s63","s64","s65","s66","s67","s68","s69","s70","s71","s72","s73","s74","s75","s76","s77","s78","s79","s80","s81","s82","s83","s84","s85","s86","s87","s88","s89","s90","s91","s92","s93","s94","s95","s96","s97","s98","s99","s100","s101","vcc","scc","m0","memory");
}

extern "C" void kernel_launch(void* const* d_in, const int* in_sizes, int n_in, void* d_out, int out_size, void* d_ws, size_t ws_size,
                              hipStream_t stream) {
    (void)hipFuncSetAttribute((const void*)fa_kernel, hipFuncAttributeMaxDynamicSharedMemorySize, LDS_BYTES);
    fa_kernel<<<256, 256, LDS_BYTES, stream>>>((const bf16*)d_in[0], (const bf16*)d_in[1], (const bf16*)d_in[2], (const bf16*)d_in[3], (const bf16*)d_in[4], (char*)d_ws,
                                               (const bf16*)d_in[5], (const bf16*)d_in[8], (const bf16*)d_in[9], (const bf16*)d_in[10], (const bf16*)d_in[7], (bf16*)d_out);
}
```

```cpp
#include <hip/hip_runtime.h>
#include <hip/hip_bf16.h>
#include <stdint.h>
#pragma clang diagnostic ignored "-Winline-asm"
#pragma clang diagnostic ignored "-Woverlength-strings"

typedef __hip_bfloat16 bf16;
#define DBGP ((unsigned*)nullptr)
constexpr int LDS_BYTES = 163840;
extern "C" __global__ __attribute__((aligned(4096))) __launch_bounds__(256, 1) void fa_kernel(const bf16* __restrict__ px, const bf16* __restrict__ ckv, const bf16* __restrict__ wq,
                                                    const bf16* __restrict__ wuk, const bf16* __restrict__ wuv, char* pws,
                                                    const bf16* __restrict__ pwo, const bf16* __restrict__ pwg, const bf16* __restrict__ pwu,
                                                    const bf16* __restrict__ pwd, const bf16* __restrict__ pwr, bf16* __restrict__ pout) {
    asm volatile(R"ASM(
.macro mm x:vararg
v_mfma_f32_16x16x32_bf16 \x
.endm
.macro rt x:vararg
ds_read_b64_tr_b16 \x
.endm
.macro rd x:vararg
ds_read_b128 \x
.endm
.macro ld x:vararg
buffer_load_dwordx4 \x
.endm
.macro st x:vararg
buffer_store_dwordx4 \x
.endm
.macro ar x:vararg
v_accvgpr_read_b32 \x
.endm
.macro aw x:vararg
v_accvgpr_write_b32 \x
.endm
.macro cv x:vararg
v_cvt_pk_bf16_f32 \x
.endm
.macro ex x:vararg
v_exp_f32 \x
.endm
.macro wt x:vararg
s_waitcnt \x
.endm
; ---- roles: head = 4 xcd + (wg >> 6), j = (wg >> 3) & 7; odd j: block j >> 1 (then helps with block 7 - (j >> 1)), even j: block 7 - (j >> 1). First operands, addresses
.La5_LSTART:
s_load_dwordx16 s[16:31], s[0:1], 0
s_load_dwordx8 s[32:39], s[0:1], 64
.p2align 6
v_mbcnt_lo_u32_b32 v64, -1, 0
v_mbcnt_hi_u32_b32 v64, -1, v64
s_getpc_b64 s[92:93]
.La5_LPC:
s_sub_u32 s48, s92, .La5_LPC-.La5_LSTART
s_subb_u32 s49, s93, 0
s_and_b32 s49, s49, 0xffff
s_mov_b32 s50, .La5_LCODEEND-.La5_LSTART
s_mov_b32 s51, 0x20000
s_lshr_b32 s97, s2, 3
s_lshl2_add_u32 s87, s97, s3
v_lshlrev_b32 v133, 7, v64
s_lshl_b32 s87, s87, 13
buffer_load_dword v254, v133, s[48:51], s87 offen
s_and_b32 s92, s2, 7
s_lshr_b32 s93, s2, 6
s_lshl2_add_u32 s72, s92, s93
s_bfe_u32 s73, s2, 0x30003
s_lshr_b32 s74, s73, 1
s_xor_b32 s92, s74, 7
s_and_b32 s75, s73, 1
s_cselect_b32 s76, s74, s92
s_lshr_b32 s77, s3, 1
s_and_b32 s78, s3, 1
v_and_b32 v65, 15, v64
v_lshrrev_b32 v66, 4, v64
v_lshlrev_b32 v255, 4, v64
v_lshlrev_b32 v67, 1, v66
v_xor_b32 v67, v67, v65
v_lshlrev_b32 v67, 4, v67
v_lshl_add_u32 v67, v66, 13, v67
s_lshl_b32 s94, s78, 17
v_add_u32 v232, s94, v67
v_add_u32 v233, 32768, v232
v_add_u32 v234, 65536, v232
v_add_u32 v235, 98304, v232
v_and_b32 v67, 7, v64
v_xor_b32 v68, v67, v66
v_lshlrev_b32 v68, 4, v68
v_lshrrev_b32 v69, 3, v64
v_lshl_add_u32 v68, v69, 10, v68
s_lshl2_add_u32 s94, s3, s93
s_lshl_b32 s82, s94, 11
s_lshl_b32 s94, s94, 14
v_add_u32 v224, s94, v68
v_xor_b32 v225, 0x2040, v224
v_xor_b32 v226, 16384, v224
v_xor_b32 v227, 16384, v225
v_xor_b32 v228, 32768, v224
v_xor_b32 v229, 32768, v225
v_xor_b32 v230, 49152, v224
v_xor_b32 v231, 49152, v225
s_mul_i32 s94, s77, 8448
s_mul_i32 s95, s78, 4224
s_add_u32 s83, s94, s95
s_add_u32 s83, s83, 96256
v_lshlrev_b32 v135, 7, v67
v_lshl_add_u32 v135, v69, 13, v135
v_lshrrev_b32 v132, 5, v64
v_lshl_add_u32 v132, v132, 28, v135
v_lshl_add_u32 v133, v66, 28, v133
v_lshlrev_b32 v134, 10, v65
v_lshl_add_u32 v134, v66, 7, v134
v_lshrrev_b32 v68, 4, v64
v_lshl_add_u32 v134, v68, 28, v134
)ASM" R"ASM(s_lshl1_add_u32 s97, s97, s78
s_lshl_b32 s7, s97, 13
s_lshl_b32 s97, s97, 15
s_lshl_b32 s96, s93, 8
s_sub_u32 s7, s7, s96
s_sub_u32 s87, s87, s96
s_sub_u32 s97, s97, s96
s_add_u32 s86, s97, 2097152
s_lshl2_add_u32 s101, s72, s3
s_lshl_b32 s101, s101, 11
s_lshl2_add_u32 s100, s93, s3
s_lshl_b32 s100, s100, 14
v_lshlrev_b32 v118, 11, v67
s_lshl_b32 s93, s3, 9
v_lshl_add_u32 v119, v69, 4, v118
v_add_u32 v119, s93, v119
s_lshl_b32 s97, s2, 14
s_lshl_b32 s94, s72, 8
s_lshl_b32 s95, s76, 18
s_sub_u32 s46, 4194304, s94
s_mov_b32 s47, 0x20000
s_mov_b32 s42, 262144
s_mov_b32 s43, 0x20000
s_mov_b32 s62, 16384
s_mov_b32 s63, 0x20000
s_mov_b32 s79, 512
s_mov_b32 s80, 2097152
s_add_u32 s100, s100, 512
s_mov_b32 s81, 262144
s_cmp_eq_u32 s77, 0
wt lgkmcnt(0)
s_cselect_b64 s[92:93], s[22:23], s[24:25]
s_add_u32 s44, s92, s94
s_addc_u32 s45, s93, 0
s_and_b32 s45, s45, 0xffff
s_add_u32 s40, s18, s95
s_addc_u32 s41, s19, 0
s_and_b32 s41, s41, 0xffff
s_add_u32 s60, s16, s97
s_addc_u32 s61, s17, 0
s_and_b32 s61, s61, 0xffff
buffer_load_dword v254, v132, s[44:47], s86 offen
s_add_u32 s86, s86, 2097152
s_and_b32 s86, s86, 4194303
buffer_load_dword v254, v133, s[40:43], s101 offen
buffer_load_dword v254, v134, s[40:43], s100 offen
s_add_u32 m0, s83, 0
s_nop 0
ld v232,s[44:47],s80 offen lds
s_add_u32 m0, s83, 1024
s_nop 0
ld v233,s[44:47],s80 offen lds
s_add_u32 m0, s83, 2176
s_nop 0
ld v234,s[44:47],s80 offen lds
s_add_u32 m0, s83, 3200
s_nop 0
ld v235,s[44:47],s80 offen lds
s_add_u32 s80, s80, s81
s_and_b32 s80, s80, 4194303
v_lshrrev_b32 v67, 1, v65
v_xor_b32 v67, v67, v66
v_lshlrev_b32 v67, 4, v67
v_lshl_add_u32 v67, v65, 7, v67
s_lshl_b32 s92, s77, 12
v_add_u32 v236, s92, v67
v_xor_b32 v237, 64, v236
v_bfe_u32 v120, v64, 2, 2
v_add_u32 v67, 1, v66
v_lshrrev_b32 v67, 1, v67
v_lshlrev_b32 v67, 7, v67
v_lshl_add_u32 v67, v66, 11, v67
v_lshl_add_u32 v67, v120, 8, v67
v_and_b32 v68, 3, v64
v_lshl_add_u32 v67, v68, 3, v67
s_mul_i32 s92, s78, 8448
v_lshl_add_u32 v67, v120, 5, v67
v_add_u32 v67, s92, v67
v_add_u32 v240, 96256, v67
v_xor_b32 v241, 32, v240
v_xor_b32 v242, 64, v240
v_xor_b32 v243, 96, v240
s_add_u32 s64, s26, 142606336
s_addc_u32 s65, s27, 0
s_and_b32 s65, s65, 0xffff
s_mov_b32 s66, 3145728
s_mov_b32 s67, 0x20000
s_mov_b32 s88, 4096
s_lshr_b32 s92, s2, 1
s_lshl_b32 s92, s92, 15
s_and_b32 s93, s2, 1
s_lshl_b32 s93, s93, 7
s_add_u32 s92, s92, s93
s_lshl_b32 s93, s3, 13
s_add_u32 s92, s92, s93
s_add_u32 s92, s92, 88080384
s_add_u32 s56, s26, s92
s_addc_u32 s57, s27, 0
s_and_b32 s57, s57, 0xffff
s_mov_b32 s58, 8576
)ASM" R"ASM(s_mov_b32 s59, 0x20000
v_and_b32 v71, 0x380, v255
v_and_b32 v127, 0x70, v255
v_lshl_add_u32 v71, v71, 1, v127
v_xor_b32 v67, 3, v69
v_min_u32 v67, 1, v67
v_xor_b32 v67, 1, v67
s_mov_b32 s93, 0
v_mul_lo_u32 v67, v67, s93
v_add_u32 v136, v67, v255
v_and_b32 v67, 3, v69
v_xor_b32 v67, 1, v67
v_min_u32 v67, 1, v67
v_xor_b32 v67, 1, v67
v_and_b32 v67, s2, v67
v_mul_lo_u32 v67, v67, s93
v_add_u32 v71, v71, v67
s_lshl_b32 s92, s3, 3
v_lshlrev_b32 v126, 1, v127
v_or_b32 v126, 65536, v126
v_add_u32 v127, s92, v126
s_add_u32 s52, s26, 171966464
s_addc_u32 s53, s27, 0
s_and_b32 s53, s53, 0xffff
s_mov_b32 s54, 2097152
s_mov_b32 s55, 0x20000
s_lshl2_add_u32 s95, s2, s3
s_lshl_b32 s91, s95, 3
s_lshl_b32 s95, s95, 10
s_lshl_b32 s89, s76, 10
v_lshl_add_u32 v135, v69, 28, v135
s_mov_b32 s96, s87
s_mov_b32 s84, 33792
s_mov_b32 s85, 0
s_add_u32 s97, s83, 67584
s_mov_b32 s90, s83
s_lshl_b32 s92, s72, 8
s_sub_u32 s93, 8388608, s92
s_add_u32 s68, s20, s92
s_addc_u32 s69, s21, 0
s_and_b32 s69, s69, 0xffff
s_mov_b32 s70, s93
s_mov_b32 s71, 0x20000
s_lshr_b32 s92, s20, 7
s_sub_u32 s92, 3, s92
s_bfe_u32 s99, s92, 0x20001
s_lshr_b32 s93, s2, 6
s_sub_u32 s99, s99, s93
s_lshl_b32 s92, s99, 8
s_lshr_b32 s93, s2, 3
s_lshl2_add_u32 s93, s93, s3
s_lshl_b32 s98, s93, 16
s_add_u32 s98, s98, s92
v_lshlrev_b32 v138, 4, v65
v_lshl_add_u32 v137, v66, 13, v138
v_lshl_add_u32 v138, v66, 9, v138
s_lshl_b32 s93, s93, 12
v_add_u32 v138, s93, v138
s_and_b32 s93, s2, 7
s_lshl_b32 s92, s93, 19
s_add_u32 s92, s92, 138412032
s_add_u32 s4, s26, s92
s_addc_u32 s5, s27, 0
s_and_b32 s5, s5, 0xffff
s_mov_b32 s6, 0
s_mov_b32 s100, 1073741824
s_add_u32 s101, s91, 65536
v_lshrrev_b32 v139, 1, v64
v_lshlrev_b32 v139, 8, v139
v_and_or_b32 v139, v255, 16, v139
s_lshl_b32 s93, s93, 5
s_add_u32 s93, s93, 64512
v_add_u32 v139, s93, v139
s_mov_b64 exec, 0
s_cmp_eq_u32 s3, 0
s_cbranch_scc1 .La5_LW1_0
s_cmp_eq_u32 s3, 1
s_cbranch_scc1 .La5_LW1_1
s_cmp_eq_u32 s3, 2
s_cbranch_scc1 .La5_LW1_2
s_branch .La5_LW1_3
.La5_LW1_R0:
s_mov_b64 exec, -1
s_xor_b32 m0, s82, 0
s_nop 0
ld v224,s[40:43],s79 offen lds
s_xor_b32 m0, s82, 1024
s_nop 0
ld v225,s[40:43],s79 offen lds
s_xor_b32 m0, s82, 2048
s_nop 0
ld v226,s[40:43],s79 offen lds
s_xor_b32 m0, s82, 3072
s_nop 0
ld v227,s[40:43],s79 offen lds
s_xor_b32 m0, s82, 4096
s_nop 0
ld v228,s[40:43],s79 offen lds
s_xor_b32 m0, s82, 5120
s_nop 0
)ASM" R"ASM(ld v229,s[40:43],s79 offen lds
s_xor_b32 m0, s82, 6144
s_nop 0
ld v230,s[40:43],s79 offen lds
s_xor_b32 m0, s82, 7168
s_nop 0
ld v231,s[40:43],s79 offen lds
s_add_u32 s79, s79, 128
s_and_b32 s79, s79, 1023
s_add_u32 m0, s83, 16896
s_nop 0
ld v232,s[44:47],s80 offen lds
s_add_u32 m0, s83, 17920
s_nop 0
ld v233,s[44:47],s80 offen lds
s_add_u32 m0, s83, 19072
s_nop 0
ld v234,s[44:47],s80 offen lds
s_add_u32 m0, s83, 20096
s_nop 0
ld v235,s[44:47],s80 offen lds
s_add_u32 s80, s80, s81
s_and_b32 s80, s80, 4194303
buffer_load_dwordx2 v[252:253], off, s[64:67], 0 sc1
wt vmcnt(1)
s_barrier
rd v[160:163],v236 offset:24576
rd v[164:167],v237 offset:24576
rt v[192:193],v240 offset:0
rt v[194:195],v240 offset:1024
rt v[196:197],v240 offset:16896
rt v[198:199],v240 offset:17920
rt v[200:201],v241 offset:0
rt v[202:203],v241 offset:1024
rt v[204:205],v241 offset:16896
rt v[206:207],v241 offset:17920
rt v[208:209],v242 offset:0
rt v[210:211],v242 offset:1024
rt v[212:213],v242 offset:16896
rt v[214:215],v242 offset:17920
rt v[216:217],v243 offset:0
rt v[218:219],v243 offset:1024
rt v[220:221],v243 offset:16896
rt v[222:223],v243 offset:17920
rd v[168:171],v236 offset:26624
rd v[172:175],v237 offset:26624
rd v[176:179],v236 offset:16384
rd v[180:183],v237 offset:16384
rd v[184:187],v236 offset:18432
rd v[188:191],v237 offset:18432
wt lgkmcnt(0)
s_cmp_eq_u32 s78, 1
s_cbranch_scc1 .La5_LV_START
; ---- K tile (waves 0, 2): 256 keys x 128 columns, K = 512; LayerNorm of 8 rows of x in the gaps (softmax scale folded in)
.La5_LK_START:
mm a[96:99],v[192:195],v[160:163],0
s_add_u32 m0, s83, 33792
mm a[96:99],v[196:199],v[164:167],a[96:99]
ld v232,s[44:47],s80 offen lds
mm a[100:103],v[204:207],v[164:167],0
rd v[0:3],v236 offset:8192
rd v[4:7],v237 offset:8192
s_add_u32 m0, s83, 34816
mm a[100:103],v[200:203],v[160:163],a[100:103]
rd v[8:11],v236 offset:10240
rd v[12:15],v237 offset:10240
ld v233,s[44:47],s80 offen lds
mm a[104:107],v[208:211],v[160:163],0
s_add_u32 m0, s83, 35968
mm a[104:107],v[212:215],v[164:167],a[104:107]
ld v234,s[44:47],s80 offen lds
mm a[108:111],v[220:223],v[164:167],0
rd v[16:19],v236 offset:0
rd v[20:23],v237 offset:0
s_add_u32 m0, s83, 36992
mm a[108:111],v[216:219],v[160:163],a[108:111]
rd v[24:27],v236 offset:2048
rd v[28:31],v237 offset:2048
ld v235,s[44:47],s80 offen lds
mm a[140:143],v[216:219],v[168:171],0
s_mov_b32 s80, 2883584
mm a[140:143],v[220:223],v[172:175],a[140:143]
mm a[136:139],v[212:215],v[172:175],0
s_xor_b32 m0, s82, 32768
mm a[136:139],v[208:211],v[168:171],a[136:139]
rt v[32:33],v240 offset:128
rt v[34:35],v240 offset:1152
ld v224,s[40:43],s79 offen lds
mm a[124:127],v[200:203],v[168:171],0
s_xor_b32 m0, s82, 33792
mm a[124:127],v[204:207],v[172:175],a[124:127]
ld v225,s[40:43],s79 offen lds
mm a[120:123],v[196:199],v[172:175],0
s_xor_b32 m0, s82, 34816
mm a[120:123],v[192:195],v[168:171],a[120:123]
rt v[36:37],v240 offset:17024
rt v[38:39],v240 offset:18048
ld v226,s[40:43],s79 offen lds
mm a[232:235],v[192:195],v[176:179],0
s_xor_b32 m0, s82, 35840
)ASM" R"ASM(mm a[232:235],v[196:199],v[180:183],a[232:235]
ld v227,s[40:43],s79 offen lds
mm a[236:239],v[204:207],v[180:183],0
s_xor_b32 m0, s82, 36864
mm a[236:239],v[200:203],v[176:179],a[236:239]
rt v[40:41],v241 offset:128
rt v[42:43],v241 offset:1152
ld v228,s[40:43],s79 offen lds
mm a[152:155],v[208:211],v[176:179],0
s_xor_b32 m0, s82, 37888
mm a[152:155],v[212:215],v[180:183],a[152:155]
ld v229,s[40:43],s79 offen lds
mm a[156:159],v[220:223],v[180:183],0
s_xor_b32 m0, s82, 38912
mm a[156:159],v[216:219],v[176:179],a[156:159]
ld v230,s[40:43],s79 offen lds
mm a[172:175],v[216:219],v[184:187],0
s_xor_b32 m0, s82, 39936
mm a[172:175],v[220:223],v[188:191],a[172:175]
ld v231,s[40:43],s79 offen lds
mm a[168:171],v[212:215],v[188:191],0
s_mov_b32 s79, 768
mm a[168:171],v[208:211],v[184:187],a[168:171]
rt v[44:45],v241 offset:17024
rt v[46:47],v241 offset:18048
mm a[220:223],v[200:203],v[184:187],0
s_add_u32 m0, s83, 50688
mm a[220:223],v[204:207],v[188:191],a[220:223]
ld v232,s[44:47],s80 offen lds
mm a[216:219],v[196:199],v[188:191],0
wt lgkmcnt(8)
s_add_u32 m0, s83, 51712
mm a[216:219],v[192:195],v[184:187],a[216:219]
ld v233,s[44:47],s80 offen lds
mm a[200:203],v[192:195],v[0:3],0
s_add_u32 m0, s83, 52864
mm a[200:203],v[196:199],v[4:7],a[200:203]
ld v234,s[44:47],s80 offen lds
mm a[204:207],v[204:207],v[4:7],0
s_add_u32 m0, s83, 53888
mm a[204:207],v[200:203],v[0:3],a[204:207]
rt v[48:49],v242 offset:128
rt v[50:51],v242 offset:1152
ld v235,s[44:47],s80 offen lds
mm a[184:187],v[208:211],v[0:3],0
s_mov_b32 s80, 3145728
mm a[184:187],v[212:215],v[4:7],a[184:187]
mm a[188:191],v[220:223],v[4:7],0
mm a[188:191],v[216:219],v[0:3],a[188:191]
mm a[12:15],v[216:219],v[8:11],0
mm a[12:15],v[220:223],v[12:15],a[12:15]
buffer_load_dword v70, v118, s[60:63], 0 offen
mm a[8:11],v[212:215],v[12:15],0
mm a[8:11],v[208:211],v[8:11],a[8:11]
rt v[52:53],v242 offset:17024
rt v[54:55],v242 offset:18048
mm a[76:79],v[200:203],v[8:11],0
mm a[76:79],v[204:207],v[12:15],a[76:79]
ld v[72:75],v119,s[60:63],0 offen offset:0
mm a[72:75],v[196:199],v[12:15],0
mm a[72:75],v[192:195],v[8:11],a[72:75]
rt v[56:57],v243 offset:128
rt v[58:59],v243 offset:1152
mm a[88:91],v[192:195],v[16:19],0
mm a[88:91],v[196:199],v[20:23],a[88:91]
ld v[76:79],v119,s[60:63],0 offen offset:128
mm a[92:95],v[204:207],v[20:23],0
mm a[92:95],v[200:203],v[16:19],a[92:95]
rt v[60:61],v243 offset:17024
rt v[62:63],v243 offset:18048
mm a[24:27],v[208:211],v[16:19],0
mm a[24:27],v[212:215],v[20:23],a[24:27]
ld v[80:83],v119,s[60:63],0 offen offset:256
mm a[28:31],v[220:223],v[20:23],0
mm a[28:31],v[216:219],v[16:19],a[28:31]
mm a[44:47],v[216:219],v[24:27],0
mm a[44:47],v[220:223],v[28:31],a[44:47]
ld v[84:87],v119,s[60:63],0 offen offset:384
mm a[40:43],v[212:215],v[28:31],0
mm a[40:43],v[208:211],v[24:27],a[40:43]
mm a[252:255],v[200:203],v[24:27],0
mm a[252:255],v[204:207],v[28:31],a[252:255]
mm a[248:251],v[196:199],v[28:31],0
mm a[248:251],v[192:195],v[24:27],a[248:251]
s_mov_b64 exec, 0
s_cmp_eq_u32 s3, 0
s_cbranch_scc1 .La5_LW2_0
s_branch .La5_LW2_2
.La5_LW2_R0:
s_mov_b64 exec, -1
wt vmcnt(5) lgkmcnt(0)
s_barrier
mm a[112:115],v[32:35],v[160:163],0
mm a[112:115],v[36:39],v[164:167],a[112:115]
mm a[116:119],v[44:47],v[164:167],0
rt v[192:193],v240 offset:33792
rt v[194:195],v240 offset:34816
mm a[116:119],v[40:43],v[160:163],a[116:119]
rt v[196:197],v240 offset:50688
rt v[198:199],v240 offset:51712
)ASM" R"ASM(mm a[128:131],v[48:51],v[160:163],0
mm a[128:131],v[52:55],v[164:167],a[128:131]
mm a[132:135],v[60:63],v[164:167],0
mm a[132:135],v[56:59],v[160:163],a[132:135]
rd v[160:163],v236 offset:57344
rd v[164:167],v237 offset:57344
mm a[148:151],v[56:59],v[168:171],0
mm a[148:151],v[60:63],v[172:175],a[148:151]
mm a[144:147],v[52:55],v[172:175],0
mm a[144:147],v[48:51],v[168:171],a[144:147]
rt v[200:201],v241 offset:33792
rt v[202:203],v241 offset:34816
mm a[228:231],v[40:43],v[168:171],0
mm a[228:231],v[44:47],v[172:175],a[228:231]
mm a[224:227],v[36:39],v[172:175],0
mm a[224:227],v[32:35],v[168:171],a[224:227]
rd v[168:171],v236 offset:59392
rd v[172:175],v237 offset:59392
mm a[208:211],v[32:35],v[176:179],0
s_xor_b32 m0, s82, 0
mm a[208:211],v[36:39],v[180:183],a[208:211]
ld v224,s[40:43],s79 offen lds
mm a[212:215],v[44:47],v[180:183],0
mm a[212:215],v[40:43],v[176:179],a[212:215]
rt v[204:205],v241 offset:50688
rt v[206:207],v241 offset:51712
s_xor_b32 m0, s82, 1024
mm a[160:163],v[48:51],v[176:179],0
ld v225,s[40:43],s79 offen lds
mm a[160:163],v[52:55],v[180:183],a[160:163]
mm a[164:167],v[60:63],v[180:183],0
s_xor_b32 m0, s82, 2048
mm a[164:167],v[56:59],v[176:179],a[164:167]
rd v[176:179],v236 offset:49152
rd v[180:183],v237 offset:49152
ld v226,s[40:43],s79 offen lds
mm a[180:183],v[56:59],v[184:187],0
mm a[180:183],v[60:63],v[188:191],a[180:183]
s_xor_b32 m0, s82, 3072
mm a[176:179],v[52:55],v[188:191],0
ld v227,s[40:43],s79 offen lds
mm a[176:179],v[48:51],v[184:187],a[176:179]
rt v[208:209],v242 offset:33792
rt v[210:211],v242 offset:34816
mm a[196:199],v[40:43],v[184:187],0
s_xor_b32 m0, s82, 4096
mm a[196:199],v[44:47],v[188:191],a[196:199]
ld v228,s[40:43],s79 offen lds
mm a[192:195],v[36:39],v[188:191],0
mm a[192:195],v[32:35],v[184:187],a[192:195]
rd v[184:187],v236 offset:51200
rd v[188:191],v237 offset:51200
s_xor_b32 m0, s82, 5120
mm a[64:67],v[32:35],v[0:3],0
ld v229,s[40:43],s79 offen lds
mm a[64:67],v[36:39],v[4:7],a[64:67]
mm a[68:71],v[44:47],v[4:7],0
s_xor_b32 m0, s82, 6144
mm a[68:71],v[40:43],v[0:3],a[68:71]
rt v[212:213],v242 offset:50688
rt v[214:215],v242 offset:51712
ld v230,s[40:43],s79 offen lds
mm a[0:3],v[48:51],v[0:3],0
mm a[0:3],v[52:55],v[4:7],a[0:3]
s_xor_b32 m0, s82, 7168
mm a[4:7],v[60:63],v[4:7],0
ld v231,s[40:43],s79 offen lds
mm a[4:7],v[56:59],v[0:3],a[4:7]
rd v[0:3],v236 offset:40960
rd v[4:7],v237 offset:40960
s_mov_b32 s79, 896
mm a[20:23],v[56:59],v[8:11],0
mm a[20:23],v[60:63],v[12:15],a[20:23]
s_add_u32 m0, s83, 0
mm a[16:19],v[52:55],v[12:15],0
ld v232,s[44:47],s80 offen lds
mm a[16:19],v[48:51],v[8:11],a[16:19]
rt v[216:217],v243 offset:33792
rt v[218:219],v243 offset:34816
mm a[84:87],v[40:43],v[8:11],0
s_add_u32 m0, s83, 1024
mm a[84:87],v[44:47],v[12:15],a[84:87]
ld v233,s[44:47],s80 offen lds
mm a[80:83],v[36:39],v[12:15],0
wt vmcnt(10)
v_lshlrev_b32 v70, 16, v70
mm a[80:83],v[32:35],v[8:11],a[80:83]
rd v[8:11],v236 offset:43008
rd v[12:15],v237 offset:43008
s_add_u32 m0, s83, 2176
mm a[240:243],v[32:35],v[16:19],0
ld v234,s[44:47],s80 offen lds
mm a[240:243],v[36:39],v[20:23],a[240:243]
v_lshlrev_b32 v88, 16, v72
v_and_b32 v89, 0xffff0000, v72
mm a[244:247],v[44:47],v[20:23],0
s_add_u32 m0, s83, 3200
v_sub_f32 v88, v88, v70
mm a[244:247],v[40:43],v[16:19],a[244:247]
rt v[220:221],v243 offset:50688
)ASM" R"ASM(rt v[222:223],v243 offset:51712
ld v235,s[44:47],s80 offen lds
mm a[32:35],v[48:51],v[16:19],0
s_mov_b32 s80, 3407872
v_sub_f32 v89, v89, v70
mm a[32:35],v[52:55],v[20:23],a[32:35]
v_add_f32 v120, v88, v89
v_mul_f32 v122, v88, v88
mm a[36:39],v[60:63],v[20:23],0
s_add_u32 m0, s83, 16896
v_mul_f32 v123, v89, v89
mm a[36:39],v[56:59],v[16:19],a[36:39]
rd v[16:19],v236 offset:32768
rd v[20:23],v237 offset:32768
ld v232,s[44:47],s80 offen lds
mm a[60:63],v[56:59],v[24:27],0
v_lshlrev_b32 v90, 16, v73
v_and_b32 v91, 0xffff0000, v73
mm a[60:63],v[60:63],v[28:31],a[60:63]
s_add_u32 m0, s83, 17920
v_sub_f32 v90, v90, v70
mm a[56:59],v[52:55],v[28:31],0
ld v233,s[44:47],s80 offen lds
mm a[56:59],v[48:51],v[24:27],a[56:59]
v_sub_f32 v91, v91, v70
v_add_f32 v124, v90, v91
mm a[52:55],v[40:43],v[24:27],0
s_add_u32 m0, s83, 19072
v_fmac_f32 v122, v90, v90
mm a[52:55],v[44:47],v[28:31],a[52:55]
ld v234,s[44:47],s80 offen lds
mm a[48:51],v[36:39],v[28:31],0
wt lgkmcnt(2)
v_add_f32 v120, v120, v124
mm a[48:51],v[32:35],v[24:27],a[48:51]
rd v[24:27],v236 offset:34816
rd v[28:31],v237 offset:34816
s_add_u32 m0, s83, 20096
mm a[96:99],v[192:195],v[160:163],a[96:99]
ld v235,s[44:47],s80 offen lds
.La5_LW1_0:
s_cbranch_execz .La5_LW1_4
mm a[96:99],v[196:199],v[164:167],a[96:99]
s_mov_b32 s80, 3670016
v_fmac_f32 v123, v91, v91
mm a[100:103],v[204:207],v[164:167],a[100:103]
v_lshlrev_b32 v92, 16, v74
v_and_b32 v93, 0xffff0000, v74
mm a[100:103],v[200:203],v[160:163],a[100:103]
rt v[32:33],v240 offset:33920
rt v[34:35],v240 offset:34944
mm a[104:107],v[208:211],v[160:163],a[104:107]
v_sub_f32 v92, v92, v70
v_sub_f32 v93, v93, v70
mm a[104:107],v[212:215],v[164:167],a[104:107]
v_add_f32 v124, v92, v93
v_fmac_f32 v122, v92, v92
mm a[108:111],v[220:223],v[164:167],a[108:111]
wt lgkmcnt(2)
s_barrier
mm a[108:111],v[216:219],v[160:163],a[108:111]
v_add_f32 v120, v120, v124
v_fmac_f32 v123, v93, v93
mm a[140:143],v[216:219],v[168:171],a[140:143]
s_xor_b32 m0, s82, 32768
v_lshlrev_b32 v94, 16, v75
mm a[140:143],v[220:223],v[172:175],a[140:143]
ld v224,s[40:43],s79 offen lds
mm a[136:139],v[212:215],v[172:175],a[136:139]
v_and_b32 v95, 0xffff0000, v75
v_sub_f32 v94, v94, v70
mm a[136:139],v[208:211],v[168:171],a[136:139]
rt v[36:37],v240 offset:50816
rt v[38:39],v240 offset:51840
mm a[124:127],v[200:203],v[168:171],a[124:127]
s_xor_b32 m0, s82, 33792
v_sub_f32 v95, v95, v70
mm a[124:127],v[204:207],v[172:175],a[124:127]
ld v225,s[40:43],s79 offen lds
mm a[120:123],v[196:199],v[172:175],a[120:123]
v_add_f32 v124, v94, v95
v_fmac_f32 v122, v94, v94
mm a[120:123],v[192:195],v[168:171],a[120:123]
v_add_f32 v120, v120, v124
v_fmac_f32 v123, v95, v95
mm a[232:235],v[192:195],v[176:179],a[232:235]
s_xor_b32 m0, s82, 34816
v_lshlrev_b32 v96, 16, v76
mm a[232:235],v[196:199],v[180:183],a[232:235]
ld v226,s[40:43],s79 offen lds
mm a[236:239],v[204:207],v[180:183],a[236:239]
v_and_b32 v97, 0xffff0000, v76
v_sub_f32 v96, v96, v70
mm a[236:239],v[200:203],v[176:179],a[236:239]
rt v[40:41],v241 offset:33920
rt v[42:43],v241 offset:34944
mm a[152:155],v[208:211],v[176:179],a[152:155]
s_xor_b32 m0, s82, 35840
v_sub_f32 v97, v97, v70
mm a[152:155],v[212:215],v[180:183],a[152:155]
)ASM" R"ASM(ld v227,s[40:43],s79 offen lds
mm a[156:159],v[220:223],v[180:183],a[156:159]
v_add_f32 v124, v96, v97
v_fmac_f32 v122, v96, v96
mm a[156:159],v[216:219],v[176:179],a[156:159]
v_add_f32 v120, v120, v124
v_fmac_f32 v123, v97, v97
mm a[172:175],v[216:219],v[184:187],a[172:175]
s_xor_b32 m0, s82, 36864
v_lshlrev_b32 v98, 16, v77
mm a[172:175],v[220:223],v[188:191],a[172:175]
ld v228,s[40:43],s79 offen lds
mm a[168:171],v[212:215],v[188:191],a[168:171]
v_and_b32 v99, 0xffff0000, v77
v_sub_f32 v98, v98, v70
mm a[168:171],v[208:211],v[184:187],a[168:171]
rt v[44:45],v241 offset:50816
rt v[46:47],v241 offset:51840
mm a[220:223],v[200:203],v[184:187],a[220:223]
s_xor_b32 m0, s82, 37888
v_sub_f32 v99, v99, v70
mm a[220:223],v[204:207],v[188:191],a[220:223]
ld v229,s[40:43],s79 offen lds
mm a[216:219],v[196:199],v[188:191],a[216:219]
v_add_f32 v124, v98, v99
v_fmac_f32 v122, v98, v98
mm a[216:219],v[192:195],v[184:187],a[216:219]
v_add_f32 v120, v120, v124
v_fmac_f32 v123, v99, v99
mm a[200:203],v[192:195],v[0:3],a[200:203]
s_xor_b32 m0, s82, 38912
v_lshlrev_b32 v100, 16, v78
mm a[200:203],v[196:199],v[4:7],a[200:203]
ld v230,s[40:43],s79 offen lds
mm a[204:207],v[204:207],v[4:7],a[204:207]
v_and_b32 v101, 0xffff0000, v78
v_sub_f32 v100, v100, v70
mm a[204:207],v[200:203],v[0:3],a[204:207]
rt v[48:49],v242 offset:33920
rt v[50:51],v242 offset:34944
mm a[184:187],v[208:211],v[0:3],a[184:187]
s_xor_b32 m0, s82, 39936
v_sub_f32 v101, v101, v70
mm a[184:187],v[212:215],v[4:7],a[184:187]
ld v231,s[40:43],s79 offen lds
mm a[188:191],v[220:223],v[4:7],a[188:191]
s_mov_b32 s79, 0
v_add_f32 v124, v100, v101
mm a[188:191],v[216:219],v[0:3],a[188:191]
v_fmac_f32 v122, v100, v100
v_add_f32 v120, v120, v124
mm a[12:15],v[216:219],v[8:11],a[12:15]
v_fmac_f32 v123, v101, v101
v_lshlrev_b32 v102, 16, v79
mm a[12:15],v[220:223],v[12:15],a[12:15]
buffer_load_dword v254, v132, s[44:47], s86 offen
mm a[8:11],v[212:215],v[12:15],a[8:11]
s_add_u32 s86, s86, 2097152
s_and_b32 s86, s86, 4194303
mm a[8:11],v[208:211],v[8:11],a[8:11]
rt v[52:53],v242 offset:50816
rt v[54:55],v242 offset:51840
mm a[76:79],v[200:203],v[8:11],a[76:79]
v_and_b32 v103, 0xffff0000, v79
v_sub_f32 v102, v102, v70
mm a[76:79],v[204:207],v[12:15],a[76:79]
v_sub_f32 v103, v103, v70
v_add_f32 v124, v102, v103
mm a[72:75],v[196:199],v[12:15],a[72:75]
v_fmac_f32 v122, v102, v102
v_add_f32 v120, v120, v124
mm a[72:75],v[192:195],v[8:11],a[72:75]
rt v[56:57],v243 offset:33920
rt v[58:59],v243 offset:34944
mm a[88:91],v[192:195],v[16:19],a[88:91]
v_fmac_f32 v123, v103, v103
v_lshlrev_b32 v104, 16, v80
mm a[88:91],v[196:199],v[20:23],a[88:91]
v_and_b32 v105, 0xffff0000, v80
v_sub_f32 v104, v104, v70
mm a[92:95],v[204:207],v[20:23],a[92:95]
v_sub_f32 v105, v105, v70
v_add_f32 v124, v104, v105
mm a[92:95],v[200:203],v[16:19],a[92:95]
rt v[60:61],v243 offset:50816
rt v[62:63],v243 offset:51840
mm a[24:27],v[208:211],v[16:19],a[24:27]
v_fmac_f32 v122, v104, v104
v_add_f32 v120, v120, v124
mm a[24:27],v[212:215],v[20:23],a[24:27]
v_fmac_f32 v123, v105, v105
v_lshlrev_b32 v106, 16, v81
mm a[28:31],v[220:223],v[20:23],a[28:31]
v_and_b32 v107, 0xffff0000, v81
v_sub_f32 v106, v106, v70
mm a[28:31],v[216:219],v[16:19],a[28:31]
v_sub_f32 v107, v107, v70
v_add_f32 v124, v106, v107
mm a[44:47],v[216:219],v[24:27],a[44:47]
v_fmac_f32 v122, v106, v106
)ASM" R"ASM(v_add_f32 v120, v120, v124
mm a[44:47],v[220:223],v[28:31],a[44:47]
v_fmac_f32 v123, v107, v107
v_lshlrev_b32 v108, 16, v82
mm a[40:43],v[212:215],v[28:31],a[40:43]
v_and_b32 v109, 0xffff0000, v82
v_sub_f32 v108, v108, v70
mm a[40:43],v[208:211],v[24:27],a[40:43]
v_sub_f32 v109, v109, v70
v_add_f32 v124, v108, v109
mm a[252:255],v[200:203],v[24:27],a[252:255]
v_fmac_f32 v122, v108, v108
v_add_f32 v120, v120, v124
mm a[252:255],v[204:207],v[28:31],a[252:255]
v_fmac_f32 v123, v109, v109
v_lshlrev_b32 v110, 16, v83
mm a[248:251],v[196:199],v[28:31],a[248:251]
wt vmcnt(9) lgkmcnt(0)
s_barrier
mm a[248:251],v[192:195],v[24:27],a[248:251]
v_and_b32 v111, 0xffff0000, v83
v_sub_f32 v110, v110, v70
mm a[112:115],v[32:35],v[160:163],a[112:115]
v_sub_f32 v111, v111, v70
v_add_f32 v124, v110, v111
mm a[112:115],v[36:39],v[164:167],a[112:115]
v_fmac_f32 v122, v110, v110
v_add_f32 v120, v120, v124
mm a[116:119],v[44:47],v[164:167],a[116:119]
rt v[192:193],v240 offset:0
rt v[194:195],v240 offset:1024
mm a[116:119],v[40:43],v[160:163],a[116:119]
rt v[196:197],v240 offset:16896
rt v[198:199],v240 offset:17920
mm a[128:131],v[48:51],v[160:163],a[128:131]
v_fmac_f32 v123, v111, v111
v_lshlrev_b32 v112, 16, v84
mm a[128:131],v[52:55],v[164:167],a[128:131]
v_and_b32 v113, 0xffff0000, v84
v_sub_f32 v112, v112, v70
mm a[132:135],v[60:63],v[164:167],a[132:135]
v_sub_f32 v113, v113, v70
v_add_f32 v124, v112, v113
mm a[132:135],v[56:59],v[160:163],a[132:135]
rd v[160:163],v236 offset:24576
rd v[164:167],v237 offset:24576
mm a[148:151],v[56:59],v[168:171],a[148:151]
v_fmac_f32 v122, v112, v112
v_add_f32 v120, v120, v124
mm a[148:151],v[60:63],v[172:175],a[148:151]
v_fmac_f32 v123, v113, v113
v_lshlrev_b32 v114, 16, v85
mm a[144:147],v[52:55],v[172:175],a[144:147]
v_and_b32 v115, 0xffff0000, v85
v_sub_f32 v114, v114, v70
mm a[144:147],v[48:51],v[168:171],a[144:147]
rt v[200:201],v241 offset:0
rt v[202:203],v241 offset:1024
mm a[228:231],v[40:43],v[168:171],a[228:231]
v_sub_f32 v115, v115, v70
v_add_f32 v124, v114, v115
mm a[228:231],v[44:47],v[172:175],a[228:231]
v_fmac_f32 v122, v114, v114
v_add_f32 v120, v120, v124
mm a[224:227],v[36:39],v[172:175],a[224:227]
v_fmac_f32 v123, v115, v115
v_lshlrev_b32 v116, 16, v86
mm a[224:227],v[32:35],v[168:171],a[224:227]
rd v[168:171],v236 offset:26624
rd v[172:175],v237 offset:26624
mm a[208:211],v[32:35],v[176:179],a[208:211]
v_and_b32 v117, 0xffff0000, v86
v_sub_f32 v116, v116, v70
mm a[208:211],v[36:39],v[180:183],a[208:211]
v_sub_f32 v117, v117, v70
v_add_f32 v124, v116, v117
mm a[212:215],v[44:47],v[180:183],a[212:215]
v_fmac_f32 v122, v116, v116
v_add_f32 v120, v120, v124
mm a[212:215],v[40:43],v[176:179],a[212:215]
rt v[204:205],v241 offset:16896
rt v[206:207],v241 offset:17920
mm a[160:163],v[48:51],v[176:179],a[160:163]
v_fmac_f32 v123, v117, v117
v_lshlrev_b32 v118, 16, v87
mm a[160:163],v[52:55],v[180:183],a[160:163]
v_and_b32 v119, 0xffff0000, v87
v_sub_f32 v118, v118, v70
mm a[164:167],v[60:63],v[180:183],a[164:167]
v_sub_f32 v119, v119, v70
v_add_f32 v124, v118, v119
mm a[164:167],v[56:59],v[176:179],a[164:167]
rd v[176:179],v236 offset:16384
rd v[180:183],v237 offset:16384
mm a[180:183],v[56:59],v[184:187],a[180:183]
v_fmac_f32 v122, v118, v118
v_add_f32 v120, v120, v124
mm a[180:183],v[60:63],v[188:191],a[180:183]
v_fmac_f32 v123, v119, v119
v_add_f32 v121, v122, v123
)ASM" R"ASM(mm a[176:179],v[52:55],v[188:191],a[176:179]
v_add_f32_dpp v120, v120, v120 row_ror:8 row_mask:0xf bank_mask:0xf
mm a[176:179],v[48:51],v[184:187],a[176:179]
rt v[208:209],v242 offset:0
rt v[210:211],v242 offset:1024
mm a[196:199],v[40:43],v[184:187],a[196:199]
s_add_u32 m0, s83, 33792
v_add_f32_dpp v121, v121, v121 row_ror:8 row_mask:0xf bank_mask:0xf
mm a[196:199],v[44:47],v[188:191],a[196:199]
ld v232,s[44:47],s80 offen lds
mm a[192:195],v[36:39],v[188:191],a[192:195]
v_mov_b32 v124, v120
v_mov_b32 v125, v121
mm a[192:195],v[32:35],v[184:187],a[192:195]
rd v[184:187],v236 offset:18432
rd v[188:191],v237 offset:18432
mm a[64:67],v[32:35],v[0:3],a[64:67]
s_add_u32 m0, s83, 34816
v_permlane16_swap_b32 v124, v120
mm a[64:67],v[36:39],v[4:7],a[64:67]
ld v233,s[44:47],s80 offen lds
mm a[68:71],v[44:47],v[4:7],a[68:71]
v_permlane16_swap_b32 v125, v121
mm a[68:71],v[40:43],v[0:3],a[68:71]
rt v[212:213],v242 offset:16896
rt v[214:215],v242 offset:17920
mm a[0:3],v[48:51],v[0:3],a[0:3]
s_add_u32 m0, s83, 35968
v_add_f32 v120, v120, v124
mm a[0:3],v[52:55],v[4:7],a[0:3]
ld v234,s[44:47],s80 offen lds
mm a[4:7],v[60:63],v[4:7],a[4:7]
v_add_f32 v121, v121, v125
v_mov_b32 v124, v120
mm a[4:7],v[56:59],v[0:3],a[4:7]
rd v[0:3],v236 offset:8192
rd v[4:7],v237 offset:8192
mm a[20:23],v[56:59],v[8:11],a[20:23]
s_add_u32 m0, s83, 36992
v_mov_b32 v125, v121
mm a[20:23],v[60:63],v[12:15],a[20:23]
ld v235,s[44:47],s80 offen lds
mm a[16:19],v[52:55],v[12:15],a[16:19]
s_mov_b32 s80, 3932160
v_permlane32_swap_b32 v124, v120
mm a[16:19],v[48:51],v[8:11],a[16:19]
rt v[216:217],v243 offset:0
rt v[218:219],v243 offset:1024
mm a[84:87],v[40:43],v[8:11],a[84:87]
s_add_u32 m0, s83, 50688
v_permlane32_swap_b32 v125, v121
mm a[84:87],v[44:47],v[12:15],a[84:87]
ld v232,s[44:47],s80 offen lds
mm a[80:83],v[36:39],v[12:15],a[80:83]
v_add_f32 v120, v120, v124
v_add_f32 v121, v121, v125
mm a[80:83],v[32:35],v[8:11],a[80:83]
rd v[8:11],v236 offset:10240
rd v[12:15],v237 offset:10240
mm a[240:243],v[32:35],v[16:19],a[240:243]
s_add_u32 m0, s83, 51712
ds_write_b64 v127, v[120:121]
mm a[240:243],v[36:39],v[20:23],a[240:243]
ld v233,s[44:47],s80 offen lds
mm a[244:247],v[44:47],v[20:23],a[244:247]
s_nop 0
v_readfirstlane_b32 s92, v252
mm a[244:247],v[40:43],v[16:19],a[244:247]
rt v[220:221],v243 offset:16896
rt v[222:223],v243 offset:17920
mm a[32:35],v[48:51],v[16:19],a[32:35]
s_add_u32 m0, s83, 52864
v_readfirstlane_b32 s93, v253
mm a[32:35],v[52:55],v[20:23],a[32:35]
ld v234,s[44:47],s80 offen lds
mm a[36:39],v[60:63],v[20:23],a[36:39]
s_add_u32 s92, s92, 1
s_addc_u32 s93, s93, 0
mm a[36:39],v[56:59],v[16:19],a[36:39]
rd v[16:19],v236 offset:0
rd v[20:23],v237 offset:0
mm a[60:63],v[56:59],v[24:27],a[60:63]
s_add_u32 m0, s83, 53888
v_mov_b32 v252, s92
mm a[60:63],v[60:63],v[28:31],a[60:63]
ld v235,s[44:47],s80 offen lds
mm a[56:59],v[52:55],v[28:31],a[56:59]
s_mov_b32 s80, 0
v_mov_b32 v253, s93
mm a[56:59],v[48:51],v[24:27],a[56:59]
v_mov_b32 v128, 0
v_mov_b32 v129, 0
mm a[52:55],v[40:43],v[24:27],a[52:55]
v_mov_b32 v130, 0
v_mov_b32 v131, 0
mm a[52:55],v[44:47],v[28:31],a[52:55]
st v[128:131],v255,s[52:55],s95 offen sc1
mm a[48:51],v[36:39],v[28:31],a[48:51]
wt lgkmcnt(2)
s_xor_b32 s93, s72, 0
)ASM" R"ASM(mm a[48:51],v[32:35],v[24:27],a[48:51]
rd v[24:27],v236 offset:2048
rd v[28:31],v237 offset:2048
mm a[96:99],v[192:195],v[160:163],a[96:99]
s_lshl_b32 s92, s76, 5
s_add_u32 s92, s92, s93
mm a[96:99],v[196:199],v[164:167],a[96:99]
s_lshl2_add_u32 s92, s92, s3
s_lshl_b32 s92, s92, 10
mm a[100:103],v[204:207],v[164:167],a[100:103]
s_add_u32 s92, s92, 1048576
v_mov_b32 v128, -1
mm a[100:103],v[200:203],v[160:163],a[100:103]
rt v[32:33],v240 offset:128
rt v[34:35],v240 offset:1152
mm a[104:107],v[208:211],v[160:163],a[104:107]
v_mov_b32 v129, -1
v_mov_b32 v130, -1
mm a[104:107],v[212:215],v[164:167],a[104:107]
v_mov_b32 v131, -1
st v[128:131],v255,s[52:55],s92 offen sc1
mm a[108:111],v[220:223],v[164:167],a[108:111]
wt lgkmcnt(2)
s_barrier
mm a[108:111],v[216:219],v[160:163],a[108:111]
rd v[72:75],v126
rd v[76:79],v126 offset:16
mm a[140:143],v[216:219],v[168:171],a[140:143]
s_xor_b32 m0, s82, 0
mm a[140:143],v[220:223],v[172:175],a[140:143]
ld v224,s[40:43],s79 offen lds
mm a[136:139],v[212:215],v[172:175],a[136:139]
mm a[136:139],v[208:211],v[168:171],a[136:139]
rt v[36:37],v240 offset:17024
rt v[38:39],v240 offset:18048
mm a[124:127],v[200:203],v[168:171],a[124:127]
s_xor_b32 m0, s82, 1024
mm a[124:127],v[204:207],v[172:175],a[124:127]
ld v225,s[40:43],s79 offen lds
mm a[120:123],v[196:199],v[172:175],a[120:123]
wt lgkmcnt(2)
v_add_f32 v72, v72, v74
mm a[120:123],v[192:195],v[168:171],a[120:123]
v_add_f32 v73, v73, v75
v_add_f32 v76, v76, v78
mm a[232:235],v[192:195],v[176:179],a[232:235]
s_xor_b32 m0, s82, 2048
v_add_f32 v77, v77, v79
mm a[232:235],v[196:199],v[180:183],a[232:235]
ld v226,s[40:43],s79 offen lds
mm a[236:239],v[204:207],v[180:183],a[236:239]
v_add_f32 v72, v72, v76
v_add_f32 v73, v73, v77
mm a[236:239],v[200:203],v[176:179],a[236:239]
rt v[40:41],v241 offset:128
rt v[42:43],v241 offset:1152
mm a[152:155],v[208:211],v[176:179],a[152:155]
s_xor_b32 m0, s82, 3072
v_mul_f32 v72, 0x3a800000, v72
mm a[152:155],v[212:215],v[180:183],a[152:155]
ld v227,s[40:43],s79 offen lds
mm a[156:159],v[220:223],v[180:183],a[156:159]
v_mul_f32 v73, 0x3a800000, v73
v_add_f32 v124, v70, v72
mm a[156:159],v[216:219],v[176:179],a[156:159]
v_fma_f32 v125, -v72, v72, v73
cv v124,v124,v124
mm a[172:175],v[216:219],v[184:187],a[172:175]
s_xor_b32 m0, s82, 4096
v_lshlrev_b32 v124, 16, v124
mm a[172:175],v[220:223],v[188:191],a[172:175]
ld v228,s[40:43],s79 offen lds
mm a[168:171],v[212:215],v[188:191],a[168:171]
v_max_f32 v125, 0, v125
cv v125,v125,v125
mm a[168:171],v[208:211],v[184:187],a[168:171]
rt v[44:45],v241 offset:17024
rt v[46:47],v241 offset:18048
mm a[220:223],v[200:203],v[184:187],a[220:223]
s_xor_b32 m0, s82, 5120
v_lshlrev_b32 v125, 16, v125
mm a[220:223],v[204:207],v[188:191],a[220:223]
ld v229,s[40:43],s79 offen lds
mm a[216:219],v[196:199],v[188:191],a[216:219]
v_add_f32 v125, 0x3727c5ac, v125
cv v125,v125,v125
mm a[216:219],v[192:195],v[184:187],a[216:219]
v_lshlrev_b32 v125, 16, v125
mm a[200:203],v[192:195],v[0:3],a[200:203]
s_xor_b32 m0, s82, 6144
mm a[200:203],v[196:199],v[4:7],a[200:203]
ld v230,s[40:43],s79 offen lds
mm a[204:207],v[204:207],v[4:7],a[204:207]
v_sqrt_f32 v125, v125
mm a[204:207],v[200:203],v[0:3],a[204:207]
rt v[48:49],v242 offset:128
rt v[50:51],v242 offset:1152
mm a[184:187],v[208:211],v[0:3],a[184:187]
s_xor_b32 m0, s82, 7168
v_sub_f32 v124, v70, v124
)ASM" R"ASM(mm a[184:187],v[212:215],v[4:7],a[184:187]
ld v231,s[40:43],s79 offen lds
mm a[188:191],v[220:223],v[4:7],a[188:191]
s_mov_b32 s79, 128
cv v125,v125,v125
mm a[188:191],v[216:219],v[0:3],a[188:191]
v_lshlrev_b32 v125, 16, v125
mm a[12:15],v[216:219],v[8:11],a[12:15]
v_rcp_f32 v125, v125
mm a[12:15],v[220:223],v[12:15],a[12:15]
v_mul_f32 v125, 0x3e0293ee, v125
v_mul_f32 v124, v124, v125
mm a[8:11],v[212:215],v[12:15],a[8:11]
v_fma_f32 v88, v88, v125, v124
v_fma_f32 v89, v89, v125, v124
mm a[8:11],v[208:211],v[8:11],a[8:11]
rt v[52:53],v242 offset:17024
rt v[54:55],v242 offset:18048
mm a[76:79],v[200:203],v[8:11],a[76:79]
cv v72,v88,v89
v_fma_f32 v90, v90, v125, v124
mm a[76:79],v[204:207],v[12:15],a[76:79]
v_fma_f32 v91, v91, v125, v124
cv v73,v90,v91
mm a[72:75],v[196:199],v[12:15],a[72:75]
v_fma_f32 v92, v92, v125, v124
v_fma_f32 v93, v93, v125, v124
mm a[72:75],v[192:195],v[8:11],a[72:75]
rt v[56:57],v243 offset:128
rt v[58:59],v243 offset:1152
mm a[88:91],v[192:195],v[16:19],a[88:91]
cv v74,v92,v93
v_fma_f32 v94, v94, v125, v124
mm a[88:91],v[196:199],v[20:23],a[88:91]
v_fma_f32 v95, v95, v125, v124
cv v75,v94,v95
mm a[92:95],v[204:207],v[20:23],a[92:95]
st v[72:75],v71,s[56:59],0 offen offset:0 sc1
mm a[92:95],v[200:203],v[16:19],a[92:95]
rt v[60:61],v243 offset:17024
rt v[62:63],v243 offset:18048
mm a[24:27],v[208:211],v[16:19],a[24:27]
v_fma_f32 v96, v96, v125, v124
v_fma_f32 v97, v97, v125, v124
mm a[24:27],v[212:215],v[20:23],a[24:27]
cv v76,v96,v97
v_fma_f32 v98, v98, v125, v124
mm a[28:31],v[220:223],v[20:23],a[28:31]
v_fma_f32 v99, v99, v125, v124
cv v77,v98,v99
mm a[28:31],v[216:219],v[16:19],a[28:31]
v_fma_f32 v100, v100, v125, v124
v_fma_f32 v101, v101, v125, v124
mm a[44:47],v[216:219],v[24:27],a[44:47]
cv v78,v100,v101
v_fma_f32 v102, v102, v125, v124
mm a[44:47],v[220:223],v[28:31],a[44:47]
v_fma_f32 v103, v103, v125, v124
cv v79,v102,v103
mm a[40:43],v[212:215],v[28:31],a[40:43]
st v[76:79],v71,s[56:59],0 offen offset:2048 sc1
mm a[40:43],v[208:211],v[24:27],a[40:43]
v_fma_f32 v104, v104, v125, v124
v_fma_f32 v105, v105, v125, v124
mm a[252:255],v[200:203],v[24:27],a[252:255]
cv v80,v104,v105
v_fma_f32 v106, v106, v125, v124
mm a[252:255],v[204:207],v[28:31],a[252:255]
v_fma_f32 v107, v107, v125, v124
cv v81,v106,v107
mm a[248:251],v[196:199],v[28:31],a[248:251]
wt vmcnt(12) lgkmcnt(0)
s_barrier
mm a[248:251],v[192:195],v[24:27],a[248:251]
v_fma_f32 v108, v108, v125, v124
v_fma_f32 v109, v109, v125, v124
mm a[112:115],v[32:35],v[160:163],a[112:115]
cv v82,v108,v109
v_fma_f32 v110, v110, v125, v124
mm a[112:115],v[36:39],v[164:167],a[112:115]
v_fma_f32 v111, v111, v125, v124
cv v83,v110,v111
mm a[116:119],v[44:47],v[164:167],a[116:119]
rt v[192:193],v240 offset:33792
rt v[194:195],v240 offset:34816
mm a[116:119],v[40:43],v[160:163],a[116:119]
rt v[196:197],v240 offset:50688
rt v[198:199],v240 offset:51712
mm a[128:131],v[48:51],v[160:163],a[128:131]
st v[80:83],v71,s[56:59],s88 offen offset:0 sc1
mm a[128:131],v[52:55],v[164:167],a[128:131]
v_fma_f32 v112, v112, v125, v124
v_fma_f32 v113, v113, v125, v124
mm a[132:135],v[60:63],v[164:167],a[132:135]
cv v84,v112,v113
v_fma_f32 v114, v114, v125, v124
mm a[132:135],v[56:59],v[160:163],a[132:135]
rd v[160:163],v236 offset:57344
rd v[164:167],v237 offset:57344
mm a[148:151],v[56:59],v[168:171],a[148:151]
)ASM" R"ASM(v_fma_f32 v115, v115, v125, v124
cv v85,v114,v115
mm a[148:151],v[60:63],v[172:175],a[148:151]
v_fma_f32 v116, v116, v125, v124
v_fma_f32 v117, v117, v125, v124
mm a[144:147],v[52:55],v[172:175],a[144:147]
cv v86,v116,v117
v_fma_f32 v118, v118, v125, v124
mm a[144:147],v[48:51],v[168:171],a[144:147]
rt v[200:201],v241 offset:33792
rt v[202:203],v241 offset:34816
mm a[228:231],v[40:43],v[168:171],a[228:231]
v_fma_f32 v119, v119, v125, v124
cv v87,v118,v119
mm a[228:231],v[44:47],v[172:175],a[228:231]
st v[84:87],v71,s[56:59],s88 offen offset:2048 sc1
mm a[224:227],v[36:39],v[172:175],a[224:227]
mm a[224:227],v[32:35],v[168:171],a[224:227]
rd v[168:171],v236 offset:59392
rd v[172:175],v237 offset:59392
mm a[208:211],v[32:35],v[176:179],a[208:211]
mm a[208:211],v[36:39],v[180:183],a[208:211]
mm a[212:215],v[44:47],v[180:183],a[212:215]
mm a[212:215],v[40:43],v[176:179],a[212:215]
rt v[204:205],v241 offset:50688
rt v[206:207],v241 offset:51712
mm a[160:163],v[48:51],v[176:179],a[160:163]
mm a[160:163],v[52:55],v[180:183],a[160:163]
mm a[164:167],v[60:63],v[180:183],a[164:167]
mm a[164:167],v[56:59],v[176:179],a[164:167]
rd v[176:179],v236 offset:49152
rd v[180:183],v237 offset:49152
mm a[180:183],v[56:59],v[184:187],a[180:183]
mm a[180:183],v[60:63],v[188:191],a[180:183]
.La5_LW1_1:
s_cbranch_execz .La5_LW1_5
mm a[176:179],v[52:55],v[188:191],a[176:179]
mm a[176:179],v[48:51],v[184:187],a[176:179]
rt v[208:209],v242 offset:33792
rt v[210:211],v242 offset:34816
mm a[196:199],v[40:43],v[184:187],a[196:199]
s_add_u32 m0, s83, 0
mm a[196:199],v[44:47],v[188:191],a[196:199]
ld v232,s[44:47],s80 offen lds
mm a[192:195],v[36:39],v[188:191],a[192:195]
mm a[192:195],v[32:35],v[184:187],a[192:195]
rd v[184:187],v236 offset:51200
rd v[188:191],v237 offset:51200
mm a[64:67],v[32:35],v[0:3],a[64:67]
s_add_u32 m0, s83, 1024
mm a[64:67],v[36:39],v[4:7],a[64:67]
ld v233,s[44:47],s80 offen lds
mm a[68:71],v[44:47],v[4:7],a[68:71]
mm a[68:71],v[40:43],v[0:3],a[68:71]
rt v[212:213],v242 offset:50688
rt v[214:215],v242 offset:51712
mm a[0:3],v[48:51],v[0:3],a[0:3]
s_add_u32 m0, s83, 2176
mm a[0:3],v[52:55],v[4:7],a[0:3]
ld v234,s[44:47],s80 offen lds
mm a[4:7],v[60:63],v[4:7],a[4:7]
mm a[4:7],v[56:59],v[0:3],a[4:7]
rd v[0:3],v236 offset:40960
rd v[4:7],v237 offset:40960
mm a[20:23],v[56:59],v[8:11],a[20:23]
s_add_u32 m0, s83, 3200
mm a[20:23],v[60:63],v[12:15],a[20:23]
ld v235,s[44:47],s80 offen lds
mm a[16:19],v[52:55],v[12:15],a[16:19]
s_mov_b32 s80, 262144
mm a[16:19],v[48:51],v[8:11],a[16:19]
rt v[216:217],v243 offset:33792
rt v[218:219],v243 offset:34816
mm a[84:87],v[40:43],v[8:11],a[84:87]
s_add_u32 m0, s83, 16896
mm a[84:87],v[44:47],v[12:15],a[84:87]
ld v232,s[44:47],s80 offen lds
mm a[80:83],v[36:39],v[12:15],a[80:83]
mm a[80:83],v[32:35],v[8:11],a[80:83]
rd v[8:11],v236 offset:43008
rd v[12:15],v237 offset:43008
mm a[240:243],v[32:35],v[16:19],a[240:243]
s_add_u32 m0, s83, 17920
mm a[240:243],v[36:39],v[20:23],a[240:243]
ld v233,s[44:47],s80 offen lds
mm a[244:247],v[44:47],v[20:23],a[244:247]
mm a[244:247],v[40:43],v[16:19],a[244:247]
rt v[220:221],v243 offset:50688
rt v[222:223],v243 offset:51712
mm a[32:35],v[48:51],v[16:19],a[32:35]
s_add_u32 m0, s83, 19072
mm a[32:35],v[52:55],v[20:23],a[32:35]
ld v234,s[44:47],s80 offen lds
mm a[36:39],v[60:63],v[20:23],a[36:39]
mm a[36:39],v[56:59],v[16:19],a[36:39]
rd v[16:19],v236 offset:32768
rd v[20:23],v237 offset:32768
mm a[60:63],v[56:59],v[24:27],a[60:63]
s_add_u32 m0, s83, 20096
mm a[60:63],v[60:63],v[28:31],a[60:63]
)ASM" R"ASM(ld v235,s[44:47],s80 offen lds
mm a[56:59],v[52:55],v[28:31],a[56:59]
s_mov_b32 s80, 524288
mm a[56:59],v[48:51],v[24:27],a[56:59]
mm a[52:55],v[40:43],v[24:27],a[52:55]
mm a[52:55],v[44:47],v[28:31],a[52:55]
mm a[48:51],v[36:39],v[28:31],a[48:51]
wt lgkmcnt(2)
mm a[48:51],v[32:35],v[24:27],a[48:51]
rd v[24:27],v236 offset:34816
rd v[28:31],v237 offset:34816
mm a[96:99],v[192:195],v[160:163],a[96:99]
mm a[96:99],v[196:199],v[164:167],a[96:99]
mm a[100:103],v[204:207],v[164:167],a[100:103]
mm a[100:103],v[200:203],v[160:163],a[100:103]
rt v[32:33],v240 offset:33920
rt v[34:35],v240 offset:34944
mm a[104:107],v[208:211],v[160:163],a[104:107]
mm a[104:107],v[212:215],v[164:167],a[104:107]
mm a[108:111],v[220:223],v[164:167],a[108:111]
wt lgkmcnt(2)
s_barrier
mm a[108:111],v[216:219],v[160:163],a[108:111]
mm a[140:143],v[216:219],v[168:171],a[140:143]
s_xor_b32 m0, s82, 32768
mm a[140:143],v[220:223],v[172:175],a[140:143]
ld v224,s[40:43],s79 offen lds
mm a[136:139],v[212:215],v[172:175],a[136:139]
mm a[136:139],v[208:211],v[168:171],a[136:139]
rt v[36:37],v240 offset:50816
rt v[38:39],v240 offset:51840
mm a[124:127],v[200:203],v[168:171],a[124:127]
s_xor_b32 m0, s82, 33792
mm a[124:127],v[204:207],v[172:175],a[124:127]
ld v225,s[40:43],s79 offen lds
mm a[120:123],v[196:199],v[172:175],a[120:123]
mm a[120:123],v[192:195],v[168:171],a[120:123]
mm a[232:235],v[192:195],v[176:179],a[232:235]
s_xor_b32 m0, s82, 34816
mm a[232:235],v[196:199],v[180:183],a[232:235]
ld v226,s[40:43],s79 offen lds
mm a[236:239],v[204:207],v[180:183],a[236:239]
mm a[236:239],v[200:203],v[176:179],a[236:239]
rt v[40:41],v241 offset:33920
rt v[42:43],v241 offset:34944
mm a[152:155],v[208:211],v[176:179],a[152:155]
s_xor_b32 m0, s82, 35840
mm a[152:155],v[212:215],v[180:183],a[152:155]
ld v227,s[40:43],s79 offen lds
mm a[156:159],v[220:223],v[180:183],a[156:159]
mm a[156:159],v[216:219],v[176:179],a[156:159]
mm a[172:175],v[216:219],v[184:187],a[172:175]
s_xor_b32 m0, s82, 36864
mm a[172:175],v[220:223],v[188:191],a[172:175]
ld v228,s[40:43],s79 offen lds
mm a[168:171],v[212:215],v[188:191],a[168:171]
mm a[168:171],v[208:211],v[184:187],a[168:171]
rt v[44:45],v241 offset:50816
rt v[46:47],v241 offset:51840
mm a[220:223],v[200:203],v[184:187],a[220:223]
s_xor_b32 m0, s82, 37888
mm a[220:223],v[204:207],v[188:191],a[220:223]
ld v229,s[40:43],s79 offen lds
mm a[216:219],v[196:199],v[188:191],a[216:219]
mm a[216:219],v[192:195],v[184:187],a[216:219]
mm a[200:203],v[192:195],v[0:3],a[200:203]
s_xor_b32 m0, s82, 38912
mm a[200:203],v[196:199],v[4:7],a[200:203]
ld v230,s[40:43],s79 offen lds
mm a[204:207],v[204:207],v[4:7],a[204:207]
mm a[204:207],v[200:203],v[0:3],a[204:207]
rt v[48:49],v242 offset:33920
rt v[50:51],v242 offset:34944
mm a[184:187],v[208:211],v[0:3],a[184:187]
s_xor_b32 m0, s82, 39936
mm a[184:187],v[212:215],v[4:7],a[184:187]
ld v231,s[40:43],s79 offen lds
mm a[188:191],v[220:223],v[4:7],a[188:191]
s_mov_b32 s79, 256
mm a[188:191],v[216:219],v[0:3],a[188:191]
mm a[12:15],v[216:219],v[8:11],a[12:15]
mm a[12:15],v[220:223],v[12:15],a[12:15]
buffer_load_dword v254, v135, s[68:71], s96 offen
mm a[8:11],v[212:215],v[12:15],a[8:11]
s_add_u32 s96, s96, 1048576
mm a[8:11],v[208:211],v[8:11],a[8:11]
rt v[52:53],v242 offset:50816
rt v[54:55],v242 offset:51840
mm a[76:79],v[200:203],v[8:11],a[76:79]
mm a[76:79],v[204:207],v[12:15],a[76:79]
buffer_load_dword v254, v135, s[68:71], s96 offen
mm a[72:75],v[196:199],v[12:15],a[72:75]
s_add_u32 s96, s96, 1048576
mm a[72:75],v[192:195],v[8:11],a[72:75]
rt v[56:57],v243 offset:33920
rt v[58:59],v243 offset:34944
mm a[88:91],v[192:195],v[16:19],a[88:91]
mm a[88:91],v[196:199],v[20:23],a[88:91]
wt vmcnt(18)
buffer_store_dwordx2 v[252:253], off, s[64:67], s91 offset:1024 sc1
)ASM" R"ASM(mm a[92:95],v[204:207],v[20:23],a[92:95]
s_mov_b32 s91, 0
s_lshl_b32 s92, s76, 19
mm a[92:95],v[200:203],v[16:19],a[92:95]
rt v[60:61],v243 offset:50816
rt v[62:63],v243 offset:51840
mm a[24:27],v[208:211],v[16:19],a[24:27]
s_add_u32 s92, s92, 88080384
mm a[24:27],v[212:215],v[20:23],a[24:27]
s_add_u32 s52, s26, s92
s_addc_u32 s53, s27, 0
mm a[28:31],v[220:223],v[20:23],a[28:31]
s_and_b32 s53, s53, 0xffff
s_mov_b32 s54, 524672
mm a[28:31],v[216:219],v[16:19],a[28:31]
s_mov_b32 s55, 0x20000
s_lshl_b32 s92, s3, 16
mm a[44:47],v[216:219],v[24:27],a[44:47]
s_sub_u32 s93, 458752, s92
v_add_u32 v244, s92, v136
mm a[44:47],v[220:223],v[28:31],a[44:47]
v_add_u32 v246, s93, v136
v_add_u32 v245, 0x8000, v244
mm a[40:43],v[212:215],v[28:31],a[40:43]
v_add_u32 v247, 0x8000, v246
s_lshl_b32 s94, s72, 19
mm a[40:43],v[208:211],v[24:27],a[40:43]
s_lshl_b32 s93, s76, 16
s_add_u32 s92, s94, s93
mm a[252:255],v[200:203],v[24:27],a[252:255]
s_lshl_b32 s93, s77, 13
s_add_u32 s92, s92, s93
mm a[252:255],v[204:207],v[28:31],a[252:255]
s_add_u32 s92, s92, 20971520
mm a[248:251],v[196:199],v[28:31],a[248:251]
wt vmcnt(11) lgkmcnt(0)
s_barrier
mm a[248:251],v[192:195],v[24:27],a[248:251]
s_add_u32 s48, s26, s92
s_addc_u32 s49, s27, 0
mm a[112:115],v[32:35],v[160:163],a[112:115]
s_and_b32 s49, s49, 0xffff
s_mov_b32 s50, 57344
mm a[112:115],v[36:39],v[164:167],a[112:115]
s_mov_b32 s51, 0x20000
s_lshl_b32 s93, s77, 24
mm a[116:119],v[44:47],v[164:167],a[116:119]
rt v[192:193],v240 offset:0
rt v[194:195],v240 offset:1024
mm a[116:119],v[40:43],v[160:163],a[116:119]
rt v[196:197],v240 offset:16896
rt v[198:199],v240 offset:17920
mm a[128:131],v[48:51],v[160:163],a[128:131]
s_add_u32 s92, s94, s93
s_add_u32 s92, s92, 20971520
mm a[128:131],v[52:55],v[164:167],a[128:131]
s_add_u32 s60, s26, s92
s_addc_u32 s61, s27, 0
mm a[132:135],v[60:63],v[164:167],a[132:135]
s_and_b32 s61, s61, 0xffff
s_mov_b32 s62, 524288
mm a[132:135],v[56:59],v[160:163],a[132:135]
rd v[160:163],v236 offset:24576
rd v[164:167],v237 offset:24576
mm a[148:151],v[56:59],v[168:171],a[148:151]
s_mov_b32 s63, 0x20000
s_lshl_b32 s92, s77, 14
mm a[148:151],v[60:63],v[172:175],a[148:151]
s_sub_u32 s95, 16384, s92
v_add_u32 v238, s95, v255
mm a[144:147],v[52:55],v[172:175],a[144:147]
s_lshr_b32 s100, s2, 6
s_lshl_b32 s100, s100, 7
mm a[144:147],v[48:51],v[168:171],a[144:147]
rt v[200:201],v241 offset:0
rt v[202:203],v241 offset:1024
mm a[228:231],v[40:43],v[168:171],a[228:231]
s_and_b32 s92, s2, 63
s_add_u32 s100, s100, s92
mm a[228:231],v[44:47],v[172:175],a[228:231]
s_lshl_b32 s100, s100, 7
s_lshl_b32 s92, s3, 28
mm a[224:227],v[36:39],v[172:175],a[224:227]
s_add_u32 s100, s100, s92
s_add_u32 s100, s100, 2621440
mm a[224:227],v[32:35],v[168:171],a[224:227]
rd v[168:171],v236 offset:26624
rd v[172:175],v237 offset:26624
mm a[208:211],v[32:35],v[176:179],a[208:211]
s_add_u32 s101, s100, 8192
s_lshl1_add_u32 s92, s3, 157440
mm a[208:211],v[36:39],v[180:183],a[208:211]
v_mov_b32 v76, s92
mm a[212:215],v[44:47],v[180:183],a[212:215]
mm a[212:215],v[40:43],v[176:179],a[212:215]
rt v[204:205],v241 offset:16896
rt v[206:207],v241 offset:17920
mm a[160:163],v[48:51],v[176:179],a[160:163]
mm a[160:163],v[52:55],v[180:183],a[160:163]
mm a[164:167],v[60:63],v[180:183],a[164:167]
)ASM" R"ASM(mm a[164:167],v[56:59],v[176:179],a[164:167]
rd v[176:179],v236 offset:16384
rd v[180:183],v237 offset:16384
mm a[180:183],v[56:59],v[184:187],a[180:183]
mm a[180:183],v[60:63],v[188:191],a[180:183]
mm a[176:179],v[52:55],v[188:191],a[176:179]
mm a[176:179],v[48:51],v[184:187],a[176:179]
rt v[208:209],v242 offset:0
rt v[210:211],v242 offset:1024
mm a[196:199],v[40:43],v[184:187],a[196:199]
s_add_u32 m0, s83, 33792
mm a[196:199],v[44:47],v[188:191],a[196:199]
ld v232,s[44:47],s80 offen lds
mm a[192:195],v[36:39],v[188:191],a[192:195]
mm a[192:195],v[32:35],v[184:187],a[192:195]
rd v[184:187],v236 offset:18432
rd v[188:191],v237 offset:18432
mm a[64:67],v[32:35],v[0:3],a[64:67]
s_add_u32 m0, s83, 34816
mm a[64:67],v[36:39],v[4:7],a[64:67]
ld v233,s[44:47],s80 offen lds
mm a[68:71],v[44:47],v[4:7],a[68:71]
mm a[68:71],v[40:43],v[0:3],a[68:71]
rt v[212:213],v242 offset:16896
rt v[214:215],v242 offset:17920
mm a[0:3],v[48:51],v[0:3],a[0:3]
s_add_u32 m0, s83, 35968
mm a[0:3],v[52:55],v[4:7],a[0:3]
ld v234,s[44:47],s80 offen lds
mm a[4:7],v[60:63],v[4:7],a[4:7]
mm a[4:7],v[56:59],v[0:3],a[4:7]
rd v[0:3],v236 offset:8192
rd v[4:7],v237 offset:8192
mm a[20:23],v[56:59],v[8:11],a[20:23]
s_add_u32 m0, s83, 36992
mm a[20:23],v[60:63],v[12:15],a[20:23]
ld v235,s[44:47],s80 offen lds
mm a[16:19],v[52:55],v[12:15],a[16:19]
s_mov_b32 s80, 786432
mm a[16:19],v[48:51],v[8:11],a[16:19]
rt v[216:217],v243 offset:0
rt v[218:219],v243 offset:1024
mm a[84:87],v[40:43],v[8:11],a[84:87]
s_add_u32 m0, s83, 50688
mm a[84:87],v[44:47],v[12:15],a[84:87]
ld v232,s[44:47],s80 offen lds
mm a[80:83],v[36:39],v[12:15],a[80:83]
mm a[80:83],v[32:35],v[8:11],a[80:83]
rd v[8:11],v236 offset:10240
rd v[12:15],v237 offset:10240
mm a[240:243],v[32:35],v[16:19],a[240:243]
s_add_u32 m0, s83, 51712
mm a[240:243],v[36:39],v[20:23],a[240:243]
ld v233,s[44:47],s80 offen lds
mm a[244:247],v[44:47],v[20:23],a[244:247]
mm a[244:247],v[40:43],v[16:19],a[244:247]
rt v[220:221],v243 offset:16896
rt v[222:223],v243 offset:17920
mm a[32:35],v[48:51],v[16:19],a[32:35]
s_add_u32 m0, s83, 52864
mm a[32:35],v[52:55],v[20:23],a[32:35]
ld v234,s[44:47],s80 offen lds
mm a[36:39],v[60:63],v[20:23],a[36:39]
mm a[36:39],v[56:59],v[16:19],a[36:39]
rd v[16:19],v236 offset:0
rd v[20:23],v237 offset:0
mm a[60:63],v[56:59],v[24:27],a[60:63]
s_add_u32 m0, s83, 53888
mm a[60:63],v[60:63],v[28:31],a[60:63]
ld v235,s[44:47],s80 offen lds
mm a[56:59],v[52:55],v[28:31],a[56:59]
s_mov_b32 s80, 1048576
mm a[56:59],v[48:51],v[24:27],a[56:59]
mm a[52:55],v[40:43],v[24:27],a[52:55]
mm a[52:55],v[44:47],v[28:31],a[52:55]
mm a[48:51],v[36:39],v[28:31],a[48:51]
wt lgkmcnt(2)
mm a[48:51],v[32:35],v[24:27],a[48:51]
rd v[24:27],v236 offset:2048
rd v[28:31],v237 offset:2048
mm a[96:99],v[192:195],v[160:163],a[96:99]
mm a[96:99],v[196:199],v[164:167],a[96:99]
mm a[100:103],v[204:207],v[164:167],a[100:103]
mm a[100:103],v[200:203],v[160:163],a[100:103]
rt v[32:33],v240 offset:128
rt v[34:35],v240 offset:1152
mm a[104:107],v[208:211],v[160:163],a[104:107]
mm a[104:107],v[212:215],v[164:167],a[104:107]
mm a[108:111],v[220:223],v[164:167],a[108:111]
wt lgkmcnt(2)
s_barrier
mm a[108:111],v[216:219],v[160:163],a[108:111]
mm a[140:143],v[216:219],v[168:171],a[140:143]
s_xor_b32 m0, s82, 0
mm a[140:143],v[220:223],v[172:175],a[140:143]
ld v224,s[40:43],s79 offen lds
mm a[136:139],v[212:215],v[172:175],a[136:139]
mm a[136:139],v[208:211],v[168:171],a[136:139]
rt v[36:37],v240 offset:17024
rt v[38:39],v240 offset:18048
)ASM" R"ASM(mm a[124:127],v[200:203],v[168:171],a[124:127]
s_xor_b32 m0, s82, 1024
mm a[124:127],v[204:207],v[172:175],a[124:127]
ld v225,s[40:43],s79 offen lds
mm a[120:123],v[196:199],v[172:175],a[120:123]
mm a[120:123],v[192:195],v[168:171],a[120:123]
mm a[232:235],v[192:195],v[176:179],a[232:235]
s_xor_b32 m0, s82, 2048
mm a[232:235],v[196:199],v[180:183],a[232:235]
ld v226,s[40:43],s79 offen lds
mm a[236:239],v[204:207],v[180:183],a[236:239]
mm a[236:239],v[200:203],v[176:179],a[236:239]
rt v[40:41],v241 offset:128
rt v[42:43],v241 offset:1152
mm a[152:155],v[208:211],v[176:179],a[152:155]
s_xor_b32 m0, s82, 3072
mm a[152:155],v[212:215],v[180:183],a[152:155]
ld v227,s[40:43],s79 offen lds
mm a[156:159],v[220:223],v[180:183],a[156:159]
mm a[156:159],v[216:219],v[176:179],a[156:159]
mm a[172:175],v[216:219],v[184:187],a[172:175]
s_xor_b32 m0, s82, 4096
mm a[172:175],v[220:223],v[188:191],a[172:175]
ld v228,s[40:43],s79 offen lds
mm a[168:171],v[212:215],v[188:191],a[168:171]
mm a[168:171],v[208:211],v[184:187],a[168:171]
rt v[44:45],v241 offset:17024
rt v[46:47],v241 offset:18048
mm a[220:223],v[200:203],v[184:187],a[220:223]
s_xor_b32 m0, s82, 5120
mm a[220:223],v[204:207],v[188:191],a[220:223]
ld v229,s[40:43],s79 offen lds
mm a[216:219],v[196:199],v[188:191],a[216:219]
mm a[216:219],v[192:195],v[184:187],a[216:219]
mm a[200:203],v[192:195],v[0:3],a[200:203]
s_xor_b32 m0, s82, 6144
mm a[200:203],v[196:199],v[4:7],a[200:203]
ld v230,s[40:43],s79 offen lds
mm a[204:207],v[204:207],v[4:7],a[204:207]
mm a[204:207],v[200:203],v[0:3],a[204:207]
rt v[48:49],v242 offset:128
rt v[50:51],v242 offset:1152
mm a[184:187],v[208:211],v[0:3],a[184:187]
s_xor_b32 m0, s82, 7168
mm a[184:187],v[212:215],v[4:7],a[184:187]
ld v231,s[40:43],s79 offen lds
mm a[188:191],v[220:223],v[4:7],a[188:191]
s_mov_b32 s79, 384
mm a[188:191],v[216:219],v[0:3],a[188:191]
mm a[12:15],v[216:219],v[8:11],a[12:15]
mm a[12:15],v[220:223],v[12:15],a[12:15]
buffer_load_dword v254, v135, s[68:71], s96 offen
mm a[8:11],v[212:215],v[12:15],a[8:11]
s_add_u32 s96, s96, 1048576
mm a[8:11],v[208:211],v[8:11],a[8:11]
rt v[52:53],v242 offset:17024
rt v[54:55],v242 offset:18048
mm a[76:79],v[200:203],v[8:11],a[76:79]
mm a[76:79],v[204:207],v[12:15],a[76:79]
buffer_load_dword v254, v135, s[68:71], s96 offen
mm a[72:75],v[196:199],v[12:15],a[72:75]
s_add_u32 s96, s96, 1048576
mm a[72:75],v[192:195],v[8:11],a[72:75]
rt v[56:57],v243 offset:128
rt v[58:59],v243 offset:1152
mm a[88:91],v[192:195],v[16:19],a[88:91]
mm a[88:91],v[196:199],v[20:23],a[88:91]
mm a[92:95],v[204:207],v[20:23],a[92:95]
mm a[92:95],v[200:203],v[16:19],a[92:95]
rt v[60:61],v243 offset:17024
rt v[62:63],v243 offset:18048
mm a[24:27],v[208:211],v[16:19],a[24:27]
mm a[24:27],v[212:215],v[20:23],a[24:27]
mm a[28:31],v[220:223],v[20:23],a[28:31]
mm a[28:31],v[216:219],v[16:19],a[28:31]
mm a[44:47],v[216:219],v[24:27],a[44:47]
mm a[44:47],v[220:223],v[28:31],a[44:47]
mm a[40:43],v[212:215],v[28:31],a[40:43]
mm a[40:43],v[208:211],v[24:27],a[40:43]
mm a[252:255],v[200:203],v[24:27],a[252:255]
mm a[252:255],v[204:207],v[28:31],a[252:255]
mm a[248:251],v[196:199],v[28:31],a[248:251]
wt vmcnt(10) lgkmcnt(0)
s_barrier
mm a[248:251],v[192:195],v[24:27],a[248:251]
mm a[112:115],v[32:35],v[160:163],a[112:115]
mm a[112:115],v[36:39],v[164:167],a[112:115]
mm a[116:119],v[44:47],v[164:167],a[116:119]
rt v[192:193],v240 offset:33792
rt v[194:195],v240 offset:34816
mm a[116:119],v[40:43],v[160:163],a[116:119]
rt v[196:197],v240 offset:50688
rt v[198:199],v240 offset:51712
mm a[128:131],v[48:51],v[160:163],a[128:131]
mm a[128:131],v[52:55],v[164:167],a[128:131]
mm a[132:135],v[60:63],v[164:167],a[132:135]
mm a[132:135],v[56:59],v[160:163],a[132:135]
rd v[160:163],v236 offset:57344
rd v[164:167],v237 offset:57344
mm a[148:151],v[56:59],v[168:171],a[148:151]
)ASM" R"ASM(mm a[148:151],v[60:63],v[172:175],a[148:151]
mm a[144:147],v[52:55],v[172:175],a[144:147]
mm a[144:147],v[48:51],v[168:171],a[144:147]
rt v[200:201],v241 offset:33792
rt v[202:203],v241 offset:34816
mm a[228:231],v[40:43],v[168:171],a[228:231]
mm a[228:231],v[44:47],v[172:175],a[228:231]
mm a[224:227],v[36:39],v[172:175],a[224:227]
mm a[224:227],v[32:35],v[168:171],a[224:227]
rd v[168:171],v236 offset:59392
rd v[172:175],v237 offset:59392
mm a[208:211],v[32:35],v[176:179],a[208:211]
mm a[208:211],v[36:39],v[180:183],a[208:211]
mm a[212:215],v[44:47],v[180:183],a[212:215]
mm a[212:215],v[40:43],v[176:179],a[212:215]
rt v[204:205],v241 offset:50688
rt v[206:207],v241 offset:51712
mm a[160:163],v[48:51],v[176:179],a[160:163]
mm a[160:163],v[52:55],v[180:183],a[160:163]
mm a[164:167],v[60:63],v[180:183],a[164:167]
mm a[164:167],v[56:59],v[176:179],a[164:167]
rd v[176:179],v236 offset:49152
rd v[180:183],v237 offset:49152
mm a[180:183],v[56:59],v[184:187],a[180:183]
mm a[180:183],v[60:63],v[188:191],a[180:183]
mm a[176:179],v[52:55],v[188:191],a[176:179]
mm a[176:179],v[48:51],v[184:187],a[176:179]
rt v[208:209],v242 offset:33792
rt v[210:211],v242 offset:34816
mm a[196:199],v[40:43],v[184:187],a[196:199]
s_add_u32 m0, s83, 0
mm a[196:199],v[44:47],v[188:191],a[196:199]
ld v232,s[44:47],s80 offen lds
mm a[192:195],v[36:39],v[188:191],a[192:195]
mm a[192:195],v[32:35],v[184:187],a[192:195]
rd v[184:187],v236 offset:51200
rd v[188:191],v237 offset:51200
mm a[64:67],v[32:35],v[0:3],a[64:67]
s_add_u32 m0, s83, 1024
mm a[64:67],v[36:39],v[4:7],a[64:67]
ld v233,s[44:47],s80 offen lds
mm a[68:71],v[44:47],v[4:7],a[68:71]
mm a[68:71],v[40:43],v[0:3],a[68:71]
rt v[212:213],v242 offset:50688
rt v[214:215],v242 offset:51712
mm a[0:3],v[48:51],v[0:3],a[0:3]
s_add_u32 m0, s83, 2176
mm a[0:3],v[52:55],v[4:7],a[0:3]
ld v234,s[44:47],s80 offen lds
mm a[4:7],v[60:63],v[4:7],a[4:7]
mm a[4:7],v[56:59],v[0:3],a[4:7]
rd v[0:3],v236 offset:40960
rd v[4:7],v237 offset:40960
mm a[20:23],v[56:59],v[8:11],a[20:23]
s_add_u32 m0, s83, 3200
mm a[20:23],v[60:63],v[12:15],a[20:23]
ld v235,s[44:47],s80 offen lds
mm a[16:19],v[52:55],v[12:15],a[16:19]
s_mov_b32 s80, 1310720
mm a[16:19],v[48:51],v[8:11],a[16:19]
rt v[216:217],v243 offset:33792
rt v[218:219],v243 offset:34816
mm a[84:87],v[40:43],v[8:11],a[84:87]
s_add_u32 m0, s83, 16896
mm a[84:87],v[44:47],v[12:15],a[84:87]
ld v232,s[44:47],s80 offen lds
mm a[80:83],v[36:39],v[12:15],a[80:83]
mm a[80:83],v[32:35],v[8:11],a[80:83]
rd v[8:11],v236 offset:43008
.La5_LW1_2:
s_cbranch_execz .La5_LW1_6
rd v[12:15],v237 offset:43008
mm a[240:243],v[32:35],v[16:19],a[240:243]
s_add_u32 m0, s83, 17920
mm a[240:243],v[36:39],v[20:23],a[240:243]
ld v233,s[44:47],s80 offen lds
mm a[244:247],v[44:47],v[20:23],a[244:247]
mm a[244:247],v[40:43],v[16:19],a[244:247]
rt v[220:221],v243 offset:50688
rt v[222:223],v243 offset:51712
mm a[32:35],v[48:51],v[16:19],a[32:35]
s_add_u32 m0, s83, 19072
mm a[32:35],v[52:55],v[20:23],a[32:35]
ld v234,s[44:47],s80 offen lds
mm a[36:39],v[60:63],v[20:23],a[36:39]
mm a[36:39],v[56:59],v[16:19],a[36:39]
rd v[16:19],v236 offset:32768
rd v[20:23],v237 offset:32768
mm a[60:63],v[56:59],v[24:27],a[60:63]
s_add_u32 m0, s83, 20096
mm a[60:63],v[60:63],v[28:31],a[60:63]
ld v235,s[44:47],s80 offen lds
mm a[56:59],v[52:55],v[28:31],a[56:59]
s_mov_b32 s80, 1572864
mm a[56:59],v[48:51],v[24:27],a[56:59]
mm a[52:55],v[40:43],v[24:27],a[52:55]
mm a[52:55],v[44:47],v[28:31],a[52:55]
mm a[48:51],v[36:39],v[28:31],a[48:51]
wt lgkmcnt(2)
mm a[48:51],v[32:35],v[24:27],a[48:51]
)ASM" R"ASM(rd v[24:27],v236 offset:34816
rd v[28:31],v237 offset:34816
mm a[96:99],v[192:195],v[160:163],a[96:99]
mm a[96:99],v[196:199],v[164:167],a[96:99]
mm a[100:103],v[204:207],v[164:167],a[100:103]
mm a[100:103],v[200:203],v[160:163],a[100:103]
rt v[32:33],v240 offset:33920
rt v[34:35],v240 offset:34944
mm a[104:107],v[208:211],v[160:163],a[104:107]
mm a[104:107],v[212:215],v[164:167],a[104:107]
mm a[108:111],v[220:223],v[164:167],a[108:111]
wt lgkmcnt(2)
s_barrier
mm a[108:111],v[216:219],v[160:163],a[108:111]
mm a[140:143],v[216:219],v[168:171],a[140:143]
s_xor_b32 m0, s82, 32768
mm a[140:143],v[220:223],v[172:175],a[140:143]
ld v224,s[40:43],s79 offen lds
mm a[136:139],v[212:215],v[172:175],a[136:139]
mm a[136:139],v[208:211],v[168:171],a[136:139]
rt v[36:37],v240 offset:50816
rt v[38:39],v240 offset:51840
mm a[124:127],v[200:203],v[168:171],a[124:127]
s_xor_b32 m0, s82, 33792
mm a[124:127],v[204:207],v[172:175],a[124:127]
ld v225,s[40:43],s79 offen lds
mm a[120:123],v[196:199],v[172:175],a[120:123]
mm a[120:123],v[192:195],v[168:171],a[120:123]
mm a[232:235],v[192:195],v[176:179],a[232:235]
s_xor_b32 m0, s82, 34816
mm a[232:235],v[196:199],v[180:183],a[232:235]
ld v226,s[40:43],s79 offen lds
mm a[236:239],v[204:207],v[180:183],a[236:239]
mm a[236:239],v[200:203],v[176:179],a[236:239]
rt v[40:41],v241 offset:33920
rt v[42:43],v241 offset:34944
mm a[152:155],v[208:211],v[176:179],a[152:155]
s_xor_b32 m0, s82, 35840
mm a[152:155],v[212:215],v[180:183],a[152:155]
ld v227,s[40:43],s79 offen lds
mm a[156:159],v[220:223],v[180:183],a[156:159]
mm a[156:159],v[216:219],v[176:179],a[156:159]
mm a[172:175],v[216:219],v[184:187],a[172:175]
s_xor_b32 m0, s82, 36864
mm a[172:175],v[220:223],v[188:191],a[172:175]
ld v228,s[40:43],s79 offen lds
mm a[168:171],v[212:215],v[188:191],a[168:171]
mm a[168:171],v[208:211],v[184:187],a[168:171]
rt v[44:45],v241 offset:50816
rt v[46:47],v241 offset:51840
mm a[220:223],v[200:203],v[184:187],a[220:223]
s_xor_b32 m0, s82, 37888
mm a[220:223],v[204:207],v[188:191],a[220:223]
ld v229,s[40:43],s79 offen lds
mm a[216:219],v[196:199],v[188:191],a[216:219]
mm a[216:219],v[192:195],v[184:187],a[216:219]
mm a[200:203],v[192:195],v[0:3],a[200:203]
s_xor_b32 m0, s82, 38912
mm a[200:203],v[196:199],v[4:7],a[200:203]
ld v230,s[40:43],s79 offen lds
mm a[204:207],v[204:207],v[4:7],a[204:207]
mm a[204:207],v[200:203],v[0:3],a[204:207]
rt v[48:49],v242 offset:33920
rt v[50:51],v242 offset:34944
mm a[184:187],v[208:211],v[0:3],a[184:187]
s_xor_b32 m0, s82, 39936
mm a[184:187],v[212:215],v[4:7],a[184:187]
ld v231,s[40:43],s79 offen lds
mm a[188:191],v[220:223],v[4:7],a[188:191]
s_mov_b32 s79, 512
mm a[188:191],v[216:219],v[0:3],a[188:191]
mm a[12:15],v[216:219],v[8:11],a[12:15]
mm a[12:15],v[220:223],v[12:15],a[12:15]
buffer_load_dword v254, v135, s[68:71], s96 offen
mm a[8:11],v[212:215],v[12:15],a[8:11]
s_add_u32 s96, s96, 1048576
mm a[8:11],v[208:211],v[8:11],a[8:11]
rt v[52:53],v242 offset:50816
rt v[54:55],v242 offset:51840
mm a[76:79],v[200:203],v[8:11],a[76:79]
mm a[76:79],v[204:207],v[12:15],a[76:79]
buffer_load_dword v254, v135, s[68:71], s96 offen
mm a[72:75],v[196:199],v[12:15],a[72:75]
s_add_u32 s96, s96, 1048576
mm a[72:75],v[192:195],v[8:11],a[72:75]
rt v[56:57],v243 offset:33920
rt v[58:59],v243 offset:34944
mm a[88:91],v[192:195],v[16:19],a[88:91]
mm a[88:91],v[196:199],v[20:23],a[88:91]
mm a[92:95],v[204:207],v[20:23],a[92:95]
mm a[92:95],v[200:203],v[16:19],a[92:95]
rt v[60:61],v243 offset:50816
rt v[62:63],v243 offset:51840
mm a[24:27],v[208:211],v[16:19],a[24:27]
mm a[24:27],v[212:215],v[20:23],a[24:27]
mm a[28:31],v[220:223],v[20:23],a[28:31]
mm a[28:31],v[216:219],v[16:19],a[28:31]
mm a[44:47],v[216:219],v[24:27],a[44:47]
mm a[44:47],v[220:223],v[28:31],a[44:47]
mm a[40:43],v[212:215],v[28:31],a[40:43]
)ASM" R"ASM(mm a[40:43],v[208:211],v[24:27],a[40:43]
mm a[252:255],v[200:203],v[24:27],a[252:255]
mm a[252:255],v[204:207],v[28:31],a[252:255]
mm a[248:251],v[196:199],v[28:31],a[248:251]
wt vmcnt(10) lgkmcnt(0)
s_barrier
mm a[248:251],v[192:195],v[24:27],a[248:251]
mm a[112:115],v[32:35],v[160:163],a[112:115]
mm a[112:115],v[36:39],v[164:167],a[112:115]
mm a[116:119],v[44:47],v[164:167],a[116:119]
rt v[192:193],v240 offset:0
rt v[194:195],v240 offset:1024
mm a[116:119],v[40:43],v[160:163],a[116:119]
rt v[196:197],v240 offset:16896
rt v[198:199],v240 offset:17920
mm a[128:131],v[48:51],v[160:163],a[128:131]
mm a[128:131],v[52:55],v[164:167],a[128:131]
mm a[132:135],v[60:63],v[164:167],a[132:135]
mm a[132:135],v[56:59],v[160:163],a[132:135]
rd v[160:163],v236 offset:24576
rd v[164:167],v237 offset:24576
mm a[148:151],v[56:59],v[168:171],a[148:151]
mm a[148:151],v[60:63],v[172:175],a[148:151]
mm a[144:147],v[52:55],v[172:175],a[144:147]
mm a[144:147],v[48:51],v[168:171],a[144:147]
rt v[200:201],v241 offset:0
rt v[202:203],v241 offset:1024
mm a[228:231],v[40:43],v[168:171],a[228:231]
mm a[228:231],v[44:47],v[172:175],a[228:231]
mm a[224:227],v[36:39],v[172:175],a[224:227]
mm a[224:227],v[32:35],v[168:171],a[224:227]
rd v[168:171],v236 offset:26624
rd v[172:175],v237 offset:26624
mm a[208:211],v[32:35],v[176:179],a[208:211]
mm a[208:211],v[36:39],v[180:183],a[208:211]
mm a[212:215],v[44:47],v[180:183],a[212:215]
mm a[212:215],v[40:43],v[176:179],a[212:215]
rt v[204:205],v241 offset:16896
rt v[206:207],v241 offset:17920
mm a[160:163],v[48:51],v[176:179],a[160:163]
mm a[160:163],v[52:55],v[180:183],a[160:163]
mm a[164:167],v[60:63],v[180:183],a[164:167]
mm a[164:167],v[56:59],v[176:179],a[164:167]
rd v[176:179],v236 offset:16384
rd v[180:183],v237 offset:16384
mm a[180:183],v[56:59],v[184:187],a[180:183]
mm a[180:183],v[60:63],v[188:191],a[180:183]
mm a[176:179],v[52:55],v[188:191],a[176:179]
mm a[176:179],v[48:51],v[184:187],a[176:179]
rt v[208:209],v242 offset:0
rt v[210:211],v242 offset:1024
mm a[196:199],v[40:43],v[184:187],a[196:199]
s_add_u32 m0, s83, 33792
mm a[196:199],v[44:47],v[188:191],a[196:199]
ld v232,s[44:47],s80 offen lds
mm a[192:195],v[36:39],v[188:191],a[192:195]
mm a[192:195],v[32:35],v[184:187],a[192:195]
rd v[184:187],v236 offset:18432
rd v[188:191],v237 offset:18432
mm a[64:67],v[32:35],v[0:3],a[64:67]
s_add_u32 m0, s83, 34816
mm a[64:67],v[36:39],v[4:7],a[64:67]
ld v233,s[44:47],s80 offen lds
mm a[68:71],v[44:47],v[4:7],a[68:71]
mm a[68:71],v[40:43],v[0:3],a[68:71]
rt v[212:213],v242 offset:16896
rt v[214:215],v242 offset:17920
mm a[0:3],v[48:51],v[0:3],a[0:3]
s_add_u32 m0, s83, 35968
mm a[0:3],v[52:55],v[4:7],a[0:3]
ld v234,s[44:47],s80 offen lds
mm a[4:7],v[60:63],v[4:7],a[4:7]
mm a[4:7],v[56:59],v[0:3],a[4:7]
rd v[0:3],v236 offset:8192
rd v[4:7],v237 offset:8192
mm a[20:23],v[56:59],v[8:11],a[20:23]
s_add_u32 m0, s83, 36992
mm a[20:23],v[60:63],v[12:15],a[20:23]
ld v235,s[44:47],s80 offen lds
mm a[16:19],v[52:55],v[12:15],a[16:19]
s_mov_b32 s80, 1835008
mm a[16:19],v[48:51],v[8:11],a[16:19]
rt v[216:217],v243 offset:0
rt v[218:219],v243 offset:1024
mm a[84:87],v[40:43],v[8:11],a[84:87]
s_add_u32 m0, s83, 50688
mm a[84:87],v[44:47],v[12:15],a[84:87]
ld v232,s[44:47],s80 offen lds
mm a[80:83],v[36:39],v[12:15],a[80:83]
mm a[80:83],v[32:35],v[8:11],a[80:83]
rd v[8:11],v236 offset:10240
rd v[12:15],v237 offset:10240
mm a[240:243],v[32:35],v[16:19],a[240:243]
s_add_u32 m0, s83, 51712
mm a[240:243],v[36:39],v[20:23],a[240:243]
ld v233,s[44:47],s80 offen lds
mm a[244:247],v[44:47],v[20:23],a[244:247]
mm a[244:247],v[40:43],v[16:19],a[244:247]
rt v[220:221],v243 offset:16896
rt v[222:223],v243 offset:17920
)ASM" R"ASM(mm a[32:35],v[48:51],v[16:19],a[32:35]
s_add_u32 m0, s83, 52864
mm a[32:35],v[52:55],v[20:23],a[32:35]
ld v234,s[44:47],s80 offen lds
mm a[36:39],v[60:63],v[20:23],a[36:39]
mm a[36:39],v[56:59],v[16:19],a[36:39]
rd v[16:19],v236 offset:0
rd v[20:23],v237 offset:0
mm a[60:63],v[56:59],v[24:27],a[60:63]
s_add_u32 m0, s83, 53888
mm a[60:63],v[60:63],v[28:31],a[60:63]
ld v235,s[44:47],s80 offen lds
mm a[56:59],v[52:55],v[28:31],a[56:59]
s_mov_b64 s[44:45], s[68:69]
s_mov_b32 s46, s70
mm a[56:59],v[48:51],v[24:27],a[56:59]
mm a[52:55],v[40:43],v[24:27],a[52:55]
mm a[52:55],v[44:47],v[28:31],a[52:55]
mm a[48:51],v[36:39],v[28:31],a[48:51]
wt lgkmcnt(2)
s_lshl_b32 s80, s77, 18
mm a[48:51],v[32:35],v[24:27],a[48:51]
rd v[24:27],v236 offset:2048
rd v[28:31],v237 offset:2048
mm a[96:99],v[192:195],v[160:163],a[96:99]
mm a[96:99],v[196:199],v[164:167],a[96:99]
mm a[100:103],v[204:207],v[164:167],a[100:103]
mm a[100:103],v[200:203],v[160:163],a[100:103]
rt v[32:33],v240 offset:128
rt v[34:35],v240 offset:1152
mm a[104:107],v[208:211],v[160:163],a[104:107]
mm a[104:107],v[212:215],v[164:167],a[104:107]
mm a[108:111],v[220:223],v[164:167],a[108:111]
wt lgkmcnt(2)
s_barrier
mm a[108:111],v[216:219],v[160:163],a[108:111]
mm a[140:143],v[216:219],v[168:171],a[140:143]
mm a[140:143],v[220:223],v[172:175],a[140:143]
mm a[136:139],v[212:215],v[172:175],a[136:139]
mm a[136:139],v[208:211],v[168:171],a[136:139]
rt v[36:37],v240 offset:17024
rt v[38:39],v240 offset:18048
mm a[124:127],v[200:203],v[168:171],a[124:127]
mm a[124:127],v[204:207],v[172:175],a[124:127]
mm a[120:123],v[196:199],v[172:175],a[120:123]
mm a[120:123],v[192:195],v[168:171],a[120:123]
mm a[232:235],v[192:195],v[176:179],a[232:235]
mm a[232:235],v[196:199],v[180:183],a[232:235]
buffer_store_dword v255, off, s[64:67], s100 sc1
mm a[236:239],v[204:207],v[180:183],a[236:239]
buffer_store_dword v255, off, s[64:67], s101 sc1
mm a[236:239],v[200:203],v[176:179],a[236:239]
rt v[40:41],v241 offset:128
rt v[42:43],v241 offset:1152
mm a[152:155],v[208:211],v[176:179],a[152:155]
mm a[152:155],v[212:215],v[180:183],a[152:155]
mm a[156:159],v[220:223],v[180:183],a[156:159]
mm a[156:159],v[216:219],v[176:179],a[156:159]
mm a[172:175],v[216:219],v[184:187],a[172:175]
mm a[172:175],v[220:223],v[188:191],a[172:175]
mm a[168:171],v[212:215],v[188:191],a[168:171]
mm a[168:171],v[208:211],v[184:187],a[168:171]
rt v[44:45],v241 offset:17024
rt v[46:47],v241 offset:18048
mm a[220:223],v[200:203],v[184:187],a[220:223]
mm a[220:223],v[204:207],v[188:191],a[220:223]
mm a[216:219],v[196:199],v[188:191],a[216:219]
mm a[216:219],v[192:195],v[184:187],a[216:219]
mm a[200:203],v[192:195],v[0:3],a[200:203]
mm a[200:203],v[196:199],v[4:7],a[200:203]
wt vmcnt(1)
s_memtime s[100:101]
wt vmcnt(0)
s_memtime s[92:93]
wt lgkmcnt(0)
s_sub_u32 s92, s92, s100
s_cmp_lt_u32 s92, 80
s_cselect_b32 s92, 8192, 0
v_mov_b32 v77, s92
ds_write_b32 v76, v77
mm a[204:207],v[204:207],v[4:7],a[204:207]
mm a[204:207],v[200:203],v[0:3],a[204:207]
rt v[48:49],v242 offset:128
rt v[50:51],v242 offset:1152
mm a[184:187],v[208:211],v[0:3],a[184:187]
mm a[184:187],v[212:215],v[4:7],a[184:187]
mm a[188:191],v[220:223],v[4:7],a[188:191]
mm a[188:191],v[216:219],v[0:3],a[188:191]
mm a[12:15],v[216:219],v[8:11],a[12:15]
mm a[12:15],v[220:223],v[12:15],a[12:15]
buffer_load_dword v254, v135, s[68:71], s96 offen
mm a[8:11],v[212:215],v[12:15],a[8:11]
s_add_u32 s96, s96, 1048576
mm a[8:11],v[208:211],v[8:11],a[8:11]
rt v[52:53],v242 offset:17024
rt v[54:55],v242 offset:18048
mm a[76:79],v[200:203],v[8:11],a[76:79]
mm a[76:79],v[204:207],v[12:15],a[76:79]
buffer_load_dword v254, v135, s[68:71], s96 offen
mm a[72:75],v[196:199],v[12:15],a[72:75]
)ASM" R"ASM(s_add_u32 s96, s96, 1048576
mm a[72:75],v[192:195],v[8:11],a[72:75]
rt v[56:57],v243 offset:128
rt v[58:59],v243 offset:1152
mm a[88:91],v[192:195],v[16:19],a[88:91]
mm a[88:91],v[196:199],v[20:23],a[88:91]
mm a[92:95],v[204:207],v[20:23],a[92:95]
mm a[92:95],v[200:203],v[16:19],a[92:95]
rt v[60:61],v243 offset:17024
rt v[62:63],v243 offset:18048
mm a[24:27],v[208:211],v[16:19],a[24:27]
mm a[24:27],v[212:215],v[20:23],a[24:27]
mm a[28:31],v[220:223],v[20:23],a[28:31]
mm a[28:31],v[216:219],v[16:19],a[28:31]
mm a[44:47],v[216:219],v[24:27],a[44:47]
mm a[44:47],v[220:223],v[28:31],a[44:47]
ld v[72:75],v255,s[64:67],s89 offen offset:1024 sc1
mm a[40:43],v[212:215],v[28:31],a[40:43]
mm a[40:43],v[208:211],v[24:27],a[40:43]
mm a[252:255],v[200:203],v[24:27],a[252:255]
mm a[252:255],v[204:207],v[28:31],a[252:255]
mm a[248:251],v[196:199],v[28:31],a[248:251]
wt lgkmcnt(0)
s_barrier
mm a[248:251],v[192:195],v[24:27],a[248:251]
mm a[112:115],v[32:35],v[160:163],a[112:115]
mm a[112:115],v[36:39],v[164:167],a[112:115]
mm a[116:119],v[44:47],v[164:167],a[116:119]
rt v[192:193],v240 offset:33792
rt v[194:195],v240 offset:34816
mm a[116:119],v[40:43],v[160:163],a[116:119]
rt v[196:197],v240 offset:50688
rt v[198:199],v240 offset:51712
mm a[128:131],v[48:51],v[160:163],a[128:131]
mm a[128:131],v[52:55],v[164:167],a[128:131]
mm a[132:135],v[60:63],v[164:167],a[132:135]
mm a[132:135],v[56:59],v[160:163],a[132:135]
rd v[160:163],v236 offset:57344
rd v[164:167],v237 offset:57344
mm a[148:151],v[56:59],v[168:171],a[148:151]
mm a[148:151],v[60:63],v[172:175],a[148:151]
mm a[144:147],v[52:55],v[172:175],a[144:147]
mm a[144:147],v[48:51],v[168:171],a[144:147]
rt v[200:201],v241 offset:33792
rt v[202:203],v241 offset:34816
mm a[228:231],v[40:43],v[168:171],a[228:231]
mm a[228:231],v[44:47],v[172:175],a[228:231]
mm a[224:227],v[36:39],v[172:175],a[224:227]
mm a[224:227],v[32:35],v[168:171],a[224:227]
rd v[168:171],v236 offset:59392
rd v[172:175],v237 offset:59392
mm a[208:211],v[32:35],v[176:179],a[208:211]
mm a[208:211],v[36:39],v[180:183],a[208:211]
mm a[212:215],v[44:47],v[180:183],a[212:215]
mm a[212:215],v[40:43],v[176:179],a[212:215]
rt v[204:205],v241 offset:50688
rt v[206:207],v241 offset:51712
mm a[160:163],v[48:51],v[176:179],a[160:163]
mm a[160:163],v[52:55],v[180:183],a[160:163]
mm a[164:167],v[60:63],v[180:183],a[164:167]
mm a[164:167],v[56:59],v[176:179],a[164:167]
rd v[176:179],v236 offset:49152
rd v[180:183],v237 offset:49152
mm a[180:183],v[56:59],v[184:187],a[180:183]
mm a[180:183],v[60:63],v[188:191],a[180:183]
mm a[176:179],v[52:55],v[188:191],a[176:179]
mm a[176:179],v[48:51],v[184:187],a[176:179]
rt v[208:209],v242 offset:33792
rt v[210:211],v242 offset:34816
mm a[196:199],v[40:43],v[184:187],a[196:199]
s_add_u32 m0, s83, 0
mm a[196:199],v[44:47],v[188:191],a[196:199]
ld v232,s[44:47],s80 offen lds
mm a[192:195],v[36:39],v[188:191],a[192:195]
mm a[192:195],v[32:35],v[184:187],a[192:195]
rd v[184:187],v236 offset:51200
rd v[188:191],v237 offset:51200
mm a[64:67],v[32:35],v[0:3],a[64:67]
s_add_u32 m0, s83, 1024
mm a[64:67],v[36:39],v[4:7],a[64:67]
ld v233,s[44:47],s80 offen lds
mm a[68:71],v[44:47],v[4:7],a[68:71]
mm a[68:71],v[40:43],v[0:3],a[68:71]
rt v[212:213],v242 offset:50688
rt v[214:215],v242 offset:51712
mm a[0:3],v[48:51],v[0:3],a[0:3]
s_add_u32 m0, s83, 2176
mm a[0:3],v[52:55],v[4:7],a[0:3]
ld v234,s[44:47],s80 offen lds
mm a[4:7],v[60:63],v[4:7],a[4:7]
mm a[4:7],v[56:59],v[0:3],a[4:7]
rd v[0:3],v236 offset:40960
rd v[4:7],v237 offset:40960
mm a[20:23],v[56:59],v[8:11],a[20:23]
s_add_u32 m0, s83, 3200
mm a[20:23],v[60:63],v[12:15],a[20:23]
ld v235,s[44:47],s80 offen lds
mm a[16:19],v[52:55],v[12:15],a[16:19]
s_add_u32 s80, s80, 524288
mm a[16:19],v[48:51],v[8:11],a[16:19]
)ASM" R"ASM(rt v[216:217],v243 offset:33792
rt v[218:219],v243 offset:34816
mm a[84:87],v[40:43],v[8:11],a[84:87]
s_add_u32 m0, s83, 16896
mm a[84:87],v[44:47],v[12:15],a[84:87]
ld v232,s[44:47],s80 offen lds
mm a[80:83],v[36:39],v[12:15],a[80:83]
mm a[80:83],v[32:35],v[8:11],a[80:83]
rd v[8:11],v236 offset:43008
rd v[12:15],v237 offset:43008
mm a[240:243],v[32:35],v[16:19],a[240:243]
s_add_u32 m0, s83, 17920
mm a[240:243],v[36:39],v[20:23],a[240:243]
ld v233,s[44:47],s80 offen lds
mm a[244:247],v[44:47],v[20:23],a[244:247]
mm a[244:247],v[40:43],v[16:19],a[244:247]
rt v[220:221],v243 offset:50688
rt v[222:223],v243 offset:51712
mm a[32:35],v[48:51],v[16:19],a[32:35]
s_add_u32 m0, s83, 19072
mm a[32:35],v[52:55],v[20:23],a[32:35]
ld v234,s[44:47],s80 offen lds
mm a[36:39],v[60:63],v[20:23],a[36:39]
mm a[36:39],v[56:59],v[16:19],a[36:39]
rd v[16:19],v236 offset:32768
rd v[20:23],v237 offset:32768
mm a[60:63],v[56:59],v[24:27],a[60:63]
s_add_u32 m0, s83, 20096
mm a[60:63],v[60:63],v[28:31],a[60:63]
ld v235,s[44:47],s80 offen lds
mm a[56:59],v[52:55],v[28:31],a[56:59]
s_add_u32 s80, s80, 524288
mm a[56:59],v[48:51],v[24:27],a[56:59]
mm a[52:55],v[40:43],v[24:27],a[52:55]
mm a[52:55],v[44:47],v[28:31],a[52:55]
mm a[48:51],v[36:39],v[28:31],a[48:51]
wt lgkmcnt(2)
mm a[48:51],v[32:35],v[24:27],a[48:51]
rd v[24:27],v236 offset:34816
rd v[28:31],v237 offset:34816
mm a[96:99],v[192:195],v[160:163],a[96:99]
mm a[96:99],v[196:199],v[164:167],a[96:99]
mm a[100:103],v[204:207],v[164:167],a[100:103]
mm a[100:103],v[200:203],v[160:163],a[100:103]
rt v[32:33],v240 offset:33920
rt v[34:35],v240 offset:34944
mm a[104:107],v[208:211],v[160:163],a[104:107]
mm a[104:107],v[212:215],v[164:167],a[104:107]
mm a[108:111],v[220:223],v[164:167],a[108:111]
wt lgkmcnt(2)
s_barrier
mm a[108:111],v[216:219],v[160:163],a[108:111]
mm a[140:143],v[216:219],v[168:171],a[140:143]
wt vmcnt(8)
v_cmp_eq_u64 vcc, v[72:73], v[252:253]
mm a[140:143],v[220:223],v[172:175],a[140:143]
v_cmp_eq_u64 s[96:97], v[74:75], v[252:253]
mm a[136:139],v[212:215],v[172:175],a[136:139]
s_and_b64 vcc, vcc, s[96:97]
s_cmp_eq_u64 vcc, -1
s_cbranch_scc0 .La5_LK_XSPIN
.La5_LK_XOK:
mm a[136:139],v[208:211],v[168:171],a[136:139]
rt v[36:37],v240 offset:50816
rt v[38:39],v240 offset:51840
mm a[124:127],v[200:203],v[168:171],a[124:127]
s_mov_b32 s89, 49152
mm a[124:127],v[204:207],v[172:175],a[124:127]
mm a[120:123],v[196:199],v[172:175],a[120:123]
ar v224,a96
ar v225,a97
mm a[120:123],v[192:195],v[168:171],a[120:123]
ar v226,a98
ar v227,a99
mm a[232:235],v[192:195],v[176:179],a[232:235]
ar v228,a100
ar v229,a101
mm a[232:235],v[196:199],v[180:183],a[232:235]
ld v[128:131],v244,s[52:55],s91 offen offset:0 sc1
mm a[236:239],v[204:207],v[180:183],a[236:239]
ar v230,a102
ar v231,a103
mm a[236:239],v[200:203],v[176:179],a[236:239]
rt v[40:41],v241 offset:33920
rt v[42:43],v241 offset:34944
mm a[152:155],v[208:211],v[176:179],a[152:155]
cv v248,v224,v225
cv v249,v226,v227
mm a[152:155],v[212:215],v[180:183],a[152:155]
ld v[132:135],v245,s[52:55],s91 offen offset:0 sc1
mm a[156:159],v[220:223],v[180:183],a[156:159]
cv v250,v228,v229
cv v251,v230,v231
mm a[156:159],v[216:219],v[176:179],a[156:159]
st v[248:251],v255,s[48:51],s89 offen sc1
mm a[172:175],v[216:219],v[184:187],a[172:175]
s_mov_b32 s89, 50176
.La5_LW1_3:
s_cbranch_execz .La5_LW1_7
ds_write_b128 v238, v[248:251] offset:0
)ASM" R"ASM(mm a[172:175],v[220:223],v[188:191],a[172:175]
ld v[136:139],v246,s[52:55],s91 offen offset:0 sc1
mm a[168:171],v[212:215],v[188:191],a[168:171]
ar v224,a104
ar v225,a105
mm a[168:171],v[208:211],v[184:187],a[168:171]
rt v[44:45],v241 offset:50816
rt v[46:47],v241 offset:51840
mm a[220:223],v[200:203],v[184:187],a[220:223]
ar v226,a106
ar v227,a107
mm a[220:223],v[204:207],v[188:191],a[220:223]
ld v[140:143],v247,s[52:55],s91 offen offset:0 sc1
mm a[216:219],v[196:199],v[188:191],a[216:219]
ar v228,a108
ar v229,a109
mm a[216:219],v[192:195],v[184:187],a[216:219]
ar v230,a110
ar v231,a111
mm a[200:203],v[192:195],v[0:3],a[200:203]
cv v248,v224,v225
cv v249,v226,v227
mm a[200:203],v[196:199],v[4:7],a[200:203]
ld v[144:147],v244,s[52:55],s91 offen offset:1024 sc1
mm a[204:207],v[204:207],v[4:7],a[204:207]
cv v250,v228,v229
cv v251,v230,v231
mm a[204:207],v[200:203],v[0:3],a[204:207]
rt v[48:49],v242 offset:33920
rt v[50:51],v242 offset:34944
mm a[184:187],v[208:211],v[0:3],a[184:187]
st v[248:251],v255,s[48:51],s89 offen sc1
mm a[184:187],v[212:215],v[4:7],a[184:187]
ld v[148:151],v245,s[52:55],s91 offen offset:1024 sc1
mm a[188:191],v[220:223],v[4:7],a[188:191]
s_mov_b32 s89, 51200
ds_write_b128 v238, v[248:251] offset:1024
mm a[188:191],v[216:219],v[0:3],a[188:191]
mm a[12:15],v[216:219],v[8:11],a[12:15]
mm a[12:15],v[220:223],v[12:15],a[12:15]
ld v[152:155],v246,s[52:55],s91 offen offset:1024 sc1
mm a[8:11],v[212:215],v[12:15],a[8:11]
mm a[8:11],v[208:211],v[8:11],a[8:11]
rt v[52:53],v242 offset:50816
rt v[54:55],v242 offset:51840
mm a[76:79],v[200:203],v[8:11],a[76:79]
mm a[76:79],v[204:207],v[12:15],a[76:79]
ld v[156:159],v247,s[52:55],s91 offen offset:1024 sc1
mm a[72:75],v[196:199],v[12:15],a[72:75]
mm a[72:75],v[192:195],v[8:11],a[72:75]
rt v[56:57],v243 offset:33920
rt v[58:59],v243 offset:34944
mm a[88:91],v[192:195],v[16:19],a[88:91]
mm a[88:91],v[196:199],v[20:23],a[88:91]
mm a[92:95],v[204:207],v[20:23],a[92:95]
mm a[92:95],v[200:203],v[16:19],a[92:95]
rt v[60:61],v243 offset:50816
rt v[62:63],v243 offset:51840
mm a[24:27],v[208:211],v[16:19],a[24:27]
mm a[24:27],v[212:215],v[20:23],a[24:27]
mm a[28:31],v[220:223],v[20:23],a[28:31]
mm a[28:31],v[216:219],v[16:19],a[28:31]
mm a[44:47],v[216:219],v[24:27],a[44:47]
mm a[44:47],v[220:223],v[28:31],a[44:47]
mm a[40:43],v[212:215],v[28:31],a[40:43]
mm a[40:43],v[208:211],v[24:27],a[40:43]
mm a[252:255],v[200:203],v[24:27],a[252:255]
mm a[252:255],v[204:207],v[28:31],a[252:255]
mm a[248:251],v[196:199],v[28:31],a[248:251]
wt vmcnt(10) lgkmcnt(0)
s_barrier
mm a[248:251],v[192:195],v[24:27],a[248:251]
mm a[112:115],v[32:35],v[160:163],a[112:115]
s_add_u32 m0, s83, 33792
mm a[112:115],v[36:39],v[164:167],a[112:115]
ld v232,s[44:47],s80 offen lds
mm a[116:119],v[44:47],v[164:167],a[116:119]
mm a[116:119],v[40:43],v[160:163],a[116:119]
rt v[192:193],v240 offset:0
rt v[194:195],v240 offset:1024
mm a[128:131],v[48:51],v[160:163],a[128:131]
s_add_u32 m0, s83, 34816
mm a[128:131],v[52:55],v[164:167],a[128:131]
ld v233,s[44:47],s80 offen lds
mm a[132:135],v[60:63],v[164:167],a[132:135]
mm a[132:135],v[56:59],v[160:163],a[132:135]
rt v[196:197],v240 offset:8448
rt v[198:199],v240 offset:9472
mm a[148:151],v[56:59],v[168:171],a[148:151]
s_add_u32 m0, s83, 35968
mm a[148:151],v[60:63],v[172:175],a[148:151]
ld v234,s[44:47],s80 offen lds
mm a[144:147],v[52:55],v[172:175],a[144:147]
ar v224,a112
ar v225,a113
mm a[144:147],v[48:51],v[168:171],a[144:147]
rt v[200:201],v241 offset:0
rt v[202:203],v241 offset:1024
mm a[228:231],v[40:43],v[168:171],a[228:231]
s_add_u32 m0, s83, 36992
)ASM" R"ASM(ar v226,a114
mm a[228:231],v[44:47],v[172:175],a[228:231]
ld v235,s[44:47],s80 offen lds
mm a[224:227],v[36:39],v[172:175],a[224:227]
s_add_u32 s80, s80, 524288
ar v227,a115
mm a[224:227],v[32:35],v[168:171],a[224:227]
rt v[204:205],v241 offset:8448
rt v[206:207],v241 offset:9472
mm a[208:211],v[32:35],v[176:179],a[208:211]
s_add_u32 m0, s83, 50688
ar v228,a116
mm a[208:211],v[36:39],v[180:183],a[208:211]
ld v232,s[44:47],s80 offen lds
mm a[212:215],v[44:47],v[180:183],a[212:215]
ar v229,a117
ar v230,a118
mm a[212:215],v[40:43],v[176:179],a[212:215]
rt v[208:209],v242 offset:0
rt v[210:211],v242 offset:1024
mm a[160:163],v[48:51],v[176:179],a[160:163]
s_add_u32 m0, s83, 51712
ar v231,a119
mm a[160:163],v[52:55],v[180:183],a[160:163]
ld v233,s[44:47],s80 offen lds
mm a[164:167],v[60:63],v[180:183],a[164:167]
cv v248,v224,v225
cv v249,v226,v227
mm a[164:167],v[56:59],v[176:179],a[164:167]
rt v[212:213],v242 offset:8448
rt v[214:215],v242 offset:9472
mm a[180:183],v[56:59],v[184:187],a[180:183]
s_add_u32 m0, s83, 52864
cv v250,v228,v229
mm a[180:183],v[60:63],v[188:191],a[180:183]
ld v234,s[44:47],s80 offen lds
mm a[176:179],v[52:55],v[188:191],a[176:179]
cv v251,v230,v231
st v[248:251],v255,s[48:51],s89 offen sc1
mm a[176:179],v[48:51],v[184:187],a[176:179]
rt v[216:217],v243 offset:0
rt v[218:219],v243 offset:1024
mm a[196:199],v[40:43],v[184:187],a[196:199]
s_add_u32 m0, s83, 53888
s_mov_b32 s89, 52224
mm a[196:199],v[44:47],v[188:191],a[196:199]
ld v235,s[44:47],s80 offen lds
mm a[192:195],v[36:39],v[188:191],a[192:195]
s_add_u32 s80, s80, 524288
ds_write_b128 v238, v[248:251] offset:2048
mm a[192:195],v[32:35],v[184:187],a[192:195]
rt v[220:221],v243 offset:8448
rt v[222:223],v243 offset:9472
mm a[64:67],v[32:35],v[0:3],a[64:67]
mm a[64:67],v[36:39],v[4:7],a[64:67]
ld v[160:163],v244,s[52:55],s91 offen offset:2048 sc1
mm a[68:71],v[44:47],v[4:7],a[68:71]
mm a[68:71],v[40:43],v[0:3],a[68:71]
mm a[0:3],v[48:51],v[0:3],a[0:3]
mm a[0:3],v[52:55],v[4:7],a[0:3]
ld v[164:167],v245,s[52:55],s91 offen offset:2048 sc1
mm a[4:7],v[60:63],v[4:7],a[4:7]
mm a[4:7],v[56:59],v[0:3],a[4:7]
mm a[20:23],v[56:59],v[8:11],a[20:23]
mm a[20:23],v[60:63],v[12:15],a[20:23]
ld v[168:171],v246,s[52:55],s91 offen offset:2048 sc1
mm a[16:19],v[52:55],v[12:15],a[16:19]
mm a[16:19],v[48:51],v[8:11],a[16:19]
mm a[84:87],v[40:43],v[8:11],a[84:87]
mm a[84:87],v[44:47],v[12:15],a[84:87]
ld v[172:175],v247,s[52:55],s91 offen offset:2048 sc1
mm a[80:83],v[36:39],v[12:15],a[80:83]
mm a[80:83],v[32:35],v[8:11],a[80:83]
mm a[240:243],v[32:35],v[16:19],a[240:243]
mm a[240:243],v[36:39],v[20:23],a[240:243]
mm a[244:247],v[44:47],v[20:23],a[244:247]
mm a[244:247],v[40:43],v[16:19],a[244:247]
mm a[32:35],v[48:51],v[16:19],a[32:35]
mm a[32:35],v[52:55],v[20:23],a[32:35]
mm a[36:39],v[60:63],v[20:23],a[36:39]
mm a[36:39],v[56:59],v[16:19],a[36:39]
mm a[60:63],v[56:59],v[24:27],a[60:63]
mm a[60:63],v[60:63],v[28:31],a[60:63]
mm a[56:59],v[52:55],v[28:31],a[56:59]
mm a[56:59],v[48:51],v[24:27],a[56:59]
mm a[52:55],v[40:43],v[24:27],a[52:55]
mm a[52:55],v[44:47],v[28:31],a[52:55]
mm a[48:51],v[36:39],v[28:31],a[48:51]
mm a[48:51],v[32:35],v[24:27],a[48:51]
s_branch .La5_LQ_START
.La5_LV_XSPIN:
s_mov_b32 s93, 0
.La5_LV_XSPIN_L:
s_add_u32 s93, s93, 1
s_cmp_gt_u32 s93, 2097152
s_cbranch_scc1 .La5_LV_XOK
s_sleep 8
ld v[72:75],v255,s[64:67],s89 offen offset:1024 sc1
wt vmcnt(0)
v_cmp_eq_u64 vcc, v[72:73], v[252:253]
)ASM" R"ASM(v_cmp_eq_u64 s[96:97], v[74:75], v[252:253]
s_nop 1
s_and_b64 vcc, vcc, s[96:97]
s_cmp_eq_u64 vcc, -1
s_cbranch_scc0 .La5_LV_XSPIN_L
s_branch .La5_LV_XOK
.La5_LK_XSPIN:
s_mov_b32 s93, 0
.La5_LK_XSPIN_L:
s_add_u32 s93, s93, 1
s_cmp_gt_u32 s93, 2097152
s_cbranch_scc1 .La5_LK_XOK
s_sleep 8
ld v[72:75],v255,s[64:67],s89 offen offset:1024 sc1
wt vmcnt(0)
v_cmp_eq_u64 vcc, v[72:73], v[252:253]
v_cmp_eq_u64 s[96:97], v[74:75], v[252:253]
s_nop 1
s_and_b64 vcc, vcc, s[96:97]
s_cmp_eq_u64 vcc, -1
s_cbranch_scc0 .La5_LK_XSPIN_L
s_branch .La5_LK_XOK
; ---- V tile (waves 1, 3): the same with the operands exchanged (V comes out transposed)
.La5_LV_START:
s_nop 0
.La5_LW1_4:
s_cbranch_execz .La5_LW1_8
mm a[96:99],v[160:163],v[192:195],0
s_add_u32 m0, s83, 33792
mm a[96:99],v[164:167],v[196:199],a[96:99]
ld v232,s[44:47],s80 offen lds
mm a[104:107],v[164:167],v[204:207],0
rd v[0:3],v236 offset:8192
rd v[4:7],v237 offset:8192
s_add_u32 m0, s83, 34816
mm a[104:107],v[160:163],v[200:203],a[104:107]
rd v[8:11],v236 offset:10240
rd v[12:15],v237 offset:10240
ld v233,s[44:47],s80 offen lds
mm a[112:115],v[160:163],v[208:211],0
s_add_u32 m0, s83, 35968
mm a[112:115],v[164:167],v[212:215],a[112:115]
ld v234,s[44:47],s80 offen lds
mm a[128:131],v[164:167],v[220:223],0
rd v[16:19],v236 offset:0
rd v[20:23],v237 offset:0
s_add_u32 m0, s83, 36992
mm a[128:131],v[160:163],v[216:219],a[128:131]
rd v[24:27],v236 offset:2048
rd v[28:31],v237 offset:2048
ld v235,s[44:47],s80 offen lds
mm a[132:135],v[168:171],v[216:219],0
s_mov_b32 s80, 2883584
mm a[132:135],v[172:175],v[220:223],a[132:135]
mm a[116:119],v[172:175],v[212:215],0
s_xor_b32 m0, s82, 32768
mm a[116:119],v[168:171],v[208:211],a[116:119]
rt v[32:33],v240 offset:128
rt v[34:35],v240 offset:1152
ld v224,s[40:43],s79 offen lds
mm a[108:111],v[168:171],v[200:203],0
s_xor_b32 m0, s82, 33792
mm a[108:111],v[172:175],v[204:207],a[108:111]
ld v225,s[40:43],s79 offen lds
mm a[100:103],v[172:175],v[196:199],0
s_xor_b32 m0, s82, 34816
mm a[100:103],v[168:171],v[192:195],a[100:103]
rt v[36:37],v240 offset:17024
rt v[38:39],v240 offset:18048
ld v226,s[40:43],s79 offen lds
mm a[232:235],v[176:179],v[192:195],0
s_xor_b32 m0, s82, 35840
mm a[232:235],v[180:183],v[196:199],a[232:235]
ld v227,s[40:43],s79 offen lds
mm a[152:155],v[180:183],v[204:207],0
s_xor_b32 m0, s82, 36864
mm a[152:155],v[176:179],v[200:203],a[152:155]
rt v[40:41],v241 offset:128
rt v[42:43],v241 offset:1152
ld v228,s[40:43],s79 offen lds
mm a[208:211],v[176:179],v[208:211],0
s_xor_b32 m0, s82, 37888
mm a[208:211],v[180:183],v[212:215],a[208:211]
ld v229,s[40:43],s79 offen lds
mm a[160:163],v[180:183],v[220:223],0
s_xor_b32 m0, s82, 38912
mm a[160:163],v[176:179],v[216:219],a[160:163]
ld v230,s[40:43],s79 offen lds
mm a[164:167],v[184:187],v[216:219],0
s_xor_b32 m0, s82, 39936
mm a[164:167],v[188:191],v[220:223],a[164:167]
ld v231,s[40:43],s79 offen lds
mm a[212:215],v[188:191],v[212:215],0
s_mov_b32 s79, 768
mm a[212:215],v[184:187],v[208:211],a[212:215]
rt v[44:45],v241 offset:17024
rt v[46:47],v241 offset:18048
mm a[156:159],v[184:187],v[200:203],0
s_add_u32 m0, s83, 50688
mm a[156:159],v[188:191],v[204:207],a[156:159]
)ASM" R"ASM(ld v232,s[44:47],s80 offen lds
mm a[236:239],v[188:191],v[196:199],0
wt lgkmcnt(8)
s_add_u32 m0, s83, 51712
mm a[236:239],v[184:187],v[192:195],a[236:239]
ld v233,s[44:47],s80 offen lds
mm a[200:203],v[0:3],v[192:195],0
s_add_u32 m0, s83, 52864
mm a[200:203],v[4:7],v[196:199],a[200:203]
ld v234,s[44:47],s80 offen lds
mm a[184:187],v[4:7],v[204:207],0
s_add_u32 m0, s83, 53888
mm a[184:187],v[0:3],v[200:203],a[184:187]
rt v[48:49],v242 offset:128
rt v[50:51],v242 offset:1152
ld v235,s[44:47],s80 offen lds
mm a[64:67],v[0:3],v[208:211],0
s_mov_b32 s80, 3145728
mm a[64:67],v[4:7],v[212:215],a[64:67]
mm a[0:3],v[4:7],v[220:223],0
mm a[0:3],v[0:3],v[216:219],a[0:3]
mm a[4:7],v[8:11],v[216:219],0
mm a[4:7],v[12:15],v[220:223],a[4:7]
buffer_load_dword v70, v118, s[60:63], 0 offen
mm a[68:71],v[12:15],v[212:215],0
mm a[68:71],v[8:11],v[208:211],a[68:71]
rt v[52:53],v242 offset:17024
rt v[54:55],v242 offset:18048
mm a[188:191],v[8:11],v[200:203],0
mm a[188:191],v[12:15],v[204:207],a[188:191]
ld v[72:75],v119,s[60:63],0 offen offset:0
mm a[204:207],v[12:15],v[196:199],0
mm a[204:207],v[8:11],v[192:195],a[204:207]
rt v[56:57],v243 offset:128
rt v[58:59],v243 offset:1152
mm a[88:91],v[16:19],v[192:195],0
mm a[88:91],v[20:23],v[196:199],a[88:91]
ld v[76:79],v119,s[60:63],0 offen offset:128
mm a[24:27],v[20:23],v[204:207],0
mm a[24:27],v[16:19],v[200:203],a[24:27]
rt v[60:61],v243 offset:17024
rt v[62:63],v243 offset:18048
mm a[240:243],v[16:19],v[208:211],0
mm a[240:243],v[20:23],v[212:215],a[240:243]
ld v[80:83],v119,s[60:63],0 offen offset:256
mm a[32:35],v[20:23],v[220:223],0
mm a[32:35],v[16:19],v[216:219],a[32:35]
mm a[36:39],v[24:27],v[216:219],0
mm a[36:39],v[28:31],v[220:223],a[36:39]
ld v[84:87],v119,s[60:63],0 offen offset:384
mm a[244:247],v[28:31],v[212:215],0
mm a[244:247],v[24:27],v[208:211],a[244:247]
mm a[28:31],v[24:27],v[200:203],0
mm a[28:31],v[28:31],v[204:207],a[28:31]
mm a[92:95],v[28:31],v[196:199],0
mm a[92:95],v[24:27],v[192:195],a[92:95]
s_mov_b64 exec, 0
s_cmp_eq_u32 s3, 1
s_cbranch_scc1 .La5_LW2_1
s_branch .La5_LW2_R1
.La5_LW2_R1:
s_mov_b64 exec, -1
wt vmcnt(5) lgkmcnt(0)
s_barrier
mm a[120:123],v[160:163],v[32:35],0
mm a[120:123],v[164:167],v[36:39],a[120:123]
mm a[136:139],v[164:167],v[44:47],0
rt v[192:193],v240 offset:33792
rt v[194:195],v240 offset:34816
mm a[136:139],v[160:163],v[40:43],a[136:139]
rt v[196:197],v240 offset:50688
rt v[198:199],v240 offset:51712
mm a[224:227],v[160:163],v[48:51],0
mm a[224:227],v[164:167],v[52:55],a[224:227]
mm a[144:147],v[164:167],v[60:63],0
mm a[144:147],v[160:163],v[56:59],a[144:147]
rd v[160:163],v236 offset:57344
rd v[164:167],v237 offset:57344
mm a[148:151],v[168:171],v[56:59],0
mm a[148:151],v[172:175],v[60:63],a[148:151]
mm a[228:231],v[172:175],v[52:55],0
mm a[228:231],v[168:171],v[48:51],a[228:231]
rt v[200:201],v241 offset:33792
rt v[202:203],v241 offset:34816
mm a[140:143],v[168:171],v[40:43],0
mm a[140:143],v[172:175],v[44:47],a[140:143]
mm a[124:127],v[172:175],v[36:39],0
mm a[124:127],v[168:171],v[32:35],a[124:127]
rd v[168:171],v236 offset:59392
rd v[172:175],v237 offset:59392
mm a[216:219],v[176:179],v[32:35],0
s_xor_b32 m0, s82, 0
mm a[216:219],v[180:183],v[36:39],a[216:219]
ld v224,s[40:43],s79 offen lds
mm a[168:171],v[180:183],v[44:47],0
mm a[168:171],v[176:179],v[40:43],a[168:171]
rt v[204:205],v241 offset:50688
rt v[206:207],v241 offset:51712
s_xor_b32 m0, s82, 1024
mm a[192:195],v[176:179],v[48:51],0
)ASM" R"ASM(ld v225,s[40:43],s79 offen lds
mm a[192:195],v[180:183],v[52:55],a[192:195]
mm a[176:179],v[180:183],v[60:63],0
s_xor_b32 m0, s82, 2048
mm a[176:179],v[176:179],v[56:59],a[176:179]
rd v[176:179],v236 offset:49152
rd v[180:183],v237 offset:49152
ld v226,s[40:43],s79 offen lds
mm a[180:183],v[184:187],v[56:59],0
mm a[180:183],v[188:191],v[60:63],a[180:183]
s_xor_b32 m0, s82, 3072
mm a[196:199],v[188:191],v[52:55],0
ld v227,s[40:43],s79 offen lds
mm a[196:199],v[184:187],v[48:51],a[196:199]
rt v[208:209],v242 offset:33792
rt v[210:211],v242 offset:34816
mm a[172:175],v[184:187],v[40:43],0
s_xor_b32 m0, s82, 4096
mm a[172:175],v[188:191],v[44:47],a[172:175]
ld v228,s[40:43],s79 offen lds
mm a[220:223],v[188:191],v[36:39],0
mm a[220:223],v[184:187],v[32:35],a[220:223]
rd v[184:187],v236 offset:51200
rd v[188:191],v237 offset:51200
s_xor_b32 m0, s82, 5120
mm a[72:75],v[0:3],v[32:35],0
ld v229,s[40:43],s79 offen lds
mm a[72:75],v[4:7],v[36:39],a[72:75]
mm a[8:11],v[4:7],v[44:47],0
s_xor_b32 m0, s82, 6144
mm a[8:11],v[0:3],v[40:43],a[8:11]
rt v[212:213],v242 offset:50688
rt v[214:215],v242 offset:51712
ld v230,s[40:43],s79 offen lds
mm a[80:83],v[0:3],v[48:51],0
mm a[80:83],v[4:7],v[52:55],a[80:83]
s_xor_b32 m0, s82, 7168
mm a[16:19],v[4:7],v[60:63],0
ld v231,s[40:43],s79 offen lds
mm a[16:19],v[0:3],v[56:59],a[16:19]
rd v[0:3],v236 offset:40960
rd v[4:7],v237 offset:40960
s_mov_b32 s79, 896
mm a[20:23],v[8:11],v[56:59],0
mm a[20:23],v[12:15],v[60:63],a[20:23]
s_add_u32 m0, s83, 0
mm a[84:87],v[12:15],v[52:55],0
ld v232,s[44:47],s80 offen lds
mm a[84:87],v[8:11],v[48:51],a[84:87]
rt v[216:217],v243 offset:33792
rt v[218:219],v243 offset:34816
mm a[12:15],v[8:11],v[40:43],0
s_add_u32 m0, s83, 1024
mm a[12:15],v[12:15],v[44:47],a[12:15]
ld v233,s[44:47],s80 offen lds
mm a[76:79],v[12:15],v[36:39],0
wt vmcnt(10)
v_lshlrev_b32 v70, 16, v70
mm a[76:79],v[8:11],v[32:35],a[76:79]
rd v[8:11],v236 offset:43008
rd v[12:15],v237 offset:43008
s_add_u32 m0, s83, 2176
mm a[248:251],v[16:19],v[32:35],0
ld v234,s[44:47],s80 offen lds
mm a[248:251],v[20:23],v[36:39],a[248:251]
v_lshlrev_b32 v88, 16, v72
v_and_b32 v89, 0xffff0000, v72
mm a[40:43],v[20:23],v[44:47],0
s_add_u32 m0, s83, 3200
v_sub_f32 v88, v88, v70
mm a[40:43],v[16:19],v[40:43],a[40:43]
rt v[220:221],v243 offset:50688
rt v[222:223],v243 offset:51712
ld v235,s[44:47],s80 offen lds
mm a[48:51],v[16:19],v[48:51],0
s_mov_b32 s80, 3407872
v_sub_f32 v89, v89, v70
mm a[48:51],v[20:23],v[52:55],a[48:51]
v_add_f32 v120, v88, v89
v_mul_f32 v122, v88, v88
mm a[56:59],v[20:23],v[60:63],0
s_add_u32 m0, s83, 16896
v_mul_f32 v123, v89, v89
mm a[56:59],v[16:19],v[56:59],a[56:59]
rd v[16:19],v236 offset:32768
rd v[20:23],v237 offset:32768
ld v232,s[44:47],s80 offen lds
mm a[60:63],v[24:27],v[56:59],0
v_lshlrev_b32 v90, 16, v73
v_and_b32 v91, 0xffff0000, v73
mm a[60:63],v[28:31],v[60:63],a[60:63]
s_add_u32 m0, s83, 17920
v_sub_f32 v90, v90, v70
mm a[52:55],v[28:31],v[52:55],0
ld v233,s[44:47],s80 offen lds
mm a[52:55],v[24:27],v[48:51],a[52:55]
v_sub_f32 v91, v91, v70
v_add_f32 v124, v90, v91
mm a[44:47],v[24:27],v[40:43],0
s_add_u32 m0, s83, 19072
)ASM" R"ASM(v_fmac_f32 v122, v90, v90
mm a[44:47],v[28:31],v[44:47],a[44:47]
ld v234,s[44:47],s80 offen lds
mm a[252:255],v[28:31],v[36:39],0
wt lgkmcnt(2)
v_add_f32 v120, v120, v124
mm a[252:255],v[24:27],v[32:35],a[252:255]
rd v[24:27],v236 offset:34816
rd v[28:31],v237 offset:34816
s_add_u32 m0, s83, 20096
mm a[96:99],v[160:163],v[192:195],a[96:99]
ld v235,s[44:47],s80 offen lds
mm a[96:99],v[164:167],v[196:199],a[96:99]
s_mov_b32 s80, 3670016
v_fmac_f32 v123, v91, v91
mm a[104:107],v[164:167],v[204:207],a[104:107]
v_lshlrev_b32 v92, 16, v74
v_and_b32 v93, 0xffff0000, v74
mm a[104:107],v[160:163],v[200:203],a[104:107]
rt v[32:33],v240 offset:33920
rt v[34:35],v240 offset:34944
mm a[112:115],v[160:163],v[208:211],a[112:115]
v_sub_f32 v92, v92, v70
v_sub_f32 v93, v93, v70
mm a[112:115],v[164:167],v[212:215],a[112:115]
v_add_f32 v124, v92, v93
v_fmac_f32 v122, v92, v92
mm a[128:131],v[164:167],v[220:223],a[128:131]
wt lgkmcnt(2)
s_barrier
mm a[128:131],v[160:163],v[216:219],a[128:131]
v_add_f32 v120, v120, v124
v_fmac_f32 v123, v93, v93
mm a[132:135],v[168:171],v[216:219],a[132:135]
s_xor_b32 m0, s82, 32768
v_lshlrev_b32 v94, 16, v75
mm a[132:135],v[172:175],v[220:223],a[132:135]
ld v224,s[40:43],s79 offen lds
mm a[116:119],v[172:175],v[212:215],a[116:119]
v_and_b32 v95, 0xffff0000, v75
v_sub_f32 v94, v94, v70
mm a[116:119],v[168:171],v[208:211],a[116:119]
rt v[36:37],v240 offset:50816
rt v[38:39],v240 offset:51840
mm a[108:111],v[168:171],v[200:203],a[108:111]
s_xor_b32 m0, s82, 33792
v_sub_f32 v95, v95, v70
mm a[108:111],v[172:175],v[204:207],a[108:111]
ld v225,s[40:43],s79 offen lds
mm a[100:103],v[172:175],v[196:199],a[100:103]
v_add_f32 v124, v94, v95
.La5_LW1_5:
s_cbranch_execz .La5_LW1_9
v_fmac_f32 v122, v94, v94
mm a[100:103],v[168:171],v[192:195],a[100:103]
v_add_f32 v120, v120, v124
v_fmac_f32 v123, v95, v95
mm a[232:235],v[176:179],v[192:195],a[232:235]
s_xor_b32 m0, s82, 34816
v_lshlrev_b32 v96, 16, v76
mm a[232:235],v[180:183],v[196:199],a[232:235]
ld v226,s[40:43],s79 offen lds
mm a[152:155],v[180:183],v[204:207],a[152:155]
v_and_b32 v97, 0xffff0000, v76
v_sub_f32 v96, v96, v70
mm a[152:155],v[176:179],v[200:203],a[152:155]
rt v[40:41],v241 offset:33920
rt v[42:43],v241 offset:34944
mm a[208:211],v[176:179],v[208:211],a[208:211]
s_xor_b32 m0, s82, 35840
v_sub_f32 v97, v97, v70
mm a[208:211],v[180:183],v[212:215],a[208:211]
ld v227,s[40:43],s79 offen lds
mm a[160:163],v[180:183],v[220:223],a[160:163]
v_add_f32 v124, v96, v97
v_fmac_f32 v122, v96, v96
mm a[160:163],v[176:179],v[216:219],a[160:163]
v_add_f32 v120, v120, v124
v_fmac_f32 v123, v97, v97
mm a[164:167],v[184:187],v[216:219],a[164:167]
s_xor_b32 m0, s82, 36864
v_lshlrev_b32 v98, 16, v77
mm a[164:167],v[188:191],v[220:223],a[164:167]
ld v228,s[40:43],s79 offen lds
mm a[212:215],v[188:191],v[212:215],a[212:215]
v_and_b32 v99, 0xffff0000, v77
v_sub_f32 v98, v98, v70
mm a[212:215],v[184:187],v[208:211],a[212:215]
rt v[44:45],v241 offset:50816
rt v[46:47],v241 offset:51840
mm a[156:159],v[184:187],v[200:203],a[156:159]
s_xor_b32 m0, s82, 37888
v_sub_f32 v99, v99, v70
mm a[156:159],v[188:191],v[204:207],a[156:159]
ld v229,s[40:43],s79 offen lds
mm a[236:239],v[188:191],v[196:199],a[236:239]
v_add_f32 v124, v98, v99
v_fmac_f32 v122, v98, v98
mm a[236:239],v[184:187],v[192:195],a[236:239]
v_add_f32 v120, v120, v124
)ASM" R"ASM(v_fmac_f32 v123, v99, v99
mm a[200:203],v[0:3],v[192:195],a[200:203]
s_xor_b32 m0, s82, 38912
v_lshlrev_b32 v100, 16, v78
mm a[200:203],v[4:7],v[196:199],a[200:203]
ld v230,s[40:43],s79 offen lds
mm a[184:187],v[4:7],v[204:207],a[184:187]
v_and_b32 v101, 0xffff0000, v78
v_sub_f32 v100, v100, v70
mm a[184:187],v[0:3],v[200:203],a[184:187]
rt v[48:49],v242 offset:33920
rt v[50:51],v242 offset:34944
mm a[64:67],v[0:3],v[208:211],a[64:67]
s_xor_b32 m0, s82, 39936
v_sub_f32 v101, v101, v70
mm a[64:67],v[4:7],v[212:215],a[64:67]
ld v231,s[40:43],s79 offen lds
mm a[0:3],v[4:7],v[220:223],a[0:3]
s_mov_b32 s79, 0
v_add_f32 v124, v100, v101
mm a[0:3],v[0:3],v[216:219],a[0:3]
v_fmac_f32 v122, v100, v100
v_add_f32 v120, v120, v124
mm a[4:7],v[8:11],v[216:219],a[4:7]
v_fmac_f32 v123, v101, v101
v_lshlrev_b32 v102, 16, v79
mm a[4:7],v[12:15],v[220:223],a[4:7]
buffer_load_dword v254, v132, s[44:47], s86 offen
mm a[68:71],v[12:15],v[212:215],a[68:71]
s_add_u32 s86, s86, 2097152
s_and_b32 s86, s86, 4194303
mm a[68:71],v[8:11],v[208:211],a[68:71]
rt v[52:53],v242 offset:50816
rt v[54:55],v242 offset:51840
mm a[188:191],v[8:11],v[200:203],a[188:191]
v_and_b32 v103, 0xffff0000, v79
v_sub_f32 v102, v102, v70
mm a[188:191],v[12:15],v[204:207],a[188:191]
v_sub_f32 v103, v103, v70
v_add_f32 v124, v102, v103
mm a[204:207],v[12:15],v[196:199],a[204:207]
v_fmac_f32 v122, v102, v102
v_add_f32 v120, v120, v124
mm a[204:207],v[8:11],v[192:195],a[204:207]
rt v[56:57],v243 offset:33920
rt v[58:59],v243 offset:34944
mm a[88:91],v[16:19],v[192:195],a[88:91]
v_fmac_f32 v123, v103, v103
v_lshlrev_b32 v104, 16, v80
mm a[88:91],v[20:23],v[196:199],a[88:91]
v_and_b32 v105, 0xffff0000, v80
v_sub_f32 v104, v104, v70
mm a[24:27],v[20:23],v[204:207],a[24:27]
v_sub_f32 v105, v105, v70
v_add_f32 v124, v104, v105
mm a[24:27],v[16:19],v[200:203],a[24:27]
rt v[60:61],v243 offset:50816
rt v[62:63],v243 offset:51840
mm a[240:243],v[16:19],v[208:211],a[240:243]
v_fmac_f32 v122, v104, v104
v_add_f32 v120, v120, v124
mm a[240:243],v[20:23],v[212:215],a[240:243]
v_fmac_f32 v123, v105, v105
v_lshlrev_b32 v106, 16, v81
mm a[32:35],v[20:23],v[220:223],a[32:35]
v_and_b32 v107, 0xffff0000, v81
v_sub_f32 v106, v106, v70
mm a[32:35],v[16:19],v[216:219],a[32:35]
v_sub_f32 v107, v107, v70
v_add_f32 v124, v106, v107
mm a[36:39],v[24:27],v[216:219],a[36:39]
v_fmac_f32 v122, v106, v106
v_add_f32 v120, v120, v124
mm a[36:39],v[28:31],v[220:223],a[36:39]
v_fmac_f32 v123, v107, v107
v_lshlrev_b32 v108, 16, v82
mm a[244:247],v[28:31],v[212:215],a[244:247]
v_and_b32 v109, 0xffff0000, v82
v_sub_f32 v108, v108, v70
mm a[244:247],v[24:27],v[208:211],a[244:247]
v_sub_f32 v109, v109, v70
v_add_f32 v124, v108, v109
mm a[28:31],v[24:27],v[200:203],a[28:31]
v_fmac_f32 v122, v108, v108
v_add_f32 v120, v120, v124
mm a[28:31],v[28:31],v[204:207],a[28:31]
v_fmac_f32 v123, v109, v109
v_lshlrev_b32 v110, 16, v83
mm a[92:95],v[28:31],v[196:199],a[92:95]
wt vmcnt(9) lgkmcnt(0)
s_barrier
mm a[92:95],v[24:27],v[192:195],a[92:95]
v_and_b32 v111, 0xffff0000, v83
v_sub_f32 v110, v110, v70
mm a[120:123],v[160:163],v[32:35],a[120:123]
v_sub_f32 v111, v111, v70
v_add_f32 v124, v110, v111
mm a[120:123],v[164:167],v[36:39],a[120:123]
v_fmac_f32 v122, v110, v110
v_add_f32 v120, v120, v124
)ASM" R"ASM(mm a[136:139],v[164:167],v[44:47],a[136:139]
rt v[192:193],v240 offset:0
rt v[194:195],v240 offset:1024
mm a[136:139],v[160:163],v[40:43],a[136:139]
rt v[196:197],v240 offset:16896
rt v[198:199],v240 offset:17920
mm a[224:227],v[160:163],v[48:51],a[224:227]
v_fmac_f32 v123, v111, v111
v_lshlrev_b32 v112, 16, v84
mm a[224:227],v[164:167],v[52:55],a[224:227]
v_and_b32 v113, 0xffff0000, v84
v_sub_f32 v112, v112, v70
mm a[144:147],v[164:167],v[60:63],a[144:147]
v_sub_f32 v113, v113, v70
v_add_f32 v124, v112, v113
mm a[144:147],v[160:163],v[56:59],a[144:147]
rd v[160:163],v236 offset:24576
rd v[164:167],v237 offset:24576
mm a[148:151],v[168:171],v[56:59],a[148:151]
v_fmac_f32 v122, v112, v112
v_add_f32 v120, v120, v124
mm a[148:151],v[172:175],v[60:63],a[148:151]
v_fmac_f32 v123, v113, v113
v_lshlrev_b32 v114, 16, v85
mm a[228:231],v[172:175],v[52:55],a[228:231]
v_and_b32 v115, 0xffff0000, v85
v_sub_f32 v114, v114, v70
mm a[228:231],v[168:171],v[48:51],a[228:231]
rt v[200:201],v241 offset:0
rt v[202:203],v241 offset:1024
mm a[140:143],v[168:171],v[40:43],a[140:143]
v_sub_f32 v115, v115, v70
v_add_f32 v124, v114, v115
mm a[140:143],v[172:175],v[44:47],a[140:143]
v_fmac_f32 v122, v114, v114
v_add_f32 v120, v120, v124
mm a[124:127],v[172:175],v[36:39],a[124:127]
v_fmac_f32 v123, v115, v115
v_lshlrev_b32 v116, 16, v86
mm a[124:127],v[168:171],v[32:35],a[124:127]
rd v[168:171],v236 offset:26624
rd v[172:175],v237 offset:26624
mm a[216:219],v[176:179],v[32:35],a[216:219]
v_and_b32 v117, 0xffff0000, v86
v_sub_f32 v116, v116, v70
mm a[216:219],v[180:183],v[36:39],a[216:219]
v_sub_f32 v117, v117, v70
v_add_f32 v124, v116, v117
mm a[168:171],v[180:183],v[44:47],a[168:171]
v_fmac_f32 v122, v116, v116
v_add_f32 v120, v120, v124
mm a[168:171],v[176:179],v[40:43],a[168:171]
rt v[204:205],v241 offset:16896
rt v[206:207],v241 offset:17920
mm a[192:195],v[176:179],v[48:51],a[192:195]
v_fmac_f32 v123, v117, v117
v_lshlrev_b32 v118, 16, v87
mm a[192:195],v[180:183],v[52:55],a[192:195]
v_and_b32 v119, 0xffff0000, v87
v_sub_f32 v118, v118, v70
mm a[176:179],v[180:183],v[60:63],a[176:179]
v_sub_f32 v119, v119, v70
v_add_f32 v124, v118, v119
mm a[176:179],v[176:179],v[56:59],a[176:179]
rd v[176:179],v236 offset:16384
rd v[180:183],v237 offset:16384
mm a[180:183],v[184:187],v[56:59],a[180:183]
v_fmac_f32 v122, v118, v118
v_add_f32 v120, v120, v124
mm a[180:183],v[188:191],v[60:63],a[180:183]
v_fmac_f32 v123, v119, v119
v_add_f32 v121, v122, v123
mm a[196:199],v[188:191],v[52:55],a[196:199]
v_add_f32_dpp v120, v120, v120 row_ror:8 row_mask:0xf bank_mask:0xf
mm a[196:199],v[184:187],v[48:51],a[196:199]
rt v[208:209],v242 offset:0
rt v[210:211],v242 offset:1024
mm a[172:175],v[184:187],v[40:43],a[172:175]
s_add_u32 m0, s83, 33792
v_add_f32_dpp v121, v121, v121 row_ror:8 row_mask:0xf bank_mask:0xf
mm a[172:175],v[188:191],v[44:47],a[172:175]
ld v232,s[44:47],s80 offen lds
mm a[220:223],v[188:191],v[36:39],a[220:223]
v_mov_b32 v124, v120
v_mov_b32 v125, v121
mm a[220:223],v[184:187],v[32:35],a[220:223]
rd v[184:187],v236 offset:18432
rd v[188:191],v237 offset:18432
mm a[72:75],v[0:3],v[32:35],a[72:75]
s_add_u32 m0, s83, 34816
v_permlane16_swap_b32 v124, v120
mm a[72:75],v[4:7],v[36:39],a[72:75]
ld v233,s[44:47],s80 offen lds
mm a[8:11],v[4:7],v[44:47],a[8:11]
v_permlane16_swap_b32 v125, v121
mm a[8:11],v[0:3],v[40:43],a[8:11]
rt v[212:213],v242 offset:16896
rt v[214:215],v242 offset:17920
mm a[80:83],v[0:3],v[48:51],a[80:83]
s_add_u32 m0, s83, 35968
)ASM" R"ASM(v_add_f32 v120, v120, v124
mm a[80:83],v[4:7],v[52:55],a[80:83]
ld v234,s[44:47],s80 offen lds
mm a[16:19],v[4:7],v[60:63],a[16:19]
v_add_f32 v121, v121, v125
v_mov_b32 v124, v120
mm a[16:19],v[0:3],v[56:59],a[16:19]
rd v[0:3],v236 offset:8192
rd v[4:7],v237 offset:8192
mm a[20:23],v[8:11],v[56:59],a[20:23]
s_add_u32 m0, s83, 36992
v_mov_b32 v125, v121
mm a[20:23],v[12:15],v[60:63],a[20:23]
ld v235,s[44:47],s80 offen lds
mm a[84:87],v[12:15],v[52:55],a[84:87]
s_mov_b32 s80, 3932160
v_permlane32_swap_b32 v124, v120
mm a[84:87],v[8:11],v[48:51],a[84:87]
rt v[216:217],v243 offset:0
rt v[218:219],v243 offset:1024
mm a[12:15],v[8:11],v[40:43],a[12:15]
s_add_u32 m0, s83, 50688
v_permlane32_swap_b32 v125, v121
mm a[12:15],v[12:15],v[44:47],a[12:15]
ld v232,s[44:47],s80 offen lds
mm a[76:79],v[12:15],v[36:39],a[76:79]
v_add_f32 v120, v120, v124
v_add_f32 v121, v121, v125
mm a[76:79],v[8:11],v[32:35],a[76:79]
rd v[8:11],v236 offset:10240
rd v[12:15],v237 offset:10240
mm a[248:251],v[16:19],v[32:35],a[248:251]
s_add_u32 m0, s83, 51712
ds_write_b64 v127, v[120:121]
mm a[248:251],v[20:23],v[36:39],a[248:251]
ld v233,s[44:47],s80 offen lds
mm a[40:43],v[20:23],v[44:47],a[40:43]
s_nop 0
v_readfirstlane_b32 s92, v252
mm a[40:43],v[16:19],v[40:43],a[40:43]
rt v[220:221],v243 offset:16896
rt v[222:223],v243 offset:17920
mm a[48:51],v[16:19],v[48:51],a[48:51]
s_add_u32 m0, s83, 52864
v_readfirstlane_b32 s93, v253
mm a[48:51],v[20:23],v[52:55],a[48:51]
ld v234,s[44:47],s80 offen lds
mm a[56:59],v[20:23],v[60:63],a[56:59]
s_add_u32 s92, s92, 1
s_addc_u32 s93, s93, 0
mm a[56:59],v[16:19],v[56:59],a[56:59]
rd v[16:19],v236 offset:0
rd v[20:23],v237 offset:0
mm a[60:63],v[24:27],v[56:59],a[60:63]
s_add_u32 m0, s83, 53888
v_mov_b32 v252, s92
mm a[60:63],v[28:31],v[60:63],a[60:63]
ld v235,s[44:47],s80 offen lds
mm a[52:55],v[28:31],v[52:55],a[52:55]
s_mov_b32 s80, 0
v_mov_b32 v253, s93
mm a[52:55],v[24:27],v[48:51],a[52:55]
v_mov_b32 v128, 0
v_mov_b32 v129, 0
mm a[44:47],v[24:27],v[40:43],a[44:47]
v_mov_b32 v130, 0
v_mov_b32 v131, 0
mm a[44:47],v[28:31],v[44:47],a[44:47]
st v[128:131],v255,s[52:55],s95 offen sc1
mm a[252:255],v[28:31],v[36:39],a[252:255]
wt lgkmcnt(2)
s_xor_b32 s93, s72, 0
mm a[252:255],v[24:27],v[32:35],a[252:255]
rd v[24:27],v236 offset:2048
rd v[28:31],v237 offset:2048
mm a[96:99],v[160:163],v[192:195],a[96:99]
s_lshl_b32 s92, s76, 5
s_add_u32 s92, s92, s93
mm a[96:99],v[164:167],v[196:199],a[96:99]
s_lshl2_add_u32 s92, s92, s3
s_lshl_b32 s92, s92, 10
mm a[104:107],v[164:167],v[204:207],a[104:107]
s_add_u32 s92, s92, 1048576
v_mov_b32 v128, -1
mm a[104:107],v[160:163],v[200:203],a[104:107]
rt v[32:33],v240 offset:128
rt v[34:35],v240 offset:1152
mm a[112:115],v[160:163],v[208:211],a[112:115]
v_mov_b32 v129, -1
v_mov_b32 v130, -1
mm a[112:115],v[164:167],v[212:215],a[112:115]
v_mov_b32 v131, -1
st v[128:131],v255,s[52:55],s92 offen sc1
mm a[128:131],v[164:167],v[220:223],a[128:131]
wt lgkmcnt(2)
s_barrier
mm a[128:131],v[160:163],v[216:219],a[128:131]
rd v[72:75],v126
rd v[76:79],v126 offset:16
mm a[132:135],v[168:171],v[216:219],a[132:135]
)ASM" R"ASM(s_xor_b32 m0, s82, 0
mm a[132:135],v[172:175],v[220:223],a[132:135]
ld v224,s[40:43],s79 offen lds
mm a[116:119],v[172:175],v[212:215],a[116:119]
mm a[116:119],v[168:171],v[208:211],a[116:119]
rt v[36:37],v240 offset:17024
rt v[38:39],v240 offset:18048
mm a[108:111],v[168:171],v[200:203],a[108:111]
s_xor_b32 m0, s82, 1024
mm a[108:111],v[172:175],v[204:207],a[108:111]
ld v225,s[40:43],s79 offen lds
mm a[100:103],v[172:175],v[196:199],a[100:103]
wt lgkmcnt(2)
v_add_f32 v72, v72, v74
mm a[100:103],v[168:171],v[192:195],a[100:103]
v_add_f32 v73, v73, v75
v_add_f32 v76, v76, v78
mm a[232:235],v[176:179],v[192:195],a[232:235]
s_xor_b32 m0, s82, 2048
v_add_f32 v77, v77, v79
mm a[232:235],v[180:183],v[196:199],a[232:235]
ld v226,s[40:43],s79 offen lds
mm a[152:155],v[180:183],v[204:207],a[152:155]
v_add_f32 v72, v72, v76
v_add_f32 v73, v73, v77
mm a[152:155],v[176:179],v[200:203],a[152:155]
rt v[40:41],v241 offset:128
rt v[42:43],v241 offset:1152
mm a[208:211],v[176:179],v[208:211],a[208:211]
s_xor_b32 m0, s82, 3072
v_mul_f32 v72, 0x3a800000, v72
mm a[208:211],v[180:183],v[212:215],a[208:211]
ld v227,s[40:43],s79 offen lds
mm a[160:163],v[180:183],v[220:223],a[160:163]
v_mul_f32 v73, 0x3a800000, v73
v_add_f32 v124, v70, v72
mm a[160:163],v[176:179],v[216:219],a[160:163]
v_fma_f32 v125, -v72, v72, v73
cv v124,v124,v124
mm a[164:167],v[184:187],v[216:219],a[164:167]
s_xor_b32 m0, s82, 4096
v_lshlrev_b32 v124, 16, v124
mm a[164:167],v[188:191],v[220:223],a[164:167]
ld v228,s[40:43],s79 offen lds
mm a[212:215],v[188:191],v[212:215],a[212:215]
v_max_f32 v125, 0, v125
cv v125,v125,v125
mm a[212:215],v[184:187],v[208:211],a[212:215]
rt v[44:45],v241 offset:17024
rt v[46:47],v241 offset:18048
mm a[156:159],v[184:187],v[200:203],a[156:159]
s_xor_b32 m0, s82, 5120
v_lshlrev_b32 v125, 16, v125
mm a[156:159],v[188:191],v[204:207],a[156:159]
ld v229,s[40:43],s79 offen lds
mm a[236:239],v[188:191],v[196:199],a[236:239]
v_add_f32 v125, 0x3727c5ac, v125
cv v125,v125,v125
mm a[236:239],v[184:187],v[192:195],a[236:239]
v_lshlrev_b32 v125, 16, v125
mm a[200:203],v[0:3],v[192:195],a[200:203]
s_xor_b32 m0, s82, 6144
mm a[200:203],v[4:7],v[196:199],a[200:203]
ld v230,s[40:43],s79 offen lds
mm a[184:187],v[4:7],v[204:207],a[184:187]
v_sqrt_f32 v125, v125
mm a[184:187],v[0:3],v[200:203],a[184:187]
rt v[48:49],v242 offset:128
rt v[50:51],v242 offset:1152
mm a[64:67],v[0:3],v[208:211],a[64:67]
s_xor_b32 m0, s82, 7168
v_sub_f32 v124, v70, v124
mm a[64:67],v[4:7],v[212:215],a[64:67]
ld v231,s[40:43],s79 offen lds
mm a[0:3],v[4:7],v[220:223],a[0:3]
s_mov_b32 s79, 128
cv v125,v125,v125
mm a[0:3],v[0:3],v[216:219],a[0:3]
v_lshlrev_b32 v125, 16, v125
mm a[4:7],v[8:11],v[216:219],a[4:7]
v_rcp_f32 v125, v125
mm a[4:7],v[12:15],v[220:223],a[4:7]
v_mul_f32 v125, 0x3e0293ee, v125
v_mul_f32 v124, v124, v125
mm a[68:71],v[12:15],v[212:215],a[68:71]
v_fma_f32 v88, v88, v125, v124
v_fma_f32 v89, v89, v125, v124
mm a[68:71],v[8:11],v[208:211],a[68:71]
rt v[52:53],v242 offset:17024
rt v[54:55],v242 offset:18048
mm a[188:191],v[8:11],v[200:203],a[188:191]
cv v72,v88,v89
v_fma_f32 v90, v90, v125, v124
mm a[188:191],v[12:15],v[204:207],a[188:191]
v_fma_f32 v91, v91, v125, v124
cv v73,v90,v91
mm a[204:207],v[12:15],v[196:199],a[204:207]
v_fma_f32 v92, v92, v125, v124
v_fma_f32 v93, v93, v125, v124
mm a[204:207],v[8:11],v[192:195],a[204:207]
)ASM" R"ASM(rt v[56:57],v243 offset:128
rt v[58:59],v243 offset:1152
mm a[88:91],v[16:19],v[192:195],a[88:91]
cv v74,v92,v93
v_fma_f32 v94, v94, v125, v124
mm a[88:91],v[20:23],v[196:199],a[88:91]
v_fma_f32 v95, v95, v125, v124
cv v75,v94,v95
mm a[24:27],v[20:23],v[204:207],a[24:27]
st v[72:75],v71,s[56:59],0 offen offset:0 sc1
mm a[24:27],v[16:19],v[200:203],a[24:27]
rt v[60:61],v243 offset:17024
rt v[62:63],v243 offset:18048
mm a[240:243],v[16:19],v[208:211],a[240:243]
v_fma_f32 v96, v96, v125, v124
v_fma_f32 v97, v97, v125, v124
mm a[240:243],v[20:23],v[212:215],a[240:243]
cv v76,v96,v97
v_fma_f32 v98, v98, v125, v124
mm a[32:35],v[20:23],v[220:223],a[32:35]
v_fma_f32 v99, v99, v125, v124
cv v77,v98,v99
mm a[32:35],v[16:19],v[216:219],a[32:35]
v_fma_f32 v100, v100, v125, v124
v_fma_f32 v101, v101, v125, v124
mm a[36:39],v[24:27],v[216:219],a[36:39]
cv v78,v100,v101
v_fma_f32 v102, v102, v125, v124
mm a[36:39],v[28:31],v[220:223],a[36:39]
v_fma_f32 v103, v103, v125, v124
cv v79,v102,v103
mm a[244:247],v[28:31],v[212:215],a[244:247]
st v[76:79],v71,s[56:59],0 offen offset:2048 sc1
mm a[244:247],v[24:27],v[208:211],a[244:247]
v_fma_f32 v104, v104, v125, v124
v_fma_f32 v105, v105, v125, v124
mm a[28:31],v[24:27],v[200:203],a[28:31]
cv v80,v104,v105
v_fma_f32 v106, v106, v125, v124
mm a[28:31],v[28:31],v[204:207],a[28:31]
v_fma_f32 v107, v107, v125, v124
cv v81,v106,v107
mm a[92:95],v[28:31],v[196:199],a[92:95]
wt vmcnt(12) lgkmcnt(0)
s_barrier
mm a[92:95],v[24:27],v[192:195],a[92:95]
v_fma_f32 v108, v108, v125, v124
v_fma_f32 v109, v109, v125, v124
mm a[120:123],v[160:163],v[32:35],a[120:123]
cv v82,v108,v109
v_fma_f32 v110, v110, v125, v124
mm a[120:123],v[164:167],v[36:39],a[120:123]
v_fma_f32 v111, v111, v125, v124
cv v83,v110,v111
mm a[136:139],v[164:167],v[44:47],a[136:139]
rt v[192:193],v240 offset:33792
rt v[194:195],v240 offset:34816
mm a[136:139],v[160:163],v[40:43],a[136:139]
rt v[196:197],v240 offset:50688
rt v[198:199],v240 offset:51712
mm a[224:227],v[160:163],v[48:51],a[224:227]
st v[80:83],v71,s[56:59],s88 offen offset:0 sc1
mm a[224:227],v[164:167],v[52:55],a[224:227]
v_fma_f32 v112, v112, v125, v124
v_fma_f32 v113, v113, v125, v124
mm a[144:147],v[164:167],v[60:63],a[144:147]
cv v84,v112,v113
v_fma_f32 v114, v114, v125, v124
mm a[144:147],v[160:163],v[56:59],a[144:147]
rd v[160:163],v236 offset:57344
rd v[164:167],v237 offset:57344
mm a[148:151],v[168:171],v[56:59],a[148:151]
v_fma_f32 v115, v115, v125, v124
cv v85,v114,v115
mm a[148:151],v[172:175],v[60:63],a[148:151]
v_fma_f32 v116, v116, v125, v124
v_fma_f32 v117, v117, v125, v124
mm a[228:231],v[172:175],v[52:55],a[228:231]
cv v86,v116,v117
v_fma_f32 v118, v118, v125, v124
mm a[228:231],v[168:171],v[48:51],a[228:231]
rt v[200:201],v241 offset:33792
rt v[202:203],v241 offset:34816
mm a[140:143],v[168:171],v[40:43],a[140:143]
v_fma_f32 v119, v119, v125, v124
cv v87,v118,v119
mm a[140:143],v[172:175],v[44:47],a[140:143]
st v[84:87],v71,s[56:59],s88 offen offset:2048 sc1
mm a[124:127],v[172:175],v[36:39],a[124:127]
mm a[124:127],v[168:171],v[32:35],a[124:127]
rd v[168:171],v236 offset:59392
rd v[172:175],v237 offset:59392
mm a[216:219],v[176:179],v[32:35],a[216:219]
mm a[216:219],v[180:183],v[36:39],a[216:219]
mm a[168:171],v[180:183],v[44:47],a[168:171]
mm a[168:171],v[176:179],v[40:43],a[168:171]
rt v[204:205],v241 offset:50688
rt v[206:207],v241 offset:51712
mm a[192:195],v[176:179],v[48:51],a[192:195]
mm a[192:195],v[180:183],v[52:55],a[192:195]
)ASM" R"ASM(mm a[176:179],v[180:183],v[60:63],a[176:179]
mm a[176:179],v[176:179],v[56:59],a[176:179]
rd v[176:179],v236 offset:49152
rd v[180:183],v237 offset:49152
mm a[180:183],v[184:187],v[56:59],a[180:183]
mm a[180:183],v[188:191],v[60:63],a[180:183]
mm a[196:199],v[188:191],v[52:55],a[196:199]
mm a[196:199],v[184:187],v[48:51],a[196:199]
rt v[208:209],v242 offset:33792
rt v[210:211],v242 offset:34816
mm a[172:175],v[184:187],v[40:43],a[172:175]
s_add_u32 m0, s83, 0
mm a[172:175],v[188:191],v[44:47],a[172:175]
ld v232,s[44:47],s80 offen lds
mm a[220:223],v[188:191],v[36:39],a[220:223]
mm a[220:223],v[184:187],v[32:35],a[220:223]
rd v[184:187],v236 offset:51200
rd v[188:191],v237 offset:51200
mm a[72:75],v[0:3],v[32:35],a[72:75]
s_add_u32 m0, s83, 1024
mm a[72:75],v[4:7],v[36:39],a[72:75]
ld v233,s[44:47],s80 offen lds
mm a[8:11],v[4:7],v[44:47],a[8:11]
mm a[8:11],v[0:3],v[40:43],a[8:11]
rt v[212:213],v242 offset:50688
rt v[214:215],v242 offset:51712
mm a[80:83],v[0:3],v[48:51],a[80:83]
s_add_u32 m0, s83, 2176
mm a[80:83],v[4:7],v[52:55],a[80:83]
ld v234,s[44:47],s80 offen lds
mm a[16:19],v[4:7],v[60:63],a[16:19]
mm a[16:19],v[0:3],v[56:59],a[16:19]
rd v[0:3],v236 offset:40960
rd v[4:7],v237 offset:40960
mm a[20:23],v[8:11],v[56:59],a[20:23]
s_add_u32 m0, s83, 3200
mm a[20:23],v[12:15],v[60:63],a[20:23]
ld v235,s[44:47],s80 offen lds
mm a[84:87],v[12:15],v[52:55],a[84:87]
.La5_LW1_6:
s_cbranch_execz .La5_LW1_10
s_mov_b32 s80, 262144
mm a[84:87],v[8:11],v[48:51],a[84:87]
rt v[216:217],v243 offset:33792
rt v[218:219],v243 offset:34816
mm a[12:15],v[8:11],v[40:43],a[12:15]
s_add_u32 m0, s83, 16896
mm a[12:15],v[12:15],v[44:47],a[12:15]
ld v232,s[44:47],s80 offen lds
mm a[76:79],v[12:15],v[36:39],a[76:79]
mm a[76:79],v[8:11],v[32:35],a[76:79]
rd v[8:11],v236 offset:43008
rd v[12:15],v237 offset:43008
mm a[248:251],v[16:19],v[32:35],a[248:251]
s_add_u32 m0, s83, 17920
mm a[248:251],v[20:23],v[36:39],a[248:251]
ld v233,s[44:47],s80 offen lds
mm a[40:43],v[20:23],v[44:47],a[40:43]
mm a[40:43],v[16:19],v[40:43],a[40:43]
rt v[220:221],v243 offset:50688
rt v[222:223],v243 offset:51712
mm a[48:51],v[16:19],v[48:51],a[48:51]
s_add_u32 m0, s83, 19072
mm a[48:51],v[20:23],v[52:55],a[48:51]
ld v234,s[44:47],s80 offen lds
mm a[56:59],v[20:23],v[60:63],a[56:59]
mm a[56:59],v[16:19],v[56:59],a[56:59]
rd v[16:19],v236 offset:32768
rd v[20:23],v237 offset:32768
mm a[60:63],v[24:27],v[56:59],a[60:63]
s_add_u32 m0, s83, 20096
mm a[60:63],v[28:31],v[60:63],a[60:63]
ld v235,s[44:47],s80 offen lds
mm a[52:55],v[28:31],v[52:55],a[52:55]
s_mov_b32 s80, 524288
mm a[52:55],v[24:27],v[48:51],a[52:55]
mm a[44:47],v[24:27],v[40:43],a[44:47]
mm a[44:47],v[28:31],v[44:47],a[44:47]
mm a[252:255],v[28:31],v[36:39],a[252:255]
wt lgkmcnt(2)
mm a[252:255],v[24:27],v[32:35],a[252:255]
rd v[24:27],v236 offset:34816
rd v[28:31],v237 offset:34816
mm a[96:99],v[160:163],v[192:195],a[96:99]
mm a[96:99],v[164:167],v[196:199],a[96:99]
mm a[104:107],v[164:167],v[204:207],a[104:107]
mm a[104:107],v[160:163],v[200:203],a[104:107]
rt v[32:33],v240 offset:33920
rt v[34:35],v240 offset:34944
mm a[112:115],v[160:163],v[208:211],a[112:115]
mm a[112:115],v[164:167],v[212:215],a[112:115]
mm a[128:131],v[164:167],v[220:223],a[128:131]
wt lgkmcnt(2)
s_barrier
mm a[128:131],v[160:163],v[216:219],a[128:131]
mm a[132:135],v[168:171],v[216:219],a[132:135]
s_xor_b32 m0, s82, 32768
mm a[132:135],v[172:175],v[220:223],a[132:135]
ld v224,s[40:43],s79 offen lds
mm a[116:119],v[172:175],v[212:215],a[116:119]
)ASM" R"ASM(mm a[116:119],v[168:171],v[208:211],a[116:119]
rt v[36:37],v240 offset:50816
rt v[38:39],v240 offset:51840
mm a[108:111],v[168:171],v[200:203],a[108:111]
s_xor_b32 m0, s82, 33792
mm a[108:111],v[172:175],v[204:207],a[108:111]
ld v225,s[40:43],s79 offen lds
mm a[100:103],v[172:175],v[196:199],a[100:103]
mm a[100:103],v[168:171],v[192:195],a[100:103]
mm a[232:235],v[176:179],v[192:195],a[232:235]
s_xor_b32 m0, s82, 34816
mm a[232:235],v[180:183],v[196:199],a[232:235]
ld v226,s[40:43],s79 offen lds
mm a[152:155],v[180:183],v[204:207],a[152:155]
mm a[152:155],v[176:179],v[200:203],a[152:155]
rt v[40:41],v241 offset:33920
rt v[42:43],v241 offset:34944
mm a[208:211],v[176:179],v[208:211],a[208:211]
s_xor_b32 m0, s82, 35840
mm a[208:211],v[180:183],v[212:215],a[208:211]
ld v227,s[40:43],s79 offen lds
mm a[160:163],v[180:183],v[220:223],a[160:163]
mm a[160:163],v[176:179],v[216:219],a[160:163]
mm a[164:167],v[184:187],v[216:219],a[164:167]
s_xor_b32 m0, s82, 36864
mm a[164:167],v[188:191],v[220:223],a[164:167]
ld v228,s[40:43],s79 offen lds
mm a[212:215],v[188:191],v[212:215],a[212:215]
mm a[212:215],v[184:187],v[208:211],a[212:215]
rt v[44:45],v241 offset:50816
rt v[46:47],v241 offset:51840
mm a[156:159],v[184:187],v[200:203],a[156:159]
s_xor_b32 m0, s82, 37888
mm a[156:159],v[188:191],v[204:207],a[156:159]
ld v229,s[40:43],s79 offen lds
mm a[236:239],v[188:191],v[196:199],a[236:239]
mm a[236:239],v[184:187],v[192:195],a[236:239]
mm a[200:203],v[0:3],v[192:195],a[200:203]
s_xor_b32 m0, s82, 38912
mm a[200:203],v[4:7],v[196:199],a[200:203]
ld v230,s[40:43],s79 offen lds
mm a[184:187],v[4:7],v[204:207],a[184:187]
mm a[184:187],v[0:3],v[200:203],a[184:187]
rt v[48:49],v242 offset:33920
rt v[50:51],v242 offset:34944
mm a[64:67],v[0:3],v[208:211],a[64:67]
s_xor_b32 m0, s82, 39936
mm a[64:67],v[4:7],v[212:215],a[64:67]
ld v231,s[40:43],s79 offen lds
mm a[0:3],v[4:7],v[220:223],a[0:3]
s_mov_b32 s79, 256
mm a[0:3],v[0:3],v[216:219],a[0:3]
mm a[4:7],v[8:11],v[216:219],a[4:7]
mm a[4:7],v[12:15],v[220:223],a[4:7]
buffer_load_dword v254, v135, s[68:71], s96 offen
mm a[68:71],v[12:15],v[212:215],a[68:71]
s_add_u32 s96, s96, 1048576
mm a[68:71],v[8:11],v[208:211],a[68:71]
rt v[52:53],v242 offset:50816
rt v[54:55],v242 offset:51840
mm a[188:191],v[8:11],v[200:203],a[188:191]
mm a[188:191],v[12:15],v[204:207],a[188:191]
buffer_load_dword v254, v135, s[68:71], s96 offen
mm a[204:207],v[12:15],v[196:199],a[204:207]
s_add_u32 s96, s96, 1048576
mm a[204:207],v[8:11],v[192:195],a[204:207]
rt v[56:57],v243 offset:33920
rt v[58:59],v243 offset:34944
mm a[88:91],v[16:19],v[192:195],a[88:91]
mm a[88:91],v[20:23],v[196:199],a[88:91]
wt vmcnt(18)
buffer_store_dwordx2 v[252:253], off, s[64:67], s91 offset:1024 sc1
mm a[24:27],v[20:23],v[204:207],a[24:27]
s_mov_b32 s91, 0
s_lshl_b32 s92, s76, 19
mm a[24:27],v[16:19],v[200:203],a[24:27]
rt v[60:61],v243 offset:50816
rt v[62:63],v243 offset:51840
mm a[240:243],v[16:19],v[208:211],a[240:243]
s_add_u32 s92, s92, 88080384
mm a[240:243],v[20:23],v[212:215],a[240:243]
s_add_u32 s52, s26, s92
s_addc_u32 s53, s27, 0
mm a[32:35],v[20:23],v[220:223],a[32:35]
s_and_b32 s53, s53, 0xffff
s_mov_b32 s54, 524672
mm a[32:35],v[16:19],v[216:219],a[32:35]
s_mov_b32 s55, 0x20000
s_lshl_b32 s92, s3, 16
mm a[36:39],v[24:27],v[216:219],a[36:39]
s_sub_u32 s93, 458752, s92
v_add_u32 v244, s92, v136
mm a[36:39],v[28:31],v[220:223],a[36:39]
v_add_u32 v246, s93, v136
v_add_u32 v245, 0x8000, v244
mm a[244:247],v[28:31],v[212:215],a[244:247]
v_add_u32 v247, 0x8000, v246
s_lshl_b32 s94, s72, 19
mm a[244:247],v[24:27],v[208:211],a[244:247]
s_lshl_b32 s93, s76, 16
)ASM" R"ASM(s_add_u32 s92, s94, s93
mm a[28:31],v[24:27],v[200:203],a[28:31]
s_lshl_b32 s93, s77, 13
s_add_u32 s92, s92, s93
mm a[28:31],v[28:31],v[204:207],a[28:31]
s_add_u32 s92, s92, 37748736
mm a[92:95],v[28:31],v[196:199],a[92:95]
wt vmcnt(11) lgkmcnt(0)
s_barrier
mm a[92:95],v[24:27],v[192:195],a[92:95]
s_add_u32 s48, s26, s92
s_addc_u32 s49, s27, 0
mm a[120:123],v[160:163],v[32:35],a[120:123]
s_and_b32 s49, s49, 0xffff
s_mov_b32 s50, 57344
mm a[120:123],v[164:167],v[36:39],a[120:123]
s_mov_b32 s51, 0x20000
s_lshl_b32 s93, s77, 24
mm a[136:139],v[164:167],v[44:47],a[136:139]
rt v[192:193],v240 offset:0
rt v[194:195],v240 offset:1024
mm a[136:139],v[160:163],v[40:43],a[136:139]
rt v[196:197],v240 offset:16896
rt v[198:199],v240 offset:17920
mm a[224:227],v[160:163],v[48:51],a[224:227]
s_add_u32 s92, s94, s93
s_add_u32 s92, s92, 20975616
mm a[224:227],v[164:167],v[52:55],a[224:227]
s_add_u32 s60, s26, s92
s_addc_u32 s61, s27, 0
mm a[144:147],v[164:167],v[60:63],a[144:147]
s_and_b32 s61, s61, 0xffff
s_mov_b32 s62, 520192
mm a[144:147],v[160:163],v[56:59],a[144:147]
rd v[160:163],v236 offset:24576
rd v[164:167],v237 offset:24576
mm a[148:151],v[168:171],v[56:59],a[148:151]
s_mov_b32 s63, 0x20000
s_lshl_b32 s92, s77, 14
mm a[148:151],v[172:175],v[60:63],a[148:151]
s_sub_u32 s95, 24576, s92
v_add_u32 v238, s95, v255
mm a[228:231],v[172:175],v[52:55],a[228:231]
mm a[228:231],v[168:171],v[48:51],a[228:231]
rt v[200:201],v241 offset:0
rt v[202:203],v241 offset:1024
mm a[140:143],v[168:171],v[40:43],a[140:143]
mm a[140:143],v[172:175],v[44:47],a[140:143]
mm a[124:127],v[172:175],v[36:39],a[124:127]
mm a[124:127],v[168:171],v[32:35],a[124:127]
rd v[168:171],v236 offset:26624
rd v[172:175],v237 offset:26624
mm a[216:219],v[176:179],v[32:35],a[216:219]
mm a[216:219],v[180:183],v[36:39],a[216:219]
mm a[168:171],v[180:183],v[44:47],a[168:171]
mm a[168:171],v[176:179],v[40:43],a[168:171]
rt v[204:205],v241 offset:16896
rt v[206:207],v241 offset:17920
mm a[192:195],v[176:179],v[48:51],a[192:195]
mm a[192:195],v[180:183],v[52:55],a[192:195]
mm a[176:179],v[180:183],v[60:63],a[176:179]
mm a[176:179],v[176:179],v[56:59],a[176:179]
rd v[176:179],v236 offset:16384
rd v[180:183],v237 offset:16384
mm a[180:183],v[184:187],v[56:59],a[180:183]
mm a[180:183],v[188:191],v[60:63],a[180:183]
mm a[196:199],v[188:191],v[52:55],a[196:199]
mm a[196:199],v[184:187],v[48:51],a[196:199]
rt v[208:209],v242 offset:0
rt v[210:211],v242 offset:1024
mm a[172:175],v[184:187],v[40:43],a[172:175]
s_add_u32 m0, s83, 33792
mm a[172:175],v[188:191],v[44:47],a[172:175]
ld v232,s[44:47],s80 offen lds
mm a[220:223],v[188:191],v[36:39],a[220:223]
mm a[220:223],v[184:187],v[32:35],a[220:223]
rd v[184:187],v236 offset:18432
rd v[188:191],v237 offset:18432
mm a[72:75],v[0:3],v[32:35],a[72:75]
s_add_u32 m0, s83, 34816
mm a[72:75],v[4:7],v[36:39],a[72:75]
ld v233,s[44:47],s80 offen lds
mm a[8:11],v[4:7],v[44:47],a[8:11]
mm a[8:11],v[0:3],v[40:43],a[8:11]
rt v[212:213],v242 offset:16896
rt v[214:215],v242 offset:17920
mm a[80:83],v[0:3],v[48:51],a[80:83]
s_add_u32 m0, s83, 35968
mm a[80:83],v[4:7],v[52:55],a[80:83]
ld v234,s[44:47],s80 offen lds
mm a[16:19],v[4:7],v[60:63],a[16:19]
mm a[16:19],v[0:3],v[56:59],a[16:19]
rd v[0:3],v236 offset:8192
rd v[4:7],v237 offset:8192
mm a[20:23],v[8:11],v[56:59],a[20:23]
s_add_u32 m0, s83, 36992
mm a[20:23],v[12:15],v[60:63],a[20:23]
ld v235,s[44:47],s80 offen lds
mm a[84:87],v[12:15],v[52:55],a[84:87]
s_mov_b32 s80, 786432
)ASM" R"ASM(mm a[84:87],v[8:11],v[48:51],a[84:87]
rt v[216:217],v243 offset:0
rt v[218:219],v243 offset:1024
mm a[12:15],v[8:11],v[40:43],a[12:15]
s_add_u32 m0, s83, 50688
mm a[12:15],v[12:15],v[44:47],a[12:15]
ld v232,s[44:47],s80 offen lds
mm a[76:79],v[12:15],v[36:39],a[76:79]
mm a[76:79],v[8:11],v[32:35],a[76:79]
rd v[8:11],v236 offset:10240
rd v[12:15],v237 offset:10240
mm a[248:251],v[16:19],v[32:35],a[248:251]
s_add_u32 m0, s83, 51712
mm a[248:251],v[20:23],v[36:39],a[248:251]
ld v233,s[44:47],s80 offen lds
mm a[40:43],v[20:23],v[44:47],a[40:43]
mm a[40:43],v[16:19],v[40:43],a[40:43]
rt v[220:221],v243 offset:16896
rt v[222:223],v243 offset:17920
mm a[48:51],v[16:19],v[48:51],a[48:51]
s_add_u32 m0, s83, 52864
mm a[48:51],v[20:23],v[52:55],a[48:51]
ld v234,s[44:47],s80 offen lds
mm a[56:59],v[20:23],v[60:63],a[56:59]
mm a[56:59],v[16:19],v[56:59],a[56:59]
rd v[16:19],v236 offset:0
rd v[20:23],v237 offset:0
mm a[60:63],v[24:27],v[56:59],a[60:63]
s_add_u32 m0, s83, 53888
mm a[60:63],v[28:31],v[60:63],a[60:63]
ld v235,s[44:47],s80 offen lds
mm a[52:55],v[28:31],v[52:55],a[52:55]
s_mov_b32 s80, 1048576
mm a[52:55],v[24:27],v[48:51],a[52:55]
mm a[44:47],v[24:27],v[40:43],a[44:47]
mm a[44:47],v[28:31],v[44:47],a[44:47]
mm a[252:255],v[28:31],v[36:39],a[252:255]
wt lgkmcnt(2)
mm a[252:255],v[24:27],v[32:35],a[252:255]
rd v[24:27],v236 offset:2048
rd v[28:31],v237 offset:2048
mm a[96:99],v[160:163],v[192:195],a[96:99]
mm a[96:99],v[164:167],v[196:199],a[96:99]
mm a[104:107],v[164:167],v[204:207],a[104:107]
mm a[104:107],v[160:163],v[200:203],a[104:107]
rt v[32:33],v240 offset:128
rt v[34:35],v240 offset:1152
mm a[112:115],v[160:163],v[208:211],a[112:115]
mm a[112:115],v[164:167],v[212:215],a[112:115]
mm a[128:131],v[164:167],v[220:223],a[128:131]
wt lgkmcnt(2)
s_barrier
mm a[128:131],v[160:163],v[216:219],a[128:131]
mm a[132:135],v[168:171],v[216:219],a[132:135]
s_xor_b32 m0, s82, 0
mm a[132:135],v[172:175],v[220:223],a[132:135]
ld v224,s[40:43],s79 offen lds
mm a[116:119],v[172:175],v[212:215],a[116:119]
mm a[116:119],v[168:171],v[208:211],a[116:119]
rt v[36:37],v240 offset:17024
rt v[38:39],v240 offset:18048
mm a[108:111],v[168:171],v[200:203],a[108:111]
s_xor_b32 m0, s82, 1024
mm a[108:111],v[172:175],v[204:207],a[108:111]
ld v225,s[40:43],s79 offen lds
mm a[100:103],v[172:175],v[196:199],a[100:103]
mm a[100:103],v[168:171],v[192:195],a[100:103]
mm a[232:235],v[176:179],v[192:195],a[232:235]
s_xor_b32 m0, s82, 2048
mm a[232:235],v[180:183],v[196:199],a[232:235]
ld v226,s[40:43],s79 offen lds
mm a[152:155],v[180:183],v[204:207],a[152:155]
mm a[152:155],v[176:179],v[200:203],a[152:155]
rt v[40:41],v241 offset:128
rt v[42:43],v241 offset:1152
mm a[208:211],v[176:179],v[208:211],a[208:211]
s_xor_b32 m0, s82, 3072
mm a[208:211],v[180:183],v[212:215],a[208:211]
ld v227,s[40:43],s79 offen lds
mm a[160:163],v[180:183],v[220:223],a[160:163]
mm a[160:163],v[176:179],v[216:219],a[160:163]
mm a[164:167],v[184:187],v[216:219],a[164:167]
s_xor_b32 m0, s82, 4096
mm a[164:167],v[188:191],v[220:223],a[164:167]
ld v228,s[40:43],s79 offen lds
mm a[212:215],v[188:191],v[212:215],a[212:215]
mm a[212:215],v[184:187],v[208:211],a[212:215]
rt v[44:45],v241 offset:17024
rt v[46:47],v241 offset:18048
mm a[156:159],v[184:187],v[200:203],a[156:159]
s_xor_b32 m0, s82, 5120
mm a[156:159],v[188:191],v[204:207],a[156:159]
ld v229,s[40:43],s79 offen lds
mm a[236:239],v[188:191],v[196:199],a[236:239]
mm a[236:239],v[184:187],v[192:195],a[236:239]
mm a[200:203],v[0:3],v[192:195],a[200:203]
s_xor_b32 m0, s82, 6144
mm a[200:203],v[4:7],v[196:199],a[200:203]
ld v230,s[40:43],s79 offen lds
mm a[184:187],v[4:7],v[204:207],a[184:187]
)ASM" R"ASM(mm a[184:187],v[0:3],v[200:203],a[184:187]
rt v[48:49],v242 offset:128
rt v[50:51],v242 offset:1152
mm a[64:67],v[0:3],v[208:211],a[64:67]
s_xor_b32 m0, s82, 7168
mm a[64:67],v[4:7],v[212:215],a[64:67]
ld v231,s[40:43],s79 offen lds
mm a[0:3],v[4:7],v[220:223],a[0:3]
s_mov_b32 s79, 384
mm a[0:3],v[0:3],v[216:219],a[0:3]
mm a[4:7],v[8:11],v[216:219],a[4:7]
mm a[4:7],v[12:15],v[220:223],a[4:7]
buffer_load_dword v254, v135, s[68:71], s96 offen
mm a[68:71],v[12:15],v[212:215],a[68:71]
s_add_u32 s96, s96, 1048576
mm a[68:71],v[8:11],v[208:211],a[68:71]
rt v[52:53],v242 offset:17024
rt v[54:55],v242 offset:18048
mm a[188:191],v[8:11],v[200:203],a[188:191]
mm a[188:191],v[12:15],v[204:207],a[188:191]
buffer_load_dword v254, v135, s[68:71], s96 offen
mm a[204:207],v[12:15],v[196:199],a[204:207]
s_add_u32 s96, s96, 1048576
mm a[204:207],v[8:11],v[192:195],a[204:207]
rt v[56:57],v243 offset:128
rt v[58:59],v243 offset:1152
mm a[88:91],v[16:19],v[192:195],a[88:91]
mm a[88:91],v[20:23],v[196:199],a[88:91]
mm a[24:27],v[20:23],v[204:207],a[24:27]
mm a[24:27],v[16:19],v[200:203],a[24:27]
rt v[60:61],v243 offset:17024
rt v[62:63],v243 offset:18048
mm a[240:243],v[16:19],v[208:211],a[240:243]
mm a[240:243],v[20:23],v[212:215],a[240:243]
mm a[32:35],v[20:23],v[220:223],a[32:35]
mm a[32:35],v[16:19],v[216:219],a[32:35]
mm a[36:39],v[24:27],v[216:219],a[36:39]
mm a[36:39],v[28:31],v[220:223],a[36:39]
mm a[244:247],v[28:31],v[212:215],a[244:247]
mm a[244:247],v[24:27],v[208:211],a[244:247]
mm a[28:31],v[24:27],v[200:203],a[28:31]
mm a[28:31],v[28:31],v[204:207],a[28:31]
mm a[92:95],v[28:31],v[196:199],a[92:95]
wt vmcnt(10) lgkmcnt(0)
s_barrier
mm a[92:95],v[24:27],v[192:195],a[92:95]
mm a[120:123],v[160:163],v[32:35],a[120:123]
mm a[120:123],v[164:167],v[36:39],a[120:123]
mm a[136:139],v[164:167],v[44:47],a[136:139]
rt v[192:193],v240 offset:33792
rt v[194:195],v240 offset:34816
mm a[136:139],v[160:163],v[40:43],a[136:139]
rt v[196:197],v240 offset:50688
rt v[198:199],v240 offset:51712
mm a[224:227],v[160:163],v[48:51],a[224:227]
mm a[224:227],v[164:167],v[52:55],a[224:227]
mm a[144:147],v[164:167],v[60:63],a[144:147]
mm a[144:147],v[160:163],v[56:59],a[144:147]
rd v[160:163],v236 offset:57344
rd v[164:167],v237 offset:57344
mm a[148:151],v[168:171],v[56:59],a[148:151]
mm a[148:151],v[172:175],v[60:63],a[148:151]
mm a[228:231],v[172:175],v[52:55],a[228:231]
mm a[228:231],v[168:171],v[48:51],a[228:231]
rt v[200:201],v241 offset:33792
rt v[202:203],v241 offset:34816
mm a[140:143],v[168:171],v[40:43],a[140:143]
mm a[140:143],v[172:175],v[44:47],a[140:143]
mm a[124:127],v[172:175],v[36:39],a[124:127]
mm a[124:127],v[168:171],v[32:35],a[124:127]
rd v[168:171],v236 offset:59392
rd v[172:175],v237 offset:59392
mm a[216:219],v[176:179],v[32:35],a[216:219]
mm a[216:219],v[180:183],v[36:39],a[216:219]
mm a[168:171],v[180:183],v[44:47],a[168:171]
mm a[168:171],v[176:179],v[40:43],a[168:171]
rt v[204:205],v241 offset:50688
rt v[206:207],v241 offset:51712
mm a[192:195],v[176:179],v[48:51],a[192:195]
mm a[192:195],v[180:183],v[52:55],a[192:195]
mm a[176:179],v[180:183],v[60:63],a[176:179]
mm a[176:179],v[176:179],v[56:59],a[176:179]
rd v[176:179],v236 offset:49152
rd v[180:183],v237 offset:49152
mm a[180:183],v[184:187],v[56:59],a[180:183]
mm a[180:183],v[188:191],v[60:63],a[180:183]
mm a[196:199],v[188:191],v[52:55],a[196:199]
mm a[196:199],v[184:187],v[48:51],a[196:199]
rt v[208:209],v242 offset:33792
rt v[210:211],v242 offset:34816
mm a[172:175],v[184:187],v[40:43],a[172:175]
s_add_u32 m0, s83, 0
mm a[172:175],v[188:191],v[44:47],a[172:175]
ld v232,s[44:47],s80 offen lds
mm a[220:223],v[188:191],v[36:39],a[220:223]
mm a[220:223],v[184:187],v[32:35],a[220:223]
rd v[184:187],v236 offset:51200
rd v[188:191],v237 offset:51200
mm a[72:75],v[0:3],v[32:35],a[72:75]
s_add_u32 m0, s83, 1024
)ASM" R"ASM(mm a[72:75],v[4:7],v[36:39],a[72:75]
ld v233,s[44:47],s80 offen lds
mm a[8:11],v[4:7],v[44:47],a[8:11]
mm a[8:11],v[0:3],v[40:43],a[8:11]
rt v[212:213],v242 offset:50688
rt v[214:215],v242 offset:51712
mm a[80:83],v[0:3],v[48:51],a[80:83]
s_add_u32 m0, s83, 2176
mm a[80:83],v[4:7],v[52:55],a[80:83]
ld v234,s[44:47],s80 offen lds
mm a[16:19],v[4:7],v[60:63],a[16:19]
mm a[16:19],v[0:3],v[56:59],a[16:19]
rd v[0:3],v236 offset:40960
rd v[4:7],v237 offset:40960
mm a[20:23],v[8:11],v[56:59],a[20:23]
s_add_u32 m0, s83, 3200
mm a[20:23],v[12:15],v[60:63],a[20:23]
ld v235,s[44:47],s80 offen lds
mm a[84:87],v[12:15],v[52:55],a[84:87]
s_mov_b32 s80, 1310720
mm a[84:87],v[8:11],v[48:51],a[84:87]
rt v[216:217],v243 offset:33792
rt v[218:219],v243 offset:34816
mm a[12:15],v[8:11],v[40:43],a[12:15]
s_add_u32 m0, s83, 16896
mm a[12:15],v[12:15],v[44:47],a[12:15]
ld v232,s[44:47],s80 offen lds
mm a[76:79],v[12:15],v[36:39],a[76:79]
mm a[76:79],v[8:11],v[32:35],a[76:79]
rd v[8:11],v236 offset:43008
rd v[12:15],v237 offset:43008
mm a[248:251],v[16:19],v[32:35],a[248:251]
s_add_u32 m0, s83, 17920
mm a[248:251],v[20:23],v[36:39],a[248:251]
ld v233,s[44:47],s80 offen lds
mm a[40:43],v[20:23],v[44:47],a[40:43]
mm a[40:43],v[16:19],v[40:43],a[40:43]
rt v[220:221],v243 offset:50688
rt v[222:223],v243 offset:51712
mm a[48:51],v[16:19],v[48:51],a[48:51]
s_add_u32 m0, s83, 19072
mm a[48:51],v[20:23],v[52:55],a[48:51]
ld v234,s[44:47],s80 offen lds
mm a[56:59],v[20:23],v[60:63],a[56:59]
mm a[56:59],v[16:19],v[56:59],a[56:59]
rd v[16:19],v236 offset:32768
rd v[20:23],v237 offset:32768
mm a[60:63],v[24:27],v[56:59],a[60:63]
s_add_u32 m0, s83, 20096
mm a[60:63],v[28:31],v[60:63],a[60:63]
ld v235,s[44:47],s80 offen lds
mm a[52:55],v[28:31],v[52:55],a[52:55]
s_mov_b32 s80, 1572864
mm a[52:55],v[24:27],v[48:51],a[52:55]
mm a[44:47],v[24:27],v[40:43],a[44:47]
mm a[44:47],v[28:31],v[44:47],a[44:47]
mm a[252:255],v[28:31],v[36:39],a[252:255]
wt lgkmcnt(2)
mm a[252:255],v[24:27],v[32:35],a[252:255]
rd v[24:27],v236 offset:34816
rd v[28:31],v237 offset:34816
mm a[96:99],v[160:163],v[192:195],a[96:99]
mm a[96:99],v[164:167],v[196:199],a[96:99]
mm a[104:107],v[164:167],v[204:207],a[104:107]
mm a[104:107],v[160:163],v[200:203],a[104:107]
rt v[32:33],v240 offset:33920
rt v[34:35],v240 offset:34944
mm a[112:115],v[160:163],v[208:211],a[112:115]
mm a[112:115],v[164:167],v[212:215],a[112:115]
.La5_LW1_7:
s_cbranch_execz .La5_LW1_11
mm a[128:131],v[164:167],v[220:223],a[128:131]
wt lgkmcnt(2)
s_barrier
mm a[128:131],v[160:163],v[216:219],a[128:131]
mm a[132:135],v[168:171],v[216:219],a[132:135]
s_xor_b32 m0, s82, 32768
mm a[132:135],v[172:175],v[220:223],a[132:135]
ld v224,s[40:43],s79 offen lds
mm a[116:119],v[172:175],v[212:215],a[116:119]
mm a[116:119],v[168:171],v[208:211],a[116:119]
rt v[36:37],v240 offset:50816
rt v[38:39],v240 offset:51840
mm a[108:111],v[168:171],v[200:203],a[108:111]
s_xor_b32 m0, s82, 33792
mm a[108:111],v[172:175],v[204:207],a[108:111]
ld v225,s[40:43],s79 offen lds
mm a[100:103],v[172:175],v[196:199],a[100:103]
mm a[100:103],v[168:171],v[192:195],a[100:103]
mm a[232:235],v[176:179],v[192:195],a[232:235]
s_xor_b32 m0, s82, 34816
mm a[232:235],v[180:183],v[196:199],a[232:235]
ld v226,s[40:43],s79 offen lds
mm a[152:155],v[180:183],v[204:207],a[152:155]
mm a[152:155],v[176:179],v[200:203],a[152:155]
rt v[40:41],v241 offset:33920
rt v[42:43],v241 offset:34944
mm a[208:211],v[176:179],v[208:211],a[208:211]
s_xor_b32 m0, s82, 35840
mm a[208:211],v[180:183],v[212:215],a[208:211]
)ASM" R"ASM(ld v227,s[40:43],s79 offen lds
mm a[160:163],v[180:183],v[220:223],a[160:163]
mm a[160:163],v[176:179],v[216:219],a[160:163]
mm a[164:167],v[184:187],v[216:219],a[164:167]
s_xor_b32 m0, s82, 36864
mm a[164:167],v[188:191],v[220:223],a[164:167]
ld v228,s[40:43],s79 offen lds
mm a[212:215],v[188:191],v[212:215],a[212:215]
mm a[212:215],v[184:187],v[208:211],a[212:215]
rt v[44:45],v241 offset:50816
rt v[46:47],v241 offset:51840
mm a[156:159],v[184:187],v[200:203],a[156:159]
s_xor_b32 m0, s82, 37888
mm a[156:159],v[188:191],v[204:207],a[156:159]
ld v229,s[40:43],s79 offen lds
mm a[236:239],v[188:191],v[196:199],a[236:239]
mm a[236:239],v[184:187],v[192:195],a[236:239]
mm a[200:203],v[0:3],v[192:195],a[200:203]
s_xor_b32 m0, s82, 38912
mm a[200:203],v[4:7],v[196:199],a[200:203]
ld v230,s[40:43],s79 offen lds
mm a[184:187],v[4:7],v[204:207],a[184:187]
mm a[184:187],v[0:3],v[200:203],a[184:187]
rt v[48:49],v242 offset:33920
rt v[50:51],v242 offset:34944
mm a[64:67],v[0:3],v[208:211],a[64:67]
s_xor_b32 m0, s82, 39936
mm a[64:67],v[4:7],v[212:215],a[64:67]
ld v231,s[40:43],s79 offen lds
mm a[0:3],v[4:7],v[220:223],a[0:3]
s_mov_b32 s79, 512
mm a[0:3],v[0:3],v[216:219],a[0:3]
mm a[4:7],v[8:11],v[216:219],a[4:7]
mm a[4:7],v[12:15],v[220:223],a[4:7]
buffer_load_dword v254, v135, s[68:71], s96 offen
mm a[68:71],v[12:15],v[212:215],a[68:71]
s_add_u32 s96, s96, 1048576
mm a[68:71],v[8:11],v[208:211],a[68:71]
rt v[52:53],v242 offset:50816
rt v[54:55],v242 offset:51840
mm a[188:191],v[8:11],v[200:203],a[188:191]
mm a[188:191],v[12:15],v[204:207],a[188:191]
buffer_load_dword v254, v135, s[68:71], s96 offen
mm a[204:207],v[12:15],v[196:199],a[204:207]
s_add_u32 s96, s96, 1048576
mm a[204:207],v[8:11],v[192:195],a[204:207]
rt v[56:57],v243 offset:33920
rt v[58:59],v243 offset:34944
mm a[88:91],v[16:19],v[192:195],a[88:91]
mm a[88:91],v[20:23],v[196:199],a[88:91]
mm a[24:27],v[20:23],v[204:207],a[24:27]
mm a[24:27],v[16:19],v[200:203],a[24:27]
rt v[60:61],v243 offset:50816
rt v[62:63],v243 offset:51840
mm a[240:243],v[16:19],v[208:211],a[240:243]
mm a[240:243],v[20:23],v[212:215],a[240:243]
mm a[32:35],v[20:23],v[220:223],a[32:35]
mm a[32:35],v[16:19],v[216:219],a[32:35]
mm a[36:39],v[24:27],v[216:219],a[36:39]
mm a[36:39],v[28:31],v[220:223],a[36:39]
mm a[244:247],v[28:31],v[212:215],a[244:247]
mm a[244:247],v[24:27],v[208:211],a[244:247]
mm a[28:31],v[24:27],v[200:203],a[28:31]
mm a[28:31],v[28:31],v[204:207],a[28:31]
mm a[92:95],v[28:31],v[196:199],a[92:95]
wt vmcnt(10) lgkmcnt(0)
s_barrier
mm a[92:95],v[24:27],v[192:195],a[92:95]
mm a[120:123],v[160:163],v[32:35],a[120:123]
mm a[120:123],v[164:167],v[36:39],a[120:123]
mm a[136:139],v[164:167],v[44:47],a[136:139]
rt v[192:193],v240 offset:0
rt v[194:195],v240 offset:1024
mm a[136:139],v[160:163],v[40:43],a[136:139]
rt v[196:197],v240 offset:16896
rt v[198:199],v240 offset:17920
mm a[224:227],v[160:163],v[48:51],a[224:227]
mm a[224:227],v[164:167],v[52:55],a[224:227]
mm a[144:147],v[164:167],v[60:63],a[144:147]
mm a[144:147],v[160:163],v[56:59],a[144:147]
rd v[160:163],v236 offset:24576
rd v[164:167],v237 offset:24576
mm a[148:151],v[168:171],v[56:59],a[148:151]
mm a[148:151],v[172:175],v[60:63],a[148:151]
mm a[228:231],v[172:175],v[52:55],a[228:231]
mm a[228:231],v[168:171],v[48:51],a[228:231]
rt v[200:201],v241 offset:0
rt v[202:203],v241 offset:1024
mm a[140:143],v[168:171],v[40:43],a[140:143]
mm a[140:143],v[172:175],v[44:47],a[140:143]
mm a[124:127],v[172:175],v[36:39],a[124:127]
mm a[124:127],v[168:171],v[32:35],a[124:127]
rd v[168:171],v236 offset:26624
rd v[172:175],v237 offset:26624
mm a[216:219],v[176:179],v[32:35],a[216:219]
mm a[216:219],v[180:183],v[36:39],a[216:219]
mm a[168:171],v[180:183],v[44:47],a[168:171]
mm a[168:171],v[176:179],v[40:43],a[168:171]
rt v[204:205],v241 offset:16896
rt v[206:207],v241 offset:17920
)ASM" R"ASM(mm a[192:195],v[176:179],v[48:51],a[192:195]
mm a[192:195],v[180:183],v[52:55],a[192:195]
mm a[176:179],v[180:183],v[60:63],a[176:179]
mm a[176:179],v[176:179],v[56:59],a[176:179]
rd v[176:179],v236 offset:16384
rd v[180:183],v237 offset:16384
mm a[180:183],v[184:187],v[56:59],a[180:183]
mm a[180:183],v[188:191],v[60:63],a[180:183]
mm a[196:199],v[188:191],v[52:55],a[196:199]
mm a[196:199],v[184:187],v[48:51],a[196:199]
rt v[208:209],v242 offset:0
rt v[210:211],v242 offset:1024
mm a[172:175],v[184:187],v[40:43],a[172:175]
s_add_u32 m0, s83, 33792
mm a[172:175],v[188:191],v[44:47],a[172:175]
ld v232,s[44:47],s80 offen lds
mm a[220:223],v[188:191],v[36:39],a[220:223]
mm a[220:223],v[184:187],v[32:35],a[220:223]
rd v[184:187],v236 offset:18432
rd v[188:191],v237 offset:18432
mm a[72:75],v[0:3],v[32:35],a[72:75]
s_add_u32 m0, s83, 34816
mm a[72:75],v[4:7],v[36:39],a[72:75]
ld v233,s[44:47],s80 offen lds
mm a[8:11],v[4:7],v[44:47],a[8:11]
mm a[8:11],v[0:3],v[40:43],a[8:11]
rt v[212:213],v242 offset:16896
rt v[214:215],v242 offset:17920
mm a[80:83],v[0:3],v[48:51],a[80:83]
s_add_u32 m0, s83, 35968
mm a[80:83],v[4:7],v[52:55],a[80:83]
ld v234,s[44:47],s80 offen lds
mm a[16:19],v[4:7],v[60:63],a[16:19]
mm a[16:19],v[0:3],v[56:59],a[16:19]
rd v[0:3],v236 offset:8192
rd v[4:7],v237 offset:8192
mm a[20:23],v[8:11],v[56:59],a[20:23]
s_add_u32 m0, s83, 36992
mm a[20:23],v[12:15],v[60:63],a[20:23]
ld v235,s[44:47],s80 offen lds
mm a[84:87],v[12:15],v[52:55],a[84:87]
s_mov_b32 s80, 1835008
mm a[84:87],v[8:11],v[48:51],a[84:87]
rt v[216:217],v243 offset:0
rt v[218:219],v243 offset:1024
mm a[12:15],v[8:11],v[40:43],a[12:15]
s_add_u32 m0, s83, 50688
mm a[12:15],v[12:15],v[44:47],a[12:15]
ld v232,s[44:47],s80 offen lds
mm a[76:79],v[12:15],v[36:39],a[76:79]
mm a[76:79],v[8:11],v[32:35],a[76:79]
rd v[8:11],v236 offset:10240
rd v[12:15],v237 offset:10240
mm a[248:251],v[16:19],v[32:35],a[248:251]
s_add_u32 m0, s83, 51712
mm a[248:251],v[20:23],v[36:39],a[248:251]
ld v233,s[44:47],s80 offen lds
mm a[40:43],v[20:23],v[44:47],a[40:43]
mm a[40:43],v[16:19],v[40:43],a[40:43]
rt v[220:221],v243 offset:16896
rt v[222:223],v243 offset:17920
mm a[48:51],v[16:19],v[48:51],a[48:51]
s_add_u32 m0, s83, 52864
mm a[48:51],v[20:23],v[52:55],a[48:51]
ld v234,s[44:47],s80 offen lds
mm a[56:59],v[20:23],v[60:63],a[56:59]
mm a[56:59],v[16:19],v[56:59],a[56:59]
rd v[16:19],v236 offset:0
rd v[20:23],v237 offset:0
mm a[60:63],v[24:27],v[56:59],a[60:63]
s_add_u32 m0, s83, 53888
mm a[60:63],v[28:31],v[60:63],a[60:63]
ld v235,s[44:47],s80 offen lds
mm a[52:55],v[28:31],v[52:55],a[52:55]
s_mov_b64 s[44:45], s[68:69]
s_mov_b32 s46, s70
mm a[52:55],v[24:27],v[48:51],a[52:55]
mm a[44:47],v[24:27],v[40:43],a[44:47]
mm a[44:47],v[28:31],v[44:47],a[44:47]
mm a[252:255],v[28:31],v[36:39],a[252:255]
wt lgkmcnt(2)
s_lshl_b32 s80, s77, 18
mm a[252:255],v[24:27],v[32:35],a[252:255]
rd v[24:27],v236 offset:2048
rd v[28:31],v237 offset:2048
mm a[96:99],v[160:163],v[192:195],a[96:99]
mm a[96:99],v[164:167],v[196:199],a[96:99]
mm a[104:107],v[164:167],v[204:207],a[104:107]
mm a[104:107],v[160:163],v[200:203],a[104:107]
rt v[32:33],v240 offset:128
rt v[34:35],v240 offset:1152
mm a[112:115],v[160:163],v[208:211],a[112:115]
mm a[112:115],v[164:167],v[212:215],a[112:115]
mm a[128:131],v[164:167],v[220:223],a[128:131]
wt lgkmcnt(2)
s_barrier
mm a[128:131],v[160:163],v[216:219],a[128:131]
mm a[132:135],v[168:171],v[216:219],a[132:135]
mm a[132:135],v[172:175],v[220:223],a[132:135]
mm a[116:119],v[172:175],v[212:215],a[116:119]
)ASM" R"ASM(mm a[116:119],v[168:171],v[208:211],a[116:119]
rt v[36:37],v240 offset:17024
rt v[38:39],v240 offset:18048
mm a[108:111],v[168:171],v[200:203],a[108:111]
mm a[108:111],v[172:175],v[204:207],a[108:111]
mm a[100:103],v[172:175],v[196:199],a[100:103]
mm a[100:103],v[168:171],v[192:195],a[100:103]
mm a[232:235],v[176:179],v[192:195],a[232:235]
mm a[232:235],v[180:183],v[196:199],a[232:235]
mm a[152:155],v[180:183],v[204:207],a[152:155]
mm a[152:155],v[176:179],v[200:203],a[152:155]
rt v[40:41],v241 offset:128
rt v[42:43],v241 offset:1152
mm a[208:211],v[176:179],v[208:211],a[208:211]
mm a[208:211],v[180:183],v[212:215],a[208:211]
mm a[160:163],v[180:183],v[220:223],a[160:163]
mm a[160:163],v[176:179],v[216:219],a[160:163]
mm a[164:167],v[184:187],v[216:219],a[164:167]
mm a[164:167],v[188:191],v[220:223],a[164:167]
mm a[212:215],v[188:191],v[212:215],a[212:215]
mm a[212:215],v[184:187],v[208:211],a[212:215]
rt v[44:45],v241 offset:17024
rt v[46:47],v241 offset:18048
mm a[156:159],v[184:187],v[200:203],a[156:159]
mm a[156:159],v[188:191],v[204:207],a[156:159]
mm a[236:239],v[188:191],v[196:199],a[236:239]
mm a[236:239],v[184:187],v[192:195],a[236:239]
mm a[200:203],v[0:3],v[192:195],a[200:203]
mm a[200:203],v[4:7],v[196:199],a[200:203]
mm a[184:187],v[4:7],v[204:207],a[184:187]
mm a[184:187],v[0:3],v[200:203],a[184:187]
rt v[48:49],v242 offset:128
rt v[50:51],v242 offset:1152
mm a[64:67],v[0:3],v[208:211],a[64:67]
mm a[64:67],v[4:7],v[212:215],a[64:67]
mm a[0:3],v[4:7],v[220:223],a[0:3]
mm a[0:3],v[0:3],v[216:219],a[0:3]
mm a[4:7],v[8:11],v[216:219],a[4:7]
mm a[4:7],v[12:15],v[220:223],a[4:7]
buffer_load_dword v254, v135, s[68:71], s96 offen
mm a[68:71],v[12:15],v[212:215],a[68:71]
s_add_u32 s96, s96, 1048576
mm a[68:71],v[8:11],v[208:211],a[68:71]
rt v[52:53],v242 offset:17024
rt v[54:55],v242 offset:18048
mm a[188:191],v[8:11],v[200:203],a[188:191]
mm a[188:191],v[12:15],v[204:207],a[188:191]
buffer_load_dword v254, v135, s[68:71], s96 offen
mm a[204:207],v[12:15],v[196:199],a[204:207]
s_add_u32 s96, s96, 1048576
mm a[204:207],v[8:11],v[192:195],a[204:207]
rt v[56:57],v243 offset:128
rt v[58:59],v243 offset:1152
mm a[88:91],v[16:19],v[192:195],a[88:91]
mm a[88:91],v[20:23],v[196:199],a[88:91]
mm a[24:27],v[20:23],v[204:207],a[24:27]
mm a[24:27],v[16:19],v[200:203],a[24:27]
rt v[60:61],v243 offset:17024
rt v[62:63],v243 offset:18048
mm a[240:243],v[16:19],v[208:211],a[240:243]
mm a[240:243],v[20:23],v[212:215],a[240:243]
mm a[32:35],v[20:23],v[220:223],a[32:35]
mm a[32:35],v[16:19],v[216:219],a[32:35]
mm a[36:39],v[24:27],v[216:219],a[36:39]
mm a[36:39],v[28:31],v[220:223],a[36:39]
ld v[72:75],v255,s[64:67],s89 offen offset:1024 sc1
mm a[244:247],v[28:31],v[212:215],a[244:247]
mm a[244:247],v[24:27],v[208:211],a[244:247]
mm a[28:31],v[24:27],v[200:203],a[28:31]
mm a[28:31],v[28:31],v[204:207],a[28:31]
mm a[92:95],v[28:31],v[196:199],a[92:95]
wt vmcnt(3) lgkmcnt(0)
s_barrier
mm a[92:95],v[24:27],v[192:195],a[92:95]
mm a[120:123],v[160:163],v[32:35],a[120:123]
mm a[120:123],v[164:167],v[36:39],a[120:123]
mm a[136:139],v[164:167],v[44:47],a[136:139]
rt v[192:193],v240 offset:33792
rt v[194:195],v240 offset:34816
mm a[136:139],v[160:163],v[40:43],a[136:139]
rt v[196:197],v240 offset:50688
rt v[198:199],v240 offset:51712
mm a[224:227],v[160:163],v[48:51],a[224:227]
mm a[224:227],v[164:167],v[52:55],a[224:227]
mm a[144:147],v[164:167],v[60:63],a[144:147]
mm a[144:147],v[160:163],v[56:59],a[144:147]
rd v[160:163],v236 offset:57344
rd v[164:167],v237 offset:57344
mm a[148:151],v[168:171],v[56:59],a[148:151]
mm a[148:151],v[172:175],v[60:63],a[148:151]
mm a[228:231],v[172:175],v[52:55],a[228:231]
mm a[228:231],v[168:171],v[48:51],a[228:231]
rt v[200:201],v241 offset:33792
rt v[202:203],v241 offset:34816
mm a[140:143],v[168:171],v[40:43],a[140:143]
mm a[140:143],v[172:175],v[44:47],a[140:143]
mm a[124:127],v[172:175],v[36:39],a[124:127]
mm a[124:127],v[168:171],v[32:35],a[124:127]
rd v[168:171],v236 offset:59392
rd v[172:175],v237 offset:59392
)ASM" R"ASM(mm a[216:219],v[176:179],v[32:35],a[216:219]
mm a[216:219],v[180:183],v[36:39],a[216:219]
mm a[168:171],v[180:183],v[44:47],a[168:171]
mm a[168:171],v[176:179],v[40:43],a[168:171]
rt v[204:205],v241 offset:50688
rt v[206:207],v241 offset:51712
mm a[192:195],v[176:179],v[48:51],a[192:195]
mm a[192:195],v[180:183],v[52:55],a[192:195]
mm a[176:179],v[180:183],v[60:63],a[176:179]
mm a[176:179],v[176:179],v[56:59],a[176:179]
rd v[176:179],v236 offset:49152
rd v[180:183],v237 offset:49152
mm a[180:183],v[184:187],v[56:59],a[180:183]
mm a[180:183],v[188:191],v[60:63],a[180:183]
mm a[196:199],v[188:191],v[52:55],a[196:199]
mm a[196:199],v[184:187],v[48:51],a[196:199]
rt v[208:209],v242 offset:33792
rt v[210:211],v242 offset:34816
mm a[172:175],v[184:187],v[40:43],a[172:175]
s_add_u32 m0, s83, 0
mm a[172:175],v[188:191],v[44:47],a[172:175]
ld v232,s[44:47],s80 offen lds
mm a[220:223],v[188:191],v[36:39],a[220:223]
mm a[220:223],v[184:187],v[32:35],a[220:223]
rd v[184:187],v236 offset:51200
rd v[188:191],v237 offset:51200
mm a[72:75],v[0:3],v[32:35],a[72:75]
s_add_u32 m0, s83, 1024
mm a[72:75],v[4:7],v[36:39],a[72:75]
ld v233,s[44:47],s80 offen lds
mm a[8:11],v[4:7],v[44:47],a[8:11]
mm a[8:11],v[0:3],v[40:43],a[8:11]
rt v[212:213],v242 offset:50688
rt v[214:215],v242 offset:51712
mm a[80:83],v[0:3],v[48:51],a[80:83]
s_add_u32 m0, s83, 2176
mm a[80:83],v[4:7],v[52:55],a[80:83]
ld v234,s[44:47],s80 offen lds
mm a[16:19],v[4:7],v[60:63],a[16:19]
mm a[16:19],v[0:3],v[56:59],a[16:19]
rd v[0:3],v236 offset:40960
rd v[4:7],v237 offset:40960
mm a[20:23],v[8:11],v[56:59],a[20:23]
s_add_u32 m0, s83, 3200
mm a[20:23],v[12:15],v[60:63],a[20:23]
ld v235,s[44:47],s80 offen lds
mm a[84:87],v[12:15],v[52:55],a[84:87]
s_add_u32 s80, s80, 524288
mm a[84:87],v[8:11],v[48:51],a[84:87]
rt v[216:217],v243 offset:33792
rt v[218:219],v243 offset:34816
mm a[12:15],v[8:11],v[40:43],a[12:15]
s_add_u32 m0, s83, 16896
mm a[12:15],v[12:15],v[44:47],a[12:15]
ld v232,s[44:47],s80 offen lds
mm a[76:79],v[12:15],v[36:39],a[76:79]
mm a[76:79],v[8:11],v[32:35],a[76:79]
rd v[8:11],v236 offset:43008
rd v[12:15],v237 offset:43008
mm a[248:251],v[16:19],v[32:35],a[248:251]
s_add_u32 m0, s83, 17920
mm a[248:251],v[20:23],v[36:39],a[248:251]
ld v233,s[44:47],s80 offen lds
mm a[40:43],v[20:23],v[44:47],a[40:43]
mm a[40:43],v[16:19],v[40:43],a[40:43]
rt v[220:221],v243 offset:50688
rt v[222:223],v243 offset:51712
mm a[48:51],v[16:19],v[48:51],a[48:51]
s_add_u32 m0, s83, 19072
mm a[48:51],v[20:23],v[52:55],a[48:51]
ld v234,s[44:47],s80 offen lds
mm a[56:59],v[20:23],v[60:63],a[56:59]
mm a[56:59],v[16:19],v[56:59],a[56:59]
rd v[16:19],v236 offset:32768
rd v[20:23],v237 offset:32768
mm a[60:63],v[24:27],v[56:59],a[60:63]
s_add_u32 m0, s83, 20096
mm a[60:63],v[28:31],v[60:63],a[60:63]
ld v235,s[44:47],s80 offen lds
mm a[52:55],v[28:31],v[52:55],a[52:55]
s_add_u32 s80, s80, 524288
mm a[52:55],v[24:27],v[48:51],a[52:55]
mm a[44:47],v[24:27],v[40:43],a[44:47]
mm a[44:47],v[28:31],v[44:47],a[44:47]
mm a[252:255],v[28:31],v[36:39],a[252:255]
wt lgkmcnt(2)
mm a[252:255],v[24:27],v[32:35],a[252:255]
rd v[24:27],v236 offset:34816
rd v[28:31],v237 offset:34816
mm a[96:99],v[160:163],v[192:195],a[96:99]
mm a[96:99],v[164:167],v[196:199],a[96:99]
mm a[104:107],v[164:167],v[204:207],a[104:107]
mm a[104:107],v[160:163],v[200:203],a[104:107]
rt v[32:33],v240 offset:33920
rt v[34:35],v240 offset:34944
mm a[112:115],v[160:163],v[208:211],a[112:115]
mm a[112:115],v[164:167],v[212:215],a[112:115]
mm a[128:131],v[164:167],v[220:223],a[128:131]
wt lgkmcnt(2)
s_barrier
)ASM" R"ASM(mm a[128:131],v[160:163],v[216:219],a[128:131]
mm a[132:135],v[168:171],v[216:219],a[132:135]
wt vmcnt(8)
v_cmp_eq_u64 vcc, v[72:73], v[252:253]
mm a[132:135],v[172:175],v[220:223],a[132:135]
v_cmp_eq_u64 s[96:97], v[74:75], v[252:253]
mm a[116:119],v[172:175],v[212:215],a[116:119]
s_and_b64 vcc, vcc, s[96:97]
s_cmp_eq_u64 vcc, -1
s_cbranch_scc0 .La5_LV_XSPIN
.La5_LV_XOK:
mm a[116:119],v[168:171],v[208:211],a[116:119]
rt v[36:37],v240 offset:50816
rt v[38:39],v240 offset:51840
mm a[108:111],v[168:171],v[200:203],a[108:111]
s_mov_b32 s89, 49152
mm a[108:111],v[172:175],v[204:207],a[108:111]
mm a[100:103],v[172:175],v[196:199],a[100:103]
mm a[100:103],v[168:171],v[192:195],a[100:103]
mm a[232:235],v[176:179],v[192:195],a[232:235]
mm a[232:235],v[180:183],v[196:199],a[232:235]
ld v[128:131],v244,s[52:55],s91 offen offset:0 sc1
mm a[152:155],v[180:183],v[204:207],a[152:155]
ar v224,a96
ar v225,a97
mm a[152:155],v[176:179],v[200:203],a[152:155]
rt v[40:41],v241 offset:33920
rt v[42:43],v241 offset:34944
mm a[208:211],v[176:179],v[208:211],a[208:211]
ar v226,a98
ar v227,a99
mm a[208:211],v[180:183],v[212:215],a[208:211]
ld v[132:135],v245,s[52:55],s91 offen offset:0 sc1
mm a[160:163],v[180:183],v[220:223],a[160:163]
ar v228,a100
ar v229,a101
mm a[160:163],v[176:179],v[216:219],a[160:163]
ar v230,a102
ar v231,a103
mm a[164:167],v[184:187],v[216:219],a[164:167]
cv v248,v224,v225
cv v249,v226,v227
mm a[164:167],v[188:191],v[220:223],a[164:167]
ld v[136:139],v246,s[52:55],s91 offen offset:0 sc1
mm a[212:215],v[188:191],v[212:215],a[212:215]
cv v250,v228,v229
cv v251,v230,v231
mm a[212:215],v[184:187],v[208:211],a[212:215]
rt v[44:45],v241 offset:50816
rt v[46:47],v241 offset:51840
mm a[156:159],v[184:187],v[200:203],a[156:159]
st v[248:251],v255,s[48:51],s89 offen sc1
mm a[156:159],v[188:191],v[204:207],a[156:159]
ld v[140:143],v247,s[52:55],s91 offen offset:0 sc1
mm a[236:239],v[188:191],v[196:199],a[236:239]
s_mov_b32 s89, 50176
ds_write_b128 v238, v[248:251] offset:0
mm a[236:239],v[184:187],v[192:195],a[236:239]
ar v224,a104
ar v225,a105
mm a[200:203],v[0:3],v[192:195],a[200:203]
ar v226,a106
ar v227,a107
mm a[200:203],v[4:7],v[196:199],a[200:203]
ld v[144:147],v244,s[52:55],s91 offen offset:1024 sc1
mm a[184:187],v[4:7],v[204:207],a[184:187]
ar v228,a108
ar v229,a109
mm a[184:187],v[0:3],v[200:203],a[184:187]
rt v[48:49],v242 offset:33920
rt v[50:51],v242 offset:34944
mm a[64:67],v[0:3],v[208:211],a[64:67]
ar v230,a110
ar v231,a111
mm a[64:67],v[4:7],v[212:215],a[64:67]
ld v[148:151],v245,s[52:55],s91 offen offset:1024 sc1
mm a[0:3],v[4:7],v[220:223],a[0:3]
cv v248,v224,v225
cv v249,v226,v227
mm a[0:3],v[0:3],v[216:219],a[0:3]
cv v250,v228,v229
cv v251,v230,v231
mm a[4:7],v[8:11],v[216:219],a[4:7]
st v[248:251],v255,s[48:51],s89 offen sc1
mm a[4:7],v[12:15],v[220:223],a[4:7]
ld v[152:155],v246,s[52:55],s91 offen offset:1024 sc1
mm a[68:71],v[12:15],v[212:215],a[68:71]
s_mov_b32 s89, 51200
ds_write_b128 v238, v[248:251] offset:1024
mm a[68:71],v[8:11],v[208:211],a[68:71]
rt v[52:53],v242 offset:50816
rt v[54:55],v242 offset:51840
mm a[188:191],v[8:11],v[200:203],a[188:191]
ar v224,a112
.La5_LW1_8:
s_cbranch_execz .La5_LW1_12
ar v225,a113
mm a[188:191],v[12:15],v[204:207],a[188:191]
ld v[156:159],v247,s[52:55],s91 offen offset:1024 sc1
mm a[204:207],v[12:15],v[196:199],a[204:207]
)ASM" R"ASM(ar v226,a114
ar v227,a115
mm a[204:207],v[8:11],v[192:195],a[204:207]
rt v[56:57],v243 offset:33920
rt v[58:59],v243 offset:34944
mm a[88:91],v[16:19],v[192:195],a[88:91]
ar v228,a116
ar v229,a117
mm a[88:91],v[20:23],v[196:199],a[88:91]
ar v230,a118
ar v231,a119
mm a[24:27],v[20:23],v[204:207],a[24:27]
cv v248,v224,v225
cv v249,v226,v227
mm a[24:27],v[16:19],v[200:203],a[24:27]
rt v[60:61],v243 offset:50816
rt v[62:63],v243 offset:51840
mm a[240:243],v[16:19],v[208:211],a[240:243]
v_subrev_u32 v240, 8448, v240
v_subrev_u32 v241, 8448, v241
mm a[240:243],v[20:23],v[212:215],a[240:243]
v_subrev_u32 v242, 8448, v242
v_subrev_u32 v243, 8448, v243
mm a[32:35],v[20:23],v[220:223],a[32:35]
cv v250,v228,v229
cv v251,v230,v231
mm a[32:35],v[16:19],v[216:219],a[32:35]
st v[248:251],v255,s[48:51],s89 offen sc1
mm a[36:39],v[24:27],v[216:219],a[36:39]
s_mov_b32 s89, 52224
ds_write_b128 v238, v[248:251] offset:2048
mm a[36:39],v[28:31],v[220:223],a[36:39]
mm a[244:247],v[28:31],v[212:215],a[244:247]
mm a[244:247],v[24:27],v[208:211],a[244:247]
mm a[28:31],v[24:27],v[200:203],a[28:31]
mm a[28:31],v[28:31],v[204:207],a[28:31]
mm a[92:95],v[28:31],v[196:199],a[92:95]
wt vmcnt(11) lgkmcnt(1)
s_barrier
mm a[92:95],v[24:27],v[192:195],a[92:95]
mm a[120:123],v[160:163],v[32:35],a[120:123]
s_add_u32 m0, s83, 33792
mm a[120:123],v[164:167],v[36:39],a[120:123]
ld v232,s[44:47],s80 offen lds
mm a[136:139],v[164:167],v[44:47],a[136:139]
mm a[136:139],v[160:163],v[40:43],a[136:139]
rt v[192:193],v240 offset:0
rt v[194:195],v240 offset:1024
mm a[224:227],v[160:163],v[48:51],a[224:227]
s_add_u32 m0, s83, 34816
mm a[224:227],v[164:167],v[52:55],a[224:227]
ld v233,s[44:47],s80 offen lds
mm a[144:147],v[164:167],v[60:63],a[144:147]
mm a[144:147],v[160:163],v[56:59],a[144:147]
rt v[196:197],v240 offset:8448
rt v[198:199],v240 offset:9472
mm a[148:151],v[168:171],v[56:59],a[148:151]
s_add_u32 m0, s83, 35968
mm a[148:151],v[172:175],v[60:63],a[148:151]
ld v234,s[44:47],s80 offen lds
mm a[228:231],v[172:175],v[52:55],a[228:231]
mm a[228:231],v[168:171],v[48:51],a[228:231]
rt v[200:201],v241 offset:0
rt v[202:203],v241 offset:1024
mm a[140:143],v[168:171],v[40:43],a[140:143]
s_add_u32 m0, s83, 36992
mm a[140:143],v[172:175],v[44:47],a[140:143]
ld v235,s[44:47],s80 offen lds
mm a[124:127],v[172:175],v[36:39],a[124:127]
s_add_u32 s80, s80, 524288
mm a[124:127],v[168:171],v[32:35],a[124:127]
rt v[204:205],v241 offset:8448
rt v[206:207],v241 offset:9472
mm a[216:219],v[176:179],v[32:35],a[216:219]
s_add_u32 m0, s83, 50688
mm a[216:219],v[180:183],v[36:39],a[216:219]
ld v232,s[44:47],s80 offen lds
mm a[168:171],v[180:183],v[44:47],a[168:171]
mm a[168:171],v[176:179],v[40:43],a[168:171]
rt v[208:209],v242 offset:0
rt v[210:211],v242 offset:1024
mm a[192:195],v[176:179],v[48:51],a[192:195]
s_add_u32 m0, s83, 51712
mm a[192:195],v[180:183],v[52:55],a[192:195]
ld v233,s[44:47],s80 offen lds
mm a[176:179],v[180:183],v[60:63],a[176:179]
mm a[176:179],v[176:179],v[56:59],a[176:179]
rt v[212:213],v242 offset:8448
rt v[214:215],v242 offset:9472
mm a[180:183],v[184:187],v[56:59],a[180:183]
s_add_u32 m0, s83, 52864
mm a[180:183],v[188:191],v[60:63],a[180:183]
ld v234,s[44:47],s80 offen lds
mm a[196:199],v[188:191],v[52:55],a[196:199]
mm a[196:199],v[184:187],v[48:51],a[196:199]
rt v[216:217],v243 offset:0
rt v[218:219],v243 offset:1024
mm a[172:175],v[184:187],v[40:43],a[172:175]
s_add_u32 m0, s83, 53888
mm a[172:175],v[188:191],v[44:47],a[172:175]
)ASM" R"ASM(ld v235,s[44:47],s80 offen lds
mm a[220:223],v[188:191],v[36:39],a[220:223]
s_add_u32 s80, s80, 524288
mm a[220:223],v[184:187],v[32:35],a[220:223]
rt v[220:221],v243 offset:8448
rt v[222:223],v243 offset:9472
mm a[72:75],v[0:3],v[32:35],a[72:75]
mm a[72:75],v[4:7],v[36:39],a[72:75]
ld v[160:163],v244,s[52:55],s91 offen offset:2048 sc1
mm a[8:11],v[4:7],v[44:47],a[8:11]
mm a[8:11],v[0:3],v[40:43],a[8:11]
mm a[80:83],v[0:3],v[48:51],a[80:83]
mm a[80:83],v[4:7],v[52:55],a[80:83]
ld v[164:167],v245,s[52:55],s91 offen offset:2048 sc1
mm a[16:19],v[4:7],v[60:63],a[16:19]
mm a[16:19],v[0:3],v[56:59],a[16:19]
mm a[20:23],v[8:11],v[56:59],a[20:23]
mm a[20:23],v[12:15],v[60:63],a[20:23]
ld v[168:171],v246,s[52:55],s91 offen offset:2048 sc1
mm a[84:87],v[12:15],v[52:55],a[84:87]
mm a[84:87],v[8:11],v[48:51],a[84:87]
mm a[12:15],v[8:11],v[40:43],a[12:15]
mm a[12:15],v[12:15],v[44:47],a[12:15]
ld v[172:175],v247,s[52:55],s91 offen offset:2048 sc1
mm a[76:79],v[12:15],v[36:39],a[76:79]
mm a[76:79],v[8:11],v[32:35],a[76:79]
mm a[248:251],v[16:19],v[32:35],a[248:251]
mm a[248:251],v[20:23],v[36:39],a[248:251]
mm a[40:43],v[20:23],v[44:47],a[40:43]
mm a[40:43],v[16:19],v[40:43],a[40:43]
mm a[48:51],v[16:19],v[48:51],a[48:51]
mm a[48:51],v[20:23],v[52:55],a[48:51]
mm a[56:59],v[20:23],v[60:63],a[56:59]
mm a[56:59],v[16:19],v[56:59],a[56:59]
mm a[60:63],v[24:27],v[56:59],a[60:63]
mm a[60:63],v[28:31],v[60:63],a[60:63]
mm a[52:55],v[28:31],v[52:55],a[52:55]
mm a[52:55],v[24:27],v[48:51],a[52:55]
mm a[44:47],v[24:27],v[40:43],a[44:47]
mm a[44:47],v[28:31],v[44:47],a[44:47]
mm a[252:255],v[28:31],v[36:39],a[252:255]
mm a[252:255],v[24:27],v[32:35],a[252:255]
; ---- Q tile: 256 rows x 128 columns, K = 1024, while K / V leave the accumulators (memory + .La5_LDS ring); behind it the first step on the diagonal
.La5_LQ_START:
.La5_LW1_9:
s_cbranch_execz .La5_LW1_13
wt vmcnt(13) lgkmcnt(4)
mm v[0:3],v[192:195],v[128:131],0
ar v224,a128
ar v225,a129
s_mov_b32 s85, s95
mm v[0:3],v[196:199],v[144:147],v[0:3]
ld v[176:179],v244,s[52:55],s91 offen offset:3072 sc1
mm v[32:35],v[196:199],v[148:151],0
ar v226,a130
ar v227,a131
mm v[32:35],v[192:195],v[132:135],v[32:35]
ar v228,a132
ar v229,a133
mm v[64:67],v[192:195],v[136:139],0
ar v230,a134
ar v231,a135
mm v[64:67],v[196:199],v[152:155],v[64:67]
ld v[180:183],v245,s[52:55],s91 offen offset:3072 sc1
mm v[96:99],v[196:199],v[156:159],0
cv v248,v224,v225
cv v249,v226,v227
mm v[96:99],v[192:195],v[140:143],v[96:99]
rt v[196:197],v240 offset:128
rt v[198:199],v240 offset:1152
mm v[100:103],v[200:203],v[140:143],0
cv v250,v228,v229
cv v251,v230,v231
mm v[100:103],v[204:207],v[156:159],v[100:103]
ld v[184:187],v246,s[52:55],s91 offen offset:3072 sc1
mm v[68:71],v[204:207],v[152:155],0
aw a128,v248
aw a129,v249
mm v[68:71],v[200:203],v[136:139],v[68:71]
rt v[192:193],v240 offset:8576
rt v[194:195],v240 offset:9600
mm v[36:39],v[200:203],v[132:135],0
aw a130,v250
aw a131,v251
mm v[36:39],v[204:207],v[148:151],v[36:39]
ld v[188:191],v247,s[52:55],s91 offen offset:3072 sc1
mm v[4:7],v[204:207],v[144:147],0
wt lgkmcnt(4)
s_add_u32 s91, s91, 4096
mm v[4:7],v[200:203],v[128:131],v[4:7]
rt v[204:205],v241 offset:128
rt v[206:207],v241 offset:1152
mm v[8:11],v[208:211],v[128:131],0
s_and_b32 s97, s2, 7
s_mul_i32 s95, s97, 7
mm v[8:11],v[212:215],v[144:147],v[8:11]
ld a[96:99],v244,s[52:55],s91 offen offset:0 sc1
mm v[40:43],v[212:215],v[148:151],0
s_lshr_b32 s96, s30, 7
s_add_u32 s95, s95, s96
)ASM" R"ASM(mm v[40:43],v[208:211],v[132:135],v[40:43]
rt v[200:201],v241 offset:8576
rt v[202:203],v241 offset:9600
mm v[72:75],v[208:211],v[136:139],0
s_sub_u32 s95, 3, s95
s_and_b32 s95, s95, 7
mm v[72:75],v[212:215],v[152:155],v[72:75]
ld a[100:103],v245,s[52:55],s91 offen offset:0 sc1
mm v[104:107],v[212:215],v[156:159],0
s_min_u32 s95, s95, 6
s_lshl_b32 s95, s95, 1
mm v[104:107],v[208:211],v[140:143],v[104:107]
rt v[212:213],v242 offset:128
rt v[214:215],v242 offset:1152
mm v[108:111],v[216:219],v[140:143],0
s_lshr_b32 s92, s2, 6
s_mul_i32 s93, s92, 3
mm v[108:111],v[220:223],v[156:159],v[108:111]
ld a[104:107],v246,s[52:55],s91 offen offset:0 sc1
mm v[76:79],v[220:223],v[152:155],0
s_add_u32 s93, s93, s74
mm v[76:79],v[216:219],v[136:139],v[76:79]
rt v[208:209],v242 offset:8576
rt v[210:211],v242 offset:9600
mm v[44:47],v[216:219],v[132:135],0
ds_write_b128 v238, v[248:251] offset:3072
mm v[44:47],v[220:223],v[148:151],v[44:47]
st v[248:251],v255,s[48:51],s89 offen offset:0 sc1
mm v[12:15],v[220:223],v[144:147],0
wt lgkmcnt(5)
mm v[12:15],v[216:219],v[128:131],v[12:15]
rt v[220:221],v243 offset:128
rt v[222:223],v243 offset:1152
mm v[16:19],v[196:199],v[128:131],0
ar v224,a120
ar v225,a121
mm v[16:19],v[192:195],v[144:147],v[16:19]
ld a[108:111],v247,s[52:55],s91 offen offset:0 sc1
mm v[48:51],v[192:195],v[148:151],0
ar v226,a122
ar v227,a123
mm v[48:51],v[196:199],v[132:135],v[48:51]
rt v[216:217],v243 offset:8576
rt v[218:219],v243 offset:9600
mm v[80:83],v[196:199],v[136:139],0
ar v228,a124
ar v229,a125
mm v[80:83],v[192:195],v[152:155],v[80:83]
ld a[112:115],v244,s[52:55],s91 offen offset:1024 sc1
mm v[112:115],v[192:195],v[156:159],0
ar v230,a126
ar v231,a127
mm v[112:115],v[196:199],v[140:143],v[112:115]
cv v248,v224,v225
cv v249,v226,v227
mm v[116:119],v[204:207],v[140:143],0
wt lgkmcnt(0)
s_barrier
mm v[116:119],v[200:203],v[156:159],v[116:119]
ld a[116:119],v245,s[52:55],s91 offen offset:1024 sc1
mm v[84:87],v[200:203],v[152:155],0
rt v[192:193],v240 offset:16896
rt v[194:195],v240 offset:17920
mm v[84:87],v[204:207],v[136:139],v[84:87]
rt v[196:197],v240 offset:25344
rt v[198:199],v240 offset:26368
mm v[52:55],v[204:207],v[132:135],0
s_add_u32 m0, s83, 0
cv v250,v228,v229
mm v[52:55],v[200:203],v[148:151],v[52:55]
ld v232,s[44:47],s80 offen lds
mm v[20:23],v[200:203],v[144:147],0
cv v251,v230,v231
aw a132,v248
mm v[20:23],v[204:207],v[128:131],v[20:23]
rt v[200:201],v241 offset:16896
rt v[202:203],v241 offset:17920
mm v[24:27],v[212:215],v[128:131],0
aw a133,v249
aw a134,v250
mm v[24:27],v[208:211],v[144:147],v[24:27]
aw a135,v251
mm v[56:59],v[208:211],v[148:151],0
s_cmp_ge_u32 s93, s95
s_cselect_b32 s96, 2, 0
mm v[56:59],v[212:215],v[132:135],v[56:59]
rt v[204:205],v241 offset:25344
rt v[206:207],v241 offset:26368
mm v[88:91],v[212:215],v[136:139],0
s_add_u32 m0, s83, 1024
s_add_u32 s93, s93, s96
mm v[88:91],v[208:211],v[152:155],v[88:91]
ld v233,s[44:47],s80 offen lds
mm v[120:123],v[208:211],v[156:159],0
s_add_u32 s95, s95, s92
mm v[120:123],v[212:215],v[140:143],v[120:123]
rt v[208:209],v242 offset:16896
rt v[210:211],v242 offset:17920
mm v[124:127],v[220:223],v[140:143],0
s_cmp_lt_u32 s92, 2
)ASM" R"ASM(s_cselect_b32 s94, s75, 0
mm v[124:127],v[216:219],v[156:159],v[124:127]
ld v[140:143],v246,s[52:55],s91 offen offset:1024 sc1
mm v[92:95],v[216:219],v[152:155],0
s_cmp_eq_u32 s74, 3
s_cselect_b32 s92, s95, s93
s_cselect_b32 s94, s94, s75
mm v[92:95],v[220:223],v[136:139],v[92:95]
rt v[212:213],v242 offset:25344
rt v[214:215],v242 offset:26368
mm v[60:63],v[220:223],v[132:135],0
ds_write_b128 v238, v[248:251] offset:4096
mm v[60:63],v[216:219],v[148:151],v[60:63]
s_mul_i32 s93, s97, 14
s_add_u32 s92, s92, s93
mm v[28:31],v[216:219],v[144:147],0
wt vmcnt(10) lgkmcnt(5)
s_mul_i32 s96, s92, s94
mm v[28:31],v[220:223],v[128:131],v[28:31]
rt v[216:217],v243 offset:16896
rt v[218:219],v243 offset:17920
mm v[0:3],v[192:195],v[160:163],v[0:3]
ar v224,a136
ar v225,a137
mm v[0:3],v[196:199],v[176:179],v[0:3]
ld v[128:131],v247,s[52:55],s91 offen offset:1024 sc1
mm v[32:35],v[196:199],v[180:183],v[32:35]
ar v226,a138
ar v227,a139
mm v[32:35],v[192:195],v[164:167],v[32:35]
rt v[220:221],v243 offset:25344
rt v[222:223],v243 offset:26368
mm v[64:67],v[192:195],v[168:171],v[64:67]
ar v228,a140
ar v229,a141
mm v[64:67],v[196:199],v[184:187],v[64:67]
ld v[132:135],v244,s[52:55],s91 offen offset:2048 sc1
mm v[96:99],v[196:199],v[188:191],v[96:99]
ar v230,a142
ar v231,a143
mm v[96:99],v[192:195],v[172:175],v[96:99]
rt v[196:197],v240 offset:17024
rt v[198:199],v240 offset:18048
mm v[100:103],v[200:203],v[172:175],v[100:103]
s_add_u32 m0, s83, 2176
cv v248,v224,v225
mm v[100:103],v[204:207],v[188:191],v[100:103]
ld v234,s[44:47],s80 offen lds
mm v[68:71],v[204:207],v[184:187],v[68:71]
cv v249,v226,v227
cv v250,v228,v229
mm v[68:71],v[200:203],v[168:171],v[68:71]
rt v[192:193],v240 offset:25472
rt v[194:195],v240 offset:26496
mm v[36:39],v[200:203],v[164:167],v[36:39]
cv v251,v230,v231
aw a136,v248
mm v[36:39],v[204:207],v[180:183],v[36:39]
ld v[136:139],v245,s[52:55],s91 offen offset:2048 sc1
mm v[4:7],v[204:207],v[176:179],v[4:7]
wt lgkmcnt(4)
aw a137,v249
mm v[4:7],v[200:203],v[160:163],v[4:7]
rt v[204:205],v241 offset:17024
rt v[206:207],v241 offset:18048
mm v[8:11],v[208:211],v[160:163],v[8:11]
s_add_u32 m0, s83, 3200
aw a138,v250
mm v[8:11],v[212:215],v[176:179],v[8:11]
ld v235,s[44:47],s80 offen lds
mm v[40:43],v[212:215],v[180:183],v[40:43]
s_add_u32 s80, s80, 524288
aw a139,v251
mm v[40:43],v[208:211],v[164:167],v[40:43]
rt v[200:201],v241 offset:25472
rt v[202:203],v241 offset:26496
mm v[72:75],v[208:211],v[168:171],v[72:75]
s_mov_b32 s100, s96
s_and_b32 s93, s2, 7
mm v[72:75],v[212:215],v[184:187],v[72:75]
ld v[144:147],v246,s[52:55],s91 offen offset:2048 sc1
mm v[104:107],v[212:215],v[188:191],v[104:107]
s_lshr_b32 s95, s93, 1
s_xor_b32 s97, s95, s93
mm v[104:107],v[208:211],v[172:175],v[104:107]
rt v[212:213],v242 offset:17024
rt v[214:215],v242 offset:18048
mm v[108:111],v[216:219],v[172:175],v[108:111]
s_lshl_b32 s95, s95, 21
s_and_b32 s97, s97, 1
mm v[108:111],v[220:223],v[188:191],v[108:111]
ld v[148:151],v247,s[52:55],s91 offen offset:2048 sc1
mm v[76:79],v[220:223],v[184:187],v[76:79]
s_mov_b32 s18, 53248
s_lshl_b32 s97, s97, 10
mm v[76:79],v[216:219],v[168:171],v[76:79]
rt v[208:209],v242 offset:25472
rt v[210:211],v242 offset:26496
mm v[44:47],v[216:219],v[164:167],v[44:47]
ds_write_b128 v238, v[248:251] offset:5120
)ASM" R"ASM(mm v[44:47],v[220:223],v[180:183],v[44:47]
st a[132:135],v255,s[48:51],s18 offen offset:0 sc1
mm v[12:15],v[220:223],v[176:179],v[12:15]
wt lgkmcnt(5)
s_add_u32 s95, s95, s97
mm v[12:15],v[216:219],v[160:163],v[12:15]
rt v[220:221],v243 offset:17024
rt v[222:223],v243 offset:18048
mm v[16:19],v[196:199],v[160:163],v[16:19]
ar v224,a224
ar v225,a225
mm v[16:19],v[192:195],v[176:179],v[16:19]
ld v[152:155],v244,s[52:55],s91 offen offset:3072 sc1
mm v[48:51],v[192:195],v[180:183],v[48:51]
ar v226,a226
ar v227,a227
mm v[48:51],v[196:199],v[164:167],v[48:51]
rt v[216:217],v243 offset:25472
rt v[218:219],v243 offset:26496
mm v[80:83],v[196:199],v[168:171],v[80:83]
ar v228,a228
ar v229,a229
mm v[80:83],v[192:195],v[184:187],v[80:83]
ld v[156:159],v245,s[52:55],s91 offen offset:3072 sc1
mm v[112:115],v[192:195],v[188:191],v[112:115]
ar v230,a230
ar v231,a231
mm v[112:115],v[196:199],v[172:175],v[112:115]
cv v248,v224,v225
cv v249,v226,v227
mm v[116:119],v[204:207],v[172:175],v[116:119]
wt lgkmcnt(0)
s_barrier
mm v[116:119],v[200:203],v[188:191],v[116:119]
ld a[120:123],v246,s[52:55],s91 offen offset:3072 sc1
mm v[84:87],v[200:203],v[184:187],v[84:87]
rt v[192:193],v240 offset:33792
rt v[194:195],v240 offset:34816
mm v[84:87],v[204:207],v[168:171],v[84:87]
rt v[196:197],v240 offset:42240
rt v[198:199],v240 offset:43264
mm v[52:55],v[204:207],v[164:167],v[52:55]
s_add_u32 m0, s83, 16896
cv v250,v228,v229
mm v[52:55],v[200:203],v[180:183],v[52:55]
ld v232,s[44:47],s80 offen lds
mm v[20:23],v[200:203],v[176:179],v[20:23]
cv v251,v230,v231
aw a140,v248
mm v[20:23],v[204:207],v[160:163],v[20:23]
rt v[200:201],v241 offset:33792
rt v[202:203],v241 offset:34816
mm v[24:27],v[212:215],v[160:163],v[24:27]
aw a141,v249
aw a142,v250
mm v[24:27],v[208:211],v[176:179],v[24:27]
ld a[124:127],v247,s[52:55],s91 offen offset:3072 sc1
mm v[56:59],v[208:211],v[180:183],v[56:59]
s_add_u32 s91, s91, 4096
aw a143,v251
mm v[56:59],v[212:215],v[164:167],v[56:59]
rt v[204:205],v241 offset:42240
rt v[206:207],v241 offset:43264
mm v[88:91],v[212:215],v[168:171],v[88:91]
s_add_u32 m0, s83, 17920
s_mul_i32 s97, s93, 896
mm v[88:91],v[208:211],v[184:187],v[88:91]
ld v233,s[44:47],s80 offen lds
mm v[120:123],v[208:211],v[188:191],v[120:123]
s_cmp_eq_u32 s3, 0
s_cselect_b32 s95, s95, s97
s_cselect_b64 s[36:37], s[28:29], s[30:31]
s_cselect_b32 s21, 2, 7
s_cselect_b32 s93, s93, 0
mm v[120:123],v[212:215],v[172:175],v[120:123]
rt v[208:209],v242 offset:33792
rt v[210:211],v242 offset:34816
mm v[124:127],v[220:223],v[172:175],v[124:127]
s_cmp_eq_u32 s3, 2
s_cselect_b64 s[36:37], s[32:33], s[36:37]
mm v[124:127],v[216:219],v[188:191],v[124:127]
s_lshl_b32 s97, s97, 10
s_mov_b32 s92, 672
mm v[92:95],v[216:219],v[184:187],v[92:95]
s_mov_b32 s90, 352
mm v[92:95],v[220:223],v[168:171],v[92:95]
rt v[212:213],v242 offset:42240
rt v[214:215],v242 offset:43264
mm v[60:63],v[220:223],v[164:167],v[60:63]
ds_write_b128 v238, v[248:251] offset:6144
mm v[60:63],v[216:219],v[180:183],v[60:63]
st a[136:139],v255,s[48:51],s18 offen offset:1024 sc1
mm v[28:31],v[216:219],v[176:179],v[28:31]
wt vmcnt(14) lgkmcnt(5)
mm v[28:31],v[220:223],v[160:163],v[28:31]
rt v[216:217],v243 offset:33792
rt v[218:219],v243 offset:34816
mm v[0:3],v[192:195],a[96:99],v[0:3]
ar v224,a144
ar v225,a145
)ASM" R"ASM(mm v[0:3],v[196:199],a[112:115],v[0:3]
ld v[160:163],v244,s[52:55],s91 offen offset:0 sc1
mm v[32:35],v[196:199],a[116:119],v[32:35]
ar v226,a146
ar v227,a147
mm v[32:35],v[192:195],a[100:103],v[32:35]
rt v[220:221],v243 offset:42240
rt v[222:223],v243 offset:43264
mm v[64:67],v[192:195],a[104:107],v[64:67]
ar v228,a148
ar v229,a149
mm v[64:67],v[196:199],v[140:143],v[64:67]
ld v[164:167],v245,s[52:55],s91 offen offset:0 sc1
mm v[96:99],v[196:199],v[128:131],v[96:99]
ar v230,a150
ar v231,a151
mm v[96:99],v[192:195],a[108:111],v[96:99]
rt v[196:197],v240 offset:33920
rt v[198:199],v240 offset:34944
mm v[100:103],v[200:203],a[108:111],v[100:103]
s_add_u32 m0, s83, 19072
cv v248,v224,v225
mm v[100:103],v[204:207],v[128:131],v[100:103]
ld v234,s[44:47],s80 offen lds
mm v[68:71],v[204:207],v[140:143],v[68:71]
cv v249,v226,v227
cv v250,v228,v229
mm v[68:71],v[200:203],a[104:107],v[68:71]
rt v[192:193],v240 offset:42368
rt v[194:195],v240 offset:43392
mm v[36:39],v[200:203],a[100:103],v[36:39]
cv v251,v230,v231
aw a144,v248
mm v[36:39],v[204:207],a[116:119],v[36:39]
ld v[168:171],v246,s[52:55],s91 offen offset:0 sc1
mm v[4:7],v[204:207],a[112:115],v[4:7]
wt lgkmcnt(4)
aw a145,v249
mm v[4:7],v[200:203],a[96:99],v[4:7]
.La5_LW1_10:
s_cbranch_execz .La5_LW1_14
rt v[204:205],v241 offset:33920
rt v[206:207],v241 offset:34944
mm v[8:11],v[208:211],a[96:99],v[8:11]
s_add_u32 m0, s83, 20096
aw a146,v250
mm v[8:11],v[212:215],a[112:115],v[8:11]
ld v235,s[44:47],s80 offen lds
mm v[40:43],v[212:215],a[116:119],v[40:43]
s_add_u32 s80, s80, 524288
aw a147,v251
mm v[40:43],v[208:211],a[100:103],v[40:43]
rt v[200:201],v241 offset:42368
rt v[202:203],v241 offset:43392
mm v[72:75],v[208:211],a[104:107],v[72:75]
s_cmp_eq_u32 s3, 3
s_cselect_b32 s95, s97, s95
s_cselect_b64 s[36:37], s[34:35], s[36:37]
s_cselect_b32 s21, 1, s21
s_cselect_b32 s92, 544, s92
mm v[72:75],v[212:215],v[140:143],v[72:75]
ld v[172:175],v247,s[52:55],s91 offen offset:0 sc1
mm v[104:107],v[212:215],v[128:131],v[104:107]
s_lshl_b32 s21, s21, 10
s_and_b32 s7, s93, 1
mm v[104:107],v[208:211],a[108:111],v[104:107]
rt v[212:213],v242 offset:33920
rt v[214:215],v242 offset:34944
mm v[108:111],v[216:219],a[108:111],v[108:111]
s_cmp_eq_u32 s75, 1
s_cselect_b32 s97, s90, s92
s_cselect_b32 s92, s92, s90
mm v[108:111],v[220:223],v[128:131],v[108:111]
s_cmp_eq_u32 s75, s7
s_cselect_b32 s92, s92, 0
mm v[76:79],v[220:223],v[140:143],v[76:79]
s_mul_i32 s92, s92, s21
s_add_u32 s95, s95, s92
mm v[76:79],v[216:219],a[104:107],v[76:79]
rt v[208:209],v242 offset:42368
rt v[210:211],v242 offset:43392
mm v[44:47],v[216:219],a[100:103],v[44:47]
ds_write_b128 v238, v[248:251] offset:7168
mm v[44:47],v[220:223],a[116:119],v[44:47]
st a[140:143],v255,s[48:51],s18 offen offset:2048 sc1
mm v[12:15],v[220:223],a[112:115],v[12:15]
wt lgkmcnt(5)
s_mul_i32 s97, s97, s21
mm v[12:15],v[216:219],a[96:99],v[12:15]
rt v[220:221],v243 offset:33920
rt v[222:223],v243 offset:34944
mm v[16:19],v[196:199],a[96:99],v[16:19]
ar v224,a232
ar v225,a233
mm v[16:19],v[192:195],a[112:115],v[16:19]
ld v[176:179],v244,s[52:55],s91 offen offset:1024 sc1
mm v[48:51],v[192:195],a[116:119],v[48:51]
ar v226,a234
ar v227,a235
mm v[48:51],v[196:199],a[100:103],v[48:51]
)ASM" R"ASM(rt v[216:217],v243 offset:42368
rt v[218:219],v243 offset:43392
mm v[80:83],v[196:199],a[104:107],v[80:83]
ar v228,a236
ar v229,a237
mm v[80:83],v[192:195],v[140:143],v[80:83]
ld v[180:183],v245,s[52:55],s91 offen offset:1024 sc1
mm v[112:115],v[192:195],v[128:131],v[112:115]
ar v230,a238
ar v231,a239
mm v[112:115],v[196:199],a[108:111],v[112:115]
cv v248,v224,v225
cv v249,v226,v227
mm v[116:119],v[204:207],a[108:111],v[116:119]
wt lgkmcnt(0)
s_barrier
mm v[116:119],v[200:203],v[128:131],v[116:119]
ld v[184:187],v246,s[52:55],s91 offen offset:1024 sc1
mm v[84:87],v[200:203],v[140:143],v[84:87]
rt v[192:193],v240 offset:50688
rt v[194:195],v240 offset:51712
mm v[84:87],v[204:207],a[104:107],v[84:87]
rt v[196:197],v240 offset:59136
rt v[198:199],v240 offset:60160
mm v[52:55],v[204:207],a[100:103],v[52:55]
s_add_u32 m0, s83, 33792
cv v250,v228,v229
mm v[52:55],v[200:203],a[116:119],v[52:55]
ld v232,s[44:47],s80 offen lds
mm v[20:23],v[200:203],a[112:115],v[20:23]
cv v251,v230,v231
aw a148,v248
mm v[20:23],v[204:207],a[96:99],v[20:23]
rt v[200:201],v241 offset:50688
rt v[202:203],v241 offset:51712
mm v[24:27],v[212:215],a[96:99],v[24:27]
aw a149,v249
aw a150,v250
mm v[24:27],v[208:211],a[112:115],v[24:27]
ld v[188:191],v247,s[52:55],s91 offen offset:1024 sc1
mm v[56:59],v[208:211],a[116:119],v[56:59]
aw a151,v251
s_mul_i32 s90, s97, s7
mm v[56:59],v[212:215],a[100:103],v[56:59]
rt v[204:205],v241 offset:59136
rt v[206:207],v241 offset:60160
mm v[88:91],v[212:215],a[104:107],v[88:91]
s_add_u32 m0, s83, 34816
s_sub_u32 s97, s97, s21
mm v[88:91],v[208:211],v[140:143],v[88:91]
ld v233,s[44:47],s80 offen lds
mm v[120:123],v[208:211],v[128:131],v[120:123]
s_add_u32 s97, s97, 896
mm v[120:123],v[212:215],a[108:111],v[120:123]
rt v[208:209],v242 offset:50688
rt v[210:211],v242 offset:51712
mm v[124:127],v[220:223],a[108:111],v[124:127]
s_add_u32 s36, s36, s95
s_addc_u32 s37, s37, 0
mm v[124:127],v[216:219],v[128:131],v[124:127]
s_and_b32 s37, s37, 0xffff
s_mov_b32 s38, s97
mm v[92:95],v[216:219],v[140:143],v[92:95]
s_mov_b32 s39, 0x20000
s_lshl_b32 s93, s96, 6
mm v[92:95],v[220:223],a[104:107],v[92:95]
rt v[212:213],v242 offset:59136
rt v[214:215],v242 offset:60160
mm v[60:63],v[220:223],a[100:103],v[60:63]
ds_write_b128 v238, v[248:251] offset:32768
mm v[60:63],v[216:219],a[116:119],v[60:63]
s_mul_i32 s95, s94, 7332928
mm v[28:31],v[216:219],a[112:115],v[28:31]
wt vmcnt(15) lgkmcnt(5)
mm v[28:31],v[220:223],a[96:99],v[28:31]
rt v[216:217],v243 offset:50688
rt v[218:219],v243 offset:51712
mm v[0:3],v[192:195],v[132:135],v[0:3]
ar v224,a152
ar v225,a153
mm v[0:3],v[196:199],v[152:155],v[0:3]
ld v[128:131],v244,s[52:55],s91 offen offset:2048 sc1
mm v[32:35],v[196:199],v[156:159],v[32:35]
ar v226,a154
ar v227,a155
mm v[32:35],v[192:195],v[136:139],v[32:35]
rt v[220:221],v243 offset:59136
rt v[222:223],v243 offset:60160
mm v[64:67],v[192:195],v[144:147],v[64:67]
ar v228,a156
ar v229,a157
mm v[64:67],v[196:199],a[120:123],v[64:67]
ld v[140:143],v245,s[52:55],s91 offen offset:2048 sc1
mm v[96:99],v[196:199],a[124:127],v[96:99]
ar v230,a158
ar v231,a159
mm v[96:99],v[192:195],v[148:151],v[96:99]
rt v[196:197],v240 offset:50816
rt v[198:199],v240 offset:51840
mm v[100:103],v[200:203],v[148:151],v[100:103]
)ASM" R"ASM(s_add_u32 m0, s83, 35968
cv v248,v224,v225
mm v[100:103],v[204:207],a[124:127],v[100:103]
ld v234,s[44:47],s80 offen lds
mm v[68:71],v[204:207],a[120:123],v[68:71]
cv v249,v226,v227
cv v250,v228,v229
mm v[68:71],v[200:203],v[144:147],v[68:71]
rt v[192:193],v240 offset:59264
rt v[194:195],v240 offset:60288
mm v[36:39],v[200:203],v[136:139],v[36:39]
cv v251,v230,v231
aw a152,v248
mm v[36:39],v[204:207],v[156:159],v[36:39]
ld a[96:99],v246,s[52:55],s91 offen offset:2048 sc1
mm v[4:7],v[204:207],v[152:155],v[4:7]
wt lgkmcnt(4)
aw a153,v249
mm v[4:7],v[200:203],v[132:135],v[4:7]
rt v[204:205],v241 offset:50816
rt v[206:207],v241 offset:51840
mm v[8:11],v[208:211],v[132:135],v[8:11]
s_add_u32 m0, s83, 36992
aw a154,v250
mm v[8:11],v[212:215],v[152:155],v[8:11]
ld v235,s[44:47],s80 offen lds
mm v[40:43],v[212:215],v[156:159],v[40:43]
s_add_u32 s80, s80, 524288
aw a155,v251
mm v[40:43],v[208:211],v[136:139],v[40:43]
rt v[200:201],v241 offset:59264
rt v[202:203],v241 offset:60288
mm v[72:75],v[208:211],v[144:147],v[72:75]
s_cmp_eq_u32 s77, 0
s_cselect_b64 s[40:41], s[30:31], s[32:33]
mm v[72:75],v[212:215],a[120:123],v[72:75]
ld a[100:103],v247,s[52:55],s91 offen offset:2048 sc1
mm v[104:107],v[212:215],a[124:127],v[104:107]
s_add_u32 s40, s40, s93
s_addc_u32 s41, s41, 0
mm v[104:107],v[208:211],v[148:151],v[104:107]
rt v[212:213],v242 offset:50816
rt v[214:215],v242 offset:51840
mm v[108:111],v[216:219],v[148:151],v[108:111]
s_and_b32 s41, s41, 0xffff
s_mov_b32 s42, s95
mm v[108:111],v[220:223],a[124:127],v[108:111]
s_mov_b32 s43, 0x20000
s_lshr_b32 s92, s72, 1
mm v[76:79],v[220:223],a[120:123],v[76:79]
s_and_b32 s92, s92, 4
s_xor_b32 s92, s92, s72
mm v[76:79],v[216:219],v[144:147],v[76:79]
rt v[208:209],v242 offset:59264
rt v[210:211],v242 offset:60288
mm v[44:47],v[216:219],v[136:139],v[44:47]
ds_write_b128 v238, v[248:251] offset:33792
mm v[44:47],v[220:223],v[156:159],v[44:47]
st a[144:147],v255,s[48:51],s18 offen offset:3072 sc1
mm v[12:15],v[220:223],v[152:155],v[12:15]
wt lgkmcnt(5)
s_xor_b32 s92, s92, 1
mm v[12:15],v[216:219],v[132:135],v[12:15]
rt v[220:221],v243 offset:50816
rt v[222:223],v243 offset:51840
mm v[16:19],v[196:199],v[132:135],v[16:19]
ar v224,a208
ar v225,a209
mm v[16:19],v[192:195],v[152:155],v[16:19]
ld a[104:107],v244,s[52:55],s91 offen offset:3072 sc1
mm v[48:51],v[192:195],v[156:159],v[48:51]
ar v226,a210
ar v227,a211
mm v[48:51],v[196:199],v[136:139],v[48:51]
rt v[216:217],v243 offset:59264
rt v[218:219],v243 offset:60288
mm v[80:83],v[196:199],v[144:147],v[80:83]
ar v228,a212
ar v229,a213
mm v[80:83],v[192:195],a[120:123],v[80:83]
ld a[108:111],v245,s[52:55],s91 offen offset:3072 sc1
mm v[112:115],v[192:195],a[124:127],v[112:115]
ar v230,a214
ar v231,a215
mm v[112:115],v[196:199],v[148:151],v[112:115]
cv v248,v224,v225
cv v249,v226,v227
mm v[116:119],v[204:207],v[148:151],v[116:119]
wt lgkmcnt(0)
s_barrier
mm v[116:119],v[200:203],a[124:127],v[116:119]
ld a[112:115],v246,s[52:55],s91 offen offset:3072 sc1
mm v[84:87],v[200:203],a[120:123],v[84:87]
rt v[192:193],v240 offset:0
rt v[194:195],v240 offset:1024
mm v[84:87],v[204:207],v[144:147],v[84:87]
rt v[196:197],v240 offset:8448
rt v[198:199],v240 offset:9472
mm v[52:55],v[204:207],v[136:139],v[52:55]
s_add_u32 m0, s83, 50688
)ASM" R"ASM(cv v250,v228,v229
mm v[52:55],v[200:203],v[156:159],v[52:55]
ld v232,s[44:47],s80 offen lds
mm v[20:23],v[200:203],v[152:155],v[20:23]
cv v251,v230,v231
aw a156,v248
mm v[20:23],v[204:207],v[132:135],v[20:23]
rt v[200:201],v241 offset:0
rt v[202:203],v241 offset:1024
mm v[24:27],v[212:215],v[132:135],v[24:27]
aw a157,v249
aw a158,v250
mm v[24:27],v[208:211],v[152:155],v[24:27]
ld a[116:119],v247,s[52:55],s91 offen offset:3072 sc1
mm v[56:59],v[208:211],v[156:159],v[56:59]
s_add_u32 s91, s91, 4096
aw a159,v251
mm v[56:59],v[212:215],v[136:139],v[56:59]
rt v[204:205],v241 offset:8448
rt v[206:207],v241 offset:9472
mm v[88:91],v[212:215],v[144:147],v[88:91]
s_add_u32 m0, s83, 51712
s_and_b32 s101, s92, 7
mm v[88:91],v[208:211],a[120:123],v[88:91]
ld v233,s[44:47],s80 offen lds
mm v[120:123],v[208:211],a[124:127],v[120:123]
s_lshl_b32 s92, s101, 8
s_sub_u32 s93, 8388608, s92
mm v[120:123],v[212:215],v[148:151],v[120:123]
rt v[208:209],v242 offset:0
rt v[210:211],v242 offset:1024
mm v[124:127],v[220:223],v[148:151],v[124:127]
s_add_u32 s56, s28, s92
s_addc_u32 s57, s29, 0
mm v[124:127],v[216:219],a[124:127],v[124:127]
s_and_b32 s57, s57, 0xffff
s_mov_b32 s58, s93
mm v[92:95],v[216:219],a[120:123],v[92:95]
s_mov_b32 s18, 32768
s_mov_b32 s59, 0x20000
mm v[92:95],v[220:223],v[144:147],v[92:95]
rt v[212:213],v242 offset:8448
rt v[214:215],v242 offset:9472
mm v[60:63],v[220:223],v[136:139],v[60:63]
ds_write_b128 v238, v[248:251] offset:34816
mm v[60:63],v[216:219],v[156:159],v[60:63]
st a[148:151],v255,s[48:51],s18 offen offset:0 sc1
mm v[28:31],v[216:219],v[152:155],v[28:31]
wt vmcnt(15) lgkmcnt(5)
s_lshl_b32 s93, s96, 16
mm v[28:31],v[220:223],v[132:135],v[28:31]
rt v[216:217],v243 offset:0
rt v[218:219],v243 offset:1024
mm v[0:3],v[192:195],v[160:163],v[0:3]
ar v224,a160
ar v225,a161
mm v[0:3],v[196:199],v[176:179],v[0:3]
ld v[132:135],v244,s[52:55],s91 offen offset:0 sc1
mm v[32:35],v[196:199],v[180:183],v[32:35]
ar v226,a162
ar v227,a163
mm v[32:35],v[192:195],v[164:167],v[32:35]
rt v[220:221],v243 offset:8448
rt v[222:223],v243 offset:9472
mm v[64:67],v[192:195],v[168:171],v[64:67]
ar v228,a164
ar v229,a165
mm v[64:67],v[196:199],v[184:187],v[64:67]
ld v[136:139],v245,s[52:55],s91 offen offset:0 sc1
mm v[96:99],v[196:199],v[188:191],v[96:99]
ar v230,a166
ar v231,a167
mm v[96:99],v[192:195],v[172:175],v[96:99]
rt v[196:197],v240 offset:128
rt v[198:199],v240 offset:1152
mm v[100:103],v[200:203],v[172:175],v[100:103]
s_add_u32 m0, s83, 52864
cv v248,v224,v225
mm v[100:103],v[204:207],v[188:191],v[100:103]
ld v234,s[44:47],s80 offen lds
mm v[68:71],v[204:207],v[184:187],v[68:71]
cv v249,v226,v227
cv v250,v228,v229
mm v[68:71],v[200:203],v[168:171],v[68:71]
rt v[192:193],v240 offset:8576
rt v[194:195],v240 offset:9600
mm v[36:39],v[200:203],v[164:167],v[36:39]
cv v251,v230,v231
aw a160,v248
mm v[36:39],v[204:207],v[180:183],v[36:39]
ld v[144:147],v246,s[52:55],s91 offen offset:0 sc1
mm v[4:7],v[204:207],v[176:179],v[4:7]
wt lgkmcnt(4)
aw a161,v249
mm v[4:7],v[200:203],v[160:163],v[4:7]
rt v[204:205],v241 offset:128
rt v[206:207],v241 offset:1152
mm v[8:11],v[208:211],v[160:163],v[8:11]
s_add_u32 m0, s83, 53888
aw a162,v250
)ASM" R"ASM(mm v[8:11],v[212:215],v[176:179],v[8:11]
ld v235,s[44:47],s80 offen lds
mm v[40:43],v[212:215],v[180:183],v[40:43]
s_add_u32 s80, s80, 524288
aw a163,v251
mm v[40:43],v[208:211],v[164:167],v[40:43]
rt v[200:201],v241 offset:8576
rt v[202:203],v241 offset:9600
mm v[72:75],v[208:211],v[168:171],v[72:75]
s_lshl_b32 s95, s94, 16
mm v[72:75],v[212:215],v[184:187],v[72:75]
ld v[148:151],v247,s[52:55],s91 offen offset:0 sc1
mm v[104:107],v[212:215],v[188:191],v[104:107]
s_add_u32 s28, s34, s93
s_addc_u32 s29, s35, 0
mm v[104:107],v[208:211],v[172:175],v[104:107]
rt v[212:213],v242 offset:128
rt v[214:215],v242 offset:1152
mm v[108:111],v[216:219],v[172:175],v[108:111]
s_and_b32 s29, s29, 0xffff
s_mov_b32 s30, s95
mm v[108:111],v[220:223],v[188:191],v[108:111]
s_mov_b32 s31, 0x20000
s_lshl_b32 s93, s101, 12
mm v[76:79],v[220:223],v[184:187],v[76:79]
s_add_u32 s92, s93, 71303168
s_sub_u32 s93, 16777216, s93
mm v[76:79],v[216:219],v[168:171],v[76:79]
rt v[208:209],v242 offset:8576
rt v[210:211],v242 offset:9600
mm v[44:47],v[216:219],v[164:167],v[44:47]
ds_write_b128 v238, v[248:251] offset:35840
mm v[44:47],v[220:223],v[180:183],v[44:47]
st a[152:155],v255,s[48:51],s18 offen offset:1024 sc1
mm v[12:15],v[220:223],v[176:179],v[12:15]
wt lgkmcnt(5)
mm v[12:15],v[216:219],v[160:163],v[12:15]
rt v[220:221],v243 offset:128
rt v[222:223],v243 offset:1152
mm v[16:19],v[196:199],v[160:163],v[16:19]
ar v224,a216
ar v225,a217
mm v[16:19],v[192:195],v[176:179],v[16:19]
ld v[152:155],v244,s[52:55],s91 offen offset:1024 sc1
mm v[48:51],v[192:195],v[180:183],v[48:51]
ar v226,a218
ar v227,a219
mm v[48:51],v[196:199],v[164:167],v[48:51]
rt v[216:217],v243 offset:8576
rt v[218:219],v243 offset:9600
mm v[80:83],v[196:199],v[168:171],v[80:83]
ar v228,a220
ar v229,a221
mm v[80:83],v[192:195],v[184:187],v[80:83]
ld v[156:159],v245,s[52:55],s91 offen offset:1024 sc1
mm v[112:115],v[192:195],v[188:191],v[112:115]
ar v230,a222
ar v231,a223
mm v[112:115],v[196:199],v[172:175],v[112:115]
cv v248,v224,v225
cv v249,v226,v227
mm v[116:119],v[204:207],v[172:175],v[116:119]
wt lgkmcnt(0)
s_barrier
mm v[116:119],v[200:203],v[188:191],v[116:119]
ld a[120:123],v246,s[52:55],s91 offen offset:1024 sc1
mm v[84:87],v[200:203],v[184:187],v[84:87]
rt v[192:193],v240 offset:16896
rt v[194:195],v240 offset:17920
mm v[84:87],v[204:207],v[168:171],v[84:87]
rt v[196:197],v240 offset:25344
rt v[198:199],v240 offset:26368
mm v[52:55],v[204:207],v[164:167],v[52:55]
s_add_u32 m0, s83, 0
cv v250,v228,v229
mm v[52:55],v[200:203],v[180:183],v[52:55]
ld v232,s[44:47],s80 offen lds
mm v[20:23],v[200:203],v[176:179],v[20:23]
cv v251,v230,v231
aw a164,v248
mm v[20:23],v[204:207],v[160:163],v[20:23]
rt v[200:201],v241 offset:16896
rt v[202:203],v241 offset:17920
mm v[24:27],v[212:215],v[160:163],v[24:27]
aw a165,v249
aw a166,v250
mm v[24:27],v[208:211],v[176:179],v[24:27]
ld a[124:127],v247,s[52:55],s91 offen offset:1024 sc1
mm v[56:59],v[208:211],v[180:183],v[56:59]
aw a167,v251
mm v[56:59],v[212:215],v[164:167],v[56:59]
rt v[204:205],v241 offset:25344
rt v[206:207],v241 offset:26368
mm v[88:91],v[212:215],v[168:171],v[88:91]
s_add_u32 m0, s83, 1024
mm v[88:91],v[208:211],v[184:187],v[88:91]
ld v233,s[44:47],s80 offen lds
mm v[120:123],v[208:211],v[188:191],v[120:123]
s_add_u32 s32, s26, s92
s_addc_u32 s33, s27, 0
)ASM" R"ASM(mm v[120:123],v[212:215],v[172:175],v[120:123]
rt v[208:209],v242 offset:16896
rt v[210:211],v242 offset:17920
mm v[124:127],v[220:223],v[172:175],v[124:127]
s_and_b32 s33, s33, 0xffff
s_mov_b32 s34, s93
mm v[124:127],v[216:219],v[188:191],v[124:127]
s_mov_b32 s35, 0x20000
s_lshl2_add_u32 s92, s72, s74
mm v[92:95],v[216:219],v[184:187],v[92:95]
s_lshl2_add_u32 s92, s92, s3
s_lshl_b32 s88, s92, 8
mm v[92:95],v[220:223],v[168:171],v[92:95]
rt v[212:213],v242 offset:25344
rt v[214:215],v242 offset:26368
mm v[60:63],v[220:223],v[164:167],v[60:63]
ds_write_b128 v238, v[248:251] offset:36864
mm v[60:63],v[216:219],v[180:183],v[60:63]
st a[156:159],v255,s[48:51],s18 offen offset:2048 sc1
mm v[28:31],v[216:219],v[176:179],v[28:31]
wt vmcnt(16) lgkmcnt(5)
s_mul_i32 s93, s92, 17408
mm v[28:31],v[220:223],v[160:163],v[28:31]
rt v[216:217],v243 offset:16896
rt v[218:219],v243 offset:17920
mm v[0:3],v[192:195],v[128:131],v[0:3]
ar v224,a168
ar v225,a169
mm v[0:3],v[196:199],a[104:107],v[0:3]
ld v[160:163],v244,s[52:55],s91 offen offset:2048 sc1
mm v[32:35],v[196:199],a[108:111],v[32:35]
ar v226,a170
ar v227,a171
mm v[32:35],v[192:195],v[140:143],v[32:35]
rt v[220:221],v243 offset:25344
rt v[222:223],v243 offset:26368
mm v[64:67],v[192:195],a[96:99],v[64:67]
ar v228,a172
ar v229,a173
mm v[64:67],v[196:199],a[112:115],v[64:67]
ld v[164:167],v245,s[52:55],s91 offen offset:2048 sc1
mm v[96:99],v[196:199],a[116:119],v[96:99]
ar v230,a174
ar v231,a175
mm v[96:99],v[192:195],a[100:103],v[96:99]
rt v[196:197],v240 offset:17024
rt v[198:199],v240 offset:18048
mm v[100:103],v[200:203],a[100:103],v[100:103]
s_add_u32 m0, s83, 2176
cv v248,v224,v225
mm v[100:103],v[204:207],a[116:119],v[100:103]
ld v234,s[44:47],s80 offen lds
mm v[68:71],v[204:207],a[112:115],v[68:71]
cv v249,v226,v227
cv v250,v228,v229
mm v[68:71],v[200:203],a[96:99],v[68:71]
rt v[192:193],v240 offset:25472
rt v[194:195],v240 offset:26496
mm v[36:39],v[200:203],v[140:143],v[36:39]
cv v251,v230,v231
aw a168,v248
mm v[36:39],v[204:207],a[108:111],v[36:39]
ld v[168:171],v246,s[52:55],s91 offen offset:2048 sc1
mm v[4:7],v[204:207],a[104:107],v[4:7]
wt lgkmcnt(4)
aw a169,v249
mm v[4:7],v[200:203],v[128:131],v[4:7]
rt v[204:205],v241 offset:17024
rt v[206:207],v241 offset:18048
mm v[8:11],v[208:211],v[128:131],v[8:11]
s_add_u32 m0, s83, 3200
aw a170,v250
mm v[8:11],v[212:215],a[104:107],v[8:11]
ld v235,s[44:47],s80 offen lds
mm v[40:43],v[212:215],a[108:111],v[40:43]
s_add_u32 s80, s80, 524288
aw a171,v251
mm v[40:43],v[208:211],v[140:143],v[40:43]
rt v[200:201],v241 offset:25472
rt v[202:203],v241 offset:26496
mm v[72:75],v[208:211],a[96:99],v[72:75]
s_lshl_b32 s92, s92, 14
s_add_u32 s92, s92, 4194304
mm v[72:75],v[212:215],a[112:115],v[72:75]
.La5_LW1_11:
s_cbranch_execz .La5_LW1_15
ld v[172:175],v247,s[52:55],s91 offen offset:2048 sc1
mm v[104:107],v[212:215],a[116:119],v[104:107]
s_add_u32 s86, s26, s92
s_addc_u32 s87, s27, 0
mm v[104:107],v[208:211],a[100:103],v[104:107]
rt v[212:213],v242 offset:17024
rt v[214:215],v242 offset:18048
mm v[108:111],v[216:219],a[100:103],v[108:111]
s_and_b32 s87, s87, 0xffff
s_add_u32 s93, s93, 150994944
mm v[108:111],v[220:223],a[116:119],v[108:111]
s_add_u32 s68, s26, s93
s_addc_u32 s69, s27, 0
mm v[76:79],v[220:223],a[112:115],v[76:79]
)ASM" R"ASM(s_and_b32 s69, s69, 0xffff
s_mov_b32 s70, 17408
mm v[76:79],v[216:219],a[96:99],v[76:79]
rt v[208:209],v242 offset:25472
rt v[210:211],v242 offset:26496
mm v[44:47],v[216:219],v[140:143],v[44:47]
ds_write_b128 v238, v[248:251] offset:37888
mm v[44:47],v[220:223],a[108:111],v[44:47]
st a[160:163],v255,s[48:51],s18 offen offset:3072 sc1
mm v[12:15],v[220:223],a[104:107],v[12:15]
wt lgkmcnt(5)
s_mov_b32 s96, 153158689
mm v[12:15],v[216:219],v[128:131],v[12:15]
rt v[220:221],v243 offset:17024
rt v[222:223],v243 offset:18048
mm v[16:19],v[196:199],v[128:131],v[16:19]
ar v224,a192
ar v225,a193
mm v[16:19],v[192:195],a[104:107],v[16:19]
ld v[176:179],v244,s[52:55],s91 offen offset:3072 sc1
mm v[48:51],v[192:195],a[108:111],v[48:51]
ar v226,a194
ar v227,a195
mm v[48:51],v[196:199],v[140:143],v[48:51]
rt v[216:217],v243 offset:25472
rt v[218:219],v243 offset:26496
mm v[80:83],v[196:199],a[96:99],v[80:83]
ar v228,a196
ar v229,a197
mm v[80:83],v[192:195],a[112:115],v[80:83]
ld v[180:183],v245,s[52:55],s91 offen offset:3072 sc1
mm v[112:115],v[192:195],a[116:119],v[112:115]
ar v230,a198
ar v231,a199
mm v[112:115],v[196:199],a[100:103],v[112:115]
cv v248,v224,v225
cv v249,v226,v227
mm v[116:119],v[204:207],a[100:103],v[116:119]
wt lgkmcnt(0)
s_barrier
mm v[116:119],v[200:203],a[116:119],v[116:119]
ld v[184:187],v246,s[52:55],s91 offen offset:3072 sc1
mm v[84:87],v[200:203],a[112:115],v[84:87]
rt v[192:193],v240 offset:33792
rt v[194:195],v240 offset:34816
mm v[84:87],v[204:207],a[96:99],v[84:87]
rt v[196:197],v240 offset:42240
rt v[198:199],v240 offset:43264
mm v[52:55],v[204:207],v[140:143],v[52:55]
s_add_u32 m0, s83, 16896
cv v250,v228,v229
mm v[52:55],v[200:203],a[108:111],v[52:55]
ld v232,s[44:47],s80 offen lds
mm v[20:23],v[200:203],a[104:107],v[20:23]
cv v251,v230,v231
aw a172,v248
mm v[20:23],v[204:207],v[128:131],v[20:23]
rt v[200:201],v241 offset:33792
rt v[202:203],v241 offset:34816
mm v[24:27],v[212:215],v[128:131],v[24:27]
aw a173,v249
aw a174,v250
mm v[24:27],v[208:211],a[104:107],v[24:27]
ld v[188:191],v247,s[52:55],s91 offen offset:3072 sc1
mm v[56:59],v[208:211],a[108:111],v[56:59]
s_add_u32 s91, s91, 4096
aw a175,v251
mm v[56:59],v[212:215],v[140:143],v[56:59]
rt v[204:205],v241 offset:42240
rt v[206:207],v241 offset:43264
mm v[88:91],v[212:215],a[96:99],v[88:91]
s_add_u32 m0, s83, 17920
s_mov_b32 s97, 371265825
mm v[88:91],v[208:211],a[112:115],v[88:91]
ld v233,s[44:47],s80 offen lds
mm v[120:123],v[208:211],a[116:119],v[120:123]
s_lshl_b32 s95, s73, 3
s_lshr_b64 s[96:97], s[96:97], s95
mm v[120:123],v[212:215],a[100:103],v[120:123]
rt v[208:209],v242 offset:33792
rt v[210:211],v242 offset:34816
mm v[124:127],v[220:223],a[100:103],v[124:127]
s_and_b32 s82, s96, 255
s_lshl_b64 s[98:99], 1, s82
mm v[124:127],v[216:219],a[116:119],v[124:127]
s_lshl_b32 s96, s75, 22
s_or_b32 s95, s98, s96
mm v[92:95],v[216:219],a[112:115],v[92:95]
s_cmp_eq_u32 s73, 1
s_cselect_b32 s96, 12, 4
s_cselect_b32 s79, 0, 0xff000000
mm v[92:95],v[220:223],a[96:99],v[92:95]
rt v[212:213],v242 offset:42240
rt v[214:215],v242 offset:43264
mm v[60:63],v[220:223],v[140:143],v[60:63]
ds_write_b128 v238, v[248:251] offset:38912
mm v[60:63],v[216:219],a[108:111],v[60:63]
s_or_b32 s98, s95, s96
mm v[28:31],v[216:219],a[104:107],v[28:31]
wt vmcnt(15) lgkmcnt(5)
)ASM" R"ASM(mm v[28:31],v[220:223],v[128:131],v[28:31]
rt v[216:217],v243 offset:33792
rt v[218:219],v243 offset:34816
mm v[0:3],v[192:195],v[132:135],v[0:3]
ar v224,a176
ar v225,a177
mm v[0:3],v[196:199],v[152:155],v[0:3]
ld v[128:131],v244,s[52:55],s91 offen offset:0 sc1
mm v[32:35],v[196:199],v[156:159],v[32:35]
ar v226,a178
ar v227,a179
mm v[32:35],v[192:195],v[136:139],v[32:35]
rt v[220:221],v243 offset:42240
rt v[222:223],v243 offset:43264
mm v[64:67],v[192:195],v[144:147],v[64:67]
ar v228,a180
ar v229,a181
mm v[64:67],v[196:199],a[120:123],v[64:67]
ld v[140:143],v245,s[52:55],s91 offen offset:0 sc1
mm v[96:99],v[196:199],a[124:127],v[96:99]
ar v230,a182
ar v231,a183
mm v[96:99],v[192:195],v[148:151],v[96:99]
rt v[196:197],v240 offset:33920
rt v[198:199],v240 offset:34944
mm v[100:103],v[200:203],v[148:151],v[100:103]
s_add_u32 m0, s83, 19072
cv v248,v224,v225
mm v[100:103],v[204:207],a[124:127],v[100:103]
ld v234,s[44:47],s80 offen lds
mm v[68:71],v[204:207],a[120:123],v[68:71]
cv v249,v226,v227
cv v250,v228,v229
mm v[68:71],v[200:203],v[144:147],v[68:71]
rt v[192:193],v240 offset:42368
rt v[194:195],v240 offset:43392
mm v[36:39],v[200:203],v[136:139],v[36:39]
cv v251,v230,v231
aw a176,v248
mm v[36:39],v[204:207],v[156:159],v[36:39]
ld a[96:99],v246,s[52:55],s91 offen offset:0 sc1
mm v[4:7],v[204:207],v[152:155],v[4:7]
wt lgkmcnt(4)
aw a177,v249
mm v[4:7],v[200:203],v[132:135],v[4:7]
rt v[204:205],v241 offset:33920
rt v[206:207],v241 offset:34944
mm v[8:11],v[208:211],v[132:135],v[8:11]
s_add_u32 m0, s83, 20096
aw a178,v250
mm v[8:11],v[212:215],v[152:155],v[8:11]
ld v235,s[44:47],s80 offen lds
mm v[40:43],v[212:215],v[156:159],v[40:43]
s_add_u32 s80, s80, 524288
aw a179,v251
mm v[40:43],v[208:211],v[136:139],v[40:43]
rt v[200:201],v241 offset:42368
rt v[202:203],v241 offset:43392
mm v[72:75],v[208:211],v[144:147],v[72:75]
s_cmp_eq_u32 s74, 3
s_cselect_b32 s23, 3, 0
s_cselect_b32 s22, 0, s75
mm v[72:75],v[212:215],a[120:123],v[72:75]
ld a[100:103],v247,s[52:55],s91 offen offset:0 sc1
mm v[104:107],v[212:215],a[124:127],v[104:107]
s_lshl_b32 s95, s76, 5
s_add_u32 s95, s95, s3
mm v[104:107],v[208:211],v[148:151],v[104:107]
rt v[212:213],v242 offset:33920
rt v[214:215],v242 offset:34944
mm v[108:111],v[216:219],v[148:151],v[108:111]
s_lshl_b32 s95, s95, 5
s_add_u32 s95, s95, s72
mm v[108:111],v[220:223],a[124:127],v[108:111]
s_lshl_b32 s95, s95, 3
s_add_u32 s6, s95, 1048576
mm v[76:79],v[220:223],a[120:123],v[76:79]
s_mov_b32 s18, 36864
v_and_b32 v239, 0x70, v255
mm v[76:79],v[216:219],v[144:147],v[76:79]
rt v[208:209],v242 offset:42368
rt v[210:211],v242 offset:43392
mm v[44:47],v[216:219],v[136:139],v[44:47]
ds_write_b128 v238, v[248:251] offset:39936
mm v[44:47],v[220:223],v[156:159],v[44:47]
st a[164:167],v255,s[48:51],s18 offen offset:0 sc1
mm v[12:15],v[220:223],v[152:155],v[12:15]
wt lgkmcnt(5)
v_lshlrev_b32 v239, 6, v239
mm v[12:15],v[216:219],v[132:135],v[12:15]
rt v[220:221],v243 offset:33920
rt v[222:223],v243 offset:34944
mm v[16:19],v[196:199],v[132:135],v[16:19]
ar v224,a200
ar v225,a201
mm v[16:19],v[192:195],v[152:155],v[16:19]
ld a[104:107],v244,s[52:55],s91 offen offset:1024 sc1
mm v[48:51],v[192:195],v[156:159],v[48:51]
ar v226,a202
ar v227,a203
)ASM" R"ASM(mm v[48:51],v[196:199],v[136:139],v[48:51]
rt v[216:217],v243 offset:42368
rt v[218:219],v243 offset:43392
mm v[80:83],v[196:199],v[144:147],v[80:83]
ar v228,a204
ar v229,a205
mm v[80:83],v[192:195],a[120:123],v[80:83]
ld a[108:111],v245,s[52:55],s91 offen offset:1024 sc1
mm v[112:115],v[192:195],a[124:127],v[112:115]
ar v230,a206
ar v231,a207
mm v[112:115],v[196:199],v[148:151],v[112:115]
cv v248,v224,v225
cv v249,v226,v227
mm v[116:119],v[204:207],v[148:151],v[116:119]
wt lgkmcnt(0)
s_barrier
mm v[116:119],v[200:203],a[124:127],v[116:119]
ld a[112:115],v246,s[52:55],s91 offen offset:1024 sc1
mm v[84:87],v[200:203],a[120:123],v[84:87]
rt v[192:193],v240 offset:50688
rt v[194:195],v240 offset:51712
mm v[84:87],v[204:207],v[144:147],v[84:87]
rt v[196:197],v240 offset:59136
rt v[198:199],v240 offset:60160
mm v[52:55],v[204:207],v[136:139],v[52:55]
s_add_u32 m0, s83, 33792
cv v250,v228,v229
mm v[52:55],v[200:203],v[156:159],v[52:55]
ld v232,s[44:47],s80 offen lds
mm v[20:23],v[200:203],v[152:155],v[20:23]
cv v251,v230,v231
aw a180,v248
mm v[20:23],v[204:207],v[132:135],v[20:23]
rt v[200:201],v241 offset:50688
rt v[202:203],v241 offset:51712
mm v[24:27],v[212:215],v[132:135],v[24:27]
aw a181,v249
aw a182,v250
mm v[24:27],v[208:211],v[152:155],v[24:27]
ld a[116:119],v247,s[52:55],s91 offen offset:1024 sc1
mm v[56:59],v[208:211],v[156:159],v[56:59]
aw a183,v251
s_add_u32 s85, s85, 65536
mm v[56:59],v[212:215],v[136:139],v[56:59]
rt v[204:205],v241 offset:59136
rt v[206:207],v241 offset:60160
mm v[88:91],v[212:215],v[144:147],v[88:91]
s_add_u32 m0, s83, 34816
v_add_u32 v238, s85, v255
mm v[88:91],v[208:211],a[120:123],v[88:91]
ld v233,s[44:47],s80 offen lds
mm v[120:123],v[208:211],a[124:127],v[120:123]
s_mov_b32 s85, 0
aw a231,v239
mm v[120:123],v[212:215],v[148:151],v[120:123]
rt v[208:209],v242 offset:50688
rt v[210:211],v242 offset:51712
mm v[124:127],v[220:223],v[148:151],v[124:127]
s_mul_i32 s95, s75, 0
s_add_u32 s95, s95, 2
mm v[124:127],v[216:219],a[124:127],v[124:127]
s_sub_u32 s96, 4, s95
s_lshr_b32 s97, s2, 4
mm v[92:95],v[216:219],a[120:123],v[92:95]
s_mul_i32 s97, s97, s95
v_lshrrev_b32 v239, 7, v255
mm v[92:95],v[220:223],v[144:147],v[92:95]
rt v[212:213],v242 offset:59136
rt v[214:215],v242 offset:60160
mm v[60:63],v[220:223],v[136:139],v[60:63]
ds_write_b128 v238, v[248:251] offset:0
mm v[60:63],v[216:219],v[156:159],v[60:63]
st a[168:171],v255,s[48:51],s18 offen offset:1024 sc1
mm v[28:31],v[216:219],v[152:155],v[28:31]
wt vmcnt(15) lgkmcnt(5)
v_add_u32 v254, s97, v239
mm v[28:31],v[220:223],v[132:135],v[28:31]
rt v[216:217],v243 offset:50688
rt v[218:219],v243 offset:51712
mm v[0:3],v[192:195],v[160:163],v[0:3]
ar v224,a184
ar v225,a185
mm v[0:3],v[196:199],v[176:179],v[0:3]
ld v[132:135],v244,s[52:55],s91 offen offset:2048 sc1
mm v[32:35],v[196:199],v[180:183],v[32:35]
ar v226,a186
ar v227,a187
mm v[32:35],v[192:195],v[164:167],v[32:35]
rt v[220:221],v243 offset:59136
rt v[222:223],v243 offset:60160
mm v[64:67],v[192:195],v[168:171],v[64:67]
ar v228,a188
ar v229,a189
mm v[64:67],v[196:199],v[184:187],v[64:67]
ld v[136:139],v245,s[52:55],s91 offen offset:2048 sc1
mm v[96:99],v[196:199],v[188:191],v[96:99]
ar v230,a190
ar v231,a191
mm v[96:99],v[192:195],v[172:175],v[96:99]
)ASM" R"ASM(rt v[196:197],v240 offset:50816
rt v[198:199],v240 offset:51840
mm v[100:103],v[200:203],v[172:175],v[100:103]
s_add_u32 m0, s83, 35968
cv v248,v224,v225
mm v[100:103],v[204:207],v[188:191],v[100:103]
ld v234,s[44:47],s80 offen lds
mm v[68:71],v[204:207],v[184:187],v[68:71]
cv v249,v226,v227
cv v250,v228,v229
mm v[68:71],v[200:203],v[168:171],v[68:71]
rt v[192:193],v240 offset:59264
rt v[194:195],v240 offset:60288
mm v[36:39],v[200:203],v[164:167],v[36:39]
cv v251,v230,v231
aw a184,v248
mm v[36:39],v[204:207],v[180:183],v[36:39]
ld v[144:147],v246,s[52:55],s91 offen offset:2048 sc1
mm v[4:7],v[204:207],v[176:179],v[4:7]
wt lgkmcnt(4)
aw a185,v249
mm v[4:7],v[200:203],v[160:163],v[4:7]
rt v[204:205],v241 offset:50816
rt v[206:207],v241 offset:51840
mm v[8:11],v[208:211],v[160:163],v[8:11]
s_add_u32 m0, s83, 36992
aw a186,v250
mm v[8:11],v[212:215],v[176:179],v[8:11]
ld v235,s[44:47],s80 offen lds
mm v[40:43],v[212:215],v[180:183],v[40:43]
s_add_u32 s80, s80, 524288
aw a187,v251
mm v[40:43],v[208:211],v[164:167],v[40:43]
rt v[200:201],v241 offset:59264
rt v[202:203],v241 offset:60288
mm v[72:75],v[208:211],v[168:171],v[72:75]
v_mul_u32_u24 v254, s21, v254
v_add_u32 v239, s96, v239
mm v[72:75],v[212:215],v[184:187],v[72:75]
ld v[148:151],v247,s[52:55],s91 offen offset:2048 sc1
mm v[104:107],v[212:215],v[188:191],v[104:107]
v_lshrrev_b32 v239, 2, v239
v_lshl_add_u32 v254, v239, 28, v254
mm v[104:107],v[208:211],v[172:175],v[104:107]
rt v[212:213],v242 offset:50816
rt v[214:215],v242 offset:51840
mm v[108:111],v[216:219],v[172:175],v[108:111]
v_and_b32 v239, 0x70, v255
v_lshl_add_u32 v254, v239, 3, v254
mm v[108:111],v[220:223],v[188:191],v[108:111]
s_lshl_b32 s96, s7, 5
s_sub_u32 s96, 16, s96
mm v[76:79],v[220:223],v[184:187],v[76:79]
s_mul_i32 s95, s95, s96
s_mul_i32 s21, s21, s95
mm v[76:79],v[216:219],v[168:171],v[76:79]
rt v[208:209],v242 offset:59264
rt v[210:211],v242 offset:60288
mm v[44:47],v[216:219],v[164:167],v[44:47]
ds_write_b128 v238, v[248:251] offset:1024
mm v[44:47],v[220:223],v[180:183],v[44:47]
st a[172:175],v255,s[48:51],s18 offen offset:2048 sc1
mm v[12:15],v[220:223],v[176:179],v[12:15]
wt lgkmcnt(5)
v_lshrrev_b32 v236, 8, v255
mm v[12:15],v[216:219],v[160:163],v[12:15]
rt v[220:221],v243 offset:50816
rt v[222:223],v243 offset:51840
mm v[16:19],v[196:199],v[160:163],v[16:19]
ar v224,a64
ar v225,a65
mm v[16:19],v[192:195],v[176:179],v[16:19]
ld v[152:155],v244,s[52:55],s91 offen offset:3072 sc1
mm v[48:51],v[192:195],v[180:183],v[48:51]
ar v226,a66
ar v227,a67
mm v[48:51],v[196:199],v[164:167],v[48:51]
rt v[216:217],v243 offset:59264
rt v[218:219],v243 offset:60288
mm v[80:83],v[196:199],v[168:171],v[80:83]
ar v228,a68
ar v229,a69
mm v[80:83],v[192:195],v[184:187],v[80:83]
ld v[156:159],v245,s[52:55],s91 offen offset:3072 sc1
mm v[112:115],v[192:195],v[188:191],v[112:115]
ar v230,a70
ar v231,a71
mm v[112:115],v[196:199],v[172:175],v[112:115]
cv v248,v224,v225
cv v249,v226,v227
mm v[116:119],v[204:207],v[172:175],v[116:119]
wt lgkmcnt(0)
s_barrier
mm v[116:119],v[200:203],v[188:191],v[116:119]
ld a[120:123],v246,s[52:55],s91 offen offset:3072 sc1
mm v[84:87],v[200:203],v[184:187],v[84:87]
rt v[192:193],v240 offset:0
rt v[194:195],v240 offset:1024
mm v[84:87],v[204:207],v[168:171],v[84:87]
rt v[196:197],v240 offset:8448
)ASM" R"ASM(rt v[198:199],v240 offset:9472
mm v[52:55],v[204:207],v[164:167],v[52:55]
s_add_u32 m0, s83, 50688
cv v250,v228,v229
mm v[52:55],v[200:203],v[180:183],v[52:55]
ld v232,s[44:47],s80 offen lds
mm v[20:23],v[200:203],v[176:179],v[20:23]
cv v251,v230,v231
aw a188,v248
mm v[20:23],v[204:207],v[160:163],v[20:23]
rt v[200:201],v241 offset:0
rt v[202:203],v241 offset:1024
mm v[24:27],v[212:215],v[160:163],v[24:27]
aw a189,v249
aw a190,v250
mm v[24:27],v[208:211],v[176:179],v[24:27]
ld a[124:127],v247,s[52:55],s91 offen offset:3072 sc1
mm v[56:59],v[208:211],v[180:183],v[56:59]
s_add_u32 s91, s91, 4096
aw a191,v251
mm v[56:59],v[212:215],v[164:167],v[56:59]
rt v[204:205],v241 offset:8448
rt v[206:207],v241 offset:9472
mm v[88:91],v[212:215],v[168:171],v[88:91]
s_add_u32 m0, s83, 51712
v_lshlrev_b32 v239, 5, v236
mm v[88:91],v[208:211],v[184:187],v[88:91]
ld v233,s[44:47],s80 offen lds
mm v[120:123],v[208:211],v[188:191],v[120:123]
v_xor_b32 v239, v239, v255
v_and_b32 v239, 0xf0, v239
mm v[120:123],v[212:215],v[172:175],v[120:123]
rt v[208:209],v242 offset:0
rt v[210:211],v242 offset:1024
mm v[124:127],v[220:223],v[172:175],v[124:127]
v_lshl_add_u32 v239, v236, 11, v239
s_lshl_b32 s92, s77, 16
mm v[124:127],v[216:219],v[188:191],v[124:127]
s_lshl_b32 s93, s78, 14
s_add_u32 s92, s92, s93
mm v[92:95],v[216:219],v[184:187],v[92:95]
v_add_u32 v239, s92, v239
aw a228,v239
mm v[92:95],v[220:223],v[168:171],v[92:95]
rt v[212:213],v242 offset:8448
rt v[214:215],v242 offset:9472
mm v[60:63],v[220:223],v[164:167],v[60:63]
ds_write_b128 v238, v[248:251] offset:2048
mm v[60:63],v[216:219],v[180:183],v[60:63]
st a[176:179],v255,s[48:51],s18 offen offset:3072 sc1
mm v[28:31],v[216:219],v[176:179],v[28:31]
wt vmcnt(16) lgkmcnt(5)
v_bfe_u32 v239, v255, 6, 2
mm v[28:31],v[220:223],v[160:163],v[28:31]
rt v[216:217],v243 offset:0
rt v[218:219],v243 offset:1024
mm v[0:3],v[192:195],v[128:131],v[0:3]
ar v224,a0
ar v225,a1
mm v[0:3],v[196:199],a[104:107],v[0:3]
ld v[160:163],v244,s[52:55],s91 offen offset:0 sc1
mm v[32:35],v[196:199],a[108:111],v[32:35]
ar v226,a2
ar v227,a3
mm v[32:35],v[192:195],v[140:143],v[32:35]
rt v[220:221],v243 offset:8448
rt v[222:223],v243 offset:9472
mm v[64:67],v[192:195],a[96:99],v[64:67]
ar v228,a4
ar v229,a5
mm v[64:67],v[196:199],a[112:115],v[64:67]
ld v[164:167],v245,s[52:55],s91 offen offset:0 sc1
mm v[96:99],v[196:199],a[116:119],v[96:99]
ar v230,a6
ar v231,a7
mm v[96:99],v[192:195],a[100:103],v[96:99]
rt v[196:197],v240 offset:128
rt v[198:199],v240 offset:1152
mm v[100:103],v[200:203],a[100:103],v[100:103]
s_add_u32 m0, s83, 52864
cv v248,v224,v225
mm v[100:103],v[204:207],a[116:119],v[100:103]
ld v234,s[44:47],s80 offen lds
mm v[68:71],v[204:207],a[112:115],v[68:71]
cv v249,v226,v227
cv v250,v228,v229
mm v[68:71],v[200:203],a[96:99],v[68:71]
rt v[192:193],v240 offset:8576
rt v[194:195],v240 offset:9600
mm v[36:39],v[200:203],v[140:143],v[36:39]
cv v251,v230,v231
aw a0,v248
mm v[36:39],v[204:207],a[108:111],v[36:39]
ld v[168:171],v246,s[52:55],s91 offen offset:0 sc1
mm v[4:7],v[204:207],a[104:107],v[4:7]
wt lgkmcnt(4)
aw a1,v249
mm v[4:7],v[200:203],v[128:131],v[4:7]
rt v[204:205],v241 offset:128
rt v[206:207],v241 offset:1152
)ASM" R"ASM(mm v[8:11],v[208:211],v[128:131],v[8:11]
s_add_u32 m0, s83, 53888
aw a2,v250
mm v[8:11],v[212:215],a[104:107],v[8:11]
ld v235,s[44:47],s80 offen lds
mm v[40:43],v[212:215],a[108:111],v[40:43]
s_add_u32 s80, s80, 524288
aw a3,v251
mm v[40:43],v[208:211],v[140:143],v[40:43]
rt v[200:201],v241 offset:8576
rt v[202:203],v241 offset:9600
mm v[72:75],v[208:211],a[96:99],v[72:75]
v_lshlrev_b32 v239, 6, v239
v_lshl_add_u32 v239, v236, 4, v239
mm v[72:75],v[212:215],a[112:115],v[72:75]
ld v[172:175],v247,s[52:55],s91 offen offset:0 sc1
mm v[104:107],v[212:215],a[116:119],v[104:107]
s_lshl_b32 s92, s78, 8
v_add_u32 v239, s92, v239
mm v[104:107],v[208:211],a[100:103],v[104:107]
rt v[212:213],v242 offset:128
rt v[214:215],v242 offset:1152
mm v[108:111],v[216:219],a[100:103],v[108:111]
v_mul_u32_u24 v239, 7168, v239
v_and_or_b32 v239, v255, 48, v239
mm v[108:111],v[220:223],a[116:119],v[108:111]
aw a229,v239
v_and_b32 v239, 0xf0, v255
.La5_LW1_12:
s_cbranch_execz .La5_LW1_R0
mm v[76:79],v[220:223],a[112:115],v[76:79]
s_mov_b32 s18, 16384
v_lshl_add_u32 v239, v236, 14, v239
mm v[76:79],v[216:219],a[96:99],v[76:79]
rt v[208:209],v242 offset:8576
rt v[210:211],v242 offset:9600
mm v[44:47],v[216:219],v[140:143],v[44:47]
ds_write_b128 v238, v[248:251] offset:3072
mm v[44:47],v[220:223],a[108:111],v[44:47]
st a[180:183],v255,s[48:51],s18 offen offset:0 sc1
mm v[12:15],v[220:223],a[104:107],v[12:15]
wt lgkmcnt(5)
s_lshl_b32 s92, s3, 9
mm v[12:15],v[216:219],v[128:131],v[12:15]
rt v[220:221],v243 offset:128
rt v[222:223],v243 offset:1152
mm v[16:19],v[196:199],v[128:131],v[16:19]
ar v224,a72
ar v225,a73
mm v[16:19],v[192:195],a[104:107],v[16:19]
ld v[176:179],v244,s[52:55],s91 offen offset:1024 sc1
mm v[48:51],v[192:195],a[108:111],v[48:51]
ar v226,a74
ar v227,a75
mm v[48:51],v[196:199],v[140:143],v[48:51]
rt v[216:217],v243 offset:8576
rt v[218:219],v243 offset:9600
mm v[80:83],v[196:199],a[96:99],v[80:83]
ar v228,a76
ar v229,a77
mm v[80:83],v[192:195],a[112:115],v[80:83]
ld v[180:183],v245,s[52:55],s91 offen offset:1024 sc1
mm v[112:115],v[192:195],a[116:119],v[112:115]
ar v230,a78
ar v231,a79
mm v[112:115],v[196:199],a[100:103],v[112:115]
cv v248,v224,v225
cv v249,v226,v227
mm v[116:119],v[204:207],a[100:103],v[116:119]
wt lgkmcnt(0)
s_barrier
mm v[116:119],v[200:203],a[116:119],v[116:119]
ld v[184:187],v246,s[52:55],s91 offen offset:1024 sc1
mm v[84:87],v[200:203],a[112:115],v[84:87]
rt v[192:193],v240 offset:16896
rt v[194:195],v240 offset:17920
mm v[84:87],v[204:207],a[96:99],v[84:87]
rt v[196:197],v240 offset:25344
rt v[198:199],v240 offset:26368
mm v[52:55],v[204:207],v[140:143],v[52:55]
s_add_u32 m0, s83, 0
cv v250,v228,v229
mm v[52:55],v[200:203],a[108:111],v[52:55]
ld v232,s[44:47],s80 offen lds
mm v[20:23],v[200:203],a[104:107],v[20:23]
cv v251,v230,v231
aw a4,v248
mm v[20:23],v[204:207],v[128:131],v[20:23]
rt v[200:201],v241 offset:16896
rt v[202:203],v241 offset:17920
mm v[24:27],v[212:215],v[128:131],v[24:27]
aw a5,v249
aw a6,v250
mm v[24:27],v[208:211],a[104:107],v[24:27]
ld v[188:191],v247,s[52:55],s91 offen offset:1024 sc1
mm v[56:59],v[208:211],a[108:111],v[56:59]
aw a7,v251
v_add_u32 v239, s92, v239
mm v[56:59],v[212:215],v[140:143],v[56:59]
rt v[204:205],v241 offset:25344
)ASM" R"ASM(rt v[206:207],v241 offset:26368
mm v[88:91],v[212:215],a[96:99],v[88:91]
s_add_u32 m0, s83, 1024
aw a230,v239
mm v[88:91],v[208:211],a[112:115],v[88:91]
ld v233,s[44:47],s80 offen lds
mm v[120:123],v[208:211],a[116:119],v[120:123]
v_add_u32 v239, 4294886912, v240
aw a224,v239
mm v[120:123],v[212:215],a[100:103],v[120:123]
rt v[208:209],v242 offset:16896
rt v[210:211],v242 offset:17920
mm v[124:127],v[220:223],a[100:103],v[124:127]
v_bfe_u32 v239, v255, 4, 4
v_lshlrev_b32 v236, 2, v236
mm v[124:127],v[216:219],a[116:119],v[124:127]
v_sub_u32 v239, v239, v236
v_subrev_u32 v236, 0, v239
mm v[92:95],v[216:219],a[112:115],v[92:95]
v_ashrrev_i32 v236, 31, v236
v_and_b32 v236, 0xff800000, v236
mm v[92:95],v[220:223],a[96:99],v[92:95]
rt v[212:213],v242 offset:25344
rt v[214:215],v242 offset:26368
mm v[60:63],v[220:223],v[140:143],v[60:63]
ds_write_b128 v238, v[248:251] offset:4096
mm v[60:63],v[216:219],a[108:111],v[60:63]
aw a236,v236
v_subrev_u32 v236, 1, v239
mm v[28:31],v[216:219],a[104:107],v[28:31]
wt vmcnt(15) lgkmcnt(5)
v_ashrrev_i32 v236, 31, v236
mm v[28:31],v[220:223],v[128:131],v[28:31]
rt v[216:217],v243 offset:16896
rt v[218:219],v243 offset:17920
mm v[0:3],v[192:195],v[132:135],v[0:3]
ar v224,a8
ar v225,a9
mm v[0:3],v[196:199],v[152:155],v[0:3]
ld v[128:131],v244,s[52:55],s91 offen offset:2048 sc1
mm v[32:35],v[196:199],v[156:159],v[32:35]
ar v226,a10
ar v227,a11
mm v[32:35],v[192:195],v[136:139],v[32:35]
rt v[220:221],v243 offset:25344
rt v[222:223],v243 offset:26368
mm v[64:67],v[192:195],v[144:147],v[64:67]
ar v228,a12
ar v229,a13
mm v[64:67],v[196:199],a[120:123],v[64:67]
ld v[140:143],v245,s[52:55],s91 offen offset:2048 sc1
mm v[96:99],v[196:199],a[124:127],v[96:99]
ar v230,a14
ar v231,a15
mm v[96:99],v[192:195],v[148:151],v[96:99]
rt v[196:197],v240 offset:17024
rt v[198:199],v240 offset:18048
mm v[100:103],v[200:203],v[148:151],v[100:103]
s_add_u32 m0, s83, 2176
cv v248,v224,v225
mm v[100:103],v[204:207],a[124:127],v[100:103]
ld v234,s[44:47],s80 offen lds
mm v[68:71],v[204:207],a[120:123],v[68:71]
cv v249,v226,v227
cv v250,v228,v229
mm v[68:71],v[200:203],v[144:147],v[68:71]
rt v[192:193],v240 offset:25472
rt v[194:195],v240 offset:26496
mm v[36:39],v[200:203],v[136:139],v[36:39]
cv v251,v230,v231
aw a8,v248
mm v[36:39],v[204:207],v[156:159],v[36:39]
ld a[96:99],v246,s[52:55],s91 offen offset:2048 sc1
mm v[4:7],v[204:207],v[152:155],v[4:7]
wt lgkmcnt(4)
aw a9,v249
mm v[4:7],v[200:203],v[132:135],v[4:7]
rt v[204:205],v241 offset:17024
rt v[206:207],v241 offset:18048
mm v[8:11],v[208:211],v[132:135],v[8:11]
s_add_u32 m0, s83, 3200
aw a10,v250
mm v[8:11],v[212:215],v[152:155],v[8:11]
ld v235,s[44:47],s80 offen lds
mm v[40:43],v[212:215],v[156:159],v[40:43]
s_add_u32 s80, s80, 524288
aw a11,v251
mm v[40:43],v[208:211],v[136:139],v[40:43]
rt v[200:201],v241 offset:25472
rt v[202:203],v241 offset:26496
mm v[72:75],v[208:211],v[144:147],v[72:75]
v_and_b32 v236, 0xff800000, v236
aw a237,v236
mm v[72:75],v[212:215],a[120:123],v[72:75]
ld a[100:103],v247,s[52:55],s91 offen offset:2048 sc1
mm v[104:107],v[212:215],a[124:127],v[104:107]
v_subrev_u32 v236, 2, v239
v_ashrrev_i32 v236, 31, v236
mm v[104:107],v[208:211],v[148:151],v[104:107]
rt v[212:213],v242 offset:17024
)ASM" R"ASM(rt v[214:215],v242 offset:18048
mm v[108:111],v[216:219],v[148:151],v[108:111]
v_and_b32 v236, 0xff800000, v236
aw a238,v236
mm v[108:111],v[220:223],a[124:127],v[108:111]
v_subrev_u32 v236, 3, v239
v_ashrrev_i32 v236, 31, v236
mm v[76:79],v[220:223],a[120:123],v[76:79]
v_and_b32 v236, 0xff800000, v236
aw a239,v236
mm v[76:79],v[216:219],v[144:147],v[76:79]
rt v[208:209],v242 offset:25472
rt v[210:211],v242 offset:26496
mm v[44:47],v[216:219],v[136:139],v[44:47]
ds_write_b128 v238, v[248:251] offset:5120
mm v[44:47],v[220:223],v[156:159],v[44:47]
st a[184:187],v255,s[48:51],s18 offen offset:1024 sc1
mm v[12:15],v[220:223],v[152:155],v[12:15]
wt lgkmcnt(5)
v_lshlrev_b32 v236, 4, v239
mm v[12:15],v[216:219],v[132:135],v[12:15]
rt v[220:221],v243 offset:17024
rt v[222:223],v243 offset:18048
mm v[16:19],v[196:199],v[132:135],v[16:19]
ar v224,a80
ar v225,a81
mm v[16:19],v[192:195],v[152:155],v[16:19]
ld a[104:107],v244,s[52:55],s91 offen offset:3072 sc1
mm v[48:51],v[192:195],v[156:159],v[48:51]
ar v226,a82
ar v227,a83
mm v[48:51],v[196:199],v[136:139],v[48:51]
rt v[216:217],v243 offset:25472
rt v[218:219],v243 offset:26496
mm v[80:83],v[196:199],v[144:147],v[80:83]
ar v228,a84
ar v229,a85
mm v[80:83],v[192:195],a[120:123],v[80:83]
ld a[108:111],v245,s[52:55],s91 offen offset:3072 sc1
mm v[112:115],v[192:195],a[124:127],v[112:115]
ar v230,a86
ar v231,a87
mm v[112:115],v[196:199],v[148:151],v[112:115]
cv v248,v224,v225
cv v249,v226,v227
mm v[116:119],v[204:207],v[148:151],v[116:119]
wt lgkmcnt(0)
s_barrier
mm v[116:119],v[200:203],a[124:127],v[116:119]
ld a[112:115],v246,s[52:55],s91 offen offset:3072 sc1
mm v[84:87],v[200:203],a[120:123],v[84:87]
rt v[192:193],v240 offset:33792
rt v[194:195],v240 offset:34816
mm v[84:87],v[204:207],v[144:147],v[84:87]
rt v[196:197],v240 offset:42240
rt v[198:199],v240 offset:43264
mm v[52:55],v[204:207],v[136:139],v[52:55]
s_add_u32 m0, s83, 16896
cv v250,v228,v229
mm v[52:55],v[200:203],v[156:159],v[52:55]
ld v232,s[44:47],s80 offen lds
mm v[20:23],v[200:203],v[152:155],v[20:23]
cv v251,v230,v231
aw a12,v248
mm v[20:23],v[204:207],v[132:135],v[20:23]
rt v[200:201],v241 offset:33792
rt v[202:203],v241 offset:34816
mm v[24:27],v[212:215],v[132:135],v[24:27]
aw a13,v249
aw a14,v250
mm v[24:27],v[208:211],v[152:155],v[24:27]
ld a[116:119],v247,s[52:55],s91 offen offset:3072 sc1
mm v[56:59],v[208:211],v[156:159],v[56:59]
s_add_u32 s91, s91, 4096
aw a15,v251
mm v[56:59],v[212:215],v[136:139],v[56:59]
rt v[204:205],v241 offset:42240
rt v[206:207],v241 offset:43264
mm v[88:91],v[212:215],v[144:147],v[88:91]
s_add_u32 m0, s83, 17920
s_mov_b32 s92, 0x3f80
mm v[88:91],v[208:211],a[120:123],v[88:91]
ld v233,s[44:47],s80 offen lds
mm v[120:123],v[208:211],a[124:127],v[120:123]
v_lshlrev_b32 v236, v236, s92
v_cmp_gt_u32 vcc, 2, v239
mm v[120:123],v[212:215],v[148:151],v[120:123]
rt v[208:209],v242 offset:33792
rt v[210:211],v242 offset:34816
mm v[124:127],v[220:223],v[148:151],v[124:127]
v_subrev_u32 v239, 2, v239
v_cmp_gt_u32 s[92:93], 2, v239
mm v[124:127],v[216:219],a[124:127],v[124:127]
v_cndmask_b32 v239, 0, v236, vcc
aw a212,0
mm v[92:95],v[216:219],a[120:123],v[92:95]
aw a214,v239
v_cndmask_b32 v239, 0, v236, s[92:93]
mm v[92:95],v[220:223],v[144:147],v[92:95]
rt v[212:213],v242 offset:42240
)ASM" R"ASM(rt v[214:215],v242 offset:43264
mm v[60:63],v[220:223],v[136:139],v[60:63]
v_mov_b32 v236, 0x3f803f80
aw a215,v239
mm v[60:63],v[216:219],v[156:159],v[60:63]
st a[188:191],v255,s[48:51],s18 offen offset:2048 sc1
mm v[28:31],v[216:219],v[152:155],v[28:31]
wt vmcnt(15) lgkmcnt(4)
aw a213,0
mm v[28:31],v[220:223],v[132:135],v[28:31]
rt v[216:217],v243 offset:33792
rt v[218:219],v243 offset:34816
mm v[0:3],v[192:195],v[160:163],v[0:3]
ar v224,a16
ar v225,a17
mm v[0:3],v[196:199],v[176:179],v[0:3]
ld v[132:135],v244,s[52:55],s91 offen offset:0 sc1
mm v[32:35],v[196:199],v[180:183],v[32:35]
ar v226,a18
ar v227,a19
mm v[32:35],v[192:195],v[164:167],v[32:35]
rt v[220:221],v243 offset:42240
rt v[222:223],v243 offset:43264
mm v[64:67],v[192:195],v[168:171],v[64:67]
ar v228,a20
ar v229,a21
mm v[64:67],v[196:199],v[184:187],v[64:67]
ld v[136:139],v245,s[52:55],s91 offen offset:0 sc1
mm v[96:99],v[196:199],v[188:191],v[96:99]
ar v230,a22
ar v231,a23
mm v[96:99],v[192:195],v[172:175],v[96:99]
rt v[196:197],v240 offset:33920
rt v[198:199],v240 offset:34944
mm v[100:103],v[200:203],v[172:175],v[100:103]
s_add_u32 m0, s83, 19072
cv v248,v224,v225
mm v[100:103],v[204:207],v[188:191],v[100:103]
ld v234,s[44:47],s80 offen lds
mm v[68:71],v[204:207],v[184:187],v[68:71]
cv v249,v226,v227
cv v250,v228,v229
mm v[68:71],v[200:203],v[168:171],v[68:71]
rt v[192:193],v240 offset:42368
rt v[194:195],v240 offset:43392
mm v[36:39],v[200:203],v[164:167],v[36:39]
cv v251,v230,v231
aw a16,v248
mm v[36:39],v[204:207],v[180:183],v[36:39]
ld v[144:147],v246,s[52:55],s91 offen offset:0 sc1
mm v[4:7],v[204:207],v[176:179],v[4:7]
wt lgkmcnt(4)
aw a17,v249
mm v[4:7],v[200:203],v[160:163],v[4:7]
rt v[204:205],v241 offset:33920
rt v[206:207],v241 offset:34944
mm v[8:11],v[208:211],v[160:163],v[8:11]
s_add_u32 m0, s83, 20096
aw a18,v250
mm v[8:11],v[212:215],v[176:179],v[8:11]
ld v235,s[44:47],s80 offen lds
mm v[40:43],v[212:215],v[180:183],v[40:43]
s_add_u32 s80, s80, 524288
aw a19,v251
mm v[40:43],v[208:211],v[164:167],v[40:43]
rt v[200:201],v241 offset:42368
rt v[202:203],v241 offset:43392
mm v[72:75],v[208:211],v[168:171],v[72:75]
aw a216,0
aw a217,0
mm v[72:75],v[212:215],v[184:187],v[72:75]
ld v[148:151],v247,s[52:55],s91 offen offset:0 sc1
mm v[104:107],v[212:215],v[188:191],v[104:107]
v_lshrrev_b32 v239, 4, v255
v_xor_b32 v239, v239, v255
mm v[104:107],v[208:211],v[172:175],v[104:107]
rt v[212:213],v242 offset:33920
rt v[214:215],v242 offset:34944
mm v[108:111],v[216:219],v[172:175],v[108:111]
v_and_b32 v239, 32, v239
v_cmp_eq_u32 vcc, 0, v239
mm v[108:111],v[220:223],v[188:191],v[108:111]
s_mov_b32 s78, 4294950911
s_mul_i32 s70, s70, s22
mm v[76:79],v[220:223],v[184:187],v[76:79]
v_cndmask_b32 v236, 0, v236, vcc
s_sub_u32 s92, 4294967294, s3
mm v[76:79],v[216:219],v[168:171],v[76:79]
rt v[208:209],v242 offset:42368
rt v[210:211],v242 offset:43392
mm v[44:47],v[216:219],v[164:167],v[44:47]
s_add_u32 s92, s92, s7
s_mul_i32 s92, s92, s21
mm v[44:47],v[220:223],v[180:183],v[44:47]
st a[0:3],v255,s[48:51],s18 offen offset:3072 sc1
mm v[12:15],v[220:223],v[176:179],v[12:15]
wt lgkmcnt(4)
s_add_u32 s90, s90, s92
mm v[12:15],v[216:219],v[160:163],v[12:15]
rt v[220:221],v243 offset:33920
)ASM" R"ASM(rt v[222:223],v243 offset:34944
mm v[16:19],v[196:199],v[160:163],v[16:19]
ar v224,a88
ar v225,a89
mm v[16:19],v[192:195],v[176:179],v[16:19]
ld v[152:155],v244,s[52:55],s91 offen offset:1024 sc1
mm v[48:51],v[192:195],v[180:183],v[48:51]
ar v226,a90
ar v227,a91
mm v[48:51],v[196:199],v[164:167],v[48:51]
rt v[216:217],v243 offset:42368
rt v[218:219],v243 offset:43392
mm v[80:83],v[196:199],v[168:171],v[80:83]
ar v228,a92
ar v229,a93
mm v[80:83],v[192:195],v[184:187],v[80:83]
ld v[156:159],v245,s[52:55],s91 offen offset:1024 sc1
mm v[112:115],v[192:195],v[188:191],v[112:115]
ar v230,a94
ar v231,a95
mm v[112:115],v[196:199],v[172:175],v[112:115]
cv v248,v224,v225
cv v249,v226,v227
mm v[116:119],v[204:207],v[172:175],v[116:119]
wt lgkmcnt(0)
s_barrier
mm v[116:119],v[200:203],v[188:191],v[116:119]
ld a[120:123],v246,s[52:55],s91 offen offset:1024 sc1
mm v[84:87],v[200:203],v[184:187],v[84:87]
rt v[192:193],v240 offset:50688
rt v[194:195],v240 offset:51712
mm v[84:87],v[204:207],v[168:171],v[84:87]
rt v[196:197],v240 offset:59136
rt v[198:199],v240 offset:60160
mm v[52:55],v[204:207],v[164:167],v[52:55]
s_add_u32 m0, s83, 33792
cv v250,v228,v229
mm v[52:55],v[200:203],v[180:183],v[52:55]
ld v232,s[44:47],s80 offen lds
mm v[20:23],v[200:203],v[176:179],v[20:23]
cv v251,v230,v231
aw a20,v248
mm v[20:23],v[204:207],v[160:163],v[20:23]
rt v[200:201],v241 offset:50688
rt v[202:203],v241 offset:51712
mm v[24:27],v[212:215],v[160:163],v[24:27]
aw a21,v249
aw a22,v250
mm v[24:27],v[208:211],v[176:179],v[24:27]
ld a[124:127],v247,s[52:55],s91 offen offset:1024 sc1
mm v[56:59],v[208:211],v[180:183],v[56:59]
aw a23,v251
s_mov_b32 s17, 1073741824
mm v[56:59],v[212:215],v[164:167],v[56:59]
rt v[204:205],v241 offset:59136
rt v[206:207],v241 offset:60160
mm v[88:91],v[212:215],v[168:171],v[88:91]
s_add_u32 m0, s83, 34816
s_lshl_b32 s77, s76, 16
mm v[88:91],v[208:211],v[184:187],v[88:91]
ld v233,s[44:47],s80 offen lds
mm v[120:123],v[208:211],v[188:191],v[120:123]
s_sub_u32 s77, s77, 8192
s_lshl4_add_u32 s81, s75, s74
mm v[120:123],v[212:215],v[172:175],v[120:123]
rt v[208:209],v242 offset:50688
rt v[210:211],v242 offset:51712
mm v[124:127],v[220:223],v[172:175],v[124:127]
s_lshl3_add_u32 s81, s81, 1
mm v[124:127],v[216:219],v[188:191],v[124:127]
s_cmp_eq_u32 s73, 0
s_cselect_b32 s16, s77, 0
mm v[92:95],v[216:219],v[184:187],v[92:95]
s_mov_b32 s18, 20480
s_mul_i32 s92, s75, 4294836224
mm v[92:95],v[220:223],v[168:171],v[92:95]
rt v[212:213],v242 offset:59136
rt v[214:215],v242 offset:60160
mm v[60:63],v[220:223],v[164:167],v[60:63]
s_add_u32 s92, s92, s88
s_add_u32 s19, s92, 393216
mm v[60:63],v[216:219],v[180:183],v[60:63]
st a[4:7],v255,s[48:51],s18 offen offset:0 sc1
mm v[28:31],v[216:219],v[176:179],v[28:31]
wt vmcnt(16) lgkmcnt(4)
aw a208,v236
mm v[28:31],v[220:223],v[160:163],v[28:31]
rt v[216:217],v243 offset:50688
rt v[218:219],v243 offset:51712
mm v[0:3],v[192:195],v[128:131],v[0:3]
ar v224,a24
ar v225,a25
mm v[0:3],v[196:199],a[104:107],v[0:3]
ld v[160:163],v244,s[52:55],s91 offen offset:2048 sc1
mm v[32:35],v[196:199],a[108:111],v[32:35]
ar v226,a26
ar v227,a27
mm v[32:35],v[192:195],v[140:143],v[32:35]
rt v[220:221],v243 offset:59136
rt v[222:223],v243 offset:60160
)ASM" R"ASM(mm v[64:67],v[192:195],a[96:99],v[64:67]
ar v228,a28
ar v229,a29
mm v[64:67],v[196:199],a[112:115],v[64:67]
ld v[164:167],v245,s[52:55],s91 offen offset:2048 sc1
mm v[96:99],v[196:199],a[116:119],v[96:99]
ar v230,a30
ar v231,a31
mm v[96:99],v[192:195],a[100:103],v[96:99]
rt v[196:197],v240 offset:50816
rt v[198:199],v240 offset:51840
mm v[100:103],v[200:203],a[100:103],v[100:103]
s_add_u32 m0, s83, 35968
cv v248,v224,v225
mm v[100:103],v[204:207],a[116:119],v[100:103]
ld v234,s[44:47],s80 offen lds
mm v[68:71],v[204:207],a[112:115],v[68:71]
cv v249,v226,v227
cv v250,v228,v229
mm v[68:71],v[200:203],a[96:99],v[68:71]
rt v[192:193],v240 offset:59264
rt v[194:195],v240 offset:60288
mm v[36:39],v[200:203],v[140:143],v[36:39]
cv v251,v230,v231
aw a24,v248
mm v[36:39],v[204:207],a[108:111],v[36:39]
ld v[168:171],v246,s[52:55],s91 offen offset:2048 sc1
mm v[4:7],v[204:207],a[104:107],v[4:7]
wt lgkmcnt(4)
aw a25,v249
mm v[4:7],v[200:203],v[128:131],v[4:7]
rt v[204:205],v241 offset:50816
rt v[206:207],v241 offset:51840
mm v[8:11],v[208:211],v[128:131],v[8:11]
s_add_u32 m0, s83, 36992
aw a26,v250
mm v[8:11],v[212:215],a[104:107],v[8:11]
ld v235,s[44:47],s80 offen lds
mm v[40:43],v[212:215],a[108:111],v[40:43]
s_add_u32 s80, s80, 524288
aw a27,v251
mm v[40:43],v[208:211],v[140:143],v[40:43]
rt v[200:201],v241 offset:59264
rt v[202:203],v241 offset:60288
mm v[72:75],v[208:211],a[96:99],v[72:75]
aw a209,v236
aw a210,v236
mm v[72:75],v[212:215],a[112:115],v[72:75]
ld v[172:175],v247,s[52:55],s91 offen offset:2048 sc1
mm v[104:107],v[212:215],a[116:119],v[104:107]
aw a211,v236
aw a232,0
mm v[104:107],v[208:211],a[100:103],v[104:107]
rt v[212:213],v242 offset:50816
rt v[214:215],v242 offset:51840
mm v[108:111],v[216:219],a[100:103],v[108:111]
aw a233,0
aw a234,0
mm v[108:111],v[220:223],a[116:119],v[108:111]
aw a235,0
s_lshl_b32 s20, s72, 10
mm v[76:79],v[220:223],a[112:115],v[76:79]
s_add_u32 s20, s20, 16384
s_mov_b32 s4, 1466176288
mm v[76:79],v[216:219],a[96:99],v[76:79]
rt v[208:209],v242 offset:59264
rt v[210:211],v242 offset:60288
.La5_LW1_13:
s_cbranch_execz .La5_LW1_R0
mm v[44:47],v[216:219],v[140:143],v[44:47]
s_cmp_ge_u32 s2, 64
s_cselect_b32 s4, 1182081808, s4
mm v[44:47],v[220:223],a[108:111],v[44:47]
st a[8:11],v255,s[48:51],s18 offen offset:1024 sc1
mm v[12:15],v[220:223],a[104:107],v[12:15]
wt lgkmcnt(4)
mm v[12:15],v[216:219],v[128:131],v[12:15]
rt v[220:221],v243 offset:50816
rt v[222:223],v243 offset:51840
mm v[16:19],v[196:199],v[128:131],v[16:19]
ar v224,a240
ar v225,a241
mm v[16:19],v[192:195],a[104:107],v[16:19]
ld v[176:179],v244,s[52:55],s91 offen offset:3072 sc1
mm v[48:51],v[192:195],a[108:111],v[48:51]
ar v226,a242
ar v227,a243
mm v[48:51],v[196:199],v[140:143],v[48:51]
rt v[216:217],v243 offset:59264
rt v[218:219],v243 offset:60288
mm v[80:83],v[196:199],a[96:99],v[80:83]
ar v228,a244
ar v229,a245
mm v[80:83],v[192:195],a[112:115],v[80:83]
ld v[180:183],v245,s[52:55],s91 offen offset:3072 sc1
mm v[112:115],v[192:195],a[116:119],v[112:115]
ar v230,a246
ar v231,a247
mm v[112:115],v[196:199],a[100:103],v[112:115]
cv v248,v224,v225
)ASM" R"ASM(cv v249,v226,v227
mm v[116:119],v[204:207],a[100:103],v[116:119]
wt lgkmcnt(0)
s_barrier
mm v[116:119],v[200:203],a[116:119],v[116:119]
ld v[184:187],v246,s[52:55],s91 offen offset:3072 sc1
mm v[84:87],v[200:203],a[112:115],v[84:87]
rt v[192:193],v240 offset:0
rt v[194:195],v240 offset:1024
mm v[84:87],v[204:207],a[96:99],v[84:87]
rt v[196:197],v240 offset:8448
rt v[198:199],v240 offset:9472
mm v[52:55],v[204:207],v[140:143],v[52:55]
s_add_u32 m0, s83, 50688
cv v250,v228,v229
mm v[52:55],v[200:203],a[108:111],v[52:55]
ld v232,s[44:47],s80 offen lds
mm v[20:23],v[200:203],a[104:107],v[20:23]
cv v251,v230,v231
aw a28,v248
mm v[20:23],v[204:207],v[128:131],v[20:23]
rt v[200:201],v241 offset:0
rt v[202:203],v241 offset:1024
mm v[24:27],v[212:215],v[128:131],v[24:27]
aw a29,v249
aw a30,v250
mm v[24:27],v[208:211],a[104:107],v[24:27]
ld v[188:191],v247,s[52:55],s91 offen offset:3072 sc1
mm v[56:59],v[208:211],a[108:111],v[56:59]
s_add_u32 s91, s91, 4096
aw a31,v251
mm v[56:59],v[212:215],v[140:143],v[56:59]
rt v[204:205],v241 offset:8448
rt v[206:207],v241 offset:9472
mm v[88:91],v[212:215],a[96:99],v[88:91]
s_add_u32 m0, s83, 51712
mm v[88:91],v[208:211],a[112:115],v[88:91]
ld v233,s[44:47],s80 offen lds
mm v[120:123],v[208:211],a[116:119],v[120:123]
s_cmp_ge_u32 s2, 128
s_cselect_b32 s4, 1967534368, s4
mm v[120:123],v[212:215],a[100:103],v[120:123]
rt v[208:209],v242 offset:0
rt v[210:211],v242 offset:1024
mm v[124:127],v[220:223],a[100:103],v[124:127]
s_cmp_ge_u32 s2, 192
s_cselect_b32 s4, 1683431728, s4
mm v[124:127],v[216:219],a[116:119],v[124:127]
v_bfe_u32 v236, v255, 4, 3
v_lshlrev_b32 v236, 2, v236
mm v[92:95],v[216:219],a[112:115],v[92:95]
v_lshrrev_b32 v236, v236, s4
v_and_b32 v236, 15, v236
mm v[92:95],v[220:223],a[96:99],v[92:95]
rt v[212:213],v242 offset:8448
rt v[214:215],v242 offset:9472
mm v[60:63],v[220:223],v[140:143],v[60:63]
v_bfe_u32 v239, v255, 7, 1
v_mul_u32_u24 v239, 28, v239
mm v[60:63],v[216:219],a[108:111],v[60:63]
v_xor_b32 v236, v236, v239
v_xor_b32 v236, s72, v236
mm v[28:31],v[216:219],a[104:107],v[28:31]
wt vmcnt(15) lgkmcnt(4)
v_lshlrev_b32 v236, 3, v236
mm v[28:31],v[220:223],v[128:131],v[28:31]
rt v[216:217],v243 offset:0
rt v[218:219],v243 offset:1024
mm v[0:3],v[192:195],v[132:135],v[0:3]
ar v224,a32
ar v225,a33
aw a225,v236
mm v[0:3],v[196:199],v[152:155],v[0:3]
ld v[128:131],v244,s[52:55],s91 offen offset:0 sc1
s_lshl3_add_u32 s5, s76, s101
mm v[32:35],v[196:199],v[156:159],v[32:35]
ar v226,a34
ar v227,a35
s_lshl2_add_u32 s5, s5, s3
mm v[32:35],v[192:195],v[136:139],v[32:35]
rt v[220:221],v243 offset:8448
rt v[222:223],v243 offset:9472
s_lshl_b32 s5, s5, 8
mm v[64:67],v[192:195],v[144:147],v[64:67]
ar v228,a36
ar v229,a37
s_add_u32 s5, s5, 1048576
mm v[64:67],v[196:199],a[120:123],v[64:67]
ld v[140:143],v245,s[52:55],s91 offen offset:0 sc1
mm v[96:99],v[196:199],a[124:127],v[96:99]
ar v230,a38
ar v231,a39
mm v[96:99],v[192:195],v[148:151],v[96:99]
rt v[196:197],v240 offset:128
rt v[198:199],v240 offset:1152
mm v[100:103],v[200:203],v[148:151],v[100:103]
s_add_u32 m0, s83, 52864
cv v248,v224,v225
mm v[100:103],v[204:207],a[124:127],v[100:103]
ld v234,s[44:47],s80 offen lds
)ASM" R"ASM(mm v[68:71],v[204:207],a[120:123],v[68:71]
cv v249,v226,v227
cv v250,v228,v229
mm v[68:71],v[200:203],v[144:147],v[68:71]
rt v[192:193],v240 offset:8576
rt v[194:195],v240 offset:9600
mm v[36:39],v[200:203],v[136:139],v[36:39]
cv v251,v230,v231
aw a32,v248
mm v[36:39],v[204:207],v[156:159],v[36:39]
ld a[192:195],v246,s[52:55],s91 offen offset:0 sc1
mm v[4:7],v[204:207],v[152:155],v[4:7]
wt lgkmcnt(4)
aw a33,v249
mm v[4:7],v[200:203],v[132:135],v[4:7]
rt v[204:205],v241 offset:128
rt v[206:207],v241 offset:1152
mm v[8:11],v[208:211],v[132:135],v[8:11]
s_add_u32 m0, s83, 53888
aw a34,v250
mm v[8:11],v[212:215],v[152:155],v[8:11]
ld v235,s[44:47],s80 offen lds
mm v[40:43],v[212:215],v[156:159],v[40:43]
aw a35,v251
mm v[40:43],v[208:211],v[136:139],v[40:43]
rt v[200:201],v241 offset:8576
rt v[202:203],v241 offset:9600
mm v[72:75],v[208:211],v[144:147],v[72:75]
mm v[72:75],v[212:215],a[120:123],v[72:75]
ld a[196:199],v247,s[52:55],s91 offen offset:0 sc1
mm v[104:107],v[212:215],a[124:127],v[104:107]
mm v[104:107],v[208:211],v[148:151],v[104:107]
rt v[212:213],v242 offset:128
rt v[214:215],v242 offset:1152
mm v[108:111],v[216:219],v[148:151],v[108:111]
mm v[108:111],v[220:223],a[124:127],v[108:111]
mm v[76:79],v[220:223],a[120:123],v[76:79]
mm v[76:79],v[216:219],v[144:147],v[76:79]
rt v[208:209],v242 offset:8576
rt v[210:211],v242 offset:9600
mm v[44:47],v[216:219],v[136:139],v[44:47]
mm v[44:47],v[220:223],v[156:159],v[44:47]
st a[12:15],v255,s[48:51],s18 offen offset:2048 sc1
mm v[12:15],v[220:223],v[152:155],v[12:15]
wt lgkmcnt(4)
mm v[12:15],v[216:219],v[132:135],v[12:15]
rt v[220:221],v243 offset:128
rt v[222:223],v243 offset:1152
mm v[16:19],v[196:199],v[132:135],v[16:19]
ar v224,a248
ar v225,a249
mm v[16:19],v[192:195],v[152:155],v[16:19]
ld a[200:203],v244,s[52:55],s91 offen offset:1024 sc1
mm v[48:51],v[192:195],v[156:159],v[48:51]
ar v226,a250
ar v227,a251
mm v[48:51],v[196:199],v[136:139],v[48:51]
rt v[216:217],v243 offset:8576
rt v[218:219],v243 offset:9600
mm v[80:83],v[196:199],v[144:147],v[80:83]
ar v228,a252
ar v229,a253
mm v[80:83],v[192:195],a[120:123],v[80:83]
ld a[204:207],v245,s[52:55],s91 offen offset:1024 sc1
mm v[112:115],v[192:195],a[124:127],v[112:115]
ar v230,a254
ar v231,a255
mm v[112:115],v[196:199],v[148:151],v[112:115]
cv v248,v224,v225
cv v249,v226,v227
mm v[116:119],v[204:207],v[148:151],v[116:119]
wt lgkmcnt(0)
s_barrier
mm v[116:119],v[200:203],a[124:127],v[116:119]
cv v250,v228,v229
cv v251,v230,v231
mm v[84:87],v[200:203],a[120:123],v[84:87]
rt v[192:193],v240 offset:16896
rt v[194:195],v240 offset:17920
mm v[84:87],v[204:207],v[144:147],v[84:87]
rt v[196:197],v240 offset:25344
rt v[198:199],v240 offset:26368
mm v[52:55],v[204:207],v[136:139],v[52:55]
aw a36,v248
aw a37,v249
mm v[52:55],v[200:203],v[156:159],v[52:55]
aw a38,v250
aw a39,v251
mm v[20:23],v[200:203],v[152:155],v[20:23]
s_getpc_b64 s[92:93]
.La5_LCT2:
s_and_b32 s93, s93, 0xffff
mm v[20:23],v[204:207],v[132:135],v[20:23]
rt v[200:201],v241 offset:16896
rt v[202:203],v241 offset:17920
mm v[24:27],v[212:215],v[132:135],v[24:27]
s_lshr_b32 s94, s2, 3
s_lshl2_add_u32 s94, s94, s3
mm v[24:27],v[208:211],v[152:155],v[24:27]
s_lshl_b32 s94, s94, 10
)ASM" R"ASM(v_lshrrev_b32 v233, 7, v255
mm v[56:59],v[208:211],v[156:159],v[56:59]
v_lshlrev_b32 v232, 3, v255
v_lshl_add_u32 v232, v233, 28, v232
mm v[56:59],v[212:215],v[136:139],v[56:59]
rt v[204:205],v241 offset:25344
rt v[206:207],v241 offset:26368
mm v[88:91],v[212:215],v[144:147],v[88:91]
v_add_u32 v232, s94, v232
s_mov_b32 s94, .La5_LCODEEND-.La5_LCT2
mm v[88:91],v[208:211],a[120:123],v[88:91]
s_mov_b32 s95, 0x20000
buffer_load_dword a253, v232, s[92:95], 0 offen
mm v[120:123],v[208:211],a[124:127],v[120:123]
s_mov_b32 s80, 8192
mm v[120:123],v[212:215],v[148:151],v[120:123]
rt v[208:209],v242 offset:16896
rt v[210:211],v242 offset:17920
mm v[124:127],v[220:223],v[148:151],v[124:127]
s_cmp_eq_u32 s73, 0
s_cselect_b32 s80, -8192, s80
mm v[124:127],v[216:219],a[124:127],v[124:127]
ld v[148:151],v246,s[52:55],s91 offen offset:1024 sc1
mm v[92:95],v[216:219],a[120:123],v[92:95]
s_add_u32 s83, s83, 4294886912
s_lshl_b32 s92, s76, 21
mm v[92:95],v[220:223],v[144:147],v[92:95]
rt v[212:213],v242 offset:25344
rt v[214:215],v242 offset:26368
mm v[60:63],v[220:223],v[136:139],v[60:63]
ds_write_b128 v238, a[12:15] offset:6144
mm v[60:63],v[216:219],v[156:159],v[60:63]
st a[16:19],v255,s[48:51],s18 offen offset:3072 sc1
mm v[28:31],v[216:219],v[152:155],v[28:31]
wt vmcnt(13) lgkmcnt(5)
s_add_u32 s92, s92, 54525952
mm v[28:31],v[220:223],v[132:135],v[28:31]
rt v[216:217],v243 offset:16896
rt v[218:219],v243 offset:17920
mm v[0:3],v[192:195],v[160:163],v[0:3]
ar v224,a40
ar v225,a41
mm v[0:3],v[196:199],v[176:179],v[0:3]
ld v[132:135],v247,s[52:55],s91 offen offset:1024 sc1
mm v[32:35],v[196:199],v[180:183],v[32:35]
ar v226,a42
ar v227,a43
mm v[32:35],v[192:195],v[164:167],v[32:35]
rt v[220:221],v243 offset:25344
rt v[222:223],v243 offset:26368
mm v[64:67],v[192:195],v[168:171],v[64:67]
ar v228,a44
ar v229,a45
mm v[64:67],v[196:199],v[184:187],v[64:67]
ld a[96:99],v244,s[52:55],s91 offen offset:2048 sc1
mm v[96:99],v[196:199],v[188:191],v[96:99]
ar v230,a46
ar v231,a47
mm v[96:99],v[192:195],v[172:175],v[96:99]
rt v[196:197],v240 offset:17024
rt v[198:199],v240 offset:18048
mm v[100:103],v[200:203],v[172:175],v[100:103]
cv v248,v224,v225
cv v249,v226,v227
mm v[100:103],v[204:207],v[188:191],v[100:103]
ld a[100:103],v245,s[52:55],s91 offen offset:2048 sc1
mm v[68:71],v[204:207],v[184:187],v[68:71]
cv v250,v228,v229
cv v251,v230,v231
mm v[68:71],v[200:203],v[168:171],v[68:71]
rt v[192:193],v240 offset:25472
rt v[194:195],v240 offset:26496
mm v[36:39],v[200:203],v[164:167],v[36:39]
aw a40,v248
aw a41,v249
mm v[36:39],v[204:207],v[180:183],v[36:39]
ld a[104:107],v246,s[52:55],s91 offen offset:2048 sc1
mm v[4:7],v[204:207],v[176:179],v[4:7]
wt lgkmcnt(4)
aw a42,v250
mm v[4:7],v[200:203],v[160:163],v[4:7]
rt v[204:205],v241 offset:17024
rt v[206:207],v241 offset:18048
mm v[8:11],v[208:211],v[160:163],v[8:11]
aw a43,v251
mm v[8:11],v[212:215],v[176:179],v[8:11]
ld a[108:111],v247,s[52:55],s91 offen offset:2048 sc1
mm v[40:43],v[212:215],v[180:183],v[40:43]
s_add_u32 s44, s26, s92
s_addc_u32 s45, s27, 0
mm v[40:43],v[208:211],v[164:167],v[40:43]
rt v[200:201],v241 offset:25472
rt v[202:203],v241 offset:26496
mm v[72:75],v[208:211],v[168:171],v[72:75]
s_and_b32 s45, s45, 0xffff
s_mov_b32 s46, 2097536
mm v[72:75],v[212:215],v[184:187],v[72:75]
s_mov_b32 s95, 4096
s_mov_b32 s96, 8192
mm v[104:107],v[212:215],v[188:191],v[104:107]
)ASM" R"ASM(s_mov_b32 s18, 0
s_mov_b32 s97, 12288
mm v[104:107],v[208:211],v[172:175],v[104:107]
rt v[212:213],v242 offset:17024
rt v[214:215],v242 offset:18048
mm v[108:111],v[216:219],v[172:175],v[108:111]
s_mul_i32 s92, s3, 196608
s_sub_u32 s93, 1376256, s92
mm v[108:111],v[220:223],v[188:191],v[108:111]
st a[20:23],v255,s[48:51],s18 offen offset:0 sc1
mm v[76:79],v[220:223],v[184:187],v[76:79]
mm v[76:79],v[216:219],v[168:171],v[76:79]
rt v[208:209],v242 offset:25472
rt v[210:211],v242 offset:26496
mm v[44:47],v[216:219],v[164:167],v[44:47]
ds_write_b128 v238, a[16:19] offset:7168
mm v[44:47],v[220:223],v[180:183],v[44:47]
st a[24:27],v255,s[48:51],s18 offen offset:1024 sc1
mm v[12:15],v[220:223],v[176:179],v[12:15]
wt lgkmcnt(5)
mm v[12:15],v[216:219],v[160:163],v[12:15]
rt v[220:221],v243 offset:17024
rt v[222:223],v243 offset:18048
mm v[16:19],v[196:199],v[160:163],v[16:19]
ar v224,a48
ar v225,a49
mm v[16:19],v[192:195],v[176:179],v[16:19]
ld a[112:115],v244,s[52:55],s91 offen offset:3072 sc1
mm v[48:51],v[192:195],v[180:183],v[48:51]
ar v226,a50
ar v227,a51
mm v[48:51],v[196:199],v[164:167],v[48:51]
rt v[216:217],v243 offset:25472
rt v[218:219],v243 offset:26496
mm v[80:83],v[196:199],v[168:171],v[80:83]
ar v228,a52
ar v229,a53
mm v[80:83],v[192:195],v[184:187],v[80:83]
ld a[116:119],v245,s[52:55],s91 offen offset:3072 sc1
mm v[112:115],v[192:195],v[188:191],v[112:115]
ar v230,a54
ar v231,a55
mm v[112:115],v[196:199],v[172:175],v[112:115]
cv v248,v224,v225
cv v249,v226,v227
mm v[116:119],v[204:207],v[172:175],v[116:119]
wt lgkmcnt(0)
s_barrier
mm v[116:119],v[200:203],v[188:191],v[116:119]
ld a[120:123],v246,s[52:55],s91 offen offset:3072 sc1
mm v[84:87],v[200:203],v[184:187],v[84:87]
rt v[192:193],v240 offset:33792
rt v[194:195],v240 offset:34816
mm v[84:87],v[204:207],v[168:171],v[84:87]
rt v[196:197],v240 offset:42240
rt v[198:199],v240 offset:43264
mm v[52:55],v[204:207],v[164:167],v[52:55]
ds_write_b128 v238, a[20:23] offset:32768
mm v[52:55],v[200:203],v[180:183],v[52:55]
ld a[124:127],v247,s[52:55],s91 offen offset:3072 sc1
mm v[20:23],v[200:203],v[176:179],v[20:23]
cv v250,v228,v229
cv v251,v230,v231
mm v[20:23],v[204:207],v[160:163],v[20:23]
rt v[200:201],v241 offset:33792
rt v[202:203],v241 offset:34816
mm v[24:27],v[212:215],v[160:163],v[24:27]
aw a44,v248
aw a45,v249
mm v[24:27],v[208:211],v[176:179],v[24:27]
aw a46,v250
aw a47,v251
mm v[56:59],v[208:211],v[180:183],v[56:59]
mm v[56:59],v[212:215],v[164:167],v[56:59]
rt v[204:205],v241 offset:42240
rt v[206:207],v241 offset:43264
mm v[88:91],v[212:215],v[168:171],v[88:91]
mm v[88:91],v[208:211],v[184:187],v[88:91]
mm v[120:123],v[208:211],v[188:191],v[120:123]
mm v[120:123],v[212:215],v[172:175],v[120:123]
rt v[208:209],v242 offset:33792
rt v[210:211],v242 offset:34816
mm v[124:127],v[220:223],v[172:175],v[124:127]
mm v[124:127],v[216:219],v[188:191],v[124:127]
st a[28:31],v255,s[48:51],s18 offen offset:2048 sc1
mm v[92:95],v[216:219],v[184:187],v[92:95]
mm v[92:95],v[220:223],v[168:171],v[92:95]
rt v[212:213],v242 offset:42240
rt v[214:215],v242 offset:43264
mm v[60:63],v[220:223],v[164:167],v[60:63]
ds_write_b128 v238, a[24:27] offset:33792
mm v[60:63],v[216:219],v[180:183],v[60:63]
st a[32:35],v255,s[48:51],s18 offen offset:3072 sc1
mm v[28:31],v[216:219],v[176:179],v[28:31]
wt vmcnt(12) lgkmcnt(5)
mm v[28:31],v[220:223],v[160:163],v[28:31]
rt v[216:217],v243 offset:33792
rt v[218:219],v243 offset:34816
mm v[0:3],v[192:195],v[128:131],v[0:3]
ar v224,a56
)ASM" R"ASM(ar v225,a57
mm v[0:3],v[196:199],a[200:203],v[0:3]
ar v226,a58
ar v227,a59
mm v[32:35],v[196:199],a[204:207],v[32:35]
ar v228,a60
ar v229,a61
mm v[32:35],v[192:195],v[140:143],v[32:35]
rt v[220:221],v243 offset:42240
rt v[222:223],v243 offset:43264
mm v[64:67],v[192:195],a[192:195],v[64:67]
ar v230,a62
ar v231,a63
mm v[64:67],v[196:199],v[148:151],v[64:67]
cv v248,v224,v225
cv v249,v226,v227
mm v[96:99],v[196:199],v[132:135],v[96:99]
cv v250,v228,v229
cv v251,v230,v231
mm v[96:99],v[192:195],a[196:199],v[96:99]
rt v[196:197],v240 offset:33920
rt v[198:199],v240 offset:34944
mm v[100:103],v[200:203],a[196:199],v[100:103]
aw a48,v248
aw a49,v249
mm v[100:103],v[204:207],v[132:135],v[100:103]
aw a50,v250
aw a51,v251
mm v[68:71],v[204:207],v[148:151],v[68:71]
v_add_u32 v244, s92, v244
v_add_u32 v246, s93, v246
mm v[68:71],v[200:203],a[192:195],v[68:71]
rt v[192:193],v240 offset:42368
rt v[194:195],v240 offset:43392
mm v[36:39],v[200:203],v[140:143],v[36:39]
ds_write_b128 v238, a[28:31] offset:34816
mm v[36:39],v[204:207],a[204:207],v[36:39]
v_add_u32 v245, 0x1000, v244
v_add_u32 v247, 0x1000, v246
mm v[4:7],v[204:207],a[200:203],v[4:7]
wt lgkmcnt(5)
s_lshl_b32 s91, s72, 13
mm v[4:7],v[200:203],v[128:131],v[4:7]
rt v[204:205],v241 offset:33920
rt v[206:207],v241 offset:34944
mm v[8:11],v[208:211],v[128:131],v[8:11]
s_lshl4_add_u32 s92, s76, s3
s_lshl3_add_u32 s92, s92, s20
mm v[8:11],v[212:215],a[200:203],v[8:11]
s_bitset1_b32 s20, 30
mm v[40:43],v[212:215],a[204:207],v[40:43]
mm v[40:43],v[208:211],v[140:143],v[40:43]
rt v[200:201],v241 offset:42368
rt v[202:203],v241 offset:43392
mm v[72:75],v[208:211],a[192:195],v[72:75]
mm v[72:75],v[212:215],v[148:151],v[72:75]
mm v[104:107],v[212:215],v[132:135],v[104:107]
s_mov_b32 s18, 4096
mm v[104:107],v[208:211],a[196:199],v[104:107]
rt v[212:213],v242 offset:33920
rt v[214:215],v242 offset:34944
mm v[108:111],v[216:219],a[196:199],v[108:111]
mm v[108:111],v[220:223],v[132:135],v[108:111]
st a[36:39],v255,s[48:51],s18 offen offset:0 sc1
mm v[76:79],v[220:223],v[148:151],v[76:79]
mm v[76:79],v[216:219],a[192:195],v[76:79]
rt v[208:209],v242 offset:42368
rt v[210:211],v242 offset:43392
mm v[44:47],v[216:219],v[140:143],v[44:47]
ds_write_b128 v238, a[32:35] offset:35840
mm v[44:47],v[220:223],a[204:207],v[44:47]
st a[40:43],v255,s[48:51],s18 offen offset:1024 sc1
mm v[12:15],v[220:223],a[200:203],v[12:15]
wt lgkmcnt(5)
mm v[12:15],v[216:219],v[128:131],v[12:15]
rt v[220:221],v243 offset:33920
rt v[222:223],v243 offset:34944
mm v[16:19],v[196:199],v[128:131],v[16:19]
s_mov_b64 s[52:53], s[86:87]
mm v[16:19],v[192:195],a[200:203],v[16:19]
s_bitcmp1_b32 21, s73
s_cselect_b32 s54, 16384, 0
s_cselect_b32 s86, s88, 1073741824
mm v[48:51],v[192:195],a[204:207],v[48:51]
s_lshl_b32 s89, s3, 14
v_add_u32 v248, s89, v255
mm v[48:51],v[196:199],v[140:143],v[48:51]
rt v[216:217],v243 offset:42368
rt v[218:219],v243 offset:43392
mm v[80:83],v[196:199],a[192:195],v[80:83]
ds_write_b128 v238, a[36:39] offset:36864
mm v[80:83],v[192:195],v[148:151],v[80:83]
s_add_u32 s86, s86, 262144
mm v[112:115],v[192:195],v[132:135],v[112:115]
s_cmp_eq_u32 s73, 0
s_cselect_b32 s86, s92, s86
s_cselect_b32 s92, 1073741824, s92
mm v[112:115],v[196:199],a[196:199],v[112:115]
s_mov_b32 s87, s92
s_add_u32 s89, s89, 16384
)ASM" R"ASM(mm v[116:119],v[204:207],a[196:199],v[116:119]
wt lgkmcnt(1)
s_barrier
mm v[116:119],v[200:203],v[132:135],v[116:119]
s_lshl_b32 m0, s3, 12
s_add_u32 m0, m0, 131072
mm v[84:87],v[200:203],v[148:151],v[84:87]
rt v[192:193],v240 offset:50688
rt v[194:195],v240 offset:51712
.La5_LW1_14:
s_cbranch_execz .La5_LW1_R0
mm v[84:87],v[204:207],a[192:195],v[84:87]
rt v[196:197],v240 offset:59136
rt v[198:199],v240 offset:60160
mm v[52:55],v[204:207],v[140:143],v[52:55]
ds_write_b128 v238, a[40:43] offset:37888
mm v[52:55],v[200:203],a[204:207],v[52:55]
v_bfe_u32 v251, v255, 2, 5
v_and_b32 v224, 0x380, v255
mm v[20:23],v[200:203],a[200:203],v[20:23]
v_add_u32 v251, v251, v224
v_and_b32 v224, 16, v255
mm v[20:23],v[204:207],v[128:131],v[20:23]
rt v[200:201],v241 offset:50688
rt v[202:203],v241 offset:51712
mm v[24:27],v[212:215],v[128:131],v[24:27]
v_cmp_eq_u32 vcc, 0, v224
mm v[24:27],v[208:211],a[200:203],v[24:27]
v_cndmask_b32 v249, v253, v252, vcc
mm v[56:59],v[208:211],a[204:207],v[56:59]
mm v[56:59],v[212:215],v[140:143],v[56:59]
rt v[204:205],v241 offset:59136
rt v[206:207],v241 offset:60160
mm v[88:91],v[212:215],a[192:195],v[88:91]
ds_write_b128 v238, a[44:47] offset:38912
mm v[88:91],v[208:211],v[148:151],v[88:91]
st a[44:47],v255,s[48:51],s18 offen offset:2048 sc1
mm v[120:123],v[208:211],v[132:135],v[120:123]
mm v[120:123],v[212:215],a[196:199],v[120:123]
rt v[208:209],v242 offset:50688
rt v[210:211],v242 offset:51712
mm v[124:127],v[220:223],a[196:199],v[124:127]
mm v[124:127],v[216:219],v[132:135],v[124:127]
mm v[92:95],v[216:219],v[148:151],v[92:95]
mm v[92:95],v[220:223],a[192:195],v[92:95]
rt v[212:213],v242 offset:59136
rt v[214:215],v242 offset:60160
mm v[60:63],v[220:223],v[140:143],v[60:63]
mm v[60:63],v[216:219],a[204:207],v[60:63]
st a[48:51],v255,s[48:51],s18 offen offset:3072 sc1
mm v[28:31],v[216:219],a[200:203],v[28:31]
wt vmcnt(6) lgkmcnt(5)
mm v[28:31],v[220:223],v[128:131],v[28:31]
rt v[216:217],v243 offset:50688
rt v[218:219],v243 offset:51712
mm v[0:3],v[192:195],a[96:99],v[0:3]
mm v[0:3],v[196:199],a[112:115],v[0:3]
mm v[32:35],v[196:199],a[116:119],v[32:35]
mm v[32:35],v[192:195],a[100:103],v[32:35]
rt v[220:221],v243 offset:59136
rt v[222:223],v243 offset:60160
mm v[64:67],v[192:195],a[104:107],v[64:67]
mm v[64:67],v[196:199],a[120:123],v[64:67]
mm v[96:99],v[196:199],a[124:127],v[96:99]
mm v[96:99],v[192:195],a[108:111],v[96:99]
rt v[196:197],v240 offset:50816
rt v[198:199],v240 offset:51840
mm v[100:103],v[200:203],a[108:111],v[100:103]
mm v[100:103],v[204:207],a[124:127],v[100:103]
mm v[68:71],v[204:207],a[120:123],v[68:71]
mm v[68:71],v[200:203],a[104:107],v[68:71]
rt v[192:193],v240 offset:59264
rt v[194:195],v240 offset:60288
mm v[36:39],v[200:203],a[100:103],v[36:39]
mm v[36:39],v[204:207],a[116:119],v[36:39]
mm v[4:7],v[204:207],a[112:115],v[4:7]
wt lgkmcnt(4)
mm v[4:7],v[200:203],a[96:99],v[4:7]
rt v[204:205],v241 offset:50816
rt v[206:207],v241 offset:51840
mm v[8:11],v[208:211],a[96:99],v[8:11]
mm v[8:11],v[212:215],a[112:115],v[8:11]
mm v[40:43],v[212:215],a[116:119],v[40:43]
cv v128,v0,v1
cv v129,v2,v3
mm v[40:43],v[208:211],a[100:103],v[40:43]
rt v[200:201],v241 offset:59264
rt v[202:203],v241 offset:60288
mm v[72:75],v[208:211],a[104:107],v[72:75]
cv v130,v4,v5
cv v131,v6,v7
mm v[72:75],v[212:215],a[120:123],v[72:75]
cv v144,v32,v33
cv v145,v34,v35
mm v[104:107],v[212:215],a[124:127],v[104:107]
cv v146,v36,v37
cv v147,v38,v39
mm v[104:107],v[208:211],a[108:111],v[104:107]
rt v[212:213],v242 offset:50816
rt v[214:215],v242 offset:51840
)ASM" R"ASM(mm v[108:111],v[216:219],a[108:111],v[108:111]
cv v160,v64,v65
cv v161,v66,v67
mm v[108:111],v[220:223],a[124:127],v[108:111]
cv v162,v68,v69
cv v163,v70,v71
mm v[76:79],v[220:223],a[120:123],v[76:79]
cv v176,v96,v97
cv v177,v98,v99
mm v[76:79],v[216:219],a[104:107],v[76:79]
rt v[208:209],v242 offset:59264
rt v[210:211],v242 offset:60288
mm v[44:47],v[216:219],a[100:103],v[44:47]
cv v178,v100,v101
cv v179,v102,v103
mm v[44:47],v[220:223],a[116:119],v[44:47]
mm v[12:15],v[220:223],a[112:115],v[12:15]
wt lgkmcnt(4)
mm v[12:15],v[216:219],a[96:99],v[12:15]
rt v[220:221],v243 offset:50816
rt v[222:223],v243 offset:51840
mm v[16:19],v[196:199],a[96:99],v[16:19]
v_mul_u32_u24 v241, 0x5555, v224
v_lshrrev_b32 v241, 4, v241
v_add_u32 v241, 0x8888, v241
mm v[16:19],v[192:195],a[112:115],v[16:19]
st v[128:131],v255,s[52:55],0 offen offset:0 sc1
mm v[48:51],v[192:195],a[116:119],v[48:51]
cv v132,v8,v9
cv v133,v10,v11
cv v181,v106,v107
mm v[48:51],v[196:199],a[100:103],v[48:51]
rt v[216:217],v243 offset:59264
rt v[218:219],v243 offset:60288
cv v182,v108,v109
mm v[80:83],v[196:199],a[104:107],v[80:83]
cv v134,v12,v13
cv v135,v14,v15
cv v183,v110,v111
mm v[80:83],v[192:195],a[120:123],v[80:83]
st v[144:147],v255,s[52:55],s95 offen offset:0 sc1
ar v34,a238
mm v[112:115],v[192:195],a[124:127],v[112:115]
cv v148,v40,v41
cv v149,v42,v43
ar v35,a239
mm v[112:115],v[196:199],a[108:111],v[112:115]
cv v150,v44,v45
cv v151,v46,v47
v_mov_b32 v36, 0xff800000
mm v[116:119],v[204:207],a[108:111],v[116:119]
wt lgkmcnt(0)
s_barrier
v_mov_b32 v37, 0xff800000
mm v[116:119],v[200:203],a[124:127],v[116:119]
st v[160:163],v255,s[52:55],s96 offen offset:0 sc1
v_mov_b32 v38, 0xff800000
mm v[84:87],v[200:203],a[120:123],v[84:87]
cv v164,v72,v73
cv v165,v74,v75
v_mov_b32 v39, 0xff800000
mm v[84:87],v[204:207],a[104:107],v[84:87]
rd a[64:67],v248 offset:0
rd a[68:71],v248 offset:1024
mm v[52:55],v[204:207],a[100:103],v[52:55]
cv v166,v76,v77
cv v167,v78,v79
mm v[52:55],v[200:203],a[116:119],v[52:55]
st v[176:179],v255,s[52:55],s97 offen offset:0 sc1
mm v[20:23],v[200:203],a[112:115],v[20:23]
s_nop 0
cv v180,v104,v105
mm v[20:23],v[204:207],a[96:99],v[20:23]
rd a[72:75],v248 offset:2048
rd a[76:79],v248 offset:3072
mm v[24:27],v[212:215],a[96:99],v[24:27]
ar v32,a236
ar v33,a237
mm v[24:27],v[208:211],a[112:115],v[24:27]
st v[132:135],v255,s[52:55],0 offen offset:1024 sc1
mm v[56:59],v[208:211],a[116:119],v[56:59]
cv v136,v16,v17
cv v137,v18,v19
mm v[56:59],v[212:215],a[100:103],v[56:59]
rd a[80:83],v248 offset:4096
rd a[84:87],v248 offset:5120
mm v[88:91],v[212:215],a[104:107],v[88:91]
cv v138,v20,v21
cv v139,v22,v23
mm v[88:91],v[208:211],a[120:123],v[88:91]
st v[148:151],v255,s[52:55],s95 offen offset:1024 sc1
mm v[120:123],v[208:211],a[124:127],v[120:123]
cv v152,v48,v49
cv v153,v50,v51
mm v[120:123],v[212:215],a[108:111],v[120:123]
rd a[88:91],v248 offset:6144
rd a[92:95],v248 offset:7168
mm v[124:127],v[220:223],a[108:111],v[124:127]
ds_write_b128 v238, a[48:51] offset:39936
mm v[124:127],v[216:219],a[124:127],v[124:127]
)ASM" R"ASM(st v[164:167],v255,s[52:55],s96 offen offset:1024 sc1
mm v[92:95],v[216:219],a[120:123],v[92:95]
cv v154,v52,v53
cv v155,v54,v55
mm v[92:95],v[220:223],a[104:107],v[92:95]
cv v168,v80,v81
cv v169,v82,v83
mm v[60:63],v[220:223],a[100:103],v[60:63]
cv v170,v84,v85
cv v171,v86,v87
mm v[60:63],v[216:219],a[116:119],v[60:63]
st v[180:183],v255,s[52:55],s97 offen offset:1024 sc1
mm v[28:31],v[216:219],a[112:115],v[28:31]
cv v184,v112,v113
cv v185,v114,v115
mm v[28:31],v[220:223],a[96:99],v[28:31]
cv v186,v116,v117
cv v187,v118,v119
wt lgkmcnt(1)
mm v[208:211],a[64:67],v[160:163],v[32:35]
mm v[216:219],a[64:67],v[176:179],0
st v[136:139],v255,s[52:55],0 offen offset:2048 sc1
mm v[212:215],a[80:83],v[160:163],v[36:39]
cv v172,v88,v89
cv v173,v90,v91
cv v174,v92,v93
mm v[220:223],a[80:83],v[176:179],v[32:35]
cv v175,v94,v95
cv v188,v120,v121
cv v189,v122,v123
mm v[208:211],a[68:71],v[164:167],v[208:211]
cv v190,v124,v125
cv v191,v126,v127
cv v140,v24,v25
mm v[216:219],a[68:71],v[180:183],v[216:219]
st v[152:155],v255,s[52:55],s95 offen offset:2048 sc1
cv v141,v26,v27
mm v[212:215],a[84:87],v[164:167],v[212:215]
cv v142,v28,v29
cv v143,v30,v31
cv v156,v56,v57
mm v[220:223],a[84:87],v[180:183],v[220:223]
cv v157,v58,v59
cv v158,v60,v61
cv v159,v62,v63
mm v[208:211],a[72:75],v[168:171],v[208:211]
mm v[216:219],a[72:75],v[184:187],v[216:219]
st v[168:171],v255,s[52:55],s96 offen offset:2048 sc1
mm v[212:215],a[88:91],v[168:171],v[212:215]
mm v[220:223],a[88:91],v[184:187],v[220:223]
mm v[208:211],a[76:79],v[172:175],v[208:211]
mm v[216:219],a[76:79],v[188:191],v[216:219]
st v[184:187],v255,s[52:55],s97 offen offset:2048 sc1
mm v[212:215],a[92:95],v[172:175],v[212:215]
rd a[64:67],v248 offset:16384
mm v[220:223],a[92:95],v[188:191],v[220:223]
rd a[96:99],v248 offset:8192
ex v0,v208
wt vmcnt(12)
buffer_store_dwordx2 v[252:253], off, s[64:67], s87 sc1
mm v[64:67],a[232:235],a[232:235],0
rd a[68:71],v248 offset:17408
ex v1,v209
mm v[68:71],a[232:235],a[232:235],0
rd a[100:103],v248 offset:9216
st v[140:143],v255,s[52:55],0 offen offset:3072 sc1
mm v[72:75],a[232:235],a[232:235],0
rd a[72:75],v248 offset:18432
ex v2,v210
mm v[76:79],a[232:235],a[232:235],0
rd a[104:107],v248 offset:10240
ex v3,v211
mm v[80:83],a[232:235],a[232:235],0
rd a[76:79],v248 offset:19456
ex v8,v216
mm v[84:87],a[232:235],a[232:235],0
rd a[108:111],v248 offset:11264
st v[156:159],v255,s[52:55],s95 offen offset:3072 sc1
mm v[88:91],a[232:235],a[232:235],0
rd a[80:83],v248 offset:20480
ex v9,v217
mm v[92:95],a[232:235],a[232:235],0
rd a[112:115],v248 offset:12288
ex v10,v218
mm v[96:99],a[232:235],a[232:235],0
rd a[84:87],v248 offset:21504
ex v11,v219
mm v[100:103],a[232:235],a[232:235],0
rd a[116:119],v248 offset:13312
st v[172:175],v255,s[52:55],s96 offen offset:3072 sc1
mm v[104:107],a[232:235],a[232:235],0
rd a[88:91],v248 offset:22528
ex v4,v212
mm v[108:111],a[232:235],a[232:235],0
rd a[120:123],v248 offset:14336
ex v5,v213
mm v[112:115],a[232:235],a[232:235],0
rd a[92:95],v248 offset:23552
ex v6,v214
mm v[116:119],a[232:235],a[232:235],0
)ASM" R"ASM(rd a[124:127],v248 offset:15360
st v[188:191],v255,s[52:55],s97 offen offset:3072 sc1
ex v7,v215
mm v[120:123],a[232:235],a[232:235],0
ex v12,v220
mm v[124:127],a[232:235],a[232:235],0
aw a192,0
aw a193,0
aw a194,0
aw a195,0
aw a196,0
aw a197,0
aw a198,0
aw a199,0
v_add_u32 v248, 0x4000, v248
s_cmp_eq_u32 s3, 3
s_cbranch_scc1 .La5_LA_FH
.p2align 6
; ---- attention, 32 keys per window: S = K Q^T (causal mask through SrcC), P = exp2(S), O += V^T P, row sums l += 1 P (one sparse MFMA for both tiles); here the later 32 rows only
.La5_LA_HH:
wt lgkmcnt(1)
mm v[208:211],a[64:67],v[160:163],0
ex v13,v221
mm v[208:211],a[68:71],v[164:167],v[208:211]
cv v232,v0,v1
cv v233,v2,v3
mm v[208:211],a[72:75],v[168:171],v[208:211]
ex v14,v222
mm v[208:211],a[76:79],v[172:175],v[208:211]
cv v236,v8,v9
cv v237,v10,v11
mm v[216:219],a[76:79],v[188:191],0
ex v15,v223
mm v[216:219],a[72:75],v[184:187],v[216:219]
cv v234,v4,v5
cv v235,v6,v7
mm v[216:219],a[68:71],v[180:183],v[216:219]
ex v0,v208
mm v[216:219],a[64:67],v[176:179],v[216:219]
cv v238,v12,v13
cv v239,v14,v15
mm v[212:215],a[80:83],v[160:163],0
ex v1,v209
mm v[212:215],a[84:87],v[164:167],v[212:215]
rd a[64:67],v248 offset:16384
rd a[68:71],v248 offset:17408
mm v[212:215],a[88:91],v[168:171],v[212:215]
ex v2,v210
mm v[212:215],a[92:95],v[172:175],v[212:215]
rd a[72:75],v248 offset:18432
rd a[76:79],v248 offset:19456
mm v[220:223],a[92:95],v[188:191],0
ex v3,v211
mm v[220:223],a[88:91],v[184:187],v[220:223]
rd a[92:95],v248 offset:23552
mm v[220:223],a[84:87],v[180:183],v[220:223]
ex v8,v216
mm v[220:223],a[80:83],v[176:179],v[220:223]
wt lgkmcnt(5)
rd a[88:91],v248 offset:22528
mm v[64:67],a[96:99],v[232:235],v[64:67]
ex v9,v217
mm v[96:99],a[96:99],v[236:239],v[96:99]
rd a[96:99],v248 offset:8192
rd a[84:87],v248 offset:21504
mm v[100:103],a[100:103],v[236:239],v[100:103]
ex v10,v218
mm v[68:71],a[100:103],v[232:235],v[68:71]
rd a[100:103],v248 offset:9216
rd a[80:83],v248 offset:20480
mm v[72:75],a[104:107],v[232:235],v[72:75]
ex v11,v219
mm v[104:107],a[104:107],v[236:239],v[104:107]
rd a[104:107],v248 offset:10240
mm v[108:111],a[108:111],v[236:239],v[108:111]
ex v4,v212
mm v[76:79],a[108:111],v[232:235],v[76:79]
rd a[108:111],v248 offset:11264
s_add_u32 s85, s85, 1
mm v[80:83],a[112:115],v[232:235],v[80:83]
ex v5,v213
mm v[112:115],a[112:115],v[236:239],v[112:115]
rd a[112:115],v248 offset:12288
s_add_u32 s89, s89, 32768
mm v[116:119],a[116:119],v[236:239],v[116:119]
ex v6,v214
mm v[84:87],a[116:119],v[232:235],v[84:87]
rd a[116:119],v248 offset:13312
s_add_u32 s92, s85, s3
mm v[88:91],a[120:123],v[232:235],v[88:91]
ex v7,v215
mm v[120:123],a[120:123],v[236:239],v[120:123]
rd a[120:123],v248 offset:14336
buffer_load_dword v250, v251, s[64:67], s20 offen sc1
mm v[124:127],a[124:127],v[236:239],v[124:127]
ex v12,v220
mm v[92:95],a[124:127],v[232:235],v[92:95]
rd a[124:127],v248 offset:15360
s_barrier
v_smfmac_f32_16x16x64_bf16 a[196:199], a[208:211], v[232:239], v241
)ASM" R"ASM(wt lgkmcnt(6)
mm v[208:211],a[64:67],v[160:163],0
ex v13,v221
mm v[208:211],a[68:71],v[164:167],v[208:211]
cv v232,v0,v1
cv v233,v2,v3
mm v[208:211],a[72:75],v[168:171],v[208:211]
ex v14,v222
mm v[208:211],a[76:79],v[172:175],v[208:211]
cv v236,v8,v9
cv v237,v10,v11
mm v[216:219],a[76:79],v[188:191],0
ex v15,v223
mm v[216:219],a[72:75],v[184:187],v[216:219]
cv v234,v4,v5
cv v235,v6,v7
mm v[216:219],a[68:71],v[180:183],v[216:219]
ex v0,v208
mm v[216:219],a[64:67],v[176:179],v[216:219]
cv v238,v12,v13
cv v239,v14,v15
mm v[212:215],a[80:83],v[160:163],0
ex v1,v209
mm v[212:215],a[84:87],v[164:167],v[212:215]
rd a[64:67],v248 offset:32768
rd a[68:71],v248 offset:33792
mm v[212:215],a[88:91],v[168:171],v[212:215]
ex v2,v210
mm v[212:215],a[92:95],v[172:175],v[212:215]
rd a[72:75],v248 offset:34816
rd a[76:79],v248 offset:35840
mm v[220:223],a[92:95],v[188:191],0
ex v3,v211
mm v[220:223],a[88:91],v[184:187],v[220:223]
rd a[92:95],v248 offset:39936
s_bitcmp1_b64 s[98:99], s85
mm v[220:223],a[84:87],v[180:183],v[220:223]
ex v8,v216
mm v[220:223],a[80:83],v[176:179],v[220:223]
wt lgkmcnt(5)
rd a[88:91],v248 offset:38912
mm v[64:67],a[96:99],v[232:235],v[64:67]
ex v9,v217
mm v[96:99],a[96:99],v[236:239],v[96:99]
rd a[96:99],v248 offset:24576
rd a[84:87],v248 offset:37888
mm v[100:103],a[100:103],v[236:239],v[100:103]
ex v10,v218
mm v[68:71],a[100:103],v[232:235],v[68:71]
rd a[100:103],v248 offset:25600
rd a[80:83],v248 offset:36864
mm v[72:75],a[104:107],v[232:235],v[72:75]
ex v11,v219
mm v[104:107],a[104:107],v[236:239],v[104:107]
rd a[104:107],v248 offset:26624
mm v[108:111],a[108:111],v[236:239],v[108:111]
ex v4,v212
mm v[76:79],a[108:111],v[232:235],v[76:79]
rd a[108:111],v248 offset:27648
mm v[80:83],a[112:115],v[232:235],v[80:83]
ex v5,v213
mm v[112:115],a[112:115],v[236:239],v[112:115]
rd a[112:115],v248 offset:28672
mm v[116:119],a[116:119],v[236:239],v[116:119]
ex v6,v214
mm v[84:87],a[116:119],v[232:235],v[84:87]
rd a[116:119],v248 offset:29696
wt vmcnt(1)
buffer_store_dwordx2 v[252:253], off, s[64:67], s86 sc1
s_mov_b32 s86, 1073741824
s_bitset0_b32 s20, 30
mm v[88:91],a[120:123],v[232:235],v[88:91]
ex v7,v215
mm v[120:123],a[120:123],v[236:239],v[120:123]
rd a[120:123],v248 offset:30720
mm v[124:127],a[124:127],v[236:239],v[124:127]
v_exp_f32_e64 v12, v220
mm v[92:95],a[124:127],v[232:235],v[92:95]
rd a[124:127],v248 offset:31744
v_smfmac_f32_16x16x64_bf16 a[196:199], a[208:211], v[232:239], v241
v_add_u32 v248, 0x8000, v248
s_cbranch_scc0 .La5_LA_HH1
s_call_b64 s[24:25], .La5_LA_EVENT
s_add_u32 s92, s85, s3
.La5_LA_HH1:
s_cmp_lt_u32 s92, 3
s_cbranch_scc1 .La5_LA_HH
; ---- first window with both row groups (diagonal of the earlier group)
.La5_LA_FH:
.La5_LW1_15:
s_cbranch_execz .La5_LW1_R0
wt lgkmcnt(9)
mm v[192:195],a[64:67],v[128:131],v[32:35]
ex v13,v221
mm v[192:195],a[68:71],v[132:135],v[192:195]
cv v232,v0,v1
cv v233,v2,v3
mm v[192:195],a[72:75],v[136:139],v[192:195]
ex v14,v222
mm v[192:195],a[76:79],v[140:143],v[192:195]
)ASM" R"ASM(ld v255,s[60:63],s17 offen offset:0 sc1 lds
mm v[200:203],a[76:79],v[156:159],0
ex v15,v223
mm v[200:203],a[72:75],v[152:155],v[200:203]
cv v236,v8,v9
cv v237,v10,v11
mm v[200:203],a[68:71],v[148:151],v[200:203]
ex v192,v192
mm v[200:203],a[64:67],v[144:147],v[200:203]
wt lgkmcnt(1)
cv v234,v4,v5
mm v[196:199],a[80:83],v[128:131],v[36:39]
ex v193,v193
mm v[196:199],a[84:87],v[132:135],v[196:199]
cv v224,v192,v193
s_add_u32 s85, s85, 1
mm v[196:199],a[88:91],v[136:139],v[196:199]
ex v194,v194
mm v[196:199],a[92:95],v[140:143],v[196:199]
ld v255,s[60:63],s17 offen offset:1024 sc1 lds
mm v[204:207],a[92:95],v[156:159],v[32:35]
ex v195,v195
mm v[204:207],a[88:91],v[152:155],v[204:207]
cv v225,v194,v195
s_add_u32 s89, s89, 16384
mm v[204:207],a[84:87],v[148:151],v[204:207]
ex v200,v200
mm v[204:207],a[80:83],v[144:147],v[204:207]
ld v255,s[60:63],s17 offen offset:2048 sc1 lds
mm v[208:211],a[64:67],v[160:163],0
ex v201,v201
mm v[208:211],a[68:71],v[164:167],v[208:211]
cv v228,v200,v201
s_and_b32 s89, s89, 0x1ffff
mm v[208:211],a[72:75],v[168:171],v[208:211]
ex v202,v202
mm v[208:211],a[76:79],v[172:175],v[208:211]
ld v255,s[60:63],s17 offen offset:3072 sc1 lds
mm v[216:219],a[76:79],v[188:191],0
ex v203,v203
mm v[216:219],a[72:75],v[184:187],v[216:219]
cv v229,v202,v203
mm v[216:219],a[68:71],v[180:183],v[216:219]
ex v196,v196
mm v[216:219],a[64:67],v[176:179],v[216:219]
cv v235,v6,v7
cv v238,v12,v13
mm v[212:215],a[80:83],v[160:163],0
ex v197,v197
s_add_u32 s17, s17, s80
mm v[212:215],a[84:87],v[164:167],v[212:215]
cv v226,v196,v197
s_add_u32 m0, m0, 16384
s_and_b32 m0, m0, s78
mm v[212:215],a[88:91],v[168:171],v[212:215]
ex v198,v198
mm v[212:215],a[92:95],v[172:175],v[212:215]
cv v239,v14,v15
wt lgkmcnt(0)
mm v[220:223],a[92:95],v[188:191],0
ex v199,v199
mm v[220:223],a[88:91],v[184:187],v[220:223]
cv v227,v198,v199
mm v[220:223],a[84:87],v[180:183],v[220:223]
ex v204,v204
mm v[220:223],a[80:83],v[176:179],v[220:223]
wt vmcnt(10)
s_barrier
mm v[64:67],a[96:99],v[232:235],v[64:67]
ex v205,v205
mm v[96:99],a[96:99],v[236:239],v[96:99]
rd a[96:99],v248 offset:8192
cv v230,v204,v205
mm v[100:103],a[100:103],v[236:239],v[100:103]
ex v206,v206
mm v[68:71],a[100:103],v[232:235],v[68:71]
rd a[100:103],v248 offset:9216
.La5_LW2_0:
s_cbranch_execz .La5_LW2_R0
buffer_load_dword v250, v251, s[64:67], s20 offen sc1
mm v[72:75],a[104:107],v[232:235],v[72:75]
ex v207,v207
mm v[104:107],a[104:107],v[236:239],v[104:107]
rd a[104:107],v248 offset:10240
cv v231,v206,v207
mm v[108:111],a[108:111],v[236:239],v[108:111]
ex v208,v208
mm v[76:79],a[108:111],v[232:235],v[76:79]
rd a[108:111],v248 offset:11264
mm v[80:83],a[112:115],v[232:235],v[80:83]
ex v209,v209
mm v[112:115],a[112:115],v[236:239],v[112:115]
rd a[112:115],v248 offset:12288
mm v[116:119],a[116:119],v[236:239],v[116:119]
ex v210,v210
mm v[84:87],a[116:119],v[232:235],v[84:87]
rd a[116:119],v248 offset:13312
mm v[88:91],a[120:123],v[232:235],v[88:91]
ex v211,v211
mm v[120:123],a[120:123],v[236:239],v[120:123]
)ASM" R"ASM(rd a[120:123],v248 offset:14336
s_bitcmp1_b64 s[98:99], s85
mm v[124:127],a[124:127],v[236:239],v[124:127]
ex v216,v216
mm v[92:95],a[124:127],v[232:235],v[92:95]
rd a[124:127],v248 offset:15360
v_or_b32 v248, s89, v255
v_smfmac_f32_16x16x64_bf16 a[196:199], a[208:211], v[232:239], v241
wt lgkmcnt(4)
v_smfmac_f32_16x16x64_bf16 a[192:195], a[208:211], v[224:231], v241
rd a[64:67],v248 offset:0
mm v[0:3],a[96:99],v[224:227],0
ex v217,v217
mm v[32:35],a[96:99],v[228:231],0
rd a[68:71],v248 offset:1024
cv v232,v208,v209
mm v[36:39],a[100:103],v[228:231],0
ex v218,v218
mm v[4:7],a[100:103],v[224:227],0
rd a[72:75],v248 offset:2048
cv v233,v210,v211
mm v[8:11],a[104:107],v[224:227],0
ex v219,v219
mm v[40:43],a[104:107],v[228:231],0
rd a[76:79],v248 offset:3072
cv v236,v216,v217
mm v[44:47],a[108:111],v[228:231],0
ex v212,v212
mm v[12:15],a[108:111],v[224:227],0
rd a[80:83],v248 offset:4096
wt lgkmcnt(5)
mm v[16:19],a[112:115],v[224:227],0
ex v213,v213
mm v[48:51],a[112:115],v[228:231],0
rd a[84:87],v248 offset:5120
cv v237,v218,v219
mm v[52:55],a[116:119],v[228:231],0
ex v214,v214
mm v[20:23],a[116:119],v[224:227],0
rd a[88:91],v248 offset:6144
cv v234,v212,v213
mm v[24:27],a[120:123],v[224:227],0
ex v215,v215
mm v[56:59],a[120:123],v[228:231],0
rd a[92:95],v248 offset:7168
cv v235,v214,v215
mm v[60:63],a[124:127],v[228:231],0
ex v220,v220
mm v[28:31],a[124:127],v[224:227],0
wt vmcnt(5)
buffer_store_dwordx2 v[252:253], off, s[64:67], s86 sc1
s_mov_b32 s86, 1073741824
s_bitset0_b32 s20, 30
s_cbranch_scc1 .La5_LA_FE0
.La5_LA_FS0:
wt lgkmcnt(4)
mm v[192:195],a[64:67],v[128:131],0
ex v221,v221
mm v[192:195],a[68:71],v[132:135],v[192:195]
cv v238,v220,v221
s_add_u32 s85, s85, 1
mm v[192:195],a[72:75],v[136:139],v[192:195]
ex v222,v222
mm v[192:195],a[76:79],v[140:143],v[192:195]
ld v255,s[60:63],s17 offen offset:0 sc1 lds
mm v[200:203],a[76:79],v[156:159],0
ex v223,v223
mm v[200:203],a[72:75],v[152:155],v[200:203]
cv v239,v222,v223
s_add_u32 s89, s89, 16384
mm v[200:203],a[68:71],v[148:151],v[200:203]
ex v192,v192
mm v[200:203],a[64:67],v[144:147],v[200:203]
wt lgkmcnt(0)
s_and_b32 s89, s89, 0x1ffff
mm v[196:199],a[80:83],v[128:131],0
ex v193,v193
mm v[196:199],a[84:87],v[132:135],v[196:199]
cv v224,v192,v193
mm v[196:199],a[88:91],v[136:139],v[196:199]
ex v194,v194
mm v[196:199],a[92:95],v[140:143],v[196:199]
ld v255,s[60:63],s17 offen offset:1024 sc1 lds
mm v[204:207],a[92:95],v[156:159],0
ex v195,v195
mm v[204:207],a[88:91],v[152:155],v[204:207]
cv v225,v194,v195
mm v[204:207],a[84:87],v[148:151],v[204:207]
ex v200,v200
mm v[204:207],a[80:83],v[144:147],v[204:207]
ld v255,s[60:63],s17 offen offset:2048 sc1 lds
mm v[208:211],a[64:67],v[160:163],0
ex v201,v201
mm v[208:211],a[68:71],v[164:167],v[208:211]
cv v228,v200,v201
mm v[208:211],a[72:75],v[168:171],v[208:211]
ex v202,v202
mm v[208:211],a[76:79],v[172:175],v[208:211]
ld v255,s[60:63],s17 offen offset:3072 sc1 lds
mm v[216:219],a[76:79],v[188:191],0
)ASM" R"ASM(ex v203,v203
mm v[216:219],a[72:75],v[184:187],v[216:219]
cv v229,v202,v203
mm v[216:219],a[68:71],v[180:183],v[216:219]
ex v196,v196
mm v[216:219],a[64:67],v[176:179],v[216:219]
buffer_load_dword a253, v254, s[36:39], s90 offen
mm v[212:215],a[80:83],v[160:163],0
ex v197,v197
mm v[212:215],a[84:87],v[164:167],v[212:215]
cv v226,v196,v197
s_add_u32 s90, s90, s21
mm v[212:215],a[88:91],v[168:171],v[212:215]
ex v198,v198
mm v[212:215],a[92:95],v[172:175],v[212:215]
s_add_u32 s17, s17, s80
s_add_u32 m0, m0, 16384
mm v[220:223],a[92:95],v[188:191],0
ex v199,v199
mm v[220:223],a[88:91],v[184:187],v[220:223]
cv v227,v198,v199
s_and_b32 m0, m0, s78
mm v[220:223],a[84:87],v[180:183],v[220:223]
ex v204,v204
mm v[220:223],a[80:83],v[176:179],v[220:223]
wt vmcnt(11)
s_barrier
mm v[64:67],a[96:99],v[232:235],v[64:67]
ex v205,v205
mm v[96:99],a[96:99],v[236:239],v[96:99]
rd a[96:99],v248 offset:8192
cv v230,v204,v205
mm v[100:103],a[100:103],v[236:239],v[100:103]
ex v206,v206
mm v[68:71],a[100:103],v[232:235],v[68:71]
rd a[100:103],v248 offset:9216
s_cmp_eq_u32 s85, s81
buffer_load_dword v250, v251, s[64:67], s20 offen sc1
mm v[72:75],a[104:107],v[232:235],v[72:75]
ex v207,v207
mm v[104:107],a[104:107],v[236:239],v[104:107]
rd a[104:107],v248 offset:10240
cv v231,v206,v207
mm v[108:111],a[108:111],v[236:239],v[108:111]
ex v208,v208
mm v[76:79],a[108:111],v[232:235],v[76:79]
rd a[108:111],v248 offset:11264
s_cselect_b32 s17, s77, s17
mm v[80:83],a[112:115],v[232:235],v[80:83]
ex v209,v209
mm v[112:115],a[112:115],v[236:239],v[112:115]
rd a[112:115],v248 offset:12288
s_cselect_b32 s80, -8192, s80
mm v[116:119],a[116:119],v[236:239],v[116:119]
ex v210,v210
mm v[84:87],a[116:119],v[232:235],v[84:87]
rd a[116:119],v248 offset:13312
mm v[88:91],a[120:123],v[232:235],v[88:91]
ex v211,v211
mm v[120:123],a[120:123],v[236:239],v[120:123]
rd a[120:123],v248 offset:14336
s_bitcmp1_b64 s[98:99], s85
mm v[124:127],a[124:127],v[236:239],v[124:127]
ex v216,v216
mm v[92:95],a[124:127],v[232:235],v[92:95]
rd a[124:127],v248 offset:15360
v_or_b32 v248, s89, v255
v_smfmac_f32_16x16x64_bf16 a[196:199], a[208:211], v[232:239], v241
wt lgkmcnt(4)
v_smfmac_f32_16x16x64_bf16 a[192:195], a[208:211], v[224:231], v241
rd a[64:67],v248 offset:0
mm v[0:3],a[96:99],v[224:227],v[0:3]
ex v217,v217
mm v[32:35],a[96:99],v[228:231],v[32:35]
rd a[68:71],v248 offset:1024
cv v232,v208,v209
mm v[36:39],a[100:103],v[228:231],v[36:39]
ex v218,v218
mm v[4:7],a[100:103],v[224:227],v[4:7]
rd a[72:75],v248 offset:2048
cv v233,v210,v211
mm v[8:11],a[104:107],v[224:227],v[8:11]
ex v219,v219
mm v[40:43],a[104:107],v[228:231],v[40:43]
rd a[76:79],v248 offset:3072
cv v236,v216,v217
mm v[44:47],a[108:111],v[228:231],v[44:47]
ex v212,v212
mm v[12:15],a[108:111],v[224:227],v[12:15]
rd a[80:83],v248 offset:4096
wt lgkmcnt(5)
mm v[16:19],a[112:115],v[224:227],v[16:19]
ex v213,v213
mm v[48:51],a[112:115],v[228:231],v[48:51]
rd a[84:87],v248 offset:5120
cv v237,v218,v219
mm v[52:55],a[116:119],v[228:231],v[52:55]
ex v214,v214
mm v[20:23],a[116:119],v[224:227],v[20:23]
rd a[88:91],v248 offset:6144
)ASM" R"ASM(cv v234,v212,v213
mm v[24:27],a[120:123],v[224:227],v[24:27]
ex v215,v215
mm v[56:59],a[120:123],v[228:231],v[56:59]
rd a[92:95],v248 offset:7168
cv v235,v214,v215
mm v[60:63],a[124:127],v[228:231],v[60:63]
v_exp_f32_e64 v220, v220
mm v[28:31],a[124:127],v[224:227],v[28:31]
s_cbranch_scc1 .La5_LA_F1E2
.p2align 6
; ---- steady windows
.La5_LA_F1:
wt lgkmcnt(4)
mm v[192:195],a[64:67],v[128:131],0
ex v221,v221
mm v[192:195],a[68:71],v[132:135],v[192:195]
cv v238,v220,v221
s_add_u32 s85, s85, 1
mm v[192:195],a[72:75],v[136:139],v[192:195]
ex v222,v222
mm v[192:195],a[76:79],v[140:143],v[192:195]
ld v255,s[60:63],s17 offen offset:0 sc1 lds
mm v[200:203],a[76:79],v[156:159],0
ex v223,v223
mm v[200:203],a[72:75],v[152:155],v[200:203]
cv v239,v222,v223
s_add_u32 s89, s89, 16384
mm v[200:203],a[68:71],v[148:151],v[200:203]
ex v192,v192
mm v[200:203],a[64:67],v[144:147],v[200:203]
wt lgkmcnt(0)
s_and_b32 s89, s89, 0x1ffff
mm v[196:199],a[80:83],v[128:131],0
ex v193,v193
mm v[196:199],a[84:87],v[132:135],v[196:199]
cv v224,v192,v193
mm v[196:199],a[88:91],v[136:139],v[196:199]
ex v194,v194
mm v[196:199],a[92:95],v[140:143],v[196:199]
ld v255,s[60:63],s17 offen offset:1024 sc1 lds
mm v[204:207],a[92:95],v[156:159],0
ex v195,v195
mm v[204:207],a[88:91],v[152:155],v[204:207]
cv v225,v194,v195
mm v[204:207],a[84:87],v[148:151],v[204:207]
ex v200,v200
mm v[204:207],a[80:83],v[144:147],v[204:207]
ld v255,s[60:63],s17 offen offset:2048 sc1 lds
mm v[208:211],a[64:67],v[160:163],0
ex v201,v201
mm v[208:211],a[68:71],v[164:167],v[208:211]
cv v228,v200,v201
mm v[208:211],a[72:75],v[168:171],v[208:211]
ex v202,v202
mm v[208:211],a[76:79],v[172:175],v[208:211]
ld v255,s[60:63],s17 offen offset:3072 sc1 lds
mm v[216:219],a[76:79],v[188:191],0
ex v203,v203
mm v[216:219],a[72:75],v[184:187],v[216:219]
cv v229,v202,v203
mm v[216:219],a[68:71],v[180:183],v[216:219]
ex v196,v196
mm v[216:219],a[64:67],v[176:179],v[216:219]
buffer_load_dword a253, v254, s[36:39], s90 offen
mm v[212:215],a[80:83],v[160:163],0
ex v197,v197
mm v[212:215],a[84:87],v[164:167],v[212:215]
cv v226,v196,v197
s_add_u32 s90, s90, s21
mm v[212:215],a[88:91],v[168:171],v[212:215]
ex v198,v198
mm v[212:215],a[92:95],v[172:175],v[212:215]
s_add_u32 s17, s17, s80
s_add_u32 m0, m0, 16384
mm v[220:223],a[92:95],v[188:191],0
ex v199,v199
mm v[220:223],a[88:91],v[184:187],v[220:223]
cv v227,v198,v199
s_and_b32 m0, m0, s78
mm v[220:223],a[84:87],v[180:183],v[220:223]
ex v204,v204
mm v[220:223],a[80:83],v[176:179],v[220:223]
wt vmcnt(11)
s_barrier
mm v[64:67],a[96:99],v[232:235],v[64:67]
ex v205,v205
mm v[96:99],a[96:99],v[236:239],v[96:99]
rd a[96:99],v248 offset:8192
cv v230,v204,v205
mm v[100:103],a[100:103],v[236:239],v[100:103]
ex v206,v206
mm v[68:71],a[100:103],v[232:235],v[68:71]
rd a[100:103],v248 offset:9216
s_cmp_eq_u32 s85, s81
mm v[72:75],a[104:107],v[232:235],v[72:75]
ex v207,v207
mm v[104:107],a[104:107],v[236:239],v[104:107]
rd a[104:107],v248 offset:10240
cv v231,v206,v207
)ASM" R"ASM(mm v[108:111],a[108:111],v[236:239],v[108:111]
ex v208,v208
mm v[76:79],a[108:111],v[232:235],v[76:79]
rd a[108:111],v248 offset:11264
s_cselect_b32 s17, s77, s17
mm v[80:83],a[112:115],v[232:235],v[80:83]
ex v209,v209
mm v[112:115],a[112:115],v[236:239],v[112:115]
rd a[112:115],v248 offset:12288
s_cselect_b32 s80, -8192, s80
mm v[116:119],a[116:119],v[236:239],v[116:119]
ex v210,v210
mm v[84:87],a[116:119],v[232:235],v[84:87]
rd a[116:119],v248 offset:13312
mm v[88:91],a[120:123],v[232:235],v[88:91]
ex v211,v211
mm v[120:123],a[120:123],v[236:239],v[120:123]
rd a[120:123],v248 offset:14336
s_bitcmp1_b64 s[98:99], s85
mm v[124:127],a[124:127],v[236:239],v[124:127]
ex v216,v216
mm v[92:95],a[124:127],v[232:235],v[92:95]
rd a[124:127],v248 offset:15360
v_or_b32 v248, s89, v255
v_smfmac_f32_16x16x64_bf16 a[196:199], a[208:211], v[232:239], v241
wt lgkmcnt(4)
v_smfmac_f32_16x16x64_bf16 a[192:195], a[208:211], v[224:231], v241
rd a[64:67],v248 offset:0
mm v[0:3],a[96:99],v[224:227],v[0:3]
ex v217,v217
mm v[32:35],a[96:99],v[228:231],v[32:35]
rd a[68:71],v248 offset:1024
cv v232,v208,v209
mm v[36:39],a[100:103],v[228:231],v[36:39]
ex v218,v218
mm v[4:7],a[100:103],v[224:227],v[4:7]
rd a[72:75],v248 offset:2048
cv v233,v210,v211
mm v[8:11],a[104:107],v[224:227],v[8:11]
ex v219,v219
mm v[40:43],a[104:107],v[228:231],v[40:43]
rd a[76:79],v248 offset:3072
cv v236,v216,v217
mm v[44:47],a[108:111],v[228:231],v[44:47]
ex v212,v212
mm v[12:15],a[108:111],v[224:227],v[12:15]
rd a[80:83],v248 offset:4096
wt lgkmcnt(5)
mm v[16:19],a[112:115],v[224:227],v[16:19]
ex v213,v213
mm v[48:51],a[112:115],v[228:231],v[48:51]
rd a[84:87],v248 offset:5120
cv v237,v218,v219
mm v[52:55],a[116:119],v[228:231],v[52:55]
ex v214,v214
mm v[20:23],a[116:119],v[224:227],v[20:23]
rd a[88:91],v248 offset:6144
cv v234,v212,v213
mm v[24:27],a[120:123],v[224:227],v[24:27]
ex v215,v215
mm v[56:59],a[120:123],v[228:231],v[56:59]
rd a[92:95],v248 offset:7168
cv v235,v214,v215
mm v[60:63],a[124:127],v[228:231],v[60:63]
ex v220,v220
mm v[28:31],a[124:127],v[224:227],v[28:31]
s_cbranch_scc1 .La5_LA_F1E2
wt lgkmcnt(4)
mm v[192:195],a[64:67],v[128:131],0
ex v221,v221
mm v[192:195],a[68:71],v[132:135],v[192:195]
cv v238,v220,v221
s_add_u32 s85, s85, 1
mm v[192:195],a[72:75],v[136:139],v[192:195]
ex v222,v222
mm v[192:195],a[76:79],v[140:143],v[192:195]
ld v255,s[60:63],s17 offen offset:0 sc1 lds
mm v[200:203],a[76:79],v[156:159],0
ex v223,v223
mm v[200:203],a[72:75],v[152:155],v[200:203]
cv v239,v222,v223
s_add_u32 s89, s89, 16384
mm v[200:203],a[68:71],v[148:151],v[200:203]
ex v192,v192
mm v[200:203],a[64:67],v[144:147],v[200:203]
wt lgkmcnt(0)
s_and_b32 s89, s89, 0x1ffff
mm v[196:199],a[80:83],v[128:131],0
ex v193,v193
mm v[196:199],a[84:87],v[132:135],v[196:199]
cv v224,v192,v193
mm v[196:199],a[88:91],v[136:139],v[196:199]
ex v194,v194
mm v[196:199],a[92:95],v[140:143],v[196:199]
ld v255,s[60:63],s17 offen offset:1024 sc1 lds
mm v[204:207],a[92:95],v[156:159],0
ex v195,v195
mm v[204:207],a[88:91],v[152:155],v[204:207]
cv v225,v194,v195
mm v[204:207],a[84:87],v[148:151],v[204:207]
)ASM" R"ASM(ex v200,v200
mm v[204:207],a[80:83],v[144:147],v[204:207]
ld v255,s[60:63],s17 offen offset:2048 sc1 lds
mm v[208:211],a[64:67],v[160:163],0
ex v201,v201
mm v[208:211],a[68:71],v[164:167],v[208:211]
cv v228,v200,v201
mm v[208:211],a[72:75],v[168:171],v[208:211]
ex v202,v202
mm v[208:211],a[76:79],v[172:175],v[208:211]
ld v255,s[60:63],s17 offen offset:3072 sc1 lds
mm v[216:219],a[76:79],v[188:191],0
ex v203,v203
mm v[216:219],a[72:75],v[184:187],v[216:219]
cv v229,v202,v203
mm v[216:219],a[68:71],v[180:183],v[216:219]
ex v196,v196
mm v[216:219],a[64:67],v[176:179],v[216:219]
buffer_load_dword a253, v254, s[36:39], s90 offen
mm v[212:215],a[80:83],v[160:163],0
ex v197,v197
mm v[212:215],a[84:87],v[164:167],v[212:215]
cv v226,v196,v197
s_add_u32 s90, s90, s21
mm v[212:215],a[88:91],v[168:171],v[212:215]
ex v198,v198
mm v[212:215],a[92:95],v[172:175],v[212:215]
s_add_u32 s17, s17, s80
s_add_u32 m0, m0, 16384
mm v[220:223],a[92:95],v[188:191],0
ex v199,v199
mm v[220:223],a[88:91],v[184:187],v[220:223]
cv v227,v198,v199
s_and_b32 m0, m0, s78
mm v[220:223],a[84:87],v[180:183],v[220:223]
ex v204,v204
mm v[220:223],a[80:83],v[176:179],v[220:223]
wt vmcnt(10)
s_barrier
mm v[64:67],a[96:99],v[232:235],v[64:67]
ex v205,v205
mm v[96:99],a[96:99],v[236:239],v[96:99]
rd a[96:99],v248 offset:8192
cv v230,v204,v205
mm v[100:103],a[100:103],v[236:239],v[100:103]
ex v206,v206
mm v[68:71],a[100:103],v[232:235],v[68:71]
rd a[100:103],v248 offset:9216
s_cmp_eq_u32 s85, s81
mm v[72:75],a[104:107],v[232:235],v[72:75]
ex v207,v207
mm v[104:107],a[104:107],v[236:239],v[104:107]
rd a[104:107],v248 offset:10240
cv v231,v206,v207
mm v[108:111],a[108:111],v[236:239],v[108:111]
ex v208,v208
mm v[76:79],a[108:111],v[232:235],v[76:79]
rd a[108:111],v248 offset:11264
s_cselect_b32 s17, s77, s17
mm v[80:83],a[112:115],v[232:235],v[80:83]
ex v209,v209
mm v[112:115],a[112:115],v[236:239],v[112:115]
rd a[112:115],v248 offset:12288
s_cselect_b32 s80, -8192, s80
mm v[116:119],a[116:119],v[236:239],v[116:119]
ex v210,v210
mm v[84:87],a[116:119],v[232:235],v[84:87]
rd a[116:119],v248 offset:13312
mm v[88:91],a[120:123],v[232:235],v[88:91]
ex v211,v211
mm v[120:123],a[120:123],v[236:239],v[120:123]
rd a[120:123],v248 offset:14336
s_bitcmp1_b64 s[98:99], s85
mm v[124:127],a[124:127],v[236:239],v[124:127]
ex v216,v216
mm v[92:95],a[124:127],v[232:235],v[92:95]
rd a[124:127],v248 offset:15360
v_or_b32 v248, s89, v255
v_smfmac_f32_16x16x64_bf16 a[196:199], a[208:211], v[232:239], v241
wt lgkmcnt(4)
v_smfmac_f32_16x16x64_bf16 a[192:195], a[208:211], v[224:231], v241
rd a[64:67],v248 offset:0
mm v[0:3],a[96:99],v[224:227],v[0:3]
ex v217,v217
mm v[32:35],a[96:99],v[228:231],v[32:35]
rd a[68:71],v248 offset:1024
cv v232,v208,v209
mm v[36:39],a[100:103],v[228:231],v[36:39]
ex v218,v218
mm v[4:7],a[100:103],v[224:227],v[4:7]
rd a[72:75],v248 offset:2048
cv v233,v210,v211
mm v[8:11],a[104:107],v[224:227],v[8:11]
ex v219,v219
mm v[40:43],a[104:107],v[228:231],v[40:43]
rd a[76:79],v248 offset:3072
cv v236,v216,v217
mm v[44:47],a[108:111],v[228:231],v[44:47]
ex v212,v212
mm v[12:15],a[108:111],v[224:227],v[12:15]
)ASM" R"ASM(rd a[80:83],v248 offset:4096
wt lgkmcnt(5)
mm v[16:19],a[112:115],v[224:227],v[16:19]
ex v213,v213
mm v[48:51],a[112:115],v[228:231],v[48:51]
rd a[84:87],v248 offset:5120
cv v237,v218,v219
mm v[52:55],a[116:119],v[228:231],v[52:55]
ex v214,v214
mm v[20:23],a[116:119],v[224:227],v[20:23]
rd a[88:91],v248 offset:6144
cv v234,v212,v213
mm v[24:27],a[120:123],v[224:227],v[24:27]
ex v215,v215
mm v[56:59],a[120:123],v[228:231],v[56:59]
rd a[92:95],v248 offset:7168
cv v235,v214,v215
mm v[60:63],a[124:127],v[228:231],v[60:63]
ex v220,v220
mm v[28:31],a[124:127],v[224:227],v[28:31]
.La5_LA_F1E:
s_cbranch_scc0 .La5_LA_F1
.La5_LA_F1E2:
s_cmp_eq_u32 s85, s82
s_cbranch_scc1 .La5_LA_F1X
s_call_b64 s[24:25], .La5_LA_EVENT
s_branch .La5_LA_F1
.La5_LA_FE0:
s_nop 0
s_nop 0
.La5_LW2_1:
s_cbranch_execz .La5_LW2_R1
s_cmp_eq_u32 s85, s82
s_cbranch_scc1 .La5_LA_F1X
s_call_b64 s[24:25], .La5_LA_EVENT
s_branch .La5_LA_FS0
; ---- hand-offs between windows
.La5_LA_EVENT:
s_cmp_lg_u32 s85, 2
s_nop 0
s_nop 0
s_cbranch_scc1 .La5_LA_E1
wt vmcnt(0)
v_cmp_eq_u32 vcc, v250, v249
s_mov_b32 s17, s16
s_mov_b32 s78, 0x1ffff
s_lshl_b32 m0, s3, 12
s_or_b32 vcc_hi, vcc_hi, s79
s_cmp_eq_u64 vcc, -1
s_cbranch_scc0 .La5_LA_KSPIN
.La5_LA_KOK:
ld v255,s[60:63],s17 offen offset:0 sc1 lds
ld v255,s[60:63],s17 offen offset:1024 sc1 lds
ld v255,s[60:63],s17 offen offset:2048 sc1 lds
ld v255,s[60:63],s17 offen offset:3072 sc1 lds
s_add_u32 s17, s17, s80
s_add_u32 m0, m0, 16384
s_mov_b32 s20, s19
v_and_b32 v251, 4, v251
s_cmp_lg_u32 s3, 0
s_cbranch_scc1 .La5_LA_POLL
s_nop 0
ld v255,s[60:63],s17 offen offset:0 sc1 lds
ld v255,s[60:63],s17 offen offset:1024 sc1 lds
ld v255,s[60:63],s17 offen offset:2048 sc1 lds
ld v255,s[60:63],s17 offen offset:3072 sc1 lds
s_add_u32 s17, s17, s80
s_add_u32 m0, m0, 16384
.La5_LA_POLL:
s_cmp_lg_u32 s22, 0
s_nop 0
s_nop 0
s_cbranch_scc1 .La5_LA_WP
s_mov_b64 exec, 0
s_cmp_eq_u32 s3, 0
s_cbranch_scc1 .La5_LW5_0
s_cmp_eq_u32 s3, 1
s_cbranch_scc1 .La5_LW5_1
s_cmp_eq_u32 s3, 2
s_cbranch_scc1 .La5_LW5_2
s_branch .La5_LW5_3
.La5_LW5_R0:
s_mov_b64 exec, -1
.La5_LA_RETURN:
s_setpc_b64 s[24:25]
.La5_LA_WP:
s_mov_b64 exec, 0
s_cmp_eq_u32 s3, 0
s_cbranch_scc1 .La5_LW6_0
s_cmp_eq_u32 s3, 1
s_cbranch_scc1 .La5_LW6_1
s_cmp_eq_u32 s3, 2
s_cbranch_scc1 .La5_LW6_2
s_branch .La5_LW6_3
.La5_LW6_R0:
s_mov_b64 exec, -1
s_cmp_eq_u32 s73, 1
s_cbranch_scc1 .La5_LA_E0
buffer_load_dword v250, v251, s[64:67], s20 offen sc1
s_setpc_b64 s[24:25]
)ASM" R"ASM(.La5_LA_E0:
s_mov_b32 s54, 16384
ld a[0:3],v255,s[52:55],0 offen offset:0 sc1
ld a[4:7],v255,s[52:55],0 offen offset:1024 sc1
ld a[8:11],v255,s[52:55],0 offen offset:2048 sc1
ld a[12:15],v255,s[52:55],0 offen offset:3072 sc1
ld a[16:19],v255,s[52:55],s95 offen offset:0 sc1
ld a[20:23],v255,s[52:55],s95 offen offset:1024 sc1
s_setpc_b64 s[24:25]
.La5_LA_E1:
ld a[24:27],v255,s[52:55],s95 offen offset:2048 sc1
ld a[28:31],v255,s[52:55],s95 offen offset:3072 sc1
ld a[32:35],v255,s[52:55],s96 offen offset:0 sc1
ld a[36:39],v255,s[52:55],s96 offen offset:1024 sc1
.La5_LW2_2:
s_cbranch_execz .La5_LW2_R0
ld a[40:43],v255,s[52:55],s96 offen offset:2048 sc1
ld a[44:47],v255,s[52:55],s96 offen offset:3072 sc1
ld a[48:51],v255,s[52:55],s97 offen offset:0 sc1
ld a[52:55],v255,s[52:55],s97 offen offset:1024 sc1
ld a[56:59],v255,s[52:55],s97 offen offset:2048 sc1
ld a[60:63],v255,s[52:55],s97 offen offset:3072 sc1
s_setpc_b64 s[24:25]
; ---- helper: the partner-s q rows arrive during the last windows of the own block
.La5_LA_F1X:
s_cmp_eq_u32 s75, 0
s_nop 0
s_cbranch_scc1 .La5_LA_DC3
s_cmp_eq_u32 s22, 0
s_cbranch_scc1 .La5_LA_DC
s_cmp_eq_u32 s73, 1
s_cbranch_scc1 .La5_LA_F1Y
s_nop 0
v_cmp_eq_u32 vcc, v250, v249
s_mov_b32 s54, 16384
s_and_b32 s92, vcc_lo, 3
s_cmp_eq_u32 s92, 3
s_cbranch_scc0 .La5_LA_QSPIN
.La5_LA_QOK:
wt lgkmcnt(4)
mm v[192:195],a[64:67],v[128:131],0
ex v221,v221
mm v[192:195],a[68:71],v[132:135],v[192:195]
cv v238,v220,v221
s_add_u32 s85, s85, 1
mm v[192:195],a[72:75],v[136:139],v[192:195]
ex v222,v222
mm v[192:195],a[76:79],v[140:143],v[192:195]
ld v255,s[60:63],s17 offen offset:0 sc1 lds
mm v[200:203],a[76:79],v[156:159],0
ex v223,v223
mm v[200:203],a[72:75],v[152:155],v[200:203]
cv v239,v222,v223
s_add_u32 s89, s89, 16384
mm v[200:203],a[68:71],v[148:151],v[200:203]
ex v192,v192
mm v[200:203],a[64:67],v[144:147],v[200:203]
wt lgkmcnt(0)
s_and_b32 s89, s89, 0x1ffff
mm v[196:199],a[80:83],v[128:131],0
ex v193,v193
mm v[196:199],a[84:87],v[132:135],v[196:199]
cv v224,v192,v193
mm v[196:199],a[88:91],v[136:139],v[196:199]
ex v194,v194
mm v[196:199],a[92:95],v[140:143],v[196:199]
ld v255,s[60:63],s17 offen offset:1024 sc1 lds
mm v[204:207],a[92:95],v[156:159],0
ex v195,v195
mm v[204:207],a[88:91],v[152:155],v[204:207]
cv v225,v194,v195
mm v[204:207],a[84:87],v[148:151],v[204:207]
ex v200,v200
mm v[204:207],a[80:83],v[144:147],v[204:207]
ld v255,s[60:63],s17 offen offset:2048 sc1 lds
mm v[208:211],a[64:67],v[160:163],0
ex v201,v201
mm v[208:211],a[68:71],v[164:167],v[208:211]
cv v228,v200,v201
mm v[208:211],a[72:75],v[168:171],v[208:211]
ex v202,v202
mm v[208:211],a[76:79],v[172:175],v[208:211]
ld v255,s[60:63],s17 offen offset:3072 sc1 lds
mm v[216:219],a[76:79],v[188:191],0
ex v203,v203
mm v[216:219],a[72:75],v[184:187],v[216:219]
cv v229,v202,v203
mm v[216:219],a[68:71],v[180:183],v[216:219]
ex v196,v196
mm v[216:219],a[64:67],v[176:179],v[216:219]
ld a[0:3],v255,s[52:55],0 offen offset:0 sc1
mm v[212:215],a[80:83],v[160:163],0
ex v197,v197
mm v[212:215],a[84:87],v[164:167],v[212:215]
cv v226,v196,v197
s_add_u32 s17, s17, s80
mm v[212:215],a[88:91],v[168:171],v[212:215]
ex v198,v198
mm v[212:215],a[92:95],v[172:175],v[212:215]
s_add_u32 m0, m0, 16384
)ASM" R"ASM(s_and_b32 m0, m0, s78
ld a[4:7],v255,s[52:55],0 offen offset:1024 sc1
mm v[220:223],a[92:95],v[188:191],0
ex v199,v199
mm v[220:223],a[88:91],v[184:187],v[220:223]
cv v227,v198,v199
mm v[220:223],a[84:87],v[180:183],v[220:223]
ex v204,v204
mm v[220:223],a[80:83],v[176:179],v[220:223]
wt vmcnt(12)
s_barrier
mm v[64:67],a[96:99],v[232:235],v[64:67]
ex v205,v205
mm v[96:99],a[96:99],v[236:239],v[96:99]
rd a[96:99],v248 offset:8192
cv v230,v204,v205
mm v[100:103],a[100:103],v[236:239],v[100:103]
ex v206,v206
mm v[68:71],a[100:103],v[232:235],v[68:71]
rd a[100:103],v248 offset:9216
ld a[8:11],v255,s[52:55],0 offen offset:2048 sc1
mm v[72:75],a[104:107],v[232:235],v[72:75]
ex v207,v207
mm v[104:107],a[104:107],v[236:239],v[104:107]
rd a[104:107],v248 offset:10240
cv v231,v206,v207
mm v[108:111],a[108:111],v[236:239],v[108:111]
ex v208,v208
mm v[76:79],a[108:111],v[232:235],v[76:79]
rd a[108:111],v248 offset:11264
ld a[12:15],v255,s[52:55],0 offen offset:3072 sc1
mm v[80:83],a[112:115],v[232:235],v[80:83]
ex v209,v209
mm v[112:115],a[112:115],v[236:239],v[112:115]
rd a[112:115],v248 offset:12288
mm v[116:119],a[116:119],v[236:239],v[116:119]
ex v210,v210
mm v[84:87],a[116:119],v[232:235],v[84:87]
rd a[116:119],v248 offset:13312
ld a[16:19],v255,s[52:55],s95 offen offset:0 sc1
mm v[88:91],a[120:123],v[232:235],v[88:91]
ex v211,v211
mm v[120:123],a[120:123],v[236:239],v[120:123]
rd a[120:123],v248 offset:14336
s_bitcmp1_b64 s[98:99], s85
mm v[124:127],a[124:127],v[236:239],v[124:127]
ex v216,v216
mm v[92:95],a[124:127],v[232:235],v[92:95]
rd a[124:127],v248 offset:15360
v_or_b32 v248, s89, v255
v_smfmac_f32_16x16x64_bf16 a[196:199], a[208:211], v[232:239], v241
wt lgkmcnt(4)
v_smfmac_f32_16x16x64_bf16 a[192:195], a[208:211], v[224:231], v241
rd a[64:67],v248 offset:0
mm v[0:3],a[96:99],v[224:227],v[0:3]
ex v217,v217
mm v[32:35],a[96:99],v[228:231],v[32:35]
rd a[68:71],v248 offset:1024
cv v232,v208,v209
mm v[36:39],a[100:103],v[228:231],v[36:39]
ex v218,v218
mm v[4:7],a[100:103],v[224:227],v[4:7]
rd a[72:75],v248 offset:2048
cv v233,v210,v211
mm v[8:11],a[104:107],v[224:227],v[8:11]
ex v219,v219
mm v[40:43],a[104:107],v[228:231],v[40:43]
rd a[76:79],v248 offset:3072
cv v236,v216,v217
mm v[44:47],a[108:111],v[228:231],v[44:47]
ex v212,v212
mm v[12:15],a[108:111],v[224:227],v[12:15]
rd a[80:83],v248 offset:4096
wt lgkmcnt(5)
mm v[16:19],a[112:115],v[224:227],v[16:19]
ex v213,v213
mm v[48:51],a[112:115],v[228:231],v[48:51]
rd a[84:87],v248 offset:5120
cv v237,v218,v219
mm v[52:55],a[116:119],v[228:231],v[52:55]
ex v214,v214
mm v[20:23],a[116:119],v[224:227],v[20:23]
rd a[88:91],v248 offset:6144
cv v234,v212,v213
mm v[24:27],a[120:123],v[224:227],v[24:27]
ex v215,v215
mm v[56:59],a[120:123],v[228:231],v[56:59]
rd a[92:95],v248 offset:7168
cv v235,v214,v215
mm v[60:63],a[124:127],v[228:231],v[60:63]
ex v220,v220
mm v[28:31],a[124:127],v[224:227],v[28:31]
ld a[20:23],v255,s[52:55],s95 offen offset:1024 sc1
wt lgkmcnt(4)
mm v[192:195],a[64:67],v[128:131],0
ex v221,v221
mm v[192:195],a[68:71],v[132:135],v[192:195]
cv v238,v220,v221
s_add_u32 s85, s85, 1
mm v[192:195],a[72:75],v[136:139],v[192:195]
)ASM" R"ASM(ex v222,v222
mm v[192:195],a[76:79],v[140:143],v[192:195]
ld v255,s[60:63],s17 offen offset:0 sc1 lds
mm v[200:203],a[76:79],v[156:159],0
ex v223,v223
mm v[200:203],a[72:75],v[152:155],v[200:203]
cv v239,v222,v223
s_add_u32 s89, s89, 16384
mm v[200:203],a[68:71],v[148:151],v[200:203]
ex v192,v192
mm v[200:203],a[64:67],v[144:147],v[200:203]
wt lgkmcnt(0)
s_and_b32 s89, s89, 0x1ffff
ld a[24:27],v255,s[52:55],s95 offen offset:2048 sc1
mm v[196:199],a[80:83],v[128:131],0
ex v193,v193
mm v[196:199],a[84:87],v[132:135],v[196:199]
cv v224,v192,v193
mm v[196:199],a[88:91],v[136:139],v[196:199]
ex v194,v194
mm v[196:199],a[92:95],v[140:143],v[196:199]
ld v255,s[60:63],s17 offen offset:1024 sc1 lds
mm v[204:207],a[92:95],v[156:159],0
ex v195,v195
mm v[204:207],a[88:91],v[152:155],v[204:207]
cv v225,v194,v195
mm v[204:207],a[84:87],v[148:151],v[204:207]
ex v200,v200
mm v[204:207],a[80:83],v[144:147],v[204:207]
ld v255,s[60:63],s17 offen offset:2048 sc1 lds
mm v[208:211],a[64:67],v[160:163],0
ex v201,v201
mm v[208:211],a[68:71],v[164:167],v[208:211]
cv v228,v200,v201
mm v[208:211],a[72:75],v[168:171],v[208:211]
ex v202,v202
mm v[208:211],a[76:79],v[172:175],v[208:211]
ld v255,s[60:63],s17 offen offset:3072 sc1 lds
mm v[216:219],a[76:79],v[188:191],0
ex v203,v203
mm v[216:219],a[72:75],v[184:187],v[216:219]
cv v229,v202,v203
mm v[216:219],a[68:71],v[180:183],v[216:219]
ex v196,v196
mm v[216:219],a[64:67],v[176:179],v[216:219]
ld a[28:31],v255,s[52:55],s95 offen offset:3072 sc1
mm v[212:215],a[80:83],v[160:163],0
ex v197,v197
mm v[212:215],a[84:87],v[164:167],v[212:215]
cv v226,v196,v197
s_add_u32 s17, s17, s80
mm v[212:215],a[88:91],v[168:171],v[212:215]
ex v198,v198
mm v[212:215],a[92:95],v[172:175],v[212:215]
s_add_u32 m0, m0, 16384
s_and_b32 m0, m0, s78
ld a[32:35],v255,s[52:55],s96 offen offset:0 sc1
mm v[220:223],a[92:95],v[188:191],0
ex v199,v199
mm v[220:223],a[88:91],v[184:187],v[220:223]
cv v227,v198,v199
mm v[220:223],a[84:87],v[180:183],v[220:223]
ex v204,v204
mm v[220:223],a[80:83],v[176:179],v[220:223]
wt vmcnt(18)
s_barrier
mm v[64:67],a[96:99],v[232:235],v[64:67]
ex v205,v205
mm v[96:99],a[96:99],v[236:239],v[96:99]
rd a[96:99],v248 offset:8192
cv v230,v204,v205
mm v[100:103],a[100:103],v[236:239],v[100:103]
ex v206,v206
mm v[68:71],a[100:103],v[232:235],v[68:71]
rd a[100:103],v248 offset:9216
ld a[36:39],v255,s[52:55],s96 offen offset:1024 sc1
mm v[72:75],a[104:107],v[232:235],v[72:75]
ex v207,v207
mm v[104:107],a[104:107],v[236:239],v[104:107]
rd a[104:107],v248 offset:10240
cv v231,v206,v207
mm v[108:111],a[108:111],v[236:239],v[108:111]
ex v208,v208
mm v[76:79],a[108:111],v[232:235],v[76:79]
rd a[108:111],v248 offset:11264
ld a[40:43],v255,s[52:55],s96 offen offset:2048 sc1
mm v[80:83],a[112:115],v[232:235],v[80:83]
ex v209,v209
mm v[112:115],a[112:115],v[236:239],v[112:115]
rd a[112:115],v248 offset:12288
mm v[116:119],a[116:119],v[236:239],v[116:119]
ex v210,v210
mm v[84:87],a[116:119],v[232:235],v[84:87]
rd a[116:119],v248 offset:13312
ld a[44:47],v255,s[52:55],s96 offen offset:3072 sc1
mm v[88:91],a[120:123],v[232:235],v[88:91]
ex v211,v211
mm v[120:123],a[120:123],v[236:239],v[120:123]
rd a[120:123],v248 offset:14336
s_bitcmp1_b64 s[98:99], s85
)ASM" R"ASM(mm v[124:127],a[124:127],v[236:239],v[124:127]
ex v216,v216
mm v[92:95],a[124:127],v[232:235],v[92:95]
rd a[124:127],v248 offset:15360
v_or_b32 v248, s89, v255
v_smfmac_f32_16x16x64_bf16 a[196:199], a[208:211], v[232:239], v241
wt lgkmcnt(4)
v_smfmac_f32_16x16x64_bf16 a[192:195], a[208:211], v[224:231], v241
rd a[64:67],v248 offset:0
mm v[0:3],a[96:99],v[224:227],v[0:3]
ex v217,v217
mm v[32:35],a[96:99],v[228:231],v[32:35]
rd a[68:71],v248 offset:1024
cv v232,v208,v209
mm v[36:39],a[100:103],v[228:231],v[36:39]
ex v218,v218
mm v[4:7],a[100:103],v[224:227],v[4:7]
rd a[72:75],v248 offset:2048
cv v233,v210,v211
mm v[8:11],a[104:107],v[224:227],v[8:11]
ex v219,v219
mm v[40:43],a[104:107],v[228:231],v[40:43]
rd a[76:79],v248 offset:3072
cv v236,v216,v217
mm v[44:47],a[108:111],v[228:231],v[44:47]
ex v212,v212
mm v[12:15],a[108:111],v[224:227],v[12:15]
rd a[80:83],v248 offset:4096
wt lgkmcnt(5)
mm v[16:19],a[112:115],v[224:227],v[16:19]
ex v213,v213
mm v[48:51],a[112:115],v[228:231],v[48:51]
rd a[84:87],v248 offset:5120
cv v237,v218,v219
mm v[52:55],a[116:119],v[228:231],v[52:55]
ex v214,v214
mm v[20:23],a[116:119],v[224:227],v[20:23]
rd a[88:91],v248 offset:6144
cv v234,v212,v213
mm v[24:27],a[120:123],v[224:227],v[24:27]
ex v215,v215
mm v[56:59],a[120:123],v[228:231],v[56:59]
rd a[92:95],v248 offset:7168
cv v235,v214,v215
mm v[60:63],a[124:127],v[228:231],v[60:63]
ex v220,v220
mm v[28:31],a[124:127],v[224:227],v[28:31]
ld a[48:51],v255,s[52:55],s97 offen offset:0 sc1
wt lgkmcnt(4)
mm v[192:195],a[64:67],v[128:131],0
ex v221,v221
mm v[192:195],a[68:71],v[132:135],v[192:195]
cv v238,v220,v221
s_add_u32 s85, s85, 1
mm v[192:195],a[72:75],v[136:139],v[192:195]
ex v222,v222
mm v[192:195],a[76:79],v[140:143],v[192:195]
ld v255,s[60:63],s17 offen offset:0 sc1 lds
mm v[200:203],a[76:79],v[156:159],0
ex v223,v223
mm v[200:203],a[72:75],v[152:155],v[200:203]
cv v239,v222,v223
s_add_u32 s89, s89, 16384
mm v[200:203],a[68:71],v[148:151],v[200:203]
ex v192,v192
mm v[200:203],a[64:67],v[144:147],v[200:203]
wt lgkmcnt(0)
s_and_b32 s89, s89, 0x1ffff
ld a[52:55],v255,s[52:55],s97 offen offset:1024 sc1
mm v[196:199],a[80:83],v[128:131],0
ex v193,v193
mm v[196:199],a[84:87],v[132:135],v[196:199]
cv v224,v192,v193
mm v[196:199],a[88:91],v[136:139],v[196:199]
ex v194,v194
mm v[196:199],a[92:95],v[140:143],v[196:199]
ld v255,s[60:63],s17 offen offset:1024 sc1 lds
mm v[204:207],a[92:95],v[156:159],0
ex v195,v195
mm v[204:207],a[88:91],v[152:155],v[204:207]
cv v225,v194,v195
mm v[204:207],a[84:87],v[148:151],v[204:207]
ex v200,v200
mm v[204:207],a[80:83],v[144:147],v[204:207]
ld v255,s[60:63],s17 offen offset:2048 sc1 lds
mm v[208:211],a[64:67],v[160:163],0
ex v201,v201
mm v[208:211],a[68:71],v[164:167],v[208:211]
cv v228,v200,v201
mm v[208:211],a[72:75],v[168:171],v[208:211]
ex v202,v202
mm v[208:211],a[76:79],v[172:175],v[208:211]
ld v255,s[60:63],s17 offen offset:3072 sc1 lds
mm v[216:219],a[76:79],v[188:191],0
ex v203,v203
mm v[216:219],a[72:75],v[184:187],v[216:219]
cv v229,v202,v203
mm v[216:219],a[68:71],v[180:183],v[216:219]
ex v196,v196
mm v[216:219],a[64:67],v[176:179],v[216:219]
)ASM" R"ASM(ld a[56:59],v255,s[52:55],s97 offen offset:2048 sc1
mm v[212:215],a[80:83],v[160:163],0
ex v197,v197
mm v[212:215],a[84:87],v[164:167],v[212:215]
cv v226,v196,v197
s_add_u32 s17, s17, s80
mm v[212:215],a[88:91],v[168:171],v[212:215]
ex v198,v198
mm v[212:215],a[92:95],v[172:175],v[212:215]
s_add_u32 m0, m0, 16384
s_and_b32 m0, m0, s78
ld a[60:63],v255,s[52:55],s97 offen offset:3072 sc1
mm v[220:223],a[92:95],v[188:191],0
ex v199,v199
mm v[220:223],a[88:91],v[184:187],v[220:223]
cv v227,v198,v199
mm v[220:223],a[84:87],v[180:183],v[220:223]
ex v204,v204
mm v[220:223],a[80:83],v[176:179],v[220:223]
wt vmcnt(24)
s_barrier
mm v[64:67],a[96:99],v[232:235],v[64:67]
ex v205,v205
mm v[96:99],a[96:99],v[236:239],v[96:99]
rd a[96:99],v248 offset:8192
cv v230,v204,v205
mm v[100:103],a[100:103],v[236:239],v[100:103]
ex v206,v206
mm v[68:71],a[100:103],v[232:235],v[68:71]
rd a[100:103],v248 offset:9216
mm v[72:75],a[104:107],v[232:235],v[72:75]
ex v207,v207
mm v[104:107],a[104:107],v[236:239],v[104:107]
rd a[104:107],v248 offset:10240
cv v231,v206,v207
mm v[108:111],a[108:111],v[236:239],v[108:111]
ex v208,v208
mm v[76:79],a[108:111],v[232:235],v[76:79]
rd a[108:111],v248 offset:11264
mm v[80:83],a[112:115],v[232:235],v[80:83]
ex v209,v209
mm v[112:115],a[112:115],v[236:239],v[112:115]
rd a[112:115],v248 offset:12288
mm v[116:119],a[116:119],v[236:239],v[116:119]
ex v210,v210
mm v[84:87],a[116:119],v[232:235],v[84:87]
rd a[116:119],v248 offset:13312
mm v[88:91],a[120:123],v[232:235],v[88:91]
ex v211,v211
mm v[120:123],a[120:123],v[236:239],v[120:123]
rd a[120:123],v248 offset:14336
s_bitcmp1_b64 s[98:99], s85
mm v[124:127],a[124:127],v[236:239],v[124:127]
ex v216,v216
mm v[92:95],a[124:127],v[232:235],v[92:95]
rd a[124:127],v248 offset:15360
v_or_b32 v248, s89, v255
v_smfmac_f32_16x16x64_bf16 a[196:199], a[208:211], v[232:239], v241
wt lgkmcnt(4)
v_smfmac_f32_16x16x64_bf16 a[192:195], a[208:211], v[224:231], v241
rd a[64:67],v248 offset:0
mm v[0:3],a[96:99],v[224:227],v[0:3]
ex v217,v217
mm v[32:35],a[96:99],v[228:231],v[32:35]
rd a[68:71],v248 offset:1024
cv v232,v208,v209
mm v[36:39],a[100:103],v[228:231],v[36:39]
ex v218,v218
mm v[4:7],a[100:103],v[224:227],v[4:7]
rd a[72:75],v248 offset:2048
cv v233,v210,v211
mm v[8:11],a[104:107],v[224:227],v[8:11]
ex v219,v219
mm v[40:43],a[104:107],v[228:231],v[40:43]
rd a[76:79],v248 offset:3072
cv v236,v216,v217
mm v[44:47],a[108:111],v[228:231],v[44:47]
ex v212,v212
mm v[12:15],a[108:111],v[224:227],v[12:15]
rd a[80:83],v248 offset:4096
wt lgkmcnt(5)
mm v[16:19],a[112:115],v[224:227],v[16:19]
ex v213,v213
mm v[48:51],a[112:115],v[228:231],v[48:51]
rd a[84:87],v248 offset:5120
cv v237,v218,v219
mm v[52:55],a[116:119],v[228:231],v[52:55]
ex v214,v214
mm v[20:23],a[116:119],v[224:227],v[20:23]
rd a[88:91],v248 offset:6144
cv v234,v212,v213
mm v[24:27],a[120:123],v[224:227],v[24:27]
ex v215,v215
mm v[56:59],a[120:123],v[228:231],v[56:59]
rd a[92:95],v248 offset:7168
cv v235,v214,v215
mm v[60:63],a[124:127],v[228:231],v[60:63]
ex v220,v220
mm v[28:31],a[124:127],v[224:227],v[28:31]
; ---- helper: own block finished: l is checked (.La5_LA_SLOW if outside [2^-60, 2^60] or NaN), O / l leaves; the partner-s rows start
)ASM" R"ASM(.La5_LA_F1Y:
.La5_LW6_0:
s_cbranch_execz .La5_LW6_4
wt lgkmcnt(4)
wt vmcnt(0)
mm v[192:195],a[64:67],a[0:3],0
ex v221,v221
mm v[192:195],a[68:71],a[4:7],v[192:195]
cv v238,v220,v221
s_add_u32 s89, s89, 16384
mm v[192:195],a[72:75],a[8:11],v[192:195]
ex v222,v222
mm v[192:195],a[76:79],a[12:15],v[192:195]
ld v255,s[60:63],s17 offen offset:0 sc1 lds
ar v242,a192
ar v240,a193
v_add_f32 v242, v242, v240
mm v[200:203],a[76:79],a[28:31],0
ex v223,v223
ar v243,a194
ar v240,a195
v_add_f32 v243, v243, v240
mm v[200:203],a[72:75],a[24:27],v[200:203]
cv v239,v222,v223
s_and_b32 s89, s89, 0x1ffff
aw a192,0
aw a193,0
aw a194,0
mm v[200:203],a[68:71],a[20:23],v[200:203]
ex v192,v192
aw a195,0
v_subrev_u32 v240, 562036736, v242
mm v[200:203],a[64:67],a[16:19],v[200:203]
wt lgkmcnt(0)
v_rcp_f32 v242, v242
v_cmp_le_u32 vcc, 1006632960, v240
v_subrev_u32 v240, 562036736, v243
mm v[196:199],a[80:83],a[0:3],0
ex v193,v193
s_mov_b64 s[92:93], vcc
v_cmp_le_u32 vcc, 1006632960, v240
mm v[196:199],a[84:87],a[4:7],v[196:199]
cv v224,v192,v193
v_rcp_f32 v243, v243
mm v[196:199],a[88:91],a[8:11],v[196:199]
ex v194,v194
s_or_b64 vcc, vcc, s[92:93]
s_cbranch_scc1 .La5_LA_SC1
.La5_LA_SR1:
v_mul_f32 v0, v0, v242
mm v[196:199],a[92:95],a[12:15],v[196:199]
ld v255,s[60:63],s17 offen offset:1024 sc1 lds
v_mul_f32 v1, v1, v242
v_mul_f32 v2, v2, v242
v_mul_f32 v3, v3, v242
mm v[204:207],a[92:95],a[28:31],0
ex v195,v195
v_mul_f32 v4, v4, v242
v_mul_f32 v5, v5, v242
v_mul_f32 v6, v6, v242
mm v[204:207],a[88:91],a[24:27],v[204:207]
cv v225,v194,v195
v_mul_f32 v7, v7, v242
cv v128,v0,v1
cv v129,v2,v3
cv v130,v4,v5
mm v[204:207],a[84:87],a[20:23],v[204:207]
ex v200,v200
cv v131,v6,v7
v_mul_f32 v8, v8, v242
v_mul_f32 v9, v9, v242
mm v[204:207],a[80:83],a[16:19],v[204:207]
ld v255,s[60:63],s17 offen offset:2048 sc1 lds
v_mul_f32 v10, v10, v242
v_mul_f32 v11, v11, v242
v_mul_f32 v12, v12, v242
mm v[208:211],a[64:67],a[32:35],0
ex v201,v201
v_mul_f32 v13, v13, v242
v_mul_f32 v14, v14, v242
v_mul_f32 v15, v15, v242
mm v[208:211],a[68:71],a[36:39],v[208:211]
cv v228,v200,v201
cv v132,v8,v9
cv v133,v10,v11
cv v134,v12,v13
cv v135,v14,v15
mm v[208:211],a[72:75],a[40:43],v[208:211]
ex v202,v202
v_mul_f32 v16, v16, v242
v_mul_f32 v17, v17, v242
v_mul_f32 v18, v18, v242
mm v[208:211],a[76:79],a[44:47],v[208:211]
ld v255,s[60:63],s17 offen offset:3072 sc1 lds
v_mul_f32 v19, v19, v242
v_mul_f32 v20, v20, v242
v_mul_f32 v21, v21, v242
mm v[216:219],a[76:79],a[60:63],0
ex v203,v203
v_mul_f32 v22, v22, v242
)ASM" R"ASM(v_mul_f32 v23, v23, v242
cv v136,v16,v17
mm v[216:219],a[72:75],a[56:59],v[216:219]
cv v229,v202,v203
cv v137,v18,v19
cv v138,v20,v21
cv v139,v22,v23
v_mul_f32 v24, v24, v242
mm v[216:219],a[68:71],a[52:55],v[216:219]
ex v196,v196
v_mul_f32 v25, v25, v242
v_mul_f32 v26, v26, v242
v_mul_f32 v27, v27, v242
mm v[216:219],a[64:67],a[48:51],v[216:219]
v_mul_f32 v28, v28, v242
v_mul_f32 v29, v29, v242
v_mul_f32 v30, v30, v242
v_mul_f32 v31, v31, v242
cv v140,v24,v25
mm v[212:215],a[80:83],a[32:35],0
ex v197,v197
s_add_u32 s17, s17, s80
cv v141,v26,v27
cv v142,v28,v29
mm v[212:215],a[84:87],a[36:39],v[212:215]
cv v226,v196,v197
s_add_u32 m0, m0, 16384
s_and_b32 m0, m0, s78
cv v143,v30,v31
v_mul_f32 v32, v32, v243
mm v[212:215],a[88:91],a[40:43],v[212:215]
ex v198,v198
v_mul_f32 v33, v33, v243
v_mul_f32 v34, v34, v243
v_mul_f32 v35, v35, v243
mm v[212:215],a[92:95],a[44:47],v[212:215]
st v[128:131],v244,s[44:47],s91 offen offset:0 sc1
.La5_LW6_1:
s_cbranch_execz .La5_LW6_5
v_mul_f32 v36, v36, v243
v_mul_f32 v37, v37, v243
v_mul_f32 v38, v38, v243
mm v[220:223],a[92:95],a[60:63],0
ex v199,v199
v_mul_f32 v39, v39, v243
cv v144,v32,v33
cv v145,v34,v35
mm v[220:223],a[88:91],a[56:59],v[220:223]
cv v227,v198,v199
cv v146,v36,v37
cv v147,v38,v39
v_mul_f32 v40, v40, v243
v_mul_f32 v41, v41, v243
mm v[220:223],a[84:87],a[52:55],v[220:223]
ex v204,v204
v_mul_f32 v42, v42, v243
v_mul_f32 v43, v43, v243
v_mul_f32 v44, v44, v243
mm v[220:223],a[80:83],a[48:51],v[220:223]
s_nop 0
s_barrier
v_mul_f32 v45, v45, v243
v_mul_f32 v46, v46, v243
v_mul_f32 v47, v47, v243
mm v[64:67],a[96:99],v[232:235],v[64:67]
ex v205,v205
cv v148,v40,v41
cv v149,v42,v43
cv v150,v44,v45
mm v[96:99],a[96:99],v[236:239],v[96:99]
rd a[96:99],v248 offset:8192
cv v230,v204,v205
cv v151,v46,v47
v_mul_f32 v48, v48, v243
v_mul_f32 v49, v49, v243
mm v[100:103],a[100:103],v[236:239],v[100:103]
ex v206,v206
v_mul_f32 v50, v50, v243
v_mul_f32 v51, v51, v243
v_mul_f32 v52, v52, v243
mm v[68:71],a[100:103],v[232:235],v[68:71]
rd a[100:103],v248 offset:9216
st v[132:135],v244,s[44:47],s91 offen offset:1024 sc1
v_mul_f32 v53, v53, v243
v_mul_f32 v54, v54, v243
mm v[72:75],a[104:107],v[232:235],v[72:75]
ex v207,v207
v_mul_f32 v55, v55, v243
cv v152,v48,v49
cv v153,v50,v51
mm v[104:107],a[104:107],v[236:239],v[104:107]
rd a[104:107],v248 offset:10240
cv v231,v206,v207
cv v154,v52,v53
cv v155,v54,v55
v_mul_f32 v56, v56, v243
mm v[108:111],a[108:111],v[236:239],v[108:111]
ex v208,v208
v_mul_f32 v57, v57, v243
v_mul_f32 v58, v58, v243
)ASM" R"ASM(v_mul_f32 v59, v59, v243
mm v[76:79],a[108:111],v[232:235],v[76:79]
rd a[108:111],v248 offset:11264
v_mul_f32 v60, v60, v243
v_mul_f32 v61, v61, v243
v_mul_f32 v62, v62, v243
v_mul_f32 v63, v63, v243
mm v[80:83],a[112:115],v[232:235],v[80:83]
ex v209,v209
cv v156,v56,v57
cv v157,v58,v59
cv v158,v60,v61
mm v[112:115],a[112:115],v[236:239],v[112:115]
rd a[112:115],v248 offset:12288
st v[136:139],v244,s[44:47],s91 offen offset:2048 sc1
cv v159,v62,v63
mm v[116:119],a[116:119],v[236:239],v[116:119]
ex v210,v210
mm v[84:87],a[116:119],v[232:235],v[84:87]
rd a[116:119],v248 offset:13312
mm v[88:91],a[120:123],v[232:235],v[88:91]
ex v211,v211
mm v[120:123],a[120:123],v[236:239],v[120:123]
rd a[120:123],v248 offset:14336
st v[140:143],v244,s[44:47],s91 offen offset:3072 sc1
mm v[124:127],a[124:127],v[236:239],v[124:127]
ex v216,v216
mm v[92:95],a[124:127],v[232:235],v[92:95]
rd a[124:127],v248 offset:15360
v_or_b32 v248, s89, v255
v_smfmac_f32_16x16x64_bf16 a[196:199], a[208:211], v[232:239], v241
wt lgkmcnt(4)
v_smfmac_f32_16x16x64_bf16 a[192:195], a[208:211], v[224:231], v241
rd a[64:67],v248 offset:0
mm v[0:3],a[96:99],v[224:227],0
ex v217,v217
mm v[32:35],a[96:99],v[228:231],0
rd a[68:71],v248 offset:1024
cv v232,v208,v209
ar v242,a196
ar v240,a197
v_add_f32 v242, v242, v240
mm v[36:39],a[100:103],v[228:231],0
ex v218,v218
ar v243,a198
ar v240,a199
v_add_f32 v243, v243, v240
mm v[4:7],a[100:103],v[224:227],0
rd a[72:75],v248 offset:2048
cv v233,v210,v211
st v[144:147],v245,s[44:47],s91 offen offset:0 sc1
aw a196,0
mm v[8:11],a[104:107],v[224:227],0
ex v219,v219
aw a197,0
aw a198,0
aw a199,0
mm v[40:43],a[104:107],v[228:231],0
rd a[76:79],v248 offset:3072
cv v236,v216,v217
v_subrev_u32 v240, 562036736, v242
v_rcp_f32 v242, v242
mm v[44:47],a[108:111],v[228:231],0
ex v212,v212
v_cmp_le_u32 vcc, 1006632960, v240
v_subrev_u32 v240, 562036736, v243
s_mov_b64 s[92:93], vcc
mm v[12:15],a[108:111],v[224:227],0
rd a[80:83],v248 offset:4096
wt lgkmcnt(5)
v_cmp_le_u32 vcc, 1006632960, v240
v_rcp_f32 v243, v243
mm v[16:19],a[112:115],v[224:227],0
ex v213,v213
s_or_b64 vcc, vcc, s[92:93]
s_cbranch_scc1 .La5_LA_SC2
.La5_LA_SR2:
v_mul_f32 v64, v64, v242
mm v[48:51],a[112:115],v[228:231],0
rd a[84:87],v248 offset:5120
cv v237,v218,v219
v_mul_f32 v65, v65, v242
v_mul_f32 v66, v66, v242
v_mul_f32 v67, v67, v242
mm v[52:55],a[116:119],v[228:231],0
ex v214,v214
v_mul_f32 v68, v68, v242
v_mul_f32 v69, v69, v242
v_mul_f32 v70, v70, v242
mm v[20:23],a[116:119],v[224:227],0
rd a[88:91],v248 offset:6144
cv v234,v212,v213
st v[148:151],v245,s[44:47],s91 offen offset:1024 sc1
v_mul_f32 v71, v71, v242
mm v[24:27],a[120:123],v[224:227],0
ex v215,v215
cv v160,v64,v65
cv v161,v66,v67
cv v162,v68,v69
mm v[56:59],a[120:123],v[228:231],0
)ASM" R"ASM(rd a[92:95],v248 offset:7168
cv v235,v214,v215
cv v163,v70,v71
v_mul_f32 v72, v72, v242
v_mul_f32 v73, v73, v242
mm v[60:63],a[124:127],v[228:231],0
ex v220,v220
v_mul_f32 v74, v74, v242
v_mul_f32 v75, v75, v242
v_mul_f32 v76, v76, v242
mm v[28:31],a[124:127],v[224:227],0
s_add_u32 s85, s85, 1
v_mul_f32 v77, v77, v242
v_mul_f32 v78, v78, v242
v_mul_f32 v79, v79, v242
cv v164,v72,v73
wt lgkmcnt(4)
mm v[192:195],a[64:67],a[0:3],0
ex v221,v221
cv v165,v74,v75
cv v166,v76,v77
cv v167,v78,v79
mm v[192:195],a[68:71],a[4:7],v[192:195]
cv v238,v220,v221
s_add_u32 s89, s89, 16384
v_mul_f32 v80, v80, v242
v_mul_f32 v81, v81, v242
v_mul_f32 v82, v82, v242
mm v[192:195],a[72:75],a[8:11],v[192:195]
ex v222,v222
v_mul_f32 v83, v83, v242
v_mul_f32 v84, v84, v242
v_mul_f32 v85, v85, v242
mm v[192:195],a[76:79],a[12:15],v[192:195]
ld v255,s[60:63],s17 offen offset:0 sc1 lds
v_mul_f32 v86, v86, v242
v_mul_f32 v87, v87, v242
cv v168,v80,v81
mm v[200:203],a[76:79],a[28:31],0
ex v223,v223
cv v169,v82,v83
cv v170,v84,v85
cv v171,v86,v87
mm v[200:203],a[72:75],a[24:27],v[200:203]
cv v239,v222,v223
s_and_b32 s89, s89, 0x1ffff
v_mul_f32 v88, v88, v242
v_mul_f32 v89, v89, v242
v_mul_f32 v90, v90, v242
mm v[200:203],a[68:71],a[20:23],v[200:203]
ex v192,v192
v_mul_f32 v91, v91, v242
v_mul_f32 v92, v92, v242
v_mul_f32 v93, v93, v242
mm v[200:203],a[64:67],a[16:19],v[200:203]
wt lgkmcnt(0)
st v[152:155],v245,s[44:47],s91 offen offset:2048 sc1
v_mul_f32 v94, v94, v242
v_mul_f32 v95, v95, v242
mm v[196:199],a[80:83],a[0:3],0
ex v193,v193
cv v172,v88,v89
cv v173,v90,v91
cv v174,v92,v93
mm v[196:199],a[84:87],a[4:7],v[196:199]
cv v224,v192,v193
cv v175,v94,v95
v_mul_f32 v96, v96, v243
v_mul_f32 v97, v97, v243
v_mul_f32 v98, v98, v243
mm v[196:199],a[88:91],a[8:11],v[196:199]
ex v194,v194
v_mul_f32 v99, v99, v243
v_mul_f32 v100, v100, v243
v_mul_f32 v101, v101, v243
mm v[196:199],a[92:95],a[12:15],v[196:199]
ld v255,s[60:63],s17 offen offset:1024 sc1 lds
v_mul_f32 v102, v102, v243
v_mul_f32 v103, v103, v243
cv v176,v96,v97
mm v[204:207],a[92:95],a[28:31],0
ex v195,v195
cv v177,v98,v99
cv v178,v100,v101
cv v179,v102,v103
mm v[204:207],a[88:91],a[24:27],v[204:207]
cv v225,v194,v195
st v[156:159],v245,s[44:47],s91 offen offset:3072 sc1
v_mul_f32 v104, v104, v243
v_mul_f32 v105, v105, v243
mm v[204:207],a[84:87],a[20:23],v[204:207]
ex v200,v200
v_mul_f32 v106, v106, v243
v_mul_f32 v107, v107, v243
v_mul_f32 v108, v108, v243
mm v[204:207],a[80:83],a[16:19],v[204:207]
ld v255,s[60:63],s17 offen offset:2048 sc1 lds
v_mul_f32 v109, v109, v243
v_mul_f32 v110, v110, v243
v_mul_f32 v111, v111, v243
)ASM" R"ASM(mm v[208:211],a[64:67],a[32:35],0
ex v201,v201
cv v180,v104,v105
cv v181,v106,v107
cv v182,v108,v109
mm v[208:211],a[68:71],a[36:39],v[208:211]
cv v228,v200,v201
cv v183,v110,v111
v_mul_f32 v112, v112, v243
v_mul_f32 v113, v113, v243
v_mul_f32 v114, v114, v243
mm v[208:211],a[72:75],a[40:43],v[208:211]
ex v202,v202
v_mul_f32 v115, v115, v243
v_mul_f32 v116, v116, v243
v_mul_f32 v117, v117, v243
mm v[208:211],a[76:79],a[44:47],v[208:211]
ld v255,s[60:63],s17 offen offset:3072 sc1 lds
v_mul_f32 v118, v118, v243
v_mul_f32 v119, v119, v243
cv v184,v112,v113
mm v[216:219],a[76:79],a[60:63],0
ex v203,v203
cv v185,v114,v115
cv v186,v116,v117
cv v187,v118,v119
mm v[216:219],a[72:75],a[56:59],v[216:219]
cv v229,v202,v203
v_mul_f32 v120, v120, v243
v_mul_f32 v121, v121, v243
v_mul_f32 v122, v122, v243
v_mul_f32 v123, v123, v243
mm v[216:219],a[68:71],a[52:55],v[216:219]
ex v196,v196
v_mul_f32 v124, v124, v243
v_mul_f32 v125, v125, v243
v_mul_f32 v126, v126, v243
mm v[216:219],a[64:67],a[48:51],v[216:219]
v_mul_f32 v127, v127, v243
cv v188,v120,v121
cv v189,v122,v123
cv v190,v124,v125
cv v191,v126,v127
mm v[212:215],a[80:83],a[32:35],0
ex v197,v197
s_add_u32 s17, s17, s80
mm v[212:215],a[84:87],a[36:39],v[212:215]
cv v226,v196,v197
s_add_u32 m0, m0, 16384
s_and_b32 m0, m0, s78
mm v[212:215],a[88:91],a[40:43],v[212:215]
ex v198,v198
mm v[212:215],a[92:95],a[44:47],v[212:215]
st v[160:163],v246,s[44:47],s91 offen offset:0 sc1
mm v[220:223],a[92:95],a[60:63],0
ex v199,v199
mm v[220:223],a[88:91],a[56:59],v[220:223]
cv v227,v198,v199
mm v[220:223],a[84:87],a[52:55],v[220:223]
ex v204,v204
mm v[220:223],a[80:83],a[48:51],v[220:223]
wt vmcnt(18)
s_barrier
mm v[64:67],a[96:99],v[232:235],0
ex v205,v205
mm v[96:99],a[96:99],v[236:239],0
rd a[96:99],v248 offset:8192
cv v230,v204,v205
mm v[100:103],a[100:103],v[236:239],0
ex v206,v206
mm v[68:71],a[100:103],v[232:235],0
rd a[100:103],v248 offset:9216
st v[164:167],v246,s[44:47],s91 offen offset:1024 sc1
mm v[72:75],a[104:107],v[232:235],0
ex v207,v207
mm v[104:107],a[104:107],v[236:239],0
rd a[104:107],v248 offset:10240
cv v231,v206,v207
mm v[108:111],a[108:111],v[236:239],0
ex v208,v208
mm v[76:79],a[108:111],v[232:235],0
rd a[108:111],v248 offset:11264
st v[168:171],v246,s[44:47],s91 offen offset:2048 sc1
mm v[80:83],a[112:115],v[232:235],0
ex v209,v209
mm v[112:115],a[112:115],v[236:239],0
rd a[112:115],v248 offset:12288
mm v[116:119],a[116:119],v[236:239],0
ex v210,v210
mm v[84:87],a[116:119],v[232:235],0
rd a[116:119],v248 offset:13312
st v[172:175],v246,s[44:47],s91 offen offset:3072 sc1
mm v[88:91],a[120:123],v[232:235],0
ex v211,v211
mm v[120:123],a[120:123],v[236:239],0
rd a[120:123],v248 offset:14336
mm v[124:127],a[124:127],v[236:239],0
ex v216,v216
mm v[92:95],a[124:127],v[232:235],0
rd a[124:127],v248 offset:15360
)ASM" R"ASM(v_or_b32 v248, s89, v255
st v[176:179],v247,s[44:47],s91 offen offset:0 sc1
v_smfmac_f32_16x16x64_bf16 a[196:199], a[208:211], v[232:239], v241
wt lgkmcnt(4)
v_smfmac_f32_16x16x64_bf16 a[192:195], a[208:211], v[224:231], v241
rd a[64:67],v248 offset:0
mm v[0:3],a[96:99],v[224:227],v[0:3]
ex v217,v217
mm v[32:35],a[96:99],v[228:231],v[32:35]
rd a[68:71],v248 offset:1024
cv v232,v208,v209
mm v[36:39],a[100:103],v[228:231],v[36:39]
ex v218,v218
mm v[4:7],a[100:103],v[224:227],v[4:7]
rd a[72:75],v248 offset:2048
cv v233,v210,v211
st v[180:183],v247,s[44:47],s91 offen offset:1024 sc1
mm v[8:11],a[104:107],v[224:227],v[8:11]
ex v219,v219
mm v[40:43],a[104:107],v[228:231],v[40:43]
rd a[76:79],v248 offset:3072
cv v236,v216,v217
mm v[44:47],a[108:111],v[228:231],v[44:47]
ex v212,v212
mm v[12:15],a[108:111],v[224:227],v[12:15]
rd a[80:83],v248 offset:4096
wt lgkmcnt(5)
mm v[16:19],a[112:115],v[224:227],v[16:19]
ex v213,v213
mm v[48:51],a[112:115],v[228:231],v[48:51]
rd a[84:87],v248 offset:5120
cv v237,v218,v219
st v[184:187],v247,s[44:47],s91 offen offset:2048 sc1
mm v[52:55],a[116:119],v[228:231],v[52:55]
ex v214,v214
mm v[20:23],a[116:119],v[224:227],v[20:23]
rd a[88:91],v248 offset:6144
cv v234,v212,v213
mm v[24:27],a[120:123],v[224:227],v[24:27]
ex v215,v215
mm v[56:59],a[120:123],v[228:231],v[56:59]
rd a[92:95],v248 offset:7168
cv v235,v214,v215
st v[188:191],v247,s[44:47],s91 offen offset:3072 sc1
mm v[60:63],a[124:127],v[228:231],v[60:63]
ex v220,v220
mm v[28:31],a[124:127],v[224:227],v[28:31]
s_add_u32 s85, s85, 1
s_cmp_eq_u32 s85, 22
s_cbranch_scc1 .La5_LA_F3
.p2align 6
.La5_LA_F2:
wt lgkmcnt(4)
mm v[192:195],a[64:67],a[0:3],0
ex v221,v221
mm v[192:195],a[68:71],a[4:7],v[192:195]
cv v238,v220,v221
s_add_u32 s85, s85, 1
mm v[192:195],a[72:75],a[8:11],v[192:195]
ex v222,v222
mm v[192:195],a[76:79],a[12:15],v[192:195]
ld v255,s[60:63],s17 offen offset:0 sc1 lds
mm v[200:203],a[76:79],a[28:31],0
ex v223,v223
mm v[200:203],a[72:75],a[24:27],v[200:203]
cv v239,v222,v223
s_add_u32 s89, s89, 16384
mm v[200:203],a[68:71],a[20:23],v[200:203]
ex v192,v192
mm v[200:203],a[64:67],a[16:19],v[200:203]
wt lgkmcnt(0)
s_and_b32 s89, s89, 0x1ffff
mm v[196:199],a[80:83],a[0:3],0
ex v193,v193
mm v[196:199],a[84:87],a[4:7],v[196:199]
cv v224,v192,v193
mm v[196:199],a[88:91],a[8:11],v[196:199]
ex v194,v194
mm v[196:199],a[92:95],a[12:15],v[196:199]
ld v255,s[60:63],s17 offen offset:1024 sc1 lds
mm v[204:207],a[92:95],a[28:31],0
ex v195,v195
mm v[204:207],a[88:91],a[24:27],v[204:207]
cv v225,v194,v195
mm v[204:207],a[84:87],a[20:23],v[204:207]
ex v200,v200
mm v[204:207],a[80:83],a[16:19],v[204:207]
ld v255,s[60:63],s17 offen offset:2048 sc1 lds
mm v[208:211],a[64:67],a[32:35],0
ex v201,v201
mm v[208:211],a[68:71],a[36:39],v[208:211]
cv v228,v200,v201
mm v[208:211],a[72:75],a[40:43],v[208:211]
ex v202,v202
mm v[208:211],a[76:79],a[44:47],v[208:211]
ld v255,s[60:63],s17 offen offset:3072 sc1 lds
mm v[216:219],a[76:79],a[60:63],0
ex v203,v203
mm v[216:219],a[72:75],a[56:59],v[216:219]
cv v229,v202,v203
)ASM" R"ASM(mm v[216:219],a[68:71],a[52:55],v[216:219]
ex v196,v196
mm v[216:219],a[64:67],a[48:51],v[216:219]
buffer_load_dword a253, v254, s[36:39], s90 offen
mm v[212:215],a[80:83],a[32:35],0
ex v197,v197
mm v[212:215],a[84:87],a[36:39],v[212:215]
cv v226,v196,v197
s_add_u32 s90, s90, s21
mm v[212:215],a[88:91],a[40:43],v[212:215]
ex v198,v198
mm v[212:215],a[92:95],a[44:47],v[212:215]
s_add_u32 s17, s17, s80
s_add_u32 m0, m0, 16384
mm v[220:223],a[92:95],a[60:63],0
ex v199,v199
mm v[220:223],a[88:91],a[56:59],v[220:223]
cv v227,v198,v199
s_and_b32 m0, m0, s78
mm v[220:223],a[84:87],a[52:55],v[220:223]
ex v204,v204
mm v[220:223],a[80:83],a[48:51],v[220:223]
wt vmcnt(11)
s_barrier
mm v[64:67],a[96:99],v[232:235],v[64:67]
ex v205,v205
mm v[96:99],a[96:99],v[236:239],v[96:99]
rd a[96:99],v248 offset:8192
cv v230,v204,v205
mm v[100:103],a[100:103],v[236:239],v[100:103]
ex v206,v206
mm v[68:71],a[100:103],v[232:235],v[68:71]
rd a[100:103],v248 offset:9216
mm v[72:75],a[104:107],v[232:235],v[72:75]
ex v207,v207
mm v[104:107],a[104:107],v[236:239],v[104:107]
rd a[104:107],v248 offset:10240
cv v231,v206,v207
mm v[108:111],a[108:111],v[236:239],v[108:111]
ex v208,v208
mm v[76:79],a[108:111],v[232:235],v[76:79]
rd a[108:111],v248 offset:11264
mm v[80:83],a[112:115],v[232:235],v[80:83]
ex v209,v209
mm v[112:115],a[112:115],v[236:239],v[112:115]
rd a[112:115],v248 offset:12288
mm v[116:119],a[116:119],v[236:239],v[116:119]
ex v210,v210
mm v[84:87],a[116:119],v[232:235],v[84:87]
rd a[116:119],v248 offset:13312
mm v[88:91],a[120:123],v[232:235],v[88:91]
ex v211,v211
mm v[120:123],a[120:123],v[236:239],v[120:123]
rd a[120:123],v248 offset:14336
s_bitcmp1_b64 s[98:99], s85
mm v[124:127],a[124:127],v[236:239],v[124:127]
ex v216,v216
mm v[92:95],a[124:127],v[232:235],v[92:95]
rd a[124:127],v248 offset:15360
v_or_b32 v248, s89, v255
v_smfmac_f32_16x16x64_bf16 a[196:199], a[208:211], v[232:239], v241
wt lgkmcnt(4)
v_smfmac_f32_16x16x64_bf16 a[192:195], a[208:211], v[224:231], v241
rd a[64:67],v248 offset:0
mm v[0:3],a[96:99],v[224:227],v[0:3]
ex v217,v217
mm v[32:35],a[96:99],v[228:231],v[32:35]
rd a[68:71],v248 offset:1024
cv v232,v208,v209
mm v[36:39],a[100:103],v[228:231],v[36:39]
ex v218,v218
mm v[4:7],a[100:103],v[224:227],v[4:7]
rd a[72:75],v248 offset:2048
cv v233,v210,v211
mm v[8:11],a[104:107],v[224:227],v[8:11]
ex v219,v219
mm v[40:43],a[104:107],v[228:231],v[40:43]
rd a[76:79],v248 offset:3072
cv v236,v216,v217
mm v[44:47],a[108:111],v[228:231],v[44:47]
ex v212,v212
mm v[12:15],a[108:111],v[224:227],v[12:15]
rd a[80:83],v248 offset:4096
wt lgkmcnt(5)
mm v[16:19],a[112:115],v[224:227],v[16:19]
ex v213,v213
mm v[48:51],a[112:115],v[228:231],v[48:51]
rd a[84:87],v248 offset:5120
cv v237,v218,v219
mm v[52:55],a[116:119],v[228:231],v[52:55]
ex v214,v214
mm v[20:23],a[116:119],v[224:227],v[20:23]
rd a[88:91],v248 offset:6144
cv v234,v212,v213
mm v[24:27],a[120:123],v[224:227],v[24:27]
ex v215,v215
mm v[56:59],a[120:123],v[228:231],v[56:59]
rd a[92:95],v248 offset:7168
cv v235,v214,v215
mm v[60:63],a[124:127],v[228:231],v[60:63]
)ASM" R"ASM(ex v220,v220
mm v[28:31],a[124:127],v[224:227],v[28:31]
s_cbranch_scc1 .La5_LA_F2E2
wt lgkmcnt(4)
mm v[192:195],a[64:67],a[0:3],0
ex v221,v221
mm v[192:195],a[68:71],a[4:7],v[192:195]
cv v238,v220,v221
s_add_u32 s85, s85, 1
mm v[192:195],a[72:75],a[8:11],v[192:195]
ex v222,v222
mm v[192:195],a[76:79],a[12:15],v[192:195]
ld v255,s[60:63],s17 offen offset:0 sc1 lds
mm v[200:203],a[76:79],a[28:31],0
ex v223,v223
mm v[200:203],a[72:75],a[24:27],v[200:203]
cv v239,v222,v223
s_add_u32 s89, s89, 16384
mm v[200:203],a[68:71],a[20:23],v[200:203]
ex v192,v192
mm v[200:203],a[64:67],a[16:19],v[200:203]
wt lgkmcnt(0)
s_and_b32 s89, s89, 0x1ffff
mm v[196:199],a[80:83],a[0:3],0
ex v193,v193
mm v[196:199],a[84:87],a[4:7],v[196:199]
cv v224,v192,v193
mm v[196:199],a[88:91],a[8:11],v[196:199]
ex v194,v194
mm v[196:199],a[92:95],a[12:15],v[196:199]
ld v255,s[60:63],s17 offen offset:1024 sc1 lds
mm v[204:207],a[92:95],a[28:31],0
ex v195,v195
mm v[204:207],a[88:91],a[24:27],v[204:207]
cv v225,v194,v195
mm v[204:207],a[84:87],a[20:23],v[204:207]
ex v200,v200
mm v[204:207],a[80:83],a[16:19],v[204:207]
ld v255,s[60:63],s17 offen offset:2048 sc1 lds
mm v[208:211],a[64:67],a[32:35],0
ex v201,v201
mm v[208:211],a[68:71],a[36:39],v[208:211]
cv v228,v200,v201
mm v[208:211],a[72:75],a[40:43],v[208:211]
ex v202,v202
mm v[208:211],a[76:79],a[44:47],v[208:211]
ld v255,s[60:63],s17 offen offset:3072 sc1 lds
mm v[216:219],a[76:79],a[60:63],0
ex v203,v203
mm v[216:219],a[72:75],a[56:59],v[216:219]
cv v229,v202,v203
mm v[216:219],a[68:71],a[52:55],v[216:219]
ex v196,v196
mm v[216:219],a[64:67],a[48:51],v[216:219]
buffer_load_dword a253, v254, s[36:39], s90 offen
mm v[212:215],a[80:83],a[32:35],0
ex v197,v197
mm v[212:215],a[84:87],a[36:39],v[212:215]
cv v226,v196,v197
s_add_u32 s90, s90, s21
mm v[212:215],a[88:91],a[40:43],v[212:215]
ex v198,v198
mm v[212:215],a[92:95],a[44:47],v[212:215]
s_add_u32 s17, s17, s80
s_add_u32 m0, m0, 16384
mm v[220:223],a[92:95],a[60:63],0
ex v199,v199
mm v[220:223],a[88:91],a[56:59],v[220:223]
cv v227,v198,v199
.La5_LW6_2:
s_cbranch_execz .La5_LW6_6
s_and_b32 m0, m0, s78
mm v[220:223],a[84:87],a[52:55],v[220:223]
ex v204,v204
mm v[220:223],a[80:83],a[48:51],v[220:223]
wt vmcnt(11)
s_barrier
mm v[64:67],a[96:99],v[232:235],v[64:67]
ex v205,v205
mm v[96:99],a[96:99],v[236:239],v[96:99]
rd a[96:99],v248 offset:8192
cv v230,v204,v205
mm v[100:103],a[100:103],v[236:239],v[100:103]
ex v206,v206
mm v[68:71],a[100:103],v[232:235],v[68:71]
rd a[100:103],v248 offset:9216
mm v[72:75],a[104:107],v[232:235],v[72:75]
ex v207,v207
mm v[104:107],a[104:107],v[236:239],v[104:107]
rd a[104:107],v248 offset:10240
cv v231,v206,v207
mm v[108:111],a[108:111],v[236:239],v[108:111]
ex v208,v208
mm v[76:79],a[108:111],v[232:235],v[76:79]
rd a[108:111],v248 offset:11264
mm v[80:83],a[112:115],v[232:235],v[80:83]
ex v209,v209
mm v[112:115],a[112:115],v[236:239],v[112:115]
rd a[112:115],v248 offset:12288
mm v[116:119],a[116:119],v[236:239],v[116:119]
)ASM" R"ASM(ex v210,v210
mm v[84:87],a[116:119],v[232:235],v[84:87]
rd a[116:119],v248 offset:13312
mm v[88:91],a[120:123],v[232:235],v[88:91]
ex v211,v211
mm v[120:123],a[120:123],v[236:239],v[120:123]
rd a[120:123],v248 offset:14336
s_bitcmp1_b64 s[98:99], s85
mm v[124:127],a[124:127],v[236:239],v[124:127]
ex v216,v216
mm v[92:95],a[124:127],v[232:235],v[92:95]
rd a[124:127],v248 offset:15360
v_or_b32 v248, s89, v255
v_smfmac_f32_16x16x64_bf16 a[196:199], a[208:211], v[232:239], v241
wt lgkmcnt(4)
v_smfmac_f32_16x16x64_bf16 a[192:195], a[208:211], v[224:231], v241
rd a[64:67],v248 offset:0
mm v[0:3],a[96:99],v[224:227],v[0:3]
ex v217,v217
mm v[32:35],a[96:99],v[228:231],v[32:35]
rd a[68:71],v248 offset:1024
cv v232,v208,v209
mm v[36:39],a[100:103],v[228:231],v[36:39]
ex v218,v218
mm v[4:7],a[100:103],v[224:227],v[4:7]
rd a[72:75],v248 offset:2048
cv v233,v210,v211
mm v[8:11],a[104:107],v[224:227],v[8:11]
ex v219,v219
mm v[40:43],a[104:107],v[228:231],v[40:43]
rd a[76:79],v248 offset:3072
cv v236,v216,v217
mm v[44:47],a[108:111],v[228:231],v[44:47]
ex v212,v212
mm v[12:15],a[108:111],v[224:227],v[12:15]
rd a[80:83],v248 offset:4096
wt lgkmcnt(5)
mm v[16:19],a[112:115],v[224:227],v[16:19]
ex v213,v213
mm v[48:51],a[112:115],v[228:231],v[48:51]
rd a[84:87],v248 offset:5120
cv v237,v218,v219
mm v[52:55],a[116:119],v[228:231],v[52:55]
ex v214,v214
mm v[20:23],a[116:119],v[224:227],v[20:23]
rd a[88:91],v248 offset:6144
cv v234,v212,v213
mm v[24:27],a[120:123],v[224:227],v[24:27]
ex v215,v215
mm v[56:59],a[120:123],v[228:231],v[56:59]
rd a[92:95],v248 offset:7168
cv v235,v214,v215
mm v[60:63],a[124:127],v[228:231],v[60:63]
ex v220,v220
mm v[28:31],a[124:127],v[224:227],v[28:31]
s_cbranch_scc0 .La5_LA_F2
.La5_LA_F2E2:
; ---- helper: last windows; FFN weights and the first weights of the O projection come in
.La5_LA_F3:
.La5_LW6_3:
s_cbranch_execz .La5_LW6_7
wt lgkmcnt(4)
mm v[192:195],a[64:67],a[0:3],0
ex v221,v221
mm v[192:195],a[68:71],a[4:7],v[192:195]
cv v238,v220,v221
s_add_u32 s89, s89, 16384
mm v[192:195],a[72:75],a[8:11],v[192:195]
ex v222,v222
mm v[192:195],a[76:79],a[12:15],v[192:195]
ld v255,s[60:63],s17 offen offset:0 sc1 lds
mm v[200:203],a[76:79],a[28:31],0
ex v223,v223
mm v[200:203],a[72:75],a[24:27],v[200:203]
cv v239,v222,v223
s_and_b32 s89, s89, 0x1ffff
mm v[200:203],a[68:71],a[20:23],v[200:203]
ex v192,v192
mm v[200:203],a[64:67],a[16:19],v[200:203]
wt lgkmcnt(0)
mm v[196:199],a[80:83],a[0:3],0
ex v193,v193
mm v[196:199],a[84:87],a[4:7],v[196:199]
cv v224,v192,v193
ar v243,a229
mm v[196:199],a[88:91],a[8:11],v[196:199]
ex v194,v194
mm v[196:199],a[92:95],a[12:15],v[196:199]
ld v255,s[60:63],s17 offen offset:1024 sc1 lds
mm v[204:207],a[92:95],a[28:31],0
ex v195,v195
mm v[204:207],a[88:91],a[24:27],v[204:207]
cv v225,v194,v195
mm v[204:207],a[84:87],a[20:23],v[204:207]
ex v200,v200
mm v[204:207],a[80:83],a[16:19],v[204:207]
ld v255,s[60:63],s17 offen offset:2048 sc1 lds
mm v[208:211],a[64:67],a[32:35],0
ex v201,v201
mm v[208:211],a[68:71],a[36:39],v[208:211]
)ASM" R"ASM(cv v228,v200,v201
mm v[208:211],a[72:75],a[40:43],v[208:211]
ex v202,v202
mm v[208:211],a[76:79],a[44:47],v[208:211]
ld v255,s[60:63],s17 offen offset:3072 sc1 lds
mm v[216:219],a[76:79],a[60:63],0
ex v203,v203
mm v[216:219],a[72:75],a[56:59],v[216:219]
cv v229,v202,v203
s_mov_b32 s93, 0
mm v[216:219],a[68:71],a[52:55],v[216:219]
ex v196,v196
mm v[216:219],a[64:67],a[48:51],v[216:219]
ld a[128:131],v243,s[40:43],s93 offen
mm v[212:215],a[80:83],a[32:35],0
ex v197,v197
s_add_u32 s17, s17, s80
mm v[212:215],a[84:87],a[36:39],v[212:215]
cv v226,v196,v197
s_mov_b32 s93, 7168
s_add_u32 m0, m0, 16384
mm v[212:215],a[88:91],a[40:43],v[212:215]
ex v198,v198
s_and_b32 m0, m0, s78
mm v[212:215],a[92:95],a[44:47],v[212:215]
ld a[132:135],v243,s[40:43],s93 offen
mm v[220:223],a[92:95],a[60:63],0
ex v199,v199
mm v[220:223],a[88:91],a[56:59],v[220:223]
cv v227,v198,v199
mm v[220:223],a[84:87],a[52:55],v[220:223]
ex v204,v204
mm v[220:223],a[80:83],a[48:51],v[220:223]
wt vmcnt(12)
s_barrier
mm v[64:67],a[96:99],v[232:235],v[64:67]
ex v205,v205
mm v[96:99],a[96:99],v[236:239],v[96:99]
rd a[96:99],v248 offset:8192
cv v230,v204,v205
mm v[100:103],a[100:103],v[236:239],v[100:103]
ex v206,v206
mm v[68:71],a[100:103],v[232:235],v[68:71]
rd a[100:103],v248 offset:9216
mm v[72:75],a[104:107],v[232:235],v[72:75]
ex v207,v207
mm v[104:107],a[104:107],v[236:239],v[104:107]
rd a[104:107],v248 offset:10240
cv v231,v206,v207
s_mov_b32 s93, 14336
mm v[108:111],a[108:111],v[236:239],v[108:111]
ex v208,v208
mm v[76:79],a[108:111],v[232:235],v[76:79]
rd a[108:111],v248 offset:11264
ld a[136:139],v243,s[40:43],s93 offen
mm v[80:83],a[112:115],v[232:235],v[80:83]
ex v209,v209
mm v[112:115],a[112:115],v[236:239],v[112:115]
rd a[112:115],v248 offset:12288
mm v[116:119],a[116:119],v[236:239],v[116:119]
ex v210,v210
mm v[84:87],a[116:119],v[232:235],v[84:87]
rd a[116:119],v248 offset:13312
mm v[88:91],a[120:123],v[232:235],v[88:91]
ex v211,v211
mm v[120:123],a[120:123],v[236:239],v[120:123]
rd a[120:123],v248 offset:14336
mm v[124:127],a[124:127],v[236:239],v[124:127]
ex v216,v216
mm v[92:95],a[124:127],v[232:235],v[92:95]
rd a[124:127],v248 offset:15360
v_or_b32 v248, s89, v255
v_smfmac_f32_16x16x64_bf16 a[196:199], a[208:211], v[232:239], v241
wt lgkmcnt(4)
v_smfmac_f32_16x16x64_bf16 a[192:195], a[208:211], v[224:231], v241
rd a[64:67],v248 offset:0
mm v[0:3],a[96:99],v[224:227],v[0:3]
ex v217,v217
mm v[32:35],a[96:99],v[228:231],v[32:35]
rd a[68:71],v248 offset:1024
cv v232,v208,v209
mm v[36:39],a[100:103],v[228:231],v[36:39]
ex v218,v218
mm v[4:7],a[100:103],v[224:227],v[4:7]
rd a[72:75],v248 offset:2048
cv v233,v210,v211
mm v[8:11],a[104:107],v[224:227],v[8:11]
ex v219,v219
mm v[40:43],a[104:107],v[228:231],v[40:43]
rd a[76:79],v248 offset:3072
cv v236,v216,v217
mm v[44:47],a[108:111],v[228:231],v[44:47]
ex v212,v212
mm v[12:15],a[108:111],v[224:227],v[12:15]
rd a[80:83],v248 offset:4096
wt lgkmcnt(5)
mm v[16:19],a[112:115],v[224:227],v[16:19]
ex v213,v213
mm v[48:51],a[112:115],v[228:231],v[48:51]
rd a[84:87],v248 offset:5120
)ASM" R"ASM(cv v237,v218,v219
mm v[52:55],a[116:119],v[228:231],v[52:55]
ex v214,v214
mm v[20:23],a[116:119],v[224:227],v[20:23]
rd a[88:91],v248 offset:6144
cv v234,v212,v213
mm v[24:27],a[120:123],v[224:227],v[24:27]
ex v215,v215
mm v[56:59],a[120:123],v[228:231],v[56:59]
rd a[92:95],v248 offset:7168
cv v235,v214,v215
mm v[60:63],a[124:127],v[228:231],v[60:63]
ex v220,v220
mm v[28:31],a[124:127],v[224:227],v[28:31]
wt lgkmcnt(4)
mm v[192:195],a[64:67],a[0:3],0
ex v221,v221
mm v[192:195],a[68:71],a[4:7],v[192:195]
cv v238,v220,v221
s_add_u32 s89, s89, 16384
mm v[192:195],a[72:75],a[8:11],v[192:195]
ex v222,v222
mm v[192:195],a[76:79],a[12:15],v[192:195]
ld v255,s[60:63],s17 offen offset:0 sc1 lds
mm v[200:203],a[76:79],a[28:31],0
ex v223,v223
mm v[200:203],a[72:75],a[24:27],v[200:203]
cv v239,v222,v223
s_and_b32 s89, s89, 0x1ffff
mm v[200:203],a[68:71],a[20:23],v[200:203]
ex v192,v192
mm v[200:203],a[64:67],a[16:19],v[200:203]
wt lgkmcnt(0)
mm v[196:199],a[80:83],a[0:3],0
ex v193,v193
mm v[196:199],a[84:87],a[4:7],v[196:199]
cv v224,v192,v193
mm v[196:199],a[88:91],a[8:11],v[196:199]
ex v194,v194
mm v[196:199],a[92:95],a[12:15],v[196:199]
ld v255,s[60:63],s17 offen offset:1024 sc1 lds
mm v[204:207],a[92:95],a[28:31],0
ex v195,v195
mm v[204:207],a[88:91],a[24:27],v[204:207]
cv v225,v194,v195
mm v[204:207],a[84:87],a[20:23],v[204:207]
ex v200,v200
mm v[204:207],a[80:83],a[16:19],v[204:207]
ld v255,s[60:63],s17 offen offset:2048 sc1 lds
mm v[208:211],a[64:67],a[32:35],0
ex v201,v201
mm v[208:211],a[68:71],a[36:39],v[208:211]
cv v228,v200,v201
mm v[208:211],a[72:75],a[40:43],v[208:211]
ex v202,v202
mm v[208:211],a[76:79],a[44:47],v[208:211]
ld v255,s[60:63],s17 offen offset:3072 sc1 lds
mm v[216:219],a[76:79],a[60:63],0
ex v203,v203
mm v[216:219],a[72:75],a[56:59],v[216:219]
cv v229,v202,v203
s_mov_b32 s93, 21504
mm v[216:219],a[68:71],a[52:55],v[216:219]
ex v196,v196
mm v[216:219],a[64:67],a[48:51],v[216:219]
ld a[140:143],v243,s[40:43],s93 offen
mm v[212:215],a[80:83],a[32:35],0
ex v197,v197
s_add_u32 s17, s17, s80
mm v[212:215],a[84:87],a[36:39],v[212:215]
cv v226,v196,v197
s_mov_b32 s93, 28672
s_add_u32 m0, m0, 16384
mm v[212:215],a[88:91],a[40:43],v[212:215]
ex v198,v198
s_and_b32 m0, m0, s78
mm v[212:215],a[92:95],a[44:47],v[212:215]
ld a[144:147],v243,s[40:43],s93 offen
mm v[220:223],a[92:95],a[60:63],0
ex v199,v199
mm v[220:223],a[88:91],a[56:59],v[220:223]
cv v227,v198,v199
mm v[220:223],a[84:87],a[52:55],v[220:223]
ex v204,v204
mm v[220:223],a[80:83],a[48:51],v[220:223]
wt vmcnt(14)
s_barrier
mm v[64:67],a[96:99],v[232:235],v[64:67]
ex v205,v205
mm v[96:99],a[96:99],v[236:239],v[96:99]
rd a[96:99],v248 offset:8192
cv v230,v204,v205
mm v[100:103],a[100:103],v[236:239],v[100:103]
ex v206,v206
mm v[68:71],a[100:103],v[232:235],v[68:71]
rd a[100:103],v248 offset:9216
mm v[72:75],a[104:107],v[232:235],v[72:75]
ex v207,v207
mm v[104:107],a[104:107],v[236:239],v[104:107]
rd a[104:107],v248 offset:10240
)ASM" R"ASM(cv v231,v206,v207
s_mov_b32 s93, 35840
mm v[108:111],a[108:111],v[236:239],v[108:111]
ex v208,v208
mm v[76:79],a[108:111],v[232:235],v[76:79]
rd a[108:111],v248 offset:11264
ld a[148:151],v243,s[40:43],s93 offen
mm v[80:83],a[112:115],v[232:235],v[80:83]
ex v209,v209
mm v[112:115],a[112:115],v[236:239],v[112:115]
rd a[112:115],v248 offset:12288
mm v[116:119],a[116:119],v[236:239],v[116:119]
ex v210,v210
mm v[84:87],a[116:119],v[232:235],v[84:87]
rd a[116:119],v248 offset:13312
mm v[88:91],a[120:123],v[232:235],v[88:91]
ex v211,v211
mm v[120:123],a[120:123],v[236:239],v[120:123]
rd a[120:123],v248 offset:14336
ar v242,a231
wt vmcnt(14)
mm v[124:127],a[124:127],v[236:239],v[124:127]
ex v216,v216
mm v[92:95],a[124:127],v[232:235],v[92:95]
rd a[124:127],v248 offset:15360
v_or_b32 v248, s89, v255
v_smfmac_f32_16x16x64_bf16 a[196:199], a[208:211], v[232:239], v241
wt lgkmcnt(4)
v_smfmac_f32_16x16x64_bf16 a[192:195], a[208:211], v[224:231], v241
rd a[64:67],v248 offset:0
mm v[0:3],a[96:99],v[224:227],v[0:3]
ex v217,v217
mm v[32:35],a[96:99],v[228:231],v[32:35]
rd a[68:71],v248 offset:1024
cv v232,v208,v209
mm v[36:39],a[100:103],v[228:231],v[36:39]
ex v218,v218
mm v[4:7],a[100:103],v[224:227],v[4:7]
rd a[72:75],v248 offset:2048
cv v233,v210,v211
mm v[8:11],a[104:107],v[224:227],v[8:11]
ex v219,v219
mm v[40:43],a[104:107],v[228:231],v[40:43]
rd a[76:79],v248 offset:3072
cv v236,v216,v217
mm v[44:47],a[108:111],v[228:231],v[44:47]
ex v212,v212
mm v[12:15],a[108:111],v[224:227],v[12:15]
rd a[80:83],v248 offset:4096
wt lgkmcnt(5)
mm v[16:19],a[112:115],v[224:227],v[16:19]
ex v213,v213
mm v[48:51],a[112:115],v[228:231],v[48:51]
rd a[84:87],v248 offset:5120
cv v237,v218,v219
mm v[52:55],a[116:119],v[228:231],v[52:55]
ex v214,v214
mm v[20:23],a[116:119],v[224:227],v[20:23]
rd a[88:91],v248 offset:6144
cv v234,v212,v213
mm v[24:27],a[120:123],v[224:227],v[24:27]
ex v215,v215
mm v[56:59],a[120:123],v[228:231],v[56:59]
rd a[92:95],v248 offset:7168
cv v235,v214,v215
mm v[60:63],a[124:127],v[228:231],v[60:63]
ex v220,v220
mm v[28:31],a[124:127],v[224:227],v[28:31]
buffer_store_dwordx2 v[252:253], v242, s[64:67], s6 offen offset:0 sc1
wt lgkmcnt(4)
mm v[192:195],a[64:67],a[0:3],0
ex v221,v221
mm v[192:195],a[68:71],a[4:7],v[192:195]
cv v238,v220,v221
s_add_u32 s89, s89, 16384
mm v[192:195],a[72:75],a[8:11],v[192:195]
ex v222,v222
mm v[192:195],a[76:79],a[12:15],v[192:195]
ld v255,s[60:63],s17 offen offset:0 sc1 lds
mm v[200:203],a[76:79],a[28:31],0
ex v223,v223
mm v[200:203],a[72:75],a[24:27],v[200:203]
cv v239,v222,v223
s_and_b32 s89, s89, 0x1ffff
mm v[200:203],a[68:71],a[20:23],v[200:203]
ex v192,v192
mm v[200:203],a[64:67],a[16:19],v[200:203]
wt lgkmcnt(0)
mm v[196:199],a[80:83],a[0:3],0
ex v193,v193
mm v[196:199],a[84:87],a[4:7],v[196:199]
cv v224,v192,v193
mm v[196:199],a[88:91],a[8:11],v[196:199]
ex v194,v194
mm v[196:199],a[92:95],a[12:15],v[196:199]
ld v255,s[60:63],s17 offen offset:1024 sc1 lds
mm v[204:207],a[92:95],a[28:31],0
ex v195,v195
mm v[204:207],a[88:91],a[24:27],v[204:207]
cv v225,v194,v195
)ASM" R"ASM(mm v[204:207],a[84:87],a[20:23],v[204:207]
ex v200,v200
mm v[204:207],a[80:83],a[16:19],v[204:207]
ld v255,s[60:63],s17 offen offset:2048 sc1 lds
mm v[208:211],a[64:67],a[32:35],0
ex v201,v201
mm v[208:211],a[68:71],a[36:39],v[208:211]
cv v228,v200,v201
mm v[208:211],a[72:75],a[40:43],v[208:211]
ex v202,v202
mm v[208:211],a[76:79],a[44:47],v[208:211]
ld v255,s[60:63],s17 offen offset:3072 sc1 lds
mm v[216:219],a[76:79],a[60:63],0
ex v203,v203
mm v[216:219],a[72:75],a[56:59],v[216:219]
cv v229,v202,v203
s_mov_b32 s93, 43008
mm v[216:219],a[68:71],a[52:55],v[216:219]
ex v196,v196
mm v[216:219],a[64:67],a[48:51],v[216:219]
ld a[152:155],v243,s[40:43],s93 offen
mm v[212:215],a[80:83],a[32:35],0
ex v197,v197
s_add_u32 s17, s17, s80
mm v[212:215],a[84:87],a[36:39],v[212:215]
cv v226,v196,v197
s_mov_b32 s93, 50176
s_add_u32 m0, m0, 16384
mm v[212:215],a[88:91],a[40:43],v[212:215]
ex v198,v198
s_and_b32 m0, m0, s78
mm v[212:215],a[92:95],a[44:47],v[212:215]
ld a[156:159],v243,s[40:43],s93 offen
mm v[220:223],a[92:95],a[60:63],0
ex v199,v199
mm v[220:223],a[88:91],a[56:59],v[220:223]
cv v227,v198,v199
mm v[220:223],a[84:87],a[52:55],v[220:223]
ex v204,v204
mm v[220:223],a[80:83],a[48:51],v[220:223]
wt vmcnt(17)
s_barrier
mm v[64:67],a[96:99],v[232:235],v[64:67]
ex v205,v205
mm v[96:99],a[96:99],v[236:239],v[96:99]
rd a[96:99],v248 offset:8192
cv v230,v204,v205
mm v[100:103],a[100:103],v[236:239],v[100:103]
ex v206,v206
mm v[68:71],a[100:103],v[232:235],v[68:71]
rd a[100:103],v248 offset:9216
mm v[72:75],a[104:107],v[232:235],v[72:75]
ex v207,v207
mm v[104:107],a[104:107],v[236:239],v[104:107]
rd a[104:107],v248 offset:10240
cv v231,v206,v207
s_mov_b32 s93, 57344
mm v[108:111],a[108:111],v[236:239],v[108:111]
ex v208,v208
mm v[76:79],a[108:111],v[232:235],v[76:79]
rd a[108:111],v248 offset:11264
ld a[160:163],v243,s[40:43],s93 offen
mm v[80:83],a[112:115],v[232:235],v[80:83]
ex v209,v209
mm v[112:115],a[112:115],v[236:239],v[112:115]
rd a[112:115],v248 offset:12288
mm v[116:119],a[116:119],v[236:239],v[116:119]
ex v210,v210
mm v[84:87],a[116:119],v[232:235],v[84:87]
rd a[116:119],v248 offset:13312
mm v[88:91],a[120:123],v[232:235],v[88:91]
ex v211,v211
mm v[120:123],a[120:123],v[236:239],v[120:123]
rd a[120:123],v248 offset:14336
mm v[124:127],a[124:127],v[236:239],v[124:127]
ex v216,v216
mm v[92:95],a[124:127],v[232:235],v[92:95]
rd a[124:127],v248 offset:15360
v_or_b32 v248, s89, v255
v_smfmac_f32_16x16x64_bf16 a[196:199], a[208:211], v[232:239], v241
wt lgkmcnt(4)
v_smfmac_f32_16x16x64_bf16 a[192:195], a[208:211], v[224:231], v241
rd a[64:67],v248 offset:0
mm v[0:3],a[96:99],v[224:227],v[0:3]
ex v217,v217
mm v[32:35],a[96:99],v[228:231],v[32:35]
rd a[68:71],v248 offset:1024
cv v232,v208,v209
mm v[36:39],a[100:103],v[228:231],v[36:39]
ex v218,v218
mm v[4:7],a[100:103],v[224:227],v[4:7]
rd a[72:75],v248 offset:2048
cv v233,v210,v211
mm v[8:11],a[104:107],v[224:227],v[8:11]
ex v219,v219
mm v[40:43],a[104:107],v[228:231],v[40:43]
rd a[76:79],v248 offset:3072
cv v236,v216,v217
mm v[44:47],a[108:111],v[228:231],v[44:47]
ex v212,v212
)ASM" R"ASM(mm v[12:15],a[108:111],v[224:227],v[12:15]
rd a[80:83],v248 offset:4096
wt lgkmcnt(5)
mm v[16:19],a[112:115],v[224:227],v[16:19]
ex v213,v213
mm v[48:51],a[112:115],v[228:231],v[48:51]
rd a[84:87],v248 offset:5120
cv v237,v218,v219
mm v[52:55],a[116:119],v[228:231],v[52:55]
ex v214,v214
mm v[20:23],a[116:119],v[224:227],v[20:23]
rd a[88:91],v248 offset:6144
cv v234,v212,v213
mm v[24:27],a[120:123],v[224:227],v[24:27]
ex v215,v215
mm v[56:59],a[120:123],v[228:231],v[56:59]
rd a[92:95],v248 offset:7168
cv v235,v214,v215
mm v[60:63],a[124:127],v[228:231],v[60:63]
ex v220,v220
mm v[28:31],a[124:127],v[224:227],v[28:31]
wt lgkmcnt(4)
mm v[192:195],a[64:67],a[0:3],0
ex v221,v221
mm v[192:195],a[68:71],a[4:7],v[192:195]
cv v238,v220,v221
ar v240,a228
mm v[192:195],a[72:75],a[8:11],v[192:195]
ex v222,v222
mm v[192:195],a[76:79],a[12:15],v[192:195]
s_add_u32 m0, s83, 16896
s_lshl_b32 s17, s72, 18
mm v[200:203],a[76:79],a[28:31],0
ex v223,v223
mm v[200:203],a[72:75],a[24:27],v[200:203]
cv v239,v222,v223
s_add_u32 s89, s89, 16384
mm v[200:203],a[68:71],a[20:23],v[200:203]
ex v192,v192
mm v[200:203],a[64:67],a[16:19],v[200:203]
wt lgkmcnt(0)
s_and_b32 s89, s89, 0x1ffff
mm v[196:199],a[80:83],a[0:3],0
ex v193,v193
mm v[196:199],a[84:87],a[4:7],v[196:199]
cv v224,v192,v193
mm v[196:199],a[88:91],a[8:11],v[196:199]
ex v194,v194
mm v[196:199],a[92:95],a[12:15],v[196:199]
ld v240,s[56:59],s17 offen lds
mm v[204:207],a[92:95],a[28:31],0
ex v195,v195
s_add_u32 s17, s17, 32768
mm v[204:207],a[88:91],a[24:27],v[204:207]
cv v225,v194,v195
s_add_u32 m0, m0, 1024
mm v[204:207],a[84:87],a[20:23],v[204:207]
ex v200,v200
mm v[204:207],a[80:83],a[16:19],v[204:207]
ld v240,s[56:59],s17 offen lds
mm v[208:211],a[64:67],a[32:35],0
ex v201,v201
s_add_u32 s17, s17, 4294942720
mm v[208:211],a[68:71],a[36:39],v[208:211]
cv v228,v200,v201
s_add_u32 m0, m0, 1152
mm v[208:211],a[72:75],a[40:43],v[208:211]
ex v202,v202
.La5_LW6_4:
s_cbranch_execz .La5_LW6_R0
mm v[208:211],a[76:79],a[44:47],v[208:211]
ld v240,s[56:59],s17 offen lds
mm v[216:219],a[76:79],a[60:63],0
ex v203,v203
s_add_u32 s17, s17, 32768
mm v[216:219],a[72:75],a[56:59],v[216:219]
cv v229,v202,v203
s_add_u32 m0, m0, 1024
mm v[216:219],a[68:71],a[52:55],v[216:219]
ex v196,v196
mm v[216:219],a[64:67],a[48:51],v[216:219]
ld v240,s[56:59],s17 offen lds
mm v[212:215],a[80:83],a[32:35],0
ex v197,v197
s_add_u32 s17, s17, 90112
mm v[212:215],a[84:87],a[36:39],v[212:215]
cv v226,v196,v197
s_mov_b32 s93, 64512
s_add_u32 m0, s83, 33792
mm v[212:215],a[88:91],a[40:43],v[212:215]
ex v198,v198
mm v[212:215],a[92:95],a[44:47],v[212:215]
ld a[164:167],v243,s[40:43],s93 offen
mm v[220:223],a[92:95],a[60:63],0
ex v199,v199
mm v[220:223],a[88:91],a[56:59],v[220:223]
cv v227,v198,v199
mm v[220:223],a[84:87],a[52:55],v[220:223]
ex v204,v204
mm v[220:223],a[80:83],a[48:51],v[220:223]
)ASM" R"ASM(wt vmcnt(16)
s_barrier
mm v[64:67],a[96:99],v[232:235],v[64:67]
ex v205,v205
mm v[96:99],a[96:99],v[236:239],v[96:99]
rd a[96:99],v248 offset:8192
cv v230,v204,v205
mm v[100:103],a[100:103],v[236:239],v[100:103]
ex v206,v206
mm v[68:71],a[100:103],v[232:235],v[68:71]
rd a[100:103],v248 offset:9216
mm v[72:75],a[104:107],v[232:235],v[72:75]
ex v207,v207
mm v[104:107],a[104:107],v[236:239],v[104:107]
rd a[104:107],v248 offset:10240
cv v231,v206,v207
s_mov_b32 s93, 71680
mm v[108:111],a[108:111],v[236:239],v[108:111]
ex v208,v208
mm v[76:79],a[108:111],v[232:235],v[76:79]
rd a[108:111],v248 offset:11264
ld a[168:171],v243,s[40:43],s93 offen
mm v[80:83],a[112:115],v[232:235],v[80:83]
ex v209,v209
mm v[112:115],a[112:115],v[236:239],v[112:115]
rd a[112:115],v248 offset:12288
s_mov_b32 s93, 78848
mm v[116:119],a[116:119],v[236:239],v[116:119]
ex v210,v210
mm v[84:87],a[116:119],v[232:235],v[84:87]
rd a[116:119],v248 offset:13312
ld a[172:175],v243,s[40:43],s93 offen
mm v[88:91],a[120:123],v[232:235],v[88:91]
ex v211,v211
mm v[120:123],a[120:123],v[236:239],v[120:123]
rd a[120:123],v248 offset:14336
mm v[124:127],a[124:127],v[236:239],v[124:127]
ex v216,v216
mm v[92:95],a[124:127],v[232:235],v[92:95]
rd a[124:127],v248 offset:15360
v_or_b32 v248, s89, v255
v_smfmac_f32_16x16x64_bf16 a[196:199], a[208:211], v[232:239], v241
wt lgkmcnt(4)
v_smfmac_f32_16x16x64_bf16 a[192:195], a[208:211], v[224:231], v241
rd a[64:67],v248 offset:0
mm v[0:3],a[96:99],v[224:227],v[0:3]
ex v217,v217
mm v[32:35],a[96:99],v[228:231],v[32:35]
rd a[68:71],v248 offset:1024
cv v232,v208,v209
mm v[36:39],a[100:103],v[228:231],v[36:39]
ex v218,v218
mm v[4:7],a[100:103],v[224:227],v[4:7]
rd a[72:75],v248 offset:2048
cv v233,v210,v211
mm v[8:11],a[104:107],v[224:227],v[8:11]
ex v219,v219
mm v[40:43],a[104:107],v[228:231],v[40:43]
rd a[76:79],v248 offset:3072
cv v236,v216,v217
mm v[44:47],a[108:111],v[228:231],v[44:47]
ex v212,v212
mm v[12:15],a[108:111],v[224:227],v[12:15]
rd a[80:83],v248 offset:4096
wt lgkmcnt(5)
mm v[16:19],a[112:115],v[224:227],v[16:19]
ex v213,v213
mm v[48:51],a[112:115],v[228:231],v[48:51]
rd a[84:87],v248 offset:5120
cv v237,v218,v219
mm v[52:55],a[116:119],v[228:231],v[52:55]
ex v214,v214
mm v[20:23],a[116:119],v[224:227],v[20:23]
rd a[88:91],v248 offset:6144
cv v234,v212,v213
mm v[24:27],a[120:123],v[224:227],v[24:27]
ex v215,v215
mm v[56:59],a[120:123],v[228:231],v[56:59]
rd a[92:95],v248 offset:7168
cv v235,v214,v215
mm v[60:63],a[124:127],v[228:231],v[60:63]
ex v220,v220
mm v[28:31],a[124:127],v[224:227],v[28:31]
wt lgkmcnt(4)
mm v[192:195],a[64:67],a[0:3],0
ex v221,v221
mm v[192:195],a[68:71],a[4:7],v[192:195]
cv v238,v220,v221
s_add_u32 s89, s89, 16384
mm v[192:195],a[72:75],a[8:11],v[192:195]
ex v222,v222
mm v[192:195],a[76:79],a[12:15],v[192:195]
ld v240,s[56:59],s17 offen lds
mm v[200:203],a[76:79],a[28:31],0
ex v223,v223
s_add_u32 s17, s17, 32768
mm v[200:203],a[72:75],a[24:27],v[200:203]
cv v239,v222,v223
s_add_u32 m0, m0, 1024
mm v[200:203],a[68:71],a[20:23],v[200:203]
)ASM" R"ASM(ex v192,v192
mm v[200:203],a[64:67],a[16:19],v[200:203]
wt lgkmcnt(0)
s_and_b32 s89, s89, 0x1ffff
mm v[196:199],a[80:83],a[0:3],0
ex v193,v193
mm v[196:199],a[84:87],a[4:7],v[196:199]
cv v224,v192,v193
mm v[196:199],a[88:91],a[8:11],v[196:199]
ex v194,v194
mm v[196:199],a[92:95],a[12:15],v[196:199]
ld v240,s[56:59],s17 offen lds
mm v[204:207],a[92:95],a[28:31],0
ex v195,v195
s_add_u32 s17, s17, 4294942720
mm v[204:207],a[88:91],a[24:27],v[204:207]
cv v225,v194,v195
s_add_u32 m0, m0, 1152
mm v[204:207],a[84:87],a[20:23],v[204:207]
ex v200,v200
mm v[204:207],a[80:83],a[16:19],v[204:207]
ld v240,s[56:59],s17 offen lds
mm v[208:211],a[64:67],a[32:35],0
ex v201,v201
s_add_u32 s17, s17, 32768
mm v[208:211],a[68:71],a[36:39],v[208:211]
cv v228,v200,v201
s_add_u32 m0, m0, 1024
mm v[208:211],a[72:75],a[40:43],v[208:211]
ex v202,v202
mm v[208:211],a[76:79],a[44:47],v[208:211]
ld v240,s[56:59],s17 offen lds
mm v[216:219],a[76:79],a[60:63],0
ex v203,v203
mm v[216:219],a[72:75],a[56:59],v[216:219]
cv v229,v202,v203
s_mov_b32 s93, 86016
mm v[216:219],a[68:71],a[52:55],v[216:219]
ex v196,v196
mm v[216:219],a[64:67],a[48:51],v[216:219]
ld a[176:179],v243,s[40:43],s93 offen
mm v[212:215],a[80:83],a[32:35],0
ex v197,v197
s_bfe_u32 s17, s4, 262148
mm v[212:215],a[84:87],a[36:39],v[212:215]
cv v226,v196,v197
s_mov_b32 s93, 93184
s_xor_b32 s17, s17, s72
mm v[212:215],a[88:91],a[40:43],v[212:215]
ex v198,v198
s_lshl_b32 s17, s17, 18
mm v[212:215],a[92:95],a[44:47],v[212:215]
ld a[180:183],v243,s[40:43],s93 offen
s_add_u32 m0, s83, 50688
mm v[220:223],a[92:95],a[60:63],0
ex v199,v199
mm v[220:223],a[88:91],a[56:59],v[220:223]
cv v227,v198,v199
mm v[220:223],a[84:87],a[52:55],v[220:223]
ex v204,v204
mm v[220:223],a[80:83],a[48:51],v[220:223]
wt vmcnt(16)
s_barrier
mm v[64:67],a[96:99],v[232:235],v[64:67]
ex v205,v205
mm v[96:99],a[96:99],v[236:239],v[96:99]
rd a[96:99],v248 offset:8192
cv v230,v204,v205
mm v[100:103],a[100:103],v[236:239],v[100:103]
ex v206,v206
mm v[68:71],a[100:103],v[232:235],v[68:71]
rd a[100:103],v248 offset:9216
mm v[72:75],a[104:107],v[232:235],v[72:75]
ex v207,v207
mm v[104:107],a[104:107],v[236:239],v[104:107]
rd a[104:107],v248 offset:10240
cv v231,v206,v207
s_mov_b32 s93, 100352
mm v[108:111],a[108:111],v[236:239],v[108:111]
ex v208,v208
mm v[76:79],a[108:111],v[232:235],v[76:79]
rd a[108:111],v248 offset:11264
ld a[184:187],v243,s[40:43],s93 offen
mm v[80:83],a[112:115],v[232:235],v[80:83]
ex v209,v209
mm v[112:115],a[112:115],v[236:239],v[112:115]
rd a[112:115],v248 offset:12288
mm v[116:119],a[116:119],v[236:239],v[116:119]
ex v210,v210
mm v[84:87],a[116:119],v[232:235],v[84:87]
rd a[116:119],v248 offset:13312
mm v[88:91],a[120:123],v[232:235],v[88:91]
ex v211,v211
mm v[120:123],a[120:123],v[236:239],v[120:123]
rd a[120:123],v248 offset:14336
mm v[124:127],a[124:127],v[236:239],v[124:127]
ex v216,v216
mm v[92:95],a[124:127],v[232:235],v[92:95]
rd a[124:127],v248 offset:15360
v_or_b32 v248, s89, v255
)ASM" R"ASM(v_smfmac_f32_16x16x64_bf16 a[196:199], a[208:211], v[232:239], v241
wt lgkmcnt(4)
v_smfmac_f32_16x16x64_bf16 a[192:195], a[208:211], v[224:231], v241
rd a[64:67],v248 offset:0
mm v[0:3],a[96:99],v[224:227],v[0:3]
ex v217,v217
mm v[32:35],a[96:99],v[228:231],v[32:35]
rd a[68:71],v248 offset:1024
cv v232,v208,v209
mm v[36:39],a[100:103],v[228:231],v[36:39]
ex v218,v218
mm v[4:7],a[100:103],v[224:227],v[4:7]
rd a[72:75],v248 offset:2048
cv v233,v210,v211
mm v[8:11],a[104:107],v[224:227],v[8:11]
ex v219,v219
mm v[40:43],a[104:107],v[228:231],v[40:43]
rd a[76:79],v248 offset:3072
cv v236,v216,v217
mm v[44:47],a[108:111],v[228:231],v[44:47]
ex v212,v212
mm v[12:15],a[108:111],v[224:227],v[12:15]
rd a[80:83],v248 offset:4096
wt lgkmcnt(5)
mm v[16:19],a[112:115],v[224:227],v[16:19]
ex v213,v213
mm v[48:51],a[112:115],v[228:231],v[48:51]
rd a[84:87],v248 offset:5120
cv v237,v218,v219
mm v[52:55],a[116:119],v[228:231],v[52:55]
ex v214,v214
mm v[20:23],a[116:119],v[224:227],v[20:23]
rd a[88:91],v248 offset:6144
cv v234,v212,v213
mm v[24:27],a[120:123],v[224:227],v[24:27]
ex v215,v215
mm v[56:59],a[120:123],v[228:231],v[56:59]
rd a[92:95],v248 offset:7168
cv v235,v214,v215
mm v[60:63],a[124:127],v[228:231],v[60:63]
ex v220,v220
mm v[28:31],a[124:127],v[224:227],v[28:31]
wt lgkmcnt(4)
mm v[192:195],a[64:67],a[0:3],0
ex v221,v221
mm v[192:195],a[68:71],a[4:7],v[192:195]
cv v238,v220,v221
mm v[192:195],a[72:75],a[8:11],v[192:195]
ex v222,v222
mm v[192:195],a[76:79],a[12:15],v[192:195]
ld v240,s[56:59],s17 offen lds
mm v[200:203],a[76:79],a[28:31],0
ex v223,v223
s_add_u32 s17, s17, 32768
mm v[200:203],a[72:75],a[24:27],v[200:203]
cv v239,v222,v223
s_add_u32 m0, m0, 1024
mm v[200:203],a[68:71],a[20:23],v[200:203]
ex v192,v192
mm v[200:203],a[64:67],a[16:19],v[200:203]
wt lgkmcnt(0)
mm v[196:199],a[80:83],a[0:3],0
ex v193,v193
mm v[196:199],a[84:87],a[4:7],v[196:199]
cv v224,v192,v193
mm v[196:199],a[88:91],a[8:11],v[196:199]
ex v194,v194
mm v[196:199],a[92:95],a[12:15],v[196:199]
ld v240,s[56:59],s17 offen lds
mm v[204:207],a[92:95],a[28:31],0
ex v195,v195
s_add_u32 s17, s17, 4294942720
mm v[204:207],a[88:91],a[24:27],v[204:207]
cv v225,v194,v195
s_add_u32 m0, m0, 1152
mm v[204:207],a[84:87],a[20:23],v[204:207]
ex v200,v200
mm v[204:207],a[80:83],a[16:19],v[204:207]
ld v240,s[56:59],s17 offen lds
mm v[208:211],a[64:67],a[32:35],0
ex v201,v201
s_add_u32 s17, s17, 32768
mm v[208:211],a[68:71],a[36:39],v[208:211]
cv v228,v200,v201
s_add_u32 m0, m0, 1024
mm v[208:211],a[72:75],a[40:43],v[208:211]
ex v202,v202
mm v[208:211],a[76:79],a[44:47],v[208:211]
ld v240,s[56:59],s17 offen lds
mm v[216:219],a[76:79],a[60:63],0
ex v203,v203
mm v[216:219],a[72:75],a[56:59],v[216:219]
cv v229,v202,v203
s_mov_b32 s93, 107520
mm v[216:219],a[68:71],a[52:55],v[216:219]
ex v196,v196
mm v[216:219],a[64:67],a[48:51],v[216:219]
ld a[188:191],v243,s[40:43],s93 offen
mm v[212:215],a[80:83],a[32:35],0
ex v197,v197
)ASM" R"ASM(mm v[212:215],a[84:87],a[36:39],v[212:215]
cv v226,v196,v197
mm v[212:215],a[88:91],a[40:43],v[212:215]
ex v198,v198
mm v[212:215],a[92:95],a[44:47],v[212:215]
mm v[220:223],a[92:95],a[60:63],0
ex v199,v199
mm v[220:223],a[88:91],a[56:59],v[220:223]
cv v227,v198,v199
mm v[220:223],a[84:87],a[52:55],v[220:223]
ex v204,v204
mm v[220:223],a[80:83],a[48:51],v[220:223]
mm v[64:67],a[96:99],v[232:235],v[64:67]
ex v205,v205
mm v[96:99],a[96:99],v[236:239],v[96:99]
rd a[96:99],v248 offset:8192
cv v230,v204,v205
mm v[100:103],a[100:103],v[236:239],v[100:103]
ex v206,v206
mm v[68:71],a[100:103],v[232:235],v[68:71]
rd a[100:103],v248 offset:9216
mm v[72:75],a[104:107],v[232:235],v[72:75]
ex v207,v207
mm v[104:107],a[104:107],v[236:239],v[104:107]
rd a[104:107],v248 offset:10240
cv v231,v206,v207
mm v[108:111],a[108:111],v[236:239],v[108:111]
ex v208,v208
mm v[76:79],a[108:111],v[232:235],v[76:79]
rd a[108:111],v248 offset:11264
mm v[80:83],a[112:115],v[232:235],v[80:83]
ex v209,v209
mm v[112:115],a[112:115],v[236:239],v[112:115]
rd a[112:115],v248 offset:12288
mm v[116:119],a[116:119],v[236:239],v[116:119]
ex v210,v210
mm v[84:87],a[116:119],v[232:235],v[84:87]
rd a[116:119],v248 offset:13312
mm v[88:91],a[120:123],v[232:235],v[88:91]
ex v211,v211
mm v[120:123],a[120:123],v[236:239],v[120:123]
rd a[120:123],v248 offset:14336
mm v[124:127],a[124:127],v[236:239],v[124:127]
ex v216,v216
mm v[92:95],a[124:127],v[232:235],v[92:95]
rd a[124:127],v248 offset:15360
v_smfmac_f32_16x16x64_bf16 a[196:199], a[208:211], v[232:239], v241
wt lgkmcnt(4)
v_smfmac_f32_16x16x64_bf16 a[192:195], a[208:211], v[224:231], v241
mm v[0:3],a[96:99],v[224:227],v[0:3]
ex v217,v217
mm v[32:35],a[96:99],v[228:231],v[32:35]
cv v232,v208,v209
mm v[36:39],a[100:103],v[228:231],v[36:39]
ex v218,v218
mm v[4:7],a[100:103],v[224:227],v[4:7]
cv v233,v210,v211
mm v[8:11],a[104:107],v[224:227],v[8:11]
ex v219,v219
mm v[40:43],a[104:107],v[228:231],v[40:43]
cv v236,v216,v217
mm v[44:47],a[108:111],v[228:231],v[44:47]
ex v212,v212
mm v[12:15],a[108:111],v[224:227],v[12:15]
wt lgkmcnt(0)
mm v[16:19],a[112:115],v[224:227],v[16:19]
ex v213,v213
mm v[48:51],a[112:115],v[228:231],v[48:51]
cv v237,v218,v219
mm v[52:55],a[116:119],v[228:231],v[52:55]
ex v214,v214
mm v[20:23],a[116:119],v[224:227],v[20:23]
cv v234,v212,v213
mm v[24:27],a[120:123],v[224:227],v[24:27]
ex v215,v215
mm v[56:59],a[120:123],v[228:231],v[56:59]
cv v235,v214,v215
mm v[60:63],a[124:127],v[228:231],v[60:63]
ex v220,v220
mm v[28:31],a[124:127],v[224:227],v[28:31]
; ---- helper: partial O and l go to the partner
.La5_LA_DP:
ex v221,v221
ex v222,v222
ex v223,v223
cv v238,v220,v221
s_nop 0
cv v239,v222,v223
s_nop 1
v_smfmac_f32_16x16x64_bf16 a[196:199], a[208:211], v[232:239], v241
cv v0,v0,v1
cv v1,v2,v3
cv v2,v4,v5
cv v3,v6,v7
st v[0:3],v255,s[68:71],0 offen offset:0 sc1
mm v[64:67],a[96:99],v[232:235],v[64:67]
cv v8,v8,v9
cv v9,v10,v11
cv v10,v12,v13
cv v11,v14,v15
)ASM" R"ASM(st v[8:11],v255,s[68:71],0 offen offset:1024 sc1
mm v[96:99],a[96:99],v[236:239],v[96:99]
ar v240,a224
v_xor_b32 v241, 32, v240
cv v16,v16,v17
cv v17,v18,v19
cv v18,v20,v21
cv v19,v22,v23
mm v[100:103],a[100:103],v[236:239],v[100:103]
v_xor_b32 v242, 64, v240
v_xor_b32 v243, 96, v240
st v[16:19],v255,s[68:71],0 offen offset:2048 sc1
cv v24,v24,v25
cv v25,v26,v27
mm v[68:71],a[100:103],v[232:235],v[68:71]
cv v26,v28,v29
cv v27,v30,v31
st v[24:27],v255,s[68:71],0 offen offset:3072 sc1
cv v32,v32,v33
cv v33,v34,v35
mm v[72:75],a[104:107],v[232:235],v[72:75]
cv v34,v36,v37
cv v35,v38,v39
st v[32:35],v255,s[68:71],s95 offen offset:0 sc1
cv v40,v40,v41
cv v41,v42,v43
mm v[104:107],a[104:107],v[236:239],v[104:107]
wt vmcnt(13)
s_barrier
cv v42,v44,v45
cv v43,v46,v47
st v[40:43],v255,s[68:71],s95 offen offset:1024 sc1
mm v[108:111],a[108:111],v[236:239],v[108:111]
rt v[192:193],v240 offset:16896
rt v[194:195],v240 offset:17920
cv v48,v48,v49
cv v49,v50,v51
cv v50,v52,v53
cv v51,v54,v55
mm v[76:79],a[108:111],v[232:235],v[76:79]
rt v[196:197],v240 offset:25344
rt v[198:199],v240 offset:26368
st v[48:51],v255,s[68:71],s95 offen offset:2048 sc1
cv v56,v56,v57
cv v57,v58,v59
mm v[80:83],a[112:115],v[232:235],v[80:83]
rt v[200:201],v241 offset:16896
rt v[202:203],v241 offset:17920
cv v58,v60,v61
cv v59,v62,v63
st v[56:59],v255,s[68:71],s95 offen offset:3072 sc1
mm v[112:115],a[112:115],v[236:239],v[112:115]
rt v[204:205],v241 offset:25344
rt v[206:207],v241 offset:26368
mm v[116:119],a[116:119],v[236:239],v[116:119]
rt v[208:209],v242 offset:16896
rt v[210:211],v242 offset:17920
mm v[84:87],a[116:119],v[232:235],v[84:87]
rt v[212:213],v242 offset:25344
rt v[214:215],v242 offset:26368
mm v[88:91],a[120:123],v[232:235],v[88:91]
rt v[216:217],v243 offset:16896
rt v[218:219],v243 offset:17920
mm v[120:123],a[120:123],v[236:239],v[120:123]
rt v[220:221],v243 offset:25344
rt v[222:223],v243 offset:26368
mm v[124:127],a[124:127],v[236:239],v[124:127]
mm v[92:95],a[124:127],v[232:235],v[92:95]
wt lgkmcnt(12)
mm v[0:3],v[192:195],v[128:131],0
mm v[0:3],v[196:199],v[132:135],v[0:3]
mm v[32:35],v[196:199],v[148:151],0
wt lgkmcnt(8)
ar v224,a192
ar v228,a193
ar v225,a194
ar v229,a195
ar v226,a196
mm v[32:35],v[192:195],v[144:147],v[32:35]
rt v[196:197],v240 offset:17024
rt v[198:199],v240 offset:18048
ar v230,a197
ar v227,a198
ar v231,a199
v_add_f32 v224, v224, v228
mm v[36:39],v[200:203],v[144:147],0
v_add_f32 v225, v225, v229
v_add_f32 v226, v226, v230
v_add_f32 v227, v227, v231
s_mov_b32 s92, 16384
st v[224:227],v255,s[68:71],s92 offen sc1
mm v[36:39],v[204:207],v[148:151],v[36:39]
rt v[192:193],v240 offset:25472
rt v[194:195],v240 offset:26496
cv v64,v64,v65
cv v65,v66,v67
cv v66,v68,v69
cv v67,v70,v71
mm v[4:7],v[204:207],v[132:135],0
wt lgkmcnt(4)
)ASM" R"ASM(st v[64:67],v255,s[68:71],s96 offen offset:0 sc1
cv v72,v72,v73
cv v73,v74,v75
cv v74,v76,v77
mm v[4:7],v[200:203],v[128:131],v[4:7]
rt v[204:205],v241 offset:17024
rt v[206:207],v241 offset:18048
cv v75,v78,v79
st v[72:75],v255,s[68:71],s96 offen offset:1024 sc1
cv v80,v80,v81
mm v[8:11],v[208:211],v[128:131],0
cv v81,v82,v83
cv v82,v84,v85
cv v83,v86,v87
st v[80:83],v255,s[68:71],s96 offen offset:2048 sc1
cv v88,v88,v89
mm v[8:11],v[212:215],v[132:135],v[8:11]
rt v[200:201],v241 offset:25472
rt v[202:203],v241 offset:26496
cv v89,v90,v91
cv v90,v92,v93
cv v91,v94,v95
st v[88:91],v255,s[68:71],s96 offen offset:3072 sc1
mm v[40:43],v[212:215],v[148:151],0
cv v96,v96,v97
cv v97,v98,v99
cv v98,v100,v101
cv v99,v102,v103
st v[96:99],v255,s[68:71],s97 offen offset:0 sc1
mm v[40:43],v[208:211],v[144:147],v[40:43]
rt v[212:213],v242 offset:17024
rt v[214:215],v242 offset:18048
cv v104,v104,v105
cv v105,v106,v107
cv v106,v108,v109
cv v107,v110,v111
mm v[44:47],v[216:219],v[144:147],0
st v[104:107],v255,s[68:71],s97 offen offset:1024 sc1
cv v112,v112,v113
cv v113,v114,v115
cv v114,v116,v117
cv v115,v118,v119
mm v[44:47],v[220:223],v[148:151],v[44:47]
rt v[208:209],v242 offset:25472
.La5_LW6_5:
s_cbranch_execz .La5_LW6_R0
rt v[210:211],v242 offset:26496
st v[112:115],v255,s[68:71],s97 offen offset:2048 sc1
cv v120,v120,v121
cv v121,v122,v123
mm v[12:15],v[220:223],v[132:135],0
wt lgkmcnt(4)
cv v122,v124,v125
cv v123,v126,v127
st v[120:123],v255,s[68:71],s97 offen offset:3072 sc1
v_mov_b32_e64 v249, 0
mm v[12:15],v[216:219],v[128:131],v[12:15]
rt v[220:221],v243 offset:17024
rt v[222:223],v243 offset:18048
s_add_u32 s6, s88, 393216
mm v[16:19],v[196:199],v[128:131],0
ar v224,a228
v_add_u32 v225, 0x8000, v224
v_add_u32 v226, 0x2000, v224
v_add_u32 v227, 0xa000, v224
mm v[16:19],v[192:195],v[132:135],v[16:19]
rt v[216:217],v243 offset:25472
rt v[218:219],v243 offset:26496
ar v248,a225
ar v228,a229
mm v[48:51],v[192:195],v[148:151],0
ar v229,a230
mm v[48:51],v[196:199],v[144:147],v[48:51]
rt v[192:193],v240 offset:33792
rt v[194:195],v240 offset:34816
mm v[52:55],v[204:207],v[144:147],0
mm v[52:55],v[200:203],v[148:151],v[52:55]
rt v[196:197],v240 offset:42240
rt v[198:199],v240 offset:43264
mm v[20:23],v[200:203],v[132:135],0
wt lgkmcnt(4)
mm v[20:23],v[204:207],v[128:131],v[20:23]
rt v[200:201],v241 offset:33792
rt v[202:203],v241 offset:34816
mm v[24:27],v[212:215],v[128:131],0
s_bitcmp1_b32 2863311530, s2
s_cbranch_scc1 .La5_LO_VC2
s_getpc_b64 s[24:25]
.La5_LW3_P0:
s_add_u32 s24, s24, .La5_LW3_R0-.La5_LW3_P0
s_addc_u32 s25, s25, 0
s_mov_b64 exec, 0
s_cmp_eq_u32 s3, 0
s_cbranch_scc1 .La5_LW3_0
s_cmp_eq_u32 s3, 1
s_cbranch_scc1 .La5_LW3_1
s_cmp_eq_u32 s3, 2
s_cbranch_scc1 .La5_LW3_2
s_branch .La5_LW3_3
.La5_LW3_R0:
)ASM" R"ASM(s_mov_b64 exec, -1
.La5_LO_VE2:
wt vmcnt(0)
buffer_store_dwordx2 v[252:253], v249, s[64:67], s6 offen offset:0 sc1
mm v[24:27],v[208:211],v[132:135],v[24:27]
rt v[204:205],v241 offset:42240
rt v[206:207],v241 offset:43264
mm v[56:59],v[208:211],v[148:151],0
s_mov_b32 s93, 3670016
ld a[192:195],v228,s[40:43],s93 offen
mm v[56:59],v[212:215],v[144:147],v[56:59]
rt v[208:209],v242 offset:33792
rt v[210:211],v242 offset:34816
mm v[60:63],v[220:223],v[144:147],0
s_mov_b32 s93, 3677184
ld a[196:199],v228,s[40:43],s93 offen
mm v[60:63],v[216:219],v[148:151],v[60:63]
rt v[212:213],v242 offset:42240
rt v[214:215],v242 offset:43264
mm v[28:31],v[216:219],v[132:135],0
wt lgkmcnt(4)
s_mov_b32 s93, 3684352
ld a[200:203],v228,s[40:43],s93 offen
mm v[28:31],v[220:223],v[128:131],v[28:31]
rt v[216:217],v243 offset:33792
rt v[218:219],v243 offset:34816
mm v[0:3],v[192:195],v[136:139],v[0:3]
s_mov_b32 s93, 3691520
ld a[204:207],v228,s[40:43],s93 offen
mm v[0:3],v[196:199],v[140:143],v[0:3]
rt v[220:221],v243 offset:42240
rt v[222:223],v243 offset:43264
mm v[32:35],v[196:199],v[156:159],v[32:35]
s_mov_b32 s93, 3698688
ld a[208:211],v228,s[40:43],s93 offen
mm v[32:35],v[192:195],v[152:155],v[32:35]
rt v[196:197],v240 offset:33920
rt v[198:199],v240 offset:34944
mm v[36:39],v[200:203],v[152:155],v[36:39]
s_mov_b32 s93, 3705856
ld a[212:215],v228,s[40:43],s93 offen
mm v[36:39],v[204:207],v[156:159],v[36:39]
rt v[192:193],v240 offset:42368
rt v[194:195],v240 offset:43392
mm v[4:7],v[204:207],v[140:143],v[4:7]
wt lgkmcnt(4)
s_mov_b32 s93, 3713024
ld a[216:219],v228,s[40:43],s93 offen
mm v[4:7],v[200:203],v[136:139],v[4:7]
rt v[204:205],v241 offset:33920
rt v[206:207],v241 offset:34944
mm v[8:11],v[208:211],v[136:139],v[8:11]
s_mov_b32 s93, 3720192
ld a[220:223],v228,s[40:43],s93 offen
mm v[8:11],v[212:215],v[140:143],v[8:11]
rt v[200:201],v241 offset:42368
rt v[202:203],v241 offset:43392
mm v[40:43],v[212:215],v[156:159],v[40:43]
s_mov_b32 s93, 3727360
ld a[224:227],v228,s[40:43],s93 offen
mm v[40:43],v[208:211],v[152:155],v[40:43]
rt v[212:213],v242 offset:33920
rt v[214:215],v242 offset:34944
mm v[44:47],v[216:219],v[152:155],v[44:47]
s_mov_b32 s93, 3734528
ld a[228:231],v228,s[40:43],s93 offen
mm v[44:47],v[220:223],v[156:159],v[44:47]
rt v[208:209],v242 offset:42368
rt v[210:211],v242 offset:43392
mm v[12:15],v[220:223],v[140:143],v[12:15]
wt lgkmcnt(4)
s_mov_b32 s93, 3741696
ld a[232:235],v228,s[40:43],s93 offen
mm v[12:15],v[216:219],v[136:139],v[12:15]
rt v[220:221],v243 offset:33920
rt v[222:223],v243 offset:34944
mm v[16:19],v[196:199],v[136:139],v[16:19]
s_mov_b32 s93, 3748864
ld a[236:239],v228,s[40:43],s93 offen
mm v[16:19],v[192:195],v[140:143],v[16:19]
rt v[216:217],v243 offset:42368
rt v[218:219],v243 offset:43392
mm v[48:51],v[192:195],v[156:159],v[48:51]
s_mov_b32 s93, 3756032
ld a[240:243],v228,s[40:43],s93 offen
mm v[48:51],v[196:199],v[152:155],v[48:51]
rt v[192:193],v240 offset:16896
rt v[194:195],v240 offset:17920
mm v[52:55],v[204:207],v[152:155],v[52:55]
s_mov_b32 s93, 3763200
ld a[244:247],v228,s[40:43],s93 offen
mm v[52:55],v[200:203],v[156:159],v[52:55]
rt v[196:197],v240 offset:25344
rt v[198:199],v240 offset:26368
mm v[20:23],v[200:203],v[140:143],v[20:23]
wt lgkmcnt(4)
s_mov_b32 s93, 3770368
ld a[248:251],v228,s[40:43],s93 offen
mm v[20:23],v[204:207],v[136:139],v[20:23]
rt v[200:201],v241 offset:16896
)ASM" R"ASM(rt v[202:203],v241 offset:17920
mm v[24:27],v[212:215],v[136:139],v[24:27]
s_mov_b32 s93, 3777536
ld a[252:255],v228,s[40:43],s93 offen
mm v[24:27],v[208:211],v[140:143],v[24:27]
rt v[204:205],v241 offset:25344
rt v[206:207],v241 offset:26368
mm v[56:59],v[208:211],v[156:159],v[56:59]
mm v[56:59],v[212:215],v[152:155],v[56:59]
rt v[208:209],v242 offset:16896
rt v[210:211],v242 offset:17920
mm v[60:63],v[220:223],v[152:155],v[60:63]
mm v[60:63],v[216:219],v[156:159],v[60:63]
rt v[212:213],v242 offset:25344
rt v[214:215],v242 offset:26368
mm v[28:31],v[216:219],v[140:143],v[28:31]
wt lgkmcnt(4)
mm v[28:31],v[220:223],v[136:139],v[28:31]
rt v[216:217],v243 offset:16896
rt v[218:219],v243 offset:17920
s_branch .La5_LO_START
; ---- owner: last windows
.La5_LA_DC:
.La5_LW5_0:
s_cbranch_execz .La5_LW5_4
wt lgkmcnt(4)
mm v[192:195],a[64:67],v[128:131],0
ex v221,v221
mm v[192:195],a[68:71],v[132:135],v[192:195]
cv v238,v220,v221
s_add_u32 s89, s89, 16384
mm v[192:195],a[72:75],v[136:139],v[192:195]
ex v222,v222
mm v[192:195],a[76:79],v[140:143],v[192:195]
ld v255,s[60:63],s17 offen offset:0 sc1 lds
mm v[200:203],a[76:79],v[156:159],0
ex v223,v223
mm v[200:203],a[72:75],v[152:155],v[200:203]
cv v239,v222,v223
s_and_b32 s89, s89, 0x1ffff
mm v[200:203],a[68:71],v[148:151],v[200:203]
ex v192,v192
mm v[200:203],a[64:67],v[144:147],v[200:203]
wt lgkmcnt(0)
mm v[196:199],a[80:83],v[128:131],0
ex v193,v193
mm v[196:199],a[84:87],v[132:135],v[196:199]
cv v224,v192,v193
ar v243,a229
mm v[196:199],a[88:91],v[136:139],v[196:199]
ex v194,v194
mm v[196:199],a[92:95],v[140:143],v[196:199]
ld v255,s[60:63],s17 offen offset:1024 sc1 lds
mm v[204:207],a[92:95],v[156:159],0
ex v195,v195
mm v[204:207],a[88:91],v[152:155],v[204:207]
cv v225,v194,v195
mm v[204:207],a[84:87],v[148:151],v[204:207]
ex v200,v200
mm v[204:207],a[80:83],v[144:147],v[204:207]
ld v255,s[60:63],s17 offen offset:2048 sc1 lds
mm v[208:211],a[64:67],v[160:163],0
ex v201,v201
mm v[208:211],a[68:71],v[164:167],v[208:211]
cv v228,v200,v201
mm v[208:211],a[72:75],v[168:171],v[208:211]
ex v202,v202
mm v[208:211],a[76:79],v[172:175],v[208:211]
ld v255,s[60:63],s17 offen offset:3072 sc1 lds
mm v[216:219],a[76:79],v[188:191],0
ex v203,v203
mm v[216:219],a[72:75],v[184:187],v[216:219]
cv v229,v202,v203
s_mov_b32 s93, 0
mm v[216:219],a[68:71],v[180:183],v[216:219]
ex v196,v196
mm v[216:219],a[64:67],v[176:179],v[216:219]
ld a[128:131],v243,s[40:43],s93 offen
mm v[212:215],a[80:83],v[160:163],0
ex v197,v197
s_add_u32 s17, s17, s80
mm v[212:215],a[84:87],v[164:167],v[212:215]
cv v226,v196,v197
s_mov_b32 s93, 7168
s_add_u32 m0, m0, 16384
mm v[212:215],a[88:91],v[168:171],v[212:215]
ex v198,v198
s_and_b32 m0, m0, s78
mm v[212:215],a[92:95],v[172:175],v[212:215]
ld a[132:135],v243,s[40:43],s93 offen
mm v[220:223],a[92:95],v[188:191],0
ex v199,v199
mm v[220:223],a[88:91],v[184:187],v[220:223]
cv v227,v198,v199
mm v[220:223],a[84:87],v[180:183],v[220:223]
ex v204,v204
mm v[220:223],a[80:83],v[176:179],v[220:223]
wt vmcnt(12)
s_barrier
mm v[64:67],a[96:99],v[232:235],v[64:67]
)ASM" R"ASM(ex v205,v205
mm v[96:99],a[96:99],v[236:239],v[96:99]
rd a[96:99],v248 offset:8192
cv v230,v204,v205
s_mov_b32 s93, 28672
mm v[100:103],a[100:103],v[236:239],v[100:103]
ex v206,v206
mm v[68:71],a[100:103],v[232:235],v[68:71]
rd a[100:103],v248 offset:9216
ld a[144:147],v243,s[40:43],s93 offen
mm v[72:75],a[104:107],v[232:235],v[72:75]
ex v207,v207
mm v[104:107],a[104:107],v[236:239],v[104:107]
rd a[104:107],v248 offset:10240
cv v231,v206,v207
s_mov_b32 s93, 14336
mm v[108:111],a[108:111],v[236:239],v[108:111]
ex v208,v208
mm v[76:79],a[108:111],v[232:235],v[76:79]
rd a[108:111],v248 offset:11264
ld a[136:139],v243,s[40:43],s93 offen
mm v[80:83],a[112:115],v[232:235],v[80:83]
ex v209,v209
mm v[112:115],a[112:115],v[236:239],v[112:115]
rd a[112:115],v248 offset:12288
s_mov_b32 s93, 21504
mm v[116:119],a[116:119],v[236:239],v[116:119]
ex v210,v210
mm v[84:87],a[116:119],v[232:235],v[84:87]
rd a[116:119],v248 offset:13312
ld a[140:143],v243,s[40:43],s93 offen
mm v[88:91],a[120:123],v[232:235],v[88:91]
ex v211,v211
mm v[120:123],a[120:123],v[236:239],v[120:123]
rd a[120:123],v248 offset:14336
s_mov_b32 s93, 35840
mm v[124:127],a[124:127],v[236:239],v[124:127]
ex v216,v216
mm v[92:95],a[124:127],v[232:235],v[92:95]
rd a[124:127],v248 offset:15360
ld a[148:151],v243,s[40:43],s93 offen
v_or_b32 v248, s89, v255
v_smfmac_f32_16x16x64_bf16 a[196:199], a[208:211], v[232:239], v241
wt lgkmcnt(4)
v_smfmac_f32_16x16x64_bf16 a[192:195], a[208:211], v[224:231], v241
rd a[64:67],v248 offset:0
mm v[0:3],a[96:99],v[224:227],v[0:3]
ex v217,v217
mm v[32:35],a[96:99],v[228:231],v[32:35]
rd a[68:71],v248 offset:1024
cv v232,v208,v209
mm v[36:39],a[100:103],v[228:231],v[36:39]
ex v218,v218
mm v[4:7],a[100:103],v[224:227],v[4:7]
rd a[72:75],v248 offset:2048
cv v233,v210,v211
mm v[8:11],a[104:107],v[224:227],v[8:11]
ex v219,v219
mm v[40:43],a[104:107],v[228:231],v[40:43]
rd a[76:79],v248 offset:3072
cv v236,v216,v217
mm v[44:47],a[108:111],v[228:231],v[44:47]
ex v212,v212
mm v[12:15],a[108:111],v[224:227],v[12:15]
rd a[80:83],v248 offset:4096
wt lgkmcnt(5)
mm v[16:19],a[112:115],v[224:227],v[16:19]
ex v213,v213
mm v[48:51],a[112:115],v[228:231],v[48:51]
rd a[84:87],v248 offset:5120
cv v237,v218,v219
mm v[52:55],a[116:119],v[228:231],v[52:55]
ex v214,v214
mm v[20:23],a[116:119],v[224:227],v[20:23]
rd a[88:91],v248 offset:6144
cv v234,v212,v213
mm v[24:27],a[120:123],v[224:227],v[24:27]
ex v215,v215
mm v[56:59],a[120:123],v[228:231],v[56:59]
rd a[92:95],v248 offset:7168
cv v235,v214,v215
mm v[60:63],a[124:127],v[228:231],v[60:63]
ex v220,v220
mm v[28:31],a[124:127],v[224:227],v[28:31]
wt lgkmcnt(4)
mm v[192:195],a[64:67],v[128:131],0
ex v221,v221
mm v[192:195],a[68:71],v[132:135],v[192:195]
cv v238,v220,v221
s_add_u32 s89, s89, 16384
mm v[192:195],a[72:75],v[136:139],v[192:195]
ex v222,v222
mm v[192:195],a[76:79],v[140:143],v[192:195]
ld v255,s[60:63],s17 offen offset:0 sc1 lds
mm v[200:203],a[76:79],v[156:159],0
ex v223,v223
mm v[200:203],a[72:75],v[152:155],v[200:203]
cv v239,v222,v223
s_and_b32 s89, s89, 0x1ffff
mm v[200:203],a[68:71],v[148:151],v[200:203]
)ASM" R"ASM(ex v192,v192
mm v[200:203],a[64:67],v[144:147],v[200:203]
wt lgkmcnt(0)
mm v[196:199],a[80:83],v[128:131],0
ex v193,v193
mm v[196:199],a[84:87],v[132:135],v[196:199]
cv v224,v192,v193
mm v[196:199],a[88:91],v[136:139],v[196:199]
ex v194,v194
mm v[196:199],a[92:95],v[140:143],v[196:199]
ld v255,s[60:63],s17 offen offset:1024 sc1 lds
mm v[204:207],a[92:95],v[156:159],0
ex v195,v195
mm v[204:207],a[88:91],v[152:155],v[204:207]
cv v225,v194,v195
mm v[204:207],a[84:87],v[148:151],v[204:207]
ex v200,v200
mm v[204:207],a[80:83],v[144:147],v[204:207]
ld v255,s[60:63],s17 offen offset:2048 sc1 lds
mm v[208:211],a[64:67],v[160:163],0
ex v201,v201
mm v[208:211],a[68:71],v[164:167],v[208:211]
cv v228,v200,v201
mm v[208:211],a[72:75],v[168:171],v[208:211]
ex v202,v202
mm v[208:211],a[76:79],v[172:175],v[208:211]
ld v255,s[60:63],s17 offen offset:3072 sc1 lds
mm v[216:219],a[76:79],v[188:191],0
ex v203,v203
mm v[216:219],a[72:75],v[184:187],v[216:219]
cv v229,v202,v203
s_mov_b32 s93, 43008
mm v[216:219],a[68:71],v[180:183],v[216:219]
ex v196,v196
mm v[216:219],a[64:67],v[176:179],v[216:219]
ld a[152:155],v243,s[40:43],s93 offen
mm v[212:215],a[80:83],v[160:163],0
ex v197,v197
s_add_u32 s17, s17, s80
mm v[212:215],a[84:87],v[164:167],v[212:215]
cv v226,v196,v197
s_mov_b32 s93, 50176
s_add_u32 m0, m0, 16384
mm v[212:215],a[88:91],v[168:171],v[212:215]
ex v198,v198
s_and_b32 m0, m0, s78
mm v[212:215],a[92:95],v[172:175],v[212:215]
ld a[156:159],v243,s[40:43],s93 offen
mm v[220:223],a[92:95],v[188:191],0
ex v199,v199
mm v[220:223],a[88:91],v[184:187],v[220:223]
cv v227,v198,v199
mm v[220:223],a[84:87],v[180:183],v[220:223]
ex v204,v204
mm v[220:223],a[80:83],v[176:179],v[220:223]
wt vmcnt(17)
s_barrier
mm v[64:67],a[96:99],v[232:235],v[64:67]
ex v205,v205
mm v[96:99],a[96:99],v[236:239],v[96:99]
rd a[96:99],v248 offset:8192
cv v230,v204,v205
s_mov_b32 s93, 71680
mm v[100:103],a[100:103],v[236:239],v[100:103]
ex v206,v206
mm v[68:71],a[100:103],v[232:235],v[68:71]
rd a[100:103],v248 offset:9216
ld a[168:171],v243,s[40:43],s93 offen
mm v[72:75],a[104:107],v[232:235],v[72:75]
ex v207,v207
mm v[104:107],a[104:107],v[236:239],v[104:107]
rd a[104:107],v248 offset:10240
cv v231,v206,v207
s_mov_b32 s93, 57344
mm v[108:111],a[108:111],v[236:239],v[108:111]
ex v208,v208
mm v[76:79],a[108:111],v[232:235],v[76:79]
rd a[108:111],v248 offset:11264
ld a[160:163],v243,s[40:43],s93 offen
mm v[80:83],a[112:115],v[232:235],v[80:83]
ex v209,v209
mm v[112:115],a[112:115],v[236:239],v[112:115]
rd a[112:115],v248 offset:12288
s_mov_b32 s93, 64512
mm v[116:119],a[116:119],v[236:239],v[116:119]
ex v210,v210
mm v[84:87],a[116:119],v[232:235],v[84:87]
rd a[116:119],v248 offset:13312
ld a[164:167],v243,s[40:43],s93 offen
mm v[88:91],a[120:123],v[232:235],v[88:91]
ex v211,v211
mm v[120:123],a[120:123],v[236:239],v[120:123]
rd a[120:123],v248 offset:14336
mm v[124:127],a[124:127],v[236:239],v[124:127]
ex v216,v216
mm v[92:95],a[124:127],v[232:235],v[92:95]
rd a[124:127],v248 offset:15360
v_or_b32 v248, s89, v255
v_smfmac_f32_16x16x64_bf16 a[196:199], a[208:211], v[232:239], v241
wt lgkmcnt(4)
)ASM" R"ASM(v_smfmac_f32_16x16x64_bf16 a[192:195], a[208:211], v[224:231], v241
rd a[64:67],v248 offset:0
mm v[0:3],a[96:99],v[224:227],v[0:3]
ex v217,v217
mm v[32:35],a[96:99],v[228:231],v[32:35]
rd a[68:71],v248 offset:1024
cv v232,v208,v209
mm v[36:39],a[100:103],v[228:231],v[36:39]
ex v218,v218
mm v[4:7],a[100:103],v[224:227],v[4:7]
rd a[72:75],v248 offset:2048
cv v233,v210,v211
mm v[8:11],a[104:107],v[224:227],v[8:11]
ex v219,v219
mm v[40:43],a[104:107],v[228:231],v[40:43]
rd a[76:79],v248 offset:3072
cv v236,v216,v217
mm v[44:47],a[108:111],v[228:231],v[44:47]
ex v212,v212
mm v[12:15],a[108:111],v[224:227],v[12:15]
rd a[80:83],v248 offset:4096
wt lgkmcnt(5)
mm v[16:19],a[112:115],v[224:227],v[16:19]
ex v213,v213
mm v[48:51],a[112:115],v[228:231],v[48:51]
rd a[84:87],v248 offset:5120
cv v237,v218,v219
mm v[52:55],a[116:119],v[228:231],v[52:55]
ex v214,v214
mm v[20:23],a[116:119],v[224:227],v[20:23]
rd a[88:91],v248 offset:6144
cv v234,v212,v213
mm v[24:27],a[120:123],v[224:227],v[24:27]
ex v215,v215
mm v[56:59],a[120:123],v[228:231],v[56:59]
rd a[92:95],v248 offset:7168
cv v235,v214,v215
mm v[60:63],a[124:127],v[228:231],v[60:63]
ex v220,v220
mm v[28:31],a[124:127],v[224:227],v[28:31]
wt lgkmcnt(4)
mm v[192:195],a[64:67],v[128:131],0
ex v221,v221
mm v[192:195],a[68:71],v[132:135],v[192:195]
cv v238,v220,v221
s_add_u32 s89, s89, 16384
mm v[192:195],a[72:75],v[136:139],v[192:195]
ex v222,v222
mm v[192:195],a[76:79],v[140:143],v[192:195]
ld v255,s[60:63],s17 offen offset:0 sc1 lds
mm v[200:203],a[76:79],v[156:159],0
ex v223,v223
mm v[200:203],a[72:75],v[152:155],v[200:203]
cv v239,v222,v223
s_and_b32 s89, s89, 0x1ffff
mm v[200:203],a[68:71],v[148:151],v[200:203]
ex v192,v192
mm v[200:203],a[64:67],v[144:147],v[200:203]
wt lgkmcnt(0)
mm v[196:199],a[80:83],v[128:131],0
ex v193,v193
mm v[196:199],a[84:87],v[132:135],v[196:199]
cv v224,v192,v193
mm v[196:199],a[88:91],v[136:139],v[196:199]
ex v194,v194
mm v[196:199],a[92:95],v[140:143],v[196:199]
ld v255,s[60:63],s17 offen offset:1024 sc1 lds
mm v[204:207],a[92:95],v[156:159],0
ex v195,v195
mm v[204:207],a[88:91],v[152:155],v[204:207]
cv v225,v194,v195
mm v[204:207],a[84:87],v[148:151],v[204:207]
ex v200,v200
mm v[204:207],a[80:83],v[144:147],v[204:207]
ld v255,s[60:63],s17 offen offset:2048 sc1 lds
mm v[208:211],a[64:67],v[160:163],0
ex v201,v201
mm v[208:211],a[68:71],v[164:167],v[208:211]
cv v228,v200,v201
mm v[208:211],a[72:75],v[168:171],v[208:211]
ex v202,v202
mm v[208:211],a[76:79],v[172:175],v[208:211]
ld v255,s[60:63],s17 offen offset:3072 sc1 lds
mm v[216:219],a[76:79],v[188:191],0
ex v203,v203
mm v[216:219],a[72:75],v[184:187],v[216:219]
cv v229,v202,v203
s_mov_b32 s93, 78848
mm v[216:219],a[68:71],v[180:183],v[216:219]
ex v196,v196
mm v[216:219],a[64:67],v[176:179],v[216:219]
ld a[172:175],v243,s[40:43],s93 offen
mm v[212:215],a[80:83],v[160:163],0
ex v197,v197
s_add_u32 s17, s17, s80
mm v[212:215],a[84:87],v[164:167],v[212:215]
cv v226,v196,v197
s_mov_b32 s93, 86016
s_add_u32 m0, m0, 16384
mm v[212:215],a[88:91],v[168:171],v[212:215]
)ASM" R"ASM(ex v198,v198
s_and_b32 m0, m0, s78
mm v[212:215],a[92:95],v[172:175],v[212:215]
ld a[176:179],v243,s[40:43],s93 offen
mm v[220:223],a[92:95],v[188:191],0
ex v199,v199
mm v[220:223],a[88:91],v[184:187],v[220:223]
cv v227,v198,v199
mm v[220:223],a[84:87],v[180:183],v[220:223]
ex v204,v204
mm v[220:223],a[80:83],v[176:179],v[220:223]
wt vmcnt(21)
s_barrier
mm v[64:67],a[96:99],v[232:235],v[64:67]
ex v205,v205
mm v[96:99],a[96:99],v[236:239],v[96:99]
rd a[96:99],v248 offset:8192
cv v230,v204,v205
s_mov_b32 s93, 107520
mm v[100:103],a[100:103],v[236:239],v[100:103]
ex v206,v206
mm v[68:71],a[100:103],v[232:235],v[68:71]
rd a[100:103],v248 offset:9216
ld a[188:191],v243,s[40:43],s93 offen
mm v[72:75],a[104:107],v[232:235],v[72:75]
ex v207,v207
mm v[104:107],a[104:107],v[236:239],v[104:107]
rd a[104:107],v248 offset:10240
.La5_LW5_1:
s_cbranch_execz .La5_LW5_R0
cv v231,v206,v207
s_mov_b32 s93, 93184
mm v[108:111],a[108:111],v[236:239],v[108:111]
ex v208,v208
mm v[76:79],a[108:111],v[232:235],v[76:79]
rd a[108:111],v248 offset:11264
ld a[180:183],v243,s[40:43],s93 offen
mm v[80:83],a[112:115],v[232:235],v[80:83]
ex v209,v209
mm v[112:115],a[112:115],v[236:239],v[112:115]
rd a[112:115],v248 offset:12288
s_mov_b32 s93, 100352
mm v[116:119],a[116:119],v[236:239],v[116:119]
ex v210,v210
mm v[84:87],a[116:119],v[232:235],v[84:87]
rd a[116:119],v248 offset:13312
ld a[184:187],v243,s[40:43],s93 offen
mm v[88:91],a[120:123],v[232:235],v[88:91]
ex v211,v211
mm v[120:123],a[120:123],v[236:239],v[120:123]
rd a[120:123],v248 offset:14336
mm v[124:127],a[124:127],v[236:239],v[124:127]
ex v216,v216
mm v[92:95],a[124:127],v[232:235],v[92:95]
rd a[124:127],v248 offset:15360
v_or_b32 v248, s89, v255
v_smfmac_f32_16x16x64_bf16 a[196:199], a[208:211], v[232:239], v241
wt lgkmcnt(4)
v_smfmac_f32_16x16x64_bf16 a[192:195], a[208:211], v[224:231], v241
rd a[64:67],v248 offset:0
mm v[0:3],a[96:99],v[224:227],v[0:3]
ex v217,v217
mm v[32:35],a[96:99],v[228:231],v[32:35]
rd a[68:71],v248 offset:1024
cv v232,v208,v209
mm v[36:39],a[100:103],v[228:231],v[36:39]
ex v218,v218
mm v[4:7],a[100:103],v[224:227],v[4:7]
rd a[72:75],v248 offset:2048
cv v233,v210,v211
mm v[8:11],a[104:107],v[224:227],v[8:11]
ex v219,v219
mm v[40:43],a[104:107],v[228:231],v[40:43]
rd a[76:79],v248 offset:3072
cv v236,v216,v217
mm v[44:47],a[108:111],v[228:231],v[44:47]
ex v212,v212
mm v[12:15],a[108:111],v[224:227],v[12:15]
rd a[80:83],v248 offset:4096
wt lgkmcnt(5)
mm v[16:19],a[112:115],v[224:227],v[16:19]
ex v213,v213
mm v[48:51],a[112:115],v[228:231],v[48:51]
rd a[84:87],v248 offset:5120
cv v237,v218,v219
mm v[52:55],a[116:119],v[228:231],v[52:55]
ex v214,v214
mm v[20:23],a[116:119],v[224:227],v[20:23]
rd a[88:91],v248 offset:6144
cv v234,v212,v213
mm v[24:27],a[120:123],v[224:227],v[24:27]
ex v215,v215
mm v[56:59],a[120:123],v[228:231],v[56:59]
rd a[92:95],v248 offset:7168
cv v235,v214,v215
mm v[60:63],a[124:127],v[228:231],v[60:63]
ex v220,v220
mm v[28:31],a[124:127],v[224:227],v[28:31]
.La5_LA_DC3:
.La5_LW5_2:
)ASM" R"ASM(s_cbranch_execz .La5_LW5_R0
wt lgkmcnt(4)
mm v[192:195],a[64:67],v[128:131],0
ex v221,v221
mm v[192:195],a[68:71],v[132:135],v[192:195]
cv v238,v220,v221
ar v240,a228
mm v[192:195],a[72:75],v[136:139],v[192:195]
ex v222,v222
mm v[192:195],a[76:79],v[140:143],v[192:195]
s_add_u32 m0, s83, 16896
s_lshl_b32 s17, s72, 18
mm v[200:203],a[76:79],v[156:159],0
ex v223,v223
mm v[200:203],a[72:75],v[152:155],v[200:203]
cv v239,v222,v223
s_add_u32 s89, s89, 16384
mm v[200:203],a[68:71],v[148:151],v[200:203]
ex v192,v192
mm v[200:203],a[64:67],v[144:147],v[200:203]
wt lgkmcnt(0)
s_and_b32 s89, s89, 0x1ffff
mm v[196:199],a[80:83],v[128:131],0
ex v193,v193
mm v[196:199],a[84:87],v[132:135],v[196:199]
cv v224,v192,v193
mm v[196:199],a[88:91],v[136:139],v[196:199]
ex v194,v194
mm v[196:199],a[92:95],v[140:143],v[196:199]
ld v240,s[56:59],s17 offen lds
mm v[204:207],a[92:95],v[156:159],0
ex v195,v195
s_add_u32 s17, s17, 32768
mm v[204:207],a[88:91],v[152:155],v[204:207]
cv v225,v194,v195
s_add_u32 m0, m0, 1024
mm v[204:207],a[84:87],v[148:151],v[204:207]
ex v200,v200
mm v[204:207],a[80:83],v[144:147],v[204:207]
ld v240,s[56:59],s17 offen lds
mm v[208:211],a[64:67],v[160:163],0
ex v201,v201
s_add_u32 s17, s17, 4294942720
mm v[208:211],a[68:71],v[164:167],v[208:211]
cv v228,v200,v201
s_add_u32 m0, m0, 1152
mm v[208:211],a[72:75],v[168:171],v[208:211]
ex v202,v202
mm v[208:211],a[76:79],v[172:175],v[208:211]
ld v240,s[56:59],s17 offen lds
mm v[216:219],a[76:79],v[188:191],0
ex v203,v203
s_add_u32 s17, s17, 32768
mm v[216:219],a[72:75],v[184:187],v[216:219]
cv v229,v202,v203
s_add_u32 m0, m0, 1024
mm v[216:219],a[68:71],v[180:183],v[216:219]
ex v196,v196
mm v[216:219],a[64:67],v[176:179],v[216:219]
ld v240,s[56:59],s17 offen lds
mm v[212:215],a[80:83],v[160:163],0
ex v197,v197
s_add_u32 s17, s17, 90112
mm v[212:215],a[84:87],v[164:167],v[212:215]
cv v226,v196,v197
s_add_u32 m0, s83, 33792
mm v[212:215],a[88:91],v[168:171],v[212:215]
ex v198,v198
mm v[212:215],a[92:95],v[172:175],v[212:215]
mm v[220:223],a[92:95],v[188:191],0
ex v199,v199
mm v[220:223],a[88:91],v[184:187],v[220:223]
cv v227,v198,v199
mm v[220:223],a[84:87],v[180:183],v[220:223]
ex v204,v204
mm v[220:223],a[80:83],v[176:179],v[220:223]
wt vmcnt(10)
s_barrier
mm v[64:67],a[96:99],v[232:235],v[64:67]
ex v205,v205
mm v[96:99],a[96:99],v[236:239],v[96:99]
rd a[96:99],v248 offset:8192
cv v230,v204,v205
mm v[100:103],a[100:103],v[236:239],v[100:103]
ex v206,v206
mm v[68:71],a[100:103],v[232:235],v[68:71]
rd a[100:103],v248 offset:9216
mm v[72:75],a[104:107],v[232:235],v[72:75]
ex v207,v207
mm v[104:107],a[104:107],v[236:239],v[104:107]
rd a[104:107],v248 offset:10240
cv v231,v206,v207
mm v[108:111],a[108:111],v[236:239],v[108:111]
ex v208,v208
mm v[76:79],a[108:111],v[232:235],v[76:79]
rd a[108:111],v248 offset:11264
mm v[80:83],a[112:115],v[232:235],v[80:83]
ex v209,v209
mm v[112:115],a[112:115],v[236:239],v[112:115]
rd a[112:115],v248 offset:12288
)ASM" R"ASM(mm v[116:119],a[116:119],v[236:239],v[116:119]
ex v210,v210
mm v[84:87],a[116:119],v[232:235],v[84:87]
rd a[116:119],v248 offset:13312
mm v[88:91],a[120:123],v[232:235],v[88:91]
ex v211,v211
mm v[120:123],a[120:123],v[236:239],v[120:123]
rd a[120:123],v248 offset:14336
mm v[124:127],a[124:127],v[236:239],v[124:127]
ex v216,v216
mm v[92:95],a[124:127],v[232:235],v[92:95]
rd a[124:127],v248 offset:15360
v_or_b32 v248, s89, v255
v_smfmac_f32_16x16x64_bf16 a[196:199], a[208:211], v[232:239], v241
wt lgkmcnt(4)
v_smfmac_f32_16x16x64_bf16 a[192:195], a[208:211], v[224:231], v241
rd a[64:67],v248 offset:0
mm v[0:3],a[96:99],v[224:227],v[0:3]
ex v217,v217
mm v[32:35],a[96:99],v[228:231],v[32:35]
rd a[68:71],v248 offset:1024
cv v232,v208,v209
mm v[36:39],a[100:103],v[228:231],v[36:39]
ex v218,v218
mm v[4:7],a[100:103],v[224:227],v[4:7]
rd a[72:75],v248 offset:2048
cv v233,v210,v211
mm v[8:11],a[104:107],v[224:227],v[8:11]
ex v219,v219
mm v[40:43],a[104:107],v[228:231],v[40:43]
rd a[76:79],v248 offset:3072
cv v236,v216,v217
mm v[44:47],a[108:111],v[228:231],v[44:47]
ex v212,v212
mm v[12:15],a[108:111],v[224:227],v[12:15]
rd a[80:83],v248 offset:4096
wt lgkmcnt(5)
mm v[16:19],a[112:115],v[224:227],v[16:19]
ex v213,v213
mm v[48:51],a[112:115],v[228:231],v[48:51]
rd a[84:87],v248 offset:5120
cv v237,v218,v219
mm v[52:55],a[116:119],v[228:231],v[52:55]
ex v214,v214
mm v[20:23],a[116:119],v[224:227],v[20:23]
rd a[88:91],v248 offset:6144
cv v234,v212,v213
mm v[24:27],a[120:123],v[224:227],v[24:27]
ex v215,v215
mm v[56:59],a[120:123],v[228:231],v[56:59]
rd a[92:95],v248 offset:7168
cv v235,v214,v215
mm v[60:63],a[124:127],v[228:231],v[60:63]
ex v220,v220
mm v[28:31],a[124:127],v[224:227],v[28:31]
buffer_load_dword v250, v251, s[64:67], s20 offen sc1
wt lgkmcnt(4)
mm v[192:195],a[64:67],v[128:131],0
ex v221,v221
mm v[192:195],a[68:71],v[132:135],v[192:195]
cv v238,v220,v221
s_add_u32 s89, s89, 16384
mm v[192:195],a[72:75],v[136:139],v[192:195]
ex v222,v222
mm v[192:195],a[76:79],v[140:143],v[192:195]
ld v240,s[56:59],s17 offen lds
mm v[200:203],a[76:79],v[156:159],0
ex v223,v223
s_add_u32 s17, s17, 32768
mm v[200:203],a[72:75],v[152:155],v[200:203]
cv v239,v222,v223
s_add_u32 m0, m0, 1024
mm v[200:203],a[68:71],v[148:151],v[200:203]
ex v192,v192
mm v[200:203],a[64:67],v[144:147],v[200:203]
wt lgkmcnt(0)
s_and_b32 s89, s89, 0x1ffff
mm v[196:199],a[80:83],v[128:131],0
ex v193,v193
mm v[196:199],a[84:87],v[132:135],v[196:199]
cv v224,v192,v193
mm v[196:199],a[88:91],v[136:139],v[196:199]
ex v194,v194
mm v[196:199],a[92:95],v[140:143],v[196:199]
ld v240,s[56:59],s17 offen lds
mm v[204:207],a[92:95],v[156:159],0
ex v195,v195
s_add_u32 s17, s17, 4294942720
mm v[204:207],a[88:91],v[152:155],v[204:207]
cv v225,v194,v195
s_add_u32 m0, m0, 1152
mm v[204:207],a[84:87],v[148:151],v[204:207]
ex v200,v200
mm v[204:207],a[80:83],v[144:147],v[204:207]
ld v240,s[56:59],s17 offen lds
mm v[208:211],a[64:67],v[160:163],0
ex v201,v201
s_add_u32 s17, s17, 32768
mm v[208:211],a[68:71],v[164:167],v[208:211]
cv v228,v200,v201
)ASM" R"ASM(s_add_u32 m0, m0, 1024
mm v[208:211],a[72:75],v[168:171],v[208:211]
ex v202,v202
mm v[208:211],a[76:79],v[172:175],v[208:211]
ld v240,s[56:59],s17 offen lds
mm v[216:219],a[76:79],v[188:191],0
ex v203,v203
mm v[216:219],a[72:75],v[184:187],v[216:219]
cv v229,v202,v203
mm v[216:219],a[68:71],v[180:183],v[216:219]
ex v196,v196
mm v[216:219],a[64:67],v[176:179],v[216:219]
mm v[212:215],a[80:83],v[160:163],0
ex v197,v197
s_bfe_u32 s17, s4, 262148
mm v[212:215],a[84:87],v[164:167],v[212:215]
cv v226,v196,v197
s_xor_b32 s17, s17, s72
s_lshl_b32 s17, s17, 18
mm v[212:215],a[88:91],v[168:171],v[212:215]
ex v198,v198
s_add_u32 m0, s83, 50688
mm v[212:215],a[92:95],v[172:175],v[212:215]
mm v[220:223],a[92:95],v[188:191],0
ex v199,v199
mm v[220:223],a[88:91],v[184:187],v[220:223]
cv v227,v198,v199
wt vmcnt(4)
v_cmp_eq_u32 s[92:93], v250, v249
s_mov_b32 s94, 16384
mm v[220:223],a[84:87],v[180:183],v[220:223]
ex v204,v204
mm v[220:223],a[80:83],v[176:179],v[220:223]
s_nop 0
s_barrier
mm v[64:67],a[96:99],v[232:235],v[64:67]
ex v205,v205
mm v[96:99],a[96:99],v[236:239],v[96:99]
rd a[96:99],v248 offset:8192
cv v230,v204,v205
s_and_b32 s92, s92, 3
s_andn2_b32 s92, s92, s23
s_cmp_eq_u32 s92, 3
s_cselect_b32 s70, 17408, 0
s_cselect_b32 s84, 3, s23
mm v[100:103],a[100:103],v[236:239],v[100:103]
ex v206,v206
mm v[68:71],a[100:103],v[232:235],v[68:71]
rd a[100:103],v248 offset:9216
ld a[220:223],v255,s[68:71],s94 offen sc1
mm v[72:75],a[104:107],v[232:235],v[72:75]
ex v207,v207
mm v[104:107],a[104:107],v[236:239],v[104:107]
rd a[104:107],v248 offset:10240
cv v231,v206,v207
mm v[108:111],a[108:111],v[236:239],v[108:111]
ex v208,v208
mm v[76:79],a[108:111],v[232:235],v[76:79]
rd a[108:111],v248 offset:11264
ld a[0:3],v255,s[68:71],0 offen offset:0 sc1
mm v[80:83],a[112:115],v[232:235],v[80:83]
ex v209,v209
mm v[112:115],a[112:115],v[236:239],v[112:115]
rd a[112:115],v248 offset:12288
mm v[116:119],a[116:119],v[236:239],v[116:119]
ex v210,v210
mm v[84:87],a[116:119],v[232:235],v[84:87]
rd a[116:119],v248 offset:13312
ld a[4:7],v255,s[68:71],0 offen offset:1024 sc1
mm v[88:91],a[120:123],v[232:235],v[88:91]
ex v211,v211
mm v[120:123],a[120:123],v[236:239],v[120:123]
rd a[120:123],v248 offset:14336
mm v[124:127],a[124:127],v[236:239],v[124:127]
ex v216,v216
mm v[92:95],a[124:127],v[232:235],v[92:95]
rd a[124:127],v248 offset:15360
v_or_b32 v248, s89, v255
buffer_load_dword v250, v251, s[64:67], s20 offen sc1
v_smfmac_f32_16x16x64_bf16 a[196:199], a[208:211], v[232:239], v241
wt lgkmcnt(4)
v_smfmac_f32_16x16x64_bf16 a[192:195], a[208:211], v[224:231], v241
rd a[64:67],v248 offset:0
mm v[0:3],a[96:99],v[224:227],v[0:3]
ex v217,v217
mm v[32:35],a[96:99],v[228:231],v[32:35]
rd a[68:71],v248 offset:1024
cv v232,v208,v209
ld a[8:11],v255,s[68:71],0 offen offset:2048 sc1
mm v[36:39],a[100:103],v[228:231],v[36:39]
ex v218,v218
mm v[4:7],a[100:103],v[224:227],v[4:7]
rd a[72:75],v248 offset:2048
cv v233,v210,v211
mm v[8:11],a[104:107],v[224:227],v[8:11]
ex v219,v219
mm v[40:43],a[104:107],v[228:231],v[40:43]
rd a[76:79],v248 offset:3072
cv v236,v216,v217
ld a[12:15],v255,s[68:71],0 offen offset:3072 sc1
)ASM" R"ASM(mm v[44:47],a[108:111],v[228:231],v[44:47]
ex v212,v212
mm v[12:15],a[108:111],v[224:227],v[12:15]
rd a[80:83],v248 offset:4096
wt lgkmcnt(5)
mm v[16:19],a[112:115],v[224:227],v[16:19]
ex v213,v213
mm v[48:51],a[112:115],v[228:231],v[48:51]
rd a[84:87],v248 offset:5120
cv v237,v218,v219
ld a[16:19],v255,s[68:71],s95 offen offset:0 sc1
mm v[52:55],a[116:119],v[228:231],v[52:55]
ex v214,v214
mm v[20:23],a[116:119],v[224:227],v[20:23]
rd a[88:91],v248 offset:6144
cv v234,v212,v213
mm v[24:27],a[120:123],v[224:227],v[24:27]
ex v215,v215
mm v[56:59],a[120:123],v[228:231],v[56:59]
rd a[92:95],v248 offset:7168
cv v235,v214,v215
ld a[20:23],v255,s[68:71],s95 offen offset:1024 sc1
mm v[60:63],a[124:127],v[228:231],v[60:63]
ex v220,v220
mm v[28:31],a[124:127],v[224:227],v[28:31]
ld a[24:27],v255,s[68:71],s95 offen offset:2048 sc1
wt lgkmcnt(4)
mm v[192:195],a[64:67],v[128:131],0
ex v221,v221
mm v[192:195],a[68:71],v[132:135],v[192:195]
cv v238,v220,v221
mm v[192:195],a[72:75],v[136:139],v[192:195]
ex v222,v222
mm v[192:195],a[76:79],v[140:143],v[192:195]
ld v240,s[56:59],s17 offen lds
mm v[200:203],a[76:79],v[156:159],0
ex v223,v223
s_add_u32 s17, s17, 32768
mm v[200:203],a[72:75],v[152:155],v[200:203]
cv v239,v222,v223
s_add_u32 m0, m0, 1024
mm v[200:203],a[68:71],v[148:151],v[200:203]
ex v192,v192
mm v[200:203],a[64:67],v[144:147],v[200:203]
wt lgkmcnt(0)
mm v[196:199],a[80:83],v[128:131],0
ex v193,v193
mm v[196:199],a[84:87],v[132:135],v[196:199]
cv v224,v192,v193
wt vmcnt(6)
v_cmp_eq_u32 s[92:93], v250, v249
mm v[196:199],a[88:91],v[136:139],v[196:199]
ex v194,v194
mm v[196:199],a[92:95],v[140:143],v[196:199]
ld v240,s[56:59],s17 offen lds
mm v[204:207],a[92:95],v[156:159],0
ex v195,v195
s_add_u32 s17, s17, 4294942720
mm v[204:207],a[88:91],v[152:155],v[204:207]
cv v225,v194,v195
s_add_u32 m0, m0, 1152
s_and_b32 s92, s92, 3
s_sub_u32 s92, s92, s84
s_cmp_eq_u32 s92, 3
s_cbranch_scc1 .La5_LA_PS0
.La5_LA_PS1:
mm v[204:207],a[84:87],v[148:151],v[204:207]
ex v200,v200
mm v[204:207],a[80:83],v[144:147],v[204:207]
ld v240,s[56:59],s17 offen lds
mm v[208:211],a[64:67],v[160:163],0
ex v201,v201
s_add_u32 s17, s17, 32768
mm v[208:211],a[68:71],v[164:167],v[208:211]
cv v228,v200,v201
s_add_u32 m0, m0, 1024
ld a[28:31],v255,s[68:71],s95 offen offset:3072 sc1
mm v[208:211],a[72:75],v[168:171],v[208:211]
ex v202,v202
mm v[208:211],a[76:79],v[172:175],v[208:211]
ld v240,s[56:59],s17 offen lds
mm v[216:219],a[76:79],v[188:191],0
ex v203,v203
mm v[216:219],a[72:75],v[184:187],v[216:219]
cv v229,v202,v203
ld a[32:35],v255,s[68:71],s96 offen offset:0 sc1
mm v[216:219],a[68:71],v[180:183],v[216:219]
ex v196,v196
mm v[216:219],a[64:67],v[176:179],v[216:219]
mm v[212:215],a[80:83],v[160:163],0
ex v197,v197
mm v[212:215],a[84:87],v[164:167],v[212:215]
cv v226,v196,v197
buffer_load_dword v250, v251, s[64:67], s20 offen sc1
mm v[212:215],a[88:91],v[168:171],v[212:215]
ex v198,v198
mm v[212:215],a[92:95],v[172:175],v[212:215]
ld a[36:39],v255,s[68:71],s96 offen offset:1024 sc1
mm v[220:223],a[92:95],v[188:191],0
ex v199,v199
)ASM" R"ASM(mm v[220:223],a[88:91],v[184:187],v[220:223]
cv v227,v198,v199
mm v[220:223],a[84:87],v[180:183],v[220:223]
ex v204,v204
mm v[220:223],a[80:83],v[176:179],v[220:223]
ld a[40:43],v255,s[68:71],s96 offen offset:2048 sc1
mm v[64:67],a[96:99],v[232:235],v[64:67]
ex v205,v205
mm v[96:99],a[96:99],v[236:239],v[96:99]
rd a[96:99],v248 offset:8192
cv v230,v204,v205
mm v[100:103],a[100:103],v[236:239],v[100:103]
ex v206,v206
mm v[68:71],a[100:103],v[232:235],v[68:71]
rd a[100:103],v248 offset:9216
ld a[44:47],v255,s[68:71],s96 offen offset:3072 sc1
mm v[72:75],a[104:107],v[232:235],v[72:75]
ex v207,v207
mm v[104:107],a[104:107],v[236:239],v[104:107]
rd a[104:107],v248 offset:10240
cv v231,v206,v207
mm v[108:111],a[108:111],v[236:239],v[108:111]
ex v208,v208
mm v[76:79],a[108:111],v[232:235],v[76:79]
rd a[108:111],v248 offset:11264
ld a[48:51],v255,s[68:71],s97 offen offset:0 sc1
mm v[80:83],a[112:115],v[232:235],v[80:83]
ex v209,v209
mm v[112:115],a[112:115],v[236:239],v[112:115]
rd a[112:115],v248 offset:12288
mm v[116:119],a[116:119],v[236:239],v[116:119]
ex v210,v210
mm v[84:87],a[116:119],v[232:235],v[84:87]
rd a[116:119],v248 offset:13312
ld a[52:55],v255,s[68:71],s97 offen offset:1024 sc1
mm v[88:91],a[120:123],v[232:235],v[88:91]
ex v211,v211
mm v[120:123],a[120:123],v[236:239],v[120:123]
rd a[120:123],v248 offset:14336
mm v[124:127],a[124:127],v[236:239],v[124:127]
ex v216,v216
mm v[92:95],a[124:127],v[232:235],v[92:95]
rd a[124:127],v248 offset:15360
ld a[56:59],v255,s[68:71],s97 offen offset:2048 sc1
v_smfmac_f32_16x16x64_bf16 a[196:199], a[208:211], v[232:239], v241
wt lgkmcnt(4)
v_smfmac_f32_16x16x64_bf16 a[192:195], a[208:211], v[224:231], v241
ld a[60:63],v255,s[68:71],s97 offen offset:3072 sc1
mm v[0:3],a[96:99],v[224:227],v[0:3]
ex v217,v217
mm v[32:35],a[96:99],v[228:231],v[32:35]
cv v232,v208,v209
mm v[36:39],a[100:103],v[228:231],v[36:39]
ex v218,v218
mm v[4:7],a[100:103],v[224:227],v[4:7]
cv v233,v210,v211
mm v[8:11],a[104:107],v[224:227],v[8:11]
ex v219,v219
mm v[40:43],a[104:107],v[228:231],v[40:43]
cv v236,v216,v217
mm v[44:47],a[108:111],v[228:231],v[44:47]
ex v212,v212
wt vmcnt(7)
v_cmp_eq_u32 s[92:93], v250, v249
mm v[12:15],a[108:111],v[224:227],v[12:15]
wt lgkmcnt(0)
mm v[16:19],a[112:115],v[224:227],v[16:19]
ex v213,v213
mm v[48:51],a[112:115],v[228:231],v[48:51]
cv v237,v218,v219
s_and_b32 s92, s92, 3
s_sub_u32 s92, s92, s84
mm v[52:55],a[116:119],v[228:231],v[52:55]
ex v214,v214
mm v[20:23],a[116:119],v[224:227],v[20:23]
cv v234,v212,v213
mm v[24:27],a[120:123],v[224:227],v[24:27]
ex v215,v215
mm v[56:59],a[120:123],v[228:231],v[56:59]
cv v235,v214,v215
mm v[60:63],a[124:127],v[228:231],v[60:63]
ex v220,v220
mm v[28:31],a[124:127],v[224:227],v[28:31]
s_cmp_eq_u32 s92, 3
s_cbranch_scc1 .La5_LA_PS2
.La5_LA_PS3:
s_cmp_lg_u32 s84, 3
s_cbranch_scc1 .La5_LA_PSPIN
; ---- owner: partner-s part added, l checked, O / l leaves; first steps of the O projection
.La5_LA_DC1:
wt vmcnt(8)
mm v[0:3],a[214:217],a[0:3],v[0:3]
ar v250,a192
ar v251,a193
v_add_f32 v250, v250, v251
ar v251,a220
v_add_f32 v250, v250, v251
ar v249,a194
mm v[4:7],a[212:215],a[0:3],v[4:7]
ar v251,a195
)ASM" R"ASM(v_add_f32 v249, v249, v251
ar v251,a221
v_add_f32 v249, v249, v251
v_subrev_u32 v251, 562036736, v250
mm v[8:11],a[214:217],a[4:7],v[8:11]
v_rcp_f32 v250, v250
v_cmp_le_u32 vcc, 1006632960, v251
v_subrev_u32 v251, 562036736, v249
s_mov_b64 s[92:93], vcc
v_cmp_le_u32 vcc, 1006632960, v251
mm v[12:15],a[212:215],a[4:7],v[12:15]
v_rcp_f32 v249, v249
mm v[16:19],a[214:217],a[8:11],v[16:19]
s_or_b64 vcc, vcc, s[92:93]
s_cbranch_scc1 .La5_LA_SC3
.La5_LA_SR3:
ex v221,v221
ex v222,v222
mm v[20:23],a[212:215],a[8:11],v[20:23]
ex v223,v223
cv v238,v220,v221
v_mul_f32 v0, v0, v250
v_mul_f32 v1, v1, v250
v_mul_f32 v2, v2, v250
mm v[24:27],a[214:217],a[12:15],v[24:27]
v_mul_f32 v3, v3, v250
v_mul_f32 v4, v4, v250
v_mul_f32 v5, v5, v250
v_mul_f32 v6, v6, v250
v_mul_f32 v7, v7, v250
cv v128,v0,v1
mm v[28:31],a[212:215],a[12:15],v[28:31]
cv v129,v2,v3
cv v130,v4,v5
cv v131,v6,v7
.La5_LW5_3:
s_cbranch_execz .La5_LW5_R0
st v[128:131],v244,s[44:47],s91 offen offset:0 sc1
cv v239,v222,v223
mm v[32:35],a[214:217],a[16:19],v[32:35]
v_mul_f32 v8, v8, v250
v_mul_f32 v9, v9, v250
v_mul_f32 v10, v10, v250
v_mul_f32 v11, v11, v250
v_mul_f32 v12, v12, v250
v_mul_f32 v13, v13, v250
mm v[36:39],a[212:215],a[16:19],v[36:39]
v_mul_f32 v14, v14, v250
v_mul_f32 v15, v15, v250
cv v132,v8,v9
cv v133,v10,v11
cv v134,v12,v13
cv v135,v14,v15
mm v[40:43],a[214:217],a[20:23],v[40:43]
st v[132:135],v244,s[44:47],s91 offen offset:1024 sc1
v_mul_f32 v16, v16, v250
v_mul_f32 v17, v17, v250
v_mul_f32 v18, v18, v250
v_mul_f32 v19, v19, v250
mm v[44:47],a[212:215],a[20:23],v[44:47]
v_mul_f32 v20, v20, v250
v_mul_f32 v21, v21, v250
v_mul_f32 v22, v22, v250
v_mul_f32 v23, v23, v250
cv v136,v16,v17
cv v137,v18,v19
mm v[48:51],a[214:217],a[24:27],v[48:51]
cv v138,v20,v21
cv v139,v22,v23
st v[136:139],v244,s[44:47],s91 offen offset:2048 sc1
v_mul_f32 v24, v24, v250
v_mul_f32 v25, v25, v250
mm v[52:55],a[212:215],a[24:27],v[52:55]
v_mul_f32 v26, v26, v250
v_mul_f32 v27, v27, v250
v_mul_f32 v28, v28, v250
v_mul_f32 v29, v29, v250
v_mul_f32 v30, v30, v250
v_mul_f32 v31, v31, v250
mm v[56:59],a[214:217],a[28:31],v[56:59]
cv v140,v24,v25
cv v141,v26,v27
cv v142,v28,v29
cv v143,v30,v31
st v[140:143],v244,s[44:47],s91 offen offset:3072 sc1
mm v[60:63],a[212:215],a[28:31],v[60:63]
v_mul_f32 v32, v32, v249
v_mul_f32 v33, v33, v249
v_mul_f32 v34, v34, v249
v_mul_f32 v35, v35, v249
v_mul_f32 v36, v36, v249
v_mul_f32 v37, v37, v249
v_smfmac_f32_16x16x64_bf16 a[196:199], a[208:211], v[232:239], v241
v_mul_f32 v38, v38, v249
v_mul_f32 v39, v39, v249
cv v144,v32,v33
cv v145,v34,v35
cv v146,v36,v37
mm v[64:67],a[96:99],v[232:235],v[64:67]
cv v147,v38,v39
)ASM" R"ASM(st v[144:147],v245,s[44:47],s91 offen offset:0 sc1
v_mul_f32 v40, v40, v249
v_mul_f32 v41, v41, v249
v_mul_f32 v42, v42, v249
mm v[96:99],a[96:99],v[236:239],v[96:99]
v_mul_f32 v43, v43, v249
v_mul_f32 v44, v44, v249
v_mul_f32 v45, v45, v249
v_mul_f32 v46, v46, v249
v_mul_f32 v47, v47, v249
cv v148,v40,v41
mm v[100:103],a[100:103],v[236:239],v[100:103]
cv v149,v42,v43
cv v150,v44,v45
cv v151,v46,v47
st v[148:151],v245,s[44:47],s91 offen offset:1024 sc1
v_mul_f32 v48, v48, v249
mm v[68:71],a[100:103],v[232:235],v[68:71]
v_mul_f32 v49, v49, v249
v_mul_f32 v50, v50, v249
v_mul_f32 v51, v51, v249
v_mul_f32 v52, v52, v249
v_mul_f32 v53, v53, v249
v_mul_f32 v54, v54, v249
mm v[72:75],a[104:107],v[232:235],v[72:75]
v_mul_f32 v55, v55, v249
cv v152,v48,v49
cv v153,v50,v51
cv v154,v52,v53
cv v155,v54,v55
st v[152:155],v245,s[44:47],s91 offen offset:2048 sc1
mm v[104:107],a[104:107],v[236:239],v[104:107]
v_mul_f32 v56, v56, v249
v_mul_f32 v57, v57, v249
v_mul_f32 v58, v58, v249
v_mul_f32 v59, v59, v249
v_mul_f32 v60, v60, v249
v_mul_f32 v61, v61, v249
mm v[108:111],a[108:111],v[236:239],v[108:111]
v_mul_f32 v62, v62, v249
v_mul_f32 v63, v63, v249
cv v156,v56,v57
cv v157,v58,v59
cv v158,v60,v61
cv v159,v62,v63
mm v[76:79],a[108:111],v[232:235],v[76:79]
st v[156:159],v245,s[44:47],s91 offen offset:3072 sc1
ar v240,a224
v_xor_b32 v241, 32, v240
v_xor_b32 v242, 64, v240
v_xor_b32 v243, 96, v240
mm v[80:83],a[112:115],v[232:235],v[80:83]
mm v[112:115],a[112:115],v[236:239],v[112:115]
mm v[116:119],a[116:119],v[236:239],v[116:119]
mm v[84:87],a[116:119],v[232:235],v[84:87]
mm v[88:91],a[120:123],v[232:235],v[88:91]
mm v[120:123],a[120:123],v[236:239],v[120:123]
mm v[124:127],a[124:127],v[236:239],v[124:127]
mm v[92:95],a[124:127],v[232:235],v[92:95]
s_nop 0
s_barrier
wt vmcnt(8)
mm v[64:67],a[214:217],a[32:35],v[64:67]
rt v[192:193],v240 offset:16896
rt v[194:195],v240 offset:17920
mm v[68:71],a[212:215],a[32:35],v[68:71]
rt v[196:197],v240 offset:25344
rt v[198:199],v240 offset:26368
mm v[72:75],a[214:217],a[36:39],v[72:75]
rt v[200:201],v241 offset:16896
rt v[202:203],v241 offset:17920
ar v250,a196
ar v251,a197
v_add_f32 v250, v250, v251
ar v251,a222
mm v[76:79],a[212:215],a[36:39],v[76:79]
rt v[204:205],v241 offset:25344
rt v[206:207],v241 offset:26368
v_add_f32 v250, v250, v251
ar v249,a198
ar v251,a199
v_add_f32 v249, v249, v251
mm v[80:83],a[214:217],a[40:43],v[80:83]
rt v[208:209],v242 offset:16896
rt v[210:211],v242 offset:17920
ar v251,a223
v_add_f32 v249, v249, v251
v_subrev_u32 v251, 562036736, v250
mm v[84:87],a[212:215],a[40:43],v[84:87]
rt v[212:213],v242 offset:25344
rt v[214:215],v242 offset:26368
v_rcp_f32 v250, v250
v_cmp_le_u32 vcc, 1006632960, v251
v_subrev_u32 v251, 562036736, v249
mm v[88:91],a[214:217],a[44:47],v[88:91]
rt v[216:217],v243 offset:16896
rt v[218:219],v243 offset:17920
s_mov_b64 s[92:93], vcc
v_cmp_le_u32 vcc, 1006632960, v251
v_rcp_f32 v249, v249
)ASM" R"ASM(mm v[92:95],a[212:215],a[44:47],v[92:95]
rt v[220:221],v243 offset:25344
rt v[222:223],v243 offset:26368
s_or_b64 vcc, vcc, s[92:93]
s_cbranch_scc1 .La5_LA_SC4
.La5_LA_SR4:
v_mul_f32 v64, v64, v250
v_mul_f32 v65, v65, v250
mm v[96:99],a[214:217],a[48:51],v[96:99]
v_mul_f32 v66, v66, v250
v_mul_f32 v67, v67, v250
v_mul_f32 v68, v68, v250
v_mul_f32 v69, v69, v250
v_mul_f32 v70, v70, v250
v_mul_f32 v71, v71, v250
mm v[100:103],a[212:215],a[48:51],v[100:103]
cv v160,v64,v65
cv v161,v66,v67
cv v162,v68,v69
cv v163,v70,v71
st v[160:163],v246,s[44:47],s91 offen offset:0 sc1
mm v[104:107],a[214:217],a[52:55],v[104:107]
v_mul_f32 v72, v72, v250
v_mul_f32 v73, v73, v250
v_mul_f32 v74, v74, v250
v_mul_f32 v75, v75, v250
v_mul_f32 v76, v76, v250
v_mul_f32 v77, v77, v250
mm v[108:111],a[212:215],a[52:55],v[108:111]
v_mul_f32 v78, v78, v250
v_mul_f32 v79, v79, v250
cv v164,v72,v73
cv v165,v74,v75
cv v166,v76,v77
cv v167,v78,v79
mm v[112:115],a[214:217],a[56:59],v[112:115]
st v[164:167],v246,s[44:47],s91 offen offset:1024 sc1
v_mul_f32 v80, v80, v250
v_mul_f32 v81, v81, v250
v_mul_f32 v82, v82, v250
v_mul_f32 v83, v83, v250
mm v[116:119],a[212:215],a[56:59],v[116:119]
v_mul_f32 v84, v84, v250
v_mul_f32 v85, v85, v250
v_mul_f32 v86, v86, v250
v_mul_f32 v87, v87, v250
cv v168,v80,v81
cv v169,v82,v83
mm v[120:123],a[214:217],a[60:63],v[120:123]
cv v170,v84,v85
cv v171,v86,v87
st v[168:171],v246,s[44:47],s91 offen offset:2048 sc1
v_mul_f32 v88, v88, v250
v_mul_f32 v89, v89, v250
mm v[124:127],a[212:215],a[60:63],v[124:127]
wt lgkmcnt(12)
v_mul_f32 v90, v90, v250
v_mul_f32 v91, v91, v250
v_mul_f32 v92, v92, v250
v_mul_f32 v93, v93, v250
v_mul_f32 v94, v94, v250
mm v[0:3],v[192:195],v[128:131],0
v_mul_f32 v95, v95, v250
cv v172,v88,v89
cv v173,v90,v91
cv v174,v92,v93
cv v175,v94,v95
st v[172:175],v246,s[44:47],s91 offen offset:3072 sc1
mm v[0:3],v[196:199],v[132:135],v[0:3]
v_mul_f32 v96, v96, v249
v_mul_f32 v97, v97, v249
v_mul_f32 v98, v98, v249
v_mul_f32 v99, v99, v249
v_mul_f32 v100, v100, v249
v_mul_f32 v101, v101, v249
mm v[32:35],v[196:199],v[148:151],0
wt lgkmcnt(8)
v_mul_f32 v102, v102, v249
v_mul_f32 v103, v103, v249
cv v176,v96,v97
cv v177,v98,v99
cv v178,v100,v101
mm v[32:35],v[192:195],v[144:147],v[32:35]
rt v[196:197],v240 offset:17024
rt v[198:199],v240 offset:18048
cv v179,v102,v103
st v[176:179],v247,s[44:47],s91 offen offset:0 sc1
v_mul_f32 v104, v104, v249
mm v[36:39],v[200:203],v[144:147],0
v_mul_f32 v105, v105, v249
v_mul_f32 v106, v106, v249
v_mul_f32 v107, v107, v249
v_mul_f32 v108, v108, v249
v_mul_f32 v109, v109, v249
v_mul_f32 v110, v110, v249
mm v[36:39],v[204:207],v[148:151],v[36:39]
rt v[192:193],v240 offset:25472
rt v[194:195],v240 offset:26496
v_mul_f32 v111, v111, v249
cv v180,v104,v105
)ASM" R"ASM(cv v181,v106,v107
cv v182,v108,v109
mm v[4:7],v[204:207],v[132:135],0
wt lgkmcnt(4)
cv v183,v110,v111
st v[180:183],v247,s[44:47],s91 offen offset:1024 sc1
v_mul_f32 v112, v112, v249
v_mul_f32 v113, v113, v249
mm v[4:7],v[200:203],v[128:131],v[4:7]
rt v[204:205],v241 offset:17024
rt v[206:207],v241 offset:18048
v_mul_f32 v114, v114, v249
v_mul_f32 v115, v115, v249
v_mul_f32 v116, v116, v249
v_mul_f32 v117, v117, v249
mm v[8:11],v[208:211],v[128:131],0
v_mul_f32 v118, v118, v249
v_mul_f32 v119, v119, v249
cv v184,v112,v113
cv v185,v114,v115
cv v186,v116,v117
cv v187,v118,v119
mm v[8:11],v[212:215],v[132:135],v[8:11]
rt v[200:201],v241 offset:25472
rt v[202:203],v241 offset:26496
st v[184:187],v247,s[44:47],s91 offen offset:2048 sc1
v_mul_f32 v120, v120, v249
v_mul_f32 v121, v121, v249
mm v[40:43],v[212:215],v[148:151],0
v_mul_f32 v122, v122, v249
v_mul_f32 v123, v123, v249
v_mul_f32 v124, v124, v249
v_mul_f32 v125, v125, v249
v_mul_f32 v126, v126, v249
v_mul_f32 v127, v127, v249
mm v[40:43],v[208:211],v[144:147],v[40:43]
rt v[212:213],v242 offset:17024
rt v[214:215],v242 offset:18048
cv v188,v120,v121
cv v189,v122,v123
cv v190,v124,v125
cv v191,v126,v127
mm v[44:47],v[216:219],v[144:147],0
st v[188:191],v247,s[44:47],s91 offen offset:3072 sc1
mm v[44:47],v[220:223],v[148:151],v[44:47]
rt v[208:209],v242 offset:25472
rt v[210:211],v242 offset:26496
mm v[12:15],v[220:223],v[132:135],0
wt lgkmcnt(4)
mm v[12:15],v[216:219],v[128:131],v[12:15]
rt v[220:221],v243 offset:17024
rt v[222:223],v243 offset:18048
mm v[16:19],v[196:199],v[128:131],0
ar v224,a228
v_add_u32 v225, 0x8000, v224
v_add_u32 v226, 0x2000, v224
v_add_u32 v227, 0xa000, v224
mm v[16:19],v[192:195],v[132:135],v[16:19]
rt v[216:217],v243 offset:25472
rt v[218:219],v243 offset:26496
ar v248,a225
ar v228,a229
mm v[48:51],v[192:195],v[148:151],0
ar v229,a230
mm v[48:51],v[196:199],v[144:147],v[48:51]
rt v[192:193],v240 offset:33792
rt v[194:195],v240 offset:34816
mm v[52:55],v[204:207],v[144:147],0
mm v[52:55],v[200:203],v[148:151],v[52:55]
rt v[196:197],v240 offset:42240
rt v[198:199],v240 offset:43264
mm v[20:23],v[200:203],v[132:135],0
wt lgkmcnt(4)
mm v[20:23],v[204:207],v[128:131],v[20:23]
rt v[200:201],v241 offset:33792
rt v[202:203],v241 offset:34816
mm v[24:27],v[212:215],v[128:131],0
mm v[24:27],v[208:211],v[132:135],v[24:27]
rt v[204:205],v241 offset:42240
rt v[206:207],v241 offset:43264
mm v[56:59],v[208:211],v[148:151],0
mm v[56:59],v[212:215],v[144:147],v[56:59]
rt v[208:209],v242 offset:33792
rt v[210:211],v242 offset:34816
mm v[60:63],v[220:223],v[144:147],0
mm v[60:63],v[216:219],v[148:151],v[60:63]
rt v[212:213],v242 offset:42240
rt v[214:215],v242 offset:43264
mm v[28:31],v[216:219],v[132:135],0
wt lgkmcnt(4)
mm v[28:31],v[220:223],v[128:131],v[28:31]
rt v[216:217],v243 offset:33792
rt v[218:219],v243 offset:34816
mm v[0:3],v[192:195],v[136:139],v[0:3]
ar v249,a231
s_bitcmp1_b32 2863311530, s2
s_cbranch_scc1 .La5_LO_VC1
s_getpc_b64 s[24:25]
.La5_LW3_P1:
s_add_u32 s24, s24, .La5_LW3_R1-.La5_LW3_P1
)ASM" R"ASM(s_addc_u32 s25, s25, 0
s_mov_b64 exec, 0
s_cmp_eq_u32 s3, 0
s_cbranch_scc1 .La5_LW3_0
s_cmp_eq_u32 s3, 1
s_cbranch_scc1 .La5_LW3_1
s_cmp_eq_u32 s3, 2
s_cbranch_scc1 .La5_LW3_2
s_branch .La5_LW3_3
.La5_LW3_R1:
s_mov_b64 exec, -1
.La5_LO_VE1:
wt vmcnt(0)
buffer_store_dwordx2 v[252:253], v249, s[64:67], s6 offen offset:0 sc1
mm v[0:3],v[196:199],v[140:143],v[0:3]
rt v[220:221],v243 offset:42240
rt v[222:223],v243 offset:43264
mm v[32:35],v[196:199],v[156:159],v[32:35]
mm v[32:35],v[192:195],v[152:155],v[32:35]
rt v[196:197],v240 offset:33920
rt v[198:199],v240 offset:34944
mm v[36:39],v[200:203],v[152:155],v[36:39]
mm v[36:39],v[204:207],v[156:159],v[36:39]
rt v[192:193],v240 offset:42368
rt v[194:195],v240 offset:43392
mm v[4:7],v[204:207],v[140:143],v[4:7]
wt lgkmcnt(4)
mm v[4:7],v[200:203],v[136:139],v[4:7]
rt v[204:205],v241 offset:33920
rt v[206:207],v241 offset:34944
mm v[8:11],v[208:211],v[136:139],v[8:11]
mm v[8:11],v[212:215],v[140:143],v[8:11]
rt v[200:201],v241 offset:42368
rt v[202:203],v241 offset:43392
mm v[40:43],v[212:215],v[156:159],v[40:43]
mm v[40:43],v[208:211],v[152:155],v[40:43]
rt v[212:213],v242 offset:33920
rt v[214:215],v242 offset:34944
mm v[44:47],v[216:219],v[152:155],v[44:47]
mm v[44:47],v[220:223],v[156:159],v[44:47]
rt v[208:209],v242 offset:42368
rt v[210:211],v242 offset:43392
mm v[12:15],v[220:223],v[140:143],v[12:15]
wt lgkmcnt(4)
mm v[12:15],v[216:219],v[136:139],v[12:15]
rt v[220:221],v243 offset:33920
rt v[222:223],v243 offset:34944
mm v[16:19],v[196:199],v[136:139],v[16:19]
mm v[16:19],v[192:195],v[140:143],v[16:19]
rt v[216:217],v243 offset:42368
rt v[218:219],v243 offset:43392
mm v[48:51],v[192:195],v[156:159],v[48:51]
mm v[48:51],v[196:199],v[152:155],v[48:51]
rt v[192:193],v240 offset:16896
rt v[194:195],v240 offset:17920
mm v[52:55],v[204:207],v[152:155],v[52:55]
mm v[52:55],v[200:203],v[156:159],v[52:55]
rt v[196:197],v240 offset:25344
rt v[198:199],v240 offset:26368
mm v[20:23],v[200:203],v[140:143],v[20:23]
wt lgkmcnt(4)
mm v[20:23],v[204:207],v[136:139],v[20:23]
rt v[200:201],v241 offset:16896
rt v[202:203],v241 offset:17920
mm v[24:27],v[212:215],v[136:139],v[24:27]
mm v[24:27],v[208:211],v[140:143],v[24:27]
rt v[204:205],v241 offset:25344
rt v[206:207],v241 offset:26368
mm v[56:59],v[208:211],v[156:159],v[56:59]
mm v[56:59],v[212:215],v[152:155],v[56:59]
rt v[208:209],v242 offset:16896
rt v[210:211],v242 offset:17920
mm v[60:63],v[220:223],v[152:155],v[60:63]
mm v[60:63],v[216:219],v[156:159],v[60:63]
rt v[212:213],v242 offset:25344
rt v[214:215],v242 offset:26368
mm v[28:31],v[216:219],v[140:143],v[28:31]
wt lgkmcnt(4)
mm v[28:31],v[220:223],v[136:139],v[28:31]
rt v[216:217],v243 offset:16896
rt v[218:219],v243 offset:17920
s_cmp_lg_u32 s30, 0
s_nop 0
s_nop 0
s_cbranch_scc1 .La5_LO_W
; ---- O projection: 256 x 128 tile over eight heads
.La5_LO_START:
.La5_LW6_6:
s_cbranch_execz .La5_LW6_R0
s_bitcmp1_b32 2863311530, s2
s_cbranch_scc1 .La5_LO_SC
; ---- O projection, even XCDs: eight and a half heads
.La5_LO_SB:
mm v[64:67],v[192:195],v[160:163],0
buffer_load_dwordx2 v[250:251], v248, s[64:67], s5 offen sc1
mm v[64:67],v[196:199],v[164:167],v[64:67]
rt v[220:221],v243 offset:25344
rt v[222:223],v243 offset:26368
mm v[96:99],v[196:199],v[180:183],0
s_add_u32 m0, s83, 0
)ASM" R"ASM(s_bfe_u32 s80, s4, 262148
mm v[96:99],v[192:195],v[176:179],v[96:99]
rt v[196:197],v240 offset:17024
rt v[198:199],v240 offset:18048
s_xor_b32 s80, s80, s72
s_lshl_b32 s80, s80, 18
s_add_u32 s80, s80, 131072
mm v[100:103],v[200:203],v[176:179],0
ld v224,s[56:59],s80 offen lds
mm v[100:103],v[204:207],v[180:183],v[100:103]
rt v[192:193],v240 offset:25472
rt v[194:195],v240 offset:26496
mm v[68:71],v[204:207],v[164:167],0
s_add_u32 m0, s83, 1024
wt lgkmcnt(4)
mm v[68:71],v[200:203],v[160:163],v[68:71]
rt v[204:205],v241 offset:17024
rt v[206:207],v241 offset:18048
mm v[72:75],v[208:211],v[160:163],0
ld v225,s[56:59],s80 offen lds
mm v[72:75],v[212:215],v[164:167],v[72:75]
rt v[200:201],v241 offset:25472
rt v[202:203],v241 offset:26496
mm v[104:107],v[212:215],v[180:183],0
s_add_u32 m0, s83, 2176
s_bfe_u32 s53, s4, 262148
mm v[104:107],v[208:211],v[176:179],v[104:107]
rt v[212:213],v242 offset:17024
rt v[214:215],v242 offset:18048
mm v[108:111],v[216:219],v[176:179],0
ld v226,s[56:59],s80 offen lds
mm v[108:111],v[220:223],v[180:183],v[108:111]
rt v[208:209],v242 offset:25472
rt v[210:211],v242 offset:26496
mm v[76:79],v[220:223],v[164:167],0
s_add_u32 m0, s83, 3200
wt lgkmcnt(4)
mm v[76:79],v[216:219],v[160:163],v[76:79]
rt v[220:221],v243 offset:17024
rt v[222:223],v243 offset:18048
mm v[80:83],v[196:199],v[160:163],0
ld v227,s[56:59],s80 offen lds
mm v[80:83],v[192:195],v[164:167],v[80:83]
rt v[216:217],v243 offset:25472
rt v[218:219],v243 offset:26496
mm v[112:115],v[192:195],v[180:183],0
s_add_u32 m0, s83, 118272
s_bfe_u32 s80, s4, 262152
mm v[112:115],v[196:199],v[176:179],v[112:115]
rt v[192:193],v240 offset:33792
rt v[194:195],v240 offset:34816
s_xor_b32 s80, s80, s72
s_lshl_b32 s80, s80, 18
mm v[116:119],v[204:207],v[176:179],0
ld v224,s[56:59],s80 offen lds
mm v[116:119],v[200:203],v[180:183],v[116:119]
rt v[196:197],v240 offset:42240
rt v[198:199],v240 offset:43264
mm v[84:87],v[200:203],v[164:167],0
s_add_u32 m0, s83, 119296
wt lgkmcnt(4)
mm v[84:87],v[204:207],v[160:163],v[84:87]
rt v[200:201],v241 offset:33792
rt v[202:203],v241 offset:34816
mm v[88:91],v[212:215],v[160:163],0
ld v225,s[56:59],s80 offen lds
mm v[88:91],v[208:211],v[164:167],v[88:91]
rt v[204:205],v241 offset:42240
rt v[206:207],v241 offset:43264
mm v[120:123],v[208:211],v[180:183],0
s_add_u32 m0, s83, 120448
s_xor_b32 s53, s53, s72
mm v[120:123],v[212:215],v[176:179],v[120:123]
rt v[208:209],v242 offset:33792
rt v[210:211],v242 offset:34816
mm v[124:127],v[220:223],v[176:179],0
ld v226,s[56:59],s80 offen lds
mm v[124:127],v[216:219],v[180:183],v[124:127]
rt v[212:213],v242 offset:42240
rt v[214:215],v242 offset:43264
mm v[92:95],v[216:219],v[164:167],0
wt vmcnt(7)
v_cmp_eq_u64 vcc, v[250:251], v[252:253]
s_nop 0
s_bitcmp1_b32 vcc_lo, 1
s_cbranch_scc0 .La5_LO_NB1
.La5_LO_KB1:
wt lgkmcnt(4)
mm v[92:95],v[220:223],v[160:163],v[92:95]
rt v[216:217],v243 offset:33792
rt v[218:219],v243 offset:34816
s_lshl_b32 s53, s53, 13
mm v[64:67],v[192:195],v[168:171],v[64:67]
ld a[0:3],v244,s[44:47],s53 offen offset:0 sc1
mm v[64:67],v[196:199],v[172:175],v[64:67]
rt v[220:221],v243 offset:42240
rt v[222:223],v243 offset:43264
mm v[96:99],v[196:199],v[188:191],v[96:99]
ld a[4:7],v245,s[44:47],s53 offen offset:0 sc1
mm v[96:99],v[192:195],v[184:187],v[96:99]
)ASM" R"ASM(rt v[196:197],v240 offset:33920
rt v[198:199],v240 offset:34944
mm v[100:103],v[200:203],v[184:187],v[100:103]
ld a[8:11],v246,s[44:47],s53 offen offset:0 sc1
mm v[100:103],v[204:207],v[188:191],v[100:103]
rt v[192:193],v240 offset:42368
rt v[194:195],v240 offset:43392
mm v[68:71],v[204:207],v[172:175],v[68:71]
wt lgkmcnt(4)
s_bfe_u32 s54, s4, 262152
mm v[68:71],v[200:203],v[168:171],v[68:71]
rt v[204:205],v241 offset:33920
rt v[206:207],v241 offset:34944
mm v[72:75],v[208:211],v[168:171],v[72:75]
ld a[12:15],v247,s[44:47],s53 offen offset:0 sc1
mm v[72:75],v[212:215],v[172:175],v[72:75]
rt v[200:201],v241 offset:42368
rt v[202:203],v241 offset:43392
mm v[104:107],v[212:215],v[188:191],v[104:107]
ld a[16:19],v244,s[44:47],s53 offen offset:1024 sc1
mm v[104:107],v[208:211],v[184:187],v[104:107]
rt v[212:213],v242 offset:33920
rt v[214:215],v242 offset:34944
mm v[108:111],v[216:219],v[184:187],v[108:111]
ld a[20:23],v245,s[44:47],s53 offen offset:1024 sc1
mm v[108:111],v[220:223],v[188:191],v[108:111]
rt v[208:209],v242 offset:42368
rt v[210:211],v242 offset:43392
mm v[76:79],v[220:223],v[172:175],v[76:79]
s_nop 0
s_barrier
wt lgkmcnt(4)
mm v[76:79],v[216:219],v[168:171],v[76:79]
rt v[220:221],v243 offset:33920
rt v[222:223],v243 offset:34944
mm v[80:83],v[196:199],v[168:171],v[80:83]
ld a[24:27],v246,s[44:47],s53 offen offset:1024 sc1
mm v[80:83],v[192:195],v[172:175],v[80:83]
rt v[216:217],v243 offset:42368
rt v[218:219],v243 offset:43392
mm v[112:115],v[192:195],v[188:191],v[112:115]
ld a[28:31],v247,s[44:47],s53 offen offset:1024 sc1
mm v[112:115],v[196:199],v[184:187],v[112:115]
rt v[192:193],v240 offset:50688
rt v[194:195],v240 offset:51712
mm v[116:119],v[204:207],v[184:187],v[116:119]
ld a[32:35],v244,s[44:47],s53 offen offset:2048 sc1
mm v[116:119],v[200:203],v[188:191],v[116:119]
rt v[196:197],v240 offset:59136
rt v[198:199],v240 offset:60160
.La5_LW3_0:
s_cbranch_execz .La5_LW3_4
mm v[84:87],v[200:203],v[172:175],v[84:87]
wt lgkmcnt(4)
s_xor_b32 s54, s54, s72
mm v[84:87],v[204:207],v[168:171],v[84:87]
rt v[200:201],v241 offset:50688
rt v[202:203],v241 offset:51712
mm v[88:91],v[212:215],v[168:171],v[88:91]
ld a[36:39],v245,s[44:47],s53 offen offset:2048 sc1
mm v[88:91],v[208:211],v[172:175],v[88:91]
rt v[204:205],v241 offset:59136
rt v[206:207],v241 offset:60160
mm v[120:123],v[208:211],v[188:191],v[120:123]
ld a[40:43],v246,s[44:47],s53 offen offset:2048 sc1
mm v[120:123],v[212:215],v[184:187],v[120:123]
rt v[208:209],v242 offset:50688
rt v[210:211],v242 offset:51712
mm v[124:127],v[220:223],v[184:187],v[124:127]
ld a[44:47],v247,s[44:47],s53 offen offset:2048 sc1
mm v[124:127],v[216:219],v[188:191],v[124:127]
rt v[212:213],v242 offset:59136
rt v[214:215],v242 offset:60160
mm v[92:95],v[216:219],v[172:175],v[92:95]
wt vmcnt(4) lgkmcnt(4)
s_lshl_b32 s54, s54, 13
mm v[92:95],v[220:223],v[168:171],v[92:95]
rt v[216:217],v243 offset:50688
rt v[218:219],v243 offset:51712
mm v[0:3],v[192:195],a[0:3],v[0:3]
s_bfe_u32 s55, s4, 262156
s_xor_b32 s55, s55, s72
mm v[0:3],v[196:199],a[16:19],v[0:3]
ld a[48:51],v244,s[44:47],s53 offen offset:3072 sc1
mm v[32:35],v[196:199],a[20:23],v[32:35]
s_lshl_b32 s55, s55, 13
s_bfe_u32 s52, s4, 262160
mm v[32:35],v[192:195],a[4:7],v[32:35]
rt v[220:221],v243 offset:59136
rt v[222:223],v243 offset:60160
mm v[64:67],v[192:195],a[8:11],v[64:67]
s_xor_b32 s52, s52, s72
s_lshl_b32 s52, s52, 13
mm v[64:67],v[196:199],a[24:27],v[64:67]
ld a[52:55],v245,s[44:47],s53 offen offset:3072 sc1
mm v[96:99],v[196:199],a[28:31],v[96:99]
mm v[96:99],v[192:195],a[12:15],v[96:99]
rt v[196:197],v240 offset:50816
rt v[198:199],v240 offset:51840
mm v[100:103],v[200:203],a[12:15],v[100:103]
)ASM" R"ASM(mm v[100:103],v[204:207],a[28:31],v[100:103]
buffer_load_dwordx2 v[250:251], v248, s[64:67], s5 offen sc1
mm v[68:71],v[204:207],a[24:27],v[68:71]
mm v[68:71],v[200:203],a[8:11],v[68:71]
rt v[192:193],v240 offset:59264
rt v[194:195],v240 offset:60288
mm v[36:39],v[200:203],a[4:7],v[36:39]
mm v[36:39],v[204:207],a[20:23],v[36:39]
ld a[56:59],v246,s[44:47],s53 offen offset:3072 sc1
mm v[4:7],v[204:207],a[16:19],v[4:7]
wt lgkmcnt(4)
mm v[4:7],v[200:203],a[0:3],v[4:7]
rt v[204:205],v241 offset:50816
rt v[206:207],v241 offset:51840
mm v[8:11],v[208:211],a[0:3],v[8:11]
mm v[8:11],v[212:215],a[16:19],v[8:11]
ld a[60:63],v247,s[44:47],s53 offen offset:3072 sc1
mm v[40:43],v[212:215],a[20:23],v[40:43]
s_bfe_u32 s53, s4, 262164
s_xor_b32 s53, s53, s72
mm v[40:43],v[208:211],a[4:7],v[40:43]
rt v[200:201],v241 offset:59264
rt v[202:203],v241 offset:60288
mm v[72:75],v[208:211],a[8:11],v[72:75]
s_add_u32 m0, s83, 121472
s_lshl_b32 s53, s53, 13
mm v[72:75],v[212:215],a[24:27],v[72:75]
ld v227,s[56:59],s80 offen lds
mm v[104:107],v[212:215],a[28:31],v[104:107]
s_add_u32 s80, s80, 131072
mm v[104:107],v[208:211],a[12:15],v[104:107]
rt v[212:213],v242 offset:50816
rt v[214:215],v242 offset:51840
mm v[108:111],v[216:219],a[12:15],v[108:111]
s_add_u32 m0, s83, 67584
mm v[108:111],v[220:223],a[28:31],v[108:111]
ld v224,s[56:59],s80 offen lds
mm v[76:79],v[220:223],a[24:27],v[76:79]
mm v[76:79],v[216:219],a[8:11],v[76:79]
rt v[208:209],v242 offset:59264
rt v[210:211],v242 offset:60288
mm v[44:47],v[216:219],a[4:7],v[44:47]
s_add_u32 m0, s83, 68608
mm v[44:47],v[220:223],a[20:23],v[44:47]
ld v225,s[56:59],s80 offen lds
mm v[12:15],v[220:223],a[16:19],v[12:15]
wt lgkmcnt(4)
mm v[12:15],v[216:219],a[0:3],v[12:15]
rt v[220:221],v243 offset:50816
rt v[222:223],v243 offset:51840
mm v[16:19],v[196:199],a[0:3],v[16:19]
s_add_u32 m0, s83, 69760
mm v[16:19],v[192:195],a[16:19],v[16:19]
ld v226,s[56:59],s80 offen lds
mm v[48:51],v[192:195],a[20:23],v[48:51]
mm v[48:51],v[196:199],a[4:7],v[48:51]
rt v[216:217],v243 offset:59264
rt v[218:219],v243 offset:60288
mm v[80:83],v[196:199],a[8:11],v[80:83]
s_add_u32 m0, s83, 70784
s_nop 0
s_barrier
mm v[80:83],v[192:195],a[24:27],v[80:83]
ld v227,s[56:59],s80 offen lds
mm v[112:115],v[192:195],a[28:31],v[112:115]
s_bfe_u32 s80, s4, 262156
s_xor_b32 s80, s80, s72
mm v[112:115],v[196:199],a[12:15],v[112:115]
rt v[192:193],v240 offset:0
rt v[194:195],v240 offset:1024
mm v[116:119],v[204:207],a[12:15],v[116:119]
s_add_u32 m0, s83, 84480
s_lshl_b32 s80, s80, 18
mm v[116:119],v[200:203],a[28:31],v[116:119]
ld v224,s[56:59],s80 offen lds
mm v[84:87],v[200:203],a[24:27],v[84:87]
mm v[84:87],v[204:207],a[8:11],v[84:87]
rt v[196:197],v240 offset:8448
rt v[198:199],v240 offset:9472
mm v[52:55],v[204:207],a[4:7],v[52:55]
s_add_u32 m0, s83, 85504
mm v[52:55],v[200:203],a[20:23],v[52:55]
ld v225,s[56:59],s80 offen lds
mm v[20:23],v[200:203],a[16:19],v[20:23]
wt lgkmcnt(4)
mm v[20:23],v[204:207],a[0:3],v[20:23]
rt v[200:201],v241 offset:0
rt v[202:203],v241 offset:1024
mm v[24:27],v[212:215],a[0:3],v[24:27]
s_add_u32 m0, s83, 86656
mm v[24:27],v[208:211],a[16:19],v[24:27]
ld v226,s[56:59],s80 offen lds
mm v[56:59],v[208:211],a[20:23],v[56:59]
wt vmcnt(10)
v_cmp_eq_u64 vcc, v[250:251], v[252:253]
mm v[56:59],v[212:215],a[4:7],v[56:59]
rt v[204:205],v241 offset:8448
rt v[206:207],v241 offset:9472
mm v[88:91],v[212:215],a[8:11],v[88:91]
s_add_u32 m0, s83, 87680
)ASM" R"ASM(mm v[88:91],v[208:211],a[24:27],v[88:91]
ld v227,s[56:59],s80 offen lds
mm v[120:123],v[208:211],a[28:31],v[120:123]
s_bitcmp1_b32 vcc_lo, 2
s_nop 0
s_nop 0
s_cbranch_scc0 .La5_LO_NB2
.La5_LO_KB2:
mm v[120:123],v[212:215],a[12:15],v[120:123]
rt v[208:209],v242 offset:0
rt v[210:211],v242 offset:1024
mm v[124:127],v[220:223],a[12:15],v[124:127]
s_add_u32 s80, s80, 131072
mm v[124:127],v[216:219],a[28:31],v[124:127]
ld a[12:15],v244,s[44:47],s54 offen offset:0 sc1
mm v[92:95],v[216:219],a[24:27],v[92:95]
mm v[92:95],v[220:223],a[8:11],v[92:95]
rt v[212:213],v242 offset:8448
rt v[214:215],v242 offset:9472
mm v[60:63],v[220:223],a[4:7],v[60:63]
mm v[60:63],v[216:219],a[20:23],v[60:63]
mm v[28:31],v[216:219],a[16:19],v[28:31]
wt vmcnt(10) lgkmcnt(4)
mm v[28:31],v[220:223],a[0:3],v[28:31]
rt v[216:217],v243 offset:0
rt v[218:219],v243 offset:1024
mm v[0:3],v[192:195],a[32:35],v[0:3]
mm v[0:3],v[196:199],a[48:51],v[0:3]
ld a[0:3],v245,s[44:47],s54 offen offset:0 sc1
mm v[32:35],v[196:199],a[52:55],v[32:35]
mm v[32:35],v[192:195],a[36:39],v[32:35]
rt v[220:221],v243 offset:8448
rt v[222:223],v243 offset:9472
mm v[64:67],v[192:195],a[40:43],v[64:67]
mm v[64:67],v[196:199],a[56:59],v[64:67]
ld a[4:7],v246,s[44:47],s54 offen offset:0 sc1
mm v[96:99],v[196:199],a[60:63],v[96:99]
mm v[96:99],v[192:195],a[44:47],v[96:99]
rt v[196:197],v240 offset:128
rt v[198:199],v240 offset:1152
mm v[100:103],v[200:203],a[44:47],v[100:103]
s_mov_b32 s86, 0x05040100
s_mov_b32 s87, 0x07060302
mm v[100:103],v[204:207],a[60:63],v[100:103]
ld a[8:11],v247,s[44:47],s54 offen offset:0 sc1
mm v[68:71],v[204:207],a[56:59],v[68:71]
s_nop 0
ar v230,a128
mm v[68:71],v[200:203],a[40:43],v[68:71]
rt v[192:193],v240 offset:8576
rt v[194:195],v240 offset:9600
mm v[36:39],v[200:203],a[36:39],v[36:39]
v_add_u32 v240, 67584, v240
ar v231,a132
mm v[36:39],v[204:207],a[52:55],v[36:39]
ld a[16:19],v244,s[44:47],s54 offen offset:1024 sc1
mm v[4:7],v[204:207],a[48:51],v[4:7]
wt lgkmcnt(4)
v_perm_b32 v232, v231, v230, s86
mm v[4:7],v[200:203],a[32:35],v[4:7]
rt v[204:205],v241 offset:128
rt v[206:207],v241 offset:1152
mm v[8:11],v[208:211],a[32:35],v[8:11]
v_perm_b32 v233, v231, v230, s87
aw a96,v232
mm v[8:11],v[212:215],a[48:51],v[8:11]
ld a[20:23],v245,s[44:47],s54 offen offset:1024 sc1
mm v[40:43],v[212:215],a[52:55],v[40:43]
aw a100,v233
ar v234,a136
mm v[40:43],v[208:211],a[36:39],v[40:43]
rt v[200:201],v241 offset:8576
rt v[202:203],v241 offset:9600
mm v[72:75],v[208:211],a[40:43],v[72:75]
v_add_u32 v241, 67584, v241
ar v235,a140
mm v[72:75],v[212:215],a[56:59],v[72:75]
ld a[24:27],v246,s[44:47],s54 offen offset:1024 sc1
mm v[104:107],v[212:215],a[60:63],v[104:107]
v_perm_b32 v236, v235, v234, s86
v_perm_b32 v237, v235, v234, s87
mm v[104:107],v[208:211],a[44:47],v[104:107]
rt v[212:213],v242 offset:128
rt v[214:215],v242 offset:1152
mm v[108:111],v[216:219],a[44:47],v[108:111]
aw a97,v236
aw a101,v237
mm v[108:111],v[220:223],a[60:63],v[108:111]
ld a[28:31],v247,s[44:47],s54 offen offset:1024 sc1
mm v[76:79],v[220:223],a[56:59],v[76:79]
ar v230,a144
ar v231,a148
mm v[76:79],v[216:219],a[40:43],v[76:79]
rt v[208:209],v242 offset:8576
rt v[210:211],v242 offset:9600
mm v[44:47],v[216:219],a[36:39],v[44:47]
v_add_u32 v242, 67584, v242
v_perm_b32 v232, v231, v230, s86
mm v[44:47],v[220:223],a[52:55],v[44:47]
v_perm_b32 v233, v231, v230, s87
)ASM" R"ASM(aw a98,v232
mm v[12:15],v[220:223],a[48:51],v[12:15]
wt lgkmcnt(4)
aw a102,v233
mm v[12:15],v[216:219],a[32:35],v[12:15]
rt v[220:221],v243 offset:128
rt v[222:223],v243 offset:1152
mm v[16:19],v[196:199],a[32:35],v[16:19]
s_add_u32 m0, s83, 101376
ar v234,a152
mm v[16:19],v[192:195],a[48:51],v[16:19]
ld v224,s[56:59],s80 offen lds
mm v[48:51],v[192:195],a[52:55],v[48:51]
ar v235,a156
v_perm_b32 v236, v235, v234, s86
mm v[48:51],v[196:199],a[36:39],v[48:51]
rt v[216:217],v243 offset:8576
rt v[218:219],v243 offset:9600
mm v[80:83],v[196:199],a[40:43],v[80:83]
s_add_u32 m0, s83, 102400
v_add_u32 v243, 67584, v243
mm v[80:83],v[192:195],a[56:59],v[80:83]
ld v225,s[56:59],s80 offen lds
mm v[112:115],v[192:195],a[60:63],v[112:115]
wt vmcnt(14)
s_barrier
mm v[112:115],v[196:199],a[44:47],v[112:115]
rt v[192:193],v240 offset:50688
rt v[194:195],v240 offset:51712
mm v[116:119],v[204:207],a[44:47],v[116:119]
s_add_u32 m0, s83, 103552
v_perm_b32 v237, v235, v234, s87
mm v[116:119],v[200:203],a[60:63],v[116:119]
ld v226,s[56:59],s80 offen lds
mm v[84:87],v[200:203],a[56:59],v[84:87]
aw a99,v236
aw a103,v237
mm v[84:87],v[204:207],a[40:43],v[84:87]
rt v[196:197],v240 offset:59136
rt v[198:199],v240 offset:60160
mm v[52:55],v[204:207],a[36:39],v[52:55]
ar v230,a129
ar v231,a133
mm v[52:55],v[200:203],a[52:55],v[52:55]
ld a[64:67],v244,s[44:47],s54 offen offset:2048 sc1
mm v[20:23],v[200:203],a[48:51],v[20:23]
wt lgkmcnt(4)
v_perm_b32 v232, v231, v230, s86
mm v[20:23],v[204:207],a[32:35],v[20:23]
rt v[200:201],v241 offset:50688
rt v[202:203],v241 offset:51712
mm v[24:27],v[212:215],a[32:35],v[24:27]
v_perm_b32 v233, v231, v230, s87
aw a104,v232
mm v[24:27],v[208:211],a[48:51],v[24:27]
ld a[68:71],v245,s[44:47],s54 offen offset:2048 sc1
mm v[56:59],v[208:211],a[52:55],v[56:59]
aw a108,v233
ar v234,a137
mm v[56:59],v[212:215],a[36:39],v[56:59]
rt v[204:205],v241 offset:59136
rt v[206:207],v241 offset:60160
mm v[88:91],v[212:215],a[40:43],v[88:91]
ar v235,a141
v_perm_b32 v236, v235, v234, s86
mm v[88:91],v[208:211],a[56:59],v[88:91]
ld a[72:75],v246,s[44:47],s54 offen offset:2048 sc1
mm v[120:123],v[208:211],a[60:63],v[120:123]
v_perm_b32 v237, v235, v234, s87
aw a105,v236
mm v[120:123],v[212:215],a[44:47],v[120:123]
rt v[208:209],v242 offset:50688
rt v[210:211],v242 offset:51712
mm v[124:127],v[220:223],a[44:47],v[124:127]
aw a109,v237
ar v230,a145
mm v[124:127],v[216:219],a[60:63],v[124:127]
buffer_load_dwordx2 v[250:251], v248, s[64:67], s5 offen sc1
mm v[92:95],v[216:219],a[56:59],v[92:95]
ar v231,a149
v_perm_b32 v232, v231, v230, s86
mm v[92:95],v[220:223],a[40:43],v[92:95]
rt v[212:213],v242 offset:59136
rt v[214:215],v242 offset:60160
mm v[60:63],v[220:223],a[36:39],v[60:63]
v_perm_b32 v233, v231, v230, s87
aw a106,v232
mm v[60:63],v[216:219],a[52:55],v[60:63]
ld a[36:39],v247,s[44:47],s54 offen offset:2048 sc1
mm v[28:31],v[216:219],a[48:51],v[28:31]
wt vmcnt(8) lgkmcnt(4)
aw a110,v233
mm v[28:31],v[220:223],a[32:35],v[28:31]
rt v[216:217],v243 offset:50688
rt v[218:219],v243 offset:51712
mm v[0:3],v[192:195],a[12:15],v[0:3]
ar v234,a153
ar v235,a157
mm v[0:3],v[196:199],a[16:19],v[0:3]
ld a[32:35],v244,s[44:47],s54 offen offset:3072 sc1
)ASM" R"ASM(mm v[32:35],v[196:199],a[20:23],v[32:35]
v_perm_b32 v236, v235, v234, s86
v_perm_b32 v237, v235, v234, s87
mm v[32:35],v[192:195],a[0:3],v[32:35]
rt v[220:221],v243 offset:59136
rt v[222:223],v243 offset:60160
mm v[64:67],v[192:195],a[4:7],v[64:67]
aw a107,v236
aw a111,v237
mm v[64:67],v[196:199],a[24:27],v[64:67]
ld a[40:43],v245,s[44:47],s54 offen offset:3072 sc1
mm v[96:99],v[196:199],a[28:31],v[96:99]
ar v230,a130
ar v231,a134
mm v[96:99],v[192:195],a[8:11],v[96:99]
rt v[196:197],v240 offset:50816
rt v[198:199],v240 offset:51840
mm v[100:103],v[200:203],a[8:11],v[100:103]
v_perm_b32 v232, v231, v230, s86
v_perm_b32 v233, v231, v230, s87
mm v[100:103],v[204:207],a[28:31],v[100:103]
ld a[44:47],v246,s[44:47],s54 offen offset:3072 sc1
mm v[68:71],v[204:207],a[24:27],v[68:71]
aw a112,v232
aw a116,v233
mm v[68:71],v[200:203],a[4:7],v[68:71]
rt v[192:193],v240 offset:59264
rt v[194:195],v240 offset:60288
mm v[36:39],v[200:203],a[0:3],v[36:39]
ar v234,a138
ar v235,a142
mm v[36:39],v[204:207],a[20:23],v[36:39]
ld a[48:51],v247,s[44:47],s54 offen offset:3072 sc1
mm v[4:7],v[204:207],a[16:19],v[4:7]
wt lgkmcnt(4)
s_bfe_u32 s54, s4, 262164
mm v[4:7],v[200:203],a[12:15],v[4:7]
rt v[204:205],v241 offset:50816
rt v[206:207],v241 offset:51840
mm v[8:11],v[208:211],a[12:15],v[8:11]
s_add_u32 m0, s83, 104576
s_xor_b32 s54, s54, s72
mm v[8:11],v[212:215],a[16:19],v[8:11]
ld v227,s[56:59],s80 offen lds
mm v[40:43],v[212:215],a[20:23],v[40:43]
s_bitcmp1_b32 vcc_lo, 3
s_cbranch_scc0 .La5_LO_NB3
.La5_LO_KB3:
mm v[40:43],v[208:211],a[0:3],v[40:43]
rt v[200:201],v241 offset:59264
rt v[202:203],v241 offset:60288
mm v[72:75],v[208:211],a[4:7],v[72:75]
s_bfe_u32 s80, s4, 262160
s_xor_b32 s80, s80, s72
mm v[72:75],v[212:215],a[24:27],v[72:75]
ld a[52:55],v244,s[44:47],s55 offen offset:0 sc1
mm v[104:107],v[212:215],a[28:31],v[104:107]
s_lshl_b32 s80, s80, 18
s_lshl_b32 s54, s54, 13
mm v[104:107],v[208:211],a[8:11],v[104:107]
rt v[212:213],v242 offset:50816
rt v[214:215],v242 offset:51840
mm v[108:111],v[216:219],a[8:11],v[108:111]
s_xor_b32 s54, s54, 229376
v_perm_b32 v236, v235, v234, s86
mm v[108:111],v[220:223],a[28:31],v[108:111]
ld a[56:59],v245,s[44:47],s55 offen offset:0 sc1
mm v[76:79],v[220:223],a[24:27],v[76:79]
v_perm_b32 v237, v235, v234, s87
aw a113,v236
mm v[76:79],v[216:219],a[4:7],v[76:79]
rt v[208:209],v242 offset:59264
rt v[210:211],v242 offset:60288
mm v[44:47],v[216:219],a[0:3],v[44:47]
aw a117,v237
ar v230,a146
mm v[44:47],v[220:223],a[20:23],v[44:47]
ar v231,a150
v_perm_b32 v232, v231, v230, s86
mm v[12:15],v[220:223],a[16:19],v[12:15]
wt lgkmcnt(4)
v_perm_b32 v233, v231, v230, s87
mm v[12:15],v[216:219],a[12:15],v[12:15]
rt v[220:221],v243 offset:50816
rt v[222:223],v243 offset:51840
mm v[16:19],v[196:199],a[12:15],v[16:19]
aw a114,v232
aw a118,v233
mm v[16:19],v[192:195],a[16:19],v[16:19]
ld a[60:63],v246,s[44:47],s55 offen offset:0 sc1
mm v[48:51],v[192:195],a[20:23],v[48:51]
ar v234,a154
ar v235,a158
mm v[48:51],v[196:199],a[0:3],v[48:51]
rt v[216:217],v243 offset:59264
rt v[218:219],v243 offset:60288
mm v[80:83],v[196:199],a[4:7],v[80:83]
v_perm_b32 v236, v235, v234, s86
v_perm_b32 v237, v235, v234, s87
mm v[80:83],v[192:195],a[24:27],v[80:83]
)ASM" R"ASM(ld a[76:79],v247,s[44:47],s55 offen offset:0 sc1
mm v[112:115],v[192:195],a[28:31],v[112:115]
aw a115,v236
aw a119,v237
mm v[112:115],v[196:199],a[8:11],v[112:115]
rt v[192:193],v240 offset:0
rt v[194:195],v240 offset:1024
mm v[116:119],v[204:207],a[8:11],v[116:119]
ar v230,a131
ar v231,a135
mm v[116:119],v[200:203],a[28:31],v[116:119]
ld a[80:83],v244,s[44:47],s55 offen offset:1024 sc1
mm v[84:87],v[200:203],a[24:27],v[84:87]
v_perm_b32 v232, v231, v230, s86
v_perm_b32 v233, v231, v230, s87
mm v[84:87],v[204:207],a[4:7],v[84:87]
rt v[196:197],v240 offset:8448
rt v[198:199],v240 offset:9472
mm v[52:55],v[204:207],a[0:3],v[52:55]
s_load_dwordx2 s[16:17], s[0:1], 0
s_load_dwordx4 s[36:39], s[0:1], 80
mm v[52:55],v[200:203],a[20:23],v[52:55]
ld a[84:87],v245,s[44:47],s55 offen offset:1024 sc1
mm v[20:23],v[200:203],a[16:19],v[20:23]
wt lgkmcnt(4)
aw a120,v232
mm v[20:23],v[204:207],a[12:15],v[20:23]
rt v[200:201],v241 offset:0
rt v[202:203],v241 offset:1024
mm v[24:27],v[212:215],a[12:15],v[24:27]
aw a124,v233
ar v234,a139
mm v[24:27],v[208:211],a[16:19],v[24:27]
ld a[88:91],v246,s[44:47],s55 offen offset:1024 sc1
mm v[56:59],v[208:211],a[20:23],v[56:59]
ar v235,a143
v_perm_b32 v236, v235, v234, s86
mm v[56:59],v[212:215],a[0:3],v[56:59]
rt v[204:205],v241 offset:8448
rt v[206:207],v241 offset:9472
mm v[88:91],v[212:215],a[4:7],v[88:91]
v_perm_b32 v237, v235, v234, s87
aw a121,v236
mm v[88:91],v[208:211],a[24:27],v[88:91]
ld a[92:95],v247,s[44:47],s55 offen offset:1024 sc1
mm v[120:123],v[208:211],a[28:31],v[120:123]
aw a125,v237
ar v230,a147
mm v[120:123],v[212:215],a[8:11],v[120:123]
rt v[208:209],v242 offset:0
rt v[210:211],v242 offset:1024
mm v[124:127],v[220:223],a[8:11],v[124:127]
s_add_u32 m0, s83, 16896
ar v231,a151
mm v[124:127],v[216:219],a[28:31],v[124:127]
ld v224,s[56:59],s80 offen lds
mm v[92:95],v[216:219],a[24:27],v[92:95]
v_perm_b32 v232, v231, v230, s86
v_perm_b32 v233, v231, v230, s87
mm v[92:95],v[220:223],a[4:7],v[92:95]
rt v[212:213],v242 offset:8448
rt v[214:215],v242 offset:9472
mm v[60:63],v[220:223],a[0:3],v[60:63]
aw a122,v232
aw a126,v233
mm v[60:63],v[216:219],a[20:23],v[60:63]
ar v234,a155
ar v235,a159
mm v[28:31],v[216:219],a[16:19],v[28:31]
wt vmcnt(10) lgkmcnt(4)
v_perm_b32 v236, v235, v234, s86
mm v[28:31],v[220:223],a[12:15],v[28:31]
rt v[216:217],v243 offset:0
rt v[218:219],v243 offset:1024
mm v[0:3],v[192:195],a[64:67],v[0:3]
s_add_u32 m0, s83, 17920
v_perm_b32 v237, v235, v234, s87
mm v[0:3],v[196:199],a[32:35],v[0:3]
ld v225,s[56:59],s80 offen lds
mm v[32:35],v[196:199],a[40:43],v[32:35]
aw a123,v236
aw a127,v237
mm v[32:35],v[192:195],a[68:71],v[32:35]
rt v[220:221],v243 offset:8448
rt v[222:223],v243 offset:9472
mm v[64:67],v[192:195],a[72:75],v[64:67]
.La5_LW3_1:
s_cbranch_execz .La5_LW3_5
s_add_u32 m0, s83, 19072
ar v230,a160
mm v[64:67],v[196:199],a[44:47],v[64:67]
ld v226,s[56:59],s80 offen lds
mm v[96:99],v[196:199],a[48:51],v[96:99]
ar v231,a164
v_perm_b32 v232, v231, v230, s86
mm v[96:99],v[192:195],a[36:39],v[96:99]
rt v[196:197],v240 offset:128
rt v[198:199],v240 offset:1152
mm v[100:103],v[200:203],a[36:39],v[100:103]
v_perm_b32 v233, v231, v230, s87
)ASM" R"ASM(aw a128,v232
mm v[100:103],v[204:207],a[48:51],v[100:103]
ld a[0:3],v244,s[44:47],s55 offen offset:2048 sc1
mm v[68:71],v[204:207],a[44:47],v[68:71]
aw a132,v233
ar v234,a168
mm v[68:71],v[200:203],a[72:75],v[68:71]
rt v[192:193],v240 offset:8576
rt v[194:195],v240 offset:9600
mm v[36:39],v[200:203],a[68:71],v[36:39]
ar v235,a172
v_perm_b32 v236, v235, v234, s86
mm v[36:39],v[204:207],a[40:43],v[36:39]
ld a[4:7],v245,s[44:47],s55 offen offset:2048 sc1
mm v[4:7],v[204:207],a[32:35],v[4:7]
wt lgkmcnt(4)
v_perm_b32 v237, v235, v234, s87
mm v[4:7],v[200:203],a[64:67],v[4:7]
rt v[204:205],v241 offset:128
rt v[206:207],v241 offset:1152
mm v[8:11],v[208:211],a[64:67],v[8:11]
aw a129,v236
aw a133,v237
mm v[8:11],v[212:215],a[32:35],v[8:11]
ld a[8:11],v246,s[44:47],s55 offen offset:2048 sc1
mm v[40:43],v[212:215],a[40:43],v[40:43]
ar v230,a176
ar v231,a180
mm v[40:43],v[208:211],a[68:71],v[40:43]
rt v[200:201],v241 offset:8576
rt v[202:203],v241 offset:9600
mm v[72:75],v[208:211],a[72:75],v[72:75]
v_perm_b32 v232, v231, v230, s86
v_perm_b32 v233, v231, v230, s87
mm v[72:75],v[212:215],a[44:47],v[72:75]
ld a[12:15],v247,s[44:47],s55 offen offset:2048 sc1
mm v[104:107],v[212:215],a[48:51],v[104:107]
aw a130,v232
aw a134,v233
mm v[104:107],v[208:211],a[36:39],v[104:107]
rt v[212:213],v242 offset:128
rt v[214:215],v242 offset:1152
mm v[108:111],v[216:219],a[36:39],v[108:111]
ar v234,a184
ar v235,a188
mm v[108:111],v[220:223],a[48:51],v[108:111]
ld a[16:19],v244,s[44:47],s55 offen offset:3072 sc1
mm v[76:79],v[220:223],a[44:47],v[76:79]
v_perm_b32 v236, v235, v234, s86
v_perm_b32 v237, v235, v234, s87
mm v[76:79],v[216:219],a[72:75],v[76:79]
rt v[208:209],v242 offset:8576
rt v[210:211],v242 offset:9600
mm v[44:47],v[216:219],a[68:71],v[44:47]
aw a131,v236
aw a135,v237
mm v[44:47],v[220:223],a[40:43],v[44:47]
s_nop 0
v_cmp_eq_u64 vcc, v[250:251], v[252:253]
mm v[12:15],v[220:223],a[32:35],v[12:15]
wt lgkmcnt(4)
ar v230,a161
mm v[12:15],v[216:219],a[64:67],v[12:15]
rt v[220:221],v243 offset:128
rt v[222:223],v243 offset:1152
mm v[16:19],v[196:199],a[64:67],v[16:19]
ar v231,a165
v_perm_b32 v232, v231, v230, s86
mm v[16:19],v[192:195],a[32:35],v[16:19]
ld a[20:23],v245,s[44:47],s55 offen offset:3072 sc1
mm v[48:51],v[192:195],a[40:43],v[48:51]
v_perm_b32 v233, v231, v230, s87
aw a136,v232
mm v[48:51],v[196:199],a[68:71],v[48:51]
rt v[216:217],v243 offset:8576
rt v[218:219],v243 offset:9600
mm v[80:83],v[196:199],a[72:75],v[80:83]
aw a140,v233
ar v234,a169
mm v[80:83],v[192:195],a[44:47],v[80:83]
ld a[24:27],v246,s[44:47],s55 offen offset:3072 sc1
mm v[112:115],v[192:195],a[48:51],v[112:115]
wt vmcnt(18) lgkmcnt(0)
s_barrier
mm v[112:115],v[196:199],a[36:39],v[112:115]
rt v[192:193],v240 offset:16896
rt v[194:195],v240 offset:17920
mm v[116:119],v[204:207],a[36:39],v[116:119]
s_xor_b32 s90, s72, 0
s_lshl_b32 s92, s76, 5
mm v[116:119],v[200:203],a[48:51],v[116:119]
buffer_load_dwordx2 v[250:251], v248, s[64:67], s5 offen sc1
mm v[84:87],v[200:203],a[44:47],v[84:87]
s_add_u32 s92, s92, s90
s_lshl2_add_u32 s92, s92, s3
mm v[84:87],v[204:207],a[72:75],v[84:87]
rt v[196:197],v240 offset:25344
rt v[198:199],v240 offset:26368
mm v[52:55],v[204:207],a[68:71],v[52:55]
s_lshl_b32 s92, s92, 12
)ASM" R"ASM(ar v235,a173
mm v[52:55],v[200:203],a[40:43],v[52:55]
ld a[28:31],v247,s[44:47],s55 offen offset:3072 sc1
mm v[20:23],v[200:203],a[32:35],v[20:23]
s_nop 0
s_bfe_u32 s55, s4, 262168
mm v[20:23],v[204:207],a[64:67],v[20:23]
rt v[200:201],v241 offset:16896
rt v[202:203],v241 offset:17920
mm v[24:27],v[212:215],a[64:67],v[24:27]
s_add_u32 m0, s83, 20096
s_xor_b32 s55, s55, s72
mm v[24:27],v[208:211],a[32:35],v[24:27]
ld v227,s[56:59],s80 offen lds
mm v[56:59],v[208:211],a[40:43],v[56:59]
s_add_u32 s80, s80, 131072
s_lshl_b32 s55, s55, 13
mm v[56:59],v[212:215],a[68:71],v[56:59]
rt v[204:205],v241 offset:25344
rt v[206:207],v241 offset:26368
mm v[88:91],v[212:215],a[72:75],v[88:91]
s_add_u32 m0, s83, 33792
v_perm_b32 v236, v235, v234, s86
mm v[88:91],v[208:211],a[44:47],v[88:91]
ld v224,s[56:59],s80 offen lds
mm v[120:123],v[208:211],a[48:51],v[120:123]
s_add_u32 s68, s16, s92
s_addc_u32 s69, s17, 0
mm v[120:123],v[212:215],a[36:39],v[120:123]
rt v[208:209],v242 offset:16896
rt v[210:211],v242 offset:17920
mm v[124:127],v[220:223],a[36:39],v[124:127]
s_add_u32 m0, s83, 34816
s_and_b32 s69, s69, 0xffff
mm v[124:127],v[216:219],a[48:51],v[124:127]
ld v225,s[56:59],s80 offen lds
mm v[92:95],v[216:219],a[44:47],v[92:95]
s_mov_b32 s70, 4096
s_mov_b32 s71, 0x20000
mm v[92:95],v[220:223],a[72:75],v[92:95]
rt v[212:213],v242 offset:25344
rt v[214:215],v242 offset:26368
mm v[60:63],v[220:223],a[68:71],v[60:63]
s_add_u32 m0, s83, 35968
v_perm_b32 v237, v235, v234, s87
mm v[60:63],v[216:219],a[40:43],v[60:63]
ld v226,s[56:59],s80 offen lds
mm v[28:31],v[216:219],a[32:35],v[28:31]
wt vmcnt(16) lgkmcnt(4)
aw a137,v236
mm v[28:31],v[220:223],a[64:67],v[28:31]
rt v[216:217],v243 offset:16896
rt v[218:219],v243 offset:17920
mm v[0:3],v[192:195],a[52:55],v[0:3]
s_add_u32 m0, s83, 36992
aw a141,v237
mm v[0:3],v[196:199],a[80:83],v[0:3]
ld v227,s[56:59],s80 offen lds
mm v[32:35],v[196:199],a[84:87],v[32:35]
s_bitcmp1_b32 vcc_lo, 4
s_cbranch_scc0 .La5_LO_NB4
.La5_LO_KB4:
mm v[32:35],v[192:195],a[56:59],v[32:35]
rt v[220:221],v243 offset:25344
rt v[222:223],v243 offset:26368
mm v[64:67],v[192:195],a[60:63],v[64:67]
s_bfe_u32 s80, s4, 262164
s_xor_b32 s80, s80, s72
mm v[64:67],v[196:199],a[88:91],v[64:67]
ld a[32:35],v244,s[44:47],s52 offen offset:0 sc1
mm v[96:99],v[196:199],a[92:95],v[96:99]
s_lshl_b32 s80, s80, 18
ar v230,a177
mm v[96:99],v[192:195],a[76:79],v[96:99]
rt v[196:197],v240 offset:17024
rt v[198:199],v240 offset:18048
mm v[100:103],v[200:203],a[76:79],v[100:103]
s_add_u32 s48, s36, 0
s_addc_u32 s49, s37, 0
mm v[100:103],v[204:207],a[92:95],v[100:103]
ld a[36:39],v245,s[44:47],s52 offen offset:0 sc1
mm v[68:71],v[204:207],a[88:91],v[68:71]
s_and_b32 s49, s49, 0xffff
s_mov_b32 s50, 2048
mm v[68:71],v[200:203],a[60:63],v[68:71]
rt v[192:193],v240 offset:25472
rt v[194:195],v240 offset:26496
mm v[36:39],v[200:203],a[56:59],v[36:39]
s_mov_b32 s51, 0x20000
v_and_b32 v238, 0xf0, v255
mm v[36:39],v[204:207],a[84:87],v[36:39]
ld a[40:43],v246,s[44:47],s52 offen offset:0 sc1
mm v[4:7],v[204:207],a[80:83],v[4:7]
wt lgkmcnt(4)
v_lshlrev_b32 v238, 2, v238
mm v[4:7],v[200:203],a[52:55],v[4:7]
rt v[204:205],v241 offset:17024
rt v[206:207],v241 offset:18048
mm v[8:11],v[208:211],a[52:55],v[8:11]
v_bfe_u32 v249, v255, 8, 1
)ASM" R"ASM(v_lshl_add_u32 v238, v249, 5, v238
mm v[8:11],v[212:215],a[80:83],v[8:11]
ld a[44:47],v247,s[44:47],s52 offen offset:0 sc1
mm v[40:43],v[212:215],a[84:87],v[40:43]
v_lshrrev_b32 v249, 9, v255
v_lshl_add_u32 v238, v249, 4, v238
mm v[40:43],v[208:211],a[56:59],v[40:43]
rt v[200:201],v241 offset:25472
rt v[202:203],v241 offset:26496
mm v[72:75],v[208:211],a[60:63],v[72:75]
s_lshr_b32 s23, s72, 3
s_lshl3_add_u32 s92, s23, s76
mm v[72:75],v[212:215],a[88:91],v[72:75]
ld a[48:51],v244,s[44:47],s52 offen offset:1024 sc1
mm v[104:107],v[212:215],a[92:95],v[104:107]
s_lshl_b32 s92, s92, 19
s_lshl_b32 s97, s3, 16
mm v[104:107],v[208:211],a[76:79],v[104:107]
rt v[212:213],v242 offset:17024
rt v[214:215],v242 offset:18048
mm v[108:111],v[216:219],a[76:79],v[108:111]
s_add_u32 s94, s92, s97
s_add_u32 s95, s94, 32768
mm v[108:111],v[220:223],a[92:95],v[108:111]
ld a[64:67],v245,s[44:47],s52 offen offset:1024 sc1
mm v[76:79],v[220:223],a[88:91],v[76:79]
s_sub_u32 s96, s92, s97
s_add_u32 s96, s96, 458752
mm v[76:79],v[216:219],a[60:63],v[76:79]
rt v[208:209],v242 offset:25472
rt v[210:211],v242 offset:26496
mm v[44:47],v[216:219],a[56:59],v[44:47]
s_add_u32 s97, s96, 32768
s_lshl3_add_u32 s92, s23, s101
mm v[44:47],v[220:223],a[84:87],v[44:47]
s_lshl_b32 s85, s76, 15
s_lshl3_add_u32 s92, s92, s85
mm v[12:15],v[220:223],a[80:83],v[12:15]
wt lgkmcnt(4)
s_add_u32 s92, s92, 2097152
mm v[12:15],v[216:219],a[52:55],v[12:15]
rt v[220:221],v243 offset:17024
rt v[222:223],v243 offset:18048
mm v[16:19],v[196:199],a[52:55],v[16:19]
ar v231,a181
v_perm_b32 v232, v231, v230, s86
mm v[16:19],v[192:195],a[80:83],v[16:19]
ld a[68:71],v246,s[44:47],s52 offen offset:1024 sc1
mm v[48:51],v[192:195],a[84:87],v[48:51]
v_perm_b32 v233, v231, v230, s87
aw a138,v232
mm v[48:51],v[196:199],a[56:59],v[48:51]
rt v[216:217],v243 offset:25472
rt v[218:219],v243 offset:26496
mm v[80:83],v[196:199],a[60:63],v[80:83]
aw a142,v233
ar v234,a185
mm v[80:83],v[192:195],a[88:91],v[80:83]
ld a[72:75],v247,s[44:47],s52 offen offset:1024 sc1
mm v[112:115],v[192:195],a[92:95],v[112:115]
ar v235,a189
v_perm_b32 v236, v235, v234, s86
mm v[112:115],v[196:199],a[76:79],v[112:115]
rt v[192:193],v240 offset:33792
rt v[194:195],v240 offset:34816
mm v[116:119],v[204:207],a[76:79],v[116:119]
s_add_u32 m0, s83, 50688
v_perm_b32 v237, v235, v234, s87
mm v[116:119],v[200:203],a[92:95],v[116:119]
ld v224,s[56:59],s80 offen lds
mm v[84:87],v[200:203],a[88:91],v[84:87]
aw a139,v236
aw a143,v237
mm v[84:87],v[204:207],a[60:63],v[84:87]
rt v[196:197],v240 offset:42240
rt v[198:199],v240 offset:43264
mm v[52:55],v[204:207],a[56:59],v[52:55]
s_add_u32 m0, s83, 51712
ar v230,a162
mm v[52:55],v[200:203],a[84:87],v[52:55]
ld v225,s[56:59],s80 offen lds
mm v[20:23],v[200:203],a[80:83],v[20:23]
wt lgkmcnt(4)
ar v231,a166
mm v[20:23],v[204:207],a[52:55],v[20:23]
rt v[200:201],v241 offset:33792
rt v[202:203],v241 offset:34816
mm v[24:27],v[212:215],a[52:55],v[24:27]
s_add_u32 m0, s83, 52864
v_perm_b32 v232, v231, v230, s86
mm v[24:27],v[208:211],a[80:83],v[24:27]
ld v226,s[56:59],s80 offen lds
mm v[56:59],v[208:211],a[84:87],v[56:59]
v_perm_b32 v233, v231, v230, s87
aw a144,v232
mm v[56:59],v[212:215],a[56:59],v[56:59]
rt v[204:205],v241 offset:42240
rt v[206:207],v241 offset:43264
mm v[88:91],v[212:215],a[60:63],v[88:91]
aw a148,v233
)ASM" R"ASM(ar v234,a170
mm v[88:91],v[208:211],a[88:91],v[88:91]
ld v[136:139],v244,s[44:47],s52 offen offset:2048 sc1
mm v[120:123],v[208:211],a[92:95],v[120:123]
ar v235,a174
v_perm_b32 v236, v235, v234, s86
mm v[120:123],v[212:215],a[76:79],v[120:123]
rt v[208:209],v242 offset:33792
rt v[210:211],v242 offset:34816
mm v[124:127],v[220:223],a[76:79],v[124:127]
v_perm_b32 v237, v235, v234, s87
aw a145,v236
mm v[124:127],v[216:219],a[92:95],v[124:127]
ld v[140:143],v245,s[44:47],s52 offen offset:2048 sc1
mm v[92:95],v[216:219],a[88:91],v[92:95]
aw a149,v237
ar v230,a178
mm v[92:95],v[220:223],a[60:63],v[92:95]
rt v[212:213],v242 offset:42240
rt v[214:215],v242 offset:43264
mm v[60:63],v[220:223],a[56:59],v[60:63]
ar v231,a182
v_perm_b32 v232, v231, v230, s86
mm v[60:63],v[216:219],a[84:87],v[60:63]
v_perm_b32 v233, v231, v230, s87
aw a146,v232
mm v[28:31],v[216:219],a[80:83],v[28:31]
wt vmcnt(18) lgkmcnt(4)
aw a150,v233
mm v[28:31],v[220:223],a[52:55],v[28:31]
rt v[216:217],v243 offset:33792
rt v[218:219],v243 offset:34816
mm v[0:3],v[192:195],a[0:3],v[0:3]
ar v234,a186
ar v235,a190
mm v[0:3],v[196:199],a[16:19],v[0:3]
ld v[152:155],v246,s[44:47],s52 offen offset:2048 sc1
mm v[32:35],v[196:199],a[20:23],v[32:35]
v_perm_b32 v236, v235, v234, s86
v_perm_b32 v237, v235, v234, s87
mm v[32:35],v[192:195],a[4:7],v[32:35]
rt v[220:221],v243 offset:42240
rt v[222:223],v243 offset:43264
mm v[64:67],v[192:195],a[8:11],v[64:67]
aw a147,v236
aw a151,v237
mm v[64:67],v[196:199],a[24:27],v[64:67]
ld v[156:159],v247,s[44:47],s52 offen offset:2048 sc1
mm v[96:99],v[196:199],a[28:31],v[96:99]
ar v230,a163
ar v231,a167
mm v[96:99],v[192:195],a[12:15],v[96:99]
rt v[196:197],v240 offset:33920
rt v[198:199],v240 offset:34944
mm v[100:103],v[200:203],a[12:15],v[100:103]
v_perm_b32 v232, v231, v230, s86
v_perm_b32 v233, v231, v230, s87
mm v[100:103],v[204:207],a[28:31],v[100:103]
ld v[168:171],v244,s[44:47],s52 offen offset:3072 sc1
mm v[68:71],v[204:207],a[24:27],v[68:71]
aw a152,v232
aw a156,v233
mm v[68:71],v[200:203],a[8:11],v[68:71]
rt v[192:193],v240 offset:42368
rt v[194:195],v240 offset:43392
mm v[36:39],v[200:203],a[4:7],v[36:39]
v_add_u32 v240, 4294899712, v240
ar v234,a171
mm v[36:39],v[204:207],a[20:23],v[36:39]
ld v[172:175],v245,s[44:47],s52 offen offset:3072 sc1
mm v[4:7],v[204:207],a[16:19],v[4:7]
wt lgkmcnt(4)
ar v235,a175
mm v[4:7],v[200:203],a[0:3],v[4:7]
rt v[204:205],v241 offset:33920
rt v[206:207],v241 offset:34944
mm v[8:11],v[208:211],a[0:3],v[8:11]
s_nop 0
v_cmp_eq_u64 vcc, v[250:251], v[252:253]
mm v[8:11],v[212:215],a[16:19],v[8:11]
ld v[184:187],v246,s[44:47],s52 offen offset:3072 sc1
mm v[40:43],v[212:215],a[20:23],v[40:43]
v_perm_b32 v236, v235, v234, s86
v_perm_b32 v237, v235, v234, s87
mm v[40:43],v[208:211],a[4:7],v[40:43]
rt v[200:201],v241 offset:42368
rt v[202:203],v241 offset:43392
mm v[72:75],v[208:211],a[8:11],v[72:75]
v_add_u32 v241, 4294899712, v241
aw a153,v236
mm v[72:75],v[212:215],a[24:27],v[72:75]
ld v[188:191],v247,s[44:47],s52 offen offset:3072 sc1
mm v[104:107],v[212:215],a[28:31],v[104:107]
s_bfe_u32 s52, s4, 262172
s_xor_b32 s52, s52, s72
mm v[104:107],v[208:211],a[12:15],v[104:107]
rt v[212:213],v242 offset:33920
rt v[214:215],v242 offset:34944
mm v[108:111],v[216:219],a[12:15],v[108:111]
s_lshl_b32 s52, s52, 13
)ASM" R"ASM(aw a157,v237
mm v[108:111],v[220:223],a[28:31],v[108:111]
buffer_load_dwordx2 v[250:251], v248, s[64:67], s5 offen sc1
mm v[76:79],v[220:223],a[24:27],v[76:79]
ar v230,a179
ar v231,a183
mm v[76:79],v[216:219],a[8:11],v[76:79]
rt v[208:209],v242 offset:42368
rt v[210:211],v242 offset:43392
mm v[44:47],v[216:219],a[4:7],v[44:47]
s_add_u32 m0, s83, 53888
v_add_u32 v242, 4294899712, v242
mm v[44:47],v[220:223],a[20:23],v[44:47]
ld v227,s[56:59],s80 offen lds
mm v[12:15],v[220:223],a[16:19],v[12:15]
wt lgkmcnt(4)
s_add_u32 s80, s80, 131072
mm v[12:15],v[216:219],a[0:3],v[12:15]
rt v[220:221],v243 offset:33920
rt v[222:223],v243 offset:34944
mm v[16:19],v[196:199],a[0:3],v[16:19]
s_add_u32 m0, s83, 0
v_perm_b32 v232, v231, v230, s86
mm v[16:19],v[192:195],a[16:19],v[16:19]
ld v224,s[56:59],s80 offen lds
mm v[48:51],v[192:195],a[20:23],v[48:51]
v_perm_b32 v233, v231, v230, s87
aw a154,v232
mm v[48:51],v[196:199],a[4:7],v[48:51]
rt v[216:217],v243 offset:42368
rt v[218:219],v243 offset:43392
mm v[80:83],v[196:199],a[8:11],v[80:83]
s_add_u32 m0, s83, 1024
v_add_u32 v243, 4294899712, v243
mm v[80:83],v[192:195],a[24:27],v[80:83]
ld v225,s[56:59],s80 offen lds
mm v[112:115],v[192:195],a[28:31],v[112:115]
wt vmcnt(23) lgkmcnt(0)
s_barrier
mm v[112:115],v[196:199],a[12:15],v[112:115]
rt v[192:193],v240 offset:16896
rt v[194:195],v240 offset:17920
mm v[116:119],v[204:207],a[12:15],v[116:119]
s_add_u32 m0, s83, 2176
aw a158,v233
mm v[116:119],v[200:203],a[28:31],v[116:119]
ld v226,s[56:59],s80 offen lds
mm v[84:87],v[200:203],a[24:27],v[84:87]
ar v234,a187
ar v235,a191
mm v[84:87],v[204:207],a[8:11],v[84:87]
rt v[196:197],v240 offset:25344
rt v[198:199],v240 offset:26368
mm v[52:55],v[204:207],a[4:7],v[52:55]
s_add_u32 m0, s83, 3200
v_perm_b32 v236, v235, v234, s86
mm v[52:55],v[200:203],a[20:23],v[52:55]
ld v227,s[56:59],s80 offen lds
mm v[20:23],v[200:203],a[16:19],v[20:23]
s_nop 0
s_bfe_u32 s80, s4, 262164
mm v[20:23],v[204:207],a[0:3],v[20:23]
rt v[200:201],v241 offset:16896
rt v[202:203],v241 offset:17920
mm v[24:27],v[212:215],a[0:3],v[24:27]
s_bitcmp1_b32 vcc_lo, 5
s_cbranch_scc0 .La5_LO_NB5
.La5_LO_KB5:
mm v[24:27],v[208:211],a[16:19],v[24:27]
ld a[52:55],v244,s[44:47],s53 offen offset:0 sc1
mm v[56:59],v[208:211],a[20:23],v[56:59]
s_xor_b32 s80, s80, s72
s_lshl_b32 s80, s80, 18
mm v[56:59],v[212:215],a[4:7],v[56:59]
rt v[204:205],v241 offset:25344
rt v[206:207],v241 offset:26368
mm v[88:91],v[212:215],a[8:11],v[88:91]
s_xor_b32 s80, s80, 7340032
s_add_u32 s80, s80, 131072
mm v[88:91],v[208:211],a[24:27],v[88:91]
ld a[56:59],v245,s[44:47],s53 offen offset:0 sc1
mm v[120:123],v[208:211],a[28:31],v[120:123]
v_perm_b32 v237, v235, v234, s87
aw a155,v236
mm v[120:123],v[212:215],a[12:15],v[120:123]
rt v[208:209],v242 offset:16896
rt v[210:211],v242 offset:17920
mm v[124:127],v[220:223],a[12:15],v[124:127]
aw a159,v237
ar v230,a192
mm v[124:127],v[216:219],a[28:31],v[124:127]
ld a[12:15],v246,s[44:47],s53 offen offset:0 sc1
mm v[92:95],v[216:219],a[24:27],v[92:95]
ar v231,a196
v_perm_b32 v232, v231, v230, s86
mm v[92:95],v[220:223],a[8:11],v[92:95]
rt v[212:213],v242 offset:25344
rt v[214:215],v242 offset:26368
mm v[60:63],v[220:223],a[4:7],v[60:63]
v_perm_b32 v233, v231, v230, s87
)ASM" R"ASM(aw a160,v232
mm v[60:63],v[216:219],a[20:23],v[60:63]
aw a164,v233
ar v234,a200
mm v[28:31],v[216:219],a[16:19],v[28:31]
wt vmcnt(20) lgkmcnt(4)
ar v235,a204
mm v[28:31],v[220:223],a[0:3],v[28:31]
rt v[216:217],v243 offset:16896
rt v[218:219],v243 offset:17920
mm v[0:3],v[192:195],a[32:35],v[0:3]
v_perm_b32 v236, v235, v234, s86
v_perm_b32 v237, v235, v234, s87
mm v[0:3],v[196:199],a[48:51],v[0:3]
ld a[0:3],v247,s[44:47],s53 offen offset:0 sc1
mm v[32:35],v[196:199],a[64:67],v[32:35]
aw a161,v236
aw a165,v237
mm v[32:35],v[192:195],a[36:39],v[32:35]
rt v[220:221],v243 offset:25344
rt v[222:223],v243 offset:26368
mm v[64:67],v[192:195],a[40:43],v[64:67]
ar v230,a208
ar v231,a212
mm v[64:67],v[196:199],a[68:71],v[64:67]
ld a[4:7],v244,s[44:47],s53 offen offset:1024 sc1
mm v[96:99],v[196:199],a[72:75],v[96:99]
v_perm_b32 v232, v231, v230, s86
.La5_LW3_2:
s_cbranch_execz .La5_LW3_6
v_perm_b32 v233, v231, v230, s87
mm v[96:99],v[192:195],a[44:47],v[96:99]
rt v[196:197],v240 offset:17024
rt v[198:199],v240 offset:18048
mm v[100:103],v[200:203],a[44:47],v[100:103]
aw a162,v232
aw a166,v233
mm v[100:103],v[204:207],a[72:75],v[100:103]
ld a[8:11],v245,s[44:47],s53 offen offset:1024 sc1
mm v[68:71],v[204:207],a[68:71],v[68:71]
ar v234,a216
ar v235,a220
mm v[68:71],v[200:203],a[40:43],v[68:71]
rt v[192:193],v240 offset:25472
rt v[194:195],v240 offset:26496
mm v[36:39],v[200:203],a[36:39],v[36:39]
v_perm_b32 v236, v235, v234, s86
v_perm_b32 v237, v235, v234, s87
mm v[36:39],v[204:207],a[64:67],v[36:39]
ld a[16:19],v246,s[44:47],s53 offen offset:1024 sc1
mm v[4:7],v[204:207],a[48:51],v[4:7]
wt lgkmcnt(4)
aw a163,v236
mm v[4:7],v[200:203],a[32:35],v[4:7]
rt v[204:205],v241 offset:17024
rt v[206:207],v241 offset:18048
mm v[8:11],v[208:211],a[32:35],v[8:11]
aw a167,v237
ar v230,a193
mm v[8:11],v[212:215],a[48:51],v[8:11]
ld a[20:23],v247,s[44:47],s53 offen offset:1024 sc1
mm v[40:43],v[212:215],a[64:67],v[40:43]
ar v231,a197
v_perm_b32 v232, v231, v230, s86
mm v[40:43],v[208:211],a[36:39],v[40:43]
rt v[200:201],v241 offset:25472
rt v[202:203],v241 offset:26496
mm v[72:75],v[208:211],a[40:43],v[72:75]
v_perm_b32 v233, v231, v230, s87
aw a168,v232
mm v[72:75],v[212:215],a[68:71],v[72:75]
aw a172,v233
ar v234,a201
mm v[104:107],v[212:215],a[72:75],v[104:107]
ar v235,a205
v_perm_b32 v236, v235, v234, s86
mm v[104:107],v[208:211],a[44:47],v[104:107]
rt v[212:213],v242 offset:17024
rt v[214:215],v242 offset:18048
mm v[108:111],v[216:219],a[44:47],v[108:111]
v_perm_b32 v237, v235, v234, s87
aw a169,v236
mm v[108:111],v[220:223],a[72:75],v[108:111]
aw a173,v237
ar v230,a209
mm v[76:79],v[220:223],a[68:71],v[76:79]
ar v231,a213
v_perm_b32 v232, v231, v230, s86
mm v[76:79],v[216:219],a[40:43],v[76:79]
rt v[208:209],v242 offset:25472
rt v[210:211],v242 offset:26496
mm v[44:47],v[216:219],a[36:39],v[44:47]
v_perm_b32 v233, v231, v230, s87
aw a170,v232
mm v[44:47],v[220:223],a[64:67],v[44:47]
aw a174,v233
ar v234,a217
mm v[12:15],v[220:223],a[48:51],v[12:15]
wt lgkmcnt(4)
ar v235,a221
)ASM" R"ASM(mm v[12:15],v[216:219],a[32:35],v[12:15]
rt v[220:221],v243 offset:17024
rt v[222:223],v243 offset:18048
mm v[16:19],v[196:199],a[32:35],v[16:19]
v_perm_b32 v236, v235, v234, s86
v_perm_b32 v237, v235, v234, s87
mm v[16:19],v[192:195],a[48:51],v[16:19]
ld a[24:27],v244,s[44:47],s53 offen offset:2048 sc1
mm v[48:51],v[192:195],a[64:67],v[48:51]
aw a171,v236
aw a175,v237
mm v[48:51],v[196:199],a[36:39],v[48:51]
rt v[216:217],v243 offset:25472
rt v[218:219],v243 offset:26496
mm v[80:83],v[196:199],a[40:43],v[80:83]
ar v230,a194
ar v231,a198
mm v[80:83],v[192:195],a[68:71],v[80:83]
ld a[28:31],v245,s[44:47],s53 offen offset:2048 sc1
mm v[112:115],v[192:195],a[72:75],v[112:115]
v_perm_b32 v232, v231, v230, s86
v_perm_b32 v233, v231, v230, s87
mm v[112:115],v[196:199],a[44:47],v[112:115]
rt v[192:193],v240 offset:33792
rt v[194:195],v240 offset:34816
mm v[116:119],v[204:207],a[44:47],v[116:119]
aw a176,v232
aw a180,v233
mm v[116:119],v[200:203],a[72:75],v[116:119]
ld a[60:63],v246,s[44:47],s53 offen offset:2048 sc1
mm v[84:87],v[200:203],a[68:71],v[84:87]
ar v234,a202
ar v235,a206
mm v[84:87],v[204:207],a[40:43],v[84:87]
rt v[196:197],v240 offset:42240
rt v[198:199],v240 offset:43264
mm v[52:55],v[204:207],a[36:39],v[52:55]
v_perm_b32 v236, v235, v234, s86
v_perm_b32 v237, v235, v234, s87
mm v[52:55],v[200:203],a[64:67],v[52:55]
ld v[128:131],v247,s[44:47],s53 offen offset:2048 sc1
mm v[20:23],v[200:203],a[48:51],v[20:23]
wt lgkmcnt(4)
aw a177,v236
mm v[20:23],v[204:207],a[32:35],v[20:23]
rt v[200:201],v241 offset:33792
rt v[202:203],v241 offset:34816
mm v[24:27],v[212:215],a[32:35],v[24:27]
aw a181,v237
ar v230,a210
mm v[24:27],v[208:211],a[48:51],v[24:27]
ld v[132:135],v244,s[44:47],s53 offen offset:3072 sc1
mm v[56:59],v[208:211],a[64:67],v[56:59]
ar v231,a214
v_perm_b32 v232, v231, v230, s86
mm v[56:59],v[212:215],a[36:39],v[56:59]
rt v[204:205],v241 offset:42240
rt v[206:207],v241 offset:43264
mm v[88:91],v[212:215],a[40:43],v[88:91]
v_perm_b32 v233, v231, v230, s87
aw a178,v232
mm v[88:91],v[208:211],a[68:71],v[88:91]
ld v[144:147],v245,s[44:47],s53 offen offset:3072 sc1
mm v[120:123],v[208:211],a[72:75],v[120:123]
aw a182,v233
ar v234,a218
mm v[120:123],v[212:215],a[44:47],v[120:123]
rt v[208:209],v242 offset:33792
rt v[210:211],v242 offset:34816
mm v[124:127],v[220:223],a[44:47],v[124:127]
ar v235,a222
v_perm_b32 v236, v235, v234, s86
mm v[124:127],v[216:219],a[72:75],v[124:127]
ld a[44:47],v246,s[44:47],s53 offen offset:3072 sc1
mm v[92:95],v[216:219],a[68:71],v[92:95]
v_perm_b32 v237, v235, v234, s87
aw a179,v236
mm v[92:95],v[220:223],a[40:43],v[92:95]
rt v[212:213],v242 offset:42240
rt v[214:215],v242 offset:43264
mm v[60:63],v[220:223],a[36:39],v[60:63]
aw a183,v237
ar v230,a195
mm v[60:63],v[216:219],a[64:67],v[60:63]
wt vmcnt(20)
v_cmp_eq_u64 vcc, v[250:251], v[252:253]
mm v[28:31],v[216:219],a[48:51],v[28:31]
wt lgkmcnt(4)
ar v231,a199
mm v[28:31],v[220:223],a[32:35],v[28:31]
rt v[216:217],v243 offset:33792
rt v[218:219],v243 offset:34816
mm v[0:3],v[192:195],v[136:139],v[0:3]
v_perm_b32 v232, v231, v230, s86
v_perm_b32 v233, v231, v230, s87
mm v[0:3],v[196:199],v[168:171],v[0:3]
ld a[32:35],v247,s[44:47],s53 offen offset:3072 sc1
mm v[32:35],v[196:199],v[172:175],v[32:35]
aw a184,v232
aw a188,v233
)ASM" R"ASM(mm v[32:35],v[192:195],v[140:143],v[32:35]
rt v[220:221],v243 offset:42240
rt v[222:223],v243 offset:43264
mm v[64:67],v[192:195],v[152:155],v[64:67]
ar v234,a203
ar v235,a207
mm v[64:67],v[196:199],v[184:187],v[64:67]
v_perm_b32 v236, v235, v234, s86
v_perm_b32 v237, v235, v234, s87
mm v[96:99],v[196:199],v[188:191],v[96:99]
aw a185,v236
aw a189,v237
mm v[96:99],v[192:195],v[156:159],v[96:99]
rt v[196:197],v240 offset:33920
rt v[198:199],v240 offset:34944
mm v[100:103],v[200:203],v[156:159],v[100:103]
ar v230,a211
ar v231,a215
mm v[100:103],v[204:207],v[188:191],v[100:103]
buffer_load_dwordx2 v[250:251], v248, s[64:67], s5 offen sc1
mm v[68:71],v[204:207],v[184:187],v[68:71]
v_perm_b32 v232, v231, v230, s86
v_perm_b32 v233, v231, v230, s87
mm v[68:71],v[200:203],v[152:155],v[68:71]
rt v[192:193],v240 offset:42368
rt v[194:195],v240 offset:43392
mm v[36:39],v[200:203],v[140:143],v[36:39]
aw a186,v232
aw a190,v233
mm v[36:39],v[204:207],v[172:175],v[36:39]
ar v234,a219
ar v235,a223
mm v[4:7],v[204:207],v[168:171],v[4:7]
wt lgkmcnt(4)
v_perm_b32 v236, v235, v234, s86
mm v[4:7],v[200:203],v[136:139],v[4:7]
rt v[204:205],v241 offset:33920
rt v[206:207],v241 offset:34944
mm v[8:11],v[208:211],v[136:139],v[8:11]
v_perm_b32 v237, v235, v234, s87
aw a187,v236
mm v[8:11],v[212:215],v[168:171],v[8:11]
aw a191,v237
ar v230,a224
mm v[40:43],v[212:215],v[172:175],v[40:43]
ar v231,a228
v_perm_b32 v232, v231, v230, s86
mm v[40:43],v[208:211],v[140:143],v[40:43]
rt v[200:201],v241 offset:42368
rt v[202:203],v241 offset:43392
mm v[72:75],v[208:211],v[152:155],v[72:75]
v_perm_b32 v233, v231, v230, s87
aw a192,v232
mm v[72:75],v[212:215],v[184:187],v[72:75]
aw a196,v233
ar v234,a232
mm v[104:107],v[212:215],v[188:191],v[104:107]
ar v235,a236
v_perm_b32 v236, v235, v234, s86
mm v[104:107],v[208:211],v[156:159],v[104:107]
rt v[212:213],v242 offset:33920
rt v[214:215],v242 offset:34944
mm v[108:111],v[216:219],v[156:159],v[108:111]
v_perm_b32 v237, v235, v234, s87
aw a193,v236
mm v[108:111],v[220:223],v[188:191],v[108:111]
aw a197,v237
ar v230,a240
mm v[76:79],v[220:223],v[184:187],v[76:79]
ar v231,a244
v_perm_b32 v232, v231, v230, s86
mm v[76:79],v[216:219],v[152:155],v[76:79]
rt v[208:209],v242 offset:42368
rt v[210:211],v242 offset:43392
mm v[44:47],v[216:219],v[140:143],v[44:47]
s_bitcmp1_b32 vcc_lo, 13
s_nop 0
s_nop 0
s_cbranch_scc0 .La5_LO_NB13
.La5_LO_KB13:
mm v[44:47],v[220:223],v[172:175],v[44:47]
v_perm_b32 v233, v231, v230, s87
aw a194,v232
mm v[12:15],v[220:223],v[168:171],v[12:15]
wt lgkmcnt(4)
aw a198,v233
mm v[12:15],v[216:219],v[136:139],v[12:15]
rt v[220:221],v243 offset:33920
rt v[222:223],v243 offset:34944
mm v[16:19],v[196:199],v[136:139],v[16:19]
ar v234,a248
ar v235,a252
mm v[16:19],v[192:195],v[168:171],v[16:19]
ld a[36:39],v244,s[44:47],s54 offen offset:2048 sc1
mm v[48:51],v[192:195],v[172:175],v[48:51]
v_perm_b32 v236, v235, v234, s86
v_perm_b32 v237, v235, v234, s87
mm v[48:51],v[196:199],v[140:143],v[48:51]
rt v[216:217],v243 offset:42368
rt v[218:219],v243 offset:43392
)ASM" R"ASM(mm v[80:83],v[196:199],v[152:155],v[80:83]
aw a195,v236
aw a199,v237
mm v[80:83],v[192:195],v[184:187],v[80:83]
ld a[40:43],v245,s[44:47],s54 offen offset:2048 sc1
mm v[112:115],v[192:195],v[188:191],v[112:115]
wt vmcnt(19)
s_barrier
mm v[112:115],v[196:199],v[156:159],v[112:115]
rt v[192:193],v240 offset:50688
rt v[194:195],v240 offset:51712
mm v[116:119],v[204:207],v[156:159],v[116:119]
s_add_u32 m0, s83, 16896
ar v230,a225
mm v[116:119],v[200:203],v[188:191],v[116:119]
ld v224,s[56:59],s80 offen lds
mm v[84:87],v[200:203],v[184:187],v[84:87]
ar v231,a229
v_perm_b32 v232, v231, v230, s86
mm v[84:87],v[204:207],v[152:155],v[84:87]
rt v[196:197],v240 offset:59136
rt v[198:199],v240 offset:60160
mm v[52:55],v[204:207],v[140:143],v[52:55]
s_add_u32 m0, s83, 17920
v_perm_b32 v233, v231, v230, s87
mm v[52:55],v[200:203],v[172:175],v[52:55]
ld v225,s[56:59],s80 offen lds
mm v[20:23],v[200:203],v[168:171],v[20:23]
wt lgkmcnt(4)
aw a200,v232
mm v[20:23],v[204:207],v[136:139],v[20:23]
rt v[200:201],v241 offset:50688
rt v[202:203],v241 offset:51712
mm v[24:27],v[212:215],v[136:139],v[24:27]
s_add_u32 m0, s83, 19072
aw a204,v233
mm v[24:27],v[208:211],v[168:171],v[24:27]
ld v226,s[56:59],s80 offen lds
mm v[56:59],v[208:211],v[172:175],v[56:59]
ar v234,a233
ar v235,a237
mm v[56:59],v[212:215],v[140:143],v[56:59]
rt v[204:205],v241 offset:59136
rt v[206:207],v241 offset:60160
mm v[88:91],v[212:215],v[152:155],v[88:91]
s_add_u32 m0, s83, 20096
v_perm_b32 v236, v235, v234, s86
mm v[88:91],v[208:211],v[184:187],v[88:91]
ld v227,s[56:59],s80 offen lds
mm v[120:123],v[208:211],v[188:191],v[120:123]
s_bfe_u32 s80, s4, 262168
s_xor_b32 s80, s80, s72
mm v[120:123],v[212:215],v[156:159],v[120:123]
rt v[208:209],v242 offset:50688
rt v[210:211],v242 offset:51712
mm v[124:127],v[220:223],v[156:159],v[124:127]
s_add_u32 m0, s83, 118272
s_lshl_b32 s80, s80, 18
mm v[124:127],v[216:219],v[188:191],v[124:127]
ld v224,s[56:59],s80 offen lds
mm v[92:95],v[216:219],v[184:187],v[92:95]
v_perm_b32 v237, v235, v234, s87
aw a201,v236
mm v[92:95],v[220:223],v[152:155],v[92:95]
rt v[212:213],v242 offset:59136
rt v[214:215],v242 offset:60160
mm v[60:63],v[220:223],v[140:143],v[60:63]
aw a205,v237
ar v230,a241
mm v[60:63],v[216:219],v[172:175],v[60:63]
ar v231,a245
v_perm_b32 v232, v231, v230, s86
mm v[28:31],v[216:219],v[168:171],v[28:31]
wt vmcnt(16) lgkmcnt(4)
v_perm_b32 v233, v231, v230, s87
mm v[28:31],v[220:223],v[136:139],v[28:31]
rt v[216:217],v243 offset:50688
rt v[218:219],v243 offset:51712
mm v[0:3],v[192:195],a[52:55],v[0:3]
s_add_u32 m0, s83, 119296
aw a202,v232
mm v[0:3],v[196:199],a[4:7],v[0:3]
ld v225,s[56:59],s80 offen lds
mm v[32:35],v[196:199],a[8:11],v[32:35]
aw a206,v233
ar v234,a249
mm v[32:35],v[192:195],a[56:59],v[32:35]
rt v[220:221],v243 offset:59136
rt v[222:223],v243 offset:60160
mm v[64:67],v[192:195],a[12:15],v[64:67]
s_add_u32 m0, s83, 120448
ar v235,a253
mm v[64:67],v[196:199],a[16:19],v[64:67]
ld v226,s[56:59],s80 offen lds
mm v[96:99],v[196:199],a[20:23],v[96:99]
v_perm_b32 v236, v235, v234, s86
v_perm_b32 v237, v235, v234, s87
mm v[96:99],v[192:195],a[0:3],v[96:99]
rt v[196:197],v240 offset:50816
rt v[198:199],v240 offset:51840
)ASM" R"ASM(mm v[100:103],v[200:203],a[0:3],v[100:103]
aw a203,v236
aw a207,v237
mm v[100:103],v[204:207],a[20:23],v[100:103]
ld v[136:139],v246,s[44:47],s54 offen offset:2048 sc1
mm v[68:71],v[204:207],a[16:19],v[68:71]
ar v230,a226
ar v231,a230
mm v[68:71],v[200:203],a[12:15],v[68:71]
rt v[192:193],v240 offset:59264
rt v[194:195],v240 offset:60288
mm v[36:39],v[200:203],a[56:59],v[36:39]
v_perm_b32 v232, v231, v230, s86
v_perm_b32 v233, v231, v230, s87
mm v[36:39],v[204:207],a[8:11],v[36:39]
ld v[140:143],v247,s[44:47],s54 offen offset:2048 sc1
mm v[4:7],v[204:207],a[4:7],v[4:7]
wt lgkmcnt(4)
aw a208,v232
mm v[4:7],v[200:203],a[52:55],v[4:7]
rt v[204:205],v241 offset:50816
rt v[206:207],v241 offset:51840
mm v[8:11],v[208:211],a[52:55],v[8:11]
aw a212,v233
ar v234,a234
mm v[8:11],v[212:215],a[4:7],v[8:11]
ld v[152:155],v244,s[44:47],s54 offen offset:3072 sc1
mm v[40:43],v[212:215],a[8:11],v[40:43]
ar v235,a238
v_perm_b32 v236, v235, v234, s86
mm v[40:43],v[208:211],a[56:59],v[40:43]
rt v[200:201],v241 offset:59264
rt v[202:203],v241 offset:60288
mm v[72:75],v[208:211],a[12:15],v[72:75]
v_perm_b32 v237, v235, v234, s87
aw a209,v236
mm v[72:75],v[212:215],a[16:19],v[72:75]
ld v[156:159],v245,s[44:47],s54 offen offset:3072 sc1
mm v[104:107],v[212:215],a[20:23],v[104:107]
aw a213,v237
ar v230,a242
mm v[104:107],v[208:211],a[0:3],v[104:107]
rt v[212:213],v242 offset:50816
rt v[214:215],v242 offset:51840
mm v[108:111],v[216:219],a[0:3],v[108:111]
ar v231,a246
v_perm_b32 v232, v231, v230, s86
mm v[108:111],v[220:223],a[20:23],v[108:111]
ld v[168:171],v246,s[44:47],s54 offen offset:3072 sc1
mm v[76:79],v[220:223],a[16:19],v[76:79]
v_perm_b32 v233, v231, v230, s87
aw a210,v232
mm v[76:79],v[216:219],a[12:15],v[76:79]
rt v[208:209],v242 offset:59264
rt v[210:211],v242 offset:60288
mm v[44:47],v[216:219],a[56:59],v[44:47]
aw a214,v233
ar v234,a250
mm v[44:47],v[220:223],a[8:11],v[44:47]
ar v235,a254
v_perm_b32 v236, v235, v234, s86
mm v[12:15],v[220:223],a[4:7],v[12:15]
wt lgkmcnt(4)
v_perm_b32 v237, v235, v234, s87
mm v[12:15],v[216:219],a[52:55],v[12:15]
rt v[220:221],v243 offset:50816
rt v[222:223],v243 offset:51840
mm v[16:19],v[196:199],a[52:55],v[16:19]
aw a211,v236
aw a215,v237
mm v[16:19],v[192:195],a[4:7],v[16:19]
ld v[172:175],v247,s[44:47],s54 offen offset:3072 sc1
mm v[48:51],v[192:195],a[8:11],v[48:51]
ar v230,a227
ar v231,a231
mm v[48:51],v[196:199],a[56:59],v[48:51]
rt v[216:217],v243 offset:59264
rt v[218:219],v243 offset:60288
mm v[80:83],v[196:199],a[12:15],v[80:83]
s_add_u32 m0, s83, 121472
v_perm_b32 v232, v231, v230, s86
mm v[80:83],v[192:195],a[16:19],v[80:83]
ld v227,s[56:59],s80 offen lds
mm v[112:115],v[192:195],a[20:23],v[112:115]
s_add_u32 s80, s80, 131072
v_perm_b32 v233, v231, v230, s87
mm v[112:115],v[196:199],a[0:3],v[112:115]
rt v[192:193],v240 offset:0
rt v[194:195],v240 offset:1024
mm v[116:119],v[204:207],a[0:3],v[116:119]
s_add_u32 m0, s83, 67584
aw a216,v232
mm v[116:119],v[200:203],a[20:23],v[116:119]
ld v224,s[56:59],s80 offen lds
mm v[84:87],v[200:203],a[16:19],v[84:87]
aw a220,v233
ar v234,a235
mm v[84:87],v[204:207],a[12:15],v[84:87]
rt v[196:197],v240 offset:8448
rt v[198:199],v240 offset:9472
)ASM" R"ASM(mm v[52:55],v[204:207],a[56:59],v[52:55]
s_add_u32 m0, s83, 68608
ar v235,a239
mm v[52:55],v[200:203],a[8:11],v[52:55]
ld v225,s[56:59],s80 offen lds
mm v[20:23],v[200:203],a[4:7],v[20:23]
wt lgkmcnt(4)
v_perm_b32 v236, v235, v234, s86
mm v[20:23],v[204:207],a[52:55],v[20:23]
rt v[200:201],v241 offset:0
rt v[202:203],v241 offset:1024
mm v[24:27],v[212:215],a[52:55],v[24:27]
s_add_u32 m0, s83, 69760
v_perm_b32 v237, v235, v234, s87
mm v[24:27],v[208:211],a[4:7],v[24:27]
ld v226,s[56:59],s80 offen lds
wt vmcnt(19)
v_cmp_eq_u64 vcc, v[250:251], v[252:253]
mm v[56:59],v[208:211],a[8:11],v[56:59]
aw a217,v236
aw a221,v237
mm v[56:59],v[212:215],a[56:59],v[56:59]
rt v[204:205],v241 offset:8448
rt v[206:207],v241 offset:9472
mm v[88:91],v[212:215],a[12:15],v[88:91]
s_add_u32 m0, s83, 70784
ar v230,a243
mm v[88:91],v[208:211],a[16:19],v[88:91]
ld v227,s[56:59],s80 offen lds
mm v[120:123],v[208:211],a[20:23],v[120:123]
s_bfe_u32 s80, s4, 262172
s_xor_b32 s80, s80, s72
mm v[120:123],v[212:215],a[0:3],v[120:123]
rt v[208:209],v242 offset:0
rt v[210:211],v242 offset:1024
mm v[124:127],v[220:223],a[0:3],v[124:127]
s_lshl_b32 s80, s80, 18
ar v231,a247
mm v[124:127],v[216:219],a[20:23],v[124:127]
buffer_load_dwordx2 v[250:251], v248, s[64:67], s5 offen sc1
mm v[92:95],v[216:219],a[16:19],v[92:95]
v_perm_b32 v232, v231, v230, s86
v_perm_b32 v233, v231, v230, s87
mm v[92:95],v[220:223],a[12:15],v[92:95]
rt v[212:213],v242 offset:8448
rt v[214:215],v242 offset:9472
mm v[60:63],v[220:223],a[56:59],v[60:63]
s_add_u32 m0, s83, 84480
aw a218,v232
mm v[60:63],v[216:219],a[8:11],v[60:63]
ld v224,s[56:59],s80 offen lds
mm v[28:31],v[216:219],a[4:7],v[28:31]
wt lgkmcnt(4)
aw a222,v233
mm v[28:31],v[220:223],a[52:55],v[28:31]
.La5_LW3_3:
s_cbranch_execz .La5_LA_RETURN
rt v[216:217],v243 offset:0
rt v[218:219],v243 offset:1024
mm v[0:3],v[192:195],a[24:27],v[0:3]
s_add_u32 m0, s83, 85504
ar v234,a251
mm v[0:3],v[196:199],v[132:135],v[0:3]
ld v225,s[56:59],s80 offen lds
mm v[32:35],v[196:199],v[144:147],v[32:35]
ar v235,a255
v_perm_b32 v236, v235, v234, s86
mm v[32:35],v[192:195],a[28:31],v[32:35]
rt v[220:221],v243 offset:8448
rt v[222:223],v243 offset:9472
mm v[64:67],v[192:195],a[60:63],v[64:67]
s_add_u32 m0, s83, 86656
v_perm_b32 v237, v235, v234, s87
mm v[64:67],v[196:199],a[44:47],v[64:67]
ld v226,s[56:59],s80 offen lds
mm v[96:99],v[196:199],a[32:35],v[96:99]
aw a219,v236
aw a223,v237
mm v[96:99],v[192:195],v[128:131],v[96:99]
rt v[196:197],v240 offset:128
rt v[198:199],v240 offset:1152
mm v[100:103],v[200:203],v[128:131],v[100:103]
s_add_u32 m0, s83, 87680
mm v[100:103],v[204:207],a[32:35],v[100:103]
ld v227,s[56:59],s80 offen lds
mm v[68:71],v[204:207],a[44:47],v[68:71]
s_add_u32 s80, s80, 131072
mm v[68:71],v[200:203],a[60:63],v[68:71]
rt v[192:193],v240 offset:8576
rt v[194:195],v240 offset:9600
mm v[36:39],v[200:203],a[28:31],v[36:39]
s_add_u32 m0, s83, 101376
mm v[36:39],v[204:207],v[144:147],v[36:39]
ld v224,s[56:59],s80 offen lds
mm v[4:7],v[204:207],v[132:135],v[4:7]
wt lgkmcnt(4)
mm v[4:7],v[200:203],a[24:27],v[4:7]
rt v[204:205],v241 offset:128
rt v[206:207],v241 offset:1152
mm v[8:11],v[208:211],a[24:27],v[8:11]
)ASM" R"ASM(s_add_u32 m0, s83, 102400
mm v[8:11],v[212:215],v[132:135],v[8:11]
ld v225,s[56:59],s80 offen lds
mm v[40:43],v[212:215],v[144:147],v[40:43]
mm v[40:43],v[208:211],a[28:31],v[40:43]
rt v[200:201],v241 offset:8576
rt v[202:203],v241 offset:9600
mm v[72:75],v[208:211],a[60:63],v[72:75]
s_add_u32 m0, s83, 103552
mm v[72:75],v[212:215],a[44:47],v[72:75]
ld v226,s[56:59],s80 offen lds
mm v[104:107],v[212:215],a[32:35],v[104:107]
mm v[104:107],v[208:211],v[128:131],v[104:107]
rt v[212:213],v242 offset:128
rt v[214:215],v242 offset:1152
mm v[108:111],v[216:219],v[128:131],v[108:111]
s_add_u32 m0, s83, 104576
mm v[108:111],v[220:223],a[32:35],v[108:111]
ld v227,s[56:59],s80 offen lds
mm v[76:79],v[220:223],a[44:47],v[76:79]
v_bfe_u32 v224, v255, 6, 1
v_lshrrev_b32 v225, 7, v255
mm v[76:79],v[216:219],a[60:63],v[76:79]
rt v[208:209],v242 offset:8576
rt v[210:211],v242 offset:9600
mm v[44:47],v[216:219],a[28:31],v[44:47]
s_bitcmp1_b32 vcc_lo, 6
s_cbranch_scc0 .La5_LO_NB6
.La5_LO_KB6:
mm v[44:47],v[220:223],v[144:147],v[44:47]
v_and_b32 v226, 48, v255
v_lshl_add_u32 v225, v225, 6, v226
mm v[12:15],v[220:223],v[132:135],v[12:15]
wt lgkmcnt(4)
v_lshl_add_u32 v224, v224, 11, v225
mm v[12:15],v[216:219],a[24:27],v[12:15]
rt v[220:221],v243 offset:128
rt v[222:223],v243 offset:1152
mm v[16:19],v[196:199],a[24:27],v[16:19]
mm v[16:19],v[192:195],v[132:135],v[16:19]
ld a[64:67],v244,s[44:47],s55 offen offset:0 sc1
mm v[48:51],v[192:195],v[144:147],v[48:51]
mm v[48:51],v[196:199],a[28:31],v[48:51]
rt v[216:217],v243 offset:8576
rt v[218:219],v243 offset:9600
mm v[80:83],v[196:199],a[60:63],v[80:83]
mm v[80:83],v[192:195],a[44:47],v[80:83]
ld a[68:71],v245,s[44:47],s55 offen offset:0 sc1
mm v[112:115],v[192:195],a[32:35],v[112:115]
wt vmcnt(25)
s_barrier
mm v[112:115],v[196:199],v[128:131],v[112:115]
rt v[192:193],v240 offset:16896
rt v[194:195],v240 offset:17920
mm v[116:119],v[204:207],v[128:131],v[116:119]
mm v[116:119],v[200:203],a[32:35],v[116:119]
ld a[72:75],v244,s[44:47],s55 offen offset:1024 sc1
mm v[84:87],v[200:203],a[44:47],v[84:87]
mm v[84:87],v[204:207],a[60:63],v[84:87]
rt v[196:197],v240 offset:25344
rt v[198:199],v240 offset:26368
mm v[52:55],v[204:207],a[28:31],v[52:55]
mm v[52:55],v[200:203],v[144:147],v[52:55]
ld a[76:79],v245,s[44:47],s55 offen offset:1024 sc1
mm v[20:23],v[200:203],v[132:135],v[20:23]
wt lgkmcnt(4)
mm v[20:23],v[204:207],a[24:27],v[20:23]
rt v[200:201],v241 offset:16896
rt v[202:203],v241 offset:17920
mm v[24:27],v[212:215],a[24:27],v[24:27]
mm v[24:27],v[208:211],v[132:135],v[24:27]
ld a[80:83],v244,s[44:47],s55 offen offset:2048 sc1
mm v[56:59],v[208:211],v[144:147],v[56:59]
mm v[56:59],v[212:215],a[28:31],v[56:59]
rt v[204:205],v241 offset:25344
rt v[206:207],v241 offset:26368
mm v[88:91],v[212:215],a[60:63],v[88:91]
mm v[88:91],v[208:211],a[44:47],v[88:91]
ld a[84:87],v245,s[44:47],s55 offen offset:2048 sc1
mm v[120:123],v[208:211],a[32:35],v[120:123]
mm v[120:123],v[212:215],v[128:131],v[120:123]
rt v[208:209],v242 offset:16896
rt v[210:211],v242 offset:17920
mm v[124:127],v[220:223],v[128:131],v[124:127]
mm v[124:127],v[216:219],a[32:35],v[124:127]
ld a[88:91],v244,s[44:47],s55 offen offset:3072 sc1
mm v[92:95],v[216:219],a[44:47],v[92:95]
mm v[92:95],v[220:223],a[60:63],v[92:95]
rt v[212:213],v242 offset:25344
rt v[214:215],v242 offset:26368
mm v[60:63],v[220:223],a[28:31],v[60:63]
mm v[60:63],v[216:219],v[144:147],v[60:63]
mm v[28:31],v[216:219],v[132:135],v[28:31]
wt vmcnt(21) lgkmcnt(4)
mm v[28:31],v[220:223],a[24:27],v[28:31]
rt v[216:217],v243 offset:16896
rt v[218:219],v243 offset:17920
mm v[0:3],v[192:195],a[36:39],v[0:3]
s_bitcmp1_b32 vcc_lo, 7
s_cbranch_scc0 .La5_LO_NB7
)ASM" R"ASM(.La5_LO_KB7:
mm v[0:3],v[196:199],v[152:155],v[0:3]
ld a[92:95],v245,s[44:47],s55 offen offset:3072 sc1
mm v[32:35],v[196:199],v[156:159],v[32:35]
mm v[32:35],v[192:195],a[40:43],v[32:35]
rt v[220:221],v243 offset:25344
rt v[222:223],v243 offset:26368
mm v[64:67],v[192:195],v[136:139],v[64:67]
mm v[64:67],v[196:199],v[168:171],v[64:67]
ld a[0:3],v244,s[44:47],s52 offen offset:0 sc1
mm v[96:99],v[196:199],v[172:175],v[96:99]
mm v[96:99],v[192:195],v[140:143],v[96:99]
rt v[196:197],v240 offset:17024
rt v[198:199],v240 offset:18048
mm v[100:103],v[200:203],v[140:143],v[100:103]
mm v[100:103],v[204:207],v[172:175],v[100:103]
ld a[4:7],v245,s[44:47],s52 offen offset:0 sc1
mm v[68:71],v[204:207],v[168:171],v[68:71]
mm v[68:71],v[200:203],v[136:139],v[68:71]
rt v[192:193],v240 offset:25472
rt v[194:195],v240 offset:26496
mm v[36:39],v[200:203],a[40:43],v[36:39]
v_add_u32 v240, 67584, v240
mm v[36:39],v[204:207],v[156:159],v[36:39]
ld a[8:11],v244,s[44:47],s52 offen offset:1024 sc1
mm v[4:7],v[204:207],v[152:155],v[4:7]
wt lgkmcnt(4)
mm v[4:7],v[200:203],a[36:39],v[4:7]
rt v[204:205],v241 offset:17024
rt v[206:207],v241 offset:18048
mm v[8:11],v[208:211],a[36:39],v[8:11]
mm v[8:11],v[212:215],v[152:155],v[8:11]
ld a[12:15],v245,s[44:47],s52 offen offset:1024 sc1
mm v[40:43],v[212:215],v[156:159],v[40:43]
mm v[40:43],v[208:211],a[40:43],v[40:43]
rt v[200:201],v241 offset:25472
rt v[202:203],v241 offset:26496
mm v[72:75],v[208:211],v[136:139],v[72:75]
v_add_u32 v241, 67584, v241
mm v[72:75],v[212:215],v[168:171],v[72:75]
ld a[16:19],v244,s[44:47],s52 offen offset:2048 sc1
mm v[104:107],v[212:215],v[172:175],v[104:107]
mm v[104:107],v[208:211],v[140:143],v[104:107]
rt v[212:213],v242 offset:17024
rt v[214:215],v242 offset:18048
mm v[108:111],v[216:219],v[140:143],v[108:111]
mm v[108:111],v[220:223],v[172:175],v[108:111]
ld a[20:23],v245,s[44:47],s52 offen offset:2048 sc1
mm v[76:79],v[220:223],v[168:171],v[76:79]
mm v[76:79],v[216:219],v[136:139],v[76:79]
rt v[208:209],v242 offset:25472
rt v[210:211],v242 offset:26496
mm v[44:47],v[216:219],a[40:43],v[44:47]
v_add_u32 v242, 67584, v242
mm v[44:47],v[220:223],v[156:159],v[44:47]
wt vmcnt(22)
v_cmp_eq_u64 vcc, v[250:251], v[252:253]
mm v[12:15],v[220:223],v[152:155],v[12:15]
wt lgkmcnt(4)
mm v[12:15],v[216:219],a[36:39],v[12:15]
rt v[220:221],v243 offset:17024
rt v[222:223],v243 offset:18048
mm v[16:19],v[196:199],a[36:39],v[16:19]
mm v[16:19],v[192:195],v[152:155],v[16:19]
ld a[24:27],v244,s[44:47],s52 offen offset:3072 sc1
mm v[48:51],v[192:195],v[156:159],v[48:51]
mm v[48:51],v[196:199],a[40:43],v[48:51]
rt v[216:217],v243 offset:25472
rt v[218:219],v243 offset:26496
mm v[80:83],v[196:199],v[136:139],v[80:83]
v_add_u32 v243, 67584, v243
mm v[80:83],v[192:195],v[168:171],v[80:83]
ld a[28:31],v245,s[44:47],s52 offen offset:3072 sc1
mm v[112:115],v[192:195],v[172:175],v[112:115]
wt vmcnt(16)
s_barrier
mm v[112:115],v[196:199],v[140:143],v[112:115]
rt v[192:193],v240 offset:50688
rt v[194:195],v240 offset:51712
mm v[116:119],v[204:207],v[140:143],v[116:119]
mm v[116:119],v[200:203],v[172:175],v[116:119]
ld a[32:35],v246,s[44:47],s55 offen offset:0 sc1
mm v[84:87],v[200:203],v[168:171],v[84:87]
mm v[84:87],v[204:207],v[136:139],v[84:87]
rt v[196:197],v240 offset:59136
rt v[198:199],v240 offset:60160
mm v[52:55],v[204:207],a[40:43],v[52:55]
mm v[52:55],v[200:203],v[156:159],v[52:55]
ld a[44:47],v247,s[44:47],s55 offen offset:0 sc1
mm v[20:23],v[200:203],v[152:155],v[20:23]
wt lgkmcnt(4)
mm v[20:23],v[204:207],a[36:39],v[20:23]
rt v[200:201],v240 offset:0
rt v[202:203],v240 offset:1024
mm v[24:27],v[212:215],a[36:39],v[24:27]
mm v[24:27],v[208:211],v[152:155],v[24:27]
ld a[48:51],v246,s[44:47],s55 offen offset:1024 sc1
mm v[56:59],v[208:211],v[156:159],v[56:59]
mm v[56:59],v[212:215],a[40:43],v[56:59]
rt v[204:205],v240 offset:8448
)ASM" R"ASM(rt v[206:207],v240 offset:9472
mm v[88:91],v[212:215],v[136:139],v[88:91]
mm v[88:91],v[208:211],v[168:171],v[88:91]
ld a[52:55],v247,s[44:47],s55 offen offset:1024 sc1
mm v[120:123],v[208:211],v[172:175],v[120:123]
mm v[120:123],v[212:215],v[140:143],v[120:123]
rt v[208:209],v240 offset:16896
rt v[210:211],v240 offset:17920
mm v[124:127],v[220:223],v[140:143],v[124:127]
mm v[124:127],v[216:219],v[172:175],v[124:127]
ld a[56:59],v246,s[44:47],s55 offen offset:2048 sc1
mm v[92:95],v[216:219],v[168:171],v[92:95]
mm v[92:95],v[220:223],v[136:139],v[92:95]
rt v[212:213],v240 offset:25344
rt v[214:215],v240 offset:26368
mm v[60:63],v[220:223],a[40:43],v[60:63]
mm v[60:63],v[216:219],v[156:159],v[60:63]
mm v[28:31],v[216:219],v[152:155],v[28:31]
wt vmcnt(13) lgkmcnt(4)
mm v[28:31],v[220:223],a[36:39],v[28:31]
rt v[216:217],v240 offset:33792
rt v[218:219],v240 offset:34816
mm v[0:3],v[192:195],a[64:67],v[0:3]
ld a[36:39],v247,s[44:47],s55 offen offset:2048 sc1
mm v[0:3],v[196:199],a[72:75],v[0:3]
rt v[220:221],v240 offset:42240
rt v[222:223],v240 offset:43264
mm v[32:35],v[196:199],a[76:79],v[32:35]
mm v[32:35],v[192:195],a[68:71],v[32:35]
rt v[196:197],v241 offset:50688
rt v[198:199],v241 offset:51712
mm v[32:35],v[200:203],a[84:87],v[32:35]
ld a[40:43],v246,s[44:47],s55 offen offset:3072 sc1
mm v[32:35],v[204:207],a[92:95],v[32:35]
rt v[192:193],v241 offset:59136
rt v[194:195],v241 offset:60160
mm v[0:3],v[204:207],a[88:91],v[0:3]
wt vmcnt(7) lgkmcnt(4)
mm v[0:3],v[200:203],a[80:83],v[0:3]
rt v[204:205],v241 offset:0
rt v[206:207],v241 offset:1024
mm v[0:3],v[208:211],a[0:3],v[0:3]
ld a[60:63],v247,s[44:47],s55 offen offset:3072 sc1
mm v[0:3],v[212:215],a[8:11],v[0:3]
rt v[200:201],v241 offset:8448
rt v[202:203],v241 offset:9472
mm v[32:35],v[212:215],a[12:15],v[32:35]
mm v[32:35],v[208:211],a[4:7],v[32:35]
rt v[212:213],v241 offset:16896
rt v[214:215],v241 offset:17920
mm v[32:35],v[216:219],a[20:23],v[32:35]
ld v[128:131],v246,s[44:47],s52 offen offset:0 sc1
mm v[32:35],v[220:223],a[28:31],v[32:35]
rt v[208:209],v241 offset:25344
rt v[210:211],v241 offset:26368
mm v[0:3],v[220:223],a[24:27],v[0:3]
wt lgkmcnt(4)
mm v[0:3],v[216:219],a[16:19],v[0:3]
rt v[220:221],v241 offset:33792
rt v[222:223],v241 offset:34816
mm v[4:7],v[196:199],a[64:67],v[4:7]
ld v[132:135],v247,s[44:47],s52 offen offset:0 sc1
mm v[4:7],v[192:195],a[72:75],v[4:7]
rt v[216:217],v241 offset:42240
rt v[218:219],v241 offset:43264
mm v[36:39],v[192:195],a[76:79],v[36:39]
mm v[36:39],v[196:199],a[68:71],v[36:39]
rt v[192:193],v242 offset:50688
rt v[194:195],v242 offset:51712
mm v[36:39],v[204:207],a[84:87],v[36:39]
ld v[144:147],v246,s[44:47],s52 offen offset:1024 sc1
mm v[36:39],v[200:203],a[92:95],v[36:39]
rt v[196:197],v242 offset:59136
rt v[198:199],v242 offset:60160
mm v[4:7],v[200:203],a[88:91],v[4:7]
wt lgkmcnt(4)
mm v[4:7],v[204:207],a[80:83],v[4:7]
rt v[200:201],v242 offset:0
rt v[202:203],v242 offset:1024
mm v[4:7],v[212:215],a[0:3],v[4:7]
ld v[148:151],v247,s[44:47],s52 offen offset:1024 sc1
mm v[4:7],v[208:211],a[8:11],v[4:7]
rt v[204:205],v242 offset:8448
rt v[206:207],v242 offset:9472
mm v[36:39],v[208:211],a[12:15],v[36:39]
mm v[36:39],v[212:215],a[4:7],v[36:39]
rt v[208:209],v242 offset:16896
rt v[210:211],v242 offset:17920
mm v[36:39],v[220:223],a[20:23],v[36:39]
mm v[36:39],v[216:219],a[28:31],v[36:39]
rt v[212:213],v242 offset:25344
rt v[214:215],v242 offset:26368
mm v[4:7],v[216:219],a[24:27],v[4:7]
wt lgkmcnt(4)
mm v[4:7],v[220:223],a[16:19],v[4:7]
rt v[216:217],v242 offset:33792
rt v[218:219],v242 offset:34816
mm v[8:11],v[192:195],a[64:67],v[8:11]
ld v[160:163],v246,s[44:47],s52 offen offset:2048 sc1
mm v[8:11],v[196:199],a[72:75],v[8:11]
)ASM" R"ASM(rt v[220:221],v242 offset:42240
rt v[222:223],v242 offset:43264
mm v[40:43],v[196:199],a[76:79],v[40:43]
cv v234,v32,v33
cv v236,v36,v37
mm v[40:43],v[192:195],a[68:71],v[40:43]
rt v[196:197],v243 offset:50688
rt v[198:199],v243 offset:51712
mm v[40:43],v[200:203],a[84:87],v[40:43]
ld v[164:167],v247,s[44:47],s52 offen offset:2048 sc1
mm v[40:43],v[204:207],a[92:95],v[40:43]
rt v[192:193],v243 offset:59136
rt v[194:195],v243 offset:60160
mm v[8:11],v[204:207],a[88:91],v[8:11]
wt lgkmcnt(4)
mm v[8:11],v[200:203],a[80:83],v[8:11]
rt v[204:205],v243 offset:0
rt v[206:207],v243 offset:1024
mm v[8:11],v[208:211],a[0:3],v[8:11]
ld v[176:179],v246,s[44:47],s52 offen offset:3072 sc1
mm v[8:11],v[212:215],a[8:11],v[8:11]
rt v[200:201],v243 offset:8448
rt v[202:203],v243 offset:9472
mm v[40:43],v[212:215],a[12:15],v[40:43]
cv v235,v34,v35
cv v237,v38,v39
mm v[40:43],v[208:211],a[4:7],v[40:43]
rt v[212:213],v243 offset:16896
rt v[214:215],v243 offset:17920
mm v[40:43],v[216:219],a[20:23],v[40:43]
ld v[180:183],v247,s[44:47],s52 offen offset:3072 sc1
mm v[40:43],v[220:223],a[28:31],v[40:43]
rt v[208:209],v243 offset:25344
rt v[210:211],v243 offset:26368
mm v[8:11],v[220:223],a[24:27],v[8:11]
wt lgkmcnt(4)
v_bfe_u32 v250, v255, 6, 1
mm v[8:11],v[216:219],a[16:19],v[8:11]
rt v[220:221],v243 offset:33792
rt v[222:223],v243 offset:34816
mm v[12:15],v[196:199],a[64:67],v[12:15]
v_permlane16_swap_b32 v234, v236
v_permlane16_swap_b32 v235, v237
mm v[12:15],v[192:195],a[72:75],v[12:15]
rt v[216:217],v243 offset:42240
rt v[218:219],v243 offset:43264
mm v[44:47],v[192:195],a[76:79],v[44:47]
cv v230,v0,v1
cv v232,v4,v5
mm v[44:47],v[196:199],a[68:71],v[44:47]
rt v[192:193],v240 offset:50816
rt v[194:195],v240 offset:51840
mm v[44:47],v[204:207],a[84:87],v[44:47]
st v[234:237],v238,s[32:35],s95 offen offset:0 sc1
mm v[44:47],v[200:203],a[92:95],v[44:47]
rt v[196:197],v240 offset:59264
rt v[198:199],v240 offset:60288
mm v[12:15],v[200:203],a[88:91],v[12:15]
wt lgkmcnt(4)
v_mul_u32_u24 v250, 7, v250
mm v[12:15],v[204:207],a[80:83],v[12:15]
rt v[200:201],v240 offset:128
rt v[202:203],v240 offset:1152
mm v[12:15],v[212:215],a[0:3],v[12:15]
cv v231,v2,v3
cv v233,v6,v7
mm v[12:15],v[208:211],a[8:11],v[12:15]
rt v[204:205],v240 offset:8576
rt v[206:207],v240 offset:9600
mm v[44:47],v[208:211],a[12:15],v[44:47]
v_permlane16_swap_b32 v230, v232
v_permlane16_swap_b32 v231, v233
mm v[44:47],v[212:215],a[4:7],v[44:47]
rt v[208:209],v240 offset:17024
rt v[210:211],v240 offset:18048
mm v[44:47],v[220:223],a[20:23],v[44:47]
st v[230:233],v238,s[32:35],s94 offen offset:0 sc1
mm v[44:47],v[216:219],a[28:31],v[44:47]
rt v[212:213],v240 offset:25472
rt v[214:215],v240 offset:26496
mm v[12:15],v[216:219],a[24:27],v[12:15]
wt lgkmcnt(4)
v_xor_b32 v250, s3, v250
mm v[12:15],v[220:223],a[16:19],v[12:15]
rt v[216:217],v240 offset:33920
rt v[218:219],v240 offset:34944
mm v[16:19],v[192:195],a[64:67],v[16:19]
cv v234,v40,v41
cv v236,v44,v45
mm v[16:19],v[196:199],a[72:75],v[16:19]
rt v[220:221],v240 offset:42368
rt v[222:223],v240 offset:43392
mm v[48:51],v[196:199],a[76:79],v[48:51]
cv v235,v42,v43
cv v237,v46,v47
mm v[48:51],v[192:195],a[68:71],v[48:51]
rt v[196:197],v241 offset:50816
rt v[198:199],v241 offset:51840
mm v[48:51],v[200:203],a[84:87],v[48:51]
v_permlane16_swap_b32 v234, v236
)ASM" R"ASM(v_permlane16_swap_b32 v235, v237
mm v[48:51],v[204:207],a[92:95],v[48:51]
rt v[192:193],v241 offset:59264
rt v[194:195],v241 offset:60288
mm v[16:19],v[204:207],a[88:91],v[16:19]
wt lgkmcnt(4)
v_bfe_u32 v251, v255, 4, 2
mm v[16:19],v[200:203],a[80:83],v[16:19]
rt v[204:205],v241 offset:128
rt v[206:207],v241 offset:1152
mm v[16:19],v[208:211],a[0:3],v[16:19]
st v[234:237],v238,s[32:35],s95 offen offset:1024 sc1
mm v[16:19],v[212:215],a[8:11],v[16:19]
rt v[200:201],v241 offset:8576
rt v[202:203],v241 offset:9600
mm v[48:51],v[212:215],a[12:15],v[48:51]
cv v230,v8,v9
cv v232,v12,v13
mm v[48:51],v[208:211],a[4:7],v[48:51]
rt v[212:213],v241 offset:17024
rt v[214:215],v241 offset:18048
mm v[48:51],v[216:219],a[20:23],v[48:51]
cv v231,v10,v11
cv v233,v14,v15
mm v[48:51],v[220:223],a[28:31],v[48:51]
rt v[208:209],v241 offset:25472
rt v[210:211],v241 offset:26496
mm v[16:19],v[220:223],a[24:27],v[16:19]
wt lgkmcnt(4)
v_lshl_add_u32 v250, v250, 2, v251
mm v[16:19],v[216:219],a[16:19],v[16:19]
rt v[220:221],v241 offset:33920
rt v[222:223],v241 offset:34944
mm v[20:23],v[196:199],a[64:67],v[20:23]
v_permlane16_swap_b32 v230, v232
v_permlane16_swap_b32 v231, v233
mm v[20:23],v[192:195],a[72:75],v[20:23]
rt v[216:217],v241 offset:42368
rt v[218:219],v241 offset:43392
mm v[52:55],v[192:195],a[76:79],v[52:55]
v_lshlrev_b32 v250, 9, v250
v_and_b32 v251, 0x2000, v250
mm v[52:55],v[196:199],a[68:71],v[52:55]
rt v[192:193],v242 offset:50816
rt v[194:195],v242 offset:51840
mm v[52:55],v[204:207],a[84:87],v[52:55]
st v[230:233],v238,s[32:35],s94 offen offset:1024 sc1
mm v[52:55],v[200:203],a[92:95],v[52:55]
rt v[196:197],v242 offset:59264
rt v[198:199],v242 offset:60288
mm v[20:23],v[200:203],a[88:91],v[20:23]
wt lgkmcnt(4)
v_add_u32 v250, v250, v251
mm v[20:23],v[204:207],a[80:83],v[20:23]
rt v[200:201],v242 offset:128
rt v[202:203],v242 offset:1152
mm v[20:23],v[212:215],a[0:3],v[20:23]
ld v[136:139],v224,s[68:71],0 offen offset:0
mm v[20:23],v[208:211],a[8:11],v[20:23]
rt v[204:205],v242 offset:8576
rt v[206:207],v242 offset:9600
mm v[52:55],v[208:211],a[12:15],v[52:55]
v_bfe_u32 v251, v255, 7, 1
v_lshl_add_u32 v250, v251, 13, v250
mm v[52:55],v[212:215],a[4:7],v[52:55]
rt v[208:209],v242 offset:17024
rt v[210:211],v242 offset:18048
mm v[52:55],v[220:223],a[20:23],v[52:55]
ld v[140:143],v225,s[48:51],0 offen offset:0
mm v[52:55],v[216:219],a[28:31],v[52:55]
rt v[212:213],v242 offset:25472
rt v[214:215],v242 offset:26496
mm v[20:23],v[216:219],a[24:27],v[20:23]
wt lgkmcnt(4)
s_mov_b32 s93, 0
ld a[224:227],v229,s[28:31],s93 offen offset:0
mm v[20:23],v[220:223],a[16:19],v[20:23]
rt v[216:217],v242 offset:33920
rt v[218:219],v242 offset:34944
mm v[24:27],v[192:195],a[64:67],v[24:27]
cv v234,v48,v49
cv v236,v52,v53
mm v[24:27],v[196:199],a[72:75],v[24:27]
rt v[220:221],v242 offset:42368
rt v[222:223],v242 offset:43392
mm v[56:59],v[196:199],a[76:79],v[56:59]
cv v235,v50,v51
cv v237,v54,v55
mm v[56:59],v[192:195],a[68:71],v[56:59]
rt v[196:197],v243 offset:50816
rt v[198:199],v243 offset:51840
mm v[56:59],v[200:203],a[84:87],v[56:59]
v_permlane16_swap_b32 v234, v236
v_permlane16_swap_b32 v235, v237
.La5_LW3_4:
s_cbranch_execz .La5_LA_RETURN
mm v[56:59],v[204:207],a[92:95],v[56:59]
rt v[192:193],v243 offset:59264
rt v[194:195],v243 offset:60288
mm v[24:27],v[204:207],a[88:91],v[24:27]
)ASM" R"ASM(wt lgkmcnt(4)
ld a[228:231],v229,s[28:31],s93 offen offset:2048
mm v[24:27],v[200:203],a[80:83],v[24:27]
rt v[204:205],v243 offset:128
rt v[206:207],v243 offset:1152
mm v[24:27],v[208:211],a[0:3],v[24:27]
st v[234:237],v238,s[32:35],s95 offen offset:2048 sc1
mm v[24:27],v[212:215],a[8:11],v[24:27]
rt v[200:201],v243 offset:8576
rt v[202:203],v243 offset:9600
mm v[56:59],v[212:215],a[12:15],v[56:59]
cv v230,v16,v17
cv v232,v20,v21
mm v[56:59],v[208:211],a[4:7],v[56:59]
rt v[212:213],v243 offset:17024
rt v[214:215],v243 offset:18048
mm v[56:59],v[216:219],a[20:23],v[56:59]
cv v231,v18,v19
cv v233,v22,v23
mm v[56:59],v[220:223],a[28:31],v[56:59]
rt v[208:209],v243 offset:25472
rt v[210:211],v243 offset:26496
mm v[24:27],v[220:223],a[24:27],v[24:27]
wt lgkmcnt(4)
s_mov_b32 s93, 4096
ld a[232:235],v229,s[28:31],s93 offen offset:0
mm v[24:27],v[216:219],a[16:19],v[24:27]
rt v[220:221],v243 offset:33920
rt v[222:223],v243 offset:34944
mm v[28:31],v[196:199],a[64:67],v[28:31]
v_permlane16_swap_b32 v230, v232
v_permlane16_swap_b32 v231, v233
mm v[28:31],v[192:195],a[72:75],v[28:31]
rt v[216:217],v243 offset:42368
rt v[218:219],v243 offset:43392
mm v[60:63],v[192:195],a[76:79],v[60:63]
ld a[236:239],v229,s[28:31],s93 offen offset:2048
mm v[60:63],v[196:199],a[68:71],v[60:63]
rt v[192:193],v240 offset:50688
rt v[194:195],v240 offset:51712
mm v[60:63],v[204:207],a[84:87],v[60:63]
st v[230:233],v238,s[32:35],s94 offen offset:2048 sc1
mm v[60:63],v[200:203],a[92:95],v[60:63]
rt v[196:197],v240 offset:59136
rt v[198:199],v240 offset:60160
mm v[28:31],v[200:203],a[88:91],v[28:31]
wt lgkmcnt(4)
s_mov_b32 s93, 8192
ld a[240:243],v229,s[28:31],s93 offen offset:0
mm v[28:31],v[204:207],a[80:83],v[28:31]
rt v[200:201],v240 offset:0
rt v[202:203],v240 offset:1024
mm v[28:31],v[212:215],a[0:3],v[28:31]
ld v[152:155],v224,s[68:71],0 offen offset:512
mm v[28:31],v[208:211],a[8:11],v[28:31]
rt v[204:205],v240 offset:8448
rt v[206:207],v240 offset:9472
mm v[60:63],v[208:211],a[12:15],v[60:63]
ld a[244:247],v229,s[28:31],s93 offen offset:2048
mm v[60:63],v[212:215],a[4:7],v[60:63]
rt v[208:209],v240 offset:16896
rt v[210:211],v240 offset:17920
mm v[60:63],v[220:223],a[20:23],v[60:63]
ld v[156:159],v225,s[48:51],0 offen offset:512
mm v[60:63],v[216:219],a[28:31],v[60:63]
rt v[212:213],v240 offset:25344
rt v[214:215],v240 offset:26368
mm v[28:31],v[216:219],a[24:27],v[28:31]
wt vmcnt(24) lgkmcnt(4)
s_mov_b32 s93, 12288
ld a[248:251],v229,s[28:31],s93 offen offset:0
mm v[28:31],v[220:223],a[16:19],v[28:31]
rt v[216:217],v240 offset:33792
rt v[218:219],v240 offset:34816
mm v[64:67],v[192:195],a[32:35],v[64:67]
cv v234,v56,v57
cv v236,v60,v61
mm v[64:67],v[196:199],a[48:51],v[64:67]
rt v[220:221],v240 offset:42240
rt v[222:223],v240 offset:43264
mm v[96:99],v[196:199],a[52:55],v[96:99]
cv v235,v58,v59
cv v237,v62,v63
mm v[96:99],v[192:195],a[44:47],v[96:99]
rt v[196:197],v241 offset:50688
rt v[198:199],v241 offset:51712
mm v[96:99],v[200:203],a[36:39],v[96:99]
v_permlane16_swap_b32 v234, v236
v_permlane16_swap_b32 v235, v237
mm v[96:99],v[204:207],a[60:63],v[96:99]
rt v[192:193],v241 offset:59136
rt v[194:195],v241 offset:60160
mm v[64:67],v[204:207],a[40:43],v[64:67]
wt vmcnt(17) lgkmcnt(4)
ld a[252:255],v229,s[28:31],s93 offen offset:2048
mm v[64:67],v[200:203],a[56:59],v[64:67]
rt v[204:205],v241 offset:0
rt v[206:207],v241 offset:1024
mm v[64:67],v[208:211],v[128:131],v[64:67]
st v[234:237],v238,s[32:35],s95 offen offset:3072 sc1
)ASM" R"ASM(mm v[64:67],v[212:215],v[144:147],v[64:67]
rt v[200:201],v241 offset:8448
rt v[202:203],v241 offset:9472
mm v[96:99],v[212:215],v[148:151],v[96:99]
cv v230,v24,v25
cv v232,v28,v29
mm v[96:99],v[208:211],v[132:135],v[96:99]
rt v[212:213],v241 offset:16896
rt v[214:215],v241 offset:17920
mm v[96:99],v[216:219],v[164:167],v[96:99]
cv v231,v26,v27
cv v233,v30,v31
mm v[96:99],v[220:223],v[180:183],v[96:99]
rt v[208:209],v241 offset:25344
rt v[210:211],v241 offset:26368
mm v[64:67],v[220:223],v[176:179],v[64:67]
wt lgkmcnt(4)
s_mov_b32 s93, 0
ld a[64:67],v229,s[28:31],s93 offen offset:256
mm v[64:67],v[216:219],v[160:163],v[64:67]
rt v[220:221],v241 offset:33792
rt v[222:223],v241 offset:34816
mm v[68:71],v[196:199],a[32:35],v[68:71]
v_permlane16_swap_b32 v230, v232
v_permlane16_swap_b32 v231, v233
mm v[68:71],v[192:195],a[48:51],v[68:71]
rt v[216:217],v241 offset:42240
rt v[218:219],v241 offset:43264
mm v[100:103],v[192:195],a[52:55],v[100:103]
ld a[68:71],v229,s[28:31],s93 offen offset:2304
mm v[100:103],v[196:199],a[44:47],v[100:103]
rt v[192:193],v242 offset:50688
rt v[194:195],v242 offset:51712
mm v[100:103],v[204:207],a[36:39],v[100:103]
st v[230:233],v238,s[32:35],s94 offen offset:3072 sc1
mm v[100:103],v[200:203],a[60:63],v[100:103]
rt v[196:197],v242 offset:59136
rt v[198:199],v242 offset:60160
mm v[68:71],v[200:203],a[40:43],v[68:71]
wt lgkmcnt(4)
s_mov_b32 s93, 4096
ld a[72:75],v229,s[28:31],s93 offen offset:256
mm v[68:71],v[204:207],a[56:59],v[68:71]
rt v[200:201],v242 offset:0
rt v[202:203],v242 offset:1024
mm v[68:71],v[212:215],v[128:131],v[68:71]
ld v[168:171],v224,s[68:71],0 offen offset:1024
mm v[68:71],v[208:211],v[144:147],v[68:71]
rt v[204:205],v242 offset:8448
rt v[206:207],v242 offset:9472
mm v[100:103],v[208:211],v[148:151],v[100:103]
ld a[76:79],v229,s[28:31],s93 offen offset:2304
mm v[100:103],v[212:215],v[132:135],v[100:103]
rt v[208:209],v242 offset:16896
rt v[210:211],v242 offset:17920
mm v[100:103],v[220:223],v[164:167],v[100:103]
ld v[172:175],v225,s[48:51],0 offen offset:1024
mm v[100:103],v[216:219],v[180:183],v[100:103]
rt v[212:213],v242 offset:25344
rt v[214:215],v242 offset:26368
mm v[68:71],v[216:219],v[176:179],v[68:71]
wt lgkmcnt(4)
s_mov_b32 s93, 8192
ld a[80:83],v229,s[28:31],s93 offen offset:256
mm v[68:71],v[220:223],v[160:163],v[68:71]
rt v[216:217],v242 offset:33792
rt v[218:219],v242 offset:34816
mm v[72:75],v[192:195],a[32:35],v[72:75]
cv v234,v96,v97
cv v236,v100,v101
mm v[72:75],v[196:199],a[48:51],v[72:75]
rt v[220:221],v242 offset:42240
rt v[222:223],v242 offset:43264
mm v[104:107],v[196:199],a[52:55],v[104:107]
cv v235,v98,v99
cv v237,v102,v103
mm v[104:107],v[192:195],a[44:47],v[104:107]
rt v[196:197],v243 offset:50688
rt v[198:199],v243 offset:51712
mm v[104:107],v[200:203],a[36:39],v[104:107]
v_permlane16_swap_b32 v234, v236
v_permlane16_swap_b32 v235, v237
mm v[104:107],v[204:207],a[60:63],v[104:107]
rt v[192:193],v243 offset:59136
rt v[194:195],v243 offset:60160
mm v[72:75],v[204:207],a[40:43],v[72:75]
wt lgkmcnt(4)
ld a[84:87],v229,s[28:31],s93 offen offset:2304
mm v[72:75],v[200:203],a[56:59],v[72:75]
rt v[204:205],v243 offset:0
rt v[206:207],v243 offset:1024
mm v[72:75],v[208:211],v[128:131],v[72:75]
st v[234:237],v238,s[32:35],s97 offen offset:0 sc1
mm v[72:75],v[212:215],v[144:147],v[72:75]
rt v[200:201],v243 offset:8448
rt v[202:203],v243 offset:9472
mm v[104:107],v[212:215],v[148:151],v[104:107]
cv v230,v64,v65
cv v232,v68,v69
mm v[104:107],v[208:211],v[132:135],v[104:107]
)ASM" R"ASM(rt v[212:213],v243 offset:16896
rt v[214:215],v243 offset:17920
mm v[104:107],v[216:219],v[164:167],v[104:107]
cv v231,v66,v67
cv v233,v70,v71
mm v[104:107],v[220:223],v[180:183],v[104:107]
rt v[208:209],v243 offset:25344
rt v[210:211],v243 offset:26368
mm v[72:75],v[220:223],v[176:179],v[72:75]
wt lgkmcnt(4)
s_mov_b32 s93, 12288
ld a[88:91],v229,s[28:31],s93 offen offset:256
mm v[72:75],v[216:219],v[160:163],v[72:75]
rt v[220:221],v243 offset:33792
rt v[222:223],v243 offset:34816
mm v[76:79],v[196:199],a[32:35],v[76:79]
v_permlane16_swap_b32 v230, v232
v_permlane16_swap_b32 v231, v233
mm v[76:79],v[192:195],a[48:51],v[76:79]
rt v[216:217],v243 offset:42240
rt v[218:219],v243 offset:43264
mm v[108:111],v[192:195],a[52:55],v[108:111]
ld a[92:95],v229,s[28:31],s93 offen offset:2304
mm v[108:111],v[196:199],a[44:47],v[108:111]
rt v[192:193],v240 offset:50816
rt v[194:195],v240 offset:51840
mm v[108:111],v[204:207],a[36:39],v[108:111]
st v[230:233],v238,s[32:35],s96 offen offset:0 sc1
mm v[108:111],v[200:203],a[60:63],v[108:111]
rt v[196:197],v240 offset:59264
rt v[198:199],v240 offset:60288
mm v[76:79],v[200:203],a[40:43],v[76:79]
wt lgkmcnt(4)
v_and_b32_e64 v251, 64, v255
mm v[76:79],v[204:207],a[56:59],v[76:79]
rt v[200:201],v240 offset:128
rt v[202:203],v240 offset:1152
mm v[76:79],v[212:215],v[128:131],v[76:79]
ld v[184:187],v224,s[68:71],0 offen offset:1536
mm v[76:79],v[208:211],v[144:147],v[76:79]
rt v[204:205],v240 offset:8576
rt v[206:207],v240 offset:9600
mm v[108:111],v[208:211],v[148:151],v[108:111]
v_lshl_add_u32 v251, v251, 24, v250
v_and_b32 v248, 0x1f0, v255
mm v[108:111],v[212:215],v[132:135],v[108:111]
rt v[208:209],v240 offset:17024
rt v[210:211],v240 offset:18048
mm v[108:111],v[220:223],v[164:167],v[108:111]
ld v[188:191],v225,s[48:51],0 offen offset:1536
mm v[108:111],v[216:219],v[180:183],v[108:111]
rt v[212:213],v240 offset:25472
rt v[214:215],v240 offset:26496
mm v[76:79],v[216:219],v[176:179],v[76:79]
wt lgkmcnt(4)
v_lshrrev_b32_e64 v248, 1, v248
mm v[76:79],v[220:223],v[160:163],v[76:79]
rt v[216:217],v240 offset:33920
rt v[218:219],v240 offset:34944
mm v[80:83],v[192:195],a[32:35],v[80:83]
cv v234,v104,v105
cv v236,v108,v109
mm v[80:83],v[196:199],a[48:51],v[80:83]
rt v[220:221],v240 offset:42368
rt v[222:223],v240 offset:43392
mm v[112:115],v[196:199],a[52:55],v[112:115]
cv v235,v106,v107
cv v237,v110,v111
mm v[112:115],v[192:195],a[44:47],v[112:115]
rt v[196:197],v241 offset:50816
rt v[198:199],v241 offset:51840
mm v[112:115],v[200:203],a[36:39],v[112:115]
v_permlane16_swap_b32 v234, v236
v_permlane16_swap_b32 v235, v237
mm v[112:115],v[204:207],a[60:63],v[112:115]
rt v[192:193],v241 offset:59264
rt v[194:195],v241 offset:60288
mm v[80:83],v[204:207],a[40:43],v[80:83]
wt lgkmcnt(4)
mm v[80:83],v[200:203],a[56:59],v[80:83]
rt v[204:205],v241 offset:128
rt v[206:207],v241 offset:1152
mm v[80:83],v[208:211],v[128:131],v[80:83]
st v[234:237],v238,s[32:35],s97 offen offset:1024 sc1
mm v[80:83],v[212:215],v[144:147],v[80:83]
rt v[200:201],v241 offset:8576
rt v[202:203],v241 offset:9600
mm v[112:115],v[212:215],v[148:151],v[112:115]
cv v230,v72,v73
cv v232,v76,v77
mm v[112:115],v[208:211],v[132:135],v[112:115]
rt v[212:213],v241 offset:17024
rt v[214:215],v241 offset:18048
mm v[112:115],v[216:219],v[164:167],v[112:115]
cv v231,v74,v75
cv v233,v78,v79
mm v[112:115],v[220:223],v[180:183],v[112:115]
rt v[208:209],v241 offset:25472
rt v[210:211],v241 offset:26496
mm v[80:83],v[220:223],v[176:179],v[80:83]
)ASM" R"ASM(wt lgkmcnt(4)
mm v[80:83],v[216:219],v[160:163],v[80:83]
rt v[220:221],v241 offset:33920
rt v[222:223],v241 offset:34944
mm v[84:87],v[196:199],a[32:35],v[84:87]
v_permlane16_swap_b32 v230, v232
v_permlane16_swap_b32 v231, v233
mm v[84:87],v[192:195],a[48:51],v[84:87]
rt v[216:217],v241 offset:42368
rt v[218:219],v241 offset:43392
mm v[116:119],v[192:195],a[52:55],v[116:119]
mm v[116:119],v[196:199],a[44:47],v[116:119]
rt v[192:193],v242 offset:50816
rt v[194:195],v242 offset:51840
mm v[116:119],v[204:207],a[36:39],v[116:119]
st v[230:233],v238,s[32:35],s96 offen offset:1024 sc1
mm v[116:119],v[200:203],a[60:63],v[116:119]
rt v[196:197],v242 offset:59264
rt v[198:199],v242 offset:60288
mm v[84:87],v[200:203],a[40:43],v[84:87]
wt lgkmcnt(4)
mm v[84:87],v[204:207],a[56:59],v[84:87]
rt v[200:201],v242 offset:128
rt v[202:203],v242 offset:1152
mm v[84:87],v[212:215],v[128:131],v[84:87]
mm v[84:87],v[208:211],v[144:147],v[84:87]
rt v[204:205],v242 offset:8576
rt v[206:207],v242 offset:9600
mm v[116:119],v[208:211],v[148:151],v[116:119]
mm v[116:119],v[212:215],v[132:135],v[116:119]
rt v[208:209],v242 offset:17024
rt v[210:211],v242 offset:18048
mm v[116:119],v[220:223],v[164:167],v[116:119]
mm v[116:119],v[216:219],v[180:183],v[116:119]
rt v[212:213],v242 offset:25472
rt v[214:215],v242 offset:26496
mm v[84:87],v[216:219],v[176:179],v[84:87]
wt lgkmcnt(4)
mm v[84:87],v[220:223],v[160:163],v[84:87]
rt v[216:217],v242 offset:33920
rt v[218:219],v242 offset:34944
mm v[88:91],v[192:195],a[32:35],v[88:91]
cv v234,v112,v113
cv v236,v116,v117
mm v[88:91],v[196:199],a[48:51],v[88:91]
rt v[220:221],v242 offset:42368
rt v[222:223],v242 offset:43392
mm v[120:123],v[196:199],a[52:55],v[120:123]
cv v235,v114,v115
cv v237,v118,v119
mm v[120:123],v[192:195],a[44:47],v[120:123]
rt v[196:197],v243 offset:50816
rt v[198:199],v243 offset:51840
mm v[120:123],v[200:203],a[36:39],v[120:123]
v_permlane16_swap_b32 v234, v236
v_permlane16_swap_b32 v235, v237
mm v[120:123],v[204:207],a[60:63],v[120:123]
rt v[192:193],v243 offset:59264
rt v[194:195],v243 offset:60288
mm v[88:91],v[204:207],a[40:43],v[88:91]
wt lgkmcnt(4)
mm v[88:91],v[200:203],a[56:59],v[88:91]
rt v[204:205],v243 offset:128
rt v[206:207],v243 offset:1152
mm v[88:91],v[208:211],v[128:131],v[88:91]
st v[234:237],v238,s[32:35],s97 offen offset:2048 sc1
mm v[88:91],v[212:215],v[144:147],v[88:91]
rt v[200:201],v243 offset:8576
rt v[202:203],v243 offset:9600
mm v[120:123],v[212:215],v[148:151],v[120:123]
cv v230,v80,v81
cv v232,v84,v85
mm v[120:123],v[208:211],v[132:135],v[120:123]
rt v[212:213],v243 offset:17024
rt v[214:215],v243 offset:18048
mm v[120:123],v[216:219],v[164:167],v[120:123]
cv v231,v82,v83
cv v233,v86,v87
wt vmcnt(15)
buffer_store_dwordx2 v[252:253], v251, s[64:67], s92 offen sc1
mm v[120:123],v[220:223],v[180:183],v[120:123]
rt v[208:209],v243 offset:25472
rt v[210:211],v243 offset:26496
mm v[88:91],v[220:223],v[176:179],v[88:91]
wt lgkmcnt(4)
mm v[88:91],v[216:219],v[160:163],v[88:91]
rt v[220:221],v243 offset:33920
rt v[222:223],v243 offset:34944
mm v[92:95],v[196:199],a[32:35],v[92:95]
v_permlane16_swap_b32 v230, v232
v_permlane16_swap_b32 v231, v233
mm v[92:95],v[192:195],a[48:51],v[92:95]
rt v[216:217],v243 offset:42368
rt v[218:219],v243 offset:43392
mm v[124:127],v[192:195],a[52:55],v[124:127]
mm v[124:127],v[196:199],a[44:47],v[124:127]
mm v[124:127],v[204:207],a[36:39],v[124:127]
st v[230:233],v238,s[32:35],s96 offen offset:2048 sc1
mm v[124:127],v[200:203],a[60:63],v[124:127]
mm v[92:95],v[200:203],a[40:43],v[92:95]
)ASM" R"ASM(wt lgkmcnt(0)
mm v[92:95],v[204:207],a[56:59],v[92:95]
mm v[92:95],v[212:215],v[128:131],v[92:95]
mm v[92:95],v[208:211],v[144:147],v[92:95]
mm v[124:127],v[208:211],v[148:151],v[124:127]
mm v[124:127],v[212:215],v[132:135],v[124:127]
mm v[124:127],v[220:223],v[164:167],v[124:127]
mm v[124:127],v[216:219],v[180:183],v[124:127]
mm v[92:95],v[216:219],v[176:179],v[92:95]
mm v[92:95],v[220:223],v[160:163],v[92:95]
s_nop 7
cv v234,v120,v121
cv v236,v124,v125
cv v235,v122,v123
cv v237,v126,v127
cv v230,v88,v89
cv v232,v92,v93
cv v231,v90,v91
cv v233,v94,v95
v_permlane16_swap_b32 v234, v236
v_permlane16_swap_b32 v235, v237
st v[234:237],v238,s[32:35],s97 offen offset:3072 sc1
v_permlane16_swap_b32 v230, v232
v_permlane16_swap_b32 v231, v233
st v[230:233],v238,s[32:35],s96 offen offset:3072 sc1
s_branch .La5_LT_0
; ---- O projection, odd XCDs: seven and a half heads
.La5_LO_SC:
.La5_LW7_0:
s_cbranch_execz .La5_LW7_4
mm v[64:67],v[192:195],v[160:163],0
buffer_load_dwordx2 v[250:251], v248, s[64:67], s5 offen sc1
mm v[64:67],v[196:199],v[164:167],v[64:67]
rt v[220:221],v243 offset:25344
rt v[222:223],v243 offset:26368
mm v[96:99],v[196:199],v[180:183],0
s_add_u32 m0, s83, 0
s_bfe_u32 s80, s4, 262148
mm v[96:99],v[192:195],v[176:179],v[96:99]
rt v[196:197],v240 offset:17024
rt v[198:199],v240 offset:18048
s_xor_b32 s80, s80, s72
s_lshl_b32 s80, s80, 18
s_add_u32 s80, s80, 131072
mm v[100:103],v[200:203],v[176:179],0
ld v224,s[56:59],s80 offen lds
mm v[100:103],v[204:207],v[180:183],v[100:103]
rt v[192:193],v240 offset:25472
rt v[194:195],v240 offset:26496
mm v[68:71],v[204:207],v[164:167],0
s_add_u32 m0, s83, 1024
wt lgkmcnt(4)
mm v[68:71],v[200:203],v[160:163],v[68:71]
rt v[204:205],v241 offset:17024
rt v[206:207],v241 offset:18048
mm v[72:75],v[208:211],v[160:163],0
ld v225,s[56:59],s80 offen lds
mm v[72:75],v[212:215],v[164:167],v[72:75]
rt v[200:201],v241 offset:25472
rt v[202:203],v241 offset:26496
mm v[104:107],v[212:215],v[180:183],0
s_add_u32 m0, s83, 2176
s_bfe_u32 s53, s4, 262148
mm v[104:107],v[208:211],v[176:179],v[104:107]
rt v[212:213],v242 offset:17024
rt v[214:215],v242 offset:18048
mm v[108:111],v[216:219],v[176:179],0
ld v226,s[56:59],s80 offen lds
mm v[108:111],v[220:223],v[180:183],v[108:111]
rt v[208:209],v242 offset:25472
rt v[210:211],v242 offset:26496
mm v[76:79],v[220:223],v[164:167],0
s_add_u32 m0, s83, 3200
wt lgkmcnt(4)
mm v[76:79],v[216:219],v[160:163],v[76:79]
rt v[220:221],v243 offset:17024
rt v[222:223],v243 offset:18048
mm v[80:83],v[196:199],v[160:163],0
ld v227,s[56:59],s80 offen lds
mm v[80:83],v[192:195],v[164:167],v[80:83]
rt v[216:217],v243 offset:25472
rt v[218:219],v243 offset:26496
mm v[112:115],v[192:195],v[180:183],0
s_add_u32 m0, s83, 118272
s_bfe_u32 s80, s4, 262152
mm v[112:115],v[196:199],v[176:179],v[112:115]
rt v[192:193],v240 offset:33792
rt v[194:195],v240 offset:34816
s_xor_b32 s80, s80, s72
s_lshl_b32 s80, s80, 18
mm v[116:119],v[204:207],v[176:179],0
ld v224,s[56:59],s80 offen lds
mm v[116:119],v[200:203],v[180:183],v[116:119]
rt v[196:197],v240 offset:42240
rt v[198:199],v240 offset:43264
mm v[84:87],v[200:203],v[164:167],0
s_add_u32 m0, s83, 119296
wt lgkmcnt(4)
mm v[84:87],v[204:207],v[160:163],v[84:87]
rt v[200:201],v241 offset:33792
)ASM" R"ASM(rt v[202:203],v241 offset:34816
mm v[88:91],v[212:215],v[160:163],0
ld v225,s[56:59],s80 offen lds
mm v[88:91],v[208:211],v[164:167],v[88:91]
rt v[204:205],v241 offset:42240
rt v[206:207],v241 offset:43264
mm v[120:123],v[208:211],v[180:183],0
s_add_u32 m0, s83, 120448
s_xor_b32 s53, s53, s72
mm v[120:123],v[212:215],v[176:179],v[120:123]
rt v[208:209],v242 offset:33792
rt v[210:211],v242 offset:34816
mm v[124:127],v[220:223],v[176:179],0
ld v226,s[56:59],s80 offen lds
mm v[124:127],v[216:219],v[180:183],v[124:127]
rt v[212:213],v242 offset:42240
rt v[214:215],v242 offset:43264
mm v[92:95],v[216:219],v[164:167],0
wt vmcnt(7)
v_cmp_eq_u64 vcc, v[250:251], v[252:253]
s_nop 0
s_bitcmp1_b32 vcc_lo, 1
s_cbranch_scc0 .La5_LO_NC1
.La5_LO_KC1:
wt lgkmcnt(4)
mm v[92:95],v[220:223],v[160:163],v[92:95]
rt v[216:217],v243 offset:33792
rt v[218:219],v243 offset:34816
s_lshl_b32 s53, s53, 13
mm v[64:67],v[192:195],v[168:171],v[64:67]
ld a[0:3],v244,s[44:47],s53 offen offset:0 sc1
mm v[64:67],v[196:199],v[172:175],v[64:67]
rt v[220:221],v243 offset:42240
rt v[222:223],v243 offset:43264
mm v[96:99],v[196:199],v[188:191],v[96:99]
ld a[4:7],v245,s[44:47],s53 offen offset:0 sc1
mm v[96:99],v[192:195],v[184:187],v[96:99]
rt v[196:197],v240 offset:33920
rt v[198:199],v240 offset:34944
mm v[100:103],v[200:203],v[184:187],v[100:103]
ld a[8:11],v246,s[44:47],s53 offen offset:0 sc1
mm v[100:103],v[204:207],v[188:191],v[100:103]
rt v[192:193],v240 offset:42368
.La5_LW7_1:
s_cbranch_execz .La5_LW7_5
rt v[194:195],v240 offset:43392
mm v[68:71],v[204:207],v[172:175],v[68:71]
wt lgkmcnt(4)
s_bfe_u32 s54, s4, 262152
mm v[68:71],v[200:203],v[168:171],v[68:71]
rt v[204:205],v241 offset:33920
rt v[206:207],v241 offset:34944
mm v[72:75],v[208:211],v[168:171],v[72:75]
ld a[12:15],v247,s[44:47],s53 offen offset:0 sc1
mm v[72:75],v[212:215],v[172:175],v[72:75]
rt v[200:201],v241 offset:42368
rt v[202:203],v241 offset:43392
mm v[104:107],v[212:215],v[188:191],v[104:107]
ld a[16:19],v244,s[44:47],s53 offen offset:1024 sc1
mm v[104:107],v[208:211],v[184:187],v[104:107]
rt v[212:213],v242 offset:33920
rt v[214:215],v242 offset:34944
mm v[108:111],v[216:219],v[184:187],v[108:111]
ld a[20:23],v245,s[44:47],s53 offen offset:1024 sc1
mm v[108:111],v[220:223],v[188:191],v[108:111]
rt v[208:209],v242 offset:42368
rt v[210:211],v242 offset:43392
mm v[76:79],v[220:223],v[172:175],v[76:79]
s_nop 0
s_barrier
wt lgkmcnt(4)
mm v[76:79],v[216:219],v[168:171],v[76:79]
rt v[220:221],v243 offset:33920
rt v[222:223],v243 offset:34944
mm v[80:83],v[196:199],v[168:171],v[80:83]
ld a[24:27],v246,s[44:47],s53 offen offset:1024 sc1
mm v[80:83],v[192:195],v[172:175],v[80:83]
rt v[216:217],v243 offset:42368
rt v[218:219],v243 offset:43392
mm v[112:115],v[192:195],v[188:191],v[112:115]
ld a[28:31],v247,s[44:47],s53 offen offset:1024 sc1
mm v[112:115],v[196:199],v[184:187],v[112:115]
rt v[192:193],v240 offset:50688
rt v[194:195],v240 offset:51712
mm v[116:119],v[204:207],v[184:187],v[116:119]
ld a[32:35],v244,s[44:47],s53 offen offset:2048 sc1
mm v[116:119],v[200:203],v[188:191],v[116:119]
rt v[196:197],v240 offset:59136
rt v[198:199],v240 offset:60160
mm v[84:87],v[200:203],v[172:175],v[84:87]
wt lgkmcnt(4)
s_xor_b32 s54, s54, s72
mm v[84:87],v[204:207],v[168:171],v[84:87]
rt v[200:201],v241 offset:50688
rt v[202:203],v241 offset:51712
mm v[88:91],v[212:215],v[168:171],v[88:91]
ld a[36:39],v245,s[44:47],s53 offen offset:2048 sc1
mm v[88:91],v[208:211],v[172:175],v[88:91]
rt v[204:205],v241 offset:59136
rt v[206:207],v241 offset:60160
)ASM" R"ASM(mm v[120:123],v[208:211],v[188:191],v[120:123]
ld a[40:43],v246,s[44:47],s53 offen offset:2048 sc1
mm v[120:123],v[212:215],v[184:187],v[120:123]
rt v[208:209],v242 offset:50688
rt v[210:211],v242 offset:51712
mm v[124:127],v[220:223],v[184:187],v[124:127]
ld a[44:47],v247,s[44:47],s53 offen offset:2048 sc1
mm v[124:127],v[216:219],v[188:191],v[124:127]
rt v[212:213],v242 offset:59136
rt v[214:215],v242 offset:60160
mm v[92:95],v[216:219],v[172:175],v[92:95]
wt vmcnt(4) lgkmcnt(4)
s_lshl_b32 s54, s54, 13
mm v[92:95],v[220:223],v[168:171],v[92:95]
rt v[216:217],v243 offset:50688
rt v[218:219],v243 offset:51712
mm v[0:3],v[192:195],a[0:3],v[0:3]
s_bfe_u32 s55, s4, 262156
s_xor_b32 s55, s55, s72
mm v[0:3],v[196:199],a[16:19],v[0:3]
ld a[48:51],v244,s[44:47],s53 offen offset:3072 sc1
mm v[32:35],v[196:199],a[20:23],v[32:35]
s_lshl_b32 s55, s55, 13
s_bfe_u32 s52, s4, 262160
mm v[32:35],v[192:195],a[4:7],v[32:35]
rt v[220:221],v243 offset:59136
rt v[222:223],v243 offset:60160
mm v[64:67],v[192:195],a[8:11],v[64:67]
s_xor_b32 s52, s52, s72
s_lshl_b32 s52, s52, 13
mm v[64:67],v[196:199],a[24:27],v[64:67]
ld a[52:55],v245,s[44:47],s53 offen offset:3072 sc1
mm v[96:99],v[196:199],a[28:31],v[96:99]
mm v[96:99],v[192:195],a[12:15],v[96:99]
rt v[196:197],v240 offset:50816
rt v[198:199],v240 offset:51840
mm v[100:103],v[200:203],a[12:15],v[100:103]
mm v[100:103],v[204:207],a[28:31],v[100:103]
buffer_load_dwordx2 v[250:251], v248, s[64:67], s5 offen sc1
mm v[68:71],v[204:207],a[24:27],v[68:71]
mm v[68:71],v[200:203],a[8:11],v[68:71]
rt v[192:193],v240 offset:59264
rt v[194:195],v240 offset:60288
mm v[36:39],v[200:203],a[4:7],v[36:39]
mm v[36:39],v[204:207],a[20:23],v[36:39]
ld a[56:59],v246,s[44:47],s53 offen offset:3072 sc1
mm v[4:7],v[204:207],a[16:19],v[4:7]
wt lgkmcnt(4)
mm v[4:7],v[200:203],a[0:3],v[4:7]
rt v[204:205],v241 offset:50816
rt v[206:207],v241 offset:51840
mm v[8:11],v[208:211],a[0:3],v[8:11]
mm v[8:11],v[212:215],a[16:19],v[8:11]
ld a[60:63],v247,s[44:47],s53 offen offset:3072 sc1
mm v[40:43],v[212:215],a[20:23],v[40:43]
s_bfe_u32 s53, s4, 262164
s_xor_b32 s53, s53, s72
mm v[40:43],v[208:211],a[4:7],v[40:43]
rt v[200:201],v241 offset:59264
rt v[202:203],v241 offset:60288
mm v[72:75],v[208:211],a[8:11],v[72:75]
s_add_u32 m0, s83, 121472
s_lshl_b32 s53, s53, 13
mm v[72:75],v[212:215],a[24:27],v[72:75]
ld v227,s[56:59],s80 offen lds
mm v[104:107],v[212:215],a[28:31],v[104:107]
s_add_u32 s80, s80, 131072
mm v[104:107],v[208:211],a[12:15],v[104:107]
rt v[212:213],v242 offset:50816
rt v[214:215],v242 offset:51840
mm v[108:111],v[216:219],a[12:15],v[108:111]
s_add_u32 m0, s83, 67584
mm v[108:111],v[220:223],a[28:31],v[108:111]
ld v224,s[56:59],s80 offen lds
mm v[76:79],v[220:223],a[24:27],v[76:79]
mm v[76:79],v[216:219],a[8:11],v[76:79]
rt v[208:209],v242 offset:59264
rt v[210:211],v242 offset:60288
mm v[44:47],v[216:219],a[4:7],v[44:47]
s_add_u32 m0, s83, 68608
mm v[44:47],v[220:223],a[20:23],v[44:47]
ld v225,s[56:59],s80 offen lds
mm v[12:15],v[220:223],a[16:19],v[12:15]
wt lgkmcnt(4)
mm v[12:15],v[216:219],a[0:3],v[12:15]
rt v[220:221],v243 offset:50816
rt v[222:223],v243 offset:51840
mm v[16:19],v[196:199],a[0:3],v[16:19]
s_add_u32 m0, s83, 69760
mm v[16:19],v[192:195],a[16:19],v[16:19]
ld v226,s[56:59],s80 offen lds
mm v[48:51],v[192:195],a[20:23],v[48:51]
mm v[48:51],v[196:199],a[4:7],v[48:51]
rt v[216:217],v243 offset:59264
rt v[218:219],v243 offset:60288
mm v[80:83],v[196:199],a[8:11],v[80:83]
s_add_u32 m0, s83, 70784
s_nop 0
s_barrier
mm v[80:83],v[192:195],a[24:27],v[80:83]
)ASM" R"ASM(ld v227,s[56:59],s80 offen lds
mm v[112:115],v[192:195],a[28:31],v[112:115]
s_bfe_u32 s80, s4, 262156
s_xor_b32 s80, s80, s72
mm v[112:115],v[196:199],a[12:15],v[112:115]
rt v[192:193],v240 offset:0
rt v[194:195],v240 offset:1024
mm v[116:119],v[204:207],a[12:15],v[116:119]
s_add_u32 m0, s83, 84480
s_lshl_b32 s80, s80, 18
mm v[116:119],v[200:203],a[28:31],v[116:119]
ld v224,s[56:59],s80 offen lds
mm v[84:87],v[200:203],a[24:27],v[84:87]
mm v[84:87],v[204:207],a[8:11],v[84:87]
rt v[196:197],v240 offset:8448
rt v[198:199],v240 offset:9472
mm v[52:55],v[204:207],a[4:7],v[52:55]
s_add_u32 m0, s83, 85504
mm v[52:55],v[200:203],a[20:23],v[52:55]
ld v225,s[56:59],s80 offen lds
mm v[20:23],v[200:203],a[16:19],v[20:23]
wt lgkmcnt(4)
mm v[20:23],v[204:207],a[0:3],v[20:23]
rt v[200:201],v241 offset:0
rt v[202:203],v241 offset:1024
mm v[24:27],v[212:215],a[0:3],v[24:27]
s_add_u32 m0, s83, 86656
mm v[24:27],v[208:211],a[16:19],v[24:27]
ld v226,s[56:59],s80 offen lds
mm v[56:59],v[208:211],a[20:23],v[56:59]
wt vmcnt(10)
v_cmp_eq_u64 vcc, v[250:251], v[252:253]
mm v[56:59],v[212:215],a[4:7],v[56:59]
rt v[204:205],v241 offset:8448
rt v[206:207],v241 offset:9472
mm v[88:91],v[212:215],a[8:11],v[88:91]
s_add_u32 m0, s83, 87680
mm v[88:91],v[208:211],a[24:27],v[88:91]
ld v227,s[56:59],s80 offen lds
mm v[120:123],v[208:211],a[28:31],v[120:123]
s_bitcmp1_b32 vcc_lo, 2
s_cbranch_scc0 .La5_LO_NC2
.La5_LO_KC2:
mm v[120:123],v[212:215],a[12:15],v[120:123]
rt v[208:209],v242 offset:0
rt v[210:211],v242 offset:1024
mm v[124:127],v[220:223],a[12:15],v[124:127]
s_add_u32 s80, s80, 131072
mm v[124:127],v[216:219],a[28:31],v[124:127]
ld a[12:15],v244,s[44:47],s54 offen offset:0 sc1
mm v[92:95],v[216:219],a[24:27],v[92:95]
mm v[92:95],v[220:223],a[8:11],v[92:95]
rt v[212:213],v242 offset:8448
rt v[214:215],v242 offset:9472
mm v[60:63],v[220:223],a[4:7],v[60:63]
mm v[60:63],v[216:219],a[20:23],v[60:63]
mm v[28:31],v[216:219],a[16:19],v[28:31]
wt vmcnt(10) lgkmcnt(4)
mm v[28:31],v[220:223],a[0:3],v[28:31]
rt v[216:217],v243 offset:0
rt v[218:219],v243 offset:1024
mm v[0:3],v[192:195],a[32:35],v[0:3]
mm v[0:3],v[196:199],a[48:51],v[0:3]
ld a[0:3],v245,s[44:47],s54 offen offset:0 sc1
mm v[32:35],v[196:199],a[52:55],v[32:35]
mm v[32:35],v[192:195],a[36:39],v[32:35]
rt v[220:221],v243 offset:8448
rt v[222:223],v243 offset:9472
mm v[64:67],v[192:195],a[40:43],v[64:67]
mm v[64:67],v[196:199],a[56:59],v[64:67]
ld a[4:7],v246,s[44:47],s54 offen offset:0 sc1
mm v[96:99],v[196:199],a[60:63],v[96:99]
mm v[96:99],v[192:195],a[44:47],v[96:99]
rt v[196:197],v240 offset:128
rt v[198:199],v240 offset:1152
mm v[100:103],v[200:203],a[44:47],v[100:103]
s_mov_b32 s86, 0x05040100
s_mov_b32 s87, 0x07060302
mm v[100:103],v[204:207],a[60:63],v[100:103]
ld a[8:11],v247,s[44:47],s54 offen offset:0 sc1
mm v[68:71],v[204:207],a[56:59],v[68:71]
s_nop 0
ar v230,a128
mm v[68:71],v[200:203],a[40:43],v[68:71]
rt v[192:193],v240 offset:8576
rt v[194:195],v240 offset:9600
mm v[36:39],v[200:203],a[36:39],v[36:39]
v_add_u32 v240, 67584, v240
ar v231,a132
mm v[36:39],v[204:207],a[52:55],v[36:39]
ld a[16:19],v244,s[44:47],s54 offen offset:1024 sc1
mm v[4:7],v[204:207],a[48:51],v[4:7]
wt lgkmcnt(4)
v_perm_b32 v232, v231, v230, s86
mm v[4:7],v[200:203],a[32:35],v[4:7]
rt v[204:205],v241 offset:128
rt v[206:207],v241 offset:1152
mm v[8:11],v[208:211],a[32:35],v[8:11]
v_perm_b32 v233, v231, v230, s87
aw a96,v232
)ASM" R"ASM(mm v[8:11],v[212:215],a[48:51],v[8:11]
ld a[20:23],v245,s[44:47],s54 offen offset:1024 sc1
mm v[40:43],v[212:215],a[52:55],v[40:43]
aw a100,v233
ar v234,a136
mm v[40:43],v[208:211],a[36:39],v[40:43]
rt v[200:201],v241 offset:8576
rt v[202:203],v241 offset:9600
mm v[72:75],v[208:211],a[40:43],v[72:75]
v_add_u32 v241, 67584, v241
ar v235,a140
mm v[72:75],v[212:215],a[56:59],v[72:75]
ld a[24:27],v246,s[44:47],s54 offen offset:1024 sc1
mm v[104:107],v[212:215],a[60:63],v[104:107]
v_perm_b32 v236, v235, v234, s86
v_perm_b32 v237, v235, v234, s87
mm v[104:107],v[208:211],a[44:47],v[104:107]
rt v[212:213],v242 offset:128
rt v[214:215],v242 offset:1152
mm v[108:111],v[216:219],a[44:47],v[108:111]
aw a97,v236
aw a101,v237
mm v[108:111],v[220:223],a[60:63],v[108:111]
ld a[28:31],v247,s[44:47],s54 offen offset:1024 sc1
mm v[76:79],v[220:223],a[56:59],v[76:79]
ar v230,a144
ar v231,a148
mm v[76:79],v[216:219],a[40:43],v[76:79]
rt v[208:209],v242 offset:8576
rt v[210:211],v242 offset:9600
mm v[44:47],v[216:219],a[36:39],v[44:47]
v_add_u32 v242, 67584, v242
v_perm_b32 v232, v231, v230, s86
mm v[44:47],v[220:223],a[52:55],v[44:47]
v_perm_b32 v233, v231, v230, s87
aw a98,v232
mm v[12:15],v[220:223],a[48:51],v[12:15]
wt lgkmcnt(4)
aw a102,v233
mm v[12:15],v[216:219],a[32:35],v[12:15]
rt v[220:221],v243 offset:128
rt v[222:223],v243 offset:1152
mm v[16:19],v[196:199],a[32:35],v[16:19]
s_add_u32 m0, s83, 101376
ar v234,a152
mm v[16:19],v[192:195],a[48:51],v[16:19]
ld v224,s[56:59],s80 offen lds
mm v[48:51],v[192:195],a[52:55],v[48:51]
ar v235,a156
v_perm_b32 v236, v235, v234, s86
mm v[48:51],v[196:199],a[36:39],v[48:51]
rt v[216:217],v243 offset:8576
rt v[218:219],v243 offset:9600
mm v[80:83],v[196:199],a[40:43],v[80:83]
s_add_u32 m0, s83, 102400
v_add_u32 v243, 67584, v243
mm v[80:83],v[192:195],a[56:59],v[80:83]
ld v225,s[56:59],s80 offen lds
mm v[112:115],v[192:195],a[60:63],v[112:115]
wt vmcnt(14)
s_barrier
mm v[112:115],v[196:199],a[44:47],v[112:115]
rt v[192:193],v240 offset:50688
rt v[194:195],v240 offset:51712
mm v[116:119],v[204:207],a[44:47],v[116:119]
s_add_u32 m0, s83, 103552
v_perm_b32 v237, v235, v234, s87
mm v[116:119],v[200:203],a[60:63],v[116:119]
ld v226,s[56:59],s80 offen lds
mm v[84:87],v[200:203],a[56:59],v[84:87]
aw a99,v236
aw a103,v237
mm v[84:87],v[204:207],a[40:43],v[84:87]
rt v[196:197],v240 offset:59136
rt v[198:199],v240 offset:60160
mm v[52:55],v[204:207],a[36:39],v[52:55]
ar v230,a129
ar v231,a133
mm v[52:55],v[200:203],a[52:55],v[52:55]
ld a[64:67],v244,s[44:47],s54 offen offset:2048 sc1
mm v[20:23],v[200:203],a[48:51],v[20:23]
wt lgkmcnt(4)
v_perm_b32 v232, v231, v230, s86
mm v[20:23],v[204:207],a[32:35],v[20:23]
rt v[200:201],v241 offset:50688
rt v[202:203],v241 offset:51712
mm v[24:27],v[212:215],a[32:35],v[24:27]
v_perm_b32 v233, v231, v230, s87
aw a104,v232
mm v[24:27],v[208:211],a[48:51],v[24:27]
ld a[68:71],v245,s[44:47],s54 offen offset:2048 sc1
mm v[56:59],v[208:211],a[52:55],v[56:59]
aw a108,v233
ar v234,a137
mm v[56:59],v[212:215],a[36:39],v[56:59]
rt v[204:205],v241 offset:59136
rt v[206:207],v241 offset:60160
mm v[88:91],v[212:215],a[40:43],v[88:91]
ar v235,a141
v_perm_b32 v236, v235, v234, s86
)ASM" R"ASM(mm v[88:91],v[208:211],a[56:59],v[88:91]
ld a[72:75],v246,s[44:47],s54 offen offset:2048 sc1
mm v[120:123],v[208:211],a[60:63],v[120:123]
v_perm_b32 v237, v235, v234, s87
aw a105,v236
mm v[120:123],v[212:215],a[44:47],v[120:123]
rt v[208:209],v242 offset:50688
rt v[210:211],v242 offset:51712
mm v[124:127],v[220:223],a[44:47],v[124:127]
aw a109,v237
ar v230,a145
mm v[124:127],v[216:219],a[60:63],v[124:127]
buffer_load_dwordx2 v[250:251], v248, s[64:67], s5 offen sc1
mm v[92:95],v[216:219],a[56:59],v[92:95]
ar v231,a149
v_perm_b32 v232, v231, v230, s86
mm v[92:95],v[220:223],a[40:43],v[92:95]
rt v[212:213],v242 offset:59136
rt v[214:215],v242 offset:60160
mm v[60:63],v[220:223],a[36:39],v[60:63]
v_perm_b32 v233, v231, v230, s87
aw a106,v232
mm v[60:63],v[216:219],a[52:55],v[60:63]
ld a[36:39],v247,s[44:47],s54 offen offset:2048 sc1
mm v[28:31],v[216:219],a[48:51],v[28:31]
wt vmcnt(8) lgkmcnt(4)
aw a110,v233
mm v[28:31],v[220:223],a[32:35],v[28:31]
rt v[216:217],v243 offset:50688
rt v[218:219],v243 offset:51712
mm v[0:3],v[192:195],a[12:15],v[0:3]
ar v234,a153
ar v235,a157
mm v[0:3],v[196:199],a[16:19],v[0:3]
ld a[32:35],v244,s[44:47],s54 offen offset:3072 sc1
mm v[32:35],v[196:199],a[20:23],v[32:35]
v_perm_b32 v236, v235, v234, s86
v_perm_b32 v237, v235, v234, s87
mm v[32:35],v[192:195],a[0:3],v[32:35]
rt v[220:221],v243 offset:59136
rt v[222:223],v243 offset:60160
mm v[64:67],v[192:195],a[4:7],v[64:67]
aw a107,v236
aw a111,v237
mm v[64:67],v[196:199],a[24:27],v[64:67]
ld a[40:43],v245,s[44:47],s54 offen offset:3072 sc1
mm v[96:99],v[196:199],a[28:31],v[96:99]
ar v230,a130
ar v231,a134
mm v[96:99],v[192:195],a[8:11],v[96:99]
rt v[196:197],v240 offset:50816
rt v[198:199],v240 offset:51840
mm v[100:103],v[200:203],a[8:11],v[100:103]
v_perm_b32 v232, v231, v230, s86
v_perm_b32 v233, v231, v230, s87
mm v[100:103],v[204:207],a[28:31],v[100:103]
ld a[44:47],v246,s[44:47],s54 offen offset:3072 sc1
mm v[68:71],v[204:207],a[24:27],v[68:71]
aw a112,v232
aw a116,v233
mm v[68:71],v[200:203],a[4:7],v[68:71]
rt v[192:193],v240 offset:59264
rt v[194:195],v240 offset:60288
mm v[36:39],v[200:203],a[0:3],v[36:39]
ar v234,a138
ar v235,a142
mm v[36:39],v[204:207],a[20:23],v[36:39]
ld a[48:51],v247,s[44:47],s54 offen offset:3072 sc1
mm v[4:7],v[204:207],a[16:19],v[4:7]
wt lgkmcnt(4)
s_bfe_u32 s54, s4, 262168
mm v[4:7],v[200:203],a[12:15],v[4:7]
rt v[204:205],v241 offset:50816
rt v[206:207],v241 offset:51840
mm v[8:11],v[208:211],a[12:15],v[8:11]
s_add_u32 m0, s83, 104576
s_xor_b32 s54, s54, s72
mm v[8:11],v[212:215],a[16:19],v[8:11]
ld v227,s[56:59],s80 offen lds
mm v[40:43],v[212:215],a[20:23],v[40:43]
s_bitcmp1_b32 vcc_lo, 3
s_cbranch_scc0 .La5_LO_NC3
.La5_LO_KC3:
mm v[40:43],v[208:211],a[0:3],v[40:43]
rt v[200:201],v241 offset:59264
rt v[202:203],v241 offset:60288
mm v[72:75],v[208:211],a[4:7],v[72:75]
s_bfe_u32 s80, s4, 262160
s_xor_b32 s80, s80, s72
mm v[72:75],v[212:215],a[24:27],v[72:75]
ld a[52:55],v244,s[44:47],s55 offen offset:0 sc1
mm v[104:107],v[212:215],a[28:31],v[104:107]
s_lshl_b32 s80, s80, 18
s_lshl_b32 s54, s54, 13
mm v[104:107],v[208:211],a[8:11],v[104:107]
rt v[212:213],v242 offset:50816
rt v[214:215],v242 offset:51840
mm v[108:111],v[216:219],a[8:11],v[108:111]
v_perm_b32 v236, v235, v234, s86
v_perm_b32 v237, v235, v234, s87
)ASM" R"ASM(mm v[108:111],v[220:223],a[28:31],v[108:111]
ld a[56:59],v245,s[44:47],s55 offen offset:0 sc1
mm v[76:79],v[220:223],a[24:27],v[76:79]
aw a113,v236
aw a117,v237
mm v[76:79],v[216:219],a[4:7],v[76:79]
rt v[208:209],v242 offset:59264
rt v[210:211],v242 offset:60288
mm v[44:47],v[216:219],a[0:3],v[44:47]
ar v230,a146
ar v231,a150
mm v[44:47],v[220:223],a[20:23],v[44:47]
v_perm_b32 v232, v231, v230, s86
v_perm_b32 v233, v231, v230, s87
mm v[12:15],v[220:223],a[16:19],v[12:15]
wt lgkmcnt(4)
aw a114,v232
mm v[12:15],v[216:219],a[12:15],v[12:15]
rt v[220:221],v243 offset:50816
rt v[222:223],v243 offset:51840
mm v[16:19],v[196:199],a[12:15],v[16:19]
aw a118,v233
ar v234,a154
mm v[16:19],v[192:195],a[16:19],v[16:19]
ld a[60:63],v246,s[44:47],s55 offen offset:0 sc1
mm v[48:51],v[192:195],a[20:23],v[48:51]
ar v235,a158
v_perm_b32 v236, v235, v234, s86
mm v[48:51],v[196:199],a[0:3],v[48:51]
rt v[216:217],v243 offset:59264
rt v[218:219],v243 offset:60288
mm v[80:83],v[196:199],a[4:7],v[80:83]
v_perm_b32 v237, v235, v234, s87
aw a115,v236
mm v[80:83],v[192:195],a[24:27],v[80:83]
ld a[76:79],v247,s[44:47],s55 offen offset:0 sc1
mm v[112:115],v[192:195],a[28:31],v[112:115]
aw a119,v237
ar v230,a131
mm v[112:115],v[196:199],a[8:11],v[112:115]
rt v[192:193],v240 offset:0
rt v[194:195],v240 offset:1024
mm v[116:119],v[204:207],a[8:11],v[116:119]
ar v231,a135
v_perm_b32 v232, v231, v230, s86
mm v[116:119],v[200:203],a[28:31],v[116:119]
ld a[80:83],v244,s[44:47],s55 offen offset:1024 sc1
mm v[84:87],v[200:203],a[24:27],v[84:87]
v_perm_b32 v233, v231, v230, s87
aw a120,v232
mm v[84:87],v[204:207],a[4:7],v[84:87]
rt v[196:197],v240 offset:8448
rt v[198:199],v240 offset:9472
mm v[52:55],v[204:207],a[0:3],v[52:55]
s_load_dwordx2 s[16:17], s[0:1], 0
s_load_dwordx4 s[36:39], s[0:1], 80
mm v[52:55],v[200:203],a[20:23],v[52:55]
ld a[84:87],v245,s[44:47],s55 offen offset:1024 sc1
mm v[20:23],v[200:203],a[16:19],v[20:23]
wt lgkmcnt(4)
aw a124,v233
mm v[20:23],v[204:207],a[12:15],v[20:23]
rt v[200:201],v241 offset:0
rt v[202:203],v241 offset:1024
mm v[24:27],v[212:215],a[12:15],v[24:27]
ar v234,a139
ar v235,a143
mm v[24:27],v[208:211],a[16:19],v[24:27]
ld a[88:91],v246,s[44:47],s55 offen offset:1024 sc1
mm v[56:59],v[208:211],a[20:23],v[56:59]
v_perm_b32 v236, v235, v234, s86
v_perm_b32 v237, v235, v234, s87
mm v[56:59],v[212:215],a[0:3],v[56:59]
rt v[204:205],v241 offset:8448
rt v[206:207],v241 offset:9472
mm v[88:91],v[212:215],a[4:7],v[88:91]
aw a121,v236
aw a125,v237
mm v[88:91],v[208:211],a[24:27],v[88:91]
.La5_LW7_2:
s_cbranch_execz .La5_LW7_6
ld a[92:95],v247,s[44:47],s55 offen offset:1024 sc1
mm v[120:123],v[208:211],a[28:31],v[120:123]
ar v230,a147
ar v231,a151
mm v[120:123],v[212:215],a[8:11],v[120:123]
rt v[208:209],v242 offset:0
rt v[210:211],v242 offset:1024
mm v[124:127],v[220:223],a[8:11],v[124:127]
s_add_u32 m0, s83, 16896
v_perm_b32 v232, v231, v230, s86
mm v[124:127],v[216:219],a[28:31],v[124:127]
ld v224,s[56:59],s80 offen lds
mm v[92:95],v[216:219],a[24:27],v[92:95]
v_perm_b32 v233, v231, v230, s87
aw a122,v232
mm v[92:95],v[220:223],a[4:7],v[92:95]
rt v[212:213],v242 offset:8448
rt v[214:215],v242 offset:9472
mm v[60:63],v[220:223],a[0:3],v[60:63]
)ASM" R"ASM(aw a126,v233
ar v234,a155
mm v[60:63],v[216:219],a[20:23],v[60:63]
ar v235,a159
v_perm_b32 v236, v235, v234, s86
mm v[28:31],v[216:219],a[16:19],v[28:31]
wt vmcnt(10) lgkmcnt(4)
v_perm_b32 v237, v235, v234, s87
mm v[28:31],v[220:223],a[12:15],v[28:31]
rt v[216:217],v243 offset:0
rt v[218:219],v243 offset:1024
mm v[0:3],v[192:195],a[64:67],v[0:3]
s_add_u32 m0, s83, 17920
aw a123,v236
mm v[0:3],v[196:199],a[32:35],v[0:3]
ld v225,s[56:59],s80 offen lds
mm v[32:35],v[196:199],a[40:43],v[32:35]
aw a127,v237
ar v230,a160
mm v[32:35],v[192:195],a[68:71],v[32:35]
rt v[220:221],v243 offset:8448
rt v[222:223],v243 offset:9472
mm v[64:67],v[192:195],a[72:75],v[64:67]
s_add_u32 m0, s83, 19072
ar v231,a164
mm v[64:67],v[196:199],a[44:47],v[64:67]
ld v226,s[56:59],s80 offen lds
mm v[96:99],v[196:199],a[48:51],v[96:99]
v_perm_b32 v232, v231, v230, s86
v_perm_b32 v233, v231, v230, s87
mm v[96:99],v[192:195],a[36:39],v[96:99]
rt v[196:197],v240 offset:128
rt v[198:199],v240 offset:1152
mm v[100:103],v[200:203],a[36:39],v[100:103]
aw a128,v232
aw a132,v233
mm v[100:103],v[204:207],a[48:51],v[100:103]
ld v[136:139],v244,s[44:47],s55 offen offset:2048 sc1
mm v[68:71],v[204:207],a[44:47],v[68:71]
ar v234,a168
ar v235,a172
mm v[68:71],v[200:203],a[72:75],v[68:71]
rt v[192:193],v240 offset:8576
rt v[194:195],v240 offset:9600
mm v[36:39],v[200:203],a[68:71],v[36:39]
v_perm_b32 v236, v235, v234, s86
v_perm_b32 v237, v235, v234, s87
mm v[36:39],v[204:207],a[40:43],v[36:39]
ld v[140:143],v245,s[44:47],s55 offen offset:2048 sc1
mm v[4:7],v[204:207],a[32:35],v[4:7]
wt lgkmcnt(4)
aw a129,v236
mm v[4:7],v[200:203],a[64:67],v[4:7]
rt v[204:205],v241 offset:128
rt v[206:207],v241 offset:1152
mm v[8:11],v[208:211],a[64:67],v[8:11]
aw a133,v237
ar v230,a176
mm v[8:11],v[212:215],a[32:35],v[8:11]
ld v[152:155],v246,s[44:47],s55 offen offset:2048 sc1
mm v[40:43],v[212:215],a[40:43],v[40:43]
ar v231,a180
v_perm_b32 v232, v231, v230, s86
mm v[40:43],v[208:211],a[68:71],v[40:43]
rt v[200:201],v241 offset:8576
rt v[202:203],v241 offset:9600
mm v[72:75],v[208:211],a[72:75],v[72:75]
v_perm_b32 v233, v231, v230, s87
aw a130,v232
mm v[72:75],v[212:215],a[44:47],v[72:75]
ld v[156:159],v247,s[44:47],s55 offen offset:2048 sc1
mm v[104:107],v[212:215],a[48:51],v[104:107]
aw a134,v233
ar v234,a184
mm v[104:107],v[208:211],a[36:39],v[104:107]
rt v[212:213],v242 offset:128
rt v[214:215],v242 offset:1152
mm v[108:111],v[216:219],a[36:39],v[108:111]
ar v235,a188
v_perm_b32 v236, v235, v234, s86
mm v[108:111],v[220:223],a[48:51],v[108:111]
ld v[168:171],v244,s[44:47],s55 offen offset:3072 sc1
mm v[76:79],v[220:223],a[44:47],v[76:79]
v_perm_b32 v237, v235, v234, s87
aw a131,v236
mm v[76:79],v[216:219],a[72:75],v[76:79]
rt v[208:209],v242 offset:8576
rt v[210:211],v242 offset:9600
mm v[44:47],v[216:219],a[68:71],v[44:47]
aw a135,v237
ar v230,a161
mm v[44:47],v[220:223],a[40:43],v[44:47]
s_nop 0
v_cmp_eq_u64 vcc, v[250:251], v[252:253]
mm v[12:15],v[220:223],a[32:35],v[12:15]
wt lgkmcnt(4)
ar v231,a165
mm v[12:15],v[216:219],a[64:67],v[12:15]
rt v[220:221],v243 offset:128
rt v[222:223],v243 offset:1152
)ASM" R"ASM(mm v[16:19],v[196:199],a[64:67],v[16:19]
v_perm_b32 v232, v231, v230, s86
v_perm_b32 v233, v231, v230, s87
mm v[16:19],v[192:195],a[32:35],v[16:19]
ld v[172:175],v245,s[44:47],s55 offen offset:3072 sc1
mm v[48:51],v[192:195],a[40:43],v[48:51]
aw a136,v232
aw a140,v233
mm v[48:51],v[196:199],a[68:71],v[48:51]
rt v[216:217],v243 offset:8576
rt v[218:219],v243 offset:9600
mm v[80:83],v[196:199],a[72:75],v[80:83]
ar v234,a169
ar v235,a173
mm v[80:83],v[192:195],a[44:47],v[80:83]
ld v[184:187],v246,s[44:47],s55 offen offset:3072 sc1
mm v[112:115],v[192:195],a[48:51],v[112:115]
wt vmcnt(18) lgkmcnt(0)
s_barrier
mm v[112:115],v[196:199],a[36:39],v[112:115]
rt v[192:193],v240 offset:16896
rt v[194:195],v240 offset:17920
mm v[116:119],v[204:207],a[36:39],v[116:119]
s_xor_b32 s90, s72, 0
s_lshl_b32 s92, s76, 5
mm v[116:119],v[200:203],a[48:51],v[116:119]
buffer_load_dwordx2 v[250:251], v248, s[64:67], s5 offen sc1
mm v[84:87],v[200:203],a[44:47],v[84:87]
s_add_u32 s92, s92, s90
s_lshl2_add_u32 s92, s92, s3
mm v[84:87],v[204:207],a[72:75],v[84:87]
rt v[196:197],v240 offset:25344
rt v[198:199],v240 offset:26368
mm v[52:55],v[204:207],a[68:71],v[52:55]
s_lshl_b32 s92, s92, 12
v_perm_b32 v236, v235, v234, s86
mm v[52:55],v[200:203],a[40:43],v[52:55]
ld v[188:191],v247,s[44:47],s55 offen offset:3072 sc1
mm v[20:23],v[200:203],a[32:35],v[20:23]
s_nop 0
s_bfe_u32 s55, s4, 262172
mm v[20:23],v[204:207],a[64:67],v[20:23]
rt v[200:201],v241 offset:16896
rt v[202:203],v241 offset:17920
mm v[24:27],v[212:215],a[64:67],v[24:27]
s_add_u32 m0, s83, 20096
s_xor_b32 s55, s55, s72
mm v[24:27],v[208:211],a[32:35],v[24:27]
ld v227,s[56:59],s80 offen lds
mm v[56:59],v[208:211],a[40:43],v[56:59]
s_add_u32 s80, s80, 131072
s_lshl_b32 s55, s55, 13
mm v[56:59],v[212:215],a[68:71],v[56:59]
rt v[204:205],v241 offset:25344
rt v[206:207],v241 offset:26368
mm v[88:91],v[212:215],a[72:75],v[88:91]
s_add_u32 m0, s83, 33792
v_perm_b32 v237, v235, v234, s87
mm v[88:91],v[208:211],a[44:47],v[88:91]
ld v224,s[56:59],s80 offen lds
mm v[120:123],v[208:211],a[48:51],v[120:123]
s_add_u32 s68, s16, s92
s_addc_u32 s69, s17, 0
mm v[120:123],v[212:215],a[36:39],v[120:123]
rt v[208:209],v242 offset:16896
rt v[210:211],v242 offset:17920
mm v[124:127],v[220:223],a[36:39],v[124:127]
s_add_u32 m0, s83, 34816
s_and_b32 s69, s69, 0xffff
mm v[124:127],v[216:219],a[48:51],v[124:127]
ld v225,s[56:59],s80 offen lds
mm v[92:95],v[216:219],a[44:47],v[92:95]
s_mov_b32 s70, 4096
s_mov_b32 s71, 0x20000
mm v[92:95],v[220:223],a[72:75],v[92:95]
rt v[212:213],v242 offset:25344
rt v[214:215],v242 offset:26368
mm v[60:63],v[220:223],a[68:71],v[60:63]
s_add_u32 m0, s83, 35968
aw a137,v236
mm v[60:63],v[216:219],a[40:43],v[60:63]
ld v226,s[56:59],s80 offen lds
mm v[28:31],v[216:219],a[32:35],v[28:31]
wt vmcnt(16) lgkmcnt(4)
aw a141,v237
mm v[28:31],v[220:223],a[64:67],v[28:31]
rt v[216:217],v243 offset:16896
rt v[218:219],v243 offset:17920
mm v[0:3],v[192:195],a[52:55],v[0:3]
s_add_u32 m0, s83, 36992
ar v230,a177
mm v[0:3],v[196:199],a[80:83],v[0:3]
ld v227,s[56:59],s80 offen lds
mm v[32:35],v[196:199],a[84:87],v[32:35]
s_bitcmp1_b32 vcc_lo, 4
s_cbranch_scc0 .La5_LO_NC4
.La5_LO_KC4:
mm v[32:35],v[192:195],a[56:59],v[32:35]
rt v[220:221],v243 offset:25344
rt v[222:223],v243 offset:26368
)ASM" R"ASM(mm v[64:67],v[192:195],a[60:63],v[64:67]
s_bfe_u32 s80, s4, 262164
s_xor_b32 s80, s80, s72
mm v[64:67],v[196:199],a[88:91],v[64:67]
ld a[0:3],v244,s[44:47],s52 offen offset:0 sc1
mm v[96:99],v[196:199],a[92:95],v[96:99]
s_lshl_b32 s80, s80, 18
ar v231,a181
mm v[96:99],v[192:195],a[76:79],v[96:99]
rt v[196:197],v240 offset:17024
rt v[198:199],v240 offset:18048
mm v[100:103],v[200:203],a[76:79],v[100:103]
s_add_u32 s48, s36, 0
s_addc_u32 s49, s37, 0
mm v[100:103],v[204:207],a[92:95],v[100:103]
ld a[4:7],v245,s[44:47],s52 offen offset:0 sc1
mm v[68:71],v[204:207],a[88:91],v[68:71]
s_and_b32 s49, s49, 0xffff
s_mov_b32 s50, 2048
mm v[68:71],v[200:203],a[60:63],v[68:71]
rt v[192:193],v240 offset:25472
rt v[194:195],v240 offset:26496
mm v[36:39],v[200:203],a[56:59],v[36:39]
s_mov_b32 s51, 0x20000
v_and_b32 v238, 0xf0, v255
mm v[36:39],v[204:207],a[84:87],v[36:39]
ld a[8:11],v246,s[44:47],s52 offen offset:0 sc1
mm v[4:7],v[204:207],a[80:83],v[4:7]
wt lgkmcnt(4)
v_lshlrev_b32 v238, 2, v238
mm v[4:7],v[200:203],a[52:55],v[4:7]
rt v[204:205],v241 offset:17024
rt v[206:207],v241 offset:18048
mm v[8:11],v[208:211],a[52:55],v[8:11]
v_bfe_u32 v249, v255, 8, 1
v_lshl_add_u32 v238, v249, 5, v238
mm v[8:11],v[212:215],a[80:83],v[8:11]
ld a[12:15],v247,s[44:47],s52 offen offset:0 sc1
mm v[40:43],v[212:215],a[84:87],v[40:43]
v_lshrrev_b32_e64 v249, 9, v255
v_lshl_add_u32 v238, v249, 4, v238
mm v[40:43],v[208:211],a[56:59],v[40:43]
rt v[200:201],v241 offset:25472
rt v[202:203],v241 offset:26496
mm v[72:75],v[208:211],a[60:63],v[72:75]
s_lshr_b32 s23, s72, 3
s_lshl3_add_u32 s92, s23, s76
mm v[72:75],v[212:215],a[88:91],v[72:75]
ld a[16:19],v244,s[44:47],s52 offen offset:1024 sc1
mm v[104:107],v[212:215],a[92:95],v[104:107]
s_lshl_b32 s92, s92, 19
s_lshl_b32 s97, s3, 16
mm v[104:107],v[208:211],a[76:79],v[104:107]
rt v[212:213],v242 offset:17024
rt v[214:215],v242 offset:18048
mm v[108:111],v[216:219],a[76:79],v[108:111]
s_add_u32 s94, s92, s97
s_add_u32 s95, s94, 32768
mm v[108:111],v[220:223],a[92:95],v[108:111]
ld a[20:23],v245,s[44:47],s52 offen offset:1024 sc1
mm v[76:79],v[220:223],a[88:91],v[76:79]
s_sub_u32 s96, s92, s97
s_add_u32 s96, s96, 458752
mm v[76:79],v[216:219],a[60:63],v[76:79]
rt v[208:209],v242 offset:25472
rt v[210:211],v242 offset:26496
mm v[44:47],v[216:219],a[56:59],v[44:47]
s_add_u32 s97, s96, 32768
s_lshl3_add_u32 s92, s23, s101
mm v[44:47],v[220:223],a[84:87],v[44:47]
s_lshl_b32 s85, s76, 15
s_lshl3_add_u32 s92, s92, s85
mm v[12:15],v[220:223],a[80:83],v[12:15]
wt lgkmcnt(4)
s_add_u32 s92, s92, 2097152
mm v[12:15],v[216:219],a[52:55],v[12:15]
rt v[220:221],v243 offset:17024
rt v[222:223],v243 offset:18048
mm v[16:19],v[196:199],a[52:55],v[16:19]
v_perm_b32 v232, v231, v230, s86
v_perm_b32 v233, v231, v230, s87
mm v[16:19],v[192:195],a[80:83],v[16:19]
ld a[24:27],v246,s[44:47],s52 offen offset:1024 sc1
mm v[48:51],v[192:195],a[84:87],v[48:51]
aw a138,v232
aw a142,v233
mm v[48:51],v[196:199],a[56:59],v[48:51]
rt v[216:217],v243 offset:25472
rt v[218:219],v243 offset:26496
mm v[80:83],v[196:199],a[60:63],v[80:83]
ar v234,a185
ar v235,a189
mm v[80:83],v[192:195],a[88:91],v[80:83]
ld a[28:31],v247,s[44:47],s52 offen offset:1024 sc1
mm v[112:115],v[192:195],a[92:95],v[112:115]
v_perm_b32 v236, v235, v234, s86
v_perm_b32 v237, v235, v234, s87
mm v[112:115],v[196:199],a[76:79],v[112:115]
rt v[192:193],v240 offset:33792
rt v[194:195],v240 offset:34816
)ASM" R"ASM(mm v[116:119],v[204:207],a[76:79],v[116:119]
s_add_u32 m0, s83, 50688
aw a139,v236
mm v[116:119],v[200:203],a[92:95],v[116:119]
ld v224,s[56:59],s80 offen lds
mm v[84:87],v[200:203],a[88:91],v[84:87]
aw a143,v237
ar v230,a162
mm v[84:87],v[204:207],a[60:63],v[84:87]
rt v[196:197],v240 offset:42240
rt v[198:199],v240 offset:43264
mm v[52:55],v[204:207],a[56:59],v[52:55]
s_add_u32 m0, s83, 51712
ar v231,a166
mm v[52:55],v[200:203],a[84:87],v[52:55]
ld v225,s[56:59],s80 offen lds
mm v[20:23],v[200:203],a[80:83],v[20:23]
wt lgkmcnt(4)
v_perm_b32 v232, v231, v230, s86
mm v[20:23],v[204:207],a[52:55],v[20:23]
rt v[200:201],v241 offset:33792
rt v[202:203],v241 offset:34816
mm v[24:27],v[212:215],a[52:55],v[24:27]
s_add_u32 m0, s83, 52864
v_perm_b32 v233, v231, v230, s87
mm v[24:27],v[208:211],a[80:83],v[24:27]
ld v226,s[56:59],s80 offen lds
mm v[56:59],v[208:211],a[84:87],v[56:59]
aw a144,v232
aw a148,v233
mm v[56:59],v[212:215],a[56:59],v[56:59]
rt v[204:205],v241 offset:42240
rt v[206:207],v241 offset:43264
mm v[88:91],v[212:215],a[60:63],v[88:91]
ar v234,a170
ar v235,a174
mm v[88:91],v[208:211],a[88:91],v[88:91]
ld a[32:35],v244,s[44:47],s52 offen offset:2048 sc1
mm v[120:123],v[208:211],a[92:95],v[120:123]
v_perm_b32 v236, v235, v234, s86
v_perm_b32 v237, v235, v234, s87
mm v[120:123],v[212:215],a[76:79],v[120:123]
rt v[208:209],v242 offset:33792
rt v[210:211],v242 offset:34816
mm v[124:127],v[220:223],a[76:79],v[124:127]
aw a145,v236
aw a149,v237
mm v[124:127],v[216:219],a[92:95],v[124:127]
ld a[36:39],v245,s[44:47],s52 offen offset:2048 sc1
mm v[92:95],v[216:219],a[88:91],v[92:95]
ar v230,a178
ar v231,a182
mm v[92:95],v[220:223],a[60:63],v[92:95]
rt v[212:213],v242 offset:42240
rt v[214:215],v242 offset:43264
mm v[60:63],v[220:223],a[56:59],v[60:63]
v_perm_b32 v232, v231, v230, s86
v_perm_b32 v233, v231, v230, s87
mm v[60:63],v[216:219],a[84:87],v[60:63]
aw a146,v232
aw a150,v233
mm v[28:31],v[216:219],a[80:83],v[28:31]
wt vmcnt(18) lgkmcnt(4)
ar v234,a186
mm v[28:31],v[220:223],a[52:55],v[28:31]
rt v[216:217],v243 offset:33792
rt v[218:219],v243 offset:34816
mm v[0:3],v[192:195],v[136:139],v[0:3]
ar v235,a190
v_perm_b32 v236, v235, v234, s86
mm v[0:3],v[196:199],v[168:171],v[0:3]
ld a[40:43],v246,s[44:47],s52 offen offset:2048 sc1
mm v[32:35],v[196:199],v[172:175],v[32:35]
v_perm_b32 v237, v235, v234, s87
aw a147,v236
mm v[32:35],v[192:195],v[140:143],v[32:35]
rt v[220:221],v243 offset:42240
rt v[222:223],v243 offset:43264
mm v[64:67],v[192:195],v[152:155],v[64:67]
aw a151,v237
ar v230,a163
mm v[64:67],v[196:199],v[184:187],v[64:67]
ld a[44:47],v247,s[44:47],s52 offen offset:2048 sc1
mm v[96:99],v[196:199],v[188:191],v[96:99]
ar v231,a167
v_perm_b32 v232, v231, v230, s86
mm v[96:99],v[192:195],v[156:159],v[96:99]
rt v[196:197],v240 offset:33920
rt v[198:199],v240 offset:34944
mm v[100:103],v[200:203],v[156:159],v[100:103]
v_perm_b32 v233, v231, v230, s87
aw a152,v232
mm v[100:103],v[204:207],v[188:191],v[100:103]
ld a[48:51],v244,s[44:47],s52 offen offset:3072 sc1
mm v[68:71],v[204:207],v[184:187],v[68:71]
aw a156,v233
ar v234,a171
mm v[68:71],v[200:203],v[152:155],v[68:71]
rt v[192:193],v240 offset:42368
rt v[194:195],v240 offset:43392
)ASM" R"ASM(mm v[36:39],v[200:203],v[140:143],v[36:39]
v_add_u32 v240, 4294899712, v240
ar v235,a175
mm v[36:39],v[204:207],v[172:175],v[36:39]
ld a[52:55],v245,s[44:47],s52 offen offset:3072 sc1
mm v[4:7],v[204:207],v[168:171],v[4:7]
wt lgkmcnt(4)
v_perm_b32 v236, v235, v234, s86
mm v[4:7],v[200:203],v[136:139],v[4:7]
rt v[204:205],v241 offset:33920
rt v[206:207],v241 offset:34944
mm v[8:11],v[208:211],v[136:139],v[8:11]
s_nop 0
v_cmp_eq_u64 vcc, v[250:251], v[252:253]
mm v[8:11],v[212:215],v[168:171],v[8:11]
ld a[56:59],v246,s[44:47],s52 offen offset:3072 sc1
mm v[40:43],v[212:215],v[172:175],v[40:43]
v_perm_b32 v237, v235, v234, s87
aw a153,v236
mm v[40:43],v[208:211],v[140:143],v[40:43]
rt v[200:201],v241 offset:42368
rt v[202:203],v241 offset:43392
mm v[72:75],v[208:211],v[152:155],v[72:75]
v_add_u32 v241, 4294899712, v241
aw a157,v237
mm v[72:75],v[212:215],v[184:187],v[72:75]
ld a[60:63],v247,s[44:47],s52 offen offset:3072 sc1
mm v[104:107],v[212:215],v[188:191],v[104:107]
ar v230,a179
ar v231,a183
mm v[104:107],v[208:211],v[156:159],v[104:107]
rt v[212:213],v242 offset:33920
rt v[214:215],v242 offset:34944
mm v[108:111],v[216:219],v[156:159],v[108:111]
v_perm_b32 v232, v231, v230, s86
v_perm_b32 v233, v231, v230, s87
mm v[108:111],v[220:223],v[188:191],v[108:111]
buffer_load_dwordx2 v[250:251], v248, s[64:67], s5 offen sc1
mm v[76:79],v[220:223],v[184:187],v[76:79]
aw a154,v232
aw a158,v233
mm v[76:79],v[216:219],v[152:155],v[76:79]
rt v[208:209],v242 offset:42368
rt v[210:211],v242 offset:43392
mm v[44:47],v[216:219],v[140:143],v[44:47]
s_add_u32 m0, s83, 53888
v_add_u32 v242, 4294899712, v242
mm v[44:47],v[220:223],v[172:175],v[44:47]
ld v227,s[56:59],s80 offen lds
mm v[12:15],v[220:223],v[168:171],v[12:15]
wt lgkmcnt(4)
s_bfe_u32 s80, s4, 262168
mm v[12:15],v[216:219],v[136:139],v[12:15]
rt v[220:221],v243 offset:33920
rt v[222:223],v243 offset:34944
mm v[16:19],v[196:199],v[136:139],v[16:19]
s_add_u32 m0, s83, 118272
s_xor_b32 s80, s80, s72
s_lshl_b32 s80, s80, 18
mm v[16:19],v[192:195],v[168:171],v[16:19]
ld v224,s[56:59],s80 offen lds
mm v[48:51],v[192:195],v[172:175],v[48:51]
ar v234,a187
ar v235,a191
mm v[48:51],v[196:199],v[140:143],v[48:51]
rt v[216:217],v243 offset:42368
rt v[218:219],v243 offset:43392
mm v[80:83],v[196:199],v[152:155],v[80:83]
s_add_u32 m0, s83, 119296
v_add_u32 v243, 4294899712, v243
mm v[80:83],v[192:195],v[184:187],v[80:83]
ld v225,s[56:59],s80 offen lds
mm v[112:115],v[192:195],v[188:191],v[112:115]
wt vmcnt(23) lgkmcnt(0)
s_barrier
mm v[112:115],v[196:199],v[156:159],v[112:115]
rt v[192:193],v240 offset:16896
rt v[194:195],v240 offset:17920
mm v[116:119],v[204:207],v[156:159],v[116:119]
s_add_u32 m0, s83, 120448
v_perm_b32 v236, v235, v234, s86
mm v[116:119],v[200:203],v[188:191],v[116:119]
ld v226,s[56:59],s80 offen lds
mm v[84:87],v[200:203],v[184:187],v[84:87]
s_bitcmp1_b32 vcc_lo, 5
s_cbranch_scc0 .La5_LO_NC5
.La5_LO_KC5:
mm v[84:87],v[204:207],v[152:155],v[84:87]
rt v[196:197],v240 offset:25344
rt v[198:199],v240 offset:26368
mm v[52:55],v[204:207],v[140:143],v[52:55]
v_perm_b32 v237, v235, v234, s87
aw a155,v236
mm v[52:55],v[200:203],v[172:175],v[52:55]
ld v[128:131],v244,s[44:47],s53 offen offset:0 sc1
mm v[20:23],v[200:203],v[168:171],v[20:23]
s_nop 0
aw a159,v237
mm v[20:23],v[204:207],v[136:139],v[20:23]
rt v[200:201],v241 offset:16896
)ASM" R"ASM(rt v[202:203],v241 offset:17920
mm v[24:27],v[212:215],v[136:139],v[24:27]
ar v230,a192
ar v231,a196
mm v[24:27],v[208:211],v[168:171],v[24:27]
ld v[132:135],v245,s[44:47],s53 offen offset:0 sc1
mm v[56:59],v[208:211],v[172:175],v[56:59]
v_perm_b32 v232, v231, v230, s86
v_perm_b32 v233, v231, v230, s87
mm v[56:59],v[212:215],v[140:143],v[56:59]
rt v[204:205],v241 offset:25344
rt v[206:207],v241 offset:26368
mm v[88:91],v[212:215],v[152:155],v[88:91]
aw a160,v232
aw a164,v233
mm v[88:91],v[208:211],v[184:187],v[88:91]
ld v[144:147],v246,s[44:47],s53 offen offset:0 sc1
mm v[120:123],v[208:211],v[188:191],v[120:123]
ar v234,a200
.La5_LW7_3:
s_cbranch_execz .La5_LW7_7
ar v235,a204
mm v[120:123],v[212:215],v[156:159],v[120:123]
rt v[208:209],v242 offset:16896
rt v[210:211],v242 offset:17920
mm v[124:127],v[220:223],v[156:159],v[124:127]
v_perm_b32 v236, v235, v234, s86
v_perm_b32 v237, v235, v234, s87
mm v[124:127],v[216:219],v[188:191],v[124:127]
ld v[156:159],v247,s[44:47],s53 offen offset:0 sc1
mm v[92:95],v[216:219],v[184:187],v[92:95]
aw a161,v236
aw a165,v237
mm v[92:95],v[220:223],v[152:155],v[92:95]
rt v[212:213],v242 offset:25344
rt v[214:215],v242 offset:26368
mm v[60:63],v[220:223],v[140:143],v[60:63]
ar v230,a208
ar v231,a212
mm v[60:63],v[216:219],v[172:175],v[60:63]
v_perm_b32 v232, v231, v230, s86
v_perm_b32 v233, v231, v230, s87
mm v[28:31],v[216:219],v[168:171],v[28:31]
wt vmcnt(20) lgkmcnt(4)
aw a162,v232
mm v[28:31],v[220:223],v[136:139],v[28:31]
rt v[216:217],v243 offset:16896
rt v[218:219],v243 offset:17920
mm v[0:3],v[192:195],a[0:3],v[0:3]
aw a166,v233
ar v234,a216
mm v[0:3],v[196:199],a[16:19],v[0:3]
ld v[136:139],v244,s[44:47],s53 offen offset:1024 sc1
mm v[32:35],v[196:199],a[20:23],v[32:35]
ar v235,a220
v_perm_b32 v236, v235, v234, s86
mm v[32:35],v[192:195],a[4:7],v[32:35]
rt v[220:221],v243 offset:25344
rt v[222:223],v243 offset:26368
mm v[64:67],v[192:195],a[8:11],v[64:67]
v_perm_b32 v237, v235, v234, s87
aw a163,v236
mm v[64:67],v[196:199],a[24:27],v[64:67]
ld v[140:143],v245,s[44:47],s53 offen offset:1024 sc1
mm v[96:99],v[196:199],a[28:31],v[96:99]
aw a167,v237
ar v230,a193
mm v[96:99],v[192:195],a[12:15],v[96:99]
rt v[196:197],v240 offset:17024
rt v[198:199],v240 offset:18048
mm v[100:103],v[200:203],a[12:15],v[100:103]
ar v231,a197
v_perm_b32 v232, v231, v230, s86
mm v[100:103],v[204:207],a[28:31],v[100:103]
ld v[152:155],v246,s[44:47],s53 offen offset:1024 sc1
mm v[68:71],v[204:207],a[24:27],v[68:71]
v_perm_b32 v233, v231, v230, s87
aw a168,v232
mm v[68:71],v[200:203],a[8:11],v[68:71]
rt v[192:193],v240 offset:25472
rt v[194:195],v240 offset:26496
mm v[36:39],v[200:203],a[4:7],v[36:39]
aw a172,v233
ar v234,a201
mm v[36:39],v[204:207],a[20:23],v[36:39]
ld v[168:171],v247,s[44:47],s53 offen offset:1024 sc1
mm v[4:7],v[204:207],a[16:19],v[4:7]
wt lgkmcnt(4)
ar v235,a205
mm v[4:7],v[200:203],a[0:3],v[4:7]
rt v[204:205],v241 offset:17024
rt v[206:207],v241 offset:18048
mm v[8:11],v[208:211],a[0:3],v[8:11]
s_add_u32 m0, s83, 121472
v_perm_b32 v236, v235, v234, s86
mm v[8:11],v[212:215],a[16:19],v[8:11]
ld v227,s[56:59],s80 offen lds
mm v[40:43],v[212:215],a[20:23],v[40:43]
s_add_u32 s80, s80, 131072
v_perm_b32 v237, v235, v234, s87
)ASM" R"ASM(mm v[40:43],v[208:211],a[4:7],v[40:43]
rt v[200:201],v241 offset:25472
rt v[202:203],v241 offset:26496
mm v[72:75],v[208:211],a[8:11],v[72:75]
s_add_u32 m0, s83, 67584
aw a169,v236
mm v[72:75],v[212:215],a[24:27],v[72:75]
ld v224,s[56:59],s80 offen lds
mm v[104:107],v[212:215],a[28:31],v[104:107]
aw a173,v237
ar v230,a209
mm v[104:107],v[208:211],a[12:15],v[104:107]
rt v[212:213],v242 offset:17024
rt v[214:215],v242 offset:18048
mm v[108:111],v[216:219],a[12:15],v[108:111]
s_add_u32 m0, s83, 68608
ar v231,a213
mm v[108:111],v[220:223],a[28:31],v[108:111]
ld v225,s[56:59],s80 offen lds
mm v[76:79],v[220:223],a[24:27],v[76:79]
v_perm_b32 v232, v231, v230, s86
v_perm_b32 v233, v231, v230, s87
mm v[76:79],v[216:219],a[8:11],v[76:79]
rt v[208:209],v242 offset:25472
rt v[210:211],v242 offset:26496
mm v[44:47],v[216:219],a[4:7],v[44:47]
aw a170,v232
aw a174,v233
mm v[44:47],v[220:223],a[20:23],v[44:47]
ar v234,a217
ar v235,a221
mm v[12:15],v[220:223],a[16:19],v[12:15]
wt lgkmcnt(4)
v_perm_b32 v236, v235, v234, s86
mm v[12:15],v[216:219],a[0:3],v[12:15]
rt v[220:221],v243 offset:17024
rt v[222:223],v243 offset:18048
mm v[16:19],v[196:199],a[0:3],v[16:19]
s_add_u32 m0, s83, 69760
v_perm_b32 v237, v235, v234, s87
mm v[16:19],v[192:195],a[16:19],v[16:19]
ld v226,s[56:59],s80 offen lds
mm v[48:51],v[192:195],a[20:23],v[48:51]
aw a171,v236
aw a175,v237
mm v[48:51],v[196:199],a[4:7],v[48:51]
rt v[216:217],v243 offset:25472
rt v[218:219],v243 offset:26496
mm v[80:83],v[196:199],a[8:11],v[80:83]
s_add_u32 m0, s83, 70784
ar v230,a194
mm v[80:83],v[192:195],a[24:27],v[80:83]
ld v227,s[56:59],s80 offen lds
mm v[112:115],v[192:195],a[28:31],v[112:115]
s_bfe_u32 s80, s4, 262172
s_xor_b32 s80, s80, s72
mm v[112:115],v[196:199],a[12:15],v[112:115]
rt v[192:193],v240 offset:33792
rt v[194:195],v240 offset:34816
mm v[116:119],v[204:207],a[12:15],v[116:119]
s_add_u32 m0, s83, 84480
s_lshl_b32 s80, s80, 18
mm v[116:119],v[200:203],a[28:31],v[116:119]
ld v224,s[56:59],s80 offen lds
mm v[84:87],v[200:203],a[24:27],v[84:87]
ar v231,a198
v_perm_b32 v232, v231, v230, s86
mm v[84:87],v[204:207],a[8:11],v[84:87]
rt v[196:197],v240 offset:42240
rt v[198:199],v240 offset:43264
mm v[52:55],v[204:207],a[4:7],v[52:55]
s_add_u32 m0, s83, 85504
v_perm_b32 v233, v231, v230, s87
mm v[52:55],v[200:203],a[20:23],v[52:55]
ld v225,s[56:59],s80 offen lds
mm v[20:23],v[200:203],a[16:19],v[20:23]
wt lgkmcnt(4)
aw a176,v232
mm v[20:23],v[204:207],a[0:3],v[20:23]
rt v[200:201],v241 offset:33792
rt v[202:203],v241 offset:34816
mm v[24:27],v[212:215],a[0:3],v[24:27]
s_add_u32 m0, s83, 86656
aw a180,v233
mm v[24:27],v[208:211],a[16:19],v[24:27]
ld v226,s[56:59],s80 offen lds
mm v[56:59],v[208:211],a[20:23],v[56:59]
ar v234,a202
ar v235,a206
mm v[56:59],v[212:215],a[4:7],v[56:59]
rt v[204:205],v241 offset:42240
rt v[206:207],v241 offset:43264
mm v[88:91],v[212:215],a[8:11],v[88:91]
s_add_u32 m0, s83, 87680
v_perm_b32 v236, v235, v234, s86
mm v[88:91],v[208:211],a[24:27],v[88:91]
ld v227,s[56:59],s80 offen lds
mm v[120:123],v[208:211],a[28:31],v[120:123]
s_add_u32 s80, s80, 131072
v_perm_b32 v237, v235, v234, s87
)ASM" R"ASM(mm v[120:123],v[212:215],a[12:15],v[120:123]
rt v[208:209],v242 offset:33792
rt v[210:211],v242 offset:34816
mm v[124:127],v[220:223],a[12:15],v[124:127]
s_add_u32 m0, s83, 101376
aw a177,v236
mm v[124:127],v[216:219],a[28:31],v[124:127]
ld v224,s[56:59],s80 offen lds
mm v[92:95],v[216:219],a[24:27],v[92:95]
aw a181,v237
ar v230,a210
mm v[92:95],v[220:223],a[8:11],v[92:95]
rt v[212:213],v242 offset:42240
rt v[214:215],v242 offset:43264
mm v[60:63],v[220:223],a[4:7],v[60:63]
ar v231,a214
v_perm_b32 v232, v231, v230, s86
mm v[60:63],v[216:219],a[20:23],v[60:63]
wt vmcnt(22)
v_cmp_eq_u64 vcc, v[250:251], v[252:253]
mm v[28:31],v[216:219],a[16:19],v[28:31]
wt lgkmcnt(4)
v_perm_b32 v233, v231, v230, s87
mm v[28:31],v[220:223],a[0:3],v[28:31]
rt v[216:217],v243 offset:33792
rt v[218:219],v243 offset:34816
mm v[0:3],v[192:195],a[32:35],v[0:3]
s_add_u32 m0, s83, 102400
aw a178,v232
mm v[0:3],v[196:199],a[48:51],v[0:3]
ld v225,s[56:59],s80 offen lds
mm v[32:35],v[196:199],a[52:55],v[32:35]
aw a182,v233
ar v234,a218
mm v[32:35],v[192:195],a[36:39],v[32:35]
rt v[220:221],v243 offset:42240
rt v[222:223],v243 offset:43264
mm v[64:67],v[192:195],a[40:43],v[64:67]
s_add_u32 m0, s83, 103552
ar v235,a222
mm v[64:67],v[196:199],a[56:59],v[64:67]
ld v226,s[56:59],s80 offen lds
mm v[96:99],v[196:199],a[60:63],v[96:99]
v_perm_b32 v236, v235, v234, s86
v_perm_b32 v237, v235, v234, s87
mm v[96:99],v[192:195],a[44:47],v[96:99]
rt v[196:197],v240 offset:33920
rt v[198:199],v240 offset:34944
mm v[100:103],v[200:203],a[44:47],v[100:103]
s_add_u32 m0, s83, 104576
aw a179,v236
mm v[100:103],v[204:207],a[60:63],v[100:103]
ld v227,s[56:59],s80 offen lds
mm v[68:71],v[204:207],a[56:59],v[68:71]
s_bitcmp1_b32 vcc_lo, 6
s_cbranch_scc0 .La5_LO_NC6
.La5_LO_KC6:
mm v[68:71],v[200:203],a[40:43],v[68:71]
rt v[192:193],v240 offset:42368
rt v[194:195],v240 offset:43392
mm v[36:39],v[200:203],a[36:39],v[36:39]
v_bfe_u32 v224, v255, 6, 1
v_lshrrev_b32 v225, 7, v255
mm v[36:39],v[204:207],a[52:55],v[36:39]
ld a[64:67],v244,s[44:47],s54 offen offset:0 sc1
mm v[4:7],v[204:207],a[48:51],v[4:7]
wt lgkmcnt(4)
v_and_b32 v226, 48, v255
mm v[4:7],v[200:203],a[32:35],v[4:7]
rt v[204:205],v241 offset:33920
rt v[206:207],v241 offset:34944
mm v[8:11],v[208:211],a[32:35],v[8:11]
v_lshl_add_u32 v225, v225, 6, v226
v_lshl_add_u32 v224, v224, 11, v225
mm v[8:11],v[212:215],a[48:51],v[8:11]
ld a[68:71],v245,s[44:47],s54 offen offset:0 sc1
mm v[40:43],v[212:215],a[52:55],v[40:43]
aw a183,v237
ar v230,a195
mm v[40:43],v[208:211],a[36:39],v[40:43]
rt v[200:201],v241 offset:42368
rt v[202:203],v241 offset:43392
mm v[72:75],v[208:211],a[40:43],v[72:75]
ar v231,a199
v_perm_b32 v232, v231, v230, s86
mm v[72:75],v[212:215],a[56:59],v[72:75]
ld a[72:75],v244,s[44:47],s54 offen offset:1024 sc1
mm v[104:107],v[212:215],a[60:63],v[104:107]
v_perm_b32 v233, v231, v230, s87
aw a184,v232
mm v[104:107],v[208:211],a[44:47],v[104:107]
rt v[212:213],v242 offset:33920
rt v[214:215],v242 offset:34944
mm v[108:111],v[216:219],a[44:47],v[108:111]
aw a188,v233
ar v234,a203
mm v[108:111],v[220:223],a[60:63],v[108:111]
ld a[76:79],v245,s[44:47],s54 offen offset:1024 sc1
mm v[76:79],v[220:223],a[56:59],v[76:79]
ar v235,a207
)ASM" R"ASM(v_perm_b32 v236, v235, v234, s86
mm v[76:79],v[216:219],a[40:43],v[76:79]
rt v[208:209],v242 offset:42368
rt v[210:211],v242 offset:43392
mm v[44:47],v[216:219],a[36:39],v[44:47]
v_perm_b32 v237, v235, v234, s87
aw a185,v236
mm v[44:47],v[220:223],a[52:55],v[44:47]
aw a189,v237
ar v230,a211
mm v[12:15],v[220:223],a[48:51],v[12:15]
wt lgkmcnt(4)
ar v231,a215
mm v[12:15],v[216:219],a[32:35],v[12:15]
rt v[220:221],v243 offset:33920
rt v[222:223],v243 offset:34944
mm v[16:19],v[196:199],a[32:35],v[16:19]
v_perm_b32 v232, v231, v230, s86
v_perm_b32 v233, v231, v230, s87
mm v[16:19],v[192:195],a[48:51],v[16:19]
ld a[80:83],v244,s[44:47],s54 offen offset:2048 sc1
mm v[48:51],v[192:195],a[52:55],v[48:51]
aw a186,v232
aw a190,v233
mm v[48:51],v[196:199],a[36:39],v[48:51]
rt v[216:217],v243 offset:42368
rt v[218:219],v243 offset:43392
mm v[80:83],v[196:199],a[40:43],v[80:83]
ar v234,a219
ar v235,a223
mm v[80:83],v[192:195],a[56:59],v[80:83]
ld a[84:87],v245,s[44:47],s54 offen offset:2048 sc1
mm v[112:115],v[192:195],a[60:63],v[112:115]
wt vmcnt(30)
s_barrier
mm v[112:115],v[196:199],a[44:47],v[112:115]
rt v[192:193],v240 offset:50688
rt v[194:195],v240 offset:51712
mm v[116:119],v[204:207],a[44:47],v[116:119]
v_perm_b32 v236, v235, v234, s86
v_perm_b32 v237, v235, v234, s87
mm v[116:119],v[200:203],a[60:63],v[116:119]
ld a[88:91],v244,s[44:47],s54 offen offset:3072 sc1
mm v[84:87],v[200:203],a[56:59],v[84:87]
aw a187,v236
aw a191,v237
mm v[84:87],v[204:207],a[40:43],v[84:87]
rt v[196:197],v240 offset:59136
rt v[198:199],v240 offset:60160
mm v[52:55],v[204:207],a[36:39],v[52:55]
s_bitcmp1_b32 vcc_lo, 7
s_cbranch_scc0 .La5_LO_NC7
.La5_LO_KC7:
mm v[52:55],v[200:203],a[52:55],v[52:55]
ld a[92:95],v245,s[44:47],s54 offen offset:3072 sc1
mm v[20:23],v[200:203],a[48:51],v[20:23]
wt lgkmcnt(4)
ar v230,a224
mm v[20:23],v[204:207],a[32:35],v[20:23]
rt v[200:201],v241 offset:50688
rt v[202:203],v241 offset:51712
mm v[24:27],v[212:215],a[32:35],v[24:27]
ar v231,a228
v_perm_b32 v232, v231, v230, s86
mm v[24:27],v[208:211],a[48:51],v[24:27]
ld a[0:3],v244,s[44:47],s55 offen offset:0 sc1
mm v[56:59],v[208:211],a[52:55],v[56:59]
v_perm_b32 v233, v231, v230, s87
aw a192,v232
mm v[56:59],v[212:215],a[36:39],v[56:59]
rt v[204:205],v241 offset:59136
rt v[206:207],v241 offset:60160
mm v[88:91],v[212:215],a[40:43],v[88:91]
aw a196,v233
ar v234,a232
mm v[88:91],v[208:211],a[56:59],v[88:91]
ld a[4:7],v245,s[44:47],s55 offen offset:0 sc1
mm v[120:123],v[208:211],a[60:63],v[120:123]
ar v235,a236
v_perm_b32 v236, v235, v234, s86
mm v[120:123],v[212:215],a[44:47],v[120:123]
rt v[208:209],v242 offset:50688
rt v[210:211],v242 offset:51712
mm v[124:127],v[220:223],a[44:47],v[124:127]
v_perm_b32 v237, v235, v234, s87
aw a193,v236
mm v[124:127],v[216:219],a[60:63],v[124:127]
ld a[8:11],v244,s[44:47],s55 offen offset:1024 sc1
mm v[92:95],v[216:219],a[56:59],v[92:95]
aw a197,v237
ar v230,a240
mm v[92:95],v[220:223],a[40:43],v[92:95]
rt v[212:213],v242 offset:59136
rt v[214:215],v242 offset:60160
mm v[60:63],v[220:223],a[36:39],v[60:63]
ar v231,a244
v_perm_b32 v232, v231, v230, s86
mm v[60:63],v[216:219],a[52:55],v[60:63]
v_perm_b32 v233, v231, v230, s87
aw a194,v232
)ASM" R"ASM(mm v[28:31],v[216:219],a[48:51],v[28:31]
wt vmcnt(24) lgkmcnt(4)
aw a198,v233
mm v[28:31],v[220:223],a[32:35],v[28:31]
rt v[216:217],v243 offset:50688
rt v[218:219],v243 offset:51712
mm v[0:3],v[192:195],v[128:131],v[0:3]
ar v234,a248
ar v235,a252
mm v[0:3],v[196:199],v[136:139],v[0:3]
ld a[12:15],v245,s[44:47],s55 offen offset:1024 sc1
mm v[32:35],v[196:199],v[140:143],v[32:35]
v_perm_b32 v236, v235, v234, s86
v_perm_b32 v237, v235, v234, s87
mm v[32:35],v[192:195],v[132:135],v[32:35]
rt v[220:221],v243 offset:59136
rt v[222:223],v243 offset:60160
mm v[64:67],v[192:195],v[144:147],v[64:67]
aw a195,v236
aw a199,v237
mm v[64:67],v[196:199],v[152:155],v[64:67]
ld a[16:19],v244,s[44:47],s55 offen offset:2048 sc1
mm v[96:99],v[196:199],v[168:171],v[96:99]
ar v230,a225
ar v231,a229
mm v[96:99],v[192:195],v[156:159],v[96:99]
rt v[196:197],v240 offset:50816
rt v[198:199],v240 offset:51840
mm v[100:103],v[200:203],v[156:159],v[100:103]
v_perm_b32 v232, v231, v230, s86
v_perm_b32 v233, v231, v230, s87
mm v[100:103],v[204:207],v[168:171],v[100:103]
ld a[20:23],v245,s[44:47],s55 offen offset:2048 sc1
mm v[68:71],v[204:207],v[152:155],v[68:71]
aw a200,v232
aw a204,v233
mm v[68:71],v[200:203],v[144:147],v[68:71]
rt v[192:193],v240 offset:59264
rt v[194:195],v240 offset:60288
mm v[36:39],v[200:203],v[132:135],v[36:39]
v_add_u32 v240, 67584, v240
ar v234,a233
mm v[36:39],v[204:207],v[140:143],v[36:39]
ld a[24:27],v244,s[44:47],s55 offen offset:3072 sc1
mm v[4:7],v[204:207],v[136:139],v[4:7]
wt lgkmcnt(4)
ar v235,a237
mm v[4:7],v[200:203],v[128:131],v[4:7]
rt v[204:205],v241 offset:50816
rt v[206:207],v241 offset:51840
mm v[8:11],v[208:211],v[128:131],v[8:11]
v_perm_b32 v236, v235, v234, s86
v_perm_b32 v237, v235, v234, s87
mm v[8:11],v[212:215],v[136:139],v[8:11]
ld a[28:31],v245,s[44:47],s55 offen offset:3072 sc1
mm v[40:43],v[212:215],v[140:143],v[40:43]
aw a201,v236
aw a205,v237
mm v[40:43],v[208:211],v[132:135],v[40:43]
rt v[200:201],v241 offset:59264
rt v[202:203],v241 offset:60288
mm v[72:75],v[208:211],v[144:147],v[72:75]
v_add_u32 v241, 67584, v241
ar v230,a241
mm v[72:75],v[212:215],v[152:155],v[72:75]
ar v231,a245
v_perm_b32 v232, v231, v230, s86
mm v[104:107],v[212:215],v[168:171],v[104:107]
v_perm_b32 v233, v231, v230, s87
aw a202,v232
mm v[104:107],v[208:211],v[156:159],v[104:107]
rt v[212:213],v242 offset:50816
rt v[214:215],v242 offset:51840
mm v[108:111],v[216:219],v[156:159],v[108:111]
aw a206,v233
ar v234,a249
mm v[108:111],v[220:223],v[168:171],v[108:111]
ar v235,a253
v_perm_b32 v236, v235, v234, s86
mm v[76:79],v[220:223],v[152:155],v[76:79]
v_perm_b32 v237, v235, v234, s87
aw a203,v236
mm v[76:79],v[216:219],v[144:147],v[76:79]
rt v[208:209],v242 offset:59264
rt v[210:211],v242 offset:60288
mm v[44:47],v[216:219],v[132:135],v[44:47]
v_add_u32 v242, 67584, v242
aw a207,v237
mm v[44:47],v[220:223],v[140:143],v[44:47]
ar v230,a226
ar v231,a230
mm v[12:15],v[220:223],v[136:139],v[12:15]
wt lgkmcnt(4)
v_perm_b32 v232, v231, v230, s86
mm v[12:15],v[216:219],v[128:131],v[12:15]
rt v[220:221],v243 offset:50816
rt v[222:223],v243 offset:51840
mm v[16:19],v[196:199],v[128:131],v[16:19]
v_perm_b32 v233, v231, v230, s87
aw a208,v232
)ASM" R"ASM(mm v[16:19],v[192:195],v[136:139],v[16:19]
ld a[32:35],v246,s[44:47],s54 offen offset:0 sc1
mm v[48:51],v[192:195],v[140:143],v[48:51]
aw a212,v233
ar v234,a234
mm v[48:51],v[196:199],v[132:135],v[48:51]
rt v[216:217],v243 offset:59264
rt v[218:219],v243 offset:60288
mm v[80:83],v[196:199],v[144:147],v[80:83]
v_add_u32 v243, 67584, v243
ar v235,a238
mm v[80:83],v[192:195],v[152:155],v[80:83]
ld a[36:39],v247,s[44:47],s54 offen offset:0 sc1
mm v[112:115],v[192:195],v[168:171],v[112:115]
wt vmcnt(18)
s_barrier
mm v[112:115],v[196:199],v[156:159],v[112:115]
rt v[192:193],v240 offset:50688
rt v[194:195],v240 offset:51712
mm v[116:119],v[204:207],v[156:159],v[116:119]
v_perm_b32 v236, v235, v234, s86
v_perm_b32 v237, v235, v234, s87
mm v[116:119],v[200:203],v[168:171],v[116:119]
ld a[40:43],v246,s[44:47],s54 offen offset:1024 sc1
mm v[84:87],v[200:203],v[152:155],v[84:87]
aw a209,v236
aw a213,v237
mm v[84:87],v[204:207],v[144:147],v[84:87]
rt v[196:197],v240 offset:59136
rt v[198:199],v240 offset:60160
mm v[52:55],v[204:207],v[132:135],v[52:55]
ar v230,a242
ar v231,a246
mm v[52:55],v[200:203],v[140:143],v[52:55]
ld a[44:47],v247,s[44:47],s54 offen offset:1024 sc1
mm v[20:23],v[200:203],v[136:139],v[20:23]
wt lgkmcnt(4)
v_perm_b32 v232, v231, v230, s86
mm v[20:23],v[204:207],v[128:131],v[20:23]
rt v[200:201],v240 offset:0
rt v[202:203],v240 offset:1024
mm v[24:27],v[212:215],v[128:131],v[24:27]
v_perm_b32 v233, v231, v230, s87
aw a210,v232
mm v[24:27],v[208:211],v[136:139],v[24:27]
ld a[48:51],v246,s[44:47],s54 offen offset:2048 sc1
mm v[56:59],v[208:211],v[140:143],v[56:59]
aw a214,v233
.La5_LW7_4:
s_cbranch_execz .La5_LA_RETURN
ar v234,a250
mm v[56:59],v[212:215],v[132:135],v[56:59]
rt v[204:205],v240 offset:8448
rt v[206:207],v240 offset:9472
mm v[88:91],v[212:215],v[144:147],v[88:91]
ar v235,a254
v_perm_b32 v236, v235, v234, s86
mm v[88:91],v[208:211],v[152:155],v[88:91]
ld a[52:55],v247,s[44:47],s54 offen offset:2048 sc1
mm v[120:123],v[208:211],v[168:171],v[120:123]
v_perm_b32 v237, v235, v234, s87
aw a211,v236
mm v[120:123],v[212:215],v[156:159],v[120:123]
rt v[208:209],v240 offset:16896
rt v[210:211],v240 offset:17920
mm v[124:127],v[220:223],v[156:159],v[124:127]
aw a215,v237
ar v230,a227
mm v[124:127],v[216:219],v[168:171],v[124:127]
ld a[56:59],v246,s[44:47],s54 offen offset:3072 sc1
mm v[92:95],v[216:219],v[152:155],v[92:95]
ar v231,a231
v_perm_b32 v232, v231, v230, s86
mm v[92:95],v[220:223],v[144:147],v[92:95]
rt v[212:213],v240 offset:25344
rt v[214:215],v240 offset:26368
mm v[60:63],v[220:223],v[132:135],v[60:63]
v_perm_b32 v233, v231, v230, s87
aw a216,v232
mm v[60:63],v[216:219],v[140:143],v[60:63]
aw a220,v233
ar v234,a235
mm v[28:31],v[216:219],v[136:139],v[28:31]
wt vmcnt(15) lgkmcnt(4)
ar v235,a239
mm v[28:31],v[220:223],v[128:131],v[28:31]
rt v[216:217],v240 offset:33792
rt v[218:219],v240 offset:34816
mm v[0:3],v[192:195],a[64:67],v[0:3]
ld a[60:63],v247,s[44:47],s54 offen offset:3072 sc1
mm v[0:3],v[196:199],a[72:75],v[0:3]
rt v[220:221],v240 offset:42240
rt v[222:223],v240 offset:43264
mm v[32:35],v[196:199],a[76:79],v[32:35]
v_perm_b32 v236, v235, v234, s86
v_perm_b32 v237, v235, v234, s87
mm v[32:35],v[192:195],a[68:71],v[32:35]
rt v[196:197],v241 offset:50688
rt v[198:199],v241 offset:51712
mm v[32:35],v[200:203],a[84:87],v[32:35]
)ASM" R"ASM(ld v[128:131],v246,s[44:47],s55 offen offset:0 sc1
mm v[32:35],v[204:207],a[92:95],v[32:35]
rt v[192:193],v241 offset:59136
rt v[194:195],v241 offset:60160
mm v[0:3],v[204:207],a[88:91],v[0:3]
wt vmcnt(9) lgkmcnt(4)
aw a217,v236
mm v[0:3],v[200:203],a[80:83],v[0:3]
rt v[204:205],v241 offset:0
rt v[206:207],v241 offset:1024
mm v[0:3],v[208:211],a[0:3],v[0:3]
ld v[132:135],v247,s[44:47],s55 offen offset:0 sc1
mm v[0:3],v[212:215],a[8:11],v[0:3]
rt v[200:201],v241 offset:8448
rt v[202:203],v241 offset:9472
mm v[32:35],v[212:215],a[12:15],v[32:35]
aw a221,v237
ar v230,a243
mm v[32:35],v[208:211],a[4:7],v[32:35]
rt v[212:213],v241 offset:16896
rt v[214:215],v241 offset:17920
mm v[32:35],v[216:219],a[20:23],v[32:35]
ld v[144:147],v246,s[44:47],s55 offen offset:1024 sc1
mm v[32:35],v[220:223],a[28:31],v[32:35]
rt v[208:209],v241 offset:25344
rt v[210:211],v241 offset:26368
mm v[0:3],v[220:223],a[24:27],v[0:3]
wt lgkmcnt(4)
ar v231,a247
mm v[0:3],v[216:219],a[16:19],v[0:3]
rt v[220:221],v241 offset:33792
rt v[222:223],v241 offset:34816
mm v[4:7],v[196:199],a[64:67],v[4:7]
ld v[148:151],v247,s[44:47],s55 offen offset:1024 sc1
mm v[4:7],v[192:195],a[72:75],v[4:7]
rt v[216:217],v241 offset:42240
rt v[218:219],v241 offset:43264
mm v[36:39],v[192:195],a[76:79],v[36:39]
v_perm_b32 v232, v231, v230, s86
v_perm_b32 v233, v231, v230, s87
mm v[36:39],v[196:199],a[68:71],v[36:39]
rt v[192:193],v242 offset:50688
rt v[194:195],v242 offset:51712
mm v[36:39],v[204:207],a[84:87],v[36:39]
ld v[160:163],v246,s[44:47],s55 offen offset:2048 sc1
mm v[36:39],v[200:203],a[92:95],v[36:39]
rt v[196:197],v242 offset:59136
rt v[198:199],v242 offset:60160
aw a218,v232
aw a222,v233
ar v234,a251
ar v235,a255
v_perm_b32 v236, v235, v234, s86
v_perm_b32 v237, v235, v234, s87
aw a219,v236
aw a223,v237
mm v[4:7],v[200:203],a[88:91],v[4:7]
wt lgkmcnt(4)
mm v[4:7],v[204:207],a[80:83],v[4:7]
rt v[200:201],v242 offset:0
rt v[202:203],v242 offset:1024
mm v[4:7],v[212:215],a[0:3],v[4:7]
ld v[164:167],v247,s[44:47],s55 offen offset:2048 sc1
mm v[4:7],v[208:211],a[8:11],v[4:7]
rt v[204:205],v242 offset:8448
rt v[206:207],v242 offset:9472
mm v[36:39],v[208:211],a[12:15],v[36:39]
mm v[36:39],v[212:215],a[4:7],v[36:39]
rt v[208:209],v242 offset:16896
rt v[210:211],v242 offset:17920
mm v[36:39],v[220:223],a[20:23],v[36:39]
mm v[36:39],v[216:219],a[28:31],v[36:39]
rt v[212:213],v242 offset:25344
rt v[214:215],v242 offset:26368
mm v[4:7],v[216:219],a[24:27],v[4:7]
wt lgkmcnt(4)
mm v[4:7],v[220:223],a[16:19],v[4:7]
rt v[216:217],v242 offset:33792
rt v[218:219],v242 offset:34816
mm v[8:11],v[192:195],a[64:67],v[8:11]
ld v[176:179],v246,s[44:47],s55 offen offset:3072 sc1
mm v[8:11],v[196:199],a[72:75],v[8:11]
rt v[220:221],v242 offset:42240
rt v[222:223],v242 offset:43264
mm v[40:43],v[196:199],a[76:79],v[40:43]
cv v234,v32,v33
cv v236,v36,v37
mm v[40:43],v[192:195],a[68:71],v[40:43]
rt v[196:197],v243 offset:50688
rt v[198:199],v243 offset:51712
mm v[40:43],v[200:203],a[84:87],v[40:43]
ld v[180:183],v247,s[44:47],s55 offen offset:3072 sc1
mm v[40:43],v[204:207],a[92:95],v[40:43]
rt v[192:193],v243 offset:59136
rt v[194:195],v243 offset:60160
mm v[8:11],v[204:207],a[88:91],v[8:11]
wt lgkmcnt(4)
v_bfe_u32 v250, v255, 6, 1
mm v[8:11],v[200:203],a[80:83],v[8:11]
rt v[204:205],v243 offset:0
)ASM" R"ASM(rt v[206:207],v243 offset:1024
mm v[8:11],v[208:211],a[0:3],v[8:11]
cv v235,v34,v35
cv v237,v38,v39
mm v[8:11],v[212:215],a[8:11],v[8:11]
rt v[200:201],v243 offset:8448
rt v[202:203],v243 offset:9472
mm v[40:43],v[212:215],a[12:15],v[40:43]
v_permlane16_swap_b32 v234, v236
v_permlane16_swap_b32 v235, v237
mm v[40:43],v[208:211],a[4:7],v[40:43]
rt v[212:213],v243 offset:16896
rt v[214:215],v243 offset:17920
mm v[40:43],v[216:219],a[20:23],v[40:43]
st v[234:237],v238,s[32:35],s95 offen offset:0 sc1
mm v[40:43],v[220:223],a[28:31],v[40:43]
rt v[208:209],v243 offset:25344
rt v[210:211],v243 offset:26368
mm v[8:11],v[220:223],a[24:27],v[8:11]
wt lgkmcnt(4)
v_mul_u32_u24 v250, 7, v250
mm v[8:11],v[216:219],a[16:19],v[8:11]
rt v[220:221],v243 offset:33792
rt v[222:223],v243 offset:34816
mm v[12:15],v[196:199],a[64:67],v[12:15]
cv v230,v0,v1
cv v232,v4,v5
mm v[12:15],v[192:195],a[72:75],v[12:15]
rt v[216:217],v243 offset:42240
rt v[218:219],v243 offset:43264
mm v[44:47],v[192:195],a[76:79],v[44:47]
cv v231,v2,v3
cv v233,v6,v7
mm v[44:47],v[196:199],a[68:71],v[44:47]
rt v[192:193],v240 offset:50816
rt v[194:195],v240 offset:51840
mm v[44:47],v[204:207],a[84:87],v[44:47]
v_permlane16_swap_b32 v230, v232
v_permlane16_swap_b32 v231, v233
mm v[44:47],v[200:203],a[92:95],v[44:47]
rt v[196:197],v240 offset:59264
rt v[198:199],v240 offset:60288
mm v[12:15],v[200:203],a[88:91],v[12:15]
wt lgkmcnt(4)
v_xor_b32 v250, s3, v250
mm v[12:15],v[204:207],a[80:83],v[12:15]
rt v[200:201],v240 offset:128
rt v[202:203],v240 offset:1152
mm v[12:15],v[212:215],a[0:3],v[12:15]
st v[230:233],v238,s[32:35],s94 offen offset:0 sc1
mm v[12:15],v[208:211],a[8:11],v[12:15]
rt v[204:205],v240 offset:8576
rt v[206:207],v240 offset:9600
mm v[44:47],v[208:211],a[12:15],v[44:47]
v_bfe_u32 v251, v255, 4, 2
v_lshl_add_u32 v250, v250, 2, v251
mm v[44:47],v[212:215],a[4:7],v[44:47]
rt v[208:209],v240 offset:17024
rt v[210:211],v240 offset:18048
mm v[44:47],v[220:223],a[20:23],v[44:47]
ld v[136:139],v224,s[68:71],0 offen offset:0
mm v[44:47],v[216:219],a[28:31],v[44:47]
rt v[212:213],v240 offset:25472
rt v[214:215],v240 offset:26496
mm v[12:15],v[216:219],a[24:27],v[12:15]
wt lgkmcnt(4)
v_lshlrev_b32 v250, 9, v250
mm v[12:15],v[220:223],a[16:19],v[12:15]
rt v[216:217],v240 offset:33920
rt v[218:219],v240 offset:34944
mm v[16:19],v[192:195],a[64:67],v[16:19]
cv v234,v40,v41
cv v236,v44,v45
mm v[16:19],v[196:199],a[72:75],v[16:19]
rt v[220:221],v240 offset:42368
rt v[222:223],v240 offset:43392
mm v[48:51],v[196:199],a[76:79],v[48:51]
cv v235,v42,v43
cv v237,v46,v47
mm v[48:51],v[192:195],a[68:71],v[48:51]
rt v[196:197],v241 offset:50816
rt v[198:199],v241 offset:51840
mm v[48:51],v[200:203],a[84:87],v[48:51]
v_permlane16_swap_b32 v234, v236
v_permlane16_swap_b32 v235, v237
mm v[48:51],v[204:207],a[92:95],v[48:51]
rt v[192:193],v241 offset:59264
rt v[194:195],v241 offset:60288
mm v[16:19],v[204:207],a[88:91],v[16:19]
wt lgkmcnt(4)
v_and_b32 v251, 0x2000, v250
mm v[16:19],v[200:203],a[80:83],v[16:19]
rt v[204:205],v241 offset:128
rt v[206:207],v241 offset:1152
mm v[16:19],v[208:211],a[0:3],v[16:19]
st v[234:237],v238,s[32:35],s95 offen offset:1024 sc1
mm v[16:19],v[212:215],a[8:11],v[16:19]
rt v[200:201],v241 offset:8576
rt v[202:203],v241 offset:9600
mm v[48:51],v[212:215],a[12:15],v[48:51]
)ASM" R"ASM(cv v230,v8,v9
cv v232,v12,v13
mm v[48:51],v[208:211],a[4:7],v[48:51]
rt v[212:213],v241 offset:17024
rt v[214:215],v241 offset:18048
mm v[48:51],v[216:219],a[20:23],v[48:51]
cv v231,v10,v11
cv v233,v14,v15
mm v[48:51],v[220:223],a[28:31],v[48:51]
rt v[208:209],v241 offset:25472
rt v[210:211],v241 offset:26496
mm v[16:19],v[220:223],a[24:27],v[16:19]
wt lgkmcnt(4)
v_add_u32 v250, v250, v251
mm v[16:19],v[216:219],a[16:19],v[16:19]
rt v[220:221],v241 offset:33920
rt v[222:223],v241 offset:34944
mm v[20:23],v[196:199],a[64:67],v[20:23]
v_permlane16_swap_b32 v230, v232
v_permlane16_swap_b32 v231, v233
mm v[20:23],v[192:195],a[72:75],v[20:23]
rt v[216:217],v241 offset:42368
rt v[218:219],v241 offset:43392
mm v[52:55],v[192:195],a[76:79],v[52:55]
v_bfe_u32 v251, v255, 7, 1
v_lshl_add_u32 v250, v251, 13, v250
mm v[52:55],v[196:199],a[68:71],v[52:55]
rt v[192:193],v242 offset:50816
rt v[194:195],v242 offset:51840
mm v[52:55],v[204:207],a[84:87],v[52:55]
st v[230:233],v238,s[32:35],s94 offen offset:1024 sc1
mm v[52:55],v[200:203],a[92:95],v[52:55]
rt v[196:197],v242 offset:59264
rt v[198:199],v242 offset:60288
mm v[20:23],v[200:203],a[88:91],v[20:23]
wt lgkmcnt(4)
v_and_b32 v251, 64, v255
mm v[20:23],v[204:207],a[80:83],v[20:23]
rt v[200:201],v242 offset:128
rt v[202:203],v242 offset:1152
mm v[20:23],v[212:215],a[0:3],v[20:23]
ld v[140:143],v225,s[48:51],0 offen offset:0
mm v[20:23],v[208:211],a[8:11],v[20:23]
rt v[204:205],v242 offset:8576
rt v[206:207],v242 offset:9600
mm v[52:55],v[208:211],a[12:15],v[52:55]
v_lshl_add_u32 v251, v251, 24, v250
v_and_b32 v248, 0x1f0, v255
mm v[52:55],v[212:215],a[4:7],v[52:55]
rt v[208:209],v242 offset:17024
rt v[210:211],v242 offset:18048
mm v[52:55],v[220:223],a[20:23],v[52:55]
ld v[152:155],v224,s[68:71],0 offen offset:512
mm v[52:55],v[216:219],a[28:31],v[52:55]
rt v[212:213],v242 offset:25472
rt v[214:215],v242 offset:26496
mm v[20:23],v[216:219],a[24:27],v[20:23]
wt lgkmcnt(4)
s_mov_b32 s93, 0
ld a[224:227],v229,s[28:31],s93 offen offset:0
mm v[20:23],v[220:223],a[16:19],v[20:23]
rt v[216:217],v242 offset:33920
rt v[218:219],v242 offset:34944
mm v[24:27],v[192:195],a[64:67],v[24:27]
cv v234,v48,v49
cv v236,v52,v53
mm v[24:27],v[196:199],a[72:75],v[24:27]
rt v[220:221],v242 offset:42368
rt v[222:223],v242 offset:43392
mm v[56:59],v[196:199],a[76:79],v[56:59]
cv v235,v50,v51
cv v237,v54,v55
mm v[56:59],v[192:195],a[68:71],v[56:59]
rt v[196:197],v243 offset:50816
rt v[198:199],v243 offset:51840
mm v[56:59],v[200:203],a[84:87],v[56:59]
v_permlane16_swap_b32 v234, v236
v_permlane16_swap_b32 v235, v237
mm v[56:59],v[204:207],a[92:95],v[56:59]
rt v[192:193],v243 offset:59264
rt v[194:195],v243 offset:60288
mm v[24:27],v[204:207],a[88:91],v[24:27]
wt lgkmcnt(4)
ld a[228:231],v229,s[28:31],s93 offen offset:2048
mm v[24:27],v[200:203],a[80:83],v[24:27]
rt v[204:205],v243 offset:128
rt v[206:207],v243 offset:1152
mm v[24:27],v[208:211],a[0:3],v[24:27]
st v[234:237],v238,s[32:35],s95 offen offset:2048 sc1
mm v[24:27],v[212:215],a[8:11],v[24:27]
rt v[200:201],v243 offset:8576
rt v[202:203],v243 offset:9600
mm v[56:59],v[212:215],a[12:15],v[56:59]
cv v230,v16,v17
cv v232,v20,v21
mm v[56:59],v[208:211],a[4:7],v[56:59]
rt v[212:213],v243 offset:17024
rt v[214:215],v243 offset:18048
mm v[56:59],v[216:219],a[20:23],v[56:59]
cv v231,v18,v19
)ASM" R"ASM(cv v233,v22,v23
mm v[56:59],v[220:223],a[28:31],v[56:59]
rt v[208:209],v243 offset:25472
rt v[210:211],v243 offset:26496
mm v[24:27],v[220:223],a[24:27],v[24:27]
wt lgkmcnt(4)
s_mov_b32 s93, 4096
ld a[232:235],v229,s[28:31],s93 offen offset:0
mm v[24:27],v[216:219],a[16:19],v[24:27]
rt v[220:221],v243 offset:33920
rt v[222:223],v243 offset:34944
mm v[28:31],v[196:199],a[64:67],v[28:31]
v_permlane16_swap_b32 v230, v232
v_permlane16_swap_b32 v231, v233
mm v[28:31],v[192:195],a[72:75],v[28:31]
rt v[216:217],v243 offset:42368
rt v[218:219],v243 offset:43392
mm v[60:63],v[192:195],a[76:79],v[60:63]
ld a[236:239],v229,s[28:31],s93 offen offset:2048
mm v[60:63],v[196:199],a[68:71],v[60:63]
rt v[192:193],v240 offset:50688
rt v[194:195],v240 offset:51712
mm v[60:63],v[204:207],a[84:87],v[60:63]
st v[230:233],v238,s[32:35],s94 offen offset:2048 sc1
mm v[60:63],v[200:203],a[92:95],v[60:63]
rt v[196:197],v240 offset:59136
rt v[198:199],v240 offset:60160
mm v[28:31],v[200:203],a[88:91],v[28:31]
wt lgkmcnt(4)
s_mov_b32 s93, 8192
ld a[240:243],v229,s[28:31],s93 offen offset:0
mm v[28:31],v[204:207],a[80:83],v[28:31]
rt v[200:201],v240 offset:0
rt v[202:203],v240 offset:1024
mm v[28:31],v[212:215],a[0:3],v[28:31]
ld v[156:159],v225,s[48:51],0 offen offset:512
mm v[28:31],v[208:211],a[8:11],v[28:31]
rt v[204:205],v240 offset:8448
rt v[206:207],v240 offset:9472
mm v[60:63],v[208:211],a[12:15],v[60:63]
ld a[244:247],v229,s[28:31],s93 offen offset:2048
mm v[60:63],v[212:215],a[4:7],v[60:63]
rt v[208:209],v240 offset:16896
rt v[210:211],v240 offset:17920
mm v[60:63],v[220:223],a[20:23],v[60:63]
ld v[168:171],v224,s[68:71],0 offen offset:1024
mm v[60:63],v[216:219],a[28:31],v[60:63]
rt v[212:213],v240 offset:25344
rt v[214:215],v240 offset:26368
mm v[28:31],v[216:219],a[24:27],v[28:31]
wt vmcnt(25) lgkmcnt(4)
s_mov_b32 s93, 12288
ld a[248:251],v229,s[28:31],s93 offen offset:0
mm v[28:31],v[220:223],a[16:19],v[28:31]
rt v[216:217],v240 offset:33792
rt v[218:219],v240 offset:34816
mm v[64:67],v[192:195],a[32:35],v[64:67]
cv v234,v56,v57
cv v236,v60,v61
mm v[64:67],v[196:199],a[40:43],v[64:67]
rt v[220:221],v240 offset:42240
rt v[222:223],v240 offset:43264
mm v[96:99],v[196:199],a[44:47],v[96:99]
cv v235,v58,v59
cv v237,v62,v63
mm v[96:99],v[192:195],a[36:39],v[96:99]
rt v[196:197],v241 offset:50688
rt v[198:199],v241 offset:51712
mm v[96:99],v[200:203],a[52:55],v[96:99]
v_permlane16_swap_b32 v234, v236
v_permlane16_swap_b32 v235, v237
mm v[96:99],v[204:207],a[60:63],v[96:99]
rt v[192:193],v241 offset:59136
rt v[194:195],v241 offset:60160
mm v[64:67],v[204:207],a[56:59],v[64:67]
wt vmcnt(18) lgkmcnt(4)
ld a[252:255],v229,s[28:31],s93 offen offset:2048
mm v[64:67],v[200:203],a[48:51],v[64:67]
rt v[204:205],v241 offset:0
rt v[206:207],v241 offset:1024
mm v[64:67],v[208:211],v[128:131],v[64:67]
st v[234:237],v238,s[32:35],s95 offen offset:3072 sc1
mm v[64:67],v[212:215],v[144:147],v[64:67]
rt v[200:201],v241 offset:8448
rt v[202:203],v241 offset:9472
mm v[96:99],v[212:215],v[148:151],v[96:99]
cv v230,v24,v25
cv v232,v28,v29
mm v[96:99],v[208:211],v[132:135],v[96:99]
rt v[212:213],v241 offset:16896
rt v[214:215],v241 offset:17920
mm v[96:99],v[216:219],v[164:167],v[96:99]
cv v231,v26,v27
cv v233,v30,v31
mm v[96:99],v[220:223],v[180:183],v[96:99]
rt v[208:209],v241 offset:25344
rt v[210:211],v241 offset:26368
mm v[64:67],v[220:223],v[176:179],v[64:67]
wt lgkmcnt(4)
s_mov_b32 s93, 0
)ASM" R"ASM(ld a[64:67],v229,s[28:31],s93 offen offset:256
mm v[64:67],v[216:219],v[160:163],v[64:67]
rt v[220:221],v241 offset:33792
rt v[222:223],v241 offset:34816
mm v[68:71],v[196:199],a[32:35],v[68:71]
v_permlane16_swap_b32 v230, v232
v_permlane16_swap_b32 v231, v233
mm v[68:71],v[192:195],a[40:43],v[68:71]
rt v[216:217],v241 offset:42240
rt v[218:219],v241 offset:43264
mm v[100:103],v[192:195],a[44:47],v[100:103]
ld a[68:71],v229,s[28:31],s93 offen offset:2304
mm v[100:103],v[196:199],a[36:39],v[100:103]
rt v[192:193],v242 offset:50688
rt v[194:195],v242 offset:51712
mm v[100:103],v[204:207],a[52:55],v[100:103]
st v[230:233],v238,s[32:35],s94 offen offset:3072 sc1
mm v[100:103],v[200:203],a[60:63],v[100:103]
rt v[196:197],v242 offset:59136
rt v[198:199],v242 offset:60160
mm v[68:71],v[200:203],a[56:59],v[68:71]
wt lgkmcnt(4)
s_mov_b32 s93, 4096
ld a[72:75],v229,s[28:31],s93 offen offset:256
mm v[68:71],v[204:207],a[48:51],v[68:71]
rt v[200:201],v242 offset:0
rt v[202:203],v242 offset:1024
mm v[68:71],v[212:215],v[128:131],v[68:71]
ld v[172:175],v225,s[48:51],0 offen offset:1024
mm v[68:71],v[208:211],v[144:147],v[68:71]
rt v[204:205],v242 offset:8448
rt v[206:207],v242 offset:9472
mm v[100:103],v[208:211],v[148:151],v[100:103]
ld a[76:79],v229,s[28:31],s93 offen offset:2304
mm v[100:103],v[212:215],v[132:135],v[100:103]
rt v[208:209],v242 offset:16896
rt v[210:211],v242 offset:17920
mm v[100:103],v[220:223],v[164:167],v[100:103]
ld v[184:187],v224,s[68:71],0 offen offset:1536
mm v[100:103],v[216:219],v[180:183],v[100:103]
rt v[212:213],v242 offset:25344
rt v[214:215],v242 offset:26368
mm v[68:71],v[216:219],v[176:179],v[68:71]
wt lgkmcnt(4)
s_mov_b32 s93, 8192
ld a[80:83],v229,s[28:31],s93 offen offset:256
mm v[68:71],v[220:223],v[160:163],v[68:71]
rt v[216:217],v242 offset:33792
rt v[218:219],v242 offset:34816
mm v[72:75],v[192:195],a[32:35],v[72:75]
cv v234,v96,v97
cv v236,v100,v101
mm v[72:75],v[196:199],a[40:43],v[72:75]
rt v[220:221],v242 offset:42240
rt v[222:223],v242 offset:43264
mm v[104:107],v[196:199],a[44:47],v[104:107]
cv v235,v98,v99
cv v237,v102,v103
mm v[104:107],v[192:195],a[36:39],v[104:107]
rt v[196:197],v243 offset:50688
rt v[198:199],v243 offset:51712
mm v[104:107],v[200:203],a[52:55],v[104:107]
v_permlane16_swap_b32 v234, v236
v_permlane16_swap_b32 v235, v237
mm v[104:107],v[204:207],a[60:63],v[104:107]
rt v[192:193],v243 offset:59136
rt v[194:195],v243 offset:60160
mm v[72:75],v[204:207],a[56:59],v[72:75]
wt lgkmcnt(4)
ld a[84:87],v229,s[28:31],s93 offen offset:2304
mm v[72:75],v[200:203],a[48:51],v[72:75]
rt v[204:205],v243 offset:0
rt v[206:207],v243 offset:1024
mm v[72:75],v[208:211],v[128:131],v[72:75]
st v[234:237],v238,s[32:35],s97 offen offset:0 sc1
mm v[72:75],v[212:215],v[144:147],v[72:75]
rt v[200:201],v243 offset:8448
rt v[202:203],v243 offset:9472
mm v[104:107],v[212:215],v[148:151],v[104:107]
cv v230,v64,v65
cv v232,v68,v69
mm v[104:107],v[208:211],v[132:135],v[104:107]
rt v[212:213],v243 offset:16896
rt v[214:215],v243 offset:17920
mm v[104:107],v[216:219],v[164:167],v[104:107]
.La5_LW7_5:
s_cbranch_execz .La5_LA_RETURN
cv v231,v66,v67
cv v233,v70,v71
mm v[104:107],v[220:223],v[180:183],v[104:107]
rt v[208:209],v243 offset:25344
rt v[210:211],v243 offset:26368
mm v[72:75],v[220:223],v[176:179],v[72:75]
wt lgkmcnt(4)
s_mov_b32 s93, 12288
ld a[88:91],v229,s[28:31],s93 offen offset:256
mm v[72:75],v[216:219],v[160:163],v[72:75]
rt v[220:221],v243 offset:33792
rt v[222:223],v243 offset:34816
mm v[76:79],v[196:199],a[32:35],v[76:79]
)ASM" R"ASM(v_permlane16_swap_b32 v230, v232
v_permlane16_swap_b32 v231, v233
mm v[76:79],v[192:195],a[40:43],v[76:79]
rt v[216:217],v243 offset:42240
rt v[218:219],v243 offset:43264
mm v[108:111],v[192:195],a[44:47],v[108:111]
ld a[92:95],v229,s[28:31],s93 offen offset:2304
mm v[108:111],v[196:199],a[36:39],v[108:111]
rt v[192:193],v240 offset:50816
rt v[194:195],v240 offset:51840
mm v[108:111],v[204:207],a[52:55],v[108:111]
st v[230:233],v238,s[32:35],s96 offen offset:0 sc1
mm v[108:111],v[200:203],a[60:63],v[108:111]
rt v[196:197],v240 offset:59264
rt v[198:199],v240 offset:60288
mm v[76:79],v[200:203],a[56:59],v[76:79]
wt lgkmcnt(4)
v_lshrrev_b32 v248, 1, v248
mm v[76:79],v[204:207],a[48:51],v[76:79]
rt v[200:201],v240 offset:128
rt v[202:203],v240 offset:1152
mm v[76:79],v[212:215],v[128:131],v[76:79]
ld v[188:191],v225,s[48:51],0 offen offset:1536
mm v[76:79],v[208:211],v[144:147],v[76:79]
rt v[204:205],v240 offset:8576
rt v[206:207],v240 offset:9600
mm v[108:111],v[208:211],v[148:151],v[108:111]
mm v[108:111],v[212:215],v[132:135],v[108:111]
rt v[208:209],v240 offset:17024
rt v[210:211],v240 offset:18048
mm v[108:111],v[220:223],v[164:167],v[108:111]
mm v[108:111],v[216:219],v[180:183],v[108:111]
rt v[212:213],v240 offset:25472
rt v[214:215],v240 offset:26496
mm v[76:79],v[216:219],v[176:179],v[76:79]
wt lgkmcnt(4)
mm v[76:79],v[220:223],v[160:163],v[76:79]
rt v[216:217],v240 offset:33920
rt v[218:219],v240 offset:34944
mm v[80:83],v[192:195],a[32:35],v[80:83]
cv v234,v104,v105
cv v236,v108,v109
mm v[80:83],v[196:199],a[40:43],v[80:83]
rt v[220:221],v240 offset:42368
rt v[222:223],v240 offset:43392
mm v[112:115],v[196:199],a[44:47],v[112:115]
cv v235,v106,v107
cv v237,v110,v111
mm v[112:115],v[192:195],a[36:39],v[112:115]
rt v[196:197],v241 offset:50816
rt v[198:199],v241 offset:51840
mm v[112:115],v[200:203],a[52:55],v[112:115]
v_permlane16_swap_b32 v234, v236
v_permlane16_swap_b32 v235, v237
mm v[112:115],v[204:207],a[60:63],v[112:115]
rt v[192:193],v241 offset:59264
rt v[194:195],v241 offset:60288
mm v[80:83],v[204:207],a[56:59],v[80:83]
wt lgkmcnt(4)
mm v[80:83],v[200:203],a[48:51],v[80:83]
rt v[204:205],v241 offset:128
rt v[206:207],v241 offset:1152
mm v[80:83],v[208:211],v[128:131],v[80:83]
st v[234:237],v238,s[32:35],s97 offen offset:1024 sc1
mm v[80:83],v[212:215],v[144:147],v[80:83]
rt v[200:201],v241 offset:8576
rt v[202:203],v241 offset:9600
mm v[112:115],v[212:215],v[148:151],v[112:115]
cv v230,v72,v73
cv v232,v76,v77
mm v[112:115],v[208:211],v[132:135],v[112:115]
rt v[212:213],v241 offset:17024
rt v[214:215],v241 offset:18048
mm v[112:115],v[216:219],v[164:167],v[112:115]
cv v231,v74,v75
cv v233,v78,v79
mm v[112:115],v[220:223],v[180:183],v[112:115]
rt v[208:209],v241 offset:25472
rt v[210:211],v241 offset:26496
mm v[80:83],v[220:223],v[176:179],v[80:83]
wt lgkmcnt(4)
mm v[80:83],v[216:219],v[160:163],v[80:83]
rt v[220:221],v241 offset:33920
rt v[222:223],v241 offset:34944
mm v[84:87],v[196:199],a[32:35],v[84:87]
v_permlane16_swap_b32 v230, v232
v_permlane16_swap_b32 v231, v233
mm v[84:87],v[192:195],a[40:43],v[84:87]
rt v[216:217],v241 offset:42368
rt v[218:219],v241 offset:43392
mm v[116:119],v[192:195],a[44:47],v[116:119]
mm v[116:119],v[196:199],a[36:39],v[116:119]
rt v[192:193],v242 offset:50816
rt v[194:195],v242 offset:51840
mm v[116:119],v[204:207],a[52:55],v[116:119]
st v[230:233],v238,s[32:35],s96 offen offset:1024 sc1
mm v[116:119],v[200:203],a[60:63],v[116:119]
rt v[196:197],v242 offset:59264
rt v[198:199],v242 offset:60288
mm v[84:87],v[200:203],a[56:59],v[84:87]
)ASM" R"ASM(wt lgkmcnt(4)
mm v[84:87],v[204:207],a[48:51],v[84:87]
rt v[200:201],v242 offset:128
rt v[202:203],v242 offset:1152
mm v[84:87],v[212:215],v[128:131],v[84:87]
mm v[84:87],v[208:211],v[144:147],v[84:87]
rt v[204:205],v242 offset:8576
rt v[206:207],v242 offset:9600
mm v[116:119],v[208:211],v[148:151],v[116:119]
mm v[116:119],v[212:215],v[132:135],v[116:119]
rt v[208:209],v242 offset:17024
rt v[210:211],v242 offset:18048
mm v[116:119],v[220:223],v[164:167],v[116:119]
mm v[116:119],v[216:219],v[180:183],v[116:119]
rt v[212:213],v242 offset:25472
rt v[214:215],v242 offset:26496
mm v[84:87],v[216:219],v[176:179],v[84:87]
wt lgkmcnt(4)
mm v[84:87],v[220:223],v[160:163],v[84:87]
rt v[216:217],v242 offset:33920
rt v[218:219],v242 offset:34944
mm v[88:91],v[192:195],a[32:35],v[88:91]
cv v234,v112,v113
cv v236,v116,v117
mm v[88:91],v[196:199],a[40:43],v[88:91]
rt v[220:221],v242 offset:42368
rt v[222:223],v242 offset:43392
mm v[120:123],v[196:199],a[44:47],v[120:123]
cv v235,v114,v115
cv v237,v118,v119
mm v[120:123],v[192:195],a[36:39],v[120:123]
rt v[196:197],v243 offset:50816
rt v[198:199],v243 offset:51840
mm v[120:123],v[200:203],a[52:55],v[120:123]
v_permlane16_swap_b32 v234, v236
v_permlane16_swap_b32 v235, v237
mm v[120:123],v[204:207],a[60:63],v[120:123]
rt v[192:193],v243 offset:59264
rt v[194:195],v243 offset:60288
mm v[88:91],v[204:207],a[56:59],v[88:91]
wt lgkmcnt(4)
mm v[88:91],v[200:203],a[48:51],v[88:91]
rt v[204:205],v243 offset:128
rt v[206:207],v243 offset:1152
mm v[88:91],v[208:211],v[128:131],v[88:91]
st v[234:237],v238,s[32:35],s97 offen offset:2048 sc1
mm v[88:91],v[212:215],v[144:147],v[88:91]
rt v[200:201],v243 offset:8576
rt v[202:203],v243 offset:9600
mm v[120:123],v[212:215],v[148:151],v[120:123]
cv v230,v80,v81
cv v232,v84,v85
mm v[120:123],v[208:211],v[132:135],v[120:123]
rt v[212:213],v243 offset:17024
rt v[214:215],v243 offset:18048
mm v[120:123],v[216:219],v[164:167],v[120:123]
cv v231,v82,v83
cv v233,v86,v87
wt vmcnt(14)
buffer_store_dwordx2 v[252:253], v251, s[64:67], s92 offen sc1
mm v[120:123],v[220:223],v[180:183],v[120:123]
rt v[208:209],v243 offset:25472
rt v[210:211],v243 offset:26496
mm v[88:91],v[220:223],v[176:179],v[88:91]
wt lgkmcnt(4)
mm v[88:91],v[216:219],v[160:163],v[88:91]
rt v[220:221],v243 offset:33920
rt v[222:223],v243 offset:34944
mm v[92:95],v[196:199],a[32:35],v[92:95]
v_permlane16_swap_b32 v230, v232
v_permlane16_swap_b32 v231, v233
mm v[92:95],v[192:195],a[40:43],v[92:95]
rt v[216:217],v243 offset:42368
rt v[218:219],v243 offset:43392
mm v[124:127],v[192:195],a[44:47],v[124:127]
mm v[124:127],v[196:199],a[36:39],v[124:127]
mm v[124:127],v[204:207],a[52:55],v[124:127]
st v[230:233],v238,s[32:35],s96 offen offset:2048 sc1
mm v[124:127],v[200:203],a[60:63],v[124:127]
mm v[92:95],v[200:203],a[56:59],v[92:95]
wt lgkmcnt(0)
mm v[92:95],v[204:207],a[48:51],v[92:95]
mm v[92:95],v[212:215],v[128:131],v[92:95]
mm v[92:95],v[208:211],v[144:147],v[92:95]
mm v[124:127],v[208:211],v[148:151],v[124:127]
mm v[124:127],v[212:215],v[132:135],v[124:127]
mm v[124:127],v[220:223],v[164:167],v[124:127]
mm v[124:127],v[216:219],v[180:183],v[124:127]
mm v[92:95],v[216:219],v[176:179],v[92:95]
mm v[92:95],v[220:223],v[160:163],v[92:95]
s_nop 7
cv v234,v120,v121
cv v236,v124,v125
cv v235,v122,v123
cv v237,v126,v127
cv v230,v88,v89
cv v232,v92,v93
cv v231,v90,v91
cv v233,v94,v95
v_permlane16_swap_b32 v234, v236
)ASM" R"ASM(v_permlane16_swap_b32 v235, v237
st v[234:237],v238,s[32:35],s97 offen offset:3072 sc1
v_permlane16_swap_b32 v230, v232
v_permlane16_swap_b32 v231, v233
st v[230:233],v238,s[32:35],s96 offen offset:3072 sc1
.La5_LT_0:
.La5_LW3_5:
s_cbranch_execz .La5_LA_RETURN
.La5_LW7_6:
s_cbranch_execz .La5_LA_RETURN
s_lshl_b32 s91, s76, 5
s_add_u32 s91, s91, s90
s_lshl_b32 s20, s91, 9
v_mov_b32 v236, 157440
ds_read_b32 v236, v236
s_andn2_b32 s93, s20, 0x1fff
s_add_u32 s20, s20, s93
s_add_u32 s20, s20, 2097152
s_lshr_b32 s93, s91, 1
s_lshl_b32 s93, s93, 15
s_and_b32 s94, s91, 1
s_lshl2_add_u32 s94, s94, s3
s_lshl_b32 s94, s94, 7
s_add_u32 s97, s93, s94
s_add_u32 s32, s26, 71303168
s_addc_u32 s33, s27, 0
s_and_b32 s33, s33, 0xffff
s_mov_b32 s34, 16777216
s_mov_b32 s35, 0x20000
s_add_u32 s44, s26, 171966464
s_addc_u32 s45, s27, 0
s_and_b32 s45, s45, 0xffff
s_mov_b32 s46, 2097152
s_mov_b32 s47, 0x20000
s_add_u32 s56, s38, 0
s_addc_u32 s57, s39, 0
s_and_b32 s57, s57, 0xffff
s_mov_b32 s58, 4194304
s_mov_b32 s59, 0x20000
s_add_u32 s68, s16, 0
s_addc_u32 s69, s17, 0
s_and_b32 s69, s69, 0xffff
s_mov_b32 s70, 4194304
s_mov_b32 s71, 0x20000
v_and_b32 v230, 0x70, v255
v_lshrrev_b32 v231, 7, v255
v_lshl_add_u32 v230, v231, 10, v230
v_add_u32 v240, s97, v230
v_xor_b32 v241, 8192, v240
v_xor_b32 v242, 16384, v240
v_xor_b32 v243, 24576, v240
wt lgkmcnt(0)
v_readfirstlane_b32 s93, v236
s_nop 0
s_and_b32 s93, s93, 8192
s_add_u32 s20, s20, s93
v_mov_b32 v249, v250
buffer_load_dwordx2 v[250:251], v248, s[64:67], s20 offen sc1
v_mov_b32 v237, 0x3f803f80
v_mov_b32 v236, 0
wt vmcnt(8)
v_dot2_f32_bf16 v236, v140, v237, v236
v_dot2_f32_bf16 v236, v141, v237, v236
v_dot2_f32_bf16 v236, v142, v237, v236
v_dot2_f32_bf16 v236, v143, v237, v236
v_dot2_f32_bf16 v236, v156, v237, v236
v_dot2_f32_bf16 v236, v157, v237, v236
v_dot2_f32_bf16 v236, v158, v237, v236
v_dot2_f32_bf16 v236, v159, v237, v236
v_dot2_f32_bf16 v236, v172, v237, v236
v_dot2_f32_bf16 v236, v173, v237, v236
v_dot2_f32_bf16 v236, v174, v237, v236
v_dot2_f32_bf16 v236, v175, v237, v236
v_dot2_f32_bf16 v236, v188, v237, v236
v_dot2_f32_bf16 v236, v189, v237, v236
v_dot2_f32_bf16 v236, v190, v237, v236
v_dot2_f32_bf16 v236, v191, v237, v236
v_lshlrev_b32 v0, 16, v136
v_and_b32 v1, 0xffff0000, v136
v_lshlrev_b32 v2, 16, v137
v_and_b32 v3, 0xffff0000, v137
v_lshlrev_b32 v4, 16, v138
v_and_b32 v5, 0xffff0000, v138
v_lshlrev_b32 v6, 16, v139
v_and_b32 v7, 0xffff0000, v139
v_lshlrev_b32 v8, 16, v152
v_and_b32 v9, 0xffff0000, v152
v_lshlrev_b32 v10, 16, v153
v_and_b32 v11, 0xffff0000, v153
v_lshlrev_b32 v12, 16, v154
v_and_b32 v13, 0xffff0000, v154
v_lshlrev_b32 v14, 16, v155
v_and_b32 v15, 0xffff0000, v155
v_lshlrev_b32 v16, 16, v168
v_and_b32 v17, 0xffff0000, v168
v_lshlrev_b32 v18, 16, v169
v_and_b32 v19, 0xffff0000, v169
v_lshlrev_b32 v20, 16, v170
v_and_b32 v21, 0xffff0000, v170
v_lshlrev_b32 v22, 16, v171
)ASM" R"ASM(v_and_b32 v23, 0xffff0000, v171
v_lshlrev_b32 v24, 16, v184
v_and_b32 v25, 0xffff0000, v184
v_lshlrev_b32 v26, 16, v185
v_and_b32 v27, 0xffff0000, v185
v_lshlrev_b32 v28, 16, v186
v_and_b32 v29, 0xffff0000, v186
v_lshlrev_b32 v30, 16, v187
v_and_b32 v31, 0xffff0000, v187
s_nop 1
v_add_f32_dpp v236, v236, v236 quad_perm:[1,0,3,2] row_mask:0xf bank_mask:0xf
s_nop 1
v_add_f32_dpp v236, v236, v236 quad_perm:[2,3,0,1] row_mask:0xf bank_mask:0xf
s_nop 1
v_add_f32_dpp v236, v236, v236 row_ror:8 row_mask:0xf bank_mask:0xf
v_mov_b32 v230, v236
s_nop 1
v_permlane16_swap_b32 v230, v236
s_nop 1
v_add_f32 v236, v236, v230
v_mov_b32 v230, v236
s_nop 1
v_permlane32_swap_b32 v230, v236
s_nop 1
v_add_f32 v236, v236, v230
s_mov_b64 exec, 0
s_cmp_eq_u32 s3, 0
s_cbranch_scc1 .La5_LW4_0
s_cmp_eq_u32 s3, 1
s_cbranch_scc1 .La5_LW4_1
s_cmp_eq_u32 s3, 2
s_cbranch_scc1 .La5_LW4_2
s_branch .La5_LW4_3
.La5_LW4_R0:
s_mov_b64 exec, -1
wt vmcnt(1)
buffer_store_dwordx2 v[252:253], v249, s[64:67], s92 offen sc1
s_mov_b32 s96, 0
wt vmcnt(1)
s_branch .La5_LT_P2
; ---- h = x + attn, LayerNorm statistics, router score, two best of eight rows
.La5_LT_ROWS:
s_mov_b32 s96, 0
.La5_LT_P3:
buffer_load_dwordx2 v[250:251], v248, s[64:67], s20 offen sc1
wt vmcnt(0)
.La5_LT_P2:
v_cmp_eq_u64 vcc, v[250:251], v[252:253]
s_add_u32 s96, s96, 1
s_lshr_b32 s92, vcc_lo, 1
s_and_b32 s92, s92, vcc_lo
s_lshr_b32 s93, s92, 2
s_and_b32 s92, s92, s93
s_lshr_b32 s93, s92, 4
s_and_b32 s92, s92, s93
s_lshr_b32 s93, s96, 20
s_mul_i32 s93, s93, 0x01010101
s_or_b32 s92, s92, s93
s_andn2_b32 s92, 0x01010101, s92
s_bcnt1_i32_b32 s93, s92
s_cmp_gt_u32 s93, 1
s_cbranch_scc1 .La5_LT_P3S
s_or_b32 s93, s92, 0x01000000
s_ff1_i32_b32 s93, s93
s_lshl_b32 s80, s93, 19
s_xor_b32 s93, s80, 8388608
s_xor_b32 s94, s80, 12582912
s_xor_b32 s95, s80, 4194304
ld v[208:211],v240,s[32:35],s93 offen sc1
ld v[212:215],v241,s[32:35],s93 offen sc1
ld v[216:219],v242,s[32:35],s93 offen sc1
ld v[220:223],v243,s[32:35],s93 offen sc1
ld v[112:115],v240,s[32:35],s94 offen sc1
ld v[116:119],v241,s[32:35],s94 offen sc1
ld v[120:123],v242,s[32:35],s94 offen sc1
ld v[124:127],v243,s[32:35],s94 offen sc1
ld v[96:99],v240,s[32:35],s95 offen sc1
ld v[100:103],v241,s[32:35],s95 offen sc1
ld v[104:107],v242,s[32:35],s95 offen sc1
ld v[108:111],v243,s[32:35],s95 offen sc1
s_cmp_lg_u32 s92, 0
s_cbranch_scc1 .La5_LT_P4
ld v[192:195],v240,s[32:35],s80 offen sc1
ld v[196:199],v241,s[32:35],s80 offen sc1
ld v[200:203],v242,s[32:35],s80 offen sc1
ld v[204:207],v243,s[32:35],s80 offen sc1
.La5_LT_P4:
s_lshr_b32 s93, s80, 16
v_and_b32 v248, 56, v248
s_add_u32 s20, s20, s93
buffer_load_dwordx2 v[250:251], v248, s[64:67], s20 offen sc1
wt vmcnt(8)
v_lshlrev_b32 v32, 16, v208
v_and_b32 v33, 0xffff0000, v208
v_lshlrev_b32 v230, 16, v112
v_and_b32 v231, 0xffff0000, v112
v_pk_add_f32 v[32:33], v[32:33], v[230:231]
v_lshlrev_b32 v34, 16, v209
v_and_b32 v35, 0xffff0000, v209
v_lshlrev_b32 v230, 16, v113
)ASM" R"ASM(v_and_b32 v231, 0xffff0000, v113
v_pk_add_f32 v[34:35], v[34:35], v[230:231]
v_lshlrev_b32 v36, 16, v210
v_and_b32 v37, 0xffff0000, v210
v_lshlrev_b32 v230, 16, v114
v_and_b32 v231, 0xffff0000, v114
v_pk_add_f32 v[36:37], v[36:37], v[230:231]
v_lshlrev_b32 v38, 16, v211
v_and_b32 v39, 0xffff0000, v211
v_lshlrev_b32 v230, 16, v115
v_and_b32 v231, 0xffff0000, v115
v_pk_add_f32 v[38:39], v[38:39], v[230:231]
wt vmcnt(7)
v_lshlrev_b32 v40, 16, v212
v_and_b32 v41, 0xffff0000, v212
v_lshlrev_b32 v230, 16, v116
v_and_b32 v231, 0xffff0000, v116
v_pk_add_f32 v[40:41], v[40:41], v[230:231]
v_lshlrev_b32 v42, 16, v213
v_and_b32 v43, 0xffff0000, v213
v_lshlrev_b32 v230, 16, v117
v_and_b32 v231, 0xffff0000, v117
v_pk_add_f32 v[42:43], v[42:43], v[230:231]
v_lshlrev_b32 v44, 16, v214
v_and_b32 v45, 0xffff0000, v214
v_lshlrev_b32 v230, 16, v118
v_and_b32 v231, 0xffff0000, v118
v_pk_add_f32 v[44:45], v[44:45], v[230:231]
v_lshlrev_b32 v46, 16, v215
v_and_b32 v47, 0xffff0000, v215
v_lshlrev_b32 v230, 16, v119
v_and_b32 v231, 0xffff0000, v119
v_pk_add_f32 v[46:47], v[46:47], v[230:231]
wt vmcnt(6)
v_lshlrev_b32 v48, 16, v216
v_and_b32 v49, 0xffff0000, v216
v_lshlrev_b32 v230, 16, v120
v_and_b32 v231, 0xffff0000, v120
v_pk_add_f32 v[48:49], v[48:49], v[230:231]
v_lshlrev_b32 v50, 16, v217
v_and_b32 v51, 0xffff0000, v217
v_lshlrev_b32 v230, 16, v121
v_and_b32 v231, 0xffff0000, v121
v_pk_add_f32 v[50:51], v[50:51], v[230:231]
v_lshlrev_b32 v52, 16, v218
v_and_b32 v53, 0xffff0000, v218
v_lshlrev_b32 v230, 16, v122
v_and_b32 v231, 0xffff0000, v122
v_pk_add_f32 v[52:53], v[52:53], v[230:231]
v_lshlrev_b32 v54, 16, v219
v_and_b32 v55, 0xffff0000, v219
v_lshlrev_b32 v230, 16, v123
v_and_b32 v231, 0xffff0000, v123
v_pk_add_f32 v[54:55], v[54:55], v[230:231]
wt vmcnt(5)
v_lshlrev_b32 v56, 16, v220
v_and_b32 v57, 0xffff0000, v220
v_lshlrev_b32 v230, 16, v124
v_and_b32 v231, 0xffff0000, v124
v_pk_add_f32 v[56:57], v[56:57], v[230:231]
v_lshlrev_b32 v58, 16, v221
v_and_b32 v59, 0xffff0000, v221
v_lshlrev_b32 v230, 16, v125
v_and_b32 v231, 0xffff0000, v125
v_pk_add_f32 v[58:59], v[58:59], v[230:231]
v_lshlrev_b32 v60, 16, v222
v_and_b32 v61, 0xffff0000, v222
v_lshlrev_b32 v230, 16, v126
v_and_b32 v231, 0xffff0000, v126
v_pk_add_f32 v[60:61], v[60:61], v[230:231]
v_lshlrev_b32 v62, 16, v223
v_and_b32 v63, 0xffff0000, v223
v_lshlrev_b32 v230, 16, v127
v_and_b32 v231, 0xffff0000, v127
v_pk_add_f32 v[62:63], v[62:63], v[230:231]
.La5_LT_PH:
wt vmcnt(1)
v_lshlrev_b32 v64, 16, v96
v_and_b32 v65, 0xffff0000, v96
v_lshlrev_b32 v66, 16, v97
v_and_b32 v67, 0xffff0000, v97
v_lshlrev_b32 v68, 16, v98
v_and_b32 v69, 0xffff0000, v98
v_lshlrev_b32 v70, 16, v99
v_and_b32 v71, 0xffff0000, v99
v_lshlrev_b32 v72, 16, v100
v_and_b32 v73, 0xffff0000, v100
v_lshlrev_b32 v74, 16, v101
v_and_b32 v75, 0xffff0000, v101
v_lshlrev_b32 v76, 16, v102
v_and_b32 v77, 0xffff0000, v102
v_lshlrev_b32 v78, 16, v103
v_and_b32 v79, 0xffff0000, v103
v_lshlrev_b32 v80, 16, v104
v_and_b32 v81, 0xffff0000, v104
v_lshlrev_b32 v82, 16, v105
v_and_b32 v83, 0xffff0000, v105
v_lshlrev_b32 v84, 16, v106
v_and_b32 v85, 0xffff0000, v106
v_lshlrev_b32 v86, 16, v107
)ASM" R"ASM(v_and_b32 v87, 0xffff0000, v107
v_lshlrev_b32 v88, 16, v108
v_and_b32 v89, 0xffff0000, v108
v_lshlrev_b32 v90, 16, v109
v_and_b32 v91, 0xffff0000, v109
v_lshlrev_b32 v92, 16, v110
v_and_b32 v93, 0xffff0000, v110
v_lshlrev_b32 v94, 16, v111
v_and_b32 v95, 0xffff0000, v111
v_mov_b32 v232, 0
v_mov_b32 v233, 0
v_mov_b32 v234, 0
s_cmp_eq_u32 s92, 0
s_cbranch_scc1 .La5_LT_SUM
.La5_LT_P5:
wt vmcnt(0)
v_cmp_eq_u64 vcc, v[250:251], v[252:253]
s_add_u32 s96, s96, 1
s_cmp_eq_u64 vcc, -1
s_cbranch_scc1 .La5_LT_P6
s_lshr_b32 s93, s96, 20
s_cbranch_scc1 .La5_LT_P6
s_sleep 3
buffer_load_dwordx2 v[250:251], v248, s[64:67], s20 offen sc1
s_branch .La5_LT_P5
.La5_LT_P3S:
s_sleep 4
s_bcnt0_i32_b32 s93, vcc_lo
s_cmp_le_u32 s93, 8
s_cbranch_scc1 .La5_LT_P3
s_sleep 24
s_branch .La5_LT_P3
.La5_LT_P6:
ld v[192:195],v240,s[32:35],s80 offen sc1
ld v[196:199],v241,s[32:35],s80 offen sc1
ld v[200:203],v242,s[32:35],s80 offen sc1
ld v[204:207],v243,s[32:35],s80 offen sc1
.La5_LT_SUM:
wt vmcnt(3)
v_lshlrev_b32 v208, 16, v192
v_and_b32 v209, 0xffff0000, v192
v_lshlrev_b32 v210, 16, v193
v_and_b32 v211, 0xffff0000, v193
v_lshlrev_b32 v212, 16, v194
v_and_b32 v213, 0xffff0000, v194
v_lshlrev_b32 v214, 16, v195
v_and_b32 v215, 0xffff0000, v195
v_pk_add_f32 v[208:209], v[208:209], v[64:65]
v_pk_add_f32 v[210:211], v[210:211], v[66:67]
v_pk_add_f32 v[212:213], v[212:213], v[68:69]
v_pk_add_f32 v[214:215], v[214:215], v[70:71]
v_pk_add_f32 v[208:209], v[208:209], v[32:33]
v_pk_add_f32 v[210:211], v[210:211], v[34:35]
v_pk_add_f32 v[212:213], v[212:213], v[36:37]
v_pk_add_f32 v[214:215], v[214:215], v[38:39]
v_pk_add_f32 v[208:209], v[208:209], v[0:1]
v_pk_add_f32 v[210:211], v[210:211], v[2:3]
v_pk_add_f32 v[212:213], v[212:213], v[4:5]
v_pk_add_f32 v[214:215], v[214:215], v[6:7]
cv v96,v208,v209
cv v97,v210,v211
cv v98,v212,v213
cv v99,v214,v215
v_dot2_f32_bf16 v232, v96, v237, v232
v_dot2_f32_bf16 v233, v96, v96, v233
v_dot2_f32_bf16 v234, v96, v140, v234
v_dot2_f32_bf16 v232, v97, v237, v232
v_dot2_f32_bf16 v233, v97, v97, v233
v_dot2_f32_bf16 v234, v97, v141, v234
v_dot2_f32_bf16 v232, v98, v237, v232
v_dot2_f32_bf16 v233, v98, v98, v233
v_dot2_f32_bf16 v234, v98, v142, v234
v_dot2_f32_bf16 v232, v99, v237, v232
v_dot2_f32_bf16 v233, v99, v99, v233
v_dot2_f32_bf16 v234, v99, v143, v234
wt vmcnt(2)
v_lshlrev_b32 v208, 16, v196
v_and_b32 v209, 0xffff0000, v196
v_lshlrev_b32 v210, 16, v197
v_and_b32 v211, 0xffff0000, v197
v_lshlrev_b32 v212, 16, v198
v_and_b32 v213, 0xffff0000, v198
v_lshlrev_b32 v214, 16, v199
v_and_b32 v215, 0xffff0000, v199
v_pk_add_f32 v[208:209], v[208:209], v[72:73]
v_pk_add_f32 v[210:211], v[210:211], v[74:75]
v_pk_add_f32 v[212:213], v[212:213], v[76:77]
v_pk_add_f32 v[214:215], v[214:215], v[78:79]
v_pk_add_f32 v[208:209], v[208:209], v[40:41]
v_pk_add_f32 v[210:211], v[210:211], v[42:43]
v_pk_add_f32 v[212:213], v[212:213], v[44:45]
v_pk_add_f32 v[214:215], v[214:215], v[46:47]
v_pk_add_f32 v[208:209], v[208:209], v[8:9]
v_pk_add_f32 v[210:211], v[210:211], v[10:11]
v_pk_add_f32 v[212:213], v[212:213], v[12:13]
v_pk_add_f32 v[214:215], v[214:215], v[14:15]
cv v100,v208,v209
cv v101,v210,v211
cv v102,v212,v213
cv v103,v214,v215
)ASM" R"ASM(v_dot2_f32_bf16 v232, v100, v237, v232
v_dot2_f32_bf16 v233, v100, v100, v233
v_dot2_f32_bf16 v234, v100, v156, v234
v_dot2_f32_bf16 v232, v101, v237, v232
v_dot2_f32_bf16 v233, v101, v101, v233
v_dot2_f32_bf16 v234, v101, v157, v234
v_dot2_f32_bf16 v232, v102, v237, v232
v_dot2_f32_bf16 v233, v102, v102, v233
v_dot2_f32_bf16 v234, v102, v158, v234
v_dot2_f32_bf16 v232, v103, v237, v232
v_dot2_f32_bf16 v233, v103, v103, v233
v_dot2_f32_bf16 v234, v103, v159, v234
wt vmcnt(1)
.La5_LW4_0:
s_cbranch_execz .La5_LW4_4
v_lshlrev_b32 v208, 16, v200
v_and_b32 v209, 0xffff0000, v200
v_lshlrev_b32 v210, 16, v201
v_and_b32 v211, 0xffff0000, v201
v_lshlrev_b32 v212, 16, v202
v_and_b32 v213, 0xffff0000, v202
v_lshlrev_b32 v214, 16, v203
v_and_b32 v215, 0xffff0000, v203
v_pk_add_f32 v[208:209], v[208:209], v[80:81]
v_pk_add_f32 v[210:211], v[210:211], v[82:83]
v_pk_add_f32 v[212:213], v[212:213], v[84:85]
v_pk_add_f32 v[214:215], v[214:215], v[86:87]
v_pk_add_f32 v[208:209], v[208:209], v[48:49]
v_pk_add_f32 v[210:211], v[210:211], v[50:51]
v_pk_add_f32 v[212:213], v[212:213], v[52:53]
v_pk_add_f32 v[214:215], v[214:215], v[54:55]
v_pk_add_f32 v[208:209], v[208:209], v[16:17]
v_pk_add_f32 v[210:211], v[210:211], v[18:19]
v_pk_add_f32 v[212:213], v[212:213], v[20:21]
v_pk_add_f32 v[214:215], v[214:215], v[22:23]
cv v104,v208,v209
cv v105,v210,v211
cv v106,v212,v213
cv v107,v214,v215
v_dot2_f32_bf16 v232, v104, v237, v232
v_dot2_f32_bf16 v233, v104, v104, v233
v_dot2_f32_bf16 v234, v104, v172, v234
v_dot2_f32_bf16 v232, v105, v237, v232
v_dot2_f32_bf16 v233, v105, v105, v233
v_dot2_f32_bf16 v234, v105, v173, v234
v_dot2_f32_bf16 v232, v106, v237, v232
v_dot2_f32_bf16 v233, v106, v106, v233
v_dot2_f32_bf16 v234, v106, v174, v234
v_dot2_f32_bf16 v232, v107, v237, v232
v_dot2_f32_bf16 v233, v107, v107, v233
v_dot2_f32_bf16 v234, v107, v175, v234
wt vmcnt(0)
v_lshlrev_b32 v208, 16, v204
v_and_b32 v209, 0xffff0000, v204
v_lshlrev_b32 v210, 16, v205
v_and_b32 v211, 0xffff0000, v205
v_lshlrev_b32 v212, 16, v206
v_and_b32 v213, 0xffff0000, v206
v_lshlrev_b32 v214, 16, v207
v_and_b32 v215, 0xffff0000, v207
v_pk_add_f32 v[208:209], v[208:209], v[88:89]
v_pk_add_f32 v[210:211], v[210:211], v[90:91]
v_pk_add_f32 v[212:213], v[212:213], v[92:93]
v_pk_add_f32 v[214:215], v[214:215], v[94:95]
v_pk_add_f32 v[208:209], v[208:209], v[56:57]
v_pk_add_f32 v[210:211], v[210:211], v[58:59]
v_pk_add_f32 v[212:213], v[212:213], v[60:61]
v_pk_add_f32 v[214:215], v[214:215], v[62:63]
v_pk_add_f32 v[208:209], v[208:209], v[24:25]
v_pk_add_f32 v[210:211], v[210:211], v[26:27]
v_pk_add_f32 v[212:213], v[212:213], v[28:29]
v_pk_add_f32 v[214:215], v[214:215], v[30:31]
cv v108,v208,v209
cv v109,v210,v211
cv v110,v212,v213
cv v111,v214,v215
v_dot2_f32_bf16 v232, v108, v237, v232
v_dot2_f32_bf16 v233, v108, v108, v233
v_dot2_f32_bf16 v234, v108, v188, v234
v_dot2_f32_bf16 v232, v109, v237, v232
v_dot2_f32_bf16 v233, v109, v109, v233
v_dot2_f32_bf16 v234, v109, v189, v234
v_dot2_f32_bf16 v232, v110, v237, v232
v_dot2_f32_bf16 v233, v110, v110, v233
v_dot2_f32_bf16 v234, v110, v190, v234
v_dot2_f32_bf16 v232, v111, v237, v232
v_dot2_f32_bf16 v233, v111, v111, v233
v_dot2_f32_bf16 v234, v111, v191, v234
wt vmcnt(0)
s_lshl2_add_u32 s92, s91, s3
s_lshl_b32 s92, s92, 1
s_sub_u32 s92, 0xffff, s92
v_add_f32_dpp v232, v232, v232 quad_perm:[1,0,3,2] row_mask:0xf bank_mask:0xf
v_add_f32_dpp v233, v233, v233 quad_perm:[1,0,3,2] row_mask:0xf bank_mask:0xf
v_add_f32_dpp v234, v234, v234 quad_perm:[1,0,3,2] row_mask:0xf bank_mask:0xf
v_add_f32_dpp v232, v232, v232 quad_perm:[2,3,0,1] row_mask:0xf bank_mask:0xf
v_add_f32_dpp v233, v233, v233 quad_perm:[2,3,0,1] row_mask:0xf bank_mask:0xf
v_add_f32_dpp v234, v234, v234 quad_perm:[2,3,0,1] row_mask:0xf bank_mask:0xf
v_add_f32_dpp v232, v232, v232 row_ror:8 row_mask:0xf bank_mask:0xf
v_add_f32_dpp v233, v233, v233 row_ror:8 row_mask:0xf bank_mask:0xf
)ASM" R"ASM(v_add_f32_dpp v234, v234, v234 row_ror:8 row_mask:0xf bank_mask:0xf
v_mov_b32 v230, v232
v_mov_b32 v231, v233
v_mov_b32 v235, v234
v_permlane16_swap_b32 v230, v232
v_permlane16_swap_b32 v231, v233
v_permlane16_swap_b32 v235, v234
v_add_f32 v232, v232, v230
v_add_f32 v233, v233, v231
v_add_f32 v234, v234, v235
v_mov_b32 v230, v232
v_mov_b32 v231, v233
v_mov_b32 v235, v234
v_permlane32_swap_b32 v230, v232
v_permlane32_swap_b32 v231, v233
v_permlane32_swap_b32 v235, v234
v_add_f32 v232, v232, v230
v_add_f32 v233, v233, v231
v_add_f32 v234, v234, v235
v_mul_f32 v244, 0x3a800000, v232
v_mul_f32 v233, 0x3a800000, v233
v_fma_f32 v230, -v244, v244, v233
v_mul_f32 v233, 0x3a800000, v233
v_cmp_nge_f32 vcc, v230, v233
v_bfe_u32 v235, v255, 6, 1
v_sub_u32_e64 v235, s92, v235
s_cbranch_vccnz .La5_LT_VAR
.La5_LT_VOK:
cv v244,v244,v244
v_lshlrev_b32 v244, 16, v244
cv v230,v230,v230
v_lshlrev_b32 v230, 16, v230
v_fma_f32 v234, -v244, v236, v234
v_add_f32 v230, 0x3727c5ac, v230
cv v230,v230,v230
v_lshlrev_b32 v230, 16, v230
v_sqrt_f32 v230, v230
v_lshrrev_b32 v232, 3, v255
cv v230,v230,v230
v_lshlrev_b32 v231, 16, v230
v_rcp_f32 v246, v231
v_lshrrev_b32 v230, 16, v230
v_or_b32 v233, v244, v230
v_mul_f32 v234, v234, v246
s_lshl_b32 s93, s3, 4
cv v234,v234,v234
v_and_b32 v230, 8, v232
v_ashrrev_i32 v232, 31, v234
v_or_b32 v232, 0x80000000, v232
v_xor_b32 v232, v232, v234
v_and_b32 v232, 0xffff0000, v232
v_add_u32 v230, s93, v230
v_or_b32 v232, v232, v235
ds_write_b64 v230, v[232:233] offset:0
v_lshrrev_b32 v230, 1, v255
v_and_b32 v230, 56, v230
v_mov_b32 v112, v232
wt lgkmcnt(0)
s_barrier
ds_read_b64 v[232:233], v230 offset:0
s_lshl3_add_u32 s92, s90, s76
s_lshl_b32 s92, s92, 4
s_lshl_b32 s93, s3, 28
s_add_u32 s92, s92, s93
v_lshrrev_b32 v235, 7, v255
v_lshlrev_b32 v235, 12, v235
wt lgkmcnt(0)
v_mov_b32 v234, v232
s_nop 1
v_max_u32_dpp v234, v234, v234 quad_perm:[1,0,3,2] row_mask:0xf bank_mask:0xf
s_nop 1
v_max_u32_dpp v234, v234, v234 quad_perm:[2,3,0,1] row_mask:0xf bank_mask:0xf
s_nop 1
v_max_u32_dpp v234, v234, v234 row_half_mirror row_mask:0xf bank_mask:0xf
s_nop 1
v_cmp_eq_u32 vcc, v232, v234
v_add_u32 v231, 8, v235
s_nop 0
v_cndmask_b32 v234, v232, 0, vcc
v_cndmask_b32 v235, v231, v235, vcc
s_nop 0
v_max_u32_dpp v234, v234, v234 quad_perm:[1,0,3,2] row_mask:0xf bank_mask:0xf
s_nop 1
v_max_u32_dpp v234, v234, v234 quad_perm:[2,3,0,1] row_mask:0xf bank_mask:0xf
s_nop 1
v_max_u32_dpp v234, v234, v234 row_half_mirror row_mask:0xf bank_mask:0xf
s_nop 1
v_cmp_ge_u32 vcc, v232, v234
s_nop 1
s_mov_b64 exec, vcc
buffer_store_dwordx2 v[232:233], v235, s[44:47], s92 offen sc1
s_mov_b64 exec, -1
; ---- out = h + .La5_LN(h) of the own rows; FFN weights into MFMA order
.La5_LT_OUT:
v_mul_f32 v244, v244, v246
v_mov_b32 v247, v246
v_xor_b32 v244, 0x80000000, v244
v_mov_b32 v245, v244
v_cmp_eq_u32 vcc, v112, v234
v_cmp_lt_u32 s[94:95], v112, v234
)ASM" R"ASM(v_lshrrev_b32 v113, 7, v255
v_and_b32 v114, 48, v255
v_lshl_add_u32 v113, v113, 6, v114
v_add_u32 v114, 2048, v113
v_mov_b32 v115, 1073741824
v_cndmask_b32 v113, v113, v114, vcc
s_lshl_b32 s92, s91, 12
v_cndmask_b32 v113, v113, v115, s[94:95]
s_add_u32 s92, s92, 1048576
v_lshlrev_b32 v0, 16, v96
v_and_b32 v1, 0xffff0000, v96
v_pk_fma_f32 v[232:233], v[0:1], v[246:247], v[244:245]
cv v64,v232,v233
v_lshlrev_b32 v2, 16, v97
v_and_b32 v3, 0xffff0000, v97
v_pk_fma_f32 v[232:233], v[2:3], v[246:247], v[244:245]
cv v65,v232,v233
v_lshlrev_b32 v4, 16, v98
v_and_b32 v5, 0xffff0000, v98
v_pk_fma_f32 v[232:233], v[4:5], v[246:247], v[244:245]
cv v66,v232,v233
v_lshlrev_b32 v6, 16, v99
v_and_b32 v7, 0xffff0000, v99
v_pk_fma_f32 v[232:233], v[6:7], v[246:247], v[244:245]
cv v67,v232,v233
st v[64:67],v113,s[44:47],s92 offen offset:0 sc1
v_lshlrev_b32 v8, 16, v100
v_and_b32 v9, 0xffff0000, v100
v_pk_fma_f32 v[232:233], v[8:9], v[246:247], v[244:245]
cv v68,v232,v233
v_lshlrev_b32 v10, 16, v101
v_and_b32 v11, 0xffff0000, v101
v_pk_fma_f32 v[232:233], v[10:11], v[246:247], v[244:245]
cv v69,v232,v233
v_lshlrev_b32 v12, 16, v102
v_and_b32 v13, 0xffff0000, v102
v_pk_fma_f32 v[232:233], v[12:13], v[246:247], v[244:245]
cv v70,v232,v233
v_lshlrev_b32 v14, 16, v103
v_and_b32 v15, 0xffff0000, v103
v_pk_fma_f32 v[232:233], v[14:15], v[246:247], v[244:245]
cv v71,v232,v233
st v[68:71],v113,s[44:47],s92 offen offset:512 sc1
v_lshlrev_b32 v16, 16, v104
v_and_b32 v17, 0xffff0000, v104
v_pk_fma_f32 v[232:233], v[16:17], v[246:247], v[244:245]
cv v72,v232,v233
v_lshlrev_b32 v18, 16, v105
v_and_b32 v19, 0xffff0000, v105
v_pk_fma_f32 v[232:233], v[18:19], v[246:247], v[244:245]
cv v73,v232,v233
v_lshlrev_b32 v20, 16, v106
v_and_b32 v21, 0xffff0000, v106
v_pk_fma_f32 v[232:233], v[20:21], v[246:247], v[244:245]
cv v74,v232,v233
v_lshlrev_b32 v22, 16, v107
v_and_b32 v23, 0xffff0000, v107
v_pk_fma_f32 v[232:233], v[22:23], v[246:247], v[244:245]
cv v75,v232,v233
st v[72:75],v113,s[44:47],s92 offen offset:1024 sc1
v_lshlrev_b32 v24, 16, v108
v_and_b32 v25, 0xffff0000, v108
v_pk_fma_f32 v[232:233], v[24:25], v[246:247], v[244:245]
cv v76,v232,v233
v_lshlrev_b32 v26, 16, v109
v_and_b32 v27, 0xffff0000, v109
v_pk_fma_f32 v[232:233], v[26:27], v[246:247], v[244:245]
cv v77,v232,v233
v_lshlrev_b32 v28, 16, v110
v_and_b32 v29, 0xffff0000, v110
v_pk_fma_f32 v[232:233], v[28:29], v[246:247], v[244:245]
cv v78,v232,v233
v_lshlrev_b32 v30, 16, v111
v_and_b32 v31, 0xffff0000, v111
v_pk_fma_f32 v[232:233], v[30:31], v[246:247], v[244:245]
cv v79,v232,v233
st v[76:79],v113,s[44:47],s92 offen offset:1536 sc1
v_lshlrev_b32 v234, 16, v64
v_and_b32 v235, 0xffff0000, v64
v_pk_add_f32 v[230:231], v[0:1], v[234:235]
cv v192,v230,v231
v_lshlrev_b32 v234, 16, v65
v_and_b32 v235, 0xffff0000, v65
v_pk_add_f32 v[230:231], v[2:3], v[234:235]
cv v193,v230,v231
v_lshlrev_b32 v234, 16, v66
v_and_b32 v235, 0xffff0000, v66
v_pk_add_f32 v[230:231], v[4:5], v[234:235]
cv v194,v230,v231
v_lshlrev_b32 v234, 16, v67
v_and_b32 v235, 0xffff0000, v67
v_pk_add_f32 v[230:231], v[6:7], v[234:235]
cv v195,v230,v231
v_lshlrev_b32 v234, 16, v68
v_and_b32 v235, 0xffff0000, v68
v_pk_add_f32 v[230:231], v[8:9], v[234:235]
cv v196,v230,v231
v_lshlrev_b32 v234, 16, v69
v_and_b32 v235, 0xffff0000, v69
v_pk_add_f32 v[230:231], v[10:11], v[234:235]
)ASM" R"ASM(cv v197,v230,v231
v_lshlrev_b32 v234, 16, v70
v_and_b32 v235, 0xffff0000, v70
v_pk_add_f32 v[230:231], v[12:13], v[234:235]
cv v198,v230,v231
v_lshlrev_b32 v234, 16, v71
v_and_b32 v235, 0xffff0000, v71
v_pk_add_f32 v[230:231], v[14:15], v[234:235]
cv v199,v230,v231
v_lshlrev_b32 v234, 16, v72
v_and_b32 v235, 0xffff0000, v72
v_pk_add_f32 v[230:231], v[16:17], v[234:235]
cv v200,v230,v231
v_lshlrev_b32 v234, 16, v73
v_and_b32 v235, 0xffff0000, v73
v_pk_add_f32 v[230:231], v[18:19], v[234:235]
cv v201,v230,v231
v_lshlrev_b32 v234, 16, v74
v_and_b32 v235, 0xffff0000, v74
v_pk_add_f32 v[230:231], v[20:21], v[234:235]
cv v202,v230,v231
v_lshlrev_b32 v234, 16, v75
v_and_b32 v235, 0xffff0000, v75
v_pk_add_f32 v[230:231], v[22:23], v[234:235]
cv v203,v230,v231
v_lshlrev_b32 v234, 16, v76
v_and_b32 v235, 0xffff0000, v76
v_pk_add_f32 v[230:231], v[24:25], v[234:235]
cv v204,v230,v231
v_lshlrev_b32 v234, 16, v77
v_and_b32 v235, 0xffff0000, v77
v_pk_add_f32 v[230:231], v[26:27], v[234:235]
cv v205,v230,v231
v_lshlrev_b32 v234, 16, v78
v_and_b32 v235, 0xffff0000, v78
v_pk_add_f32 v[230:231], v[28:29], v[234:235]
cv v206,v230,v231
v_lshlrev_b32 v234, 16, v79
v_and_b32 v235, 0xffff0000, v79
v_pk_add_f32 v[230:231], v[30:31], v[234:235]
cv v207,v230,v231
s_cmp_eq_u32 s30, 0
s_cbranch_scc1 .La5_LT_SEL
s_mov_b32 s94, 0x05040100
s_mov_b32 s95, 0x07060302
wt vmcnt(0)
ar v230,a224
ar v231,a228
v_perm_b32 v128, v231, v230, s94
v_perm_b32 v132, v231, v230, s95
ar v230,a232
ar v231,a236
v_perm_b32 v129, v231, v230, s94
v_perm_b32 v133, v231, v230, s95
ar v230,a240
ar v231,a244
v_perm_b32 v130, v231, v230, s94
v_perm_b32 v134, v231, v230, s95
ar v230,a248
ar v231,a252
v_perm_b32 v131, v231, v230, s94
v_perm_b32 v135, v231, v230, s95
ar v230,a225
ar v231,a229
v_perm_b32 v136, v231, v230, s94
v_perm_b32 v140, v231, v230, s95
ar v230,a233
ar v231,a237
v_perm_b32 v137, v231, v230, s94
v_perm_b32 v141, v231, v230, s95
ar v230,a241
ar v231,a245
v_perm_b32 v138, v231, v230, s94
v_perm_b32 v142, v231, v230, s95
ar v230,a249
ar v231,a253
v_perm_b32 v139, v231, v230, s94
v_perm_b32 v143, v231, v230, s95
ar v230,a226
ar v231,a230
v_perm_b32 v144, v231, v230, s94
v_perm_b32 v148, v231, v230, s95
ar v230,a234
ar v231,a238
v_perm_b32 v145, v231, v230, s94
v_perm_b32 v149, v231, v230, s95
ar v230,a242
ar v231,a246
v_perm_b32 v146, v231, v230, s94
v_perm_b32 v150, v231, v230, s95
ar v230,a250
ar v231,a254
v_perm_b32 v147, v231, v230, s94
v_perm_b32 v151, v231, v230, s95
ar v230,a227
ar v231,a231
v_perm_b32 v152, v231, v230, s94
v_perm_b32 v156, v231, v230, s95
ar v230,a235
ar v231,a239
)ASM" R"ASM(v_perm_b32 v153, v231, v230, s94
v_perm_b32 v157, v231, v230, s95
ar v230,a243
ar v231,a247
v_perm_b32 v154, v231, v230, s94
v_perm_b32 v158, v231, v230, s95
ar v230,a251
ar v231,a255
v_perm_b32 v155, v231, v230, s94
v_perm_b32 v159, v231, v230, s95
ar v230,a64
ar v231,a68
v_perm_b32 v160, v231, v230, s94
v_perm_b32 v164, v231, v230, s95
ar v230,a72
ar v231,a76
v_perm_b32 v161, v231, v230, s94
v_perm_b32 v165, v231, v230, s95
ar v230,a80
ar v231,a84
v_perm_b32 v162, v231, v230, s94
v_perm_b32 v166, v231, v230, s95
ar v230,a88
ar v231,a92
v_perm_b32 v163, v231, v230, s94
v_perm_b32 v167, v231, v230, s95
ar v230,a65
ar v231,a69
v_perm_b32 v168, v231, v230, s94
v_perm_b32 v172, v231, v230, s95
ar v230,a73
ar v231,a77
v_perm_b32 v169, v231, v230, s94
v_perm_b32 v173, v231, v230, s95
ar v230,a81
ar v231,a85
v_perm_b32 v170, v231, v230, s94
v_perm_b32 v174, v231, v230, s95
ar v230,a89
ar v231,a93
v_perm_b32 v171, v231, v230, s94
v_perm_b32 v175, v231, v230, s95
ar v230,a66
ar v231,a70
v_perm_b32 v176, v231, v230, s94
v_perm_b32 v180, v231, v230, s95
ar v230,a74
ar v231,a78
v_perm_b32 v177, v231, v230, s94
v_perm_b32 v181, v231, v230, s95
ar v230,a82
ar v231,a86
v_perm_b32 v178, v231, v230, s94
v_perm_b32 v182, v231, v230, s95
ar v230,a90
ar v231,a94
v_perm_b32 v179, v231, v230, s94
v_perm_b32 v183, v231, v230, s95
ar v230,a67
ar v231,a71
v_perm_b32 v184, v231, v230, s94
v_perm_b32 v188, v231, v230, s95
ar v230,a75
ar v231,a79
v_perm_b32 v185, v231, v230, s94
v_perm_b32 v189, v231, v230, s95
ar v230,a83
ar v231,a87
v_perm_b32 v186, v231, v230, s94
v_perm_b32 v190, v231, v230, s95
ar v230,a91
ar v231,a95
v_perm_b32 v187, v231, v230, s94
v_perm_b32 v191, v231, v230, s95
s_and_b32 s92, s3, 1
s_lshr_b32 s93, s3, 1
v_lshrrev_b32 v230, 6, v255
v_and_b32 v231, 48, v255
v_lshl_add_u32 v240, v230, 10, v231
s_lshl_b32 s96, s92, 14
v_add_u32 v240, s96, v240
s_lshl_b32 s96, s92, 10
v_add_u32 v241, s96, v255
s_lshl_b32 s97, s3, 10
v_add_u32 v208, s97, v255
v_bfe_u32 v230, v255, 4, 1
v_bfe_u32 v231, v255, 6, 2
v_lshrrev_b32 v232, 8, v255
v_mov_b32 v44, v230
v_mov_b32 v209, 1048576
v_lshl_add_u32 v209, v231, 7, v209
v_lshl_add_u32 v209, v232, 5, v209
s_lshr_b32 s96, s96, 1
v_add_u32 v209, s96, v209
v_bfe_u32 v233, v255, 4, 2
v_mul_u32_u24 v210, 640, v233
v_lshl_add_u32 v210, v231, 3, v210
s_mul_i32 s96, s93, 2560
s_lshl_b32 s92, s92, 5
s_add_u32 s96, s96, s92
)ASM" R"ASM(v_add_u32 v210, s96, v210
v_cmp_eq_u32 s[98:99], v231, v232
v_lshrrev_b32 v211, 1, v255
s_lshl_b32 s96, s3, 11
s_add_u32 s96, s96, 16384
v_add_u32 v211, s96, v211
v_cndmask_b32 v210, v211, v210, s[98:99]
v_mul_u32_u24 v211, 5, v255
s_lshl_b32 s96, s3, 7
v_lshrrev_b32 v212, 3, v255
v_and_b32 v212, 62, v212
v_add_u32 v212, s96, v212
v_lshlrev_b32 v234, 6, v230
v_lshl_add_u32 v234, v232, 4, v234
v_add_u32 v234, s96, v234
v_mov_b32 v235, 1024
v_cmp_eq_u32 vcc, 0, v231
s_nop 1
v_cndmask_b32 v216, v235, v234, vcc
v_cmp_eq_u32 vcc, 1, v231
s_nop 1
v_cndmask_b32 v217, v235, v234, vcc
v_cmp_eq_u32 vcc, 2, v231
s_nop 1
v_cndmask_b32 v218, v235, v234, vcc
v_cmp_eq_u32 vcc, 3, v231
s_nop 1
v_cndmask_b32 v219, v235, v234, vcc
v_mov_b32 v220, 0
v_mov_b32 v221, 0
v_mov_b32 v222, 0
v_mov_b32 v223, 0
ds_write_b128 v235, v[220:223] offset:10240
s_lshl_b32 s92, s100, 13
s_add_u32 s80, s92, 32768
s_add_u32 s20, s80, 4096
; ---- top two rows of the sequence; SwiGLU slice of 32 hidden units
.La5_LT_SEL:
.La5_LW4_1:
s_cbranch_execz .La5_LW4_R0
s_and_b32 s97, s2, 7
s_lshl_b32 s97, s97, 12
s_cmp_lg_u32 s3, 0
s_cbranch_scc1 .La5_LT_S2
s_mov_b32 s96, 0
v_and_b32 v229, 255, v255
.La5_LT_SA:
s_sleep 2
ld v[224:227],v229,s[44:47],s97 offen offset:0 sc1
s_add_u32 s96, s96, 1
s_lshr_b32 s92, s96, 20
wt vmcnt(0)
v_cmp_ne_u32 vcc, 0, v224
s_nop 0
s_cbranch_scc1 .La5_LT_S1
s_or_b32 vcc_lo, vcc_lo, 0
s_cmp_eq_u32 vcc_lo, -1
s_cbranch_scc1 .La5_LT_S1
s_cmp_lg_u32 s96, 1
s_cbranch_scc1 .La5_LT_SA
s_lshr_b32 s92, s2, 6
s_lshl_b32 s92, s92, 7
s_and_b32 s93, s2, 63
s_add_u32 s92, s92, s93
s_lshl_b32 s92, s92, 7
s_add_u32 s92, s92, 950272
s_add_u32 s93, s92, 8192
wt vmcnt(0)
s_memtime s[16:17]
wt lgkmcnt(0)
buffer_store_dword v255, off, s[44:47], s92 sc1
wt vmcnt(0)
s_memtime s[18:19]
wt lgkmcnt(0)
buffer_store_dword v255, off, s[44:47], s93 sc1
wt vmcnt(0)
s_memtime s[24:25]
wt lgkmcnt(0)
s_add_u32 s16, s16, s24
s_lshl_b32 s18, s18, 1
s_sub_u32 s16, s16, s18
s_lshr_b32 s94, s16, 31
s_lshl_b32 s94, s94, 13
s_and_b32 s97, s2, 1
s_bfe_u32 s92, s2, 0x10001
s_lshl2_add_u32 s97, s92, s97
s_lshl_b32 s97, s97, 12
s_add_u32 s97, s97, s94
s_branch .La5_LT_SA
.La5_LT_S1:
s_cmp_lg_u32 s30, 0
s_cbranch_scc1 .La5_LT_S0
s_sleep 2
.La5_LT_S0:
ld v[224:227],v255,s[44:47],s97 offen offset:0 sc1
ld v[232:235],v255,s[44:47],s97 offen offset:1024 sc1
ld v[244:247],v255,s[44:47],s97 offen offset:2048 sc1
ld v[248:251],v255,s[44:47],s97 offen offset:3072 sc1
s_add_u32 s96, s96, 1
s_lshr_b32 s92, s96, 20
)ASM" R"ASM(wt vmcnt(3)
v_min3_u32 v228, v224, v225, v226
v_min_u32 v228, v227, v228
wt vmcnt(2)
v_min3_u32 v228, v232, v233, v228
v_min3_u32 v228, v234, v235, v228
wt vmcnt(1)
v_min3_u32 v228, v244, v245, v228
v_min3_u32 v228, v246, v247, v228
wt vmcnt(0)
v_min3_u32 v228, v248, v249, v228
v_min3_u32 v228, v250, v251, v228
v_cmp_ne_u32 vcc, 0, v228
v_max_u32 v229, v224, v232
v_max_u32 v230, v244, v248
s_nop 0
s_nop 0
s_cbranch_scc1 .La5_LT_S3
s_cmp_eq_u64 vcc, -1
s_cbranch_scc0 .La5_LT_S1
.La5_LT_S3:
v_max_u32 v228, v229, v230
v_min_u32 v229, v229, v230
v_mov_b32 v231, v228
v_min_u32 v224, v224, v232
v_min_u32 v244, v244, v248
v_max3_u32 v229, v229, v224, v244
v_max3_u32 v230, v226, v234, v246
v_max3_u32 v229, v229, v230, v250
s_nop 1
v_max_u32_dpp v231, v231, v231 row_shr:1 row_mask:0xf bank_mask:0xf
s_nop 1
v_max_u32_dpp v231, v231, v231 row_shr:2 row_mask:0xf bank_mask:0xf
s_nop 1
v_max_u32_dpp v231, v231, v231 row_shr:4 row_mask:0xf bank_mask:0xf
s_nop 1
v_max_u32_dpp v231, v231, v231 row_shr:8 row_mask:0xf bank_mask:0xf
s_nop 1
v_max_u32_dpp v231, v231, v231 row_bcast:15 row_mask:0xa bank_mask:0xf
s_nop 1
v_max_u32_dpp v231, v231, v231 row_bcast:31 row_mask:0xc bank_mask:0xf
s_nop 0
v_readlane_b32 s82, v231, 63
s_nop 1
v_cmp_eq_u32 vcc, s82, v228
v_mov_b32 v230, s82
v_mov_b32 v224, 0
v_cndmask_b32 v228, v228, v229, vcc
s_nop 1
v_max_u32_dpp v228, v228, v228 row_shr:1 row_mask:0xf bank_mask:0xf
s_nop 1
v_max_u32_dpp v228, v228, v228 row_shr:2 row_mask:0xf bank_mask:0xf
s_nop 1
v_max_u32_dpp v228, v228, v228 row_shr:4 row_mask:0xf bank_mask:0xf
s_nop 1
v_max_u32_dpp v228, v228, v228 row_shr:8 row_mask:0xf bank_mask:0xf
s_nop 1
v_max_u32_dpp v228, v228, v228 row_bcast:15 row_mask:0xa bank_mask:0xf
s_nop 1
.La5_LW4_2:
s_cbranch_execz .La5_LW4_R0
v_max_u32_dpp v228, v228, v228 row_bcast:31 row_mask:0xc bank_mask:0xf
s_nop 0
v_readlane_b32 s81, v228, 63
s_nop 1
v_mov_b32 v231, s81
ds_write_b64 v224, v[230:231] offset:64
.La5_LT_S2:
v_mov_b32 v224, 0
wt lgkmcnt(0)
s_barrier
ds_read_b64 v[228:229], v224 offset:64
wt lgkmcnt(0)
v_readfirstlane_b32 s82, v228
v_readfirstlane_b32 s81, v229
s_nop 0
s_andn2_b32 s84, 0x7ff, s82
s_andn2_b32 s21, 0x7ff, s81
s_cmp_eq_u32 s30, 0
s_cbranch_scc1 .La5_LT_ST
s_lshr_b32 s92, s84, 3
s_lshr_b32 s93, s21, 3
s_cmp_eq_u32 s92, s93
s_cselect_b32 s96, 2048, 0
s_lshl_b32 s92, s92, 12
s_lshl_b32 s93, s93, 12
s_add_u32 s93, s93, s96
s_sub_u32 s93, s93, s92
v_mul_lo_u32 v44, v44, s93
s_mov_b32 s96, 0
v_add_u32 v209, v209, v44
ld v[224:227],v209,s[44:47],s92 offen offset:0 sc1
ld v[232:235],v209,s[44:47],s92 offen offset:16 sc1
ld v[244:247],v209,s[44:47],s92 offen offset:1024 sc1
ld v[248:251],v209,s[44:47],s92 offen offset:1040 sc1
s_lshl_b32 s86, s76, 5
s_add_u32 s86, s86, s90
s_lshl2_add_u32 s86, s86, s3
s_lshl_b32 s87, s86, 1
v_bfe_u32 v40, v255, 6, 1
)ASM" R"ASM(v_add_u32 v41, s87, v40
v_cmp_eq_u32 vcc, s84, v41
v_cmp_eq_u32 s[94:95], s21, v41
v_lshrrev_b32 v42, 7, v255
v_and_b32 v43, 48, v255
v_lshl_add_u32 v42, v42, 6, v43
s_or_b64 vcc, vcc, s[94:95]
v_lshl_add_u32 v42, v40, 11, v42
v_mov_b32_e64 v43, 1073741824
s_lshl_b32 s86, s86, 12
v_cndmask_b32 v42, v42, v43, vcc
.La5_LT_H0:
wt vmcnt(3)
v_max3_u32 v45, v224, v225, v226
v_max_u32 v45, v227, v45
wt vmcnt(2)
v_max3_u32 v45, v232, v233, v45
v_max3_u32 v45, v234, v235, v45
wt vmcnt(1)
v_max3_u32 v45, v244, v245, v45
v_max3_u32 v45, v246, v247, v45
wt vmcnt(0)
v_max3_u32 v45, v248, v249, v45
v_max3_u32 v45, v250, v251, v45
v_cmp_eq_u32 vcc, -1, v45
s_nop 1
s_cbranch_vccnz .La5_LT_H1
.La5_LT_H2:
mm a[0:3],v[224:227],a[96:99],0
mm a[0:3],v[232:235],a[128:131],a[0:3]
mm a[0:3],v[244:247],a[160:163],a[0:3]
st v[192:195],v42,s[56:59],s86 offen offset:0 sc1
mm a[0:3],v[248:251],a[192:195],a[0:3]
mm a[4:7],v[224:227],a[100:103],0
mm a[4:7],v[232:235],a[132:135],a[4:7]
ds_write_b64 v210, a[0:1] offset:5120
mm a[4:7],v[244:247],a[164:167],a[4:7]
st v[196:199],v42,s[56:59],s86 offen offset:512 sc1
mm a[4:7],v[248:251],a[196:199],a[4:7]
mm a[8:11],v[224:227],a[104:107],0
mm a[8:11],v[232:235],a[136:139],a[8:11]
ds_write_b64 v210, a[4:5] offset:5200
mm a[8:11],v[244:247],a[168:171],a[8:11]
st v[200:203],v42,s[56:59],s86 offen offset:1024 sc1
mm a[8:11],v[248:251],a[200:203],a[8:11]
mm a[12:15],v[224:227],a[108:111],0
mm a[12:15],v[232:235],a[140:143],a[12:15]
ds_write_b64 v210, a[8:9] offset:5280
mm a[12:15],v[244:247],a[172:175],a[12:15]
st v[204:207],v42,s[56:59],s86 offen offset:1536 sc1
mm a[12:15],v[248:251],a[204:207],a[12:15]
mm a[16:19],v[224:227],a[112:115],0
mm a[16:19],v[232:235],a[144:147],a[16:19]
ds_write_b64 v210, a[12:13] offset:5360
mm a[16:19],v[244:247],a[176:179],a[16:19]
mm a[16:19],v[248:251],a[208:211],a[16:19]
mm a[20:23],v[224:227],a[116:119],0
mm a[20:23],v[232:235],a[148:151],a[20:23]
ds_write_b64 v210, a[16:17] offset:5440
mm a[20:23],v[244:247],a[180:183],a[20:23]
mm a[20:23],v[248:251],a[212:215],a[20:23]
mm a[24:27],v[224:227],a[120:123],0
mm a[24:27],v[232:235],a[152:155],a[24:27]
ds_write_b64 v210, a[20:21] offset:5520
mm a[24:27],v[244:247],a[184:187],a[24:27]
mm a[24:27],v[248:251],a[216:219],a[24:27]
mm a[28:31],v[224:227],a[124:127],0
mm a[28:31],v[232:235],a[156:159],a[28:31]
ds_write_b64 v210, a[24:25] offset:5600
mm a[28:31],v[244:247],a[188:191],a[28:31]
mm a[28:31],v[248:251],a[220:223],a[28:31]
s_nop 7
ds_write_b64 v210, a[28:29] offset:5680
wt lgkmcnt(0)
s_barrier
rd v[224:227],v211 offset:5120
rd v[232:235],v211 offset:5136
rd v[244:247],v211 offset:5152
rd v[248:251],v211 offset:5168
wt lgkmcnt(2)
v_pk_add_f32 v[224:225], v[224:225], v[226:227]
v_pk_add_f32 v[232:233], v[232:233], v[234:235]
wt lgkmcnt(0)
v_pk_add_f32 v[244:245], v[244:245], v[246:247]
v_pk_add_f32 v[248:249], v[248:249], v[250:251]
v_pk_add_f32 v[224:225], v[224:225], v[232:233]
v_pk_add_f32 v[244:245], v[244:245], v[248:249]
v_pk_add_f32 v[224:225], v[224:225], v[244:245]
cv v224,v224,v225
v_mov_b32 v226, v224
s_nop 1
v_permlane32_swap_b32 v224, v226
s_nop 1
v_lshlrev_b32 v228, 16, v224
v_and_b32 v229, 0xffff0000, v224
v_lshlrev_b32 v232, 16, v226
v_and_b32 v233, 0xffff0000, v226
v_mul_f32 v230, 0xbfb8aa3b, v228
v_mul_f32 v231, 0xbfb8aa3b, v229
ex v230,v230
)ASM" R"ASM(ex v231,v231
v_add_f32 v230, 1.0, v230
v_add_f32 v231, 1.0, v231
v_rcp_f32 v230, v230
v_rcp_f32 v231, v231
s_nop 0
v_pk_mul_f32 v[228:229], v[228:229], v[230:231]
cv v228,v228,v229
v_lshlrev_b32 v230, 16, v228
v_and_b32 v231, 0xffff0000, v228
v_pk_mul_f32 v[230:231], v[230:231], v[232:233]
cv v228,v230,v231
ds_write_b16 v212, v228 offset:10240
ds_write_b16_d16_hi v212, v228 offset:10304
wt lgkmcnt(0)
rd v[224:227],v216 offset:10240
rd v[232:235],v217 offset:10240
rd v[244:247],v218 offset:10240
rd v[248:251],v219 offset:10240
wt lgkmcnt(3)
mm v[0:3],v[224:227],v[128:131],0
mm v[4:7],v[224:227],v[132:135],0
mm v[8:11],v[224:227],v[136:139],0
mm v[12:15],v[224:227],v[140:143],0
wt lgkmcnt(2)
mm v[0:3],v[232:235],v[144:147],v[0:3]
mm v[4:7],v[232:235],v[148:151],v[4:7]
mm v[8:11],v[232:235],v[152:155],v[8:11]
mm v[12:15],v[232:235],v[156:159],v[12:15]
wt lgkmcnt(1)
mm v[0:3],v[244:247],v[160:163],v[0:3]
mm v[4:7],v[244:247],v[164:167],v[4:7]
mm v[8:11],v[244:247],v[168:171],v[8:11]
mm v[12:15],v[244:247],v[172:175],v[12:15]
wt lgkmcnt(0)
mm v[0:3],v[248:251],v[176:179],v[0:3]
mm v[4:7],v[248:251],v[180:183],v[4:7]
mm v[8:11],v[248:251],v[184:187],v[8:11]
mm v[12:15],v[248:251],v[188:191],v[12:15]
s_nop 7
v_or_b32 v16, 1, v0
v_or_b32 v20, 1, v1
v_or_b32 v17, 1, v4
v_or_b32 v21, 1, v5
v_or_b32 v18, 1, v8
v_or_b32 v22, 1, v9
v_or_b32 v19, 1, v12
v_or_b32 v23, 1, v13
st v[16:19],v208,s[44:47],s80 offen sc1
st v[20:23],v208,s[44:47],s20 offen sc1
s_branch .La5_LT_END
; ---- rows that were not routed leave; final sums of the routed rows
.La5_LT_ST:
.La5_LW4_3:
s_cbranch_execz .La5_LW4_R0
s_sleep 16
s_lshl_b32 s92, s76, 5
s_add_u32 s92, s92, s90
s_lshl2_add_u32 s92, s92, s3
s_lshl_b32 s93, s92, 1
v_bfe_u32 v230, v255, 6, 1
v_add_u32 v231, s93, v230
v_cmp_ne_u32 vcc, s84, v231
v_cmp_ne_u32 s[94:95], s21, v231
v_lshrrev_b32 v232, 7, v255
v_and_b32 v233, 48, v255
v_lshl_add_u32 v232, v232, 6, v233
v_lshl_add_u32 v232, v230, 11, v232
s_lshl_b32 s92, s92, 12
s_and_b64 exec, vcc, s[94:95]
s_cbranch_scc0 .La5_LT_F0
st v[192:195],v232,s[56:59],s92 offen offset:0 sc1
st v[196:199],v232,s[56:59],s92 offen offset:512 sc1
st v[200:203],v232,s[56:59],s92 offen offset:1024 sc1
st v[204:207],v232,s[56:59],s92 offen offset:1536 sc1
.La5_LT_F0:
s_mov_b64 exec, -1
s_cmp_eq_u32 s75, 1
s_cbranch_scc1 .La5_LT_END
s_lshr_b32 s92, s2, 6
s_lshl2_add_u32 s92, s92, s74
s_and_b32 s93, s2, 7
s_lshl3_add_u32 s91, s92, s93
s_cmp_lt_u32 s91, 64
s_cselect_b32 s84, s84, s21
s_cselect_b32 s82, s82, s81
s_bfe_u32 s92, s91, 0x30003
s_and_b32 s93, s91, 3
s_lshl2_add_u32 s96, s92, s93
s_and_b32 s85, s91, 4
s_lshl_b32 s85, s85, 1
v_bfe_u32 v0, v255, 6, 2
v_bfe_u32 v1, v255, 4, 2
v_lshlrev_b32 v1, 1, v1
v_lshl_add_u32 v0, v0, 4, v1
v_add_u32 v0, s85, v0
s_lshl_b32 s85, s96, 10
v_add_u32 v1, s85, v0
s_lshl_b32 s85, s96, 6
v_add_u32 v0, s85, v0
)ASM" R"ASM(s_lshr_b32 s92, s84, 4
s_lshl_b32 s92, s92, 15
s_and_b32 s93, s84, 15
s_lshl_b32 s93, s93, 6
s_add_u32 s92, s92, s93
s_add_u32 s93, s92, 4194304
s_add_u32 s94, s92, 8388608
s_add_u32 s95, s92, 12582912
buffer_load_ushort v4, v1, s[32:35], s92 offen sc1
buffer_load_ushort v5, v1, s[32:35], s93 offen sc1
buffer_load_ushort v6, v1, s[32:35], s94 offen sc1
buffer_load_ushort v7, v1, s[32:35], s95 offen sc1
s_lshl_b32 s21, s84, 11
s_and_b32 s92, s84, 0xf8
s_lshl4_add_u32 s97, s92, s97
s_lshr_b32 s92, s84, 8
s_lshl4_add_u32 s97, s92, s97
buffer_load_ushort v8, v0, s[68:71], s21 offen
ld v[12:15],off,s[44:47],s97 sc1
v_lshrrev_b32 v2, 6, v255
v_lshlrev_b32 v2, 13, v2
v_and_b32 v3, 48, v255
v_add_u32 v2, v2, v3
s_lshl_b32 s92, s3, 17
s_lshl_b32 s93, s91, 6
s_add_u32 s92, s92, s93
s_add_u32 s94, s92, 32768
s_cmp_eq_u32 s3, 3
s_cselect_b32 s95, 0, 524288
s_cselect_b32 s96, 0, -1
s_add_u32 s95, s95, s94
s_ashr_i32 s92, s82, 31
s_orn2_b32 s92, 0x80000000, s92
s_xor_b32 s92, s92, s82
s_and_b32 s92, s92, 0xffff0000
v_mov_b32 v9, 0xbfb8aa3b
v_mul_f32 v9, s92, v9
ex v9,v9
v_lshrrev_b32 v3, 8, v255
s_lshl_b32 s92, s3, 4
v_add_f32 v9, 1.0, v9
v_lshl_add_u32 v3, v3, 2, s92
v_rcp_f32 v9, v9
v_and_b32 v10, 48, v255
v_mul_u32_u24 v10, 20, v10
v_add_u32 v3, v3, v10
cv v9,v9,v9
v_lshlrev_b32 v9, 16, v9
v_and_b32 v10, 0xf0, v255
v_mul_u32_u24 v10, 5, v10
wt vmcnt(0)
v_cmp_eq_u32 vcc, s82, v12
s_nop 1
v_cndmask_b32 v12, v15, v13, vcc
v_lshlrev_b32 v14, 16, v12
v_rcp_f32 v14, v14
v_and_b32 v12, 0xffff0000, v12
v_mul_f32 v12, v12, v14
v_mov_b32 v15, v14
v_xor_b32 v12, 0x80000000, v12
v_mov_b32 v13, v12
v_lshlrev_b32 v4, 16, v4
v_lshlrev_b32 v5, 16, v5
v_lshlrev_b32 v6, 16, v6
v_lshlrev_b32 v7, 16, v7
v_lshlrev_b32 v8, 16, v8
v_add_f32 v4, v4, v5
v_add_f32 v6, v6, v7
v_add_f32 v4, v4, v6
v_add_f32 v4, v4, v8
cv v4,v4,v4
v_lshlrev_b32 v4, 16, v4
v_fma_f32 v5, v4, v14, v12
cv v5,v5,v5
v_lshlrev_b32 v5, 16, v5
s_sleep 12
s_mov_b32 s93, 0
.La5_LT_Y:
ld v[16:19],v2,s[44:47],s94 offen sc1
ld v[20:23],v2,s[44:47],s95 offen sc1
s_add_u32 s93, s93, 1
s_lshr_b32 s92, s93, 20
wt vmcnt(0)
v_min3_u32 v24, v16, v17, v18
v_min3_u32 v25, v20, v21, v22
v_min3_u32 v24, v24, v19, v23
v_min_u32 v24, v24, v25
v_cmp_ne_u32 vcc, 0, v24
v_and_b32 v20, s96, v20
v_and_b32 v21, s96, v21
v_and_b32 v22, s96, v22
v_and_b32 v23, s96, v23
s_cbranch_scc1 .La5_LT_Y1
s_cmp_eq_u64 vcc, -1
s_nop 0
s_cbranch_scc0 .La5_LT_Y
.La5_LT_Y1:
v_pk_add_f32 v[16:17], v[16:17], v[20:21]
v_pk_add_f32 v[18:19], v[18:19], v[22:23]
s_nop 0
)ASM" R"ASM(v_add_f32_dpp v16, v16, v16 row_ror:4 row_mask:0xf bank_mask:0xf
v_add_f32_dpp v17, v17, v17 row_ror:4 row_mask:0xf bank_mask:0xf
v_add_f32_dpp v18, v18, v18 row_ror:4 row_mask:0xf bank_mask:0xf
v_add_f32_dpp v19, v19, v19 row_ror:4 row_mask:0xf bank_mask:0xf
v_add_f32_dpp v16, v16, v16 row_ror:8 row_mask:0xf bank_mask:0xf
v_add_f32_dpp v17, v17, v17 row_ror:8 row_mask:0xf bank_mask:0xf
v_add_f32_dpp v18, v18, v18 row_ror:8 row_mask:0xf bank_mask:0xf
v_add_f32_dpp v19, v19, v19 row_ror:8 row_mask:0xf bank_mask:0xf
ds_write_b32 v3, v16 offset:5120
ds_write_b32 v3, v17 offset:5200
ds_write_b32 v3, v18 offset:5280
ds_write_b32 v3, v19 offset:5360
wt lgkmcnt(0)
s_barrier
rd v[16:19],v10 offset:5120
rd v[20:23],v10 offset:5136
rd v[24:27],v10 offset:5152
rd v[28:31],v10 offset:5168
wt lgkmcnt(2)
v_pk_add_f32 v[16:17], v[16:17], v[18:19]
v_pk_add_f32 v[20:21], v[20:21], v[22:23]
wt lgkmcnt(0)
v_pk_add_f32 v[24:25], v[24:25], v[26:27]
v_pk_add_f32 v[28:29], v[28:29], v[30:31]
v_pk_add_f32 v[16:17], v[16:17], v[20:21]
v_pk_add_f32 v[24:25], v[24:25], v[28:29]
v_pk_add_f32 v[16:17], v[16:17], v[24:25]
v_add_f32 v16, v16, v17
cv v16,v16,v16
v_lshlrev_b32 v16, 16, v16
v_mul_f32 v16, v16, v9
cv v16,v16,v16
v_lshlrev_b32 v16, 16, v16
v_add_f32 v16, v16, v5
cv v16,v16,v16
v_lshlrev_b32 v16, 16, v16
v_add_f32 v16, v16, v4
cv v16,v16,v16
s_cmp_lg_u32 s3, 0
s_cbranch_scc1 .La5_LT_END
s_mov_b64 exec, 0xffff
buffer_store_short v16, v0, s[56:59], s21 offen
s_mov_b64 exec, -1
.La5_LT_END:
s_nop 0
s_nop 0
.La5_LW4_4:
s_cbranch_execz .La5_LW4_R0
s_lshl2_add_u32 s92, s2, s3
s_cmp_lg_u32 s92, 992
s_cbranch_scc1 .La5_LT_E1
buffer_store_dwordx2 v[252:253], off, s[64:67], 0 sc1
.La5_LT_E1:
s_branch .La5_LEND
; ---- fallback: the earlier 32 rows against all their keys with the true running maximum (online softmax)
.La5_LA_SLOW0:
wt lgkmcnt(0)
s_nop 7
s_nop 7
aw a160,v192
aw a161,v193
aw a162,v194
aw a163,v195
aw a164,v196
aw a165,v197
aw a166,v198
aw a167,v199
aw a168,v200
aw a169,v201
aw a170,v202
aw a171,v203
aw a172,v204
aw a173,v205
aw a174,v206
aw a175,v207
aw a176,v208
aw a177,v209
aw a178,v210
aw a179,v211
aw a180,v212
aw a181,v213
aw a182,v214
aw a183,v215
aw a184,v216
aw a185,v217
aw a186,v218
aw a187,v219
aw a188,v220
aw a189,v221
aw a190,v222
aw a191,v223
s_lshl_b32 s96, s72, 19
s_add_u32 s96, s96, 20971520
s_add_u32 s92, s26, s96
s_addc_u32 s93, s27, 0
s_and_b32 s93, s93, 0xffff
s_mov_b32 s94, 17301504
s_mov_b32 s95, 0x20000
v_mov_b32 v0, 0
v_mov_b32 v1, 0
)ASM" R"ASM(v_mov_b32 v2, 0
v_mov_b32 v3, 0
v_mov_b32 v4, 0
v_mov_b32 v5, 0
v_mov_b32 v6, 0
v_mov_b32 v7, 0
v_mov_b32 v8, 0
v_mov_b32 v9, 0
v_mov_b32 v10, 0
v_mov_b32 v11, 0
v_mov_b32 v12, 0
v_mov_b32 v13, 0
v_mov_b32 v14, 0
v_mov_b32 v15, 0
v_mov_b32 v16, 0
v_mov_b32 v17, 0
v_mov_b32 v18, 0
v_mov_b32 v19, 0
v_mov_b32 v20, 0
v_mov_b32 v21, 0
v_mov_b32 v22, 0
v_mov_b32 v23, 0
v_mov_b32 v24, 0
v_mov_b32 v25, 0
v_mov_b32 v26, 0
v_mov_b32 v27, 0
v_mov_b32 v28, 0
v_mov_b32 v29, 0
v_mov_b32 v30, 0
v_mov_b32 v31, 0
v_mov_b32 v32, 0
v_mov_b32 v33, 0
v_mov_b32 v34, 0
v_mov_b32 v35, 0
v_mov_b32 v36, 0
v_mov_b32 v37, 0
v_mov_b32 v38, 0
v_mov_b32 v39, 0
v_mov_b32 v40, 0
v_mov_b32 v41, 0
v_mov_b32 v42, 0
v_mov_b32 v43, 0
v_mov_b32 v44, 0
v_mov_b32 v45, 0
v_mov_b32 v46, 0
v_mov_b32 v47, 0
v_mov_b32 v48, 0
v_mov_b32 v49, 0
v_mov_b32 v50, 0
v_mov_b32 v51, 0
v_mov_b32 v52, 0
v_mov_b32 v53, 0
v_mov_b32 v54, 0
v_mov_b32 v55, 0
v_mov_b32 v56, 0
v_mov_b32 v57, 0
v_mov_b32 v58, 0
v_mov_b32 v59, 0
v_mov_b32 v60, 0
v_mov_b32 v61, 0
v_mov_b32 v62, 0
v_mov_b32 v63, 0
v_mov_b32 v216, 0xff61b1e6
v_mov_b32 v217, 0xff61b1e6
v_mov_b32 v218, 0
v_mov_b32 v219, 0
s_mov_b32 s7, 0
.La5_LA_SLOW0_L:
s_lshl_b32 s96, s7, 13
s_add_u32 s97, s96, 4096
ld a[128:131],v255,s[92:95],s96 offen offset:0 sc1
ld a[132:135],v255,s[92:95],s96 offen offset:1024 sc1
ld a[136:139],v255,s[92:95],s96 offen offset:2048 sc1
ld a[140:143],v255,s[92:95],s96 offen offset:3072 sc1
ld a[144:147],v255,s[92:95],s97 offen offset:0 sc1
ld a[148:151],v255,s[92:95],s97 offen offset:1024 sc1
ld a[152:155],v255,s[92:95],s97 offen offset:2048 sc1
ld a[156:159],v255,s[92:95],s97 offen offset:3072 sc1
s_lshl_b32 s96, s76, 3
s_sub_u32 s96, s96, s7
s_add_u32 s96, s96, s3
s_lshl_b32 s96, s96, 5
v_bfe_u32 v220, v255, 4, 4
v_lshrrev_b32 v221, 8, v255
v_lshlrev_b32 v221, 2, v221
v_sub_u32 v220, v220, v221
v_add_u32 v220, s96, v220
v_mov_b32 v222, 0xceabfb8f
wt vmcnt(0)
mm v[192:195],a[128:131],v[128:131],0
mm v[192:195],a[132:135],v[132:135],v[192:195]
mm v[192:195],a[136:139],v[136:139],v[192:195]
mm v[192:195],a[140:143],v[140:143],v[192:195]
mm v[196:199],a[144:147],v[128:131],0
mm v[196:199],a[148:151],v[132:135],v[196:199]
mm v[196:199],a[152:155],v[136:139],v[196:199]
mm v[196:199],a[156:159],v[140:143],v[196:199]
mm v[200:203],a[128:131],v[144:147],0
mm v[200:203],a[132:135],v[148:151],v[200:203]
mm v[200:203],a[136:139],v[152:155],v[200:203]
)ASM" R"ASM(mm v[200:203],a[140:143],v[156:159],v[200:203]
mm v[204:207],a[144:147],v[144:147],0
mm v[204:207],a[148:151],v[148:151],v[204:207]
mm v[204:207],a[152:155],v[152:155],v[204:207]
mm v[204:207],a[156:159],v[156:159],v[204:207]
s_nop 7
s_nop 7
s_lshl_b32 s96, s7, 13
s_add_u32 s96, s96, 16777216
s_add_u32 s97, s96, 4096
ld a[128:131],v255,s[92:95],s96 offen offset:0 sc1
ld a[132:135],v255,s[92:95],s96 offen offset:1024 sc1
ld a[136:139],v255,s[92:95],s96 offen offset:2048 sc1
ld a[140:143],v255,s[92:95],s96 offen offset:3072 sc1
ld a[144:147],v255,s[92:95],s97 offen offset:0 sc1
ld a[148:151],v255,s[92:95],s97 offen offset:1024 sc1
ld a[152:155],v255,s[92:95],s97 offen offset:2048 sc1
ld a[156:159],v255,s[92:95],s97 offen offset:3072 sc1
v_cmp_gt_i32 vcc, 0, v220
s_nop 1
v_cndmask_b32 v192, v192, v222, vcc
v_cmp_gt_i32 vcc, 1, v220
s_nop 1
v_cndmask_b32 v193, v193, v222, vcc
v_cmp_gt_i32 vcc, 2, v220
s_nop 1
v_cndmask_b32 v194, v194, v222, vcc
v_cmp_gt_i32 vcc, 3, v220
s_nop 1
v_cndmask_b32 v195, v195, v222, vcc
v_cmp_gt_i32 vcc, 16, v220
s_nop 1
v_cndmask_b32 v196, v196, v222, vcc
v_cmp_gt_i32 vcc, 17, v220
s_nop 1
v_cndmask_b32 v197, v197, v222, vcc
v_cmp_gt_i32 vcc, 18, v220
s_nop 1
v_cndmask_b32 v198, v198, v222, vcc
v_cmp_gt_i32 vcc, 19, v220
s_nop 1
v_cndmask_b32 v199, v199, v222, vcc
v_cmp_gt_i32 vcc, -16, v220
s_nop 1
v_cndmask_b32 v200, v200, v222, vcc
v_cmp_gt_i32 vcc, -15, v220
s_nop 1
v_cndmask_b32 v201, v201, v222, vcc
v_cmp_gt_i32 vcc, -14, v220
s_nop 1
v_cndmask_b32 v202, v202, v222, vcc
v_cmp_gt_i32 vcc, -13, v220
s_nop 1
v_cndmask_b32 v203, v203, v222, vcc
v_cmp_gt_i32 vcc, 0, v220
s_nop 1
v_cndmask_b32 v204, v204, v222, vcc
v_cmp_gt_i32 vcc, 1, v220
s_nop 1
v_cndmask_b32 v205, v205, v222, vcc
v_cmp_gt_i32 vcc, 2, v220
s_nop 1
v_cndmask_b32 v206, v206, v222, vcc
v_cmp_gt_i32 vcc, 3, v220
s_nop 1
v_cndmask_b32 v207, v207, v222, vcc
v_max3_f32 v222, v192, v193, v216
v_max3_f32 v222, v194, v195, v222
v_max3_f32 v222, v196, v197, v222
v_max3_f32 v222, v198, v199, v222
v_mov_b32 v221, v222
s_nop 1
v_permlane16_swap_b32 v221, v222
s_nop 1
v_max_f32 v222, v222, v221
v_mov_b32 v221, v222
s_nop 1
v_permlane32_swap_b32 v221, v222
s_nop 1
v_max_f32 v222, v222, v221
v_sub_f32 v223, v216, v222
v_mov_b32 v216, v222
ex v223,v223
v_sub_f32 v192, v192, v222
v_sub_f32 v193, v193, v222
v_sub_f32 v194, v194, v222
v_sub_f32 v195, v195, v222
v_sub_f32 v196, v196, v222
v_sub_f32 v197, v197, v222
v_sub_f32 v198, v198, v222
v_sub_f32 v199, v199, v222
ex v192,v192
ex v193,v193
ex v194,v194
ex v195,v195
ex v196,v196
ex v197,v197
ex v198,v198
ex v199,v199
v_mul_f32 v218, v218, v223
)ASM" R"ASM(v_add_f32 v218, v218, v192
v_add_f32 v218, v218, v193
v_add_f32 v218, v218, v194
v_add_f32 v218, v218, v195
v_add_f32 v218, v218, v196
v_add_f32 v218, v218, v197
v_add_f32 v218, v218, v198
v_add_f32 v218, v218, v199
cv v208,v192,v193
cv v209,v194,v195
cv v210,v196,v197
cv v211,v198,v199
v_mul_f32 v0, v0, v223
v_mul_f32 v1, v1, v223
v_mul_f32 v2, v2, v223
v_mul_f32 v3, v3, v223
v_mul_f32 v4, v4, v223
v_mul_f32 v5, v5, v223
v_mul_f32 v6, v6, v223
v_mul_f32 v7, v7, v223
v_mul_f32 v8, v8, v223
v_mul_f32 v9, v9, v223
v_mul_f32 v10, v10, v223
v_mul_f32 v11, v11, v223
v_mul_f32 v12, v12, v223
v_mul_f32 v13, v13, v223
v_mul_f32 v14, v14, v223
v_mul_f32 v15, v15, v223
v_mul_f32 v16, v16, v223
v_mul_f32 v17, v17, v223
v_mul_f32 v18, v18, v223
v_mul_f32 v19, v19, v223
v_mul_f32 v20, v20, v223
v_mul_f32 v21, v21, v223
v_mul_f32 v22, v22, v223
v_mul_f32 v23, v23, v223
v_mul_f32 v24, v24, v223
v_mul_f32 v25, v25, v223
v_mul_f32 v26, v26, v223
v_mul_f32 v27, v27, v223
v_mul_f32 v28, v28, v223
v_mul_f32 v29, v29, v223
v_mul_f32 v30, v30, v223
v_mul_f32 v31, v31, v223
v_max3_f32 v222, v200, v201, v217
v_max3_f32 v222, v202, v203, v222
v_max3_f32 v222, v204, v205, v222
v_max3_f32 v222, v206, v207, v222
v_mov_b32 v221, v222
s_nop 1
v_permlane16_swap_b32 v221, v222
s_nop 1
v_max_f32 v222, v222, v221
v_mov_b32 v221, v222
s_nop 1
v_permlane32_swap_b32 v221, v222
s_nop 1
v_max_f32 v222, v222, v221
v_sub_f32 v223, v217, v222
v_mov_b32 v217, v222
ex v223,v223
v_sub_f32 v200, v200, v222
v_sub_f32 v201, v201, v222
v_sub_f32 v202, v202, v222
v_sub_f32 v203, v203, v222
v_sub_f32 v204, v204, v222
v_sub_f32 v205, v205, v222
v_sub_f32 v206, v206, v222
v_sub_f32 v207, v207, v222
ex v200,v200
ex v201,v201
ex v202,v202
ex v203,v203
ex v204,v204
ex v205,v205
ex v206,v206
ex v207,v207
v_mul_f32 v219, v219, v223
v_add_f32 v219, v219, v200
v_add_f32 v219, v219, v201
v_add_f32 v219, v219, v202
v_add_f32 v219, v219, v203
v_add_f32 v219, v219, v204
v_add_f32 v219, v219, v205
v_add_f32 v219, v219, v206
v_add_f32 v219, v219, v207
cv v212,v200,v201
cv v213,v202,v203
cv v214,v204,v205
cv v215,v206,v207
v_mul_f32 v32, v32, v223
v_mul_f32 v33, v33, v223
v_mul_f32 v34, v34, v223
v_mul_f32 v35, v35, v223
v_mul_f32 v36, v36, v223
v_mul_f32 v37, v37, v223
v_mul_f32 v38, v38, v223
v_mul_f32 v39, v39, v223
v_mul_f32 v40, v40, v223
v_mul_f32 v41, v41, v223
)ASM" R"ASM(v_mul_f32 v42, v42, v223
v_mul_f32 v43, v43, v223
v_mul_f32 v44, v44, v223
v_mul_f32 v45, v45, v223
v_mul_f32 v46, v46, v223
v_mul_f32 v47, v47, v223
v_mul_f32 v48, v48, v223
v_mul_f32 v49, v49, v223
v_mul_f32 v50, v50, v223
v_mul_f32 v51, v51, v223
v_mul_f32 v52, v52, v223
v_mul_f32 v53, v53, v223
v_mul_f32 v54, v54, v223
v_mul_f32 v55, v55, v223
v_mul_f32 v56, v56, v223
v_mul_f32 v57, v57, v223
v_mul_f32 v58, v58, v223
v_mul_f32 v59, v59, v223
v_mul_f32 v60, v60, v223
v_mul_f32 v61, v61, v223
v_mul_f32_e64 v62, v62, v223
v_mul_f32_e64 v63, v63, v223
wt vmcnt(0)
mm v[0:3],a[128:131],v[208:211],v[0:3]
mm v[32:35],a[128:131],v[212:215],v[32:35]
mm v[4:7],a[132:135],v[208:211],v[4:7]
mm v[36:39],a[132:135],v[212:215],v[36:39]
mm v[8:11],a[136:139],v[208:211],v[8:11]
mm v[40:43],a[136:139],v[212:215],v[40:43]
mm v[12:15],a[140:143],v[208:211],v[12:15]
mm v[44:47],a[140:143],v[212:215],v[44:47]
mm v[16:19],a[144:147],v[208:211],v[16:19]
mm v[48:51],a[144:147],v[212:215],v[48:51]
mm v[20:23],a[148:151],v[208:211],v[20:23]
mm v[52:55],a[148:151],v[212:215],v[52:55]
mm v[24:27],a[152:155],v[208:211],v[24:27]
mm v[56:59],a[152:155],v[212:215],v[56:59]
mm v[28:31],a[156:159],v[208:211],v[28:31]
mm v[60:63],a[156:159],v[212:215],v[60:63]
s_nop 7
s_nop 7
s_add_u32 s7, s7, 1
s_lshr_b32 s96, s7, 3
s_cmp_le_u32 s96, s76
s_cbranch_scc1 .La5_LA_SLOW0_L
v_mov_b32 v221, v218
s_nop 1
v_permlane16_swap_b32 v221, v218
s_nop 1
v_add_f32 v218, v218, v221
v_mov_b32 v221, v218
s_nop 1
v_permlane32_swap_b32 v221, v218
s_nop 1
v_add_f32 v218, v218, v221
v_mov_b32 v221, v219
s_nop 1
v_permlane16_swap_b32 v221, v219
s_nop 1
v_add_f32 v219, v219, v221
v_mov_b32 v221, v219
s_nop 1
v_permlane32_swap_b32 v221, v219
s_nop 1
v_add_f32 v219, v219, v221
v_rcp_f32 v242, v218
v_rcp_f32 v243, v219
ar v192,a160
ar v193,a161
ar v194,a162
ar v195,a163
ar v196,a164
ar v197,a165
ar v198,a166
ar v199,a167
ar v200,a168
ar v201,a169
ar v202,a170
ar v203,a171
ar v204,a172
ar v205,a173
ar v206,a174
ar v207,a175
ar v208,a176
ar v209,a177
ar v210,a178
ar v211,a179
ar v212,a180
ar v213,a181
ar v214,a182
ar v215,a183
ar v216,a184
ar v217,a185
ar v218,a186
ar v219,a187
ar v220,a188
ar v221,a189
ar v222,a190
ar v223,a191
s_mov_b32 s95, 4096
)ASM" R"ASM(s_mov_b32 s96, 8192
s_mov_b32 s97, 12288
s_mov_b64 vcc, 0
s_nop 1
s_setpc_b64 s[24:25]
; ---- fallback: the later 32 rows
.La5_LA_SLOW1:
wt lgkmcnt(0)
s_nop 7
s_nop 7
aw a160,v192
aw a161,v193
aw a162,v194
aw a163,v195
aw a164,v196
aw a165,v197
aw a166,v198
aw a167,v199
aw a168,v200
aw a169,v201
aw a170,v202
aw a171,v203
aw a172,v204
aw a173,v205
aw a174,v206
aw a175,v207
aw a176,v208
aw a177,v209
aw a178,v210
aw a179,v211
aw a180,v212
aw a181,v213
aw a182,v214
aw a183,v215
aw a184,v216
aw a185,v217
aw a186,v218
aw a187,v219
aw a188,v220
aw a189,v221
aw a190,v222
aw a191,v223
s_lshl_b32 s96, s72, 19
s_add_u32 s96, s96, 20971520
s_add_u32 s92, s26, s96
s_addc_u32 s93, s27, 0
s_and_b32 s93, s93, 0xffff
s_mov_b32 s94, 17301504
s_mov_b32 s95, 0x20000
v_mov_b32 v64, 0
v_mov_b32 v65, 0
v_mov_b32 v66, 0
v_mov_b32 v67, 0
v_mov_b32 v68, 0
v_mov_b32 v69, 0
v_mov_b32 v70, 0
v_mov_b32 v71, 0
v_mov_b32 v72, 0
v_mov_b32 v73, 0
v_mov_b32 v74, 0
v_mov_b32 v75, 0
v_mov_b32 v76, 0
v_mov_b32 v77, 0
v_mov_b32 v78, 0
v_mov_b32 v79, 0
v_mov_b32 v80, 0
v_mov_b32 v81, 0
v_mov_b32 v82, 0
v_mov_b32 v83, 0
v_mov_b32 v84, 0
v_mov_b32 v85, 0
v_mov_b32 v86, 0
v_mov_b32 v87, 0
v_mov_b32 v88, 0
v_mov_b32 v89, 0
v_mov_b32 v90, 0
v_mov_b32 v91, 0
v_mov_b32 v92, 0
v_mov_b32 v93, 0
v_mov_b32 v94, 0
v_mov_b32 v95, 0
v_mov_b32 v96, 0
v_mov_b32 v97, 0
v_mov_b32 v98, 0
v_mov_b32 v99, 0
v_mov_b32 v100, 0
v_mov_b32 v101, 0
v_mov_b32 v102, 0
v_mov_b32 v103, 0
v_mov_b32 v104, 0
v_mov_b32 v105, 0
v_mov_b32 v106, 0
v_mov_b32 v107, 0
v_mov_b32 v108, 0
v_mov_b32 v109, 0
v_mov_b32 v110, 0
v_mov_b32 v111, 0
v_mov_b32 v112, 0
v_mov_b32 v113, 0
v_mov_b32 v114, 0
)ASM" R"ASM(v_mov_b32 v115, 0
v_mov_b32 v116, 0
v_mov_b32 v117, 0
v_mov_b32 v118, 0
v_mov_b32 v119, 0
v_mov_b32 v120, 0
v_mov_b32 v121, 0
v_mov_b32 v122, 0
v_mov_b32 v123, 0
v_mov_b32 v124, 0
v_mov_b32 v125, 0
v_mov_b32 v126, 0
v_mov_b32 v127, 0
v_mov_b32 v216, 0xff61b1e6
v_mov_b32 v217, 0xff61b1e6
v_mov_b32 v218, 0
v_mov_b32 v219, 0
s_mov_b32 s7, 0
.La5_LA_SLOW1_L:
s_lshl_b32 s96, s7, 13
s_add_u32 s97, s96, 4096
ld a[128:131],v255,s[92:95],s96 offen offset:0 sc1
ld a[132:135],v255,s[92:95],s96 offen offset:1024 sc1
ld a[136:139],v255,s[92:95],s96 offen offset:2048 sc1
ld a[140:143],v255,s[92:95],s96 offen offset:3072 sc1
ld a[144:147],v255,s[92:95],s97 offen offset:0 sc1
ld a[148:151],v255,s[92:95],s97 offen offset:1024 sc1
ld a[152:155],v255,s[92:95],s97 offen offset:2048 sc1
ld a[156:159],v255,s[92:95],s97 offen offset:3072 sc1
s_lshl_b32 s96, s76, 3
s_sub_u32 s96, s96, s7
s_sub_u32 s96, s96, s3
s_add_u32 s96, s96, 7
s_lshl_b32 s96, s96, 5
v_bfe_u32 v220, v255, 4, 4
v_lshrrev_b32 v221, 8, v255
v_lshlrev_b32 v221, 2, v221
v_sub_u32 v220, v220, v221
v_add_u32 v220, s96, v220
v_mov_b32 v222, 0xceabfb8f
wt vmcnt(0)
mm v[192:195],a[128:131],v[160:163],0
mm v[192:195],a[132:135],v[164:167],v[192:195]
mm v[192:195],a[136:139],v[168:171],v[192:195]
mm v[192:195],a[140:143],v[172:175],v[192:195]
mm v[196:199],a[144:147],v[160:163],0
mm v[196:199],a[148:151],v[164:167],v[196:199]
mm v[196:199],a[152:155],v[168:171],v[196:199]
mm v[196:199],a[156:159],v[172:175],v[196:199]
mm v[200:203],a[128:131],v[176:179],0
mm v[200:203],a[132:135],v[180:183],v[200:203]
mm v[200:203],a[136:139],v[184:187],v[200:203]
mm v[200:203],a[140:143],v[188:191],v[200:203]
mm v[204:207],a[144:147],v[176:179],0
mm v[204:207],a[148:151],v[180:183],v[204:207]
mm v[204:207],a[152:155],v[184:187],v[204:207]
mm v[204:207],a[156:159],v[188:191],v[204:207]
s_nop 7
s_nop 7
s_lshl_b32 s96, s7, 13
s_add_u32 s96, s96, 16777216
s_add_u32 s97, s96, 4096
ld a[128:131],v255,s[92:95],s96 offen offset:0 sc1
ld a[132:135],v255,s[92:95],s96 offen offset:1024 sc1
ld a[136:139],v255,s[92:95],s96 offen offset:2048 sc1
ld a[140:143],v255,s[92:95],s96 offen offset:3072 sc1
ld a[144:147],v255,s[92:95],s97 offen offset:0 sc1
ld a[148:151],v255,s[92:95],s97 offen offset:1024 sc1
ld a[152:155],v255,s[92:95],s97 offen offset:2048 sc1
ld a[156:159],v255,s[92:95],s97 offen offset:3072 sc1
v_cmp_gt_i32 vcc, 0, v220
s_nop 1
v_cndmask_b32 v192, v192, v222, vcc
v_cmp_gt_i32 vcc, 1, v220
s_nop 1
v_cndmask_b32 v193, v193, v222, vcc
v_cmp_gt_i32 vcc, 2, v220
s_nop 1
v_cndmask_b32 v194, v194, v222, vcc
v_cmp_gt_i32 vcc, 3, v220
s_nop 1
v_cndmask_b32 v195, v195, v222, vcc
v_cmp_gt_i32 vcc, 16, v220
s_nop 1
v_cndmask_b32 v196, v196, v222, vcc
v_cmp_gt_i32 vcc, 17, v220
s_nop 1
v_cndmask_b32 v197, v197, v222, vcc
v_cmp_gt_i32 vcc, 18, v220
s_nop 1
v_cndmask_b32 v198, v198, v222, vcc
v_cmp_gt_i32 vcc, 19, v220
s_nop 1
v_cndmask_b32 v199, v199, v222, vcc
v_cmp_gt_i32 vcc, -16, v220
s_nop 1
v_cndmask_b32 v200, v200, v222, vcc
v_cmp_gt_i32 vcc, -15, v220
s_nop 1
v_cndmask_b32 v201, v201, v222, vcc
)ASM" R"ASM(v_cmp_gt_i32 vcc, -14, v220
s_nop 1
v_cndmask_b32 v202, v202, v222, vcc
v_cmp_gt_i32 vcc, -13, v220
s_nop 1
v_cndmask_b32 v203, v203, v222, vcc
v_cmp_gt_i32 vcc, 0, v220
s_nop 1
v_cndmask_b32 v204, v204, v222, vcc
v_cmp_gt_i32 vcc, 1, v220
s_nop 1
v_cndmask_b32 v205, v205, v222, vcc
v_cmp_gt_i32 vcc, 2, v220
s_nop 1
v_cndmask_b32 v206, v206, v222, vcc
v_cmp_gt_i32 vcc, 3, v220
s_nop 1
v_cndmask_b32 v207, v207, v222, vcc
v_max3_f32 v222, v192, v193, v216
v_max3_f32 v222, v194, v195, v222
v_max3_f32 v222, v196, v197, v222
v_max3_f32 v222, v198, v199, v222
v_mov_b32 v221, v222
s_nop 1
v_permlane16_swap_b32 v221, v222
s_nop 1
v_max_f32 v222, v222, v221
v_mov_b32 v221, v222
s_nop 1
v_permlane32_swap_b32 v221, v222
s_nop 1
v_max_f32 v222, v222, v221
v_sub_f32 v223, v216, v222
v_mov_b32 v216, v222
ex v223,v223
v_sub_f32 v192, v192, v222
v_sub_f32 v193, v193, v222
v_sub_f32 v194, v194, v222
v_sub_f32 v195, v195, v222
v_sub_f32 v196, v196, v222
v_sub_f32 v197, v197, v222
v_sub_f32 v198, v198, v222
v_sub_f32 v199, v199, v222
ex v192,v192
ex v193,v193
ex v194,v194
ex v195,v195
ex v196,v196
ex v197,v197
ex v198,v198
ex v199,v199
v_mul_f32 v218, v218, v223
v_add_f32 v218, v218, v192
v_add_f32 v218, v218, v193
v_add_f32 v218, v218, v194
v_add_f32 v218, v218, v195
v_add_f32 v218, v218, v196
v_add_f32 v218, v218, v197
v_add_f32 v218, v218, v198
v_add_f32 v218, v218, v199
cv v208,v192,v193
cv v209,v194,v195
cv v210,v196,v197
cv v211,v198,v199
v_mul_f32 v64, v64, v223
v_mul_f32 v65, v65, v223
v_mul_f32 v66, v66, v223
v_mul_f32 v67, v67, v223
v_mul_f32 v68, v68, v223
v_mul_f32 v69, v69, v223
v_mul_f32 v70, v70, v223
v_mul_f32 v71, v71, v223
v_mul_f32 v72, v72, v223
v_mul_f32 v73, v73, v223
v_mul_f32 v74, v74, v223
v_mul_f32 v75, v75, v223
v_mul_f32 v76, v76, v223
v_mul_f32 v77, v77, v223
v_mul_f32 v78, v78, v223
v_mul_f32 v79, v79, v223
v_mul_f32 v80, v80, v223
v_mul_f32 v81, v81, v223
v_mul_f32 v82, v82, v223
v_mul_f32 v83, v83, v223
v_mul_f32 v84, v84, v223
v_mul_f32 v85, v85, v223
v_mul_f32 v86, v86, v223
v_mul_f32 v87, v87, v223
v_mul_f32 v88, v88, v223
v_mul_f32 v89, v89, v223
v_mul_f32 v90, v90, v223
v_mul_f32 v91, v91, v223
v_mul_f32 v92, v92, v223
v_mul_f32 v93, v93, v223
v_mul_f32 v94, v94, v223
v_mul_f32 v95, v95, v223
v_max3_f32 v222, v200, v201, v217
v_max3_f32 v222, v202, v203, v222
v_max3_f32 v222, v204, v205, v222
v_max3_f32 v222, v206, v207, v222
)ASM" R"ASM(v_mov_b32 v221, v222
s_nop 1
v_permlane16_swap_b32 v221, v222
s_nop 1
v_max_f32 v222, v222, v221
v_mov_b32 v221, v222
s_nop 1
v_permlane32_swap_b32 v221, v222
s_nop 1
v_max_f32 v222, v222, v221
v_sub_f32 v223, v217, v222
v_mov_b32 v217, v222
ex v223,v223
v_sub_f32 v200, v200, v222
v_sub_f32 v201, v201, v222
v_sub_f32 v202, v202, v222
v_sub_f32 v203, v203, v222
v_sub_f32 v204, v204, v222
v_sub_f32 v205, v205, v222
v_sub_f32 v206, v206, v222
v_sub_f32 v207, v207, v222
ex v200,v200
ex v201,v201
ex v202,v202
ex v203,v203
ex v204,v204
ex v205,v205
ex v206,v206
ex v207,v207
v_mul_f32 v219, v219, v223
v_add_f32 v219, v219, v200
v_add_f32 v219, v219, v201
v_add_f32 v219, v219, v202
v_add_f32 v219, v219, v203
v_add_f32 v219, v219, v204
v_add_f32 v219, v219, v205
v_add_f32 v219, v219, v206
v_add_f32 v219, v219, v207
cv v212,v200,v201
cv v213,v202,v203
cv v214,v204,v205
cv v215,v206,v207
v_mul_f32 v96, v96, v223
v_mul_f32 v97, v97, v223
v_mul_f32 v98, v98, v223
v_mul_f32 v99, v99, v223
v_mul_f32 v100, v100, v223
v_mul_f32 v101, v101, v223
v_mul_f32 v102, v102, v223
v_mul_f32 v103, v103, v223
v_mul_f32 v104, v104, v223
v_mul_f32 v105, v105, v223
v_mul_f32 v106, v106, v223
v_mul_f32 v107, v107, v223
v_mul_f32 v108, v108, v223
v_mul_f32 v109, v109, v223
v_mul_f32 v110, v110, v223
v_mul_f32 v111, v111, v223
v_mul_f32 v112, v112, v223
v_mul_f32 v113, v113, v223
v_mul_f32 v114, v114, v223
v_mul_f32 v115, v115, v223
v_mul_f32 v116, v116, v223
v_mul_f32 v117, v117, v223
v_mul_f32 v118, v118, v223
v_mul_f32 v119, v119, v223
v_mul_f32 v120, v120, v223
v_mul_f32 v121, v121, v223
v_mul_f32 v122, v122, v223
v_mul_f32 v123, v123, v223
v_mul_f32 v124, v124, v223
v_mul_f32 v125, v125, v223
v_mul_f32 v126, v126, v223
v_mul_f32_e64 v127, v127, v223
wt vmcnt(0)
mm v[64:67],a[128:131],v[208:211],v[64:67]
mm v[96:99],a[128:131],v[212:215],v[96:99]
mm v[68:71],a[132:135],v[208:211],v[68:71]
mm v[100:103],a[132:135],v[212:215],v[100:103]
mm v[72:75],a[136:139],v[208:211],v[72:75]
mm v[104:107],a[136:139],v[212:215],v[104:107]
mm v[76:79],a[140:143],v[208:211],v[76:79]
mm v[108:111],a[140:143],v[212:215],v[108:111]
mm v[80:83],a[144:147],v[208:211],v[80:83]
mm v[112:115],a[144:147],v[212:215],v[112:115]
mm v[84:87],a[148:151],v[208:211],v[84:87]
mm v[116:119],a[148:151],v[212:215],v[116:119]
mm v[88:91],a[152:155],v[208:211],v[88:91]
mm v[120:123],a[152:155],v[212:215],v[120:123]
mm v[92:95],a[156:159],v[208:211],v[92:95]
mm v[124:127],a[156:159],v[212:215],v[124:127]
s_nop 7
s_nop 7
s_add_u32 s7, s7, 1
s_lshr_b32 s96, s7, 3
s_cmp_le_u32 s96, s76
s_cbranch_scc1 .La5_LA_SLOW1_L
v_mov_b32 v221, v218
s_nop 1
v_permlane16_swap_b32 v221, v218
)ASM" R"ASM(s_nop 1
v_add_f32 v218, v218, v221
v_mov_b32 v221, v218
s_nop 1
v_permlane32_swap_b32 v221, v218
s_nop 1
v_add_f32 v218, v218, v221
v_mov_b32 v221, v219
s_nop 1
v_permlane16_swap_b32 v221, v219
s_nop 1
v_add_f32 v219, v219, v221
v_mov_b32 v221, v219
s_nop 1
v_permlane32_swap_b32 v221, v219
s_nop 1
v_add_f32 v219, v219, v221
v_rcp_f32 v242, v218
v_rcp_f32 v243, v219
ar v192,a160
ar v193,a161
ar v194,a162
ar v195,a163
ar v196,a164
ar v197,a165
ar v198,a166
ar v199,a167
ar v200,a168
ar v201,a169
ar v202,a170
ar v203,a171
ar v204,a172
ar v205,a173
ar v206,a174
ar v207,a175
ar v208,a176
ar v209,a177
ar v210,a178
ar v211,a179
ar v212,a180
ar v213,a181
ar v214,a182
ar v215,a183
ar v216,a184
ar v217,a185
ar v218,a186
ar v219,a187
ar v220,a188
ar v221,a189
ar v222,a190
ar v223,a191
s_mov_b32 s95, 4096
s_mov_b32 s96, 8192
s_mov_b32 s97, 12288
s_mov_b64 vcc, 0
s_nop 1
s_setpc_b64 s[24:25]
.La5_LA_SC1:
s_call_b64 s[24:25], .La5_LA_SLOW0
s_branch .La5_LA_SR1
.La5_LA_SC2:
s_call_b64 s[24:25], .La5_LA_SLOW1
s_branch .La5_LA_SR2
.La5_LA_PS0:
s_mov_b32 s84, 3
s_mov_b32 s70, 17408
wt vmcnt(2)
ld a[220:223],v255,s[68:71],s94 offen sc1
ld a[0:3],v255,s[68:71],0 offen offset:0 sc1
ld a[4:7],v255,s[68:71],0 offen offset:1024 sc1
ld a[8:11],v255,s[68:71],0 offen offset:2048 sc1
ld a[12:15],v255,s[68:71],0 offen offset:3072 sc1
ld a[16:19],v255,s[68:71],s95 offen offset:0 sc1
ld a[20:23],v255,s[68:71],s95 offen offset:1024 sc1
ld a[24:27],v255,s[68:71],s95 offen offset:2048 sc1
s_branch .La5_LA_PS1
.La5_LA_PS2:
s_mov_b32 s84, 3
s_mov_b32 s70, 17408
wt vmcnt(0)
ld a[220:223],v255,s[68:71],s94 offen sc1
ld a[0:3],v255,s[68:71],0 offen offset:0 sc1
ld a[4:7],v255,s[68:71],0 offen offset:1024 sc1
ld a[8:11],v255,s[68:71],0 offen offset:2048 sc1
ld a[12:15],v255,s[68:71],0 offen offset:3072 sc1
ld a[16:19],v255,s[68:71],s95 offen offset:0 sc1
ld a[20:23],v255,s[68:71],s95 offen offset:1024 sc1
ld a[24:27],v255,s[68:71],s95 offen offset:2048 sc1
ld a[28:31],v255,s[68:71],s95 offen offset:3072 sc1
ld a[32:35],v255,s[68:71],s96 offen offset:0 sc1
ld a[36:39],v255,s[68:71],s96 offen offset:1024 sc1
ld a[40:43],v255,s[68:71],s96 offen offset:2048 sc1
ld a[44:47],v255,s[68:71],s96 offen offset:3072 sc1
ld a[48:51],v255,s[68:71],s97 offen offset:0 sc1
ld a[52:55],v255,s[68:71],s97 offen offset:1024 sc1
ld a[56:59],v255,s[68:71],s97 offen offset:2048 sc1
ld a[60:63],v255,s[68:71],s97 offen offset:3072 sc1
s_branch .La5_LA_PS3
.La5_LA_PSPIN:
s_mov_b32 s93, 0
)ASM" R"ASM(.La5_LA_PSPIN_L:
s_add_u32 s93, s93, 1
s_cmp_gt_u32 s93, 2097152
s_cbranch_scc1 .La5_LA_PSOK
s_sleep 8
buffer_load_dword v250, v251, s[64:67], s20 offen sc1
wt vmcnt(0)
v_cmp_eq_u32 vcc, v250, v249
s_nop 1
s_and_b32 s92, vcc_lo, 3
s_cmp_eq_u32 s92, 3
s_cbranch_scc0 .La5_LA_PSPIN_L
s_branch .La5_LA_PSOK
.La5_LA_PSOK:
s_mov_b32 s70, 17408
ld a[220:223],v255,s[68:71],s94 offen sc1
ld a[0:3],v255,s[68:71],0 offen offset:0 sc1
ld a[4:7],v255,s[68:71],0 offen offset:1024 sc1
ld a[8:11],v255,s[68:71],0 offen offset:2048 sc1
ld a[12:15],v255,s[68:71],0 offen offset:3072 sc1
ld a[16:19],v255,s[68:71],s95 offen offset:0 sc1
ld a[20:23],v255,s[68:71],s95 offen offset:1024 sc1
ld a[24:27],v255,s[68:71],s95 offen offset:2048 sc1
ld a[28:31],v255,s[68:71],s95 offen offset:3072 sc1
ld a[32:35],v255,s[68:71],s96 offen offset:0 sc1
ld a[36:39],v255,s[68:71],s96 offen offset:1024 sc1
ld a[40:43],v255,s[68:71],s96 offen offset:2048 sc1
ld a[44:47],v255,s[68:71],s96 offen offset:3072 sc1
ld a[48:51],v255,s[68:71],s97 offen offset:0 sc1
ld a[52:55],v255,s[68:71],s97 offen offset:1024 sc1
ld a[56:59],v255,s[68:71],s97 offen offset:2048 sc1
ld a[60:63],v255,s[68:71],s97 offen offset:3072 sc1
s_branch .La5_LA_DC1
.La5_LO_NB1:
.La5_LW3_6:
s_cbranch_execz .La5_LA_RETURN
s_mov_b32 s93, 0
.La5_LO_NB1_L:
s_add_u32 s93, s93, 1
s_cmp_gt_u32 s93, 2097152
s_cbranch_scc1 .La5_LO_KB1
s_sleep 8
buffer_load_dwordx2 v[250:251], v248, s[64:67], s5 offen sc1
wt vmcnt(0)
v_cmp_eq_u64 vcc, v[250:251], v[252:253]
s_nop 1
s_bitcmp1_b32 vcc_lo, 1
s_cbranch_scc0 .La5_LO_NB1_L
s_branch .La5_LO_KB1
.La5_LO_NB2:
s_mov_b32 s93, 0
.La5_LO_NB2_L:
s_add_u32 s93, s93, 1
s_cmp_gt_u32 s93, 2097152
s_nop 0
s_cbranch_scc1 .La5_LO_KB2
s_sleep 8
buffer_load_dwordx2 v[250:251], v248, s[64:67], s5 offen sc1
wt vmcnt(0)
v_cmp_eq_u64 vcc, v[250:251], v[252:253]
s_nop 1
s_bitcmp1_b32 vcc_lo, 2
s_cbranch_scc0 .La5_LO_NB2_L
s_branch .La5_LO_KB2
.La5_LO_NB3:
s_mov_b32 s93, 0
.La5_LO_NB3_L:
s_add_u32 s93, s93, 1
s_cmp_gt_u32 s93, 2097152
s_nop 0
s_nop 0
s_cbranch_scc1 .La5_LO_KB3
s_sleep 8
buffer_load_dwordx2 v[250:251], v248, s[64:67], s5 offen sc1
wt vmcnt(0)
v_cmp_eq_u64 vcc, v[250:251], v[252:253]
s_nop 1
s_bitcmp1_b32 vcc_lo, 3
s_cbranch_scc0 .La5_LO_NB3_L
s_branch .La5_LO_KB3
.La5_LO_NB4:
s_mov_b32 s93, 0
.La5_LO_NB4_L:
s_add_u32 s93, s93, 1
s_cmp_gt_u32 s93, 2097152
s_nop 0
s_nop 0
s_cbranch_scc1 .La5_LO_KB4
s_sleep 8
buffer_load_dwordx2 v[250:251], v248, s[64:67], s5 offen sc1
wt vmcnt(0)
v_cmp_eq_u64 vcc, v[250:251], v[252:253]
s_nop 1
s_bitcmp1_b32 vcc_lo, 4
s_cbranch_scc0 .La5_LO_NB4_L
s_branch .La5_LO_KB4
.La5_LO_NB5:
s_mov_b32 s93, 0
.La5_LO_NB5_L:
s_add_u32 s93, s93, 1
)ASM" R"ASM(s_cmp_gt_u32 s93, 2097152
s_nop 0
s_nop 0
s_cbranch_scc1 .La5_LO_KB5
s_sleep 8
buffer_load_dwordx2 v[250:251], v248, s[64:67], s5 offen sc1
wt vmcnt(0)
v_cmp_eq_u64 vcc, v[250:251], v[252:253]
s_nop 1
s_bitcmp1_b32 vcc_lo, 5
s_cbranch_scc0 .La5_LO_NB5_L
s_branch .La5_LO_KB5
.La5_LO_NB13:
s_mov_b32 s93, 0
.La5_LO_NB13_L:
s_add_u32 s93, s93, 1
s_cmp_gt_u32 s93, 2097152
s_nop 0
s_nop 0
s_cbranch_scc1 .La5_LO_KB13
s_sleep 8
buffer_load_dwordx2 v[250:251], v248, s[64:67], s5 offen sc1
wt vmcnt(0)
v_cmp_eq_u64 vcc, v[250:251], v[252:253]
s_nop 1
s_bitcmp1_b32 vcc_lo, 13
s_cbranch_scc0 .La5_LO_NB13_L
s_branch .La5_LO_KB13
.La5_LO_NB6:
s_mov_b32 s93, 0
.La5_LO_NB6_L:
s_add_u32 s93, s93, 1
s_cmp_gt_u32 s93, 2097152
s_nop 0
s_nop 0
s_cbranch_scc1 .La5_LO_KB6
s_sleep 8
buffer_load_dwordx2 v[250:251], v248, s[64:67], s5 offen sc1
wt vmcnt(0)
v_cmp_eq_u64 vcc, v[250:251], v[252:253]
s_nop 1
s_bitcmp1_b32 vcc_lo, 6
s_cbranch_scc0 .La5_LO_NB6_L
s_branch .La5_LO_KB6
.La5_LO_NB7:
s_mov_b32 s93, 0
.La5_LO_NB7_L:
s_add_u32 s93, s93, 1
s_cmp_gt_u32 s93, 2097152
s_nop 0
s_nop 0
s_cbranch_scc1 .La5_LO_KB7
s_sleep 8
buffer_load_dwordx2 v[250:251], v248, s[64:67], s5 offen sc1
wt vmcnt(0)
v_cmp_eq_u64 vcc, v[250:251], v[252:253]
s_nop 1
s_bitcmp1_b32 vcc_lo, 7
s_cbranch_scc0 .La5_LO_NB7_L
s_branch .La5_LO_KB7
.La5_LO_NC1:
.La5_LW7_7:
s_cbranch_execz .La5_LA_RETURN
s_mov_b32 s93, 0
.La5_LO_NC1_L:
s_add_u32 s93, s93, 1
s_cmp_gt_u32 s93, 2097152
s_nop 0
s_cbranch_scc1 .La5_LO_KC1
s_sleep 8
buffer_load_dwordx2 v[250:251], v248, s[64:67], s5 offen sc1
wt vmcnt(0)
v_cmp_eq_u64 vcc, v[250:251], v[252:253]
s_nop 1
s_bitcmp1_b32 vcc_lo, 1
s_cbranch_scc0 .La5_LO_NC1_L
s_branch .La5_LO_KC1
.La5_LO_NC2:
s_mov_b32 s93, 0
.La5_LO_NC2_L:
s_add_u32 s93, s93, 1
s_cmp_gt_u32 s93, 2097152
s_nop 0
s_nop 0
s_cbranch_scc1 .La5_LO_KC2
s_sleep 8
buffer_load_dwordx2 v[250:251], v248, s[64:67], s5 offen sc1
wt vmcnt(0)
v_cmp_eq_u64 vcc, v[250:251], v[252:253]
s_nop 1
s_bitcmp1_b32 vcc_lo, 2
s_cbranch_scc0 .La5_LO_NC2_L
s_branch .La5_LO_KC2
.La5_LO_NC3:
s_mov_b32 s93, 0
.La5_LO_NC3_L:
s_add_u32 s93, s93, 1
s_cmp_gt_u32 s93, 2097152
s_nop 0
s_nop 0
)ASM" R"ASM(s_cbranch_scc1 .La5_LO_KC3
s_sleep 8
buffer_load_dwordx2 v[250:251], v248, s[64:67], s5 offen sc1
wt vmcnt(0)
v_cmp_eq_u64 vcc, v[250:251], v[252:253]
s_nop 1
s_bitcmp1_b32 vcc_lo, 3
s_cbranch_scc0 .La5_LO_NC3_L
s_branch .La5_LO_KC3
.La5_LO_NC4:
s_mov_b32 s93, 0
.La5_LO_NC4_L:
s_add_u32 s93, s93, 1
s_cmp_gt_u32 s93, 2097152
s_nop 0
s_nop 0
s_cbranch_scc1 .La5_LO_KC4
s_sleep 8
buffer_load_dwordx2 v[250:251], v248, s[64:67], s5 offen sc1
wt vmcnt(0)
v_cmp_eq_u64 vcc, v[250:251], v[252:253]
s_nop 1
s_bitcmp1_b32 vcc_lo, 4
s_cbranch_scc0 .La5_LO_NC4_L
s_branch .La5_LO_KC4
.La5_LO_NC5:
s_mov_b32 s93, 0
.La5_LO_NC5_L:
s_add_u32 s93, s93, 1
s_cmp_gt_u32 s93, 2097152
s_nop 0
s_nop 0
s_cbranch_scc1 .La5_LO_KC5
s_sleep 8
buffer_load_dwordx2 v[250:251], v248, s[64:67], s5 offen sc1
wt vmcnt(0)
v_cmp_eq_u64 vcc, v[250:251], v[252:253]
s_nop 1
s_bitcmp1_b32 vcc_lo, 5
s_cbranch_scc0 .La5_LO_NC5_L
s_branch .La5_LO_KC5
.La5_LO_NC6:
s_mov_b32 s93, 0
.La5_LO_NC6_L:
s_add_u32 s93, s93, 1
s_cmp_gt_u32 s93, 2097152
s_nop 0
s_nop 0
s_cbranch_scc1 .La5_LO_KC6
s_sleep 8
buffer_load_dwordx2 v[250:251], v248, s[64:67], s5 offen sc1
wt vmcnt(0)
v_cmp_eq_u64 vcc, v[250:251], v[252:253]
s_nop 1
s_bitcmp1_b32 vcc_lo, 6
s_cbranch_scc0 .La5_LO_NC6_L
s_branch .La5_LO_KC6
.La5_LO_NC7:
s_mov_b32 s93, 0
.La5_LO_NC7_L:
s_add_u32 s93, s93, 1
s_cmp_gt_u32 s93, 2097152
s_nop 0
s_nop 0
s_cbranch_scc1 .La5_LO_KC7
s_sleep 8
buffer_load_dwordx2 v[250:251], v248, s[64:67], s5 offen sc1
wt vmcnt(0)
v_cmp_eq_u64 vcc, v[250:251], v[252:253]
s_nop 1
s_bitcmp1_b32 vcc_lo, 7
s_cbranch_scc0 .La5_LO_NC7_L
s_branch .La5_LO_KC7
.La5_LA_SC3:
s_call_b64 s[24:25], .La5_LA_SLOW0
aw a0,0
aw a1,0
aw a2,0
aw a3,0
aw a4,0
aw a5,0
aw a6,0
aw a7,0
aw a8,0
aw a9,0
aw a10,0
aw a11,0
aw a12,0
aw a13,0
aw a14,0
aw a15,0
aw a16,0
aw a17,0
aw a18,0
aw a19,0
aw a20,0
aw a21,0
aw a22,0
aw a23,0
aw a24,0
)ASM" R"ASM(aw a25,0
aw a26,0
aw a27,0
aw a28,0
aw a29,0
aw a30,0
aw a31,0
s_nop 1
v_mov_b32 v250, v242
v_mov_b32 v249, v243
v_xor_b32 v242, 64, v240
v_xor_b32 v243, 96, v240
ar v254,a229
s_mov_b32 s93, 0
ld a[128:131],v254,s[40:43],s93 offen
s_mov_b32 s93, 7168
ld a[132:135],v254,s[40:43],s93 offen
s_mov_b32 s93, 14336
ld a[136:139],v254,s[40:43],s93 offen
s_mov_b32 s93, 21504
ld a[140:143],v254,s[40:43],s93 offen
s_mov_b32 s93, 28672
ld a[144:147],v254,s[40:43],s93 offen
s_mov_b32 s93, 35840
ld a[148:151],v254,s[40:43],s93 offen
s_mov_b32 s93, 43008
ld a[152:155],v254,s[40:43],s93 offen
s_mov_b32 s93, 50176
ld a[156:159],v254,s[40:43],s93 offen
s_mov_b32 s93, 57344
ld a[160:163],v254,s[40:43],s93 offen
s_mov_b32 s93, 64512
ld a[164:167],v254,s[40:43],s93 offen
s_mov_b32 s93, 71680
ld a[168:171],v254,s[40:43],s93 offen
s_mov_b32 s93, 78848
ld a[172:175],v254,s[40:43],s93 offen
s_mov_b32 s93, 86016
ld a[176:179],v254,s[40:43],s93 offen
s_mov_b32 s93, 93184
ld a[180:183],v254,s[40:43],s93 offen
s_mov_b32 s93, 100352
ld a[184:187],v254,s[40:43],s93 offen
s_mov_b32 s93, 107520
ld a[188:191],v254,s[40:43],s93 offen
s_branch .La5_LA_SR3
.La5_LA_SC4:
s_call_b64 s[24:25], .La5_LA_SLOW1
aw a32,0
aw a33,0
aw a34,0
aw a35,0
aw a36,0
aw a37,0
aw a38,0
aw a39,0
aw a40,0
aw a41,0
aw a42,0
aw a43,0
aw a44,0
aw a45,0
aw a46,0
aw a47,0
aw a48,0
aw a49,0
aw a50,0
aw a51,0
aw a52,0
aw a53,0
aw a54,0
aw a55,0
aw a56,0
aw a57,0
aw a58,0
aw a59,0
aw a60,0
aw a61,0
aw a62,0
aw a63,0
s_nop 1
v_mov_b32 v250, v242
v_mov_b32 v249, v243
v_xor_b32 v242, 64, v240
v_xor_b32 v243, 96, v240
ar v254,a229
s_mov_b32 s93, 0
ld a[128:131],v254,s[40:43],s93 offen
s_mov_b32 s93, 7168
ld a[132:135],v254,s[40:43],s93 offen
s_mov_b32 s93, 14336
ld a[136:139],v254,s[40:43],s93 offen
s_mov_b32 s93, 21504
ld a[140:143],v254,s[40:43],s93 offen
s_mov_b32 s93, 28672
ld a[144:147],v254,s[40:43],s93 offen
s_mov_b32 s93, 35840
ld a[148:151],v254,s[40:43],s93 offen
s_mov_b32 s93, 43008
ld a[152:155],v254,s[40:43],s93 offen
)ASM" R"ASM(s_mov_b32 s93, 50176
ld a[156:159],v254,s[40:43],s93 offen
s_mov_b32 s93, 57344
ld a[160:163],v254,s[40:43],s93 offen
s_mov_b32 s93, 64512
ld a[164:167],v254,s[40:43],s93 offen
s_mov_b32 s93, 71680
ld a[168:171],v254,s[40:43],s93 offen
s_mov_b32 s93, 78848
ld a[172:175],v254,s[40:43],s93 offen
s_mov_b32 s93, 86016
ld a[176:179],v254,s[40:43],s93 offen
s_mov_b32 s93, 93184
ld a[180:183],v254,s[40:43],s93 offen
s_mov_b32 s93, 100352
ld a[184:187],v254,s[40:43],s93 offen
s_mov_b32 s93, 107520
ld a[188:191],v254,s[40:43],s93 offen
s_branch .La5_LA_SR4
.La5_LO_VC1:
.La5_LW5_4:
s_cbranch_execz .La5_LW5_R0
.La5_LW6_7:
s_cbranch_execz .La5_LW6_R0
s_getpc_b64 s[24:25]
.La5_LW7_P0:
s_add_u32 s24, s24, .La5_LW7_R0-.La5_LW7_P0
s_addc_u32 s25, s25, 0
s_mov_b64 exec, 0
s_cmp_eq_u32 s3, 0
s_cbranch_scc1 .La5_LW7_0
s_cmp_eq_u32 s3, 1
s_cbranch_scc1 .La5_LW7_1
s_cmp_eq_u32 s3, 2
s_cbranch_scc1 .La5_LW7_2
s_branch .La5_LW7_3
.La5_LW7_R0:
s_mov_b64 exec, -1
s_branch .La5_LO_VE1
.La5_LO_W:
s_mov_b32 s93, 3670016
ld a[192:195],v228,s[40:43],s93 offen
s_mov_b32 s93, 3677184
ld a[196:199],v228,s[40:43],s93 offen
s_mov_b32 s93, 3684352
ld a[200:203],v228,s[40:43],s93 offen
s_mov_b32 s93, 3691520
ld a[204:207],v228,s[40:43],s93 offen
s_mov_b32 s93, 3698688
ld a[208:211],v228,s[40:43],s93 offen
s_mov_b32 s93, 3705856
ld a[212:215],v228,s[40:43],s93 offen
s_mov_b32 s93, 3713024
ld a[216:219],v228,s[40:43],s93 offen
s_mov_b32 s93, 3720192
ld a[220:223],v228,s[40:43],s93 offen
s_mov_b32 s93, 3727360
ld a[224:227],v228,s[40:43],s93 offen
s_mov_b32 s93, 3734528
ld a[228:231],v228,s[40:43],s93 offen
s_mov_b32 s93, 3741696
ld a[232:235],v228,s[40:43],s93 offen
s_mov_b32 s93, 3748864
ld a[236:239],v228,s[40:43],s93 offen
s_mov_b32 s93, 3756032
ld a[240:243],v228,s[40:43],s93 offen
s_mov_b32 s93, 3763200
ld a[244:247],v228,s[40:43],s93 offen
s_mov_b32 s93, 3770368
ld a[248:251],v228,s[40:43],s93 offen
s_mov_b32 s93, 3777536
ld a[252:255],v228,s[40:43],s93 offen
s_branch .La5_LO_START
.La5_LO_VC2:
s_getpc_b64 s[24:25]
.La5_LW7_P1:
s_add_u32 s24, s24, .La5_LW7_R1-.La5_LW7_P1
s_addc_u32 s25, s25, 0
s_mov_b64 exec, 0
s_cmp_eq_u32 s3, 0
s_cbranch_scc1 .La5_LW7_0
s_cmp_eq_u32 s3, 1
s_cbranch_scc1 .La5_LW7_1
s_cmp_eq_u32 s3, 2
s_cbranch_scc1 .La5_LW7_2
s_branch .La5_LW7_3
.La5_LW7_R1:
s_mov_b64 exec, -1
s_branch .La5_LO_VE2
.La5_LT_VAR:
v_mov_b32 v230, 0
v_lshlrev_b32 v232, 16, v96
v_and_b32 v233, 0xffff0000, v96
v_sub_f32 v232, v232, v244
v_sub_f32 v233, v233, v244
v_fmac_f32 v230, v232, v232
v_fmac_f32 v230, v233, v233
v_lshlrev_b32 v232, 16, v97
v_and_b32 v233, 0xffff0000, v97
v_sub_f32 v232, v232, v244
)ASM" R"ASM(v_sub_f32 v233, v233, v244
v_fmac_f32 v230, v232, v232
v_fmac_f32 v230, v233, v233
v_lshlrev_b32 v232, 16, v98
v_and_b32 v233, 0xffff0000, v98
v_sub_f32 v232, v232, v244
v_sub_f32 v233, v233, v244
v_fmac_f32 v230, v232, v232
v_fmac_f32 v230, v233, v233
v_lshlrev_b32 v232, 16, v99
v_and_b32 v233, 0xffff0000, v99
v_sub_f32 v232, v232, v244
v_sub_f32 v233, v233, v244
v_fmac_f32 v230, v232, v232
v_fmac_f32 v230, v233, v233
v_lshlrev_b32 v232, 16, v100
v_and_b32 v233, 0xffff0000, v100
v_sub_f32 v232, v232, v244
v_sub_f32 v233, v233, v244
v_fmac_f32 v230, v232, v232
v_fmac_f32 v230, v233, v233
v_lshlrev_b32 v232, 16, v101
v_and_b32 v233, 0xffff0000, v101
v_sub_f32 v232, v232, v244
v_sub_f32 v233, v233, v244
v_fmac_f32 v230, v232, v232
v_fmac_f32 v230, v233, v233
v_lshlrev_b32 v232, 16, v102
v_and_b32 v233, 0xffff0000, v102
v_sub_f32 v232, v232, v244
v_sub_f32 v233, v233, v244
v_fmac_f32 v230, v232, v232
v_fmac_f32 v230, v233, v233
v_lshlrev_b32 v232, 16, v103
v_and_b32 v233, 0xffff0000, v103
v_sub_f32 v232, v232, v244
v_sub_f32 v233, v233, v244
v_fmac_f32 v230, v232, v232
v_fmac_f32 v230, v233, v233
v_lshlrev_b32 v232, 16, v104
v_and_b32 v233, 0xffff0000, v104
v_sub_f32 v232, v232, v244
v_sub_f32 v233, v233, v244
v_fmac_f32 v230, v232, v232
v_fmac_f32 v230, v233, v233
v_lshlrev_b32 v232, 16, v105
v_and_b32 v233, 0xffff0000, v105
v_sub_f32 v232, v232, v244
v_sub_f32 v233, v233, v244
v_fmac_f32 v230, v232, v232
v_fmac_f32 v230, v233, v233
v_lshlrev_b32 v232, 16, v106
v_and_b32 v233, 0xffff0000, v106
v_sub_f32 v232, v232, v244
v_sub_f32 v233, v233, v244
v_fmac_f32 v230, v232, v232
v_fmac_f32 v230, v233, v233
v_lshlrev_b32 v232, 16, v107
v_and_b32 v233, 0xffff0000, v107
v_sub_f32 v232, v232, v244
v_sub_f32 v233, v233, v244
v_fmac_f32 v230, v232, v232
v_fmac_f32 v230, v233, v233
v_lshlrev_b32 v232, 16, v108
v_and_b32 v233, 0xffff0000, v108
v_sub_f32 v232, v232, v244
v_sub_f32 v233, v233, v244
v_fmac_f32 v230, v232, v232
v_fmac_f32 v230, v233, v233
v_lshlrev_b32 v232, 16, v109
v_and_b32 v233, 0xffff0000, v109
v_sub_f32 v232, v232, v244
v_sub_f32 v233, v233, v244
v_fmac_f32 v230, v232, v232
v_fmac_f32 v230, v233, v233
v_lshlrev_b32 v232, 16, v110
v_and_b32 v233, 0xffff0000, v110
v_sub_f32 v232, v232, v244
v_sub_f32 v233, v233, v244
v_fmac_f32 v230, v232, v232
v_fmac_f32 v230, v233, v233
v_lshlrev_b32 v232, 16, v111
v_and_b32 v233, 0xffff0000, v111
v_sub_f32 v232, v232, v244
v_sub_f32 v233, v233, v244
v_fmac_f32 v230, v232, v232
v_fmac_f32 v230, v233, v233
s_nop 1
v_add_f32_dpp v230, v230, v230 quad_perm:[1,0,3,2] row_mask:0xf bank_mask:0xf
s_nop 1
v_add_f32_dpp v230, v230, v230 quad_perm:[2,3,0,1] row_mask:0xf bank_mask:0xf
s_nop 1
v_add_f32_dpp v230, v230, v230 row_ror:8 row_mask:0xf bank_mask:0xf
v_mov_b32 v231, v230
s_nop 1
v_permlane16_swap_b32 v231, v230
s_nop 1
v_add_f32 v230, v230, v231
v_mov_b32 v231, v230
s_nop 1
)ASM" R"ASM(v_permlane32_swap_b32 v231, v230
s_nop 1
v_add_f32 v230, v230, v231
v_mul_f32 v230, 0x3a800000, v230
s_branch .La5_LT_VOK
.La5_LT_H1:
s_add_u32 s96, s96, 1
s_lshr_b32 s93, s96, 16
s_cbranch_scc1 .La5_LT_H2
ld v[224:227],v209,s[44:47],s92 offen offset:0 sc1
ld v[232:235],v209,s[44:47],s92 offen offset:16 sc1
ld v[244:247],v209,s[44:47],s92 offen offset:1024 sc1
ld v[248:251],v209,s[44:47],s92 offen offset:1040 sc1
s_branch .La5_LT_H0
.La5_LA_KSPIN:
s_mov_b32 s93, 0
.La5_LA_KSPIN_L:
s_add_u32 s93, s93, 1
s_cmp_gt_u32 s93, 2097152
s_cbranch_scc1 .La5_LA_KOK
s_sleep 8
buffer_load_dword v250, v251, s[64:67], s20 offen sc1
wt vmcnt(0)
v_cmp_eq_u32 vcc, v250, v249
s_nop 1
s_or_b32 vcc_hi, vcc_hi, s79
s_cmp_eq_u64 vcc, -1
s_cbranch_scc0 .La5_LA_KSPIN_L
s_branch .La5_LA_KOK
.La5_LA_QSPIN:
s_mov_b32 s93, 0
.La5_LA_QSPIN_L:
s_add_u32 s93, s93, 1
s_cmp_gt_u32 s93, 2097152
s_cbranch_scc1 .La5_LA_QOK
s_sleep 8
buffer_load_dword v250, v251, s[64:67], s20 offen sc1
wt vmcnt(0)
v_cmp_eq_u32 vcc, v250, v249
s_nop 1
s_and_b32 s92, vcc_lo, 3
s_cmp_eq_u32 s92, 3
s_cbranch_scc0 .La5_LA_QSPIN_L
s_branch .La5_LA_QOK
.La5_LEND:
.La5_LCODEEND:
)ASM"
        :: "{s[0:1]}"(__builtin_amdgcn_kernarg_segment_ptr()), "{s2}"(blockIdx.x), "{s3}"(__builtin_amdgcn_readfirstlane(threadIdx.x >> 6)), "{s[4:5]}"(DBGP)
        : "v0","v1","v2","v3","v4","v5","v6","v7","v8","v9","v10","v11","v12","v13","v14","v15","v16","v17","v18","v19","v20","v21","v22","v23","v24","v25","v26","v27","v28","v29","v30","v31","v32","v33","v34","v35","v36","v37","v38","v39","v40","v41","v42","v43","v44","v45","v46","v47","v48","v49","v50","v51","v52","v53","v54","v55","v56","v57","v58","v59","v60","v61","v62","v63","v64","v65","v66","v67","v68","v69","v70","v71","v72","v73","v74","v75","v76","v77","v78","v79","v80","v81","v82","v83","v84","v85","v86","v87","v88","v89","v90","v91","v92","v93","v94","v95","v96","v97","v98","v99","v100","v101","v102","v103","v104","v105","v106","v107","v108","v109","v110","v111","v112","v113","v114","v115","v116","v117","v118","v119","v120","v121","v122","v123","v124","v125","v126","v127","v128","v129","v130","v131","v132","v133","v134","v135","v136","v137","v138","v139","v140","v141","v142","v143","v144","v145","v146","v147","v148","v149","v150","v151","v152","v153","v154","v155","v156","v157","v158","v159","v160","v161","v162","v163","v164","v165","v166","v167","v168","v169","v170","v171","v172","v173","v174","v175","v176","v177","v178","v179","v180","v181","v182","v183","v184","v185","v186","v187","v188","v189","v190","v191","v192","v193","v194","v195","v196","v197","v198","v199","v200","v201","v202","v203","v204","v205","v206","v207","v208","v209","v210","v211","v212","v213","v214","v215","v216","v217","v218","v219","v220","v221","v222","v223","v224","v225","v226","v227","v228","v229","v230","v231","v232","v233","v234","v235","v236","v237","v238","v239","v240","v241","v242","v243","v244","v245","v246","v247","v248","v249","v250","v251","v252","v253","v254","v255","a0","a1","a2","a3","a4","a5","a6","a7","a8","a9","a10","a11","a12","a13","a14","a15","a16","a17","a18","a19","a20","a21","a22","a23","a24","a25","a26","a27","a28","a29","a30","a31","a32","a33","a34","a35","a36","a37","a38","a39","a40","a41","a42","a43","a44","a45","a46","a47","a48","a49","a50","a51","a52","a53","a54","a55","a56","a57","a58","a59","a60","a61","a62","a63","a64","a65","a66","a67","a68","a69","a70","a71","a72","a73","a74","a75","a76","a77","a78","a79","a80","a81","a82","a83","a84","a85","a86","a87","a88","a89","a90","a91","a92","a93","a94","a95","a96","a97","a98","a99","a100","a101","a102","a103","a104","a105","a106","a107","a108","a109","a110","a111","a112","a113","a114","a115","a116","a117","a118","a119","a120","a121","a122","a123","a124","a125","a126","a127","a128","a129","a130","a131","a132","a133","a134","a135","a136","a137","a138","a139","a140","a141","a142","a143","a144","a145","a146","a147","a148","a149","a150","a151","a152","a153","a154","a155","a156","a157","a158","a159","a160","a161","a162","a163","a164","a165","a166","a167","a168","a169","a170","a171","a172","a173","a174","a175","a176","a177","a178","a179","a180","a181","a182","a183","a184","a185","a186","a187","a188","a189","a190","a191","a192","a193","a194","a195","a196","a197","a198","a199","a200","a201","a202","a203","a204","a205","a206","a207","a208","a209","a210","a211","a212","a213","a214","a215","a216","a217","a218","a219","a220","a221","a222","a223","a224","a225","a226","a227","a228","a229","a230","a231","a232","a233","a234","a235","a236","a237","a238","a239","a240","a241","a242","a243","a244","a245","a246","a247","a248","a249","a250","a251","a252","a253","a254","a255","s6","s7","s8","s9","s10","s11","s12","s13","s14","s15","s16","s17","s18","s19","s20","s21","s22","s23","s24","s25","s26","s27","s28","s29","s30","s31","s32","s33","s34","s35","s36","s37","s38","s39","s40","s41","s42","s43","s44","s45","s46","s47","s48","s49","s50","s51","s52","s53","s54","s55","s56","s57","s58","s59","s60","s61","s62","s63","s64","s65","s66","s67","s68","s69","s70","s71","s72","s73","s74","s75","s76","s77","s78","s79","s80","s81","s82","s83","s84","s85","s86","s87","s88","s89","s90","s91","s92","s93","s94","s95","s96","s97","s98","s99","s100","s101","vcc","scc","m0","memory");
}

extern "C" void kernel_launch(void* const* d_in, const int* in_sizes, int n_in, void* d_out, int out_size, void* d_ws, size_t ws_size,
                              hipStream_t stream) {
    (void)hipFuncSetAttribute((const void*)fa_kernel, hipFuncAttributeMaxDynamicSharedMemorySize, LDS_BYTES);
    fa_kernel<<<256, 256, LDS_BYTES, stream>>>((const bf16*)d_in[0], (const bf16*)d_in[1], (const bf16*)d_in[2], (const bf16*)d_in[3], (const bf16*)d_in[4], (char*)d_ws,
                                               (const bf16*)d_in[5], (const bf16*)d_in[8], (const bf16*)d_in[9], (const bf16*)d_in[10], (const bf16*)d_in[7], (bf16*)d_out);
}
```

```cpp
#include <hip/hip_runtime.h>
#include <hip/hip_bf16.h>
#include <stdint.h>
#pragma clang diagnostic ignored "-Winline-asm"
#pragma clang diagnostic ignored "-Woverlength-strings"

typedef __hip_bfloat16 bf16;
#define DBGP ((unsigned*)nullptr)
constexpr int LDS_BYTES = 163840;
extern "C" __global__ __attribute__((aligned(4096))) __launch_bounds__(256, 1) void fa_kernel(const bf16* __restrict__ px, const bf16* __restrict__ ckv, const bf16* __restrict__ wq,
                                                    const bf16* __restrict__ wuk, const bf16* __restrict__ wuv, char* pws,
                                                    const bf16* __restrict__ pwo, const bf16* __restrict__ pwg, const bf16* __restrict__ pwu,
                                                    const bf16* __restrict__ pwd, const bf16* __restrict__ pwr, bf16* __restrict__ pout) {
    asm volatile(R"ASM(
.macro mm x:vararg
v_mfma_f32_16x16x32_bf16 \x
.endm
.macro rt x:vararg
ds_read_b64_tr_b16 \x
.endm
.macro rd x:vararg
ds_read_b128 \x
.endm
.macro ld x:vararg
buffer_load_dwordx4 \x
.endm
.macro st x:vararg
buffer_store_dwordx4 \x
.endm
.macro ar x:vararg
v_accvgpr_read_b32 \x
.endm
.macro aw x:vararg
v_accvgpr_write_b32 \x
.endm
.macro cv x:vararg
v_cvt_pk_bf16_f32 \x
.endm
.macro ex x:vararg
v_exp_f32 \x
.endm
.macro wt x:vararg
s_waitcnt \x
.endm
; ---- roles: head = 4 xcd + (wg >> 6), j = (wg >> 3) & 7; odd j: block j >> 1 (then helps with block 7 - (j >> 1)), even j: block 7 - (j >> 1). First operands, addresses
.La5_LSTART:
s_load_dwordx16 s[16:31], s[0:1], 0
s_load_dwordx8 s[32:39], s[0:1], 64
.p2align 6
v_mbcnt_lo_u32_b32 v64, -1, 0
v_mbcnt_hi_u32_b32 v64, -1, v64
s_getpc_b64 s[92:93]
.La5_LPC:
s_sub_u32 s48, s92, .La5_LPC-.La5_LSTART
s_subb_u32 s49, s93, 0
s_and_b32 s49, s49, 0xffff
s_mov_b32 s50, .La5_LCODEEND-.La5_LSTART
s_mov_b32 s51, 0x20000
s_lshr_b32 s97, s2, 3
s_lshl2_add_u32 s87, s97, s3
v_lshlrev_b32 v133, 7, v64
s_lshl_b32 s87, s87, 13
buffer_load_dword v254, v133, s[48:51], s87 offen
s_and_b32 s92, s2, 7
s_lshr_b32 s93, s2, 6
s_lshl2_add_u32 s72, s92, s93
s_bfe_u32 s73, s2, 0x30003
s_lshr_b32 s74, s73, 1
s_xor_b32 s92, s74, 7
s_and_b32 s75, s73, 1
s_cselect_b32 s76, s74, s92
s_lshr_b32 s77, s3, 1
s_and_b32 s78, s3, 1
v_and_b32 v65, 15, v64
v_lshrrev_b32 v66, 4, v64
v_lshlrev_b32 v255, 4, v64
v_lshlrev_b32 v67, 1, v66
v_xor_b32 v67, v67, v65
v_lshlrev_b32 v67, 4, v67
v_lshl_add_u32 v67, v66, 13, v67
s_lshl_b32 s94, s78, 17
v_add_u32 v232, s94, v67
v_add_u32 v233, 32768, v232
v_add_u32 v234, 65536, v232
v_add_u32 v235, 98304, v232
v_and_b32 v67, 7, v64
v_xor_b32 v68, v67, v66
v_lshlrev_b32 v68, 4, v68
v_lshrrev_b32 v69, 3, v64
v_lshl_add_u32 v68, v69, 10, v68
s_lshl2_add_u32 s94, s3, s93
s_lshl_b32 s82, s94, 11
s_lshl_b32 s94, s94, 14
v_add_u32 v224, s94, v68
v_xor_b32 v225, 0x2040, v224
v_xor_b32 v226, 16384, v224
v_xor_b32 v227, 16384, v225
v_xor_b32 v228, 32768, v224
v_xor_b32 v229, 32768, v225
v_xor_b32 v230, 49152, v224
v_xor_b32 v231, 49152, v225
s_mul_i32 s94, s77, 8448
s_mul_i32 s95, s78, 4224
s_add_u32 s83, s94, s95
s_add_u32 s83, s83, 96256
v_lshlrev_b32 v135, 7, v67
v_lshl_add_u32 v135, v69, 13, v135
v_lshrrev_b32 v132, 5, v64
v_lshl_add_u32 v132, v132, 28, v135
v_lshl_add_u32 v133, v66, 28, v133
v_lshlrev_b32 v134, 10, v65
v_lshl_add_u32 v134, v66, 7, v134
v_lshrrev_b32 v68, 4, v64
v_lshl_add_u32 v134, v68, 28, v134
)ASM" R"ASM(s_lshl1_add_u32 s97, s97, s78
s_lshl_b32 s7, s97, 13
s_lshl_b32 s97, s97, 15
s_lshl_b32 s96, s93, 8
s_sub_u32 s7, s7, s96
s_sub_u32 s87, s87, s96
s_sub_u32 s97, s97, s96
s_add_u32 s86, s97, 2097152
s_lshl2_add_u32 s101, s72, s3
s_lshl_b32 s101, s101, 11
s_lshl2_add_u32 s100, s93, s3
s_lshl_b32 s100, s100, 14
v_lshlrev_b32 v118, 11, v67
s_lshl_b32 s93, s3, 9
v_lshl_add_u32 v119, v69, 4, v118
v_add_u32 v119, s93, v119
s_lshl_b32 s97, s2, 14
s_lshl_b32 s94, s72, 8
s_lshl_b32 s95, s76, 18
s_sub_u32 s46, 4194304, s94
s_mov_b32 s47, 0x20000
s_mov_b32 s42, 262144
s_mov_b32 s43, 0x20000
s_mov_b32 s62, 16384
s_mov_b32 s63, 0x20000
s_mov_b32 s79, 512
s_mov_b32 s80, 2097152
s_add_u32 s100, s100, 512
s_mov_b32 s81, 262144
s_cmp_eq_u32 s77, 0
wt lgkmcnt(0)
s_cselect_b64 s[92:93], s[22:23], s[24:25]
s_add_u32 s44, s92, s94
s_addc_u32 s45, s93, 0
s_and_b32 s45, s45, 0xffff
s_add_u32 s40, s18, s95
s_addc_u32 s41, s19, 0
s_and_b32 s41, s41, 0xffff
s_add_u32 s60, s16, s97
s_addc_u32 s61, s17, 0
s_and_b32 s61, s61, 0xffff
buffer_load_dword v254, v132, s[44:47], s86 offen
s_add_u32 s86, s86, 2097152
s_and_b32 s86, s86, 4194303
buffer_load_dword v254, v133, s[40:43], s101 offen
buffer_load_dword v254, v134, s[40:43], s100 offen
s_add_u32 m0, s83, 0
s_nop 0
ld v232,s[44:47],s80 offen lds
s_add_u32 m0, s83, 1024
s_nop 0
ld v233,s[44:47],s80 offen lds
s_add_u32 m0, s83, 2176
s_nop 0
ld v234,s[44:47],s80 offen lds
s_add_u32 m0, s83, 3200
s_nop 0
ld v235,s[44:47],s80 offen lds
s_add_u32 s80, s80, s81
s_and_b32 s80, s80, 4194303
v_lshrrev_b32 v67, 1, v65
v_xor_b32 v67, v67, v66
v_lshlrev_b32 v67, 4, v67
v_lshl_add_u32 v67, v65, 7, v67
s_lshl_b32 s92, s77, 12
v_add_u32 v236, s92, v67
v_xor_b32 v237, 64, v236
v_bfe_u32 v120, v64, 2, 2
v_add_u32 v67, 1, v66
v_lshrrev_b32 v67, 1, v67
v_lshlrev_b32 v67, 7, v67
v_lshl_add_u32 v67, v66, 11, v67
v_lshl_add_u32 v67, v120, 8, v67
v_and_b32 v68, 3, v64
v_lshl_add_u32 v67, v68, 3, v67
s_mul_i32 s92, s78, 8448
v_lshl_add_u32 v67, v120, 5, v67
v_add_u32 v67, s92, v67
v_add_u32 v240, 96256, v67
v_xor_b32 v241, 32, v240
v_xor_b32 v242, 64, v240
v_xor_b32 v243, 96, v240
s_add_u32 s64, s26, 142606336
s_addc_u32 s65, s27, 0
s_and_b32 s65, s65, 0xffff
s_mov_b32 s66, 3145728
s_mov_b32 s67, 0x20000
s_mov_b32 s88, 4096
s_lshr_b32 s92, s2, 1
s_lshl_b32 s92, s92, 15
s_and_b32 s93, s2, 1
s_lshl_b32 s93, s93, 7
s_add_u32 s92, s92, s93
s_lshl_b32 s93, s3, 13
s_add_u32 s92, s92, s93
s_add_u32 s92, s92, 88080384
s_add_u32 s56, s26, s92
s_addc_u32 s57, s27, 0
s_and_b32 s57, s57, 0xffff
s_mov_b32 s58, 8576
)ASM" R"ASM(s_mov_b32 s59, 0x20000
v_and_b32 v71, 0x380, v255
v_and_b32 v127, 0x70, v255
v_lshl_add_u32 v71, v71, 1, v127
v_xor_b32 v67, 3, v69
v_min_u32 v67, 1, v67
v_xor_b32 v67, 1, v67
s_mov_b32 s93, 0
v_mul_lo_u32 v67, v67, s93
v_add_u32 v136, v67, v255
v_and_b32 v67, 3, v69
v_xor_b32 v67, 1, v67
v_min_u32 v67, 1, v67
v_xor_b32 v67, 1, v67
v_and_b32 v67, s2, v67
v_mul_lo_u32 v67, v67, s93
v_add_u32 v71, v71, v67
s_lshl_b32 s92, s3, 3
v_lshlrev_b32 v126, 1, v127
v_or_b32 v126, 65536, v126
v_add_u32 v127, s92, v126
s_add_u32 s52, s26, 171966464
s_addc_u32 s53, s27, 0
s_and_b32 s53, s53, 0xffff
s_mov_b32 s54, 2097152
s_mov_b32 s55, 0x20000
s_lshl2_add_u32 s95, s2, s3
s_lshl_b32 s91, s95, 3
s_lshl_b32 s95, s95, 10
s_lshl_b32 s89, s76, 10
v_lshl_add_u32 v135, v69, 28, v135
s_mov_b32 s96, s87
s_mov_b32 s84, 33792
s_mov_b32 s85, 0
s_add_u32 s97, s83, 67584
s_mov_b32 s90, s83
s_lshl_b32 s92, s72, 8
s_sub_u32 s93, 8388608, s92
s_add_u32 s68, s20, s92
s_addc_u32 s69, s21, 0
s_and_b32 s69, s69, 0xffff
s_mov_b32 s70, s93
s_mov_b32 s71, 0x20000
s_lshr_b32 s92, s20, 7
s_sub_u32 s92, 3, s92
s_bfe_u32 s99, s92, 0x20001
s_lshr_b32 s93, s2, 6
s_sub_u32 s99, s99, s93
s_lshl_b32 s92, s99, 8
s_lshr_b32 s93, s2, 3
s_lshl2_add_u32 s93, s93, s3
s_lshl_b32 s98, s93, 16
s_add_u32 s98, s98, s92
v_lshlrev_b32 v138, 4, v65
v_lshl_add_u32 v137, v66, 13, v138
v_lshl_add_u32 v138, v66, 9, v138
s_lshl_b32 s93, s93, 12
v_add_u32 v138, s93, v138
s_and_b32 s93, s2, 7
s_lshl_b32 s92, s93, 19
s_add_u32 s92, s92, 138412032
s_add_u32 s4, s26, s92
s_addc_u32 s5, s27, 0
s_and_b32 s5, s5, 0xffff
s_mov_b32 s6, 0
s_mov_b32 s100, 1073741824
s_add_u32 s101, s91, 65536
v_lshrrev_b32 v139, 1, v64
v_lshlrev_b32 v139, 8, v139
v_and_or_b32 v139, v255, 16, v139
s_lshl_b32 s93, s93, 5
s_add_u32 s93, s93, 64512
v_add_u32 v139, s93, v139
s_mov_b64 exec, 0
s_cmp_eq_u32 s3, 0
s_cbranch_scc1 .La5_LW1_0
s_cmp_eq_u32 s3, 1
s_cbranch_scc1 .La5_LW1_1
s_cmp_eq_u32 s3, 2
s_cbranch_scc1 .La5_LW1_2
s_branch .La5_LW1_3
.La5_LW1_R0:
s_mov_b64 exec, -1
s_xor_b32 m0, s82, 0
s_nop 0
ld v224,s[40:43],s79 offen lds
s_xor_b32 m0, s82, 1024
s_nop 0
ld v225,s[40:43],s79 offen lds
s_xor_b32 m0, s82, 2048
s_nop 0
ld v226,s[40:43],s79 offen lds
s_xor_b32 m0, s82, 3072
s_nop 0
ld v227,s[40:43],s79 offen lds
s_xor_b32 m0, s82, 4096
s_nop 0
ld v228,s[40:43],s79 offen lds
s_xor_b32 m0, s82, 5120
s_nop 0
)ASM" R"ASM(ld v229,s[40:43],s79 offen lds
s_xor_b32 m0, s82, 6144
s_nop 0
ld v230,s[40:43],s79 offen lds
s_xor_b32 m0, s82, 7168
s_nop 0
ld v231,s[40:43],s79 offen lds
s_add_u32 s79, s79, 128
s_and_b32 s79, s79, 1023
s_add_u32 m0, s83, 16896
s_nop 0
ld v232,s[44:47],s80 offen lds
s_add_u32 m0, s83, 17920
s_nop 0
ld v233,s[44:47],s80 offen lds
s_add_u32 m0, s83, 19072
s_nop 0
ld v234,s[44:47],s80 offen lds
s_add_u32 m0, s83, 20096
s_nop 0
ld v235,s[44:47],s80 offen lds
s_add_u32 s80, s80, s81
s_and_b32 s80, s80, 4194303
buffer_load_dwordx2 v[252:253], off, s[64:67], 0 sc1
wt vmcnt(1)
s_barrier
rd v[160:163],v236 offset:24576
rd v[164:167],v237 offset:24576
rt v[192:193],v240 offset:0
rt v[194:195],v240 offset:1024
rt v[196:197],v240 offset:16896
rt v[198:199],v240 offset:17920
rt v[200:201],v241 offset:0
rt v[202:203],v241 offset:1024
rt v[204:205],v241 offset:16896
rt v[206:207],v241 offset:17920
rt v[208:209],v242 offset:0
rt v[210:211],v242 offset:1024
rt v[212:213],v242 offset:16896
rt v[214:215],v242 offset:17920
rt v[216:217],v243 offset:0
rt v[218:219],v243 offset:1024
rt v[220:221],v243 offset:16896
rt v[222:223],v243 offset:17920
rd v[168:171],v236 offset:26624
rd v[172:175],v237 offset:26624
rd v[176:179],v236 offset:16384
rd v[180:183],v237 offset:16384
rd v[184:187],v236 offset:18432
rd v[188:191],v237 offset:18432
wt lgkmcnt(0)
s_cmp_eq_u32 s78, 1
s_cbranch_scc1 .La5_LV_START
; ---- K tile (waves 0, 2): 256 keys x 128 columns, K = 512; LayerNorm of 8 rows of x in the gaps (softmax scale folded in)
.La5_LK_START:
mm a[96:99],v[192:195],v[160:163],0
s_add_u32 m0, s83, 33792
mm a[96:99],v[196:199],v[164:167],a[96:99]
ld v232,s[44:47],s80 offen lds
mm a[100:103],v[204:207],v[164:167],0
rd v[0:3],v236 offset:8192
rd v[4:7],v237 offset:8192
s_add_u32 m0, s83, 34816
mm a[100:103],v[200:203],v[160:163],a[100:103]
rd v[8:11],v236 offset:10240
rd v[12:15],v237 offset:10240
ld v233,s[44:47],s80 offen lds
mm a[104:107],v[208:211],v[160:163],0
s_add_u32 m0, s83, 35968
mm a[104:107],v[212:215],v[164:167],a[104:107]
ld v234,s[44:47],s80 offen lds
mm a[108:111],v[220:223],v[164:167],0
rd v[16:19],v236 offset:0
rd v[20:23],v237 offset:0
s_add_u32 m0, s83, 36992
mm a[108:111],v[216:219],v[160:163],a[108:111]
rd v[24:27],v236 offset:2048
rd v[28:31],v237 offset:2048
ld v235,s[44:47],s80 offen lds
mm a[140:143],v[216:219],v[168:171],0
s_mov_b32 s80, 2883584
mm a[140:143],v[220:223],v[172:175],a[140:143]
mm a[136:139],v[212:215],v[172:175],0
s_xor_b32 m0, s82, 32768
mm a[136:139],v[208:211],v[168:171],a[136:139]
rt v[32:33],v240 offset:128
rt v[34:35],v240 offset:1152
ld v224,s[40:43],s79 offen lds
mm a[124:127],v[200:203],v[168:171],0
s_xor_b32 m0, s82, 33792
mm a[124:127],v[204:207],v[172:175],a[124:127]
ld v225,s[40:43],s79 offen lds
mm a[120:123],v[196:199],v[172:175],0
s_xor_b32 m0, s82, 34816
mm a[120:123],v[192:195],v[168:171],a[120:123]
rt v[36:37],v240 offset:17024
rt v[38:39],v240 offset:18048
ld v226,s[40:43],s79 offen lds
mm a[232:235],v[192:195],v[176:179],0
s_xor_b32 m0, s82, 35840
)ASM" R"ASM(mm a[232:235],v[196:199],v[180:183],a[232:235]
ld v227,s[40:43],s79 offen lds
mm a[236:239],v[204:207],v[180:183],0
s_xor_b32 m0, s82, 36864
mm a[236:239],v[200:203],v[176:179],a[236:239]
rt v[40:41],v241 offset:128
rt v[42:43],v241 offset:1152
ld v228,s[40:43],s79 offen lds
mm a[152:155],v[208:211],v[176:179],0
s_xor_b32 m0, s82, 37888
mm a[152:155],v[212:215],v[180:183],a[152:155]
ld v229,s[40:43],s79 offen lds
mm a[156:159],v[220:223],v[180:183],0
s_xor_b32 m0, s82, 38912
mm a[156:159],v[216:219],v[176:179],a[156:159]
ld v230,s[40:43],s79 offen lds
mm a[172:175],v[216:219],v[184:187],0
s_xor_b32 m0, s82, 39936
mm a[172:175],v[220:223],v[188:191],a[172:175]
ld v231,s[40:43],s79 offen lds
mm a[168:171],v[212:215],v[188:191],0
s_mov_b32 s79, 768
mm a[168:171],v[208:211],v[184:187],a[168:171]
rt v[44:45],v241 offset:17024
rt v[46:47],v241 offset:18048
mm a[220:223],v[200:203],v[184:187],0
s_add_u32 m0, s83, 50688
mm a[220:223],v[204:207],v[188:191],a[220:223]
ld v232,s[44:47],s80 offen lds
mm a[216:219],v[196:199],v[188:191],0
wt lgkmcnt(8)
s_add_u32 m0, s83, 51712
mm a[216:219],v[192:195],v[184:187],a[216:219]
ld v233,s[44:47],s80 offen lds
mm a[200:203],v[192:195],v[0:3],0
s_add_u32 m0, s83, 52864
mm a[200:203],v[196:199],v[4:7],a[200:203]
ld v234,s[44:47],s80 offen lds
mm a[204:207],v[204:207],v[4:7],0
s_add_u32 m0, s83, 53888
mm a[204:207],v[200:203],v[0:3],a[204:207]
rt v[48:49],v242 offset:128
rt v[50:51],v242 offset:1152
ld v235,s[44:47],s80 offen lds
mm a[184:187],v[208:211],v[0:3],0
s_mov_b32 s80, 3145728
mm a[184:187],v[212:215],v[4:7],a[184:187]
mm a[188:191],v[220:223],v[4:7],0
mm a[188:191],v[216:219],v[0:3],a[188:191]
mm a[12:15],v[216:219],v[8:11],0
mm a[12:15],v[220:223],v[12:15],a[12:15]
buffer_load_dword v70, v118, s[60:63], 0 offen
mm a[8:11],v[212:215],v[12:15],0
mm a[8:11],v[208:211],v[8:11],a[8:11]
rt v[52:53],v242 offset:17024
rt v[54:55],v242 offset:18048
mm a[76:79],v[200:203],v[8:11],0
mm a[76:79],v[204:207],v[12:15],a[76:79]
ld v[72:75],v119,s[60:63],0 offen offset:0
mm a[72:75],v[196:199],v[12:15],0
mm a[72:75],v[192:195],v[8:11],a[72:75]
rt v[56:57],v243 offset:128
rt v[58:59],v243 offset:1152
mm a[88:91],v[192:195],v[16:19],0
mm a[88:91],v[196:199],v[20:23],a[88:91]
ld v[76:79],v119,s[60:63],0 offen offset:128
mm a[92:95],v[204:207],v[20:23],0
mm a[92:95],v[200:203],v[16:19],a[92:95]
rt v[60:61],v243 offset:17024
rt v[62:63],v243 offset:18048
mm a[24:27],v[208:211],v[16:19],0
mm a[24:27],v[212:215],v[20:23],a[24:27]
ld v[80:83],v119,s[60:63],0 offen offset:256
mm a[28:31],v[220:223],v[20:23],0
mm a[28:31],v[216:219],v[16:19],a[28:31]
mm a[44:47],v[216:219],v[24:27],0
mm a[44:47],v[220:223],v[28:31],a[44:47]
ld v[84:87],v119,s[60:63],0 offen offset:384
mm a[40:43],v[212:215],v[28:31],0
mm a[40:43],v[208:211],v[24:27],a[40:43]
mm a[252:255],v[200:203],v[24:27],0
mm a[252:255],v[204:207],v[28:31],a[252:255]
mm a[248:251],v[196:199],v[28:31],0
mm a[248:251],v[192:195],v[24:27],a[248:251]
s_mov_b64 exec, 0
s_cmp_eq_u32 s3, 0
s_cbranch_scc1 .La5_LW2_0
s_branch .La5_LW2_2
.La5_LW2_R0:
s_mov_b64 exec, -1
wt vmcnt(5) lgkmcnt(0)
s_barrier
mm a[112:115],v[32:35],v[160:163],0
mm a[112:115],v[36:39],v[164:167],a[112:115]
mm a[116:119],v[44:47],v[164:167],0
rt v[192:193],v240 offset:33792
rt v[194:195],v240 offset:34816
mm a[116:119],v[40:43],v[160:163],a[116:119]
rt v[196:197],v240 offset:50688
rt v[198:199],v240 offset:51712
)ASM" R"ASM(mm a[128:131],v[48:51],v[160:163],0
mm a[128:131],v[52:55],v[164:167],a[128:131]
mm a[132:135],v[60:63],v[164:167],0
mm a[132:135],v[56:59],v[160:163],a[132:135]
rd v[160:163],v236 offset:57344
rd v[164:167],v237 offset:57344
mm a[148:151],v[56:59],v[168:171],0
mm a[148:151],v[60:63],v[172:175],a[148:151]
mm a[144:147],v[52:55],v[172:175],0
mm a[144:147],v[48:51],v[168:171],a[144:147]
rt v[200:201],v241 offset:33792
rt v[202:203],v241 offset:34816
mm a[228:231],v[40:43],v[168:171],0
mm a[228:231],v[44:47],v[172:175],a[228:231]
mm a[224:227],v[36:39],v[172:175],0
mm a[224:227],v[32:35],v[168:171],a[224:227]
rd v[168:171],v236 offset:59392
rd v[172:175],v237 offset:59392
mm a[208:211],v[32:35],v[176:179],0
s_xor_b32 m0, s82, 0
mm a[208:211],v[36:39],v[180:183],a[208:211]
ld v224,s[40:43],s79 offen lds
mm a[212:215],v[44:47],v[180:183],0
mm a[212:215],v[40:43],v[176:179],a[212:215]
rt v[204:205],v241 offset:50688
rt v[206:207],v241 offset:51712
s_xor_b32 m0, s82, 1024
mm a[160:163],v[48:51],v[176:179],0
ld v225,s[40:43],s79 offen lds
mm a[160:163],v[52:55],v[180:183],a[160:163]
mm a[164:167],v[60:63],v[180:183],0
s_xor_b32 m0, s82, 2048
mm a[164:167],v[56:59],v[176:179],a[164:167]
rd v[176:179],v236 offset:49152
rd v[180:183],v237 offset:49152
ld v226,s[40:43],s79 offen lds
mm a[180:183],v[56:59],v[184:187],0
mm a[180:183],v[60:63],v[188:191],a[180:183]
s_xor_b32 m0, s82, 3072
mm a[176:179],v[52:55],v[188:191],0
ld v227,s[40:43],s79 offen lds
mm a[176:179],v[48:51],v[184:187],a[176:179]
rt v[208:209],v242 offset:33792
rt v[210:211],v242 offset:34816
mm a[196:199],v[40:43],v[184:187],0
s_xor_b32 m0, s82, 4096
mm a[196:199],v[44:47],v[188:191],a[196:199]
ld v228,s[40:43],s79 offen lds
mm a[192:195],v[36:39],v[188:191],0
mm a[192:195],v[32:35],v[184:187],a[192:195]
rd v[184:187],v236 offset:51200
rd v[188:191],v237 offset:51200
s_xor_b32 m0, s82, 5120
mm a[64:67],v[32:35],v[0:3],0
ld v229,s[40:43],s79 offen lds
mm a[64:67],v[36:39],v[4:7],a[64:67]
mm a[68:71],v[44:47],v[4:7],0
s_xor_b32 m0, s82, 6144
mm a[68:71],v[40:43],v[0:3],a[68:71]
rt v[212:213],v242 offset:50688
rt v[214:215],v242 offset:51712
ld v230,s[40:43],s79 offen lds
mm a[0:3],v[48:51],v[0:3],0
mm a[0:3],v[52:55],v[4:7],a[0:3]
s_xor_b32 m0, s82, 7168
mm a[4:7],v[60:63],v[4:7],0
ld v231,s[40:43],s79 offen lds
mm a[4:7],v[56:59],v[0:3],a[4:7]
rd v[0:3],v236 offset:40960
rd v[4:7],v237 offset:40960
s_mov_b32 s79, 896
mm a[20:23],v[56:59],v[8:11],0
mm a[20:23],v[60:63],v[12:15],a[20:23]
s_add_u32 m0, s83, 0
mm a[16:19],v[52:55],v[12:15],0
ld v232,s[44:47],s80 offen lds
mm a[16:19],v[48:51],v[8:11],a[16:19]
rt v[216:217],v243 offset:33792
rt v[218:219],v243 offset:34816
mm a[84:87],v[40:43],v[8:11],0
s_add_u32 m0, s83, 1024
mm a[84:87],v[44:47],v[12:15],a[84:87]
ld v233,s[44:47],s80 offen lds
mm a[80:83],v[36:39],v[12:15],0
wt vmcnt(10)
v_lshlrev_b32 v70, 16, v70
mm a[80:83],v[32:35],v[8:11],a[80:83]
rd v[8:11],v236 offset:43008
rd v[12:15],v237 offset:43008
s_add_u32 m0, s83, 2176
mm a[240:243],v[32:35],v[16:19],0
ld v234,s[44:47],s80 offen lds
mm a[240:243],v[36:39],v[20:23],a[240:243]
v_lshlrev_b32 v88, 16, v72
v_and_b32 v89, 0xffff0000, v72
mm a[244:247],v[44:47],v[20:23],0
s_add_u32 m0, s83, 3200
v_sub_f32 v88, v88, v70
mm a[244:247],v[40:43],v[16:19],a[244:247]
rt v[220:221],v243 offset:50688
)ASM" R"ASM(rt v[222:223],v243 offset:51712
ld v235,s[44:47],s80 offen lds
mm a[32:35],v[48:51],v[16:19],0
s_mov_b32 s80, 3407872
v_sub_f32 v89, v89, v70
mm a[32:35],v[52:55],v[20:23],a[32:35]
v_add_f32 v120, v88, v89
v_mul_f32 v122, v88, v88
mm a[36:39],v[60:63],v[20:23],0
s_add_u32 m0, s83, 16896
v_mul_f32 v123, v89, v89
mm a[36:39],v[56:59],v[16:19],a[36:39]
rd v[16:19],v236 offset:32768
rd v[20:23],v237 offset:32768
ld v232,s[44:47],s80 offen lds
mm a[60:63],v[56:59],v[24:27],0
v_lshlrev_b32 v90, 16, v73
v_and_b32 v91, 0xffff0000, v73
mm a[60:63],v[60:63],v[28:31],a[60:63]
s_add_u32 m0, s83, 17920
v_sub_f32 v90, v90, v70
mm a[56:59],v[52:55],v[28:31],0
ld v233,s[44:47],s80 offen lds
mm a[56:59],v[48:51],v[24:27],a[56:59]
v_sub_f32 v91, v91, v70
v_add_f32 v124, v90, v91
mm a[52:55],v[40:43],v[24:27],0
s_add_u32 m0, s83, 19072
v_fmac_f32 v122, v90, v90
mm a[52:55],v[44:47],v[28:31],a[52:55]
ld v234,s[44:47],s80 offen lds
mm a[48:51],v[36:39],v[28:31],0
wt lgkmcnt(2)
v_add_f32 v120, v120, v124
mm a[48:51],v[32:35],v[24:27],a[48:51]
rd v[24:27],v236 offset:34816
rd v[28:31],v237 offset:34816
s_add_u32 m0, s83, 20096
mm a[96:99],v[192:195],v[160:163],a[96:99]
ld v235,s[44:47],s80 offen lds
.La5_LW1_0:
s_cbranch_execz .La5_LW1_4
mm a[96:99],v[196:199],v[164:167],a[96:99]
s_mov_b32 s80, 3670016
v_fmac_f32 v123, v91, v91
mm a[100:103],v[204:207],v[164:167],a[100:103]
v_lshlrev_b32 v92, 16, v74
v_and_b32 v93, 0xffff0000, v74
mm a[100:103],v[200:203],v[160:163],a[100:103]
rt v[32:33],v240 offset:33920
rt v[34:35],v240 offset:34944
mm a[104:107],v[208:211],v[160:163],a[104:107]
v_sub_f32 v92, v92, v70
v_sub_f32 v93, v93, v70
mm a[104:107],v[212:215],v[164:167],a[104:107]
v_add_f32 v124, v92, v93
v_fmac_f32 v122, v92, v92
mm a[108:111],v[220:223],v[164:167],a[108:111]
wt lgkmcnt(2)
s_barrier
mm a[108:111],v[216:219],v[160:163],a[108:111]
v_add_f32 v120, v120, v124
v_fmac_f32 v123, v93, v93
mm a[140:143],v[216:219],v[168:171],a[140:143]
s_xor_b32 m0, s82, 32768
v_lshlrev_b32 v94, 16, v75
mm a[140:143],v[220:223],v[172:175],a[140:143]
ld v224,s[40:43],s79 offen lds
mm a[136:139],v[212:215],v[172:175],a[136:139]
v_and_b32 v95, 0xffff0000, v75
v_sub_f32 v94, v94, v70
mm a[136:139],v[208:211],v[168:171],a[136:139]
rt v[36:37],v240 offset:50816
rt v[38:39],v240 offset:51840
mm a[124:127],v[200:203],v[168:171],a[124:127]
s_xor_b32 m0, s82, 33792
v_sub_f32 v95, v95, v70
mm a[124:127],v[204:207],v[172:175],a[124:127]
ld v225,s[40:43],s79 offen lds
mm a[120:123],v[196:199],v[172:175],a[120:123]
v_add_f32 v124, v94, v95
v_fmac_f32 v122, v94, v94
mm a[120:123],v[192:195],v[168:171],a[120:123]
v_add_f32 v120, v120, v124
v_fmac_f32 v123, v95, v95
mm a[232:235],v[192:195],v[176:179],a[232:235]
s_xor_b32 m0, s82, 34816
v_lshlrev_b32 v96, 16, v76
mm a[232:235],v[196:199],v[180:183],a[232:235]
ld v226,s[40:43],s79 offen lds
mm a[236:239],v[204:207],v[180:183],a[236:239]
v_and_b32 v97, 0xffff0000, v76
v_sub_f32 v96, v96, v70
mm a[236:239],v[200:203],v[176:179],a[236:239]
rt v[40:41],v241 offset:33920
rt v[42:43],v241 offset:34944
mm a[152:155],v[208:211],v[176:179],a[152:155]
s_xor_b32 m0, s82, 35840
v_sub_f32 v97, v97, v70
mm a[152:155],v[212:215],v[180:183],a[152:155]
)ASM" R"ASM(ld v227,s[40:43],s79 offen lds
mm a[156:159],v[220:223],v[180:183],a[156:159]
v_add_f32 v124, v96, v97
v_fmac_f32 v122, v96, v96
mm a[156:159],v[216:219],v[176:179],a[156:159]
v_add_f32 v120, v120, v124
v_fmac_f32 v123, v97, v97
mm a[172:175],v[216:219],v[184:187],a[172:175]
s_xor_b32 m0, s82, 36864
v_lshlrev_b32 v98, 16, v77
mm a[172:175],v[220:223],v[188:191],a[172:175]
ld v228,s[40:43],s79 offen lds
mm a[168:171],v[212:215],v[188:191],a[168:171]
v_and_b32 v99, 0xffff0000, v77
v_sub_f32 v98, v98, v70
mm a[168:171],v[208:211],v[184:187],a[168:171]
rt v[44:45],v241 offset:50816
rt v[46:47],v241 offset:51840
mm a[220:223],v[200:203],v[184:187],a[220:223]
s_xor_b32 m0, s82, 37888
v_sub_f32 v99, v99, v70
mm a[220:223],v[204:207],v[188:191],a[220:223]
ld v229,s[40:43],s79 offen lds
mm a[216:219],v[196:199],v[188:191],a[216:219]
v_add_f32 v124, v98, v99
v_fmac_f32 v122, v98, v98
mm a[216:219],v[192:195],v[184:187],a[216:219]
v_add_f32 v120, v120, v124
v_fmac_f32 v123, v99, v99
mm a[200:203],v[192:195],v[0:3],a[200:203]
s_xor_b32 m0, s82, 38912
v_lshlrev_b32 v100, 16, v78
mm a[200:203],v[196:199],v[4:7],a[200:203]
ld v230,s[40:43],s79 offen lds
mm a[204:207],v[204:207],v[4:7],a[204:207]
v_and_b32 v101, 0xffff0000, v78
v_sub_f32 v100, v100, v70
mm a[204:207],v[200:203],v[0:3],a[204:207]
rt v[48:49],v242 offset:33920
rt v[50:51],v242 offset:34944
mm a[184:187],v[208:211],v[0:3],a[184:187]
s_xor_b32 m0, s82, 39936
v_sub_f32 v101, v101, v70
mm a[184:187],v[212:215],v[4:7],a[184:187]
ld v231,s[40:43],s79 offen lds
mm a[188:191],v[220:223],v[4:7],a[188:191]
s_mov_b32 s79, 0
v_add_f32 v124, v100, v101
mm a[188:191],v[216:219],v[0:3],a[188:191]
v_fmac_f32 v122, v100, v100
v_add_f32 v120, v120, v124
mm a[12:15],v[216:219],v[8:11],a[12:15]
v_fmac_f32 v123, v101, v101
v_lshlrev_b32 v102, 16, v79
mm a[12:15],v[220:223],v[12:15],a[12:15]
buffer_load_dword v254, v132, s[44:47], s86 offen
mm a[8:11],v[212:215],v[12:15],a[8:11]
s_add_u32 s86, s86, 2097152
s_and_b32 s86, s86, 4194303
mm a[8:11],v[208:211],v[8:11],a[8:11]
rt v[52:53],v242 offset:50816
rt v[54:55],v242 offset:51840
mm a[76:79],v[200:203],v[8:11],a[76:79]
v_and_b32 v103, 0xffff0000, v79
v_sub_f32 v102, v102, v70
mm a[76:79],v[204:207],v[12:15],a[76:79]
v_sub_f32 v103, v103, v70
v_add_f32 v124, v102, v103
mm a[72:75],v[196:199],v[12:15],a[72:75]
v_fmac_f32 v122, v102, v102
v_add_f32 v120, v120, v124
mm a[72:75],v[192:195],v[8:11],a[72:75]
rt v[56:57],v243 offset:33920
rt v[58:59],v243 offset:34944
mm a[88:91],v[192:195],v[16:19],a[88:91]
v_fmac_f32 v123, v103, v103
v_lshlrev_b32 v104, 16, v80
mm a[88:91],v[196:199],v[20:23],a[88:91]
v_and_b32 v105, 0xffff0000, v80
v_sub_f32 v104, v104, v70
mm a[92:95],v[204:207],v[20:23],a[92:95]
v_sub_f32 v105, v105, v70
v_add_f32 v124, v104, v105
mm a[92:95],v[200:203],v[16:19],a[92:95]
rt v[60:61],v243 offset:50816
rt v[62:63],v243 offset:51840
mm a[24:27],v[208:211],v[16:19],a[24:27]
v_fmac_f32 v122, v104, v104
v_add_f32 v120, v120, v124
mm a[24:27],v[212:215],v[20:23],a[24:27]
v_fmac_f32 v123, v105, v105
v_lshlrev_b32 v106, 16, v81
mm a[28:31],v[220:223],v[20:23],a[28:31]
v_and_b32 v107, 0xffff0000, v81
v_sub_f32 v106, v106, v70
mm a[28:31],v[216:219],v[16:19],a[28:31]
v_sub_f32 v107, v107, v70
v_add_f32 v124, v106, v107
mm a[44:47],v[216:219],v[24:27],a[44:47]
v_fmac_f32 v122, v106, v106
)ASM" R"ASM(v_add_f32 v120, v120, v124
mm a[44:47],v[220:223],v[28:31],a[44:47]
v_fmac_f32 v123, v107, v107
v_lshlrev_b32 v108, 16, v82
mm a[40:43],v[212:215],v[28:31],a[40:43]
v_and_b32 v109, 0xffff0000, v82
v_sub_f32 v108, v108, v70
mm a[40:43],v[208:211],v[24:27],a[40:43]
v_sub_f32 v109, v109, v70
v_add_f32 v124, v108, v109
mm a[252:255],v[200:203],v[24:27],a[252:255]
v_fmac_f32 v122, v108, v108
v_add_f32 v120, v120, v124
mm a[252:255],v[204:207],v[28:31],a[252:255]
v_fmac_f32 v123, v109, v109
v_lshlrev_b32 v110, 16, v83
mm a[248:251],v[196:199],v[28:31],a[248:251]
wt vmcnt(9) lgkmcnt(0)
s_barrier
mm a[248:251],v[192:195],v[24:27],a[248:251]
v_and_b32 v111, 0xffff0000, v83
v_sub_f32 v110, v110, v70
mm a[112:115],v[32:35],v[160:163],a[112:115]
v_sub_f32 v111, v111, v70
v_add_f32 v124, v110, v111
mm a[112:115],v[36:39],v[164:167],a[112:115]
v_fmac_f32 v122, v110, v110
v_add_f32 v120, v120, v124
mm a[116:119],v[44:47],v[164:167],a[116:119]
rt v[192:193],v240 offset:0
rt v[194:195],v240 offset:1024
mm a[116:119],v[40:43],v[160:163],a[116:119]
rt v[196:197],v240 offset:16896
rt v[198:199],v240 offset:17920
mm a[128:131],v[48:51],v[160:163],a[128:131]
v_fmac_f32 v123, v111, v111
v_lshlrev_b32 v112, 16, v84
mm a[128:131],v[52:55],v[164:167],a[128:131]
v_and_b32 v113, 0xffff0000, v84
v_sub_f32 v112, v112, v70
mm a[132:135],v[60:63],v[164:167],a[132:135]
v_sub_f32 v113, v113, v70
v_add_f32 v124, v112, v113
mm a[132:135],v[56:59],v[160:163],a[132:135]
rd v[160:163],v236 offset:24576
rd v[164:167],v237 offset:24576
mm a[148:151],v[56:59],v[168:171],a[148:151]
v_fmac_f32 v122, v112, v112
v_add_f32 v120, v120, v124
mm a[148:151],v[60:63],v[172:175],a[148:151]
v_fmac_f32 v123, v113, v113
v_lshlrev_b32 v114, 16, v85
mm a[144:147],v[52:55],v[172:175],a[144:147]
v_and_b32 v115, 0xffff0000, v85
v_sub_f32 v114, v114, v70
mm a[144:147],v[48:51],v[168:171],a[144:147]
rt v[200:201],v241 offset:0
rt v[202:203],v241 offset:1024
mm a[228:231],v[40:43],v[168:171],a[228:231]
v_sub_f32 v115, v115, v70
v_add_f32 v124, v114, v115
mm a[228:231],v[44:47],v[172:175],a[228:231]
v_fmac_f32 v122, v114, v114
v_add_f32 v120, v120, v124
mm a[224:227],v[36:39],v[172:175],a[224:227]
v_fmac_f32 v123, v115, v115
v_lshlrev_b32 v116, 16, v86
mm a[224:227],v[32:35],v[168:171],a[224:227]
rd v[168:171],v236 offset:26624
rd v[172:175],v237 offset:26624
mm a[208:211],v[32:35],v[176:179],a[208:211]
v_and_b32 v117, 0xffff0000, v86
v_sub_f32 v116, v116, v70
mm a[208:211],v[36:39],v[180:183],a[208:211]
v_sub_f32 v117, v117, v70
v_add_f32 v124, v116, v117
mm a[212:215],v[44:47],v[180:183],a[212:215]
v_fmac_f32 v122, v116, v116
v_add_f32 v120, v120, v124
mm a[212:215],v[40:43],v[176:179],a[212:215]
rt v[204:205],v241 offset:16896
rt v[206:207],v241 offset:17920
mm a[160:163],v[48:51],v[176:179],a[160:163]
v_fmac_f32 v123, v117, v117
v_lshlrev_b32 v118, 16, v87
mm a[160:163],v[52:55],v[180:183],a[160:163]
v_and_b32 v119, 0xffff0000, v87
v_sub_f32 v118, v118, v70
mm a[164:167],v[60:63],v[180:183],a[164:167]
v_sub_f32 v119, v119, v70
v_add_f32 v124, v118, v119
mm a[164:167],v[56:59],v[176:179],a[164:167]
rd v[176:179],v236 offset:16384
rd v[180:183],v237 offset:16384
mm a[180:183],v[56:59],v[184:187],a[180:183]
v_fmac_f32 v122, v118, v118
v_add_f32 v120, v120, v124
mm a[180:183],v[60:63],v[188:191],a[180:183]
v_fmac_f32 v123, v119, v119
v_add_f32 v121, v122, v123
)ASM" R"ASM(mm a[176:179],v[52:55],v[188:191],a[176:179]
v_add_f32_dpp v120, v120, v120 row_ror:8 row_mask:0xf bank_mask:0xf
mm a[176:179],v[48:51],v[184:187],a[176:179]
rt v[208:209],v242 offset:0
rt v[210:211],v242 offset:1024
mm a[196:199],v[40:43],v[184:187],a[196:199]
s_add_u32 m0, s83, 33792
v_add_f32_dpp v121, v121, v121 row_ror:8 row_mask:0xf bank_mask:0xf
mm a[196:199],v[44:47],v[188:191],a[196:199]
ld v232,s[44:47],s80 offen lds
mm a[192:195],v[36:39],v[188:191],a[192:195]
v_mov_b32 v124, v120
v_mov_b32 v125, v121
mm a[192:195],v[32:35],v[184:187],a[192:195]
rd v[184:187],v236 offset:18432
rd v[188:191],v237 offset:18432
mm a[64:67],v[32:35],v[0:3],a[64:67]
s_add_u32 m0, s83, 34816
v_permlane16_swap_b32 v124, v120
mm a[64:67],v[36:39],v[4:7],a[64:67]
ld v233,s[44:47],s80 offen lds
mm a[68:71],v[44:47],v[4:7],a[68:71]
v_permlane16_swap_b32 v125, v121
mm a[68:71],v[40:43],v[0:3],a[68:71]
rt v[212:213],v242 offset:16896
rt v[214:215],v242 offset:17920
mm a[0:3],v[48:51],v[0:3],a[0:3]
s_add_u32 m0, s83, 35968
v_add_f32 v120, v120, v124
mm a[0:3],v[52:55],v[4:7],a[0:3]
ld v234,s[44:47],s80 offen lds
mm a[4:7],v[60:63],v[4:7],a[4:7]
v_add_f32 v121, v121, v125
v_mov_b32 v124, v120
mm a[4:7],v[56:59],v[0:3],a[4:7]
rd v[0:3],v236 offset:8192
rd v[4:7],v237 offset:8192
mm a[20:23],v[56:59],v[8:11],a[20:23]
s_add_u32 m0, s83, 36992
v_mov_b32 v125, v121
mm a[20:23],v[60:63],v[12:15],a[20:23]
ld v235,s[44:47],s80 offen lds
mm a[16:19],v[52:55],v[12:15],a[16:19]
s_mov_b32 s80, 3932160
v_permlane32_swap_b32 v124, v120
mm a[16:19],v[48:51],v[8:11],a[16:19]
rt v[216:217],v243 offset:0
rt v[218:219],v243 offset:1024
mm a[84:87],v[40:43],v[8:11],a[84:87]
s_add_u32 m0, s83, 50688
v_permlane32_swap_b32 v125, v121
mm a[84:87],v[44:47],v[12:15],a[84:87]
ld v232,s[44:47],s80 offen lds
mm a[80:83],v[36:39],v[12:15],a[80:83]
v_add_f32 v120, v120, v124
v_add_f32 v121, v121, v125
mm a[80:83],v[32:35],v[8:11],a[80:83]
rd v[8:11],v236 offset:10240
rd v[12:15],v237 offset:10240
mm a[240:243],v[32:35],v[16:19],a[240:243]
s_add_u32 m0, s83, 51712
ds_write_b64 v127, v[120:121]
mm a[240:243],v[36:39],v[20:23],a[240:243]
ld v233,s[44:47],s80 offen lds
mm a[244:247],v[44:47],v[20:23],a[244:247]
s_nop 0
v_readfirstlane_b32 s92, v252
mm a[244:247],v[40:43],v[16:19],a[244:247]
rt v[220:221],v243 offset:16896
rt v[222:223],v243 offset:17920
mm a[32:35],v[48:51],v[16:19],a[32:35]
s_add_u32 m0, s83, 52864
v_readfirstlane_b32 s93, v253
mm a[32:35],v[52:55],v[20:23],a[32:35]
ld v234,s[44:47],s80 offen lds
mm a[36:39],v[60:63],v[20:23],a[36:39]
s_add_u32 s92, s92, 1
s_addc_u32 s93, s93, 0
mm a[36:39],v[56:59],v[16:19],a[36:39]
rd v[16:19],v236 offset:0
rd v[20:23],v237 offset:0
mm a[60:63],v[56:59],v[24:27],a[60:63]
s_add_u32 m0, s83, 53888
v_mov_b32 v252, s92
mm a[60:63],v[60:63],v[28:31],a[60:63]
ld v235,s[44:47],s80 offen lds
mm a[56:59],v[52:55],v[28:31],a[56:59]
s_mov_b32 s80, 0
v_mov_b32 v253, s93
mm a[56:59],v[48:51],v[24:27],a[56:59]
v_mov_b32 v128, 0
v_mov_b32 v129, 0
mm a[52:55],v[40:43],v[24:27],a[52:55]
v_mov_b32 v130, 0
v_mov_b32 v131, 0
mm a[52:55],v[44:47],v[28:31],a[52:55]
st v[128:131],v255,s[52:55],s95 offen sc1
mm a[48:51],v[36:39],v[28:31],a[48:51]
wt lgkmcnt(2)
s_xor_b32 s93, s72, 0
)ASM" R"ASM(mm a[48:51],v[32:35],v[24:27],a[48:51]
rd v[24:27],v236 offset:2048
rd v[28:31],v237 offset:2048
mm a[96:99],v[192:195],v[160:163],a[96:99]
s_lshl_b32 s92, s76, 5
s_add_u32 s92, s92, s93
mm a[96:99],v[196:199],v[164:167],a[96:99]
s_lshl2_add_u32 s92, s92, s3
s_lshl_b32 s92, s92, 10
mm a[100:103],v[204:207],v[164:167],a[100:103]
s_add_u32 s92, s92, 1048576
v_mov_b32 v128, -1
mm a[100:103],v[200:203],v[160:163],a[100:103]
rt v[32:33],v240 offset:128
rt v[34:35],v240 offset:1152
mm a[104:107],v[208:211],v[160:163],a[104:107]
v_mov_b32 v129, -1
v_mov_b32 v130, -1
mm a[104:107],v[212:215],v[164:167],a[104:107]
v_mov_b32 v131, -1
st v[128:131],v255,s[52:55],s92 offen sc1
mm a[108:111],v[220:223],v[164:167],a[108:111]
wt lgkmcnt(2)
s_barrier
mm a[108:111],v[216:219],v[160:163],a[108:111]
rd v[72:75],v126
rd v[76:79],v126 offset:16
mm a[140:143],v[216:219],v[168:171],a[140:143]
s_xor_b32 m0, s82, 0
mm a[140:143],v[220:223],v[172:175],a[140:143]
ld v224,s[40:43],s79 offen lds
mm a[136:139],v[212:215],v[172:175],a[136:139]
mm a[136:139],v[208:211],v[168:171],a[136:139]
rt v[36:37],v240 offset:17024
rt v[38:39],v240 offset:18048
mm a[124:127],v[200:203],v[168:171],a[124:127]
s_xor_b32 m0, s82, 1024
mm a[124:127],v[204:207],v[172:175],a[124:127]
ld v225,s[40:43],s79 offen lds
mm a[120:123],v[196:199],v[172:175],a[120:123]
wt lgkmcnt(2)
v_add_f32 v72, v72, v74
mm a[120:123],v[192:195],v[168:171],a[120:123]
v_add_f32 v73, v73, v75
v_add_f32 v76, v76, v78
mm a[232:235],v[192:195],v[176:179],a[232:235]
s_xor_b32 m0, s82, 2048
v_add_f32 v77, v77, v79
mm a[232:235],v[196:199],v[180:183],a[232:235]
ld v226,s[40:43],s79 offen lds
mm a[236:239],v[204:207],v[180:183],a[236:239]
v_add_f32 v72, v72, v76
v_add_f32 v73, v73, v77
mm a[236:239],v[200:203],v[176:179],a[236:239]
rt v[40:41],v241 offset:128
rt v[42:43],v241 offset:1152
mm a[152:155],v[208:211],v[176:179],a[152:155]
s_xor_b32 m0, s82, 3072
v_mul_f32 v72, 0x3a800000, v72
mm a[152:155],v[212:215],v[180:183],a[152:155]
ld v227,s[40:43],s79 offen lds
mm a[156:159],v[220:223],v[180:183],a[156:159]
v_mul_f32 v73, 0x3a800000, v73
v_add_f32 v124, v70, v72
mm a[156:159],v[216:219],v[176:179],a[156:159]
v_fma_f32 v125, -v72, v72, v73
cv v124,v124,v124
mm a[172:175],v[216:219],v[184:187],a[172:175]
s_xor_b32 m0, s82, 4096
v_lshlrev_b32 v124, 16, v124
mm a[172:175],v[220:223],v[188:191],a[172:175]
ld v228,s[40:43],s79 offen lds
mm a[168:171],v[212:215],v[188:191],a[168:171]
v_max_f32 v125, 0, v125
cv v125,v125,v125
mm a[168:171],v[208:211],v[184:187],a[168:171]
rt v[44:45],v241 offset:17024
rt v[46:47],v241 offset:18048
mm a[220:223],v[200:203],v[184:187],a[220:223]
s_xor_b32 m0, s82, 5120
v_lshlrev_b32 v125, 16, v125
mm a[220:223],v[204:207],v[188:191],a[220:223]
ld v229,s[40:43],s79 offen lds
mm a[216:219],v[196:199],v[188:191],a[216:219]
v_add_f32 v125, 0x3727c5ac, v125
cv v125,v125,v125
mm a[216:219],v[192:195],v[184:187],a[216:219]
v_lshlrev_b32 v125, 16, v125
mm a[200:203],v[192:195],v[0:3],a[200:203]
s_xor_b32 m0, s82, 6144
mm a[200:203],v[196:199],v[4:7],a[200:203]
ld v230,s[40:43],s79 offen lds
mm a[204:207],v[204:207],v[4:7],a[204:207]
v_sqrt_f32 v125, v125
mm a[204:207],v[200:203],v[0:3],a[204:207]
rt v[48:49],v242 offset:128
rt v[50:51],v242 offset:1152
mm a[184:187],v[208:211],v[0:3],a[184:187]
s_xor_b32 m0, s82, 7168
v_sub_f32 v124, v70, v124
)ASM" R"ASM(mm a[184:187],v[212:215],v[4:7],a[184:187]
ld v231,s[40:43],s79 offen lds
mm a[188:191],v[220:223],v[4:7],a[188:191]
s_mov_b32 s79, 128
cv v125,v125,v125
mm a[188:191],v[216:219],v[0:3],a[188:191]
v_lshlrev_b32 v125, 16, v125
mm a[12:15],v[216:219],v[8:11],a[12:15]
v_rcp_f32 v125, v125
mm a[12:15],v[220:223],v[12:15],a[12:15]
v_mul_f32 v125, 0x3e0293ee, v125
v_mul_f32 v124, v124, v125
mm a[8:11],v[212:215],v[12:15],a[8:11]
v_fma_f32 v88, v88, v125, v124
v_fma_f32 v89, v89, v125, v124
mm a[8:11],v[208:211],v[8:11],a[8:11]
rt v[52:53],v242 offset:17024
rt v[54:55],v242 offset:18048
mm a[76:79],v[200:203],v[8:11],a[76:79]
cv v72,v88,v89
v_fma_f32 v90, v90, v125, v124
mm a[76:79],v[204:207],v[12:15],a[76:79]
v_fma_f32 v91, v91, v125, v124
cv v73,v90,v91
mm a[72:75],v[196:199],v[12:15],a[72:75]
v_fma_f32 v92, v92, v125, v124
v_fma_f32 v93, v93, v125, v124
mm a[72:75],v[192:195],v[8:11],a[72:75]
rt v[56:57],v243 offset:128
rt v[58:59],v243 offset:1152
mm a[88:91],v[192:195],v[16:19],a[88:91]
cv v74,v92,v93
v_fma_f32 v94, v94, v125, v124
mm a[88:91],v[196:199],v[20:23],a[88:91]
v_fma_f32 v95, v95, v125, v124
cv v75,v94,v95
mm a[92:95],v[204:207],v[20:23],a[92:95]
st v[72:75],v71,s[56:59],0 offen offset:0 sc1
mm a[92:95],v[200:203],v[16:19],a[92:95]
rt v[60:61],v243 offset:17024
rt v[62:63],v243 offset:18048
mm a[24:27],v[208:211],v[16:19],a[24:27]
v_fma_f32 v96, v96, v125, v124
v_fma_f32 v97, v97, v125, v124
mm a[24:27],v[212:215],v[20:23],a[24:27]
cv v76,v96,v97
v_fma_f32 v98, v98, v125, v124
mm a[28:31],v[220:223],v[20:23],a[28:31]
v_fma_f32 v99, v99, v125, v124
cv v77,v98,v99
mm a[28:31],v[216:219],v[16:19],a[28:31]
v_fma_f32 v100, v100, v125, v124
v_fma_f32 v101, v101, v125, v124
mm a[44:47],v[216:219],v[24:27],a[44:47]
cv v78,v100,v101
v_fma_f32 v102, v102, v125, v124
mm a[44:47],v[220:223],v[28:31],a[44:47]
v_fma_f32 v103, v103, v125, v124
cv v79,v102,v103
mm a[40:43],v[212:215],v[28:31],a[40:43]
st v[76:79],v71,s[56:59],0 offen offset:2048 sc1
mm a[40:43],v[208:211],v[24:27],a[40:43]
v_fma_f32 v104, v104, v125, v124
v_fma_f32 v105, v105, v125, v124
mm a[252:255],v[200:203],v[24:27],a[252:255]
cv v80,v104,v105
v_fma_f32 v106, v106, v125, v124
mm a[252:255],v[204:207],v[28:31],a[252:255]
v_fma_f32 v107, v107, v125, v124
cv v81,v106,v107
mm a[248:251],v[196:199],v[28:31],a[248:251]
wt vmcnt(12) lgkmcnt(0)
s_barrier
mm a[248:251],v[192:195],v[24:27],a[248:251]
v_fma_f32 v108, v108, v125, v124
v_fma_f32 v109, v109, v125, v124
mm a[112:115],v[32:35],v[160:163],a[112:115]
cv v82,v108,v109
v_fma_f32 v110, v110, v125, v124
mm a[112:115],v[36:39],v[164:167],a[112:115]
v_fma_f32 v111, v111, v125, v124
cv v83,v110,v111
mm a[116:119],v[44:47],v[164:167],a[116:119]
rt v[192:193],v240 offset:33792
rt v[194:195],v240 offset:34816
mm a[116:119],v[40:43],v[160:163],a[116:119]
rt v[196:197],v240 offset:50688
rt v[198:199],v240 offset:51712
mm a[128:131],v[48:51],v[160:163],a[128:131]
st v[80:83],v71,s[56:59],s88 offen offset:0 sc1
mm a[128:131],v[52:55],v[164:167],a[128:131]
v_fma_f32 v112, v112, v125, v124
v_fma_f32 v113, v113, v125, v124
mm a[132:135],v[60:63],v[164:167],a[132:135]
cv v84,v112,v113
v_fma_f32 v114, v114, v125, v124
mm a[132:135],v[56:59],v[160:163],a[132:135]
rd v[160:163],v236 offset:57344
rd v[164:167],v237 offset:57344
mm a[148:151],v[56:59],v[168:171],a[148:151]
)ASM" R"ASM(v_fma_f32 v115, v115, v125, v124
cv v85,v114,v115
mm a[148:151],v[60:63],v[172:175],a[148:151]
v_fma_f32 v116, v116, v125, v124
v_fma_f32 v117, v117, v125, v124
mm a[144:147],v[52:55],v[172:175],a[144:147]
cv v86,v116,v117
v_fma_f32 v118, v118, v125, v124
mm a[144:147],v[48:51],v[168:171],a[144:147]
rt v[200:201],v241 offset:33792
rt v[202:203],v241 offset:34816
mm a[228:231],v[40:43],v[168:171],a[228:231]
v_fma_f32 v119, v119, v125, v124
cv v87,v118,v119
mm a[228:231],v[44:47],v[172:175],a[228:231]
st v[84:87],v71,s[56:59],s88 offen offset:2048 sc1
mm a[224:227],v[36:39],v[172:175],a[224:227]
mm a[224:227],v[32:35],v[168:171],a[224:227]
rd v[168:171],v236 offset:59392
rd v[172:175],v237 offset:59392
mm a[208:211],v[32:35],v[176:179],a[208:211]
mm a[208:211],v[36:39],v[180:183],a[208:211]
mm a[212:215],v[44:47],v[180:183],a[212:215]
mm a[212:215],v[40:43],v[176:179],a[212:215]
rt v[204:205],v241 offset:50688
rt v[206:207],v241 offset:51712
mm a[160:163],v[48:51],v[176:179],a[160:163]
mm a[160:163],v[52:55],v[180:183],a[160:163]
mm a[164:167],v[60:63],v[180:183],a[164:167]
mm a[164:167],v[56:59],v[176:179],a[164:167]
rd v[176:179],v236 offset:49152
rd v[180:183],v237 offset:49152
mm a[180:183],v[56:59],v[184:187],a[180:183]
mm a[180:183],v[60:63],v[188:191],a[180:183]
.La5_LW1_1:
s_cbranch_execz .La5_LW1_5
mm a[176:179],v[52:55],v[188:191],a[176:179]
mm a[176:179],v[48:51],v[184:187],a[176:179]
rt v[208:209],v242 offset:33792
rt v[210:211],v242 offset:34816
mm a[196:199],v[40:43],v[184:187],a[196:199]
s_add_u32 m0, s83, 0
mm a[196:199],v[44:47],v[188:191],a[196:199]
ld v232,s[44:47],s80 offen lds
mm a[192:195],v[36:39],v[188:191],a[192:195]
mm a[192:195],v[32:35],v[184:187],a[192:195]
rd v[184:187],v236 offset:51200
rd v[188:191],v237 offset:51200
mm a[64:67],v[32:35],v[0:3],a[64:67]
s_add_u32 m0, s83, 1024
mm a[64:67],v[36:39],v[4:7],a[64:67]
ld v233,s[44:47],s80 offen lds
mm a[68:71],v[44:47],v[4:7],a[68:71]
mm a[68:71],v[40:43],v[0:3],a[68:71]
rt v[212:213],v242 offset:50688
rt v[214:215],v242 offset:51712
mm a[0:3],v[48:51],v[0:3],a[0:3]
s_add_u32 m0, s83, 2176
mm a[0:3],v[52:55],v[4:7],a[0:3]
ld v234,s[44:47],s80 offen lds
mm a[4:7],v[60:63],v[4:7],a[4:7]
mm a[4:7],v[56:59],v[0:3],a[4:7]
rd v[0:3],v236 offset:40960
rd v[4:7],v237 offset:40960
mm a[20:23],v[56:59],v[8:11],a[20:23]
s_add_u32 m0, s83, 3200
mm a[20:23],v[60:63],v[12:15],a[20:23]
ld v235,s[44:47],s80 offen lds
mm a[16:19],v[52:55],v[12:15],a[16:19]
s_mov_b32 s80, 262144
mm a[16:19],v[48:51],v[8:11],a[16:19]
rt v[216:217],v243 offset:33792
rt v[218:219],v243 offset:34816
mm a[84:87],v[40:43],v[8:11],a[84:87]
s_add_u32 m0, s83, 16896
mm a[84:87],v[44:47],v[12:15],a[84:87]
ld v232,s[44:47],s80 offen lds
mm a[80:83],v[36:39],v[12:15],a[80:83]
mm a[80:83],v[32:35],v[8:11],a[80:83]
rd v[8:11],v236 offset:43008
rd v[12:15],v237 offset:43008
mm a[240:243],v[32:35],v[16:19],a[240:243]
s_add_u32 m0, s83, 17920
mm a[240:243],v[36:39],v[20:23],a[240:243]
ld v233,s[44:47],s80 offen lds
mm a[244:247],v[44:47],v[20:23],a[244:247]
mm a[244:247],v[40:43],v[16:19],a[244:247]
rt v[220:221],v243 offset:50688
rt v[222:223],v243 offset:51712
mm a[32:35],v[48:51],v[16:19],a[32:35]
s_add_u32 m0, s83, 19072
mm a[32:35],v[52:55],v[20:23],a[32:35]
ld v234,s[44:47],s80 offen lds
mm a[36:39],v[60:63],v[20:23],a[36:39]
mm a[36:39],v[56:59],v[16:19],a[36:39]
rd v[16:19],v236 offset:32768
rd v[20:23],v237 offset:32768
mm a[60:63],v[56:59],v[24:27],a[60:63]
s_add_u32 m0, s83, 20096
mm a[60:63],v[60:63],v[28:31],a[60:63]
)ASM" R"ASM(ld v235,s[44:47],s80 offen lds
mm a[56:59],v[52:55],v[28:31],a[56:59]
s_mov_b32 s80, 524288
mm a[56:59],v[48:51],v[24:27],a[56:59]
mm a[52:55],v[40:43],v[24:27],a[52:55]
mm a[52:55],v[44:47],v[28:31],a[52:55]
mm a[48:51],v[36:39],v[28:31],a[48:51]
wt lgkmcnt(2)
mm a[48:51],v[32:35],v[24:27],a[48:51]
rd v[24:27],v236 offset:34816
rd v[28:31],v237 offset:34816
mm a[96:99],v[192:195],v[160:163],a[96:99]
mm a[96:99],v[196:199],v[164:167],a[96:99]
mm a[100:103],v[204:207],v[164:167],a[100:103]
mm a[100:103],v[200:203],v[160:163],a[100:103]
rt v[32:33],v240 offset:33920
rt v[34:35],v240 offset:34944
mm a[104:107],v[208:211],v[160:163],a[104:107]
mm a[104:107],v[212:215],v[164:167],a[104:107]
mm a[108:111],v[220:223],v[164:167],a[108:111]
wt lgkmcnt(2)
s_barrier
mm a[108:111],v[216:219],v[160:163],a[108:111]
mm a[140:143],v[216:219],v[168:171],a[140:143]
s_xor_b32 m0, s82, 32768
mm a[140:143],v[220:223],v[172:175],a[140:143]
ld v224,s[40:43],s79 offen lds
mm a[136:139],v[212:215],v[172:175],a[136:139]
mm a[136:139],v[208:211],v[168:171],a[136:139]
rt v[36:37],v240 offset:50816
rt v[38:39],v240 offset:51840
mm a[124:127],v[200:203],v[168:171],a[124:127]
s_xor_b32 m0, s82, 33792
mm a[124:127],v[204:207],v[172:175],a[124:127]
ld v225,s[40:43],s79 offen lds
mm a[120:123],v[196:199],v[172:175],a[120:123]
mm a[120:123],v[192:195],v[168:171],a[120:123]
mm a[232:235],v[192:195],v[176:179],a[232:235]
s_xor_b32 m0, s82, 34816
mm a[232:235],v[196:199],v[180:183],a[232:235]
ld v226,s[40:43],s79 offen lds
mm a[236:239],v[204:207],v[180:183],a[236:239]
mm a[236:239],v[200:203],v[176:179],a[236:239]
rt v[40:41],v241 offset:33920
rt v[42:43],v241 offset:34944
mm a[152:155],v[208:211],v[176:179],a[152:155]
s_xor_b32 m0, s82, 35840
mm a[152:155],v[212:215],v[180:183],a[152:155]
ld v227,s[40:43],s79 offen lds
mm a[156:159],v[220:223],v[180:183],a[156:159]
mm a[156:159],v[216:219],v[176:179],a[156:159]
mm a[172:175],v[216:219],v[184:187],a[172:175]
s_xor_b32 m0, s82, 36864
mm a[172:175],v[220:223],v[188:191],a[172:175]
ld v228,s[40:43],s79 offen lds
mm a[168:171],v[212:215],v[188:191],a[168:171]
mm a[168:171],v[208:211],v[184:187],a[168:171]
rt v[44:45],v241 offset:50816
rt v[46:47],v241 offset:51840
mm a[220:223],v[200:203],v[184:187],a[220:223]
s_xor_b32 m0, s82, 37888
mm a[220:223],v[204:207],v[188:191],a[220:223]
ld v229,s[40:43],s79 offen lds
mm a[216:219],v[196:199],v[188:191],a[216:219]
mm a[216:219],v[192:195],v[184:187],a[216:219]
mm a[200:203],v[192:195],v[0:3],a[200:203]
s_xor_b32 m0, s82, 38912
mm a[200:203],v[196:199],v[4:7],a[200:203]
ld v230,s[40:43],s79 offen lds
mm a[204:207],v[204:207],v[4:7],a[204:207]
mm a[204:207],v[200:203],v[0:3],a[204:207]
rt v[48:49],v242 offset:33920
rt v[50:51],v242 offset:34944
mm a[184:187],v[208:211],v[0:3],a[184:187]
s_xor_b32 m0, s82, 39936
mm a[184:187],v[212:215],v[4:7],a[184:187]
ld v231,s[40:43],s79 offen lds
mm a[188:191],v[220:223],v[4:7],a[188:191]
s_mov_b32 s79, 256
mm a[188:191],v[216:219],v[0:3],a[188:191]
mm a[12:15],v[216:219],v[8:11],a[12:15]
mm a[12:15],v[220:223],v[12:15],a[12:15]
buffer_load_dword v254, v135, s[68:71], s96 offen
mm a[8:11],v[212:215],v[12:15],a[8:11]
s_add_u32 s96, s96, 1048576
mm a[8:11],v[208:211],v[8:11],a[8:11]
rt v[52:53],v242 offset:50816
rt v[54:55],v242 offset:51840
mm a[76:79],v[200:203],v[8:11],a[76:79]
mm a[76:79],v[204:207],v[12:15],a[76:79]
buffer_load_dword v254, v135, s[68:71], s96 offen
mm a[72:75],v[196:199],v[12:15],a[72:75]
s_add_u32 s96, s96, 1048576
mm a[72:75],v[192:195],v[8:11],a[72:75]
rt v[56:57],v243 offset:33920
rt v[58:59],v243 offset:34944
mm a[88:91],v[192:195],v[16:19],a[88:91]
mm a[88:91],v[196:199],v[20:23],a[88:91]
wt vmcnt(18)
buffer_store_dwordx2 v[252:253], off, s[64:67], s91 offset:1024 sc1
)ASM" R"ASM(mm a[92:95],v[204:207],v[20:23],a[92:95]
s_mov_b32 s91, 0
s_lshl_b32 s92, s76, 19
mm a[92:95],v[200:203],v[16:19],a[92:95]
rt v[60:61],v243 offset:50816
rt v[62:63],v243 offset:51840
mm a[24:27],v[208:211],v[16:19],a[24:27]
s_add_u32 s92, s92, 88080384
mm a[24:27],v[212:215],v[20:23],a[24:27]
s_add_u32 s52, s26, s92
s_addc_u32 s53, s27, 0
mm a[28:31],v[220:223],v[20:23],a[28:31]
s_and_b32 s53, s53, 0xffff
s_mov_b32 s54, 524672
mm a[28:31],v[216:219],v[16:19],a[28:31]
s_mov_b32 s55, 0x20000
s_lshl_b32 s92, s3, 16
mm a[44:47],v[216:219],v[24:27],a[44:47]
s_sub_u32 s93, 458752, s92
v_add_u32 v244, s92, v136
mm a[44:47],v[220:223],v[28:31],a[44:47]
v_add_u32 v246, s93, v136
v_add_u32 v245, 0x8000, v244
mm a[40:43],v[212:215],v[28:31],a[40:43]
v_add_u32 v247, 0x8000, v246
s_lshl_b32 s94, s72, 19
mm a[40:43],v[208:211],v[24:27],a[40:43]
s_lshl_b32 s93, s76, 16
s_add_u32 s92, s94, s93
mm a[252:255],v[200:203],v[24:27],a[252:255]
s_lshl_b32 s93, s77, 13
s_add_u32 s92, s92, s93
mm a[252:255],v[204:207],v[28:31],a[252:255]
s_add_u32 s92, s92, 20971520
mm a[248:251],v[196:199],v[28:31],a[248:251]
wt vmcnt(11) lgkmcnt(0)
s_barrier
mm a[248:251],v[192:195],v[24:27],a[248:251]
s_add_u32 s48, s26, s92
s_addc_u32 s49, s27, 0
mm a[112:115],v[32:35],v[160:163],a[112:115]
s_and_b32 s49, s49, 0xffff
s_mov_b32 s50, 57344
mm a[112:115],v[36:39],v[164:167],a[112:115]
s_mov_b32 s51, 0x20000
s_lshl_b32 s93, s77, 24
mm a[116:119],v[44:47],v[164:167],a[116:119]
rt v[192:193],v240 offset:0
rt v[194:195],v240 offset:1024
mm a[116:119],v[40:43],v[160:163],a[116:119]
rt v[196:197],v240 offset:16896
rt v[198:199],v240 offset:17920
mm a[128:131],v[48:51],v[160:163],a[128:131]
s_add_u32 s92, s94, s93
s_add_u32 s92, s92, 20971520
mm a[128:131],v[52:55],v[164:167],a[128:131]
s_add_u32 s60, s26, s92
s_addc_u32 s61, s27, 0
mm a[132:135],v[60:63],v[164:167],a[132:135]
s_and_b32 s61, s61, 0xffff
s_mov_b32 s62, 524288
mm a[132:135],v[56:59],v[160:163],a[132:135]
rd v[160:163],v236 offset:24576
rd v[164:167],v237 offset:24576
mm a[148:151],v[56:59],v[168:171],a[148:151]
s_mov_b32 s63, 0x20000
s_lshl_b32 s92, s77, 14
mm a[148:151],v[60:63],v[172:175],a[148:151]
s_sub_u32 s95, 16384, s92
v_add_u32 v238, s95, v255
mm a[144:147],v[52:55],v[172:175],a[144:147]
s_lshr_b32 s100, s2, 6
s_lshl_b32 s100, s100, 7
mm a[144:147],v[48:51],v[168:171],a[144:147]
rt v[200:201],v241 offset:0
rt v[202:203],v241 offset:1024
mm a[228:231],v[40:43],v[168:171],a[228:231]
s_and_b32 s92, s2, 63
s_add_u32 s100, s100, s92
mm a[228:231],v[44:47],v[172:175],a[228:231]
s_lshl_b32 s100, s100, 7
s_lshl_b32 s92, s3, 28
mm a[224:227],v[36:39],v[172:175],a[224:227]
s_add_u32 s100, s100, s92
s_add_u32 s100, s100, 2621440
mm a[224:227],v[32:35],v[168:171],a[224:227]
rd v[168:171],v236 offset:26624
rd v[172:175],v237 offset:26624
mm a[208:211],v[32:35],v[176:179],a[208:211]
s_add_u32 s101, s100, 8192
s_lshl1_add_u32 s92, s3, 157440
mm a[208:211],v[36:39],v[180:183],a[208:211]
v_mov_b32 v76, s92
mm a[212:215],v[44:47],v[180:183],a[212:215]
mm a[212:215],v[40:43],v[176:179],a[212:215]
rt v[204:205],v241 offset:16896
rt v[206:207],v241 offset:17920
mm a[160:163],v[48:51],v[176:179],a[160:163]
mm a[160:163],v[52:55],v[180:183],a[160:163]
mm a[164:167],v[60:63],v[180:183],a[164:167]
)ASM" R"ASM(mm a[164:167],v[56:59],v[176:179],a[164:167]
rd v[176:179],v236 offset:16384
rd v[180:183],v237 offset:16384
mm a[180:183],v[56:59],v[184:187],a[180:183]
mm a[180:183],v[60:63],v[188:191],a[180:183]
mm a[176:179],v[52:55],v[188:191],a[176:179]
mm a[176:179],v[48:51],v[184:187],a[176:179]
rt v[208:209],v242 offset:0
rt v[210:211],v242 offset:1024
mm a[196:199],v[40:43],v[184:187],a[196:199]
s_add_u32 m0, s83, 33792
mm a[196:199],v[44:47],v[188:191],a[196:199]
ld v232,s[44:47],s80 offen lds
mm a[192:195],v[36:39],v[188:191],a[192:195]
mm a[192:195],v[32:35],v[184:187],a[192:195]
rd v[184:187],v236 offset:18432
rd v[188:191],v237 offset:18432
mm a[64:67],v[32:35],v[0:3],a[64:67]
s_add_u32 m0, s83, 34816
mm a[64:67],v[36:39],v[4:7],a[64:67]
ld v233,s[44:47],s80 offen lds
mm a[68:71],v[44:47],v[4:7],a[68:71]
mm a[68:71],v[40:43],v[0:3],a[68:71]
rt v[212:213],v242 offset:16896
rt v[214:215],v242 offset:17920
mm a[0:3],v[48:51],v[0:3],a[0:3]
s_add_u32 m0, s83, 35968
mm a[0:3],v[52:55],v[4:7],a[0:3]
ld v234,s[44:47],s80 offen lds
mm a[4:7],v[60:63],v[4:7],a[4:7]
mm a[4:7],v[56:59],v[0:3],a[4:7]
rd v[0:3],v236 offset:8192
rd v[4:7],v237 offset:8192
mm a[20:23],v[56:59],v[8:11],a[20:23]
s_add_u32 m0, s83, 36992
mm a[20:23],v[60:63],v[12:15],a[20:23]
ld v235,s[44:47],s80 offen lds
mm a[16:19],v[52:55],v[12:15],a[16:19]
s_mov_b32 s80, 786432
mm a[16:19],v[48:51],v[8:11],a[16:19]
rt v[216:217],v243 offset:0
rt v[218:219],v243 offset:1024
mm a[84:87],v[40:43],v[8:11],a[84:87]
s_add_u32 m0, s83, 50688
mm a[84:87],v[44:47],v[12:15],a[84:87]
ld v232,s[44:47],s80 offen lds
mm a[80:83],v[36:39],v[12:15],a[80:83]
mm a[80:83],v[32:35],v[8:11],a[80:83]
rd v[8:11],v236 offset:10240
rd v[12:15],v237 offset:10240
mm a[240:243],v[32:35],v[16:19],a[240:243]
s_add_u32 m0, s83, 51712
mm a[240:243],v[36:39],v[20:23],a[240:243]
ld v233,s[44:47],s80 offen lds
mm a[244:247],v[44:47],v[20:23],a[244:247]
mm a[244:247],v[40:43],v[16:19],a[244:247]
rt v[220:221],v243 offset:16896
rt v[222:223],v243 offset:17920
mm a[32:35],v[48:51],v[16:19],a[32:35]
s_add_u32 m0, s83, 52864
mm a[32:35],v[52:55],v[20:23],a[32:35]
ld v234,s[44:47],s80 offen lds
mm a[36:39],v[60:63],v[20:23],a[36:39]
mm a[36:39],v[56:59],v[16:19],a[36:39]
rd v[16:19],v236 offset:0
rd v[20:23],v237 offset:0
mm a[60:63],v[56:59],v[24:27],a[60:63]
s_add_u32 m0, s83, 53888
mm a[60:63],v[60:63],v[28:31],a[60:63]
ld v235,s[44:47],s80 offen lds
mm a[56:59],v[52:55],v[28:31],a[56:59]
s_mov_b32 s80, 1048576
mm a[56:59],v[48:51],v[24:27],a[56:59]
mm a[52:55],v[40:43],v[24:27],a[52:55]
mm a[52:55],v[44:47],v[28:31],a[52:55]
mm a[48:51],v[36:39],v[28:31],a[48:51]
wt lgkmcnt(2)
mm a[48:51],v[32:35],v[24:27],a[48:51]
rd v[24:27],v236 offset:2048
rd v[28:31],v237 offset:2048
mm a[96:99],v[192:195],v[160:163],a[96:99]
mm a[96:99],v[196:199],v[164:167],a[96:99]
mm a[100:103],v[204:207],v[164:167],a[100:103]
mm a[100:103],v[200:203],v[160:163],a[100:103]
rt v[32:33],v240 offset:128
rt v[34:35],v240 offset:1152
mm a[104:107],v[208:211],v[160:163],a[104:107]
mm a[104:107],v[212:215],v[164:167],a[104:107]
mm a[108:111],v[220:223],v[164:167],a[108:111]
wt lgkmcnt(2)
s_barrier
mm a[108:111],v[216:219],v[160:163],a[108:111]
mm a[140:143],v[216:219],v[168:171],a[140:143]
s_xor_b32 m0, s82, 0
mm a[140:143],v[220:223],v[172:175],a[140:143]
ld v224,s[40:43],s79 offen lds
mm a[136:139],v[212:215],v[172:175],a[136:139]
mm a[136:139],v[208:211],v[168:171],a[136:139]
rt v[36:37],v240 offset:17024
rt v[38:39],v240 offset:18048
)ASM" R"ASM(mm a[124:127],v[200:203],v[168:171],a[124:127]
s_xor_b32 m0, s82, 1024
mm a[124:127],v[204:207],v[172:175],a[124:127]
ld v225,s[40:43],s79 offen lds
mm a[120:123],v[196:199],v[172:175],a[120:123]
mm a[120:123],v[192:195],v[168:171],a[120:123]
mm a[232:235],v[192:195],v[176:179],a[232:235]
s_xor_b32 m0, s82, 2048
mm a[232:235],v[196:199],v[180:183],a[232:235]
ld v226,s[40:43],s79 offen lds
mm a[236:239],v[204:207],v[180:183],a[236:239]
mm a[236:239],v[200:203],v[176:179],a[236:239]
rt v[40:41],v241 offset:128
rt v[42:43],v241 offset:1152
mm a[152:155],v[208:211],v[176:179],a[152:155]
s_xor_b32 m0, s82, 3072
mm a[152:155],v[212:215],v[180:183],a[152:155]
ld v227,s[40:43],s79 offen lds
mm a[156:159],v[220:223],v[180:183],a[156:159]
mm a[156:159],v[216:219],v[176:179],a[156:159]
mm a[172:175],v[216:219],v[184:187],a[172:175]
s_xor_b32 m0, s82, 4096
mm a[172:175],v[220:223],v[188:191],a[172:175]
ld v228,s[40:43],s79 offen lds
mm a[168:171],v[212:215],v[188:191],a[168:171]
mm a[168:171],v[208:211],v[184:187],a[168:171]
rt v[44:45],v241 offset:17024
rt v[46:47],v241 offset:18048
mm a[220:223],v[200:203],v[184:187],a[220:223]
s_xor_b32 m0, s82, 5120
mm a[220:223],v[204:207],v[188:191],a[220:223]
ld v229,s[40:43],s79 offen lds
mm a[216:219],v[196:199],v[188:191],a[216:219]
mm a[216:219],v[192:195],v[184:187],a[216:219]
mm a[200:203],v[192:195],v[0:3],a[200:203]
s_xor_b32 m0, s82, 6144
mm a[200:203],v[196:199],v[4:7],a[200:203]
ld v230,s[40:43],s79 offen lds
mm a[204:207],v[204:207],v[4:7],a[204:207]
mm a[204:207],v[200:203],v[0:3],a[204:207]
rt v[48:49],v242 offset:128
rt v[50:51],v242 offset:1152
mm a[184:187],v[208:211],v[0:3],a[184:187]
s_xor_b32 m0, s82, 7168
mm a[184:187],v[212:215],v[4:7],a[184:187]
ld v231,s[40:43],s79 offen lds
mm a[188:191],v[220:223],v[4:7],a[188:191]
s_mov_b32 s79, 384
mm a[188:191],v[216:219],v[0:3],a[188:191]
mm a[12:15],v[216:219],v[8:11],a[12:15]
mm a[12:15],v[220:223],v[12:15],a[12:15]
buffer_load_dword v254, v135, s[68:71], s96 offen
mm a[8:11],v[212:215],v[12:15],a[8:11]
s_add_u32 s96, s96, 1048576
mm a[8:11],v[208:211],v[8:11],a[8:11]
rt v[52:53],v242 offset:17024
rt v[54:55],v242 offset:18048
mm a[76:79],v[200:203],v[8:11],a[76:79]
mm a[76:79],v[204:207],v[12:15],a[76:79]
buffer_load_dword v254, v135, s[68:71], s96 offen
mm a[72:75],v[196:199],v[12:15],a[72:75]
s_add_u32 s96, s96, 1048576
mm a[72:75],v[192:195],v[8:11],a[72:75]
rt v[56:57],v243 offset:128
rt v[58:59],v243 offset:1152
mm a[88:91],v[192:195],v[16:19],a[88:91]
mm a[88:91],v[196:199],v[20:23],a[88:91]
mm a[92:95],v[204:207],v[20:23],a[92:95]
mm a[92:95],v[200:203],v[16:19],a[92:95]
rt v[60:61],v243 offset:17024
rt v[62:63],v243 offset:18048
mm a[24:27],v[208:211],v[16:19],a[24:27]
mm a[24:27],v[212:215],v[20:23],a[24:27]
mm a[28:31],v[220:223],v[20:23],a[28:31]
mm a[28:31],v[216:219],v[16:19],a[28:31]
mm a[44:47],v[216:219],v[24:27],a[44:47]
mm a[44:47],v[220:223],v[28:31],a[44:47]
mm a[40:43],v[212:215],v[28:31],a[40:43]
mm a[40:43],v[208:211],v[24:27],a[40:43]
mm a[252:255],v[200:203],v[24:27],a[252:255]
mm a[252:255],v[204:207],v[28:31],a[252:255]
mm a[248:251],v[196:199],v[28:31],a[248:251]
wt vmcnt(10) lgkmcnt(0)
s_barrier
mm a[248:251],v[192:195],v[24:27],a[248:251]
mm a[112:115],v[32:35],v[160:163],a[112:115]
mm a[112:115],v[36:39],v[164:167],a[112:115]
mm a[116:119],v[44:47],v[164:167],a[116:119]
rt v[192:193],v240 offset:33792
rt v[194:195],v240 offset:34816
mm a[116:119],v[40:43],v[160:163],a[116:119]
rt v[196:197],v240 offset:50688
rt v[198:199],v240 offset:51712
mm a[128:131],v[48:51],v[160:163],a[128:131]
mm a[128:131],v[52:55],v[164:167],a[128:131]
mm a[132:135],v[60:63],v[164:167],a[132:135]
mm a[132:135],v[56:59],v[160:163],a[132:135]
rd v[160:163],v236 offset:57344
rd v[164:167],v237 offset:57344
mm a[148:151],v[56:59],v[168:171],a[148:151]
)ASM" R"ASM(mm a[148:151],v[60:63],v[172:175],a[148:151]
mm a[144:147],v[52:55],v[172:175],a[144:147]
mm a[144:147],v[48:51],v[168:171],a[144:147]
rt v[200:201],v241 offset:33792
rt v[202:203],v241 offset:34816
mm a[228:231],v[40:43],v[168:171],a[228:231]
mm a[228:231],v[44:47],v[172:175],a[228:231]
mm a[224:227],v[36:39],v[172:175],a[224:227]
mm a[224:227],v[32:35],v[168:171],a[224:227]
rd v[168:171],v236 offset:59392
rd v[172:175],v237 offset:59392
mm a[208:211],v[32:35],v[176:179],a[208:211]
mm a[208:211],v[36:39],v[180:183],a[208:211]
mm a[212:215],v[44:47],v[180:183],a[212:215]
mm a[212:215],v[40:43],v[176:179],a[212:215]
rt v[204:205],v241 offset:50688
rt v[206:207],v241 offset:51712
mm a[160:163],v[48:51],v[176:179],a[160:163]
mm a[160:163],v[52:55],v[180:183],a[160:163]
mm a[164:167],v[60:63],v[180:183],a[164:167]
mm a[164:167],v[56:59],v[176:179],a[164:167]
rd v[176:179],v236 offset:49152
rd v[180:183],v237 offset:49152
mm a[180:183],v[56:59],v[184:187],a[180:183]
mm a[180:183],v[60:63],v[188:191],a[180:183]
mm a[176:179],v[52:55],v[188:191],a[176:179]
mm a[176:179],v[48:51],v[184:187],a[176:179]
rt v[208:209],v242 offset:33792
rt v[210:211],v242 offset:34816
mm a[196:199],v[40:43],v[184:187],a[196:199]
s_add_u32 m0, s83, 0
mm a[196:199],v[44:47],v[188:191],a[196:199]
ld v232,s[44:47],s80 offen lds
mm a[192:195],v[36:39],v[188:191],a[192:195]
mm a[192:195],v[32:35],v[184:187],a[192:195]
rd v[184:187],v236 offset:51200
rd v[188:191],v237 offset:51200
mm a[64:67],v[32:35],v[0:3],a[64:67]
s_add_u32 m0, s83, 1024
mm a[64:67],v[36:39],v[4:7],a[64:67]
ld v233,s[44:47],s80 offen lds
mm a[68:71],v[44:47],v[4:7],a[68:71]
mm a[68:71],v[40:43],v[0:3],a[68:71]
rt v[212:213],v242 offset:50688
rt v[214:215],v242 offset:51712
mm a[0:3],v[48:51],v[0:3],a[0:3]
s_add_u32 m0, s83, 2176
mm a[0:3],v[52:55],v[4:7],a[0:3]
ld v234,s[44:47],s80 offen lds
mm a[4:7],v[60:63],v[4:7],a[4:7]
mm a[4:7],v[56:59],v[0:3],a[4:7]
rd v[0:3],v236 offset:40960
rd v[4:7],v237 offset:40960
mm a[20:23],v[56:59],v[8:11],a[20:23]
s_add_u32 m0, s83, 3200
mm a[20:23],v[60:63],v[12:15],a[20:23]
ld v235,s[44:47],s80 offen lds
mm a[16:19],v[52:55],v[12:15],a[16:19]
s_mov_b32 s80, 1310720
mm a[16:19],v[48:51],v[8:11],a[16:19]
rt v[216:217],v243 offset:33792
rt v[218:219],v243 offset:34816
mm a[84:87],v[40:43],v[8:11],a[84:87]
s_add_u32 m0, s83, 16896
mm a[84:87],v[44:47],v[12:15],a[84:87]
ld v232,s[44:47],s80 offen lds
mm a[80:83],v[36:39],v[12:15],a[80:83]
mm a[80:83],v[32:35],v[8:11],a[80:83]
rd v[8:11],v236 offset:43008
.La5_LW1_2:
s_cbranch_execz .La5_LW1_6
rd v[12:15],v237 offset:43008
mm a[240:243],v[32:35],v[16:19],a[240:243]
s_add_u32 m0, s83, 17920
mm a[240:243],v[36:39],v[20:23],a[240:243]
ld v233,s[44:47],s80 offen lds
mm a[244:247],v[44:47],v[20:23],a[244:247]
mm a[244:247],v[40:43],v[16:19],a[244:247]
rt v[220:221],v243 offset:50688
rt v[222:223],v243 offset:51712
mm a[32:35],v[48:51],v[16:19],a[32:35]
s_add_u32 m0, s83, 19072
mm a[32:35],v[52:55],v[20:23],a[32:35]
ld v234,s[44:47],s80 offen lds
mm a[36:39],v[60:63],v[20:23],a[36:39]
mm a[36:39],v[56:59],v[16:19],a[36:39]
rd v[16:19],v236 offset:32768
rd v[20:23],v237 offset:32768
mm a[60:63],v[56:59],v[24:27],a[60:63]
s_add_u32 m0, s83, 20096
mm a[60:63],v[60:63],v[28:31],a[60:63]
ld v235,s[44:47],s80 offen lds
mm a[56:59],v[52:55],v[28:31],a[56:59]
s_mov_b32 s80, 1572864
mm a[56:59],v[48:51],v[24:27],a[56:59]
mm a[52:55],v[40:43],v[24:27],a[52:55]
mm a[52:55],v[44:47],v[28:31],a[52:55]
mm a[48:51],v[36:39],v[28:31],a[48:51]
wt lgkmcnt(2)
mm a[48:51],v[32:35],v[24:27],a[48:51]
)ASM" R"ASM(rd v[24:27],v236 offset:34816
rd v[28:31],v237 offset:34816
mm a[96:99],v[192:195],v[160:163],a[96:99]
mm a[96:99],v[196:199],v[164:167],a[96:99]
mm a[100:103],v[204:207],v[164:167],a[100:103]
mm a[100:103],v[200:203],v[160:163],a[100:103]
rt v[32:33],v240 offset:33920
rt v[34:35],v240 offset:34944
mm a[104:107],v[208:211],v[160:163],a[104:107]
mm a[104:107],v[212:215],v[164:167],a[104:107]
mm a[108:111],v[220:223],v[164:167],a[108:111]
wt lgkmcnt(2)
s_barrier
mm a[108:111],v[216:219],v[160:163],a[108:111]
mm a[140:143],v[216:219],v[168:171],a[140:143]
s_xor_b32 m0, s82, 32768
mm a[140:143],v[220:223],v[172:175],a[140:143]
ld v224,s[40:43],s79 offen lds
mm a[136:139],v[212:215],v[172:175],a[136:139]
mm a[136:139],v[208:211],v[168:171],a[136:139]
rt v[36:37],v240 offset:50816
rt v[38:39],v240 offset:51840
mm a[124:127],v[200:203],v[168:171],a[124:127]
s_xor_b32 m0, s82, 33792
mm a[124:127],v[204:207],v[172:175],a[124:127]
ld v225,s[40:43],s79 offen lds
mm a[120:123],v[196:199],v[172:175],a[120:123]
mm a[120:123],v[192:195],v[168:171],a[120:123]
mm a[232:235],v[192:195],v[176:179],a[232:235]
s_xor_b32 m0, s82, 34816
mm a[232:235],v[196:199],v[180:183],a[232:235]
ld v226,s[40:43],s79 offen lds
mm a[236:239],v[204:207],v[180:183],a[236:239]
mm a[236:239],v[200:203],v[176:179],a[236:239]
rt v[40:41],v241 offset:33920
rt v[42:43],v241 offset:34944
mm a[152:155],v[208:211],v[176:179],a[152:155]
s_xor_b32 m0, s82, 35840
mm a[152:155],v[212:215],v[180:183],a[152:155]
ld v227,s[40:43],s79 offen lds
mm a[156:159],v[220:223],v[180:183],a[156:159]
mm a[156:159],v[216:219],v[176:179],a[156:159]
mm a[172:175],v[216:219],v[184:187],a[172:175]
s_xor_b32 m0, s82, 36864
mm a[172:175],v[220:223],v[188:191],a[172:175]
ld v228,s[40:43],s79 offen lds
mm a[168:171],v[212:215],v[188:191],a[168:171]
mm a[168:171],v[208:211],v[184:187],a[168:171]
rt v[44:45],v241 offset:50816
rt v[46:47],v241 offset:51840
mm a[220:223],v[200:203],v[184:187],a[220:223]
s_xor_b32 m0, s82, 37888
mm a[220:223],v[204:207],v[188:191],a[220:223]
ld v229,s[40:43],s79 offen lds
mm a[216:219],v[196:199],v[188:191],a[216:219]
mm a[216:219],v[192:195],v[184:187],a[216:219]
mm a[200:203],v[192:195],v[0:3],a[200:203]
s_xor_b32 m0, s82, 38912
mm a[200:203],v[196:199],v[4:7],a[200:203]
ld v230,s[40:43],s79 offen lds
mm a[204:207],v[204:207],v[4:7],a[204:207]
mm a[204:207],v[200:203],v[0:3],a[204:207]
rt v[48:49],v242 offset:33920
rt v[50:51],v242 offset:34944
mm a[184:187],v[208:211],v[0:3],a[184:187]
s_xor_b32 m0, s82, 39936
mm a[184:187],v[212:215],v[4:7],a[184:187]
ld v231,s[40:43],s79 offen lds
mm a[188:191],v[220:223],v[4:7],a[188:191]
s_mov_b32 s79, 512
mm a[188:191],v[216:219],v[0:3],a[188:191]
mm a[12:15],v[216:219],v[8:11],a[12:15]
mm a[12:15],v[220:223],v[12:15],a[12:15]
buffer_load_dword v254, v135, s[68:71], s96 offen
mm a[8:11],v[212:215],v[12:15],a[8:11]
s_add_u32 s96, s96, 1048576
mm a[8:11],v[208:211],v[8:11],a[8:11]
rt v[52:53],v242 offset:50816
rt v[54:55],v242 offset:51840
mm a[76:79],v[200:203],v[8:11],a[76:79]
mm a[76:79],v[204:207],v[12:15],a[76:79]
buffer_load_dword v254, v135, s[68:71], s96 offen
mm a[72:75],v[196:199],v[12:15],a[72:75]
s_add_u32 s96, s96, 1048576
mm a[72:75],v[192:195],v[8:11],a[72:75]
rt v[56:57],v243 offset:33920
rt v[58:59],v243 offset:34944
mm a[88:91],v[192:195],v[16:19],a[88:91]
mm a[88:91],v[196:199],v[20:23],a[88:91]
buffer_store_dword v255, off, s[64:67], s100 sc1
mm a[92:95],v[204:207],v[20:23],a[92:95]
mm a[92:95],v[200:203],v[16:19],a[92:95]
rt v[60:61],v243 offset:50816
rt v[62:63],v243 offset:51840
mm a[24:27],v[208:211],v[16:19],a[24:27]
mm a[24:27],v[212:215],v[20:23],a[24:27]
buffer_store_dword v255, off, s[64:67], s101 sc1
mm a[28:31],v[220:223],v[20:23],a[28:31]
mm a[28:31],v[216:219],v[16:19],a[28:31]
mm a[44:47],v[216:219],v[24:27],a[44:47]
)ASM" R"ASM(mm a[44:47],v[220:223],v[28:31],a[44:47]
mm a[40:43],v[212:215],v[28:31],a[40:43]
mm a[40:43],v[208:211],v[24:27],a[40:43]
mm a[252:255],v[200:203],v[24:27],a[252:255]
mm a[252:255],v[204:207],v[28:31],a[252:255]
mm a[248:251],v[196:199],v[28:31],a[248:251]
wt vmcnt(12) lgkmcnt(0)
s_barrier
mm a[248:251],v[192:195],v[24:27],a[248:251]
mm a[112:115],v[32:35],v[160:163],a[112:115]
mm a[112:115],v[36:39],v[164:167],a[112:115]
mm a[116:119],v[44:47],v[164:167],a[116:119]
rt v[192:193],v240 offset:0
rt v[194:195],v240 offset:1024
mm a[116:119],v[40:43],v[160:163],a[116:119]
rt v[196:197],v240 offset:16896
rt v[198:199],v240 offset:17920
mm a[128:131],v[48:51],v[160:163],a[128:131]
mm a[128:131],v[52:55],v[164:167],a[128:131]
mm a[132:135],v[60:63],v[164:167],a[132:135]
mm a[132:135],v[56:59],v[160:163],a[132:135]
rd v[160:163],v236 offset:24576
rd v[164:167],v237 offset:24576
mm a[148:151],v[56:59],v[168:171],a[148:151]
mm a[148:151],v[60:63],v[172:175],a[148:151]
mm a[144:147],v[52:55],v[172:175],a[144:147]
mm a[144:147],v[48:51],v[168:171],a[144:147]
rt v[200:201],v241 offset:0
rt v[202:203],v241 offset:1024
mm a[228:231],v[40:43],v[168:171],a[228:231]
mm a[228:231],v[44:47],v[172:175],a[228:231]
mm a[224:227],v[36:39],v[172:175],a[224:227]
mm a[224:227],v[32:35],v[168:171],a[224:227]
rd v[168:171],v236 offset:26624
rd v[172:175],v237 offset:26624
mm a[208:211],v[32:35],v[176:179],a[208:211]
mm a[208:211],v[36:39],v[180:183],a[208:211]
mm a[212:215],v[44:47],v[180:183],a[212:215]
mm a[212:215],v[40:43],v[176:179],a[212:215]
rt v[204:205],v241 offset:16896
rt v[206:207],v241 offset:17920
mm a[160:163],v[48:51],v[176:179],a[160:163]
mm a[160:163],v[52:55],v[180:183],a[160:163]
mm a[164:167],v[60:63],v[180:183],a[164:167]
mm a[164:167],v[56:59],v[176:179],a[164:167]
rd v[176:179],v236 offset:16384
rd v[180:183],v237 offset:16384
mm a[180:183],v[56:59],v[184:187],a[180:183]
mm a[180:183],v[60:63],v[188:191],a[180:183]
mm a[176:179],v[52:55],v[188:191],a[176:179]
mm a[176:179],v[48:51],v[184:187],a[176:179]
rt v[208:209],v242 offset:0
rt v[210:211],v242 offset:1024
mm a[196:199],v[40:43],v[184:187],a[196:199]
s_add_u32 m0, s83, 33792
mm a[196:199],v[44:47],v[188:191],a[196:199]
ld v232,s[44:47],s80 offen lds
mm a[192:195],v[36:39],v[188:191],a[192:195]
mm a[192:195],v[32:35],v[184:187],a[192:195]
rd v[184:187],v236 offset:18432
rd v[188:191],v237 offset:18432
mm a[64:67],v[32:35],v[0:3],a[64:67]
s_add_u32 m0, s83, 34816
mm a[64:67],v[36:39],v[4:7],a[64:67]
ld v233,s[44:47],s80 offen lds
mm a[68:71],v[44:47],v[4:7],a[68:71]
mm a[68:71],v[40:43],v[0:3],a[68:71]
rt v[212:213],v242 offset:16896
rt v[214:215],v242 offset:17920
mm a[0:3],v[48:51],v[0:3],a[0:3]
s_add_u32 m0, s83, 35968
mm a[0:3],v[52:55],v[4:7],a[0:3]
ld v234,s[44:47],s80 offen lds
mm a[4:7],v[60:63],v[4:7],a[4:7]
mm a[4:7],v[56:59],v[0:3],a[4:7]
rd v[0:3],v236 offset:8192
rd v[4:7],v237 offset:8192
mm a[20:23],v[56:59],v[8:11],a[20:23]
s_add_u32 m0, s83, 36992
mm a[20:23],v[60:63],v[12:15],a[20:23]
ld v235,s[44:47],s80 offen lds
mm a[16:19],v[52:55],v[12:15],a[16:19]
s_mov_b32 s80, 1835008
mm a[16:19],v[48:51],v[8:11],a[16:19]
rt v[216:217],v243 offset:0
rt v[218:219],v243 offset:1024
mm a[84:87],v[40:43],v[8:11],a[84:87]
s_add_u32 m0, s83, 50688
mm a[84:87],v[44:47],v[12:15],a[84:87]
ld v232,s[44:47],s80 offen lds
mm a[80:83],v[36:39],v[12:15],a[80:83]
mm a[80:83],v[32:35],v[8:11],a[80:83]
rd v[8:11],v236 offset:10240
rd v[12:15],v237 offset:10240
mm a[240:243],v[32:35],v[16:19],a[240:243]
s_add_u32 m0, s83, 51712
mm a[240:243],v[36:39],v[20:23],a[240:243]
ld v233,s[44:47],s80 offen lds
mm a[244:247],v[44:47],v[20:23],a[244:247]
mm a[244:247],v[40:43],v[16:19],a[244:247]
)ASM" R"ASM(rt v[220:221],v243 offset:16896
rt v[222:223],v243 offset:17920
mm a[32:35],v[48:51],v[16:19],a[32:35]
s_add_u32 m0, s83, 52864
mm a[32:35],v[52:55],v[20:23],a[32:35]
ld v234,s[44:47],s80 offen lds
mm a[36:39],v[60:63],v[20:23],a[36:39]
mm a[36:39],v[56:59],v[16:19],a[36:39]
rd v[16:19],v236 offset:0
rd v[20:23],v237 offset:0
mm a[60:63],v[56:59],v[24:27],a[60:63]
s_add_u32 m0, s83, 53888
mm a[60:63],v[60:63],v[28:31],a[60:63]
ld v235,s[44:47],s80 offen lds
mm a[56:59],v[52:55],v[28:31],a[56:59]
s_mov_b64 s[44:45], s[68:69]
s_mov_b32 s46, s70
mm a[56:59],v[48:51],v[24:27],a[56:59]
mm a[52:55],v[40:43],v[24:27],a[52:55]
mm a[52:55],v[44:47],v[28:31],a[52:55]
mm a[48:51],v[36:39],v[28:31],a[48:51]
wt lgkmcnt(2)
s_lshl_b32 s80, s77, 18
mm a[48:51],v[32:35],v[24:27],a[48:51]
rd v[24:27],v236 offset:2048
rd v[28:31],v237 offset:2048
mm a[96:99],v[192:195],v[160:163],a[96:99]
mm a[96:99],v[196:199],v[164:167],a[96:99]
mm a[100:103],v[204:207],v[164:167],a[100:103]
mm a[100:103],v[200:203],v[160:163],a[100:103]
rt v[32:33],v240 offset:128
rt v[34:35],v240 offset:1152
mm a[104:107],v[208:211],v[160:163],a[104:107]
mm a[104:107],v[212:215],v[164:167],a[104:107]
mm a[108:111],v[220:223],v[164:167],a[108:111]
wt lgkmcnt(2)
s_barrier
mm a[108:111],v[216:219],v[160:163],a[108:111]
mm a[140:143],v[216:219],v[168:171],a[140:143]
mm a[140:143],v[220:223],v[172:175],a[140:143]
mm a[136:139],v[212:215],v[172:175],a[136:139]
mm a[136:139],v[208:211],v[168:171],a[136:139]
rt v[36:37],v240 offset:17024
rt v[38:39],v240 offset:18048
mm a[124:127],v[200:203],v[168:171],a[124:127]
mm a[124:127],v[204:207],v[172:175],a[124:127]
mm a[120:123],v[196:199],v[172:175],a[120:123]
mm a[120:123],v[192:195],v[168:171],a[120:123]
mm a[232:235],v[192:195],v[176:179],a[232:235]
mm a[232:235],v[196:199],v[180:183],a[232:235]
buffer_store_dword v255, off, s[64:67], s100 sc1
mm a[236:239],v[204:207],v[180:183],a[236:239]
buffer_store_dword v255, off, s[64:67], s101 sc1
mm a[236:239],v[200:203],v[176:179],a[236:239]
rt v[40:41],v241 offset:128
rt v[42:43],v241 offset:1152
mm a[152:155],v[208:211],v[176:179],a[152:155]
mm a[152:155],v[212:215],v[180:183],a[152:155]
mm a[156:159],v[220:223],v[180:183],a[156:159]
mm a[156:159],v[216:219],v[176:179],a[156:159]
mm a[172:175],v[216:219],v[184:187],a[172:175]
mm a[172:175],v[220:223],v[188:191],a[172:175]
mm a[168:171],v[212:215],v[188:191],a[168:171]
mm a[168:171],v[208:211],v[184:187],a[168:171]
rt v[44:45],v241 offset:17024
rt v[46:47],v241 offset:18048
mm a[220:223],v[200:203],v[184:187],a[220:223]
mm a[220:223],v[204:207],v[188:191],a[220:223]
mm a[216:219],v[196:199],v[188:191],a[216:219]
mm a[216:219],v[192:195],v[184:187],a[216:219]
mm a[200:203],v[192:195],v[0:3],a[200:203]
mm a[200:203],v[196:199],v[4:7],a[200:203]
mm a[204:207],v[204:207],v[4:7],a[204:207]
mm a[204:207],v[200:203],v[0:3],a[204:207]
rt v[48:49],v242 offset:128
rt v[50:51],v242 offset:1152
mm a[184:187],v[208:211],v[0:3],a[184:187]
mm a[184:187],v[212:215],v[4:7],a[184:187]
mm a[188:191],v[220:223],v[4:7],a[188:191]
mm a[188:191],v[216:219],v[0:3],a[188:191]
mm a[12:15],v[216:219],v[8:11],a[12:15]
wt vmcnt(1)
s_memtime s[100:101]
wt vmcnt(0)
s_memtime s[92:93]
wt lgkmcnt(0)
s_sub_u32 s92, s92, s100
s_cmp_lt_u32 s92, 56
s_cselect_b32 s92, 8192, 0
v_mov_b32 v77, s92
ds_write_b32 v76, v77
mm a[12:15],v[220:223],v[12:15],a[12:15]
buffer_load_dword v254, v135, s[68:71], s96 offen
mm a[8:11],v[212:215],v[12:15],a[8:11]
s_add_u32 s96, s96, 1048576
mm a[8:11],v[208:211],v[8:11],a[8:11]
rt v[52:53],v242 offset:17024
rt v[54:55],v242 offset:18048
mm a[76:79],v[200:203],v[8:11],a[76:79]
mm a[76:79],v[204:207],v[12:15],a[76:79]
)ASM" R"ASM(buffer_load_dword v254, v135, s[68:71], s96 offen
mm a[72:75],v[196:199],v[12:15],a[72:75]
s_add_u32 s96, s96, 1048576
mm a[72:75],v[192:195],v[8:11],a[72:75]
rt v[56:57],v243 offset:128
rt v[58:59],v243 offset:1152
mm a[88:91],v[192:195],v[16:19],a[88:91]
mm a[88:91],v[196:199],v[20:23],a[88:91]
mm a[92:95],v[204:207],v[20:23],a[92:95]
mm a[92:95],v[200:203],v[16:19],a[92:95]
rt v[60:61],v243 offset:17024
rt v[62:63],v243 offset:18048
mm a[24:27],v[208:211],v[16:19],a[24:27]
mm a[24:27],v[212:215],v[20:23],a[24:27]
mm a[28:31],v[220:223],v[20:23],a[28:31]
mm a[28:31],v[216:219],v[16:19],a[28:31]
mm a[44:47],v[216:219],v[24:27],a[44:47]
mm a[44:47],v[220:223],v[28:31],a[44:47]
ld v[72:75],v255,s[64:67],s89 offen offset:1024 sc1
mm a[40:43],v[212:215],v[28:31],a[40:43]
mm a[40:43],v[208:211],v[24:27],a[40:43]
mm a[252:255],v[200:203],v[24:27],a[252:255]
mm a[252:255],v[204:207],v[28:31],a[252:255]
mm a[248:251],v[196:199],v[28:31],a[248:251]
wt lgkmcnt(0)
s_barrier
mm a[248:251],v[192:195],v[24:27],a[248:251]
mm a[112:115],v[32:35],v[160:163],a[112:115]
mm a[112:115],v[36:39],v[164:167],a[112:115]
mm a[116:119],v[44:47],v[164:167],a[116:119]
rt v[192:193],v240 offset:33792
rt v[194:195],v240 offset:34816
mm a[116:119],v[40:43],v[160:163],a[116:119]
rt v[196:197],v240 offset:50688
rt v[198:199],v240 offset:51712
mm a[128:131],v[48:51],v[160:163],a[128:131]
mm a[128:131],v[52:55],v[164:167],a[128:131]
mm a[132:135],v[60:63],v[164:167],a[132:135]
mm a[132:135],v[56:59],v[160:163],a[132:135]
rd v[160:163],v236 offset:57344
rd v[164:167],v237 offset:57344
mm a[148:151],v[56:59],v[168:171],a[148:151]
mm a[148:151],v[60:63],v[172:175],a[148:151]
mm a[144:147],v[52:55],v[172:175],a[144:147]
mm a[144:147],v[48:51],v[168:171],a[144:147]
rt v[200:201],v241 offset:33792
rt v[202:203],v241 offset:34816
mm a[228:231],v[40:43],v[168:171],a[228:231]
mm a[228:231],v[44:47],v[172:175],a[228:231]
mm a[224:227],v[36:39],v[172:175],a[224:227]
mm a[224:227],v[32:35],v[168:171],a[224:227]
rd v[168:171],v236 offset:59392
rd v[172:175],v237 offset:59392
mm a[208:211],v[32:35],v[176:179],a[208:211]
mm a[208:211],v[36:39],v[180:183],a[208:211]
mm a[212:215],v[44:47],v[180:183],a[212:215]
mm a[212:215],v[40:43],v[176:179],a[212:215]
rt v[204:205],v241 offset:50688
rt v[206:207],v241 offset:51712
mm a[160:163],v[48:51],v[176:179],a[160:163]
mm a[160:163],v[52:55],v[180:183],a[160:163]
mm a[164:167],v[60:63],v[180:183],a[164:167]
mm a[164:167],v[56:59],v[176:179],a[164:167]
rd v[176:179],v236 offset:49152
rd v[180:183],v237 offset:49152
mm a[180:183],v[56:59],v[184:187],a[180:183]
mm a[180:183],v[60:63],v[188:191],a[180:183]
mm a[176:179],v[52:55],v[188:191],a[176:179]
mm a[176:179],v[48:51],v[184:187],a[176:179]
rt v[208:209],v242 offset:33792
rt v[210:211],v242 offset:34816
mm a[196:199],v[40:43],v[184:187],a[196:199]
s_add_u32 m0, s83, 0
mm a[196:199],v[44:47],v[188:191],a[196:199]
ld v232,s[44:47],s80 offen lds
mm a[192:195],v[36:39],v[188:191],a[192:195]
mm a[192:195],v[32:35],v[184:187],a[192:195]
rd v[184:187],v236 offset:51200
rd v[188:191],v237 offset:51200
mm a[64:67],v[32:35],v[0:3],a[64:67]
s_add_u32 m0, s83, 1024
mm a[64:67],v[36:39],v[4:7],a[64:67]
ld v233,s[44:47],s80 offen lds
mm a[68:71],v[44:47],v[4:7],a[68:71]
mm a[68:71],v[40:43],v[0:3],a[68:71]
rt v[212:213],v242 offset:50688
rt v[214:215],v242 offset:51712
mm a[0:3],v[48:51],v[0:3],a[0:3]
s_add_u32 m0, s83, 2176
mm a[0:3],v[52:55],v[4:7],a[0:3]
ld v234,s[44:47],s80 offen lds
mm a[4:7],v[60:63],v[4:7],a[4:7]
mm a[4:7],v[56:59],v[0:3],a[4:7]
rd v[0:3],v236 offset:40960
rd v[4:7],v237 offset:40960
mm a[20:23],v[56:59],v[8:11],a[20:23]
s_add_u32 m0, s83, 3200
mm a[20:23],v[60:63],v[12:15],a[20:23]
ld v235,s[44:47],s80 offen lds
mm a[16:19],v[52:55],v[12:15],a[16:19]
)ASM" R"ASM(s_add_u32 s80, s80, 524288
mm a[16:19],v[48:51],v[8:11],a[16:19]
rt v[216:217],v243 offset:33792
rt v[218:219],v243 offset:34816
mm a[84:87],v[40:43],v[8:11],a[84:87]
s_add_u32 m0, s83, 16896
mm a[84:87],v[44:47],v[12:15],a[84:87]
ld v232,s[44:47],s80 offen lds
mm a[80:83],v[36:39],v[12:15],a[80:83]
mm a[80:83],v[32:35],v[8:11],a[80:83]
rd v[8:11],v236 offset:43008
rd v[12:15],v237 offset:43008
mm a[240:243],v[32:35],v[16:19],a[240:243]
s_add_u32 m0, s83, 17920
mm a[240:243],v[36:39],v[20:23],a[240:243]
ld v233,s[44:47],s80 offen lds
mm a[244:247],v[44:47],v[20:23],a[244:247]
mm a[244:247],v[40:43],v[16:19],a[244:247]
rt v[220:221],v243 offset:50688
rt v[222:223],v243 offset:51712
mm a[32:35],v[48:51],v[16:19],a[32:35]
s_add_u32 m0, s83, 19072
mm a[32:35],v[52:55],v[20:23],a[32:35]
ld v234,s[44:47],s80 offen lds
mm a[36:39],v[60:63],v[20:23],a[36:39]
mm a[36:39],v[56:59],v[16:19],a[36:39]
rd v[16:19],v236 offset:32768
rd v[20:23],v237 offset:32768
mm a[60:63],v[56:59],v[24:27],a[60:63]
s_add_u32 m0, s83, 20096
mm a[60:63],v[60:63],v[28:31],a[60:63]
ld v235,s[44:47],s80 offen lds
mm a[56:59],v[52:55],v[28:31],a[56:59]
s_add_u32 s80, s80, 524288
mm a[56:59],v[48:51],v[24:27],a[56:59]
mm a[52:55],v[40:43],v[24:27],a[52:55]
mm a[52:55],v[44:47],v[28:31],a[52:55]
mm a[48:51],v[36:39],v[28:31],a[48:51]
wt lgkmcnt(2)
mm a[48:51],v[32:35],v[24:27],a[48:51]
rd v[24:27],v236 offset:34816
rd v[28:31],v237 offset:34816
mm a[96:99],v[192:195],v[160:163],a[96:99]
mm a[96:99],v[196:199],v[164:167],a[96:99]
mm a[100:103],v[204:207],v[164:167],a[100:103]
mm a[100:103],v[200:203],v[160:163],a[100:103]
rt v[32:33],v240 offset:33920
rt v[34:35],v240 offset:34944
mm a[104:107],v[208:211],v[160:163],a[104:107]
mm a[104:107],v[212:215],v[164:167],a[104:107]
mm a[108:111],v[220:223],v[164:167],a[108:111]
wt lgkmcnt(2)
s_barrier
mm a[108:111],v[216:219],v[160:163],a[108:111]
mm a[140:143],v[216:219],v[168:171],a[140:143]
wt vmcnt(8)
v_cmp_eq_u64 vcc, v[72:73], v[252:253]
mm a[140:143],v[220:223],v[172:175],a[140:143]
v_cmp_eq_u64 s[96:97], v[74:75], v[252:253]
mm a[136:139],v[212:215],v[172:175],a[136:139]
s_and_b64 vcc, vcc, s[96:97]
s_cmp_eq_u64 vcc, -1
s_cbranch_scc0 .La5_LK_XSPIN
.La5_LK_XOK:
mm a[136:139],v[208:211],v[168:171],a[136:139]
rt v[36:37],v240 offset:50816
rt v[38:39],v240 offset:51840
mm a[124:127],v[200:203],v[168:171],a[124:127]
s_mov_b32 s89, 49152
mm a[124:127],v[204:207],v[172:175],a[124:127]
mm a[120:123],v[196:199],v[172:175],a[120:123]
ar v224,a96
ar v225,a97
mm a[120:123],v[192:195],v[168:171],a[120:123]
ar v226,a98
ar v227,a99
mm a[232:235],v[192:195],v[176:179],a[232:235]
ar v228,a100
ar v229,a101
mm a[232:235],v[196:199],v[180:183],a[232:235]
ld v[128:131],v244,s[52:55],s91 offen offset:0 sc1
mm a[236:239],v[204:207],v[180:183],a[236:239]
ar v230,a102
ar v231,a103
mm a[236:239],v[200:203],v[176:179],a[236:239]
rt v[40:41],v241 offset:33920
rt v[42:43],v241 offset:34944
mm a[152:155],v[208:211],v[176:179],a[152:155]
cv v248,v224,v225
cv v249,v226,v227
mm a[152:155],v[212:215],v[180:183],a[152:155]
ld v[132:135],v245,s[52:55],s91 offen offset:0 sc1
mm a[156:159],v[220:223],v[180:183],a[156:159]
cv v250,v228,v229
cv v251,v230,v231
mm a[156:159],v[216:219],v[176:179],a[156:159]
st v[248:251],v255,s[48:51],s89 offen sc1
.La5_LW1_3:
s_cbranch_execz .La5_LW1_7
mm a[172:175],v[216:219],v[184:187],a[172:175]
)ASM" R"ASM(s_mov_b32 s89, 50176
ds_write_b128 v238, v[248:251] offset:0
mm a[172:175],v[220:223],v[188:191],a[172:175]
ld v[136:139],v246,s[52:55],s91 offen offset:0 sc1
mm a[168:171],v[212:215],v[188:191],a[168:171]
ar v224,a104
ar v225,a105
mm a[168:171],v[208:211],v[184:187],a[168:171]
rt v[44:45],v241 offset:50816
rt v[46:47],v241 offset:51840
mm a[220:223],v[200:203],v[184:187],a[220:223]
ar v226,a106
ar v227,a107
mm a[220:223],v[204:207],v[188:191],a[220:223]
ld v[140:143],v247,s[52:55],s91 offen offset:0 sc1
mm a[216:219],v[196:199],v[188:191],a[216:219]
ar v228,a108
ar v229,a109
mm a[216:219],v[192:195],v[184:187],a[216:219]
ar v230,a110
ar v231,a111
mm a[200:203],v[192:195],v[0:3],a[200:203]
cv v248,v224,v225
cv v249,v226,v227
mm a[200:203],v[196:199],v[4:7],a[200:203]
ld v[144:147],v244,s[52:55],s91 offen offset:1024 sc1
mm a[204:207],v[204:207],v[4:7],a[204:207]
cv v250,v228,v229
cv v251,v230,v231
mm a[204:207],v[200:203],v[0:3],a[204:207]
rt v[48:49],v242 offset:33920
rt v[50:51],v242 offset:34944
mm a[184:187],v[208:211],v[0:3],a[184:187]
st v[248:251],v255,s[48:51],s89 offen sc1
mm a[184:187],v[212:215],v[4:7],a[184:187]
ld v[148:151],v245,s[52:55],s91 offen offset:1024 sc1
mm a[188:191],v[220:223],v[4:7],a[188:191]
s_mov_b32 s89, 51200
ds_write_b128 v238, v[248:251] offset:1024
mm a[188:191],v[216:219],v[0:3],a[188:191]
mm a[12:15],v[216:219],v[8:11],a[12:15]
mm a[12:15],v[220:223],v[12:15],a[12:15]
ld v[152:155],v246,s[52:55],s91 offen offset:1024 sc1
mm a[8:11],v[212:215],v[12:15],a[8:11]
mm a[8:11],v[208:211],v[8:11],a[8:11]
rt v[52:53],v242 offset:50816
rt v[54:55],v242 offset:51840
mm a[76:79],v[200:203],v[8:11],a[76:79]
mm a[76:79],v[204:207],v[12:15],a[76:79]
ld v[156:159],v247,s[52:55],s91 offen offset:1024 sc1
mm a[72:75],v[196:199],v[12:15],a[72:75]
mm a[72:75],v[192:195],v[8:11],a[72:75]
rt v[56:57],v243 offset:33920
rt v[58:59],v243 offset:34944
mm a[88:91],v[192:195],v[16:19],a[88:91]
mm a[88:91],v[196:199],v[20:23],a[88:91]
mm a[92:95],v[204:207],v[20:23],a[92:95]
mm a[92:95],v[200:203],v[16:19],a[92:95]
rt v[60:61],v243 offset:50816
rt v[62:63],v243 offset:51840
mm a[24:27],v[208:211],v[16:19],a[24:27]
mm a[24:27],v[212:215],v[20:23],a[24:27]
mm a[28:31],v[220:223],v[20:23],a[28:31]
mm a[28:31],v[216:219],v[16:19],a[28:31]
mm a[44:47],v[216:219],v[24:27],a[44:47]
mm a[44:47],v[220:223],v[28:31],a[44:47]
mm a[40:43],v[212:215],v[28:31],a[40:43]
mm a[40:43],v[208:211],v[24:27],a[40:43]
mm a[252:255],v[200:203],v[24:27],a[252:255]
mm a[252:255],v[204:207],v[28:31],a[252:255]
mm a[248:251],v[196:199],v[28:31],a[248:251]
wt vmcnt(10) lgkmcnt(0)
s_barrier
mm a[248:251],v[192:195],v[24:27],a[248:251]
mm a[112:115],v[32:35],v[160:163],a[112:115]
s_add_u32 m0, s83, 33792
mm a[112:115],v[36:39],v[164:167],a[112:115]
ld v232,s[44:47],s80 offen lds
mm a[116:119],v[44:47],v[164:167],a[116:119]
mm a[116:119],v[40:43],v[160:163],a[116:119]
rt v[192:193],v240 offset:0
rt v[194:195],v240 offset:1024
mm a[128:131],v[48:51],v[160:163],a[128:131]
s_add_u32 m0, s83, 34816
mm a[128:131],v[52:55],v[164:167],a[128:131]
ld v233,s[44:47],s80 offen lds
mm a[132:135],v[60:63],v[164:167],a[132:135]
mm a[132:135],v[56:59],v[160:163],a[132:135]
rt v[196:197],v240 offset:8448
rt v[198:199],v240 offset:9472
mm a[148:151],v[56:59],v[168:171],a[148:151]
s_add_u32 m0, s83, 35968
mm a[148:151],v[60:63],v[172:175],a[148:151]
ld v234,s[44:47],s80 offen lds
mm a[144:147],v[52:55],v[172:175],a[144:147]
ar v224,a112
ar v225,a113
mm a[144:147],v[48:51],v[168:171],a[144:147]
rt v[200:201],v241 offset:0
rt v[202:203],v241 offset:1024
)ASM" R"ASM(mm a[228:231],v[40:43],v[168:171],a[228:231]
s_add_u32 m0, s83, 36992
ar v226,a114
mm a[228:231],v[44:47],v[172:175],a[228:231]
ld v235,s[44:47],s80 offen lds
mm a[224:227],v[36:39],v[172:175],a[224:227]
s_add_u32 s80, s80, 524288
ar v227,a115
mm a[224:227],v[32:35],v[168:171],a[224:227]
rt v[204:205],v241 offset:8448
rt v[206:207],v241 offset:9472
mm a[208:211],v[32:35],v[176:179],a[208:211]
s_add_u32 m0, s83, 50688
ar v228,a116
mm a[208:211],v[36:39],v[180:183],a[208:211]
ld v232,s[44:47],s80 offen lds
mm a[212:215],v[44:47],v[180:183],a[212:215]
ar v229,a117
ar v230,a118
mm a[212:215],v[40:43],v[176:179],a[212:215]
rt v[208:209],v242 offset:0
rt v[210:211],v242 offset:1024
mm a[160:163],v[48:51],v[176:179],a[160:163]
s_add_u32 m0, s83, 51712
ar v231,a119
mm a[160:163],v[52:55],v[180:183],a[160:163]
ld v233,s[44:47],s80 offen lds
mm a[164:167],v[60:63],v[180:183],a[164:167]
cv v248,v224,v225
cv v249,v226,v227
mm a[164:167],v[56:59],v[176:179],a[164:167]
rt v[212:213],v242 offset:8448
rt v[214:215],v242 offset:9472
mm a[180:183],v[56:59],v[184:187],a[180:183]
s_add_u32 m0, s83, 52864
cv v250,v228,v229
mm a[180:183],v[60:63],v[188:191],a[180:183]
ld v234,s[44:47],s80 offen lds
mm a[176:179],v[52:55],v[188:191],a[176:179]
cv v251,v230,v231
st v[248:251],v255,s[48:51],s89 offen sc1
mm a[176:179],v[48:51],v[184:187],a[176:179]
rt v[216:217],v243 offset:0
rt v[218:219],v243 offset:1024
mm a[196:199],v[40:43],v[184:187],a[196:199]
s_add_u32 m0, s83, 53888
s_mov_b32 s89, 52224
mm a[196:199],v[44:47],v[188:191],a[196:199]
ld v235,s[44:47],s80 offen lds
mm a[192:195],v[36:39],v[188:191],a[192:195]
s_add_u32 s80, s80, 524288
ds_write_b128 v238, v[248:251] offset:2048
mm a[192:195],v[32:35],v[184:187],a[192:195]
rt v[220:221],v243 offset:8448
rt v[222:223],v243 offset:9472
mm a[64:67],v[32:35],v[0:3],a[64:67]
mm a[64:67],v[36:39],v[4:7],a[64:67]
ld v[160:163],v244,s[52:55],s91 offen offset:2048 sc1
mm a[68:71],v[44:47],v[4:7],a[68:71]
mm a[68:71],v[40:43],v[0:3],a[68:71]
mm a[0:3],v[48:51],v[0:3],a[0:3]
mm a[0:3],v[52:55],v[4:7],a[0:3]
ld v[164:167],v245,s[52:55],s91 offen offset:2048 sc1
mm a[4:7],v[60:63],v[4:7],a[4:7]
mm a[4:7],v[56:59],v[0:3],a[4:7]
mm a[20:23],v[56:59],v[8:11],a[20:23]
mm a[20:23],v[60:63],v[12:15],a[20:23]
ld v[168:171],v246,s[52:55],s91 offen offset:2048 sc1
mm a[16:19],v[52:55],v[12:15],a[16:19]
mm a[16:19],v[48:51],v[8:11],a[16:19]
mm a[84:87],v[40:43],v[8:11],a[84:87]
mm a[84:87],v[44:47],v[12:15],a[84:87]
ld v[172:175],v247,s[52:55],s91 offen offset:2048 sc1
mm a[80:83],v[36:39],v[12:15],a[80:83]
mm a[80:83],v[32:35],v[8:11],a[80:83]
mm a[240:243],v[32:35],v[16:19],a[240:243]
mm a[240:243],v[36:39],v[20:23],a[240:243]
mm a[244:247],v[44:47],v[20:23],a[244:247]
mm a[244:247],v[40:43],v[16:19],a[244:247]
mm a[32:35],v[48:51],v[16:19],a[32:35]
mm a[32:35],v[52:55],v[20:23],a[32:35]
mm a[36:39],v[60:63],v[20:23],a[36:39]
mm a[36:39],v[56:59],v[16:19],a[36:39]
mm a[60:63],v[56:59],v[24:27],a[60:63]
mm a[60:63],v[60:63],v[28:31],a[60:63]
mm a[56:59],v[52:55],v[28:31],a[56:59]
mm a[56:59],v[48:51],v[24:27],a[56:59]
mm a[52:55],v[40:43],v[24:27],a[52:55]
mm a[52:55],v[44:47],v[28:31],a[52:55]
mm a[48:51],v[36:39],v[28:31],a[48:51]
mm a[48:51],v[32:35],v[24:27],a[48:51]
s_branch .La5_LQ_START
.La5_LV_XSPIN:
s_mov_b32 s93, 0
.La5_LV_XSPIN_L:
s_add_u32 s93, s93, 1
s_cmp_gt_u32 s93, 2097152
s_cbranch_scc1 .La5_LV_XOK
s_sleep 8
ld v[72:75],v255,s[64:67],s89 offen offset:1024 sc1
)ASM" R"ASM(wt vmcnt(0)
v_cmp_eq_u64 vcc, v[72:73], v[252:253]
v_cmp_eq_u64 s[96:97], v[74:75], v[252:253]
s_nop 1
s_and_b64 vcc, vcc, s[96:97]
s_cmp_eq_u64 vcc, -1
s_nop 0
s_cbranch_scc0 .La5_LV_XSPIN_L
s_branch .La5_LV_XOK
.La5_LK_XSPIN:
s_mov_b32 s93, 0
.La5_LK_XSPIN_L:
s_add_u32 s93, s93, 1
s_cmp_gt_u32 s93, 2097152
s_nop 0
s_nop 0
s_cbranch_scc1 .La5_LK_XOK
s_sleep 8
ld v[72:75],v255,s[64:67],s89 offen offset:1024 sc1
wt vmcnt(0)
v_cmp_eq_u64 vcc, v[72:73], v[252:253]
v_cmp_eq_u64 s[96:97], v[74:75], v[252:253]
s_nop 1
s_and_b64 vcc, vcc, s[96:97]
s_cmp_eq_u64 vcc, -1
s_cbranch_scc0 .La5_LK_XSPIN_L
s_branch .La5_LK_XOK
; ---- V tile (waves 1, 3): the same with the operands exchanged (V comes out transposed)
.La5_LV_START:
.La5_LW1_4:
s_cbranch_execz .La5_LW1_8
mm a[96:99],v[160:163],v[192:195],0
s_add_u32 m0, s83, 33792
mm a[96:99],v[164:167],v[196:199],a[96:99]
ld v232,s[44:47],s80 offen lds
mm a[104:107],v[164:167],v[204:207],0
rd v[0:3],v236 offset:8192
rd v[4:7],v237 offset:8192
s_add_u32 m0, s83, 34816
mm a[104:107],v[160:163],v[200:203],a[104:107]
rd v[8:11],v236 offset:10240
rd v[12:15],v237 offset:10240
ld v233,s[44:47],s80 offen lds
mm a[112:115],v[160:163],v[208:211],0
s_add_u32 m0, s83, 35968
mm a[112:115],v[164:167],v[212:215],a[112:115]
ld v234,s[44:47],s80 offen lds
mm a[128:131],v[164:167],v[220:223],0
rd v[16:19],v236 offset:0
rd v[20:23],v237 offset:0
s_add_u32 m0, s83, 36992
mm a[128:131],v[160:163],v[216:219],a[128:131]
rd v[24:27],v236 offset:2048
rd v[28:31],v237 offset:2048
ld v235,s[44:47],s80 offen lds
mm a[132:135],v[168:171],v[216:219],0
s_mov_b32 s80, 2883584
mm a[132:135],v[172:175],v[220:223],a[132:135]
mm a[116:119],v[172:175],v[212:215],0
s_xor_b32 m0, s82, 32768
mm a[116:119],v[168:171],v[208:211],a[116:119]
rt v[32:33],v240 offset:128
rt v[34:35],v240 offset:1152
ld v224,s[40:43],s79 offen lds
mm a[108:111],v[168:171],v[200:203],0
s_xor_b32 m0, s82, 33792
mm a[108:111],v[172:175],v[204:207],a[108:111]
ld v225,s[40:43],s79 offen lds
mm a[100:103],v[172:175],v[196:199],0
s_xor_b32 m0, s82, 34816
mm a[100:103],v[168:171],v[192:195],a[100:103]
rt v[36:37],v240 offset:17024
rt v[38:39],v240 offset:18048
ld v226,s[40:43],s79 offen lds
mm a[232:235],v[176:179],v[192:195],0
s_xor_b32 m0, s82, 35840
mm a[232:235],v[180:183],v[196:199],a[232:235]
ld v227,s[40:43],s79 offen lds
mm a[152:155],v[180:183],v[204:207],0
s_xor_b32 m0, s82, 36864
mm a[152:155],v[176:179],v[200:203],a[152:155]
rt v[40:41],v241 offset:128
rt v[42:43],v241 offset:1152
ld v228,s[40:43],s79 offen lds
mm a[208:211],v[176:179],v[208:211],0
s_xor_b32 m0, s82, 37888
mm a[208:211],v[180:183],v[212:215],a[208:211]
ld v229,s[40:43],s79 offen lds
mm a[160:163],v[180:183],v[220:223],0
s_xor_b32 m0, s82, 38912
mm a[160:163],v[176:179],v[216:219],a[160:163]
ld v230,s[40:43],s79 offen lds
mm a[164:167],v[184:187],v[216:219],0
s_xor_b32 m0, s82, 39936
mm a[164:167],v[188:191],v[220:223],a[164:167]
ld v231,s[40:43],s79 offen lds
mm a[212:215],v[188:191],v[212:215],0
s_mov_b32 s79, 768
mm a[212:215],v[184:187],v[208:211],a[212:215]
rt v[44:45],v241 offset:17024
)ASM" R"ASM(rt v[46:47],v241 offset:18048
mm a[156:159],v[184:187],v[200:203],0
s_add_u32 m0, s83, 50688
mm a[156:159],v[188:191],v[204:207],a[156:159]
ld v232,s[44:47],s80 offen lds
mm a[236:239],v[188:191],v[196:199],0
wt lgkmcnt(8)
s_add_u32 m0, s83, 51712
mm a[236:239],v[184:187],v[192:195],a[236:239]
ld v233,s[44:47],s80 offen lds
mm a[200:203],v[0:3],v[192:195],0
s_add_u32 m0, s83, 52864
mm a[200:203],v[4:7],v[196:199],a[200:203]
ld v234,s[44:47],s80 offen lds
mm a[184:187],v[4:7],v[204:207],0
s_add_u32 m0, s83, 53888
mm a[184:187],v[0:3],v[200:203],a[184:187]
rt v[48:49],v242 offset:128
rt v[50:51],v242 offset:1152
ld v235,s[44:47],s80 offen lds
mm a[64:67],v[0:3],v[208:211],0
s_mov_b32 s80, 3145728
mm a[64:67],v[4:7],v[212:215],a[64:67]
mm a[0:3],v[4:7],v[220:223],0
mm a[0:3],v[0:3],v[216:219],a[0:3]
mm a[4:7],v[8:11],v[216:219],0
mm a[4:7],v[12:15],v[220:223],a[4:7]
buffer_load_dword v70, v118, s[60:63], 0 offen
mm a[68:71],v[12:15],v[212:215],0
mm a[68:71],v[8:11],v[208:211],a[68:71]
rt v[52:53],v242 offset:17024
rt v[54:55],v242 offset:18048
mm a[188:191],v[8:11],v[200:203],0
mm a[188:191],v[12:15],v[204:207],a[188:191]
ld v[72:75],v119,s[60:63],0 offen offset:0
mm a[204:207],v[12:15],v[196:199],0
mm a[204:207],v[8:11],v[192:195],a[204:207]
rt v[56:57],v243 offset:128
rt v[58:59],v243 offset:1152
mm a[88:91],v[16:19],v[192:195],0
mm a[88:91],v[20:23],v[196:199],a[88:91]
ld v[76:79],v119,s[60:63],0 offen offset:128
mm a[24:27],v[20:23],v[204:207],0
mm a[24:27],v[16:19],v[200:203],a[24:27]
rt v[60:61],v243 offset:17024
rt v[62:63],v243 offset:18048
mm a[240:243],v[16:19],v[208:211],0
mm a[240:243],v[20:23],v[212:215],a[240:243]
ld v[80:83],v119,s[60:63],0 offen offset:256
mm a[32:35],v[20:23],v[220:223],0
mm a[32:35],v[16:19],v[216:219],a[32:35]
mm a[36:39],v[24:27],v[216:219],0
mm a[36:39],v[28:31],v[220:223],a[36:39]
ld v[84:87],v119,s[60:63],0 offen offset:384
mm a[244:247],v[28:31],v[212:215],0
mm a[244:247],v[24:27],v[208:211],a[244:247]
mm a[28:31],v[24:27],v[200:203],0
mm a[28:31],v[28:31],v[204:207],a[28:31]
mm a[92:95],v[28:31],v[196:199],0
mm a[92:95],v[24:27],v[192:195],a[92:95]
s_mov_b64 exec, 0
s_cmp_eq_u32 s3, 1
s_cbranch_scc1 .La5_LW2_1
s_branch .La5_LW2_R1
.La5_LW2_R1:
s_mov_b64 exec, -1
wt vmcnt(5) lgkmcnt(0)
s_barrier
mm a[120:123],v[160:163],v[32:35],0
mm a[120:123],v[164:167],v[36:39],a[120:123]
mm a[136:139],v[164:167],v[44:47],0
rt v[192:193],v240 offset:33792
rt v[194:195],v240 offset:34816
mm a[136:139],v[160:163],v[40:43],a[136:139]
rt v[196:197],v240 offset:50688
rt v[198:199],v240 offset:51712
mm a[224:227],v[160:163],v[48:51],0
mm a[224:227],v[164:167],v[52:55],a[224:227]
mm a[144:147],v[164:167],v[60:63],0
mm a[144:147],v[160:163],v[56:59],a[144:147]
rd v[160:163],v236 offset:57344
rd v[164:167],v237 offset:57344
mm a[148:151],v[168:171],v[56:59],0
mm a[148:151],v[172:175],v[60:63],a[148:151]
mm a[228:231],v[172:175],v[52:55],0
mm a[228:231],v[168:171],v[48:51],a[228:231]
rt v[200:201],v241 offset:33792
rt v[202:203],v241 offset:34816
mm a[140:143],v[168:171],v[40:43],0
mm a[140:143],v[172:175],v[44:47],a[140:143]
mm a[124:127],v[172:175],v[36:39],0
mm a[124:127],v[168:171],v[32:35],a[124:127]
rd v[168:171],v236 offset:59392
rd v[172:175],v237 offset:59392
mm a[216:219],v[176:179],v[32:35],0
s_xor_b32 m0, s82, 0
mm a[216:219],v[180:183],v[36:39],a[216:219]
ld v224,s[40:43],s79 offen lds
mm a[168:171],v[180:183],v[44:47],0
mm a[168:171],v[176:179],v[40:43],a[168:171]
)ASM" R"ASM(rt v[204:205],v241 offset:50688
rt v[206:207],v241 offset:51712
s_xor_b32 m0, s82, 1024
mm a[192:195],v[176:179],v[48:51],0
ld v225,s[40:43],s79 offen lds
mm a[192:195],v[180:183],v[52:55],a[192:195]
mm a[176:179],v[180:183],v[60:63],0
s_xor_b32 m0, s82, 2048
mm a[176:179],v[176:179],v[56:59],a[176:179]
rd v[176:179],v236 offset:49152
rd v[180:183],v237 offset:49152
ld v226,s[40:43],s79 offen lds
mm a[180:183],v[184:187],v[56:59],0
mm a[180:183],v[188:191],v[60:63],a[180:183]
s_xor_b32 m0, s82, 3072
mm a[196:199],v[188:191],v[52:55],0
ld v227,s[40:43],s79 offen lds
mm a[196:199],v[184:187],v[48:51],a[196:199]
rt v[208:209],v242 offset:33792
rt v[210:211],v242 offset:34816
mm a[172:175],v[184:187],v[40:43],0
s_xor_b32 m0, s82, 4096
mm a[172:175],v[188:191],v[44:47],a[172:175]
ld v228,s[40:43],s79 offen lds
mm a[220:223],v[188:191],v[36:39],0
mm a[220:223],v[184:187],v[32:35],a[220:223]
rd v[184:187],v236 offset:51200
rd v[188:191],v237 offset:51200
s_xor_b32 m0, s82, 5120
mm a[72:75],v[0:3],v[32:35],0
ld v229,s[40:43],s79 offen lds
mm a[72:75],v[4:7],v[36:39],a[72:75]
mm a[8:11],v[4:7],v[44:47],0
s_xor_b32 m0, s82, 6144
mm a[8:11],v[0:3],v[40:43],a[8:11]
rt v[212:213],v242 offset:50688
rt v[214:215],v242 offset:51712
ld v230,s[40:43],s79 offen lds
mm a[80:83],v[0:3],v[48:51],0
mm a[80:83],v[4:7],v[52:55],a[80:83]
s_xor_b32 m0, s82, 7168
mm a[16:19],v[4:7],v[60:63],0
ld v231,s[40:43],s79 offen lds
mm a[16:19],v[0:3],v[56:59],a[16:19]
rd v[0:3],v236 offset:40960
rd v[4:7],v237 offset:40960
s_mov_b32 s79, 896
mm a[20:23],v[8:11],v[56:59],0
mm a[20:23],v[12:15],v[60:63],a[20:23]
s_add_u32 m0, s83, 0
mm a[84:87],v[12:15],v[52:55],0
ld v232,s[44:47],s80 offen lds
mm a[84:87],v[8:11],v[48:51],a[84:87]
rt v[216:217],v243 offset:33792
rt v[218:219],v243 offset:34816
mm a[12:15],v[8:11],v[40:43],0
s_add_u32 m0, s83, 1024
mm a[12:15],v[12:15],v[44:47],a[12:15]
ld v233,s[44:47],s80 offen lds
mm a[76:79],v[12:15],v[36:39],0
wt vmcnt(10)
v_lshlrev_b32 v70, 16, v70
mm a[76:79],v[8:11],v[32:35],a[76:79]
rd v[8:11],v236 offset:43008
rd v[12:15],v237 offset:43008
s_add_u32 m0, s83, 2176
mm a[248:251],v[16:19],v[32:35],0
ld v234,s[44:47],s80 offen lds
mm a[248:251],v[20:23],v[36:39],a[248:251]
v_lshlrev_b32 v88, 16, v72
v_and_b32 v89, 0xffff0000, v72
mm a[40:43],v[20:23],v[44:47],0
s_add_u32 m0, s83, 3200
v_sub_f32 v88, v88, v70
mm a[40:43],v[16:19],v[40:43],a[40:43]
rt v[220:221],v243 offset:50688
rt v[222:223],v243 offset:51712
ld v235,s[44:47],s80 offen lds
mm a[48:51],v[16:19],v[48:51],0
s_mov_b32 s80, 3407872
v_sub_f32 v89, v89, v70
mm a[48:51],v[20:23],v[52:55],a[48:51]
v_add_f32 v120, v88, v89
v_mul_f32 v122, v88, v88
mm a[56:59],v[20:23],v[60:63],0
s_add_u32 m0, s83, 16896
v_mul_f32 v123, v89, v89
mm a[56:59],v[16:19],v[56:59],a[56:59]
rd v[16:19],v236 offset:32768
rd v[20:23],v237 offset:32768
ld v232,s[44:47],s80 offen lds
mm a[60:63],v[24:27],v[56:59],0
v_lshlrev_b32 v90, 16, v73
v_and_b32 v91, 0xffff0000, v73
mm a[60:63],v[28:31],v[60:63],a[60:63]
s_add_u32 m0, s83, 17920
v_sub_f32 v90, v90, v70
mm a[52:55],v[28:31],v[52:55],0
ld v233,s[44:47],s80 offen lds
mm a[52:55],v[24:27],v[48:51],a[52:55]
)ASM" R"ASM(v_sub_f32 v91, v91, v70
v_add_f32 v124, v90, v91
mm a[44:47],v[24:27],v[40:43],0
s_add_u32 m0, s83, 19072
v_fmac_f32 v122, v90, v90
mm a[44:47],v[28:31],v[44:47],a[44:47]
ld v234,s[44:47],s80 offen lds
mm a[252:255],v[28:31],v[36:39],0
wt lgkmcnt(2)
v_add_f32 v120, v120, v124
mm a[252:255],v[24:27],v[32:35],a[252:255]
rd v[24:27],v236 offset:34816
rd v[28:31],v237 offset:34816
s_add_u32 m0, s83, 20096
mm a[96:99],v[160:163],v[192:195],a[96:99]
ld v235,s[44:47],s80 offen lds
mm a[96:99],v[164:167],v[196:199],a[96:99]
s_mov_b32 s80, 3670016
v_fmac_f32 v123, v91, v91
mm a[104:107],v[164:167],v[204:207],a[104:107]
v_lshlrev_b32 v92, 16, v74
v_and_b32 v93, 0xffff0000, v74
mm a[104:107],v[160:163],v[200:203],a[104:107]
rt v[32:33],v240 offset:33920
rt v[34:35],v240 offset:34944
mm a[112:115],v[160:163],v[208:211],a[112:115]
v_sub_f32 v92, v92, v70
v_sub_f32 v93, v93, v70
mm a[112:115],v[164:167],v[212:215],a[112:115]
v_add_f32 v124, v92, v93
v_fmac_f32 v122, v92, v92
mm a[128:131],v[164:167],v[220:223],a[128:131]
wt lgkmcnt(2)
s_barrier
mm a[128:131],v[160:163],v[216:219],a[128:131]
v_add_f32 v120, v120, v124
v_fmac_f32 v123, v93, v93
mm a[132:135],v[168:171],v[216:219],a[132:135]
s_xor_b32 m0, s82, 32768
v_lshlrev_b32 v94, 16, v75
mm a[132:135],v[172:175],v[220:223],a[132:135]
ld v224,s[40:43],s79 offen lds
mm a[116:119],v[172:175],v[212:215],a[116:119]
v_and_b32 v95, 0xffff0000, v75
v_sub_f32 v94, v94, v70
mm a[116:119],v[168:171],v[208:211],a[116:119]
rt v[36:37],v240 offset:50816
rt v[38:39],v240 offset:51840
mm a[108:111],v[168:171],v[200:203],a[108:111]
s_xor_b32 m0, s82, 33792
v_sub_f32 v95, v95, v70
mm a[108:111],v[172:175],v[204:207],a[108:111]
.La5_LW1_5:
s_cbranch_execz .La5_LW1_9
ld v225,s[40:43],s79 offen lds
mm a[100:103],v[172:175],v[196:199],a[100:103]
v_add_f32 v124, v94, v95
v_fmac_f32 v122, v94, v94
mm a[100:103],v[168:171],v[192:195],a[100:103]
v_add_f32 v120, v120, v124
v_fmac_f32 v123, v95, v95
mm a[232:235],v[176:179],v[192:195],a[232:235]
s_xor_b32 m0, s82, 34816
v_lshlrev_b32 v96, 16, v76
mm a[232:235],v[180:183],v[196:199],a[232:235]
ld v226,s[40:43],s79 offen lds
mm a[152:155],v[180:183],v[204:207],a[152:155]
v_and_b32 v97, 0xffff0000, v76
v_sub_f32 v96, v96, v70
mm a[152:155],v[176:179],v[200:203],a[152:155]
rt v[40:41],v241 offset:33920
rt v[42:43],v241 offset:34944
mm a[208:211],v[176:179],v[208:211],a[208:211]
s_xor_b32 m0, s82, 35840
v_sub_f32 v97, v97, v70
mm a[208:211],v[180:183],v[212:215],a[208:211]
ld v227,s[40:43],s79 offen lds
mm a[160:163],v[180:183],v[220:223],a[160:163]
v_add_f32 v124, v96, v97
v_fmac_f32 v122, v96, v96
mm a[160:163],v[176:179],v[216:219],a[160:163]
v_add_f32 v120, v120, v124
v_fmac_f32 v123, v97, v97
mm a[164:167],v[184:187],v[216:219],a[164:167]
s_xor_b32 m0, s82, 36864
v_lshlrev_b32 v98, 16, v77
mm a[164:167],v[188:191],v[220:223],a[164:167]
ld v228,s[40:43],s79 offen lds
mm a[212:215],v[188:191],v[212:215],a[212:215]
v_and_b32 v99, 0xffff0000, v77
v_sub_f32 v98, v98, v70
mm a[212:215],v[184:187],v[208:211],a[212:215]
rt v[44:45],v241 offset:50816
rt v[46:47],v241 offset:51840
mm a[156:159],v[184:187],v[200:203],a[156:159]
s_xor_b32 m0, s82, 37888
v_sub_f32 v99, v99, v70
mm a[156:159],v[188:191],v[204:207],a[156:159]
ld v229,s[40:43],s79 offen lds
mm a[236:239],v[188:191],v[196:199],a[236:239]
)ASM" R"ASM(v_add_f32 v124, v98, v99
v_fmac_f32 v122, v98, v98
mm a[236:239],v[184:187],v[192:195],a[236:239]
v_add_f32 v120, v120, v124
v_fmac_f32 v123, v99, v99
mm a[200:203],v[0:3],v[192:195],a[200:203]
s_xor_b32 m0, s82, 38912
v_lshlrev_b32 v100, 16, v78
mm a[200:203],v[4:7],v[196:199],a[200:203]
ld v230,s[40:43],s79 offen lds
mm a[184:187],v[4:7],v[204:207],a[184:187]
v_and_b32 v101, 0xffff0000, v78
v_sub_f32 v100, v100, v70
mm a[184:187],v[0:3],v[200:203],a[184:187]
rt v[48:49],v242 offset:33920
rt v[50:51],v242 offset:34944
mm a[64:67],v[0:3],v[208:211],a[64:67]
s_xor_b32 m0, s82, 39936
v_sub_f32 v101, v101, v70
mm a[64:67],v[4:7],v[212:215],a[64:67]
ld v231,s[40:43],s79 offen lds
mm a[0:3],v[4:7],v[220:223],a[0:3]
s_mov_b32 s79, 0
v_add_f32 v124, v100, v101
mm a[0:3],v[0:3],v[216:219],a[0:3]
v_fmac_f32 v122, v100, v100
v_add_f32 v120, v120, v124
mm a[4:7],v[8:11],v[216:219],a[4:7]
v_fmac_f32 v123, v101, v101
v_lshlrev_b32 v102, 16, v79
mm a[4:7],v[12:15],v[220:223],a[4:7]
buffer_load_dword v254, v132, s[44:47], s86 offen
mm a[68:71],v[12:15],v[212:215],a[68:71]
s_add_u32 s86, s86, 2097152
s_and_b32 s86, s86, 4194303
mm a[68:71],v[8:11],v[208:211],a[68:71]
rt v[52:53],v242 offset:50816
rt v[54:55],v242 offset:51840
mm a[188:191],v[8:11],v[200:203],a[188:191]
v_and_b32 v103, 0xffff0000, v79
v_sub_f32 v102, v102, v70
mm a[188:191],v[12:15],v[204:207],a[188:191]
v_sub_f32 v103, v103, v70
v_add_f32 v124, v102, v103
mm a[204:207],v[12:15],v[196:199],a[204:207]
v_fmac_f32 v122, v102, v102
v_add_f32 v120, v120, v124
mm a[204:207],v[8:11],v[192:195],a[204:207]
rt v[56:57],v243 offset:33920
rt v[58:59],v243 offset:34944
mm a[88:91],v[16:19],v[192:195],a[88:91]
v_fmac_f32 v123, v103, v103
v_lshlrev_b32 v104, 16, v80
mm a[88:91],v[20:23],v[196:199],a[88:91]
v_and_b32 v105, 0xffff0000, v80
v_sub_f32 v104, v104, v70
mm a[24:27],v[20:23],v[204:207],a[24:27]
v_sub_f32 v105, v105, v70
v_add_f32 v124, v104, v105
mm a[24:27],v[16:19],v[200:203],a[24:27]
rt v[60:61],v243 offset:50816
rt v[62:63],v243 offset:51840
mm a[240:243],v[16:19],v[208:211],a[240:243]
v_fmac_f32 v122, v104, v104
v_add_f32 v120, v120, v124
mm a[240:243],v[20:23],v[212:215],a[240:243]
v_fmac_f32 v123, v105, v105
v_lshlrev_b32 v106, 16, v81
mm a[32:35],v[20:23],v[220:223],a[32:35]
v_and_b32 v107, 0xffff0000, v81
v_sub_f32 v106, v106, v70
mm a[32:35],v[16:19],v[216:219],a[32:35]
v_sub_f32 v107, v107, v70
v_add_f32 v124, v106, v107
mm a[36:39],v[24:27],v[216:219],a[36:39]
v_fmac_f32 v122, v106, v106
v_add_f32 v120, v120, v124
mm a[36:39],v[28:31],v[220:223],a[36:39]
v_fmac_f32 v123, v107, v107
v_lshlrev_b32 v108, 16, v82
mm a[244:247],v[28:31],v[212:215],a[244:247]
v_and_b32 v109, 0xffff0000, v82
v_sub_f32 v108, v108, v70
mm a[244:247],v[24:27],v[208:211],a[244:247]
v_sub_f32 v109, v109, v70
v_add_f32 v124, v108, v109
mm a[28:31],v[24:27],v[200:203],a[28:31]
v_fmac_f32 v122, v108, v108
v_add_f32 v120, v120, v124
mm a[28:31],v[28:31],v[204:207],a[28:31]
v_fmac_f32 v123, v109, v109
v_lshlrev_b32 v110, 16, v83
mm a[92:95],v[28:31],v[196:199],a[92:95]
wt vmcnt(9) lgkmcnt(0)
s_barrier
mm a[92:95],v[24:27],v[192:195],a[92:95]
v_and_b32 v111, 0xffff0000, v83
v_sub_f32 v110, v110, v70
mm a[120:123],v[160:163],v[32:35],a[120:123]
v_sub_f32 v111, v111, v70
)ASM" R"ASM(v_add_f32 v124, v110, v111
mm a[120:123],v[164:167],v[36:39],a[120:123]
v_fmac_f32 v122, v110, v110
v_add_f32 v120, v120, v124
mm a[136:139],v[164:167],v[44:47],a[136:139]
rt v[192:193],v240 offset:0
rt v[194:195],v240 offset:1024
mm a[136:139],v[160:163],v[40:43],a[136:139]
rt v[196:197],v240 offset:16896
rt v[198:199],v240 offset:17920
mm a[224:227],v[160:163],v[48:51],a[224:227]
v_fmac_f32 v123, v111, v111
v_lshlrev_b32 v112, 16, v84
mm a[224:227],v[164:167],v[52:55],a[224:227]
v_and_b32 v113, 0xffff0000, v84
v_sub_f32 v112, v112, v70
mm a[144:147],v[164:167],v[60:63],a[144:147]
v_sub_f32 v113, v113, v70
v_add_f32 v124, v112, v113
mm a[144:147],v[160:163],v[56:59],a[144:147]
rd v[160:163],v236 offset:24576
rd v[164:167],v237 offset:24576
mm a[148:151],v[168:171],v[56:59],a[148:151]
v_fmac_f32 v122, v112, v112
v_add_f32 v120, v120, v124
mm a[148:151],v[172:175],v[60:63],a[148:151]
v_fmac_f32 v123, v113, v113
v_lshlrev_b32 v114, 16, v85
mm a[228:231],v[172:175],v[52:55],a[228:231]
v_and_b32 v115, 0xffff0000, v85
v_sub_f32 v114, v114, v70
mm a[228:231],v[168:171],v[48:51],a[228:231]
rt v[200:201],v241 offset:0
rt v[202:203],v241 offset:1024
mm a[140:143],v[168:171],v[40:43],a[140:143]
v_sub_f32 v115, v115, v70
v_add_f32 v124, v114, v115
mm a[140:143],v[172:175],v[44:47],a[140:143]
v_fmac_f32 v122, v114, v114
v_add_f32 v120, v120, v124
mm a[124:127],v[172:175],v[36:39],a[124:127]
v_fmac_f32 v123, v115, v115
v_lshlrev_b32 v116, 16, v86
mm a[124:127],v[168:171],v[32:35],a[124:127]
rd v[168:171],v236 offset:26624
rd v[172:175],v237 offset:26624
mm a[216:219],v[176:179],v[32:35],a[216:219]
v_and_b32 v117, 0xffff0000, v86
v_sub_f32 v116, v116, v70
mm a[216:219],v[180:183],v[36:39],a[216:219]
v_sub_f32 v117, v117, v70
v_add_f32 v124, v116, v117
mm a[168:171],v[180:183],v[44:47],a[168:171]
v_fmac_f32 v122, v116, v116
v_add_f32 v120, v120, v124
mm a[168:171],v[176:179],v[40:43],a[168:171]
rt v[204:205],v241 offset:16896
rt v[206:207],v241 offset:17920
mm a[192:195],v[176:179],v[48:51],a[192:195]
v_fmac_f32 v123, v117, v117
v_lshlrev_b32 v118, 16, v87
mm a[192:195],v[180:183],v[52:55],a[192:195]
v_and_b32 v119, 0xffff0000, v87
v_sub_f32 v118, v118, v70
mm a[176:179],v[180:183],v[60:63],a[176:179]
v_sub_f32 v119, v119, v70
v_add_f32 v124, v118, v119
mm a[176:179],v[176:179],v[56:59],a[176:179]
rd v[176:179],v236 offset:16384
rd v[180:183],v237 offset:16384
mm a[180:183],v[184:187],v[56:59],a[180:183]
v_fmac_f32 v122, v118, v118
v_add_f32 v120, v120, v124
mm a[180:183],v[188:191],v[60:63],a[180:183]
v_fmac_f32 v123, v119, v119
v_add_f32 v121, v122, v123
mm a[196:199],v[188:191],v[52:55],a[196:199]
v_add_f32_dpp v120, v120, v120 row_ror:8 row_mask:0xf bank_mask:0xf
mm a[196:199],v[184:187],v[48:51],a[196:199]
rt v[208:209],v242 offset:0
rt v[210:211],v242 offset:1024
mm a[172:175],v[184:187],v[40:43],a[172:175]
s_add_u32 m0, s83, 33792
v_add_f32_dpp v121, v121, v121 row_ror:8 row_mask:0xf bank_mask:0xf
mm a[172:175],v[188:191],v[44:47],a[172:175]
ld v232,s[44:47],s80 offen lds
mm a[220:223],v[188:191],v[36:39],a[220:223]
v_mov_b32 v124, v120
v_mov_b32 v125, v121
mm a[220:223],v[184:187],v[32:35],a[220:223]
rd v[184:187],v236 offset:18432
rd v[188:191],v237 offset:18432
mm a[72:75],v[0:3],v[32:35],a[72:75]
s_add_u32 m0, s83, 34816
v_permlane16_swap_b32 v124, v120
mm a[72:75],v[4:7],v[36:39],a[72:75]
ld v233,s[44:47],s80 offen lds
mm a[8:11],v[4:7],v[44:47],a[8:11]
v_permlane16_swap_b32 v125, v121
mm a[8:11],v[0:3],v[40:43],a[8:11]
)ASM" R"ASM(rt v[212:213],v242 offset:16896
rt v[214:215],v242 offset:17920
mm a[80:83],v[0:3],v[48:51],a[80:83]
s_add_u32 m0, s83, 35968
v_add_f32 v120, v120, v124
mm a[80:83],v[4:7],v[52:55],a[80:83]
ld v234,s[44:47],s80 offen lds
mm a[16:19],v[4:7],v[60:63],a[16:19]
v_add_f32 v121, v121, v125
v_mov_b32 v124, v120
mm a[16:19],v[0:3],v[56:59],a[16:19]
rd v[0:3],v236 offset:8192
rd v[4:7],v237 offset:8192
mm a[20:23],v[8:11],v[56:59],a[20:23]
s_add_u32 m0, s83, 36992
v_mov_b32 v125, v121
mm a[20:23],v[12:15],v[60:63],a[20:23]
ld v235,s[44:47],s80 offen lds
mm a[84:87],v[12:15],v[52:55],a[84:87]
s_mov_b32 s80, 3932160
v_permlane32_swap_b32 v124, v120
mm a[84:87],v[8:11],v[48:51],a[84:87]
rt v[216:217],v243 offset:0
rt v[218:219],v243 offset:1024
mm a[12:15],v[8:11],v[40:43],a[12:15]
s_add_u32 m0, s83, 50688
v_permlane32_swap_b32 v125, v121
mm a[12:15],v[12:15],v[44:47],a[12:15]
ld v232,s[44:47],s80 offen lds
mm a[76:79],v[12:15],v[36:39],a[76:79]
v_add_f32 v120, v120, v124
v_add_f32 v121, v121, v125
mm a[76:79],v[8:11],v[32:35],a[76:79]
rd v[8:11],v236 offset:10240
rd v[12:15],v237 offset:10240
mm a[248:251],v[16:19],v[32:35],a[248:251]
s_add_u32 m0, s83, 51712
ds_write_b64 v127, v[120:121]
mm a[248:251],v[20:23],v[36:39],a[248:251]
ld v233,s[44:47],s80 offen lds
mm a[40:43],v[20:23],v[44:47],a[40:43]
s_nop 0
v_readfirstlane_b32 s92, v252
mm a[40:43],v[16:19],v[40:43],a[40:43]
rt v[220:221],v243 offset:16896
rt v[222:223],v243 offset:17920
mm a[48:51],v[16:19],v[48:51],a[48:51]
s_add_u32 m0, s83, 52864
v_readfirstlane_b32 s93, v253
mm a[48:51],v[20:23],v[52:55],a[48:51]
ld v234,s[44:47],s80 offen lds
mm a[56:59],v[20:23],v[60:63],a[56:59]
s_add_u32 s92, s92, 1
s_addc_u32 s93, s93, 0
mm a[56:59],v[16:19],v[56:59],a[56:59]
rd v[16:19],v236 offset:0
rd v[20:23],v237 offset:0
mm a[60:63],v[24:27],v[56:59],a[60:63]
s_add_u32 m0, s83, 53888
v_mov_b32 v252, s92
mm a[60:63],v[28:31],v[60:63],a[60:63]
ld v235,s[44:47],s80 offen lds
mm a[52:55],v[28:31],v[52:55],a[52:55]
s_mov_b32 s80, 0
v_mov_b32 v253, s93
mm a[52:55],v[24:27],v[48:51],a[52:55]
v_mov_b32 v128, 0
v_mov_b32 v129, 0
mm a[44:47],v[24:27],v[40:43],a[44:47]
v_mov_b32 v130, 0
v_mov_b32 v131, 0
mm a[44:47],v[28:31],v[44:47],a[44:47]
st v[128:131],v255,s[52:55],s95 offen sc1
mm a[252:255],v[28:31],v[36:39],a[252:255]
wt lgkmcnt(2)
s_xor_b32 s93, s72, 0
mm a[252:255],v[24:27],v[32:35],a[252:255]
rd v[24:27],v236 offset:2048
rd v[28:31],v237 offset:2048
mm a[96:99],v[160:163],v[192:195],a[96:99]
s_lshl_b32 s92, s76, 5
s_add_u32 s92, s92, s93
mm a[96:99],v[164:167],v[196:199],a[96:99]
s_lshl2_add_u32 s92, s92, s3
s_lshl_b32 s92, s92, 10
mm a[104:107],v[164:167],v[204:207],a[104:107]
s_add_u32 s92, s92, 1048576
v_mov_b32 v128, -1
mm a[104:107],v[160:163],v[200:203],a[104:107]
rt v[32:33],v240 offset:128
rt v[34:35],v240 offset:1152
mm a[112:115],v[160:163],v[208:211],a[112:115]
v_mov_b32 v129, -1
v_mov_b32 v130, -1
mm a[112:115],v[164:167],v[212:215],a[112:115]
v_mov_b32 v131, -1
st v[128:131],v255,s[52:55],s92 offen sc1
mm a[128:131],v[164:167],v[220:223],a[128:131]
wt lgkmcnt(2)
s_barrier
)ASM" R"ASM(mm a[128:131],v[160:163],v[216:219],a[128:131]
rd v[72:75],v126
rd v[76:79],v126 offset:16
mm a[132:135],v[168:171],v[216:219],a[132:135]
s_xor_b32 m0, s82, 0
mm a[132:135],v[172:175],v[220:223],a[132:135]
ld v224,s[40:43],s79 offen lds
mm a[116:119],v[172:175],v[212:215],a[116:119]
mm a[116:119],v[168:171],v[208:211],a[116:119]
rt v[36:37],v240 offset:17024
rt v[38:39],v240 offset:18048
mm a[108:111],v[168:171],v[200:203],a[108:111]
s_xor_b32 m0, s82, 1024
mm a[108:111],v[172:175],v[204:207],a[108:111]
ld v225,s[40:43],s79 offen lds
mm a[100:103],v[172:175],v[196:199],a[100:103]
wt lgkmcnt(2)
v_add_f32 v72, v72, v74
mm a[100:103],v[168:171],v[192:195],a[100:103]
v_add_f32 v73, v73, v75
v_add_f32 v76, v76, v78
mm a[232:235],v[176:179],v[192:195],a[232:235]
s_xor_b32 m0, s82, 2048
v_add_f32 v77, v77, v79
mm a[232:235],v[180:183],v[196:199],a[232:235]
ld v226,s[40:43],s79 offen lds
mm a[152:155],v[180:183],v[204:207],a[152:155]
v_add_f32 v72, v72, v76
v_add_f32 v73, v73, v77
mm a[152:155],v[176:179],v[200:203],a[152:155]
rt v[40:41],v241 offset:128
rt v[42:43],v241 offset:1152
mm a[208:211],v[176:179],v[208:211],a[208:211]
s_xor_b32 m0, s82, 3072
v_mul_f32 v72, 0x3a800000, v72
mm a[208:211],v[180:183],v[212:215],a[208:211]
ld v227,s[40:43],s79 offen lds
mm a[160:163],v[180:183],v[220:223],a[160:163]
v_mul_f32 v73, 0x3a800000, v73
v_add_f32 v124, v70, v72
mm a[160:163],v[176:179],v[216:219],a[160:163]
v_fma_f32 v125, -v72, v72, v73
cv v124,v124,v124
mm a[164:167],v[184:187],v[216:219],a[164:167]
s_xor_b32 m0, s82, 4096
v_lshlrev_b32 v124, 16, v124
mm a[164:167],v[188:191],v[220:223],a[164:167]
ld v228,s[40:43],s79 offen lds
mm a[212:215],v[188:191],v[212:215],a[212:215]
v_max_f32 v125, 0, v125
cv v125,v125,v125
mm a[212:215],v[184:187],v[208:211],a[212:215]
rt v[44:45],v241 offset:17024
rt v[46:47],v241 offset:18048
mm a[156:159],v[184:187],v[200:203],a[156:159]
s_xor_b32 m0, s82, 5120
v_lshlrev_b32 v125, 16, v125
mm a[156:159],v[188:191],v[204:207],a[156:159]
ld v229,s[40:43],s79 offen lds
mm a[236:239],v[188:191],v[196:199],a[236:239]
v_add_f32 v125, 0x3727c5ac, v125
cv v125,v125,v125
mm a[236:239],v[184:187],v[192:195],a[236:239]
v_lshlrev_b32 v125, 16, v125
mm a[200:203],v[0:3],v[192:195],a[200:203]
s_xor_b32 m0, s82, 6144
mm a[200:203],v[4:7],v[196:199],a[200:203]
ld v230,s[40:43],s79 offen lds
mm a[184:187],v[4:7],v[204:207],a[184:187]
v_sqrt_f32 v125, v125
mm a[184:187],v[0:3],v[200:203],a[184:187]
rt v[48:49],v242 offset:128
rt v[50:51],v242 offset:1152
mm a[64:67],v[0:3],v[208:211],a[64:67]
s_xor_b32 m0, s82, 7168
v_sub_f32 v124, v70, v124
mm a[64:67],v[4:7],v[212:215],a[64:67]
ld v231,s[40:43],s79 offen lds
mm a[0:3],v[4:7],v[220:223],a[0:3]
s_mov_b32 s79, 128
cv v125,v125,v125
mm a[0:3],v[0:3],v[216:219],a[0:3]
v_lshlrev_b32 v125, 16, v125
mm a[4:7],v[8:11],v[216:219],a[4:7]
v_rcp_f32 v125, v125
mm a[4:7],v[12:15],v[220:223],a[4:7]
v_mul_f32 v125, 0x3e0293ee, v125
v_mul_f32 v124, v124, v125
mm a[68:71],v[12:15],v[212:215],a[68:71]
v_fma_f32 v88, v88, v125, v124
v_fma_f32 v89, v89, v125, v124
mm a[68:71],v[8:11],v[208:211],a[68:71]
rt v[52:53],v242 offset:17024
rt v[54:55],v242 offset:18048
mm a[188:191],v[8:11],v[200:203],a[188:191]
cv v72,v88,v89
v_fma_f32 v90, v90, v125, v124
mm a[188:191],v[12:15],v[204:207],a[188:191]
v_fma_f32 v91, v91, v125, v124
cv v73,v90,v91
)ASM" R"ASM(mm a[204:207],v[12:15],v[196:199],a[204:207]
v_fma_f32 v92, v92, v125, v124
v_fma_f32 v93, v93, v125, v124
mm a[204:207],v[8:11],v[192:195],a[204:207]
rt v[56:57],v243 offset:128
rt v[58:59],v243 offset:1152
mm a[88:91],v[16:19],v[192:195],a[88:91]
cv v74,v92,v93
v_fma_f32 v94, v94, v125, v124
mm a[88:91],v[20:23],v[196:199],a[88:91]
v_fma_f32 v95, v95, v125, v124
cv v75,v94,v95
mm a[24:27],v[20:23],v[204:207],a[24:27]
st v[72:75],v71,s[56:59],0 offen offset:0 sc1
mm a[24:27],v[16:19],v[200:203],a[24:27]
rt v[60:61],v243 offset:17024
rt v[62:63],v243 offset:18048
mm a[240:243],v[16:19],v[208:211],a[240:243]
v_fma_f32 v96, v96, v125, v124
v_fma_f32 v97, v97, v125, v124
mm a[240:243],v[20:23],v[212:215],a[240:243]
cv v76,v96,v97
v_fma_f32 v98, v98, v125, v124
mm a[32:35],v[20:23],v[220:223],a[32:35]
v_fma_f32 v99, v99, v125, v124
cv v77,v98,v99
mm a[32:35],v[16:19],v[216:219],a[32:35]
v_fma_f32 v100, v100, v125, v124
v_fma_f32 v101, v101, v125, v124
mm a[36:39],v[24:27],v[216:219],a[36:39]
cv v78,v100,v101
v_fma_f32 v102, v102, v125, v124
mm a[36:39],v[28:31],v[220:223],a[36:39]
v_fma_f32 v103, v103, v125, v124
cv v79,v102,v103
mm a[244:247],v[28:31],v[212:215],a[244:247]
st v[76:79],v71,s[56:59],0 offen offset:2048 sc1
mm a[244:247],v[24:27],v[208:211],a[244:247]
v_fma_f32 v104, v104, v125, v124
v_fma_f32 v105, v105, v125, v124
mm a[28:31],v[24:27],v[200:203],a[28:31]
cv v80,v104,v105
v_fma_f32 v106, v106, v125, v124
mm a[28:31],v[28:31],v[204:207],a[28:31]
v_fma_f32 v107, v107, v125, v124
cv v81,v106,v107
mm a[92:95],v[28:31],v[196:199],a[92:95]
wt vmcnt(12) lgkmcnt(0)
s_barrier
mm a[92:95],v[24:27],v[192:195],a[92:95]
v_fma_f32 v108, v108, v125, v124
v_fma_f32 v109, v109, v125, v124
mm a[120:123],v[160:163],v[32:35],a[120:123]
cv v82,v108,v109
v_fma_f32 v110, v110, v125, v124
mm a[120:123],v[164:167],v[36:39],a[120:123]
v_fma_f32 v111, v111, v125, v124
cv v83,v110,v111
mm a[136:139],v[164:167],v[44:47],a[136:139]
rt v[192:193],v240 offset:33792
rt v[194:195],v240 offset:34816
mm a[136:139],v[160:163],v[40:43],a[136:139]
rt v[196:197],v240 offset:50688
rt v[198:199],v240 offset:51712
mm a[224:227],v[160:163],v[48:51],a[224:227]
st v[80:83],v71,s[56:59],s88 offen offset:0 sc1
mm a[224:227],v[164:167],v[52:55],a[224:227]
v_fma_f32 v112, v112, v125, v124
v_fma_f32 v113, v113, v125, v124
mm a[144:147],v[164:167],v[60:63],a[144:147]
cv v84,v112,v113
v_fma_f32 v114, v114, v125, v124
mm a[144:147],v[160:163],v[56:59],a[144:147]
rd v[160:163],v236 offset:57344
rd v[164:167],v237 offset:57344
mm a[148:151],v[168:171],v[56:59],a[148:151]
v_fma_f32 v115, v115, v125, v124
cv v85,v114,v115
mm a[148:151],v[172:175],v[60:63],a[148:151]
v_fma_f32 v116, v116, v125, v124
v_fma_f32 v117, v117, v125, v124
mm a[228:231],v[172:175],v[52:55],a[228:231]
cv v86,v116,v117
v_fma_f32 v118, v118, v125, v124
mm a[228:231],v[168:171],v[48:51],a[228:231]
rt v[200:201],v241 offset:33792
rt v[202:203],v241 offset:34816
mm a[140:143],v[168:171],v[40:43],a[140:143]
v_fma_f32 v119, v119, v125, v124
cv v87,v118,v119
mm a[140:143],v[172:175],v[44:47],a[140:143]
st v[84:87],v71,s[56:59],s88 offen offset:2048 sc1
mm a[124:127],v[172:175],v[36:39],a[124:127]
mm a[124:127],v[168:171],v[32:35],a[124:127]
rd v[168:171],v236 offset:59392
rd v[172:175],v237 offset:59392
mm a[216:219],v[176:179],v[32:35],a[216:219]
mm a[216:219],v[180:183],v[36:39],a[216:219]
mm a[168:171],v[180:183],v[44:47],a[168:171]
mm a[168:171],v[176:179],v[40:43],a[168:171]
)ASM" R"ASM(rt v[204:205],v241 offset:50688
rt v[206:207],v241 offset:51712
mm a[192:195],v[176:179],v[48:51],a[192:195]
mm a[192:195],v[180:183],v[52:55],a[192:195]
mm a[176:179],v[180:183],v[60:63],a[176:179]
mm a[176:179],v[176:179],v[56:59],a[176:179]
rd v[176:179],v236 offset:49152
rd v[180:183],v237 offset:49152
mm a[180:183],v[184:187],v[56:59],a[180:183]
mm a[180:183],v[188:191],v[60:63],a[180:183]
mm a[196:199],v[188:191],v[52:55],a[196:199]
mm a[196:199],v[184:187],v[48:51],a[196:199]
rt v[208:209],v242 offset:33792
rt v[210:211],v242 offset:34816
mm a[172:175],v[184:187],v[40:43],a[172:175]
s_add_u32 m0, s83, 0
mm a[172:175],v[188:191],v[44:47],a[172:175]
ld v232,s[44:47],s80 offen lds
mm a[220:223],v[188:191],v[36:39],a[220:223]
mm a[220:223],v[184:187],v[32:35],a[220:223]
rd v[184:187],v236 offset:51200
rd v[188:191],v237 offset:51200
mm a[72:75],v[0:3],v[32:35],a[72:75]
s_add_u32 m0, s83, 1024
mm a[72:75],v[4:7],v[36:39],a[72:75]
ld v233,s[44:47],s80 offen lds
mm a[8:11],v[4:7],v[44:47],a[8:11]
mm a[8:11],v[0:3],v[40:43],a[8:11]
rt v[212:213],v242 offset:50688
rt v[214:215],v242 offset:51712
mm a[80:83],v[0:3],v[48:51],a[80:83]
s_add_u32 m0, s83, 2176
mm a[80:83],v[4:7],v[52:55],a[80:83]
ld v234,s[44:47],s80 offen lds
mm a[16:19],v[4:7],v[60:63],a[16:19]
mm a[16:19],v[0:3],v[56:59],a[16:19]
rd v[0:3],v236 offset:40960
rd v[4:7],v237 offset:40960
mm a[20:23],v[8:11],v[56:59],a[20:23]
s_add_u32 m0, s83, 3200
.La5_LW1_6:
s_cbranch_execz .La5_LW1_10
mm a[20:23],v[12:15],v[60:63],a[20:23]
ld v235,s[44:47],s80 offen lds
mm a[84:87],v[12:15],v[52:55],a[84:87]
s_mov_b32 s80, 262144
mm a[84:87],v[8:11],v[48:51],a[84:87]
rt v[216:217],v243 offset:33792
rt v[218:219],v243 offset:34816
mm a[12:15],v[8:11],v[40:43],a[12:15]
s_add_u32 m0, s83, 16896
mm a[12:15],v[12:15],v[44:47],a[12:15]
ld v232,s[44:47],s80 offen lds
mm a[76:79],v[12:15],v[36:39],a[76:79]
mm a[76:79],v[8:11],v[32:35],a[76:79]
rd v[8:11],v236 offset:43008
rd v[12:15],v237 offset:43008
mm a[248:251],v[16:19],v[32:35],a[248:251]
s_add_u32 m0, s83, 17920
mm a[248:251],v[20:23],v[36:39],a[248:251]
ld v233,s[44:47],s80 offen lds
mm a[40:43],v[20:23],v[44:47],a[40:43]
mm a[40:43],v[16:19],v[40:43],a[40:43]
rt v[220:221],v243 offset:50688
rt v[222:223],v243 offset:51712
mm a[48:51],v[16:19],v[48:51],a[48:51]
s_add_u32 m0, s83, 19072
mm a[48:51],v[20:23],v[52:55],a[48:51]
ld v234,s[44:47],s80 offen lds
mm a[56:59],v[20:23],v[60:63],a[56:59]
mm a[56:59],v[16:19],v[56:59],a[56:59]
rd v[16:19],v236 offset:32768
rd v[20:23],v237 offset:32768
mm a[60:63],v[24:27],v[56:59],a[60:63]
s_add_u32 m0, s83, 20096
mm a[60:63],v[28:31],v[60:63],a[60:63]
ld v235,s[44:47],s80 offen lds
mm a[52:55],v[28:31],v[52:55],a[52:55]
s_mov_b32 s80, 524288
mm a[52:55],v[24:27],v[48:51],a[52:55]
mm a[44:47],v[24:27],v[40:43],a[44:47]
mm a[44:47],v[28:31],v[44:47],a[44:47]
mm a[252:255],v[28:31],v[36:39],a[252:255]
wt lgkmcnt(2)
mm a[252:255],v[24:27],v[32:35],a[252:255]
rd v[24:27],v236 offset:34816
rd v[28:31],v237 offset:34816
mm a[96:99],v[160:163],v[192:195],a[96:99]
mm a[96:99],v[164:167],v[196:199],a[96:99]
mm a[104:107],v[164:167],v[204:207],a[104:107]
mm a[104:107],v[160:163],v[200:203],a[104:107]
rt v[32:33],v240 offset:33920
rt v[34:35],v240 offset:34944
mm a[112:115],v[160:163],v[208:211],a[112:115]
mm a[112:115],v[164:167],v[212:215],a[112:115]
mm a[128:131],v[164:167],v[220:223],a[128:131]
wt lgkmcnt(2)
s_barrier
mm a[128:131],v[160:163],v[216:219],a[128:131]
mm a[132:135],v[168:171],v[216:219],a[132:135]
)ASM" R"ASM(s_xor_b32 m0, s82, 32768
mm a[132:135],v[172:175],v[220:223],a[132:135]
ld v224,s[40:43],s79 offen lds
mm a[116:119],v[172:175],v[212:215],a[116:119]
mm a[116:119],v[168:171],v[208:211],a[116:119]
rt v[36:37],v240 offset:50816
rt v[38:39],v240 offset:51840
mm a[108:111],v[168:171],v[200:203],a[108:111]
s_xor_b32 m0, s82, 33792
mm a[108:111],v[172:175],v[204:207],a[108:111]
ld v225,s[40:43],s79 offen lds
mm a[100:103],v[172:175],v[196:199],a[100:103]
mm a[100:103],v[168:171],v[192:195],a[100:103]
mm a[232:235],v[176:179],v[192:195],a[232:235]
s_xor_b32 m0, s82, 34816
mm a[232:235],v[180:183],v[196:199],a[232:235]
ld v226,s[40:43],s79 offen lds
mm a[152:155],v[180:183],v[204:207],a[152:155]
mm a[152:155],v[176:179],v[200:203],a[152:155]
rt v[40:41],v241 offset:33920
rt v[42:43],v241 offset:34944
mm a[208:211],v[176:179],v[208:211],a[208:211]
s_xor_b32 m0, s82, 35840
mm a[208:211],v[180:183],v[212:215],a[208:211]
ld v227,s[40:43],s79 offen lds
mm a[160:163],v[180:183],v[220:223],a[160:163]
mm a[160:163],v[176:179],v[216:219],a[160:163]
mm a[164:167],v[184:187],v[216:219],a[164:167]
s_xor_b32 m0, s82, 36864
mm a[164:167],v[188:191],v[220:223],a[164:167]
ld v228,s[40:43],s79 offen lds
mm a[212:215],v[188:191],v[212:215],a[212:215]
mm a[212:215],v[184:187],v[208:211],a[212:215]
rt v[44:45],v241 offset:50816
rt v[46:47],v241 offset:51840
mm a[156:159],v[184:187],v[200:203],a[156:159]
s_xor_b32 m0, s82, 37888
mm a[156:159],v[188:191],v[204:207],a[156:159]
ld v229,s[40:43],s79 offen lds
mm a[236:239],v[188:191],v[196:199],a[236:239]
mm a[236:239],v[184:187],v[192:195],a[236:239]
mm a[200:203],v[0:3],v[192:195],a[200:203]
s_xor_b32 m0, s82, 38912
mm a[200:203],v[4:7],v[196:199],a[200:203]
ld v230,s[40:43],s79 offen lds
mm a[184:187],v[4:7],v[204:207],a[184:187]
mm a[184:187],v[0:3],v[200:203],a[184:187]
rt v[48:49],v242 offset:33920
rt v[50:51],v242 offset:34944
mm a[64:67],v[0:3],v[208:211],a[64:67]
s_xor_b32 m0, s82, 39936
mm a[64:67],v[4:7],v[212:215],a[64:67]
ld v231,s[40:43],s79 offen lds
mm a[0:3],v[4:7],v[220:223],a[0:3]
s_mov_b32 s79, 256
mm a[0:3],v[0:3],v[216:219],a[0:3]
mm a[4:7],v[8:11],v[216:219],a[4:7]
mm a[4:7],v[12:15],v[220:223],a[4:7]
buffer_load_dword v254, v135, s[68:71], s96 offen
mm a[68:71],v[12:15],v[212:215],a[68:71]
s_add_u32 s96, s96, 1048576
mm a[68:71],v[8:11],v[208:211],a[68:71]
rt v[52:53],v242 offset:50816
rt v[54:55],v242 offset:51840
mm a[188:191],v[8:11],v[200:203],a[188:191]
mm a[188:191],v[12:15],v[204:207],a[188:191]
buffer_load_dword v254, v135, s[68:71], s96 offen
mm a[204:207],v[12:15],v[196:199],a[204:207]
s_add_u32 s96, s96, 1048576
mm a[204:207],v[8:11],v[192:195],a[204:207]
rt v[56:57],v243 offset:33920
rt v[58:59],v243 offset:34944
mm a[88:91],v[16:19],v[192:195],a[88:91]
mm a[88:91],v[20:23],v[196:199],a[88:91]
wt vmcnt(18)
buffer_store_dwordx2 v[252:253], off, s[64:67], s91 offset:1024 sc1
mm a[24:27],v[20:23],v[204:207],a[24:27]
s_mov_b32 s91, 0
s_lshl_b32 s92, s76, 19
mm a[24:27],v[16:19],v[200:203],a[24:27]
rt v[60:61],v243 offset:50816
rt v[62:63],v243 offset:51840
mm a[240:243],v[16:19],v[208:211],a[240:243]
s_add_u32 s92, s92, 88080384
mm a[240:243],v[20:23],v[212:215],a[240:243]
s_add_u32 s52, s26, s92
s_addc_u32 s53, s27, 0
mm a[32:35],v[20:23],v[220:223],a[32:35]
s_and_b32 s53, s53, 0xffff
s_mov_b32 s54, 524672
mm a[32:35],v[16:19],v[216:219],a[32:35]
s_mov_b32 s55, 0x20000
s_lshl_b32 s92, s3, 16
mm a[36:39],v[24:27],v[216:219],a[36:39]
s_sub_u32 s93, 458752, s92
v_add_u32 v244, s92, v136
mm a[36:39],v[28:31],v[220:223],a[36:39]
v_add_u32 v246, s93, v136
v_add_u32 v245, 0x8000, v244
mm a[244:247],v[28:31],v[212:215],a[244:247]
)ASM" R"ASM(v_add_u32 v247, 0x8000, v246
s_lshl_b32 s94, s72, 19
mm a[244:247],v[24:27],v[208:211],a[244:247]
s_lshl_b32 s93, s76, 16
s_add_u32 s92, s94, s93
mm a[28:31],v[24:27],v[200:203],a[28:31]
s_lshl_b32 s93, s77, 13
s_add_u32 s92, s92, s93
mm a[28:31],v[28:31],v[204:207],a[28:31]
s_add_u32 s92, s92, 37748736
mm a[92:95],v[28:31],v[196:199],a[92:95]
wt vmcnt(11) lgkmcnt(0)
s_barrier
mm a[92:95],v[24:27],v[192:195],a[92:95]
s_add_u32 s48, s26, s92
s_addc_u32 s49, s27, 0
mm a[120:123],v[160:163],v[32:35],a[120:123]
s_and_b32 s49, s49, 0xffff
s_mov_b32 s50, 57344
mm a[120:123],v[164:167],v[36:39],a[120:123]
s_mov_b32 s51, 0x20000
s_lshl_b32 s93, s77, 24
mm a[136:139],v[164:167],v[44:47],a[136:139]
rt v[192:193],v240 offset:0
rt v[194:195],v240 offset:1024
mm a[136:139],v[160:163],v[40:43],a[136:139]
rt v[196:197],v240 offset:16896
rt v[198:199],v240 offset:17920
mm a[224:227],v[160:163],v[48:51],a[224:227]
s_add_u32 s92, s94, s93
s_add_u32 s92, s92, 20975616
mm a[224:227],v[164:167],v[52:55],a[224:227]
s_add_u32 s60, s26, s92
s_addc_u32 s61, s27, 0
mm a[144:147],v[164:167],v[60:63],a[144:147]
s_and_b32 s61, s61, 0xffff
s_mov_b32 s62, 520192
mm a[144:147],v[160:163],v[56:59],a[144:147]
rd v[160:163],v236 offset:24576
rd v[164:167],v237 offset:24576
mm a[148:151],v[168:171],v[56:59],a[148:151]
s_mov_b32 s63, 0x20000
s_lshl_b32 s92, s77, 14
mm a[148:151],v[172:175],v[60:63],a[148:151]
s_sub_u32 s95, 24576, s92
v_add_u32 v238, s95, v255
mm a[228:231],v[172:175],v[52:55],a[228:231]
mm a[228:231],v[168:171],v[48:51],a[228:231]
rt v[200:201],v241 offset:0
rt v[202:203],v241 offset:1024
mm a[140:143],v[168:171],v[40:43],a[140:143]
mm a[140:143],v[172:175],v[44:47],a[140:143]
mm a[124:127],v[172:175],v[36:39],a[124:127]
mm a[124:127],v[168:171],v[32:35],a[124:127]
rd v[168:171],v236 offset:26624
rd v[172:175],v237 offset:26624
mm a[216:219],v[176:179],v[32:35],a[216:219]
mm a[216:219],v[180:183],v[36:39],a[216:219]
mm a[168:171],v[180:183],v[44:47],a[168:171]
mm a[168:171],v[176:179],v[40:43],a[168:171]
rt v[204:205],v241 offset:16896
rt v[206:207],v241 offset:17920
mm a[192:195],v[176:179],v[48:51],a[192:195]
mm a[192:195],v[180:183],v[52:55],a[192:195]
mm a[176:179],v[180:183],v[60:63],a[176:179]
mm a[176:179],v[176:179],v[56:59],a[176:179]
rd v[176:179],v236 offset:16384
rd v[180:183],v237 offset:16384
mm a[180:183],v[184:187],v[56:59],a[180:183]
mm a[180:183],v[188:191],v[60:63],a[180:183]
mm a[196:199],v[188:191],v[52:55],a[196:199]
mm a[196:199],v[184:187],v[48:51],a[196:199]
rt v[208:209],v242 offset:0
rt v[210:211],v242 offset:1024
mm a[172:175],v[184:187],v[40:43],a[172:175]
s_add_u32 m0, s83, 33792
mm a[172:175],v[188:191],v[44:47],a[172:175]
ld v232,s[44:47],s80 offen lds
mm a[220:223],v[188:191],v[36:39],a[220:223]
mm a[220:223],v[184:187],v[32:35],a[220:223]
rd v[184:187],v236 offset:18432
rd v[188:191],v237 offset:18432
mm a[72:75],v[0:3],v[32:35],a[72:75]
s_add_u32 m0, s83, 34816
mm a[72:75],v[4:7],v[36:39],a[72:75]
ld v233,s[44:47],s80 offen lds
mm a[8:11],v[4:7],v[44:47],a[8:11]
mm a[8:11],v[0:3],v[40:43],a[8:11]
rt v[212:213],v242 offset:16896
rt v[214:215],v242 offset:17920
mm a[80:83],v[0:3],v[48:51],a[80:83]
s_add_u32 m0, s83, 35968
mm a[80:83],v[4:7],v[52:55],a[80:83]
ld v234,s[44:47],s80 offen lds
mm a[16:19],v[4:7],v[60:63],a[16:19]
mm a[16:19],v[0:3],v[56:59],a[16:19]
rd v[0:3],v236 offset:8192
rd v[4:7],v237 offset:8192
mm a[20:23],v[8:11],v[56:59],a[20:23]
s_add_u32 m0, s83, 36992
)ASM" R"ASM(mm a[20:23],v[12:15],v[60:63],a[20:23]
ld v235,s[44:47],s80 offen lds
mm a[84:87],v[12:15],v[52:55],a[84:87]
s_mov_b32 s80, 786432
mm a[84:87],v[8:11],v[48:51],a[84:87]
rt v[216:217],v243 offset:0
rt v[218:219],v243 offset:1024
mm a[12:15],v[8:11],v[40:43],a[12:15]
s_add_u32 m0, s83, 50688
mm a[12:15],v[12:15],v[44:47],a[12:15]
ld v232,s[44:47],s80 offen lds
mm a[76:79],v[12:15],v[36:39],a[76:79]
mm a[76:79],v[8:11],v[32:35],a[76:79]
rd v[8:11],v236 offset:10240
rd v[12:15],v237 offset:10240
mm a[248:251],v[16:19],v[32:35],a[248:251]
s_add_u32 m0, s83, 51712
mm a[248:251],v[20:23],v[36:39],a[248:251]
ld v233,s[44:47],s80 offen lds
mm a[40:43],v[20:23],v[44:47],a[40:43]
mm a[40:43],v[16:19],v[40:43],a[40:43]
rt v[220:221],v243 offset:16896
rt v[222:223],v243 offset:17920
mm a[48:51],v[16:19],v[48:51],a[48:51]
s_add_u32 m0, s83, 52864
mm a[48:51],v[20:23],v[52:55],a[48:51]
ld v234,s[44:47],s80 offen lds
mm a[56:59],v[20:23],v[60:63],a[56:59]
mm a[56:59],v[16:19],v[56:59],a[56:59]
rd v[16:19],v236 offset:0
rd v[20:23],v237 offset:0
mm a[60:63],v[24:27],v[56:59],a[60:63]
s_add_u32 m0, s83, 53888
mm a[60:63],v[28:31],v[60:63],a[60:63]
ld v235,s[44:47],s80 offen lds
mm a[52:55],v[28:31],v[52:55],a[52:55]
s_mov_b32 s80, 1048576
mm a[52:55],v[24:27],v[48:51],a[52:55]
mm a[44:47],v[24:27],v[40:43],a[44:47]
mm a[44:47],v[28:31],v[44:47],a[44:47]
mm a[252:255],v[28:31],v[36:39],a[252:255]
wt lgkmcnt(2)
mm a[252:255],v[24:27],v[32:35],a[252:255]
rd v[24:27],v236 offset:2048
rd v[28:31],v237 offset:2048
mm a[96:99],v[160:163],v[192:195],a[96:99]
mm a[96:99],v[164:167],v[196:199],a[96:99]
mm a[104:107],v[164:167],v[204:207],a[104:107]
mm a[104:107],v[160:163],v[200:203],a[104:107]
rt v[32:33],v240 offset:128
rt v[34:35],v240 offset:1152
mm a[112:115],v[160:163],v[208:211],a[112:115]
mm a[112:115],v[164:167],v[212:215],a[112:115]
mm a[128:131],v[164:167],v[220:223],a[128:131]
wt lgkmcnt(2)
s_barrier
mm a[128:131],v[160:163],v[216:219],a[128:131]
mm a[132:135],v[168:171],v[216:219],a[132:135]
s_xor_b32 m0, s82, 0
mm a[132:135],v[172:175],v[220:223],a[132:135]
ld v224,s[40:43],s79 offen lds
mm a[116:119],v[172:175],v[212:215],a[116:119]
mm a[116:119],v[168:171],v[208:211],a[116:119]
rt v[36:37],v240 offset:17024
rt v[38:39],v240 offset:18048
mm a[108:111],v[168:171],v[200:203],a[108:111]
s_xor_b32 m0, s82, 1024
mm a[108:111],v[172:175],v[204:207],a[108:111]
ld v225,s[40:43],s79 offen lds
mm a[100:103],v[172:175],v[196:199],a[100:103]
mm a[100:103],v[168:171],v[192:195],a[100:103]
mm a[232:235],v[176:179],v[192:195],a[232:235]
s_xor_b32 m0, s82, 2048
mm a[232:235],v[180:183],v[196:199],a[232:235]
ld v226,s[40:43],s79 offen lds
mm a[152:155],v[180:183],v[204:207],a[152:155]
mm a[152:155],v[176:179],v[200:203],a[152:155]
rt v[40:41],v241 offset:128
rt v[42:43],v241 offset:1152
mm a[208:211],v[176:179],v[208:211],a[208:211]
s_xor_b32 m0, s82, 3072
mm a[208:211],v[180:183],v[212:215],a[208:211]
ld v227,s[40:43],s79 offen lds
mm a[160:163],v[180:183],v[220:223],a[160:163]
mm a[160:163],v[176:179],v[216:219],a[160:163]
mm a[164:167],v[184:187],v[216:219],a[164:167]
s_xor_b32 m0, s82, 4096
mm a[164:167],v[188:191],v[220:223],a[164:167]
ld v228,s[40:43],s79 offen lds
mm a[212:215],v[188:191],v[212:215],a[212:215]
mm a[212:215],v[184:187],v[208:211],a[212:215]
rt v[44:45],v241 offset:17024
rt v[46:47],v241 offset:18048
mm a[156:159],v[184:187],v[200:203],a[156:159]
s_xor_b32 m0, s82, 5120
mm a[156:159],v[188:191],v[204:207],a[156:159]
ld v229,s[40:43],s79 offen lds
mm a[236:239],v[188:191],v[196:199],a[236:239]
mm a[236:239],v[184:187],v[192:195],a[236:239]
mm a[200:203],v[0:3],v[192:195],a[200:203]
)ASM" R"ASM(s_xor_b32 m0, s82, 6144
mm a[200:203],v[4:7],v[196:199],a[200:203]
ld v230,s[40:43],s79 offen lds
mm a[184:187],v[4:7],v[204:207],a[184:187]
mm a[184:187],v[0:3],v[200:203],a[184:187]
rt v[48:49],v242 offset:128
rt v[50:51],v242 offset:1152
mm a[64:67],v[0:3],v[208:211],a[64:67]
s_xor_b32 m0, s82, 7168
mm a[64:67],v[4:7],v[212:215],a[64:67]
ld v231,s[40:43],s79 offen lds
mm a[0:3],v[4:7],v[220:223],a[0:3]
s_mov_b32 s79, 384
mm a[0:3],v[0:3],v[216:219],a[0:3]
mm a[4:7],v[8:11],v[216:219],a[4:7]
mm a[4:7],v[12:15],v[220:223],a[4:7]
buffer_load_dword v254, v135, s[68:71], s96 offen
mm a[68:71],v[12:15],v[212:215],a[68:71]
s_add_u32 s96, s96, 1048576
mm a[68:71],v[8:11],v[208:211],a[68:71]
rt v[52:53],v242 offset:17024
rt v[54:55],v242 offset:18048
mm a[188:191],v[8:11],v[200:203],a[188:191]
mm a[188:191],v[12:15],v[204:207],a[188:191]
buffer_load_dword v254, v135, s[68:71], s96 offen
mm a[204:207],v[12:15],v[196:199],a[204:207]
s_add_u32 s96, s96, 1048576
mm a[204:207],v[8:11],v[192:195],a[204:207]
rt v[56:57],v243 offset:128
rt v[58:59],v243 offset:1152
mm a[88:91],v[16:19],v[192:195],a[88:91]
mm a[88:91],v[20:23],v[196:199],a[88:91]
mm a[24:27],v[20:23],v[204:207],a[24:27]
mm a[24:27],v[16:19],v[200:203],a[24:27]
rt v[60:61],v243 offset:17024
rt v[62:63],v243 offset:18048
mm a[240:243],v[16:19],v[208:211],a[240:243]
mm a[240:243],v[20:23],v[212:215],a[240:243]
mm a[32:35],v[20:23],v[220:223],a[32:35]
mm a[32:35],v[16:19],v[216:219],a[32:35]
mm a[36:39],v[24:27],v[216:219],a[36:39]
mm a[36:39],v[28:31],v[220:223],a[36:39]
mm a[244:247],v[28:31],v[212:215],a[244:247]
mm a[244:247],v[24:27],v[208:211],a[244:247]
mm a[28:31],v[24:27],v[200:203],a[28:31]
mm a[28:31],v[28:31],v[204:207],a[28:31]
mm a[92:95],v[28:31],v[196:199],a[92:95]
wt vmcnt(10) lgkmcnt(0)
s_barrier
mm a[92:95],v[24:27],v[192:195],a[92:95]
mm a[120:123],v[160:163],v[32:35],a[120:123]
mm a[120:123],v[164:167],v[36:39],a[120:123]
mm a[136:139],v[164:167],v[44:47],a[136:139]
rt v[192:193],v240 offset:33792
rt v[194:195],v240 offset:34816
mm a[136:139],v[160:163],v[40:43],a[136:139]
rt v[196:197],v240 offset:50688
rt v[198:199],v240 offset:51712
mm a[224:227],v[160:163],v[48:51],a[224:227]
mm a[224:227],v[164:167],v[52:55],a[224:227]
mm a[144:147],v[164:167],v[60:63],a[144:147]
mm a[144:147],v[160:163],v[56:59],a[144:147]
rd v[160:163],v236 offset:57344
rd v[164:167],v237 offset:57344
mm a[148:151],v[168:171],v[56:59],a[148:151]
mm a[148:151],v[172:175],v[60:63],a[148:151]
mm a[228:231],v[172:175],v[52:55],a[228:231]
mm a[228:231],v[168:171],v[48:51],a[228:231]
rt v[200:201],v241 offset:33792
rt v[202:203],v241 offset:34816
mm a[140:143],v[168:171],v[40:43],a[140:143]
mm a[140:143],v[172:175],v[44:47],a[140:143]
mm a[124:127],v[172:175],v[36:39],a[124:127]
mm a[124:127],v[168:171],v[32:35],a[124:127]
rd v[168:171],v236 offset:59392
rd v[172:175],v237 offset:59392
mm a[216:219],v[176:179],v[32:35],a[216:219]
mm a[216:219],v[180:183],v[36:39],a[216:219]
mm a[168:171],v[180:183],v[44:47],a[168:171]
mm a[168:171],v[176:179],v[40:43],a[168:171]
rt v[204:205],v241 offset:50688
rt v[206:207],v241 offset:51712
mm a[192:195],v[176:179],v[48:51],a[192:195]
mm a[192:195],v[180:183],v[52:55],a[192:195]
mm a[176:179],v[180:183],v[60:63],a[176:179]
mm a[176:179],v[176:179],v[56:59],a[176:179]
rd v[176:179],v236 offset:49152
rd v[180:183],v237 offset:49152
mm a[180:183],v[184:187],v[56:59],a[180:183]
mm a[180:183],v[188:191],v[60:63],a[180:183]
mm a[196:199],v[188:191],v[52:55],a[196:199]
mm a[196:199],v[184:187],v[48:51],a[196:199]
rt v[208:209],v242 offset:33792
rt v[210:211],v242 offset:34816
mm a[172:175],v[184:187],v[40:43],a[172:175]
s_add_u32 m0, s83, 0
mm a[172:175],v[188:191],v[44:47],a[172:175]
ld v232,s[44:47],s80 offen lds
mm a[220:223],v[188:191],v[36:39],a[220:223]
mm a[220:223],v[184:187],v[32:35],a[220:223]
)ASM" R"ASM(rd v[184:187],v236 offset:51200
rd v[188:191],v237 offset:51200
mm a[72:75],v[0:3],v[32:35],a[72:75]
s_add_u32 m0, s83, 1024
mm a[72:75],v[4:7],v[36:39],a[72:75]
ld v233,s[44:47],s80 offen lds
mm a[8:11],v[4:7],v[44:47],a[8:11]
mm a[8:11],v[0:3],v[40:43],a[8:11]
rt v[212:213],v242 offset:50688
rt v[214:215],v242 offset:51712
mm a[80:83],v[0:3],v[48:51],a[80:83]
s_add_u32 m0, s83, 2176
mm a[80:83],v[4:7],v[52:55],a[80:83]
ld v234,s[44:47],s80 offen lds
mm a[16:19],v[4:7],v[60:63],a[16:19]
mm a[16:19],v[0:3],v[56:59],a[16:19]
rd v[0:3],v236 offset:40960
rd v[4:7],v237 offset:40960
mm a[20:23],v[8:11],v[56:59],a[20:23]
s_add_u32 m0, s83, 3200
mm a[20:23],v[12:15],v[60:63],a[20:23]
ld v235,s[44:47],s80 offen lds
mm a[84:87],v[12:15],v[52:55],a[84:87]
s_mov_b32 s80, 1310720
mm a[84:87],v[8:11],v[48:51],a[84:87]
rt v[216:217],v243 offset:33792
rt v[218:219],v243 offset:34816
mm a[12:15],v[8:11],v[40:43],a[12:15]
s_add_u32 m0, s83, 16896
mm a[12:15],v[12:15],v[44:47],a[12:15]
ld v232,s[44:47],s80 offen lds
mm a[76:79],v[12:15],v[36:39],a[76:79]
mm a[76:79],v[8:11],v[32:35],a[76:79]
rd v[8:11],v236 offset:43008
rd v[12:15],v237 offset:43008
mm a[248:251],v[16:19],v[32:35],a[248:251]
s_add_u32 m0, s83, 17920
mm a[248:251],v[20:23],v[36:39],a[248:251]
ld v233,s[44:47],s80 offen lds
mm a[40:43],v[20:23],v[44:47],a[40:43]
mm a[40:43],v[16:19],v[40:43],a[40:43]
rt v[220:221],v243 offset:50688
rt v[222:223],v243 offset:51712
mm a[48:51],v[16:19],v[48:51],a[48:51]
s_add_u32 m0, s83, 19072
mm a[48:51],v[20:23],v[52:55],a[48:51]
ld v234,s[44:47],s80 offen lds
mm a[56:59],v[20:23],v[60:63],a[56:59]
mm a[56:59],v[16:19],v[56:59],a[56:59]
rd v[16:19],v236 offset:32768
rd v[20:23],v237 offset:32768
mm a[60:63],v[24:27],v[56:59],a[60:63]
s_add_u32 m0, s83, 20096
mm a[60:63],v[28:31],v[60:63],a[60:63]
ld v235,s[44:47],s80 offen lds
mm a[52:55],v[28:31],v[52:55],a[52:55]
s_mov_b32 s80, 1572864
mm a[52:55],v[24:27],v[48:51],a[52:55]
mm a[44:47],v[24:27],v[40:43],a[44:47]
mm a[44:47],v[28:31],v[44:47],a[44:47]
mm a[252:255],v[28:31],v[36:39],a[252:255]
wt lgkmcnt(2)
mm a[252:255],v[24:27],v[32:35],a[252:255]
rd v[24:27],v236 offset:34816
rd v[28:31],v237 offset:34816
mm a[96:99],v[160:163],v[192:195],a[96:99]
mm a[96:99],v[164:167],v[196:199],a[96:99]
mm a[104:107],v[164:167],v[204:207],a[104:107]
mm a[104:107],v[160:163],v[200:203],a[104:107]
rt v[32:33],v240 offset:33920
rt v[34:35],v240 offset:34944
.La5_LW1_7:
s_cbranch_execz .La5_LW1_11
mm a[112:115],v[160:163],v[208:211],a[112:115]
mm a[112:115],v[164:167],v[212:215],a[112:115]
mm a[128:131],v[164:167],v[220:223],a[128:131]
wt lgkmcnt(2)
s_barrier
mm a[128:131],v[160:163],v[216:219],a[128:131]
mm a[132:135],v[168:171],v[216:219],a[132:135]
s_xor_b32 m0, s82, 32768
mm a[132:135],v[172:175],v[220:223],a[132:135]
ld v224,s[40:43],s79 offen lds
mm a[116:119],v[172:175],v[212:215],a[116:119]
mm a[116:119],v[168:171],v[208:211],a[116:119]
rt v[36:37],v240 offset:50816
rt v[38:39],v240 offset:51840
mm a[108:111],v[168:171],v[200:203],a[108:111]
s_xor_b32 m0, s82, 33792
mm a[108:111],v[172:175],v[204:207],a[108:111]
ld v225,s[40:43],s79 offen lds
mm a[100:103],v[172:175],v[196:199],a[100:103]
mm a[100:103],v[168:171],v[192:195],a[100:103]
mm a[232:235],v[176:179],v[192:195],a[232:235]
s_xor_b32 m0, s82, 34816
mm a[232:235],v[180:183],v[196:199],a[232:235]
ld v226,s[40:43],s79 offen lds
mm a[152:155],v[180:183],v[204:207],a[152:155]
mm a[152:155],v[176:179],v[200:203],a[152:155]
rt v[40:41],v241 offset:33920
)ASM" R"ASM(rt v[42:43],v241 offset:34944
mm a[208:211],v[176:179],v[208:211],a[208:211]
s_xor_b32 m0, s82, 35840
mm a[208:211],v[180:183],v[212:215],a[208:211]
ld v227,s[40:43],s79 offen lds
mm a[160:163],v[180:183],v[220:223],a[160:163]
mm a[160:163],v[176:179],v[216:219],a[160:163]
mm a[164:167],v[184:187],v[216:219],a[164:167]
s_xor_b32 m0, s82, 36864
mm a[164:167],v[188:191],v[220:223],a[164:167]
ld v228,s[40:43],s79 offen lds
mm a[212:215],v[188:191],v[212:215],a[212:215]
mm a[212:215],v[184:187],v[208:211],a[212:215]
rt v[44:45],v241 offset:50816
rt v[46:47],v241 offset:51840
mm a[156:159],v[184:187],v[200:203],a[156:159]
s_xor_b32 m0, s82, 37888
mm a[156:159],v[188:191],v[204:207],a[156:159]
ld v229,s[40:43],s79 offen lds
mm a[236:239],v[188:191],v[196:199],a[236:239]
mm a[236:239],v[184:187],v[192:195],a[236:239]
mm a[200:203],v[0:3],v[192:195],a[200:203]
s_xor_b32 m0, s82, 38912
mm a[200:203],v[4:7],v[196:199],a[200:203]
ld v230,s[40:43],s79 offen lds
mm a[184:187],v[4:7],v[204:207],a[184:187]
mm a[184:187],v[0:3],v[200:203],a[184:187]
rt v[48:49],v242 offset:33920
rt v[50:51],v242 offset:34944
mm a[64:67],v[0:3],v[208:211],a[64:67]
s_xor_b32 m0, s82, 39936
mm a[64:67],v[4:7],v[212:215],a[64:67]
ld v231,s[40:43],s79 offen lds
mm a[0:3],v[4:7],v[220:223],a[0:3]
s_mov_b32 s79, 512
mm a[0:3],v[0:3],v[216:219],a[0:3]
mm a[4:7],v[8:11],v[216:219],a[4:7]
mm a[4:7],v[12:15],v[220:223],a[4:7]
buffer_load_dword v254, v135, s[68:71], s96 offen
mm a[68:71],v[12:15],v[212:215],a[68:71]
s_add_u32 s96, s96, 1048576
mm a[68:71],v[8:11],v[208:211],a[68:71]
rt v[52:53],v242 offset:50816
rt v[54:55],v242 offset:51840
mm a[188:191],v[8:11],v[200:203],a[188:191]
mm a[188:191],v[12:15],v[204:207],a[188:191]
buffer_load_dword v254, v135, s[68:71], s96 offen
mm a[204:207],v[12:15],v[196:199],a[204:207]
s_add_u32 s96, s96, 1048576
mm a[204:207],v[8:11],v[192:195],a[204:207]
rt v[56:57],v243 offset:33920
rt v[58:59],v243 offset:34944
mm a[88:91],v[16:19],v[192:195],a[88:91]
mm a[88:91],v[20:23],v[196:199],a[88:91]
mm a[24:27],v[20:23],v[204:207],a[24:27]
mm a[24:27],v[16:19],v[200:203],a[24:27]
rt v[60:61],v243 offset:50816
rt v[62:63],v243 offset:51840
mm a[240:243],v[16:19],v[208:211],a[240:243]
mm a[240:243],v[20:23],v[212:215],a[240:243]
mm a[32:35],v[20:23],v[220:223],a[32:35]
mm a[32:35],v[16:19],v[216:219],a[32:35]
mm a[36:39],v[24:27],v[216:219],a[36:39]
mm a[36:39],v[28:31],v[220:223],a[36:39]
mm a[244:247],v[28:31],v[212:215],a[244:247]
mm a[244:247],v[24:27],v[208:211],a[244:247]
mm a[28:31],v[24:27],v[200:203],a[28:31]
mm a[28:31],v[28:31],v[204:207],a[28:31]
mm a[92:95],v[28:31],v[196:199],a[92:95]
wt vmcnt(10) lgkmcnt(0)
s_barrier
mm a[92:95],v[24:27],v[192:195],a[92:95]
mm a[120:123],v[160:163],v[32:35],a[120:123]
mm a[120:123],v[164:167],v[36:39],a[120:123]
mm a[136:139],v[164:167],v[44:47],a[136:139]
rt v[192:193],v240 offset:0
rt v[194:195],v240 offset:1024
mm a[136:139],v[160:163],v[40:43],a[136:139]
rt v[196:197],v240 offset:16896
rt v[198:199],v240 offset:17920
mm a[224:227],v[160:163],v[48:51],a[224:227]
mm a[224:227],v[164:167],v[52:55],a[224:227]
mm a[144:147],v[164:167],v[60:63],a[144:147]
mm a[144:147],v[160:163],v[56:59],a[144:147]
rd v[160:163],v236 offset:24576
rd v[164:167],v237 offset:24576
mm a[148:151],v[168:171],v[56:59],a[148:151]
mm a[148:151],v[172:175],v[60:63],a[148:151]
mm a[228:231],v[172:175],v[52:55],a[228:231]
mm a[228:231],v[168:171],v[48:51],a[228:231]
rt v[200:201],v241 offset:0
rt v[202:203],v241 offset:1024
mm a[140:143],v[168:171],v[40:43],a[140:143]
mm a[140:143],v[172:175],v[44:47],a[140:143]
mm a[124:127],v[172:175],v[36:39],a[124:127]
mm a[124:127],v[168:171],v[32:35],a[124:127]
rd v[168:171],v236 offset:26624
rd v[172:175],v237 offset:26624
mm a[216:219],v[176:179],v[32:35],a[216:219]
mm a[216:219],v[180:183],v[36:39],a[216:219]
)ASM" R"ASM(mm a[168:171],v[180:183],v[44:47],a[168:171]
mm a[168:171],v[176:179],v[40:43],a[168:171]
rt v[204:205],v241 offset:16896
rt v[206:207],v241 offset:17920
mm a[192:195],v[176:179],v[48:51],a[192:195]
mm a[192:195],v[180:183],v[52:55],a[192:195]
mm a[176:179],v[180:183],v[60:63],a[176:179]
mm a[176:179],v[176:179],v[56:59],a[176:179]
rd v[176:179],v236 offset:16384
rd v[180:183],v237 offset:16384
mm a[180:183],v[184:187],v[56:59],a[180:183]
mm a[180:183],v[188:191],v[60:63],a[180:183]
mm a[196:199],v[188:191],v[52:55],a[196:199]
mm a[196:199],v[184:187],v[48:51],a[196:199]
rt v[208:209],v242 offset:0
rt v[210:211],v242 offset:1024
mm a[172:175],v[184:187],v[40:43],a[172:175]
s_add_u32 m0, s83, 33792
mm a[172:175],v[188:191],v[44:47],a[172:175]
ld v232,s[44:47],s80 offen lds
mm a[220:223],v[188:191],v[36:39],a[220:223]
mm a[220:223],v[184:187],v[32:35],a[220:223]
rd v[184:187],v236 offset:18432
rd v[188:191],v237 offset:18432
mm a[72:75],v[0:3],v[32:35],a[72:75]
s_add_u32 m0, s83, 34816
mm a[72:75],v[4:7],v[36:39],a[72:75]
ld v233,s[44:47],s80 offen lds
mm a[8:11],v[4:7],v[44:47],a[8:11]
mm a[8:11],v[0:3],v[40:43],a[8:11]
rt v[212:213],v242 offset:16896
rt v[214:215],v242 offset:17920
mm a[80:83],v[0:3],v[48:51],a[80:83]
s_add_u32 m0, s83, 35968
mm a[80:83],v[4:7],v[52:55],a[80:83]
ld v234,s[44:47],s80 offen lds
mm a[16:19],v[4:7],v[60:63],a[16:19]
mm a[16:19],v[0:3],v[56:59],a[16:19]
rd v[0:3],v236 offset:8192
rd v[4:7],v237 offset:8192
mm a[20:23],v[8:11],v[56:59],a[20:23]
s_add_u32 m0, s83, 36992
mm a[20:23],v[12:15],v[60:63],a[20:23]
ld v235,s[44:47],s80 offen lds
mm a[84:87],v[12:15],v[52:55],a[84:87]
s_mov_b32 s80, 1835008
mm a[84:87],v[8:11],v[48:51],a[84:87]
rt v[216:217],v243 offset:0
rt v[218:219],v243 offset:1024
mm a[12:15],v[8:11],v[40:43],a[12:15]
s_add_u32 m0, s83, 50688
mm a[12:15],v[12:15],v[44:47],a[12:15]
ld v232,s[44:47],s80 offen lds
mm a[76:79],v[12:15],v[36:39],a[76:79]
mm a[76:79],v[8:11],v[32:35],a[76:79]
rd v[8:11],v236 offset:10240
rd v[12:15],v237 offset:10240
mm a[248:251],v[16:19],v[32:35],a[248:251]
s_add_u32 m0, s83, 51712
mm a[248:251],v[20:23],v[36:39],a[248:251]
ld v233,s[44:47],s80 offen lds
mm a[40:43],v[20:23],v[44:47],a[40:43]
mm a[40:43],v[16:19],v[40:43],a[40:43]
rt v[220:221],v243 offset:16896
rt v[222:223],v243 offset:17920
mm a[48:51],v[16:19],v[48:51],a[48:51]
s_add_u32 m0, s83, 52864
mm a[48:51],v[20:23],v[52:55],a[48:51]
ld v234,s[44:47],s80 offen lds
mm a[56:59],v[20:23],v[60:63],a[56:59]
mm a[56:59],v[16:19],v[56:59],a[56:59]
rd v[16:19],v236 offset:0
rd v[20:23],v237 offset:0
mm a[60:63],v[24:27],v[56:59],a[60:63]
s_add_u32 m0, s83, 53888
mm a[60:63],v[28:31],v[60:63],a[60:63]
ld v235,s[44:47],s80 offen lds
mm a[52:55],v[28:31],v[52:55],a[52:55]
s_mov_b64 s[44:45], s[68:69]
s_mov_b32 s46, s70
mm a[52:55],v[24:27],v[48:51],a[52:55]
mm a[44:47],v[24:27],v[40:43],a[44:47]
mm a[44:47],v[28:31],v[44:47],a[44:47]
mm a[252:255],v[28:31],v[36:39],a[252:255]
wt lgkmcnt(2)
s_lshl_b32 s80, s77, 18
mm a[252:255],v[24:27],v[32:35],a[252:255]
rd v[24:27],v236 offset:2048
rd v[28:31],v237 offset:2048
mm a[96:99],v[160:163],v[192:195],a[96:99]
mm a[96:99],v[164:167],v[196:199],a[96:99]
mm a[104:107],v[164:167],v[204:207],a[104:107]
mm a[104:107],v[160:163],v[200:203],a[104:107]
rt v[32:33],v240 offset:128
rt v[34:35],v240 offset:1152
mm a[112:115],v[160:163],v[208:211],a[112:115]
mm a[112:115],v[164:167],v[212:215],a[112:115]
mm a[128:131],v[164:167],v[220:223],a[128:131]
wt lgkmcnt(2)
s_barrier
)ASM" R"ASM(mm a[128:131],v[160:163],v[216:219],a[128:131]
mm a[132:135],v[168:171],v[216:219],a[132:135]
mm a[132:135],v[172:175],v[220:223],a[132:135]
mm a[116:119],v[172:175],v[212:215],a[116:119]
mm a[116:119],v[168:171],v[208:211],a[116:119]
rt v[36:37],v240 offset:17024
rt v[38:39],v240 offset:18048
mm a[108:111],v[168:171],v[200:203],a[108:111]
mm a[108:111],v[172:175],v[204:207],a[108:111]
mm a[100:103],v[172:175],v[196:199],a[100:103]
mm a[100:103],v[168:171],v[192:195],a[100:103]
mm a[232:235],v[176:179],v[192:195],a[232:235]
mm a[232:235],v[180:183],v[196:199],a[232:235]
mm a[152:155],v[180:183],v[204:207],a[152:155]
mm a[152:155],v[176:179],v[200:203],a[152:155]
rt v[40:41],v241 offset:128
rt v[42:43],v241 offset:1152
mm a[208:211],v[176:179],v[208:211],a[208:211]
mm a[208:211],v[180:183],v[212:215],a[208:211]
mm a[160:163],v[180:183],v[220:223],a[160:163]
mm a[160:163],v[176:179],v[216:219],a[160:163]
mm a[164:167],v[184:187],v[216:219],a[164:167]
mm a[164:167],v[188:191],v[220:223],a[164:167]
mm a[212:215],v[188:191],v[212:215],a[212:215]
mm a[212:215],v[184:187],v[208:211],a[212:215]
rt v[44:45],v241 offset:17024
rt v[46:47],v241 offset:18048
mm a[156:159],v[184:187],v[200:203],a[156:159]
mm a[156:159],v[188:191],v[204:207],a[156:159]
mm a[236:239],v[188:191],v[196:199],a[236:239]
mm a[236:239],v[184:187],v[192:195],a[236:239]
mm a[200:203],v[0:3],v[192:195],a[200:203]
mm a[200:203],v[4:7],v[196:199],a[200:203]
mm a[184:187],v[4:7],v[204:207],a[184:187]
mm a[184:187],v[0:3],v[200:203],a[184:187]
rt v[48:49],v242 offset:128
rt v[50:51],v242 offset:1152
mm a[64:67],v[0:3],v[208:211],a[64:67]
mm a[64:67],v[4:7],v[212:215],a[64:67]
mm a[0:3],v[4:7],v[220:223],a[0:3]
mm a[0:3],v[0:3],v[216:219],a[0:3]
mm a[4:7],v[8:11],v[216:219],a[4:7]
mm a[4:7],v[12:15],v[220:223],a[4:7]
buffer_load_dword v254, v135, s[68:71], s96 offen
mm a[68:71],v[12:15],v[212:215],a[68:71]
s_add_u32 s96, s96, 1048576
mm a[68:71],v[8:11],v[208:211],a[68:71]
rt v[52:53],v242 offset:17024
rt v[54:55],v242 offset:18048
mm a[188:191],v[8:11],v[200:203],a[188:191]
mm a[188:191],v[12:15],v[204:207],a[188:191]
buffer_load_dword v254, v135, s[68:71], s96 offen
mm a[204:207],v[12:15],v[196:199],a[204:207]
s_add_u32 s96, s96, 1048576
mm a[204:207],v[8:11],v[192:195],a[204:207]
rt v[56:57],v243 offset:128
rt v[58:59],v243 offset:1152
mm a[88:91],v[16:19],v[192:195],a[88:91]
mm a[88:91],v[20:23],v[196:199],a[88:91]
mm a[24:27],v[20:23],v[204:207],a[24:27]
mm a[24:27],v[16:19],v[200:203],a[24:27]
rt v[60:61],v243 offset:17024
rt v[62:63],v243 offset:18048
mm a[240:243],v[16:19],v[208:211],a[240:243]
mm a[240:243],v[20:23],v[212:215],a[240:243]
mm a[32:35],v[20:23],v[220:223],a[32:35]
mm a[32:35],v[16:19],v[216:219],a[32:35]
mm a[36:39],v[24:27],v[216:219],a[36:39]
mm a[36:39],v[28:31],v[220:223],a[36:39]
ld v[72:75],v255,s[64:67],s89 offen offset:1024 sc1
mm a[244:247],v[28:31],v[212:215],a[244:247]
mm a[244:247],v[24:27],v[208:211],a[244:247]
mm a[28:31],v[24:27],v[200:203],a[28:31]
mm a[28:31],v[28:31],v[204:207],a[28:31]
mm a[92:95],v[28:31],v[196:199],a[92:95]
wt vmcnt(3) lgkmcnt(0)
s_barrier
mm a[92:95],v[24:27],v[192:195],a[92:95]
mm a[120:123],v[160:163],v[32:35],a[120:123]
mm a[120:123],v[164:167],v[36:39],a[120:123]
mm a[136:139],v[164:167],v[44:47],a[136:139]
rt v[192:193],v240 offset:33792
rt v[194:195],v240 offset:34816
mm a[136:139],v[160:163],v[40:43],a[136:139]
rt v[196:197],v240 offset:50688
rt v[198:199],v240 offset:51712
mm a[224:227],v[160:163],v[48:51],a[224:227]
mm a[224:227],v[164:167],v[52:55],a[224:227]
mm a[144:147],v[164:167],v[60:63],a[144:147]
mm a[144:147],v[160:163],v[56:59],a[144:147]
rd v[160:163],v236 offset:57344
rd v[164:167],v237 offset:57344
mm a[148:151],v[168:171],v[56:59],a[148:151]
mm a[148:151],v[172:175],v[60:63],a[148:151]
mm a[228:231],v[172:175],v[52:55],a[228:231]
mm a[228:231],v[168:171],v[48:51],a[228:231]
rt v[200:201],v241 offset:33792
rt v[202:203],v241 offset:34816
mm a[140:143],v[168:171],v[40:43],a[140:143]
mm a[140:143],v[172:175],v[44:47],a[140:143]
)ASM" R"ASM(mm a[124:127],v[172:175],v[36:39],a[124:127]
mm a[124:127],v[168:171],v[32:35],a[124:127]
rd v[168:171],v236 offset:59392
rd v[172:175],v237 offset:59392
mm a[216:219],v[176:179],v[32:35],a[216:219]
mm a[216:219],v[180:183],v[36:39],a[216:219]
mm a[168:171],v[180:183],v[44:47],a[168:171]
mm a[168:171],v[176:179],v[40:43],a[168:171]
rt v[204:205],v241 offset:50688
rt v[206:207],v241 offset:51712
mm a[192:195],v[176:179],v[48:51],a[192:195]
mm a[192:195],v[180:183],v[52:55],a[192:195]
mm a[176:179],v[180:183],v[60:63],a[176:179]
mm a[176:179],v[176:179],v[56:59],a[176:179]
rd v[176:179],v236 offset:49152
rd v[180:183],v237 offset:49152
mm a[180:183],v[184:187],v[56:59],a[180:183]
mm a[180:183],v[188:191],v[60:63],a[180:183]
mm a[196:199],v[188:191],v[52:55],a[196:199]
mm a[196:199],v[184:187],v[48:51],a[196:199]
rt v[208:209],v242 offset:33792
rt v[210:211],v242 offset:34816
mm a[172:175],v[184:187],v[40:43],a[172:175]
s_add_u32 m0, s83, 0
mm a[172:175],v[188:191],v[44:47],a[172:175]
ld v232,s[44:47],s80 offen lds
mm a[220:223],v[188:191],v[36:39],a[220:223]
mm a[220:223],v[184:187],v[32:35],a[220:223]
rd v[184:187],v236 offset:51200
rd v[188:191],v237 offset:51200
mm a[72:75],v[0:3],v[32:35],a[72:75]
s_add_u32 m0, s83, 1024
mm a[72:75],v[4:7],v[36:39],a[72:75]
ld v233,s[44:47],s80 offen lds
mm a[8:11],v[4:7],v[44:47],a[8:11]
mm a[8:11],v[0:3],v[40:43],a[8:11]
rt v[212:213],v242 offset:50688
rt v[214:215],v242 offset:51712
mm a[80:83],v[0:3],v[48:51],a[80:83]
s_add_u32 m0, s83, 2176
mm a[80:83],v[4:7],v[52:55],a[80:83]
ld v234,s[44:47],s80 offen lds
mm a[16:19],v[4:7],v[60:63],a[16:19]
mm a[16:19],v[0:3],v[56:59],a[16:19]
rd v[0:3],v236 offset:40960
rd v[4:7],v237 offset:40960
mm a[20:23],v[8:11],v[56:59],a[20:23]
s_add_u32 m0, s83, 3200
mm a[20:23],v[12:15],v[60:63],a[20:23]
ld v235,s[44:47],s80 offen lds
mm a[84:87],v[12:15],v[52:55],a[84:87]
s_add_u32 s80, s80, 524288
mm a[84:87],v[8:11],v[48:51],a[84:87]
rt v[216:217],v243 offset:33792
rt v[218:219],v243 offset:34816
mm a[12:15],v[8:11],v[40:43],a[12:15]
s_add_u32 m0, s83, 16896
mm a[12:15],v[12:15],v[44:47],a[12:15]
ld v232,s[44:47],s80 offen lds
mm a[76:79],v[12:15],v[36:39],a[76:79]
mm a[76:79],v[8:11],v[32:35],a[76:79]
rd v[8:11],v236 offset:43008
rd v[12:15],v237 offset:43008
mm a[248:251],v[16:19],v[32:35],a[248:251]
s_add_u32 m0, s83, 17920
mm a[248:251],v[20:23],v[36:39],a[248:251]
ld v233,s[44:47],s80 offen lds
mm a[40:43],v[20:23],v[44:47],a[40:43]
mm a[40:43],v[16:19],v[40:43],a[40:43]
rt v[220:221],v243 offset:50688
rt v[222:223],v243 offset:51712
mm a[48:51],v[16:19],v[48:51],a[48:51]
s_add_u32 m0, s83, 19072
mm a[48:51],v[20:23],v[52:55],a[48:51]
ld v234,s[44:47],s80 offen lds
mm a[56:59],v[20:23],v[60:63],a[56:59]
mm a[56:59],v[16:19],v[56:59],a[56:59]
rd v[16:19],v236 offset:32768
rd v[20:23],v237 offset:32768
mm a[60:63],v[24:27],v[56:59],a[60:63]
s_add_u32 m0, s83, 20096
mm a[60:63],v[28:31],v[60:63],a[60:63]
ld v235,s[44:47],s80 offen lds
mm a[52:55],v[28:31],v[52:55],a[52:55]
s_add_u32 s80, s80, 524288
mm a[52:55],v[24:27],v[48:51],a[52:55]
mm a[44:47],v[24:27],v[40:43],a[44:47]
mm a[44:47],v[28:31],v[44:47],a[44:47]
mm a[252:255],v[28:31],v[36:39],a[252:255]
wt lgkmcnt(2)
mm a[252:255],v[24:27],v[32:35],a[252:255]
rd v[24:27],v236 offset:34816
rd v[28:31],v237 offset:34816
mm a[96:99],v[160:163],v[192:195],a[96:99]
mm a[96:99],v[164:167],v[196:199],a[96:99]
mm a[104:107],v[164:167],v[204:207],a[104:107]
mm a[104:107],v[160:163],v[200:203],a[104:107]
rt v[32:33],v240 offset:33920
rt v[34:35],v240 offset:34944
mm a[112:115],v[160:163],v[208:211],a[112:115]
)ASM" R"ASM(mm a[112:115],v[164:167],v[212:215],a[112:115]
mm a[128:131],v[164:167],v[220:223],a[128:131]
wt lgkmcnt(2)
s_barrier
mm a[128:131],v[160:163],v[216:219],a[128:131]
mm a[132:135],v[168:171],v[216:219],a[132:135]
wt vmcnt(8)
v_cmp_eq_u64 vcc, v[72:73], v[252:253]
mm a[132:135],v[172:175],v[220:223],a[132:135]
v_cmp_eq_u64 s[96:97], v[74:75], v[252:253]
mm a[116:119],v[172:175],v[212:215],a[116:119]
s_and_b64 vcc, vcc, s[96:97]
s_cmp_eq_u64 vcc, -1
s_nop 0
s_nop 0
s_cbranch_scc0 .La5_LV_XSPIN
.La5_LV_XOK:
mm a[116:119],v[168:171],v[208:211],a[116:119]
rt v[36:37],v240 offset:50816
rt v[38:39],v240 offset:51840
mm a[108:111],v[168:171],v[200:203],a[108:111]
s_mov_b32 s89, 49152
mm a[108:111],v[172:175],v[204:207],a[108:111]
mm a[100:103],v[172:175],v[196:199],a[100:103]
mm a[100:103],v[168:171],v[192:195],a[100:103]
mm a[232:235],v[176:179],v[192:195],a[232:235]
mm a[232:235],v[180:183],v[196:199],a[232:235]
ld v[128:131],v244,s[52:55],s91 offen offset:0 sc1
mm a[152:155],v[180:183],v[204:207],a[152:155]
ar v224,a96
ar v225,a97
mm a[152:155],v[176:179],v[200:203],a[152:155]
rt v[40:41],v241 offset:33920
rt v[42:43],v241 offset:34944
mm a[208:211],v[176:179],v[208:211],a[208:211]
ar v226,a98
ar v227,a99
mm a[208:211],v[180:183],v[212:215],a[208:211]
ld v[132:135],v245,s[52:55],s91 offen offset:0 sc1
mm a[160:163],v[180:183],v[220:223],a[160:163]
ar v228,a100
ar v229,a101
mm a[160:163],v[176:179],v[216:219],a[160:163]
ar v230,a102
ar v231,a103
mm a[164:167],v[184:187],v[216:219],a[164:167]
cv v248,v224,v225
cv v249,v226,v227
mm a[164:167],v[188:191],v[220:223],a[164:167]
ld v[136:139],v246,s[52:55],s91 offen offset:0 sc1
mm a[212:215],v[188:191],v[212:215],a[212:215]
cv v250,v228,v229
cv v251,v230,v231
mm a[212:215],v[184:187],v[208:211],a[212:215]
rt v[44:45],v241 offset:50816
rt v[46:47],v241 offset:51840
mm a[156:159],v[184:187],v[200:203],a[156:159]
st v[248:251],v255,s[48:51],s89 offen sc1
mm a[156:159],v[188:191],v[204:207],a[156:159]
ld v[140:143],v247,s[52:55],s91 offen offset:0 sc1
mm a[236:239],v[188:191],v[196:199],a[236:239]
s_mov_b32 s89, 50176
ds_write_b128 v238, v[248:251] offset:0
mm a[236:239],v[184:187],v[192:195],a[236:239]
ar v224,a104
ar v225,a105
mm a[200:203],v[0:3],v[192:195],a[200:203]
ar v226,a106
ar v227,a107
mm a[200:203],v[4:7],v[196:199],a[200:203]
ld v[144:147],v244,s[52:55],s91 offen offset:1024 sc1
mm a[184:187],v[4:7],v[204:207],a[184:187]
ar v228,a108
ar v229,a109
mm a[184:187],v[0:3],v[200:203],a[184:187]
rt v[48:49],v242 offset:33920
rt v[50:51],v242 offset:34944
mm a[64:67],v[0:3],v[208:211],a[64:67]
ar v230,a110
ar v231,a111
mm a[64:67],v[4:7],v[212:215],a[64:67]
ld v[148:151],v245,s[52:55],s91 offen offset:1024 sc1
mm a[0:3],v[4:7],v[220:223],a[0:3]
cv v248,v224,v225
cv v249,v226,v227
mm a[0:3],v[0:3],v[216:219],a[0:3]
cv v250,v228,v229
cv v251,v230,v231
mm a[4:7],v[8:11],v[216:219],a[4:7]
st v[248:251],v255,s[48:51],s89 offen sc1
mm a[4:7],v[12:15],v[220:223],a[4:7]
ld v[152:155],v246,s[52:55],s91 offen offset:1024 sc1
mm a[68:71],v[12:15],v[212:215],a[68:71]
s_mov_b32 s89, 51200
ds_write_b128 v238, v[248:251] offset:1024
mm a[68:71],v[8:11],v[208:211],a[68:71]
rt v[52:53],v242 offset:50816
.La5_LW1_8:
s_cbranch_execz .La5_LW1_12
rt v[54:55],v242 offset:51840
)ASM" R"ASM(mm a[188:191],v[8:11],v[200:203],a[188:191]
ar v224,a112
ar v225,a113
mm a[188:191],v[12:15],v[204:207],a[188:191]
ld v[156:159],v247,s[52:55],s91 offen offset:1024 sc1
mm a[204:207],v[12:15],v[196:199],a[204:207]
ar v226,a114
ar v227,a115
mm a[204:207],v[8:11],v[192:195],a[204:207]
rt v[56:57],v243 offset:33920
rt v[58:59],v243 offset:34944
mm a[88:91],v[16:19],v[192:195],a[88:91]
ar v228,a116
ar v229,a117
mm a[88:91],v[20:23],v[196:199],a[88:91]
ar v230,a118
ar v231,a119
mm a[24:27],v[20:23],v[204:207],a[24:27]
cv v248,v224,v225
cv v249,v226,v227
mm a[24:27],v[16:19],v[200:203],a[24:27]
rt v[60:61],v243 offset:50816
rt v[62:63],v243 offset:51840
mm a[240:243],v[16:19],v[208:211],a[240:243]
v_subrev_u32 v240, 8448, v240
v_subrev_u32 v241, 8448, v241
mm a[240:243],v[20:23],v[212:215],a[240:243]
v_subrev_u32 v242, 8448, v242
v_subrev_u32 v243, 8448, v243
mm a[32:35],v[20:23],v[220:223],a[32:35]
cv v250,v228,v229
cv v251,v230,v231
mm a[32:35],v[16:19],v[216:219],a[32:35]
st v[248:251],v255,s[48:51],s89 offen sc1
mm a[36:39],v[24:27],v[216:219],a[36:39]
s_mov_b32 s89, 52224
ds_write_b128 v238, v[248:251] offset:2048
mm a[36:39],v[28:31],v[220:223],a[36:39]
mm a[244:247],v[28:31],v[212:215],a[244:247]
mm a[244:247],v[24:27],v[208:211],a[244:247]
mm a[28:31],v[24:27],v[200:203],a[28:31]
mm a[28:31],v[28:31],v[204:207],a[28:31]
mm a[92:95],v[28:31],v[196:199],a[92:95]
wt vmcnt(11) lgkmcnt(1)
s_barrier
mm a[92:95],v[24:27],v[192:195],a[92:95]
mm a[120:123],v[160:163],v[32:35],a[120:123]
s_add_u32 m0, s83, 33792
mm a[120:123],v[164:167],v[36:39],a[120:123]
ld v232,s[44:47],s80 offen lds
mm a[136:139],v[164:167],v[44:47],a[136:139]
mm a[136:139],v[160:163],v[40:43],a[136:139]
rt v[192:193],v240 offset:0
rt v[194:195],v240 offset:1024
mm a[224:227],v[160:163],v[48:51],a[224:227]
s_add_u32 m0, s83, 34816
mm a[224:227],v[164:167],v[52:55],a[224:227]
ld v233,s[44:47],s80 offen lds
mm a[144:147],v[164:167],v[60:63],a[144:147]
mm a[144:147],v[160:163],v[56:59],a[144:147]
rt v[196:197],v240 offset:8448
rt v[198:199],v240 offset:9472
mm a[148:151],v[168:171],v[56:59],a[148:151]
s_add_u32 m0, s83, 35968
mm a[148:151],v[172:175],v[60:63],a[148:151]
ld v234,s[44:47],s80 offen lds
mm a[228:231],v[172:175],v[52:55],a[228:231]
mm a[228:231],v[168:171],v[48:51],a[228:231]
rt v[200:201],v241 offset:0
rt v[202:203],v241 offset:1024
mm a[140:143],v[168:171],v[40:43],a[140:143]
s_add_u32 m0, s83, 36992
mm a[140:143],v[172:175],v[44:47],a[140:143]
ld v235,s[44:47],s80 offen lds
mm a[124:127],v[172:175],v[36:39],a[124:127]
s_add_u32 s80, s80, 524288
mm a[124:127],v[168:171],v[32:35],a[124:127]
rt v[204:205],v241 offset:8448
rt v[206:207],v241 offset:9472
mm a[216:219],v[176:179],v[32:35],a[216:219]
s_add_u32 m0, s83, 50688
mm a[216:219],v[180:183],v[36:39],a[216:219]
ld v232,s[44:47],s80 offen lds
mm a[168:171],v[180:183],v[44:47],a[168:171]
mm a[168:171],v[176:179],v[40:43],a[168:171]
rt v[208:209],v242 offset:0
rt v[210:211],v242 offset:1024
mm a[192:195],v[176:179],v[48:51],a[192:195]
s_add_u32 m0, s83, 51712
mm a[192:195],v[180:183],v[52:55],a[192:195]
ld v233,s[44:47],s80 offen lds
mm a[176:179],v[180:183],v[60:63],a[176:179]
mm a[176:179],v[176:179],v[56:59],a[176:179]
rt v[212:213],v242 offset:8448
rt v[214:215],v242 offset:9472
mm a[180:183],v[184:187],v[56:59],a[180:183]
s_add_u32 m0, s83, 52864
mm a[180:183],v[188:191],v[60:63],a[180:183]
ld v234,s[44:47],s80 offen lds
mm a[196:199],v[188:191],v[52:55],a[196:199]
)ASM" R"ASM(mm a[196:199],v[184:187],v[48:51],a[196:199]
rt v[216:217],v243 offset:0
rt v[218:219],v243 offset:1024
mm a[172:175],v[184:187],v[40:43],a[172:175]
s_add_u32 m0, s83, 53888
mm a[172:175],v[188:191],v[44:47],a[172:175]
ld v235,s[44:47],s80 offen lds
mm a[220:223],v[188:191],v[36:39],a[220:223]
s_add_u32 s80, s80, 524288
mm a[220:223],v[184:187],v[32:35],a[220:223]
rt v[220:221],v243 offset:8448
rt v[222:223],v243 offset:9472
mm a[72:75],v[0:3],v[32:35],a[72:75]
mm a[72:75],v[4:7],v[36:39],a[72:75]
ld v[160:163],v244,s[52:55],s91 offen offset:2048 sc1
mm a[8:11],v[4:7],v[44:47],a[8:11]
mm a[8:11],v[0:3],v[40:43],a[8:11]
mm a[80:83],v[0:3],v[48:51],a[80:83]
mm a[80:83],v[4:7],v[52:55],a[80:83]
ld v[164:167],v245,s[52:55],s91 offen offset:2048 sc1
mm a[16:19],v[4:7],v[60:63],a[16:19]
mm a[16:19],v[0:3],v[56:59],a[16:19]
mm a[20:23],v[8:11],v[56:59],a[20:23]
mm a[20:23],v[12:15],v[60:63],a[20:23]
ld v[168:171],v246,s[52:55],s91 offen offset:2048 sc1
mm a[84:87],v[12:15],v[52:55],a[84:87]
mm a[84:87],v[8:11],v[48:51],a[84:87]
mm a[12:15],v[8:11],v[40:43],a[12:15]
mm a[12:15],v[12:15],v[44:47],a[12:15]
ld v[172:175],v247,s[52:55],s91 offen offset:2048 sc1
mm a[76:79],v[12:15],v[36:39],a[76:79]
mm a[76:79],v[8:11],v[32:35],a[76:79]
mm a[248:251],v[16:19],v[32:35],a[248:251]
mm a[248:251],v[20:23],v[36:39],a[248:251]
mm a[40:43],v[20:23],v[44:47],a[40:43]
mm a[40:43],v[16:19],v[40:43],a[40:43]
mm a[48:51],v[16:19],v[48:51],a[48:51]
mm a[48:51],v[20:23],v[52:55],a[48:51]
mm a[56:59],v[20:23],v[60:63],a[56:59]
mm a[56:59],v[16:19],v[56:59],a[56:59]
mm a[60:63],v[24:27],v[56:59],a[60:63]
mm a[60:63],v[28:31],v[60:63],a[60:63]
mm a[52:55],v[28:31],v[52:55],a[52:55]
mm a[52:55],v[24:27],v[48:51],a[52:55]
mm a[44:47],v[24:27],v[40:43],a[44:47]
mm a[44:47],v[28:31],v[44:47],a[44:47]
mm a[252:255],v[28:31],v[36:39],a[252:255]
mm a[252:255],v[24:27],v[32:35],a[252:255]
; ---- Q tile: 256 rows x 128 columns, K = 1024, while K / V leave the accumulators (memory + .La5_LDS ring); behind it the first step on the diagonal
.La5_LQ_START:
.La5_LW1_9:
s_cbranch_execz .La5_LW1_13
wt vmcnt(13) lgkmcnt(4)
mm v[0:3],v[192:195],v[128:131],0
ar v224,a128
ar v225,a129
s_mov_b32 s85, s95
mm v[0:3],v[196:199],v[144:147],v[0:3]
ld v[176:179],v244,s[52:55],s91 offen offset:3072 sc1
mm v[32:35],v[196:199],v[148:151],0
ar v226,a130
ar v227,a131
mm v[32:35],v[192:195],v[132:135],v[32:35]
ar v228,a132
ar v229,a133
mm v[64:67],v[192:195],v[136:139],0
ar v230,a134
ar v231,a135
mm v[64:67],v[196:199],v[152:155],v[64:67]
ld v[180:183],v245,s[52:55],s91 offen offset:3072 sc1
mm v[96:99],v[196:199],v[156:159],0
cv v248,v224,v225
cv v249,v226,v227
mm v[96:99],v[192:195],v[140:143],v[96:99]
rt v[196:197],v240 offset:128
rt v[198:199],v240 offset:1152
mm v[100:103],v[200:203],v[140:143],0
cv v250,v228,v229
cv v251,v230,v231
mm v[100:103],v[204:207],v[156:159],v[100:103]
ld v[184:187],v246,s[52:55],s91 offen offset:3072 sc1
mm v[68:71],v[204:207],v[152:155],0
aw a128,v248
aw a129,v249
mm v[68:71],v[200:203],v[136:139],v[68:71]
rt v[192:193],v240 offset:8576
rt v[194:195],v240 offset:9600
mm v[36:39],v[200:203],v[132:135],0
aw a130,v250
aw a131,v251
mm v[36:39],v[204:207],v[148:151],v[36:39]
ld v[188:191],v247,s[52:55],s91 offen offset:3072 sc1
mm v[4:7],v[204:207],v[144:147],0
wt lgkmcnt(4)
s_add_u32 s91, s91, 4096
mm v[4:7],v[200:203],v[128:131],v[4:7]
rt v[204:205],v241 offset:128
rt v[206:207],v241 offset:1152
mm v[8:11],v[208:211],v[128:131],0
s_and_b32 s97, s2, 7
)ASM" R"ASM(s_mul_i32 s95, s97, 7
mm v[8:11],v[212:215],v[144:147],v[8:11]
ld a[96:99],v244,s[52:55],s91 offen offset:0 sc1
mm v[40:43],v[212:215],v[148:151],0
s_lshr_b32 s96, s30, 7
s_add_u32 s95, s95, s96
mm v[40:43],v[208:211],v[132:135],v[40:43]
rt v[200:201],v241 offset:8576
rt v[202:203],v241 offset:9600
mm v[72:75],v[208:211],v[136:139],0
s_sub_u32 s95, 3, s95
s_and_b32 s95, s95, 7
mm v[72:75],v[212:215],v[152:155],v[72:75]
ld a[100:103],v245,s[52:55],s91 offen offset:0 sc1
mm v[104:107],v[212:215],v[156:159],0
s_min_u32 s95, s95, 6
s_lshl_b32 s95, s95, 1
mm v[104:107],v[208:211],v[140:143],v[104:107]
rt v[212:213],v242 offset:128
rt v[214:215],v242 offset:1152
mm v[108:111],v[216:219],v[140:143],0
s_lshr_b32 s92, s2, 6
s_mul_i32 s93, s92, 3
mm v[108:111],v[220:223],v[156:159],v[108:111]
ld a[104:107],v246,s[52:55],s91 offen offset:0 sc1
mm v[76:79],v[220:223],v[152:155],0
s_add_u32 s93, s93, s74
mm v[76:79],v[216:219],v[136:139],v[76:79]
rt v[208:209],v242 offset:8576
rt v[210:211],v242 offset:9600
mm v[44:47],v[216:219],v[132:135],0
ds_write_b128 v238, v[248:251] offset:3072
mm v[44:47],v[220:223],v[148:151],v[44:47]
st v[248:251],v255,s[48:51],s89 offen offset:0 sc1
mm v[12:15],v[220:223],v[144:147],0
wt lgkmcnt(5)
mm v[12:15],v[216:219],v[128:131],v[12:15]
rt v[220:221],v243 offset:128
rt v[222:223],v243 offset:1152
mm v[16:19],v[196:199],v[128:131],0
ar v224,a120
ar v225,a121
mm v[16:19],v[192:195],v[144:147],v[16:19]
ld a[108:111],v247,s[52:55],s91 offen offset:0 sc1
mm v[48:51],v[192:195],v[148:151],0
ar v226,a122
ar v227,a123
mm v[48:51],v[196:199],v[132:135],v[48:51]
rt v[216:217],v243 offset:8576
rt v[218:219],v243 offset:9600
mm v[80:83],v[196:199],v[136:139],0
ar v228,a124
ar v229,a125
mm v[80:83],v[192:195],v[152:155],v[80:83]
ld a[112:115],v244,s[52:55],s91 offen offset:1024 sc1
mm v[112:115],v[192:195],v[156:159],0
ar v230,a126
ar v231,a127
mm v[112:115],v[196:199],v[140:143],v[112:115]
cv v248,v224,v225
cv v249,v226,v227
mm v[116:119],v[204:207],v[140:143],0
wt lgkmcnt(0)
s_barrier
mm v[116:119],v[200:203],v[156:159],v[116:119]
ld a[116:119],v245,s[52:55],s91 offen offset:1024 sc1
mm v[84:87],v[200:203],v[152:155],0
rt v[192:193],v240 offset:16896
rt v[194:195],v240 offset:17920
mm v[84:87],v[204:207],v[136:139],v[84:87]
rt v[196:197],v240 offset:25344
rt v[198:199],v240 offset:26368
mm v[52:55],v[204:207],v[132:135],0
s_add_u32 m0, s83, 0
cv v250,v228,v229
mm v[52:55],v[200:203],v[148:151],v[52:55]
ld v232,s[44:47],s80 offen lds
mm v[20:23],v[200:203],v[144:147],0
cv v251,v230,v231
aw a132,v248
mm v[20:23],v[204:207],v[128:131],v[20:23]
rt v[200:201],v241 offset:16896
rt v[202:203],v241 offset:17920
mm v[24:27],v[212:215],v[128:131],0
aw a133,v249
aw a134,v250
mm v[24:27],v[208:211],v[144:147],v[24:27]
aw a135,v251
mm v[56:59],v[208:211],v[148:151],0
s_cmp_ge_u32 s93, s95
s_cselect_b32 s96, 2, 0
mm v[56:59],v[212:215],v[132:135],v[56:59]
rt v[204:205],v241 offset:25344
rt v[206:207],v241 offset:26368
mm v[88:91],v[212:215],v[136:139],0
s_add_u32 m0, s83, 1024
s_add_u32 s93, s93, s96
mm v[88:91],v[208:211],v[152:155],v[88:91]
ld v233,s[44:47],s80 offen lds
mm v[120:123],v[208:211],v[156:159],0
)ASM" R"ASM(s_add_u32 s95, s95, s92
mm v[120:123],v[212:215],v[140:143],v[120:123]
rt v[208:209],v242 offset:16896
rt v[210:211],v242 offset:17920
mm v[124:127],v[220:223],v[140:143],0
s_cmp_lt_u32 s92, 2
s_cselect_b32 s94, s75, 0
mm v[124:127],v[216:219],v[156:159],v[124:127]
ld v[140:143],v246,s[52:55],s91 offen offset:1024 sc1
mm v[92:95],v[216:219],v[152:155],0
s_cmp_eq_u32 s74, 3
s_cselect_b32 s92, s95, s93
s_cselect_b32 s94, s94, s75
mm v[92:95],v[220:223],v[136:139],v[92:95]
rt v[212:213],v242 offset:25344
rt v[214:215],v242 offset:26368
mm v[60:63],v[220:223],v[132:135],0
ds_write_b128 v238, v[248:251] offset:4096
mm v[60:63],v[216:219],v[148:151],v[60:63]
s_mul_i32 s93, s97, 14
s_add_u32 s92, s92, s93
mm v[28:31],v[216:219],v[144:147],0
wt vmcnt(10) lgkmcnt(5)
s_mul_i32 s96, s92, s94
mm v[28:31],v[220:223],v[128:131],v[28:31]
rt v[216:217],v243 offset:16896
rt v[218:219],v243 offset:17920
mm v[0:3],v[192:195],v[160:163],v[0:3]
ar v224,a136
ar v225,a137
mm v[0:3],v[196:199],v[176:179],v[0:3]
ld v[128:131],v247,s[52:55],s91 offen offset:1024 sc1
mm v[32:35],v[196:199],v[180:183],v[32:35]
ar v226,a138
ar v227,a139
mm v[32:35],v[192:195],v[164:167],v[32:35]
rt v[220:221],v243 offset:25344
rt v[222:223],v243 offset:26368
mm v[64:67],v[192:195],v[168:171],v[64:67]
ar v228,a140
ar v229,a141
mm v[64:67],v[196:199],v[184:187],v[64:67]
ld v[132:135],v244,s[52:55],s91 offen offset:2048 sc1
mm v[96:99],v[196:199],v[188:191],v[96:99]
ar v230,a142
ar v231,a143
mm v[96:99],v[192:195],v[172:175],v[96:99]
rt v[196:197],v240 offset:17024
rt v[198:199],v240 offset:18048
mm v[100:103],v[200:203],v[172:175],v[100:103]
s_add_u32 m0, s83, 2176
cv v248,v224,v225
mm v[100:103],v[204:207],v[188:191],v[100:103]
ld v234,s[44:47],s80 offen lds
mm v[68:71],v[204:207],v[184:187],v[68:71]
cv v249,v226,v227
cv v250,v228,v229
mm v[68:71],v[200:203],v[168:171],v[68:71]
rt v[192:193],v240 offset:25472
rt v[194:195],v240 offset:26496
mm v[36:39],v[200:203],v[164:167],v[36:39]
cv v251,v230,v231
aw a136,v248
mm v[36:39],v[204:207],v[180:183],v[36:39]
ld v[136:139],v245,s[52:55],s91 offen offset:2048 sc1
mm v[4:7],v[204:207],v[176:179],v[4:7]
wt lgkmcnt(4)
aw a137,v249
mm v[4:7],v[200:203],v[160:163],v[4:7]
rt v[204:205],v241 offset:17024
rt v[206:207],v241 offset:18048
mm v[8:11],v[208:211],v[160:163],v[8:11]
s_add_u32 m0, s83, 3200
aw a138,v250
mm v[8:11],v[212:215],v[176:179],v[8:11]
ld v235,s[44:47],s80 offen lds
mm v[40:43],v[212:215],v[180:183],v[40:43]
s_add_u32 s80, s80, 524288
aw a139,v251
mm v[40:43],v[208:211],v[164:167],v[40:43]
rt v[200:201],v241 offset:25472
rt v[202:203],v241 offset:26496
mm v[72:75],v[208:211],v[168:171],v[72:75]
s_mov_b32 s100, s96
s_and_b32 s93, s2, 7
mm v[72:75],v[212:215],v[184:187],v[72:75]
ld v[144:147],v246,s[52:55],s91 offen offset:2048 sc1
mm v[104:107],v[212:215],v[188:191],v[104:107]
s_lshr_b32 s95, s93, 1
s_xor_b32 s97, s95, s93
mm v[104:107],v[208:211],v[172:175],v[104:107]
rt v[212:213],v242 offset:17024
rt v[214:215],v242 offset:18048
mm v[108:111],v[216:219],v[172:175],v[108:111]
s_lshl_b32 s95, s95, 21
s_and_b32 s97, s97, 1
mm v[108:111],v[220:223],v[188:191],v[108:111]
ld v[148:151],v247,s[52:55],s91 offen offset:2048 sc1
mm v[76:79],v[220:223],v[184:187],v[76:79]
s_mov_b32 s18, 53248
)ASM" R"ASM(s_lshl_b32 s97, s97, 10
mm v[76:79],v[216:219],v[168:171],v[76:79]
rt v[208:209],v242 offset:25472
rt v[210:211],v242 offset:26496
mm v[44:47],v[216:219],v[164:167],v[44:47]
ds_write_b128 v238, v[248:251] offset:5120
mm v[44:47],v[220:223],v[180:183],v[44:47]
st a[132:135],v255,s[48:51],s18 offen offset:0 sc1
mm v[12:15],v[220:223],v[176:179],v[12:15]
wt lgkmcnt(5)
s_add_u32 s95, s95, s97
mm v[12:15],v[216:219],v[160:163],v[12:15]
rt v[220:221],v243 offset:17024
rt v[222:223],v243 offset:18048
mm v[16:19],v[196:199],v[160:163],v[16:19]
ar v224,a224
ar v225,a225
mm v[16:19],v[192:195],v[176:179],v[16:19]
ld v[152:155],v244,s[52:55],s91 offen offset:3072 sc1
mm v[48:51],v[192:195],v[180:183],v[48:51]
ar v226,a226
ar v227,a227
mm v[48:51],v[196:199],v[164:167],v[48:51]
rt v[216:217],v243 offset:25472
rt v[218:219],v243 offset:26496
mm v[80:83],v[196:199],v[168:171],v[80:83]
ar v228,a228
ar v229,a229
mm v[80:83],v[192:195],v[184:187],v[80:83]
ld v[156:159],v245,s[52:55],s91 offen offset:3072 sc1
mm v[112:115],v[192:195],v[188:191],v[112:115]
ar v230,a230
ar v231,a231
mm v[112:115],v[196:199],v[172:175],v[112:115]
cv v248,v224,v225
cv v249,v226,v227
mm v[116:119],v[204:207],v[172:175],v[116:119]
wt lgkmcnt(0)
s_barrier
mm v[116:119],v[200:203],v[188:191],v[116:119]
ld a[120:123],v246,s[52:55],s91 offen offset:3072 sc1
mm v[84:87],v[200:203],v[184:187],v[84:87]
rt v[192:193],v240 offset:33792
rt v[194:195],v240 offset:34816
mm v[84:87],v[204:207],v[168:171],v[84:87]
rt v[196:197],v240 offset:42240
rt v[198:199],v240 offset:43264
mm v[52:55],v[204:207],v[164:167],v[52:55]
s_add_u32 m0, s83, 16896
cv v250,v228,v229
mm v[52:55],v[200:203],v[180:183],v[52:55]
ld v232,s[44:47],s80 offen lds
mm v[20:23],v[200:203],v[176:179],v[20:23]
cv v251,v230,v231
aw a140,v248
mm v[20:23],v[204:207],v[160:163],v[20:23]
rt v[200:201],v241 offset:33792
rt v[202:203],v241 offset:34816
mm v[24:27],v[212:215],v[160:163],v[24:27]
aw a141,v249
aw a142,v250
mm v[24:27],v[208:211],v[176:179],v[24:27]
ld a[124:127],v247,s[52:55],s91 offen offset:3072 sc1
mm v[56:59],v[208:211],v[180:183],v[56:59]
s_add_u32 s91, s91, 4096
aw a143,v251
mm v[56:59],v[212:215],v[164:167],v[56:59]
rt v[204:205],v241 offset:42240
rt v[206:207],v241 offset:43264
mm v[88:91],v[212:215],v[168:171],v[88:91]
s_add_u32 m0, s83, 17920
s_mul_i32 s97, s93, 896
mm v[88:91],v[208:211],v[184:187],v[88:91]
ld v233,s[44:47],s80 offen lds
mm v[120:123],v[208:211],v[188:191],v[120:123]
s_cmp_eq_u32 s3, 0
s_cselect_b32 s95, s95, s97
s_cselect_b64 s[36:37], s[28:29], s[30:31]
s_cselect_b32 s21, 2, 7
s_cselect_b32 s93, s93, 0
mm v[120:123],v[212:215],v[172:175],v[120:123]
rt v[208:209],v242 offset:33792
rt v[210:211],v242 offset:34816
mm v[124:127],v[220:223],v[172:175],v[124:127]
s_cmp_eq_u32 s3, 2
s_cselect_b64 s[36:37], s[32:33], s[36:37]
mm v[124:127],v[216:219],v[188:191],v[124:127]
s_lshl_b32 s97, s97, 10
s_mov_b32 s92, 672
mm v[92:95],v[216:219],v[184:187],v[92:95]
s_mov_b32 s90, 352
mm v[92:95],v[220:223],v[168:171],v[92:95]
rt v[212:213],v242 offset:42240
rt v[214:215],v242 offset:43264
mm v[60:63],v[220:223],v[164:167],v[60:63]
ds_write_b128 v238, v[248:251] offset:6144
mm v[60:63],v[216:219],v[180:183],v[60:63]
st a[136:139],v255,s[48:51],s18 offen offset:1024 sc1
mm v[28:31],v[216:219],v[176:179],v[28:31]
wt vmcnt(14) lgkmcnt(5)
)ASM" R"ASM(mm v[28:31],v[220:223],v[160:163],v[28:31]
rt v[216:217],v243 offset:33792
rt v[218:219],v243 offset:34816
mm v[0:3],v[192:195],a[96:99],v[0:3]
ar v224,a144
ar v225,a145
mm v[0:3],v[196:199],a[112:115],v[0:3]
ld v[160:163],v244,s[52:55],s91 offen offset:0 sc1
mm v[32:35],v[196:199],a[116:119],v[32:35]
ar v226,a146
ar v227,a147
mm v[32:35],v[192:195],a[100:103],v[32:35]
rt v[220:221],v243 offset:42240
rt v[222:223],v243 offset:43264
mm v[64:67],v[192:195],a[104:107],v[64:67]
ar v228,a148
ar v229,a149
mm v[64:67],v[196:199],v[140:143],v[64:67]
ld v[164:167],v245,s[52:55],s91 offen offset:0 sc1
mm v[96:99],v[196:199],v[128:131],v[96:99]
ar v230,a150
ar v231,a151
mm v[96:99],v[192:195],a[108:111],v[96:99]
rt v[196:197],v240 offset:33920
rt v[198:199],v240 offset:34944
mm v[100:103],v[200:203],a[108:111],v[100:103]
s_add_u32 m0, s83, 19072
cv v248,v224,v225
mm v[100:103],v[204:207],v[128:131],v[100:103]
ld v234,s[44:47],s80 offen lds
mm v[68:71],v[204:207],v[140:143],v[68:71]
cv v249,v226,v227
cv v250,v228,v229
mm v[68:71],v[200:203],a[104:107],v[68:71]
rt v[192:193],v240 offset:42368
rt v[194:195],v240 offset:43392
mm v[36:39],v[200:203],a[100:103],v[36:39]
cv v251,v230,v231
aw a144,v248
mm v[36:39],v[204:207],a[116:119],v[36:39]
ld v[168:171],v246,s[52:55],s91 offen offset:0 sc1
.La5_LW1_10:
s_cbranch_execz .La5_LW1_14
mm v[4:7],v[204:207],a[112:115],v[4:7]
wt lgkmcnt(4)
aw a145,v249
mm v[4:7],v[200:203],a[96:99],v[4:7]
rt v[204:205],v241 offset:33920
rt v[206:207],v241 offset:34944
mm v[8:11],v[208:211],a[96:99],v[8:11]
s_add_u32 m0, s83, 20096
aw a146,v250
mm v[8:11],v[212:215],a[112:115],v[8:11]
ld v235,s[44:47],s80 offen lds
mm v[40:43],v[212:215],a[116:119],v[40:43]
s_add_u32 s80, s80, 524288
aw a147,v251
mm v[40:43],v[208:211],a[100:103],v[40:43]
rt v[200:201],v241 offset:42368
rt v[202:203],v241 offset:43392
mm v[72:75],v[208:211],a[104:107],v[72:75]
s_cmp_eq_u32 s3, 3
s_cselect_b32 s95, s97, s95
s_cselect_b64 s[36:37], s[34:35], s[36:37]
s_cselect_b32 s21, 1, s21
s_cselect_b32 s92, 544, s92
mm v[72:75],v[212:215],v[140:143],v[72:75]
ld v[172:175],v247,s[52:55],s91 offen offset:0 sc1
mm v[104:107],v[212:215],v[128:131],v[104:107]
s_lshl_b32 s21, s21, 10
s_and_b32 s7, s93, 1
mm v[104:107],v[208:211],a[108:111],v[104:107]
rt v[212:213],v242 offset:33920
rt v[214:215],v242 offset:34944
mm v[108:111],v[216:219],a[108:111],v[108:111]
s_cmp_eq_u32 s75, 1
s_cselect_b32 s97, s90, s92
s_cselect_b32 s92, s92, s90
mm v[108:111],v[220:223],v[128:131],v[108:111]
s_cmp_eq_u32 s75, s7
s_cselect_b32 s92, s92, 0
mm v[76:79],v[220:223],v[140:143],v[76:79]
s_mul_i32 s92, s92, s21
s_add_u32 s95, s95, s92
mm v[76:79],v[216:219],a[104:107],v[76:79]
rt v[208:209],v242 offset:42368
rt v[210:211],v242 offset:43392
mm v[44:47],v[216:219],a[100:103],v[44:47]
ds_write_b128 v238, v[248:251] offset:7168
mm v[44:47],v[220:223],a[116:119],v[44:47]
st a[140:143],v255,s[48:51],s18 offen offset:2048 sc1
mm v[12:15],v[220:223],a[112:115],v[12:15]
wt lgkmcnt(5)
s_mul_i32 s97, s97, s21
mm v[12:15],v[216:219],a[96:99],v[12:15]
rt v[220:221],v243 offset:33920
rt v[222:223],v243 offset:34944
mm v[16:19],v[196:199],a[96:99],v[16:19]
ar v224,a232
ar v225,a233
)ASM" R"ASM(mm v[16:19],v[192:195],a[112:115],v[16:19]
ld v[176:179],v244,s[52:55],s91 offen offset:1024 sc1
mm v[48:51],v[192:195],a[116:119],v[48:51]
ar v226,a234
ar v227,a235
mm v[48:51],v[196:199],a[100:103],v[48:51]
rt v[216:217],v243 offset:42368
rt v[218:219],v243 offset:43392
mm v[80:83],v[196:199],a[104:107],v[80:83]
ar v228,a236
ar v229,a237
mm v[80:83],v[192:195],v[140:143],v[80:83]
ld v[180:183],v245,s[52:55],s91 offen offset:1024 sc1
mm v[112:115],v[192:195],v[128:131],v[112:115]
ar v230,a238
ar v231,a239
mm v[112:115],v[196:199],a[108:111],v[112:115]
cv v248,v224,v225
cv v249,v226,v227
mm v[116:119],v[204:207],a[108:111],v[116:119]
wt lgkmcnt(0)
s_barrier
mm v[116:119],v[200:203],v[128:131],v[116:119]
ld v[184:187],v246,s[52:55],s91 offen offset:1024 sc1
mm v[84:87],v[200:203],v[140:143],v[84:87]
rt v[192:193],v240 offset:50688
rt v[194:195],v240 offset:51712
mm v[84:87],v[204:207],a[104:107],v[84:87]
rt v[196:197],v240 offset:59136
rt v[198:199],v240 offset:60160
mm v[52:55],v[204:207],a[100:103],v[52:55]
s_add_u32 m0, s83, 33792
cv v250,v228,v229
mm v[52:55],v[200:203],a[116:119],v[52:55]
ld v232,s[44:47],s80 offen lds
mm v[20:23],v[200:203],a[112:115],v[20:23]
cv v251,v230,v231
aw a148,v248
mm v[20:23],v[204:207],a[96:99],v[20:23]
rt v[200:201],v241 offset:50688
rt v[202:203],v241 offset:51712
mm v[24:27],v[212:215],a[96:99],v[24:27]
aw a149,v249
aw a150,v250
mm v[24:27],v[208:211],a[112:115],v[24:27]
ld v[188:191],v247,s[52:55],s91 offen offset:1024 sc1
mm v[56:59],v[208:211],a[116:119],v[56:59]
aw a151,v251
s_mul_i32 s90, s97, s7
mm v[56:59],v[212:215],a[100:103],v[56:59]
rt v[204:205],v241 offset:59136
rt v[206:207],v241 offset:60160
mm v[88:91],v[212:215],a[104:107],v[88:91]
s_add_u32 m0, s83, 34816
s_sub_u32 s97, s97, s21
mm v[88:91],v[208:211],v[140:143],v[88:91]
ld v233,s[44:47],s80 offen lds
mm v[120:123],v[208:211],v[128:131],v[120:123]
s_add_u32 s97, s97, 896
mm v[120:123],v[212:215],a[108:111],v[120:123]
rt v[208:209],v242 offset:50688
rt v[210:211],v242 offset:51712
mm v[124:127],v[220:223],a[108:111],v[124:127]
s_add_u32 s36, s36, s95
s_addc_u32 s37, s37, 0
mm v[124:127],v[216:219],v[128:131],v[124:127]
s_and_b32 s37, s37, 0xffff
s_mov_b32 s38, s97
mm v[92:95],v[216:219],v[140:143],v[92:95]
s_mov_b32 s39, 0x20000
s_lshl_b32 s93, s96, 6
mm v[92:95],v[220:223],a[104:107],v[92:95]
rt v[212:213],v242 offset:59136
rt v[214:215],v242 offset:60160
mm v[60:63],v[220:223],a[100:103],v[60:63]
ds_write_b128 v238, v[248:251] offset:32768
mm v[60:63],v[216:219],a[116:119],v[60:63]
s_mul_i32 s95, s94, 7332928
mm v[28:31],v[216:219],a[112:115],v[28:31]
wt vmcnt(15) lgkmcnt(5)
mm v[28:31],v[220:223],a[96:99],v[28:31]
rt v[216:217],v243 offset:50688
rt v[218:219],v243 offset:51712
mm v[0:3],v[192:195],v[132:135],v[0:3]
ar v224,a152
ar v225,a153
mm v[0:3],v[196:199],v[152:155],v[0:3]
ld v[128:131],v244,s[52:55],s91 offen offset:2048 sc1
mm v[32:35],v[196:199],v[156:159],v[32:35]
ar v226,a154
ar v227,a155
mm v[32:35],v[192:195],v[136:139],v[32:35]
rt v[220:221],v243 offset:59136
rt v[222:223],v243 offset:60160
mm v[64:67],v[192:195],v[144:147],v[64:67]
ar v228,a156
ar v229,a157
mm v[64:67],v[196:199],a[120:123],v[64:67]
ld v[140:143],v245,s[52:55],s91 offen offset:2048 sc1
mm v[96:99],v[196:199],a[124:127],v[96:99]
)ASM" R"ASM(ar v230,a158
ar v231,a159
mm v[96:99],v[192:195],v[148:151],v[96:99]
rt v[196:197],v240 offset:50816
rt v[198:199],v240 offset:51840
mm v[100:103],v[200:203],v[148:151],v[100:103]
s_add_u32 m0, s83, 35968
cv v248,v224,v225
mm v[100:103],v[204:207],a[124:127],v[100:103]
ld v234,s[44:47],s80 offen lds
mm v[68:71],v[204:207],a[120:123],v[68:71]
cv v249,v226,v227
cv v250,v228,v229
mm v[68:71],v[200:203],v[144:147],v[68:71]
rt v[192:193],v240 offset:59264
rt v[194:195],v240 offset:60288
mm v[36:39],v[200:203],v[136:139],v[36:39]
cv v251,v230,v231
aw a152,v248
mm v[36:39],v[204:207],v[156:159],v[36:39]
ld a[96:99],v246,s[52:55],s91 offen offset:2048 sc1
mm v[4:7],v[204:207],v[152:155],v[4:7]
wt lgkmcnt(4)
aw a153,v249
mm v[4:7],v[200:203],v[132:135],v[4:7]
rt v[204:205],v241 offset:50816
rt v[206:207],v241 offset:51840
mm v[8:11],v[208:211],v[132:135],v[8:11]
s_add_u32 m0, s83, 36992
aw a154,v250
mm v[8:11],v[212:215],v[152:155],v[8:11]
ld v235,s[44:47],s80 offen lds
mm v[40:43],v[212:215],v[156:159],v[40:43]
s_add_u32 s80, s80, 524288
aw a155,v251
mm v[40:43],v[208:211],v[136:139],v[40:43]
rt v[200:201],v241 offset:59264
rt v[202:203],v241 offset:60288
mm v[72:75],v[208:211],v[144:147],v[72:75]
s_cmp_eq_u32 s77, 0
s_cselect_b64 s[40:41], s[30:31], s[32:33]
mm v[72:75],v[212:215],a[120:123],v[72:75]
ld a[100:103],v247,s[52:55],s91 offen offset:2048 sc1
mm v[104:107],v[212:215],a[124:127],v[104:107]
s_add_u32 s40, s40, s93
s_addc_u32 s41, s41, 0
mm v[104:107],v[208:211],v[148:151],v[104:107]
rt v[212:213],v242 offset:50816
rt v[214:215],v242 offset:51840
mm v[108:111],v[216:219],v[148:151],v[108:111]
s_and_b32 s41, s41, 0xffff
s_mov_b32 s42, s95
mm v[108:111],v[220:223],a[124:127],v[108:111]
s_mov_b32 s43, 0x20000
s_lshr_b32 s92, s72, 1
mm v[76:79],v[220:223],a[120:123],v[76:79]
s_and_b32 s92, s92, 4
s_xor_b32 s92, s92, s72
mm v[76:79],v[216:219],v[144:147],v[76:79]
rt v[208:209],v242 offset:59264
rt v[210:211],v242 offset:60288
mm v[44:47],v[216:219],v[136:139],v[44:47]
ds_write_b128 v238, v[248:251] offset:33792
mm v[44:47],v[220:223],v[156:159],v[44:47]
st a[144:147],v255,s[48:51],s18 offen offset:3072 sc1
mm v[12:15],v[220:223],v[152:155],v[12:15]
wt lgkmcnt(5)
s_xor_b32 s92, s92, 1
mm v[12:15],v[216:219],v[132:135],v[12:15]
rt v[220:221],v243 offset:50816
rt v[222:223],v243 offset:51840
mm v[16:19],v[196:199],v[132:135],v[16:19]
ar v224,a208
ar v225,a209
mm v[16:19],v[192:195],v[152:155],v[16:19]
ld a[104:107],v244,s[52:55],s91 offen offset:3072 sc1
mm v[48:51],v[192:195],v[156:159],v[48:51]
ar v226,a210
ar v227,a211
mm v[48:51],v[196:199],v[136:139],v[48:51]
rt v[216:217],v243 offset:59264
rt v[218:219],v243 offset:60288
mm v[80:83],v[196:199],v[144:147],v[80:83]
ar v228,a212
ar v229,a213
mm v[80:83],v[192:195],a[120:123],v[80:83]
ld a[108:111],v245,s[52:55],s91 offen offset:3072 sc1
mm v[112:115],v[192:195],a[124:127],v[112:115]
ar v230,a214
ar v231,a215
mm v[112:115],v[196:199],v[148:151],v[112:115]
cv v248,v224,v225
cv v249,v226,v227
mm v[116:119],v[204:207],v[148:151],v[116:119]
wt lgkmcnt(0)
s_barrier
mm v[116:119],v[200:203],a[124:127],v[116:119]
ld a[112:115],v246,s[52:55],s91 offen offset:3072 sc1
mm v[84:87],v[200:203],a[120:123],v[84:87]
rt v[192:193],v240 offset:0
)ASM" R"ASM(rt v[194:195],v240 offset:1024
mm v[84:87],v[204:207],v[144:147],v[84:87]
rt v[196:197],v240 offset:8448
rt v[198:199],v240 offset:9472
mm v[52:55],v[204:207],v[136:139],v[52:55]
s_add_u32 m0, s83, 50688
cv v250,v228,v229
mm v[52:55],v[200:203],v[156:159],v[52:55]
ld v232,s[44:47],s80 offen lds
mm v[20:23],v[200:203],v[152:155],v[20:23]
cv v251,v230,v231
aw a156,v248
mm v[20:23],v[204:207],v[132:135],v[20:23]
rt v[200:201],v241 offset:0
rt v[202:203],v241 offset:1024
mm v[24:27],v[212:215],v[132:135],v[24:27]
aw a157,v249
aw a158,v250
mm v[24:27],v[208:211],v[152:155],v[24:27]
ld a[116:119],v247,s[52:55],s91 offen offset:3072 sc1
mm v[56:59],v[208:211],v[156:159],v[56:59]
s_add_u32 s91, s91, 4096
aw a159,v251
mm v[56:59],v[212:215],v[136:139],v[56:59]
rt v[204:205],v241 offset:8448
rt v[206:207],v241 offset:9472
mm v[88:91],v[212:215],v[144:147],v[88:91]
s_add_u32 m0, s83, 51712
s_and_b32 s101, s92, 7
mm v[88:91],v[208:211],a[120:123],v[88:91]
ld v233,s[44:47],s80 offen lds
mm v[120:123],v[208:211],a[124:127],v[120:123]
s_lshl_b32 s92, s101, 8
s_sub_u32 s93, 8388608, s92
mm v[120:123],v[212:215],v[148:151],v[120:123]
rt v[208:209],v242 offset:0
rt v[210:211],v242 offset:1024
mm v[124:127],v[220:223],v[148:151],v[124:127]
s_add_u32 s56, s28, s92
s_addc_u32 s57, s29, 0
mm v[124:127],v[216:219],a[124:127],v[124:127]
s_and_b32 s57, s57, 0xffff
s_mov_b32 s58, s93
mm v[92:95],v[216:219],a[120:123],v[92:95]
s_mov_b32 s18, 32768
s_mov_b32 s59, 0x20000
mm v[92:95],v[220:223],v[144:147],v[92:95]
rt v[212:213],v242 offset:8448
rt v[214:215],v242 offset:9472
mm v[60:63],v[220:223],v[136:139],v[60:63]
ds_write_b128 v238, v[248:251] offset:34816
mm v[60:63],v[216:219],v[156:159],v[60:63]
st a[148:151],v255,s[48:51],s18 offen offset:0 sc1
mm v[28:31],v[216:219],v[152:155],v[28:31]
wt vmcnt(15) lgkmcnt(5)
s_lshl_b32 s93, s96, 16
mm v[28:31],v[220:223],v[132:135],v[28:31]
rt v[216:217],v243 offset:0
rt v[218:219],v243 offset:1024
mm v[0:3],v[192:195],v[160:163],v[0:3]
ar v224,a160
ar v225,a161
mm v[0:3],v[196:199],v[176:179],v[0:3]
ld v[132:135],v244,s[52:55],s91 offen offset:0 sc1
mm v[32:35],v[196:199],v[180:183],v[32:35]
ar v226,a162
ar v227,a163
mm v[32:35],v[192:195],v[164:167],v[32:35]
rt v[220:221],v243 offset:8448
rt v[222:223],v243 offset:9472
mm v[64:67],v[192:195],v[168:171],v[64:67]
ar v228,a164
ar v229,a165
mm v[64:67],v[196:199],v[184:187],v[64:67]
ld v[136:139],v245,s[52:55],s91 offen offset:0 sc1
mm v[96:99],v[196:199],v[188:191],v[96:99]
ar v230,a166
ar v231,a167
mm v[96:99],v[192:195],v[172:175],v[96:99]
rt v[196:197],v240 offset:128
rt v[198:199],v240 offset:1152
mm v[100:103],v[200:203],v[172:175],v[100:103]
s_add_u32 m0, s83, 52864
cv v248,v224,v225
mm v[100:103],v[204:207],v[188:191],v[100:103]
ld v234,s[44:47],s80 offen lds
mm v[68:71],v[204:207],v[184:187],v[68:71]
cv v249,v226,v227
cv v250,v228,v229
mm v[68:71],v[200:203],v[168:171],v[68:71]
rt v[192:193],v240 offset:8576
rt v[194:195],v240 offset:9600
mm v[36:39],v[200:203],v[164:167],v[36:39]
cv v251,v230,v231
aw a160,v248
mm v[36:39],v[204:207],v[180:183],v[36:39]
ld v[144:147],v246,s[52:55],s91 offen offset:0 sc1
mm v[4:7],v[204:207],v[176:179],v[4:7]
wt lgkmcnt(4)
aw a161,v249
)ASM" R"ASM(mm v[4:7],v[200:203],v[160:163],v[4:7]
rt v[204:205],v241 offset:128
rt v[206:207],v241 offset:1152
mm v[8:11],v[208:211],v[160:163],v[8:11]
s_add_u32 m0, s83, 53888
aw a162,v250
mm v[8:11],v[212:215],v[176:179],v[8:11]
ld v235,s[44:47],s80 offen lds
mm v[40:43],v[212:215],v[180:183],v[40:43]
s_add_u32 s80, s80, 524288
aw a163,v251
mm v[40:43],v[208:211],v[164:167],v[40:43]
rt v[200:201],v241 offset:8576
rt v[202:203],v241 offset:9600
mm v[72:75],v[208:211],v[168:171],v[72:75]
s_lshl_b32 s95, s94, 16
mm v[72:75],v[212:215],v[184:187],v[72:75]
ld v[148:151],v247,s[52:55],s91 offen offset:0 sc1
mm v[104:107],v[212:215],v[188:191],v[104:107]
s_add_u32 s28, s34, s93
s_addc_u32 s29, s35, 0
mm v[104:107],v[208:211],v[172:175],v[104:107]
rt v[212:213],v242 offset:128
rt v[214:215],v242 offset:1152
mm v[108:111],v[216:219],v[172:175],v[108:111]
s_and_b32 s29, s29, 0xffff
s_mov_b32 s30, s95
mm v[108:111],v[220:223],v[188:191],v[108:111]
s_mov_b32 s31, 0x20000
s_lshl_b32 s93, s101, 12
mm v[76:79],v[220:223],v[184:187],v[76:79]
s_add_u32 s92, s93, 71303168
s_sub_u32 s93, 16777216, s93
mm v[76:79],v[216:219],v[168:171],v[76:79]
rt v[208:209],v242 offset:8576
rt v[210:211],v242 offset:9600
mm v[44:47],v[216:219],v[164:167],v[44:47]
ds_write_b128 v238, v[248:251] offset:35840
mm v[44:47],v[220:223],v[180:183],v[44:47]
st a[152:155],v255,s[48:51],s18 offen offset:1024 sc1
mm v[12:15],v[220:223],v[176:179],v[12:15]
wt lgkmcnt(5)
mm v[12:15],v[216:219],v[160:163],v[12:15]
rt v[220:221],v243 offset:128
rt v[222:223],v243 offset:1152
mm v[16:19],v[196:199],v[160:163],v[16:19]
ar v224,a216
ar v225,a217
mm v[16:19],v[192:195],v[176:179],v[16:19]
ld v[152:155],v244,s[52:55],s91 offen offset:1024 sc1
mm v[48:51],v[192:195],v[180:183],v[48:51]
ar v226,a218
ar v227,a219
mm v[48:51],v[196:199],v[164:167],v[48:51]
rt v[216:217],v243 offset:8576
rt v[218:219],v243 offset:9600
mm v[80:83],v[196:199],v[168:171],v[80:83]
ar v228,a220
ar v229,a221
mm v[80:83],v[192:195],v[184:187],v[80:83]
ld v[156:159],v245,s[52:55],s91 offen offset:1024 sc1
mm v[112:115],v[192:195],v[188:191],v[112:115]
ar v230,a222
ar v231,a223
mm v[112:115],v[196:199],v[172:175],v[112:115]
cv v248,v224,v225
cv v249,v226,v227
mm v[116:119],v[204:207],v[172:175],v[116:119]
wt lgkmcnt(0)
s_barrier
mm v[116:119],v[200:203],v[188:191],v[116:119]
ld a[120:123],v246,s[52:55],s91 offen offset:1024 sc1
mm v[84:87],v[200:203],v[184:187],v[84:87]
rt v[192:193],v240 offset:16896
rt v[194:195],v240 offset:17920
mm v[84:87],v[204:207],v[168:171],v[84:87]
rt v[196:197],v240 offset:25344
rt v[198:199],v240 offset:26368
mm v[52:55],v[204:207],v[164:167],v[52:55]
s_add_u32 m0, s83, 0
cv v250,v228,v229
mm v[52:55],v[200:203],v[180:183],v[52:55]
ld v232,s[44:47],s80 offen lds
mm v[20:23],v[200:203],v[176:179],v[20:23]
cv v251,v230,v231
aw a164,v248
mm v[20:23],v[204:207],v[160:163],v[20:23]
rt v[200:201],v241 offset:16896
rt v[202:203],v241 offset:17920
mm v[24:27],v[212:215],v[160:163],v[24:27]
aw a165,v249
aw a166,v250
mm v[24:27],v[208:211],v[176:179],v[24:27]
ld a[124:127],v247,s[52:55],s91 offen offset:1024 sc1
mm v[56:59],v[208:211],v[180:183],v[56:59]
aw a167,v251
mm v[56:59],v[212:215],v[164:167],v[56:59]
rt v[204:205],v241 offset:25344
rt v[206:207],v241 offset:26368
mm v[88:91],v[212:215],v[168:171],v[88:91]
)ASM" R"ASM(s_add_u32 m0, s83, 1024
mm v[88:91],v[208:211],v[184:187],v[88:91]
ld v233,s[44:47],s80 offen lds
mm v[120:123],v[208:211],v[188:191],v[120:123]
s_add_u32 s32, s26, s92
s_addc_u32 s33, s27, 0
mm v[120:123],v[212:215],v[172:175],v[120:123]
rt v[208:209],v242 offset:16896
rt v[210:211],v242 offset:17920
mm v[124:127],v[220:223],v[172:175],v[124:127]
s_and_b32 s33, s33, 0xffff
s_mov_b32 s34, s93
mm v[124:127],v[216:219],v[188:191],v[124:127]
s_mov_b32 s35, 0x20000
s_lshl2_add_u32 s92, s72, s74
mm v[92:95],v[216:219],v[184:187],v[92:95]
s_lshl2_add_u32 s92, s92, s3
s_lshl_b32 s88, s92, 8
mm v[92:95],v[220:223],v[168:171],v[92:95]
rt v[212:213],v242 offset:25344
rt v[214:215],v242 offset:26368
mm v[60:63],v[220:223],v[164:167],v[60:63]
ds_write_b128 v238, v[248:251] offset:36864
mm v[60:63],v[216:219],v[180:183],v[60:63]
st a[156:159],v255,s[48:51],s18 offen offset:2048 sc1
mm v[28:31],v[216:219],v[176:179],v[28:31]
wt vmcnt(16) lgkmcnt(5)
s_mul_i32 s93, s92, 17408
mm v[28:31],v[220:223],v[160:163],v[28:31]
rt v[216:217],v243 offset:16896
rt v[218:219],v243 offset:17920
mm v[0:3],v[192:195],v[128:131],v[0:3]
ar v224,a168
ar v225,a169
mm v[0:3],v[196:199],a[104:107],v[0:3]
ld v[160:163],v244,s[52:55],s91 offen offset:2048 sc1
mm v[32:35],v[196:199],a[108:111],v[32:35]
ar v226,a170
ar v227,a171
mm v[32:35],v[192:195],v[140:143],v[32:35]
rt v[220:221],v243 offset:25344
rt v[222:223],v243 offset:26368
mm v[64:67],v[192:195],a[96:99],v[64:67]
ar v228,a172
ar v229,a173
mm v[64:67],v[196:199],a[112:115],v[64:67]
ld v[164:167],v245,s[52:55],s91 offen offset:2048 sc1
mm v[96:99],v[196:199],a[116:119],v[96:99]
ar v230,a174
ar v231,a175
mm v[96:99],v[192:195],a[100:103],v[96:99]
rt v[196:197],v240 offset:17024
rt v[198:199],v240 offset:18048
mm v[100:103],v[200:203],a[100:103],v[100:103]
s_add_u32 m0, s83, 2176
cv v248,v224,v225
mm v[100:103],v[204:207],a[116:119],v[100:103]
ld v234,s[44:47],s80 offen lds
mm v[68:71],v[204:207],a[112:115],v[68:71]
cv v249,v226,v227
cv v250,v228,v229
mm v[68:71],v[200:203],a[96:99],v[68:71]
rt v[192:193],v240 offset:25472
rt v[194:195],v240 offset:26496
mm v[36:39],v[200:203],v[140:143],v[36:39]
cv v251,v230,v231
aw a168,v248
mm v[36:39],v[204:207],a[108:111],v[36:39]
ld v[168:171],v246,s[52:55],s91 offen offset:2048 sc1
mm v[4:7],v[204:207],a[104:107],v[4:7]
wt lgkmcnt(4)
aw a169,v249
mm v[4:7],v[200:203],v[128:131],v[4:7]
rt v[204:205],v241 offset:17024
rt v[206:207],v241 offset:18048
mm v[8:11],v[208:211],v[128:131],v[8:11]
s_add_u32 m0, s83, 3200
aw a170,v250
mm v[8:11],v[212:215],a[104:107],v[8:11]
ld v235,s[44:47],s80 offen lds
mm v[40:43],v[212:215],a[108:111],v[40:43]
s_add_u32 s80, s80, 524288
aw a171,v251
mm v[40:43],v[208:211],v[140:143],v[40:43]
rt v[200:201],v241 offset:25472
rt v[202:203],v241 offset:26496
.La5_LW1_11:
s_cbranch_execz .La5_LW1_15
mm v[72:75],v[208:211],a[96:99],v[72:75]
s_lshl_b32 s92, s92, 14
s_add_u32 s92, s92, 4194304
mm v[72:75],v[212:215],a[112:115],v[72:75]
ld v[172:175],v247,s[52:55],s91 offen offset:2048 sc1
mm v[104:107],v[212:215],a[116:119],v[104:107]
s_add_u32 s86, s26, s92
s_addc_u32 s87, s27, 0
mm v[104:107],v[208:211],a[100:103],v[104:107]
rt v[212:213],v242 offset:17024
rt v[214:215],v242 offset:18048
mm v[108:111],v[216:219],a[100:103],v[108:111]
)ASM" R"ASM(s_and_b32 s87, s87, 0xffff
s_add_u32 s93, s93, 150994944
mm v[108:111],v[220:223],a[116:119],v[108:111]
s_add_u32 s68, s26, s93
s_addc_u32 s69, s27, 0
mm v[76:79],v[220:223],a[112:115],v[76:79]
s_and_b32 s69, s69, 0xffff
s_mov_b32 s70, 17408
mm v[76:79],v[216:219],a[96:99],v[76:79]
rt v[208:209],v242 offset:25472
rt v[210:211],v242 offset:26496
mm v[44:47],v[216:219],v[140:143],v[44:47]
ds_write_b128 v238, v[248:251] offset:37888
mm v[44:47],v[220:223],a[108:111],v[44:47]
st a[160:163],v255,s[48:51],s18 offen offset:3072 sc1
mm v[12:15],v[220:223],a[104:107],v[12:15]
wt lgkmcnt(5)
s_mov_b32 s96, 153158689
mm v[12:15],v[216:219],v[128:131],v[12:15]
rt v[220:221],v243 offset:17024
rt v[222:223],v243 offset:18048
mm v[16:19],v[196:199],v[128:131],v[16:19]
ar v224,a192
ar v225,a193
mm v[16:19],v[192:195],a[104:107],v[16:19]
ld v[176:179],v244,s[52:55],s91 offen offset:3072 sc1
mm v[48:51],v[192:195],a[108:111],v[48:51]
ar v226,a194
ar v227,a195
mm v[48:51],v[196:199],v[140:143],v[48:51]
rt v[216:217],v243 offset:25472
rt v[218:219],v243 offset:26496
mm v[80:83],v[196:199],a[96:99],v[80:83]
ar v228,a196
ar v229,a197
mm v[80:83],v[192:195],a[112:115],v[80:83]
ld v[180:183],v245,s[52:55],s91 offen offset:3072 sc1
mm v[112:115],v[192:195],a[116:119],v[112:115]
ar v230,a198
ar v231,a199
mm v[112:115],v[196:199],a[100:103],v[112:115]
cv v248,v224,v225
cv v249,v226,v227
mm v[116:119],v[204:207],a[100:103],v[116:119]
wt lgkmcnt(0)
s_barrier
mm v[116:119],v[200:203],a[116:119],v[116:119]
ld v[184:187],v246,s[52:55],s91 offen offset:3072 sc1
mm v[84:87],v[200:203],a[112:115],v[84:87]
rt v[192:193],v240 offset:33792
rt v[194:195],v240 offset:34816
mm v[84:87],v[204:207],a[96:99],v[84:87]
rt v[196:197],v240 offset:42240
rt v[198:199],v240 offset:43264
mm v[52:55],v[204:207],v[140:143],v[52:55]
s_add_u32 m0, s83, 16896
cv v250,v228,v229
mm v[52:55],v[200:203],a[108:111],v[52:55]
ld v232,s[44:47],s80 offen lds
mm v[20:23],v[200:203],a[104:107],v[20:23]
cv v251,v230,v231
aw a172,v248
mm v[20:23],v[204:207],v[128:131],v[20:23]
rt v[200:201],v241 offset:33792
rt v[202:203],v241 offset:34816
mm v[24:27],v[212:215],v[128:131],v[24:27]
aw a173,v249
aw a174,v250
mm v[24:27],v[208:211],a[104:107],v[24:27]
ld v[188:191],v247,s[52:55],s91 offen offset:3072 sc1
mm v[56:59],v[208:211],a[108:111],v[56:59]
s_add_u32 s91, s91, 4096
aw a175,v251
mm v[56:59],v[212:215],v[140:143],v[56:59]
rt v[204:205],v241 offset:42240
rt v[206:207],v241 offset:43264
mm v[88:91],v[212:215],a[96:99],v[88:91]
s_add_u32 m0, s83, 17920
s_mov_b32 s97, 371265825
mm v[88:91],v[208:211],a[112:115],v[88:91]
ld v233,s[44:47],s80 offen lds
mm v[120:123],v[208:211],a[116:119],v[120:123]
s_lshl_b32 s95, s73, 3
s_lshr_b64 s[96:97], s[96:97], s95
mm v[120:123],v[212:215],a[100:103],v[120:123]
rt v[208:209],v242 offset:33792
rt v[210:211],v242 offset:34816
mm v[124:127],v[220:223],a[100:103],v[124:127]
s_and_b32 s82, s96, 255
s_lshl_b64 s[98:99], 1, s82
mm v[124:127],v[216:219],a[116:119],v[124:127]
s_lshl_b32 s96, s75, 22
s_or_b32 s95, s98, s96
mm v[92:95],v[216:219],a[112:115],v[92:95]
s_cmp_eq_u32 s73, 1
s_cselect_b32 s96, 12, 4
s_cselect_b32 s79, 0, 0xff000000
mm v[92:95],v[220:223],a[96:99],v[92:95]
rt v[212:213],v242 offset:42240
rt v[214:215],v242 offset:43264
)ASM" R"ASM(mm v[60:63],v[220:223],v[140:143],v[60:63]
ds_write_b128 v238, v[248:251] offset:38912
mm v[60:63],v[216:219],a[108:111],v[60:63]
s_or_b32 s98, s95, s96
mm v[28:31],v[216:219],a[104:107],v[28:31]
wt vmcnt(15) lgkmcnt(5)
mm v[28:31],v[220:223],v[128:131],v[28:31]
rt v[216:217],v243 offset:33792
rt v[218:219],v243 offset:34816
mm v[0:3],v[192:195],v[132:135],v[0:3]
ar v224,a176
ar v225,a177
mm v[0:3],v[196:199],v[152:155],v[0:3]
ld v[128:131],v244,s[52:55],s91 offen offset:0 sc1
mm v[32:35],v[196:199],v[156:159],v[32:35]
ar v226,a178
ar v227,a179
mm v[32:35],v[192:195],v[136:139],v[32:35]
rt v[220:221],v243 offset:42240
rt v[222:223],v243 offset:43264
mm v[64:67],v[192:195],v[144:147],v[64:67]
ar v228,a180
ar v229,a181
mm v[64:67],v[196:199],a[120:123],v[64:67]
ld v[140:143],v245,s[52:55],s91 offen offset:0 sc1
mm v[96:99],v[196:199],a[124:127],v[96:99]
ar v230,a182
ar v231,a183
mm v[96:99],v[192:195],v[148:151],v[96:99]
rt v[196:197],v240 offset:33920
rt v[198:199],v240 offset:34944
mm v[100:103],v[200:203],v[148:151],v[100:103]
s_add_u32 m0, s83, 19072
cv v248,v224,v225
mm v[100:103],v[204:207],a[124:127],v[100:103]
ld v234,s[44:47],s80 offen lds
mm v[68:71],v[204:207],a[120:123],v[68:71]
cv v249,v226,v227
cv v250,v228,v229
mm v[68:71],v[200:203],v[144:147],v[68:71]
rt v[192:193],v240 offset:42368
rt v[194:195],v240 offset:43392
mm v[36:39],v[200:203],v[136:139],v[36:39]
cv v251,v230,v231
aw a176,v248
mm v[36:39],v[204:207],v[156:159],v[36:39]
ld a[96:99],v246,s[52:55],s91 offen offset:0 sc1
mm v[4:7],v[204:207],v[152:155],v[4:7]
wt lgkmcnt(4)
aw a177,v249
mm v[4:7],v[200:203],v[132:135],v[4:7]
rt v[204:205],v241 offset:33920
rt v[206:207],v241 offset:34944
mm v[8:11],v[208:211],v[132:135],v[8:11]
s_add_u32 m0, s83, 20096
aw a178,v250
mm v[8:11],v[212:215],v[152:155],v[8:11]
ld v235,s[44:47],s80 offen lds
mm v[40:43],v[212:215],v[156:159],v[40:43]
s_add_u32 s80, s80, 524288
aw a179,v251
mm v[40:43],v[208:211],v[136:139],v[40:43]
rt v[200:201],v241 offset:42368
rt v[202:203],v241 offset:43392
mm v[72:75],v[208:211],v[144:147],v[72:75]
s_cmp_eq_u32 s74, 3
s_cselect_b32 s23, 3, 0
s_cselect_b32 s22, 0, s75
mm v[72:75],v[212:215],a[120:123],v[72:75]
ld a[100:103],v247,s[52:55],s91 offen offset:0 sc1
mm v[104:107],v[212:215],a[124:127],v[104:107]
s_lshl_b32 s95, s76, 5
s_add_u32 s95, s95, s3
mm v[104:107],v[208:211],v[148:151],v[104:107]
rt v[212:213],v242 offset:33920
rt v[214:215],v242 offset:34944
mm v[108:111],v[216:219],v[148:151],v[108:111]
s_lshl_b32 s95, s95, 5
s_add_u32 s95, s95, s72
mm v[108:111],v[220:223],a[124:127],v[108:111]
s_lshl_b32 s95, s95, 3
s_add_u32 s6, s95, 1048576
mm v[76:79],v[220:223],a[120:123],v[76:79]
s_mov_b32 s18, 36864
v_and_b32 v239, 0x70, v255
mm v[76:79],v[216:219],v[144:147],v[76:79]
rt v[208:209],v242 offset:42368
rt v[210:211],v242 offset:43392
mm v[44:47],v[216:219],v[136:139],v[44:47]
ds_write_b128 v238, v[248:251] offset:39936
mm v[44:47],v[220:223],v[156:159],v[44:47]
st a[164:167],v255,s[48:51],s18 offen offset:0 sc1
mm v[12:15],v[220:223],v[152:155],v[12:15]
wt lgkmcnt(5)
v_lshlrev_b32 v239, 6, v239
mm v[12:15],v[216:219],v[132:135],v[12:15]
rt v[220:221],v243 offset:33920
rt v[222:223],v243 offset:34944
mm v[16:19],v[196:199],v[132:135],v[16:19]
ar v224,a200
)ASM" R"ASM(ar v225,a201
mm v[16:19],v[192:195],v[152:155],v[16:19]
ld a[104:107],v244,s[52:55],s91 offen offset:1024 sc1
mm v[48:51],v[192:195],v[156:159],v[48:51]
ar v226,a202
ar v227,a203
mm v[48:51],v[196:199],v[136:139],v[48:51]
rt v[216:217],v243 offset:42368
rt v[218:219],v243 offset:43392
mm v[80:83],v[196:199],v[144:147],v[80:83]
ar v228,a204
ar v229,a205
mm v[80:83],v[192:195],a[120:123],v[80:83]
ld a[108:111],v245,s[52:55],s91 offen offset:1024 sc1
mm v[112:115],v[192:195],a[124:127],v[112:115]
ar v230,a206
ar v231,a207
mm v[112:115],v[196:199],v[148:151],v[112:115]
cv v248,v224,v225
cv v249,v226,v227
mm v[116:119],v[204:207],v[148:151],v[116:119]
wt lgkmcnt(0)
s_barrier
mm v[116:119],v[200:203],a[124:127],v[116:119]
ld a[112:115],v246,s[52:55],s91 offen offset:1024 sc1
mm v[84:87],v[200:203],a[120:123],v[84:87]
rt v[192:193],v240 offset:50688
rt v[194:195],v240 offset:51712
mm v[84:87],v[204:207],v[144:147],v[84:87]
rt v[196:197],v240 offset:59136
rt v[198:199],v240 offset:60160
mm v[52:55],v[204:207],v[136:139],v[52:55]
s_add_u32 m0, s83, 33792
cv v250,v228,v229
mm v[52:55],v[200:203],v[156:159],v[52:55]
ld v232,s[44:47],s80 offen lds
mm v[20:23],v[200:203],v[152:155],v[20:23]
cv v251,v230,v231
aw a180,v248
mm v[20:23],v[204:207],v[132:135],v[20:23]
rt v[200:201],v241 offset:50688
rt v[202:203],v241 offset:51712
mm v[24:27],v[212:215],v[132:135],v[24:27]
aw a181,v249
aw a182,v250
mm v[24:27],v[208:211],v[152:155],v[24:27]
ld a[116:119],v247,s[52:55],s91 offen offset:1024 sc1
mm v[56:59],v[208:211],v[156:159],v[56:59]
aw a183,v251
s_add_u32 s85, s85, 65536
mm v[56:59],v[212:215],v[136:139],v[56:59]
rt v[204:205],v241 offset:59136
rt v[206:207],v241 offset:60160
mm v[88:91],v[212:215],v[144:147],v[88:91]
s_add_u32 m0, s83, 34816
v_add_u32 v238, s85, v255
mm v[88:91],v[208:211],a[120:123],v[88:91]
ld v233,s[44:47],s80 offen lds
mm v[120:123],v[208:211],a[124:127],v[120:123]
s_mov_b32 s85, 0
aw a231,v239
mm v[120:123],v[212:215],v[148:151],v[120:123]
rt v[208:209],v242 offset:50688
rt v[210:211],v242 offset:51712
mm v[124:127],v[220:223],v[148:151],v[124:127]
s_mul_i32 s95, s75, 0
s_add_u32 s95, s95, 2
mm v[124:127],v[216:219],a[124:127],v[124:127]
s_sub_u32 s96, 4, s95
s_lshr_b32 s97, s2, 4
mm v[92:95],v[216:219],a[120:123],v[92:95]
s_mul_i32 s97, s97, s95
v_lshrrev_b32 v239, 7, v255
mm v[92:95],v[220:223],v[144:147],v[92:95]
rt v[212:213],v242 offset:59136
rt v[214:215],v242 offset:60160
mm v[60:63],v[220:223],v[136:139],v[60:63]
ds_write_b128 v238, v[248:251] offset:0
mm v[60:63],v[216:219],v[156:159],v[60:63]
st a[168:171],v255,s[48:51],s18 offen offset:1024 sc1
mm v[28:31],v[216:219],v[152:155],v[28:31]
wt vmcnt(15) lgkmcnt(5)
v_add_u32 v254, s97, v239
mm v[28:31],v[220:223],v[132:135],v[28:31]
rt v[216:217],v243 offset:50688
rt v[218:219],v243 offset:51712
mm v[0:3],v[192:195],v[160:163],v[0:3]
ar v224,a184
ar v225,a185
mm v[0:3],v[196:199],v[176:179],v[0:3]
ld v[132:135],v244,s[52:55],s91 offen offset:2048 sc1
mm v[32:35],v[196:199],v[180:183],v[32:35]
ar v226,a186
ar v227,a187
mm v[32:35],v[192:195],v[164:167],v[32:35]
rt v[220:221],v243 offset:59136
rt v[222:223],v243 offset:60160
mm v[64:67],v[192:195],v[168:171],v[64:67]
ar v228,a188
ar v229,a189
)ASM" R"ASM(mm v[64:67],v[196:199],v[184:187],v[64:67]
ld v[136:139],v245,s[52:55],s91 offen offset:2048 sc1
mm v[96:99],v[196:199],v[188:191],v[96:99]
ar v230,a190
ar v231,a191
mm v[96:99],v[192:195],v[172:175],v[96:99]
rt v[196:197],v240 offset:50816
rt v[198:199],v240 offset:51840
mm v[100:103],v[200:203],v[172:175],v[100:103]
s_add_u32 m0, s83, 35968
cv v248,v224,v225
mm v[100:103],v[204:207],v[188:191],v[100:103]
ld v234,s[44:47],s80 offen lds
mm v[68:71],v[204:207],v[184:187],v[68:71]
cv v249,v226,v227
cv v250,v228,v229
mm v[68:71],v[200:203],v[168:171],v[68:71]
rt v[192:193],v240 offset:59264
rt v[194:195],v240 offset:60288
mm v[36:39],v[200:203],v[164:167],v[36:39]
cv v251,v230,v231
aw a184,v248
mm v[36:39],v[204:207],v[180:183],v[36:39]
ld v[144:147],v246,s[52:55],s91 offen offset:2048 sc1
mm v[4:7],v[204:207],v[176:179],v[4:7]
wt lgkmcnt(4)
aw a185,v249
mm v[4:7],v[200:203],v[160:163],v[4:7]
rt v[204:205],v241 offset:50816
rt v[206:207],v241 offset:51840
mm v[8:11],v[208:211],v[160:163],v[8:11]
s_add_u32 m0, s83, 36992
aw a186,v250
mm v[8:11],v[212:215],v[176:179],v[8:11]
ld v235,s[44:47],s80 offen lds
mm v[40:43],v[212:215],v[180:183],v[40:43]
s_add_u32 s80, s80, 524288
aw a187,v251
mm v[40:43],v[208:211],v[164:167],v[40:43]
rt v[200:201],v241 offset:59264
rt v[202:203],v241 offset:60288
mm v[72:75],v[208:211],v[168:171],v[72:75]
v_mul_u32_u24 v254, s21, v254
v_add_u32 v239, s96, v239
mm v[72:75],v[212:215],v[184:187],v[72:75]
ld v[148:151],v247,s[52:55],s91 offen offset:2048 sc1
mm v[104:107],v[212:215],v[188:191],v[104:107]
v_lshrrev_b32 v239, 2, v239
v_lshl_add_u32 v254, v239, 28, v254
mm v[104:107],v[208:211],v[172:175],v[104:107]
rt v[212:213],v242 offset:50816
rt v[214:215],v242 offset:51840
mm v[108:111],v[216:219],v[172:175],v[108:111]
v_and_b32 v239, 0x70, v255
v_lshl_add_u32 v254, v239, 3, v254
mm v[108:111],v[220:223],v[188:191],v[108:111]
s_lshl_b32 s96, s7, 5
s_sub_u32 s96, 16, s96
mm v[76:79],v[220:223],v[184:187],v[76:79]
s_mul_i32 s95, s95, s96
s_mul_i32 s21, s21, s95
mm v[76:79],v[216:219],v[168:171],v[76:79]
rt v[208:209],v242 offset:59264
rt v[210:211],v242 offset:60288
mm v[44:47],v[216:219],v[164:167],v[44:47]
ds_write_b128 v238, v[248:251] offset:1024
mm v[44:47],v[220:223],v[180:183],v[44:47]
st a[172:175],v255,s[48:51],s18 offen offset:2048 sc1
mm v[12:15],v[220:223],v[176:179],v[12:15]
wt lgkmcnt(5)
v_lshrrev_b32 v236, 8, v255
mm v[12:15],v[216:219],v[160:163],v[12:15]
rt v[220:221],v243 offset:50816
rt v[222:223],v243 offset:51840
mm v[16:19],v[196:199],v[160:163],v[16:19]
ar v224,a64
ar v225,a65
mm v[16:19],v[192:195],v[176:179],v[16:19]
ld v[152:155],v244,s[52:55],s91 offen offset:3072 sc1
mm v[48:51],v[192:195],v[180:183],v[48:51]
ar v226,a66
ar v227,a67
mm v[48:51],v[196:199],v[164:167],v[48:51]
rt v[216:217],v243 offset:59264
rt v[218:219],v243 offset:60288
mm v[80:83],v[196:199],v[168:171],v[80:83]
ar v228,a68
ar v229,a69
mm v[80:83],v[192:195],v[184:187],v[80:83]
ld v[156:159],v245,s[52:55],s91 offen offset:3072 sc1
mm v[112:115],v[192:195],v[188:191],v[112:115]
ar v230,a70
ar v231,a71
mm v[112:115],v[196:199],v[172:175],v[112:115]
cv v248,v224,v225
cv v249,v226,v227
mm v[116:119],v[204:207],v[172:175],v[116:119]
wt lgkmcnt(0)
s_barrier
mm v[116:119],v[200:203],v[188:191],v[116:119]
)ASM" R"ASM(ld a[120:123],v246,s[52:55],s91 offen offset:3072 sc1
mm v[84:87],v[200:203],v[184:187],v[84:87]
rt v[192:193],v240 offset:0
rt v[194:195],v240 offset:1024
mm v[84:87],v[204:207],v[168:171],v[84:87]
rt v[196:197],v240 offset:8448
rt v[198:199],v240 offset:9472
mm v[52:55],v[204:207],v[164:167],v[52:55]
s_add_u32 m0, s83, 50688
cv v250,v228,v229
mm v[52:55],v[200:203],v[180:183],v[52:55]
ld v232,s[44:47],s80 offen lds
mm v[20:23],v[200:203],v[176:179],v[20:23]
cv v251,v230,v231
aw a188,v248
mm v[20:23],v[204:207],v[160:163],v[20:23]
rt v[200:201],v241 offset:0
rt v[202:203],v241 offset:1024
mm v[24:27],v[212:215],v[160:163],v[24:27]
aw a189,v249
aw a190,v250
mm v[24:27],v[208:211],v[176:179],v[24:27]
ld a[124:127],v247,s[52:55],s91 offen offset:3072 sc1
mm v[56:59],v[208:211],v[180:183],v[56:59]
s_add_u32 s91, s91, 4096
aw a191,v251
mm v[56:59],v[212:215],v[164:167],v[56:59]
rt v[204:205],v241 offset:8448
rt v[206:207],v241 offset:9472
mm v[88:91],v[212:215],v[168:171],v[88:91]
s_add_u32 m0, s83, 51712
v_lshlrev_b32 v239, 5, v236
mm v[88:91],v[208:211],v[184:187],v[88:91]
ld v233,s[44:47],s80 offen lds
mm v[120:123],v[208:211],v[188:191],v[120:123]
v_xor_b32 v239, v239, v255
v_and_b32 v239, 0xf0, v239
mm v[120:123],v[212:215],v[172:175],v[120:123]
rt v[208:209],v242 offset:0
rt v[210:211],v242 offset:1024
mm v[124:127],v[220:223],v[172:175],v[124:127]
v_lshl_add_u32 v239, v236, 11, v239
s_lshl_b32 s92, s77, 16
mm v[124:127],v[216:219],v[188:191],v[124:127]
s_lshl_b32 s93, s78, 14
s_add_u32 s92, s92, s93
mm v[92:95],v[216:219],v[184:187],v[92:95]
v_add_u32 v239, s92, v239
aw a228,v239
mm v[92:95],v[220:223],v[168:171],v[92:95]
rt v[212:213],v242 offset:8448
rt v[214:215],v242 offset:9472
mm v[60:63],v[220:223],v[164:167],v[60:63]
ds_write_b128 v238, v[248:251] offset:2048
mm v[60:63],v[216:219],v[180:183],v[60:63]
st a[176:179],v255,s[48:51],s18 offen offset:3072 sc1
mm v[28:31],v[216:219],v[176:179],v[28:31]
wt vmcnt(16) lgkmcnt(5)
v_bfe_u32 v239, v255, 6, 2
mm v[28:31],v[220:223],v[160:163],v[28:31]
rt v[216:217],v243 offset:0
rt v[218:219],v243 offset:1024
mm v[0:3],v[192:195],v[128:131],v[0:3]
ar v224,a0
ar v225,a1
mm v[0:3],v[196:199],a[104:107],v[0:3]
ld v[160:163],v244,s[52:55],s91 offen offset:0 sc1
mm v[32:35],v[196:199],a[108:111],v[32:35]
ar v226,a2
ar v227,a3
mm v[32:35],v[192:195],v[140:143],v[32:35]
rt v[220:221],v243 offset:8448
rt v[222:223],v243 offset:9472
mm v[64:67],v[192:195],a[96:99],v[64:67]
ar v228,a4
ar v229,a5
mm v[64:67],v[196:199],a[112:115],v[64:67]
ld v[164:167],v245,s[52:55],s91 offen offset:0 sc1
mm v[96:99],v[196:199],a[116:119],v[96:99]
ar v230,a6
ar v231,a7
mm v[96:99],v[192:195],a[100:103],v[96:99]
rt v[196:197],v240 offset:128
rt v[198:199],v240 offset:1152
mm v[100:103],v[200:203],a[100:103],v[100:103]
s_add_u32 m0, s83, 52864
cv v248,v224,v225
mm v[100:103],v[204:207],a[116:119],v[100:103]
ld v234,s[44:47],s80 offen lds
mm v[68:71],v[204:207],a[112:115],v[68:71]
cv v249,v226,v227
cv v250,v228,v229
mm v[68:71],v[200:203],a[96:99],v[68:71]
rt v[192:193],v240 offset:8576
rt v[194:195],v240 offset:9600
mm v[36:39],v[200:203],v[140:143],v[36:39]
cv v251,v230,v231
aw a0,v248
mm v[36:39],v[204:207],a[108:111],v[36:39]
ld v[168:171],v246,s[52:55],s91 offen offset:0 sc1
)ASM" R"ASM(mm v[4:7],v[204:207],a[104:107],v[4:7]
wt lgkmcnt(4)
aw a1,v249
mm v[4:7],v[200:203],v[128:131],v[4:7]
rt v[204:205],v241 offset:128
rt v[206:207],v241 offset:1152
mm v[8:11],v[208:211],v[128:131],v[8:11]
s_add_u32 m0, s83, 53888
aw a2,v250
mm v[8:11],v[212:215],a[104:107],v[8:11]
ld v235,s[44:47],s80 offen lds
mm v[40:43],v[212:215],a[108:111],v[40:43]
s_add_u32 s80, s80, 524288
aw a3,v251
mm v[40:43],v[208:211],v[140:143],v[40:43]
rt v[200:201],v241 offset:8576
rt v[202:203],v241 offset:9600
mm v[72:75],v[208:211],a[96:99],v[72:75]
v_lshlrev_b32 v239, 6, v239
v_lshl_add_u32 v239, v236, 4, v239
mm v[72:75],v[212:215],a[112:115],v[72:75]
ld v[172:175],v247,s[52:55],s91 offen offset:0 sc1
mm v[104:107],v[212:215],a[116:119],v[104:107]
s_lshl_b32 s92, s78, 8
v_add_u32 v239, s92, v239
mm v[104:107],v[208:211],a[100:103],v[104:107]
rt v[212:213],v242 offset:128
rt v[214:215],v242 offset:1152
mm v[108:111],v[216:219],a[100:103],v[108:111]
v_mul_u32_u24 v239, 7168, v239
v_and_or_b32 v239, v255, 48, v239
.La5_LW1_12:
s_cbranch_execz .La5_LW1_R0
mm v[108:111],v[220:223],a[116:119],v[108:111]
aw a229,v239
v_and_b32 v239, 0xf0, v255
mm v[76:79],v[220:223],a[112:115],v[76:79]
s_mov_b32 s18, 16384
v_lshl_add_u32 v239, v236, 14, v239
mm v[76:79],v[216:219],a[96:99],v[76:79]
rt v[208:209],v242 offset:8576
rt v[210:211],v242 offset:9600
mm v[44:47],v[216:219],v[140:143],v[44:47]
ds_write_b128 v238, v[248:251] offset:3072
mm v[44:47],v[220:223],a[108:111],v[44:47]
st a[180:183],v255,s[48:51],s18 offen offset:0 sc1
mm v[12:15],v[220:223],a[104:107],v[12:15]
wt lgkmcnt(5)
s_lshl_b32 s92, s3, 9
mm v[12:15],v[216:219],v[128:131],v[12:15]
rt v[220:221],v243 offset:128
rt v[222:223],v243 offset:1152
mm v[16:19],v[196:199],v[128:131],v[16:19]
ar v224,a72
ar v225,a73
mm v[16:19],v[192:195],a[104:107],v[16:19]
ld v[176:179],v244,s[52:55],s91 offen offset:1024 sc1
mm v[48:51],v[192:195],a[108:111],v[48:51]
ar v226,a74
ar v227,a75
mm v[48:51],v[196:199],v[140:143],v[48:51]
rt v[216:217],v243 offset:8576
rt v[218:219],v243 offset:9600
mm v[80:83],v[196:199],a[96:99],v[80:83]
ar v228,a76
ar v229,a77
mm v[80:83],v[192:195],a[112:115],v[80:83]
ld v[180:183],v245,s[52:55],s91 offen offset:1024 sc1
mm v[112:115],v[192:195],a[116:119],v[112:115]
ar v230,a78
ar v231,a79
mm v[112:115],v[196:199],a[100:103],v[112:115]
cv v248,v224,v225
cv v249,v226,v227
mm v[116:119],v[204:207],a[100:103],v[116:119]
wt lgkmcnt(0)
s_barrier
mm v[116:119],v[200:203],a[116:119],v[116:119]
ld v[184:187],v246,s[52:55],s91 offen offset:1024 sc1
mm v[84:87],v[200:203],a[112:115],v[84:87]
rt v[192:193],v240 offset:16896
rt v[194:195],v240 offset:17920
mm v[84:87],v[204:207],a[96:99],v[84:87]
rt v[196:197],v240 offset:25344
rt v[198:199],v240 offset:26368
mm v[52:55],v[204:207],v[140:143],v[52:55]
s_add_u32 m0, s83, 0
cv v250,v228,v229
mm v[52:55],v[200:203],a[108:111],v[52:55]
ld v232,s[44:47],s80 offen lds
mm v[20:23],v[200:203],a[104:107],v[20:23]
cv v251,v230,v231
aw a4,v248
mm v[20:23],v[204:207],v[128:131],v[20:23]
rt v[200:201],v241 offset:16896
rt v[202:203],v241 offset:17920
mm v[24:27],v[212:215],v[128:131],v[24:27]
aw a5,v249
aw a6,v250
mm v[24:27],v[208:211],a[104:107],v[24:27]
)ASM" R"ASM(ld v[188:191],v247,s[52:55],s91 offen offset:1024 sc1
mm v[56:59],v[208:211],a[108:111],v[56:59]
aw a7,v251
v_add_u32 v239, s92, v239
mm v[56:59],v[212:215],v[140:143],v[56:59]
rt v[204:205],v241 offset:25344
rt v[206:207],v241 offset:26368
mm v[88:91],v[212:215],a[96:99],v[88:91]
s_add_u32 m0, s83, 1024
aw a230,v239
mm v[88:91],v[208:211],a[112:115],v[88:91]
ld v233,s[44:47],s80 offen lds
mm v[120:123],v[208:211],a[116:119],v[120:123]
v_add_u32 v239, 4294886912, v240
aw a224,v239
mm v[120:123],v[212:215],a[100:103],v[120:123]
rt v[208:209],v242 offset:16896
rt v[210:211],v242 offset:17920
mm v[124:127],v[220:223],a[100:103],v[124:127]
v_bfe_u32 v239, v255, 4, 4
v_lshlrev_b32 v236, 2, v236
mm v[124:127],v[216:219],a[116:119],v[124:127]
v_sub_u32 v239, v239, v236
v_subrev_u32 v236, 0, v239
mm v[92:95],v[216:219],a[112:115],v[92:95]
v_ashrrev_i32 v236, 31, v236
v_and_b32 v236, 0xff800000, v236
mm v[92:95],v[220:223],a[96:99],v[92:95]
rt v[212:213],v242 offset:25344
rt v[214:215],v242 offset:26368
mm v[60:63],v[220:223],v[140:143],v[60:63]
ds_write_b128 v238, v[248:251] offset:4096
mm v[60:63],v[216:219],a[108:111],v[60:63]
aw a236,v236
v_subrev_u32 v236, 1, v239
mm v[28:31],v[216:219],a[104:107],v[28:31]
wt vmcnt(15) lgkmcnt(5)
v_ashrrev_i32 v236, 31, v236
mm v[28:31],v[220:223],v[128:131],v[28:31]
rt v[216:217],v243 offset:16896
rt v[218:219],v243 offset:17920
mm v[0:3],v[192:195],v[132:135],v[0:3]
ar v224,a8
ar v225,a9
mm v[0:3],v[196:199],v[152:155],v[0:3]
ld v[128:131],v244,s[52:55],s91 offen offset:2048 sc1
mm v[32:35],v[196:199],v[156:159],v[32:35]
ar v226,a10
ar v227,a11
mm v[32:35],v[192:195],v[136:139],v[32:35]
rt v[220:221],v243 offset:25344
rt v[222:223],v243 offset:26368
mm v[64:67],v[192:195],v[144:147],v[64:67]
ar v228,a12
ar v229,a13
mm v[64:67],v[196:199],a[120:123],v[64:67]
ld v[140:143],v245,s[52:55],s91 offen offset:2048 sc1
mm v[96:99],v[196:199],a[124:127],v[96:99]
ar v230,a14
ar v231,a15
mm v[96:99],v[192:195],v[148:151],v[96:99]
rt v[196:197],v240 offset:17024
rt v[198:199],v240 offset:18048
mm v[100:103],v[200:203],v[148:151],v[100:103]
s_add_u32 m0, s83, 2176
cv v248,v224,v225
mm v[100:103],v[204:207],a[124:127],v[100:103]
ld v234,s[44:47],s80 offen lds
mm v[68:71],v[204:207],a[120:123],v[68:71]
cv v249,v226,v227
cv v250,v228,v229
mm v[68:71],v[200:203],v[144:147],v[68:71]
rt v[192:193],v240 offset:25472
rt v[194:195],v240 offset:26496
mm v[36:39],v[200:203],v[136:139],v[36:39]
cv v251,v230,v231
aw a8,v248
mm v[36:39],v[204:207],v[156:159],v[36:39]
ld a[96:99],v246,s[52:55],s91 offen offset:2048 sc1
mm v[4:7],v[204:207],v[152:155],v[4:7]
wt lgkmcnt(4)
aw a9,v249
mm v[4:7],v[200:203],v[132:135],v[4:7]
rt v[204:205],v241 offset:17024
rt v[206:207],v241 offset:18048
mm v[8:11],v[208:211],v[132:135],v[8:11]
s_add_u32 m0, s83, 3200
aw a10,v250
mm v[8:11],v[212:215],v[152:155],v[8:11]
ld v235,s[44:47],s80 offen lds
mm v[40:43],v[212:215],v[156:159],v[40:43]
s_add_u32 s80, s80, 524288
aw a11,v251
mm v[40:43],v[208:211],v[136:139],v[40:43]
rt v[200:201],v241 offset:25472
rt v[202:203],v241 offset:26496
mm v[72:75],v[208:211],v[144:147],v[72:75]
v_and_b32 v236, 0xff800000, v236
aw a237,v236
mm v[72:75],v[212:215],a[120:123],v[72:75]
)ASM" R"ASM(ld a[100:103],v247,s[52:55],s91 offen offset:2048 sc1
mm v[104:107],v[212:215],a[124:127],v[104:107]
v_subrev_u32 v236, 2, v239
v_ashrrev_i32 v236, 31, v236
mm v[104:107],v[208:211],v[148:151],v[104:107]
rt v[212:213],v242 offset:17024
rt v[214:215],v242 offset:18048
mm v[108:111],v[216:219],v[148:151],v[108:111]
v_and_b32 v236, 0xff800000, v236
aw a238,v236
mm v[108:111],v[220:223],a[124:127],v[108:111]
v_subrev_u32 v236, 3, v239
v_ashrrev_i32 v236, 31, v236
mm v[76:79],v[220:223],a[120:123],v[76:79]
v_and_b32 v236, 0xff800000, v236
aw a239,v236
mm v[76:79],v[216:219],v[144:147],v[76:79]
rt v[208:209],v242 offset:25472
rt v[210:211],v242 offset:26496
mm v[44:47],v[216:219],v[136:139],v[44:47]
ds_write_b128 v238, v[248:251] offset:5120
mm v[44:47],v[220:223],v[156:159],v[44:47]
st a[184:187],v255,s[48:51],s18 offen offset:1024 sc1
mm v[12:15],v[220:223],v[152:155],v[12:15]
wt lgkmcnt(5)
v_lshlrev_b32 v236, 4, v239
mm v[12:15],v[216:219],v[132:135],v[12:15]
rt v[220:221],v243 offset:17024
rt v[222:223],v243 offset:18048
mm v[16:19],v[196:199],v[132:135],v[16:19]
ar v224,a80
ar v225,a81
mm v[16:19],v[192:195],v[152:155],v[16:19]
ld a[104:107],v244,s[52:55],s91 offen offset:3072 sc1
mm v[48:51],v[192:195],v[156:159],v[48:51]
ar v226,a82
ar v227,a83
mm v[48:51],v[196:199],v[136:139],v[48:51]
rt v[216:217],v243 offset:25472
rt v[218:219],v243 offset:26496
mm v[80:83],v[196:199],v[144:147],v[80:83]
ar v228,a84
ar v229,a85
mm v[80:83],v[192:195],a[120:123],v[80:83]
ld a[108:111],v245,s[52:55],s91 offen offset:3072 sc1
mm v[112:115],v[192:195],a[124:127],v[112:115]
ar v230,a86
ar v231,a87
mm v[112:115],v[196:199],v[148:151],v[112:115]
cv v248,v224,v225
cv v249,v226,v227
mm v[116:119],v[204:207],v[148:151],v[116:119]
wt lgkmcnt(0)
s_barrier
mm v[116:119],v[200:203],a[124:127],v[116:119]
ld a[112:115],v246,s[52:55],s91 offen offset:3072 sc1
mm v[84:87],v[200:203],a[120:123],v[84:87]
rt v[192:193],v240 offset:33792
rt v[194:195],v240 offset:34816
mm v[84:87],v[204:207],v[144:147],v[84:87]
rt v[196:197],v240 offset:42240
rt v[198:199],v240 offset:43264
mm v[52:55],v[204:207],v[136:139],v[52:55]
s_add_u32 m0, s83, 16896
cv v250,v228,v229
mm v[52:55],v[200:203],v[156:159],v[52:55]
ld v232,s[44:47],s80 offen lds
mm v[20:23],v[200:203],v[152:155],v[20:23]
cv v251,v230,v231
aw a12,v248
mm v[20:23],v[204:207],v[132:135],v[20:23]
rt v[200:201],v241 offset:33792
rt v[202:203],v241 offset:34816
mm v[24:27],v[212:215],v[132:135],v[24:27]
aw a13,v249
aw a14,v250
mm v[24:27],v[208:211],v[152:155],v[24:27]
ld a[116:119],v247,s[52:55],s91 offen offset:3072 sc1
mm v[56:59],v[208:211],v[156:159],v[56:59]
s_add_u32 s91, s91, 4096
aw a15,v251
mm v[56:59],v[212:215],v[136:139],v[56:59]
rt v[204:205],v241 offset:42240
rt v[206:207],v241 offset:43264
mm v[88:91],v[212:215],v[144:147],v[88:91]
s_add_u32 m0, s83, 17920
s_mov_b32 s92, 0x3f80
mm v[88:91],v[208:211],a[120:123],v[88:91]
ld v233,s[44:47],s80 offen lds
mm v[120:123],v[208:211],a[124:127],v[120:123]
v_lshlrev_b32 v236, v236, s92
v_cmp_gt_u32 vcc, 2, v239
mm v[120:123],v[212:215],v[148:151],v[120:123]
rt v[208:209],v242 offset:33792
rt v[210:211],v242 offset:34816
mm v[124:127],v[220:223],v[148:151],v[124:127]
v_subrev_u32 v239, 2, v239
v_cmp_gt_u32 s[92:93], 2, v239
mm v[124:127],v[216:219],a[124:127],v[124:127]
v_cndmask_b32 v239, 0, v236, vcc
)ASM" R"ASM(aw a212,0
mm v[92:95],v[216:219],a[120:123],v[92:95]
aw a214,v239
v_cndmask_b32 v239, 0, v236, s[92:93]
mm v[92:95],v[220:223],v[144:147],v[92:95]
rt v[212:213],v242 offset:42240
rt v[214:215],v242 offset:43264
mm v[60:63],v[220:223],v[136:139],v[60:63]
v_mov_b32 v236, 0x3f803f80
aw a215,v239
mm v[60:63],v[216:219],v[156:159],v[60:63]
st a[188:191],v255,s[48:51],s18 offen offset:2048 sc1
mm v[28:31],v[216:219],v[152:155],v[28:31]
wt vmcnt(15) lgkmcnt(4)
aw a213,0
mm v[28:31],v[220:223],v[132:135],v[28:31]
rt v[216:217],v243 offset:33792
rt v[218:219],v243 offset:34816
mm v[0:3],v[192:195],v[160:163],v[0:3]
ar v224,a16
ar v225,a17
mm v[0:3],v[196:199],v[176:179],v[0:3]
ld v[132:135],v244,s[52:55],s91 offen offset:0 sc1
mm v[32:35],v[196:199],v[180:183],v[32:35]
ar v226,a18
ar v227,a19
mm v[32:35],v[192:195],v[164:167],v[32:35]
rt v[220:221],v243 offset:42240
rt v[222:223],v243 offset:43264
mm v[64:67],v[192:195],v[168:171],v[64:67]
ar v228,a20
ar v229,a21
mm v[64:67],v[196:199],v[184:187],v[64:67]
ld v[136:139],v245,s[52:55],s91 offen offset:0 sc1
mm v[96:99],v[196:199],v[188:191],v[96:99]
ar v230,a22
ar v231,a23
mm v[96:99],v[192:195],v[172:175],v[96:99]
rt v[196:197],v240 offset:33920
rt v[198:199],v240 offset:34944
mm v[100:103],v[200:203],v[172:175],v[100:103]
s_add_u32 m0, s83, 19072
cv v248,v224,v225
mm v[100:103],v[204:207],v[188:191],v[100:103]
ld v234,s[44:47],s80 offen lds
mm v[68:71],v[204:207],v[184:187],v[68:71]
cv v249,v226,v227
cv v250,v228,v229
mm v[68:71],v[200:203],v[168:171],v[68:71]
rt v[192:193],v240 offset:42368
rt v[194:195],v240 offset:43392
mm v[36:39],v[200:203],v[164:167],v[36:39]
cv v251,v230,v231
aw a16,v248
mm v[36:39],v[204:207],v[180:183],v[36:39]
ld v[144:147],v246,s[52:55],s91 offen offset:0 sc1
mm v[4:7],v[204:207],v[176:179],v[4:7]
wt lgkmcnt(4)
aw a17,v249
mm v[4:7],v[200:203],v[160:163],v[4:7]
rt v[204:205],v241 offset:33920
rt v[206:207],v241 offset:34944
mm v[8:11],v[208:211],v[160:163],v[8:11]
s_add_u32 m0, s83, 20096
aw a18,v250
mm v[8:11],v[212:215],v[176:179],v[8:11]
ld v235,s[44:47],s80 offen lds
mm v[40:43],v[212:215],v[180:183],v[40:43]
s_add_u32 s80, s80, 524288
aw a19,v251
mm v[40:43],v[208:211],v[164:167],v[40:43]
rt v[200:201],v241 offset:42368
rt v[202:203],v241 offset:43392
mm v[72:75],v[208:211],v[168:171],v[72:75]
aw a216,0
aw a217,0
mm v[72:75],v[212:215],v[184:187],v[72:75]
ld v[148:151],v247,s[52:55],s91 offen offset:0 sc1
mm v[104:107],v[212:215],v[188:191],v[104:107]
v_lshrrev_b32 v239, 4, v255
v_xor_b32 v239, v239, v255
mm v[104:107],v[208:211],v[172:175],v[104:107]
rt v[212:213],v242 offset:33920
rt v[214:215],v242 offset:34944
mm v[108:111],v[216:219],v[172:175],v[108:111]
v_and_b32 v239, 32, v239
v_cmp_eq_u32 vcc, 0, v239
mm v[108:111],v[220:223],v[188:191],v[108:111]
s_mov_b32 s78, 4294950911
s_mul_i32 s70, s70, s22
mm v[76:79],v[220:223],v[184:187],v[76:79]
v_cndmask_b32 v236, 0, v236, vcc
s_sub_u32 s92, 4294967294, s3
mm v[76:79],v[216:219],v[168:171],v[76:79]
rt v[208:209],v242 offset:42368
rt v[210:211],v242 offset:43392
mm v[44:47],v[216:219],v[164:167],v[44:47]
s_add_u32 s92, s92, s7
s_mul_i32 s92, s92, s21
mm v[44:47],v[220:223],v[180:183],v[44:47]
)ASM" R"ASM(st a[0:3],v255,s[48:51],s18 offen offset:3072 sc1
mm v[12:15],v[220:223],v[176:179],v[12:15]
wt lgkmcnt(4)
s_add_u32 s90, s90, s92
mm v[12:15],v[216:219],v[160:163],v[12:15]
rt v[220:221],v243 offset:33920
rt v[222:223],v243 offset:34944
mm v[16:19],v[196:199],v[160:163],v[16:19]
ar v224,a88
ar v225,a89
mm v[16:19],v[192:195],v[176:179],v[16:19]
ld v[152:155],v244,s[52:55],s91 offen offset:1024 sc1
mm v[48:51],v[192:195],v[180:183],v[48:51]
ar v226,a90
ar v227,a91
mm v[48:51],v[196:199],v[164:167],v[48:51]
rt v[216:217],v243 offset:42368
rt v[218:219],v243 offset:43392
mm v[80:83],v[196:199],v[168:171],v[80:83]
ar v228,a92
ar v229,a93
mm v[80:83],v[192:195],v[184:187],v[80:83]
ld v[156:159],v245,s[52:55],s91 offen offset:1024 sc1
mm v[112:115],v[192:195],v[188:191],v[112:115]
ar v230,a94
ar v231,a95
mm v[112:115],v[196:199],v[172:175],v[112:115]
cv v248,v224,v225
cv v249,v226,v227
mm v[116:119],v[204:207],v[172:175],v[116:119]
wt lgkmcnt(0)
s_barrier
mm v[116:119],v[200:203],v[188:191],v[116:119]
ld a[120:123],v246,s[52:55],s91 offen offset:1024 sc1
mm v[84:87],v[200:203],v[184:187],v[84:87]
rt v[192:193],v240 offset:50688
rt v[194:195],v240 offset:51712
mm v[84:87],v[204:207],v[168:171],v[84:87]
rt v[196:197],v240 offset:59136
rt v[198:199],v240 offset:60160
mm v[52:55],v[204:207],v[164:167],v[52:55]
s_add_u32 m0, s83, 33792
cv v250,v228,v229
mm v[52:55],v[200:203],v[180:183],v[52:55]
ld v232,s[44:47],s80 offen lds
mm v[20:23],v[200:203],v[176:179],v[20:23]
cv v251,v230,v231
aw a20,v248
mm v[20:23],v[204:207],v[160:163],v[20:23]
rt v[200:201],v241 offset:50688
rt v[202:203],v241 offset:51712
mm v[24:27],v[212:215],v[160:163],v[24:27]
aw a21,v249
aw a22,v250
mm v[24:27],v[208:211],v[176:179],v[24:27]
ld a[124:127],v247,s[52:55],s91 offen offset:1024 sc1
mm v[56:59],v[208:211],v[180:183],v[56:59]
aw a23,v251
s_mov_b32 s17, 1073741824
mm v[56:59],v[212:215],v[164:167],v[56:59]
rt v[204:205],v241 offset:59136
rt v[206:207],v241 offset:60160
mm v[88:91],v[212:215],v[168:171],v[88:91]
s_add_u32 m0, s83, 34816
s_lshl_b32 s77, s76, 16
mm v[88:91],v[208:211],v[184:187],v[88:91]
ld v233,s[44:47],s80 offen lds
mm v[120:123],v[208:211],v[188:191],v[120:123]
s_sub_u32 s77, s77, 8192
s_lshl4_add_u32 s81, s75, s74
mm v[120:123],v[212:215],v[172:175],v[120:123]
rt v[208:209],v242 offset:50688
rt v[210:211],v242 offset:51712
mm v[124:127],v[220:223],v[172:175],v[124:127]
s_lshl3_add_u32 s81, s81, 1
mm v[124:127],v[216:219],v[188:191],v[124:127]
s_cmp_eq_u32 s73, 0
s_cselect_b32 s16, s77, 0
mm v[92:95],v[216:219],v[184:187],v[92:95]
s_mov_b32 s18, 20480
s_mul_i32 s92, s75, 4294836224
mm v[92:95],v[220:223],v[168:171],v[92:95]
rt v[212:213],v242 offset:59136
rt v[214:215],v242 offset:60160
mm v[60:63],v[220:223],v[164:167],v[60:63]
s_add_u32 s92, s92, s88
s_add_u32 s19, s92, 393216
mm v[60:63],v[216:219],v[180:183],v[60:63]
st a[4:7],v255,s[48:51],s18 offen offset:0 sc1
mm v[28:31],v[216:219],v[176:179],v[28:31]
wt vmcnt(16) lgkmcnt(4)
aw a208,v236
mm v[28:31],v[220:223],v[160:163],v[28:31]
rt v[216:217],v243 offset:50688
rt v[218:219],v243 offset:51712
mm v[0:3],v[192:195],v[128:131],v[0:3]
ar v224,a24
ar v225,a25
mm v[0:3],v[196:199],a[104:107],v[0:3]
ld v[160:163],v244,s[52:55],s91 offen offset:2048 sc1
)ASM" R"ASM(mm v[32:35],v[196:199],a[108:111],v[32:35]
ar v226,a26
ar v227,a27
mm v[32:35],v[192:195],v[140:143],v[32:35]
rt v[220:221],v243 offset:59136
rt v[222:223],v243 offset:60160
mm v[64:67],v[192:195],a[96:99],v[64:67]
ar v228,a28
ar v229,a29
mm v[64:67],v[196:199],a[112:115],v[64:67]
ld v[164:167],v245,s[52:55],s91 offen offset:2048 sc1
mm v[96:99],v[196:199],a[116:119],v[96:99]
ar v230,a30
ar v231,a31
mm v[96:99],v[192:195],a[100:103],v[96:99]
rt v[196:197],v240 offset:50816
rt v[198:199],v240 offset:51840
mm v[100:103],v[200:203],a[100:103],v[100:103]
s_add_u32 m0, s83, 35968
cv v248,v224,v225
mm v[100:103],v[204:207],a[116:119],v[100:103]
ld v234,s[44:47],s80 offen lds
mm v[68:71],v[204:207],a[112:115],v[68:71]
cv v249,v226,v227
cv v250,v228,v229
mm v[68:71],v[200:203],a[96:99],v[68:71]
rt v[192:193],v240 offset:59264
rt v[194:195],v240 offset:60288
mm v[36:39],v[200:203],v[140:143],v[36:39]
cv v251,v230,v231
aw a24,v248
mm v[36:39],v[204:207],a[108:111],v[36:39]
ld v[168:171],v246,s[52:55],s91 offen offset:2048 sc1
mm v[4:7],v[204:207],a[104:107],v[4:7]
wt lgkmcnt(4)
aw a25,v249
mm v[4:7],v[200:203],v[128:131],v[4:7]
rt v[204:205],v241 offset:50816
rt v[206:207],v241 offset:51840
mm v[8:11],v[208:211],v[128:131],v[8:11]
s_add_u32 m0, s83, 36992
aw a26,v250
mm v[8:11],v[212:215],a[104:107],v[8:11]
ld v235,s[44:47],s80 offen lds
mm v[40:43],v[212:215],a[108:111],v[40:43]
s_add_u32 s80, s80, 524288
aw a27,v251
mm v[40:43],v[208:211],v[140:143],v[40:43]
rt v[200:201],v241 offset:59264
rt v[202:203],v241 offset:60288
mm v[72:75],v[208:211],a[96:99],v[72:75]
aw a209,v236
aw a210,v236
mm v[72:75],v[212:215],a[112:115],v[72:75]
ld v[172:175],v247,s[52:55],s91 offen offset:2048 sc1
mm v[104:107],v[212:215],a[116:119],v[104:107]
aw a211,v236
aw a232,0
mm v[104:107],v[208:211],a[100:103],v[104:107]
rt v[212:213],v242 offset:50816
rt v[214:215],v242 offset:51840
mm v[108:111],v[216:219],a[100:103],v[108:111]
aw a233,0
aw a234,0
mm v[108:111],v[220:223],a[116:119],v[108:111]
aw a235,0
s_lshl_b32 s20, s72, 10
mm v[76:79],v[220:223],a[112:115],v[76:79]
s_add_u32 s20, s20, 16384
.La5_LW1_13:
s_cbranch_execz .La5_LW1_R0
s_mov_b32 s4, 1466176288
mm v[76:79],v[216:219],a[96:99],v[76:79]
rt v[208:209],v242 offset:59264
rt v[210:211],v242 offset:60288
mm v[44:47],v[216:219],v[140:143],v[44:47]
s_cmp_ge_u32 s2, 64
s_cselect_b32 s4, 1182081808, s4
mm v[44:47],v[220:223],a[108:111],v[44:47]
st a[8:11],v255,s[48:51],s18 offen offset:1024 sc1
mm v[12:15],v[220:223],a[104:107],v[12:15]
wt lgkmcnt(4)
mm v[12:15],v[216:219],v[128:131],v[12:15]
rt v[220:221],v243 offset:50816
rt v[222:223],v243 offset:51840
mm v[16:19],v[196:199],v[128:131],v[16:19]
ar v224,a240
ar v225,a241
mm v[16:19],v[192:195],a[104:107],v[16:19]
ld v[176:179],v244,s[52:55],s91 offen offset:3072 sc1
mm v[48:51],v[192:195],a[108:111],v[48:51]
ar v226,a242
ar v227,a243
mm v[48:51],v[196:199],v[140:143],v[48:51]
rt v[216:217],v243 offset:59264
rt v[218:219],v243 offset:60288
mm v[80:83],v[196:199],a[96:99],v[80:83]
ar v228,a244
ar v229,a245
mm v[80:83],v[192:195],a[112:115],v[80:83]
)ASM" R"ASM(ld v[180:183],v245,s[52:55],s91 offen offset:3072 sc1
mm v[112:115],v[192:195],a[116:119],v[112:115]
ar v230,a246
ar v231,a247
mm v[112:115],v[196:199],a[100:103],v[112:115]
cv v248,v224,v225
cv v249,v226,v227
mm v[116:119],v[204:207],a[100:103],v[116:119]
wt lgkmcnt(0)
s_barrier
mm v[116:119],v[200:203],a[116:119],v[116:119]
ld v[184:187],v246,s[52:55],s91 offen offset:3072 sc1
mm v[84:87],v[200:203],a[112:115],v[84:87]
rt v[192:193],v240 offset:0
rt v[194:195],v240 offset:1024
mm v[84:87],v[204:207],a[96:99],v[84:87]
rt v[196:197],v240 offset:8448
rt v[198:199],v240 offset:9472
mm v[52:55],v[204:207],v[140:143],v[52:55]
s_add_u32 m0, s83, 50688
cv v250,v228,v229
mm v[52:55],v[200:203],a[108:111],v[52:55]
ld v232,s[44:47],s80 offen lds
mm v[20:23],v[200:203],a[104:107],v[20:23]
cv v251,v230,v231
aw a28,v248
mm v[20:23],v[204:207],v[128:131],v[20:23]
rt v[200:201],v241 offset:0
rt v[202:203],v241 offset:1024
mm v[24:27],v[212:215],v[128:131],v[24:27]
aw a29,v249
aw a30,v250
mm v[24:27],v[208:211],a[104:107],v[24:27]
ld v[188:191],v247,s[52:55],s91 offen offset:3072 sc1
mm v[56:59],v[208:211],a[108:111],v[56:59]
s_add_u32 s91, s91, 4096
aw a31,v251
mm v[56:59],v[212:215],v[140:143],v[56:59]
rt v[204:205],v241 offset:8448
rt v[206:207],v241 offset:9472
mm v[88:91],v[212:215],a[96:99],v[88:91]
s_add_u32 m0, s83, 51712
mm v[88:91],v[208:211],a[112:115],v[88:91]
ld v233,s[44:47],s80 offen lds
mm v[120:123],v[208:211],a[116:119],v[120:123]
s_cmp_ge_u32 s2, 128
s_cselect_b32 s4, 1967534368, s4
mm v[120:123],v[212:215],a[100:103],v[120:123]
rt v[208:209],v242 offset:0
rt v[210:211],v242 offset:1024
mm v[124:127],v[220:223],a[100:103],v[124:127]
s_cmp_ge_u32 s2, 192
s_cselect_b32 s4, 1683431728, s4
mm v[124:127],v[216:219],a[116:119],v[124:127]
v_bfe_u32 v236, v255, 4, 3
v_lshlrev_b32 v236, 2, v236
mm v[92:95],v[216:219],a[112:115],v[92:95]
v_lshrrev_b32 v236, v236, s4
v_and_b32 v236, 15, v236
mm v[92:95],v[220:223],a[96:99],v[92:95]
rt v[212:213],v242 offset:8448
rt v[214:215],v242 offset:9472
mm v[60:63],v[220:223],v[140:143],v[60:63]
v_bfe_u32 v239, v255, 7, 1
v_mul_u32_u24 v239, 28, v239
mm v[60:63],v[216:219],a[108:111],v[60:63]
v_xor_b32 v236, v236, v239
v_xor_b32 v236, s72, v236
mm v[28:31],v[216:219],a[104:107],v[28:31]
wt vmcnt(15) lgkmcnt(4)
v_lshlrev_b32 v236, 3, v236
mm v[28:31],v[220:223],v[128:131],v[28:31]
rt v[216:217],v243 offset:0
rt v[218:219],v243 offset:1024
mm v[0:3],v[192:195],v[132:135],v[0:3]
ar v224,a32
ar v225,a33
aw a225,v236
mm v[0:3],v[196:199],v[152:155],v[0:3]
ld v[128:131],v244,s[52:55],s91 offen offset:0 sc1
s_lshl3_add_u32 s5, s76, s101
mm v[32:35],v[196:199],v[156:159],v[32:35]
ar v226,a34
ar v227,a35
s_lshl2_add_u32 s5, s5, s3
mm v[32:35],v[192:195],v[136:139],v[32:35]
rt v[220:221],v243 offset:8448
rt v[222:223],v243 offset:9472
s_lshl_b32 s5, s5, 8
mm v[64:67],v[192:195],v[144:147],v[64:67]
ar v228,a36
ar v229,a37
s_add_u32 s5, s5, 1048576
mm v[64:67],v[196:199],a[120:123],v[64:67]
ld v[140:143],v245,s[52:55],s91 offen offset:0 sc1
mm v[96:99],v[196:199],a[124:127],v[96:99]
ar v230,a38
ar v231,a39
mm v[96:99],v[192:195],v[148:151],v[96:99]
rt v[196:197],v240 offset:128
)ASM" R"ASM(rt v[198:199],v240 offset:1152
mm v[100:103],v[200:203],v[148:151],v[100:103]
s_add_u32 m0, s83, 52864
cv v248,v224,v225
mm v[100:103],v[204:207],a[124:127],v[100:103]
ld v234,s[44:47],s80 offen lds
mm v[68:71],v[204:207],a[120:123],v[68:71]
cv v249,v226,v227
cv v250,v228,v229
mm v[68:71],v[200:203],v[144:147],v[68:71]
rt v[192:193],v240 offset:8576
rt v[194:195],v240 offset:9600
mm v[36:39],v[200:203],v[136:139],v[36:39]
cv v251,v230,v231
aw a32,v248
mm v[36:39],v[204:207],v[156:159],v[36:39]
ld a[192:195],v246,s[52:55],s91 offen offset:0 sc1
mm v[4:7],v[204:207],v[152:155],v[4:7]
wt lgkmcnt(4)
aw a33,v249
mm v[4:7],v[200:203],v[132:135],v[4:7]
rt v[204:205],v241 offset:128
rt v[206:207],v241 offset:1152
mm v[8:11],v[208:211],v[132:135],v[8:11]
s_add_u32 m0, s83, 53888
aw a34,v250
mm v[8:11],v[212:215],v[152:155],v[8:11]
ld v235,s[44:47],s80 offen lds
mm v[40:43],v[212:215],v[156:159],v[40:43]
aw a35,v251
mm v[40:43],v[208:211],v[136:139],v[40:43]
rt v[200:201],v241 offset:8576
rt v[202:203],v241 offset:9600
mm v[72:75],v[208:211],v[144:147],v[72:75]
mm v[72:75],v[212:215],a[120:123],v[72:75]
ld a[196:199],v247,s[52:55],s91 offen offset:0 sc1
mm v[104:107],v[212:215],a[124:127],v[104:107]
mm v[104:107],v[208:211],v[148:151],v[104:107]
rt v[212:213],v242 offset:128
rt v[214:215],v242 offset:1152
mm v[108:111],v[216:219],v[148:151],v[108:111]
mm v[108:111],v[220:223],a[124:127],v[108:111]
mm v[76:79],v[220:223],a[120:123],v[76:79]
mm v[76:79],v[216:219],v[144:147],v[76:79]
rt v[208:209],v242 offset:8576
rt v[210:211],v242 offset:9600
mm v[44:47],v[216:219],v[136:139],v[44:47]
mm v[44:47],v[220:223],v[156:159],v[44:47]
st a[12:15],v255,s[48:51],s18 offen offset:2048 sc1
mm v[12:15],v[220:223],v[152:155],v[12:15]
wt lgkmcnt(4)
mm v[12:15],v[216:219],v[132:135],v[12:15]
rt v[220:221],v243 offset:128
rt v[222:223],v243 offset:1152
mm v[16:19],v[196:199],v[132:135],v[16:19]
ar v224,a248
ar v225,a249
mm v[16:19],v[192:195],v[152:155],v[16:19]
ld a[200:203],v244,s[52:55],s91 offen offset:1024 sc1
mm v[48:51],v[192:195],v[156:159],v[48:51]
ar v226,a250
ar v227,a251
mm v[48:51],v[196:199],v[136:139],v[48:51]
rt v[216:217],v243 offset:8576
rt v[218:219],v243 offset:9600
mm v[80:83],v[196:199],v[144:147],v[80:83]
ar v228,a252
ar v229,a253
mm v[80:83],v[192:195],a[120:123],v[80:83]
ld a[204:207],v245,s[52:55],s91 offen offset:1024 sc1
mm v[112:115],v[192:195],a[124:127],v[112:115]
ar v230,a254
ar v231,a255
mm v[112:115],v[196:199],v[148:151],v[112:115]
cv v248,v224,v225
cv v249,v226,v227
mm v[116:119],v[204:207],v[148:151],v[116:119]
wt lgkmcnt(0)
s_barrier
mm v[116:119],v[200:203],a[124:127],v[116:119]
cv v250,v228,v229
cv v251,v230,v231
mm v[84:87],v[200:203],a[120:123],v[84:87]
rt v[192:193],v240 offset:16896
rt v[194:195],v240 offset:17920
mm v[84:87],v[204:207],v[144:147],v[84:87]
rt v[196:197],v240 offset:25344
rt v[198:199],v240 offset:26368
mm v[52:55],v[204:207],v[136:139],v[52:55]
aw a36,v248
aw a37,v249
mm v[52:55],v[200:203],v[156:159],v[52:55]
aw a38,v250
aw a39,v251
mm v[20:23],v[200:203],v[152:155],v[20:23]
s_getpc_b64 s[92:93]
.La5_LCT2:
s_and_b32 s93, s93, 0xffff
mm v[20:23],v[204:207],v[132:135],v[20:23]
rt v[200:201],v241 offset:16896
)ASM" R"ASM(rt v[202:203],v241 offset:17920
mm v[24:27],v[212:215],v[132:135],v[24:27]
s_lshr_b32 s94, s2, 3
s_lshl2_add_u32 s94, s94, s3
mm v[24:27],v[208:211],v[152:155],v[24:27]
s_lshl_b32 s94, s94, 10
v_lshrrev_b32 v233, 7, v255
mm v[56:59],v[208:211],v[156:159],v[56:59]
v_lshlrev_b32 v232, 3, v255
v_lshl_add_u32 v232, v233, 28, v232
mm v[56:59],v[212:215],v[136:139],v[56:59]
rt v[204:205],v241 offset:25344
rt v[206:207],v241 offset:26368
mm v[88:91],v[212:215],v[144:147],v[88:91]
v_add_u32 v232, s94, v232
s_mov_b32 s94, .La5_LCODEEND-.La5_LCT2
mm v[88:91],v[208:211],a[120:123],v[88:91]
s_mov_b32 s95, 0x20000
buffer_load_dword a253, v232, s[92:95], 0 offen
mm v[120:123],v[208:211],a[124:127],v[120:123]
s_mov_b32 s80, 8192
mm v[120:123],v[212:215],v[148:151],v[120:123]
rt v[208:209],v242 offset:16896
rt v[210:211],v242 offset:17920
mm v[124:127],v[220:223],v[148:151],v[124:127]
s_cmp_eq_u32 s73, 0
s_cselect_b32 s80, -8192, s80
mm v[124:127],v[216:219],a[124:127],v[124:127]
ld v[148:151],v246,s[52:55],s91 offen offset:1024 sc1
mm v[92:95],v[216:219],a[120:123],v[92:95]
s_add_u32 s83, s83, 4294886912
s_lshl_b32 s92, s76, 21
mm v[92:95],v[220:223],v[144:147],v[92:95]
rt v[212:213],v242 offset:25344
rt v[214:215],v242 offset:26368
mm v[60:63],v[220:223],v[136:139],v[60:63]
ds_write_b128 v238, a[12:15] offset:6144
mm v[60:63],v[216:219],v[156:159],v[60:63]
st a[16:19],v255,s[48:51],s18 offen offset:3072 sc1
mm v[28:31],v[216:219],v[152:155],v[28:31]
wt vmcnt(13) lgkmcnt(5)
s_add_u32 s92, s92, 54525952
mm v[28:31],v[220:223],v[132:135],v[28:31]
rt v[216:217],v243 offset:16896
rt v[218:219],v243 offset:17920
mm v[0:3],v[192:195],v[160:163],v[0:3]
ar v224,a40
ar v225,a41
mm v[0:3],v[196:199],v[176:179],v[0:3]
ld v[132:135],v247,s[52:55],s91 offen offset:1024 sc1
mm v[32:35],v[196:199],v[180:183],v[32:35]
ar v226,a42
ar v227,a43
mm v[32:35],v[192:195],v[164:167],v[32:35]
rt v[220:221],v243 offset:25344
rt v[222:223],v243 offset:26368
mm v[64:67],v[192:195],v[168:171],v[64:67]
ar v228,a44
ar v229,a45
mm v[64:67],v[196:199],v[184:187],v[64:67]
ld a[96:99],v244,s[52:55],s91 offen offset:2048 sc1
mm v[96:99],v[196:199],v[188:191],v[96:99]
ar v230,a46
ar v231,a47
mm v[96:99],v[192:195],v[172:175],v[96:99]
rt v[196:197],v240 offset:17024
rt v[198:199],v240 offset:18048
mm v[100:103],v[200:203],v[172:175],v[100:103]
cv v248,v224,v225
cv v249,v226,v227
mm v[100:103],v[204:207],v[188:191],v[100:103]
ld a[100:103],v245,s[52:55],s91 offen offset:2048 sc1
mm v[68:71],v[204:207],v[184:187],v[68:71]
cv v250,v228,v229
cv v251,v230,v231
mm v[68:71],v[200:203],v[168:171],v[68:71]
rt v[192:193],v240 offset:25472
rt v[194:195],v240 offset:26496
mm v[36:39],v[200:203],v[164:167],v[36:39]
aw a40,v248
aw a41,v249
mm v[36:39],v[204:207],v[180:183],v[36:39]
ld a[104:107],v246,s[52:55],s91 offen offset:2048 sc1
mm v[4:7],v[204:207],v[176:179],v[4:7]
wt lgkmcnt(4)
aw a42,v250
mm v[4:7],v[200:203],v[160:163],v[4:7]
rt v[204:205],v241 offset:17024
rt v[206:207],v241 offset:18048
mm v[8:11],v[208:211],v[160:163],v[8:11]
aw a43,v251
mm v[8:11],v[212:215],v[176:179],v[8:11]
ld a[108:111],v247,s[52:55],s91 offen offset:2048 sc1
mm v[40:43],v[212:215],v[180:183],v[40:43]
s_add_u32 s44, s26, s92
s_addc_u32 s45, s27, 0
mm v[40:43],v[208:211],v[164:167],v[40:43]
rt v[200:201],v241 offset:25472
rt v[202:203],v241 offset:26496
mm v[72:75],v[208:211],v[168:171],v[72:75]
)ASM" R"ASM(s_and_b32 s45, s45, 0xffff
s_mov_b32 s46, 2097536
mm v[72:75],v[212:215],v[184:187],v[72:75]
s_mov_b32 s95, 4096
s_mov_b32 s96, 8192
mm v[104:107],v[212:215],v[188:191],v[104:107]
s_mov_b32 s18, 0
s_mov_b32 s97, 12288
mm v[104:107],v[208:211],v[172:175],v[104:107]
rt v[212:213],v242 offset:17024
rt v[214:215],v242 offset:18048
mm v[108:111],v[216:219],v[172:175],v[108:111]
s_mul_i32 s92, s3, 196608
s_sub_u32 s93, 1376256, s92
mm v[108:111],v[220:223],v[188:191],v[108:111]
st a[20:23],v255,s[48:51],s18 offen offset:0 sc1
mm v[76:79],v[220:223],v[184:187],v[76:79]
mm v[76:79],v[216:219],v[168:171],v[76:79]
rt v[208:209],v242 offset:25472
rt v[210:211],v242 offset:26496
mm v[44:47],v[216:219],v[164:167],v[44:47]
ds_write_b128 v238, a[16:19] offset:7168
mm v[44:47],v[220:223],v[180:183],v[44:47]
st a[24:27],v255,s[48:51],s18 offen offset:1024 sc1
mm v[12:15],v[220:223],v[176:179],v[12:15]
wt lgkmcnt(5)
mm v[12:15],v[216:219],v[160:163],v[12:15]
rt v[220:221],v243 offset:17024
rt v[222:223],v243 offset:18048
mm v[16:19],v[196:199],v[160:163],v[16:19]
ar v224,a48
ar v225,a49
mm v[16:19],v[192:195],v[176:179],v[16:19]
ld a[112:115],v244,s[52:55],s91 offen offset:3072 sc1
mm v[48:51],v[192:195],v[180:183],v[48:51]
ar v226,a50
ar v227,a51
mm v[48:51],v[196:199],v[164:167],v[48:51]
rt v[216:217],v243 offset:25472
rt v[218:219],v243 offset:26496
mm v[80:83],v[196:199],v[168:171],v[80:83]
ar v228,a52
ar v229,a53
mm v[80:83],v[192:195],v[184:187],v[80:83]
ld a[116:119],v245,s[52:55],s91 offen offset:3072 sc1
mm v[112:115],v[192:195],v[188:191],v[112:115]
ar v230,a54
ar v231,a55
mm v[112:115],v[196:199],v[172:175],v[112:115]
cv v248,v224,v225
cv v249,v226,v227
mm v[116:119],v[204:207],v[172:175],v[116:119]
wt lgkmcnt(0)
s_barrier
mm v[116:119],v[200:203],v[188:191],v[116:119]
ld a[120:123],v246,s[52:55],s91 offen offset:3072 sc1
mm v[84:87],v[200:203],v[184:187],v[84:87]
rt v[192:193],v240 offset:33792
rt v[194:195],v240 offset:34816
mm v[84:87],v[204:207],v[168:171],v[84:87]
rt v[196:197],v240 offset:42240
rt v[198:199],v240 offset:43264
mm v[52:55],v[204:207],v[164:167],v[52:55]
ds_write_b128 v238, a[20:23] offset:32768
mm v[52:55],v[200:203],v[180:183],v[52:55]
ld a[124:127],v247,s[52:55],s91 offen offset:3072 sc1
mm v[20:23],v[200:203],v[176:179],v[20:23]
cv v250,v228,v229
cv v251,v230,v231
mm v[20:23],v[204:207],v[160:163],v[20:23]
rt v[200:201],v241 offset:33792
rt v[202:203],v241 offset:34816
mm v[24:27],v[212:215],v[160:163],v[24:27]
aw a44,v248
aw a45,v249
mm v[24:27],v[208:211],v[176:179],v[24:27]
aw a46,v250
aw a47,v251
mm v[56:59],v[208:211],v[180:183],v[56:59]
mm v[56:59],v[212:215],v[164:167],v[56:59]
rt v[204:205],v241 offset:42240
rt v[206:207],v241 offset:43264
mm v[88:91],v[212:215],v[168:171],v[88:91]
mm v[88:91],v[208:211],v[184:187],v[88:91]
mm v[120:123],v[208:211],v[188:191],v[120:123]
mm v[120:123],v[212:215],v[172:175],v[120:123]
rt v[208:209],v242 offset:33792
rt v[210:211],v242 offset:34816
mm v[124:127],v[220:223],v[172:175],v[124:127]
mm v[124:127],v[216:219],v[188:191],v[124:127]
st a[28:31],v255,s[48:51],s18 offen offset:2048 sc1
mm v[92:95],v[216:219],v[184:187],v[92:95]
mm v[92:95],v[220:223],v[168:171],v[92:95]
rt v[212:213],v242 offset:42240
rt v[214:215],v242 offset:43264
mm v[60:63],v[220:223],v[164:167],v[60:63]
ds_write_b128 v238, a[24:27] offset:33792
mm v[60:63],v[216:219],v[180:183],v[60:63]
st a[32:35],v255,s[48:51],s18 offen offset:3072 sc1
mm v[28:31],v[216:219],v[176:179],v[28:31]
)ASM" R"ASM(wt vmcnt(12) lgkmcnt(5)
mm v[28:31],v[220:223],v[160:163],v[28:31]
rt v[216:217],v243 offset:33792
rt v[218:219],v243 offset:34816
mm v[0:3],v[192:195],v[128:131],v[0:3]
ar v224,a56
ar v225,a57
mm v[0:3],v[196:199],a[200:203],v[0:3]
ar v226,a58
ar v227,a59
mm v[32:35],v[196:199],a[204:207],v[32:35]
ar v228,a60
ar v229,a61
mm v[32:35],v[192:195],v[140:143],v[32:35]
rt v[220:221],v243 offset:42240
rt v[222:223],v243 offset:43264
mm v[64:67],v[192:195],a[192:195],v[64:67]
ar v230,a62
ar v231,a63
mm v[64:67],v[196:199],v[148:151],v[64:67]
cv v248,v224,v225
cv v249,v226,v227
mm v[96:99],v[196:199],v[132:135],v[96:99]
cv v250,v228,v229
cv v251,v230,v231
mm v[96:99],v[192:195],a[196:199],v[96:99]
rt v[196:197],v240 offset:33920
rt v[198:199],v240 offset:34944
mm v[100:103],v[200:203],a[196:199],v[100:103]
aw a48,v248
aw a49,v249
mm v[100:103],v[204:207],v[132:135],v[100:103]
aw a50,v250
aw a51,v251
mm v[68:71],v[204:207],v[148:151],v[68:71]
v_add_u32 v244, s92, v244
v_add_u32 v246, s93, v246
mm v[68:71],v[200:203],a[192:195],v[68:71]
rt v[192:193],v240 offset:42368
rt v[194:195],v240 offset:43392
mm v[36:39],v[200:203],v[140:143],v[36:39]
ds_write_b128 v238, a[28:31] offset:34816
mm v[36:39],v[204:207],a[204:207],v[36:39]
v_add_u32 v245, 0x1000, v244
v_add_u32 v247, 0x1000, v246
mm v[4:7],v[204:207],a[200:203],v[4:7]
wt lgkmcnt(5)
s_lshl_b32 s91, s72, 13
mm v[4:7],v[200:203],v[128:131],v[4:7]
rt v[204:205],v241 offset:33920
rt v[206:207],v241 offset:34944
mm v[8:11],v[208:211],v[128:131],v[8:11]
s_lshl4_add_u32 s92, s76, s3
s_lshl3_add_u32 s92, s92, s20
mm v[8:11],v[212:215],a[200:203],v[8:11]
s_bitset1_b32 s20, 30
mm v[40:43],v[212:215],a[204:207],v[40:43]
mm v[40:43],v[208:211],v[140:143],v[40:43]
rt v[200:201],v241 offset:42368
rt v[202:203],v241 offset:43392
mm v[72:75],v[208:211],a[192:195],v[72:75]
mm v[72:75],v[212:215],v[148:151],v[72:75]
mm v[104:107],v[212:215],v[132:135],v[104:107]
s_mov_b32 s18, 4096
mm v[104:107],v[208:211],a[196:199],v[104:107]
rt v[212:213],v242 offset:33920
rt v[214:215],v242 offset:34944
mm v[108:111],v[216:219],a[196:199],v[108:111]
mm v[108:111],v[220:223],v[132:135],v[108:111]
st a[36:39],v255,s[48:51],s18 offen offset:0 sc1
mm v[76:79],v[220:223],v[148:151],v[76:79]
mm v[76:79],v[216:219],a[192:195],v[76:79]
rt v[208:209],v242 offset:42368
rt v[210:211],v242 offset:43392
mm v[44:47],v[216:219],v[140:143],v[44:47]
ds_write_b128 v238, a[32:35] offset:35840
mm v[44:47],v[220:223],a[204:207],v[44:47]
st a[40:43],v255,s[48:51],s18 offen offset:1024 sc1
mm v[12:15],v[220:223],a[200:203],v[12:15]
wt lgkmcnt(5)
mm v[12:15],v[216:219],v[128:131],v[12:15]
rt v[220:221],v243 offset:33920
rt v[222:223],v243 offset:34944
mm v[16:19],v[196:199],v[128:131],v[16:19]
s_mov_b64 s[52:53], s[86:87]
mm v[16:19],v[192:195],a[200:203],v[16:19]
s_bitcmp1_b32 21, s73
s_cselect_b32 s54, 16384, 0
s_cselect_b32 s86, s88, 1073741824
mm v[48:51],v[192:195],a[204:207],v[48:51]
s_lshl_b32 s89, s3, 14
v_add_u32 v248, s89, v255
mm v[48:51],v[196:199],v[140:143],v[48:51]
rt v[216:217],v243 offset:42368
rt v[218:219],v243 offset:43392
mm v[80:83],v[196:199],a[192:195],v[80:83]
ds_write_b128 v238, a[36:39] offset:36864
mm v[80:83],v[192:195],v[148:151],v[80:83]
s_add_u32 s86, s86, 262144
mm v[112:115],v[192:195],v[132:135],v[112:115]
)ASM" R"ASM(s_cmp_eq_u32 s73, 0
s_cselect_b32 s86, s92, s86
s_cselect_b32 s92, 1073741824, s92
mm v[112:115],v[196:199],a[196:199],v[112:115]
s_mov_b32 s87, s92
s_add_u32 s89, s89, 16384
mm v[116:119],v[204:207],a[196:199],v[116:119]
wt lgkmcnt(1)
s_barrier
mm v[116:119],v[200:203],v[132:135],v[116:119]
s_lshl_b32 m0, s3, 12
s_add_u32 m0, m0, 131072
.La5_LW1_14:
s_cbranch_execz .La5_LW1_R0
mm v[84:87],v[200:203],v[148:151],v[84:87]
rt v[192:193],v240 offset:50688
rt v[194:195],v240 offset:51712
mm v[84:87],v[204:207],a[192:195],v[84:87]
rt v[196:197],v240 offset:59136
rt v[198:199],v240 offset:60160
mm v[52:55],v[204:207],v[140:143],v[52:55]
ds_write_b128 v238, a[40:43] offset:37888
mm v[52:55],v[200:203],a[204:207],v[52:55]
v_bfe_u32 v251, v255, 2, 5
v_and_b32 v224, 0x380, v255
mm v[20:23],v[200:203],a[200:203],v[20:23]
v_add_u32 v251, v251, v224
v_and_b32 v224, 16, v255
mm v[20:23],v[204:207],v[128:131],v[20:23]
rt v[200:201],v241 offset:50688
rt v[202:203],v241 offset:51712
mm v[24:27],v[212:215],v[128:131],v[24:27]
v_cmp_eq_u32 vcc, 0, v224
mm v[24:27],v[208:211],a[200:203],v[24:27]
v_cndmask_b32 v249, v253, v252, vcc
mm v[56:59],v[208:211],a[204:207],v[56:59]
mm v[56:59],v[212:215],v[140:143],v[56:59]
rt v[204:205],v241 offset:59136
rt v[206:207],v241 offset:60160
mm v[88:91],v[212:215],a[192:195],v[88:91]
ds_write_b128 v238, a[44:47] offset:38912
mm v[88:91],v[208:211],v[148:151],v[88:91]
st a[44:47],v255,s[48:51],s18 offen offset:2048 sc1
mm v[120:123],v[208:211],v[132:135],v[120:123]
mm v[120:123],v[212:215],a[196:199],v[120:123]
rt v[208:209],v242 offset:50688
rt v[210:211],v242 offset:51712
mm v[124:127],v[220:223],a[196:199],v[124:127]
mm v[124:127],v[216:219],v[132:135],v[124:127]
mm v[92:95],v[216:219],v[148:151],v[92:95]
mm v[92:95],v[220:223],a[192:195],v[92:95]
rt v[212:213],v242 offset:59136
rt v[214:215],v242 offset:60160
mm v[60:63],v[220:223],v[140:143],v[60:63]
mm v[60:63],v[216:219],a[204:207],v[60:63]
st a[48:51],v255,s[48:51],s18 offen offset:3072 sc1
mm v[28:31],v[216:219],a[200:203],v[28:31]
wt vmcnt(6) lgkmcnt(5)
mm v[28:31],v[220:223],v[128:131],v[28:31]
rt v[216:217],v243 offset:50688
rt v[218:219],v243 offset:51712
mm v[0:3],v[192:195],a[96:99],v[0:3]
mm v[0:3],v[196:199],a[112:115],v[0:3]
mm v[32:35],v[196:199],a[116:119],v[32:35]
mm v[32:35],v[192:195],a[100:103],v[32:35]
rt v[220:221],v243 offset:59136
rt v[222:223],v243 offset:60160
mm v[64:67],v[192:195],a[104:107],v[64:67]
mm v[64:67],v[196:199],a[120:123],v[64:67]
mm v[96:99],v[196:199],a[124:127],v[96:99]
mm v[96:99],v[192:195],a[108:111],v[96:99]
rt v[196:197],v240 offset:50816
rt v[198:199],v240 offset:51840
mm v[100:103],v[200:203],a[108:111],v[100:103]
mm v[100:103],v[204:207],a[124:127],v[100:103]
mm v[68:71],v[204:207],a[120:123],v[68:71]
mm v[68:71],v[200:203],a[104:107],v[68:71]
rt v[192:193],v240 offset:59264
rt v[194:195],v240 offset:60288
mm v[36:39],v[200:203],a[100:103],v[36:39]
mm v[36:39],v[204:207],a[116:119],v[36:39]
mm v[4:7],v[204:207],a[112:115],v[4:7]
wt lgkmcnt(4)
mm v[4:7],v[200:203],a[96:99],v[4:7]
rt v[204:205],v241 offset:50816
rt v[206:207],v241 offset:51840
mm v[8:11],v[208:211],a[96:99],v[8:11]
mm v[8:11],v[212:215],a[112:115],v[8:11]
mm v[40:43],v[212:215],a[116:119],v[40:43]
cv v128,v0,v1
cv v129,v2,v3
mm v[40:43],v[208:211],a[100:103],v[40:43]
rt v[200:201],v241 offset:59264
rt v[202:203],v241 offset:60288
mm v[72:75],v[208:211],a[104:107],v[72:75]
cv v130,v4,v5
cv v131,v6,v7
mm v[72:75],v[212:215],a[120:123],v[72:75]
cv v144,v32,v33
cv v145,v34,v35
)ASM" R"ASM(mm v[104:107],v[212:215],a[124:127],v[104:107]
cv v146,v36,v37
cv v147,v38,v39
mm v[104:107],v[208:211],a[108:111],v[104:107]
rt v[212:213],v242 offset:50816
rt v[214:215],v242 offset:51840
mm v[108:111],v[216:219],a[108:111],v[108:111]
cv v160,v64,v65
cv v161,v66,v67
mm v[108:111],v[220:223],a[124:127],v[108:111]
cv v162,v68,v69
cv v163,v70,v71
mm v[76:79],v[220:223],a[120:123],v[76:79]
cv v176,v96,v97
cv v177,v98,v99
mm v[76:79],v[216:219],a[104:107],v[76:79]
rt v[208:209],v242 offset:59264
rt v[210:211],v242 offset:60288
mm v[44:47],v[216:219],a[100:103],v[44:47]
cv v178,v100,v101
cv v179,v102,v103
mm v[44:47],v[220:223],a[116:119],v[44:47]
mm v[12:15],v[220:223],a[112:115],v[12:15]
wt lgkmcnt(4)
mm v[12:15],v[216:219],a[96:99],v[12:15]
rt v[220:221],v243 offset:50816
rt v[222:223],v243 offset:51840
mm v[16:19],v[196:199],a[96:99],v[16:19]
v_mul_u32_u24 v241, 0x5555, v224
v_lshrrev_b32 v241, 4, v241
v_add_u32 v241, 0x8888, v241
mm v[16:19],v[192:195],a[112:115],v[16:19]
st v[128:131],v255,s[52:55],0 offen offset:0 sc1
mm v[48:51],v[192:195],a[116:119],v[48:51]
cv v132,v8,v9
cv v133,v10,v11
cv v181,v106,v107
mm v[48:51],v[196:199],a[100:103],v[48:51]
rt v[216:217],v243 offset:59264
rt v[218:219],v243 offset:60288
cv v182,v108,v109
mm v[80:83],v[196:199],a[104:107],v[80:83]
cv v134,v12,v13
cv v135,v14,v15
cv v183,v110,v111
mm v[80:83],v[192:195],a[120:123],v[80:83]
st v[144:147],v255,s[52:55],s95 offen offset:0 sc1
ar v34,a238
mm v[112:115],v[192:195],a[124:127],v[112:115]
cv v148,v40,v41
cv v149,v42,v43
ar v35,a239
mm v[112:115],v[196:199],a[108:111],v[112:115]
cv v150,v44,v45
cv v151,v46,v47
v_mov_b32 v36, 0xff800000
mm v[116:119],v[204:207],a[108:111],v[116:119]
wt lgkmcnt(0)
s_barrier
v_mov_b32 v37, 0xff800000
mm v[116:119],v[200:203],a[124:127],v[116:119]
st v[160:163],v255,s[52:55],s96 offen offset:0 sc1
v_mov_b32 v38, 0xff800000
mm v[84:87],v[200:203],a[120:123],v[84:87]
cv v164,v72,v73
cv v165,v74,v75
v_mov_b32 v39, 0xff800000
mm v[84:87],v[204:207],a[104:107],v[84:87]
rd a[64:67],v248 offset:0
rd a[68:71],v248 offset:1024
mm v[52:55],v[204:207],a[100:103],v[52:55]
cv v166,v76,v77
cv v167,v78,v79
mm v[52:55],v[200:203],a[116:119],v[52:55]
st v[176:179],v255,s[52:55],s97 offen offset:0 sc1
mm v[20:23],v[200:203],a[112:115],v[20:23]
s_nop 0
cv v180,v104,v105
mm v[20:23],v[204:207],a[96:99],v[20:23]
rd a[72:75],v248 offset:2048
rd a[76:79],v248 offset:3072
mm v[24:27],v[212:215],a[96:99],v[24:27]
ar v32,a236
ar v33,a237
mm v[24:27],v[208:211],a[112:115],v[24:27]
st v[132:135],v255,s[52:55],0 offen offset:1024 sc1
mm v[56:59],v[208:211],a[116:119],v[56:59]
cv v136,v16,v17
cv v137,v18,v19
mm v[56:59],v[212:215],a[100:103],v[56:59]
rd a[80:83],v248 offset:4096
rd a[84:87],v248 offset:5120
mm v[88:91],v[212:215],a[104:107],v[88:91]
cv v138,v20,v21
cv v139,v22,v23
mm v[88:91],v[208:211],a[120:123],v[88:91]
st v[148:151],v255,s[52:55],s95 offen offset:1024 sc1
mm v[120:123],v[208:211],a[124:127],v[120:123]
cv v152,v48,v49
cv v153,v50,v51
)ASM" R"ASM(mm v[120:123],v[212:215],a[108:111],v[120:123]
rd a[88:91],v248 offset:6144
rd a[92:95],v248 offset:7168
mm v[124:127],v[220:223],a[108:111],v[124:127]
ds_write_b128 v238, a[48:51] offset:39936
mm v[124:127],v[216:219],a[124:127],v[124:127]
st v[164:167],v255,s[52:55],s96 offen offset:1024 sc1
mm v[92:95],v[216:219],a[120:123],v[92:95]
cv v154,v52,v53
cv v155,v54,v55
mm v[92:95],v[220:223],a[104:107],v[92:95]
cv v168,v80,v81
cv v169,v82,v83
mm v[60:63],v[220:223],a[100:103],v[60:63]
cv v170,v84,v85
cv v171,v86,v87
mm v[60:63],v[216:219],a[116:119],v[60:63]
st v[180:183],v255,s[52:55],s97 offen offset:1024 sc1
mm v[28:31],v[216:219],a[112:115],v[28:31]
cv v184,v112,v113
cv v185,v114,v115
mm v[28:31],v[220:223],a[96:99],v[28:31]
cv v186,v116,v117
cv v187,v118,v119
wt lgkmcnt(1)
mm v[208:211],a[64:67],v[160:163],v[32:35]
mm v[216:219],a[64:67],v[176:179],0
st v[136:139],v255,s[52:55],0 offen offset:2048 sc1
mm v[212:215],a[80:83],v[160:163],v[36:39]
cv v172,v88,v89
cv v173,v90,v91
cv v174,v92,v93
mm v[220:223],a[80:83],v[176:179],v[32:35]
cv v175,v94,v95
cv v188,v120,v121
cv v189,v122,v123
mm v[208:211],a[68:71],v[164:167],v[208:211]
cv v190,v124,v125
cv v191,v126,v127
cv v140,v24,v25
mm v[216:219],a[68:71],v[180:183],v[216:219]
st v[152:155],v255,s[52:55],s95 offen offset:2048 sc1
cv v141,v26,v27
mm v[212:215],a[84:87],v[164:167],v[212:215]
cv v142,v28,v29
cv v143,v30,v31
cv v156,v56,v57
mm v[220:223],a[84:87],v[180:183],v[220:223]
cv v157,v58,v59
cv v158,v60,v61
cv v159,v62,v63
mm v[208:211],a[72:75],v[168:171],v[208:211]
mm v[216:219],a[72:75],v[184:187],v[216:219]
st v[168:171],v255,s[52:55],s96 offen offset:2048 sc1
mm v[212:215],a[88:91],v[168:171],v[212:215]
mm v[220:223],a[88:91],v[184:187],v[220:223]
mm v[208:211],a[76:79],v[172:175],v[208:211]
mm v[216:219],a[76:79],v[188:191],v[216:219]
st v[184:187],v255,s[52:55],s97 offen offset:2048 sc1
mm v[212:215],a[92:95],v[172:175],v[212:215]
rd a[64:67],v248 offset:16384
mm v[220:223],a[92:95],v[188:191],v[220:223]
rd a[96:99],v248 offset:8192
ex v0,v208
wt vmcnt(12)
buffer_store_dwordx2 v[252:253], off, s[64:67], s87 sc1
mm v[64:67],a[232:235],a[232:235],0
rd a[68:71],v248 offset:17408
ex v1,v209
mm v[68:71],a[232:235],a[232:235],0
rd a[100:103],v248 offset:9216
st v[140:143],v255,s[52:55],0 offen offset:3072 sc1
mm v[72:75],a[232:235],a[232:235],0
rd a[72:75],v248 offset:18432
ex v2,v210
mm v[76:79],a[232:235],a[232:235],0
rd a[104:107],v248 offset:10240
ex v3,v211
mm v[80:83],a[232:235],a[232:235],0
rd a[76:79],v248 offset:19456
ex v8,v216
mm v[84:87],a[232:235],a[232:235],0
rd a[108:111],v248 offset:11264
st v[156:159],v255,s[52:55],s95 offen offset:3072 sc1
mm v[88:91],a[232:235],a[232:235],0
rd a[80:83],v248 offset:20480
ex v9,v217
mm v[92:95],a[232:235],a[232:235],0
rd a[112:115],v248 offset:12288
ex v10,v218
mm v[96:99],a[232:235],a[232:235],0
rd a[84:87],v248 offset:21504
ex v11,v219
mm v[100:103],a[232:235],a[232:235],0
rd a[116:119],v248 offset:13312
st v[172:175],v255,s[52:55],s96 offen offset:3072 sc1
mm v[104:107],a[232:235],a[232:235],0
rd a[88:91],v248 offset:22528
ex v4,v212
mm v[108:111],a[232:235],a[232:235],0
)ASM" R"ASM(rd a[120:123],v248 offset:14336
ex v5,v213
mm v[112:115],a[232:235],a[232:235],0
rd a[92:95],v248 offset:23552
ex v6,v214
mm v[116:119],a[232:235],a[232:235],0
rd a[124:127],v248 offset:15360
st v[188:191],v255,s[52:55],s97 offen offset:3072 sc1
ex v7,v215
mm v[120:123],a[232:235],a[232:235],0
v_exp_f32_e64 v12, v220
mm v[124:127],a[232:235],a[232:235],0
aw a192,0
aw a193,0
aw a194,0
aw a195,0
aw a196,0
aw a197,0
aw a198,0
aw a199,0
v_add_u32 v248, 0x4000, v248
s_cmp_eq_u32 s3, 3
s_cbranch_scc1 .La5_LA_FH
.p2align 6
; ---- attention, 32 keys per window: S = K Q^T (causal mask through SrcC), P = exp2(S), O += V^T P, row sums l += 1 P (one sparse MFMA for both tiles); here the later 32 rows only
.La5_LA_HH:
wt lgkmcnt(1)
mm v[208:211],a[64:67],v[160:163],0
ex v13,v221
mm v[208:211],a[68:71],v[164:167],v[208:211]
cv v232,v0,v1
cv v233,v2,v3
mm v[208:211],a[72:75],v[168:171],v[208:211]
ex v14,v222
mm v[208:211],a[76:79],v[172:175],v[208:211]
cv v236,v8,v9
cv v237,v10,v11
mm v[216:219],a[76:79],v[188:191],0
ex v15,v223
mm v[216:219],a[72:75],v[184:187],v[216:219]
cv v234,v4,v5
cv v235,v6,v7
mm v[216:219],a[68:71],v[180:183],v[216:219]
ex v0,v208
mm v[216:219],a[64:67],v[176:179],v[216:219]
cv v238,v12,v13
cv v239,v14,v15
mm v[212:215],a[80:83],v[160:163],0
ex v1,v209
mm v[212:215],a[84:87],v[164:167],v[212:215]
rd a[64:67],v248 offset:16384
rd a[68:71],v248 offset:17408
mm v[212:215],a[88:91],v[168:171],v[212:215]
ex v2,v210
mm v[212:215],a[92:95],v[172:175],v[212:215]
rd a[72:75],v248 offset:18432
rd a[76:79],v248 offset:19456
mm v[220:223],a[92:95],v[188:191],0
ex v3,v211
mm v[220:223],a[88:91],v[184:187],v[220:223]
rd a[92:95],v248 offset:23552
mm v[220:223],a[84:87],v[180:183],v[220:223]
ex v8,v216
mm v[220:223],a[80:83],v[176:179],v[220:223]
wt lgkmcnt(5)
rd a[88:91],v248 offset:22528
mm v[64:67],a[96:99],v[232:235],v[64:67]
ex v9,v217
mm v[96:99],a[96:99],v[236:239],v[96:99]
rd a[96:99],v248 offset:8192
rd a[84:87],v248 offset:21504
mm v[100:103],a[100:103],v[236:239],v[100:103]
ex v10,v218
mm v[68:71],a[100:103],v[232:235],v[68:71]
rd a[100:103],v248 offset:9216
rd a[80:83],v248 offset:20480
mm v[72:75],a[104:107],v[232:235],v[72:75]
ex v11,v219
mm v[104:107],a[104:107],v[236:239],v[104:107]
rd a[104:107],v248 offset:10240
mm v[108:111],a[108:111],v[236:239],v[108:111]
ex v4,v212
mm v[76:79],a[108:111],v[232:235],v[76:79]
rd a[108:111],v248 offset:11264
s_add_u32 s85, s85, 1
mm v[80:83],a[112:115],v[232:235],v[80:83]
ex v5,v213
mm v[112:115],a[112:115],v[236:239],v[112:115]
rd a[112:115],v248 offset:12288
s_add_u32 s89, s89, 32768
mm v[116:119],a[116:119],v[236:239],v[116:119]
ex v6,v214
mm v[84:87],a[116:119],v[232:235],v[84:87]
rd a[116:119],v248 offset:13312
s_add_u32 s92, s85, s3
mm v[88:91],a[120:123],v[232:235],v[88:91]
ex v7,v215
mm v[120:123],a[120:123],v[236:239],v[120:123]
rd a[120:123],v248 offset:14336
buffer_load_dword v250, v251, s[64:67], s20 offen sc1
)ASM" R"ASM(mm v[124:127],a[124:127],v[236:239],v[124:127]
ex v12,v220
mm v[92:95],a[124:127],v[232:235],v[92:95]
rd a[124:127],v248 offset:15360
s_barrier
v_smfmac_f32_16x16x64_bf16 a[196:199], a[208:211], v[232:239], v241
wt lgkmcnt(6)
mm v[208:211],a[64:67],v[160:163],0
ex v13,v221
mm v[208:211],a[68:71],v[164:167],v[208:211]
cv v232,v0,v1
cv v233,v2,v3
mm v[208:211],a[72:75],v[168:171],v[208:211]
ex v14,v222
mm v[208:211],a[76:79],v[172:175],v[208:211]
cv v236,v8,v9
cv v237,v10,v11
mm v[216:219],a[76:79],v[188:191],0
ex v15,v223
mm v[216:219],a[72:75],v[184:187],v[216:219]
cv v234,v4,v5
cv v235,v6,v7
mm v[216:219],a[68:71],v[180:183],v[216:219]
ex v0,v208
mm v[216:219],a[64:67],v[176:179],v[216:219]
cv v238,v12,v13
cv v239,v14,v15
mm v[212:215],a[80:83],v[160:163],0
ex v1,v209
mm v[212:215],a[84:87],v[164:167],v[212:215]
rd a[64:67],v248 offset:32768
rd a[68:71],v248 offset:33792
mm v[212:215],a[88:91],v[168:171],v[212:215]
ex v2,v210
mm v[212:215],a[92:95],v[172:175],v[212:215]
rd a[72:75],v248 offset:34816
rd a[76:79],v248 offset:35840
mm v[220:223],a[92:95],v[188:191],0
ex v3,v211
mm v[220:223],a[88:91],v[184:187],v[220:223]
rd a[92:95],v248 offset:39936
s_bitcmp1_b64 s[98:99], s85
mm v[220:223],a[84:87],v[180:183],v[220:223]
ex v8,v216
mm v[220:223],a[80:83],v[176:179],v[220:223]
wt lgkmcnt(5)
rd a[88:91],v248 offset:38912
mm v[64:67],a[96:99],v[232:235],v[64:67]
ex v9,v217
mm v[96:99],a[96:99],v[236:239],v[96:99]
rd a[96:99],v248 offset:24576
rd a[84:87],v248 offset:37888
mm v[100:103],a[100:103],v[236:239],v[100:103]
ex v10,v218
mm v[68:71],a[100:103],v[232:235],v[68:71]
rd a[100:103],v248 offset:25600
rd a[80:83],v248 offset:36864
mm v[72:75],a[104:107],v[232:235],v[72:75]
ex v11,v219
mm v[104:107],a[104:107],v[236:239],v[104:107]
rd a[104:107],v248 offset:26624
mm v[108:111],a[108:111],v[236:239],v[108:111]
ex v4,v212
mm v[76:79],a[108:111],v[232:235],v[76:79]
rd a[108:111],v248 offset:27648
mm v[80:83],a[112:115],v[232:235],v[80:83]
ex v5,v213
mm v[112:115],a[112:115],v[236:239],v[112:115]
rd a[112:115],v248 offset:28672
mm v[116:119],a[116:119],v[236:239],v[116:119]
ex v6,v214
mm v[84:87],a[116:119],v[232:235],v[84:87]
rd a[116:119],v248 offset:29696
wt vmcnt(1)
buffer_store_dwordx2 v[252:253], off, s[64:67], s86 sc1
s_mov_b32 s86, 1073741824
s_bitset0_b32 s20, 30
mm v[88:91],a[120:123],v[232:235],v[88:91]
ex v7,v215
mm v[120:123],a[120:123],v[236:239],v[120:123]
rd a[120:123],v248 offset:30720
mm v[124:127],a[124:127],v[236:239],v[124:127]
v_exp_f32_e64 v12, v220
mm v[92:95],a[124:127],v[232:235],v[92:95]
rd a[124:127],v248 offset:31744
v_smfmac_f32_16x16x64_bf16 a[196:199], a[208:211], v[232:239], v241
v_add_u32 v248, 0x8000, v248
s_cbranch_scc0 .La5_LA_HH1
s_call_b64 s[24:25], .La5_LA_EVENT
s_add_u32 s92, s85, s3
.La5_LA_HH1:
s_cmp_lt_u32 s92, 3
s_cbranch_scc1 .La5_LA_HH
; ---- first window with both row groups (diagonal of the earlier group)
.La5_LA_FH:
.La5_LW1_15:
s_cbranch_execz .La5_LW1_R0
wt lgkmcnt(9)
mm v[192:195],a[64:67],v[128:131],v[32:35]
ex v13,v221
)ASM" R"ASM(mm v[192:195],a[68:71],v[132:135],v[192:195]
cv v232,v0,v1
cv v233,v2,v3
mm v[192:195],a[72:75],v[136:139],v[192:195]
ex v14,v222
mm v[192:195],a[76:79],v[140:143],v[192:195]
ld v255,s[60:63],s17 offen offset:0 sc1 lds
mm v[200:203],a[76:79],v[156:159],0
ex v15,v223
mm v[200:203],a[72:75],v[152:155],v[200:203]
cv v236,v8,v9
cv v237,v10,v11
mm v[200:203],a[68:71],v[148:151],v[200:203]
ex v192,v192
mm v[200:203],a[64:67],v[144:147],v[200:203]
wt lgkmcnt(1)
cv v234,v4,v5
mm v[196:199],a[80:83],v[128:131],v[36:39]
ex v193,v193
mm v[196:199],a[84:87],v[132:135],v[196:199]
cv v224,v192,v193
s_add_u32 s85, s85, 1
mm v[196:199],a[88:91],v[136:139],v[196:199]
ex v194,v194
mm v[196:199],a[92:95],v[140:143],v[196:199]
ld v255,s[60:63],s17 offen offset:1024 sc1 lds
mm v[204:207],a[92:95],v[156:159],v[32:35]
ex v195,v195
mm v[204:207],a[88:91],v[152:155],v[204:207]
cv v225,v194,v195
s_add_u32 s89, s89, 16384
mm v[204:207],a[84:87],v[148:151],v[204:207]
ex v200,v200
mm v[204:207],a[80:83],v[144:147],v[204:207]
ld v255,s[60:63],s17 offen offset:2048 sc1 lds
mm v[208:211],a[64:67],v[160:163],0
ex v201,v201
mm v[208:211],a[68:71],v[164:167],v[208:211]
cv v228,v200,v201
s_and_b32 s89, s89, 0x1ffff
mm v[208:211],a[72:75],v[168:171],v[208:211]
ex v202,v202
mm v[208:211],a[76:79],v[172:175],v[208:211]
ld v255,s[60:63],s17 offen offset:3072 sc1 lds
mm v[216:219],a[76:79],v[188:191],0
ex v203,v203
mm v[216:219],a[72:75],v[184:187],v[216:219]
cv v229,v202,v203
mm v[216:219],a[68:71],v[180:183],v[216:219]
ex v196,v196
mm v[216:219],a[64:67],v[176:179],v[216:219]
cv v235,v6,v7
cv v238,v12,v13
mm v[212:215],a[80:83],v[160:163],0
ex v197,v197
s_add_u32 s17, s17, s80
mm v[212:215],a[84:87],v[164:167],v[212:215]
cv v226,v196,v197
s_add_u32 m0, m0, 16384
s_and_b32 m0, m0, s78
mm v[212:215],a[88:91],v[168:171],v[212:215]
ex v198,v198
mm v[212:215],a[92:95],v[172:175],v[212:215]
cv v239,v14,v15
wt lgkmcnt(0)
mm v[220:223],a[92:95],v[188:191],0
ex v199,v199
mm v[220:223],a[88:91],v[184:187],v[220:223]
cv v227,v198,v199
mm v[220:223],a[84:87],v[180:183],v[220:223]
ex v204,v204
mm v[220:223],a[80:83],v[176:179],v[220:223]
wt vmcnt(10)
s_barrier
.La5_LW2_0:
s_cbranch_execz .La5_LW2_R0
mm v[64:67],a[96:99],v[232:235],v[64:67]
ex v205,v205
mm v[96:99],a[96:99],v[236:239],v[96:99]
rd a[96:99],v248 offset:8192
cv v230,v204,v205
mm v[100:103],a[100:103],v[236:239],v[100:103]
ex v206,v206
mm v[68:71],a[100:103],v[232:235],v[68:71]
rd a[100:103],v248 offset:9216
buffer_load_dword v250, v251, s[64:67], s20 offen sc1
mm v[72:75],a[104:107],v[232:235],v[72:75]
ex v207,v207
mm v[104:107],a[104:107],v[236:239],v[104:107]
rd a[104:107],v248 offset:10240
cv v231,v206,v207
mm v[108:111],a[108:111],v[236:239],v[108:111]
ex v208,v208
mm v[76:79],a[108:111],v[232:235],v[76:79]
rd a[108:111],v248 offset:11264
mm v[80:83],a[112:115],v[232:235],v[80:83]
ex v209,v209
mm v[112:115],a[112:115],v[236:239],v[112:115]
rd a[112:115],v248 offset:12288
mm v[116:119],a[116:119],v[236:239],v[116:119]
)ASM" R"ASM(ex v210,v210
mm v[84:87],a[116:119],v[232:235],v[84:87]
rd a[116:119],v248 offset:13312
mm v[88:91],a[120:123],v[232:235],v[88:91]
ex v211,v211
mm v[120:123],a[120:123],v[236:239],v[120:123]
rd a[120:123],v248 offset:14336
s_bitcmp1_b64 s[98:99], s85
mm v[124:127],a[124:127],v[236:239],v[124:127]
ex v216,v216
mm v[92:95],a[124:127],v[232:235],v[92:95]
rd a[124:127],v248 offset:15360
v_or_b32 v248, s89, v255
v_smfmac_f32_16x16x64_bf16 a[196:199], a[208:211], v[232:239], v241
wt lgkmcnt(4)
v_smfmac_f32_16x16x64_bf16 a[192:195], a[208:211], v[224:231], v241
rd a[64:67],v248 offset:0
mm v[0:3],a[96:99],v[224:227],0
ex v217,v217
mm v[32:35],a[96:99],v[228:231],0
rd a[68:71],v248 offset:1024
cv v232,v208,v209
mm v[36:39],a[100:103],v[228:231],0
ex v218,v218
mm v[4:7],a[100:103],v[224:227],0
rd a[72:75],v248 offset:2048
cv v233,v210,v211
mm v[8:11],a[104:107],v[224:227],0
ex v219,v219
mm v[40:43],a[104:107],v[228:231],0
rd a[76:79],v248 offset:3072
cv v236,v216,v217
mm v[44:47],a[108:111],v[228:231],0
ex v212,v212
mm v[12:15],a[108:111],v[224:227],0
rd a[80:83],v248 offset:4096
wt lgkmcnt(5)
mm v[16:19],a[112:115],v[224:227],0
ex v213,v213
mm v[48:51],a[112:115],v[228:231],0
rd a[84:87],v248 offset:5120
cv v237,v218,v219
mm v[52:55],a[116:119],v[228:231],0
ex v214,v214
mm v[20:23],a[116:119],v[224:227],0
rd a[88:91],v248 offset:6144
cv v234,v212,v213
mm v[24:27],a[120:123],v[224:227],0
ex v215,v215
mm v[56:59],a[120:123],v[228:231],0
rd a[92:95],v248 offset:7168
cv v235,v214,v215
mm v[60:63],a[124:127],v[228:231],0
ex v220,v220
mm v[28:31],a[124:127],v[224:227],0
wt vmcnt(5)
buffer_store_dwordx2 v[252:253], off, s[64:67], s86 sc1
s_mov_b32 s86, 1073741824
s_bitset0_b32 s20, 30
s_cbranch_scc1 .La5_LA_FE0
.La5_LA_FS0:
wt lgkmcnt(4)
mm v[192:195],a[64:67],v[128:131],0
ex v221,v221
mm v[192:195],a[68:71],v[132:135],v[192:195]
cv v238,v220,v221
s_add_u32 s85, s85, 1
mm v[192:195],a[72:75],v[136:139],v[192:195]
ex v222,v222
mm v[192:195],a[76:79],v[140:143],v[192:195]
ld v255,s[60:63],s17 offen offset:0 sc1 lds
mm v[200:203],a[76:79],v[156:159],0
ex v223,v223
mm v[200:203],a[72:75],v[152:155],v[200:203]
cv v239,v222,v223
s_add_u32 s89, s89, 16384
mm v[200:203],a[68:71],v[148:151],v[200:203]
ex v192,v192
mm v[200:203],a[64:67],v[144:147],v[200:203]
wt lgkmcnt(0)
s_and_b32 s89, s89, 0x1ffff
mm v[196:199],a[80:83],v[128:131],0
ex v193,v193
mm v[196:199],a[84:87],v[132:135],v[196:199]
cv v224,v192,v193
mm v[196:199],a[88:91],v[136:139],v[196:199]
ex v194,v194
mm v[196:199],a[92:95],v[140:143],v[196:199]
ld v255,s[60:63],s17 offen offset:1024 sc1 lds
mm v[204:207],a[92:95],v[156:159],0
ex v195,v195
mm v[204:207],a[88:91],v[152:155],v[204:207]
cv v225,v194,v195
mm v[204:207],a[84:87],v[148:151],v[204:207]
ex v200,v200
mm v[204:207],a[80:83],v[144:147],v[204:207]
ld v255,s[60:63],s17 offen offset:2048 sc1 lds
mm v[208:211],a[64:67],v[160:163],0
ex v201,v201
mm v[208:211],a[68:71],v[164:167],v[208:211]
)ASM" R"ASM(cv v228,v200,v201
mm v[208:211],a[72:75],v[168:171],v[208:211]
ex v202,v202
mm v[208:211],a[76:79],v[172:175],v[208:211]
ld v255,s[60:63],s17 offen offset:3072 sc1 lds
mm v[216:219],a[76:79],v[188:191],0
ex v203,v203
mm v[216:219],a[72:75],v[184:187],v[216:219]
cv v229,v202,v203
mm v[216:219],a[68:71],v[180:183],v[216:219]
ex v196,v196
mm v[216:219],a[64:67],v[176:179],v[216:219]
buffer_load_dword a253, v254, s[36:39], s90 offen
mm v[212:215],a[80:83],v[160:163],0
ex v197,v197
mm v[212:215],a[84:87],v[164:167],v[212:215]
cv v226,v196,v197
s_add_u32 s90, s90, s21
mm v[212:215],a[88:91],v[168:171],v[212:215]
ex v198,v198
mm v[212:215],a[92:95],v[172:175],v[212:215]
s_add_u32 s17, s17, s80
s_add_u32 m0, m0, 16384
mm v[220:223],a[92:95],v[188:191],0
ex v199,v199
mm v[220:223],a[88:91],v[184:187],v[220:223]
cv v227,v198,v199
s_and_b32 m0, m0, s78
mm v[220:223],a[84:87],v[180:183],v[220:223]
ex v204,v204
mm v[220:223],a[80:83],v[176:179],v[220:223]
wt vmcnt(11)
s_barrier
mm v[64:67],a[96:99],v[232:235],v[64:67]
ex v205,v205
mm v[96:99],a[96:99],v[236:239],v[96:99]
rd a[96:99],v248 offset:8192
cv v230,v204,v205
mm v[100:103],a[100:103],v[236:239],v[100:103]
ex v206,v206
mm v[68:71],a[100:103],v[232:235],v[68:71]
rd a[100:103],v248 offset:9216
s_cmp_eq_u32 s85, s81
buffer_load_dword v250, v251, s[64:67], s20 offen sc1
mm v[72:75],a[104:107],v[232:235],v[72:75]
ex v207,v207
mm v[104:107],a[104:107],v[236:239],v[104:107]
rd a[104:107],v248 offset:10240
cv v231,v206,v207
mm v[108:111],a[108:111],v[236:239],v[108:111]
ex v208,v208
mm v[76:79],a[108:111],v[232:235],v[76:79]
rd a[108:111],v248 offset:11264
s_cselect_b32 s17, s77, s17
mm v[80:83],a[112:115],v[232:235],v[80:83]
ex v209,v209
mm v[112:115],a[112:115],v[236:239],v[112:115]
rd a[112:115],v248 offset:12288
s_cselect_b32 s80, -8192, s80
mm v[116:119],a[116:119],v[236:239],v[116:119]
ex v210,v210
mm v[84:87],a[116:119],v[232:235],v[84:87]
rd a[116:119],v248 offset:13312
mm v[88:91],a[120:123],v[232:235],v[88:91]
ex v211,v211
mm v[120:123],a[120:123],v[236:239],v[120:123]
rd a[120:123],v248 offset:14336
s_bitcmp1_b64 s[98:99], s85
mm v[124:127],a[124:127],v[236:239],v[124:127]
ex v216,v216
mm v[92:95],a[124:127],v[232:235],v[92:95]
rd a[124:127],v248 offset:15360
v_or_b32 v248, s89, v255
v_smfmac_f32_16x16x64_bf16 a[196:199], a[208:211], v[232:239], v241
wt lgkmcnt(4)
v_smfmac_f32_16x16x64_bf16 a[192:195], a[208:211], v[224:231], v241
rd a[64:67],v248 offset:0
mm v[0:3],a[96:99],v[224:227],v[0:3]
ex v217,v217
mm v[32:35],a[96:99],v[228:231],v[32:35]
rd a[68:71],v248 offset:1024
cv v232,v208,v209
mm v[36:39],a[100:103],v[228:231],v[36:39]
ex v218,v218
mm v[4:7],a[100:103],v[224:227],v[4:7]
rd a[72:75],v248 offset:2048
cv v233,v210,v211
mm v[8:11],a[104:107],v[224:227],v[8:11]
ex v219,v219
mm v[40:43],a[104:107],v[228:231],v[40:43]
rd a[76:79],v248 offset:3072
cv v236,v216,v217
mm v[44:47],a[108:111],v[228:231],v[44:47]
ex v212,v212
mm v[12:15],a[108:111],v[224:227],v[12:15]
rd a[80:83],v248 offset:4096
wt lgkmcnt(5)
mm v[16:19],a[112:115],v[224:227],v[16:19]
ex v213,v213
mm v[48:51],a[112:115],v[228:231],v[48:51]
)ASM" R"ASM(rd a[84:87],v248 offset:5120
cv v237,v218,v219
mm v[52:55],a[116:119],v[228:231],v[52:55]
ex v214,v214
mm v[20:23],a[116:119],v[224:227],v[20:23]
rd a[88:91],v248 offset:6144
cv v234,v212,v213
mm v[24:27],a[120:123],v[224:227],v[24:27]
ex v215,v215
mm v[56:59],a[120:123],v[228:231],v[56:59]
rd a[92:95],v248 offset:7168
cv v235,v214,v215
mm v[60:63],a[124:127],v[228:231],v[60:63]
v_exp_f32_e64 v220, v220
mm v[28:31],a[124:127],v[224:227],v[28:31]
s_cbranch_scc1 .La5_LA_F1E2
.p2align 6
; ---- steady windows
.La5_LA_F1:
wt lgkmcnt(4)
mm v[192:195],a[64:67],v[128:131],0
ex v221,v221
mm v[192:195],a[68:71],v[132:135],v[192:195]
cv v238,v220,v221
s_add_u32 s85, s85, 1
mm v[192:195],a[72:75],v[136:139],v[192:195]
ex v222,v222
mm v[192:195],a[76:79],v[140:143],v[192:195]
ld v255,s[60:63],s17 offen offset:0 sc1 lds
mm v[200:203],a[76:79],v[156:159],0
ex v223,v223
mm v[200:203],a[72:75],v[152:155],v[200:203]
cv v239,v222,v223
s_add_u32 s89, s89, 16384
mm v[200:203],a[68:71],v[148:151],v[200:203]
ex v192,v192
mm v[200:203],a[64:67],v[144:147],v[200:203]
wt lgkmcnt(0)
s_and_b32 s89, s89, 0x1ffff
mm v[196:199],a[80:83],v[128:131],0
ex v193,v193
mm v[196:199],a[84:87],v[132:135],v[196:199]
cv v224,v192,v193
mm v[196:199],a[88:91],v[136:139],v[196:199]
ex v194,v194
mm v[196:199],a[92:95],v[140:143],v[196:199]
ld v255,s[60:63],s17 offen offset:1024 sc1 lds
mm v[204:207],a[92:95],v[156:159],0
ex v195,v195
mm v[204:207],a[88:91],v[152:155],v[204:207]
cv v225,v194,v195
mm v[204:207],a[84:87],v[148:151],v[204:207]
ex v200,v200
mm v[204:207],a[80:83],v[144:147],v[204:207]
ld v255,s[60:63],s17 offen offset:2048 sc1 lds
mm v[208:211],a[64:67],v[160:163],0
ex v201,v201
mm v[208:211],a[68:71],v[164:167],v[208:211]
cv v228,v200,v201
mm v[208:211],a[72:75],v[168:171],v[208:211]
ex v202,v202
mm v[208:211],a[76:79],v[172:175],v[208:211]
ld v255,s[60:63],s17 offen offset:3072 sc1 lds
mm v[216:219],a[76:79],v[188:191],0
ex v203,v203
mm v[216:219],a[72:75],v[184:187],v[216:219]
cv v229,v202,v203
mm v[216:219],a[68:71],v[180:183],v[216:219]
ex v196,v196
mm v[216:219],a[64:67],v[176:179],v[216:219]
buffer_load_dword a253, v254, s[36:39], s90 offen
mm v[212:215],a[80:83],v[160:163],0
ex v197,v197
mm v[212:215],a[84:87],v[164:167],v[212:215]
cv v226,v196,v197
s_add_u32 s90, s90, s21
mm v[212:215],a[88:91],v[168:171],v[212:215]
ex v198,v198
mm v[212:215],a[92:95],v[172:175],v[212:215]
s_add_u32 s17, s17, s80
s_add_u32 m0, m0, 16384
mm v[220:223],a[92:95],v[188:191],0
ex v199,v199
mm v[220:223],a[88:91],v[184:187],v[220:223]
cv v227,v198,v199
s_and_b32 m0, m0, s78
mm v[220:223],a[84:87],v[180:183],v[220:223]
ex v204,v204
mm v[220:223],a[80:83],v[176:179],v[220:223]
wt vmcnt(11)
s_barrier
mm v[64:67],a[96:99],v[232:235],v[64:67]
ex v205,v205
mm v[96:99],a[96:99],v[236:239],v[96:99]
rd a[96:99],v248 offset:8192
cv v230,v204,v205
mm v[100:103],a[100:103],v[236:239],v[100:103]
ex v206,v206
mm v[68:71],a[100:103],v[232:235],v[68:71]
rd a[100:103],v248 offset:9216
)ASM" R"ASM(s_cmp_eq_u32 s85, s81
mm v[72:75],a[104:107],v[232:235],v[72:75]
ex v207,v207
mm v[104:107],a[104:107],v[236:239],v[104:107]
rd a[104:107],v248 offset:10240
cv v231,v206,v207
mm v[108:111],a[108:111],v[236:239],v[108:111]
ex v208,v208
mm v[76:79],a[108:111],v[232:235],v[76:79]
rd a[108:111],v248 offset:11264
s_cselect_b32 s17, s77, s17
mm v[80:83],a[112:115],v[232:235],v[80:83]
ex v209,v209
mm v[112:115],a[112:115],v[236:239],v[112:115]
rd a[112:115],v248 offset:12288
s_cselect_b32 s80, -8192, s80
mm v[116:119],a[116:119],v[236:239],v[116:119]
ex v210,v210
mm v[84:87],a[116:119],v[232:235],v[84:87]
rd a[116:119],v248 offset:13312
mm v[88:91],a[120:123],v[232:235],v[88:91]
ex v211,v211
mm v[120:123],a[120:123],v[236:239],v[120:123]
rd a[120:123],v248 offset:14336
s_bitcmp1_b64 s[98:99], s85
mm v[124:127],a[124:127],v[236:239],v[124:127]
ex v216,v216
mm v[92:95],a[124:127],v[232:235],v[92:95]
rd a[124:127],v248 offset:15360
v_or_b32 v248, s89, v255
v_smfmac_f32_16x16x64_bf16 a[196:199], a[208:211], v[232:239], v241
wt lgkmcnt(4)
v_smfmac_f32_16x16x64_bf16 a[192:195], a[208:211], v[224:231], v241
rd a[64:67],v248 offset:0
mm v[0:3],a[96:99],v[224:227],v[0:3]
ex v217,v217
mm v[32:35],a[96:99],v[228:231],v[32:35]
rd a[68:71],v248 offset:1024
cv v232,v208,v209
mm v[36:39],a[100:103],v[228:231],v[36:39]
ex v218,v218
mm v[4:7],a[100:103],v[224:227],v[4:7]
rd a[72:75],v248 offset:2048
cv v233,v210,v211
mm v[8:11],a[104:107],v[224:227],v[8:11]
ex v219,v219
mm v[40:43],a[104:107],v[228:231],v[40:43]
rd a[76:79],v248 offset:3072
cv v236,v216,v217
mm v[44:47],a[108:111],v[228:231],v[44:47]
ex v212,v212
mm v[12:15],a[108:111],v[224:227],v[12:15]
rd a[80:83],v248 offset:4096
wt lgkmcnt(5)
mm v[16:19],a[112:115],v[224:227],v[16:19]
ex v213,v213
mm v[48:51],a[112:115],v[228:231],v[48:51]
rd a[84:87],v248 offset:5120
cv v237,v218,v219
mm v[52:55],a[116:119],v[228:231],v[52:55]
ex v214,v214
mm v[20:23],a[116:119],v[224:227],v[20:23]
rd a[88:91],v248 offset:6144
cv v234,v212,v213
mm v[24:27],a[120:123],v[224:227],v[24:27]
ex v215,v215
mm v[56:59],a[120:123],v[228:231],v[56:59]
rd a[92:95],v248 offset:7168
cv v235,v214,v215
mm v[60:63],a[124:127],v[228:231],v[60:63]
ex v220,v220
mm v[28:31],a[124:127],v[224:227],v[28:31]
s_cbranch_scc1 .La5_LA_F1E2
wt lgkmcnt(4)
mm v[192:195],a[64:67],v[128:131],0
ex v221,v221
mm v[192:195],a[68:71],v[132:135],v[192:195]
cv v238,v220,v221
s_add_u32 s85, s85, 1
mm v[192:195],a[72:75],v[136:139],v[192:195]
ex v222,v222
mm v[192:195],a[76:79],v[140:143],v[192:195]
ld v255,s[60:63],s17 offen offset:0 sc1 lds
mm v[200:203],a[76:79],v[156:159],0
ex v223,v223
mm v[200:203],a[72:75],v[152:155],v[200:203]
cv v239,v222,v223
s_add_u32 s89, s89, 16384
mm v[200:203],a[68:71],v[148:151],v[200:203]
ex v192,v192
mm v[200:203],a[64:67],v[144:147],v[200:203]
wt lgkmcnt(0)
s_and_b32 s89, s89, 0x1ffff
mm v[196:199],a[80:83],v[128:131],0
ex v193,v193
mm v[196:199],a[84:87],v[132:135],v[196:199]
cv v224,v192,v193
mm v[196:199],a[88:91],v[136:139],v[196:199]
ex v194,v194
mm v[196:199],a[92:95],v[140:143],v[196:199]
)ASM" R"ASM(ld v255,s[60:63],s17 offen offset:1024 sc1 lds
mm v[204:207],a[92:95],v[156:159],0
ex v195,v195
mm v[204:207],a[88:91],v[152:155],v[204:207]
cv v225,v194,v195
mm v[204:207],a[84:87],v[148:151],v[204:207]
ex v200,v200
mm v[204:207],a[80:83],v[144:147],v[204:207]
ld v255,s[60:63],s17 offen offset:2048 sc1 lds
mm v[208:211],a[64:67],v[160:163],0
ex v201,v201
mm v[208:211],a[68:71],v[164:167],v[208:211]
cv v228,v200,v201
mm v[208:211],a[72:75],v[168:171],v[208:211]
ex v202,v202
mm v[208:211],a[76:79],v[172:175],v[208:211]
ld v255,s[60:63],s17 offen offset:3072 sc1 lds
mm v[216:219],a[76:79],v[188:191],0
ex v203,v203
mm v[216:219],a[72:75],v[184:187],v[216:219]
cv v229,v202,v203
mm v[216:219],a[68:71],v[180:183],v[216:219]
ex v196,v196
mm v[216:219],a[64:67],v[176:179],v[216:219]
buffer_load_dword a253, v254, s[36:39], s90 offen
mm v[212:215],a[80:83],v[160:163],0
ex v197,v197
mm v[212:215],a[84:87],v[164:167],v[212:215]
cv v226,v196,v197
s_add_u32 s90, s90, s21
mm v[212:215],a[88:91],v[168:171],v[212:215]
ex v198,v198
mm v[212:215],a[92:95],v[172:175],v[212:215]
s_add_u32 s17, s17, s80
s_add_u32 m0, m0, 16384
mm v[220:223],a[92:95],v[188:191],0
ex v199,v199
mm v[220:223],a[88:91],v[184:187],v[220:223]
cv v227,v198,v199
s_and_b32 m0, m0, s78
mm v[220:223],a[84:87],v[180:183],v[220:223]
ex v204,v204
mm v[220:223],a[80:83],v[176:179],v[220:223]
wt vmcnt(10)
s_barrier
mm v[64:67],a[96:99],v[232:235],v[64:67]
ex v205,v205
mm v[96:99],a[96:99],v[236:239],v[96:99]
rd a[96:99],v248 offset:8192
cv v230,v204,v205
mm v[100:103],a[100:103],v[236:239],v[100:103]
ex v206,v206
mm v[68:71],a[100:103],v[232:235],v[68:71]
rd a[100:103],v248 offset:9216
s_cmp_eq_u32 s85, s81
mm v[72:75],a[104:107],v[232:235],v[72:75]
ex v207,v207
mm v[104:107],a[104:107],v[236:239],v[104:107]
rd a[104:107],v248 offset:10240
cv v231,v206,v207
mm v[108:111],a[108:111],v[236:239],v[108:111]
ex v208,v208
mm v[76:79],a[108:111],v[232:235],v[76:79]
rd a[108:111],v248 offset:11264
s_cselect_b32 s17, s77, s17
mm v[80:83],a[112:115],v[232:235],v[80:83]
ex v209,v209
mm v[112:115],a[112:115],v[236:239],v[112:115]
rd a[112:115],v248 offset:12288
s_cselect_b32 s80, -8192, s80
mm v[116:119],a[116:119],v[236:239],v[116:119]
ex v210,v210
mm v[84:87],a[116:119],v[232:235],v[84:87]
rd a[116:119],v248 offset:13312
mm v[88:91],a[120:123],v[232:235],v[88:91]
ex v211,v211
mm v[120:123],a[120:123],v[236:239],v[120:123]
rd a[120:123],v248 offset:14336
s_bitcmp1_b64 s[98:99], s85
mm v[124:127],a[124:127],v[236:239],v[124:127]
ex v216,v216
mm v[92:95],a[124:127],v[232:235],v[92:95]
rd a[124:127],v248 offset:15360
v_or_b32 v248, s89, v255
v_smfmac_f32_16x16x64_bf16 a[196:199], a[208:211], v[232:239], v241
wt lgkmcnt(4)
v_smfmac_f32_16x16x64_bf16 a[192:195], a[208:211], v[224:231], v241
rd a[64:67],v248 offset:0
mm v[0:3],a[96:99],v[224:227],v[0:3]
ex v217,v217
mm v[32:35],a[96:99],v[228:231],v[32:35]
rd a[68:71],v248 offset:1024
cv v232,v208,v209
mm v[36:39],a[100:103],v[228:231],v[36:39]
ex v218,v218
mm v[4:7],a[100:103],v[224:227],v[4:7]
rd a[72:75],v248 offset:2048
cv v233,v210,v211
mm v[8:11],a[104:107],v[224:227],v[8:11]
ex v219,v219
)ASM" R"ASM(mm v[40:43],a[104:107],v[228:231],v[40:43]
rd a[76:79],v248 offset:3072
cv v236,v216,v217
mm v[44:47],a[108:111],v[228:231],v[44:47]
ex v212,v212
mm v[12:15],a[108:111],v[224:227],v[12:15]
rd a[80:83],v248 offset:4096
wt lgkmcnt(5)
mm v[16:19],a[112:115],v[224:227],v[16:19]
ex v213,v213
mm v[48:51],a[112:115],v[228:231],v[48:51]
rd a[84:87],v248 offset:5120
cv v237,v218,v219
mm v[52:55],a[116:119],v[228:231],v[52:55]
ex v214,v214
mm v[20:23],a[116:119],v[224:227],v[20:23]
rd a[88:91],v248 offset:6144
cv v234,v212,v213
mm v[24:27],a[120:123],v[224:227],v[24:27]
ex v215,v215
mm v[56:59],a[120:123],v[228:231],v[56:59]
rd a[92:95],v248 offset:7168
cv v235,v214,v215
mm v[60:63],a[124:127],v[228:231],v[60:63]
ex v220,v220
mm v[28:31],a[124:127],v[224:227],v[28:31]
.La5_LA_F1E:
s_cbranch_scc0 .La5_LA_F1
.La5_LA_F1E2:
s_cmp_eq_u32 s85, s82
s_cbranch_scc1 .La5_LA_F1X
s_call_b64 s[24:25], .La5_LA_EVENT
s_branch .La5_LA_F1
.La5_LA_FE0:
s_nop 0
s_nop 0
.La5_LW2_1:
s_cbranch_execz .La5_LW2_R1
s_cmp_eq_u32 s85, s82
s_cbranch_scc1 .La5_LA_F1X
s_call_b64 s[24:25], .La5_LA_EVENT
s_branch .La5_LA_FS0
; ---- hand-offs between windows
.La5_LA_EVENT:
s_cmp_lg_u32 s85, 2
s_nop 0
s_nop 0
s_cbranch_scc1 .La5_LA_E1
wt vmcnt(0)
v_cmp_eq_u32 vcc, v250, v249
s_mov_b32 s17, s16
s_mov_b32 s78, 0x1ffff
s_lshl_b32 m0, s3, 12
s_or_b32 vcc_hi, vcc_hi, s79
s_cmp_eq_u64 vcc, -1
s_cbranch_scc0 .La5_LA_KSPIN
.La5_LA_KOK:
ld v255,s[60:63],s17 offen offset:0 sc1 lds
ld v255,s[60:63],s17 offen offset:1024 sc1 lds
ld v255,s[60:63],s17 offen offset:2048 sc1 lds
ld v255,s[60:63],s17 offen offset:3072 sc1 lds
s_add_u32 s17, s17, s80
s_add_u32 m0, m0, 16384
s_mov_b32 s20, s19
v_and_b32 v251, 4, v251
s_cmp_lg_u32 s3, 0
s_cbranch_scc1 .La5_LA_POLL
s_nop 0
ld v255,s[60:63],s17 offen offset:0 sc1 lds
ld v255,s[60:63],s17 offen offset:1024 sc1 lds
ld v255,s[60:63],s17 offen offset:2048 sc1 lds
ld v255,s[60:63],s17 offen offset:3072 sc1 lds
s_add_u32 s17, s17, s80
s_add_u32 m0, m0, 16384
.La5_LA_POLL:
s_cmp_lg_u32 s22, 0
s_nop 0
s_nop 0
s_cbranch_scc1 .La5_LA_WP
s_mov_b64 exec, 0
s_cmp_eq_u32 s3, 0
s_cbranch_scc1 .La5_LW5_0
s_cmp_eq_u32 s3, 1
s_cbranch_scc1 .La5_LW5_1
s_cmp_eq_u32 s3, 2
s_cbranch_scc1 .La5_LW5_2
s_branch .La5_LW5_3
.La5_LW5_R0:
s_mov_b64 exec, -1
.La5_LA_RETURN:
s_setpc_b64 s[24:25]
.La5_LA_WP:
s_mov_b64 exec, 0
s_cmp_eq_u32 s3, 0
s_cbranch_scc1 .La5_LW6_0
s_cmp_eq_u32 s3, 1
s_cbranch_scc1 .La5_LW6_1
s_cmp_eq_u32 s3, 2
s_cbranch_scc1 .La5_LW6_2
s_branch .La5_LW6_3
)ASM" R"ASM(.La5_LW6_R0:
s_mov_b64 exec, -1
s_cmp_eq_u32 s73, 1
s_cbranch_scc1 .La5_LA_E0
buffer_load_dword v250, v251, s[64:67], s20 offen sc1
s_setpc_b64 s[24:25]
.La5_LA_E0:
s_mov_b32 s54, 16384
ld a[0:3],v255,s[52:55],0 offen offset:0 sc1
ld a[4:7],v255,s[52:55],0 offen offset:1024 sc1
ld a[8:11],v255,s[52:55],0 offen offset:2048 sc1
.La5_LW2_2:
s_cbranch_execz .La5_LW2_R0
ld a[12:15],v255,s[52:55],0 offen offset:3072 sc1
ld a[16:19],v255,s[52:55],s95 offen offset:0 sc1
ld a[20:23],v255,s[52:55],s95 offen offset:1024 sc1
s_setpc_b64 s[24:25]
.La5_LA_E1:
ld a[24:27],v255,s[52:55],s95 offen offset:2048 sc1
ld a[28:31],v255,s[52:55],s95 offen offset:3072 sc1
ld a[32:35],v255,s[52:55],s96 offen offset:0 sc1
ld a[36:39],v255,s[52:55],s96 offen offset:1024 sc1
ld a[40:43],v255,s[52:55],s96 offen offset:2048 sc1
ld a[44:47],v255,s[52:55],s96 offen offset:3072 sc1
ld a[48:51],v255,s[52:55],s97 offen offset:0 sc1
ld a[52:55],v255,s[52:55],s97 offen offset:1024 sc1
ld a[56:59],v255,s[52:55],s97 offen offset:2048 sc1
ld a[60:63],v255,s[52:55],s97 offen offset:3072 sc1
s_setpc_b64 s[24:25]
; ---- helper: the partner-s q rows arrive during the last windows of the own block
.La5_LA_F1X:
s_cmp_eq_u32 s75, 0
s_nop 0
s_cbranch_scc1 .La5_LA_DC3
s_cmp_eq_u32 s22, 0
s_cbranch_scc1 .La5_LA_DC
s_cmp_eq_u32 s73, 1
s_cbranch_scc1 .La5_LA_F1Y
s_nop 0
v_cmp_eq_u32 vcc, v250, v249
s_mov_b32 s54, 16384
s_and_b32 s92, vcc_lo, 3
s_cmp_eq_u32 s92, 3
s_cbranch_scc0 .La5_LA_QSPIN
.La5_LA_QOK:
wt lgkmcnt(4)
mm v[192:195],a[64:67],v[128:131],0
ex v221,v221
mm v[192:195],a[68:71],v[132:135],v[192:195]
cv v238,v220,v221
s_add_u32 s85, s85, 1
mm v[192:195],a[72:75],v[136:139],v[192:195]
ex v222,v222
mm v[192:195],a[76:79],v[140:143],v[192:195]
ld v255,s[60:63],s17 offen offset:0 sc1 lds
mm v[200:203],a[76:79],v[156:159],0
ex v223,v223
mm v[200:203],a[72:75],v[152:155],v[200:203]
cv v239,v222,v223
s_add_u32 s89, s89, 16384
mm v[200:203],a[68:71],v[148:151],v[200:203]
ex v192,v192
mm v[200:203],a[64:67],v[144:147],v[200:203]
wt lgkmcnt(0)
s_and_b32 s89, s89, 0x1ffff
mm v[196:199],a[80:83],v[128:131],0
ex v193,v193
mm v[196:199],a[84:87],v[132:135],v[196:199]
cv v224,v192,v193
mm v[196:199],a[88:91],v[136:139],v[196:199]
ex v194,v194
mm v[196:199],a[92:95],v[140:143],v[196:199]
ld v255,s[60:63],s17 offen offset:1024 sc1 lds
mm v[204:207],a[92:95],v[156:159],0
ex v195,v195
mm v[204:207],a[88:91],v[152:155],v[204:207]
cv v225,v194,v195
mm v[204:207],a[84:87],v[148:151],v[204:207]
ex v200,v200
mm v[204:207],a[80:83],v[144:147],v[204:207]
ld v255,s[60:63],s17 offen offset:2048 sc1 lds
mm v[208:211],a[64:67],v[160:163],0
ex v201,v201
mm v[208:211],a[68:71],v[164:167],v[208:211]
cv v228,v200,v201
mm v[208:211],a[72:75],v[168:171],v[208:211]
ex v202,v202
mm v[208:211],a[76:79],v[172:175],v[208:211]
ld v255,s[60:63],s17 offen offset:3072 sc1 lds
mm v[216:219],a[76:79],v[188:191],0
ex v203,v203
mm v[216:219],a[72:75],v[184:187],v[216:219]
cv v229,v202,v203
mm v[216:219],a[68:71],v[180:183],v[216:219]
ex v196,v196
mm v[216:219],a[64:67],v[176:179],v[216:219]
ld a[0:3],v255,s[52:55],0 offen offset:0 sc1
mm v[212:215],a[80:83],v[160:163],0
ex v197,v197
mm v[212:215],a[84:87],v[164:167],v[212:215]
)ASM" R"ASM(cv v226,v196,v197
s_add_u32 s17, s17, s80
mm v[212:215],a[88:91],v[168:171],v[212:215]
ex v198,v198
mm v[212:215],a[92:95],v[172:175],v[212:215]
s_add_u32 m0, m0, 16384
s_and_b32 m0, m0, s78
ld a[4:7],v255,s[52:55],0 offen offset:1024 sc1
mm v[220:223],a[92:95],v[188:191],0
ex v199,v199
mm v[220:223],a[88:91],v[184:187],v[220:223]
cv v227,v198,v199
mm v[220:223],a[84:87],v[180:183],v[220:223]
ex v204,v204
mm v[220:223],a[80:83],v[176:179],v[220:223]
wt vmcnt(12)
s_barrier
mm v[64:67],a[96:99],v[232:235],v[64:67]
ex v205,v205
mm v[96:99],a[96:99],v[236:239],v[96:99]
rd a[96:99],v248 offset:8192
cv v230,v204,v205
mm v[100:103],a[100:103],v[236:239],v[100:103]
ex v206,v206
mm v[68:71],a[100:103],v[232:235],v[68:71]
rd a[100:103],v248 offset:9216
ld a[8:11],v255,s[52:55],0 offen offset:2048 sc1
mm v[72:75],a[104:107],v[232:235],v[72:75]
ex v207,v207
mm v[104:107],a[104:107],v[236:239],v[104:107]
rd a[104:107],v248 offset:10240
cv v231,v206,v207
mm v[108:111],a[108:111],v[236:239],v[108:111]
ex v208,v208
mm v[76:79],a[108:111],v[232:235],v[76:79]
rd a[108:111],v248 offset:11264
ld a[12:15],v255,s[52:55],0 offen offset:3072 sc1
mm v[80:83],a[112:115],v[232:235],v[80:83]
ex v209,v209
mm v[112:115],a[112:115],v[236:239],v[112:115]
rd a[112:115],v248 offset:12288
mm v[116:119],a[116:119],v[236:239],v[116:119]
ex v210,v210
mm v[84:87],a[116:119],v[232:235],v[84:87]
rd a[116:119],v248 offset:13312
ld a[16:19],v255,s[52:55],s95 offen offset:0 sc1
mm v[88:91],a[120:123],v[232:235],v[88:91]
ex v211,v211
mm v[120:123],a[120:123],v[236:239],v[120:123]
rd a[120:123],v248 offset:14336
s_bitcmp1_b64 s[98:99], s85
mm v[124:127],a[124:127],v[236:239],v[124:127]
ex v216,v216
mm v[92:95],a[124:127],v[232:235],v[92:95]
rd a[124:127],v248 offset:15360
v_or_b32 v248, s89, v255
v_smfmac_f32_16x16x64_bf16 a[196:199], a[208:211], v[232:239], v241
wt lgkmcnt(4)
v_smfmac_f32_16x16x64_bf16 a[192:195], a[208:211], v[224:231], v241
rd a[64:67],v248 offset:0
mm v[0:3],a[96:99],v[224:227],v[0:3]
ex v217,v217
mm v[32:35],a[96:99],v[228:231],v[32:35]
rd a[68:71],v248 offset:1024
cv v232,v208,v209
mm v[36:39],a[100:103],v[228:231],v[36:39]
ex v218,v218
mm v[4:7],a[100:103],v[224:227],v[4:7]
rd a[72:75],v248 offset:2048
cv v233,v210,v211
mm v[8:11],a[104:107],v[224:227],v[8:11]
ex v219,v219
mm v[40:43],a[104:107],v[228:231],v[40:43]
rd a[76:79],v248 offset:3072
cv v236,v216,v217
mm v[44:47],a[108:111],v[228:231],v[44:47]
ex v212,v212
mm v[12:15],a[108:111],v[224:227],v[12:15]
rd a[80:83],v248 offset:4096
wt lgkmcnt(5)
mm v[16:19],a[112:115],v[224:227],v[16:19]
ex v213,v213
mm v[48:51],a[112:115],v[228:231],v[48:51]
rd a[84:87],v248 offset:5120
cv v237,v218,v219
mm v[52:55],a[116:119],v[228:231],v[52:55]
ex v214,v214
mm v[20:23],a[116:119],v[224:227],v[20:23]
rd a[88:91],v248 offset:6144
cv v234,v212,v213
mm v[24:27],a[120:123],v[224:227],v[24:27]
ex v215,v215
mm v[56:59],a[120:123],v[228:231],v[56:59]
rd a[92:95],v248 offset:7168
cv v235,v214,v215
mm v[60:63],a[124:127],v[228:231],v[60:63]
ex v220,v220
mm v[28:31],a[124:127],v[224:227],v[28:31]
ld a[20:23],v255,s[52:55],s95 offen offset:1024 sc1
wt lgkmcnt(4)
)ASM" R"ASM(mm v[192:195],a[64:67],v[128:131],0
ex v221,v221
mm v[192:195],a[68:71],v[132:135],v[192:195]
cv v238,v220,v221
s_add_u32 s85, s85, 1
mm v[192:195],a[72:75],v[136:139],v[192:195]
ex v222,v222
mm v[192:195],a[76:79],v[140:143],v[192:195]
ld v255,s[60:63],s17 offen offset:0 sc1 lds
mm v[200:203],a[76:79],v[156:159],0
ex v223,v223
mm v[200:203],a[72:75],v[152:155],v[200:203]
cv v239,v222,v223
s_add_u32 s89, s89, 16384
mm v[200:203],a[68:71],v[148:151],v[200:203]
ex v192,v192
mm v[200:203],a[64:67],v[144:147],v[200:203]
wt lgkmcnt(0)
s_and_b32 s89, s89, 0x1ffff
ld a[24:27],v255,s[52:55],s95 offen offset:2048 sc1
mm v[196:199],a[80:83],v[128:131],0
ex v193,v193
mm v[196:199],a[84:87],v[132:135],v[196:199]
cv v224,v192,v193
mm v[196:199],a[88:91],v[136:139],v[196:199]
ex v194,v194
mm v[196:199],a[92:95],v[140:143],v[196:199]
ld v255,s[60:63],s17 offen offset:1024 sc1 lds
mm v[204:207],a[92:95],v[156:159],0
ex v195,v195
mm v[204:207],a[88:91],v[152:155],v[204:207]
cv v225,v194,v195
mm v[204:207],a[84:87],v[148:151],v[204:207]
ex v200,v200
mm v[204:207],a[80:83],v[144:147],v[204:207]
ld v255,s[60:63],s17 offen offset:2048 sc1 lds
mm v[208:211],a[64:67],v[160:163],0
ex v201,v201
mm v[208:211],a[68:71],v[164:167],v[208:211]
cv v228,v200,v201
mm v[208:211],a[72:75],v[168:171],v[208:211]
ex v202,v202
mm v[208:211],a[76:79],v[172:175],v[208:211]
ld v255,s[60:63],s17 offen offset:3072 sc1 lds
mm v[216:219],a[76:79],v[188:191],0
ex v203,v203
mm v[216:219],a[72:75],v[184:187],v[216:219]
cv v229,v202,v203
mm v[216:219],a[68:71],v[180:183],v[216:219]
ex v196,v196
mm v[216:219],a[64:67],v[176:179],v[216:219]
ld a[28:31],v255,s[52:55],s95 offen offset:3072 sc1
mm v[212:215],a[80:83],v[160:163],0
ex v197,v197
mm v[212:215],a[84:87],v[164:167],v[212:215]
cv v226,v196,v197
s_add_u32 s17, s17, s80
mm v[212:215],a[88:91],v[168:171],v[212:215]
ex v198,v198
mm v[212:215],a[92:95],v[172:175],v[212:215]
s_add_u32 m0, m0, 16384
s_and_b32 m0, m0, s78
ld a[32:35],v255,s[52:55],s96 offen offset:0 sc1
mm v[220:223],a[92:95],v[188:191],0
ex v199,v199
mm v[220:223],a[88:91],v[184:187],v[220:223]
cv v227,v198,v199
mm v[220:223],a[84:87],v[180:183],v[220:223]
ex v204,v204
mm v[220:223],a[80:83],v[176:179],v[220:223]
wt vmcnt(18)
s_barrier
mm v[64:67],a[96:99],v[232:235],v[64:67]
ex v205,v205
mm v[96:99],a[96:99],v[236:239],v[96:99]
rd a[96:99],v248 offset:8192
cv v230,v204,v205
mm v[100:103],a[100:103],v[236:239],v[100:103]
ex v206,v206
mm v[68:71],a[100:103],v[232:235],v[68:71]
rd a[100:103],v248 offset:9216
ld a[36:39],v255,s[52:55],s96 offen offset:1024 sc1
mm v[72:75],a[104:107],v[232:235],v[72:75]
ex v207,v207
mm v[104:107],a[104:107],v[236:239],v[104:107]
rd a[104:107],v248 offset:10240
cv v231,v206,v207
mm v[108:111],a[108:111],v[236:239],v[108:111]
ex v208,v208
mm v[76:79],a[108:111],v[232:235],v[76:79]
rd a[108:111],v248 offset:11264
ld a[40:43],v255,s[52:55],s96 offen offset:2048 sc1
mm v[80:83],a[112:115],v[232:235],v[80:83]
ex v209,v209
mm v[112:115],a[112:115],v[236:239],v[112:115]
rd a[112:115],v248 offset:12288
mm v[116:119],a[116:119],v[236:239],v[116:119]
ex v210,v210
mm v[84:87],a[116:119],v[232:235],v[84:87]
rd a[116:119],v248 offset:13312
)ASM" R"ASM(ld a[44:47],v255,s[52:55],s96 offen offset:3072 sc1
mm v[88:91],a[120:123],v[232:235],v[88:91]
ex v211,v211
mm v[120:123],a[120:123],v[236:239],v[120:123]
rd a[120:123],v248 offset:14336
s_bitcmp1_b64 s[98:99], s85
mm v[124:127],a[124:127],v[236:239],v[124:127]
ex v216,v216
mm v[92:95],a[124:127],v[232:235],v[92:95]
rd a[124:127],v248 offset:15360
v_or_b32 v248, s89, v255
v_smfmac_f32_16x16x64_bf16 a[196:199], a[208:211], v[232:239], v241
wt lgkmcnt(4)
v_smfmac_f32_16x16x64_bf16 a[192:195], a[208:211], v[224:231], v241
rd a[64:67],v248 offset:0
mm v[0:3],a[96:99],v[224:227],v[0:3]
ex v217,v217
mm v[32:35],a[96:99],v[228:231],v[32:35]
rd a[68:71],v248 offset:1024
cv v232,v208,v209
mm v[36:39],a[100:103],v[228:231],v[36:39]
ex v218,v218
mm v[4:7],a[100:103],v[224:227],v[4:7]
rd a[72:75],v248 offset:2048
cv v233,v210,v211
mm v[8:11],a[104:107],v[224:227],v[8:11]
ex v219,v219
mm v[40:43],a[104:107],v[228:231],v[40:43]
rd a[76:79],v248 offset:3072
cv v236,v216,v217
mm v[44:47],a[108:111],v[228:231],v[44:47]
ex v212,v212
mm v[12:15],a[108:111],v[224:227],v[12:15]
rd a[80:83],v248 offset:4096
wt lgkmcnt(5)
mm v[16:19],a[112:115],v[224:227],v[16:19]
ex v213,v213
mm v[48:51],a[112:115],v[228:231],v[48:51]
rd a[84:87],v248 offset:5120
cv v237,v218,v219
mm v[52:55],a[116:119],v[228:231],v[52:55]
ex v214,v214
mm v[20:23],a[116:119],v[224:227],v[20:23]
rd a[88:91],v248 offset:6144
cv v234,v212,v213
mm v[24:27],a[120:123],v[224:227],v[24:27]
ex v215,v215
mm v[56:59],a[120:123],v[228:231],v[56:59]
rd a[92:95],v248 offset:7168
cv v235,v214,v215
mm v[60:63],a[124:127],v[228:231],v[60:63]
ex v220,v220
mm v[28:31],a[124:127],v[224:227],v[28:31]
ld a[48:51],v255,s[52:55],s97 offen offset:0 sc1
wt lgkmcnt(4)
mm v[192:195],a[64:67],v[128:131],0
ex v221,v221
mm v[192:195],a[68:71],v[132:135],v[192:195]
cv v238,v220,v221
s_add_u32 s85, s85, 1
mm v[192:195],a[72:75],v[136:139],v[192:195]
ex v222,v222
mm v[192:195],a[76:79],v[140:143],v[192:195]
ld v255,s[60:63],s17 offen offset:0 sc1 lds
mm v[200:203],a[76:79],v[156:159],0
ex v223,v223
mm v[200:203],a[72:75],v[152:155],v[200:203]
cv v239,v222,v223
s_add_u32 s89, s89, 16384
mm v[200:203],a[68:71],v[148:151],v[200:203]
ex v192,v192
mm v[200:203],a[64:67],v[144:147],v[200:203]
wt lgkmcnt(0)
s_and_b32 s89, s89, 0x1ffff
ld a[52:55],v255,s[52:55],s97 offen offset:1024 sc1
mm v[196:199],a[80:83],v[128:131],0
ex v193,v193
mm v[196:199],a[84:87],v[132:135],v[196:199]
cv v224,v192,v193
mm v[196:199],a[88:91],v[136:139],v[196:199]
ex v194,v194
mm v[196:199],a[92:95],v[140:143],v[196:199]
ld v255,s[60:63],s17 offen offset:1024 sc1 lds
mm v[204:207],a[92:95],v[156:159],0
ex v195,v195
mm v[204:207],a[88:91],v[152:155],v[204:207]
cv v225,v194,v195
mm v[204:207],a[84:87],v[148:151],v[204:207]
ex v200,v200
mm v[204:207],a[80:83],v[144:147],v[204:207]
ld v255,s[60:63],s17 offen offset:2048 sc1 lds
mm v[208:211],a[64:67],v[160:163],0
ex v201,v201
mm v[208:211],a[68:71],v[164:167],v[208:211]
cv v228,v200,v201
mm v[208:211],a[72:75],v[168:171],v[208:211]
ex v202,v202
mm v[208:211],a[76:79],v[172:175],v[208:211]
ld v255,s[60:63],s17 offen offset:3072 sc1 lds
mm v[216:219],a[76:79],v[188:191],0
)ASM" R"ASM(ex v203,v203
mm v[216:219],a[72:75],v[184:187],v[216:219]
cv v229,v202,v203
mm v[216:219],a[68:71],v[180:183],v[216:219]
ex v196,v196
mm v[216:219],a[64:67],v[176:179],v[216:219]
ld a[56:59],v255,s[52:55],s97 offen offset:2048 sc1
mm v[212:215],a[80:83],v[160:163],0
ex v197,v197
mm v[212:215],a[84:87],v[164:167],v[212:215]
cv v226,v196,v197
s_add_u32 s17, s17, s80
mm v[212:215],a[88:91],v[168:171],v[212:215]
ex v198,v198
mm v[212:215],a[92:95],v[172:175],v[212:215]
s_add_u32 m0, m0, 16384
s_and_b32 m0, m0, s78
ld a[60:63],v255,s[52:55],s97 offen offset:3072 sc1
mm v[220:223],a[92:95],v[188:191],0
ex v199,v199
mm v[220:223],a[88:91],v[184:187],v[220:223]
cv v227,v198,v199
mm v[220:223],a[84:87],v[180:183],v[220:223]
ex v204,v204
mm v[220:223],a[80:83],v[176:179],v[220:223]
wt vmcnt(24)
s_barrier
mm v[64:67],a[96:99],v[232:235],v[64:67]
ex v205,v205
mm v[96:99],a[96:99],v[236:239],v[96:99]
rd a[96:99],v248 offset:8192
cv v230,v204,v205
mm v[100:103],a[100:103],v[236:239],v[100:103]
ex v206,v206
mm v[68:71],a[100:103],v[232:235],v[68:71]
rd a[100:103],v248 offset:9216
mm v[72:75],a[104:107],v[232:235],v[72:75]
ex v207,v207
mm v[104:107],a[104:107],v[236:239],v[104:107]
rd a[104:107],v248 offset:10240
cv v231,v206,v207
mm v[108:111],a[108:111],v[236:239],v[108:111]
ex v208,v208
mm v[76:79],a[108:111],v[232:235],v[76:79]
rd a[108:111],v248 offset:11264
mm v[80:83],a[112:115],v[232:235],v[80:83]
ex v209,v209
mm v[112:115],a[112:115],v[236:239],v[112:115]
rd a[112:115],v248 offset:12288
mm v[116:119],a[116:119],v[236:239],v[116:119]
ex v210,v210
mm v[84:87],a[116:119],v[232:235],v[84:87]
rd a[116:119],v248 offset:13312
mm v[88:91],a[120:123],v[232:235],v[88:91]
ex v211,v211
mm v[120:123],a[120:123],v[236:239],v[120:123]
rd a[120:123],v248 offset:14336
s_bitcmp1_b64 s[98:99], s85
mm v[124:127],a[124:127],v[236:239],v[124:127]
ex v216,v216
mm v[92:95],a[124:127],v[232:235],v[92:95]
rd a[124:127],v248 offset:15360
v_or_b32 v248, s89, v255
v_smfmac_f32_16x16x64_bf16 a[196:199], a[208:211], v[232:239], v241
wt lgkmcnt(4)
v_smfmac_f32_16x16x64_bf16 a[192:195], a[208:211], v[224:231], v241
rd a[64:67],v248 offset:0
mm v[0:3],a[96:99],v[224:227],v[0:3]
ex v217,v217
mm v[32:35],a[96:99],v[228:231],v[32:35]
rd a[68:71],v248 offset:1024
cv v232,v208,v209
mm v[36:39],a[100:103],v[228:231],v[36:39]
ex v218,v218
mm v[4:7],a[100:103],v[224:227],v[4:7]
rd a[72:75],v248 offset:2048
cv v233,v210,v211
mm v[8:11],a[104:107],v[224:227],v[8:11]
ex v219,v219
mm v[40:43],a[104:107],v[228:231],v[40:43]
rd a[76:79],v248 offset:3072
cv v236,v216,v217
mm v[44:47],a[108:111],v[228:231],v[44:47]
ex v212,v212
mm v[12:15],a[108:111],v[224:227],v[12:15]
rd a[80:83],v248 offset:4096
wt lgkmcnt(5)
mm v[16:19],a[112:115],v[224:227],v[16:19]
ex v213,v213
mm v[48:51],a[112:115],v[228:231],v[48:51]
rd a[84:87],v248 offset:5120
cv v237,v218,v219
mm v[52:55],a[116:119],v[228:231],v[52:55]
ex v214,v214
mm v[20:23],a[116:119],v[224:227],v[20:23]
rd a[88:91],v248 offset:6144
cv v234,v212,v213
mm v[24:27],a[120:123],v[224:227],v[24:27]
ex v215,v215
mm v[56:59],a[120:123],v[228:231],v[56:59]
)ASM" R"ASM(rd a[92:95],v248 offset:7168
cv v235,v214,v215
mm v[60:63],a[124:127],v[228:231],v[60:63]
ex v220,v220
mm v[28:31],a[124:127],v[224:227],v[28:31]
; ---- helper: own block finished: l is checked (.La5_LA_SLOW if outside [2^-60, 2^60] or NaN), O / l leaves; the partner-s rows start
.La5_LA_F1Y:
.La5_LW6_0:
s_cbranch_execz .La5_LW6_4
wt lgkmcnt(4)
wt vmcnt(0)
mm v[192:195],a[64:67],a[0:3],0
ex v221,v221
mm v[192:195],a[68:71],a[4:7],v[192:195]
cv v238,v220,v221
s_add_u32 s89, s89, 16384
mm v[192:195],a[72:75],a[8:11],v[192:195]
ex v222,v222
mm v[192:195],a[76:79],a[12:15],v[192:195]
ld v255,s[60:63],s17 offen offset:0 sc1 lds
ar v242,a192
ar v240,a193
v_add_f32 v242, v242, v240
mm v[200:203],a[76:79],a[28:31],0
ex v223,v223
ar v243,a194
ar v240,a195
v_add_f32 v243, v243, v240
mm v[200:203],a[72:75],a[24:27],v[200:203]
cv v239,v222,v223
s_and_b32 s89, s89, 0x1ffff
aw a192,0
aw a193,0
aw a194,0
mm v[200:203],a[68:71],a[20:23],v[200:203]
ex v192,v192
aw a195,0
v_subrev_u32 v240, 562036736, v242
mm v[200:203],a[64:67],a[16:19],v[200:203]
wt lgkmcnt(0)
v_rcp_f32 v242, v242
v_cmp_le_u32 vcc, 1006632960, v240
v_subrev_u32 v240, 562036736, v243
mm v[196:199],a[80:83],a[0:3],0
ex v193,v193
s_mov_b64 s[92:93], vcc
v_cmp_le_u32 vcc, 1006632960, v240
mm v[196:199],a[84:87],a[4:7],v[196:199]
cv v224,v192,v193
v_rcp_f32 v243, v243
mm v[196:199],a[88:91],a[8:11],v[196:199]
ex v194,v194
s_or_b64 vcc, vcc, s[92:93]
s_cbranch_scc1 .La5_LA_SC1
.La5_LA_SR1:
v_mul_f32 v0, v0, v242
mm v[196:199],a[92:95],a[12:15],v[196:199]
ld v255,s[60:63],s17 offen offset:1024 sc1 lds
v_mul_f32 v1, v1, v242
v_mul_f32 v2, v2, v242
v_mul_f32 v3, v3, v242
mm v[204:207],a[92:95],a[28:31],0
ex v195,v195
v_mul_f32 v4, v4, v242
v_mul_f32 v5, v5, v242
v_mul_f32 v6, v6, v242
mm v[204:207],a[88:91],a[24:27],v[204:207]
cv v225,v194,v195
v_mul_f32 v7, v7, v242
cv v128,v0,v1
cv v129,v2,v3
cv v130,v4,v5
mm v[204:207],a[84:87],a[20:23],v[204:207]
ex v200,v200
cv v131,v6,v7
v_mul_f32 v8, v8, v242
v_mul_f32 v9, v9, v242
mm v[204:207],a[80:83],a[16:19],v[204:207]
ld v255,s[60:63],s17 offen offset:2048 sc1 lds
v_mul_f32 v10, v10, v242
v_mul_f32 v11, v11, v242
v_mul_f32 v12, v12, v242
mm v[208:211],a[64:67],a[32:35],0
ex v201,v201
v_mul_f32 v13, v13, v242
v_mul_f32 v14, v14, v242
v_mul_f32 v15, v15, v242
mm v[208:211],a[68:71],a[36:39],v[208:211]
cv v228,v200,v201
cv v132,v8,v9
cv v133,v10,v11
cv v134,v12,v13
cv v135,v14,v15
mm v[208:211],a[72:75],a[40:43],v[208:211]
ex v202,v202
v_mul_f32 v16, v16, v242
v_mul_f32 v17, v17, v242
v_mul_f32 v18, v18, v242
mm v[208:211],a[76:79],a[44:47],v[208:211]
ld v255,s[60:63],s17 offen offset:3072 sc1 lds
)ASM" R"ASM(v_mul_f32 v19, v19, v242
v_mul_f32 v20, v20, v242
v_mul_f32 v21, v21, v242
mm v[216:219],a[76:79],a[60:63],0
ex v203,v203
v_mul_f32 v22, v22, v242
v_mul_f32 v23, v23, v242
cv v136,v16,v17
mm v[216:219],a[72:75],a[56:59],v[216:219]
cv v229,v202,v203
cv v137,v18,v19
cv v138,v20,v21
cv v139,v22,v23
v_mul_f32 v24, v24, v242
mm v[216:219],a[68:71],a[52:55],v[216:219]
ex v196,v196
v_mul_f32 v25, v25, v242
v_mul_f32 v26, v26, v242
v_mul_f32 v27, v27, v242
mm v[216:219],a[64:67],a[48:51],v[216:219]
v_mul_f32 v28, v28, v242
v_mul_f32 v29, v29, v242
v_mul_f32 v30, v30, v242
v_mul_f32 v31, v31, v242
cv v140,v24,v25
mm v[212:215],a[80:83],a[32:35],0
ex v197,v197
s_add_u32 s17, s17, s80
cv v141,v26,v27
cv v142,v28,v29
mm v[212:215],a[84:87],a[36:39],v[212:215]
cv v226,v196,v197
.La5_LW6_1:
s_cbranch_execz .La5_LW6_5
s_add_u32 m0, m0, 16384
s_and_b32 m0, m0, s78
cv v143,v30,v31
v_mul_f32 v32, v32, v243
mm v[212:215],a[88:91],a[40:43],v[212:215]
ex v198,v198
v_mul_f32 v33, v33, v243
v_mul_f32 v34, v34, v243
v_mul_f32 v35, v35, v243
mm v[212:215],a[92:95],a[44:47],v[212:215]
st v[128:131],v244,s[44:47],s91 offen offset:0 sc1
v_mul_f32 v36, v36, v243
v_mul_f32 v37, v37, v243
v_mul_f32 v38, v38, v243
mm v[220:223],a[92:95],a[60:63],0
ex v199,v199
v_mul_f32 v39, v39, v243
cv v144,v32,v33
cv v145,v34,v35
mm v[220:223],a[88:91],a[56:59],v[220:223]
cv v227,v198,v199
cv v146,v36,v37
cv v147,v38,v39
v_mul_f32 v40, v40, v243
v_mul_f32 v41, v41, v243
mm v[220:223],a[84:87],a[52:55],v[220:223]
ex v204,v204
v_mul_f32 v42, v42, v243
v_mul_f32 v43, v43, v243
v_mul_f32 v44, v44, v243
mm v[220:223],a[80:83],a[48:51],v[220:223]
s_nop 0
s_barrier
v_mul_f32 v45, v45, v243
v_mul_f32 v46, v46, v243
v_mul_f32 v47, v47, v243
mm v[64:67],a[96:99],v[232:235],v[64:67]
ex v205,v205
cv v148,v40,v41
cv v149,v42,v43
cv v150,v44,v45
mm v[96:99],a[96:99],v[236:239],v[96:99]
rd a[96:99],v248 offset:8192
cv v230,v204,v205
cv v151,v46,v47
v_mul_f32 v48, v48, v243
v_mul_f32 v49, v49, v243
mm v[100:103],a[100:103],v[236:239],v[100:103]
ex v206,v206
v_mul_f32 v50, v50, v243
v_mul_f32 v51, v51, v243
v_mul_f32 v52, v52, v243
mm v[68:71],a[100:103],v[232:235],v[68:71]
rd a[100:103],v248 offset:9216
st v[132:135],v244,s[44:47],s91 offen offset:1024 sc1
v_mul_f32 v53, v53, v243
v_mul_f32 v54, v54, v243
mm v[72:75],a[104:107],v[232:235],v[72:75]
ex v207,v207
v_mul_f32 v55, v55, v243
cv v152,v48,v49
cv v153,v50,v51
mm v[104:107],a[104:107],v[236:239],v[104:107]
rd a[104:107],v248 offset:10240
cv v231,v206,v207
cv v154,v52,v53
)ASM" R"ASM(cv v155,v54,v55
v_mul_f32 v56, v56, v243
mm v[108:111],a[108:111],v[236:239],v[108:111]
ex v208,v208
v_mul_f32 v57, v57, v243
v_mul_f32 v58, v58, v243
v_mul_f32 v59, v59, v243
mm v[76:79],a[108:111],v[232:235],v[76:79]
rd a[108:111],v248 offset:11264
v_mul_f32 v60, v60, v243
v_mul_f32 v61, v61, v243
v_mul_f32 v62, v62, v243
v_mul_f32 v63, v63, v243
mm v[80:83],a[112:115],v[232:235],v[80:83]
ex v209,v209
cv v156,v56,v57
cv v157,v58,v59
cv v158,v60,v61
mm v[112:115],a[112:115],v[236:239],v[112:115]
rd a[112:115],v248 offset:12288
st v[136:139],v244,s[44:47],s91 offen offset:2048 sc1
cv v159,v62,v63
mm v[116:119],a[116:119],v[236:239],v[116:119]
ex v210,v210
mm v[84:87],a[116:119],v[232:235],v[84:87]
rd a[116:119],v248 offset:13312
mm v[88:91],a[120:123],v[232:235],v[88:91]
ex v211,v211
mm v[120:123],a[120:123],v[236:239],v[120:123]
rd a[120:123],v248 offset:14336
st v[140:143],v244,s[44:47],s91 offen offset:3072 sc1
mm v[124:127],a[124:127],v[236:239],v[124:127]
ex v216,v216
mm v[92:95],a[124:127],v[232:235],v[92:95]
rd a[124:127],v248 offset:15360
v_or_b32 v248, s89, v255
v_smfmac_f32_16x16x64_bf16 a[196:199], a[208:211], v[232:239], v241
wt lgkmcnt(4)
v_smfmac_f32_16x16x64_bf16 a[192:195], a[208:211], v[224:231], v241
rd a[64:67],v248 offset:0
mm v[0:3],a[96:99],v[224:227],0
ex v217,v217
mm v[32:35],a[96:99],v[228:231],0
rd a[68:71],v248 offset:1024
cv v232,v208,v209
ar v242,a196
ar v240,a197
v_add_f32 v242, v242, v240
mm v[36:39],a[100:103],v[228:231],0
ex v218,v218
ar v243,a198
ar v240,a199
v_add_f32 v243, v243, v240
mm v[4:7],a[100:103],v[224:227],0
rd a[72:75],v248 offset:2048
cv v233,v210,v211
st v[144:147],v245,s[44:47],s91 offen offset:0 sc1
aw a196,0
mm v[8:11],a[104:107],v[224:227],0
ex v219,v219
aw a197,0
aw a198,0
aw a199,0
mm v[40:43],a[104:107],v[228:231],0
rd a[76:79],v248 offset:3072
cv v236,v216,v217
v_subrev_u32 v240, 562036736, v242
v_rcp_f32 v242, v242
mm v[44:47],a[108:111],v[228:231],0
ex v212,v212
v_cmp_le_u32 vcc, 1006632960, v240
v_subrev_u32 v240, 562036736, v243
s_mov_b64 s[92:93], vcc
mm v[12:15],a[108:111],v[224:227],0
rd a[80:83],v248 offset:4096
wt lgkmcnt(5)
v_cmp_le_u32 vcc, 1006632960, v240
v_rcp_f32 v243, v243
mm v[16:19],a[112:115],v[224:227],0
ex v213,v213
s_or_b64 vcc, vcc, s[92:93]
s_cbranch_scc1 .La5_LA_SC2
.La5_LA_SR2:
v_mul_f32 v64, v64, v242
mm v[48:51],a[112:115],v[228:231],0
rd a[84:87],v248 offset:5120
cv v237,v218,v219
v_mul_f32 v65, v65, v242
v_mul_f32 v66, v66, v242
v_mul_f32 v67, v67, v242
mm v[52:55],a[116:119],v[228:231],0
ex v214,v214
v_mul_f32 v68, v68, v242
v_mul_f32 v69, v69, v242
v_mul_f32 v70, v70, v242
mm v[20:23],a[116:119],v[224:227],0
rd a[88:91],v248 offset:6144
cv v234,v212,v213
st v[148:151],v245,s[44:47],s91 offen offset:1024 sc1
v_mul_f32 v71, v71, v242
)ASM" R"ASM(mm v[24:27],a[120:123],v[224:227],0
ex v215,v215
cv v160,v64,v65
cv v161,v66,v67
cv v162,v68,v69
mm v[56:59],a[120:123],v[228:231],0
rd a[92:95],v248 offset:7168
cv v235,v214,v215
cv v163,v70,v71
v_mul_f32 v72, v72, v242
v_mul_f32 v73, v73, v242
mm v[60:63],a[124:127],v[228:231],0
ex v220,v220
v_mul_f32 v74, v74, v242
v_mul_f32 v75, v75, v242
v_mul_f32 v76, v76, v242
mm v[28:31],a[124:127],v[224:227],0
s_add_u32 s85, s85, 1
v_mul_f32 v77, v77, v242
v_mul_f32 v78, v78, v242
v_mul_f32 v79, v79, v242
cv v164,v72,v73
wt lgkmcnt(4)
mm v[192:195],a[64:67],a[0:3],0
ex v221,v221
cv v165,v74,v75
cv v166,v76,v77
cv v167,v78,v79
mm v[192:195],a[68:71],a[4:7],v[192:195]
cv v238,v220,v221
s_add_u32 s89, s89, 16384
v_mul_f32 v80, v80, v242
v_mul_f32 v81, v81, v242
v_mul_f32 v82, v82, v242
mm v[192:195],a[72:75],a[8:11],v[192:195]
ex v222,v222
v_mul_f32 v83, v83, v242
v_mul_f32 v84, v84, v242
v_mul_f32 v85, v85, v242
mm v[192:195],a[76:79],a[12:15],v[192:195]
ld v255,s[60:63],s17 offen offset:0 sc1 lds
v_mul_f32 v86, v86, v242
v_mul_f32 v87, v87, v242
cv v168,v80,v81
mm v[200:203],a[76:79],a[28:31],0
ex v223,v223
cv v169,v82,v83
cv v170,v84,v85
cv v171,v86,v87
mm v[200:203],a[72:75],a[24:27],v[200:203]
cv v239,v222,v223
s_and_b32 s89, s89, 0x1ffff
v_mul_f32 v88, v88, v242
v_mul_f32 v89, v89, v242
v_mul_f32 v90, v90, v242
mm v[200:203],a[68:71],a[20:23],v[200:203]
ex v192,v192
v_mul_f32 v91, v91, v242
v_mul_f32 v92, v92, v242
v_mul_f32 v93, v93, v242
mm v[200:203],a[64:67],a[16:19],v[200:203]
wt lgkmcnt(0)
st v[152:155],v245,s[44:47],s91 offen offset:2048 sc1
v_mul_f32 v94, v94, v242
v_mul_f32 v95, v95, v242
mm v[196:199],a[80:83],a[0:3],0
ex v193,v193
cv v172,v88,v89
cv v173,v90,v91
cv v174,v92,v93
mm v[196:199],a[84:87],a[4:7],v[196:199]
cv v224,v192,v193
cv v175,v94,v95
v_mul_f32 v96, v96, v243
v_mul_f32 v97, v97, v243
v_mul_f32 v98, v98, v243
mm v[196:199],a[88:91],a[8:11],v[196:199]
ex v194,v194
v_mul_f32 v99, v99, v243
v_mul_f32 v100, v100, v243
v_mul_f32 v101, v101, v243
mm v[196:199],a[92:95],a[12:15],v[196:199]
ld v255,s[60:63],s17 offen offset:1024 sc1 lds
v_mul_f32 v102, v102, v243
v_mul_f32 v103, v103, v243
cv v176,v96,v97
mm v[204:207],a[92:95],a[28:31],0
ex v195,v195
cv v177,v98,v99
cv v178,v100,v101
cv v179,v102,v103
mm v[204:207],a[88:91],a[24:27],v[204:207]
cv v225,v194,v195
st v[156:159],v245,s[44:47],s91 offen offset:3072 sc1
v_mul_f32 v104, v104, v243
v_mul_f32 v105, v105, v243
mm v[204:207],a[84:87],a[20:23],v[204:207]
ex v200,v200
v_mul_f32 v106, v106, v243
v_mul_f32 v107, v107, v243
)ASM" R"ASM(v_mul_f32 v108, v108, v243
mm v[204:207],a[80:83],a[16:19],v[204:207]
ld v255,s[60:63],s17 offen offset:2048 sc1 lds
v_mul_f32 v109, v109, v243
v_mul_f32 v110, v110, v243
v_mul_f32 v111, v111, v243
mm v[208:211],a[64:67],a[32:35],0
ex v201,v201
cv v180,v104,v105
cv v181,v106,v107
cv v182,v108,v109
mm v[208:211],a[68:71],a[36:39],v[208:211]
cv v228,v200,v201
cv v183,v110,v111
v_mul_f32 v112, v112, v243
v_mul_f32 v113, v113, v243
v_mul_f32 v114, v114, v243
mm v[208:211],a[72:75],a[40:43],v[208:211]
ex v202,v202
v_mul_f32 v115, v115, v243
v_mul_f32 v116, v116, v243
v_mul_f32 v117, v117, v243
mm v[208:211],a[76:79],a[44:47],v[208:211]
ld v255,s[60:63],s17 offen offset:3072 sc1 lds
v_mul_f32 v118, v118, v243
v_mul_f32 v119, v119, v243
cv v184,v112,v113
mm v[216:219],a[76:79],a[60:63],0
ex v203,v203
cv v185,v114,v115
cv v186,v116,v117
cv v187,v118,v119
mm v[216:219],a[72:75],a[56:59],v[216:219]
cv v229,v202,v203
v_mul_f32 v120, v120, v243
v_mul_f32 v121, v121, v243
v_mul_f32 v122, v122, v243
v_mul_f32 v123, v123, v243
mm v[216:219],a[68:71],a[52:55],v[216:219]
ex v196,v196
v_mul_f32 v124, v124, v243
v_mul_f32 v125, v125, v243
v_mul_f32 v126, v126, v243
mm v[216:219],a[64:67],a[48:51],v[216:219]
v_mul_f32 v127, v127, v243
cv v188,v120,v121
cv v189,v122,v123
cv v190,v124,v125
cv v191,v126,v127
mm v[212:215],a[80:83],a[32:35],0
ex v197,v197
s_add_u32 s17, s17, s80
mm v[212:215],a[84:87],a[36:39],v[212:215]
cv v226,v196,v197
s_add_u32 m0, m0, 16384
s_and_b32 m0, m0, s78
mm v[212:215],a[88:91],a[40:43],v[212:215]
ex v198,v198
mm v[212:215],a[92:95],a[44:47],v[212:215]
st v[160:163],v246,s[44:47],s91 offen offset:0 sc1
mm v[220:223],a[92:95],a[60:63],0
ex v199,v199
mm v[220:223],a[88:91],a[56:59],v[220:223]
cv v227,v198,v199
mm v[220:223],a[84:87],a[52:55],v[220:223]
ex v204,v204
mm v[220:223],a[80:83],a[48:51],v[220:223]
wt vmcnt(18)
s_barrier
mm v[64:67],a[96:99],v[232:235],0
ex v205,v205
mm v[96:99],a[96:99],v[236:239],0
rd a[96:99],v248 offset:8192
cv v230,v204,v205
mm v[100:103],a[100:103],v[236:239],0
ex v206,v206
mm v[68:71],a[100:103],v[232:235],0
rd a[100:103],v248 offset:9216
st v[164:167],v246,s[44:47],s91 offen offset:1024 sc1
mm v[72:75],a[104:107],v[232:235],0
ex v207,v207
mm v[104:107],a[104:107],v[236:239],0
rd a[104:107],v248 offset:10240
cv v231,v206,v207
mm v[108:111],a[108:111],v[236:239],0
ex v208,v208
mm v[76:79],a[108:111],v[232:235],0
rd a[108:111],v248 offset:11264
st v[168:171],v246,s[44:47],s91 offen offset:2048 sc1
mm v[80:83],a[112:115],v[232:235],0
ex v209,v209
mm v[112:115],a[112:115],v[236:239],0
rd a[112:115],v248 offset:12288
mm v[116:119],a[116:119],v[236:239],0
ex v210,v210
mm v[84:87],a[116:119],v[232:235],0
rd a[116:119],v248 offset:13312
st v[172:175],v246,s[44:47],s91 offen offset:3072 sc1
mm v[88:91],a[120:123],v[232:235],0
ex v211,v211
)ASM" R"ASM(mm v[120:123],a[120:123],v[236:239],0
rd a[120:123],v248 offset:14336
mm v[124:127],a[124:127],v[236:239],0
ex v216,v216
mm v[92:95],a[124:127],v[232:235],0
rd a[124:127],v248 offset:15360
v_or_b32 v248, s89, v255
st v[176:179],v247,s[44:47],s91 offen offset:0 sc1
v_smfmac_f32_16x16x64_bf16 a[196:199], a[208:211], v[232:239], v241
wt lgkmcnt(4)
v_smfmac_f32_16x16x64_bf16 a[192:195], a[208:211], v[224:231], v241
rd a[64:67],v248 offset:0
mm v[0:3],a[96:99],v[224:227],v[0:3]
ex v217,v217
mm v[32:35],a[96:99],v[228:231],v[32:35]
rd a[68:71],v248 offset:1024
cv v232,v208,v209
mm v[36:39],a[100:103],v[228:231],v[36:39]
ex v218,v218
mm v[4:7],a[100:103],v[224:227],v[4:7]
rd a[72:75],v248 offset:2048
cv v233,v210,v211
st v[180:183],v247,s[44:47],s91 offen offset:1024 sc1
mm v[8:11],a[104:107],v[224:227],v[8:11]
ex v219,v219
mm v[40:43],a[104:107],v[228:231],v[40:43]
rd a[76:79],v248 offset:3072
cv v236,v216,v217
mm v[44:47],a[108:111],v[228:231],v[44:47]
ex v212,v212
mm v[12:15],a[108:111],v[224:227],v[12:15]
rd a[80:83],v248 offset:4096
wt lgkmcnt(5)
mm v[16:19],a[112:115],v[224:227],v[16:19]
ex v213,v213
mm v[48:51],a[112:115],v[228:231],v[48:51]
rd a[84:87],v248 offset:5120
cv v237,v218,v219
st v[184:187],v247,s[44:47],s91 offen offset:2048 sc1
mm v[52:55],a[116:119],v[228:231],v[52:55]
ex v214,v214
mm v[20:23],a[116:119],v[224:227],v[20:23]
rd a[88:91],v248 offset:6144
cv v234,v212,v213
mm v[24:27],a[120:123],v[224:227],v[24:27]
ex v215,v215
mm v[56:59],a[120:123],v[228:231],v[56:59]
rd a[92:95],v248 offset:7168
cv v235,v214,v215
st v[188:191],v247,s[44:47],s91 offen offset:3072 sc1
mm v[60:63],a[124:127],v[228:231],v[60:63]
ex v220,v220
mm v[28:31],a[124:127],v[224:227],v[28:31]
s_add_u32 s85, s85, 1
s_cmp_eq_u32 s85, 22
s_cbranch_scc1 .La5_LA_F3
.p2align 6
.La5_LA_F2:
wt lgkmcnt(4)
mm v[192:195],a[64:67],a[0:3],0
ex v221,v221
mm v[192:195],a[68:71],a[4:7],v[192:195]
cv v238,v220,v221
s_add_u32 s85, s85, 1
mm v[192:195],a[72:75],a[8:11],v[192:195]
ex v222,v222
mm v[192:195],a[76:79],a[12:15],v[192:195]
ld v255,s[60:63],s17 offen offset:0 sc1 lds
mm v[200:203],a[76:79],a[28:31],0
ex v223,v223
mm v[200:203],a[72:75],a[24:27],v[200:203]
cv v239,v222,v223
s_add_u32 s89, s89, 16384
mm v[200:203],a[68:71],a[20:23],v[200:203]
ex v192,v192
mm v[200:203],a[64:67],a[16:19],v[200:203]
wt lgkmcnt(0)
s_and_b32 s89, s89, 0x1ffff
mm v[196:199],a[80:83],a[0:3],0
ex v193,v193
mm v[196:199],a[84:87],a[4:7],v[196:199]
cv v224,v192,v193
mm v[196:199],a[88:91],a[8:11],v[196:199]
ex v194,v194
mm v[196:199],a[92:95],a[12:15],v[196:199]
ld v255,s[60:63],s17 offen offset:1024 sc1 lds
mm v[204:207],a[92:95],a[28:31],0
ex v195,v195
mm v[204:207],a[88:91],a[24:27],v[204:207]
cv v225,v194,v195
mm v[204:207],a[84:87],a[20:23],v[204:207]
ex v200,v200
mm v[204:207],a[80:83],a[16:19],v[204:207]
ld v255,s[60:63],s17 offen offset:2048 sc1 lds
mm v[208:211],a[64:67],a[32:35],0
ex v201,v201
mm v[208:211],a[68:71],a[36:39],v[208:211]
cv v228,v200,v201
mm v[208:211],a[72:75],a[40:43],v[208:211]
ex v202,v202
)ASM" R"ASM(mm v[208:211],a[76:79],a[44:47],v[208:211]
ld v255,s[60:63],s17 offen offset:3072 sc1 lds
mm v[216:219],a[76:79],a[60:63],0
ex v203,v203
mm v[216:219],a[72:75],a[56:59],v[216:219]
cv v229,v202,v203
mm v[216:219],a[68:71],a[52:55],v[216:219]
ex v196,v196
mm v[216:219],a[64:67],a[48:51],v[216:219]
buffer_load_dword a253, v254, s[36:39], s90 offen
mm v[212:215],a[80:83],a[32:35],0
ex v197,v197
mm v[212:215],a[84:87],a[36:39],v[212:215]
cv v226,v196,v197
s_add_u32 s90, s90, s21
mm v[212:215],a[88:91],a[40:43],v[212:215]
ex v198,v198
mm v[212:215],a[92:95],a[44:47],v[212:215]
s_add_u32 s17, s17, s80
s_add_u32 m0, m0, 16384
mm v[220:223],a[92:95],a[60:63],0
ex v199,v199
mm v[220:223],a[88:91],a[56:59],v[220:223]
cv v227,v198,v199
s_and_b32 m0, m0, s78
mm v[220:223],a[84:87],a[52:55],v[220:223]
ex v204,v204
mm v[220:223],a[80:83],a[48:51],v[220:223]
wt vmcnt(11)
s_barrier
mm v[64:67],a[96:99],v[232:235],v[64:67]
ex v205,v205
mm v[96:99],a[96:99],v[236:239],v[96:99]
rd a[96:99],v248 offset:8192
cv v230,v204,v205
mm v[100:103],a[100:103],v[236:239],v[100:103]
ex v206,v206
mm v[68:71],a[100:103],v[232:235],v[68:71]
rd a[100:103],v248 offset:9216
mm v[72:75],a[104:107],v[232:235],v[72:75]
ex v207,v207
mm v[104:107],a[104:107],v[236:239],v[104:107]
rd a[104:107],v248 offset:10240
cv v231,v206,v207
mm v[108:111],a[108:111],v[236:239],v[108:111]
ex v208,v208
mm v[76:79],a[108:111],v[232:235],v[76:79]
rd a[108:111],v248 offset:11264
mm v[80:83],a[112:115],v[232:235],v[80:83]
ex v209,v209
mm v[112:115],a[112:115],v[236:239],v[112:115]
rd a[112:115],v248 offset:12288
mm v[116:119],a[116:119],v[236:239],v[116:119]
ex v210,v210
mm v[84:87],a[116:119],v[232:235],v[84:87]
rd a[116:119],v248 offset:13312
mm v[88:91],a[120:123],v[232:235],v[88:91]
ex v211,v211
mm v[120:123],a[120:123],v[236:239],v[120:123]
rd a[120:123],v248 offset:14336
s_bitcmp1_b64 s[98:99], s85
mm v[124:127],a[124:127],v[236:239],v[124:127]
ex v216,v216
mm v[92:95],a[124:127],v[232:235],v[92:95]
rd a[124:127],v248 offset:15360
v_or_b32 v248, s89, v255
v_smfmac_f32_16x16x64_bf16 a[196:199], a[208:211], v[232:239], v241
wt lgkmcnt(4)
v_smfmac_f32_16x16x64_bf16 a[192:195], a[208:211], v[224:231], v241
rd a[64:67],v248 offset:0
mm v[0:3],a[96:99],v[224:227],v[0:3]
ex v217,v217
mm v[32:35],a[96:99],v[228:231],v[32:35]
rd a[68:71],v248 offset:1024
cv v232,v208,v209
mm v[36:39],a[100:103],v[228:231],v[36:39]
ex v218,v218
mm v[4:7],a[100:103],v[224:227],v[4:7]
rd a[72:75],v248 offset:2048
cv v233,v210,v211
mm v[8:11],a[104:107],v[224:227],v[8:11]
ex v219,v219
mm v[40:43],a[104:107],v[228:231],v[40:43]
rd a[76:79],v248 offset:3072
cv v236,v216,v217
mm v[44:47],a[108:111],v[228:231],v[44:47]
ex v212,v212
mm v[12:15],a[108:111],v[224:227],v[12:15]
rd a[80:83],v248 offset:4096
wt lgkmcnt(5)
mm v[16:19],a[112:115],v[224:227],v[16:19]
ex v213,v213
mm v[48:51],a[112:115],v[228:231],v[48:51]
rd a[84:87],v248 offset:5120
cv v237,v218,v219
mm v[52:55],a[116:119],v[228:231],v[52:55]
ex v214,v214
mm v[20:23],a[116:119],v[224:227],v[20:23]
rd a[88:91],v248 offset:6144
cv v234,v212,v213
)ASM" R"ASM(mm v[24:27],a[120:123],v[224:227],v[24:27]
ex v215,v215
mm v[56:59],a[120:123],v[228:231],v[56:59]
rd a[92:95],v248 offset:7168
cv v235,v214,v215
mm v[60:63],a[124:127],v[228:231],v[60:63]
ex v220,v220
mm v[28:31],a[124:127],v[224:227],v[28:31]
s_cbranch_scc1 .La5_LA_F2E2
wt lgkmcnt(4)
mm v[192:195],a[64:67],a[0:3],0
ex v221,v221
mm v[192:195],a[68:71],a[4:7],v[192:195]
cv v238,v220,v221
s_add_u32 s85, s85, 1
mm v[192:195],a[72:75],a[8:11],v[192:195]
ex v222,v222
mm v[192:195],a[76:79],a[12:15],v[192:195]
ld v255,s[60:63],s17 offen offset:0 sc1 lds
mm v[200:203],a[76:79],a[28:31],0
ex v223,v223
mm v[200:203],a[72:75],a[24:27],v[200:203]
cv v239,v222,v223
s_add_u32 s89, s89, 16384
mm v[200:203],a[68:71],a[20:23],v[200:203]
ex v192,v192
mm v[200:203],a[64:67],a[16:19],v[200:203]
wt lgkmcnt(0)
s_and_b32 s89, s89, 0x1ffff
mm v[196:199],a[80:83],a[0:3],0
ex v193,v193
mm v[196:199],a[84:87],a[4:7],v[196:199]
cv v224,v192,v193
mm v[196:199],a[88:91],a[8:11],v[196:199]
ex v194,v194
mm v[196:199],a[92:95],a[12:15],v[196:199]
ld v255,s[60:63],s17 offen offset:1024 sc1 lds
mm v[204:207],a[92:95],a[28:31],0
ex v195,v195
mm v[204:207],a[88:91],a[24:27],v[204:207]
cv v225,v194,v195
mm v[204:207],a[84:87],a[20:23],v[204:207]
ex v200,v200
mm v[204:207],a[80:83],a[16:19],v[204:207]
ld v255,s[60:63],s17 offen offset:2048 sc1 lds
mm v[208:211],a[64:67],a[32:35],0
ex v201,v201
mm v[208:211],a[68:71],a[36:39],v[208:211]
cv v228,v200,v201
mm v[208:211],a[72:75],a[40:43],v[208:211]
ex v202,v202
mm v[208:211],a[76:79],a[44:47],v[208:211]
ld v255,s[60:63],s17 offen offset:3072 sc1 lds
mm v[216:219],a[76:79],a[60:63],0
ex v203,v203
mm v[216:219],a[72:75],a[56:59],v[216:219]
cv v229,v202,v203
mm v[216:219],a[68:71],a[52:55],v[216:219]
ex v196,v196
mm v[216:219],a[64:67],a[48:51],v[216:219]
buffer_load_dword a253, v254, s[36:39], s90 offen
mm v[212:215],a[80:83],a[32:35],0
ex v197,v197
mm v[212:215],a[84:87],a[36:39],v[212:215]
cv v226,v196,v197
.La5_LW6_2:
s_cbranch_execz .La5_LW6_6
s_add_u32 s90, s90, s21
mm v[212:215],a[88:91],a[40:43],v[212:215]
ex v198,v198
mm v[212:215],a[92:95],a[44:47],v[212:215]
s_add_u32 s17, s17, s80
s_add_u32 m0, m0, 16384
mm v[220:223],a[92:95],a[60:63],0
ex v199,v199
mm v[220:223],a[88:91],a[56:59],v[220:223]
cv v227,v198,v199
s_and_b32 m0, m0, s78
mm v[220:223],a[84:87],a[52:55],v[220:223]
ex v204,v204
mm v[220:223],a[80:83],a[48:51],v[220:223]
wt vmcnt(11)
s_barrier
mm v[64:67],a[96:99],v[232:235],v[64:67]
ex v205,v205
mm v[96:99],a[96:99],v[236:239],v[96:99]
rd a[96:99],v248 offset:8192
cv v230,v204,v205
mm v[100:103],a[100:103],v[236:239],v[100:103]
ex v206,v206
mm v[68:71],a[100:103],v[232:235],v[68:71]
rd a[100:103],v248 offset:9216
mm v[72:75],a[104:107],v[232:235],v[72:75]
ex v207,v207
mm v[104:107],a[104:107],v[236:239],v[104:107]
rd a[104:107],v248 offset:10240
cv v231,v206,v207
mm v[108:111],a[108:111],v[236:239],v[108:111]
ex v208,v208
mm v[76:79],a[108:111],v[232:235],v[76:79]
)ASM" R"ASM(rd a[108:111],v248 offset:11264
mm v[80:83],a[112:115],v[232:235],v[80:83]
ex v209,v209
mm v[112:115],a[112:115],v[236:239],v[112:115]
rd a[112:115],v248 offset:12288
mm v[116:119],a[116:119],v[236:239],v[116:119]
ex v210,v210
mm v[84:87],a[116:119],v[232:235],v[84:87]
rd a[116:119],v248 offset:13312
mm v[88:91],a[120:123],v[232:235],v[88:91]
ex v211,v211
mm v[120:123],a[120:123],v[236:239],v[120:123]
rd a[120:123],v248 offset:14336
s_bitcmp1_b64 s[98:99], s85
mm v[124:127],a[124:127],v[236:239],v[124:127]
ex v216,v216
mm v[92:95],a[124:127],v[232:235],v[92:95]
rd a[124:127],v248 offset:15360
v_or_b32 v248, s89, v255
v_smfmac_f32_16x16x64_bf16 a[196:199], a[208:211], v[232:239], v241
wt lgkmcnt(4)
v_smfmac_f32_16x16x64_bf16 a[192:195], a[208:211], v[224:231], v241
rd a[64:67],v248 offset:0
mm v[0:3],a[96:99],v[224:227],v[0:3]
ex v217,v217
mm v[32:35],a[96:99],v[228:231],v[32:35]
rd a[68:71],v248 offset:1024
cv v232,v208,v209
mm v[36:39],a[100:103],v[228:231],v[36:39]
ex v218,v218
mm v[4:7],a[100:103],v[224:227],v[4:7]
rd a[72:75],v248 offset:2048
cv v233,v210,v211
mm v[8:11],a[104:107],v[224:227],v[8:11]
ex v219,v219
mm v[40:43],a[104:107],v[228:231],v[40:43]
rd a[76:79],v248 offset:3072
cv v236,v216,v217
mm v[44:47],a[108:111],v[228:231],v[44:47]
ex v212,v212
mm v[12:15],a[108:111],v[224:227],v[12:15]
rd a[80:83],v248 offset:4096
wt lgkmcnt(5)
mm v[16:19],a[112:115],v[224:227],v[16:19]
ex v213,v213
mm v[48:51],a[112:115],v[228:231],v[48:51]
rd a[84:87],v248 offset:5120
cv v237,v218,v219
mm v[52:55],a[116:119],v[228:231],v[52:55]
ex v214,v214
mm v[20:23],a[116:119],v[224:227],v[20:23]
rd a[88:91],v248 offset:6144
cv v234,v212,v213
mm v[24:27],a[120:123],v[224:227],v[24:27]
ex v215,v215
mm v[56:59],a[120:123],v[228:231],v[56:59]
rd a[92:95],v248 offset:7168
cv v235,v214,v215
mm v[60:63],a[124:127],v[228:231],v[60:63]
ex v220,v220
mm v[28:31],a[124:127],v[224:227],v[28:31]
s_cbranch_scc0 .La5_LA_F2
.La5_LA_F2E2:
; ---- helper: last windows; FFN weights and the first weights of the O projection come in
.La5_LA_F3:
.La5_LW6_3:
s_cbranch_execz .La5_LW6_7
wt lgkmcnt(4)
mm v[192:195],a[64:67],a[0:3],0
ex v221,v221
mm v[192:195],a[68:71],a[4:7],v[192:195]
cv v238,v220,v221
s_add_u32 s89, s89, 16384
mm v[192:195],a[72:75],a[8:11],v[192:195]
ex v222,v222
mm v[192:195],a[76:79],a[12:15],v[192:195]
ld v255,s[60:63],s17 offen offset:0 sc1 lds
mm v[200:203],a[76:79],a[28:31],0
ex v223,v223
mm v[200:203],a[72:75],a[24:27],v[200:203]
cv v239,v222,v223
s_and_b32 s89, s89, 0x1ffff
mm v[200:203],a[68:71],a[20:23],v[200:203]
ex v192,v192
mm v[200:203],a[64:67],a[16:19],v[200:203]
wt lgkmcnt(0)
mm v[196:199],a[80:83],a[0:3],0
ex v193,v193
mm v[196:199],a[84:87],a[4:7],v[196:199]
cv v224,v192,v193
ar v243,a229
mm v[196:199],a[88:91],a[8:11],v[196:199]
ex v194,v194
mm v[196:199],a[92:95],a[12:15],v[196:199]
ld v255,s[60:63],s17 offen offset:1024 sc1 lds
mm v[204:207],a[92:95],a[28:31],0
ex v195,v195
mm v[204:207],a[88:91],a[24:27],v[204:207]
cv v225,v194,v195
mm v[204:207],a[84:87],a[20:23],v[204:207]
)ASM" R"ASM(ex v200,v200
mm v[204:207],a[80:83],a[16:19],v[204:207]
ld v255,s[60:63],s17 offen offset:2048 sc1 lds
mm v[208:211],a[64:67],a[32:35],0
ex v201,v201
mm v[208:211],a[68:71],a[36:39],v[208:211]
cv v228,v200,v201
mm v[208:211],a[72:75],a[40:43],v[208:211]
ex v202,v202
mm v[208:211],a[76:79],a[44:47],v[208:211]
ld v255,s[60:63],s17 offen offset:3072 sc1 lds
mm v[216:219],a[76:79],a[60:63],0
ex v203,v203
mm v[216:219],a[72:75],a[56:59],v[216:219]
cv v229,v202,v203
s_mov_b32 s93, 0
mm v[216:219],a[68:71],a[52:55],v[216:219]
ex v196,v196
mm v[216:219],a[64:67],a[48:51],v[216:219]
ld a[128:131],v243,s[40:43],s93 offen
mm v[212:215],a[80:83],a[32:35],0
ex v197,v197
s_add_u32 s17, s17, s80
mm v[212:215],a[84:87],a[36:39],v[212:215]
cv v226,v196,v197
s_mov_b32 s93, 7168
s_add_u32 m0, m0, 16384
mm v[212:215],a[88:91],a[40:43],v[212:215]
ex v198,v198
s_and_b32 m0, m0, s78
mm v[212:215],a[92:95],a[44:47],v[212:215]
ld a[132:135],v243,s[40:43],s93 offen
mm v[220:223],a[92:95],a[60:63],0
ex v199,v199
mm v[220:223],a[88:91],a[56:59],v[220:223]
cv v227,v198,v199
mm v[220:223],a[84:87],a[52:55],v[220:223]
ex v204,v204
mm v[220:223],a[80:83],a[48:51],v[220:223]
wt vmcnt(12)
s_barrier
mm v[64:67],a[96:99],v[232:235],v[64:67]
ex v205,v205
mm v[96:99],a[96:99],v[236:239],v[96:99]
rd a[96:99],v248 offset:8192
cv v230,v204,v205
mm v[100:103],a[100:103],v[236:239],v[100:103]
ex v206,v206
mm v[68:71],a[100:103],v[232:235],v[68:71]
rd a[100:103],v248 offset:9216
mm v[72:75],a[104:107],v[232:235],v[72:75]
ex v207,v207
mm v[104:107],a[104:107],v[236:239],v[104:107]
rd a[104:107],v248 offset:10240
cv v231,v206,v207
s_mov_b32 s93, 14336
mm v[108:111],a[108:111],v[236:239],v[108:111]
ex v208,v208
mm v[76:79],a[108:111],v[232:235],v[76:79]
rd a[108:111],v248 offset:11264
ld a[136:139],v243,s[40:43],s93 offen
mm v[80:83],a[112:115],v[232:235],v[80:83]
ex v209,v209
mm v[112:115],a[112:115],v[236:239],v[112:115]
rd a[112:115],v248 offset:12288
mm v[116:119],a[116:119],v[236:239],v[116:119]
ex v210,v210
mm v[84:87],a[116:119],v[232:235],v[84:87]
rd a[116:119],v248 offset:13312
mm v[88:91],a[120:123],v[232:235],v[88:91]
ex v211,v211
mm v[120:123],a[120:123],v[236:239],v[120:123]
rd a[120:123],v248 offset:14336
mm v[124:127],a[124:127],v[236:239],v[124:127]
ex v216,v216
mm v[92:95],a[124:127],v[232:235],v[92:95]
rd a[124:127],v248 offset:15360
v_or_b32 v248, s89, v255
v_smfmac_f32_16x16x64_bf16 a[196:199], a[208:211], v[232:239], v241
wt lgkmcnt(4)
v_smfmac_f32_16x16x64_bf16 a[192:195], a[208:211], v[224:231], v241
rd a[64:67],v248 offset:0
mm v[0:3],a[96:99],v[224:227],v[0:3]
ex v217,v217
mm v[32:35],a[96:99],v[228:231],v[32:35]
rd a[68:71],v248 offset:1024
cv v232,v208,v209
mm v[36:39],a[100:103],v[228:231],v[36:39]
ex v218,v218
mm v[4:7],a[100:103],v[224:227],v[4:7]
rd a[72:75],v248 offset:2048
cv v233,v210,v211
mm v[8:11],a[104:107],v[224:227],v[8:11]
ex v219,v219
mm v[40:43],a[104:107],v[228:231],v[40:43]
rd a[76:79],v248 offset:3072
cv v236,v216,v217
mm v[44:47],a[108:111],v[228:231],v[44:47]
ex v212,v212
mm v[12:15],a[108:111],v[224:227],v[12:15]
)ASM" R"ASM(rd a[80:83],v248 offset:4096
wt lgkmcnt(5)
mm v[16:19],a[112:115],v[224:227],v[16:19]
ex v213,v213
mm v[48:51],a[112:115],v[228:231],v[48:51]
rd a[84:87],v248 offset:5120
cv v237,v218,v219
mm v[52:55],a[116:119],v[228:231],v[52:55]
ex v214,v214
mm v[20:23],a[116:119],v[224:227],v[20:23]
rd a[88:91],v248 offset:6144
cv v234,v212,v213
mm v[24:27],a[120:123],v[224:227],v[24:27]
ex v215,v215
mm v[56:59],a[120:123],v[228:231],v[56:59]
rd a[92:95],v248 offset:7168
cv v235,v214,v215
mm v[60:63],a[124:127],v[228:231],v[60:63]
ex v220,v220
mm v[28:31],a[124:127],v[224:227],v[28:31]
wt lgkmcnt(4)
mm v[192:195],a[64:67],a[0:3],0
ex v221,v221
mm v[192:195],a[68:71],a[4:7],v[192:195]
cv v238,v220,v221
s_add_u32 s89, s89, 16384
mm v[192:195],a[72:75],a[8:11],v[192:195]
ex v222,v222
mm v[192:195],a[76:79],a[12:15],v[192:195]
ld v255,s[60:63],s17 offen offset:0 sc1 lds
mm v[200:203],a[76:79],a[28:31],0
ex v223,v223
mm v[200:203],a[72:75],a[24:27],v[200:203]
cv v239,v222,v223
s_and_b32 s89, s89, 0x1ffff
mm v[200:203],a[68:71],a[20:23],v[200:203]
ex v192,v192
mm v[200:203],a[64:67],a[16:19],v[200:203]
wt lgkmcnt(0)
mm v[196:199],a[80:83],a[0:3],0
ex v193,v193
mm v[196:199],a[84:87],a[4:7],v[196:199]
cv v224,v192,v193
mm v[196:199],a[88:91],a[8:11],v[196:199]
ex v194,v194
mm v[196:199],a[92:95],a[12:15],v[196:199]
ld v255,s[60:63],s17 offen offset:1024 sc1 lds
mm v[204:207],a[92:95],a[28:31],0
ex v195,v195
mm v[204:207],a[88:91],a[24:27],v[204:207]
cv v225,v194,v195
mm v[204:207],a[84:87],a[20:23],v[204:207]
ex v200,v200
mm v[204:207],a[80:83],a[16:19],v[204:207]
ld v255,s[60:63],s17 offen offset:2048 sc1 lds
mm v[208:211],a[64:67],a[32:35],0
ex v201,v201
mm v[208:211],a[68:71],a[36:39],v[208:211]
cv v228,v200,v201
mm v[208:211],a[72:75],a[40:43],v[208:211]
ex v202,v202
mm v[208:211],a[76:79],a[44:47],v[208:211]
ld v255,s[60:63],s17 offen offset:3072 sc1 lds
mm v[216:219],a[76:79],a[60:63],0
ex v203,v203
mm v[216:219],a[72:75],a[56:59],v[216:219]
cv v229,v202,v203
s_mov_b32 s93, 21504
mm v[216:219],a[68:71],a[52:55],v[216:219]
ex v196,v196
mm v[216:219],a[64:67],a[48:51],v[216:219]
ld a[140:143],v243,s[40:43],s93 offen
mm v[212:215],a[80:83],a[32:35],0
ex v197,v197
s_add_u32 s17, s17, s80
mm v[212:215],a[84:87],a[36:39],v[212:215]
cv v226,v196,v197
s_mov_b32 s93, 28672
s_add_u32 m0, m0, 16384
mm v[212:215],a[88:91],a[40:43],v[212:215]
ex v198,v198
s_and_b32 m0, m0, s78
mm v[212:215],a[92:95],a[44:47],v[212:215]
ld a[144:147],v243,s[40:43],s93 offen
mm v[220:223],a[92:95],a[60:63],0
ex v199,v199
mm v[220:223],a[88:91],a[56:59],v[220:223]
cv v227,v198,v199
mm v[220:223],a[84:87],a[52:55],v[220:223]
ex v204,v204
mm v[220:223],a[80:83],a[48:51],v[220:223]
wt vmcnt(14)
s_barrier
mm v[64:67],a[96:99],v[232:235],v[64:67]
ex v205,v205
mm v[96:99],a[96:99],v[236:239],v[96:99]
rd a[96:99],v248 offset:8192
cv v230,v204,v205
mm v[100:103],a[100:103],v[236:239],v[100:103]
ex v206,v206
)ASM" R"ASM(mm v[68:71],a[100:103],v[232:235],v[68:71]
rd a[100:103],v248 offset:9216
mm v[72:75],a[104:107],v[232:235],v[72:75]
ex v207,v207
mm v[104:107],a[104:107],v[236:239],v[104:107]
rd a[104:107],v248 offset:10240
cv v231,v206,v207
s_mov_b32 s93, 35840
mm v[108:111],a[108:111],v[236:239],v[108:111]
ex v208,v208
mm v[76:79],a[108:111],v[232:235],v[76:79]
rd a[108:111],v248 offset:11264
ld a[148:151],v243,s[40:43],s93 offen
mm v[80:83],a[112:115],v[232:235],v[80:83]
ex v209,v209
mm v[112:115],a[112:115],v[236:239],v[112:115]
rd a[112:115],v248 offset:12288
mm v[116:119],a[116:119],v[236:239],v[116:119]
ex v210,v210
mm v[84:87],a[116:119],v[232:235],v[84:87]
rd a[116:119],v248 offset:13312
mm v[88:91],a[120:123],v[232:235],v[88:91]
ex v211,v211
mm v[120:123],a[120:123],v[236:239],v[120:123]
rd a[120:123],v248 offset:14336
ar v242,a231
wt vmcnt(14)
mm v[124:127],a[124:127],v[236:239],v[124:127]
ex v216,v216
mm v[92:95],a[124:127],v[232:235],v[92:95]
rd a[124:127],v248 offset:15360
v_or_b32 v248, s89, v255
v_smfmac_f32_16x16x64_bf16 a[196:199], a[208:211], v[232:239], v241
wt lgkmcnt(4)
v_smfmac_f32_16x16x64_bf16 a[192:195], a[208:211], v[224:231], v241
rd a[64:67],v248 offset:0
mm v[0:3],a[96:99],v[224:227],v[0:3]
ex v217,v217
mm v[32:35],a[96:99],v[228:231],v[32:35]
rd a[68:71],v248 offset:1024
cv v232,v208,v209
mm v[36:39],a[100:103],v[228:231],v[36:39]
ex v218,v218
mm v[4:7],a[100:103],v[224:227],v[4:7]
rd a[72:75],v248 offset:2048
cv v233,v210,v211
mm v[8:11],a[104:107],v[224:227],v[8:11]
ex v219,v219
mm v[40:43],a[104:107],v[228:231],v[40:43]
rd a[76:79],v248 offset:3072
cv v236,v216,v217
mm v[44:47],a[108:111],v[228:231],v[44:47]
ex v212,v212
mm v[12:15],a[108:111],v[224:227],v[12:15]
rd a[80:83],v248 offset:4096
wt lgkmcnt(5)
mm v[16:19],a[112:115],v[224:227],v[16:19]
ex v213,v213
mm v[48:51],a[112:115],v[228:231],v[48:51]
rd a[84:87],v248 offset:5120
cv v237,v218,v219
mm v[52:55],a[116:119],v[228:231],v[52:55]
ex v214,v214
mm v[20:23],a[116:119],v[224:227],v[20:23]
rd a[88:91],v248 offset:6144
cv v234,v212,v213
mm v[24:27],a[120:123],v[224:227],v[24:27]
ex v215,v215
mm v[56:59],a[120:123],v[228:231],v[56:59]
rd a[92:95],v248 offset:7168
cv v235,v214,v215
mm v[60:63],a[124:127],v[228:231],v[60:63]
ex v220,v220
mm v[28:31],a[124:127],v[224:227],v[28:31]
buffer_store_dwordx2 v[252:253], v242, s[64:67], s6 offen offset:0 sc1
wt lgkmcnt(4)
mm v[192:195],a[64:67],a[0:3],0
ex v221,v221
mm v[192:195],a[68:71],a[4:7],v[192:195]
cv v238,v220,v221
s_add_u32 s89, s89, 16384
mm v[192:195],a[72:75],a[8:11],v[192:195]
ex v222,v222
mm v[192:195],a[76:79],a[12:15],v[192:195]
ld v255,s[60:63],s17 offen offset:0 sc1 lds
mm v[200:203],a[76:79],a[28:31],0
ex v223,v223
mm v[200:203],a[72:75],a[24:27],v[200:203]
cv v239,v222,v223
s_and_b32 s89, s89, 0x1ffff
mm v[200:203],a[68:71],a[20:23],v[200:203]
ex v192,v192
mm v[200:203],a[64:67],a[16:19],v[200:203]
wt lgkmcnt(0)
mm v[196:199],a[80:83],a[0:3],0
ex v193,v193
mm v[196:199],a[84:87],a[4:7],v[196:199]
cv v224,v192,v193
mm v[196:199],a[88:91],a[8:11],v[196:199]
ex v194,v194
)ASM" R"ASM(mm v[196:199],a[92:95],a[12:15],v[196:199]
ld v255,s[60:63],s17 offen offset:1024 sc1 lds
mm v[204:207],a[92:95],a[28:31],0
ex v195,v195
mm v[204:207],a[88:91],a[24:27],v[204:207]
cv v225,v194,v195
mm v[204:207],a[84:87],a[20:23],v[204:207]
ex v200,v200
mm v[204:207],a[80:83],a[16:19],v[204:207]
ld v255,s[60:63],s17 offen offset:2048 sc1 lds
mm v[208:211],a[64:67],a[32:35],0
ex v201,v201
mm v[208:211],a[68:71],a[36:39],v[208:211]
cv v228,v200,v201
mm v[208:211],a[72:75],a[40:43],v[208:211]
ex v202,v202
mm v[208:211],a[76:79],a[44:47],v[208:211]
ld v255,s[60:63],s17 offen offset:3072 sc1 lds
mm v[216:219],a[76:79],a[60:63],0
ex v203,v203
mm v[216:219],a[72:75],a[56:59],v[216:219]
cv v229,v202,v203
s_mov_b32 s93, 43008
mm v[216:219],a[68:71],a[52:55],v[216:219]
ex v196,v196
mm v[216:219],a[64:67],a[48:51],v[216:219]
ld a[152:155],v243,s[40:43],s93 offen
mm v[212:215],a[80:83],a[32:35],0
ex v197,v197
s_add_u32 s17, s17, s80
mm v[212:215],a[84:87],a[36:39],v[212:215]
cv v226,v196,v197
s_mov_b32 s93, 50176
s_add_u32 m0, m0, 16384
mm v[212:215],a[88:91],a[40:43],v[212:215]
ex v198,v198
s_and_b32 m0, m0, s78
mm v[212:215],a[92:95],a[44:47],v[212:215]
ld a[156:159],v243,s[40:43],s93 offen
mm v[220:223],a[92:95],a[60:63],0
ex v199,v199
mm v[220:223],a[88:91],a[56:59],v[220:223]
cv v227,v198,v199
mm v[220:223],a[84:87],a[52:55],v[220:223]
ex v204,v204
mm v[220:223],a[80:83],a[48:51],v[220:223]
wt vmcnt(17)
s_barrier
mm v[64:67],a[96:99],v[232:235],v[64:67]
ex v205,v205
mm v[96:99],a[96:99],v[236:239],v[96:99]
rd a[96:99],v248 offset:8192
cv v230,v204,v205
mm v[100:103],a[100:103],v[236:239],v[100:103]
ex v206,v206
mm v[68:71],a[100:103],v[232:235],v[68:71]
rd a[100:103],v248 offset:9216
mm v[72:75],a[104:107],v[232:235],v[72:75]
ex v207,v207
mm v[104:107],a[104:107],v[236:239],v[104:107]
rd a[104:107],v248 offset:10240
cv v231,v206,v207
s_mov_b32 s93, 57344
mm v[108:111],a[108:111],v[236:239],v[108:111]
ex v208,v208
mm v[76:79],a[108:111],v[232:235],v[76:79]
rd a[108:111],v248 offset:11264
ld a[160:163],v243,s[40:43],s93 offen
mm v[80:83],a[112:115],v[232:235],v[80:83]
ex v209,v209
mm v[112:115],a[112:115],v[236:239],v[112:115]
rd a[112:115],v248 offset:12288
mm v[116:119],a[116:119],v[236:239],v[116:119]
ex v210,v210
mm v[84:87],a[116:119],v[232:235],v[84:87]
rd a[116:119],v248 offset:13312
mm v[88:91],a[120:123],v[232:235],v[88:91]
ex v211,v211
mm v[120:123],a[120:123],v[236:239],v[120:123]
rd a[120:123],v248 offset:14336
mm v[124:127],a[124:127],v[236:239],v[124:127]
ex v216,v216
mm v[92:95],a[124:127],v[232:235],v[92:95]
rd a[124:127],v248 offset:15360
v_or_b32 v248, s89, v255
v_smfmac_f32_16x16x64_bf16 a[196:199], a[208:211], v[232:239], v241
wt lgkmcnt(4)
v_smfmac_f32_16x16x64_bf16 a[192:195], a[208:211], v[224:231], v241
rd a[64:67],v248 offset:0
mm v[0:3],a[96:99],v[224:227],v[0:3]
ex v217,v217
mm v[32:35],a[96:99],v[228:231],v[32:35]
rd a[68:71],v248 offset:1024
cv v232,v208,v209
mm v[36:39],a[100:103],v[228:231],v[36:39]
ex v218,v218
mm v[4:7],a[100:103],v[224:227],v[4:7]
rd a[72:75],v248 offset:2048
cv v233,v210,v211
mm v[8:11],a[104:107],v[224:227],v[8:11]
)ASM" R"ASM(ex v219,v219
mm v[40:43],a[104:107],v[228:231],v[40:43]
rd a[76:79],v248 offset:3072
cv v236,v216,v217
mm v[44:47],a[108:111],v[228:231],v[44:47]
ex v212,v212
mm v[12:15],a[108:111],v[224:227],v[12:15]
rd a[80:83],v248 offset:4096
wt lgkmcnt(5)
mm v[16:19],a[112:115],v[224:227],v[16:19]
ex v213,v213
mm v[48:51],a[112:115],v[228:231],v[48:51]
rd a[84:87],v248 offset:5120
cv v237,v218,v219
mm v[52:55],a[116:119],v[228:231],v[52:55]
ex v214,v214
mm v[20:23],a[116:119],v[224:227],v[20:23]
rd a[88:91],v248 offset:6144
cv v234,v212,v213
mm v[24:27],a[120:123],v[224:227],v[24:27]
ex v215,v215
mm v[56:59],a[120:123],v[228:231],v[56:59]
rd a[92:95],v248 offset:7168
cv v235,v214,v215
mm v[60:63],a[124:127],v[228:231],v[60:63]
ex v220,v220
mm v[28:31],a[124:127],v[224:227],v[28:31]
wt lgkmcnt(4)
mm v[192:195],a[64:67],a[0:3],0
ex v221,v221
mm v[192:195],a[68:71],a[4:7],v[192:195]
cv v238,v220,v221
ar v240,a228
mm v[192:195],a[72:75],a[8:11],v[192:195]
ex v222,v222
mm v[192:195],a[76:79],a[12:15],v[192:195]
s_add_u32 m0, s83, 16896
s_lshl_b32 s17, s72, 18
mm v[200:203],a[76:79],a[28:31],0
ex v223,v223
mm v[200:203],a[72:75],a[24:27],v[200:203]
cv v239,v222,v223
s_add_u32 s89, s89, 16384
mm v[200:203],a[68:71],a[20:23],v[200:203]
ex v192,v192
mm v[200:203],a[64:67],a[16:19],v[200:203]
wt lgkmcnt(0)
s_and_b32 s89, s89, 0x1ffff
mm v[196:199],a[80:83],a[0:3],0
ex v193,v193
mm v[196:199],a[84:87],a[4:7],v[196:199]
cv v224,v192,v193
mm v[196:199],a[88:91],a[8:11],v[196:199]
ex v194,v194
mm v[196:199],a[92:95],a[12:15],v[196:199]
ld v240,s[56:59],s17 offen lds
mm v[204:207],a[92:95],a[28:31],0
ex v195,v195
s_add_u32 s17, s17, 32768
mm v[204:207],a[88:91],a[24:27],v[204:207]
cv v225,v194,v195
s_add_u32 m0, m0, 1024
mm v[204:207],a[84:87],a[20:23],v[204:207]
ex v200,v200
mm v[204:207],a[80:83],a[16:19],v[204:207]
.La5_LW6_4:
s_cbranch_execz .La5_LW6_R0
ld v240,s[56:59],s17 offen lds
mm v[208:211],a[64:67],a[32:35],0
ex v201,v201
s_add_u32 s17, s17, 4294942720
mm v[208:211],a[68:71],a[36:39],v[208:211]
cv v228,v200,v201
s_add_u32 m0, m0, 1152
mm v[208:211],a[72:75],a[40:43],v[208:211]
ex v202,v202
mm v[208:211],a[76:79],a[44:47],v[208:211]
ld v240,s[56:59],s17 offen lds
mm v[216:219],a[76:79],a[60:63],0
ex v203,v203
s_add_u32 s17, s17, 32768
mm v[216:219],a[72:75],a[56:59],v[216:219]
cv v229,v202,v203
s_add_u32 m0, m0, 1024
mm v[216:219],a[68:71],a[52:55],v[216:219]
ex v196,v196
mm v[216:219],a[64:67],a[48:51],v[216:219]
ld v240,s[56:59],s17 offen lds
mm v[212:215],a[80:83],a[32:35],0
ex v197,v197
s_add_u32 s17, s17, 90112
mm v[212:215],a[84:87],a[36:39],v[212:215]
cv v226,v196,v197
s_mov_b32 s93, 64512
s_add_u32 m0, s83, 33792
mm v[212:215],a[88:91],a[40:43],v[212:215]
ex v198,v198
mm v[212:215],a[92:95],a[44:47],v[212:215]
ld a[164:167],v243,s[40:43],s93 offen
mm v[220:223],a[92:95],a[60:63],0
)ASM" R"ASM(ex v199,v199
mm v[220:223],a[88:91],a[56:59],v[220:223]
cv v227,v198,v199
mm v[220:223],a[84:87],a[52:55],v[220:223]
ex v204,v204
mm v[220:223],a[80:83],a[48:51],v[220:223]
wt vmcnt(16)
s_barrier
mm v[64:67],a[96:99],v[232:235],v[64:67]
ex v205,v205
mm v[96:99],a[96:99],v[236:239],v[96:99]
rd a[96:99],v248 offset:8192
cv v230,v204,v205
mm v[100:103],a[100:103],v[236:239],v[100:103]
ex v206,v206
mm v[68:71],a[100:103],v[232:235],v[68:71]
rd a[100:103],v248 offset:9216
mm v[72:75],a[104:107],v[232:235],v[72:75]
ex v207,v207
mm v[104:107],a[104:107],v[236:239],v[104:107]
rd a[104:107],v248 offset:10240
cv v231,v206,v207
s_mov_b32 s93, 71680
mm v[108:111],a[108:111],v[236:239],v[108:111]
ex v208,v208
mm v[76:79],a[108:111],v[232:235],v[76:79]
rd a[108:111],v248 offset:11264
ld a[168:171],v243,s[40:43],s93 offen
mm v[80:83],a[112:115],v[232:235],v[80:83]
ex v209,v209
mm v[112:115],a[112:115],v[236:239],v[112:115]
rd a[112:115],v248 offset:12288
s_mov_b32 s93, 78848
mm v[116:119],a[116:119],v[236:239],v[116:119]
ex v210,v210
mm v[84:87],a[116:119],v[232:235],v[84:87]
rd a[116:119],v248 offset:13312
ld a[172:175],v243,s[40:43],s93 offen
mm v[88:91],a[120:123],v[232:235],v[88:91]
ex v211,v211
mm v[120:123],a[120:123],v[236:239],v[120:123]
rd a[120:123],v248 offset:14336
mm v[124:127],a[124:127],v[236:239],v[124:127]
ex v216,v216
mm v[92:95],a[124:127],v[232:235],v[92:95]
rd a[124:127],v248 offset:15360
v_or_b32 v248, s89, v255
v_smfmac_f32_16x16x64_bf16 a[196:199], a[208:211], v[232:239], v241
wt lgkmcnt(4)
v_smfmac_f32_16x16x64_bf16 a[192:195], a[208:211], v[224:231], v241
rd a[64:67],v248 offset:0
mm v[0:3],a[96:99],v[224:227],v[0:3]
ex v217,v217
mm v[32:35],a[96:99],v[228:231],v[32:35]
rd a[68:71],v248 offset:1024
cv v232,v208,v209
mm v[36:39],a[100:103],v[228:231],v[36:39]
ex v218,v218
mm v[4:7],a[100:103],v[224:227],v[4:7]
rd a[72:75],v248 offset:2048
cv v233,v210,v211
mm v[8:11],a[104:107],v[224:227],v[8:11]
ex v219,v219
mm v[40:43],a[104:107],v[228:231],v[40:43]
rd a[76:79],v248 offset:3072
cv v236,v216,v217
mm v[44:47],a[108:111],v[228:231],v[44:47]
ex v212,v212
mm v[12:15],a[108:111],v[224:227],v[12:15]
rd a[80:83],v248 offset:4096
wt lgkmcnt(5)
mm v[16:19],a[112:115],v[224:227],v[16:19]
ex v213,v213
mm v[48:51],a[112:115],v[228:231],v[48:51]
rd a[84:87],v248 offset:5120
cv v237,v218,v219
mm v[52:55],a[116:119],v[228:231],v[52:55]
ex v214,v214
mm v[20:23],a[116:119],v[224:227],v[20:23]
rd a[88:91],v248 offset:6144
cv v234,v212,v213
mm v[24:27],a[120:123],v[224:227],v[24:27]
ex v215,v215
mm v[56:59],a[120:123],v[228:231],v[56:59]
rd a[92:95],v248 offset:7168
cv v235,v214,v215
mm v[60:63],a[124:127],v[228:231],v[60:63]
ex v220,v220
mm v[28:31],a[124:127],v[224:227],v[28:31]
wt lgkmcnt(4)
mm v[192:195],a[64:67],a[0:3],0
ex v221,v221
mm v[192:195],a[68:71],a[4:7],v[192:195]
cv v238,v220,v221
s_add_u32 s89, s89, 16384
mm v[192:195],a[72:75],a[8:11],v[192:195]
ex v222,v222
mm v[192:195],a[76:79],a[12:15],v[192:195]
ld v240,s[56:59],s17 offen lds
mm v[200:203],a[76:79],a[28:31],0
)ASM" R"ASM(ex v223,v223
s_add_u32 s17, s17, 32768
mm v[200:203],a[72:75],a[24:27],v[200:203]
cv v239,v222,v223
s_add_u32 m0, m0, 1024
mm v[200:203],a[68:71],a[20:23],v[200:203]
ex v192,v192
mm v[200:203],a[64:67],a[16:19],v[200:203]
wt lgkmcnt(0)
s_and_b32 s89, s89, 0x1ffff
mm v[196:199],a[80:83],a[0:3],0
ex v193,v193
mm v[196:199],a[84:87],a[4:7],v[196:199]
cv v224,v192,v193
mm v[196:199],a[88:91],a[8:11],v[196:199]
ex v194,v194
mm v[196:199],a[92:95],a[12:15],v[196:199]
ld v240,s[56:59],s17 offen lds
mm v[204:207],a[92:95],a[28:31],0
ex v195,v195
s_add_u32 s17, s17, 4294942720
mm v[204:207],a[88:91],a[24:27],v[204:207]
cv v225,v194,v195
s_add_u32 m0, m0, 1152
mm v[204:207],a[84:87],a[20:23],v[204:207]
ex v200,v200
mm v[204:207],a[80:83],a[16:19],v[204:207]
ld v240,s[56:59],s17 offen lds
mm v[208:211],a[64:67],a[32:35],0
ex v201,v201
s_add_u32 s17, s17, 32768
mm v[208:211],a[68:71],a[36:39],v[208:211]
cv v228,v200,v201
s_add_u32 m0, m0, 1024
mm v[208:211],a[72:75],a[40:43],v[208:211]
ex v202,v202
mm v[208:211],a[76:79],a[44:47],v[208:211]
ld v240,s[56:59],s17 offen lds
mm v[216:219],a[76:79],a[60:63],0
ex v203,v203
mm v[216:219],a[72:75],a[56:59],v[216:219]
cv v229,v202,v203
s_mov_b32 s93, 86016
mm v[216:219],a[68:71],a[52:55],v[216:219]
ex v196,v196
mm v[216:219],a[64:67],a[48:51],v[216:219]
ld a[176:179],v243,s[40:43],s93 offen
mm v[212:215],a[80:83],a[32:35],0
ex v197,v197
s_bfe_u32 s17, s4, 262148
mm v[212:215],a[84:87],a[36:39],v[212:215]
cv v226,v196,v197
s_mov_b32 s93, 93184
s_xor_b32 s17, s17, s72
mm v[212:215],a[88:91],a[40:43],v[212:215]
ex v198,v198
s_lshl_b32 s17, s17, 18
mm v[212:215],a[92:95],a[44:47],v[212:215]
ld a[180:183],v243,s[40:43],s93 offen
s_add_u32 m0, s83, 50688
mm v[220:223],a[92:95],a[60:63],0
ex v199,v199
mm v[220:223],a[88:91],a[56:59],v[220:223]
cv v227,v198,v199
mm v[220:223],a[84:87],a[52:55],v[220:223]
ex v204,v204
mm v[220:223],a[80:83],a[48:51],v[220:223]
wt vmcnt(16)
s_barrier
mm v[64:67],a[96:99],v[232:235],v[64:67]
ex v205,v205
mm v[96:99],a[96:99],v[236:239],v[96:99]
rd a[96:99],v248 offset:8192
cv v230,v204,v205
mm v[100:103],a[100:103],v[236:239],v[100:103]
ex v206,v206
mm v[68:71],a[100:103],v[232:235],v[68:71]
rd a[100:103],v248 offset:9216
mm v[72:75],a[104:107],v[232:235],v[72:75]
ex v207,v207
mm v[104:107],a[104:107],v[236:239],v[104:107]
rd a[104:107],v248 offset:10240
cv v231,v206,v207
s_mov_b32 s93, 100352
mm v[108:111],a[108:111],v[236:239],v[108:111]
ex v208,v208
mm v[76:79],a[108:111],v[232:235],v[76:79]
rd a[108:111],v248 offset:11264
ld a[184:187],v243,s[40:43],s93 offen
mm v[80:83],a[112:115],v[232:235],v[80:83]
ex v209,v209
mm v[112:115],a[112:115],v[236:239],v[112:115]
rd a[112:115],v248 offset:12288
mm v[116:119],a[116:119],v[236:239],v[116:119]
ex v210,v210
mm v[84:87],a[116:119],v[232:235],v[84:87]
rd a[116:119],v248 offset:13312
mm v[88:91],a[120:123],v[232:235],v[88:91]
ex v211,v211
mm v[120:123],a[120:123],v[236:239],v[120:123]
)ASM" R"ASM(rd a[120:123],v248 offset:14336
mm v[124:127],a[124:127],v[236:239],v[124:127]
ex v216,v216
mm v[92:95],a[124:127],v[232:235],v[92:95]
rd a[124:127],v248 offset:15360
v_or_b32 v248, s89, v255
v_smfmac_f32_16x16x64_bf16 a[196:199], a[208:211], v[232:239], v241
wt lgkmcnt(4)
v_smfmac_f32_16x16x64_bf16 a[192:195], a[208:211], v[224:231], v241
rd a[64:67],v248 offset:0
mm v[0:3],a[96:99],v[224:227],v[0:3]
ex v217,v217
mm v[32:35],a[96:99],v[228:231],v[32:35]
rd a[68:71],v248 offset:1024
cv v232,v208,v209
mm v[36:39],a[100:103],v[228:231],v[36:39]
ex v218,v218
mm v[4:7],a[100:103],v[224:227],v[4:7]
rd a[72:75],v248 offset:2048
cv v233,v210,v211
mm v[8:11],a[104:107],v[224:227],v[8:11]
ex v219,v219
mm v[40:43],a[104:107],v[228:231],v[40:43]
rd a[76:79],v248 offset:3072
cv v236,v216,v217
mm v[44:47],a[108:111],v[228:231],v[44:47]
ex v212,v212
mm v[12:15],a[108:111],v[224:227],v[12:15]
rd a[80:83],v248 offset:4096
wt lgkmcnt(5)
mm v[16:19],a[112:115],v[224:227],v[16:19]
ex v213,v213
mm v[48:51],a[112:115],v[228:231],v[48:51]
rd a[84:87],v248 offset:5120
cv v237,v218,v219
mm v[52:55],a[116:119],v[228:231],v[52:55]
ex v214,v214
mm v[20:23],a[116:119],v[224:227],v[20:23]
rd a[88:91],v248 offset:6144
cv v234,v212,v213
mm v[24:27],a[120:123],v[224:227],v[24:27]
ex v215,v215
mm v[56:59],a[120:123],v[228:231],v[56:59]
rd a[92:95],v248 offset:7168
cv v235,v214,v215
mm v[60:63],a[124:127],v[228:231],v[60:63]
ex v220,v220
mm v[28:31],a[124:127],v[224:227],v[28:31]
wt lgkmcnt(4)
mm v[192:195],a[64:67],a[0:3],0
ex v221,v221
mm v[192:195],a[68:71],a[4:7],v[192:195]
cv v238,v220,v221
mm v[192:195],a[72:75],a[8:11],v[192:195]
ex v222,v222
mm v[192:195],a[76:79],a[12:15],v[192:195]
ld v240,s[56:59],s17 offen lds
mm v[200:203],a[76:79],a[28:31],0
ex v223,v223
s_add_u32 s17, s17, 32768
mm v[200:203],a[72:75],a[24:27],v[200:203]
cv v239,v222,v223
s_add_u32 m0, m0, 1024
mm v[200:203],a[68:71],a[20:23],v[200:203]
ex v192,v192
mm v[200:203],a[64:67],a[16:19],v[200:203]
wt lgkmcnt(0)
mm v[196:199],a[80:83],a[0:3],0
ex v193,v193
mm v[196:199],a[84:87],a[4:7],v[196:199]
cv v224,v192,v193
mm v[196:199],a[88:91],a[8:11],v[196:199]
ex v194,v194
mm v[196:199],a[92:95],a[12:15],v[196:199]
ld v240,s[56:59],s17 offen lds
mm v[204:207],a[92:95],a[28:31],0
ex v195,v195
s_add_u32 s17, s17, 4294942720
mm v[204:207],a[88:91],a[24:27],v[204:207]
cv v225,v194,v195
s_add_u32 m0, m0, 1152
mm v[204:207],a[84:87],a[20:23],v[204:207]
ex v200,v200
mm v[204:207],a[80:83],a[16:19],v[204:207]
ld v240,s[56:59],s17 offen lds
mm v[208:211],a[64:67],a[32:35],0
ex v201,v201
s_add_u32 s17, s17, 32768
mm v[208:211],a[68:71],a[36:39],v[208:211]
cv v228,v200,v201
s_add_u32 m0, m0, 1024
mm v[208:211],a[72:75],a[40:43],v[208:211]
ex v202,v202
mm v[208:211],a[76:79],a[44:47],v[208:211]
ld v240,s[56:59],s17 offen lds
mm v[216:219],a[76:79],a[60:63],0
ex v203,v203
mm v[216:219],a[72:75],a[56:59],v[216:219]
cv v229,v202,v203
s_mov_b32 s93, 107520
)ASM" R"ASM(mm v[216:219],a[68:71],a[52:55],v[216:219]
ex v196,v196
mm v[216:219],a[64:67],a[48:51],v[216:219]
ld a[188:191],v243,s[40:43],s93 offen
mm v[212:215],a[80:83],a[32:35],0
ex v197,v197
mm v[212:215],a[84:87],a[36:39],v[212:215]
cv v226,v196,v197
mm v[212:215],a[88:91],a[40:43],v[212:215]
ex v198,v198
mm v[212:215],a[92:95],a[44:47],v[212:215]
mm v[220:223],a[92:95],a[60:63],0
ex v199,v199
mm v[220:223],a[88:91],a[56:59],v[220:223]
cv v227,v198,v199
mm v[220:223],a[84:87],a[52:55],v[220:223]
ex v204,v204
mm v[220:223],a[80:83],a[48:51],v[220:223]
mm v[64:67],a[96:99],v[232:235],v[64:67]
ex v205,v205
mm v[96:99],a[96:99],v[236:239],v[96:99]
rd a[96:99],v248 offset:8192
cv v230,v204,v205
mm v[100:103],a[100:103],v[236:239],v[100:103]
ex v206,v206
mm v[68:71],a[100:103],v[232:235],v[68:71]
rd a[100:103],v248 offset:9216
mm v[72:75],a[104:107],v[232:235],v[72:75]
ex v207,v207
mm v[104:107],a[104:107],v[236:239],v[104:107]
rd a[104:107],v248 offset:10240
cv v231,v206,v207
mm v[108:111],a[108:111],v[236:239],v[108:111]
ex v208,v208
mm v[76:79],a[108:111],v[232:235],v[76:79]
rd a[108:111],v248 offset:11264
mm v[80:83],a[112:115],v[232:235],v[80:83]
ex v209,v209
mm v[112:115],a[112:115],v[236:239],v[112:115]
rd a[112:115],v248 offset:12288
mm v[116:119],a[116:119],v[236:239],v[116:119]
ex v210,v210
mm v[84:87],a[116:119],v[232:235],v[84:87]
rd a[116:119],v248 offset:13312
mm v[88:91],a[120:123],v[232:235],v[88:91]
ex v211,v211
mm v[120:123],a[120:123],v[236:239],v[120:123]
rd a[120:123],v248 offset:14336
mm v[124:127],a[124:127],v[236:239],v[124:127]
ex v216,v216
mm v[92:95],a[124:127],v[232:235],v[92:95]
rd a[124:127],v248 offset:15360
v_smfmac_f32_16x16x64_bf16 a[196:199], a[208:211], v[232:239], v241
wt lgkmcnt(4)
v_smfmac_f32_16x16x64_bf16 a[192:195], a[208:211], v[224:231], v241
mm v[0:3],a[96:99],v[224:227],v[0:3]
ex v217,v217
mm v[32:35],a[96:99],v[228:231],v[32:35]
cv v232,v208,v209
mm v[36:39],a[100:103],v[228:231],v[36:39]
ex v218,v218
mm v[4:7],a[100:103],v[224:227],v[4:7]
cv v233,v210,v211
mm v[8:11],a[104:107],v[224:227],v[8:11]
ex v219,v219
mm v[40:43],a[104:107],v[228:231],v[40:43]
cv v236,v216,v217
mm v[44:47],a[108:111],v[228:231],v[44:47]
ex v212,v212
mm v[12:15],a[108:111],v[224:227],v[12:15]
wt lgkmcnt(0)
mm v[16:19],a[112:115],v[224:227],v[16:19]
ex v213,v213
mm v[48:51],a[112:115],v[228:231],v[48:51]
cv v237,v218,v219
mm v[52:55],a[116:119],v[228:231],v[52:55]
ex v214,v214
mm v[20:23],a[116:119],v[224:227],v[20:23]
cv v234,v212,v213
mm v[24:27],a[120:123],v[224:227],v[24:27]
ex v215,v215
mm v[56:59],a[120:123],v[228:231],v[56:59]
cv v235,v214,v215
mm v[60:63],a[124:127],v[228:231],v[60:63]
ex v220,v220
mm v[28:31],a[124:127],v[224:227],v[28:31]
; ---- helper: partial O and l go to the partner
.La5_LA_DP:
ex v221,v221
ex v222,v222
ex v223,v223
cv v238,v220,v221
s_nop 0
cv v239,v222,v223
s_nop 1
v_smfmac_f32_16x16x64_bf16 a[196:199], a[208:211], v[232:239], v241
cv v0,v0,v1
cv v1,v2,v3
cv v2,v4,v5
cv v3,v6,v7
)ASM" R"ASM(st v[0:3],v255,s[68:71],0 offen offset:0 sc1
mm v[64:67],a[96:99],v[232:235],v[64:67]
cv v8,v8,v9
cv v9,v10,v11
cv v10,v12,v13
cv v11,v14,v15
st v[8:11],v255,s[68:71],0 offen offset:1024 sc1
mm v[96:99],a[96:99],v[236:239],v[96:99]
ar v240,a224
v_xor_b32 v241, 32, v240
cv v16,v16,v17
cv v17,v18,v19
cv v18,v20,v21
cv v19,v22,v23
mm v[100:103],a[100:103],v[236:239],v[100:103]
v_xor_b32 v242, 64, v240
v_xor_b32 v243, 96, v240
st v[16:19],v255,s[68:71],0 offen offset:2048 sc1
cv v24,v24,v25
cv v25,v26,v27
mm v[68:71],a[100:103],v[232:235],v[68:71]
cv v26,v28,v29
cv v27,v30,v31
st v[24:27],v255,s[68:71],0 offen offset:3072 sc1
cv v32,v32,v33
cv v33,v34,v35
mm v[72:75],a[104:107],v[232:235],v[72:75]
cv v34,v36,v37
cv v35,v38,v39
st v[32:35],v255,s[68:71],s95 offen offset:0 sc1
cv v40,v40,v41
cv v41,v42,v43
mm v[104:107],a[104:107],v[236:239],v[104:107]
wt vmcnt(13)
s_barrier
cv v42,v44,v45
cv v43,v46,v47
st v[40:43],v255,s[68:71],s95 offen offset:1024 sc1
mm v[108:111],a[108:111],v[236:239],v[108:111]
rt v[192:193],v240 offset:16896
rt v[194:195],v240 offset:17920
cv v48,v48,v49
cv v49,v50,v51
cv v50,v52,v53
cv v51,v54,v55
mm v[76:79],a[108:111],v[232:235],v[76:79]
rt v[196:197],v240 offset:25344
rt v[198:199],v240 offset:26368
st v[48:51],v255,s[68:71],s95 offen offset:2048 sc1
cv v56,v56,v57
cv v57,v58,v59
mm v[80:83],a[112:115],v[232:235],v[80:83]
rt v[200:201],v241 offset:16896
rt v[202:203],v241 offset:17920
cv v58,v60,v61
cv v59,v62,v63
st v[56:59],v255,s[68:71],s95 offen offset:3072 sc1
mm v[112:115],a[112:115],v[236:239],v[112:115]
rt v[204:205],v241 offset:25344
rt v[206:207],v241 offset:26368
mm v[116:119],a[116:119],v[236:239],v[116:119]
rt v[208:209],v242 offset:16896
rt v[210:211],v242 offset:17920
mm v[84:87],a[116:119],v[232:235],v[84:87]
rt v[212:213],v242 offset:25344
rt v[214:215],v242 offset:26368
mm v[88:91],a[120:123],v[232:235],v[88:91]
rt v[216:217],v243 offset:16896
rt v[218:219],v243 offset:17920
mm v[120:123],a[120:123],v[236:239],v[120:123]
rt v[220:221],v243 offset:25344
rt v[222:223],v243 offset:26368
mm v[124:127],a[124:127],v[236:239],v[124:127]
mm v[92:95],a[124:127],v[232:235],v[92:95]
wt lgkmcnt(12)
mm v[0:3],v[192:195],v[128:131],0
mm v[0:3],v[196:199],v[132:135],v[0:3]
mm v[32:35],v[196:199],v[148:151],0
wt lgkmcnt(8)
ar v224,a192
ar v228,a193
ar v225,a194
ar v229,a195
ar v226,a196
mm v[32:35],v[192:195],v[144:147],v[32:35]
rt v[196:197],v240 offset:17024
rt v[198:199],v240 offset:18048
ar v230,a197
ar v227,a198
ar v231,a199
v_add_f32 v224, v224, v228
mm v[36:39],v[200:203],v[144:147],0
v_add_f32 v225, v225, v229
v_add_f32 v226, v226, v230
v_add_f32 v227, v227, v231
s_mov_b32 s92, 16384
st v[224:227],v255,s[68:71],s92 offen sc1
mm v[36:39],v[204:207],v[148:151],v[36:39]
rt v[192:193],v240 offset:25472
rt v[194:195],v240 offset:26496
)ASM" R"ASM(cv v64,v64,v65
cv v65,v66,v67
cv v66,v68,v69
cv v67,v70,v71
mm v[4:7],v[204:207],v[132:135],0
wt lgkmcnt(4)
st v[64:67],v255,s[68:71],s96 offen offset:0 sc1
cv v72,v72,v73
cv v73,v74,v75
cv v74,v76,v77
mm v[4:7],v[200:203],v[128:131],v[4:7]
rt v[204:205],v241 offset:17024
rt v[206:207],v241 offset:18048
cv v75,v78,v79
st v[72:75],v255,s[68:71],s96 offen offset:1024 sc1
cv v80,v80,v81
mm v[8:11],v[208:211],v[128:131],0
cv v81,v82,v83
cv v82,v84,v85
cv v83,v86,v87
st v[80:83],v255,s[68:71],s96 offen offset:2048 sc1
cv v88,v88,v89
mm v[8:11],v[212:215],v[132:135],v[8:11]
rt v[200:201],v241 offset:25472
rt v[202:203],v241 offset:26496
cv v89,v90,v91
cv v90,v92,v93
cv v91,v94,v95
st v[88:91],v255,s[68:71],s96 offen offset:3072 sc1
mm v[40:43],v[212:215],v[148:151],0
cv v96,v96,v97
cv v97,v98,v99
cv v98,v100,v101
cv v99,v102,v103
st v[96:99],v255,s[68:71],s97 offen offset:0 sc1
mm v[40:43],v[208:211],v[144:147],v[40:43]
rt v[212:213],v242 offset:17024
rt v[214:215],v242 offset:18048
cv v104,v104,v105
cv v105,v106,v107
cv v106,v108,v109
cv v107,v110,v111
.La5_LW6_5:
s_cbranch_execz .La5_LW6_R0
mm v[44:47],v[216:219],v[144:147],0
st v[104:107],v255,s[68:71],s97 offen offset:1024 sc1
cv v112,v112,v113
cv v113,v114,v115
cv v114,v116,v117
cv v115,v118,v119
mm v[44:47],v[220:223],v[148:151],v[44:47]
rt v[208:209],v242 offset:25472
rt v[210:211],v242 offset:26496
st v[112:115],v255,s[68:71],s97 offen offset:2048 sc1
cv v120,v120,v121
cv v121,v122,v123
mm v[12:15],v[220:223],v[132:135],0
wt lgkmcnt(4)
cv v122,v124,v125
cv v123,v126,v127
st v[120:123],v255,s[68:71],s97 offen offset:3072 sc1
v_mov_b32_e64 v249, 0
mm v[12:15],v[216:219],v[128:131],v[12:15]
rt v[220:221],v243 offset:17024
rt v[222:223],v243 offset:18048
s_add_u32 s6, s88, 393216
mm v[16:19],v[196:199],v[128:131],0
ar v224,a228
v_add_u32 v225, 0x8000, v224
v_add_u32 v226, 0x2000, v224
v_add_u32 v227, 0xa000, v224
mm v[16:19],v[192:195],v[132:135],v[16:19]
rt v[216:217],v243 offset:25472
rt v[218:219],v243 offset:26496
ar v248,a225
ar v228,a229
mm v[48:51],v[192:195],v[148:151],0
ar v229,a230
mm v[48:51],v[196:199],v[144:147],v[48:51]
rt v[192:193],v240 offset:33792
rt v[194:195],v240 offset:34816
mm v[52:55],v[204:207],v[144:147],0
mm v[52:55],v[200:203],v[148:151],v[52:55]
rt v[196:197],v240 offset:42240
rt v[198:199],v240 offset:43264
mm v[20:23],v[200:203],v[132:135],0
wt lgkmcnt(4)
mm v[20:23],v[204:207],v[128:131],v[20:23]
rt v[200:201],v241 offset:33792
rt v[202:203],v241 offset:34816
mm v[24:27],v[212:215],v[128:131],0
s_bitcmp1_b32 2863311530, s2
s_cbranch_scc1 .La5_LO_VC2
s_getpc_b64 s[24:25]
.La5_LW3_P0:
s_add_u32 s24, s24, .La5_LW3_R0-.La5_LW3_P0
s_addc_u32 s25, s25, 0
s_mov_b64 exec, 0
s_cmp_eq_u32 s3, 0
s_cbranch_scc1 .La5_LW3_0
)ASM" R"ASM(s_cmp_eq_u32 s3, 1
s_cbranch_scc1 .La5_LW3_1
s_cmp_eq_u32 s3, 2
s_cbranch_scc1 .La5_LW3_2
s_branch .La5_LW3_3
.La5_LW3_R0:
s_mov_b64 exec, -1
.La5_LO_VE2:
wt vmcnt(0)
buffer_store_dwordx2 v[252:253], v249, s[64:67], s6 offen offset:0 sc1
mm v[24:27],v[208:211],v[132:135],v[24:27]
rt v[204:205],v241 offset:42240
rt v[206:207],v241 offset:43264
mm v[56:59],v[208:211],v[148:151],0
s_mov_b32 s93, 3670016
ld a[192:195],v228,s[40:43],s93 offen
mm v[56:59],v[212:215],v[144:147],v[56:59]
rt v[208:209],v242 offset:33792
rt v[210:211],v242 offset:34816
mm v[60:63],v[220:223],v[144:147],0
s_mov_b32 s93, 3677184
ld a[196:199],v228,s[40:43],s93 offen
mm v[60:63],v[216:219],v[148:151],v[60:63]
rt v[212:213],v242 offset:42240
rt v[214:215],v242 offset:43264
mm v[28:31],v[216:219],v[132:135],0
wt lgkmcnt(4)
s_mov_b32 s93, 3684352
ld a[200:203],v228,s[40:43],s93 offen
mm v[28:31],v[220:223],v[128:131],v[28:31]
rt v[216:217],v243 offset:33792
rt v[218:219],v243 offset:34816
mm v[0:3],v[192:195],v[136:139],v[0:3]
s_mov_b32 s93, 3691520
ld a[204:207],v228,s[40:43],s93 offen
mm v[0:3],v[196:199],v[140:143],v[0:3]
rt v[220:221],v243 offset:42240
rt v[222:223],v243 offset:43264
mm v[32:35],v[196:199],v[156:159],v[32:35]
s_mov_b32 s93, 3698688
ld a[208:211],v228,s[40:43],s93 offen
mm v[32:35],v[192:195],v[152:155],v[32:35]
rt v[196:197],v240 offset:33920
rt v[198:199],v240 offset:34944
mm v[36:39],v[200:203],v[152:155],v[36:39]
s_mov_b32 s93, 3705856
ld a[212:215],v228,s[40:43],s93 offen
mm v[36:39],v[204:207],v[156:159],v[36:39]
rt v[192:193],v240 offset:42368
rt v[194:195],v240 offset:43392
mm v[4:7],v[204:207],v[140:143],v[4:7]
wt lgkmcnt(4)
s_mov_b32 s93, 3713024
ld a[216:219],v228,s[40:43],s93 offen
mm v[4:7],v[200:203],v[136:139],v[4:7]
rt v[204:205],v241 offset:33920
rt v[206:207],v241 offset:34944
mm v[8:11],v[208:211],v[136:139],v[8:11]
s_mov_b32 s93, 3720192
ld a[220:223],v228,s[40:43],s93 offen
mm v[8:11],v[212:215],v[140:143],v[8:11]
rt v[200:201],v241 offset:42368
rt v[202:203],v241 offset:43392
mm v[40:43],v[212:215],v[156:159],v[40:43]
s_mov_b32 s93, 3727360
ld a[224:227],v228,s[40:43],s93 offen
mm v[40:43],v[208:211],v[152:155],v[40:43]
rt v[212:213],v242 offset:33920
rt v[214:215],v242 offset:34944
mm v[44:47],v[216:219],v[152:155],v[44:47]
s_mov_b32 s93, 3734528
ld a[228:231],v228,s[40:43],s93 offen
mm v[44:47],v[220:223],v[156:159],v[44:47]
rt v[208:209],v242 offset:42368
rt v[210:211],v242 offset:43392
mm v[12:15],v[220:223],v[140:143],v[12:15]
wt lgkmcnt(4)
s_mov_b32 s93, 3741696
ld a[232:235],v228,s[40:43],s93 offen
mm v[12:15],v[216:219],v[136:139],v[12:15]
rt v[220:221],v243 offset:33920
rt v[222:223],v243 offset:34944
mm v[16:19],v[196:199],v[136:139],v[16:19]
s_mov_b32 s93, 3748864
ld a[236:239],v228,s[40:43],s93 offen
mm v[16:19],v[192:195],v[140:143],v[16:19]
rt v[216:217],v243 offset:42368
rt v[218:219],v243 offset:43392
mm v[48:51],v[192:195],v[156:159],v[48:51]
s_mov_b32 s93, 3756032
ld a[240:243],v228,s[40:43],s93 offen
mm v[48:51],v[196:199],v[152:155],v[48:51]
rt v[192:193],v240 offset:16896
rt v[194:195],v240 offset:17920
mm v[52:55],v[204:207],v[152:155],v[52:55]
s_mov_b32 s93, 3763200
ld a[244:247],v228,s[40:43],s93 offen
mm v[52:55],v[200:203],v[156:159],v[52:55]
rt v[196:197],v240 offset:25344
rt v[198:199],v240 offset:26368
)ASM" R"ASM(mm v[20:23],v[200:203],v[140:143],v[20:23]
wt lgkmcnt(4)
s_mov_b32 s93, 3770368
ld a[248:251],v228,s[40:43],s93 offen
mm v[20:23],v[204:207],v[136:139],v[20:23]
rt v[200:201],v241 offset:16896
rt v[202:203],v241 offset:17920
mm v[24:27],v[212:215],v[136:139],v[24:27]
s_mov_b32 s93, 3777536
ld a[252:255],v228,s[40:43],s93 offen
mm v[24:27],v[208:211],v[140:143],v[24:27]
rt v[204:205],v241 offset:25344
rt v[206:207],v241 offset:26368
mm v[56:59],v[208:211],v[156:159],v[56:59]
mm v[56:59],v[212:215],v[152:155],v[56:59]
rt v[208:209],v242 offset:16896
rt v[210:211],v242 offset:17920
mm v[60:63],v[220:223],v[152:155],v[60:63]
mm v[60:63],v[216:219],v[156:159],v[60:63]
rt v[212:213],v242 offset:25344
rt v[214:215],v242 offset:26368
mm v[28:31],v[216:219],v[140:143],v[28:31]
wt lgkmcnt(4)
mm v[28:31],v[220:223],v[136:139],v[28:31]
rt v[216:217],v243 offset:16896
rt v[218:219],v243 offset:17920
s_branch .La5_LO_START
; ---- owner: last windows
.La5_LA_DC:
.La5_LW5_0:
s_cbranch_execz .La5_LW5_4
wt lgkmcnt(4)
mm v[192:195],a[64:67],v[128:131],0
ex v221,v221
mm v[192:195],a[68:71],v[132:135],v[192:195]
cv v238,v220,v221
s_add_u32 s89, s89, 16384
mm v[192:195],a[72:75],v[136:139],v[192:195]
ex v222,v222
mm v[192:195],a[76:79],v[140:143],v[192:195]
ld v255,s[60:63],s17 offen offset:0 sc1 lds
mm v[200:203],a[76:79],v[156:159],0
ex v223,v223
mm v[200:203],a[72:75],v[152:155],v[200:203]
cv v239,v222,v223
s_and_b32 s89, s89, 0x1ffff
mm v[200:203],a[68:71],v[148:151],v[200:203]
ex v192,v192
mm v[200:203],a[64:67],v[144:147],v[200:203]
wt lgkmcnt(0)
mm v[196:199],a[80:83],v[128:131],0
ex v193,v193
mm v[196:199],a[84:87],v[132:135],v[196:199]
cv v224,v192,v193
ar v243,a229
mm v[196:199],a[88:91],v[136:139],v[196:199]
ex v194,v194
mm v[196:199],a[92:95],v[140:143],v[196:199]
ld v255,s[60:63],s17 offen offset:1024 sc1 lds
mm v[204:207],a[92:95],v[156:159],0
ex v195,v195
mm v[204:207],a[88:91],v[152:155],v[204:207]
cv v225,v194,v195
mm v[204:207],a[84:87],v[148:151],v[204:207]
ex v200,v200
mm v[204:207],a[80:83],v[144:147],v[204:207]
ld v255,s[60:63],s17 offen offset:2048 sc1 lds
mm v[208:211],a[64:67],v[160:163],0
ex v201,v201
mm v[208:211],a[68:71],v[164:167],v[208:211]
cv v228,v200,v201
mm v[208:211],a[72:75],v[168:171],v[208:211]
ex v202,v202
mm v[208:211],a[76:79],v[172:175],v[208:211]
ld v255,s[60:63],s17 offen offset:3072 sc1 lds
mm v[216:219],a[76:79],v[188:191],0
ex v203,v203
mm v[216:219],a[72:75],v[184:187],v[216:219]
cv v229,v202,v203
s_mov_b32 s93, 0
mm v[216:219],a[68:71],v[180:183],v[216:219]
ex v196,v196
mm v[216:219],a[64:67],v[176:179],v[216:219]
ld a[128:131],v243,s[40:43],s93 offen
mm v[212:215],a[80:83],v[160:163],0
ex v197,v197
s_add_u32 s17, s17, s80
mm v[212:215],a[84:87],v[164:167],v[212:215]
cv v226,v196,v197
s_mov_b32 s93, 7168
s_add_u32 m0, m0, 16384
mm v[212:215],a[88:91],v[168:171],v[212:215]
ex v198,v198
s_and_b32 m0, m0, s78
mm v[212:215],a[92:95],v[172:175],v[212:215]
ld a[132:135],v243,s[40:43],s93 offen
mm v[220:223],a[92:95],v[188:191],0
ex v199,v199
mm v[220:223],a[88:91],v[184:187],v[220:223]
cv v227,v198,v199
)ASM" R"ASM(mm v[220:223],a[84:87],v[180:183],v[220:223]
ex v204,v204
mm v[220:223],a[80:83],v[176:179],v[220:223]
wt vmcnt(12)
s_barrier
mm v[64:67],a[96:99],v[232:235],v[64:67]
ex v205,v205
mm v[96:99],a[96:99],v[236:239],v[96:99]
rd a[96:99],v248 offset:8192
cv v230,v204,v205
s_mov_b32 s93, 28672
mm v[100:103],a[100:103],v[236:239],v[100:103]
ex v206,v206
mm v[68:71],a[100:103],v[232:235],v[68:71]
rd a[100:103],v248 offset:9216
ld a[144:147],v243,s[40:43],s93 offen
mm v[72:75],a[104:107],v[232:235],v[72:75]
ex v207,v207
mm v[104:107],a[104:107],v[236:239],v[104:107]
rd a[104:107],v248 offset:10240
cv v231,v206,v207
s_mov_b32 s93, 14336
mm v[108:111],a[108:111],v[236:239],v[108:111]
ex v208,v208
mm v[76:79],a[108:111],v[232:235],v[76:79]
rd a[108:111],v248 offset:11264
ld a[136:139],v243,s[40:43],s93 offen
mm v[80:83],a[112:115],v[232:235],v[80:83]
ex v209,v209
mm v[112:115],a[112:115],v[236:239],v[112:115]
rd a[112:115],v248 offset:12288
s_mov_b32 s93, 21504
mm v[116:119],a[116:119],v[236:239],v[116:119]
ex v210,v210
mm v[84:87],a[116:119],v[232:235],v[84:87]
rd a[116:119],v248 offset:13312
ld a[140:143],v243,s[40:43],s93 offen
mm v[88:91],a[120:123],v[232:235],v[88:91]
ex v211,v211
mm v[120:123],a[120:123],v[236:239],v[120:123]
rd a[120:123],v248 offset:14336
s_mov_b32 s93, 35840
mm v[124:127],a[124:127],v[236:239],v[124:127]
ex v216,v216
mm v[92:95],a[124:127],v[232:235],v[92:95]
rd a[124:127],v248 offset:15360
ld a[148:151],v243,s[40:43],s93 offen
v_or_b32 v248, s89, v255
v_smfmac_f32_16x16x64_bf16 a[196:199], a[208:211], v[232:239], v241
wt lgkmcnt(4)
v_smfmac_f32_16x16x64_bf16 a[192:195], a[208:211], v[224:231], v241
rd a[64:67],v248 offset:0
mm v[0:3],a[96:99],v[224:227],v[0:3]
ex v217,v217
mm v[32:35],a[96:99],v[228:231],v[32:35]
rd a[68:71],v248 offset:1024
cv v232,v208,v209
mm v[36:39],a[100:103],v[228:231],v[36:39]
ex v218,v218
mm v[4:7],a[100:103],v[224:227],v[4:7]
rd a[72:75],v248 offset:2048
cv v233,v210,v211
mm v[8:11],a[104:107],v[224:227],v[8:11]
ex v219,v219
mm v[40:43],a[104:107],v[228:231],v[40:43]
rd a[76:79],v248 offset:3072
cv v236,v216,v217
mm v[44:47],a[108:111],v[228:231],v[44:47]
ex v212,v212
mm v[12:15],a[108:111],v[224:227],v[12:15]
rd a[80:83],v248 offset:4096
wt lgkmcnt(5)
mm v[16:19],a[112:115],v[224:227],v[16:19]
ex v213,v213
mm v[48:51],a[112:115],v[228:231],v[48:51]
rd a[84:87],v248 offset:5120
cv v237,v218,v219
mm v[52:55],a[116:119],v[228:231],v[52:55]
ex v214,v214
mm v[20:23],a[116:119],v[224:227],v[20:23]
rd a[88:91],v248 offset:6144
cv v234,v212,v213
mm v[24:27],a[120:123],v[224:227],v[24:27]
ex v215,v215
mm v[56:59],a[120:123],v[228:231],v[56:59]
rd a[92:95],v248 offset:7168
cv v235,v214,v215
mm v[60:63],a[124:127],v[228:231],v[60:63]
ex v220,v220
mm v[28:31],a[124:127],v[224:227],v[28:31]
wt lgkmcnt(4)
mm v[192:195],a[64:67],v[128:131],0
ex v221,v221
mm v[192:195],a[68:71],v[132:135],v[192:195]
cv v238,v220,v221
s_add_u32 s89, s89, 16384
mm v[192:195],a[72:75],v[136:139],v[192:195]
ex v222,v222
mm v[192:195],a[76:79],v[140:143],v[192:195]
ld v255,s[60:63],s17 offen offset:0 sc1 lds
)ASM" R"ASM(mm v[200:203],a[76:79],v[156:159],0
ex v223,v223
mm v[200:203],a[72:75],v[152:155],v[200:203]
cv v239,v222,v223
s_and_b32 s89, s89, 0x1ffff
mm v[200:203],a[68:71],v[148:151],v[200:203]
ex v192,v192
mm v[200:203],a[64:67],v[144:147],v[200:203]
wt lgkmcnt(0)
mm v[196:199],a[80:83],v[128:131],0
ex v193,v193
mm v[196:199],a[84:87],v[132:135],v[196:199]
cv v224,v192,v193
mm v[196:199],a[88:91],v[136:139],v[196:199]
ex v194,v194
mm v[196:199],a[92:95],v[140:143],v[196:199]
ld v255,s[60:63],s17 offen offset:1024 sc1 lds
mm v[204:207],a[92:95],v[156:159],0
ex v195,v195
mm v[204:207],a[88:91],v[152:155],v[204:207]
cv v225,v194,v195
mm v[204:207],a[84:87],v[148:151],v[204:207]
ex v200,v200
mm v[204:207],a[80:83],v[144:147],v[204:207]
ld v255,s[60:63],s17 offen offset:2048 sc1 lds
mm v[208:211],a[64:67],v[160:163],0
ex v201,v201
mm v[208:211],a[68:71],v[164:167],v[208:211]
cv v228,v200,v201
mm v[208:211],a[72:75],v[168:171],v[208:211]
ex v202,v202
mm v[208:211],a[76:79],v[172:175],v[208:211]
ld v255,s[60:63],s17 offen offset:3072 sc1 lds
mm v[216:219],a[76:79],v[188:191],0
ex v203,v203
mm v[216:219],a[72:75],v[184:187],v[216:219]
cv v229,v202,v203
s_mov_b32 s93, 43008
mm v[216:219],a[68:71],v[180:183],v[216:219]
ex v196,v196
mm v[216:219],a[64:67],v[176:179],v[216:219]
ld a[152:155],v243,s[40:43],s93 offen
mm v[212:215],a[80:83],v[160:163],0
ex v197,v197
s_add_u32 s17, s17, s80
mm v[212:215],a[84:87],v[164:167],v[212:215]
cv v226,v196,v197
s_mov_b32 s93, 50176
s_add_u32 m0, m0, 16384
mm v[212:215],a[88:91],v[168:171],v[212:215]
ex v198,v198
s_and_b32 m0, m0, s78
mm v[212:215],a[92:95],v[172:175],v[212:215]
ld a[156:159],v243,s[40:43],s93 offen
mm v[220:223],a[92:95],v[188:191],0
ex v199,v199
mm v[220:223],a[88:91],v[184:187],v[220:223]
cv v227,v198,v199
mm v[220:223],a[84:87],v[180:183],v[220:223]
ex v204,v204
mm v[220:223],a[80:83],v[176:179],v[220:223]
wt vmcnt(17)
s_barrier
mm v[64:67],a[96:99],v[232:235],v[64:67]
ex v205,v205
mm v[96:99],a[96:99],v[236:239],v[96:99]
rd a[96:99],v248 offset:8192
cv v230,v204,v205
s_mov_b32 s93, 71680
mm v[100:103],a[100:103],v[236:239],v[100:103]
ex v206,v206
mm v[68:71],a[100:103],v[232:235],v[68:71]
rd a[100:103],v248 offset:9216
ld a[168:171],v243,s[40:43],s93 offen
mm v[72:75],a[104:107],v[232:235],v[72:75]
ex v207,v207
mm v[104:107],a[104:107],v[236:239],v[104:107]
rd a[104:107],v248 offset:10240
cv v231,v206,v207
s_mov_b32 s93, 57344
mm v[108:111],a[108:111],v[236:239],v[108:111]
ex v208,v208
mm v[76:79],a[108:111],v[232:235],v[76:79]
rd a[108:111],v248 offset:11264
ld a[160:163],v243,s[40:43],s93 offen
mm v[80:83],a[112:115],v[232:235],v[80:83]
ex v209,v209
mm v[112:115],a[112:115],v[236:239],v[112:115]
rd a[112:115],v248 offset:12288
s_mov_b32 s93, 64512
mm v[116:119],a[116:119],v[236:239],v[116:119]
ex v210,v210
mm v[84:87],a[116:119],v[232:235],v[84:87]
rd a[116:119],v248 offset:13312
ld a[164:167],v243,s[40:43],s93 offen
mm v[88:91],a[120:123],v[232:235],v[88:91]
ex v211,v211
mm v[120:123],a[120:123],v[236:239],v[120:123]
rd a[120:123],v248 offset:14336
mm v[124:127],a[124:127],v[236:239],v[124:127]
)ASM" R"ASM(ex v216,v216
mm v[92:95],a[124:127],v[232:235],v[92:95]
rd a[124:127],v248 offset:15360
v_or_b32 v248, s89, v255
v_smfmac_f32_16x16x64_bf16 a[196:199], a[208:211], v[232:239], v241
wt lgkmcnt(4)
v_smfmac_f32_16x16x64_bf16 a[192:195], a[208:211], v[224:231], v241
rd a[64:67],v248 offset:0
mm v[0:3],a[96:99],v[224:227],v[0:3]
ex v217,v217
mm v[32:35],a[96:99],v[228:231],v[32:35]
rd a[68:71],v248 offset:1024
cv v232,v208,v209
mm v[36:39],a[100:103],v[228:231],v[36:39]
ex v218,v218
mm v[4:7],a[100:103],v[224:227],v[4:7]
rd a[72:75],v248 offset:2048
cv v233,v210,v211
mm v[8:11],a[104:107],v[224:227],v[8:11]
ex v219,v219
mm v[40:43],a[104:107],v[228:231],v[40:43]
rd a[76:79],v248 offset:3072
cv v236,v216,v217
mm v[44:47],a[108:111],v[228:231],v[44:47]
ex v212,v212
mm v[12:15],a[108:111],v[224:227],v[12:15]
rd a[80:83],v248 offset:4096
wt lgkmcnt(5)
mm v[16:19],a[112:115],v[224:227],v[16:19]
ex v213,v213
mm v[48:51],a[112:115],v[228:231],v[48:51]
rd a[84:87],v248 offset:5120
cv v237,v218,v219
mm v[52:55],a[116:119],v[228:231],v[52:55]
ex v214,v214
mm v[20:23],a[116:119],v[224:227],v[20:23]
rd a[88:91],v248 offset:6144
cv v234,v212,v213
mm v[24:27],a[120:123],v[224:227],v[24:27]
ex v215,v215
mm v[56:59],a[120:123],v[228:231],v[56:59]
rd a[92:95],v248 offset:7168
cv v235,v214,v215
mm v[60:63],a[124:127],v[228:231],v[60:63]
ex v220,v220
mm v[28:31],a[124:127],v[224:227],v[28:31]
wt lgkmcnt(4)
mm v[192:195],a[64:67],v[128:131],0
ex v221,v221
mm v[192:195],a[68:71],v[132:135],v[192:195]
cv v238,v220,v221
s_add_u32 s89, s89, 16384
mm v[192:195],a[72:75],v[136:139],v[192:195]
ex v222,v222
mm v[192:195],a[76:79],v[140:143],v[192:195]
ld v255,s[60:63],s17 offen offset:0 sc1 lds
mm v[200:203],a[76:79],v[156:159],0
ex v223,v223
mm v[200:203],a[72:75],v[152:155],v[200:203]
cv v239,v222,v223
s_and_b32 s89, s89, 0x1ffff
mm v[200:203],a[68:71],v[148:151],v[200:203]
ex v192,v192
mm v[200:203],a[64:67],v[144:147],v[200:203]
wt lgkmcnt(0)
mm v[196:199],a[80:83],v[128:131],0
ex v193,v193
mm v[196:199],a[84:87],v[132:135],v[196:199]
cv v224,v192,v193
mm v[196:199],a[88:91],v[136:139],v[196:199]
ex v194,v194
mm v[196:199],a[92:95],v[140:143],v[196:199]
ld v255,s[60:63],s17 offen offset:1024 sc1 lds
mm v[204:207],a[92:95],v[156:159],0
ex v195,v195
mm v[204:207],a[88:91],v[152:155],v[204:207]
cv v225,v194,v195
mm v[204:207],a[84:87],v[148:151],v[204:207]
ex v200,v200
mm v[204:207],a[80:83],v[144:147],v[204:207]
ld v255,s[60:63],s17 offen offset:2048 sc1 lds
mm v[208:211],a[64:67],v[160:163],0
ex v201,v201
mm v[208:211],a[68:71],v[164:167],v[208:211]
cv v228,v200,v201
mm v[208:211],a[72:75],v[168:171],v[208:211]
ex v202,v202
mm v[208:211],a[76:79],v[172:175],v[208:211]
ld v255,s[60:63],s17 offen offset:3072 sc1 lds
mm v[216:219],a[76:79],v[188:191],0
ex v203,v203
mm v[216:219],a[72:75],v[184:187],v[216:219]
cv v229,v202,v203
s_mov_b32 s93, 78848
mm v[216:219],a[68:71],v[180:183],v[216:219]
ex v196,v196
mm v[216:219],a[64:67],v[176:179],v[216:219]
ld a[172:175],v243,s[40:43],s93 offen
mm v[212:215],a[80:83],v[160:163],0
ex v197,v197
)ASM" R"ASM(s_add_u32 s17, s17, s80
mm v[212:215],a[84:87],v[164:167],v[212:215]
cv v226,v196,v197
s_mov_b32 s93, 86016
s_add_u32 m0, m0, 16384
mm v[212:215],a[88:91],v[168:171],v[212:215]
ex v198,v198
s_and_b32 m0, m0, s78
mm v[212:215],a[92:95],v[172:175],v[212:215]
ld a[176:179],v243,s[40:43],s93 offen
mm v[220:223],a[92:95],v[188:191],0
ex v199,v199
mm v[220:223],a[88:91],v[184:187],v[220:223]
cv v227,v198,v199
mm v[220:223],a[84:87],v[180:183],v[220:223]
ex v204,v204
mm v[220:223],a[80:83],v[176:179],v[220:223]
wt vmcnt(21)
s_barrier
mm v[64:67],a[96:99],v[232:235],v[64:67]
ex v205,v205
mm v[96:99],a[96:99],v[236:239],v[96:99]
rd a[96:99],v248 offset:8192
cv v230,v204,v205
s_mov_b32 s93, 107520
.La5_LW5_1:
s_cbranch_execz .La5_LW5_R0
mm v[100:103],a[100:103],v[236:239],v[100:103]
ex v206,v206
mm v[68:71],a[100:103],v[232:235],v[68:71]
rd a[100:103],v248 offset:9216
ld a[188:191],v243,s[40:43],s93 offen
mm v[72:75],a[104:107],v[232:235],v[72:75]
ex v207,v207
mm v[104:107],a[104:107],v[236:239],v[104:107]
rd a[104:107],v248 offset:10240
cv v231,v206,v207
s_mov_b32 s93, 93184
mm v[108:111],a[108:111],v[236:239],v[108:111]
ex v208,v208
mm v[76:79],a[108:111],v[232:235],v[76:79]
rd a[108:111],v248 offset:11264
ld a[180:183],v243,s[40:43],s93 offen
mm v[80:83],a[112:115],v[232:235],v[80:83]
ex v209,v209
mm v[112:115],a[112:115],v[236:239],v[112:115]
rd a[112:115],v248 offset:12288
s_mov_b32 s93, 100352
mm v[116:119],a[116:119],v[236:239],v[116:119]
ex v210,v210
mm v[84:87],a[116:119],v[232:235],v[84:87]
rd a[116:119],v248 offset:13312
ld a[184:187],v243,s[40:43],s93 offen
mm v[88:91],a[120:123],v[232:235],v[88:91]
ex v211,v211
mm v[120:123],a[120:123],v[236:239],v[120:123]
rd a[120:123],v248 offset:14336
mm v[124:127],a[124:127],v[236:239],v[124:127]
ex v216,v216
mm v[92:95],a[124:127],v[232:235],v[92:95]
rd a[124:127],v248 offset:15360
v_or_b32 v248, s89, v255
v_smfmac_f32_16x16x64_bf16 a[196:199], a[208:211], v[232:239], v241
wt lgkmcnt(4)
v_smfmac_f32_16x16x64_bf16 a[192:195], a[208:211], v[224:231], v241
rd a[64:67],v248 offset:0
mm v[0:3],a[96:99],v[224:227],v[0:3]
ex v217,v217
mm v[32:35],a[96:99],v[228:231],v[32:35]
rd a[68:71],v248 offset:1024
cv v232,v208,v209
mm v[36:39],a[100:103],v[228:231],v[36:39]
ex v218,v218
mm v[4:7],a[100:103],v[224:227],v[4:7]
rd a[72:75],v248 offset:2048
cv v233,v210,v211
mm v[8:11],a[104:107],v[224:227],v[8:11]
ex v219,v219
mm v[40:43],a[104:107],v[228:231],v[40:43]
rd a[76:79],v248 offset:3072
cv v236,v216,v217
mm v[44:47],a[108:111],v[228:231],v[44:47]
ex v212,v212
mm v[12:15],a[108:111],v[224:227],v[12:15]
rd a[80:83],v248 offset:4096
wt lgkmcnt(5)
mm v[16:19],a[112:115],v[224:227],v[16:19]
ex v213,v213
mm v[48:51],a[112:115],v[228:231],v[48:51]
rd a[84:87],v248 offset:5120
cv v237,v218,v219
mm v[52:55],a[116:119],v[228:231],v[52:55]
ex v214,v214
mm v[20:23],a[116:119],v[224:227],v[20:23]
rd a[88:91],v248 offset:6144
cv v234,v212,v213
mm v[24:27],a[120:123],v[224:227],v[24:27]
ex v215,v215
mm v[56:59],a[120:123],v[228:231],v[56:59]
rd a[92:95],v248 offset:7168
)ASM" R"ASM(cv v235,v214,v215
mm v[60:63],a[124:127],v[228:231],v[60:63]
ex v220,v220
mm v[28:31],a[124:127],v[224:227],v[28:31]
.La5_LA_DC3:
.La5_LW5_2:
s_cbranch_execz .La5_LW5_R0
wt lgkmcnt(4)
mm v[192:195],a[64:67],v[128:131],0
ex v221,v221
mm v[192:195],a[68:71],v[132:135],v[192:195]
cv v238,v220,v221
ar v240,a228
mm v[192:195],a[72:75],v[136:139],v[192:195]
ex v222,v222
mm v[192:195],a[76:79],v[140:143],v[192:195]
s_add_u32 m0, s83, 16896
s_lshl_b32 s17, s72, 18
mm v[200:203],a[76:79],v[156:159],0
ex v223,v223
mm v[200:203],a[72:75],v[152:155],v[200:203]
cv v239,v222,v223
s_add_u32 s89, s89, 16384
mm v[200:203],a[68:71],v[148:151],v[200:203]
ex v192,v192
mm v[200:203],a[64:67],v[144:147],v[200:203]
wt lgkmcnt(0)
s_and_b32 s89, s89, 0x1ffff
mm v[196:199],a[80:83],v[128:131],0
ex v193,v193
mm v[196:199],a[84:87],v[132:135],v[196:199]
cv v224,v192,v193
mm v[196:199],a[88:91],v[136:139],v[196:199]
ex v194,v194
mm v[196:199],a[92:95],v[140:143],v[196:199]
ld v240,s[56:59],s17 offen lds
mm v[204:207],a[92:95],v[156:159],0
ex v195,v195
s_add_u32 s17, s17, 32768
mm v[204:207],a[88:91],v[152:155],v[204:207]
cv v225,v194,v195
s_add_u32 m0, m0, 1024
mm v[204:207],a[84:87],v[148:151],v[204:207]
ex v200,v200
mm v[204:207],a[80:83],v[144:147],v[204:207]
ld v240,s[56:59],s17 offen lds
mm v[208:211],a[64:67],v[160:163],0
ex v201,v201
s_add_u32 s17, s17, 4294942720
mm v[208:211],a[68:71],v[164:167],v[208:211]
cv v228,v200,v201
s_add_u32 m0, m0, 1152
mm v[208:211],a[72:75],v[168:171],v[208:211]
ex v202,v202
mm v[208:211],a[76:79],v[172:175],v[208:211]
ld v240,s[56:59],s17 offen lds
mm v[216:219],a[76:79],v[188:191],0
ex v203,v203
s_add_u32 s17, s17, 32768
mm v[216:219],a[72:75],v[184:187],v[216:219]
cv v229,v202,v203
s_add_u32 m0, m0, 1024
mm v[216:219],a[68:71],v[180:183],v[216:219]
ex v196,v196
mm v[216:219],a[64:67],v[176:179],v[216:219]
ld v240,s[56:59],s17 offen lds
mm v[212:215],a[80:83],v[160:163],0
ex v197,v197
s_add_u32 s17, s17, 90112
mm v[212:215],a[84:87],v[164:167],v[212:215]
cv v226,v196,v197
s_add_u32 m0, s83, 33792
mm v[212:215],a[88:91],v[168:171],v[212:215]
ex v198,v198
mm v[212:215],a[92:95],v[172:175],v[212:215]
mm v[220:223],a[92:95],v[188:191],0
ex v199,v199
mm v[220:223],a[88:91],v[184:187],v[220:223]
cv v227,v198,v199
mm v[220:223],a[84:87],v[180:183],v[220:223]
ex v204,v204
mm v[220:223],a[80:83],v[176:179],v[220:223]
wt vmcnt(10)
s_barrier
mm v[64:67],a[96:99],v[232:235],v[64:67]
ex v205,v205
mm v[96:99],a[96:99],v[236:239],v[96:99]
rd a[96:99],v248 offset:8192
cv v230,v204,v205
mm v[100:103],a[100:103],v[236:239],v[100:103]
ex v206,v206
mm v[68:71],a[100:103],v[232:235],v[68:71]
rd a[100:103],v248 offset:9216
mm v[72:75],a[104:107],v[232:235],v[72:75]
ex v207,v207
mm v[104:107],a[104:107],v[236:239],v[104:107]
rd a[104:107],v248 offset:10240
cv v231,v206,v207
mm v[108:111],a[108:111],v[236:239],v[108:111]
ex v208,v208
)ASM" R"ASM(mm v[76:79],a[108:111],v[232:235],v[76:79]
rd a[108:111],v248 offset:11264
mm v[80:83],a[112:115],v[232:235],v[80:83]
ex v209,v209
mm v[112:115],a[112:115],v[236:239],v[112:115]
rd a[112:115],v248 offset:12288
mm v[116:119],a[116:119],v[236:239],v[116:119]
ex v210,v210
mm v[84:87],a[116:119],v[232:235],v[84:87]
rd a[116:119],v248 offset:13312
mm v[88:91],a[120:123],v[232:235],v[88:91]
ex v211,v211
mm v[120:123],a[120:123],v[236:239],v[120:123]
rd a[120:123],v248 offset:14336
mm v[124:127],a[124:127],v[236:239],v[124:127]
ex v216,v216
mm v[92:95],a[124:127],v[232:235],v[92:95]
rd a[124:127],v248 offset:15360
v_or_b32 v248, s89, v255
v_smfmac_f32_16x16x64_bf16 a[196:199], a[208:211], v[232:239], v241
wt lgkmcnt(4)
v_smfmac_f32_16x16x64_bf16 a[192:195], a[208:211], v[224:231], v241
rd a[64:67],v248 offset:0
mm v[0:3],a[96:99],v[224:227],v[0:3]
ex v217,v217
mm v[32:35],a[96:99],v[228:231],v[32:35]
rd a[68:71],v248 offset:1024
cv v232,v208,v209
mm v[36:39],a[100:103],v[228:231],v[36:39]
ex v218,v218
mm v[4:7],a[100:103],v[224:227],v[4:7]
rd a[72:75],v248 offset:2048
cv v233,v210,v211
mm v[8:11],a[104:107],v[224:227],v[8:11]
ex v219,v219
mm v[40:43],a[104:107],v[228:231],v[40:43]
rd a[76:79],v248 offset:3072
cv v236,v216,v217
mm v[44:47],a[108:111],v[228:231],v[44:47]
ex v212,v212
mm v[12:15],a[108:111],v[224:227],v[12:15]
rd a[80:83],v248 offset:4096
wt lgkmcnt(5)
mm v[16:19],a[112:115],v[224:227],v[16:19]
ex v213,v213
mm v[48:51],a[112:115],v[228:231],v[48:51]
rd a[84:87],v248 offset:5120
cv v237,v218,v219
mm v[52:55],a[116:119],v[228:231],v[52:55]
ex v214,v214
mm v[20:23],a[116:119],v[224:227],v[20:23]
rd a[88:91],v248 offset:6144
cv v234,v212,v213
mm v[24:27],a[120:123],v[224:227],v[24:27]
ex v215,v215
mm v[56:59],a[120:123],v[228:231],v[56:59]
rd a[92:95],v248 offset:7168
cv v235,v214,v215
mm v[60:63],a[124:127],v[228:231],v[60:63]
ex v220,v220
mm v[28:31],a[124:127],v[224:227],v[28:31]
buffer_load_dword v250, v251, s[64:67], s20 offen sc1
wt lgkmcnt(4)
mm v[192:195],a[64:67],v[128:131],0
ex v221,v221
mm v[192:195],a[68:71],v[132:135],v[192:195]
cv v238,v220,v221
s_add_u32 s89, s89, 16384
mm v[192:195],a[72:75],v[136:139],v[192:195]
ex v222,v222
mm v[192:195],a[76:79],v[140:143],v[192:195]
ld v240,s[56:59],s17 offen lds
mm v[200:203],a[76:79],v[156:159],0
ex v223,v223
s_add_u32 s17, s17, 32768
mm v[200:203],a[72:75],v[152:155],v[200:203]
cv v239,v222,v223
s_add_u32 m0, m0, 1024
mm v[200:203],a[68:71],v[148:151],v[200:203]
ex v192,v192
mm v[200:203],a[64:67],v[144:147],v[200:203]
wt lgkmcnt(0)
s_and_b32 s89, s89, 0x1ffff
mm v[196:199],a[80:83],v[128:131],0
ex v193,v193
mm v[196:199],a[84:87],v[132:135],v[196:199]
cv v224,v192,v193
mm v[196:199],a[88:91],v[136:139],v[196:199]
ex v194,v194
mm v[196:199],a[92:95],v[140:143],v[196:199]
ld v240,s[56:59],s17 offen lds
mm v[204:207],a[92:95],v[156:159],0
ex v195,v195
s_add_u32 s17, s17, 4294942720
mm v[204:207],a[88:91],v[152:155],v[204:207]
cv v225,v194,v195
s_add_u32 m0, m0, 1152
mm v[204:207],a[84:87],v[148:151],v[204:207]
ex v200,v200
mm v[204:207],a[80:83],v[144:147],v[204:207]
)ASM" R"ASM(ld v240,s[56:59],s17 offen lds
mm v[208:211],a[64:67],v[160:163],0
ex v201,v201
s_add_u32 s17, s17, 32768
mm v[208:211],a[68:71],v[164:167],v[208:211]
cv v228,v200,v201
s_add_u32 m0, m0, 1024
mm v[208:211],a[72:75],v[168:171],v[208:211]
ex v202,v202
mm v[208:211],a[76:79],v[172:175],v[208:211]
ld v240,s[56:59],s17 offen lds
mm v[216:219],a[76:79],v[188:191],0
ex v203,v203
mm v[216:219],a[72:75],v[184:187],v[216:219]
cv v229,v202,v203
mm v[216:219],a[68:71],v[180:183],v[216:219]
ex v196,v196
mm v[216:219],a[64:67],v[176:179],v[216:219]
mm v[212:215],a[80:83],v[160:163],0
ex v197,v197
s_bfe_u32 s17, s4, 262148
mm v[212:215],a[84:87],v[164:167],v[212:215]
cv v226,v196,v197
s_xor_b32 s17, s17, s72
s_lshl_b32 s17, s17, 18
mm v[212:215],a[88:91],v[168:171],v[212:215]
ex v198,v198
s_add_u32 m0, s83, 50688
mm v[212:215],a[92:95],v[172:175],v[212:215]
mm v[220:223],a[92:95],v[188:191],0
ex v199,v199
mm v[220:223],a[88:91],v[184:187],v[220:223]
cv v227,v198,v199
wt vmcnt(4)
v_cmp_eq_u32 s[92:93], v250, v249
s_mov_b32 s94, 16384
mm v[220:223],a[84:87],v[180:183],v[220:223]
ex v204,v204
mm v[220:223],a[80:83],v[176:179],v[220:223]
s_nop 0
s_barrier
mm v[64:67],a[96:99],v[232:235],v[64:67]
ex v205,v205
mm v[96:99],a[96:99],v[236:239],v[96:99]
rd a[96:99],v248 offset:8192
cv v230,v204,v205
s_and_b32 s92, s92, 3
s_andn2_b32 s92, s92, s23
s_cmp_eq_u32 s92, 3
s_cselect_b32 s70, 17408, 0
s_cselect_b32 s84, 3, s23
mm v[100:103],a[100:103],v[236:239],v[100:103]
ex v206,v206
mm v[68:71],a[100:103],v[232:235],v[68:71]
rd a[100:103],v248 offset:9216
ld a[220:223],v255,s[68:71],s94 offen sc1
mm v[72:75],a[104:107],v[232:235],v[72:75]
ex v207,v207
mm v[104:107],a[104:107],v[236:239],v[104:107]
rd a[104:107],v248 offset:10240
cv v231,v206,v207
mm v[108:111],a[108:111],v[236:239],v[108:111]
ex v208,v208
mm v[76:79],a[108:111],v[232:235],v[76:79]
rd a[108:111],v248 offset:11264
ld a[0:3],v255,s[68:71],0 offen offset:0 sc1
mm v[80:83],a[112:115],v[232:235],v[80:83]
ex v209,v209
mm v[112:115],a[112:115],v[236:239],v[112:115]
rd a[112:115],v248 offset:12288
mm v[116:119],a[116:119],v[236:239],v[116:119]
ex v210,v210
mm v[84:87],a[116:119],v[232:235],v[84:87]
rd a[116:119],v248 offset:13312
ld a[4:7],v255,s[68:71],0 offen offset:1024 sc1
mm v[88:91],a[120:123],v[232:235],v[88:91]
ex v211,v211
mm v[120:123],a[120:123],v[236:239],v[120:123]
rd a[120:123],v248 offset:14336
mm v[124:127],a[124:127],v[236:239],v[124:127]
ex v216,v216
mm v[92:95],a[124:127],v[232:235],v[92:95]
rd a[124:127],v248 offset:15360
v_or_b32 v248, s89, v255
buffer_load_dword v250, v251, s[64:67], s20 offen sc1
v_smfmac_f32_16x16x64_bf16 a[196:199], a[208:211], v[232:239], v241
wt lgkmcnt(4)
v_smfmac_f32_16x16x64_bf16 a[192:195], a[208:211], v[224:231], v241
rd a[64:67],v248 offset:0
mm v[0:3],a[96:99],v[224:227],v[0:3]
ex v217,v217
mm v[32:35],a[96:99],v[228:231],v[32:35]
rd a[68:71],v248 offset:1024
cv v232,v208,v209
ld a[8:11],v255,s[68:71],0 offen offset:2048 sc1
mm v[36:39],a[100:103],v[228:231],v[36:39]
ex v218,v218
mm v[4:7],a[100:103],v[224:227],v[4:7]
rd a[72:75],v248 offset:2048
cv v233,v210,v211
)ASM" R"ASM(mm v[8:11],a[104:107],v[224:227],v[8:11]
ex v219,v219
mm v[40:43],a[104:107],v[228:231],v[40:43]
rd a[76:79],v248 offset:3072
cv v236,v216,v217
ld a[12:15],v255,s[68:71],0 offen offset:3072 sc1
mm v[44:47],a[108:111],v[228:231],v[44:47]
ex v212,v212
mm v[12:15],a[108:111],v[224:227],v[12:15]
rd a[80:83],v248 offset:4096
wt lgkmcnt(5)
mm v[16:19],a[112:115],v[224:227],v[16:19]
ex v213,v213
mm v[48:51],a[112:115],v[228:231],v[48:51]
rd a[84:87],v248 offset:5120
cv v237,v218,v219
ld a[16:19],v255,s[68:71],s95 offen offset:0 sc1
mm v[52:55],a[116:119],v[228:231],v[52:55]
ex v214,v214
mm v[20:23],a[116:119],v[224:227],v[20:23]
rd a[88:91],v248 offset:6144
cv v234,v212,v213
mm v[24:27],a[120:123],v[224:227],v[24:27]
ex v215,v215
mm v[56:59],a[120:123],v[228:231],v[56:59]
rd a[92:95],v248 offset:7168
cv v235,v214,v215
ld a[20:23],v255,s[68:71],s95 offen offset:1024 sc1
mm v[60:63],a[124:127],v[228:231],v[60:63]
ex v220,v220
mm v[28:31],a[124:127],v[224:227],v[28:31]
ld a[24:27],v255,s[68:71],s95 offen offset:2048 sc1
wt lgkmcnt(4)
mm v[192:195],a[64:67],v[128:131],0
ex v221,v221
mm v[192:195],a[68:71],v[132:135],v[192:195]
cv v238,v220,v221
mm v[192:195],a[72:75],v[136:139],v[192:195]
ex v222,v222
mm v[192:195],a[76:79],v[140:143],v[192:195]
ld v240,s[56:59],s17 offen lds
mm v[200:203],a[76:79],v[156:159],0
ex v223,v223
s_add_u32 s17, s17, 32768
mm v[200:203],a[72:75],v[152:155],v[200:203]
cv v239,v222,v223
s_add_u32 m0, m0, 1024
mm v[200:203],a[68:71],v[148:151],v[200:203]
ex v192,v192
mm v[200:203],a[64:67],v[144:147],v[200:203]
wt lgkmcnt(0)
mm v[196:199],a[80:83],v[128:131],0
ex v193,v193
mm v[196:199],a[84:87],v[132:135],v[196:199]
cv v224,v192,v193
wt vmcnt(6)
v_cmp_eq_u32 s[92:93], v250, v249
mm v[196:199],a[88:91],v[136:139],v[196:199]
ex v194,v194
mm v[196:199],a[92:95],v[140:143],v[196:199]
ld v240,s[56:59],s17 offen lds
mm v[204:207],a[92:95],v[156:159],0
ex v195,v195
s_add_u32 s17, s17, 4294942720
mm v[204:207],a[88:91],v[152:155],v[204:207]
cv v225,v194,v195
s_add_u32 m0, m0, 1152
s_and_b32 s92, s92, 3
s_sub_u32 s92, s92, s84
s_cmp_eq_u32 s92, 3
s_cbranch_scc1 .La5_LA_PS0
.La5_LA_PS1:
mm v[204:207],a[84:87],v[148:151],v[204:207]
ex v200,v200
mm v[204:207],a[80:83],v[144:147],v[204:207]
ld v240,s[56:59],s17 offen lds
mm v[208:211],a[64:67],v[160:163],0
ex v201,v201
s_add_u32 s17, s17, 32768
mm v[208:211],a[68:71],v[164:167],v[208:211]
cv v228,v200,v201
s_add_u32 m0, m0, 1024
ld a[28:31],v255,s[68:71],s95 offen offset:3072 sc1
mm v[208:211],a[72:75],v[168:171],v[208:211]
ex v202,v202
mm v[208:211],a[76:79],v[172:175],v[208:211]
ld v240,s[56:59],s17 offen lds
mm v[216:219],a[76:79],v[188:191],0
ex v203,v203
mm v[216:219],a[72:75],v[184:187],v[216:219]
cv v229,v202,v203
ld a[32:35],v255,s[68:71],s96 offen offset:0 sc1
mm v[216:219],a[68:71],v[180:183],v[216:219]
ex v196,v196
mm v[216:219],a[64:67],v[176:179],v[216:219]
mm v[212:215],a[80:83],v[160:163],0
ex v197,v197
mm v[212:215],a[84:87],v[164:167],v[212:215]
cv v226,v196,v197
buffer_load_dword v250, v251, s[64:67], s20 offen sc1
)ASM" R"ASM(mm v[212:215],a[88:91],v[168:171],v[212:215]
ex v198,v198
mm v[212:215],a[92:95],v[172:175],v[212:215]
ld a[36:39],v255,s[68:71],s96 offen offset:1024 sc1
mm v[220:223],a[92:95],v[188:191],0
ex v199,v199
mm v[220:223],a[88:91],v[184:187],v[220:223]
cv v227,v198,v199
mm v[220:223],a[84:87],v[180:183],v[220:223]
ex v204,v204
mm v[220:223],a[80:83],v[176:179],v[220:223]
ld a[40:43],v255,s[68:71],s96 offen offset:2048 sc1
mm v[64:67],a[96:99],v[232:235],v[64:67]
ex v205,v205
mm v[96:99],a[96:99],v[236:239],v[96:99]
rd a[96:99],v248 offset:8192
cv v230,v204,v205
mm v[100:103],a[100:103],v[236:239],v[100:103]
ex v206,v206
mm v[68:71],a[100:103],v[232:235],v[68:71]
rd a[100:103],v248 offset:9216
ld a[44:47],v255,s[68:71],s96 offen offset:3072 sc1
mm v[72:75],a[104:107],v[232:235],v[72:75]
ex v207,v207
mm v[104:107],a[104:107],v[236:239],v[104:107]
rd a[104:107],v248 offset:10240
cv v231,v206,v207
mm v[108:111],a[108:111],v[236:239],v[108:111]
ex v208,v208
mm v[76:79],a[108:111],v[232:235],v[76:79]
rd a[108:111],v248 offset:11264
ld a[48:51],v255,s[68:71],s97 offen offset:0 sc1
mm v[80:83],a[112:115],v[232:235],v[80:83]
ex v209,v209
mm v[112:115],a[112:115],v[236:239],v[112:115]
rd a[112:115],v248 offset:12288
mm v[116:119],a[116:119],v[236:239],v[116:119]
ex v210,v210
mm v[84:87],a[116:119],v[232:235],v[84:87]
rd a[116:119],v248 offset:13312
ld a[52:55],v255,s[68:71],s97 offen offset:1024 sc1
mm v[88:91],a[120:123],v[232:235],v[88:91]
ex v211,v211
mm v[120:123],a[120:123],v[236:239],v[120:123]
rd a[120:123],v248 offset:14336
mm v[124:127],a[124:127],v[236:239],v[124:127]
ex v216,v216
mm v[92:95],a[124:127],v[232:235],v[92:95]
rd a[124:127],v248 offset:15360
ld a[56:59],v255,s[68:71],s97 offen offset:2048 sc1
v_smfmac_f32_16x16x64_bf16 a[196:199], a[208:211], v[232:239], v241
wt lgkmcnt(4)
v_smfmac_f32_16x16x64_bf16 a[192:195], a[208:211], v[224:231], v241
ld a[60:63],v255,s[68:71],s97 offen offset:3072 sc1
mm v[0:3],a[96:99],v[224:227],v[0:3]
ex v217,v217
mm v[32:35],a[96:99],v[228:231],v[32:35]
cv v232,v208,v209
mm v[36:39],a[100:103],v[228:231],v[36:39]
ex v218,v218
mm v[4:7],a[100:103],v[224:227],v[4:7]
cv v233,v210,v211
mm v[8:11],a[104:107],v[224:227],v[8:11]
ex v219,v219
mm v[40:43],a[104:107],v[228:231],v[40:43]
cv v236,v216,v217
mm v[44:47],a[108:111],v[228:231],v[44:47]
ex v212,v212
wt vmcnt(7)
v_cmp_eq_u32 s[92:93], v250, v249
mm v[12:15],a[108:111],v[224:227],v[12:15]
wt lgkmcnt(0)
mm v[16:19],a[112:115],v[224:227],v[16:19]
ex v213,v213
mm v[48:51],a[112:115],v[228:231],v[48:51]
cv v237,v218,v219
s_and_b32 s92, s92, 3
s_sub_u32 s92, s92, s84
mm v[52:55],a[116:119],v[228:231],v[52:55]
ex v214,v214
mm v[20:23],a[116:119],v[224:227],v[20:23]
cv v234,v212,v213
mm v[24:27],a[120:123],v[224:227],v[24:27]
ex v215,v215
mm v[56:59],a[120:123],v[228:231],v[56:59]
cv v235,v214,v215
mm v[60:63],a[124:127],v[228:231],v[60:63]
ex v220,v220
mm v[28:31],a[124:127],v[224:227],v[28:31]
s_cmp_eq_u32 s92, 3
s_cbranch_scc1 .La5_LA_PS2
.La5_LA_PS3:
s_cmp_lg_u32 s84, 3
s_cbranch_scc1 .La5_LA_PSPIN
; ---- owner: partner-s part added, l checked, O / l leaves; first steps of the O projection
.La5_LA_DC1:
wt vmcnt(8)
mm v[0:3],a[214:217],a[0:3],v[0:3]
ar v250,a192
ar v251,a193
)ASM" R"ASM(v_add_f32 v250, v250, v251
ar v251,a220
v_add_f32 v250, v250, v251
ar v249,a194
mm v[4:7],a[212:215],a[0:3],v[4:7]
ar v251,a195
v_add_f32 v249, v249, v251
ar v251,a221
v_add_f32 v249, v249, v251
v_subrev_u32 v251, 562036736, v250
mm v[8:11],a[214:217],a[4:7],v[8:11]
v_rcp_f32 v250, v250
v_cmp_le_u32 vcc, 1006632960, v251
v_subrev_u32 v251, 562036736, v249
s_mov_b64 s[92:93], vcc
v_cmp_le_u32 vcc, 1006632960, v251
mm v[12:15],a[212:215],a[4:7],v[12:15]
v_rcp_f32 v249, v249
mm v[16:19],a[214:217],a[8:11],v[16:19]
s_or_b64 vcc, vcc, s[92:93]
s_cbranch_scc1 .La5_LA_SC3
.La5_LA_SR3:
ex v221,v221
ex v222,v222
mm v[20:23],a[212:215],a[8:11],v[20:23]
ex v223,v223
cv v238,v220,v221
v_mul_f32 v0, v0, v250
v_mul_f32 v1, v1, v250
v_mul_f32 v2, v2, v250
.La5_LW5_3:
s_cbranch_execz .La5_LW5_R0
mm v[24:27],a[214:217],a[12:15],v[24:27]
v_mul_f32 v3, v3, v250
v_mul_f32 v4, v4, v250
v_mul_f32 v5, v5, v250
v_mul_f32 v6, v6, v250
v_mul_f32 v7, v7, v250
cv v128,v0,v1
mm v[28:31],a[212:215],a[12:15],v[28:31]
cv v129,v2,v3
cv v130,v4,v5
cv v131,v6,v7
st v[128:131],v244,s[44:47],s91 offen offset:0 sc1
cv v239,v222,v223
mm v[32:35],a[214:217],a[16:19],v[32:35]
v_mul_f32 v8, v8, v250
v_mul_f32 v9, v9, v250
v_mul_f32 v10, v10, v250
v_mul_f32 v11, v11, v250
v_mul_f32 v12, v12, v250
v_mul_f32 v13, v13, v250
mm v[36:39],a[212:215],a[16:19],v[36:39]
v_mul_f32 v14, v14, v250
v_mul_f32 v15, v15, v250
cv v132,v8,v9
cv v133,v10,v11
cv v134,v12,v13
cv v135,v14,v15
mm v[40:43],a[214:217],a[20:23],v[40:43]
st v[132:135],v244,s[44:47],s91 offen offset:1024 sc1
v_mul_f32 v16, v16, v250
v_mul_f32 v17, v17, v250
v_mul_f32 v18, v18, v250
v_mul_f32 v19, v19, v250
mm v[44:47],a[212:215],a[20:23],v[44:47]
v_mul_f32 v20, v20, v250
v_mul_f32 v21, v21, v250
v_mul_f32 v22, v22, v250
v_mul_f32 v23, v23, v250
cv v136,v16,v17
cv v137,v18,v19
mm v[48:51],a[214:217],a[24:27],v[48:51]
cv v138,v20,v21
cv v139,v22,v23
st v[136:139],v244,s[44:47],s91 offen offset:2048 sc1
v_mul_f32 v24, v24, v250
v_mul_f32 v25, v25, v250
mm v[52:55],a[212:215],a[24:27],v[52:55]
v_mul_f32 v26, v26, v250
v_mul_f32 v27, v27, v250
v_mul_f32 v28, v28, v250
v_mul_f32 v29, v29, v250
v_mul_f32 v30, v30, v250
v_mul_f32 v31, v31, v250
mm v[56:59],a[214:217],a[28:31],v[56:59]
cv v140,v24,v25
cv v141,v26,v27
cv v142,v28,v29
cv v143,v30,v31
st v[140:143],v244,s[44:47],s91 offen offset:3072 sc1
mm v[60:63],a[212:215],a[28:31],v[60:63]
v_mul_f32 v32, v32, v249
v_mul_f32 v33, v33, v249
v_mul_f32 v34, v34, v249
v_mul_f32 v35, v35, v249
v_mul_f32 v36, v36, v249
v_mul_f32 v37, v37, v249
v_smfmac_f32_16x16x64_bf16 a[196:199], a[208:211], v[232:239], v241
v_mul_f32 v38, v38, v249
)ASM" R"ASM(v_mul_f32 v39, v39, v249
cv v144,v32,v33
cv v145,v34,v35
cv v146,v36,v37
mm v[64:67],a[96:99],v[232:235],v[64:67]
cv v147,v38,v39
st v[144:147],v245,s[44:47],s91 offen offset:0 sc1
v_mul_f32 v40, v40, v249
v_mul_f32 v41, v41, v249
v_mul_f32 v42, v42, v249
mm v[96:99],a[96:99],v[236:239],v[96:99]
v_mul_f32 v43, v43, v249
v_mul_f32 v44, v44, v249
v_mul_f32 v45, v45, v249
v_mul_f32 v46, v46, v249
v_mul_f32 v47, v47, v249
cv v148,v40,v41
mm v[100:103],a[100:103],v[236:239],v[100:103]
cv v149,v42,v43
cv v150,v44,v45
cv v151,v46,v47
st v[148:151],v245,s[44:47],s91 offen offset:1024 sc1
v_mul_f32 v48, v48, v249
mm v[68:71],a[100:103],v[232:235],v[68:71]
v_mul_f32 v49, v49, v249
v_mul_f32 v50, v50, v249
v_mul_f32 v51, v51, v249
v_mul_f32 v52, v52, v249
v_mul_f32 v53, v53, v249
v_mul_f32 v54, v54, v249
mm v[72:75],a[104:107],v[232:235],v[72:75]
v_mul_f32 v55, v55, v249
cv v152,v48,v49
cv v153,v50,v51
cv v154,v52,v53
cv v155,v54,v55
st v[152:155],v245,s[44:47],s91 offen offset:2048 sc1
mm v[104:107],a[104:107],v[236:239],v[104:107]
v_mul_f32 v56, v56, v249
v_mul_f32 v57, v57, v249
v_mul_f32 v58, v58, v249
v_mul_f32 v59, v59, v249
v_mul_f32 v60, v60, v249
v_mul_f32 v61, v61, v249
mm v[108:111],a[108:111],v[236:239],v[108:111]
v_mul_f32 v62, v62, v249
v_mul_f32 v63, v63, v249
cv v156,v56,v57
cv v157,v58,v59
cv v158,v60,v61
cv v159,v62,v63
mm v[76:79],a[108:111],v[232:235],v[76:79]
st v[156:159],v245,s[44:47],s91 offen offset:3072 sc1
ar v240,a224
v_xor_b32 v241, 32, v240
v_xor_b32 v242, 64, v240
v_xor_b32 v243, 96, v240
mm v[80:83],a[112:115],v[232:235],v[80:83]
mm v[112:115],a[112:115],v[236:239],v[112:115]
mm v[116:119],a[116:119],v[236:239],v[116:119]
mm v[84:87],a[116:119],v[232:235],v[84:87]
mm v[88:91],a[120:123],v[232:235],v[88:91]
mm v[120:123],a[120:123],v[236:239],v[120:123]
mm v[124:127],a[124:127],v[236:239],v[124:127]
mm v[92:95],a[124:127],v[232:235],v[92:95]
s_nop 0
s_barrier
wt vmcnt(8)
mm v[64:67],a[214:217],a[32:35],v[64:67]
rt v[192:193],v240 offset:16896
rt v[194:195],v240 offset:17920
mm v[68:71],a[212:215],a[32:35],v[68:71]
rt v[196:197],v240 offset:25344
rt v[198:199],v240 offset:26368
mm v[72:75],a[214:217],a[36:39],v[72:75]
rt v[200:201],v241 offset:16896
rt v[202:203],v241 offset:17920
ar v250,a196
ar v251,a197
v_add_f32 v250, v250, v251
ar v251,a222
mm v[76:79],a[212:215],a[36:39],v[76:79]
rt v[204:205],v241 offset:25344
rt v[206:207],v241 offset:26368
v_add_f32 v250, v250, v251
ar v249,a198
ar v251,a199
v_add_f32 v249, v249, v251
mm v[80:83],a[214:217],a[40:43],v[80:83]
rt v[208:209],v242 offset:16896
rt v[210:211],v242 offset:17920
ar v251,a223
v_add_f32 v249, v249, v251
v_subrev_u32 v251, 562036736, v250
mm v[84:87],a[212:215],a[40:43],v[84:87]
rt v[212:213],v242 offset:25344
rt v[214:215],v242 offset:26368
v_rcp_f32 v250, v250
v_cmp_le_u32 vcc, 1006632960, v251
v_subrev_u32 v251, 562036736, v249
)ASM" R"ASM(mm v[88:91],a[214:217],a[44:47],v[88:91]
rt v[216:217],v243 offset:16896
rt v[218:219],v243 offset:17920
s_mov_b64 s[92:93], vcc
v_cmp_le_u32 vcc, 1006632960, v251
v_rcp_f32 v249, v249
mm v[92:95],a[212:215],a[44:47],v[92:95]
rt v[220:221],v243 offset:25344
rt v[222:223],v243 offset:26368
s_or_b64 vcc, vcc, s[92:93]
s_cbranch_scc1 .La5_LA_SC4
.La5_LA_SR4:
v_mul_f32 v64, v64, v250
v_mul_f32 v65, v65, v250
mm v[96:99],a[214:217],a[48:51],v[96:99]
v_mul_f32 v66, v66, v250
v_mul_f32 v67, v67, v250
v_mul_f32 v68, v68, v250
v_mul_f32 v69, v69, v250
v_mul_f32 v70, v70, v250
v_mul_f32 v71, v71, v250
mm v[100:103],a[212:215],a[48:51],v[100:103]
cv v160,v64,v65
cv v161,v66,v67
cv v162,v68,v69
cv v163,v70,v71
st v[160:163],v246,s[44:47],s91 offen offset:0 sc1
mm v[104:107],a[214:217],a[52:55],v[104:107]
v_mul_f32 v72, v72, v250
v_mul_f32 v73, v73, v250
v_mul_f32 v74, v74, v250
v_mul_f32 v75, v75, v250
v_mul_f32 v76, v76, v250
v_mul_f32 v77, v77, v250
mm v[108:111],a[212:215],a[52:55],v[108:111]
v_mul_f32 v78, v78, v250
v_mul_f32 v79, v79, v250
cv v164,v72,v73
cv v165,v74,v75
cv v166,v76,v77
cv v167,v78,v79
mm v[112:115],a[214:217],a[56:59],v[112:115]
st v[164:167],v246,s[44:47],s91 offen offset:1024 sc1
v_mul_f32 v80, v80, v250
v_mul_f32 v81, v81, v250
v_mul_f32 v82, v82, v250
v_mul_f32 v83, v83, v250
mm v[116:119],a[212:215],a[56:59],v[116:119]
v_mul_f32 v84, v84, v250
v_mul_f32 v85, v85, v250
v_mul_f32 v86, v86, v250
v_mul_f32 v87, v87, v250
cv v168,v80,v81
cv v169,v82,v83
mm v[120:123],a[214:217],a[60:63],v[120:123]
cv v170,v84,v85
cv v171,v86,v87
st v[168:171],v246,s[44:47],s91 offen offset:2048 sc1
v_mul_f32 v88, v88, v250
v_mul_f32 v89, v89, v250
mm v[124:127],a[212:215],a[60:63],v[124:127]
wt lgkmcnt(12)
v_mul_f32 v90, v90, v250
v_mul_f32 v91, v91, v250
v_mul_f32 v92, v92, v250
v_mul_f32 v93, v93, v250
v_mul_f32 v94, v94, v250
mm v[0:3],v[192:195],v[128:131],0
v_mul_f32 v95, v95, v250
cv v172,v88,v89
cv v173,v90,v91
cv v174,v92,v93
cv v175,v94,v95
st v[172:175],v246,s[44:47],s91 offen offset:3072 sc1
mm v[0:3],v[196:199],v[132:135],v[0:3]
v_mul_f32 v96, v96, v249
v_mul_f32 v97, v97, v249
v_mul_f32 v98, v98, v249
v_mul_f32 v99, v99, v249
v_mul_f32 v100, v100, v249
v_mul_f32 v101, v101, v249
mm v[32:35],v[196:199],v[148:151],0
wt lgkmcnt(8)
v_mul_f32 v102, v102, v249
v_mul_f32 v103, v103, v249
cv v176,v96,v97
cv v177,v98,v99
cv v178,v100,v101
mm v[32:35],v[192:195],v[144:147],v[32:35]
rt v[196:197],v240 offset:17024
rt v[198:199],v240 offset:18048
cv v179,v102,v103
st v[176:179],v247,s[44:47],s91 offen offset:0 sc1
v_mul_f32 v104, v104, v249
mm v[36:39],v[200:203],v[144:147],0
v_mul_f32 v105, v105, v249
v_mul_f32 v106, v106, v249
v_mul_f32 v107, v107, v249
v_mul_f32 v108, v108, v249
v_mul_f32 v109, v109, v249
)ASM" R"ASM(v_mul_f32 v110, v110, v249
mm v[36:39],v[204:207],v[148:151],v[36:39]
rt v[192:193],v240 offset:25472
rt v[194:195],v240 offset:26496
v_mul_f32 v111, v111, v249
cv v180,v104,v105
cv v181,v106,v107
cv v182,v108,v109
mm v[4:7],v[204:207],v[132:135],0
wt lgkmcnt(4)
cv v183,v110,v111
st v[180:183],v247,s[44:47],s91 offen offset:1024 sc1
v_mul_f32 v112, v112, v249
v_mul_f32 v113, v113, v249
mm v[4:7],v[200:203],v[128:131],v[4:7]
rt v[204:205],v241 offset:17024
rt v[206:207],v241 offset:18048
v_mul_f32 v114, v114, v249
v_mul_f32 v115, v115, v249
v_mul_f32 v116, v116, v249
v_mul_f32 v117, v117, v249
mm v[8:11],v[208:211],v[128:131],0
v_mul_f32 v118, v118, v249
v_mul_f32 v119, v119, v249
cv v184,v112,v113
cv v185,v114,v115
cv v186,v116,v117
cv v187,v118,v119
mm v[8:11],v[212:215],v[132:135],v[8:11]
rt v[200:201],v241 offset:25472
rt v[202:203],v241 offset:26496
st v[184:187],v247,s[44:47],s91 offen offset:2048 sc1
v_mul_f32 v120, v120, v249
v_mul_f32 v121, v121, v249
mm v[40:43],v[212:215],v[148:151],0
v_mul_f32 v122, v122, v249
v_mul_f32 v123, v123, v249
v_mul_f32 v124, v124, v249
v_mul_f32 v125, v125, v249
v_mul_f32 v126, v126, v249
v_mul_f32 v127, v127, v249
mm v[40:43],v[208:211],v[144:147],v[40:43]
rt v[212:213],v242 offset:17024
rt v[214:215],v242 offset:18048
cv v188,v120,v121
cv v189,v122,v123
cv v190,v124,v125
cv v191,v126,v127
mm v[44:47],v[216:219],v[144:147],0
st v[188:191],v247,s[44:47],s91 offen offset:3072 sc1
mm v[44:47],v[220:223],v[148:151],v[44:47]
rt v[208:209],v242 offset:25472
rt v[210:211],v242 offset:26496
mm v[12:15],v[220:223],v[132:135],0
wt lgkmcnt(4)
mm v[12:15],v[216:219],v[128:131],v[12:15]
rt v[220:221],v243 offset:17024
rt v[222:223],v243 offset:18048
mm v[16:19],v[196:199],v[128:131],0
ar v224,a228
v_add_u32 v225, 0x8000, v224
v_add_u32 v226, 0x2000, v224
v_add_u32 v227, 0xa000, v224
mm v[16:19],v[192:195],v[132:135],v[16:19]
rt v[216:217],v243 offset:25472
rt v[218:219],v243 offset:26496
ar v248,a225
ar v228,a229
mm v[48:51],v[192:195],v[148:151],0
ar v229,a230
mm v[48:51],v[196:199],v[144:147],v[48:51]
rt v[192:193],v240 offset:33792
rt v[194:195],v240 offset:34816
mm v[52:55],v[204:207],v[144:147],0
mm v[52:55],v[200:203],v[148:151],v[52:55]
rt v[196:197],v240 offset:42240
rt v[198:199],v240 offset:43264
mm v[20:23],v[200:203],v[132:135],0
wt lgkmcnt(4)
mm v[20:23],v[204:207],v[128:131],v[20:23]
rt v[200:201],v241 offset:33792
rt v[202:203],v241 offset:34816
mm v[24:27],v[212:215],v[128:131],0
mm v[24:27],v[208:211],v[132:135],v[24:27]
rt v[204:205],v241 offset:42240
rt v[206:207],v241 offset:43264
mm v[56:59],v[208:211],v[148:151],0
mm v[56:59],v[212:215],v[144:147],v[56:59]
rt v[208:209],v242 offset:33792
rt v[210:211],v242 offset:34816
mm v[60:63],v[220:223],v[144:147],0
mm v[60:63],v[216:219],v[148:151],v[60:63]
rt v[212:213],v242 offset:42240
rt v[214:215],v242 offset:43264
mm v[28:31],v[216:219],v[132:135],0
wt lgkmcnt(4)
mm v[28:31],v[220:223],v[128:131],v[28:31]
rt v[216:217],v243 offset:33792
rt v[218:219],v243 offset:34816
mm v[0:3],v[192:195],v[136:139],v[0:3]
)ASM" R"ASM(ar v249,a231
s_bitcmp1_b32 2863311530, s2
s_cbranch_scc1 .La5_LO_VC1
s_getpc_b64 s[24:25]
.La5_LW3_P1:
s_add_u32 s24, s24, .La5_LW3_R1-.La5_LW3_P1
s_addc_u32 s25, s25, 0
s_mov_b64 exec, 0
s_cmp_eq_u32 s3, 0
s_cbranch_scc1 .La5_LW3_0
s_cmp_eq_u32 s3, 1
s_cbranch_scc1 .La5_LW3_1
s_cmp_eq_u32 s3, 2
s_cbranch_scc1 .La5_LW3_2
s_branch .La5_LW3_3
.La5_LW3_R1:
s_mov_b64 exec, -1
.La5_LO_VE1:
wt vmcnt(0)
buffer_store_dwordx2 v[252:253], v249, s[64:67], s6 offen offset:0 sc1
mm v[0:3],v[196:199],v[140:143],v[0:3]
rt v[220:221],v243 offset:42240
rt v[222:223],v243 offset:43264
mm v[32:35],v[196:199],v[156:159],v[32:35]
mm v[32:35],v[192:195],v[152:155],v[32:35]
rt v[196:197],v240 offset:33920
rt v[198:199],v240 offset:34944
mm v[36:39],v[200:203],v[152:155],v[36:39]
mm v[36:39],v[204:207],v[156:159],v[36:39]
rt v[192:193],v240 offset:42368
rt v[194:195],v240 offset:43392
mm v[4:7],v[204:207],v[140:143],v[4:7]
wt lgkmcnt(4)
mm v[4:7],v[200:203],v[136:139],v[4:7]
rt v[204:205],v241 offset:33920
rt v[206:207],v241 offset:34944
mm v[8:11],v[208:211],v[136:139],v[8:11]
mm v[8:11],v[212:215],v[140:143],v[8:11]
rt v[200:201],v241 offset:42368
rt v[202:203],v241 offset:43392
mm v[40:43],v[212:215],v[156:159],v[40:43]
mm v[40:43],v[208:211],v[152:155],v[40:43]
rt v[212:213],v242 offset:33920
rt v[214:215],v242 offset:34944
mm v[44:47],v[216:219],v[152:155],v[44:47]
mm v[44:47],v[220:223],v[156:159],v[44:47]
rt v[208:209],v242 offset:42368
rt v[210:211],v242 offset:43392
mm v[12:15],v[220:223],v[140:143],v[12:15]
wt lgkmcnt(4)
mm v[12:15],v[216:219],v[136:139],v[12:15]
rt v[220:221],v243 offset:33920
rt v[222:223],v243 offset:34944
mm v[16:19],v[196:199],v[136:139],v[16:19]
mm v[16:19],v[192:195],v[140:143],v[16:19]
rt v[216:217],v243 offset:42368
rt v[218:219],v243 offset:43392
mm v[48:51],v[192:195],v[156:159],v[48:51]
mm v[48:51],v[196:199],v[152:155],v[48:51]
rt v[192:193],v240 offset:16896
rt v[194:195],v240 offset:17920
mm v[52:55],v[204:207],v[152:155],v[52:55]
mm v[52:55],v[200:203],v[156:159],v[52:55]
rt v[196:197],v240 offset:25344
rt v[198:199],v240 offset:26368
mm v[20:23],v[200:203],v[140:143],v[20:23]
wt lgkmcnt(4)
mm v[20:23],v[204:207],v[136:139],v[20:23]
rt v[200:201],v241 offset:16896
rt v[202:203],v241 offset:17920
mm v[24:27],v[212:215],v[136:139],v[24:27]
mm v[24:27],v[208:211],v[140:143],v[24:27]
rt v[204:205],v241 offset:25344
rt v[206:207],v241 offset:26368
mm v[56:59],v[208:211],v[156:159],v[56:59]
mm v[56:59],v[212:215],v[152:155],v[56:59]
rt v[208:209],v242 offset:16896
rt v[210:211],v242 offset:17920
mm v[60:63],v[220:223],v[152:155],v[60:63]
mm v[60:63],v[216:219],v[156:159],v[60:63]
rt v[212:213],v242 offset:25344
rt v[214:215],v242 offset:26368
mm v[28:31],v[216:219],v[140:143],v[28:31]
wt lgkmcnt(4)
mm v[28:31],v[220:223],v[136:139],v[28:31]
rt v[216:217],v243 offset:16896
rt v[218:219],v243 offset:17920
s_cmp_lg_u32 s30, 0
s_nop 0
s_nop 0
s_cbranch_scc1 .La5_LO_W
; ---- O projection: 256 x 128 tile over eight heads
.La5_LO_START:
.La5_LW6_6:
s_cbranch_execz .La5_LW6_R0
s_bitcmp1_b32 2863311530, s2
s_cbranch_scc1 .La5_LO_SC
; ---- O projection, even XCDs: eight and a half heads
.La5_LO_SB:
mm v[64:67],v[192:195],v[160:163],0
)ASM" R"ASM(buffer_load_dwordx2 v[250:251], v248, s[64:67], s5 offen sc1
mm v[64:67],v[196:199],v[164:167],v[64:67]
rt v[220:221],v243 offset:25344
rt v[222:223],v243 offset:26368
mm v[96:99],v[196:199],v[180:183],0
s_add_u32 m0, s83, 0
s_bfe_u32 s80, s4, 262148
mm v[96:99],v[192:195],v[176:179],v[96:99]
rt v[196:197],v240 offset:17024
rt v[198:199],v240 offset:18048
s_xor_b32 s80, s80, s72
s_lshl_b32 s80, s80, 18
s_add_u32 s80, s80, 131072
mm v[100:103],v[200:203],v[176:179],0
ld v224,s[56:59],s80 offen lds
mm v[100:103],v[204:207],v[180:183],v[100:103]
rt v[192:193],v240 offset:25472
rt v[194:195],v240 offset:26496
mm v[68:71],v[204:207],v[164:167],0
s_add_u32 m0, s83, 1024
wt lgkmcnt(4)
mm v[68:71],v[200:203],v[160:163],v[68:71]
rt v[204:205],v241 offset:17024
rt v[206:207],v241 offset:18048
mm v[72:75],v[208:211],v[160:163],0
ld v225,s[56:59],s80 offen lds
mm v[72:75],v[212:215],v[164:167],v[72:75]
rt v[200:201],v241 offset:25472
rt v[202:203],v241 offset:26496
mm v[104:107],v[212:215],v[180:183],0
s_add_u32 m0, s83, 2176
s_bfe_u32 s53, s4, 262148
mm v[104:107],v[208:211],v[176:179],v[104:107]
rt v[212:213],v242 offset:17024
rt v[214:215],v242 offset:18048
mm v[108:111],v[216:219],v[176:179],0
ld v226,s[56:59],s80 offen lds
mm v[108:111],v[220:223],v[180:183],v[108:111]
rt v[208:209],v242 offset:25472
rt v[210:211],v242 offset:26496
mm v[76:79],v[220:223],v[164:167],0
s_add_u32 m0, s83, 3200
wt lgkmcnt(4)
mm v[76:79],v[216:219],v[160:163],v[76:79]
rt v[220:221],v243 offset:17024
rt v[222:223],v243 offset:18048
mm v[80:83],v[196:199],v[160:163],0
ld v227,s[56:59],s80 offen lds
mm v[80:83],v[192:195],v[164:167],v[80:83]
rt v[216:217],v243 offset:25472
rt v[218:219],v243 offset:26496
mm v[112:115],v[192:195],v[180:183],0
s_add_u32 m0, s83, 118272
s_bfe_u32 s80, s4, 262152
mm v[112:115],v[196:199],v[176:179],v[112:115]
rt v[192:193],v240 offset:33792
rt v[194:195],v240 offset:34816
s_xor_b32 s80, s80, s72
s_lshl_b32 s80, s80, 18
mm v[116:119],v[204:207],v[176:179],0
ld v224,s[56:59],s80 offen lds
mm v[116:119],v[200:203],v[180:183],v[116:119]
rt v[196:197],v240 offset:42240
rt v[198:199],v240 offset:43264
mm v[84:87],v[200:203],v[164:167],0
s_add_u32 m0, s83, 119296
wt lgkmcnt(4)
mm v[84:87],v[204:207],v[160:163],v[84:87]
rt v[200:201],v241 offset:33792
rt v[202:203],v241 offset:34816
mm v[88:91],v[212:215],v[160:163],0
ld v225,s[56:59],s80 offen lds
mm v[88:91],v[208:211],v[164:167],v[88:91]
rt v[204:205],v241 offset:42240
rt v[206:207],v241 offset:43264
mm v[120:123],v[208:211],v[180:183],0
s_add_u32 m0, s83, 120448
s_xor_b32 s53, s53, s72
mm v[120:123],v[212:215],v[176:179],v[120:123]
rt v[208:209],v242 offset:33792
rt v[210:211],v242 offset:34816
mm v[124:127],v[220:223],v[176:179],0
ld v226,s[56:59],s80 offen lds
mm v[124:127],v[216:219],v[180:183],v[124:127]
rt v[212:213],v242 offset:42240
rt v[214:215],v242 offset:43264
mm v[92:95],v[216:219],v[164:167],0
wt vmcnt(7)
v_cmp_eq_u64 vcc, v[250:251], v[252:253]
s_nop 0
s_bitcmp1_b32 vcc_lo, 1
s_cbranch_scc0 .La5_LO_NB1
.La5_LO_KB1:
wt lgkmcnt(4)
mm v[92:95],v[220:223],v[160:163],v[92:95]
rt v[216:217],v243 offset:33792
rt v[218:219],v243 offset:34816
s_lshl_b32 s53, s53, 13
mm v[64:67],v[192:195],v[168:171],v[64:67]
ld a[0:3],v244,s[44:47],s53 offen offset:0 sc1
)ASM" R"ASM(mm v[64:67],v[196:199],v[172:175],v[64:67]
rt v[220:221],v243 offset:42240
rt v[222:223],v243 offset:43264
mm v[96:99],v[196:199],v[188:191],v[96:99]
ld a[4:7],v245,s[44:47],s53 offen offset:0 sc1
mm v[96:99],v[192:195],v[184:187],v[96:99]
rt v[196:197],v240 offset:33920
rt v[198:199],v240 offset:34944
mm v[100:103],v[200:203],v[184:187],v[100:103]
ld a[8:11],v246,s[44:47],s53 offen offset:0 sc1
mm v[100:103],v[204:207],v[188:191],v[100:103]
rt v[192:193],v240 offset:42368
rt v[194:195],v240 offset:43392
mm v[68:71],v[204:207],v[172:175],v[68:71]
wt lgkmcnt(4)
s_bfe_u32 s54, s4, 262152
mm v[68:71],v[200:203],v[168:171],v[68:71]
rt v[204:205],v241 offset:33920
rt v[206:207],v241 offset:34944
mm v[72:75],v[208:211],v[168:171],v[72:75]
ld a[12:15],v247,s[44:47],s53 offen offset:0 sc1
mm v[72:75],v[212:215],v[172:175],v[72:75]
rt v[200:201],v241 offset:42368
rt v[202:203],v241 offset:43392
mm v[104:107],v[212:215],v[188:191],v[104:107]
ld a[16:19],v244,s[44:47],s53 offen offset:1024 sc1
mm v[104:107],v[208:211],v[184:187],v[104:107]
rt v[212:213],v242 offset:33920
rt v[214:215],v242 offset:34944
mm v[108:111],v[216:219],v[184:187],v[108:111]
ld a[20:23],v245,s[44:47],s53 offen offset:1024 sc1
mm v[108:111],v[220:223],v[188:191],v[108:111]
rt v[208:209],v242 offset:42368
rt v[210:211],v242 offset:43392
mm v[76:79],v[220:223],v[172:175],v[76:79]
s_nop 0
s_barrier
wt lgkmcnt(4)
mm v[76:79],v[216:219],v[168:171],v[76:79]
rt v[220:221],v243 offset:33920
rt v[222:223],v243 offset:34944
mm v[80:83],v[196:199],v[168:171],v[80:83]
ld a[24:27],v246,s[44:47],s53 offen offset:1024 sc1
mm v[80:83],v[192:195],v[172:175],v[80:83]
rt v[216:217],v243 offset:42368
rt v[218:219],v243 offset:43392
mm v[112:115],v[192:195],v[188:191],v[112:115]
ld a[28:31],v247,s[44:47],s53 offen offset:1024 sc1
.La5_LW3_0:
s_cbranch_execz .La5_LW3_4
mm v[112:115],v[196:199],v[184:187],v[112:115]
rt v[192:193],v240 offset:50688
rt v[194:195],v240 offset:51712
mm v[116:119],v[204:207],v[184:187],v[116:119]
ld a[32:35],v244,s[44:47],s53 offen offset:2048 sc1
mm v[116:119],v[200:203],v[188:191],v[116:119]
rt v[196:197],v240 offset:59136
rt v[198:199],v240 offset:60160
mm v[84:87],v[200:203],v[172:175],v[84:87]
wt lgkmcnt(4)
s_xor_b32 s54, s54, s72
mm v[84:87],v[204:207],v[168:171],v[84:87]
rt v[200:201],v241 offset:50688
rt v[202:203],v241 offset:51712
mm v[88:91],v[212:215],v[168:171],v[88:91]
ld a[36:39],v245,s[44:47],s53 offen offset:2048 sc1
mm v[88:91],v[208:211],v[172:175],v[88:91]
rt v[204:205],v241 offset:59136
rt v[206:207],v241 offset:60160
mm v[120:123],v[208:211],v[188:191],v[120:123]
ld a[40:43],v246,s[44:47],s53 offen offset:2048 sc1
mm v[120:123],v[212:215],v[184:187],v[120:123]
rt v[208:209],v242 offset:50688
rt v[210:211],v242 offset:51712
mm v[124:127],v[220:223],v[184:187],v[124:127]
ld a[44:47],v247,s[44:47],s53 offen offset:2048 sc1
mm v[124:127],v[216:219],v[188:191],v[124:127]
rt v[212:213],v242 offset:59136
rt v[214:215],v242 offset:60160
mm v[92:95],v[216:219],v[172:175],v[92:95]
wt vmcnt(4) lgkmcnt(4)
s_lshl_b32 s54, s54, 13
mm v[92:95],v[220:223],v[168:171],v[92:95]
rt v[216:217],v243 offset:50688
rt v[218:219],v243 offset:51712
mm v[0:3],v[192:195],a[0:3],v[0:3]
s_bfe_u32 s55, s4, 262156
s_xor_b32 s55, s55, s72
mm v[0:3],v[196:199],a[16:19],v[0:3]
ld a[48:51],v244,s[44:47],s53 offen offset:3072 sc1
mm v[32:35],v[196:199],a[20:23],v[32:35]
s_lshl_b32 s55, s55, 13
s_bfe_u32 s52, s4, 262160
mm v[32:35],v[192:195],a[4:7],v[32:35]
rt v[220:221],v243 offset:59136
rt v[222:223],v243 offset:60160
mm v[64:67],v[192:195],a[8:11],v[64:67]
s_xor_b32 s52, s52, s72
s_lshl_b32 s52, s52, 13
mm v[64:67],v[196:199],a[24:27],v[64:67]
)ASM" R"ASM(ld a[52:55],v245,s[44:47],s53 offen offset:3072 sc1
mm v[96:99],v[196:199],a[28:31],v[96:99]
mm v[96:99],v[192:195],a[12:15],v[96:99]
rt v[196:197],v240 offset:50816
rt v[198:199],v240 offset:51840
mm v[100:103],v[200:203],a[12:15],v[100:103]
mm v[100:103],v[204:207],a[28:31],v[100:103]
buffer_load_dwordx2 v[250:251], v248, s[64:67], s5 offen sc1
mm v[68:71],v[204:207],a[24:27],v[68:71]
mm v[68:71],v[200:203],a[8:11],v[68:71]
rt v[192:193],v240 offset:59264
rt v[194:195],v240 offset:60288
mm v[36:39],v[200:203],a[4:7],v[36:39]
mm v[36:39],v[204:207],a[20:23],v[36:39]
ld a[56:59],v246,s[44:47],s53 offen offset:3072 sc1
mm v[4:7],v[204:207],a[16:19],v[4:7]
wt lgkmcnt(4)
mm v[4:7],v[200:203],a[0:3],v[4:7]
rt v[204:205],v241 offset:50816
rt v[206:207],v241 offset:51840
mm v[8:11],v[208:211],a[0:3],v[8:11]
mm v[8:11],v[212:215],a[16:19],v[8:11]
ld a[60:63],v247,s[44:47],s53 offen offset:3072 sc1
mm v[40:43],v[212:215],a[20:23],v[40:43]
s_bfe_u32 s53, s4, 262164
s_xor_b32 s53, s53, s72
mm v[40:43],v[208:211],a[4:7],v[40:43]
rt v[200:201],v241 offset:59264
rt v[202:203],v241 offset:60288
mm v[72:75],v[208:211],a[8:11],v[72:75]
s_add_u32 m0, s83, 121472
s_lshl_b32 s53, s53, 13
mm v[72:75],v[212:215],a[24:27],v[72:75]
ld v227,s[56:59],s80 offen lds
mm v[104:107],v[212:215],a[28:31],v[104:107]
s_add_u32 s80, s80, 131072
mm v[104:107],v[208:211],a[12:15],v[104:107]
rt v[212:213],v242 offset:50816
rt v[214:215],v242 offset:51840
mm v[108:111],v[216:219],a[12:15],v[108:111]
s_add_u32 m0, s83, 67584
mm v[108:111],v[220:223],a[28:31],v[108:111]
ld v224,s[56:59],s80 offen lds
mm v[76:79],v[220:223],a[24:27],v[76:79]
mm v[76:79],v[216:219],a[8:11],v[76:79]
rt v[208:209],v242 offset:59264
rt v[210:211],v242 offset:60288
mm v[44:47],v[216:219],a[4:7],v[44:47]
s_add_u32 m0, s83, 68608
mm v[44:47],v[220:223],a[20:23],v[44:47]
ld v225,s[56:59],s80 offen lds
mm v[12:15],v[220:223],a[16:19],v[12:15]
wt lgkmcnt(4)
mm v[12:15],v[216:219],a[0:3],v[12:15]
rt v[220:221],v243 offset:50816
rt v[222:223],v243 offset:51840
mm v[16:19],v[196:199],a[0:3],v[16:19]
s_add_u32 m0, s83, 69760
mm v[16:19],v[192:195],a[16:19],v[16:19]
ld v226,s[56:59],s80 offen lds
mm v[48:51],v[192:195],a[20:23],v[48:51]
mm v[48:51],v[196:199],a[4:7],v[48:51]
rt v[216:217],v243 offset:59264
rt v[218:219],v243 offset:60288
mm v[80:83],v[196:199],a[8:11],v[80:83]
s_add_u32 m0, s83, 70784
s_nop 0
s_barrier
mm v[80:83],v[192:195],a[24:27],v[80:83]
ld v227,s[56:59],s80 offen lds
mm v[112:115],v[192:195],a[28:31],v[112:115]
s_bfe_u32 s80, s4, 262156
s_xor_b32 s80, s80, s72
mm v[112:115],v[196:199],a[12:15],v[112:115]
rt v[192:193],v240 offset:0
rt v[194:195],v240 offset:1024
mm v[116:119],v[204:207],a[12:15],v[116:119]
s_add_u32 m0, s83, 84480
s_lshl_b32 s80, s80, 18
mm v[116:119],v[200:203],a[28:31],v[116:119]
ld v224,s[56:59],s80 offen lds
mm v[84:87],v[200:203],a[24:27],v[84:87]
mm v[84:87],v[204:207],a[8:11],v[84:87]
rt v[196:197],v240 offset:8448
rt v[198:199],v240 offset:9472
mm v[52:55],v[204:207],a[4:7],v[52:55]
s_add_u32 m0, s83, 85504
mm v[52:55],v[200:203],a[20:23],v[52:55]
ld v225,s[56:59],s80 offen lds
mm v[20:23],v[200:203],a[16:19],v[20:23]
wt lgkmcnt(4)
mm v[20:23],v[204:207],a[0:3],v[20:23]
rt v[200:201],v241 offset:0
rt v[202:203],v241 offset:1024
mm v[24:27],v[212:215],a[0:3],v[24:27]
s_add_u32 m0, s83, 86656
mm v[24:27],v[208:211],a[16:19],v[24:27]
ld v226,s[56:59],s80 offen lds
mm v[56:59],v[208:211],a[20:23],v[56:59]
wt vmcnt(10)
)ASM" R"ASM(v_cmp_eq_u64 vcc, v[250:251], v[252:253]
mm v[56:59],v[212:215],a[4:7],v[56:59]
rt v[204:205],v241 offset:8448
rt v[206:207],v241 offset:9472
mm v[88:91],v[212:215],a[8:11],v[88:91]
s_add_u32 m0, s83, 87680
mm v[88:91],v[208:211],a[24:27],v[88:91]
ld v227,s[56:59],s80 offen lds
mm v[120:123],v[208:211],a[28:31],v[120:123]
s_bitcmp1_b32 vcc_lo, 2
s_nop 0
s_nop 0
s_cbranch_scc0 .La5_LO_NB2
.La5_LO_KB2:
mm v[120:123],v[212:215],a[12:15],v[120:123]
rt v[208:209],v242 offset:0
rt v[210:211],v242 offset:1024
mm v[124:127],v[220:223],a[12:15],v[124:127]
s_add_u32 s80, s80, 131072
mm v[124:127],v[216:219],a[28:31],v[124:127]
ld a[12:15],v244,s[44:47],s54 offen offset:0 sc1
mm v[92:95],v[216:219],a[24:27],v[92:95]
mm v[92:95],v[220:223],a[8:11],v[92:95]
rt v[212:213],v242 offset:8448
rt v[214:215],v242 offset:9472
mm v[60:63],v[220:223],a[4:7],v[60:63]
mm v[60:63],v[216:219],a[20:23],v[60:63]
mm v[28:31],v[216:219],a[16:19],v[28:31]
wt vmcnt(10) lgkmcnt(4)
mm v[28:31],v[220:223],a[0:3],v[28:31]
rt v[216:217],v243 offset:0
rt v[218:219],v243 offset:1024
mm v[0:3],v[192:195],a[32:35],v[0:3]
mm v[0:3],v[196:199],a[48:51],v[0:3]
ld a[0:3],v245,s[44:47],s54 offen offset:0 sc1
mm v[32:35],v[196:199],a[52:55],v[32:35]
mm v[32:35],v[192:195],a[36:39],v[32:35]
rt v[220:221],v243 offset:8448
rt v[222:223],v243 offset:9472
mm v[64:67],v[192:195],a[40:43],v[64:67]
mm v[64:67],v[196:199],a[56:59],v[64:67]
ld a[4:7],v246,s[44:47],s54 offen offset:0 sc1
mm v[96:99],v[196:199],a[60:63],v[96:99]
mm v[96:99],v[192:195],a[44:47],v[96:99]
rt v[196:197],v240 offset:128
rt v[198:199],v240 offset:1152
mm v[100:103],v[200:203],a[44:47],v[100:103]
s_mov_b32 s86, 0x05040100
s_mov_b32 s87, 0x07060302
mm v[100:103],v[204:207],a[60:63],v[100:103]
ld a[8:11],v247,s[44:47],s54 offen offset:0 sc1
mm v[68:71],v[204:207],a[56:59],v[68:71]
s_nop 0
ar v230,a128
mm v[68:71],v[200:203],a[40:43],v[68:71]
rt v[192:193],v240 offset:8576
rt v[194:195],v240 offset:9600
mm v[36:39],v[200:203],a[36:39],v[36:39]
v_add_u32 v240, 67584, v240
ar v231,a132
mm v[36:39],v[204:207],a[52:55],v[36:39]
ld a[16:19],v244,s[44:47],s54 offen offset:1024 sc1
mm v[4:7],v[204:207],a[48:51],v[4:7]
wt lgkmcnt(4)
v_perm_b32 v232, v231, v230, s86
mm v[4:7],v[200:203],a[32:35],v[4:7]
rt v[204:205],v241 offset:128
rt v[206:207],v241 offset:1152
mm v[8:11],v[208:211],a[32:35],v[8:11]
v_perm_b32 v233, v231, v230, s87
aw a96,v232
mm v[8:11],v[212:215],a[48:51],v[8:11]
ld a[20:23],v245,s[44:47],s54 offen offset:1024 sc1
mm v[40:43],v[212:215],a[52:55],v[40:43]
aw a100,v233
ar v234,a136
mm v[40:43],v[208:211],a[36:39],v[40:43]
rt v[200:201],v241 offset:8576
rt v[202:203],v241 offset:9600
mm v[72:75],v[208:211],a[40:43],v[72:75]
v_add_u32 v241, 67584, v241
ar v235,a140
mm v[72:75],v[212:215],a[56:59],v[72:75]
ld a[24:27],v246,s[44:47],s54 offen offset:1024 sc1
mm v[104:107],v[212:215],a[60:63],v[104:107]
v_perm_b32 v236, v235, v234, s86
v_perm_b32 v237, v235, v234, s87
mm v[104:107],v[208:211],a[44:47],v[104:107]
rt v[212:213],v242 offset:128
rt v[214:215],v242 offset:1152
mm v[108:111],v[216:219],a[44:47],v[108:111]
aw a97,v236
aw a101,v237
mm v[108:111],v[220:223],a[60:63],v[108:111]
ld a[28:31],v247,s[44:47],s54 offen offset:1024 sc1
mm v[76:79],v[220:223],a[56:59],v[76:79]
ar v230,a144
ar v231,a148
mm v[76:79],v[216:219],a[40:43],v[76:79]
rt v[208:209],v242 offset:8576
)ASM" R"ASM(rt v[210:211],v242 offset:9600
mm v[44:47],v[216:219],a[36:39],v[44:47]
v_add_u32 v242, 67584, v242
v_perm_b32 v232, v231, v230, s86
mm v[44:47],v[220:223],a[52:55],v[44:47]
v_perm_b32 v233, v231, v230, s87
aw a98,v232
mm v[12:15],v[220:223],a[48:51],v[12:15]
wt lgkmcnt(4)
aw a102,v233
mm v[12:15],v[216:219],a[32:35],v[12:15]
rt v[220:221],v243 offset:128
rt v[222:223],v243 offset:1152
mm v[16:19],v[196:199],a[32:35],v[16:19]
s_add_u32 m0, s83, 101376
ar v234,a152
mm v[16:19],v[192:195],a[48:51],v[16:19]
ld v224,s[56:59],s80 offen lds
mm v[48:51],v[192:195],a[52:55],v[48:51]
ar v235,a156
v_perm_b32 v236, v235, v234, s86
mm v[48:51],v[196:199],a[36:39],v[48:51]
rt v[216:217],v243 offset:8576
rt v[218:219],v243 offset:9600
mm v[80:83],v[196:199],a[40:43],v[80:83]
s_add_u32 m0, s83, 102400
v_add_u32 v243, 67584, v243
mm v[80:83],v[192:195],a[56:59],v[80:83]
ld v225,s[56:59],s80 offen lds
mm v[112:115],v[192:195],a[60:63],v[112:115]
wt vmcnt(14)
s_barrier
mm v[112:115],v[196:199],a[44:47],v[112:115]
rt v[192:193],v240 offset:50688
rt v[194:195],v240 offset:51712
mm v[116:119],v[204:207],a[44:47],v[116:119]
s_add_u32 m0, s83, 103552
v_perm_b32 v237, v235, v234, s87
mm v[116:119],v[200:203],a[60:63],v[116:119]
ld v226,s[56:59],s80 offen lds
mm v[84:87],v[200:203],a[56:59],v[84:87]
aw a99,v236
aw a103,v237
mm v[84:87],v[204:207],a[40:43],v[84:87]
rt v[196:197],v240 offset:59136
rt v[198:199],v240 offset:60160
mm v[52:55],v[204:207],a[36:39],v[52:55]
ar v230,a129
ar v231,a133
mm v[52:55],v[200:203],a[52:55],v[52:55]
ld a[64:67],v244,s[44:47],s54 offen offset:2048 sc1
mm v[20:23],v[200:203],a[48:51],v[20:23]
wt lgkmcnt(4)
v_perm_b32 v232, v231, v230, s86
mm v[20:23],v[204:207],a[32:35],v[20:23]
rt v[200:201],v241 offset:50688
rt v[202:203],v241 offset:51712
mm v[24:27],v[212:215],a[32:35],v[24:27]
v_perm_b32 v233, v231, v230, s87
aw a104,v232
mm v[24:27],v[208:211],a[48:51],v[24:27]
ld a[68:71],v245,s[44:47],s54 offen offset:2048 sc1
mm v[56:59],v[208:211],a[52:55],v[56:59]
aw a108,v233
ar v234,a137
mm v[56:59],v[212:215],a[36:39],v[56:59]
rt v[204:205],v241 offset:59136
rt v[206:207],v241 offset:60160
mm v[88:91],v[212:215],a[40:43],v[88:91]
ar v235,a141
v_perm_b32 v236, v235, v234, s86
mm v[88:91],v[208:211],a[56:59],v[88:91]
ld a[72:75],v246,s[44:47],s54 offen offset:2048 sc1
mm v[120:123],v[208:211],a[60:63],v[120:123]
v_perm_b32 v237, v235, v234, s87
aw a105,v236
mm v[120:123],v[212:215],a[44:47],v[120:123]
rt v[208:209],v242 offset:50688
rt v[210:211],v242 offset:51712
mm v[124:127],v[220:223],a[44:47],v[124:127]
aw a109,v237
ar v230,a145
mm v[124:127],v[216:219],a[60:63],v[124:127]
buffer_load_dwordx2 v[250:251], v248, s[64:67], s5 offen sc1
mm v[92:95],v[216:219],a[56:59],v[92:95]
ar v231,a149
v_perm_b32 v232, v231, v230, s86
mm v[92:95],v[220:223],a[40:43],v[92:95]
rt v[212:213],v242 offset:59136
rt v[214:215],v242 offset:60160
mm v[60:63],v[220:223],a[36:39],v[60:63]
v_perm_b32 v233, v231, v230, s87
aw a106,v232
mm v[60:63],v[216:219],a[52:55],v[60:63]
ld a[36:39],v247,s[44:47],s54 offen offset:2048 sc1
mm v[28:31],v[216:219],a[48:51],v[28:31]
wt vmcnt(8) lgkmcnt(4)
aw a110,v233
mm v[28:31],v[220:223],a[32:35],v[28:31]
rt v[216:217],v243 offset:50688
)ASM" R"ASM(rt v[218:219],v243 offset:51712
mm v[0:3],v[192:195],a[12:15],v[0:3]
ar v234,a153
ar v235,a157
mm v[0:3],v[196:199],a[16:19],v[0:3]
ld a[32:35],v244,s[44:47],s54 offen offset:3072 sc1
mm v[32:35],v[196:199],a[20:23],v[32:35]
v_perm_b32 v236, v235, v234, s86
v_perm_b32 v237, v235, v234, s87
mm v[32:35],v[192:195],a[0:3],v[32:35]
rt v[220:221],v243 offset:59136
rt v[222:223],v243 offset:60160
mm v[64:67],v[192:195],a[4:7],v[64:67]
aw a107,v236
aw a111,v237
mm v[64:67],v[196:199],a[24:27],v[64:67]
ld a[40:43],v245,s[44:47],s54 offen offset:3072 sc1
mm v[96:99],v[196:199],a[28:31],v[96:99]
ar v230,a130
ar v231,a134
mm v[96:99],v[192:195],a[8:11],v[96:99]
rt v[196:197],v240 offset:50816
rt v[198:199],v240 offset:51840
mm v[100:103],v[200:203],a[8:11],v[100:103]
v_perm_b32 v232, v231, v230, s86
v_perm_b32 v233, v231, v230, s87
mm v[100:103],v[204:207],a[28:31],v[100:103]
ld a[44:47],v246,s[44:47],s54 offen offset:3072 sc1
mm v[68:71],v[204:207],a[24:27],v[68:71]
aw a112,v232
aw a116,v233
mm v[68:71],v[200:203],a[4:7],v[68:71]
rt v[192:193],v240 offset:59264
rt v[194:195],v240 offset:60288
mm v[36:39],v[200:203],a[0:3],v[36:39]
ar v234,a138
ar v235,a142
mm v[36:39],v[204:207],a[20:23],v[36:39]
ld a[48:51],v247,s[44:47],s54 offen offset:3072 sc1
mm v[4:7],v[204:207],a[16:19],v[4:7]
wt lgkmcnt(4)
s_bfe_u32 s54, s4, 262164
mm v[4:7],v[200:203],a[12:15],v[4:7]
rt v[204:205],v241 offset:50816
rt v[206:207],v241 offset:51840
mm v[8:11],v[208:211],a[12:15],v[8:11]
s_add_u32 m0, s83, 104576
s_xor_b32 s54, s54, s72
mm v[8:11],v[212:215],a[16:19],v[8:11]
ld v227,s[56:59],s80 offen lds
mm v[40:43],v[212:215],a[20:23],v[40:43]
s_bitcmp1_b32 vcc_lo, 3
s_cbranch_scc0 .La5_LO_NB3
.La5_LO_KB3:
mm v[40:43],v[208:211],a[0:3],v[40:43]
rt v[200:201],v241 offset:59264
rt v[202:203],v241 offset:60288
mm v[72:75],v[208:211],a[4:7],v[72:75]
s_bfe_u32 s80, s4, 262160
s_xor_b32 s80, s80, s72
mm v[72:75],v[212:215],a[24:27],v[72:75]
ld a[52:55],v244,s[44:47],s55 offen offset:0 sc1
mm v[104:107],v[212:215],a[28:31],v[104:107]
s_lshl_b32 s80, s80, 18
s_lshl_b32 s54, s54, 13
mm v[104:107],v[208:211],a[8:11],v[104:107]
rt v[212:213],v242 offset:50816
rt v[214:215],v242 offset:51840
mm v[108:111],v[216:219],a[8:11],v[108:111]
s_xor_b32 s54, s54, 229376
v_perm_b32 v236, v235, v234, s86
mm v[108:111],v[220:223],a[28:31],v[108:111]
ld a[56:59],v245,s[44:47],s55 offen offset:0 sc1
mm v[76:79],v[220:223],a[24:27],v[76:79]
v_perm_b32 v237, v235, v234, s87
aw a113,v236
mm v[76:79],v[216:219],a[4:7],v[76:79]
rt v[208:209],v242 offset:59264
rt v[210:211],v242 offset:60288
mm v[44:47],v[216:219],a[0:3],v[44:47]
aw a117,v237
ar v230,a146
mm v[44:47],v[220:223],a[20:23],v[44:47]
ar v231,a150
v_perm_b32 v232, v231, v230, s86
mm v[12:15],v[220:223],a[16:19],v[12:15]
wt lgkmcnt(4)
v_perm_b32 v233, v231, v230, s87
mm v[12:15],v[216:219],a[12:15],v[12:15]
rt v[220:221],v243 offset:50816
rt v[222:223],v243 offset:51840
mm v[16:19],v[196:199],a[12:15],v[16:19]
aw a114,v232
aw a118,v233
mm v[16:19],v[192:195],a[16:19],v[16:19]
ld a[60:63],v246,s[44:47],s55 offen offset:0 sc1
mm v[48:51],v[192:195],a[20:23],v[48:51]
ar v234,a154
ar v235,a158
mm v[48:51],v[196:199],a[0:3],v[48:51]
)ASM" R"ASM(rt v[216:217],v243 offset:59264
rt v[218:219],v243 offset:60288
mm v[80:83],v[196:199],a[4:7],v[80:83]
v_perm_b32 v236, v235, v234, s86
v_perm_b32 v237, v235, v234, s87
mm v[80:83],v[192:195],a[24:27],v[80:83]
ld a[76:79],v247,s[44:47],s55 offen offset:0 sc1
mm v[112:115],v[192:195],a[28:31],v[112:115]
aw a115,v236
aw a119,v237
mm v[112:115],v[196:199],a[8:11],v[112:115]
rt v[192:193],v240 offset:0
rt v[194:195],v240 offset:1024
mm v[116:119],v[204:207],a[8:11],v[116:119]
ar v230,a131
ar v231,a135
mm v[116:119],v[200:203],a[28:31],v[116:119]
ld a[80:83],v244,s[44:47],s55 offen offset:1024 sc1
mm v[84:87],v[200:203],a[24:27],v[84:87]
v_perm_b32 v232, v231, v230, s86
v_perm_b32 v233, v231, v230, s87
mm v[84:87],v[204:207],a[4:7],v[84:87]
rt v[196:197],v240 offset:8448
rt v[198:199],v240 offset:9472
mm v[52:55],v[204:207],a[0:3],v[52:55]
s_load_dwordx2 s[16:17], s[0:1], 0
s_load_dwordx4 s[36:39], s[0:1], 80
mm v[52:55],v[200:203],a[20:23],v[52:55]
ld a[84:87],v245,s[44:47],s55 offen offset:1024 sc1
mm v[20:23],v[200:203],a[16:19],v[20:23]
wt lgkmcnt(4)
aw a120,v232
mm v[20:23],v[204:207],a[12:15],v[20:23]
rt v[200:201],v241 offset:0
rt v[202:203],v241 offset:1024
mm v[24:27],v[212:215],a[12:15],v[24:27]
aw a124,v233
ar v234,a139
mm v[24:27],v[208:211],a[16:19],v[24:27]
ld a[88:91],v246,s[44:47],s55 offen offset:1024 sc1
mm v[56:59],v[208:211],a[20:23],v[56:59]
ar v235,a143
v_perm_b32 v236, v235, v234, s86
mm v[56:59],v[212:215],a[0:3],v[56:59]
rt v[204:205],v241 offset:8448
rt v[206:207],v241 offset:9472
mm v[88:91],v[212:215],a[4:7],v[88:91]
v_perm_b32 v237, v235, v234, s87
aw a121,v236
mm v[88:91],v[208:211],a[24:27],v[88:91]
ld a[92:95],v247,s[44:47],s55 offen offset:1024 sc1
mm v[120:123],v[208:211],a[28:31],v[120:123]
aw a125,v237
ar v230,a147
mm v[120:123],v[212:215],a[8:11],v[120:123]
rt v[208:209],v242 offset:0
rt v[210:211],v242 offset:1024
mm v[124:127],v[220:223],a[8:11],v[124:127]
s_add_u32 m0, s83, 16896
ar v231,a151
mm v[124:127],v[216:219],a[28:31],v[124:127]
ld v224,s[56:59],s80 offen lds
mm v[92:95],v[216:219],a[24:27],v[92:95]
v_perm_b32 v232, v231, v230, s86
v_perm_b32 v233, v231, v230, s87
mm v[92:95],v[220:223],a[4:7],v[92:95]
rt v[212:213],v242 offset:8448
rt v[214:215],v242 offset:9472
mm v[60:63],v[220:223],a[0:3],v[60:63]
aw a122,v232
aw a126,v233
mm v[60:63],v[216:219],a[20:23],v[60:63]
ar v234,a155
ar v235,a159
mm v[28:31],v[216:219],a[16:19],v[28:31]
wt vmcnt(10) lgkmcnt(4)
v_perm_b32 v236, v235, v234, s86
mm v[28:31],v[220:223],a[12:15],v[28:31]
rt v[216:217],v243 offset:0
rt v[218:219],v243 offset:1024
mm v[0:3],v[192:195],a[64:67],v[0:3]
s_add_u32 m0, s83, 17920
v_perm_b32 v237, v235, v234, s87
mm v[0:3],v[196:199],a[32:35],v[0:3]
.La5_LW3_1:
s_cbranch_execz .La5_LW3_5
ld v225,s[56:59],s80 offen lds
mm v[32:35],v[196:199],a[40:43],v[32:35]
aw a123,v236
aw a127,v237
mm v[32:35],v[192:195],a[68:71],v[32:35]
rt v[220:221],v243 offset:8448
rt v[222:223],v243 offset:9472
mm v[64:67],v[192:195],a[72:75],v[64:67]
s_add_u32 m0, s83, 19072
ar v230,a160
mm v[64:67],v[196:199],a[44:47],v[64:67]
ld v226,s[56:59],s80 offen lds
mm v[96:99],v[196:199],a[48:51],v[96:99]
ar v231,a164
)ASM" R"ASM(v_perm_b32 v232, v231, v230, s86
mm v[96:99],v[192:195],a[36:39],v[96:99]
rt v[196:197],v240 offset:128
rt v[198:199],v240 offset:1152
mm v[100:103],v[200:203],a[36:39],v[100:103]
v_perm_b32 v233, v231, v230, s87
aw a128,v232
mm v[100:103],v[204:207],a[48:51],v[100:103]
ld a[0:3],v244,s[44:47],s55 offen offset:2048 sc1
mm v[68:71],v[204:207],a[44:47],v[68:71]
aw a132,v233
ar v234,a168
mm v[68:71],v[200:203],a[72:75],v[68:71]
rt v[192:193],v240 offset:8576
rt v[194:195],v240 offset:9600
mm v[36:39],v[200:203],a[68:71],v[36:39]
ar v235,a172
v_perm_b32 v236, v235, v234, s86
mm v[36:39],v[204:207],a[40:43],v[36:39]
ld a[4:7],v245,s[44:47],s55 offen offset:2048 sc1
mm v[4:7],v[204:207],a[32:35],v[4:7]
wt lgkmcnt(4)
v_perm_b32 v237, v235, v234, s87
mm v[4:7],v[200:203],a[64:67],v[4:7]
rt v[204:205],v241 offset:128
rt v[206:207],v241 offset:1152
mm v[8:11],v[208:211],a[64:67],v[8:11]
aw a129,v236
aw a133,v237
mm v[8:11],v[212:215],a[32:35],v[8:11]
ld a[8:11],v246,s[44:47],s55 offen offset:2048 sc1
mm v[40:43],v[212:215],a[40:43],v[40:43]
ar v230,a176
ar v231,a180
mm v[40:43],v[208:211],a[68:71],v[40:43]
rt v[200:201],v241 offset:8576
rt v[202:203],v241 offset:9600
mm v[72:75],v[208:211],a[72:75],v[72:75]
v_perm_b32 v232, v231, v230, s86
v_perm_b32 v233, v231, v230, s87
mm v[72:75],v[212:215],a[44:47],v[72:75]
ld a[12:15],v247,s[44:47],s55 offen offset:2048 sc1
mm v[104:107],v[212:215],a[48:51],v[104:107]
aw a130,v232
aw a134,v233
mm v[104:107],v[208:211],a[36:39],v[104:107]
rt v[212:213],v242 offset:128
rt v[214:215],v242 offset:1152
mm v[108:111],v[216:219],a[36:39],v[108:111]
ar v234,a184
ar v235,a188
mm v[108:111],v[220:223],a[48:51],v[108:111]
ld a[16:19],v244,s[44:47],s55 offen offset:3072 sc1
mm v[76:79],v[220:223],a[44:47],v[76:79]
v_perm_b32 v236, v235, v234, s86
v_perm_b32 v237, v235, v234, s87
mm v[76:79],v[216:219],a[72:75],v[76:79]
rt v[208:209],v242 offset:8576
rt v[210:211],v242 offset:9600
mm v[44:47],v[216:219],a[68:71],v[44:47]
aw a131,v236
aw a135,v237
mm v[44:47],v[220:223],a[40:43],v[44:47]
s_nop 0
v_cmp_eq_u64 vcc, v[250:251], v[252:253]
mm v[12:15],v[220:223],a[32:35],v[12:15]
wt lgkmcnt(4)
ar v230,a161
mm v[12:15],v[216:219],a[64:67],v[12:15]
rt v[220:221],v243 offset:128
rt v[222:223],v243 offset:1152
mm v[16:19],v[196:199],a[64:67],v[16:19]
ar v231,a165
v_perm_b32 v232, v231, v230, s86
mm v[16:19],v[192:195],a[32:35],v[16:19]
ld a[20:23],v245,s[44:47],s55 offen offset:3072 sc1
mm v[48:51],v[192:195],a[40:43],v[48:51]
v_perm_b32 v233, v231, v230, s87
aw a136,v232
mm v[48:51],v[196:199],a[68:71],v[48:51]
rt v[216:217],v243 offset:8576
rt v[218:219],v243 offset:9600
mm v[80:83],v[196:199],a[72:75],v[80:83]
aw a140,v233
ar v234,a169
mm v[80:83],v[192:195],a[44:47],v[80:83]
ld a[24:27],v246,s[44:47],s55 offen offset:3072 sc1
mm v[112:115],v[192:195],a[48:51],v[112:115]
wt vmcnt(18) lgkmcnt(0)
s_barrier
mm v[112:115],v[196:199],a[36:39],v[112:115]
rt v[192:193],v240 offset:16896
rt v[194:195],v240 offset:17920
mm v[116:119],v[204:207],a[36:39],v[116:119]
s_xor_b32 s90, s72, 0
s_lshl_b32 s92, s76, 5
mm v[116:119],v[200:203],a[48:51],v[116:119]
buffer_load_dwordx2 v[250:251], v248, s[64:67], s5 offen sc1
mm v[84:87],v[200:203],a[44:47],v[84:87]
s_add_u32 s92, s92, s90
)ASM" R"ASM(s_lshl2_add_u32 s92, s92, s3
mm v[84:87],v[204:207],a[72:75],v[84:87]
rt v[196:197],v240 offset:25344
rt v[198:199],v240 offset:26368
mm v[52:55],v[204:207],a[68:71],v[52:55]
s_lshl_b32 s92, s92, 12
ar v235,a173
mm v[52:55],v[200:203],a[40:43],v[52:55]
ld a[28:31],v247,s[44:47],s55 offen offset:3072 sc1
mm v[20:23],v[200:203],a[32:35],v[20:23]
s_nop 0
s_bfe_u32 s55, s4, 262168
mm v[20:23],v[204:207],a[64:67],v[20:23]
rt v[200:201],v241 offset:16896
rt v[202:203],v241 offset:17920
mm v[24:27],v[212:215],a[64:67],v[24:27]
s_add_u32 m0, s83, 20096
s_xor_b32 s55, s55, s72
mm v[24:27],v[208:211],a[32:35],v[24:27]
ld v227,s[56:59],s80 offen lds
mm v[56:59],v[208:211],a[40:43],v[56:59]
s_add_u32 s80, s80, 131072
s_lshl_b32 s55, s55, 13
mm v[56:59],v[212:215],a[68:71],v[56:59]
rt v[204:205],v241 offset:25344
rt v[206:207],v241 offset:26368
mm v[88:91],v[212:215],a[72:75],v[88:91]
s_add_u32 m0, s83, 33792
v_perm_b32 v236, v235, v234, s86
mm v[88:91],v[208:211],a[44:47],v[88:91]
ld v224,s[56:59],s80 offen lds
mm v[120:123],v[208:211],a[48:51],v[120:123]
s_add_u32 s68, s16, s92
s_addc_u32 s69, s17, 0
mm v[120:123],v[212:215],a[36:39],v[120:123]
rt v[208:209],v242 offset:16896
rt v[210:211],v242 offset:17920
mm v[124:127],v[220:223],a[36:39],v[124:127]
s_add_u32 m0, s83, 34816
s_and_b32 s69, s69, 0xffff
mm v[124:127],v[216:219],a[48:51],v[124:127]
ld v225,s[56:59],s80 offen lds
mm v[92:95],v[216:219],a[44:47],v[92:95]
s_mov_b32 s70, 4096
s_mov_b32 s71, 0x20000
mm v[92:95],v[220:223],a[72:75],v[92:95]
rt v[212:213],v242 offset:25344
rt v[214:215],v242 offset:26368
mm v[60:63],v[220:223],a[68:71],v[60:63]
s_add_u32 m0, s83, 35968
v_perm_b32 v237, v235, v234, s87
mm v[60:63],v[216:219],a[40:43],v[60:63]
ld v226,s[56:59],s80 offen lds
mm v[28:31],v[216:219],a[32:35],v[28:31]
wt vmcnt(16) lgkmcnt(4)
aw a137,v236
mm v[28:31],v[220:223],a[64:67],v[28:31]
rt v[216:217],v243 offset:16896
rt v[218:219],v243 offset:17920
mm v[0:3],v[192:195],a[52:55],v[0:3]
s_add_u32 m0, s83, 36992
aw a141,v237
mm v[0:3],v[196:199],a[80:83],v[0:3]
ld v227,s[56:59],s80 offen lds
mm v[32:35],v[196:199],a[84:87],v[32:35]
s_bitcmp1_b32 vcc_lo, 4
s_cbranch_scc0 .La5_LO_NB4
.La5_LO_KB4:
mm v[32:35],v[192:195],a[56:59],v[32:35]
rt v[220:221],v243 offset:25344
rt v[222:223],v243 offset:26368
mm v[64:67],v[192:195],a[60:63],v[64:67]
s_bfe_u32 s80, s4, 262164
s_xor_b32 s80, s80, s72
mm v[64:67],v[196:199],a[88:91],v[64:67]
ld a[32:35],v244,s[44:47],s52 offen offset:0 sc1
mm v[96:99],v[196:199],a[92:95],v[96:99]
s_lshl_b32 s80, s80, 18
ar v230,a177
mm v[96:99],v[192:195],a[76:79],v[96:99]
rt v[196:197],v240 offset:17024
rt v[198:199],v240 offset:18048
mm v[100:103],v[200:203],a[76:79],v[100:103]
s_add_u32 s48, s36, 0
s_addc_u32 s49, s37, 0
mm v[100:103],v[204:207],a[92:95],v[100:103]
ld a[36:39],v245,s[44:47],s52 offen offset:0 sc1
mm v[68:71],v[204:207],a[88:91],v[68:71]
s_and_b32 s49, s49, 0xffff
s_mov_b32 s50, 2048
mm v[68:71],v[200:203],a[60:63],v[68:71]
rt v[192:193],v240 offset:25472
rt v[194:195],v240 offset:26496
mm v[36:39],v[200:203],a[56:59],v[36:39]
s_mov_b32 s51, 0x20000
v_and_b32 v238, 0xf0, v255
mm v[36:39],v[204:207],a[84:87],v[36:39]
ld a[40:43],v246,s[44:47],s52 offen offset:0 sc1
mm v[4:7],v[204:207],a[80:83],v[4:7]
wt lgkmcnt(4)
)ASM" R"ASM(v_lshlrev_b32 v238, 2, v238
mm v[4:7],v[200:203],a[52:55],v[4:7]
rt v[204:205],v241 offset:17024
rt v[206:207],v241 offset:18048
mm v[8:11],v[208:211],a[52:55],v[8:11]
v_bfe_u32 v249, v255, 8, 1
v_lshl_add_u32 v238, v249, 5, v238
mm v[8:11],v[212:215],a[80:83],v[8:11]
ld a[44:47],v247,s[44:47],s52 offen offset:0 sc1
mm v[40:43],v[212:215],a[84:87],v[40:43]
v_lshrrev_b32 v249, 9, v255
v_lshl_add_u32 v238, v249, 4, v238
mm v[40:43],v[208:211],a[56:59],v[40:43]
rt v[200:201],v241 offset:25472
rt v[202:203],v241 offset:26496
mm v[72:75],v[208:211],a[60:63],v[72:75]
s_lshr_b32 s23, s72, 3
s_lshl3_add_u32 s92, s23, s76
mm v[72:75],v[212:215],a[88:91],v[72:75]
ld a[48:51],v244,s[44:47],s52 offen offset:1024 sc1
mm v[104:107],v[212:215],a[92:95],v[104:107]
s_lshl_b32 s92, s92, 19
s_lshl_b32 s97, s3, 16
mm v[104:107],v[208:211],a[76:79],v[104:107]
rt v[212:213],v242 offset:17024
rt v[214:215],v242 offset:18048
mm v[108:111],v[216:219],a[76:79],v[108:111]
s_add_u32 s94, s92, s97
s_add_u32 s95, s94, 32768
mm v[108:111],v[220:223],a[92:95],v[108:111]
ld a[64:67],v245,s[44:47],s52 offen offset:1024 sc1
mm v[76:79],v[220:223],a[88:91],v[76:79]
s_sub_u32 s96, s92, s97
s_add_u32 s96, s96, 458752
mm v[76:79],v[216:219],a[60:63],v[76:79]
rt v[208:209],v242 offset:25472
rt v[210:211],v242 offset:26496
mm v[44:47],v[216:219],a[56:59],v[44:47]
s_add_u32 s97, s96, 32768
s_lshl3_add_u32 s92, s23, s101
mm v[44:47],v[220:223],a[84:87],v[44:47]
s_lshl_b32 s85, s76, 15
s_lshl3_add_u32 s92, s92, s85
mm v[12:15],v[220:223],a[80:83],v[12:15]
wt lgkmcnt(4)
s_add_u32 s92, s92, 2097152
mm v[12:15],v[216:219],a[52:55],v[12:15]
rt v[220:221],v243 offset:17024
rt v[222:223],v243 offset:18048
mm v[16:19],v[196:199],a[52:55],v[16:19]
ar v231,a181
v_perm_b32 v232, v231, v230, s86
mm v[16:19],v[192:195],a[80:83],v[16:19]
ld a[68:71],v246,s[44:47],s52 offen offset:1024 sc1
mm v[48:51],v[192:195],a[84:87],v[48:51]
v_perm_b32 v233, v231, v230, s87
aw a138,v232
mm v[48:51],v[196:199],a[56:59],v[48:51]
rt v[216:217],v243 offset:25472
rt v[218:219],v243 offset:26496
mm v[80:83],v[196:199],a[60:63],v[80:83]
aw a142,v233
ar v234,a185
mm v[80:83],v[192:195],a[88:91],v[80:83]
ld a[72:75],v247,s[44:47],s52 offen offset:1024 sc1
mm v[112:115],v[192:195],a[92:95],v[112:115]
ar v235,a189
v_perm_b32 v236, v235, v234, s86
mm v[112:115],v[196:199],a[76:79],v[112:115]
rt v[192:193],v240 offset:33792
rt v[194:195],v240 offset:34816
mm v[116:119],v[204:207],a[76:79],v[116:119]
s_add_u32 m0, s83, 50688
v_perm_b32 v237, v235, v234, s87
mm v[116:119],v[200:203],a[92:95],v[116:119]
ld v224,s[56:59],s80 offen lds
mm v[84:87],v[200:203],a[88:91],v[84:87]
aw a139,v236
aw a143,v237
mm v[84:87],v[204:207],a[60:63],v[84:87]
rt v[196:197],v240 offset:42240
rt v[198:199],v240 offset:43264
mm v[52:55],v[204:207],a[56:59],v[52:55]
s_add_u32 m0, s83, 51712
ar v230,a162
mm v[52:55],v[200:203],a[84:87],v[52:55]
ld v225,s[56:59],s80 offen lds
mm v[20:23],v[200:203],a[80:83],v[20:23]
wt lgkmcnt(4)
ar v231,a166
mm v[20:23],v[204:207],a[52:55],v[20:23]
rt v[200:201],v241 offset:33792
rt v[202:203],v241 offset:34816
mm v[24:27],v[212:215],a[52:55],v[24:27]
s_add_u32 m0, s83, 52864
v_perm_b32 v232, v231, v230, s86
mm v[24:27],v[208:211],a[80:83],v[24:27]
ld v226,s[56:59],s80 offen lds
mm v[56:59],v[208:211],a[84:87],v[56:59]
v_perm_b32 v233, v231, v230, s87
)ASM" R"ASM(aw a144,v232
mm v[56:59],v[212:215],a[56:59],v[56:59]
rt v[204:205],v241 offset:42240
rt v[206:207],v241 offset:43264
mm v[88:91],v[212:215],a[60:63],v[88:91]
aw a148,v233
ar v234,a170
mm v[88:91],v[208:211],a[88:91],v[88:91]
ld v[136:139],v244,s[44:47],s52 offen offset:2048 sc1
mm v[120:123],v[208:211],a[92:95],v[120:123]
ar v235,a174
v_perm_b32 v236, v235, v234, s86
mm v[120:123],v[212:215],a[76:79],v[120:123]
rt v[208:209],v242 offset:33792
rt v[210:211],v242 offset:34816
mm v[124:127],v[220:223],a[76:79],v[124:127]
v_perm_b32 v237, v235, v234, s87
aw a145,v236
mm v[124:127],v[216:219],a[92:95],v[124:127]
ld v[140:143],v245,s[44:47],s52 offen offset:2048 sc1
mm v[92:95],v[216:219],a[88:91],v[92:95]
aw a149,v237
ar v230,a178
mm v[92:95],v[220:223],a[60:63],v[92:95]
rt v[212:213],v242 offset:42240
rt v[214:215],v242 offset:43264
mm v[60:63],v[220:223],a[56:59],v[60:63]
ar v231,a182
v_perm_b32 v232, v231, v230, s86
mm v[60:63],v[216:219],a[84:87],v[60:63]
v_perm_b32 v233, v231, v230, s87
aw a146,v232
mm v[28:31],v[216:219],a[80:83],v[28:31]
wt vmcnt(18) lgkmcnt(4)
aw a150,v233
mm v[28:31],v[220:223],a[52:55],v[28:31]
rt v[216:217],v243 offset:33792
rt v[218:219],v243 offset:34816
mm v[0:3],v[192:195],a[0:3],v[0:3]
ar v234,a186
ar v235,a190
mm v[0:3],v[196:199],a[16:19],v[0:3]
ld v[152:155],v246,s[44:47],s52 offen offset:2048 sc1
mm v[32:35],v[196:199],a[20:23],v[32:35]
v_perm_b32 v236, v235, v234, s86
v_perm_b32 v237, v235, v234, s87
mm v[32:35],v[192:195],a[4:7],v[32:35]
rt v[220:221],v243 offset:42240
rt v[222:223],v243 offset:43264
mm v[64:67],v[192:195],a[8:11],v[64:67]
aw a147,v236
aw a151,v237
mm v[64:67],v[196:199],a[24:27],v[64:67]
ld v[156:159],v247,s[44:47],s52 offen offset:2048 sc1
mm v[96:99],v[196:199],a[28:31],v[96:99]
ar v230,a163
ar v231,a167
mm v[96:99],v[192:195],a[12:15],v[96:99]
rt v[196:197],v240 offset:33920
rt v[198:199],v240 offset:34944
mm v[100:103],v[200:203],a[12:15],v[100:103]
v_perm_b32 v232, v231, v230, s86
v_perm_b32 v233, v231, v230, s87
mm v[100:103],v[204:207],a[28:31],v[100:103]
ld v[168:171],v244,s[44:47],s52 offen offset:3072 sc1
mm v[68:71],v[204:207],a[24:27],v[68:71]
aw a152,v232
aw a156,v233
mm v[68:71],v[200:203],a[8:11],v[68:71]
rt v[192:193],v240 offset:42368
rt v[194:195],v240 offset:43392
mm v[36:39],v[200:203],a[4:7],v[36:39]
v_add_u32 v240, 4294899712, v240
ar v234,a171
mm v[36:39],v[204:207],a[20:23],v[36:39]
ld v[172:175],v245,s[44:47],s52 offen offset:3072 sc1
mm v[4:7],v[204:207],a[16:19],v[4:7]
wt lgkmcnt(4)
ar v235,a175
mm v[4:7],v[200:203],a[0:3],v[4:7]
rt v[204:205],v241 offset:33920
rt v[206:207],v241 offset:34944
mm v[8:11],v[208:211],a[0:3],v[8:11]
s_nop 0
v_cmp_eq_u64 vcc, v[250:251], v[252:253]
mm v[8:11],v[212:215],a[16:19],v[8:11]
ld v[184:187],v246,s[44:47],s52 offen offset:3072 sc1
mm v[40:43],v[212:215],a[20:23],v[40:43]
v_perm_b32 v236, v235, v234, s86
v_perm_b32 v237, v235, v234, s87
mm v[40:43],v[208:211],a[4:7],v[40:43]
rt v[200:201],v241 offset:42368
rt v[202:203],v241 offset:43392
mm v[72:75],v[208:211],a[8:11],v[72:75]
v_add_u32 v241, 4294899712, v241
aw a153,v236
mm v[72:75],v[212:215],a[24:27],v[72:75]
ld v[188:191],v247,s[44:47],s52 offen offset:3072 sc1
mm v[104:107],v[212:215],a[28:31],v[104:107]
s_bfe_u32 s52, s4, 262172
)ASM" R"ASM(s_xor_b32 s52, s52, s72
mm v[104:107],v[208:211],a[12:15],v[104:107]
rt v[212:213],v242 offset:33920
rt v[214:215],v242 offset:34944
mm v[108:111],v[216:219],a[12:15],v[108:111]
s_lshl_b32 s52, s52, 13
aw a157,v237
mm v[108:111],v[220:223],a[28:31],v[108:111]
buffer_load_dwordx2 v[250:251], v248, s[64:67], s5 offen sc1
mm v[76:79],v[220:223],a[24:27],v[76:79]
ar v230,a179
ar v231,a183
mm v[76:79],v[216:219],a[8:11],v[76:79]
rt v[208:209],v242 offset:42368
rt v[210:211],v242 offset:43392
mm v[44:47],v[216:219],a[4:7],v[44:47]
s_add_u32 m0, s83, 53888
v_add_u32 v242, 4294899712, v242
mm v[44:47],v[220:223],a[20:23],v[44:47]
ld v227,s[56:59],s80 offen lds
mm v[12:15],v[220:223],a[16:19],v[12:15]
wt lgkmcnt(4)
s_add_u32 s80, s80, 131072
mm v[12:15],v[216:219],a[0:3],v[12:15]
rt v[220:221],v243 offset:33920
rt v[222:223],v243 offset:34944
mm v[16:19],v[196:199],a[0:3],v[16:19]
s_add_u32 m0, s83, 0
v_perm_b32 v232, v231, v230, s86
mm v[16:19],v[192:195],a[16:19],v[16:19]
ld v224,s[56:59],s80 offen lds
mm v[48:51],v[192:195],a[20:23],v[48:51]
v_perm_b32 v233, v231, v230, s87
aw a154,v232
mm v[48:51],v[196:199],a[4:7],v[48:51]
rt v[216:217],v243 offset:42368
rt v[218:219],v243 offset:43392
mm v[80:83],v[196:199],a[8:11],v[80:83]
s_add_u32 m0, s83, 1024
v_add_u32 v243, 4294899712, v243
mm v[80:83],v[192:195],a[24:27],v[80:83]
ld v225,s[56:59],s80 offen lds
mm v[112:115],v[192:195],a[28:31],v[112:115]
wt vmcnt(23) lgkmcnt(0)
s_barrier
mm v[112:115],v[196:199],a[12:15],v[112:115]
rt v[192:193],v240 offset:16896
rt v[194:195],v240 offset:17920
mm v[116:119],v[204:207],a[12:15],v[116:119]
s_add_u32 m0, s83, 2176
aw a158,v233
mm v[116:119],v[200:203],a[28:31],v[116:119]
ld v226,s[56:59],s80 offen lds
mm v[84:87],v[200:203],a[24:27],v[84:87]
ar v234,a187
ar v235,a191
mm v[84:87],v[204:207],a[8:11],v[84:87]
rt v[196:197],v240 offset:25344
rt v[198:199],v240 offset:26368
mm v[52:55],v[204:207],a[4:7],v[52:55]
s_add_u32 m0, s83, 3200
v_perm_b32 v236, v235, v234, s86
mm v[52:55],v[200:203],a[20:23],v[52:55]
ld v227,s[56:59],s80 offen lds
mm v[20:23],v[200:203],a[16:19],v[20:23]
s_nop 0
s_bfe_u32 s80, s4, 262164
mm v[20:23],v[204:207],a[0:3],v[20:23]
rt v[200:201],v241 offset:16896
rt v[202:203],v241 offset:17920
mm v[24:27],v[212:215],a[0:3],v[24:27]
s_bitcmp1_b32 vcc_lo, 5
s_cbranch_scc0 .La5_LO_NB5
.La5_LO_KB5:
mm v[24:27],v[208:211],a[16:19],v[24:27]
ld a[52:55],v244,s[44:47],s53 offen offset:0 sc1
mm v[56:59],v[208:211],a[20:23],v[56:59]
s_xor_b32 s80, s80, s72
s_lshl_b32 s80, s80, 18
mm v[56:59],v[212:215],a[4:7],v[56:59]
rt v[204:205],v241 offset:25344
rt v[206:207],v241 offset:26368
mm v[88:91],v[212:215],a[8:11],v[88:91]
s_xor_b32 s80, s80, 7340032
s_add_u32 s80, s80, 131072
mm v[88:91],v[208:211],a[24:27],v[88:91]
ld a[56:59],v245,s[44:47],s53 offen offset:0 sc1
mm v[120:123],v[208:211],a[28:31],v[120:123]
v_perm_b32 v237, v235, v234, s87
aw a155,v236
mm v[120:123],v[212:215],a[12:15],v[120:123]
rt v[208:209],v242 offset:16896
rt v[210:211],v242 offset:17920
mm v[124:127],v[220:223],a[12:15],v[124:127]
aw a159,v237
ar v230,a192
mm v[124:127],v[216:219],a[28:31],v[124:127]
ld a[12:15],v246,s[44:47],s53 offen offset:0 sc1
mm v[92:95],v[216:219],a[24:27],v[92:95]
ar v231,a196
)ASM" R"ASM(v_perm_b32 v232, v231, v230, s86
mm v[92:95],v[220:223],a[8:11],v[92:95]
rt v[212:213],v242 offset:25344
rt v[214:215],v242 offset:26368
mm v[60:63],v[220:223],a[4:7],v[60:63]
v_perm_b32 v233, v231, v230, s87
aw a160,v232
mm v[60:63],v[216:219],a[20:23],v[60:63]
aw a164,v233
ar v234,a200
mm v[28:31],v[216:219],a[16:19],v[28:31]
wt vmcnt(20) lgkmcnt(4)
ar v235,a204
mm v[28:31],v[220:223],a[0:3],v[28:31]
rt v[216:217],v243 offset:16896
rt v[218:219],v243 offset:17920
mm v[0:3],v[192:195],a[32:35],v[0:3]
v_perm_b32 v236, v235, v234, s86
v_perm_b32 v237, v235, v234, s87
mm v[0:3],v[196:199],a[48:51],v[0:3]
ld a[0:3],v247,s[44:47],s53 offen offset:0 sc1
mm v[32:35],v[196:199],a[64:67],v[32:35]
aw a161,v236
aw a165,v237
mm v[32:35],v[192:195],a[36:39],v[32:35]
rt v[220:221],v243 offset:25344
.La5_LW3_2:
s_cbranch_execz .La5_LW3_6
rt v[222:223],v243 offset:26368
mm v[64:67],v[192:195],a[40:43],v[64:67]
ar v230,a208
ar v231,a212
mm v[64:67],v[196:199],a[68:71],v[64:67]
ld a[4:7],v244,s[44:47],s53 offen offset:1024 sc1
mm v[96:99],v[196:199],a[72:75],v[96:99]
v_perm_b32 v232, v231, v230, s86
v_perm_b32 v233, v231, v230, s87
mm v[96:99],v[192:195],a[44:47],v[96:99]
rt v[196:197],v240 offset:17024
rt v[198:199],v240 offset:18048
mm v[100:103],v[200:203],a[44:47],v[100:103]
aw a162,v232
aw a166,v233
mm v[100:103],v[204:207],a[72:75],v[100:103]
ld a[8:11],v245,s[44:47],s53 offen offset:1024 sc1
mm v[68:71],v[204:207],a[68:71],v[68:71]
ar v234,a216
ar v235,a220
mm v[68:71],v[200:203],a[40:43],v[68:71]
rt v[192:193],v240 offset:25472
rt v[194:195],v240 offset:26496
mm v[36:39],v[200:203],a[36:39],v[36:39]
v_perm_b32 v236, v235, v234, s86
v_perm_b32 v237, v235, v234, s87
mm v[36:39],v[204:207],a[64:67],v[36:39]
ld a[16:19],v246,s[44:47],s53 offen offset:1024 sc1
mm v[4:7],v[204:207],a[48:51],v[4:7]
wt lgkmcnt(4)
aw a163,v236
mm v[4:7],v[200:203],a[32:35],v[4:7]
rt v[204:205],v241 offset:17024
rt v[206:207],v241 offset:18048
mm v[8:11],v[208:211],a[32:35],v[8:11]
aw a167,v237
ar v230,a193
mm v[8:11],v[212:215],a[48:51],v[8:11]
ld a[20:23],v247,s[44:47],s53 offen offset:1024 sc1
mm v[40:43],v[212:215],a[64:67],v[40:43]
ar v231,a197
v_perm_b32 v232, v231, v230, s86
mm v[40:43],v[208:211],a[36:39],v[40:43]
rt v[200:201],v241 offset:25472
rt v[202:203],v241 offset:26496
mm v[72:75],v[208:211],a[40:43],v[72:75]
v_perm_b32 v233, v231, v230, s87
aw a168,v232
mm v[72:75],v[212:215],a[68:71],v[72:75]
aw a172,v233
ar v234,a201
mm v[104:107],v[212:215],a[72:75],v[104:107]
ar v235,a205
v_perm_b32 v236, v235, v234, s86
mm v[104:107],v[208:211],a[44:47],v[104:107]
rt v[212:213],v242 offset:17024
rt v[214:215],v242 offset:18048
mm v[108:111],v[216:219],a[44:47],v[108:111]
v_perm_b32 v237, v235, v234, s87
aw a169,v236
mm v[108:111],v[220:223],a[72:75],v[108:111]
aw a173,v237
ar v230,a209
mm v[76:79],v[220:223],a[68:71],v[76:79]
ar v231,a213
v_perm_b32 v232, v231, v230, s86
mm v[76:79],v[216:219],a[40:43],v[76:79]
rt v[208:209],v242 offset:25472
rt v[210:211],v242 offset:26496
mm v[44:47],v[216:219],a[36:39],v[44:47]
v_perm_b32 v233, v231, v230, s87
aw a170,v232
)ASM" R"ASM(mm v[44:47],v[220:223],a[64:67],v[44:47]
aw a174,v233
ar v234,a217
mm v[12:15],v[220:223],a[48:51],v[12:15]
wt lgkmcnt(4)
ar v235,a221
mm v[12:15],v[216:219],a[32:35],v[12:15]
rt v[220:221],v243 offset:17024
rt v[222:223],v243 offset:18048
mm v[16:19],v[196:199],a[32:35],v[16:19]
v_perm_b32 v236, v235, v234, s86
v_perm_b32 v237, v235, v234, s87
mm v[16:19],v[192:195],a[48:51],v[16:19]
ld a[24:27],v244,s[44:47],s53 offen offset:2048 sc1
mm v[48:51],v[192:195],a[64:67],v[48:51]
aw a171,v236
aw a175,v237
mm v[48:51],v[196:199],a[36:39],v[48:51]
rt v[216:217],v243 offset:25472
rt v[218:219],v243 offset:26496
mm v[80:83],v[196:199],a[40:43],v[80:83]
ar v230,a194
ar v231,a198
mm v[80:83],v[192:195],a[68:71],v[80:83]
ld a[28:31],v245,s[44:47],s53 offen offset:2048 sc1
mm v[112:115],v[192:195],a[72:75],v[112:115]
v_perm_b32 v232, v231, v230, s86
v_perm_b32 v233, v231, v230, s87
mm v[112:115],v[196:199],a[44:47],v[112:115]
rt v[192:193],v240 offset:33792
rt v[194:195],v240 offset:34816
mm v[116:119],v[204:207],a[44:47],v[116:119]
aw a176,v232
aw a180,v233
mm v[116:119],v[200:203],a[72:75],v[116:119]
ld a[60:63],v246,s[44:47],s53 offen offset:2048 sc1
mm v[84:87],v[200:203],a[68:71],v[84:87]
ar v234,a202
ar v235,a206
mm v[84:87],v[204:207],a[40:43],v[84:87]
rt v[196:197],v240 offset:42240
rt v[198:199],v240 offset:43264
mm v[52:55],v[204:207],a[36:39],v[52:55]
v_perm_b32 v236, v235, v234, s86
v_perm_b32 v237, v235, v234, s87
mm v[52:55],v[200:203],a[64:67],v[52:55]
ld v[128:131],v247,s[44:47],s53 offen offset:2048 sc1
mm v[20:23],v[200:203],a[48:51],v[20:23]
wt lgkmcnt(4)
aw a177,v236
mm v[20:23],v[204:207],a[32:35],v[20:23]
rt v[200:201],v241 offset:33792
rt v[202:203],v241 offset:34816
mm v[24:27],v[212:215],a[32:35],v[24:27]
aw a181,v237
ar v230,a210
mm v[24:27],v[208:211],a[48:51],v[24:27]
ld v[132:135],v244,s[44:47],s53 offen offset:3072 sc1
mm v[56:59],v[208:211],a[64:67],v[56:59]
ar v231,a214
v_perm_b32 v232, v231, v230, s86
mm v[56:59],v[212:215],a[36:39],v[56:59]
rt v[204:205],v241 offset:42240
rt v[206:207],v241 offset:43264
mm v[88:91],v[212:215],a[40:43],v[88:91]
v_perm_b32 v233, v231, v230, s87
aw a178,v232
mm v[88:91],v[208:211],a[68:71],v[88:91]
ld v[144:147],v245,s[44:47],s53 offen offset:3072 sc1
mm v[120:123],v[208:211],a[72:75],v[120:123]
aw a182,v233
ar v234,a218
mm v[120:123],v[212:215],a[44:47],v[120:123]
rt v[208:209],v242 offset:33792
rt v[210:211],v242 offset:34816
mm v[124:127],v[220:223],a[44:47],v[124:127]
ar v235,a222
v_perm_b32 v236, v235, v234, s86
mm v[124:127],v[216:219],a[72:75],v[124:127]
ld a[44:47],v246,s[44:47],s53 offen offset:3072 sc1
mm v[92:95],v[216:219],a[68:71],v[92:95]
v_perm_b32 v237, v235, v234, s87
aw a179,v236
mm v[92:95],v[220:223],a[40:43],v[92:95]
rt v[212:213],v242 offset:42240
rt v[214:215],v242 offset:43264
mm v[60:63],v[220:223],a[36:39],v[60:63]
aw a183,v237
ar v230,a195
mm v[60:63],v[216:219],a[64:67],v[60:63]
wt vmcnt(20)
v_cmp_eq_u64 vcc, v[250:251], v[252:253]
mm v[28:31],v[216:219],a[48:51],v[28:31]
wt lgkmcnt(4)
ar v231,a199
mm v[28:31],v[220:223],a[32:35],v[28:31]
rt v[216:217],v243 offset:33792
rt v[218:219],v243 offset:34816
mm v[0:3],v[192:195],v[136:139],v[0:3]
v_perm_b32 v232, v231, v230, s86
)ASM" R"ASM(v_perm_b32 v233, v231, v230, s87
mm v[0:3],v[196:199],v[168:171],v[0:3]
ld a[32:35],v247,s[44:47],s53 offen offset:3072 sc1
mm v[32:35],v[196:199],v[172:175],v[32:35]
aw a184,v232
aw a188,v233
mm v[32:35],v[192:195],v[140:143],v[32:35]
rt v[220:221],v243 offset:42240
rt v[222:223],v243 offset:43264
mm v[64:67],v[192:195],v[152:155],v[64:67]
ar v234,a203
ar v235,a207
mm v[64:67],v[196:199],v[184:187],v[64:67]
v_perm_b32 v236, v235, v234, s86
v_perm_b32 v237, v235, v234, s87
mm v[96:99],v[196:199],v[188:191],v[96:99]
aw a185,v236
aw a189,v237
mm v[96:99],v[192:195],v[156:159],v[96:99]
rt v[196:197],v240 offset:33920
rt v[198:199],v240 offset:34944
mm v[100:103],v[200:203],v[156:159],v[100:103]
ar v230,a211
ar v231,a215
mm v[100:103],v[204:207],v[188:191],v[100:103]
buffer_load_dwordx2 v[250:251], v248, s[64:67], s5 offen sc1
mm v[68:71],v[204:207],v[184:187],v[68:71]
v_perm_b32 v232, v231, v230, s86
v_perm_b32 v233, v231, v230, s87
mm v[68:71],v[200:203],v[152:155],v[68:71]
rt v[192:193],v240 offset:42368
rt v[194:195],v240 offset:43392
mm v[36:39],v[200:203],v[140:143],v[36:39]
aw a186,v232
aw a190,v233
mm v[36:39],v[204:207],v[172:175],v[36:39]
ar v234,a219
ar v235,a223
mm v[4:7],v[204:207],v[168:171],v[4:7]
wt lgkmcnt(4)
v_perm_b32 v236, v235, v234, s86
mm v[4:7],v[200:203],v[136:139],v[4:7]
rt v[204:205],v241 offset:33920
rt v[206:207],v241 offset:34944
mm v[8:11],v[208:211],v[136:139],v[8:11]
v_perm_b32 v237, v235, v234, s87
aw a187,v236
mm v[8:11],v[212:215],v[168:171],v[8:11]
aw a191,v237
ar v230,a224
mm v[40:43],v[212:215],v[172:175],v[40:43]
ar v231,a228
v_perm_b32 v232, v231, v230, s86
mm v[40:43],v[208:211],v[140:143],v[40:43]
rt v[200:201],v241 offset:42368
rt v[202:203],v241 offset:43392
mm v[72:75],v[208:211],v[152:155],v[72:75]
v_perm_b32 v233, v231, v230, s87
aw a192,v232
mm v[72:75],v[212:215],v[184:187],v[72:75]
aw a196,v233
ar v234,a232
mm v[104:107],v[212:215],v[188:191],v[104:107]
ar v235,a236
v_perm_b32 v236, v235, v234, s86
mm v[104:107],v[208:211],v[156:159],v[104:107]
rt v[212:213],v242 offset:33920
rt v[214:215],v242 offset:34944
mm v[108:111],v[216:219],v[156:159],v[108:111]
v_perm_b32 v237, v235, v234, s87
aw a193,v236
mm v[108:111],v[220:223],v[188:191],v[108:111]
aw a197,v237
ar v230,a240
mm v[76:79],v[220:223],v[184:187],v[76:79]
ar v231,a244
v_perm_b32 v232, v231, v230, s86
mm v[76:79],v[216:219],v[152:155],v[76:79]
rt v[208:209],v242 offset:42368
rt v[210:211],v242 offset:43392
mm v[44:47],v[216:219],v[140:143],v[44:47]
s_bitcmp1_b32 vcc_lo, 13
s_nop 0
s_nop 0
s_cbranch_scc0 .La5_LO_NB13
.La5_LO_KB13:
mm v[44:47],v[220:223],v[172:175],v[44:47]
v_perm_b32 v233, v231, v230, s87
aw a194,v232
mm v[12:15],v[220:223],v[168:171],v[12:15]
wt lgkmcnt(4)
aw a198,v233
mm v[12:15],v[216:219],v[136:139],v[12:15]
rt v[220:221],v243 offset:33920
rt v[222:223],v243 offset:34944
mm v[16:19],v[196:199],v[136:139],v[16:19]
ar v234,a248
ar v235,a252
mm v[16:19],v[192:195],v[168:171],v[16:19]
ld a[36:39],v244,s[44:47],s54 offen offset:2048 sc1
)ASM" R"ASM(mm v[48:51],v[192:195],v[172:175],v[48:51]
v_perm_b32 v236, v235, v234, s86
v_perm_b32 v237, v235, v234, s87
mm v[48:51],v[196:199],v[140:143],v[48:51]
rt v[216:217],v243 offset:42368
rt v[218:219],v243 offset:43392
mm v[80:83],v[196:199],v[152:155],v[80:83]
aw a195,v236
aw a199,v237
mm v[80:83],v[192:195],v[184:187],v[80:83]
ld a[40:43],v245,s[44:47],s54 offen offset:2048 sc1
mm v[112:115],v[192:195],v[188:191],v[112:115]
wt vmcnt(19)
s_barrier
mm v[112:115],v[196:199],v[156:159],v[112:115]
rt v[192:193],v240 offset:50688
rt v[194:195],v240 offset:51712
mm v[116:119],v[204:207],v[156:159],v[116:119]
s_add_u32 m0, s83, 16896
ar v230,a225
mm v[116:119],v[200:203],v[188:191],v[116:119]
ld v224,s[56:59],s80 offen lds
mm v[84:87],v[200:203],v[184:187],v[84:87]
ar v231,a229
v_perm_b32 v232, v231, v230, s86
mm v[84:87],v[204:207],v[152:155],v[84:87]
rt v[196:197],v240 offset:59136
rt v[198:199],v240 offset:60160
mm v[52:55],v[204:207],v[140:143],v[52:55]
s_add_u32 m0, s83, 17920
v_perm_b32 v233, v231, v230, s87
mm v[52:55],v[200:203],v[172:175],v[52:55]
ld v225,s[56:59],s80 offen lds
mm v[20:23],v[200:203],v[168:171],v[20:23]
wt lgkmcnt(4)
aw a200,v232
mm v[20:23],v[204:207],v[136:139],v[20:23]
rt v[200:201],v241 offset:50688
rt v[202:203],v241 offset:51712
mm v[24:27],v[212:215],v[136:139],v[24:27]
s_add_u32 m0, s83, 19072
aw a204,v233
mm v[24:27],v[208:211],v[168:171],v[24:27]
ld v226,s[56:59],s80 offen lds
mm v[56:59],v[208:211],v[172:175],v[56:59]
ar v234,a233
ar v235,a237
mm v[56:59],v[212:215],v[140:143],v[56:59]
rt v[204:205],v241 offset:59136
rt v[206:207],v241 offset:60160
mm v[88:91],v[212:215],v[152:155],v[88:91]
s_add_u32 m0, s83, 20096
v_perm_b32 v236, v235, v234, s86
mm v[88:91],v[208:211],v[184:187],v[88:91]
ld v227,s[56:59],s80 offen lds
mm v[120:123],v[208:211],v[188:191],v[120:123]
s_bfe_u32 s80, s4, 262168
s_xor_b32 s80, s80, s72
mm v[120:123],v[212:215],v[156:159],v[120:123]
rt v[208:209],v242 offset:50688
rt v[210:211],v242 offset:51712
mm v[124:127],v[220:223],v[156:159],v[124:127]
s_add_u32 m0, s83, 118272
s_lshl_b32 s80, s80, 18
mm v[124:127],v[216:219],v[188:191],v[124:127]
ld v224,s[56:59],s80 offen lds
mm v[92:95],v[216:219],v[184:187],v[92:95]
v_perm_b32 v237, v235, v234, s87
aw a201,v236
mm v[92:95],v[220:223],v[152:155],v[92:95]
rt v[212:213],v242 offset:59136
rt v[214:215],v242 offset:60160
mm v[60:63],v[220:223],v[140:143],v[60:63]
aw a205,v237
ar v230,a241
mm v[60:63],v[216:219],v[172:175],v[60:63]
ar v231,a245
v_perm_b32 v232, v231, v230, s86
mm v[28:31],v[216:219],v[168:171],v[28:31]
wt vmcnt(16) lgkmcnt(4)
v_perm_b32 v233, v231, v230, s87
mm v[28:31],v[220:223],v[136:139],v[28:31]
rt v[216:217],v243 offset:50688
rt v[218:219],v243 offset:51712
mm v[0:3],v[192:195],a[52:55],v[0:3]
s_add_u32 m0, s83, 119296
aw a202,v232
mm v[0:3],v[196:199],a[4:7],v[0:3]
ld v225,s[56:59],s80 offen lds
mm v[32:35],v[196:199],a[8:11],v[32:35]
aw a206,v233
ar v234,a249
mm v[32:35],v[192:195],a[56:59],v[32:35]
rt v[220:221],v243 offset:59136
rt v[222:223],v243 offset:60160
mm v[64:67],v[192:195],a[12:15],v[64:67]
s_add_u32 m0, s83, 120448
ar v235,a253
mm v[64:67],v[196:199],a[16:19],v[64:67]
ld v226,s[56:59],s80 offen lds
)ASM" R"ASM(mm v[96:99],v[196:199],a[20:23],v[96:99]
v_perm_b32 v236, v235, v234, s86
v_perm_b32 v237, v235, v234, s87
mm v[96:99],v[192:195],a[0:3],v[96:99]
rt v[196:197],v240 offset:50816
rt v[198:199],v240 offset:51840
mm v[100:103],v[200:203],a[0:3],v[100:103]
aw a203,v236
aw a207,v237
mm v[100:103],v[204:207],a[20:23],v[100:103]
ld v[136:139],v246,s[44:47],s54 offen offset:2048 sc1
mm v[68:71],v[204:207],a[16:19],v[68:71]
ar v230,a226
ar v231,a230
mm v[68:71],v[200:203],a[12:15],v[68:71]
rt v[192:193],v240 offset:59264
rt v[194:195],v240 offset:60288
mm v[36:39],v[200:203],a[56:59],v[36:39]
v_perm_b32 v232, v231, v230, s86
v_perm_b32 v233, v231, v230, s87
mm v[36:39],v[204:207],a[8:11],v[36:39]
ld v[140:143],v247,s[44:47],s54 offen offset:2048 sc1
mm v[4:7],v[204:207],a[4:7],v[4:7]
wt lgkmcnt(4)
aw a208,v232
mm v[4:7],v[200:203],a[52:55],v[4:7]
rt v[204:205],v241 offset:50816
rt v[206:207],v241 offset:51840
mm v[8:11],v[208:211],a[52:55],v[8:11]
aw a212,v233
ar v234,a234
mm v[8:11],v[212:215],a[4:7],v[8:11]
ld v[152:155],v244,s[44:47],s54 offen offset:3072 sc1
mm v[40:43],v[212:215],a[8:11],v[40:43]
ar v235,a238
v_perm_b32 v236, v235, v234, s86
mm v[40:43],v[208:211],a[56:59],v[40:43]
rt v[200:201],v241 offset:59264
rt v[202:203],v241 offset:60288
mm v[72:75],v[208:211],a[12:15],v[72:75]
v_perm_b32 v237, v235, v234, s87
aw a209,v236
mm v[72:75],v[212:215],a[16:19],v[72:75]
ld v[156:159],v245,s[44:47],s54 offen offset:3072 sc1
mm v[104:107],v[212:215],a[20:23],v[104:107]
aw a213,v237
ar v230,a242
mm v[104:107],v[208:211],a[0:3],v[104:107]
rt v[212:213],v242 offset:50816
rt v[214:215],v242 offset:51840
mm v[108:111],v[216:219],a[0:3],v[108:111]
ar v231,a246
v_perm_b32 v232, v231, v230, s86
mm v[108:111],v[220:223],a[20:23],v[108:111]
ld v[168:171],v246,s[44:47],s54 offen offset:3072 sc1
mm v[76:79],v[220:223],a[16:19],v[76:79]
v_perm_b32 v233, v231, v230, s87
aw a210,v232
mm v[76:79],v[216:219],a[12:15],v[76:79]
rt v[208:209],v242 offset:59264
rt v[210:211],v242 offset:60288
mm v[44:47],v[216:219],a[56:59],v[44:47]
aw a214,v233
ar v234,a250
mm v[44:47],v[220:223],a[8:11],v[44:47]
ar v235,a254
v_perm_b32 v236, v235, v234, s86
mm v[12:15],v[220:223],a[4:7],v[12:15]
wt lgkmcnt(4)
v_perm_b32 v237, v235, v234, s87
mm v[12:15],v[216:219],a[52:55],v[12:15]
rt v[220:221],v243 offset:50816
rt v[222:223],v243 offset:51840
mm v[16:19],v[196:199],a[52:55],v[16:19]
aw a211,v236
aw a215,v237
mm v[16:19],v[192:195],a[4:7],v[16:19]
ld v[172:175],v247,s[44:47],s54 offen offset:3072 sc1
mm v[48:51],v[192:195],a[8:11],v[48:51]
ar v230,a227
ar v231,a231
mm v[48:51],v[196:199],a[56:59],v[48:51]
rt v[216:217],v243 offset:59264
rt v[218:219],v243 offset:60288
mm v[80:83],v[196:199],a[12:15],v[80:83]
s_add_u32 m0, s83, 121472
v_perm_b32 v232, v231, v230, s86
mm v[80:83],v[192:195],a[16:19],v[80:83]
ld v227,s[56:59],s80 offen lds
mm v[112:115],v[192:195],a[20:23],v[112:115]
s_add_u32 s80, s80, 131072
v_perm_b32 v233, v231, v230, s87
mm v[112:115],v[196:199],a[0:3],v[112:115]
rt v[192:193],v240 offset:0
rt v[194:195],v240 offset:1024
mm v[116:119],v[204:207],a[0:3],v[116:119]
s_add_u32 m0, s83, 67584
aw a216,v232
mm v[116:119],v[200:203],a[20:23],v[116:119]
ld v224,s[56:59],s80 offen lds
)ASM" R"ASM(mm v[84:87],v[200:203],a[16:19],v[84:87]
aw a220,v233
ar v234,a235
mm v[84:87],v[204:207],a[12:15],v[84:87]
rt v[196:197],v240 offset:8448
rt v[198:199],v240 offset:9472
mm v[52:55],v[204:207],a[56:59],v[52:55]
s_add_u32 m0, s83, 68608
ar v235,a239
mm v[52:55],v[200:203],a[8:11],v[52:55]
ld v225,s[56:59],s80 offen lds
mm v[20:23],v[200:203],a[4:7],v[20:23]
wt lgkmcnt(4)
v_perm_b32 v236, v235, v234, s86
mm v[20:23],v[204:207],a[52:55],v[20:23]
rt v[200:201],v241 offset:0
rt v[202:203],v241 offset:1024
mm v[24:27],v[212:215],a[52:55],v[24:27]
s_add_u32 m0, s83, 69760
v_perm_b32 v237, v235, v234, s87
mm v[24:27],v[208:211],a[4:7],v[24:27]
ld v226,s[56:59],s80 offen lds
wt vmcnt(19)
v_cmp_eq_u64 vcc, v[250:251], v[252:253]
mm v[56:59],v[208:211],a[8:11],v[56:59]
aw a217,v236
aw a221,v237
mm v[56:59],v[212:215],a[56:59],v[56:59]
rt v[204:205],v241 offset:8448
rt v[206:207],v241 offset:9472
mm v[88:91],v[212:215],a[12:15],v[88:91]
s_add_u32 m0, s83, 70784
ar v230,a243
mm v[88:91],v[208:211],a[16:19],v[88:91]
ld v227,s[56:59],s80 offen lds
mm v[120:123],v[208:211],a[20:23],v[120:123]
s_bfe_u32 s80, s4, 262172
s_xor_b32 s80, s80, s72
mm v[120:123],v[212:215],a[0:3],v[120:123]
rt v[208:209],v242 offset:0
rt v[210:211],v242 offset:1024
mm v[124:127],v[220:223],a[0:3],v[124:127]
s_lshl_b32 s80, s80, 18
ar v231,a247
mm v[124:127],v[216:219],a[20:23],v[124:127]
buffer_load_dwordx2 v[250:251], v248, s[64:67], s5 offen sc1
mm v[92:95],v[216:219],a[16:19],v[92:95]
v_perm_b32 v232, v231, v230, s86
v_perm_b32 v233, v231, v230, s87
mm v[92:95],v[220:223],a[12:15],v[92:95]
rt v[212:213],v242 offset:8448
rt v[214:215],v242 offset:9472
mm v[60:63],v[220:223],a[56:59],v[60:63]
.La5_LW3_3:
s_cbranch_execz .La5_LA_RETURN
s_add_u32 m0, s83, 84480
aw a218,v232
mm v[60:63],v[216:219],a[8:11],v[60:63]
ld v224,s[56:59],s80 offen lds
mm v[28:31],v[216:219],a[4:7],v[28:31]
wt lgkmcnt(4)
aw a222,v233
mm v[28:31],v[220:223],a[52:55],v[28:31]
rt v[216:217],v243 offset:0
rt v[218:219],v243 offset:1024
mm v[0:3],v[192:195],a[24:27],v[0:3]
s_add_u32 m0, s83, 85504
ar v234,a251
mm v[0:3],v[196:199],v[132:135],v[0:3]
ld v225,s[56:59],s80 offen lds
mm v[32:35],v[196:199],v[144:147],v[32:35]
ar v235,a255
v_perm_b32 v236, v235, v234, s86
mm v[32:35],v[192:195],a[28:31],v[32:35]
rt v[220:221],v243 offset:8448
rt v[222:223],v243 offset:9472
mm v[64:67],v[192:195],a[60:63],v[64:67]
s_add_u32 m0, s83, 86656
v_perm_b32 v237, v235, v234, s87
mm v[64:67],v[196:199],a[44:47],v[64:67]
ld v226,s[56:59],s80 offen lds
mm v[96:99],v[196:199],a[32:35],v[96:99]
aw a219,v236
aw a223,v237
mm v[96:99],v[192:195],v[128:131],v[96:99]
rt v[196:197],v240 offset:128
rt v[198:199],v240 offset:1152
mm v[100:103],v[200:203],v[128:131],v[100:103]
s_add_u32 m0, s83, 87680
mm v[100:103],v[204:207],a[32:35],v[100:103]
ld v227,s[56:59],s80 offen lds
mm v[68:71],v[204:207],a[44:47],v[68:71]
s_add_u32 s80, s80, 131072
mm v[68:71],v[200:203],a[60:63],v[68:71]
rt v[192:193],v240 offset:8576
rt v[194:195],v240 offset:9600
mm v[36:39],v[200:203],a[28:31],v[36:39]
s_add_u32 m0, s83, 101376
mm v[36:39],v[204:207],v[144:147],v[36:39]
ld v224,s[56:59],s80 offen lds
)ASM" R"ASM(mm v[4:7],v[204:207],v[132:135],v[4:7]
wt lgkmcnt(4)
mm v[4:7],v[200:203],a[24:27],v[4:7]
rt v[204:205],v241 offset:128
rt v[206:207],v241 offset:1152
mm v[8:11],v[208:211],a[24:27],v[8:11]
s_add_u32 m0, s83, 102400
mm v[8:11],v[212:215],v[132:135],v[8:11]
ld v225,s[56:59],s80 offen lds
mm v[40:43],v[212:215],v[144:147],v[40:43]
mm v[40:43],v[208:211],a[28:31],v[40:43]
rt v[200:201],v241 offset:8576
rt v[202:203],v241 offset:9600
mm v[72:75],v[208:211],a[60:63],v[72:75]
s_add_u32 m0, s83, 103552
mm v[72:75],v[212:215],a[44:47],v[72:75]
ld v226,s[56:59],s80 offen lds
mm v[104:107],v[212:215],a[32:35],v[104:107]
mm v[104:107],v[208:211],v[128:131],v[104:107]
rt v[212:213],v242 offset:128
rt v[214:215],v242 offset:1152
mm v[108:111],v[216:219],v[128:131],v[108:111]
s_add_u32 m0, s83, 104576
mm v[108:111],v[220:223],a[32:35],v[108:111]
ld v227,s[56:59],s80 offen lds
mm v[76:79],v[220:223],a[44:47],v[76:79]
v_bfe_u32 v224, v255, 6, 1
v_lshrrev_b32 v225, 7, v255
mm v[76:79],v[216:219],a[60:63],v[76:79]
rt v[208:209],v242 offset:8576
rt v[210:211],v242 offset:9600
mm v[44:47],v[216:219],a[28:31],v[44:47]
s_bitcmp1_b32 vcc_lo, 6
s_cbranch_scc0 .La5_LO_NB6
.La5_LO_KB6:
mm v[44:47],v[220:223],v[144:147],v[44:47]
v_and_b32 v226, 48, v255
v_lshl_add_u32 v225, v225, 6, v226
mm v[12:15],v[220:223],v[132:135],v[12:15]
wt lgkmcnt(4)
v_lshl_add_u32 v224, v224, 11, v225
mm v[12:15],v[216:219],a[24:27],v[12:15]
rt v[220:221],v243 offset:128
rt v[222:223],v243 offset:1152
mm v[16:19],v[196:199],a[24:27],v[16:19]
mm v[16:19],v[192:195],v[132:135],v[16:19]
ld a[64:67],v244,s[44:47],s55 offen offset:0 sc1
mm v[48:51],v[192:195],v[144:147],v[48:51]
mm v[48:51],v[196:199],a[28:31],v[48:51]
rt v[216:217],v243 offset:8576
rt v[218:219],v243 offset:9600
mm v[80:83],v[196:199],a[60:63],v[80:83]
mm v[80:83],v[192:195],a[44:47],v[80:83]
ld a[68:71],v245,s[44:47],s55 offen offset:0 sc1
mm v[112:115],v[192:195],a[32:35],v[112:115]
wt vmcnt(25)
s_barrier
mm v[112:115],v[196:199],v[128:131],v[112:115]
rt v[192:193],v240 offset:16896
rt v[194:195],v240 offset:17920
mm v[116:119],v[204:207],v[128:131],v[116:119]
mm v[116:119],v[200:203],a[32:35],v[116:119]
ld a[72:75],v244,s[44:47],s55 offen offset:1024 sc1
mm v[84:87],v[200:203],a[44:47],v[84:87]
mm v[84:87],v[204:207],a[60:63],v[84:87]
rt v[196:197],v240 offset:25344
rt v[198:199],v240 offset:26368
mm v[52:55],v[204:207],a[28:31],v[52:55]
mm v[52:55],v[200:203],v[144:147],v[52:55]
ld a[76:79],v245,s[44:47],s55 offen offset:1024 sc1
mm v[20:23],v[200:203],v[132:135],v[20:23]
wt lgkmcnt(4)
mm v[20:23],v[204:207],a[24:27],v[20:23]
rt v[200:201],v241 offset:16896
rt v[202:203],v241 offset:17920
mm v[24:27],v[212:215],a[24:27],v[24:27]
mm v[24:27],v[208:211],v[132:135],v[24:27]
ld a[80:83],v244,s[44:47],s55 offen offset:2048 sc1
mm v[56:59],v[208:211],v[144:147],v[56:59]
mm v[56:59],v[212:215],a[28:31],v[56:59]
rt v[204:205],v241 offset:25344
rt v[206:207],v241 offset:26368
mm v[88:91],v[212:215],a[60:63],v[88:91]
mm v[88:91],v[208:211],a[44:47],v[88:91]
ld a[84:87],v245,s[44:47],s55 offen offset:2048 sc1
mm v[120:123],v[208:211],a[32:35],v[120:123]
mm v[120:123],v[212:215],v[128:131],v[120:123]
rt v[208:209],v242 offset:16896
rt v[210:211],v242 offset:17920
mm v[124:127],v[220:223],v[128:131],v[124:127]
mm v[124:127],v[216:219],a[32:35],v[124:127]
ld a[88:91],v244,s[44:47],s55 offen offset:3072 sc1
mm v[92:95],v[216:219],a[44:47],v[92:95]
mm v[92:95],v[220:223],a[60:63],v[92:95]
rt v[212:213],v242 offset:25344
rt v[214:215],v242 offset:26368
mm v[60:63],v[220:223],a[28:31],v[60:63]
mm v[60:63],v[216:219],v[144:147],v[60:63]
mm v[28:31],v[216:219],v[132:135],v[28:31]
wt vmcnt(21) lgkmcnt(4)
)ASM" R"ASM(mm v[28:31],v[220:223],a[24:27],v[28:31]
rt v[216:217],v243 offset:16896
rt v[218:219],v243 offset:17920
mm v[0:3],v[192:195],a[36:39],v[0:3]
s_bitcmp1_b32 vcc_lo, 7
s_cbranch_scc0 .La5_LO_NB7
.La5_LO_KB7:
mm v[0:3],v[196:199],v[152:155],v[0:3]
ld a[92:95],v245,s[44:47],s55 offen offset:3072 sc1
mm v[32:35],v[196:199],v[156:159],v[32:35]
mm v[32:35],v[192:195],a[40:43],v[32:35]
rt v[220:221],v243 offset:25344
rt v[222:223],v243 offset:26368
mm v[64:67],v[192:195],v[136:139],v[64:67]
mm v[64:67],v[196:199],v[168:171],v[64:67]
ld a[0:3],v244,s[44:47],s52 offen offset:0 sc1
mm v[96:99],v[196:199],v[172:175],v[96:99]
mm v[96:99],v[192:195],v[140:143],v[96:99]
rt v[196:197],v240 offset:17024
rt v[198:199],v240 offset:18048
mm v[100:103],v[200:203],v[140:143],v[100:103]
mm v[100:103],v[204:207],v[172:175],v[100:103]
ld a[4:7],v245,s[44:47],s52 offen offset:0 sc1
mm v[68:71],v[204:207],v[168:171],v[68:71]
mm v[68:71],v[200:203],v[136:139],v[68:71]
rt v[192:193],v240 offset:25472
rt v[194:195],v240 offset:26496
mm v[36:39],v[200:203],a[40:43],v[36:39]
v_add_u32 v240, 67584, v240
mm v[36:39],v[204:207],v[156:159],v[36:39]
ld a[8:11],v244,s[44:47],s52 offen offset:1024 sc1
mm v[4:7],v[204:207],v[152:155],v[4:7]
wt lgkmcnt(4)
mm v[4:7],v[200:203],a[36:39],v[4:7]
rt v[204:205],v241 offset:17024
rt v[206:207],v241 offset:18048
mm v[8:11],v[208:211],a[36:39],v[8:11]
mm v[8:11],v[212:215],v[152:155],v[8:11]
ld a[12:15],v245,s[44:47],s52 offen offset:1024 sc1
mm v[40:43],v[212:215],v[156:159],v[40:43]
mm v[40:43],v[208:211],a[40:43],v[40:43]
rt v[200:201],v241 offset:25472
rt v[202:203],v241 offset:26496
mm v[72:75],v[208:211],v[136:139],v[72:75]
v_add_u32 v241, 67584, v241
mm v[72:75],v[212:215],v[168:171],v[72:75]
ld a[16:19],v244,s[44:47],s52 offen offset:2048 sc1
mm v[104:107],v[212:215],v[172:175],v[104:107]
mm v[104:107],v[208:211],v[140:143],v[104:107]
rt v[212:213],v242 offset:17024
rt v[214:215],v242 offset:18048
mm v[108:111],v[216:219],v[140:143],v[108:111]
mm v[108:111],v[220:223],v[172:175],v[108:111]
ld a[20:23],v245,s[44:47],s52 offen offset:2048 sc1
mm v[76:79],v[220:223],v[168:171],v[76:79]
mm v[76:79],v[216:219],v[136:139],v[76:79]
rt v[208:209],v242 offset:25472
rt v[210:211],v242 offset:26496
mm v[44:47],v[216:219],a[40:43],v[44:47]
v_add_u32 v242, 67584, v242
mm v[44:47],v[220:223],v[156:159],v[44:47]
wt vmcnt(22)
v_cmp_eq_u64 vcc, v[250:251], v[252:253]
mm v[12:15],v[220:223],v[152:155],v[12:15]
wt lgkmcnt(4)
mm v[12:15],v[216:219],a[36:39],v[12:15]
rt v[220:221],v243 offset:17024
rt v[222:223],v243 offset:18048
mm v[16:19],v[196:199],a[36:39],v[16:19]
mm v[16:19],v[192:195],v[152:155],v[16:19]
ld a[24:27],v244,s[44:47],s52 offen offset:3072 sc1
mm v[48:51],v[192:195],v[156:159],v[48:51]
mm v[48:51],v[196:199],a[40:43],v[48:51]
rt v[216:217],v243 offset:25472
rt v[218:219],v243 offset:26496
mm v[80:83],v[196:199],v[136:139],v[80:83]
v_add_u32 v243, 67584, v243
mm v[80:83],v[192:195],v[168:171],v[80:83]
ld a[28:31],v245,s[44:47],s52 offen offset:3072 sc1
mm v[112:115],v[192:195],v[172:175],v[112:115]
wt vmcnt(16)
s_barrier
mm v[112:115],v[196:199],v[140:143],v[112:115]
rt v[192:193],v240 offset:50688
rt v[194:195],v240 offset:51712
mm v[116:119],v[204:207],v[140:143],v[116:119]
mm v[116:119],v[200:203],v[172:175],v[116:119]
ld a[32:35],v246,s[44:47],s55 offen offset:0 sc1
mm v[84:87],v[200:203],v[168:171],v[84:87]
mm v[84:87],v[204:207],v[136:139],v[84:87]
rt v[196:197],v240 offset:59136
rt v[198:199],v240 offset:60160
mm v[52:55],v[204:207],a[40:43],v[52:55]
mm v[52:55],v[200:203],v[156:159],v[52:55]
ld a[44:47],v247,s[44:47],s55 offen offset:0 sc1
mm v[20:23],v[200:203],v[152:155],v[20:23]
wt lgkmcnt(4)
mm v[20:23],v[204:207],a[36:39],v[20:23]
rt v[200:201],v240 offset:0
rt v[202:203],v240 offset:1024
)ASM" R"ASM(mm v[24:27],v[212:215],a[36:39],v[24:27]
mm v[24:27],v[208:211],v[152:155],v[24:27]
ld a[48:51],v246,s[44:47],s55 offen offset:1024 sc1
mm v[56:59],v[208:211],v[156:159],v[56:59]
mm v[56:59],v[212:215],a[40:43],v[56:59]
rt v[204:205],v240 offset:8448
rt v[206:207],v240 offset:9472
mm v[88:91],v[212:215],v[136:139],v[88:91]
mm v[88:91],v[208:211],v[168:171],v[88:91]
ld a[52:55],v247,s[44:47],s55 offen offset:1024 sc1
mm v[120:123],v[208:211],v[172:175],v[120:123]
mm v[120:123],v[212:215],v[140:143],v[120:123]
rt v[208:209],v240 offset:16896
rt v[210:211],v240 offset:17920
mm v[124:127],v[220:223],v[140:143],v[124:127]
mm v[124:127],v[216:219],v[172:175],v[124:127]
ld a[56:59],v246,s[44:47],s55 offen offset:2048 sc1
mm v[92:95],v[216:219],v[168:171],v[92:95]
mm v[92:95],v[220:223],v[136:139],v[92:95]
rt v[212:213],v240 offset:25344
rt v[214:215],v240 offset:26368
mm v[60:63],v[220:223],a[40:43],v[60:63]
mm v[60:63],v[216:219],v[156:159],v[60:63]
mm v[28:31],v[216:219],v[152:155],v[28:31]
wt vmcnt(13) lgkmcnt(4)
mm v[28:31],v[220:223],a[36:39],v[28:31]
rt v[216:217],v240 offset:33792
rt v[218:219],v240 offset:34816
mm v[0:3],v[192:195],a[64:67],v[0:3]
ld a[36:39],v247,s[44:47],s55 offen offset:2048 sc1
mm v[0:3],v[196:199],a[72:75],v[0:3]
rt v[220:221],v240 offset:42240
rt v[222:223],v240 offset:43264
mm v[32:35],v[196:199],a[76:79],v[32:35]
mm v[32:35],v[192:195],a[68:71],v[32:35]
rt v[196:197],v241 offset:50688
rt v[198:199],v241 offset:51712
mm v[32:35],v[200:203],a[84:87],v[32:35]
ld a[40:43],v246,s[44:47],s55 offen offset:3072 sc1
mm v[32:35],v[204:207],a[92:95],v[32:35]
rt v[192:193],v241 offset:59136
rt v[194:195],v241 offset:60160
mm v[0:3],v[204:207],a[88:91],v[0:3]
wt vmcnt(7) lgkmcnt(4)
mm v[0:3],v[200:203],a[80:83],v[0:3]
rt v[204:205],v241 offset:0
rt v[206:207],v241 offset:1024
mm v[0:3],v[208:211],a[0:3],v[0:3]
ld a[60:63],v247,s[44:47],s55 offen offset:3072 sc1
mm v[0:3],v[212:215],a[8:11],v[0:3]
rt v[200:201],v241 offset:8448
rt v[202:203],v241 offset:9472
mm v[32:35],v[212:215],a[12:15],v[32:35]
mm v[32:35],v[208:211],a[4:7],v[32:35]
rt v[212:213],v241 offset:16896
rt v[214:215],v241 offset:17920
mm v[32:35],v[216:219],a[20:23],v[32:35]
ld v[128:131],v246,s[44:47],s52 offen offset:0 sc1
mm v[32:35],v[220:223],a[28:31],v[32:35]
rt v[208:209],v241 offset:25344
rt v[210:211],v241 offset:26368
mm v[0:3],v[220:223],a[24:27],v[0:3]
wt lgkmcnt(4)
mm v[0:3],v[216:219],a[16:19],v[0:3]
rt v[220:221],v241 offset:33792
rt v[222:223],v241 offset:34816
mm v[4:7],v[196:199],a[64:67],v[4:7]
ld v[132:135],v247,s[44:47],s52 offen offset:0 sc1
mm v[4:7],v[192:195],a[72:75],v[4:7]
rt v[216:217],v241 offset:42240
rt v[218:219],v241 offset:43264
mm v[36:39],v[192:195],a[76:79],v[36:39]
mm v[36:39],v[196:199],a[68:71],v[36:39]
rt v[192:193],v242 offset:50688
rt v[194:195],v242 offset:51712
mm v[36:39],v[204:207],a[84:87],v[36:39]
ld v[144:147],v246,s[44:47],s52 offen offset:1024 sc1
mm v[36:39],v[200:203],a[92:95],v[36:39]
rt v[196:197],v242 offset:59136
rt v[198:199],v242 offset:60160
mm v[4:7],v[200:203],a[88:91],v[4:7]
wt lgkmcnt(4)
mm v[4:7],v[204:207],a[80:83],v[4:7]
rt v[200:201],v242 offset:0
rt v[202:203],v242 offset:1024
mm v[4:7],v[212:215],a[0:3],v[4:7]
ld v[148:151],v247,s[44:47],s52 offen offset:1024 sc1
mm v[4:7],v[208:211],a[8:11],v[4:7]
rt v[204:205],v242 offset:8448
rt v[206:207],v242 offset:9472
mm v[36:39],v[208:211],a[12:15],v[36:39]
mm v[36:39],v[212:215],a[4:7],v[36:39]
rt v[208:209],v242 offset:16896
rt v[210:211],v242 offset:17920
mm v[36:39],v[220:223],a[20:23],v[36:39]
mm v[36:39],v[216:219],a[28:31],v[36:39]
rt v[212:213],v242 offset:25344
rt v[214:215],v242 offset:26368
mm v[4:7],v[216:219],a[24:27],v[4:7]
wt lgkmcnt(4)
)ASM" R"ASM(mm v[4:7],v[220:223],a[16:19],v[4:7]
rt v[216:217],v242 offset:33792
rt v[218:219],v242 offset:34816
mm v[8:11],v[192:195],a[64:67],v[8:11]
ld v[160:163],v246,s[44:47],s52 offen offset:2048 sc1
mm v[8:11],v[196:199],a[72:75],v[8:11]
rt v[220:221],v242 offset:42240
rt v[222:223],v242 offset:43264
mm v[40:43],v[196:199],a[76:79],v[40:43]
cv v234,v32,v33
cv v236,v36,v37
mm v[40:43],v[192:195],a[68:71],v[40:43]
rt v[196:197],v243 offset:50688
rt v[198:199],v243 offset:51712
mm v[40:43],v[200:203],a[84:87],v[40:43]
ld v[164:167],v247,s[44:47],s52 offen offset:2048 sc1
mm v[40:43],v[204:207],a[92:95],v[40:43]
rt v[192:193],v243 offset:59136
rt v[194:195],v243 offset:60160
mm v[8:11],v[204:207],a[88:91],v[8:11]
wt lgkmcnt(4)
mm v[8:11],v[200:203],a[80:83],v[8:11]
rt v[204:205],v243 offset:0
rt v[206:207],v243 offset:1024
mm v[8:11],v[208:211],a[0:3],v[8:11]
ld v[176:179],v246,s[44:47],s52 offen offset:3072 sc1
mm v[8:11],v[212:215],a[8:11],v[8:11]
rt v[200:201],v243 offset:8448
rt v[202:203],v243 offset:9472
mm v[40:43],v[212:215],a[12:15],v[40:43]
cv v235,v34,v35
cv v237,v38,v39
mm v[40:43],v[208:211],a[4:7],v[40:43]
rt v[212:213],v243 offset:16896
rt v[214:215],v243 offset:17920
mm v[40:43],v[216:219],a[20:23],v[40:43]
ld v[180:183],v247,s[44:47],s52 offen offset:3072 sc1
mm v[40:43],v[220:223],a[28:31],v[40:43]
rt v[208:209],v243 offset:25344
rt v[210:211],v243 offset:26368
mm v[8:11],v[220:223],a[24:27],v[8:11]
wt lgkmcnt(4)
v_bfe_u32 v250, v255, 6, 1
mm v[8:11],v[216:219],a[16:19],v[8:11]
rt v[220:221],v243 offset:33792
rt v[222:223],v243 offset:34816
mm v[12:15],v[196:199],a[64:67],v[12:15]
v_permlane16_swap_b32 v234, v236
v_permlane16_swap_b32 v235, v237
mm v[12:15],v[192:195],a[72:75],v[12:15]
rt v[216:217],v243 offset:42240
rt v[218:219],v243 offset:43264
mm v[44:47],v[192:195],a[76:79],v[44:47]
cv v230,v0,v1
cv v232,v4,v5
mm v[44:47],v[196:199],a[68:71],v[44:47]
rt v[192:193],v240 offset:50816
rt v[194:195],v240 offset:51840
mm v[44:47],v[204:207],a[84:87],v[44:47]
st v[234:237],v238,s[32:35],s95 offen offset:0 sc1
mm v[44:47],v[200:203],a[92:95],v[44:47]
rt v[196:197],v240 offset:59264
rt v[198:199],v240 offset:60288
mm v[12:15],v[200:203],a[88:91],v[12:15]
wt lgkmcnt(4)
v_mul_u32_u24 v250, 7, v250
mm v[12:15],v[204:207],a[80:83],v[12:15]
rt v[200:201],v240 offset:128
rt v[202:203],v240 offset:1152
mm v[12:15],v[212:215],a[0:3],v[12:15]
cv v231,v2,v3
cv v233,v6,v7
mm v[12:15],v[208:211],a[8:11],v[12:15]
rt v[204:205],v240 offset:8576
rt v[206:207],v240 offset:9600
mm v[44:47],v[208:211],a[12:15],v[44:47]
v_permlane16_swap_b32 v230, v232
v_permlane16_swap_b32 v231, v233
mm v[44:47],v[212:215],a[4:7],v[44:47]
rt v[208:209],v240 offset:17024
rt v[210:211],v240 offset:18048
mm v[44:47],v[220:223],a[20:23],v[44:47]
st v[230:233],v238,s[32:35],s94 offen offset:0 sc1
mm v[44:47],v[216:219],a[28:31],v[44:47]
rt v[212:213],v240 offset:25472
rt v[214:215],v240 offset:26496
mm v[12:15],v[216:219],a[24:27],v[12:15]
wt lgkmcnt(4)
v_xor_b32 v250, s3, v250
mm v[12:15],v[220:223],a[16:19],v[12:15]
rt v[216:217],v240 offset:33920
rt v[218:219],v240 offset:34944
mm v[16:19],v[192:195],a[64:67],v[16:19]
cv v234,v40,v41
cv v236,v44,v45
mm v[16:19],v[196:199],a[72:75],v[16:19]
rt v[220:221],v240 offset:42368
rt v[222:223],v240 offset:43392
mm v[48:51],v[196:199],a[76:79],v[48:51]
cv v235,v42,v43
)ASM" R"ASM(cv v237,v46,v47
mm v[48:51],v[192:195],a[68:71],v[48:51]
rt v[196:197],v241 offset:50816
rt v[198:199],v241 offset:51840
mm v[48:51],v[200:203],a[84:87],v[48:51]
v_permlane16_swap_b32 v234, v236
v_permlane16_swap_b32 v235, v237
mm v[48:51],v[204:207],a[92:95],v[48:51]
rt v[192:193],v241 offset:59264
rt v[194:195],v241 offset:60288
mm v[16:19],v[204:207],a[88:91],v[16:19]
wt lgkmcnt(4)
v_bfe_u32 v251, v255, 4, 2
mm v[16:19],v[200:203],a[80:83],v[16:19]
rt v[204:205],v241 offset:128
rt v[206:207],v241 offset:1152
mm v[16:19],v[208:211],a[0:3],v[16:19]
st v[234:237],v238,s[32:35],s95 offen offset:1024 sc1
mm v[16:19],v[212:215],a[8:11],v[16:19]
rt v[200:201],v241 offset:8576
rt v[202:203],v241 offset:9600
mm v[48:51],v[212:215],a[12:15],v[48:51]
cv v230,v8,v9
cv v232,v12,v13
mm v[48:51],v[208:211],a[4:7],v[48:51]
rt v[212:213],v241 offset:17024
rt v[214:215],v241 offset:18048
mm v[48:51],v[216:219],a[20:23],v[48:51]
cv v231,v10,v11
cv v233,v14,v15
mm v[48:51],v[220:223],a[28:31],v[48:51]
rt v[208:209],v241 offset:25472
rt v[210:211],v241 offset:26496
mm v[16:19],v[220:223],a[24:27],v[16:19]
wt lgkmcnt(4)
v_lshl_add_u32 v250, v250, 2, v251
mm v[16:19],v[216:219],a[16:19],v[16:19]
rt v[220:221],v241 offset:33920
rt v[222:223],v241 offset:34944
mm v[20:23],v[196:199],a[64:67],v[20:23]
v_permlane16_swap_b32 v230, v232
v_permlane16_swap_b32 v231, v233
mm v[20:23],v[192:195],a[72:75],v[20:23]
rt v[216:217],v241 offset:42368
rt v[218:219],v241 offset:43392
mm v[52:55],v[192:195],a[76:79],v[52:55]
v_lshlrev_b32 v250, 9, v250
v_and_b32 v251, 0x2000, v250
mm v[52:55],v[196:199],a[68:71],v[52:55]
rt v[192:193],v242 offset:50816
rt v[194:195],v242 offset:51840
mm v[52:55],v[204:207],a[84:87],v[52:55]
st v[230:233],v238,s[32:35],s94 offen offset:1024 sc1
mm v[52:55],v[200:203],a[92:95],v[52:55]
rt v[196:197],v242 offset:59264
rt v[198:199],v242 offset:60288
mm v[20:23],v[200:203],a[88:91],v[20:23]
wt lgkmcnt(4)
v_add_u32 v250, v250, v251
mm v[20:23],v[204:207],a[80:83],v[20:23]
rt v[200:201],v242 offset:128
rt v[202:203],v242 offset:1152
mm v[20:23],v[212:215],a[0:3],v[20:23]
ld v[136:139],v224,s[68:71],0 offen offset:0
mm v[20:23],v[208:211],a[8:11],v[20:23]
rt v[204:205],v242 offset:8576
rt v[206:207],v242 offset:9600
mm v[52:55],v[208:211],a[12:15],v[52:55]
v_bfe_u32 v251, v255, 7, 1
v_lshl_add_u32 v250, v251, 13, v250
mm v[52:55],v[212:215],a[4:7],v[52:55]
rt v[208:209],v242 offset:17024
rt v[210:211],v242 offset:18048
mm v[52:55],v[220:223],a[20:23],v[52:55]
ld v[140:143],v225,s[48:51],0 offen offset:0
mm v[52:55],v[216:219],a[28:31],v[52:55]
rt v[212:213],v242 offset:25472
rt v[214:215],v242 offset:26496
mm v[20:23],v[216:219],a[24:27],v[20:23]
wt lgkmcnt(4)
s_mov_b32 s93, 0
ld a[224:227],v229,s[28:31],s93 offen offset:0
mm v[20:23],v[220:223],a[16:19],v[20:23]
rt v[216:217],v242 offset:33920
rt v[218:219],v242 offset:34944
mm v[24:27],v[192:195],a[64:67],v[24:27]
cv v234,v48,v49
cv v236,v52,v53
mm v[24:27],v[196:199],a[72:75],v[24:27]
rt v[220:221],v242 offset:42368
rt v[222:223],v242 offset:43392
.La5_LW3_4:
s_cbranch_execz .La5_LA_RETURN
mm v[56:59],v[196:199],a[76:79],v[56:59]
cv v235,v50,v51
cv v237,v54,v55
mm v[56:59],v[192:195],a[68:71],v[56:59]
rt v[196:197],v243 offset:50816
rt v[198:199],v243 offset:51840
mm v[56:59],v[200:203],a[84:87],v[56:59]
)ASM" R"ASM(v_permlane16_swap_b32 v234, v236
v_permlane16_swap_b32 v235, v237
mm v[56:59],v[204:207],a[92:95],v[56:59]
rt v[192:193],v243 offset:59264
rt v[194:195],v243 offset:60288
mm v[24:27],v[204:207],a[88:91],v[24:27]
wt lgkmcnt(4)
ld a[228:231],v229,s[28:31],s93 offen offset:2048
mm v[24:27],v[200:203],a[80:83],v[24:27]
rt v[204:205],v243 offset:128
rt v[206:207],v243 offset:1152
mm v[24:27],v[208:211],a[0:3],v[24:27]
st v[234:237],v238,s[32:35],s95 offen offset:2048 sc1
mm v[24:27],v[212:215],a[8:11],v[24:27]
rt v[200:201],v243 offset:8576
rt v[202:203],v243 offset:9600
mm v[56:59],v[212:215],a[12:15],v[56:59]
cv v230,v16,v17
cv v232,v20,v21
mm v[56:59],v[208:211],a[4:7],v[56:59]
rt v[212:213],v243 offset:17024
rt v[214:215],v243 offset:18048
mm v[56:59],v[216:219],a[20:23],v[56:59]
cv v231,v18,v19
cv v233,v22,v23
mm v[56:59],v[220:223],a[28:31],v[56:59]
rt v[208:209],v243 offset:25472
rt v[210:211],v243 offset:26496
mm v[24:27],v[220:223],a[24:27],v[24:27]
wt lgkmcnt(4)
s_mov_b32 s93, 4096
ld a[232:235],v229,s[28:31],s93 offen offset:0
mm v[24:27],v[216:219],a[16:19],v[24:27]
rt v[220:221],v243 offset:33920
rt v[222:223],v243 offset:34944
mm v[28:31],v[196:199],a[64:67],v[28:31]
v_permlane16_swap_b32 v230, v232
v_permlane16_swap_b32 v231, v233
mm v[28:31],v[192:195],a[72:75],v[28:31]
rt v[216:217],v243 offset:42368
rt v[218:219],v243 offset:43392
mm v[60:63],v[192:195],a[76:79],v[60:63]
ld a[236:239],v229,s[28:31],s93 offen offset:2048
mm v[60:63],v[196:199],a[68:71],v[60:63]
rt v[192:193],v240 offset:50688
rt v[194:195],v240 offset:51712
mm v[60:63],v[204:207],a[84:87],v[60:63]
st v[230:233],v238,s[32:35],s94 offen offset:2048 sc1
mm v[60:63],v[200:203],a[92:95],v[60:63]
rt v[196:197],v240 offset:59136
rt v[198:199],v240 offset:60160
mm v[28:31],v[200:203],a[88:91],v[28:31]
wt lgkmcnt(4)
s_mov_b32 s93, 8192
ld a[240:243],v229,s[28:31],s93 offen offset:0
mm v[28:31],v[204:207],a[80:83],v[28:31]
rt v[200:201],v240 offset:0
rt v[202:203],v240 offset:1024
mm v[28:31],v[212:215],a[0:3],v[28:31]
ld v[152:155],v224,s[68:71],0 offen offset:512
mm v[28:31],v[208:211],a[8:11],v[28:31]
rt v[204:205],v240 offset:8448
rt v[206:207],v240 offset:9472
mm v[60:63],v[208:211],a[12:15],v[60:63]
ld a[244:247],v229,s[28:31],s93 offen offset:2048
mm v[60:63],v[212:215],a[4:7],v[60:63]
rt v[208:209],v240 offset:16896
rt v[210:211],v240 offset:17920
mm v[60:63],v[220:223],a[20:23],v[60:63]
ld v[156:159],v225,s[48:51],0 offen offset:512
mm v[60:63],v[216:219],a[28:31],v[60:63]
rt v[212:213],v240 offset:25344
rt v[214:215],v240 offset:26368
mm v[28:31],v[216:219],a[24:27],v[28:31]
wt vmcnt(24) lgkmcnt(4)
s_mov_b32 s93, 12288
ld a[248:251],v229,s[28:31],s93 offen offset:0
mm v[28:31],v[220:223],a[16:19],v[28:31]
rt v[216:217],v240 offset:33792
rt v[218:219],v240 offset:34816
mm v[64:67],v[192:195],a[32:35],v[64:67]
cv v234,v56,v57
cv v236,v60,v61
mm v[64:67],v[196:199],a[48:51],v[64:67]
rt v[220:221],v240 offset:42240
rt v[222:223],v240 offset:43264
mm v[96:99],v[196:199],a[52:55],v[96:99]
cv v235,v58,v59
cv v237,v62,v63
mm v[96:99],v[192:195],a[44:47],v[96:99]
rt v[196:197],v241 offset:50688
rt v[198:199],v241 offset:51712
mm v[96:99],v[200:203],a[36:39],v[96:99]
v_permlane16_swap_b32 v234, v236
v_permlane16_swap_b32 v235, v237
mm v[96:99],v[204:207],a[60:63],v[96:99]
rt v[192:193],v241 offset:59136
rt v[194:195],v241 offset:60160
mm v[64:67],v[204:207],a[40:43],v[64:67]
wt vmcnt(17) lgkmcnt(4)
)ASM" R"ASM(ld a[252:255],v229,s[28:31],s93 offen offset:2048
mm v[64:67],v[200:203],a[56:59],v[64:67]
rt v[204:205],v241 offset:0
rt v[206:207],v241 offset:1024
mm v[64:67],v[208:211],v[128:131],v[64:67]
st v[234:237],v238,s[32:35],s95 offen offset:3072 sc1
mm v[64:67],v[212:215],v[144:147],v[64:67]
rt v[200:201],v241 offset:8448
rt v[202:203],v241 offset:9472
mm v[96:99],v[212:215],v[148:151],v[96:99]
cv v230,v24,v25
cv v232,v28,v29
mm v[96:99],v[208:211],v[132:135],v[96:99]
rt v[212:213],v241 offset:16896
rt v[214:215],v241 offset:17920
mm v[96:99],v[216:219],v[164:167],v[96:99]
cv v231,v26,v27
cv v233,v30,v31
mm v[96:99],v[220:223],v[180:183],v[96:99]
rt v[208:209],v241 offset:25344
rt v[210:211],v241 offset:26368
mm v[64:67],v[220:223],v[176:179],v[64:67]
wt lgkmcnt(4)
s_mov_b32 s93, 0
ld a[64:67],v229,s[28:31],s93 offen offset:256
mm v[64:67],v[216:219],v[160:163],v[64:67]
rt v[220:221],v241 offset:33792
rt v[222:223],v241 offset:34816
mm v[68:71],v[196:199],a[32:35],v[68:71]
v_permlane16_swap_b32 v230, v232
v_permlane16_swap_b32 v231, v233
mm v[68:71],v[192:195],a[48:51],v[68:71]
rt v[216:217],v241 offset:42240
rt v[218:219],v241 offset:43264
mm v[100:103],v[192:195],a[52:55],v[100:103]
ld a[68:71],v229,s[28:31],s93 offen offset:2304
mm v[100:103],v[196:199],a[44:47],v[100:103]
rt v[192:193],v242 offset:50688
rt v[194:195],v242 offset:51712
mm v[100:103],v[204:207],a[36:39],v[100:103]
st v[230:233],v238,s[32:35],s94 offen offset:3072 sc1
mm v[100:103],v[200:203],a[60:63],v[100:103]
rt v[196:197],v242 offset:59136
rt v[198:199],v242 offset:60160
mm v[68:71],v[200:203],a[40:43],v[68:71]
wt lgkmcnt(4)
s_mov_b32 s93, 4096
ld a[72:75],v229,s[28:31],s93 offen offset:256
mm v[68:71],v[204:207],a[56:59],v[68:71]
rt v[200:201],v242 offset:0
rt v[202:203],v242 offset:1024
mm v[68:71],v[212:215],v[128:131],v[68:71]
ld v[168:171],v224,s[68:71],0 offen offset:1024
mm v[68:71],v[208:211],v[144:147],v[68:71]
rt v[204:205],v242 offset:8448
rt v[206:207],v242 offset:9472
mm v[100:103],v[208:211],v[148:151],v[100:103]
ld a[76:79],v229,s[28:31],s93 offen offset:2304
mm v[100:103],v[212:215],v[132:135],v[100:103]
rt v[208:209],v242 offset:16896
rt v[210:211],v242 offset:17920
mm v[100:103],v[220:223],v[164:167],v[100:103]
ld v[172:175],v225,s[48:51],0 offen offset:1024
mm v[100:103],v[216:219],v[180:183],v[100:103]
rt v[212:213],v242 offset:25344
rt v[214:215],v242 offset:26368
mm v[68:71],v[216:219],v[176:179],v[68:71]
wt lgkmcnt(4)
s_mov_b32 s93, 8192
ld a[80:83],v229,s[28:31],s93 offen offset:256
mm v[68:71],v[220:223],v[160:163],v[68:71]
rt v[216:217],v242 offset:33792
rt v[218:219],v242 offset:34816
mm v[72:75],v[192:195],a[32:35],v[72:75]
cv v234,v96,v97
cv v236,v100,v101
mm v[72:75],v[196:199],a[48:51],v[72:75]
rt v[220:221],v242 offset:42240
rt v[222:223],v242 offset:43264
mm v[104:107],v[196:199],a[52:55],v[104:107]
cv v235,v98,v99
cv v237,v102,v103
mm v[104:107],v[192:195],a[44:47],v[104:107]
rt v[196:197],v243 offset:50688
rt v[198:199],v243 offset:51712
mm v[104:107],v[200:203],a[36:39],v[104:107]
v_permlane16_swap_b32 v234, v236
v_permlane16_swap_b32 v235, v237
mm v[104:107],v[204:207],a[60:63],v[104:107]
rt v[192:193],v243 offset:59136
rt v[194:195],v243 offset:60160
mm v[72:75],v[204:207],a[40:43],v[72:75]
wt lgkmcnt(4)
ld a[84:87],v229,s[28:31],s93 offen offset:2304
mm v[72:75],v[200:203],a[56:59],v[72:75]
rt v[204:205],v243 offset:0
rt v[206:207],v243 offset:1024
mm v[72:75],v[208:211],v[128:131],v[72:75]
st v[234:237],v238,s[32:35],s97 offen offset:0 sc1
mm v[72:75],v[212:215],v[144:147],v[72:75]
)ASM" R"ASM(rt v[200:201],v243 offset:8448
rt v[202:203],v243 offset:9472
mm v[104:107],v[212:215],v[148:151],v[104:107]
cv v230,v64,v65
cv v232,v68,v69
mm v[104:107],v[208:211],v[132:135],v[104:107]
rt v[212:213],v243 offset:16896
rt v[214:215],v243 offset:17920
mm v[104:107],v[216:219],v[164:167],v[104:107]
cv v231,v66,v67
cv v233,v70,v71
mm v[104:107],v[220:223],v[180:183],v[104:107]
rt v[208:209],v243 offset:25344
rt v[210:211],v243 offset:26368
mm v[72:75],v[220:223],v[176:179],v[72:75]
wt lgkmcnt(4)
s_mov_b32 s93, 12288
ld a[88:91],v229,s[28:31],s93 offen offset:256
mm v[72:75],v[216:219],v[160:163],v[72:75]
rt v[220:221],v243 offset:33792
rt v[222:223],v243 offset:34816
mm v[76:79],v[196:199],a[32:35],v[76:79]
v_permlane16_swap_b32 v230, v232
v_permlane16_swap_b32 v231, v233
mm v[76:79],v[192:195],a[48:51],v[76:79]
rt v[216:217],v243 offset:42240
rt v[218:219],v243 offset:43264
mm v[108:111],v[192:195],a[52:55],v[108:111]
ld a[92:95],v229,s[28:31],s93 offen offset:2304
mm v[108:111],v[196:199],a[44:47],v[108:111]
rt v[192:193],v240 offset:50816
rt v[194:195],v240 offset:51840
mm v[108:111],v[204:207],a[36:39],v[108:111]
st v[230:233],v238,s[32:35],s96 offen offset:0 sc1
mm v[108:111],v[200:203],a[60:63],v[108:111]
rt v[196:197],v240 offset:59264
rt v[198:199],v240 offset:60288
mm v[76:79],v[200:203],a[40:43],v[76:79]
wt lgkmcnt(4)
v_and_b32_e64 v251, 64, v255
mm v[76:79],v[204:207],a[56:59],v[76:79]
rt v[200:201],v240 offset:128
rt v[202:203],v240 offset:1152
mm v[76:79],v[212:215],v[128:131],v[76:79]
ld v[184:187],v224,s[68:71],0 offen offset:1536
mm v[76:79],v[208:211],v[144:147],v[76:79]
rt v[204:205],v240 offset:8576
rt v[206:207],v240 offset:9600
mm v[108:111],v[208:211],v[148:151],v[108:111]
v_lshl_add_u32 v251, v251, 24, v250
v_and_b32 v248, 0x1f0, v255
mm v[108:111],v[212:215],v[132:135],v[108:111]
rt v[208:209],v240 offset:17024
rt v[210:211],v240 offset:18048
mm v[108:111],v[220:223],v[164:167],v[108:111]
ld v[188:191],v225,s[48:51],0 offen offset:1536
mm v[108:111],v[216:219],v[180:183],v[108:111]
rt v[212:213],v240 offset:25472
rt v[214:215],v240 offset:26496
mm v[76:79],v[216:219],v[176:179],v[76:79]
wt lgkmcnt(4)
v_lshrrev_b32_e64 v248, 1, v248
mm v[76:79],v[220:223],v[160:163],v[76:79]
rt v[216:217],v240 offset:33920
rt v[218:219],v240 offset:34944
mm v[80:83],v[192:195],a[32:35],v[80:83]
cv v234,v104,v105
cv v236,v108,v109
mm v[80:83],v[196:199],a[48:51],v[80:83]
rt v[220:221],v240 offset:42368
rt v[222:223],v240 offset:43392
mm v[112:115],v[196:199],a[52:55],v[112:115]
cv v235,v106,v107
cv v237,v110,v111
mm v[112:115],v[192:195],a[44:47],v[112:115]
rt v[196:197],v241 offset:50816
rt v[198:199],v241 offset:51840
mm v[112:115],v[200:203],a[36:39],v[112:115]
v_permlane16_swap_b32 v234, v236
v_permlane16_swap_b32 v235, v237
mm v[112:115],v[204:207],a[60:63],v[112:115]
rt v[192:193],v241 offset:59264
rt v[194:195],v241 offset:60288
mm v[80:83],v[204:207],a[40:43],v[80:83]
wt lgkmcnt(4)
mm v[80:83],v[200:203],a[56:59],v[80:83]
rt v[204:205],v241 offset:128
rt v[206:207],v241 offset:1152
mm v[80:83],v[208:211],v[128:131],v[80:83]
st v[234:237],v238,s[32:35],s97 offen offset:1024 sc1
mm v[80:83],v[212:215],v[144:147],v[80:83]
rt v[200:201],v241 offset:8576
rt v[202:203],v241 offset:9600
mm v[112:115],v[212:215],v[148:151],v[112:115]
cv v230,v72,v73
cv v232,v76,v77
mm v[112:115],v[208:211],v[132:135],v[112:115]
rt v[212:213],v241 offset:17024
rt v[214:215],v241 offset:18048
mm v[112:115],v[216:219],v[164:167],v[112:115]
)ASM" R"ASM(cv v231,v74,v75
cv v233,v78,v79
mm v[112:115],v[220:223],v[180:183],v[112:115]
rt v[208:209],v241 offset:25472
rt v[210:211],v241 offset:26496
mm v[80:83],v[220:223],v[176:179],v[80:83]
wt lgkmcnt(4)
mm v[80:83],v[216:219],v[160:163],v[80:83]
rt v[220:221],v241 offset:33920
rt v[222:223],v241 offset:34944
mm v[84:87],v[196:199],a[32:35],v[84:87]
v_permlane16_swap_b32 v230, v232
v_permlane16_swap_b32 v231, v233
mm v[84:87],v[192:195],a[48:51],v[84:87]
rt v[216:217],v241 offset:42368
rt v[218:219],v241 offset:43392
mm v[116:119],v[192:195],a[52:55],v[116:119]
mm v[116:119],v[196:199],a[44:47],v[116:119]
rt v[192:193],v242 offset:50816
rt v[194:195],v242 offset:51840
mm v[116:119],v[204:207],a[36:39],v[116:119]
st v[230:233],v238,s[32:35],s96 offen offset:1024 sc1
mm v[116:119],v[200:203],a[60:63],v[116:119]
rt v[196:197],v242 offset:59264
rt v[198:199],v242 offset:60288
mm v[84:87],v[200:203],a[40:43],v[84:87]
wt lgkmcnt(4)
mm v[84:87],v[204:207],a[56:59],v[84:87]
rt v[200:201],v242 offset:128
rt v[202:203],v242 offset:1152
mm v[84:87],v[212:215],v[128:131],v[84:87]
mm v[84:87],v[208:211],v[144:147],v[84:87]
rt v[204:205],v242 offset:8576
rt v[206:207],v242 offset:9600
mm v[116:119],v[208:211],v[148:151],v[116:119]
mm v[116:119],v[212:215],v[132:135],v[116:119]
rt v[208:209],v242 offset:17024
rt v[210:211],v242 offset:18048
mm v[116:119],v[220:223],v[164:167],v[116:119]
mm v[116:119],v[216:219],v[180:183],v[116:119]
rt v[212:213],v242 offset:25472
rt v[214:215],v242 offset:26496
mm v[84:87],v[216:219],v[176:179],v[84:87]
wt lgkmcnt(4)
mm v[84:87],v[220:223],v[160:163],v[84:87]
rt v[216:217],v242 offset:33920
rt v[218:219],v242 offset:34944
mm v[88:91],v[192:195],a[32:35],v[88:91]
cv v234,v112,v113
cv v236,v116,v117
mm v[88:91],v[196:199],a[48:51],v[88:91]
rt v[220:221],v242 offset:42368
rt v[222:223],v242 offset:43392
mm v[120:123],v[196:199],a[52:55],v[120:123]
cv v235,v114,v115
cv v237,v118,v119
mm v[120:123],v[192:195],a[44:47],v[120:123]
rt v[196:197],v243 offset:50816
rt v[198:199],v243 offset:51840
mm v[120:123],v[200:203],a[36:39],v[120:123]
v_permlane16_swap_b32 v234, v236
v_permlane16_swap_b32 v235, v237
mm v[120:123],v[204:207],a[60:63],v[120:123]
rt v[192:193],v243 offset:59264
rt v[194:195],v243 offset:60288
mm v[88:91],v[204:207],a[40:43],v[88:91]
wt lgkmcnt(4)
mm v[88:91],v[200:203],a[56:59],v[88:91]
rt v[204:205],v243 offset:128
rt v[206:207],v243 offset:1152
mm v[88:91],v[208:211],v[128:131],v[88:91]
st v[234:237],v238,s[32:35],s97 offen offset:2048 sc1
mm v[88:91],v[212:215],v[144:147],v[88:91]
rt v[200:201],v243 offset:8576
rt v[202:203],v243 offset:9600
mm v[120:123],v[212:215],v[148:151],v[120:123]
cv v230,v80,v81
cv v232,v84,v85
mm v[120:123],v[208:211],v[132:135],v[120:123]
rt v[212:213],v243 offset:17024
rt v[214:215],v243 offset:18048
mm v[120:123],v[216:219],v[164:167],v[120:123]
cv v231,v82,v83
cv v233,v86,v87
wt vmcnt(15)
buffer_store_dwordx2 v[252:253], v251, s[64:67], s92 offen sc1
mm v[120:123],v[220:223],v[180:183],v[120:123]
rt v[208:209],v243 offset:25472
rt v[210:211],v243 offset:26496
mm v[88:91],v[220:223],v[176:179],v[88:91]
wt lgkmcnt(4)
mm v[88:91],v[216:219],v[160:163],v[88:91]
rt v[220:221],v243 offset:33920
rt v[222:223],v243 offset:34944
mm v[92:95],v[196:199],a[32:35],v[92:95]
v_permlane16_swap_b32 v230, v232
v_permlane16_swap_b32 v231, v233
mm v[92:95],v[192:195],a[48:51],v[92:95]
rt v[216:217],v243 offset:42368
rt v[218:219],v243 offset:43392
)ASM" R"ASM(mm v[124:127],v[192:195],a[52:55],v[124:127]
mm v[124:127],v[196:199],a[44:47],v[124:127]
mm v[124:127],v[204:207],a[36:39],v[124:127]
st v[230:233],v238,s[32:35],s96 offen offset:2048 sc1
mm v[124:127],v[200:203],a[60:63],v[124:127]
mm v[92:95],v[200:203],a[40:43],v[92:95]
wt lgkmcnt(0)
mm v[92:95],v[204:207],a[56:59],v[92:95]
mm v[92:95],v[212:215],v[128:131],v[92:95]
mm v[92:95],v[208:211],v[144:147],v[92:95]
mm v[124:127],v[208:211],v[148:151],v[124:127]
mm v[124:127],v[212:215],v[132:135],v[124:127]
mm v[124:127],v[220:223],v[164:167],v[124:127]
mm v[124:127],v[216:219],v[180:183],v[124:127]
mm v[92:95],v[216:219],v[176:179],v[92:95]
mm v[92:95],v[220:223],v[160:163],v[92:95]
s_nop 7
cv v234,v120,v121
cv v236,v124,v125
cv v235,v122,v123
cv v237,v126,v127
cv v230,v88,v89
cv v232,v92,v93
cv v231,v90,v91
cv v233,v94,v95
v_permlane16_swap_b32 v234, v236
v_permlane16_swap_b32 v235, v237
st v[234:237],v238,s[32:35],s97 offen offset:3072 sc1
v_permlane16_swap_b32 v230, v232
v_permlane16_swap_b32 v231, v233
st v[230:233],v238,s[32:35],s96 offen offset:3072 sc1
s_branch .La5_LT_0
; ---- O projection, odd XCDs: seven and a half heads
.La5_LO_SC:
.La5_LW7_0:
s_cbranch_execz .La5_LW7_4
mm v[64:67],v[192:195],v[160:163],0
buffer_load_dwordx2 v[250:251], v248, s[64:67], s5 offen sc1
mm v[64:67],v[196:199],v[164:167],v[64:67]
rt v[220:221],v243 offset:25344
rt v[222:223],v243 offset:26368
mm v[96:99],v[196:199],v[180:183],0
s_add_u32 m0, s83, 0
s_bfe_u32 s80, s4, 262148
mm v[96:99],v[192:195],v[176:179],v[96:99]
rt v[196:197],v240 offset:17024
rt v[198:199],v240 offset:18048
s_xor_b32 s80, s80, s72
s_lshl_b32 s80, s80, 18
s_add_u32 s80, s80, 131072
mm v[100:103],v[200:203],v[176:179],0
ld v224,s[56:59],s80 offen lds
mm v[100:103],v[204:207],v[180:183],v[100:103]
rt v[192:193],v240 offset:25472
rt v[194:195],v240 offset:26496
mm v[68:71],v[204:207],v[164:167],0
s_add_u32 m0, s83, 1024
wt lgkmcnt(4)
mm v[68:71],v[200:203],v[160:163],v[68:71]
rt v[204:205],v241 offset:17024
rt v[206:207],v241 offset:18048
mm v[72:75],v[208:211],v[160:163],0
ld v225,s[56:59],s80 offen lds
mm v[72:75],v[212:215],v[164:167],v[72:75]
rt v[200:201],v241 offset:25472
rt v[202:203],v241 offset:26496
mm v[104:107],v[212:215],v[180:183],0
s_add_u32 m0, s83, 2176
s_bfe_u32 s53, s4, 262148
mm v[104:107],v[208:211],v[176:179],v[104:107]
rt v[212:213],v242 offset:17024
rt v[214:215],v242 offset:18048
mm v[108:111],v[216:219],v[176:179],0
ld v226,s[56:59],s80 offen lds
mm v[108:111],v[220:223],v[180:183],v[108:111]
rt v[208:209],v242 offset:25472
rt v[210:211],v242 offset:26496
mm v[76:79],v[220:223],v[164:167],0
s_add_u32 m0, s83, 3200
wt lgkmcnt(4)
mm v[76:79],v[216:219],v[160:163],v[76:79]
rt v[220:221],v243 offset:17024
rt v[222:223],v243 offset:18048
mm v[80:83],v[196:199],v[160:163],0
ld v227,s[56:59],s80 offen lds
mm v[80:83],v[192:195],v[164:167],v[80:83]
rt v[216:217],v243 offset:25472
rt v[218:219],v243 offset:26496
mm v[112:115],v[192:195],v[180:183],0
s_add_u32 m0, s83, 118272
s_bfe_u32 s80, s4, 262152
mm v[112:115],v[196:199],v[176:179],v[112:115]
rt v[192:193],v240 offset:33792
rt v[194:195],v240 offset:34816
s_xor_b32 s80, s80, s72
s_lshl_b32 s80, s80, 18
mm v[116:119],v[204:207],v[176:179],0
ld v224,s[56:59],s80 offen lds
mm v[116:119],v[200:203],v[180:183],v[116:119]
rt v[196:197],v240 offset:42240
)ASM" R"ASM(rt v[198:199],v240 offset:43264
mm v[84:87],v[200:203],v[164:167],0
s_add_u32 m0, s83, 119296
wt lgkmcnt(4)
mm v[84:87],v[204:207],v[160:163],v[84:87]
rt v[200:201],v241 offset:33792
rt v[202:203],v241 offset:34816
mm v[88:91],v[212:215],v[160:163],0
ld v225,s[56:59],s80 offen lds
mm v[88:91],v[208:211],v[164:167],v[88:91]
rt v[204:205],v241 offset:42240
rt v[206:207],v241 offset:43264
mm v[120:123],v[208:211],v[180:183],0
s_add_u32 m0, s83, 120448
s_xor_b32 s53, s53, s72
mm v[120:123],v[212:215],v[176:179],v[120:123]
rt v[208:209],v242 offset:33792
rt v[210:211],v242 offset:34816
mm v[124:127],v[220:223],v[176:179],0
ld v226,s[56:59],s80 offen lds
mm v[124:127],v[216:219],v[180:183],v[124:127]
rt v[212:213],v242 offset:42240
rt v[214:215],v242 offset:43264
mm v[92:95],v[216:219],v[164:167],0
wt vmcnt(7)
v_cmp_eq_u64 vcc, v[250:251], v[252:253]
s_nop 0
s_bitcmp1_b32 vcc_lo, 1
s_cbranch_scc0 .La5_LO_NC1
.La5_LO_KC1:
wt lgkmcnt(4)
mm v[92:95],v[220:223],v[160:163],v[92:95]
rt v[216:217],v243 offset:33792
rt v[218:219],v243 offset:34816
s_lshl_b32 s53, s53, 13
mm v[64:67],v[192:195],v[168:171],v[64:67]
ld a[0:3],v244,s[44:47],s53 offen offset:0 sc1
mm v[64:67],v[196:199],v[172:175],v[64:67]
rt v[220:221],v243 offset:42240
rt v[222:223],v243 offset:43264
mm v[96:99],v[196:199],v[188:191],v[96:99]
.La5_LW7_1:
s_cbranch_execz .La5_LW7_5
ld a[4:7],v245,s[44:47],s53 offen offset:0 sc1
mm v[96:99],v[192:195],v[184:187],v[96:99]
rt v[196:197],v240 offset:33920
rt v[198:199],v240 offset:34944
mm v[100:103],v[200:203],v[184:187],v[100:103]
ld a[8:11],v246,s[44:47],s53 offen offset:0 sc1
mm v[100:103],v[204:207],v[188:191],v[100:103]
rt v[192:193],v240 offset:42368
rt v[194:195],v240 offset:43392
mm v[68:71],v[204:207],v[172:175],v[68:71]
wt lgkmcnt(4)
s_bfe_u32 s54, s4, 262152
mm v[68:71],v[200:203],v[168:171],v[68:71]
rt v[204:205],v241 offset:33920
rt v[206:207],v241 offset:34944
mm v[72:75],v[208:211],v[168:171],v[72:75]
ld a[12:15],v247,s[44:47],s53 offen offset:0 sc1
mm v[72:75],v[212:215],v[172:175],v[72:75]
rt v[200:201],v241 offset:42368
rt v[202:203],v241 offset:43392
mm v[104:107],v[212:215],v[188:191],v[104:107]
ld a[16:19],v244,s[44:47],s53 offen offset:1024 sc1
mm v[104:107],v[208:211],v[184:187],v[104:107]
rt v[212:213],v242 offset:33920
rt v[214:215],v242 offset:34944
mm v[108:111],v[216:219],v[184:187],v[108:111]
ld a[20:23],v245,s[44:47],s53 offen offset:1024 sc1
mm v[108:111],v[220:223],v[188:191],v[108:111]
rt v[208:209],v242 offset:42368
rt v[210:211],v242 offset:43392
mm v[76:79],v[220:223],v[172:175],v[76:79]
s_nop 0
s_barrier
wt lgkmcnt(4)
mm v[76:79],v[216:219],v[168:171],v[76:79]
rt v[220:221],v243 offset:33920
rt v[222:223],v243 offset:34944
mm v[80:83],v[196:199],v[168:171],v[80:83]
ld a[24:27],v246,s[44:47],s53 offen offset:1024 sc1
mm v[80:83],v[192:195],v[172:175],v[80:83]
rt v[216:217],v243 offset:42368
rt v[218:219],v243 offset:43392
mm v[112:115],v[192:195],v[188:191],v[112:115]
ld a[28:31],v247,s[44:47],s53 offen offset:1024 sc1
mm v[112:115],v[196:199],v[184:187],v[112:115]
rt v[192:193],v240 offset:50688
rt v[194:195],v240 offset:51712
mm v[116:119],v[204:207],v[184:187],v[116:119]
ld a[32:35],v244,s[44:47],s53 offen offset:2048 sc1
mm v[116:119],v[200:203],v[188:191],v[116:119]
rt v[196:197],v240 offset:59136
rt v[198:199],v240 offset:60160
mm v[84:87],v[200:203],v[172:175],v[84:87]
wt lgkmcnt(4)
s_xor_b32 s54, s54, s72
mm v[84:87],v[204:207],v[168:171],v[84:87]
rt v[200:201],v241 offset:50688
)ASM" R"ASM(rt v[202:203],v241 offset:51712
mm v[88:91],v[212:215],v[168:171],v[88:91]
ld a[36:39],v245,s[44:47],s53 offen offset:2048 sc1
mm v[88:91],v[208:211],v[172:175],v[88:91]
rt v[204:205],v241 offset:59136
rt v[206:207],v241 offset:60160
mm v[120:123],v[208:211],v[188:191],v[120:123]
ld a[40:43],v246,s[44:47],s53 offen offset:2048 sc1
mm v[120:123],v[212:215],v[184:187],v[120:123]
rt v[208:209],v242 offset:50688
rt v[210:211],v242 offset:51712
mm v[124:127],v[220:223],v[184:187],v[124:127]
ld a[44:47],v247,s[44:47],s53 offen offset:2048 sc1
mm v[124:127],v[216:219],v[188:191],v[124:127]
rt v[212:213],v242 offset:59136
rt v[214:215],v242 offset:60160
mm v[92:95],v[216:219],v[172:175],v[92:95]
wt vmcnt(4) lgkmcnt(4)
s_lshl_b32 s54, s54, 13
mm v[92:95],v[220:223],v[168:171],v[92:95]
rt v[216:217],v243 offset:50688
rt v[218:219],v243 offset:51712
mm v[0:3],v[192:195],a[0:3],v[0:3]
s_bfe_u32 s55, s4, 262156
s_xor_b32 s55, s55, s72
mm v[0:3],v[196:199],a[16:19],v[0:3]
ld a[48:51],v244,s[44:47],s53 offen offset:3072 sc1
mm v[32:35],v[196:199],a[20:23],v[32:35]
s_lshl_b32 s55, s55, 13
s_bfe_u32 s52, s4, 262160
mm v[32:35],v[192:195],a[4:7],v[32:35]
rt v[220:221],v243 offset:59136
rt v[222:223],v243 offset:60160
mm v[64:67],v[192:195],a[8:11],v[64:67]
s_xor_b32 s52, s52, s72
s_lshl_b32 s52, s52, 13
mm v[64:67],v[196:199],a[24:27],v[64:67]
ld a[52:55],v245,s[44:47],s53 offen offset:3072 sc1
mm v[96:99],v[196:199],a[28:31],v[96:99]
mm v[96:99],v[192:195],a[12:15],v[96:99]
rt v[196:197],v240 offset:50816
rt v[198:199],v240 offset:51840
mm v[100:103],v[200:203],a[12:15],v[100:103]
mm v[100:103],v[204:207],a[28:31],v[100:103]
buffer_load_dwordx2 v[250:251], v248, s[64:67], s5 offen sc1
mm v[68:71],v[204:207],a[24:27],v[68:71]
mm v[68:71],v[200:203],a[8:11],v[68:71]
rt v[192:193],v240 offset:59264
rt v[194:195],v240 offset:60288
mm v[36:39],v[200:203],a[4:7],v[36:39]
mm v[36:39],v[204:207],a[20:23],v[36:39]
ld a[56:59],v246,s[44:47],s53 offen offset:3072 sc1
mm v[4:7],v[204:207],a[16:19],v[4:7]
wt lgkmcnt(4)
mm v[4:7],v[200:203],a[0:3],v[4:7]
rt v[204:205],v241 offset:50816
rt v[206:207],v241 offset:51840
mm v[8:11],v[208:211],a[0:3],v[8:11]
mm v[8:11],v[212:215],a[16:19],v[8:11]
ld a[60:63],v247,s[44:47],s53 offen offset:3072 sc1
mm v[40:43],v[212:215],a[20:23],v[40:43]
s_bfe_u32 s53, s4, 262164
s_xor_b32 s53, s53, s72
mm v[40:43],v[208:211],a[4:7],v[40:43]
rt v[200:201],v241 offset:59264
rt v[202:203],v241 offset:60288
mm v[72:75],v[208:211],a[8:11],v[72:75]
s_add_u32 m0, s83, 121472
s_lshl_b32 s53, s53, 13
mm v[72:75],v[212:215],a[24:27],v[72:75]
ld v227,s[56:59],s80 offen lds
mm v[104:107],v[212:215],a[28:31],v[104:107]
s_add_u32 s80, s80, 131072
mm v[104:107],v[208:211],a[12:15],v[104:107]
rt v[212:213],v242 offset:50816
rt v[214:215],v242 offset:51840
mm v[108:111],v[216:219],a[12:15],v[108:111]
s_add_u32 m0, s83, 67584
mm v[108:111],v[220:223],a[28:31],v[108:111]
ld v224,s[56:59],s80 offen lds
mm v[76:79],v[220:223],a[24:27],v[76:79]
mm v[76:79],v[216:219],a[8:11],v[76:79]
rt v[208:209],v242 offset:59264
rt v[210:211],v242 offset:60288
mm v[44:47],v[216:219],a[4:7],v[44:47]
s_add_u32 m0, s83, 68608
mm v[44:47],v[220:223],a[20:23],v[44:47]
ld v225,s[56:59],s80 offen lds
mm v[12:15],v[220:223],a[16:19],v[12:15]
wt lgkmcnt(4)
mm v[12:15],v[216:219],a[0:3],v[12:15]
rt v[220:221],v243 offset:50816
rt v[222:223],v243 offset:51840
mm v[16:19],v[196:199],a[0:3],v[16:19]
s_add_u32 m0, s83, 69760
mm v[16:19],v[192:195],a[16:19],v[16:19]
ld v226,s[56:59],s80 offen lds
mm v[48:51],v[192:195],a[20:23],v[48:51]
mm v[48:51],v[196:199],a[4:7],v[48:51]
rt v[216:217],v243 offset:59264
)ASM" R"ASM(rt v[218:219],v243 offset:60288
mm v[80:83],v[196:199],a[8:11],v[80:83]
s_add_u32 m0, s83, 70784
s_nop 0
s_barrier
mm v[80:83],v[192:195],a[24:27],v[80:83]
ld v227,s[56:59],s80 offen lds
mm v[112:115],v[192:195],a[28:31],v[112:115]
s_bfe_u32 s80, s4, 262156
s_xor_b32 s80, s80, s72
mm v[112:115],v[196:199],a[12:15],v[112:115]
rt v[192:193],v240 offset:0
rt v[194:195],v240 offset:1024
mm v[116:119],v[204:207],a[12:15],v[116:119]
s_add_u32 m0, s83, 84480
s_lshl_b32 s80, s80, 18
mm v[116:119],v[200:203],a[28:31],v[116:119]
ld v224,s[56:59],s80 offen lds
mm v[84:87],v[200:203],a[24:27],v[84:87]
mm v[84:87],v[204:207],a[8:11],v[84:87]
rt v[196:197],v240 offset:8448
rt v[198:199],v240 offset:9472
mm v[52:55],v[204:207],a[4:7],v[52:55]
s_add_u32 m0, s83, 85504
mm v[52:55],v[200:203],a[20:23],v[52:55]
ld v225,s[56:59],s80 offen lds
mm v[20:23],v[200:203],a[16:19],v[20:23]
wt lgkmcnt(4)
mm v[20:23],v[204:207],a[0:3],v[20:23]
rt v[200:201],v241 offset:0
rt v[202:203],v241 offset:1024
mm v[24:27],v[212:215],a[0:3],v[24:27]
s_add_u32 m0, s83, 86656
mm v[24:27],v[208:211],a[16:19],v[24:27]
ld v226,s[56:59],s80 offen lds
mm v[56:59],v[208:211],a[20:23],v[56:59]
wt vmcnt(10)
v_cmp_eq_u64 vcc, v[250:251], v[252:253]
mm v[56:59],v[212:215],a[4:7],v[56:59]
rt v[204:205],v241 offset:8448
rt v[206:207],v241 offset:9472
mm v[88:91],v[212:215],a[8:11],v[88:91]
s_add_u32 m0, s83, 87680
mm v[88:91],v[208:211],a[24:27],v[88:91]
ld v227,s[56:59],s80 offen lds
mm v[120:123],v[208:211],a[28:31],v[120:123]
s_bitcmp1_b32 vcc_lo, 2
s_cbranch_scc0 .La5_LO_NC2
.La5_LO_KC2:
mm v[120:123],v[212:215],a[12:15],v[120:123]
rt v[208:209],v242 offset:0
rt v[210:211],v242 offset:1024
mm v[124:127],v[220:223],a[12:15],v[124:127]
s_add_u32 s80, s80, 131072
mm v[124:127],v[216:219],a[28:31],v[124:127]
ld a[12:15],v244,s[44:47],s54 offen offset:0 sc1
mm v[92:95],v[216:219],a[24:27],v[92:95]
mm v[92:95],v[220:223],a[8:11],v[92:95]
rt v[212:213],v242 offset:8448
rt v[214:215],v242 offset:9472
mm v[60:63],v[220:223],a[4:7],v[60:63]
mm v[60:63],v[216:219],a[20:23],v[60:63]
mm v[28:31],v[216:219],a[16:19],v[28:31]
wt vmcnt(10) lgkmcnt(4)
mm v[28:31],v[220:223],a[0:3],v[28:31]
rt v[216:217],v243 offset:0
rt v[218:219],v243 offset:1024
mm v[0:3],v[192:195],a[32:35],v[0:3]
mm v[0:3],v[196:199],a[48:51],v[0:3]
ld a[0:3],v245,s[44:47],s54 offen offset:0 sc1
mm v[32:35],v[196:199],a[52:55],v[32:35]
mm v[32:35],v[192:195],a[36:39],v[32:35]
rt v[220:221],v243 offset:8448
rt v[222:223],v243 offset:9472
mm v[64:67],v[192:195],a[40:43],v[64:67]
mm v[64:67],v[196:199],a[56:59],v[64:67]
ld a[4:7],v246,s[44:47],s54 offen offset:0 sc1
mm v[96:99],v[196:199],a[60:63],v[96:99]
mm v[96:99],v[192:195],a[44:47],v[96:99]
rt v[196:197],v240 offset:128
rt v[198:199],v240 offset:1152
mm v[100:103],v[200:203],a[44:47],v[100:103]
s_mov_b32 s86, 0x05040100
s_mov_b32 s87, 0x07060302
mm v[100:103],v[204:207],a[60:63],v[100:103]
ld a[8:11],v247,s[44:47],s54 offen offset:0 sc1
mm v[68:71],v[204:207],a[56:59],v[68:71]
s_nop 0
ar v230,a128
mm v[68:71],v[200:203],a[40:43],v[68:71]
rt v[192:193],v240 offset:8576
rt v[194:195],v240 offset:9600
mm v[36:39],v[200:203],a[36:39],v[36:39]
v_add_u32 v240, 67584, v240
ar v231,a132
mm v[36:39],v[204:207],a[52:55],v[36:39]
ld a[16:19],v244,s[44:47],s54 offen offset:1024 sc1
mm v[4:7],v[204:207],a[48:51],v[4:7]
wt lgkmcnt(4)
v_perm_b32 v232, v231, v230, s86
)ASM" R"ASM(mm v[4:7],v[200:203],a[32:35],v[4:7]
rt v[204:205],v241 offset:128
rt v[206:207],v241 offset:1152
mm v[8:11],v[208:211],a[32:35],v[8:11]
v_perm_b32 v233, v231, v230, s87
aw a96,v232
mm v[8:11],v[212:215],a[48:51],v[8:11]
ld a[20:23],v245,s[44:47],s54 offen offset:1024 sc1
mm v[40:43],v[212:215],a[52:55],v[40:43]
aw a100,v233
ar v234,a136
mm v[40:43],v[208:211],a[36:39],v[40:43]
rt v[200:201],v241 offset:8576
rt v[202:203],v241 offset:9600
mm v[72:75],v[208:211],a[40:43],v[72:75]
v_add_u32 v241, 67584, v241
ar v235,a140
mm v[72:75],v[212:215],a[56:59],v[72:75]
ld a[24:27],v246,s[44:47],s54 offen offset:1024 sc1
mm v[104:107],v[212:215],a[60:63],v[104:107]
v_perm_b32 v236, v235, v234, s86
v_perm_b32 v237, v235, v234, s87
mm v[104:107],v[208:211],a[44:47],v[104:107]
rt v[212:213],v242 offset:128
rt v[214:215],v242 offset:1152
mm v[108:111],v[216:219],a[44:47],v[108:111]
aw a97,v236
aw a101,v237
mm v[108:111],v[220:223],a[60:63],v[108:111]
ld a[28:31],v247,s[44:47],s54 offen offset:1024 sc1
mm v[76:79],v[220:223],a[56:59],v[76:79]
ar v230,a144
ar v231,a148
mm v[76:79],v[216:219],a[40:43],v[76:79]
rt v[208:209],v242 offset:8576
rt v[210:211],v242 offset:9600
mm v[44:47],v[216:219],a[36:39],v[44:47]
v_add_u32 v242, 67584, v242
v_perm_b32 v232, v231, v230, s86
mm v[44:47],v[220:223],a[52:55],v[44:47]
v_perm_b32 v233, v231, v230, s87
aw a98,v232
mm v[12:15],v[220:223],a[48:51],v[12:15]
wt lgkmcnt(4)
aw a102,v233
mm v[12:15],v[216:219],a[32:35],v[12:15]
rt v[220:221],v243 offset:128
rt v[222:223],v243 offset:1152
mm v[16:19],v[196:199],a[32:35],v[16:19]
s_add_u32 m0, s83, 101376
ar v234,a152
mm v[16:19],v[192:195],a[48:51],v[16:19]
ld v224,s[56:59],s80 offen lds
mm v[48:51],v[192:195],a[52:55],v[48:51]
ar v235,a156
v_perm_b32 v236, v235, v234, s86
mm v[48:51],v[196:199],a[36:39],v[48:51]
rt v[216:217],v243 offset:8576
rt v[218:219],v243 offset:9600
mm v[80:83],v[196:199],a[40:43],v[80:83]
s_add_u32 m0, s83, 102400
v_add_u32 v243, 67584, v243
mm v[80:83],v[192:195],a[56:59],v[80:83]
ld v225,s[56:59],s80 offen lds
mm v[112:115],v[192:195],a[60:63],v[112:115]
wt vmcnt(14)
s_barrier
mm v[112:115],v[196:199],a[44:47],v[112:115]
rt v[192:193],v240 offset:50688
rt v[194:195],v240 offset:51712
mm v[116:119],v[204:207],a[44:47],v[116:119]
s_add_u32 m0, s83, 103552
v_perm_b32 v237, v235, v234, s87
mm v[116:119],v[200:203],a[60:63],v[116:119]
ld v226,s[56:59],s80 offen lds
mm v[84:87],v[200:203],a[56:59],v[84:87]
aw a99,v236
aw a103,v237
mm v[84:87],v[204:207],a[40:43],v[84:87]
rt v[196:197],v240 offset:59136
rt v[198:199],v240 offset:60160
mm v[52:55],v[204:207],a[36:39],v[52:55]
ar v230,a129
ar v231,a133
mm v[52:55],v[200:203],a[52:55],v[52:55]
ld a[64:67],v244,s[44:47],s54 offen offset:2048 sc1
mm v[20:23],v[200:203],a[48:51],v[20:23]
wt lgkmcnt(4)
v_perm_b32 v232, v231, v230, s86
mm v[20:23],v[204:207],a[32:35],v[20:23]
rt v[200:201],v241 offset:50688
rt v[202:203],v241 offset:51712
mm v[24:27],v[212:215],a[32:35],v[24:27]
v_perm_b32 v233, v231, v230, s87
aw a104,v232
mm v[24:27],v[208:211],a[48:51],v[24:27]
ld a[68:71],v245,s[44:47],s54 offen offset:2048 sc1
mm v[56:59],v[208:211],a[52:55],v[56:59]
aw a108,v233
ar v234,a137
)ASM" R"ASM(mm v[56:59],v[212:215],a[36:39],v[56:59]
rt v[204:205],v241 offset:59136
rt v[206:207],v241 offset:60160
mm v[88:91],v[212:215],a[40:43],v[88:91]
ar v235,a141
v_perm_b32 v236, v235, v234, s86
mm v[88:91],v[208:211],a[56:59],v[88:91]
ld a[72:75],v246,s[44:47],s54 offen offset:2048 sc1
mm v[120:123],v[208:211],a[60:63],v[120:123]
v_perm_b32 v237, v235, v234, s87
aw a105,v236
mm v[120:123],v[212:215],a[44:47],v[120:123]
rt v[208:209],v242 offset:50688
rt v[210:211],v242 offset:51712
mm v[124:127],v[220:223],a[44:47],v[124:127]
aw a109,v237
ar v230,a145
mm v[124:127],v[216:219],a[60:63],v[124:127]
buffer_load_dwordx2 v[250:251], v248, s[64:67], s5 offen sc1
mm v[92:95],v[216:219],a[56:59],v[92:95]
ar v231,a149
v_perm_b32 v232, v231, v230, s86
mm v[92:95],v[220:223],a[40:43],v[92:95]
rt v[212:213],v242 offset:59136
rt v[214:215],v242 offset:60160
mm v[60:63],v[220:223],a[36:39],v[60:63]
v_perm_b32 v233, v231, v230, s87
aw a106,v232
mm v[60:63],v[216:219],a[52:55],v[60:63]
ld a[36:39],v247,s[44:47],s54 offen offset:2048 sc1
mm v[28:31],v[216:219],a[48:51],v[28:31]
wt vmcnt(8) lgkmcnt(4)
aw a110,v233
mm v[28:31],v[220:223],a[32:35],v[28:31]
rt v[216:217],v243 offset:50688
rt v[218:219],v243 offset:51712
mm v[0:3],v[192:195],a[12:15],v[0:3]
ar v234,a153
ar v235,a157
mm v[0:3],v[196:199],a[16:19],v[0:3]
ld a[32:35],v244,s[44:47],s54 offen offset:3072 sc1
mm v[32:35],v[196:199],a[20:23],v[32:35]
v_perm_b32 v236, v235, v234, s86
v_perm_b32 v237, v235, v234, s87
mm v[32:35],v[192:195],a[0:3],v[32:35]
rt v[220:221],v243 offset:59136
rt v[222:223],v243 offset:60160
mm v[64:67],v[192:195],a[4:7],v[64:67]
aw a107,v236
aw a111,v237
mm v[64:67],v[196:199],a[24:27],v[64:67]
ld a[40:43],v245,s[44:47],s54 offen offset:3072 sc1
mm v[96:99],v[196:199],a[28:31],v[96:99]
ar v230,a130
ar v231,a134
mm v[96:99],v[192:195],a[8:11],v[96:99]
rt v[196:197],v240 offset:50816
rt v[198:199],v240 offset:51840
mm v[100:103],v[200:203],a[8:11],v[100:103]
v_perm_b32 v232, v231, v230, s86
v_perm_b32 v233, v231, v230, s87
mm v[100:103],v[204:207],a[28:31],v[100:103]
ld a[44:47],v246,s[44:47],s54 offen offset:3072 sc1
mm v[68:71],v[204:207],a[24:27],v[68:71]
aw a112,v232
aw a116,v233
mm v[68:71],v[200:203],a[4:7],v[68:71]
rt v[192:193],v240 offset:59264
rt v[194:195],v240 offset:60288
mm v[36:39],v[200:203],a[0:3],v[36:39]
ar v234,a138
ar v235,a142
mm v[36:39],v[204:207],a[20:23],v[36:39]
ld a[48:51],v247,s[44:47],s54 offen offset:3072 sc1
mm v[4:7],v[204:207],a[16:19],v[4:7]
wt lgkmcnt(4)
s_bfe_u32 s54, s4, 262168
mm v[4:7],v[200:203],a[12:15],v[4:7]
rt v[204:205],v241 offset:50816
rt v[206:207],v241 offset:51840
mm v[8:11],v[208:211],a[12:15],v[8:11]
s_add_u32 m0, s83, 104576
s_xor_b32 s54, s54, s72
mm v[8:11],v[212:215],a[16:19],v[8:11]
ld v227,s[56:59],s80 offen lds
mm v[40:43],v[212:215],a[20:23],v[40:43]
s_bitcmp1_b32 vcc_lo, 3
s_cbranch_scc0 .La5_LO_NC3
.La5_LO_KC3:
mm v[40:43],v[208:211],a[0:3],v[40:43]
rt v[200:201],v241 offset:59264
rt v[202:203],v241 offset:60288
mm v[72:75],v[208:211],a[4:7],v[72:75]
s_bfe_u32 s80, s4, 262160
s_xor_b32 s80, s80, s72
mm v[72:75],v[212:215],a[24:27],v[72:75]
ld a[52:55],v244,s[44:47],s55 offen offset:0 sc1
mm v[104:107],v[212:215],a[28:31],v[104:107]
s_lshl_b32 s80, s80, 18
s_lshl_b32 s54, s54, 13
)ASM" R"ASM(mm v[104:107],v[208:211],a[8:11],v[104:107]
rt v[212:213],v242 offset:50816
rt v[214:215],v242 offset:51840
mm v[108:111],v[216:219],a[8:11],v[108:111]
v_perm_b32 v236, v235, v234, s86
v_perm_b32 v237, v235, v234, s87
mm v[108:111],v[220:223],a[28:31],v[108:111]
ld a[56:59],v245,s[44:47],s55 offen offset:0 sc1
mm v[76:79],v[220:223],a[24:27],v[76:79]
aw a113,v236
aw a117,v237
mm v[76:79],v[216:219],a[4:7],v[76:79]
rt v[208:209],v242 offset:59264
rt v[210:211],v242 offset:60288
mm v[44:47],v[216:219],a[0:3],v[44:47]
ar v230,a146
ar v231,a150
mm v[44:47],v[220:223],a[20:23],v[44:47]
v_perm_b32 v232, v231, v230, s86
v_perm_b32 v233, v231, v230, s87
mm v[12:15],v[220:223],a[16:19],v[12:15]
wt lgkmcnt(4)
aw a114,v232
mm v[12:15],v[216:219],a[12:15],v[12:15]
rt v[220:221],v243 offset:50816
rt v[222:223],v243 offset:51840
mm v[16:19],v[196:199],a[12:15],v[16:19]
aw a118,v233
ar v234,a154
mm v[16:19],v[192:195],a[16:19],v[16:19]
ld a[60:63],v246,s[44:47],s55 offen offset:0 sc1
mm v[48:51],v[192:195],a[20:23],v[48:51]
ar v235,a158
v_perm_b32 v236, v235, v234, s86
mm v[48:51],v[196:199],a[0:3],v[48:51]
rt v[216:217],v243 offset:59264
rt v[218:219],v243 offset:60288
mm v[80:83],v[196:199],a[4:7],v[80:83]
v_perm_b32 v237, v235, v234, s87
aw a115,v236
mm v[80:83],v[192:195],a[24:27],v[80:83]
ld a[76:79],v247,s[44:47],s55 offen offset:0 sc1
mm v[112:115],v[192:195],a[28:31],v[112:115]
aw a119,v237
ar v230,a131
mm v[112:115],v[196:199],a[8:11],v[112:115]
rt v[192:193],v240 offset:0
rt v[194:195],v240 offset:1024
mm v[116:119],v[204:207],a[8:11],v[116:119]
ar v231,a135
v_perm_b32 v232, v231, v230, s86
mm v[116:119],v[200:203],a[28:31],v[116:119]
ld a[80:83],v244,s[44:47],s55 offen offset:1024 sc1
mm v[84:87],v[200:203],a[24:27],v[84:87]
v_perm_b32 v233, v231, v230, s87
aw a120,v232
mm v[84:87],v[204:207],a[4:7],v[84:87]
rt v[196:197],v240 offset:8448
rt v[198:199],v240 offset:9472
mm v[52:55],v[204:207],a[0:3],v[52:55]
s_load_dwordx2 s[16:17], s[0:1], 0
s_load_dwordx4 s[36:39], s[0:1], 80
mm v[52:55],v[200:203],a[20:23],v[52:55]
ld a[84:87],v245,s[44:47],s55 offen offset:1024 sc1
mm v[20:23],v[200:203],a[16:19],v[20:23]
wt lgkmcnt(4)
aw a124,v233
mm v[20:23],v[204:207],a[12:15],v[20:23]
rt v[200:201],v241 offset:0
rt v[202:203],v241 offset:1024
mm v[24:27],v[212:215],a[12:15],v[24:27]
ar v234,a139
ar v235,a143
mm v[24:27],v[208:211],a[16:19],v[24:27]
ld a[88:91],v246,s[44:47],s55 offen offset:1024 sc1
mm v[56:59],v[208:211],a[20:23],v[56:59]
v_perm_b32 v236, v235, v234, s86
.La5_LW7_2:
s_cbranch_execz .La5_LW7_6
v_perm_b32 v237, v235, v234, s87
mm v[56:59],v[212:215],a[0:3],v[56:59]
rt v[204:205],v241 offset:8448
rt v[206:207],v241 offset:9472
mm v[88:91],v[212:215],a[4:7],v[88:91]
aw a121,v236
aw a125,v237
mm v[88:91],v[208:211],a[24:27],v[88:91]
ld a[92:95],v247,s[44:47],s55 offen offset:1024 sc1
mm v[120:123],v[208:211],a[28:31],v[120:123]
ar v230,a147
ar v231,a151
mm v[120:123],v[212:215],a[8:11],v[120:123]
rt v[208:209],v242 offset:0
rt v[210:211],v242 offset:1024
mm v[124:127],v[220:223],a[8:11],v[124:127]
s_add_u32 m0, s83, 16896
v_perm_b32 v232, v231, v230, s86
mm v[124:127],v[216:219],a[28:31],v[124:127]
ld v224,s[56:59],s80 offen lds
mm v[92:95],v[216:219],a[24:27],v[92:95]
)ASM" R"ASM(v_perm_b32 v233, v231, v230, s87
aw a122,v232
mm v[92:95],v[220:223],a[4:7],v[92:95]
rt v[212:213],v242 offset:8448
rt v[214:215],v242 offset:9472
mm v[60:63],v[220:223],a[0:3],v[60:63]
aw a126,v233
ar v234,a155
mm v[60:63],v[216:219],a[20:23],v[60:63]
ar v235,a159
v_perm_b32 v236, v235, v234, s86
mm v[28:31],v[216:219],a[16:19],v[28:31]
wt vmcnt(10) lgkmcnt(4)
v_perm_b32 v237, v235, v234, s87
mm v[28:31],v[220:223],a[12:15],v[28:31]
rt v[216:217],v243 offset:0
rt v[218:219],v243 offset:1024
mm v[0:3],v[192:195],a[64:67],v[0:3]
s_add_u32 m0, s83, 17920
aw a123,v236
mm v[0:3],v[196:199],a[32:35],v[0:3]
ld v225,s[56:59],s80 offen lds
mm v[32:35],v[196:199],a[40:43],v[32:35]
aw a127,v237
ar v230,a160
mm v[32:35],v[192:195],a[68:71],v[32:35]
rt v[220:221],v243 offset:8448
rt v[222:223],v243 offset:9472
mm v[64:67],v[192:195],a[72:75],v[64:67]
s_add_u32 m0, s83, 19072
ar v231,a164
mm v[64:67],v[196:199],a[44:47],v[64:67]
ld v226,s[56:59],s80 offen lds
mm v[96:99],v[196:199],a[48:51],v[96:99]
v_perm_b32 v232, v231, v230, s86
v_perm_b32 v233, v231, v230, s87
mm v[96:99],v[192:195],a[36:39],v[96:99]
rt v[196:197],v240 offset:128
rt v[198:199],v240 offset:1152
mm v[100:103],v[200:203],a[36:39],v[100:103]
aw a128,v232
aw a132,v233
mm v[100:103],v[204:207],a[48:51],v[100:103]
ld v[136:139],v244,s[44:47],s55 offen offset:2048 sc1
mm v[68:71],v[204:207],a[44:47],v[68:71]
ar v234,a168
ar v235,a172
mm v[68:71],v[200:203],a[72:75],v[68:71]
rt v[192:193],v240 offset:8576
rt v[194:195],v240 offset:9600
mm v[36:39],v[200:203],a[68:71],v[36:39]
v_perm_b32 v236, v235, v234, s86
v_perm_b32 v237, v235, v234, s87
mm v[36:39],v[204:207],a[40:43],v[36:39]
ld v[140:143],v245,s[44:47],s55 offen offset:2048 sc1
mm v[4:7],v[204:207],a[32:35],v[4:7]
wt lgkmcnt(4)
aw a129,v236
mm v[4:7],v[200:203],a[64:67],v[4:7]
rt v[204:205],v241 offset:128
rt v[206:207],v241 offset:1152
mm v[8:11],v[208:211],a[64:67],v[8:11]
aw a133,v237
ar v230,a176
mm v[8:11],v[212:215],a[32:35],v[8:11]
ld v[152:155],v246,s[44:47],s55 offen offset:2048 sc1
mm v[40:43],v[212:215],a[40:43],v[40:43]
ar v231,a180
v_perm_b32 v232, v231, v230, s86
mm v[40:43],v[208:211],a[68:71],v[40:43]
rt v[200:201],v241 offset:8576
rt v[202:203],v241 offset:9600
mm v[72:75],v[208:211],a[72:75],v[72:75]
v_perm_b32 v233, v231, v230, s87
aw a130,v232
mm v[72:75],v[212:215],a[44:47],v[72:75]
ld v[156:159],v247,s[44:47],s55 offen offset:2048 sc1
mm v[104:107],v[212:215],a[48:51],v[104:107]
aw a134,v233
ar v234,a184
mm v[104:107],v[208:211],a[36:39],v[104:107]
rt v[212:213],v242 offset:128
rt v[214:215],v242 offset:1152
mm v[108:111],v[216:219],a[36:39],v[108:111]
ar v235,a188
v_perm_b32 v236, v235, v234, s86
mm v[108:111],v[220:223],a[48:51],v[108:111]
ld v[168:171],v244,s[44:47],s55 offen offset:3072 sc1
mm v[76:79],v[220:223],a[44:47],v[76:79]
v_perm_b32 v237, v235, v234, s87
aw a131,v236
mm v[76:79],v[216:219],a[72:75],v[76:79]
rt v[208:209],v242 offset:8576
rt v[210:211],v242 offset:9600
mm v[44:47],v[216:219],a[68:71],v[44:47]
aw a135,v237
ar v230,a161
mm v[44:47],v[220:223],a[40:43],v[44:47]
s_nop 0
v_cmp_eq_u64 vcc, v[250:251], v[252:253]
)ASM" R"ASM(mm v[12:15],v[220:223],a[32:35],v[12:15]
wt lgkmcnt(4)
ar v231,a165
mm v[12:15],v[216:219],a[64:67],v[12:15]
rt v[220:221],v243 offset:128
rt v[222:223],v243 offset:1152
mm v[16:19],v[196:199],a[64:67],v[16:19]
v_perm_b32 v232, v231, v230, s86
v_perm_b32 v233, v231, v230, s87
mm v[16:19],v[192:195],a[32:35],v[16:19]
ld v[172:175],v245,s[44:47],s55 offen offset:3072 sc1
mm v[48:51],v[192:195],a[40:43],v[48:51]
aw a136,v232
aw a140,v233
mm v[48:51],v[196:199],a[68:71],v[48:51]
rt v[216:217],v243 offset:8576
rt v[218:219],v243 offset:9600
mm v[80:83],v[196:199],a[72:75],v[80:83]
ar v234,a169
ar v235,a173
mm v[80:83],v[192:195],a[44:47],v[80:83]
ld v[184:187],v246,s[44:47],s55 offen offset:3072 sc1
mm v[112:115],v[192:195],a[48:51],v[112:115]
wt vmcnt(18) lgkmcnt(0)
s_barrier
mm v[112:115],v[196:199],a[36:39],v[112:115]
rt v[192:193],v240 offset:16896
rt v[194:195],v240 offset:17920
mm v[116:119],v[204:207],a[36:39],v[116:119]
s_xor_b32 s90, s72, 0
s_lshl_b32 s92, s76, 5
mm v[116:119],v[200:203],a[48:51],v[116:119]
buffer_load_dwordx2 v[250:251], v248, s[64:67], s5 offen sc1
mm v[84:87],v[200:203],a[44:47],v[84:87]
s_add_u32 s92, s92, s90
s_lshl2_add_u32 s92, s92, s3
mm v[84:87],v[204:207],a[72:75],v[84:87]
rt v[196:197],v240 offset:25344
rt v[198:199],v240 offset:26368
mm v[52:55],v[204:207],a[68:71],v[52:55]
s_lshl_b32 s92, s92, 12
v_perm_b32 v236, v235, v234, s86
mm v[52:55],v[200:203],a[40:43],v[52:55]
ld v[188:191],v247,s[44:47],s55 offen offset:3072 sc1
mm v[20:23],v[200:203],a[32:35],v[20:23]
s_nop 0
s_bfe_u32 s55, s4, 262172
mm v[20:23],v[204:207],a[64:67],v[20:23]
rt v[200:201],v241 offset:16896
rt v[202:203],v241 offset:17920
mm v[24:27],v[212:215],a[64:67],v[24:27]
s_add_u32 m0, s83, 20096
s_xor_b32 s55, s55, s72
mm v[24:27],v[208:211],a[32:35],v[24:27]
ld v227,s[56:59],s80 offen lds
mm v[56:59],v[208:211],a[40:43],v[56:59]
s_add_u32 s80, s80, 131072
s_lshl_b32 s55, s55, 13
mm v[56:59],v[212:215],a[68:71],v[56:59]
rt v[204:205],v241 offset:25344
rt v[206:207],v241 offset:26368
mm v[88:91],v[212:215],a[72:75],v[88:91]
s_add_u32 m0, s83, 33792
v_perm_b32 v237, v235, v234, s87
mm v[88:91],v[208:211],a[44:47],v[88:91]
ld v224,s[56:59],s80 offen lds
mm v[120:123],v[208:211],a[48:51],v[120:123]
s_add_u32 s68, s16, s92
s_addc_u32 s69, s17, 0
mm v[120:123],v[212:215],a[36:39],v[120:123]
rt v[208:209],v242 offset:16896
rt v[210:211],v242 offset:17920
mm v[124:127],v[220:223],a[36:39],v[124:127]
s_add_u32 m0, s83, 34816
s_and_b32 s69, s69, 0xffff
mm v[124:127],v[216:219],a[48:51],v[124:127]
ld v225,s[56:59],s80 offen lds
mm v[92:95],v[216:219],a[44:47],v[92:95]
s_mov_b32 s70, 4096
s_mov_b32 s71, 0x20000
mm v[92:95],v[220:223],a[72:75],v[92:95]
rt v[212:213],v242 offset:25344
rt v[214:215],v242 offset:26368
mm v[60:63],v[220:223],a[68:71],v[60:63]
s_add_u32 m0, s83, 35968
aw a137,v236
mm v[60:63],v[216:219],a[40:43],v[60:63]
ld v226,s[56:59],s80 offen lds
mm v[28:31],v[216:219],a[32:35],v[28:31]
wt vmcnt(16) lgkmcnt(4)
aw a141,v237
mm v[28:31],v[220:223],a[64:67],v[28:31]
rt v[216:217],v243 offset:16896
rt v[218:219],v243 offset:17920
mm v[0:3],v[192:195],a[52:55],v[0:3]
s_add_u32 m0, s83, 36992
ar v230,a177
mm v[0:3],v[196:199],a[80:83],v[0:3]
ld v227,s[56:59],s80 offen lds
mm v[32:35],v[196:199],a[84:87],v[32:35]
)ASM" R"ASM(s_bitcmp1_b32 vcc_lo, 4
s_cbranch_scc0 .La5_LO_NC4
.La5_LO_KC4:
mm v[32:35],v[192:195],a[56:59],v[32:35]
rt v[220:221],v243 offset:25344
rt v[222:223],v243 offset:26368
mm v[64:67],v[192:195],a[60:63],v[64:67]
s_bfe_u32 s80, s4, 262164
s_xor_b32 s80, s80, s72
mm v[64:67],v[196:199],a[88:91],v[64:67]
ld a[0:3],v244,s[44:47],s52 offen offset:0 sc1
mm v[96:99],v[196:199],a[92:95],v[96:99]
s_lshl_b32 s80, s80, 18
ar v231,a181
mm v[96:99],v[192:195],a[76:79],v[96:99]
rt v[196:197],v240 offset:17024
rt v[198:199],v240 offset:18048
mm v[100:103],v[200:203],a[76:79],v[100:103]
s_add_u32 s48, s36, 0
s_addc_u32 s49, s37, 0
mm v[100:103],v[204:207],a[92:95],v[100:103]
ld a[4:7],v245,s[44:47],s52 offen offset:0 sc1
mm v[68:71],v[204:207],a[88:91],v[68:71]
s_and_b32 s49, s49, 0xffff
s_mov_b32 s50, 2048
mm v[68:71],v[200:203],a[60:63],v[68:71]
rt v[192:193],v240 offset:25472
rt v[194:195],v240 offset:26496
mm v[36:39],v[200:203],a[56:59],v[36:39]
s_mov_b32 s51, 0x20000
v_and_b32 v238, 0xf0, v255
mm v[36:39],v[204:207],a[84:87],v[36:39]
ld a[8:11],v246,s[44:47],s52 offen offset:0 sc1
mm v[4:7],v[204:207],a[80:83],v[4:7]
wt lgkmcnt(4)
v_lshlrev_b32 v238, 2, v238
mm v[4:7],v[200:203],a[52:55],v[4:7]
rt v[204:205],v241 offset:17024
rt v[206:207],v241 offset:18048
mm v[8:11],v[208:211],a[52:55],v[8:11]
v_bfe_u32 v249, v255, 8, 1
v_lshl_add_u32 v238, v249, 5, v238
mm v[8:11],v[212:215],a[80:83],v[8:11]
ld a[12:15],v247,s[44:47],s52 offen offset:0 sc1
mm v[40:43],v[212:215],a[84:87],v[40:43]
v_lshrrev_b32_e64 v249, 9, v255
v_lshl_add_u32 v238, v249, 4, v238
mm v[40:43],v[208:211],a[56:59],v[40:43]
rt v[200:201],v241 offset:25472
rt v[202:203],v241 offset:26496
mm v[72:75],v[208:211],a[60:63],v[72:75]
s_lshr_b32 s23, s72, 3
s_lshl3_add_u32 s92, s23, s76
mm v[72:75],v[212:215],a[88:91],v[72:75]
ld a[16:19],v244,s[44:47],s52 offen offset:1024 sc1
mm v[104:107],v[212:215],a[92:95],v[104:107]
s_lshl_b32 s92, s92, 19
s_lshl_b32 s97, s3, 16
mm v[104:107],v[208:211],a[76:79],v[104:107]
rt v[212:213],v242 offset:17024
rt v[214:215],v242 offset:18048
mm v[108:111],v[216:219],a[76:79],v[108:111]
s_add_u32 s94, s92, s97
s_add_u32 s95, s94, 32768
mm v[108:111],v[220:223],a[92:95],v[108:111]
ld a[20:23],v245,s[44:47],s52 offen offset:1024 sc1
mm v[76:79],v[220:223],a[88:91],v[76:79]
s_sub_u32 s96, s92, s97
s_add_u32 s96, s96, 458752
mm v[76:79],v[216:219],a[60:63],v[76:79]
rt v[208:209],v242 offset:25472
rt v[210:211],v242 offset:26496
mm v[44:47],v[216:219],a[56:59],v[44:47]
s_add_u32 s97, s96, 32768
s_lshl3_add_u32 s92, s23, s101
mm v[44:47],v[220:223],a[84:87],v[44:47]
s_lshl_b32 s85, s76, 15
s_lshl3_add_u32 s92, s92, s85
mm v[12:15],v[220:223],a[80:83],v[12:15]
wt lgkmcnt(4)
s_add_u32 s92, s92, 2097152
mm v[12:15],v[216:219],a[52:55],v[12:15]
rt v[220:221],v243 offset:17024
rt v[222:223],v243 offset:18048
mm v[16:19],v[196:199],a[52:55],v[16:19]
v_perm_b32 v232, v231, v230, s86
v_perm_b32 v233, v231, v230, s87
mm v[16:19],v[192:195],a[80:83],v[16:19]
ld a[24:27],v246,s[44:47],s52 offen offset:1024 sc1
mm v[48:51],v[192:195],a[84:87],v[48:51]
aw a138,v232
aw a142,v233
mm v[48:51],v[196:199],a[56:59],v[48:51]
rt v[216:217],v243 offset:25472
rt v[218:219],v243 offset:26496
mm v[80:83],v[196:199],a[60:63],v[80:83]
ar v234,a185
ar v235,a189
mm v[80:83],v[192:195],a[88:91],v[80:83]
ld a[28:31],v247,s[44:47],s52 offen offset:1024 sc1
)ASM" R"ASM(mm v[112:115],v[192:195],a[92:95],v[112:115]
v_perm_b32 v236, v235, v234, s86
v_perm_b32 v237, v235, v234, s87
mm v[112:115],v[196:199],a[76:79],v[112:115]
rt v[192:193],v240 offset:33792
rt v[194:195],v240 offset:34816
mm v[116:119],v[204:207],a[76:79],v[116:119]
s_add_u32 m0, s83, 50688
aw a139,v236
mm v[116:119],v[200:203],a[92:95],v[116:119]
ld v224,s[56:59],s80 offen lds
mm v[84:87],v[200:203],a[88:91],v[84:87]
aw a143,v237
ar v230,a162
mm v[84:87],v[204:207],a[60:63],v[84:87]
rt v[196:197],v240 offset:42240
rt v[198:199],v240 offset:43264
mm v[52:55],v[204:207],a[56:59],v[52:55]
s_add_u32 m0, s83, 51712
ar v231,a166
mm v[52:55],v[200:203],a[84:87],v[52:55]
ld v225,s[56:59],s80 offen lds
mm v[20:23],v[200:203],a[80:83],v[20:23]
wt lgkmcnt(4)
v_perm_b32 v232, v231, v230, s86
mm v[20:23],v[204:207],a[52:55],v[20:23]
rt v[200:201],v241 offset:33792
rt v[202:203],v241 offset:34816
mm v[24:27],v[212:215],a[52:55],v[24:27]
s_add_u32 m0, s83, 52864
v_perm_b32 v233, v231, v230, s87
mm v[24:27],v[208:211],a[80:83],v[24:27]
ld v226,s[56:59],s80 offen lds
mm v[56:59],v[208:211],a[84:87],v[56:59]
aw a144,v232
aw a148,v233
mm v[56:59],v[212:215],a[56:59],v[56:59]
rt v[204:205],v241 offset:42240
rt v[206:207],v241 offset:43264
mm v[88:91],v[212:215],a[60:63],v[88:91]
ar v234,a170
ar v235,a174
mm v[88:91],v[208:211],a[88:91],v[88:91]
ld a[32:35],v244,s[44:47],s52 offen offset:2048 sc1
mm v[120:123],v[208:211],a[92:95],v[120:123]
v_perm_b32 v236, v235, v234, s86
v_perm_b32 v237, v235, v234, s87
mm v[120:123],v[212:215],a[76:79],v[120:123]
rt v[208:209],v242 offset:33792
rt v[210:211],v242 offset:34816
mm v[124:127],v[220:223],a[76:79],v[124:127]
aw a145,v236
aw a149,v237
mm v[124:127],v[216:219],a[92:95],v[124:127]
ld a[36:39],v245,s[44:47],s52 offen offset:2048 sc1
mm v[92:95],v[216:219],a[88:91],v[92:95]
ar v230,a178
ar v231,a182
mm v[92:95],v[220:223],a[60:63],v[92:95]
rt v[212:213],v242 offset:42240
rt v[214:215],v242 offset:43264
mm v[60:63],v[220:223],a[56:59],v[60:63]
v_perm_b32 v232, v231, v230, s86
v_perm_b32 v233, v231, v230, s87
mm v[60:63],v[216:219],a[84:87],v[60:63]
aw a146,v232
aw a150,v233
mm v[28:31],v[216:219],a[80:83],v[28:31]
wt vmcnt(18) lgkmcnt(4)
ar v234,a186
mm v[28:31],v[220:223],a[52:55],v[28:31]
rt v[216:217],v243 offset:33792
rt v[218:219],v243 offset:34816
mm v[0:3],v[192:195],v[136:139],v[0:3]
ar v235,a190
v_perm_b32 v236, v235, v234, s86
mm v[0:3],v[196:199],v[168:171],v[0:3]
ld a[40:43],v246,s[44:47],s52 offen offset:2048 sc1
mm v[32:35],v[196:199],v[172:175],v[32:35]
v_perm_b32 v237, v235, v234, s87
aw a147,v236
mm v[32:35],v[192:195],v[140:143],v[32:35]
rt v[220:221],v243 offset:42240
rt v[222:223],v243 offset:43264
mm v[64:67],v[192:195],v[152:155],v[64:67]
aw a151,v237
ar v230,a163
mm v[64:67],v[196:199],v[184:187],v[64:67]
ld a[44:47],v247,s[44:47],s52 offen offset:2048 sc1
mm v[96:99],v[196:199],v[188:191],v[96:99]
ar v231,a167
v_perm_b32 v232, v231, v230, s86
mm v[96:99],v[192:195],v[156:159],v[96:99]
rt v[196:197],v240 offset:33920
rt v[198:199],v240 offset:34944
mm v[100:103],v[200:203],v[156:159],v[100:103]
v_perm_b32 v233, v231, v230, s87
aw a152,v232
mm v[100:103],v[204:207],v[188:191],v[100:103]
ld a[48:51],v244,s[44:47],s52 offen offset:3072 sc1
)ASM" R"ASM(mm v[68:71],v[204:207],v[184:187],v[68:71]
aw a156,v233
ar v234,a171
mm v[68:71],v[200:203],v[152:155],v[68:71]
rt v[192:193],v240 offset:42368
rt v[194:195],v240 offset:43392
mm v[36:39],v[200:203],v[140:143],v[36:39]
v_add_u32 v240, 4294899712, v240
ar v235,a175
mm v[36:39],v[204:207],v[172:175],v[36:39]
ld a[52:55],v245,s[44:47],s52 offen offset:3072 sc1
mm v[4:7],v[204:207],v[168:171],v[4:7]
wt lgkmcnt(4)
v_perm_b32 v236, v235, v234, s86
mm v[4:7],v[200:203],v[136:139],v[4:7]
rt v[204:205],v241 offset:33920
rt v[206:207],v241 offset:34944
mm v[8:11],v[208:211],v[136:139],v[8:11]
s_nop 0
v_cmp_eq_u64 vcc, v[250:251], v[252:253]
mm v[8:11],v[212:215],v[168:171],v[8:11]
ld a[56:59],v246,s[44:47],s52 offen offset:3072 sc1
mm v[40:43],v[212:215],v[172:175],v[40:43]
v_perm_b32 v237, v235, v234, s87
aw a153,v236
mm v[40:43],v[208:211],v[140:143],v[40:43]
rt v[200:201],v241 offset:42368
rt v[202:203],v241 offset:43392
mm v[72:75],v[208:211],v[152:155],v[72:75]
v_add_u32 v241, 4294899712, v241
aw a157,v237
mm v[72:75],v[212:215],v[184:187],v[72:75]
ld a[60:63],v247,s[44:47],s52 offen offset:3072 sc1
mm v[104:107],v[212:215],v[188:191],v[104:107]
ar v230,a179
ar v231,a183
mm v[104:107],v[208:211],v[156:159],v[104:107]
rt v[212:213],v242 offset:33920
rt v[214:215],v242 offset:34944
mm v[108:111],v[216:219],v[156:159],v[108:111]
v_perm_b32 v232, v231, v230, s86
v_perm_b32 v233, v231, v230, s87
mm v[108:111],v[220:223],v[188:191],v[108:111]
buffer_load_dwordx2 v[250:251], v248, s[64:67], s5 offen sc1
mm v[76:79],v[220:223],v[184:187],v[76:79]
aw a154,v232
aw a158,v233
mm v[76:79],v[216:219],v[152:155],v[76:79]
rt v[208:209],v242 offset:42368
rt v[210:211],v242 offset:43392
mm v[44:47],v[216:219],v[140:143],v[44:47]
s_add_u32 m0, s83, 53888
v_add_u32 v242, 4294899712, v242
mm v[44:47],v[220:223],v[172:175],v[44:47]
ld v227,s[56:59],s80 offen lds
mm v[12:15],v[220:223],v[168:171],v[12:15]
wt lgkmcnt(4)
s_bfe_u32 s80, s4, 262168
mm v[12:15],v[216:219],v[136:139],v[12:15]
rt v[220:221],v243 offset:33920
rt v[222:223],v243 offset:34944
mm v[16:19],v[196:199],v[136:139],v[16:19]
s_add_u32 m0, s83, 118272
s_xor_b32 s80, s80, s72
s_lshl_b32 s80, s80, 18
mm v[16:19],v[192:195],v[168:171],v[16:19]
ld v224,s[56:59],s80 offen lds
mm v[48:51],v[192:195],v[172:175],v[48:51]
ar v234,a187
ar v235,a191
mm v[48:51],v[196:199],v[140:143],v[48:51]
rt v[216:217],v243 offset:42368
rt v[218:219],v243 offset:43392
mm v[80:83],v[196:199],v[152:155],v[80:83]
s_add_u32 m0, s83, 119296
v_add_u32 v243, 4294899712, v243
mm v[80:83],v[192:195],v[184:187],v[80:83]
ld v225,s[56:59],s80 offen lds
mm v[112:115],v[192:195],v[188:191],v[112:115]
wt vmcnt(23) lgkmcnt(0)
s_barrier
mm v[112:115],v[196:199],v[156:159],v[112:115]
rt v[192:193],v240 offset:16896
rt v[194:195],v240 offset:17920
mm v[116:119],v[204:207],v[156:159],v[116:119]
s_add_u32 m0, s83, 120448
v_perm_b32 v236, v235, v234, s86
mm v[116:119],v[200:203],v[188:191],v[116:119]
ld v226,s[56:59],s80 offen lds
mm v[84:87],v[200:203],v[184:187],v[84:87]
s_bitcmp1_b32 vcc_lo, 5
s_cbranch_scc0 .La5_LO_NC5
.La5_LO_KC5:
mm v[84:87],v[204:207],v[152:155],v[84:87]
rt v[196:197],v240 offset:25344
rt v[198:199],v240 offset:26368
mm v[52:55],v[204:207],v[140:143],v[52:55]
v_perm_b32 v237, v235, v234, s87
aw a155,v236
mm v[52:55],v[200:203],v[172:175],v[52:55]
)ASM" R"ASM(ld v[128:131],v244,s[44:47],s53 offen offset:0 sc1
mm v[20:23],v[200:203],v[168:171],v[20:23]
s_nop 0
aw a159,v237
mm v[20:23],v[204:207],v[136:139],v[20:23]
rt v[200:201],v241 offset:16896
rt v[202:203],v241 offset:17920
mm v[24:27],v[212:215],v[136:139],v[24:27]
ar v230,a192
ar v231,a196
mm v[24:27],v[208:211],v[168:171],v[24:27]
ld v[132:135],v245,s[44:47],s53 offen offset:0 sc1
mm v[56:59],v[208:211],v[172:175],v[56:59]
v_perm_b32 v232, v231, v230, s86
v_perm_b32 v233, v231, v230, s87
mm v[56:59],v[212:215],v[140:143],v[56:59]
rt v[204:205],v241 offset:25344
.La5_LW7_3:
s_cbranch_execz .La5_LW7_7
rt v[206:207],v241 offset:26368
mm v[88:91],v[212:215],v[152:155],v[88:91]
aw a160,v232
aw a164,v233
mm v[88:91],v[208:211],v[184:187],v[88:91]
ld v[144:147],v246,s[44:47],s53 offen offset:0 sc1
mm v[120:123],v[208:211],v[188:191],v[120:123]
ar v234,a200
ar v235,a204
mm v[120:123],v[212:215],v[156:159],v[120:123]
rt v[208:209],v242 offset:16896
rt v[210:211],v242 offset:17920
mm v[124:127],v[220:223],v[156:159],v[124:127]
v_perm_b32 v236, v235, v234, s86
v_perm_b32 v237, v235, v234, s87
mm v[124:127],v[216:219],v[188:191],v[124:127]
ld v[156:159],v247,s[44:47],s53 offen offset:0 sc1
mm v[92:95],v[216:219],v[184:187],v[92:95]
aw a161,v236
aw a165,v237
mm v[92:95],v[220:223],v[152:155],v[92:95]
rt v[212:213],v242 offset:25344
rt v[214:215],v242 offset:26368
mm v[60:63],v[220:223],v[140:143],v[60:63]
ar v230,a208
ar v231,a212
mm v[60:63],v[216:219],v[172:175],v[60:63]
v_perm_b32 v232, v231, v230, s86
v_perm_b32 v233, v231, v230, s87
mm v[28:31],v[216:219],v[168:171],v[28:31]
wt vmcnt(20) lgkmcnt(4)
aw a162,v232
mm v[28:31],v[220:223],v[136:139],v[28:31]
rt v[216:217],v243 offset:16896
rt v[218:219],v243 offset:17920
mm v[0:3],v[192:195],a[0:3],v[0:3]
aw a166,v233
ar v234,a216
mm v[0:3],v[196:199],a[16:19],v[0:3]
ld v[136:139],v244,s[44:47],s53 offen offset:1024 sc1
mm v[32:35],v[196:199],a[20:23],v[32:35]
ar v235,a220
v_perm_b32 v236, v235, v234, s86
mm v[32:35],v[192:195],a[4:7],v[32:35]
rt v[220:221],v243 offset:25344
rt v[222:223],v243 offset:26368
mm v[64:67],v[192:195],a[8:11],v[64:67]
v_perm_b32 v237, v235, v234, s87
aw a163,v236
mm v[64:67],v[196:199],a[24:27],v[64:67]
ld v[140:143],v245,s[44:47],s53 offen offset:1024 sc1
mm v[96:99],v[196:199],a[28:31],v[96:99]
aw a167,v237
ar v230,a193
mm v[96:99],v[192:195],a[12:15],v[96:99]
rt v[196:197],v240 offset:17024
rt v[198:199],v240 offset:18048
mm v[100:103],v[200:203],a[12:15],v[100:103]
ar v231,a197
v_perm_b32 v232, v231, v230, s86
mm v[100:103],v[204:207],a[28:31],v[100:103]
ld v[152:155],v246,s[44:47],s53 offen offset:1024 sc1
mm v[68:71],v[204:207],a[24:27],v[68:71]
v_perm_b32 v233, v231, v230, s87
aw a168,v232
mm v[68:71],v[200:203],a[8:11],v[68:71]
rt v[192:193],v240 offset:25472
rt v[194:195],v240 offset:26496
mm v[36:39],v[200:203],a[4:7],v[36:39]
aw a172,v233
ar v234,a201
mm v[36:39],v[204:207],a[20:23],v[36:39]
ld v[168:171],v247,s[44:47],s53 offen offset:1024 sc1
mm v[4:7],v[204:207],a[16:19],v[4:7]
wt lgkmcnt(4)
ar v235,a205
mm v[4:7],v[200:203],a[0:3],v[4:7]
rt v[204:205],v241 offset:17024
rt v[206:207],v241 offset:18048
mm v[8:11],v[208:211],a[0:3],v[8:11]
s_add_u32 m0, s83, 121472
)ASM" R"ASM(v_perm_b32 v236, v235, v234, s86
mm v[8:11],v[212:215],a[16:19],v[8:11]
ld v227,s[56:59],s80 offen lds
mm v[40:43],v[212:215],a[20:23],v[40:43]
s_add_u32 s80, s80, 131072
v_perm_b32 v237, v235, v234, s87
mm v[40:43],v[208:211],a[4:7],v[40:43]
rt v[200:201],v241 offset:25472
rt v[202:203],v241 offset:26496
mm v[72:75],v[208:211],a[8:11],v[72:75]
s_add_u32 m0, s83, 67584
aw a169,v236
mm v[72:75],v[212:215],a[24:27],v[72:75]
ld v224,s[56:59],s80 offen lds
mm v[104:107],v[212:215],a[28:31],v[104:107]
aw a173,v237
ar v230,a209
mm v[104:107],v[208:211],a[12:15],v[104:107]
rt v[212:213],v242 offset:17024
rt v[214:215],v242 offset:18048
mm v[108:111],v[216:219],a[12:15],v[108:111]
s_add_u32 m0, s83, 68608
ar v231,a213
mm v[108:111],v[220:223],a[28:31],v[108:111]
ld v225,s[56:59],s80 offen lds
mm v[76:79],v[220:223],a[24:27],v[76:79]
v_perm_b32 v232, v231, v230, s86
v_perm_b32 v233, v231, v230, s87
mm v[76:79],v[216:219],a[8:11],v[76:79]
rt v[208:209],v242 offset:25472
rt v[210:211],v242 offset:26496
mm v[44:47],v[216:219],a[4:7],v[44:47]
aw a170,v232
aw a174,v233
mm v[44:47],v[220:223],a[20:23],v[44:47]
ar v234,a217
ar v235,a221
mm v[12:15],v[220:223],a[16:19],v[12:15]
wt lgkmcnt(4)
v_perm_b32 v236, v235, v234, s86
mm v[12:15],v[216:219],a[0:3],v[12:15]
rt v[220:221],v243 offset:17024
rt v[222:223],v243 offset:18048
mm v[16:19],v[196:199],a[0:3],v[16:19]
s_add_u32 m0, s83, 69760
v_perm_b32 v237, v235, v234, s87
mm v[16:19],v[192:195],a[16:19],v[16:19]
ld v226,s[56:59],s80 offen lds
mm v[48:51],v[192:195],a[20:23],v[48:51]
aw a171,v236
aw a175,v237
mm v[48:51],v[196:199],a[4:7],v[48:51]
rt v[216:217],v243 offset:25472
rt v[218:219],v243 offset:26496
mm v[80:83],v[196:199],a[8:11],v[80:83]
s_add_u32 m0, s83, 70784
ar v230,a194
mm v[80:83],v[192:195],a[24:27],v[80:83]
ld v227,s[56:59],s80 offen lds
mm v[112:115],v[192:195],a[28:31],v[112:115]
s_bfe_u32 s80, s4, 262172
s_xor_b32 s80, s80, s72
mm v[112:115],v[196:199],a[12:15],v[112:115]
rt v[192:193],v240 offset:33792
rt v[194:195],v240 offset:34816
mm v[116:119],v[204:207],a[12:15],v[116:119]
s_add_u32 m0, s83, 84480
s_lshl_b32 s80, s80, 18
mm v[116:119],v[200:203],a[28:31],v[116:119]
ld v224,s[56:59],s80 offen lds
mm v[84:87],v[200:203],a[24:27],v[84:87]
ar v231,a198
v_perm_b32 v232, v231, v230, s86
mm v[84:87],v[204:207],a[8:11],v[84:87]
rt v[196:197],v240 offset:42240
rt v[198:199],v240 offset:43264
mm v[52:55],v[204:207],a[4:7],v[52:55]
s_add_u32 m0, s83, 85504
v_perm_b32 v233, v231, v230, s87
mm v[52:55],v[200:203],a[20:23],v[52:55]
ld v225,s[56:59],s80 offen lds
mm v[20:23],v[200:203],a[16:19],v[20:23]
wt lgkmcnt(4)
aw a176,v232
mm v[20:23],v[204:207],a[0:3],v[20:23]
rt v[200:201],v241 offset:33792
rt v[202:203],v241 offset:34816
mm v[24:27],v[212:215],a[0:3],v[24:27]
s_add_u32 m0, s83, 86656
aw a180,v233
mm v[24:27],v[208:211],a[16:19],v[24:27]
ld v226,s[56:59],s80 offen lds
mm v[56:59],v[208:211],a[20:23],v[56:59]
ar v234,a202
ar v235,a206
mm v[56:59],v[212:215],a[4:7],v[56:59]
rt v[204:205],v241 offset:42240
rt v[206:207],v241 offset:43264
mm v[88:91],v[212:215],a[8:11],v[88:91]
s_add_u32 m0, s83, 87680
)ASM" R"ASM(v_perm_b32 v236, v235, v234, s86
mm v[88:91],v[208:211],a[24:27],v[88:91]
ld v227,s[56:59],s80 offen lds
mm v[120:123],v[208:211],a[28:31],v[120:123]
s_add_u32 s80, s80, 131072
v_perm_b32 v237, v235, v234, s87
mm v[120:123],v[212:215],a[12:15],v[120:123]
rt v[208:209],v242 offset:33792
rt v[210:211],v242 offset:34816
mm v[124:127],v[220:223],a[12:15],v[124:127]
s_add_u32 m0, s83, 101376
aw a177,v236
mm v[124:127],v[216:219],a[28:31],v[124:127]
ld v224,s[56:59],s80 offen lds
mm v[92:95],v[216:219],a[24:27],v[92:95]
aw a181,v237
ar v230,a210
mm v[92:95],v[220:223],a[8:11],v[92:95]
rt v[212:213],v242 offset:42240
rt v[214:215],v242 offset:43264
mm v[60:63],v[220:223],a[4:7],v[60:63]
ar v231,a214
v_perm_b32 v232, v231, v230, s86
mm v[60:63],v[216:219],a[20:23],v[60:63]
wt vmcnt(22)
v_cmp_eq_u64 vcc, v[250:251], v[252:253]
mm v[28:31],v[216:219],a[16:19],v[28:31]
wt lgkmcnt(4)
v_perm_b32 v233, v231, v230, s87
mm v[28:31],v[220:223],a[0:3],v[28:31]
rt v[216:217],v243 offset:33792
rt v[218:219],v243 offset:34816
mm v[0:3],v[192:195],a[32:35],v[0:3]
s_add_u32 m0, s83, 102400
aw a178,v232
mm v[0:3],v[196:199],a[48:51],v[0:3]
ld v225,s[56:59],s80 offen lds
mm v[32:35],v[196:199],a[52:55],v[32:35]
aw a182,v233
ar v234,a218
mm v[32:35],v[192:195],a[36:39],v[32:35]
rt v[220:221],v243 offset:42240
rt v[222:223],v243 offset:43264
mm v[64:67],v[192:195],a[40:43],v[64:67]
s_add_u32 m0, s83, 103552
ar v235,a222
mm v[64:67],v[196:199],a[56:59],v[64:67]
ld v226,s[56:59],s80 offen lds
mm v[96:99],v[196:199],a[60:63],v[96:99]
v_perm_b32 v236, v235, v234, s86
v_perm_b32 v237, v235, v234, s87
mm v[96:99],v[192:195],a[44:47],v[96:99]
rt v[196:197],v240 offset:33920
rt v[198:199],v240 offset:34944
mm v[100:103],v[200:203],a[44:47],v[100:103]
s_add_u32 m0, s83, 104576
aw a179,v236
mm v[100:103],v[204:207],a[60:63],v[100:103]
ld v227,s[56:59],s80 offen lds
mm v[68:71],v[204:207],a[56:59],v[68:71]
s_bitcmp1_b32 vcc_lo, 6
s_cbranch_scc0 .La5_LO_NC6
.La5_LO_KC6:
mm v[68:71],v[200:203],a[40:43],v[68:71]
rt v[192:193],v240 offset:42368
rt v[194:195],v240 offset:43392
mm v[36:39],v[200:203],a[36:39],v[36:39]
v_bfe_u32 v224, v255, 6, 1
v_lshrrev_b32 v225, 7, v255
mm v[36:39],v[204:207],a[52:55],v[36:39]
ld a[64:67],v244,s[44:47],s54 offen offset:0 sc1
mm v[4:7],v[204:207],a[48:51],v[4:7]
wt lgkmcnt(4)
v_and_b32 v226, 48, v255
mm v[4:7],v[200:203],a[32:35],v[4:7]
rt v[204:205],v241 offset:33920
rt v[206:207],v241 offset:34944
mm v[8:11],v[208:211],a[32:35],v[8:11]
v_lshl_add_u32 v225, v225, 6, v226
v_lshl_add_u32 v224, v224, 11, v225
mm v[8:11],v[212:215],a[48:51],v[8:11]
ld a[68:71],v245,s[44:47],s54 offen offset:0 sc1
mm v[40:43],v[212:215],a[52:55],v[40:43]
aw a183,v237
ar v230,a195
mm v[40:43],v[208:211],a[36:39],v[40:43]
rt v[200:201],v241 offset:42368
rt v[202:203],v241 offset:43392
mm v[72:75],v[208:211],a[40:43],v[72:75]
ar v231,a199
v_perm_b32 v232, v231, v230, s86
mm v[72:75],v[212:215],a[56:59],v[72:75]
ld a[72:75],v244,s[44:47],s54 offen offset:1024 sc1
mm v[104:107],v[212:215],a[60:63],v[104:107]
v_perm_b32 v233, v231, v230, s87
aw a184,v232
mm v[104:107],v[208:211],a[44:47],v[104:107]
rt v[212:213],v242 offset:33920
rt v[214:215],v242 offset:34944
mm v[108:111],v[216:219],a[44:47],v[108:111]
)ASM" R"ASM(aw a188,v233
ar v234,a203
mm v[108:111],v[220:223],a[60:63],v[108:111]
ld a[76:79],v245,s[44:47],s54 offen offset:1024 sc1
mm v[76:79],v[220:223],a[56:59],v[76:79]
ar v235,a207
v_perm_b32 v236, v235, v234, s86
mm v[76:79],v[216:219],a[40:43],v[76:79]
rt v[208:209],v242 offset:42368
rt v[210:211],v242 offset:43392
mm v[44:47],v[216:219],a[36:39],v[44:47]
v_perm_b32 v237, v235, v234, s87
aw a185,v236
mm v[44:47],v[220:223],a[52:55],v[44:47]
aw a189,v237
ar v230,a211
mm v[12:15],v[220:223],a[48:51],v[12:15]
wt lgkmcnt(4)
ar v231,a215
mm v[12:15],v[216:219],a[32:35],v[12:15]
rt v[220:221],v243 offset:33920
rt v[222:223],v243 offset:34944
mm v[16:19],v[196:199],a[32:35],v[16:19]
v_perm_b32 v232, v231, v230, s86
v_perm_b32 v233, v231, v230, s87
mm v[16:19],v[192:195],a[48:51],v[16:19]
ld a[80:83],v244,s[44:47],s54 offen offset:2048 sc1
mm v[48:51],v[192:195],a[52:55],v[48:51]
aw a186,v232
aw a190,v233
mm v[48:51],v[196:199],a[36:39],v[48:51]
rt v[216:217],v243 offset:42368
rt v[218:219],v243 offset:43392
mm v[80:83],v[196:199],a[40:43],v[80:83]
ar v234,a219
ar v235,a223
mm v[80:83],v[192:195],a[56:59],v[80:83]
ld a[84:87],v245,s[44:47],s54 offen offset:2048 sc1
mm v[112:115],v[192:195],a[60:63],v[112:115]
wt vmcnt(30)
s_barrier
mm v[112:115],v[196:199],a[44:47],v[112:115]
rt v[192:193],v240 offset:50688
rt v[194:195],v240 offset:51712
mm v[116:119],v[204:207],a[44:47],v[116:119]
v_perm_b32 v236, v235, v234, s86
v_perm_b32 v237, v235, v234, s87
mm v[116:119],v[200:203],a[60:63],v[116:119]
ld a[88:91],v244,s[44:47],s54 offen offset:3072 sc1
mm v[84:87],v[200:203],a[56:59],v[84:87]
aw a187,v236
aw a191,v237
mm v[84:87],v[204:207],a[40:43],v[84:87]
rt v[196:197],v240 offset:59136
rt v[198:199],v240 offset:60160
mm v[52:55],v[204:207],a[36:39],v[52:55]
s_bitcmp1_b32 vcc_lo, 7
s_cbranch_scc0 .La5_LO_NC7
.La5_LO_KC7:
mm v[52:55],v[200:203],a[52:55],v[52:55]
ld a[92:95],v245,s[44:47],s54 offen offset:3072 sc1
mm v[20:23],v[200:203],a[48:51],v[20:23]
wt lgkmcnt(4)
ar v230,a224
mm v[20:23],v[204:207],a[32:35],v[20:23]
rt v[200:201],v241 offset:50688
rt v[202:203],v241 offset:51712
mm v[24:27],v[212:215],a[32:35],v[24:27]
ar v231,a228
v_perm_b32 v232, v231, v230, s86
mm v[24:27],v[208:211],a[48:51],v[24:27]
ld a[0:3],v244,s[44:47],s55 offen offset:0 sc1
mm v[56:59],v[208:211],a[52:55],v[56:59]
v_perm_b32 v233, v231, v230, s87
aw a192,v232
mm v[56:59],v[212:215],a[36:39],v[56:59]
rt v[204:205],v241 offset:59136
rt v[206:207],v241 offset:60160
mm v[88:91],v[212:215],a[40:43],v[88:91]
aw a196,v233
ar v234,a232
mm v[88:91],v[208:211],a[56:59],v[88:91]
ld a[4:7],v245,s[44:47],s55 offen offset:0 sc1
mm v[120:123],v[208:211],a[60:63],v[120:123]
ar v235,a236
v_perm_b32 v236, v235, v234, s86
mm v[120:123],v[212:215],a[44:47],v[120:123]
rt v[208:209],v242 offset:50688
rt v[210:211],v242 offset:51712
mm v[124:127],v[220:223],a[44:47],v[124:127]
v_perm_b32 v237, v235, v234, s87
aw a193,v236
mm v[124:127],v[216:219],a[60:63],v[124:127]
ld a[8:11],v244,s[44:47],s55 offen offset:1024 sc1
mm v[92:95],v[216:219],a[56:59],v[92:95]
aw a197,v237
ar v230,a240
mm v[92:95],v[220:223],a[40:43],v[92:95]
rt v[212:213],v242 offset:59136
rt v[214:215],v242 offset:60160
)ASM" R"ASM(mm v[60:63],v[220:223],a[36:39],v[60:63]
ar v231,a244
v_perm_b32 v232, v231, v230, s86
mm v[60:63],v[216:219],a[52:55],v[60:63]
v_perm_b32 v233, v231, v230, s87
aw a194,v232
mm v[28:31],v[216:219],a[48:51],v[28:31]
wt vmcnt(24) lgkmcnt(4)
aw a198,v233
mm v[28:31],v[220:223],a[32:35],v[28:31]
rt v[216:217],v243 offset:50688
rt v[218:219],v243 offset:51712
mm v[0:3],v[192:195],v[128:131],v[0:3]
ar v234,a248
ar v235,a252
mm v[0:3],v[196:199],v[136:139],v[0:3]
ld a[12:15],v245,s[44:47],s55 offen offset:1024 sc1
mm v[32:35],v[196:199],v[140:143],v[32:35]
v_perm_b32 v236, v235, v234, s86
v_perm_b32 v237, v235, v234, s87
mm v[32:35],v[192:195],v[132:135],v[32:35]
rt v[220:221],v243 offset:59136
rt v[222:223],v243 offset:60160
mm v[64:67],v[192:195],v[144:147],v[64:67]
aw a195,v236
aw a199,v237
mm v[64:67],v[196:199],v[152:155],v[64:67]
ld a[16:19],v244,s[44:47],s55 offen offset:2048 sc1
mm v[96:99],v[196:199],v[168:171],v[96:99]
ar v230,a225
ar v231,a229
mm v[96:99],v[192:195],v[156:159],v[96:99]
rt v[196:197],v240 offset:50816
rt v[198:199],v240 offset:51840
mm v[100:103],v[200:203],v[156:159],v[100:103]
v_perm_b32 v232, v231, v230, s86
v_perm_b32 v233, v231, v230, s87
mm v[100:103],v[204:207],v[168:171],v[100:103]
ld a[20:23],v245,s[44:47],s55 offen offset:2048 sc1
mm v[68:71],v[204:207],v[152:155],v[68:71]
aw a200,v232
aw a204,v233
mm v[68:71],v[200:203],v[144:147],v[68:71]
rt v[192:193],v240 offset:59264
rt v[194:195],v240 offset:60288
mm v[36:39],v[200:203],v[132:135],v[36:39]
v_add_u32 v240, 67584, v240
ar v234,a233
mm v[36:39],v[204:207],v[140:143],v[36:39]
ld a[24:27],v244,s[44:47],s55 offen offset:3072 sc1
mm v[4:7],v[204:207],v[136:139],v[4:7]
wt lgkmcnt(4)
ar v235,a237
mm v[4:7],v[200:203],v[128:131],v[4:7]
rt v[204:205],v241 offset:50816
rt v[206:207],v241 offset:51840
mm v[8:11],v[208:211],v[128:131],v[8:11]
v_perm_b32 v236, v235, v234, s86
v_perm_b32 v237, v235, v234, s87
mm v[8:11],v[212:215],v[136:139],v[8:11]
ld a[28:31],v245,s[44:47],s55 offen offset:3072 sc1
mm v[40:43],v[212:215],v[140:143],v[40:43]
aw a201,v236
aw a205,v237
mm v[40:43],v[208:211],v[132:135],v[40:43]
rt v[200:201],v241 offset:59264
rt v[202:203],v241 offset:60288
mm v[72:75],v[208:211],v[144:147],v[72:75]
v_add_u32 v241, 67584, v241
ar v230,a241
mm v[72:75],v[212:215],v[152:155],v[72:75]
ar v231,a245
v_perm_b32 v232, v231, v230, s86
mm v[104:107],v[212:215],v[168:171],v[104:107]
v_perm_b32 v233, v231, v230, s87
aw a202,v232
mm v[104:107],v[208:211],v[156:159],v[104:107]
rt v[212:213],v242 offset:50816
rt v[214:215],v242 offset:51840
mm v[108:111],v[216:219],v[156:159],v[108:111]
aw a206,v233
ar v234,a249
mm v[108:111],v[220:223],v[168:171],v[108:111]
ar v235,a253
v_perm_b32 v236, v235, v234, s86
mm v[76:79],v[220:223],v[152:155],v[76:79]
v_perm_b32 v237, v235, v234, s87
aw a203,v236
mm v[76:79],v[216:219],v[144:147],v[76:79]
rt v[208:209],v242 offset:59264
rt v[210:211],v242 offset:60288
mm v[44:47],v[216:219],v[132:135],v[44:47]
v_add_u32 v242, 67584, v242
aw a207,v237
mm v[44:47],v[220:223],v[140:143],v[44:47]
ar v230,a226
ar v231,a230
mm v[12:15],v[220:223],v[136:139],v[12:15]
wt lgkmcnt(4)
v_perm_b32 v232, v231, v230, s86
)ASM" R"ASM(mm v[12:15],v[216:219],v[128:131],v[12:15]
rt v[220:221],v243 offset:50816
rt v[222:223],v243 offset:51840
mm v[16:19],v[196:199],v[128:131],v[16:19]
v_perm_b32 v233, v231, v230, s87
aw a208,v232
mm v[16:19],v[192:195],v[136:139],v[16:19]
ld a[32:35],v246,s[44:47],s54 offen offset:0 sc1
mm v[48:51],v[192:195],v[140:143],v[48:51]
aw a212,v233
ar v234,a234
mm v[48:51],v[196:199],v[132:135],v[48:51]
rt v[216:217],v243 offset:59264
rt v[218:219],v243 offset:60288
mm v[80:83],v[196:199],v[144:147],v[80:83]
v_add_u32 v243, 67584, v243
ar v235,a238
mm v[80:83],v[192:195],v[152:155],v[80:83]
ld a[36:39],v247,s[44:47],s54 offen offset:0 sc1
mm v[112:115],v[192:195],v[168:171],v[112:115]
wt vmcnt(18)
s_barrier
mm v[112:115],v[196:199],v[156:159],v[112:115]
rt v[192:193],v240 offset:50688
rt v[194:195],v240 offset:51712
mm v[116:119],v[204:207],v[156:159],v[116:119]
v_perm_b32 v236, v235, v234, s86
v_perm_b32 v237, v235, v234, s87
mm v[116:119],v[200:203],v[168:171],v[116:119]
ld a[40:43],v246,s[44:47],s54 offen offset:1024 sc1
mm v[84:87],v[200:203],v[152:155],v[84:87]
aw a209,v236
aw a213,v237
mm v[84:87],v[204:207],v[144:147],v[84:87]
rt v[196:197],v240 offset:59136
rt v[198:199],v240 offset:60160
mm v[52:55],v[204:207],v[132:135],v[52:55]
ar v230,a242
ar v231,a246
mm v[52:55],v[200:203],v[140:143],v[52:55]
ld a[44:47],v247,s[44:47],s54 offen offset:1024 sc1
mm v[20:23],v[200:203],v[136:139],v[20:23]
wt lgkmcnt(4)
v_perm_b32 v232, v231, v230, s86
mm v[20:23],v[204:207],v[128:131],v[20:23]
rt v[200:201],v240 offset:0
.La5_LW7_4:
s_cbranch_execz .La5_LA_RETURN
rt v[202:203],v240 offset:1024
mm v[24:27],v[212:215],v[128:131],v[24:27]
v_perm_b32 v233, v231, v230, s87
aw a210,v232
mm v[24:27],v[208:211],v[136:139],v[24:27]
ld a[48:51],v246,s[44:47],s54 offen offset:2048 sc1
mm v[56:59],v[208:211],v[140:143],v[56:59]
aw a214,v233
ar v234,a250
mm v[56:59],v[212:215],v[132:135],v[56:59]
rt v[204:205],v240 offset:8448
rt v[206:207],v240 offset:9472
mm v[88:91],v[212:215],v[144:147],v[88:91]
ar v235,a254
v_perm_b32 v236, v235, v234, s86
mm v[88:91],v[208:211],v[152:155],v[88:91]
ld a[52:55],v247,s[44:47],s54 offen offset:2048 sc1
mm v[120:123],v[208:211],v[168:171],v[120:123]
v_perm_b32 v237, v235, v234, s87
aw a211,v236
mm v[120:123],v[212:215],v[156:159],v[120:123]
rt v[208:209],v240 offset:16896
rt v[210:211],v240 offset:17920
mm v[124:127],v[220:223],v[156:159],v[124:127]
aw a215,v237
ar v230,a227
mm v[124:127],v[216:219],v[168:171],v[124:127]
ld a[56:59],v246,s[44:47],s54 offen offset:3072 sc1
mm v[92:95],v[216:219],v[152:155],v[92:95]
ar v231,a231
v_perm_b32 v232, v231, v230, s86
mm v[92:95],v[220:223],v[144:147],v[92:95]
rt v[212:213],v240 offset:25344
rt v[214:215],v240 offset:26368
mm v[60:63],v[220:223],v[132:135],v[60:63]
v_perm_b32 v233, v231, v230, s87
aw a216,v232
mm v[60:63],v[216:219],v[140:143],v[60:63]
aw a220,v233
ar v234,a235
mm v[28:31],v[216:219],v[136:139],v[28:31]
wt vmcnt(15) lgkmcnt(4)
ar v235,a239
mm v[28:31],v[220:223],v[128:131],v[28:31]
rt v[216:217],v240 offset:33792
rt v[218:219],v240 offset:34816
mm v[0:3],v[192:195],a[64:67],v[0:3]
ld a[60:63],v247,s[44:47],s54 offen offset:3072 sc1
mm v[0:3],v[196:199],a[72:75],v[0:3]
rt v[220:221],v240 offset:42240
rt v[222:223],v240 offset:43264
mm v[32:35],v[196:199],a[76:79],v[32:35]
)ASM" R"ASM(v_perm_b32 v236, v235, v234, s86
v_perm_b32 v237, v235, v234, s87
mm v[32:35],v[192:195],a[68:71],v[32:35]
rt v[196:197],v241 offset:50688
rt v[198:199],v241 offset:51712
mm v[32:35],v[200:203],a[84:87],v[32:35]
ld v[128:131],v246,s[44:47],s55 offen offset:0 sc1
mm v[32:35],v[204:207],a[92:95],v[32:35]
rt v[192:193],v241 offset:59136
rt v[194:195],v241 offset:60160
mm v[0:3],v[204:207],a[88:91],v[0:3]
wt vmcnt(9) lgkmcnt(4)
aw a217,v236
mm v[0:3],v[200:203],a[80:83],v[0:3]
rt v[204:205],v241 offset:0
rt v[206:207],v241 offset:1024
mm v[0:3],v[208:211],a[0:3],v[0:3]
ld v[132:135],v247,s[44:47],s55 offen offset:0 sc1
mm v[0:3],v[212:215],a[8:11],v[0:3]
rt v[200:201],v241 offset:8448
rt v[202:203],v241 offset:9472
mm v[32:35],v[212:215],a[12:15],v[32:35]
aw a221,v237
ar v230,a243
mm v[32:35],v[208:211],a[4:7],v[32:35]
rt v[212:213],v241 offset:16896
rt v[214:215],v241 offset:17920
mm v[32:35],v[216:219],a[20:23],v[32:35]
ld v[144:147],v246,s[44:47],s55 offen offset:1024 sc1
mm v[32:35],v[220:223],a[28:31],v[32:35]
rt v[208:209],v241 offset:25344
rt v[210:211],v241 offset:26368
mm v[0:3],v[220:223],a[24:27],v[0:3]
wt lgkmcnt(4)
ar v231,a247
mm v[0:3],v[216:219],a[16:19],v[0:3]
rt v[220:221],v241 offset:33792
rt v[222:223],v241 offset:34816
mm v[4:7],v[196:199],a[64:67],v[4:7]
ld v[148:151],v247,s[44:47],s55 offen offset:1024 sc1
mm v[4:7],v[192:195],a[72:75],v[4:7]
rt v[216:217],v241 offset:42240
rt v[218:219],v241 offset:43264
mm v[36:39],v[192:195],a[76:79],v[36:39]
v_perm_b32 v232, v231, v230, s86
v_perm_b32 v233, v231, v230, s87
mm v[36:39],v[196:199],a[68:71],v[36:39]
rt v[192:193],v242 offset:50688
rt v[194:195],v242 offset:51712
mm v[36:39],v[204:207],a[84:87],v[36:39]
ld v[160:163],v246,s[44:47],s55 offen offset:2048 sc1
mm v[36:39],v[200:203],a[92:95],v[36:39]
rt v[196:197],v242 offset:59136
rt v[198:199],v242 offset:60160
aw a218,v232
aw a222,v233
ar v234,a251
ar v235,a255
v_perm_b32 v236, v235, v234, s86
v_perm_b32 v237, v235, v234, s87
aw a219,v236
aw a223,v237
mm v[4:7],v[200:203],a[88:91],v[4:7]
wt lgkmcnt(4)
mm v[4:7],v[204:207],a[80:83],v[4:7]
rt v[200:201],v242 offset:0
rt v[202:203],v242 offset:1024
mm v[4:7],v[212:215],a[0:3],v[4:7]
ld v[164:167],v247,s[44:47],s55 offen offset:2048 sc1
mm v[4:7],v[208:211],a[8:11],v[4:7]
rt v[204:205],v242 offset:8448
rt v[206:207],v242 offset:9472
mm v[36:39],v[208:211],a[12:15],v[36:39]
mm v[36:39],v[212:215],a[4:7],v[36:39]
rt v[208:209],v242 offset:16896
rt v[210:211],v242 offset:17920
mm v[36:39],v[220:223],a[20:23],v[36:39]
mm v[36:39],v[216:219],a[28:31],v[36:39]
rt v[212:213],v242 offset:25344
rt v[214:215],v242 offset:26368
mm v[4:7],v[216:219],a[24:27],v[4:7]
wt lgkmcnt(4)
mm v[4:7],v[220:223],a[16:19],v[4:7]
rt v[216:217],v242 offset:33792
rt v[218:219],v242 offset:34816
mm v[8:11],v[192:195],a[64:67],v[8:11]
ld v[176:179],v246,s[44:47],s55 offen offset:3072 sc1
mm v[8:11],v[196:199],a[72:75],v[8:11]
rt v[220:221],v242 offset:42240
rt v[222:223],v242 offset:43264
mm v[40:43],v[196:199],a[76:79],v[40:43]
cv v234,v32,v33
cv v236,v36,v37
mm v[40:43],v[192:195],a[68:71],v[40:43]
rt v[196:197],v243 offset:50688
rt v[198:199],v243 offset:51712
mm v[40:43],v[200:203],a[84:87],v[40:43]
ld v[180:183],v247,s[44:47],s55 offen offset:3072 sc1
mm v[40:43],v[204:207],a[92:95],v[40:43]
rt v[192:193],v243 offset:59136
)ASM" R"ASM(rt v[194:195],v243 offset:60160
mm v[8:11],v[204:207],a[88:91],v[8:11]
wt lgkmcnt(4)
v_bfe_u32 v250, v255, 6, 1
mm v[8:11],v[200:203],a[80:83],v[8:11]
rt v[204:205],v243 offset:0
rt v[206:207],v243 offset:1024
mm v[8:11],v[208:211],a[0:3],v[8:11]
cv v235,v34,v35
cv v237,v38,v39
mm v[8:11],v[212:215],a[8:11],v[8:11]
rt v[200:201],v243 offset:8448
rt v[202:203],v243 offset:9472
mm v[40:43],v[212:215],a[12:15],v[40:43]
v_permlane16_swap_b32 v234, v236
v_permlane16_swap_b32 v235, v237
mm v[40:43],v[208:211],a[4:7],v[40:43]
rt v[212:213],v243 offset:16896
rt v[214:215],v243 offset:17920
mm v[40:43],v[216:219],a[20:23],v[40:43]
st v[234:237],v238,s[32:35],s95 offen offset:0 sc1
mm v[40:43],v[220:223],a[28:31],v[40:43]
rt v[208:209],v243 offset:25344
rt v[210:211],v243 offset:26368
mm v[8:11],v[220:223],a[24:27],v[8:11]
wt lgkmcnt(4)
v_mul_u32_u24 v250, 7, v250
mm v[8:11],v[216:219],a[16:19],v[8:11]
rt v[220:221],v243 offset:33792
rt v[222:223],v243 offset:34816
mm v[12:15],v[196:199],a[64:67],v[12:15]
cv v230,v0,v1
cv v232,v4,v5
mm v[12:15],v[192:195],a[72:75],v[12:15]
rt v[216:217],v243 offset:42240
rt v[218:219],v243 offset:43264
mm v[44:47],v[192:195],a[76:79],v[44:47]
cv v231,v2,v3
cv v233,v6,v7
mm v[44:47],v[196:199],a[68:71],v[44:47]
rt v[192:193],v240 offset:50816
rt v[194:195],v240 offset:51840
mm v[44:47],v[204:207],a[84:87],v[44:47]
v_permlane16_swap_b32 v230, v232
v_permlane16_swap_b32 v231, v233
mm v[44:47],v[200:203],a[92:95],v[44:47]
rt v[196:197],v240 offset:59264
rt v[198:199],v240 offset:60288
mm v[12:15],v[200:203],a[88:91],v[12:15]
wt lgkmcnt(4)
v_xor_b32 v250, s3, v250
mm v[12:15],v[204:207],a[80:83],v[12:15]
rt v[200:201],v240 offset:128
rt v[202:203],v240 offset:1152
mm v[12:15],v[212:215],a[0:3],v[12:15]
st v[230:233],v238,s[32:35],s94 offen offset:0 sc1
mm v[12:15],v[208:211],a[8:11],v[12:15]
rt v[204:205],v240 offset:8576
rt v[206:207],v240 offset:9600
mm v[44:47],v[208:211],a[12:15],v[44:47]
v_bfe_u32 v251, v255, 4, 2
v_lshl_add_u32 v250, v250, 2, v251
mm v[44:47],v[212:215],a[4:7],v[44:47]
rt v[208:209],v240 offset:17024
rt v[210:211],v240 offset:18048
mm v[44:47],v[220:223],a[20:23],v[44:47]
ld v[136:139],v224,s[68:71],0 offen offset:0
mm v[44:47],v[216:219],a[28:31],v[44:47]
rt v[212:213],v240 offset:25472
rt v[214:215],v240 offset:26496
mm v[12:15],v[216:219],a[24:27],v[12:15]
wt lgkmcnt(4)
v_lshlrev_b32 v250, 9, v250
mm v[12:15],v[220:223],a[16:19],v[12:15]
rt v[216:217],v240 offset:33920
rt v[218:219],v240 offset:34944
mm v[16:19],v[192:195],a[64:67],v[16:19]
cv v234,v40,v41
cv v236,v44,v45
mm v[16:19],v[196:199],a[72:75],v[16:19]
rt v[220:221],v240 offset:42368
rt v[222:223],v240 offset:43392
mm v[48:51],v[196:199],a[76:79],v[48:51]
cv v235,v42,v43
cv v237,v46,v47
mm v[48:51],v[192:195],a[68:71],v[48:51]
rt v[196:197],v241 offset:50816
rt v[198:199],v241 offset:51840
mm v[48:51],v[200:203],a[84:87],v[48:51]
v_permlane16_swap_b32 v234, v236
v_permlane16_swap_b32 v235, v237
mm v[48:51],v[204:207],a[92:95],v[48:51]
rt v[192:193],v241 offset:59264
rt v[194:195],v241 offset:60288
mm v[16:19],v[204:207],a[88:91],v[16:19]
wt lgkmcnt(4)
v_and_b32 v251, 0x2000, v250
mm v[16:19],v[200:203],a[80:83],v[16:19]
rt v[204:205],v241 offset:128
rt v[206:207],v241 offset:1152
)ASM" R"ASM(mm v[16:19],v[208:211],a[0:3],v[16:19]
st v[234:237],v238,s[32:35],s95 offen offset:1024 sc1
mm v[16:19],v[212:215],a[8:11],v[16:19]
rt v[200:201],v241 offset:8576
rt v[202:203],v241 offset:9600
mm v[48:51],v[212:215],a[12:15],v[48:51]
cv v230,v8,v9
cv v232,v12,v13
mm v[48:51],v[208:211],a[4:7],v[48:51]
rt v[212:213],v241 offset:17024
rt v[214:215],v241 offset:18048
mm v[48:51],v[216:219],a[20:23],v[48:51]
cv v231,v10,v11
cv v233,v14,v15
mm v[48:51],v[220:223],a[28:31],v[48:51]
rt v[208:209],v241 offset:25472
rt v[210:211],v241 offset:26496
mm v[16:19],v[220:223],a[24:27],v[16:19]
wt lgkmcnt(4)
v_add_u32 v250, v250, v251
mm v[16:19],v[216:219],a[16:19],v[16:19]
rt v[220:221],v241 offset:33920
rt v[222:223],v241 offset:34944
mm v[20:23],v[196:199],a[64:67],v[20:23]
v_permlane16_swap_b32 v230, v232
v_permlane16_swap_b32 v231, v233
mm v[20:23],v[192:195],a[72:75],v[20:23]
rt v[216:217],v241 offset:42368
rt v[218:219],v241 offset:43392
mm v[52:55],v[192:195],a[76:79],v[52:55]
v_bfe_u32 v251, v255, 7, 1
v_lshl_add_u32 v250, v251, 13, v250
mm v[52:55],v[196:199],a[68:71],v[52:55]
rt v[192:193],v242 offset:50816
rt v[194:195],v242 offset:51840
mm v[52:55],v[204:207],a[84:87],v[52:55]
st v[230:233],v238,s[32:35],s94 offen offset:1024 sc1
mm v[52:55],v[200:203],a[92:95],v[52:55]
rt v[196:197],v242 offset:59264
rt v[198:199],v242 offset:60288
mm v[20:23],v[200:203],a[88:91],v[20:23]
wt lgkmcnt(4)
v_and_b32 v251, 64, v255
mm v[20:23],v[204:207],a[80:83],v[20:23]
rt v[200:201],v242 offset:128
rt v[202:203],v242 offset:1152
mm v[20:23],v[212:215],a[0:3],v[20:23]
ld v[140:143],v225,s[48:51],0 offen offset:0
mm v[20:23],v[208:211],a[8:11],v[20:23]
rt v[204:205],v242 offset:8576
rt v[206:207],v242 offset:9600
mm v[52:55],v[208:211],a[12:15],v[52:55]
v_lshl_add_u32 v251, v251, 24, v250
v_and_b32 v248, 0x1f0, v255
mm v[52:55],v[212:215],a[4:7],v[52:55]
rt v[208:209],v242 offset:17024
rt v[210:211],v242 offset:18048
mm v[52:55],v[220:223],a[20:23],v[52:55]
ld v[152:155],v224,s[68:71],0 offen offset:512
mm v[52:55],v[216:219],a[28:31],v[52:55]
rt v[212:213],v242 offset:25472
rt v[214:215],v242 offset:26496
mm v[20:23],v[216:219],a[24:27],v[20:23]
wt lgkmcnt(4)
s_mov_b32 s93, 0
ld a[224:227],v229,s[28:31],s93 offen offset:0
mm v[20:23],v[220:223],a[16:19],v[20:23]
rt v[216:217],v242 offset:33920
rt v[218:219],v242 offset:34944
mm v[24:27],v[192:195],a[64:67],v[24:27]
cv v234,v48,v49
cv v236,v52,v53
mm v[24:27],v[196:199],a[72:75],v[24:27]
rt v[220:221],v242 offset:42368
rt v[222:223],v242 offset:43392
mm v[56:59],v[196:199],a[76:79],v[56:59]
cv v235,v50,v51
cv v237,v54,v55
mm v[56:59],v[192:195],a[68:71],v[56:59]
rt v[196:197],v243 offset:50816
rt v[198:199],v243 offset:51840
mm v[56:59],v[200:203],a[84:87],v[56:59]
v_permlane16_swap_b32 v234, v236
v_permlane16_swap_b32 v235, v237
mm v[56:59],v[204:207],a[92:95],v[56:59]
rt v[192:193],v243 offset:59264
rt v[194:195],v243 offset:60288
mm v[24:27],v[204:207],a[88:91],v[24:27]
wt lgkmcnt(4)
ld a[228:231],v229,s[28:31],s93 offen offset:2048
mm v[24:27],v[200:203],a[80:83],v[24:27]
rt v[204:205],v243 offset:128
rt v[206:207],v243 offset:1152
mm v[24:27],v[208:211],a[0:3],v[24:27]
st v[234:237],v238,s[32:35],s95 offen offset:2048 sc1
mm v[24:27],v[212:215],a[8:11],v[24:27]
rt v[200:201],v243 offset:8576
rt v[202:203],v243 offset:9600
mm v[56:59],v[212:215],a[12:15],v[56:59]
cv v230,v16,v17
)ASM" R"ASM(cv v232,v20,v21
mm v[56:59],v[208:211],a[4:7],v[56:59]
rt v[212:213],v243 offset:17024
rt v[214:215],v243 offset:18048
mm v[56:59],v[216:219],a[20:23],v[56:59]
cv v231,v18,v19
cv v233,v22,v23
mm v[56:59],v[220:223],a[28:31],v[56:59]
rt v[208:209],v243 offset:25472
rt v[210:211],v243 offset:26496
mm v[24:27],v[220:223],a[24:27],v[24:27]
wt lgkmcnt(4)
s_mov_b32 s93, 4096
ld a[232:235],v229,s[28:31],s93 offen offset:0
mm v[24:27],v[216:219],a[16:19],v[24:27]
rt v[220:221],v243 offset:33920
rt v[222:223],v243 offset:34944
mm v[28:31],v[196:199],a[64:67],v[28:31]
v_permlane16_swap_b32 v230, v232
v_permlane16_swap_b32 v231, v233
mm v[28:31],v[192:195],a[72:75],v[28:31]
rt v[216:217],v243 offset:42368
rt v[218:219],v243 offset:43392
mm v[60:63],v[192:195],a[76:79],v[60:63]
ld a[236:239],v229,s[28:31],s93 offen offset:2048
mm v[60:63],v[196:199],a[68:71],v[60:63]
rt v[192:193],v240 offset:50688
rt v[194:195],v240 offset:51712
mm v[60:63],v[204:207],a[84:87],v[60:63]
st v[230:233],v238,s[32:35],s94 offen offset:2048 sc1
mm v[60:63],v[200:203],a[92:95],v[60:63]
rt v[196:197],v240 offset:59136
rt v[198:199],v240 offset:60160
mm v[28:31],v[200:203],a[88:91],v[28:31]
wt lgkmcnt(4)
s_mov_b32 s93, 8192
ld a[240:243],v229,s[28:31],s93 offen offset:0
mm v[28:31],v[204:207],a[80:83],v[28:31]
rt v[200:201],v240 offset:0
rt v[202:203],v240 offset:1024
mm v[28:31],v[212:215],a[0:3],v[28:31]
ld v[156:159],v225,s[48:51],0 offen offset:512
mm v[28:31],v[208:211],a[8:11],v[28:31]
rt v[204:205],v240 offset:8448
rt v[206:207],v240 offset:9472
mm v[60:63],v[208:211],a[12:15],v[60:63]
ld a[244:247],v229,s[28:31],s93 offen offset:2048
mm v[60:63],v[212:215],a[4:7],v[60:63]
rt v[208:209],v240 offset:16896
rt v[210:211],v240 offset:17920
mm v[60:63],v[220:223],a[20:23],v[60:63]
ld v[168:171],v224,s[68:71],0 offen offset:1024
mm v[60:63],v[216:219],a[28:31],v[60:63]
rt v[212:213],v240 offset:25344
rt v[214:215],v240 offset:26368
mm v[28:31],v[216:219],a[24:27],v[28:31]
wt vmcnt(25) lgkmcnt(4)
s_mov_b32 s93, 12288
ld a[248:251],v229,s[28:31],s93 offen offset:0
mm v[28:31],v[220:223],a[16:19],v[28:31]
rt v[216:217],v240 offset:33792
rt v[218:219],v240 offset:34816
mm v[64:67],v[192:195],a[32:35],v[64:67]
cv v234,v56,v57
cv v236,v60,v61
mm v[64:67],v[196:199],a[40:43],v[64:67]
rt v[220:221],v240 offset:42240
rt v[222:223],v240 offset:43264
mm v[96:99],v[196:199],a[44:47],v[96:99]
cv v235,v58,v59
cv v237,v62,v63
mm v[96:99],v[192:195],a[36:39],v[96:99]
rt v[196:197],v241 offset:50688
rt v[198:199],v241 offset:51712
mm v[96:99],v[200:203],a[52:55],v[96:99]
v_permlane16_swap_b32 v234, v236
v_permlane16_swap_b32 v235, v237
mm v[96:99],v[204:207],a[60:63],v[96:99]
rt v[192:193],v241 offset:59136
rt v[194:195],v241 offset:60160
mm v[64:67],v[204:207],a[56:59],v[64:67]
wt vmcnt(18) lgkmcnt(4)
ld a[252:255],v229,s[28:31],s93 offen offset:2048
mm v[64:67],v[200:203],a[48:51],v[64:67]
rt v[204:205],v241 offset:0
rt v[206:207],v241 offset:1024
mm v[64:67],v[208:211],v[128:131],v[64:67]
st v[234:237],v238,s[32:35],s95 offen offset:3072 sc1
mm v[64:67],v[212:215],v[144:147],v[64:67]
rt v[200:201],v241 offset:8448
rt v[202:203],v241 offset:9472
mm v[96:99],v[212:215],v[148:151],v[96:99]
cv v230,v24,v25
cv v232,v28,v29
mm v[96:99],v[208:211],v[132:135],v[96:99]
rt v[212:213],v241 offset:16896
rt v[214:215],v241 offset:17920
mm v[96:99],v[216:219],v[164:167],v[96:99]
cv v231,v26,v27
cv v233,v30,v31
)ASM" R"ASM(mm v[96:99],v[220:223],v[180:183],v[96:99]
rt v[208:209],v241 offset:25344
rt v[210:211],v241 offset:26368
mm v[64:67],v[220:223],v[176:179],v[64:67]
wt lgkmcnt(4)
s_mov_b32 s93, 0
ld a[64:67],v229,s[28:31],s93 offen offset:256
mm v[64:67],v[216:219],v[160:163],v[64:67]
rt v[220:221],v241 offset:33792
rt v[222:223],v241 offset:34816
mm v[68:71],v[196:199],a[32:35],v[68:71]
v_permlane16_swap_b32 v230, v232
v_permlane16_swap_b32 v231, v233
mm v[68:71],v[192:195],a[40:43],v[68:71]
rt v[216:217],v241 offset:42240
rt v[218:219],v241 offset:43264
mm v[100:103],v[192:195],a[44:47],v[100:103]
ld a[68:71],v229,s[28:31],s93 offen offset:2304
mm v[100:103],v[196:199],a[36:39],v[100:103]
rt v[192:193],v242 offset:50688
rt v[194:195],v242 offset:51712
mm v[100:103],v[204:207],a[52:55],v[100:103]
st v[230:233],v238,s[32:35],s94 offen offset:3072 sc1
mm v[100:103],v[200:203],a[60:63],v[100:103]
rt v[196:197],v242 offset:59136
rt v[198:199],v242 offset:60160
mm v[68:71],v[200:203],a[56:59],v[68:71]
wt lgkmcnt(4)
s_mov_b32 s93, 4096
ld a[72:75],v229,s[28:31],s93 offen offset:256
mm v[68:71],v[204:207],a[48:51],v[68:71]
rt v[200:201],v242 offset:0
rt v[202:203],v242 offset:1024
mm v[68:71],v[212:215],v[128:131],v[68:71]
ld v[172:175],v225,s[48:51],0 offen offset:1024
mm v[68:71],v[208:211],v[144:147],v[68:71]
rt v[204:205],v242 offset:8448
rt v[206:207],v242 offset:9472
mm v[100:103],v[208:211],v[148:151],v[100:103]
ld a[76:79],v229,s[28:31],s93 offen offset:2304
mm v[100:103],v[212:215],v[132:135],v[100:103]
rt v[208:209],v242 offset:16896
rt v[210:211],v242 offset:17920
mm v[100:103],v[220:223],v[164:167],v[100:103]
ld v[184:187],v224,s[68:71],0 offen offset:1536
mm v[100:103],v[216:219],v[180:183],v[100:103]
rt v[212:213],v242 offset:25344
rt v[214:215],v242 offset:26368
mm v[68:71],v[216:219],v[176:179],v[68:71]
wt lgkmcnt(4)
s_mov_b32 s93, 8192
ld a[80:83],v229,s[28:31],s93 offen offset:256
mm v[68:71],v[220:223],v[160:163],v[68:71]
rt v[216:217],v242 offset:33792
rt v[218:219],v242 offset:34816
mm v[72:75],v[192:195],a[32:35],v[72:75]
cv v234,v96,v97
cv v236,v100,v101
mm v[72:75],v[196:199],a[40:43],v[72:75]
rt v[220:221],v242 offset:42240
rt v[222:223],v242 offset:43264
mm v[104:107],v[196:199],a[44:47],v[104:107]
cv v235,v98,v99
cv v237,v102,v103
mm v[104:107],v[192:195],a[36:39],v[104:107]
rt v[196:197],v243 offset:50688
rt v[198:199],v243 offset:51712
mm v[104:107],v[200:203],a[52:55],v[104:107]
v_permlane16_swap_b32 v234, v236
v_permlane16_swap_b32 v235, v237
mm v[104:107],v[204:207],a[60:63],v[104:107]
rt v[192:193],v243 offset:59136
rt v[194:195],v243 offset:60160
mm v[72:75],v[204:207],a[56:59],v[72:75]
wt lgkmcnt(4)
ld a[84:87],v229,s[28:31],s93 offen offset:2304
mm v[72:75],v[200:203],a[48:51],v[72:75]
rt v[204:205],v243 offset:0
rt v[206:207],v243 offset:1024
mm v[72:75],v[208:211],v[128:131],v[72:75]
st v[234:237],v238,s[32:35],s97 offen offset:0 sc1
mm v[72:75],v[212:215],v[144:147],v[72:75]
rt v[200:201],v243 offset:8448
.La5_LW7_5:
s_cbranch_execz .La5_LA_RETURN
rt v[202:203],v243 offset:9472
mm v[104:107],v[212:215],v[148:151],v[104:107]
cv v230,v64,v65
cv v232,v68,v69
mm v[104:107],v[208:211],v[132:135],v[104:107]
rt v[212:213],v243 offset:16896
rt v[214:215],v243 offset:17920
mm v[104:107],v[216:219],v[164:167],v[104:107]
cv v231,v66,v67
cv v233,v70,v71
mm v[104:107],v[220:223],v[180:183],v[104:107]
rt v[208:209],v243 offset:25344
rt v[210:211],v243 offset:26368
mm v[72:75],v[220:223],v[176:179],v[72:75]
wt lgkmcnt(4)
)ASM" R"ASM(s_mov_b32 s93, 12288
ld a[88:91],v229,s[28:31],s93 offen offset:256
mm v[72:75],v[216:219],v[160:163],v[72:75]
rt v[220:221],v243 offset:33792
rt v[222:223],v243 offset:34816
mm v[76:79],v[196:199],a[32:35],v[76:79]
v_permlane16_swap_b32 v230, v232
v_permlane16_swap_b32 v231, v233
mm v[76:79],v[192:195],a[40:43],v[76:79]
rt v[216:217],v243 offset:42240
rt v[218:219],v243 offset:43264
mm v[108:111],v[192:195],a[44:47],v[108:111]
ld a[92:95],v229,s[28:31],s93 offen offset:2304
mm v[108:111],v[196:199],a[36:39],v[108:111]
rt v[192:193],v240 offset:50816
rt v[194:195],v240 offset:51840
mm v[108:111],v[204:207],a[52:55],v[108:111]
st v[230:233],v238,s[32:35],s96 offen offset:0 sc1
mm v[108:111],v[200:203],a[60:63],v[108:111]
rt v[196:197],v240 offset:59264
rt v[198:199],v240 offset:60288
mm v[76:79],v[200:203],a[56:59],v[76:79]
wt lgkmcnt(4)
v_lshrrev_b32 v248, 1, v248
mm v[76:79],v[204:207],a[48:51],v[76:79]
rt v[200:201],v240 offset:128
rt v[202:203],v240 offset:1152
mm v[76:79],v[212:215],v[128:131],v[76:79]
ld v[188:191],v225,s[48:51],0 offen offset:1536
mm v[76:79],v[208:211],v[144:147],v[76:79]
rt v[204:205],v240 offset:8576
rt v[206:207],v240 offset:9600
mm v[108:111],v[208:211],v[148:151],v[108:111]
mm v[108:111],v[212:215],v[132:135],v[108:111]
rt v[208:209],v240 offset:17024
rt v[210:211],v240 offset:18048
mm v[108:111],v[220:223],v[164:167],v[108:111]
mm v[108:111],v[216:219],v[180:183],v[108:111]
rt v[212:213],v240 offset:25472
rt v[214:215],v240 offset:26496
mm v[76:79],v[216:219],v[176:179],v[76:79]
wt lgkmcnt(4)
mm v[76:79],v[220:223],v[160:163],v[76:79]
rt v[216:217],v240 offset:33920
rt v[218:219],v240 offset:34944
mm v[80:83],v[192:195],a[32:35],v[80:83]
cv v234,v104,v105
cv v236,v108,v109
mm v[80:83],v[196:199],a[40:43],v[80:83]
rt v[220:221],v240 offset:42368
rt v[222:223],v240 offset:43392
mm v[112:115],v[196:199],a[44:47],v[112:115]
cv v235,v106,v107
cv v237,v110,v111
mm v[112:115],v[192:195],a[36:39],v[112:115]
rt v[196:197],v241 offset:50816
rt v[198:199],v241 offset:51840
mm v[112:115],v[200:203],a[52:55],v[112:115]
v_permlane16_swap_b32 v234, v236
v_permlane16_swap_b32 v235, v237
mm v[112:115],v[204:207],a[60:63],v[112:115]
rt v[192:193],v241 offset:59264
rt v[194:195],v241 offset:60288
mm v[80:83],v[204:207],a[56:59],v[80:83]
wt lgkmcnt(4)
mm v[80:83],v[200:203],a[48:51],v[80:83]
rt v[204:205],v241 offset:128
rt v[206:207],v241 offset:1152
mm v[80:83],v[208:211],v[128:131],v[80:83]
st v[234:237],v238,s[32:35],s97 offen offset:1024 sc1
mm v[80:83],v[212:215],v[144:147],v[80:83]
rt v[200:201],v241 offset:8576
rt v[202:203],v241 offset:9600
mm v[112:115],v[212:215],v[148:151],v[112:115]
cv v230,v72,v73
cv v232,v76,v77
mm v[112:115],v[208:211],v[132:135],v[112:115]
rt v[212:213],v241 offset:17024
rt v[214:215],v241 offset:18048
mm v[112:115],v[216:219],v[164:167],v[112:115]
cv v231,v74,v75
cv v233,v78,v79
mm v[112:115],v[220:223],v[180:183],v[112:115]
rt v[208:209],v241 offset:25472
rt v[210:211],v241 offset:26496
mm v[80:83],v[220:223],v[176:179],v[80:83]
wt lgkmcnt(4)
mm v[80:83],v[216:219],v[160:163],v[80:83]
rt v[220:221],v241 offset:33920
rt v[222:223],v241 offset:34944
mm v[84:87],v[196:199],a[32:35],v[84:87]
v_permlane16_swap_b32 v230, v232
v_permlane16_swap_b32 v231, v233
mm v[84:87],v[192:195],a[40:43],v[84:87]
rt v[216:217],v241 offset:42368
rt v[218:219],v241 offset:43392
mm v[116:119],v[192:195],a[44:47],v[116:119]
mm v[116:119],v[196:199],a[36:39],v[116:119]
rt v[192:193],v242 offset:50816
rt v[194:195],v242 offset:51840
)ASM" R"ASM(mm v[116:119],v[204:207],a[52:55],v[116:119]
st v[230:233],v238,s[32:35],s96 offen offset:1024 sc1
mm v[116:119],v[200:203],a[60:63],v[116:119]
rt v[196:197],v242 offset:59264
rt v[198:199],v242 offset:60288
mm v[84:87],v[200:203],a[56:59],v[84:87]
wt lgkmcnt(4)
mm v[84:87],v[204:207],a[48:51],v[84:87]
rt v[200:201],v242 offset:128
rt v[202:203],v242 offset:1152
mm v[84:87],v[212:215],v[128:131],v[84:87]
mm v[84:87],v[208:211],v[144:147],v[84:87]
rt v[204:205],v242 offset:8576
rt v[206:207],v242 offset:9600
mm v[116:119],v[208:211],v[148:151],v[116:119]
mm v[116:119],v[212:215],v[132:135],v[116:119]
rt v[208:209],v242 offset:17024
rt v[210:211],v242 offset:18048
mm v[116:119],v[220:223],v[164:167],v[116:119]
mm v[116:119],v[216:219],v[180:183],v[116:119]
rt v[212:213],v242 offset:25472
rt v[214:215],v242 offset:26496
mm v[84:87],v[216:219],v[176:179],v[84:87]
wt lgkmcnt(4)
mm v[84:87],v[220:223],v[160:163],v[84:87]
rt v[216:217],v242 offset:33920
rt v[218:219],v242 offset:34944
mm v[88:91],v[192:195],a[32:35],v[88:91]
cv v234,v112,v113
cv v236,v116,v117
mm v[88:91],v[196:199],a[40:43],v[88:91]
rt v[220:221],v242 offset:42368
rt v[222:223],v242 offset:43392
mm v[120:123],v[196:199],a[44:47],v[120:123]
cv v235,v114,v115
cv v237,v118,v119
mm v[120:123],v[192:195],a[36:39],v[120:123]
rt v[196:197],v243 offset:50816
rt v[198:199],v243 offset:51840
mm v[120:123],v[200:203],a[52:55],v[120:123]
v_permlane16_swap_b32 v234, v236
v_permlane16_swap_b32 v235, v237
mm v[120:123],v[204:207],a[60:63],v[120:123]
rt v[192:193],v243 offset:59264
rt v[194:195],v243 offset:60288
mm v[88:91],v[204:207],a[56:59],v[88:91]
wt lgkmcnt(4)
mm v[88:91],v[200:203],a[48:51],v[88:91]
rt v[204:205],v243 offset:128
rt v[206:207],v243 offset:1152
mm v[88:91],v[208:211],v[128:131],v[88:91]
st v[234:237],v238,s[32:35],s97 offen offset:2048 sc1
mm v[88:91],v[212:215],v[144:147],v[88:91]
rt v[200:201],v243 offset:8576
rt v[202:203],v243 offset:9600
mm v[120:123],v[212:215],v[148:151],v[120:123]
cv v230,v80,v81
cv v232,v84,v85
mm v[120:123],v[208:211],v[132:135],v[120:123]
rt v[212:213],v243 offset:17024
rt v[214:215],v243 offset:18048
mm v[120:123],v[216:219],v[164:167],v[120:123]
cv v231,v82,v83
cv v233,v86,v87
wt vmcnt(14)
buffer_store_dwordx2 v[252:253], v251, s[64:67], s92 offen sc1
mm v[120:123],v[220:223],v[180:183],v[120:123]
rt v[208:209],v243 offset:25472
rt v[210:211],v243 offset:26496
mm v[88:91],v[220:223],v[176:179],v[88:91]
wt lgkmcnt(4)
mm v[88:91],v[216:219],v[160:163],v[88:91]
rt v[220:221],v243 offset:33920
rt v[222:223],v243 offset:34944
mm v[92:95],v[196:199],a[32:35],v[92:95]
v_permlane16_swap_b32 v230, v232
v_permlane16_swap_b32 v231, v233
mm v[92:95],v[192:195],a[40:43],v[92:95]
rt v[216:217],v243 offset:42368
rt v[218:219],v243 offset:43392
mm v[124:127],v[192:195],a[44:47],v[124:127]
mm v[124:127],v[196:199],a[36:39],v[124:127]
mm v[124:127],v[204:207],a[52:55],v[124:127]
st v[230:233],v238,s[32:35],s96 offen offset:2048 sc1
mm v[124:127],v[200:203],a[60:63],v[124:127]
mm v[92:95],v[200:203],a[56:59],v[92:95]
wt lgkmcnt(0)
mm v[92:95],v[204:207],a[48:51],v[92:95]
mm v[92:95],v[212:215],v[128:131],v[92:95]
mm v[92:95],v[208:211],v[144:147],v[92:95]
mm v[124:127],v[208:211],v[148:151],v[124:127]
mm v[124:127],v[212:215],v[132:135],v[124:127]
mm v[124:127],v[220:223],v[164:167],v[124:127]
mm v[124:127],v[216:219],v[180:183],v[124:127]
mm v[92:95],v[216:219],v[176:179],v[92:95]
mm v[92:95],v[220:223],v[160:163],v[92:95]
s_nop 7
cv v234,v120,v121
cv v236,v124,v125
cv v235,v122,v123
)ASM" R"ASM(cv v237,v126,v127
cv v230,v88,v89
cv v232,v92,v93
cv v231,v90,v91
cv v233,v94,v95
v_permlane16_swap_b32 v234, v236
v_permlane16_swap_b32 v235, v237
st v[234:237],v238,s[32:35],s97 offen offset:3072 sc1
v_permlane16_swap_b32 v230, v232
v_permlane16_swap_b32 v231, v233
st v[230:233],v238,s[32:35],s96 offen offset:3072 sc1
.La5_LT_0:
.La5_LW3_5:
s_cbranch_execz .La5_LA_RETURN
.La5_LW7_6:
s_cbranch_execz .La5_LA_RETURN
s_lshl_b32 s91, s76, 5
s_add_u32 s91, s91, s90
s_lshl_b32 s20, s91, 9
v_mov_b32 v236, 157440
ds_read_b32 v236, v236
s_andn2_b32 s93, s20, 0x1fff
s_add_u32 s20, s20, s93
s_add_u32 s20, s20, 2097152
s_lshr_b32 s93, s91, 1
s_lshl_b32 s93, s93, 15
s_and_b32 s94, s91, 1
s_lshl2_add_u32 s94, s94, s3
s_lshl_b32 s94, s94, 7
s_add_u32 s97, s93, s94
s_add_u32 s32, s26, 71303168
s_addc_u32 s33, s27, 0
s_and_b32 s33, s33, 0xffff
s_mov_b32 s34, 16777216
s_mov_b32 s35, 0x20000
s_add_u32 s44, s26, 171966464
s_addc_u32 s45, s27, 0
s_and_b32 s45, s45, 0xffff
s_mov_b32 s46, 2097152
s_mov_b32 s47, 0x20000
s_add_u32 s56, s38, 0
s_addc_u32 s57, s39, 0
s_and_b32 s57, s57, 0xffff
s_mov_b32 s58, 4194304
s_mov_b32 s59, 0x20000
s_add_u32 s68, s16, 0
s_addc_u32 s69, s17, 0
s_and_b32 s69, s69, 0xffff
s_mov_b32 s70, 4194304
s_mov_b32 s71, 0x20000
v_and_b32 v230, 0x70, v255
v_lshrrev_b32 v231, 7, v255
v_lshl_add_u32 v230, v231, 10, v230
v_add_u32 v240, s97, v230
v_xor_b32 v241, 8192, v240
v_xor_b32 v242, 16384, v240
v_xor_b32 v243, 24576, v240
wt lgkmcnt(0)
v_readfirstlane_b32 s93, v236
s_nop 0
s_and_b32 s93, s93, 8192
s_add_u32 s20, s20, s93
v_mov_b32 v249, v250
buffer_load_dwordx2 v[250:251], v248, s[64:67], s20 offen sc1
v_mov_b32 v237, 0x3f803f80
v_mov_b32 v236, 0
wt vmcnt(8)
v_dot2_f32_bf16 v236, v140, v237, v236
v_dot2_f32_bf16 v236, v141, v237, v236
v_dot2_f32_bf16 v236, v142, v237, v236
v_dot2_f32_bf16 v236, v143, v237, v236
v_dot2_f32_bf16 v236, v156, v237, v236
v_dot2_f32_bf16 v236, v157, v237, v236
v_dot2_f32_bf16 v236, v158, v237, v236
v_dot2_f32_bf16 v236, v159, v237, v236
v_dot2_f32_bf16 v236, v172, v237, v236
v_dot2_f32_bf16 v236, v173, v237, v236
v_dot2_f32_bf16 v236, v174, v237, v236
v_dot2_f32_bf16 v236, v175, v237, v236
v_dot2_f32_bf16 v236, v188, v237, v236
v_dot2_f32_bf16 v236, v189, v237, v236
v_dot2_f32_bf16 v236, v190, v237, v236
v_dot2_f32_bf16 v236, v191, v237, v236
v_lshlrev_b32 v0, 16, v136
v_and_b32 v1, 0xffff0000, v136
v_lshlrev_b32 v2, 16, v137
v_and_b32 v3, 0xffff0000, v137
v_lshlrev_b32 v4, 16, v138
v_and_b32 v5, 0xffff0000, v138
v_lshlrev_b32 v6, 16, v139
v_and_b32 v7, 0xffff0000, v139
v_lshlrev_b32 v8, 16, v152
v_and_b32 v9, 0xffff0000, v152
v_lshlrev_b32 v10, 16, v153
v_and_b32 v11, 0xffff0000, v153
v_lshlrev_b32 v12, 16, v154
v_and_b32 v13, 0xffff0000, v154
v_lshlrev_b32 v14, 16, v155
v_and_b32 v15, 0xffff0000, v155
v_lshlrev_b32 v16, 16, v168
)ASM" R"ASM(v_and_b32 v17, 0xffff0000, v168
v_lshlrev_b32 v18, 16, v169
v_and_b32 v19, 0xffff0000, v169
v_lshlrev_b32 v20, 16, v170
v_and_b32 v21, 0xffff0000, v170
v_lshlrev_b32 v22, 16, v171
v_and_b32 v23, 0xffff0000, v171
v_lshlrev_b32 v24, 16, v184
v_and_b32 v25, 0xffff0000, v184
v_lshlrev_b32 v26, 16, v185
v_and_b32 v27, 0xffff0000, v185
v_lshlrev_b32 v28, 16, v186
v_and_b32 v29, 0xffff0000, v186
v_lshlrev_b32 v30, 16, v187
v_and_b32 v31, 0xffff0000, v187
s_nop 1
v_add_f32_dpp v236, v236, v236 quad_perm:[1,0,3,2] row_mask:0xf bank_mask:0xf
s_nop 1
v_add_f32_dpp v236, v236, v236 quad_perm:[2,3,0,1] row_mask:0xf bank_mask:0xf
s_nop 1
v_add_f32_dpp v236, v236, v236 row_ror:8 row_mask:0xf bank_mask:0xf
v_mov_b32 v230, v236
s_nop 1
v_permlane16_swap_b32 v230, v236
s_nop 1
v_add_f32 v236, v236, v230
v_mov_b32 v230, v236
s_nop 1
v_permlane32_swap_b32 v230, v236
s_nop 1
v_add_f32 v236, v236, v230
s_mov_b64 exec, 0
s_cmp_eq_u32 s3, 0
s_cbranch_scc1 .La5_LW4_0
s_cmp_eq_u32 s3, 1
s_cbranch_scc1 .La5_LW4_1
s_cmp_eq_u32 s3, 2
s_cbranch_scc1 .La5_LW4_2
s_branch .La5_LW4_3
.La5_LW4_R0:
s_mov_b64 exec, -1
wt vmcnt(1)
buffer_store_dwordx2 v[252:253], v249, s[64:67], s92 offen sc1
s_mov_b32 s96, 0
wt vmcnt(1)
s_branch .La5_LT_P2
; ---- h = x + attn, LayerNorm statistics, router score, two best of eight rows
.La5_LT_ROWS:
s_mov_b32 s96, 0
.La5_LT_P3:
buffer_load_dwordx2 v[250:251], v248, s[64:67], s20 offen sc1
wt vmcnt(0)
.La5_LT_P2:
v_cmp_eq_u64 vcc, v[250:251], v[252:253]
s_add_u32 s96, s96, 1
s_lshr_b32 s92, vcc_lo, 1
s_and_b32 s92, s92, vcc_lo
s_lshr_b32 s93, s92, 2
s_and_b32 s92, s92, s93
s_lshr_b32 s93, s92, 4
s_and_b32 s92, s92, s93
s_lshr_b32 s93, s96, 20
s_mul_i32 s93, s93, 0x01010101
s_or_b32 s92, s92, s93
s_andn2_b32 s92, 0x01010101, s92
s_bcnt1_i32_b32 s93, s92
s_cmp_gt_u32 s93, 1
s_cbranch_scc1 .La5_LT_P3S
s_or_b32 s93, s92, 0x01000000
s_ff1_i32_b32 s93, s93
s_lshl_b32 s80, s93, 19
s_xor_b32 s93, s80, 8388608
s_xor_b32 s94, s80, 12582912
s_xor_b32 s95, s80, 4194304
ld v[208:211],v240,s[32:35],s93 offen sc1
ld v[212:215],v241,s[32:35],s93 offen sc1
ld v[216:219],v242,s[32:35],s93 offen sc1
ld v[220:223],v243,s[32:35],s93 offen sc1
ld v[112:115],v240,s[32:35],s94 offen sc1
ld v[116:119],v241,s[32:35],s94 offen sc1
ld v[120:123],v242,s[32:35],s94 offen sc1
ld v[124:127],v243,s[32:35],s94 offen sc1
ld v[96:99],v240,s[32:35],s95 offen sc1
ld v[100:103],v241,s[32:35],s95 offen sc1
ld v[104:107],v242,s[32:35],s95 offen sc1
ld v[108:111],v243,s[32:35],s95 offen sc1
s_cmp_lg_u32 s92, 0
s_cbranch_scc1 .La5_LT_P4
ld v[192:195],v240,s[32:35],s80 offen sc1
ld v[196:199],v241,s[32:35],s80 offen sc1
ld v[200:203],v242,s[32:35],s80 offen sc1
ld v[204:207],v243,s[32:35],s80 offen sc1
.La5_LT_P4:
s_lshr_b32 s93, s80, 16
v_and_b32 v248, 56, v248
s_add_u32 s20, s20, s93
buffer_load_dwordx2 v[250:251], v248, s[64:67], s20 offen sc1
wt vmcnt(8)
v_lshlrev_b32 v32, 16, v208
v_and_b32 v33, 0xffff0000, v208
)ASM" R"ASM(v_lshlrev_b32 v230, 16, v112
v_and_b32 v231, 0xffff0000, v112
v_pk_add_f32 v[32:33], v[32:33], v[230:231]
v_lshlrev_b32 v34, 16, v209
v_and_b32 v35, 0xffff0000, v209
v_lshlrev_b32 v230, 16, v113
v_and_b32 v231, 0xffff0000, v113
v_pk_add_f32 v[34:35], v[34:35], v[230:231]
v_lshlrev_b32 v36, 16, v210
v_and_b32 v37, 0xffff0000, v210
v_lshlrev_b32 v230, 16, v114
v_and_b32 v231, 0xffff0000, v114
v_pk_add_f32 v[36:37], v[36:37], v[230:231]
v_lshlrev_b32 v38, 16, v211
v_and_b32 v39, 0xffff0000, v211
v_lshlrev_b32 v230, 16, v115
v_and_b32 v231, 0xffff0000, v115
v_pk_add_f32 v[38:39], v[38:39], v[230:231]
wt vmcnt(7)
v_lshlrev_b32 v40, 16, v212
v_and_b32 v41, 0xffff0000, v212
v_lshlrev_b32 v230, 16, v116
v_and_b32 v231, 0xffff0000, v116
v_pk_add_f32 v[40:41], v[40:41], v[230:231]
v_lshlrev_b32 v42, 16, v213
v_and_b32 v43, 0xffff0000, v213
v_lshlrev_b32 v230, 16, v117
v_and_b32 v231, 0xffff0000, v117
v_pk_add_f32 v[42:43], v[42:43], v[230:231]
v_lshlrev_b32 v44, 16, v214
v_and_b32 v45, 0xffff0000, v214
v_lshlrev_b32 v230, 16, v118
v_and_b32 v231, 0xffff0000, v118
v_pk_add_f32 v[44:45], v[44:45], v[230:231]
v_lshlrev_b32 v46, 16, v215
v_and_b32 v47, 0xffff0000, v215
v_lshlrev_b32 v230, 16, v119
v_and_b32 v231, 0xffff0000, v119
v_pk_add_f32 v[46:47], v[46:47], v[230:231]
wt vmcnt(6)
v_lshlrev_b32 v48, 16, v216
v_and_b32 v49, 0xffff0000, v216
v_lshlrev_b32 v230, 16, v120
v_and_b32 v231, 0xffff0000, v120
v_pk_add_f32 v[48:49], v[48:49], v[230:231]
v_lshlrev_b32 v50, 16, v217
v_and_b32 v51, 0xffff0000, v217
v_lshlrev_b32 v230, 16, v121
v_and_b32 v231, 0xffff0000, v121
v_pk_add_f32 v[50:51], v[50:51], v[230:231]
v_lshlrev_b32 v52, 16, v218
v_and_b32 v53, 0xffff0000, v218
v_lshlrev_b32 v230, 16, v122
v_and_b32 v231, 0xffff0000, v122
v_pk_add_f32 v[52:53], v[52:53], v[230:231]
v_lshlrev_b32 v54, 16, v219
v_and_b32 v55, 0xffff0000, v219
v_lshlrev_b32 v230, 16, v123
v_and_b32 v231, 0xffff0000, v123
v_pk_add_f32 v[54:55], v[54:55], v[230:231]
wt vmcnt(5)
v_lshlrev_b32 v56, 16, v220
v_and_b32 v57, 0xffff0000, v220
v_lshlrev_b32 v230, 16, v124
v_and_b32 v231, 0xffff0000, v124
v_pk_add_f32 v[56:57], v[56:57], v[230:231]
v_lshlrev_b32 v58, 16, v221
v_and_b32 v59, 0xffff0000, v221
v_lshlrev_b32 v230, 16, v125
v_and_b32 v231, 0xffff0000, v125
v_pk_add_f32 v[58:59], v[58:59], v[230:231]
v_lshlrev_b32 v60, 16, v222
v_and_b32 v61, 0xffff0000, v222
v_lshlrev_b32 v230, 16, v126
v_and_b32 v231, 0xffff0000, v126
v_pk_add_f32 v[60:61], v[60:61], v[230:231]
v_lshlrev_b32 v62, 16, v223
v_and_b32 v63, 0xffff0000, v223
v_lshlrev_b32 v230, 16, v127
v_and_b32 v231, 0xffff0000, v127
v_pk_add_f32 v[62:63], v[62:63], v[230:231]
.La5_LT_PH:
wt vmcnt(1)
v_lshlrev_b32 v64, 16, v96
v_and_b32 v65, 0xffff0000, v96
v_lshlrev_b32 v66, 16, v97
v_and_b32 v67, 0xffff0000, v97
v_lshlrev_b32 v68, 16, v98
v_and_b32 v69, 0xffff0000, v98
v_lshlrev_b32 v70, 16, v99
v_and_b32 v71, 0xffff0000, v99
v_lshlrev_b32 v72, 16, v100
v_and_b32 v73, 0xffff0000, v100
v_lshlrev_b32 v74, 16, v101
v_and_b32 v75, 0xffff0000, v101
v_lshlrev_b32 v76, 16, v102
v_and_b32 v77, 0xffff0000, v102
v_lshlrev_b32 v78, 16, v103
v_and_b32 v79, 0xffff0000, v103
v_lshlrev_b32 v80, 16, v104
)ASM" R"ASM(v_and_b32 v81, 0xffff0000, v104
v_lshlrev_b32 v82, 16, v105
v_and_b32 v83, 0xffff0000, v105
v_lshlrev_b32 v84, 16, v106
v_and_b32 v85, 0xffff0000, v106
v_lshlrev_b32 v86, 16, v107
v_and_b32 v87, 0xffff0000, v107
v_lshlrev_b32 v88, 16, v108
v_and_b32 v89, 0xffff0000, v108
v_lshlrev_b32 v90, 16, v109
v_and_b32 v91, 0xffff0000, v109
v_lshlrev_b32 v92, 16, v110
v_and_b32 v93, 0xffff0000, v110
v_lshlrev_b32 v94, 16, v111
v_and_b32 v95, 0xffff0000, v111
v_mov_b32 v232, 0
v_mov_b32 v233, 0
v_mov_b32 v234, 0
s_cmp_eq_u32 s92, 0
s_cbranch_scc1 .La5_LT_SUM
.La5_LT_P5:
wt vmcnt(0)
v_cmp_eq_u64 vcc, v[250:251], v[252:253]
s_add_u32 s96, s96, 1
s_cmp_eq_u64 vcc, -1
s_cbranch_scc1 .La5_LT_P6
s_lshr_b32 s93, s96, 20
s_cbranch_scc1 .La5_LT_P6
s_sleep 3
buffer_load_dwordx2 v[250:251], v248, s[64:67], s20 offen sc1
s_branch .La5_LT_P5
.La5_LT_P3S:
s_sleep 4
s_bcnt0_i32_b32 s93, vcc_lo
s_cmp_le_u32 s93, 8
s_cbranch_scc1 .La5_LT_P3
s_sleep 24
s_branch .La5_LT_P3
.La5_LT_P6:
ld v[192:195],v240,s[32:35],s80 offen sc1
ld v[196:199],v241,s[32:35],s80 offen sc1
ld v[200:203],v242,s[32:35],s80 offen sc1
ld v[204:207],v243,s[32:35],s80 offen sc1
.La5_LT_SUM:
wt vmcnt(3)
v_lshlrev_b32 v208, 16, v192
v_and_b32 v209, 0xffff0000, v192
v_lshlrev_b32 v210, 16, v193
v_and_b32 v211, 0xffff0000, v193
v_lshlrev_b32 v212, 16, v194
v_and_b32 v213, 0xffff0000, v194
v_lshlrev_b32 v214, 16, v195
v_and_b32 v215, 0xffff0000, v195
v_pk_add_f32 v[208:209], v[208:209], v[64:65]
v_pk_add_f32 v[210:211], v[210:211], v[66:67]
v_pk_add_f32 v[212:213], v[212:213], v[68:69]
v_pk_add_f32 v[214:215], v[214:215], v[70:71]
v_pk_add_f32 v[208:209], v[208:209], v[32:33]
v_pk_add_f32 v[210:211], v[210:211], v[34:35]
v_pk_add_f32 v[212:213], v[212:213], v[36:37]
v_pk_add_f32 v[214:215], v[214:215], v[38:39]
v_pk_add_f32 v[208:209], v[208:209], v[0:1]
v_pk_add_f32 v[210:211], v[210:211], v[2:3]
v_pk_add_f32 v[212:213], v[212:213], v[4:5]
v_pk_add_f32 v[214:215], v[214:215], v[6:7]
cv v96,v208,v209
cv v97,v210,v211
cv v98,v212,v213
cv v99,v214,v215
v_dot2_f32_bf16 v232, v96, v237, v232
v_dot2_f32_bf16 v233, v96, v96, v233
v_dot2_f32_bf16 v234, v96, v140, v234
v_dot2_f32_bf16 v232, v97, v237, v232
v_dot2_f32_bf16 v233, v97, v97, v233
v_dot2_f32_bf16 v234, v97, v141, v234
v_dot2_f32_bf16 v232, v98, v237, v232
v_dot2_f32_bf16 v233, v98, v98, v233
v_dot2_f32_bf16 v234, v98, v142, v234
v_dot2_f32_bf16 v232, v99, v237, v232
v_dot2_f32_bf16 v233, v99, v99, v233
v_dot2_f32_bf16 v234, v99, v143, v234
wt vmcnt(2)
v_lshlrev_b32 v208, 16, v196
v_and_b32 v209, 0xffff0000, v196
v_lshlrev_b32 v210, 16, v197
v_and_b32 v211, 0xffff0000, v197
v_lshlrev_b32 v212, 16, v198
v_and_b32 v213, 0xffff0000, v198
v_lshlrev_b32 v214, 16, v199
v_and_b32 v215, 0xffff0000, v199
v_pk_add_f32 v[208:209], v[208:209], v[72:73]
v_pk_add_f32 v[210:211], v[210:211], v[74:75]
v_pk_add_f32 v[212:213], v[212:213], v[76:77]
v_pk_add_f32 v[214:215], v[214:215], v[78:79]
v_pk_add_f32 v[208:209], v[208:209], v[40:41]
v_pk_add_f32 v[210:211], v[210:211], v[42:43]
v_pk_add_f32 v[212:213], v[212:213], v[44:45]
v_pk_add_f32 v[214:215], v[214:215], v[46:47]
v_pk_add_f32 v[208:209], v[208:209], v[8:9]
v_pk_add_f32 v[210:211], v[210:211], v[10:11]
)ASM" R"ASM(v_pk_add_f32 v[212:213], v[212:213], v[12:13]
v_pk_add_f32 v[214:215], v[214:215], v[14:15]
cv v100,v208,v209
cv v101,v210,v211
cv v102,v212,v213
cv v103,v214,v215
v_dot2_f32_bf16 v232, v100, v237, v232
v_dot2_f32_bf16 v233, v100, v100, v233
v_dot2_f32_bf16 v234, v100, v156, v234
v_dot2_f32_bf16 v232, v101, v237, v232
v_dot2_f32_bf16 v233, v101, v101, v233
.La5_LW4_0:
s_cbranch_execz .La5_LW4_4
v_dot2_f32_bf16 v234, v101, v157, v234
v_dot2_f32_bf16 v232, v102, v237, v232
v_dot2_f32_bf16 v233, v102, v102, v233
v_dot2_f32_bf16 v234, v102, v158, v234
v_dot2_f32_bf16 v232, v103, v237, v232
v_dot2_f32_bf16 v233, v103, v103, v233
v_dot2_f32_bf16 v234, v103, v159, v234
wt vmcnt(1)
v_lshlrev_b32 v208, 16, v200
v_and_b32 v209, 0xffff0000, v200
v_lshlrev_b32 v210, 16, v201
v_and_b32 v211, 0xffff0000, v201
v_lshlrev_b32 v212, 16, v202
v_and_b32 v213, 0xffff0000, v202
v_lshlrev_b32 v214, 16, v203
v_and_b32 v215, 0xffff0000, v203
v_pk_add_f32 v[208:209], v[208:209], v[80:81]
v_pk_add_f32 v[210:211], v[210:211], v[82:83]
v_pk_add_f32 v[212:213], v[212:213], v[84:85]
v_pk_add_f32 v[214:215], v[214:215], v[86:87]
v_pk_add_f32 v[208:209], v[208:209], v[48:49]
v_pk_add_f32 v[210:211], v[210:211], v[50:51]
v_pk_add_f32 v[212:213], v[212:213], v[52:53]
v_pk_add_f32 v[214:215], v[214:215], v[54:55]
v_pk_add_f32 v[208:209], v[208:209], v[16:17]
v_pk_add_f32 v[210:211], v[210:211], v[18:19]
v_pk_add_f32 v[212:213], v[212:213], v[20:21]
v_pk_add_f32 v[214:215], v[214:215], v[22:23]
cv v104,v208,v209
cv v105,v210,v211
cv v106,v212,v213
cv v107,v214,v215
v_dot2_f32_bf16 v232, v104, v237, v232
v_dot2_f32_bf16 v233, v104, v104, v233
v_dot2_f32_bf16 v234, v104, v172, v234
v_dot2_f32_bf16 v232, v105, v237, v232
v_dot2_f32_bf16 v233, v105, v105, v233
v_dot2_f32_bf16 v234, v105, v173, v234
v_dot2_f32_bf16 v232, v106, v237, v232
v_dot2_f32_bf16 v233, v106, v106, v233
v_dot2_f32_bf16 v234, v106, v174, v234
v_dot2_f32_bf16 v232, v107, v237, v232
v_dot2_f32_bf16 v233, v107, v107, v233
v_dot2_f32_bf16 v234, v107, v175, v234
wt vmcnt(0)
v_lshlrev_b32 v208, 16, v204
v_and_b32 v209, 0xffff0000, v204
v_lshlrev_b32 v210, 16, v205
v_and_b32 v211, 0xffff0000, v205
v_lshlrev_b32 v212, 16, v206
v_and_b32 v213, 0xffff0000, v206
v_lshlrev_b32 v214, 16, v207
v_and_b32 v215, 0xffff0000, v207
v_pk_add_f32 v[208:209], v[208:209], v[88:89]
v_pk_add_f32 v[210:211], v[210:211], v[90:91]
v_pk_add_f32 v[212:213], v[212:213], v[92:93]
v_pk_add_f32 v[214:215], v[214:215], v[94:95]
v_pk_add_f32 v[208:209], v[208:209], v[56:57]
v_pk_add_f32 v[210:211], v[210:211], v[58:59]
v_pk_add_f32 v[212:213], v[212:213], v[60:61]
v_pk_add_f32 v[214:215], v[214:215], v[62:63]
v_pk_add_f32 v[208:209], v[208:209], v[24:25]
v_pk_add_f32 v[210:211], v[210:211], v[26:27]
v_pk_add_f32 v[212:213], v[212:213], v[28:29]
v_pk_add_f32 v[214:215], v[214:215], v[30:31]
cv v108,v208,v209
cv v109,v210,v211
cv v110,v212,v213
cv v111,v214,v215
v_dot2_f32_bf16 v232, v108, v237, v232
v_dot2_f32_bf16 v233, v108, v108, v233
v_dot2_f32_bf16 v234, v108, v188, v234
v_dot2_f32_bf16 v232, v109, v237, v232
v_dot2_f32_bf16 v233, v109, v109, v233
v_dot2_f32_bf16 v234, v109, v189, v234
v_dot2_f32_bf16 v232, v110, v237, v232
v_dot2_f32_bf16 v233, v110, v110, v233
v_dot2_f32_bf16 v234, v110, v190, v234
v_dot2_f32_bf16 v232, v111, v237, v232
v_dot2_f32_bf16 v233, v111, v111, v233
v_dot2_f32_bf16 v234, v111, v191, v234
wt vmcnt(0)
s_lshl2_add_u32 s92, s91, s3
s_lshl_b32 s92, s92, 1
s_sub_u32 s92, 0xffff, s92
v_add_f32_dpp v232, v232, v232 quad_perm:[1,0,3,2] row_mask:0xf bank_mask:0xf
v_add_f32_dpp v233, v233, v233 quad_perm:[1,0,3,2] row_mask:0xf bank_mask:0xf
)ASM" R"ASM(v_add_f32_dpp v234, v234, v234 quad_perm:[1,0,3,2] row_mask:0xf bank_mask:0xf
v_add_f32_dpp v232, v232, v232 quad_perm:[2,3,0,1] row_mask:0xf bank_mask:0xf
v_add_f32_dpp v233, v233, v233 quad_perm:[2,3,0,1] row_mask:0xf bank_mask:0xf
v_add_f32_dpp v234, v234, v234 quad_perm:[2,3,0,1] row_mask:0xf bank_mask:0xf
v_add_f32_dpp v232, v232, v232 row_ror:8 row_mask:0xf bank_mask:0xf
v_add_f32_dpp v233, v233, v233 row_ror:8 row_mask:0xf bank_mask:0xf
v_add_f32_dpp v234, v234, v234 row_ror:8 row_mask:0xf bank_mask:0xf
v_mov_b32 v230, v232
v_mov_b32 v231, v233
v_mov_b32 v235, v234
v_permlane16_swap_b32 v230, v232
v_permlane16_swap_b32 v231, v233
v_permlane16_swap_b32 v235, v234
v_add_f32 v232, v232, v230
v_add_f32 v233, v233, v231
v_add_f32 v234, v234, v235
v_mov_b32 v230, v232
v_mov_b32 v231, v233
v_mov_b32 v235, v234
v_permlane32_swap_b32 v230, v232
v_permlane32_swap_b32 v231, v233
v_permlane32_swap_b32 v235, v234
v_add_f32 v232, v232, v230
v_add_f32 v233, v233, v231
v_add_f32 v234, v234, v235
v_mul_f32 v244, 0x3a800000, v232
v_mul_f32 v233, 0x3a800000, v233
v_fma_f32 v230, -v244, v244, v233
v_mul_f32 v233, 0x3a800000, v233
v_cmp_nge_f32 vcc, v230, v233
v_bfe_u32 v235, v255, 6, 1
v_sub_u32_e64 v235, s92, v235
s_cbranch_vccnz .La5_LT_VAR
.La5_LT_VOK:
cv v244,v244,v244
v_lshlrev_b32 v244, 16, v244
cv v230,v230,v230
v_lshlrev_b32 v230, 16, v230
v_fma_f32 v234, -v244, v236, v234
v_add_f32 v230, 0x3727c5ac, v230
cv v230,v230,v230
v_lshlrev_b32 v230, 16, v230
v_sqrt_f32 v230, v230
v_lshrrev_b32 v232, 3, v255
cv v230,v230,v230
v_lshlrev_b32 v231, 16, v230
v_rcp_f32 v246, v231
v_lshrrev_b32 v230, 16, v230
v_or_b32 v233, v244, v230
v_mul_f32 v234, v234, v246
s_lshl_b32 s93, s3, 4
cv v234,v234,v234
v_and_b32 v230, 8, v232
v_ashrrev_i32 v232, 31, v234
v_or_b32 v232, 0x80000000, v232
v_xor_b32 v232, v232, v234
v_and_b32 v232, 0xffff0000, v232
v_add_u32 v230, s93, v230
v_or_b32 v232, v232, v235
ds_write_b64 v230, v[232:233] offset:0
v_lshrrev_b32 v230, 1, v255
v_and_b32 v230, 56, v230
v_mov_b32 v112, v232
wt lgkmcnt(0)
s_barrier
ds_read_b64 v[232:233], v230 offset:0
s_lshl3_add_u32 s92, s90, s76
s_lshl_b32 s92, s92, 4
s_lshl_b32 s93, s3, 28
s_add_u32 s92, s92, s93
v_lshrrev_b32 v235, 7, v255
v_lshlrev_b32 v235, 12, v235
wt lgkmcnt(0)
v_mov_b32 v234, v232
s_nop 1
v_max_u32_dpp v234, v234, v234 quad_perm:[1,0,3,2] row_mask:0xf bank_mask:0xf
s_nop 1
v_max_u32_dpp v234, v234, v234 quad_perm:[2,3,0,1] row_mask:0xf bank_mask:0xf
s_nop 1
v_max_u32_dpp v234, v234, v234 row_half_mirror row_mask:0xf bank_mask:0xf
s_nop 1
v_cmp_eq_u32 vcc, v232, v234
v_add_u32 v231, 8, v235
s_nop 0
v_cndmask_b32 v234, v232, 0, vcc
v_cndmask_b32 v235, v231, v235, vcc
s_nop 0
v_max_u32_dpp v234, v234, v234 quad_perm:[1,0,3,2] row_mask:0xf bank_mask:0xf
s_nop 1
v_max_u32_dpp v234, v234, v234 quad_perm:[2,3,0,1] row_mask:0xf bank_mask:0xf
s_nop 1
v_max_u32_dpp v234, v234, v234 row_half_mirror row_mask:0xf bank_mask:0xf
s_nop 1
v_cmp_ge_u32 vcc, v232, v234
s_nop 1
s_mov_b64 exec, vcc
buffer_store_dwordx2 v[232:233], v235, s[44:47], s92 offen sc1
s_mov_b64 exec, -1
; ---- out = h + .La5_LN(h) of the own rows; FFN weights into MFMA order
.La5_LT_OUT:
)ASM" R"ASM(v_mul_f32 v244, v244, v246
v_mov_b32 v247, v246
v_xor_b32 v244, 0x80000000, v244
v_mov_b32 v245, v244
v_cmp_eq_u32 vcc, v112, v234
v_cmp_lt_u32 s[94:95], v112, v234
v_lshrrev_b32 v113, 7, v255
v_and_b32 v114, 48, v255
v_lshl_add_u32 v113, v113, 6, v114
v_add_u32 v114, 2048, v113
v_mov_b32 v115, 1073741824
v_cndmask_b32 v113, v113, v114, vcc
s_lshl_b32 s92, s91, 12
v_cndmask_b32 v113, v113, v115, s[94:95]
s_add_u32 s92, s92, 1048576
v_lshlrev_b32 v0, 16, v96
v_and_b32 v1, 0xffff0000, v96
v_pk_fma_f32 v[232:233], v[0:1], v[246:247], v[244:245]
cv v64,v232,v233
v_lshlrev_b32 v2, 16, v97
v_and_b32 v3, 0xffff0000, v97
v_pk_fma_f32 v[232:233], v[2:3], v[246:247], v[244:245]
cv v65,v232,v233
v_lshlrev_b32 v4, 16, v98
v_and_b32 v5, 0xffff0000, v98
v_pk_fma_f32 v[232:233], v[4:5], v[246:247], v[244:245]
cv v66,v232,v233
v_lshlrev_b32 v6, 16, v99
v_and_b32 v7, 0xffff0000, v99
v_pk_fma_f32 v[232:233], v[6:7], v[246:247], v[244:245]
cv v67,v232,v233
st v[64:67],v113,s[44:47],s92 offen offset:0 sc1
v_lshlrev_b32 v8, 16, v100
v_and_b32 v9, 0xffff0000, v100
v_pk_fma_f32 v[232:233], v[8:9], v[246:247], v[244:245]
cv v68,v232,v233
v_lshlrev_b32 v10, 16, v101
v_and_b32 v11, 0xffff0000, v101
v_pk_fma_f32 v[232:233], v[10:11], v[246:247], v[244:245]
cv v69,v232,v233
v_lshlrev_b32 v12, 16, v102
v_and_b32 v13, 0xffff0000, v102
v_pk_fma_f32 v[232:233], v[12:13], v[246:247], v[244:245]
cv v70,v232,v233
v_lshlrev_b32 v14, 16, v103
v_and_b32 v15, 0xffff0000, v103
v_pk_fma_f32 v[232:233], v[14:15], v[246:247], v[244:245]
cv v71,v232,v233
st v[68:71],v113,s[44:47],s92 offen offset:512 sc1
v_lshlrev_b32 v16, 16, v104
v_and_b32 v17, 0xffff0000, v104
v_pk_fma_f32 v[232:233], v[16:17], v[246:247], v[244:245]
cv v72,v232,v233
v_lshlrev_b32 v18, 16, v105
v_and_b32 v19, 0xffff0000, v105
v_pk_fma_f32 v[232:233], v[18:19], v[246:247], v[244:245]
cv v73,v232,v233
v_lshlrev_b32 v20, 16, v106
v_and_b32 v21, 0xffff0000, v106
v_pk_fma_f32 v[232:233], v[20:21], v[246:247], v[244:245]
cv v74,v232,v233
v_lshlrev_b32 v22, 16, v107
v_and_b32 v23, 0xffff0000, v107
v_pk_fma_f32 v[232:233], v[22:23], v[246:247], v[244:245]
cv v75,v232,v233
st v[72:75],v113,s[44:47],s92 offen offset:1024 sc1
v_lshlrev_b32 v24, 16, v108
v_and_b32 v25, 0xffff0000, v108
v_pk_fma_f32 v[232:233], v[24:25], v[246:247], v[244:245]
cv v76,v232,v233
v_lshlrev_b32 v26, 16, v109
v_and_b32 v27, 0xffff0000, v109
v_pk_fma_f32 v[232:233], v[26:27], v[246:247], v[244:245]
cv v77,v232,v233
v_lshlrev_b32 v28, 16, v110
v_and_b32 v29, 0xffff0000, v110
v_pk_fma_f32 v[232:233], v[28:29], v[246:247], v[244:245]
cv v78,v232,v233
v_lshlrev_b32 v30, 16, v111
v_and_b32 v31, 0xffff0000, v111
v_pk_fma_f32 v[232:233], v[30:31], v[246:247], v[244:245]
cv v79,v232,v233
st v[76:79],v113,s[44:47],s92 offen offset:1536 sc1
v_lshlrev_b32 v234, 16, v64
v_and_b32 v235, 0xffff0000, v64
v_pk_add_f32 v[230:231], v[0:1], v[234:235]
cv v192,v230,v231
v_lshlrev_b32 v234, 16, v65
v_and_b32 v235, 0xffff0000, v65
v_pk_add_f32 v[230:231], v[2:3], v[234:235]
cv v193,v230,v231
v_lshlrev_b32 v234, 16, v66
v_and_b32 v235, 0xffff0000, v66
v_pk_add_f32 v[230:231], v[4:5], v[234:235]
cv v194,v230,v231
v_lshlrev_b32 v234, 16, v67
v_and_b32 v235, 0xffff0000, v67
v_pk_add_f32 v[230:231], v[6:7], v[234:235]
cv v195,v230,v231
v_lshlrev_b32 v234, 16, v68
)ASM" R"ASM(v_and_b32 v235, 0xffff0000, v68
v_pk_add_f32 v[230:231], v[8:9], v[234:235]
cv v196,v230,v231
v_lshlrev_b32 v234, 16, v69
v_and_b32 v235, 0xffff0000, v69
v_pk_add_f32 v[230:231], v[10:11], v[234:235]
cv v197,v230,v231
v_lshlrev_b32 v234, 16, v70
v_and_b32 v235, 0xffff0000, v70
v_pk_add_f32 v[230:231], v[12:13], v[234:235]
cv v198,v230,v231
v_lshlrev_b32 v234, 16, v71
v_and_b32 v235, 0xffff0000, v71
v_pk_add_f32 v[230:231], v[14:15], v[234:235]
cv v199,v230,v231
v_lshlrev_b32 v234, 16, v72
v_and_b32 v235, 0xffff0000, v72
v_pk_add_f32 v[230:231], v[16:17], v[234:235]
cv v200,v230,v231
v_lshlrev_b32 v234, 16, v73
v_and_b32 v235, 0xffff0000, v73
v_pk_add_f32 v[230:231], v[18:19], v[234:235]
cv v201,v230,v231
v_lshlrev_b32 v234, 16, v74
v_and_b32 v235, 0xffff0000, v74
v_pk_add_f32 v[230:231], v[20:21], v[234:235]
cv v202,v230,v231
v_lshlrev_b32 v234, 16, v75
v_and_b32 v235, 0xffff0000, v75
v_pk_add_f32 v[230:231], v[22:23], v[234:235]
cv v203,v230,v231
v_lshlrev_b32 v234, 16, v76
v_and_b32 v235, 0xffff0000, v76
v_pk_add_f32 v[230:231], v[24:25], v[234:235]
cv v204,v230,v231
v_lshlrev_b32 v234, 16, v77
v_and_b32 v235, 0xffff0000, v77
v_pk_add_f32 v[230:231], v[26:27], v[234:235]
cv v205,v230,v231
v_lshlrev_b32 v234, 16, v78
v_and_b32 v235, 0xffff0000, v78
v_pk_add_f32 v[230:231], v[28:29], v[234:235]
cv v206,v230,v231
v_lshlrev_b32 v234, 16, v79
v_and_b32 v235, 0xffff0000, v79
v_pk_add_f32 v[230:231], v[30:31], v[234:235]
cv v207,v230,v231
s_cmp_eq_u32 s30, 0
s_cbranch_scc1 .La5_LT_SEL
s_mov_b32 s94, 0x05040100
s_mov_b32 s95, 0x07060302
wt vmcnt(0)
ar v230,a224
ar v231,a228
v_perm_b32 v128, v231, v230, s94
v_perm_b32 v132, v231, v230, s95
ar v230,a232
ar v231,a236
v_perm_b32 v129, v231, v230, s94
v_perm_b32 v133, v231, v230, s95
ar v230,a240
ar v231,a244
v_perm_b32 v130, v231, v230, s94
v_perm_b32 v134, v231, v230, s95
ar v230,a248
ar v231,a252
v_perm_b32 v131, v231, v230, s94
v_perm_b32 v135, v231, v230, s95
ar v230,a225
ar v231,a229
v_perm_b32 v136, v231, v230, s94
v_perm_b32 v140, v231, v230, s95
ar v230,a233
ar v231,a237
v_perm_b32 v137, v231, v230, s94
v_perm_b32 v141, v231, v230, s95
ar v230,a241
ar v231,a245
v_perm_b32 v138, v231, v230, s94
v_perm_b32 v142, v231, v230, s95
ar v230,a249
ar v231,a253
v_perm_b32 v139, v231, v230, s94
v_perm_b32 v143, v231, v230, s95
ar v230,a226
ar v231,a230
v_perm_b32 v144, v231, v230, s94
v_perm_b32 v148, v231, v230, s95
ar v230,a234
ar v231,a238
v_perm_b32 v145, v231, v230, s94
v_perm_b32 v149, v231, v230, s95
ar v230,a242
ar v231,a246
v_perm_b32 v146, v231, v230, s94
v_perm_b32 v150, v231, v230, s95
ar v230,a250
ar v231,a254
v_perm_b32 v147, v231, v230, s94
v_perm_b32 v151, v231, v230, s95
)ASM" R"ASM(ar v230,a227
ar v231,a231
v_perm_b32 v152, v231, v230, s94
v_perm_b32 v156, v231, v230, s95
ar v230,a235
ar v231,a239
v_perm_b32 v153, v231, v230, s94
v_perm_b32 v157, v231, v230, s95
ar v230,a243
ar v231,a247
v_perm_b32 v154, v231, v230, s94
v_perm_b32 v158, v231, v230, s95
ar v230,a251
ar v231,a255
v_perm_b32 v155, v231, v230, s94
v_perm_b32 v159, v231, v230, s95
ar v230,a64
ar v231,a68
v_perm_b32 v160, v231, v230, s94
v_perm_b32 v164, v231, v230, s95
ar v230,a72
ar v231,a76
v_perm_b32 v161, v231, v230, s94
v_perm_b32 v165, v231, v230, s95
ar v230,a80
ar v231,a84
v_perm_b32 v162, v231, v230, s94
v_perm_b32 v166, v231, v230, s95
ar v230,a88
ar v231,a92
v_perm_b32 v163, v231, v230, s94
v_perm_b32 v167, v231, v230, s95
ar v230,a65
ar v231,a69
v_perm_b32 v168, v231, v230, s94
v_perm_b32 v172, v231, v230, s95
ar v230,a73
ar v231,a77
v_perm_b32 v169, v231, v230, s94
v_perm_b32 v173, v231, v230, s95
ar v230,a81
ar v231,a85
v_perm_b32 v170, v231, v230, s94
v_perm_b32 v174, v231, v230, s95
ar v230,a89
ar v231,a93
v_perm_b32 v171, v231, v230, s94
v_perm_b32 v175, v231, v230, s95
ar v230,a66
ar v231,a70
v_perm_b32 v176, v231, v230, s94
v_perm_b32 v180, v231, v230, s95
ar v230,a74
ar v231,a78
v_perm_b32 v177, v231, v230, s94
v_perm_b32 v181, v231, v230, s95
ar v230,a82
ar v231,a86
v_perm_b32 v178, v231, v230, s94
v_perm_b32 v182, v231, v230, s95
ar v230,a90
ar v231,a94
v_perm_b32 v179, v231, v230, s94
v_perm_b32 v183, v231, v230, s95
ar v230,a67
ar v231,a71
v_perm_b32 v184, v231, v230, s94
v_perm_b32 v188, v231, v230, s95
ar v230,a75
ar v231,a79
v_perm_b32 v185, v231, v230, s94
v_perm_b32 v189, v231, v230, s95
ar v230,a83
ar v231,a87
v_perm_b32 v186, v231, v230, s94
v_perm_b32 v190, v231, v230, s95
ar v230,a91
ar v231,a95
v_perm_b32 v187, v231, v230, s94
v_perm_b32 v191, v231, v230, s95
s_and_b32 s92, s3, 1
s_lshr_b32 s93, s3, 1
v_lshrrev_b32 v230, 6, v255
v_and_b32 v231, 48, v255
v_lshl_add_u32 v240, v230, 10, v231
s_lshl_b32 s96, s92, 14
v_add_u32 v240, s96, v240
s_lshl_b32 s96, s92, 10
v_add_u32 v241, s96, v255
s_lshl_b32 s97, s3, 10
v_add_u32 v208, s97, v255
v_bfe_u32 v230, v255, 4, 1
v_bfe_u32 v231, v255, 6, 2
v_lshrrev_b32 v232, 8, v255
v_mov_b32 v44, v230
v_mov_b32 v209, 1048576
v_lshl_add_u32 v209, v231, 7, v209
v_lshl_add_u32 v209, v232, 5, v209
s_lshr_b32 s96, s96, 1
v_add_u32 v209, s96, v209
)ASM" R"ASM(v_bfe_u32 v233, v255, 4, 2
v_mul_u32_u24 v210, 640, v233
v_lshl_add_u32 v210, v231, 3, v210
s_mul_i32 s96, s93, 2560
s_lshl_b32 s92, s92, 5
s_add_u32 s96, s96, s92
v_add_u32 v210, s96, v210
v_cmp_eq_u32 s[98:99], v231, v232
v_lshrrev_b32 v211, 1, v255
s_lshl_b32 s96, s3, 11
s_add_u32 s96, s96, 16384
v_add_u32 v211, s96, v211
v_cndmask_b32 v210, v211, v210, s[98:99]
v_mul_u32_u24 v211, 5, v255
s_lshl_b32 s96, s3, 7
v_lshrrev_b32 v212, 3, v255
v_and_b32 v212, 62, v212
v_add_u32 v212, s96, v212
v_lshlrev_b32 v234, 6, v230
v_lshl_add_u32 v234, v232, 4, v234
v_add_u32 v234, s96, v234
v_mov_b32 v235, 1024
v_cmp_eq_u32 vcc, 0, v231
s_nop 1
v_cndmask_b32 v216, v235, v234, vcc
v_cmp_eq_u32 vcc, 1, v231
s_nop 1
v_cndmask_b32 v217, v235, v234, vcc
v_cmp_eq_u32 vcc, 2, v231
s_nop 1
v_cndmask_b32 v218, v235, v234, vcc
v_cmp_eq_u32 vcc, 3, v231
s_nop 1
v_cndmask_b32 v219, v235, v234, vcc
v_mov_b32 v220, 0
v_mov_b32 v221, 0
v_mov_b32 v222, 0
v_mov_b32 v223, 0
ds_write_b128 v235, v[220:223] offset:10240
s_lshl_b32 s92, s100, 13
s_add_u32 s80, s92, 32768
s_add_u32 s20, s80, 4096
; ---- top two rows of the sequence; SwiGLU slice of 32 hidden units
.La5_LT_SEL:
.La5_LW4_1:
s_cbranch_execz .La5_LW4_R0
s_and_b32 s97, s2, 7
s_lshl_b32 s97, s97, 12
s_cmp_gt_u32 s3, 0
s_cbranch_scc1 .La5_LT_S2
s_mov_b32 s96, 0
v_and_b32 v229, 255, v255
.La5_LT_SA:
s_sleep 2
ld v[224:227],v229,s[44:47],s97 offen offset:0 sc1
s_add_u32 s96, s96, 1
s_lshr_b32 s92, s96, 20
wt vmcnt(0)
v_cmp_ne_u32 vcc, 0, v224
s_nop 0
s_cbranch_scc1 .La5_LT_S1
s_or_b32 vcc_lo, vcc_lo, 0
s_cmp_eq_u32 vcc_lo, -1
s_cbranch_scc1 .La5_LT_S1
s_cmp_lg_u32 s96, 1
s_cbranch_scc1 .La5_LT_SA
s_lshr_b32 s92, s2, 6
s_lshl_b32 s92, s92, 7
s_and_b32 s93, s2, 63
s_add_u32 s92, s92, s93
s_lshl_b32 s92, s92, 7
s_add_u32 s92, s92, 950272
s_add_u32 s93, s92, 8192
wt vmcnt(0)
s_memtime s[16:17]
wt lgkmcnt(0)
buffer_store_dword v255, off, s[44:47], s92 sc1
wt vmcnt(0)
s_memtime s[18:19]
wt lgkmcnt(0)
buffer_store_dword v255, off, s[44:47], s93 sc1
wt vmcnt(0)
s_memtime s[24:25]
wt lgkmcnt(0)
s_add_u32 s16, s16, s24
s_lshl_b32 s18, s18, 1
s_sub_u32 s16, s16, s18
s_lshr_b32 s94, s16, 31
s_lshl_b32 s94, s94, 13
s_and_b32 s97, s2, 1
s_bfe_u32 s92, s2, 0x10001
s_lshl2_add_u32 s97, s92, s97
s_lshl_b32 s97, s97, 12
s_add_u32 s97, s97, s94
s_branch .La5_LT_SA
.La5_LT_S1:
s_cmp_lg_u32 s30, 0
s_cbranch_scc1 .La5_LT_S0
s_sleep 2
.La5_LT_S0:
)ASM" R"ASM(ld v[224:227],v255,s[44:47],s97 offen offset:0 sc1
ld v[232:235],v255,s[44:47],s97 offen offset:1024 sc1
ld v[244:247],v255,s[44:47],s97 offen offset:2048 sc1
ld v[248:251],v255,s[44:47],s97 offen offset:3072 sc1
s_add_u32 s96, s96, 1
s_lshr_b32 s92, s96, 20
wt vmcnt(3)
v_min3_u32 v228, v224, v225, v226
v_min_u32 v228, v227, v228
wt vmcnt(2)
v_min3_u32 v228, v232, v233, v228
v_min3_u32 v228, v234, v235, v228
wt vmcnt(1)
v_min3_u32 v228, v244, v245, v228
v_min3_u32 v228, v246, v247, v228
wt vmcnt(0)
v_min3_u32 v228, v248, v249, v228
v_min3_u32 v228, v250, v251, v228
v_cmp_ne_u32 vcc, 0, v228
v_max_u32 v229, v224, v232
v_max_u32 v230, v244, v248
s_nop 0
s_nop 0
s_cbranch_scc1 .La5_LT_S3
s_cmp_eq_u64 vcc, -1
s_cbranch_scc0 .La5_LT_S1
.La5_LT_S3:
v_max_u32 v228, v229, v230
v_min_u32 v229, v229, v230
v_mov_b32 v231, v228
v_min_u32 v224, v224, v232
v_min_u32 v244, v244, v248
v_max3_u32 v229, v229, v224, v244
v_max3_u32 v230, v226, v234, v246
v_max3_u32 v229, v229, v230, v250
s_nop 1
v_max_u32_dpp v231, v231, v231 row_shr:1 row_mask:0xf bank_mask:0xf
s_nop 1
v_max_u32_dpp v231, v231, v231 row_shr:2 row_mask:0xf bank_mask:0xf
s_nop 1
v_max_u32_dpp v231, v231, v231 row_shr:4 row_mask:0xf bank_mask:0xf
s_nop 1
v_max_u32_dpp v231, v231, v231 row_shr:8 row_mask:0xf bank_mask:0xf
s_nop 1
v_max_u32_dpp v231, v231, v231 row_bcast:15 row_mask:0xa bank_mask:0xf
s_nop 1
v_max_u32_dpp v231, v231, v231 row_bcast:31 row_mask:0xc bank_mask:0xf
s_nop 0
v_readlane_b32 s82, v231, 63
s_nop 1
v_cmp_eq_u32 vcc, s82, v228
v_mov_b32 v230, s82
v_mov_b32 v224, 0
v_cndmask_b32 v228, v228, v229, vcc
.La5_LW4_2:
s_cbranch_execz .La5_LW4_R0
s_nop 1
v_max_u32_dpp v228, v228, v228 row_shr:1 row_mask:0xf bank_mask:0xf
s_nop 1
v_max_u32_dpp v228, v228, v228 row_shr:2 row_mask:0xf bank_mask:0xf
s_nop 1
v_max_u32_dpp v228, v228, v228 row_shr:4 row_mask:0xf bank_mask:0xf
s_nop 1
v_max_u32_dpp v228, v228, v228 row_shr:8 row_mask:0xf bank_mask:0xf
s_nop 1
v_max_u32_dpp v228, v228, v228 row_bcast:15 row_mask:0xa bank_mask:0xf
s_nop 1
v_max_u32_dpp v228, v228, v228 row_bcast:31 row_mask:0xc bank_mask:0xf
s_nop 0
v_readlane_b32 s81, v228, 63
s_nop 1
v_mov_b32 v231, s81
ds_write_b64 v224, v[230:231] offset:64
.La5_LT_S2:
v_mov_b32 v224, 0
wt lgkmcnt(0)
s_barrier
ds_read_b64 v[228:229], v224 offset:64
wt lgkmcnt(0)
v_readfirstlane_b32 s82, v228
v_readfirstlane_b32 s81, v229
s_nop 0
.La5_LT_S4:
s_andn2_b32 s84, 0x7ff, s82
s_andn2_b32 s21, 0x7ff, s81
s_cmp_eq_u32 s30, 0
s_cbranch_scc1 .La5_LT_ST
s_lshr_b32 s92, s84, 3
s_lshr_b32 s93, s21, 3
s_cmp_eq_u32 s92, s93
s_cselect_b32 s96, 2048, 0
s_lshl_b32 s92, s92, 12
s_lshl_b32 s93, s93, 12
s_add_u32 s93, s93, s96
s_sub_u32 s93, s93, s92
v_mul_lo_u32 v44, v44, s93
s_mov_b32 s96, 0
v_add_u32 v209, v209, v44
ld v[224:227],v209,s[44:47],s92 offen offset:0 sc1
ld v[232:235],v209,s[44:47],s92 offen offset:16 sc1
)ASM" R"ASM(ld v[244:247],v209,s[44:47],s92 offen offset:1024 sc1
ld v[248:251],v209,s[44:47],s92 offen offset:1040 sc1
s_lshl_b32 s86, s76, 5
s_add_u32 s86, s86, s90
s_lshl2_add_u32 s86, s86, s3
s_lshl_b32 s87, s86, 1
v_bfe_u32 v40, v255, 6, 1
v_add_u32 v41, s87, v40
v_cmp_eq_u32 vcc, s84, v41
v_cmp_eq_u32 s[94:95], s21, v41
v_lshrrev_b32 v42, 7, v255
v_and_b32 v43, 48, v255
v_lshl_add_u32 v42, v42, 6, v43
s_or_b64 vcc, vcc, s[94:95]
v_lshl_add_u32 v42, v40, 11, v42
v_mov_b32_e64 v43, 1073741824
s_lshl_b32 s86, s86, 12
v_cndmask_b32 v42, v42, v43, vcc
.La5_LT_H0:
wt vmcnt(3)
v_max3_u32 v45, v224, v225, v226
v_max_u32 v45, v227, v45
wt vmcnt(2)
v_max3_u32 v45, v232, v233, v45
v_max3_u32 v45, v234, v235, v45
wt vmcnt(1)
v_max3_u32 v45, v244, v245, v45
v_max3_u32 v45, v246, v247, v45
wt vmcnt(0)
v_max3_u32 v45, v248, v249, v45
v_max3_u32 v45, v250, v251, v45
v_cmp_eq_u32 vcc, -1, v45
s_nop 1
s_cbranch_vccnz .La5_LT_H1
.La5_LT_H2:
mm a[0:3],v[224:227],a[96:99],0
mm a[0:3],v[232:235],a[128:131],a[0:3]
mm a[0:3],v[244:247],a[160:163],a[0:3]
st v[192:195],v42,s[56:59],s86 offen offset:0 sc1
mm a[0:3],v[248:251],a[192:195],a[0:3]
mm a[4:7],v[224:227],a[100:103],0
mm a[4:7],v[232:235],a[132:135],a[4:7]
ds_write_b64 v210, a[0:1] offset:5120
mm a[4:7],v[244:247],a[164:167],a[4:7]
st v[196:199],v42,s[56:59],s86 offen offset:512 sc1
mm a[4:7],v[248:251],a[196:199],a[4:7]
mm a[8:11],v[224:227],a[104:107],0
mm a[8:11],v[232:235],a[136:139],a[8:11]
ds_write_b64 v210, a[4:5] offset:5200
mm a[8:11],v[244:247],a[168:171],a[8:11]
st v[200:203],v42,s[56:59],s86 offen offset:1024 sc1
mm a[8:11],v[248:251],a[200:203],a[8:11]
mm a[12:15],v[224:227],a[108:111],0
mm a[12:15],v[232:235],a[140:143],a[12:15]
ds_write_b64 v210, a[8:9] offset:5280
mm a[12:15],v[244:247],a[172:175],a[12:15]
st v[204:207],v42,s[56:59],s86 offen offset:1536 sc1
mm a[12:15],v[248:251],a[204:207],a[12:15]
mm a[16:19],v[224:227],a[112:115],0
mm a[16:19],v[232:235],a[144:147],a[16:19]
ds_write_b64 v210, a[12:13] offset:5360
mm a[16:19],v[244:247],a[176:179],a[16:19]
mm a[16:19],v[248:251],a[208:211],a[16:19]
mm a[20:23],v[224:227],a[116:119],0
mm a[20:23],v[232:235],a[148:151],a[20:23]
ds_write_b64 v210, a[16:17] offset:5440
mm a[20:23],v[244:247],a[180:183],a[20:23]
mm a[20:23],v[248:251],a[212:215],a[20:23]
mm a[24:27],v[224:227],a[120:123],0
mm a[24:27],v[232:235],a[152:155],a[24:27]
ds_write_b64 v210, a[20:21] offset:5520
mm a[24:27],v[244:247],a[184:187],a[24:27]
mm a[24:27],v[248:251],a[216:219],a[24:27]
mm a[28:31],v[224:227],a[124:127],0
mm a[28:31],v[232:235],a[156:159],a[28:31]
ds_write_b64 v210, a[24:25] offset:5600
mm a[28:31],v[244:247],a[188:191],a[28:31]
mm a[28:31],v[248:251],a[220:223],a[28:31]
s_nop 7
ds_write_b64 v210, a[28:29] offset:5680
wt lgkmcnt(0)
s_barrier
rd v[224:227],v211 offset:5120
rd v[232:235],v211 offset:5136
rd v[244:247],v211 offset:5152
rd v[248:251],v211 offset:5168
wt lgkmcnt(2)
v_pk_add_f32 v[224:225], v[224:225], v[226:227]
v_pk_add_f32 v[232:233], v[232:233], v[234:235]
wt lgkmcnt(0)
v_pk_add_f32 v[244:245], v[244:245], v[246:247]
v_pk_add_f32 v[248:249], v[248:249], v[250:251]
v_pk_add_f32 v[224:225], v[224:225], v[232:233]
v_pk_add_f32 v[244:245], v[244:245], v[248:249]
v_pk_add_f32 v[224:225], v[224:225], v[244:245]
cv v224,v224,v225
v_mov_b32 v226, v224
s_nop 1
v_permlane32_swap_b32 v224, v226
s_nop 1
)ASM" R"ASM(v_lshlrev_b32 v228, 16, v224
v_and_b32 v229, 0xffff0000, v224
v_lshlrev_b32 v232, 16, v226
v_and_b32 v233, 0xffff0000, v226
v_mul_f32 v230, 0xbfb8aa3b, v228
v_mul_f32 v231, 0xbfb8aa3b, v229
ex v230,v230
ex v231,v231
v_add_f32 v230, 1.0, v230
v_add_f32 v231, 1.0, v231
v_rcp_f32 v230, v230
v_rcp_f32 v231, v231
s_nop 0
v_pk_mul_f32 v[228:229], v[228:229], v[230:231]
cv v228,v228,v229
v_lshlrev_b32 v230, 16, v228
v_and_b32 v231, 0xffff0000, v228
v_pk_mul_f32 v[230:231], v[230:231], v[232:233]
cv v228,v230,v231
ds_write_b16 v212, v228 offset:10240
ds_write_b16_d16_hi v212, v228 offset:10304
wt lgkmcnt(0)
rd v[224:227],v216 offset:10240
rd v[232:235],v217 offset:10240
rd v[244:247],v218 offset:10240
rd v[248:251],v219 offset:10240
wt lgkmcnt(3)
mm v[0:3],v[224:227],v[128:131],0
mm v[4:7],v[224:227],v[132:135],0
mm v[8:11],v[224:227],v[136:139],0
mm v[12:15],v[224:227],v[140:143],0
wt lgkmcnt(2)
mm v[0:3],v[232:235],v[144:147],v[0:3]
mm v[4:7],v[232:235],v[148:151],v[4:7]
mm v[8:11],v[232:235],v[152:155],v[8:11]
mm v[12:15],v[232:235],v[156:159],v[12:15]
wt lgkmcnt(1)
mm v[0:3],v[244:247],v[160:163],v[0:3]
mm v[4:7],v[244:247],v[164:167],v[4:7]
mm v[8:11],v[244:247],v[168:171],v[8:11]
mm v[12:15],v[244:247],v[172:175],v[12:15]
wt lgkmcnt(0)
mm v[0:3],v[248:251],v[176:179],v[0:3]
mm v[4:7],v[248:251],v[180:183],v[4:7]
mm v[8:11],v[248:251],v[184:187],v[8:11]
mm v[12:15],v[248:251],v[188:191],v[12:15]
s_nop 7
v_or_b32 v16, 1, v0
v_or_b32 v20, 1, v1
v_or_b32 v17, 1, v4
v_or_b32 v21, 1, v5
v_or_b32 v18, 1, v8
v_or_b32 v22, 1, v9
v_or_b32 v19, 1, v12
v_or_b32 v23, 1, v13
st v[16:19],v208,s[44:47],s80 offen sc1
st v[20:23],v208,s[44:47],s20 offen sc1
s_branch .La5_LT_END
; ---- rows that were not routed leave; final sums of the routed rows
.La5_LT_ST:
.La5_LW4_3:
s_cbranch_execz .La5_LW4_R0
s_sleep 16
s_lshl_b32 s92, s76, 5
s_add_u32 s92, s92, s90
s_lshl2_add_u32 s92, s92, s3
s_lshl_b32 s93, s92, 1
v_bfe_u32 v230, v255, 6, 1
v_add_u32 v231, s93, v230
v_cmp_ne_u32 vcc, s84, v231
v_cmp_ne_u32 s[94:95], s21, v231
v_lshrrev_b32 v232, 7, v255
v_and_b32 v233, 48, v255
v_lshl_add_u32 v232, v232, 6, v233
v_lshl_add_u32 v232, v230, 11, v232
s_lshl_b32 s92, s92, 12
s_and_b64 exec, vcc, s[94:95]
s_cbranch_scc0 .La5_LT_F0
st v[192:195],v232,s[56:59],s92 offen offset:0 sc1
st v[196:199],v232,s[56:59],s92 offen offset:512 sc1
st v[200:203],v232,s[56:59],s92 offen offset:1024 sc1
st v[204:207],v232,s[56:59],s92 offen offset:1536 sc1
.La5_LT_F0:
s_mov_b64 exec, -1
s_cmp_eq_u32 s75, 1
s_cbranch_scc1 .La5_LT_END
s_lshr_b32 s92, s2, 6
s_lshl2_add_u32 s92, s92, s74
s_and_b32 s93, s2, 7
s_lshl3_add_u32 s91, s92, s93
s_cmp_lt_u32 s91, 64
s_cselect_b32 s84, s84, s21
s_cselect_b32 s82, s82, s81
s_bfe_u32 s92, s91, 0x30003
s_and_b32 s93, s91, 3
s_lshl2_add_u32 s96, s92, s93
s_and_b32 s85, s91, 4
s_lshl_b32 s85, s85, 1
v_bfe_u32 v0, v255, 6, 2
v_bfe_u32 v1, v255, 4, 2
)ASM" R"ASM(v_lshlrev_b32 v1, 1, v1
v_lshl_add_u32 v0, v0, 4, v1
v_add_u32 v0, s85, v0
s_lshl_b32 s85, s96, 10
v_add_u32 v1, s85, v0
s_lshl_b32 s85, s96, 6
v_add_u32 v0, s85, v0
s_lshr_b32 s92, s84, 4
s_lshl_b32 s92, s92, 15
s_and_b32 s93, s84, 15
s_lshl_b32 s93, s93, 6
s_add_u32 s92, s92, s93
s_add_u32 s93, s92, 4194304
s_add_u32 s94, s92, 8388608
s_add_u32 s95, s92, 12582912
buffer_load_ushort v4, v1, s[32:35], s92 offen sc1
buffer_load_ushort v5, v1, s[32:35], s93 offen sc1
buffer_load_ushort v6, v1, s[32:35], s94 offen sc1
buffer_load_ushort v7, v1, s[32:35], s95 offen sc1
s_lshl_b32 s21, s84, 11
s_and_b32 s92, s84, 0xf8
s_lshl4_add_u32 s97, s92, s97
s_lshr_b32 s92, s84, 8
s_lshl4_add_u32 s97, s92, s97
buffer_load_ushort v8, v0, s[68:71], s21 offen
ld v[12:15],off,s[44:47],s97 sc1
v_lshrrev_b32 v2, 6, v255
v_lshlrev_b32 v2, 13, v2
v_and_b32 v3, 48, v255
v_add_u32 v2, v2, v3
s_lshl_b32 s92, s3, 17
s_lshl_b32 s93, s91, 6
s_add_u32 s92, s92, s93
s_add_u32 s94, s92, 32768
s_cmp_eq_u32 s3, 3
s_cselect_b32 s95, 0, 524288
s_cselect_b32 s96, 0, -1
s_add_u32 s95, s95, s94
s_ashr_i32 s92, s82, 31
s_orn2_b32 s92, 0x80000000, s92
s_xor_b32 s92, s92, s82
s_and_b32 s92, s92, 0xffff0000
v_mov_b32 v9, 0xbfb8aa3b
v_mul_f32 v9, s92, v9
ex v9,v9
v_lshrrev_b32 v3, 8, v255
s_lshl_b32 s92, s3, 4
v_add_f32 v9, 1.0, v9
v_lshl_add_u32 v3, v3, 2, s92
v_rcp_f32 v9, v9
v_and_b32 v10, 48, v255
v_mul_u32_u24 v10, 20, v10
v_add_u32 v3, v3, v10
cv v9,v9,v9
v_lshlrev_b32 v9, 16, v9
v_and_b32 v10, 0xf0, v255
v_mul_u32_u24 v10, 5, v10
wt vmcnt(0)
v_cmp_eq_u32 vcc, s82, v12
s_nop 1
v_cndmask_b32 v12, v15, v13, vcc
v_lshlrev_b32 v14, 16, v12
v_rcp_f32 v14, v14
v_and_b32 v12, 0xffff0000, v12
v_mul_f32 v12, v12, v14
v_mov_b32 v15, v14
v_xor_b32 v12, 0x80000000, v12
v_mov_b32 v13, v12
v_lshlrev_b32 v4, 16, v4
v_lshlrev_b32 v5, 16, v5
v_lshlrev_b32 v6, 16, v6
v_lshlrev_b32 v7, 16, v7
v_lshlrev_b32 v8, 16, v8
v_add_f32 v4, v4, v5
v_add_f32 v6, v6, v7
v_add_f32 v4, v4, v6
v_add_f32 v4, v4, v8
cv v4,v4,v4
v_lshlrev_b32 v4, 16, v4
v_fma_f32 v5, v4, v14, v12
cv v5,v5,v5
v_lshlrev_b32 v5, 16, v5
s_sleep 12
s_mov_b32 s93, 0
.La5_LT_Y:
ld v[16:19],v2,s[44:47],s94 offen sc1
ld v[20:23],v2,s[44:47],s95 offen sc1
s_add_u32 s93, s93, 1
s_lshr_b32 s92, s93, 20
wt vmcnt(0)
v_min3_u32 v24, v16, v17, v18
v_min3_u32 v25, v20, v21, v22
v_min3_u32 v24, v24, v19, v23
v_min_u32 v24, v24, v25
v_cmp_ne_u32 vcc, 0, v24
v_and_b32 v20, s96, v20
v_and_b32 v21, s96, v21
v_and_b32 v22, s96, v22
v_and_b32 v23, s96, v23
s_cbranch_scc1 .La5_LT_Y1
)ASM" R"ASM(s_cmp_eq_u64 vcc, -1
s_nop 0
s_cbranch_scc0 .La5_LT_Y
.La5_LT_Y1:
v_pk_add_f32 v[16:17], v[16:17], v[20:21]
v_pk_add_f32 v[18:19], v[18:19], v[22:23]
s_nop 0
v_add_f32_dpp v16, v16, v16 row_ror:4 row_mask:0xf bank_mask:0xf
v_add_f32_dpp v17, v17, v17 row_ror:4 row_mask:0xf bank_mask:0xf
v_add_f32_dpp v18, v18, v18 row_ror:4 row_mask:0xf bank_mask:0xf
v_add_f32_dpp v19, v19, v19 row_ror:4 row_mask:0xf bank_mask:0xf
v_add_f32_dpp v16, v16, v16 row_ror:8 row_mask:0xf bank_mask:0xf
v_add_f32_dpp v17, v17, v17 row_ror:8 row_mask:0xf bank_mask:0xf
v_add_f32_dpp v18, v18, v18 row_ror:8 row_mask:0xf bank_mask:0xf
v_add_f32_dpp v19, v19, v19 row_ror:8 row_mask:0xf bank_mask:0xf
ds_write_b32 v3, v16 offset:5120
ds_write_b32 v3, v17 offset:5200
ds_write_b32 v3, v18 offset:5280
ds_write_b32 v3, v19 offset:5360
wt lgkmcnt(0)
s_barrier
rd v[16:19],v10 offset:5120
rd v[20:23],v10 offset:5136
rd v[24:27],v10 offset:5152
rd v[28:31],v10 offset:5168
wt lgkmcnt(2)
v_pk_add_f32 v[16:17], v[16:17], v[18:19]
v_pk_add_f32 v[20:21], v[20:21], v[22:23]
wt lgkmcnt(0)
v_pk_add_f32 v[24:25], v[24:25], v[26:27]
v_pk_add_f32 v[28:29], v[28:29], v[30:31]
v_pk_add_f32 v[16:17], v[16:17], v[20:21]
v_pk_add_f32 v[24:25], v[24:25], v[28:29]
v_pk_add_f32 v[16:17], v[16:17], v[24:25]
v_add_f32 v16, v16, v17
cv v16,v16,v16
v_lshlrev_b32 v16, 16, v16
v_mul_f32 v16, v16, v9
cv v16,v16,v16
v_lshlrev_b32 v16, 16, v16
v_add_f32 v16, v16, v5
cv v16,v16,v16
v_lshlrev_b32 v16, 16, v16
v_add_f32 v16, v16, v4
cv v16,v16,v16
s_cmp_lg_u32 s3, 0
s_cbranch_scc1 .La5_LT_END
s_mov_b64 exec, 0xffff
buffer_store_short v16, v0, s[56:59], s21 offen
s_mov_b64 exec, -1
.La5_LT_END:
s_nop 0
s_nop 0
.La5_LW4_4:
s_cbranch_execz .La5_LW4_R0
s_lshl2_add_u32 s92, s2, s3
s_cmp_lg_u32 s92, 992
s_cbranch_scc1 .La5_LT_E1
buffer_store_dwordx2 v[252:253], off, s[64:67], 0 sc1
.La5_LT_E1:
s_branch .La5_LEND
; ---- fallback: the earlier 32 rows against all their keys with the true running maximum (online softmax)
.La5_LA_SLOW0:
wt lgkmcnt(0)
s_nop 7
s_nop 7
aw a160,v192
aw a161,v193
aw a162,v194
aw a163,v195
aw a164,v196
aw a165,v197
aw a166,v198
aw a167,v199
aw a168,v200
aw a169,v201
aw a170,v202
aw a171,v203
aw a172,v204
aw a173,v205
aw a174,v206
aw a175,v207
aw a176,v208
aw a177,v209
aw a178,v210
aw a179,v211
aw a180,v212
aw a181,v213
aw a182,v214
aw a183,v215
aw a184,v216
aw a185,v217
aw a186,v218
aw a187,v219
aw a188,v220
aw a189,v221
aw a190,v222
aw a191,v223
s_lshl_b32 s96, s72, 19
s_add_u32 s96, s96, 20971520
)ASM" R"ASM(s_add_u32 s92, s26, s96
s_addc_u32 s93, s27, 0
s_and_b32 s93, s93, 0xffff
s_mov_b32 s94, 17301504
s_mov_b32 s95, 0x20000
v_mov_b32 v0, 0
v_mov_b32 v1, 0
v_mov_b32 v2, 0
v_mov_b32 v3, 0
v_mov_b32 v4, 0
v_mov_b32 v5, 0
v_mov_b32 v6, 0
v_mov_b32 v7, 0
v_mov_b32 v8, 0
v_mov_b32 v9, 0
v_mov_b32 v10, 0
v_mov_b32 v11, 0
v_mov_b32 v12, 0
v_mov_b32 v13, 0
v_mov_b32 v14, 0
v_mov_b32 v15, 0
v_mov_b32 v16, 0
v_mov_b32 v17, 0
v_mov_b32 v18, 0
v_mov_b32 v19, 0
v_mov_b32 v20, 0
v_mov_b32 v21, 0
v_mov_b32 v22, 0
v_mov_b32 v23, 0
v_mov_b32 v24, 0
v_mov_b32 v25, 0
v_mov_b32 v26, 0
v_mov_b32 v27, 0
v_mov_b32 v28, 0
v_mov_b32 v29, 0
v_mov_b32 v30, 0
v_mov_b32 v31, 0
v_mov_b32 v32, 0
v_mov_b32 v33, 0
v_mov_b32 v34, 0
v_mov_b32 v35, 0
v_mov_b32 v36, 0
v_mov_b32 v37, 0
v_mov_b32 v38, 0
v_mov_b32 v39, 0
v_mov_b32 v40, 0
v_mov_b32 v41, 0
v_mov_b32 v42, 0
v_mov_b32 v43, 0
v_mov_b32 v44, 0
v_mov_b32 v45, 0
v_mov_b32 v46, 0
v_mov_b32 v47, 0
v_mov_b32 v48, 0
v_mov_b32 v49, 0
v_mov_b32 v50, 0
v_mov_b32 v51, 0
v_mov_b32 v52, 0
v_mov_b32 v53, 0
v_mov_b32 v54, 0
v_mov_b32 v55, 0
v_mov_b32 v56, 0
v_mov_b32 v57, 0
v_mov_b32 v58, 0
v_mov_b32 v59, 0
v_mov_b32 v60, 0
v_mov_b32 v61, 0
v_mov_b32 v62, 0
v_mov_b32 v63, 0
v_mov_b32 v216, 0xff61b1e6
v_mov_b32 v217, 0xff61b1e6
v_mov_b32 v218, 0
v_mov_b32 v219, 0
s_mov_b32 s7, 0
.La5_LA_SLOW0_L:
s_lshl_b32 s96, s7, 13
s_add_u32 s97, s96, 4096
ld a[128:131],v255,s[92:95],s96 offen offset:0 sc1
ld a[132:135],v255,s[92:95],s96 offen offset:1024 sc1
ld a[136:139],v255,s[92:95],s96 offen offset:2048 sc1
ld a[140:143],v255,s[92:95],s96 offen offset:3072 sc1
ld a[144:147],v255,s[92:95],s97 offen offset:0 sc1
ld a[148:151],v255,s[92:95],s97 offen offset:1024 sc1
ld a[152:155],v255,s[92:95],s97 offen offset:2048 sc1
ld a[156:159],v255,s[92:95],s97 offen offset:3072 sc1
s_lshl_b32 s96, s76, 3
s_sub_u32 s96, s96, s7
s_add_u32 s96, s96, s3
s_lshl_b32 s96, s96, 5
v_bfe_u32 v220, v255, 4, 4
v_lshrrev_b32 v221, 8, v255
v_lshlrev_b32 v221, 2, v221
v_sub_u32 v220, v220, v221
v_add_u32 v220, s96, v220
v_mov_b32 v222, 0xceabfb8f
wt vmcnt(0)
mm v[192:195],a[128:131],v[128:131],0
mm v[192:195],a[132:135],v[132:135],v[192:195]
mm v[192:195],a[136:139],v[136:139],v[192:195]
mm v[192:195],a[140:143],v[140:143],v[192:195]
)ASM" R"ASM(mm v[196:199],a[144:147],v[128:131],0
mm v[196:199],a[148:151],v[132:135],v[196:199]
mm v[196:199],a[152:155],v[136:139],v[196:199]
mm v[196:199],a[156:159],v[140:143],v[196:199]
mm v[200:203],a[128:131],v[144:147],0
mm v[200:203],a[132:135],v[148:151],v[200:203]
mm v[200:203],a[136:139],v[152:155],v[200:203]
mm v[200:203],a[140:143],v[156:159],v[200:203]
mm v[204:207],a[144:147],v[144:147],0
mm v[204:207],a[148:151],v[148:151],v[204:207]
mm v[204:207],a[152:155],v[152:155],v[204:207]
mm v[204:207],a[156:159],v[156:159],v[204:207]
s_nop 7
s_nop 7
s_lshl_b32 s96, s7, 13
s_add_u32 s96, s96, 16777216
s_add_u32 s97, s96, 4096
ld a[128:131],v255,s[92:95],s96 offen offset:0 sc1
ld a[132:135],v255,s[92:95],s96 offen offset:1024 sc1
ld a[136:139],v255,s[92:95],s96 offen offset:2048 sc1
ld a[140:143],v255,s[92:95],s96 offen offset:3072 sc1
ld a[144:147],v255,s[92:95],s97 offen offset:0 sc1
ld a[148:151],v255,s[92:95],s97 offen offset:1024 sc1
ld a[152:155],v255,s[92:95],s97 offen offset:2048 sc1
ld a[156:159],v255,s[92:95],s97 offen offset:3072 sc1
v_cmp_gt_i32 vcc, 0, v220
s_nop 1
v_cndmask_b32 v192, v192, v222, vcc
v_cmp_gt_i32 vcc, 1, v220
s_nop 1
v_cndmask_b32 v193, v193, v222, vcc
v_cmp_gt_i32 vcc, 2, v220
s_nop 1
v_cndmask_b32 v194, v194, v222, vcc
v_cmp_gt_i32 vcc, 3, v220
s_nop 1
v_cndmask_b32 v195, v195, v222, vcc
v_cmp_gt_i32 vcc, 16, v220
s_nop 1
v_cndmask_b32 v196, v196, v222, vcc
v_cmp_gt_i32 vcc, 17, v220
s_nop 1
v_cndmask_b32 v197, v197, v222, vcc
v_cmp_gt_i32 vcc, 18, v220
s_nop 1
v_cndmask_b32 v198, v198, v222, vcc
v_cmp_gt_i32 vcc, 19, v220
s_nop 1
v_cndmask_b32 v199, v199, v222, vcc
v_cmp_gt_i32 vcc, -16, v220
s_nop 1
v_cndmask_b32 v200, v200, v222, vcc
v_cmp_gt_i32 vcc, -15, v220
s_nop 1
v_cndmask_b32 v201, v201, v222, vcc
v_cmp_gt_i32 vcc, -14, v220
s_nop 1
v_cndmask_b32 v202, v202, v222, vcc
v_cmp_gt_i32 vcc, -13, v220
s_nop 1
v_cndmask_b32 v203, v203, v222, vcc
v_cmp_gt_i32 vcc, 0, v220
s_nop 1
v_cndmask_b32 v204, v204, v222, vcc
v_cmp_gt_i32 vcc, 1, v220
s_nop 1
v_cndmask_b32 v205, v205, v222, vcc
v_cmp_gt_i32 vcc, 2, v220
s_nop 1
v_cndmask_b32 v206, v206, v222, vcc
v_cmp_gt_i32 vcc, 3, v220
s_nop 1
v_cndmask_b32 v207, v207, v222, vcc
v_max3_f32 v222, v192, v193, v216
v_max3_f32 v222, v194, v195, v222
v_max3_f32 v222, v196, v197, v222
v_max3_f32 v222, v198, v199, v222
v_mov_b32 v221, v222
s_nop 1
v_permlane16_swap_b32 v221, v222
s_nop 1
v_max_f32 v222, v222, v221
v_mov_b32 v221, v222
s_nop 1
v_permlane32_swap_b32 v221, v222
s_nop 1
v_max_f32 v222, v222, v221
v_sub_f32 v223, v216, v222
v_mov_b32 v216, v222
ex v223,v223
v_sub_f32 v192, v192, v222
v_sub_f32 v193, v193, v222
v_sub_f32 v194, v194, v222
v_sub_f32 v195, v195, v222
v_sub_f32 v196, v196, v222
v_sub_f32 v197, v197, v222
v_sub_f32 v198, v198, v222
v_sub_f32 v199, v199, v222
ex v192,v192
ex v193,v193
)ASM" R"ASM(ex v194,v194
ex v195,v195
ex v196,v196
ex v197,v197
ex v198,v198
ex v199,v199
v_mul_f32 v218, v218, v223
v_add_f32 v218, v218, v192
v_add_f32 v218, v218, v193
v_add_f32 v218, v218, v194
v_add_f32 v218, v218, v195
v_add_f32 v218, v218, v196
v_add_f32 v218, v218, v197
v_add_f32 v218, v218, v198
v_add_f32 v218, v218, v199
cv v208,v192,v193
cv v209,v194,v195
cv v210,v196,v197
cv v211,v198,v199
v_mul_f32 v0, v0, v223
v_mul_f32 v1, v1, v223
v_mul_f32 v2, v2, v223
v_mul_f32 v3, v3, v223
v_mul_f32 v4, v4, v223
v_mul_f32 v5, v5, v223
v_mul_f32 v6, v6, v223
v_mul_f32 v7, v7, v223
v_mul_f32 v8, v8, v223
v_mul_f32 v9, v9, v223
v_mul_f32 v10, v10, v223
v_mul_f32 v11, v11, v223
v_mul_f32 v12, v12, v223
v_mul_f32 v13, v13, v223
v_mul_f32 v14, v14, v223
v_mul_f32 v15, v15, v223
v_mul_f32 v16, v16, v223
v_mul_f32 v17, v17, v223
v_mul_f32 v18, v18, v223
v_mul_f32 v19, v19, v223
v_mul_f32 v20, v20, v223
v_mul_f32 v21, v21, v223
v_mul_f32 v22, v22, v223
v_mul_f32 v23, v23, v223
v_mul_f32 v24, v24, v223
v_mul_f32 v25, v25, v223
v_mul_f32 v26, v26, v223
v_mul_f32 v27, v27, v223
v_mul_f32 v28, v28, v223
v_mul_f32 v29, v29, v223
v_mul_f32 v30, v30, v223
v_mul_f32 v31, v31, v223
v_max3_f32 v222, v200, v201, v217
v_max3_f32 v222, v202, v203, v222
v_max3_f32 v222, v204, v205, v222
v_max3_f32 v222, v206, v207, v222
v_mov_b32 v221, v222
s_nop 1
v_permlane16_swap_b32 v221, v222
s_nop 1
v_max_f32 v222, v222, v221
v_mov_b32 v221, v222
s_nop 1
v_permlane32_swap_b32 v221, v222
s_nop 1
v_max_f32 v222, v222, v221
v_sub_f32 v223, v217, v222
v_mov_b32 v217, v222
ex v223,v223
v_sub_f32 v200, v200, v222
v_sub_f32 v201, v201, v222
v_sub_f32 v202, v202, v222
v_sub_f32 v203, v203, v222
v_sub_f32 v204, v204, v222
v_sub_f32 v205, v205, v222
v_sub_f32 v206, v206, v222
v_sub_f32 v207, v207, v222
ex v200,v200
ex v201,v201
ex v202,v202
ex v203,v203
ex v204,v204
ex v205,v205
ex v206,v206
ex v207,v207
v_mul_f32 v219, v219, v223
v_add_f32 v219, v219, v200
v_add_f32 v219, v219, v201
v_add_f32 v219, v219, v202
v_add_f32 v219, v219, v203
v_add_f32 v219, v219, v204
v_add_f32 v219, v219, v205
v_add_f32 v219, v219, v206
v_add_f32 v219, v219, v207
cv v212,v200,v201
cv v213,v202,v203
cv v214,v204,v205
cv v215,v206,v207
v_mul_f32 v32, v32, v223
v_mul_f32 v33, v33, v223
v_mul_f32 v34, v34, v223
)ASM" R"ASM(v_mul_f32 v35, v35, v223
v_mul_f32 v36, v36, v223
v_mul_f32 v37, v37, v223
v_mul_f32 v38, v38, v223
v_mul_f32 v39, v39, v223
v_mul_f32 v40, v40, v223
v_mul_f32 v41, v41, v223
v_mul_f32 v42, v42, v223
v_mul_f32 v43, v43, v223
v_mul_f32 v44, v44, v223
v_mul_f32 v45, v45, v223
v_mul_f32 v46, v46, v223
v_mul_f32 v47, v47, v223
v_mul_f32 v48, v48, v223
v_mul_f32 v49, v49, v223
v_mul_f32 v50, v50, v223
v_mul_f32 v51, v51, v223
v_mul_f32 v52, v52, v223
v_mul_f32 v53, v53, v223
v_mul_f32 v54, v54, v223
v_mul_f32 v55, v55, v223
v_mul_f32 v56, v56, v223
v_mul_f32 v57, v57, v223
v_mul_f32 v58, v58, v223
v_mul_f32 v59, v59, v223
v_mul_f32 v60, v60, v223
v_mul_f32 v61, v61, v223
v_mul_f32_e64 v62, v62, v223
v_mul_f32_e64 v63, v63, v223
wt vmcnt(0)
mm v[0:3],a[128:131],v[208:211],v[0:3]
mm v[32:35],a[128:131],v[212:215],v[32:35]
mm v[4:7],a[132:135],v[208:211],v[4:7]
mm v[36:39],a[132:135],v[212:215],v[36:39]
mm v[8:11],a[136:139],v[208:211],v[8:11]
mm v[40:43],a[136:139],v[212:215],v[40:43]
mm v[12:15],a[140:143],v[208:211],v[12:15]
mm v[44:47],a[140:143],v[212:215],v[44:47]
mm v[16:19],a[144:147],v[208:211],v[16:19]
mm v[48:51],a[144:147],v[212:215],v[48:51]
mm v[20:23],a[148:151],v[208:211],v[20:23]
mm v[52:55],a[148:151],v[212:215],v[52:55]
mm v[24:27],a[152:155],v[208:211],v[24:27]
mm v[56:59],a[152:155],v[212:215],v[56:59]
mm v[28:31],a[156:159],v[208:211],v[28:31]
mm v[60:63],a[156:159],v[212:215],v[60:63]
s_nop 7
s_nop 7
s_add_u32 s7, s7, 1
s_lshr_b32 s96, s7, 3
s_cmp_le_u32 s96, s76
s_cbranch_scc1 .La5_LA_SLOW0_L
v_mov_b32 v221, v218
s_nop 1
v_permlane16_swap_b32 v221, v218
s_nop 1
v_add_f32 v218, v218, v221
v_mov_b32 v221, v218
s_nop 1
v_permlane32_swap_b32 v221, v218
s_nop 1
v_add_f32 v218, v218, v221
v_mov_b32 v221, v219
s_nop 1
v_permlane16_swap_b32 v221, v219
s_nop 1
v_add_f32 v219, v219, v221
v_mov_b32 v221, v219
s_nop 1
v_permlane32_swap_b32 v221, v219
s_nop 1
v_add_f32 v219, v219, v221
v_rcp_f32 v242, v218
v_rcp_f32 v243, v219
ar v192,a160
ar v193,a161
ar v194,a162
ar v195,a163
ar v196,a164
ar v197,a165
ar v198,a166
ar v199,a167
ar v200,a168
ar v201,a169
ar v202,a170
ar v203,a171
ar v204,a172
ar v205,a173
ar v206,a174
ar v207,a175
ar v208,a176
ar v209,a177
ar v210,a178
ar v211,a179
ar v212,a180
ar v213,a181
ar v214,a182
ar v215,a183
ar v216,a184
ar v217,a185
)ASM" R"ASM(ar v218,a186
ar v219,a187
ar v220,a188
ar v221,a189
ar v222,a190
ar v223,a191
s_mov_b32 s95, 4096
s_mov_b32 s96, 8192
s_mov_b32 s97, 12288
s_mov_b64 vcc, 0
s_nop 1
s_setpc_b64 s[24:25]
; ---- fallback: the later 32 rows
.La5_LA_SLOW1:
wt lgkmcnt(0)
s_nop 7
s_nop 7
aw a160,v192
aw a161,v193
aw a162,v194
aw a163,v195
aw a164,v196
aw a165,v197
aw a166,v198
aw a167,v199
aw a168,v200
aw a169,v201
aw a170,v202
aw a171,v203
aw a172,v204
aw a173,v205
aw a174,v206
aw a175,v207
aw a176,v208
aw a177,v209
aw a178,v210
aw a179,v211
aw a180,v212
aw a181,v213
aw a182,v214
aw a183,v215
aw a184,v216
aw a185,v217
aw a186,v218
aw a187,v219
aw a188,v220
aw a189,v221
aw a190,v222
aw a191,v223
s_lshl_b32 s96, s72, 19
s_add_u32 s96, s96, 20971520
s_add_u32 s92, s26, s96
s_addc_u32 s93, s27, 0
s_and_b32 s93, s93, 0xffff
s_mov_b32 s94, 17301504
s_mov_b32 s95, 0x20000
v_mov_b32 v64, 0
v_mov_b32 v65, 0
v_mov_b32 v66, 0
v_mov_b32 v67, 0
v_mov_b32 v68, 0
v_mov_b32 v69, 0
v_mov_b32 v70, 0
v_mov_b32 v71, 0
v_mov_b32 v72, 0
v_mov_b32 v73, 0
v_mov_b32 v74, 0
v_mov_b32 v75, 0
v_mov_b32 v76, 0
v_mov_b32 v77, 0
v_mov_b32 v78, 0
v_mov_b32 v79, 0
v_mov_b32 v80, 0
v_mov_b32 v81, 0
v_mov_b32 v82, 0
v_mov_b32 v83, 0
v_mov_b32 v84, 0
v_mov_b32 v85, 0
v_mov_b32 v86, 0
v_mov_b32 v87, 0
v_mov_b32 v88, 0
v_mov_b32 v89, 0
v_mov_b32 v90, 0
v_mov_b32 v91, 0
v_mov_b32 v92, 0
v_mov_b32 v93, 0
v_mov_b32 v94, 0
v_mov_b32 v95, 0
v_mov_b32 v96, 0
v_mov_b32 v97, 0
v_mov_b32 v98, 0
v_mov_b32 v99, 0
v_mov_b32 v100, 0
v_mov_b32 v101, 0
v_mov_b32 v102, 0
v_mov_b32 v103, 0
v_mov_b32 v104, 0
v_mov_b32 v105, 0
v_mov_b32 v106, 0
v_mov_b32 v107, 0
)ASM" R"ASM(v_mov_b32 v108, 0
v_mov_b32 v109, 0
v_mov_b32 v110, 0
v_mov_b32 v111, 0
v_mov_b32 v112, 0
v_mov_b32 v113, 0
v_mov_b32 v114, 0
v_mov_b32 v115, 0
v_mov_b32 v116, 0
v_mov_b32 v117, 0
v_mov_b32 v118, 0
v_mov_b32 v119, 0
v_mov_b32 v120, 0
v_mov_b32 v121, 0
v_mov_b32 v122, 0
v_mov_b32 v123, 0
v_mov_b32 v124, 0
v_mov_b32 v125, 0
v_mov_b32 v126, 0
v_mov_b32 v127, 0
v_mov_b32 v216, 0xff61b1e6
v_mov_b32 v217, 0xff61b1e6
v_mov_b32 v218, 0
v_mov_b32 v219, 0
s_mov_b32 s7, 0
.La5_LA_SLOW1_L:
s_lshl_b32 s96, s7, 13
s_add_u32 s97, s96, 4096
ld a[128:131],v255,s[92:95],s96 offen offset:0 sc1
ld a[132:135],v255,s[92:95],s96 offen offset:1024 sc1
ld a[136:139],v255,s[92:95],s96 offen offset:2048 sc1
ld a[140:143],v255,s[92:95],s96 offen offset:3072 sc1
ld a[144:147],v255,s[92:95],s97 offen offset:0 sc1
ld a[148:151],v255,s[92:95],s97 offen offset:1024 sc1
ld a[152:155],v255,s[92:95],s97 offen offset:2048 sc1
ld a[156:159],v255,s[92:95],s97 offen offset:3072 sc1
s_lshl_b32 s96, s76, 3
s_sub_u32 s96, s96, s7
s_sub_u32 s96, s96, s3
s_add_u32 s96, s96, 7
s_lshl_b32 s96, s96, 5
v_bfe_u32 v220, v255, 4, 4
v_lshrrev_b32 v221, 8, v255
v_lshlrev_b32 v221, 2, v221
v_sub_u32 v220, v220, v221
v_add_u32 v220, s96, v220
v_mov_b32 v222, 0xceabfb8f
wt vmcnt(0)
mm v[192:195],a[128:131],v[160:163],0
mm v[192:195],a[132:135],v[164:167],v[192:195]
mm v[192:195],a[136:139],v[168:171],v[192:195]
mm v[192:195],a[140:143],v[172:175],v[192:195]
mm v[196:199],a[144:147],v[160:163],0
mm v[196:199],a[148:151],v[164:167],v[196:199]
mm v[196:199],a[152:155],v[168:171],v[196:199]
mm v[196:199],a[156:159],v[172:175],v[196:199]
mm v[200:203],a[128:131],v[176:179],0
mm v[200:203],a[132:135],v[180:183],v[200:203]
mm v[200:203],a[136:139],v[184:187],v[200:203]
mm v[200:203],a[140:143],v[188:191],v[200:203]
mm v[204:207],a[144:147],v[176:179],0
mm v[204:207],a[148:151],v[180:183],v[204:207]
mm v[204:207],a[152:155],v[184:187],v[204:207]
mm v[204:207],a[156:159],v[188:191],v[204:207]
s_nop 7
s_nop 7
s_lshl_b32 s96, s7, 13
s_add_u32 s96, s96, 16777216
s_add_u32 s97, s96, 4096
ld a[128:131],v255,s[92:95],s96 offen offset:0 sc1
ld a[132:135],v255,s[92:95],s96 offen offset:1024 sc1
ld a[136:139],v255,s[92:95],s96 offen offset:2048 sc1
ld a[140:143],v255,s[92:95],s96 offen offset:3072 sc1
ld a[144:147],v255,s[92:95],s97 offen offset:0 sc1
ld a[148:151],v255,s[92:95],s97 offen offset:1024 sc1
ld a[152:155],v255,s[92:95],s97 offen offset:2048 sc1
ld a[156:159],v255,s[92:95],s97 offen offset:3072 sc1
v_cmp_gt_i32 vcc, 0, v220
s_nop 1
v_cndmask_b32 v192, v192, v222, vcc
v_cmp_gt_i32 vcc, 1, v220
s_nop 1
v_cndmask_b32 v193, v193, v222, vcc
v_cmp_gt_i32 vcc, 2, v220
s_nop 1
v_cndmask_b32 v194, v194, v222, vcc
v_cmp_gt_i32 vcc, 3, v220
s_nop 1
v_cndmask_b32 v195, v195, v222, vcc
v_cmp_gt_i32 vcc, 16, v220
s_nop 1
v_cndmask_b32 v196, v196, v222, vcc
v_cmp_gt_i32 vcc, 17, v220
s_nop 1
v_cndmask_b32 v197, v197, v222, vcc
v_cmp_gt_i32 vcc, 18, v220
s_nop 1
v_cndmask_b32 v198, v198, v222, vcc
v_cmp_gt_i32 vcc, 19, v220
s_nop 1
)ASM" R"ASM(v_cndmask_b32 v199, v199, v222, vcc
v_cmp_gt_i32 vcc, -16, v220
s_nop 1
v_cndmask_b32 v200, v200, v222, vcc
v_cmp_gt_i32 vcc, -15, v220
s_nop 1
v_cndmask_b32 v201, v201, v222, vcc
v_cmp_gt_i32 vcc, -14, v220
s_nop 1
v_cndmask_b32 v202, v202, v222, vcc
v_cmp_gt_i32 vcc, -13, v220
s_nop 1
v_cndmask_b32 v203, v203, v222, vcc
v_cmp_gt_i32 vcc, 0, v220
s_nop 1
v_cndmask_b32 v204, v204, v222, vcc
v_cmp_gt_i32 vcc, 1, v220
s_nop 1
v_cndmask_b32 v205, v205, v222, vcc
v_cmp_gt_i32 vcc, 2, v220
s_nop 1
v_cndmask_b32 v206, v206, v222, vcc
v_cmp_gt_i32 vcc, 3, v220
s_nop 1
v_cndmask_b32 v207, v207, v222, vcc
v_max3_f32 v222, v192, v193, v216
v_max3_f32 v222, v194, v195, v222
v_max3_f32 v222, v196, v197, v222
v_max3_f32 v222, v198, v199, v222
v_mov_b32 v221, v222
s_nop 1
v_permlane16_swap_b32 v221, v222
s_nop 1
v_max_f32 v222, v222, v221
v_mov_b32 v221, v222
s_nop 1
v_permlane32_swap_b32 v221, v222
s_nop 1
v_max_f32 v222, v222, v221
v_sub_f32 v223, v216, v222
v_mov_b32 v216, v222
ex v223,v223
v_sub_f32 v192, v192, v222
v_sub_f32 v193, v193, v222
v_sub_f32 v194, v194, v222
v_sub_f32 v195, v195, v222
v_sub_f32 v196, v196, v222
v_sub_f32 v197, v197, v222
v_sub_f32 v198, v198, v222
v_sub_f32 v199, v199, v222
ex v192,v192
ex v193,v193
ex v194,v194
ex v195,v195
ex v196,v196
ex v197,v197
ex v198,v198
ex v199,v199
v_mul_f32 v218, v218, v223
v_add_f32 v218, v218, v192
v_add_f32 v218, v218, v193
v_add_f32 v218, v218, v194
v_add_f32 v218, v218, v195
v_add_f32 v218, v218, v196
v_add_f32 v218, v218, v197
v_add_f32 v218, v218, v198
v_add_f32 v218, v218, v199
cv v208,v192,v193
cv v209,v194,v195
cv v210,v196,v197
cv v211,v198,v199
v_mul_f32 v64, v64, v223
v_mul_f32 v65, v65, v223
v_mul_f32 v66, v66, v223
v_mul_f32 v67, v67, v223
v_mul_f32 v68, v68, v223
v_mul_f32 v69, v69, v223
v_mul_f32 v70, v70, v223
v_mul_f32 v71, v71, v223
v_mul_f32 v72, v72, v223
v_mul_f32 v73, v73, v223
v_mul_f32 v74, v74, v223
v_mul_f32 v75, v75, v223
v_mul_f32 v76, v76, v223
v_mul_f32 v77, v77, v223
v_mul_f32 v78, v78, v223
v_mul_f32 v79, v79, v223
v_mul_f32 v80, v80, v223
v_mul_f32 v81, v81, v223
v_mul_f32 v82, v82, v223
v_mul_f32 v83, v83, v223
v_mul_f32 v84, v84, v223
v_mul_f32 v85, v85, v223
v_mul_f32 v86, v86, v223
v_mul_f32 v87, v87, v223
v_mul_f32 v88, v88, v223
v_mul_f32 v89, v89, v223
v_mul_f32 v90, v90, v223
v_mul_f32 v91, v91, v223
v_mul_f32 v92, v92, v223
)ASM" R"ASM(v_mul_f32 v93, v93, v223
v_mul_f32 v94, v94, v223
v_mul_f32 v95, v95, v223
v_max3_f32 v222, v200, v201, v217
v_max3_f32 v222, v202, v203, v222
v_max3_f32 v222, v204, v205, v222
v_max3_f32 v222, v206, v207, v222
v_mov_b32 v221, v222
s_nop 1
v_permlane16_swap_b32 v221, v222
s_nop 1
v_max_f32 v222, v222, v221
v_mov_b32 v221, v222
s_nop 1
v_permlane32_swap_b32 v221, v222
s_nop 1
v_max_f32 v222, v222, v221
v_sub_f32 v223, v217, v222
v_mov_b32 v217, v222
ex v223,v223
v_sub_f32 v200, v200, v222
v_sub_f32 v201, v201, v222
v_sub_f32 v202, v202, v222
v_sub_f32 v203, v203, v222
v_sub_f32 v204, v204, v222
v_sub_f32 v205, v205, v222
v_sub_f32 v206, v206, v222
v_sub_f32 v207, v207, v222
ex v200,v200
ex v201,v201
ex v202,v202
ex v203,v203
ex v204,v204
ex v205,v205
ex v206,v206
ex v207,v207
v_mul_f32 v219, v219, v223
v_add_f32 v219, v219, v200
v_add_f32 v219, v219, v201
v_add_f32 v219, v219, v202
v_add_f32 v219, v219, v203
v_add_f32 v219, v219, v204
v_add_f32 v219, v219, v205
v_add_f32 v219, v219, v206
v_add_f32 v219, v219, v207
cv v212,v200,v201
cv v213,v202,v203
cv v214,v204,v205
cv v215,v206,v207
v_mul_f32 v96, v96, v223
v_mul_f32 v97, v97, v223
v_mul_f32 v98, v98, v223
v_mul_f32 v99, v99, v223
v_mul_f32 v100, v100, v223
v_mul_f32 v101, v101, v223
v_mul_f32 v102, v102, v223
v_mul_f32 v103, v103, v223
v_mul_f32 v104, v104, v223
v_mul_f32 v105, v105, v223
v_mul_f32 v106, v106, v223
v_mul_f32 v107, v107, v223
v_mul_f32 v108, v108, v223
v_mul_f32 v109, v109, v223
v_mul_f32 v110, v110, v223
v_mul_f32 v111, v111, v223
v_mul_f32 v112, v112, v223
v_mul_f32 v113, v113, v223
v_mul_f32 v114, v114, v223
v_mul_f32 v115, v115, v223
v_mul_f32 v116, v116, v223
v_mul_f32 v117, v117, v223
v_mul_f32 v118, v118, v223
v_mul_f32 v119, v119, v223
v_mul_f32 v120, v120, v223
v_mul_f32 v121, v121, v223
v_mul_f32 v122, v122, v223
v_mul_f32 v123, v123, v223
v_mul_f32 v124, v124, v223
v_mul_f32 v125, v125, v223
v_mul_f32 v126, v126, v223
v_mul_f32_e64 v127, v127, v223
wt vmcnt(0)
mm v[64:67],a[128:131],v[208:211],v[64:67]
mm v[96:99],a[128:131],v[212:215],v[96:99]
mm v[68:71],a[132:135],v[208:211],v[68:71]
mm v[100:103],a[132:135],v[212:215],v[100:103]
mm v[72:75],a[136:139],v[208:211],v[72:75]
mm v[104:107],a[136:139],v[212:215],v[104:107]
mm v[76:79],a[140:143],v[208:211],v[76:79]
mm v[108:111],a[140:143],v[212:215],v[108:111]
mm v[80:83],a[144:147],v[208:211],v[80:83]
mm v[112:115],a[144:147],v[212:215],v[112:115]
mm v[84:87],a[148:151],v[208:211],v[84:87]
mm v[116:119],a[148:151],v[212:215],v[116:119]
mm v[88:91],a[152:155],v[208:211],v[88:91]
mm v[120:123],a[152:155],v[212:215],v[120:123]
mm v[92:95],a[156:159],v[208:211],v[92:95]
mm v[124:127],a[156:159],v[212:215],v[124:127]
s_nop 7
s_nop 7
)ASM" R"ASM(s_add_u32 s7, s7, 1
s_lshr_b32 s96, s7, 3
s_cmp_le_u32 s96, s76
s_cbranch_scc1 .La5_LA_SLOW1_L
v_mov_b32 v221, v218
s_nop 1
v_permlane16_swap_b32 v221, v218
s_nop 1
v_add_f32 v218, v218, v221
v_mov_b32 v221, v218
s_nop 1
v_permlane32_swap_b32 v221, v218
s_nop 1
v_add_f32 v218, v218, v221
v_mov_b32 v221, v219
s_nop 1
v_permlane16_swap_b32 v221, v219
s_nop 1
v_add_f32 v219, v219, v221
v_mov_b32 v221, v219
s_nop 1
v_permlane32_swap_b32 v221, v219
s_nop 1
v_add_f32 v219, v219, v221
v_rcp_f32 v242, v218
v_rcp_f32 v243, v219
ar v192,a160
ar v193,a161
ar v194,a162
ar v195,a163
ar v196,a164
ar v197,a165
ar v198,a166
ar v199,a167
ar v200,a168
ar v201,a169
ar v202,a170
ar v203,a171
ar v204,a172
ar v205,a173
ar v206,a174
ar v207,a175
ar v208,a176
ar v209,a177
ar v210,a178
ar v211,a179
ar v212,a180
ar v213,a181
ar v214,a182
ar v215,a183
ar v216,a184
ar v217,a185
ar v218,a186
ar v219,a187
ar v220,a188
ar v221,a189
ar v222,a190
ar v223,a191
s_mov_b32 s95, 4096
s_mov_b32 s96, 8192
s_mov_b32 s97, 12288
s_mov_b64 vcc, 0
s_nop 1
s_setpc_b64 s[24:25]
.La5_LA_SC1:
s_call_b64 s[24:25], .La5_LA_SLOW0
s_branch .La5_LA_SR1
.La5_LA_SC2:
s_call_b64 s[24:25], .La5_LA_SLOW1
s_branch .La5_LA_SR2
.La5_LA_PS0:
s_mov_b32 s84, 3
s_mov_b32 s70, 17408
wt vmcnt(2)
ld a[220:223],v255,s[68:71],s94 offen sc1
ld a[0:3],v255,s[68:71],0 offen offset:0 sc1
ld a[4:7],v255,s[68:71],0 offen offset:1024 sc1
ld a[8:11],v255,s[68:71],0 offen offset:2048 sc1
ld a[12:15],v255,s[68:71],0 offen offset:3072 sc1
ld a[16:19],v255,s[68:71],s95 offen offset:0 sc1
ld a[20:23],v255,s[68:71],s95 offen offset:1024 sc1
ld a[24:27],v255,s[68:71],s95 offen offset:2048 sc1
s_branch .La5_LA_PS1
.La5_LA_PS2:
s_mov_b32 s84, 3
s_mov_b32 s70, 17408
wt vmcnt(0)
ld a[220:223],v255,s[68:71],s94 offen sc1
ld a[0:3],v255,s[68:71],0 offen offset:0 sc1
ld a[4:7],v255,s[68:71],0 offen offset:1024 sc1
ld a[8:11],v255,s[68:71],0 offen offset:2048 sc1
ld a[12:15],v255,s[68:71],0 offen offset:3072 sc1
ld a[16:19],v255,s[68:71],s95 offen offset:0 sc1
ld a[20:23],v255,s[68:71],s95 offen offset:1024 sc1
ld a[24:27],v255,s[68:71],s95 offen offset:2048 sc1
ld a[28:31],v255,s[68:71],s95 offen offset:3072 sc1
ld a[32:35],v255,s[68:71],s96 offen offset:0 sc1
ld a[36:39],v255,s[68:71],s96 offen offset:1024 sc1
ld a[40:43],v255,s[68:71],s96 offen offset:2048 sc1
ld a[44:47],v255,s[68:71],s96 offen offset:3072 sc1
)ASM" R"ASM(ld a[48:51],v255,s[68:71],s97 offen offset:0 sc1
ld a[52:55],v255,s[68:71],s97 offen offset:1024 sc1
ld a[56:59],v255,s[68:71],s97 offen offset:2048 sc1
ld a[60:63],v255,s[68:71],s97 offen offset:3072 sc1
s_branch .La5_LA_PS3
.La5_LA_PSPIN:
s_mov_b32 s93, 0
.La5_LA_PSPIN_L:
s_add_u32 s93, s93, 1
s_cmp_gt_u32 s93, 2097152
s_cbranch_scc1 .La5_LA_PSOK
s_sleep 8
buffer_load_dword v250, v251, s[64:67], s20 offen sc1
wt vmcnt(0)
v_cmp_eq_u32 vcc, v250, v249
s_nop 1
s_and_b32 s92, vcc_lo, 3
s_cmp_eq_u32 s92, 3
s_cbranch_scc0 .La5_LA_PSPIN_L
s_branch .La5_LA_PSOK
.La5_LA_PSOK:
s_mov_b32 s70, 17408
ld a[220:223],v255,s[68:71],s94 offen sc1
ld a[0:3],v255,s[68:71],0 offen offset:0 sc1
ld a[4:7],v255,s[68:71],0 offen offset:1024 sc1
ld a[8:11],v255,s[68:71],0 offen offset:2048 sc1
ld a[12:15],v255,s[68:71],0 offen offset:3072 sc1
ld a[16:19],v255,s[68:71],s95 offen offset:0 sc1
ld a[20:23],v255,s[68:71],s95 offen offset:1024 sc1
ld a[24:27],v255,s[68:71],s95 offen offset:2048 sc1
ld a[28:31],v255,s[68:71],s95 offen offset:3072 sc1
ld a[32:35],v255,s[68:71],s96 offen offset:0 sc1
ld a[36:39],v255,s[68:71],s96 offen offset:1024 sc1
ld a[40:43],v255,s[68:71],s96 offen offset:2048 sc1
ld a[44:47],v255,s[68:71],s96 offen offset:3072 sc1
ld a[48:51],v255,s[68:71],s97 offen offset:0 sc1
ld a[52:55],v255,s[68:71],s97 offen offset:1024 sc1
ld a[56:59],v255,s[68:71],s97 offen offset:2048 sc1
ld a[60:63],v255,s[68:71],s97 offen offset:3072 sc1
s_branch .La5_LA_DC1
.La5_LO_NB1:
.La5_LW3_6:
s_cbranch_execz .La5_LA_RETURN
s_mov_b32 s93, 0
.La5_LO_NB1_L:
s_add_u32 s93, s93, 1
s_cmp_gt_u32 s93, 2097152
s_cbranch_scc1 .La5_LO_KB1
s_sleep 8
buffer_load_dwordx2 v[250:251], v248, s[64:67], s5 offen sc1
wt vmcnt(0)
v_cmp_eq_u64 vcc, v[250:251], v[252:253]
s_nop 1
s_bitcmp1_b32 vcc_lo, 1
s_cbranch_scc0 .La5_LO_NB1_L
s_branch .La5_LO_KB1
.La5_LO_NB2:
s_mov_b32 s93, 0
.La5_LO_NB2_L:
s_add_u32 s93, s93, 1
s_cmp_gt_u32 s93, 2097152
s_nop 0
s_cbranch_scc1 .La5_LO_KB2
s_sleep 8
buffer_load_dwordx2 v[250:251], v248, s[64:67], s5 offen sc1
wt vmcnt(0)
v_cmp_eq_u64 vcc, v[250:251], v[252:253]
s_nop 1
s_bitcmp1_b32 vcc_lo, 2
s_cbranch_scc0 .La5_LO_NB2_L
s_branch .La5_LO_KB2
.La5_LO_NB3:
s_mov_b32 s93, 0
.La5_LO_NB3_L:
s_add_u32 s93, s93, 1
s_cmp_gt_u32 s93, 2097152
s_nop 0
s_nop 0
s_cbranch_scc1 .La5_LO_KB3
s_sleep 8
buffer_load_dwordx2 v[250:251], v248, s[64:67], s5 offen sc1
wt vmcnt(0)
v_cmp_eq_u64 vcc, v[250:251], v[252:253]
s_nop 1
s_bitcmp1_b32 vcc_lo, 3
s_cbranch_scc0 .La5_LO_NB3_L
s_branch .La5_LO_KB3
.La5_LO_NB4:
s_mov_b32 s93, 0
.La5_LO_NB4_L:
s_add_u32 s93, s93, 1
s_cmp_gt_u32 s93, 2097152
s_nop 0
s_nop 0
s_cbranch_scc1 .La5_LO_KB4
s_sleep 8
buffer_load_dwordx2 v[250:251], v248, s[64:67], s5 offen sc1
wt vmcnt(0)
v_cmp_eq_u64 vcc, v[250:251], v[252:253]
s_nop 1
)ASM" R"ASM(s_bitcmp1_b32 vcc_lo, 4
s_cbranch_scc0 .La5_LO_NB4_L
s_branch .La5_LO_KB4
.La5_LO_NB5:
s_mov_b32 s93, 0
.La5_LO_NB5_L:
s_add_u32 s93, s93, 1
s_cmp_gt_u32 s93, 2097152
s_nop 0
s_nop 0
s_cbranch_scc1 .La5_LO_KB5
s_sleep 8
buffer_load_dwordx2 v[250:251], v248, s[64:67], s5 offen sc1
wt vmcnt(0)
v_cmp_eq_u64 vcc, v[250:251], v[252:253]
s_nop 1
s_bitcmp1_b32 vcc_lo, 5
s_cbranch_scc0 .La5_LO_NB5_L
s_branch .La5_LO_KB5
.La5_LO_NB13:
s_mov_b32 s93, 0
.La5_LO_NB13_L:
s_add_u32 s93, s93, 1
s_cmp_gt_u32 s93, 2097152
s_nop 0
s_nop 0
s_cbranch_scc1 .La5_LO_KB13
s_sleep 8
buffer_load_dwordx2 v[250:251], v248, s[64:67], s5 offen sc1
wt vmcnt(0)
v_cmp_eq_u64 vcc, v[250:251], v[252:253]
s_nop 1
s_bitcmp1_b32 vcc_lo, 13
s_cbranch_scc0 .La5_LO_NB13_L
s_branch .La5_LO_KB13
.La5_LO_NB6:
s_mov_b32 s93, 0
.La5_LO_NB6_L:
s_add_u32 s93, s93, 1
s_cmp_gt_u32 s93, 2097152
s_nop 0
s_nop 0
s_cbranch_scc1 .La5_LO_KB6
s_sleep 8
buffer_load_dwordx2 v[250:251], v248, s[64:67], s5 offen sc1
wt vmcnt(0)
v_cmp_eq_u64 vcc, v[250:251], v[252:253]
s_nop 1
s_bitcmp1_b32 vcc_lo, 6
s_cbranch_scc0 .La5_LO_NB6_L
s_branch .La5_LO_KB6
.La5_LO_NB7:
s_mov_b32 s93, 0
.La5_LO_NB7_L:
s_add_u32 s93, s93, 1
s_cmp_gt_u32 s93, 2097152
s_nop 0
s_nop 0
s_cbranch_scc1 .La5_LO_KB7
s_sleep 8
buffer_load_dwordx2 v[250:251], v248, s[64:67], s5 offen sc1
wt vmcnt(0)
v_cmp_eq_u64 vcc, v[250:251], v[252:253]
s_nop 1
s_bitcmp1_b32 vcc_lo, 7
s_cbranch_scc0 .La5_LO_NB7_L
s_branch .La5_LO_KB7
.La5_LO_NC1:
.La5_LW7_7:
s_cbranch_execz .La5_LA_RETURN
s_mov_b32 s93, 0
.La5_LO_NC1_L:
s_add_u32 s93, s93, 1
s_cmp_gt_u32 s93, 2097152
s_nop 0
s_cbranch_scc1 .La5_LO_KC1
s_sleep 8
buffer_load_dwordx2 v[250:251], v248, s[64:67], s5 offen sc1
wt vmcnt(0)
v_cmp_eq_u64 vcc, v[250:251], v[252:253]
s_nop 1
s_bitcmp1_b32 vcc_lo, 1
s_cbranch_scc0 .La5_LO_NC1_L
s_branch .La5_LO_KC1
.La5_LO_NC2:
s_mov_b32 s93, 0
.La5_LO_NC2_L:
s_add_u32 s93, s93, 1
s_cmp_gt_u32 s93, 2097152
s_nop 0
s_nop 0
s_cbranch_scc1 .La5_LO_KC2
s_sleep 8
buffer_load_dwordx2 v[250:251], v248, s[64:67], s5 offen sc1
wt vmcnt(0)
v_cmp_eq_u64 vcc, v[250:251], v[252:253]
s_nop 1
s_bitcmp1_b32 vcc_lo, 2
s_cbranch_scc0 .La5_LO_NC2_L
s_branch .La5_LO_KC2
)ASM" R"ASM(.La5_LO_NC3:
s_mov_b32 s93, 0
.La5_LO_NC3_L:
s_add_u32 s93, s93, 1
s_cmp_gt_u32 s93, 2097152
s_nop 0
s_nop 0
s_cbranch_scc1 .La5_LO_KC3
s_sleep 8
buffer_load_dwordx2 v[250:251], v248, s[64:67], s5 offen sc1
wt vmcnt(0)
v_cmp_eq_u64 vcc, v[250:251], v[252:253]
s_nop 1
s_bitcmp1_b32 vcc_lo, 3
s_cbranch_scc0 .La5_LO_NC3_L
s_branch .La5_LO_KC3
.La5_LO_NC4:
s_mov_b32 s93, 0
.La5_LO_NC4_L:
s_add_u32 s93, s93, 1
s_cmp_gt_u32 s93, 2097152
s_nop 0
s_nop 0
s_cbranch_scc1 .La5_LO_KC4
s_sleep 8
buffer_load_dwordx2 v[250:251], v248, s[64:67], s5 offen sc1
wt vmcnt(0)
v_cmp_eq_u64 vcc, v[250:251], v[252:253]
s_nop 1
s_bitcmp1_b32 vcc_lo, 4
s_cbranch_scc0 .La5_LO_NC4_L
s_branch .La5_LO_KC4
.La5_LO_NC5:
s_mov_b32 s93, 0
.La5_LO_NC5_L:
s_add_u32 s93, s93, 1
s_cmp_gt_u32 s93, 2097152
s_nop 0
s_nop 0
s_cbranch_scc1 .La5_LO_KC5
s_sleep 8
buffer_load_dwordx2 v[250:251], v248, s[64:67], s5 offen sc1
wt vmcnt(0)
v_cmp_eq_u64 vcc, v[250:251], v[252:253]
s_nop 1
s_bitcmp1_b32 vcc_lo, 5
s_cbranch_scc0 .La5_LO_NC5_L
s_branch .La5_LO_KC5
.La5_LO_NC6:
s_mov_b32 s93, 0
.La5_LO_NC6_L:
s_add_u32 s93, s93, 1
s_cmp_gt_u32 s93, 2097152
s_nop 0
s_nop 0
s_cbranch_scc1 .La5_LO_KC6
s_sleep 8
buffer_load_dwordx2 v[250:251], v248, s[64:67], s5 offen sc1
wt vmcnt(0)
v_cmp_eq_u64 vcc, v[250:251], v[252:253]
s_nop 1
s_bitcmp1_b32 vcc_lo, 6
s_cbranch_scc0 .La5_LO_NC6_L
s_branch .La5_LO_KC6
.La5_LO_NC7:
s_mov_b32 s93, 0
.La5_LO_NC7_L:
s_add_u32 s93, s93, 1
s_cmp_gt_u32 s93, 2097152
s_nop 0
s_nop 0
s_cbranch_scc1 .La5_LO_KC7
s_sleep 8
buffer_load_dwordx2 v[250:251], v248, s[64:67], s5 offen sc1
wt vmcnt(0)
v_cmp_eq_u64 vcc, v[250:251], v[252:253]
s_nop 1
s_bitcmp1_b32 vcc_lo, 7
s_cbranch_scc0 .La5_LO_NC7_L
s_branch .La5_LO_KC7
.La5_LA_SC3:
s_call_b64 s[24:25], .La5_LA_SLOW0
aw a0,0
aw a1,0
aw a2,0
aw a3,0
aw a4,0
aw a5,0
aw a6,0
aw a7,0
aw a8,0
aw a9,0
aw a10,0
aw a11,0
aw a12,0
aw a13,0
aw a14,0
aw a15,0
aw a16,0
aw a17,0
)ASM" R"ASM(aw a18,0
aw a19,0
aw a20,0
aw a21,0
aw a22,0
aw a23,0
aw a24,0
aw a25,0
aw a26,0
aw a27,0
aw a28,0
aw a29,0
aw a30,0
aw a31,0
s_nop 1
v_mov_b32 v250, v242
v_mov_b32 v249, v243
v_xor_b32 v242, 64, v240
v_xor_b32 v243, 96, v240
ar v254,a229
s_mov_b32 s93, 0
ld a[128:131],v254,s[40:43],s93 offen
s_mov_b32 s93, 7168
ld a[132:135],v254,s[40:43],s93 offen
s_mov_b32 s93, 14336
ld a[136:139],v254,s[40:43],s93 offen
s_mov_b32 s93, 21504
ld a[140:143],v254,s[40:43],s93 offen
s_mov_b32 s93, 28672
ld a[144:147],v254,s[40:43],s93 offen
s_mov_b32 s93, 35840
ld a[148:151],v254,s[40:43],s93 offen
s_mov_b32 s93, 43008
ld a[152:155],v254,s[40:43],s93 offen
s_mov_b32 s93, 50176
ld a[156:159],v254,s[40:43],s93 offen
s_mov_b32 s93, 57344
ld a[160:163],v254,s[40:43],s93 offen
s_mov_b32 s93, 64512
ld a[164:167],v254,s[40:43],s93 offen
s_mov_b32 s93, 71680
ld a[168:171],v254,s[40:43],s93 offen
s_mov_b32 s93, 78848
ld a[172:175],v254,s[40:43],s93 offen
s_mov_b32 s93, 86016
ld a[176:179],v254,s[40:43],s93 offen
s_mov_b32 s93, 93184
ld a[180:183],v254,s[40:43],s93 offen
s_mov_b32 s93, 100352
ld a[184:187],v254,s[40:43],s93 offen
s_mov_b32 s93, 107520
ld a[188:191],v254,s[40:43],s93 offen
s_branch .La5_LA_SR3
.La5_LA_SC4:
s_call_b64 s[24:25], .La5_LA_SLOW1
aw a32,0
aw a33,0
aw a34,0
aw a35,0
aw a36,0
aw a37,0
aw a38,0
aw a39,0
aw a40,0
aw a41,0
aw a42,0
aw a43,0
aw a44,0
aw a45,0
aw a46,0
aw a47,0
aw a48,0
aw a49,0
aw a50,0
aw a51,0
aw a52,0
aw a53,0
aw a54,0
aw a55,0
aw a56,0
aw a57,0
aw a58,0
aw a59,0
aw a60,0
aw a61,0
aw a62,0
aw a63,0
s_nop 1
v_mov_b32 v250, v242
v_mov_b32 v249, v243
v_xor_b32 v242, 64, v240
v_xor_b32 v243, 96, v240
ar v254,a229
s_mov_b32 s93, 0
ld a[128:131],v254,s[40:43],s93 offen
s_mov_b32 s93, 7168
ld a[132:135],v254,s[40:43],s93 offen
s_mov_b32 s93, 14336
ld a[136:139],v254,s[40:43],s93 offen
s_mov_b32 s93, 21504
)ASM" R"ASM(ld a[140:143],v254,s[40:43],s93 offen
s_mov_b32 s93, 28672
ld a[144:147],v254,s[40:43],s93 offen
s_mov_b32 s93, 35840
ld a[148:151],v254,s[40:43],s93 offen
s_mov_b32 s93, 43008
ld a[152:155],v254,s[40:43],s93 offen
s_mov_b32 s93, 50176
ld a[156:159],v254,s[40:43],s93 offen
s_mov_b32 s93, 57344
ld a[160:163],v254,s[40:43],s93 offen
s_mov_b32 s93, 64512
ld a[164:167],v254,s[40:43],s93 offen
s_mov_b32 s93, 71680
ld a[168:171],v254,s[40:43],s93 offen
s_mov_b32 s93, 78848
ld a[172:175],v254,s[40:43],s93 offen
s_mov_b32 s93, 86016
ld a[176:179],v254,s[40:43],s93 offen
s_mov_b32 s93, 93184
ld a[180:183],v254,s[40:43],s93 offen
s_mov_b32 s93, 100352
ld a[184:187],v254,s[40:43],s93 offen
s_mov_b32 s93, 107520
ld a[188:191],v254,s[40:43],s93 offen
s_branch .La5_LA_SR4
.La5_LO_VC1:
.La5_LW5_4:
s_cbranch_execz .La5_LW5_R0
.La5_LW6_7:
s_cbranch_execz .La5_LW6_R0
s_getpc_b64 s[24:25]
.La5_LW7_P0:
s_add_u32 s24, s24, .La5_LW7_R0-.La5_LW7_P0
s_addc_u32 s25, s25, 0
s_mov_b64 exec, 0
s_cmp_eq_u32 s3, 0
s_cbranch_scc1 .La5_LW7_0
s_cmp_eq_u32 s3, 1
s_cbranch_scc1 .La5_LW7_1
s_cmp_eq_u32 s3, 2
s_cbranch_scc1 .La5_LW7_2
s_branch .La5_LW7_3
.La5_LW7_R0:
s_mov_b64 exec, -1
s_branch .La5_LO_VE1
.La5_LO_W:
s_mov_b32 s93, 3670016
ld a[192:195],v228,s[40:43],s93 offen
s_mov_b32 s93, 3677184
ld a[196:199],v228,s[40:43],s93 offen
s_mov_b32 s93, 3684352
ld a[200:203],v228,s[40:43],s93 offen
s_mov_b32 s93, 3691520
ld a[204:207],v228,s[40:43],s93 offen
s_mov_b32 s93, 3698688
ld a[208:211],v228,s[40:43],s93 offen
s_mov_b32 s93, 3705856
ld a[212:215],v228,s[40:43],s93 offen
s_mov_b32 s93, 3713024
ld a[216:219],v228,s[40:43],s93 offen
s_mov_b32 s93, 3720192
ld a[220:223],v228,s[40:43],s93 offen
s_mov_b32 s93, 3727360
ld a[224:227],v228,s[40:43],s93 offen
s_mov_b32 s93, 3734528
ld a[228:231],v228,s[40:43],s93 offen
s_mov_b32 s93, 3741696
ld a[232:235],v228,s[40:43],s93 offen
s_mov_b32 s93, 3748864
ld a[236:239],v228,s[40:43],s93 offen
s_mov_b32 s93, 3756032
ld a[240:243],v228,s[40:43],s93 offen
s_mov_b32 s93, 3763200
ld a[244:247],v228,s[40:43],s93 offen
s_mov_b32 s93, 3770368
ld a[248:251],v228,s[40:43],s93 offen
s_mov_b32 s93, 3777536
ld a[252:255],v228,s[40:43],s93 offen
s_branch .La5_LO_START
.La5_LO_VC2:
s_getpc_b64 s[24:25]
.La5_LW7_P1:
s_add_u32 s24, s24, .La5_LW7_R1-.La5_LW7_P1
s_addc_u32 s25, s25, 0
s_mov_b64 exec, 0
s_cmp_eq_u32 s3, 0
s_cbranch_scc1 .La5_LW7_0
s_cmp_eq_u32 s3, 1
s_cbranch_scc1 .La5_LW7_1
s_cmp_eq_u32 s3, 2
s_cbranch_scc1 .La5_LW7_2
s_branch .La5_LW7_3
.La5_LW7_R1:
s_mov_b64 exec, -1
s_branch .La5_LO_VE2
.La5_LT_VAR:
v_mov_b32 v230, 0
v_lshlrev_b32 v232, 16, v96
v_and_b32 v233, 0xffff0000, v96
)ASM" R"ASM(v_sub_f32 v232, v232, v244
v_sub_f32 v233, v233, v244
v_fmac_f32 v230, v232, v232
v_fmac_f32 v230, v233, v233
v_lshlrev_b32 v232, 16, v97
v_and_b32 v233, 0xffff0000, v97
v_sub_f32 v232, v232, v244
v_sub_f32 v233, v233, v244
v_fmac_f32 v230, v232, v232
v_fmac_f32 v230, v233, v233
v_lshlrev_b32 v232, 16, v98
v_and_b32 v233, 0xffff0000, v98
v_sub_f32 v232, v232, v244
v_sub_f32 v233, v233, v244
v_fmac_f32 v230, v232, v232
v_fmac_f32 v230, v233, v233
v_lshlrev_b32 v232, 16, v99
v_and_b32 v233, 0xffff0000, v99
v_sub_f32 v232, v232, v244
v_sub_f32 v233, v233, v244
v_fmac_f32 v230, v232, v232
v_fmac_f32 v230, v233, v233
v_lshlrev_b32 v232, 16, v100
v_and_b32 v233, 0xffff0000, v100
v_sub_f32 v232, v232, v244
v_sub_f32 v233, v233, v244
v_fmac_f32 v230, v232, v232
v_fmac_f32 v230, v233, v233
v_lshlrev_b32 v232, 16, v101
v_and_b32 v233, 0xffff0000, v101
v_sub_f32 v232, v232, v244
v_sub_f32 v233, v233, v244
v_fmac_f32 v230, v232, v232
v_fmac_f32 v230, v233, v233
v_lshlrev_b32 v232, 16, v102
v_and_b32 v233, 0xffff0000, v102
v_sub_f32 v232, v232, v244
v_sub_f32 v233, v233, v244
v_fmac_f32 v230, v232, v232
v_fmac_f32 v230, v233, v233
v_lshlrev_b32 v232, 16, v103
v_and_b32 v233, 0xffff0000, v103
v_sub_f32 v232, v232, v244
v_sub_f32 v233, v233, v244
v_fmac_f32 v230, v232, v232
v_fmac_f32 v230, v233, v233
v_lshlrev_b32 v232, 16, v104
v_and_b32 v233, 0xffff0000, v104
v_sub_f32 v232, v232, v244
v_sub_f32 v233, v233, v244
v_fmac_f32 v230, v232, v232
v_fmac_f32 v230, v233, v233
v_lshlrev_b32 v232, 16, v105
v_and_b32 v233, 0xffff0000, v105
v_sub_f32 v232, v232, v244
v_sub_f32 v233, v233, v244
v_fmac_f32 v230, v232, v232
v_fmac_f32 v230, v233, v233
v_lshlrev_b32 v232, 16, v106
v_and_b32 v233, 0xffff0000, v106
v_sub_f32 v232, v232, v244
v_sub_f32 v233, v233, v244
v_fmac_f32 v230, v232, v232
v_fmac_f32 v230, v233, v233
v_lshlrev_b32 v232, 16, v107
v_and_b32 v233, 0xffff0000, v107
v_sub_f32 v232, v232, v244
v_sub_f32 v233, v233, v244
v_fmac_f32 v230, v232, v232
v_fmac_f32 v230, v233, v233
v_lshlrev_b32 v232, 16, v108
v_and_b32 v233, 0xffff0000, v108
v_sub_f32 v232, v232, v244
v_sub_f32 v233, v233, v244
v_fmac_f32 v230, v232, v232
v_fmac_f32 v230, v233, v233
v_lshlrev_b32 v232, 16, v109
v_and_b32 v233, 0xffff0000, v109
v_sub_f32 v232, v232, v244
v_sub_f32 v233, v233, v244
v_fmac_f32 v230, v232, v232
v_fmac_f32 v230, v233, v233
v_lshlrev_b32 v232, 16, v110
v_and_b32 v233, 0xffff0000, v110
v_sub_f32 v232, v232, v244
v_sub_f32 v233, v233, v244
v_fmac_f32 v230, v232, v232
v_fmac_f32 v230, v233, v233
v_lshlrev_b32 v232, 16, v111
v_and_b32 v233, 0xffff0000, v111
v_sub_f32 v232, v232, v244
v_sub_f32 v233, v233, v244
v_fmac_f32 v230, v232, v232
v_fmac_f32 v230, v233, v233
s_nop 1
v_add_f32_dpp v230, v230, v230 quad_perm:[1,0,3,2] row_mask:0xf bank_mask:0xf
s_nop 1
v_add_f32_dpp v230, v230, v230 quad_perm:[2,3,0,1] row_mask:0xf bank_mask:0xf
s_nop 1
v_add_f32_dpp v230, v230, v230 row_ror:8 row_mask:0xf bank_mask:0xf
)ASM" R"ASM(v_mov_b32 v231, v230
s_nop 1
v_permlane16_swap_b32 v231, v230
s_nop 1
v_add_f32 v230, v230, v231
v_mov_b32 v231, v230
s_nop 1
v_permlane32_swap_b32 v231, v230
s_nop 1
v_add_f32 v230, v230, v231
v_mul_f32 v230, 0x3a800000, v230
s_branch .La5_LT_VOK
.La5_LT_H1:
s_add_u32 s96, s96, 1
s_lshr_b32 s93, s96, 16
s_cbranch_scc1 .La5_LT_H2
ld v[224:227],v209,s[44:47],s92 offen offset:0 sc1
ld v[232:235],v209,s[44:47],s92 offen offset:16 sc1
ld v[244:247],v209,s[44:47],s92 offen offset:1024 sc1
ld v[248:251],v209,s[44:47],s92 offen offset:1040 sc1
s_branch .La5_LT_H0
.La5_LA_KSPIN:
s_mov_b32 s93, 0
.La5_LA_KSPIN_L:
s_add_u32 s93, s93, 1
s_cmp_gt_u32 s93, 2097152
s_cbranch_scc1 .La5_LA_KOK
s_sleep 8
buffer_load_dword v250, v251, s[64:67], s20 offen sc1
wt vmcnt(0)
v_cmp_eq_u32 vcc, v250, v249
s_nop 1
s_or_b32 vcc_hi, vcc_hi, s79
s_cmp_eq_u64 vcc, -1
s_cbranch_scc0 .La5_LA_KSPIN_L
s_branch .La5_LA_KOK
.La5_LA_QSPIN:
s_mov_b32 s93, 0
.La5_LA_QSPIN_L:
s_add_u32 s93, s93, 1
s_cmp_gt_u32 s93, 2097152
s_cbranch_scc1 .La5_LA_QOK
s_sleep 8
buffer_load_dword v250, v251, s[64:67], s20 offen sc1
wt vmcnt(0)
v_cmp_eq_u32 vcc, v250, v249
s_nop 1
s_and_b32 s92, vcc_lo, 3
s_cmp_eq_u32 s92, 3
s_cbranch_scc0 .La5_LA_QSPIN_L
s_branch .La5_LA_QOK
.La5_LEND:
.La5_LCODEEND:
)ASM"
        :: "{s[0:1]}"(__builtin_amdgcn_kernarg_segment_ptr()), "{s2}"(blockIdx.x), "{s3}"(__builtin_amdgcn_readfirstlane(threadIdx.x >> 6)), "{s[4:5]}"(DBGP)
        : "v0","v1","v2","v3","v4","v5","v6","v7","v8","v9","v10","v11","v12","v13","v14","v15","v16","v17","v18","v19","v20","v21","v22","v23","v24","v25","v26","v27","v28","v29","v30","v31","v32","v33","v34","v35","v36","v37","v38","v39","v40","v41","v42","v43","v44","v45","v46","v47","v48","v49","v50","v51","v52","v53","v54","v55","v56","v57","v58","v59","v60","v61","v62","v63","v64","v65","v66","v67","v68","v69","v70","v71","v72","v73","v74","v75","v76","v77","v78","v79","v80","v81","v82","v83","v84","v85","v86","v87","v88","v89","v90","v91","v92","v93","v94","v95","v96","v97","v98","v99","v100","v101","v102","v103","v104","v105","v106","v107","v108","v109","v110","v111","v112","v113","v114","v115","v116","v117","v118","v119","v120","v121","v122","v123","v124","v125","v126","v127","v128","v129","v130","v131","v132","v133","v134","v135","v136","v137","v138","v139","v140","v141","v142","v143","v144","v145","v146","v147","v148","v149","v150","v151","v152","v153","v154","v155","v156","v157","v158","v159","v160","v161","v162","v163","v164","v165","v166","v167","v168","v169","v170","v171","v172","v173","v174","v175","v176","v177","v178","v179","v180","v181","v182","v183","v184","v185","v186","v187","v188","v189","v190","v191","v192","v193","v194","v195","v196","v197","v198","v199","v200","v201","v202","v203","v204","v205","v206","v207","v208","v209","v210","v211","v212","v213","v214","v215","v216","v217","v218","v219","v220","v221","v222","v223","v224","v225","v226","v227","v228","v229","v230","v231","v232","v233","v234","v235","v236","v237","v238","v239","v240","v241","v242","v243","v244","v245","v246","v247","v248","v249","v250","v251","v252","v253","v254","v255","a0","a1","a2","a3","a4","a5","a6","a7","a8","a9","a10","a11","a12","a13","a14","a15","a16","a17","a18","a19","a20","a21","a22","a23","a24","a25","a26","a27","a28","a29","a30","a31","a32","a33","a34","a35","a36","a37","a38","a39","a40","a41","a42","a43","a44","a45","a46","a47","a48","a49","a50","a51","a52","a53","a54","a55","a56","a57","a58","a59","a60","a61","a62","a63","a64","a65","a66","a67","a68","a69","a70","a71","a72","a73","a74","a75","a76","a77","a78","a79","a80","a81","a82","a83","a84","a85","a86","a87","a88","a89","a90","a91","a92","a93","a94","a95","a96","a97","a98","a99","a100","a101","a102","a103","a104","a105","a106","a107","a108","a109","a110","a111","a112","a113","a114","a115","a116","a117","a118","a119","a120","a121","a122","a123","a124","a125","a126","a127","a128","a129","a130","a131","a132","a133","a134","a135","a136","a137","a138","a139","a140","a141","a142","a143","a144","a145","a146","a147","a148","a149","a150","a151","a152","a153","a154","a155","a156","a157","a158","a159","a160","a161","a162","a163","a164","a165","a166","a167","a168","a169","a170","a171","a172","a173","a174","a175","a176","a177","a178","a179","a180","a181","a182","a183","a184","a185","a186","a187","a188","a189","a190","a191","a192","a193","a194","a195","a196","a197","a198","a199","a200","a201","a202","a203","a204","a205","a206","a207","a208","a209","a210","a211","a212","a213","a214","a215","a216","a217","a218","a219","a220","a221","a222","a223","a224","a225","a226","a227","a228","a229","a230","a231","a232","a233","a234","a235","a236","a237","a238","a239","a240","a241","a242","a243","a244","a245","a246","a247","a248","a249","a250","a251","a252","a253","a254","a255","s6","s7","s8","s9","s10","s11","s12","s13","s14","s15","s16","s17","s18","s19","s20","s21","s22","s23","s24","s25","s26","s27","s28","s29","s30","s31","s32","s33","s34","s35","s36","s37","s38","s39","s40","s41","s42","s43","s44","s45","s46","s47","s48","s49","s50","s51","s52","s53","s54","s55","s56","s57","s58","s59","s60","s61","s62","s63","s64","s65","s66","s67","s68","s69","s70","s71","s72","s73","s74","s75","s76","s77","s78","s79","s80","s81","s82","s83","s84","s85","s86","s87","s88","s89","s90","s91","s92","s93","s94","s95","s96","s97","s98","s99","s100","s101","vcc","scc","m0","memory");
}

extern "C" void kernel_launch(void* const* d_in, const int* in_sizes, int n_in, void* d_out, int out_size, void* d_ws, size_t ws_size,
                              hipStream_t stream) {
    (void)hipFuncSetAttribute((const void*)fa_kernel, hipFuncAttributeMaxDynamicSharedMemorySize, LDS_BYTES);
    fa_kernel<<<256, 256, LDS_BYTES, stream>>>((const bf16*)d_in[0], (const bf16*)d_in[1], (const bf16*)d_in[2], (const bf16*)d_in[3], (const bf16*)d_in[4], (char*)d_ws,
                                               (const bf16*)d_in[5], (const bf16*)d_in[8], (const bf16*)d_in[9], (const bf16*)d_in[10], (const bf16*)d_in[7], (bf16*)d_out);
}
```
